```python
import torch

def _softmax(x, axis=-1):
    return torch.softmax(x, dim=axis)
def _gelu(x):
    return torch.nn.functional.gelu(x, approximate="tanh")
def _silu(x):
    return torch.nn.functional.silu(x)
def _sigmoid(x):
    return torch.sigmoid(x)
def _layernorm(x, axis=-1, eps=1e-5):
    m = x.mean(dim=axis, keepdim=True)
    v = x.var(dim=axis, keepdim=True, unbiased=False)
    return (x - m) / torch.sqrt(v + eps)
def _scatter_add(tgt, idx, src, axis=0):
    return tgt.clone().index_add_(axis, idx, src)
def _segment_sum(src, seg, n):
    out = torch.zeros((n,) + src.shape[1:], dtype=src.dtype)
    return out.index_add_(0, seg, src)
def _pad(x, pw):
    flat = tuple(v for pair in reversed(pw) for v in pair)
    return torch.nn.functional.pad(x, flat)

kernel_name = 'standalone__cross_network_dcn__recsys'

def setup_inputs():

    _w = (lambda a: (lambda t: t.to(torch.bfloat16) if t.is_floating_point() else t)(torch.as_tensor(a)))
    B, F, D, L = 16384, 100, 16, 6
    Din = min(F * D, 1024)
    return {
        "x0": _w((lambda *s: torch.randn(*s, dtype=torch.bfloat16) * 0.05)(B, Din)),
        "ws": _w((lambda *s: torch.randn(*s, dtype=torch.bfloat16) * 0.8)(L, Din, Din) * (Din ** -0.5)),
        "bs": _w((lambda *s: torch.randn(*s, dtype=torch.bfloat16) * 3.2)(L, Din) * 0.01),
    }

def reference(x0, ws, bs):
    x = x0
    L = ws.shape[0]
    for l in range(L):
        proj = torch.einsum('bd,de->be', x, ws[l]) + bs[l]
        x = x0 * proj + x
    return x
```

```cpp
#include <hip/hip_runtime.h>


constexpr int kD = 1024;
constexpr unsigned kWFlagOff = 131072, kWOff = 1u << 20, kWStream = 200 * 8192, kXchOff = 32u << 20, kDbgOff = 224u << 20;

__device__ __forceinline__ unsigned unit_src(unsigned v, unsigned h, unsigned w) {
  const unsigned l = v >> 5, u = v & 31, first = l ? 0u : ~0u;
  const unsigned split = (((first ? 0xff000000u : 0xff0000ffu) >> u) & 1) - 1;
  const unsigned part = first ? 0u : u >> 4, g = ((first ? 0xff000000u : 0xff00ff00u) >> u) & 1;
  const unsigned c = u >> (split & 1), jh = (u >> (~split & 2)) & 1;
  const unsigned kc = (first && u < 16) ? 8 * (1 - h) + (u >> 1) : 8 * (h ^ part) + 2 * ((w + c) & 3) + g;
  return (l * 1024 + 64 * kc) * 2048 + jh * 128;
}

__global__ __launch_bounds__(256, 1)
void dcn_cross_kernel(const unsigned short* __restrict__ x0, const unsigned short* __restrict__ ws, const unsigned short* __restrict__ bs,
                      unsigned char* __restrict__ wsp, unsigned short* __restrict__ out) {
  __shared__ __attribute__((aligned(16))) unsigned char lds[163840];
  const unsigned b = blockIdx.x;
  constexpr unsigned kPx = 0 ? 1u : 8u;
  const unsigned h = 0 ? (b & 1) : ((b >> 3) & 1), rb = 0 ? (b >> 1) : ((b >> 4) * 8 + (b & 7));
  const unsigned w = __builtin_amdgcn_readfirstlane(threadIdx.x >> 6);
  const unsigned col = (512 * h + 128 * w) * 2;
  const unsigned long long wsb = (unsigned long long)wsp;
  const unsigned long long xo = (unsigned long long)(x0 + (size_t)rb * 128 * kD);
  const unsigned long long ob = (unsigned long long)(out + (size_t)rb * 128 * kD) + col;
  const unsigned long long wr = (unsigned long long)ws + col;
  const unsigned long long bb = (unsigned long long)bs;
  const unsigned wo = ((unsigned)wsb > ~(15u << 20) ? 16u << 20 : kWOff) + (h * 4 + w) * kWStream;
  const unsigned xb = kXchOff + ((0u - (unsigned)wsb) & 0x1fffffu);
  const unsigned eo = xb + b * 524288 + w * 32768, ep = xb + (b ^ kPx) * 524288 + w * 32768;
  const unsigned fo = b * 512 + w * 128, fp = (b ^ kPx) * 512 + w * 128;
  const unsigned db = kDbgOff + (b * 4 + w) * 512;
  const unsigned v1 = rb + 12;
  const unsigned rs1 = __builtin_amdgcn_readfirstlane(unit_src(v1, h, w)), rs2 = __builtin_amdgcn_readfirstlane(unit_src(64 + rb, h, w));
  const unsigned rd1 = (v1 < 64 ? v1 : 192u) * 8192, rn1 = v1 < 64 ? 12582912u - 1792u : 0u;
  const unsigned wfa = kWFlagOff + (4 * h + w) * 1024, wfo = wfa + rb * 8;
  const unsigned ldsa = (unsigned)(size_t)(__attribute__((address_space(3))) unsigned char*)lds;
#define LO(x) "s"((unsigned)(x))
#define HI(x) "s"((unsigned)((x) >> 32))
  asm volatile(
"L_main_entry:\n"
"v_and_b32 v12, 63, %18\n"
"s_getpc_b64 s[96:97]\n"
"L_pc:\n"
"s_lshr_b32 s66, %26, 3\n"
"s_lshl_b32 s66, s66, 2\n"
"s_add_u32 s66, s66, %16\n"
"s_mul_i32 s66, s66, 640\n"
"s_and_b32 s97, s97, 0xffff\n"
"s_mov_b32 s98, L_end-L_pc\n"
"s_mov_b32 s99, 0x20000\n"
"s_min_u32 s66, s66, L_end-L_pc-640\n"
"v_min_u32 v13, 9, v12\n"
"v_lshlrev_b32 v13, 6, v13\n"
"buffer_load_dword v11, v13, s[96:99], s66 offen\n"
"s_mov_b32 s56, %0\n"
"s_and_b32 s57, %1, 0xffff\n"
"s_mov_b32 s58, 171966464\n"
"s_mov_b32 s59, 0x20000\n"
"s_mov_b32 s66, 139264\n"
"buffer_load_dwordx2 v[32:33], off, s[56:59], s66 sc1\n"
"s_mov_b32 s52, %6\n"
"s_and_b32 s53, %7, 0xffff\n"
"s_mov_b32 s54, 12581120\n"
"s_mov_b32 s55, 0x20000\n"
"v_lshrrev_b32 v13, 3, v12\n"
"v_and_b32 v15, 7, v12\n"
"v_xor_b32 v15, v15, v13\n"
"v_lshlrev_b32 v13, 11, v13\n"
"v_lshl_add_u32 v6, v15, 4, v13\n"
"s_mul_i32 s73, %16, 40960\n"
"s_add_u32 s64, s73, %15\n"
"s_andn2_b32 s74, 1024, %17\n"
"s_lshl_b32 s75, s74, 10\n"
"s_lshr_b32 s66, %26, 4\n"
"s_lshl_b32 s66, s66, 2\n"
"s_add_u32 s66, s66, %16\n"
"s_mul_i32 s66, s66, 12288\n"
"s_add_u32 s66, s66, s75\n"
"s_lshl_b32 s67, %16, 8\n"
"s_sub_u32 s66, s66, s67\n"
"v_min_u32 v14, 47, v12\n"
"v_lshrrev_b32 v15, 3, v14\n"
"v_and_b32 v14, 7, v14\n"
"v_lshlrev_b32 v14, 7, v14\n"
"v_lshl_add_u32 v14, v15, 11, v14\n"
"buffer_load_dword v11, v14, s[52:55], s66 offen\n"
"s_mov_b32 s96, s52\n"
"s_mov_b32 s97, s53\n"
"s_mov_b32 s98, %23\n"
"s_mov_b32 s99, 0x20000\n"
"s_add_u32 m0, s64, 32768\n"
"s_add_u32 s67, %20, 0\n"
"buffer_load_dwordx4 v6, s[96:99], s67 offen offset:0  lds\n"
"s_add_u32 s67, %20, 15360\n"
"buffer_load_dwordx4 v6, s[96:99], s67 offen offset:1024  lds\n"
"s_add_u32 s67, %20, 30720\n"
"buffer_load_dwordx4 v6, s[96:99], s67 offen offset:2048  lds\n"
"s_add_u32 s67, %20, 46080\n"
"buffer_load_dwordx4 v6, s[96:99], s67 offen offset:3072  lds\n"
"s_add_u32 m0, s64, 36864\n"
"s_add_u32 s67, %20, 65536\n"
"buffer_load_dwordx4 v6, s[96:99], s67 offen offset:0  lds\n"
"s_add_u32 s67, %20, 80896\n"
"buffer_load_dwordx4 v6, s[96:99], s67 offen offset:1024  lds\n"
"s_add_u32 s67, %20, 96256\n"
"buffer_load_dwordx4 v6, s[96:99], s67 offen offset:2048  lds\n"
"s_add_u32 s67, %20, 111616\n"
"buffer_load_dwordx4 v6, s[96:99], s67 offen offset:3072  lds\n"
"s_add_u32 m0, s64, 0\n"
"s_add_u32 s67, s75, 0\n"
"buffer_load_dwordx4 v6, s[52:55], s67 offen offset:0  lds\n"
"s_add_u32 s67, s75, 15360\n"
"buffer_load_dwordx4 v6, s[52:55], s67 offen offset:1024  lds\n"
"s_add_u32 s67, s75, 30720\n"
"buffer_load_dwordx4 v6, s[52:55], s67 offen offset:2048  lds\n"
"s_add_u32 s67, s75, 46080\n"
"buffer_load_dwordx4 v6, s[52:55], s67 offen offset:3072  lds\n"
"s_add_u32 m0, s64, 4096\n"
"s_add_u32 s67, s75, 65536\n"
"buffer_load_dwordx4 v6, s[52:55], s67 offen offset:0  lds\n"
"s_add_u32 s67, s75, 80896\n"
"buffer_load_dwordx4 v6, s[52:55], s67 offen offset:1024  lds\n"
"s_add_u32 s67, s75, 96256\n"
"buffer_load_dwordx4 v6, s[52:55], s67 offen offset:2048  lds\n"
"s_add_u32 s67, s75, 111616\n"
"buffer_load_dwordx4 v6, s[52:55], s67 offen offset:3072  lds\n"
"s_mov_b32 s48, %2\n"
"s_and_b32 s49, %3, 0xffff\n"
"s_mov_b32 s50, 64512\n"
"s_mov_b32 s51, 0x20000\n"
"s_lshl_b32 s66, %16, 16\n"
"s_add_u32 s66, s66, s74\n"
"s_add_u32 s48, s48, s66\n"
"s_addc_u32 s49, s49, 0\n"
"s_lshl_b32 s72, %16, 12\n"
"s_add_u32 s72, s72, %15\n"
"s_add_u32 m0, s72, 16384\n"
"s_mov_b32 s67, 0\n"
"buffer_load_dwordx4 v6, s[48:51], s67 offen offset:0  lds\n"
"s_mov_b32 s67, 15360\n"
"buffer_load_dwordx4 v6, s[48:51], s67 offen offset:1024  lds\n"
"s_mov_b32 s67, 30720\n"
"buffer_load_dwordx4 v6, s[48:51], s67 offen offset:2048  lds\n"
"s_mov_b32 s67, 46080\n"
"buffer_load_dwordx4 v6, s[48:51], s67 offen offset:3072  lds\n"
"s_mov_b32 s44, %8\n"
"s_and_b32 s45, %9, 0xffff\n"
"s_mov_b32 s46, 12288\n"
"s_mov_b32 s47, 0x20000\n"
"v_lshlrev_b32 v0, 4, v12\n"
"s_mov_b32 s61, %17\n"
"v_lshrrev_b32 v16, 4, v0\n"
"v_and_b32 v16, 0x30, v16\n"
"buffer_load_dwordx2 v[18:19], v16, s[44:47], s61 offen offset:0\n"
"v_lshrrev_b32 v20, 4, v0\n"
"v_and_b32 v20, 0x30, v20\n"
"buffer_load_dwordx2 v[22:23], v20, s[44:47], s61 offen offset:8\n"
"v_lshrrev_b32 v24, 4, v0\n"
"v_and_b32 v24, 0x30, v24\n"
"buffer_load_dwordx2 v[26:27], v24, s[44:47], s61 offen offset:64\n"
"v_lshrrev_b32 v28, 4, v0\n"
"v_and_b32 v28, 0x30, v28\n"
"buffer_load_dwordx2 v[30:31], v28, s[44:47], s61 offen offset:72\n"
"s_add_u32 m0, s64, 8192\n"
"s_add_u32 s67, s75, 128\n"
"buffer_load_dwordx4 v6, s[52:55], s67 offen offset:0  lds\n"
"s_add_u32 s67, s75, 15488\n"
"buffer_load_dwordx4 v6, s[52:55], s67 offen offset:1024  lds\n"
"s_add_u32 s67, s75, 30848\n"
"buffer_load_dwordx4 v6, s[52:55], s67 offen offset:2048  lds\n"
"s_add_u32 s67, s75, 46208\n"
"buffer_load_dwordx4 v6, s[52:55], s67 offen offset:3072  lds\n"
"s_add_u32 m0, s64, 12288\n"
"s_add_u32 s67, s75, 65664\n"
"buffer_load_dwordx4 v6, s[52:55], s67 offen offset:0  lds\n"
"s_add_u32 s67, s75, 81024\n"
"buffer_load_dwordx4 v6, s[52:55], s67 offen offset:1024  lds\n"
"s_add_u32 s67, s75, 96384\n"
"buffer_load_dwordx4 v6, s[52:55], s67 offen offset:2048  lds\n"
"s_add_u32 s67, s75, 111744\n"
"buffer_load_dwordx4 v6, s[52:55], s67 offen offset:3072  lds\n"
"s_add_u32 m0, s72, 57344\n"
"s_mov_b32 s67, 128\n"
"buffer_load_dwordx4 v6, s[48:51], s67 offen offset:0  lds\n"
"s_mov_b32 s67, 15488\n"
"buffer_load_dwordx4 v6, s[48:51], s67 offen offset:1024  lds\n"
"s_mov_b32 s67, 30848\n"
"buffer_load_dwordx4 v6, s[48:51], s67 offen offset:2048  lds\n"
"s_mov_b32 s67, 46208\n"
"buffer_load_dwordx4 v6, s[48:51], s67 offen offset:3072  lds\n"
"v_add_u32 v1, 4096, v0\n"
"s_add_u32 s36, s56, %10\n"
"s_addc_u32 s37, s57, 0\n"
"s_mov_b32 s38, 1572864\n"
"s_mov_b32 s39, 0x20000\n"
"s_mov_b32 s60, 98304\n"
"s_add_u32 s34, s36, 102400\n"
"s_mov_b32 s35, s37\n"
"v_and_b32 v13, 15, v12\n"
"v_lshrrev_b32 v14, 4, v12\n"
"v_and_b32 v15, 7, v12\n"
"v_xor_b32 v15, v15, v14\n"
"v_lshlrev_b32 v7, 7, v13\n"
"v_lshl_add_u32 v7, v15, 4, v7\n"
"v_add_u32 v7, s64, v7\n"
"v_xor_b32 v8, 64, v7\n"
"v_bfe_u32 v13, v12, 2, 2\n"
"v_and_b32 v15, 3, v12\n"
"v_xor_b32 v15, v15, v13\n"
"v_lshl_add_u32 v13, v14, 3, v13\n"
"v_lshlrev_b32 v13, 7, v13\n"
"v_lshl_add_u32 v9, v15, 4, v13\n"
"v_add_u32 v9, s64, v9\n"
"s_mov_b32 s40, %2\n"
"s_and_b32 s41, %3, 0xffff\n"
"s_mov_b32 s42, 260352\n"
"s_mov_b32 s43, 0x20000\n"
"s_add_u32 s40, s40, %17\n"
"s_addc_u32 s41, s41, 0\n"
"s_mov_b32 s80, 0\n"
"s_mov_b32 s81, 15360\n"
"s_mov_b32 s82, 30720\n"
"s_mov_b32 s83, 46080\n"
"s_mov_b32 s84, 65536\n"
"s_mov_b32 s85, 80896\n"
"s_mov_b32 s86, 96256\n"
"s_mov_b32 s87, 111616\n"
"s_mov_b32 s88, 131072\n"
"s_mov_b32 s89, 146432\n"
"s_mov_b32 s90, 161792\n"
"s_mov_b32 s91, 177152\n"
"s_mov_b32 s92, 196608\n"
"s_mov_b32 s93, 211968\n"
"s_mov_b32 s94, 227328\n"
"s_mov_b32 s95, 242688\n"
"s_mov_b32 s62, 0\n"
"s_mov_b32 s63, 0xffff0000\n"
"s_add_u32 s66, %16, 0\n"
"s_and_b32 s66, s66, 3\n"
"s_mul_i32 s66, s66, 40960\n"
"s_add_u32 s66, s66, %15\n"
"v_add_u32 v2, s66, v0\n"
"s_add_u32 s66, %16, 1\n"
"s_and_b32 s66, s66, 3\n"
"s_mul_i32 s66, s66, 40960\n"
"s_add_u32 s66, s66, %15\n"
"v_add_u32 v3, s66, v0\n"
"s_add_u32 s66, %16, 2\n"
"s_and_b32 s66, s66, 3\n"
"s_mul_i32 s66, s66, 40960\n"
"s_add_u32 s66, s66, %15\n"
"v_add_u32 v4, s66, v0\n"
"s_add_u32 s66, %16, 3\n"
"s_and_b32 s66, s66, 3\n"
"s_mul_i32 s66, s66, 40960\n"
"s_add_u32 s66, s66, %15\n"
"v_add_u32 v5, s66, v0\n"
"s_sub_u32 s66, 0, s73\n"
"v_add_u32 v12, s66, v7\n"
"v_add_u32 v13, s66, v8\n"
"s_waitcnt vmcnt(37)\n"
"v_readfirstlane_b32 s78, v32\n"
"v_readfirstlane_b32 s79, v33\n"
"s_mov_b32 s68, 0x8000\n"
"s_nop 3\n"
"s_add_u32 s78, s78, 16\n"
"s_add_u32 s79, s79, 16\n"
"s_mov_b32 s65, s78\n"
"s_waitcnt vmcnt(28)\n"
"ds_read_b64_tr_b16 a[192:193], v9 offset:32768\n"
"ds_read_b64_tr_b16 a[194:195], v9 offset:33344\n"
"ds_read_b64_tr_b16 a[196:197], v9 offset:36864\n"
"ds_read_b64_tr_b16 a[198:199], v9 offset:37440\n"
"ds_read_b64_tr_b16 a[200:201], v9 offset:32776\n"
"ds_read_b64_tr_b16 a[202:203], v9 offset:33352\n"
"ds_read_b64_tr_b16 a[204:205], v9 offset:36872\n"
"ds_read_b64_tr_b16 a[206:207], v9 offset:37448\n"
"ds_read_b64_tr_b16 a[208:209], v9 offset:32832\n"
"ds_read_b64_tr_b16 a[210:211], v9 offset:33280\n"
"ds_read_b64_tr_b16 a[212:213], v9 offset:36928\n"
"ds_read_b64_tr_b16 a[214:215], v9 offset:37376\n"
"ds_read_b64_tr_b16 a[216:217], v9 offset:32840\n"
"ds_read_b64_tr_b16 a[218:219], v9 offset:33288\n"
"ds_read_b64_tr_b16 a[220:221], v9 offset:36936\n"
"ds_read_b64_tr_b16 a[222:223], v9 offset:37384\n"
"s_waitcnt vmcnt(20)\n"
"ds_read_b64_tr_b16 a[32:33], v9 offset:0\n"
"ds_read_b64_tr_b16 a[34:35], v9 offset:576\n"
"ds_read_b64_tr_b16 a[36:37], v9 offset:4096\n"
"ds_read_b64_tr_b16 a[38:39], v9 offset:4672\n"
"ds_read_b64_tr_b16 a[40:41], v9 offset:8\n"
"ds_read_b64_tr_b16 a[42:43], v9 offset:584\n"
"ds_read_b64_tr_b16 a[44:45], v9 offset:4104\n"
"ds_read_b64_tr_b16 a[46:47], v9 offset:4680\n"
"s_waitcnt lgkmcnt(8)\n"
"s_add_u32 m0, s64, 32768\n"
"s_add_u32 s67, s75, 131072\n"
"buffer_load_dwordx4 v6, s[52:55], s67 offen offset:0  lds\n"
"s_add_u32 s67, s75, 146432\n"
"buffer_load_dwordx4 v6, s[52:55], s67 offen offset:1024  lds\n"
"s_add_u32 s67, s75, 161792\n"
"buffer_load_dwordx4 v6, s[52:55], s67 offen offset:2048  lds\n"
"s_add_u32 s67, s75, 177152\n"
"buffer_load_dwordx4 v6, s[52:55], s67 offen offset:3072  lds\n"
"s_add_u32 m0, s64, 36864\n"
"s_add_u32 s67, s75, 196608\n"
"buffer_load_dwordx4 v6, s[52:55], s67 offen offset:0  lds\n"
"s_add_u32 s67, s75, 211968\n"
"buffer_load_dwordx4 v6, s[52:55], s67 offen offset:1024  lds\n"
"s_add_u32 s67, s75, 227328\n"
"buffer_load_dwordx4 v6, s[52:55], s67 offen offset:2048  lds\n"
"s_add_u32 s67, s75, 242688\n"
"buffer_load_dwordx4 v6, s[52:55], s67 offen offset:3072  lds\n"
"s_waitcnt vmcnt(24)\n"
"s_barrier\n"
"ds_read_b128 a[100:103], v13 offset:16384\n"
"ds_read_b128 a[96:99], v12 offset:16384\n"
"ds_read_b128 a[108:111], v13 offset:18432\n"
"ds_read_b128 a[104:107], v12 offset:18432\n"
"ds_read_b128 a[116:119], v13 offset:20480\n"
"ds_read_b128 a[112:115], v12 offset:20480\n"
"ds_read_b128 a[124:127], v13 offset:22528\n"
"ds_read_b128 a[120:123], v12 offset:22528\n"
"ds_read_b128 a[132:135], v13 offset:24576\n"
"ds_read_b128 a[128:131], v12 offset:24576\n"
"ds_read_b128 a[140:143], v13 offset:26624\n"
"ds_read_b128 a[136:139], v12 offset:26624\n"
"ds_read_b128 a[148:151], v13 offset:28672\n"
"ds_read_b128 a[144:147], v12 offset:28672\n"
"ds_read_b128 a[156:159], v13 offset:30720\n"
"ds_read_b128 a[152:155], v12 offset:30720\n"
"s_waitcnt vmcnt(20)\n"
"v_lshlrev_b32 v16, 16, v18\n"
"v_and_b32 v17, s63, v18\n"
"v_lshlrev_b32 v18, 16, v19\n"
"v_and_b32 v19, s63, v19\n"
"v_add_f32 v16, 1.0, v16\n"
"v_add_f32 v17, 1.0, v17\n"
"v_add_f32 v18, 1.0, v18\n"
"v_add_f32 v19, 1.0, v19\n"
"v_lshlrev_b32 v20, 16, v22\n"
"v_and_b32 v21, s63, v22\n"
"v_lshlrev_b32 v22, 16, v23\n"
"v_and_b32 v23, s63, v23\n"
"v_add_f32 v20, 1.0, v20\n"
"v_add_f32 v21, 1.0, v21\n"
"v_add_f32 v22, 1.0, v22\n"
"v_add_f32 v23, 1.0, v23\n"
"v_lshlrev_b32 v24, 16, v26\n"
"v_and_b32 v25, s63, v26\n"
"v_lshlrev_b32 v26, 16, v27\n"
"v_and_b32 v27, s63, v27\n"
"v_add_f32 v24, 1.0, v24\n"
"v_add_f32 v25, 1.0, v25\n"
"v_add_f32 v26, 1.0, v26\n"
"v_add_f32 v27, 1.0, v27\n"
"v_lshlrev_b32 v28, 16, v30\n"
"v_and_b32 v29, s63, v30\n"
"v_lshlrev_b32 v30, 16, v31\n"
"v_and_b32 v31, s63, v31\n"
"v_add_f32 v28, 1.0, v28\n"
"v_add_f32 v29, 1.0, v29\n"
"v_add_f32 v30, 1.0, v30\n"
"v_add_f32 v31, 1.0, v31\n"
"v_accvgpr_write_b32 a0, v16\n"
"v_accvgpr_write_b32 a1, v17\n"
"v_accvgpr_write_b32 a2, v18\n"
"v_accvgpr_write_b32 a3, v19\n"
"v_accvgpr_write_b32 a4, v20\n"
"v_accvgpr_write_b32 a5, v21\n"
"v_accvgpr_write_b32 a6, v22\n"
"v_accvgpr_write_b32 a7, v23\n"
"v_accvgpr_write_b32 a8, v24\n"
"v_accvgpr_write_b32 a9, v25\n"
"v_accvgpr_write_b32 a10, v26\n"
"v_accvgpr_write_b32 a11, v27\n"
"v_accvgpr_write_b32 a12, v28\n"
"v_accvgpr_write_b32 a13, v29\n"
"v_accvgpr_write_b32 a14, v30\n"
"v_accvgpr_write_b32 a15, v31\n"
"s_nop 1\n"
"s_waitcnt lgkmcnt(12)\n"
"v_mfma_f32_16x16x32_bf16 v[32:35], a[32:35], a[96:99], v[16:19]\n"
"ds_read_b64_tr_b16 a[48:49], v9 offset:64\n"
"v_mfma_f32_16x16x32_bf16 v[32:35], a[36:39], a[100:103], v[32:35]\n"
"ds_read_b64_tr_b16 a[50:51], v9 offset:512\n"
"v_mfma_f32_16x16x32_bf16 v[60:63], a[44:47], a[100:103], v[20:23]\n"
"ds_read_b64_tr_b16 a[52:53], v9 offset:4160\n"
"v_mfma_f32_16x16x32_bf16 v[60:63], a[40:43], a[96:99], v[60:63]\n"
"ds_read_b64_tr_b16 a[54:55], v9 offset:4608\n"
"v_mfma_f32_16x16x32_bf16 v[64:67], a[40:43], a[104:107], v[20:23]\n"
"ds_read_b64_tr_b16 a[56:57], v9 offset:72\n"
"v_mfma_f32_16x16x32_bf16 v[64:67], a[44:47], a[108:111], v[64:67]\n"
"ds_read_b64_tr_b16 a[58:59], v9 offset:520\n"
"v_mfma_f32_16x16x32_bf16 v[36:39], a[36:39], a[108:111], v[16:19]\n"
"ds_read_b64_tr_b16 a[60:61], v9 offset:4168\n"
"v_mfma_f32_16x16x32_bf16 v[36:39], a[32:35], a[104:107], v[36:39]\n"
"s_waitcnt lgkmcnt(15)\n"
"ds_read_b64_tr_b16 a[62:63], v9 offset:4616\n"
"v_mfma_f32_16x16x32_bf16 v[40:43], a[32:35], a[112:115], v[16:19]\n"
"v_mfma_f32_16x16x32_bf16 v[40:43], a[36:39], a[116:119], v[40:43]\n"
"v_mfma_f32_16x16x32_bf16 v[68:71], a[44:47], a[116:119], v[20:23]\n"
"v_mfma_f32_16x16x32_bf16 v[68:71], a[40:43], a[112:115], v[68:71]\n"
"v_mfma_f32_16x16x32_bf16 v[72:75], a[40:43], a[120:123], v[20:23]\n"
"v_mfma_f32_16x16x32_bf16 v[72:75], a[44:47], a[124:127], v[72:75]\n"
"v_mfma_f32_16x16x32_bf16 v[44:47], a[36:39], a[124:127], v[16:19]\n"
"s_waitcnt vmcnt(12)\n"
"ds_read_b64_tr_b16 a[64:65], v9 offset:8192\n"
"v_mfma_f32_16x16x32_bf16 v[44:47], a[32:35], a[120:123], v[44:47]\n"
"s_waitcnt lgkmcnt(13)\n"
"ds_read_b64_tr_b16 a[66:67], v9 offset:8768\n"
"v_mfma_f32_16x16x32_bf16 v[48:51], a[32:35], a[128:131], v[16:19]\n"
"ds_read_b64_tr_b16 a[68:69], v9 offset:12288\n"
"v_mfma_f32_16x16x32_bf16 v[48:51], a[36:39], a[132:135], v[48:51]\n"
"s_add_u32 m0, s72, 98304\n"
"ds_read_b64_tr_b16 a[70:71], v9 offset:12864\n"
"v_mfma_f32_16x16x32_bf16 v[76:79], a[44:47], a[132:135], v[20:23]\n"
"ds_read_b64_tr_b16 a[72:73], v9 offset:8200\n"
"v_mfma_f32_16x16x32_bf16 v[76:79], a[40:43], a[128:131], v[76:79]\n"
"s_mov_b32 s67, 256\n"
"ds_read_b64_tr_b16 a[74:75], v9 offset:8776\n"
"v_mfma_f32_16x16x32_bf16 v[80:83], a[40:43], a[136:139], v[20:23]\n"
"ds_read_b64_tr_b16 a[76:77], v9 offset:12296\n"
"v_mfma_f32_16x16x32_bf16 v[80:83], a[44:47], a[140:143], v[80:83]\n"
"ds_read_b64_tr_b16 a[78:79], v9 offset:12872\n"
"v_mfma_f32_16x16x32_bf16 v[52:55], a[36:39], a[140:143], v[16:19]\n"
"buffer_load_dwordx4 v6, s[48:51], s67 offen offset:0  lds\n"
"v_mfma_f32_16x16x32_bf16 v[52:55], a[32:35], a[136:139], v[52:55]\n"
"s_waitcnt lgkmcnt(15)\n"
"v_mfma_f32_16x16x32_bf16 v[56:59], a[32:35], a[144:147], v[16:19]\n"
"s_mov_b32 s67, 15616\n"
"v_mfma_f32_16x16x32_bf16 v[56:59], a[36:39], a[148:151], v[56:59]\n"
"v_mfma_f32_16x16x32_bf16 v[84:87], a[44:47], a[148:151], v[20:23]\n"
"v_mfma_f32_16x16x32_bf16 v[84:87], a[40:43], a[144:147], v[84:87]\n"
"buffer_load_dwordx4 v6, s[48:51], s67 offen offset:1024  lds\n"
"v_mfma_f32_16x16x32_bf16 a[4:7], a[40:43], a[152:155], a[4:7]\n"
"s_mov_b32 s67, 30976\n"
"v_mfma_f32_16x16x32_bf16 a[4:7], a[44:47], a[156:159], a[4:7]\n"
"buffer_load_dwordx4 v6, s[48:51], s67 offen offset:2048  lds\n"
"v_mfma_f32_16x16x32_bf16 a[0:3], a[36:39], a[156:159], a[0:3]\n"
"ds_read_b64_tr_b16 a[80:81], v9 offset:8256\n"
"v_mfma_f32_16x16x32_bf16 a[0:3], a[32:35], a[152:155], a[0:3]\n"
"s_waitcnt lgkmcnt(9)\n"
"ds_read_b64_tr_b16 a[82:83], v9 offset:8704\n"
"v_mfma_f32_16x16x32_bf16 v[88:91], a[48:51], a[96:99], v[24:27]\n"
"ds_read_b64_tr_b16 a[84:85], v9 offset:12352\n"
"v_mfma_f32_16x16x32_bf16 v[88:91], a[52:55], a[100:103], v[88:91]\n"
"ds_read_b64_tr_b16 a[86:87], v9 offset:12800\n"
"v_mfma_f32_16x16x32_bf16 v[116:119], a[60:63], a[100:103], v[28:31]\n"
"ds_read_b64_tr_b16 a[88:89], v9 offset:8264\n"
"v_mfma_f32_16x16x32_bf16 v[116:119], a[56:59], a[96:99], v[116:119]\n"
"s_mov_b32 s67, 46336\n"
"ds_read_b64_tr_b16 a[90:91], v9 offset:8712\n"
"v_mfma_f32_16x16x32_bf16 v[120:123], a[56:59], a[104:107], v[28:31]\n"
"ds_read_b64_tr_b16 a[92:93], v9 offset:12360\n"
"v_mfma_f32_16x16x32_bf16 v[120:123], a[60:63], a[108:111], v[120:123]\n"
"ds_read_b64_tr_b16 a[94:95], v9 offset:12808\n"
"v_mfma_f32_16x16x32_bf16 v[92:95], a[52:55], a[108:111], v[24:27]\n"
"buffer_load_dwordx4 v6, s[48:51], s67 offen offset:3072  lds\n"
"v_mfma_f32_16x16x32_bf16 v[92:95], a[48:51], a[104:107], v[92:95]\n"
"v_mfma_f32_16x16x32_bf16 v[96:99], a[48:51], a[112:115], v[24:27]\n"
"v_mfma_f32_16x16x32_bf16 v[96:99], a[52:55], a[116:119], v[96:99]\n"
"s_add_u32 m0, s64, 0\n"
"v_mfma_f32_16x16x32_bf16 v[124:127], a[60:63], a[116:119], v[28:31]\n"
"v_mfma_f32_16x16x32_bf16 v[124:127], a[56:59], a[112:115], v[124:127]\n"
"s_add_u32 s67, s75, 131200\n"
"v_mfma_f32_16x16x32_bf16 v[128:131], a[56:59], a[120:123], v[28:31]\n"
"v_mfma_f32_16x16x32_bf16 v[128:131], a[60:63], a[124:127], v[128:131]\n"
"buffer_load_dwordx4 v6, s[52:55], s67 offen offset:0  lds\n"
"v_mfma_f32_16x16x32_bf16 v[100:103], a[52:55], a[124:127], v[24:27]\n"
"v_mfma_f32_16x16x32_bf16 v[100:103], a[48:51], a[120:123], v[100:103]\n"
"s_add_u32 s67, s75, 146560\n"
"v_mfma_f32_16x16x32_bf16 v[104:107], a[48:51], a[128:131], v[24:27]\n"
"v_mfma_f32_16x16x32_bf16 v[104:107], a[52:55], a[132:135], v[104:107]\n"
"buffer_load_dwordx4 v6, s[52:55], s67 offen offset:1024  lds\n"
"v_mfma_f32_16x16x32_bf16 v[132:135], a[60:63], a[132:135], v[28:31]\n"
"v_mfma_f32_16x16x32_bf16 v[132:135], a[56:59], a[128:131], v[132:135]\n"
"s_add_u32 s67, s75, 161920\n"
"v_mfma_f32_16x16x32_bf16 v[136:139], a[56:59], a[136:139], v[28:31]\n"
"v_mfma_f32_16x16x32_bf16 v[136:139], a[60:63], a[140:143], v[136:139]\n"
"buffer_load_dwordx4 v6, s[52:55], s67 offen offset:2048  lds\n"
"v_mfma_f32_16x16x32_bf16 v[108:111], a[52:55], a[140:143], v[24:27]\n"
"v_mfma_f32_16x16x32_bf16 v[108:111], a[48:51], a[136:139], v[108:111]\n"
"s_add_u32 s67, s75, 177280\n"
"v_mfma_f32_16x16x32_bf16 v[112:115], a[48:51], a[144:147], v[24:27]\n"
"v_mfma_f32_16x16x32_bf16 v[112:115], a[52:55], a[148:151], v[112:115]\n"
"v_mfma_f32_16x16x32_bf16 v[140:143], a[60:63], a[148:151], v[28:31]\n"
"v_mfma_f32_16x16x32_bf16 v[140:143], a[56:59], a[144:147], v[140:143]\n"
"v_mfma_f32_16x16x32_bf16 a[12:15], a[56:59], a[152:155], a[12:15]\n"
"buffer_store_dwordx4 a[192:195], v0, s[36:39], %22 offen offset:0 sc1\n"
"buffer_load_dwordx4 v6, s[52:55], s67 offen offset:3072  lds\n"
"s_add_u32 m0, s64, 4096\n"
"v_mfma_f32_16x16x32_bf16 a[12:15], a[60:63], a[156:159], a[12:15]\n"
"s_add_u32 s67, s75, 196736\n"
"v_mfma_f32_16x16x32_bf16 a[8:11], a[52:55], a[156:159], a[8:11]\n"
"v_mfma_f32_16x16x32_bf16 a[8:11], a[48:51], a[152:155], a[8:11]\n"
"s_waitcnt lgkmcnt(8)\n"
"buffer_load_dwordx4 v6, s[52:55], s67 offen offset:0  lds\n"
"v_mfma_f32_16x16x32_bf16 v[144:147], a[64:67], a[96:99], 1.0\n"
"v_mfma_f32_16x16x32_bf16 v[144:147], a[68:71], a[100:103], v[144:147]\n"
"s_add_u32 s67, s75, 212096\n"
"v_mfma_f32_16x16x32_bf16 v[172:175], a[76:79], a[100:103], 1.0\n"
"v_mfma_f32_16x16x32_bf16 v[172:175], a[72:75], a[96:99], v[172:175]\n"
"buffer_load_dwordx4 v6, s[52:55], s67 offen offset:1024  lds\n"
"v_mfma_f32_16x16x32_bf16 v[176:179], a[72:75], a[104:107], 1.0\n"
"v_mfma_f32_16x16x32_bf16 v[176:179], a[76:79], a[108:111], v[176:179]\n"
"s_add_u32 s67, s75, 227456\n"
"v_mfma_f32_16x16x32_bf16 v[148:151], a[68:71], a[108:111], 1.0\n"
"buffer_store_dwordx4 a[196:199], v0, s[36:39], %22 offen offset:1024 sc1\n"
"buffer_load_dwordx4 v6, s[52:55], s67 offen offset:2048  lds\n"
"v_mfma_f32_16x16x32_bf16 v[148:151], a[64:67], a[104:107], v[148:151]\n"
"v_mfma_f32_16x16x32_bf16 v[152:155], a[64:67], a[112:115], 1.0\n"
"v_mfma_f32_16x16x32_bf16 v[152:155], a[68:71], a[116:119], v[152:155]\n"
"s_add_u32 s67, s75, 242816\n"
"v_mfma_f32_16x16x32_bf16 v[180:183], a[76:79], a[116:119], 1.0\n"
"v_mfma_f32_16x16x32_bf16 v[180:183], a[72:75], a[112:115], v[180:183]\n"
"buffer_load_dwordx4 v6, s[52:55], s67 offen offset:3072  lds\n"
"v_mfma_f32_16x16x32_bf16 v[184:187], a[72:75], a[120:123], 1.0\n"
"v_mfma_f32_16x16x32_bf16 v[184:187], a[76:79], a[124:127], v[184:187]\n"
"v_mfma_f32_16x16x32_bf16 v[156:159], a[68:71], a[124:127], 1.0\n"
"v_mfma_f32_16x16x32_bf16 v[156:159], a[64:67], a[120:123], v[156:159]\n"
"v_mfma_f32_16x16x32_bf16 v[160:163], a[64:67], a[128:131], 1.0\n"
"buffer_store_dwordx4 a[200:203], v0, s[36:39], %22 offen offset:2048 sc1\n"
"v_mfma_f32_16x16x32_bf16 v[160:163], a[68:71], a[132:135], v[160:163]\n"
"v_mfma_f32_16x16x32_bf16 v[188:191], a[76:79], a[132:135], 1.0\n"
"v_mfma_f32_16x16x32_bf16 v[188:191], a[72:75], a[128:131], v[188:191]\n"
"v_mfma_f32_16x16x32_bf16 v[192:195], a[72:75], a[136:139], 1.0\n"
"s_sub_u32 s66, 40960, s73\n"
"v_mfma_f32_16x16x32_bf16 v[192:195], a[76:79], a[140:143], v[192:195]\n"
"v_mfma_f32_16x16x32_bf16 v[164:167], a[68:71], a[140:143], 1.0\n"
"v_add_u32 v12, s66, v7\n"
"v_mfma_f32_16x16x32_bf16 v[164:167], a[64:67], a[136:139], v[164:167]\n"
"v_mfma_f32_16x16x32_bf16 v[168:171], a[64:67], a[144:147], 1.0\n"
"v_add_u32 v13, s66, v8\n"
"v_mfma_f32_16x16x32_bf16 v[168:171], a[68:71], a[148:151], v[168:171]\n"
"v_mfma_f32_16x16x32_bf16 v[196:199], a[76:79], a[148:151], 1.0\n"
"buffer_store_dwordx4 a[204:207], v0, s[36:39], %22 offen offset:3072 sc1\n"
"v_mfma_f32_16x16x32_bf16 v[196:199], a[72:75], a[144:147], v[196:199]\n"
"v_mfma_f32_16x16x32_bf16 a[20:23], a[72:75], a[152:155], 1.0\n"
"v_mfma_f32_16x16x32_bf16 a[20:23], a[76:79], a[156:159], a[20:23]\n"
"v_mfma_f32_16x16x32_bf16 a[16:19], a[68:71], a[156:159], 1.0\n"
"s_waitcnt vmcnt(24)\n"
"s_barrier\n"
"v_mfma_f32_16x16x32_bf16 a[16:19], a[64:67], a[152:155], a[16:19]\n"
"s_waitcnt lgkmcnt(0)\n"
"v_mfma_f32_16x16x32_bf16 v[200:203], a[80:83], a[96:99], 1.0\n"
"s_waitcnt vmcnt(16)\n"
"ds_read_b64_tr_b16 a[32:33], v9 offset:32768\n"
"v_mfma_f32_16x16x32_bf16 v[200:203], a[84:87], a[100:103], v[200:203]\n"
"s_add_u32 m0, s72, 139264\n"
"ds_read_b64_tr_b16 a[34:35], v9 offset:33344\n"
"v_mfma_f32_16x16x32_bf16 v[228:231], a[92:95], a[100:103], 1.0\n"
"s_mov_b32 s67, 384\n"
"ds_read_b128 a[100:103], v13 offset:16384\n"
"v_mfma_f32_16x16x32_bf16 v[228:231], a[88:91], a[96:99], v[228:231]\n"
"ds_read_b128 a[96:99], v12 offset:16384\n"
"v_mfma_f32_16x16x32_bf16 v[232:235], a[88:91], a[104:107], 1.0\n"
"buffer_store_dwordx4 a[208:211], v1, s[36:39], %22 offen offset:0 sc1\n"
"ds_read_b64_tr_b16 a[36:37], v9 offset:36864\n"
"buffer_load_dwordx4 v6, s[48:51], s67 offen offset:0  lds\n"
"v_mfma_f32_16x16x32_bf16 v[232:235], a[92:95], a[108:111], v[232:235]\n"
"ds_read_b64_tr_b16 a[38:39], v9 offset:37440\n"
"v_mfma_f32_16x16x32_bf16 v[204:207], a[84:87], a[108:111], 1.0\n"
"s_mov_b32 s67, 15744\n"
"ds_read_b128 a[108:111], v13 offset:18432\n"
"v_mfma_f32_16x16x32_bf16 v[204:207], a[80:83], a[104:107], v[204:207]\n"
"ds_read_b128 a[104:107], v12 offset:18432\n"
"v_mfma_f32_16x16x32_bf16 v[208:211], a[80:83], a[112:115], 1.0\n"
"ds_read_b64_tr_b16 a[40:41], v9 offset:32776\n"
"buffer_load_dwordx4 v6, s[48:51], s67 offen offset:1024  lds\n"
"v_mfma_f32_16x16x32_bf16 v[208:211], a[84:87], a[116:119], v[208:211]\n"
"ds_read_b64_tr_b16 a[42:43], v9 offset:33352\n"
"v_mfma_f32_16x16x32_bf16 v[236:239], a[92:95], a[116:119], 1.0\n"
"s_mov_b32 s67, 31104\n"
"ds_read_b128 a[116:119], v13 offset:20480\n"
"v_mfma_f32_16x16x32_bf16 v[236:239], a[88:91], a[112:115], v[236:239]\n"
"ds_read_b128 a[112:115], v12 offset:20480\n"
"v_mfma_f32_16x16x32_bf16 v[240:243], a[88:91], a[120:123], 1.0\n"
"ds_read_b64_tr_b16 a[44:45], v9 offset:36872\n"
"v_mfma_f32_16x16x32_bf16 v[240:243], a[92:95], a[124:127], v[240:243]\n"
"ds_read_b64_tr_b16 a[46:47], v9 offset:37448\n"
"v_mfma_f32_16x16x32_bf16 v[212:215], a[84:87], a[124:127], 1.0\n"
"buffer_store_dwordx4 a[212:215], v1, s[36:39], %22 offen offset:1024 sc1\n"
"ds_read_b128 a[124:127], v13 offset:22528\n"
"buffer_load_dwordx4 v6, s[48:51], s67 offen offset:2048  lds\n"
"v_mfma_f32_16x16x32_bf16 v[212:215], a[80:83], a[120:123], v[212:215]\n"
"s_mov_b32 s67, 46464\n"
"ds_read_b128 a[120:123], v12 offset:22528\n"
"v_mfma_f32_16x16x32_bf16 v[216:219], a[80:83], a[128:131], 1.0\n"
"buffer_load_dwordx4 v6, s[48:51], s67 offen offset:3072  lds\n"
"v_mfma_f32_16x16x32_bf16 v[216:219], a[84:87], a[132:135], v[216:219]\n"
"v_mfma_f32_16x16x32_bf16 v[244:247], a[92:95], a[132:135], 1.0\n"
"ds_read_b128 a[132:135], v13 offset:24576\n"
"v_mfma_f32_16x16x32_bf16 v[244:247], a[88:91], a[128:131], v[244:247]\n"
"s_add_u32 m0, s64, 8192\n"
"ds_read_b128 a[128:131], v12 offset:24576\n"
"v_mfma_f32_16x16x32_bf16 v[248:251], a[88:91], a[136:139], 1.0\n"
"v_mfma_f32_16x16x32_bf16 v[248:251], a[92:95], a[140:143], v[248:251]\n"
"s_add_u32 s67, s75, 262144\n"
"v_mfma_f32_16x16x32_bf16 v[220:223], a[84:87], a[140:143], 1.0\n"
"ds_read_b128 a[140:143], v13 offset:26624\n"
"v_mfma_f32_16x16x32_bf16 v[220:223], a[80:83], a[136:139], v[220:223]\n"
"ds_read_b128 a[136:139], v12 offset:26624\n"
"v_mfma_f32_16x16x32_bf16 v[224:227], a[80:83], a[144:147], 1.0\n"
"buffer_store_dwordx4 a[216:219], v1, s[36:39], %22 offen offset:2048 sc1\n"
"buffer_load_dwordx4 v6, s[52:55], s67 offen offset:0  lds\n"
"s_add_u32 s67, s75, 277504\n"
"v_mfma_f32_16x16x32_bf16 v[224:227], a[84:87], a[148:151], v[224:227]\n"
"buffer_load_dwordx4 v6, s[52:55], s67 offen offset:1024  lds\n"
"v_mfma_f32_16x16x32_bf16 v[252:255], a[92:95], a[148:151], 1.0\n"
"ds_read_b128 a[148:151], v13 offset:28672\n"
"v_mfma_f32_16x16x32_bf16 v[252:255], a[88:91], a[144:147], v[252:255]\n"
"ds_read_b128 a[144:147], v12 offset:28672\n"
"v_mfma_f32_16x16x32_bf16 a[28:31], a[88:91], a[152:155], 1.0\n"
"s_add_u32 s67, s75, 292864\n"
"v_mfma_f32_16x16x32_bf16 a[28:31], a[92:95], a[156:159], a[28:31]\n"
"buffer_load_dwordx4 v6, s[52:55], s67 offen offset:2048  lds\n"
"v_mfma_f32_16x16x32_bf16 a[24:27], a[84:87], a[156:159], 1.0\n"
"ds_read_b128 a[156:159], v13 offset:30720\n"
"v_mfma_f32_16x16x32_bf16 a[24:27], a[80:83], a[152:155], a[24:27]\n"
"s_waitcnt lgkmcnt(9)\n"
"ds_read_b128 a[152:155], v12 offset:30720\n"
"v_mfma_f32_16x16x32_bf16 v[32:35], a[32:35], a[96:99], v[32:35]\n"
"ds_read_b64_tr_b16 a[48:49], v9 offset:32832\n"
"v_mfma_f32_16x16x32_bf16 v[32:35], a[36:39], a[100:103], v[32:35]\n"
"s_add_u32 s67, s75, 308224\n"
"ds_read_b64_tr_b16 a[50:51], v9 offset:33280\n"
"v_mfma_f32_16x16x32_bf16 v[60:63], a[44:47], a[100:103], v[60:63]\n"
"buffer_store_dwordx4 a[220:223], v1, s[36:39], %22 offen offset:3072 sc1\n"
"buffer_load_dwordx4 v6, s[52:55], s67 offen offset:3072  lds\n"
"v_mfma_f32_16x16x32_bf16 v[60:63], a[40:43], a[96:99], v[60:63]\n"
"v_mfma_f32_16x16x32_bf16 v[64:67], a[40:43], a[104:107], v[64:67]\n"
"ds_read_b64_tr_b16 a[52:53], v9 offset:36928\n"
"v_mfma_f32_16x16x32_bf16 v[64:67], a[44:47], a[108:111], v[64:67]\n"
"s_add_u32 m0, s64, 12288\n"
"ds_read_b64_tr_b16 a[54:55], v9 offset:37376\n"
"v_mfma_f32_16x16x32_bf16 v[36:39], a[36:39], a[108:111], v[36:39]\n"
"v_mfma_f32_16x16x32_bf16 v[36:39], a[32:35], a[104:107], v[36:39]\n"
"s_add_u32 s67, s75, 327680\n"
"s_waitcnt lgkmcnt(12)\n"
"v_mfma_f32_16x16x32_bf16 v[40:43], a[32:35], a[112:115], v[40:43]\n"
"ds_read_b64_tr_b16 a[56:57], v9 offset:32840\n"
"v_mfma_f32_16x16x32_bf16 v[40:43], a[36:39], a[116:119], v[40:43]\n"
"ds_read_b64_tr_b16 a[58:59], v9 offset:33288\n"
"v_mfma_f32_16x16x32_bf16 v[68:71], a[44:47], a[116:119], v[68:71]\n"
"buffer_load_dwordx4 v6, s[52:55], s67 offen offset:0  lds\n"
"v_mfma_f32_16x16x32_bf16 v[68:71], a[40:43], a[112:115], v[68:71]\n"
"s_add_u32 s67, s75, 343040\n"
"v_mfma_f32_16x16x32_bf16 v[72:75], a[40:43], a[120:123], v[72:75]\n"
"ds_read_b64_tr_b16 a[60:61], v9 offset:36936\n"
"v_mfma_f32_16x16x32_bf16 v[72:75], a[44:47], a[124:127], v[72:75]\n"
"ds_read_b64_tr_b16 a[62:63], v9 offset:37384\n"
"v_mfma_f32_16x16x32_bf16 v[44:47], a[36:39], a[124:127], v[44:47]\n"
"buffer_load_dwordx4 v6, s[52:55], s67 offen offset:1024  lds\n"
"v_mfma_f32_16x16x32_bf16 v[44:47], a[32:35], a[120:123], v[44:47]\n"
"s_add_u32 s67, s75, 358400\n"
"s_waitcnt lgkmcnt(12)\n"
"v_mfma_f32_16x16x32_bf16 v[48:51], a[32:35], a[128:131], v[48:51]\n"
"v_mfma_f32_16x16x32_bf16 v[48:51], a[36:39], a[132:135], v[48:51]\n"
"buffer_load_dwordx4 v6, s[52:55], s67 offen offset:2048  lds\n"
"v_mfma_f32_16x16x32_bf16 v[76:79], a[44:47], a[132:135], v[76:79]\n"
"v_mfma_f32_16x16x32_bf16 v[76:79], a[40:43], a[128:131], v[76:79]\n"
"s_add_u32 s67, s75, 373760\n"
"v_mfma_f32_16x16x32_bf16 v[80:83], a[40:43], a[136:139], v[80:83]\n"
"v_mfma_f32_16x16x32_bf16 v[80:83], a[44:47], a[140:143], v[80:83]\n"
"buffer_load_dwordx4 v6, s[52:55], s67 offen offset:3072  lds\n"
"v_mfma_f32_16x16x32_bf16 v[52:55], a[36:39], a[140:143], v[52:55]\n"
"v_mfma_f32_16x16x32_bf16 v[52:55], a[32:35], a[136:139], v[52:55]\n"
"s_waitcnt lgkmcnt(8)\n"
"v_mfma_f32_16x16x32_bf16 v[56:59], a[32:35], a[144:147], v[56:59]\n"
"v_mfma_f32_16x16x32_bf16 v[56:59], a[36:39], a[148:151], v[56:59]\n"
"v_mfma_f32_16x16x32_bf16 v[84:87], a[44:47], a[148:151], v[84:87]\n"
"v_mfma_f32_16x16x32_bf16 v[84:87], a[40:43], a[144:147], v[84:87]\n"
"v_mfma_f32_16x16x32_bf16 a[4:7], a[40:43], a[152:155], a[4:7]\n"
"v_mfma_f32_16x16x32_bf16 a[4:7], a[44:47], a[156:159], a[4:7]\n"
"v_mfma_f32_16x16x32_bf16 a[0:3], a[36:39], a[156:159], a[0:3]\n"
"v_mfma_f32_16x16x32_bf16 a[0:3], a[32:35], a[152:155], a[0:3]\n"
"s_waitcnt lgkmcnt(0)\n"
"v_mfma_f32_16x16x32_bf16 v[88:91], a[48:51], a[96:99], v[88:91]\n"
"s_waitcnt vmcnt(18)\n"
"ds_read_b64_tr_b16 a[64:65], v9 offset:0\n"
"v_mfma_f32_16x16x32_bf16 v[88:91], a[52:55], a[100:103], v[88:91]\n"
"ds_read_b64_tr_b16 a[66:67], v9 offset:576\n"
"v_mfma_f32_16x16x32_bf16 v[116:119], a[60:63], a[100:103], v[116:119]\n"
"v_mfma_f32_16x16x32_bf16 v[116:119], a[56:59], a[96:99], v[116:119]\n"
"s_add_u32 m0, s64, 32768\n"
"v_mfma_f32_16x16x32_bf16 v[120:123], a[56:59], a[104:107], v[120:123]\n"
"ds_read_b64_tr_b16 a[68:69], v9 offset:4096\n"
"v_mfma_f32_16x16x32_bf16 v[120:123], a[60:63], a[108:111], v[120:123]\n"
"s_add_u32 s67, s75, 262272\n"
"ds_read_b64_tr_b16 a[70:71], v9 offset:4672\n"
"v_mfma_f32_16x16x32_bf16 v[92:95], a[52:55], a[108:111], v[92:95]\n"
"v_mfma_f32_16x16x32_bf16 v[92:95], a[48:51], a[104:107], v[92:95]\n"
"buffer_load_dwordx4 v6, s[52:55], s67 offen offset:0  lds\n"
"v_mfma_f32_16x16x32_bf16 v[96:99], a[48:51], a[112:115], v[96:99]\n"
"ds_read_b64_tr_b16 a[72:73], v9 offset:8\n"
"v_mfma_f32_16x16x32_bf16 v[96:99], a[52:55], a[116:119], v[96:99]\n"
"s_add_u32 s67, s75, 277632\n"
"ds_read_b64_tr_b16 a[74:75], v9 offset:584\n"
"v_mfma_f32_16x16x32_bf16 v[124:127], a[60:63], a[116:119], v[124:127]\n"
"v_mfma_f32_16x16x32_bf16 v[124:127], a[56:59], a[112:115], v[124:127]\n"
"buffer_load_dwordx4 v6, s[52:55], s67 offen offset:1024  lds\n"
"v_mfma_f32_16x16x32_bf16 v[128:131], a[56:59], a[120:123], v[128:131]\n"
"ds_read_b64_tr_b16 a[76:77], v9 offset:4104\n"
"v_mfma_f32_16x16x32_bf16 v[128:131], a[60:63], a[124:127], v[128:131]\n"
"s_add_u32 s67, s75, 292992\n"
"ds_read_b64_tr_b16 a[78:79], v9 offset:4680\n"
"v_mfma_f32_16x16x32_bf16 v[100:103], a[52:55], a[124:127], v[100:103]\n"
"v_mfma_f32_16x16x32_bf16 v[100:103], a[48:51], a[120:123], v[100:103]\n"
"buffer_load_dwordx4 v6, s[52:55], s67 offen offset:2048  lds\n"
"v_mfma_f32_16x16x32_bf16 v[104:107], a[48:51], a[128:131], v[104:107]\n"
"v_mfma_f32_16x16x32_bf16 v[104:107], a[52:55], a[132:135], v[104:107]\n"
"s_add_u32 s67, s75, 308352\n"
"v_mfma_f32_16x16x32_bf16 v[132:135], a[60:63], a[132:135], v[132:135]\n"
"v_mfma_f32_16x16x32_bf16 v[132:135], a[56:59], a[128:131], v[132:135]\n"
"buffer_load_dwordx4 v6, s[52:55], s67 offen offset:3072  lds\n"
"v_mfma_f32_16x16x32_bf16 v[136:139], a[56:59], a[136:139], v[136:139]\n"
"v_mfma_f32_16x16x32_bf16 v[136:139], a[60:63], a[140:143], v[136:139]\n"
"s_add_u32 m0, s64, 36864\n"
"v_mfma_f32_16x16x32_bf16 v[108:111], a[52:55], a[140:143], v[108:111]\n"
"v_mfma_f32_16x16x32_bf16 v[108:111], a[48:51], a[136:139], v[108:111]\n"
"s_add_u32 s67, s75, 327808\n"
"v_mfma_f32_16x16x32_bf16 v[112:115], a[48:51], a[144:147], v[112:115]\n"
"v_mfma_f32_16x16x32_bf16 v[112:115], a[52:55], a[148:151], v[112:115]\n"
"buffer_load_dwordx4 v6, s[52:55], s67 offen offset:0  lds\n"
"v_mfma_f32_16x16x32_bf16 v[140:143], a[60:63], a[148:151], v[140:143]\n"
"v_mfma_f32_16x16x32_bf16 v[140:143], a[56:59], a[144:147], v[140:143]\n"
"s_add_u32 s67, s75, 343168\n"
"v_mfma_f32_16x16x32_bf16 a[12:15], a[56:59], a[152:155], a[12:15]\n"
"v_mfma_f32_16x16x32_bf16 a[12:15], a[60:63], a[156:159], a[12:15]\n"
"buffer_load_dwordx4 v6, s[52:55], s67 offen offset:1024  lds\n"
"v_mfma_f32_16x16x32_bf16 a[8:11], a[52:55], a[156:159], a[8:11]\n"
"v_mfma_f32_16x16x32_bf16 a[8:11], a[48:51], a[152:155], a[8:11]\n"
"s_add_u32 s67, s75, 358528\n"
"s_waitcnt lgkmcnt(0)\n"
"v_mfma_f32_16x16x32_bf16 v[144:147], a[64:67], a[96:99], v[144:147]\n"
"ds_read_b64_tr_b16 a[80:81], v9 offset:64\n"
"v_mfma_f32_16x16x32_bf16 v[144:147], a[68:71], a[100:103], v[144:147]\n"
"ds_read_b64_tr_b16 a[82:83], v9 offset:512\n"
"v_mfma_f32_16x16x32_bf16 v[172:175], a[76:79], a[100:103], v[172:175]\n"
"buffer_load_dwordx4 v6, s[52:55], s67 offen offset:2048  lds\n"
"v_mfma_f32_16x16x32_bf16 v[172:175], a[72:75], a[96:99], v[172:175]\n"
"s_add_u32 s67, s75, 373888\n"
"v_mfma_f32_16x16x32_bf16 v[176:179], a[72:75], a[104:107], v[176:179]\n"
"ds_read_b64_tr_b16 a[84:85], v9 offset:4160\n"
"v_mfma_f32_16x16x32_bf16 v[176:179], a[76:79], a[108:111], v[176:179]\n"
"ds_read_b64_tr_b16 a[86:87], v9 offset:4608\n"
"v_mfma_f32_16x16x32_bf16 v[148:151], a[68:71], a[108:111], v[148:151]\n"
"buffer_load_dwordx4 v6, s[52:55], s67 offen offset:3072  lds\n"
"v_mfma_f32_16x16x32_bf16 v[148:151], a[64:67], a[104:107], v[148:151]\n"
"v_mfma_f32_16x16x32_bf16 v[152:155], a[64:67], a[112:115], v[152:155]\n"
"ds_read_b64_tr_b16 a[88:89], v9 offset:72\n"
"v_mfma_f32_16x16x32_bf16 v[152:155], a[68:71], a[116:119], v[152:155]\n"
"ds_read_b64_tr_b16 a[90:91], v9 offset:520\n"
"v_mfma_f32_16x16x32_bf16 v[180:183], a[76:79], a[116:119], v[180:183]\n"
"v_mfma_f32_16x16x32_bf16 v[180:183], a[72:75], a[112:115], v[180:183]\n"
"v_mfma_f32_16x16x32_bf16 v[184:187], a[72:75], a[120:123], v[184:187]\n"
"ds_read_b64_tr_b16 a[92:93], v9 offset:4168\n"
"v_mfma_f32_16x16x32_bf16 v[184:187], a[76:79], a[124:127], v[184:187]\n"
"ds_read_b64_tr_b16 a[94:95], v9 offset:4616\n"
"v_mfma_f32_16x16x32_bf16 v[156:159], a[68:71], a[124:127], v[156:159]\n"
"v_mfma_f32_16x16x32_bf16 v[156:159], a[64:67], a[120:123], v[156:159]\n"
"v_mfma_f32_16x16x32_bf16 v[160:163], a[64:67], a[128:131], v[160:163]\n"
"v_mfma_f32_16x16x32_bf16 v[160:163], a[68:71], a[132:135], v[160:163]\n"
"v_mfma_f32_16x16x32_bf16 v[188:191], a[76:79], a[132:135], v[188:191]\n"
"v_mfma_f32_16x16x32_bf16 v[188:191], a[72:75], a[128:131], v[188:191]\n"
"v_mfma_f32_16x16x32_bf16 v[192:195], a[72:75], a[136:139], v[192:195]\n"
"s_sub_u32 s66, 81920, s73\n"
"v_mfma_f32_16x16x32_bf16 v[192:195], a[76:79], a[140:143], v[192:195]\n"
"v_mfma_f32_16x16x32_bf16 v[164:167], a[68:71], a[140:143], v[164:167]\n"
"v_add_u32 v12, s66, v7\n"
"v_mfma_f32_16x16x32_bf16 v[164:167], a[64:67], a[136:139], v[164:167]\n"
"v_mfma_f32_16x16x32_bf16 v[168:171], a[64:67], a[144:147], v[168:171]\n"
"v_add_u32 v13, s66, v8\n"
"v_mfma_f32_16x16x32_bf16 v[168:171], a[68:71], a[148:151], v[168:171]\n"
"v_mfma_f32_16x16x32_bf16 v[196:199], a[76:79], a[148:151], v[196:199]\n"
"v_mfma_f32_16x16x32_bf16 v[196:199], a[72:75], a[144:147], v[196:199]\n"
"v_mfma_f32_16x16x32_bf16 a[20:23], a[72:75], a[152:155], a[20:23]\n"
"v_mfma_f32_16x16x32_bf16 a[20:23], a[76:79], a[156:159], a[20:23]\n"
"v_mfma_f32_16x16x32_bf16 a[16:19], a[68:71], a[156:159], a[16:19]\n"
"s_barrier\n"
"v_mfma_f32_16x16x32_bf16 a[16:19], a[64:67], a[152:155], a[16:19]\n"
"s_waitcnt lgkmcnt(0)\n"
"v_mfma_f32_16x16x32_bf16 v[200:203], a[80:83], a[96:99], v[200:203]\n"
"s_waitcnt vmcnt(8)\n"
"ds_read_b64_tr_b16 a[32:33], v9 offset:8192\n"
"v_mfma_f32_16x16x32_bf16 v[200:203], a[84:87], a[100:103], v[200:203]\n"
"s_add_u32 m0, s72, 16384\n"
"ds_read_b64_tr_b16 a[34:35], v9 offset:8768\n"
"v_mfma_f32_16x16x32_bf16 v[228:231], a[92:95], a[100:103], v[228:231]\n"
"s_mov_b32 s67, 512\n"
"ds_read_b128 a[100:103], v13 offset:16384\n"
"v_mfma_f32_16x16x32_bf16 v[228:231], a[88:91], a[96:99], v[228:231]\n"
"ds_read_b128 a[96:99], v12 offset:16384\n"
"v_mfma_f32_16x16x32_bf16 v[232:235], a[88:91], a[104:107], v[232:235]\n"
"ds_read_b64_tr_b16 a[36:37], v9 offset:12288\n"
"buffer_load_dwordx4 v6, s[48:51], s67 offen offset:0  lds\n"
"v_mfma_f32_16x16x32_bf16 v[232:235], a[92:95], a[108:111], v[232:235]\n"
"ds_read_b64_tr_b16 a[38:39], v9 offset:12864\n"
"v_mfma_f32_16x16x32_bf16 v[204:207], a[84:87], a[108:111], v[204:207]\n"
"s_mov_b32 s67, 15872\n"
"ds_read_b128 a[108:111], v13 offset:18432\n"
"v_mfma_f32_16x16x32_bf16 v[204:207], a[80:83], a[104:107], v[204:207]\n"
"ds_read_b128 a[104:107], v12 offset:18432\n"
"v_mfma_f32_16x16x32_bf16 v[208:211], a[80:83], a[112:115], v[208:211]\n"
"ds_read_b64_tr_b16 a[40:41], v9 offset:8200\n"
"buffer_load_dwordx4 v6, s[48:51], s67 offen offset:1024  lds\n"
"v_mfma_f32_16x16x32_bf16 v[208:211], a[84:87], a[116:119], v[208:211]\n"
"ds_read_b64_tr_b16 a[42:43], v9 offset:8776\n"
"v_mfma_f32_16x16x32_bf16 v[236:239], a[92:95], a[116:119], v[236:239]\n"
"s_mov_b32 s67, 31232\n"
"ds_read_b128 a[116:119], v13 offset:20480\n"
"v_mfma_f32_16x16x32_bf16 v[236:239], a[88:91], a[112:115], v[236:239]\n"
"ds_read_b128 a[112:115], v12 offset:20480\n"
"v_mfma_f32_16x16x32_bf16 v[240:243], a[88:91], a[120:123], v[240:243]\n"
"ds_read_b64_tr_b16 a[44:45], v9 offset:12296\n"
"buffer_load_dwordx4 v6, s[48:51], s67 offen offset:2048  lds\n"
"v_mfma_f32_16x16x32_bf16 v[240:243], a[92:95], a[124:127], v[240:243]\n"
"ds_read_b64_tr_b16 a[46:47], v9 offset:12872\n"
"v_mfma_f32_16x16x32_bf16 v[212:215], a[84:87], a[124:127], v[212:215]\n"
"s_mov_b32 s67, 46592\n"
"ds_read_b128 a[124:127], v13 offset:22528\n"
"v_mfma_f32_16x16x32_bf16 v[212:215], a[80:83], a[120:123], v[212:215]\n"
"ds_read_b128 a[120:123], v12 offset:22528\n"
"v_mfma_f32_16x16x32_bf16 v[216:219], a[80:83], a[128:131], v[216:219]\n"
"buffer_load_dwordx4 v6, s[48:51], s67 offen offset:3072  lds\n"
"v_mfma_f32_16x16x32_bf16 v[216:219], a[84:87], a[132:135], v[216:219]\n"
"v_mfma_f32_16x16x32_bf16 v[244:247], a[92:95], a[132:135], v[244:247]\n"
"ds_read_b128 a[132:135], v13 offset:24576\n"
"v_mfma_f32_16x16x32_bf16 v[244:247], a[88:91], a[128:131], v[244:247]\n"
"s_add_u32 m0, s64, 0\n"
"ds_read_b128 a[128:131], v12 offset:24576\n"
"v_mfma_f32_16x16x32_bf16 v[248:251], a[88:91], a[136:139], v[248:251]\n"
"s_add_u32 s67, s75, 393216\n"
"v_mfma_f32_16x16x32_bf16 v[248:251], a[92:95], a[140:143], v[248:251]\n"
"buffer_load_dwordx4 v6, s[52:55], s67 offen offset:0  lds\n"
"v_mfma_f32_16x16x32_bf16 v[220:223], a[84:87], a[140:143], v[220:223]\n"
"ds_read_b128 a[140:143], v13 offset:26624\n"
"v_mfma_f32_16x16x32_bf16 v[220:223], a[80:83], a[136:139], v[220:223]\n"
"ds_read_b128 a[136:139], v12 offset:26624\n"
"v_mfma_f32_16x16x32_bf16 v[224:227], a[80:83], a[144:147], v[224:227]\n"
"s_add_u32 s67, s75, 408576\n"
"v_mfma_f32_16x16x32_bf16 v[224:227], a[84:87], a[148:151], v[224:227]\n"
"buffer_load_dwordx4 v6, s[52:55], s67 offen offset:1024  lds\n"
"v_mfma_f32_16x16x32_bf16 v[252:255], a[92:95], a[148:151], v[252:255]\n"
"ds_read_b128 a[148:151], v13 offset:28672\n"
"v_mfma_f32_16x16x32_bf16 v[252:255], a[88:91], a[144:147], v[252:255]\n"
"ds_read_b128 a[144:147], v12 offset:28672\n"
"v_mfma_f32_16x16x32_bf16 a[28:31], a[88:91], a[152:155], a[28:31]\n"
"s_add_u32 s67, s75, 423936\n"
"v_mfma_f32_16x16x32_bf16 a[28:31], a[92:95], a[156:159], a[28:31]\n"
"buffer_load_dwordx4 v6, s[52:55], s67 offen offset:2048  lds\n"
"v_mfma_f32_16x16x32_bf16 a[24:27], a[84:87], a[156:159], a[24:27]\n"
"ds_read_b128 a[156:159], v13 offset:30720\n"
"v_mfma_f32_16x16x32_bf16 a[24:27], a[80:83], a[152:155], a[24:27]\n"
"s_waitcnt lgkmcnt(9)\n"
"ds_read_b128 a[152:155], v12 offset:30720\n"
"v_mfma_f32_16x16x32_bf16 v[32:35], a[32:35], a[96:99], v[32:35]\n"
"ds_read_b64_tr_b16 a[48:49], v9 offset:8256\n"
"v_mfma_f32_16x16x32_bf16 v[32:35], a[36:39], a[100:103], v[32:35]\n"
"s_add_u32 s67, s75, 439296\n"
"ds_read_b64_tr_b16 a[50:51], v9 offset:8704\n"
"v_mfma_f32_16x16x32_bf16 v[60:63], a[44:47], a[100:103], v[60:63]\n"
"v_mfma_f32_16x16x32_bf16 v[60:63], a[40:43], a[96:99], v[60:63]\n"
"buffer_load_dwordx4 v6, s[52:55], s67 offen offset:3072  lds\n"
"v_mfma_f32_16x16x32_bf16 v[64:67], a[40:43], a[104:107], v[64:67]\n"
"ds_read_b64_tr_b16 a[52:53], v9 offset:12352\n"
"v_mfma_f32_16x16x32_bf16 v[64:67], a[44:47], a[108:111], v[64:67]\n"
"s_add_u32 m0, s64, 4096\n"
"ds_read_b64_tr_b16 a[54:55], v9 offset:12800\n"
"v_mfma_f32_16x16x32_bf16 v[36:39], a[36:39], a[108:111], v[36:39]\n"
"v_mfma_f32_16x16x32_bf16 v[36:39], a[32:35], a[104:107], v[36:39]\n"
"s_add_u32 s67, s75, 458752\n"
"s_waitcnt lgkmcnt(12)\n"
"v_mfma_f32_16x16x32_bf16 v[40:43], a[32:35], a[112:115], v[40:43]\n"
"ds_read_b64_tr_b16 a[56:57], v9 offset:8264\n"
"v_mfma_f32_16x16x32_bf16 v[40:43], a[36:39], a[116:119], v[40:43]\n"
"ds_read_b64_tr_b16 a[58:59], v9 offset:8712\n"
"v_mfma_f32_16x16x32_bf16 v[68:71], a[44:47], a[116:119], v[68:71]\n"
"buffer_load_dwordx4 v6, s[52:55], s67 offen offset:0  lds\n"
"v_mfma_f32_16x16x32_bf16 v[68:71], a[40:43], a[112:115], v[68:71]\n"
"s_add_u32 s67, s75, 474112\n"
"v_mfma_f32_16x16x32_bf16 v[72:75], a[40:43], a[120:123], v[72:75]\n"
"ds_read_b64_tr_b16 a[60:61], v9 offset:12360\n"
"v_mfma_f32_16x16x32_bf16 v[72:75], a[44:47], a[124:127], v[72:75]\n"
"ds_read_b64_tr_b16 a[62:63], v9 offset:12808\n"
"v_mfma_f32_16x16x32_bf16 v[44:47], a[36:39], a[124:127], v[44:47]\n"
"buffer_load_dwordx4 v6, s[52:55], s67 offen offset:1024  lds\n"
"v_mfma_f32_16x16x32_bf16 v[44:47], a[32:35], a[120:123], v[44:47]\n"
"s_add_u32 s67, s75, 489472\n"
"s_waitcnt lgkmcnt(12)\n"
"v_mfma_f32_16x16x32_bf16 v[48:51], a[32:35], a[128:131], v[48:51]\n"
"v_mfma_f32_16x16x32_bf16 v[48:51], a[36:39], a[132:135], v[48:51]\n"
"buffer_load_dwordx4 v6, s[52:55], s67 offen offset:2048  lds\n"
"v_mfma_f32_16x16x32_bf16 v[76:79], a[44:47], a[132:135], v[76:79]\n"
"v_mfma_f32_16x16x32_bf16 v[76:79], a[40:43], a[128:131], v[76:79]\n"
"s_add_u32 s67, s75, 504832\n"
"v_mfma_f32_16x16x32_bf16 v[80:83], a[40:43], a[136:139], v[80:83]\n"
"v_mfma_f32_16x16x32_bf16 v[80:83], a[44:47], a[140:143], v[80:83]\n"
"buffer_load_dwordx4 v6, s[52:55], s67 offen offset:3072  lds\n"
"v_mfma_f32_16x16x32_bf16 v[52:55], a[36:39], a[140:143], v[52:55]\n"
"v_mfma_f32_16x16x32_bf16 v[52:55], a[32:35], a[136:139], v[52:55]\n"
"s_waitcnt lgkmcnt(8)\n"
"v_mfma_f32_16x16x32_bf16 v[56:59], a[32:35], a[144:147], v[56:59]\n"
"v_mfma_f32_16x16x32_bf16 v[56:59], a[36:39], a[148:151], v[56:59]\n"
"v_mfma_f32_16x16x32_bf16 v[84:87], a[44:47], a[148:151], v[84:87]\n"
"v_mfma_f32_16x16x32_bf16 v[84:87], a[40:43], a[144:147], v[84:87]\n"
"v_mfma_f32_16x16x32_bf16 a[4:7], a[40:43], a[152:155], a[4:7]\n"
"v_mfma_f32_16x16x32_bf16 a[4:7], a[44:47], a[156:159], a[4:7]\n"
"v_mfma_f32_16x16x32_bf16 a[0:3], a[36:39], a[156:159], a[0:3]\n"
"v_mfma_f32_16x16x32_bf16 a[0:3], a[32:35], a[152:155], a[0:3]\n"
"s_waitcnt lgkmcnt(0)\n"
"v_mfma_f32_16x16x32_bf16 v[88:91], a[48:51], a[96:99], v[88:91]\n"
"s_waitcnt vmcnt(12)\n"
"ds_read_b64_tr_b16 a[64:65], v9 offset:32768\n"
"v_mfma_f32_16x16x32_bf16 v[88:91], a[52:55], a[100:103], v[88:91]\n"
"ds_read_b64_tr_b16 a[66:67], v9 offset:33344\n"
"v_mfma_f32_16x16x32_bf16 v[116:119], a[60:63], a[100:103], v[116:119]\n"
"v_mfma_f32_16x16x32_bf16 v[116:119], a[56:59], a[96:99], v[116:119]\n"
"s_add_u32 m0, s64, 8192\n"
"v_mfma_f32_16x16x32_bf16 v[120:123], a[56:59], a[104:107], v[120:123]\n"
"ds_read_b64_tr_b16 a[68:69], v9 offset:36864\n"
"v_mfma_f32_16x16x32_bf16 v[120:123], a[60:63], a[108:111], v[120:123]\n"
"s_add_u32 s67, s75, 393344\n"
"ds_read_b64_tr_b16 a[70:71], v9 offset:37440\n"
"v_mfma_f32_16x16x32_bf16 v[92:95], a[52:55], a[108:111], v[92:95]\n"
"buffer_load_dwordx4 v6, s[52:55], s67 offen offset:0  lds\n"
"v_mfma_f32_16x16x32_bf16 v[92:95], a[48:51], a[104:107], v[92:95]\n"
"s_add_u32 s70, s78, 1\n"
"v_mfma_f32_16x16x32_bf16 v[96:99], a[48:51], a[112:115], v[96:99]\n"
"ds_read_b64_tr_b16 a[72:73], v9 offset:32776\n"
"v_mfma_f32_16x16x32_bf16 v[96:99], a[52:55], a[116:119], v[96:99]\n"
"s_add_u32 s67, s75, 408704\n"
"ds_read_b64_tr_b16 a[74:75], v9 offset:33352\n"
"v_mov_b32 v14, s70\n"
"v_mfma_f32_16x16x32_bf16 v[124:127], a[60:63], a[116:119], v[124:127]\n"
"v_mov_b32 v15, s79\n"
"v_mfma_f32_16x16x32_bf16 v[124:127], a[56:59], a[112:115], v[124:127]\n"
"buffer_load_dwordx4 v6, s[52:55], s67 offen offset:1024  lds\n"
"v_mfma_f32_16x16x32_bf16 v[128:131], a[56:59], a[120:123], v[128:131]\n"
"buffer_store_dwordx2 v[14:15], off, s[56:59], %24 sc1\n"
"ds_read_b64_tr_b16 a[76:77], v9 offset:36872\n"
"v_mfma_f32_16x16x32_bf16 v[128:131], a[60:63], a[124:127], v[128:131]\n"
"s_add_u32 s67, s75, 424064\n"
"ds_read_b64_tr_b16 a[78:79], v9 offset:37448\n"
"v_mfma_f32_16x16x32_bf16 v[100:103], a[52:55], a[124:127], v[100:103]\n"
"v_mfma_f32_16x16x32_bf16 v[100:103], a[48:51], a[120:123], v[100:103]\n"
"buffer_load_dwordx4 v6, s[52:55], s67 offen offset:2048  lds\n"
"v_mfma_f32_16x16x32_bf16 v[104:107], a[48:51], a[128:131], v[104:107]\n"
"v_mfma_f32_16x16x32_bf16 v[104:107], a[52:55], a[132:135], v[104:107]\n"
"s_add_u32 s67, s75, 439424\n"
"v_mfma_f32_16x16x32_bf16 v[132:135], a[60:63], a[132:135], v[132:135]\n"
"v_mfma_f32_16x16x32_bf16 v[132:135], a[56:59], a[128:131], v[132:135]\n"
"buffer_load_dwordx4 v6, s[52:55], s67 offen offset:3072  lds\n"
"v_mfma_f32_16x16x32_bf16 v[136:139], a[56:59], a[136:139], v[136:139]\n"
"v_mfma_f32_16x16x32_bf16 v[136:139], a[60:63], a[140:143], v[136:139]\n"
"s_add_u32 m0, s64, 12288\n"
"v_mfma_f32_16x16x32_bf16 v[108:111], a[52:55], a[140:143], v[108:111]\n"
"v_mfma_f32_16x16x32_bf16 v[108:111], a[48:51], a[136:139], v[108:111]\n"
"s_add_u32 s67, s75, 458880\n"
"v_mfma_f32_16x16x32_bf16 v[112:115], a[48:51], a[144:147], v[112:115]\n"
"v_mfma_f32_16x16x32_bf16 v[112:115], a[52:55], a[148:151], v[112:115]\n"
"buffer_load_dwordx4 v6, s[52:55], s67 offen offset:0  lds\n"
"v_mfma_f32_16x16x32_bf16 v[140:143], a[60:63], a[148:151], v[140:143]\n"
"v_mfma_f32_16x16x32_bf16 v[140:143], a[56:59], a[144:147], v[140:143]\n"
"s_add_u32 s67, s75, 474240\n"
"v_mfma_f32_16x16x32_bf16 a[12:15], a[56:59], a[152:155], a[12:15]\n"
"v_mfma_f32_16x16x32_bf16 a[12:15], a[60:63], a[156:159], a[12:15]\n"
"buffer_load_dwordx4 v6, s[52:55], s67 offen offset:1024  lds\n"
"v_mfma_f32_16x16x32_bf16 a[8:11], a[52:55], a[156:159], a[8:11]\n"
"v_mfma_f32_16x16x32_bf16 a[8:11], a[48:51], a[152:155], a[8:11]\n"
"s_add_u32 s67, s75, 489600\n"
"s_waitcnt lgkmcnt(0)\n"
"v_mfma_f32_16x16x32_bf16 v[144:147], a[64:67], a[96:99], v[144:147]\n"
"ds_read_b64_tr_b16 a[80:81], v9 offset:32832\n"
"v_mfma_f32_16x16x32_bf16 v[144:147], a[68:71], a[100:103], v[144:147]\n"
"ds_read_b64_tr_b16 a[82:83], v9 offset:33280\n"
"v_mfma_f32_16x16x32_bf16 v[172:175], a[76:79], a[100:103], v[172:175]\n"
"buffer_load_dwordx4 v6, s[52:55], s67 offen offset:2048  lds\n"
"v_mfma_f32_16x16x32_bf16 v[172:175], a[72:75], a[96:99], v[172:175]\n"
"s_add_u32 s67, s75, 504960\n"
"v_mfma_f32_16x16x32_bf16 v[176:179], a[72:75], a[104:107], v[176:179]\n"
"ds_read_b64_tr_b16 a[84:85], v9 offset:36928\n"
"v_mfma_f32_16x16x32_bf16 v[176:179], a[76:79], a[108:111], v[176:179]\n"
"ds_read_b64_tr_b16 a[86:87], v9 offset:37376\n"
"v_mfma_f32_16x16x32_bf16 v[148:151], a[68:71], a[108:111], v[148:151]\n"
"buffer_load_dwordx4 v6, s[52:55], s67 offen offset:3072  lds\n"
"v_mfma_f32_16x16x32_bf16 v[148:151], a[64:67], a[104:107], v[148:151]\n"
"v_mfma_f32_16x16x32_bf16 v[152:155], a[64:67], a[112:115], v[152:155]\n"
"ds_read_b64_tr_b16 a[88:89], v9 offset:32840\n"
"v_mfma_f32_16x16x32_bf16 v[152:155], a[68:71], a[116:119], v[152:155]\n"
"ds_read_b64_tr_b16 a[90:91], v9 offset:33288\n"
"v_mfma_f32_16x16x32_bf16 v[180:183], a[76:79], a[116:119], v[180:183]\n"
"v_mfma_f32_16x16x32_bf16 v[180:183], a[72:75], a[112:115], v[180:183]\n"
"v_mov_b32 v16, -1\n"
"v_mfma_f32_16x16x32_bf16 v[184:187], a[72:75], a[120:123], v[184:187]\n"
"ds_read_b64_tr_b16 a[92:93], v9 offset:36936\n"
"v_mfma_f32_16x16x32_bf16 v[184:187], a[76:79], a[124:127], v[184:187]\n"
"ds_read_b64_tr_b16 a[94:95], v9 offset:37384\n"
"v_mfma_f32_16x16x32_bf16 v[156:159], a[68:71], a[124:127], v[156:159]\n"
"v_mov_b32 v17, -1\n"
"v_mfma_f32_16x16x32_bf16 v[156:159], a[64:67], a[120:123], v[156:159]\n"
"v_mfma_f32_16x16x32_bf16 v[160:163], a[64:67], a[128:131], v[160:163]\n"
"v_mfma_f32_16x16x32_bf16 v[160:163], a[68:71], a[132:135], v[160:163]\n"
"v_mfma_f32_16x16x32_bf16 v[188:191], a[76:79], a[132:135], v[188:191]\n"
"v_mov_b32 v18, -1\n"
"v_mfma_f32_16x16x32_bf16 v[188:191], a[72:75], a[128:131], v[188:191]\n"
"v_mfma_f32_16x16x32_bf16 v[192:195], a[72:75], a[136:139], v[192:195]\n"
"s_sub_u32 s66, 122880, s73\n"
"v_mfma_f32_16x16x32_bf16 v[192:195], a[76:79], a[140:143], v[192:195]\n"
"v_mov_b32 v19, -1\n"
"v_mfma_f32_16x16x32_bf16 v[164:167], a[68:71], a[140:143], v[164:167]\n"
"v_add_u32 v12, s66, v7\n"
"v_mfma_f32_16x16x32_bf16 v[164:167], a[64:67], a[136:139], v[164:167]\n"
"v_mfma_f32_16x16x32_bf16 v[168:171], a[64:67], a[144:147], v[168:171]\n"
"s_add_u32 s69, %11, 8192\n"
"v_add_u32 v13, s66, v8\n"
"v_mfma_f32_16x16x32_bf16 v[168:171], a[68:71], a[148:151], v[168:171]\n"
"v_mfma_f32_16x16x32_bf16 v[196:199], a[76:79], a[148:151], v[196:199]\n"
"v_mfma_f32_16x16x32_bf16 v[196:199], a[72:75], a[144:147], v[196:199]\n"
"v_mfma_f32_16x16x32_bf16 a[20:23], a[72:75], a[152:155], a[20:23]\n"
"buffer_store_dwordx4 v[16:19], v0, s[56:59], %11 offen sc1\n"
"v_mfma_f32_16x16x32_bf16 a[20:23], a[76:79], a[156:159], a[20:23]\n"
"v_mfma_f32_16x16x32_bf16 a[16:19], a[68:71], a[156:159], a[16:19]\n"
"s_barrier\n"
"v_mfma_f32_16x16x32_bf16 a[16:19], a[64:67], a[152:155], a[16:19]\n"
"s_waitcnt lgkmcnt(0)\n"
"v_mfma_f32_16x16x32_bf16 v[200:203], a[80:83], a[96:99], v[200:203]\n"
"s_waitcnt vmcnt(10)\n"
"ds_read_b64_tr_b16 a[32:33], v9 offset:0\n"
"v_mfma_f32_16x16x32_bf16 v[200:203], a[84:87], a[100:103], v[200:203]\n"
"s_add_u32 m0, s72, 57344\n"
"ds_read_b64_tr_b16 a[34:35], v9 offset:576\n"
"v_mfma_f32_16x16x32_bf16 v[228:231], a[92:95], a[100:103], v[228:231]\n"
"s_mov_b32 s67, 640\n"
"ds_read_b128 a[100:103], v13 offset:16384\n"
"v_mfma_f32_16x16x32_bf16 v[228:231], a[88:91], a[96:99], v[228:231]\n"
"ds_read_b128 a[96:99], v12 offset:16384\n"
"v_mfma_f32_16x16x32_bf16 v[232:235], a[88:91], a[104:107], v[232:235]\n"
"ds_read_b64_tr_b16 a[36:37], v9 offset:4096\n"
"buffer_load_dwordx4 v6, s[48:51], s67 offen offset:0  lds\n"
"v_mfma_f32_16x16x32_bf16 v[232:235], a[92:95], a[108:111], v[232:235]\n"
"ds_read_b64_tr_b16 a[38:39], v9 offset:4672\n"
"v_mfma_f32_16x16x32_bf16 v[204:207], a[84:87], a[108:111], v[204:207]\n"
"s_mov_b32 s67, 16000\n"
"ds_read_b128 a[108:111], v13 offset:18432\n"
"v_mfma_f32_16x16x32_bf16 v[204:207], a[80:83], a[104:107], v[204:207]\n"
"ds_read_b128 a[104:107], v12 offset:18432\n"
"v_mfma_f32_16x16x32_bf16 v[208:211], a[80:83], a[112:115], v[208:211]\n"
"ds_read_b64_tr_b16 a[40:41], v9 offset:8\n"
"buffer_load_dwordx4 v6, s[48:51], s67 offen offset:1024  lds\n"
"v_mfma_f32_16x16x32_bf16 v[208:211], a[84:87], a[116:119], v[208:211]\n"
"ds_read_b64_tr_b16 a[42:43], v9 offset:584\n"
"v_mfma_f32_16x16x32_bf16 v[236:239], a[92:95], a[116:119], v[236:239]\n"
"s_mov_b32 s67, 31360\n"
"ds_read_b128 a[116:119], v13 offset:20480\n"
"v_mfma_f32_16x16x32_bf16 v[236:239], a[88:91], a[112:115], v[236:239]\n"
"ds_read_b128 a[112:115], v12 offset:20480\n"
"v_mfma_f32_16x16x32_bf16 v[240:243], a[88:91], a[120:123], v[240:243]\n"
"ds_read_b64_tr_b16 a[44:45], v9 offset:4104\n"
"buffer_load_dwordx4 v6, s[48:51], s67 offen offset:2048  lds\n"
"v_mfma_f32_16x16x32_bf16 v[240:243], a[92:95], a[124:127], v[240:243]\n"
"ds_read_b64_tr_b16 a[46:47], v9 offset:4680\n"
"v_mfma_f32_16x16x32_bf16 v[212:215], a[84:87], a[124:127], v[212:215]\n"
"s_mov_b32 s67, 46720\n"
"ds_read_b128 a[124:127], v13 offset:22528\n"
"v_mfma_f32_16x16x32_bf16 v[212:215], a[80:83], a[120:123], v[212:215]\n"
"ds_read_b128 a[120:123], v12 offset:22528\n"
"v_mfma_f32_16x16x32_bf16 v[216:219], a[80:83], a[128:131], v[216:219]\n"
"buffer_load_dwordx4 v6, s[48:51], s67 offen offset:3072  lds\n"
"v_mfma_f32_16x16x32_bf16 v[216:219], a[84:87], a[132:135], v[216:219]\n"
"v_mfma_f32_16x16x32_bf16 v[244:247], a[92:95], a[132:135], v[244:247]\n"
"ds_read_b128 a[132:135], v13 offset:24576\n"
"v_mfma_f32_16x16x32_bf16 v[244:247], a[88:91], a[128:131], v[244:247]\n"
"s_add_u32 m0, s64, 32768\n"
"ds_read_b128 a[128:131], v12 offset:24576\n"
"v_mfma_f32_16x16x32_bf16 v[248:251], a[88:91], a[136:139], v[248:251]\n"
"s_add_u32 s67, s75, 524288\n"
"v_mfma_f32_16x16x32_bf16 v[248:251], a[92:95], a[140:143], v[248:251]\n"
"buffer_load_dwordx4 v6, s[52:55], s67 offen offset:0  lds\n"
"v_mfma_f32_16x16x32_bf16 v[220:223], a[84:87], a[140:143], v[220:223]\n"
"ds_read_b128 a[140:143], v13 offset:26624\n"
"v_mfma_f32_16x16x32_bf16 v[220:223], a[80:83], a[136:139], v[220:223]\n"
"ds_read_b128 a[136:139], v12 offset:26624\n"
"v_mfma_f32_16x16x32_bf16 v[224:227], a[80:83], a[144:147], v[224:227]\n"
"s_add_u32 s67, s75, 539648\n"
"v_mfma_f32_16x16x32_bf16 v[224:227], a[84:87], a[148:151], v[224:227]\n"
"buffer_load_dwordx4 v6, s[52:55], s67 offen offset:1024  lds\n"
"v_mfma_f32_16x16x32_bf16 v[252:255], a[92:95], a[148:151], v[252:255]\n"
"ds_read_b128 a[148:151], v13 offset:28672\n"
"v_mfma_f32_16x16x32_bf16 v[252:255], a[88:91], a[144:147], v[252:255]\n"
"ds_read_b128 a[144:147], v12 offset:28672\n"
"v_mfma_f32_16x16x32_bf16 a[28:31], a[88:91], a[152:155], a[28:31]\n"
"s_add_u32 s67, s75, 555008\n"
"v_mfma_f32_16x16x32_bf16 a[28:31], a[92:95], a[156:159], a[28:31]\n"
"buffer_load_dwordx4 v6, s[52:55], s67 offen offset:2048  lds\n"
"v_mfma_f32_16x16x32_bf16 a[24:27], a[84:87], a[156:159], a[24:27]\n"
"ds_read_b128 a[156:159], v13 offset:30720\n"
"v_mfma_f32_16x16x32_bf16 a[24:27], a[80:83], a[152:155], a[24:27]\n"
"s_waitcnt lgkmcnt(9)\n"
"ds_read_b128 a[152:155], v12 offset:30720\n"
"v_mfma_f32_16x16x32_bf16 v[32:35], a[32:35], a[96:99], v[32:35]\n"
"ds_read_b64_tr_b16 a[48:49], v9 offset:64\n"
"v_mfma_f32_16x16x32_bf16 v[32:35], a[36:39], a[100:103], v[32:35]\n"
"s_add_u32 s67, s75, 570368\n"
"ds_read_b64_tr_b16 a[50:51], v9 offset:512\n"
"v_mfma_f32_16x16x32_bf16 v[60:63], a[44:47], a[100:103], v[60:63]\n"
"v_mfma_f32_16x16x32_bf16 v[60:63], a[40:43], a[96:99], v[60:63]\n"
"buffer_load_dwordx4 v6, s[52:55], s67 offen offset:3072  lds\n"
"v_mfma_f32_16x16x32_bf16 v[64:67], a[40:43], a[104:107], v[64:67]\n"
"ds_read_b64_tr_b16 a[52:53], v9 offset:4160\n"
"v_mfma_f32_16x16x32_bf16 v[64:67], a[44:47], a[108:111], v[64:67]\n"
"s_add_u32 m0, s64, 36864\n"
"ds_read_b64_tr_b16 a[54:55], v9 offset:4608\n"
"v_mfma_f32_16x16x32_bf16 v[36:39], a[36:39], a[108:111], v[36:39]\n"
"v_mfma_f32_16x16x32_bf16 v[36:39], a[32:35], a[104:107], v[36:39]\n"
"s_add_u32 s67, s75, 589824\n"
"s_waitcnt lgkmcnt(12)\n"
"v_mfma_f32_16x16x32_bf16 v[40:43], a[32:35], a[112:115], v[40:43]\n"
"ds_read_b64_tr_b16 a[56:57], v9 offset:72\n"
"v_mfma_f32_16x16x32_bf16 v[40:43], a[36:39], a[116:119], v[40:43]\n"
"ds_read_b64_tr_b16 a[58:59], v9 offset:520\n"
"v_mfma_f32_16x16x32_bf16 v[68:71], a[44:47], a[116:119], v[68:71]\n"
"buffer_load_dwordx4 v6, s[52:55], s67 offen offset:0  lds\n"
"v_mfma_f32_16x16x32_bf16 v[68:71], a[40:43], a[112:115], v[68:71]\n"
"s_add_u32 s67, s75, 605184\n"
"v_mfma_f32_16x16x32_bf16 v[72:75], a[40:43], a[120:123], v[72:75]\n"
"ds_read_b64_tr_b16 a[60:61], v9 offset:4168\n"
"v_mfma_f32_16x16x32_bf16 v[72:75], a[44:47], a[124:127], v[72:75]\n"
"ds_read_b64_tr_b16 a[62:63], v9 offset:4616\n"
"v_mfma_f32_16x16x32_bf16 v[44:47], a[36:39], a[124:127], v[44:47]\n"
"buffer_load_dwordx4 v6, s[52:55], s67 offen offset:1024  lds\n"
"v_mfma_f32_16x16x32_bf16 v[44:47], a[32:35], a[120:123], v[44:47]\n"
"s_add_u32 s67, s75, 620544\n"
"s_waitcnt lgkmcnt(12)\n"
"v_mfma_f32_16x16x32_bf16 v[48:51], a[32:35], a[128:131], v[48:51]\n"
"v_mfma_f32_16x16x32_bf16 v[48:51], a[36:39], a[132:135], v[48:51]\n"
"buffer_load_dwordx4 v6, s[52:55], s67 offen offset:2048  lds\n"
"v_mfma_f32_16x16x32_bf16 v[76:79], a[44:47], a[132:135], v[76:79]\n"
"v_mfma_f32_16x16x32_bf16 v[76:79], a[40:43], a[128:131], v[76:79]\n"
"s_add_u32 s67, s75, 635904\n"
"v_mfma_f32_16x16x32_bf16 v[80:83], a[40:43], a[136:139], v[80:83]\n"
"v_mfma_f32_16x16x32_bf16 v[80:83], a[44:47], a[140:143], v[80:83]\n"
"buffer_load_dwordx4 v6, s[52:55], s67 offen offset:3072  lds\n"
"v_mfma_f32_16x16x32_bf16 v[52:55], a[36:39], a[140:143], v[52:55]\n"
"v_mfma_f32_16x16x32_bf16 v[52:55], a[32:35], a[136:139], v[52:55]\n"
"s_waitcnt lgkmcnt(8)\n"
"v_mfma_f32_16x16x32_bf16 v[56:59], a[32:35], a[144:147], v[56:59]\n"
"v_mfma_f32_16x16x32_bf16 v[56:59], a[36:39], a[148:151], v[56:59]\n"
"v_mfma_f32_16x16x32_bf16 v[84:87], a[44:47], a[148:151], v[84:87]\n"
"v_mfma_f32_16x16x32_bf16 v[84:87], a[40:43], a[144:147], v[84:87]\n"
"v_mfma_f32_16x16x32_bf16 a[4:7], a[40:43], a[152:155], a[4:7]\n"
"v_mfma_f32_16x16x32_bf16 a[4:7], a[44:47], a[156:159], a[4:7]\n"
"v_mfma_f32_16x16x32_bf16 a[0:3], a[36:39], a[156:159], a[0:3]\n"
"v_mfma_f32_16x16x32_bf16 a[0:3], a[32:35], a[152:155], a[0:3]\n"
"s_waitcnt lgkmcnt(0)\n"
"v_mfma_f32_16x16x32_bf16 v[88:91], a[48:51], a[96:99], v[88:91]\n"
"s_waitcnt vmcnt(13)\n"
"ds_read_b64_tr_b16 a[64:65], v9 offset:8192\n"
"v_mfma_f32_16x16x32_bf16 v[88:91], a[52:55], a[100:103], v[88:91]\n"
"ds_read_b64_tr_b16 a[66:67], v9 offset:8768\n"
"v_mfma_f32_16x16x32_bf16 v[116:119], a[60:63], a[100:103], v[116:119]\n"
"v_mfma_f32_16x16x32_bf16 v[116:119], a[56:59], a[96:99], v[116:119]\n"
"s_add_u32 m0, s64, 0\n"
"v_mfma_f32_16x16x32_bf16 v[120:123], a[56:59], a[104:107], v[120:123]\n"
"ds_read_b64_tr_b16 a[68:69], v9 offset:12288\n"
"v_mfma_f32_16x16x32_bf16 v[120:123], a[60:63], a[108:111], v[120:123]\n"
"s_add_u32 s67, s75, 524416\n"
"ds_read_b64_tr_b16 a[70:71], v9 offset:12864\n"
"v_mfma_f32_16x16x32_bf16 v[92:95], a[52:55], a[108:111], v[92:95]\n"
"v_mfma_f32_16x16x32_bf16 v[92:95], a[48:51], a[104:107], v[92:95]\n"
"buffer_load_dwordx4 v6, s[52:55], s67 offen offset:0  lds\n"
"v_mfma_f32_16x16x32_bf16 v[96:99], a[48:51], a[112:115], v[96:99]\n"
"ds_read_b64_tr_b16 a[72:73], v9 offset:8200\n"
"v_mfma_f32_16x16x32_bf16 v[96:99], a[52:55], a[116:119], v[96:99]\n"
"s_add_u32 s67, s75, 539776\n"
"ds_read_b64_tr_b16 a[74:75], v9 offset:8776\n"
"v_mfma_f32_16x16x32_bf16 v[124:127], a[60:63], a[116:119], v[124:127]\n"
"v_mfma_f32_16x16x32_bf16 v[124:127], a[56:59], a[112:115], v[124:127]\n"
"buffer_load_dwordx4 v6, s[52:55], s67 offen offset:1024  lds\n"
"v_mfma_f32_16x16x32_bf16 v[128:131], a[56:59], a[120:123], v[128:131]\n"
"ds_read_b64_tr_b16 a[76:77], v9 offset:12296\n"
"v_mfma_f32_16x16x32_bf16 v[128:131], a[60:63], a[124:127], v[128:131]\n"
"s_add_u32 s67, s75, 555136\n"
"ds_read_b64_tr_b16 a[78:79], v9 offset:12872\n"
"v_mfma_f32_16x16x32_bf16 v[100:103], a[52:55], a[124:127], v[100:103]\n"
"v_mfma_f32_16x16x32_bf16 v[100:103], a[48:51], a[120:123], v[100:103]\n"
"buffer_load_dwordx4 v6, s[52:55], s67 offen offset:2048  lds\n"
"v_mfma_f32_16x16x32_bf16 v[104:107], a[48:51], a[128:131], v[104:107]\n"
"v_mfma_f32_16x16x32_bf16 v[104:107], a[52:55], a[132:135], v[104:107]\n"
"s_add_u32 s67, s75, 570496\n"
"v_mfma_f32_16x16x32_bf16 v[132:135], a[60:63], a[132:135], v[132:135]\n"
"v_mfma_f32_16x16x32_bf16 v[132:135], a[56:59], a[128:131], v[132:135]\n"
"buffer_load_dwordx4 v6, s[52:55], s67 offen offset:3072  lds\n"
"v_mfma_f32_16x16x32_bf16 v[136:139], a[56:59], a[136:139], v[136:139]\n"
"v_mfma_f32_16x16x32_bf16 v[136:139], a[60:63], a[140:143], v[136:139]\n"
"s_add_u32 m0, s64, 4096\n"
"v_mfma_f32_16x16x32_bf16 v[108:111], a[52:55], a[140:143], v[108:111]\n"
"v_mfma_f32_16x16x32_bf16 v[108:111], a[48:51], a[136:139], v[108:111]\n"
"s_add_u32 s67, s75, 589952\n"
"v_mfma_f32_16x16x32_bf16 v[112:115], a[48:51], a[144:147], v[112:115]\n"
"v_mfma_f32_16x16x32_bf16 v[112:115], a[52:55], a[148:151], v[112:115]\n"
"buffer_load_dwordx4 v6, s[52:55], s67 offen offset:0  lds\n"
"v_mfma_f32_16x16x32_bf16 v[140:143], a[60:63], a[148:151], v[140:143]\n"
"v_mfma_f32_16x16x32_bf16 v[140:143], a[56:59], a[144:147], v[140:143]\n"
"s_add_u32 s67, s75, 605312\n"
"v_mfma_f32_16x16x32_bf16 a[12:15], a[56:59], a[152:155], a[12:15]\n"
"v_mfma_f32_16x16x32_bf16 a[12:15], a[60:63], a[156:159], a[12:15]\n"
"buffer_load_dwordx4 v6, s[52:55], s67 offen offset:1024  lds\n"
"v_mfma_f32_16x16x32_bf16 a[8:11], a[52:55], a[156:159], a[8:11]\n"
"v_mfma_f32_16x16x32_bf16 a[8:11], a[48:51], a[152:155], a[8:11]\n"
"s_add_u32 s67, s75, 620672\n"
"s_waitcnt lgkmcnt(0)\n"
"v_mfma_f32_16x16x32_bf16 v[144:147], a[64:67], a[96:99], v[144:147]\n"
"ds_read_b64_tr_b16 a[80:81], v9 offset:8256\n"
"v_mfma_f32_16x16x32_bf16 v[144:147], a[68:71], a[100:103], v[144:147]\n"
"ds_read_b64_tr_b16 a[82:83], v9 offset:8704\n"
"v_mfma_f32_16x16x32_bf16 v[172:175], a[76:79], a[100:103], v[172:175]\n"
"buffer_load_dwordx4 v6, s[52:55], s67 offen offset:2048  lds\n"
"v_mfma_f32_16x16x32_bf16 v[172:175], a[72:75], a[96:99], v[172:175]\n"
"s_add_u32 s67, s75, 636032\n"
"v_mfma_f32_16x16x32_bf16 v[176:179], a[72:75], a[104:107], v[176:179]\n"
"ds_read_b64_tr_b16 a[84:85], v9 offset:12352\n"
"v_mfma_f32_16x16x32_bf16 v[176:179], a[76:79], a[108:111], v[176:179]\n"
"ds_read_b64_tr_b16 a[86:87], v9 offset:12800\n"
"v_mfma_f32_16x16x32_bf16 v[148:151], a[68:71], a[108:111], v[148:151]\n"
"buffer_load_dwordx4 v6, s[52:55], s67 offen offset:3072  lds\n"
"v_mfma_f32_16x16x32_bf16 v[148:151], a[64:67], a[104:107], v[148:151]\n"
"v_mfma_f32_16x16x32_bf16 v[152:155], a[64:67], a[112:115], v[152:155]\n"
"ds_read_b64_tr_b16 a[88:89], v9 offset:8264\n"
"v_mfma_f32_16x16x32_bf16 v[152:155], a[68:71], a[116:119], v[152:155]\n"
"ds_read_b64_tr_b16 a[90:91], v9 offset:8712\n"
"v_mfma_f32_16x16x32_bf16 v[180:183], a[76:79], a[116:119], v[180:183]\n"
"v_mfma_f32_16x16x32_bf16 v[180:183], a[72:75], a[112:115], v[180:183]\n"
"v_mfma_f32_16x16x32_bf16 v[184:187], a[72:75], a[120:123], v[184:187]\n"
"ds_read_b64_tr_b16 a[92:93], v9 offset:12360\n"
"v_mfma_f32_16x16x32_bf16 v[184:187], a[76:79], a[124:127], v[184:187]\n"
"ds_read_b64_tr_b16 a[94:95], v9 offset:12808\n"
"v_mfma_f32_16x16x32_bf16 v[156:159], a[68:71], a[124:127], v[156:159]\n"
"v_mfma_f32_16x16x32_bf16 v[156:159], a[64:67], a[120:123], v[156:159]\n"
"v_mfma_f32_16x16x32_bf16 v[160:163], a[64:67], a[128:131], v[160:163]\n"
"v_mfma_f32_16x16x32_bf16 v[160:163], a[68:71], a[132:135], v[160:163]\n"
"v_mfma_f32_16x16x32_bf16 v[188:191], a[76:79], a[132:135], v[188:191]\n"
"v_mfma_f32_16x16x32_bf16 v[188:191], a[72:75], a[128:131], v[188:191]\n"
"v_mfma_f32_16x16x32_bf16 v[192:195], a[72:75], a[136:139], v[192:195]\n"
"s_sub_u32 s66, 0, s73\n"
"v_mfma_f32_16x16x32_bf16 v[192:195], a[76:79], a[140:143], v[192:195]\n"
"v_mfma_f32_16x16x32_bf16 v[164:167], a[68:71], a[140:143], v[164:167]\n"
"v_add_u32 v12, s66, v7\n"
"v_mfma_f32_16x16x32_bf16 v[164:167], a[64:67], a[136:139], v[164:167]\n"
"v_mfma_f32_16x16x32_bf16 v[168:171], a[64:67], a[144:147], v[168:171]\n"
"v_add_u32 v13, s66, v8\n"
"v_mfma_f32_16x16x32_bf16 v[168:171], a[68:71], a[148:151], v[168:171]\n"
"v_mfma_f32_16x16x32_bf16 v[196:199], a[76:79], a[148:151], v[196:199]\n"
"v_mfma_f32_16x16x32_bf16 v[196:199], a[72:75], a[144:147], v[196:199]\n"
"v_mfma_f32_16x16x32_bf16 a[20:23], a[72:75], a[152:155], a[20:23]\n"
"buffer_store_dwordx4 v[16:19], v0, s[56:59], s69 offen sc1\n"
"v_mfma_f32_16x16x32_bf16 a[20:23], a[76:79], a[156:159], a[20:23]\n"
"v_mfma_f32_16x16x32_bf16 a[16:19], a[68:71], a[156:159], a[16:19]\n"
"s_barrier\n"
"v_mfma_f32_16x16x32_bf16 a[16:19], a[64:67], a[152:155], a[16:19]\n"
"s_waitcnt lgkmcnt(0)\n"
"v_mfma_f32_16x16x32_bf16 v[200:203], a[80:83], a[96:99], v[200:203]\n"
"s_waitcnt vmcnt(9)\n"
"ds_read_b64_tr_b16 a[32:33], v9 offset:32768\n"
"v_mfma_f32_16x16x32_bf16 v[200:203], a[84:87], a[100:103], v[200:203]\n"
"s_add_u32 m0, s72, 98304\n"
"ds_read_b64_tr_b16 a[34:35], v9 offset:33344\n"
"v_mfma_f32_16x16x32_bf16 v[228:231], a[92:95], a[100:103], v[228:231]\n"
"s_mov_b32 s67, 768\n"
"ds_read_b128 a[100:103], v13 offset:16384\n"
"v_mfma_f32_16x16x32_bf16 v[228:231], a[88:91], a[96:99], v[228:231]\n"
"ds_read_b128 a[96:99], v12 offset:16384\n"
"v_mfma_f32_16x16x32_bf16 v[232:235], a[88:91], a[104:107], v[232:235]\n"
"ds_read_b64_tr_b16 a[36:37], v9 offset:36864\n"
"buffer_load_dwordx4 v6, s[48:51], s67 offen offset:0  lds\n"
"v_mfma_f32_16x16x32_bf16 v[232:235], a[92:95], a[108:111], v[232:235]\n"
"ds_read_b64_tr_b16 a[38:39], v9 offset:37440\n"
"v_mfma_f32_16x16x32_bf16 v[204:207], a[84:87], a[108:111], v[204:207]\n"
"s_mov_b32 s67, 16128\n"
"ds_read_b128 a[108:111], v13 offset:18432\n"
"v_mfma_f32_16x16x32_bf16 v[204:207], a[80:83], a[104:107], v[204:207]\n"
"ds_read_b128 a[104:107], v12 offset:18432\n"
"v_mfma_f32_16x16x32_bf16 v[208:211], a[80:83], a[112:115], v[208:211]\n"
"ds_read_b64_tr_b16 a[40:41], v9 offset:32776\n"
"buffer_load_dwordx4 v6, s[48:51], s67 offen offset:1024  lds\n"
"v_mfma_f32_16x16x32_bf16 v[208:211], a[84:87], a[116:119], v[208:211]\n"
"ds_read_b64_tr_b16 a[42:43], v9 offset:33352\n"
"v_mfma_f32_16x16x32_bf16 v[236:239], a[92:95], a[116:119], v[236:239]\n"
"s_mov_b32 s67, 31488\n"
"ds_read_b128 a[116:119], v13 offset:20480\n"
"v_mfma_f32_16x16x32_bf16 v[236:239], a[88:91], a[112:115], v[236:239]\n"
"ds_read_b128 a[112:115], v12 offset:20480\n"
"v_mfma_f32_16x16x32_bf16 v[240:243], a[88:91], a[120:123], v[240:243]\n"
"ds_read_b64_tr_b16 a[44:45], v9 offset:36872\n"
"buffer_load_dwordx4 v6, s[48:51], s67 offen offset:2048  lds\n"
"v_mfma_f32_16x16x32_bf16 v[240:243], a[92:95], a[124:127], v[240:243]\n"
"ds_read_b64_tr_b16 a[46:47], v9 offset:37448\n"
"v_mfma_f32_16x16x32_bf16 v[212:215], a[84:87], a[124:127], v[212:215]\n"
"s_mov_b32 s67, 46848\n"
"ds_read_b128 a[124:127], v13 offset:22528\n"
"v_mfma_f32_16x16x32_bf16 v[212:215], a[80:83], a[120:123], v[212:215]\n"
"ds_read_b128 a[120:123], v12 offset:22528\n"
"v_mfma_f32_16x16x32_bf16 v[216:219], a[80:83], a[128:131], v[216:219]\n"
"buffer_load_dwordx4 v6, s[48:51], s67 offen offset:3072  lds\n"
"v_mfma_f32_16x16x32_bf16 v[216:219], a[84:87], a[132:135], v[216:219]\n"
"v_mfma_f32_16x16x32_bf16 v[244:247], a[92:95], a[132:135], v[244:247]\n"
"ds_read_b128 a[132:135], v13 offset:24576\n"
"v_mfma_f32_16x16x32_bf16 v[244:247], a[88:91], a[128:131], v[244:247]\n"
"s_add_u32 m0, s64, 8192\n"
"ds_read_b128 a[128:131], v12 offset:24576\n"
"v_mfma_f32_16x16x32_bf16 v[248:251], a[88:91], a[136:139], v[248:251]\n"
"s_add_u32 s67, s75, 655360\n"
"v_mfma_f32_16x16x32_bf16 v[248:251], a[92:95], a[140:143], v[248:251]\n"
"buffer_load_dwordx4 v6, s[52:55], s67 offen offset:0  lds\n"
"v_mfma_f32_16x16x32_bf16 v[220:223], a[84:87], a[140:143], v[220:223]\n"
"ds_read_b128 a[140:143], v13 offset:26624\n"
"v_mfma_f32_16x16x32_bf16 v[220:223], a[80:83], a[136:139], v[220:223]\n"
"ds_read_b128 a[136:139], v12 offset:26624\n"
"v_mfma_f32_16x16x32_bf16 v[224:227], a[80:83], a[144:147], v[224:227]\n"
"s_add_u32 s67, s75, 670720\n"
"v_mfma_f32_16x16x32_bf16 v[224:227], a[84:87], a[148:151], v[224:227]\n"
"buffer_load_dwordx4 v6, s[52:55], s67 offen offset:1024  lds\n"
"v_mfma_f32_16x16x32_bf16 v[252:255], a[92:95], a[148:151], v[252:255]\n"
"ds_read_b128 a[148:151], v13 offset:28672\n"
"v_mfma_f32_16x16x32_bf16 v[252:255], a[88:91], a[144:147], v[252:255]\n"
"ds_read_b128 a[144:147], v12 offset:28672\n"
"v_mfma_f32_16x16x32_bf16 a[28:31], a[88:91], a[152:155], a[28:31]\n"
"s_add_u32 s67, s75, 686080\n"
"v_mfma_f32_16x16x32_bf16 a[28:31], a[92:95], a[156:159], a[28:31]\n"
"buffer_load_dwordx4 v6, s[52:55], s67 offen offset:2048  lds\n"
"v_mfma_f32_16x16x32_bf16 a[24:27], a[84:87], a[156:159], a[24:27]\n"
"ds_read_b128 a[156:159], v13 offset:30720\n"
"v_mfma_f32_16x16x32_bf16 a[24:27], a[80:83], a[152:155], a[24:27]\n"
"s_waitcnt lgkmcnt(9)\n"
"ds_read_b128 a[152:155], v12 offset:30720\n"
"v_mfma_f32_16x16x32_bf16 v[32:35], a[32:35], a[96:99], v[32:35]\n"
"ds_read_b64_tr_b16 a[48:49], v9 offset:32832\n"
"v_mfma_f32_16x16x32_bf16 v[32:35], a[36:39], a[100:103], v[32:35]\n"
"s_add_u32 s67, s75, 701440\n"
"ds_read_b64_tr_b16 a[50:51], v9 offset:33280\n"
"v_mfma_f32_16x16x32_bf16 v[60:63], a[44:47], a[100:103], v[60:63]\n"
"v_mfma_f32_16x16x32_bf16 v[60:63], a[40:43], a[96:99], v[60:63]\n"
"buffer_load_dwordx4 v6, s[52:55], s67 offen offset:3072  lds\n"
"v_mfma_f32_16x16x32_bf16 v[64:67], a[40:43], a[104:107], v[64:67]\n"
"ds_read_b64_tr_b16 a[52:53], v9 offset:36928\n"
"v_mfma_f32_16x16x32_bf16 v[64:67], a[44:47], a[108:111], v[64:67]\n"
"s_add_u32 m0, s64, 12288\n"
"ds_read_b64_tr_b16 a[54:55], v9 offset:37376\n"
"v_mfma_f32_16x16x32_bf16 v[36:39], a[36:39], a[108:111], v[36:39]\n"
"v_mfma_f32_16x16x32_bf16 v[36:39], a[32:35], a[104:107], v[36:39]\n"
"s_add_u32 s67, s75, 720896\n"
"s_waitcnt lgkmcnt(12)\n"
"v_mfma_f32_16x16x32_bf16 v[40:43], a[32:35], a[112:115], v[40:43]\n"
"ds_read_b64_tr_b16 a[56:57], v9 offset:32840\n"
"v_mfma_f32_16x16x32_bf16 v[40:43], a[36:39], a[116:119], v[40:43]\n"
"ds_read_b64_tr_b16 a[58:59], v9 offset:33288\n"
"v_mfma_f32_16x16x32_bf16 v[68:71], a[44:47], a[116:119], v[68:71]\n"
"buffer_load_dwordx4 v6, s[52:55], s67 offen offset:0  lds\n"
"v_mfma_f32_16x16x32_bf16 v[68:71], a[40:43], a[112:115], v[68:71]\n"
"s_add_u32 s67, s75, 736256\n"
"v_mfma_f32_16x16x32_bf16 v[72:75], a[40:43], a[120:123], v[72:75]\n"
"ds_read_b64_tr_b16 a[60:61], v9 offset:36936\n"
"v_mfma_f32_16x16x32_bf16 v[72:75], a[44:47], a[124:127], v[72:75]\n"
"ds_read_b64_tr_b16 a[62:63], v9 offset:37384\n"
"v_mfma_f32_16x16x32_bf16 v[44:47], a[36:39], a[124:127], v[44:47]\n"
"buffer_load_dwordx4 v6, s[52:55], s67 offen offset:1024  lds\n"
"v_mfma_f32_16x16x32_bf16 v[44:47], a[32:35], a[120:123], v[44:47]\n"
"s_add_u32 s67, s75, 751616\n"
"s_waitcnt lgkmcnt(12)\n"
"v_mfma_f32_16x16x32_bf16 v[48:51], a[32:35], a[128:131], v[48:51]\n"
"v_mfma_f32_16x16x32_bf16 v[48:51], a[36:39], a[132:135], v[48:51]\n"
"buffer_load_dwordx4 v6, s[52:55], s67 offen offset:2048  lds\n"
"v_mfma_f32_16x16x32_bf16 v[76:79], a[44:47], a[132:135], v[76:79]\n"
"v_mfma_f32_16x16x32_bf16 v[76:79], a[40:43], a[128:131], v[76:79]\n"
"s_add_u32 s67, s75, 766976\n"
"v_mfma_f32_16x16x32_bf16 v[80:83], a[40:43], a[136:139], v[80:83]\n"
"v_mfma_f32_16x16x32_bf16 v[80:83], a[44:47], a[140:143], v[80:83]\n"
"buffer_load_dwordx4 v6, s[52:55], s67 offen offset:3072  lds\n"
"v_mfma_f32_16x16x32_bf16 v[52:55], a[36:39], a[140:143], v[52:55]\n"
"v_mfma_f32_16x16x32_bf16 v[52:55], a[32:35], a[136:139], v[52:55]\n"
"s_waitcnt lgkmcnt(8)\n"
"v_mfma_f32_16x16x32_bf16 v[56:59], a[32:35], a[144:147], v[56:59]\n"
"v_mfma_f32_16x16x32_bf16 v[56:59], a[36:39], a[148:151], v[56:59]\n"
"v_mfma_f32_16x16x32_bf16 v[84:87], a[44:47], a[148:151], v[84:87]\n"
"v_mfma_f32_16x16x32_bf16 v[84:87], a[40:43], a[144:147], v[84:87]\n"
"v_mfma_f32_16x16x32_bf16 a[4:7], a[40:43], a[152:155], a[4:7]\n"
"v_mfma_f32_16x16x32_bf16 a[4:7], a[44:47], a[156:159], a[4:7]\n"
"v_mfma_f32_16x16x32_bf16 a[0:3], a[36:39], a[156:159], a[0:3]\n"
"v_mfma_f32_16x16x32_bf16 a[0:3], a[32:35], a[152:155], a[0:3]\n"
"s_waitcnt lgkmcnt(0)\n"
"v_mfma_f32_16x16x32_bf16 v[88:91], a[48:51], a[96:99], v[88:91]\n"
"s_waitcnt vmcnt(13)\n"
"ds_read_b64_tr_b16 a[64:65], v9 offset:0\n"
"v_mfma_f32_16x16x32_bf16 v[88:91], a[52:55], a[100:103], v[88:91]\n"
"ds_read_b64_tr_b16 a[66:67], v9 offset:576\n"
"v_mfma_f32_16x16x32_bf16 v[116:119], a[60:63], a[100:103], v[116:119]\n"
"v_mfma_f32_16x16x32_bf16 v[116:119], a[56:59], a[96:99], v[116:119]\n"
"s_add_u32 m0, s64, 32768\n"
"v_mfma_f32_16x16x32_bf16 v[120:123], a[56:59], a[104:107], v[120:123]\n"
"ds_read_b64_tr_b16 a[68:69], v9 offset:4096\n"
"v_mfma_f32_16x16x32_bf16 v[120:123], a[60:63], a[108:111], v[120:123]\n"
"s_add_u32 s67, s75, 655488\n"
"ds_read_b64_tr_b16 a[70:71], v9 offset:4672\n"
"v_mfma_f32_16x16x32_bf16 v[92:95], a[52:55], a[108:111], v[92:95]\n"
"v_mfma_f32_16x16x32_bf16 v[92:95], a[48:51], a[104:107], v[92:95]\n"
"buffer_load_dwordx4 v6, s[52:55], s67 offen offset:0  lds\n"
"v_mfma_f32_16x16x32_bf16 v[96:99], a[48:51], a[112:115], v[96:99]\n"
"ds_read_b64_tr_b16 a[72:73], v9 offset:8\n"
"v_mfma_f32_16x16x32_bf16 v[96:99], a[52:55], a[116:119], v[96:99]\n"
"s_add_u32 s67, s75, 670848\n"
"ds_read_b64_tr_b16 a[74:75], v9 offset:584\n"
"v_mfma_f32_16x16x32_bf16 v[124:127], a[60:63], a[116:119], v[124:127]\n"
"v_mfma_f32_16x16x32_bf16 v[124:127], a[56:59], a[112:115], v[124:127]\n"
"buffer_load_dwordx4 v6, s[52:55], s67 offen offset:1024  lds\n"
"v_mfma_f32_16x16x32_bf16 v[128:131], a[56:59], a[120:123], v[128:131]\n"
"ds_read_b64_tr_b16 a[76:77], v9 offset:4104\n"
"v_mfma_f32_16x16x32_bf16 v[128:131], a[60:63], a[124:127], v[128:131]\n"
"s_add_u32 s67, s75, 686208\n"
"ds_read_b64_tr_b16 a[78:79], v9 offset:4680\n"
"v_mfma_f32_16x16x32_bf16 v[100:103], a[52:55], a[124:127], v[100:103]\n"
"v_mfma_f32_16x16x32_bf16 v[100:103], a[48:51], a[120:123], v[100:103]\n"
"buffer_load_dwordx4 v6, s[52:55], s67 offen offset:2048  lds\n"
"v_mfma_f32_16x16x32_bf16 v[104:107], a[48:51], a[128:131], v[104:107]\n"
"v_mfma_f32_16x16x32_bf16 v[104:107], a[52:55], a[132:135], v[104:107]\n"
"s_add_u32 s67, s75, 701568\n"
"v_mfma_f32_16x16x32_bf16 v[132:135], a[60:63], a[132:135], v[132:135]\n"
"v_mfma_f32_16x16x32_bf16 v[132:135], a[56:59], a[128:131], v[132:135]\n"
"buffer_load_dwordx4 v6, s[52:55], s67 offen offset:3072  lds\n"
"v_mfma_f32_16x16x32_bf16 v[136:139], a[56:59], a[136:139], v[136:139]\n"
"v_mfma_f32_16x16x32_bf16 v[136:139], a[60:63], a[140:143], v[136:139]\n"
"s_add_u32 m0, s64, 36864\n"
"v_mfma_f32_16x16x32_bf16 v[108:111], a[52:55], a[140:143], v[108:111]\n"
"v_mfma_f32_16x16x32_bf16 v[108:111], a[48:51], a[136:139], v[108:111]\n"
"s_add_u32 s67, s75, 721024\n"
"v_mfma_f32_16x16x32_bf16 v[112:115], a[48:51], a[144:147], v[112:115]\n"
"v_mfma_f32_16x16x32_bf16 v[112:115], a[52:55], a[148:151], v[112:115]\n"
"buffer_load_dwordx4 v6, s[52:55], s67 offen offset:0  lds\n"
"v_mfma_f32_16x16x32_bf16 v[140:143], a[60:63], a[148:151], v[140:143]\n"
"buffer_load_dwordx4 v[24:27], v0, s[56:59], %25 offen sc1\n"
"v_mfma_f32_16x16x32_bf16 v[140:143], a[56:59], a[144:147], v[140:143]\n"
"s_add_u32 s67, s75, 736384\n"
"v_mfma_f32_16x16x32_bf16 a[12:15], a[56:59], a[152:155], a[12:15]\n"
"v_mfma_f32_16x16x32_bf16 a[12:15], a[60:63], a[156:159], a[12:15]\n"
"buffer_load_dwordx4 v6, s[52:55], s67 offen offset:1024  lds\n"
"v_mfma_f32_16x16x32_bf16 a[8:11], a[52:55], a[156:159], a[8:11]\n"
"v_mfma_f32_16x16x32_bf16 a[8:11], a[48:51], a[152:155], a[8:11]\n"
"s_add_u32 s67, s75, 751744\n"
"s_waitcnt lgkmcnt(0)\n"
"v_mfma_f32_16x16x32_bf16 v[144:147], a[64:67], a[96:99], v[144:147]\n"
"ds_read_b64_tr_b16 a[80:81], v9 offset:64\n"
"v_mfma_f32_16x16x32_bf16 v[144:147], a[68:71], a[100:103], v[144:147]\n"
"ds_read_b64_tr_b16 a[82:83], v9 offset:512\n"
"v_mfma_f32_16x16x32_bf16 v[172:175], a[76:79], a[100:103], v[172:175]\n"
"buffer_load_dwordx4 v6, s[52:55], s67 offen offset:2048  lds\n"
"v_mfma_f32_16x16x32_bf16 v[172:175], a[72:75], a[96:99], v[172:175]\n"
"s_add_u32 s67, s75, 767104\n"
"v_mfma_f32_16x16x32_bf16 v[176:179], a[72:75], a[104:107], v[176:179]\n"
"ds_read_b64_tr_b16 a[84:85], v9 offset:4160\n"
"v_mfma_f32_16x16x32_bf16 v[176:179], a[76:79], a[108:111], v[176:179]\n"
"ds_read_b64_tr_b16 a[86:87], v9 offset:4608\n"
"v_mfma_f32_16x16x32_bf16 v[148:151], a[68:71], a[108:111], v[148:151]\n"
"buffer_load_dwordx4 v6, s[52:55], s67 offen offset:3072  lds\n"
"v_mfma_f32_16x16x32_bf16 v[148:151], a[64:67], a[104:107], v[148:151]\n"
"v_mfma_f32_16x16x32_bf16 v[152:155], a[64:67], a[112:115], v[152:155]\n"
"ds_read_b64_tr_b16 a[88:89], v9 offset:72\n"
"v_mfma_f32_16x16x32_bf16 v[152:155], a[68:71], a[116:119], v[152:155]\n"
"ds_read_b64_tr_b16 a[90:91], v9 offset:520\n"
"v_mfma_f32_16x16x32_bf16 v[180:183], a[76:79], a[116:119], v[180:183]\n"
"v_mfma_f32_16x16x32_bf16 v[180:183], a[72:75], a[112:115], v[180:183]\n"
"v_mfma_f32_16x16x32_bf16 v[184:187], a[72:75], a[120:123], v[184:187]\n"
"ds_read_b64_tr_b16 a[92:93], v9 offset:4168\n"
"v_mfma_f32_16x16x32_bf16 v[184:187], a[76:79], a[124:127], v[184:187]\n"
"ds_read_b64_tr_b16 a[94:95], v9 offset:4616\n"
"v_mfma_f32_16x16x32_bf16 v[156:159], a[68:71], a[124:127], v[156:159]\n"
"v_mfma_f32_16x16x32_bf16 v[156:159], a[64:67], a[120:123], v[156:159]\n"
"v_mfma_f32_16x16x32_bf16 v[160:163], a[64:67], a[128:131], v[160:163]\n"
"v_mfma_f32_16x16x32_bf16 v[160:163], a[68:71], a[132:135], v[160:163]\n"
"v_mfma_f32_16x16x32_bf16 v[188:191], a[76:79], a[132:135], v[188:191]\n"
"v_mfma_f32_16x16x32_bf16 v[188:191], a[72:75], a[128:131], v[188:191]\n"
"v_mfma_f32_16x16x32_bf16 v[192:195], a[72:75], a[136:139], v[192:195]\n"
"s_sub_u32 s66, 40960, s73\n"
"v_mfma_f32_16x16x32_bf16 v[192:195], a[76:79], a[140:143], v[192:195]\n"
"v_mfma_f32_16x16x32_bf16 v[164:167], a[68:71], a[140:143], v[164:167]\n"
"v_add_u32 v12, s66, v7\n"
"v_mfma_f32_16x16x32_bf16 v[164:167], a[64:67], a[136:139], v[164:167]\n"
"v_mfma_f32_16x16x32_bf16 v[168:171], a[64:67], a[144:147], v[168:171]\n"
"v_add_u32 v13, s66, v8\n"
"v_mfma_f32_16x16x32_bf16 v[168:171], a[68:71], a[148:151], v[168:171]\n"
"v_mfma_f32_16x16x32_bf16 v[196:199], a[76:79], a[148:151], v[196:199]\n"
"v_mfma_f32_16x16x32_bf16 v[196:199], a[72:75], a[144:147], v[196:199]\n"
"v_mfma_f32_16x16x32_bf16 a[20:23], a[72:75], a[152:155], a[20:23]\n"
"v_mfma_f32_16x16x32_bf16 a[20:23], a[76:79], a[156:159], a[20:23]\n"
"v_mfma_f32_16x16x32_bf16 a[16:19], a[68:71], a[156:159], a[16:19]\n"
"s_barrier\n"
"v_mfma_f32_16x16x32_bf16 a[16:19], a[64:67], a[152:155], a[16:19]\n"
"s_waitcnt lgkmcnt(0)\n"
"v_mfma_f32_16x16x32_bf16 v[200:203], a[80:83], a[96:99], v[200:203]\n"
"s_waitcnt vmcnt(9)\n"
"ds_read_b64_tr_b16 a[32:33], v9 offset:8192\n"
"v_mfma_f32_16x16x32_bf16 v[200:203], a[84:87], a[100:103], v[200:203]\n"
"s_add_u32 m0, s72, 139264\n"
"ds_read_b64_tr_b16 a[34:35], v9 offset:8768\n"
"v_mfma_f32_16x16x32_bf16 v[228:231], a[92:95], a[100:103], v[228:231]\n"
"s_mov_b32 s67, 896\n"
"ds_read_b128 a[100:103], v13 offset:16384\n"
"v_mfma_f32_16x16x32_bf16 v[228:231], a[88:91], a[96:99], v[228:231]\n"
"ds_read_b128 a[96:99], v12 offset:16384\n"
"v_mfma_f32_16x16x32_bf16 v[232:235], a[88:91], a[104:107], v[232:235]\n"
"ds_read_b64_tr_b16 a[36:37], v9 offset:12288\n"
"buffer_load_dwordx4 v6, s[48:51], s67 offen offset:0  lds\n"
"v_mfma_f32_16x16x32_bf16 v[232:235], a[92:95], a[108:111], v[232:235]\n"
"ds_read_b64_tr_b16 a[38:39], v9 offset:12864\n"
"v_mfma_f32_16x16x32_bf16 v[204:207], a[84:87], a[108:111], v[204:207]\n"
"s_mov_b32 s67, 16256\n"
"ds_read_b128 a[108:111], v13 offset:18432\n"
"v_mfma_f32_16x16x32_bf16 v[204:207], a[80:83], a[104:107], v[204:207]\n"
"ds_read_b128 a[104:107], v12 offset:18432\n"
"v_mfma_f32_16x16x32_bf16 v[208:211], a[80:83], a[112:115], v[208:211]\n"
"ds_read_b64_tr_b16 a[40:41], v9 offset:8200\n"
"buffer_load_dwordx4 v6, s[48:51], s67 offen offset:1024  lds\n"
"v_mfma_f32_16x16x32_bf16 v[208:211], a[84:87], a[116:119], v[208:211]\n"
"ds_read_b64_tr_b16 a[42:43], v9 offset:8776\n"
"v_mfma_f32_16x16x32_bf16 v[236:239], a[92:95], a[116:119], v[236:239]\n"
"s_mov_b32 s67, 31616\n"
"ds_read_b128 a[116:119], v13 offset:20480\n"
"v_mfma_f32_16x16x32_bf16 v[236:239], a[88:91], a[112:115], v[236:239]\n"
"ds_read_b128 a[112:115], v12 offset:20480\n"
"v_mfma_f32_16x16x32_bf16 v[240:243], a[88:91], a[120:123], v[240:243]\n"
"ds_read_b64_tr_b16 a[44:45], v9 offset:12296\n"
"buffer_load_dwordx4 v6, s[48:51], s67 offen offset:2048  lds\n"
"v_mfma_f32_16x16x32_bf16 v[240:243], a[92:95], a[124:127], v[240:243]\n"
"ds_read_b64_tr_b16 a[46:47], v9 offset:12872\n"
"v_mfma_f32_16x16x32_bf16 v[212:215], a[84:87], a[124:127], v[212:215]\n"
"s_mov_b32 s67, 46976\n"
"ds_read_b128 a[124:127], v13 offset:22528\n"
"v_mfma_f32_16x16x32_bf16 v[212:215], a[80:83], a[120:123], v[212:215]\n"
"ds_read_b128 a[120:123], v12 offset:22528\n"
"v_mfma_f32_16x16x32_bf16 v[216:219], a[80:83], a[128:131], v[216:219]\n"
"buffer_load_dwordx4 v6, s[48:51], s67 offen offset:3072  lds\n"
"v_mfma_f32_16x16x32_bf16 v[216:219], a[84:87], a[132:135], v[216:219]\n"
"v_mfma_f32_16x16x32_bf16 v[244:247], a[92:95], a[132:135], v[244:247]\n"
"ds_read_b128 a[132:135], v13 offset:24576\n"
"v_mfma_f32_16x16x32_bf16 v[244:247], a[88:91], a[128:131], v[244:247]\n"
"ds_read_b128 a[128:131], v12 offset:24576\n"
"v_mfma_f32_16x16x32_bf16 v[248:251], a[88:91], a[136:139], v[248:251]\n"
"v_mfma_f32_16x16x32_bf16 v[248:251], a[92:95], a[140:143], v[248:251]\n"
"v_mfma_f32_16x16x32_bf16 v[220:223], a[84:87], a[140:143], v[220:223]\n"
"s_add_u32 m0, s64, 0\n"
"ds_read_b128 a[140:143], v13 offset:26624\n"
"v_mfma_f32_16x16x32_bf16 v[220:223], a[80:83], a[136:139], v[220:223]\n"
"ds_read_b128 a[136:139], v12 offset:26624\n"
"v_mfma_f32_16x16x32_bf16 v[224:227], a[80:83], a[144:147], v[224:227]\n"
"v_mfma_f32_16x16x32_bf16 v[224:227], a[84:87], a[148:151], v[224:227]\n"
"v_mfma_f32_16x16x32_bf16 v[252:255], a[92:95], a[148:151], v[252:255]\n"
"ds_read_b128 a[148:151], v13 offset:28672\n"
"v_mfma_f32_16x16x32_bf16 v[252:255], a[88:91], a[144:147], v[252:255]\n"
"ds_read_b128 a[144:147], v12 offset:28672\n"
"v_mfma_f32_16x16x32_bf16 a[28:31], a[88:91], a[152:155], a[28:31]\n"
"v_mfma_f32_16x16x32_bf16 a[28:31], a[92:95], a[156:159], a[28:31]\n"
"buffer_load_dwordx4 v6, s[40:43], s80 offen offset:0  lds\n"
"v_mfma_f32_16x16x32_bf16 a[24:27], a[84:87], a[156:159], a[24:27]\n"
"ds_read_b128 a[156:159], v13 offset:30720\n"
"v_mfma_f32_16x16x32_bf16 a[24:27], a[80:83], a[152:155], a[24:27]\n"
"s_waitcnt lgkmcnt(9)\n"
"ds_read_b128 a[152:155], v12 offset:30720\n"
"v_mfma_f32_16x16x32_bf16 v[32:35], a[32:35], a[96:99], v[32:35]\n"
"ds_read_b64_tr_b16 a[48:49], v9 offset:8256\n"
"v_mfma_f32_16x16x32_bf16 v[32:35], a[36:39], a[100:103], v[32:35]\n"
"ds_read_b64_tr_b16 a[50:51], v9 offset:8704\n"
"v_mfma_f32_16x16x32_bf16 v[60:63], a[44:47], a[100:103], v[60:63]\n"
"v_mfma_f32_16x16x32_bf16 v[60:63], a[40:43], a[96:99], v[60:63]\n"
"v_mfma_f32_16x16x32_bf16 v[64:67], a[40:43], a[104:107], v[64:67]\n"
"ds_read_b64_tr_b16 a[52:53], v9 offset:12352\n"
"v_mfma_f32_16x16x32_bf16 v[64:67], a[44:47], a[108:111], v[64:67]\n"
"ds_read_b64_tr_b16 a[54:55], v9 offset:12800\n"
"v_mfma_f32_16x16x32_bf16 v[36:39], a[36:39], a[108:111], v[36:39]\n"
"v_mfma_f32_16x16x32_bf16 v[36:39], a[32:35], a[104:107], v[36:39]\n"
"s_waitcnt lgkmcnt(12)\n"
"buffer_load_dwordx4 v6, s[40:43], s81 offen offset:1024  lds\n"
"v_mfma_f32_16x16x32_bf16 v[40:43], a[32:35], a[112:115], v[40:43]\n"
"ds_read_b64_tr_b16 a[56:57], v9 offset:8264\n"
"v_mfma_f32_16x16x32_bf16 v[40:43], a[36:39], a[116:119], v[40:43]\n"
"ds_read_b64_tr_b16 a[58:59], v9 offset:8712\n"
"v_mfma_f32_16x16x32_bf16 v[68:71], a[44:47], a[116:119], v[68:71]\n"
"v_mfma_f32_16x16x32_bf16 v[68:71], a[40:43], a[112:115], v[68:71]\n"
"v_mfma_f32_16x16x32_bf16 v[72:75], a[40:43], a[120:123], v[72:75]\n"
"ds_read_b64_tr_b16 a[60:61], v9 offset:12360\n"
"v_mfma_f32_16x16x32_bf16 v[72:75], a[44:47], a[124:127], v[72:75]\n"
"ds_read_b64_tr_b16 a[62:63], v9 offset:12808\n"
"v_mfma_f32_16x16x32_bf16 v[44:47], a[36:39], a[124:127], v[44:47]\n"
"v_mfma_f32_16x16x32_bf16 v[44:47], a[32:35], a[120:123], v[44:47]\n"
"s_waitcnt lgkmcnt(12)\n"
"buffer_load_dwordx4 v6, s[40:43], s82 offen offset:2048  lds\n"
"v_mfma_f32_16x16x32_bf16 v[48:51], a[32:35], a[128:131], v[48:51]\n"
"v_mfma_f32_16x16x32_bf16 v[48:51], a[36:39], a[132:135], v[48:51]\n"
"v_mfma_f32_16x16x32_bf16 v[76:79], a[44:47], a[132:135], v[76:79]\n"
"v_mfma_f32_16x16x32_bf16 v[76:79], a[40:43], a[128:131], v[76:79]\n"
"v_mfma_f32_16x16x32_bf16 v[80:83], a[40:43], a[136:139], v[80:83]\n"
"v_mfma_f32_16x16x32_bf16 v[80:83], a[44:47], a[140:143], v[80:83]\n"
"v_mfma_f32_16x16x32_bf16 v[52:55], a[36:39], a[140:143], v[52:55]\n"
"s_waitcnt vmcnt(10)\n"
"v_subrev_u32 v24, s70, v24\n"
"v_mfma_f32_16x16x32_bf16 v[52:55], a[32:35], a[136:139], v[52:55]\n"
"s_waitcnt lgkmcnt(8)\n"
"buffer_load_dwordx4 v6, s[40:43], s83 offen offset:3072  lds\n"
"v_xor_b32 v25, s79, v25\n"
"v_mfma_f32_16x16x32_bf16 v[56:59], a[32:35], a[144:147], v[56:59]\n"
"v_subrev_u32 v26, s70, v26\n"
"v_mfma_f32_16x16x32_bf16 v[56:59], a[36:39], a[148:151], v[56:59]\n"
"v_xor_b32 v27, s79, v27\n"
"v_mfma_f32_16x16x32_bf16 v[84:87], a[44:47], a[148:151], v[84:87]\n"
"v_or_b32 v24, v24, v26\n"
"v_mfma_f32_16x16x32_bf16 v[84:87], a[40:43], a[144:147], v[84:87]\n"
"v_or_b32 v25, v25, v27\n"
"v_mfma_f32_16x16x32_bf16 a[4:7], a[40:43], a[152:155], a[4:7]\n"
"v_or_b32 v24, v24, v25\n"
"v_mfma_f32_16x16x32_bf16 a[4:7], a[44:47], a[156:159], a[4:7]\n"
"v_cmp_lt_u32 vcc, 15, v24\n"
"v_mfma_f32_16x16x32_bf16 a[0:3], a[36:39], a[156:159], a[0:3]\n"
"s_cbranch_vccnz L_slow3\n"
"L_back3:\n"
"v_mfma_f32_16x16x32_bf16 a[0:3], a[32:35], a[152:155], a[0:3]\n"
"s_add_u32 m0, s64, 4096\n"
"s_waitcnt lgkmcnt(0)\n"
"v_mfma_f32_16x16x32_bf16 v[88:91], a[48:51], a[96:99], v[88:91]\n"
"s_waitcnt vmcnt(8)\n"
"ds_read_b64_tr_b16 a[64:65], v9 offset:32768\n"
"v_mfma_f32_16x16x32_bf16 v[88:91], a[52:55], a[100:103], v[88:91]\n"
"global_load_dwordx4 a[32:35], v0, s[34:35] offset:-4096 sc1\n"
"ds_read_b64_tr_b16 a[66:67], v9 offset:33344\n"
"v_mfma_f32_16x16x32_bf16 v[116:119], a[60:63], a[100:103], v[116:119]\n"
"v_mfma_f32_16x16x32_bf16 v[116:119], a[56:59], a[96:99], v[116:119]\n"
"v_mfma_f32_16x16x32_bf16 v[120:123], a[56:59], a[104:107], v[120:123]\n"
"ds_read_b64_tr_b16 a[68:69], v9 offset:36864\n"
"v_mfma_f32_16x16x32_bf16 v[120:123], a[60:63], a[108:111], v[120:123]\n"
"ds_read_b64_tr_b16 a[70:71], v9 offset:37440\n"
"v_mfma_f32_16x16x32_bf16 v[92:95], a[52:55], a[108:111], v[92:95]\n"
"v_mfma_f32_16x16x32_bf16 v[92:95], a[48:51], a[104:107], v[92:95]\n"
"buffer_load_dwordx4 v6, s[40:43], s84 offen offset:0  lds\n"
"v_mfma_f32_16x16x32_bf16 v[96:99], a[48:51], a[112:115], v[96:99]\n"
"ds_read_b64_tr_b16 a[72:73], v9 offset:32776\n"
"v_mfma_f32_16x16x32_bf16 v[96:99], a[52:55], a[116:119], v[96:99]\n"
"global_load_dwordx4 a[36:39], v0, s[34:35] offset:-3072 sc1\n"
"ds_read_b64_tr_b16 a[74:75], v9 offset:33352\n"
"v_mfma_f32_16x16x32_bf16 v[124:127], a[60:63], a[116:119], v[124:127]\n"
"v_mfma_f32_16x16x32_bf16 v[124:127], a[56:59], a[112:115], v[124:127]\n"
"v_mfma_f32_16x16x32_bf16 v[128:131], a[56:59], a[120:123], v[128:131]\n"
"ds_read_b64_tr_b16 a[76:77], v9 offset:36872\n"
"v_mfma_f32_16x16x32_bf16 v[128:131], a[60:63], a[124:127], v[128:131]\n"
"ds_read_b64_tr_b16 a[78:79], v9 offset:37448\n"
"v_mfma_f32_16x16x32_bf16 v[100:103], a[52:55], a[124:127], v[100:103]\n"
"v_mfma_f32_16x16x32_bf16 v[100:103], a[48:51], a[120:123], v[100:103]\n"
"v_mfma_f32_16x16x32_bf16 v[104:107], a[48:51], a[128:131], v[104:107]\n"
"buffer_load_dwordx4 v6, s[40:43], s85 offen offset:1024  lds\n"
"v_mfma_f32_16x16x32_bf16 v[104:107], a[52:55], a[132:135], v[104:107]\n"
"global_load_dwordx4 a[40:43], v0, s[34:35] offset:-2048 sc1\n"
"v_mfma_f32_16x16x32_bf16 v[132:135], a[60:63], a[132:135], v[132:135]\n"
"v_mfma_f32_16x16x32_bf16 v[132:135], a[56:59], a[128:131], v[132:135]\n"
"v_mfma_f32_16x16x32_bf16 v[136:139], a[56:59], a[136:139], v[136:139]\n"
"v_mfma_f32_16x16x32_bf16 v[136:139], a[60:63], a[140:143], v[136:139]\n"
"v_mfma_f32_16x16x32_bf16 v[108:111], a[52:55], a[140:143], v[108:111]\n"
"v_mfma_f32_16x16x32_bf16 v[108:111], a[48:51], a[136:139], v[108:111]\n"
"v_mfma_f32_16x16x32_bf16 v[112:115], a[48:51], a[144:147], v[112:115]\n"
"buffer_load_dwordx4 v6, s[40:43], s86 offen offset:2048  lds\n"
"v_mfma_f32_16x16x32_bf16 v[112:115], a[52:55], a[148:151], v[112:115]\n"
"global_load_dwordx4 a[44:47], v0, s[34:35] offset:-1024 sc1\n"
"v_mfma_f32_16x16x32_bf16 v[140:143], a[60:63], a[148:151], v[140:143]\n"
"v_mfma_f32_16x16x32_bf16 v[140:143], a[56:59], a[144:147], v[140:143]\n"
"v_mfma_f32_16x16x32_bf16 a[12:15], a[56:59], a[152:155], a[12:15]\n"
"s_add_u32 s96, s56, %11\n"
"s_addc_u32 s97, s57, 0\n"
"v_mfma_f32_16x16x32_bf16 a[12:15], a[60:63], a[156:159], a[12:15]\n"
"v_mfma_f32_16x16x32_bf16 a[8:11], a[52:55], a[156:159], a[8:11]\n"
"s_mov_b32 s69, 0\n"
"v_mfma_f32_16x16x32_bf16 a[8:11], a[48:51], a[152:155], a[8:11]\n"
"s_waitcnt lgkmcnt(0)\n"
"v_mfma_f32_16x16x32_bf16 v[144:147], a[64:67], a[96:99], v[144:147]\n"
"ds_read_b64_tr_b16 a[80:81], v9 offset:32832\n"
"v_mfma_f32_16x16x32_bf16 v[144:147], a[68:71], a[100:103], v[144:147]\n"
"global_load_dwordx4 a[48:51], v0, s[34:35] offset:0 sc1\n"
"ds_read_b64_tr_b16 a[82:83], v9 offset:33280\n"
"v_mfma_f32_16x16x32_bf16 v[172:175], a[76:79], a[100:103], v[172:175]\n"
"buffer_load_dwordx4 v6, s[40:43], s87 offen offset:3072  lds\n"
"v_mfma_f32_16x16x32_bf16 v[172:175], a[72:75], a[96:99], v[172:175]\n"
"v_mfma_f32_16x16x32_bf16 v[176:179], a[72:75], a[104:107], v[176:179]\n"
"ds_read_b64_tr_b16 a[84:85], v9 offset:36928\n"
"v_mfma_f32_16x16x32_bf16 v[176:179], a[76:79], a[108:111], v[176:179]\n"
"ds_read_b64_tr_b16 a[86:87], v9 offset:37376\n"
"v_mfma_f32_16x16x32_bf16 v[148:151], a[68:71], a[108:111], v[148:151]\n"
"v_mfma_f32_16x16x32_bf16 v[148:151], a[64:67], a[104:107], v[148:151]\n"
"v_mfma_f32_16x16x32_bf16 v[152:155], a[64:67], a[112:115], v[152:155]\n"
"ds_read_b64_tr_b16 a[88:89], v9 offset:32840\n"
"v_mfma_f32_16x16x32_bf16 v[152:155], a[68:71], a[116:119], v[152:155]\n"
"global_load_dwordx4 a[52:55], v0, s[34:35] offset:1024 sc1\n"
"ds_read_b64_tr_b16 a[90:91], v9 offset:33288\n"
"v_mfma_f32_16x16x32_bf16 v[180:183], a[76:79], a[116:119], v[180:183]\n"
"s_add_u32 m0, s64, 8192\n"
"v_mfma_f32_16x16x32_bf16 v[180:183], a[72:75], a[112:115], v[180:183]\n"
"v_mfma_f32_16x16x32_bf16 v[184:187], a[72:75], a[120:123], v[184:187]\n"
"ds_read_b64_tr_b16 a[92:93], v9 offset:36936\n"
"v_mfma_f32_16x16x32_bf16 v[184:187], a[76:79], a[124:127], v[184:187]\n"
"ds_read_b64_tr_b16 a[94:95], v9 offset:37384\n"
"v_mfma_f32_16x16x32_bf16 v[156:159], a[68:71], a[124:127], v[156:159]\n"
"v_mfma_f32_16x16x32_bf16 v[156:159], a[64:67], a[120:123], v[156:159]\n"
"v_mfma_f32_16x16x32_bf16 v[160:163], a[64:67], a[128:131], v[160:163]\n"
"v_mfma_f32_16x16x32_bf16 v[160:163], a[68:71], a[132:135], v[160:163]\n"
"global_load_dwordx4 a[56:59], v0, s[34:35] offset:2048 sc1\n"
"v_mfma_f32_16x16x32_bf16 v[188:191], a[76:79], a[132:135], v[188:191]\n"
"buffer_load_dwordx4 v6, s[40:43], s88 offen offset:0  lds\n"
"v_mfma_f32_16x16x32_bf16 v[188:191], a[72:75], a[128:131], v[188:191]\n"
"v_mfma_f32_16x16x32_bf16 v[192:195], a[72:75], a[136:139], v[192:195]\n"
"s_sub_u32 s66, 81920, s73\n"
"v_mfma_f32_16x16x32_bf16 v[192:195], a[76:79], a[140:143], v[192:195]\n"
"v_mfma_f32_16x16x32_bf16 v[164:167], a[68:71], a[140:143], v[164:167]\n"
"v_add_u32 v12, s66, v7\n"
"v_mfma_f32_16x16x32_bf16 v[164:167], a[64:67], a[136:139], v[164:167]\n"
"v_mfma_f32_16x16x32_bf16 v[168:171], a[64:67], a[144:147], v[168:171]\n"
"v_add_u32 v13, s66, v8\n"
"v_mfma_f32_16x16x32_bf16 v[168:171], a[68:71], a[148:151], v[168:171]\n"
"global_load_dwordx4 a[60:63], v0, s[34:35] offset:3072 sc1\n"
"v_mfma_f32_16x16x32_bf16 v[196:199], a[76:79], a[148:151], v[196:199]\n"
"v_mfma_f32_16x16x32_bf16 v[196:199], a[72:75], a[144:147], v[196:199]\n"
"buffer_load_dwordx4 v6, s[40:43], s89 offen offset:1024  lds\n"
"v_mfma_f32_16x16x32_bf16 a[20:23], a[72:75], a[152:155], a[20:23]\n"
"s_add_u32 s34, s34, 8192\n"
"v_mfma_f32_16x16x32_bf16 a[20:23], a[76:79], a[156:159], a[20:23]\n"
"v_mfma_f32_16x16x32_bf16 a[16:19], a[68:71], a[156:159], a[16:19]\n"
"s_barrier\n"
"v_mfma_f32_16x16x32_bf16 a[16:19], a[64:67], a[152:155], a[16:19]\n"
"s_waitcnt lgkmcnt(0)\n"
"v_mfma_f32_16x16x32_bf16 v[200:203], a[80:83], a[96:99], v[200:203]\n"
"v_mfma_f32_16x16x32_bf16 v[200:203], a[84:87], a[100:103], v[200:203]\n"
"global_load_dwordx4 a[64:67], v0, s[34:35] offset:-4096 sc1\n"
"v_mfma_f32_16x16x32_bf16 v[228:231], a[92:95], a[100:103], v[228:231]\n"
"ds_read_b128 a[100:103], v13 offset:16384\n"
"v_mfma_f32_16x16x32_bf16 v[228:231], a[88:91], a[96:99], v[228:231]\n"
"ds_read_b128 a[96:99], v12 offset:16384\n"
"v_mfma_f32_16x16x32_bf16 v[232:235], a[88:91], a[104:107], v[232:235]\n"
"buffer_load_dwordx4 v6, s[40:43], s90 offen offset:2048  lds\n"
"v_mfma_f32_16x16x32_bf16 v[232:235], a[92:95], a[108:111], v[232:235]\n"
"v_mfma_f32_16x16x32_bf16 v[204:207], a[84:87], a[108:111], v[204:207]\n"
"ds_read_b128 a[108:111], v13 offset:18432\n"
"v_mfma_f32_16x16x32_bf16 v[204:207], a[80:83], a[104:107], v[204:207]\n"
"ds_read_b128 a[104:107], v12 offset:18432\n"
"v_mfma_f32_16x16x32_bf16 v[208:211], a[80:83], a[112:115], v[208:211]\n"
"v_mfma_f32_16x16x32_bf16 v[208:211], a[84:87], a[116:119], v[208:211]\n"
"global_load_dwordx4 a[68:71], v0, s[34:35] offset:-3072 sc1\n"
"v_mfma_f32_16x16x32_bf16 v[236:239], a[92:95], a[116:119], v[236:239]\n"
"ds_read_b128 a[116:119], v13 offset:20480\n"
"v_mfma_f32_16x16x32_bf16 v[236:239], a[88:91], a[112:115], v[236:239]\n"
"ds_read_b128 a[112:115], v12 offset:20480\n"
"v_mfma_f32_16x16x32_bf16 v[240:243], a[88:91], a[120:123], v[240:243]\n"
"buffer_load_dwordx4 v6, s[40:43], s91 offen offset:3072  lds\n"
"v_mfma_f32_16x16x32_bf16 v[240:243], a[92:95], a[124:127], v[240:243]\n"
"v_mfma_f32_16x16x32_bf16 v[212:215], a[84:87], a[124:127], v[212:215]\n"
"ds_read_b128 a[124:127], v13 offset:22528\n"
"v_mfma_f32_16x16x32_bf16 v[212:215], a[80:83], a[120:123], v[212:215]\n"
"ds_read_b128 a[120:123], v12 offset:22528\n"
"v_mfma_f32_16x16x32_bf16 v[216:219], a[80:83], a[128:131], v[216:219]\n"
"v_mfma_f32_16x16x32_bf16 v[216:219], a[84:87], a[132:135], v[216:219]\n"
"global_load_dwordx4 a[72:75], v0, s[34:35] offset:-2048 sc1\n"
"v_mfma_f32_16x16x32_bf16 v[244:247], a[92:95], a[132:135], v[244:247]\n"
"ds_read_b128 a[132:135], v13 offset:24576\n"
"v_mfma_f32_16x16x32_bf16 v[244:247], a[88:91], a[128:131], v[244:247]\n"
"ds_read_b128 a[128:131], v12 offset:24576\n"
"v_mfma_f32_16x16x32_bf16 v[248:251], a[88:91], a[136:139], v[248:251]\n"
"s_add_u32 m0, s64, 12288\n"
"v_mfma_f32_16x16x32_bf16 v[248:251], a[92:95], a[140:143], v[248:251]\n"
"v_mfma_f32_16x16x32_bf16 v[220:223], a[84:87], a[140:143], v[220:223]\n"
"ds_read_b128 a[140:143], v13 offset:26624\n"
"v_mfma_f32_16x16x32_bf16 v[220:223], a[80:83], a[136:139], v[220:223]\n"
"ds_read_b128 a[136:139], v12 offset:26624\n"
"v_mfma_f32_16x16x32_bf16 v[224:227], a[80:83], a[144:147], v[224:227]\n"
"v_mfma_f32_16x16x32_bf16 v[224:227], a[84:87], a[148:151], v[224:227]\n"
"global_load_dwordx4 a[76:79], v0, s[34:35] offset:-1024 sc1\n"
"v_mfma_f32_16x16x32_bf16 v[252:255], a[92:95], a[148:151], v[252:255]\n"
"ds_read_b128 a[148:151], v13 offset:28672\n"
"v_mfma_f32_16x16x32_bf16 v[252:255], a[88:91], a[144:147], v[252:255]\n"
"ds_read_b128 a[144:147], v12 offset:28672\n"
"v_mfma_f32_16x16x32_bf16 a[28:31], a[88:91], a[152:155], a[28:31]\n"
"buffer_load_dwordx4 v6, s[40:43], s92 offen offset:0  lds\n"
"v_mfma_f32_16x16x32_bf16 a[28:31], a[92:95], a[156:159], a[28:31]\n"
"v_mfma_f32_16x16x32_bf16 a[24:27], a[84:87], a[156:159], a[24:27]\n"
"ds_read_b128 a[156:159], v13 offset:30720\n"
"v_mfma_f32_16x16x32_bf16 a[24:27], a[80:83], a[152:155], a[24:27]\n"
"s_waitcnt vmcnt(14) lgkmcnt(11)\n"
"ds_read_b128 a[152:155], v12 offset:30720\n"
"v_mfma_f32_16x16x32_bf16 v[32:35], a[32:35], a[96:99], v[32:35]\n"
"v_mfma_f32_16x16x32_bf16 v[32:35], a[36:39], a[100:103], v[32:35]\n"
"global_load_dwordx4 a[80:83], v0, s[34:35] offset:0 sc1\n"
"v_mfma_f32_16x16x32_bf16 v[60:63], a[44:47], a[100:103], v[60:63]\n"
"v_mfma_f32_16x16x32_bf16 v[60:63], a[40:43], a[96:99], v[60:63]\n"
"v_mfma_f32_16x16x32_bf16 v[64:67], a[40:43], a[104:107], v[64:67]\n"
"v_mfma_f32_16x16x32_bf16 v[64:67], a[44:47], a[108:111], v[64:67]\n"
"buffer_load_dwordx4 v6, s[40:43], s93 offen offset:1024  lds\n"
"v_mfma_f32_16x16x32_bf16 v[36:39], a[36:39], a[108:111], v[36:39]\n"
"v_mfma_f32_16x16x32_bf16 v[36:39], a[32:35], a[104:107], v[36:39]\n"
"s_waitcnt lgkmcnt(8)\n"
"v_mfma_f32_16x16x32_bf16 v[40:43], a[32:35], a[112:115], v[40:43]\n"
"v_mfma_f32_16x16x32_bf16 v[40:43], a[36:39], a[116:119], v[40:43]\n"
"global_load_dwordx4 a[84:87], v0, s[34:35] offset:1024 sc1\n"
"v_mfma_f32_16x16x32_bf16 v[68:71], a[44:47], a[116:119], v[68:71]\n"
"v_mfma_f32_16x16x32_bf16 v[68:71], a[40:43], a[112:115], v[68:71]\n"
"v_mfma_f32_16x16x32_bf16 v[72:75], a[40:43], a[120:123], v[72:75]\n"
"v_mfma_f32_16x16x32_bf16 v[72:75], a[44:47], a[124:127], v[72:75]\n"
"buffer_load_dwordx4 v6, s[40:43], s94 offen offset:2048  lds\n"
"v_mfma_f32_16x16x32_bf16 v[44:47], a[36:39], a[124:127], v[44:47]\n"
"v_mfma_f32_16x16x32_bf16 v[44:47], a[32:35], a[120:123], v[44:47]\n"
"s_waitcnt lgkmcnt(4)\n"
"v_mfma_f32_16x16x32_bf16 v[48:51], a[32:35], a[128:131], v[48:51]\n"
"v_mfma_f32_16x16x32_bf16 v[48:51], a[36:39], a[132:135], v[48:51]\n"
"global_load_dwordx4 a[88:91], v0, s[34:35] offset:2048 sc1\n"
"v_mfma_f32_16x16x32_bf16 v[76:79], a[44:47], a[132:135], v[76:79]\n"
"v_mfma_f32_16x16x32_bf16 v[76:79], a[40:43], a[128:131], v[76:79]\n"
"v_mfma_f32_16x16x32_bf16 v[80:83], a[40:43], a[136:139], v[80:83]\n"
"v_mfma_f32_16x16x32_bf16 v[80:83], a[44:47], a[140:143], v[80:83]\n"
"buffer_load_dwordx4 v6, s[40:43], s95 offen offset:3072  lds\n"
"v_mfma_f32_16x16x32_bf16 v[52:55], a[36:39], a[140:143], v[52:55]\n"
"v_mfma_f32_16x16x32_bf16 v[52:55], a[32:35], a[136:139], v[52:55]\n"
"s_waitcnt lgkmcnt(0)\n"
"v_mfma_f32_16x16x32_bf16 v[56:59], a[32:35], a[144:147], v[56:59]\n"
"v_mfma_f32_16x16x32_bf16 v[56:59], a[36:39], a[148:151], v[56:59]\n"
"global_load_dwordx4 a[92:95], v0, s[34:35] offset:3072 sc1\n"
"v_mfma_f32_16x16x32_bf16 v[84:87], a[44:47], a[148:151], v[84:87]\n"
"v_mfma_f32_16x16x32_bf16 v[84:87], a[40:43], a[144:147], v[84:87]\n"
"v_mfma_f32_16x16x32_bf16 a[4:7], a[40:43], a[152:155], a[4:7]\n"
"s_add_u32 s34, s34, 8192\n"
"s_add_u32 m0, s64, 32768\n"
"v_mfma_f32_16x16x32_bf16 a[4:7], a[44:47], a[156:159], a[4:7]\n"
"v_mfma_f32_16x16x32_bf16 a[0:3], a[36:39], a[156:159], a[0:3]\n"
"v_mfma_f32_16x16x32_bf16 a[0:3], a[32:35], a[152:155], a[0:3]\n"
"s_waitcnt vmcnt(15)\n"
"v_mfma_f32_16x16x32_bf16 v[88:91], a[48:51], a[96:99], v[88:91]\n"
"s_add_u32 s67, %21, 0\n"
"v_mfma_f32_16x16x32_bf16 v[88:91], a[52:55], a[100:103], v[88:91]\n"
"global_load_dwordx4 a[32:35], v0, s[34:35] offset:-4096 sc1\n"
"v_mfma_f32_16x16x32_bf16 v[116:119], a[60:63], a[100:103], v[116:119]\n"
"v_mfma_f32_16x16x32_bf16 v[116:119], a[56:59], a[96:99], v[116:119]\n"
"v_mfma_f32_16x16x32_bf16 v[120:123], a[56:59], a[104:107], v[120:123]\n"
"v_mfma_f32_16x16x32_bf16 v[120:123], a[60:63], a[108:111], v[120:123]\n"
"buffer_load_dwordx4 v6, s[52:55], s67 offen offset:0  lds\n"
"v_mfma_f32_16x16x32_bf16 v[92:95], a[52:55], a[108:111], v[92:95]\n"
"v_mfma_f32_16x16x32_bf16 v[92:95], a[48:51], a[104:107], v[92:95]\n"
"v_mfma_f32_16x16x32_bf16 v[96:99], a[48:51], a[112:115], v[96:99]\n"
"v_mfma_f32_16x16x32_bf16 v[96:99], a[52:55], a[116:119], v[96:99]\n"
"global_load_dwordx4 a[36:39], v0, s[34:35] offset:-3072 sc1\n"
"v_mfma_f32_16x16x32_bf16 v[124:127], a[60:63], a[116:119], v[124:127]\n"
"s_add_u32 s67, %21, 15360\n"
"v_mfma_f32_16x16x32_bf16 v[124:127], a[56:59], a[112:115], v[124:127]\n"
"v_mfma_f32_16x16x32_bf16 v[128:131], a[56:59], a[120:123], v[128:131]\n"
"v_mfma_f32_16x16x32_bf16 v[128:131], a[60:63], a[124:127], v[128:131]\n"
"v_mfma_f32_16x16x32_bf16 v[100:103], a[52:55], a[124:127], v[100:103]\n"
"v_mfma_f32_16x16x32_bf16 v[100:103], a[48:51], a[120:123], v[100:103]\n"
"buffer_load_dwordx4 v6, s[52:55], s67 offen offset:1024  lds\n"
"v_mfma_f32_16x16x32_bf16 v[104:107], a[48:51], a[128:131], v[104:107]\n"
"v_mfma_f32_16x16x32_bf16 v[104:107], a[52:55], a[132:135], v[104:107]\n"
"global_load_dwordx4 a[40:43], v0, s[34:35] offset:-2048 sc1\n"
"v_mfma_f32_16x16x32_bf16 v[132:135], a[60:63], a[132:135], v[132:135]\n"
"v_mfma_f32_16x16x32_bf16 v[132:135], a[56:59], a[128:131], v[132:135]\n"
"s_add_u32 s67, %21, 30720\n"
"v_mfma_f32_16x16x32_bf16 v[136:139], a[56:59], a[136:139], v[136:139]\n"
"v_mfma_f32_16x16x32_bf16 v[136:139], a[60:63], a[140:143], v[136:139]\n"
"v_mfma_f32_16x16x32_bf16 v[108:111], a[52:55], a[140:143], v[108:111]\n"
"v_mfma_f32_16x16x32_bf16 v[108:111], a[48:51], a[136:139], v[108:111]\n"
"v_mfma_f32_16x16x32_bf16 v[112:115], a[48:51], a[144:147], v[112:115]\n"
"buffer_load_dwordx4 v6, s[52:55], s67 offen offset:2048  lds\n"
"v_mfma_f32_16x16x32_bf16 v[112:115], a[52:55], a[148:151], v[112:115]\n"
"global_load_dwordx4 a[44:47], v0, s[34:35] offset:-1024 sc1\n"
"v_mfma_f32_16x16x32_bf16 v[140:143], a[60:63], a[148:151], v[140:143]\n"
"v_mfma_f32_16x16x32_bf16 v[140:143], a[56:59], a[144:147], v[140:143]\n"
"v_mfma_f32_16x16x32_bf16 a[12:15], a[56:59], a[152:155], a[12:15]\n"
"v_mfma_f32_16x16x32_bf16 a[12:15], a[60:63], a[156:159], a[12:15]\n"
"s_add_u32 s67, %21, 46080\n"
"v_mfma_f32_16x16x32_bf16 a[8:11], a[52:55], a[156:159], a[8:11]\n"
"v_mfma_f32_16x16x32_bf16 a[8:11], a[48:51], a[152:155], a[8:11]\n"
"s_waitcnt vmcnt(15)\n"
"v_mfma_f32_16x16x32_bf16 v[144:147], a[64:67], a[96:99], v[144:147]\n"
"v_mfma_f32_16x16x32_bf16 v[144:147], a[68:71], a[100:103], v[144:147]\n"
"global_load_dwordx4 a[48:51], v0, s[34:35] offset:0 sc1\n"
"v_mfma_f32_16x16x32_bf16 v[172:175], a[76:79], a[100:103], v[172:175]\n"
"buffer_load_dwordx4 v6, s[52:55], s67 offen offset:3072  lds\n"
"v_mfma_f32_16x16x32_bf16 v[172:175], a[72:75], a[96:99], v[172:175]\n"
"v_mfma_f32_16x16x32_bf16 v[176:179], a[72:75], a[104:107], v[176:179]\n"
"v_mfma_f32_16x16x32_bf16 v[176:179], a[76:79], a[108:111], v[176:179]\n"
"v_mfma_f32_16x16x32_bf16 v[148:151], a[68:71], a[108:111], v[148:151]\n"
"s_add_u32 m0, s64, 36864\n"
"v_mfma_f32_16x16x32_bf16 v[148:151], a[64:67], a[104:107], v[148:151]\n"
"v_mfma_f32_16x16x32_bf16 v[152:155], a[64:67], a[112:115], v[152:155]\n"
"v_mfma_f32_16x16x32_bf16 v[152:155], a[68:71], a[116:119], v[152:155]\n"
"global_load_dwordx4 a[52:55], v0, s[34:35] offset:1024 sc1\n"
"v_mfma_f32_16x16x32_bf16 v[180:183], a[76:79], a[116:119], v[180:183]\n"
"v_mfma_f32_16x16x32_bf16 v[180:183], a[72:75], a[112:115], v[180:183]\n"
"s_add_u32 s67, %21, 65536\n"
"v_mfma_f32_16x16x32_bf16 v[184:187], a[72:75], a[120:123], v[184:187]\n"
"v_mfma_f32_16x16x32_bf16 v[184:187], a[76:79], a[124:127], v[184:187]\n"
"v_mfma_f32_16x16x32_bf16 v[156:159], a[68:71], a[124:127], v[156:159]\n"
"v_mfma_f32_16x16x32_bf16 v[156:159], a[64:67], a[120:123], v[156:159]\n"
"buffer_load_dwordx4 v6, s[52:55], s67 offen offset:0  lds\n"
"v_mfma_f32_16x16x32_bf16 v[160:163], a[64:67], a[128:131], v[160:163]\n"
"v_mfma_f32_16x16x32_bf16 v[160:163], a[68:71], a[132:135], v[160:163]\n"
"global_load_dwordx4 a[56:59], v0, s[34:35] offset:2048 sc1\n"
"v_mfma_f32_16x16x32_bf16 v[188:191], a[76:79], a[132:135], v[188:191]\n"
"v_mfma_f32_16x16x32_bf16 v[188:191], a[72:75], a[128:131], v[188:191]\n"
"v_mfma_f32_16x16x32_bf16 v[192:195], a[72:75], a[136:139], v[192:195]\n"
"s_sub_u32 s66, 122880, s73\n"
"s_add_u32 s67, %21, 80896\n"
"v_mfma_f32_16x16x32_bf16 v[192:195], a[76:79], a[140:143], v[192:195]\n"
"v_mfma_f32_16x16x32_bf16 v[164:167], a[68:71], a[140:143], v[164:167]\n"
"v_add_u32 v12, s66, v7\n"
"v_mfma_f32_16x16x32_bf16 v[164:167], a[64:67], a[136:139], v[164:167]\n"
"v_mfma_f32_16x16x32_bf16 v[168:171], a[64:67], a[144:147], v[168:171]\n"
"v_add_u32 v13, s66, v8\n"
"v_mfma_f32_16x16x32_bf16 v[168:171], a[68:71], a[148:151], v[168:171]\n"
"global_load_dwordx4 a[60:63], v0, s[34:35] offset:3072 sc1\n"
"v_mfma_f32_16x16x32_bf16 v[196:199], a[76:79], a[148:151], v[196:199]\n"
"buffer_load_dwordx4 v6, s[52:55], s67 offen offset:1024  lds\n"
"v_mfma_f32_16x16x32_bf16 v[196:199], a[72:75], a[144:147], v[196:199]\n"
"v_mfma_f32_16x16x32_bf16 a[20:23], a[72:75], a[152:155], a[20:23]\n"
"s_add_u32 s34, s34, 8192\n"
"v_mfma_f32_16x16x32_bf16 a[20:23], a[76:79], a[156:159], a[20:23]\n"
"s_add_u32 s67, %21, 96256\n"
"v_mfma_f32_16x16x32_bf16 a[16:19], a[68:71], a[156:159], a[16:19]\n"
"s_barrier\n"
"v_mfma_f32_16x16x32_bf16 a[16:19], a[64:67], a[152:155], a[16:19]\n"
"s_waitcnt vmcnt(14)\n"
"v_mfma_f32_16x16x32_bf16 v[200:203], a[80:83], a[96:99], v[200:203]\n"
"buffer_load_dwordx4 v6, s[52:55], s67 offen offset:2048  lds\n"
"v_mfma_f32_16x16x32_bf16 v[200:203], a[84:87], a[100:103], v[200:203]\n"
"global_load_dwordx4 a[64:67], v0, s[34:35] offset:-4096 sc1\n"
"v_mfma_f32_16x16x32_bf16 v[228:231], a[92:95], a[100:103], v[228:231]\n"
"ds_read_b128 a[100:103], v13 offset:16384\n"
"v_mfma_f32_16x16x32_bf16 v[228:231], a[88:91], a[96:99], v[228:231]\n"
"ds_read_b128 a[96:99], v12 offset:16384\n"
"v_mfma_f32_16x16x32_bf16 v[232:235], a[88:91], a[104:107], v[232:235]\n"
"v_mfma_f32_16x16x32_bf16 v[232:235], a[92:95], a[108:111], v[232:235]\n"
"v_mfma_f32_16x16x32_bf16 v[204:207], a[84:87], a[108:111], v[204:207]\n"
"ds_read_b128 a[108:111], v13 offset:18432\n"
"v_mfma_f32_16x16x32_bf16 v[204:207], a[80:83], a[104:107], v[204:207]\n"
"s_add_u32 s67, %21, 111616\n"
"ds_read_b128 a[104:107], v12 offset:18432\n"
"v_mfma_f32_16x16x32_bf16 v[208:211], a[80:83], a[112:115], v[208:211]\n"
"v_mfma_f32_16x16x32_bf16 v[208:211], a[84:87], a[116:119], v[208:211]\n"
"global_load_dwordx4 a[68:71], v0, s[34:35] offset:-3072 sc1\n"
"v_mfma_f32_16x16x32_bf16 v[236:239], a[92:95], a[116:119], v[236:239]\n"
"ds_read_b128 a[116:119], v13 offset:20480\n"
"v_mfma_f32_16x16x32_bf16 v[236:239], a[88:91], a[112:115], v[236:239]\n"
"ds_read_b128 a[112:115], v12 offset:20480\n"
"v_mfma_f32_16x16x32_bf16 v[240:243], a[88:91], a[120:123], v[240:243]\n"
"buffer_load_dwordx4 v6, s[52:55], s67 offen offset:3072  lds\n"
"v_mfma_f32_16x16x32_bf16 v[240:243], a[92:95], a[124:127], v[240:243]\n"
"v_mfma_f32_16x16x32_bf16 v[212:215], a[84:87], a[124:127], v[212:215]\n"
"ds_read_b128 a[124:127], v13 offset:22528\n"
"v_mfma_f32_16x16x32_bf16 v[212:215], a[80:83], a[120:123], v[212:215]\n"
"ds_read_b128 a[120:123], v12 offset:22528\n"
"v_mfma_f32_16x16x32_bf16 v[216:219], a[80:83], a[128:131], v[216:219]\n"
"v_mfma_f32_16x16x32_bf16 v[216:219], a[84:87], a[132:135], v[216:219]\n"
"global_load_dwordx4 a[72:75], v0, s[34:35] offset:-2048 sc1\n"
"v_mfma_f32_16x16x32_bf16 v[244:247], a[92:95], a[132:135], v[244:247]\n"
"ds_read_b128 a[132:135], v13 offset:24576\n"
"v_mfma_f32_16x16x32_bf16 v[244:247], a[88:91], a[128:131], v[244:247]\n"
"ds_read_b128 a[128:131], v12 offset:24576\n"
"v_mfma_f32_16x16x32_bf16 v[248:251], a[88:91], a[136:139], v[248:251]\n"
"v_mfma_f32_16x16x32_bf16 v[248:251], a[92:95], a[140:143], v[248:251]\n"
"v_mfma_f32_16x16x32_bf16 v[220:223], a[84:87], a[140:143], v[220:223]\n"
"ds_read_b128 a[140:143], v13 offset:26624\n"
"v_mfma_f32_16x16x32_bf16 v[220:223], a[80:83], a[136:139], v[220:223]\n"
"ds_read_b128 a[136:139], v12 offset:26624\n"
"v_mfma_f32_16x16x32_bf16 v[224:227], a[80:83], a[144:147], v[224:227]\n"
"v_mfma_f32_16x16x32_bf16 v[224:227], a[84:87], a[148:151], v[224:227]\n"
"global_load_dwordx4 a[76:79], v0, s[34:35] offset:-1024 sc1\n"
"v_mfma_f32_16x16x32_bf16 v[252:255], a[92:95], a[148:151], v[252:255]\n"
"ds_read_b128 a[148:151], v13 offset:28672\n"
"v_mfma_f32_16x16x32_bf16 v[252:255], a[88:91], a[144:147], v[252:255]\n"
"ds_read_b128 a[144:147], v12 offset:28672\n"
"v_mfma_f32_16x16x32_bf16 a[28:31], a[88:91], a[152:155], a[28:31]\n"
"v_mfma_f32_16x16x32_bf16 a[28:31], a[92:95], a[156:159], a[28:31]\n"
"v_mfma_f32_16x16x32_bf16 a[24:27], a[84:87], a[156:159], a[24:27]\n"
"ds_read_b128 a[156:159], v13 offset:30720\n"
"v_mfma_f32_16x16x32_bf16 a[24:27], a[80:83], a[152:155], a[24:27]\n"
"s_waitcnt vmcnt(13) lgkmcnt(11)\n"
"ds_read_b128 a[152:155], v12 offset:30720\n"
"v_mfma_f32_16x16x32_bf16 v[32:35], a[32:35], a[96:99], v[32:35]\n"
"v_mfma_f32_16x16x32_bf16 v[32:35], a[36:39], a[100:103], v[32:35]\n"
"global_load_dwordx4 a[80:83], v0, s[34:35] offset:0 sc1\n"
"v_mfma_f32_16x16x32_bf16 v[60:63], a[44:47], a[100:103], v[60:63]\n"
"s_sub_u32 s71, %24, %25\n"
"v_mfma_f32_16x16x32_bf16 v[60:63], a[40:43], a[96:99], v[60:63]\n"
"s_lshl_b32 s71, s71, 10\n"
"v_mfma_f32_16x16x32_bf16 v[64:67], a[40:43], a[104:107], v[64:67]\n"
"v_mfma_f32_16x16x32_bf16 v[64:67], a[44:47], a[108:111], v[64:67]\n"
"v_mfma_f32_16x16x32_bf16 v[36:39], a[36:39], a[108:111], v[36:39]\n"
"v_mfma_f32_16x16x32_bf16 v[36:39], a[32:35], a[104:107], v[36:39]\n"
"s_waitcnt lgkmcnt(8)\n"
"v_mfma_f32_16x16x32_bf16 v[40:43], a[32:35], a[112:115], v[40:43]\n"
"v_mfma_f32_16x16x32_bf16 v[40:43], a[36:39], a[116:119], v[40:43]\n"
"global_load_dwordx4 a[84:87], v0, s[34:35] offset:1024 sc1\n"
"v_mfma_f32_16x16x32_bf16 v[68:71], a[44:47], a[116:119], v[68:71]\n"
"v_mfma_f32_16x16x32_bf16 v[68:71], a[40:43], a[112:115], v[68:71]\n"
"v_mfma_f32_16x16x32_bf16 v[72:75], a[40:43], a[120:123], v[72:75]\n"
"v_mfma_f32_16x16x32_bf16 v[72:75], a[44:47], a[124:127], v[72:75]\n"
"v_mfma_f32_16x16x32_bf16 v[44:47], a[36:39], a[124:127], v[44:47]\n"
"v_mfma_f32_16x16x32_bf16 v[44:47], a[32:35], a[120:123], v[44:47]\n"
"s_waitcnt lgkmcnt(4)\n"
"v_mfma_f32_16x16x32_bf16 v[48:51], a[32:35], a[128:131], v[48:51]\n"
"v_mfma_f32_16x16x32_bf16 v[48:51], a[36:39], a[132:135], v[48:51]\n"
"global_load_dwordx4 a[88:91], v0, s[34:35] offset:2048 sc1\n"
"v_mfma_f32_16x16x32_bf16 v[76:79], a[44:47], a[132:135], v[76:79]\n"
"v_mfma_f32_16x16x32_bf16 v[76:79], a[40:43], a[128:131], v[76:79]\n"
"v_mfma_f32_16x16x32_bf16 v[80:83], a[40:43], a[136:139], v[80:83]\n"
"v_mfma_f32_16x16x32_bf16 v[80:83], a[44:47], a[140:143], v[80:83]\n"
"v_mfma_f32_16x16x32_bf16 v[52:55], a[36:39], a[140:143], v[52:55]\n"
"v_mfma_f32_16x16x32_bf16 v[52:55], a[32:35], a[136:139], v[52:55]\n"
"s_waitcnt lgkmcnt(0)\n"
"v_mfma_f32_16x16x32_bf16 v[56:59], a[32:35], a[144:147], v[56:59]\n"
"v_mfma_f32_16x16x32_bf16 v[56:59], a[36:39], a[148:151], v[56:59]\n"
"global_load_dwordx4 a[92:95], v0, s[34:35] offset:3072 sc1\n"
"s_mov_b32 s98, 0\n"
"v_mfma_f32_16x16x32_bf16 v[84:87], a[44:47], a[148:151], v[84:87]\n"
"s_load_dword s98, s[96:97], 0 glc\n"
"v_mfma_f32_16x16x32_bf16 v[84:87], a[40:43], a[144:147], v[84:87]\n"
"v_mfma_f32_16x16x32_bf16 a[4:7], a[40:43], a[152:155], a[4:7]\n"
"s_add_u32 s34, s34, 8192\n"
"v_mfma_f32_16x16x32_bf16 a[4:7], a[44:47], a[156:159], a[4:7]\n"
"v_mfma_f32_16x16x32_bf16 a[0:3], a[36:39], a[156:159], a[0:3]\n"
"v_mfma_f32_16x16x32_bf16 a[0:3], a[32:35], a[152:155], a[0:3]\n"
"s_waitcnt vmcnt(11)\n"
"v_mfma_f32_16x16x32_bf16 v[88:91], a[48:51], a[96:99], v[88:91]\n"
"v_mfma_f32_16x16x32_bf16 v[88:91], a[52:55], a[100:103], v[88:91]\n"
"global_load_dwordx4 a[32:35], v0, s[34:35] offset:-4096 sc1\n"
"v_mfma_f32_16x16x32_bf16 v[116:119], a[60:63], a[100:103], v[116:119]\n"
"v_mfma_f32_16x16x32_bf16 v[116:119], a[56:59], a[96:99], v[116:119]\n"
"v_mfma_f32_16x16x32_bf16 v[120:123], a[56:59], a[104:107], v[120:123]\n"
"s_add_u32 s71, s71, 524288\n"
"v_mfma_f32_16x16x32_bf16 v[120:123], a[60:63], a[108:111], v[120:123]\n"
"v_mfma_f32_16x16x32_bf16 v[92:95], a[52:55], a[108:111], v[92:95]\n"
"v_mfma_f32_16x16x32_bf16 v[92:95], a[48:51], a[104:107], v[92:95]\n"
"s_add_u32 s69, s69, s98\n"
"v_mfma_f32_16x16x32_bf16 v[96:99], a[48:51], a[112:115], v[96:99]\n"
"s_add_u32 s69, s69, s98\n"
"v_mfma_f32_16x16x32_bf16 v[96:99], a[52:55], a[116:119], v[96:99]\n"
"global_load_dwordx4 a[36:39], v0, s[34:35] offset:-3072 sc1\n"
"s_add_u32 s69, s69, s98\n"
"v_mfma_f32_16x16x32_bf16 v[124:127], a[60:63], a[116:119], v[124:127]\n"
"s_add_u32 s69, s69, s98\n"
"v_mfma_f32_16x16x32_bf16 v[124:127], a[56:59], a[112:115], v[124:127]\n"
"s_add_u32 s69, s69, s98\n"
"v_mfma_f32_16x16x32_bf16 v[128:131], a[56:59], a[120:123], v[128:131]\n"
"s_add_u32 s69, s69, s98\n"
"v_mfma_f32_16x16x32_bf16 v[128:131], a[60:63], a[124:127], v[128:131]\n"
"s_add_u32 s69, s69, s98\n"
"v_mfma_f32_16x16x32_bf16 v[100:103], a[52:55], a[124:127], v[100:103]\n"
"s_add_u32 s69, s69, s98\n"
"v_mfma_f32_16x16x32_bf16 v[100:103], a[48:51], a[120:123], v[100:103]\n"
"s_add_u32 s69, s69, s98\n"
"v_mfma_f32_16x16x32_bf16 v[104:107], a[48:51], a[128:131], v[104:107]\n"
"s_add_u32 s69, s69, s98\n"
"v_mfma_f32_16x16x32_bf16 v[104:107], a[52:55], a[132:135], v[104:107]\n"
"global_load_dwordx4 a[40:43], v0, s[34:35] offset:-2048 sc1\n"
"s_add_u32 s69, s69, s98\n"
"v_mfma_f32_16x16x32_bf16 v[132:135], a[60:63], a[132:135], v[132:135]\n"
"s_add_u32 s69, s69, s98\n"
"v_mfma_f32_16x16x32_bf16 v[132:135], a[56:59], a[128:131], v[132:135]\n"
"s_add_u32 s69, s69, s98\n"
"v_mfma_f32_16x16x32_bf16 v[136:139], a[56:59], a[136:139], v[136:139]\n"
"s_add_u32 s69, s69, s98\n"
"v_mfma_f32_16x16x32_bf16 v[136:139], a[60:63], a[140:143], v[136:139]\n"
"s_add_u32 s69, s69, s98\n"
"v_mfma_f32_16x16x32_bf16 v[108:111], a[52:55], a[140:143], v[108:111]\n"
"s_add_u32 s69, s69, s98\n"
"v_mfma_f32_16x16x32_bf16 v[108:111], a[48:51], a[136:139], v[108:111]\n"
"s_add_u32 s69, s69, s98\n"
"v_mfma_f32_16x16x32_bf16 v[112:115], a[48:51], a[144:147], v[112:115]\n"
"s_add_u32 s69, s69, s98\n"
"v_mfma_f32_16x16x32_bf16 v[112:115], a[52:55], a[148:151], v[112:115]\n"
"global_load_dwordx4 a[44:47], v0, s[34:35] offset:-1024 sc1\n"
"s_add_u32 s69, s69, s98\n"
"v_mfma_f32_16x16x32_bf16 v[140:143], a[60:63], a[148:151], v[140:143]\n"
"s_add_u32 s69, s69, s98\n"
"s_waitcnt lgkmcnt(0)\n"
"v_mfma_f32_16x16x32_bf16 v[140:143], a[56:59], a[144:147], v[140:143]\n"
"s_mov_b32 s98, 0\n"
"v_mfma_f32_16x16x32_bf16 a[12:15], a[56:59], a[152:155], a[12:15]\n"
"s_load_dword s98, s[96:97], 8320 glc\n"
"v_mfma_f32_16x16x32_bf16 a[12:15], a[60:63], a[156:159], a[12:15]\n"
"v_mfma_f32_16x16x32_bf16 a[8:11], a[52:55], a[156:159], a[8:11]\n"
"v_mfma_f32_16x16x32_bf16 a[8:11], a[48:51], a[152:155], a[8:11]\n"
"s_waitcnt vmcnt(8)\n"
"v_mfma_f32_16x16x32_bf16 v[144:147], a[64:67], a[96:99], v[144:147]\n"
"v_mfma_f32_16x16x32_bf16 v[144:147], a[68:71], a[100:103], v[144:147]\n"
"global_load_dwordx4 a[48:51], v0, s[34:35] offset:0 sc1\n"
"v_mfma_f32_16x16x32_bf16 v[172:175], a[76:79], a[100:103], v[172:175]\n"
"v_mfma_f32_16x16x32_bf16 v[172:175], a[72:75], a[96:99], v[172:175]\n"
"v_mfma_f32_16x16x32_bf16 v[176:179], a[72:75], a[104:107], v[176:179]\n"
"v_mfma_f32_16x16x32_bf16 v[176:179], a[76:79], a[108:111], v[176:179]\n"
"v_mfma_f32_16x16x32_bf16 v[148:151], a[68:71], a[108:111], v[148:151]\n"
"v_mfma_f32_16x16x32_bf16 v[148:151], a[64:67], a[104:107], v[148:151]\n"
"v_mfma_f32_16x16x32_bf16 v[152:155], a[64:67], a[112:115], v[152:155]\n"
"v_mfma_f32_16x16x32_bf16 v[152:155], a[68:71], a[116:119], v[152:155]\n"
"global_load_dwordx4 a[52:55], v0, s[34:35] offset:1024 sc1\n"
"s_sub_u32 s69, s69, s98\n"
"v_mfma_f32_16x16x32_bf16 v[180:183], a[76:79], a[116:119], v[180:183]\n"
"s_sub_u32 s69, s69, s98\n"
"v_mfma_f32_16x16x32_bf16 v[180:183], a[72:75], a[112:115], v[180:183]\n"
"s_sub_u32 s69, s69, s98\n"
"v_mfma_f32_16x16x32_bf16 v[184:187], a[72:75], a[120:123], v[184:187]\n"
"s_sub_u32 s69, s69, s98\n"
"v_mfma_f32_16x16x32_bf16 v[184:187], a[76:79], a[124:127], v[184:187]\n"
"s_sub_u32 s69, s69, s98\n"
"v_mfma_f32_16x16x32_bf16 v[156:159], a[68:71], a[124:127], v[156:159]\n"
"s_sub_u32 s69, s69, s98\n"
"v_mfma_f32_16x16x32_bf16 v[156:159], a[64:67], a[120:123], v[156:159]\n"
"s_sub_u32 s69, s69, s98\n"
"v_mfma_f32_16x16x32_bf16 v[160:163], a[64:67], a[128:131], v[160:163]\n"
"s_sub_u32 s69, s69, s98\n"
"v_mfma_f32_16x16x32_bf16 v[160:163], a[68:71], a[132:135], v[160:163]\n"
"global_load_dwordx4 a[56:59], v0, s[34:35] offset:2048 sc1\n"
"s_sub_u32 s69, s69, s98\n"
"v_mfma_f32_16x16x32_bf16 v[188:191], a[76:79], a[132:135], v[188:191]\n"
"s_sub_u32 s69, s69, s98\n"
"v_mfma_f32_16x16x32_bf16 v[188:191], a[72:75], a[128:131], v[188:191]\n"
"s_sub_u32 s69, s69, s98\n"
"v_mfma_f32_16x16x32_bf16 v[192:195], a[72:75], a[136:139], v[192:195]\n"
"s_sub_u32 s69, s69, s98\n"
"v_sub_u32 v13, v2, v2\n"
"v_mfma_f32_16x16x32_bf16 v[192:195], a[76:79], a[140:143], v[192:195]\n"
"s_sub_u32 s69, s69, s98\n"
"v_mfma_f32_16x16x32_bf16 v[164:167], a[68:71], a[140:143], v[164:167]\n"
"s_sub_u32 s69, s69, s98\n"
"v_add_u32 v12, v13, v7\n"
"v_mfma_f32_16x16x32_bf16 v[164:167], a[64:67], a[136:139], v[164:167]\n"
"s_sub_u32 s69, s69, s98\n"
"v_mfma_f32_16x16x32_bf16 v[168:171], a[64:67], a[144:147], v[168:171]\n"
"s_sub_u32 s69, s69, s98\n"
"v_add_u32 v13, v13, v8\n"
"v_mfma_f32_16x16x32_bf16 v[168:171], a[68:71], a[148:151], v[168:171]\n"
"global_load_dwordx4 a[60:63], v0, s[34:35] offset:3072 sc1\n"
"s_sub_u32 s69, s69, s98\n"
"v_mfma_f32_16x16x32_bf16 v[196:199], a[76:79], a[148:151], v[196:199]\n"
"s_sub_u32 s69, s69, s98\n"
"v_mfma_f32_16x16x32_bf16 v[196:199], a[72:75], a[144:147], v[196:199]\n"
"s_sub_u32 s69, s69, s98\n"
"v_mfma_f32_16x16x32_bf16 a[20:23], a[72:75], a[152:155], a[20:23]\n"
"s_add_u32 s34, s34, 8192\n"
"s_sub_u32 s69, s69, s98\n"
"s_waitcnt lgkmcnt(0)\n"
"v_mfma_f32_16x16x32_bf16 a[20:23], a[76:79], a[156:159], a[20:23]\n"
"v_mfma_f32_16x16x32_bf16 a[16:19], a[68:71], a[156:159], a[16:19]\n"
"v_mfma_f32_16x16x32_bf16 a[16:19], a[64:67], a[152:155], a[16:19]\n"
"s_waitcnt vmcnt(8)\n"
"v_mfma_f32_16x16x32_bf16 v[200:203], a[80:83], a[96:99], v[200:203]\n"
"ds_read_b64_tr_b16 a[192:193], v9 offset:32768\n"
"v_mfma_f32_16x16x32_bf16 v[200:203], a[84:87], a[100:103], v[200:203]\n"
"global_load_dwordx4 a[64:67], v0, s[34:35] offset:-4096 sc1\n"
"ds_read_b64_tr_b16 a[194:195], v9 offset:33344\n"
"v_mfma_f32_16x16x32_bf16 v[228:231], a[92:95], a[100:103], v[228:231]\n"
"ds_read_b128 a[100:103], v13 offset:0\n"
"v_mfma_f32_16x16x32_bf16 v[228:231], a[88:91], a[96:99], v[228:231]\n"
"ds_read_b128 a[96:99], v12 offset:0\n"
"v_mfma_f32_16x16x32_bf16 v[232:235], a[88:91], a[104:107], v[232:235]\n"
"ds_read_b64_tr_b16 a[196:197], v9 offset:36864\n"
"v_mfma_f32_16x16x32_bf16 v[232:235], a[92:95], a[108:111], v[232:235]\n"
"ds_read_b64_tr_b16 a[198:199], v9 offset:37440\n"
"v_mfma_f32_16x16x32_bf16 v[204:207], a[84:87], a[108:111], v[204:207]\n"
"ds_read_b128 a[108:111], v13 offset:2048\n"
"v_mfma_f32_16x16x32_bf16 v[204:207], a[80:83], a[104:107], v[204:207]\n"
"ds_read_b128 a[104:107], v12 offset:2048\n"
"v_mfma_f32_16x16x32_bf16 v[208:211], a[80:83], a[112:115], v[208:211]\n"
"ds_read_b64_tr_b16 a[200:201], v9 offset:32776\n"
"v_mfma_f32_16x16x32_bf16 v[208:211], a[84:87], a[116:119], v[208:211]\n"
"global_load_dwordx4 a[68:71], v0, s[34:35] offset:-3072 sc1\n"
"ds_read_b64_tr_b16 a[202:203], v9 offset:33352\n"
"v_mfma_f32_16x16x32_bf16 v[236:239], a[92:95], a[116:119], v[236:239]\n"
"ds_read_b128 a[116:119], v13 offset:4096\n"
"v_mfma_f32_16x16x32_bf16 v[236:239], a[88:91], a[112:115], v[236:239]\n"
"ds_read_b128 a[112:115], v12 offset:4096\n"
"v_mfma_f32_16x16x32_bf16 v[240:243], a[88:91], a[120:123], v[240:243]\n"
"ds_read_b64_tr_b16 a[204:205], v9 offset:36872\n"
"v_mfma_f32_16x16x32_bf16 v[240:243], a[92:95], a[124:127], v[240:243]\n"
"ds_read_b64_tr_b16 a[206:207], v9 offset:37448\n"
"v_mfma_f32_16x16x32_bf16 v[212:215], a[84:87], a[124:127], v[212:215]\n"
"ds_read_b128 a[124:127], v13 offset:6144\n"
"v_mfma_f32_16x16x32_bf16 v[212:215], a[80:83], a[120:123], v[212:215]\n"
"ds_read_b128 a[120:123], v12 offset:6144\n"
"v_mfma_f32_16x16x32_bf16 v[216:219], a[80:83], a[128:131], v[216:219]\n"
"ds_read_b64_tr_b16 a[208:209], v9 offset:32832\n"
"v_mfma_f32_16x16x32_bf16 v[216:219], a[84:87], a[132:135], v[216:219]\n"
"global_load_dwordx4 a[72:75], v0, s[34:35] offset:-2048 sc1\n"
"ds_read_b64_tr_b16 a[210:211], v9 offset:33280\n"
"v_mfma_f32_16x16x32_bf16 v[244:247], a[92:95], a[132:135], v[244:247]\n"
"ds_read_b128 a[132:135], v13 offset:8192\n"
"v_mfma_f32_16x16x32_bf16 v[244:247], a[88:91], a[128:131], v[244:247]\n"
"ds_read_b128 a[128:131], v12 offset:8192\n"
"v_mfma_f32_16x16x32_bf16 v[248:251], a[88:91], a[136:139], v[248:251]\n"
"ds_read_b64_tr_b16 a[212:213], v9 offset:36928\n"
"v_mfma_f32_16x16x32_bf16 v[248:251], a[92:95], a[140:143], v[248:251]\n"
"ds_read_b64_tr_b16 a[214:215], v9 offset:37376\n"
"v_mfma_f32_16x16x32_bf16 v[220:223], a[84:87], a[140:143], v[220:223]\n"
"ds_read_b128 a[140:143], v13 offset:10240\n"
"v_mfma_f32_16x16x32_bf16 v[220:223], a[80:83], a[136:139], v[220:223]\n"
"ds_read_b128 a[136:139], v12 offset:10240\n"
"v_mfma_f32_16x16x32_bf16 v[224:227], a[80:83], a[144:147], v[224:227]\n"
"ds_read_b64_tr_b16 a[216:217], v9 offset:32840\n"
"v_mfma_f32_16x16x32_bf16 v[224:227], a[84:87], a[148:151], v[224:227]\n"
"global_load_dwordx4 a[76:79], v0, s[34:35] offset:-1024 sc1\n"
"ds_read_b64_tr_b16 a[218:219], v9 offset:33288\n"
"v_mfma_f32_16x16x32_bf16 v[252:255], a[92:95], a[148:151], v[252:255]\n"
"ds_read_b128 a[148:151], v13 offset:12288\n"
"v_mfma_f32_16x16x32_bf16 v[252:255], a[88:91], a[144:147], v[252:255]\n"
"ds_read_b128 a[144:147], v12 offset:12288\n"
"v_mfma_f32_16x16x32_bf16 a[28:31], a[88:91], a[152:155], a[28:31]\n"
"ds_read_b64_tr_b16 a[220:221], v9 offset:36936\n"
"v_mfma_f32_16x16x32_bf16 a[28:31], a[92:95], a[156:159], a[28:31]\n"
"ds_read_b64_tr_b16 a[222:223], v9 offset:37384\n"
"v_mfma_f32_16x16x32_bf16 a[24:27], a[84:87], a[156:159], a[24:27]\n"
"ds_read_b128 a[156:159], v13 offset:14336\n"
"v_mfma_f32_16x16x32_bf16 a[24:27], a[80:83], a[152:155], a[24:27]\n"
"s_waitcnt vmcnt(8) lgkmcnt(15)\n"
"ds_read_b128 a[152:155], v12 offset:14336\n"
"v_mfma_f32_16x16x32_bf16 v[32:35], a[32:35], a[96:99], v[32:35]\n"
"v_mfma_f32_16x16x32_bf16 v[32:35], a[36:39], a[100:103], v[32:35]\n"
"global_load_dwordx4 a[80:83], v0, s[34:35] offset:0 sc1\n"
"v_mfma_f32_16x16x32_bf16 v[60:63], a[44:47], a[100:103], v[60:63]\n"
"v_mfma_f32_16x16x32_bf16 v[60:63], a[40:43], a[96:99], v[60:63]\n"
"v_mfma_f32_16x16x32_bf16 v[64:67], a[40:43], a[104:107], v[64:67]\n"
"s_barrier\n"
"v_mfma_f32_16x16x32_bf16 v[64:67], a[44:47], a[108:111], v[64:67]\n"
"v_mfma_f32_16x16x32_bf16 v[36:39], a[36:39], a[108:111], v[36:39]\n"
"v_mfma_f32_16x16x32_bf16 v[36:39], a[32:35], a[104:107], v[36:39]\n"
"v_mfma_f32_16x16x32_bf16 v[40:43], a[32:35], a[112:115], v[40:43]\n"
"ds_write_b128 v2, a[96:99] offset:32768\n"
"v_mfma_f32_16x16x32_bf16 v[40:43], a[36:39], a[116:119], v[40:43]\n"
"global_load_dwordx4 a[84:87], v0, s[34:35] offset:1024 sc1\n"
"v_mfma_f32_16x16x32_bf16 v[68:71], a[44:47], a[116:119], v[68:71]\n"
"v_mfma_f32_16x16x32_bf16 v[68:71], a[40:43], a[112:115], v[68:71]\n"
"s_add_u32 m0, s64, 16256\n"
"ds_write_b128 v2, a[100:103] offset:33792\n"
"v_mfma_f32_16x16x32_bf16 v[72:75], a[40:43], a[120:123], v[72:75]\n"
"v_mfma_f32_16x16x32_bf16 v[72:75], a[44:47], a[124:127], v[72:75]\n"
"ds_write_b128 v2, a[104:107] offset:34816\n"
"v_mfma_f32_16x16x32_bf16 v[44:47], a[36:39], a[124:127], v[44:47]\n"
"v_mfma_f32_16x16x32_bf16 v[44:47], a[32:35], a[120:123], v[44:47]\n"
"s_waitcnt lgkmcnt(11)\n"
"ds_write_b128 v2, a[108:111] offset:35840\n"
"v_mfma_f32_16x16x32_bf16 v[48:51], a[32:35], a[128:131], v[48:51]\n"
"v_mfma_f32_16x16x32_bf16 v[48:51], a[36:39], a[132:135], v[48:51]\n"
"global_load_dwordx4 a[88:91], v0, s[34:35] offset:2048 sc1\n"
"v_mfma_f32_16x16x32_bf16 v[76:79], a[44:47], a[132:135], v[76:79]\n"
"ds_write_b128 v2, a[112:115] offset:36864\n"
"v_mfma_f32_16x16x32_bf16 v[76:79], a[40:43], a[128:131], v[76:79]\n"
"ds_write_b128 v2, a[116:119] offset:37888\n"
"v_mfma_f32_16x16x32_bf16 v[80:83], a[40:43], a[136:139], v[80:83]\n"
"v_mfma_f32_16x16x32_bf16 v[80:83], a[44:47], a[140:143], v[80:83]\n"
"buffer_load_dwordx4 v6, s[40:43], s80 offen offset:128  lds\n"
"v_mfma_f32_16x16x32_bf16 v[52:55], a[36:39], a[140:143], v[52:55]\n"
"ds_write_b128 v2, a[120:123] offset:38912\n"
"v_mfma_f32_16x16x32_bf16 v[52:55], a[32:35], a[136:139], v[52:55]\n"
"s_waitcnt lgkmcnt(7)\n"
"ds_write_b128 v2, a[124:127] offset:39936\n"
"v_mfma_f32_16x16x32_bf16 v[56:59], a[32:35], a[144:147], v[56:59]\n"
"v_mfma_f32_16x16x32_bf16 v[56:59], a[36:39], a[148:151], v[56:59]\n"
"global_load_dwordx4 a[92:95], v0, s[34:35] offset:3072 sc1\n"
"s_mov_b32 s98, 0\n"
"v_mfma_f32_16x16x32_bf16 v[84:87], a[44:47], a[148:151], v[84:87]\n"
"s_load_dword s98, s[96:97], 512 glc\n"
"v_mfma_f32_16x16x32_bf16 v[84:87], a[40:43], a[144:147], v[84:87]\n"
"v_mfma_f32_16x16x32_bf16 a[4:7], a[40:43], a[152:155], a[4:7]\n"
"s_add_u32 s34, s34, 8192\n"
"v_mfma_f32_16x16x32_bf16 a[4:7], a[44:47], a[156:159], a[4:7]\n"
"v_mfma_f32_16x16x32_bf16 a[0:3], a[36:39], a[156:159], a[0:3]\n"
"v_accvgpr_mov_b32 a160, a128\n"
"v_mfma_f32_16x16x32_bf16 a[0:3], a[32:35], a[152:155], a[0:3]\n"
"s_waitcnt vmcnt(9)\n"
"buffer_load_dwordx4 v6, s[40:43], s81 offen offset:1152  lds\n"
"v_mfma_f32_16x16x32_bf16 v[88:91], a[48:51], a[96:99], v[88:91]\n"
"v_accvgpr_mov_b32 a161, a129\n"
"v_mfma_f32_16x16x32_bf16 v[88:91], a[52:55], a[100:103], v[88:91]\n"
"global_load_dwordx4 a[32:35], v0, s[34:35] offset:-4096 sc1\n"
"v_mfma_f32_16x16x32_bf16 v[116:119], a[60:63], a[100:103], v[116:119]\n"
"v_mfma_f32_16x16x32_bf16 v[116:119], a[56:59], a[96:99], v[116:119]\n"
"v_accvgpr_mov_b32 a162, a130\n"
"v_mfma_f32_16x16x32_bf16 v[120:123], a[56:59], a[104:107], v[120:123]\n"
"v_mfma_f32_16x16x32_bf16 v[120:123], a[60:63], a[108:111], v[120:123]\n"
"v_accvgpr_mov_b32 a163, a131\n"
"v_mfma_f32_16x16x32_bf16 v[92:95], a[52:55], a[108:111], v[92:95]\n"
"v_mfma_f32_16x16x32_bf16 v[92:95], a[48:51], a[104:107], v[92:95]\n"
"s_add_u32 s69, s69, s98\n"
"v_accvgpr_mov_b32 a164, a132\n"
"v_mfma_f32_16x16x32_bf16 v[96:99], a[48:51], a[112:115], v[96:99]\n"
"s_add_u32 s69, s69, s98\n"
"v_accvgpr_mov_b32 a165, a133\n"
"v_mfma_f32_16x16x32_bf16 v[96:99], a[52:55], a[116:119], v[96:99]\n"
"global_load_dwordx4 a[36:39], v0, s[34:35] offset:-3072 sc1\n"
"s_add_u32 s69, s69, s98\n"
"v_mfma_f32_16x16x32_bf16 v[124:127], a[60:63], a[116:119], v[124:127]\n"
"buffer_load_dwordx4 v6, s[40:43], s82 offen offset:2176  lds\n"
"s_add_u32 s69, s69, s98\n"
"v_mfma_f32_16x16x32_bf16 v[124:127], a[56:59], a[112:115], v[124:127]\n"
"s_add_u32 s69, s69, s98\n"
"v_accvgpr_mov_b32 a166, a134\n"
"v_mfma_f32_16x16x32_bf16 v[128:131], a[56:59], a[120:123], v[128:131]\n"
"s_add_u32 s69, s69, s98\n"
"v_mfma_f32_16x16x32_bf16 v[128:131], a[60:63], a[124:127], v[128:131]\n"
"s_add_u32 s69, s69, s98\n"
"v_accvgpr_mov_b32 a167, a135\n"
"v_mfma_f32_16x16x32_bf16 v[100:103], a[52:55], a[124:127], v[100:103]\n"
"s_add_u32 s69, s69, s98\n"
"v_mfma_f32_16x16x32_bf16 v[100:103], a[48:51], a[120:123], v[100:103]\n"
"s_add_u32 s69, s69, s98\n"
"v_accvgpr_mov_b32 a168, a136\n"
"v_mfma_f32_16x16x32_bf16 v[104:107], a[48:51], a[128:131], v[104:107]\n"
"s_add_u32 s69, s69, s98\n"
"v_mfma_f32_16x16x32_bf16 v[104:107], a[52:55], a[132:135], v[104:107]\n"
"global_load_dwordx4 a[40:43], v0, s[34:35] offset:-2048 sc1\n"
"s_add_u32 s69, s69, s98\n"
"v_mfma_f32_16x16x32_bf16 v[132:135], a[60:63], a[132:135], v[132:135]\n"
"s_add_u32 s69, s69, s98\n"
"v_accvgpr_mov_b32 a169, a137\n"
"v_mfma_f32_16x16x32_bf16 v[132:135], a[56:59], a[128:131], v[132:135]\n"
"s_add_u32 s69, s69, s98\n"
"v_accvgpr_mov_b32 a170, a138\n"
"v_mfma_f32_16x16x32_bf16 v[136:139], a[56:59], a[136:139], v[136:139]\n"
"buffer_load_dwordx4 v6, s[40:43], s83 offen offset:3200  lds\n"
"s_add_u32 s69, s69, s98\n"
"v_mfma_f32_16x16x32_bf16 v[136:139], a[60:63], a[140:143], v[136:139]\n"
"s_add_u32 s69, s69, s98\n"
"v_mfma_f32_16x16x32_bf16 v[108:111], a[52:55], a[140:143], v[108:111]\n"
"s_add_u32 s69, s69, s98\n"
"v_accvgpr_mov_b32 a171, a139\n"
"v_mfma_f32_16x16x32_bf16 v[108:111], a[48:51], a[136:139], v[108:111]\n"
"s_add_u32 s69, s69, s98\n"
"v_mfma_f32_16x16x32_bf16 v[112:115], a[48:51], a[144:147], v[112:115]\n"
"s_add_u32 s69, s69, s98\n"
"v_accvgpr_mov_b32 a172, a140\n"
"v_mfma_f32_16x16x32_bf16 v[112:115], a[52:55], a[148:151], v[112:115]\n"
"global_load_dwordx4 a[44:47], v0, s[34:35] offset:-1024 sc1\n"
"s_add_u32 s69, s69, s98\n"
"v_mfma_f32_16x16x32_bf16 v[140:143], a[60:63], a[148:151], v[140:143]\n"
"s_add_u32 s69, s69, s98\n"
"s_waitcnt lgkmcnt(0)\n"
"v_mfma_f32_16x16x32_bf16 v[140:143], a[56:59], a[144:147], v[140:143]\n"
"s_mov_b32 s98, 0\n"
"v_accvgpr_mov_b32 a173, a141\n"
"v_mfma_f32_16x16x32_bf16 a[12:15], a[56:59], a[152:155], a[12:15]\n"
"s_load_dword s98, s[96:97], 8832 glc\n"
"v_accvgpr_mov_b32 a174, a142\n"
"v_mfma_f32_16x16x32_bf16 a[12:15], a[60:63], a[156:159], a[12:15]\n"
"v_mfma_f32_16x16x32_bf16 a[8:11], a[52:55], a[156:159], a[8:11]\n"
"s_add_u32 m0, s64, 20352\n"
"v_accvgpr_mov_b32 a175, a143\n"
"v_mfma_f32_16x16x32_bf16 a[8:11], a[48:51], a[152:155], a[8:11]\n"
"s_waitcnt vmcnt(12)\n"
"v_mfma_f32_16x16x32_bf16 v[144:147], a[64:67], a[96:99], v[144:147]\n"
"v_accvgpr_mov_b32 a176, a144\n"
"v_mfma_f32_16x16x32_bf16 v[144:147], a[68:71], a[100:103], v[144:147]\n"
"global_load_dwordx4 a[48:51], v0, s[34:35] offset:0 sc1\n"
"v_mfma_f32_16x16x32_bf16 v[172:175], a[76:79], a[100:103], v[172:175]\n"
"v_accvgpr_mov_b32 a177, a145\n"
"v_mfma_f32_16x16x32_bf16 v[172:175], a[72:75], a[96:99], v[172:175]\n"
"v_mfma_f32_16x16x32_bf16 v[176:179], a[72:75], a[104:107], v[176:179]\n"
"v_accvgpr_mov_b32 a178, a146\n"
"v_mfma_f32_16x16x32_bf16 v[176:179], a[76:79], a[108:111], v[176:179]\n"
"v_mfma_f32_16x16x32_bf16 v[148:151], a[68:71], a[108:111], v[148:151]\n"
"v_accvgpr_mov_b32 a179, a147\n"
"v_mfma_f32_16x16x32_bf16 v[148:151], a[64:67], a[104:107], v[148:151]\n"
"v_accvgpr_mov_b32 a180, a148\n"
"v_mfma_f32_16x16x32_bf16 v[152:155], a[64:67], a[112:115], v[152:155]\n"
"buffer_load_dwordx4 v6, s[40:43], s84 offen offset:128  lds\n"
"v_mfma_f32_16x16x32_bf16 v[152:155], a[68:71], a[116:119], v[152:155]\n"
"global_load_dwordx4 a[52:55], v0, s[34:35] offset:1024 sc1\n"
"s_sub_u32 s69, s69, s98\n"
"v_mfma_f32_16x16x32_bf16 v[180:183], a[76:79], a[116:119], v[180:183]\n"
"s_sub_u32 s69, s69, s98\n"
"v_mfma_f32_16x16x32_bf16 v[180:183], a[72:75], a[112:115], v[180:183]\n"
"s_sub_u32 s69, s69, s98\n"
"v_accvgpr_mov_b32 a181, a149\n"
"v_mfma_f32_16x16x32_bf16 v[184:187], a[72:75], a[120:123], v[184:187]\n"
"s_sub_u32 s69, s69, s98\n"
"v_mfma_f32_16x16x32_bf16 v[184:187], a[76:79], a[124:127], v[184:187]\n"
"s_sub_u32 s69, s69, s98\n"
"v_accvgpr_mov_b32 a182, a150\n"
"v_mfma_f32_16x16x32_bf16 v[156:159], a[68:71], a[124:127], v[156:159]\n"
"s_sub_u32 s69, s69, s98\n"
"v_mfma_f32_16x16x32_bf16 v[156:159], a[64:67], a[120:123], v[156:159]\n"
"s_sub_u32 s69, s69, s98\n"
"v_accvgpr_mov_b32 a183, a151\n"
"v_mfma_f32_16x16x32_bf16 v[160:163], a[64:67], a[128:131], v[160:163]\n"
"s_sub_u32 s69, s69, s98\n"
"v_accvgpr_mov_b32 a184, a152\n"
"v_mfma_f32_16x16x32_bf16 v[160:163], a[68:71], a[132:135], v[160:163]\n"
"global_load_dwordx4 a[56:59], v0, s[34:35] offset:2048 sc1\n"
"s_sub_u32 s69, s69, s98\n"
"v_mfma_f32_16x16x32_bf16 v[188:191], a[76:79], a[132:135], v[188:191]\n"
"s_sub_u32 s69, s69, s98\n"
"v_accvgpr_mov_b32 a185, a153\n"
"v_mfma_f32_16x16x32_bf16 v[188:191], a[72:75], a[128:131], v[188:191]\n"
"buffer_load_dwordx4 v6, s[40:43], s85 offen offset:1152  lds\n"
"s_sub_u32 s69, s69, s98\n"
"v_mfma_f32_16x16x32_bf16 v[192:195], a[72:75], a[136:139], v[192:195]\n"
"s_sub_u32 s69, s69, s98\n"
"v_sub_u32 v13, v3, v2\n"
"v_mfma_f32_16x16x32_bf16 v[192:195], a[76:79], a[140:143], v[192:195]\n"
"s_sub_u32 s69, s69, s98\n"
"v_accvgpr_mov_b32 a186, a154\n"
"v_mfma_f32_16x16x32_bf16 v[164:167], a[68:71], a[140:143], v[164:167]\n"
"s_sub_u32 s69, s69, s98\n"
"v_add_u32 v12, v13, v7\n"
"v_mfma_f32_16x16x32_bf16 v[164:167], a[64:67], a[136:139], v[164:167]\n"
"s_sub_u32 s69, s69, s98\n"
"v_accvgpr_mov_b32 a187, a155\n"
"v_mfma_f32_16x16x32_bf16 v[168:171], a[64:67], a[144:147], v[168:171]\n"
"s_sub_u32 s69, s69, s98\n"
"v_add_u32 v13, v13, v8\n"
"v_mfma_f32_16x16x32_bf16 v[168:171], a[68:71], a[148:151], v[168:171]\n"
"global_load_dwordx4 a[60:63], v0, s[34:35] offset:3072 sc1\n"
"s_sub_u32 s69, s69, s98\n"
"v_mfma_f32_16x16x32_bf16 v[196:199], a[76:79], a[148:151], v[196:199]\n"
"s_sub_u32 s69, s69, s98\n"
"v_accvgpr_mov_b32 a188, a156\n"
"v_mfma_f32_16x16x32_bf16 v[196:199], a[72:75], a[144:147], v[196:199]\n"
"s_sub_u32 s69, s69, s98\n"
"v_accvgpr_mov_b32 a189, a157\n"
"v_mfma_f32_16x16x32_bf16 a[20:23], a[72:75], a[152:155], a[20:23]\n"
"s_add_u32 s34, s34, 8192\n"
"s_sub_u32 s69, s69, s98\n"
"s_waitcnt lgkmcnt(0)\n"
"v_mfma_f32_16x16x32_bf16 a[20:23], a[76:79], a[156:159], a[20:23]\n"
"buffer_load_dwordx4 v6, s[40:43], s86 offen offset:2176  lds\n"
"v_mfma_f32_16x16x32_bf16 a[16:19], a[68:71], a[156:159], a[16:19]\n"
"v_accvgpr_mov_b32 a190, a158\n"
"v_mfma_f32_16x16x32_bf16 a[16:19], a[64:67], a[152:155], a[16:19]\n"
"s_waitcnt vmcnt(14)\n"
"v_accvgpr_mov_b32 a191, a159\n"
"v_mfma_f32_16x16x32_bf16 v[200:203], a[80:83], a[96:99], v[200:203]\n"
"v_mfma_f32_16x16x32_bf16 v[200:203], a[84:87], a[100:103], v[200:203]\n"
"global_load_dwordx4 a[64:67], v0, s[34:35] offset:-4096 sc1\n"
"v_mfma_f32_16x16x32_bf16 v[228:231], a[92:95], a[100:103], v[228:231]\n"
"ds_read_b128 a[100:103], v13 offset:0\n"
"v_mfma_f32_16x16x32_bf16 v[228:231], a[88:91], a[96:99], v[228:231]\n"
"ds_read_b128 a[96:99], v12 offset:0\n"
"v_mfma_f32_16x16x32_bf16 v[232:235], a[88:91], a[104:107], v[232:235]\n"
"v_mfma_f32_16x16x32_bf16 v[232:235], a[92:95], a[108:111], v[232:235]\n"
"v_mfma_f32_16x16x32_bf16 v[204:207], a[84:87], a[108:111], v[204:207]\n"
"ds_read_b128 a[108:111], v13 offset:2048\n"
"v_mfma_f32_16x16x32_bf16 v[204:207], a[80:83], a[104:107], v[204:207]\n"
"ds_read_b128 a[104:107], v12 offset:2048\n"
"v_mfma_f32_16x16x32_bf16 v[208:211], a[80:83], a[112:115], v[208:211]\n"
"buffer_load_dwordx4 v6, s[40:43], s87 offen offset:3200  lds\n"
"v_mfma_f32_16x16x32_bf16 v[208:211], a[84:87], a[116:119], v[208:211]\n"
"global_load_dwordx4 a[68:71], v0, s[34:35] offset:-3072 sc1\n"
"v_mfma_f32_16x16x32_bf16 v[236:239], a[92:95], a[116:119], v[236:239]\n"
"ds_read_b128 a[116:119], v13 offset:4096\n"
"v_mfma_f32_16x16x32_bf16 v[236:239], a[88:91], a[112:115], v[236:239]\n"
"ds_read_b128 a[112:115], v12 offset:4096\n"
"v_mfma_f32_16x16x32_bf16 v[240:243], a[88:91], a[120:123], v[240:243]\n"
"v_mfma_f32_16x16x32_bf16 v[240:243], a[92:95], a[124:127], v[240:243]\n"
"v_mfma_f32_16x16x32_bf16 v[212:215], a[84:87], a[124:127], v[212:215]\n"
"ds_read_b128 a[124:127], v13 offset:6144\n"
"v_mfma_f32_16x16x32_bf16 v[212:215], a[80:83], a[120:123], v[212:215]\n"
"ds_read_b128 a[120:123], v12 offset:6144\n"
"v_mfma_f32_16x16x32_bf16 v[216:219], a[80:83], a[128:131], v[216:219]\n"
"v_mfma_f32_16x16x32_bf16 v[216:219], a[84:87], a[132:135], v[216:219]\n"
"global_load_dwordx4 a[72:75], v0, s[34:35] offset:-2048 sc1\n"
"v_mfma_f32_16x16x32_bf16 v[244:247], a[92:95], a[132:135], v[244:247]\n"
"s_add_u32 m0, s64, 24448\n"
"ds_read_b128 a[132:135], v13 offset:8192\n"
"v_mfma_f32_16x16x32_bf16 v[244:247], a[88:91], a[128:131], v[244:247]\n"
"ds_read_b128 a[128:131], v12 offset:8192\n"
"v_mfma_f32_16x16x32_bf16 v[248:251], a[88:91], a[136:139], v[248:251]\n"
"v_mfma_f32_16x16x32_bf16 v[248:251], a[92:95], a[140:143], v[248:251]\n"
"v_mfma_f32_16x16x32_bf16 v[220:223], a[84:87], a[140:143], v[220:223]\n"
"ds_read_b128 a[140:143], v13 offset:10240\n"
"v_mfma_f32_16x16x32_bf16 v[220:223], a[80:83], a[136:139], v[220:223]\n"
"ds_read_b128 a[136:139], v12 offset:10240\n"
"v_mfma_f32_16x16x32_bf16 v[224:227], a[80:83], a[144:147], v[224:227]\n"
"v_mfma_f32_16x16x32_bf16 v[224:227], a[84:87], a[148:151], v[224:227]\n"
"global_load_dwordx4 a[76:79], v0, s[34:35] offset:-1024 sc1\n"
"v_mfma_f32_16x16x32_bf16 v[252:255], a[92:95], a[148:151], v[252:255]\n"
"ds_read_b128 a[148:151], v13 offset:12288\n"
"v_mfma_f32_16x16x32_bf16 v[252:255], a[88:91], a[144:147], v[252:255]\n"
"ds_read_b128 a[144:147], v12 offset:12288\n"
"v_mfma_f32_16x16x32_bf16 a[28:31], a[88:91], a[152:155], a[28:31]\n"
"buffer_load_dwordx4 v6, s[40:43], s88 offen offset:128  lds\n"
"v_mfma_f32_16x16x32_bf16 a[28:31], a[92:95], a[156:159], a[28:31]\n"
"v_mfma_f32_16x16x32_bf16 a[24:27], a[84:87], a[156:159], a[24:27]\n"
"ds_read_b128 a[156:159], v13 offset:14336\n"
"v_mfma_f32_16x16x32_bf16 a[24:27], a[80:83], a[152:155], a[24:27]\n"
"s_waitcnt vmcnt(13) lgkmcnt(11)\n"
"ds_read_b128 a[152:155], v12 offset:14336\n"
"v_mfma_f32_16x16x32_bf16 v[32:35], a[32:35], a[96:99], v[32:35]\n"
"v_mfma_f32_16x16x32_bf16 v[32:35], a[36:39], a[100:103], v[32:35]\n"
"global_load_dwordx4 a[80:83], v0, s[34:35] offset:0 sc1\n"
"v_mfma_f32_16x16x32_bf16 v[60:63], a[44:47], a[100:103], v[60:63]\n"
"v_mfma_f32_16x16x32_bf16 v[60:63], a[40:43], a[96:99], v[60:63]\n"
"v_mfma_f32_16x16x32_bf16 v[64:67], a[40:43], a[104:107], v[64:67]\n"
"v_mfma_f32_16x16x32_bf16 v[64:67], a[44:47], a[108:111], v[64:67]\n"
"v_mfma_f32_16x16x32_bf16 v[36:39], a[36:39], a[108:111], v[36:39]\n"
"buffer_load_dwordx4 v6, s[40:43], s89 offen offset:1152  lds\n"
"v_mfma_f32_16x16x32_bf16 v[36:39], a[32:35], a[104:107], v[36:39]\n"
"s_waitcnt lgkmcnt(8)\n"
"v_mfma_f32_16x16x32_bf16 v[40:43], a[32:35], a[112:115], v[40:43]\n"
"v_mfma_f32_16x16x32_bf16 v[40:43], a[36:39], a[116:119], v[40:43]\n"
"global_load_dwordx4 a[84:87], v0, s[34:35] offset:1024 sc1\n"
"v_mfma_f32_16x16x32_bf16 v[68:71], a[44:47], a[116:119], v[68:71]\n"
"v_mfma_f32_16x16x32_bf16 v[68:71], a[40:43], a[112:115], v[68:71]\n"
"v_mfma_f32_16x16x32_bf16 v[72:75], a[40:43], a[120:123], v[72:75]\n"
"v_mfma_f32_16x16x32_bf16 v[72:75], a[44:47], a[124:127], v[72:75]\n"
"v_mfma_f32_16x16x32_bf16 v[44:47], a[36:39], a[124:127], v[44:47]\n"
"v_mfma_f32_16x16x32_bf16 v[44:47], a[32:35], a[120:123], v[44:47]\n"
"s_waitcnt lgkmcnt(4)\n"
"v_mfma_f32_16x16x32_bf16 v[48:51], a[32:35], a[128:131], v[48:51]\n"
"buffer_load_dwordx4 v6, s[40:43], s90 offen offset:2176  lds\n"
"v_mfma_f32_16x16x32_bf16 v[48:51], a[36:39], a[132:135], v[48:51]\n"
"global_load_dwordx4 a[88:91], v0, s[34:35] offset:2048 sc1\n"
"v_mfma_f32_16x16x32_bf16 v[76:79], a[44:47], a[132:135], v[76:79]\n"
"v_mfma_f32_16x16x32_bf16 v[76:79], a[40:43], a[128:131], v[76:79]\n"
"v_mfma_f32_16x16x32_bf16 v[80:83], a[40:43], a[136:139], v[80:83]\n"
"v_mfma_f32_16x16x32_bf16 v[80:83], a[44:47], a[140:143], v[80:83]\n"
"v_mfma_f32_16x16x32_bf16 v[52:55], a[36:39], a[140:143], v[52:55]\n"
"v_mfma_f32_16x16x32_bf16 v[52:55], a[32:35], a[136:139], v[52:55]\n"
"s_waitcnt lgkmcnt(0)\n"
"v_mfma_f32_16x16x32_bf16 v[56:59], a[32:35], a[144:147], v[56:59]\n"
"v_mfma_f32_16x16x32_bf16 v[56:59], a[36:39], a[148:151], v[56:59]\n"
"global_load_dwordx4 a[92:95], v0, s[34:35] offset:3072 sc1\n"
"s_mov_b32 s98, 0\n"
"v_mfma_f32_16x16x32_bf16 v[84:87], a[44:47], a[148:151], v[84:87]\n"
"s_load_dword s98, s[96:97], 8448 glc\n"
"v_mfma_f32_16x16x32_bf16 v[84:87], a[40:43], a[144:147], v[84:87]\n"
"buffer_load_dwordx4 v6, s[40:43], s91 offen offset:3200  lds\n"
"v_mfma_f32_16x16x32_bf16 a[4:7], a[40:43], a[152:155], a[4:7]\n"
"s_add_u32 s34, s34, 8192\n"
"v_mfma_f32_16x16x32_bf16 a[4:7], a[44:47], a[156:159], a[4:7]\n"
"v_mfma_f32_16x16x32_bf16 a[0:3], a[36:39], a[156:159], a[0:3]\n"
"v_mfma_f32_16x16x32_bf16 a[0:3], a[32:35], a[152:155], a[0:3]\n"
"s_waitcnt vmcnt(14)\n"
"v_mfma_f32_16x16x32_bf16 v[88:91], a[48:51], a[96:99], v[88:91]\n"
"v_mfma_f32_16x16x32_bf16 v[88:91], a[52:55], a[100:103], v[88:91]\n"
"global_load_dwordx4 a[32:35], v0, s[34:35] offset:-4096 sc1\n"
"v_mfma_f32_16x16x32_bf16 v[116:119], a[60:63], a[100:103], v[116:119]\n"
"v_mfma_f32_16x16x32_bf16 v[116:119], a[56:59], a[96:99], v[116:119]\n"
"v_mfma_f32_16x16x32_bf16 v[120:123], a[56:59], a[104:107], v[120:123]\n"
"v_mfma_f32_16x16x32_bf16 v[120:123], a[60:63], a[108:111], v[120:123]\n"
"s_add_u32 m0, s64, 28544\n"
"v_mfma_f32_16x16x32_bf16 v[92:95], a[52:55], a[108:111], v[92:95]\n"
"v_mfma_f32_16x16x32_bf16 v[92:95], a[48:51], a[104:107], v[92:95]\n"
"s_sub_u32 s69, s69, s98\n"
"v_mfma_f32_16x16x32_bf16 v[96:99], a[48:51], a[112:115], v[96:99]\n"
"s_sub_u32 s69, s69, s98\n"
"v_mfma_f32_16x16x32_bf16 v[96:99], a[52:55], a[116:119], v[96:99]\n"
"global_load_dwordx4 a[36:39], v0, s[34:35] offset:-3072 sc1\n"
"s_sub_u32 s69, s69, s98\n"
"v_mfma_f32_16x16x32_bf16 v[124:127], a[60:63], a[116:119], v[124:127]\n"
"s_sub_u32 s69, s69, s98\n"
"v_mfma_f32_16x16x32_bf16 v[124:127], a[56:59], a[112:115], v[124:127]\n"
"s_sub_u32 s69, s69, s98\n"
"v_mfma_f32_16x16x32_bf16 v[128:131], a[56:59], a[120:123], v[128:131]\n"
"s_sub_u32 s69, s69, s98\n"
"v_mfma_f32_16x16x32_bf16 v[128:131], a[60:63], a[124:127], v[128:131]\n"
"s_sub_u32 s69, s69, s98\n"
"v_mfma_f32_16x16x32_bf16 v[100:103], a[52:55], a[124:127], v[100:103]\n"
"s_sub_u32 s69, s69, s98\n"
"v_mfma_f32_16x16x32_bf16 v[100:103], a[48:51], a[120:123], v[100:103]\n"
"buffer_load_dwordx4 v6, s[40:43], s92 offen offset:128  lds\n"
"s_sub_u32 s69, s69, s98\n"
"v_mfma_f32_16x16x32_bf16 v[104:107], a[48:51], a[128:131], v[104:107]\n"
"s_sub_u32 s69, s69, s98\n"
"v_mfma_f32_16x16x32_bf16 v[104:107], a[52:55], a[132:135], v[104:107]\n"
"global_load_dwordx4 a[40:43], v0, s[34:35] offset:-2048 sc1\n"
"s_sub_u32 s69, s69, s98\n"
"v_mfma_f32_16x16x32_bf16 v[132:135], a[60:63], a[132:135], v[132:135]\n"
"s_sub_u32 s69, s69, s98\n"
"v_mfma_f32_16x16x32_bf16 v[132:135], a[56:59], a[128:131], v[132:135]\n"
"s_sub_u32 s69, s69, s98\n"
"v_mfma_f32_16x16x32_bf16 v[136:139], a[56:59], a[136:139], v[136:139]\n"
"s_sub_u32 s69, s69, s98\n"
"v_mfma_f32_16x16x32_bf16 v[136:139], a[60:63], a[140:143], v[136:139]\n"
"s_sub_u32 s69, s69, s98\n"
"v_mfma_f32_16x16x32_bf16 v[108:111], a[52:55], a[140:143], v[108:111]\n"
"s_sub_u32 s69, s69, s98\n"
"v_mfma_f32_16x16x32_bf16 v[108:111], a[48:51], a[136:139], v[108:111]\n"
"s_sub_u32 s69, s69, s98\n"
"v_mfma_f32_16x16x32_bf16 v[112:115], a[48:51], a[144:147], v[112:115]\n"
"s_sub_u32 s69, s69, s98\n"
"v_mfma_f32_16x16x32_bf16 v[112:115], a[52:55], a[148:151], v[112:115]\n"
"global_load_dwordx4 a[44:47], v0, s[34:35] offset:-1024 sc1\n"
"s_sub_u32 s69, s69, s98\n"
"v_mfma_f32_16x16x32_bf16 v[140:143], a[60:63], a[148:151], v[140:143]\n"
"buffer_load_dwordx4 v6, s[40:43], s93 offen offset:1152  lds\n"
"s_sub_u32 s69, s69, s98\n"
"s_waitcnt lgkmcnt(0)\n"
"v_mfma_f32_16x16x32_bf16 v[140:143], a[56:59], a[144:147], v[140:143]\n"
"s_mov_b32 s98, 0\n"
"v_mfma_f32_16x16x32_bf16 a[12:15], a[56:59], a[152:155], a[12:15]\n"
"s_load_dword s98, s[96:97], 384 glc\n"
"v_mfma_f32_16x16x32_bf16 a[12:15], a[60:63], a[156:159], a[12:15]\n"
"v_mfma_f32_16x16x32_bf16 a[8:11], a[52:55], a[156:159], a[8:11]\n"
"v_mfma_f32_16x16x32_bf16 a[8:11], a[48:51], a[152:155], a[8:11]\n"
"s_waitcnt vmcnt(14)\n"
"v_mfma_f32_16x16x32_bf16 v[144:147], a[64:67], a[96:99], v[144:147]\n"
"v_mfma_f32_16x16x32_bf16 v[144:147], a[68:71], a[100:103], v[144:147]\n"
"global_load_dwordx4 a[48:51], v0, s[34:35] offset:0 sc1\n"
"v_mfma_f32_16x16x32_bf16 v[172:175], a[76:79], a[100:103], v[172:175]\n"
"v_mfma_f32_16x16x32_bf16 v[172:175], a[72:75], a[96:99], v[172:175]\n"
"v_mfma_f32_16x16x32_bf16 v[176:179], a[72:75], a[104:107], v[176:179]\n"
"buffer_load_dwordx4 v6, s[40:43], s94 offen offset:2176  lds\n"
"v_mfma_f32_16x16x32_bf16 v[176:179], a[76:79], a[108:111], v[176:179]\n"
"v_mfma_f32_16x16x32_bf16 v[148:151], a[68:71], a[108:111], v[148:151]\n"
"v_mfma_f32_16x16x32_bf16 v[148:151], a[64:67], a[104:107], v[148:151]\n"
"v_mfma_f32_16x16x32_bf16 v[152:155], a[64:67], a[112:115], v[152:155]\n"
"v_mfma_f32_16x16x32_bf16 v[152:155], a[68:71], a[116:119], v[152:155]\n"
"global_load_dwordx4 a[52:55], v0, s[34:35] offset:1024 sc1\n"
"s_add_u32 s69, s69, s98\n"
"v_mfma_f32_16x16x32_bf16 v[180:183], a[76:79], a[116:119], v[180:183]\n"
"s_add_u32 s69, s69, s98\n"
"v_mfma_f32_16x16x32_bf16 v[180:183], a[72:75], a[112:115], v[180:183]\n"
"s_add_u32 s69, s69, s98\n"
"v_mfma_f32_16x16x32_bf16 v[184:187], a[72:75], a[120:123], v[184:187]\n"
"s_add_u32 s69, s69, s98\n"
"v_mfma_f32_16x16x32_bf16 v[184:187], a[76:79], a[124:127], v[184:187]\n"
"s_add_u32 s69, s69, s98\n"
"v_mfma_f32_16x16x32_bf16 v[156:159], a[68:71], a[124:127], v[156:159]\n"
"buffer_load_dwordx4 v6, s[40:43], s95 offen offset:3200  lds\n"
"s_add_u32 s69, s69, s98\n"
"v_mfma_f32_16x16x32_bf16 v[156:159], a[64:67], a[120:123], v[156:159]\n"
"s_add_u32 s69, s69, s98\n"
"v_mfma_f32_16x16x32_bf16 v[160:163], a[64:67], a[128:131], v[160:163]\n"
"s_add_u32 s69, s69, s98\n"
"v_mfma_f32_16x16x32_bf16 v[160:163], a[68:71], a[132:135], v[160:163]\n"
"global_load_dwordx4 a[56:59], v0, s[34:35] offset:2048 sc1\n"
"s_add_u32 s69, s69, s98\n"
"v_mfma_f32_16x16x32_bf16 v[188:191], a[76:79], a[132:135], v[188:191]\n"
"s_add_u32 s69, s69, s98\n"
"v_mfma_f32_16x16x32_bf16 v[188:191], a[72:75], a[128:131], v[188:191]\n"
"s_add_u32 s69, s69, s98\n"
"v_mfma_f32_16x16x32_bf16 v[192:195], a[72:75], a[136:139], v[192:195]\n"
"s_add_u32 s69, s69, s98\n"
"v_sub_u32 v13, v4, v2\n"
"v_mfma_f32_16x16x32_bf16 v[192:195], a[76:79], a[140:143], v[192:195]\n"
"s_add_u32 s69, s69, s98\n"
"v_mfma_f32_16x16x32_bf16 v[164:167], a[68:71], a[140:143], v[164:167]\n"
"s_add_u32 s69, s69, s98\n"
"v_add_u32 v12, v13, v7\n"
"v_mfma_f32_16x16x32_bf16 v[164:167], a[64:67], a[136:139], v[164:167]\n"
"s_add_u32 s69, s69, s98\n"
"v_mfma_f32_16x16x32_bf16 v[168:171], a[64:67], a[144:147], v[168:171]\n"
"s_add_u32 s69, s69, s98\n"
"v_add_u32 v13, v13, v8\n"
"v_mfma_f32_16x16x32_bf16 v[168:171], a[68:71], a[148:151], v[168:171]\n"
"global_load_dwordx4 a[60:63], v0, s[34:35] offset:3072 sc1\n"
"s_add_u32 s69, s69, s98\n"
"v_mfma_f32_16x16x32_bf16 v[196:199], a[76:79], a[148:151], v[196:199]\n"
"s_add_u32 s69, s69, s98\n"
"v_mfma_f32_16x16x32_bf16 v[196:199], a[72:75], a[144:147], v[196:199]\n"
"s_add_u32 s69, s69, s98\n"
"v_mfma_f32_16x16x32_bf16 a[20:23], a[72:75], a[152:155], a[20:23]\n"
"s_add_u32 s34, s34, 8192\n"
"s_add_u32 s69, s69, s98\n"
"s_waitcnt lgkmcnt(0)\n"
"v_mfma_f32_16x16x32_bf16 a[20:23], a[76:79], a[156:159], a[20:23]\n"
"v_mfma_f32_16x16x32_bf16 a[16:19], a[68:71], a[156:159], a[16:19]\n"
"buffer_store_dwordx4 a[192:195], v0, s[36:39], s71 offen offset:0 sc1\n"
"v_mfma_f32_16x16x32_bf16 a[16:19], a[64:67], a[152:155], a[16:19]\n"
"s_waitcnt vmcnt(14)\n"
"v_mfma_f32_16x16x32_bf16 v[200:203], a[80:83], a[96:99], v[200:203]\n"
"v_mfma_f32_16x16x32_bf16 v[200:203], a[84:87], a[100:103], v[200:203]\n"
"global_load_dwordx4 a[64:67], v0, s[34:35] offset:-4096 sc1\n"
"v_mfma_f32_16x16x32_bf16 v[228:231], a[92:95], a[100:103], v[228:231]\n"
"ds_read_b128 a[100:103], v13 offset:0\n"
"v_mfma_f32_16x16x32_bf16 v[228:231], a[88:91], a[96:99], v[228:231]\n"
"ds_read_b128 a[96:99], v12 offset:0\n"
"v_mfma_f32_16x16x32_bf16 v[232:235], a[88:91], a[104:107], v[232:235]\n"
"v_mfma_f32_16x16x32_bf16 v[232:235], a[92:95], a[108:111], v[232:235]\n"
"v_mfma_f32_16x16x32_bf16 v[204:207], a[84:87], a[108:111], v[204:207]\n"
"ds_read_b128 a[108:111], v13 offset:2048\n"
"v_mfma_f32_16x16x32_bf16 v[204:207], a[80:83], a[104:107], v[204:207]\n"
"ds_read_b128 a[104:107], v12 offset:2048\n"
"v_mfma_f32_16x16x32_bf16 v[208:211], a[80:83], a[112:115], v[208:211]\n"
"v_mfma_f32_16x16x32_bf16 v[208:211], a[84:87], a[116:119], v[208:211]\n"
"global_load_dwordx4 a[68:71], v0, s[34:35] offset:-3072 sc1\n"
"v_mfma_f32_16x16x32_bf16 v[236:239], a[92:95], a[116:119], v[236:239]\n"
"ds_read_b128 a[116:119], v13 offset:4096\n"
"v_mfma_f32_16x16x32_bf16 v[236:239], a[88:91], a[112:115], v[236:239]\n"
"ds_read_b128 a[112:115], v12 offset:4096\n"
"v_mfma_f32_16x16x32_bf16 v[240:243], a[88:91], a[120:123], v[240:243]\n"
"v_mfma_f32_16x16x32_bf16 v[240:243], a[92:95], a[124:127], v[240:243]\n"
"v_mfma_f32_16x16x32_bf16 v[212:215], a[84:87], a[124:127], v[212:215]\n"
"ds_read_b128 a[124:127], v13 offset:6144\n"
"v_mfma_f32_16x16x32_bf16 v[212:215], a[80:83], a[120:123], v[212:215]\n"
"ds_read_b128 a[120:123], v12 offset:6144\n"
"v_mfma_f32_16x16x32_bf16 v[216:219], a[80:83], a[128:131], v[216:219]\n"
"v_mfma_f32_16x16x32_bf16 v[216:219], a[84:87], a[132:135], v[216:219]\n"
"global_load_dwordx4 a[72:75], v0, s[34:35] offset:-2048 sc1\n"
"v_mfma_f32_16x16x32_bf16 v[244:247], a[92:95], a[132:135], v[244:247]\n"
"buffer_store_dwordx4 a[196:199], v0, s[36:39], s71 offen offset:1024 sc1\n"
"ds_read_b128 a[132:135], v13 offset:8192\n"
"v_mfma_f32_16x16x32_bf16 v[244:247], a[88:91], a[128:131], v[244:247]\n"
"ds_read_b128 a[128:131], v12 offset:8192\n"
"v_mfma_f32_16x16x32_bf16 v[248:251], a[88:91], a[136:139], v[248:251]\n"
"v_mfma_f32_16x16x32_bf16 v[248:251], a[92:95], a[140:143], v[248:251]\n"
"v_mfma_f32_16x16x32_bf16 v[220:223], a[84:87], a[140:143], v[220:223]\n"
"ds_read_b128 a[140:143], v13 offset:10240\n"
"v_mfma_f32_16x16x32_bf16 v[220:223], a[80:83], a[136:139], v[220:223]\n"
"ds_read_b128 a[136:139], v12 offset:10240\n"
"v_mfma_f32_16x16x32_bf16 v[224:227], a[80:83], a[144:147], v[224:227]\n"
"v_mfma_f32_16x16x32_bf16 v[224:227], a[84:87], a[148:151], v[224:227]\n"
"global_load_dwordx4 a[76:79], v0, s[34:35] offset:-1024 sc1\n"
"v_mfma_f32_16x16x32_bf16 v[252:255], a[92:95], a[148:151], v[252:255]\n"
"ds_read_b128 a[148:151], v13 offset:12288\n"
"v_mfma_f32_16x16x32_bf16 v[252:255], a[88:91], a[144:147], v[252:255]\n"
"ds_read_b128 a[144:147], v12 offset:12288\n"
"v_mfma_f32_16x16x32_bf16 a[28:31], a[88:91], a[152:155], a[28:31]\n"
"v_mfma_f32_16x16x32_bf16 a[28:31], a[92:95], a[156:159], a[28:31]\n"
"v_mfma_f32_16x16x32_bf16 a[24:27], a[84:87], a[156:159], a[24:27]\n"
"ds_read_b128 a[156:159], v13 offset:14336\n"
"v_mfma_f32_16x16x32_bf16 a[24:27], a[80:83], a[152:155], a[24:27]\n"
"s_waitcnt vmcnt(13) lgkmcnt(11)\n"
"ds_read_b128 a[152:155], v12 offset:14336\n"
"v_mfma_f32_16x16x32_bf16 v[32:35], a[32:35], a[96:99], v[32:35]\n"
"v_mfma_f32_16x16x32_bf16 v[32:35], a[36:39], a[100:103], v[32:35]\n"
"global_load_dwordx4 a[80:83], v0, s[34:35] offset:0 sc1\n"
"v_mfma_f32_16x16x32_bf16 v[60:63], a[44:47], a[100:103], v[60:63]\n"
"v_mfma_f32_16x16x32_bf16 v[60:63], a[40:43], a[96:99], v[60:63]\n"
"v_mfma_f32_16x16x32_bf16 v[64:67], a[40:43], a[104:107], v[64:67]\n"
"v_mfma_f32_16x16x32_bf16 v[64:67], a[44:47], a[108:111], v[64:67]\n"
"v_mfma_f32_16x16x32_bf16 v[36:39], a[36:39], a[108:111], v[36:39]\n"
"buffer_store_dwordx4 a[200:203], v0, s[36:39], s71 offen offset:2048 sc1\n"
"v_mfma_f32_16x16x32_bf16 v[36:39], a[32:35], a[104:107], v[36:39]\n"
"s_waitcnt lgkmcnt(8)\n"
"v_mfma_f32_16x16x32_bf16 v[40:43], a[32:35], a[112:115], v[40:43]\n"
"v_mfma_f32_16x16x32_bf16 v[40:43], a[36:39], a[116:119], v[40:43]\n"
"global_load_dwordx4 a[84:87], v0, s[34:35] offset:1024 sc1\n"
"v_mfma_f32_16x16x32_bf16 v[68:71], a[44:47], a[116:119], v[68:71]\n"
"v_mfma_f32_16x16x32_bf16 v[68:71], a[40:43], a[112:115], v[68:71]\n"
"v_mfma_f32_16x16x32_bf16 v[72:75], a[40:43], a[120:123], v[72:75]\n"
"v_mfma_f32_16x16x32_bf16 v[72:75], a[44:47], a[124:127], v[72:75]\n"
"v_mfma_f32_16x16x32_bf16 v[44:47], a[36:39], a[124:127], v[44:47]\n"
"v_mfma_f32_16x16x32_bf16 v[44:47], a[32:35], a[120:123], v[44:47]\n"
"s_waitcnt lgkmcnt(4)\n"
"v_mfma_f32_16x16x32_bf16 v[48:51], a[32:35], a[128:131], v[48:51]\n"
"v_mfma_f32_16x16x32_bf16 v[48:51], a[36:39], a[132:135], v[48:51]\n"
"global_load_dwordx4 a[88:91], v0, s[34:35] offset:2048 sc1\n"
"v_mfma_f32_16x16x32_bf16 v[76:79], a[44:47], a[132:135], v[76:79]\n"
"v_mfma_f32_16x16x32_bf16 v[76:79], a[40:43], a[128:131], v[76:79]\n"
"v_mfma_f32_16x16x32_bf16 v[80:83], a[40:43], a[136:139], v[80:83]\n"
"v_mfma_f32_16x16x32_bf16 v[80:83], a[44:47], a[140:143], v[80:83]\n"
"v_mfma_f32_16x16x32_bf16 v[52:55], a[36:39], a[140:143], v[52:55]\n"
"v_mfma_f32_16x16x32_bf16 v[52:55], a[32:35], a[136:139], v[52:55]\n"
"s_waitcnt lgkmcnt(0)\n"
"v_mfma_f32_16x16x32_bf16 v[56:59], a[32:35], a[144:147], v[56:59]\n"
"v_mfma_f32_16x16x32_bf16 v[56:59], a[36:39], a[148:151], v[56:59]\n"
"global_load_dwordx4 a[92:95], v0, s[34:35] offset:3072 sc1\n"
"v_mfma_f32_16x16x32_bf16 v[84:87], a[44:47], a[148:151], v[84:87]\n"
"buffer_store_dwordx4 a[204:207], v0, s[36:39], s71 offen offset:3072 sc1\n"
"v_mfma_f32_16x16x32_bf16 v[84:87], a[40:43], a[144:147], v[84:87]\n"
"v_mfma_f32_16x16x32_bf16 a[4:7], a[40:43], a[152:155], a[4:7]\n"
"s_add_u32 s34, s34, 8192\n"
"v_mfma_f32_16x16x32_bf16 a[4:7], a[44:47], a[156:159], a[4:7]\n"
"v_mfma_f32_16x16x32_bf16 a[0:3], a[36:39], a[156:159], a[0:3]\n"
"v_mfma_f32_16x16x32_bf16 a[0:3], a[32:35], a[152:155], a[0:3]\n"
"s_waitcnt vmcnt(12)\n"
"v_mfma_f32_16x16x32_bf16 v[88:91], a[48:51], a[96:99], v[88:91]\n"
"v_mfma_f32_16x16x32_bf16 v[88:91], a[52:55], a[100:103], v[88:91]\n"
"global_load_dwordx4 a[32:35], v0, s[34:35] offset:-4096 sc1\n"
"v_mfma_f32_16x16x32_bf16 v[116:119], a[60:63], a[100:103], v[116:119]\n"
"v_mfma_f32_16x16x32_bf16 v[116:119], a[56:59], a[96:99], v[116:119]\n"
"v_mfma_f32_16x16x32_bf16 v[120:123], a[56:59], a[104:107], v[120:123]\n"
"v_mfma_f32_16x16x32_bf16 v[120:123], a[60:63], a[108:111], v[120:123]\n"
"v_mfma_f32_16x16x32_bf16 v[92:95], a[52:55], a[108:111], v[92:95]\n"
"v_mfma_f32_16x16x32_bf16 v[92:95], a[48:51], a[104:107], v[92:95]\n"
"v_mfma_f32_16x16x32_bf16 v[96:99], a[48:51], a[112:115], v[96:99]\n"
"v_mfma_f32_16x16x32_bf16 v[96:99], a[52:55], a[116:119], v[96:99]\n"
"global_load_dwordx4 a[36:39], v0, s[34:35] offset:-3072 sc1\n"
"v_mfma_f32_16x16x32_bf16 v[124:127], a[60:63], a[116:119], v[124:127]\n"
"v_mfma_f32_16x16x32_bf16 v[124:127], a[56:59], a[112:115], v[124:127]\n"
"v_mfma_f32_16x16x32_bf16 v[128:131], a[56:59], a[120:123], v[128:131]\n"
"v_mfma_f32_16x16x32_bf16 v[128:131], a[60:63], a[124:127], v[128:131]\n"
"v_mfma_f32_16x16x32_bf16 v[100:103], a[52:55], a[124:127], v[100:103]\n"
"buffer_store_dwordx4 a[208:211], v1, s[36:39], s71 offen offset:0 sc1\n"
"v_mfma_f32_16x16x32_bf16 v[100:103], a[48:51], a[120:123], v[100:103]\n"
"v_mfma_f32_16x16x32_bf16 v[104:107], a[48:51], a[128:131], v[104:107]\n"
"v_mfma_f32_16x16x32_bf16 v[104:107], a[52:55], a[132:135], v[104:107]\n"
"global_load_dwordx4 a[40:43], v0, s[34:35] offset:-2048 sc1\n"
"v_mfma_f32_16x16x32_bf16 v[132:135], a[60:63], a[132:135], v[132:135]\n"
"v_mfma_f32_16x16x32_bf16 v[132:135], a[56:59], a[128:131], v[132:135]\n"
"v_mfma_f32_16x16x32_bf16 v[136:139], a[56:59], a[136:139], v[136:139]\n"
"v_mfma_f32_16x16x32_bf16 v[136:139], a[60:63], a[140:143], v[136:139]\n"
"v_mfma_f32_16x16x32_bf16 v[108:111], a[52:55], a[140:143], v[108:111]\n"
"v_mfma_f32_16x16x32_bf16 v[108:111], a[48:51], a[136:139], v[108:111]\n"
"v_mfma_f32_16x16x32_bf16 v[112:115], a[48:51], a[144:147], v[112:115]\n"
"v_mfma_f32_16x16x32_bf16 v[112:115], a[52:55], a[148:151], v[112:115]\n"
"global_load_dwordx4 a[44:47], v0, s[34:35] offset:-1024 sc1\n"
"v_mfma_f32_16x16x32_bf16 v[140:143], a[60:63], a[148:151], v[140:143]\n"
"v_mfma_f32_16x16x32_bf16 v[140:143], a[56:59], a[144:147], v[140:143]\n"
"v_mfma_f32_16x16x32_bf16 a[12:15], a[56:59], a[152:155], a[12:15]\n"
"v_mfma_f32_16x16x32_bf16 a[12:15], a[60:63], a[156:159], a[12:15]\n"
"v_lshrrev_b32 v16, 4, v0\n"
"v_mfma_f32_16x16x32_bf16 a[8:11], a[52:55], a[156:159], a[8:11]\n"
"v_mfma_f32_16x16x32_bf16 a[8:11], a[48:51], a[152:155], a[8:11]\n"
"s_waitcnt vmcnt(11)\n"
"v_and_b32 v16, 0x30, v16\n"
"v_mfma_f32_16x16x32_bf16 v[144:147], a[64:67], a[96:99], v[144:147]\n"
"v_mfma_f32_16x16x32_bf16 v[144:147], a[68:71], a[100:103], v[144:147]\n"
"global_load_dwordx4 a[48:51], v0, s[34:35] offset:0 sc1\n"
"v_mfma_f32_16x16x32_bf16 v[172:175], a[76:79], a[100:103], v[172:175]\n"
"buffer_store_dwordx4 a[212:215], v1, s[36:39], s71 offen offset:1024 sc1\n"
"buffer_load_dwordx2 v[18:19], v16, s[44:47], s61 offen offset:128\n"
"v_mfma_f32_16x16x32_bf16 v[172:175], a[72:75], a[96:99], v[172:175]\n"
"v_mfma_f32_16x16x32_bf16 v[176:179], a[72:75], a[104:107], v[176:179]\n"
"v_mfma_f32_16x16x32_bf16 v[176:179], a[76:79], a[108:111], v[176:179]\n"
"v_lshrrev_b32 v20, 4, v0\n"
"v_mfma_f32_16x16x32_bf16 v[148:151], a[68:71], a[108:111], v[148:151]\n"
"v_mfma_f32_16x16x32_bf16 v[148:151], a[64:67], a[104:107], v[148:151]\n"
"v_and_b32 v20, 0x30, v20\n"
"v_mfma_f32_16x16x32_bf16 v[152:155], a[64:67], a[112:115], v[152:155]\n"
"v_mfma_f32_16x16x32_bf16 v[152:155], a[68:71], a[116:119], v[152:155]\n"
"global_load_dwordx4 a[52:55], v0, s[34:35] offset:1024 sc1\n"
"v_mfma_f32_16x16x32_bf16 v[180:183], a[76:79], a[116:119], v[180:183]\n"
"buffer_load_dwordx2 v[22:23], v20, s[44:47], s61 offen offset:136\n"
"v_mfma_f32_16x16x32_bf16 v[180:183], a[72:75], a[112:115], v[180:183]\n"
"v_mfma_f32_16x16x32_bf16 v[184:187], a[72:75], a[120:123], v[184:187]\n"
"v_lshrrev_b32 v24, 4, v0\n"
"v_mfma_f32_16x16x32_bf16 v[184:187], a[76:79], a[124:127], v[184:187]\n"
"v_mfma_f32_16x16x32_bf16 v[156:159], a[68:71], a[124:127], v[156:159]\n"
"v_mfma_f32_16x16x32_bf16 v[156:159], a[64:67], a[120:123], v[156:159]\n"
"v_and_b32 v24, 0x30, v24\n"
"v_mfma_f32_16x16x32_bf16 v[160:163], a[64:67], a[128:131], v[160:163]\n"
"v_mfma_f32_16x16x32_bf16 v[160:163], a[68:71], a[132:135], v[160:163]\n"
"global_load_dwordx4 a[56:59], v0, s[34:35] offset:2048 sc1\n"
"v_mfma_f32_16x16x32_bf16 v[188:191], a[76:79], a[132:135], v[188:191]\n"
"buffer_load_dwordx2 v[26:27], v24, s[44:47], s61 offen offset:192\n"
"v_mfma_f32_16x16x32_bf16 v[188:191], a[72:75], a[128:131], v[188:191]\n"
"v_lshrrev_b32 v28, 4, v0\n"
"v_mfma_f32_16x16x32_bf16 v[192:195], a[72:75], a[136:139], v[192:195]\n"
"v_sub_u32 v13, v5, v2\n"
"v_mfma_f32_16x16x32_bf16 v[192:195], a[76:79], a[140:143], v[192:195]\n"
"v_and_b32 v28, 0x30, v28\n"
"v_mfma_f32_16x16x32_bf16 v[164:167], a[68:71], a[140:143], v[164:167]\n"
"buffer_store_dwordx4 a[216:219], v1, s[36:39], s71 offen offset:2048 sc1\n"
"buffer_load_dwordx2 v[30:31], v28, s[44:47], s61 offen offset:200\n"
"v_add_u32 v12, v13, v7\n"
"v_mfma_f32_16x16x32_bf16 v[164:167], a[64:67], a[136:139], v[164:167]\n"
"v_mfma_f32_16x16x32_bf16 v[168:171], a[64:67], a[144:147], v[168:171]\n"
"v_add_u32 v13, v13, v8\n"
"v_mfma_f32_16x16x32_bf16 v[168:171], a[68:71], a[148:151], v[168:171]\n"
"global_load_dwordx4 a[60:63], v0, s[34:35] offset:3072 sc1\n"
"v_mfma_f32_16x16x32_bf16 v[196:199], a[76:79], a[148:151], v[196:199]\n"
"v_mfma_f32_16x16x32_bf16 v[196:199], a[72:75], a[144:147], v[196:199]\n"
"v_mfma_f32_16x16x32_bf16 a[20:23], a[72:75], a[152:155], a[20:23]\n"
"s_add_u32 s34, s34, 8192\n"
"v_mfma_f32_16x16x32_bf16 a[20:23], a[76:79], a[156:159], a[20:23]\n"
"v_mfma_f32_16x16x32_bf16 a[16:19], a[68:71], a[156:159], a[16:19]\n"
"v_mfma_f32_16x16x32_bf16 a[16:19], a[64:67], a[152:155], a[16:19]\n"
"s_waitcnt vmcnt(16)\n"
"v_mfma_f32_16x16x32_bf16 v[200:203], a[80:83], a[96:99], v[200:203]\n"
"v_mfma_f32_16x16x32_bf16 v[200:203], a[84:87], a[100:103], v[200:203]\n"
"global_load_dwordx4 a[64:67], v0, s[34:35] offset:-4096 sc1\n"
"v_mfma_f32_16x16x32_bf16 v[228:231], a[92:95], a[100:103], v[228:231]\n"
"ds_read_b128 a[100:103], v13 offset:0\n"
"v_mfma_f32_16x16x32_bf16 v[228:231], a[88:91], a[96:99], v[228:231]\n"
"ds_read_b128 a[96:99], v12 offset:0\n"
"v_mfma_f32_16x16x32_bf16 v[232:235], a[88:91], a[104:107], v[232:235]\n"
"s_cmp_le_i32 s69, -4\n"
"s_cselect_b32 s66, 2, 1\n"
"v_mfma_f32_16x16x32_bf16 v[232:235], a[92:95], a[108:111], v[232:235]\n"
"v_mfma_f32_16x16x32_bf16 v[204:207], a[84:87], a[108:111], v[204:207]\n"
"ds_read_b128 a[108:111], v13 offset:2048\n"
"v_mfma_f32_16x16x32_bf16 v[204:207], a[80:83], a[104:107], v[204:207]\n"
"ds_read_b128 a[104:107], v12 offset:2048\n"
"v_mfma_f32_16x16x32_bf16 v[208:211], a[80:83], a[112:115], v[208:211]\n"
"s_cmp_ge_i32 s69, 4\n"
"s_cselect_b32 s66, 3, s66\n"
"v_mfma_f32_16x16x32_bf16 v[208:211], a[84:87], a[116:119], v[208:211]\n"
"global_load_dwordx4 a[68:71], v0, s[34:35] offset:-3072 sc1\n"
"v_mfma_f32_16x16x32_bf16 v[236:239], a[92:95], a[116:119], v[236:239]\n"
"buffer_store_dwordx4 a[220:223], v1, s[36:39], s71 offen offset:3072 sc1\n"
"ds_read_b128 a[116:119], v13 offset:4096\n"
"v_mfma_f32_16x16x32_bf16 v[236:239], a[88:91], a[112:115], v[236:239]\n"
"ds_read_b128 a[112:115], v12 offset:4096\n"
"v_mfma_f32_16x16x32_bf16 v[240:243], a[88:91], a[120:123], v[240:243]\n"
"s_lshl_b32 s73, s66, 13\n"
"s_xor_b32 s40, s79, s66\n"
"v_mfma_f32_16x16x32_bf16 v[240:243], a[92:95], a[124:127], v[240:243]\n"
"v_mfma_f32_16x16x32_bf16 v[212:215], a[84:87], a[124:127], v[212:215]\n"
"ds_read_b128 a[124:127], v13 offset:6144\n"
"v_mfma_f32_16x16x32_bf16 v[212:215], a[80:83], a[120:123], v[212:215]\n"
"ds_read_b128 a[120:123], v12 offset:6144\n"
"v_mfma_f32_16x16x32_bf16 v[216:219], a[80:83], a[128:131], v[216:219]\n"
"s_cmp_eq_u32 s66, 3\n"
"s_cselect_b32 s72, 8192, 0\n"
"v_mfma_f32_16x16x32_bf16 v[216:219], a[84:87], a[132:135], v[216:219]\n"
"global_load_dwordx4 a[72:75], v0, s[34:35] offset:-2048 sc1\n"
"v_mfma_f32_16x16x32_bf16 v[244:247], a[92:95], a[132:135], v[244:247]\n"
"ds_read_b128 a[132:135], v13 offset:8192\n"
"v_mfma_f32_16x16x32_bf16 v[244:247], a[88:91], a[128:131], v[244:247]\n"
"ds_read_b128 a[128:131], v12 offset:8192\n"
"v_mfma_f32_16x16x32_bf16 v[248:251], a[88:91], a[136:139], v[248:251]\n"
"v_mfma_f32_16x16x32_bf16 v[248:251], a[92:95], a[140:143], v[248:251]\n"
"v_mfma_f32_16x16x32_bf16 v[220:223], a[84:87], a[140:143], v[220:223]\n"
"ds_read_b128 a[140:143], v13 offset:10240\n"
"v_mfma_f32_16x16x32_bf16 v[220:223], a[80:83], a[136:139], v[220:223]\n"
"ds_read_b128 a[136:139], v12 offset:10240\n"
"v_mfma_f32_16x16x32_bf16 v[224:227], a[80:83], a[144:147], v[224:227]\n"
"v_mfma_f32_16x16x32_bf16 v[224:227], a[84:87], a[148:151], v[224:227]\n"
"global_load_dwordx4 a[76:79], v0, s[34:35] offset:-1024 sc1\n"
"v_mfma_f32_16x16x32_bf16 v[252:255], a[92:95], a[148:151], v[252:255]\n"
"ds_read_b128 a[148:151], v13 offset:12288\n"
"v_mfma_f32_16x16x32_bf16 v[252:255], a[88:91], a[144:147], v[252:255]\n"
"ds_read_b128 a[144:147], v12 offset:12288\n"
"v_mfma_f32_16x16x32_bf16 a[28:31], a[88:91], a[152:155], a[28:31]\n"
"v_mfma_f32_16x16x32_bf16 a[28:31], a[92:95], a[156:159], a[28:31]\n"
"v_mfma_f32_16x16x32_bf16 a[24:27], a[84:87], a[156:159], a[24:27]\n"
"ds_read_b128 a[156:159], v13 offset:14336\n"
"v_mfma_f32_16x16x32_bf16 a[24:27], a[80:83], a[152:155], a[24:27]\n"
"s_waitcnt vmcnt(15) lgkmcnt(11)\n"
"ds_read_b128 a[152:155], v12 offset:14336\n"
"v_mfma_f32_16x16x32_bf16 v[32:35], a[32:35], a[96:99], v[32:35]\n"
"v_mfma_f32_16x16x32_bf16 v[32:35], a[36:39], a[100:103], v[32:35]\n"
"global_load_dwordx4 a[80:83], v0, s[34:35] offset:0 sc1\n"
"v_mfma_f32_16x16x32_bf16 v[60:63], a[44:47], a[100:103], v[60:63]\n"
"v_mfma_f32_16x16x32_bf16 v[60:63], a[40:43], a[96:99], v[60:63]\n"
"v_mfma_f32_16x16x32_bf16 v[64:67], a[40:43], a[104:107], v[64:67]\n"
"s_add_u32 s52, s56, %11\n"
"s_addc_u32 s53, s57, 0\n"
"v_mfma_f32_16x16x32_bf16 v[64:67], a[44:47], a[108:111], v[64:67]\n"
"v_mfma_f32_16x16x32_bf16 v[36:39], a[36:39], a[108:111], v[36:39]\n"
"s_add_u32 s52, s52, 4096\n"
"s_addc_u32 s53, s53, 0\n"
"v_mfma_f32_16x16x32_bf16 v[36:39], a[32:35], a[104:107], v[36:39]\n"
"s_waitcnt lgkmcnt(8)\n"
"v_mfma_f32_16x16x32_bf16 v[40:43], a[32:35], a[112:115], v[40:43]\n"
"s_add_u32 s54, s52, s73\n"
"s_addc_u32 s55, s53, 0\n"
"v_mfma_f32_16x16x32_bf16 v[40:43], a[36:39], a[116:119], v[40:43]\n"
"global_load_dwordx4 a[84:87], v0, s[34:35] offset:1024 sc1\n"
"v_mfma_f32_16x16x32_bf16 v[68:71], a[44:47], a[116:119], v[68:71]\n"
"s_add_u32 s52, s52, s72\n"
"s_addc_u32 s53, s53, 0\n"
"v_mfma_f32_16x16x32_bf16 v[68:71], a[40:43], a[112:115], v[68:71]\n"
"v_mfma_f32_16x16x32_bf16 v[72:75], a[40:43], a[120:123], v[72:75]\n"
"s_add_u32 s48, s56, %12\n"
"s_addc_u32 s49, s57, 0\n"
"v_mfma_f32_16x16x32_bf16 v[72:75], a[44:47], a[124:127], v[72:75]\n"
"v_mfma_f32_16x16x32_bf16 v[44:47], a[36:39], a[124:127], v[44:47]\n"
"s_mov_b32 s50, 32768\n"
"v_mfma_f32_16x16x32_bf16 v[44:47], a[32:35], a[120:123], v[44:47]\n"
"s_waitcnt lgkmcnt(4)\n"
"v_mfma_f32_16x16x32_bf16 v[48:51], a[32:35], a[128:131], v[48:51]\n"
"v_mfma_f32_16x16x32_bf16 v[48:51], a[36:39], a[132:135], v[48:51]\n"
"global_load_dwordx4 a[88:91], v0, s[34:35] offset:2048 sc1\n"
"v_mfma_f32_16x16x32_bf16 v[76:79], a[44:47], a[132:135], v[76:79]\n"
"v_mfma_f32_16x16x32_bf16 v[76:79], a[40:43], a[128:131], v[76:79]\n"
"v_mfma_f32_16x16x32_bf16 v[80:83], a[40:43], a[136:139], v[80:83]\n"
"v_mfma_f32_16x16x32_bf16 v[80:83], a[44:47], a[140:143], v[80:83]\n"
"v_mfma_f32_16x16x32_bf16 v[52:55], a[36:39], a[140:143], v[52:55]\n"
"v_mfma_f32_16x16x32_bf16 v[52:55], a[32:35], a[136:139], v[52:55]\n"
"s_waitcnt lgkmcnt(0)\n"
"v_mfma_f32_16x16x32_bf16 v[56:59], a[32:35], a[144:147], v[56:59]\n"
"v_mfma_f32_16x16x32_bf16 v[56:59], a[36:39], a[148:151], v[56:59]\n"
"global_load_dwordx4 a[92:95], v0, s[34:35] offset:3072 sc1\n"
"v_mfma_f32_16x16x32_bf16 v[84:87], a[44:47], a[148:151], v[84:87]\n"
"v_mfma_f32_16x16x32_bf16 v[84:87], a[40:43], a[144:147], v[84:87]\n"
"v_mfma_f32_16x16x32_bf16 a[4:7], a[40:43], a[152:155], a[4:7]\n"
"s_add_u32 s34, s34, 8192\n"
"v_mfma_f32_16x16x32_bf16 a[4:7], a[44:47], a[156:159], a[4:7]\n"
"v_mfma_f32_16x16x32_bf16 a[0:3], a[36:39], a[156:159], a[0:3]\n"
"v_mfma_f32_16x16x32_bf16 a[0:3], a[32:35], a[152:155], a[0:3]\n"
"s_waitcnt vmcnt(9)\n"
"ds_read_b128 a[192:195], v7 offset:16384\n"
"v_mfma_f32_16x16x32_bf16 v[88:91], a[48:51], a[96:99], v[88:91]\n"
"ds_read_b128 a[196:199], v8 offset:16384\n"
"v_mfma_f32_16x16x32_bf16 v[88:91], a[52:55], a[100:103], v[88:91]\n"
"global_load_dwordx4 a[32:35], v0, s[34:35] offset:-4096 sc1\n"
"v_mfma_f32_16x16x32_bf16 v[116:119], a[60:63], a[100:103], v[116:119]\n"
"v_mfma_f32_16x16x32_bf16 v[116:119], a[56:59], a[96:99], v[116:119]\n"
"ds_read_b128 a[200:203], v7 offset:18432\n"
"v_mfma_f32_16x16x32_bf16 v[120:123], a[56:59], a[104:107], v[120:123]\n"
"v_mfma_f32_16x16x32_bf16 v[120:123], a[60:63], a[108:111], v[120:123]\n"
"ds_read_b128 a[204:207], v8 offset:18432\n"
"v_mfma_f32_16x16x32_bf16 v[92:95], a[52:55], a[108:111], v[92:95]\n"
"v_mfma_f32_16x16x32_bf16 v[92:95], a[48:51], a[104:107], v[92:95]\n"
"ds_read_b128 a[208:211], v7 offset:20480\n"
"v_mfma_f32_16x16x32_bf16 v[96:99], a[48:51], a[112:115], v[96:99]\n"
"ds_read_b128 a[212:215], v8 offset:20480\n"
"v_mfma_f32_16x16x32_bf16 v[96:99], a[52:55], a[116:119], v[96:99]\n"
"global_load_dwordx4 a[36:39], v0, s[34:35] offset:-3072 sc1\n"
"v_mfma_f32_16x16x32_bf16 v[124:127], a[60:63], a[116:119], v[124:127]\n"
"v_mfma_f32_16x16x32_bf16 v[124:127], a[56:59], a[112:115], v[124:127]\n"
"ds_read_b128 a[216:219], v7 offset:22528\n"
"v_mfma_f32_16x16x32_bf16 v[128:131], a[56:59], a[120:123], v[128:131]\n"
"v_mfma_f32_16x16x32_bf16 v[128:131], a[60:63], a[124:127], v[128:131]\n"
"ds_read_b128 a[220:223], v8 offset:22528\n"
"v_mfma_f32_16x16x32_bf16 v[100:103], a[52:55], a[124:127], v[100:103]\n"
"v_mfma_f32_16x16x32_bf16 v[100:103], a[48:51], a[120:123], v[100:103]\n"
"ds_read_b128 a[224:227], v7 offset:24576\n"
"v_mfma_f32_16x16x32_bf16 v[104:107], a[48:51], a[128:131], v[104:107]\n"
"ds_read_b128 a[228:231], v8 offset:24576\n"
"v_mfma_f32_16x16x32_bf16 v[104:107], a[52:55], a[132:135], v[104:107]\n"
"global_load_dwordx4 a[40:43], v0, s[34:35] offset:-2048 sc1\n"
"v_mfma_f32_16x16x32_bf16 v[132:135], a[60:63], a[132:135], v[132:135]\n"
"v_mfma_f32_16x16x32_bf16 v[132:135], a[56:59], a[128:131], v[132:135]\n"
"ds_read_b128 a[232:235], v7 offset:26624\n"
"v_mfma_f32_16x16x32_bf16 v[136:139], a[56:59], a[136:139], v[136:139]\n"
"v_mfma_f32_16x16x32_bf16 v[136:139], a[60:63], a[140:143], v[136:139]\n"
"ds_read_b128 a[236:239], v8 offset:26624\n"
"v_mfma_f32_16x16x32_bf16 v[108:111], a[52:55], a[140:143], v[108:111]\n"
"v_mfma_f32_16x16x32_bf16 v[108:111], a[48:51], a[136:139], v[108:111]\n"
"ds_read_b128 a[240:243], v7 offset:28672\n"
"v_mfma_f32_16x16x32_bf16 v[112:115], a[48:51], a[144:147], v[112:115]\n"
"ds_read_b128 a[244:247], v8 offset:28672\n"
"v_mfma_f32_16x16x32_bf16 v[112:115], a[52:55], a[148:151], v[112:115]\n"
"global_load_dwordx4 a[44:47], v0, s[34:35] offset:-1024 sc1\n"
"v_mfma_f32_16x16x32_bf16 v[140:143], a[60:63], a[148:151], v[140:143]\n"
"v_mfma_f32_16x16x32_bf16 v[140:143], a[56:59], a[144:147], v[140:143]\n"
"ds_read_b128 a[248:251], v7 offset:30720\n"
"v_mfma_f32_16x16x32_bf16 a[12:15], a[56:59], a[152:155], a[12:15]\n"
"v_mfma_f32_16x16x32_bf16 a[12:15], a[60:63], a[156:159], a[12:15]\n"
"ds_read_b128 a[252:255], v8 offset:30720\n"
"v_mfma_f32_16x16x32_bf16 a[8:11], a[52:55], a[156:159], a[8:11]\n"
"v_mfma_f32_16x16x32_bf16 a[8:11], a[48:51], a[152:155], a[8:11]\n"
"s_waitcnt vmcnt(8) lgkmcnt(14)\n"
"ds_write_b128 v2, a[192:195] offset:16384\n"
"v_mfma_f32_16x16x32_bf16 v[144:147], a[64:67], a[96:99], v[144:147]\n"
"v_mfma_f32_16x16x32_bf16 v[144:147], a[68:71], a[100:103], v[144:147]\n"
"global_load_dwordx4 a[48:51], v0, s[34:35] offset:0 sc1\n"
"v_mfma_f32_16x16x32_bf16 v[172:175], a[76:79], a[100:103], v[172:175]\n"
"ds_write_b128 v2, a[196:199] offset:17408\n"
"v_mfma_f32_16x16x32_bf16 v[172:175], a[72:75], a[96:99], v[172:175]\n"
"s_waitcnt lgkmcnt(14)\n"
"ds_write_b128 v2, a[200:203] offset:18432\n"
"v_mfma_f32_16x16x32_bf16 v[176:179], a[72:75], a[104:107], v[176:179]\n"
"v_mfma_f32_16x16x32_bf16 v[176:179], a[76:79], a[108:111], v[176:179]\n"
"ds_write_b128 v2, a[204:207] offset:19456\n"
"v_mfma_f32_16x16x32_bf16 v[148:151], a[68:71], a[108:111], v[148:151]\n"
"v_mfma_f32_16x16x32_bf16 v[148:151], a[64:67], a[104:107], v[148:151]\n"
"s_waitcnt lgkmcnt(14)\n"
"ds_write_b128 v2, a[208:211] offset:20480\n"
"v_mfma_f32_16x16x32_bf16 v[152:155], a[64:67], a[112:115], v[152:155]\n"
"v_mfma_f32_16x16x32_bf16 v[152:155], a[68:71], a[116:119], v[152:155]\n"
"global_load_dwordx4 a[52:55], v0, s[34:35] offset:1024 sc1\n"
"v_mfma_f32_16x16x32_bf16 v[180:183], a[76:79], a[116:119], v[180:183]\n"
"ds_write_b128 v2, a[212:215] offset:21504\n"
"v_mfma_f32_16x16x32_bf16 v[180:183], a[72:75], a[112:115], v[180:183]\n"
"s_waitcnt lgkmcnt(14)\n"
"ds_write_b128 v2, a[216:219] offset:22528\n"
"v_mfma_f32_16x16x32_bf16 v[184:187], a[72:75], a[120:123], v[184:187]\n"
"v_mfma_f32_16x16x32_bf16 v[184:187], a[76:79], a[124:127], v[184:187]\n"
"ds_write_b128 v2, a[220:223] offset:23552\n"
"v_mfma_f32_16x16x32_bf16 v[156:159], a[68:71], a[124:127], v[156:159]\n"
"v_mfma_f32_16x16x32_bf16 v[156:159], a[64:67], a[120:123], v[156:159]\n"
"s_waitcnt lgkmcnt(14)\n"
"ds_write_b128 v2, a[224:227] offset:24576\n"
"v_mfma_f32_16x16x32_bf16 v[160:163], a[64:67], a[128:131], v[160:163]\n"
"v_mfma_f32_16x16x32_bf16 v[160:163], a[68:71], a[132:135], v[160:163]\n"
"global_load_dwordx4 a[56:59], v0, s[34:35] offset:2048 sc1\n"
"v_mfma_f32_16x16x32_bf16 v[188:191], a[76:79], a[132:135], v[188:191]\n"
"ds_write_b128 v2, a[228:231] offset:25600\n"
"v_mfma_f32_16x16x32_bf16 v[188:191], a[72:75], a[128:131], v[188:191]\n"
"s_waitcnt lgkmcnt(14)\n"
"ds_write_b128 v2, a[232:235] offset:26624\n"
"v_mfma_f32_16x16x32_bf16 v[192:195], a[72:75], a[136:139], v[192:195]\n"
"v_mfma_f32_16x16x32_bf16 v[192:195], a[76:79], a[140:143], v[192:195]\n"
"s_add_u32 s70, s78, 2\n"
"ds_write_b128 v2, a[236:239] offset:27648\n"
"v_mfma_f32_16x16x32_bf16 v[164:167], a[68:71], a[140:143], v[164:167]\n"
"s_waitcnt lgkmcnt(14)\n"
"ds_write_b128 v2, a[240:243] offset:28672\n"
"v_mfma_f32_16x16x32_bf16 v[164:167], a[64:67], a[136:139], v[164:167]\n"
"v_mov_b32 v14, s70\n"
"v_mfma_f32_16x16x32_bf16 v[168:171], a[64:67], a[144:147], v[168:171]\n"
"v_mov_b32 v15, s79\n"
"v_mfma_f32_16x16x32_bf16 v[168:171], a[68:71], a[148:151], v[168:171]\n"
"global_load_dwordx4 a[60:63], v0, s[34:35] offset:3072 sc1\n"
"v_mfma_f32_16x16x32_bf16 v[196:199], a[76:79], a[148:151], v[196:199]\n"
"buffer_store_dwordx2 v[14:15], off, s[56:59], %24 sc1\n"
"v_mfma_f32_16x16x32_bf16 v[196:199], a[72:75], a[144:147], v[196:199]\n"
"s_waitcnt lgkmcnt(13)\n"
"ds_write_b128 v2, a[244:247] offset:29696\n"
"v_mfma_f32_16x16x32_bf16 a[20:23], a[72:75], a[152:155], a[20:23]\n"
"s_add_u32 s34, s34, 8192\n"
"ds_write_b128 v2, a[248:251] offset:30720\n"
"v_mfma_f32_16x16x32_bf16 a[20:23], a[76:79], a[156:159], a[20:23]\n"
"ds_write_b128 v2, a[252:255] offset:31744\n"
"v_mfma_f32_16x16x32_bf16 a[16:19], a[68:71], a[156:159], a[16:19]\n"
"v_mfma_f32_16x16x32_bf16 a[16:19], a[64:67], a[152:155], a[16:19]\n"
"s_waitcnt vmcnt(9)\n"
"v_mfma_f32_16x16x32_bf16 v[200:203], a[80:83], a[96:99], v[200:203]\n"
"v_mfma_f32_16x16x32_bf16 v[200:203], a[84:87], a[100:103], v[200:203]\n"
"global_load_dwordx4 a[64:67], v0, s[34:35] offset:-4096 sc1\n"
"v_mfma_f32_16x16x32_bf16 v[228:231], a[92:95], a[100:103], v[228:231]\n"
"ds_read_b128 a[100:103], v2 offset:17408\n"
"v_mfma_f32_16x16x32_bf16 v[228:231], a[88:91], a[96:99], v[228:231]\n"
"ds_read_b128 a[96:99], v2 offset:16384\n"
"v_mfma_f32_16x16x32_bf16 v[232:235], a[88:91], a[104:107], v[232:235]\n"
"v_mfma_f32_16x16x32_bf16 v[232:235], a[92:95], a[108:111], v[232:235]\n"
"v_mfma_f32_16x16x32_bf16 v[204:207], a[84:87], a[108:111], v[204:207]\n"
"s_mov_b32 s70, 0\n"
"ds_read_b128 a[108:111], v2 offset:19456\n"
"v_mfma_f32_16x16x32_bf16 v[204:207], a[80:83], a[104:107], v[204:207]\n"
"ds_read_b128 a[104:107], v2 offset:18432\n"
"v_mfma_f32_16x16x32_bf16 v[208:211], a[80:83], a[112:115], v[208:211]\n"
"v_mfma_f32_16x16x32_bf16 v[208:211], a[84:87], a[116:119], v[208:211]\n"
"global_load_dwordx4 a[68:71], v0, s[34:35] offset:-3072 sc1\n"
"v_mfma_f32_16x16x32_bf16 v[236:239], a[92:95], a[116:119], v[236:239]\n"
"ds_read_b128 a[116:119], v2 offset:21504\n"
"v_mfma_f32_16x16x32_bf16 v[236:239], a[88:91], a[112:115], v[236:239]\n"
"s_mov_b32 s71, 0\n"
"ds_read_b128 a[112:115], v2 offset:20480\n"
"v_mfma_f32_16x16x32_bf16 v[240:243], a[88:91], a[120:123], v[240:243]\n"
"v_mfma_f32_16x16x32_bf16 v[240:243], a[92:95], a[124:127], v[240:243]\n"
"v_mfma_f32_16x16x32_bf16 v[212:215], a[84:87], a[124:127], v[212:215]\n"
"ds_read_b128 a[124:127], v2 offset:23552\n"
"v_mfma_f32_16x16x32_bf16 v[212:215], a[80:83], a[120:123], v[212:215]\n"
"s_mov_b32 s98, 32768\n"
"ds_read_b128 a[120:123], v2 offset:22528\n"
"v_mfma_f32_16x16x32_bf16 v[216:219], a[80:83], a[128:131], v[216:219]\n"
"v_mfma_f32_16x16x32_bf16 v[216:219], a[84:87], a[132:135], v[216:219]\n"
"global_load_dwordx4 a[72:75], v0, s[34:35] offset:-2048 sc1\n"
"v_mfma_f32_16x16x32_bf16 v[244:247], a[92:95], a[132:135], v[244:247]\n"
"ds_read_b128 a[132:135], v2 offset:25600\n"
"v_mfma_f32_16x16x32_bf16 v[244:247], a[88:91], a[128:131], v[244:247]\n"
"ds_read_b128 a[128:131], v2 offset:24576\n"
"v_mfma_f32_16x16x32_bf16 v[248:251], a[88:91], a[136:139], v[248:251]\n"
"s_mov_b32 s99, 0x20000\n"
"v_mfma_f32_16x16x32_bf16 v[248:251], a[92:95], a[140:143], v[248:251]\n"
"v_mfma_f32_16x16x32_bf16 v[220:223], a[84:87], a[140:143], v[220:223]\n"
"ds_read_b128 a[140:143], v2 offset:27648\n"
"v_mfma_f32_16x16x32_bf16 v[220:223], a[80:83], a[136:139], v[220:223]\n"
"ds_read_b128 a[136:139], v2 offset:26624\n"
"v_mfma_f32_16x16x32_bf16 v[224:227], a[80:83], a[144:147], v[224:227]\n"
"v_mfma_f32_16x16x32_bf16 v[224:227], a[84:87], a[148:151], v[224:227]\n"
"global_load_dwordx4 a[76:79], v0, s[34:35] offset:-1024 sc1\n"
"v_mfma_f32_16x16x32_bf16 v[252:255], a[92:95], a[148:151], v[252:255]\n"
"ds_read_b128 a[148:151], v2 offset:29696\n"
"v_mfma_f32_16x16x32_bf16 v[252:255], a[88:91], a[144:147], v[252:255]\n"
"ds_read_b128 a[144:147], v2 offset:28672\n"
"v_mfma_f32_16x16x32_bf16 a[28:31], a[88:91], a[152:155], a[28:31]\n"
"v_mfma_f32_16x16x32_bf16 a[28:31], a[92:95], a[156:159], a[28:31]\n"
"v_mfma_f32_16x16x32_bf16 a[24:27], a[84:87], a[156:159], a[24:27]\n"
"ds_read_b128 a[156:159], v2 offset:31744\n"
"v_mfma_f32_16x16x32_bf16 a[24:27], a[80:83], a[152:155], a[24:27]\n"
"s_waitcnt lgkmcnt(15)\n"
"ds_read_b128 a[152:155], v2 offset:30720\n"
"s_branch L_P5\n"
"L_layer:\n"
"s_waitcnt vmcnt(11) lgkmcnt(14)\n"
"v_mfma_f32_16x16x32_bf16 v[32:35], a[32:35], a[96:99], v[32:35]\n"
"s_mov_b64 s[96:97], s[52:53]\n"
"v_accvgpr_read_b32 v16, a192\n"
"v_accvgpr_read_b32 v17, a193\n"
"v_mfma_f32_16x16x32_bf16 v[32:35], a[36:39], a[100:103], v[32:35]\n"
"global_load_dwordx4 a[80:83], v0, s[34:35] offset:0 sc1\n"
"v_accvgpr_read_b32 v18, a194\n"
"v_mfma_f32_16x16x32_bf16 v[60:63], a[44:47], a[100:103], v[60:63]\n"
"s_mov_b64 s[98:99], s[54:55]\n"
"v_accvgpr_read_b32 v19, a195\n"
"v_mfma_f32_16x16x32_bf16 v[60:63], a[40:43], a[96:99], v[60:63]\n"
"v_lshlrev_b32 v12, 16, v16\n"
"v_and_b32 v13, s63, v16\n"
"v_mfma_f32_16x16x32_bf16 v[64:67], a[40:43], a[104:107], v[64:67]\n"
"s_add_u32 s52, s52, 131072\n"
"s_addc_u32 s53, s53, 0\n"
"v_lshlrev_b32 v14, 16, v17\n"
"v_mfma_f32_16x16x32_bf16 v[64:67], a[44:47], a[108:111], v[64:67]\n"
"v_and_b32 v15, s63, v17\n"
"v_mul_f32 v12, v12, v144\n"
"v_mfma_f32_16x16x32_bf16 v[36:39], a[36:39], a[108:111], v[36:39]\n"
"s_add_u32 s54, s54, 131072\n"
"s_addc_u32 s55, s55, 0\n"
"v_mul_f32 v13, v13, v145\n"
"v_mfma_f32_16x16x32_bf16 v[36:39], a[32:35], a[104:107], v[36:39]\n"
"s_waitcnt lgkmcnt(9)\n"
"v_mul_f32 v14, v14, v146\n"
"v_mul_f32 v15, v15, v147\n"
"v_mfma_f32_16x16x32_bf16 v[40:43], a[32:35], a[112:115], v[40:43]\n"
"ds_read_b128 v[20:23], v2 offset:0\n"
"v_cvt_pk_bf16_f32 v24, v12, v13\n"
"v_mfma_f32_16x16x32_bf16 v[40:43], a[36:39], a[116:119], v[40:43]\n"
"global_load_dwordx4 a[84:87], v0, s[34:35] offset:1024 sc1\n"
"v_cvt_pk_bf16_f32 v25, v14, v15\n"
"v_mfma_f32_16x16x32_bf16 v[68:71], a[44:47], a[116:119], v[68:71]\n"
"ds_write_b64 v2, v[24:25] offset:16384\n"
"v_mfma_f32_16x16x32_bf16 v[68:71], a[40:43], a[112:115], v[68:71]\n"
"v_lshlrev_b32 v12, 16, v18\n"
"v_and_b32 v13, s63, v18\n"
"v_mfma_f32_16x16x32_bf16 v[72:75], a[40:43], a[120:123], v[72:75]\n"
"s_waitcnt lgkmcnt(5)\n"
"s_barrier\n"
"v_mfma_f32_16x16x32_bf16 v[72:75], a[44:47], a[124:127], v[72:75]\n"
"v_lshlrev_b32 v14, 16, v19\n"
"v_and_b32 v15, s63, v19\n"
"v_mfma_f32_16x16x32_bf16 v[44:47], a[36:39], a[124:127], v[44:47]\n"
"v_mul_f32 v12, v12, v172\n"
"v_mul_f32 v13, v13, v173\n"
"v_mfma_f32_16x16x32_bf16 v[44:47], a[32:35], a[120:123], v[44:47]\n"
"v_mul_f32 v14, v14, v174\n"
"v_mfma_f32_16x16x32_bf16 v[48:51], a[32:35], a[128:131], v[48:51]\n"
"v_mul_f32 v15, v15, v175\n"
"v_mfma_f32_16x16x32_bf16 v[48:51], a[36:39], a[132:135], v[48:51]\n"
"global_load_dwordx4 a[88:91], v0, s[34:35] offset:2048 sc1\n"
"v_mfma_f32_16x16x32_bf16 v[76:79], a[44:47], a[132:135], v[76:79]\n"
"v_cvt_pk_bf16_f32 v26, v12, v13\n"
"v_mfma_f32_16x16x32_bf16 v[76:79], a[40:43], a[128:131], v[76:79]\n"
"v_cvt_pk_bf16_f32 v27, v14, v15\n"
"v_mfma_f32_16x16x32_bf16 v[80:83], a[40:43], a[136:139], v[80:83]\n"
"s_waitcnt lgkmcnt(1)\n"
"global_store_dwordx4 v0, v[20:23], s[96:97] offset:-4096 sc1\n"
"ds_write_b64 v2, v[26:27] offset:16392\n"
"v_accvgpr_read_b32 v16, a196\n"
"v_mfma_f32_16x16x32_bf16 v[80:83], a[44:47], a[140:143], v[80:83]\n"
"v_accvgpr_read_b32 v17, a197\n"
"v_accvgpr_read_b32 v18, a198\n"
"v_mfma_f32_16x16x32_bf16 v[52:55], a[36:39], a[140:143], v[52:55]\n"
"v_accvgpr_read_b32 v19, a199\n"
"v_lshlrev_b32 v12, 16, v16\n"
"v_mfma_f32_16x16x32_bf16 v[52:55], a[32:35], a[136:139], v[52:55]\n"
"v_and_b32 v13, s63, v16\n"
"v_lshlrev_b32 v14, 16, v17\n"
"v_mfma_f32_16x16x32_bf16 v[56:59], a[32:35], a[144:147], v[56:59]\n"
"v_and_b32 v15, s63, v17\n"
"v_mul_f32 v12, v12, v200\n"
"v_mfma_f32_16x16x32_bf16 v[56:59], a[36:39], a[148:151], v[56:59]\n"
"global_load_dwordx4 a[92:95], v0, s[34:35] offset:3072 sc1\n"
"v_mul_f32 v13, v13, v201\n"
"v_mfma_f32_16x16x32_bf16 v[84:87], a[44:47], a[148:151], v[84:87]\n"
"v_mul_f32 v14, v14, v202\n"
"v_mul_f32 v15, v15, v203\n"
"v_mfma_f32_16x16x32_bf16 v[84:87], a[40:43], a[144:147], v[84:87]\n"
"v_cvt_pk_bf16_f32 v28, v12, v13\n"
"v_cvt_pk_bf16_f32 v29, v14, v15\n"
"v_mfma_f32_16x16x32_bf16 a[4:7], a[40:43], a[152:155], a[4:7]\n"
"s_add_u32 s34, s34, 8192\n"
"ds_write_b64 v2, v[28:29] offset:17408\n"
"v_mfma_f32_16x16x32_bf16 a[4:7], a[44:47], a[156:159], a[4:7]\n"
"v_lshlrev_b32 v12, 16, v18\n"
"v_and_b32 v13, s63, v18\n"
"v_mfma_f32_16x16x32_bf16 a[0:3], a[36:39], a[156:159], a[0:3]\n"
"v_lshlrev_b32 v14, 16, v19\n"
"v_and_b32 v15, s63, v19\n"
"v_mfma_f32_16x16x32_bf16 a[0:3], a[32:35], a[152:155], a[0:3]\n"
"s_waitcnt vmcnt(11)\n"
"v_mul_f32 v12, v12, v228\n"
"v_mul_f32 v13, v13, v229\n"
"v_mfma_f32_16x16x32_bf16 v[88:91], a[48:51], a[96:99], v[88:91]\n"
"ds_read_b128 v[20:23], v2 offset:5120\n"
"v_mul_f32 v14, v14, v230\n"
"v_mfma_f32_16x16x32_bf16 v[88:91], a[52:55], a[100:103], v[88:91]\n"
"global_load_dwordx4 a[32:35], v0, s[34:35] offset:-4096 sc1\n"
"v_mul_f32 v15, v15, v231\n"
"v_mfma_f32_16x16x32_bf16 v[116:119], a[60:63], a[100:103], v[116:119]\n"
"ds_read_b128 a[100:103], v3 offset:1024\n"
"v_cvt_pk_bf16_f32 v30, v12, v13\n"
"v_mfma_f32_16x16x32_bf16 v[116:119], a[56:59], a[96:99], v[116:119]\n"
"ds_read_b128 a[96:99], v3 offset:0\n"
"v_cvt_pk_bf16_f32 v31, v14, v15\n"
"v_mfma_f32_16x16x32_bf16 v[120:123], a[56:59], a[104:107], v[120:123]\n"
"ds_write_b64 v2, v[30:31] offset:17416\n"
"v_mfma_f32_16x16x32_bf16 v[120:123], a[60:63], a[108:111], v[120:123]\n"
"v_accvgpr_read_b32 v16, a200\n"
"v_accvgpr_read_b32 v17, a201\n"
"v_mfma_f32_16x16x32_bf16 v[92:95], a[52:55], a[108:111], v[92:95]\n"
"ds_read_b128 a[108:111], v3 offset:3072\n"
"v_accvgpr_read_b32 v18, a202\n"
"v_mfma_f32_16x16x32_bf16 v[92:95], a[48:51], a[104:107], v[92:95]\n"
"ds_read_b128 a[104:107], v3 offset:2048\n"
"v_accvgpr_read_b32 v19, a203\n"
"v_mfma_f32_16x16x32_bf16 v[96:99], a[48:51], a[112:115], v[96:99]\n"
"v_lshlrev_b32 v12, 16, v16\n"
"v_and_b32 v13, s63, v16\n"
"v_mfma_f32_16x16x32_bf16 v[96:99], a[52:55], a[116:119], v[96:99]\n"
"global_load_dwordx4 a[36:39], v0, s[34:35] offset:-3072 sc1\n"
"v_lshlrev_b32 v14, 16, v17\n"
"v_mfma_f32_16x16x32_bf16 v[124:127], a[60:63], a[116:119], v[124:127]\n"
"ds_read_b128 a[116:119], v3 offset:5120\n"
"v_and_b32 v15, s63, v17\n"
"v_mfma_f32_16x16x32_bf16 v[124:127], a[56:59], a[112:115], v[124:127]\n"
"ds_read_b128 a[112:115], v3 offset:4096\n"
"v_mfma_f32_16x16x32_bf16 v[128:131], a[56:59], a[120:123], v[128:131]\n"
"s_waitcnt lgkmcnt(7)\n"
"global_store_dwordx4 v0, v[20:23], s[96:97] offset:1024 sc1\n"
"v_mul_f32 v12, v12, v148\n"
"v_mul_f32 v13, v13, v149\n"
"v_mul_f32 v14, v14, v150\n"
"v_mul_f32 v15, v15, v151\n"
"v_mfma_f32_16x16x32_bf16 v[128:131], a[60:63], a[124:127], v[128:131]\n"
"v_cvt_pk_bf16_f32 v24, v12, v13\n"
"v_mfma_f32_16x16x32_bf16 v[100:103], a[52:55], a[124:127], v[100:103]\n"
"ds_read_b128 a[124:127], v3 offset:7168\n"
"v_cvt_pk_bf16_f32 v25, v14, v15\n"
"v_mfma_f32_16x16x32_bf16 v[100:103], a[48:51], a[120:123], v[100:103]\n"
"ds_read_b128 a[120:123], v3 offset:6144\n"
"v_mfma_f32_16x16x32_bf16 v[104:107], a[48:51], a[128:131], v[104:107]\n"
"ds_write_b64 v2, v[24:25] offset:18432\n"
"v_mfma_f32_16x16x32_bf16 v[104:107], a[52:55], a[132:135], v[104:107]\n"
"global_load_dwordx4 a[40:43], v0, s[34:35] offset:-2048 sc1\n"
"v_lshlrev_b32 v12, 16, v18\n"
"v_mfma_f32_16x16x32_bf16 v[132:135], a[60:63], a[132:135], v[132:135]\n"
"ds_read_b128 a[132:135], v3 offset:9216\n"
"v_and_b32 v13, s63, v18\n"
"v_mfma_f32_16x16x32_bf16 v[132:135], a[56:59], a[128:131], v[132:135]\n"
"ds_read_b128 a[128:131], v3 offset:8192\n"
"v_lshlrev_b32 v14, 16, v19\n"
"v_mfma_f32_16x16x32_bf16 v[136:139], a[56:59], a[136:139], v[136:139]\n"
"v_and_b32 v15, s63, v19\n"
"v_mul_f32 v12, v12, v176\n"
"v_mfma_f32_16x16x32_bf16 v[136:139], a[60:63], a[140:143], v[136:139]\n"
"v_mul_f32 v13, v13, v177\n"
"v_mul_f32 v14, v14, v178\n"
"v_mfma_f32_16x16x32_bf16 v[108:111], a[52:55], a[140:143], v[108:111]\n"
"ds_read_b128 a[140:143], v3 offset:11264\n"
"v_mul_f32 v15, v15, v179\n"
"v_mfma_f32_16x16x32_bf16 v[108:111], a[48:51], a[136:139], v[108:111]\n"
"ds_read_b128 a[136:139], v3 offset:10240\n"
"v_cvt_pk_bf16_f32 v26, v12, v13\n"
"v_mfma_f32_16x16x32_bf16 v[112:115], a[48:51], a[144:147], v[112:115]\n"
"ds_read_b128 v[20:23], v2 offset:10240\n"
"v_cvt_pk_bf16_f32 v27, v14, v15\n"
"v_mfma_f32_16x16x32_bf16 v[112:115], a[52:55], a[148:151], v[112:115]\n"
"global_load_dwordx4 a[44:47], v0, s[34:35] offset:-1024 sc1\n"
"ds_write_b64 v2, v[26:27] offset:18440\n"
"v_mfma_f32_16x16x32_bf16 v[140:143], a[60:63], a[148:151], v[140:143]\n"
"ds_read_b128 a[148:151], v3 offset:13312\n"
"v_accvgpr_read_b32 v16, a204\n"
"v_mfma_f32_16x16x32_bf16 v[140:143], a[56:59], a[144:147], v[140:143]\n"
"ds_read_b128 a[144:147], v3 offset:12288\n"
"v_accvgpr_read_b32 v17, a205\n"
"v_mfma_f32_16x16x32_bf16 a[12:15], a[56:59], a[152:155], a[12:15]\n"
"v_accvgpr_read_b32 v18, a206\n"
"v_accvgpr_read_b32 v19, a207\n"
"v_mfma_f32_16x16x32_bf16 a[12:15], a[60:63], a[156:159], a[12:15]\n"
"v_lshlrev_b32 v12, 16, v16\n"
"v_and_b32 v13, s63, v16\n"
"v_mfma_f32_16x16x32_bf16 a[8:11], a[52:55], a[156:159], a[8:11]\n"
"ds_read_b128 a[156:159], v3 offset:15360\n"
"v_lshlrev_b32 v14, 16, v17\n"
"v_mfma_f32_16x16x32_bf16 a[8:11], a[48:51], a[152:155], a[8:11]\n"
"s_waitcnt vmcnt(11) lgkmcnt(14)\n"
"ds_read_b128 a[152:155], v3 offset:14336\n"
"v_and_b32 v15, s63, v17\n"
"v_mfma_f32_16x16x32_bf16 v[32:35], a[64:67], a[96:99], v[32:35]\n"
"v_mul_f32 v12, v12, v204\n"
"v_mul_f32 v13, v13, v205\n"
"v_mfma_f32_16x16x32_bf16 v[32:35], a[68:71], a[100:103], v[32:35]\n"
"global_load_dwordx4 a[48:51], v0, s[34:35] offset:0 sc1\n"
"v_mul_f32 v14, v14, v206\n"
"v_mfma_f32_16x16x32_bf16 v[60:63], a[76:79], a[100:103], v[60:63]\n"
"v_mul_f32 v15, v15, v207\n"
"v_cvt_pk_bf16_f32 v28, v12, v13\n"
"v_mfma_f32_16x16x32_bf16 v[60:63], a[72:75], a[96:99], v[60:63]\n"
"v_cvt_pk_bf16_f32 v29, v14, v15\n"
"v_mfma_f32_16x16x32_bf16 v[64:67], a[72:75], a[104:107], v[64:67]\n"
"s_waitcnt lgkmcnt(5)\n"
"global_store_dwordx4 v0, v[20:23], s[98:99] offset:-2048 sc1\n"
"ds_write_b64 v2, v[28:29] offset:19456\n"
"v_lshlrev_b32 v12, 16, v18\n"
"v_mfma_f32_16x16x32_bf16 v[64:67], a[76:79], a[108:111], v[64:67]\n"
"v_and_b32 v13, s63, v18\n"
"v_lshlrev_b32 v14, 16, v19\n"
"v_mfma_f32_16x16x32_bf16 v[36:39], a[68:71], a[108:111], v[36:39]\n"
"v_and_b32 v15, s63, v19\n"
"v_mul_f32 v12, v12, v232\n"
"v_mfma_f32_16x16x32_bf16 v[36:39], a[64:67], a[104:107], v[36:39]\n"
"v_mul_f32 v13, v13, v233\n"
"v_mul_f32 v14, v14, v234\n"
"v_mfma_f32_16x16x32_bf16 v[40:43], a[64:67], a[112:115], v[40:43]\n"
"v_mul_f32 v15, v15, v235\n"
"v_cvt_pk_bf16_f32 v30, v12, v13\n"
"v_mfma_f32_16x16x32_bf16 v[40:43], a[68:71], a[116:119], v[40:43]\n"
"global_load_dwordx4 a[52:55], v0, s[34:35] offset:1024 sc1\n"
"v_cvt_pk_bf16_f32 v31, v14, v15\n"
"v_mfma_f32_16x16x32_bf16 v[68:71], a[76:79], a[116:119], v[68:71]\n"
"ds_write_b64 v2, v[30:31] offset:19464\n"
"v_mfma_f32_16x16x32_bf16 v[68:71], a[72:75], a[112:115], v[68:71]\n"
"v_accvgpr_read_b32 v16, a208\n"
"v_accvgpr_read_b32 v17, a209\n"
"v_mfma_f32_16x16x32_bf16 v[72:75], a[72:75], a[120:123], v[72:75]\n"
"v_accvgpr_read_b32 v18, a210\n"
"v_accvgpr_read_b32 v19, a211\n"
"v_mfma_f32_16x16x32_bf16 v[72:75], a[76:79], a[124:127], v[72:75]\n"
"v_lshlrev_b32 v12, 16, v16\n"
"v_mfma_f32_16x16x32_bf16 v[44:47], a[68:71], a[124:127], v[44:47]\n"
"v_and_b32 v13, s63, v16\n"
"v_mfma_f32_16x16x32_bf16 v[44:47], a[64:67], a[120:123], v[44:47]\n"
"v_lshlrev_b32 v14, 16, v17\n"
"v_and_b32 v15, s63, v17\n"
"v_mfma_f32_16x16x32_bf16 v[48:51], a[64:67], a[128:131], v[48:51]\n"
"v_mul_f32 v12, v12, v152\n"
"v_mfma_f32_16x16x32_bf16 v[48:51], a[68:71], a[132:135], v[48:51]\n"
"global_load_dwordx4 a[56:59], v0, s[34:35] offset:2048 sc1\n"
"v_mul_f32 v13, v13, v153\n"
"v_mfma_f32_16x16x32_bf16 v[76:79], a[76:79], a[132:135], v[76:79]\n"
"v_mul_f32 v14, v14, v154\n"
"v_mul_f32 v15, v15, v155\n"
"v_mfma_f32_16x16x32_bf16 v[76:79], a[72:75], a[128:131], v[76:79]\n"
"v_cvt_pk_bf16_f32 v24, v12, v13\n"
"v_cvt_pk_bf16_f32 v25, v14, v15\n"
"v_mfma_f32_16x16x32_bf16 v[80:83], a[72:75], a[136:139], v[80:83]\n"
"ds_write_b64 v2, v[24:25] offset:20480\n"
"v_mfma_f32_16x16x32_bf16 v[80:83], a[76:79], a[140:143], v[80:83]\n"
"v_lshlrev_b32 v12, 16, v18\n"
"v_mfma_f32_16x16x32_bf16 v[52:55], a[68:71], a[140:143], v[52:55]\n"
"v_and_b32 v13, s63, v18\n"
"v_lshlrev_b32 v14, 16, v19\n"
"v_mfma_f32_16x16x32_bf16 v[52:55], a[64:67], a[136:139], v[52:55]\n"
"s_waitcnt lgkmcnt(3)\n"
"v_and_b32 v15, s63, v19\n"
"v_mul_f32 v12, v12, v180\n"
"v_mfma_f32_16x16x32_bf16 v[56:59], a[64:67], a[144:147], v[56:59]\n"
"v_mul_f32 v13, v13, v181\n"
"v_mul_f32 v14, v14, v182\n"
"v_mfma_f32_16x16x32_bf16 v[56:59], a[68:71], a[148:151], v[56:59]\n"
"global_load_dwordx4 a[60:63], v0, s[34:35] offset:3072 sc1\n"
"v_mul_f32 v15, v15, v183\n"
"v_mfma_f32_16x16x32_bf16 v[84:87], a[76:79], a[148:151], v[84:87]\n"
"v_cvt_pk_bf16_f32 v26, v12, v13\n"
"v_cvt_pk_bf16_f32 v27, v14, v15\n"
"v_mfma_f32_16x16x32_bf16 v[84:87], a[72:75], a[144:147], v[84:87]\n"
"ds_write_b64 v2, v[26:27] offset:20488\n"
"v_mfma_f32_16x16x32_bf16 a[4:7], a[72:75], a[152:155], a[4:7]\n"
"s_add_u32 s34, s34, 8192\n"
"v_accvgpr_read_b32 v16, a212\n"
"v_mfma_f32_16x16x32_bf16 a[4:7], a[76:79], a[156:159], a[4:7]\n"
"v_accvgpr_read_b32 v17, a213\n"
"v_accvgpr_read_b32 v18, a214\n"
"v_mfma_f32_16x16x32_bf16 a[0:3], a[68:71], a[156:159], a[0:3]\n"
"v_accvgpr_read_b32 v19, a215\n"
"v_lshlrev_b32 v12, 16, v16\n"
"v_mfma_f32_16x16x32_bf16 a[0:3], a[64:67], a[152:155], a[0:3]\n"
"s_waitcnt vmcnt(10)\n"
"v_and_b32 v13, s63, v16\n"
"v_lshlrev_b32 v14, 16, v17\n"
"v_mfma_f32_16x16x32_bf16 v[88:91], a[80:83], a[96:99], v[88:91]\n"
"v_and_b32 v15, s63, v17\n"
"v_mul_f32 v12, v12, v208\n"
"v_mfma_f32_16x16x32_bf16 v[88:91], a[84:87], a[100:103], v[88:91]\n"
"global_load_dwordx4 a[64:67], v0, s[34:35] offset:-4096 sc1\n"
"v_mul_f32 v13, v13, v209\n"
"v_mfma_f32_16x16x32_bf16 v[116:119], a[92:95], a[100:103], v[116:119]\n"
"ds_read_b128 a[100:103], v4 offset:1024\n"
"v_mul_f32 v14, v14, v210\n"
"v_mfma_f32_16x16x32_bf16 v[116:119], a[88:91], a[96:99], v[116:119]\n"
"ds_read_b128 a[96:99], v4 offset:0\n"
"v_mul_f32 v15, v15, v211\n"
"v_mfma_f32_16x16x32_bf16 v[120:123], a[88:91], a[104:107], v[120:123]\n"
"v_cvt_pk_bf16_f32 v28, v12, v13\n"
"v_cvt_pk_bf16_f32 v29, v14, v15\n"
"v_mfma_f32_16x16x32_bf16 v[120:123], a[92:95], a[108:111], v[120:123]\n"
"ds_write_b64 v2, v[28:29] offset:21504\n"
"v_mfma_f32_16x16x32_bf16 v[92:95], a[84:87], a[108:111], v[92:95]\n"
"ds_read_b128 a[108:111], v4 offset:3072\n"
"v_mfma_f32_16x16x32_bf16 v[92:95], a[80:83], a[104:107], v[92:95]\n"
"ds_read_b128 a[104:107], v4 offset:2048\n"
"v_mfma_f32_16x16x32_bf16 v[96:99], a[80:83], a[112:115], v[96:99]\n"
"v_lshlrev_b32 v12, 16, v18\n"
"v_and_b32 v13, s63, v18\n"
"v_mfma_f32_16x16x32_bf16 v[96:99], a[84:87], a[116:119], v[96:99]\n"
"global_load_dwordx4 a[68:71], v0, s[34:35] offset:-3072 sc1\n"
"v_lshlrev_b32 v14, 16, v19\n"
"v_mfma_f32_16x16x32_bf16 v[124:127], a[92:95], a[116:119], v[124:127]\n"
"ds_read_b128 a[116:119], v4 offset:5120\n"
"v_and_b32 v15, s63, v19\n"
"v_mfma_f32_16x16x32_bf16 v[124:127], a[88:91], a[112:115], v[124:127]\n"
"ds_read_b128 a[112:115], v4 offset:4096\n"
"v_mul_f32 v12, v12, v236\n"
"v_mfma_f32_16x16x32_bf16 v[128:131], a[88:91], a[120:123], v[128:131]\n"
"v_mul_f32 v13, v13, v237\n"
"v_mul_f32 v14, v14, v238\n"
"v_mfma_f32_16x16x32_bf16 v[128:131], a[92:95], a[124:127], v[128:131]\n"
"v_mul_f32 v15, v15, v239\n"
"v_cvt_pk_bf16_f32 v30, v12, v13\n"
"v_mfma_f32_16x16x32_bf16 v[100:103], a[84:87], a[124:127], v[100:103]\n"
"ds_read_b128 a[124:127], v4 offset:7168\n"
"v_cvt_pk_bf16_f32 v31, v14, v15\n"
"v_mfma_f32_16x16x32_bf16 v[100:103], a[80:83], a[120:123], v[100:103]\n"
"ds_read_b128 a[120:123], v4 offset:6144\n"
"v_mfma_f32_16x16x32_bf16 v[104:107], a[80:83], a[128:131], v[104:107]\n"
"ds_write_b64 v2, v[30:31] offset:21512\n"
"v_mfma_f32_16x16x32_bf16 v[104:107], a[84:87], a[132:135], v[104:107]\n"
"global_load_dwordx4 a[72:75], v0, s[34:35] offset:-2048 sc1\n"
"v_accvgpr_read_b32 v16, a216\n"
"v_mfma_f32_16x16x32_bf16 v[132:135], a[92:95], a[132:135], v[132:135]\n"
"ds_read_b128 a[132:135], v4 offset:9216\n"
"v_accvgpr_read_b32 v17, a217\n"
"v_mfma_f32_16x16x32_bf16 v[132:135], a[88:91], a[128:131], v[132:135]\n"
"ds_read_b128 a[128:131], v4 offset:8192\n"
"v_accvgpr_read_b32 v18, a218\n"
"v_mfma_f32_16x16x32_bf16 v[136:139], a[88:91], a[136:139], v[136:139]\n"
"v_accvgpr_read_b32 v19, a219\n"
"v_lshlrev_b32 v12, 16, v16\n"
"v_mfma_f32_16x16x32_bf16 v[136:139], a[92:95], a[140:143], v[136:139]\n"
"v_and_b32 v13, s63, v16\n"
"v_lshlrev_b32 v14, 16, v17\n"
"v_mfma_f32_16x16x32_bf16 v[108:111], a[84:87], a[140:143], v[108:111]\n"
"ds_read_b128 a[140:143], v4 offset:11264\n"
"v_and_b32 v15, s63, v17\n"
"v_mfma_f32_16x16x32_bf16 v[108:111], a[80:83], a[136:139], v[108:111]\n"
"ds_read_b128 a[136:139], v4 offset:10240\n"
"v_mul_f32 v12, v12, v156\n"
"v_mfma_f32_16x16x32_bf16 v[112:115], a[80:83], a[144:147], v[112:115]\n"
"v_mul_f32 v13, v13, v157\n"
"v_mul_f32 v14, v14, v158\n"
"v_mfma_f32_16x16x32_bf16 v[112:115], a[84:87], a[148:151], v[112:115]\n"
"global_load_dwordx4 a[76:79], v0, s[34:35] offset:-1024 sc1\n"
"v_mul_f32 v15, v15, v159\n"
"v_mfma_f32_16x16x32_bf16 v[140:143], a[92:95], a[148:151], v[140:143]\n"
"ds_read_b128 a[148:151], v4 offset:13312\n"
"v_cvt_pk_bf16_f32 v24, v12, v13\n"
"v_mfma_f32_16x16x32_bf16 v[140:143], a[88:91], a[144:147], v[140:143]\n"
"ds_read_b128 a[144:147], v4 offset:12288\n"
"v_cvt_pk_bf16_f32 v25, v14, v15\n"
"v_mfma_f32_16x16x32_bf16 a[12:15], a[88:91], a[152:155], a[12:15]\n"
"ds_write_b64 v2, v[24:25] offset:22528\n"
"v_mfma_f32_16x16x32_bf16 a[12:15], a[92:95], a[156:159], a[12:15]\n"
"v_lshlrev_b32 v12, 16, v18\n"
"v_and_b32 v13, s63, v18\n"
"v_mfma_f32_16x16x32_bf16 a[8:11], a[84:87], a[156:159], a[8:11]\n"
"ds_read_b128 a[156:159], v4 offset:15360\n"
"v_lshlrev_b32 v14, 16, v19\n"
"v_mfma_f32_16x16x32_bf16 a[8:11], a[80:83], a[152:155], a[8:11]\n"
"s_waitcnt vmcnt(9) lgkmcnt(13)\n"
"ds_read_b128 a[152:155], v4 offset:14336\n"
"v_and_b32 v15, s63, v19\n"
"v_mfma_f32_16x16x32_bf16 v[32:35], a[32:35], a[96:99], v[32:35]\n"
"v_mul_f32 v12, v12, v184\n"
"v_mul_f32 v13, v13, v185\n"
"v_mfma_f32_16x16x32_bf16 v[32:35], a[36:39], a[100:103], v[32:35]\n"
"global_load_dwordx4 a[80:83], v0, s[34:35] offset:0 sc1\n"
"v_mul_f32 v14, v14, v186\n"
"v_mfma_f32_16x16x32_bf16 v[60:63], a[44:47], a[100:103], v[60:63]\n"
"s_add_u32 s65, s65, 1\n"
"v_mul_f32 v15, v15, v187\n"
"v_cvt_pk_bf16_f32 v26, v12, v13\n"
"v_mfma_f32_16x16x32_bf16 v[60:63], a[40:43], a[96:99], v[60:63]\n"
"v_cvt_pk_bf16_f32 v27, v14, v15\n"
"v_mfma_f32_16x16x32_bf16 v[64:67], a[40:43], a[104:107], v[64:67]\n"
"ds_write_b64 v2, v[26:27] offset:22536\n"
"v_mfma_f32_16x16x32_bf16 v[64:67], a[44:47], a[108:111], v[64:67]\n"
"v_accvgpr_read_b32 v16, a220\n"
"v_accvgpr_read_b32 v17, a221\n"
"v_mfma_f32_16x16x32_bf16 v[36:39], a[36:39], a[108:111], v[36:39]\n"
"v_accvgpr_read_b32 v18, a222\n"
"v_accvgpr_read_b32 v19, a223\n"
"v_mfma_f32_16x16x32_bf16 v[36:39], a[32:35], a[104:107], v[36:39]\n"
"s_waitcnt lgkmcnt(11)\n"
"v_lshlrev_b32 v12, 16, v16\n"
"v_and_b32 v13, s63, v16\n"
"v_mfma_f32_16x16x32_bf16 v[40:43], a[32:35], a[112:115], v[40:43]\n"
"v_lshlrev_b32 v14, 16, v17\n"
"v_and_b32 v15, s63, v17\n"
"v_mfma_f32_16x16x32_bf16 v[40:43], a[36:39], a[116:119], v[40:43]\n"
"global_load_dwordx4 a[84:87], v0, s[34:35] offset:1024 sc1\n"
"v_mul_f32 v12, v12, v212\n"
"v_mfma_f32_16x16x32_bf16 v[68:71], a[44:47], a[116:119], v[68:71]\n"
"v_mul_f32 v13, v13, v213\n"
"v_mul_f32 v14, v14, v214\n"
"v_mfma_f32_16x16x32_bf16 v[68:71], a[40:43], a[112:115], v[68:71]\n"
"v_mul_f32 v15, v15, v215\n"
"v_cvt_pk_bf16_f32 v28, v12, v13\n"
"v_mfma_f32_16x16x32_bf16 v[72:75], a[40:43], a[120:123], v[72:75]\n"
"v_cvt_pk_bf16_f32 v29, v14, v15\n"
"v_mfma_f32_16x16x32_bf16 v[72:75], a[44:47], a[124:127], v[72:75]\n"
"ds_write_b64 v2, v[28:29] offset:23552\n"
"v_mfma_f32_16x16x32_bf16 v[44:47], a[36:39], a[124:127], v[44:47]\n"
"v_lshlrev_b32 v12, 16, v18\n"
"v_and_b32 v13, s63, v18\n"
"v_mfma_f32_16x16x32_bf16 v[44:47], a[32:35], a[120:123], v[44:47]\n"
"s_waitcnt lgkmcnt(7)\n"
"v_lshlrev_b32 v14, 16, v19\n"
"v_and_b32 v15, s63, v19\n"
"v_mfma_f32_16x16x32_bf16 v[48:51], a[32:35], a[128:131], v[48:51]\n"
"v_mul_f32 v12, v12, v240\n"
"v_mul_f32 v13, v13, v241\n"
"v_mfma_f32_16x16x32_bf16 v[48:51], a[36:39], a[132:135], v[48:51]\n"
"global_load_dwordx4 a[88:91], v0, s[34:35] offset:2048 sc1\n"
"v_mul_f32 v14, v14, v242\n"
"v_mfma_f32_16x16x32_bf16 v[76:79], a[44:47], a[132:135], v[76:79]\n"
"v_mul_f32 v15, v15, v243\n"
"v_mfma_f32_16x16x32_bf16 v[76:79], a[40:43], a[128:131], v[76:79]\n"
"v_cvt_pk_bf16_f32 v30, v12, v13\n"
"v_cvt_pk_bf16_f32 v31, v14, v15\n"
"v_mfma_f32_16x16x32_bf16 v[80:83], a[40:43], a[136:139], v[80:83]\n"
"ds_write_b64 v2, v[30:31] offset:23560\n"
"v_mfma_f32_16x16x32_bf16 v[80:83], a[44:47], a[140:143], v[80:83]\n"
"v_accvgpr_read_b32 v16, a224\n"
"v_accvgpr_read_b32 v17, a225\n"
"v_mfma_f32_16x16x32_bf16 v[52:55], a[36:39], a[140:143], v[52:55]\n"
"v_accvgpr_read_b32 v18, a226\n"
"v_mfma_f32_16x16x32_bf16 v[52:55], a[32:35], a[136:139], v[52:55]\n"
"s_waitcnt lgkmcnt(3)\n"
"v_accvgpr_read_b32 v19, a227\n"
"v_mfma_f32_16x16x32_bf16 v[56:59], a[32:35], a[144:147], v[56:59]\n"
"v_lshlrev_b32 v12, 16, v16\n"
"v_and_b32 v13, s63, v16\n"
"v_mfma_f32_16x16x32_bf16 v[56:59], a[36:39], a[148:151], v[56:59]\n"
"global_load_dwordx4 a[92:95], v0, s[34:35] offset:3072 sc1\n"
"v_lshlrev_b32 v14, 16, v17\n"
"v_mfma_f32_16x16x32_bf16 v[84:87], a[44:47], a[148:151], v[84:87]\n"
"v_and_b32 v15, s63, v17\n"
"v_mul_f32 v12, v12, v160\n"
"v_mfma_f32_16x16x32_bf16 v[84:87], a[40:43], a[144:147], v[84:87]\n"
"v_mul_f32 v13, v13, v161\n"
"v_mfma_f32_16x16x32_bf16 a[4:7], a[40:43], a[152:155], a[4:7]\n"
"s_add_u32 s34, s34, 8192\n"
"v_mul_f32 v14, v14, v162\n"
"v_mfma_f32_16x16x32_bf16 a[4:7], a[44:47], a[156:159], a[4:7]\n"
"v_mul_f32 v15, v15, v163\n"
"v_cvt_pk_bf16_f32 v24, v12, v13\n"
"v_mfma_f32_16x16x32_bf16 a[0:3], a[36:39], a[156:159], a[0:3]\n"
"v_cvt_pk_bf16_f32 v25, v14, v15\n"
"v_mfma_f32_16x16x32_bf16 a[0:3], a[32:35], a[152:155], a[0:3]\n"
"s_waitcnt vmcnt(8)\n"
"ds_write_b64 v2, v[24:25] offset:24576\n"
"v_mfma_f32_16x16x32_bf16 v[88:91], a[48:51], a[96:99], v[88:91]\n"
"v_lshlrev_b32 v12, 16, v18\n"
"v_and_b32 v13, s63, v18\n"
"v_mfma_f32_16x16x32_bf16 v[88:91], a[52:55], a[100:103], v[88:91]\n"
"global_load_dwordx4 a[32:35], v0, s[34:35] offset:-4096 sc1\n"
"v_lshlrev_b32 v14, 16, v19\n"
"v_mfma_f32_16x16x32_bf16 v[116:119], a[60:63], a[100:103], v[116:119]\n"
"ds_read_b128 a[100:103], v5 offset:1024\n"
"v_and_b32 v15, s63, v19\n"
"v_mfma_f32_16x16x32_bf16 v[116:119], a[56:59], a[96:99], v[116:119]\n"
"ds_read_b128 a[96:99], v5 offset:0\n"
"v_mul_f32 v12, v12, v188\n"
"v_mfma_f32_16x16x32_bf16 v[120:123], a[56:59], a[104:107], v[120:123]\n"
"v_mul_f32 v13, v13, v189\n"
"v_mul_f32 v14, v14, v190\n"
"v_mfma_f32_16x16x32_bf16 v[120:123], a[60:63], a[108:111], v[120:123]\n"
"v_mul_f32 v15, v15, v191\n"
"v_cvt_pk_bf16_f32 v26, v12, v13\n"
"v_mfma_f32_16x16x32_bf16 v[92:95], a[52:55], a[108:111], v[92:95]\n"
"ds_read_b128 a[108:111], v5 offset:3072\n"
"v_cvt_pk_bf16_f32 v27, v14, v15\n"
"v_mfma_f32_16x16x32_bf16 v[92:95], a[48:51], a[104:107], v[92:95]\n"
"global_store_dwordx4 v0, v[24:27], s[54:55] offset:-4096 sc1\n"
"ds_read_b128 a[104:107], v5 offset:2048\n"
"ds_write_b64 v2, v[26:27] offset:24584\n"
"v_mfma_f32_16x16x32_bf16 v[96:99], a[48:51], a[112:115], v[96:99]\n"
"v_accvgpr_read_b32 v16, a228\n"
"v_accvgpr_read_b32 v17, a229\n"
"v_mfma_f32_16x16x32_bf16 v[96:99], a[52:55], a[116:119], v[96:99]\n"
"global_load_dwordx4 a[36:39], v0, s[34:35] offset:-3072 sc1\n"
"v_accvgpr_read_b32 v18, a230\n"
"v_mfma_f32_16x16x32_bf16 v[124:127], a[60:63], a[116:119], v[124:127]\n"
"ds_read_b128 a[116:119], v5 offset:5120\n"
"v_accvgpr_read_b32 v19, a231\n"
"v_mfma_f32_16x16x32_bf16 v[124:127], a[56:59], a[112:115], v[124:127]\n"
"ds_read_b128 a[112:115], v5 offset:4096\n"
"v_lshlrev_b32 v12, 16, v16\n"
"v_mfma_f32_16x16x32_bf16 v[128:131], a[56:59], a[120:123], v[128:131]\n"
"v_and_b32 v13, s63, v16\n"
"v_lshlrev_b32 v14, 16, v17\n"
"v_mfma_f32_16x16x32_bf16 v[128:131], a[60:63], a[124:127], v[128:131]\n"
"v_and_b32 v15, s63, v17\n"
"v_mfma_f32_16x16x32_bf16 v[100:103], a[52:55], a[124:127], v[100:103]\n"
"ds_read_b128 a[124:127], v5 offset:7168\n"
"v_mul_f32 v12, v12, v216\n"
"v_mfma_f32_16x16x32_bf16 v[100:103], a[48:51], a[120:123], v[100:103]\n"
"ds_read_b128 a[120:123], v5 offset:6144\n"
"v_mul_f32 v13, v13, v217\n"
"v_mfma_f32_16x16x32_bf16 v[104:107], a[48:51], a[128:131], v[104:107]\n"
"v_mul_f32 v14, v14, v218\n"
"v_mul_f32 v15, v15, v219\n"
"v_mfma_f32_16x16x32_bf16 v[104:107], a[52:55], a[132:135], v[104:107]\n"
"global_load_dwordx4 a[40:43], v0, s[34:35] offset:-2048 sc1\n"
"v_cvt_pk_bf16_f32 v28, v12, v13\n"
"v_mfma_f32_16x16x32_bf16 v[132:135], a[60:63], a[132:135], v[132:135]\n"
"ds_read_b128 a[132:135], v5 offset:9216\n"
"v_cvt_pk_bf16_f32 v29, v14, v15\n"
"v_mfma_f32_16x16x32_bf16 v[132:135], a[56:59], a[128:131], v[132:135]\n"
"ds_read_b128 a[128:131], v5 offset:8192\n"
"v_mfma_f32_16x16x32_bf16 v[136:139], a[56:59], a[136:139], v[136:139]\n"
"ds_write_b64 v2, v[28:29] offset:25600\n"
"v_mfma_f32_16x16x32_bf16 v[136:139], a[60:63], a[140:143], v[136:139]\n"
"v_lshlrev_b32 v12, 16, v18\n"
"v_and_b32 v13, s63, v18\n"
"v_mfma_f32_16x16x32_bf16 v[108:111], a[52:55], a[140:143], v[108:111]\n"
"ds_read_b128 a[140:143], v5 offset:11264\n"
"v_lshlrev_b32 v14, 16, v19\n"
"v_mfma_f32_16x16x32_bf16 v[108:111], a[48:51], a[136:139], v[108:111]\n"
"ds_read_b128 a[136:139], v5 offset:10240\n"
"v_and_b32 v15, s63, v19\n"
"v_mfma_f32_16x16x32_bf16 v[112:115], a[48:51], a[144:147], v[112:115]\n"
"v_mul_f32 v12, v12, v244\n"
"v_mul_f32 v13, v13, v245\n"
"v_mfma_f32_16x16x32_bf16 v[112:115], a[52:55], a[148:151], v[112:115]\n"
"global_load_dwordx4 a[44:47], v0, s[34:35] offset:-1024 sc1\n"
"v_mul_f32 v14, v14, v246\n"
"v_mfma_f32_16x16x32_bf16 v[140:143], a[60:63], a[148:151], v[140:143]\n"
"ds_read_b128 a[148:151], v5 offset:13312\n"
"v_mul_f32 v15, v15, v247\n"
"v_mfma_f32_16x16x32_bf16 v[140:143], a[56:59], a[144:147], v[140:143]\n"
"ds_read_b128 a[144:147], v5 offset:12288\n"
"v_cvt_pk_bf16_f32 v30, v12, v13\n"
"v_mfma_f32_16x16x32_bf16 a[12:15], a[56:59], a[152:155], a[12:15]\n"
"v_cvt_pk_bf16_f32 v31, v14, v15\n"
"v_mfma_f32_16x16x32_bf16 a[12:15], a[60:63], a[156:159], a[12:15]\n"
"global_store_dwordx4 v0, v[28:31], s[54:55] offset:-3072 sc1\n"
"ds_write_b64 v2, v[30:31] offset:25608\n"
"v_accvgpr_read_b32 v16, a232\n"
"v_mfma_f32_16x16x32_bf16 a[8:11], a[52:55], a[156:159], a[8:11]\n"
"ds_read_b128 a[156:159], v5 offset:15360\n"
"v_accvgpr_read_b32 v17, a233\n"
"v_mfma_f32_16x16x32_bf16 a[8:11], a[48:51], a[152:155], a[8:11]\n"
"s_waitcnt vmcnt(10) lgkmcnt(14)\n"
"ds_read_b128 a[152:155], v5 offset:14336\n"
"v_accvgpr_read_b32 v18, a234\n"
"v_mfma_f32_16x16x32_bf16 v[32:35], a[64:67], a[96:99], v[32:35]\n"
"v_accvgpr_read_b32 v19, a235\n"
"v_lshlrev_b32 v12, 16, v16\n"
"v_mfma_f32_16x16x32_bf16 v[32:35], a[68:71], a[100:103], v[32:35]\n"
"global_load_dwordx4 a[48:51], v0, s[34:35] offset:0 sc1\n"
"v_and_b32 v13, s63, v16\n"
"v_mfma_f32_16x16x32_bf16 v[60:63], a[76:79], a[100:103], v[60:63]\n"
"v_lshlrev_b32 v14, 16, v17\n"
"v_and_b32 v15, s63, v17\n"
"v_mfma_f32_16x16x32_bf16 v[60:63], a[72:75], a[96:99], v[60:63]\n"
"v_mul_f32 v12, v12, v164\n"
"v_mul_f32 v13, v13, v165\n"
"v_mfma_f32_16x16x32_bf16 v[64:67], a[72:75], a[104:107], v[64:67]\n"
"v_mul_f32 v14, v14, v166\n"
"v_mul_f32 v15, v15, v167\n"
"v_mfma_f32_16x16x32_bf16 v[64:67], a[76:79], a[108:111], v[64:67]\n"
"v_cvt_pk_bf16_f32 v24, v12, v13\n"
"v_cvt_pk_bf16_f32 v25, v14, v15\n"
"v_mfma_f32_16x16x32_bf16 v[36:39], a[68:71], a[108:111], v[36:39]\n"
"ds_write_b64 v2, v[24:25] offset:26624\n"
"v_mfma_f32_16x16x32_bf16 v[36:39], a[64:67], a[104:107], v[36:39]\n"
"s_waitcnt lgkmcnt(11)\n"
"v_lshlrev_b32 v12, 16, v18\n"
"v_and_b32 v13, s63, v18\n"
"v_mfma_f32_16x16x32_bf16 v[40:43], a[64:67], a[112:115], v[40:43]\n"
"v_lshlrev_b32 v14, 16, v19\n"
"v_and_b32 v15, s63, v19\n"
"v_mfma_f32_16x16x32_bf16 v[40:43], a[68:71], a[116:119], v[40:43]\n"
"global_load_dwordx4 a[52:55], v0, s[34:35] offset:1024 sc1\n"
"v_mul_f32 v12, v12, v192\n"
"v_mfma_f32_16x16x32_bf16 v[68:71], a[76:79], a[116:119], v[68:71]\n"
"v_mul_f32 v13, v13, v193\n"
"v_mul_f32 v14, v14, v194\n"
"v_mfma_f32_16x16x32_bf16 v[68:71], a[72:75], a[112:115], v[68:71]\n"
"v_mul_f32 v15, v15, v195\n"
"v_mfma_f32_16x16x32_bf16 v[72:75], a[72:75], a[120:123], v[72:75]\n"
"v_cvt_pk_bf16_f32 v26, v12, v13\n"
"v_mfma_f32_16x16x32_bf16 v[72:75], a[76:79], a[124:127], v[72:75]\n"
"v_cvt_pk_bf16_f32 v27, v14, v15\n"
"v_mfma_f32_16x16x32_bf16 v[44:47], a[68:71], a[124:127], v[44:47]\n"
"global_store_dwordx4 v0, v[24:27], s[54:55] offset:-2048 sc1\n"
"ds_write_b64 v2, v[26:27] offset:26632\n"
"v_accvgpr_read_b32 v16, a236\n"
"v_mfma_f32_16x16x32_bf16 v[44:47], a[64:67], a[120:123], v[44:47]\n"
"s_waitcnt lgkmcnt(7)\n"
"v_accvgpr_read_b32 v17, a237\n"
"v_accvgpr_read_b32 v18, a238\n"
"v_mfma_f32_16x16x32_bf16 v[48:51], a[64:67], a[128:131], v[48:51]\n"
"v_accvgpr_read_b32 v19, a239\n"
"v_lshlrev_b32 v12, 16, v16\n"
"v_mfma_f32_16x16x32_bf16 v[48:51], a[68:71], a[132:135], v[48:51]\n"
"global_load_dwordx4 a[56:59], v0, s[34:35] offset:2048 sc1\n"
"v_and_b32 v13, s63, v16\n"
"v_mfma_f32_16x16x32_bf16 v[76:79], a[76:79], a[132:135], v[76:79]\n"
"v_lshlrev_b32 v14, 16, v17\n"
"v_and_b32 v15, s63, v17\n"
"v_mfma_f32_16x16x32_bf16 v[76:79], a[72:75], a[128:131], v[76:79]\n"
"v_mul_f32 v12, v12, v220\n"
"v_mul_f32 v13, v13, v221\n"
"v_mfma_f32_16x16x32_bf16 v[80:83], a[72:75], a[136:139], v[80:83]\n"
"v_mul_f32 v14, v14, v222\n"
"v_mul_f32 v15, v15, v223\n"
"v_mfma_f32_16x16x32_bf16 v[80:83], a[76:79], a[140:143], v[80:83]\n"
"v_cvt_pk_bf16_f32 v28, v12, v13\n"
"v_cvt_pk_bf16_f32 v29, v14, v15\n"
"v_mfma_f32_16x16x32_bf16 v[52:55], a[68:71], a[140:143], v[52:55]\n"
"ds_write_b64 v2, v[28:29] offset:27648\n"
"v_mfma_f32_16x16x32_bf16 v[52:55], a[64:67], a[136:139], v[52:55]\n"
"s_waitcnt lgkmcnt(3)\n"
"v_mov_b32 v8, s65\n"
"v_mfma_f32_16x16x32_bf16 v[56:59], a[64:67], a[144:147], v[56:59]\n"
"v_mov_b32 v9, s40\n"
"v_mfma_f32_16x16x32_bf16 v[56:59], a[68:71], a[148:151], v[56:59]\n"
"global_load_dwordx4 a[60:63], v0, s[34:35] offset:3072 sc1\n"
"v_lshlrev_b32 v12, 16, v18\n"
"v_mfma_f32_16x16x32_bf16 v[84:87], a[76:79], a[148:151], v[84:87]\n"
"buffer_store_dwordx2 v[8:9], off, s[56:59], %13 sc1\n"
"v_and_b32 v13, s63, v18\n"
"v_mfma_f32_16x16x32_bf16 v[84:87], a[72:75], a[144:147], v[84:87]\n"
"v_lshlrev_b32 v14, 16, v19\n"
"v_mfma_f32_16x16x32_bf16 a[4:7], a[72:75], a[152:155], a[4:7]\n"
"s_add_u32 s34, s34, 8192\n"
"v_and_b32 v15, s63, v19\n"
"v_mul_f32 v12, v12, v248\n"
"v_mfma_f32_16x16x32_bf16 a[4:7], a[76:79], a[156:159], a[4:7]\n"
"v_mul_f32 v13, v13, v249\n"
"v_mfma_f32_16x16x32_bf16 a[0:3], a[68:71], a[156:159], a[0:3]\n"
"v_mul_f32 v14, v14, v250\n"
"v_mul_f32 v15, v15, v251\n"
"v_mfma_f32_16x16x32_bf16 a[0:3], a[64:67], a[152:155], a[0:3]\n"
"s_waitcnt vmcnt(12)\n"
"v_cvt_pk_bf16_f32 v30, v12, v13\n"
"v_cvt_pk_bf16_f32 v31, v14, v15\n"
"v_mfma_f32_16x16x32_bf16 v[88:91], a[80:83], a[96:99], v[88:91]\n"
"global_store_dwordx4 v0, v[28:31], s[54:55] offset:-1024 sc1\n"
"ds_write_b64 v2, v[30:31] offset:27656\n"
"v_mfma_f32_16x16x32_bf16 v[88:91], a[84:87], a[100:103], v[88:91]\n"
"global_load_dwordx4 a[64:67], v0, s[34:35] offset:-4096 sc1\n"
"v_accvgpr_read_b32 v16, a240\n"
"v_mfma_f32_16x16x32_bf16 v[116:119], a[92:95], a[100:103], v[116:119]\n"
"ds_read_b128 a[100:103], v2 offset:1024\n"
"v_accvgpr_read_b32 v17, a241\n"
"v_mfma_f32_16x16x32_bf16 v[116:119], a[88:91], a[96:99], v[116:119]\n"
"ds_read_b128 a[96:99], v2 offset:0\n"
"v_accvgpr_read_b32 v18, a242\n"
"v_mfma_f32_16x16x32_bf16 v[120:123], a[88:91], a[104:107], v[120:123]\n"
"v_accvgpr_read_b32 v19, a243\n"
"v_lshlrev_b32 v12, 16, v16\n"
"v_mfma_f32_16x16x32_bf16 v[120:123], a[92:95], a[108:111], v[120:123]\n"
"v_and_b32 v13, s63, v16\n"
"v_lshlrev_b32 v14, 16, v17\n"
"v_mfma_f32_16x16x32_bf16 v[92:95], a[84:87], a[108:111], v[92:95]\n"
"ds_read_b128 a[108:111], v2 offset:3072\n"
"v_and_b32 v15, s63, v17\n"
"v_mfma_f32_16x16x32_bf16 v[92:95], a[80:83], a[104:107], v[92:95]\n"
"ds_read_b128 a[104:107], v2 offset:2048\n"
"v_mul_f32 v12, v12, v168\n"
"v_mfma_f32_16x16x32_bf16 v[96:99], a[80:83], a[112:115], v[96:99]\n"
"v_mul_f32 v13, v13, v169\n"
"v_mul_f32 v14, v14, v170\n"
"v_mfma_f32_16x16x32_bf16 v[96:99], a[84:87], a[116:119], v[96:99]\n"
"global_load_dwordx4 a[68:71], v0, s[34:35] offset:-3072 sc1\n"
"v_mul_f32 v15, v15, v171\n"
"v_mfma_f32_16x16x32_bf16 v[124:127], a[92:95], a[116:119], v[124:127]\n"
"ds_read_b128 a[116:119], v2 offset:5120\n"
"v_cvt_pk_bf16_f32 v24, v12, v13\n"
"v_mfma_f32_16x16x32_bf16 v[124:127], a[88:91], a[112:115], v[124:127]\n"
"ds_read_b128 a[112:115], v2 offset:4096\n"
"v_cvt_pk_bf16_f32 v25, v14, v15\n"
"v_mfma_f32_16x16x32_bf16 v[128:131], a[88:91], a[120:123], v[128:131]\n"
"ds_write_b64 v2, v[24:25] offset:28672\n"
"v_mfma_f32_16x16x32_bf16 v[128:131], a[92:95], a[124:127], v[128:131]\n"
"v_lshlrev_b32 v12, 16, v18\n"
"v_and_b32 v13, s63, v18\n"
"v_mfma_f32_16x16x32_bf16 v[100:103], a[84:87], a[124:127], v[100:103]\n"
"ds_read_b128 a[124:127], v2 offset:7168\n"
"v_lshlrev_b32 v14, 16, v19\n"
"v_mfma_f32_16x16x32_bf16 v[100:103], a[80:83], a[120:123], v[100:103]\n"
"ds_read_b128 a[120:123], v2 offset:6144\n"
"v_and_b32 v15, s63, v19\n"
"v_mfma_f32_16x16x32_bf16 v[104:107], a[80:83], a[128:131], v[104:107]\n"
"v_mul_f32 v12, v12, v196\n"
"v_mul_f32 v13, v13, v197\n"
"v_mfma_f32_16x16x32_bf16 v[104:107], a[84:87], a[132:135], v[104:107]\n"
"global_load_dwordx4 a[72:75], v0, s[34:35] offset:-2048 sc1\n"
"v_mul_f32 v14, v14, v198\n"
"v_mfma_f32_16x16x32_bf16 v[132:135], a[92:95], a[132:135], v[132:135]\n"
"ds_read_b128 a[132:135], v2 offset:9216\n"
"v_mul_f32 v15, v15, v199\n"
"v_mfma_f32_16x16x32_bf16 v[132:135], a[88:91], a[128:131], v[132:135]\n"
"ds_read_b128 a[128:131], v2 offset:8192\n"
"v_cvt_pk_bf16_f32 v26, v12, v13\n"
"v_mfma_f32_16x16x32_bf16 v[136:139], a[88:91], a[136:139], v[136:139]\n"
"v_cvt_pk_bf16_f32 v27, v14, v15\n"
"v_mfma_f32_16x16x32_bf16 v[136:139], a[92:95], a[140:143], v[136:139]\n"
"global_store_dwordx4 v0, v[24:27], s[54:55] offset:0 sc1\n"
"ds_write_b64 v2, v[26:27] offset:28680\n"
"v_accvgpr_read_b32 v16, a248\n"
"v_mfma_f32_16x16x32_bf16 v[108:111], a[84:87], a[140:143], v[108:111]\n"
"ds_read_b128 a[140:143], v2 offset:11264\n"
"v_accvgpr_read_b32 v17, a249\n"
"v_mfma_f32_16x16x32_bf16 v[108:111], a[80:83], a[136:139], v[108:111]\n"
"ds_read_b128 a[136:139], v2 offset:10240\n"
"v_accvgpr_read_b32 v18, a250\n"
"v_mfma_f32_16x16x32_bf16 v[112:115], a[80:83], a[144:147], v[112:115]\n"
"v_accvgpr_read_b32 v19, a251\n"
"v_lshlrev_b32 v12, 16, v16\n"
"v_mfma_f32_16x16x32_bf16 v[112:115], a[84:87], a[148:151], v[112:115]\n"
"global_load_dwordx4 a[76:79], v0, s[34:35] offset:-1024 sc1\n"
"v_and_b32 v13, s63, v16\n"
"v_mfma_f32_16x16x32_bf16 v[140:143], a[92:95], a[148:151], v[140:143]\n"
"ds_read_b128 a[148:151], v2 offset:13312\n"
"v_lshlrev_b32 v14, 16, v17\n"
"v_mfma_f32_16x16x32_bf16 v[140:143], a[88:91], a[144:147], v[140:143]\n"
"ds_read_b128 a[144:147], v2 offset:12288\n"
"v_and_b32 v15, s63, v17\n"
"v_mfma_f32_16x16x32_bf16 a[12:15], a[88:91], a[152:155], a[12:15]\n"
"v_accvgpr_read_b32 v10, a16\n"
"v_accvgpr_read_b32 v11, a17\n"
"v_mfma_f32_16x16x32_bf16 a[12:15], a[92:95], a[156:159], a[12:15]\n"
"v_mul_f32 v12, v12, v10\n"
"v_mul_f32 v13, v13, v11\n"
"v_mfma_f32_16x16x32_bf16 a[8:11], a[84:87], a[156:159], a[8:11]\n"
"ds_read_b128 a[156:159], v2 offset:15360\n"
"v_accvgpr_read_b32 v10, a18\n"
"v_mfma_f32_16x16x32_bf16 a[8:11], a[80:83], a[152:155], a[8:11]\n"
"s_waitcnt vmcnt(13) lgkmcnt(13)\n"
"ds_read_b128 a[152:155], v2 offset:14336\n"
"v_accvgpr_read_b32 v11, a19\n"
"v_mfma_f32_16x16x32_bf16 v[144:147], a[32:35], a[96:99], v[144:147]\n"
"v_mul_f32 v14, v14, v10\n"
"v_mul_f32 v15, v15, v11\n"
"v_mfma_f32_16x16x32_bf16 v[144:147], a[36:39], a[100:103], v[144:147]\n"
"global_load_dwordx4 a[80:83], v0, s[34:35] offset:0 sc1\n"
"v_cvt_pk_bf16_f32 v28, v12, v13\n"
"v_mfma_f32_16x16x32_bf16 v[172:175], a[44:47], a[100:103], v[172:175]\n"
"v_cvt_pk_bf16_f32 v29, v14, v15\n"
"v_mfma_f32_16x16x32_bf16 v[172:175], a[40:43], a[96:99], v[172:175]\n"
"ds_write_b64 v2, v[28:29] offset:30720\n"
"v_mfma_f32_16x16x32_bf16 v[176:179], a[40:43], a[104:107], v[176:179]\n"
"v_lshlrev_b32 v12, 16, v18\n"
"v_and_b32 v13, s63, v18\n"
"v_mfma_f32_16x16x32_bf16 v[176:179], a[44:47], a[108:111], v[176:179]\n"
"v_lshlrev_b32 v14, 16, v19\n"
"v_and_b32 v15, s63, v19\n"
"v_mfma_f32_16x16x32_bf16 v[148:151], a[36:39], a[108:111], v[148:151]\n"
"v_accvgpr_read_b32 v10, a20\n"
"v_mfma_f32_16x16x32_bf16 v[148:151], a[32:35], a[104:107], v[148:151]\n"
"s_waitcnt lgkmcnt(10)\n"
"v_accvgpr_read_b32 v11, a21\n"
"v_mul_f32 v12, v12, v10\n"
"v_mfma_f32_16x16x32_bf16 v[152:155], a[32:35], a[112:115], v[152:155]\n"
"v_mul_f32 v13, v13, v11\n"
"v_mfma_f32_16x16x32_bf16 v[152:155], a[36:39], a[116:119], v[152:155]\n"
"global_load_dwordx4 a[84:87], v0, s[34:35] offset:1024 sc1\n"
"v_accvgpr_read_b32 v10, a22\n"
"v_mfma_f32_16x16x32_bf16 v[180:183], a[44:47], a[116:119], v[180:183]\n"
"v_accvgpr_read_b32 v11, a23\n"
"v_mul_f32 v14, v14, v10\n"
"v_mfma_f32_16x16x32_bf16 v[180:183], a[40:43], a[112:115], v[180:183]\n"
"v_mul_f32 v15, v15, v11\n"
"v_mfma_f32_16x16x32_bf16 v[184:187], a[40:43], a[120:123], v[184:187]\n"
"v_cvt_pk_bf16_f32 v30, v12, v13\n"
"v_cvt_pk_bf16_f32 v31, v14, v15\n"
"v_mfma_f32_16x16x32_bf16 v[184:187], a[44:47], a[124:127], v[184:187]\n"
"global_store_dwordx4 v0, v[28:31], s[54:55] offset:2048 sc1\n"
"ds_write_b64 v2, v[30:31] offset:30728\n"
"v_mfma_f32_16x16x32_bf16 v[156:159], a[36:39], a[124:127], v[156:159]\n"
"v_accvgpr_read_b32 v16, a244\n"
"v_mfma_f32_16x16x32_bf16 v[156:159], a[32:35], a[120:123], v[156:159]\n"
"s_waitcnt lgkmcnt(6)\n"
"v_accvgpr_read_b32 v17, a245\n"
"v_mfma_f32_16x16x32_bf16 v[160:163], a[32:35], a[128:131], v[160:163]\n"
"v_accvgpr_read_b32 v18, a246\n"
"v_accvgpr_read_b32 v19, a247\n"
"v_mfma_f32_16x16x32_bf16 v[160:163], a[36:39], a[132:135], v[160:163]\n"
"global_load_dwordx4 a[88:91], v0, s[34:35] offset:2048 sc1\n"
"v_lshlrev_b32 v12, 16, v16\n"
"v_mfma_f32_16x16x32_bf16 v[188:191], a[44:47], a[132:135], v[188:191]\n"
"v_and_b32 v13, s63, v16\n"
"v_mfma_f32_16x16x32_bf16 v[188:191], a[40:43], a[128:131], v[188:191]\n"
"v_lshlrev_b32 v14, 16, v17\n"
"v_and_b32 v15, s63, v17\n"
"v_mfma_f32_16x16x32_bf16 v[192:195], a[40:43], a[136:139], v[192:195]\n"
"v_mul_f32 v12, v12, v224\n"
"v_mfma_f32_16x16x32_bf16 v[192:195], a[44:47], a[140:143], v[192:195]\n"
"v_mul_f32 v13, v13, v225\n"
"v_mfma_f32_16x16x32_bf16 v[164:167], a[36:39], a[140:143], v[164:167]\n"
"v_mul_f32 v14, v14, v226\n"
"v_mul_f32 v15, v15, v227\n"
"v_mfma_f32_16x16x32_bf16 v[164:167], a[32:35], a[136:139], v[164:167]\n"
"s_waitcnt lgkmcnt(2)\n"
"v_cvt_pk_bf16_f32 v24, v12, v13\n"
"v_cvt_pk_bf16_f32 v25, v14, v15\n"
"v_mfma_f32_16x16x32_bf16 v[168:171], a[32:35], a[144:147], v[168:171]\n"
"ds_write_b64 v2, v[24:25] offset:29696\n"
"v_mfma_f32_16x16x32_bf16 v[168:171], a[36:39], a[148:151], v[168:171]\n"
"global_load_dwordx4 a[92:95], v0, s[34:35] offset:3072 sc1\n"
"v_lshlrev_b32 v12, 16, v18\n"
"v_mfma_f32_16x16x32_bf16 v[196:199], a[44:47], a[148:151], v[196:199]\n"
"v_and_b32 v13, s63, v18\n"
"v_mfma_f32_16x16x32_bf16 v[196:199], a[40:43], a[144:147], v[196:199]\n"
"v_lshlrev_b32 v14, 16, v19\n"
"v_and_b32 v15, s63, v19\n"
"v_mfma_f32_16x16x32_bf16 a[20:23], a[40:43], a[152:155], a[20:23]\n"
"s_add_u32 s34, s34, 8192\n"
"v_mul_f32 v12, v12, v252\n"
"v_mul_f32 v13, v13, v253\n"
"v_mfma_f32_16x16x32_bf16 a[20:23], a[44:47], a[156:159], a[20:23]\n"
"v_mul_f32 v14, v14, v254\n"
"v_mul_f32 v15, v15, v255\n"
"v_mfma_f32_16x16x32_bf16 a[16:19], a[36:39], a[156:159], a[16:19]\n"
"v_cvt_pk_bf16_f32 v26, v12, v13\n"
"v_cvt_pk_bf16_f32 v27, v14, v15\n"
"v_mfma_f32_16x16x32_bf16 a[16:19], a[32:35], a[152:155], a[16:19]\n"
"s_waitcnt vmcnt(12)\n"
"global_store_dwordx4 v0, v[24:27], s[54:55] offset:1024 sc1\n"
"ds_write_b64 v2, v[26:27] offset:29704\n"
"v_mfma_f32_16x16x32_bf16 v[200:203], a[48:51], a[96:99], v[200:203]\n"
"v_accvgpr_read_b32 v16, a252\n"
"v_accvgpr_read_b32 v17, a253\n"
"v_mfma_f32_16x16x32_bf16 v[200:203], a[52:55], a[100:103], v[200:203]\n"
"global_load_dwordx4 a[32:35], v0, s[34:35] offset:-4096 sc1\n"
"v_accvgpr_read_b32 v18, a254\n"
"v_mfma_f32_16x16x32_bf16 v[228:231], a[60:63], a[100:103], v[228:231]\n"
"ds_read_b128 a[100:103], v3 offset:1024\n"
"v_accvgpr_read_b32 v19, a255\n"
"v_mfma_f32_16x16x32_bf16 v[228:231], a[56:59], a[96:99], v[228:231]\n"
"ds_read_b128 a[96:99], v3 offset:0\n"
"v_lshlrev_b32 v12, 16, v16\n"
"v_mfma_f32_16x16x32_bf16 v[232:235], a[56:59], a[104:107], v[232:235]\n"
"v_and_b32 v13, s63, v16\n"
"v_lshlrev_b32 v14, 16, v17\n"
"v_mfma_f32_16x16x32_bf16 v[232:235], a[60:63], a[108:111], v[232:235]\n"
"v_and_b32 v15, s63, v17\n"
"v_accvgpr_read_b32 v10, a24\n"
"v_mfma_f32_16x16x32_bf16 v[204:207], a[52:55], a[108:111], v[204:207]\n"
"ds_read_b128 a[108:111], v3 offset:3072\n"
"v_accvgpr_read_b32 v11, a25\n"
"v_mfma_f32_16x16x32_bf16 v[204:207], a[48:51], a[104:107], v[204:207]\n"
"ds_read_b128 a[104:107], v3 offset:2048\n"
"v_mul_f32 v12, v12, v10\n"
"v_mfma_f32_16x16x32_bf16 v[208:211], a[48:51], a[112:115], v[208:211]\n"
"v_mul_f32 v13, v13, v11\n"
"v_accvgpr_read_b32 v10, a26\n"
"v_mfma_f32_16x16x32_bf16 v[208:211], a[52:55], a[116:119], v[208:211]\n"
"global_load_dwordx4 a[36:39], v0, s[34:35] offset:-3072 sc1\n"
"v_accvgpr_read_b32 v11, a27\n"
"v_mfma_f32_16x16x32_bf16 v[236:239], a[60:63], a[116:119], v[236:239]\n"
"ds_read_b128 a[116:119], v3 offset:5120\n"
"v_mul_f32 v14, v14, v10\n"
"v_mfma_f32_16x16x32_bf16 v[236:239], a[56:59], a[112:115], v[236:239]\n"
"ds_read_b128 a[112:115], v3 offset:4096\n"
"v_mul_f32 v15, v15, v11\n"
"v_mfma_f32_16x16x32_bf16 v[240:243], a[56:59], a[120:123], v[240:243]\n"
"v_cvt_pk_bf16_f32 v28, v12, v13\n"
"v_cvt_pk_bf16_f32 v29, v14, v15\n"
"v_mfma_f32_16x16x32_bf16 v[240:243], a[60:63], a[124:127], v[240:243]\n"
"ds_write_b64 v2, v[28:29] offset:31744\n"
"v_mfma_f32_16x16x32_bf16 v[212:215], a[52:55], a[124:127], v[212:215]\n"
"ds_read_b128 a[124:127], v3 offset:7168\n"
"v_mfma_f32_16x16x32_bf16 v[212:215], a[48:51], a[120:123], v[212:215]\n"
"ds_read_b128 a[120:123], v3 offset:6144\n"
"v_lshlrev_b32 v12, 16, v18\n"
"v_mfma_f32_16x16x32_bf16 v[216:219], a[48:51], a[128:131], v[216:219]\n"
"v_and_b32 v13, s63, v18\n"
"v_lshlrev_b32 v14, 16, v19\n"
"v_mfma_f32_16x16x32_bf16 v[216:219], a[52:55], a[132:135], v[216:219]\n"
"global_load_dwordx4 a[40:43], v0, s[34:35] offset:-2048 sc1\n"
"v_and_b32 v15, s63, v19\n"
"v_mfma_f32_16x16x32_bf16 v[244:247], a[60:63], a[132:135], v[244:247]\n"
"ds_read_b128 a[132:135], v3 offset:9216\n"
"v_accvgpr_read_b32 v10, a28\n"
"v_mfma_f32_16x16x32_bf16 v[244:247], a[56:59], a[128:131], v[244:247]\n"
"ds_read_b128 a[128:131], v3 offset:8192\n"
"v_accvgpr_read_b32 v11, a29\n"
"v_mfma_f32_16x16x32_bf16 v[248:251], a[56:59], a[136:139], v[248:251]\n"
"v_mul_f32 v12, v12, v10\n"
"v_mul_f32 v13, v13, v11\n"
"v_mfma_f32_16x16x32_bf16 v[248:251], a[60:63], a[140:143], v[248:251]\n"
"v_accvgpr_read_b32 v10, a30\n"
"v_mfma_f32_16x16x32_bf16 v[220:223], a[52:55], a[140:143], v[220:223]\n"
"ds_read_b128 a[140:143], v3 offset:11264\n"
"v_accvgpr_read_b32 v11, a31\n"
"v_mfma_f32_16x16x32_bf16 v[220:223], a[48:51], a[136:139], v[220:223]\n"
"ds_read_b128 a[136:139], v3 offset:10240\n"
"v_mul_f32 v14, v14, v10\n"
"v_mfma_f32_16x16x32_bf16 v[224:227], a[48:51], a[144:147], v[224:227]\n"
"v_mul_f32 v15, v15, v11\n"
"v_cvt_pk_bf16_f32 v30, v12, v13\n"
"v_mfma_f32_16x16x32_bf16 v[224:227], a[52:55], a[148:151], v[224:227]\n"
"global_load_dwordx4 a[44:47], v0, s[34:35] offset:-1024 sc1\n"
"v_cvt_pk_bf16_f32 v31, v14, v15\n"
"v_mfma_f32_16x16x32_bf16 v[252:255], a[60:63], a[148:151], v[252:255]\n"
"global_store_dwordx4 v0, v[28:31], s[54:55] offset:3072 sc1\n"
"ds_read_b128 a[148:151], v3 offset:13312\n"
"ds_write_b64 v2, v[30:31] offset:31752\n"
"v_mfma_f32_16x16x32_bf16 v[252:255], a[56:59], a[144:147], v[252:255]\n"
"ds_read_b128 a[144:147], v3 offset:12288\n"
"v_mfma_f32_16x16x32_bf16 a[28:31], a[56:59], a[152:155], a[28:31]\n"
"v_mfma_f32_16x16x32_bf16 a[28:31], a[60:63], a[156:159], a[28:31]\n"
"v_mfma_f32_16x16x32_bf16 a[24:27], a[52:55], a[156:159], a[24:27]\n"
"ds_read_b128 a[156:159], v3 offset:15360\n"
"v_mfma_f32_16x16x32_bf16 a[24:27], a[48:51], a[152:155], a[24:27]\n"
"s_waitcnt vmcnt(11) lgkmcnt(13)\n"
"ds_read_b128 a[152:155], v3 offset:14336\n"
"v_mfma_f32_16x16x32_bf16 v[144:147], a[64:67], a[96:99], v[144:147]\n"
"ds_read_b128 v[24:27], v2 offset:16384\n"
"v_mfma_f32_16x16x32_bf16 v[144:147], a[68:71], a[100:103], v[144:147]\n"
"global_load_dwordx4 a[48:51], v0, s[34:35] offset:0 sc1\n"
"v_mfma_f32_16x16x32_bf16 v[172:175], a[76:79], a[100:103], v[172:175]\n"
"v_mfma_f32_16x16x32_bf16 v[172:175], a[72:75], a[96:99], v[172:175]\n"
"v_mfma_f32_16x16x32_bf16 v[176:179], a[72:75], a[104:107], v[176:179]\n"
"v_mfma_f32_16x16x32_bf16 v[176:179], a[76:79], a[108:111], v[176:179]\n"
"v_mfma_f32_16x16x32_bf16 v[148:151], a[68:71], a[108:111], v[148:151]\n"
"v_mfma_f32_16x16x32_bf16 v[148:151], a[64:67], a[104:107], v[148:151]\n"
"s_waitcnt lgkmcnt(10)\n"
"v_mfma_f32_16x16x32_bf16 v[152:155], a[64:67], a[112:115], v[152:155]\n"
"v_mfma_f32_16x16x32_bf16 v[152:155], a[68:71], a[116:119], v[152:155]\n"
"global_load_dwordx4 a[52:55], v0, s[34:35] offset:1024 sc1\n"
"v_mfma_f32_16x16x32_bf16 v[180:183], a[76:79], a[116:119], v[180:183]\n"
"v_mfma_f32_16x16x32_bf16 v[180:183], a[72:75], a[112:115], v[180:183]\n"
"v_mfma_f32_16x16x32_bf16 v[184:187], a[72:75], a[120:123], v[184:187]\n"
"s_waitcnt lgkmcnt(0)\n"
"global_store_dwordx4 v0, v[24:27], s[52:53] offset:-4096 sc1\n"
"v_mfma_f32_16x16x32_bf16 v[184:187], a[76:79], a[124:127], v[184:187]\n"
"v_mfma_f32_16x16x32_bf16 v[156:159], a[68:71], a[124:127], v[156:159]\n"
"v_mfma_f32_16x16x32_bf16 v[156:159], a[64:67], a[120:123], v[156:159]\n"
"v_mfma_f32_16x16x32_bf16 v[160:163], a[64:67], a[128:131], v[160:163]\n"
"v_mfma_f32_16x16x32_bf16 v[160:163], a[68:71], a[132:135], v[160:163]\n"
"global_load_dwordx4 a[56:59], v0, s[34:35] offset:2048 sc1\n"
"v_mfma_f32_16x16x32_bf16 v[188:191], a[76:79], a[132:135], v[188:191]\n"
"v_mfma_f32_16x16x32_bf16 v[188:191], a[72:75], a[128:131], v[188:191]\n"
"v_mfma_f32_16x16x32_bf16 v[192:195], a[72:75], a[136:139], v[192:195]\n"
"v_mfma_f32_16x16x32_bf16 v[192:195], a[76:79], a[140:143], v[192:195]\n"
"v_mfma_f32_16x16x32_bf16 v[164:167], a[68:71], a[140:143], v[164:167]\n"
"v_mfma_f32_16x16x32_bf16 v[164:167], a[64:67], a[136:139], v[164:167]\n"
"v_mfma_f32_16x16x32_bf16 v[168:171], a[64:67], a[144:147], v[168:171]\n"
"ds_read_b128 v[28:31], v2 offset:17408\n"
"v_mfma_f32_16x16x32_bf16 v[168:171], a[68:71], a[148:151], v[168:171]\n"
"global_load_dwordx4 a[60:63], v0, s[34:35] offset:3072 sc1\n"
"v_mfma_f32_16x16x32_bf16 v[196:199], a[76:79], a[148:151], v[196:199]\n"
"v_mfma_f32_16x16x32_bf16 v[196:199], a[72:75], a[144:147], v[196:199]\n"
"v_mfma_f32_16x16x32_bf16 a[20:23], a[72:75], a[152:155], a[20:23]\n"
"s_add_u32 s34, s34, 8192\n"
"v_mfma_f32_16x16x32_bf16 a[20:23], a[76:79], a[156:159], a[20:23]\n"
"v_mfma_f32_16x16x32_bf16 a[16:19], a[68:71], a[156:159], a[16:19]\n"
"v_mfma_f32_16x16x32_bf16 a[16:19], a[64:67], a[152:155], a[16:19]\n"
"s_waitcnt vmcnt(11)\n"
"v_mfma_f32_16x16x32_bf16 v[200:203], a[80:83], a[96:99], v[200:203]\n"
"v_mfma_f32_16x16x32_bf16 v[200:203], a[84:87], a[100:103], v[200:203]\n"
"global_load_dwordx4 a[64:67], v0, s[34:35] offset:-4096 sc1\n"
"v_mfma_f32_16x16x32_bf16 v[228:231], a[92:95], a[100:103], v[228:231]\n"
"ds_read_b128 a[100:103], v4 offset:1024\n"
"v_mfma_f32_16x16x32_bf16 v[228:231], a[88:91], a[96:99], v[228:231]\n"
"ds_read_b128 a[96:99], v4 offset:0\n"
"v_mfma_f32_16x16x32_bf16 v[232:235], a[88:91], a[104:107], v[232:235]\n"
"s_waitcnt lgkmcnt(2)\n"
"global_store_dwordx4 v0, v[28:31], s[52:53] offset:-3072 sc1\n"
"v_mfma_f32_16x16x32_bf16 v[232:235], a[92:95], a[108:111], v[232:235]\n"
"v_mfma_f32_16x16x32_bf16 v[204:207], a[84:87], a[108:111], v[204:207]\n"
"ds_read_b128 a[108:111], v4 offset:3072\n"
"v_mfma_f32_16x16x32_bf16 v[204:207], a[80:83], a[104:107], v[204:207]\n"
"ds_read_b128 a[104:107], v4 offset:2048\n"
"v_mfma_f32_16x16x32_bf16 v[208:211], a[80:83], a[112:115], v[208:211]\n"
"v_mfma_f32_16x16x32_bf16 v[208:211], a[84:87], a[116:119], v[208:211]\n"
"global_load_dwordx4 a[68:71], v0, s[34:35] offset:-3072 sc1\n"
"v_mfma_f32_16x16x32_bf16 v[236:239], a[92:95], a[116:119], v[236:239]\n"
"ds_read_b128 a[116:119], v4 offset:5120\n"
"v_mfma_f32_16x16x32_bf16 v[236:239], a[88:91], a[112:115], v[236:239]\n"
"ds_read_b128 a[112:115], v4 offset:4096\n"
"v_mfma_f32_16x16x32_bf16 v[240:243], a[88:91], a[120:123], v[240:243]\n"
"v_mfma_f32_16x16x32_bf16 v[240:243], a[92:95], a[124:127], v[240:243]\n"
"v_mfma_f32_16x16x32_bf16 v[212:215], a[84:87], a[124:127], v[212:215]\n"
"ds_read_b128 a[124:127], v4 offset:7168\n"
"v_mfma_f32_16x16x32_bf16 v[212:215], a[80:83], a[120:123], v[212:215]\n"
"ds_read_b128 a[120:123], v4 offset:6144\n"
"v_mfma_f32_16x16x32_bf16 v[216:219], a[80:83], a[128:131], v[216:219]\n"
"ds_read_b128 v[24:27], v2 offset:18432\n"
"v_mfma_f32_16x16x32_bf16 v[216:219], a[84:87], a[132:135], v[216:219]\n"
"global_load_dwordx4 a[72:75], v0, s[34:35] offset:-2048 sc1\n"
"v_mfma_f32_16x16x32_bf16 v[244:247], a[92:95], a[132:135], v[244:247]\n"
"ds_read_b128 a[132:135], v4 offset:9216\n"
"v_mfma_f32_16x16x32_bf16 v[244:247], a[88:91], a[128:131], v[244:247]\n"
"ds_read_b128 a[128:131], v4 offset:8192\n"
"v_mfma_f32_16x16x32_bf16 v[248:251], a[88:91], a[136:139], v[248:251]\n"
"buffer_load_dwordx2 v[8:9], off, s[56:59], %14 sc1\n"
"v_mfma_f32_16x16x32_bf16 v[248:251], a[92:95], a[140:143], v[248:251]\n"
"v_mfma_f32_16x16x32_bf16 v[220:223], a[84:87], a[140:143], v[220:223]\n"
"ds_read_b128 a[140:143], v4 offset:11264\n"
"v_mfma_f32_16x16x32_bf16 v[220:223], a[80:83], a[136:139], v[220:223]\n"
"ds_read_b128 a[136:139], v4 offset:10240\n"
"v_mfma_f32_16x16x32_bf16 v[224:227], a[80:83], a[144:147], v[224:227]\n"
"v_mfma_f32_16x16x32_bf16 v[224:227], a[84:87], a[148:151], v[224:227]\n"
"global_load_dwordx4 a[76:79], v0, s[34:35] offset:-1024 sc1\n"
"v_mfma_f32_16x16x32_bf16 v[252:255], a[92:95], a[148:151], v[252:255]\n"
"ds_read_b128 a[148:151], v4 offset:13312\n"
"v_mfma_f32_16x16x32_bf16 v[252:255], a[88:91], a[144:147], v[252:255]\n"
"ds_read_b128 a[144:147], v4 offset:12288\n"
"v_mfma_f32_16x16x32_bf16 a[28:31], a[88:91], a[152:155], a[28:31]\n"
"s_waitcnt lgkmcnt(6)\n"
"global_store_dwordx4 v0, v[24:27], s[52:53] offset:-2048 sc1\n"
"v_mfma_f32_16x16x32_bf16 a[28:31], a[92:95], a[156:159], a[28:31]\n"
"v_mfma_f32_16x16x32_bf16 a[24:27], a[84:87], a[156:159], a[24:27]\n"
"ds_read_b128 a[156:159], v4 offset:15360\n"
"v_mfma_f32_16x16x32_bf16 a[24:27], a[80:83], a[152:155], a[24:27]\n"
"s_waitcnt vmcnt(13)\n"
"ds_read_b128 a[152:155], v4 offset:14336\n"
"v_mfma_f32_16x16x32_bf16 v[144:147], a[32:35], a[96:99], v[144:147]\n"
"v_mfma_f32_16x16x32_bf16 v[144:147], a[36:39], a[100:103], v[144:147]\n"
"global_load_dwordx4 a[80:83], v0, s[34:35] offset:0 sc1\n"
"v_mfma_f32_16x16x32_bf16 v[172:175], a[44:47], a[100:103], v[172:175]\n"
"v_mfma_f32_16x16x32_bf16 v[172:175], a[40:43], a[96:99], v[172:175]\n"
"v_mfma_f32_16x16x32_bf16 v[176:179], a[40:43], a[104:107], v[176:179]\n"
"v_mfma_f32_16x16x32_bf16 v[176:179], a[44:47], a[108:111], v[176:179]\n"
"v_mfma_f32_16x16x32_bf16 v[148:151], a[36:39], a[108:111], v[148:151]\n"
"v_mfma_f32_16x16x32_bf16 v[148:151], a[32:35], a[104:107], v[148:151]\n"
"v_mfma_f32_16x16x32_bf16 v[152:155], a[32:35], a[112:115], v[152:155]\n"
"ds_read_b128 v[28:31], v2 offset:19456\n"
"v_mfma_f32_16x16x32_bf16 v[152:155], a[36:39], a[116:119], v[152:155]\n"
"global_load_dwordx4 a[84:87], v0, s[34:35] offset:1024 sc1\n"
"v_mfma_f32_16x16x32_bf16 v[180:183], a[44:47], a[116:119], v[180:183]\n"
"v_mfma_f32_16x16x32_bf16 v[180:183], a[40:43], a[112:115], v[180:183]\n"
"v_mfma_f32_16x16x32_bf16 v[184:187], a[40:43], a[120:123], v[184:187]\n"
"v_mfma_f32_16x16x32_bf16 v[184:187], a[44:47], a[124:127], v[184:187]\n"
"v_mfma_f32_16x16x32_bf16 v[156:159], a[36:39], a[124:127], v[156:159]\n"
"v_mfma_f32_16x16x32_bf16 v[156:159], a[32:35], a[120:123], v[156:159]\n"
"s_waitcnt lgkmcnt(5)\n"
"v_mfma_f32_16x16x32_bf16 v[160:163], a[32:35], a[128:131], v[160:163]\n"
"v_mfma_f32_16x16x32_bf16 v[160:163], a[36:39], a[132:135], v[160:163]\n"
"global_load_dwordx4 a[88:91], v0, s[34:35] offset:2048 sc1\n"
"v_mfma_f32_16x16x32_bf16 v[188:191], a[44:47], a[132:135], v[188:191]\n"
"v_mfma_f32_16x16x32_bf16 v[188:191], a[40:43], a[128:131], v[188:191]\n"
"v_mfma_f32_16x16x32_bf16 v[192:195], a[40:43], a[136:139], v[192:195]\n"
"s_waitcnt lgkmcnt(0)\n"
"global_store_dwordx4 v0, v[28:31], s[52:53] offset:-1024 sc1\n"
"v_mfma_f32_16x16x32_bf16 v[192:195], a[44:47], a[140:143], v[192:195]\n"
"v_mfma_f32_16x16x32_bf16 v[164:167], a[36:39], a[140:143], v[164:167]\n"
"v_mfma_f32_16x16x32_bf16 v[164:167], a[32:35], a[136:139], v[164:167]\n"
"v_mfma_f32_16x16x32_bf16 v[168:171], a[32:35], a[144:147], v[168:171]\n"
"v_mfma_f32_16x16x32_bf16 v[168:171], a[36:39], a[148:151], v[168:171]\n"
"global_load_dwordx4 a[92:95], v0, s[34:35] offset:3072 sc1\n"
"v_mfma_f32_16x16x32_bf16 v[196:199], a[44:47], a[148:151], v[196:199]\n"
"v_mfma_f32_16x16x32_bf16 v[196:199], a[40:43], a[144:147], v[196:199]\n"
"v_mfma_f32_16x16x32_bf16 a[20:23], a[40:43], a[152:155], a[20:23]\n"
"s_add_u32 s34, s34, 8192\n"
"v_mfma_f32_16x16x32_bf16 a[20:23], a[44:47], a[156:159], a[20:23]\n"
"v_mfma_f32_16x16x32_bf16 a[16:19], a[36:39], a[156:159], a[16:19]\n"
"v_mfma_f32_16x16x32_bf16 a[16:19], a[32:35], a[152:155], a[16:19]\n"
"s_waitcnt vmcnt(12)\n"
"v_mfma_f32_16x16x32_bf16 v[200:203], a[48:51], a[96:99], v[200:203]\n"
"ds_read_b128 v[24:27], v2 offset:20480\n"
"v_mfma_f32_16x16x32_bf16 v[200:203], a[52:55], a[100:103], v[200:203]\n"
"global_load_dwordx4 a[32:35], v0, s[34:35] offset:-4096 sc1\n"
"v_mfma_f32_16x16x32_bf16 v[228:231], a[60:63], a[100:103], v[228:231]\n"
"ds_read_b128 a[100:103], v5 offset:1024\n"
"v_mfma_f32_16x16x32_bf16 v[228:231], a[56:59], a[96:99], v[228:231]\n"
"ds_read_b128 a[96:99], v5 offset:0\n"
"v_mfma_f32_16x16x32_bf16 v[232:235], a[56:59], a[104:107], v[232:235]\n"
"v_mfma_f32_16x16x32_bf16 v[232:235], a[60:63], a[108:111], v[232:235]\n"
"v_mfma_f32_16x16x32_bf16 v[204:207], a[52:55], a[108:111], v[204:207]\n"
"ds_read_b128 a[108:111], v5 offset:3072\n"
"v_mfma_f32_16x16x32_bf16 v[204:207], a[48:51], a[104:107], v[204:207]\n"
"ds_read_b128 a[104:107], v5 offset:2048\n"
"v_mfma_f32_16x16x32_bf16 v[208:211], a[48:51], a[112:115], v[208:211]\n"
"v_mfma_f32_16x16x32_bf16 v[208:211], a[52:55], a[116:119], v[208:211]\n"
"global_load_dwordx4 a[36:39], v0, s[34:35] offset:-3072 sc1\n"
"v_mfma_f32_16x16x32_bf16 v[236:239], a[60:63], a[116:119], v[236:239]\n"
"ds_read_b128 a[116:119], v5 offset:5120\n"
"v_mfma_f32_16x16x32_bf16 v[236:239], a[56:59], a[112:115], v[236:239]\n"
"ds_read_b128 a[112:115], v5 offset:4096\n"
"v_mfma_f32_16x16x32_bf16 v[240:243], a[56:59], a[120:123], v[240:243]\n"
"s_waitcnt lgkmcnt(6)\n"
"global_store_dwordx4 v0, v[24:27], s[52:53] offset:0 sc1\n"
"v_mfma_f32_16x16x32_bf16 v[240:243], a[60:63], a[124:127], v[240:243]\n"
"v_mfma_f32_16x16x32_bf16 v[212:215], a[52:55], a[124:127], v[212:215]\n"
"ds_read_b128 a[124:127], v5 offset:7168\n"
"v_mfma_f32_16x16x32_bf16 v[212:215], a[48:51], a[120:123], v[212:215]\n"
"ds_read_b128 a[120:123], v5 offset:6144\n"
"v_mfma_f32_16x16x32_bf16 v[216:219], a[48:51], a[128:131], v[216:219]\n"
"v_mfma_f32_16x16x32_bf16 v[216:219], a[52:55], a[132:135], v[216:219]\n"
"global_load_dwordx4 a[40:43], v0, s[34:35] offset:-2048 sc1\n"
"v_mfma_f32_16x16x32_bf16 v[244:247], a[60:63], a[132:135], v[244:247]\n"
"ds_read_b128 a[132:135], v5 offset:9216\n"
"v_mfma_f32_16x16x32_bf16 v[244:247], a[56:59], a[128:131], v[244:247]\n"
"ds_read_b128 a[128:131], v5 offset:8192\n"
"v_mfma_f32_16x16x32_bf16 v[248:251], a[56:59], a[136:139], v[248:251]\n"
"v_mfma_f32_16x16x32_bf16 v[248:251], a[60:63], a[140:143], v[248:251]\n"
"v_mfma_f32_16x16x32_bf16 v[220:223], a[52:55], a[140:143], v[220:223]\n"
"ds_read_b128 a[140:143], v5 offset:11264\n"
"v_mfma_f32_16x16x32_bf16 v[220:223], a[48:51], a[136:139], v[220:223]\n"
"ds_read_b128 a[136:139], v5 offset:10240\n"
"v_mfma_f32_16x16x32_bf16 v[224:227], a[48:51], a[144:147], v[224:227]\n"
"ds_read_b128 v[28:31], v2 offset:21504\n"
"v_mfma_f32_16x16x32_bf16 v[224:227], a[52:55], a[148:151], v[224:227]\n"
"global_load_dwordx4 a[44:47], v0, s[34:35] offset:-1024 sc1\n"
"v_mfma_f32_16x16x32_bf16 v[252:255], a[60:63], a[148:151], v[252:255]\n"
"ds_read_b128 a[148:151], v5 offset:13312\n"
"v_mfma_f32_16x16x32_bf16 v[252:255], a[56:59], a[144:147], v[252:255]\n"
"ds_read_b128 a[144:147], v5 offset:12288\n"
"v_mfma_f32_16x16x32_bf16 a[28:31], a[56:59], a[152:155], a[28:31]\n"
"v_mfma_f32_16x16x32_bf16 a[28:31], a[60:63], a[156:159], a[28:31]\n"
"v_mfma_f32_16x16x32_bf16 a[24:27], a[52:55], a[156:159], a[24:27]\n"
"ds_read_b128 a[156:159], v5 offset:15360\n"
"v_mfma_f32_16x16x32_bf16 a[24:27], a[48:51], a[152:155], a[24:27]\n"
"s_waitcnt vmcnt(11) lgkmcnt(12)\n"
"ds_read_b128 a[152:155], v5 offset:14336\n"
"v_mfma_f32_16x16x32_bf16 v[144:147], a[64:67], a[96:99], v[144:147]\n"
"v_mfma_f32_16x16x32_bf16 v[144:147], a[68:71], a[100:103], v[144:147]\n"
"global_load_dwordx4 a[48:51], v0, s[34:35] offset:0 sc1\n"
"v_mfma_f32_16x16x32_bf16 v[172:175], a[76:79], a[100:103], v[172:175]\n"
"v_mfma_f32_16x16x32_bf16 v[172:175], a[72:75], a[96:99], v[172:175]\n"
"v_mfma_f32_16x16x32_bf16 v[176:179], a[72:75], a[104:107], v[176:179]\n"
"s_waitcnt lgkmcnt(4)\n"
"global_store_dwordx4 v0, v[28:31], s[52:53] offset:1024 sc1\n"
"v_mfma_f32_16x16x32_bf16 v[176:179], a[76:79], a[108:111], v[176:179]\n"
"v_mfma_f32_16x16x32_bf16 v[148:151], a[68:71], a[108:111], v[148:151]\n"
"v_mfma_f32_16x16x32_bf16 v[148:151], a[64:67], a[104:107], v[148:151]\n"
"v_mfma_f32_16x16x32_bf16 v[152:155], a[64:67], a[112:115], v[152:155]\n"
"v_mfma_f32_16x16x32_bf16 v[152:155], a[68:71], a[116:119], v[152:155]\n"
"global_load_dwordx4 a[52:55], v0, s[34:35] offset:1024 sc1\n"
"v_mfma_f32_16x16x32_bf16 v[180:183], a[76:79], a[116:119], v[180:183]\n"
"v_mfma_f32_16x16x32_bf16 v[180:183], a[72:75], a[112:115], v[180:183]\n"
"v_mfma_f32_16x16x32_bf16 v[184:187], a[72:75], a[120:123], v[184:187]\n"
"v_mfma_f32_16x16x32_bf16 v[184:187], a[76:79], a[124:127], v[184:187]\n"
"v_mfma_f32_16x16x32_bf16 v[156:159], a[68:71], a[124:127], v[156:159]\n"
"v_mfma_f32_16x16x32_bf16 v[156:159], a[64:67], a[120:123], v[156:159]\n"
"v_mfma_f32_16x16x32_bf16 v[160:163], a[64:67], a[128:131], v[160:163]\n"
"ds_read_b128 v[24:27], v2 offset:22528\n"
"v_mfma_f32_16x16x32_bf16 v[160:163], a[68:71], a[132:135], v[160:163]\n"
"global_load_dwordx4 a[56:59], v0, s[34:35] offset:2048 sc1\n"
"v_mfma_f32_16x16x32_bf16 v[188:191], a[76:79], a[132:135], v[188:191]\n"
"v_mfma_f32_16x16x32_bf16 v[188:191], a[72:75], a[128:131], v[188:191]\n"
"v_mfma_f32_16x16x32_bf16 v[192:195], a[72:75], a[136:139], v[192:195]\n"
"v_mfma_f32_16x16x32_bf16 v[192:195], a[76:79], a[140:143], v[192:195]\n"
"v_mfma_f32_16x16x32_bf16 v[164:167], a[68:71], a[140:143], v[164:167]\n"
"v_mfma_f32_16x16x32_bf16 v[164:167], a[64:67], a[136:139], v[164:167]\n"
"s_waitcnt lgkmcnt(1)\n"
"v_mfma_f32_16x16x32_bf16 v[168:171], a[64:67], a[144:147], v[168:171]\n"
"v_mfma_f32_16x16x32_bf16 v[168:171], a[68:71], a[148:151], v[168:171]\n"
"global_load_dwordx4 a[60:63], v0, s[34:35] offset:3072 sc1\n"
"v_mfma_f32_16x16x32_bf16 v[196:199], a[76:79], a[148:151], v[196:199]\n"
"v_mfma_f32_16x16x32_bf16 v[196:199], a[72:75], a[144:147], v[196:199]\n"
"v_mfma_f32_16x16x32_bf16 a[20:23], a[72:75], a[152:155], a[20:23]\n"
"s_waitcnt lgkmcnt(0)\n"
"global_store_dwordx4 v0, v[24:27], s[52:53] offset:2048 sc1\n"
"s_add_u32 s34, s34, 8192\n"
"v_mfma_f32_16x16x32_bf16 a[20:23], a[76:79], a[156:159], a[20:23]\n"
"v_mfma_f32_16x16x32_bf16 a[16:19], a[68:71], a[156:159], a[16:19]\n"
"v_mfma_f32_16x16x32_bf16 a[16:19], a[64:67], a[152:155], a[16:19]\n"
"s_waitcnt vmcnt(11)\n"
"v_mfma_f32_16x16x32_bf16 v[200:203], a[80:83], a[96:99], v[200:203]\n"
"v_mfma_f32_16x16x32_bf16 v[200:203], a[84:87], a[100:103], v[200:203]\n"
"global_load_dwordx4 a[64:67], v0, s[34:35] offset:-4096 sc1\n"
"v_mfma_f32_16x16x32_bf16 v[228:231], a[92:95], a[100:103], v[228:231]\n"
"ds_read_b128 a[100:103], v2 offset:17408\n"
"v_mfma_f32_16x16x32_bf16 v[228:231], a[88:91], a[96:99], v[228:231]\n"
"ds_read_b128 a[96:99], v2 offset:16384\n"
"v_mfma_f32_16x16x32_bf16 v[232:235], a[88:91], a[104:107], v[232:235]\n"
"v_mfma_f32_16x16x32_bf16 v[232:235], a[92:95], a[108:111], v[232:235]\n"
"v_mfma_f32_16x16x32_bf16 v[204:207], a[84:87], a[108:111], v[204:207]\n"
"ds_read_b128 a[108:111], v2 offset:19456\n"
"v_mfma_f32_16x16x32_bf16 v[204:207], a[80:83], a[104:107], v[204:207]\n"
"ds_read_b128 a[104:107], v2 offset:18432\n"
"v_mfma_f32_16x16x32_bf16 v[208:211], a[80:83], a[112:115], v[208:211]\n"
"ds_read_b128 v[28:31], v2 offset:23552\n"
"v_mfma_f32_16x16x32_bf16 v[208:211], a[84:87], a[116:119], v[208:211]\n"
"global_load_dwordx4 a[68:71], v0, s[34:35] offset:-3072 sc1\n"
"v_mfma_f32_16x16x32_bf16 v[236:239], a[92:95], a[116:119], v[236:239]\n"
"ds_read_b128 a[116:119], v2 offset:21504\n"
"v_mfma_f32_16x16x32_bf16 v[236:239], a[88:91], a[112:115], v[236:239]\n"
"ds_read_b128 a[112:115], v2 offset:20480\n"
"v_mfma_f32_16x16x32_bf16 v[240:243], a[88:91], a[120:123], v[240:243]\n"
"v_mfma_f32_16x16x32_bf16 v[240:243], a[92:95], a[124:127], v[240:243]\n"
"v_mfma_f32_16x16x32_bf16 v[212:215], a[84:87], a[124:127], v[212:215]\n"
"ds_read_b128 a[124:127], v2 offset:23552\n"
"v_mfma_f32_16x16x32_bf16 v[212:215], a[80:83], a[120:123], v[212:215]\n"
"ds_read_b128 a[120:123], v2 offset:22528\n"
"v_mfma_f32_16x16x32_bf16 v[216:219], a[80:83], a[128:131], v[216:219]\n"
"v_mfma_f32_16x16x32_bf16 v[216:219], a[84:87], a[132:135], v[216:219]\n"
"global_load_dwordx4 a[72:75], v0, s[34:35] offset:-2048 sc1\n"
"v_mfma_f32_16x16x32_bf16 v[244:247], a[92:95], a[132:135], v[244:247]\n"
"ds_read_b128 a[132:135], v2 offset:25600\n"
"v_mfma_f32_16x16x32_bf16 v[244:247], a[88:91], a[128:131], v[244:247]\n"
"ds_read_b128 a[128:131], v2 offset:24576\n"
"v_mfma_f32_16x16x32_bf16 v[248:251], a[88:91], a[136:139], v[248:251]\n"
"s_waitcnt lgkmcnt(6)\n"
"global_store_dwordx4 v0, v[28:31], s[52:53] offset:3072 sc1\n"
"v_mfma_f32_16x16x32_bf16 v[248:251], a[92:95], a[140:143], v[248:251]\n"
"v_mfma_f32_16x16x32_bf16 v[220:223], a[84:87], a[140:143], v[220:223]\n"
"ds_read_b128 a[140:143], v2 offset:27648\n"
"v_mfma_f32_16x16x32_bf16 v[220:223], a[80:83], a[136:139], v[220:223]\n"
"ds_read_b128 a[136:139], v2 offset:26624\n"
"v_mfma_f32_16x16x32_bf16 v[224:227], a[80:83], a[144:147], v[224:227]\n"
"v_mfma_f32_16x16x32_bf16 v[224:227], a[84:87], a[148:151], v[224:227]\n"
"global_load_dwordx4 a[76:79], v0, s[34:35] offset:-1024 sc1\n"
"v_mfma_f32_16x16x32_bf16 v[252:255], a[92:95], a[148:151], v[252:255]\n"
"ds_read_b128 a[148:151], v2 offset:29696\n"
"v_mfma_f32_16x16x32_bf16 v[252:255], a[88:91], a[144:147], v[252:255]\n"
"ds_read_b128 a[144:147], v2 offset:28672\n"
"v_mfma_f32_16x16x32_bf16 a[28:31], a[88:91], a[152:155], a[28:31]\n"
"v_mfma_f32_16x16x32_bf16 a[28:31], a[92:95], a[156:159], a[28:31]\n"
"v_mfma_f32_16x16x32_bf16 a[24:27], a[84:87], a[156:159], a[24:27]\n"
"ds_read_b128 a[156:159], v2 offset:31744\n"
"v_mfma_f32_16x16x32_bf16 a[24:27], a[80:83], a[152:155], a[24:27]\n"
"s_waitcnt vmcnt(11)\n"
"ds_read_b128 a[152:155], v2 offset:30720\n"
"v_mfma_f32_16x16x32_bf16 v[32:35], a[32:35], a[96:99], v[32:35]\n"
"v_mfma_f32_16x16x32_bf16 v[32:35], a[36:39], a[100:103], v[32:35]\n"
"global_load_dwordx4 a[80:83], v0, s[34:35] offset:0 sc1\n"
"v_mfma_f32_16x16x32_bf16 v[60:63], a[44:47], a[100:103], v[60:63]\n"
"s_barrier\n"
"v_mfma_f32_16x16x32_bf16 v[60:63], a[40:43], a[96:99], v[60:63]\n"
"v_mfma_f32_16x16x32_bf16 v[64:67], a[40:43], a[104:107], v[64:67]\n"
"v_mfma_f32_16x16x32_bf16 v[64:67], a[44:47], a[108:111], v[64:67]\n"
"v_mfma_f32_16x16x32_bf16 v[36:39], a[36:39], a[108:111], v[36:39]\n"
"v_subrev_u32 v8, s65, v8\n"
"v_mfma_f32_16x16x32_bf16 v[36:39], a[32:35], a[104:107], v[36:39]\n"
"s_waitcnt lgkmcnt(8)\n"
"v_xor_b32 v9, s79, v9\n"
"v_mfma_f32_16x16x32_bf16 v[40:43], a[32:35], a[112:115], v[40:43]\n"
"v_or_b32 v8, v8, v9\n"
"v_mfma_f32_16x16x32_bf16 v[40:43], a[36:39], a[116:119], v[40:43]\n"
"global_load_dwordx4 a[84:87], v0, s[34:35] offset:1024 sc1\n"
"v_cmp_lt_u32 vcc, 15, v8\n"
"v_mfma_f32_16x16x32_bf16 v[68:71], a[44:47], a[116:119], v[68:71]\n"
"s_cbranch_vccnz L_slow0\n"
"L_back0:\n"
"v_mfma_f32_16x16x32_bf16 v[68:71], a[40:43], a[112:115], v[68:71]\n"
"v_readfirstlane_b32 s66, v9\n"
"v_mfma_f32_16x16x32_bf16 v[72:75], a[40:43], a[120:123], v[72:75]\n"
"v_mfma_f32_16x16x32_bf16 v[72:75], a[44:47], a[124:127], v[72:75]\n"
"s_and_b32 s66, s66, 3\n"
"v_mfma_f32_16x16x32_bf16 v[44:47], a[36:39], a[124:127], v[44:47]\n"
"s_lshl_b32 s69, s66, 13\n"
"v_mfma_f32_16x16x32_bf16 v[44:47], a[32:35], a[120:123], v[44:47]\n"
"s_cmp_eq_u32 s66, 3\n"
"s_cselect_b32 s75, 8192, 0\n"
"s_waitcnt lgkmcnt(4)\n"
"v_mfma_f32_16x16x32_bf16 v[48:51], a[32:35], a[128:131], v[48:51]\n"
"v_mfma_f32_16x16x32_bf16 v[48:51], a[36:39], a[132:135], v[48:51]\n"
"global_load_dwordx4 a[88:91], v0, s[34:35] offset:2048 sc1\n"
"v_lshrrev_b32 v16, 4, v0\n"
"v_mfma_f32_16x16x32_bf16 v[76:79], a[44:47], a[132:135], v[76:79]\n"
"v_and_b32 v16, 0x30, v16\n"
"v_mfma_f32_16x16x32_bf16 v[76:79], a[40:43], a[128:131], v[76:79]\n"
"buffer_load_dwordx2 v[18:19], v16, s[44:47], s61 offen offset:0\n"
"v_mfma_f32_16x16x32_bf16 v[80:83], a[40:43], a[136:139], v[80:83]\n"
"v_mfma_f32_16x16x32_bf16 v[80:83], a[44:47], a[140:143], v[80:83]\n"
"v_mfma_f32_16x16x32_bf16 v[52:55], a[36:39], a[140:143], v[52:55]\n"
"v_mfma_f32_16x16x32_bf16 v[52:55], a[32:35], a[136:139], v[52:55]\n"
"s_waitcnt lgkmcnt(0)\n"
"v_mfma_f32_16x16x32_bf16 v[56:59], a[32:35], a[144:147], v[56:59]\n"
"s_add_u32 m0, s64, 0\n"
"v_mfma_f32_16x16x32_bf16 v[56:59], a[36:39], a[148:151], v[56:59]\n"
"global_load_dwordx4 a[92:95], v0, s[34:35] offset:3072 sc1\n"
"v_lshrrev_b32 v20, 4, v0\n"
"v_mfma_f32_16x16x32_bf16 v[84:87], a[44:47], a[148:151], v[84:87]\n"
"v_and_b32 v20, 0x30, v20\n"
"v_mfma_f32_16x16x32_bf16 v[84:87], a[40:43], a[144:147], v[84:87]\n"
"buffer_load_dwordx2 v[22:23], v20, s[44:47], s61 offen offset:8\n"
"v_mfma_f32_16x16x32_bf16 a[4:7], a[40:43], a[152:155], a[4:7]\n"
"s_add_u32 s34, s34, 8192\n"
"v_mfma_f32_16x16x32_bf16 a[4:7], a[44:47], a[156:159], a[4:7]\n"
"v_mfma_f32_16x16x32_bf16 a[0:3], a[36:39], a[156:159], a[0:3]\n"
"v_mfma_f32_16x16x32_bf16 a[0:3], a[32:35], a[152:155], a[0:3]\n"
"s_waitcnt vmcnt(12)\n"
"v_mfma_f32_16x16x32_bf16 v[88:91], a[48:51], a[96:99], v[88:91]\n"
"v_mfma_f32_16x16x32_bf16 v[88:91], a[52:55], a[100:103], v[88:91]\n"
"global_load_dwordx4 a[32:35], v0, s[34:35] offset:-4096 sc1\n"
"v_mfma_f32_16x16x32_bf16 v[116:119], a[60:63], a[100:103], v[116:119]\n"
"v_mfma_f32_16x16x32_bf16 v[116:119], a[56:59], a[96:99], v[116:119]\n"
"v_mfma_f32_16x16x32_bf16 v[120:123], a[56:59], a[104:107], v[120:123]\n"
"v_mfma_f32_16x16x32_bf16 v[120:123], a[60:63], a[108:111], v[120:123]\n"
"v_mfma_f32_16x16x32_bf16 v[92:95], a[52:55], a[108:111], v[92:95]\n"
"v_mfma_f32_16x16x32_bf16 v[92:95], a[48:51], a[104:107], v[92:95]\n"
"v_mfma_f32_16x16x32_bf16 v[96:99], a[48:51], a[112:115], v[96:99]\n"
"buffer_load_dwordx4 v0, s[48:51], s75 offen offset:0 sc1 lds\n"
"v_mfma_f32_16x16x32_bf16 v[96:99], a[52:55], a[116:119], v[96:99]\n"
"global_load_dwordx4 a[36:39], v0, s[34:35] offset:-3072 sc1\n"
"v_mfma_f32_16x16x32_bf16 v[124:127], a[60:63], a[116:119], v[124:127]\n"
"v_mfma_f32_16x16x32_bf16 v[124:127], a[56:59], a[112:115], v[124:127]\n"
"v_mfma_f32_16x16x32_bf16 v[128:131], a[56:59], a[120:123], v[128:131]\n"
"v_mfma_f32_16x16x32_bf16 v[128:131], a[60:63], a[124:127], v[128:131]\n"
"v_mfma_f32_16x16x32_bf16 v[100:103], a[52:55], a[124:127], v[100:103]\n"
"v_mfma_f32_16x16x32_bf16 v[100:103], a[48:51], a[120:123], v[100:103]\n"
"v_mfma_f32_16x16x32_bf16 v[104:107], a[48:51], a[128:131], v[104:107]\n"
"v_mfma_f32_16x16x32_bf16 v[104:107], a[52:55], a[132:135], v[104:107]\n"
"global_load_dwordx4 a[40:43], v0, s[34:35] offset:-2048 sc1\n"
"v_mfma_f32_16x16x32_bf16 v[132:135], a[60:63], a[132:135], v[132:135]\n"
"v_mfma_f32_16x16x32_bf16 v[132:135], a[56:59], a[128:131], v[132:135]\n"
"v_mfma_f32_16x16x32_bf16 v[136:139], a[56:59], a[136:139], v[136:139]\n"
"v_mfma_f32_16x16x32_bf16 v[136:139], a[60:63], a[140:143], v[136:139]\n"
"v_mfma_f32_16x16x32_bf16 v[108:111], a[52:55], a[140:143], v[108:111]\n"
"v_mfma_f32_16x16x32_bf16 v[108:111], a[48:51], a[136:139], v[108:111]\n"
"v_mfma_f32_16x16x32_bf16 v[112:115], a[48:51], a[144:147], v[112:115]\n"
"buffer_load_dwordx4 v0, s[48:51], s75 offen offset:1024 sc1 lds\n"
"v_mfma_f32_16x16x32_bf16 v[112:115], a[52:55], a[148:151], v[112:115]\n"
"global_load_dwordx4 a[44:47], v0, s[34:35] offset:-1024 sc1\n"
"v_mfma_f32_16x16x32_bf16 v[140:143], a[60:63], a[148:151], v[140:143]\n"
"v_mfma_f32_16x16x32_bf16 v[140:143], a[56:59], a[144:147], v[140:143]\n"
"v_mfma_f32_16x16x32_bf16 a[12:15], a[56:59], a[152:155], a[12:15]\n"
"v_mfma_f32_16x16x32_bf16 a[12:15], a[60:63], a[156:159], a[12:15]\n"
"v_mfma_f32_16x16x32_bf16 a[8:11], a[52:55], a[156:159], a[8:11]\n"
"v_mfma_f32_16x16x32_bf16 a[8:11], a[48:51], a[152:155], a[8:11]\n"
"s_waitcnt vmcnt(12)\n"
"v_mfma_f32_16x16x32_bf16 v[144:147], a[64:67], a[96:99], v[144:147]\n"
"v_mfma_f32_16x16x32_bf16 v[144:147], a[68:71], a[100:103], v[144:147]\n"
"global_load_dwordx4 a[48:51], v0, s[34:35] offset:0 sc1\n"
"v_mfma_f32_16x16x32_bf16 v[172:175], a[76:79], a[100:103], v[172:175]\n"
"v_mfma_f32_16x16x32_bf16 v[172:175], a[72:75], a[96:99], v[172:175]\n"
"v_mfma_f32_16x16x32_bf16 v[176:179], a[72:75], a[104:107], v[176:179]\n"
"v_mfma_f32_16x16x32_bf16 v[176:179], a[76:79], a[108:111], v[176:179]\n"
"v_mfma_f32_16x16x32_bf16 v[148:151], a[68:71], a[108:111], v[148:151]\n"
"v_mfma_f32_16x16x32_bf16 v[148:151], a[64:67], a[104:107], v[148:151]\n"
"buffer_load_dwordx4 v0, s[48:51], s75 offen offset:2048 sc1 lds\n"
"v_mfma_f32_16x16x32_bf16 v[152:155], a[64:67], a[112:115], v[152:155]\n"
"v_mfma_f32_16x16x32_bf16 v[152:155], a[68:71], a[116:119], v[152:155]\n"
"global_load_dwordx4 a[52:55], v0, s[34:35] offset:1024 sc1\n"
"v_mfma_f32_16x16x32_bf16 v[180:183], a[76:79], a[116:119], v[180:183]\n"
"v_mfma_f32_16x16x32_bf16 v[180:183], a[72:75], a[112:115], v[180:183]\n"
"v_mfma_f32_16x16x32_bf16 v[184:187], a[72:75], a[120:123], v[184:187]\n"
"v_mfma_f32_16x16x32_bf16 v[184:187], a[76:79], a[124:127], v[184:187]\n"
"v_mfma_f32_16x16x32_bf16 v[156:159], a[68:71], a[124:127], v[156:159]\n"
"v_mfma_f32_16x16x32_bf16 v[156:159], a[64:67], a[120:123], v[156:159]\n"
"v_mfma_f32_16x16x32_bf16 v[160:163], a[64:67], a[128:131], v[160:163]\n"
"v_mfma_f32_16x16x32_bf16 v[160:163], a[68:71], a[132:135], v[160:163]\n"
"global_load_dwordx4 a[56:59], v0, s[34:35] offset:2048 sc1\n"
"v_mfma_f32_16x16x32_bf16 v[188:191], a[76:79], a[132:135], v[188:191]\n"
"v_mfma_f32_16x16x32_bf16 v[188:191], a[72:75], a[128:131], v[188:191]\n"
"v_mfma_f32_16x16x32_bf16 v[192:195], a[72:75], a[136:139], v[192:195]\n"
"v_mfma_f32_16x16x32_bf16 v[192:195], a[76:79], a[140:143], v[192:195]\n"
"v_mfma_f32_16x16x32_bf16 v[164:167], a[68:71], a[140:143], v[164:167]\n"
"v_mfma_f32_16x16x32_bf16 v[164:167], a[64:67], a[136:139], v[164:167]\n"
"buffer_load_dwordx4 v0, s[48:51], s75 offen offset:3072 sc1 lds\n"
"v_mfma_f32_16x16x32_bf16 v[168:171], a[64:67], a[144:147], v[168:171]\n"
"v_mfma_f32_16x16x32_bf16 v[168:171], a[68:71], a[148:151], v[168:171]\n"
"global_load_dwordx4 a[60:63], v0, s[34:35] offset:3072 sc1\n"
"v_mfma_f32_16x16x32_bf16 v[196:199], a[76:79], a[148:151], v[196:199]\n"
"v_mfma_f32_16x16x32_bf16 v[196:199], a[72:75], a[144:147], v[196:199]\n"
"v_mfma_f32_16x16x32_bf16 a[20:23], a[72:75], a[152:155], a[20:23]\n"
"s_add_u32 s34, s34, 8192\n"
"v_mfma_f32_16x16x32_bf16 a[20:23], a[76:79], a[156:159], a[20:23]\n"
"v_mfma_f32_16x16x32_bf16 a[16:19], a[68:71], a[156:159], a[16:19]\n"
"v_mfma_f32_16x16x32_bf16 a[16:19], a[64:67], a[152:155], a[16:19]\n"
"s_waitcnt vmcnt(13)\n"
"v_mfma_f32_16x16x32_bf16 v[200:203], a[80:83], a[96:99], v[200:203]\n"
"v_mfma_f32_16x16x32_bf16 v[200:203], a[84:87], a[100:103], v[200:203]\n"
"global_load_dwordx4 a[64:67], v0, s[34:35] offset:-4096 sc1\n"
"v_mfma_f32_16x16x32_bf16 v[228:231], a[92:95], a[100:103], v[228:231]\n"
"ds_read_b128 a[100:103], v3 offset:17408\n"
"v_mfma_f32_16x16x32_bf16 v[228:231], a[88:91], a[96:99], v[228:231]\n"
"ds_read_b128 a[96:99], v3 offset:16384\n"
"v_mfma_f32_16x16x32_bf16 v[232:235], a[88:91], a[104:107], v[232:235]\n"
"v_mfma_f32_16x16x32_bf16 v[232:235], a[92:95], a[108:111], v[232:235]\n"
"v_mfma_f32_16x16x32_bf16 v[204:207], a[84:87], a[108:111], v[204:207]\n"
"ds_read_b128 a[108:111], v3 offset:19456\n"
"v_mfma_f32_16x16x32_bf16 v[204:207], a[80:83], a[104:107], v[204:207]\n"
"s_add_u32 m0, s64, 4096\n"
"ds_read_b128 a[104:107], v3 offset:18432\n"
"v_mfma_f32_16x16x32_bf16 v[208:211], a[80:83], a[112:115], v[208:211]\n"
"v_mfma_f32_16x16x32_bf16 v[208:211], a[84:87], a[116:119], v[208:211]\n"
"global_load_dwordx4 a[68:71], v0, s[34:35] offset:-3072 sc1\n"
"v_mfma_f32_16x16x32_bf16 v[236:239], a[92:95], a[116:119], v[236:239]\n"
"ds_read_b128 a[116:119], v3 offset:21504\n"
"v_mfma_f32_16x16x32_bf16 v[236:239], a[88:91], a[112:115], v[236:239]\n"
"ds_read_b128 a[112:115], v3 offset:20480\n"
"v_mfma_f32_16x16x32_bf16 v[240:243], a[88:91], a[120:123], v[240:243]\n"
"v_mfma_f32_16x16x32_bf16 v[240:243], a[92:95], a[124:127], v[240:243]\n"
"v_mfma_f32_16x16x32_bf16 v[212:215], a[84:87], a[124:127], v[212:215]\n"
"ds_read_b128 a[124:127], v3 offset:23552\n"
"v_mfma_f32_16x16x32_bf16 v[212:215], a[80:83], a[120:123], v[212:215]\n"
"ds_read_b128 a[120:123], v3 offset:22528\n"
"v_mfma_f32_16x16x32_bf16 v[216:219], a[80:83], a[128:131], v[216:219]\n"
"v_mfma_f32_16x16x32_bf16 v[216:219], a[84:87], a[132:135], v[216:219]\n"
"global_load_dwordx4 a[72:75], v0, s[34:35] offset:-2048 sc1\n"
"v_mfma_f32_16x16x32_bf16 v[244:247], a[92:95], a[132:135], v[244:247]\n"
"ds_read_b128 a[132:135], v3 offset:25600\n"
"v_mfma_f32_16x16x32_bf16 v[244:247], a[88:91], a[128:131], v[244:247]\n"
"ds_read_b128 a[128:131], v3 offset:24576\n"
"v_mfma_f32_16x16x32_bf16 v[248:251], a[88:91], a[136:139], v[248:251]\n"
"v_mfma_f32_16x16x32_bf16 v[248:251], a[92:95], a[140:143], v[248:251]\n"
"v_mfma_f32_16x16x32_bf16 v[220:223], a[84:87], a[140:143], v[220:223]\n"
"ds_read_b128 a[140:143], v3 offset:27648\n"
"v_mfma_f32_16x16x32_bf16 v[220:223], a[80:83], a[136:139], v[220:223]\n"
"ds_read_b128 a[136:139], v3 offset:26624\n"
"v_mfma_f32_16x16x32_bf16 v[224:227], a[80:83], a[144:147], v[224:227]\n"
"buffer_load_dwordx4 v1, s[48:51], s75 offen offset:0 sc1 lds\n"
"v_mfma_f32_16x16x32_bf16 v[224:227], a[84:87], a[148:151], v[224:227]\n"
"global_load_dwordx4 a[76:79], v0, s[34:35] offset:-1024 sc1\n"
"v_mfma_f32_16x16x32_bf16 v[252:255], a[92:95], a[148:151], v[252:255]\n"
"ds_read_b128 a[148:151], v3 offset:29696\n"
"v_mfma_f32_16x16x32_bf16 v[252:255], a[88:91], a[144:147], v[252:255]\n"
"ds_read_b128 a[144:147], v3 offset:28672\n"
"v_mfma_f32_16x16x32_bf16 a[28:31], a[88:91], a[152:155], a[28:31]\n"
"v_mfma_f32_16x16x32_bf16 a[28:31], a[92:95], a[156:159], a[28:31]\n"
"v_mfma_f32_16x16x32_bf16 a[24:27], a[84:87], a[156:159], a[24:27]\n"
"ds_read_b128 a[156:159], v3 offset:31744\n"
"v_mfma_f32_16x16x32_bf16 a[24:27], a[80:83], a[152:155], a[24:27]\n"
"s_waitcnt vmcnt(11) lgkmcnt(11)\n"
"ds_read_b128 a[152:155], v3 offset:30720\n"
"v_mfma_f32_16x16x32_bf16 v[32:35], a[32:35], a[96:99], v[32:35]\n"
"v_mfma_f32_16x16x32_bf16 v[32:35], a[36:39], a[100:103], v[32:35]\n"
"global_load_dwordx4 a[80:83], v0, s[34:35] offset:0 sc1\n"
"v_mfma_f32_16x16x32_bf16 v[60:63], a[44:47], a[100:103], v[60:63]\n"
"v_mfma_f32_16x16x32_bf16 v[60:63], a[40:43], a[96:99], v[60:63]\n"
"v_mfma_f32_16x16x32_bf16 v[64:67], a[40:43], a[104:107], v[64:67]\n"
"v_mfma_f32_16x16x32_bf16 v[64:67], a[44:47], a[108:111], v[64:67]\n"
"s_add_u32 s65, s65, 1\n"
"v_mfma_f32_16x16x32_bf16 v[36:39], a[36:39], a[108:111], v[36:39]\n"
"buffer_load_dwordx4 v1, s[48:51], s75 offen offset:1024 sc1 lds\n"
"v_mfma_f32_16x16x32_bf16 v[36:39], a[32:35], a[104:107], v[36:39]\n"
"s_waitcnt lgkmcnt(8)\n"
"v_mov_b32 v8, s65\n"
"v_mfma_f32_16x16x32_bf16 v[40:43], a[32:35], a[112:115], v[40:43]\n"
"v_mov_b32 v9, s40\n"
"v_mfma_f32_16x16x32_bf16 v[40:43], a[36:39], a[116:119], v[40:43]\n"
"global_load_dwordx4 a[84:87], v0, s[34:35] offset:1024 sc1\n"
"v_mfma_f32_16x16x32_bf16 v[68:71], a[44:47], a[116:119], v[68:71]\n"
"buffer_store_dwordx2 v[8:9], off, s[56:59], %13 sc1\n"
"v_mfma_f32_16x16x32_bf16 v[68:71], a[40:43], a[112:115], v[68:71]\n"
"v_mfma_f32_16x16x32_bf16 v[72:75], a[40:43], a[120:123], v[72:75]\n"
"v_mfma_f32_16x16x32_bf16 v[72:75], a[44:47], a[124:127], v[72:75]\n"
"v_mfma_f32_16x16x32_bf16 v[44:47], a[36:39], a[124:127], v[44:47]\n"
"v_mfma_f32_16x16x32_bf16 v[44:47], a[32:35], a[120:123], v[44:47]\n"
"s_waitcnt lgkmcnt(4)\n"
"v_mfma_f32_16x16x32_bf16 v[48:51], a[32:35], a[128:131], v[48:51]\n"
"v_mfma_f32_16x16x32_bf16 v[48:51], a[36:39], a[132:135], v[48:51]\n"
"global_load_dwordx4 a[88:91], v0, s[34:35] offset:2048 sc1\n"
"v_mfma_f32_16x16x32_bf16 v[76:79], a[44:47], a[132:135], v[76:79]\n"
"v_mfma_f32_16x16x32_bf16 v[76:79], a[40:43], a[128:131], v[76:79]\n"
"v_mfma_f32_16x16x32_bf16 v[80:83], a[40:43], a[136:139], v[80:83]\n"
"v_mfma_f32_16x16x32_bf16 v[80:83], a[44:47], a[140:143], v[80:83]\n"
"v_mfma_f32_16x16x32_bf16 v[52:55], a[36:39], a[140:143], v[52:55]\n"
"buffer_load_dwordx4 v1, s[48:51], s75 offen offset:2048 sc1 lds\n"
"v_mfma_f32_16x16x32_bf16 v[52:55], a[32:35], a[136:139], v[52:55]\n"
"s_waitcnt lgkmcnt(0)\n"
"v_mfma_f32_16x16x32_bf16 v[56:59], a[32:35], a[144:147], v[56:59]\n"
"v_mfma_f32_16x16x32_bf16 v[56:59], a[36:39], a[148:151], v[56:59]\n"
"global_load_dwordx4 a[92:95], v0, s[34:35] offset:3072 sc1\n"
"v_mfma_f32_16x16x32_bf16 v[84:87], a[44:47], a[148:151], v[84:87]\n"
"v_mfma_f32_16x16x32_bf16 v[84:87], a[40:43], a[144:147], v[84:87]\n"
"v_mfma_f32_16x16x32_bf16 a[4:7], a[40:43], a[152:155], a[4:7]\n"
"s_add_u32 s34, s34, 8192\n"
"v_mfma_f32_16x16x32_bf16 a[4:7], a[44:47], a[156:159], a[4:7]\n"
"v_mfma_f32_16x16x32_bf16 a[0:3], a[36:39], a[156:159], a[0:3]\n"
"v_mfma_f32_16x16x32_bf16 a[0:3], a[32:35], a[152:155], a[0:3]\n"
"s_waitcnt vmcnt(12)\n"
"v_mfma_f32_16x16x32_bf16 v[88:91], a[48:51], a[96:99], v[88:91]\n"
"v_mfma_f32_16x16x32_bf16 v[88:91], a[52:55], a[100:103], v[88:91]\n"
"global_load_dwordx4 a[32:35], v0, s[34:35] offset:-4096 sc1\n"
"v_mfma_f32_16x16x32_bf16 v[116:119], a[60:63], a[100:103], v[116:119]\n"
"v_mfma_f32_16x16x32_bf16 v[116:119], a[56:59], a[96:99], v[116:119]\n"
"v_mfma_f32_16x16x32_bf16 v[120:123], a[56:59], a[104:107], v[120:123]\n"
"v_mfma_f32_16x16x32_bf16 v[120:123], a[60:63], a[108:111], v[120:123]\n"
"v_mfma_f32_16x16x32_bf16 v[92:95], a[52:55], a[108:111], v[92:95]\n"
"buffer_load_dwordx4 v1, s[48:51], s75 offen offset:3072 sc1 lds\n"
"v_mfma_f32_16x16x32_bf16 v[92:95], a[48:51], a[104:107], v[92:95]\n"
"v_mfma_f32_16x16x32_bf16 v[96:99], a[48:51], a[112:115], v[96:99]\n"
"v_mfma_f32_16x16x32_bf16 v[96:99], a[52:55], a[116:119], v[96:99]\n"
"global_load_dwordx4 a[36:39], v0, s[34:35] offset:-3072 sc1\n"
"v_mfma_f32_16x16x32_bf16 v[124:127], a[60:63], a[116:119], v[124:127]\n"
"v_mfma_f32_16x16x32_bf16 v[124:127], a[56:59], a[112:115], v[124:127]\n"
"v_mfma_f32_16x16x32_bf16 v[128:131], a[56:59], a[120:123], v[128:131]\n"
"v_mfma_f32_16x16x32_bf16 v[128:131], a[60:63], a[124:127], v[128:131]\n"
"v_mfma_f32_16x16x32_bf16 v[100:103], a[52:55], a[124:127], v[100:103]\n"
"v_mfma_f32_16x16x32_bf16 v[100:103], a[48:51], a[120:123], v[100:103]\n"
"v_mfma_f32_16x16x32_bf16 v[104:107], a[48:51], a[128:131], v[104:107]\n"
"v_mfma_f32_16x16x32_bf16 v[104:107], a[52:55], a[132:135], v[104:107]\n"
"global_load_dwordx4 a[40:43], v0, s[34:35] offset:-2048 sc1\n"
"v_mfma_f32_16x16x32_bf16 v[132:135], a[60:63], a[132:135], v[132:135]\n"
"v_mfma_f32_16x16x32_bf16 v[132:135], a[56:59], a[128:131], v[132:135]\n"
"v_mfma_f32_16x16x32_bf16 v[136:139], a[56:59], a[136:139], v[136:139]\n"
"v_mfma_f32_16x16x32_bf16 v[136:139], a[60:63], a[140:143], v[136:139]\n"
"s_add_u32 m0, s64, 8192\n"
"v_mfma_f32_16x16x32_bf16 v[108:111], a[52:55], a[140:143], v[108:111]\n"
"v_mfma_f32_16x16x32_bf16 v[108:111], a[48:51], a[136:139], v[108:111]\n"
"v_mfma_f32_16x16x32_bf16 v[112:115], a[48:51], a[144:147], v[112:115]\n"
"v_mfma_f32_16x16x32_bf16 v[112:115], a[52:55], a[148:151], v[112:115]\n"
"global_load_dwordx4 a[44:47], v0, s[34:35] offset:-1024 sc1\n"
"v_mfma_f32_16x16x32_bf16 v[140:143], a[60:63], a[148:151], v[140:143]\n"
"v_mfma_f32_16x16x32_bf16 v[140:143], a[56:59], a[144:147], v[140:143]\n"
"v_mfma_f32_16x16x32_bf16 a[12:15], a[56:59], a[152:155], a[12:15]\n"
"v_mfma_f32_16x16x32_bf16 a[12:15], a[60:63], a[156:159], a[12:15]\n"
"v_mfma_f32_16x16x32_bf16 a[8:11], a[52:55], a[156:159], a[8:11]\n"
"v_mfma_f32_16x16x32_bf16 a[8:11], a[48:51], a[152:155], a[8:11]\n"
"s_waitcnt vmcnt(12)\n"
"v_mfma_f32_16x16x32_bf16 v[144:147], a[64:67], a[96:99], v[144:147]\n"
"v_mfma_f32_16x16x32_bf16 v[144:147], a[68:71], a[100:103], v[144:147]\n"
"global_load_dwordx4 a[48:51], v0, s[34:35] offset:0 sc1\n"
"v_mfma_f32_16x16x32_bf16 v[172:175], a[76:79], a[100:103], v[172:175]\n"
"v_mfma_f32_16x16x32_bf16 v[172:175], a[72:75], a[96:99], v[172:175]\n"
"v_mfma_f32_16x16x32_bf16 v[176:179], a[72:75], a[104:107], v[176:179]\n"
"v_mfma_f32_16x16x32_bf16 v[176:179], a[76:79], a[108:111], v[176:179]\n"
"buffer_load_dwordx4 v0, s[48:51], s69 offen offset:0 sc1 lds\n"
"v_mfma_f32_16x16x32_bf16 v[148:151], a[68:71], a[108:111], v[148:151]\n"
"v_mfma_f32_16x16x32_bf16 v[148:151], a[64:67], a[104:107], v[148:151]\n"
"v_mfma_f32_16x16x32_bf16 v[152:155], a[64:67], a[112:115], v[152:155]\n"
"v_mfma_f32_16x16x32_bf16 v[152:155], a[68:71], a[116:119], v[152:155]\n"
"global_load_dwordx4 a[52:55], v0, s[34:35] offset:1024 sc1\n"
"v_mfma_f32_16x16x32_bf16 v[180:183], a[76:79], a[116:119], v[180:183]\n"
"v_mfma_f32_16x16x32_bf16 v[180:183], a[72:75], a[112:115], v[180:183]\n"
"v_mfma_f32_16x16x32_bf16 v[184:187], a[72:75], a[120:123], v[184:187]\n"
"v_mfma_f32_16x16x32_bf16 v[184:187], a[76:79], a[124:127], v[184:187]\n"
"v_mfma_f32_16x16x32_bf16 v[156:159], a[68:71], a[124:127], v[156:159]\n"
"v_mfma_f32_16x16x32_bf16 v[156:159], a[64:67], a[120:123], v[156:159]\n"
"v_mfma_f32_16x16x32_bf16 v[160:163], a[64:67], a[128:131], v[160:163]\n"
"v_mfma_f32_16x16x32_bf16 v[160:163], a[68:71], a[132:135], v[160:163]\n"
"global_load_dwordx4 a[56:59], v0, s[34:35] offset:2048 sc1\n"
"v_mfma_f32_16x16x32_bf16 v[188:191], a[76:79], a[132:135], v[188:191]\n"
"v_mfma_f32_16x16x32_bf16 v[188:191], a[72:75], a[128:131], v[188:191]\n"
"v_mfma_f32_16x16x32_bf16 v[192:195], a[72:75], a[136:139], v[192:195]\n"
"v_mfma_f32_16x16x32_bf16 v[192:195], a[76:79], a[140:143], v[192:195]\n"
"buffer_load_dwordx4 v0, s[48:51], s69 offen offset:1024 sc1 lds\n"
"v_mfma_f32_16x16x32_bf16 v[164:167], a[68:71], a[140:143], v[164:167]\n"
"v_mfma_f32_16x16x32_bf16 v[164:167], a[64:67], a[136:139], v[164:167]\n"
"v_mfma_f32_16x16x32_bf16 v[168:171], a[64:67], a[144:147], v[168:171]\n"
"v_mfma_f32_16x16x32_bf16 v[168:171], a[68:71], a[148:151], v[168:171]\n"
"global_load_dwordx4 a[60:63], v0, s[34:35] offset:3072 sc1\n"
"v_mfma_f32_16x16x32_bf16 v[196:199], a[76:79], a[148:151], v[196:199]\n"
"v_mfma_f32_16x16x32_bf16 v[196:199], a[72:75], a[144:147], v[196:199]\n"
"v_mfma_f32_16x16x32_bf16 a[20:23], a[72:75], a[152:155], a[20:23]\n"
"s_add_u32 s34, s34, 8192\n"
"v_mfma_f32_16x16x32_bf16 a[20:23], a[76:79], a[156:159], a[20:23]\n"
"v_mfma_f32_16x16x32_bf16 a[16:19], a[68:71], a[156:159], a[16:19]\n"
"v_mfma_f32_16x16x32_bf16 a[16:19], a[64:67], a[152:155], a[16:19]\n"
"s_waitcnt vmcnt(11)\n"
"v_mfma_f32_16x16x32_bf16 v[200:203], a[80:83], a[96:99], v[200:203]\n"
"v_mfma_f32_16x16x32_bf16 v[200:203], a[84:87], a[100:103], v[200:203]\n"
"global_load_dwordx4 a[64:67], v0, s[34:35] offset:-4096 sc1\n"
"v_mfma_f32_16x16x32_bf16 v[228:231], a[92:95], a[100:103], v[228:231]\n"
"ds_read_b128 a[100:103], v4 offset:17408\n"
"v_mfma_f32_16x16x32_bf16 v[228:231], a[88:91], a[96:99], v[228:231]\n"
"ds_read_b128 a[96:99], v4 offset:16384\n"
"v_mfma_f32_16x16x32_bf16 v[232:235], a[88:91], a[104:107], v[232:235]\n"
"buffer_load_dwordx4 v0, s[48:51], s69 offen offset:2048 sc1 lds\n"
"v_mfma_f32_16x16x32_bf16 v[232:235], a[92:95], a[108:111], v[232:235]\n"
"v_mfma_f32_16x16x32_bf16 v[204:207], a[84:87], a[108:111], v[204:207]\n"
"ds_read_b128 a[108:111], v4 offset:19456\n"
"v_mfma_f32_16x16x32_bf16 v[204:207], a[80:83], a[104:107], v[204:207]\n"
"ds_read_b128 a[104:107], v4 offset:18432\n"
"v_mfma_f32_16x16x32_bf16 v[208:211], a[80:83], a[112:115], v[208:211]\n"
"v_mfma_f32_16x16x32_bf16 v[208:211], a[84:87], a[116:119], v[208:211]\n"
"global_load_dwordx4 a[68:71], v0, s[34:35] offset:-3072 sc1\n"
"v_mfma_f32_16x16x32_bf16 v[236:239], a[92:95], a[116:119], v[236:239]\n"
"ds_read_b128 a[116:119], v4 offset:21504\n"
"v_mfma_f32_16x16x32_bf16 v[236:239], a[88:91], a[112:115], v[236:239]\n"
"ds_read_b128 a[112:115], v4 offset:20480\n"
"v_mfma_f32_16x16x32_bf16 v[240:243], a[88:91], a[120:123], v[240:243]\n"
"v_mfma_f32_16x16x32_bf16 v[240:243], a[92:95], a[124:127], v[240:243]\n"
"v_mfma_f32_16x16x32_bf16 v[212:215], a[84:87], a[124:127], v[212:215]\n"
"ds_read_b128 a[124:127], v4 offset:23552\n"
"v_mfma_f32_16x16x32_bf16 v[212:215], a[80:83], a[120:123], v[212:215]\n"
"ds_read_b128 a[120:123], v4 offset:22528\n"
"v_mfma_f32_16x16x32_bf16 v[216:219], a[80:83], a[128:131], v[216:219]\n"
"v_mfma_f32_16x16x32_bf16 v[216:219], a[84:87], a[132:135], v[216:219]\n"
"global_load_dwordx4 a[72:75], v0, s[34:35] offset:-2048 sc1\n"
"v_mfma_f32_16x16x32_bf16 v[244:247], a[92:95], a[132:135], v[244:247]\n"
"ds_read_b128 a[132:135], v4 offset:25600\n"
"v_mfma_f32_16x16x32_bf16 v[244:247], a[88:91], a[128:131], v[244:247]\n"
"ds_read_b128 a[128:131], v4 offset:24576\n"
"v_mfma_f32_16x16x32_bf16 v[248:251], a[88:91], a[136:139], v[248:251]\n"
"buffer_load_dwordx4 v0, s[48:51], s69 offen offset:3072 sc1 lds\n"
"v_mfma_f32_16x16x32_bf16 v[248:251], a[92:95], a[140:143], v[248:251]\n"
"v_mfma_f32_16x16x32_bf16 v[220:223], a[84:87], a[140:143], v[220:223]\n"
"ds_read_b128 a[140:143], v4 offset:27648\n"
"v_mfma_f32_16x16x32_bf16 v[220:223], a[80:83], a[136:139], v[220:223]\n"
"ds_read_b128 a[136:139], v4 offset:26624\n"
"v_mfma_f32_16x16x32_bf16 v[224:227], a[80:83], a[144:147], v[224:227]\n"
"v_mfma_f32_16x16x32_bf16 v[224:227], a[84:87], a[148:151], v[224:227]\n"
"global_load_dwordx4 a[76:79], v0, s[34:35] offset:-1024 sc1\n"
"v_mfma_f32_16x16x32_bf16 v[252:255], a[92:95], a[148:151], v[252:255]\n"
"ds_read_b128 a[148:151], v4 offset:29696\n"
"v_mfma_f32_16x16x32_bf16 v[252:255], a[88:91], a[144:147], v[252:255]\n"
"ds_read_b128 a[144:147], v4 offset:28672\n"
"v_mfma_f32_16x16x32_bf16 a[28:31], a[88:91], a[152:155], a[28:31]\n"
"v_mfma_f32_16x16x32_bf16 a[28:31], a[92:95], a[156:159], a[28:31]\n"
"v_mfma_f32_16x16x32_bf16 a[24:27], a[84:87], a[156:159], a[24:27]\n"
"ds_read_b128 a[156:159], v4 offset:31744\n"
"v_mfma_f32_16x16x32_bf16 a[24:27], a[80:83], a[152:155], a[24:27]\n"
"s_waitcnt vmcnt(12) lgkmcnt(11)\n"
"ds_read_b128 a[152:155], v4 offset:30720\n"
"v_mfma_f32_16x16x32_bf16 v[32:35], a[32:35], a[96:99], v[32:35]\n"
"v_mfma_f32_16x16x32_bf16 v[32:35], a[36:39], a[100:103], v[32:35]\n"
"global_load_dwordx4 a[80:83], v0, s[34:35] offset:0 sc1\n"
"v_mfma_f32_16x16x32_bf16 v[60:63], a[44:47], a[100:103], v[60:63]\n"
"v_mfma_f32_16x16x32_bf16 v[60:63], a[40:43], a[96:99], v[60:63]\n"
"v_mfma_f32_16x16x32_bf16 v[64:67], a[40:43], a[104:107], v[64:67]\n"
"s_add_u32 m0, s64, 12288\n"
"v_mfma_f32_16x16x32_bf16 v[64:67], a[44:47], a[108:111], v[64:67]\n"
"v_mfma_f32_16x16x32_bf16 v[36:39], a[36:39], a[108:111], v[36:39]\n"
"v_mfma_f32_16x16x32_bf16 v[36:39], a[32:35], a[104:107], v[36:39]\n"
"s_waitcnt lgkmcnt(8)\n"
"v_mfma_f32_16x16x32_bf16 v[40:43], a[32:35], a[112:115], v[40:43]\n"
"v_mfma_f32_16x16x32_bf16 v[40:43], a[36:39], a[116:119], v[40:43]\n"
"global_load_dwordx4 a[84:87], v0, s[34:35] offset:1024 sc1\n"
"v_mfma_f32_16x16x32_bf16 v[68:71], a[44:47], a[116:119], v[68:71]\n"
"v_mfma_f32_16x16x32_bf16 v[68:71], a[40:43], a[112:115], v[68:71]\n"
"v_mfma_f32_16x16x32_bf16 v[72:75], a[40:43], a[120:123], v[72:75]\n"
"v_mfma_f32_16x16x32_bf16 v[72:75], a[44:47], a[124:127], v[72:75]\n"
"v_mfma_f32_16x16x32_bf16 v[44:47], a[36:39], a[124:127], v[44:47]\n"
"v_mfma_f32_16x16x32_bf16 v[44:47], a[32:35], a[120:123], v[44:47]\n"
"s_waitcnt lgkmcnt(4)\n"
"v_mfma_f32_16x16x32_bf16 v[48:51], a[32:35], a[128:131], v[48:51]\n"
"v_mfma_f32_16x16x32_bf16 v[48:51], a[36:39], a[132:135], v[48:51]\n"
"global_load_dwordx4 a[88:91], v0, s[34:35] offset:2048 sc1\n"
"v_mfma_f32_16x16x32_bf16 v[76:79], a[44:47], a[132:135], v[76:79]\n"
"v_mfma_f32_16x16x32_bf16 v[76:79], a[40:43], a[128:131], v[76:79]\n"
"v_mfma_f32_16x16x32_bf16 v[80:83], a[40:43], a[136:139], v[80:83]\n"
"buffer_load_dwordx4 v1, s[48:51], s69 offen offset:0 sc1 lds\n"
"v_mfma_f32_16x16x32_bf16 v[80:83], a[44:47], a[140:143], v[80:83]\n"
"v_mfma_f32_16x16x32_bf16 v[52:55], a[36:39], a[140:143], v[52:55]\n"
"v_mfma_f32_16x16x32_bf16 v[52:55], a[32:35], a[136:139], v[52:55]\n"
"s_waitcnt lgkmcnt(0)\n"
"v_mfma_f32_16x16x32_bf16 v[56:59], a[32:35], a[144:147], v[56:59]\n"
"v_mfma_f32_16x16x32_bf16 v[56:59], a[36:39], a[148:151], v[56:59]\n"
"global_load_dwordx4 a[92:95], v0, s[34:35] offset:3072 sc1\n"
"v_mfma_f32_16x16x32_bf16 v[84:87], a[44:47], a[148:151], v[84:87]\n"
"v_mfma_f32_16x16x32_bf16 v[84:87], a[40:43], a[144:147], v[84:87]\n"
"v_mfma_f32_16x16x32_bf16 a[4:7], a[40:43], a[152:155], a[4:7]\n"
"s_add_u32 s34, s34, 8192\n"
"v_mfma_f32_16x16x32_bf16 a[4:7], a[44:47], a[156:159], a[4:7]\n"
"v_mfma_f32_16x16x32_bf16 a[0:3], a[36:39], a[156:159], a[0:3]\n"
"v_mfma_f32_16x16x32_bf16 a[0:3], a[32:35], a[152:155], a[0:3]\n"
"s_waitcnt vmcnt(11)\n"
"v_mfma_f32_16x16x32_bf16 v[88:91], a[48:51], a[96:99], v[88:91]\n"
"v_mfma_f32_16x16x32_bf16 v[88:91], a[52:55], a[100:103], v[88:91]\n"
"global_load_dwordx4 a[32:35], v0, s[34:35] offset:-4096 sc1\n"
"v_mfma_f32_16x16x32_bf16 v[116:119], a[60:63], a[100:103], v[116:119]\n"
"v_mfma_f32_16x16x32_bf16 v[116:119], a[56:59], a[96:99], v[116:119]\n"
"buffer_load_dwordx4 v1, s[48:51], s69 offen offset:1024 sc1 lds\n"
"v_mfma_f32_16x16x32_bf16 v[120:123], a[56:59], a[104:107], v[120:123]\n"
"v_mfma_f32_16x16x32_bf16 v[120:123], a[60:63], a[108:111], v[120:123]\n"
"v_mfma_f32_16x16x32_bf16 v[92:95], a[52:55], a[108:111], v[92:95]\n"
"v_mfma_f32_16x16x32_bf16 v[92:95], a[48:51], a[104:107], v[92:95]\n"
"v_mfma_f32_16x16x32_bf16 v[96:99], a[48:51], a[112:115], v[96:99]\n"
"v_mfma_f32_16x16x32_bf16 v[96:99], a[52:55], a[116:119], v[96:99]\n"
"global_load_dwordx4 a[36:39], v0, s[34:35] offset:-3072 sc1\n"
"v_mfma_f32_16x16x32_bf16 v[124:127], a[60:63], a[116:119], v[124:127]\n"
"v_mfma_f32_16x16x32_bf16 v[124:127], a[56:59], a[112:115], v[124:127]\n"
"v_mfma_f32_16x16x32_bf16 v[128:131], a[56:59], a[120:123], v[128:131]\n"
"v_mfma_f32_16x16x32_bf16 v[128:131], a[60:63], a[124:127], v[128:131]\n"
"v_mfma_f32_16x16x32_bf16 v[100:103], a[52:55], a[124:127], v[100:103]\n"
"v_mfma_f32_16x16x32_bf16 v[100:103], a[48:51], a[120:123], v[100:103]\n"
"v_mfma_f32_16x16x32_bf16 v[104:107], a[48:51], a[128:131], v[104:107]\n"
"v_mfma_f32_16x16x32_bf16 v[104:107], a[52:55], a[132:135], v[104:107]\n"
"global_load_dwordx4 a[40:43], v0, s[34:35] offset:-2048 sc1\n"
"v_mfma_f32_16x16x32_bf16 v[132:135], a[60:63], a[132:135], v[132:135]\n"
"v_mfma_f32_16x16x32_bf16 v[132:135], a[56:59], a[128:131], v[132:135]\n"
"buffer_load_dwordx4 v1, s[48:51], s69 offen offset:2048 sc1 lds\n"
"v_mfma_f32_16x16x32_bf16 v[136:139], a[56:59], a[136:139], v[136:139]\n"
"v_mfma_f32_16x16x32_bf16 v[136:139], a[60:63], a[140:143], v[136:139]\n"
"v_mfma_f32_16x16x32_bf16 v[108:111], a[52:55], a[140:143], v[108:111]\n"
"v_mfma_f32_16x16x32_bf16 v[108:111], a[48:51], a[136:139], v[108:111]\n"
"v_mfma_f32_16x16x32_bf16 v[112:115], a[48:51], a[144:147], v[112:115]\n"
"v_mfma_f32_16x16x32_bf16 v[112:115], a[52:55], a[148:151], v[112:115]\n"
"global_load_dwordx4 a[44:47], v0, s[34:35] offset:-1024 sc1\n"
"v_mfma_f32_16x16x32_bf16 v[140:143], a[60:63], a[148:151], v[140:143]\n"
"v_mfma_f32_16x16x32_bf16 v[140:143], a[56:59], a[144:147], v[140:143]\n"
"v_mfma_f32_16x16x32_bf16 a[12:15], a[56:59], a[152:155], a[12:15]\n"
"v_mfma_f32_16x16x32_bf16 a[12:15], a[60:63], a[156:159], a[12:15]\n"
"v_mfma_f32_16x16x32_bf16 a[8:11], a[52:55], a[156:159], a[8:11]\n"
"v_mfma_f32_16x16x32_bf16 a[8:11], a[48:51], a[152:155], a[8:11]\n"
"s_waitcnt vmcnt(11)\n"
"v_mfma_f32_16x16x32_bf16 v[144:147], a[64:67], a[96:99], v[144:147]\n"
"v_mfma_f32_16x16x32_bf16 v[144:147], a[68:71], a[100:103], v[144:147]\n"
"global_load_dwordx4 a[48:51], v0, s[34:35] offset:0 sc1\n"
"v_mfma_f32_16x16x32_bf16 v[172:175], a[76:79], a[100:103], v[172:175]\n"
"v_mfma_f32_16x16x32_bf16 v[172:175], a[72:75], a[96:99], v[172:175]\n"
"buffer_load_dwordx4 v1, s[48:51], s69 offen offset:3072 sc1 lds\n"
"v_mfma_f32_16x16x32_bf16 v[176:179], a[72:75], a[104:107], v[176:179]\n"
"v_mfma_f32_16x16x32_bf16 v[176:179], a[76:79], a[108:111], v[176:179]\n"
"v_mfma_f32_16x16x32_bf16 v[148:151], a[68:71], a[108:111], v[148:151]\n"
"v_mfma_f32_16x16x32_bf16 v[148:151], a[64:67], a[104:107], v[148:151]\n"
"v_mfma_f32_16x16x32_bf16 v[152:155], a[64:67], a[112:115], v[152:155]\n"
"v_lshlrev_b32 v16, 16, v18\n"
"v_mfma_f32_16x16x32_bf16 v[152:155], a[68:71], a[116:119], v[152:155]\n"
"global_load_dwordx4 a[52:55], v0, s[34:35] offset:1024 sc1\n"
"v_and_b32 v17, s63, v18\n"
"v_mfma_f32_16x16x32_bf16 v[180:183], a[76:79], a[116:119], v[180:183]\n"
"v_lshlrev_b32 v18, 16, v19\n"
"v_mfma_f32_16x16x32_bf16 v[180:183], a[72:75], a[112:115], v[180:183]\n"
"v_and_b32 v19, s63, v19\n"
"v_mfma_f32_16x16x32_bf16 v[184:187], a[72:75], a[120:123], v[184:187]\n"
"v_add_f32 v32, v32, v16\n"
"v_mfma_f32_16x16x32_bf16 v[184:187], a[76:79], a[124:127], v[184:187]\n"
"v_add_f32 v33, v33, v17\n"
"v_add_f32 v34, v34, v18\n"
"v_mfma_f32_16x16x32_bf16 v[156:159], a[68:71], a[124:127], v[156:159]\n"
"v_add_f32 v35, v35, v19\n"
"v_mfma_f32_16x16x32_bf16 v[156:159], a[64:67], a[120:123], v[156:159]\n"
"v_add_f32 v36, v36, v16\n"
"v_mfma_f32_16x16x32_bf16 v[160:163], a[64:67], a[128:131], v[160:163]\n"
"v_add_f32 v37, v37, v17\n"
"v_mfma_f32_16x16x32_bf16 v[160:163], a[68:71], a[132:135], v[160:163]\n"
"global_load_dwordx4 a[56:59], v0, s[34:35] offset:2048 sc1\n"
"v_add_f32 v38, v38, v18\n"
"v_mfma_f32_16x16x32_bf16 v[188:191], a[76:79], a[132:135], v[188:191]\n"
"v_add_f32 v39, v39, v19\n"
"v_mfma_f32_16x16x32_bf16 v[188:191], a[72:75], a[128:131], v[188:191]\n"
"v_add_f32 v40, v40, v16\n"
"v_mfma_f32_16x16x32_bf16 v[192:195], a[72:75], a[136:139], v[192:195]\n"
"v_add_f32 v41, v41, v17\n"
"v_mfma_f32_16x16x32_bf16 v[192:195], a[76:79], a[140:143], v[192:195]\n"
"v_add_f32 v42, v42, v18\n"
"v_mfma_f32_16x16x32_bf16 v[164:167], a[68:71], a[140:143], v[164:167]\n"
"v_add_f32 v43, v43, v19\n"
"v_mfma_f32_16x16x32_bf16 v[164:167], a[64:67], a[136:139], v[164:167]\n"
"v_add_f32 v44, v44, v16\n"
"v_add_f32 v45, v45, v17\n"
"v_mfma_f32_16x16x32_bf16 v[168:171], a[64:67], a[144:147], v[168:171]\n"
"v_add_f32 v46, v46, v18\n"
"v_mfma_f32_16x16x32_bf16 v[168:171], a[68:71], a[148:151], v[168:171]\n"
"global_load_dwordx4 a[60:63], v0, s[34:35] offset:3072 sc1\n"
"v_add_f32 v47, v47, v19\n"
"v_mfma_f32_16x16x32_bf16 v[196:199], a[76:79], a[148:151], v[196:199]\n"
"v_add_f32 v48, v48, v16\n"
"v_mfma_f32_16x16x32_bf16 v[196:199], a[72:75], a[144:147], v[196:199]\n"
"v_add_f32 v49, v49, v17\n"
"v_mfma_f32_16x16x32_bf16 a[20:23], a[72:75], a[152:155], a[20:23]\n"
"s_add_u32 s34, s34, 8192\n"
"v_add_f32 v50, v50, v18\n"
"v_mfma_f32_16x16x32_bf16 a[20:23], a[76:79], a[156:159], a[20:23]\n"
"v_add_f32 v51, v51, v19\n"
"v_mfma_f32_16x16x32_bf16 a[16:19], a[68:71], a[156:159], a[16:19]\n"
"v_add_f32 v52, v52, v16\n"
"v_mfma_f32_16x16x32_bf16 a[16:19], a[64:67], a[152:155], a[16:19]\n"
"s_waitcnt vmcnt(11)\n"
"v_add_f32 v53, v53, v17\n"
"v_mfma_f32_16x16x32_bf16 v[200:203], a[80:83], a[96:99], v[200:203]\n"
"v_add_f32 v54, v54, v18\n"
"v_add_f32 v55, v55, v19\n"
"v_mfma_f32_16x16x32_bf16 v[200:203], a[84:87], a[100:103], v[200:203]\n"
"global_load_dwordx4 a[64:67], v0, s[34:35] offset:-4096 sc1\n"
"v_add_f32 v56, v56, v16\n"
"v_mfma_f32_16x16x32_bf16 v[228:231], a[92:95], a[100:103], v[228:231]\n"
"ds_read_b128 a[100:103], v5 offset:17408\n"
"v_add_f32 v57, v57, v17\n"
"v_mfma_f32_16x16x32_bf16 v[228:231], a[88:91], a[96:99], v[228:231]\n"
"ds_read_b128 a[96:99], v5 offset:16384\n"
"v_add_f32 v58, v58, v18\n"
"v_mfma_f32_16x16x32_bf16 v[232:235], a[88:91], a[104:107], v[232:235]\n"
"v_add_f32 v59, v59, v19\n"
"v_mfma_f32_16x16x32_bf16 v[232:235], a[92:95], a[108:111], v[232:235]\n"
"v_accvgpr_read_b32 v12, a0\n"
"v_mfma_f32_16x16x32_bf16 v[204:207], a[84:87], a[108:111], v[204:207]\n"
"ds_read_b128 a[108:111], v5 offset:19456\n"
"v_accvgpr_read_b32 v13, a1\n"
"v_mfma_f32_16x16x32_bf16 v[204:207], a[80:83], a[104:107], v[204:207]\n"
"ds_read_b128 a[104:107], v5 offset:18432\n"
"v_accvgpr_read_b32 v14, a2\n"
"v_mfma_f32_16x16x32_bf16 v[208:211], a[80:83], a[112:115], v[208:211]\n"
"v_accvgpr_read_b32 v15, a3\n"
"v_mfma_f32_16x16x32_bf16 v[208:211], a[84:87], a[116:119], v[208:211]\n"
"global_load_dwordx4 a[68:71], v0, s[34:35] offset:-3072 sc1\n"
"v_add_f32 v12, v12, v16\n"
"v_mfma_f32_16x16x32_bf16 v[236:239], a[92:95], a[116:119], v[236:239]\n"
"ds_read_b128 a[116:119], v5 offset:21504\n"
"v_add_f32 v13, v13, v17\n"
"v_mfma_f32_16x16x32_bf16 v[236:239], a[88:91], a[112:115], v[236:239]\n"
"ds_read_b128 a[112:115], v5 offset:20480\n"
"v_add_f32 v14, v14, v18\n"
"v_mfma_f32_16x16x32_bf16 v[240:243], a[88:91], a[120:123], v[240:243]\n"
"v_add_f32 v15, v15, v19\n"
"v_accvgpr_write_b32 a0, v12\n"
"v_mfma_f32_16x16x32_bf16 v[240:243], a[92:95], a[124:127], v[240:243]\n"
"v_accvgpr_write_b32 a1, v13\n"
"v_mfma_f32_16x16x32_bf16 v[212:215], a[84:87], a[124:127], v[212:215]\n"
"ds_read_b128 a[124:127], v5 offset:23552\n"
"v_accvgpr_write_b32 a2, v14\n"
"v_mfma_f32_16x16x32_bf16 v[212:215], a[80:83], a[120:123], v[212:215]\n"
"ds_read_b128 a[120:123], v5 offset:22528\n"
"v_accvgpr_write_b32 a3, v15\n"
"v_mfma_f32_16x16x32_bf16 v[216:219], a[80:83], a[128:131], v[216:219]\n"
"v_mfma_f32_16x16x32_bf16 v[216:219], a[84:87], a[132:135], v[216:219]\n"
"global_load_dwordx4 a[72:75], v0, s[34:35] offset:-2048 sc1\n"
"v_lshrrev_b32 v16, 4, v0\n"
"v_mfma_f32_16x16x32_bf16 v[244:247], a[92:95], a[132:135], v[244:247]\n"
"ds_read_b128 a[132:135], v5 offset:25600\n"
"v_and_b32 v16, 0x30, v16\n"
"v_mfma_f32_16x16x32_bf16 v[244:247], a[88:91], a[128:131], v[244:247]\n"
"ds_read_b128 a[128:131], v5 offset:24576\n"
"buffer_load_dwordx2 v[18:19], v16, s[44:47], s61 offen offset:64\n"
"v_mfma_f32_16x16x32_bf16 v[248:251], a[88:91], a[136:139], v[248:251]\n"
"v_mfma_f32_16x16x32_bf16 v[248:251], a[92:95], a[140:143], v[248:251]\n"
"v_mfma_f32_16x16x32_bf16 v[220:223], a[84:87], a[140:143], v[220:223]\n"
"ds_read_b128 a[140:143], v5 offset:27648\n"
"v_mfma_f32_16x16x32_bf16 v[220:223], a[80:83], a[136:139], v[220:223]\n"
"ds_read_b128 a[136:139], v5 offset:26624\n"
"v_mfma_f32_16x16x32_bf16 v[224:227], a[80:83], a[144:147], v[224:227]\n"
"v_mfma_f32_16x16x32_bf16 v[224:227], a[84:87], a[148:151], v[224:227]\n"
"global_load_dwordx4 a[76:79], v0, s[34:35] offset:-1024 sc1\n"
"v_mfma_f32_16x16x32_bf16 v[252:255], a[92:95], a[148:151], v[252:255]\n"
"ds_read_b128 a[148:151], v5 offset:29696\n"
"v_mfma_f32_16x16x32_bf16 v[252:255], a[88:91], a[144:147], v[252:255]\n"
"ds_read_b128 a[144:147], v5 offset:28672\n"
"v_mfma_f32_16x16x32_bf16 a[28:31], a[88:91], a[152:155], a[28:31]\n"
"v_mfma_f32_16x16x32_bf16 a[28:31], a[92:95], a[156:159], a[28:31]\n"
"v_mfma_f32_16x16x32_bf16 a[24:27], a[84:87], a[156:159], a[24:27]\n"
"ds_read_b128 a[156:159], v5 offset:31744\n"
"buffer_load_dwordx2 v[8:9], off, s[56:59], %14 sc1\n"
"v_mfma_f32_16x16x32_bf16 a[24:27], a[80:83], a[152:155], a[24:27]\n"
"s_waitcnt vmcnt(11) lgkmcnt(11)\n"
"ds_read_b128 a[152:155], v5 offset:30720\n"
"v_mfma_f32_16x16x32_bf16 v[32:35], a[32:35], a[96:99], v[32:35]\n"
"v_mfma_f32_16x16x32_bf16 v[32:35], a[36:39], a[100:103], v[32:35]\n"
"global_load_dwordx4 a[80:83], v0, s[34:35] offset:0 sc1\n"
"v_mfma_f32_16x16x32_bf16 v[60:63], a[44:47], a[100:103], v[60:63]\n"
"v_mfma_f32_16x16x32_bf16 v[60:63], a[40:43], a[96:99], v[60:63]\n"
"v_mfma_f32_16x16x32_bf16 v[64:67], a[40:43], a[104:107], v[64:67]\n"
"v_mfma_f32_16x16x32_bf16 v[64:67], a[44:47], a[108:111], v[64:67]\n"
"v_mfma_f32_16x16x32_bf16 v[36:39], a[36:39], a[108:111], v[36:39]\n"
"v_mfma_f32_16x16x32_bf16 v[36:39], a[32:35], a[104:107], v[36:39]\n"
"s_waitcnt lgkmcnt(8)\n"
"v_mfma_f32_16x16x32_bf16 v[40:43], a[32:35], a[112:115], v[40:43]\n"
"v_mfma_f32_16x16x32_bf16 v[40:43], a[36:39], a[116:119], v[40:43]\n"
"global_load_dwordx4 a[84:87], v0, s[34:35] offset:1024 sc1\n"
"v_mfma_f32_16x16x32_bf16 v[68:71], a[44:47], a[116:119], v[68:71]\n"
"v_mfma_f32_16x16x32_bf16 v[68:71], a[40:43], a[112:115], v[68:71]\n"
"v_mfma_f32_16x16x32_bf16 v[72:75], a[40:43], a[120:123], v[72:75]\n"
"v_mfma_f32_16x16x32_bf16 v[72:75], a[44:47], a[124:127], v[72:75]\n"
"v_mfma_f32_16x16x32_bf16 v[44:47], a[36:39], a[124:127], v[44:47]\n"
"v_mfma_f32_16x16x32_bf16 v[44:47], a[32:35], a[120:123], v[44:47]\n"
"s_waitcnt lgkmcnt(4)\n"
"v_mfma_f32_16x16x32_bf16 v[48:51], a[32:35], a[128:131], v[48:51]\n"
"v_mfma_f32_16x16x32_bf16 v[48:51], a[36:39], a[132:135], v[48:51]\n"
"global_load_dwordx4 a[88:91], v0, s[34:35] offset:2048 sc1\n"
"v_mfma_f32_16x16x32_bf16 v[76:79], a[44:47], a[132:135], v[76:79]\n"
"v_mfma_f32_16x16x32_bf16 v[76:79], a[40:43], a[128:131], v[76:79]\n"
"v_mfma_f32_16x16x32_bf16 v[80:83], a[40:43], a[136:139], v[80:83]\n"
"v_mfma_f32_16x16x32_bf16 v[80:83], a[44:47], a[140:143], v[80:83]\n"
"v_mfma_f32_16x16x32_bf16 v[52:55], a[36:39], a[140:143], v[52:55]\n"
"v_mfma_f32_16x16x32_bf16 v[52:55], a[32:35], a[136:139], v[52:55]\n"
"s_waitcnt lgkmcnt(0)\n"
"v_mfma_f32_16x16x32_bf16 v[56:59], a[32:35], a[144:147], v[56:59]\n"
"v_mfma_f32_16x16x32_bf16 v[56:59], a[36:39], a[148:151], v[56:59]\n"
"global_load_dwordx4 a[92:95], v0, s[34:35] offset:3072 sc1\n"
"v_mfma_f32_16x16x32_bf16 v[84:87], a[44:47], a[148:151], v[84:87]\n"
"v_mfma_f32_16x16x32_bf16 v[84:87], a[40:43], a[144:147], v[84:87]\n"
"v_mfma_f32_16x16x32_bf16 a[4:7], a[40:43], a[152:155], a[4:7]\n"
"s_add_u32 s34, s34, 8192\n"
"v_mfma_f32_16x16x32_bf16 a[4:7], a[44:47], a[156:159], a[4:7]\n"
"v_mfma_f32_16x16x32_bf16 a[0:3], a[36:39], a[156:159], a[0:3]\n"
"v_mfma_f32_16x16x32_bf16 a[0:3], a[32:35], a[152:155], a[0:3]\n"
"s_waitcnt vmcnt(10)\n"
"v_mfma_f32_16x16x32_bf16 v[88:91], a[48:51], a[96:99], v[88:91]\n"
"v_mfma_f32_16x16x32_bf16 v[88:91], a[52:55], a[100:103], v[88:91]\n"
"global_load_dwordx4 a[32:35], v0, s[34:35] offset:-4096 sc1\n"
"v_mfma_f32_16x16x32_bf16 v[116:119], a[60:63], a[100:103], v[116:119]\n"
"v_mfma_f32_16x16x32_bf16 v[116:119], a[56:59], a[96:99], v[116:119]\n"
"v_mfma_f32_16x16x32_bf16 v[120:123], a[56:59], a[104:107], v[120:123]\n"
"v_mfma_f32_16x16x32_bf16 v[120:123], a[60:63], a[108:111], v[120:123]\n"
"v_mfma_f32_16x16x32_bf16 v[92:95], a[52:55], a[108:111], v[92:95]\n"
"v_mfma_f32_16x16x32_bf16 v[92:95], a[48:51], a[104:107], v[92:95]\n"
"v_mfma_f32_16x16x32_bf16 v[96:99], a[48:51], a[112:115], v[96:99]\n"
"v_mfma_f32_16x16x32_bf16 v[96:99], a[52:55], a[116:119], v[96:99]\n"
"global_load_dwordx4 a[36:39], v0, s[34:35] offset:-3072 sc1\n"
"v_mfma_f32_16x16x32_bf16 v[124:127], a[60:63], a[116:119], v[124:127]\n"
"v_mfma_f32_16x16x32_bf16 v[124:127], a[56:59], a[112:115], v[124:127]\n"
"v_mfma_f32_16x16x32_bf16 v[128:131], a[56:59], a[120:123], v[128:131]\n"
"v_mfma_f32_16x16x32_bf16 v[128:131], a[60:63], a[124:127], v[128:131]\n"
"v_mfma_f32_16x16x32_bf16 v[100:103], a[52:55], a[124:127], v[100:103]\n"
"v_mfma_f32_16x16x32_bf16 v[100:103], a[48:51], a[120:123], v[100:103]\n"
"v_mfma_f32_16x16x32_bf16 v[104:107], a[48:51], a[128:131], v[104:107]\n"
"v_mfma_f32_16x16x32_bf16 v[104:107], a[52:55], a[132:135], v[104:107]\n"
"global_load_dwordx4 a[40:43], v0, s[34:35] offset:-2048 sc1\n"
"v_mfma_f32_16x16x32_bf16 v[132:135], a[60:63], a[132:135], v[132:135]\n"
"v_mfma_f32_16x16x32_bf16 v[132:135], a[56:59], a[128:131], v[132:135]\n"
"v_mfma_f32_16x16x32_bf16 v[136:139], a[56:59], a[136:139], v[136:139]\n"
"v_mfma_f32_16x16x32_bf16 v[136:139], a[60:63], a[140:143], v[136:139]\n"
"v_mfma_f32_16x16x32_bf16 v[108:111], a[52:55], a[140:143], v[108:111]\n"
"v_mfma_f32_16x16x32_bf16 v[108:111], a[48:51], a[136:139], v[108:111]\n"
"v_mfma_f32_16x16x32_bf16 v[112:115], a[48:51], a[144:147], v[112:115]\n"
"v_mfma_f32_16x16x32_bf16 v[112:115], a[52:55], a[148:151], v[112:115]\n"
"global_load_dwordx4 a[44:47], v0, s[34:35] offset:-1024 sc1\n"
"v_mfma_f32_16x16x32_bf16 v[140:143], a[60:63], a[148:151], v[140:143]\n"
"v_mfma_f32_16x16x32_bf16 v[140:143], a[56:59], a[144:147], v[140:143]\n"
"v_mfma_f32_16x16x32_bf16 a[12:15], a[56:59], a[152:155], a[12:15]\n"
"v_mfma_f32_16x16x32_bf16 a[12:15], a[60:63], a[156:159], a[12:15]\n"
"v_mfma_f32_16x16x32_bf16 a[8:11], a[52:55], a[156:159], a[8:11]\n"
"v_mfma_f32_16x16x32_bf16 a[8:11], a[48:51], a[152:155], a[8:11]\n"
"s_waitcnt vmcnt(9)\n"
"v_mfma_f32_16x16x32_bf16 v[144:147], a[64:67], a[96:99], v[144:147]\n"
"v_mfma_f32_16x16x32_bf16 v[144:147], a[68:71], a[100:103], v[144:147]\n"
"global_load_dwordx4 a[48:51], v0, s[34:35] offset:0 sc1\n"
"v_mfma_f32_16x16x32_bf16 v[172:175], a[76:79], a[100:103], v[172:175]\n"
"v_mfma_f32_16x16x32_bf16 v[172:175], a[72:75], a[96:99], v[172:175]\n"
"v_mfma_f32_16x16x32_bf16 v[176:179], a[72:75], a[104:107], v[176:179]\n"
"v_mfma_f32_16x16x32_bf16 v[176:179], a[76:79], a[108:111], v[176:179]\n"
"v_mfma_f32_16x16x32_bf16 v[148:151], a[68:71], a[108:111], v[148:151]\n"
"v_mfma_f32_16x16x32_bf16 v[148:151], a[64:67], a[104:107], v[148:151]\n"
"v_mfma_f32_16x16x32_bf16 v[152:155], a[64:67], a[112:115], v[152:155]\n"
"v_lshlrev_b32 v20, 16, v22\n"
"v_mfma_f32_16x16x32_bf16 v[152:155], a[68:71], a[116:119], v[152:155]\n"
"global_load_dwordx4 a[52:55], v0, s[34:35] offset:1024 sc1\n"
"v_and_b32 v21, s63, v22\n"
"v_mfma_f32_16x16x32_bf16 v[180:183], a[76:79], a[116:119], v[180:183]\n"
"v_lshlrev_b32 v22, 16, v23\n"
"v_mfma_f32_16x16x32_bf16 v[180:183], a[72:75], a[112:115], v[180:183]\n"
"v_and_b32 v23, s63, v23\n"
"v_mfma_f32_16x16x32_bf16 v[184:187], a[72:75], a[120:123], v[184:187]\n"
"v_add_f32 v60, v60, v20\n"
"v_mfma_f32_16x16x32_bf16 v[184:187], a[76:79], a[124:127], v[184:187]\n"
"v_add_f32 v61, v61, v21\n"
"v_add_f32 v62, v62, v22\n"
"v_mfma_f32_16x16x32_bf16 v[156:159], a[68:71], a[124:127], v[156:159]\n"
"v_add_f32 v63, v63, v23\n"
"v_mfma_f32_16x16x32_bf16 v[156:159], a[64:67], a[120:123], v[156:159]\n"
"v_add_f32 v64, v64, v20\n"
"v_mfma_f32_16x16x32_bf16 v[160:163], a[64:67], a[128:131], v[160:163]\n"
"v_add_f32 v65, v65, v21\n"
"v_mfma_f32_16x16x32_bf16 v[160:163], a[68:71], a[132:135], v[160:163]\n"
"global_load_dwordx4 a[56:59], v0, s[34:35] offset:2048 sc1\n"
"v_add_f32 v66, v66, v22\n"
"v_mfma_f32_16x16x32_bf16 v[188:191], a[76:79], a[132:135], v[188:191]\n"
"v_add_f32 v67, v67, v23\n"
"v_mfma_f32_16x16x32_bf16 v[188:191], a[72:75], a[128:131], v[188:191]\n"
"v_add_f32 v68, v68, v20\n"
"v_mfma_f32_16x16x32_bf16 v[192:195], a[72:75], a[136:139], v[192:195]\n"
"v_add_f32 v69, v69, v21\n"
"v_mfma_f32_16x16x32_bf16 v[192:195], a[76:79], a[140:143], v[192:195]\n"
"v_add_f32 v70, v70, v22\n"
"v_mfma_f32_16x16x32_bf16 v[164:167], a[68:71], a[140:143], v[164:167]\n"
"v_add_f32 v71, v71, v23\n"
"v_mfma_f32_16x16x32_bf16 v[164:167], a[64:67], a[136:139], v[164:167]\n"
"v_add_f32 v72, v72, v20\n"
"v_add_f32 v73, v73, v21\n"
"v_mfma_f32_16x16x32_bf16 v[168:171], a[64:67], a[144:147], v[168:171]\n"
"v_add_f32 v74, v74, v22\n"
"v_mfma_f32_16x16x32_bf16 v[168:171], a[68:71], a[148:151], v[168:171]\n"
"global_load_dwordx4 a[60:63], v0, s[34:35] offset:3072 sc1\n"
"v_add_f32 v75, v75, v23\n"
"v_mfma_f32_16x16x32_bf16 v[196:199], a[76:79], a[148:151], v[196:199]\n"
"v_add_f32 v76, v76, v20\n"
"v_mfma_f32_16x16x32_bf16 v[196:199], a[72:75], a[144:147], v[196:199]\n"
"v_add_f32 v77, v77, v21\n"
"v_mfma_f32_16x16x32_bf16 a[20:23], a[72:75], a[152:155], a[20:23]\n"
"s_add_u32 s34, s34, 8192\n"
"v_add_f32 v78, v78, v22\n"
"v_mfma_f32_16x16x32_bf16 a[20:23], a[76:79], a[156:159], a[20:23]\n"
"v_add_f32 v79, v79, v23\n"
"v_mfma_f32_16x16x32_bf16 a[16:19], a[68:71], a[156:159], a[16:19]\n"
"v_add_f32 v80, v80, v20\n"
"v_mfma_f32_16x16x32_bf16 a[16:19], a[64:67], a[152:155], a[16:19]\n"
"s_waitcnt vmcnt(8)\n"
"v_add_f32 v81, v81, v21\n"
"v_mfma_f32_16x16x32_bf16 v[200:203], a[80:83], a[96:99], v[200:203]\n"
"v_add_f32 v82, v82, v22\n"
"v_add_f32 v83, v83, v23\n"
"v_mfma_f32_16x16x32_bf16 v[200:203], a[84:87], a[100:103], v[200:203]\n"
"global_load_dwordx4 a[64:67], v0, s[34:35] offset:-4096 sc1\n"
"v_add_f32 v84, v84, v20\n"
"v_mfma_f32_16x16x32_bf16 v[228:231], a[92:95], a[100:103], v[228:231]\n"
"ds_read_b128 a[100:103], v2 offset:1024\n"
"v_add_f32 v85, v85, v21\n"
"v_mfma_f32_16x16x32_bf16 v[228:231], a[88:91], a[96:99], v[228:231]\n"
"ds_read_b128 a[96:99], v2 offset:0\n"
"v_add_f32 v86, v86, v22\n"
"v_mfma_f32_16x16x32_bf16 v[232:235], a[88:91], a[104:107], v[232:235]\n"
"v_add_f32 v87, v87, v23\n"
"v_mfma_f32_16x16x32_bf16 v[232:235], a[92:95], a[108:111], v[232:235]\n"
"v_accvgpr_read_b32 v12, a4\n"
"v_mfma_f32_16x16x32_bf16 v[204:207], a[84:87], a[108:111], v[204:207]\n"
"ds_read_b128 a[108:111], v2 offset:3072\n"
"v_accvgpr_read_b32 v13, a5\n"
"v_mfma_f32_16x16x32_bf16 v[204:207], a[80:83], a[104:107], v[204:207]\n"
"ds_read_b128 a[104:107], v2 offset:2048\n"
"v_accvgpr_read_b32 v14, a6\n"
"v_mfma_f32_16x16x32_bf16 v[208:211], a[80:83], a[112:115], v[208:211]\n"
"v_accvgpr_read_b32 v15, a7\n"
"v_mfma_f32_16x16x32_bf16 v[208:211], a[84:87], a[116:119], v[208:211]\n"
"global_load_dwordx4 a[68:71], v0, s[34:35] offset:-3072 sc1\n"
"v_add_f32 v12, v12, v20\n"
"v_mfma_f32_16x16x32_bf16 v[236:239], a[92:95], a[116:119], v[236:239]\n"
"ds_read_b128 a[116:119], v2 offset:5120\n"
"v_add_f32 v13, v13, v21\n"
"v_mfma_f32_16x16x32_bf16 v[236:239], a[88:91], a[112:115], v[236:239]\n"
"ds_read_b128 a[112:115], v2 offset:4096\n"
"v_add_f32 v14, v14, v22\n"
"v_mfma_f32_16x16x32_bf16 v[240:243], a[88:91], a[120:123], v[240:243]\n"
"v_add_f32 v15, v15, v23\n"
"v_accvgpr_write_b32 a4, v12\n"
"v_mfma_f32_16x16x32_bf16 v[240:243], a[92:95], a[124:127], v[240:243]\n"
"v_accvgpr_write_b32 a5, v13\n"
"v_mfma_f32_16x16x32_bf16 v[212:215], a[84:87], a[124:127], v[212:215]\n"
"ds_read_b128 a[124:127], v2 offset:7168\n"
"v_accvgpr_write_b32 a6, v14\n"
"v_mfma_f32_16x16x32_bf16 v[212:215], a[80:83], a[120:123], v[212:215]\n"
"ds_read_b128 a[120:123], v2 offset:6144\n"
"v_accvgpr_write_b32 a7, v15\n"
"v_mfma_f32_16x16x32_bf16 v[216:219], a[80:83], a[128:131], v[216:219]\n"
"v_mfma_f32_16x16x32_bf16 v[216:219], a[84:87], a[132:135], v[216:219]\n"
"global_load_dwordx4 a[72:75], v0, s[34:35] offset:-2048 sc1\n"
"v_lshrrev_b32 v20, 4, v0\n"
"v_mfma_f32_16x16x32_bf16 v[244:247], a[92:95], a[132:135], v[244:247]\n"
"ds_read_b128 a[132:135], v2 offset:9216\n"
"v_and_b32 v20, 0x30, v20\n"
"v_mfma_f32_16x16x32_bf16 v[244:247], a[88:91], a[128:131], v[244:247]\n"
"ds_read_b128 a[128:131], v2 offset:8192\n"
"buffer_load_dwordx2 v[22:23], v20, s[44:47], s61 offen offset:72\n"
"v_mfma_f32_16x16x32_bf16 v[248:251], a[88:91], a[136:139], v[248:251]\n"
"v_mfma_f32_16x16x32_bf16 v[248:251], a[92:95], a[140:143], v[248:251]\n"
"v_mfma_f32_16x16x32_bf16 v[220:223], a[84:87], a[140:143], v[220:223]\n"
"ds_read_b128 a[140:143], v2 offset:11264\n"
"v_mfma_f32_16x16x32_bf16 v[220:223], a[80:83], a[136:139], v[220:223]\n"
"ds_read_b128 a[136:139], v2 offset:10240\n"
"v_mfma_f32_16x16x32_bf16 v[224:227], a[80:83], a[144:147], v[224:227]\n"
"v_mfma_f32_16x16x32_bf16 v[224:227], a[84:87], a[148:151], v[224:227]\n"
"global_load_dwordx4 a[76:79], v0, s[34:35] offset:-1024 sc1\n"
"v_mfma_f32_16x16x32_bf16 v[252:255], a[92:95], a[148:151], v[252:255]\n"
"ds_read_b128 a[148:151], v2 offset:13312\n"
"v_mfma_f32_16x16x32_bf16 v[252:255], a[88:91], a[144:147], v[252:255]\n"
"ds_read_b128 a[144:147], v2 offset:12288\n"
"v_mfma_f32_16x16x32_bf16 a[28:31], a[88:91], a[152:155], a[28:31]\n"
"v_mfma_f32_16x16x32_bf16 a[28:31], a[92:95], a[156:159], a[28:31]\n"
"v_mfma_f32_16x16x32_bf16 a[24:27], a[84:87], a[156:159], a[24:27]\n"
"ds_read_b128 a[156:159], v2 offset:15360\n"
"v_mfma_f32_16x16x32_bf16 a[24:27], a[80:83], a[152:155], a[24:27]\n"
"ds_read_b128 a[152:155], v2 offset:14336\n"
"s_waitcnt vmcnt(9) lgkmcnt(12)\n"
"v_mfma_f32_16x16x32_bf16 v[32:35], a[32:35], a[96:99], v[32:35]\n"
"s_add_u32 s48, s48, 131072\n"
"s_addc_u32 s49, s49, 0\n"
"v_mfma_f32_16x16x32_bf16 v[32:35], a[36:39], a[100:103], v[32:35]\n"
"global_load_dwordx4 a[80:83], v0, s[34:35] offset:0 sc1\n"
"v_mfma_f32_16x16x32_bf16 v[60:63], a[44:47], a[100:103], v[60:63]\n"
"s_barrier\n"
"v_mfma_f32_16x16x32_bf16 v[60:63], a[40:43], a[96:99], v[60:63]\n"
"v_mfma_f32_16x16x32_bf16 v[64:67], a[40:43], a[104:107], v[64:67]\n"
"v_mfma_f32_16x16x32_bf16 v[64:67], a[44:47], a[108:111], v[64:67]\n"
"v_mfma_f32_16x16x32_bf16 v[36:39], a[36:39], a[108:111], v[36:39]\n"
"v_subrev_u32 v8, s65, v8\n"
"v_mfma_f32_16x16x32_bf16 v[36:39], a[32:35], a[104:107], v[36:39]\n"
"s_waitcnt lgkmcnt(8)\n"
"v_xor_b32 v9, s79, v9\n"
"v_mfma_f32_16x16x32_bf16 v[40:43], a[32:35], a[112:115], v[40:43]\n"
"v_or_b32 v8, v8, v9\n"
"v_mfma_f32_16x16x32_bf16 v[40:43], a[36:39], a[116:119], v[40:43]\n"
"global_load_dwordx4 a[84:87], v0, s[34:35] offset:1024 sc1\n"
"v_cmp_lt_u32 vcc, 15, v8\n"
"v_mfma_f32_16x16x32_bf16 v[68:71], a[44:47], a[116:119], v[68:71]\n"
"s_cbranch_vccnz L_slow1\n"
"L_back1:\n"
"v_mfma_f32_16x16x32_bf16 v[68:71], a[40:43], a[112:115], v[68:71]\n"
"v_mfma_f32_16x16x32_bf16 v[72:75], a[40:43], a[120:123], v[72:75]\n"
"v_mfma_f32_16x16x32_bf16 v[72:75], a[44:47], a[124:127], v[72:75]\n"
"v_mfma_f32_16x16x32_bf16 v[44:47], a[36:39], a[124:127], v[44:47]\n"
"v_mfma_f32_16x16x32_bf16 v[44:47], a[32:35], a[120:123], v[44:47]\n"
"s_waitcnt lgkmcnt(4)\n"
"v_mfma_f32_16x16x32_bf16 v[48:51], a[32:35], a[128:131], v[48:51]\n"
"v_mfma_f32_16x16x32_bf16 v[48:51], a[36:39], a[132:135], v[48:51]\n"
"global_load_dwordx4 a[88:91], v0, s[34:35] offset:2048 sc1\n"
"v_mfma_f32_16x16x32_bf16 v[76:79], a[44:47], a[132:135], v[76:79]\n"
"v_mfma_f32_16x16x32_bf16 v[76:79], a[40:43], a[128:131], v[76:79]\n"
"s_add_u32 m0, s64, 16384\n"
"v_mfma_f32_16x16x32_bf16 v[80:83], a[40:43], a[136:139], v[80:83]\n"
"v_mfma_f32_16x16x32_bf16 v[80:83], a[44:47], a[140:143], v[80:83]\n"
"v_mfma_f32_16x16x32_bf16 v[52:55], a[36:39], a[140:143], v[52:55]\n"
"v_mfma_f32_16x16x32_bf16 v[52:55], a[32:35], a[136:139], v[52:55]\n"
"s_waitcnt lgkmcnt(0)\n"
"v_mfma_f32_16x16x32_bf16 v[56:59], a[32:35], a[144:147], v[56:59]\n"
"v_mfma_f32_16x16x32_bf16 v[56:59], a[36:39], a[148:151], v[56:59]\n"
"global_load_dwordx4 a[92:95], v0, s[34:35] offset:3072 sc1\n"
"v_mfma_f32_16x16x32_bf16 v[84:87], a[44:47], a[148:151], v[84:87]\n"
"v_mfma_f32_16x16x32_bf16 v[84:87], a[40:43], a[144:147], v[84:87]\n"
"v_mfma_f32_16x16x32_bf16 a[4:7], a[40:43], a[152:155], a[4:7]\n"
"s_add_u32 s34, s34, 8192\n"
"v_mfma_f32_16x16x32_bf16 a[4:7], a[44:47], a[156:159], a[4:7]\n"
"v_mfma_f32_16x16x32_bf16 a[0:3], a[36:39], a[156:159], a[0:3]\n"
"v_mfma_f32_16x16x32_bf16 a[0:3], a[32:35], a[152:155], a[0:3]\n"
"s_waitcnt vmcnt(9)\n"
"v_mfma_f32_16x16x32_bf16 v[88:91], a[48:51], a[96:99], v[88:91]\n"
"v_mfma_f32_16x16x32_bf16 v[88:91], a[52:55], a[100:103], v[88:91]\n"
"global_load_dwordx4 a[32:35], v0, s[34:35] offset:-4096 sc1\n"
"v_mfma_f32_16x16x32_bf16 v[116:119], a[60:63], a[100:103], v[116:119]\n"
"buffer_load_dwordx4 v0, s[48:51], s75 offen offset:0 sc1 lds\n"
"v_mfma_f32_16x16x32_bf16 v[116:119], a[56:59], a[96:99], v[116:119]\n"
"v_mfma_f32_16x16x32_bf16 v[120:123], a[56:59], a[104:107], v[120:123]\n"
"v_mfma_f32_16x16x32_bf16 v[120:123], a[60:63], a[108:111], v[120:123]\n"
"v_mfma_f32_16x16x32_bf16 v[92:95], a[52:55], a[108:111], v[92:95]\n"
"v_mfma_f32_16x16x32_bf16 v[92:95], a[48:51], a[104:107], v[92:95]\n"
"v_mfma_f32_16x16x32_bf16 v[96:99], a[48:51], a[112:115], v[96:99]\n"
"v_mfma_f32_16x16x32_bf16 v[96:99], a[52:55], a[116:119], v[96:99]\n"
"global_load_dwordx4 a[36:39], v0, s[34:35] offset:-3072 sc1\n"
"v_mfma_f32_16x16x32_bf16 v[124:127], a[60:63], a[116:119], v[124:127]\n"
"v_mfma_f32_16x16x32_bf16 v[124:127], a[56:59], a[112:115], v[124:127]\n"
"v_mfma_f32_16x16x32_bf16 v[128:131], a[56:59], a[120:123], v[128:131]\n"
"v_mfma_f32_16x16x32_bf16 v[128:131], a[60:63], a[124:127], v[128:131]\n"
"v_mfma_f32_16x16x32_bf16 v[100:103], a[52:55], a[124:127], v[100:103]\n"
"v_mfma_f32_16x16x32_bf16 v[100:103], a[48:51], a[120:123], v[100:103]\n"
"v_mfma_f32_16x16x32_bf16 v[104:107], a[48:51], a[128:131], v[104:107]\n"
"v_mfma_f32_16x16x32_bf16 v[104:107], a[52:55], a[132:135], v[104:107]\n"
"global_load_dwordx4 a[40:43], v0, s[34:35] offset:-2048 sc1\n"
"v_mfma_f32_16x16x32_bf16 v[132:135], a[60:63], a[132:135], v[132:135]\n"
"buffer_load_dwordx4 v0, s[48:51], s75 offen offset:1024 sc1 lds\n"
"v_mfma_f32_16x16x32_bf16 v[132:135], a[56:59], a[128:131], v[132:135]\n"
"v_mfma_f32_16x16x32_bf16 v[136:139], a[56:59], a[136:139], v[136:139]\n"
"v_mfma_f32_16x16x32_bf16 v[136:139], a[60:63], a[140:143], v[136:139]\n"
"v_mfma_f32_16x16x32_bf16 v[108:111], a[52:55], a[140:143], v[108:111]\n"
"v_mfma_f32_16x16x32_bf16 v[108:111], a[48:51], a[136:139], v[108:111]\n"
"v_mfma_f32_16x16x32_bf16 v[112:115], a[48:51], a[144:147], v[112:115]\n"
"v_mfma_f32_16x16x32_bf16 v[112:115], a[52:55], a[148:151], v[112:115]\n"
"global_load_dwordx4 a[44:47], v0, s[34:35] offset:-1024 sc1\n"
"v_mfma_f32_16x16x32_bf16 v[140:143], a[60:63], a[148:151], v[140:143]\n"
"v_mfma_f32_16x16x32_bf16 v[140:143], a[56:59], a[144:147], v[140:143]\n"
"v_mfma_f32_16x16x32_bf16 a[12:15], a[56:59], a[152:155], a[12:15]\n"
"v_mfma_f32_16x16x32_bf16 a[12:15], a[60:63], a[156:159], a[12:15]\n"
"v_mfma_f32_16x16x32_bf16 a[8:11], a[52:55], a[156:159], a[8:11]\n"
"v_mfma_f32_16x16x32_bf16 a[8:11], a[48:51], a[152:155], a[8:11]\n"
"s_waitcnt vmcnt(10)\n"
"v_mfma_f32_16x16x32_bf16 v[144:147], a[64:67], a[96:99], v[144:147]\n"
"v_mfma_f32_16x16x32_bf16 v[144:147], a[68:71], a[100:103], v[144:147]\n"
"global_load_dwordx4 a[48:51], v0, s[34:35] offset:0 sc1\n"
"v_mfma_f32_16x16x32_bf16 v[172:175], a[76:79], a[100:103], v[172:175]\n"
"v_mfma_f32_16x16x32_bf16 v[172:175], a[72:75], a[96:99], v[172:175]\n"
"buffer_load_dwordx4 v0, s[48:51], s75 offen offset:2048 sc1 lds\n"
"v_mfma_f32_16x16x32_bf16 v[176:179], a[72:75], a[104:107], v[176:179]\n"
"v_mfma_f32_16x16x32_bf16 v[176:179], a[76:79], a[108:111], v[176:179]\n"
"v_mfma_f32_16x16x32_bf16 v[148:151], a[68:71], a[108:111], v[148:151]\n"
"v_mfma_f32_16x16x32_bf16 v[148:151], a[64:67], a[104:107], v[148:151]\n"
"v_mfma_f32_16x16x32_bf16 v[152:155], a[64:67], a[112:115], v[152:155]\n"
"v_lshlrev_b32 v16, 16, v18\n"
"v_mfma_f32_16x16x32_bf16 v[152:155], a[68:71], a[116:119], v[152:155]\n"
"global_load_dwordx4 a[52:55], v0, s[34:35] offset:1024 sc1\n"
"v_and_b32 v17, s63, v18\n"
"v_mfma_f32_16x16x32_bf16 v[180:183], a[76:79], a[116:119], v[180:183]\n"
"v_lshlrev_b32 v18, 16, v19\n"
"v_mfma_f32_16x16x32_bf16 v[180:183], a[72:75], a[112:115], v[180:183]\n"
"v_and_b32 v19, s63, v19\n"
"v_mfma_f32_16x16x32_bf16 v[184:187], a[72:75], a[120:123], v[184:187]\n"
"v_add_f32 v88, v88, v16\n"
"v_mfma_f32_16x16x32_bf16 v[184:187], a[76:79], a[124:127], v[184:187]\n"
"v_add_f32 v89, v89, v17\n"
"v_add_f32 v90, v90, v18\n"
"v_mfma_f32_16x16x32_bf16 v[156:159], a[68:71], a[124:127], v[156:159]\n"
"v_add_f32 v91, v91, v19\n"
"v_mfma_f32_16x16x32_bf16 v[156:159], a[64:67], a[120:123], v[156:159]\n"
"v_add_f32 v92, v92, v16\n"
"v_mfma_f32_16x16x32_bf16 v[160:163], a[64:67], a[128:131], v[160:163]\n"
"v_add_f32 v93, v93, v17\n"
"v_mfma_f32_16x16x32_bf16 v[160:163], a[68:71], a[132:135], v[160:163]\n"
"global_load_dwordx4 a[56:59], v0, s[34:35] offset:2048 sc1\n"
"v_add_f32 v94, v94, v18\n"
"v_mfma_f32_16x16x32_bf16 v[188:191], a[76:79], a[132:135], v[188:191]\n"
"buffer_load_dwordx4 v0, s[48:51], s75 offen offset:3072 sc1 lds\n"
"v_mfma_f32_16x16x32_bf16 v[188:191], a[72:75], a[128:131], v[188:191]\n"
"v_add_f32 v95, v95, v19\n"
"v_add_f32 v96, v96, v16\n"
"v_mfma_f32_16x16x32_bf16 v[192:195], a[72:75], a[136:139], v[192:195]\n"
"v_add_f32 v97, v97, v17\n"
"v_mfma_f32_16x16x32_bf16 v[192:195], a[76:79], a[140:143], v[192:195]\n"
"v_add_f32 v98, v98, v18\n"
"v_mfma_f32_16x16x32_bf16 v[164:167], a[68:71], a[140:143], v[164:167]\n"
"v_add_f32 v99, v99, v19\n"
"v_mfma_f32_16x16x32_bf16 v[164:167], a[64:67], a[136:139], v[164:167]\n"
"v_add_f32 v100, v100, v16\n"
"v_add_f32 v101, v101, v17\n"
"v_mfma_f32_16x16x32_bf16 v[168:171], a[64:67], a[144:147], v[168:171]\n"
"v_add_f32 v102, v102, v18\n"
"v_mfma_f32_16x16x32_bf16 v[168:171], a[68:71], a[148:151], v[168:171]\n"
"global_load_dwordx4 a[60:63], v0, s[34:35] offset:3072 sc1\n"
"v_add_f32 v103, v103, v19\n"
"v_mfma_f32_16x16x32_bf16 v[196:199], a[76:79], a[148:151], v[196:199]\n"
"v_add_f32 v104, v104, v16\n"
"v_mfma_f32_16x16x32_bf16 v[196:199], a[72:75], a[144:147], v[196:199]\n"
"v_add_f32 v105, v105, v17\n"
"v_mfma_f32_16x16x32_bf16 a[20:23], a[72:75], a[152:155], a[20:23]\n"
"s_add_u32 s34, s34, 8192\n"
"v_add_f32 v106, v106, v18\n"
"v_mfma_f32_16x16x32_bf16 a[20:23], a[76:79], a[156:159], a[20:23]\n"
"v_add_f32 v107, v107, v19\n"
"v_mfma_f32_16x16x32_bf16 a[16:19], a[68:71], a[156:159], a[16:19]\n"
"v_add_f32 v108, v108, v16\n"
"v_mfma_f32_16x16x32_bf16 a[16:19], a[64:67], a[152:155], a[16:19]\n"
"s_waitcnt vmcnt(12)\n"
"v_add_f32 v109, v109, v17\n"
"v_mfma_f32_16x16x32_bf16 v[200:203], a[80:83], a[96:99], v[200:203]\n"
"v_add_f32 v110, v110, v18\n"
"v_add_f32 v111, v111, v19\n"
"v_mfma_f32_16x16x32_bf16 v[200:203], a[84:87], a[100:103], v[200:203]\n"
"global_load_dwordx4 a[64:67], v0, s[34:35] offset:-4096 sc1\n"
"v_add_f32 v112, v112, v16\n"
"v_mfma_f32_16x16x32_bf16 v[228:231], a[92:95], a[100:103], v[228:231]\n"
"ds_read_b128 a[100:103], v3 offset:1024\n"
"v_add_f32 v113, v113, v17\n"
"v_mfma_f32_16x16x32_bf16 v[228:231], a[88:91], a[96:99], v[228:231]\n"
"s_add_u32 m0, s64, 20480\n"
"ds_read_b128 a[96:99], v3 offset:0\n"
"v_add_f32 v114, v114, v18\n"
"v_mfma_f32_16x16x32_bf16 v[232:235], a[88:91], a[104:107], v[232:235]\n"
"v_add_f32 v115, v115, v19\n"
"v_mfma_f32_16x16x32_bf16 v[232:235], a[92:95], a[108:111], v[232:235]\n"
"v_accvgpr_read_b32 v12, a8\n"
"v_mfma_f32_16x16x32_bf16 v[204:207], a[84:87], a[108:111], v[204:207]\n"
"ds_read_b128 a[108:111], v3 offset:3072\n"
"v_accvgpr_read_b32 v13, a9\n"
"v_mfma_f32_16x16x32_bf16 v[204:207], a[80:83], a[104:107], v[204:207]\n"
"ds_read_b128 a[104:107], v3 offset:2048\n"
"v_accvgpr_read_b32 v14, a10\n"
"v_mfma_f32_16x16x32_bf16 v[208:211], a[80:83], a[112:115], v[208:211]\n"
"v_accvgpr_read_b32 v15, a11\n"
"v_mfma_f32_16x16x32_bf16 v[208:211], a[84:87], a[116:119], v[208:211]\n"
"global_load_dwordx4 a[68:71], v0, s[34:35] offset:-3072 sc1\n"
"v_add_f32 v12, v12, v16\n"
"v_mfma_f32_16x16x32_bf16 v[236:239], a[92:95], a[116:119], v[236:239]\n"
"ds_read_b128 a[116:119], v3 offset:5120\n"
"v_add_f32 v13, v13, v17\n"
"v_mfma_f32_16x16x32_bf16 v[236:239], a[88:91], a[112:115], v[236:239]\n"
"ds_read_b128 a[112:115], v3 offset:4096\n"
"v_add_f32 v14, v14, v18\n"
"v_mfma_f32_16x16x32_bf16 v[240:243], a[88:91], a[120:123], v[240:243]\n"
"v_add_f32 v15, v15, v19\n"
"v_accvgpr_write_b32 a8, v12\n"
"v_mfma_f32_16x16x32_bf16 v[240:243], a[92:95], a[124:127], v[240:243]\n"
"v_accvgpr_write_b32 a9, v13\n"
"v_mfma_f32_16x16x32_bf16 v[212:215], a[84:87], a[124:127], v[212:215]\n"
"ds_read_b128 a[124:127], v3 offset:7168\n"
"v_accvgpr_write_b32 a10, v14\n"
"v_mfma_f32_16x16x32_bf16 v[212:215], a[80:83], a[120:123], v[212:215]\n"
"ds_read_b128 a[120:123], v3 offset:6144\n"
"v_accvgpr_write_b32 a11, v15\n"
"v_mfma_f32_16x16x32_bf16 v[216:219], a[80:83], a[128:131], v[216:219]\n"
"v_mfma_f32_16x16x32_bf16 v[216:219], a[84:87], a[132:135], v[216:219]\n"
"global_load_dwordx4 a[72:75], v0, s[34:35] offset:-2048 sc1\n"
"v_mfma_f32_16x16x32_bf16 v[244:247], a[92:95], a[132:135], v[244:247]\n"
"ds_read_b128 a[132:135], v3 offset:9216\n"
"v_mfma_f32_16x16x32_bf16 v[244:247], a[88:91], a[128:131], v[244:247]\n"
"ds_read_b128 a[128:131], v3 offset:8192\n"
"v_mfma_f32_16x16x32_bf16 v[248:251], a[88:91], a[136:139], v[248:251]\n"
"buffer_load_dwordx4 v1, s[48:51], s75 offen offset:0 sc1 lds\n"
"v_mfma_f32_16x16x32_bf16 v[248:251], a[92:95], a[140:143], v[248:251]\n"
"v_mfma_f32_16x16x32_bf16 v[220:223], a[84:87], a[140:143], v[220:223]\n"
"ds_read_b128 a[140:143], v3 offset:11264\n"
"v_mfma_f32_16x16x32_bf16 v[220:223], a[80:83], a[136:139], v[220:223]\n"
"ds_read_b128 a[136:139], v3 offset:10240\n"
"v_mfma_f32_16x16x32_bf16 v[224:227], a[80:83], a[144:147], v[224:227]\n"
"v_mfma_f32_16x16x32_bf16 v[224:227], a[84:87], a[148:151], v[224:227]\n"
"global_load_dwordx4 a[76:79], v0, s[34:35] offset:-1024 sc1\n"
"v_mfma_f32_16x16x32_bf16 v[252:255], a[92:95], a[148:151], v[252:255]\n"
"ds_read_b128 a[148:151], v3 offset:13312\n"
"v_mfma_f32_16x16x32_bf16 v[252:255], a[88:91], a[144:147], v[252:255]\n"
"ds_read_b128 a[144:147], v3 offset:12288\n"
"v_mfma_f32_16x16x32_bf16 a[28:31], a[88:91], a[152:155], a[28:31]\n"
"v_mfma_f32_16x16x32_bf16 a[28:31], a[92:95], a[156:159], a[28:31]\n"
"v_mfma_f32_16x16x32_bf16 a[24:27], a[84:87], a[156:159], a[24:27]\n"
"ds_read_b128 a[156:159], v3 offset:15360\n"
"v_mfma_f32_16x16x32_bf16 a[24:27], a[80:83], a[152:155], a[24:27]\n"
"s_waitcnt vmcnt(11) lgkmcnt(11)\n"
"ds_read_b128 a[152:155], v3 offset:14336\n"
"v_mfma_f32_16x16x32_bf16 v[32:35], a[32:35], a[96:99], v[32:35]\n"
"v_mfma_f32_16x16x32_bf16 v[32:35], a[36:39], a[100:103], v[32:35]\n"
"global_load_dwordx4 a[80:83], v0, s[34:35] offset:0 sc1\n"
"v_mfma_f32_16x16x32_bf16 v[60:63], a[44:47], a[100:103], v[60:63]\n"
"v_mfma_f32_16x16x32_bf16 v[60:63], a[40:43], a[96:99], v[60:63]\n"
"buffer_load_dwordx4 v1, s[48:51], s75 offen offset:1024 sc1 lds\n"
"v_mfma_f32_16x16x32_bf16 v[64:67], a[40:43], a[104:107], v[64:67]\n"
"v_mfma_f32_16x16x32_bf16 v[64:67], a[44:47], a[108:111], v[64:67]\n"
"v_mfma_f32_16x16x32_bf16 v[36:39], a[36:39], a[108:111], v[36:39]\n"
"v_mfma_f32_16x16x32_bf16 v[36:39], a[32:35], a[104:107], v[36:39]\n"
"s_waitcnt lgkmcnt(8)\n"
"v_mfma_f32_16x16x32_bf16 v[40:43], a[32:35], a[112:115], v[40:43]\n"
"v_mfma_f32_16x16x32_bf16 v[40:43], a[36:39], a[116:119], v[40:43]\n"
"global_load_dwordx4 a[84:87], v0, s[34:35] offset:1024 sc1\n"
"v_mfma_f32_16x16x32_bf16 v[68:71], a[44:47], a[116:119], v[68:71]\n"
"v_mfma_f32_16x16x32_bf16 v[68:71], a[40:43], a[112:115], v[68:71]\n"
"v_mfma_f32_16x16x32_bf16 v[72:75], a[40:43], a[120:123], v[72:75]\n"
"v_mfma_f32_16x16x32_bf16 v[72:75], a[44:47], a[124:127], v[72:75]\n"
"v_mfma_f32_16x16x32_bf16 v[44:47], a[36:39], a[124:127], v[44:47]\n"
"v_mfma_f32_16x16x32_bf16 v[44:47], a[32:35], a[120:123], v[44:47]\n"
"s_waitcnt lgkmcnt(4)\n"
"v_mfma_f32_16x16x32_bf16 v[48:51], a[32:35], a[128:131], v[48:51]\n"
"v_mfma_f32_16x16x32_bf16 v[48:51], a[36:39], a[132:135], v[48:51]\n"
"global_load_dwordx4 a[88:91], v0, s[34:35] offset:2048 sc1\n"
"v_mfma_f32_16x16x32_bf16 v[76:79], a[44:47], a[132:135], v[76:79]\n"
"v_mfma_f32_16x16x32_bf16 v[76:79], a[40:43], a[128:131], v[76:79]\n"
"buffer_load_dwordx4 v1, s[48:51], s75 offen offset:2048 sc1 lds\n"
"v_mfma_f32_16x16x32_bf16 v[80:83], a[40:43], a[136:139], v[80:83]\n"
"v_mfma_f32_16x16x32_bf16 v[80:83], a[44:47], a[140:143], v[80:83]\n"
"v_mfma_f32_16x16x32_bf16 v[52:55], a[36:39], a[140:143], v[52:55]\n"
"v_mfma_f32_16x16x32_bf16 v[52:55], a[32:35], a[136:139], v[52:55]\n"
"s_waitcnt lgkmcnt(0)\n"
"v_mfma_f32_16x16x32_bf16 v[56:59], a[32:35], a[144:147], v[56:59]\n"
"v_mfma_f32_16x16x32_bf16 v[56:59], a[36:39], a[148:151], v[56:59]\n"
"global_load_dwordx4 a[92:95], v0, s[34:35] offset:3072 sc1\n"
"v_mfma_f32_16x16x32_bf16 v[84:87], a[44:47], a[148:151], v[84:87]\n"
"v_mfma_f32_16x16x32_bf16 v[84:87], a[40:43], a[144:147], v[84:87]\n"
"v_mfma_f32_16x16x32_bf16 a[4:7], a[40:43], a[152:155], a[4:7]\n"
"s_add_u32 s34, s34, 8192\n"
"v_mfma_f32_16x16x32_bf16 a[4:7], a[44:47], a[156:159], a[4:7]\n"
"v_mfma_f32_16x16x32_bf16 a[0:3], a[36:39], a[156:159], a[0:3]\n"
"v_mfma_f32_16x16x32_bf16 a[0:3], a[32:35], a[152:155], a[0:3]\n"
"s_waitcnt vmcnt(11)\n"
"v_mfma_f32_16x16x32_bf16 v[88:91], a[48:51], a[96:99], v[88:91]\n"
"v_mfma_f32_16x16x32_bf16 v[88:91], a[52:55], a[100:103], v[88:91]\n"
"global_load_dwordx4 a[32:35], v0, s[34:35] offset:-4096 sc1\n"
"v_mfma_f32_16x16x32_bf16 v[116:119], a[60:63], a[100:103], v[116:119]\n"
"v_mfma_f32_16x16x32_bf16 v[116:119], a[56:59], a[96:99], v[116:119]\n"
"buffer_load_dwordx4 v1, s[48:51], s75 offen offset:3072 sc1 lds\n"
"v_mfma_f32_16x16x32_bf16 v[120:123], a[56:59], a[104:107], v[120:123]\n"
"v_mfma_f32_16x16x32_bf16 v[120:123], a[60:63], a[108:111], v[120:123]\n"
"v_mfma_f32_16x16x32_bf16 v[92:95], a[52:55], a[108:111], v[92:95]\n"
"v_mfma_f32_16x16x32_bf16 v[92:95], a[48:51], a[104:107], v[92:95]\n"
"v_mfma_f32_16x16x32_bf16 v[96:99], a[48:51], a[112:115], v[96:99]\n"
"v_mfma_f32_16x16x32_bf16 v[96:99], a[52:55], a[116:119], v[96:99]\n"
"global_load_dwordx4 a[36:39], v0, s[34:35] offset:-3072 sc1\n"
"v_mfma_f32_16x16x32_bf16 v[124:127], a[60:63], a[116:119], v[124:127]\n"
"v_mfma_f32_16x16x32_bf16 v[124:127], a[56:59], a[112:115], v[124:127]\n"
"v_mfma_f32_16x16x32_bf16 v[128:131], a[56:59], a[120:123], v[128:131]\n"
"v_mfma_f32_16x16x32_bf16 v[128:131], a[60:63], a[124:127], v[128:131]\n"
"v_mfma_f32_16x16x32_bf16 v[100:103], a[52:55], a[124:127], v[100:103]\n"
"v_mfma_f32_16x16x32_bf16 v[100:103], a[48:51], a[120:123], v[100:103]\n"
"v_mfma_f32_16x16x32_bf16 v[104:107], a[48:51], a[128:131], v[104:107]\n"
"v_mfma_f32_16x16x32_bf16 v[104:107], a[52:55], a[132:135], v[104:107]\n"
"global_load_dwordx4 a[40:43], v0, s[34:35] offset:-2048 sc1\n"
"v_mfma_f32_16x16x32_bf16 v[132:135], a[60:63], a[132:135], v[132:135]\n"
"v_mfma_f32_16x16x32_bf16 v[132:135], a[56:59], a[128:131], v[132:135]\n"
"s_add_u32 m0, s64, 24576\n"
"v_mfma_f32_16x16x32_bf16 v[136:139], a[56:59], a[136:139], v[136:139]\n"
"v_mfma_f32_16x16x32_bf16 v[136:139], a[60:63], a[140:143], v[136:139]\n"
"v_mfma_f32_16x16x32_bf16 v[108:111], a[52:55], a[140:143], v[108:111]\n"
"v_mfma_f32_16x16x32_bf16 v[108:111], a[48:51], a[136:139], v[108:111]\n"
"v_mfma_f32_16x16x32_bf16 v[112:115], a[48:51], a[144:147], v[112:115]\n"
"v_mfma_f32_16x16x32_bf16 v[112:115], a[52:55], a[148:151], v[112:115]\n"
"global_load_dwordx4 a[44:47], v0, s[34:35] offset:-1024 sc1\n"
"v_mfma_f32_16x16x32_bf16 v[140:143], a[60:63], a[148:151], v[140:143]\n"
"v_mfma_f32_16x16x32_bf16 v[140:143], a[56:59], a[144:147], v[140:143]\n"
"v_mfma_f32_16x16x32_bf16 a[12:15], a[56:59], a[152:155], a[12:15]\n"
"v_mfma_f32_16x16x32_bf16 a[12:15], a[60:63], a[156:159], a[12:15]\n"
"v_mfma_f32_16x16x32_bf16 a[8:11], a[52:55], a[156:159], a[8:11]\n"
"v_mfma_f32_16x16x32_bf16 a[8:11], a[48:51], a[152:155], a[8:11]\n"
"s_waitcnt vmcnt(11)\n"
"v_mfma_f32_16x16x32_bf16 v[144:147], a[64:67], a[96:99], v[144:147]\n"
"v_mfma_f32_16x16x32_bf16 v[144:147], a[68:71], a[100:103], v[144:147]\n"
"global_load_dwordx4 a[48:51], v0, s[34:35] offset:0 sc1\n"
"v_mfma_f32_16x16x32_bf16 v[172:175], a[76:79], a[100:103], v[172:175]\n"
"v_mfma_f32_16x16x32_bf16 v[172:175], a[72:75], a[96:99], v[172:175]\n"
"buffer_load_dwordx4 v0, s[48:51], s69 offen offset:0 sc1 lds\n"
"v_mfma_f32_16x16x32_bf16 v[176:179], a[72:75], a[104:107], v[176:179]\n"
"v_mfma_f32_16x16x32_bf16 v[176:179], a[76:79], a[108:111], v[176:179]\n"
"v_mfma_f32_16x16x32_bf16 v[148:151], a[68:71], a[108:111], v[148:151]\n"
"v_mfma_f32_16x16x32_bf16 v[148:151], a[64:67], a[104:107], v[148:151]\n"
"v_mfma_f32_16x16x32_bf16 v[152:155], a[64:67], a[112:115], v[152:155]\n"
"v_lshlrev_b32 v20, 16, v22\n"
"v_mfma_f32_16x16x32_bf16 v[152:155], a[68:71], a[116:119], v[152:155]\n"
"global_load_dwordx4 a[52:55], v0, s[34:35] offset:1024 sc1\n"
"v_and_b32 v21, s63, v22\n"
"v_mfma_f32_16x16x32_bf16 v[180:183], a[76:79], a[116:119], v[180:183]\n"
"v_lshlrev_b32 v22, 16, v23\n"
"v_mfma_f32_16x16x32_bf16 v[180:183], a[72:75], a[112:115], v[180:183]\n"
"v_and_b32 v23, s63, v23\n"
"v_mfma_f32_16x16x32_bf16 v[184:187], a[72:75], a[120:123], v[184:187]\n"
"v_add_f32 v116, v116, v20\n"
"v_mfma_f32_16x16x32_bf16 v[184:187], a[76:79], a[124:127], v[184:187]\n"
"v_add_f32 v117, v117, v21\n"
"v_add_f32 v118, v118, v22\n"
"v_mfma_f32_16x16x32_bf16 v[156:159], a[68:71], a[124:127], v[156:159]\n"
"v_add_f32 v119, v119, v23\n"
"v_mfma_f32_16x16x32_bf16 v[156:159], a[64:67], a[120:123], v[156:159]\n"
"v_add_f32 v120, v120, v20\n"
"v_mfma_f32_16x16x32_bf16 v[160:163], a[64:67], a[128:131], v[160:163]\n"
"v_add_f32 v121, v121, v21\n"
"v_mfma_f32_16x16x32_bf16 v[160:163], a[68:71], a[132:135], v[160:163]\n"
"global_load_dwordx4 a[56:59], v0, s[34:35] offset:2048 sc1\n"
"v_add_f32 v122, v122, v22\n"
"v_mfma_f32_16x16x32_bf16 v[188:191], a[76:79], a[132:135], v[188:191]\n"
"v_add_f32 v123, v123, v23\n"
"v_add_f32 v124, v124, v20\n"
"v_mfma_f32_16x16x32_bf16 v[188:191], a[72:75], a[128:131], v[188:191]\n"
"buffer_load_dwordx4 v0, s[48:51], s69 offen offset:1024 sc1 lds\n"
"v_mfma_f32_16x16x32_bf16 v[192:195], a[72:75], a[136:139], v[192:195]\n"
"v_add_f32 v125, v125, v21\n"
"v_mfma_f32_16x16x32_bf16 v[192:195], a[76:79], a[140:143], v[192:195]\n"
"v_add_f32 v126, v126, v22\n"
"v_mfma_f32_16x16x32_bf16 v[164:167], a[68:71], a[140:143], v[164:167]\n"
"v_add_f32 v127, v127, v23\n"
"v_mfma_f32_16x16x32_bf16 v[164:167], a[64:67], a[136:139], v[164:167]\n"
"v_add_f32 v128, v128, v20\n"
"v_add_f32 v129, v129, v21\n"
"v_mfma_f32_16x16x32_bf16 v[168:171], a[64:67], a[144:147], v[168:171]\n"
"v_add_f32 v130, v130, v22\n"
"v_mfma_f32_16x16x32_bf16 v[168:171], a[68:71], a[148:151], v[168:171]\n"
"global_load_dwordx4 a[60:63], v0, s[34:35] offset:3072 sc1\n"
"v_add_f32 v131, v131, v23\n"
"v_mfma_f32_16x16x32_bf16 v[196:199], a[76:79], a[148:151], v[196:199]\n"
"v_add_f32 v132, v132, v20\n"
"v_mfma_f32_16x16x32_bf16 v[196:199], a[72:75], a[144:147], v[196:199]\n"
"v_add_f32 v133, v133, v21\n"
"v_mfma_f32_16x16x32_bf16 a[20:23], a[72:75], a[152:155], a[20:23]\n"
"s_add_u32 s34, s34, 8192\n"
"v_add_f32 v134, v134, v22\n"
"v_mfma_f32_16x16x32_bf16 a[20:23], a[76:79], a[156:159], a[20:23]\n"
"v_add_f32 v135, v135, v23\n"
"v_mfma_f32_16x16x32_bf16 a[16:19], a[68:71], a[156:159], a[16:19]\n"
"v_add_f32 v136, v136, v20\n"
"v_mfma_f32_16x16x32_bf16 a[16:19], a[64:67], a[152:155], a[16:19]\n"
"s_waitcnt vmcnt(11)\n"
"v_add_f32 v137, v137, v21\n"
"v_mfma_f32_16x16x32_bf16 v[200:203], a[80:83], a[96:99], v[200:203]\n"
"v_add_f32 v138, v138, v22\n"
"v_add_f32 v139, v139, v23\n"
"v_mfma_f32_16x16x32_bf16 v[200:203], a[84:87], a[100:103], v[200:203]\n"
"global_load_dwordx4 a[64:67], v0, s[34:35] offset:-4096 sc1\n"
"v_add_f32 v140, v140, v20\n"
"v_mfma_f32_16x16x32_bf16 v[228:231], a[92:95], a[100:103], v[228:231]\n"
"ds_read_b128 a[100:103], v4 offset:1024\n"
"v_add_f32 v141, v141, v21\n"
"v_mfma_f32_16x16x32_bf16 v[228:231], a[88:91], a[96:99], v[228:231]\n"
"ds_read_b128 a[96:99], v4 offset:0\n"
"v_add_f32 v142, v142, v22\n"
"v_mfma_f32_16x16x32_bf16 v[232:235], a[88:91], a[104:107], v[232:235]\n"
"buffer_load_dwordx4 v0, s[48:51], s69 offen offset:2048 sc1 lds\n"
"v_mfma_f32_16x16x32_bf16 v[232:235], a[92:95], a[108:111], v[232:235]\n"
"v_add_f32 v143, v143, v23\n"
"v_accvgpr_read_b32 v12, a12\n"
"v_mfma_f32_16x16x32_bf16 v[204:207], a[84:87], a[108:111], v[204:207]\n"
"ds_read_b128 a[108:111], v4 offset:3072\n"
"v_accvgpr_read_b32 v13, a13\n"
"v_mfma_f32_16x16x32_bf16 v[204:207], a[80:83], a[104:107], v[204:207]\n"
"ds_read_b128 a[104:107], v4 offset:2048\n"
"v_accvgpr_read_b32 v14, a14\n"
"v_mfma_f32_16x16x32_bf16 v[208:211], a[80:83], a[112:115], v[208:211]\n"
"v_accvgpr_read_b32 v15, a15\n"
"v_mfma_f32_16x16x32_bf16 v[208:211], a[84:87], a[116:119], v[208:211]\n"
"global_load_dwordx4 a[68:71], v0, s[34:35] offset:-3072 sc1\n"
"v_add_f32 v12, v12, v20\n"
"v_mfma_f32_16x16x32_bf16 v[236:239], a[92:95], a[116:119], v[236:239]\n"
"ds_read_b128 a[116:119], v4 offset:5120\n"
"v_add_f32 v13, v13, v21\n"
"v_mfma_f32_16x16x32_bf16 v[236:239], a[88:91], a[112:115], v[236:239]\n"
"ds_read_b128 a[112:115], v4 offset:4096\n"
"v_add_f32 v14, v14, v22\n"
"v_mfma_f32_16x16x32_bf16 v[240:243], a[88:91], a[120:123], v[240:243]\n"
"v_add_f32 v15, v15, v23\n"
"v_accvgpr_write_b32 a12, v12\n"
"v_mfma_f32_16x16x32_bf16 v[240:243], a[92:95], a[124:127], v[240:243]\n"
"v_accvgpr_write_b32 a13, v13\n"
"v_mfma_f32_16x16x32_bf16 v[212:215], a[84:87], a[124:127], v[212:215]\n"
"ds_read_b128 a[124:127], v4 offset:7168\n"
"v_accvgpr_write_b32 a14, v14\n"
"v_mfma_f32_16x16x32_bf16 v[212:215], a[80:83], a[120:123], v[212:215]\n"
"ds_read_b128 a[120:123], v4 offset:6144\n"
"v_accvgpr_write_b32 a15, v15\n"
"v_mfma_f32_16x16x32_bf16 v[216:219], a[80:83], a[128:131], v[216:219]\n"
"v_mfma_f32_16x16x32_bf16 v[216:219], a[84:87], a[132:135], v[216:219]\n"
"global_load_dwordx4 a[72:75], v0, s[34:35] offset:-2048 sc1\n"
"v_mfma_f32_16x16x32_bf16 v[244:247], a[92:95], a[132:135], v[244:247]\n"
"ds_read_b128 a[132:135], v4 offset:9216\n"
"v_mfma_f32_16x16x32_bf16 v[244:247], a[88:91], a[128:131], v[244:247]\n"
"ds_read_b128 a[128:131], v4 offset:8192\n"
"v_mfma_f32_16x16x32_bf16 v[248:251], a[88:91], a[136:139], v[248:251]\n"
"buffer_load_dwordx4 v0, s[48:51], s69 offen offset:3072 sc1 lds\n"
"v_mfma_f32_16x16x32_bf16 v[248:251], a[92:95], a[140:143], v[248:251]\n"
"v_mfma_f32_16x16x32_bf16 v[220:223], a[84:87], a[140:143], v[220:223]\n"
"ds_read_b128 a[140:143], v4 offset:11264\n"
"v_mfma_f32_16x16x32_bf16 v[220:223], a[80:83], a[136:139], v[220:223]\n"
"ds_read_b128 a[136:139], v4 offset:10240\n"
"v_mfma_f32_16x16x32_bf16 v[224:227], a[80:83], a[144:147], v[224:227]\n"
"v_mfma_f32_16x16x32_bf16 v[224:227], a[84:87], a[148:151], v[224:227]\n"
"global_load_dwordx4 a[76:79], v0, s[34:35] offset:-1024 sc1\n"
"v_mfma_f32_16x16x32_bf16 v[252:255], a[92:95], a[148:151], v[252:255]\n"
"ds_read_b128 a[148:151], v4 offset:13312\n"
"v_mfma_f32_16x16x32_bf16 v[252:255], a[88:91], a[144:147], v[252:255]\n"
"ds_read_b128 a[144:147], v4 offset:12288\n"
"v_mfma_f32_16x16x32_bf16 a[28:31], a[88:91], a[152:155], a[28:31]\n"
"v_mfma_f32_16x16x32_bf16 a[28:31], a[92:95], a[156:159], a[28:31]\n"
"v_mfma_f32_16x16x32_bf16 a[24:27], a[84:87], a[156:159], a[24:27]\n"
"ds_read_b128 a[156:159], v4 offset:15360\n"
"v_mfma_f32_16x16x32_bf16 a[24:27], a[80:83], a[152:155], a[24:27]\n"
"s_waitcnt vmcnt(12) lgkmcnt(11)\n"
"ds_read_b128 a[152:155], v4 offset:14336\n"
"v_mfma_f32_16x16x32_bf16 v[32:35], a[32:35], a[96:99], v[32:35]\n"
"v_mfma_f32_16x16x32_bf16 v[32:35], a[36:39], a[100:103], v[32:35]\n"
"global_load_dwordx4 a[80:83], v0, s[34:35] offset:0 sc1\n"
"v_lshrrev_b32 v16, 4, v0\n"
"v_mfma_f32_16x16x32_bf16 v[60:63], a[44:47], a[100:103], v[60:63]\n"
"v_and_b32 v16, 0x30, v16\n"
"v_mfma_f32_16x16x32_bf16 v[60:63], a[40:43], a[96:99], v[60:63]\n"
"s_add_u32 m0, s64, 28672\n"
"buffer_load_dwordx2 v[18:19], v16, s[44:47], s61 offen offset:128\n"
"v_mfma_f32_16x16x32_bf16 v[64:67], a[40:43], a[104:107], v[64:67]\n"
"v_mfma_f32_16x16x32_bf16 v[64:67], a[44:47], a[108:111], v[64:67]\n"
"v_mfma_f32_16x16x32_bf16 v[36:39], a[36:39], a[108:111], v[36:39]\n"
"v_mfma_f32_16x16x32_bf16 v[36:39], a[32:35], a[104:107], v[36:39]\n"
"s_waitcnt lgkmcnt(8)\n"
"v_mfma_f32_16x16x32_bf16 v[40:43], a[32:35], a[112:115], v[40:43]\n"
"v_mfma_f32_16x16x32_bf16 v[40:43], a[36:39], a[116:119], v[40:43]\n"
"global_load_dwordx4 a[84:87], v0, s[34:35] offset:1024 sc1\n"
"v_lshrrev_b32 v20, 4, v0\n"
"v_mfma_f32_16x16x32_bf16 v[68:71], a[44:47], a[116:119], v[68:71]\n"
"v_and_b32 v20, 0x30, v20\n"
"v_mfma_f32_16x16x32_bf16 v[68:71], a[40:43], a[112:115], v[68:71]\n"
"buffer_load_dwordx2 v[22:23], v20, s[44:47], s61 offen offset:136\n"
"v_mfma_f32_16x16x32_bf16 v[72:75], a[40:43], a[120:123], v[72:75]\n"
"v_mfma_f32_16x16x32_bf16 v[72:75], a[44:47], a[124:127], v[72:75]\n"
"v_mfma_f32_16x16x32_bf16 v[44:47], a[36:39], a[124:127], v[44:47]\n"
"v_mfma_f32_16x16x32_bf16 v[44:47], a[32:35], a[120:123], v[44:47]\n"
"s_waitcnt lgkmcnt(4)\n"
"v_mfma_f32_16x16x32_bf16 v[48:51], a[32:35], a[128:131], v[48:51]\n"
"v_mfma_f32_16x16x32_bf16 v[48:51], a[36:39], a[132:135], v[48:51]\n"
"global_load_dwordx4 a[88:91], v0, s[34:35] offset:2048 sc1\n"
"v_lshrrev_b32 v24, 4, v0\n"
"v_mfma_f32_16x16x32_bf16 v[76:79], a[44:47], a[132:135], v[76:79]\n"
"v_and_b32 v24, 0x30, v24\n"
"v_mfma_f32_16x16x32_bf16 v[76:79], a[40:43], a[128:131], v[76:79]\n"
"buffer_load_dwordx4 v1, s[48:51], s69 offen offset:0 sc1 lds\n"
"buffer_load_dwordx2 v[26:27], v24, s[44:47], s61 offen offset:192\n"
"v_mfma_f32_16x16x32_bf16 v[80:83], a[40:43], a[136:139], v[80:83]\n"
"v_mfma_f32_16x16x32_bf16 v[80:83], a[44:47], a[140:143], v[80:83]\n"
"v_mfma_f32_16x16x32_bf16 v[52:55], a[36:39], a[140:143], v[52:55]\n"
"v_mfma_f32_16x16x32_bf16 v[52:55], a[32:35], a[136:139], v[52:55]\n"
"s_waitcnt lgkmcnt(0)\n"
"v_mfma_f32_16x16x32_bf16 v[56:59], a[32:35], a[144:147], v[56:59]\n"
"v_mfma_f32_16x16x32_bf16 v[56:59], a[36:39], a[148:151], v[56:59]\n"
"global_load_dwordx4 a[92:95], v0, s[34:35] offset:3072 sc1\n"
"v_lshrrev_b32 v28, 4, v0\n"
"v_mfma_f32_16x16x32_bf16 v[84:87], a[44:47], a[148:151], v[84:87]\n"
"v_and_b32 v28, 0x30, v28\n"
"v_mfma_f32_16x16x32_bf16 v[84:87], a[40:43], a[144:147], v[84:87]\n"
"buffer_load_dwordx2 v[30:31], v28, s[44:47], s61 offen offset:200\n"
"v_mfma_f32_16x16x32_bf16 a[4:7], a[40:43], a[152:155], a[4:7]\n"
"s_add_u32 s34, s34, 8192\n"
"v_mfma_f32_16x16x32_bf16 a[4:7], a[44:47], a[156:159], a[4:7]\n"
"v_mfma_f32_16x16x32_bf16 a[0:3], a[36:39], a[156:159], a[0:3]\n"
"v_mfma_f32_16x16x32_bf16 a[0:3], a[32:35], a[152:155], a[0:3]\n"
"s_waitcnt vmcnt(15)\n"
"v_mfma_f32_16x16x32_bf16 v[88:91], a[48:51], a[96:99], v[88:91]\n"
"v_mfma_f32_16x16x32_bf16 v[88:91], a[52:55], a[100:103], v[88:91]\n"
"global_load_dwordx4 a[32:35], v0, s[34:35] offset:-4096 sc1\n"
"v_mfma_f32_16x16x32_bf16 v[116:119], a[60:63], a[100:103], v[116:119]\n"
"v_mfma_f32_16x16x32_bf16 v[116:119], a[56:59], a[96:99], v[116:119]\n"
"buffer_load_dwordx4 v1, s[48:51], s69 offen offset:1024 sc1 lds\n"
"v_mfma_f32_16x16x32_bf16 v[120:123], a[56:59], a[104:107], v[120:123]\n"
"v_mfma_f32_16x16x32_bf16 v[120:123], a[60:63], a[108:111], v[120:123]\n"
"v_mfma_f32_16x16x32_bf16 v[92:95], a[52:55], a[108:111], v[92:95]\n"
"v_mfma_f32_16x16x32_bf16 v[92:95], a[48:51], a[104:107], v[92:95]\n"
"v_mfma_f32_16x16x32_bf16 v[96:99], a[48:51], a[112:115], v[96:99]\n"
"buffer_load_dwordx4 v[12:15], v0, s[56:59], %25 offen sc1\n"
"v_mfma_f32_16x16x32_bf16 v[96:99], a[52:55], a[116:119], v[96:99]\n"
"global_load_dwordx4 a[36:39], v0, s[34:35] offset:-3072 sc1\n"
"v_mfma_f32_16x16x32_bf16 v[124:127], a[60:63], a[116:119], v[124:127]\n"
"v_mfma_f32_16x16x32_bf16 v[124:127], a[56:59], a[112:115], v[124:127]\n"
"v_mfma_f32_16x16x32_bf16 v[128:131], a[56:59], a[120:123], v[128:131]\n"
"v_mfma_f32_16x16x32_bf16 v[128:131], a[60:63], a[124:127], v[128:131]\n"
"v_mfma_f32_16x16x32_bf16 v[100:103], a[52:55], a[124:127], v[100:103]\n"
"v_mfma_f32_16x16x32_bf16 v[100:103], a[48:51], a[120:123], v[100:103]\n"
"v_mfma_f32_16x16x32_bf16 v[104:107], a[48:51], a[128:131], v[104:107]\n"
"v_mfma_f32_16x16x32_bf16 v[104:107], a[52:55], a[132:135], v[104:107]\n"
"global_load_dwordx4 a[40:43], v0, s[34:35] offset:-2048 sc1\n"
"v_mfma_f32_16x16x32_bf16 v[132:135], a[60:63], a[132:135], v[132:135]\n"
"v_mfma_f32_16x16x32_bf16 v[132:135], a[56:59], a[128:131], v[132:135]\n"
"buffer_load_dwordx4 v1, s[48:51], s69 offen offset:2048 sc1 lds\n"
"v_mfma_f32_16x16x32_bf16 v[136:139], a[56:59], a[136:139], v[136:139]\n"
"v_mfma_f32_16x16x32_bf16 v[136:139], a[60:63], a[140:143], v[136:139]\n"
"v_mfma_f32_16x16x32_bf16 v[108:111], a[52:55], a[140:143], v[108:111]\n"
"v_mfma_f32_16x16x32_bf16 v[108:111], a[48:51], a[136:139], v[108:111]\n"
"v_mfma_f32_16x16x32_bf16 v[112:115], a[48:51], a[144:147], v[112:115]\n"
"v_mfma_f32_16x16x32_bf16 v[112:115], a[52:55], a[148:151], v[112:115]\n"
"global_load_dwordx4 a[44:47], v0, s[34:35] offset:-1024 sc1\n"
"v_mfma_f32_16x16x32_bf16 v[140:143], a[60:63], a[148:151], v[140:143]\n"
"v_mfma_f32_16x16x32_bf16 v[140:143], a[56:59], a[144:147], v[140:143]\n"
"v_mfma_f32_16x16x32_bf16 a[12:15], a[56:59], a[152:155], a[12:15]\n"
"v_mfma_f32_16x16x32_bf16 a[12:15], a[60:63], a[156:159], a[12:15]\n"
"v_mfma_f32_16x16x32_bf16 a[8:11], a[52:55], a[156:159], a[8:11]\n"
"v_mfma_f32_16x16x32_bf16 a[8:11], a[48:51], a[152:155], a[8:11]\n"
"s_waitcnt vmcnt(16)\n"
"v_mfma_f32_16x16x32_bf16 v[144:147], a[64:67], a[96:99], v[144:147]\n"
"v_mfma_f32_16x16x32_bf16 v[144:147], a[68:71], a[100:103], v[144:147]\n"
"global_load_dwordx4 a[48:51], v0, s[34:35] offset:0 sc1\n"
"v_mfma_f32_16x16x32_bf16 v[172:175], a[76:79], a[100:103], v[172:175]\n"
"v_mfma_f32_16x16x32_bf16 v[172:175], a[72:75], a[96:99], v[172:175]\n"
"buffer_load_dwordx4 v1, s[48:51], s69 offen offset:3072 sc1 lds\n"
"v_mfma_f32_16x16x32_bf16 v[176:179], a[72:75], a[104:107], v[176:179]\n"
"v_mfma_f32_16x16x32_bf16 v[176:179], a[76:79], a[108:111], v[176:179]\n"
"v_mfma_f32_16x16x32_bf16 v[148:151], a[68:71], a[108:111], v[148:151]\n"
"v_mfma_f32_16x16x32_bf16 v[148:151], a[64:67], a[104:107], v[148:151]\n"
"v_mfma_f32_16x16x32_bf16 v[152:155], a[64:67], a[112:115], v[152:155]\n"
"v_mfma_f32_16x16x32_bf16 v[152:155], a[68:71], a[116:119], v[152:155]\n"
"global_load_dwordx4 a[52:55], v0, s[34:35] offset:1024 sc1\n"
"v_mfma_f32_16x16x32_bf16 v[180:183], a[76:79], a[116:119], v[180:183]\n"
"v_mfma_f32_16x16x32_bf16 v[180:183], a[72:75], a[112:115], v[180:183]\n"
"v_mfma_f32_16x16x32_bf16 v[184:187], a[72:75], a[120:123], v[184:187]\n"
"v_mfma_f32_16x16x32_bf16 v[184:187], a[76:79], a[124:127], v[184:187]\n"
"v_mfma_f32_16x16x32_bf16 v[156:159], a[68:71], a[124:127], v[156:159]\n"
"v_mfma_f32_16x16x32_bf16 v[156:159], a[64:67], a[120:123], v[156:159]\n"
"v_mfma_f32_16x16x32_bf16 v[160:163], a[64:67], a[128:131], v[160:163]\n"
"v_mfma_f32_16x16x32_bf16 v[160:163], a[68:71], a[132:135], v[160:163]\n"
"global_load_dwordx4 a[56:59], v0, s[34:35] offset:2048 sc1\n"
"v_mfma_f32_16x16x32_bf16 v[188:191], a[76:79], a[132:135], v[188:191]\n"
"v_mfma_f32_16x16x32_bf16 v[188:191], a[72:75], a[128:131], v[188:191]\n"
"v_mfma_f32_16x16x32_bf16 v[192:195], a[72:75], a[136:139], v[192:195]\n"
"v_mfma_f32_16x16x32_bf16 v[192:195], a[76:79], a[140:143], v[192:195]\n"
"v_mfma_f32_16x16x32_bf16 v[164:167], a[68:71], a[140:143], v[164:167]\n"
"v_mfma_f32_16x16x32_bf16 v[164:167], a[64:67], a[136:139], v[164:167]\n"
"v_mfma_f32_16x16x32_bf16 v[168:171], a[64:67], a[144:147], v[168:171]\n"
"v_mfma_f32_16x16x32_bf16 v[168:171], a[68:71], a[148:151], v[168:171]\n"
"global_load_dwordx4 a[60:63], v0, s[34:35] offset:3072 sc1\n"
"v_mfma_f32_16x16x32_bf16 v[196:199], a[76:79], a[148:151], v[196:199]\n"
"v_mfma_f32_16x16x32_bf16 v[196:199], a[72:75], a[144:147], v[196:199]\n"
"v_mfma_f32_16x16x32_bf16 a[20:23], a[72:75], a[152:155], a[20:23]\n"
"s_add_u32 s34, s34, 8192\n"
"v_mfma_f32_16x16x32_bf16 a[20:23], a[76:79], a[156:159], a[20:23]\n"
"v_mfma_f32_16x16x32_bf16 a[16:19], a[68:71], a[156:159], a[16:19]\n"
"v_mfma_f32_16x16x32_bf16 a[16:19], a[64:67], a[152:155], a[16:19]\n"
"s_waitcnt vmcnt(13)\n"
"v_mfma_f32_16x16x32_bf16 v[200:203], a[80:83], a[96:99], v[200:203]\n"
"v_mfma_f32_16x16x32_bf16 v[200:203], a[84:87], a[100:103], v[200:203]\n"
"global_load_dwordx4 a[64:67], v0, s[34:35] offset:-4096 sc1\n"
"v_mfma_f32_16x16x32_bf16 v[228:231], a[92:95], a[100:103], v[228:231]\n"
"ds_read_b128 a[100:103], v5 offset:1024\n"
"v_mfma_f32_16x16x32_bf16 v[228:231], a[88:91], a[96:99], v[228:231]\n"
"ds_read_b128 a[96:99], v5 offset:0\n"
"v_mfma_f32_16x16x32_bf16 v[232:235], a[88:91], a[104:107], v[232:235]\n"
"v_mfma_f32_16x16x32_bf16 v[232:235], a[92:95], a[108:111], v[232:235]\n"
"v_mfma_f32_16x16x32_bf16 v[204:207], a[84:87], a[108:111], v[204:207]\n"
"ds_read_b128 a[108:111], v5 offset:3072\n"
"v_mfma_f32_16x16x32_bf16 v[204:207], a[80:83], a[104:107], v[204:207]\n"
"ds_read_b128 a[104:107], v5 offset:2048\n"
"v_mfma_f32_16x16x32_bf16 v[208:211], a[80:83], a[112:115], v[208:211]\n"
"v_mfma_f32_16x16x32_bf16 v[208:211], a[84:87], a[116:119], v[208:211]\n"
"global_load_dwordx4 a[68:71], v0, s[34:35] offset:-3072 sc1\n"
"v_mfma_f32_16x16x32_bf16 v[236:239], a[92:95], a[116:119], v[236:239]\n"
"ds_read_b128 a[116:119], v5 offset:5120\n"
"v_mfma_f32_16x16x32_bf16 v[236:239], a[88:91], a[112:115], v[236:239]\n"
"ds_read_b128 a[112:115], v5 offset:4096\n"
"v_mfma_f32_16x16x32_bf16 v[240:243], a[88:91], a[120:123], v[240:243]\n"
"v_mfma_f32_16x16x32_bf16 v[240:243], a[92:95], a[124:127], v[240:243]\n"
"v_mfma_f32_16x16x32_bf16 v[212:215], a[84:87], a[124:127], v[212:215]\n"
"ds_read_b128 a[124:127], v5 offset:7168\n"
"v_mfma_f32_16x16x32_bf16 v[212:215], a[80:83], a[120:123], v[212:215]\n"
"ds_read_b128 a[120:123], v5 offset:6144\n"
"v_mfma_f32_16x16x32_bf16 v[216:219], a[80:83], a[128:131], v[216:219]\n"
"v_mfma_f32_16x16x32_bf16 v[216:219], a[84:87], a[132:135], v[216:219]\n"
"global_load_dwordx4 a[72:75], v0, s[34:35] offset:-2048 sc1\n"
"v_mfma_f32_16x16x32_bf16 v[244:247], a[92:95], a[132:135], v[244:247]\n"
"ds_read_b128 a[132:135], v5 offset:9216\n"
"v_mfma_f32_16x16x32_bf16 v[244:247], a[88:91], a[128:131], v[244:247]\n"
"ds_read_b128 a[128:131], v5 offset:8192\n"
"v_mfma_f32_16x16x32_bf16 v[248:251], a[88:91], a[136:139], v[248:251]\n"
"v_mfma_f32_16x16x32_bf16 v[248:251], a[92:95], a[140:143], v[248:251]\n"
"v_mfma_f32_16x16x32_bf16 v[220:223], a[84:87], a[140:143], v[220:223]\n"
"ds_read_b128 a[140:143], v5 offset:11264\n"
"v_mfma_f32_16x16x32_bf16 v[220:223], a[80:83], a[136:139], v[220:223]\n"
"ds_read_b128 a[136:139], v5 offset:10240\n"
"v_mfma_f32_16x16x32_bf16 v[224:227], a[80:83], a[144:147], v[224:227]\n"
"v_mfma_f32_16x16x32_bf16 v[224:227], a[84:87], a[148:151], v[224:227]\n"
"global_load_dwordx4 a[76:79], v0, s[34:35] offset:-1024 sc1\n"
"v_mfma_f32_16x16x32_bf16 v[252:255], a[92:95], a[148:151], v[252:255]\n"
"ds_read_b128 a[148:151], v5 offset:13312\n"
"v_mfma_f32_16x16x32_bf16 v[252:255], a[88:91], a[144:147], v[252:255]\n"
"ds_read_b128 a[144:147], v5 offset:12288\n"
"v_mfma_f32_16x16x32_bf16 a[28:31], a[88:91], a[152:155], a[28:31]\n"
"v_mfma_f32_16x16x32_bf16 a[28:31], a[92:95], a[156:159], a[28:31]\n"
"v_mfma_f32_16x16x32_bf16 a[24:27], a[84:87], a[156:159], a[24:27]\n"
"ds_read_b128 a[156:159], v5 offset:15360\n"
"v_mfma_f32_16x16x32_bf16 a[24:27], a[80:83], a[152:155], a[24:27]\n"
"s_waitcnt vmcnt(9) lgkmcnt(11)\n"
"ds_read_b128 a[152:155], v5 offset:14336\n"
"v_mfma_f32_16x16x32_bf16 v[32:35], a[32:35], a[96:99], v[32:35]\n"
"v_mfma_f32_16x16x32_bf16 v[32:35], a[36:39], a[100:103], v[32:35]\n"
"global_load_dwordx4 a[80:83], v0, s[34:35] offset:0 sc1\n"
"v_mfma_f32_16x16x32_bf16 v[60:63], a[44:47], a[100:103], v[60:63]\n"
"v_mfma_f32_16x16x32_bf16 v[60:63], a[40:43], a[96:99], v[60:63]\n"
"v_mfma_f32_16x16x32_bf16 v[64:67], a[40:43], a[104:107], v[64:67]\n"
"v_mfma_f32_16x16x32_bf16 v[64:67], a[44:47], a[108:111], v[64:67]\n"
"v_mfma_f32_16x16x32_bf16 v[36:39], a[36:39], a[108:111], v[36:39]\n"
"v_mfma_f32_16x16x32_bf16 v[36:39], a[32:35], a[104:107], v[36:39]\n"
"s_waitcnt lgkmcnt(8)\n"
"v_mfma_f32_16x16x32_bf16 v[40:43], a[32:35], a[112:115], v[40:43]\n"
"v_mfma_f32_16x16x32_bf16 v[40:43], a[36:39], a[116:119], v[40:43]\n"
"global_load_dwordx4 a[84:87], v0, s[34:35] offset:1024 sc1\n"
"v_mfma_f32_16x16x32_bf16 v[68:71], a[44:47], a[116:119], v[68:71]\n"
"v_mfma_f32_16x16x32_bf16 v[68:71], a[40:43], a[112:115], v[68:71]\n"
"v_mfma_f32_16x16x32_bf16 v[72:75], a[40:43], a[120:123], v[72:75]\n"
"v_mfma_f32_16x16x32_bf16 v[72:75], a[44:47], a[124:127], v[72:75]\n"
"v_mfma_f32_16x16x32_bf16 v[44:47], a[36:39], a[124:127], v[44:47]\n"
"v_mfma_f32_16x16x32_bf16 v[44:47], a[32:35], a[120:123], v[44:47]\n"
"s_waitcnt lgkmcnt(4)\n"
"v_mfma_f32_16x16x32_bf16 v[48:51], a[32:35], a[128:131], v[48:51]\n"
"s_lshr_b32 s66, 20, s62\n"
"v_mfma_f32_16x16x32_bf16 v[48:51], a[36:39], a[132:135], v[48:51]\n"
"global_load_dwordx4 a[88:91], v0, s[34:35] offset:2048 sc1\n"
"v_mfma_f32_16x16x32_bf16 v[76:79], a[44:47], a[132:135], v[76:79]\n"
"s_and_b32 s66, s66, 1\n"
"s_cselect_b32 s70, 4294574080, 0\n"
"s_cselect_b32 s71, -1, 0\n"
"s_cselect_b32 s66, 0, 131072\n"
"v_mfma_f32_16x16x32_bf16 v[76:79], a[40:43], a[128:131], v[76:79]\n"
"v_mfma_f32_16x16x32_bf16 v[80:83], a[40:43], a[136:139], v[80:83]\n"
"s_add_u32 s70, s70, s66\n"
"v_mfma_f32_16x16x32_bf16 v[80:83], a[44:47], a[140:143], v[80:83]\n"
"v_mfma_f32_16x16x32_bf16 v[52:55], a[36:39], a[140:143], v[52:55]\n"
"v_mfma_f32_16x16x32_bf16 v[52:55], a[32:35], a[136:139], v[52:55]\n"
"s_waitcnt lgkmcnt(0)\n"
"v_mfma_f32_16x16x32_bf16 v[56:59], a[32:35], a[144:147], v[56:59]\n"
"v_mfma_f32_16x16x32_bf16 v[56:59], a[36:39], a[148:151], v[56:59]\n"
"global_load_dwordx4 a[92:95], v0, s[34:35] offset:3072 sc1\n"
"v_mfma_f32_16x16x32_bf16 v[84:87], a[44:47], a[148:151], v[84:87]\n"
"v_mfma_f32_16x16x32_bf16 v[84:87], a[40:43], a[144:147], v[84:87]\n"
"v_mfma_f32_16x16x32_bf16 a[4:7], a[40:43], a[152:155], a[4:7]\n"
"s_add_u32 s34, s34, 8192\n"
"v_mfma_f32_16x16x32_bf16 a[4:7], a[44:47], a[156:159], a[4:7]\n"
"v_mfma_f32_16x16x32_bf16 a[0:3], a[36:39], a[156:159], a[0:3]\n"
"v_mfma_f32_16x16x32_bf16 a[0:3], a[32:35], a[152:155], a[0:3]\n"
"s_waitcnt vmcnt(8)\n"
"v_mfma_f32_16x16x32_bf16 v[88:91], a[48:51], a[96:99], v[88:91]\n"
"v_mfma_f32_16x16x32_bf16 v[88:91], a[52:55], a[100:103], v[88:91]\n"
"global_load_dwordx4 a[32:35], v0, s[34:35] offset:-4096 sc1\n"
"v_mfma_f32_16x16x32_bf16 v[116:119], a[60:63], a[100:103], v[116:119]\n"
"v_mfma_f32_16x16x32_bf16 v[116:119], a[56:59], a[96:99], v[116:119]\n"
"v_mfma_f32_16x16x32_bf16 v[120:123], a[56:59], a[104:107], v[120:123]\n"
"s_add_u32 s67, s78, 2\n"
"v_mfma_f32_16x16x32_bf16 v[120:123], a[60:63], a[108:111], v[120:123]\n"
"v_mfma_f32_16x16x32_bf16 v[92:95], a[52:55], a[108:111], v[92:95]\n"
"v_mfma_f32_16x16x32_bf16 v[92:95], a[48:51], a[104:107], v[92:95]\n"
"v_mfma_f32_16x16x32_bf16 v[96:99], a[48:51], a[112:115], v[96:99]\n"
"v_mfma_f32_16x16x32_bf16 v[96:99], a[52:55], a[116:119], v[96:99]\n"
"global_load_dwordx4 a[36:39], v0, s[34:35] offset:-3072 sc1\n"
"v_mfma_f32_16x16x32_bf16 v[124:127], a[60:63], a[116:119], v[124:127]\n"
"v_mfma_f32_16x16x32_bf16 v[124:127], a[56:59], a[112:115], v[124:127]\n"
"v_mfma_f32_16x16x32_bf16 v[128:131], a[56:59], a[120:123], v[128:131]\n"
"v_mfma_f32_16x16x32_bf16 v[128:131], a[60:63], a[124:127], v[128:131]\n"
"v_mfma_f32_16x16x32_bf16 v[100:103], a[52:55], a[124:127], v[100:103]\n"
"v_mfma_f32_16x16x32_bf16 v[100:103], a[48:51], a[120:123], v[100:103]\n"
"v_mfma_f32_16x16x32_bf16 v[104:107], a[48:51], a[128:131], v[104:107]\n"
"v_mfma_f32_16x16x32_bf16 v[104:107], a[52:55], a[132:135], v[104:107]\n"
"global_load_dwordx4 a[40:43], v0, s[34:35] offset:-2048 sc1\n"
"v_mfma_f32_16x16x32_bf16 v[132:135], a[60:63], a[132:135], v[132:135]\n"
"v_mfma_f32_16x16x32_bf16 v[132:135], a[56:59], a[128:131], v[132:135]\n"
"v_mfma_f32_16x16x32_bf16 v[136:139], a[56:59], a[136:139], v[136:139]\n"
"v_mfma_f32_16x16x32_bf16 v[136:139], a[60:63], a[140:143], v[136:139]\n"
"v_mfma_f32_16x16x32_bf16 v[108:111], a[52:55], a[140:143], v[108:111]\n"
"v_subrev_u32 v12, s67, v12\n"
"v_mfma_f32_16x16x32_bf16 v[108:111], a[48:51], a[136:139], v[108:111]\n"
"v_xor_b32 v13, s79, v13\n"
"v_mfma_f32_16x16x32_bf16 v[112:115], a[48:51], a[144:147], v[112:115]\n"
"v_subrev_u32 v14, s67, v14\n"
"v_mfma_f32_16x16x32_bf16 v[112:115], a[52:55], a[148:151], v[112:115]\n"
"global_load_dwordx4 a[44:47], v0, s[34:35] offset:-1024 sc1\n"
"v_xor_b32 v15, s79, v15\n"
"v_mfma_f32_16x16x32_bf16 v[140:143], a[60:63], a[148:151], v[140:143]\n"
"v_or_b32 v12, v12, v14\n"
"v_mfma_f32_16x16x32_bf16 v[140:143], a[56:59], a[144:147], v[140:143]\n"
"v_or_b32 v13, v13, v15\n"
"v_mfma_f32_16x16x32_bf16 a[12:15], a[56:59], a[152:155], a[12:15]\n"
"v_or_b32 v12, v12, v13\n"
"v_mfma_f32_16x16x32_bf16 a[12:15], a[60:63], a[156:159], a[12:15]\n"
"v_cmp_lt_u32 vcc, 15, v12\n"
"v_mfma_f32_16x16x32_bf16 a[8:11], a[52:55], a[156:159], a[8:11]\n"
"s_cbranch_vccnz L_slow2\n"
"L_back2:\n"
"v_mfma_f32_16x16x32_bf16 a[8:11], a[48:51], a[152:155], a[8:11]\n"
"s_waitcnt vmcnt(8)\n"
"v_mfma_f32_16x16x32_bf16 v[144:147], a[64:67], a[96:99], v[144:147]\n"
"v_mfma_f32_16x16x32_bf16 v[144:147], a[68:71], a[100:103], v[144:147]\n"
"global_load_dwordx4 a[48:51], v0, s[34:35] offset:0 sc1\n"
"v_mfma_f32_16x16x32_bf16 v[172:175], a[76:79], a[100:103], v[172:175]\n"
"v_mfma_f32_16x16x32_bf16 v[172:175], a[72:75], a[96:99], v[172:175]\n"
"v_mfma_f32_16x16x32_bf16 v[176:179], a[72:75], a[104:107], v[176:179]\n"
"v_mfma_f32_16x16x32_bf16 v[176:179], a[76:79], a[108:111], v[176:179]\n"
"v_mfma_f32_16x16x32_bf16 v[148:151], a[68:71], a[108:111], v[148:151]\n"
"v_mfma_f32_16x16x32_bf16 v[148:151], a[64:67], a[104:107], v[148:151]\n"
"v_mfma_f32_16x16x32_bf16 v[152:155], a[64:67], a[112:115], v[152:155]\n"
"v_mfma_f32_16x16x32_bf16 v[152:155], a[68:71], a[116:119], v[152:155]\n"
"global_load_dwordx4 a[52:55], v0, s[34:35] offset:1024 sc1\n"
"v_mfma_f32_16x16x32_bf16 v[180:183], a[76:79], a[116:119], v[180:183]\n"
"v_mfma_f32_16x16x32_bf16 v[180:183], a[72:75], a[112:115], v[180:183]\n"
"v_mfma_f32_16x16x32_bf16 v[184:187], a[72:75], a[120:123], v[184:187]\n"
"v_mfma_f32_16x16x32_bf16 v[184:187], a[76:79], a[124:127], v[184:187]\n"
"v_mfma_f32_16x16x32_bf16 v[156:159], a[68:71], a[124:127], v[156:159]\n"
"v_mfma_f32_16x16x32_bf16 v[156:159], a[64:67], a[120:123], v[156:159]\n"
"v_mfma_f32_16x16x32_bf16 v[160:163], a[64:67], a[128:131], v[160:163]\n"
"v_mfma_f32_16x16x32_bf16 v[160:163], a[68:71], a[132:135], v[160:163]\n"
"global_load_dwordx4 a[56:59], v0, s[34:35] offset:2048 sc1\n"
"v_mfma_f32_16x16x32_bf16 v[188:191], a[76:79], a[132:135], v[188:191]\n"
"v_mfma_f32_16x16x32_bf16 v[188:191], a[72:75], a[128:131], v[188:191]\n"
"v_mfma_f32_16x16x32_bf16 v[192:195], a[72:75], a[136:139], v[192:195]\n"
"v_mfma_f32_16x16x32_bf16 v[192:195], a[76:79], a[140:143], v[192:195]\n"
"v_mfma_f32_16x16x32_bf16 v[164:167], a[68:71], a[140:143], v[164:167]\n"
"v_mfma_f32_16x16x32_bf16 v[164:167], a[64:67], a[136:139], v[164:167]\n"
"v_mfma_f32_16x16x32_bf16 v[168:171], a[64:67], a[144:147], v[168:171]\n"
"v_mfma_f32_16x16x32_bf16 v[168:171], a[68:71], a[148:151], v[168:171]\n"
"global_load_dwordx4 a[60:63], v0, s[34:35] offset:3072 sc1\n"
"v_mfma_f32_16x16x32_bf16 v[196:199], a[76:79], a[148:151], v[196:199]\n"
"v_mfma_f32_16x16x32_bf16 v[196:199], a[72:75], a[144:147], v[196:199]\n"
"v_mfma_f32_16x16x32_bf16 a[20:23], a[72:75], a[152:155], a[20:23]\n"
"s_add_u32 s34, s34, 8192\n"
"v_mfma_f32_16x16x32_bf16 a[20:23], a[76:79], a[156:159], a[20:23]\n"
"v_mfma_f32_16x16x32_bf16 a[16:19], a[68:71], a[156:159], a[16:19]\n"
"v_mfma_f32_16x16x32_bf16 a[16:19], a[64:67], a[152:155], a[16:19]\n"
"s_waitcnt vmcnt(8)\n"
"v_mfma_f32_16x16x32_bf16 v[200:203], a[80:83], a[96:99], v[200:203]\n"
"v_mfma_f32_16x16x32_bf16 v[200:203], a[84:87], a[100:103], v[200:203]\n"
"global_load_dwordx4 a[64:67], v0, s[34:35] offset:-4096 sc1\n"
"v_mfma_f32_16x16x32_bf16 v[228:231], a[92:95], a[100:103], v[228:231]\n"
"ds_read_b128 a[100:103], v2 offset:17408\n"
"v_mfma_f32_16x16x32_bf16 v[228:231], a[88:91], a[96:99], v[228:231]\n"
"ds_read_b128 a[96:99], v2 offset:16384\n"
"v_mfma_f32_16x16x32_bf16 v[232:235], a[88:91], a[104:107], v[232:235]\n"
"v_mfma_f32_16x16x32_bf16 v[232:235], a[92:95], a[108:111], v[232:235]\n"
"v_mfma_f32_16x16x32_bf16 v[204:207], a[84:87], a[108:111], v[204:207]\n"
"ds_read_b128 a[108:111], v2 offset:19456\n"
"v_mfma_f32_16x16x32_bf16 v[204:207], a[80:83], a[104:107], v[204:207]\n"
"ds_read_b128 a[104:107], v2 offset:18432\n"
"v_mfma_f32_16x16x32_bf16 v[208:211], a[80:83], a[112:115], v[208:211]\n"
"v_mfma_f32_16x16x32_bf16 v[208:211], a[84:87], a[116:119], v[208:211]\n"
"global_load_dwordx4 a[68:71], v0, s[34:35] offset:-3072 sc1\n"
"v_mfma_f32_16x16x32_bf16 v[236:239], a[92:95], a[116:119], v[236:239]\n"
"ds_read_b128 a[116:119], v2 offset:21504\n"
"v_mfma_f32_16x16x32_bf16 v[236:239], a[88:91], a[112:115], v[236:239]\n"
"ds_read_b128 a[112:115], v2 offset:20480\n"
"v_mfma_f32_16x16x32_bf16 v[240:243], a[88:91], a[120:123], v[240:243]\n"
"v_mfma_f32_16x16x32_bf16 v[240:243], a[92:95], a[124:127], v[240:243]\n"
"v_mfma_f32_16x16x32_bf16 v[212:215], a[84:87], a[124:127], v[212:215]\n"
"ds_read_b128 a[124:127], v2 offset:23552\n"
"v_mfma_f32_16x16x32_bf16 v[212:215], a[80:83], a[120:123], v[212:215]\n"
"ds_read_b128 a[120:123], v2 offset:22528\n"
"v_mfma_f32_16x16x32_bf16 v[216:219], a[80:83], a[128:131], v[216:219]\n"
"v_mfma_f32_16x16x32_bf16 v[216:219], a[84:87], a[132:135], v[216:219]\n"
"global_load_dwordx4 a[72:75], v0, s[34:35] offset:-2048 sc1\n"
"v_mfma_f32_16x16x32_bf16 v[244:247], a[92:95], a[132:135], v[244:247]\n"
"ds_read_b128 a[132:135], v2 offset:25600\n"
"v_mfma_f32_16x16x32_bf16 v[244:247], a[88:91], a[128:131], v[244:247]\n"
"ds_read_b128 a[128:131], v2 offset:24576\n"
"v_mfma_f32_16x16x32_bf16 v[248:251], a[88:91], a[136:139], v[248:251]\n"
"v_mfma_f32_16x16x32_bf16 v[248:251], a[92:95], a[140:143], v[248:251]\n"
"v_mfma_f32_16x16x32_bf16 v[220:223], a[84:87], a[140:143], v[220:223]\n"
"ds_read_b128 a[140:143], v2 offset:27648\n"
"v_mfma_f32_16x16x32_bf16 v[220:223], a[80:83], a[136:139], v[220:223]\n"
"ds_read_b128 a[136:139], v2 offset:26624\n"
"v_mfma_f32_16x16x32_bf16 v[224:227], a[80:83], a[144:147], v[224:227]\n"
"v_mfma_f32_16x16x32_bf16 v[224:227], a[84:87], a[148:151], v[224:227]\n"
"global_load_dwordx4 a[76:79], v0, s[34:35] offset:-1024 sc1\n"
"v_mfma_f32_16x16x32_bf16 v[252:255], a[92:95], a[148:151], v[252:255]\n"
"ds_read_b128 a[148:151], v2 offset:29696\n"
"v_mfma_f32_16x16x32_bf16 v[252:255], a[88:91], a[144:147], v[252:255]\n"
"ds_read_b128 a[144:147], v2 offset:28672\n"
"v_mfma_f32_16x16x32_bf16 a[28:31], a[88:91], a[152:155], a[28:31]\n"
"v_mfma_f32_16x16x32_bf16 a[28:31], a[92:95], a[156:159], a[28:31]\n"
"v_mfma_f32_16x16x32_bf16 a[24:27], a[84:87], a[156:159], a[24:27]\n"
"ds_read_b128 a[156:159], v2 offset:31744\n"
"v_mfma_f32_16x16x32_bf16 a[24:27], a[80:83], a[152:155], a[24:27]\n"
"ds_read_b128 a[152:155], v2 offset:30720\n"
"L_P5:\n"
"s_waitcnt vmcnt(8) lgkmcnt(12)\n"
"v_mfma_f32_16x16x32_bf16 v[32:35], a[32:35], a[96:99], v[32:35]\n"
"v_mfma_f32_16x16x32_bf16 v[32:35], a[36:39], a[100:103], v[32:35]\n"
"global_load_dwordx4 a[80:83], v0, s[34:35] offset:0 sc1\n"
"v_mfma_f32_16x16x32_bf16 v[60:63], a[44:47], a[100:103], v[60:63]\n"
"s_barrier\n"
"v_mfma_f32_16x16x32_bf16 v[60:63], a[40:43], a[96:99], v[60:63]\n"
"s_add_u32 s48, s48, s70\n"
"s_addc_u32 s49, s49, s71\n"
"v_mfma_f32_16x16x32_bf16 v[64:67], a[40:43], a[104:107], v[64:67]\n"
"v_mfma_f32_16x16x32_bf16 v[64:67], a[44:47], a[108:111], v[64:67]\n"
"v_mfma_f32_16x16x32_bf16 v[36:39], a[36:39], a[108:111], v[36:39]\n"
"s_add_u32 s52, s52, s70\n"
"s_addc_u32 s53, s53, s71\n"
"v_mfma_f32_16x16x32_bf16 v[36:39], a[32:35], a[104:107], v[36:39]\n"
"s_waitcnt lgkmcnt(8)\n"
"v_mfma_f32_16x16x32_bf16 v[40:43], a[32:35], a[112:115], v[40:43]\n"
"s_add_u32 s54, s54, s70\n"
"s_addc_u32 s55, s55, s71\n"
"v_mfma_f32_16x16x32_bf16 v[40:43], a[36:39], a[116:119], v[40:43]\n"
"global_load_dwordx4 a[84:87], v0, s[34:35] offset:1024 sc1\n"
"v_mfma_f32_16x16x32_bf16 v[68:71], a[44:47], a[116:119], v[68:71]\n"
"v_mfma_f32_16x16x32_bf16 v[68:71], a[40:43], a[112:115], v[68:71]\n"
"v_mfma_f32_16x16x32_bf16 v[72:75], a[40:43], a[120:123], v[72:75]\n"
"v_mfma_f32_16x16x32_bf16 v[72:75], a[44:47], a[124:127], v[72:75]\n"
"s_add_u32 s61, s61, 2048\n"
"v_mfma_f32_16x16x32_bf16 v[44:47], a[36:39], a[124:127], v[44:47]\n"
"v_mfma_f32_16x16x32_bf16 v[44:47], a[32:35], a[120:123], v[44:47]\n"
"s_waitcnt lgkmcnt(4)\n"
"v_mfma_f32_16x16x32_bf16 v[48:51], a[32:35], a[128:131], v[48:51]\n"
"v_lshlrev_b32 v16, 16, v18\n"
"v_mfma_f32_16x16x32_bf16 v[48:51], a[36:39], a[132:135], v[48:51]\n"
"global_load_dwordx4 a[88:91], v0, s[34:35] offset:2048 sc1\n"
"v_and_b32 v17, s63, v18\n"
"v_mfma_f32_16x16x32_bf16 v[76:79], a[44:47], a[132:135], v[76:79]\n"
"v_lshlrev_b32 v18, 16, v19\n"
"v_mfma_f32_16x16x32_bf16 v[76:79], a[40:43], a[128:131], v[76:79]\n"
"v_and_b32 v19, s63, v19\n"
"v_mfma_f32_16x16x32_bf16 v[80:83], a[40:43], a[136:139], v[80:83]\n"
"v_add_f32 v144, v144, v16\n"
"v_add_f32 v145, v145, v17\n"
"v_mfma_f32_16x16x32_bf16 v[80:83], a[44:47], a[140:143], v[80:83]\n"
"v_add_f32 v146, v146, v18\n"
"v_mfma_f32_16x16x32_bf16 v[52:55], a[36:39], a[140:143], v[52:55]\n"
"v_add_f32 v147, v147, v19\n"
"v_mfma_f32_16x16x32_bf16 v[52:55], a[32:35], a[136:139], v[52:55]\n"
"s_waitcnt lgkmcnt(0)\n"
"v_add_f32 v148, v148, v16\n"
"v_mfma_f32_16x16x32_bf16 v[56:59], a[32:35], a[144:147], v[56:59]\n"
"v_add_f32 v149, v149, v17\n"
"v_mfma_f32_16x16x32_bf16 v[56:59], a[36:39], a[148:151], v[56:59]\n"
"global_load_dwordx4 a[92:95], v0, s[34:35] offset:3072 sc1\n"
"v_add_f32 v150, v150, v18\n"
"v_mfma_f32_16x16x32_bf16 v[84:87], a[44:47], a[148:151], v[84:87]\n"
"v_add_f32 v151, v151, v19\n"
"v_mfma_f32_16x16x32_bf16 v[84:87], a[40:43], a[144:147], v[84:87]\n"
"v_add_f32 v152, v152, v16\n"
"v_mfma_f32_16x16x32_bf16 a[4:7], a[40:43], a[152:155], a[4:7]\n"
"s_add_u32 s34, s34, 8192\n"
"v_add_f32 v153, v153, v17\n"
"v_mfma_f32_16x16x32_bf16 a[4:7], a[44:47], a[156:159], a[4:7]\n"
"v_add_f32 v154, v154, v18\n"
"v_mfma_f32_16x16x32_bf16 a[0:3], a[36:39], a[156:159], a[0:3]\n"
"v_add_f32 v155, v155, v19\n"
"v_add_f32 v156, v156, v16\n"
"v_mfma_f32_16x16x32_bf16 a[0:3], a[32:35], a[152:155], a[0:3]\n"
"s_waitcnt vmcnt(8)\n"
"v_add_f32 v157, v157, v17\n"
"v_mfma_f32_16x16x32_bf16 v[88:91], a[48:51], a[96:99], v[88:91]\n"
"v_add_f32 v158, v158, v18\n"
"v_mfma_f32_16x16x32_bf16 v[88:91], a[52:55], a[100:103], v[88:91]\n"
"global_load_dwordx4 a[32:35], v0, s[34:35] offset:-4096 sc1\n"
"v_add_f32 v159, v159, v19\n"
"v_mfma_f32_16x16x32_bf16 v[116:119], a[60:63], a[100:103], v[116:119]\n"
"ds_read_b128 a[100:103], v3 offset:17408\n"
"v_add_f32 v160, v160, v16\n"
"v_mfma_f32_16x16x32_bf16 v[116:119], a[56:59], a[96:99], v[116:119]\n"
"ds_read_b128 a[96:99], v3 offset:16384\n"
"v_add_f32 v161, v161, v17\n"
"v_mfma_f32_16x16x32_bf16 v[120:123], a[56:59], a[104:107], v[120:123]\n"
"v_add_f32 v162, v162, v18\n"
"v_mfma_f32_16x16x32_bf16 v[120:123], a[60:63], a[108:111], v[120:123]\n"
"v_add_f32 v163, v163, v19\n"
"v_mfma_f32_16x16x32_bf16 v[92:95], a[52:55], a[108:111], v[92:95]\n"
"ds_read_b128 a[108:111], v3 offset:19456\n"
"v_add_f32 v164, v164, v16\n"
"v_mfma_f32_16x16x32_bf16 v[92:95], a[48:51], a[104:107], v[92:95]\n"
"ds_read_b128 a[104:107], v3 offset:18432\n"
"v_add_f32 v165, v165, v17\n"
"v_mfma_f32_16x16x32_bf16 v[96:99], a[48:51], a[112:115], v[96:99]\n"
"v_add_f32 v166, v166, v18\n"
"v_add_f32 v167, v167, v19\n"
"v_mfma_f32_16x16x32_bf16 v[96:99], a[52:55], a[116:119], v[96:99]\n"
"global_load_dwordx4 a[36:39], v0, s[34:35] offset:-3072 sc1\n"
"v_add_f32 v168, v168, v16\n"
"v_mfma_f32_16x16x32_bf16 v[124:127], a[60:63], a[116:119], v[124:127]\n"
"ds_read_b128 a[116:119], v3 offset:21504\n"
"v_add_f32 v169, v169, v17\n"
"v_mfma_f32_16x16x32_bf16 v[124:127], a[56:59], a[112:115], v[124:127]\n"
"ds_read_b128 a[112:115], v3 offset:20480\n"
"v_add_f32 v170, v170, v18\n"
"v_mfma_f32_16x16x32_bf16 v[128:131], a[56:59], a[120:123], v[128:131]\n"
"v_add_f32 v171, v171, v19\n"
"v_mfma_f32_16x16x32_bf16 v[128:131], a[60:63], a[124:127], v[128:131]\n"
"v_accvgpr_read_b32 v12, a16\n"
"v_mfma_f32_16x16x32_bf16 v[100:103], a[52:55], a[124:127], v[100:103]\n"
"ds_read_b128 a[124:127], v3 offset:23552\n"
"v_accvgpr_read_b32 v13, a17\n"
"v_mfma_f32_16x16x32_bf16 v[100:103], a[48:51], a[120:123], v[100:103]\n"
"ds_read_b128 a[120:123], v3 offset:22528\n"
"v_accvgpr_read_b32 v14, a18\n"
"v_mfma_f32_16x16x32_bf16 v[104:107], a[48:51], a[128:131], v[104:107]\n"
"v_accvgpr_read_b32 v15, a19\n"
"v_add_f32 v12, v12, v16\n"
"v_mfma_f32_16x16x32_bf16 v[104:107], a[52:55], a[132:135], v[104:107]\n"
"global_load_dwordx4 a[40:43], v0, s[34:35] offset:-2048 sc1\n"
"v_add_f32 v13, v13, v17\n"
"v_mfma_f32_16x16x32_bf16 v[132:135], a[60:63], a[132:135], v[132:135]\n"
"ds_read_b128 a[132:135], v3 offset:25600\n"
"v_add_f32 v14, v14, v18\n"
"v_mfma_f32_16x16x32_bf16 v[132:135], a[56:59], a[128:131], v[132:135]\n"
"ds_read_b128 a[128:131], v3 offset:24576\n"
"v_add_f32 v15, v15, v19\n"
"v_mfma_f32_16x16x32_bf16 v[136:139], a[56:59], a[136:139], v[136:139]\n"
"v_accvgpr_write_b32 a16, v12\n"
"v_mfma_f32_16x16x32_bf16 v[136:139], a[60:63], a[140:143], v[136:139]\n"
"v_accvgpr_write_b32 a17, v13\n"
"v_mfma_f32_16x16x32_bf16 v[108:111], a[52:55], a[140:143], v[108:111]\n"
"ds_read_b128 a[140:143], v3 offset:27648\n"
"v_accvgpr_write_b32 a18, v14\n"
"v_mfma_f32_16x16x32_bf16 v[108:111], a[48:51], a[136:139], v[108:111]\n"
"ds_read_b128 a[136:139], v3 offset:26624\n"
"v_accvgpr_write_b32 a19, v15\n"
"v_mfma_f32_16x16x32_bf16 v[112:115], a[48:51], a[144:147], v[112:115]\n"
"v_lshlrev_b32 v20, 16, v22\n"
"v_and_b32 v21, s63, v22\n"
"v_mfma_f32_16x16x32_bf16 v[112:115], a[52:55], a[148:151], v[112:115]\n"
"global_load_dwordx4 a[44:47], v0, s[34:35] offset:-1024 sc1\n"
"v_lshlrev_b32 v22, 16, v23\n"
"v_mfma_f32_16x16x32_bf16 v[140:143], a[60:63], a[148:151], v[140:143]\n"
"ds_read_b128 a[148:151], v3 offset:29696\n"
"v_and_b32 v23, s63, v23\n"
"v_mfma_f32_16x16x32_bf16 v[140:143], a[56:59], a[144:147], v[140:143]\n"
"ds_read_b128 a[144:147], v3 offset:28672\n"
"v_add_f32 v172, v172, v20\n"
"v_mfma_f32_16x16x32_bf16 a[12:15], a[56:59], a[152:155], a[12:15]\n"
"v_add_f32 v173, v173, v21\n"
"v_mfma_f32_16x16x32_bf16 a[12:15], a[60:63], a[156:159], a[12:15]\n"
"v_add_f32 v174, v174, v22\n"
"v_mfma_f32_16x16x32_bf16 a[8:11], a[52:55], a[156:159], a[8:11]\n"
"ds_read_b128 a[156:159], v3 offset:31744\n"
"v_add_f32 v175, v175, v23\n"
"v_mfma_f32_16x16x32_bf16 a[8:11], a[48:51], a[152:155], a[8:11]\n"
"s_waitcnt vmcnt(8) lgkmcnt(11)\n"
"ds_read_b128 a[152:155], v3 offset:30720\n"
"v_add_f32 v176, v176, v20\n"
"v_mfma_f32_16x16x32_bf16 v[32:35], a[64:67], a[96:99], v[32:35]\n"
"v_add_f32 v177, v177, v21\n"
"v_mfma_f32_16x16x32_bf16 v[32:35], a[68:71], a[100:103], v[32:35]\n"
"global_load_dwordx4 a[48:51], v0, s[34:35] offset:0 sc1\n"
"v_add_f32 v178, v178, v22\n"
"v_mfma_f32_16x16x32_bf16 v[60:63], a[76:79], a[100:103], v[60:63]\n"
"v_add_f32 v179, v179, v23\n"
"v_mfma_f32_16x16x32_bf16 v[60:63], a[72:75], a[96:99], v[60:63]\n"
"v_add_f32 v180, v180, v20\n"
"v_add_f32 v181, v181, v21\n"
"v_mfma_f32_16x16x32_bf16 v[64:67], a[72:75], a[104:107], v[64:67]\n"
"v_add_f32 v182, v182, v22\n"
"v_mfma_f32_16x16x32_bf16 v[64:67], a[76:79], a[108:111], v[64:67]\n"
"v_add_f32 v183, v183, v23\n"
"v_mfma_f32_16x16x32_bf16 v[36:39], a[68:71], a[108:111], v[36:39]\n"
"v_add_f32 v184, v184, v20\n"
"v_mfma_f32_16x16x32_bf16 v[36:39], a[64:67], a[104:107], v[36:39]\n"
"s_waitcnt lgkmcnt(8)\n"
"v_add_f32 v185, v185, v21\n"
"v_mfma_f32_16x16x32_bf16 v[40:43], a[64:67], a[112:115], v[40:43]\n"
"v_add_f32 v186, v186, v22\n"
"v_mfma_f32_16x16x32_bf16 v[40:43], a[68:71], a[116:119], v[40:43]\n"
"global_load_dwordx4 a[52:55], v0, s[34:35] offset:1024 sc1\n"
"v_add_f32 v187, v187, v23\n"
"v_mfma_f32_16x16x32_bf16 v[68:71], a[76:79], a[116:119], v[68:71]\n"
"v_add_f32 v188, v188, v20\n"
"v_mfma_f32_16x16x32_bf16 v[68:71], a[72:75], a[112:115], v[68:71]\n"
"v_add_f32 v189, v189, v21\n"
"v_mfma_f32_16x16x32_bf16 v[72:75], a[72:75], a[120:123], v[72:75]\n"
"v_add_f32 v190, v190, v22\n"
"v_add_f32 v191, v191, v23\n"
"v_mfma_f32_16x16x32_bf16 v[72:75], a[76:79], a[124:127], v[72:75]\n"
"v_add_f32 v192, v192, v20\n"
"v_mfma_f32_16x16x32_bf16 v[44:47], a[68:71], a[124:127], v[44:47]\n"
"v_add_f32 v193, v193, v21\n"
"v_mfma_f32_16x16x32_bf16 v[44:47], a[64:67], a[120:123], v[44:47]\n"
"s_waitcnt lgkmcnt(4)\n"
"v_add_f32 v194, v194, v22\n"
"v_mfma_f32_16x16x32_bf16 v[48:51], a[64:67], a[128:131], v[48:51]\n"
"v_add_f32 v195, v195, v23\n"
"v_mfma_f32_16x16x32_bf16 v[48:51], a[68:71], a[132:135], v[48:51]\n"
"global_load_dwordx4 a[56:59], v0, s[34:35] offset:2048 sc1\n"
"v_add_f32 v196, v196, v20\n"
"v_mfma_f32_16x16x32_bf16 v[76:79], a[76:79], a[132:135], v[76:79]\n"
"v_add_f32 v197, v197, v21\n"
"v_mfma_f32_16x16x32_bf16 v[76:79], a[72:75], a[128:131], v[76:79]\n"
"v_add_f32 v198, v198, v22\n"
"v_mfma_f32_16x16x32_bf16 v[80:83], a[72:75], a[136:139], v[80:83]\n"
"v_add_f32 v199, v199, v23\n"
"v_mfma_f32_16x16x32_bf16 v[80:83], a[76:79], a[140:143], v[80:83]\n"
"v_accvgpr_read_b32 v12, a20\n"
"v_mfma_f32_16x16x32_bf16 v[52:55], a[68:71], a[140:143], v[52:55]\n"
"v_accvgpr_read_b32 v13, a21\n"
"v_accvgpr_read_b32 v14, a22\n"
"v_mfma_f32_16x16x32_bf16 v[52:55], a[64:67], a[136:139], v[52:55]\n"
"s_waitcnt lgkmcnt(0)\n"
"v_accvgpr_read_b32 v15, a23\n"
"v_mfma_f32_16x16x32_bf16 v[56:59], a[64:67], a[144:147], v[56:59]\n"
"v_add_f32 v12, v12, v20\n"
"v_mfma_f32_16x16x32_bf16 v[56:59], a[68:71], a[148:151], v[56:59]\n"
"global_load_dwordx4 a[60:63], v0, s[34:35] offset:3072 sc1\n"
"v_add_f32 v13, v13, v21\n"
"v_mfma_f32_16x16x32_bf16 v[84:87], a[76:79], a[148:151], v[84:87]\n"
"v_add_f32 v14, v14, v22\n"
"v_mfma_f32_16x16x32_bf16 v[84:87], a[72:75], a[144:147], v[84:87]\n"
"v_add_f32 v15, v15, v23\n"
"v_mfma_f32_16x16x32_bf16 a[4:7], a[72:75], a[152:155], a[4:7]\n"
"s_add_u32 s34, s34, 8192\n"
"v_accvgpr_write_b32 a20, v12\n"
"v_mfma_f32_16x16x32_bf16 a[4:7], a[76:79], a[156:159], a[4:7]\n"
"v_accvgpr_write_b32 a21, v13\n"
"v_mfma_f32_16x16x32_bf16 a[0:3], a[68:71], a[156:159], a[0:3]\n"
"v_accvgpr_write_b32 a22, v14\n"
"v_mfma_f32_16x16x32_bf16 a[0:3], a[64:67], a[152:155], a[0:3]\n"
"s_waitcnt vmcnt(8)\n"
"v_accvgpr_write_b32 a23, v15\n"
"v_lshlrev_b32 v24, 16, v26\n"
"v_mfma_f32_16x16x32_bf16 v[88:91], a[80:83], a[96:99], v[88:91]\n"
"v_and_b32 v25, s63, v26\n"
"v_mfma_f32_16x16x32_bf16 v[88:91], a[84:87], a[100:103], v[88:91]\n"
"global_load_dwordx4 a[64:67], v0, s[34:35] offset:-4096 sc1\n"
"v_lshlrev_b32 v26, 16, v27\n"
"v_mfma_f32_16x16x32_bf16 v[116:119], a[92:95], a[100:103], v[116:119]\n"
"ds_read_b128 a[100:103], v4 offset:17408\n"
"v_and_b32 v27, s63, v27\n"
"v_mfma_f32_16x16x32_bf16 v[116:119], a[88:91], a[96:99], v[116:119]\n"
"ds_read_b128 a[96:99], v4 offset:16384\n"
"v_add_f32 v200, v200, v24\n"
"v_mfma_f32_16x16x32_bf16 v[120:123], a[88:91], a[104:107], v[120:123]\n"
"v_add_f32 v201, v201, v25\n"
"v_mfma_f32_16x16x32_bf16 v[120:123], a[92:95], a[108:111], v[120:123]\n"
"v_add_f32 v202, v202, v26\n"
"v_mfma_f32_16x16x32_bf16 v[92:95], a[84:87], a[108:111], v[92:95]\n"
"ds_read_b128 a[108:111], v4 offset:19456\n"
"v_add_f32 v203, v203, v27\n"
"v_mfma_f32_16x16x32_bf16 v[92:95], a[80:83], a[104:107], v[92:95]\n"
"ds_read_b128 a[104:107], v4 offset:18432\n"
"v_add_f32 v204, v204, v24\n"
"v_mfma_f32_16x16x32_bf16 v[96:99], a[80:83], a[112:115], v[96:99]\n"
"v_add_f32 v205, v205, v25\n"
"v_add_f32 v206, v206, v26\n"
"v_mfma_f32_16x16x32_bf16 v[96:99], a[84:87], a[116:119], v[96:99]\n"
"global_load_dwordx4 a[68:71], v0, s[34:35] offset:-3072 sc1\n"
"v_add_f32 v207, v207, v27\n"
"v_mfma_f32_16x16x32_bf16 v[124:127], a[92:95], a[116:119], v[124:127]\n"
"ds_read_b128 a[116:119], v4 offset:21504\n"
"v_add_f32 v208, v208, v24\n"
"v_mfma_f32_16x16x32_bf16 v[124:127], a[88:91], a[112:115], v[124:127]\n"
"ds_read_b128 a[112:115], v4 offset:20480\n"
"v_add_f32 v209, v209, v25\n"
"v_mfma_f32_16x16x32_bf16 v[128:131], a[88:91], a[120:123], v[128:131]\n"
"v_add_f32 v210, v210, v26\n"
"v_mfma_f32_16x16x32_bf16 v[128:131], a[92:95], a[124:127], v[128:131]\n"
"v_add_f32 v211, v211, v27\n"
"v_mfma_f32_16x16x32_bf16 v[100:103], a[84:87], a[124:127], v[100:103]\n"
"ds_read_b128 a[124:127], v4 offset:23552\n"
"v_add_f32 v212, v212, v24\n"
"v_mfma_f32_16x16x32_bf16 v[100:103], a[80:83], a[120:123], v[100:103]\n"
"ds_read_b128 a[120:123], v4 offset:22528\n"
"v_add_f32 v213, v213, v25\n"
"v_mfma_f32_16x16x32_bf16 v[104:107], a[80:83], a[128:131], v[104:107]\n"
"v_add_f32 v214, v214, v26\n"
"v_add_f32 v215, v215, v27\n"
"v_mfma_f32_16x16x32_bf16 v[104:107], a[84:87], a[132:135], v[104:107]\n"
"global_load_dwordx4 a[72:75], v0, s[34:35] offset:-2048 sc1\n"
"v_add_f32 v216, v216, v24\n"
"v_mfma_f32_16x16x32_bf16 v[132:135], a[92:95], a[132:135], v[132:135]\n"
"ds_read_b128 a[132:135], v4 offset:25600\n"
"v_add_f32 v217, v217, v25\n"
"v_mfma_f32_16x16x32_bf16 v[132:135], a[88:91], a[128:131], v[132:135]\n"
"ds_read_b128 a[128:131], v4 offset:24576\n"
"v_add_f32 v218, v218, v26\n"
"v_mfma_f32_16x16x32_bf16 v[136:139], a[88:91], a[136:139], v[136:139]\n"
"v_add_f32 v219, v219, v27\n"
"v_mfma_f32_16x16x32_bf16 v[136:139], a[92:95], a[140:143], v[136:139]\n"
"v_add_f32 v220, v220, v24\n"
"v_mfma_f32_16x16x32_bf16 v[108:111], a[84:87], a[140:143], v[108:111]\n"
"ds_read_b128 a[140:143], v4 offset:27648\n"
"v_add_f32 v221, v221, v25\n"
"v_mfma_f32_16x16x32_bf16 v[108:111], a[80:83], a[136:139], v[108:111]\n"
"ds_read_b128 a[136:139], v4 offset:26624\n"
"v_add_f32 v222, v222, v26\n"
"v_mfma_f32_16x16x32_bf16 v[112:115], a[80:83], a[144:147], v[112:115]\n"
"v_add_f32 v223, v223, v27\n"
"v_mfma_f32_16x16x32_bf16 v[112:115], a[84:87], a[148:151], v[112:115]\n"
"global_load_dwordx4 a[76:79], v0, s[34:35] offset:-1024 sc1\n"
"v_add_f32 v224, v224, v24\n"
"v_mfma_f32_16x16x32_bf16 v[140:143], a[92:95], a[148:151], v[140:143]\n"
"ds_read_b128 a[148:151], v4 offset:29696\n"
"v_add_f32 v225, v225, v25\n"
"v_mfma_f32_16x16x32_bf16 v[140:143], a[88:91], a[144:147], v[140:143]\n"
"ds_read_b128 a[144:147], v4 offset:28672\n"
"v_add_f32 v226, v226, v26\n"
"v_mfma_f32_16x16x32_bf16 a[12:15], a[88:91], a[152:155], a[12:15]\n"
"v_add_f32 v227, v227, v27\n"
"v_accvgpr_read_b32 v12, a24\n"
"v_mfma_f32_16x16x32_bf16 a[12:15], a[92:95], a[156:159], a[12:15]\n"
"v_accvgpr_read_b32 v13, a25\n"
"v_mfma_f32_16x16x32_bf16 a[8:11], a[84:87], a[156:159], a[8:11]\n"
"ds_read_b128 a[156:159], v4 offset:31744\n"
"v_accvgpr_read_b32 v14, a26\n"
"v_mfma_f32_16x16x32_bf16 a[8:11], a[80:83], a[152:155], a[8:11]\n"
"s_waitcnt vmcnt(8) lgkmcnt(11)\n"
"ds_read_b128 a[152:155], v4 offset:30720\n"
"v_accvgpr_read_b32 v15, a27\n"
"v_mfma_f32_16x16x32_bf16 v[32:35], a[32:35], a[96:99], v[32:35]\n"
"v_add_f32 v12, v12, v24\n"
"v_mfma_f32_16x16x32_bf16 v[32:35], a[36:39], a[100:103], v[32:35]\n"
"global_load_dwordx4 a[80:83], v0, s[34:35] offset:0 sc1\n"
"v_add_f32 v13, v13, v25\n"
"v_mfma_f32_16x16x32_bf16 v[60:63], a[44:47], a[100:103], v[60:63]\n"
"v_add_f32 v14, v14, v26\n"
"v_mfma_f32_16x16x32_bf16 v[60:63], a[40:43], a[96:99], v[60:63]\n"
"v_add_f32 v15, v15, v27\n"
"v_mfma_f32_16x16x32_bf16 v[64:67], a[40:43], a[104:107], v[64:67]\n"
"v_accvgpr_write_b32 a24, v12\n"
"v_accvgpr_write_b32 a25, v13\n"
"v_mfma_f32_16x16x32_bf16 v[64:67], a[44:47], a[108:111], v[64:67]\n"
"v_accvgpr_write_b32 a26, v14\n"
"v_mfma_f32_16x16x32_bf16 v[36:39], a[36:39], a[108:111], v[36:39]\n"
"v_accvgpr_write_b32 a27, v15\n"
"v_mfma_f32_16x16x32_bf16 v[36:39], a[32:35], a[104:107], v[36:39]\n"
"s_waitcnt lgkmcnt(8)\n"
"v_lshlrev_b32 v28, 16, v30\n"
"v_mfma_f32_16x16x32_bf16 v[40:43], a[32:35], a[112:115], v[40:43]\n"
"v_and_b32 v29, s63, v30\n"
"v_mfma_f32_16x16x32_bf16 v[40:43], a[36:39], a[116:119], v[40:43]\n"
"global_load_dwordx4 a[84:87], v0, s[34:35] offset:1024 sc1\n"
"v_lshlrev_b32 v30, 16, v31\n"
"v_mfma_f32_16x16x32_bf16 v[68:71], a[44:47], a[116:119], v[68:71]\n"
"v_and_b32 v31, s63, v31\n"
"v_mfma_f32_16x16x32_bf16 v[68:71], a[40:43], a[112:115], v[68:71]\n"
"v_add_f32 v228, v228, v28\n"
"v_mfma_f32_16x16x32_bf16 v[72:75], a[40:43], a[120:123], v[72:75]\n"
"v_add_f32 v229, v229, v29\n"
"v_mfma_f32_16x16x32_bf16 v[72:75], a[44:47], a[124:127], v[72:75]\n"
"v_add_f32 v230, v230, v30\n"
"v_mfma_f32_16x16x32_bf16 v[44:47], a[36:39], a[124:127], v[44:47]\n"
"v_add_f32 v231, v231, v31\n"
"v_add_f32 v232, v232, v28\n"
"v_mfma_f32_16x16x32_bf16 v[44:47], a[32:35], a[120:123], v[44:47]\n"
"s_waitcnt lgkmcnt(4)\n"
"v_add_f32 v233, v233, v29\n"
"v_mfma_f32_16x16x32_bf16 v[48:51], a[32:35], a[128:131], v[48:51]\n"
"v_add_f32 v234, v234, v30\n"
"v_mfma_f32_16x16x32_bf16 v[48:51], a[36:39], a[132:135], v[48:51]\n"
"global_load_dwordx4 a[88:91], v0, s[34:35] offset:2048 sc1\n"
"v_add_f32 v235, v235, v31\n"
"v_mfma_f32_16x16x32_bf16 v[76:79], a[44:47], a[132:135], v[76:79]\n"
"v_add_f32 v236, v236, v28\n"
"v_mfma_f32_16x16x32_bf16 v[76:79], a[40:43], a[128:131], v[76:79]\n"
"v_add_f32 v237, v237, v29\n"
"v_mfma_f32_16x16x32_bf16 v[80:83], a[40:43], a[136:139], v[80:83]\n"
"v_add_f32 v238, v238, v30\n"
"v_mfma_f32_16x16x32_bf16 v[80:83], a[44:47], a[140:143], v[80:83]\n"
"v_add_f32 v239, v239, v31\n"
"v_mfma_f32_16x16x32_bf16 v[52:55], a[36:39], a[140:143], v[52:55]\n"
"v_add_f32 v240, v240, v28\n"
"v_mfma_f32_16x16x32_bf16 v[52:55], a[32:35], a[136:139], v[52:55]\n"
"s_waitcnt lgkmcnt(0)\n"
"v_add_f32 v241, v241, v29\n"
"v_add_f32 v242, v242, v30\n"
"v_mfma_f32_16x16x32_bf16 v[56:59], a[32:35], a[144:147], v[56:59]\n"
"v_add_f32 v243, v243, v31\n"
"v_mfma_f32_16x16x32_bf16 v[56:59], a[36:39], a[148:151], v[56:59]\n"
"global_load_dwordx4 a[92:95], v0, s[34:35] offset:3072 sc1\n"
"v_add_f32 v244, v244, v28\n"
"v_mfma_f32_16x16x32_bf16 v[84:87], a[44:47], a[148:151], v[84:87]\n"
"v_add_f32 v245, v245, v29\n"
"v_mfma_f32_16x16x32_bf16 v[84:87], a[40:43], a[144:147], v[84:87]\n"
"v_add_f32 v246, v246, v30\n"
"v_mfma_f32_16x16x32_bf16 a[4:7], a[40:43], a[152:155], a[4:7]\n"
"s_add_u32 s34, s34, 8192\n"
"v_add_f32 v247, v247, v31\n"
"v_mfma_f32_16x16x32_bf16 a[4:7], a[44:47], a[156:159], a[4:7]\n"
"v_add_f32 v248, v248, v28\n"
"v_mfma_f32_16x16x32_bf16 a[0:3], a[36:39], a[156:159], a[0:3]\n"
"v_add_f32 v249, v249, v29\n"
"v_mfma_f32_16x16x32_bf16 a[0:3], a[32:35], a[152:155], a[0:3]\n"
"s_waitcnt vmcnt(8)\n"
"v_add_f32 v250, v250, v30\n"
"v_mfma_f32_16x16x32_bf16 v[88:91], a[48:51], a[96:99], v[88:91]\n"
"v_add_f32 v251, v251, v31\n"
"v_add_f32 v252, v252, v28\n"
"v_mfma_f32_16x16x32_bf16 v[88:91], a[52:55], a[100:103], v[88:91]\n"
"global_load_dwordx4 a[32:35], v0, s[34:35] offset:-4096 sc1\n"
"v_add_f32 v253, v253, v29\n"
"v_mfma_f32_16x16x32_bf16 v[116:119], a[60:63], a[100:103], v[116:119]\n"
"ds_read_b128 a[100:103], v5 offset:17408\n"
"v_add_f32 v254, v254, v30\n"
"v_mfma_f32_16x16x32_bf16 v[116:119], a[56:59], a[96:99], v[116:119]\n"
"ds_read_b128 a[96:99], v5 offset:16384\n"
"v_add_f32 v255, v255, v31\n"
"v_mfma_f32_16x16x32_bf16 v[120:123], a[56:59], a[104:107], v[120:123]\n"
"v_accvgpr_read_b32 v12, a28\n"
"v_mfma_f32_16x16x32_bf16 v[120:123], a[60:63], a[108:111], v[120:123]\n"
"v_accvgpr_read_b32 v13, a29\n"
"v_mfma_f32_16x16x32_bf16 v[92:95], a[52:55], a[108:111], v[92:95]\n"
"ds_read_b128 a[108:111], v5 offset:19456\n"
"v_accvgpr_read_b32 v14, a30\n"
"v_mfma_f32_16x16x32_bf16 v[92:95], a[48:51], a[104:107], v[92:95]\n"
"ds_read_b128 a[104:107], v5 offset:18432\n"
"v_accvgpr_read_b32 v15, a31\n"
"v_mfma_f32_16x16x32_bf16 v[96:99], a[48:51], a[112:115], v[96:99]\n"
"v_add_f32 v12, v12, v28\n"
"v_add_f32 v13, v13, v29\n"
"v_mfma_f32_16x16x32_bf16 v[96:99], a[52:55], a[116:119], v[96:99]\n"
"global_load_dwordx4 a[36:39], v0, s[34:35] offset:-3072 sc1\n"
"v_add_f32 v14, v14, v30\n"
"v_mfma_f32_16x16x32_bf16 v[124:127], a[60:63], a[116:119], v[124:127]\n"
"ds_read_b128 a[116:119], v5 offset:21504\n"
"v_add_f32 v15, v15, v31\n"
"v_mfma_f32_16x16x32_bf16 v[124:127], a[56:59], a[112:115], v[124:127]\n"
"ds_read_b128 a[112:115], v5 offset:20480\n"
"v_accvgpr_write_b32 a28, v12\n"
"v_mfma_f32_16x16x32_bf16 v[128:131], a[56:59], a[120:123], v[128:131]\n"
"v_accvgpr_write_b32 a29, v13\n"
"v_mfma_f32_16x16x32_bf16 v[128:131], a[60:63], a[124:127], v[128:131]\n"
"v_accvgpr_write_b32 a30, v14\n"
"v_mfma_f32_16x16x32_bf16 v[100:103], a[52:55], a[124:127], v[100:103]\n"
"ds_read_b128 a[124:127], v5 offset:23552\n"
"v_accvgpr_write_b32 a31, v15\n"
"v_mfma_f32_16x16x32_bf16 v[100:103], a[48:51], a[120:123], v[100:103]\n"
"ds_read_b128 a[120:123], v5 offset:22528\n"
"v_mfma_f32_16x16x32_bf16 v[104:107], a[48:51], a[128:131], v[104:107]\n"
"v_mfma_f32_16x16x32_bf16 v[104:107], a[52:55], a[132:135], v[104:107]\n"
"global_load_dwordx4 a[40:43], v0, s[34:35] offset:-2048 sc1\n"
"v_mfma_f32_16x16x32_bf16 v[132:135], a[60:63], a[132:135], v[132:135]\n"
"ds_read_b128 a[132:135], v5 offset:25600\n"
"v_mfma_f32_16x16x32_bf16 v[132:135], a[56:59], a[128:131], v[132:135]\n"
"ds_read_b128 a[128:131], v5 offset:24576\n"
"v_mfma_f32_16x16x32_bf16 v[136:139], a[56:59], a[136:139], v[136:139]\n"
"ds_read_b128 v[16:19], v2 offset:32768\n"
"v_mfma_f32_16x16x32_bf16 v[136:139], a[60:63], a[140:143], v[136:139]\n"
"v_mfma_f32_16x16x32_bf16 v[108:111], a[52:55], a[140:143], v[108:111]\n"
"ds_read_b128 a[140:143], v5 offset:27648\n"
"v_mfma_f32_16x16x32_bf16 v[108:111], a[48:51], a[136:139], v[108:111]\n"
"ds_read_b128 a[136:139], v5 offset:26624\n"
"v_mfma_f32_16x16x32_bf16 v[112:115], a[48:51], a[144:147], v[112:115]\n"
"v_mfma_f32_16x16x32_bf16 v[112:115], a[52:55], a[148:151], v[112:115]\n"
"global_load_dwordx4 a[44:47], v0, s[34:35] offset:-1024 sc1\n"
"v_mfma_f32_16x16x32_bf16 v[140:143], a[60:63], a[148:151], v[140:143]\n"
"ds_read_b128 a[148:151], v5 offset:29696\n"
"v_mfma_f32_16x16x32_bf16 v[140:143], a[56:59], a[144:147], v[140:143]\n"
"ds_read_b128 a[144:147], v5 offset:28672\n"
"v_mfma_f32_16x16x32_bf16 a[12:15], a[56:59], a[152:155], a[12:15]\n"
"v_mfma_f32_16x16x32_bf16 a[12:15], a[60:63], a[156:159], a[12:15]\n"
"v_mfma_f32_16x16x32_bf16 a[8:11], a[52:55], a[156:159], a[8:11]\n"
"s_cmp_eq_u32 s62, 5\n"
"ds_read_b128 a[156:159], v5 offset:31744\n"
"v_mfma_f32_16x16x32_bf16 a[8:11], a[48:51], a[152:155], a[8:11]\n"
"ds_read_b128 a[152:155], v5 offset:30720\n"
"s_cbranch_scc1 L_last\n"
"s_waitcnt vmcnt(8) lgkmcnt(13)\n"
"v_mfma_f32_16x16x32_bf16 v[32:35], a[64:67], a[96:99], v[32:35]\n"
"v_mfma_f32_16x16x32_bf16 v[32:35], a[68:71], a[100:103], v[32:35]\n"
"global_load_dwordx4 a[48:51], v0, s[34:35] offset:0 sc1\n"
"v_mfma_f32_16x16x32_bf16 v[60:63], a[76:79], a[100:103], v[60:63]\n"
"v_mfma_f32_16x16x32_bf16 v[60:63], a[72:75], a[96:99], v[60:63]\n"
"v_mfma_f32_16x16x32_bf16 v[64:67], a[72:75], a[104:107], v[64:67]\n"
"v_mfma_f32_16x16x32_bf16 v[64:67], a[76:79], a[108:111], v[64:67]\n"
"v_mfma_f32_16x16x32_bf16 v[36:39], a[68:71], a[108:111], v[36:39]\n"
"s_waitcnt lgkmcnt(6)\n"
"v_lshlrev_b32 v12, 16, v16\n"
"v_mfma_f32_16x16x32_bf16 v[36:39], a[64:67], a[104:107], v[36:39]\n"
"v_and_b32 v13, s63, v16\n"
"v_lshlrev_b32 v14, 16, v17\n"
"v_mfma_f32_16x16x32_bf16 v[40:43], a[64:67], a[112:115], v[40:43]\n"
"v_and_b32 v15, s63, v17\n"
"v_mfma_f32_16x16x32_bf16 v[40:43], a[68:71], a[116:119], v[40:43]\n"
"global_load_dwordx4 a[52:55], v0, s[34:35] offset:1024 sc1\n"
"ds_read_b128 v[20:23], v2 offset:34816\n"
"v_mfma_f32_16x16x32_bf16 v[68:71], a[76:79], a[116:119], v[68:71]\n"
"v_mul_f32 v12, v12, v32\n"
"v_mul_f32 v13, v13, v33\n"
"v_mfma_f32_16x16x32_bf16 v[68:71], a[72:75], a[112:115], v[68:71]\n"
"v_mul_f32 v14, v14, v34\n"
"v_mfma_f32_16x16x32_bf16 v[72:75], a[72:75], a[120:123], v[72:75]\n"
"v_mul_f32 v15, v15, v35\n"
"v_cvt_pk_bf16_f32 v24, v12, v13\n"
"v_mfma_f32_16x16x32_bf16 v[72:75], a[76:79], a[124:127], v[72:75]\n"
"v_cvt_pk_bf16_f32 v25, v14, v15\n"
"v_mfma_f32_16x16x32_bf16 v[44:47], a[68:71], a[124:127], v[44:47]\n"
"ds_write_b64 v2, v[24:25] offset:0\n"
"v_mfma_f32_16x16x32_bf16 v[44:47], a[64:67], a[120:123], v[44:47]\n"
"s_waitcnt lgkmcnt(6)\n"
"v_lshlrev_b32 v12, 16, v18\n"
"v_and_b32 v13, s63, v18\n"
"v_mfma_f32_16x16x32_bf16 v[48:51], a[64:67], a[128:131], v[48:51]\n"
"v_lshlrev_b32 v14, 16, v19\n"
"v_mfma_f32_16x16x32_bf16 v[48:51], a[68:71], a[132:135], v[48:51]\n"
"global_load_dwordx4 a[56:59], v0, s[34:35] offset:2048 sc1\n"
"v_and_b32 v15, s63, v19\n"
"v_mfma_f32_16x16x32_bf16 v[76:79], a[76:79], a[132:135], v[76:79]\n"
"v_mul_f32 v12, v12, v60\n"
"v_mul_f32 v13, v13, v61\n"
"v_mfma_f32_16x16x32_bf16 v[76:79], a[72:75], a[128:131], v[76:79]\n"
"v_mul_f32 v14, v14, v62\n"
"v_mfma_f32_16x16x32_bf16 v[80:83], a[72:75], a[136:139], v[80:83]\n"
"v_mul_f32 v15, v15, v63\n"
"v_mfma_f32_16x16x32_bf16 v[80:83], a[76:79], a[140:143], v[80:83]\n"
"v_cvt_pk_bf16_f32 v26, v12, v13\n"
"v_cvt_pk_bf16_f32 v27, v14, v15\n"
"v_mfma_f32_16x16x32_bf16 v[52:55], a[68:71], a[140:143], v[52:55]\n"
"ds_write_b64 v2, v[26:27] offset:8\n"
"v_mfma_f32_16x16x32_bf16 v[52:55], a[64:67], a[136:139], v[52:55]\n"
"s_waitcnt lgkmcnt(2)\n"
"v_lshlrev_b32 v12, 16, v20\n"
"v_and_b32 v13, s63, v20\n"
"v_mfma_f32_16x16x32_bf16 v[56:59], a[64:67], a[144:147], v[56:59]\n"
"v_lshlrev_b32 v14, 16, v21\n"
"v_and_b32 v15, s63, v21\n"
"v_mfma_f32_16x16x32_bf16 v[56:59], a[68:71], a[148:151], v[56:59]\n"
"global_load_dwordx4 a[60:63], v0, s[34:35] offset:3072 sc1\n"
"ds_read_b128 v[16:19], v2 offset:33792\n"
"v_mfma_f32_16x16x32_bf16 v[84:87], a[76:79], a[148:151], v[84:87]\n"
"v_mul_f32 v12, v12, v36\n"
"v_mul_f32 v13, v13, v37\n"
"v_mfma_f32_16x16x32_bf16 v[84:87], a[72:75], a[144:147], v[84:87]\n"
"v_mul_f32 v14, v14, v38\n"
"v_mul_f32 v15, v15, v39\n"
"v_mfma_f32_16x16x32_bf16 a[4:7], a[72:75], a[152:155], a[4:7]\n"
"s_add_u32 s34, s34, 8192\n"
"v_cvt_pk_bf16_f32 v28, v12, v13\n"
"v_cvt_pk_bf16_f32 v29, v14, v15\n"
"v_mfma_f32_16x16x32_bf16 a[4:7], a[76:79], a[156:159], a[4:7]\n"
"ds_write_b64 v2, v[28:29] offset:2048\n"
"v_mfma_f32_16x16x32_bf16 a[0:3], a[68:71], a[156:159], a[0:3]\n"
"v_lshlrev_b32 v12, 16, v22\n"
"v_mfma_f32_16x16x32_bf16 a[0:3], a[64:67], a[152:155], a[0:3]\n"
"s_waitcnt vmcnt(8)\n"
"v_and_b32 v13, s63, v22\n"
"v_lshlrev_b32 v14, 16, v23\n"
"v_mfma_f32_16x16x32_bf16 v[88:91], a[80:83], a[96:99], v[88:91]\n"
"v_and_b32 v15, s63, v23\n"
"v_mul_f32 v12, v12, v64\n"
"v_mfma_f32_16x16x32_bf16 v[88:91], a[84:87], a[100:103], v[88:91]\n"
"global_load_dwordx4 a[64:67], v0, s[34:35] offset:-4096 sc1\n"
"v_mul_f32 v13, v13, v65\n"
"v_mfma_f32_16x16x32_bf16 v[116:119], a[92:95], a[100:103], v[116:119]\n"
"ds_read_b128 a[100:103], v2 offset:17408\n"
"v_mul_f32 v14, v14, v66\n"
"v_mfma_f32_16x16x32_bf16 v[116:119], a[88:91], a[96:99], v[116:119]\n"
"ds_read_b128 a[96:99], v2 offset:16384\n"
"v_mul_f32 v15, v15, v67\n"
"v_mfma_f32_16x16x32_bf16 v[120:123], a[88:91], a[104:107], v[120:123]\n"
"v_cvt_pk_bf16_f32 v30, v12, v13\n"
"v_cvt_pk_bf16_f32 v31, v14, v15\n"
"v_mfma_f32_16x16x32_bf16 v[120:123], a[92:95], a[108:111], v[120:123]\n"
"global_store_dwordx4 v0, v[28:31], s[52:53] offset:-2048 sc1\n"
"ds_write_b64 v2, v[30:31] offset:2056\n"
"v_mfma_f32_16x16x32_bf16 v[92:95], a[84:87], a[108:111], v[92:95]\n"
"s_waitcnt lgkmcnt(4)\n"
"ds_read_b128 a[108:111], v2 offset:19456\n"
"v_lshlrev_b32 v12, 16, v16\n"
"v_mfma_f32_16x16x32_bf16 v[92:95], a[80:83], a[104:107], v[92:95]\n"
"ds_read_b128 a[104:107], v2 offset:18432\n"
"v_and_b32 v13, s63, v16\n"
"v_mfma_f32_16x16x32_bf16 v[96:99], a[80:83], a[112:115], v[96:99]\n"
"v_lshlrev_b32 v14, 16, v17\n"
"v_and_b32 v15, s63, v17\n"
"v_mfma_f32_16x16x32_bf16 v[96:99], a[84:87], a[116:119], v[96:99]\n"
"global_load_dwordx4 a[68:71], v0, s[34:35] offset:-3072 sc1\n"
"ds_read_b128 v[20:23], v2 offset:35840\n"
"v_mfma_f32_16x16x32_bf16 v[124:127], a[92:95], a[116:119], v[124:127]\n"
"ds_read_b128 a[116:119], v2 offset:21504\n"
"v_mfma_f32_16x16x32_bf16 v[124:127], a[88:91], a[112:115], v[124:127]\n"
"ds_read_b128 a[112:115], v2 offset:20480\n"
"v_mfma_f32_16x16x32_bf16 v[128:131], a[88:91], a[120:123], v[128:131]\n"
"v_mul_f32 v12, v12, v88\n"
"v_mul_f32 v13, v13, v89\n"
"v_mfma_f32_16x16x32_bf16 v[128:131], a[92:95], a[124:127], v[128:131]\n"
"v_mul_f32 v14, v14, v90\n"
"v_mul_f32 v15, v15, v91\n"
"v_mfma_f32_16x16x32_bf16 v[100:103], a[84:87], a[124:127], v[100:103]\n"
"ds_read_b128 a[124:127], v2 offset:23552\n"
"v_cvt_pk_bf16_f32 v24, v12, v13\n"
"v_mfma_f32_16x16x32_bf16 v[100:103], a[80:83], a[120:123], v[100:103]\n"
"ds_read_b128 a[120:123], v2 offset:22528\n"
"v_cvt_pk_bf16_f32 v25, v14, v15\n"
"v_mfma_f32_16x16x32_bf16 v[104:107], a[80:83], a[128:131], v[104:107]\n"
"ds_write_b64 v2, v[24:25] offset:1024\n"
"v_mfma_f32_16x16x32_bf16 v[104:107], a[84:87], a[132:135], v[104:107]\n"
"global_load_dwordx4 a[72:75], v0, s[34:35] offset:-2048 sc1\n"
"v_lshlrev_b32 v12, 16, v18\n"
"v_mfma_f32_16x16x32_bf16 v[132:135], a[92:95], a[132:135], v[132:135]\n"
"ds_read_b128 a[132:135], v2 offset:25600\n"
"v_and_b32 v13, s63, v18\n"
"v_mfma_f32_16x16x32_bf16 v[132:135], a[88:91], a[128:131], v[132:135]\n"
"ds_read_b128 a[128:131], v2 offset:24576\n"
"v_lshlrev_b32 v14, 16, v19\n"
"v_mfma_f32_16x16x32_bf16 v[136:139], a[88:91], a[136:139], v[136:139]\n"
"v_and_b32 v15, s63, v19\n"
"v_mul_f32 v12, v12, v116\n"
"v_mfma_f32_16x16x32_bf16 v[136:139], a[92:95], a[140:143], v[136:139]\n"
"v_mul_f32 v13, v13, v117\n"
"v_mul_f32 v14, v14, v118\n"
"v_mfma_f32_16x16x32_bf16 v[108:111], a[84:87], a[140:143], v[108:111]\n"
"ds_read_b128 a[140:143], v2 offset:27648\n"
"v_mul_f32 v15, v15, v119\n"
"v_mfma_f32_16x16x32_bf16 v[108:111], a[80:83], a[136:139], v[108:111]\n"
"ds_read_b128 a[136:139], v2 offset:26624\n"
"v_cvt_pk_bf16_f32 v26, v12, v13\n"
"v_mfma_f32_16x16x32_bf16 v[112:115], a[80:83], a[144:147], v[112:115]\n"
"v_cvt_pk_bf16_f32 v27, v14, v15\n"
"v_mfma_f32_16x16x32_bf16 v[112:115], a[84:87], a[148:151], v[112:115]\n"
"global_load_dwordx4 a[76:79], v0, s[34:35] offset:-1024 sc1\n"
"v_mfma_f32_16x16x32_bf16 v[140:143], a[92:95], a[148:151], v[140:143]\n"
"global_store_dwordx4 v0, v[24:27], s[52:53] offset:-3072 sc1\n"
"ds_read_b128 a[148:151], v2 offset:29696\n"
"ds_write_b64 v2, v[26:27] offset:1032\n"
"v_mfma_f32_16x16x32_bf16 v[140:143], a[88:91], a[144:147], v[140:143]\n"
"s_waitcnt lgkmcnt(11)\n"
"ds_read_b128 a[144:147], v2 offset:28672\n"
"v_lshlrev_b32 v12, 16, v20\n"
"v_mfma_f32_16x16x32_bf16 a[12:15], a[88:91], a[152:155], a[12:15]\n"
"v_and_b32 v13, s63, v20\n"
"v_lshlrev_b32 v14, 16, v21\n"
"v_mfma_f32_16x16x32_bf16 a[12:15], a[92:95], a[156:159], a[12:15]\n"
"ds_read_b128 v[16:19], v2 offset:36864\n"
"v_and_b32 v15, s63, v21\n"
"v_mfma_f32_16x16x32_bf16 a[8:11], a[84:87], a[156:159], a[8:11]\n"
"ds_read_b128 a[156:159], v2 offset:31744\n"
"v_mul_f32 v12, v12, v92\n"
"v_mfma_f32_16x16x32_bf16 a[8:11], a[80:83], a[152:155], a[8:11]\n"
"s_waitcnt vmcnt(10)\n"
"ds_read_b128 a[152:155], v2 offset:30720\n"
"v_mul_f32 v13, v13, v93\n"
"v_mfma_f32_16x16x32_bf16 v[144:147], a[32:35], a[96:99], v[144:147]\n"
"v_mul_f32 v14, v14, v94\n"
"v_mul_f32 v15, v15, v95\n"
"v_mfma_f32_16x16x32_bf16 v[144:147], a[36:39], a[100:103], v[144:147]\n"
"global_load_dwordx4 a[80:83], v0, s[34:35] offset:0 sc1\n"
"v_cvt_pk_bf16_f32 v28, v12, v13\n"
"v_mfma_f32_16x16x32_bf16 v[172:175], a[44:47], a[100:103], v[172:175]\n"
"v_cvt_pk_bf16_f32 v29, v14, v15\n"
"v_mfma_f32_16x16x32_bf16 v[172:175], a[40:43], a[96:99], v[172:175]\n"
"ds_write_b64 v2, v[28:29] offset:3072\n"
"v_mfma_f32_16x16x32_bf16 v[176:179], a[40:43], a[104:107], v[176:179]\n"
"v_lshlrev_b32 v12, 16, v22\n"
"v_and_b32 v13, s63, v22\n"
"v_mfma_f32_16x16x32_bf16 v[176:179], a[44:47], a[108:111], v[176:179]\n"
"v_lshlrev_b32 v14, 16, v23\n"
"v_and_b32 v15, s63, v23\n"
"v_mfma_f32_16x16x32_bf16 v[148:151], a[36:39], a[108:111], v[148:151]\n"
"v_mul_f32 v12, v12, v120\n"
"v_mfma_f32_16x16x32_bf16 v[148:151], a[32:35], a[104:107], v[148:151]\n"
"s_waitcnt lgkmcnt(12)\n"
"v_mul_f32 v13, v13, v121\n"
"v_mul_f32 v14, v14, v122\n"
"v_mfma_f32_16x16x32_bf16 v[152:155], a[32:35], a[112:115], v[152:155]\n"
"v_mul_f32 v15, v15, v123\n"
"v_mfma_f32_16x16x32_bf16 v[152:155], a[36:39], a[116:119], v[152:155]\n"
"global_load_dwordx4 a[84:87], v0, s[34:35] offset:1024 sc1\n"
"v_cvt_pk_bf16_f32 v30, v12, v13\n"
"v_mfma_f32_16x16x32_bf16 v[180:183], a[44:47], a[116:119], v[180:183]\n"
"v_cvt_pk_bf16_f32 v31, v14, v15\n"
"v_mfma_f32_16x16x32_bf16 v[180:183], a[40:43], a[112:115], v[180:183]\n"
"global_store_dwordx4 v0, v[28:31], s[52:53] offset:-1024 sc1\n"
"ds_write_b64 v2, v[30:31] offset:3080\n"
"v_mfma_f32_16x16x32_bf16 v[184:187], a[40:43], a[120:123], v[184:187]\n"
"s_waitcnt lgkmcnt(4)\n"
"v_lshlrev_b32 v12, 16, v16\n"
"v_and_b32 v13, s63, v16\n"
"v_mfma_f32_16x16x32_bf16 v[184:187], a[44:47], a[124:127], v[184:187]\n"
"v_lshlrev_b32 v14, 16, v17\n"
"v_mfma_f32_16x16x32_bf16 v[156:159], a[36:39], a[124:127], v[156:159]\n"
"v_and_b32 v15, s63, v17\n"
"v_mfma_f32_16x16x32_bf16 v[156:159], a[32:35], a[120:123], v[156:159]\n"
"ds_read_b128 v[20:23], v2 offset:37888\n"
"v_mul_f32 v12, v12, v40\n"
"v_mfma_f32_16x16x32_bf16 v[160:163], a[32:35], a[128:131], v[160:163]\n"
"v_mul_f32 v13, v13, v41\n"
"v_mfma_f32_16x16x32_bf16 v[160:163], a[36:39], a[132:135], v[160:163]\n"
"global_load_dwordx4 a[88:91], v0, s[34:35] offset:2048 sc1\n"
"v_mul_f32 v14, v14, v42\n"
"v_mfma_f32_16x16x32_bf16 v[188:191], a[44:47], a[132:135], v[188:191]\n"
"v_mul_f32 v15, v15, v43\n"
"v_cvt_pk_bf16_f32 v24, v12, v13\n"
"v_mfma_f32_16x16x32_bf16 v[188:191], a[40:43], a[128:131], v[188:191]\n"
"v_cvt_pk_bf16_f32 v25, v14, v15\n"
"v_mfma_f32_16x16x32_bf16 v[192:195], a[40:43], a[136:139], v[192:195]\n"
"ds_write_b64 v2, v[24:25] offset:4096\n"
"v_mfma_f32_16x16x32_bf16 v[192:195], a[44:47], a[140:143], v[192:195]\n"
"v_lshlrev_b32 v12, 16, v18\n"
"v_and_b32 v13, s63, v18\n"
"v_mfma_f32_16x16x32_bf16 v[164:167], a[36:39], a[140:143], v[164:167]\n"
"v_lshlrev_b32 v14, 16, v19\n"
"v_mfma_f32_16x16x32_bf16 v[164:167], a[32:35], a[136:139], v[164:167]\n"
"s_waitcnt lgkmcnt(4)\n"
"v_and_b32 v15, s63, v19\n"
"v_mul_f32 v12, v12, v68\n"
"v_mfma_f32_16x16x32_bf16 v[168:171], a[32:35], a[144:147], v[168:171]\n"
"v_mul_f32 v13, v13, v69\n"
"v_mul_f32 v14, v14, v70\n"
"v_mfma_f32_16x16x32_bf16 v[168:171], a[36:39], a[148:151], v[168:171]\n"
"global_load_dwordx4 a[92:95], v0, s[34:35] offset:3072 sc1\n"
"v_mul_f32 v15, v15, v71\n"
"v_mfma_f32_16x16x32_bf16 v[196:199], a[44:47], a[148:151], v[196:199]\n"
"v_cvt_pk_bf16_f32 v26, v12, v13\n"
"v_cvt_pk_bf16_f32 v27, v14, v15\n"
"v_mfma_f32_16x16x32_bf16 v[196:199], a[40:43], a[144:147], v[196:199]\n"
"s_waitcnt lgkmcnt(1)\n"
"global_store_dwordx4 v0, v[24:27], s[52:53] offset:0 sc1\n"
"ds_write_b64 v2, v[26:27] offset:4104\n"
"v_mfma_f32_16x16x32_bf16 a[20:23], a[40:43], a[152:155], a[20:23]\n"
"s_add_u32 s34, s34, 8192\n"
"v_lshlrev_b32 v12, 16, v20\n"
"v_mfma_f32_16x16x32_bf16 a[20:23], a[44:47], a[156:159], a[20:23]\n"
"v_and_b32 v13, s63, v20\n"
"v_mfma_f32_16x16x32_bf16 a[16:19], a[36:39], a[156:159], a[16:19]\n"
"v_lshlrev_b32 v14, 16, v21\n"
"v_and_b32 v15, s63, v21\n"
"v_mfma_f32_16x16x32_bf16 a[16:19], a[32:35], a[152:155], a[16:19]\n"
"s_waitcnt vmcnt(12)\n"
"ds_read_b128 v[16:19], v2 offset:38912\n"
"v_mul_f32 v12, v12, v96\n"
"v_mfma_f32_16x16x32_bf16 v[200:203], a[48:51], a[96:99], v[200:203]\n"
"v_mul_f32 v13, v13, v97\n"
"v_mul_f32 v14, v14, v98\n"
"v_mfma_f32_16x16x32_bf16 v[200:203], a[52:55], a[100:103], v[200:203]\n"
"global_load_dwordx4 a[32:35], v0, s[34:35] offset:-4096 sc1\n"
"v_mul_f32 v15, v15, v99\n"
"v_mfma_f32_16x16x32_bf16 v[228:231], a[60:63], a[100:103], v[228:231]\n"
"ds_read_b128 a[100:103], v3 offset:17408\n"
"v_cvt_pk_bf16_f32 v28, v12, v13\n"
"v_mfma_f32_16x16x32_bf16 v[228:231], a[56:59], a[96:99], v[228:231]\n"
"ds_read_b128 a[96:99], v3 offset:16384\n"
"v_cvt_pk_bf16_f32 v29, v14, v15\n"
"v_mfma_f32_16x16x32_bf16 v[232:235], a[56:59], a[104:107], v[232:235]\n"
"ds_write_b64 v2, v[28:29] offset:5120\n"
"v_mfma_f32_16x16x32_bf16 v[232:235], a[60:63], a[108:111], v[232:235]\n"
"v_lshlrev_b32 v12, 16, v22\n"
"v_mfma_f32_16x16x32_bf16 v[204:207], a[52:55], a[108:111], v[204:207]\n"
"ds_read_b128 a[108:111], v3 offset:19456\n"
"v_and_b32 v13, s63, v22\n"
"v_mfma_f32_16x16x32_bf16 v[204:207], a[48:51], a[104:107], v[204:207]\n"
"ds_read_b128 a[104:107], v3 offset:18432\n"
"v_lshlrev_b32 v14, 16, v23\n"
"v_mfma_f32_16x16x32_bf16 v[208:211], a[48:51], a[112:115], v[208:211]\n"
"v_and_b32 v15, s63, v23\n"
"v_mul_f32 v12, v12, v124\n"
"v_mfma_f32_16x16x32_bf16 v[208:211], a[52:55], a[116:119], v[208:211]\n"
"global_load_dwordx4 a[36:39], v0, s[34:35] offset:-3072 sc1\n"
"v_mul_f32 v13, v13, v125\n"
"v_mfma_f32_16x16x32_bf16 v[236:239], a[60:63], a[116:119], v[236:239]\n"
"ds_read_b128 a[116:119], v3 offset:21504\n"
"v_mul_f32 v14, v14, v126\n"
"v_mfma_f32_16x16x32_bf16 v[236:239], a[56:59], a[112:115], v[236:239]\n"
"ds_read_b128 a[112:115], v3 offset:20480\n"
"v_mul_f32 v15, v15, v127\n"
"v_mfma_f32_16x16x32_bf16 v[240:243], a[56:59], a[120:123], v[240:243]\n"
"v_cvt_pk_bf16_f32 v30, v12, v13\n"
"v_cvt_pk_bf16_f32 v31, v14, v15\n"
"v_mfma_f32_16x16x32_bf16 v[240:243], a[60:63], a[124:127], v[240:243]\n"
"ds_write_b64 v2, v[30:31] offset:5128\n"
"v_mfma_f32_16x16x32_bf16 v[212:215], a[52:55], a[124:127], v[212:215]\n"
"s_waitcnt lgkmcnt(8)\n"
"ds_read_b128 a[124:127], v3 offset:23552\n"
"v_lshlrev_b32 v12, 16, v16\n"
"v_mfma_f32_16x16x32_bf16 v[212:215], a[48:51], a[120:123], v[212:215]\n"
"ds_read_b128 a[120:123], v3 offset:22528\n"
"v_and_b32 v13, s63, v16\n"
"v_mfma_f32_16x16x32_bf16 v[216:219], a[48:51], a[128:131], v[216:219]\n"
"v_lshlrev_b32 v14, 16, v17\n"
"v_and_b32 v15, s63, v17\n"
"v_mfma_f32_16x16x32_bf16 v[216:219], a[52:55], a[132:135], v[216:219]\n"
"global_load_dwordx4 a[40:43], v0, s[34:35] offset:-2048 sc1\n"
"ds_read_b128 v[20:23], v2 offset:39936\n"
"v_mfma_f32_16x16x32_bf16 v[244:247], a[60:63], a[132:135], v[244:247]\n"
"ds_read_b128 a[132:135], v3 offset:25600\n"
"v_mul_f32 v12, v12, v44\n"
"v_mfma_f32_16x16x32_bf16 v[244:247], a[56:59], a[128:131], v[244:247]\n"
"ds_read_b128 a[128:131], v3 offset:24576\n"
"v_mul_f32 v13, v13, v45\n"
"v_mfma_f32_16x16x32_bf16 v[248:251], a[56:59], a[136:139], v[248:251]\n"
"v_mul_f32 v14, v14, v46\n"
"v_mul_f32 v15, v15, v47\n"
"v_mfma_f32_16x16x32_bf16 v[248:251], a[60:63], a[140:143], v[248:251]\n"
"v_cvt_pk_bf16_f32 v24, v12, v13\n"
"v_mfma_f32_16x16x32_bf16 v[220:223], a[52:55], a[140:143], v[220:223]\n"
"ds_read_b128 a[140:143], v3 offset:27648\n"
"v_cvt_pk_bf16_f32 v25, v14, v15\n"
"v_mfma_f32_16x16x32_bf16 v[220:223], a[48:51], a[136:139], v[220:223]\n"
"ds_read_b128 a[136:139], v3 offset:26624\n"
"v_mfma_f32_16x16x32_bf16 v[224:227], a[48:51], a[144:147], v[224:227]\n"
"ds_write_b64 v2, v[24:25] offset:6144\n"
"v_mfma_f32_16x16x32_bf16 v[224:227], a[52:55], a[148:151], v[224:227]\n"
"global_load_dwordx4 a[44:47], v0, s[34:35] offset:-1024 sc1\n"
"v_lshlrev_b32 v12, 16, v18\n"
"v_mfma_f32_16x16x32_bf16 v[252:255], a[60:63], a[148:151], v[252:255]\n"
"ds_read_b128 a[148:151], v3 offset:29696\n"
"v_and_b32 v13, s63, v18\n"
"v_mfma_f32_16x16x32_bf16 v[252:255], a[56:59], a[144:147], v[252:255]\n"
"ds_read_b128 a[144:147], v3 offset:28672\n"
"v_lshlrev_b32 v14, 16, v19\n"
"v_mfma_f32_16x16x32_bf16 a[28:31], a[56:59], a[152:155], a[28:31]\n"
"v_and_b32 v15, s63, v19\n"
"v_mul_f32 v12, v12, v72\n"
"v_mfma_f32_16x16x32_bf16 a[28:31], a[60:63], a[156:159], a[28:31]\n"
"v_mul_f32 v13, v13, v73\n"
"v_mul_f32 v14, v14, v74\n"
"v_mfma_f32_16x16x32_bf16 a[24:27], a[52:55], a[156:159], a[24:27]\n"
"ds_read_b128 a[156:159], v3 offset:31744\n"
"v_mul_f32 v15, v15, v75\n"
"v_mfma_f32_16x16x32_bf16 a[24:27], a[48:51], a[152:155], a[24:27]\n"
"s_waitcnt vmcnt(11) lgkmcnt(14)\n"
"ds_read_b128 a[152:155], v3 offset:30720\n"
"v_cvt_pk_bf16_f32 v26, v12, v13\n"
"v_mfma_f32_16x16x32_bf16 v[144:147], a[64:67], a[96:99], v[144:147]\n"
"v_cvt_pk_bf16_f32 v27, v14, v15\n"
"v_mfma_f32_16x16x32_bf16 v[144:147], a[68:71], a[100:103], v[144:147]\n"
"global_load_dwordx4 a[48:51], v0, s[34:35] offset:0 sc1\n"
"v_mfma_f32_16x16x32_bf16 v[172:175], a[76:79], a[100:103], v[172:175]\n"
"s_waitcnt lgkmcnt(9)\n"
"global_store_dwordx4 v0, v[24:27], s[52:53] offset:2048 sc1\n"
"ds_write_b64 v2, v[26:27] offset:6152\n"
"v_lshlrev_b32 v12, 16, v20\n"
"v_mfma_f32_16x16x32_bf16 v[172:175], a[72:75], a[96:99], v[172:175]\n"
"v_and_b32 v13, s63, v20\n"
"v_lshlrev_b32 v14, 16, v21\n"
"v_mfma_f32_16x16x32_bf16 v[176:179], a[72:75], a[104:107], v[176:179]\n"
"v_and_b32 v15, s63, v21\n"
"v_mul_f32 v12, v12, v100\n"
"v_mfma_f32_16x16x32_bf16 v[176:179], a[76:79], a[108:111], v[176:179]\n"
"v_mul_f32 v13, v13, v101\n"
"v_mul_f32 v14, v14, v102\n"
"v_mfma_f32_16x16x32_bf16 v[148:151], a[68:71], a[108:111], v[148:151]\n"
"v_mul_f32 v15, v15, v103\n"
"v_cvt_pk_bf16_f32 v28, v12, v13\n"
"v_mfma_f32_16x16x32_bf16 v[148:151], a[64:67], a[104:107], v[148:151]\n"
"v_cvt_pk_bf16_f32 v29, v14, v15\n"
"v_mfma_f32_16x16x32_bf16 v[152:155], a[64:67], a[112:115], v[152:155]\n"
"ds_write_b64 v2, v[28:29] offset:7168\n"
"v_mfma_f32_16x16x32_bf16 v[152:155], a[68:71], a[116:119], v[152:155]\n"
"global_load_dwordx4 a[52:55], v0, s[34:35] offset:1024 sc1\n"
"v_lshlrev_b32 v12, 16, v22\n"
"v_mfma_f32_16x16x32_bf16 v[180:183], a[76:79], a[116:119], v[180:183]\n"
"v_and_b32 v13, s63, v22\n"
"v_lshlrev_b32 v14, 16, v23\n"
"v_mfma_f32_16x16x32_bf16 v[180:183], a[72:75], a[112:115], v[180:183]\n"
"v_and_b32 v15, s63, v23\n"
"v_mul_f32 v12, v12, v128\n"
"v_mfma_f32_16x16x32_bf16 v[184:187], a[72:75], a[120:123], v[184:187]\n"
"v_mul_f32 v13, v13, v129\n"
"v_mfma_f32_16x16x32_bf16 v[184:187], a[76:79], a[124:127], v[184:187]\n"
"v_mul_f32 v14, v14, v130\n"
"v_mul_f32 v15, v15, v131\n"
"v_mfma_f32_16x16x32_bf16 v[156:159], a[68:71], a[124:127], v[156:159]\n"
"v_cvt_pk_bf16_f32 v30, v12, v13\n"
"v_mfma_f32_16x16x32_bf16 v[156:159], a[64:67], a[120:123], v[156:159]\n"
"s_waitcnt lgkmcnt(7)\n"
"v_cvt_pk_bf16_f32 v31, v14, v15\n"
"v_mfma_f32_16x16x32_bf16 v[160:163], a[64:67], a[128:131], v[160:163]\n"
"global_store_dwordx4 v0, v[28:31], s[52:53] offset:3072 sc1\n"
"ds_write_b64 v2, v[30:31] offset:7176\n"
"v_mfma_f32_16x16x32_bf16 v[160:163], a[68:71], a[132:135], v[160:163]\n"
"global_load_dwordx4 a[56:59], v0, s[34:35] offset:2048 sc1\n"
"v_accvgpr_read_b32 v16, a160\n"
"v_mfma_f32_16x16x32_bf16 v[188:191], a[76:79], a[132:135], v[188:191]\n"
"v_accvgpr_read_b32 v17, a161\n"
"v_accvgpr_read_b32 v18, a162\n"
"v_mfma_f32_16x16x32_bf16 v[188:191], a[72:75], a[128:131], v[188:191]\n"
"v_accvgpr_read_b32 v19, a163\n"
"v_mfma_f32_16x16x32_bf16 v[192:195], a[72:75], a[136:139], v[192:195]\n"
"v_lshlrev_b32 v12, 16, v16\n"
"v_mfma_f32_16x16x32_bf16 v[192:195], a[76:79], a[140:143], v[192:195]\n"
"v_and_b32 v13, s63, v16\n"
"v_lshlrev_b32 v14, 16, v17\n"
"v_mfma_f32_16x16x32_bf16 v[164:167], a[68:71], a[140:143], v[164:167]\n"
"v_and_b32 v15, s63, v17\n"
"v_mfma_f32_16x16x32_bf16 v[164:167], a[64:67], a[136:139], v[164:167]\n"
"s_waitcnt lgkmcnt(3)\n"
"v_mul_f32 v12, v12, v48\n"
"v_mfma_f32_16x16x32_bf16 v[168:171], a[64:67], a[144:147], v[168:171]\n"
"v_mul_f32 v13, v13, v49\n"
"v_mul_f32 v14, v14, v50\n"
"v_mfma_f32_16x16x32_bf16 v[168:171], a[68:71], a[148:151], v[168:171]\n"
"global_load_dwordx4 a[60:63], v0, s[34:35] offset:3072 sc1\n"
"v_mul_f32 v15, v15, v51\n"
"v_mfma_f32_16x16x32_bf16 v[196:199], a[76:79], a[148:151], v[196:199]\n"
"v_cvt_pk_bf16_f32 v24, v12, v13\n"
"v_cvt_pk_bf16_f32 v25, v14, v15\n"
"v_mfma_f32_16x16x32_bf16 v[196:199], a[72:75], a[144:147], v[196:199]\n"
"ds_write_b64 v2, v[24:25] offset:8192\n"
"v_mfma_f32_16x16x32_bf16 a[20:23], a[72:75], a[152:155], a[20:23]\n"
"s_add_u32 s34, s34, 8192\n"
"v_lshlrev_b32 v12, 16, v18\n"
"v_mfma_f32_16x16x32_bf16 a[20:23], a[76:79], a[156:159], a[20:23]\n"
"v_and_b32 v13, s63, v18\n"
"v_lshlrev_b32 v14, 16, v19\n"
"v_mfma_f32_16x16x32_bf16 a[16:19], a[68:71], a[156:159], a[16:19]\n"
"v_and_b32 v15, s63, v19\n"
"v_mfma_f32_16x16x32_bf16 a[16:19], a[64:67], a[152:155], a[16:19]\n"
"s_waitcnt vmcnt(11)\n"
"v_mul_f32 v12, v12, v76\n"
"v_mfma_f32_16x16x32_bf16 v[200:203], a[80:83], a[96:99], v[200:203]\n"
"v_mul_f32 v13, v13, v77\n"
"v_mul_f32 v14, v14, v78\n"
"v_mfma_f32_16x16x32_bf16 v[200:203], a[84:87], a[100:103], v[200:203]\n"
"global_load_dwordx4 a[64:67], v0, s[34:35] offset:-4096 sc1\n"
"v_mul_f32 v15, v15, v79\n"
"v_mfma_f32_16x16x32_bf16 v[228:231], a[92:95], a[100:103], v[228:231]\n"
"ds_read_b128 a[100:103], v4 offset:17408\n"
"v_cvt_pk_bf16_f32 v26, v12, v13\n"
"v_mfma_f32_16x16x32_bf16 v[228:231], a[88:91], a[96:99], v[228:231]\n"
"ds_read_b128 a[96:99], v4 offset:16384\n"
"v_cvt_pk_bf16_f32 v27, v14, v15\n"
"v_mfma_f32_16x16x32_bf16 v[232:235], a[88:91], a[104:107], v[232:235]\n"
"global_store_dwordx4 v0, v[24:27], s[54:55] offset:-4096 sc1\n"
"ds_write_b64 v2, v[26:27] offset:8200\n"
"v_mfma_f32_16x16x32_bf16 v[232:235], a[92:95], a[108:111], v[232:235]\n"
"v_accvgpr_read_b32 v16, a164\n"
"v_accvgpr_read_b32 v17, a165\n"
"v_mfma_f32_16x16x32_bf16 v[204:207], a[84:87], a[108:111], v[204:207]\n"
"ds_read_b128 a[108:111], v4 offset:19456\n"
"v_accvgpr_read_b32 v18, a166\n"
"v_mfma_f32_16x16x32_bf16 v[204:207], a[80:83], a[104:107], v[204:207]\n"
"ds_read_b128 a[104:107], v4 offset:18432\n"
"v_accvgpr_read_b32 v19, a167\n"
"v_mfma_f32_16x16x32_bf16 v[208:211], a[80:83], a[112:115], v[208:211]\n"
"v_lshlrev_b32 v12, 16, v16\n"
"v_and_b32 v13, s63, v16\n"
"v_mfma_f32_16x16x32_bf16 v[208:211], a[84:87], a[116:119], v[208:211]\n"
"global_load_dwordx4 a[68:71], v0, s[34:35] offset:-3072 sc1\n"
"v_lshlrev_b32 v14, 16, v17\n"
"v_mfma_f32_16x16x32_bf16 v[236:239], a[92:95], a[116:119], v[236:239]\n"
"ds_read_b128 a[116:119], v4 offset:21504\n"
"v_and_b32 v15, s63, v17\n"
"v_mfma_f32_16x16x32_bf16 v[236:239], a[88:91], a[112:115], v[236:239]\n"
"ds_read_b128 a[112:115], v4 offset:20480\n"
"v_mul_f32 v12, v12, v104\n"
"v_mfma_f32_16x16x32_bf16 v[240:243], a[88:91], a[120:123], v[240:243]\n"
"v_mul_f32 v13, v13, v105\n"
"v_mul_f32 v14, v14, v106\n"
"v_mfma_f32_16x16x32_bf16 v[240:243], a[92:95], a[124:127], v[240:243]\n"
"v_mul_f32 v15, v15, v107\n"
"v_cvt_pk_bf16_f32 v28, v12, v13\n"
"v_mfma_f32_16x16x32_bf16 v[212:215], a[84:87], a[124:127], v[212:215]\n"
"ds_read_b128 a[124:127], v4 offset:23552\n"
"v_cvt_pk_bf16_f32 v29, v14, v15\n"
"v_mfma_f32_16x16x32_bf16 v[212:215], a[80:83], a[120:123], v[212:215]\n"
"ds_read_b128 a[120:123], v4 offset:22528\n"
"v_mfma_f32_16x16x32_bf16 v[216:219], a[80:83], a[128:131], v[216:219]\n"
"ds_write_b64 v2, v[28:29] offset:9216\n"
"v_mfma_f32_16x16x32_bf16 v[216:219], a[84:87], a[132:135], v[216:219]\n"
"global_load_dwordx4 a[72:75], v0, s[34:35] offset:-2048 sc1\n"
"v_lshlrev_b32 v12, 16, v18\n"
"v_mfma_f32_16x16x32_bf16 v[244:247], a[92:95], a[132:135], v[244:247]\n"
"ds_read_b128 a[132:135], v4 offset:25600\n"
"v_and_b32 v13, s63, v18\n"
"v_mfma_f32_16x16x32_bf16 v[244:247], a[88:91], a[128:131], v[244:247]\n"
"ds_read_b128 a[128:131], v4 offset:24576\n"
"v_lshlrev_b32 v14, 16, v19\n"
"v_mfma_f32_16x16x32_bf16 v[248:251], a[88:91], a[136:139], v[248:251]\n"
"v_and_b32 v15, s63, v19\n"
"v_mul_f32 v12, v12, v132\n"
"v_mfma_f32_16x16x32_bf16 v[248:251], a[92:95], a[140:143], v[248:251]\n"
"v_mul_f32 v13, v13, v133\n"
"v_mul_f32 v14, v14, v134\n"
"v_mfma_f32_16x16x32_bf16 v[220:223], a[84:87], a[140:143], v[220:223]\n"
"ds_read_b128 a[140:143], v4 offset:27648\n"
"v_mul_f32 v15, v15, v135\n"
"v_mfma_f32_16x16x32_bf16 v[220:223], a[80:83], a[136:139], v[220:223]\n"
"ds_read_b128 a[136:139], v4 offset:26624\n"
"v_cvt_pk_bf16_f32 v30, v12, v13\n"
"v_mfma_f32_16x16x32_bf16 v[224:227], a[80:83], a[144:147], v[224:227]\n"
"v_cvt_pk_bf16_f32 v31, v14, v15\n"
"v_mfma_f32_16x16x32_bf16 v[224:227], a[84:87], a[148:151], v[224:227]\n"
"global_load_dwordx4 a[76:79], v0, s[34:35] offset:-1024 sc1\n"
"v_mfma_f32_16x16x32_bf16 v[252:255], a[92:95], a[148:151], v[252:255]\n"
"global_store_dwordx4 v0, v[28:31], s[54:55] offset:-3072 sc1\n"
"ds_read_b128 a[148:151], v4 offset:29696\n"
"ds_write_b64 v2, v[30:31] offset:9224\n"
"v_mfma_f32_16x16x32_bf16 v[252:255], a[88:91], a[144:147], v[252:255]\n"
"ds_read_b128 a[144:147], v4 offset:28672\n"
"v_accvgpr_read_b32 v16, a168\n"
"v_mfma_f32_16x16x32_bf16 a[28:31], a[88:91], a[152:155], a[28:31]\n"
"v_accvgpr_read_b32 v17, a169\n"
"v_accvgpr_read_b32 v18, a170\n"
"v_mfma_f32_16x16x32_bf16 a[28:31], a[92:95], a[156:159], a[28:31]\n"
"v_accvgpr_read_b32 v19, a171\n"
"v_lshlrev_b32 v12, 16, v16\n"
"v_mfma_f32_16x16x32_bf16 a[24:27], a[84:87], a[156:159], a[24:27]\n"
"ds_read_b128 a[156:159], v4 offset:31744\n"
"v_and_b32 v13, s63, v16\n"
"v_mfma_f32_16x16x32_bf16 a[24:27], a[80:83], a[152:155], a[24:27]\n"
"s_waitcnt vmcnt(12) lgkmcnt(13)\n"
"ds_read_b128 a[152:155], v4 offset:30720\n"
"v_lshlrev_b32 v14, 16, v17\n"
"v_mfma_f32_16x16x32_bf16 v[144:147], a[32:35], a[96:99], v[144:147]\n"
"v_and_b32 v15, s63, v17\n"
"v_mul_f32 v12, v12, v52\n"
"v_mfma_f32_16x16x32_bf16 v[144:147], a[36:39], a[100:103], v[144:147]\n"
"global_load_dwordx4 a[80:83], v0, s[34:35] offset:0 sc1\n"
"v_mul_f32 v13, v13, v53\n"
"v_mfma_f32_16x16x32_bf16 v[172:175], a[44:47], a[100:103], v[172:175]\n"
"v_mul_f32 v14, v14, v54\n"
"v_mul_f32 v15, v15, v55\n"
"v_mfma_f32_16x16x32_bf16 v[172:175], a[40:43], a[96:99], v[172:175]\n"
"v_cvt_pk_bf16_f32 v24, v12, v13\n"
"v_cvt_pk_bf16_f32 v25, v14, v15\n"
"v_mfma_f32_16x16x32_bf16 v[176:179], a[40:43], a[104:107], v[176:179]\n"
"ds_write_b64 v2, v[24:25] offset:10240\n"
"v_mfma_f32_16x16x32_bf16 v[176:179], a[44:47], a[108:111], v[176:179]\n"
"v_lshlrev_b32 v12, 16, v18\n"
"v_and_b32 v13, s63, v18\n"
"v_mfma_f32_16x16x32_bf16 v[148:151], a[36:39], a[108:111], v[148:151]\n"
"v_lshlrev_b32 v14, 16, v19\n"
"v_and_b32 v15, s63, v19\n"
"v_mfma_f32_16x16x32_bf16 v[148:151], a[32:35], a[104:107], v[148:151]\n"
"s_waitcnt lgkmcnt(11)\n"
"v_mul_f32 v12, v12, v80\n"
"v_mul_f32 v13, v13, v81\n"
"v_mfma_f32_16x16x32_bf16 v[152:155], a[32:35], a[112:115], v[152:155]\n"
"v_mul_f32 v14, v14, v82\n"
"v_mfma_f32_16x16x32_bf16 v[152:155], a[36:39], a[116:119], v[152:155]\n"
"global_load_dwordx4 a[84:87], v0, s[34:35] offset:1024 sc1\n"
"v_mul_f32 v15, v15, v83\n"
"v_mfma_f32_16x16x32_bf16 v[180:183], a[44:47], a[116:119], v[180:183]\n"
"v_cvt_pk_bf16_f32 v26, v12, v13\n"
"v_cvt_pk_bf16_f32 v27, v14, v15\n"
"v_mfma_f32_16x16x32_bf16 v[180:183], a[40:43], a[112:115], v[180:183]\n"
"ds_write_b64 v2, v[26:27] offset:10248\n"
"v_mfma_f32_16x16x32_bf16 v[184:187], a[40:43], a[120:123], v[184:187]\n"
"v_accvgpr_read_b32 v16, a172\n"
"v_mfma_f32_16x16x32_bf16 v[184:187], a[44:47], a[124:127], v[184:187]\n"
"v_accvgpr_read_b32 v17, a173\n"
"v_accvgpr_read_b32 v18, a174\n"
"v_mfma_f32_16x16x32_bf16 v[156:159], a[36:39], a[124:127], v[156:159]\n"
"v_accvgpr_read_b32 v19, a175\n"
"v_mfma_f32_16x16x32_bf16 v[156:159], a[32:35], a[120:123], v[156:159]\n"
"s_waitcnt lgkmcnt(7)\n"
"v_lshlrev_b32 v12, 16, v16\n"
"v_and_b32 v13, s63, v16\n"
"v_mfma_f32_16x16x32_bf16 v[160:163], a[32:35], a[128:131], v[160:163]\n"
"v_lshlrev_b32 v14, 16, v17\n"
"v_mfma_f32_16x16x32_bf16 v[160:163], a[36:39], a[132:135], v[160:163]\n"
"global_load_dwordx4 a[88:91], v0, s[34:35] offset:2048 sc1\n"
"v_and_b32 v15, s63, v17\n"
"v_mfma_f32_16x16x32_bf16 v[188:191], a[44:47], a[132:135], v[188:191]\n"
"v_mul_f32 v12, v12, v108\n"
"v_mul_f32 v13, v13, v109\n"
"v_mfma_f32_16x16x32_bf16 v[188:191], a[40:43], a[128:131], v[188:191]\n"
"v_mul_f32 v14, v14, v110\n"
"v_mfma_f32_16x16x32_bf16 v[192:195], a[40:43], a[136:139], v[192:195]\n"
"v_mul_f32 v15, v15, v111\n"
"v_mfma_f32_16x16x32_bf16 v[192:195], a[44:47], a[140:143], v[192:195]\n"
"v_cvt_pk_bf16_f32 v28, v12, v13\n"
"v_cvt_pk_bf16_f32 v29, v14, v15\n"
"v_mfma_f32_16x16x32_bf16 v[164:167], a[36:39], a[140:143], v[164:167]\n"
"ds_write_b64 v2, v[28:29] offset:11264\n"
"v_mfma_f32_16x16x32_bf16 v[164:167], a[32:35], a[136:139], v[164:167]\n"
"s_waitcnt lgkmcnt(3)\n"
"v_lshlrev_b32 v12, 16, v18\n"
"v_mfma_f32_16x16x32_bf16 v[168:171], a[32:35], a[144:147], v[168:171]\n"
"v_and_b32 v13, s63, v18\n"
"v_lshlrev_b32 v14, 16, v19\n"
"v_mfma_f32_16x16x32_bf16 v[168:171], a[36:39], a[148:151], v[168:171]\n"
"global_load_dwordx4 a[92:95], v0, s[34:35] offset:3072 sc1\n"
"v_and_b32 v15, s63, v19\n"
"v_mfma_f32_16x16x32_bf16 v[196:199], a[44:47], a[148:151], v[196:199]\n"
"v_mul_f32 v12, v12, v136\n"
"v_mul_f32 v13, v13, v137\n"
"v_mfma_f32_16x16x32_bf16 v[196:199], a[40:43], a[144:147], v[196:199]\n"
"v_mul_f32 v14, v14, v138\n"
"v_mfma_f32_16x16x32_bf16 a[20:23], a[40:43], a[152:155], a[20:23]\n"
"s_add_u32 s34, s34, 8192\n"
"v_mul_f32 v15, v15, v139\n"
"v_mfma_f32_16x16x32_bf16 a[20:23], a[44:47], a[156:159], a[20:23]\n"
"v_cvt_pk_bf16_f32 v30, v12, v13\n"
"v_cvt_pk_bf16_f32 v31, v14, v15\n"
"v_mfma_f32_16x16x32_bf16 a[16:19], a[36:39], a[156:159], a[16:19]\n"
"global_store_dwordx4 v0, v[28:31], s[54:55] offset:-1024 sc1\n"
"ds_write_b64 v2, v[30:31] offset:11272\n"
"v_accvgpr_read_b32 v16, a176\n"
"v_mfma_f32_16x16x32_bf16 a[16:19], a[32:35], a[152:155], a[16:19]\n"
"s_waitcnt vmcnt(11)\n"
"v_accvgpr_read_b32 v17, a177\n"
"v_accvgpr_read_b32 v18, a178\n"
"v_mfma_f32_16x16x32_bf16 v[200:203], a[48:51], a[96:99], v[200:203]\n"
"v_accvgpr_read_b32 v19, a179\n"
"v_lshlrev_b32 v12, 16, v16\n"
"v_mfma_f32_16x16x32_bf16 v[200:203], a[52:55], a[100:103], v[200:203]\n"
"global_load_dwordx4 a[32:35], v0, s[34:35] offset:-4096 sc1\n"
"v_and_b32 v13, s63, v16\n"
"v_mfma_f32_16x16x32_bf16 v[228:231], a[60:63], a[100:103], v[228:231]\n"
"ds_read_b128 a[100:103], v5 offset:17408\n"
"v_lshlrev_b32 v14, 16, v17\n"
"v_mfma_f32_16x16x32_bf16 v[228:231], a[56:59], a[96:99], v[228:231]\n"
"ds_read_b128 a[96:99], v5 offset:16384\n"
"v_and_b32 v15, s63, v17\n"
"v_mfma_f32_16x16x32_bf16 v[232:235], a[56:59], a[104:107], v[232:235]\n"
"v_mul_f32 v12, v12, v56\n"
"v_mul_f32 v13, v13, v57\n"
"v_mfma_f32_16x16x32_bf16 v[232:235], a[60:63], a[108:111], v[232:235]\n"
"v_mul_f32 v14, v14, v58\n"
"v_mul_f32 v15, v15, v59\n"
"v_mfma_f32_16x16x32_bf16 v[204:207], a[52:55], a[108:111], v[204:207]\n"
"ds_read_b128 a[108:111], v5 offset:19456\n"
"v_cvt_pk_bf16_f32 v24, v12, v13\n"
"v_mfma_f32_16x16x32_bf16 v[204:207], a[48:51], a[104:107], v[204:207]\n"
"ds_read_b128 a[104:107], v5 offset:18432\n"
"v_cvt_pk_bf16_f32 v25, v14, v15\n"
"v_mfma_f32_16x16x32_bf16 v[208:211], a[48:51], a[112:115], v[208:211]\n"
"ds_write_b64 v2, v[24:25] offset:12288\n"
"v_mfma_f32_16x16x32_bf16 v[208:211], a[52:55], a[116:119], v[208:211]\n"
"global_load_dwordx4 a[36:39], v0, s[34:35] offset:-3072 sc1\n"
"v_mfma_f32_16x16x32_bf16 v[236:239], a[60:63], a[116:119], v[236:239]\n"
"ds_read_b128 a[116:119], v5 offset:21504\n"
"v_mfma_f32_16x16x32_bf16 v[236:239], a[56:59], a[112:115], v[236:239]\n"
"ds_read_b128 a[112:115], v5 offset:20480\n"
"v_lshlrev_b32 v12, 16, v18\n"
"v_mfma_f32_16x16x32_bf16 v[240:243], a[56:59], a[120:123], v[240:243]\n"
"v_and_b32 v13, s63, v18\n"
"v_lshlrev_b32 v14, 16, v19\n"
"v_mfma_f32_16x16x32_bf16 v[240:243], a[60:63], a[124:127], v[240:243]\n"
"v_and_b32 v15, s63, v19\n"
"v_mul_f32 v12, v12, v84\n"
"v_mfma_f32_16x16x32_bf16 v[212:215], a[52:55], a[124:127], v[212:215]\n"
"ds_read_b128 a[124:127], v5 offset:23552\n"
"v_mul_f32 v13, v13, v85\n"
"v_mfma_f32_16x16x32_bf16 v[212:215], a[48:51], a[120:123], v[212:215]\n"
"ds_read_b128 a[120:123], v5 offset:22528\n"
"v_mul_f32 v14, v14, v86\n"
"v_mfma_f32_16x16x32_bf16 v[216:219], a[48:51], a[128:131], v[216:219]\n"
"v_mul_f32 v15, v15, v87\n"
"v_cvt_pk_bf16_f32 v26, v12, v13\n"
"v_mfma_f32_16x16x32_bf16 v[216:219], a[52:55], a[132:135], v[216:219]\n"
"global_load_dwordx4 a[40:43], v0, s[34:35] offset:-2048 sc1\n"
"v_cvt_pk_bf16_f32 v27, v14, v15\n"
"v_mfma_f32_16x16x32_bf16 v[244:247], a[60:63], a[132:135], v[244:247]\n"
"global_store_dwordx4 v0, v[24:27], s[54:55] offset:0 sc1\n"
"ds_read_b128 a[132:135], v5 offset:25600\n"
"ds_write_b64 v2, v[26:27] offset:12296\n"
"v_mfma_f32_16x16x32_bf16 v[244:247], a[56:59], a[128:131], v[244:247]\n"
"ds_read_b128 a[128:131], v5 offset:24576\n"
"v_accvgpr_read_b32 v16, a180\n"
"v_mfma_f32_16x16x32_bf16 v[248:251], a[56:59], a[136:139], v[248:251]\n"
"v_accvgpr_read_b32 v17, a181\n"
"v_accvgpr_read_b32 v18, a182\n"
"v_mfma_f32_16x16x32_bf16 v[248:251], a[60:63], a[140:143], v[248:251]\n"
"v_accvgpr_read_b32 v19, a183\n"
"v_lshlrev_b32 v12, 16, v16\n"
"v_mfma_f32_16x16x32_bf16 v[220:223], a[52:55], a[140:143], v[220:223]\n"
"ds_read_b128 a[140:143], v5 offset:27648\n"
"v_and_b32 v13, s63, v16\n"
"v_mfma_f32_16x16x32_bf16 v[220:223], a[48:51], a[136:139], v[220:223]\n"
"ds_read_b128 a[136:139], v5 offset:26624\n"
"v_lshlrev_b32 v14, 16, v17\n"
"v_mfma_f32_16x16x32_bf16 v[224:227], a[48:51], a[144:147], v[224:227]\n"
"v_and_b32 v15, s63, v17\n"
"v_mul_f32 v12, v12, v112\n"
"v_mfma_f32_16x16x32_bf16 v[224:227], a[52:55], a[148:151], v[224:227]\n"
"global_load_dwordx4 a[44:47], v0, s[34:35] offset:-1024 sc1\n"
"v_mul_f32 v13, v13, v113\n"
"v_mfma_f32_16x16x32_bf16 v[252:255], a[60:63], a[148:151], v[252:255]\n"
"ds_read_b128 a[148:151], v5 offset:29696\n"
"v_mul_f32 v14, v14, v114\n"
"v_mfma_f32_16x16x32_bf16 v[252:255], a[56:59], a[144:147], v[252:255]\n"
"ds_read_b128 a[144:147], v5 offset:28672\n"
"v_mul_f32 v15, v15, v115\n"
"v_mfma_f32_16x16x32_bf16 a[28:31], a[56:59], a[152:155], a[28:31]\n"
"v_cvt_pk_bf16_f32 v28, v12, v13\n"
"v_cvt_pk_bf16_f32 v29, v14, v15\n"
"v_mfma_f32_16x16x32_bf16 a[28:31], a[60:63], a[156:159], a[28:31]\n"
"ds_write_b64 v2, v[28:29] offset:13312\n"
"v_mfma_f32_16x16x32_bf16 a[24:27], a[52:55], a[156:159], a[24:27]\n"
"ds_read_b128 a[156:159], v5 offset:31744\n"
"v_lshlrev_b32 v12, 16, v18\n"
"v_mfma_f32_16x16x32_bf16 a[24:27], a[48:51], a[152:155], a[24:27]\n"
"s_waitcnt vmcnt(11) lgkmcnt(14)\n"
"ds_read_b128 a[152:155], v5 offset:30720\n"
"v_and_b32 v13, s63, v18\n"
"v_mfma_f32_16x16x32_bf16 v[144:147], a[64:67], a[96:99], v[144:147]\n"
"v_lshlrev_b32 v14, 16, v19\n"
"v_mfma_f32_16x16x32_bf16 v[144:147], a[68:71], a[100:103], v[144:147]\n"
"global_load_dwordx4 a[48:51], v0, s[34:35] offset:0 sc1\n"
"v_and_b32 v15, s63, v19\n"
"v_mfma_f32_16x16x32_bf16 v[172:175], a[76:79], a[100:103], v[172:175]\n"
"v_mul_f32 v12, v12, v140\n"
"v_mul_f32 v13, v13, v141\n"
"v_mfma_f32_16x16x32_bf16 v[172:175], a[72:75], a[96:99], v[172:175]\n"
"v_mul_f32 v14, v14, v142\n"
"v_mfma_f32_16x16x32_bf16 v[176:179], a[72:75], a[104:107], v[176:179]\n"
"v_mul_f32 v15, v15, v143\n"
"v_cvt_pk_bf16_f32 v30, v12, v13\n"
"v_mfma_f32_16x16x32_bf16 v[176:179], a[76:79], a[108:111], v[176:179]\n"
"v_cvt_pk_bf16_f32 v31, v14, v15\n"
"v_mfma_f32_16x16x32_bf16 v[148:151], a[68:71], a[108:111], v[148:151]\n"
"global_store_dwordx4 v0, v[28:31], s[54:55] offset:1024 sc1\n"
"ds_write_b64 v2, v[30:31] offset:13320\n"
"v_mfma_f32_16x16x32_bf16 v[148:151], a[64:67], a[104:107], v[148:151]\n"
"s_waitcnt lgkmcnt(11)\n"
"v_accvgpr_read_b32 v16, a184\n"
"v_mfma_f32_16x16x32_bf16 v[152:155], a[64:67], a[112:115], v[152:155]\n"
"v_accvgpr_read_b32 v17, a185\n"
"v_mfma_f32_16x16x32_bf16 v[152:155], a[68:71], a[116:119], v[152:155]\n"
"global_load_dwordx4 a[52:55], v0, s[34:35] offset:1024 sc1\n"
"v_accvgpr_read_b32 v18, a186\n"
"v_mfma_f32_16x16x32_bf16 v[180:183], a[76:79], a[116:119], v[180:183]\n"
"v_accvgpr_read_b32 v19, a187\n"
"v_lshlrev_b32 v12, 16, v16\n"
"v_mfma_f32_16x16x32_bf16 v[180:183], a[72:75], a[112:115], v[180:183]\n"
"v_and_b32 v13, s63, v16\n"
"v_mfma_f32_16x16x32_bf16 v[184:187], a[72:75], a[120:123], v[184:187]\n"
"v_lshlrev_b32 v14, 16, v17\n"
"v_mfma_f32_16x16x32_bf16 v[184:187], a[76:79], a[124:127], v[184:187]\n"
"v_and_b32 v15, s63, v17\n"
"v_accvgpr_read_b32 v10, a0\n"
"v_mfma_f32_16x16x32_bf16 v[156:159], a[68:71], a[124:127], v[156:159]\n"
"v_accvgpr_read_b32 v11, a1\n"
"v_mfma_f32_16x16x32_bf16 v[156:159], a[64:67], a[120:123], v[156:159]\n"
"s_waitcnt lgkmcnt(6)\n"
"v_mul_f32 v12, v12, v10\n"
"v_mul_f32 v13, v13, v11\n"
"v_mfma_f32_16x16x32_bf16 v[160:163], a[64:67], a[128:131], v[160:163]\n"
"v_accvgpr_read_b32 v10, a2\n"
"v_mfma_f32_16x16x32_bf16 v[160:163], a[68:71], a[132:135], v[160:163]\n"
"global_load_dwordx4 a[56:59], v0, s[34:35] offset:2048 sc1\n"
"v_accvgpr_read_b32 v11, a3\n"
"v_mfma_f32_16x16x32_bf16 v[188:191], a[76:79], a[132:135], v[188:191]\n"
"v_mul_f32 v14, v14, v10\n"
"v_mul_f32 v15, v15, v11\n"
"v_mfma_f32_16x16x32_bf16 v[188:191], a[72:75], a[128:131], v[188:191]\n"
"v_cvt_pk_bf16_f32 v24, v12, v13\n"
"v_mfma_f32_16x16x32_bf16 v[192:195], a[72:75], a[136:139], v[192:195]\n"
"v_cvt_pk_bf16_f32 v25, v14, v15\n"
"v_mfma_f32_16x16x32_bf16 v[192:195], a[76:79], a[140:143], v[192:195]\n"
"ds_write_b64 v2, v[24:25] offset:14336\n"
"v_mfma_f32_16x16x32_bf16 v[164:167], a[68:71], a[140:143], v[164:167]\n"
"v_lshlrev_b32 v12, 16, v18\n"
"v_and_b32 v13, s63, v18\n"
"v_mfma_f32_16x16x32_bf16 v[164:167], a[64:67], a[136:139], v[164:167]\n"
"s_waitcnt lgkmcnt(2)\n"
"v_lshlrev_b32 v14, 16, v19\n"
"v_mfma_f32_16x16x32_bf16 v[168:171], a[64:67], a[144:147], v[168:171]\n"
"v_and_b32 v15, s63, v19\n"
"v_accvgpr_read_b32 v10, a4\n"
"v_mfma_f32_16x16x32_bf16 v[168:171], a[68:71], a[148:151], v[168:171]\n"
"global_load_dwordx4 a[60:63], v0, s[34:35] offset:3072 sc1\n"
"v_accvgpr_read_b32 v11, a5\n"
"v_mfma_f32_16x16x32_bf16 v[196:199], a[76:79], a[148:151], v[196:199]\n"
"v_mul_f32 v12, v12, v10\n"
"v_mul_f32 v13, v13, v11\n"
"v_mfma_f32_16x16x32_bf16 v[196:199], a[72:75], a[144:147], v[196:199]\n"
"v_accvgpr_read_b32 v10, a6\n"
"v_mfma_f32_16x16x32_bf16 a[20:23], a[72:75], a[152:155], a[20:23]\n"
"s_add_u32 s34, s34, 8192\n"
"v_accvgpr_read_b32 v11, a7\n"
"v_mfma_f32_16x16x32_bf16 a[20:23], a[76:79], a[156:159], a[20:23]\n"
"v_mul_f32 v14, v14, v10\n"
"v_mul_f32 v15, v15, v11\n"
"v_mfma_f32_16x16x32_bf16 a[16:19], a[68:71], a[156:159], a[16:19]\n"
"v_cvt_pk_bf16_f32 v26, v12, v13\n"
"v_cvt_pk_bf16_f32 v27, v14, v15\n"
"v_mfma_f32_16x16x32_bf16 a[16:19], a[64:67], a[152:155], a[16:19]\n"
"s_waitcnt vmcnt(11)\n"
"global_store_dwordx4 v0, v[24:27], s[54:55] offset:2048 sc1\n"
"ds_write_b64 v2, v[26:27] offset:14344\n"
"v_mfma_f32_16x16x32_bf16 v[200:203], a[80:83], a[96:99], v[200:203]\n"
"v_accvgpr_read_b32 v16, a188\n"
"v_accvgpr_read_b32 v17, a189\n"
"v_mfma_f32_16x16x32_bf16 v[200:203], a[84:87], a[100:103], v[200:203]\n"
"global_load_dwordx4 a[64:67], v0, s[34:35] offset:-4096 sc1\n"
"v_accvgpr_read_b32 v18, a190\n"
"v_mfma_f32_16x16x32_bf16 v[228:231], a[92:95], a[100:103], v[228:231]\n"
"ds_read_b128 a[100:103], v2 offset:1024\n"
"v_accvgpr_read_b32 v19, a191\n"
"v_mfma_f32_16x16x32_bf16 v[228:231], a[88:91], a[96:99], v[228:231]\n"
"ds_read_b128 a[96:99], v2 offset:0\n"
"v_lshlrev_b32 v12, 16, v16\n"
"v_mfma_f32_16x16x32_bf16 v[232:235], a[88:91], a[104:107], v[232:235]\n"
"v_and_b32 v13, s63, v16\n"
"v_lshlrev_b32 v14, 16, v17\n"
"v_mfma_f32_16x16x32_bf16 v[232:235], a[92:95], a[108:111], v[232:235]\n"
"v_and_b32 v15, s63, v17\n"
"v_accvgpr_read_b32 v10, a8\n"
"v_mfma_f32_16x16x32_bf16 v[204:207], a[84:87], a[108:111], v[204:207]\n"
"ds_read_b128 a[108:111], v2 offset:3072\n"
"v_accvgpr_read_b32 v11, a9\n"
"v_mfma_f32_16x16x32_bf16 v[204:207], a[80:83], a[104:107], v[204:207]\n"
"ds_read_b128 a[104:107], v2 offset:2048\n"
"v_mul_f32 v12, v12, v10\n"
"v_mfma_f32_16x16x32_bf16 v[208:211], a[80:83], a[112:115], v[208:211]\n"
"v_mul_f32 v13, v13, v11\n"
"v_accvgpr_read_b32 v10, a10\n"
"v_mfma_f32_16x16x32_bf16 v[208:211], a[84:87], a[116:119], v[208:211]\n"
"global_load_dwordx4 a[68:71], v0, s[34:35] offset:-3072 sc1\n"
"v_accvgpr_read_b32 v11, a11\n"
"v_mfma_f32_16x16x32_bf16 v[236:239], a[92:95], a[116:119], v[236:239]\n"
"ds_read_b128 a[116:119], v2 offset:5120\n"
"v_mul_f32 v14, v14, v10\n"
"v_mfma_f32_16x16x32_bf16 v[236:239], a[88:91], a[112:115], v[236:239]\n"
"ds_read_b128 a[112:115], v2 offset:4096\n"
"v_mul_f32 v15, v15, v11\n"
"v_mfma_f32_16x16x32_bf16 v[240:243], a[88:91], a[120:123], v[240:243]\n"
"v_cvt_pk_bf16_f32 v28, v12, v13\n"
"v_cvt_pk_bf16_f32 v29, v14, v15\n"
"v_mfma_f32_16x16x32_bf16 v[240:243], a[92:95], a[124:127], v[240:243]\n"
"ds_write_b64 v2, v[28:29] offset:15360\n"
"v_mfma_f32_16x16x32_bf16 v[212:215], a[84:87], a[124:127], v[212:215]\n"
"ds_read_b128 a[124:127], v2 offset:7168\n"
"v_mfma_f32_16x16x32_bf16 v[212:215], a[80:83], a[120:123], v[212:215]\n"
"ds_read_b128 a[120:123], v2 offset:6144\n"
"v_lshlrev_b32 v12, 16, v18\n"
"v_mfma_f32_16x16x32_bf16 v[216:219], a[80:83], a[128:131], v[216:219]\n"
"v_and_b32 v13, s63, v18\n"
"v_lshlrev_b32 v14, 16, v19\n"
"v_mfma_f32_16x16x32_bf16 v[216:219], a[84:87], a[132:135], v[216:219]\n"
"global_load_dwordx4 a[72:75], v0, s[34:35] offset:-2048 sc1\n"
"v_and_b32 v15, s63, v19\n"
"v_mfma_f32_16x16x32_bf16 v[244:247], a[92:95], a[132:135], v[244:247]\n"
"ds_read_b128 a[132:135], v2 offset:9216\n"
"v_accvgpr_read_b32 v10, a12\n"
"v_mfma_f32_16x16x32_bf16 v[244:247], a[88:91], a[128:131], v[244:247]\n"
"ds_read_b128 a[128:131], v2 offset:8192\n"
"v_accvgpr_read_b32 v11, a13\n"
"v_mfma_f32_16x16x32_bf16 v[248:251], a[88:91], a[136:139], v[248:251]\n"
"v_mul_f32 v12, v12, v10\n"
"v_mul_f32 v13, v13, v11\n"
"v_mfma_f32_16x16x32_bf16 v[248:251], a[92:95], a[140:143], v[248:251]\n"
"v_accvgpr_read_b32 v10, a14\n"
"v_accvgpr_read_b32 v11, a15\n"
"v_mfma_f32_16x16x32_bf16 v[220:223], a[84:87], a[140:143], v[220:223]\n"
"ds_read_b128 a[140:143], v2 offset:11264\n"
"v_mul_f32 v14, v14, v10\n"
"v_mfma_f32_16x16x32_bf16 v[220:223], a[80:83], a[136:139], v[220:223]\n"
"ds_read_b128 a[136:139], v2 offset:10240\n"
"v_mul_f32 v15, v15, v11\n"
"v_mfma_f32_16x16x32_bf16 v[224:227], a[80:83], a[144:147], v[224:227]\n"
"v_cvt_pk_bf16_f32 v30, v12, v13\n"
"v_mfma_f32_16x16x32_bf16 v[224:227], a[84:87], a[148:151], v[224:227]\n"
"global_load_dwordx4 a[76:79], v0, s[34:35] offset:-1024 sc1\n"
"v_cvt_pk_bf16_f32 v31, v14, v15\n"
"v_mfma_f32_16x16x32_bf16 v[252:255], a[92:95], a[148:151], v[252:255]\n"
"global_store_dwordx4 v0, v[28:31], s[54:55] offset:3072 sc1\n"
"ds_read_b128 a[148:151], v2 offset:13312\n"
"ds_write_b64 v2, v[30:31] offset:15368\n"
"v_mfma_f32_16x16x32_bf16 v[252:255], a[88:91], a[144:147], v[252:255]\n"
"ds_read_b128 a[144:147], v2 offset:12288\n"
"v_mfma_f32_16x16x32_bf16 a[28:31], a[88:91], a[152:155], a[28:31]\n"
"s_add_u32 s62, s62, 1\n"
"v_mfma_f32_16x16x32_bf16 a[28:31], a[92:95], a[156:159], a[28:31]\n"
"v_mfma_f32_16x16x32_bf16 a[24:27], a[84:87], a[156:159], a[24:27]\n"
"ds_read_b128 a[156:159], v2 offset:15360\n"
"v_mfma_f32_16x16x32_bf16 a[24:27], a[80:83], a[152:155], a[24:27]\n"
"ds_read_b128 a[152:155], v2 offset:14336\n"
"s_branch L_layer\n"
"L_last:\n"
"s_waitcnt vmcnt(8) lgkmcnt(13)\n"
"v_mfma_f32_16x16x32_bf16 v[32:35], a[64:67], a[96:99], v[32:35]\n"
"v_mfma_f32_16x16x32_bf16 v[32:35], a[68:71], a[100:103], v[32:35]\n"
"global_load_dwordx4 a[48:51], v0, s[34:35] offset:0 sc1\n"
"v_mfma_f32_16x16x32_bf16 v[60:63], a[76:79], a[100:103], v[60:63]\n"
"v_mfma_f32_16x16x32_bf16 v[60:63], a[72:75], a[96:99], v[60:63]\n"
"v_mfma_f32_16x16x32_bf16 v[64:67], a[72:75], a[104:107], v[64:67]\n"
"v_mfma_f32_16x16x32_bf16 v[64:67], a[76:79], a[108:111], v[64:67]\n"
"v_mfma_f32_16x16x32_bf16 v[36:39], a[68:71], a[108:111], v[36:39]\n"
"s_mov_b32 s52, %4\n"
"s_and_b32 s53, %5, 0xffff\n"
"v_mfma_f32_16x16x32_bf16 v[36:39], a[64:67], a[104:107], v[36:39]\n"
"s_mov_b32 s54, 260352\n"
"s_waitcnt lgkmcnt(9)\n"
"v_mfma_f32_16x16x32_bf16 v[40:43], a[64:67], a[112:115], v[40:43]\n"
"s_mov_b32 s55, 0x20000\n"
"v_xor_b32 v8, 64, v7\n"
"v_mfma_f32_16x16x32_bf16 v[40:43], a[68:71], a[116:119], v[40:43]\n"
"global_load_dwordx4 a[52:55], v0, s[34:35] offset:1024 sc1\n"
"v_mfma_f32_16x16x32_bf16 v[68:71], a[76:79], a[116:119], v[68:71]\n"
"s_waitcnt lgkmcnt(6)\n"
"v_lshlrev_b32 v12, 16, v16\n"
"v_and_b32 v13, s63, v16\n"
"v_mfma_f32_16x16x32_bf16 v[68:71], a[72:75], a[112:115], v[68:71]\n"
"v_lshlrev_b32 v14, 16, v17\n"
"v_mfma_f32_16x16x32_bf16 v[72:75], a[72:75], a[120:123], v[72:75]\n"
"v_and_b32 v15, s63, v17\n"
"v_mfma_f32_16x16x32_bf16 v[72:75], a[76:79], a[124:127], v[72:75]\n"
"ds_read_b128 v[20:23], v2 offset:34816\n"
"v_mul_f32 v12, v12, v32\n"
"v_mfma_f32_16x16x32_bf16 v[44:47], a[68:71], a[124:127], v[44:47]\n"
"v_mul_f32 v13, v13, v33\n"
"v_mfma_f32_16x16x32_bf16 v[44:47], a[64:67], a[120:123], v[44:47]\n"
"s_waitcnt lgkmcnt(5)\n"
"v_mul_f32 v14, v14, v34\n"
"v_mul_f32 v15, v15, v35\n"
"v_mfma_f32_16x16x32_bf16 v[48:51], a[64:67], a[128:131], v[48:51]\n"
"v_cvt_pk_bf16_f32 v24, v12, v13\n"
"v_mfma_f32_16x16x32_bf16 v[48:51], a[68:71], a[132:135], v[48:51]\n"
"global_load_dwordx4 a[56:59], v0, s[34:35] offset:2048 sc1\n"
"v_cvt_pk_bf16_f32 v25, v14, v15\n"
"v_mfma_f32_16x16x32_bf16 v[76:79], a[76:79], a[132:135], v[76:79]\n"
"v_lshlrev_b32 v12, 16, v18\n"
"v_and_b32 v13, s63, v18\n"
"v_mfma_f32_16x16x32_bf16 v[76:79], a[72:75], a[128:131], v[76:79]\n"
"v_lshlrev_b32 v14, 16, v19\n"
"v_mfma_f32_16x16x32_bf16 v[80:83], a[72:75], a[136:139], v[80:83]\n"
"v_and_b32 v15, s63, v19\n"
"v_mfma_f32_16x16x32_bf16 v[80:83], a[76:79], a[140:143], v[80:83]\n"
"v_mul_f32 v12, v12, v60\n"
"v_mul_f32 v13, v13, v61\n"
"v_mfma_f32_16x16x32_bf16 v[52:55], a[68:71], a[140:143], v[52:55]\n"
"v_mul_f32 v14, v14, v62\n"
"v_mfma_f32_16x16x32_bf16 v[52:55], a[64:67], a[136:139], v[52:55]\n"
"s_waitcnt lgkmcnt(1)\n"
"v_mul_f32 v15, v15, v63\n"
"v_mfma_f32_16x16x32_bf16 v[56:59], a[64:67], a[144:147], v[56:59]\n"
"v_cvt_pk_bf16_f32 v26, v12, v13\n"
"v_cvt_pk_bf16_f32 v27, v14, v15\n"
"v_mfma_f32_16x16x32_bf16 v[56:59], a[68:71], a[148:151], v[56:59]\n"
"global_load_dwordx4 a[60:63], v0, s[34:35] offset:3072 sc1\n"
"v_mfma_f32_16x16x32_bf16 v[84:87], a[76:79], a[148:151], v[84:87]\n"
"ds_write_b128 v7, v[24:27] offset:0\n"
"v_mfma_f32_16x16x32_bf16 v[84:87], a[72:75], a[144:147], v[84:87]\n"
"s_waitcnt lgkmcnt(1)\n"
"v_lshlrev_b32 v12, 16, v20\n"
"v_and_b32 v13, s63, v20\n"
"v_mfma_f32_16x16x32_bf16 a[4:7], a[72:75], a[152:155], a[4:7]\n"
"s_add_u32 s34, s34, 8192\n"
"v_lshlrev_b32 v14, 16, v21\n"
"v_and_b32 v15, s63, v21\n"
"v_mfma_f32_16x16x32_bf16 a[4:7], a[76:79], a[156:159], a[4:7]\n"
"ds_read_b128 v[16:19], v2 offset:33792\n"
"v_mul_f32 v12, v12, v36\n"
"v_mfma_f32_16x16x32_bf16 a[0:3], a[68:71], a[156:159], a[0:3]\n"
"v_mul_f32 v13, v13, v37\n"
"v_mul_f32 v14, v14, v38\n"
"v_mfma_f32_16x16x32_bf16 a[0:3], a[64:67], a[152:155], a[0:3]\n"
"s_waitcnt vmcnt(8)\n"
"v_mul_f32 v15, v15, v39\n"
"v_cvt_pk_bf16_f32 v28, v12, v13\n"
"v_mfma_f32_16x16x32_bf16 v[88:91], a[80:83], a[96:99], v[88:91]\n"
"v_cvt_pk_bf16_f32 v29, v14, v15\n"
"v_lshlrev_b32 v12, 16, v22\n"
"v_mfma_f32_16x16x32_bf16 v[88:91], a[84:87], a[100:103], v[88:91]\n"
"global_load_dwordx4 a[64:67], v0, s[34:35] offset:-4096 sc1\n"
"v_and_b32 v13, s63, v22\n"
"v_mfma_f32_16x16x32_bf16 v[116:119], a[92:95], a[100:103], v[116:119]\n"
"ds_read_b128 a[100:103], v2 offset:17408\n"
"v_lshlrev_b32 v14, 16, v23\n"
"v_mfma_f32_16x16x32_bf16 v[116:119], a[88:91], a[96:99], v[116:119]\n"
"ds_read_b128 a[96:99], v2 offset:16384\n"
"v_and_b32 v15, s63, v23\n"
"v_mfma_f32_16x16x32_bf16 v[120:123], a[88:91], a[104:107], v[120:123]\n"
"v_mul_f32 v12, v12, v64\n"
"v_mul_f32 v13, v13, v65\n"
"v_mfma_f32_16x16x32_bf16 v[120:123], a[92:95], a[108:111], v[120:123]\n"
"v_mul_f32 v14, v14, v66\n"
"v_mul_f32 v15, v15, v67\n"
"v_mfma_f32_16x16x32_bf16 v[92:95], a[84:87], a[108:111], v[92:95]\n"
"ds_read_b128 a[108:111], v2 offset:19456\n"
"v_cvt_pk_bf16_f32 v30, v12, v13\n"
"v_mfma_f32_16x16x32_bf16 v[92:95], a[80:83], a[104:107], v[92:95]\n"
"ds_read_b128 a[104:107], v2 offset:18432\n"
"v_cvt_pk_bf16_f32 v31, v14, v15\n"
"v_mfma_f32_16x16x32_bf16 v[96:99], a[80:83], a[112:115], v[96:99]\n"
"ds_write_b128 v7, v[28:31] offset:2048\n"
"v_mfma_f32_16x16x32_bf16 v[96:99], a[84:87], a[116:119], v[96:99]\n"
"global_load_dwordx4 a[68:71], v0, s[34:35] offset:-3072 sc1\n"
"v_mfma_f32_16x16x32_bf16 v[124:127], a[92:95], a[116:119], v[124:127]\n"
"s_waitcnt lgkmcnt(5)\n"
"ds_read_b128 a[116:119], v2 offset:21504\n"
"v_lshlrev_b32 v12, 16, v16\n"
"v_mfma_f32_16x16x32_bf16 v[124:127], a[88:91], a[112:115], v[124:127]\n"
"ds_read_b128 a[112:115], v2 offset:20480\n"
"v_and_b32 v13, s63, v16\n"
"v_mfma_f32_16x16x32_bf16 v[128:131], a[88:91], a[120:123], v[128:131]\n"
"v_lshlrev_b32 v14, 16, v17\n"
"v_and_b32 v15, s63, v17\n"
"v_mfma_f32_16x16x32_bf16 v[128:131], a[92:95], a[124:127], v[128:131]\n"
"ds_read_b128 v[20:23], v2 offset:35840\n"
"v_mul_f32 v12, v12, v88\n"
"v_mfma_f32_16x16x32_bf16 v[100:103], a[84:87], a[124:127], v[100:103]\n"
"ds_read_b128 a[124:127], v2 offset:23552\n"
"v_mul_f32 v13, v13, v89\n"
"v_mfma_f32_16x16x32_bf16 v[100:103], a[80:83], a[120:123], v[100:103]\n"
"ds_read_b128 a[120:123], v2 offset:22528\n"
"v_mul_f32 v14, v14, v90\n"
"v_mfma_f32_16x16x32_bf16 v[104:107], a[80:83], a[128:131], v[104:107]\n"
"v_mul_f32 v15, v15, v91\n"
"v_cvt_pk_bf16_f32 v24, v12, v13\n"
"v_mfma_f32_16x16x32_bf16 v[104:107], a[84:87], a[132:135], v[104:107]\n"
"global_load_dwordx4 a[72:75], v0, s[34:35] offset:-2048 sc1\n"
"v_cvt_pk_bf16_f32 v25, v14, v15\n"
"v_mfma_f32_16x16x32_bf16 v[132:135], a[92:95], a[132:135], v[132:135]\n"
"ds_read_b128 a[132:135], v2 offset:25600\n"
"v_lshlrev_b32 v12, 16, v18\n"
"v_mfma_f32_16x16x32_bf16 v[132:135], a[88:91], a[128:131], v[132:135]\n"
"ds_read_b128 a[128:131], v2 offset:24576\n"
"v_and_b32 v13, s63, v18\n"
"v_mfma_f32_16x16x32_bf16 v[136:139], a[88:91], a[136:139], v[136:139]\n"
"v_lshlrev_b32 v14, 16, v19\n"
"v_and_b32 v15, s63, v19\n"
"v_mfma_f32_16x16x32_bf16 v[136:139], a[92:95], a[140:143], v[136:139]\n"
"v_mul_f32 v12, v12, v116\n"
"v_mfma_f32_16x16x32_bf16 v[108:111], a[84:87], a[140:143], v[108:111]\n"
"ds_read_b128 a[140:143], v2 offset:27648\n"
"v_mul_f32 v13, v13, v117\n"
"v_mfma_f32_16x16x32_bf16 v[108:111], a[80:83], a[136:139], v[108:111]\n"
"ds_read_b128 a[136:139], v2 offset:26624\n"
"v_mul_f32 v14, v14, v118\n"
"v_mfma_f32_16x16x32_bf16 v[112:115], a[80:83], a[144:147], v[112:115]\n"
"v_mul_f32 v15, v15, v119\n"
"v_cvt_pk_bf16_f32 v26, v12, v13\n"
"v_mfma_f32_16x16x32_bf16 v[112:115], a[84:87], a[148:151], v[112:115]\n"
"global_load_dwordx4 a[76:79], v0, s[34:35] offset:-1024 sc1\n"
"v_cvt_pk_bf16_f32 v27, v14, v15\n"
"v_mfma_f32_16x16x32_bf16 v[140:143], a[92:95], a[148:151], v[140:143]\n"
"ds_read_b128 a[148:151], v2 offset:29696\n"
"v_mfma_f32_16x16x32_bf16 v[140:143], a[88:91], a[144:147], v[140:143]\n"
"ds_read_b128 a[144:147], v2 offset:28672\n"
"v_mfma_f32_16x16x32_bf16 a[12:15], a[88:91], a[152:155], a[12:15]\n"
"ds_write_b128 v8, v[24:27] offset:0\n"
"v_mfma_f32_16x16x32_bf16 a[12:15], a[92:95], a[156:159], a[12:15]\n"
"ds_read_b128 v[32:35], v2 offset:0\n"
"v_mfma_f32_16x16x32_bf16 a[8:11], a[84:87], a[156:159], a[8:11]\n"
"ds_read_b128 a[156:159], v2 offset:31744\n"
"v_mfma_f32_16x16x32_bf16 a[8:11], a[80:83], a[152:155], a[8:11]\n"
"s_waitcnt vmcnt(8) lgkmcnt(15)\n"
"ds_read_b128 a[152:155], v2 offset:30720\n"
"v_mfma_f32_16x16x32_bf16 v[144:147], a[32:35], a[96:99], v[144:147]\n"
"s_waitcnt lgkmcnt(12)\n"
"ds_read_b128 v[60:63], v2 offset:1024\n"
"v_lshlrev_b32 v12, 16, v20\n"
"v_mfma_f32_16x16x32_bf16 v[144:147], a[36:39], a[100:103], v[144:147]\n"
"global_load_dwordx4 a[80:83], v0, s[34:35] offset:0 sc1\n"
"v_and_b32 v13, s63, v20\n"
"v_mfma_f32_16x16x32_bf16 v[172:175], a[44:47], a[100:103], v[172:175]\n"
"v_lshlrev_b32 v14, 16, v21\n"
"v_and_b32 v15, s63, v21\n"
"v_mfma_f32_16x16x32_bf16 v[172:175], a[40:43], a[96:99], v[172:175]\n"
"ds_read_b128 v[16:19], v2 offset:36864\n"
"v_mul_f32 v12, v12, v92\n"
"v_mfma_f32_16x16x32_bf16 v[176:179], a[40:43], a[104:107], v[176:179]\n"
"v_mul_f32 v13, v13, v93\n"
"v_mul_f32 v14, v14, v94\n"
"v_mfma_f32_16x16x32_bf16 v[176:179], a[44:47], a[108:111], v[176:179]\n"
"v_mul_f32 v15, v15, v95\n"
"v_cvt_pk_bf16_f32 v28, v12, v13\n"
"v_mfma_f32_16x16x32_bf16 v[148:151], a[36:39], a[108:111], v[148:151]\n"
"v_cvt_pk_bf16_f32 v29, v14, v15\n"
"v_lshlrev_b32 v12, 16, v22\n"
"v_mfma_f32_16x16x32_bf16 v[148:151], a[32:35], a[104:107], v[148:151]\n"
"s_waitcnt lgkmcnt(12)\n"
"v_and_b32 v13, s63, v22\n"
"v_lshlrev_b32 v14, 16, v23\n"
"v_mfma_f32_16x16x32_bf16 v[152:155], a[32:35], a[112:115], v[152:155]\n"
"v_and_b32 v15, s63, v23\n"
"v_mul_f32 v12, v12, v120\n"
"v_mfma_f32_16x16x32_bf16 v[152:155], a[36:39], a[116:119], v[152:155]\n"
"global_load_dwordx4 a[84:87], v0, s[34:35] offset:1024 sc1\n"
"v_mul_f32 v13, v13, v121\n"
"v_mfma_f32_16x16x32_bf16 v[180:183], a[44:47], a[116:119], v[180:183]\n"
"v_mul_f32 v14, v14, v122\n"
"v_mul_f32 v15, v15, v123\n"
"v_mfma_f32_16x16x32_bf16 v[180:183], a[40:43], a[112:115], v[180:183]\n"
"v_cvt_pk_bf16_f32 v30, v12, v13\n"
"v_mfma_f32_16x16x32_bf16 v[184:187], a[40:43], a[120:123], v[184:187]\n"
"v_cvt_pk_bf16_f32 v31, v14, v15\n"
"v_mfma_f32_16x16x32_bf16 v[184:187], a[44:47], a[124:127], v[184:187]\n"
"ds_write_b128 v8, v[28:31] offset:2048\n"
"v_mfma_f32_16x16x32_bf16 v[156:159], a[36:39], a[124:127], v[156:159]\n"
"s_waitcnt lgkmcnt(5)\n"
"buffer_store_dwordx4 v[32:35], v6, s[52:55], s80 offen offset:0 sc1\n"
"ds_read_b128 v[36:39], v2 offset:2048\n"
"ds_read_b128 v[64:67], v2 offset:3072\n"
"v_mfma_f32_16x16x32_bf16 v[156:159], a[32:35], a[120:123], v[156:159]\n"
"s_waitcnt lgkmcnt(3)\n"
"buffer_store_dwordx4 v[60:63], v6, s[52:55], s81 offen offset:1024 sc1\n"
"v_lshlrev_b32 v12, 16, v16\n"
"v_and_b32 v13, s63, v16\n"
"v_lshlrev_b32 v14, 16, v17\n"
"v_mfma_f32_16x16x32_bf16 v[160:163], a[32:35], a[128:131], v[160:163]\n"
"v_and_b32 v15, s63, v17\n"
"v_mfma_f32_16x16x32_bf16 v[160:163], a[36:39], a[132:135], v[160:163]\n"
"global_load_dwordx4 a[88:91], v0, s[34:35] offset:2048 sc1\n"
"v_mfma_f32_16x16x32_bf16 v[188:191], a[44:47], a[132:135], v[188:191]\n"
"ds_read_b128 v[20:23], v2 offset:37888\n"
"v_mfma_f32_16x16x32_bf16 v[188:191], a[40:43], a[128:131], v[188:191]\n"
"v_mul_f32 v12, v12, v40\n"
"v_mul_f32 v13, v13, v41\n"
"v_mfma_f32_16x16x32_bf16 v[192:195], a[40:43], a[136:139], v[192:195]\n"
"v_mul_f32 v14, v14, v42\n"
"v_mfma_f32_16x16x32_bf16 v[192:195], a[44:47], a[140:143], v[192:195]\n"
"v_mul_f32 v15, v15, v43\n"
"v_cvt_pk_bf16_f32 v24, v12, v13\n"
"v_mfma_f32_16x16x32_bf16 v[164:167], a[36:39], a[140:143], v[164:167]\n"
"v_cvt_pk_bf16_f32 v25, v14, v15\n"
"v_mfma_f32_16x16x32_bf16 v[164:167], a[32:35], a[136:139], v[164:167]\n"
"v_lshlrev_b32 v12, 16, v18\n"
"v_mfma_f32_16x16x32_bf16 v[168:171], a[32:35], a[144:147], v[168:171]\n"
"v_and_b32 v13, s63, v18\n"
"v_lshlrev_b32 v14, 16, v19\n"
"v_mfma_f32_16x16x32_bf16 v[168:171], a[36:39], a[148:151], v[168:171]\n"
"global_load_dwordx4 a[92:95], v0, s[34:35] offset:3072 sc1\n"
"v_and_b32 v15, s63, v19\n"
"v_mfma_f32_16x16x32_bf16 v[196:199], a[44:47], a[148:151], v[196:199]\n"
"v_mul_f32 v12, v12, v68\n"
"v_mfma_f32_16x16x32_bf16 v[196:199], a[40:43], a[144:147], v[196:199]\n"
"v_mul_f32 v13, v13, v69\n"
"v_mul_f32 v14, v14, v70\n"
"v_mfma_f32_16x16x32_bf16 a[20:23], a[40:43], a[152:155], a[20:23]\n"
"s_add_u32 s34, s34, 8192\n"
"v_mul_f32 v15, v15, v71\n"
"v_mfma_f32_16x16x32_bf16 a[20:23], a[44:47], a[156:159], a[20:23]\n"
"v_cvt_pk_bf16_f32 v26, v12, v13\n"
"v_cvt_pk_bf16_f32 v27, v14, v15\n"
"v_mfma_f32_16x16x32_bf16 a[16:19], a[36:39], a[156:159], a[16:19]\n"
"ds_write_b128 v7, v[24:27] offset:4096\n"
"v_mfma_f32_16x16x32_bf16 a[16:19], a[32:35], a[152:155], a[16:19]\n"
"s_waitcnt lgkmcnt(3)\n"
"buffer_store_dwordx4 v[36:39], v6, s[52:55], s82 offen offset:2048 sc1\n"
"s_waitcnt vmcnt(11)\n"
"v_mfma_f32_16x16x32_bf16 v[200:203], a[48:51], a[96:99], v[200:203]\n"
"s_waitcnt lgkmcnt(1)\n"
"buffer_store_dwordx4 v[64:67], v6, s[52:55], s83 offen offset:3072 sc1\n"
"v_lshlrev_b32 v12, 16, v20\n"
"v_and_b32 v13, s63, v20\n"
"v_lshlrev_b32 v14, 16, v21\n"
"v_mfma_f32_16x16x32_bf16 v[200:203], a[52:55], a[100:103], v[200:203]\n"
"global_load_dwordx4 a[32:35], v0, s[34:35] offset:-4096 sc1\n"
"v_mfma_f32_16x16x32_bf16 v[228:231], a[60:63], a[100:103], v[228:231]\n"
"ds_read_b128 a[100:103], v3 offset:17408\n"
"v_mfma_f32_16x16x32_bf16 v[228:231], a[56:59], a[96:99], v[228:231]\n"
"ds_read_b128 a[96:99], v3 offset:16384\n"
"v_and_b32 v15, s63, v21\n"
"v_mfma_f32_16x16x32_bf16 v[232:235], a[56:59], a[104:107], v[232:235]\n"
"ds_read_b128 v[16:19], v2 offset:38912\n"
"v_mul_f32 v12, v12, v96\n"
"v_mfma_f32_16x16x32_bf16 v[232:235], a[60:63], a[108:111], v[232:235]\n"
"v_mul_f32 v13, v13, v97\n"
"v_mul_f32 v14, v14, v98\n"
"v_mfma_f32_16x16x32_bf16 v[204:207], a[52:55], a[108:111], v[204:207]\n"
"ds_read_b128 a[108:111], v3 offset:19456\n"
"v_mul_f32 v15, v15, v99\n"
"v_mfma_f32_16x16x32_bf16 v[204:207], a[48:51], a[104:107], v[204:207]\n"
"ds_read_b128 a[104:107], v3 offset:18432\n"
"v_cvt_pk_bf16_f32 v28, v12, v13\n"
"v_mfma_f32_16x16x32_bf16 v[208:211], a[48:51], a[112:115], v[208:211]\n"
"v_cvt_pk_bf16_f32 v29, v14, v15\n"
"v_lshlrev_b32 v12, 16, v22\n"
"v_mfma_f32_16x16x32_bf16 v[208:211], a[52:55], a[116:119], v[208:211]\n"
"global_load_dwordx4 a[36:39], v0, s[34:35] offset:-3072 sc1\n"
"v_and_b32 v13, s63, v22\n"
"v_mfma_f32_16x16x32_bf16 v[236:239], a[60:63], a[116:119], v[236:239]\n"
"ds_read_b128 a[116:119], v3 offset:21504\n"
"v_lshlrev_b32 v14, 16, v23\n"
"v_mfma_f32_16x16x32_bf16 v[236:239], a[56:59], a[112:115], v[236:239]\n"
"ds_read_b128 a[112:115], v3 offset:20480\n"
"v_and_b32 v15, s63, v23\n"
"v_mfma_f32_16x16x32_bf16 v[240:243], a[56:59], a[120:123], v[240:243]\n"
"v_mul_f32 v12, v12, v124\n"
"v_mul_f32 v13, v13, v125\n"
"v_mfma_f32_16x16x32_bf16 v[240:243], a[60:63], a[124:127], v[240:243]\n"
"v_mul_f32 v14, v14, v126\n"
"v_mul_f32 v15, v15, v127\n"
"v_mfma_f32_16x16x32_bf16 v[212:215], a[52:55], a[124:127], v[212:215]\n"
"ds_read_b128 a[124:127], v3 offset:23552\n"
"v_cvt_pk_bf16_f32 v30, v12, v13\n"
"v_mfma_f32_16x16x32_bf16 v[212:215], a[48:51], a[120:123], v[212:215]\n"
"ds_read_b128 a[120:123], v3 offset:22528\n"
"v_cvt_pk_bf16_f32 v31, v14, v15\n"
"v_mfma_f32_16x16x32_bf16 v[216:219], a[48:51], a[128:131], v[216:219]\n"
"ds_write_b128 v8, v[28:31] offset:4096\n"
"v_mfma_f32_16x16x32_bf16 v[216:219], a[52:55], a[132:135], v[216:219]\n"
"global_load_dwordx4 a[40:43], v0, s[34:35] offset:-2048 sc1\n"
"ds_read_b128 v[40:43], v2 offset:4096\n"
"v_mfma_f32_16x16x32_bf16 v[244:247], a[60:63], a[132:135], v[244:247]\n"
"ds_read_b128 a[132:135], v3 offset:25600\n"
"v_mfma_f32_16x16x32_bf16 v[244:247], a[56:59], a[128:131], v[244:247]\n"
"ds_read_b128 a[128:131], v3 offset:24576\n"
"v_mfma_f32_16x16x32_bf16 v[248:251], a[56:59], a[136:139], v[248:251]\n"
"s_waitcnt lgkmcnt(10)\n"
"ds_read_b128 v[68:71], v2 offset:5120\n"
"v_lshlrev_b32 v12, 16, v16\n"
"v_mfma_f32_16x16x32_bf16 v[248:251], a[60:63], a[140:143], v[248:251]\n"
"v_and_b32 v13, s63, v16\n"
"v_lshlrev_b32 v14, 16, v17\n"
"v_mfma_f32_16x16x32_bf16 v[220:223], a[52:55], a[140:143], v[220:223]\n"
"ds_read_b128 a[140:143], v3 offset:27648\n"
"v_and_b32 v15, s63, v17\n"
"v_mfma_f32_16x16x32_bf16 v[220:223], a[48:51], a[136:139], v[220:223]\n"
"ds_read_b128 a[136:139], v3 offset:26624\n"
"v_mfma_f32_16x16x32_bf16 v[224:227], a[48:51], a[144:147], v[224:227]\n"
"ds_read_b128 v[20:23], v2 offset:39936\n"
"v_mul_f32 v12, v12, v44\n"
"v_mfma_f32_16x16x32_bf16 v[224:227], a[52:55], a[148:151], v[224:227]\n"
"global_load_dwordx4 a[44:47], v0, s[34:35] offset:-1024 sc1\n"
"v_mul_f32 v13, v13, v45\n"
"v_mfma_f32_16x16x32_bf16 v[252:255], a[60:63], a[148:151], v[252:255]\n"
"ds_read_b128 a[148:151], v3 offset:29696\n"
"v_mul_f32 v14, v14, v46\n"
"v_mfma_f32_16x16x32_bf16 v[252:255], a[56:59], a[144:147], v[252:255]\n"
"ds_read_b128 a[144:147], v3 offset:28672\n"
"v_mul_f32 v15, v15, v47\n"
"v_mfma_f32_16x16x32_bf16 a[28:31], a[56:59], a[152:155], a[28:31]\n"
"v_cvt_pk_bf16_f32 v24, v12, v13\n"
"v_cvt_pk_bf16_f32 v25, v14, v15\n"
"v_mfma_f32_16x16x32_bf16 a[28:31], a[60:63], a[156:159], a[28:31]\n"
"v_lshlrev_b32 v12, 16, v18\n"
"v_and_b32 v13, s63, v18\n"
"v_mfma_f32_16x16x32_bf16 a[24:27], a[52:55], a[156:159], a[24:27]\n"
"ds_read_b128 a[156:159], v3 offset:31744\n"
"v_lshlrev_b32 v14, 16, v19\n"
"v_mfma_f32_16x16x32_bf16 a[24:27], a[48:51], a[152:155], a[24:27]\n"
"s_waitcnt vmcnt(12) lgkmcnt(15)\n"
"ds_read_b128 a[152:155], v3 offset:30720\n"
"v_and_b32 v15, s63, v19\n"
"v_mfma_f32_16x16x32_bf16 v[144:147], a[64:67], a[96:99], v[144:147]\n"
"v_mul_f32 v12, v12, v72\n"
"v_mul_f32 v13, v13, v73\n"
"v_mfma_f32_16x16x32_bf16 v[144:147], a[68:71], a[100:103], v[144:147]\n"
"global_load_dwordx4 a[48:51], v0, s[34:35] offset:0 sc1\n"
"v_mul_f32 v14, v14, v74\n"
"v_mfma_f32_16x16x32_bf16 v[172:175], a[76:79], a[100:103], v[172:175]\n"
"v_mul_f32 v15, v15, v75\n"
"v_cvt_pk_bf16_f32 v26, v12, v13\n"
"v_mfma_f32_16x16x32_bf16 v[172:175], a[72:75], a[96:99], v[172:175]\n"
"v_cvt_pk_bf16_f32 v27, v14, v15\n"
"v_mfma_f32_16x16x32_bf16 v[176:179], a[72:75], a[104:107], v[176:179]\n"
"ds_write_b128 v7, v[24:27] offset:6144\n"
"v_mfma_f32_16x16x32_bf16 v[176:179], a[76:79], a[108:111], v[176:179]\n"
"s_waitcnt lgkmcnt(11)\n"
"buffer_store_dwordx4 v[40:43], v6, s[52:55], s84 offen offset:0 sc1\n"
"v_mfma_f32_16x16x32_bf16 v[148:151], a[68:71], a[108:111], v[148:151]\n"
"s_waitcnt lgkmcnt(5)\n"
"buffer_store_dwordx4 v[68:71], v6, s[52:55], s85 offen offset:1024 sc1\n"
"v_lshlrev_b32 v12, 16, v20\n"
"v_and_b32 v13, s63, v20\n"
"v_lshlrev_b32 v14, 16, v21\n"
"v_mfma_f32_16x16x32_bf16 v[148:151], a[64:67], a[104:107], v[148:151]\n"
"v_and_b32 v15, s63, v21\n"
"v_mul_f32 v12, v12, v100\n"
"v_mfma_f32_16x16x32_bf16 v[152:155], a[64:67], a[112:115], v[152:155]\n"
"v_mul_f32 v13, v13, v101\n"
"v_mul_f32 v14, v14, v102\n"
"v_mfma_f32_16x16x32_bf16 v[152:155], a[68:71], a[116:119], v[152:155]\n"
"global_load_dwordx4 a[52:55], v0, s[34:35] offset:1024 sc1\n"
"v_mul_f32 v15, v15, v103\n"
"v_mfma_f32_16x16x32_bf16 v[180:183], a[76:79], a[116:119], v[180:183]\n"
"v_cvt_pk_bf16_f32 v28, v12, v13\n"
"v_mfma_f32_16x16x32_bf16 v[180:183], a[72:75], a[112:115], v[180:183]\n"
"v_cvt_pk_bf16_f32 v29, v14, v15\n"
"v_lshlrev_b32 v12, 16, v22\n"
"v_mfma_f32_16x16x32_bf16 v[184:187], a[72:75], a[120:123], v[184:187]\n"
"v_and_b32 v13, s63, v22\n"
"v_mfma_f32_16x16x32_bf16 v[184:187], a[76:79], a[124:127], v[184:187]\n"
"v_lshlrev_b32 v14, 16, v23\n"
"v_and_b32 v15, s63, v23\n"
"v_mfma_f32_16x16x32_bf16 v[156:159], a[68:71], a[124:127], v[156:159]\n"
"v_mul_f32 v12, v12, v128\n"
"v_mfma_f32_16x16x32_bf16 v[156:159], a[64:67], a[120:123], v[156:159]\n"
"v_mul_f32 v13, v13, v129\n"
"v_mfma_f32_16x16x32_bf16 v[160:163], a[64:67], a[128:131], v[160:163]\n"
"v_mul_f32 v14, v14, v130\n"
"v_mul_f32 v15, v15, v131\n"
"v_mfma_f32_16x16x32_bf16 v[160:163], a[68:71], a[132:135], v[160:163]\n"
"global_load_dwordx4 a[56:59], v0, s[34:35] offset:2048 sc1\n"
"v_cvt_pk_bf16_f32 v30, v12, v13\n"
"v_mfma_f32_16x16x32_bf16 v[188:191], a[76:79], a[132:135], v[188:191]\n"
"v_cvt_pk_bf16_f32 v31, v14, v15\n"
"v_mfma_f32_16x16x32_bf16 v[188:191], a[72:75], a[128:131], v[188:191]\n"
"ds_write_b128 v8, v[28:31] offset:6144\n"
"v_mfma_f32_16x16x32_bf16 v[192:195], a[72:75], a[136:139], v[192:195]\n"
"ds_read_b128 v[44:47], v2 offset:6144\n"
"v_mfma_f32_16x16x32_bf16 v[192:195], a[76:79], a[140:143], v[192:195]\n"
"ds_read_b128 v[72:75], v2 offset:7168\n"
"v_accvgpr_read_b32 v16, a160\n"
"v_mfma_f32_16x16x32_bf16 v[164:167], a[68:71], a[140:143], v[164:167]\n"
"v_accvgpr_read_b32 v17, a161\n"
"v_accvgpr_read_b32 v18, a162\n"
"v_mfma_f32_16x16x32_bf16 v[164:167], a[64:67], a[136:139], v[164:167]\n"
"s_waitcnt lgkmcnt(4)\n"
"v_accvgpr_read_b32 v19, a163\n"
"v_mfma_f32_16x16x32_bf16 v[168:171], a[64:67], a[144:147], v[168:171]\n"
"v_lshlrev_b32 v12, 16, v16\n"
"v_and_b32 v13, s63, v16\n"
"v_mfma_f32_16x16x32_bf16 v[168:171], a[68:71], a[148:151], v[168:171]\n"
"global_load_dwordx4 a[60:63], v0, s[34:35] offset:3072 sc1\n"
"v_lshlrev_b32 v14, 16, v17\n"
"v_mfma_f32_16x16x32_bf16 v[196:199], a[76:79], a[148:151], v[196:199]\n"
"v_and_b32 v15, s63, v17\n"
"v_mfma_f32_16x16x32_bf16 v[196:199], a[72:75], a[144:147], v[196:199]\n"
"v_mul_f32 v12, v12, v48\n"
"v_mul_f32 v13, v13, v49\n"
"v_mfma_f32_16x16x32_bf16 a[20:23], a[72:75], a[152:155], a[20:23]\n"
"s_add_u32 s34, s34, 8192\n"
"v_mul_f32 v14, v14, v50\n"
"v_mfma_f32_16x16x32_bf16 a[20:23], a[76:79], a[156:159], a[20:23]\n"
"v_mul_f32 v15, v15, v51\n"
"v_mfma_f32_16x16x32_bf16 a[16:19], a[68:71], a[156:159], a[16:19]\n"
"v_cvt_pk_bf16_f32 v24, v12, v13\n"
"v_cvt_pk_bf16_f32 v25, v14, v15\n"
"v_mfma_f32_16x16x32_bf16 a[16:19], a[64:67], a[152:155], a[16:19]\n"
"s_waitcnt vmcnt(12)\n"
"v_lshlrev_b32 v12, 16, v18\n"
"v_mfma_f32_16x16x32_bf16 v[200:203], a[80:83], a[96:99], v[200:203]\n"
"v_and_b32 v13, s63, v18\n"
"v_lshlrev_b32 v14, 16, v19\n"
"v_mfma_f32_16x16x32_bf16 v[200:203], a[84:87], a[100:103], v[200:203]\n"
"global_load_dwordx4 a[64:67], v0, s[34:35] offset:-4096 sc1\n"
"v_and_b32 v15, s63, v19\n"
"v_mfma_f32_16x16x32_bf16 v[228:231], a[92:95], a[100:103], v[228:231]\n"
"ds_read_b128 a[100:103], v4 offset:17408\n"
"v_mul_f32 v12, v12, v76\n"
"v_mfma_f32_16x16x32_bf16 v[228:231], a[88:91], a[96:99], v[228:231]\n"
"ds_read_b128 a[96:99], v4 offset:16384\n"
"v_mul_f32 v13, v13, v77\n"
"v_mfma_f32_16x16x32_bf16 v[232:235], a[88:91], a[104:107], v[232:235]\n"
"v_mul_f32 v14, v14, v78\n"
"v_mul_f32 v15, v15, v79\n"
"v_mfma_f32_16x16x32_bf16 v[232:235], a[92:95], a[108:111], v[232:235]\n"
"v_cvt_pk_bf16_f32 v26, v12, v13\n"
"v_mfma_f32_16x16x32_bf16 v[204:207], a[84:87], a[108:111], v[204:207]\n"
"ds_read_b128 a[108:111], v4 offset:19456\n"
"v_cvt_pk_bf16_f32 v27, v14, v15\n"
"v_mfma_f32_16x16x32_bf16 v[204:207], a[80:83], a[104:107], v[204:207]\n"
"ds_read_b128 a[104:107], v4 offset:18432\n"
"v_mfma_f32_16x16x32_bf16 v[208:211], a[80:83], a[112:115], v[208:211]\n"
"ds_write_b128 v7, v[24:27] offset:8192\n"
"v_mfma_f32_16x16x32_bf16 v[208:211], a[84:87], a[116:119], v[208:211]\n"
"global_load_dwordx4 a[68:71], v0, s[34:35] offset:-3072 sc1\n"
"v_mfma_f32_16x16x32_bf16 v[236:239], a[92:95], a[116:119], v[236:239]\n"
"s_waitcnt lgkmcnt(6)\n"
"buffer_store_dwordx4 v[44:47], v6, s[52:55], s86 offen offset:2048 sc1\n"
"ds_read_b128 a[116:119], v4 offset:21504\n"
"v_mfma_f32_16x16x32_bf16 v[236:239], a[88:91], a[112:115], v[236:239]\n"
"s_waitcnt lgkmcnt(6)\n"
"buffer_store_dwordx4 v[72:75], v6, s[52:55], s87 offen offset:3072 sc1\n"
"ds_read_b128 a[112:115], v4 offset:20480\n"
"v_accvgpr_read_b32 v16, a164\n"
"v_accvgpr_read_b32 v17, a165\n"
"v_mfma_f32_16x16x32_bf16 v[240:243], a[88:91], a[120:123], v[240:243]\n"
"v_accvgpr_read_b32 v18, a166\n"
"v_accvgpr_read_b32 v19, a167\n"
"v_mfma_f32_16x16x32_bf16 v[240:243], a[92:95], a[124:127], v[240:243]\n"
"v_lshlrev_b32 v12, 16, v16\n"
"v_and_b32 v13, s63, v16\n"
"v_mfma_f32_16x16x32_bf16 v[212:215], a[84:87], a[124:127], v[212:215]\n"
"ds_read_b128 a[124:127], v4 offset:23552\n"
"v_lshlrev_b32 v14, 16, v17\n"
"v_mfma_f32_16x16x32_bf16 v[212:215], a[80:83], a[120:123], v[212:215]\n"
"ds_read_b128 a[120:123], v4 offset:22528\n"
"v_and_b32 v15, s63, v17\n"
"v_mfma_f32_16x16x32_bf16 v[216:219], a[80:83], a[128:131], v[216:219]\n"
"v_mul_f32 v12, v12, v104\n"
"v_mul_f32 v13, v13, v105\n"
"v_mfma_f32_16x16x32_bf16 v[216:219], a[84:87], a[132:135], v[216:219]\n"
"global_load_dwordx4 a[72:75], v0, s[34:35] offset:-2048 sc1\n"
"v_mul_f32 v14, v14, v106\n"
"v_mfma_f32_16x16x32_bf16 v[244:247], a[92:95], a[132:135], v[244:247]\n"
"ds_read_b128 a[132:135], v4 offset:25600\n"
"v_mul_f32 v15, v15, v107\n"
"v_mfma_f32_16x16x32_bf16 v[244:247], a[88:91], a[128:131], v[244:247]\n"
"ds_read_b128 a[128:131], v4 offset:24576\n"
"v_cvt_pk_bf16_f32 v28, v12, v13\n"
"v_mfma_f32_16x16x32_bf16 v[248:251], a[88:91], a[136:139], v[248:251]\n"
"v_cvt_pk_bf16_f32 v29, v14, v15\n"
"v_lshlrev_b32 v12, 16, v18\n"
"v_mfma_f32_16x16x32_bf16 v[248:251], a[92:95], a[140:143], v[248:251]\n"
"v_and_b32 v13, s63, v18\n"
"v_lshlrev_b32 v14, 16, v19\n"
"v_mfma_f32_16x16x32_bf16 v[220:223], a[84:87], a[140:143], v[220:223]\n"
"ds_read_b128 a[140:143], v4 offset:27648\n"
"v_and_b32 v15, s63, v19\n"
"v_mfma_f32_16x16x32_bf16 v[220:223], a[80:83], a[136:139], v[220:223]\n"
"ds_read_b128 a[136:139], v4 offset:26624\n"
"v_mul_f32 v12, v12, v132\n"
"v_mfma_f32_16x16x32_bf16 v[224:227], a[80:83], a[144:147], v[224:227]\n"
"v_mul_f32 v13, v13, v133\n"
"v_mul_f32 v14, v14, v134\n"
"v_mfma_f32_16x16x32_bf16 v[224:227], a[84:87], a[148:151], v[224:227]\n"
"global_load_dwordx4 a[76:79], v0, s[34:35] offset:-1024 sc1\n"
"v_mul_f32 v15, v15, v135\n"
"v_mfma_f32_16x16x32_bf16 v[252:255], a[92:95], a[148:151], v[252:255]\n"
"ds_read_b128 a[148:151], v4 offset:29696\n"
"v_cvt_pk_bf16_f32 v30, v12, v13\n"
"v_mfma_f32_16x16x32_bf16 v[252:255], a[88:91], a[144:147], v[252:255]\n"
"ds_read_b128 a[144:147], v4 offset:28672\n"
"v_cvt_pk_bf16_f32 v31, v14, v15\n"
"v_mfma_f32_16x16x32_bf16 a[28:31], a[88:91], a[152:155], a[28:31]\n"
"ds_write_b128 v8, v[28:31] offset:8192\n"
"v_mfma_f32_16x16x32_bf16 a[28:31], a[92:95], a[156:159], a[28:31]\n"
"ds_read_b128 v[48:51], v2 offset:8192\n"
"v_mfma_f32_16x16x32_bf16 a[24:27], a[84:87], a[156:159], a[24:27]\n"
"ds_read_b128 a[156:159], v4 offset:31744\n"
"v_mfma_f32_16x16x32_bf16 a[24:27], a[80:83], a[152:155], a[24:27]\n"
"s_waitcnt vmcnt(12) lgkmcnt(14)\n"
"ds_read_b128 a[152:155], v4 offset:30720\n"
"v_mfma_f32_16x16x32_bf16 v[144:147], a[32:35], a[96:99], v[144:147]\n"
"ds_read_b128 v[76:79], v2 offset:9216\n"
"v_accvgpr_read_b32 v16, a168\n"
"v_mfma_f32_16x16x32_bf16 v[144:147], a[36:39], a[100:103], v[144:147]\n"
"global_load_dwordx4 a[80:83], v0, s[34:35] offset:0 sc1\n"
"v_accvgpr_read_b32 v17, a169\n"
"v_mfma_f32_16x16x32_bf16 v[172:175], a[44:47], a[100:103], v[172:175]\n"
"v_accvgpr_read_b32 v18, a170\n"
"v_accvgpr_read_b32 v19, a171\n"
"v_mfma_f32_16x16x32_bf16 v[172:175], a[40:43], a[96:99], v[172:175]\n"
"v_lshlrev_b32 v12, 16, v16\n"
"v_and_b32 v13, s63, v16\n"
"v_mfma_f32_16x16x32_bf16 v[176:179], a[40:43], a[104:107], v[176:179]\n"
"v_lshlrev_b32 v14, 16, v17\n"
"v_and_b32 v15, s63, v17\n"
"v_mfma_f32_16x16x32_bf16 v[176:179], a[44:47], a[108:111], v[176:179]\n"
"v_mul_f32 v12, v12, v52\n"
"v_mul_f32 v13, v13, v53\n"
"v_mfma_f32_16x16x32_bf16 v[148:151], a[36:39], a[108:111], v[148:151]\n"
"v_mul_f32 v14, v14, v54\n"
"v_mul_f32 v15, v15, v55\n"
"v_mfma_f32_16x16x32_bf16 v[148:151], a[32:35], a[104:107], v[148:151]\n"
"s_waitcnt lgkmcnt(11)\n"
"v_cvt_pk_bf16_f32 v24, v12, v13\n"
"v_cvt_pk_bf16_f32 v25, v14, v15\n"
"v_mfma_f32_16x16x32_bf16 v[152:155], a[32:35], a[112:115], v[152:155]\n"
"v_lshlrev_b32 v12, 16, v18\n"
"v_and_b32 v13, s63, v18\n"
"v_mfma_f32_16x16x32_bf16 v[152:155], a[36:39], a[116:119], v[152:155]\n"
"global_load_dwordx4 a[84:87], v0, s[34:35] offset:1024 sc1\n"
"v_lshlrev_b32 v14, 16, v19\n"
"v_mfma_f32_16x16x32_bf16 v[180:183], a[44:47], a[116:119], v[180:183]\n"
"v_and_b32 v15, s63, v19\n"
"v_mul_f32 v12, v12, v80\n"
"v_mfma_f32_16x16x32_bf16 v[180:183], a[40:43], a[112:115], v[180:183]\n"
"v_mul_f32 v13, v13, v81\n"
"v_mul_f32 v14, v14, v82\n"
"v_mfma_f32_16x16x32_bf16 v[184:187], a[40:43], a[120:123], v[184:187]\n"
"v_mul_f32 v15, v15, v83\n"
"v_mfma_f32_16x16x32_bf16 v[184:187], a[44:47], a[124:127], v[184:187]\n"
"v_cvt_pk_bf16_f32 v26, v12, v13\n"
"v_cvt_pk_bf16_f32 v27, v14, v15\n"
"v_mfma_f32_16x16x32_bf16 v[156:159], a[36:39], a[124:127], v[156:159]\n"
"ds_write_b128 v7, v[24:27] offset:10240\n"
"v_mfma_f32_16x16x32_bf16 v[156:159], a[32:35], a[120:123], v[156:159]\n"
"s_waitcnt lgkmcnt(4)\n"
"buffer_store_dwordx4 v[48:51], v6, s[52:55], s88 offen offset:0 sc1\n"
"v_mfma_f32_16x16x32_bf16 v[160:163], a[32:35], a[128:131], v[160:163]\n"
"s_waitcnt lgkmcnt(1)\n"
"buffer_store_dwordx4 v[76:79], v6, s[52:55], s89 offen offset:1024 sc1\n"
"v_accvgpr_read_b32 v16, a172\n"
"v_accvgpr_read_b32 v17, a173\n"
"v_accvgpr_read_b32 v18, a174\n"
"v_mfma_f32_16x16x32_bf16 v[160:163], a[36:39], a[132:135], v[160:163]\n"
"global_load_dwordx4 a[88:91], v0, s[34:35] offset:2048 sc1\n"
"v_mfma_f32_16x16x32_bf16 v[188:191], a[44:47], a[132:135], v[188:191]\n"
"v_accvgpr_read_b32 v19, a175\n"
"v_mfma_f32_16x16x32_bf16 v[188:191], a[40:43], a[128:131], v[188:191]\n"
"v_lshlrev_b32 v12, 16, v16\n"
"v_mfma_f32_16x16x32_bf16 v[192:195], a[40:43], a[136:139], v[192:195]\n"
"v_and_b32 v13, s63, v16\n"
"v_mfma_f32_16x16x32_bf16 v[192:195], a[44:47], a[140:143], v[192:195]\n"
"v_lshlrev_b32 v14, 16, v17\n"
"v_mfma_f32_16x16x32_bf16 v[164:167], a[36:39], a[140:143], v[164:167]\n"
"v_and_b32 v15, s63, v17\n"
"v_mul_f32 v12, v12, v108\n"
"v_mfma_f32_16x16x32_bf16 v[164:167], a[32:35], a[136:139], v[164:167]\n"
"v_mul_f32 v13, v13, v109\n"
"v_mfma_f32_16x16x32_bf16 v[168:171], a[32:35], a[144:147], v[168:171]\n"
"v_mul_f32 v14, v14, v110\n"
"v_mfma_f32_16x16x32_bf16 v[168:171], a[36:39], a[148:151], v[168:171]\n"
"global_load_dwordx4 a[92:95], v0, s[34:35] offset:3072 sc1\n"
"v_mul_f32 v15, v15, v111\n"
"v_mfma_f32_16x16x32_bf16 v[196:199], a[44:47], a[148:151], v[196:199]\n"
"v_cvt_pk_bf16_f32 v28, v12, v13\n"
"v_cvt_pk_bf16_f32 v29, v14, v15\n"
"v_mfma_f32_16x16x32_bf16 v[196:199], a[40:43], a[144:147], v[196:199]\n"
"v_lshlrev_b32 v12, 16, v18\n"
"v_and_b32 v13, s63, v18\n"
"v_mfma_f32_16x16x32_bf16 a[20:23], a[40:43], a[152:155], a[20:23]\n"
"s_add_u32 s34, s34, 8192\n"
"v_lshlrev_b32 v14, 16, v19\n"
"v_and_b32 v15, s63, v19\n"
"v_mfma_f32_16x16x32_bf16 a[20:23], a[44:47], a[156:159], a[20:23]\n"
"v_mul_f32 v12, v12, v136\n"
"v_mul_f32 v13, v13, v137\n"
"v_mfma_f32_16x16x32_bf16 a[16:19], a[36:39], a[156:159], a[16:19]\n"
"v_mul_f32 v14, v14, v138\n"
"v_mul_f32 v15, v15, v139\n"
"v_mfma_f32_16x16x32_bf16 a[16:19], a[32:35], a[152:155], a[16:19]\n"
"s_waitcnt vmcnt(12)\n"
"v_cvt_pk_bf16_f32 v30, v12, v13\n"
"v_cvt_pk_bf16_f32 v31, v14, v15\n"
"v_mfma_f32_16x16x32_bf16 v[200:203], a[48:51], a[96:99], v[200:203]\n"
"ds_write_b128 v8, v[28:31] offset:10240\n"
"v_mfma_f32_16x16x32_bf16 v[200:203], a[52:55], a[100:103], v[200:203]\n"
"ds_read_b128 v[52:55], v2 offset:10240\n"
"v_mfma_f32_16x16x32_bf16 v[228:231], a[60:63], a[100:103], v[228:231]\n"
"ds_read_b128 a[100:103], v5 offset:17408\n"
"v_mfma_f32_16x16x32_bf16 v[228:231], a[56:59], a[96:99], v[228:231]\n"
"ds_read_b128 a[96:99], v5 offset:16384\n"
"v_mfma_f32_16x16x32_bf16 v[232:235], a[56:59], a[104:107], v[232:235]\n"
"ds_read_b128 v[80:83], v2 offset:11264\n"
"v_accvgpr_read_b32 v16, a176\n"
"v_mfma_f32_16x16x32_bf16 v[232:235], a[60:63], a[108:111], v[232:235]\n"
"v_accvgpr_read_b32 v17, a177\n"
"v_mfma_f32_16x16x32_bf16 v[204:207], a[52:55], a[108:111], v[204:207]\n"
"ds_read_b128 a[108:111], v5 offset:19456\n"
"v_accvgpr_read_b32 v18, a178\n"
"v_mfma_f32_16x16x32_bf16 v[204:207], a[48:51], a[104:107], v[204:207]\n"
"ds_read_b128 a[104:107], v5 offset:18432\n"
"v_accvgpr_read_b32 v19, a179\n"
"v_mfma_f32_16x16x32_bf16 v[208:211], a[48:51], a[112:115], v[208:211]\n"
"v_lshlrev_b32 v12, 16, v16\n"
"v_and_b32 v13, s63, v16\n"
"v_mfma_f32_16x16x32_bf16 v[208:211], a[52:55], a[116:119], v[208:211]\n"
"v_lshlrev_b32 v14, 16, v17\n"
"v_and_b32 v15, s63, v17\n"
"v_mfma_f32_16x16x32_bf16 v[236:239], a[60:63], a[116:119], v[236:239]\n"
"ds_read_b128 a[116:119], v5 offset:21504\n"
"v_mul_f32 v12, v12, v56\n"
"v_mfma_f32_16x16x32_bf16 v[236:239], a[56:59], a[112:115], v[236:239]\n"
"ds_read_b128 a[112:115], v5 offset:20480\n"
"v_mul_f32 v13, v13, v57\n"
"v_mfma_f32_16x16x32_bf16 v[240:243], a[56:59], a[120:123], v[240:243]\n"
"v_mul_f32 v14, v14, v58\n"
"v_mfma_f32_16x16x32_bf16 v[240:243], a[60:63], a[124:127], v[240:243]\n"
"v_mul_f32 v15, v15, v59\n"
"v_cvt_pk_bf16_f32 v24, v12, v13\n"
"v_mfma_f32_16x16x32_bf16 v[212:215], a[52:55], a[124:127], v[212:215]\n"
"ds_read_b128 a[124:127], v5 offset:23552\n"
"v_cvt_pk_bf16_f32 v25, v14, v15\n"
"v_mfma_f32_16x16x32_bf16 v[212:215], a[48:51], a[120:123], v[212:215]\n"
"ds_read_b128 a[120:123], v5 offset:22528\n"
"v_lshlrev_b32 v12, 16, v18\n"
"v_mfma_f32_16x16x32_bf16 v[216:219], a[48:51], a[128:131], v[216:219]\n"
"v_and_b32 v13, s63, v18\n"
"v_lshlrev_b32 v14, 16, v19\n"
"v_mfma_f32_16x16x32_bf16 v[216:219], a[52:55], a[132:135], v[216:219]\n"
"v_and_b32 v15, s63, v19\n"
"v_mul_f32 v12, v12, v84\n"
"v_mfma_f32_16x16x32_bf16 v[244:247], a[60:63], a[132:135], v[244:247]\n"
"ds_read_b128 a[132:135], v5 offset:25600\n"
"v_mul_f32 v13, v13, v85\n"
"v_mfma_f32_16x16x32_bf16 v[244:247], a[56:59], a[128:131], v[244:247]\n"
"ds_read_b128 a[128:131], v5 offset:24576\n"
"v_mul_f32 v14, v14, v86\n"
"v_mfma_f32_16x16x32_bf16 v[248:251], a[56:59], a[136:139], v[248:251]\n"
"v_mul_f32 v15, v15, v87\n"
"v_mfma_f32_16x16x32_bf16 v[248:251], a[60:63], a[140:143], v[248:251]\n"
"v_cvt_pk_bf16_f32 v26, v12, v13\n"
"v_cvt_pk_bf16_f32 v27, v14, v15\n"
"v_mfma_f32_16x16x32_bf16 v[220:223], a[52:55], a[140:143], v[220:223]\n"
"ds_read_b128 a[140:143], v5 offset:27648\n"
"v_mfma_f32_16x16x32_bf16 v[220:223], a[48:51], a[136:139], v[220:223]\n"
"ds_read_b128 a[136:139], v5 offset:26624\n"
"v_mfma_f32_16x16x32_bf16 v[224:227], a[48:51], a[144:147], v[224:227]\n"
"ds_write_b128 v7, v[24:27] offset:12288\n"
"v_mfma_f32_16x16x32_bf16 v[224:227], a[52:55], a[148:151], v[224:227]\n"
"s_waitcnt lgkmcnt(14)\n"
"buffer_store_dwordx4 v[52:55], v6, s[52:55], s90 offen offset:2048 sc1\n"
"v_mfma_f32_16x16x32_bf16 v[252:255], a[60:63], a[148:151], v[252:255]\n"
"s_waitcnt lgkmcnt(11)\n"
"buffer_store_dwordx4 v[80:83], v6, s[52:55], s91 offen offset:3072 sc1\n"
"ds_read_b128 a[148:151], v5 offset:29696\n"
"v_accvgpr_read_b32 v16, a180\n"
"v_accvgpr_read_b32 v17, a181\n"
"v_mfma_f32_16x16x32_bf16 v[252:255], a[56:59], a[144:147], v[252:255]\n"
"ds_read_b128 a[144:147], v5 offset:28672\n"
"v_accvgpr_read_b32 v18, a182\n"
"v_mfma_f32_16x16x32_bf16 a[28:31], a[56:59], a[152:155], a[28:31]\n"
"v_accvgpr_read_b32 v19, a183\n"
"v_lshlrev_b32 v12, 16, v16\n"
"v_mfma_f32_16x16x32_bf16 a[28:31], a[60:63], a[156:159], a[28:31]\n"
"v_and_b32 v13, s63, v16\n"
"v_lshlrev_b32 v14, 16, v17\n"
"v_mfma_f32_16x16x32_bf16 a[24:27], a[52:55], a[156:159], a[24:27]\n"
"ds_read_b128 a[156:159], v5 offset:31744\n"
"v_and_b32 v15, s63, v17\n"
"v_mfma_f32_16x16x32_bf16 a[24:27], a[48:51], a[152:155], a[24:27]\n"
"s_waitcnt vmcnt(8) lgkmcnt(12)\n"
"ds_read_b128 a[152:155], v5 offset:30720\n"
"v_mul_f32 v12, v12, v112\n"
"v_mfma_f32_16x16x32_bf16 v[144:147], a[64:67], a[96:99], v[144:147]\n"
"v_mul_f32 v13, v13, v113\n"
"v_mul_f32 v14, v14, v114\n"
"v_mfma_f32_16x16x32_bf16 v[144:147], a[68:71], a[100:103], v[144:147]\n"
"v_mul_f32 v15, v15, v115\n"
"v_cvt_pk_bf16_f32 v28, v12, v13\n"
"v_mfma_f32_16x16x32_bf16 v[172:175], a[76:79], a[100:103], v[172:175]\n"
"v_cvt_pk_bf16_f32 v29, v14, v15\n"
"v_mfma_f32_16x16x32_bf16 v[172:175], a[72:75], a[96:99], v[172:175]\n"
"v_lshlrev_b32 v12, 16, v18\n"
"v_and_b32 v13, s63, v18\n"
"v_mfma_f32_16x16x32_bf16 v[176:179], a[72:75], a[104:107], v[176:179]\n"
"v_lshlrev_b32 v14, 16, v19\n"
"v_mfma_f32_16x16x32_bf16 v[176:179], a[76:79], a[108:111], v[176:179]\n"
"v_and_b32 v15, s63, v19\n"
"v_mfma_f32_16x16x32_bf16 v[148:151], a[68:71], a[108:111], v[148:151]\n"
"v_mul_f32 v12, v12, v140\n"
"v_mul_f32 v13, v13, v141\n"
"v_mfma_f32_16x16x32_bf16 v[148:151], a[64:67], a[104:107], v[148:151]\n"
"s_waitcnt lgkmcnt(9)\n"
"v_mul_f32 v14, v14, v142\n"
"v_mfma_f32_16x16x32_bf16 v[152:155], a[64:67], a[112:115], v[152:155]\n"
"v_mul_f32 v15, v15, v143\n"
"v_mfma_f32_16x16x32_bf16 v[152:155], a[68:71], a[116:119], v[152:155]\n"
"v_cvt_pk_bf16_f32 v30, v12, v13\n"
"v_cvt_pk_bf16_f32 v31, v14, v15\n"
"v_mfma_f32_16x16x32_bf16 v[180:183], a[76:79], a[116:119], v[180:183]\n"
"ds_write_b128 v8, v[28:31] offset:12288\n"
"v_mfma_f32_16x16x32_bf16 v[180:183], a[72:75], a[112:115], v[180:183]\n"
"ds_read_b128 v[56:59], v2 offset:12288\n"
"v_mfma_f32_16x16x32_bf16 v[184:187], a[72:75], a[120:123], v[184:187]\n"
"ds_read_b128 v[84:87], v2 offset:13312\n"
"v_accvgpr_read_b32 v16, a184\n"
"v_mfma_f32_16x16x32_bf16 v[184:187], a[76:79], a[124:127], v[184:187]\n"
"v_accvgpr_read_b32 v17, a185\n"
"v_mfma_f32_16x16x32_bf16 v[156:159], a[68:71], a[124:127], v[156:159]\n"
"v_accvgpr_read_b32 v18, a186\n"
"v_accvgpr_read_b32 v19, a187\n"
"v_mfma_f32_16x16x32_bf16 v[156:159], a[64:67], a[120:123], v[156:159]\n"
"s_waitcnt lgkmcnt(8)\n"
"v_lshlrev_b32 v12, 16, v16\n"
"v_mfma_f32_16x16x32_bf16 v[160:163], a[64:67], a[128:131], v[160:163]\n"
"v_and_b32 v13, s63, v16\n"
"v_mfma_f32_16x16x32_bf16 v[160:163], a[68:71], a[132:135], v[160:163]\n"
"v_lshlrev_b32 v14, 16, v17\n"
"v_and_b32 v15, s63, v17\n"
"v_mfma_f32_16x16x32_bf16 v[188:191], a[76:79], a[132:135], v[188:191]\n"
"v_accvgpr_read_b32 v10, a0\n"
"v_mfma_f32_16x16x32_bf16 v[188:191], a[72:75], a[128:131], v[188:191]\n"
"v_accvgpr_read_b32 v11, a1\n"
"v_mfma_f32_16x16x32_bf16 v[192:195], a[72:75], a[136:139], v[192:195]\n"
"v_mul_f32 v12, v12, v10\n"
"v_mul_f32 v13, v13, v11\n"
"v_mfma_f32_16x16x32_bf16 v[192:195], a[76:79], a[140:143], v[192:195]\n"
"v_accvgpr_read_b32 v10, a2\n"
"v_mfma_f32_16x16x32_bf16 v[164:167], a[68:71], a[140:143], v[164:167]\n"
"v_accvgpr_read_b32 v11, a3\n"
"v_mfma_f32_16x16x32_bf16 v[164:167], a[64:67], a[136:139], v[164:167]\n"
"s_waitcnt lgkmcnt(3)\n"
"v_mul_f32 v14, v14, v10\n"
"v_mul_f32 v15, v15, v11\n"
"v_mfma_f32_16x16x32_bf16 v[168:171], a[64:67], a[144:147], v[168:171]\n"
"v_cvt_pk_bf16_f32 v24, v12, v13\n"
"v_mfma_f32_16x16x32_bf16 v[168:171], a[68:71], a[148:151], v[168:171]\n"
"v_cvt_pk_bf16_f32 v25, v14, v15\n"
"v_mfma_f32_16x16x32_bf16 v[196:199], a[76:79], a[148:151], v[196:199]\n"
"v_lshlrev_b32 v12, 16, v18\n"
"v_and_b32 v13, s63, v18\n"
"v_mfma_f32_16x16x32_bf16 v[196:199], a[72:75], a[144:147], v[196:199]\n"
"v_lshlrev_b32 v14, 16, v19\n"
"v_mfma_f32_16x16x32_bf16 a[20:23], a[72:75], a[152:155], a[20:23]\n"
"v_and_b32 v15, s63, v19\n"
"v_accvgpr_read_b32 v10, a4\n"
"v_mfma_f32_16x16x32_bf16 a[20:23], a[76:79], a[156:159], a[20:23]\n"
"v_accvgpr_read_b32 v11, a5\n"
"v_mfma_f32_16x16x32_bf16 a[16:19], a[68:71], a[156:159], a[16:19]\n"
"v_mul_f32 v12, v12, v10\n"
"v_mfma_f32_16x16x32_bf16 a[16:19], a[64:67], a[152:155], a[16:19]\n"
"s_waitcnt vmcnt(2)\n"
"v_mul_f32 v13, v13, v11\n"
"v_accvgpr_read_b32 v10, a6\n"
"v_mfma_f32_16x16x32_bf16 v[200:203], a[80:83], a[96:99], v[200:203]\n"
"v_accvgpr_read_b32 v11, a7\n"
"v_mfma_f32_16x16x32_bf16 v[200:203], a[84:87], a[100:103], v[200:203]\n"
"v_mul_f32 v14, v14, v10\n"
"v_mfma_f32_16x16x32_bf16 v[228:231], a[92:95], a[100:103], v[228:231]\n"
"v_mul_f32 v15, v15, v11\n"
"v_cvt_pk_bf16_f32 v26, v12, v13\n"
"v_mfma_f32_16x16x32_bf16 v[228:231], a[88:91], a[96:99], v[228:231]\n"
"v_cvt_pk_bf16_f32 v27, v14, v15\n"
"v_mfma_f32_16x16x32_bf16 v[232:235], a[88:91], a[104:107], v[232:235]\n"
"ds_write_b128 v7, v[24:27] offset:14336\n"
"v_mfma_f32_16x16x32_bf16 v[232:235], a[92:95], a[108:111], v[232:235]\n"
"s_waitcnt lgkmcnt(2)\n"
"buffer_store_dwordx4 v[56:59], v6, s[52:55], s92 offen offset:0 sc1\n"
"v_mfma_f32_16x16x32_bf16 v[204:207], a[84:87], a[108:111], v[204:207]\n"
"s_waitcnt lgkmcnt(1)\n"
"buffer_store_dwordx4 v[84:87], v6, s[52:55], s93 offen offset:1024 sc1\n"
"v_accvgpr_read_b32 v16, a188\n"
"v_accvgpr_read_b32 v17, a189\n"
"v_accvgpr_read_b32 v18, a190\n"
"v_mfma_f32_16x16x32_bf16 v[204:207], a[80:83], a[104:107], v[204:207]\n"
"v_accvgpr_read_b32 v19, a191\n"
"v_mfma_f32_16x16x32_bf16 v[208:211], a[80:83], a[112:115], v[208:211]\n"
"v_lshlrev_b32 v12, 16, v16\n"
"v_mfma_f32_16x16x32_bf16 v[208:211], a[84:87], a[116:119], v[208:211]\n"
"v_and_b32 v13, s63, v16\n"
"v_mfma_f32_16x16x32_bf16 v[236:239], a[92:95], a[116:119], v[236:239]\n"
"v_lshlrev_b32 v14, 16, v17\n"
"v_mfma_f32_16x16x32_bf16 v[236:239], a[88:91], a[112:115], v[236:239]\n"
"v_and_b32 v15, s63, v17\n"
"v_mfma_f32_16x16x32_bf16 v[240:243], a[88:91], a[120:123], v[240:243]\n"
"v_accvgpr_read_b32 v10, a8\n"
"v_mfma_f32_16x16x32_bf16 v[240:243], a[92:95], a[124:127], v[240:243]\n"
"v_accvgpr_read_b32 v11, a9\n"
"v_mul_f32 v12, v12, v10\n"
"v_mfma_f32_16x16x32_bf16 v[212:215], a[84:87], a[124:127], v[212:215]\n"
"v_mul_f32 v13, v13, v11\n"
"v_mfma_f32_16x16x32_bf16 v[212:215], a[80:83], a[120:123], v[212:215]\n"
"v_accvgpr_read_b32 v10, a10\n"
"v_mfma_f32_16x16x32_bf16 v[216:219], a[80:83], a[128:131], v[216:219]\n"
"v_accvgpr_read_b32 v11, a11\n"
"v_mul_f32 v14, v14, v10\n"
"v_mfma_f32_16x16x32_bf16 v[216:219], a[84:87], a[132:135], v[216:219]\n"
"v_mul_f32 v15, v15, v11\n"
"v_mfma_f32_16x16x32_bf16 v[244:247], a[92:95], a[132:135], v[244:247]\n"
"v_cvt_pk_bf16_f32 v28, v12, v13\n"
"v_mfma_f32_16x16x32_bf16 v[244:247], a[88:91], a[128:131], v[244:247]\n"
"v_cvt_pk_bf16_f32 v29, v14, v15\n"
"v_lshlrev_b32 v12, 16, v18\n"
"v_mfma_f32_16x16x32_bf16 v[248:251], a[88:91], a[136:139], v[248:251]\n"
"v_and_b32 v13, s63, v18\n"
"v_mfma_f32_16x16x32_bf16 v[248:251], a[92:95], a[140:143], v[248:251]\n"
"v_lshlrev_b32 v14, 16, v19\n"
"v_mfma_f32_16x16x32_bf16 v[220:223], a[84:87], a[140:143], v[220:223]\n"
"v_and_b32 v15, s63, v19\n"
"v_accvgpr_read_b32 v10, a12\n"
"v_mfma_f32_16x16x32_bf16 v[220:223], a[80:83], a[136:139], v[220:223]\n"
"v_accvgpr_read_b32 v11, a13\n"
"v_mfma_f32_16x16x32_bf16 v[224:227], a[80:83], a[144:147], v[224:227]\n"
"v_mul_f32 v12, v12, v10\n"
"v_mul_f32 v13, v13, v11\n"
"v_mfma_f32_16x16x32_bf16 v[224:227], a[84:87], a[148:151], v[224:227]\n"
"v_accvgpr_read_b32 v10, a14\n"
"v_mfma_f32_16x16x32_bf16 v[252:255], a[92:95], a[148:151], v[252:255]\n"
"v_accvgpr_read_b32 v11, a15\n"
"v_mfma_f32_16x16x32_bf16 v[252:255], a[88:91], a[144:147], v[252:255]\n"
"v_mul_f32 v14, v14, v10\n"
"v_mul_f32 v15, v15, v11\n"
"v_mfma_f32_16x16x32_bf16 a[28:31], a[88:91], a[152:155], a[28:31]\n"
"s_add_u32 s62, s62, 1\n"
"v_cvt_pk_bf16_f32 v30, v12, v13\n"
"v_cvt_pk_bf16_f32 v31, v14, v15\n"
"v_mfma_f32_16x16x32_bf16 a[28:31], a[92:95], a[156:159], a[28:31]\n"
"ds_write_b128 v8, v[28:31] offset:14336\n"
"v_mfma_f32_16x16x32_bf16 a[24:27], a[84:87], a[156:159], a[24:27]\n"
"ds_read_b128 v[52:55], v2 offset:14336\n"
"v_mfma_f32_16x16x32_bf16 a[24:27], a[80:83], a[152:155], a[24:27]\n"
"ds_read_b128 v[80:83], v2 offset:15360\n"
"s_nop 7\n"
"v_accvgpr_read_b32 v16, a192\n"
"v_accvgpr_read_b32 v17, a193\n"
"v_accvgpr_read_b32 v18, a194\n"
"v_accvgpr_read_b32 v19, a195\n"
"v_lshlrev_b32 v12, 16, v16\n"
"v_and_b32 v13, s63, v16\n"
"v_lshlrev_b32 v14, 16, v17\n"
"v_and_b32 v15, s63, v17\n"
"v_pk_mul_f32 v[12:13], v[12:13], v[144:145]\n"
"v_pk_mul_f32 v[14:15], v[14:15], v[146:147]\n"
"v_cvt_pk_bf16_f32 v24, v12, v13\n"
"v_cvt_pk_bf16_f32 v25, v14, v15\n"
"v_lshlrev_b32 v12, 16, v18\n"
"v_and_b32 v13, s63, v18\n"
"v_lshlrev_b32 v14, 16, v19\n"
"v_and_b32 v15, s63, v19\n"
"v_pk_mul_f32 v[12:13], v[12:13], v[172:173]\n"
"v_pk_mul_f32 v[14:15], v[14:15], v[174:175]\n"
"v_cvt_pk_bf16_f32 v26, v12, v13\n"
"v_cvt_pk_bf16_f32 v27, v14, v15\n"
"ds_write_b128 v7, v[24:27] offset:0\n"
"s_waitcnt lgkmcnt(2)\n"
"buffer_store_dwordx4 v[52:55], v6, s[52:55], s94 offen offset:2048 sc1\n"
"s_waitcnt lgkmcnt(1)\n"
"buffer_store_dwordx4 v[80:83], v6, s[52:55], s95 offen offset:3072 sc1\n"
"v_accvgpr_read_b32 v16, a196\n"
"v_accvgpr_read_b32 v17, a197\n"
"v_accvgpr_read_b32 v18, a198\n"
"v_accvgpr_read_b32 v19, a199\n"
"v_lshlrev_b32 v12, 16, v16\n"
"v_and_b32 v13, s63, v16\n"
"v_lshlrev_b32 v14, 16, v17\n"
"v_and_b32 v15, s63, v17\n"
"v_pk_mul_f32 v[12:13], v[12:13], v[200:201]\n"
"v_pk_mul_f32 v[14:15], v[14:15], v[202:203]\n"
"v_cvt_pk_bf16_f32 v28, v12, v13\n"
"v_cvt_pk_bf16_f32 v29, v14, v15\n"
"v_lshlrev_b32 v12, 16, v18\n"
"v_and_b32 v13, s63, v18\n"
"v_lshlrev_b32 v14, 16, v19\n"
"v_and_b32 v15, s63, v19\n"
"v_pk_mul_f32 v[12:13], v[12:13], v[228:229]\n"
"v_pk_mul_f32 v[14:15], v[14:15], v[230:231]\n"
"v_cvt_pk_bf16_f32 v30, v12, v13\n"
"v_cvt_pk_bf16_f32 v31, v14, v15\n"
"ds_write_b128 v8, v[28:31] offset:0\n"
"ds_read_b128 v[144:147], v2 offset:0\n"
"ds_read_b128 v[172:175], v2 offset:1024\n"
"v_accvgpr_read_b32 v16, a200\n"
"v_accvgpr_read_b32 v17, a201\n"
"v_accvgpr_read_b32 v18, a202\n"
"v_accvgpr_read_b32 v19, a203\n"
"v_lshlrev_b32 v12, 16, v16\n"
"v_and_b32 v13, s63, v16\n"
"v_lshlrev_b32 v14, 16, v17\n"
"v_and_b32 v15, s63, v17\n"
"v_pk_mul_f32 v[12:13], v[12:13], v[148:149]\n"
"v_pk_mul_f32 v[14:15], v[14:15], v[150:151]\n"
"v_cvt_pk_bf16_f32 v24, v12, v13\n"
"v_cvt_pk_bf16_f32 v25, v14, v15\n"
"v_lshlrev_b32 v12, 16, v18\n"
"v_and_b32 v13, s63, v18\n"
"v_lshlrev_b32 v14, 16, v19\n"
"v_and_b32 v15, s63, v19\n"
"v_pk_mul_f32 v[12:13], v[12:13], v[176:177]\n"
"v_pk_mul_f32 v[14:15], v[14:15], v[178:179]\n"
"v_cvt_pk_bf16_f32 v26, v12, v13\n"
"v_cvt_pk_bf16_f32 v27, v14, v15\n"
"ds_write_b128 v7, v[24:27] offset:2048\n"
"s_waitcnt lgkmcnt(2)\n"
"buffer_store_dwordx4 v[144:147], v6, s[52:55], s80 offen offset:128 sc1\n"
"s_waitcnt lgkmcnt(1)\n"
"buffer_store_dwordx4 v[172:175], v6, s[52:55], s81 offen offset:1152 sc1\n"
"v_accvgpr_read_b32 v16, a204\n"
"v_accvgpr_read_b32 v17, a205\n"
"v_accvgpr_read_b32 v18, a206\n"
"v_accvgpr_read_b32 v19, a207\n"
"v_lshlrev_b32 v12, 16, v16\n"
"v_and_b32 v13, s63, v16\n"
"v_lshlrev_b32 v14, 16, v17\n"
"v_and_b32 v15, s63, v17\n"
"v_pk_mul_f32 v[12:13], v[12:13], v[204:205]\n"
"v_pk_mul_f32 v[14:15], v[14:15], v[206:207]\n"
"v_cvt_pk_bf16_f32 v28, v12, v13\n"
"v_cvt_pk_bf16_f32 v29, v14, v15\n"
"v_lshlrev_b32 v12, 16, v18\n"
"v_and_b32 v13, s63, v18\n"
"v_lshlrev_b32 v14, 16, v19\n"
"v_and_b32 v15, s63, v19\n"
"v_pk_mul_f32 v[12:13], v[12:13], v[232:233]\n"
"v_pk_mul_f32 v[14:15], v[14:15], v[234:235]\n"
"v_cvt_pk_bf16_f32 v30, v12, v13\n"
"v_cvt_pk_bf16_f32 v31, v14, v15\n"
"ds_write_b128 v8, v[28:31] offset:2048\n"
"ds_read_b128 v[148:151], v2 offset:2048\n"
"ds_read_b128 v[176:179], v2 offset:3072\n"
"v_accvgpr_read_b32 v16, a208\n"
"v_accvgpr_read_b32 v17, a209\n"
"v_accvgpr_read_b32 v18, a210\n"
"v_accvgpr_read_b32 v19, a211\n"
"v_lshlrev_b32 v12, 16, v16\n"
"v_and_b32 v13, s63, v16\n"
"v_lshlrev_b32 v14, 16, v17\n"
"v_and_b32 v15, s63, v17\n"
"v_pk_mul_f32 v[12:13], v[12:13], v[152:153]\n"
"v_pk_mul_f32 v[14:15], v[14:15], v[154:155]\n"
"v_cvt_pk_bf16_f32 v24, v12, v13\n"
"v_cvt_pk_bf16_f32 v25, v14, v15\n"
"v_lshlrev_b32 v12, 16, v18\n"
"v_and_b32 v13, s63, v18\n"
"v_lshlrev_b32 v14, 16, v19\n"
"v_and_b32 v15, s63, v19\n"
"v_pk_mul_f32 v[12:13], v[12:13], v[180:181]\n"
"v_pk_mul_f32 v[14:15], v[14:15], v[182:183]\n"
"v_cvt_pk_bf16_f32 v26, v12, v13\n"
"v_cvt_pk_bf16_f32 v27, v14, v15\n"
"ds_write_b128 v7, v[24:27] offset:4096\n"
"s_waitcnt lgkmcnt(2)\n"
"buffer_store_dwordx4 v[148:151], v6, s[52:55], s82 offen offset:2176 sc1\n"
"s_waitcnt lgkmcnt(1)\n"
"buffer_store_dwordx4 v[176:179], v6, s[52:55], s83 offen offset:3200 sc1\n"
"v_accvgpr_read_b32 v16, a212\n"
"v_accvgpr_read_b32 v17, a213\n"
"v_accvgpr_read_b32 v18, a214\n"
"v_accvgpr_read_b32 v19, a215\n"
"v_lshlrev_b32 v12, 16, v16\n"
"v_and_b32 v13, s63, v16\n"
"v_lshlrev_b32 v14, 16, v17\n"
"v_and_b32 v15, s63, v17\n"
"v_pk_mul_f32 v[12:13], v[12:13], v[208:209]\n"
"v_pk_mul_f32 v[14:15], v[14:15], v[210:211]\n"
"v_cvt_pk_bf16_f32 v28, v12, v13\n"
"v_cvt_pk_bf16_f32 v29, v14, v15\n"
"v_lshlrev_b32 v12, 16, v18\n"
"v_and_b32 v13, s63, v18\n"
"v_lshlrev_b32 v14, 16, v19\n"
"v_and_b32 v15, s63, v19\n"
"v_pk_mul_f32 v[12:13], v[12:13], v[236:237]\n"
"v_pk_mul_f32 v[14:15], v[14:15], v[238:239]\n"
"v_cvt_pk_bf16_f32 v30, v12, v13\n"
"v_cvt_pk_bf16_f32 v31, v14, v15\n"
"ds_write_b128 v8, v[28:31] offset:4096\n"
"ds_read_b128 v[152:155], v2 offset:4096\n"
"ds_read_b128 v[180:183], v2 offset:5120\n"
"v_accvgpr_read_b32 v16, a216\n"
"v_accvgpr_read_b32 v17, a217\n"
"v_accvgpr_read_b32 v18, a218\n"
"v_accvgpr_read_b32 v19, a219\n"
"v_lshlrev_b32 v12, 16, v16\n"
"v_and_b32 v13, s63, v16\n"
"v_lshlrev_b32 v14, 16, v17\n"
"v_and_b32 v15, s63, v17\n"
"v_pk_mul_f32 v[12:13], v[12:13], v[156:157]\n"
"v_pk_mul_f32 v[14:15], v[14:15], v[158:159]\n"
"v_cvt_pk_bf16_f32 v24, v12, v13\n"
"v_cvt_pk_bf16_f32 v25, v14, v15\n"
"v_lshlrev_b32 v12, 16, v18\n"
"v_and_b32 v13, s63, v18\n"
"v_lshlrev_b32 v14, 16, v19\n"
"v_and_b32 v15, s63, v19\n"
"v_pk_mul_f32 v[12:13], v[12:13], v[184:185]\n"
"v_pk_mul_f32 v[14:15], v[14:15], v[186:187]\n"
"v_cvt_pk_bf16_f32 v26, v12, v13\n"
"v_cvt_pk_bf16_f32 v27, v14, v15\n"
"ds_write_b128 v7, v[24:27] offset:6144\n"
"s_waitcnt lgkmcnt(2)\n"
"buffer_store_dwordx4 v[152:155], v6, s[52:55], s84 offen offset:128 sc1\n"
"s_waitcnt lgkmcnt(1)\n"
"buffer_store_dwordx4 v[180:183], v6, s[52:55], s85 offen offset:1152 sc1\n"
"v_accvgpr_read_b32 v16, a220\n"
"v_accvgpr_read_b32 v17, a221\n"
"v_accvgpr_read_b32 v18, a222\n"
"v_accvgpr_read_b32 v19, a223\n"
"v_lshlrev_b32 v12, 16, v16\n"
"v_and_b32 v13, s63, v16\n"
"v_lshlrev_b32 v14, 16, v17\n"
"v_and_b32 v15, s63, v17\n"
"v_pk_mul_f32 v[12:13], v[12:13], v[212:213]\n"
"v_pk_mul_f32 v[14:15], v[14:15], v[214:215]\n"
"v_cvt_pk_bf16_f32 v28, v12, v13\n"
"v_cvt_pk_bf16_f32 v29, v14, v15\n"
"v_lshlrev_b32 v12, 16, v18\n"
"v_and_b32 v13, s63, v18\n"
"v_lshlrev_b32 v14, 16, v19\n"
"v_and_b32 v15, s63, v19\n"
"v_pk_mul_f32 v[12:13], v[12:13], v[240:241]\n"
"v_pk_mul_f32 v[14:15], v[14:15], v[242:243]\n"
"v_cvt_pk_bf16_f32 v30, v12, v13\n"
"v_cvt_pk_bf16_f32 v31, v14, v15\n"
"ds_write_b128 v8, v[28:31] offset:6144\n"
"ds_read_b128 v[156:159], v2 offset:6144\n"
"ds_read_b128 v[184:187], v2 offset:7168\n"
"v_accvgpr_read_b32 v16, a224\n"
"v_accvgpr_read_b32 v17, a225\n"
"v_accvgpr_read_b32 v18, a226\n"
"v_accvgpr_read_b32 v19, a227\n"
"v_lshlrev_b32 v12, 16, v16\n"
"v_and_b32 v13, s63, v16\n"
"v_lshlrev_b32 v14, 16, v17\n"
"v_and_b32 v15, s63, v17\n"
"v_pk_mul_f32 v[12:13], v[12:13], v[160:161]\n"
"v_pk_mul_f32 v[14:15], v[14:15], v[162:163]\n"
"v_cvt_pk_bf16_f32 v24, v12, v13\n"
"v_cvt_pk_bf16_f32 v25, v14, v15\n"
"v_lshlrev_b32 v12, 16, v18\n"
"v_and_b32 v13, s63, v18\n"
"v_lshlrev_b32 v14, 16, v19\n"
"v_and_b32 v15, s63, v19\n"
"v_pk_mul_f32 v[12:13], v[12:13], v[188:189]\n"
"v_pk_mul_f32 v[14:15], v[14:15], v[190:191]\n"
"v_cvt_pk_bf16_f32 v26, v12, v13\n"
"v_cvt_pk_bf16_f32 v27, v14, v15\n"
"ds_write_b128 v7, v[24:27] offset:8192\n"
"s_waitcnt lgkmcnt(2)\n"
"buffer_store_dwordx4 v[156:159], v6, s[52:55], s86 offen offset:2176 sc1\n"
"s_waitcnt lgkmcnt(1)\n"
"buffer_store_dwordx4 v[184:187], v6, s[52:55], s87 offen offset:3200 sc1\n"
"v_accvgpr_read_b32 v16, a228\n"
"v_accvgpr_read_b32 v17, a229\n"
"v_accvgpr_read_b32 v18, a230\n"
"v_accvgpr_read_b32 v19, a231\n"
"v_lshlrev_b32 v12, 16, v16\n"
"v_and_b32 v13, s63, v16\n"
"v_lshlrev_b32 v14, 16, v17\n"
"v_and_b32 v15, s63, v17\n"
"v_pk_mul_f32 v[12:13], v[12:13], v[216:217]\n"
"v_pk_mul_f32 v[14:15], v[14:15], v[218:219]\n"
"v_cvt_pk_bf16_f32 v28, v12, v13\n"
"v_cvt_pk_bf16_f32 v29, v14, v15\n"
"v_lshlrev_b32 v12, 16, v18\n"
"v_and_b32 v13, s63, v18\n"
"v_lshlrev_b32 v14, 16, v19\n"
"v_and_b32 v15, s63, v19\n"
"v_pk_mul_f32 v[12:13], v[12:13], v[244:245]\n"
"v_pk_mul_f32 v[14:15], v[14:15], v[246:247]\n"
"v_cvt_pk_bf16_f32 v30, v12, v13\n"
"v_cvt_pk_bf16_f32 v31, v14, v15\n"
"ds_write_b128 v8, v[28:31] offset:8192\n"
"ds_read_b128 v[160:163], v2 offset:8192\n"
"ds_read_b128 v[188:191], v2 offset:9216\n"
"v_accvgpr_read_b32 v16, a232\n"
"v_accvgpr_read_b32 v17, a233\n"
"v_accvgpr_read_b32 v18, a234\n"
"v_accvgpr_read_b32 v19, a235\n"
"v_lshlrev_b32 v12, 16, v16\n"
"v_and_b32 v13, s63, v16\n"
"v_lshlrev_b32 v14, 16, v17\n"
"v_and_b32 v15, s63, v17\n"
"v_pk_mul_f32 v[12:13], v[12:13], v[164:165]\n"
"v_pk_mul_f32 v[14:15], v[14:15], v[166:167]\n"
"v_cvt_pk_bf16_f32 v24, v12, v13\n"
"v_cvt_pk_bf16_f32 v25, v14, v15\n"
"v_lshlrev_b32 v12, 16, v18\n"
"v_and_b32 v13, s63, v18\n"
"v_lshlrev_b32 v14, 16, v19\n"
"v_and_b32 v15, s63, v19\n"
"v_pk_mul_f32 v[12:13], v[12:13], v[192:193]\n"
"v_pk_mul_f32 v[14:15], v[14:15], v[194:195]\n"
"v_cvt_pk_bf16_f32 v26, v12, v13\n"
"v_cvt_pk_bf16_f32 v27, v14, v15\n"
"ds_write_b128 v7, v[24:27] offset:10240\n"
"s_waitcnt lgkmcnt(2)\n"
"buffer_store_dwordx4 v[160:163], v6, s[52:55], s88 offen offset:128 sc1\n"
"s_waitcnt lgkmcnt(1)\n"
"buffer_store_dwordx4 v[188:191], v6, s[52:55], s89 offen offset:1152 sc1\n"
"v_accvgpr_read_b32 v16, a236\n"
"v_accvgpr_read_b32 v17, a237\n"
"v_accvgpr_read_b32 v18, a238\n"
"v_accvgpr_read_b32 v19, a239\n"
"v_lshlrev_b32 v12, 16, v16\n"
"v_and_b32 v13, s63, v16\n"
"v_lshlrev_b32 v14, 16, v17\n"
"v_and_b32 v15, s63, v17\n"
"v_pk_mul_f32 v[12:13], v[12:13], v[220:221]\n"
"v_pk_mul_f32 v[14:15], v[14:15], v[222:223]\n"
"v_cvt_pk_bf16_f32 v28, v12, v13\n"
"v_cvt_pk_bf16_f32 v29, v14, v15\n"
"v_lshlrev_b32 v12, 16, v18\n"
"v_and_b32 v13, s63, v18\n"
"v_lshlrev_b32 v14, 16, v19\n"
"v_and_b32 v15, s63, v19\n"
"v_pk_mul_f32 v[12:13], v[12:13], v[248:249]\n"
"v_pk_mul_f32 v[14:15], v[14:15], v[250:251]\n"
"v_cvt_pk_bf16_f32 v30, v12, v13\n"
"v_cvt_pk_bf16_f32 v31, v14, v15\n"
"ds_write_b128 v8, v[28:31] offset:10240\n"
"ds_read_b128 v[164:167], v2 offset:10240\n"
"ds_read_b128 v[192:195], v2 offset:11264\n"
"v_accvgpr_read_b32 v16, a240\n"
"v_accvgpr_read_b32 v17, a241\n"
"v_accvgpr_read_b32 v18, a242\n"
"v_accvgpr_read_b32 v19, a243\n"
"v_lshlrev_b32 v12, 16, v16\n"
"v_and_b32 v13, s63, v16\n"
"v_lshlrev_b32 v14, 16, v17\n"
"v_and_b32 v15, s63, v17\n"
"v_pk_mul_f32 v[12:13], v[12:13], v[168:169]\n"
"v_pk_mul_f32 v[14:15], v[14:15], v[170:171]\n"
"v_cvt_pk_bf16_f32 v24, v12, v13\n"
"v_cvt_pk_bf16_f32 v25, v14, v15\n"
"v_lshlrev_b32 v12, 16, v18\n"
"v_and_b32 v13, s63, v18\n"
"v_lshlrev_b32 v14, 16, v19\n"
"v_and_b32 v15, s63, v19\n"
"v_pk_mul_f32 v[12:13], v[12:13], v[196:197]\n"
"v_pk_mul_f32 v[14:15], v[14:15], v[198:199]\n"
"v_cvt_pk_bf16_f32 v26, v12, v13\n"
"v_cvt_pk_bf16_f32 v27, v14, v15\n"
"ds_write_b128 v7, v[24:27] offset:12288\n"
"s_waitcnt lgkmcnt(2)\n"
"buffer_store_dwordx4 v[164:167], v6, s[52:55], s90 offen offset:2176 sc1\n"
"s_waitcnt lgkmcnt(1)\n"
"buffer_store_dwordx4 v[192:195], v6, s[52:55], s91 offen offset:3200 sc1\n"
"v_accvgpr_read_b32 v16, a244\n"
"v_accvgpr_read_b32 v17, a245\n"
"v_accvgpr_read_b32 v18, a246\n"
"v_accvgpr_read_b32 v19, a247\n"
"v_lshlrev_b32 v12, 16, v16\n"
"v_and_b32 v13, s63, v16\n"
"v_lshlrev_b32 v14, 16, v17\n"
"v_and_b32 v15, s63, v17\n"
"v_pk_mul_f32 v[12:13], v[12:13], v[224:225]\n"
"v_pk_mul_f32 v[14:15], v[14:15], v[226:227]\n"
"v_cvt_pk_bf16_f32 v28, v12, v13\n"
"v_cvt_pk_bf16_f32 v29, v14, v15\n"
"v_lshlrev_b32 v12, 16, v18\n"
"v_and_b32 v13, s63, v18\n"
"v_lshlrev_b32 v14, 16, v19\n"
"v_and_b32 v15, s63, v19\n"
"v_pk_mul_f32 v[12:13], v[12:13], v[252:253]\n"
"v_pk_mul_f32 v[14:15], v[14:15], v[254:255]\n"
"v_cvt_pk_bf16_f32 v30, v12, v13\n"
"v_cvt_pk_bf16_f32 v31, v14, v15\n"
"ds_write_b128 v8, v[28:31] offset:12288\n"
"ds_read_b128 v[168:171], v2 offset:12288\n"
"ds_read_b128 v[196:199], v2 offset:13312\n"
"v_accvgpr_read_b32 v16, a248\n"
"v_accvgpr_read_b32 v17, a249\n"
"v_accvgpr_read_b32 v18, a250\n"
"v_accvgpr_read_b32 v19, a251\n"
"v_lshlrev_b32 v12, 16, v16\n"
"v_and_b32 v13, s63, v16\n"
"v_lshlrev_b32 v14, 16, v17\n"
"v_and_b32 v15, s63, v17\n"
"v_accvgpr_read_b32 v10, a16\n"
"v_accvgpr_read_b32 v11, a17\n"
"v_mul_f32 v12, v12, v10\n"
"v_mul_f32 v13, v13, v11\n"
"v_accvgpr_read_b32 v10, a18\n"
"v_accvgpr_read_b32 v11, a19\n"
"v_mul_f32 v14, v14, v10\n"
"v_mul_f32 v15, v15, v11\n"
"v_cvt_pk_bf16_f32 v24, v12, v13\n"
"v_cvt_pk_bf16_f32 v25, v14, v15\n"
"v_lshlrev_b32 v12, 16, v18\n"
"v_and_b32 v13, s63, v18\n"
"v_lshlrev_b32 v14, 16, v19\n"
"v_and_b32 v15, s63, v19\n"
"v_accvgpr_read_b32 v10, a20\n"
"v_accvgpr_read_b32 v11, a21\n"
"v_mul_f32 v12, v12, v10\n"
"v_mul_f32 v13, v13, v11\n"
"v_accvgpr_read_b32 v10, a22\n"
"v_accvgpr_read_b32 v11, a23\n"
"v_mul_f32 v14, v14, v10\n"
"v_mul_f32 v15, v15, v11\n"
"v_cvt_pk_bf16_f32 v26, v12, v13\n"
"v_cvt_pk_bf16_f32 v27, v14, v15\n"
"ds_write_b128 v7, v[24:27] offset:14336\n"
"s_waitcnt lgkmcnt(2)\n"
"buffer_store_dwordx4 v[168:171], v6, s[52:55], s92 offen offset:128 sc1\n"
"s_waitcnt lgkmcnt(1)\n"
"buffer_store_dwordx4 v[196:199], v6, s[52:55], s93 offen offset:1152 sc1\n"
"v_accvgpr_read_b32 v16, a252\n"
"v_accvgpr_read_b32 v17, a253\n"
"v_accvgpr_read_b32 v18, a254\n"
"v_accvgpr_read_b32 v19, a255\n"
"v_lshlrev_b32 v12, 16, v16\n"
"v_and_b32 v13, s63, v16\n"
"v_lshlrev_b32 v14, 16, v17\n"
"v_and_b32 v15, s63, v17\n"
"v_accvgpr_read_b32 v10, a24\n"
"v_accvgpr_read_b32 v11, a25\n"
"v_mul_f32 v12, v12, v10\n"
"v_mul_f32 v13, v13, v11\n"
"v_accvgpr_read_b32 v10, a26\n"
"v_accvgpr_read_b32 v11, a27\n"
"v_mul_f32 v14, v14, v10\n"
"v_mul_f32 v15, v15, v11\n"
"v_cvt_pk_bf16_f32 v28, v12, v13\n"
"v_cvt_pk_bf16_f32 v29, v14, v15\n"
"v_lshlrev_b32 v12, 16, v18\n"
"v_and_b32 v13, s63, v18\n"
"v_lshlrev_b32 v14, 16, v19\n"
"v_and_b32 v15, s63, v19\n"
"v_accvgpr_read_b32 v10, a28\n"
"v_accvgpr_read_b32 v11, a29\n"
"v_mul_f32 v12, v12, v10\n"
"v_mul_f32 v13, v13, v11\n"
"v_accvgpr_read_b32 v10, a30\n"
"v_accvgpr_read_b32 v11, a31\n"
"v_mul_f32 v14, v14, v10\n"
"v_mul_f32 v15, v15, v11\n"
"v_cvt_pk_bf16_f32 v30, v12, v13\n"
"v_cvt_pk_bf16_f32 v31, v14, v15\n"
"ds_write_b128 v8, v[28:31] offset:14336\n"
"ds_read_b128 v[164:167], v2 offset:14336\n"
"ds_read_b128 v[192:195], v2 offset:15360\n"
"s_waitcnt lgkmcnt(1)\n"
"buffer_store_dwordx4 v[164:167], v6, s[52:55], s94 offen offset:2176 sc1\n"
"s_waitcnt lgkmcnt(0)\n"
"buffer_store_dwordx4 v[192:195], v6, s[52:55], s95 offen offset:3200 sc1\n"
"s_or_b32 s66, %26, %16\n"
"s_cbranch_scc1 L_end\n"
"v_mov_b32 v10, s78\n"
"v_mov_b32 v11, s79\n"
"s_mov_b32 s66, 139264\n"
"buffer_store_dwordx2 v[10:11], off, s[56:59], s66 sc1\n"
"s_branch L_end\n"
"L_slow0:\n"
"s_cmp_eq_u32 s68, 0\n"
"s_cbranch_scc1 L_back0\n"
"s_sub_u32 s68, s68, 1\n"
"s_sleep 2\n"
"buffer_load_dwordx2 v[8:9], off, s[56:59], %14 sc1\n"
"s_waitcnt vmcnt(0)\n"
"v_subrev_u32 v8, s65, v8\n"
"v_xor_b32 v9, s79, v9\n"
"v_or_b32 v8, v8, v9\n"
"v_cmp_lt_u32 vcc, 15, v8\n"
"s_nop 4\n"
"s_cbranch_vccnz L_slow0\n"
"s_branch L_back0\n"
"L_slow1:\n"
"s_cmp_eq_u32 s68, 0\n"
"s_cbranch_scc1 L_back1\n"
"s_sub_u32 s68, s68, 1\n"
"s_sleep 2\n"
"buffer_load_dwordx2 v[8:9], off, s[56:59], %14 sc1\n"
"s_waitcnt vmcnt(0)\n"
"v_subrev_u32 v8, s65, v8\n"
"v_xor_b32 v9, s79, v9\n"
"v_or_b32 v8, v8, v9\n"
"v_cmp_lt_u32 vcc, 15, v8\n"
"s_nop 4\n"
"s_cbranch_vccnz L_slow1\n"
"s_branch L_back1\n"
"L_slow2:\n"
"s_cmp_eq_u32 s68, 0\n"
"s_cbranch_scc1 L_back2\n"
"s_sub_u32 s68, s68, 1\n"
"s_sleep 2\n"
"buffer_load_dwordx4 v[12:15], v0, s[56:59], %25 offen sc1\n"
"s_waitcnt vmcnt(0)\n"
"v_subrev_u32 v12, s67, v12\n"
"v_xor_b32 v13, s79, v13\n"
"v_subrev_u32 v14, s67, v14\n"
"v_xor_b32 v15, s79, v15\n"
"v_or_b32 v12, v12, v14\n"
"v_or_b32 v13, v13, v15\n"
"v_or_b32 v12, v12, v13\n"
"v_cmp_lt_u32 vcc, 15, v12\n"
"s_nop 4\n"
"s_cbranch_vccnz L_slow2\n"
"s_branch L_back2\n"
"L_slow3:\n"
"s_cmp_eq_u32 s68, 0\n"
"s_cbranch_scc1 L_back3\n"
"s_sub_u32 s68, s68, 1\n"
"s_sleep 2\n"
"buffer_load_dwordx4 v[24:27], v0, s[56:59], %25 offen sc1\n"
"s_waitcnt vmcnt(0)\n"
"v_subrev_u32 v24, s70, v24\n"
"v_xor_b32 v25, s79, v25\n"
"v_subrev_u32 v26, s70, v26\n"
"v_xor_b32 v27, s79, v27\n"
"v_or_b32 v24, v24, v26\n"
"v_or_b32 v25, v25, v27\n"
"v_or_b32 v24, v24, v25\n"
"v_cmp_lt_u32 vcc, 15, v24\n"
"s_nop 4\n"
"s_cbranch_vccnz L_slow3\n"
"s_branch L_back3\n"
"L_end:\n"
"s_waitcnt vmcnt(0) lgkmcnt(0)\n"
    :: LO(wsb), HI(wsb), LO(xo), HI(xo), LO(ob), HI(ob), LO(wr), HI(wr), LO(bb), HI(bb), "s"(wo), "s"(eo), "s"(ep), "s"(fo), "s"(fp),
       "s"(ldsa), "s"(w), "s"(col), "v"(threadIdx.x), "s"(db), "s"(rs1), "s"(rs2), "s"(rd1), "s"(rn1), "s"(wfo), "s"(wfa), "s"(b)
    : "memory", "scc", "vcc",
"v1","v2","v3","v4","v5","v6","v7","v8","v9","v10","v11","v12","v13","v14","v15","v16","v17","v18","v19","v20","v21","v22","v23","v24","v25","v26","v27","v28","v29","v30","v31","v32","v33","v34","v35","v36","v37","v38","v39","v40","v41","v42","v43","v44","v45","v46","v47","v48","v49","v50","v51","v52","v53","v54","v55","v56","v57","v58","v59","v60","v61","v62","v63","v64","v65","v66","v67","v68","v69","v70","v71","v72","v73","v74","v75","v76","v77","v78","v79","v80","v81","v82","v83","v84","v85","v86","v87","v88","v89","v90","v91","v92","v93","v94","v95","v96","v97","v98","v99","v100","v101","v102","v103","v104","v105","v106","v107","v108","v109","v110","v111","v112","v113","v114","v115","v116","v117","v118","v119","v120","v121","v122","v123","v124","v125","v126","v127","v128","v129","v130","v131","v132","v133","v134","v135","v136","v137","v138","v139","v140","v141","v142","v143","v144","v145","v146","v147","v148","v149","v150","v151","v152","v153","v154","v155","v156","v157","v158","v159","v160","v161","v162","v163","v164","v165","v166","v167","v168","v169","v170","v171","v172","v173","v174","v175","v176","v177","v178","v179","v180","v181","v182","v183","v184","v185","v186","v187","v188","v189","v190","v191","v192","v193","v194","v195","v196","v197","v198","v199","v200","v201","v202","v203","v204","v205","v206","v207","v208","v209","v210","v211","v212","v213","v214","v215","v216","v217","v218","v219","v220","v221","v222","v223","v224","v225","v226","v227","v228","v229","v230","v231","v232","v233","v234","v235","v236","v237","v238","v239","v240","v241","v242","v243","v244","v245","v246","v247","v248","v249","v250","v251","v252","v253","v254","v255",
"a0","a1","a2","a3","a4","a5","a6","a7","a8","a9","a10","a11","a12","a13","a14","a15","a16","a17","a18","a19","a20","a21","a22","a23","a24","a25","a26","a27","a28","a29","a30","a31","a32","a33","a34","a35","a36","a37","a38","a39","a40","a41","a42","a43","a44","a45","a46","a47","a48","a49","a50","a51","a52","a53","a54","a55","a56","a57","a58","a59","a60","a61","a62","a63","a64","a65","a66","a67","a68","a69","a70","a71","a72","a73","a74","a75","a76","a77","a78","a79","a80","a81","a82","a83","a84","a85","a86","a87","a88","a89","a90","a91","a92","a93","a94","a95","a96","a97","a98","a99","a100","a101","a102","a103","a104","a105","a106","a107","a108","a109","a110","a111","a112","a113","a114","a115","a116","a117","a118","a119","a120","a121","a122","a123","a124","a125","a126","a127","a128","a129","a130","a131","a132","a133","a134","a135","a136","a137","a138","a139","a140","a141","a142","a143","a144","a145","a146","a147","a148","a149","a150","a151","a152","a153","a154","a155","a156","a157","a158","a159","a160","a161","a162","a163","a164","a165","a166","a167","a168","a169","a170","a171","a172","a173","a174","a175","a176","a177","a178","a179","a180","a181","a182","a183","a184","a185","a186","a187","a188","a189","a190","a191","a192","a193","a194","a195","a196","a197","a198","a199","a200","a201","a202","a203","a204","a205","a206","a207","a208","a209","a210","a211","a212","a213","a214","a215","a216","a217","a218","a219","a220","a221","a222","a223","a224","a225","a226","a227","a228","a229","a230","a231","a232","a233","a234","a235","a236","a237","a238","a239","a240","a241","a242","a243","a244","a245","a246","a247","a248","a249","a250","a251","a252","a253","a254","a255",
"s34","s35","s36","s37","s38","s39","s40","s41","s42","s43","s44","s45","s46","s47","s48","s49","s50","s51","s52","s53","s54","s55","s56","s57","s58","s59","s60","s61","s62","s63","s64","s65","s66","s67","s68","s69","s70","s71","s72","s73","s74","s75","s76","s77","s78","s79","s80","s81","s82","s83","s84","s85","s86","s87","s88","s89","s90","s91","s92","s93","s94","s95","s96","s97","s98","s99");
}

extern "C" void kernel_launch(void* const* d_in, const int* in_sizes, int n_in,
                              void* d_out, int out_size, void* d_ws, size_t ws_size,
                              hipStream_t stream) {
  const unsigned short* x0 = (const unsigned short*)d_in[0];
  const unsigned short* ws = (const unsigned short*)d_in[1];
  const unsigned short* bs = (const unsigned short*)d_in[2];
  dcn_cross_kernel<<<256, 256, 0, stream>>>(x0, ws, bs, (unsigned char*)d_ws, (unsigned short*)d_out);
}
```

```cpp
#include <hip/hip_runtime.h>


constexpr int kD = 1024;
constexpr unsigned kWFlagOff = 131072, kWOff = 1u << 20, kWStream = 200 * 8192, kXchOff = 32u << 20, kDbgOff = 224u << 20;

__device__ __forceinline__ unsigned unit_src(unsigned v, unsigned h, unsigned w) {
  const unsigned l = v >> 5, u = v & 31;
  const unsigned split = ((0xff0000ffu >> u) & 1) - 1;
  const unsigned part = l ? u >> 4 : 0u, g = (0xff00ff00u >> u) & 1;
  const unsigned c = u >> (split & 1), jh = (u >> (~split & 2)) & 1;
  const unsigned kc = 8 * (h ^ part) + 2 * ((w + c) & 3) + g;
  return (l * 1024 + 64 * kc) * 2048 + jh * 128;
}

__global__ __launch_bounds__(256, 1)
void dcn_cross_kernel(const unsigned short* __restrict__ x0, const unsigned short* __restrict__ ws, const unsigned short* __restrict__ bs,
                      unsigned char* __restrict__ wsp, unsigned short* __restrict__ out) {
  __shared__ __attribute__((aligned(16))) unsigned char lds[163840];
  const unsigned b = blockIdx.x;
  constexpr unsigned kPx = 0 ? 1u : 8u;
  const unsigned h = 0 ? (b & 1) : ((b >> 3) & 1), rb = 0 ? (b >> 1) : ((b >> 4) * 8 + (b & 7));
  const unsigned w = __builtin_amdgcn_readfirstlane(threadIdx.x >> 6);
  const unsigned col = (512 * h + 128 * w) * 2;
  const unsigned long long wsb = (unsigned long long)wsp;
  const unsigned long long xo = (unsigned long long)(x0 + (size_t)rb * 128 * kD);
  const unsigned long long ob = (unsigned long long)(out + (size_t)rb * 128 * kD) + col;
  const unsigned long long wr = (unsigned long long)ws + col;
  const unsigned long long bb = (unsigned long long)bs;
  const unsigned wo = ((unsigned)wsb > ~(15u << 20) ? 16u << 20 : kWOff) + (h * 4 + w) * kWStream;
  const unsigned xb = kXchOff + ((0u - (unsigned)wsb) & 0x1fffffu);
  const unsigned eo = xb + b * 524288 + w * 32768, ep = xb + (b ^ kPx) * 524288 + w * 32768;
  const unsigned fo = b * 512 + w * 128, fp = (b ^ kPx) * 512 + w * 128;
  const unsigned db = kDbgOff + (b * 4 + w) * 512;
  const unsigned v1 = 24 + (rb >> 1), v2 = 64 + (rb >> 1), hk = rb & 1;
  const unsigned rs1 = v1 < 64 ? __builtin_amdgcn_readfirstlane(unit_src(v1, h, w)) + hk * 65536 : (1 - h) << 20, rd1 = v1 < 64 ? v1 * 8192 + hk * 1024 : 192 * 8192;
  const unsigned rs2 = __builtin_amdgcn_readfirstlane(unit_src(v2, h, w)) + hk * 65536, rn1 = v2 * 8192 + hk * 1024;
  const unsigned wfa = kWFlagOff + (4 * h + w) * 1024, wfo = wfa + rb * 8;
  const unsigned ldsa = (unsigned)(size_t)(__attribute__((address_space(3))) unsigned char*)lds;
#define LO(x) "s"((unsigned)(x))
#define HI(x) "s"((unsigned)((x) >> 32))
  asm volatile(
"L_main_entry:\n"
"v_and_b32 v12, 63, %18\n"
"v_lshrrev_b32 v13, 3, v12\n"
"v_and_b32 v15, 7, v12\n"
"v_lshlrev_b32 v10, 11, v13\n"
"s_andn2_b32 s60, 1024, %17\n"
"s_lshl_b32 s75, s60, 10\n"
"s_mov_b32 s52, %6\n"
"s_and_b32 s53, %7, 0xffff\n"
"s_mov_b32 s54, 12581120\n"
"s_mov_b32 s55, 0x20000\n"
"v_xor_b32 v6, v15, v13\n"
"v_lshl_add_u32 v6, v6, 4, v10\n"
"s_mov_b32 s48, %2\n"
"s_and_b32 s49, %3, 0xffff\n"
"s_mov_b32 s50, 65536\n"
"s_mov_b32 s51, 0x20000\n"
"s_lshl_b32 s66, %16, 16\n"
"s_add_u32 s48, s48, s66\n"
"s_addc_u32 s49, s49, 0\n"
"s_lshl_b32 s72, %16, 12\n"
"s_add_u32 s72, s72, %15\n"
"s_add_u32 m0, s72, 16384\n"
"s_add_u32 s67, s60, 0\n"
"buffer_load_dwordx4 v6, s[48:51], s67 offen offset:0  lds\n"
"s_add_u32 s67, s60, 15360\n"
"buffer_load_dwordx4 v6, s[48:51], s67 offen offset:1024  lds\n"
"s_add_u32 s67, s60, 30720\n"
"buffer_load_dwordx4 v6, s[48:51], s67 offen offset:2048  lds\n"
"s_add_u32 s67, s60, 46080\n"
"buffer_load_dwordx4 v6, s[48:51], s67 offen offset:3072  lds\n"
"s_mul_i32 s73, %16, 40960\n"
"s_add_u32 s64, s73, %15\n"
"v_lshrrev_b32 v11, 2, v12\n"
"v_and_b32 v11, 4, v11\n"
"v_xor_b32 v11, v11, v15\n"
"v_lshl_add_u32 v11, v11, 4, v10\n"
"s_add_u32 m0, s64, 0\n"
"s_add_u32 s67, s75, 0\n"
"buffer_load_dwordx4 v11, s[52:55], s67 offen offset:0 lds\n"
"s_add_u32 s67, s75, 15360\n"
"buffer_load_dwordx4 v11, s[52:55], s67 offen offset:1024 lds\n"
"s_add_u32 s67, s75, 30720\n"
"buffer_load_dwordx4 v11, s[52:55], s67 offen offset:2048 lds\n"
"s_add_u32 s67, s75, 46080\n"
"buffer_load_dwordx4 v11, s[52:55], s67 offen offset:3072 lds\n"
"s_add_u32 m0, s64, 4096\n"
"s_add_u32 s67, s75, 65536\n"
"buffer_load_dwordx4 v11, s[52:55], s67 offen offset:0 lds\n"
"s_add_u32 s67, s75, 80896\n"
"buffer_load_dwordx4 v11, s[52:55], s67 offen offset:1024 lds\n"
"s_add_u32 s67, s75, 96256\n"
"buffer_load_dwordx4 v11, s[52:55], s67 offen offset:2048 lds\n"
"s_add_u32 s67, s75, 111616\n"
"buffer_load_dwordx4 v11, s[52:55], s67 offen offset:3072 lds\n"
"s_mov_b32 s44, %8\n"
"s_and_b32 s45, %9, 0xffff\n"
"s_mov_b32 s46, 12288\n"
"s_mov_b32 s47, 0x20000\n"
"v_lshlrev_b32 v0, 4, v12\n"
"s_mov_b32 s61, %17\n"
"v_lshrrev_b32 v16, 4, v0\n"
"v_and_b32 v16, 0x30, v16\n"
"buffer_load_dwordx2 v[18:19], v16, s[44:47], s61 offen offset:0\n"
"v_lshrrev_b32 v20, 4, v0\n"
"v_and_b32 v20, 0x30, v20\n"
"buffer_load_dwordx2 v[22:23], v20, s[44:47], s61 offen offset:8\n"
"v_lshrrev_b32 v24, 4, v0\n"
"v_and_b32 v24, 0x30, v24\n"
"buffer_load_dwordx2 v[26:27], v24, s[44:47], s61 offen offset:64\n"
"v_lshrrev_b32 v28, 4, v0\n"
"v_and_b32 v28, 0x30, v28\n"
"buffer_load_dwordx2 v[30:31], v28, s[44:47], s61 offen offset:72\n"
"s_getpc_b64 s[96:97]\n"
"L_pc:\n"
"s_lshr_b32 s66, %26, 3\n"
"s_lshl_b32 s66, s66, 2\n"
"s_add_u32 s66, s66, %16\n"
"s_mul_i32 s66, s66, 640\n"
"s_and_b32 s97, s97, 0xffff\n"
"s_mov_b32 s98, L_end-L_pc\n"
"s_mov_b32 s99, 0x20000\n"
"s_min_u32 s66, s66, L_end-L_pc-640\n"
"v_min_u32 v9, 9, v12\n"
"v_lshlrev_b32 v9, 6, v9\n"
"buffer_load_dword a252, v9, s[96:99], s66 offen\n"
"s_lshr_b32 s65, %26, 4\n"
"s_lshl_b32 s65, s65, 2\n"
"s_add_u32 s65, s65, %16\n"
"s_lshl_b32 s65, s65, 11\n"
"s_lshl_b32 s67, %16, 8\n"
"s_sub_u32 s65, s65, s67\n"
"v_and_b32 v14, 1, v13\n"
"v_lshlrev_b32 v14, 17, v14\n"
"v_lshl_add_u32 v14, v15, 7, v14\n"
"s_add_u32 s70, s75, 131072\n"
"s_add_u32 s70, s70, s65\n"
"buffer_load_dword a252, v14, s[52:55], s70 offen\n"
"s_add_u32 m0, s64, 8192\n"
"s_add_u32 s67, s75, 128\n"
"buffer_load_dwordx4 v11, s[52:55], s67 offen offset:0 lds\n"
"s_add_u32 s67, s75, 15488\n"
"buffer_load_dwordx4 v11, s[52:55], s67 offen offset:1024 lds\n"
"s_add_u32 s67, s75, 30848\n"
"buffer_load_dwordx4 v11, s[52:55], s67 offen offset:2048 lds\n"
"s_add_u32 s67, s75, 46208\n"
"buffer_load_dwordx4 v11, s[52:55], s67 offen offset:3072 lds\n"
"s_add_u32 m0, s64, 12288\n"
"s_add_u32 s67, s75, 65664\n"
"buffer_load_dwordx4 v11, s[52:55], s67 offen offset:0 lds\n"
"s_add_u32 s67, s75, 81024\n"
"buffer_load_dwordx4 v11, s[52:55], s67 offen offset:1024 lds\n"
"s_add_u32 s67, s75, 96384\n"
"buffer_load_dwordx4 v11, s[52:55], s67 offen offset:2048 lds\n"
"s_add_u32 s67, s75, 111744\n"
"buffer_load_dwordx4 v11, s[52:55], s67 offen offset:3072 lds\n"
"s_add_u32 m0, s72, 57344\n"
"s_add_u32 s67, s60, 128\n"
"buffer_load_dwordx4 v6, s[48:51], s67 offen offset:0  lds\n"
"s_add_u32 s67, s60, 15488\n"
"buffer_load_dwordx4 v6, s[48:51], s67 offen offset:1024  lds\n"
"s_add_u32 s67, s60, 30848\n"
"buffer_load_dwordx4 v6, s[48:51], s67 offen offset:2048  lds\n"
"s_add_u32 s67, s60, 46208\n"
"buffer_load_dwordx4 v6, s[48:51], s67 offen offset:3072  lds\n"
"s_mov_b32 s56, %0\n"
"s_and_b32 s57, %1, 0xffff\n"
"s_mov_b32 s58, 171966464\n"
"s_mov_b32 s59, 0x20000\n"
"s_mov_b32 s67, 139264\n"
"buffer_load_dwordx2 v[252:253], off, s[56:59], s67 sc1\n"
"v_add_u32 v1, 4096, v0\n"
"s_add_u32 s36, s56, %10\n"
"s_addc_u32 s37, s57, 0\n"
"s_mov_b32 s38, 1572864\n"
"s_mov_b32 s39, 0x20000\n"
"s_add_u32 s34, s36, 200704\n"
"s_mov_b32 s35, s37\n"
"v_and_b32 v13, 15, v12\n"
"v_lshrrev_b32 v10, 4, v12\n"
"v_xor_b32 v15, v15, v10\n"
"v_lshlrev_b32 v7, 7, v13\n"
"v_lshl_add_u32 v7, v15, 4, v7\n"
"v_add_u32 v7, s64, v7\n"
"v_xor_b32 v8, 64, v7\n"
"v_bfe_u32 v13, v12, 2, 2\n"
"v_and_b32 v15, 3, v12\n"
"v_bfe_u32 v9, v12, 3, 1\n"
"v_lshl_add_u32 v15, v9, 2, v15\n"
"v_lshl_add_u32 v13, v10, 3, v13\n"
"v_lshlrev_b32 v13, 7, v13\n"
"v_lshl_add_u32 v9, v15, 4, v13\n"
"v_add_u32 v9, s64, v9\n"
"v_xor_b32 v15, 64, v9\n"
"s_mov_b32 s40, %2\n"
"s_and_b32 s41, %3, 0xffff\n"
"s_mov_b32 s42, 260352\n"
"s_mov_b32 s43, 0x20000\n"
"s_add_u32 s40, s40, %17\n"
"s_addc_u32 s41, s41, 0\n"
"s_mov_b32 s80, 0\n"
"s_mov_b32 s81, 15360\n"
"s_mov_b32 s82, 30720\n"
"s_mov_b32 s83, 46080\n"
"s_mov_b32 s84, 65536\n"
"s_mov_b32 s85, 80896\n"
"s_mov_b32 s86, 96256\n"
"s_mov_b32 s87, 111616\n"
"s_mov_b32 s88, 131072\n"
"s_mov_b32 s89, 146432\n"
"s_mov_b32 s90, 161792\n"
"s_mov_b32 s91, 177152\n"
"s_mov_b32 s92, 196608\n"
"s_mov_b32 s93, 211968\n"
"s_mov_b32 s94, 227328\n"
"s_mov_b32 s95, 242688\n"
"s_mov_b32 s62, 0\n"
"s_mov_b32 s63, 0xffff0000\n"
"s_mov_b32 s68, 0x8000\n"
"s_add_u32 s66, %16, 0\n"
"s_and_b32 s66, s66, 3\n"
"s_mul_i32 s66, s66, 40960\n"
"s_add_u32 s66, s66, %15\n"
"v_add_u32 v2, s66, v0\n"
"s_add_u32 s66, %16, 1\n"
"s_and_b32 s66, s66, 3\n"
"s_mul_i32 s66, s66, 40960\n"
"s_add_u32 s66, s66, %15\n"
"v_add_u32 v3, s66, v0\n"
"s_add_u32 s66, %16, 2\n"
"s_and_b32 s66, s66, 3\n"
"s_mul_i32 s66, s66, 40960\n"
"s_add_u32 s66, s66, %15\n"
"v_add_u32 v4, s66, v0\n"
"s_add_u32 s66, %16, 3\n"
"s_and_b32 s66, s66, 3\n"
"s_mul_i32 s66, s66, 40960\n"
"s_add_u32 s66, s66, %15\n"
"v_add_u32 v5, s66, v0\n"
"s_sub_u32 s66, 0, s73\n"
"v_add_u32 v12, s66, v7\n"
"v_add_u32 v13, s66, v8\n"
"s_waitcnt vmcnt(27)\n"
"s_barrier\n"
"ds_read_b128 a[100:103], v13 offset:16384\n"
"ds_read_b128 a[96:99], v12 offset:16384\n"
"ds_read_b128 a[108:111], v13 offset:18432\n"
"ds_read_b128 a[104:107], v12 offset:18432\n"
"ds_read_b128 a[116:119], v13 offset:20480\n"
"ds_read_b128 a[112:115], v12 offset:20480\n"
"ds_read_b128 a[124:127], v13 offset:22528\n"
"ds_read_b128 a[120:123], v12 offset:22528\n"
"ds_read_b128 a[132:135], v13 offset:24576\n"
"ds_read_b128 a[128:131], v12 offset:24576\n"
"ds_read_b128 a[140:143], v13 offset:26624\n"
"ds_read_b128 a[136:139], v12 offset:26624\n"
"ds_read_b128 a[148:151], v13 offset:28672\n"
"ds_read_b128 a[144:147], v12 offset:28672\n"
"ds_read_b128 a[156:159], v13 offset:30720\n"
"ds_read_b128 a[152:155], v12 offset:30720\n"
"s_waitcnt vmcnt(19)\n"
"ds_read_b64_tr_b16 a[32:33], v9 offset:0\n"
"ds_read_b64_tr_b16 a[34:35], v9 offset:512\n"
"ds_read_b64_tr_b16 a[36:37], v9 offset:4096\n"
"ds_read_b64_tr_b16 a[38:39], v9 offset:4608\n"
"ds_read_b64_tr_b16 a[40:41], v9 offset:8\n"
"ds_read_b64_tr_b16 a[42:43], v9 offset:520\n"
"ds_read_b64_tr_b16 a[44:45], v9 offset:4104\n"
"ds_read_b64_tr_b16 a[46:47], v9 offset:4616\n"
"ds_read_b64_tr_b16 a[48:49], v15 offset:0\n"
"ds_read_b64_tr_b16 a[50:51], v15 offset:512\n"
"ds_read_b64_tr_b16 a[52:53], v15 offset:4096\n"
"ds_read_b64_tr_b16 a[54:55], v15 offset:4608\n"
"ds_read_b64_tr_b16 a[56:57], v15 offset:8\n"
"ds_read_b64_tr_b16 a[58:59], v15 offset:520\n"
"ds_read_b64_tr_b16 a[60:61], v15 offset:4104\n"
"ds_read_b64_tr_b16 a[62:63], v15 offset:4616\n"
"s_waitcnt vmcnt(15)\n"
"v_lshlrev_b32 v16, 16, v18\n"
"v_and_b32 v17, s63, v18\n"
"v_lshlrev_b32 v18, 16, v19\n"
"v_and_b32 v19, s63, v19\n"
"v_add_f32 v16, 1.0, v16\n"
"v_add_f32 v17, 1.0, v17\n"
"v_add_f32 v18, 1.0, v18\n"
"v_add_f32 v19, 1.0, v19\n"
"v_lshlrev_b32 v20, 16, v22\n"
"v_and_b32 v21, s63, v22\n"
"v_lshlrev_b32 v22, 16, v23\n"
"v_and_b32 v23, s63, v23\n"
"v_add_f32 v20, 1.0, v20\n"
"v_add_f32 v21, 1.0, v21\n"
"v_add_f32 v22, 1.0, v22\n"
"v_add_f32 v23, 1.0, v23\n"
"v_lshlrev_b32 v24, 16, v26\n"
"v_and_b32 v25, s63, v26\n"
"v_lshlrev_b32 v26, 16, v27\n"
"v_and_b32 v27, s63, v27\n"
"v_add_f32 v24, 1.0, v24\n"
"v_add_f32 v25, 1.0, v25\n"
"v_add_f32 v26, 1.0, v26\n"
"v_add_f32 v27, 1.0, v27\n"
"v_lshlrev_b32 v28, 16, v30\n"
"v_and_b32 v29, s63, v30\n"
"v_lshlrev_b32 v30, 16, v31\n"
"v_and_b32 v31, s63, v31\n"
"v_add_f32 v28, 1.0, v28\n"
"v_add_f32 v29, 1.0, v29\n"
"v_add_f32 v30, 1.0, v30\n"
"v_add_f32 v31, 1.0, v31\n"
"v_accvgpr_write_b32 a0, v16\n"
"v_accvgpr_write_b32 a1, v17\n"
"v_accvgpr_write_b32 a2, v18\n"
"v_accvgpr_write_b32 a3, v19\n"
"v_accvgpr_write_b32 a4, v20\n"
"v_accvgpr_write_b32 a5, v21\n"
"v_accvgpr_write_b32 a6, v22\n"
"v_accvgpr_write_b32 a7, v23\n"
"v_accvgpr_write_b32 a8, v24\n"
"v_accvgpr_write_b32 a9, v25\n"
"v_accvgpr_write_b32 a10, v26\n"
"v_accvgpr_write_b32 a11, v27\n"
"v_accvgpr_write_b32 a12, v28\n"
"v_accvgpr_write_b32 a13, v29\n"
"v_accvgpr_write_b32 a14, v30\n"
"v_accvgpr_write_b32 a15, v31\n"
"s_nop 1\n"
"s_waitcnt lgkmcnt(8)\n"
"v_mfma_f32_16x16x32_bf16 v[32:35], a[32:35], a[96:99], v[16:19]\n"
"s_add_u32 s70, s75, 393216\n"
"v_mfma_f32_16x16x32_bf16 v[32:35], a[36:39], a[100:103], v[32:35]\n"
"s_add_u32 s70, s70, s65\n"
"v_mfma_f32_16x16x32_bf16 v[60:63], a[44:47], a[100:103], v[20:23]\n"
"buffer_load_dword a252, v14, s[52:55], s70 offen\n"
"v_mfma_f32_16x16x32_bf16 v[60:63], a[40:43], a[96:99], v[60:63]\n"
"s_add_u32 m0, s64, 32768\n"
"v_mfma_f32_16x16x32_bf16 v[64:67], a[40:43], a[104:107], v[20:23]\n"
"s_add_u32 s67, s75, 131072\n"
"v_mfma_f32_16x16x32_bf16 v[64:67], a[44:47], a[108:111], v[64:67]\n"
"buffer_load_dwordx4 v11, s[52:55], s67 offen offset:0 lds\n"
"v_mfma_f32_16x16x32_bf16 v[36:39], a[36:39], a[108:111], v[16:19]\n"
"s_add_u32 s67, s75, 146432\n"
"v_mfma_f32_16x16x32_bf16 v[36:39], a[32:35], a[104:107], v[36:39]\n"
"v_mfma_f32_16x16x32_bf16 v[40:43], a[32:35], a[112:115], v[16:19]\n"
"s_waitcnt lgkmcnt(0)\n"
"buffer_load_dwordx4 v11, s[52:55], s67 offen offset:1024 lds\n"
"v_mfma_f32_16x16x32_bf16 v[40:43], a[36:39], a[116:119], v[40:43]\n"
"v_mfma_f32_16x16x32_bf16 v[68:71], a[44:47], a[116:119], v[20:23]\n"
"s_add_u32 s67, s75, 161792\n"
"v_mfma_f32_16x16x32_bf16 v[68:71], a[40:43], a[112:115], v[68:71]\n"
"v_mfma_f32_16x16x32_bf16 v[72:75], a[40:43], a[120:123], v[20:23]\n"
"buffer_load_dwordx4 v11, s[52:55], s67 offen offset:2048 lds\n"
"v_mfma_f32_16x16x32_bf16 v[72:75], a[44:47], a[124:127], v[72:75]\n"
"v_mfma_f32_16x16x32_bf16 v[44:47], a[36:39], a[124:127], v[16:19]\n"
"s_add_u32 s67, s75, 177152\n"
"v_mfma_f32_16x16x32_bf16 v[44:47], a[32:35], a[120:123], v[44:47]\n"
"v_mfma_f32_16x16x32_bf16 v[48:51], a[32:35], a[128:131], v[16:19]\n"
"buffer_load_dwordx4 v11, s[52:55], s67 offen offset:3072 lds\n"
"v_mfma_f32_16x16x32_bf16 v[48:51], a[36:39], a[132:135], v[48:51]\n"
"v_mfma_f32_16x16x32_bf16 v[76:79], a[44:47], a[132:135], v[20:23]\n"
"s_add_u32 m0, s64, 36864\n"
"v_mfma_f32_16x16x32_bf16 v[76:79], a[40:43], a[128:131], v[76:79]\n"
"v_mfma_f32_16x16x32_bf16 v[80:83], a[40:43], a[136:139], v[20:23]\n"
"s_add_u32 s67, s75, 196608\n"
"v_mfma_f32_16x16x32_bf16 v[80:83], a[44:47], a[140:143], v[80:83]\n"
"v_mfma_f32_16x16x32_bf16 v[52:55], a[36:39], a[140:143], v[16:19]\n"
"buffer_load_dwordx4 v11, s[52:55], s67 offen offset:0 lds\n"
"v_mfma_f32_16x16x32_bf16 v[52:55], a[32:35], a[136:139], v[52:55]\n"
"v_mfma_f32_16x16x32_bf16 v[56:59], a[32:35], a[144:147], v[16:19]\n"
"s_add_u32 s67, s75, 211968\n"
"v_mfma_f32_16x16x32_bf16 v[56:59], a[36:39], a[148:151], v[56:59]\n"
"v_mfma_f32_16x16x32_bf16 v[84:87], a[44:47], a[148:151], v[20:23]\n"
"buffer_load_dwordx4 v11, s[52:55], s67 offen offset:1024 lds\n"
"v_mfma_f32_16x16x32_bf16 v[84:87], a[40:43], a[144:147], v[84:87]\n"
"v_mfma_f32_16x16x32_bf16 a[4:7], a[40:43], a[152:155], a[4:7]\n"
"s_add_u32 s67, s75, 227328\n"
"v_mfma_f32_16x16x32_bf16 a[4:7], a[44:47], a[156:159], a[4:7]\n"
"v_mfma_f32_16x16x32_bf16 a[0:3], a[36:39], a[156:159], a[0:3]\n"
"buffer_load_dwordx4 v11, s[52:55], s67 offen offset:2048 lds\n"
"v_mfma_f32_16x16x32_bf16 a[0:3], a[32:35], a[152:155], a[0:3]\n"
"v_mfma_f32_16x16x32_bf16 v[88:91], a[48:51], a[96:99], v[24:27]\n"
"s_add_u32 s67, s75, 242688\n"
"v_mfma_f32_16x16x32_bf16 v[88:91], a[52:55], a[100:103], v[88:91]\n"
"v_mfma_f32_16x16x32_bf16 v[116:119], a[60:63], a[100:103], v[28:31]\n"
"v_mfma_f32_16x16x32_bf16 v[116:119], a[56:59], a[96:99], v[116:119]\n"
"buffer_load_dwordx4 v11, s[52:55], s67 offen offset:3072 lds\n"
"v_mfma_f32_16x16x32_bf16 v[120:123], a[56:59], a[104:107], v[28:31]\n"
"s_add_u32 m0, s64, 0\n"
"v_mfma_f32_16x16x32_bf16 v[120:123], a[60:63], a[108:111], v[120:123]\n"
"v_mfma_f32_16x16x32_bf16 v[92:95], a[52:55], a[108:111], v[24:27]\n"
"s_add_u32 s67, s75, 131200\n"
"v_mfma_f32_16x16x32_bf16 v[92:95], a[48:51], a[104:107], v[92:95]\n"
"v_mfma_f32_16x16x32_bf16 v[96:99], a[48:51], a[112:115], v[24:27]\n"
"buffer_load_dwordx4 v11, s[52:55], s67 offen offset:0 lds\n"
"v_mfma_f32_16x16x32_bf16 v[96:99], a[52:55], a[116:119], v[96:99]\n"
"v_mfma_f32_16x16x32_bf16 v[124:127], a[60:63], a[116:119], v[28:31]\n"
"s_add_u32 s67, s75, 146560\n"
"v_mfma_f32_16x16x32_bf16 v[124:127], a[56:59], a[112:115], v[124:127]\n"
"buffer_load_dwordx4 v11, s[52:55], s67 offen offset:1024 lds\n"
"v_mfma_f32_16x16x32_bf16 v[128:131], a[56:59], a[120:123], v[28:31]\n"
"s_waitcnt vmcnt(16)\n"
"ds_read_b64_tr_b16 a[64:65], v9 offset:8192\n"
"v_mfma_f32_16x16x32_bf16 v[128:131], a[60:63], a[124:127], v[128:131]\n"
"s_add_u32 s67, s75, 161920\n"
"ds_read_b64_tr_b16 a[66:67], v9 offset:8704\n"
"v_mfma_f32_16x16x32_bf16 v[100:103], a[52:55], a[124:127], v[24:27]\n"
"ds_read_b64_tr_b16 a[68:69], v9 offset:12288\n"
"v_mfma_f32_16x16x32_bf16 v[100:103], a[48:51], a[120:123], v[100:103]\n"
"ds_read_b64_tr_b16 a[70:71], v9 offset:12800\n"
"v_mfma_f32_16x16x32_bf16 v[104:107], a[48:51], a[128:131], v[24:27]\n"
"ds_read_b64_tr_b16 a[72:73], v9 offset:8200\n"
"v_mfma_f32_16x16x32_bf16 v[104:107], a[52:55], a[132:135], v[104:107]\n"
"ds_read_b64_tr_b16 a[74:75], v9 offset:8712\n"
"buffer_load_dwordx4 v11, s[52:55], s67 offen offset:2048 lds\n"
"v_mfma_f32_16x16x32_bf16 v[132:135], a[60:63], a[132:135], v[28:31]\n"
"ds_read_b64_tr_b16 a[76:77], v9 offset:12296\n"
"v_mfma_f32_16x16x32_bf16 v[132:135], a[56:59], a[128:131], v[132:135]\n"
"s_add_u32 s67, s75, 177280\n"
"ds_read_b64_tr_b16 a[78:79], v9 offset:12808\n"
"v_mfma_f32_16x16x32_bf16 v[136:139], a[56:59], a[136:139], v[28:31]\n"
"buffer_load_dwordx4 v11, s[52:55], s67 offen offset:3072 lds\n"
"v_mfma_f32_16x16x32_bf16 v[136:139], a[60:63], a[140:143], v[136:139]\n"
"ds_read_b64_tr_b16 a[80:81], v15 offset:8192\n"
"v_mfma_f32_16x16x32_bf16 v[108:111], a[52:55], a[140:143], v[24:27]\n"
"s_add_u32 m0, s64, 4096\n"
"ds_read_b64_tr_b16 a[82:83], v15 offset:8704\n"
"v_mfma_f32_16x16x32_bf16 v[108:111], a[48:51], a[136:139], v[108:111]\n"
"ds_read_b64_tr_b16 a[84:85], v15 offset:12288\n"
"v_mfma_f32_16x16x32_bf16 v[112:115], a[48:51], a[144:147], v[24:27]\n"
"s_add_u32 s67, s75, 196736\n"
"ds_read_b64_tr_b16 a[86:87], v15 offset:12800\n"
"v_mfma_f32_16x16x32_bf16 v[112:115], a[52:55], a[148:151], v[112:115]\n"
"ds_read_b64_tr_b16 a[88:89], v15 offset:8200\n"
"v_mfma_f32_16x16x32_bf16 v[140:143], a[60:63], a[148:151], v[28:31]\n"
"ds_read_b64_tr_b16 a[90:91], v15 offset:8712\n"
"buffer_load_dwordx4 v11, s[52:55], s67 offen offset:0 lds\n"
"v_mfma_f32_16x16x32_bf16 v[140:143], a[56:59], a[144:147], v[140:143]\n"
"ds_read_b64_tr_b16 a[92:93], v15 offset:12296\n"
"v_mfma_f32_16x16x32_bf16 a[12:15], a[56:59], a[152:155], a[12:15]\n"
"s_add_u32 s67, s75, 212096\n"
"ds_read_b64_tr_b16 a[94:95], v15 offset:12808\n"
"v_mfma_f32_16x16x32_bf16 a[12:15], a[60:63], a[156:159], a[12:15]\n"
"v_mfma_f32_16x16x32_bf16 a[8:11], a[52:55], a[156:159], a[8:11]\n"
"buffer_load_dwordx4 v11, s[52:55], s67 offen offset:1024 lds\n"
"v_mfma_f32_16x16x32_bf16 a[8:11], a[48:51], a[152:155], a[8:11]\n"
"s_waitcnt lgkmcnt(8)\n"
"v_mfma_f32_16x16x32_bf16 v[144:147], a[64:67], a[96:99], 1.0\n"
"s_add_u32 s67, s75, 227456\n"
"v_mfma_f32_16x16x32_bf16 v[144:147], a[68:71], a[100:103], v[144:147]\n"
"v_mfma_f32_16x16x32_bf16 v[172:175], a[76:79], a[100:103], 1.0\n"
"s_waitcnt vmcnt(15) lgkmcnt(0)\n"
"v_readfirstlane_b32 s78, v252\n"
"v_readfirstlane_b32 s79, v253\n"
"v_mfma_f32_16x16x32_bf16 v[172:175], a[72:75], a[96:99], v[172:175]\n"
"buffer_load_dwordx4 v11, s[52:55], s67 offen offset:2048 lds\n"
"v_mfma_f32_16x16x32_bf16 v[176:179], a[72:75], a[104:107], 1.0\n"
"v_mfma_f32_16x16x32_bf16 v[176:179], a[76:79], a[108:111], v[176:179]\n"
"s_add_u32 s67, s75, 242816\n"
"v_mfma_f32_16x16x32_bf16 v[148:151], a[68:71], a[108:111], 1.0\n"
"s_add_u32 s78, s78, 16\n"
"v_mfma_f32_16x16x32_bf16 v[148:151], a[64:67], a[104:107], v[148:151]\n"
"s_add_u32 s79, s79, 16\n"
"v_mfma_f32_16x16x32_bf16 v[152:155], a[64:67], a[112:115], 1.0\n"
"s_sub_u32 s66, 40960, s73\n"
"v_mfma_f32_16x16x32_bf16 v[152:155], a[68:71], a[116:119], v[152:155]\n"
"buffer_load_dwordx4 v11, s[52:55], s67 offen offset:3072 lds\n"
"v_mfma_f32_16x16x32_bf16 v[180:183], a[76:79], a[116:119], 1.0\n"
"s_add_u32 m0, s72, 98304\n"
"v_add_u32 v12, s66, v7\n"
"v_mfma_f32_16x16x32_bf16 v[180:183], a[72:75], a[112:115], v[180:183]\n"
"v_mfma_f32_16x16x32_bf16 v[184:187], a[72:75], a[120:123], 1.0\n"
"s_add_u32 s67, s60, 256\n"
"v_add_u32 v13, s66, v8\n"
"v_mfma_f32_16x16x32_bf16 v[184:187], a[76:79], a[124:127], v[184:187]\n"
"buffer_load_dwordx4 v6, s[48:51], s67 offen offset:0  lds\n"
"v_mfma_f32_16x16x32_bf16 v[156:159], a[68:71], a[124:127], 1.0\n"
"v_mfma_f32_16x16x32_bf16 v[156:159], a[64:67], a[120:123], v[156:159]\n"
"s_add_u32 s67, s60, 15616\n"
"v_mfma_f32_16x16x32_bf16 v[160:163], a[64:67], a[128:131], 1.0\n"
"v_mfma_f32_16x16x32_bf16 v[160:163], a[68:71], a[132:135], v[160:163]\n"
"buffer_load_dwordx4 v6, s[48:51], s67 offen offset:1024  lds\n"
"v_mfma_f32_16x16x32_bf16 v[188:191], a[76:79], a[132:135], 1.0\n"
"v_mfma_f32_16x16x32_bf16 v[188:191], a[72:75], a[128:131], v[188:191]\n"
"s_add_u32 s67, s60, 30976\n"
"v_mfma_f32_16x16x32_bf16 v[192:195], a[72:75], a[136:139], 1.0\n"
"v_mfma_f32_16x16x32_bf16 v[192:195], a[76:79], a[140:143], v[192:195]\n"
"buffer_load_dwordx4 v6, s[48:51], s67 offen offset:2048  lds\n"
"v_mfma_f32_16x16x32_bf16 v[164:167], a[68:71], a[140:143], 1.0\n"
"v_mfma_f32_16x16x32_bf16 v[164:167], a[64:67], a[136:139], v[164:167]\n"
"s_add_u32 s67, s60, 46336\n"
"v_mfma_f32_16x16x32_bf16 v[168:171], a[64:67], a[144:147], 1.0\n"
"v_mfma_f32_16x16x32_bf16 v[168:171], a[68:71], a[148:151], v[168:171]\n"
"v_mfma_f32_16x16x32_bf16 v[196:199], a[76:79], a[148:151], 1.0\n"
"buffer_load_dwordx4 v6, s[48:51], s67 offen offset:3072  lds\n"
"v_mfma_f32_16x16x32_bf16 v[196:199], a[72:75], a[144:147], v[196:199]\n"
"s_add_u32 m0, s72, 139264\n"
"v_mfma_f32_16x16x32_bf16 a[20:23], a[72:75], a[152:155], 1.0\n"
"v_mfma_f32_16x16x32_bf16 a[20:23], a[76:79], a[156:159], a[20:23]\n"
"s_add_u32 s67, %20, 0\n"
"v_mfma_f32_16x16x32_bf16 a[16:19], a[68:71], a[156:159], 1.0\n"
"buffer_load_dwordx4 v11, s[52:55], s67 offen offset:0 nt lds\n"
"v_mfma_f32_16x16x32_bf16 a[16:19], a[64:67], a[152:155], a[16:19]\n"
"s_barrier\n"
"s_waitcnt vmcnt(13)\n"
"ds_read_b64_tr_b16 a[32:33], v9 offset:32768\n"
"v_mfma_f32_16x16x32_bf16 v[200:203], a[80:83], a[96:99], 1.0\n"
"s_add_u32 s67, %20, 15360\n"
"ds_read_b64_tr_b16 a[34:35], v9 offset:33280\n"
"v_mfma_f32_16x16x32_bf16 v[200:203], a[84:87], a[100:103], v[200:203]\n"
"ds_read_b64_tr_b16 a[36:37], v9 offset:36864\n"
"v_mfma_f32_16x16x32_bf16 v[228:231], a[92:95], a[100:103], 1.0\n"
"ds_read_b128 a[100:103], v13 offset:16384\n"
"v_mfma_f32_16x16x32_bf16 v[228:231], a[88:91], a[96:99], v[228:231]\n"
"ds_read_b128 a[96:99], v12 offset:16384\n"
"buffer_load_dwordx4 v11, s[52:55], s67 offen offset:1024 nt lds\n"
"v_mfma_f32_16x16x32_bf16 v[232:235], a[88:91], a[104:107], 1.0\n"
"ds_read_b64_tr_b16 a[38:39], v9 offset:37376\n"
"v_mfma_f32_16x16x32_bf16 v[232:235], a[92:95], a[108:111], v[232:235]\n"
"s_add_u32 s67, %20, 30720\n"
"ds_read_b64_tr_b16 a[40:41], v9 offset:32776\n"
"v_mfma_f32_16x16x32_bf16 v[204:207], a[84:87], a[108:111], 1.0\n"
"ds_read_b128 a[108:111], v13 offset:18432\n"
"v_mfma_f32_16x16x32_bf16 v[204:207], a[80:83], a[104:107], v[204:207]\n"
"ds_read_b128 a[104:107], v12 offset:18432\n"
"v_mfma_f32_16x16x32_bf16 v[208:211], a[80:83], a[112:115], 1.0\n"
"ds_read_b64_tr_b16 a[42:43], v9 offset:33288\n"
"buffer_load_dwordx4 v11, s[52:55], s67 offen offset:2048 nt lds\n"
"v_mfma_f32_16x16x32_bf16 v[208:211], a[84:87], a[116:119], v[208:211]\n"
"ds_read_b64_tr_b16 a[44:45], v9 offset:36872\n"
"v_mfma_f32_16x16x32_bf16 v[236:239], a[92:95], a[116:119], 1.0\n"
"s_add_u32 s67, %20, 46080\n"
"ds_read_b128 a[116:119], v13 offset:20480\n"
"v_mfma_f32_16x16x32_bf16 v[236:239], a[88:91], a[112:115], v[236:239]\n"
"ds_read_b128 a[112:115], v12 offset:20480\n"
"v_mfma_f32_16x16x32_bf16 v[240:243], a[88:91], a[120:123], 1.0\n"
"ds_read_b64_tr_b16 a[46:47], v9 offset:37384\n"
"buffer_load_dwordx4 v11, s[52:55], s67 offen offset:3072 nt lds\n"
"v_mfma_f32_16x16x32_bf16 v[240:243], a[92:95], a[124:127], v[240:243]\n"
"ds_read_b64_tr_b16 a[48:49], v15 offset:32768\n"
"v_mfma_f32_16x16x32_bf16 v[212:215], a[84:87], a[124:127], 1.0\n"
"ds_read_b128 a[124:127], v13 offset:22528\n"
"v_mfma_f32_16x16x32_bf16 v[212:215], a[80:83], a[120:123], v[212:215]\n"
"ds_read_b128 a[120:123], v12 offset:22528\n"
"v_mfma_f32_16x16x32_bf16 v[216:219], a[80:83], a[128:131], 1.0\n"
"s_add_u32 m0, s64, 8192\n"
"ds_read_b64_tr_b16 a[50:51], v15 offset:33280\n"
"v_mfma_f32_16x16x32_bf16 v[216:219], a[84:87], a[132:135], v[216:219]\n"
"ds_read_b64_tr_b16 a[52:53], v15 offset:36864\n"
"v_mfma_f32_16x16x32_bf16 v[244:247], a[92:95], a[132:135], 1.0\n"
"ds_read_b128 a[132:135], v13 offset:24576\n"
"v_mfma_f32_16x16x32_bf16 v[244:247], a[88:91], a[128:131], v[244:247]\n"
"ds_read_b128 a[128:131], v12 offset:24576\n"
"v_mfma_f32_16x16x32_bf16 v[248:251], a[88:91], a[136:139], 1.0\n"
"ds_read_b64_tr_b16 a[54:55], v15 offset:37376\n"
"v_mfma_f32_16x16x32_bf16 v[248:251], a[92:95], a[140:143], v[248:251]\n"
"ds_read_b64_tr_b16 a[56:57], v15 offset:32776\n"
"v_mfma_f32_16x16x32_bf16 v[220:223], a[84:87], a[140:143], 1.0\n"
"ds_read_b128 a[140:143], v13 offset:26624\n"
"v_mfma_f32_16x16x32_bf16 v[220:223], a[80:83], a[136:139], v[220:223]\n"
"s_add_u32 s67, s75, 262144\n"
"ds_read_b128 a[136:139], v12 offset:26624\n"
"v_mfma_f32_16x16x32_bf16 v[224:227], a[80:83], a[144:147], 1.0\n"
"ds_read_b64_tr_b16 a[58:59], v15 offset:33288\n"
"v_mfma_f32_16x16x32_bf16 v[224:227], a[84:87], a[148:151], v[224:227]\n"
"ds_read_b64_tr_b16 a[60:61], v15 offset:36872\n"
"v_mfma_f32_16x16x32_bf16 v[252:255], a[92:95], a[148:151], 1.0\n"
"ds_read_b128 a[148:151], v13 offset:28672\n"
"v_mfma_f32_16x16x32_bf16 v[252:255], a[88:91], a[144:147], v[252:255]\n"
"ds_read_b128 a[144:147], v12 offset:28672\n"
"ds_read_b64_tr_b16 a[62:63], v15 offset:37384\n"
"v_mfma_f32_16x16x32_bf16 a[28:31], a[88:91], a[152:155], 1.0\n"
"v_mfma_f32_16x16x32_bf16 a[28:31], a[92:95], a[156:159], a[28:31]\n"
"v_mfma_f32_16x16x32_bf16 a[24:27], a[84:87], a[156:159], 1.0\n"
"ds_read_b128 a[156:159], v13 offset:30720\n"
"v_mfma_f32_16x16x32_bf16 a[24:27], a[80:83], a[152:155], a[24:27]\n"
"s_waitcnt lgkmcnt(15)\n"
"ds_read_b128 a[152:155], v12 offset:30720\n"
"v_mfma_f32_16x16x32_bf16 v[32:35], a[32:35], a[96:99], v[32:35]\n"
"buffer_load_dwordx4 v11, s[52:55], s67 offen offset:0 lds\n"
"v_mfma_f32_16x16x32_bf16 v[32:35], a[36:39], a[100:103], v[32:35]\n"
"s_waitcnt lgkmcnt(2)\n"
"v_mfma_f32_16x16x32_bf16 v[60:63], a[44:47], a[100:103], v[60:63]\n"
"s_add_u32 s67, s75, 277504\n"
"v_mfma_f32_16x16x32_bf16 v[60:63], a[40:43], a[96:99], v[60:63]\n"
"v_mfma_f32_16x16x32_bf16 v[64:67], a[40:43], a[104:107], v[64:67]\n"
"buffer_load_dwordx4 v11, s[52:55], s67 offen offset:1024 lds\n"
"v_mfma_f32_16x16x32_bf16 v[64:67], a[44:47], a[108:111], v[64:67]\n"
"v_mfma_f32_16x16x32_bf16 v[36:39], a[36:39], a[108:111], v[36:39]\n"
"s_add_u32 s67, s75, 292864\n"
"v_mfma_f32_16x16x32_bf16 v[36:39], a[32:35], a[104:107], v[36:39]\n"
"v_mfma_f32_16x16x32_bf16 v[40:43], a[32:35], a[112:115], v[40:43]\n"
"v_mfma_f32_16x16x32_bf16 v[40:43], a[36:39], a[116:119], v[40:43]\n"
"buffer_load_dwordx4 v11, s[52:55], s67 offen offset:2048 lds\n"
"v_mfma_f32_16x16x32_bf16 v[68:71], a[44:47], a[116:119], v[68:71]\n"
"v_mfma_f32_16x16x32_bf16 v[68:71], a[40:43], a[112:115], v[68:71]\n"
"s_add_u32 s67, s75, 308224\n"
"v_mfma_f32_16x16x32_bf16 v[72:75], a[40:43], a[120:123], v[72:75]\n"
"v_mfma_f32_16x16x32_bf16 v[72:75], a[44:47], a[124:127], v[72:75]\n"
"v_mfma_f32_16x16x32_bf16 v[44:47], a[36:39], a[124:127], v[44:47]\n"
"v_mov_b32 v16, -1\n"
"v_mfma_f32_16x16x32_bf16 v[44:47], a[32:35], a[120:123], v[44:47]\n"
"buffer_load_dwordx4 v11, s[52:55], s67 offen offset:3072 lds\n"
"v_mfma_f32_16x16x32_bf16 v[48:51], a[32:35], a[128:131], v[48:51]\n"
"s_add_u32 m0, s64, 12288\n"
"v_mfma_f32_16x16x32_bf16 v[48:51], a[36:39], a[132:135], v[48:51]\n"
"s_add_u32 s67, s75, 327680\n"
"v_lshrrev_b32 v17, 2, v0\n"
"v_mfma_f32_16x16x32_bf16 v[76:79], a[44:47], a[132:135], v[76:79]\n"
"s_waitcnt vmcnt(12)\n"
"ds_read_b64_tr_b16 a[64:65], v9 offset:0\n"
"v_mfma_f32_16x16x32_bf16 v[76:79], a[40:43], a[128:131], v[76:79]\n"
"ds_read_b64_tr_b16 a[66:67], v9 offset:512\n"
"buffer_load_dwordx4 v11, s[52:55], s67 offen offset:0 lds\n"
"v_mfma_f32_16x16x32_bf16 v[80:83], a[40:43], a[136:139], v[80:83]\n"
"ds_read_b64_tr_b16 a[68:69], v9 offset:4096\n"
"v_mfma_f32_16x16x32_bf16 v[80:83], a[44:47], a[140:143], v[80:83]\n"
"s_add_u32 s67, s75, 343040\n"
"ds_read_b64_tr_b16 a[70:71], v9 offset:4608\n"
"v_mfma_f32_16x16x32_bf16 v[52:55], a[36:39], a[140:143], v[52:55]\n"
"ds_read_b64_tr_b16 a[72:73], v9 offset:8\n"
"v_mfma_f32_16x16x32_bf16 v[52:55], a[32:35], a[136:139], v[52:55]\n"
"s_waitcnt lgkmcnt(5)\n"
"ds_read_b64_tr_b16 a[74:75], v9 offset:520\n"
"buffer_load_dwordx4 v11, s[52:55], s67 offen offset:1024 lds\n"
"v_mfma_f32_16x16x32_bf16 v[56:59], a[32:35], a[144:147], v[56:59]\n"
"s_add_u32 s67, s75, 358400\n"
"ds_read_b64_tr_b16 a[76:77], v9 offset:4104\n"
"v_mfma_f32_16x16x32_bf16 v[56:59], a[36:39], a[148:151], v[56:59]\n"
"s_add_u32 s69, %11, 8192\n"
"ds_read_b64_tr_b16 a[78:79], v9 offset:4616\n"
"v_mfma_f32_16x16x32_bf16 v[84:87], a[44:47], a[148:151], v[84:87]\n"
"buffer_load_dwordx4 v11, s[52:55], s67 offen offset:2048 lds\n"
"v_mfma_f32_16x16x32_bf16 v[84:87], a[40:43], a[144:147], v[84:87]\n"
"ds_read_b64_tr_b16 a[80:81], v15 offset:0\n"
"v_mfma_f32_16x16x32_bf16 a[4:7], a[40:43], a[152:155], a[4:7]\n"
"s_add_u32 s67, s75, 373760\n"
"ds_read_b64_tr_b16 a[82:83], v15 offset:512\n"
"v_mfma_f32_16x16x32_bf16 a[4:7], a[44:47], a[156:159], a[4:7]\n"
"ds_read_b64_tr_b16 a[84:85], v15 offset:4096\n"
"v_mfma_f32_16x16x32_bf16 a[0:3], a[36:39], a[156:159], a[0:3]\n"
"ds_read_b64_tr_b16 a[86:87], v15 offset:4608\n"
"v_mfma_f32_16x16x32_bf16 a[0:3], a[32:35], a[152:155], a[0:3]\n"
"ds_read_b64_tr_b16 a[88:89], v15 offset:8\n"
"v_mfma_f32_16x16x32_bf16 v[88:91], a[48:51], a[96:99], v[88:91]\n"
"ds_read_b64_tr_b16 a[90:91], v15 offset:520\n"
"buffer_load_dwordx4 v11, s[52:55], s67 offen offset:3072 lds\n"
"v_mfma_f32_16x16x32_bf16 v[88:91], a[52:55], a[100:103], v[88:91]\n"
"ds_read_b64_tr_b16 a[92:93], v15 offset:4104\n"
"v_mfma_f32_16x16x32_bf16 v[116:119], a[60:63], a[100:103], v[116:119]\n"
"s_add_u32 m0, s64, 0\n"
"ds_read_b64_tr_b16 a[94:95], v15 offset:4616\n"
"v_mfma_f32_16x16x32_bf16 v[116:119], a[56:59], a[96:99], v[116:119]\n"
"v_mfma_f32_16x16x32_bf16 v[120:123], a[56:59], a[104:107], v[120:123]\n"
"v_mfma_f32_16x16x32_bf16 v[120:123], a[60:63], a[108:111], v[120:123]\n"
"v_mfma_f32_16x16x32_bf16 v[92:95], a[52:55], a[108:111], v[92:95]\n"
"s_add_u32 s67, s75, 262272\n"
"v_mfma_f32_16x16x32_bf16 v[92:95], a[48:51], a[104:107], v[92:95]\n"
"v_mfma_f32_16x16x32_bf16 v[96:99], a[48:51], a[112:115], v[96:99]\n"
"s_waitcnt lgkmcnt(0)\n"
"v_mfma_f32_16x16x32_bf16 v[96:99], a[52:55], a[116:119], v[96:99]\n"
"buffer_load_dwordx4 v11, s[52:55], s67 offen offset:0 lds\n"
"v_mfma_f32_16x16x32_bf16 v[124:127], a[60:63], a[116:119], v[124:127]\n"
"v_mfma_f32_16x16x32_bf16 v[124:127], a[56:59], a[112:115], v[124:127]\n"
"s_add_u32 s67, s75, 277632\n"
"v_mfma_f32_16x16x32_bf16 v[128:131], a[56:59], a[120:123], v[128:131]\n"
"v_mfma_f32_16x16x32_bf16 v[128:131], a[60:63], a[124:127], v[128:131]\n"
"buffer_load_dwordx4 v11, s[52:55], s67 offen offset:1024 lds\n"
"v_mfma_f32_16x16x32_bf16 v[100:103], a[52:55], a[124:127], v[100:103]\n"
"v_mfma_f32_16x16x32_bf16 v[100:103], a[48:51], a[120:123], v[100:103]\n"
"s_add_u32 s67, s75, 292992\n"
"v_mfma_f32_16x16x32_bf16 v[104:107], a[48:51], a[128:131], v[104:107]\n"
"v_mfma_f32_16x16x32_bf16 v[104:107], a[52:55], a[132:135], v[104:107]\n"
"v_mfma_f32_16x16x32_bf16 v[132:135], a[60:63], a[132:135], v[132:135]\n"
"buffer_load_dwordx4 v11, s[52:55], s67 offen offset:2048 lds\n"
"v_mfma_f32_16x16x32_bf16 v[132:135], a[56:59], a[128:131], v[132:135]\n"
"v_mfma_f32_16x16x32_bf16 v[136:139], a[56:59], a[136:139], v[136:139]\n"
"s_add_u32 s67, s75, 308352\n"
"v_mfma_f32_16x16x32_bf16 v[136:139], a[60:63], a[140:143], v[136:139]\n"
"v_mfma_f32_16x16x32_bf16 v[108:111], a[52:55], a[140:143], v[108:111]\n"
"buffer_load_dwordx4 v11, s[52:55], s67 offen offset:3072 lds\n"
"v_mfma_f32_16x16x32_bf16 v[108:111], a[48:51], a[136:139], v[108:111]\n"
"v_mfma_f32_16x16x32_bf16 v[112:115], a[48:51], a[144:147], v[112:115]\n"
"s_add_u32 m0, s64, 4096\n"
"v_mfma_f32_16x16x32_bf16 v[112:115], a[52:55], a[148:151], v[112:115]\n"
"s_sub_u32 s71, s72, s64\n"
"v_mfma_f32_16x16x32_bf16 v[140:143], a[60:63], a[148:151], v[140:143]\n"
"s_add_u32 s67, s75, 327808\n"
"v_mfma_f32_16x16x32_bf16 v[140:143], a[56:59], a[144:147], v[140:143]\n"
"buffer_load_dwordx4 v11, s[52:55], s67 offen offset:0 lds\n"
"v_mfma_f32_16x16x32_bf16 a[12:15], a[56:59], a[152:155], a[12:15]\n"
"s_add_u32 s71, s71, 139264\n"
"v_mfma_f32_16x16x32_bf16 a[12:15], a[60:63], a[156:159], a[12:15]\n"
"s_add_u32 s67, s75, 343168\n"
"v_mfma_f32_16x16x32_bf16 a[8:11], a[52:55], a[156:159], a[8:11]\n"
"v_add_u32 v20, s71, v9\n"
"v_mfma_f32_16x16x32_bf16 a[8:11], a[48:51], a[152:155], a[8:11]\n"
"v_mfma_f32_16x16x32_bf16 v[144:147], a[64:67], a[96:99], v[144:147]\n"
"buffer_load_dwordx4 v11, s[52:55], s67 offen offset:1024 lds\n"
"v_mfma_f32_16x16x32_bf16 v[144:147], a[68:71], a[100:103], v[144:147]\n"
"v_add_u32 v21, s71, v15\n"
"v_mfma_f32_16x16x32_bf16 v[172:175], a[76:79], a[100:103], v[172:175]\n"
"s_add_u32 s67, s75, 358528\n"
"v_mfma_f32_16x16x32_bf16 v[172:175], a[72:75], a[96:99], v[172:175]\n"
"v_mfma_f32_16x16x32_bf16 v[176:179], a[72:75], a[104:107], v[176:179]\n"
"s_waitcnt vmcnt(14)\n"
"ds_read_b64_tr_b16 a[160:161], v20 offset:0\n"
"v_mfma_f32_16x16x32_bf16 v[176:179], a[76:79], a[108:111], v[176:179]\n"
"buffer_load_dwordx4 v11, s[52:55], s67 offen offset:2048 lds\n"
"v_mfma_f32_16x16x32_bf16 v[148:151], a[68:71], a[108:111], v[148:151]\n"
"ds_read_b64_tr_b16 a[162:163], v20 offset:512\n"
"v_mfma_f32_16x16x32_bf16 v[148:151], a[64:67], a[104:107], v[148:151]\n"
"s_add_u32 s67, s75, 373888\n"
"ds_read_b64_tr_b16 a[164:165], v20 offset:8\n"
"v_mfma_f32_16x16x32_bf16 v[152:155], a[64:67], a[112:115], v[152:155]\n"
"s_sub_u32 s66, 81920, s73\n"
"ds_read_b64_tr_b16 a[166:167], v20 offset:520\n"
"v_mfma_f32_16x16x32_bf16 v[152:155], a[68:71], a[116:119], v[152:155]\n"
"ds_read_b64_tr_b16 a[168:169], v21 offset:0\n"
"buffer_load_dwordx4 v11, s[52:55], s67 offen offset:3072 lds\n"
"v_mfma_f32_16x16x32_bf16 v[180:183], a[76:79], a[116:119], v[180:183]\n"
"v_add_u32 v12, s66, v7\n"
"v_mfma_f32_16x16x32_bf16 v[180:183], a[72:75], a[112:115], v[180:183]\n"
"ds_read_b64_tr_b16 a[170:171], v21 offset:512\n"
"v_mfma_f32_16x16x32_bf16 v[184:187], a[72:75], a[120:123], v[184:187]\n"
"s_add_u32 m0, s72, 139264\n"
"v_add_u32 v13, s66, v8\n"
"v_mfma_f32_16x16x32_bf16 v[184:187], a[76:79], a[124:127], v[184:187]\n"
"ds_read_b64_tr_b16 a[172:173], v21 offset:8\n"
"v_mfma_f32_16x16x32_bf16 v[156:159], a[68:71], a[124:127], v[156:159]\n"
"ds_read_b64_tr_b16 a[174:175], v21 offset:520\n"
"v_mfma_f32_16x16x32_bf16 v[156:159], a[64:67], a[120:123], v[156:159]\n"
"v_mfma_f32_16x16x32_bf16 v[160:163], a[64:67], a[128:131], v[160:163]\n"
"s_add_u32 s67, s60, 384\n"
"v_mfma_f32_16x16x32_bf16 v[160:163], a[68:71], a[132:135], v[160:163]\n"
"v_mfma_f32_16x16x32_bf16 v[188:191], a[76:79], a[132:135], v[188:191]\n"
"v_mfma_f32_16x16x32_bf16 v[188:191], a[72:75], a[128:131], v[188:191]\n"
"v_mfma_f32_16x16x32_bf16 v[192:195], a[72:75], a[136:139], v[192:195]\n"
"s_waitcnt lgkmcnt(0)\n"
"v_mfma_f32_16x16x32_bf16 v[192:195], a[76:79], a[140:143], v[192:195]\n"
"buffer_load_dwordx4 v6, s[48:51], s67 offen offset:0  lds\n"
"v_mfma_f32_16x16x32_bf16 v[164:167], a[68:71], a[140:143], v[164:167]\n"
"v_mfma_f32_16x16x32_bf16 v[164:167], a[64:67], a[136:139], v[164:167]\n"
"s_add_u32 s67, s60, 15744\n"
"v_mfma_f32_16x16x32_bf16 v[168:171], a[64:67], a[144:147], v[168:171]\n"
"v_mfma_f32_16x16x32_bf16 v[168:171], a[68:71], a[148:151], v[168:171]\n"
"v_mfma_f32_16x16x32_bf16 v[196:199], a[76:79], a[148:151], v[196:199]\n"
"buffer_load_dwordx4 v6, s[48:51], s67 offen offset:1024  lds\n"
"v_mfma_f32_16x16x32_bf16 v[196:199], a[72:75], a[144:147], v[196:199]\n"
"v_mfma_f32_16x16x32_bf16 a[20:23], a[72:75], a[152:155], a[20:23]\n"
"s_add_u32 s67, s60, 31104\n"
"v_mfma_f32_16x16x32_bf16 a[20:23], a[76:79], a[156:159], a[20:23]\n"
"v_mfma_f32_16x16x32_bf16 a[16:19], a[68:71], a[156:159], a[16:19]\n"
"buffer_load_dwordx4 v6, s[48:51], s67 offen offset:2048  lds\n"
"v_mfma_f32_16x16x32_bf16 a[16:19], a[64:67], a[152:155], a[16:19]\n"
"s_barrier\n"
"s_waitcnt vmcnt(11)\n"
"ds_read_b64_tr_b16 a[32:33], v9 offset:8192\n"
"v_mfma_f32_16x16x32_bf16 v[200:203], a[80:83], a[96:99], v[200:203]\n"
"s_add_u32 s67, s60, 46464\n"
"ds_read_b64_tr_b16 a[34:35], v9 offset:8704\n"
"v_mfma_f32_16x16x32_bf16 v[200:203], a[84:87], a[100:103], v[200:203]\n"
"ds_read_b64_tr_b16 a[36:37], v9 offset:12288\n"
"v_mfma_f32_16x16x32_bf16 v[228:231], a[92:95], a[100:103], v[228:231]\n"
"ds_read_b128 a[100:103], v13 offset:16384\n"
"v_mfma_f32_16x16x32_bf16 v[228:231], a[88:91], a[96:99], v[228:231]\n"
"ds_read_b128 a[96:99], v12 offset:16384\n"
"buffer_load_dwordx4 v6, s[48:51], s67 offen offset:3072  lds\n"
"v_mfma_f32_16x16x32_bf16 v[232:235], a[88:91], a[104:107], v[232:235]\n"
"ds_read_b64_tr_b16 a[38:39], v9 offset:12800\n"
"v_mfma_f32_16x16x32_bf16 v[232:235], a[92:95], a[108:111], v[232:235]\n"
"s_add_u32 s70, s75, 655360\n"
"ds_read_b64_tr_b16 a[40:41], v9 offset:8200\n"
"v_mfma_f32_16x16x32_bf16 v[204:207], a[84:87], a[108:111], v[204:207]\n"
"ds_read_b128 a[108:111], v13 offset:18432\n"
"v_mfma_f32_16x16x32_bf16 v[204:207], a[80:83], a[104:107], v[204:207]\n"
"s_add_u32 s70, s70, s65\n"
"ds_read_b128 a[104:107], v12 offset:18432\n"
"v_mfma_f32_16x16x32_bf16 v[208:211], a[80:83], a[112:115], v[208:211]\n"
"ds_read_b64_tr_b16 a[42:43], v9 offset:8712\n"
"v_mfma_f32_16x16x32_bf16 v[208:211], a[84:87], a[116:119], v[208:211]\n"
"ds_read_b64_tr_b16 a[44:45], v9 offset:12296\n"
"buffer_load_dword v23, v14, s[52:55], s70 offen\n"
"v_mfma_f32_16x16x32_bf16 v[236:239], a[92:95], a[116:119], v[236:239]\n"
"ds_read_b128 a[116:119], v13 offset:20480\n"
"v_mfma_f32_16x16x32_bf16 v[236:239], a[88:91], a[112:115], v[236:239]\n"
"buffer_store_dwordx4 a[160:163], v0, s[36:39], %22 offen offset:0 sc1\n"
"ds_read_b128 a[112:115], v12 offset:20480\n"
"v_mfma_f32_16x16x32_bf16 v[240:243], a[88:91], a[120:123], v[240:243]\n"
"ds_read_b64_tr_b16 a[46:47], v9 offset:12808\n"
"v_mfma_f32_16x16x32_bf16 v[240:243], a[92:95], a[124:127], v[240:243]\n"
"ds_read_b64_tr_b16 a[48:49], v15 offset:8192\n"
"v_mfma_f32_16x16x32_bf16 v[212:215], a[84:87], a[124:127], v[212:215]\n"
"ds_read_b128 a[124:127], v13 offset:22528\n"
"v_mfma_f32_16x16x32_bf16 v[212:215], a[80:83], a[120:123], v[212:215]\n"
"ds_read_b128 a[120:123], v12 offset:22528\n"
"v_mfma_f32_16x16x32_bf16 v[216:219], a[80:83], a[128:131], v[216:219]\n"
"s_add_u32 m0, s64, 8192\n"
"ds_read_b64_tr_b16 a[50:51], v15 offset:8704\n"
"v_mfma_f32_16x16x32_bf16 v[216:219], a[84:87], a[132:135], v[216:219]\n"
"ds_read_b64_tr_b16 a[52:53], v15 offset:12288\n"
"v_mfma_f32_16x16x32_bf16 v[244:247], a[92:95], a[132:135], v[244:247]\n"
"ds_read_b128 a[132:135], v13 offset:24576\n"
"v_mfma_f32_16x16x32_bf16 v[244:247], a[88:91], a[128:131], v[244:247]\n"
"ds_read_b128 a[128:131], v12 offset:24576\n"
"v_mfma_f32_16x16x32_bf16 v[248:251], a[88:91], a[136:139], v[248:251]\n"
"ds_read_b64_tr_b16 a[54:55], v15 offset:12800\n"
"v_mfma_f32_16x16x32_bf16 v[248:251], a[92:95], a[140:143], v[248:251]\n"
"ds_read_b64_tr_b16 a[56:57], v15 offset:8200\n"
"v_mfma_f32_16x16x32_bf16 v[220:223], a[84:87], a[140:143], v[220:223]\n"
"ds_read_b128 a[140:143], v13 offset:26624\n"
"v_mfma_f32_16x16x32_bf16 v[220:223], a[80:83], a[136:139], v[220:223]\n"
"ds_read_b128 a[136:139], v12 offset:26624\n"
"v_mfma_f32_16x16x32_bf16 v[224:227], a[80:83], a[144:147], v[224:227]\n"
"s_add_u32 s67, s75, 393216\n"
"ds_read_b64_tr_b16 a[58:59], v15 offset:8712\n"
"v_mfma_f32_16x16x32_bf16 v[224:227], a[84:87], a[148:151], v[224:227]\n"
"ds_read_b64_tr_b16 a[60:61], v15 offset:12296\n"
"v_mfma_f32_16x16x32_bf16 v[252:255], a[92:95], a[148:151], v[252:255]\n"
"ds_read_b128 a[148:151], v13 offset:28672\n"
"v_mfma_f32_16x16x32_bf16 v[252:255], a[88:91], a[144:147], v[252:255]\n"
"ds_read_b128 a[144:147], v12 offset:28672\n"
"ds_read_b64_tr_b16 a[62:63], v15 offset:12808\n"
"v_mfma_f32_16x16x32_bf16 a[28:31], a[88:91], a[152:155], a[28:31]\n"
"v_mfma_f32_16x16x32_bf16 a[28:31], a[92:95], a[156:159], a[28:31]\n"
"v_mfma_f32_16x16x32_bf16 a[24:27], a[84:87], a[156:159], a[24:27]\n"
"ds_read_b128 a[156:159], v13 offset:30720\n"
"v_mfma_f32_16x16x32_bf16 a[24:27], a[80:83], a[152:155], a[24:27]\n"
"s_waitcnt lgkmcnt(15)\n"
"ds_read_b128 a[152:155], v12 offset:30720\n"
"v_mfma_f32_16x16x32_bf16 v[32:35], a[32:35], a[96:99], v[32:35]\n"
"v_mfma_f32_16x16x32_bf16 v[32:35], a[36:39], a[100:103], v[32:35]\n"
"s_waitcnt lgkmcnt(2)\n"
"v_mfma_f32_16x16x32_bf16 v[60:63], a[44:47], a[100:103], v[60:63]\n"
"buffer_load_dwordx4 v11, s[52:55], s67 offen offset:0 lds\n"
"v_mfma_f32_16x16x32_bf16 v[60:63], a[40:43], a[96:99], v[60:63]\n"
"v_mfma_f32_16x16x32_bf16 v[64:67], a[40:43], a[104:107], v[64:67]\n"
"s_add_u32 s67, s75, 408576\n"
"v_mfma_f32_16x16x32_bf16 v[64:67], a[44:47], a[108:111], v[64:67]\n"
"v_mfma_f32_16x16x32_bf16 v[36:39], a[36:39], a[108:111], v[36:39]\n"
"v_mfma_f32_16x16x32_bf16 v[36:39], a[32:35], a[104:107], v[36:39]\n"
"buffer_load_dwordx4 v11, s[52:55], s67 offen offset:1024 lds\n"
"v_mfma_f32_16x16x32_bf16 v[40:43], a[32:35], a[112:115], v[40:43]\n"
"v_mfma_f32_16x16x32_bf16 v[40:43], a[36:39], a[116:119], v[40:43]\n"
"s_add_u32 s67, s75, 423936\n"
"v_mfma_f32_16x16x32_bf16 v[68:71], a[44:47], a[116:119], v[68:71]\n"
"v_mfma_f32_16x16x32_bf16 v[68:71], a[40:43], a[112:115], v[68:71]\n"
"v_mfma_f32_16x16x32_bf16 v[72:75], a[40:43], a[120:123], v[72:75]\n"
"buffer_load_dwordx4 v11, s[52:55], s67 offen offset:2048 lds\n"
"v_mfma_f32_16x16x32_bf16 v[72:75], a[44:47], a[124:127], v[72:75]\n"
"v_mfma_f32_16x16x32_bf16 v[44:47], a[36:39], a[124:127], v[44:47]\n"
"s_add_u32 s67, s75, 439296\n"
"v_mfma_f32_16x16x32_bf16 v[44:47], a[32:35], a[120:123], v[44:47]\n"
"v_mfma_f32_16x16x32_bf16 v[48:51], a[32:35], a[128:131], v[48:51]\n"
"buffer_load_dwordx4 v11, s[52:55], s67 offen offset:3072 lds\n"
"v_mfma_f32_16x16x32_bf16 v[48:51], a[36:39], a[132:135], v[48:51]\n"
"s_add_u32 m0, s64, 12288\n"
"v_mfma_f32_16x16x32_bf16 v[76:79], a[44:47], a[132:135], v[76:79]\n"
"s_waitcnt vmcnt(10)\n"
"ds_read_b64_tr_b16 a[64:65], v9 offset:0\n"
"v_mfma_f32_16x16x32_bf16 v[76:79], a[40:43], a[128:131], v[76:79]\n"
"ds_read_b64_tr_b16 a[66:67], v9 offset:512\n"
"v_mfma_f32_16x16x32_bf16 v[80:83], a[40:43], a[136:139], v[80:83]\n"
"s_add_u32 s67, s75, 458752\n"
"ds_read_b64_tr_b16 a[68:69], v9 offset:4096\n"
"v_mfma_f32_16x16x32_bf16 v[80:83], a[44:47], a[140:143], v[80:83]\n"
"ds_read_b64_tr_b16 a[70:71], v9 offset:4608\n"
"v_mfma_f32_16x16x32_bf16 v[52:55], a[36:39], a[140:143], v[52:55]\n"
"ds_read_b64_tr_b16 a[72:73], v9 offset:8\n"
"buffer_load_dwordx4 v11, s[52:55], s67 offen offset:0 lds\n"
"v_mfma_f32_16x16x32_bf16 v[52:55], a[32:35], a[136:139], v[52:55]\n"
"s_waitcnt lgkmcnt(5)\n"
"ds_read_b64_tr_b16 a[74:75], v9 offset:520\n"
"v_mfma_f32_16x16x32_bf16 v[56:59], a[32:35], a[144:147], v[56:59]\n"
"s_add_u32 s67, s75, 474112\n"
"ds_read_b64_tr_b16 a[76:77], v9 offset:4104\n"
"v_mfma_f32_16x16x32_bf16 v[56:59], a[36:39], a[148:151], v[56:59]\n"
"ds_read_b64_tr_b16 a[78:79], v9 offset:4616\n"
"v_mfma_f32_16x16x32_bf16 v[84:87], a[44:47], a[148:151], v[84:87]\n"
"buffer_load_dwordx4 v11, s[52:55], s67 offen offset:1024 lds\n"
"v_mfma_f32_16x16x32_bf16 v[84:87], a[40:43], a[144:147], v[84:87]\n"
"ds_read_b64_tr_b16 a[80:81], v15 offset:0\n"
"v_mfma_f32_16x16x32_bf16 a[4:7], a[40:43], a[152:155], a[4:7]\n"
"s_add_u32 s67, s75, 489472\n"
"ds_read_b64_tr_b16 a[82:83], v15 offset:512\n"
"v_mfma_f32_16x16x32_bf16 a[4:7], a[44:47], a[156:159], a[4:7]\n"
"ds_read_b64_tr_b16 a[84:85], v15 offset:4096\n"
"v_mfma_f32_16x16x32_bf16 a[0:3], a[36:39], a[156:159], a[0:3]\n"
"ds_read_b64_tr_b16 a[86:87], v15 offset:4608\n"
"buffer_load_dwordx4 v11, s[52:55], s67 offen offset:2048 lds\n"
"v_mfma_f32_16x16x32_bf16 a[0:3], a[32:35], a[152:155], a[0:3]\n"
"ds_read_b64_tr_b16 a[88:89], v15 offset:8\n"
"v_mfma_f32_16x16x32_bf16 v[88:91], a[48:51], a[96:99], v[88:91]\n"
"s_add_u32 s67, s75, 504832\n"
"ds_read_b64_tr_b16 a[90:91], v15 offset:520\n"
"v_mfma_f32_16x16x32_bf16 v[88:91], a[52:55], a[100:103], v[88:91]\n"
"ds_read_b64_tr_b16 a[92:93], v15 offset:4104\n"
"v_mfma_f32_16x16x32_bf16 v[116:119], a[60:63], a[100:103], v[116:119]\n"
"ds_read_b64_tr_b16 a[94:95], v15 offset:4616\n"
"v_mfma_f32_16x16x32_bf16 v[116:119], a[56:59], a[96:99], v[116:119]\n"
"buffer_load_dwordx4 v11, s[52:55], s67 offen offset:3072 lds\n"
"v_mfma_f32_16x16x32_bf16 v[120:123], a[56:59], a[104:107], v[120:123]\n"
"v_mfma_f32_16x16x32_bf16 v[120:123], a[60:63], a[108:111], v[120:123]\n"
"s_add_u32 m0, s64, 0\n"
"v_mfma_f32_16x16x32_bf16 v[92:95], a[52:55], a[108:111], v[92:95]\n"
"v_mfma_f32_16x16x32_bf16 v[92:95], a[48:51], a[104:107], v[92:95]\n"
"s_add_u32 s67, s75, 393344\n"
"v_mfma_f32_16x16x32_bf16 v[96:99], a[48:51], a[112:115], v[96:99]\n"
"s_waitcnt lgkmcnt(0)\n"
"v_mfma_f32_16x16x32_bf16 v[96:99], a[52:55], a[116:119], v[96:99]\n"
"buffer_load_dwordx4 v11, s[52:55], s67 offen offset:0 lds\n"
"v_mfma_f32_16x16x32_bf16 v[124:127], a[60:63], a[116:119], v[124:127]\n"
"v_mfma_f32_16x16x32_bf16 v[124:127], a[56:59], a[112:115], v[124:127]\n"
"s_add_u32 s67, s75, 408704\n"
"v_mfma_f32_16x16x32_bf16 v[128:131], a[56:59], a[120:123], v[128:131]\n"
"v_mfma_f32_16x16x32_bf16 v[128:131], a[60:63], a[124:127], v[128:131]\n"
"v_mfma_f32_16x16x32_bf16 v[100:103], a[52:55], a[124:127], v[100:103]\n"
"buffer_load_dwordx4 v11, s[52:55], s67 offen offset:1024 lds\n"
"v_mfma_f32_16x16x32_bf16 v[100:103], a[48:51], a[120:123], v[100:103]\n"
"v_mfma_f32_16x16x32_bf16 v[104:107], a[48:51], a[128:131], v[104:107]\n"
"s_add_u32 s67, s75, 424064\n"
"v_mfma_f32_16x16x32_bf16 v[104:107], a[52:55], a[132:135], v[104:107]\n"
"v_mfma_f32_16x16x32_bf16 v[132:135], a[60:63], a[132:135], v[132:135]\n"
"v_mfma_f32_16x16x32_bf16 v[132:135], a[56:59], a[128:131], v[132:135]\n"
"buffer_load_dwordx4 v11, s[52:55], s67 offen offset:2048 lds\n"
"v_mfma_f32_16x16x32_bf16 v[136:139], a[56:59], a[136:139], v[136:139]\n"
"v_mfma_f32_16x16x32_bf16 v[136:139], a[60:63], a[140:143], v[136:139]\n"
"s_add_u32 s67, s75, 439424\n"
"v_mfma_f32_16x16x32_bf16 v[108:111], a[52:55], a[140:143], v[108:111]\n"
"v_mfma_f32_16x16x32_bf16 v[108:111], a[48:51], a[136:139], v[108:111]\n"
"buffer_load_dwordx4 v11, s[52:55], s67 offen offset:3072 lds\n"
"v_mfma_f32_16x16x32_bf16 v[112:115], a[48:51], a[144:147], v[112:115]\n"
"v_mfma_f32_16x16x32_bf16 v[112:115], a[52:55], a[148:151], v[112:115]\n"
"s_add_u32 m0, s64, 4096\n"
"v_mfma_f32_16x16x32_bf16 v[140:143], a[60:63], a[148:151], v[140:143]\n"
"v_mfma_f32_16x16x32_bf16 v[140:143], a[56:59], a[144:147], v[140:143]\n"
"s_add_u32 s67, s75, 458880\n"
"v_mfma_f32_16x16x32_bf16 a[12:15], a[56:59], a[152:155], a[12:15]\n"
"v_mfma_f32_16x16x32_bf16 a[12:15], a[60:63], a[156:159], a[12:15]\n"
"buffer_load_dwordx4 v11, s[52:55], s67 offen offset:0 lds\n"
"v_mfma_f32_16x16x32_bf16 a[8:11], a[52:55], a[156:159], a[8:11]\n"
"v_mfma_f32_16x16x32_bf16 a[8:11], a[48:51], a[152:155], a[8:11]\n"
"s_add_u32 s67, s75, 474240\n"
"v_mfma_f32_16x16x32_bf16 v[144:147], a[64:67], a[96:99], v[144:147]\n"
"v_mfma_f32_16x16x32_bf16 v[144:147], a[68:71], a[100:103], v[144:147]\n"
"buffer_load_dwordx4 v11, s[52:55], s67 offen offset:1024 lds\n"
"v_mfma_f32_16x16x32_bf16 v[172:175], a[76:79], a[100:103], v[172:175]\n"
"v_mfma_f32_16x16x32_bf16 v[172:175], a[72:75], a[96:99], v[172:175]\n"
"s_add_u32 s67, s75, 489600\n"
"v_mfma_f32_16x16x32_bf16 v[176:179], a[72:75], a[104:107], v[176:179]\n"
"v_mfma_f32_16x16x32_bf16 v[176:179], a[76:79], a[108:111], v[176:179]\n"
"buffer_load_dwordx4 v11, s[52:55], s67 offen offset:2048 lds\n"
"v_mfma_f32_16x16x32_bf16 v[148:151], a[68:71], a[108:111], v[148:151]\n"
"v_mfma_f32_16x16x32_bf16 v[148:151], a[64:67], a[104:107], v[148:151]\n"
"s_add_u32 s67, s75, 504960\n"
"v_mfma_f32_16x16x32_bf16 v[152:155], a[64:67], a[112:115], v[152:155]\n"
"s_sub_u32 s66, 122880, s73\n"
"v_mfma_f32_16x16x32_bf16 v[152:155], a[68:71], a[116:119], v[152:155]\n"
"v_mfma_f32_16x16x32_bf16 v[180:183], a[76:79], a[116:119], v[180:183]\n"
"v_add_u32 v12, s66, v7\n"
"v_mfma_f32_16x16x32_bf16 v[180:183], a[72:75], a[112:115], v[180:183]\n"
"buffer_load_dwordx4 v11, s[52:55], s67 offen offset:3072 lds\n"
"v_mfma_f32_16x16x32_bf16 v[184:187], a[72:75], a[120:123], v[184:187]\n"
"s_add_u32 m0, s72, 16384\n"
"v_add_u32 v13, s66, v8\n"
"v_mfma_f32_16x16x32_bf16 v[184:187], a[76:79], a[124:127], v[184:187]\n"
"v_mfma_f32_16x16x32_bf16 v[156:159], a[68:71], a[124:127], v[156:159]\n"
"s_add_u32 s67, s60, 512\n"
"v_mfma_f32_16x16x32_bf16 v[156:159], a[64:67], a[120:123], v[156:159]\n"
"buffer_load_dwordx4 v6, s[48:51], s67 offen offset:0  lds\n"
"v_mfma_f32_16x16x32_bf16 v[160:163], a[64:67], a[128:131], v[160:163]\n"
"v_mfma_f32_16x16x32_bf16 v[160:163], a[68:71], a[132:135], v[160:163]\n"
"s_add_u32 s67, s60, 15872\n"
"v_mfma_f32_16x16x32_bf16 v[188:191], a[76:79], a[132:135], v[188:191]\n"
"v_mfma_f32_16x16x32_bf16 v[188:191], a[72:75], a[128:131], v[188:191]\n"
"buffer_load_dwordx4 v6, s[48:51], s67 offen offset:1024  lds\n"
"v_mfma_f32_16x16x32_bf16 v[192:195], a[72:75], a[136:139], v[192:195]\n"
"v_mfma_f32_16x16x32_bf16 v[192:195], a[76:79], a[140:143], v[192:195]\n"
"s_add_u32 s67, s60, 31232\n"
"v_mfma_f32_16x16x32_bf16 v[164:167], a[68:71], a[140:143], v[164:167]\n"
"v_mfma_f32_16x16x32_bf16 v[164:167], a[64:67], a[136:139], v[164:167]\n"
"buffer_load_dwordx4 v6, s[48:51], s67 offen offset:2048  lds\n"
"v_mfma_f32_16x16x32_bf16 v[168:171], a[64:67], a[144:147], v[168:171]\n"
"v_mfma_f32_16x16x32_bf16 v[168:171], a[68:71], a[148:151], v[168:171]\n"
"s_add_u32 s67, s60, 46592\n"
"v_mfma_f32_16x16x32_bf16 v[196:199], a[76:79], a[148:151], v[196:199]\n"
"v_mfma_f32_16x16x32_bf16 v[196:199], a[72:75], a[144:147], v[196:199]\n"
"v_mfma_f32_16x16x32_bf16 a[20:23], a[72:75], a[152:155], a[20:23]\n"
"buffer_load_dwordx4 v6, s[48:51], s67 offen offset:3072  lds\n"
"v_mfma_f32_16x16x32_bf16 a[20:23], a[76:79], a[156:159], a[20:23]\n"
"v_mfma_f32_16x16x32_bf16 a[16:19], a[68:71], a[156:159], a[16:19]\n"
"s_add_u32 s70, s75, 786432\n"
"s_add_u32 s70, s70, s65\n"
"v_mfma_f32_16x16x32_bf16 a[16:19], a[64:67], a[152:155], a[16:19]\n"
"s_waitcnt vmcnt(22)\n"
"s_barrier\n"
"v_mfma_f32_16x16x32_bf16 v[200:203], a[80:83], a[96:99], v[200:203]\n"
"s_waitcnt vmcnt(12)\n"
"ds_read_b64_tr_b16 a[32:33], v9 offset:8192\n"
"v_mfma_f32_16x16x32_bf16 v[200:203], a[84:87], a[100:103], v[200:203]\n"
"ds_read_b64_tr_b16 a[34:35], v9 offset:8704\n"
"v_mfma_f32_16x16x32_bf16 v[228:231], a[92:95], a[100:103], v[228:231]\n"
"ds_read_b128 a[100:103], v13 offset:16384\n"
"buffer_load_dword v23, v14, s[52:55], s70 offen\n"
"v_mfma_f32_16x16x32_bf16 v[228:231], a[88:91], a[96:99], v[228:231]\n"
"ds_read_b128 a[96:99], v12 offset:16384\n"
"v_mfma_f32_16x16x32_bf16 v[232:235], a[88:91], a[104:107], v[232:235]\n"
"ds_read_b64_tr_b16 a[36:37], v9 offset:12288\n"
"v_mfma_f32_16x16x32_bf16 v[232:235], a[92:95], a[108:111], v[232:235]\n"
"ds_read_b64_tr_b16 a[38:39], v9 offset:12800\n"
"v_mfma_f32_16x16x32_bf16 v[204:207], a[84:87], a[108:111], v[204:207]\n"
"ds_read_b128 a[108:111], v13 offset:18432\n"
"v_mfma_f32_16x16x32_bf16 v[204:207], a[80:83], a[104:107], v[204:207]\n"
"buffer_store_dwordx4 a[164:167], v0, s[36:39], %22 offen offset:2048 sc1\n"
"ds_read_b128 a[104:107], v12 offset:18432\n"
"ds_read_b64_tr_b16 a[40:41], v9 offset:8200\n"
"v_mfma_f32_16x16x32_bf16 v[208:211], a[80:83], a[112:115], v[208:211]\n"
"ds_read_b64_tr_b16 a[42:43], v9 offset:8712\n"
"v_mfma_f32_16x16x32_bf16 v[208:211], a[84:87], a[116:119], v[208:211]\n"
"ds_read_b64_tr_b16 a[44:45], v9 offset:12296\n"
"v_mfma_f32_16x16x32_bf16 v[236:239], a[92:95], a[116:119], v[236:239]\n"
"ds_read_b128 a[116:119], v13 offset:20480\n"
"v_mfma_f32_16x16x32_bf16 v[236:239], a[88:91], a[112:115], v[236:239]\n"
"ds_read_b128 a[112:115], v12 offset:20480\n"
"v_mfma_f32_16x16x32_bf16 v[240:243], a[88:91], a[120:123], v[240:243]\n"
"s_add_u32 m0, s64, 8192\n"
"ds_read_b64_tr_b16 a[46:47], v9 offset:12808\n"
"v_mfma_f32_16x16x32_bf16 v[240:243], a[92:95], a[124:127], v[240:243]\n"
"ds_read_b64_tr_b16 a[48:49], v15 offset:8192\n"
"v_mfma_f32_16x16x32_bf16 v[212:215], a[84:87], a[124:127], v[212:215]\n"
"ds_read_b128 a[124:127], v13 offset:22528\n"
"v_mfma_f32_16x16x32_bf16 v[212:215], a[80:83], a[120:123], v[212:215]\n"
"ds_read_b128 a[120:123], v12 offset:22528\n"
"v_mfma_f32_16x16x32_bf16 v[216:219], a[80:83], a[128:131], v[216:219]\n"
"ds_read_b64_tr_b16 a[50:51], v15 offset:8704\n"
"v_mfma_f32_16x16x32_bf16 v[216:219], a[84:87], a[132:135], v[216:219]\n"
"ds_read_b64_tr_b16 a[52:53], v15 offset:12288\n"
"v_mfma_f32_16x16x32_bf16 v[244:247], a[92:95], a[132:135], v[244:247]\n"
"ds_read_b128 a[132:135], v13 offset:24576\n"
"v_mfma_f32_16x16x32_bf16 v[244:247], a[88:91], a[128:131], v[244:247]\n"
"ds_read_b128 a[128:131], v12 offset:24576\n"
"v_mfma_f32_16x16x32_bf16 v[248:251], a[88:91], a[136:139], v[248:251]\n"
"ds_read_b64_tr_b16 a[54:55], v15 offset:12800\n"
"v_mfma_f32_16x16x32_bf16 v[248:251], a[92:95], a[140:143], v[248:251]\n"
"ds_read_b64_tr_b16 a[56:57], v15 offset:8200\n"
"v_mfma_f32_16x16x32_bf16 v[220:223], a[84:87], a[140:143], v[220:223]\n"
"s_add_u32 s67, s75, 524288\n"
"ds_read_b128 a[140:143], v13 offset:26624\n"
"v_mfma_f32_16x16x32_bf16 v[220:223], a[80:83], a[136:139], v[220:223]\n"
"ds_read_b128 a[136:139], v12 offset:26624\n"
"v_mfma_f32_16x16x32_bf16 v[224:227], a[80:83], a[144:147], v[224:227]\n"
"ds_read_b64_tr_b16 a[58:59], v15 offset:8712\n"
"v_mfma_f32_16x16x32_bf16 v[224:227], a[84:87], a[148:151], v[224:227]\n"
"ds_read_b64_tr_b16 a[60:61], v15 offset:12296\n"
"v_mfma_f32_16x16x32_bf16 v[252:255], a[92:95], a[148:151], v[252:255]\n"
"ds_read_b128 a[148:151], v13 offset:28672\n"
"v_mfma_f32_16x16x32_bf16 v[252:255], a[88:91], a[144:147], v[252:255]\n"
"ds_read_b128 a[144:147], v12 offset:28672\n"
"ds_read_b64_tr_b16 a[62:63], v15 offset:12808\n"
"v_mfma_f32_16x16x32_bf16 a[28:31], a[88:91], a[152:155], a[28:31]\n"
"v_mfma_f32_16x16x32_bf16 a[28:31], a[92:95], a[156:159], a[28:31]\n"
"v_mfma_f32_16x16x32_bf16 a[24:27], a[84:87], a[156:159], a[24:27]\n"
"ds_read_b128 a[156:159], v13 offset:30720\n"
"v_mfma_f32_16x16x32_bf16 a[24:27], a[80:83], a[152:155], a[24:27]\n"
"s_waitcnt lgkmcnt(15)\n"
"ds_read_b128 a[152:155], v12 offset:30720\n"
"v_mfma_f32_16x16x32_bf16 v[32:35], a[32:35], a[96:99], v[32:35]\n"
"v_mfma_f32_16x16x32_bf16 v[32:35], a[36:39], a[100:103], v[32:35]\n"
"s_waitcnt lgkmcnt(2)\n"
"v_mfma_f32_16x16x32_bf16 v[60:63], a[44:47], a[100:103], v[60:63]\n"
"buffer_load_dwordx4 v11, s[52:55], s67 offen offset:0 lds\n"
"v_mfma_f32_16x16x32_bf16 v[60:63], a[40:43], a[96:99], v[60:63]\n"
"v_mfma_f32_16x16x32_bf16 v[64:67], a[40:43], a[104:107], v[64:67]\n"
"s_add_u32 s67, s75, 539648\n"
"v_mfma_f32_16x16x32_bf16 v[64:67], a[44:47], a[108:111], v[64:67]\n"
"v_mfma_f32_16x16x32_bf16 v[36:39], a[36:39], a[108:111], v[36:39]\n"
"v_mfma_f32_16x16x32_bf16 v[36:39], a[32:35], a[104:107], v[36:39]\n"
"buffer_load_dwordx4 v11, s[52:55], s67 offen offset:1024 lds\n"
"v_mfma_f32_16x16x32_bf16 v[40:43], a[32:35], a[112:115], v[40:43]\n"
"v_mfma_f32_16x16x32_bf16 v[40:43], a[36:39], a[116:119], v[40:43]\n"
"s_add_u32 s67, s75, 555008\n"
"v_mfma_f32_16x16x32_bf16 v[68:71], a[44:47], a[116:119], v[68:71]\n"
"v_mfma_f32_16x16x32_bf16 v[68:71], a[40:43], a[112:115], v[68:71]\n"
"buffer_load_dwordx4 v11, s[52:55], s67 offen offset:2048 lds\n"
"v_mfma_f32_16x16x32_bf16 v[72:75], a[40:43], a[120:123], v[72:75]\n"
"v_mfma_f32_16x16x32_bf16 v[72:75], a[44:47], a[124:127], v[72:75]\n"
"s_add_u32 s67, s75, 570368\n"
"v_mfma_f32_16x16x32_bf16 v[44:47], a[36:39], a[124:127], v[44:47]\n"
"v_mfma_f32_16x16x32_bf16 v[44:47], a[32:35], a[120:123], v[44:47]\n"
"v_mfma_f32_16x16x32_bf16 v[48:51], a[32:35], a[128:131], v[48:51]\n"
"buffer_load_dwordx4 v11, s[52:55], s67 offen offset:3072 lds\n"
"v_mfma_f32_16x16x32_bf16 v[48:51], a[36:39], a[132:135], v[48:51]\n"
"s_add_u32 m0, s64, 12288\n"
"v_mfma_f32_16x16x32_bf16 v[76:79], a[44:47], a[132:135], v[76:79]\n"
"s_waitcnt vmcnt(10)\n"
"ds_read_b64_tr_b16 a[64:65], v9 offset:0\n"
"v_mfma_f32_16x16x32_bf16 v[76:79], a[40:43], a[128:131], v[76:79]\n"
"ds_read_b64_tr_b16 a[66:67], v9 offset:512\n"
"v_mfma_f32_16x16x32_bf16 v[80:83], a[40:43], a[136:139], v[80:83]\n"
"s_add_u32 s67, s75, 589824\n"
"ds_read_b64_tr_b16 a[68:69], v9 offset:4096\n"
"v_mfma_f32_16x16x32_bf16 v[80:83], a[44:47], a[140:143], v[80:83]\n"
"ds_read_b64_tr_b16 a[70:71], v9 offset:4608\n"
"v_mfma_f32_16x16x32_bf16 v[52:55], a[36:39], a[140:143], v[52:55]\n"
"ds_read_b64_tr_b16 a[72:73], v9 offset:8\n"
"buffer_load_dwordx4 v11, s[52:55], s67 offen offset:0 lds\n"
"v_mfma_f32_16x16x32_bf16 v[52:55], a[32:35], a[136:139], v[52:55]\n"
"s_waitcnt lgkmcnt(5)\n"
"ds_read_b64_tr_b16 a[74:75], v9 offset:520\n"
"v_mfma_f32_16x16x32_bf16 v[56:59], a[32:35], a[144:147], v[56:59]\n"
"s_add_u32 s67, s75, 605184\n"
"ds_read_b64_tr_b16 a[76:77], v9 offset:4104\n"
"v_mfma_f32_16x16x32_bf16 v[56:59], a[36:39], a[148:151], v[56:59]\n"
"ds_read_b64_tr_b16 a[78:79], v9 offset:4616\n"
"v_mfma_f32_16x16x32_bf16 v[84:87], a[44:47], a[148:151], v[84:87]\n"
"buffer_load_dwordx4 v11, s[52:55], s67 offen offset:1024 lds\n"
"v_mfma_f32_16x16x32_bf16 v[84:87], a[40:43], a[144:147], v[84:87]\n"
"ds_read_b64_tr_b16 a[80:81], v15 offset:0\n"
"v_mfma_f32_16x16x32_bf16 a[4:7], a[40:43], a[152:155], a[4:7]\n"
"s_add_u32 s67, s75, 620544\n"
"ds_read_b64_tr_b16 a[82:83], v15 offset:512\n"
"v_mfma_f32_16x16x32_bf16 a[4:7], a[44:47], a[156:159], a[4:7]\n"
"ds_read_b64_tr_b16 a[84:85], v15 offset:4096\n"
"v_mfma_f32_16x16x32_bf16 a[0:3], a[36:39], a[156:159], a[0:3]\n"
"ds_read_b64_tr_b16 a[86:87], v15 offset:4608\n"
"buffer_load_dwordx4 v11, s[52:55], s67 offen offset:2048 lds\n"
"v_mfma_f32_16x16x32_bf16 a[0:3], a[32:35], a[152:155], a[0:3]\n"
"ds_read_b64_tr_b16 a[88:89], v15 offset:8\n"
"v_mfma_f32_16x16x32_bf16 v[88:91], a[48:51], a[96:99], v[88:91]\n"
"s_add_u32 s67, s75, 635904\n"
"ds_read_b64_tr_b16 a[90:91], v15 offset:520\n"
"v_mfma_f32_16x16x32_bf16 v[88:91], a[52:55], a[100:103], v[88:91]\n"
"ds_read_b64_tr_b16 a[92:93], v15 offset:4104\n"
"v_mfma_f32_16x16x32_bf16 v[116:119], a[60:63], a[100:103], v[116:119]\n"
"ds_read_b64_tr_b16 a[94:95], v15 offset:4616\n"
"v_mfma_f32_16x16x32_bf16 v[116:119], a[56:59], a[96:99], v[116:119]\n"
"buffer_load_dwordx4 v11, s[52:55], s67 offen offset:3072 lds\n"
"v_mfma_f32_16x16x32_bf16 v[120:123], a[56:59], a[104:107], v[120:123]\n"
"v_mfma_f32_16x16x32_bf16 v[120:123], a[60:63], a[108:111], v[120:123]\n"
"s_add_u32 m0, s64, 0\n"
"v_mfma_f32_16x16x32_bf16 v[92:95], a[52:55], a[108:111], v[92:95]\n"
"v_mfma_f32_16x16x32_bf16 v[92:95], a[48:51], a[104:107], v[92:95]\n"
"s_add_u32 s67, s75, 524416\n"
"v_mfma_f32_16x16x32_bf16 v[96:99], a[48:51], a[112:115], v[96:99]\n"
"s_waitcnt lgkmcnt(0)\n"
"v_mfma_f32_16x16x32_bf16 v[96:99], a[52:55], a[116:119], v[96:99]\n"
"buffer_load_dwordx4 v11, s[52:55], s67 offen offset:0 lds\n"
"v_mfma_f32_16x16x32_bf16 v[124:127], a[60:63], a[116:119], v[124:127]\n"
"v_mfma_f32_16x16x32_bf16 v[124:127], a[56:59], a[112:115], v[124:127]\n"
"s_add_u32 s67, s75, 539776\n"
"v_mfma_f32_16x16x32_bf16 v[128:131], a[56:59], a[120:123], v[128:131]\n"
"v_mfma_f32_16x16x32_bf16 v[128:131], a[60:63], a[124:127], v[128:131]\n"
"v_mfma_f32_16x16x32_bf16 v[100:103], a[52:55], a[124:127], v[100:103]\n"
"buffer_load_dwordx4 v11, s[52:55], s67 offen offset:1024 lds\n"
"v_mfma_f32_16x16x32_bf16 v[100:103], a[48:51], a[120:123], v[100:103]\n"
"v_mfma_f32_16x16x32_bf16 v[104:107], a[48:51], a[128:131], v[104:107]\n"
"s_add_u32 s67, s75, 555136\n"
"v_mfma_f32_16x16x32_bf16 v[104:107], a[52:55], a[132:135], v[104:107]\n"
"v_mfma_f32_16x16x32_bf16 v[132:135], a[60:63], a[132:135], v[132:135]\n"
"buffer_load_dwordx4 v11, s[52:55], s67 offen offset:2048 lds\n"
"v_mfma_f32_16x16x32_bf16 v[132:135], a[56:59], a[128:131], v[132:135]\n"
"v_mfma_f32_16x16x32_bf16 v[136:139], a[56:59], a[136:139], v[136:139]\n"
"s_add_u32 s67, s75, 570496\n"
"v_mfma_f32_16x16x32_bf16 v[136:139], a[60:63], a[140:143], v[136:139]\n"
"v_mfma_f32_16x16x32_bf16 v[108:111], a[52:55], a[140:143], v[108:111]\n"
"v_mfma_f32_16x16x32_bf16 v[108:111], a[48:51], a[136:139], v[108:111]\n"
"buffer_load_dwordx4 v11, s[52:55], s67 offen offset:3072 lds\n"
"v_mfma_f32_16x16x32_bf16 v[112:115], a[48:51], a[144:147], v[112:115]\n"
"s_add_u32 m0, s64, 4096\n"
"v_mfma_f32_16x16x32_bf16 v[112:115], a[52:55], a[148:151], v[112:115]\n"
"v_mfma_f32_16x16x32_bf16 v[140:143], a[60:63], a[148:151], v[140:143]\n"
"s_add_u32 s67, s75, 589952\n"
"v_mfma_f32_16x16x32_bf16 v[140:143], a[56:59], a[144:147], v[140:143]\n"
"v_mfma_f32_16x16x32_bf16 a[12:15], a[56:59], a[152:155], a[12:15]\n"
"buffer_load_dwordx4 v11, s[52:55], s67 offen offset:0 lds\n"
"v_mfma_f32_16x16x32_bf16 a[12:15], a[60:63], a[156:159], a[12:15]\n"
"v_mfma_f32_16x16x32_bf16 a[8:11], a[52:55], a[156:159], a[8:11]\n"
"s_add_u32 s67, s75, 605312\n"
"v_mfma_f32_16x16x32_bf16 a[8:11], a[48:51], a[152:155], a[8:11]\n"
"v_mfma_f32_16x16x32_bf16 v[144:147], a[64:67], a[96:99], v[144:147]\n"
"buffer_load_dwordx4 v11, s[52:55], s67 offen offset:1024 lds\n"
"v_mfma_f32_16x16x32_bf16 v[144:147], a[68:71], a[100:103], v[144:147]\n"
"v_mfma_f32_16x16x32_bf16 v[172:175], a[76:79], a[100:103], v[172:175]\n"
"s_add_u32 s67, s75, 620672\n"
"v_mfma_f32_16x16x32_bf16 v[172:175], a[72:75], a[96:99], v[172:175]\n"
"v_mfma_f32_16x16x32_bf16 v[176:179], a[72:75], a[104:107], v[176:179]\n"
"v_mfma_f32_16x16x32_bf16 v[176:179], a[76:79], a[108:111], v[176:179]\n"
"buffer_load_dwordx4 v11, s[52:55], s67 offen offset:2048 lds\n"
"v_mfma_f32_16x16x32_bf16 v[148:151], a[68:71], a[108:111], v[148:151]\n"
"v_mfma_f32_16x16x32_bf16 v[148:151], a[64:67], a[104:107], v[148:151]\n"
"s_add_u32 s67, s75, 636032\n"
"v_mfma_f32_16x16x32_bf16 v[152:155], a[64:67], a[112:115], v[152:155]\n"
"s_sub_u32 s66, 0, s73\n"
"v_mfma_f32_16x16x32_bf16 v[152:155], a[68:71], a[116:119], v[152:155]\n"
"buffer_load_dwordx4 v11, s[52:55], s67 offen offset:3072 lds\n"
"v_mfma_f32_16x16x32_bf16 v[180:183], a[76:79], a[116:119], v[180:183]\n"
"v_add_u32 v12, s66, v7\n"
"v_mfma_f32_16x16x32_bf16 v[180:183], a[72:75], a[112:115], v[180:183]\n"
"s_add_u32 m0, s72, 57344\n"
"v_mfma_f32_16x16x32_bf16 v[184:187], a[72:75], a[120:123], v[184:187]\n"
"v_add_u32 v13, s66, v8\n"
"v_mfma_f32_16x16x32_bf16 v[184:187], a[76:79], a[124:127], v[184:187]\n"
"s_add_u32 s67, s60, 640\n"
"v_mfma_f32_16x16x32_bf16 v[156:159], a[68:71], a[124:127], v[156:159]\n"
"v_mfma_f32_16x16x32_bf16 v[156:159], a[64:67], a[120:123], v[156:159]\n"
"buffer_load_dwordx4 v6, s[48:51], s67 offen offset:0  lds\n"
"v_mfma_f32_16x16x32_bf16 v[160:163], a[64:67], a[128:131], v[160:163]\n"
"v_mfma_f32_16x16x32_bf16 v[160:163], a[68:71], a[132:135], v[160:163]\n"
"s_add_u32 s67, s60, 16000\n"
"v_mfma_f32_16x16x32_bf16 v[188:191], a[76:79], a[132:135], v[188:191]\n"
"v_mfma_f32_16x16x32_bf16 v[188:191], a[72:75], a[128:131], v[188:191]\n"
"buffer_load_dwordx4 v6, s[48:51], s67 offen offset:1024  lds\n"
"v_mfma_f32_16x16x32_bf16 v[192:195], a[72:75], a[136:139], v[192:195]\n"
"v_mfma_f32_16x16x32_bf16 v[192:195], a[76:79], a[140:143], v[192:195]\n"
"s_add_u32 s67, s60, 31360\n"
"v_mfma_f32_16x16x32_bf16 v[164:167], a[68:71], a[140:143], v[164:167]\n"
"v_mfma_f32_16x16x32_bf16 v[164:167], a[64:67], a[136:139], v[164:167]\n"
"buffer_load_dwordx4 v6, s[48:51], s67 offen offset:2048  lds\n"
"v_mfma_f32_16x16x32_bf16 v[168:171], a[64:67], a[144:147], v[168:171]\n"
"v_mfma_f32_16x16x32_bf16 v[168:171], a[68:71], a[148:151], v[168:171]\n"
"s_add_u32 s67, s60, 46720\n"
"v_mfma_f32_16x16x32_bf16 v[196:199], a[76:79], a[148:151], v[196:199]\n"
"v_mfma_f32_16x16x32_bf16 v[196:199], a[72:75], a[144:147], v[196:199]\n"
"v_mfma_f32_16x16x32_bf16 a[20:23], a[72:75], a[152:155], a[20:23]\n"
"buffer_load_dwordx4 v6, s[48:51], s67 offen offset:3072  lds\n"
"v_mfma_f32_16x16x32_bf16 a[20:23], a[76:79], a[156:159], a[20:23]\n"
"s_sub_u32 s70, 1048576, s75\n"
"v_and_b32 v22, 0x20000, v14\n"
"v_mfma_f32_16x16x32_bf16 a[16:19], a[68:71], a[156:159], a[16:19]\n"
"v_add_u32 v14, v22, v14\n"
"v_mfma_f32_16x16x32_bf16 a[16:19], a[64:67], a[152:155], a[16:19]\n"
"s_waitcnt vmcnt(22)\n"
"s_barrier\n"
"v_mfma_f32_16x16x32_bf16 v[200:203], a[80:83], a[96:99], v[200:203]\n"
"s_waitcnt vmcnt(12)\n"
"ds_read_b64_tr_b16 a[32:33], v9 offset:8192\n"
"v_mfma_f32_16x16x32_bf16 v[200:203], a[84:87], a[100:103], v[200:203]\n"
"s_add_u32 s70, s70, s65\n"
"ds_read_b64_tr_b16 a[34:35], v9 offset:8704\n"
"buffer_load_dword v23, v14, s[52:55], s70 offen\n"
"v_mfma_f32_16x16x32_bf16 v[228:231], a[92:95], a[100:103], v[228:231]\n"
"ds_read_b128 a[100:103], v13 offset:16384\n"
"v_mfma_f32_16x16x32_bf16 v[228:231], a[88:91], a[96:99], v[228:231]\n"
"ds_read_b128 a[96:99], v12 offset:16384\n"
"v_mfma_f32_16x16x32_bf16 v[232:235], a[88:91], a[104:107], v[232:235]\n"
"ds_read_b64_tr_b16 a[36:37], v9 offset:12288\n"
"v_mfma_f32_16x16x32_bf16 v[232:235], a[92:95], a[108:111], v[232:235]\n"
"ds_read_b64_tr_b16 a[38:39], v9 offset:12800\n"
"v_mfma_f32_16x16x32_bf16 v[204:207], a[84:87], a[108:111], v[204:207]\n"
"ds_read_b128 a[108:111], v13 offset:18432\n"
"v_mfma_f32_16x16x32_bf16 v[204:207], a[80:83], a[104:107], v[204:207]\n"
"buffer_store_dwordx4 a[168:171], v1, s[36:39], %22 offen offset:0 sc1\n"
"ds_read_b128 a[104:107], v12 offset:18432\n"
"ds_read_b64_tr_b16 a[40:41], v9 offset:8200\n"
"v_mfma_f32_16x16x32_bf16 v[208:211], a[80:83], a[112:115], v[208:211]\n"
"ds_read_b64_tr_b16 a[42:43], v9 offset:8712\n"
"v_mfma_f32_16x16x32_bf16 v[208:211], a[84:87], a[116:119], v[208:211]\n"
"ds_read_b64_tr_b16 a[44:45], v9 offset:12296\n"
"v_mfma_f32_16x16x32_bf16 v[236:239], a[92:95], a[116:119], v[236:239]\n"
"ds_read_b128 a[116:119], v13 offset:20480\n"
"v_mfma_f32_16x16x32_bf16 v[236:239], a[88:91], a[112:115], v[236:239]\n"
"ds_read_b128 a[112:115], v12 offset:20480\n"
"v_mfma_f32_16x16x32_bf16 v[240:243], a[88:91], a[120:123], v[240:243]\n"
"s_add_u32 m0, s64, 8192\n"
"ds_read_b64_tr_b16 a[46:47], v9 offset:12808\n"
"v_mfma_f32_16x16x32_bf16 v[240:243], a[92:95], a[124:127], v[240:243]\n"
"ds_read_b64_tr_b16 a[48:49], v15 offset:8192\n"
"v_mfma_f32_16x16x32_bf16 v[212:215], a[84:87], a[124:127], v[212:215]\n"
"ds_read_b128 a[124:127], v13 offset:22528\n"
"v_mfma_f32_16x16x32_bf16 v[212:215], a[80:83], a[120:123], v[212:215]\n"
"ds_read_b128 a[120:123], v12 offset:22528\n"
"v_mfma_f32_16x16x32_bf16 v[216:219], a[80:83], a[128:131], v[216:219]\n"
"ds_read_b64_tr_b16 a[50:51], v15 offset:8704\n"
"v_mfma_f32_16x16x32_bf16 v[216:219], a[84:87], a[132:135], v[216:219]\n"
"ds_read_b64_tr_b16 a[52:53], v15 offset:12288\n"
"v_mfma_f32_16x16x32_bf16 v[244:247], a[92:95], a[132:135], v[244:247]\n"
"ds_read_b128 a[132:135], v13 offset:24576\n"
"v_mfma_f32_16x16x32_bf16 v[244:247], a[88:91], a[128:131], v[244:247]\n"
"ds_read_b128 a[128:131], v12 offset:24576\n"
"v_mfma_f32_16x16x32_bf16 v[248:251], a[88:91], a[136:139], v[248:251]\n"
"ds_read_b64_tr_b16 a[54:55], v15 offset:12800\n"
"v_mfma_f32_16x16x32_bf16 v[248:251], a[92:95], a[140:143], v[248:251]\n"
"ds_read_b64_tr_b16 a[56:57], v15 offset:8200\n"
"v_mfma_f32_16x16x32_bf16 v[220:223], a[84:87], a[140:143], v[220:223]\n"
"s_add_u32 s67, s75, 655360\n"
"ds_read_b128 a[140:143], v13 offset:26624\n"
"v_mfma_f32_16x16x32_bf16 v[220:223], a[80:83], a[136:139], v[220:223]\n"
"ds_read_b128 a[136:139], v12 offset:26624\n"
"v_mfma_f32_16x16x32_bf16 v[224:227], a[80:83], a[144:147], v[224:227]\n"
"ds_read_b64_tr_b16 a[58:59], v15 offset:8712\n"
"v_mfma_f32_16x16x32_bf16 v[224:227], a[84:87], a[148:151], v[224:227]\n"
"ds_read_b64_tr_b16 a[60:61], v15 offset:12296\n"
"v_mfma_f32_16x16x32_bf16 v[252:255], a[92:95], a[148:151], v[252:255]\n"
"ds_read_b128 a[148:151], v13 offset:28672\n"
"v_mfma_f32_16x16x32_bf16 v[252:255], a[88:91], a[144:147], v[252:255]\n"
"ds_read_b128 a[144:147], v12 offset:28672\n"
"ds_read_b64_tr_b16 a[62:63], v15 offset:12808\n"
"v_mfma_f32_16x16x32_bf16 a[28:31], a[88:91], a[152:155], a[28:31]\n"
"v_mfma_f32_16x16x32_bf16 a[28:31], a[92:95], a[156:159], a[28:31]\n"
"v_mfma_f32_16x16x32_bf16 a[24:27], a[84:87], a[156:159], a[24:27]\n"
"ds_read_b128 a[156:159], v13 offset:30720\n"
"v_mfma_f32_16x16x32_bf16 a[24:27], a[80:83], a[152:155], a[24:27]\n"
"s_waitcnt lgkmcnt(15)\n"
"ds_read_b128 a[152:155], v12 offset:30720\n"
"v_mfma_f32_16x16x32_bf16 v[32:35], a[32:35], a[96:99], v[32:35]\n"
"v_mfma_f32_16x16x32_bf16 v[32:35], a[36:39], a[100:103], v[32:35]\n"
"s_waitcnt lgkmcnt(2)\n"
"v_mfma_f32_16x16x32_bf16 v[60:63], a[44:47], a[100:103], v[60:63]\n"
"buffer_load_dwordx4 v11, s[52:55], s67 offen offset:0 lds\n"
"v_mfma_f32_16x16x32_bf16 v[60:63], a[40:43], a[96:99], v[60:63]\n"
"v_mfma_f32_16x16x32_bf16 v[64:67], a[40:43], a[104:107], v[64:67]\n"
"s_add_u32 s67, s75, 670720\n"
"v_mfma_f32_16x16x32_bf16 v[64:67], a[44:47], a[108:111], v[64:67]\n"
"v_mfma_f32_16x16x32_bf16 v[36:39], a[36:39], a[108:111], v[36:39]\n"
"buffer_load_dwordx4 v11, s[52:55], s67 offen offset:1024 lds\n"
"v_mfma_f32_16x16x32_bf16 v[36:39], a[32:35], a[104:107], v[36:39]\n"
"v_mfma_f32_16x16x32_bf16 v[40:43], a[32:35], a[112:115], v[40:43]\n"
"s_add_u32 s67, s75, 686080\n"
"v_mfma_f32_16x16x32_bf16 v[40:43], a[36:39], a[116:119], v[40:43]\n"
"v_mfma_f32_16x16x32_bf16 v[68:71], a[44:47], a[116:119], v[68:71]\n"
"v_mfma_f32_16x16x32_bf16 v[68:71], a[40:43], a[112:115], v[68:71]\n"
"buffer_load_dwordx4 v11, s[52:55], s67 offen offset:2048 lds\n"
"v_mfma_f32_16x16x32_bf16 v[72:75], a[40:43], a[120:123], v[72:75]\n"
"v_mfma_f32_16x16x32_bf16 v[72:75], a[44:47], a[124:127], v[72:75]\n"
"s_add_u32 s67, s75, 701440\n"
"v_mfma_f32_16x16x32_bf16 v[44:47], a[36:39], a[124:127], v[44:47]\n"
"v_mfma_f32_16x16x32_bf16 v[44:47], a[32:35], a[120:123], v[44:47]\n"
"v_mfma_f32_16x16x32_bf16 v[48:51], a[32:35], a[128:131], v[48:51]\n"
"buffer_load_dwordx4 v11, s[52:55], s67 offen offset:3072 lds\n"
"v_mfma_f32_16x16x32_bf16 v[48:51], a[36:39], a[132:135], v[48:51]\n"
"s_add_u32 m0, s64, 12288\n"
"v_mfma_f32_16x16x32_bf16 v[76:79], a[44:47], a[132:135], v[76:79]\n"
"s_waitcnt vmcnt(10)\n"
"ds_read_b64_tr_b16 a[64:65], v9 offset:0\n"
"v_mfma_f32_16x16x32_bf16 v[76:79], a[40:43], a[128:131], v[76:79]\n"
"s_add_u32 s67, s75, 720896\n"
"ds_read_b64_tr_b16 a[66:67], v9 offset:512\n"
"v_mfma_f32_16x16x32_bf16 v[80:83], a[40:43], a[136:139], v[80:83]\n"
"ds_read_b64_tr_b16 a[68:69], v9 offset:4096\n"
"v_mfma_f32_16x16x32_bf16 v[80:83], a[44:47], a[140:143], v[80:83]\n"
"ds_read_b64_tr_b16 a[70:71], v9 offset:4608\n"
"buffer_load_dwordx4 v11, s[52:55], s67 offen offset:0 lds\n"
"v_mfma_f32_16x16x32_bf16 v[52:55], a[36:39], a[140:143], v[52:55]\n"
"s_add_u32 s67, s75, 736256\n"
"ds_read_b64_tr_b16 a[72:73], v9 offset:8\n"
"v_mfma_f32_16x16x32_bf16 v[52:55], a[32:35], a[136:139], v[52:55]\n"
"s_waitcnt lgkmcnt(5)\n"
"ds_read_b64_tr_b16 a[74:75], v9 offset:520\n"
"v_mfma_f32_16x16x32_bf16 v[56:59], a[32:35], a[144:147], v[56:59]\n"
"ds_read_b64_tr_b16 a[76:77], v9 offset:4104\n"
"v_mfma_f32_16x16x32_bf16 v[56:59], a[36:39], a[148:151], v[56:59]\n"
"ds_read_b64_tr_b16 a[78:79], v9 offset:4616\n"
"v_mfma_f32_16x16x32_bf16 v[84:87], a[44:47], a[148:151], v[84:87]\n"
"buffer_load_dwordx4 v11, s[52:55], s67 offen offset:1024 lds\n"
"v_mfma_f32_16x16x32_bf16 v[84:87], a[40:43], a[144:147], v[84:87]\n"
"ds_read_b64_tr_b16 a[80:81], v15 offset:0\n"
"v_mfma_f32_16x16x32_bf16 a[4:7], a[40:43], a[152:155], a[4:7]\n"
"s_add_u32 s67, s75, 751616\n"
"ds_read_b64_tr_b16 a[82:83], v15 offset:512\n"
"v_mfma_f32_16x16x32_bf16 a[4:7], a[44:47], a[156:159], a[4:7]\n"
"ds_read_b64_tr_b16 a[84:85], v15 offset:4096\n"
"v_mfma_f32_16x16x32_bf16 a[0:3], a[36:39], a[156:159], a[0:3]\n"
"ds_read_b64_tr_b16 a[86:87], v15 offset:4608\n"
"buffer_load_dwordx4 v11, s[52:55], s67 offen offset:2048 lds\n"
"v_mfma_f32_16x16x32_bf16 a[0:3], a[32:35], a[152:155], a[0:3]\n"
"ds_read_b64_tr_b16 a[88:89], v15 offset:8\n"
"v_mfma_f32_16x16x32_bf16 v[88:91], a[48:51], a[96:99], v[88:91]\n"
"s_add_u32 s67, s75, 766976\n"
"ds_read_b64_tr_b16 a[90:91], v15 offset:520\n"
"v_mfma_f32_16x16x32_bf16 v[88:91], a[52:55], a[100:103], v[88:91]\n"
"ds_read_b64_tr_b16 a[92:93], v15 offset:4104\n"
"v_mfma_f32_16x16x32_bf16 v[116:119], a[60:63], a[100:103], v[116:119]\n"
"ds_read_b64_tr_b16 a[94:95], v15 offset:4616\n"
"v_mfma_f32_16x16x32_bf16 v[116:119], a[56:59], a[96:99], v[116:119]\n"
"buffer_load_dwordx4 v11, s[52:55], s67 offen offset:3072 lds\n"
"v_mfma_f32_16x16x32_bf16 v[120:123], a[56:59], a[104:107], v[120:123]\n"
"v_mfma_f32_16x16x32_bf16 v[120:123], a[60:63], a[108:111], v[120:123]\n"
"s_add_u32 m0, s64, 0\n"
"v_mfma_f32_16x16x32_bf16 v[92:95], a[52:55], a[108:111], v[92:95]\n"
"v_mfma_f32_16x16x32_bf16 v[92:95], a[48:51], a[104:107], v[92:95]\n"
"s_add_u32 s67, s75, 655488\n"
"v_mfma_f32_16x16x32_bf16 v[96:99], a[48:51], a[112:115], v[96:99]\n"
"s_waitcnt lgkmcnt(0)\n"
"v_mfma_f32_16x16x32_bf16 v[96:99], a[52:55], a[116:119], v[96:99]\n"
"buffer_load_dwordx4 v11, s[52:55], s67 offen offset:0 lds\n"
"v_mfma_f32_16x16x32_bf16 v[124:127], a[60:63], a[116:119], v[124:127]\n"
"v_mfma_f32_16x16x32_bf16 v[124:127], a[56:59], a[112:115], v[124:127]\n"
"s_add_u32 s67, s75, 670848\n"
"v_mfma_f32_16x16x32_bf16 v[128:131], a[56:59], a[120:123], v[128:131]\n"
"v_mfma_f32_16x16x32_bf16 v[128:131], a[60:63], a[124:127], v[128:131]\n"
"buffer_load_dwordx4 v11, s[52:55], s67 offen offset:1024 lds\n"
"v_mfma_f32_16x16x32_bf16 v[100:103], a[52:55], a[124:127], v[100:103]\n"
"v_mfma_f32_16x16x32_bf16 v[100:103], a[48:51], a[120:123], v[100:103]\n"
"s_add_u32 s67, s75, 686208\n"
"v_mfma_f32_16x16x32_bf16 v[104:107], a[48:51], a[128:131], v[104:107]\n"
"v_mfma_f32_16x16x32_bf16 v[104:107], a[52:55], a[132:135], v[104:107]\n"
"v_mfma_f32_16x16x32_bf16 v[132:135], a[60:63], a[132:135], v[132:135]\n"
"buffer_load_dwordx4 v11, s[52:55], s67 offen offset:2048 lds\n"
"v_mfma_f32_16x16x32_bf16 v[132:135], a[56:59], a[128:131], v[132:135]\n"
"v_mfma_f32_16x16x32_bf16 v[136:139], a[56:59], a[136:139], v[136:139]\n"
"s_add_u32 s67, s75, 701568\n"
"v_mfma_f32_16x16x32_bf16 v[136:139], a[60:63], a[140:143], v[136:139]\n"
"v_mfma_f32_16x16x32_bf16 v[108:111], a[52:55], a[140:143], v[108:111]\n"
"buffer_load_dwordx4 v11, s[52:55], s67 offen offset:3072 lds\n"
"v_mfma_f32_16x16x32_bf16 v[108:111], a[48:51], a[136:139], v[108:111]\n"
"v_mfma_f32_16x16x32_bf16 v[112:115], a[48:51], a[144:147], v[112:115]\n"
"s_add_u32 m0, s64, 4096\n"
"v_mfma_f32_16x16x32_bf16 v[112:115], a[52:55], a[148:151], v[112:115]\n"
"v_mfma_f32_16x16x32_bf16 v[140:143], a[60:63], a[148:151], v[140:143]\n"
"s_add_u32 s67, s75, 721024\n"
"v_mfma_f32_16x16x32_bf16 v[140:143], a[56:59], a[144:147], v[140:143]\n"
"v_mfma_f32_16x16x32_bf16 a[12:15], a[56:59], a[152:155], a[12:15]\n"
"buffer_load_dwordx4 v11, s[52:55], s67 offen offset:0 lds\n"
"v_mfma_f32_16x16x32_bf16 a[12:15], a[60:63], a[156:159], a[12:15]\n"
"v_mfma_f32_16x16x32_bf16 a[8:11], a[52:55], a[156:159], a[8:11]\n"
"s_add_u32 s67, s75, 736384\n"
"v_mfma_f32_16x16x32_bf16 a[8:11], a[48:51], a[152:155], a[8:11]\n"
"v_mfma_f32_16x16x32_bf16 v[144:147], a[64:67], a[96:99], v[144:147]\n"
"buffer_load_dwordx4 v11, s[52:55], s67 offen offset:1024 lds\n"
"v_mfma_f32_16x16x32_bf16 v[144:147], a[68:71], a[100:103], v[144:147]\n"
"v_mfma_f32_16x16x32_bf16 v[172:175], a[76:79], a[100:103], v[172:175]\n"
"s_add_u32 s67, s75, 751744\n"
"v_mfma_f32_16x16x32_bf16 v[172:175], a[72:75], a[96:99], v[172:175]\n"
"v_mfma_f32_16x16x32_bf16 v[176:179], a[72:75], a[104:107], v[176:179]\n"
"buffer_load_dwordx4 v11, s[52:55], s67 offen offset:2048 lds\n"
"v_mfma_f32_16x16x32_bf16 v[176:179], a[76:79], a[108:111], v[176:179]\n"
"v_mfma_f32_16x16x32_bf16 v[148:151], a[68:71], a[108:111], v[148:151]\n"
"s_add_u32 s67, s75, 767104\n"
"v_mfma_f32_16x16x32_bf16 v[148:151], a[64:67], a[104:107], v[148:151]\n"
"v_mfma_f32_16x16x32_bf16 v[152:155], a[64:67], a[112:115], v[152:155]\n"
"s_sub_u32 s66, 40960, s73\n"
"v_mfma_f32_16x16x32_bf16 v[152:155], a[68:71], a[116:119], v[152:155]\n"
"buffer_load_dwordx4 v11, s[52:55], s67 offen offset:3072 lds\n"
"v_mfma_f32_16x16x32_bf16 v[180:183], a[76:79], a[116:119], v[180:183]\n"
"s_add_u32 m0, s72, 98304\n"
"v_add_u32 v12, s66, v7\n"
"v_mfma_f32_16x16x32_bf16 v[180:183], a[72:75], a[112:115], v[180:183]\n"
"v_mfma_f32_16x16x32_bf16 v[184:187], a[72:75], a[120:123], v[184:187]\n"
"s_add_u32 s67, s60, 768\n"
"v_add_u32 v13, s66, v8\n"
"v_mfma_f32_16x16x32_bf16 v[184:187], a[76:79], a[124:127], v[184:187]\n"
"v_mfma_f32_16x16x32_bf16 v[156:159], a[68:71], a[124:127], v[156:159]\n"
"buffer_load_dwordx4 v6, s[48:51], s67 offen offset:0  lds\n"
"v_mfma_f32_16x16x32_bf16 v[156:159], a[64:67], a[120:123], v[156:159]\n"
"v_mfma_f32_16x16x32_bf16 v[160:163], a[64:67], a[128:131], v[160:163]\n"
"s_add_u32 s67, s60, 16128\n"
"v_mfma_f32_16x16x32_bf16 v[160:163], a[68:71], a[132:135], v[160:163]\n"
"v_mfma_f32_16x16x32_bf16 v[188:191], a[76:79], a[132:135], v[188:191]\n"
"buffer_load_dwordx4 v6, s[48:51], s67 offen offset:1024  lds\n"
"v_mfma_f32_16x16x32_bf16 v[188:191], a[72:75], a[128:131], v[188:191]\n"
"v_mfma_f32_16x16x32_bf16 v[192:195], a[72:75], a[136:139], v[192:195]\n"
"s_add_u32 s67, s60, 31488\n"
"v_mfma_f32_16x16x32_bf16 v[192:195], a[76:79], a[140:143], v[192:195]\n"
"v_mfma_f32_16x16x32_bf16 v[164:167], a[68:71], a[140:143], v[164:167]\n"
"v_mfma_f32_16x16x32_bf16 v[164:167], a[64:67], a[136:139], v[164:167]\n"
"buffer_load_dwordx4 v6, s[48:51], s67 offen offset:2048  lds\n"
"v_mfma_f32_16x16x32_bf16 v[168:171], a[64:67], a[144:147], v[168:171]\n"
"v_mfma_f32_16x16x32_bf16 v[168:171], a[68:71], a[148:151], v[168:171]\n"
"s_add_u32 s67, s60, 46848\n"
"v_mfma_f32_16x16x32_bf16 v[196:199], a[76:79], a[148:151], v[196:199]\n"
"v_mfma_f32_16x16x32_bf16 v[196:199], a[72:75], a[144:147], v[196:199]\n"
"buffer_load_dwordx4 v6, s[48:51], s67 offen offset:3072  lds\n"
"v_mfma_f32_16x16x32_bf16 a[20:23], a[72:75], a[152:155], a[20:23]\n"
"v_mfma_f32_16x16x32_bf16 a[20:23], a[76:79], a[156:159], a[20:23]\n"
"s_sub_u32 s70, 1572864, s75\n"
"v_mfma_f32_16x16x32_bf16 a[16:19], a[68:71], a[156:159], a[16:19]\n"
"s_add_u32 s70, s70, s65\n"
"v_mfma_f32_16x16x32_bf16 a[16:19], a[64:67], a[152:155], a[16:19]\n"
"s_waitcnt vmcnt(22)\n"
"s_barrier\n"
"v_mfma_f32_16x16x32_bf16 v[200:203], a[80:83], a[96:99], v[200:203]\n"
"s_waitcnt vmcnt(12)\n"
"ds_read_b64_tr_b16 a[32:33], v9 offset:8192\n"
"v_mfma_f32_16x16x32_bf16 v[200:203], a[84:87], a[100:103], v[200:203]\n"
"ds_read_b64_tr_b16 a[34:35], v9 offset:8704\n"
"buffer_load_dword v23, v14, s[52:55], s70 offen\n"
"v_mfma_f32_16x16x32_bf16 v[228:231], a[92:95], a[100:103], v[228:231]\n"
"ds_read_b128 a[100:103], v13 offset:16384\n"
"v_mfma_f32_16x16x32_bf16 v[228:231], a[88:91], a[96:99], v[228:231]\n"
"ds_read_b128 a[96:99], v12 offset:16384\n"
"v_mfma_f32_16x16x32_bf16 v[232:235], a[88:91], a[104:107], v[232:235]\n"
"ds_read_b64_tr_b16 a[36:37], v9 offset:12288\n"
"v_mfma_f32_16x16x32_bf16 v[232:235], a[92:95], a[108:111], v[232:235]\n"
"ds_read_b64_tr_b16 a[38:39], v9 offset:12800\n"
"v_mfma_f32_16x16x32_bf16 v[204:207], a[84:87], a[108:111], v[204:207]\n"
"ds_read_b128 a[108:111], v13 offset:18432\n"
"v_mfma_f32_16x16x32_bf16 v[204:207], a[80:83], a[104:107], v[204:207]\n"
"buffer_store_dwordx4 a[172:175], v1, s[36:39], %22 offen offset:2048 sc1\n"
"ds_read_b128 a[104:107], v12 offset:18432\n"
"ds_read_b64_tr_b16 a[40:41], v9 offset:8200\n"
"v_mfma_f32_16x16x32_bf16 v[208:211], a[80:83], a[112:115], v[208:211]\n"
"ds_read_b64_tr_b16 a[42:43], v9 offset:8712\n"
"v_mfma_f32_16x16x32_bf16 v[208:211], a[84:87], a[116:119], v[208:211]\n"
"ds_read_b64_tr_b16 a[44:45], v9 offset:12296\n"
"v_mfma_f32_16x16x32_bf16 v[236:239], a[92:95], a[116:119], v[236:239]\n"
"ds_read_b128 a[116:119], v13 offset:20480\n"
"v_mfma_f32_16x16x32_bf16 v[236:239], a[88:91], a[112:115], v[236:239]\n"
"ds_read_b128 a[112:115], v12 offset:20480\n"
"v_mfma_f32_16x16x32_bf16 v[240:243], a[88:91], a[120:123], v[240:243]\n"
"s_add_u32 m0, s64, 8192\n"
"ds_read_b64_tr_b16 a[46:47], v9 offset:12808\n"
"v_mfma_f32_16x16x32_bf16 v[240:243], a[92:95], a[124:127], v[240:243]\n"
"ds_read_b64_tr_b16 a[48:49], v15 offset:8192\n"
"v_mfma_f32_16x16x32_bf16 v[212:215], a[84:87], a[124:127], v[212:215]\n"
"ds_read_b128 a[124:127], v13 offset:22528\n"
"v_mfma_f32_16x16x32_bf16 v[212:215], a[80:83], a[120:123], v[212:215]\n"
"ds_read_b128 a[120:123], v12 offset:22528\n"
"v_mfma_f32_16x16x32_bf16 v[216:219], a[80:83], a[128:131], v[216:219]\n"
"ds_read_b64_tr_b16 a[50:51], v15 offset:8704\n"
"v_mfma_f32_16x16x32_bf16 v[216:219], a[84:87], a[132:135], v[216:219]\n"
"ds_read_b64_tr_b16 a[52:53], v15 offset:12288\n"
"v_mfma_f32_16x16x32_bf16 v[244:247], a[92:95], a[132:135], v[244:247]\n"
"ds_read_b128 a[132:135], v13 offset:24576\n"
"v_mfma_f32_16x16x32_bf16 v[244:247], a[88:91], a[128:131], v[244:247]\n"
"ds_read_b128 a[128:131], v12 offset:24576\n"
"v_mfma_f32_16x16x32_bf16 v[248:251], a[88:91], a[136:139], v[248:251]\n"
"ds_read_b64_tr_b16 a[54:55], v15 offset:12800\n"
"v_mfma_f32_16x16x32_bf16 v[248:251], a[92:95], a[140:143], v[248:251]\n"
"ds_read_b64_tr_b16 a[56:57], v15 offset:8200\n"
"v_mfma_f32_16x16x32_bf16 v[220:223], a[84:87], a[140:143], v[220:223]\n"
"s_add_u32 s67, s75, 786432\n"
"ds_read_b128 a[140:143], v13 offset:26624\n"
"v_mfma_f32_16x16x32_bf16 v[220:223], a[80:83], a[136:139], v[220:223]\n"
"ds_read_b128 a[136:139], v12 offset:26624\n"
"v_mfma_f32_16x16x32_bf16 v[224:227], a[80:83], a[144:147], v[224:227]\n"
"ds_read_b64_tr_b16 a[58:59], v15 offset:8712\n"
"v_mfma_f32_16x16x32_bf16 v[224:227], a[84:87], a[148:151], v[224:227]\n"
"ds_read_b64_tr_b16 a[60:61], v15 offset:12296\n"
"v_mfma_f32_16x16x32_bf16 v[252:255], a[92:95], a[148:151], v[252:255]\n"
"ds_read_b128 a[148:151], v13 offset:28672\n"
"v_mfma_f32_16x16x32_bf16 v[252:255], a[88:91], a[144:147], v[252:255]\n"
"ds_read_b128 a[144:147], v12 offset:28672\n"
"ds_read_b64_tr_b16 a[62:63], v15 offset:12808\n"
"v_mfma_f32_16x16x32_bf16 a[28:31], a[88:91], a[152:155], a[28:31]\n"
"v_mfma_f32_16x16x32_bf16 a[28:31], a[92:95], a[156:159], a[28:31]\n"
"v_mfma_f32_16x16x32_bf16 a[24:27], a[84:87], a[156:159], a[24:27]\n"
"ds_read_b128 a[156:159], v13 offset:30720\n"
"v_mfma_f32_16x16x32_bf16 a[24:27], a[80:83], a[152:155], a[24:27]\n"
"s_waitcnt lgkmcnt(15)\n"
"ds_read_b128 a[152:155], v12 offset:30720\n"
"v_mfma_f32_16x16x32_bf16 v[32:35], a[32:35], a[96:99], v[32:35]\n"
"v_mfma_f32_16x16x32_bf16 v[32:35], a[36:39], a[100:103], v[32:35]\n"
"s_waitcnt lgkmcnt(2)\n"
"v_mfma_f32_16x16x32_bf16 v[60:63], a[44:47], a[100:103], v[60:63]\n"
"buffer_load_dwordx4 v11, s[52:55], s67 offen offset:0 lds\n"
"v_mfma_f32_16x16x32_bf16 v[60:63], a[40:43], a[96:99], v[60:63]\n"
"v_mfma_f32_16x16x32_bf16 v[64:67], a[40:43], a[104:107], v[64:67]\n"
"s_add_u32 s67, s75, 801792\n"
"v_mfma_f32_16x16x32_bf16 v[64:67], a[44:47], a[108:111], v[64:67]\n"
"v_mfma_f32_16x16x32_bf16 v[36:39], a[36:39], a[108:111], v[36:39]\n"
"v_mfma_f32_16x16x32_bf16 v[36:39], a[32:35], a[104:107], v[36:39]\n"
"buffer_load_dwordx4 v11, s[52:55], s67 offen offset:1024 lds\n"
"v_mfma_f32_16x16x32_bf16 v[40:43], a[32:35], a[112:115], v[40:43]\n"
"v_mfma_f32_16x16x32_bf16 v[40:43], a[36:39], a[116:119], v[40:43]\n"
"s_add_u32 s67, s75, 817152\n"
"v_mfma_f32_16x16x32_bf16 v[68:71], a[44:47], a[116:119], v[68:71]\n"
"v_mfma_f32_16x16x32_bf16 v[68:71], a[40:43], a[112:115], v[68:71]\n"
"v_mfma_f32_16x16x32_bf16 v[72:75], a[40:43], a[120:123], v[72:75]\n"
"buffer_load_dwordx4 v11, s[52:55], s67 offen offset:2048 lds\n"
"v_mfma_f32_16x16x32_bf16 v[72:75], a[44:47], a[124:127], v[72:75]\n"
"v_mfma_f32_16x16x32_bf16 v[44:47], a[36:39], a[124:127], v[44:47]\n"
"s_add_u32 s67, s75, 832512\n"
"v_mfma_f32_16x16x32_bf16 v[44:47], a[32:35], a[120:123], v[44:47]\n"
"v_mfma_f32_16x16x32_bf16 v[48:51], a[32:35], a[128:131], v[48:51]\n"
"buffer_load_dwordx4 v11, s[52:55], s67 offen offset:3072 lds\n"
"v_mfma_f32_16x16x32_bf16 v[48:51], a[36:39], a[132:135], v[48:51]\n"
"s_add_u32 m0, s64, 12288\n"
"v_mfma_f32_16x16x32_bf16 v[76:79], a[44:47], a[132:135], v[76:79]\n"
"s_waitcnt vmcnt(10)\n"
"ds_read_b64_tr_b16 a[64:65], v9 offset:0\n"
"v_mfma_f32_16x16x32_bf16 v[76:79], a[40:43], a[128:131], v[76:79]\n"
"ds_read_b64_tr_b16 a[66:67], v9 offset:512\n"
"v_mfma_f32_16x16x32_bf16 v[80:83], a[40:43], a[136:139], v[80:83]\n"
"s_add_u32 s67, s75, 851968\n"
"ds_read_b64_tr_b16 a[68:69], v9 offset:4096\n"
"v_mfma_f32_16x16x32_bf16 v[80:83], a[44:47], a[140:143], v[80:83]\n"
"ds_read_b64_tr_b16 a[70:71], v9 offset:4608\n"
"v_mfma_f32_16x16x32_bf16 v[52:55], a[36:39], a[140:143], v[52:55]\n"
"ds_read_b64_tr_b16 a[72:73], v9 offset:8\n"
"buffer_load_dwordx4 v11, s[52:55], s67 offen offset:0 lds\n"
"v_mfma_f32_16x16x32_bf16 v[52:55], a[32:35], a[136:139], v[52:55]\n"
"s_waitcnt lgkmcnt(5)\n"
"ds_read_b64_tr_b16 a[74:75], v9 offset:520\n"
"v_mfma_f32_16x16x32_bf16 v[56:59], a[32:35], a[144:147], v[56:59]\n"
"s_add_u32 s67, s75, 867328\n"
"ds_read_b64_tr_b16 a[76:77], v9 offset:4104\n"
"v_mfma_f32_16x16x32_bf16 v[56:59], a[36:39], a[148:151], v[56:59]\n"
"ds_read_b64_tr_b16 a[78:79], v9 offset:4616\n"
"v_mfma_f32_16x16x32_bf16 v[84:87], a[44:47], a[148:151], v[84:87]\n"
"buffer_load_dwordx4 v11, s[52:55], s67 offen offset:1024 lds\n"
"v_mfma_f32_16x16x32_bf16 v[84:87], a[40:43], a[144:147], v[84:87]\n"
"ds_read_b64_tr_b16 a[80:81], v15 offset:0\n"
"v_mfma_f32_16x16x32_bf16 a[4:7], a[40:43], a[152:155], a[4:7]\n"
"s_add_u32 s67, s75, 882688\n"
"ds_read_b64_tr_b16 a[82:83], v15 offset:512\n"
"v_mfma_f32_16x16x32_bf16 a[4:7], a[44:47], a[156:159], a[4:7]\n"
"ds_read_b64_tr_b16 a[84:85], v15 offset:4096\n"
"v_mfma_f32_16x16x32_bf16 a[0:3], a[36:39], a[156:159], a[0:3]\n"
"ds_read_b64_tr_b16 a[86:87], v15 offset:4608\n"
"buffer_load_dwordx4 v11, s[52:55], s67 offen offset:2048 lds\n"
"v_mfma_f32_16x16x32_bf16 a[0:3], a[32:35], a[152:155], a[0:3]\n"
"ds_read_b64_tr_b16 a[88:89], v15 offset:8\n"
"v_mfma_f32_16x16x32_bf16 v[88:91], a[48:51], a[96:99], v[88:91]\n"
"s_add_u32 s67, s75, 898048\n"
"ds_read_b64_tr_b16 a[90:91], v15 offset:520\n"
"v_mfma_f32_16x16x32_bf16 v[88:91], a[52:55], a[100:103], v[88:91]\n"
"ds_read_b64_tr_b16 a[92:93], v15 offset:4104\n"
"v_mfma_f32_16x16x32_bf16 v[116:119], a[60:63], a[100:103], v[116:119]\n"
"ds_read_b64_tr_b16 a[94:95], v15 offset:4616\n"
"v_mfma_f32_16x16x32_bf16 v[116:119], a[56:59], a[96:99], v[116:119]\n"
"buffer_load_dwordx4 v11, s[52:55], s67 offen offset:3072 lds\n"
"v_mfma_f32_16x16x32_bf16 v[120:123], a[56:59], a[104:107], v[120:123]\n"
"v_mfma_f32_16x16x32_bf16 v[120:123], a[60:63], a[108:111], v[120:123]\n"
"s_add_u32 m0, s64, 0\n"
"v_mfma_f32_16x16x32_bf16 v[92:95], a[52:55], a[108:111], v[92:95]\n"
"v_mfma_f32_16x16x32_bf16 v[92:95], a[48:51], a[104:107], v[92:95]\n"
"s_add_u32 s67, s75, 786560\n"
"v_mfma_f32_16x16x32_bf16 v[96:99], a[48:51], a[112:115], v[96:99]\n"
"s_waitcnt lgkmcnt(0)\n"
"v_mfma_f32_16x16x32_bf16 v[96:99], a[52:55], a[116:119], v[96:99]\n"
"buffer_load_dwordx4 v11, s[52:55], s67 offen offset:0 lds\n"
"v_mfma_f32_16x16x32_bf16 v[124:127], a[60:63], a[116:119], v[124:127]\n"
"v_mfma_f32_16x16x32_bf16 v[124:127], a[56:59], a[112:115], v[124:127]\n"
"s_add_u32 s67, s75, 801920\n"
"v_mfma_f32_16x16x32_bf16 v[128:131], a[56:59], a[120:123], v[128:131]\n"
"v_mfma_f32_16x16x32_bf16 v[128:131], a[60:63], a[124:127], v[128:131]\n"
"v_mfma_f32_16x16x32_bf16 v[100:103], a[52:55], a[124:127], v[100:103]\n"
"buffer_load_dwordx4 v11, s[52:55], s67 offen offset:1024 lds\n"
"v_mfma_f32_16x16x32_bf16 v[100:103], a[48:51], a[120:123], v[100:103]\n"
"v_mfma_f32_16x16x32_bf16 v[104:107], a[48:51], a[128:131], v[104:107]\n"
"s_add_u32 s67, s75, 817280\n"
"v_mfma_f32_16x16x32_bf16 v[104:107], a[52:55], a[132:135], v[104:107]\n"
"v_mfma_f32_16x16x32_bf16 v[132:135], a[60:63], a[132:135], v[132:135]\n"
"buffer_load_dwordx4 v11, s[52:55], s67 offen offset:2048 lds\n"
"v_mfma_f32_16x16x32_bf16 v[132:135], a[56:59], a[128:131], v[132:135]\n"
"v_mfma_f32_16x16x32_bf16 v[136:139], a[56:59], a[136:139], v[136:139]\n"
"s_add_u32 s67, s75, 832640\n"
"v_mfma_f32_16x16x32_bf16 v[136:139], a[60:63], a[140:143], v[136:139]\n"
"v_mfma_f32_16x16x32_bf16 v[108:111], a[52:55], a[140:143], v[108:111]\n"
"v_mfma_f32_16x16x32_bf16 v[108:111], a[48:51], a[136:139], v[108:111]\n"
"buffer_load_dwordx4 v11, s[52:55], s67 offen offset:3072 lds\n"
"v_mfma_f32_16x16x32_bf16 v[112:115], a[48:51], a[144:147], v[112:115]\n"
"v_mfma_f32_16x16x32_bf16 v[112:115], a[52:55], a[148:151], v[112:115]\n"
"s_add_u32 m0, s64, 4096\n"
"v_mfma_f32_16x16x32_bf16 v[140:143], a[60:63], a[148:151], v[140:143]\n"
"v_mfma_f32_16x16x32_bf16 v[140:143], a[56:59], a[144:147], v[140:143]\n"
"s_add_u32 s67, s75, 852096\n"
"v_mfma_f32_16x16x32_bf16 a[12:15], a[56:59], a[152:155], a[12:15]\n"
"v_mfma_f32_16x16x32_bf16 a[12:15], a[60:63], a[156:159], a[12:15]\n"
"buffer_load_dwordx4 v11, s[52:55], s67 offen offset:0 lds\n"
"v_mfma_f32_16x16x32_bf16 a[8:11], a[52:55], a[156:159], a[8:11]\n"
"v_mfma_f32_16x16x32_bf16 a[8:11], a[48:51], a[152:155], a[8:11]\n"
"s_add_u32 s67, s75, 867456\n"
"v_mfma_f32_16x16x32_bf16 v[144:147], a[64:67], a[96:99], v[144:147]\n"
"v_mfma_f32_16x16x32_bf16 v[144:147], a[68:71], a[100:103], v[144:147]\n"
"buffer_load_dwordx4 v11, s[52:55], s67 offen offset:1024 lds\n"
"v_mfma_f32_16x16x32_bf16 v[172:175], a[76:79], a[100:103], v[172:175]\n"
"v_mfma_f32_16x16x32_bf16 v[172:175], a[72:75], a[96:99], v[172:175]\n"
"s_add_u32 s67, s75, 882816\n"
"v_mfma_f32_16x16x32_bf16 v[176:179], a[72:75], a[104:107], v[176:179]\n"
"v_mfma_f32_16x16x32_bf16 v[176:179], a[76:79], a[108:111], v[176:179]\n"
"buffer_load_dwordx4 v11, s[52:55], s67 offen offset:2048 lds\n"
"v_mfma_f32_16x16x32_bf16 v[148:151], a[68:71], a[108:111], v[148:151]\n"
"v_mfma_f32_16x16x32_bf16 v[148:151], a[64:67], a[104:107], v[148:151]\n"
"s_add_u32 s67, s75, 898176\n"
"v_mfma_f32_16x16x32_bf16 v[152:155], a[64:67], a[112:115], v[152:155]\n"
"s_sub_u32 s66, 81920, s73\n"
"v_mfma_f32_16x16x32_bf16 v[152:155], a[68:71], a[116:119], v[152:155]\n"
"buffer_load_dwordx4 v11, s[52:55], s67 offen offset:3072 lds\n"
"v_mfma_f32_16x16x32_bf16 v[180:183], a[76:79], a[116:119], v[180:183]\n"
"v_add_u32 v12, s66, v7\n"
"v_mfma_f32_16x16x32_bf16 v[180:183], a[72:75], a[112:115], v[180:183]\n"
"s_add_u32 m0, s72, 139264\n"
"v_mfma_f32_16x16x32_bf16 v[184:187], a[72:75], a[120:123], v[184:187]\n"
"v_add_u32 v13, s66, v8\n"
"v_mfma_f32_16x16x32_bf16 v[184:187], a[76:79], a[124:127], v[184:187]\n"
"s_add_u32 s67, s60, 896\n"
"v_mfma_f32_16x16x32_bf16 v[156:159], a[68:71], a[124:127], v[156:159]\n"
"v_mfma_f32_16x16x32_bf16 v[156:159], a[64:67], a[120:123], v[156:159]\n"
"buffer_load_dwordx4 v6, s[48:51], s67 offen offset:0  lds\n"
"v_mfma_f32_16x16x32_bf16 v[160:163], a[64:67], a[128:131], v[160:163]\n"
"v_mfma_f32_16x16x32_bf16 v[160:163], a[68:71], a[132:135], v[160:163]\n"
"s_add_u32 s67, s60, 16256\n"
"v_mfma_f32_16x16x32_bf16 v[188:191], a[76:79], a[132:135], v[188:191]\n"
"v_mfma_f32_16x16x32_bf16 v[188:191], a[72:75], a[128:131], v[188:191]\n"
"buffer_load_dwordx4 v6, s[48:51], s67 offen offset:1024  lds\n"
"v_mfma_f32_16x16x32_bf16 v[192:195], a[72:75], a[136:139], v[192:195]\n"
"v_mfma_f32_16x16x32_bf16 v[192:195], a[76:79], a[140:143], v[192:195]\n"
"s_add_u32 s67, s60, 31616\n"
"v_mfma_f32_16x16x32_bf16 v[164:167], a[68:71], a[140:143], v[164:167]\n"
"v_mfma_f32_16x16x32_bf16 v[164:167], a[64:67], a[136:139], v[164:167]\n"
"buffer_load_dwordx4 v6, s[48:51], s67 offen offset:2048  lds\n"
"v_mfma_f32_16x16x32_bf16 v[168:171], a[64:67], a[144:147], v[168:171]\n"
"v_mfma_f32_16x16x32_bf16 v[168:171], a[68:71], a[148:151], v[168:171]\n"
"s_add_u32 s67, s60, 46976\n"
"v_mfma_f32_16x16x32_bf16 v[196:199], a[76:79], a[148:151], v[196:199]\n"
"v_mfma_f32_16x16x32_bf16 v[196:199], a[72:75], a[144:147], v[196:199]\n"
"v_mfma_f32_16x16x32_bf16 a[20:23], a[72:75], a[152:155], a[20:23]\n"
"buffer_load_dwordx4 v6, s[48:51], s67 offen offset:3072  lds\n"
"v_mfma_f32_16x16x32_bf16 a[20:23], a[76:79], a[156:159], a[20:23]\n"
"v_mfma_f32_16x16x32_bf16 a[16:19], a[68:71], a[156:159], a[16:19]\n"
"s_add_u32 m0, s64, 32640\n"
"v_mfma_f32_16x16x32_bf16 a[16:19], a[64:67], a[152:155], a[16:19]\n"
"s_waitcnt vmcnt(22)\n"
"s_barrier\n"
"v_mfma_f32_16x16x32_bf16 v[200:203], a[80:83], a[96:99], v[200:203]\n"
"s_waitcnt vmcnt(12)\n"
"ds_read_b64_tr_b16 a[32:33], v9 offset:8192\n"
"buffer_load_dwordx4 v6, s[40:43], s80 offen offset:128  lds\n"
"v_mfma_f32_16x16x32_bf16 v[200:203], a[84:87], a[100:103], v[200:203]\n"
"ds_read_b64_tr_b16 a[34:35], v9 offset:8704\n"
"v_mfma_f32_16x16x32_bf16 v[228:231], a[92:95], a[100:103], v[228:231]\n"
"ds_read_b128 a[100:103], v13 offset:16384\n"
"v_mfma_f32_16x16x32_bf16 v[228:231], a[88:91], a[96:99], v[228:231]\n"
"ds_read_b128 a[96:99], v12 offset:16384\n"
"v_mfma_f32_16x16x32_bf16 v[232:235], a[88:91], a[104:107], v[232:235]\n"
"ds_read_b64_tr_b16 a[36:37], v9 offset:12288\n"
"buffer_load_dwordx4 v6, s[40:43], s81 offen offset:1152  lds\n"
"v_mfma_f32_16x16x32_bf16 v[232:235], a[92:95], a[108:111], v[232:235]\n"
"ds_read_b64_tr_b16 a[38:39], v9 offset:12800\n"
"v_mfma_f32_16x16x32_bf16 v[204:207], a[84:87], a[108:111], v[204:207]\n"
"ds_read_b128 a[108:111], v13 offset:18432\n"
"v_mfma_f32_16x16x32_bf16 v[204:207], a[80:83], a[104:107], v[204:207]\n"
"ds_read_b128 a[104:107], v12 offset:18432\n"
"ds_read_b64_tr_b16 a[40:41], v9 offset:8200\n"
"v_mfma_f32_16x16x32_bf16 v[208:211], a[80:83], a[112:115], v[208:211]\n"
"ds_read_b64_tr_b16 a[42:43], v9 offset:8712\n"
"v_mfma_f32_16x16x32_bf16 v[208:211], a[84:87], a[116:119], v[208:211]\n"
"ds_read_b64_tr_b16 a[44:45], v9 offset:12296\n"
"buffer_load_dwordx4 v6, s[40:43], s82 offen offset:2176  lds\n"
"v_mfma_f32_16x16x32_bf16 v[236:239], a[92:95], a[116:119], v[236:239]\n"
"ds_read_b128 a[116:119], v13 offset:20480\n"
"v_mfma_f32_16x16x32_bf16 v[236:239], a[88:91], a[112:115], v[236:239]\n"
"ds_read_b128 a[112:115], v12 offset:20480\n"
"v_mfma_f32_16x16x32_bf16 v[240:243], a[88:91], a[120:123], v[240:243]\n"
"ds_read_b64_tr_b16 a[46:47], v9 offset:12808\n"
"v_mfma_f32_16x16x32_bf16 v[240:243], a[92:95], a[124:127], v[240:243]\n"
"ds_read_b64_tr_b16 a[48:49], v15 offset:8192\n"
"buffer_load_dwordx4 v6, s[40:43], s83 offen offset:3200  lds\n"
"v_mfma_f32_16x16x32_bf16 v[212:215], a[84:87], a[124:127], v[212:215]\n"
"ds_read_b128 a[124:127], v13 offset:22528\n"
"v_mfma_f32_16x16x32_bf16 v[212:215], a[80:83], a[120:123], v[212:215]\n"
"ds_read_b128 a[120:123], v12 offset:22528\n"
"v_mfma_f32_16x16x32_bf16 v[216:219], a[80:83], a[128:131], v[216:219]\n"
"ds_read_b64_tr_b16 a[50:51], v15 offset:8704\n"
"v_mfma_f32_16x16x32_bf16 v[216:219], a[84:87], a[132:135], v[216:219]\n"
"ds_read_b64_tr_b16 a[52:53], v15 offset:12288\n"
"v_mfma_f32_16x16x32_bf16 v[244:247], a[92:95], a[132:135], v[244:247]\n"
"ds_read_b128 a[132:135], v13 offset:24576\n"
"buffer_store_dword v16, v17, s[56:59], %11 offen sc1\n"
"v_mfma_f32_16x16x32_bf16 v[244:247], a[88:91], a[128:131], v[244:247]\n"
"ds_read_b128 a[128:131], v12 offset:24576\n"
"v_mfma_f32_16x16x32_bf16 v[248:251], a[88:91], a[136:139], v[248:251]\n"
"ds_read_b64_tr_b16 a[54:55], v15 offset:12800\n"
"v_mfma_f32_16x16x32_bf16 v[248:251], a[92:95], a[140:143], v[248:251]\n"
"ds_read_b64_tr_b16 a[56:57], v15 offset:8200\n"
"v_mfma_f32_16x16x32_bf16 v[220:223], a[84:87], a[140:143], v[220:223]\n"
"ds_read_b128 a[140:143], v13 offset:26624\n"
"buffer_store_dword v16, v17, s[56:59], s69 offen sc1\n"
"v_mfma_f32_16x16x32_bf16 v[220:223], a[80:83], a[136:139], v[220:223]\n"
"ds_read_b128 a[136:139], v12 offset:26624\n"
"v_mfma_f32_16x16x32_bf16 v[224:227], a[80:83], a[144:147], v[224:227]\n"
"s_add_u32 s66, s78, 1\n"
"ds_read_b64_tr_b16 a[58:59], v15 offset:8712\n"
"v_mfma_f32_16x16x32_bf16 v[224:227], a[84:87], a[148:151], v[224:227]\n"
"s_add_u32 m0, s64, 8192\n"
"ds_read_b64_tr_b16 a[60:61], v15 offset:12296\n"
"v_mfma_f32_16x16x32_bf16 v[252:255], a[92:95], a[148:151], v[252:255]\n"
"ds_read_b128 a[148:151], v13 offset:28672\n"
"v_mov_b32 v28, s66\n"
"v_mfma_f32_16x16x32_bf16 v[252:255], a[88:91], a[144:147], v[252:255]\n"
"ds_read_b128 a[144:147], v12 offset:28672\n"
"ds_read_b64_tr_b16 a[62:63], v15 offset:12808\n"
"v_mov_b32 v29, s79\n"
"v_mfma_f32_16x16x32_bf16 a[28:31], a[88:91], a[152:155], a[28:31]\n"
"v_mfma_f32_16x16x32_bf16 a[28:31], a[92:95], a[156:159], a[28:31]\n"
"buffer_store_dwordx2 v[28:29], off, s[56:59], %24 sc1\n"
"s_add_u32 s67, s75, 917504\n"
"v_mfma_f32_16x16x32_bf16 a[24:27], a[84:87], a[156:159], a[24:27]\n"
"ds_read_b128 a[156:159], v13 offset:30720\n"
"v_mfma_f32_16x16x32_bf16 a[24:27], a[80:83], a[152:155], a[24:27]\n"
"s_waitcnt lgkmcnt(15)\n"
"ds_read_b128 a[152:155], v12 offset:30720\n"
"v_mfma_f32_16x16x32_bf16 v[32:35], a[32:35], a[96:99], v[32:35]\n"
"v_mfma_f32_16x16x32_bf16 v[32:35], a[36:39], a[100:103], v[32:35]\n"
"s_waitcnt lgkmcnt(2)\n"
"v_mfma_f32_16x16x32_bf16 v[60:63], a[44:47], a[100:103], v[60:63]\n"
"buffer_load_dwordx4 v11, s[52:55], s67 offen offset:0 lds\n"
"v_mfma_f32_16x16x32_bf16 v[60:63], a[40:43], a[96:99], v[60:63]\n"
"v_mfma_f32_16x16x32_bf16 v[64:67], a[40:43], a[104:107], v[64:67]\n"
"s_add_u32 s67, s75, 932864\n"
"v_mfma_f32_16x16x32_bf16 v[64:67], a[44:47], a[108:111], v[64:67]\n"
"v_mfma_f32_16x16x32_bf16 v[36:39], a[36:39], a[108:111], v[36:39]\n"
"v_mfma_f32_16x16x32_bf16 v[36:39], a[32:35], a[104:107], v[36:39]\n"
"buffer_load_dwordx4 v11, s[52:55], s67 offen offset:1024 lds\n"
"v_mfma_f32_16x16x32_bf16 v[40:43], a[32:35], a[112:115], v[40:43]\n"
"v_mfma_f32_16x16x32_bf16 v[40:43], a[36:39], a[116:119], v[40:43]\n"
"s_add_u32 s67, s75, 948224\n"
"v_mfma_f32_16x16x32_bf16 v[68:71], a[44:47], a[116:119], v[68:71]\n"
"v_mfma_f32_16x16x32_bf16 v[68:71], a[40:43], a[112:115], v[68:71]\n"
"buffer_load_dwordx4 v11, s[52:55], s67 offen offset:2048 lds\n"
"v_mfma_f32_16x16x32_bf16 v[72:75], a[40:43], a[120:123], v[72:75]\n"
"v_mfma_f32_16x16x32_bf16 v[72:75], a[44:47], a[124:127], v[72:75]\n"
"s_add_u32 s67, s75, 963584\n"
"v_mfma_f32_16x16x32_bf16 v[44:47], a[36:39], a[124:127], v[44:47]\n"
"v_mfma_f32_16x16x32_bf16 v[44:47], a[32:35], a[120:123], v[44:47]\n"
"v_mfma_f32_16x16x32_bf16 v[48:51], a[32:35], a[128:131], v[48:51]\n"
"buffer_load_dwordx4 v11, s[52:55], s67 offen offset:3072 lds\n"
"v_mfma_f32_16x16x32_bf16 v[48:51], a[36:39], a[132:135], v[48:51]\n"
"s_add_u32 m0, s64, 12288\n"
"v_mfma_f32_16x16x32_bf16 v[76:79], a[44:47], a[132:135], v[76:79]\n"
"s_waitcnt vmcnt(15)\n"
"ds_read_b64_tr_b16 a[64:65], v9 offset:0\n"
"v_mfma_f32_16x16x32_bf16 v[76:79], a[40:43], a[128:131], v[76:79]\n"
"s_add_u32 s67, s75, 983040\n"
"ds_read_b64_tr_b16 a[66:67], v9 offset:512\n"
"v_mfma_f32_16x16x32_bf16 v[80:83], a[40:43], a[136:139], v[80:83]\n"
"ds_read_b64_tr_b16 a[68:69], v9 offset:4096\n"
"v_mfma_f32_16x16x32_bf16 v[80:83], a[44:47], a[140:143], v[80:83]\n"
"ds_read_b64_tr_b16 a[70:71], v9 offset:4608\n"
"buffer_load_dwordx4 v11, s[52:55], s67 offen offset:0 lds\n"
"v_mfma_f32_16x16x32_bf16 v[52:55], a[36:39], a[140:143], v[52:55]\n"
"s_add_u32 s67, s75, 998400\n"
"ds_read_b64_tr_b16 a[72:73], v9 offset:8\n"
"v_mfma_f32_16x16x32_bf16 v[52:55], a[32:35], a[136:139], v[52:55]\n"
"s_waitcnt lgkmcnt(5)\n"
"ds_read_b64_tr_b16 a[74:75], v9 offset:520\n"
"v_mfma_f32_16x16x32_bf16 v[56:59], a[32:35], a[144:147], v[56:59]\n"
"ds_read_b64_tr_b16 a[76:77], v9 offset:4104\n"
"v_mfma_f32_16x16x32_bf16 v[56:59], a[36:39], a[148:151], v[56:59]\n"
"ds_read_b64_tr_b16 a[78:79], v9 offset:4616\n"
"v_mfma_f32_16x16x32_bf16 v[84:87], a[44:47], a[148:151], v[84:87]\n"
"buffer_load_dwordx4 v11, s[52:55], s67 offen offset:1024 lds\n"
"v_mfma_f32_16x16x32_bf16 v[84:87], a[40:43], a[144:147], v[84:87]\n"
"ds_read_b64_tr_b16 a[80:81], v15 offset:0\n"
"v_mfma_f32_16x16x32_bf16 a[4:7], a[40:43], a[152:155], a[4:7]\n"
"s_add_u32 s67, s75, 1013760\n"
"ds_read_b64_tr_b16 a[82:83], v15 offset:512\n"
"v_mfma_f32_16x16x32_bf16 a[4:7], a[44:47], a[156:159], a[4:7]\n"
"ds_read_b64_tr_b16 a[84:85], v15 offset:4096\n"
"v_mfma_f32_16x16x32_bf16 a[0:3], a[36:39], a[156:159], a[0:3]\n"
"ds_read_b64_tr_b16 a[86:87], v15 offset:4608\n"
"buffer_load_dwordx4 v11, s[52:55], s67 offen offset:2048 lds\n"
"v_mfma_f32_16x16x32_bf16 a[0:3], a[32:35], a[152:155], a[0:3]\n"
"ds_read_b64_tr_b16 a[88:89], v15 offset:8\n"
"v_mfma_f32_16x16x32_bf16 v[88:91], a[48:51], a[96:99], v[88:91]\n"
"s_add_u32 s67, s75, 1029120\n"
"ds_read_b64_tr_b16 a[90:91], v15 offset:520\n"
"v_mfma_f32_16x16x32_bf16 v[88:91], a[52:55], a[100:103], v[88:91]\n"
"ds_read_b64_tr_b16 a[92:93], v15 offset:4104\n"
"v_mfma_f32_16x16x32_bf16 v[116:119], a[60:63], a[100:103], v[116:119]\n"
"ds_read_b64_tr_b16 a[94:95], v15 offset:4616\n"
"v_mfma_f32_16x16x32_bf16 v[116:119], a[56:59], a[96:99], v[116:119]\n"
"buffer_load_dwordx4 v11, s[52:55], s67 offen offset:3072 lds\n"
"v_mfma_f32_16x16x32_bf16 v[120:123], a[56:59], a[104:107], v[120:123]\n"
"v_mfma_f32_16x16x32_bf16 v[120:123], a[60:63], a[108:111], v[120:123]\n"
"s_add_u32 m0, s64, 0\n"
"v_mfma_f32_16x16x32_bf16 v[92:95], a[52:55], a[108:111], v[92:95]\n"
"v_mfma_f32_16x16x32_bf16 v[92:95], a[48:51], a[104:107], v[92:95]\n"
"s_add_u32 s67, s75, 917632\n"
"v_mfma_f32_16x16x32_bf16 v[96:99], a[48:51], a[112:115], v[96:99]\n"
"s_waitcnt lgkmcnt(0)\n"
"v_mfma_f32_16x16x32_bf16 v[96:99], a[52:55], a[116:119], v[96:99]\n"
"buffer_load_dwordx4 v11, s[52:55], s67 offen offset:0 lds\n"
"v_mfma_f32_16x16x32_bf16 v[124:127], a[60:63], a[116:119], v[124:127]\n"
"v_mfma_f32_16x16x32_bf16 v[124:127], a[56:59], a[112:115], v[124:127]\n"
"s_add_u32 s67, s75, 932992\n"
"v_mfma_f32_16x16x32_bf16 v[128:131], a[56:59], a[120:123], v[128:131]\n"
"v_mfma_f32_16x16x32_bf16 v[128:131], a[60:63], a[124:127], v[128:131]\n"
"buffer_load_dwordx4 v11, s[52:55], s67 offen offset:1024 lds\n"
"v_mfma_f32_16x16x32_bf16 v[100:103], a[52:55], a[124:127], v[100:103]\n"
"v_mfma_f32_16x16x32_bf16 v[100:103], a[48:51], a[120:123], v[100:103]\n"
"s_add_u32 s67, s75, 948352\n"
"v_mfma_f32_16x16x32_bf16 v[104:107], a[48:51], a[128:131], v[104:107]\n"
"v_mfma_f32_16x16x32_bf16 v[104:107], a[52:55], a[132:135], v[104:107]\n"
"v_mfma_f32_16x16x32_bf16 v[132:135], a[60:63], a[132:135], v[132:135]\n"
"buffer_load_dwordx4 v11, s[52:55], s67 offen offset:2048 lds\n"
"v_mfma_f32_16x16x32_bf16 v[132:135], a[56:59], a[128:131], v[132:135]\n"
"v_mfma_f32_16x16x32_bf16 v[136:139], a[56:59], a[136:139], v[136:139]\n"
"s_add_u32 s67, s75, 963712\n"
"v_mfma_f32_16x16x32_bf16 v[136:139], a[60:63], a[140:143], v[136:139]\n"
"v_mfma_f32_16x16x32_bf16 v[108:111], a[52:55], a[140:143], v[108:111]\n"
"v_mfma_f32_16x16x32_bf16 v[108:111], a[48:51], a[136:139], v[108:111]\n"
"buffer_load_dwordx4 v11, s[52:55], s67 offen offset:3072 lds\n"
"v_mfma_f32_16x16x32_bf16 v[112:115], a[48:51], a[144:147], v[112:115]\n"
"s_add_u32 m0, s64, 4096\n"
"v_mfma_f32_16x16x32_bf16 v[112:115], a[52:55], a[148:151], v[112:115]\n"
"v_mfma_f32_16x16x32_bf16 v[140:143], a[60:63], a[148:151], v[140:143]\n"
"s_add_u32 s67, s75, 983168\n"
"v_mfma_f32_16x16x32_bf16 v[140:143], a[56:59], a[144:147], v[140:143]\n"
"v_mfma_f32_16x16x32_bf16 a[12:15], a[56:59], a[152:155], a[12:15]\n"
"buffer_load_dwordx4 v11, s[52:55], s67 offen offset:0 lds\n"
"v_mfma_f32_16x16x32_bf16 a[12:15], a[60:63], a[156:159], a[12:15]\n"
"s_sub_u32 s71, s64, s64\n"
"v_mfma_f32_16x16x32_bf16 a[8:11], a[52:55], a[156:159], a[8:11]\n"
"s_add_u32 s67, s75, 998528\n"
"v_mfma_f32_16x16x32_bf16 a[8:11], a[48:51], a[152:155], a[8:11]\n"
"v_mfma_f32_16x16x32_bf16 v[144:147], a[64:67], a[96:99], v[144:147]\n"
"s_add_u32 s71, s71, 32768\n"
"v_mfma_f32_16x16x32_bf16 v[144:147], a[68:71], a[100:103], v[144:147]\n"
"buffer_load_dwordx4 v11, s[52:55], s67 offen offset:1024 lds\n"
"v_mfma_f32_16x16x32_bf16 v[172:175], a[76:79], a[100:103], v[172:175]\n"
"v_add_u32 v20, s71, v7\n"
"v_mfma_f32_16x16x32_bf16 v[172:175], a[72:75], a[96:99], v[172:175]\n"
"s_add_u32 s67, s75, 1013888\n"
"v_mfma_f32_16x16x32_bf16 v[176:179], a[72:75], a[104:107], v[176:179]\n"
"buffer_load_dwordx4 v11, s[52:55], s67 offen offset:2048 lds\n"
"v_mfma_f32_16x16x32_bf16 v[176:179], a[76:79], a[108:111], v[176:179]\n"
"v_add_u32 v21, s71, v8\n"
"v_mfma_f32_16x16x32_bf16 v[148:151], a[68:71], a[108:111], v[148:151]\n"
"s_add_u32 s67, s75, 1029248\n"
"v_mfma_f32_16x16x32_bf16 v[148:151], a[64:67], a[104:107], v[148:151]\n"
"v_mfma_f32_16x16x32_bf16 v[152:155], a[64:67], a[112:115], v[152:155]\n"
"s_sub_u32 s66, 122880, s73\n"
"v_mfma_f32_16x16x32_bf16 v[152:155], a[68:71], a[116:119], v[152:155]\n"
"s_waitcnt vmcnt(18)\n"
"ds_read_b128 a[192:195], v20 offset:0\n"
"buffer_load_dwordx4 v11, s[52:55], s67 offen offset:3072 lds\n"
"v_mfma_f32_16x16x32_bf16 v[180:183], a[76:79], a[116:119], v[180:183]\n"
"ds_read_b128 a[196:199], v21 offset:0\n"
"v_add_u32 v12, s66, v7\n"
"v_mfma_f32_16x16x32_bf16 v[180:183], a[72:75], a[112:115], v[180:183]\n"
"s_add_u32 m0, s72, 57344\n"
"ds_read_b128 a[200:203], v20 offset:2048\n"
"v_mfma_f32_16x16x32_bf16 v[184:187], a[72:75], a[120:123], v[184:187]\n"
"v_add_u32 v13, s66, v8\n"
"v_mfma_f32_16x16x32_bf16 v[184:187], a[76:79], a[124:127], v[184:187]\n"
"s_add_u32 s67, %21, 0\n"
"ds_read_b128 a[204:207], v21 offset:2048\n"
"v_mfma_f32_16x16x32_bf16 v[156:159], a[68:71], a[124:127], v[156:159]\n"
"v_mfma_f32_16x16x32_bf16 v[156:159], a[64:67], a[120:123], v[156:159]\n"
"buffer_load_dwordx4 v11, s[52:55], s67 offen offset:0 nt lds\n"
"v_mfma_f32_16x16x32_bf16 v[160:163], a[64:67], a[128:131], v[160:163]\n"
"v_mfma_f32_16x16x32_bf16 v[160:163], a[68:71], a[132:135], v[160:163]\n"
"s_add_u32 s67, %21, 15360\n"
"v_mfma_f32_16x16x32_bf16 v[188:191], a[76:79], a[132:135], v[188:191]\n"
"v_mfma_f32_16x16x32_bf16 v[188:191], a[72:75], a[128:131], v[188:191]\n"
"s_waitcnt lgkmcnt(0)\n"
"buffer_load_dwordx4 v11, s[52:55], s67 offen offset:1024 nt lds\n"
"v_mfma_f32_16x16x32_bf16 v[192:195], a[72:75], a[136:139], v[192:195]\n"
"v_mfma_f32_16x16x32_bf16 v[192:195], a[76:79], a[140:143], v[192:195]\n"
"s_add_u32 s67, %21, 30720\n"
"v_mfma_f32_16x16x32_bf16 v[164:167], a[68:71], a[140:143], v[164:167]\n"
"v_mfma_f32_16x16x32_bf16 v[164:167], a[64:67], a[136:139], v[164:167]\n"
"buffer_load_dwordx4 v11, s[52:55], s67 offen offset:2048 nt lds\n"
"v_mfma_f32_16x16x32_bf16 v[168:171], a[64:67], a[144:147], v[168:171]\n"
"v_mfma_f32_16x16x32_bf16 v[168:171], a[68:71], a[148:151], v[168:171]\n"
"s_add_u32 s67, %21, 46080\n"
"v_mfma_f32_16x16x32_bf16 v[196:199], a[76:79], a[148:151], v[196:199]\n"
"v_mfma_f32_16x16x32_bf16 v[196:199], a[72:75], a[144:147], v[196:199]\n"
"buffer_load_dwordx4 v11, s[52:55], s67 offen offset:3072 nt lds\n"
"v_mfma_f32_16x16x32_bf16 a[20:23], a[72:75], a[152:155], a[20:23]\n"
"s_cmp_lt_u32 %16, 2\n"
"s_cselect_b32 s67, 16384, 0\n"
"v_mfma_f32_16x16x32_bf16 a[20:23], a[76:79], a[156:159], a[20:23]\n"
"v_mfma_f32_16x16x32_bf16 a[16:19], a[68:71], a[156:159], a[16:19]\n"
"s_add_u32 s67, s67, s72\n"
"s_add_u32 m0, s67, 16384\n"
"v_mfma_f32_16x16x32_bf16 a[16:19], a[64:67], a[152:155], a[16:19]\n"
"s_barrier\n"
"s_waitcnt vmcnt(12)\n"
"ds_read_b64_tr_b16 a[32:33], v9 offset:8192\n"
"v_mfma_f32_16x16x32_bf16 v[200:203], a[80:83], a[96:99], v[200:203]\n"
"s_sub_u32 s67, 1024, s60\n"
"ds_read_b64_tr_b16 a[34:35], v9 offset:8704\n"
"v_mfma_f32_16x16x32_bf16 v[200:203], a[84:87], a[100:103], v[200:203]\n"
"ds_read_b64_tr_b16 a[36:37], v9 offset:12288\n"
"v_mfma_f32_16x16x32_bf16 v[228:231], a[92:95], a[100:103], v[228:231]\n"
"ds_read_b128 a[100:103], v13 offset:16384\n"
"buffer_load_dwordx4 v6, s[48:51], s67 offen offset:0  lds\n"
"v_mfma_f32_16x16x32_bf16 v[228:231], a[88:91], a[96:99], v[228:231]\n"
"ds_read_b128 a[96:99], v12 offset:16384\n"
"v_mfma_f32_16x16x32_bf16 v[232:235], a[88:91], a[104:107], v[232:235]\n"
"s_sub_u32 s67, 16384, s60\n"
"ds_read_b64_tr_b16 a[38:39], v9 offset:12800\n"
"v_mfma_f32_16x16x32_bf16 v[232:235], a[92:95], a[108:111], v[232:235]\n"
"ds_read_b64_tr_b16 a[40:41], v9 offset:8200\n"
"v_mfma_f32_16x16x32_bf16 v[204:207], a[84:87], a[108:111], v[204:207]\n"
"ds_read_b128 a[108:111], v13 offset:18432\n"
"buffer_load_dwordx4 v6, s[48:51], s67 offen offset:1024  lds\n"
"v_mfma_f32_16x16x32_bf16 v[204:207], a[80:83], a[104:107], v[204:207]\n"
"ds_read_b128 a[104:107], v12 offset:18432\n"
"v_mfma_f32_16x16x32_bf16 v[208:211], a[80:83], a[112:115], v[208:211]\n"
"s_sub_u32 s67, 31744, s60\n"
"ds_read_b64_tr_b16 a[42:43], v9 offset:8712\n"
"v_mfma_f32_16x16x32_bf16 v[208:211], a[84:87], a[116:119], v[208:211]\n"
"ds_read_b64_tr_b16 a[44:45], v9 offset:12296\n"
"v_mfma_f32_16x16x32_bf16 v[236:239], a[92:95], a[116:119], v[236:239]\n"
"ds_read_b128 a[116:119], v13 offset:20480\n"
"buffer_load_dwordx4 v6, s[48:51], s67 offen offset:2048  lds\n"
"v_mfma_f32_16x16x32_bf16 v[236:239], a[88:91], a[112:115], v[236:239]\n"
"ds_read_b128 a[112:115], v12 offset:20480\n"
"v_mfma_f32_16x16x32_bf16 v[240:243], a[88:91], a[120:123], v[240:243]\n"
"s_sub_u32 s67, 47104, s60\n"
"ds_read_b64_tr_b16 a[46:47], v9 offset:12808\n"
"v_mfma_f32_16x16x32_bf16 v[240:243], a[92:95], a[124:127], v[240:243]\n"
"ds_read_b64_tr_b16 a[48:49], v15 offset:8192\n"
"v_mfma_f32_16x16x32_bf16 v[212:215], a[84:87], a[124:127], v[212:215]\n"
"ds_read_b128 a[124:127], v13 offset:22528\n"
"v_mfma_f32_16x16x32_bf16 v[212:215], a[80:83], a[120:123], v[212:215]\n"
"ds_read_b128 a[120:123], v12 offset:22528\n"
"buffer_load_dwordx4 v6, s[48:51], s67 offen offset:3072  lds\n"
"v_mfma_f32_16x16x32_bf16 v[216:219], a[80:83], a[128:131], v[216:219]\n"
"ds_read_b64_tr_b16 a[50:51], v15 offset:8704\n"
"v_mfma_f32_16x16x32_bf16 v[216:219], a[84:87], a[132:135], v[216:219]\n"
"ds_read_b64_tr_b16 a[52:53], v15 offset:12288\n"
"v_mfma_f32_16x16x32_bf16 v[244:247], a[92:95], a[132:135], v[244:247]\n"
"ds_read_b128 a[132:135], v13 offset:24576\n"
"v_mfma_f32_16x16x32_bf16 v[244:247], a[88:91], a[128:131], v[244:247]\n"
"s_add_u32 m0, s64, 8192\n"
"ds_read_b128 a[128:131], v12 offset:24576\n"
"v_mfma_f32_16x16x32_bf16 v[248:251], a[88:91], a[136:139], v[248:251]\n"
"ds_read_b64_tr_b16 a[54:55], v15 offset:12800\n"
"v_mfma_f32_16x16x32_bf16 v[248:251], a[92:95], a[140:143], v[248:251]\n"
"ds_read_b64_tr_b16 a[56:57], v15 offset:8200\n"
"v_mfma_f32_16x16x32_bf16 v[220:223], a[84:87], a[140:143], v[220:223]\n"
"ds_read_b128 a[140:143], v13 offset:26624\n"
"v_mfma_f32_16x16x32_bf16 v[220:223], a[80:83], a[136:139], v[220:223]\n"
"ds_read_b128 a[136:139], v12 offset:26624\n"
"v_mfma_f32_16x16x32_bf16 v[224:227], a[80:83], a[144:147], v[224:227]\n"
"ds_read_b64_tr_b16 a[58:59], v15 offset:8712\n"
"v_mfma_f32_16x16x32_bf16 v[224:227], a[84:87], a[148:151], v[224:227]\n"
"ds_read_b64_tr_b16 a[60:61], v15 offset:12296\n"
"v_mfma_f32_16x16x32_bf16 v[252:255], a[92:95], a[148:151], v[252:255]\n"
"s_sub_u32 s67, 1048576, s75\n"
"ds_read_b128 a[148:151], v13 offset:28672\n"
"v_mfma_f32_16x16x32_bf16 v[252:255], a[88:91], a[144:147], v[252:255]\n"
"ds_read_b128 a[144:147], v12 offset:28672\n"
"ds_read_b64_tr_b16 a[62:63], v15 offset:12808\n"
"v_mfma_f32_16x16x32_bf16 a[28:31], a[88:91], a[152:155], a[28:31]\n"
"v_mfma_f32_16x16x32_bf16 a[28:31], a[92:95], a[156:159], a[28:31]\n"
"v_mfma_f32_16x16x32_bf16 a[24:27], a[84:87], a[156:159], a[24:27]\n"
"ds_read_b128 a[156:159], v13 offset:30720\n"
"v_mfma_f32_16x16x32_bf16 a[24:27], a[80:83], a[152:155], a[24:27]\n"
"s_waitcnt lgkmcnt(15)\n"
"ds_read_b128 a[152:155], v12 offset:30720\n"
"v_mfma_f32_16x16x32_bf16 v[32:35], a[32:35], a[96:99], v[32:35]\n"
"v_mfma_f32_16x16x32_bf16 v[32:35], a[36:39], a[100:103], v[32:35]\n"
"s_waitcnt lgkmcnt(2)\n"
"v_mfma_f32_16x16x32_bf16 v[60:63], a[44:47], a[100:103], v[60:63]\n"
"buffer_load_dwordx4 v11, s[52:55], s67 offen offset:0 lds\n"
"v_mfma_f32_16x16x32_bf16 v[60:63], a[40:43], a[96:99], v[60:63]\n"
"v_mfma_f32_16x16x32_bf16 v[64:67], a[40:43], a[104:107], v[64:67]\n"
"s_sub_u32 s67, 1063936, s75\n"
"v_mfma_f32_16x16x32_bf16 v[64:67], a[44:47], a[108:111], v[64:67]\n"
"v_mfma_f32_16x16x32_bf16 v[36:39], a[36:39], a[108:111], v[36:39]\n"
"v_mfma_f32_16x16x32_bf16 v[36:39], a[32:35], a[104:107], v[36:39]\n"
"buffer_load_dwordx4 v11, s[52:55], s67 offen offset:1024 lds\n"
"v_mfma_f32_16x16x32_bf16 v[40:43], a[32:35], a[112:115], v[40:43]\n"
"v_mfma_f32_16x16x32_bf16 v[40:43], a[36:39], a[116:119], v[40:43]\n"
"s_sub_u32 s67, 1079296, s75\n"
"v_mfma_f32_16x16x32_bf16 v[68:71], a[44:47], a[116:119], v[68:71]\n"
"v_mfma_f32_16x16x32_bf16 v[68:71], a[40:43], a[112:115], v[68:71]\n"
"v_mfma_f32_16x16x32_bf16 v[72:75], a[40:43], a[120:123], v[72:75]\n"
"buffer_load_dwordx4 v11, s[52:55], s67 offen offset:2048 lds\n"
"v_mfma_f32_16x16x32_bf16 v[72:75], a[44:47], a[124:127], v[72:75]\n"
"v_mfma_f32_16x16x32_bf16 v[44:47], a[36:39], a[124:127], v[44:47]\n"
"s_sub_u32 s67, 1094656, s75\n"
"v_mfma_f32_16x16x32_bf16 v[44:47], a[32:35], a[120:123], v[44:47]\n"
"v_mfma_f32_16x16x32_bf16 v[48:51], a[32:35], a[128:131], v[48:51]\n"
"buffer_load_dwordx4 v11, s[52:55], s67 offen offset:3072 lds\n"
"v_mfma_f32_16x16x32_bf16 v[48:51], a[36:39], a[132:135], v[48:51]\n"
"s_add_u32 m0, s64, 12288\n"
"v_mfma_f32_16x16x32_bf16 v[76:79], a[44:47], a[132:135], v[76:79]\n"
"s_waitcnt vmcnt(12)\n"
"ds_read_b64_tr_b16 a[64:65], v9 offset:0\n"
"v_mfma_f32_16x16x32_bf16 v[76:79], a[40:43], a[128:131], v[76:79]\n"
"ds_read_b64_tr_b16 a[66:67], v9 offset:512\n"
"v_mfma_f32_16x16x32_bf16 v[80:83], a[40:43], a[136:139], v[80:83]\n"
"s_sub_u32 s67, 1114112, s75\n"
"ds_read_b64_tr_b16 a[68:69], v9 offset:4096\n"
"v_mfma_f32_16x16x32_bf16 v[80:83], a[44:47], a[140:143], v[80:83]\n"
"ds_read_b64_tr_b16 a[70:71], v9 offset:4608\n"
"v_mfma_f32_16x16x32_bf16 v[52:55], a[36:39], a[140:143], v[52:55]\n"
"ds_read_b64_tr_b16 a[72:73], v9 offset:8\n"
"buffer_load_dwordx4 v11, s[52:55], s67 offen offset:0 lds\n"
"v_mfma_f32_16x16x32_bf16 v[52:55], a[32:35], a[136:139], v[52:55]\n"
"s_waitcnt lgkmcnt(5)\n"
"ds_read_b64_tr_b16 a[74:75], v9 offset:520\n"
"v_mfma_f32_16x16x32_bf16 v[56:59], a[32:35], a[144:147], v[56:59]\n"
"s_sub_u32 s67, 1129472, s75\n"
"ds_read_b64_tr_b16 a[76:77], v9 offset:4104\n"
"v_mfma_f32_16x16x32_bf16 v[56:59], a[36:39], a[148:151], v[56:59]\n"
"ds_read_b64_tr_b16 a[78:79], v9 offset:4616\n"
"v_mfma_f32_16x16x32_bf16 v[84:87], a[44:47], a[148:151], v[84:87]\n"
"buffer_load_dwordx4 v11, s[52:55], s67 offen offset:1024 lds\n"
"v_mfma_f32_16x16x32_bf16 v[84:87], a[40:43], a[144:147], v[84:87]\n"
"ds_read_b64_tr_b16 a[80:81], v15 offset:0\n"
"v_mfma_f32_16x16x32_bf16 a[4:7], a[40:43], a[152:155], a[4:7]\n"
"s_sub_u32 s67, 1144832, s75\n"
"ds_read_b64_tr_b16 a[82:83], v15 offset:512\n"
"v_mfma_f32_16x16x32_bf16 a[4:7], a[44:47], a[156:159], a[4:7]\n"
"ds_read_b64_tr_b16 a[84:85], v15 offset:4096\n"
"v_mfma_f32_16x16x32_bf16 a[0:3], a[36:39], a[156:159], a[0:3]\n"
"s_add_u32 s96, s56, %11\n"
"ds_read_b64_tr_b16 a[86:87], v15 offset:4608\n"
"s_addc_u32 s97, s57, 0\n"
"v_mfma_f32_16x16x32_bf16 a[0:3], a[32:35], a[152:155], a[0:3]\n"
"ds_read_b64_tr_b16 a[88:89], v15 offset:8\n"
"buffer_load_dwordx4 v11, s[52:55], s67 offen offset:2048 lds\n"
"v_mfma_f32_16x16x32_bf16 v[88:91], a[48:51], a[96:99], v[88:91]\n"
"s_mov_b32 s69, 0\n"
"ds_read_b64_tr_b16 a[90:91], v15 offset:520\n"
"v_mfma_f32_16x16x32_bf16 v[88:91], a[52:55], a[100:103], v[88:91]\n"
"s_sub_u32 s67, 1160192, s75\n"
"ds_read_b64_tr_b16 a[92:93], v15 offset:4104\n"
"v_mfma_f32_16x16x32_bf16 v[116:119], a[60:63], a[100:103], v[116:119]\n"
"ds_read_b64_tr_b16 a[94:95], v15 offset:4616\n"
"v_mfma_f32_16x16x32_bf16 v[116:119], a[56:59], a[96:99], v[116:119]\n"
"buffer_load_dwordx4 v11, s[52:55], s67 offen offset:3072 lds\n"
"v_mfma_f32_16x16x32_bf16 v[120:123], a[56:59], a[104:107], v[120:123]\n"
"v_mfma_f32_16x16x32_bf16 v[120:123], a[60:63], a[108:111], v[120:123]\n"
"s_add_u32 m0, s64, 0\n"
"v_mfma_f32_16x16x32_bf16 v[92:95], a[52:55], a[108:111], v[92:95]\n"
"v_mfma_f32_16x16x32_bf16 v[92:95], a[48:51], a[104:107], v[92:95]\n"
"s_sub_u32 s67, 1048704, s75\n"
"v_mfma_f32_16x16x32_bf16 v[96:99], a[48:51], a[112:115], v[96:99]\n"
"s_waitcnt lgkmcnt(0)\n"
"v_mfma_f32_16x16x32_bf16 v[96:99], a[52:55], a[116:119], v[96:99]\n"
"s_mov_b32 s98, 0\n"
"v_mfma_f32_16x16x32_bf16 v[124:127], a[60:63], a[116:119], v[124:127]\n"
"s_load_dword s98, s[96:97], 0 glc\n"
"buffer_load_dwordx4 v11, s[52:55], s67 offen offset:0 lds\n"
"v_mfma_f32_16x16x32_bf16 v[124:127], a[56:59], a[112:115], v[124:127]\n"
"v_mfma_f32_16x16x32_bf16 v[128:131], a[56:59], a[120:123], v[128:131]\n"
"s_sub_u32 s67, 1064064, s75\n"
"v_mfma_f32_16x16x32_bf16 v[128:131], a[60:63], a[124:127], v[128:131]\n"
"v_mfma_f32_16x16x32_bf16 v[100:103], a[52:55], a[124:127], v[100:103]\n"
"buffer_load_dwordx4 v11, s[52:55], s67 offen offset:1024 lds\n"
"v_mfma_f32_16x16x32_bf16 v[100:103], a[48:51], a[120:123], v[100:103]\n"
"v_mfma_f32_16x16x32_bf16 v[104:107], a[48:51], a[128:131], v[104:107]\n"
"s_sub_u32 s67, 1079424, s75\n"
"v_mfma_f32_16x16x32_bf16 v[104:107], a[52:55], a[132:135], v[104:107]\n"
"v_mfma_f32_16x16x32_bf16 v[132:135], a[60:63], a[132:135], v[132:135]\n"
"buffer_load_dwordx4 v11, s[52:55], s67 offen offset:2048 lds\n"
"v_mfma_f32_16x16x32_bf16 v[132:135], a[56:59], a[128:131], v[132:135]\n"
"v_mfma_f32_16x16x32_bf16 v[136:139], a[56:59], a[136:139], v[136:139]\n"
"s_sub_u32 s67, 1094784, s75\n"
"v_mfma_f32_16x16x32_bf16 v[136:139], a[60:63], a[140:143], v[136:139]\n"
"v_mfma_f32_16x16x32_bf16 v[108:111], a[52:55], a[140:143], v[108:111]\n"
"buffer_load_dwordx4 v11, s[52:55], s67 offen offset:3072 lds\n"
"v_mfma_f32_16x16x32_bf16 v[108:111], a[48:51], a[136:139], v[108:111]\n"
"s_add_u32 s69, s69, s98\n"
"v_mfma_f32_16x16x32_bf16 v[112:115], a[48:51], a[144:147], v[112:115]\n"
"s_add_u32 s69, s69, s98\n"
"s_add_u32 m0, s64, 4096\n"
"v_mfma_f32_16x16x32_bf16 v[112:115], a[52:55], a[148:151], v[112:115]\n"
"v_mfma_f32_16x16x32_bf16 v[140:143], a[60:63], a[148:151], v[140:143]\n"
"s_add_u32 s69, s69, s98\n"
"s_sub_u32 s67, 1114240, s75\n"
"v_mfma_f32_16x16x32_bf16 v[140:143], a[56:59], a[144:147], v[140:143]\n"
"s_add_u32 s69, s69, s98\n"
"v_mfma_f32_16x16x32_bf16 a[12:15], a[56:59], a[152:155], a[12:15]\n"
"s_add_u32 s69, s69, s98\n"
"v_mfma_f32_16x16x32_bf16 a[12:15], a[60:63], a[156:159], a[12:15]\n"
"buffer_load_dwordx4 v11, s[52:55], s67 offen offset:0 lds\n"
"v_mfma_f32_16x16x32_bf16 a[8:11], a[52:55], a[156:159], a[8:11]\n"
"s_add_u32 s69, s69, s98\n"
"v_mfma_f32_16x16x32_bf16 a[8:11], a[48:51], a[152:155], a[8:11]\n"
"s_add_u32 s69, s69, s98\n"
"s_sub_u32 s67, 1129600, s75\n"
"v_mfma_f32_16x16x32_bf16 v[144:147], a[64:67], a[96:99], v[144:147]\n"
"s_add_u32 s69, s69, s98\n"
"v_mfma_f32_16x16x32_bf16 v[144:147], a[68:71], a[100:103], v[144:147]\n"
"s_sub_u32 s71, s72, s64\n"
"buffer_load_dwordx4 v11, s[52:55], s67 offen offset:1024 lds\n"
"v_mfma_f32_16x16x32_bf16 v[172:175], a[76:79], a[100:103], v[172:175]\n"
"s_add_u32 s69, s69, s98\n"
"v_mfma_f32_16x16x32_bf16 v[172:175], a[72:75], a[96:99], v[172:175]\n"
"s_add_u32 s69, s69, s98\n"
"s_sub_u32 s67, 1144960, s75\n"
"v_mfma_f32_16x16x32_bf16 v[176:179], a[72:75], a[104:107], v[176:179]\n"
"s_add_u32 s69, s69, s98\n"
"s_add_u32 s71, s71, 57344\n"
"v_mfma_f32_16x16x32_bf16 v[176:179], a[76:79], a[108:111], v[176:179]\n"
"buffer_load_dwordx4 v11, s[52:55], s67 offen offset:2048 lds\n"
"v_mfma_f32_16x16x32_bf16 v[148:151], a[68:71], a[108:111], v[148:151]\n"
"s_add_u32 s69, s69, s98\n"
"v_add_u32 v20, s71, v9\n"
"v_mfma_f32_16x16x32_bf16 v[148:151], a[64:67], a[104:107], v[148:151]\n"
"s_add_u32 s69, s69, s98\n"
"s_sub_u32 s67, 1160320, s75\n"
"v_mfma_f32_16x16x32_bf16 v[152:155], a[64:67], a[112:115], v[152:155]\n"
"s_sub_u32 s66, 0, s73\n"
"s_add_u32 s69, s69, s98\n"
"v_mfma_f32_16x16x32_bf16 v[152:155], a[68:71], a[116:119], v[152:155]\n"
"buffer_load_dwordx4 v11, s[52:55], s67 offen offset:3072 lds\n"
"v_add_u32 v21, s71, v15\n"
"v_mfma_f32_16x16x32_bf16 v[180:183], a[76:79], a[116:119], v[180:183]\n"
"s_add_u32 s69, s69, s98\n"
"v_add_u32 v12, s66, v7\n"
"s_waitcnt lgkmcnt(0)\n"
"v_mfma_f32_16x16x32_bf16 v[180:183], a[72:75], a[112:115], v[180:183]\n"
"s_cmp_lt_u32 %16, 2\n"
"s_cselect_b32 s67, 16384, 0\n"
"v_mfma_f32_16x16x32_bf16 v[184:187], a[72:75], a[120:123], v[184:187]\n"
"v_add_u32 v13, s66, v8\n"
"v_mfma_f32_16x16x32_bf16 v[184:187], a[76:79], a[124:127], v[184:187]\n"
"s_waitcnt vmcnt(20)\n"
"ds_read_b64_tr_b16 a[176:177], v20 offset:0\n"
"v_mfma_f32_16x16x32_bf16 v[156:159], a[68:71], a[124:127], v[156:159]\n"
"ds_read_b64_tr_b16 a[178:179], v20 offset:512\n"
"v_mfma_f32_16x16x32_bf16 v[156:159], a[64:67], a[120:123], v[156:159]\n"
"ds_read_b64_tr_b16 a[180:181], v20 offset:8\n"
"v_mfma_f32_16x16x32_bf16 v[160:163], a[64:67], a[128:131], v[160:163]\n"
"s_add_u32 s67, s67, s72\n"
"ds_read_b64_tr_b16 a[182:183], v20 offset:520\n"
"v_mfma_f32_16x16x32_bf16 v[160:163], a[68:71], a[132:135], v[160:163]\n"
"v_mfma_f32_16x16x32_bf16 v[188:191], a[76:79], a[132:135], v[188:191]\n"
"ds_read_b64_tr_b16 a[184:185], v21 offset:0\n"
"v_mfma_f32_16x16x32_bf16 v[188:191], a[72:75], a[128:131], v[188:191]\n"
"ds_read_b64_tr_b16 a[186:187], v21 offset:512\n"
"v_mfma_f32_16x16x32_bf16 v[192:195], a[72:75], a[136:139], v[192:195]\n"
"s_add_u32 m0, s67, 57344\n"
"ds_read_b64_tr_b16 a[188:189], v21 offset:8\n"
"v_mfma_f32_16x16x32_bf16 v[192:195], a[76:79], a[140:143], v[192:195]\n"
"v_mfma_f32_16x16x32_bf16 v[164:167], a[68:71], a[140:143], v[164:167]\n"
"ds_read_b64_tr_b16 a[190:191], v21 offset:520\n"
"v_mfma_f32_16x16x32_bf16 v[164:167], a[64:67], a[136:139], v[164:167]\n"
"v_mfma_f32_16x16x32_bf16 v[168:171], a[64:67], a[144:147], v[168:171]\n"
"s_sub_u32 s67, 1280, s60\n"
"v_mfma_f32_16x16x32_bf16 v[168:171], a[68:71], a[148:151], v[168:171]\n"
"v_mfma_f32_16x16x32_bf16 v[196:199], a[76:79], a[148:151], v[196:199]\n"
"v_mfma_f32_16x16x32_bf16 v[196:199], a[72:75], a[144:147], v[196:199]\n"
"v_mfma_f32_16x16x32_bf16 a[20:23], a[72:75], a[152:155], a[20:23]\n"
"s_waitcnt lgkmcnt(0)\n"
"v_mfma_f32_16x16x32_bf16 a[20:23], a[76:79], a[156:159], a[20:23]\n"
"buffer_load_dwordx4 v6, s[48:51], s67 offen offset:0  lds\n"
"v_mfma_f32_16x16x32_bf16 a[16:19], a[68:71], a[156:159], a[16:19]\n"
"s_sub_u32 s67, 16640, s60\n"
"v_mfma_f32_16x16x32_bf16 a[16:19], a[64:67], a[152:155], a[16:19]\n"
"s_waitcnt vmcnt(17)\n"
"s_barrier\n"
"v_mfma_f32_16x16x32_bf16 v[200:203], a[80:83], a[96:99], v[200:203]\n"
"s_waitcnt vmcnt(9)\n"
"ds_read_b64_tr_b16 a[32:33], v9 offset:8192\n"
"v_mfma_f32_16x16x32_bf16 v[200:203], a[84:87], a[100:103], v[200:203]\n"
"ds_read_b64_tr_b16 a[34:35], v9 offset:8704\n"
"v_mfma_f32_16x16x32_bf16 v[228:231], a[92:95], a[100:103], v[228:231]\n"
"ds_read_b128 a[100:103], v13 offset:32768\n"
"buffer_load_dwordx4 v6, s[48:51], s67 offen offset:1024  lds\n"
"v_mfma_f32_16x16x32_bf16 v[228:231], a[88:91], a[96:99], v[228:231]\n"
"ds_read_b128 a[96:99], v12 offset:32768\n"
"v_mfma_f32_16x16x32_bf16 v[232:235], a[88:91], a[104:107], v[232:235]\n"
"s_sub_u32 s67, 32000, s60\n"
"ds_read_b64_tr_b16 a[36:37], v9 offset:12288\n"
"v_mfma_f32_16x16x32_bf16 v[232:235], a[92:95], a[108:111], v[232:235]\n"
"ds_read_b64_tr_b16 a[38:39], v9 offset:12800\n"
"v_mfma_f32_16x16x32_bf16 v[204:207], a[84:87], a[108:111], v[204:207]\n"
"ds_read_b128 a[108:111], v13 offset:34816\n"
"buffer_load_dwordx4 v6, s[48:51], s67 offen offset:2048  lds\n"
"v_mfma_f32_16x16x32_bf16 v[204:207], a[80:83], a[104:107], v[204:207]\n"
"ds_read_b128 a[104:107], v12 offset:34816\n"
"ds_read_b64_tr_b16 a[40:41], v9 offset:8200\n"
"v_mfma_f32_16x16x32_bf16 v[208:211], a[80:83], a[112:115], v[208:211]\n"
"s_sub_u32 s67, 47360, s60\n"
"ds_read_b64_tr_b16 a[42:43], v9 offset:8712\n"
"v_mfma_f32_16x16x32_bf16 v[208:211], a[84:87], a[116:119], v[208:211]\n"
"ds_read_b64_tr_b16 a[44:45], v9 offset:12296\n"
"v_mfma_f32_16x16x32_bf16 v[236:239], a[92:95], a[116:119], v[236:239]\n"
"ds_read_b128 a[116:119], v13 offset:36864\n"
"v_mfma_f32_16x16x32_bf16 v[236:239], a[88:91], a[112:115], v[236:239]\n"
"ds_read_b128 a[112:115], v12 offset:36864\n"
"buffer_load_dwordx4 v6, s[48:51], s67 offen offset:3072  lds\n"
"v_mfma_f32_16x16x32_bf16 v[240:243], a[88:91], a[120:123], v[240:243]\n"
"ds_read_b64_tr_b16 a[46:47], v9 offset:12808\n"
"v_mfma_f32_16x16x32_bf16 v[240:243], a[92:95], a[124:127], v[240:243]\n"
"s_add_u32 m0, s64, 20352\n"
"ds_read_b64_tr_b16 a[48:49], v15 offset:8192\n"
"v_mfma_f32_16x16x32_bf16 v[212:215], a[84:87], a[124:127], v[212:215]\n"
"ds_read_b128 a[124:127], v13 offset:38912\n"
"v_mfma_f32_16x16x32_bf16 v[212:215], a[80:83], a[120:123], v[212:215]\n"
"ds_read_b128 a[120:123], v12 offset:38912\n"
"buffer_load_dwordx4 v6, s[40:43], s84 offen offset:128  lds\n"
"v_mfma_f32_16x16x32_bf16 v[216:219], a[80:83], a[128:131], v[216:219]\n"
"ds_read_b64_tr_b16 a[50:51], v15 offset:8704\n"
"v_mfma_f32_16x16x32_bf16 v[216:219], a[84:87], a[132:135], v[216:219]\n"
"ds_read_b64_tr_b16 a[52:53], v15 offset:12288\n"
"v_mfma_f32_16x16x32_bf16 v[244:247], a[92:95], a[132:135], v[244:247]\n"
"ds_read_b128 a[132:135], v13 offset:24576\n"
"v_mfma_f32_16x16x32_bf16 v[244:247], a[88:91], a[128:131], v[244:247]\n"
"ds_read_b128 a[128:131], v12 offset:24576\n"
"buffer_load_dwordx4 v6, s[40:43], s85 offen offset:1152  lds\n"
"v_mfma_f32_16x16x32_bf16 v[248:251], a[88:91], a[136:139], v[248:251]\n"
"ds_read_b64_tr_b16 a[54:55], v15 offset:12800\n"
"v_mfma_f32_16x16x32_bf16 v[248:251], a[92:95], a[140:143], v[248:251]\n"
"ds_read_b64_tr_b16 a[56:57], v15 offset:8200\n"
"v_mfma_f32_16x16x32_bf16 v[220:223], a[84:87], a[140:143], v[220:223]\n"
"ds_read_b128 a[140:143], v13 offset:26624\n"
"v_mfma_f32_16x16x32_bf16 v[220:223], a[80:83], a[136:139], v[220:223]\n"
"ds_read_b128 a[136:139], v12 offset:26624\n"
"v_mfma_f32_16x16x32_bf16 v[224:227], a[80:83], a[144:147], v[224:227]\n"
"ds_read_b64_tr_b16 a[58:59], v15 offset:8712\n"
"buffer_load_dwordx4 v6, s[40:43], s86 offen offset:2176  lds\n"
"v_mfma_f32_16x16x32_bf16 v[224:227], a[84:87], a[148:151], v[224:227]\n"
"ds_read_b64_tr_b16 a[60:61], v15 offset:12296\n"
"v_mfma_f32_16x16x32_bf16 v[252:255], a[92:95], a[148:151], v[252:255]\n"
"ds_read_b128 a[148:151], v13 offset:28672\n"
"v_mfma_f32_16x16x32_bf16 v[252:255], a[88:91], a[144:147], v[252:255]\n"
"ds_read_b128 a[144:147], v12 offset:28672\n"
"ds_read_b64_tr_b16 a[62:63], v15 offset:12808\n"
"v_mfma_f32_16x16x32_bf16 a[28:31], a[88:91], a[152:155], a[28:31]\n"
"buffer_load_dwordx4 v6, s[40:43], s87 offen offset:3200  lds\n"
"v_mfma_f32_16x16x32_bf16 a[28:31], a[92:95], a[156:159], a[28:31]\n"
"v_mfma_f32_16x16x32_bf16 a[24:27], a[84:87], a[156:159], a[24:27]\n"
"ds_read_b128 a[156:159], v13 offset:30720\n"
"v_mfma_f32_16x16x32_bf16 a[24:27], a[80:83], a[152:155], a[24:27]\n"
"s_waitcnt lgkmcnt(15)\n"
"ds_read_b128 a[152:155], v12 offset:30720\n"
"v_mfma_f32_16x16x32_bf16 v[32:35], a[32:35], a[96:99], v[32:35]\n"
"v_mfma_f32_16x16x32_bf16 v[32:35], a[36:39], a[100:103], v[32:35]\n"
"s_waitcnt lgkmcnt(2)\n"
"buffer_store_dwordx4 a[176:179], v0, s[36:39], %23 offen offset:0 sc1\n"
"v_mfma_f32_16x16x32_bf16 v[60:63], a[44:47], a[100:103], v[60:63]\n"
"v_mfma_f32_16x16x32_bf16 v[60:63], a[40:43], a[96:99], v[60:63]\n"
"s_add_u32 m0, s64, 8192\n"
"v_mfma_f32_16x16x32_bf16 v[64:67], a[40:43], a[104:107], v[64:67]\n"
"v_mfma_f32_16x16x32_bf16 v[64:67], a[44:47], a[108:111], v[64:67]\n"
"s_sub_u32 s67, 1310720, s75\n"
"v_mfma_f32_16x16x32_bf16 v[36:39], a[36:39], a[108:111], v[36:39]\n"
"v_mfma_f32_16x16x32_bf16 v[36:39], a[32:35], a[104:107], v[36:39]\n"
"buffer_load_dwordx4 v11, s[52:55], s67 offen offset:0 lds\n"
"v_mfma_f32_16x16x32_bf16 v[40:43], a[32:35], a[112:115], v[40:43]\n"
"v_mfma_f32_16x16x32_bf16 v[40:43], a[36:39], a[116:119], v[40:43]\n"
"s_sub_u32 s67, 1326080, s75\n"
"v_mfma_f32_16x16x32_bf16 v[68:71], a[44:47], a[116:119], v[68:71]\n"
"v_mfma_f32_16x16x32_bf16 v[68:71], a[40:43], a[112:115], v[68:71]\n"
"buffer_load_dwordx4 v11, s[52:55], s67 offen offset:1024 lds\n"
"v_mfma_f32_16x16x32_bf16 v[72:75], a[40:43], a[120:123], v[72:75]\n"
"v_mfma_f32_16x16x32_bf16 v[72:75], a[44:47], a[124:127], v[72:75]\n"
"s_sub_u32 s67, 1341440, s75\n"
"v_mfma_f32_16x16x32_bf16 v[44:47], a[36:39], a[124:127], v[44:47]\n"
"v_mfma_f32_16x16x32_bf16 v[44:47], a[32:35], a[120:123], v[44:47]\n"
"v_mfma_f32_16x16x32_bf16 v[48:51], a[32:35], a[128:131], v[48:51]\n"
"buffer_load_dwordx4 v11, s[52:55], s67 offen offset:2048 lds\n"
"v_mfma_f32_16x16x32_bf16 v[48:51], a[36:39], a[132:135], v[48:51]\n"
"s_sub_u32 s67, 1356800, s75\n"
"v_mfma_f32_16x16x32_bf16 v[76:79], a[44:47], a[132:135], v[76:79]\n"
"s_waitcnt vmcnt(12)\n"
"ds_read_b64_tr_b16 a[64:65], v9 offset:0\n"
"v_mfma_f32_16x16x32_bf16 v[76:79], a[40:43], a[128:131], v[76:79]\n"
"ds_read_b64_tr_b16 a[66:67], v9 offset:512\n"
"v_mfma_f32_16x16x32_bf16 v[80:83], a[40:43], a[136:139], v[80:83]\n"
"ds_read_b64_tr_b16 a[68:69], v9 offset:4096\n"
"buffer_load_dwordx4 v11, s[52:55], s67 offen offset:3072 lds\n"
"v_mfma_f32_16x16x32_bf16 v[80:83], a[44:47], a[140:143], v[80:83]\n"
"ds_read_b64_tr_b16 a[70:71], v9 offset:4608\n"
"v_mfma_f32_16x16x32_bf16 v[52:55], a[36:39], a[140:143], v[52:55]\n"
"s_add_u32 m0, s64, 12288\n"
"ds_read_b64_tr_b16 a[72:73], v9 offset:8\n"
"v_mfma_f32_16x16x32_bf16 v[52:55], a[32:35], a[136:139], v[52:55]\n"
"s_waitcnt lgkmcnt(5)\n"
"ds_read_b64_tr_b16 a[74:75], v9 offset:520\n"
"v_mfma_f32_16x16x32_bf16 v[56:59], a[32:35], a[144:147], v[56:59]\n"
"s_sub_u32 s67, 1376256, s75\n"
"ds_read_b64_tr_b16 a[76:77], v9 offset:4104\n"
"v_mfma_f32_16x16x32_bf16 v[56:59], a[36:39], a[148:151], v[56:59]\n"
"ds_read_b64_tr_b16 a[78:79], v9 offset:4616\n"
"v_mfma_f32_16x16x32_bf16 v[84:87], a[44:47], a[148:151], v[84:87]\n"
"buffer_load_dwordx4 v11, s[52:55], s67 offen offset:0 lds\n"
"v_mfma_f32_16x16x32_bf16 v[84:87], a[40:43], a[144:147], v[84:87]\n"
"ds_read_b64_tr_b16 a[80:81], v15 offset:0\n"
"v_mfma_f32_16x16x32_bf16 a[4:7], a[40:43], a[152:155], a[4:7]\n"
"s_sub_u32 s67, 1391616, s75\n"
"ds_read_b64_tr_b16 a[82:83], v15 offset:512\n"
"v_mfma_f32_16x16x32_bf16 a[4:7], a[44:47], a[156:159], a[4:7]\n"
"ds_read_b64_tr_b16 a[84:85], v15 offset:4096\n"
"v_mfma_f32_16x16x32_bf16 a[0:3], a[36:39], a[156:159], a[0:3]\n"
"ds_read_b64_tr_b16 a[86:87], v15 offset:4608\n"
"buffer_load_dwordx4 v11, s[52:55], s67 offen offset:1024 lds\n"
"v_mfma_f32_16x16x32_bf16 a[0:3], a[32:35], a[152:155], a[0:3]\n"
"ds_read_b64_tr_b16 a[88:89], v15 offset:8\n"
"v_mfma_f32_16x16x32_bf16 v[88:91], a[48:51], a[96:99], v[88:91]\n"
"s_sub_u32 s67, 1406976, s75\n"
"ds_read_b64_tr_b16 a[90:91], v15 offset:520\n"
"v_mfma_f32_16x16x32_bf16 v[88:91], a[52:55], a[100:103], v[88:91]\n"
"ds_read_b64_tr_b16 a[92:93], v15 offset:4104\n"
"v_mfma_f32_16x16x32_bf16 v[116:119], a[60:63], a[100:103], v[116:119]\n"
"ds_read_b64_tr_b16 a[94:95], v15 offset:4616\n"
"v_mfma_f32_16x16x32_bf16 v[116:119], a[56:59], a[96:99], v[116:119]\n"
"buffer_load_dwordx4 v11, s[52:55], s67 offen offset:2048 lds\n"
"v_mfma_f32_16x16x32_bf16 v[120:123], a[56:59], a[104:107], v[120:123]\n"
"v_mfma_f32_16x16x32_bf16 v[120:123], a[60:63], a[108:111], v[120:123]\n"
"s_sub_u32 s67, 1422336, s75\n"
"v_mfma_f32_16x16x32_bf16 v[92:95], a[52:55], a[108:111], v[92:95]\n"
"v_mfma_f32_16x16x32_bf16 v[92:95], a[48:51], a[104:107], v[92:95]\n"
"buffer_load_dwordx4 v11, s[52:55], s67 offen offset:3072 lds\n"
"v_mfma_f32_16x16x32_bf16 v[96:99], a[48:51], a[112:115], v[96:99]\n"
"s_waitcnt lgkmcnt(0)\n"
"s_add_u32 m0, s64, 0\n"
"v_mfma_f32_16x16x32_bf16 v[96:99], a[52:55], a[116:119], v[96:99]\n"
"s_mov_b32 s98, 0\n"
"v_mfma_f32_16x16x32_bf16 v[124:127], a[60:63], a[116:119], v[124:127]\n"
"s_load_dword s98, s[96:97], 8192 glc\n"
"s_sub_u32 s67, 1310848, s75\n"
"v_mfma_f32_16x16x32_bf16 v[124:127], a[56:59], a[112:115], v[124:127]\n"
"v_mfma_f32_16x16x32_bf16 v[128:131], a[56:59], a[120:123], v[128:131]\n"
"buffer_load_dwordx4 v11, s[52:55], s67 offen offset:0 lds\n"
"v_mfma_f32_16x16x32_bf16 v[128:131], a[60:63], a[124:127], v[128:131]\n"
"v_mfma_f32_16x16x32_bf16 v[100:103], a[52:55], a[124:127], v[100:103]\n"
"s_sub_u32 s67, 1326208, s75\n"
"v_mfma_f32_16x16x32_bf16 v[100:103], a[48:51], a[120:123], v[100:103]\n"
"v_mfma_f32_16x16x32_bf16 v[104:107], a[48:51], a[128:131], v[104:107]\n"
"buffer_load_dwordx4 v11, s[52:55], s67 offen offset:1024 lds\n"
"v_mfma_f32_16x16x32_bf16 v[104:107], a[52:55], a[132:135], v[104:107]\n"
"v_mfma_f32_16x16x32_bf16 v[132:135], a[60:63], a[132:135], v[132:135]\n"
"s_sub_u32 s67, 1341568, s75\n"
"v_mfma_f32_16x16x32_bf16 v[132:135], a[56:59], a[128:131], v[132:135]\n"
"v_mfma_f32_16x16x32_bf16 v[136:139], a[56:59], a[136:139], v[136:139]\n"
"buffer_load_dwordx4 v11, s[52:55], s67 offen offset:2048 lds\n"
"v_mfma_f32_16x16x32_bf16 v[136:139], a[60:63], a[140:143], v[136:139]\n"
"v_mfma_f32_16x16x32_bf16 v[108:111], a[52:55], a[140:143], v[108:111]\n"
"s_sub_u32 s67, 1356928, s75\n"
"v_mfma_f32_16x16x32_bf16 v[108:111], a[48:51], a[136:139], v[108:111]\n"
"s_sub_u32 s69, s69, s98\n"
"v_mfma_f32_16x16x32_bf16 v[112:115], a[48:51], a[144:147], v[112:115]\n"
"s_sub_u32 s69, s69, s98\n"
"v_mfma_f32_16x16x32_bf16 v[112:115], a[52:55], a[148:151], v[112:115]\n"
"buffer_load_dwordx4 v11, s[52:55], s67 offen offset:3072 lds\n"
"v_mfma_f32_16x16x32_bf16 v[140:143], a[60:63], a[148:151], v[140:143]\n"
"s_sub_u32 s69, s69, s98\n"
"v_mfma_f32_16x16x32_bf16 v[140:143], a[56:59], a[144:147], v[140:143]\n"
"s_sub_u32 s69, s69, s98\n"
"s_add_u32 m0, s64, 4096\n"
"v_mfma_f32_16x16x32_bf16 a[12:15], a[56:59], a[152:155], a[12:15]\n"
"s_sub_u32 s69, s69, s98\n"
"s_sub_u32 s67, 1376384, s75\n"
"v_mfma_f32_16x16x32_bf16 a[12:15], a[60:63], a[156:159], a[12:15]\n"
"buffer_load_dwordx4 v11, s[52:55], s67 offen offset:0 lds\n"
"v_mfma_f32_16x16x32_bf16 a[8:11], a[52:55], a[156:159], a[8:11]\n"
"s_sub_u32 s69, s69, s98\n"
"v_mfma_f32_16x16x32_bf16 a[8:11], a[48:51], a[152:155], a[8:11]\n"
"s_sub_u32 s69, s69, s98\n"
"s_sub_u32 s67, 1391744, s75\n"
"v_mfma_f32_16x16x32_bf16 v[144:147], a[64:67], a[96:99], v[144:147]\n"
"s_sub_u32 s69, s69, s98\n"
"v_mfma_f32_16x16x32_bf16 v[144:147], a[68:71], a[100:103], v[144:147]\n"
"buffer_load_dwordx4 v11, s[52:55], s67 offen offset:1024 lds\n"
"v_mfma_f32_16x16x32_bf16 v[172:175], a[76:79], a[100:103], v[172:175]\n"
"s_sub_u32 s69, s69, s98\n"
"v_mfma_f32_16x16x32_bf16 v[172:175], a[72:75], a[96:99], v[172:175]\n"
"s_sub_u32 s69, s69, s98\n"
"s_sub_u32 s67, 1407104, s75\n"
"v_mfma_f32_16x16x32_bf16 v[176:179], a[72:75], a[104:107], v[176:179]\n"
"s_sub_u32 s69, s69, s98\n"
"v_mfma_f32_16x16x32_bf16 v[176:179], a[76:79], a[108:111], v[176:179]\n"
"s_sub_u32 s71, s64, s64\n"
"v_mfma_f32_16x16x32_bf16 v[148:151], a[68:71], a[108:111], v[148:151]\n"
"s_sub_u32 s69, s69, s98\n"
"buffer_load_dwordx4 v11, s[52:55], s67 offen offset:2048 lds\n"
"v_mfma_f32_16x16x32_bf16 v[148:151], a[64:67], a[104:107], v[148:151]\n"
"s_sub_u32 s69, s69, s98\n"
"s_add_u32 s71, s71, 20480\n"
"v_mfma_f32_16x16x32_bf16 v[152:155], a[64:67], a[112:115], v[152:155]\n"
"s_sub_u32 s66, 40960, s73\n"
"s_sub_u32 s69, s69, s98\n"
"v_mfma_f32_16x16x32_bf16 v[152:155], a[68:71], a[116:119], v[152:155]\n"
"s_sub_u32 s67, 1422464, s75\n"
"v_mfma_f32_16x16x32_bf16 v[180:183], a[76:79], a[116:119], v[180:183]\n"
"s_sub_u32 s69, s69, s98\n"
"v_add_u32 v12, s66, v7\n"
"s_waitcnt lgkmcnt(0)\n"
"v_mfma_f32_16x16x32_bf16 v[180:183], a[72:75], a[112:115], v[180:183]\n"
"buffer_load_dwordx4 v11, s[52:55], s67 offen offset:3072 lds\n"
"v_add_u32 v20, s71, v7\n"
"v_mfma_f32_16x16x32_bf16 v[184:187], a[72:75], a[120:123], v[184:187]\n"
"s_cmp_lt_u32 %16, 2\n"
"s_cselect_b32 s67, 16384, 0\n"
"v_add_u32 v13, s66, v8\n"
"v_mfma_f32_16x16x32_bf16 v[184:187], a[76:79], a[124:127], v[184:187]\n"
"s_add_u32 s67, s67, s72\n"
"v_add_u32 v21, s71, v8\n"
"v_mfma_f32_16x16x32_bf16 v[156:159], a[68:71], a[124:127], v[156:159]\n"
"v_mfma_f32_16x16x32_bf16 v[156:159], a[64:67], a[120:123], v[156:159]\n"
"s_add_u32 m0, s67, 98304\n"
"s_sub_u32 s67, 1536, s60\n"
"v_mfma_f32_16x16x32_bf16 v[160:163], a[64:67], a[128:131], v[160:163]\n"
"s_waitcnt vmcnt(17)\n"
"ds_read_b128 a[208:211], v20 offset:0\n"
"v_mfma_f32_16x16x32_bf16 v[160:163], a[68:71], a[132:135], v[160:163]\n"
"buffer_load_dwordx4 v6, s[48:51], s67 offen offset:0  lds\n"
"v_mfma_f32_16x16x32_bf16 v[188:191], a[76:79], a[132:135], v[188:191]\n"
"ds_read_b128 a[212:215], v21 offset:0\n"
"v_mfma_f32_16x16x32_bf16 v[188:191], a[72:75], a[128:131], v[188:191]\n"
"s_sub_u32 s67, 16896, s60\n"
"ds_read_b128 a[216:219], v20 offset:2048\n"
"v_mfma_f32_16x16x32_bf16 v[192:195], a[72:75], a[136:139], v[192:195]\n"
"v_mfma_f32_16x16x32_bf16 v[192:195], a[76:79], a[140:143], v[192:195]\n"
"ds_read_b128 a[220:223], v21 offset:2048\n"
"v_mfma_f32_16x16x32_bf16 v[164:167], a[68:71], a[140:143], v[164:167]\n"
"buffer_load_dwordx4 v6, s[48:51], s67 offen offset:1024  lds\n"
"v_mfma_f32_16x16x32_bf16 v[164:167], a[64:67], a[136:139], v[164:167]\n"
"v_mfma_f32_16x16x32_bf16 v[168:171], a[64:67], a[144:147], v[168:171]\n"
"s_sub_u32 s67, 32256, s60\n"
"v_mfma_f32_16x16x32_bf16 v[168:171], a[68:71], a[148:151], v[168:171]\n"
"v_mfma_f32_16x16x32_bf16 v[196:199], a[76:79], a[148:151], v[196:199]\n"
"buffer_load_dwordx4 v6, s[48:51], s67 offen offset:2048  lds\n"
"v_mfma_f32_16x16x32_bf16 v[196:199], a[72:75], a[144:147], v[196:199]\n"
"s_waitcnt lgkmcnt(0)\n"
"v_mfma_f32_16x16x32_bf16 a[20:23], a[72:75], a[152:155], a[20:23]\n"
"s_sub_u32 s67, 47616, s60\n"
"v_mfma_f32_16x16x32_bf16 a[20:23], a[76:79], a[156:159], a[20:23]\n"
"v_mfma_f32_16x16x32_bf16 a[16:19], a[68:71], a[156:159], a[16:19]\n"
"buffer_load_dwordx4 v6, s[48:51], s67 offen offset:3072  lds\n"
"v_mfma_f32_16x16x32_bf16 a[16:19], a[64:67], a[152:155], a[16:19]\n"
"s_barrier\n"
"s_waitcnt vmcnt(12)\n"
"ds_read_b64_tr_b16 a[32:33], v9 offset:8192\n"
"v_mfma_f32_16x16x32_bf16 v[200:203], a[80:83], a[96:99], v[200:203]\n"
"s_add_u32 m0, s64, 16384\n"
"ds_read_b64_tr_b16 a[34:35], v9 offset:8704\n"
"v_mfma_f32_16x16x32_bf16 v[200:203], a[84:87], a[100:103], v[200:203]\n"
"s_add_u32 s67, %21, 4194304\n"
"ds_read_b64_tr_b16 a[36:37], v9 offset:12288\n"
"v_mfma_f32_16x16x32_bf16 v[228:231], a[92:95], a[100:103], v[228:231]\n"
"ds_read_b128 a[100:103], v13 offset:32768\n"
"v_mfma_f32_16x16x32_bf16 v[228:231], a[88:91], a[96:99], v[228:231]\n"
"ds_read_b128 a[96:99], v12 offset:32768\n"
"buffer_load_dwordx4 v11, s[52:55], s67 offen offset:0 nt lds\n"
"v_mfma_f32_16x16x32_bf16 v[232:235], a[88:91], a[104:107], v[232:235]\n"
"ds_read_b64_tr_b16 a[38:39], v9 offset:12800\n"
"v_mfma_f32_16x16x32_bf16 v[232:235], a[92:95], a[108:111], v[232:235]\n"
"s_add_u32 s67, %21, 4209664\n"
"ds_read_b64_tr_b16 a[40:41], v9 offset:8200\n"
"v_mfma_f32_16x16x32_bf16 v[204:207], a[84:87], a[108:111], v[204:207]\n"
"ds_read_b128 a[108:111], v13 offset:34816\n"
"v_mfma_f32_16x16x32_bf16 v[204:207], a[80:83], a[104:107], v[204:207]\n"
"ds_read_b128 a[104:107], v12 offset:34816\n"
"buffer_load_dwordx4 v11, s[52:55], s67 offen offset:1024 nt lds\n"
"v_mfma_f32_16x16x32_bf16 v[208:211], a[80:83], a[112:115], v[208:211]\n"
"ds_read_b64_tr_b16 a[42:43], v9 offset:8712\n"
"v_mfma_f32_16x16x32_bf16 v[208:211], a[84:87], a[116:119], v[208:211]\n"
"s_add_u32 s67, %21, 4225024\n"
"ds_read_b64_tr_b16 a[44:45], v9 offset:12296\n"
"v_mfma_f32_16x16x32_bf16 v[236:239], a[92:95], a[116:119], v[236:239]\n"
"ds_read_b128 a[116:119], v13 offset:36864\n"
"v_mfma_f32_16x16x32_bf16 v[236:239], a[88:91], a[112:115], v[236:239]\n"
"ds_read_b128 a[112:115], v12 offset:36864\n"
"v_mfma_f32_16x16x32_bf16 v[240:243], a[88:91], a[120:123], v[240:243]\n"
"ds_read_b64_tr_b16 a[46:47], v9 offset:12808\n"
"buffer_load_dwordx4 v11, s[52:55], s67 offen offset:2048 nt lds\n"
"v_mfma_f32_16x16x32_bf16 v[240:243], a[92:95], a[124:127], v[240:243]\n"
"ds_read_b64_tr_b16 a[48:49], v15 offset:8192\n"
"v_mfma_f32_16x16x32_bf16 v[212:215], a[84:87], a[124:127], v[212:215]\n"
"s_add_u32 s67, %21, 4240384\n"
"ds_read_b128 a[124:127], v13 offset:38912\n"
"v_mfma_f32_16x16x32_bf16 v[212:215], a[80:83], a[120:123], v[212:215]\n"
"ds_read_b128 a[120:123], v12 offset:38912\n"
"v_mfma_f32_16x16x32_bf16 v[216:219], a[80:83], a[128:131], v[216:219]\n"
"ds_read_b64_tr_b16 a[50:51], v15 offset:8704\n"
"buffer_load_dwordx4 v11, s[52:55], s67 offen offset:3072 nt lds\n"
"v_mfma_f32_16x16x32_bf16 v[216:219], a[84:87], a[132:135], v[216:219]\n"
"ds_read_b64_tr_b16 a[52:53], v15 offset:12288\n"
"v_mfma_f32_16x16x32_bf16 v[244:247], a[92:95], a[132:135], v[244:247]\n"
"ds_read_b128 a[132:135], v13 offset:24576\n"
"v_mfma_f32_16x16x32_bf16 v[244:247], a[88:91], a[128:131], v[244:247]\n"
"ds_read_b128 a[128:131], v12 offset:24576\n"
"v_mfma_f32_16x16x32_bf16 v[248:251], a[88:91], a[136:139], v[248:251]\n"
"s_add_u32 m0, s64, 8192\n"
"ds_read_b64_tr_b16 a[54:55], v15 offset:12800\n"
"v_mfma_f32_16x16x32_bf16 v[248:251], a[92:95], a[140:143], v[248:251]\n"
"ds_read_b64_tr_b16 a[56:57], v15 offset:8200\n"
"v_mfma_f32_16x16x32_bf16 v[220:223], a[84:87], a[140:143], v[220:223]\n"
"ds_read_b128 a[140:143], v13 offset:26624\n"
"v_mfma_f32_16x16x32_bf16 v[220:223], a[80:83], a[136:139], v[220:223]\n"
"ds_read_b128 a[136:139], v12 offset:26624\n"
"v_mfma_f32_16x16x32_bf16 v[224:227], a[80:83], a[144:147], v[224:227]\n"
"ds_read_b64_tr_b16 a[58:59], v15 offset:8712\n"
"v_mfma_f32_16x16x32_bf16 v[224:227], a[84:87], a[148:151], v[224:227]\n"
"ds_read_b64_tr_b16 a[60:61], v15 offset:12296\n"
"v_mfma_f32_16x16x32_bf16 v[252:255], a[92:95], a[148:151], v[252:255]\n"
"s_sub_u32 s67, 1572864, s75\n"
"ds_read_b128 a[148:151], v13 offset:28672\n"
"v_mfma_f32_16x16x32_bf16 v[252:255], a[88:91], a[144:147], v[252:255]\n"
"ds_read_b128 a[144:147], v12 offset:28672\n"
"ds_read_b64_tr_b16 a[62:63], v15 offset:12808\n"
"v_mfma_f32_16x16x32_bf16 a[28:31], a[88:91], a[152:155], a[28:31]\n"
"v_mfma_f32_16x16x32_bf16 a[28:31], a[92:95], a[156:159], a[28:31]\n"
"v_mfma_f32_16x16x32_bf16 a[24:27], a[84:87], a[156:159], a[24:27]\n"
"ds_read_b128 a[156:159], v13 offset:30720\n"
"v_mfma_f32_16x16x32_bf16 a[24:27], a[80:83], a[152:155], a[24:27]\n"
"s_waitcnt lgkmcnt(15)\n"
"ds_read_b128 a[152:155], v12 offset:30720\n"
"v_mfma_f32_16x16x32_bf16 v[32:35], a[32:35], a[96:99], v[32:35]\n"
"v_mfma_f32_16x16x32_bf16 v[32:35], a[36:39], a[100:103], v[32:35]\n"
"s_waitcnt lgkmcnt(2)\n"
"v_mfma_f32_16x16x32_bf16 v[60:63], a[44:47], a[100:103], v[60:63]\n"
"buffer_load_dwordx4 v11, s[52:55], s67 offen offset:0 lds\n"
"v_mfma_f32_16x16x32_bf16 v[60:63], a[40:43], a[96:99], v[60:63]\n"
"v_mfma_f32_16x16x32_bf16 v[64:67], a[40:43], a[104:107], v[64:67]\n"
"s_sub_u32 s67, 1588224, s75\n"
"v_mfma_f32_16x16x32_bf16 v[64:67], a[44:47], a[108:111], v[64:67]\n"
"v_mfma_f32_16x16x32_bf16 v[36:39], a[36:39], a[108:111], v[36:39]\n"
"buffer_load_dwordx4 v11, s[52:55], s67 offen offset:1024 lds\n"
"v_mfma_f32_16x16x32_bf16 v[36:39], a[32:35], a[104:107], v[36:39]\n"
"v_mfma_f32_16x16x32_bf16 v[40:43], a[32:35], a[112:115], v[40:43]\n"
"s_sub_u32 s67, 1603584, s75\n"
"v_mfma_f32_16x16x32_bf16 v[40:43], a[36:39], a[116:119], v[40:43]\n"
"v_mfma_f32_16x16x32_bf16 v[68:71], a[44:47], a[116:119], v[68:71]\n"
"v_mfma_f32_16x16x32_bf16 v[68:71], a[40:43], a[112:115], v[68:71]\n"
"buffer_load_dwordx4 v11, s[52:55], s67 offen offset:2048 lds\n"
"v_mfma_f32_16x16x32_bf16 v[72:75], a[40:43], a[120:123], v[72:75]\n"
"v_mfma_f32_16x16x32_bf16 v[72:75], a[44:47], a[124:127], v[72:75]\n"
"s_sub_u32 s67, 1618944, s75\n"
"v_mfma_f32_16x16x32_bf16 v[44:47], a[36:39], a[124:127], v[44:47]\n"
"v_mfma_f32_16x16x32_bf16 v[44:47], a[32:35], a[120:123], v[44:47]\n"
"v_mfma_f32_16x16x32_bf16 v[48:51], a[32:35], a[128:131], v[48:51]\n"
"buffer_load_dwordx4 v11, s[52:55], s67 offen offset:3072 lds\n"
"v_mfma_f32_16x16x32_bf16 v[48:51], a[36:39], a[132:135], v[48:51]\n"
"s_add_u32 m0, s64, 12288\n"
"v_mfma_f32_16x16x32_bf16 v[76:79], a[44:47], a[132:135], v[76:79]\n"
"s_waitcnt vmcnt(12)\n"
"ds_read_b64_tr_b16 a[64:65], v9 offset:0\n"
"v_mfma_f32_16x16x32_bf16 v[76:79], a[40:43], a[128:131], v[76:79]\n"
"s_sub_u32 s67, 1638400, s75\n"
"ds_read_b64_tr_b16 a[66:67], v9 offset:512\n"
"v_mfma_f32_16x16x32_bf16 v[80:83], a[40:43], a[136:139], v[80:83]\n"
"ds_read_b64_tr_b16 a[68:69], v9 offset:4096\n"
"v_mfma_f32_16x16x32_bf16 v[80:83], a[44:47], a[140:143], v[80:83]\n"
"ds_read_b64_tr_b16 a[70:71], v9 offset:4608\n"
"buffer_load_dwordx4 v11, s[52:55], s67 offen offset:0 lds\n"
"v_mfma_f32_16x16x32_bf16 v[52:55], a[36:39], a[140:143], v[52:55]\n"
"s_sub_u32 s67, 1653760, s75\n"
"ds_read_b64_tr_b16 a[72:73], v9 offset:8\n"
"v_mfma_f32_16x16x32_bf16 v[52:55], a[32:35], a[136:139], v[52:55]\n"
"s_waitcnt lgkmcnt(5)\n"
"ds_read_b64_tr_b16 a[74:75], v9 offset:520\n"
"v_mfma_f32_16x16x32_bf16 v[56:59], a[32:35], a[144:147], v[56:59]\n"
"ds_read_b64_tr_b16 a[76:77], v9 offset:4104\n"
"v_mfma_f32_16x16x32_bf16 v[56:59], a[36:39], a[148:151], v[56:59]\n"
"ds_read_b64_tr_b16 a[78:79], v9 offset:4616\n"
"v_mfma_f32_16x16x32_bf16 v[84:87], a[44:47], a[148:151], v[84:87]\n"
"buffer_load_dwordx4 v11, s[52:55], s67 offen offset:1024 lds\n"
"v_mfma_f32_16x16x32_bf16 v[84:87], a[40:43], a[144:147], v[84:87]\n"
"ds_read_b64_tr_b16 a[80:81], v15 offset:0\n"
"v_mfma_f32_16x16x32_bf16 a[4:7], a[40:43], a[152:155], a[4:7]\n"
"s_sub_u32 s67, 1669120, s75\n"
"ds_read_b64_tr_b16 a[82:83], v15 offset:512\n"
"v_mfma_f32_16x16x32_bf16 a[4:7], a[44:47], a[156:159], a[4:7]\n"
"ds_read_b64_tr_b16 a[84:85], v15 offset:4096\n"
"v_mfma_f32_16x16x32_bf16 a[0:3], a[36:39], a[156:159], a[0:3]\n"
"ds_read_b64_tr_b16 a[86:87], v15 offset:4608\n"
"buffer_load_dwordx4 v11, s[52:55], s67 offen offset:2048 lds\n"
"v_mfma_f32_16x16x32_bf16 a[0:3], a[32:35], a[152:155], a[0:3]\n"
"ds_read_b64_tr_b16 a[88:89], v15 offset:8\n"
"v_mfma_f32_16x16x32_bf16 v[88:91], a[48:51], a[96:99], v[88:91]\n"
"s_sub_u32 s67, 1684480, s75\n"
"ds_read_b64_tr_b16 a[90:91], v15 offset:520\n"
"v_mfma_f32_16x16x32_bf16 v[88:91], a[52:55], a[100:103], v[88:91]\n"
"ds_read_b64_tr_b16 a[92:93], v15 offset:4104\n"
"v_mfma_f32_16x16x32_bf16 v[116:119], a[60:63], a[100:103], v[116:119]\n"
"ds_read_b64_tr_b16 a[94:95], v15 offset:4616\n"
"v_mfma_f32_16x16x32_bf16 v[116:119], a[56:59], a[96:99], v[116:119]\n"
"buffer_load_dwordx4 v11, s[52:55], s67 offen offset:3072 lds\n"
"v_mfma_f32_16x16x32_bf16 v[120:123], a[56:59], a[104:107], v[120:123]\n"
"v_mfma_f32_16x16x32_bf16 v[120:123], a[60:63], a[108:111], v[120:123]\n"
"s_add_u32 m0, s64, 0\n"
"v_mfma_f32_16x16x32_bf16 v[92:95], a[52:55], a[108:111], v[92:95]\n"
"v_mfma_f32_16x16x32_bf16 v[92:95], a[48:51], a[104:107], v[92:95]\n"
"s_sub_u32 s67, 1572992, s75\n"
"v_mfma_f32_16x16x32_bf16 v[96:99], a[48:51], a[112:115], v[96:99]\n"
"s_waitcnt lgkmcnt(0)\n"
"v_mfma_f32_16x16x32_bf16 v[96:99], a[52:55], a[116:119], v[96:99]\n"
"s_mov_b32 s98, 0\n"
"v_mfma_f32_16x16x32_bf16 v[124:127], a[60:63], a[116:119], v[124:127]\n"
"s_load_dword s98, s[96:97], 8320 glc\n"
"buffer_load_dwordx4 v11, s[52:55], s67 offen offset:0 lds\n"
"v_mfma_f32_16x16x32_bf16 v[124:127], a[56:59], a[112:115], v[124:127]\n"
"v_mfma_f32_16x16x32_bf16 v[128:131], a[56:59], a[120:123], v[128:131]\n"
"s_sub_u32 s67, 1588352, s75\n"
"v_mfma_f32_16x16x32_bf16 v[128:131], a[60:63], a[124:127], v[128:131]\n"
"v_mfma_f32_16x16x32_bf16 v[100:103], a[52:55], a[124:127], v[100:103]\n"
"buffer_load_dwordx4 v11, s[52:55], s67 offen offset:1024 lds\n"
"v_mfma_f32_16x16x32_bf16 v[100:103], a[48:51], a[120:123], v[100:103]\n"
"v_mfma_f32_16x16x32_bf16 v[104:107], a[48:51], a[128:131], v[104:107]\n"
"s_sub_u32 s67, 1603712, s75\n"
"v_mfma_f32_16x16x32_bf16 v[104:107], a[52:55], a[132:135], v[104:107]\n"
"v_mfma_f32_16x16x32_bf16 v[132:135], a[60:63], a[132:135], v[132:135]\n"
"v_mfma_f32_16x16x32_bf16 v[132:135], a[56:59], a[128:131], v[132:135]\n"
"buffer_load_dwordx4 v11, s[52:55], s67 offen offset:2048 lds\n"
"v_mfma_f32_16x16x32_bf16 v[136:139], a[56:59], a[136:139], v[136:139]\n"
"v_mfma_f32_16x16x32_bf16 v[136:139], a[60:63], a[140:143], v[136:139]\n"
"s_sub_u32 s67, 1619072, s75\n"
"v_mfma_f32_16x16x32_bf16 v[108:111], a[52:55], a[140:143], v[108:111]\n"
"buffer_load_dwordx4 v11, s[52:55], s67 offen offset:3072 lds\n"
"v_mfma_f32_16x16x32_bf16 v[108:111], a[48:51], a[136:139], v[108:111]\n"
"s_sub_u32 s69, s69, s98\n"
"v_mfma_f32_16x16x32_bf16 v[112:115], a[48:51], a[144:147], v[112:115]\n"
"s_sub_u32 s69, s69, s98\n"
"s_add_u32 m0, s64, 4096\n"
"v_mfma_f32_16x16x32_bf16 v[112:115], a[52:55], a[148:151], v[112:115]\n"
"v_mfma_f32_16x16x32_bf16 v[140:143], a[60:63], a[148:151], v[140:143]\n"
"s_sub_u32 s69, s69, s98\n"
"s_sub_u32 s67, 1638528, s75\n"
"v_mfma_f32_16x16x32_bf16 v[140:143], a[56:59], a[144:147], v[140:143]\n"
"s_sub_u32 s69, s69, s98\n"
"v_mfma_f32_16x16x32_bf16 a[12:15], a[56:59], a[152:155], a[12:15]\n"
"s_sub_u32 s69, s69, s98\n"
"v_mfma_f32_16x16x32_bf16 a[12:15], a[60:63], a[156:159], a[12:15]\n"
"buffer_load_dwordx4 v11, s[52:55], s67 offen offset:0 lds\n"
"v_mfma_f32_16x16x32_bf16 a[8:11], a[52:55], a[156:159], a[8:11]\n"
"s_sub_u32 s69, s69, s98\n"
"v_mfma_f32_16x16x32_bf16 a[8:11], a[48:51], a[152:155], a[8:11]\n"
"s_sub_u32 s69, s69, s98\n"
"s_sub_u32 s67, 1653888, s75\n"
"v_mfma_f32_16x16x32_bf16 v[144:147], a[64:67], a[96:99], v[144:147]\n"
"s_sub_u32 s69, s69, s98\n"
"v_mfma_f32_16x16x32_bf16 v[144:147], a[68:71], a[100:103], v[144:147]\n"
"buffer_load_dwordx4 v11, s[52:55], s67 offen offset:1024 lds\n"
"v_mfma_f32_16x16x32_bf16 v[172:175], a[76:79], a[100:103], v[172:175]\n"
"s_sub_u32 s69, s69, s98\n"
"v_mfma_f32_16x16x32_bf16 v[172:175], a[72:75], a[96:99], v[172:175]\n"
"s_sub_u32 s69, s69, s98\n"
"s_sub_u32 s67, 1669248, s75\n"
"v_mfma_f32_16x16x32_bf16 v[176:179], a[72:75], a[104:107], v[176:179]\n"
"s_sub_u32 s69, s69, s98\n"
"v_mfma_f32_16x16x32_bf16 v[176:179], a[76:79], a[108:111], v[176:179]\n"
"s_sub_u32 s71, s64, s64\n"
"buffer_load_dwordx4 v11, s[52:55], s67 offen offset:2048 lds\n"
"v_mfma_f32_16x16x32_bf16 v[148:151], a[68:71], a[108:111], v[148:151]\n"
"s_sub_u32 s69, s69, s98\n"
"s_sub_u32 s67, 1684608, s75\n"
"v_mfma_f32_16x16x32_bf16 v[148:151], a[64:67], a[104:107], v[148:151]\n"
"s_sub_u32 s69, s69, s98\n"
"s_add_u32 s71, s71, 16384\n"
"v_mfma_f32_16x16x32_bf16 v[152:155], a[64:67], a[112:115], v[152:155]\n"
"s_sub_u32 s66, 81920, s73\n"
"s_sub_u32 s69, s69, s98\n"
"v_mfma_f32_16x16x32_bf16 v[152:155], a[68:71], a[116:119], v[152:155]\n"
"buffer_load_dwordx4 v11, s[52:55], s67 offen offset:3072 lds\n"
"v_mfma_f32_16x16x32_bf16 v[180:183], a[76:79], a[116:119], v[180:183]\n"
"s_sub_u32 s69, s69, s98\n"
"v_add_u32 v12, s66, v7\n"
"s_waitcnt lgkmcnt(0)\n"
"v_mfma_f32_16x16x32_bf16 v[180:183], a[72:75], a[112:115], v[180:183]\n"
"s_cmp_lt_u32 %16, 2\n"
"s_cselect_b32 s67, 16384, 0\n"
"v_add_u32 v20, s71, v9\n"
"v_mfma_f32_16x16x32_bf16 v[184:187], a[72:75], a[120:123], v[184:187]\n"
"s_add_u32 s67, s67, s72\n"
"v_add_u32 v13, s66, v8\n"
"v_mfma_f32_16x16x32_bf16 v[184:187], a[76:79], a[124:127], v[184:187]\n"
"s_add_u32 m0, s67, 139264\n"
"v_add_u32 v21, s71, v15\n"
"v_mfma_f32_16x16x32_bf16 v[156:159], a[68:71], a[124:127], v[156:159]\n"
"s_sub_u32 s67, 1792, s60\n"
"v_mfma_f32_16x16x32_bf16 v[156:159], a[64:67], a[120:123], v[156:159]\n"
"buffer_load_dwordx4 v6, s[48:51], s67 offen offset:0  lds\n"
"v_mfma_f32_16x16x32_bf16 v[160:163], a[64:67], a[128:131], v[160:163]\n"
"s_waitcnt vmcnt(17)\n"
"ds_read_b64_tr_b16 a[160:161], v20 offset:0\n"
"v_mfma_f32_16x16x32_bf16 v[160:163], a[68:71], a[132:135], v[160:163]\n"
"s_sub_u32 s67, 17152, s60\n"
"v_mfma_f32_16x16x32_bf16 v[188:191], a[76:79], a[132:135], v[188:191]\n"
"ds_read_b64_tr_b16 a[162:163], v20 offset:512\n"
"v_mfma_f32_16x16x32_bf16 v[188:191], a[72:75], a[128:131], v[188:191]\n"
"ds_read_b64_tr_b16 a[164:165], v20 offset:8\n"
"buffer_load_dwordx4 v6, s[48:51], s67 offen offset:1024  lds\n"
"v_mfma_f32_16x16x32_bf16 v[192:195], a[72:75], a[136:139], v[192:195]\n"
"ds_read_b64_tr_b16 a[166:167], v20 offset:520\n"
"v_mfma_f32_16x16x32_bf16 v[192:195], a[76:79], a[140:143], v[192:195]\n"
"s_sub_u32 s67, 32512, s60\n"
"v_mfma_f32_16x16x32_bf16 v[164:167], a[68:71], a[140:143], v[164:167]\n"
"ds_read_b64_tr_b16 a[168:169], v21 offset:0\n"
"v_mfma_f32_16x16x32_bf16 v[164:167], a[64:67], a[136:139], v[164:167]\n"
"ds_read_b64_tr_b16 a[170:171], v21 offset:512\n"
"buffer_load_dwordx4 v6, s[48:51], s67 offen offset:2048  lds\n"
"v_mfma_f32_16x16x32_bf16 v[168:171], a[64:67], a[144:147], v[168:171]\n"
"ds_read_b64_tr_b16 a[172:173], v21 offset:8\n"
"v_mfma_f32_16x16x32_bf16 v[168:171], a[68:71], a[148:151], v[168:171]\n"
"s_sub_u32 s67, 47872, s60\n"
"v_mfma_f32_16x16x32_bf16 v[196:199], a[76:79], a[148:151], v[196:199]\n"
"ds_read_b64_tr_b16 a[174:175], v21 offset:520\n"
"v_mfma_f32_16x16x32_bf16 v[196:199], a[72:75], a[144:147], v[196:199]\n"
"v_mfma_f32_16x16x32_bf16 a[20:23], a[72:75], a[152:155], a[20:23]\n"
"buffer_load_dwordx4 v6, s[48:51], s67 offen offset:3072  lds\n"
"v_mfma_f32_16x16x32_bf16 a[20:23], a[76:79], a[156:159], a[20:23]\n"
"v_mfma_f32_16x16x32_bf16 a[16:19], a[68:71], a[156:159], a[16:19]\n"
"s_add_u32 m0, s64, 20352\n"
"v_mfma_f32_16x16x32_bf16 a[16:19], a[64:67], a[152:155], a[16:19]\n"
"s_barrier\n"
"s_waitcnt vmcnt(12)\n"
"ds_read_b64_tr_b16 a[32:33], v9 offset:8192\n"
"v_mfma_f32_16x16x32_bf16 v[200:203], a[80:83], a[96:99], v[200:203]\n"
"s_waitcnt lgkmcnt(1)\n"
"ds_read_b64_tr_b16 a[34:35], v9 offset:8704\n"
"v_mfma_f32_16x16x32_bf16 v[200:203], a[84:87], a[100:103], v[200:203]\n"
"ds_read_b64_tr_b16 a[36:37], v9 offset:12288\n"
"buffer_load_dwordx4 v6, s[40:43], s88 offen offset:128  lds\n"
"v_mfma_f32_16x16x32_bf16 v[228:231], a[92:95], a[100:103], v[228:231]\n"
"ds_read_b128 a[100:103], v13 offset:32768\n"
"v_mfma_f32_16x16x32_bf16 v[228:231], a[88:91], a[96:99], v[228:231]\n"
"ds_read_b128 a[96:99], v12 offset:32768\n"
"v_mfma_f32_16x16x32_bf16 v[232:235], a[88:91], a[104:107], v[232:235]\n"
"ds_read_b64_tr_b16 a[38:39], v9 offset:12800\n"
"buffer_load_dwordx4 v6, s[40:43], s89 offen offset:1152  lds\n"
"v_mfma_f32_16x16x32_bf16 v[232:235], a[92:95], a[108:111], v[232:235]\n"
"ds_read_b64_tr_b16 a[40:41], v9 offset:8200\n"
"v_mfma_f32_16x16x32_bf16 v[204:207], a[84:87], a[108:111], v[204:207]\n"
"ds_read_b128 a[108:111], v13 offset:34816\n"
"v_mfma_f32_16x16x32_bf16 v[204:207], a[80:83], a[104:107], v[204:207]\n"
"ds_read_b128 a[104:107], v12 offset:34816\n"
"v_mfma_f32_16x16x32_bf16 v[208:211], a[80:83], a[112:115], v[208:211]\n"
"ds_read_b64_tr_b16 a[42:43], v9 offset:8712\n"
"v_mfma_f32_16x16x32_bf16 v[208:211], a[84:87], a[116:119], v[208:211]\n"
"ds_read_b64_tr_b16 a[44:45], v9 offset:12296\n"
"buffer_load_dwordx4 v6, s[40:43], s90 offen offset:2176  lds\n"
"v_mfma_f32_16x16x32_bf16 v[236:239], a[92:95], a[116:119], v[236:239]\n"
"ds_read_b128 a[116:119], v13 offset:36864\n"
"v_mfma_f32_16x16x32_bf16 v[236:239], a[88:91], a[112:115], v[236:239]\n"
"ds_read_b128 a[112:115], v12 offset:36864\n"
"v_mfma_f32_16x16x32_bf16 v[240:243], a[88:91], a[120:123], v[240:243]\n"
"ds_read_b64_tr_b16 a[46:47], v9 offset:12808\n"
"v_mfma_f32_16x16x32_bf16 v[240:243], a[92:95], a[124:127], v[240:243]\n"
"ds_read_b64_tr_b16 a[48:49], v15 offset:8192\n"
"buffer_load_dwordx4 v6, s[40:43], s91 offen offset:3200  lds\n"
"v_mfma_f32_16x16x32_bf16 v[212:215], a[84:87], a[124:127], v[212:215]\n"
"ds_read_b128 a[124:127], v13 offset:38912\n"
"v_mfma_f32_16x16x32_bf16 v[212:215], a[80:83], a[120:123], v[212:215]\n"
"ds_read_b128 a[120:123], v12 offset:38912\n"
"v_mfma_f32_16x16x32_bf16 v[216:219], a[80:83], a[128:131], v[216:219]\n"
"ds_read_b64_tr_b16 a[50:51], v15 offset:8704\n"
"v_mfma_f32_16x16x32_bf16 v[216:219], a[84:87], a[132:135], v[216:219]\n"
"ds_read_b64_tr_b16 a[52:53], v15 offset:12288\n"
"v_mfma_f32_16x16x32_bf16 v[244:247], a[92:95], a[132:135], v[244:247]\n"
"buffer_store_dwordx4 a[180:183], v0, s[36:39], %23 offen offset:2048 sc1\n"
"ds_read_b128 a[132:135], v13 offset:24576\n"
"v_mfma_f32_16x16x32_bf16 v[244:247], a[88:91], a[128:131], v[244:247]\n"
"ds_read_b128 a[128:131], v12 offset:24576\n"
"v_mfma_f32_16x16x32_bf16 v[248:251], a[88:91], a[136:139], v[248:251]\n"
"ds_read_b64_tr_b16 a[54:55], v15 offset:12800\n"
"v_mfma_f32_16x16x32_bf16 v[248:251], a[92:95], a[140:143], v[248:251]\n"
"s_add_u32 m0, s64, 8192\n"
"ds_read_b64_tr_b16 a[56:57], v15 offset:8200\n"
"v_mfma_f32_16x16x32_bf16 v[220:223], a[84:87], a[140:143], v[220:223]\n"
"ds_read_b128 a[140:143], v13 offset:26624\n"
"v_mfma_f32_16x16x32_bf16 v[220:223], a[80:83], a[136:139], v[220:223]\n"
"ds_read_b128 a[136:139], v12 offset:26624\n"
"v_mfma_f32_16x16x32_bf16 v[224:227], a[80:83], a[144:147], v[224:227]\n"
"ds_read_b64_tr_b16 a[58:59], v15 offset:8712\n"
"v_mfma_f32_16x16x32_bf16 v[224:227], a[84:87], a[148:151], v[224:227]\n"
"ds_read_b64_tr_b16 a[60:61], v15 offset:12296\n"
"v_mfma_f32_16x16x32_bf16 v[252:255], a[92:95], a[148:151], v[252:255]\n"
"ds_read_b128 a[148:151], v13 offset:28672\n"
"v_mfma_f32_16x16x32_bf16 v[252:255], a[88:91], a[144:147], v[252:255]\n"
"ds_read_b128 a[144:147], v12 offset:28672\n"
"s_sub_u32 s67, 1835008, s75\n"
"ds_read_b64_tr_b16 a[62:63], v15 offset:12808\n"
"v_mfma_f32_16x16x32_bf16 a[28:31], a[88:91], a[152:155], a[28:31]\n"
"v_mfma_f32_16x16x32_bf16 a[28:31], a[92:95], a[156:159], a[28:31]\n"
"v_mfma_f32_16x16x32_bf16 a[24:27], a[84:87], a[156:159], a[24:27]\n"
"ds_read_b128 a[156:159], v13 offset:30720\n"
"v_mfma_f32_16x16x32_bf16 a[24:27], a[80:83], a[152:155], a[24:27]\n"
"s_waitcnt lgkmcnt(15)\n"
"ds_read_b128 a[152:155], v12 offset:30720\n"
"v_mfma_f32_16x16x32_bf16 v[32:35], a[32:35], a[96:99], v[32:35]\n"
"v_mfma_f32_16x16x32_bf16 v[32:35], a[36:39], a[100:103], v[32:35]\n"
"s_waitcnt lgkmcnt(2)\n"
"v_mfma_f32_16x16x32_bf16 v[60:63], a[44:47], a[100:103], v[60:63]\n"
"buffer_load_dwordx4 v11, s[52:55], s67 offen offset:0 lds\n"
"v_mfma_f32_16x16x32_bf16 v[60:63], a[40:43], a[96:99], v[60:63]\n"
"v_mfma_f32_16x16x32_bf16 v[64:67], a[40:43], a[104:107], v[64:67]\n"
"s_sub_u32 s67, 1850368, s75\n"
"v_mfma_f32_16x16x32_bf16 v[64:67], a[44:47], a[108:111], v[64:67]\n"
"v_mfma_f32_16x16x32_bf16 v[36:39], a[36:39], a[108:111], v[36:39]\n"
"buffer_load_dwordx4 v11, s[52:55], s67 offen offset:1024 lds\n"
"v_mfma_f32_16x16x32_bf16 v[36:39], a[32:35], a[104:107], v[36:39]\n"
"v_mfma_f32_16x16x32_bf16 v[40:43], a[32:35], a[112:115], v[40:43]\n"
"s_sub_u32 s67, 1865728, s75\n"
"v_mfma_f32_16x16x32_bf16 v[40:43], a[36:39], a[116:119], v[40:43]\n"
"v_mfma_f32_16x16x32_bf16 v[68:71], a[44:47], a[116:119], v[68:71]\n"
"v_mfma_f32_16x16x32_bf16 v[68:71], a[40:43], a[112:115], v[68:71]\n"
"buffer_load_dwordx4 v11, s[52:55], s67 offen offset:2048 lds\n"
"v_mfma_f32_16x16x32_bf16 v[72:75], a[40:43], a[120:123], v[72:75]\n"
"v_mfma_f32_16x16x32_bf16 v[72:75], a[44:47], a[124:127], v[72:75]\n"
"s_sub_u32 s67, 1881088, s75\n"
"v_mfma_f32_16x16x32_bf16 v[44:47], a[36:39], a[124:127], v[44:47]\n"
"v_mfma_f32_16x16x32_bf16 v[44:47], a[32:35], a[120:123], v[44:47]\n"
"buffer_load_dwordx4 v11, s[52:55], s67 offen offset:3072 lds\n"
"v_mfma_f32_16x16x32_bf16 v[48:51], a[32:35], a[128:131], v[48:51]\n"
"v_mfma_f32_16x16x32_bf16 v[48:51], a[36:39], a[132:135], v[48:51]\n"
"s_add_u32 m0, s64, 12288\n"
"v_mfma_f32_16x16x32_bf16 v[76:79], a[44:47], a[132:135], v[76:79]\n"
"s_waitcnt vmcnt(13)\n"
"ds_read_b64_tr_b16 a[64:65], v9 offset:0\n"
"v_mfma_f32_16x16x32_bf16 v[76:79], a[40:43], a[128:131], v[76:79]\n"
"s_sub_u32 s67, 1900544, s75\n"
"ds_read_b64_tr_b16 a[66:67], v9 offset:512\n"
"v_mfma_f32_16x16x32_bf16 v[80:83], a[40:43], a[136:139], v[80:83]\n"
"ds_read_b64_tr_b16 a[68:69], v9 offset:4096\n"
"v_mfma_f32_16x16x32_bf16 v[80:83], a[44:47], a[140:143], v[80:83]\n"
"ds_read_b64_tr_b16 a[70:71], v9 offset:4608\n"
"buffer_load_dwordx4 v11, s[52:55], s67 offen offset:0 lds\n"
"v_mfma_f32_16x16x32_bf16 v[52:55], a[36:39], a[140:143], v[52:55]\n"
"s_sub_u32 s67, 1915904, s75\n"
"ds_read_b64_tr_b16 a[72:73], v9 offset:8\n"
"v_mfma_f32_16x16x32_bf16 v[52:55], a[32:35], a[136:139], v[52:55]\n"
"s_waitcnt lgkmcnt(5)\n"
"ds_read_b64_tr_b16 a[74:75], v9 offset:520\n"
"v_mfma_f32_16x16x32_bf16 v[56:59], a[32:35], a[144:147], v[56:59]\n"
"ds_read_b64_tr_b16 a[76:77], v9 offset:4104\n"
"v_mfma_f32_16x16x32_bf16 v[56:59], a[36:39], a[148:151], v[56:59]\n"
"ds_read_b64_tr_b16 a[78:79], v9 offset:4616\n"
"v_mfma_f32_16x16x32_bf16 v[84:87], a[44:47], a[148:151], v[84:87]\n"
"buffer_load_dwordx4 v11, s[52:55], s67 offen offset:1024 lds\n"
"v_mfma_f32_16x16x32_bf16 v[84:87], a[40:43], a[144:147], v[84:87]\n"
"ds_read_b64_tr_b16 a[80:81], v15 offset:0\n"
"v_mfma_f32_16x16x32_bf16 a[4:7], a[40:43], a[152:155], a[4:7]\n"
"s_sub_u32 s67, 1931264, s75\n"
"ds_read_b64_tr_b16 a[82:83], v15 offset:512\n"
"v_mfma_f32_16x16x32_bf16 a[4:7], a[44:47], a[156:159], a[4:7]\n"
"ds_read_b64_tr_b16 a[84:85], v15 offset:4096\n"
"v_mfma_f32_16x16x32_bf16 a[0:3], a[36:39], a[156:159], a[0:3]\n"
"ds_read_b64_tr_b16 a[86:87], v15 offset:4608\n"
"buffer_load_dwordx4 v11, s[52:55], s67 offen offset:2048 lds\n"
"v_mfma_f32_16x16x32_bf16 a[0:3], a[32:35], a[152:155], a[0:3]\n"
"ds_read_b64_tr_b16 a[88:89], v15 offset:8\n"
"v_mfma_f32_16x16x32_bf16 v[88:91], a[48:51], a[96:99], v[88:91]\n"
"s_sub_u32 s67, 1946624, s75\n"
"ds_read_b64_tr_b16 a[90:91], v15 offset:520\n"
"v_mfma_f32_16x16x32_bf16 v[88:91], a[52:55], a[100:103], v[88:91]\n"
"ds_read_b64_tr_b16 a[92:93], v15 offset:4104\n"
"v_mfma_f32_16x16x32_bf16 v[116:119], a[60:63], a[100:103], v[116:119]\n"
"ds_read_b64_tr_b16 a[94:95], v15 offset:4616\n"
"v_mfma_f32_16x16x32_bf16 v[116:119], a[56:59], a[96:99], v[116:119]\n"
"buffer_load_dwordx4 v11, s[52:55], s67 offen offset:3072 lds\n"
"v_mfma_f32_16x16x32_bf16 v[120:123], a[56:59], a[104:107], v[120:123]\n"
"v_mfma_f32_16x16x32_bf16 v[120:123], a[60:63], a[108:111], v[120:123]\n"
"s_add_u32 m0, s64, 16256\n"
"v_mfma_f32_16x16x32_bf16 v[92:95], a[52:55], a[108:111], v[92:95]\n"
"v_mfma_f32_16x16x32_bf16 v[92:95], a[48:51], a[104:107], v[92:95]\n"
"buffer_load_dwordx4 v6, s[40:43], s92 offen offset:128  lds\n"
"v_mfma_f32_16x16x32_bf16 v[96:99], a[48:51], a[112:115], v[96:99]\n"
"s_waitcnt lgkmcnt(0)\n"
"v_mfma_f32_16x16x32_bf16 v[96:99], a[52:55], a[116:119], v[96:99]\n"
"s_mov_b32 s98, 0\n"
"v_mfma_f32_16x16x32_bf16 v[124:127], a[60:63], a[116:119], v[124:127]\n"
"s_load_dword s98, s[96:97], 128 glc\n"
"buffer_load_dwordx4 v6, s[40:43], s93 offen offset:1152  lds\n"
"v_mfma_f32_16x16x32_bf16 v[124:127], a[56:59], a[112:115], v[124:127]\n"
"v_mfma_f32_16x16x32_bf16 v[128:131], a[56:59], a[120:123], v[128:131]\n"
"v_mfma_f32_16x16x32_bf16 v[128:131], a[60:63], a[124:127], v[128:131]\n"
"v_mfma_f32_16x16x32_bf16 v[100:103], a[52:55], a[124:127], v[100:103]\n"
"v_mfma_f32_16x16x32_bf16 v[100:103], a[48:51], a[120:123], v[100:103]\n"
"buffer_load_dwordx4 v6, s[40:43], s94 offen offset:2176  lds\n"
"v_mfma_f32_16x16x32_bf16 v[104:107], a[48:51], a[128:131], v[104:107]\n"
"v_mfma_f32_16x16x32_bf16 v[104:107], a[52:55], a[132:135], v[104:107]\n"
"v_mfma_f32_16x16x32_bf16 v[132:135], a[60:63], a[132:135], v[132:135]\n"
"v_mfma_f32_16x16x32_bf16 v[132:135], a[56:59], a[128:131], v[132:135]\n"
"buffer_load_dwordx4 v6, s[40:43], s95 offen offset:3200  lds\n"
"v_mfma_f32_16x16x32_bf16 v[136:139], a[56:59], a[136:139], v[136:139]\n"
"v_mfma_f32_16x16x32_bf16 v[136:139], a[60:63], a[140:143], v[136:139]\n"
"s_add_u32 m0, s64, 0\n"
"v_mfma_f32_16x16x32_bf16 v[108:111], a[52:55], a[140:143], v[108:111]\n"
"v_mfma_f32_16x16x32_bf16 v[108:111], a[48:51], a[136:139], v[108:111]\n"
"s_add_u32 s69, s69, s98\n"
"s_sub_u32 s67, 1835136, s75\n"
"v_mfma_f32_16x16x32_bf16 v[112:115], a[48:51], a[144:147], v[112:115]\n"
"s_add_u32 s69, s69, s98\n"
"v_mfma_f32_16x16x32_bf16 v[112:115], a[52:55], a[148:151], v[112:115]\n"
"buffer_load_dwordx4 v11, s[52:55], s67 offen offset:0 lds\n"
"v_mfma_f32_16x16x32_bf16 v[140:143], a[60:63], a[148:151], v[140:143]\n"
"s_add_u32 s69, s69, s98\n"
"buffer_load_dwordx4 v[24:27], v0, s[56:59], %25 offen sc1\n"
"v_mfma_f32_16x16x32_bf16 v[140:143], a[56:59], a[144:147], v[140:143]\n"
"s_add_u32 s69, s69, s98\n"
"s_sub_u32 s67, 1850496, s75\n"
"v_mfma_f32_16x16x32_bf16 a[12:15], a[56:59], a[152:155], a[12:15]\n"
"s_add_u32 s69, s69, s98\n"
"v_mfma_f32_16x16x32_bf16 a[12:15], a[60:63], a[156:159], a[12:15]\n"
"buffer_load_dwordx4 v11, s[52:55], s67 offen offset:1024 lds\n"
"v_mfma_f32_16x16x32_bf16 a[8:11], a[52:55], a[156:159], a[8:11]\n"
"s_add_u32 s69, s69, s98\n"
"v_mfma_f32_16x16x32_bf16 a[8:11], a[48:51], a[152:155], a[8:11]\n"
"s_add_u32 s69, s69, s98\n"
"s_sub_u32 s67, 1865856, s75\n"
"v_mfma_f32_16x16x32_bf16 v[144:147], a[64:67], a[96:99], v[144:147]\n"
"s_add_u32 s69, s69, s98\n"
"v_mfma_f32_16x16x32_bf16 v[144:147], a[68:71], a[100:103], v[144:147]\n"
"buffer_load_dwordx4 v11, s[52:55], s67 offen offset:2048 lds\n"
"v_mfma_f32_16x16x32_bf16 v[172:175], a[76:79], a[100:103], v[172:175]\n"
"s_add_u32 s69, s69, s98\n"
"v_mfma_f32_16x16x32_bf16 v[172:175], a[72:75], a[96:99], v[172:175]\n"
"s_add_u32 s69, s69, s98\n"
"s_sub_u32 s67, 1881216, s75\n"
"v_mfma_f32_16x16x32_bf16 v[176:179], a[72:75], a[104:107], v[176:179]\n"
"s_add_u32 s69, s69, s98\n"
"v_mfma_f32_16x16x32_bf16 v[176:179], a[76:79], a[108:111], v[176:179]\n"
"s_sub_u32 s71, s64, s64\n"
"v_mfma_f32_16x16x32_bf16 v[148:151], a[68:71], a[108:111], v[148:151]\n"
"s_add_u32 s69, s69, s98\n"
"buffer_load_dwordx4 v11, s[52:55], s67 offen offset:3072 lds\n"
"v_mfma_f32_16x16x32_bf16 v[148:151], a[64:67], a[104:107], v[148:151]\n"
"s_add_u32 s69, s69, s98\n"
"s_add_u32 s71, s71, 20480\n"
"v_mfma_f32_16x16x32_bf16 v[152:155], a[64:67], a[112:115], v[152:155]\n"
"s_sub_u32 s66, 122880, s73\n"
"s_add_u32 s69, s69, s98\n"
"v_mfma_f32_16x16x32_bf16 v[152:155], a[68:71], a[116:119], v[152:155]\n"
"s_add_u32 m0, s64, 4096\n"
"s_sub_u32 s67, 1900672, s75\n"
"v_mfma_f32_16x16x32_bf16 v[180:183], a[76:79], a[116:119], v[180:183]\n"
"s_add_u32 s69, s69, s98\n"
"v_add_u32 v12, s66, v7\n"
"s_waitcnt lgkmcnt(0)\n"
"v_mfma_f32_16x16x32_bf16 v[180:183], a[72:75], a[112:115], v[180:183]\n"
"v_add_u32 v20, s71, v7\n"
"v_mfma_f32_16x16x32_bf16 v[184:187], a[72:75], a[120:123], v[184:187]\n"
"buffer_load_dwordx4 v11, s[52:55], s67 offen offset:0 lds\n"
"v_add_u32 v13, s66, v8\n"
"v_mfma_f32_16x16x32_bf16 v[184:187], a[76:79], a[124:127], v[184:187]\n"
"v_add_u32 v21, s71, v8\n"
"v_mfma_f32_16x16x32_bf16 v[156:159], a[68:71], a[124:127], v[156:159]\n"
"s_sub_u32 s67, 1916032, s75\n"
"v_mfma_f32_16x16x32_bf16 v[156:159], a[64:67], a[120:123], v[156:159]\n"
"v_mfma_f32_16x16x32_bf16 v[160:163], a[64:67], a[128:131], v[160:163]\n"
"s_waitcnt vmcnt(19)\n"
"ds_read_b128 a[224:227], v20 offset:0\n"
"v_mfma_f32_16x16x32_bf16 v[160:163], a[68:71], a[132:135], v[160:163]\n"
"buffer_load_dwordx4 v11, s[52:55], s67 offen offset:1024 lds\n"
"v_mfma_f32_16x16x32_bf16 v[188:191], a[76:79], a[132:135], v[188:191]\n"
"ds_read_b128 a[228:231], v21 offset:0\n"
"v_mfma_f32_16x16x32_bf16 v[188:191], a[72:75], a[128:131], v[188:191]\n"
"s_sub_u32 s67, 1931392, s75\n"
"ds_read_b128 a[232:235], v20 offset:2048\n"
"v_mfma_f32_16x16x32_bf16 v[192:195], a[72:75], a[136:139], v[192:195]\n"
"buffer_load_dwordx4 v11, s[52:55], s67 offen offset:2048 lds\n"
"v_mfma_f32_16x16x32_bf16 v[192:195], a[76:79], a[140:143], v[192:195]\n"
"ds_read_b128 a[236:239], v21 offset:2048\n"
"v_mfma_f32_16x16x32_bf16 v[164:167], a[68:71], a[140:143], v[164:167]\n"
"s_sub_u32 s67, 1946752, s75\n"
"v_mfma_f32_16x16x32_bf16 v[164:167], a[64:67], a[136:139], v[164:167]\n"
"v_mfma_f32_16x16x32_bf16 v[168:171], a[64:67], a[144:147], v[168:171]\n"
"v_mfma_f32_16x16x32_bf16 v[168:171], a[68:71], a[148:151], v[168:171]\n"
"buffer_load_dwordx4 v11, s[52:55], s67 offen offset:3072 lds\n"
"v_mfma_f32_16x16x32_bf16 v[196:199], a[76:79], a[148:151], v[196:199]\n"
"v_mfma_f32_16x16x32_bf16 v[196:199], a[72:75], a[144:147], v[196:199]\n"
"s_waitcnt lgkmcnt(0)\n"
"v_mfma_f32_16x16x32_bf16 a[20:23], a[72:75], a[152:155], a[20:23]\n"
"v_mfma_f32_16x16x32_bf16 a[20:23], a[76:79], a[156:159], a[20:23]\n"
"v_mfma_f32_16x16x32_bf16 a[16:19], a[68:71], a[156:159], a[16:19]\n"
"v_mfma_f32_16x16x32_bf16 a[16:19], a[64:67], a[152:155], a[16:19]\n"
"s_barrier\n"
"s_waitcnt vmcnt(13)\n"
"ds_read_b64_tr_b16 a[32:33], v9 offset:8192\n"
"v_mfma_f32_16x16x32_bf16 v[200:203], a[80:83], a[96:99], v[200:203]\n"
"buffer_store_dwordx4 a[184:187], v1, s[36:39], %23 offen offset:0 sc1\n"
"ds_read_b64_tr_b16 a[34:35], v9 offset:8704\n"
"v_mfma_f32_16x16x32_bf16 v[200:203], a[84:87], a[100:103], v[200:203]\n"
"ds_read_b64_tr_b16 a[36:37], v9 offset:12288\n"
"v_mfma_f32_16x16x32_bf16 v[228:231], a[92:95], a[100:103], v[228:231]\n"
"ds_read_b128 a[100:103], v13 offset:32768\n"
"v_mfma_f32_16x16x32_bf16 v[228:231], a[88:91], a[96:99], v[228:231]\n"
"ds_read_b128 a[96:99], v12 offset:32768\n"
"v_mfma_f32_16x16x32_bf16 v[232:235], a[88:91], a[104:107], v[232:235]\n"
"ds_read_b64_tr_b16 a[38:39], v9 offset:12800\n"
"v_mfma_f32_16x16x32_bf16 v[232:235], a[92:95], a[108:111], v[232:235]\n"
"ds_read_b64_tr_b16 a[40:41], v9 offset:8200\n"
"v_mfma_f32_16x16x32_bf16 v[204:207], a[84:87], a[108:111], v[204:207]\n"
"ds_read_b128 a[108:111], v13 offset:34816\n"
"v_mfma_f32_16x16x32_bf16 v[204:207], a[80:83], a[104:107], v[204:207]\n"
"ds_read_b128 a[104:107], v12 offset:34816\n"
"v_mfma_f32_16x16x32_bf16 v[208:211], a[80:83], a[112:115], v[208:211]\n"
"ds_read_b64_tr_b16 a[42:43], v9 offset:8712\n"
"v_mfma_f32_16x16x32_bf16 v[208:211], a[84:87], a[116:119], v[208:211]\n"
"ds_read_b64_tr_b16 a[44:45], v9 offset:12296\n"
"v_mfma_f32_16x16x32_bf16 v[236:239], a[92:95], a[116:119], v[236:239]\n"
"ds_read_b128 a[116:119], v13 offset:36864\n"
"v_mfma_f32_16x16x32_bf16 v[236:239], a[88:91], a[112:115], v[236:239]\n"
"buffer_store_dwordx4 a[188:191], v1, s[36:39], %23 offen offset:2048 sc1\n"
"ds_read_b128 a[112:115], v12 offset:36864\n"
"v_mfma_f32_16x16x32_bf16 v[240:243], a[88:91], a[120:123], v[240:243]\n"
"ds_read_b64_tr_b16 a[46:47], v9 offset:12808\n"
"v_mfma_f32_16x16x32_bf16 v[240:243], a[92:95], a[124:127], v[240:243]\n"
"ds_read_b64_tr_b16 a[48:49], v15 offset:8192\n"
"v_mfma_f32_16x16x32_bf16 v[212:215], a[84:87], a[124:127], v[212:215]\n"
"ds_read_b128 a[124:127], v13 offset:38912\n"
"v_mfma_f32_16x16x32_bf16 v[212:215], a[80:83], a[120:123], v[212:215]\n"
"s_add_u32 s74, %23, 524288\n"
"ds_read_b128 a[120:123], v12 offset:38912\n"
"v_mfma_f32_16x16x32_bf16 v[216:219], a[80:83], a[128:131], v[216:219]\n"
"ds_read_b64_tr_b16 a[50:51], v15 offset:8704\n"
"v_mfma_f32_16x16x32_bf16 v[216:219], a[84:87], a[132:135], v[216:219]\n"
"ds_read_b64_tr_b16 a[52:53], v15 offset:12288\n"
"v_mfma_f32_16x16x32_bf16 v[244:247], a[92:95], a[132:135], v[244:247]\n"
"ds_read_b128 a[132:135], v13 offset:24576\n"
"v_mfma_f32_16x16x32_bf16 v[244:247], a[88:91], a[128:131], v[244:247]\n"
"ds_read_b128 a[128:131], v12 offset:24576\n"
"v_mfma_f32_16x16x32_bf16 v[248:251], a[88:91], a[136:139], v[248:251]\n"
"ds_read_b64_tr_b16 a[54:55], v15 offset:12800\n"
"v_mfma_f32_16x16x32_bf16 v[248:251], a[92:95], a[140:143], v[248:251]\n"
"ds_read_b64_tr_b16 a[56:57], v15 offset:8200\n"
"v_mfma_f32_16x16x32_bf16 v[220:223], a[84:87], a[140:143], v[220:223]\n"
"ds_read_b128 a[140:143], v13 offset:26624\n"
"v_mfma_f32_16x16x32_bf16 v[220:223], a[80:83], a[136:139], v[220:223]\n"
"ds_read_b128 a[136:139], v12 offset:26624\n"
"v_mfma_f32_16x16x32_bf16 v[224:227], a[80:83], a[144:147], v[224:227]\n"
"buffer_store_dwordx4 a[160:163], v0, s[36:39], s74 offen offset:0 sc1\n"
"ds_read_b64_tr_b16 a[58:59], v15 offset:8712\n"
"v_mfma_f32_16x16x32_bf16 v[224:227], a[84:87], a[148:151], v[224:227]\n"
"ds_read_b64_tr_b16 a[60:61], v15 offset:12296\n"
"v_mfma_f32_16x16x32_bf16 v[252:255], a[92:95], a[148:151], v[252:255]\n"
"ds_read_b128 a[148:151], v13 offset:28672\n"
"v_mfma_f32_16x16x32_bf16 v[252:255], a[88:91], a[144:147], v[252:255]\n"
"ds_read_b128 a[144:147], v12 offset:28672\n"
"s_sub_u32 s71, s64, s64\n"
"ds_read_b64_tr_b16 a[62:63], v15 offset:12808\n"
"v_mfma_f32_16x16x32_bf16 a[28:31], a[88:91], a[152:155], a[28:31]\n"
"v_mfma_f32_16x16x32_bf16 a[28:31], a[92:95], a[156:159], a[28:31]\n"
"v_mfma_f32_16x16x32_bf16 a[24:27], a[84:87], a[156:159], a[24:27]\n"
"s_add_u32 s71, s71, 16384\n"
"ds_read_b128 a[156:159], v13 offset:30720\n"
"v_mfma_f32_16x16x32_bf16 a[24:27], a[80:83], a[152:155], a[24:27]\n"
"s_waitcnt lgkmcnt(15)\n"
"ds_read_b128 a[152:155], v12 offset:30720\n"
"v_mfma_f32_16x16x32_bf16 v[32:35], a[32:35], a[96:99], v[32:35]\n"
"v_add_u32 v20, s71, v7\n"
"v_mfma_f32_16x16x32_bf16 v[32:35], a[36:39], a[100:103], v[32:35]\n"
"s_waitcnt lgkmcnt(2)\n"
"v_mfma_f32_16x16x32_bf16 v[60:63], a[44:47], a[100:103], v[60:63]\n"
"v_mfma_f32_16x16x32_bf16 v[60:63], a[40:43], a[96:99], v[60:63]\n"
"v_add_u32 v21, s71, v8\n"
"v_mfma_f32_16x16x32_bf16 v[64:67], a[40:43], a[104:107], v[64:67]\n"
"buffer_store_dwordx4 a[164:167], v0, s[36:39], s74 offen offset:2048 sc1\n"
"v_mfma_f32_16x16x32_bf16 v[64:67], a[44:47], a[108:111], v[64:67]\n"
"v_mfma_f32_16x16x32_bf16 v[36:39], a[36:39], a[108:111], v[36:39]\n"
"s_waitcnt vmcnt(13)\n"
"ds_read_b128 a[240:243], v20 offset:0\n"
"v_mfma_f32_16x16x32_bf16 v[36:39], a[32:35], a[104:107], v[36:39]\n"
"v_mfma_f32_16x16x32_bf16 v[40:43], a[32:35], a[112:115], v[40:43]\n"
"ds_read_b128 a[244:247], v21 offset:0\n"
"v_mfma_f32_16x16x32_bf16 v[40:43], a[36:39], a[116:119], v[40:43]\n"
"ds_read_b128 a[248:251], v20 offset:2048\n"
"v_mfma_f32_16x16x32_bf16 v[68:71], a[44:47], a[116:119], v[68:71]\n"
"s_add_u32 s70, s78, 1\n"
"v_mfma_f32_16x16x32_bf16 v[68:71], a[40:43], a[112:115], v[68:71]\n"
"ds_read_b128 a[252:255], v21 offset:2048\n"
"v_mfma_f32_16x16x32_bf16 v[72:75], a[40:43], a[120:123], v[72:75]\n"
"v_mov_b32 v16, s69\n"
"v_mfma_f32_16x16x32_bf16 v[72:75], a[44:47], a[124:127], v[72:75]\n"
"v_mfma_f32_16x16x32_bf16 v[44:47], a[36:39], a[124:127], v[44:47]\n"
"v_mfma_f32_16x16x32_bf16 v[44:47], a[32:35], a[120:123], v[44:47]\n"
"v_mfma_f32_16x16x32_bf16 v[48:51], a[32:35], a[128:131], v[48:51]\n"
"v_mfma_f32_16x16x32_bf16 v[48:51], a[36:39], a[132:135], v[48:51]\n"
"s_waitcnt lgkmcnt(0)\n"
"buffer_store_dwordx4 a[168:171], v1, s[36:39], s74 offen offset:0 sc1\n"
"ds_write_b32 v2, v16 offset:8192\n"
"v_mfma_f32_16x16x32_bf16 v[76:79], a[44:47], a[132:135], v[76:79]\n"
"s_waitcnt vmcnt(5)\n"
"ds_read_b64_tr_b16 a[64:65], v9 offset:0\n"
"v_mfma_f32_16x16x32_bf16 v[76:79], a[40:43], a[128:131], v[76:79]\n"
"ds_read_b64_tr_b16 a[66:67], v9 offset:512\n"
"v_mfma_f32_16x16x32_bf16 v[80:83], a[40:43], a[136:139], v[80:83]\n"
"ds_read_b64_tr_b16 a[68:69], v9 offset:4096\n"
"v_mfma_f32_16x16x32_bf16 v[80:83], a[44:47], a[140:143], v[80:83]\n"
"ds_read_b64_tr_b16 a[70:71], v9 offset:4608\n"
"v_mfma_f32_16x16x32_bf16 v[52:55], a[36:39], a[140:143], v[52:55]\n"
"ds_read_b64_tr_b16 a[72:73], v9 offset:8\n"
"v_subrev_u32 v24, s70, v24\n"
"v_mfma_f32_16x16x32_bf16 v[52:55], a[32:35], a[136:139], v[52:55]\n"
"ds_read_b64_tr_b16 a[74:75], v9 offset:520\n"
"v_xor_b32 v25, s79, v25\n"
"v_mfma_f32_16x16x32_bf16 v[56:59], a[32:35], a[144:147], v[56:59]\n"
"ds_read_b64_tr_b16 a[76:77], v9 offset:4104\n"
"v_subrev_u32 v26, s70, v26\n"
"v_mfma_f32_16x16x32_bf16 v[56:59], a[36:39], a[148:151], v[56:59]\n"
"ds_read_b64_tr_b16 a[78:79], v9 offset:4616\n"
"v_xor_b32 v27, s79, v27\n"
"v_mfma_f32_16x16x32_bf16 v[84:87], a[44:47], a[148:151], v[84:87]\n"
"ds_read_b64_tr_b16 a[80:81], v15 offset:0\n"
"v_or_b32 v24, v24, v26\n"
"v_mfma_f32_16x16x32_bf16 v[84:87], a[40:43], a[144:147], v[84:87]\n"
"ds_read_b64_tr_b16 a[82:83], v15 offset:512\n"
"v_or_b32 v25, v25, v27\n"
"v_mfma_f32_16x16x32_bf16 a[4:7], a[40:43], a[152:155], a[4:7]\n"
"ds_read_b64_tr_b16 a[84:85], v15 offset:4096\n"
"v_or_b32 v24, v24, v25\n"
"v_mfma_f32_16x16x32_bf16 a[4:7], a[44:47], a[156:159], a[4:7]\n"
"ds_read_b64_tr_b16 a[86:87], v15 offset:4608\n"
"v_cmp_lt_u32 vcc, 15, v24\n"
"v_mfma_f32_16x16x32_bf16 a[0:3], a[36:39], a[156:159], a[0:3]\n"
"buffer_store_dwordx4 a[172:175], v1, s[36:39], s74 offen offset:2048 sc1\n"
"ds_read_b64_tr_b16 a[88:89], v15 offset:8\n"
"s_cbranch_vccnz L_slow3\n"
"L_back3:\n"
"v_mfma_f32_16x16x32_bf16 a[0:3], a[32:35], a[152:155], a[0:3]\n"
"ds_read_b64_tr_b16 a[90:91], v15 offset:520\n"
"v_mfma_f32_16x16x32_bf16 v[88:91], a[48:51], a[96:99], v[88:91]\n"
"s_waitcnt lgkmcnt(14)\n"
"ds_read_b64_tr_b16 a[92:93], v15 offset:4104\n"
"v_mfma_f32_16x16x32_bf16 v[88:91], a[52:55], a[100:103], v[88:91]\n"
"global_load_dwordx4 a[32:35], v0, s[34:35] offset:-4096 sc1\n"
"s_barrier\n"
"v_mfma_f32_16x16x32_bf16 v[116:119], a[60:63], a[100:103], v[116:119]\n"
"ds_read_b64_tr_b16 a[94:95], v15 offset:4616\n"
"v_mfma_f32_16x16x32_bf16 v[116:119], a[56:59], a[96:99], v[116:119]\n"
"ds_write_b64 v2, a[192:193] offset:16384\n"
"v_mfma_f32_16x16x32_bf16 v[120:123], a[56:59], a[104:107], v[120:123]\n"
"ds_write_b64 v2, a[194:195] offset:16392\n"
"v_mfma_f32_16x16x32_bf16 v[120:123], a[60:63], a[108:111], v[120:123]\n"
"ds_read_b32 v17, v3 offset:8192\n"
"v_mfma_f32_16x16x32_bf16 v[92:95], a[52:55], a[108:111], v[92:95]\n"
"ds_read_b128 a[160:163], v7 offset:24576\n"
"v_mfma_f32_16x16x32_bf16 v[92:95], a[48:51], a[104:107], v[92:95]\n"
"ds_read_b32 v18, v4 offset:8192\n"
"v_mfma_f32_16x16x32_bf16 v[96:99], a[48:51], a[112:115], v[96:99]\n"
"s_waitcnt lgkmcnt(5)\n"
"ds_read_b128 a[164:167], v8 offset:24576\n"
"v_mfma_f32_16x16x32_bf16 v[96:99], a[52:55], a[116:119], v[96:99]\n"
"global_load_dwordx4 a[36:39], v0, s[34:35] offset:-3072 sc1\n"
"v_mfma_f32_16x16x32_bf16 v[124:127], a[60:63], a[116:119], v[124:127]\n"
"ds_read_b128 a[168:171], v7 offset:26624\n"
"v_mfma_f32_16x16x32_bf16 v[124:127], a[56:59], a[112:115], v[124:127]\n"
"ds_read_b128 a[172:175], v8 offset:26624\n"
"v_mfma_f32_16x16x32_bf16 v[128:131], a[56:59], a[120:123], v[128:131]\n"
"ds_read_b32 v19, v5 offset:8192\n"
"v_mfma_f32_16x16x32_bf16 v[128:131], a[60:63], a[124:127], v[128:131]\n"
"ds_read_b128 a[176:179], v7 offset:28672\n"
"v_mfma_f32_16x16x32_bf16 v[100:103], a[52:55], a[124:127], v[100:103]\n"
"ds_read_b128 a[180:183], v8 offset:28672\n"
"v_mfma_f32_16x16x32_bf16 v[100:103], a[48:51], a[120:123], v[100:103]\n"
"ds_write_b64 v2, a[196:197] offset:17408\n"
"v_mfma_f32_16x16x32_bf16 v[104:107], a[48:51], a[128:131], v[104:107]\n"
"ds_read_b128 a[184:187], v7 offset:30720\n"
"v_mfma_f32_16x16x32_bf16 v[104:107], a[52:55], a[132:135], v[104:107]\n"
"global_load_dwordx4 a[40:43], v0, s[34:35] offset:-2048 sc1\n"
"v_mfma_f32_16x16x32_bf16 v[132:135], a[60:63], a[132:135], v[132:135]\n"
"ds_read_b128 a[188:191], v8 offset:30720\n"
"v_mfma_f32_16x16x32_bf16 v[132:135], a[56:59], a[128:131], v[132:135]\n"
"ds_write_b64 v2, a[198:199] offset:17416\n"
"v_mfma_f32_16x16x32_bf16 v[136:139], a[56:59], a[136:139], v[136:139]\n"
"ds_write_b64 v2, a[200:201] offset:18432\n"
"v_mfma_f32_16x16x32_bf16 v[136:139], a[60:63], a[140:143], v[136:139]\n"
"ds_write_b64 v2, a[202:203] offset:18440\n"
"v_mfma_f32_16x16x32_bf16 v[108:111], a[52:55], a[140:143], v[108:111]\n"
"ds_write_b64 v2, a[204:205] offset:19456\n"
"v_mfma_f32_16x16x32_bf16 v[108:111], a[48:51], a[136:139], v[108:111]\n"
"s_waitcnt lgkmcnt(9)\n"
"v_add_u32 v16, v16, v17\n"
"v_mfma_f32_16x16x32_bf16 v[112:115], a[48:51], a[144:147], v[112:115]\n"
"ds_write_b64 v2, a[206:207] offset:19464\n"
"v_mfma_f32_16x16x32_bf16 v[112:115], a[52:55], a[148:151], v[112:115]\n"
"global_load_dwordx4 a[44:47], v0, s[34:35] offset:-1024 sc1\n"
"v_mfma_f32_16x16x32_bf16 v[140:143], a[60:63], a[148:151], v[140:143]\n"
"ds_write_b64 v2, a[208:209] offset:20480\n"
"v_mfma_f32_16x16x32_bf16 v[140:143], a[56:59], a[144:147], v[140:143]\n"
"v_add3_u32 v16, v16, v18, v19\n"
"v_mfma_f32_16x16x32_bf16 a[12:15], a[56:59], a[152:155], a[12:15]\n"
"ds_write_b64 v2, a[210:211] offset:20488\n"
"v_mfma_f32_16x16x32_bf16 a[12:15], a[60:63], a[156:159], a[12:15]\n"
"ds_write_b64 v2, a[212:213] offset:21504\n"
"v_mfma_f32_16x16x32_bf16 a[8:11], a[52:55], a[156:159], a[8:11]\n"
"ds_write_b64 v2, a[214:215] offset:21512\n"
"v_mfma_f32_16x16x32_bf16 a[8:11], a[48:51], a[152:155], a[8:11]\n"
"v_readfirstlane_b32 s69, v16\n"
"v_mfma_f32_16x16x32_bf16 v[144:147], a[64:67], a[96:99], v[144:147]\n"
"ds_write_b64 v2, a[216:217] offset:22528\n"
"v_mfma_f32_16x16x32_bf16 v[144:147], a[68:71], a[100:103], v[144:147]\n"
"global_load_dwordx4 a[48:51], v0, s[34:35] offset:0 sc1\n"
"v_mfma_f32_16x16x32_bf16 v[172:175], a[76:79], a[100:103], v[172:175]\n"
"ds_write_b64 v2, a[218:219] offset:22536\n"
"v_mfma_f32_16x16x32_bf16 v[172:175], a[72:75], a[96:99], v[172:175]\n"
"ds_write_b64 v2, a[220:221] offset:23552\n"
"v_mfma_f32_16x16x32_bf16 v[176:179], a[72:75], a[104:107], v[176:179]\n"
"ds_write_b64 v2, a[222:223] offset:23560\n"
"v_mfma_f32_16x16x32_bf16 v[176:179], a[76:79], a[108:111], v[176:179]\n"
"ds_write_b64 v2, a[224:225] offset:24576\n"
"v_mfma_f32_16x16x32_bf16 v[148:151], a[68:71], a[108:111], v[148:151]\n"
"s_cmp_le_i32 s69, -4\n"
"s_cselect_b32 s66, 2, 1\n"
"v_mfma_f32_16x16x32_bf16 v[148:151], a[64:67], a[104:107], v[148:151]\n"
"ds_write_b64 v2, a[226:227] offset:24584\n"
"v_mfma_f32_16x16x32_bf16 v[152:155], a[64:67], a[112:115], v[152:155]\n"
"s_cmp_ge_i32 s69, 4\n"
"s_cselect_b32 s66, 3, s66\n"
"v_mfma_f32_16x16x32_bf16 v[152:155], a[68:71], a[116:119], v[152:155]\n"
"global_load_dwordx4 a[52:55], v0, s[34:35] offset:1024 sc1\n"
"v_mfma_f32_16x16x32_bf16 v[180:183], a[76:79], a[116:119], v[180:183]\n"
"s_lshl_b32 s73, s66, 13\n"
"s_xor_b32 s60, s79, s66\n"
"v_mfma_f32_16x16x32_bf16 v[180:183], a[72:75], a[112:115], v[180:183]\n"
"ds_write_b64 v2, a[228:229] offset:25600\n"
"v_mfma_f32_16x16x32_bf16 v[184:187], a[72:75], a[120:123], v[184:187]\n"
"s_cmp_eq_u32 s66, 3\n"
"s_cselect_b32 s72, 8192, 0\n"
"v_mfma_f32_16x16x32_bf16 v[184:187], a[76:79], a[124:127], v[184:187]\n"
"ds_write_b64 v2, a[230:231] offset:25608\n"
"v_mfma_f32_16x16x32_bf16 v[156:159], a[68:71], a[124:127], v[156:159]\n"
"s_add_u32 s52, s56, %11\n"
"ds_write_b64 v2, a[232:233] offset:26624\n"
"s_addc_u32 s53, s57, 0\n"
"v_mfma_f32_16x16x32_bf16 v[156:159], a[64:67], a[120:123], v[156:159]\n"
"ds_write_b64 v2, a[234:235] offset:26632\n"
"v_mfma_f32_16x16x32_bf16 v[160:163], a[64:67], a[128:131], v[160:163]\n"
"s_add_u32 s52, s52, 4096\n"
"ds_write_b64 v2, a[236:237] offset:27648\n"
"s_addc_u32 s53, s53, 0\n"
"v_mfma_f32_16x16x32_bf16 v[160:163], a[68:71], a[132:135], v[160:163]\n"
"global_load_dwordx4 a[56:59], v0, s[34:35] offset:2048 sc1\n"
"v_mfma_f32_16x16x32_bf16 v[188:191], a[76:79], a[132:135], v[188:191]\n"
"s_add_u32 s54, s52, s73\n"
"ds_write_b64 v2, a[238:239] offset:27656\n"
"s_addc_u32 s55, s53, 0\n"
"v_mfma_f32_16x16x32_bf16 v[188:191], a[72:75], a[128:131], v[188:191]\n"
"ds_write_b64 v2, a[240:241] offset:28672\n"
"v_mfma_f32_16x16x32_bf16 v[192:195], a[72:75], a[136:139], v[192:195]\n"
"s_add_u32 s52, s52, s72\n"
"ds_write_b64 v2, a[242:243] offset:28680\n"
"s_addc_u32 s53, s53, 0\n"
"v_mfma_f32_16x16x32_bf16 v[192:195], a[76:79], a[140:143], v[192:195]\n"
"ds_write_b64 v2, a[244:245] offset:29696\n"
"v_mfma_f32_16x16x32_bf16 v[164:167], a[68:71], a[140:143], v[164:167]\n"
"s_add_u32 s48, s56, %12\n"
"ds_write_b64 v2, a[246:247] offset:29704\n"
"s_addc_u32 s49, s57, 0\n"
"v_mfma_f32_16x16x32_bf16 v[164:167], a[64:67], a[136:139], v[164:167]\n"
"s_add_u32 s74, s78, 2\n"
"v_mfma_f32_16x16x32_bf16 v[168:171], a[64:67], a[144:147], v[168:171]\n"
"s_mov_b32 s50, 32768\n"
"ds_write_b64 v2, a[248:249] offset:30720\n"
"v_mfma_f32_16x16x32_bf16 v[168:171], a[68:71], a[148:151], v[168:171]\n"
"global_load_dwordx4 a[60:63], v0, s[34:35] offset:3072 sc1\n"
"v_mov_b32 v28, s74\n"
"v_mfma_f32_16x16x32_bf16 v[196:199], a[76:79], a[148:151], v[196:199]\n"
"s_mov_b32 s70, 0\n"
"v_mov_b32 v29, s79\n"
"v_mfma_f32_16x16x32_bf16 v[196:199], a[72:75], a[144:147], v[196:199]\n"
"ds_write_b64 v2, a[250:251] offset:30728\n"
"v_mfma_f32_16x16x32_bf16 a[20:23], a[72:75], a[152:155], a[20:23]\n"
"s_waitcnt vmcnt(8)\n"
"buffer_store_dwordx2 v[28:29], off, s[56:59], %24 sc1\n"
"s_add_u32 s34, s34, 8192\n"
"s_mov_b32 s71, 0\n"
"v_mfma_f32_16x16x32_bf16 a[20:23], a[76:79], a[156:159], a[20:23]\n"
"ds_write_b64 v2, a[252:253] offset:31744\n"
"v_mfma_f32_16x16x32_bf16 a[16:19], a[68:71], a[156:159], a[16:19]\n"
"s_mov_b32 s65, s78\n"
"ds_write_b64 v2, a[254:255] offset:31752\n"
"v_mfma_f32_16x16x32_bf16 a[16:19], a[64:67], a[152:155], a[16:19]\n"
"v_mfma_f32_16x16x32_bf16 v[200:203], a[80:83], a[96:99], v[200:203]\n"
"v_lshrrev_b32 v16, 4, v0\n"
"v_mfma_f32_16x16x32_bf16 v[200:203], a[84:87], a[100:103], v[200:203]\n"
"global_load_dwordx4 a[64:67], v0, s[34:35] offset:-4096 sc1\n"
"v_mfma_f32_16x16x32_bf16 v[228:231], a[92:95], a[100:103], v[228:231]\n"
"ds_read_b128 a[100:103], v2 offset:17408\n"
"v_mfma_f32_16x16x32_bf16 v[228:231], a[88:91], a[96:99], v[228:231]\n"
"ds_read_b128 a[96:99], v2 offset:16384\n"
"v_mfma_f32_16x16x32_bf16 v[232:235], a[88:91], a[104:107], v[232:235]\n"
"v_and_b32 v16, 0x30, v16\n"
"v_mfma_f32_16x16x32_bf16 v[232:235], a[92:95], a[108:111], v[232:235]\n"
"buffer_load_dwordx2 v[18:19], v16, s[44:47], s61 offen offset:128\n"
"v_mfma_f32_16x16x32_bf16 v[204:207], a[84:87], a[108:111], v[204:207]\n"
"ds_read_b128 a[108:111], v2 offset:19456\n"
"v_mfma_f32_16x16x32_bf16 v[204:207], a[80:83], a[104:107], v[204:207]\n"
"ds_read_b128 a[104:107], v2 offset:18432\n"
"v_mfma_f32_16x16x32_bf16 v[208:211], a[80:83], a[112:115], v[208:211]\n"
"v_lshrrev_b32 v20, 4, v0\n"
"v_mfma_f32_16x16x32_bf16 v[208:211], a[84:87], a[116:119], v[208:211]\n"
"global_load_dwordx4 a[68:71], v0, s[34:35] offset:-3072 sc1\n"
"v_mfma_f32_16x16x32_bf16 v[236:239], a[92:95], a[116:119], v[236:239]\n"
"ds_read_b128 a[116:119], v2 offset:21504\n"
"v_mfma_f32_16x16x32_bf16 v[236:239], a[88:91], a[112:115], v[236:239]\n"
"ds_read_b128 a[112:115], v2 offset:20480\n"
"v_mfma_f32_16x16x32_bf16 v[240:243], a[88:91], a[120:123], v[240:243]\n"
"v_and_b32 v20, 0x30, v20\n"
"v_mfma_f32_16x16x32_bf16 v[240:243], a[92:95], a[124:127], v[240:243]\n"
"buffer_load_dwordx2 v[22:23], v20, s[44:47], s61 offen offset:136\n"
"v_mfma_f32_16x16x32_bf16 v[212:215], a[84:87], a[124:127], v[212:215]\n"
"ds_read_b128 a[124:127], v2 offset:23552\n"
"v_mfma_f32_16x16x32_bf16 v[212:215], a[80:83], a[120:123], v[212:215]\n"
"ds_read_b128 a[120:123], v2 offset:22528\n"
"v_mfma_f32_16x16x32_bf16 v[216:219], a[80:83], a[128:131], v[216:219]\n"
"v_lshrrev_b32 v24, 4, v0\n"
"v_mfma_f32_16x16x32_bf16 v[216:219], a[84:87], a[132:135], v[216:219]\n"
"global_load_dwordx4 a[72:75], v0, s[34:35] offset:-2048 sc1\n"
"v_mfma_f32_16x16x32_bf16 v[244:247], a[92:95], a[132:135], v[244:247]\n"
"ds_read_b128 a[132:135], v2 offset:25600\n"
"v_and_b32 v24, 0x30, v24\n"
"v_mfma_f32_16x16x32_bf16 v[244:247], a[88:91], a[128:131], v[244:247]\n"
"ds_read_b128 a[128:131], v2 offset:24576\n"
"v_mfma_f32_16x16x32_bf16 v[248:251], a[88:91], a[136:139], v[248:251]\n"
"buffer_load_dwordx2 v[26:27], v24, s[44:47], s61 offen offset:192\n"
"v_mfma_f32_16x16x32_bf16 v[248:251], a[92:95], a[140:143], v[248:251]\n"
"v_lshrrev_b32 v28, 4, v0\n"
"v_mfma_f32_16x16x32_bf16 v[220:223], a[84:87], a[140:143], v[220:223]\n"
"ds_read_b128 a[140:143], v2 offset:27648\n"
"v_mfma_f32_16x16x32_bf16 v[220:223], a[80:83], a[136:139], v[220:223]\n"
"ds_read_b128 a[136:139], v2 offset:26624\n"
"v_mfma_f32_16x16x32_bf16 v[224:227], a[80:83], a[144:147], v[224:227]\n"
"v_and_b32 v28, 0x30, v28\n"
"v_mfma_f32_16x16x32_bf16 v[224:227], a[84:87], a[148:151], v[224:227]\n"
"global_load_dwordx4 a[76:79], v0, s[34:35] offset:-1024 sc1\n"
"v_mfma_f32_16x16x32_bf16 v[252:255], a[92:95], a[148:151], v[252:255]\n"
"ds_read_b128 a[148:151], v2 offset:29696\n"
"v_mfma_f32_16x16x32_bf16 v[252:255], a[88:91], a[144:147], v[252:255]\n"
"ds_read_b128 a[144:147], v2 offset:28672\n"
"v_mfma_f32_16x16x32_bf16 a[28:31], a[88:91], a[152:155], a[28:31]\n"
"buffer_load_dwordx2 v[30:31], v28, s[44:47], s61 offen offset:200\n"
"v_mfma_f32_16x16x32_bf16 a[28:31], a[92:95], a[156:159], a[28:31]\n"
"v_mfma_f32_16x16x32_bf16 a[24:27], a[84:87], a[156:159], a[24:27]\n"
"ds_read_b128 a[156:159], v2 offset:31744\n"
"v_mfma_f32_16x16x32_bf16 a[24:27], a[80:83], a[152:155], a[24:27]\n"
"s_waitcnt lgkmcnt(15)\n"
"ds_read_b128 a[152:155], v2 offset:30720\n"
"s_branch L_P5\n"
"L_layer:\n"
"s_waitcnt vmcnt(11) lgkmcnt(14)\n"
"v_mfma_f32_16x16x32_bf16 v[32:35], a[32:35], a[96:99], v[32:35]\n"
"s_mov_b64 s[96:97], s[52:53]\n"
"v_accvgpr_read_b32 v16, a192\n"
"v_accvgpr_read_b32 v17, a193\n"
"v_mfma_f32_16x16x32_bf16 v[32:35], a[36:39], a[100:103], v[32:35]\n"
"global_load_dwordx4 a[80:83], v0, s[34:35] offset:0 sc1\n"
"v_accvgpr_read_b32 v18, a194\n"
"v_mfma_f32_16x16x32_bf16 v[60:63], a[44:47], a[100:103], v[60:63]\n"
"s_mov_b64 s[98:99], s[54:55]\n"
"v_accvgpr_read_b32 v19, a195\n"
"v_mfma_f32_16x16x32_bf16 v[60:63], a[40:43], a[96:99], v[60:63]\n"
"v_lshlrev_b32 v12, 16, v16\n"
"v_and_b32 v13, s63, v16\n"
"v_mfma_f32_16x16x32_bf16 v[64:67], a[40:43], a[104:107], v[64:67]\n"
"s_add_u32 s52, s52, 131072\n"
"s_addc_u32 s53, s53, 0\n"
"v_lshlrev_b32 v14, 16, v17\n"
"v_mfma_f32_16x16x32_bf16 v[64:67], a[44:47], a[108:111], v[64:67]\n"
"v_and_b32 v15, s63, v17\n"
"v_mul_f32 v12, v12, v144\n"
"v_mfma_f32_16x16x32_bf16 v[36:39], a[36:39], a[108:111], v[36:39]\n"
"s_add_u32 s54, s54, 131072\n"
"s_addc_u32 s55, s55, 0\n"
"v_mul_f32 v13, v13, v145\n"
"v_mfma_f32_16x16x32_bf16 v[36:39], a[32:35], a[104:107], v[36:39]\n"
"s_waitcnt lgkmcnt(9)\n"
"v_mul_f32 v14, v14, v146\n"
"v_mul_f32 v15, v15, v147\n"
"v_mfma_f32_16x16x32_bf16 v[40:43], a[32:35], a[112:115], v[40:43]\n"
"ds_read_b128 v[20:23], v2 offset:0\n"
"v_cvt_pk_bf16_f32 v24, v12, v13\n"
"v_mfma_f32_16x16x32_bf16 v[40:43], a[36:39], a[116:119], v[40:43]\n"
"global_load_dwordx4 a[84:87], v0, s[34:35] offset:1024 sc1\n"
"v_cvt_pk_bf16_f32 v25, v14, v15\n"
"v_mfma_f32_16x16x32_bf16 v[68:71], a[44:47], a[116:119], v[68:71]\n"
"ds_write_b64 v2, v[24:25] offset:16384\n"
"v_mfma_f32_16x16x32_bf16 v[68:71], a[40:43], a[112:115], v[68:71]\n"
"v_lshlrev_b32 v12, 16, v18\n"
"v_and_b32 v13, s63, v18\n"
"v_mfma_f32_16x16x32_bf16 v[72:75], a[40:43], a[120:123], v[72:75]\n"
"s_waitcnt lgkmcnt(5)\n"
"s_barrier\n"
"v_mfma_f32_16x16x32_bf16 v[72:75], a[44:47], a[124:127], v[72:75]\n"
"v_lshlrev_b32 v14, 16, v19\n"
"v_and_b32 v15, s63, v19\n"
"v_mfma_f32_16x16x32_bf16 v[44:47], a[36:39], a[124:127], v[44:47]\n"
"v_mul_f32 v12, v12, v172\n"
"v_mul_f32 v13, v13, v173\n"
"v_mfma_f32_16x16x32_bf16 v[44:47], a[32:35], a[120:123], v[44:47]\n"
"v_mul_f32 v14, v14, v174\n"
"v_mfma_f32_16x16x32_bf16 v[48:51], a[32:35], a[128:131], v[48:51]\n"
"v_mul_f32 v15, v15, v175\n"
"v_mfma_f32_16x16x32_bf16 v[48:51], a[36:39], a[132:135], v[48:51]\n"
"global_load_dwordx4 a[88:91], v0, s[34:35] offset:2048 sc1\n"
"v_mfma_f32_16x16x32_bf16 v[76:79], a[44:47], a[132:135], v[76:79]\n"
"v_cvt_pk_bf16_f32 v26, v12, v13\n"
"v_mfma_f32_16x16x32_bf16 v[76:79], a[40:43], a[128:131], v[76:79]\n"
"v_cvt_pk_bf16_f32 v27, v14, v15\n"
"v_mfma_f32_16x16x32_bf16 v[80:83], a[40:43], a[136:139], v[80:83]\n"
"s_waitcnt lgkmcnt(1)\n"
"global_store_dwordx4 v0, v[20:23], s[96:97] offset:-4096 sc1\n"
"ds_write_b64 v2, v[26:27] offset:16392\n"
"v_accvgpr_read_b32 v16, a196\n"
"v_mfma_f32_16x16x32_bf16 v[80:83], a[44:47], a[140:143], v[80:83]\n"
"v_accvgpr_read_b32 v17, a197\n"
"v_accvgpr_read_b32 v18, a198\n"
"v_mfma_f32_16x16x32_bf16 v[52:55], a[36:39], a[140:143], v[52:55]\n"
"v_accvgpr_read_b32 v19, a199\n"
"v_lshlrev_b32 v12, 16, v16\n"
"v_mfma_f32_16x16x32_bf16 v[52:55], a[32:35], a[136:139], v[52:55]\n"
"v_and_b32 v13, s63, v16\n"
"v_lshlrev_b32 v14, 16, v17\n"
"v_mfma_f32_16x16x32_bf16 v[56:59], a[32:35], a[144:147], v[56:59]\n"
"v_and_b32 v15, s63, v17\n"
"v_mul_f32 v12, v12, v200\n"
"v_mfma_f32_16x16x32_bf16 v[56:59], a[36:39], a[148:151], v[56:59]\n"
"global_load_dwordx4 a[92:95], v0, s[34:35] offset:3072 sc1\n"
"v_mul_f32 v13, v13, v201\n"
"v_mfma_f32_16x16x32_bf16 v[84:87], a[44:47], a[148:151], v[84:87]\n"
"v_mul_f32 v14, v14, v202\n"
"v_mul_f32 v15, v15, v203\n"
"v_mfma_f32_16x16x32_bf16 v[84:87], a[40:43], a[144:147], v[84:87]\n"
"v_cvt_pk_bf16_f32 v28, v12, v13\n"
"v_cvt_pk_bf16_f32 v29, v14, v15\n"
"v_mfma_f32_16x16x32_bf16 a[4:7], a[40:43], a[152:155], a[4:7]\n"
"s_add_u32 s34, s34, 8192\n"
"ds_write_b64 v2, v[28:29] offset:17408\n"
"v_mfma_f32_16x16x32_bf16 a[4:7], a[44:47], a[156:159], a[4:7]\n"
"v_lshlrev_b32 v12, 16, v18\n"
"v_and_b32 v13, s63, v18\n"
"v_mfma_f32_16x16x32_bf16 a[0:3], a[36:39], a[156:159], a[0:3]\n"
"v_lshlrev_b32 v14, 16, v19\n"
"v_and_b32 v15, s63, v19\n"
"v_mfma_f32_16x16x32_bf16 a[0:3], a[32:35], a[152:155], a[0:3]\n"
"s_waitcnt vmcnt(11)\n"
"v_mul_f32 v12, v12, v228\n"
"v_mul_f32 v13, v13, v229\n"
"v_mfma_f32_16x16x32_bf16 v[88:91], a[48:51], a[96:99], v[88:91]\n"
"ds_read_b128 v[20:23], v2 offset:5120\n"
"v_mul_f32 v14, v14, v230\n"
"v_mfma_f32_16x16x32_bf16 v[88:91], a[52:55], a[100:103], v[88:91]\n"
"global_load_dwordx4 a[32:35], v0, s[34:35] offset:-4096 sc1\n"
"v_mul_f32 v15, v15, v231\n"
"v_mfma_f32_16x16x32_bf16 v[116:119], a[60:63], a[100:103], v[116:119]\n"
"ds_read_b128 a[100:103], v3 offset:1024\n"
"v_cvt_pk_bf16_f32 v30, v12, v13\n"
"v_mfma_f32_16x16x32_bf16 v[116:119], a[56:59], a[96:99], v[116:119]\n"
"ds_read_b128 a[96:99], v3 offset:0\n"
"v_cvt_pk_bf16_f32 v31, v14, v15\n"
"v_mfma_f32_16x16x32_bf16 v[120:123], a[56:59], a[104:107], v[120:123]\n"
"ds_write_b64 v2, v[30:31] offset:17416\n"
"v_mfma_f32_16x16x32_bf16 v[120:123], a[60:63], a[108:111], v[120:123]\n"
"v_accvgpr_read_b32 v16, a200\n"
"v_accvgpr_read_b32 v17, a201\n"
"v_mfma_f32_16x16x32_bf16 v[92:95], a[52:55], a[108:111], v[92:95]\n"
"ds_read_b128 a[108:111], v3 offset:3072\n"
"v_accvgpr_read_b32 v18, a202\n"
"v_mfma_f32_16x16x32_bf16 v[92:95], a[48:51], a[104:107], v[92:95]\n"
"ds_read_b128 a[104:107], v3 offset:2048\n"
"v_accvgpr_read_b32 v19, a203\n"
"v_mfma_f32_16x16x32_bf16 v[96:99], a[48:51], a[112:115], v[96:99]\n"
"v_lshlrev_b32 v12, 16, v16\n"
"v_and_b32 v13, s63, v16\n"
"v_mfma_f32_16x16x32_bf16 v[96:99], a[52:55], a[116:119], v[96:99]\n"
"global_load_dwordx4 a[36:39], v0, s[34:35] offset:-3072 sc1\n"
"v_lshlrev_b32 v14, 16, v17\n"
"v_mfma_f32_16x16x32_bf16 v[124:127], a[60:63], a[116:119], v[124:127]\n"
"ds_read_b128 a[116:119], v3 offset:5120\n"
"v_and_b32 v15, s63, v17\n"
"v_mfma_f32_16x16x32_bf16 v[124:127], a[56:59], a[112:115], v[124:127]\n"
"ds_read_b128 a[112:115], v3 offset:4096\n"
"v_mfma_f32_16x16x32_bf16 v[128:131], a[56:59], a[120:123], v[128:131]\n"
"s_waitcnt lgkmcnt(7)\n"
"global_store_dwordx4 v0, v[20:23], s[96:97] offset:1024 sc1\n"
"v_mul_f32 v12, v12, v148\n"
"v_mul_f32 v13, v13, v149\n"
"v_mul_f32 v14, v14, v150\n"
"v_mul_f32 v15, v15, v151\n"
"v_mfma_f32_16x16x32_bf16 v[128:131], a[60:63], a[124:127], v[128:131]\n"
"v_cvt_pk_bf16_f32 v24, v12, v13\n"
"v_mfma_f32_16x16x32_bf16 v[100:103], a[52:55], a[124:127], v[100:103]\n"
"ds_read_b128 a[124:127], v3 offset:7168\n"
"v_cvt_pk_bf16_f32 v25, v14, v15\n"
"v_mfma_f32_16x16x32_bf16 v[100:103], a[48:51], a[120:123], v[100:103]\n"
"ds_read_b128 a[120:123], v3 offset:6144\n"
"v_mfma_f32_16x16x32_bf16 v[104:107], a[48:51], a[128:131], v[104:107]\n"
"ds_write_b64 v2, v[24:25] offset:18432\n"
"v_mfma_f32_16x16x32_bf16 v[104:107], a[52:55], a[132:135], v[104:107]\n"
"global_load_dwordx4 a[40:43], v0, s[34:35] offset:-2048 sc1\n"
"v_lshlrev_b32 v12, 16, v18\n"
"v_mfma_f32_16x16x32_bf16 v[132:135], a[60:63], a[132:135], v[132:135]\n"
"ds_read_b128 a[132:135], v3 offset:9216\n"
"v_and_b32 v13, s63, v18\n"
"v_mfma_f32_16x16x32_bf16 v[132:135], a[56:59], a[128:131], v[132:135]\n"
"ds_read_b128 a[128:131], v3 offset:8192\n"
"v_lshlrev_b32 v14, 16, v19\n"
"v_mfma_f32_16x16x32_bf16 v[136:139], a[56:59], a[136:139], v[136:139]\n"
"v_and_b32 v15, s63, v19\n"
"v_mul_f32 v12, v12, v176\n"
"v_mfma_f32_16x16x32_bf16 v[136:139], a[60:63], a[140:143], v[136:139]\n"
"v_mul_f32 v13, v13, v177\n"
"v_mul_f32 v14, v14, v178\n"
"v_mfma_f32_16x16x32_bf16 v[108:111], a[52:55], a[140:143], v[108:111]\n"
"ds_read_b128 a[140:143], v3 offset:11264\n"
"v_mul_f32 v15, v15, v179\n"
"v_mfma_f32_16x16x32_bf16 v[108:111], a[48:51], a[136:139], v[108:111]\n"
"ds_read_b128 a[136:139], v3 offset:10240\n"
"v_cvt_pk_bf16_f32 v26, v12, v13\n"
"v_mfma_f32_16x16x32_bf16 v[112:115], a[48:51], a[144:147], v[112:115]\n"
"ds_read_b128 v[20:23], v2 offset:10240\n"
"v_cvt_pk_bf16_f32 v27, v14, v15\n"
"v_mfma_f32_16x16x32_bf16 v[112:115], a[52:55], a[148:151], v[112:115]\n"
"global_load_dwordx4 a[44:47], v0, s[34:35] offset:-1024 sc1\n"
"ds_write_b64 v2, v[26:27] offset:18440\n"
"v_mfma_f32_16x16x32_bf16 v[140:143], a[60:63], a[148:151], v[140:143]\n"
"ds_read_b128 a[148:151], v3 offset:13312\n"
"v_accvgpr_read_b32 v16, a204\n"
"v_mfma_f32_16x16x32_bf16 v[140:143], a[56:59], a[144:147], v[140:143]\n"
"ds_read_b128 a[144:147], v3 offset:12288\n"
"v_accvgpr_read_b32 v17, a205\n"
"v_mfma_f32_16x16x32_bf16 a[12:15], a[56:59], a[152:155], a[12:15]\n"
"v_accvgpr_read_b32 v18, a206\n"
"v_accvgpr_read_b32 v19, a207\n"
"v_mfma_f32_16x16x32_bf16 a[12:15], a[60:63], a[156:159], a[12:15]\n"
"v_lshlrev_b32 v12, 16, v16\n"
"v_and_b32 v13, s63, v16\n"
"v_mfma_f32_16x16x32_bf16 a[8:11], a[52:55], a[156:159], a[8:11]\n"
"ds_read_b128 a[156:159], v3 offset:15360\n"
"v_lshlrev_b32 v14, 16, v17\n"
"v_mfma_f32_16x16x32_bf16 a[8:11], a[48:51], a[152:155], a[8:11]\n"
"s_waitcnt vmcnt(11) lgkmcnt(14)\n"
"ds_read_b128 a[152:155], v3 offset:14336\n"
"v_and_b32 v15, s63, v17\n"
"v_mfma_f32_16x16x32_bf16 v[32:35], a[64:67], a[96:99], v[32:35]\n"
"v_mul_f32 v12, v12, v204\n"
"v_mul_f32 v13, v13, v205\n"
"v_mfma_f32_16x16x32_bf16 v[32:35], a[68:71], a[100:103], v[32:35]\n"
"global_load_dwordx4 a[48:51], v0, s[34:35] offset:0 sc1\n"
"v_mul_f32 v14, v14, v206\n"
"v_mfma_f32_16x16x32_bf16 v[60:63], a[76:79], a[100:103], v[60:63]\n"
"v_mul_f32 v15, v15, v207\n"
"v_cvt_pk_bf16_f32 v28, v12, v13\n"
"v_mfma_f32_16x16x32_bf16 v[60:63], a[72:75], a[96:99], v[60:63]\n"
"v_cvt_pk_bf16_f32 v29, v14, v15\n"
"v_mfma_f32_16x16x32_bf16 v[64:67], a[72:75], a[104:107], v[64:67]\n"
"s_waitcnt lgkmcnt(5)\n"
"global_store_dwordx4 v0, v[20:23], s[98:99] offset:-2048 sc1\n"
"ds_write_b64 v2, v[28:29] offset:19456\n"
"v_lshlrev_b32 v12, 16, v18\n"
"v_mfma_f32_16x16x32_bf16 v[64:67], a[76:79], a[108:111], v[64:67]\n"
"v_and_b32 v13, s63, v18\n"
"v_lshlrev_b32 v14, 16, v19\n"
"v_mfma_f32_16x16x32_bf16 v[36:39], a[68:71], a[108:111], v[36:39]\n"
"v_and_b32 v15, s63, v19\n"
"v_mul_f32 v12, v12, v232\n"
"v_mfma_f32_16x16x32_bf16 v[36:39], a[64:67], a[104:107], v[36:39]\n"
"v_mul_f32 v13, v13, v233\n"
"v_mul_f32 v14, v14, v234\n"
"v_mfma_f32_16x16x32_bf16 v[40:43], a[64:67], a[112:115], v[40:43]\n"
"v_mul_f32 v15, v15, v235\n"
"v_cvt_pk_bf16_f32 v30, v12, v13\n"
"v_mfma_f32_16x16x32_bf16 v[40:43], a[68:71], a[116:119], v[40:43]\n"
"global_load_dwordx4 a[52:55], v0, s[34:35] offset:1024 sc1\n"
"v_cvt_pk_bf16_f32 v31, v14, v15\n"
"v_mfma_f32_16x16x32_bf16 v[68:71], a[76:79], a[116:119], v[68:71]\n"
"ds_write_b64 v2, v[30:31] offset:19464\n"
"v_mfma_f32_16x16x32_bf16 v[68:71], a[72:75], a[112:115], v[68:71]\n"
"v_accvgpr_read_b32 v16, a208\n"
"v_accvgpr_read_b32 v17, a209\n"
"v_mfma_f32_16x16x32_bf16 v[72:75], a[72:75], a[120:123], v[72:75]\n"
"v_accvgpr_read_b32 v18, a210\n"
"v_accvgpr_read_b32 v19, a211\n"
"v_mfma_f32_16x16x32_bf16 v[72:75], a[76:79], a[124:127], v[72:75]\n"
"v_lshlrev_b32 v12, 16, v16\n"
"v_mfma_f32_16x16x32_bf16 v[44:47], a[68:71], a[124:127], v[44:47]\n"
"v_and_b32 v13, s63, v16\n"
"v_mfma_f32_16x16x32_bf16 v[44:47], a[64:67], a[120:123], v[44:47]\n"
"v_lshlrev_b32 v14, 16, v17\n"
"v_and_b32 v15, s63, v17\n"
"v_mfma_f32_16x16x32_bf16 v[48:51], a[64:67], a[128:131], v[48:51]\n"
"v_mul_f32 v12, v12, v152\n"
"v_mfma_f32_16x16x32_bf16 v[48:51], a[68:71], a[132:135], v[48:51]\n"
"global_load_dwordx4 a[56:59], v0, s[34:35] offset:2048 sc1\n"
"v_mul_f32 v13, v13, v153\n"
"v_mfma_f32_16x16x32_bf16 v[76:79], a[76:79], a[132:135], v[76:79]\n"
"v_mul_f32 v14, v14, v154\n"
"v_mul_f32 v15, v15, v155\n"
"v_mfma_f32_16x16x32_bf16 v[76:79], a[72:75], a[128:131], v[76:79]\n"
"v_cvt_pk_bf16_f32 v24, v12, v13\n"
"v_cvt_pk_bf16_f32 v25, v14, v15\n"
"v_mfma_f32_16x16x32_bf16 v[80:83], a[72:75], a[136:139], v[80:83]\n"
"ds_write_b64 v2, v[24:25] offset:20480\n"
"v_mfma_f32_16x16x32_bf16 v[80:83], a[76:79], a[140:143], v[80:83]\n"
"v_lshlrev_b32 v12, 16, v18\n"
"v_mfma_f32_16x16x32_bf16 v[52:55], a[68:71], a[140:143], v[52:55]\n"
"v_and_b32 v13, s63, v18\n"
"v_lshlrev_b32 v14, 16, v19\n"
"v_mfma_f32_16x16x32_bf16 v[52:55], a[64:67], a[136:139], v[52:55]\n"
"s_waitcnt lgkmcnt(3)\n"
"v_and_b32 v15, s63, v19\n"
"v_mul_f32 v12, v12, v180\n"
"v_mfma_f32_16x16x32_bf16 v[56:59], a[64:67], a[144:147], v[56:59]\n"
"v_mul_f32 v13, v13, v181\n"
"v_mul_f32 v14, v14, v182\n"
"v_mfma_f32_16x16x32_bf16 v[56:59], a[68:71], a[148:151], v[56:59]\n"
"global_load_dwordx4 a[60:63], v0, s[34:35] offset:3072 sc1\n"
"v_mul_f32 v15, v15, v183\n"
"v_mfma_f32_16x16x32_bf16 v[84:87], a[76:79], a[148:151], v[84:87]\n"
"v_cvt_pk_bf16_f32 v26, v12, v13\n"
"v_cvt_pk_bf16_f32 v27, v14, v15\n"
"v_mfma_f32_16x16x32_bf16 v[84:87], a[72:75], a[144:147], v[84:87]\n"
"ds_write_b64 v2, v[26:27] offset:20488\n"
"v_mfma_f32_16x16x32_bf16 a[4:7], a[72:75], a[152:155], a[4:7]\n"
"s_add_u32 s34, s34, 8192\n"
"v_accvgpr_read_b32 v16, a212\n"
"v_mfma_f32_16x16x32_bf16 a[4:7], a[76:79], a[156:159], a[4:7]\n"
"v_accvgpr_read_b32 v17, a213\n"
"v_accvgpr_read_b32 v18, a214\n"
"v_mfma_f32_16x16x32_bf16 a[0:3], a[68:71], a[156:159], a[0:3]\n"
"v_accvgpr_read_b32 v19, a215\n"
"v_lshlrev_b32 v12, 16, v16\n"
"v_mfma_f32_16x16x32_bf16 a[0:3], a[64:67], a[152:155], a[0:3]\n"
"s_waitcnt vmcnt(10)\n"
"v_and_b32 v13, s63, v16\n"
"v_lshlrev_b32 v14, 16, v17\n"
"v_mfma_f32_16x16x32_bf16 v[88:91], a[80:83], a[96:99], v[88:91]\n"
"v_and_b32 v15, s63, v17\n"
"v_mul_f32 v12, v12, v208\n"
"v_mfma_f32_16x16x32_bf16 v[88:91], a[84:87], a[100:103], v[88:91]\n"
"global_load_dwordx4 a[64:67], v0, s[34:35] offset:-4096 sc1\n"
"v_mul_f32 v13, v13, v209\n"
"v_mfma_f32_16x16x32_bf16 v[116:119], a[92:95], a[100:103], v[116:119]\n"
"ds_read_b128 a[100:103], v4 offset:1024\n"
"v_mul_f32 v14, v14, v210\n"
"v_mfma_f32_16x16x32_bf16 v[116:119], a[88:91], a[96:99], v[116:119]\n"
"ds_read_b128 a[96:99], v4 offset:0\n"
"v_mul_f32 v15, v15, v211\n"
"v_mfma_f32_16x16x32_bf16 v[120:123], a[88:91], a[104:107], v[120:123]\n"
"v_cvt_pk_bf16_f32 v28, v12, v13\n"
"v_cvt_pk_bf16_f32 v29, v14, v15\n"
"v_mfma_f32_16x16x32_bf16 v[120:123], a[92:95], a[108:111], v[120:123]\n"
"ds_write_b64 v2, v[28:29] offset:21504\n"
"v_mfma_f32_16x16x32_bf16 v[92:95], a[84:87], a[108:111], v[92:95]\n"
"ds_read_b128 a[108:111], v4 offset:3072\n"
"v_mfma_f32_16x16x32_bf16 v[92:95], a[80:83], a[104:107], v[92:95]\n"
"ds_read_b128 a[104:107], v4 offset:2048\n"
"v_mfma_f32_16x16x32_bf16 v[96:99], a[80:83], a[112:115], v[96:99]\n"
"v_lshlrev_b32 v12, 16, v18\n"
"v_and_b32 v13, s63, v18\n"
"v_mfma_f32_16x16x32_bf16 v[96:99], a[84:87], a[116:119], v[96:99]\n"
"global_load_dwordx4 a[68:71], v0, s[34:35] offset:-3072 sc1\n"
"v_lshlrev_b32 v14, 16, v19\n"
"v_mfma_f32_16x16x32_bf16 v[124:127], a[92:95], a[116:119], v[124:127]\n"
"ds_read_b128 a[116:119], v4 offset:5120\n"
"v_and_b32 v15, s63, v19\n"
"v_mfma_f32_16x16x32_bf16 v[124:127], a[88:91], a[112:115], v[124:127]\n"
"ds_read_b128 a[112:115], v4 offset:4096\n"
"v_mul_f32 v12, v12, v236\n"
"v_mfma_f32_16x16x32_bf16 v[128:131], a[88:91], a[120:123], v[128:131]\n"
"v_mul_f32 v13, v13, v237\n"
"v_mul_f32 v14, v14, v238\n"
"v_mfma_f32_16x16x32_bf16 v[128:131], a[92:95], a[124:127], v[128:131]\n"
"v_mul_f32 v15, v15, v239\n"
"v_cvt_pk_bf16_f32 v30, v12, v13\n"
"v_mfma_f32_16x16x32_bf16 v[100:103], a[84:87], a[124:127], v[100:103]\n"
"ds_read_b128 a[124:127], v4 offset:7168\n"
"v_cvt_pk_bf16_f32 v31, v14, v15\n"
"v_mfma_f32_16x16x32_bf16 v[100:103], a[80:83], a[120:123], v[100:103]\n"
"ds_read_b128 a[120:123], v4 offset:6144\n"
"v_mfma_f32_16x16x32_bf16 v[104:107], a[80:83], a[128:131], v[104:107]\n"
"ds_write_b64 v2, v[30:31] offset:21512\n"
"v_mfma_f32_16x16x32_bf16 v[104:107], a[84:87], a[132:135], v[104:107]\n"
"global_load_dwordx4 a[72:75], v0, s[34:35] offset:-2048 sc1\n"
"v_accvgpr_read_b32 v16, a216\n"
"v_mfma_f32_16x16x32_bf16 v[132:135], a[92:95], a[132:135], v[132:135]\n"
"ds_read_b128 a[132:135], v4 offset:9216\n"
"v_accvgpr_read_b32 v17, a217\n"
"v_mfma_f32_16x16x32_bf16 v[132:135], a[88:91], a[128:131], v[132:135]\n"
"ds_read_b128 a[128:131], v4 offset:8192\n"
"v_accvgpr_read_b32 v18, a218\n"
"v_mfma_f32_16x16x32_bf16 v[136:139], a[88:91], a[136:139], v[136:139]\n"
"v_accvgpr_read_b32 v19, a219\n"
"v_lshlrev_b32 v12, 16, v16\n"
"v_mfma_f32_16x16x32_bf16 v[136:139], a[92:95], a[140:143], v[136:139]\n"
"v_and_b32 v13, s63, v16\n"
"v_lshlrev_b32 v14, 16, v17\n"
"v_mfma_f32_16x16x32_bf16 v[108:111], a[84:87], a[140:143], v[108:111]\n"
"ds_read_b128 a[140:143], v4 offset:11264\n"
"v_and_b32 v15, s63, v17\n"
"v_mfma_f32_16x16x32_bf16 v[108:111], a[80:83], a[136:139], v[108:111]\n"
"ds_read_b128 a[136:139], v4 offset:10240\n"
"v_mul_f32 v12, v12, v156\n"
"v_mfma_f32_16x16x32_bf16 v[112:115], a[80:83], a[144:147], v[112:115]\n"
"v_mul_f32 v13, v13, v157\n"
"v_mul_f32 v14, v14, v158\n"
"v_mfma_f32_16x16x32_bf16 v[112:115], a[84:87], a[148:151], v[112:115]\n"
"global_load_dwordx4 a[76:79], v0, s[34:35] offset:-1024 sc1\n"
"v_mul_f32 v15, v15, v159\n"
"v_mfma_f32_16x16x32_bf16 v[140:143], a[92:95], a[148:151], v[140:143]\n"
"ds_read_b128 a[148:151], v4 offset:13312\n"
"v_cvt_pk_bf16_f32 v24, v12, v13\n"
"v_mfma_f32_16x16x32_bf16 v[140:143], a[88:91], a[144:147], v[140:143]\n"
"ds_read_b128 a[144:147], v4 offset:12288\n"
"v_cvt_pk_bf16_f32 v25, v14, v15\n"
"v_mfma_f32_16x16x32_bf16 a[12:15], a[88:91], a[152:155], a[12:15]\n"
"ds_write_b64 v2, v[24:25] offset:22528\n"
"v_mfma_f32_16x16x32_bf16 a[12:15], a[92:95], a[156:159], a[12:15]\n"
"v_lshlrev_b32 v12, 16, v18\n"
"v_and_b32 v13, s63, v18\n"
"v_mfma_f32_16x16x32_bf16 a[8:11], a[84:87], a[156:159], a[8:11]\n"
"ds_read_b128 a[156:159], v4 offset:15360\n"
"v_lshlrev_b32 v14, 16, v19\n"
"v_mfma_f32_16x16x32_bf16 a[8:11], a[80:83], a[152:155], a[8:11]\n"
"s_waitcnt vmcnt(9) lgkmcnt(13)\n"
"ds_read_b128 a[152:155], v4 offset:14336\n"
"v_and_b32 v15, s63, v19\n"
"v_mfma_f32_16x16x32_bf16 v[32:35], a[32:35], a[96:99], v[32:35]\n"
"v_mul_f32 v12, v12, v184\n"
"v_mul_f32 v13, v13, v185\n"
"v_mfma_f32_16x16x32_bf16 v[32:35], a[36:39], a[100:103], v[32:35]\n"
"global_load_dwordx4 a[80:83], v0, s[34:35] offset:0 sc1\n"
"v_mul_f32 v14, v14, v186\n"
"v_mfma_f32_16x16x32_bf16 v[60:63], a[44:47], a[100:103], v[60:63]\n"
"s_add_u32 s65, s65, 1\n"
"v_mul_f32 v15, v15, v187\n"
"v_cvt_pk_bf16_f32 v26, v12, v13\n"
"v_mfma_f32_16x16x32_bf16 v[60:63], a[40:43], a[96:99], v[60:63]\n"
"v_cvt_pk_bf16_f32 v27, v14, v15\n"
"v_mfma_f32_16x16x32_bf16 v[64:67], a[40:43], a[104:107], v[64:67]\n"
"ds_write_b64 v2, v[26:27] offset:22536\n"
"v_mfma_f32_16x16x32_bf16 v[64:67], a[44:47], a[108:111], v[64:67]\n"
"v_accvgpr_read_b32 v16, a220\n"
"v_accvgpr_read_b32 v17, a221\n"
"v_mfma_f32_16x16x32_bf16 v[36:39], a[36:39], a[108:111], v[36:39]\n"
"v_accvgpr_read_b32 v18, a222\n"
"v_accvgpr_read_b32 v19, a223\n"
"v_mfma_f32_16x16x32_bf16 v[36:39], a[32:35], a[104:107], v[36:39]\n"
"s_waitcnt lgkmcnt(11)\n"
"v_lshlrev_b32 v12, 16, v16\n"
"v_and_b32 v13, s63, v16\n"
"v_mfma_f32_16x16x32_bf16 v[40:43], a[32:35], a[112:115], v[40:43]\n"
"v_lshlrev_b32 v14, 16, v17\n"
"v_and_b32 v15, s63, v17\n"
"v_mfma_f32_16x16x32_bf16 v[40:43], a[36:39], a[116:119], v[40:43]\n"
"global_load_dwordx4 a[84:87], v0, s[34:35] offset:1024 sc1\n"
"v_mul_f32 v12, v12, v212\n"
"v_mfma_f32_16x16x32_bf16 v[68:71], a[44:47], a[116:119], v[68:71]\n"
"v_mul_f32 v13, v13, v213\n"
"v_mul_f32 v14, v14, v214\n"
"v_mfma_f32_16x16x32_bf16 v[68:71], a[40:43], a[112:115], v[68:71]\n"
"v_mul_f32 v15, v15, v215\n"
"v_cvt_pk_bf16_f32 v28, v12, v13\n"
"v_mfma_f32_16x16x32_bf16 v[72:75], a[40:43], a[120:123], v[72:75]\n"
"v_cvt_pk_bf16_f32 v29, v14, v15\n"
"v_mfma_f32_16x16x32_bf16 v[72:75], a[44:47], a[124:127], v[72:75]\n"
"ds_write_b64 v2, v[28:29] offset:23552\n"
"v_mfma_f32_16x16x32_bf16 v[44:47], a[36:39], a[124:127], v[44:47]\n"
"v_lshlrev_b32 v12, 16, v18\n"
"v_and_b32 v13, s63, v18\n"
"v_mfma_f32_16x16x32_bf16 v[44:47], a[32:35], a[120:123], v[44:47]\n"
"s_waitcnt lgkmcnt(7)\n"
"v_lshlrev_b32 v14, 16, v19\n"
"v_and_b32 v15, s63, v19\n"
"v_mfma_f32_16x16x32_bf16 v[48:51], a[32:35], a[128:131], v[48:51]\n"
"v_mul_f32 v12, v12, v240\n"
"v_mul_f32 v13, v13, v241\n"
"v_mfma_f32_16x16x32_bf16 v[48:51], a[36:39], a[132:135], v[48:51]\n"
"global_load_dwordx4 a[88:91], v0, s[34:35] offset:2048 sc1\n"
"v_mul_f32 v14, v14, v242\n"
"v_mfma_f32_16x16x32_bf16 v[76:79], a[44:47], a[132:135], v[76:79]\n"
"v_mul_f32 v15, v15, v243\n"
"v_mfma_f32_16x16x32_bf16 v[76:79], a[40:43], a[128:131], v[76:79]\n"
"v_cvt_pk_bf16_f32 v30, v12, v13\n"
"v_cvt_pk_bf16_f32 v31, v14, v15\n"
"v_mfma_f32_16x16x32_bf16 v[80:83], a[40:43], a[136:139], v[80:83]\n"
"ds_write_b64 v2, v[30:31] offset:23560\n"
"v_mfma_f32_16x16x32_bf16 v[80:83], a[44:47], a[140:143], v[80:83]\n"
"v_accvgpr_read_b32 v16, a224\n"
"v_accvgpr_read_b32 v17, a225\n"
"v_mfma_f32_16x16x32_bf16 v[52:55], a[36:39], a[140:143], v[52:55]\n"
"v_accvgpr_read_b32 v18, a226\n"
"v_mfma_f32_16x16x32_bf16 v[52:55], a[32:35], a[136:139], v[52:55]\n"
"s_waitcnt lgkmcnt(3)\n"
"v_accvgpr_read_b32 v19, a227\n"
"v_mfma_f32_16x16x32_bf16 v[56:59], a[32:35], a[144:147], v[56:59]\n"
"v_lshlrev_b32 v12, 16, v16\n"
"v_and_b32 v13, s63, v16\n"
"v_mfma_f32_16x16x32_bf16 v[56:59], a[36:39], a[148:151], v[56:59]\n"
"global_load_dwordx4 a[92:95], v0, s[34:35] offset:3072 sc1\n"
"v_lshlrev_b32 v14, 16, v17\n"
"v_mfma_f32_16x16x32_bf16 v[84:87], a[44:47], a[148:151], v[84:87]\n"
"v_and_b32 v15, s63, v17\n"
"v_mul_f32 v12, v12, v160\n"
"v_mfma_f32_16x16x32_bf16 v[84:87], a[40:43], a[144:147], v[84:87]\n"
"v_mul_f32 v13, v13, v161\n"
"v_mfma_f32_16x16x32_bf16 a[4:7], a[40:43], a[152:155], a[4:7]\n"
"s_add_u32 s34, s34, 8192\n"
"v_mul_f32 v14, v14, v162\n"
"v_mfma_f32_16x16x32_bf16 a[4:7], a[44:47], a[156:159], a[4:7]\n"
"v_mul_f32 v15, v15, v163\n"
"v_cvt_pk_bf16_f32 v24, v12, v13\n"
"v_mfma_f32_16x16x32_bf16 a[0:3], a[36:39], a[156:159], a[0:3]\n"
"v_cvt_pk_bf16_f32 v25, v14, v15\n"
"v_mfma_f32_16x16x32_bf16 a[0:3], a[32:35], a[152:155], a[0:3]\n"
"s_waitcnt vmcnt(8)\n"
"ds_write_b64 v2, v[24:25] offset:24576\n"
"v_mfma_f32_16x16x32_bf16 v[88:91], a[48:51], a[96:99], v[88:91]\n"
"v_lshlrev_b32 v12, 16, v18\n"
"v_and_b32 v13, s63, v18\n"
"v_mfma_f32_16x16x32_bf16 v[88:91], a[52:55], a[100:103], v[88:91]\n"
"global_load_dwordx4 a[32:35], v0, s[34:35] offset:-4096 sc1\n"
"v_lshlrev_b32 v14, 16, v19\n"
"v_mfma_f32_16x16x32_bf16 v[116:119], a[60:63], a[100:103], v[116:119]\n"
"ds_read_b128 a[100:103], v5 offset:1024\n"
"v_and_b32 v15, s63, v19\n"
"v_mfma_f32_16x16x32_bf16 v[116:119], a[56:59], a[96:99], v[116:119]\n"
"ds_read_b128 a[96:99], v5 offset:0\n"
"v_mul_f32 v12, v12, v188\n"
"v_mfma_f32_16x16x32_bf16 v[120:123], a[56:59], a[104:107], v[120:123]\n"
"v_mul_f32 v13, v13, v189\n"
"v_mul_f32 v14, v14, v190\n"
"v_mfma_f32_16x16x32_bf16 v[120:123], a[60:63], a[108:111], v[120:123]\n"
"v_mul_f32 v15, v15, v191\n"
"v_cvt_pk_bf16_f32 v26, v12, v13\n"
"v_mfma_f32_16x16x32_bf16 v[92:95], a[52:55], a[108:111], v[92:95]\n"
"ds_read_b128 a[108:111], v5 offset:3072\n"
"v_cvt_pk_bf16_f32 v27, v14, v15\n"
"v_mfma_f32_16x16x32_bf16 v[92:95], a[48:51], a[104:107], v[92:95]\n"
"global_store_dwordx4 v0, v[24:27], s[54:55] offset:-4096 sc1\n"
"ds_read_b128 a[104:107], v5 offset:2048\n"
"ds_write_b64 v2, v[26:27] offset:24584\n"
"v_mfma_f32_16x16x32_bf16 v[96:99], a[48:51], a[112:115], v[96:99]\n"
"v_accvgpr_read_b32 v16, a228\n"
"v_accvgpr_read_b32 v17, a229\n"
"v_mfma_f32_16x16x32_bf16 v[96:99], a[52:55], a[116:119], v[96:99]\n"
"global_load_dwordx4 a[36:39], v0, s[34:35] offset:-3072 sc1\n"
"v_accvgpr_read_b32 v18, a230\n"
"v_mfma_f32_16x16x32_bf16 v[124:127], a[60:63], a[116:119], v[124:127]\n"
"ds_read_b128 a[116:119], v5 offset:5120\n"
"v_accvgpr_read_b32 v19, a231\n"
"v_mfma_f32_16x16x32_bf16 v[124:127], a[56:59], a[112:115], v[124:127]\n"
"ds_read_b128 a[112:115], v5 offset:4096\n"
"v_lshlrev_b32 v12, 16, v16\n"
"v_mfma_f32_16x16x32_bf16 v[128:131], a[56:59], a[120:123], v[128:131]\n"
"v_and_b32 v13, s63, v16\n"
"v_lshlrev_b32 v14, 16, v17\n"
"v_mfma_f32_16x16x32_bf16 v[128:131], a[60:63], a[124:127], v[128:131]\n"
"v_and_b32 v15, s63, v17\n"
"v_mfma_f32_16x16x32_bf16 v[100:103], a[52:55], a[124:127], v[100:103]\n"
"ds_read_b128 a[124:127], v5 offset:7168\n"
"v_mul_f32 v12, v12, v216\n"
"v_mfma_f32_16x16x32_bf16 v[100:103], a[48:51], a[120:123], v[100:103]\n"
"ds_read_b128 a[120:123], v5 offset:6144\n"
"v_mul_f32 v13, v13, v217\n"
"v_mfma_f32_16x16x32_bf16 v[104:107], a[48:51], a[128:131], v[104:107]\n"
"v_mul_f32 v14, v14, v218\n"
"v_mul_f32 v15, v15, v219\n"
"v_mfma_f32_16x16x32_bf16 v[104:107], a[52:55], a[132:135], v[104:107]\n"
"global_load_dwordx4 a[40:43], v0, s[34:35] offset:-2048 sc1\n"
"v_cvt_pk_bf16_f32 v28, v12, v13\n"
"v_mfma_f32_16x16x32_bf16 v[132:135], a[60:63], a[132:135], v[132:135]\n"
"ds_read_b128 a[132:135], v5 offset:9216\n"
"v_cvt_pk_bf16_f32 v29, v14, v15\n"
"v_mfma_f32_16x16x32_bf16 v[132:135], a[56:59], a[128:131], v[132:135]\n"
"ds_read_b128 a[128:131], v5 offset:8192\n"
"v_mfma_f32_16x16x32_bf16 v[136:139], a[56:59], a[136:139], v[136:139]\n"
"ds_write_b64 v2, v[28:29] offset:25600\n"
"v_mfma_f32_16x16x32_bf16 v[136:139], a[60:63], a[140:143], v[136:139]\n"
"v_lshlrev_b32 v12, 16, v18\n"
"v_and_b32 v13, s63, v18\n"
"v_mfma_f32_16x16x32_bf16 v[108:111], a[52:55], a[140:143], v[108:111]\n"
"ds_read_b128 a[140:143], v5 offset:11264\n"
"v_lshlrev_b32 v14, 16, v19\n"
"v_mfma_f32_16x16x32_bf16 v[108:111], a[48:51], a[136:139], v[108:111]\n"
"ds_read_b128 a[136:139], v5 offset:10240\n"
"v_and_b32 v15, s63, v19\n"
"v_mfma_f32_16x16x32_bf16 v[112:115], a[48:51], a[144:147], v[112:115]\n"
"v_mul_f32 v12, v12, v244\n"
"v_mul_f32 v13, v13, v245\n"
"v_mfma_f32_16x16x32_bf16 v[112:115], a[52:55], a[148:151], v[112:115]\n"
"global_load_dwordx4 a[44:47], v0, s[34:35] offset:-1024 sc1\n"
"v_mul_f32 v14, v14, v246\n"
"v_mfma_f32_16x16x32_bf16 v[140:143], a[60:63], a[148:151], v[140:143]\n"
"ds_read_b128 a[148:151], v5 offset:13312\n"
"v_mul_f32 v15, v15, v247\n"
"v_mfma_f32_16x16x32_bf16 v[140:143], a[56:59], a[144:147], v[140:143]\n"
"ds_read_b128 a[144:147], v5 offset:12288\n"
"v_cvt_pk_bf16_f32 v30, v12, v13\n"
"v_mfma_f32_16x16x32_bf16 a[12:15], a[56:59], a[152:155], a[12:15]\n"
"v_cvt_pk_bf16_f32 v31, v14, v15\n"
"v_mfma_f32_16x16x32_bf16 a[12:15], a[60:63], a[156:159], a[12:15]\n"
"global_store_dwordx4 v0, v[28:31], s[54:55] offset:-3072 sc1\n"
"ds_write_b64 v2, v[30:31] offset:25608\n"
"v_accvgpr_read_b32 v16, a232\n"
"v_mfma_f32_16x16x32_bf16 a[8:11], a[52:55], a[156:159], a[8:11]\n"
"ds_read_b128 a[156:159], v5 offset:15360\n"
"v_accvgpr_read_b32 v17, a233\n"
"v_mfma_f32_16x16x32_bf16 a[8:11], a[48:51], a[152:155], a[8:11]\n"
"s_waitcnt vmcnt(10) lgkmcnt(14)\n"
"ds_read_b128 a[152:155], v5 offset:14336\n"
"v_accvgpr_read_b32 v18, a234\n"
"v_mfma_f32_16x16x32_bf16 v[32:35], a[64:67], a[96:99], v[32:35]\n"
"v_accvgpr_read_b32 v19, a235\n"
"v_lshlrev_b32 v12, 16, v16\n"
"v_mfma_f32_16x16x32_bf16 v[32:35], a[68:71], a[100:103], v[32:35]\n"
"global_load_dwordx4 a[48:51], v0, s[34:35] offset:0 sc1\n"
"v_and_b32 v13, s63, v16\n"
"v_mfma_f32_16x16x32_bf16 v[60:63], a[76:79], a[100:103], v[60:63]\n"
"v_lshlrev_b32 v14, 16, v17\n"
"v_and_b32 v15, s63, v17\n"
"v_mfma_f32_16x16x32_bf16 v[60:63], a[72:75], a[96:99], v[60:63]\n"
"v_mul_f32 v12, v12, v164\n"
"v_mul_f32 v13, v13, v165\n"
"v_mfma_f32_16x16x32_bf16 v[64:67], a[72:75], a[104:107], v[64:67]\n"
"v_mul_f32 v14, v14, v166\n"
"v_mul_f32 v15, v15, v167\n"
"v_mfma_f32_16x16x32_bf16 v[64:67], a[76:79], a[108:111], v[64:67]\n"
"v_cvt_pk_bf16_f32 v24, v12, v13\n"
"v_cvt_pk_bf16_f32 v25, v14, v15\n"
"v_mfma_f32_16x16x32_bf16 v[36:39], a[68:71], a[108:111], v[36:39]\n"
"ds_write_b64 v2, v[24:25] offset:26624\n"
"v_mfma_f32_16x16x32_bf16 v[36:39], a[64:67], a[104:107], v[36:39]\n"
"s_waitcnt lgkmcnt(11)\n"
"v_lshlrev_b32 v12, 16, v18\n"
"v_and_b32 v13, s63, v18\n"
"v_mfma_f32_16x16x32_bf16 v[40:43], a[64:67], a[112:115], v[40:43]\n"
"v_lshlrev_b32 v14, 16, v19\n"
"v_and_b32 v15, s63, v19\n"
"v_mfma_f32_16x16x32_bf16 v[40:43], a[68:71], a[116:119], v[40:43]\n"
"global_load_dwordx4 a[52:55], v0, s[34:35] offset:1024 sc1\n"
"v_mul_f32 v12, v12, v192\n"
"v_mfma_f32_16x16x32_bf16 v[68:71], a[76:79], a[116:119], v[68:71]\n"
"v_mul_f32 v13, v13, v193\n"
"v_mul_f32 v14, v14, v194\n"
"v_mfma_f32_16x16x32_bf16 v[68:71], a[72:75], a[112:115], v[68:71]\n"
"v_mul_f32 v15, v15, v195\n"
"v_mfma_f32_16x16x32_bf16 v[72:75], a[72:75], a[120:123], v[72:75]\n"
"v_cvt_pk_bf16_f32 v26, v12, v13\n"
"v_mfma_f32_16x16x32_bf16 v[72:75], a[76:79], a[124:127], v[72:75]\n"
"v_cvt_pk_bf16_f32 v27, v14, v15\n"
"v_mfma_f32_16x16x32_bf16 v[44:47], a[68:71], a[124:127], v[44:47]\n"
"global_store_dwordx4 v0, v[24:27], s[54:55] offset:-2048 sc1\n"
"ds_write_b64 v2, v[26:27] offset:26632\n"
"v_accvgpr_read_b32 v16, a236\n"
"v_mfma_f32_16x16x32_bf16 v[44:47], a[64:67], a[120:123], v[44:47]\n"
"s_waitcnt lgkmcnt(7)\n"
"v_accvgpr_read_b32 v17, a237\n"
"v_accvgpr_read_b32 v18, a238\n"
"v_mfma_f32_16x16x32_bf16 v[48:51], a[64:67], a[128:131], v[48:51]\n"
"v_accvgpr_read_b32 v19, a239\n"
"v_lshlrev_b32 v12, 16, v16\n"
"v_mfma_f32_16x16x32_bf16 v[48:51], a[68:71], a[132:135], v[48:51]\n"
"global_load_dwordx4 a[56:59], v0, s[34:35] offset:2048 sc1\n"
"v_and_b32 v13, s63, v16\n"
"v_mfma_f32_16x16x32_bf16 v[76:79], a[76:79], a[132:135], v[76:79]\n"
"v_lshlrev_b32 v14, 16, v17\n"
"v_and_b32 v15, s63, v17\n"
"v_mfma_f32_16x16x32_bf16 v[76:79], a[72:75], a[128:131], v[76:79]\n"
"v_mul_f32 v12, v12, v220\n"
"v_mul_f32 v13, v13, v221\n"
"v_mfma_f32_16x16x32_bf16 v[80:83], a[72:75], a[136:139], v[80:83]\n"
"v_mul_f32 v14, v14, v222\n"
"v_mul_f32 v15, v15, v223\n"
"v_mfma_f32_16x16x32_bf16 v[80:83], a[76:79], a[140:143], v[80:83]\n"
"v_cvt_pk_bf16_f32 v28, v12, v13\n"
"v_cvt_pk_bf16_f32 v29, v14, v15\n"
"v_mfma_f32_16x16x32_bf16 v[52:55], a[68:71], a[140:143], v[52:55]\n"
"ds_write_b64 v2, v[28:29] offset:27648\n"
"v_mfma_f32_16x16x32_bf16 v[52:55], a[64:67], a[136:139], v[52:55]\n"
"s_waitcnt lgkmcnt(3)\n"
"v_mov_b32 v8, s65\n"
"v_mfma_f32_16x16x32_bf16 v[56:59], a[64:67], a[144:147], v[56:59]\n"
"v_mov_b32 v9, s60\n"
"v_mfma_f32_16x16x32_bf16 v[56:59], a[68:71], a[148:151], v[56:59]\n"
"global_load_dwordx4 a[60:63], v0, s[34:35] offset:3072 sc1\n"
"v_lshlrev_b32 v12, 16, v18\n"
"v_mfma_f32_16x16x32_bf16 v[84:87], a[76:79], a[148:151], v[84:87]\n"
"buffer_store_dwordx2 v[8:9], off, s[56:59], %13 sc1\n"
"v_and_b32 v13, s63, v18\n"
"v_mfma_f32_16x16x32_bf16 v[84:87], a[72:75], a[144:147], v[84:87]\n"
"v_lshlrev_b32 v14, 16, v19\n"
"v_mfma_f32_16x16x32_bf16 a[4:7], a[72:75], a[152:155], a[4:7]\n"
"s_add_u32 s34, s34, 8192\n"
"v_and_b32 v15, s63, v19\n"
"v_mul_f32 v12, v12, v248\n"
"v_mfma_f32_16x16x32_bf16 a[4:7], a[76:79], a[156:159], a[4:7]\n"
"v_mul_f32 v13, v13, v249\n"
"v_mfma_f32_16x16x32_bf16 a[0:3], a[68:71], a[156:159], a[0:3]\n"
"v_mul_f32 v14, v14, v250\n"
"v_mul_f32 v15, v15, v251\n"
"v_mfma_f32_16x16x32_bf16 a[0:3], a[64:67], a[152:155], a[0:3]\n"
"s_waitcnt vmcnt(12)\n"
"v_cvt_pk_bf16_f32 v30, v12, v13\n"
"v_cvt_pk_bf16_f32 v31, v14, v15\n"
"v_mfma_f32_16x16x32_bf16 v[88:91], a[80:83], a[96:99], v[88:91]\n"
"global_store_dwordx4 v0, v[28:31], s[54:55] offset:-1024 sc1\n"
"ds_write_b64 v2, v[30:31] offset:27656\n"
"v_mfma_f32_16x16x32_bf16 v[88:91], a[84:87], a[100:103], v[88:91]\n"
"global_load_dwordx4 a[64:67], v0, s[34:35] offset:-4096 sc1\n"
"v_accvgpr_read_b32 v16, a240\n"
"v_mfma_f32_16x16x32_bf16 v[116:119], a[92:95], a[100:103], v[116:119]\n"
"ds_read_b128 a[100:103], v2 offset:1024\n"
"v_accvgpr_read_b32 v17, a241\n"
"v_mfma_f32_16x16x32_bf16 v[116:119], a[88:91], a[96:99], v[116:119]\n"
"ds_read_b128 a[96:99], v2 offset:0\n"
"v_accvgpr_read_b32 v18, a242\n"
"v_mfma_f32_16x16x32_bf16 v[120:123], a[88:91], a[104:107], v[120:123]\n"
"v_accvgpr_read_b32 v19, a243\n"
"v_lshlrev_b32 v12, 16, v16\n"
"v_mfma_f32_16x16x32_bf16 v[120:123], a[92:95], a[108:111], v[120:123]\n"
"v_and_b32 v13, s63, v16\n"
"v_lshlrev_b32 v14, 16, v17\n"
"v_mfma_f32_16x16x32_bf16 v[92:95], a[84:87], a[108:111], v[92:95]\n"
"ds_read_b128 a[108:111], v2 offset:3072\n"
"v_and_b32 v15, s63, v17\n"
"v_mfma_f32_16x16x32_bf16 v[92:95], a[80:83], a[104:107], v[92:95]\n"
"ds_read_b128 a[104:107], v2 offset:2048\n"
"v_mul_f32 v12, v12, v168\n"
"v_mfma_f32_16x16x32_bf16 v[96:99], a[80:83], a[112:115], v[96:99]\n"
"v_mul_f32 v13, v13, v169\n"
"v_mul_f32 v14, v14, v170\n"
"v_mfma_f32_16x16x32_bf16 v[96:99], a[84:87], a[116:119], v[96:99]\n"
"global_load_dwordx4 a[68:71], v0, s[34:35] offset:-3072 sc1\n"
"v_mul_f32 v15, v15, v171\n"
"v_mfma_f32_16x16x32_bf16 v[124:127], a[92:95], a[116:119], v[124:127]\n"
"ds_read_b128 a[116:119], v2 offset:5120\n"
"v_cvt_pk_bf16_f32 v24, v12, v13\n"
"v_mfma_f32_16x16x32_bf16 v[124:127], a[88:91], a[112:115], v[124:127]\n"
"ds_read_b128 a[112:115], v2 offset:4096\n"
"v_cvt_pk_bf16_f32 v25, v14, v15\n"
"v_mfma_f32_16x16x32_bf16 v[128:131], a[88:91], a[120:123], v[128:131]\n"
"ds_write_b64 v2, v[24:25] offset:28672\n"
"v_mfma_f32_16x16x32_bf16 v[128:131], a[92:95], a[124:127], v[128:131]\n"
"v_lshlrev_b32 v12, 16, v18\n"
"v_and_b32 v13, s63, v18\n"
"v_mfma_f32_16x16x32_bf16 v[100:103], a[84:87], a[124:127], v[100:103]\n"
"ds_read_b128 a[124:127], v2 offset:7168\n"
"v_lshlrev_b32 v14, 16, v19\n"
"v_mfma_f32_16x16x32_bf16 v[100:103], a[80:83], a[120:123], v[100:103]\n"
"ds_read_b128 a[120:123], v2 offset:6144\n"
"v_and_b32 v15, s63, v19\n"
"v_mfma_f32_16x16x32_bf16 v[104:107], a[80:83], a[128:131], v[104:107]\n"
"v_mul_f32 v12, v12, v196\n"
"v_mul_f32 v13, v13, v197\n"
"v_mfma_f32_16x16x32_bf16 v[104:107], a[84:87], a[132:135], v[104:107]\n"
"global_load_dwordx4 a[72:75], v0, s[34:35] offset:-2048 sc1\n"
"v_mul_f32 v14, v14, v198\n"
"v_mfma_f32_16x16x32_bf16 v[132:135], a[92:95], a[132:135], v[132:135]\n"
"ds_read_b128 a[132:135], v2 offset:9216\n"
"v_mul_f32 v15, v15, v199\n"
"v_mfma_f32_16x16x32_bf16 v[132:135], a[88:91], a[128:131], v[132:135]\n"
"ds_read_b128 a[128:131], v2 offset:8192\n"
"v_cvt_pk_bf16_f32 v26, v12, v13\n"
"v_mfma_f32_16x16x32_bf16 v[136:139], a[88:91], a[136:139], v[136:139]\n"
"v_cvt_pk_bf16_f32 v27, v14, v15\n"
"v_mfma_f32_16x16x32_bf16 v[136:139], a[92:95], a[140:143], v[136:139]\n"
"global_store_dwordx4 v0, v[24:27], s[54:55] offset:0 sc1\n"
"ds_write_b64 v2, v[26:27] offset:28680\n"
"v_accvgpr_read_b32 v16, a248\n"
"v_mfma_f32_16x16x32_bf16 v[108:111], a[84:87], a[140:143], v[108:111]\n"
"ds_read_b128 a[140:143], v2 offset:11264\n"
"v_accvgpr_read_b32 v17, a249\n"
"v_mfma_f32_16x16x32_bf16 v[108:111], a[80:83], a[136:139], v[108:111]\n"
"ds_read_b128 a[136:139], v2 offset:10240\n"
"v_accvgpr_read_b32 v18, a250\n"
"v_mfma_f32_16x16x32_bf16 v[112:115], a[80:83], a[144:147], v[112:115]\n"
"v_accvgpr_read_b32 v19, a251\n"
"v_lshlrev_b32 v12, 16, v16\n"
"v_mfma_f32_16x16x32_bf16 v[112:115], a[84:87], a[148:151], v[112:115]\n"
"global_load_dwordx4 a[76:79], v0, s[34:35] offset:-1024 sc1\n"
"v_and_b32 v13, s63, v16\n"
"v_mfma_f32_16x16x32_bf16 v[140:143], a[92:95], a[148:151], v[140:143]\n"
"ds_read_b128 a[148:151], v2 offset:13312\n"
"v_lshlrev_b32 v14, 16, v17\n"
"v_mfma_f32_16x16x32_bf16 v[140:143], a[88:91], a[144:147], v[140:143]\n"
"ds_read_b128 a[144:147], v2 offset:12288\n"
"v_and_b32 v15, s63, v17\n"
"v_mfma_f32_16x16x32_bf16 a[12:15], a[88:91], a[152:155], a[12:15]\n"
"v_accvgpr_read_b32 v10, a16\n"
"v_accvgpr_read_b32 v11, a17\n"
"v_mfma_f32_16x16x32_bf16 a[12:15], a[92:95], a[156:159], a[12:15]\n"
"v_mul_f32 v12, v12, v10\n"
"v_mul_f32 v13, v13, v11\n"
"v_mfma_f32_16x16x32_bf16 a[8:11], a[84:87], a[156:159], a[8:11]\n"
"ds_read_b128 a[156:159], v2 offset:15360\n"
"v_accvgpr_read_b32 v10, a18\n"
"v_mfma_f32_16x16x32_bf16 a[8:11], a[80:83], a[152:155], a[8:11]\n"
"s_waitcnt vmcnt(13) lgkmcnt(13)\n"
"ds_read_b128 a[152:155], v2 offset:14336\n"
"v_accvgpr_read_b32 v11, a19\n"
"v_mfma_f32_16x16x32_bf16 v[144:147], a[32:35], a[96:99], v[144:147]\n"
"v_mul_f32 v14, v14, v10\n"
"v_mul_f32 v15, v15, v11\n"
"v_mfma_f32_16x16x32_bf16 v[144:147], a[36:39], a[100:103], v[144:147]\n"
"global_load_dwordx4 a[80:83], v0, s[34:35] offset:0 sc1\n"
"v_cvt_pk_bf16_f32 v28, v12, v13\n"
"v_mfma_f32_16x16x32_bf16 v[172:175], a[44:47], a[100:103], v[172:175]\n"
"v_cvt_pk_bf16_f32 v29, v14, v15\n"
"v_mfma_f32_16x16x32_bf16 v[172:175], a[40:43], a[96:99], v[172:175]\n"
"ds_write_b64 v2, v[28:29] offset:30720\n"
"v_mfma_f32_16x16x32_bf16 v[176:179], a[40:43], a[104:107], v[176:179]\n"
"v_lshlrev_b32 v12, 16, v18\n"
"v_and_b32 v13, s63, v18\n"
"v_mfma_f32_16x16x32_bf16 v[176:179], a[44:47], a[108:111], v[176:179]\n"
"v_lshlrev_b32 v14, 16, v19\n"
"v_and_b32 v15, s63, v19\n"
"v_mfma_f32_16x16x32_bf16 v[148:151], a[36:39], a[108:111], v[148:151]\n"
"v_accvgpr_read_b32 v10, a20\n"
"v_mfma_f32_16x16x32_bf16 v[148:151], a[32:35], a[104:107], v[148:151]\n"
"s_waitcnt lgkmcnt(10)\n"
"v_accvgpr_read_b32 v11, a21\n"
"v_mul_f32 v12, v12, v10\n"
"v_mfma_f32_16x16x32_bf16 v[152:155], a[32:35], a[112:115], v[152:155]\n"
"v_mul_f32 v13, v13, v11\n"
"v_mfma_f32_16x16x32_bf16 v[152:155], a[36:39], a[116:119], v[152:155]\n"
"global_load_dwordx4 a[84:87], v0, s[34:35] offset:1024 sc1\n"
"v_accvgpr_read_b32 v10, a22\n"
"v_mfma_f32_16x16x32_bf16 v[180:183], a[44:47], a[116:119], v[180:183]\n"
"v_accvgpr_read_b32 v11, a23\n"
"v_mul_f32 v14, v14, v10\n"
"v_mfma_f32_16x16x32_bf16 v[180:183], a[40:43], a[112:115], v[180:183]\n"
"v_mul_f32 v15, v15, v11\n"
"v_mfma_f32_16x16x32_bf16 v[184:187], a[40:43], a[120:123], v[184:187]\n"
"v_cvt_pk_bf16_f32 v30, v12, v13\n"
"v_cvt_pk_bf16_f32 v31, v14, v15\n"
"v_mfma_f32_16x16x32_bf16 v[184:187], a[44:47], a[124:127], v[184:187]\n"
"global_store_dwordx4 v0, v[28:31], s[54:55] offset:2048 sc1\n"
"ds_write_b64 v2, v[30:31] offset:30728\n"
"v_mfma_f32_16x16x32_bf16 v[156:159], a[36:39], a[124:127], v[156:159]\n"
"v_accvgpr_read_b32 v16, a244\n"
"v_mfma_f32_16x16x32_bf16 v[156:159], a[32:35], a[120:123], v[156:159]\n"
"s_waitcnt lgkmcnt(6)\n"
"v_accvgpr_read_b32 v17, a245\n"
"v_mfma_f32_16x16x32_bf16 v[160:163], a[32:35], a[128:131], v[160:163]\n"
"v_accvgpr_read_b32 v18, a246\n"
"v_accvgpr_read_b32 v19, a247\n"
"v_mfma_f32_16x16x32_bf16 v[160:163], a[36:39], a[132:135], v[160:163]\n"
"global_load_dwordx4 a[88:91], v0, s[34:35] offset:2048 sc1\n"
"v_lshlrev_b32 v12, 16, v16\n"
"v_mfma_f32_16x16x32_bf16 v[188:191], a[44:47], a[132:135], v[188:191]\n"
"v_and_b32 v13, s63, v16\n"
"v_mfma_f32_16x16x32_bf16 v[188:191], a[40:43], a[128:131], v[188:191]\n"
"v_lshlrev_b32 v14, 16, v17\n"
"v_and_b32 v15, s63, v17\n"
"v_mfma_f32_16x16x32_bf16 v[192:195], a[40:43], a[136:139], v[192:195]\n"
"v_mul_f32 v12, v12, v224\n"
"v_mfma_f32_16x16x32_bf16 v[192:195], a[44:47], a[140:143], v[192:195]\n"
"v_mul_f32 v13, v13, v225\n"
"v_mfma_f32_16x16x32_bf16 v[164:167], a[36:39], a[140:143], v[164:167]\n"
"v_mul_f32 v14, v14, v226\n"
"v_mul_f32 v15, v15, v227\n"
"v_mfma_f32_16x16x32_bf16 v[164:167], a[32:35], a[136:139], v[164:167]\n"
"s_waitcnt lgkmcnt(2)\n"
"v_cvt_pk_bf16_f32 v24, v12, v13\n"
"v_cvt_pk_bf16_f32 v25, v14, v15\n"
"v_mfma_f32_16x16x32_bf16 v[168:171], a[32:35], a[144:147], v[168:171]\n"
"ds_write_b64 v2, v[24:25] offset:29696\n"
"v_mfma_f32_16x16x32_bf16 v[168:171], a[36:39], a[148:151], v[168:171]\n"
"global_load_dwordx4 a[92:95], v0, s[34:35] offset:3072 sc1\n"
"v_lshlrev_b32 v12, 16, v18\n"
"v_mfma_f32_16x16x32_bf16 v[196:199], a[44:47], a[148:151], v[196:199]\n"
"v_and_b32 v13, s63, v18\n"
"v_mfma_f32_16x16x32_bf16 v[196:199], a[40:43], a[144:147], v[196:199]\n"
"v_lshlrev_b32 v14, 16, v19\n"
"v_and_b32 v15, s63, v19\n"
"v_mfma_f32_16x16x32_bf16 a[20:23], a[40:43], a[152:155], a[20:23]\n"
"s_add_u32 s34, s34, 8192\n"
"v_mul_f32 v12, v12, v252\n"
"v_mul_f32 v13, v13, v253\n"
"v_mfma_f32_16x16x32_bf16 a[20:23], a[44:47], a[156:159], a[20:23]\n"
"v_mul_f32 v14, v14, v254\n"
"v_mul_f32 v15, v15, v255\n"
"v_mfma_f32_16x16x32_bf16 a[16:19], a[36:39], a[156:159], a[16:19]\n"
"v_cvt_pk_bf16_f32 v26, v12, v13\n"
"v_cvt_pk_bf16_f32 v27, v14, v15\n"
"v_mfma_f32_16x16x32_bf16 a[16:19], a[32:35], a[152:155], a[16:19]\n"
"s_waitcnt vmcnt(12)\n"
"global_store_dwordx4 v0, v[24:27], s[54:55] offset:1024 sc1\n"
"ds_write_b64 v2, v[26:27] offset:29704\n"
"v_mfma_f32_16x16x32_bf16 v[200:203], a[48:51], a[96:99], v[200:203]\n"
"v_accvgpr_read_b32 v16, a252\n"
"v_accvgpr_read_b32 v17, a253\n"
"v_mfma_f32_16x16x32_bf16 v[200:203], a[52:55], a[100:103], v[200:203]\n"
"global_load_dwordx4 a[32:35], v0, s[34:35] offset:-4096 sc1\n"
"v_accvgpr_read_b32 v18, a254\n"
"v_mfma_f32_16x16x32_bf16 v[228:231], a[60:63], a[100:103], v[228:231]\n"
"ds_read_b128 a[100:103], v3 offset:1024\n"
"v_accvgpr_read_b32 v19, a255\n"
"v_mfma_f32_16x16x32_bf16 v[228:231], a[56:59], a[96:99], v[228:231]\n"
"ds_read_b128 a[96:99], v3 offset:0\n"
"v_lshlrev_b32 v12, 16, v16\n"
"v_mfma_f32_16x16x32_bf16 v[232:235], a[56:59], a[104:107], v[232:235]\n"
"v_and_b32 v13, s63, v16\n"
"v_lshlrev_b32 v14, 16, v17\n"
"v_mfma_f32_16x16x32_bf16 v[232:235], a[60:63], a[108:111], v[232:235]\n"
"v_and_b32 v15, s63, v17\n"
"v_accvgpr_read_b32 v10, a24\n"
"v_mfma_f32_16x16x32_bf16 v[204:207], a[52:55], a[108:111], v[204:207]\n"
"ds_read_b128 a[108:111], v3 offset:3072\n"
"v_accvgpr_read_b32 v11, a25\n"
"v_mfma_f32_16x16x32_bf16 v[204:207], a[48:51], a[104:107], v[204:207]\n"
"ds_read_b128 a[104:107], v3 offset:2048\n"
"v_mul_f32 v12, v12, v10\n"
"v_mfma_f32_16x16x32_bf16 v[208:211], a[48:51], a[112:115], v[208:211]\n"
"v_mul_f32 v13, v13, v11\n"
"v_accvgpr_read_b32 v10, a26\n"
"v_mfma_f32_16x16x32_bf16 v[208:211], a[52:55], a[116:119], v[208:211]\n"
"global_load_dwordx4 a[36:39], v0, s[34:35] offset:-3072 sc1\n"
"v_accvgpr_read_b32 v11, a27\n"
"v_mfma_f32_16x16x32_bf16 v[236:239], a[60:63], a[116:119], v[236:239]\n"
"ds_read_b128 a[116:119], v3 offset:5120\n"
"v_mul_f32 v14, v14, v10\n"
"v_mfma_f32_16x16x32_bf16 v[236:239], a[56:59], a[112:115], v[236:239]\n"
"ds_read_b128 a[112:115], v3 offset:4096\n"
"v_mul_f32 v15, v15, v11\n"
"v_mfma_f32_16x16x32_bf16 v[240:243], a[56:59], a[120:123], v[240:243]\n"
"v_cvt_pk_bf16_f32 v28, v12, v13\n"
"v_cvt_pk_bf16_f32 v29, v14, v15\n"
"v_mfma_f32_16x16x32_bf16 v[240:243], a[60:63], a[124:127], v[240:243]\n"
"ds_write_b64 v2, v[28:29] offset:31744\n"
"v_mfma_f32_16x16x32_bf16 v[212:215], a[52:55], a[124:127], v[212:215]\n"
"ds_read_b128 a[124:127], v3 offset:7168\n"
"v_mfma_f32_16x16x32_bf16 v[212:215], a[48:51], a[120:123], v[212:215]\n"
"ds_read_b128 a[120:123], v3 offset:6144\n"
"v_lshlrev_b32 v12, 16, v18\n"
"v_mfma_f32_16x16x32_bf16 v[216:219], a[48:51], a[128:131], v[216:219]\n"
"v_and_b32 v13, s63, v18\n"
"v_lshlrev_b32 v14, 16, v19\n"
"v_mfma_f32_16x16x32_bf16 v[216:219], a[52:55], a[132:135], v[216:219]\n"
"global_load_dwordx4 a[40:43], v0, s[34:35] offset:-2048 sc1\n"
"v_and_b32 v15, s63, v19\n"
"v_mfma_f32_16x16x32_bf16 v[244:247], a[60:63], a[132:135], v[244:247]\n"
"ds_read_b128 a[132:135], v3 offset:9216\n"
"v_accvgpr_read_b32 v10, a28\n"
"v_mfma_f32_16x16x32_bf16 v[244:247], a[56:59], a[128:131], v[244:247]\n"
"ds_read_b128 a[128:131], v3 offset:8192\n"
"v_accvgpr_read_b32 v11, a29\n"
"v_mfma_f32_16x16x32_bf16 v[248:251], a[56:59], a[136:139], v[248:251]\n"
"v_mul_f32 v12, v12, v10\n"
"v_mul_f32 v13, v13, v11\n"
"v_mfma_f32_16x16x32_bf16 v[248:251], a[60:63], a[140:143], v[248:251]\n"
"v_accvgpr_read_b32 v10, a30\n"
"v_mfma_f32_16x16x32_bf16 v[220:223], a[52:55], a[140:143], v[220:223]\n"
"ds_read_b128 a[140:143], v3 offset:11264\n"
"v_accvgpr_read_b32 v11, a31\n"
"v_mfma_f32_16x16x32_bf16 v[220:223], a[48:51], a[136:139], v[220:223]\n"
"ds_read_b128 a[136:139], v3 offset:10240\n"
"v_mul_f32 v14, v14, v10\n"
"v_mfma_f32_16x16x32_bf16 v[224:227], a[48:51], a[144:147], v[224:227]\n"
"v_mul_f32 v15, v15, v11\n"
"v_cvt_pk_bf16_f32 v30, v12, v13\n"
"v_mfma_f32_16x16x32_bf16 v[224:227], a[52:55], a[148:151], v[224:227]\n"
"global_load_dwordx4 a[44:47], v0, s[34:35] offset:-1024 sc1\n"
"v_cvt_pk_bf16_f32 v31, v14, v15\n"
"v_mfma_f32_16x16x32_bf16 v[252:255], a[60:63], a[148:151], v[252:255]\n"
"global_store_dwordx4 v0, v[28:31], s[54:55] offset:3072 sc1\n"
"ds_read_b128 a[148:151], v3 offset:13312\n"
"ds_write_b64 v2, v[30:31] offset:31752\n"
"v_mfma_f32_16x16x32_bf16 v[252:255], a[56:59], a[144:147], v[252:255]\n"
"ds_read_b128 a[144:147], v3 offset:12288\n"
"v_mfma_f32_16x16x32_bf16 a[28:31], a[56:59], a[152:155], a[28:31]\n"
"v_mfma_f32_16x16x32_bf16 a[28:31], a[60:63], a[156:159], a[28:31]\n"
"v_mfma_f32_16x16x32_bf16 a[24:27], a[52:55], a[156:159], a[24:27]\n"
"ds_read_b128 a[156:159], v3 offset:15360\n"
"v_mfma_f32_16x16x32_bf16 a[24:27], a[48:51], a[152:155], a[24:27]\n"
"s_waitcnt vmcnt(11) lgkmcnt(13)\n"
"ds_read_b128 a[152:155], v3 offset:14336\n"
"v_mfma_f32_16x16x32_bf16 v[144:147], a[64:67], a[96:99], v[144:147]\n"
"ds_read_b128 v[24:27], v2 offset:16384\n"
"v_mfma_f32_16x16x32_bf16 v[144:147], a[68:71], a[100:103], v[144:147]\n"
"global_load_dwordx4 a[48:51], v0, s[34:35] offset:0 sc1\n"
"v_mfma_f32_16x16x32_bf16 v[172:175], a[76:79], a[100:103], v[172:175]\n"
"v_mfma_f32_16x16x32_bf16 v[172:175], a[72:75], a[96:99], v[172:175]\n"
"v_mfma_f32_16x16x32_bf16 v[176:179], a[72:75], a[104:107], v[176:179]\n"
"v_mfma_f32_16x16x32_bf16 v[176:179], a[76:79], a[108:111], v[176:179]\n"
"v_mfma_f32_16x16x32_bf16 v[148:151], a[68:71], a[108:111], v[148:151]\n"
"v_mfma_f32_16x16x32_bf16 v[148:151], a[64:67], a[104:107], v[148:151]\n"
"s_waitcnt lgkmcnt(10)\n"
"v_mfma_f32_16x16x32_bf16 v[152:155], a[64:67], a[112:115], v[152:155]\n"
"v_mfma_f32_16x16x32_bf16 v[152:155], a[68:71], a[116:119], v[152:155]\n"
"global_load_dwordx4 a[52:55], v0, s[34:35] offset:1024 sc1\n"
"v_mfma_f32_16x16x32_bf16 v[180:183], a[76:79], a[116:119], v[180:183]\n"
"v_mfma_f32_16x16x32_bf16 v[180:183], a[72:75], a[112:115], v[180:183]\n"
"v_mfma_f32_16x16x32_bf16 v[184:187], a[72:75], a[120:123], v[184:187]\n"
"s_waitcnt lgkmcnt(0)\n"
"global_store_dwordx4 v0, v[24:27], s[52:53] offset:-4096 sc1\n"
"v_mfma_f32_16x16x32_bf16 v[184:187], a[76:79], a[124:127], v[184:187]\n"
"v_mfma_f32_16x16x32_bf16 v[156:159], a[68:71], a[124:127], v[156:159]\n"
"v_mfma_f32_16x16x32_bf16 v[156:159], a[64:67], a[120:123], v[156:159]\n"
"v_mfma_f32_16x16x32_bf16 v[160:163], a[64:67], a[128:131], v[160:163]\n"
"v_mfma_f32_16x16x32_bf16 v[160:163], a[68:71], a[132:135], v[160:163]\n"
"global_load_dwordx4 a[56:59], v0, s[34:35] offset:2048 sc1\n"
"v_mfma_f32_16x16x32_bf16 v[188:191], a[76:79], a[132:135], v[188:191]\n"
"v_mfma_f32_16x16x32_bf16 v[188:191], a[72:75], a[128:131], v[188:191]\n"
"v_mfma_f32_16x16x32_bf16 v[192:195], a[72:75], a[136:139], v[192:195]\n"
"v_mfma_f32_16x16x32_bf16 v[192:195], a[76:79], a[140:143], v[192:195]\n"
"v_mfma_f32_16x16x32_bf16 v[164:167], a[68:71], a[140:143], v[164:167]\n"
"v_mfma_f32_16x16x32_bf16 v[164:167], a[64:67], a[136:139], v[164:167]\n"
"v_mfma_f32_16x16x32_bf16 v[168:171], a[64:67], a[144:147], v[168:171]\n"
"ds_read_b128 v[28:31], v2 offset:17408\n"
"v_mfma_f32_16x16x32_bf16 v[168:171], a[68:71], a[148:151], v[168:171]\n"
"global_load_dwordx4 a[60:63], v0, s[34:35] offset:3072 sc1\n"
"v_mfma_f32_16x16x32_bf16 v[196:199], a[76:79], a[148:151], v[196:199]\n"
"v_mfma_f32_16x16x32_bf16 v[196:199], a[72:75], a[144:147], v[196:199]\n"
"v_mfma_f32_16x16x32_bf16 a[20:23], a[72:75], a[152:155], a[20:23]\n"
"s_add_u32 s34, s34, 8192\n"
"v_mfma_f32_16x16x32_bf16 a[20:23], a[76:79], a[156:159], a[20:23]\n"
"v_mfma_f32_16x16x32_bf16 a[16:19], a[68:71], a[156:159], a[16:19]\n"
"v_mfma_f32_16x16x32_bf16 a[16:19], a[64:67], a[152:155], a[16:19]\n"
"s_waitcnt vmcnt(11)\n"
"v_mfma_f32_16x16x32_bf16 v[200:203], a[80:83], a[96:99], v[200:203]\n"
"v_mfma_f32_16x16x32_bf16 v[200:203], a[84:87], a[100:103], v[200:203]\n"
"global_load_dwordx4 a[64:67], v0, s[34:35] offset:-4096 sc1\n"
"v_mfma_f32_16x16x32_bf16 v[228:231], a[92:95], a[100:103], v[228:231]\n"
"ds_read_b128 a[100:103], v4 offset:1024\n"
"v_mfma_f32_16x16x32_bf16 v[228:231], a[88:91], a[96:99], v[228:231]\n"
"ds_read_b128 a[96:99], v4 offset:0\n"
"v_mfma_f32_16x16x32_bf16 v[232:235], a[88:91], a[104:107], v[232:235]\n"
"s_waitcnt lgkmcnt(2)\n"
"global_store_dwordx4 v0, v[28:31], s[52:53] offset:-3072 sc1\n"
"v_mfma_f32_16x16x32_bf16 v[232:235], a[92:95], a[108:111], v[232:235]\n"
"v_mfma_f32_16x16x32_bf16 v[204:207], a[84:87], a[108:111], v[204:207]\n"
"ds_read_b128 a[108:111], v4 offset:3072\n"
"v_mfma_f32_16x16x32_bf16 v[204:207], a[80:83], a[104:107], v[204:207]\n"
"ds_read_b128 a[104:107], v4 offset:2048\n"
"v_mfma_f32_16x16x32_bf16 v[208:211], a[80:83], a[112:115], v[208:211]\n"
"v_mfma_f32_16x16x32_bf16 v[208:211], a[84:87], a[116:119], v[208:211]\n"
"global_load_dwordx4 a[68:71], v0, s[34:35] offset:-3072 sc1\n"
"v_mfma_f32_16x16x32_bf16 v[236:239], a[92:95], a[116:119], v[236:239]\n"
"ds_read_b128 a[116:119], v4 offset:5120\n"
"v_mfma_f32_16x16x32_bf16 v[236:239], a[88:91], a[112:115], v[236:239]\n"
"ds_read_b128 a[112:115], v4 offset:4096\n"
"v_mfma_f32_16x16x32_bf16 v[240:243], a[88:91], a[120:123], v[240:243]\n"
"v_mfma_f32_16x16x32_bf16 v[240:243], a[92:95], a[124:127], v[240:243]\n"
"v_mfma_f32_16x16x32_bf16 v[212:215], a[84:87], a[124:127], v[212:215]\n"
"ds_read_b128 a[124:127], v4 offset:7168\n"
"v_mfma_f32_16x16x32_bf16 v[212:215], a[80:83], a[120:123], v[212:215]\n"
"ds_read_b128 a[120:123], v4 offset:6144\n"
"v_mfma_f32_16x16x32_bf16 v[216:219], a[80:83], a[128:131], v[216:219]\n"
"ds_read_b128 v[24:27], v2 offset:18432\n"
"v_mfma_f32_16x16x32_bf16 v[216:219], a[84:87], a[132:135], v[216:219]\n"
"global_load_dwordx4 a[72:75], v0, s[34:35] offset:-2048 sc1\n"
"v_mfma_f32_16x16x32_bf16 v[244:247], a[92:95], a[132:135], v[244:247]\n"
"ds_read_b128 a[132:135], v4 offset:9216\n"
"v_mfma_f32_16x16x32_bf16 v[244:247], a[88:91], a[128:131], v[244:247]\n"
"ds_read_b128 a[128:131], v4 offset:8192\n"
"v_mfma_f32_16x16x32_bf16 v[248:251], a[88:91], a[136:139], v[248:251]\n"
"buffer_load_dwordx2 v[8:9], off, s[56:59], %14 sc1\n"
"v_mfma_f32_16x16x32_bf16 v[248:251], a[92:95], a[140:143], v[248:251]\n"
"v_mfma_f32_16x16x32_bf16 v[220:223], a[84:87], a[140:143], v[220:223]\n"
"ds_read_b128 a[140:143], v4 offset:11264\n"
"v_mfma_f32_16x16x32_bf16 v[220:223], a[80:83], a[136:139], v[220:223]\n"
"ds_read_b128 a[136:139], v4 offset:10240\n"
"v_mfma_f32_16x16x32_bf16 v[224:227], a[80:83], a[144:147], v[224:227]\n"
"v_mfma_f32_16x16x32_bf16 v[224:227], a[84:87], a[148:151], v[224:227]\n"
"global_load_dwordx4 a[76:79], v0, s[34:35] offset:-1024 sc1\n"
"v_mfma_f32_16x16x32_bf16 v[252:255], a[92:95], a[148:151], v[252:255]\n"
"ds_read_b128 a[148:151], v4 offset:13312\n"
"v_mfma_f32_16x16x32_bf16 v[252:255], a[88:91], a[144:147], v[252:255]\n"
"ds_read_b128 a[144:147], v4 offset:12288\n"
"v_mfma_f32_16x16x32_bf16 a[28:31], a[88:91], a[152:155], a[28:31]\n"
"s_waitcnt lgkmcnt(6)\n"
"global_store_dwordx4 v0, v[24:27], s[52:53] offset:-2048 sc1\n"
"v_mfma_f32_16x16x32_bf16 a[28:31], a[92:95], a[156:159], a[28:31]\n"
"v_mfma_f32_16x16x32_bf16 a[24:27], a[84:87], a[156:159], a[24:27]\n"
"ds_read_b128 a[156:159], v4 offset:15360\n"
"v_mfma_f32_16x16x32_bf16 a[24:27], a[80:83], a[152:155], a[24:27]\n"
"s_waitcnt vmcnt(13)\n"
"ds_read_b128 a[152:155], v4 offset:14336\n"
"v_mfma_f32_16x16x32_bf16 v[144:147], a[32:35], a[96:99], v[144:147]\n"
"v_mfma_f32_16x16x32_bf16 v[144:147], a[36:39], a[100:103], v[144:147]\n"
"global_load_dwordx4 a[80:83], v0, s[34:35] offset:0 sc1\n"
"v_mfma_f32_16x16x32_bf16 v[172:175], a[44:47], a[100:103], v[172:175]\n"
"v_mfma_f32_16x16x32_bf16 v[172:175], a[40:43], a[96:99], v[172:175]\n"
"v_mfma_f32_16x16x32_bf16 v[176:179], a[40:43], a[104:107], v[176:179]\n"
"v_mfma_f32_16x16x32_bf16 v[176:179], a[44:47], a[108:111], v[176:179]\n"
"v_mfma_f32_16x16x32_bf16 v[148:151], a[36:39], a[108:111], v[148:151]\n"
"v_mfma_f32_16x16x32_bf16 v[148:151], a[32:35], a[104:107], v[148:151]\n"
"v_mfma_f32_16x16x32_bf16 v[152:155], a[32:35], a[112:115], v[152:155]\n"
"ds_read_b128 v[28:31], v2 offset:19456\n"
"v_mfma_f32_16x16x32_bf16 v[152:155], a[36:39], a[116:119], v[152:155]\n"
"global_load_dwordx4 a[84:87], v0, s[34:35] offset:1024 sc1\n"
"v_mfma_f32_16x16x32_bf16 v[180:183], a[44:47], a[116:119], v[180:183]\n"
"v_mfma_f32_16x16x32_bf16 v[180:183], a[40:43], a[112:115], v[180:183]\n"
"v_mfma_f32_16x16x32_bf16 v[184:187], a[40:43], a[120:123], v[184:187]\n"
"v_mfma_f32_16x16x32_bf16 v[184:187], a[44:47], a[124:127], v[184:187]\n"
"v_mfma_f32_16x16x32_bf16 v[156:159], a[36:39], a[124:127], v[156:159]\n"
"v_mfma_f32_16x16x32_bf16 v[156:159], a[32:35], a[120:123], v[156:159]\n"
"s_waitcnt lgkmcnt(5)\n"
"v_mfma_f32_16x16x32_bf16 v[160:163], a[32:35], a[128:131], v[160:163]\n"
"v_mfma_f32_16x16x32_bf16 v[160:163], a[36:39], a[132:135], v[160:163]\n"
"global_load_dwordx4 a[88:91], v0, s[34:35] offset:2048 sc1\n"
"v_mfma_f32_16x16x32_bf16 v[188:191], a[44:47], a[132:135], v[188:191]\n"
"v_mfma_f32_16x16x32_bf16 v[188:191], a[40:43], a[128:131], v[188:191]\n"
"v_mfma_f32_16x16x32_bf16 v[192:195], a[40:43], a[136:139], v[192:195]\n"
"s_waitcnt lgkmcnt(0)\n"
"global_store_dwordx4 v0, v[28:31], s[52:53] offset:-1024 sc1\n"
"v_mfma_f32_16x16x32_bf16 v[192:195], a[44:47], a[140:143], v[192:195]\n"
"v_mfma_f32_16x16x32_bf16 v[164:167], a[36:39], a[140:143], v[164:167]\n"
"v_mfma_f32_16x16x32_bf16 v[164:167], a[32:35], a[136:139], v[164:167]\n"
"v_mfma_f32_16x16x32_bf16 v[168:171], a[32:35], a[144:147], v[168:171]\n"
"v_mfma_f32_16x16x32_bf16 v[168:171], a[36:39], a[148:151], v[168:171]\n"
"global_load_dwordx4 a[92:95], v0, s[34:35] offset:3072 sc1\n"
"v_mfma_f32_16x16x32_bf16 v[196:199], a[44:47], a[148:151], v[196:199]\n"
"v_mfma_f32_16x16x32_bf16 v[196:199], a[40:43], a[144:147], v[196:199]\n"
"v_mfma_f32_16x16x32_bf16 a[20:23], a[40:43], a[152:155], a[20:23]\n"
"s_add_u32 s34, s34, 8192\n"
"v_mfma_f32_16x16x32_bf16 a[20:23], a[44:47], a[156:159], a[20:23]\n"
"v_mfma_f32_16x16x32_bf16 a[16:19], a[36:39], a[156:159], a[16:19]\n"
"v_mfma_f32_16x16x32_bf16 a[16:19], a[32:35], a[152:155], a[16:19]\n"
"s_waitcnt vmcnt(12)\n"
"v_mfma_f32_16x16x32_bf16 v[200:203], a[48:51], a[96:99], v[200:203]\n"
"ds_read_b128 v[24:27], v2 offset:20480\n"
"v_mfma_f32_16x16x32_bf16 v[200:203], a[52:55], a[100:103], v[200:203]\n"
"global_load_dwordx4 a[32:35], v0, s[34:35] offset:-4096 sc1\n"
"v_mfma_f32_16x16x32_bf16 v[228:231], a[60:63], a[100:103], v[228:231]\n"
"ds_read_b128 a[100:103], v5 offset:1024\n"
"v_mfma_f32_16x16x32_bf16 v[228:231], a[56:59], a[96:99], v[228:231]\n"
"ds_read_b128 a[96:99], v5 offset:0\n"
"v_mfma_f32_16x16x32_bf16 v[232:235], a[56:59], a[104:107], v[232:235]\n"
"v_mfma_f32_16x16x32_bf16 v[232:235], a[60:63], a[108:111], v[232:235]\n"
"v_mfma_f32_16x16x32_bf16 v[204:207], a[52:55], a[108:111], v[204:207]\n"
"ds_read_b128 a[108:111], v5 offset:3072\n"
"v_mfma_f32_16x16x32_bf16 v[204:207], a[48:51], a[104:107], v[204:207]\n"
"ds_read_b128 a[104:107], v5 offset:2048\n"
"v_mfma_f32_16x16x32_bf16 v[208:211], a[48:51], a[112:115], v[208:211]\n"
"v_mfma_f32_16x16x32_bf16 v[208:211], a[52:55], a[116:119], v[208:211]\n"
"global_load_dwordx4 a[36:39], v0, s[34:35] offset:-3072 sc1\n"
"v_mfma_f32_16x16x32_bf16 v[236:239], a[60:63], a[116:119], v[236:239]\n"
"ds_read_b128 a[116:119], v5 offset:5120\n"
"v_mfma_f32_16x16x32_bf16 v[236:239], a[56:59], a[112:115], v[236:239]\n"
"ds_read_b128 a[112:115], v5 offset:4096\n"
"v_mfma_f32_16x16x32_bf16 v[240:243], a[56:59], a[120:123], v[240:243]\n"
"s_waitcnt lgkmcnt(6)\n"
"global_store_dwordx4 v0, v[24:27], s[52:53] offset:0 sc1\n"
"v_mfma_f32_16x16x32_bf16 v[240:243], a[60:63], a[124:127], v[240:243]\n"
"v_mfma_f32_16x16x32_bf16 v[212:215], a[52:55], a[124:127], v[212:215]\n"
"ds_read_b128 a[124:127], v5 offset:7168\n"
"v_mfma_f32_16x16x32_bf16 v[212:215], a[48:51], a[120:123], v[212:215]\n"
"ds_read_b128 a[120:123], v5 offset:6144\n"
"v_mfma_f32_16x16x32_bf16 v[216:219], a[48:51], a[128:131], v[216:219]\n"
"v_mfma_f32_16x16x32_bf16 v[216:219], a[52:55], a[132:135], v[216:219]\n"
"global_load_dwordx4 a[40:43], v0, s[34:35] offset:-2048 sc1\n"
"v_mfma_f32_16x16x32_bf16 v[244:247], a[60:63], a[132:135], v[244:247]\n"
"ds_read_b128 a[132:135], v5 offset:9216\n"
"v_mfma_f32_16x16x32_bf16 v[244:247], a[56:59], a[128:131], v[244:247]\n"
"ds_read_b128 a[128:131], v5 offset:8192\n"
"v_mfma_f32_16x16x32_bf16 v[248:251], a[56:59], a[136:139], v[248:251]\n"
"v_mfma_f32_16x16x32_bf16 v[248:251], a[60:63], a[140:143], v[248:251]\n"
"v_mfma_f32_16x16x32_bf16 v[220:223], a[52:55], a[140:143], v[220:223]\n"
"ds_read_b128 a[140:143], v5 offset:11264\n"
"v_mfma_f32_16x16x32_bf16 v[220:223], a[48:51], a[136:139], v[220:223]\n"
"ds_read_b128 a[136:139], v5 offset:10240\n"
"v_mfma_f32_16x16x32_bf16 v[224:227], a[48:51], a[144:147], v[224:227]\n"
"ds_read_b128 v[28:31], v2 offset:21504\n"
"v_mfma_f32_16x16x32_bf16 v[224:227], a[52:55], a[148:151], v[224:227]\n"
"global_load_dwordx4 a[44:47], v0, s[34:35] offset:-1024 sc1\n"
"v_mfma_f32_16x16x32_bf16 v[252:255], a[60:63], a[148:151], v[252:255]\n"
"ds_read_b128 a[148:151], v5 offset:13312\n"
"v_mfma_f32_16x16x32_bf16 v[252:255], a[56:59], a[144:147], v[252:255]\n"
"ds_read_b128 a[144:147], v5 offset:12288\n"
"v_mfma_f32_16x16x32_bf16 a[28:31], a[56:59], a[152:155], a[28:31]\n"
"v_mfma_f32_16x16x32_bf16 a[28:31], a[60:63], a[156:159], a[28:31]\n"
"v_mfma_f32_16x16x32_bf16 a[24:27], a[52:55], a[156:159], a[24:27]\n"
"ds_read_b128 a[156:159], v5 offset:15360\n"
"v_mfma_f32_16x16x32_bf16 a[24:27], a[48:51], a[152:155], a[24:27]\n"
"s_waitcnt vmcnt(11) lgkmcnt(12)\n"
"ds_read_b128 a[152:155], v5 offset:14336\n"
"v_mfma_f32_16x16x32_bf16 v[144:147], a[64:67], a[96:99], v[144:147]\n"
"v_mfma_f32_16x16x32_bf16 v[144:147], a[68:71], a[100:103], v[144:147]\n"
"global_load_dwordx4 a[48:51], v0, s[34:35] offset:0 sc1\n"
"v_mfma_f32_16x16x32_bf16 v[172:175], a[76:79], a[100:103], v[172:175]\n"
"v_mfma_f32_16x16x32_bf16 v[172:175], a[72:75], a[96:99], v[172:175]\n"
"v_mfma_f32_16x16x32_bf16 v[176:179], a[72:75], a[104:107], v[176:179]\n"
"s_waitcnt lgkmcnt(4)\n"
"global_store_dwordx4 v0, v[28:31], s[52:53] offset:1024 sc1\n"
"v_mfma_f32_16x16x32_bf16 v[176:179], a[76:79], a[108:111], v[176:179]\n"
"v_mfma_f32_16x16x32_bf16 v[148:151], a[68:71], a[108:111], v[148:151]\n"
"v_mfma_f32_16x16x32_bf16 v[148:151], a[64:67], a[104:107], v[148:151]\n"
"v_mfma_f32_16x16x32_bf16 v[152:155], a[64:67], a[112:115], v[152:155]\n"
"v_mfma_f32_16x16x32_bf16 v[152:155], a[68:71], a[116:119], v[152:155]\n"
"global_load_dwordx4 a[52:55], v0, s[34:35] offset:1024 sc1\n"
"v_mfma_f32_16x16x32_bf16 v[180:183], a[76:79], a[116:119], v[180:183]\n"
"v_mfma_f32_16x16x32_bf16 v[180:183], a[72:75], a[112:115], v[180:183]\n"
"v_mfma_f32_16x16x32_bf16 v[184:187], a[72:75], a[120:123], v[184:187]\n"
"v_mfma_f32_16x16x32_bf16 v[184:187], a[76:79], a[124:127], v[184:187]\n"
"v_mfma_f32_16x16x32_bf16 v[156:159], a[68:71], a[124:127], v[156:159]\n"
"v_mfma_f32_16x16x32_bf16 v[156:159], a[64:67], a[120:123], v[156:159]\n"
"v_mfma_f32_16x16x32_bf16 v[160:163], a[64:67], a[128:131], v[160:163]\n"
"ds_read_b128 v[24:27], v2 offset:22528\n"
"v_mfma_f32_16x16x32_bf16 v[160:163], a[68:71], a[132:135], v[160:163]\n"
"global_load_dwordx4 a[56:59], v0, s[34:35] offset:2048 sc1\n"
"v_mfma_f32_16x16x32_bf16 v[188:191], a[76:79], a[132:135], v[188:191]\n"
"v_mfma_f32_16x16x32_bf16 v[188:191], a[72:75], a[128:131], v[188:191]\n"
"v_mfma_f32_16x16x32_bf16 v[192:195], a[72:75], a[136:139], v[192:195]\n"
"v_mfma_f32_16x16x32_bf16 v[192:195], a[76:79], a[140:143], v[192:195]\n"
"v_mfma_f32_16x16x32_bf16 v[164:167], a[68:71], a[140:143], v[164:167]\n"
"v_mfma_f32_16x16x32_bf16 v[164:167], a[64:67], a[136:139], v[164:167]\n"
"s_waitcnt lgkmcnt(1)\n"
"v_mfma_f32_16x16x32_bf16 v[168:171], a[64:67], a[144:147], v[168:171]\n"
"v_mfma_f32_16x16x32_bf16 v[168:171], a[68:71], a[148:151], v[168:171]\n"
"global_load_dwordx4 a[60:63], v0, s[34:35] offset:3072 sc1\n"
"v_mfma_f32_16x16x32_bf16 v[196:199], a[76:79], a[148:151], v[196:199]\n"
"v_mfma_f32_16x16x32_bf16 v[196:199], a[72:75], a[144:147], v[196:199]\n"
"v_mfma_f32_16x16x32_bf16 a[20:23], a[72:75], a[152:155], a[20:23]\n"
"s_waitcnt lgkmcnt(0)\n"
"global_store_dwordx4 v0, v[24:27], s[52:53] offset:2048 sc1\n"
"s_add_u32 s34, s34, 8192\n"
"v_mfma_f32_16x16x32_bf16 a[20:23], a[76:79], a[156:159], a[20:23]\n"
"v_mfma_f32_16x16x32_bf16 a[16:19], a[68:71], a[156:159], a[16:19]\n"
"v_mfma_f32_16x16x32_bf16 a[16:19], a[64:67], a[152:155], a[16:19]\n"
"s_waitcnt vmcnt(11)\n"
"v_mfma_f32_16x16x32_bf16 v[200:203], a[80:83], a[96:99], v[200:203]\n"
"v_mfma_f32_16x16x32_bf16 v[200:203], a[84:87], a[100:103], v[200:203]\n"
"global_load_dwordx4 a[64:67], v0, s[34:35] offset:-4096 sc1\n"
"v_mfma_f32_16x16x32_bf16 v[228:231], a[92:95], a[100:103], v[228:231]\n"
"ds_read_b128 a[100:103], v2 offset:17408\n"
"v_mfma_f32_16x16x32_bf16 v[228:231], a[88:91], a[96:99], v[228:231]\n"
"ds_read_b128 a[96:99], v2 offset:16384\n"
"v_mfma_f32_16x16x32_bf16 v[232:235], a[88:91], a[104:107], v[232:235]\n"
"v_mfma_f32_16x16x32_bf16 v[232:235], a[92:95], a[108:111], v[232:235]\n"
"v_mfma_f32_16x16x32_bf16 v[204:207], a[84:87], a[108:111], v[204:207]\n"
"ds_read_b128 a[108:111], v2 offset:19456\n"
"v_mfma_f32_16x16x32_bf16 v[204:207], a[80:83], a[104:107], v[204:207]\n"
"ds_read_b128 a[104:107], v2 offset:18432\n"
"v_mfma_f32_16x16x32_bf16 v[208:211], a[80:83], a[112:115], v[208:211]\n"
"ds_read_b128 v[28:31], v2 offset:23552\n"
"v_mfma_f32_16x16x32_bf16 v[208:211], a[84:87], a[116:119], v[208:211]\n"
"global_load_dwordx4 a[68:71], v0, s[34:35] offset:-3072 sc1\n"
"v_mfma_f32_16x16x32_bf16 v[236:239], a[92:95], a[116:119], v[236:239]\n"
"ds_read_b128 a[116:119], v2 offset:21504\n"
"v_mfma_f32_16x16x32_bf16 v[236:239], a[88:91], a[112:115], v[236:239]\n"
"ds_read_b128 a[112:115], v2 offset:20480\n"
"v_mfma_f32_16x16x32_bf16 v[240:243], a[88:91], a[120:123], v[240:243]\n"
"v_mfma_f32_16x16x32_bf16 v[240:243], a[92:95], a[124:127], v[240:243]\n"
"v_mfma_f32_16x16x32_bf16 v[212:215], a[84:87], a[124:127], v[212:215]\n"
"ds_read_b128 a[124:127], v2 offset:23552\n"
"v_mfma_f32_16x16x32_bf16 v[212:215], a[80:83], a[120:123], v[212:215]\n"
"ds_read_b128 a[120:123], v2 offset:22528\n"
"v_mfma_f32_16x16x32_bf16 v[216:219], a[80:83], a[128:131], v[216:219]\n"
"v_mfma_f32_16x16x32_bf16 v[216:219], a[84:87], a[132:135], v[216:219]\n"
"global_load_dwordx4 a[72:75], v0, s[34:35] offset:-2048 sc1\n"
"v_mfma_f32_16x16x32_bf16 v[244:247], a[92:95], a[132:135], v[244:247]\n"
"ds_read_b128 a[132:135], v2 offset:25600\n"
"v_mfma_f32_16x16x32_bf16 v[244:247], a[88:91], a[128:131], v[244:247]\n"
"ds_read_b128 a[128:131], v2 offset:24576\n"
"v_mfma_f32_16x16x32_bf16 v[248:251], a[88:91], a[136:139], v[248:251]\n"
"s_waitcnt lgkmcnt(6)\n"
"global_store_dwordx4 v0, v[28:31], s[52:53] offset:3072 sc1\n"
"v_mfma_f32_16x16x32_bf16 v[248:251], a[92:95], a[140:143], v[248:251]\n"
"v_mfma_f32_16x16x32_bf16 v[220:223], a[84:87], a[140:143], v[220:223]\n"
"ds_read_b128 a[140:143], v2 offset:27648\n"
"v_mfma_f32_16x16x32_bf16 v[220:223], a[80:83], a[136:139], v[220:223]\n"
"ds_read_b128 a[136:139], v2 offset:26624\n"
"v_mfma_f32_16x16x32_bf16 v[224:227], a[80:83], a[144:147], v[224:227]\n"
"v_mfma_f32_16x16x32_bf16 v[224:227], a[84:87], a[148:151], v[224:227]\n"
"global_load_dwordx4 a[76:79], v0, s[34:35] offset:-1024 sc1\n"
"v_mfma_f32_16x16x32_bf16 v[252:255], a[92:95], a[148:151], v[252:255]\n"
"ds_read_b128 a[148:151], v2 offset:29696\n"
"v_mfma_f32_16x16x32_bf16 v[252:255], a[88:91], a[144:147], v[252:255]\n"
"ds_read_b128 a[144:147], v2 offset:28672\n"
"v_mfma_f32_16x16x32_bf16 a[28:31], a[88:91], a[152:155], a[28:31]\n"
"v_mfma_f32_16x16x32_bf16 a[28:31], a[92:95], a[156:159], a[28:31]\n"
"v_mfma_f32_16x16x32_bf16 a[24:27], a[84:87], a[156:159], a[24:27]\n"
"ds_read_b128 a[156:159], v2 offset:31744\n"
"v_mfma_f32_16x16x32_bf16 a[24:27], a[80:83], a[152:155], a[24:27]\n"
"s_waitcnt vmcnt(11)\n"
"ds_read_b128 a[152:155], v2 offset:30720\n"
"v_mfma_f32_16x16x32_bf16 v[32:35], a[32:35], a[96:99], v[32:35]\n"
"v_mfma_f32_16x16x32_bf16 v[32:35], a[36:39], a[100:103], v[32:35]\n"
"global_load_dwordx4 a[80:83], v0, s[34:35] offset:0 sc1\n"
"v_mfma_f32_16x16x32_bf16 v[60:63], a[44:47], a[100:103], v[60:63]\n"
"s_barrier\n"
"v_mfma_f32_16x16x32_bf16 v[60:63], a[40:43], a[96:99], v[60:63]\n"
"v_mfma_f32_16x16x32_bf16 v[64:67], a[40:43], a[104:107], v[64:67]\n"
"v_mfma_f32_16x16x32_bf16 v[64:67], a[44:47], a[108:111], v[64:67]\n"
"v_mfma_f32_16x16x32_bf16 v[36:39], a[36:39], a[108:111], v[36:39]\n"
"v_subrev_u32 v8, s65, v8\n"
"v_mfma_f32_16x16x32_bf16 v[36:39], a[32:35], a[104:107], v[36:39]\n"
"s_waitcnt lgkmcnt(8)\n"
"v_xor_b32 v9, s79, v9\n"
"v_mfma_f32_16x16x32_bf16 v[40:43], a[32:35], a[112:115], v[40:43]\n"
"v_or_b32 v8, v8, v9\n"
"v_mfma_f32_16x16x32_bf16 v[40:43], a[36:39], a[116:119], v[40:43]\n"
"global_load_dwordx4 a[84:87], v0, s[34:35] offset:1024 sc1\n"
"v_cmp_lt_u32 vcc, 15, v8\n"
"v_mfma_f32_16x16x32_bf16 v[68:71], a[44:47], a[116:119], v[68:71]\n"
"s_cbranch_vccnz L_slow0\n"
"L_back0:\n"
"v_mfma_f32_16x16x32_bf16 v[68:71], a[40:43], a[112:115], v[68:71]\n"
"v_readfirstlane_b32 s66, v9\n"
"v_mfma_f32_16x16x32_bf16 v[72:75], a[40:43], a[120:123], v[72:75]\n"
"v_mfma_f32_16x16x32_bf16 v[72:75], a[44:47], a[124:127], v[72:75]\n"
"s_and_b32 s66, s66, 3\n"
"v_mfma_f32_16x16x32_bf16 v[44:47], a[36:39], a[124:127], v[44:47]\n"
"s_lshl_b32 s69, s66, 13\n"
"v_mfma_f32_16x16x32_bf16 v[44:47], a[32:35], a[120:123], v[44:47]\n"
"s_cmp_eq_u32 s66, 3\n"
"s_cselect_b32 s75, 8192, 0\n"
"s_waitcnt lgkmcnt(4)\n"
"v_mfma_f32_16x16x32_bf16 v[48:51], a[32:35], a[128:131], v[48:51]\n"
"v_mfma_f32_16x16x32_bf16 v[48:51], a[36:39], a[132:135], v[48:51]\n"
"global_load_dwordx4 a[88:91], v0, s[34:35] offset:2048 sc1\n"
"v_lshrrev_b32 v16, 4, v0\n"
"v_mfma_f32_16x16x32_bf16 v[76:79], a[44:47], a[132:135], v[76:79]\n"
"v_and_b32 v16, 0x30, v16\n"
"v_mfma_f32_16x16x32_bf16 v[76:79], a[40:43], a[128:131], v[76:79]\n"
"buffer_load_dwordx2 v[18:19], v16, s[44:47], s61 offen offset:0\n"
"v_mfma_f32_16x16x32_bf16 v[80:83], a[40:43], a[136:139], v[80:83]\n"
"v_mfma_f32_16x16x32_bf16 v[80:83], a[44:47], a[140:143], v[80:83]\n"
"v_mfma_f32_16x16x32_bf16 v[52:55], a[36:39], a[140:143], v[52:55]\n"
"v_mfma_f32_16x16x32_bf16 v[52:55], a[32:35], a[136:139], v[52:55]\n"
"s_waitcnt lgkmcnt(0)\n"
"v_mfma_f32_16x16x32_bf16 v[56:59], a[32:35], a[144:147], v[56:59]\n"
"s_add_u32 m0, s64, 0\n"
"v_mfma_f32_16x16x32_bf16 v[56:59], a[36:39], a[148:151], v[56:59]\n"
"global_load_dwordx4 a[92:95], v0, s[34:35] offset:3072 sc1\n"
"v_lshrrev_b32 v20, 4, v0\n"
"v_mfma_f32_16x16x32_bf16 v[84:87], a[44:47], a[148:151], v[84:87]\n"
"v_and_b32 v20, 0x30, v20\n"
"v_mfma_f32_16x16x32_bf16 v[84:87], a[40:43], a[144:147], v[84:87]\n"
"buffer_load_dwordx2 v[22:23], v20, s[44:47], s61 offen offset:8\n"
"v_mfma_f32_16x16x32_bf16 a[4:7], a[40:43], a[152:155], a[4:7]\n"
"s_add_u32 s34, s34, 8192\n"
"v_mfma_f32_16x16x32_bf16 a[4:7], a[44:47], a[156:159], a[4:7]\n"
"v_mfma_f32_16x16x32_bf16 a[0:3], a[36:39], a[156:159], a[0:3]\n"
"v_mfma_f32_16x16x32_bf16 a[0:3], a[32:35], a[152:155], a[0:3]\n"
"s_waitcnt vmcnt(12)\n"
"v_mfma_f32_16x16x32_bf16 v[88:91], a[48:51], a[96:99], v[88:91]\n"
"v_mfma_f32_16x16x32_bf16 v[88:91], a[52:55], a[100:103], v[88:91]\n"
"global_load_dwordx4 a[32:35], v0, s[34:35] offset:-4096 sc1\n"
"v_mfma_f32_16x16x32_bf16 v[116:119], a[60:63], a[100:103], v[116:119]\n"
"v_mfma_f32_16x16x32_bf16 v[116:119], a[56:59], a[96:99], v[116:119]\n"
"v_mfma_f32_16x16x32_bf16 v[120:123], a[56:59], a[104:107], v[120:123]\n"
"v_mfma_f32_16x16x32_bf16 v[120:123], a[60:63], a[108:111], v[120:123]\n"
"v_mfma_f32_16x16x32_bf16 v[92:95], a[52:55], a[108:111], v[92:95]\n"
"v_mfma_f32_16x16x32_bf16 v[92:95], a[48:51], a[104:107], v[92:95]\n"
"v_mfma_f32_16x16x32_bf16 v[96:99], a[48:51], a[112:115], v[96:99]\n"
"buffer_load_dwordx4 v0, s[48:51], s75 offen offset:0 sc1 lds\n"
"v_mfma_f32_16x16x32_bf16 v[96:99], a[52:55], a[116:119], v[96:99]\n"
"global_load_dwordx4 a[36:39], v0, s[34:35] offset:-3072 sc1\n"
"v_mfma_f32_16x16x32_bf16 v[124:127], a[60:63], a[116:119], v[124:127]\n"
"v_mfma_f32_16x16x32_bf16 v[124:127], a[56:59], a[112:115], v[124:127]\n"
"v_mfma_f32_16x16x32_bf16 v[128:131], a[56:59], a[120:123], v[128:131]\n"
"v_mfma_f32_16x16x32_bf16 v[128:131], a[60:63], a[124:127], v[128:131]\n"
"v_mfma_f32_16x16x32_bf16 v[100:103], a[52:55], a[124:127], v[100:103]\n"
"v_mfma_f32_16x16x32_bf16 v[100:103], a[48:51], a[120:123], v[100:103]\n"
"v_mfma_f32_16x16x32_bf16 v[104:107], a[48:51], a[128:131], v[104:107]\n"
"v_mfma_f32_16x16x32_bf16 v[104:107], a[52:55], a[132:135], v[104:107]\n"
"global_load_dwordx4 a[40:43], v0, s[34:35] offset:-2048 sc1\n"
"v_mfma_f32_16x16x32_bf16 v[132:135], a[60:63], a[132:135], v[132:135]\n"
"v_mfma_f32_16x16x32_bf16 v[132:135], a[56:59], a[128:131], v[132:135]\n"
"v_mfma_f32_16x16x32_bf16 v[136:139], a[56:59], a[136:139], v[136:139]\n"
"v_mfma_f32_16x16x32_bf16 v[136:139], a[60:63], a[140:143], v[136:139]\n"
"v_mfma_f32_16x16x32_bf16 v[108:111], a[52:55], a[140:143], v[108:111]\n"
"v_mfma_f32_16x16x32_bf16 v[108:111], a[48:51], a[136:139], v[108:111]\n"
"v_mfma_f32_16x16x32_bf16 v[112:115], a[48:51], a[144:147], v[112:115]\n"
"buffer_load_dwordx4 v0, s[48:51], s75 offen offset:1024 sc1 lds\n"
"v_mfma_f32_16x16x32_bf16 v[112:115], a[52:55], a[148:151], v[112:115]\n"
"global_load_dwordx4 a[44:47], v0, s[34:35] offset:-1024 sc1\n"
"v_mfma_f32_16x16x32_bf16 v[140:143], a[60:63], a[148:151], v[140:143]\n"
"v_mfma_f32_16x16x32_bf16 v[140:143], a[56:59], a[144:147], v[140:143]\n"
"v_mfma_f32_16x16x32_bf16 a[12:15], a[56:59], a[152:155], a[12:15]\n"
"v_mfma_f32_16x16x32_bf16 a[12:15], a[60:63], a[156:159], a[12:15]\n"
"v_mfma_f32_16x16x32_bf16 a[8:11], a[52:55], a[156:159], a[8:11]\n"
"v_mfma_f32_16x16x32_bf16 a[8:11], a[48:51], a[152:155], a[8:11]\n"
"s_waitcnt vmcnt(12)\n"
"v_mfma_f32_16x16x32_bf16 v[144:147], a[64:67], a[96:99], v[144:147]\n"
"v_mfma_f32_16x16x32_bf16 v[144:147], a[68:71], a[100:103], v[144:147]\n"
"global_load_dwordx4 a[48:51], v0, s[34:35] offset:0 sc1\n"
"v_mfma_f32_16x16x32_bf16 v[172:175], a[76:79], a[100:103], v[172:175]\n"
"v_mfma_f32_16x16x32_bf16 v[172:175], a[72:75], a[96:99], v[172:175]\n"
"v_mfma_f32_16x16x32_bf16 v[176:179], a[72:75], a[104:107], v[176:179]\n"
"v_mfma_f32_16x16x32_bf16 v[176:179], a[76:79], a[108:111], v[176:179]\n"
"v_mfma_f32_16x16x32_bf16 v[148:151], a[68:71], a[108:111], v[148:151]\n"
"v_mfma_f32_16x16x32_bf16 v[148:151], a[64:67], a[104:107], v[148:151]\n"
"buffer_load_dwordx4 v0, s[48:51], s75 offen offset:2048 sc1 lds\n"
"v_mfma_f32_16x16x32_bf16 v[152:155], a[64:67], a[112:115], v[152:155]\n"
"v_mfma_f32_16x16x32_bf16 v[152:155], a[68:71], a[116:119], v[152:155]\n"
"global_load_dwordx4 a[52:55], v0, s[34:35] offset:1024 sc1\n"
"v_mfma_f32_16x16x32_bf16 v[180:183], a[76:79], a[116:119], v[180:183]\n"
"v_mfma_f32_16x16x32_bf16 v[180:183], a[72:75], a[112:115], v[180:183]\n"
"v_mfma_f32_16x16x32_bf16 v[184:187], a[72:75], a[120:123], v[184:187]\n"
"v_mfma_f32_16x16x32_bf16 v[184:187], a[76:79], a[124:127], v[184:187]\n"
"v_mfma_f32_16x16x32_bf16 v[156:159], a[68:71], a[124:127], v[156:159]\n"
"v_mfma_f32_16x16x32_bf16 v[156:159], a[64:67], a[120:123], v[156:159]\n"
"v_mfma_f32_16x16x32_bf16 v[160:163], a[64:67], a[128:131], v[160:163]\n"
"v_mfma_f32_16x16x32_bf16 v[160:163], a[68:71], a[132:135], v[160:163]\n"
"global_load_dwordx4 a[56:59], v0, s[34:35] offset:2048 sc1\n"
"v_mfma_f32_16x16x32_bf16 v[188:191], a[76:79], a[132:135], v[188:191]\n"
"v_mfma_f32_16x16x32_bf16 v[188:191], a[72:75], a[128:131], v[188:191]\n"
"v_mfma_f32_16x16x32_bf16 v[192:195], a[72:75], a[136:139], v[192:195]\n"
"v_mfma_f32_16x16x32_bf16 v[192:195], a[76:79], a[140:143], v[192:195]\n"
"v_mfma_f32_16x16x32_bf16 v[164:167], a[68:71], a[140:143], v[164:167]\n"
"v_mfma_f32_16x16x32_bf16 v[164:167], a[64:67], a[136:139], v[164:167]\n"
"buffer_load_dwordx4 v0, s[48:51], s75 offen offset:3072 sc1 lds\n"
"v_mfma_f32_16x16x32_bf16 v[168:171], a[64:67], a[144:147], v[168:171]\n"
"v_mfma_f32_16x16x32_bf16 v[168:171], a[68:71], a[148:151], v[168:171]\n"
"global_load_dwordx4 a[60:63], v0, s[34:35] offset:3072 sc1\n"
"v_mfma_f32_16x16x32_bf16 v[196:199], a[76:79], a[148:151], v[196:199]\n"
"v_mfma_f32_16x16x32_bf16 v[196:199], a[72:75], a[144:147], v[196:199]\n"
"v_mfma_f32_16x16x32_bf16 a[20:23], a[72:75], a[152:155], a[20:23]\n"
"s_add_u32 s34, s34, 8192\n"
"v_mfma_f32_16x16x32_bf16 a[20:23], a[76:79], a[156:159], a[20:23]\n"
"v_mfma_f32_16x16x32_bf16 a[16:19], a[68:71], a[156:159], a[16:19]\n"
"v_mfma_f32_16x16x32_bf16 a[16:19], a[64:67], a[152:155], a[16:19]\n"
"s_waitcnt vmcnt(13)\n"
"v_mfma_f32_16x16x32_bf16 v[200:203], a[80:83], a[96:99], v[200:203]\n"
"v_mfma_f32_16x16x32_bf16 v[200:203], a[84:87], a[100:103], v[200:203]\n"
"global_load_dwordx4 a[64:67], v0, s[34:35] offset:-4096 sc1\n"
"v_mfma_f32_16x16x32_bf16 v[228:231], a[92:95], a[100:103], v[228:231]\n"
"ds_read_b128 a[100:103], v3 offset:17408\n"
"v_mfma_f32_16x16x32_bf16 v[228:231], a[88:91], a[96:99], v[228:231]\n"
"ds_read_b128 a[96:99], v3 offset:16384\n"
"v_mfma_f32_16x16x32_bf16 v[232:235], a[88:91], a[104:107], v[232:235]\n"
"v_mfma_f32_16x16x32_bf16 v[232:235], a[92:95], a[108:111], v[232:235]\n"
"v_mfma_f32_16x16x32_bf16 v[204:207], a[84:87], a[108:111], v[204:207]\n"
"ds_read_b128 a[108:111], v3 offset:19456\n"
"v_mfma_f32_16x16x32_bf16 v[204:207], a[80:83], a[104:107], v[204:207]\n"
"s_add_u32 m0, s64, 4096\n"
"ds_read_b128 a[104:107], v3 offset:18432\n"
"v_mfma_f32_16x16x32_bf16 v[208:211], a[80:83], a[112:115], v[208:211]\n"
"v_mfma_f32_16x16x32_bf16 v[208:211], a[84:87], a[116:119], v[208:211]\n"
"global_load_dwordx4 a[68:71], v0, s[34:35] offset:-3072 sc1\n"
"v_mfma_f32_16x16x32_bf16 v[236:239], a[92:95], a[116:119], v[236:239]\n"
"ds_read_b128 a[116:119], v3 offset:21504\n"
"v_mfma_f32_16x16x32_bf16 v[236:239], a[88:91], a[112:115], v[236:239]\n"
"ds_read_b128 a[112:115], v3 offset:20480\n"
"v_mfma_f32_16x16x32_bf16 v[240:243], a[88:91], a[120:123], v[240:243]\n"
"v_mfma_f32_16x16x32_bf16 v[240:243], a[92:95], a[124:127], v[240:243]\n"
"v_mfma_f32_16x16x32_bf16 v[212:215], a[84:87], a[124:127], v[212:215]\n"
"ds_read_b128 a[124:127], v3 offset:23552\n"
"v_mfma_f32_16x16x32_bf16 v[212:215], a[80:83], a[120:123], v[212:215]\n"
"ds_read_b128 a[120:123], v3 offset:22528\n"
"v_mfma_f32_16x16x32_bf16 v[216:219], a[80:83], a[128:131], v[216:219]\n"
"v_mfma_f32_16x16x32_bf16 v[216:219], a[84:87], a[132:135], v[216:219]\n"
"global_load_dwordx4 a[72:75], v0, s[34:35] offset:-2048 sc1\n"
"v_mfma_f32_16x16x32_bf16 v[244:247], a[92:95], a[132:135], v[244:247]\n"
"ds_read_b128 a[132:135], v3 offset:25600\n"
"v_mfma_f32_16x16x32_bf16 v[244:247], a[88:91], a[128:131], v[244:247]\n"
"ds_read_b128 a[128:131], v3 offset:24576\n"
"v_mfma_f32_16x16x32_bf16 v[248:251], a[88:91], a[136:139], v[248:251]\n"
"v_mfma_f32_16x16x32_bf16 v[248:251], a[92:95], a[140:143], v[248:251]\n"
"v_mfma_f32_16x16x32_bf16 v[220:223], a[84:87], a[140:143], v[220:223]\n"
"ds_read_b128 a[140:143], v3 offset:27648\n"
"v_mfma_f32_16x16x32_bf16 v[220:223], a[80:83], a[136:139], v[220:223]\n"
"ds_read_b128 a[136:139], v3 offset:26624\n"
"v_mfma_f32_16x16x32_bf16 v[224:227], a[80:83], a[144:147], v[224:227]\n"
"buffer_load_dwordx4 v1, s[48:51], s75 offen offset:0 sc1 lds\n"
"v_mfma_f32_16x16x32_bf16 v[224:227], a[84:87], a[148:151], v[224:227]\n"
"global_load_dwordx4 a[76:79], v0, s[34:35] offset:-1024 sc1\n"
"v_mfma_f32_16x16x32_bf16 v[252:255], a[92:95], a[148:151], v[252:255]\n"
"ds_read_b128 a[148:151], v3 offset:29696\n"
"v_mfma_f32_16x16x32_bf16 v[252:255], a[88:91], a[144:147], v[252:255]\n"
"ds_read_b128 a[144:147], v3 offset:28672\n"
"v_mfma_f32_16x16x32_bf16 a[28:31], a[88:91], a[152:155], a[28:31]\n"
"v_mfma_f32_16x16x32_bf16 a[28:31], a[92:95], a[156:159], a[28:31]\n"
"v_mfma_f32_16x16x32_bf16 a[24:27], a[84:87], a[156:159], a[24:27]\n"
"ds_read_b128 a[156:159], v3 offset:31744\n"
"v_mfma_f32_16x16x32_bf16 a[24:27], a[80:83], a[152:155], a[24:27]\n"
"s_waitcnt vmcnt(11) lgkmcnt(11)\n"
"ds_read_b128 a[152:155], v3 offset:30720\n"
"v_mfma_f32_16x16x32_bf16 v[32:35], a[32:35], a[96:99], v[32:35]\n"
"v_mfma_f32_16x16x32_bf16 v[32:35], a[36:39], a[100:103], v[32:35]\n"
"global_load_dwordx4 a[80:83], v0, s[34:35] offset:0 sc1\n"
"v_mfma_f32_16x16x32_bf16 v[60:63], a[44:47], a[100:103], v[60:63]\n"
"v_mfma_f32_16x16x32_bf16 v[60:63], a[40:43], a[96:99], v[60:63]\n"
"v_mfma_f32_16x16x32_bf16 v[64:67], a[40:43], a[104:107], v[64:67]\n"
"v_mfma_f32_16x16x32_bf16 v[64:67], a[44:47], a[108:111], v[64:67]\n"
"s_add_u32 s65, s65, 1\n"
"v_mfma_f32_16x16x32_bf16 v[36:39], a[36:39], a[108:111], v[36:39]\n"
"buffer_load_dwordx4 v1, s[48:51], s75 offen offset:1024 sc1 lds\n"
"v_mfma_f32_16x16x32_bf16 v[36:39], a[32:35], a[104:107], v[36:39]\n"
"s_waitcnt lgkmcnt(8)\n"
"v_mov_b32 v8, s65\n"
"v_mfma_f32_16x16x32_bf16 v[40:43], a[32:35], a[112:115], v[40:43]\n"
"v_mov_b32 v9, s60\n"
"v_mfma_f32_16x16x32_bf16 v[40:43], a[36:39], a[116:119], v[40:43]\n"
"global_load_dwordx4 a[84:87], v0, s[34:35] offset:1024 sc1\n"
"v_mfma_f32_16x16x32_bf16 v[68:71], a[44:47], a[116:119], v[68:71]\n"
"buffer_store_dwordx2 v[8:9], off, s[56:59], %13 sc1\n"
"v_mfma_f32_16x16x32_bf16 v[68:71], a[40:43], a[112:115], v[68:71]\n"
"v_mfma_f32_16x16x32_bf16 v[72:75], a[40:43], a[120:123], v[72:75]\n"
"v_mfma_f32_16x16x32_bf16 v[72:75], a[44:47], a[124:127], v[72:75]\n"
"v_mfma_f32_16x16x32_bf16 v[44:47], a[36:39], a[124:127], v[44:47]\n"
"v_mfma_f32_16x16x32_bf16 v[44:47], a[32:35], a[120:123], v[44:47]\n"
"s_waitcnt lgkmcnt(4)\n"
"v_mfma_f32_16x16x32_bf16 v[48:51], a[32:35], a[128:131], v[48:51]\n"
"v_mfma_f32_16x16x32_bf16 v[48:51], a[36:39], a[132:135], v[48:51]\n"
"global_load_dwordx4 a[88:91], v0, s[34:35] offset:2048 sc1\n"
"v_mfma_f32_16x16x32_bf16 v[76:79], a[44:47], a[132:135], v[76:79]\n"
"v_mfma_f32_16x16x32_bf16 v[76:79], a[40:43], a[128:131], v[76:79]\n"
"v_mfma_f32_16x16x32_bf16 v[80:83], a[40:43], a[136:139], v[80:83]\n"
"v_mfma_f32_16x16x32_bf16 v[80:83], a[44:47], a[140:143], v[80:83]\n"
"v_mfma_f32_16x16x32_bf16 v[52:55], a[36:39], a[140:143], v[52:55]\n"
"buffer_load_dwordx4 v1, s[48:51], s75 offen offset:2048 sc1 lds\n"
"v_mfma_f32_16x16x32_bf16 v[52:55], a[32:35], a[136:139], v[52:55]\n"
"s_waitcnt lgkmcnt(0)\n"
"v_mfma_f32_16x16x32_bf16 v[56:59], a[32:35], a[144:147], v[56:59]\n"
"v_mfma_f32_16x16x32_bf16 v[56:59], a[36:39], a[148:151], v[56:59]\n"
"global_load_dwordx4 a[92:95], v0, s[34:35] offset:3072 sc1\n"
"v_mfma_f32_16x16x32_bf16 v[84:87], a[44:47], a[148:151], v[84:87]\n"
"v_mfma_f32_16x16x32_bf16 v[84:87], a[40:43], a[144:147], v[84:87]\n"
"v_mfma_f32_16x16x32_bf16 a[4:7], a[40:43], a[152:155], a[4:7]\n"
"s_add_u32 s34, s34, 8192\n"
"v_mfma_f32_16x16x32_bf16 a[4:7], a[44:47], a[156:159], a[4:7]\n"
"v_mfma_f32_16x16x32_bf16 a[0:3], a[36:39], a[156:159], a[0:3]\n"
"v_mfma_f32_16x16x32_bf16 a[0:3], a[32:35], a[152:155], a[0:3]\n"
"s_waitcnt vmcnt(12)\n"
"v_mfma_f32_16x16x32_bf16 v[88:91], a[48:51], a[96:99], v[88:91]\n"
"v_mfma_f32_16x16x32_bf16 v[88:91], a[52:55], a[100:103], v[88:91]\n"
"global_load_dwordx4 a[32:35], v0, s[34:35] offset:-4096 sc1\n"
"v_mfma_f32_16x16x32_bf16 v[116:119], a[60:63], a[100:103], v[116:119]\n"
"v_mfma_f32_16x16x32_bf16 v[116:119], a[56:59], a[96:99], v[116:119]\n"
"v_mfma_f32_16x16x32_bf16 v[120:123], a[56:59], a[104:107], v[120:123]\n"
"v_mfma_f32_16x16x32_bf16 v[120:123], a[60:63], a[108:111], v[120:123]\n"
"v_mfma_f32_16x16x32_bf16 v[92:95], a[52:55], a[108:111], v[92:95]\n"
"buffer_load_dwordx4 v1, s[48:51], s75 offen offset:3072 sc1 lds\n"
"v_mfma_f32_16x16x32_bf16 v[92:95], a[48:51], a[104:107], v[92:95]\n"
"v_mfma_f32_16x16x32_bf16 v[96:99], a[48:51], a[112:115], v[96:99]\n"
"v_mfma_f32_16x16x32_bf16 v[96:99], a[52:55], a[116:119], v[96:99]\n"
"global_load_dwordx4 a[36:39], v0, s[34:35] offset:-3072 sc1\n"
"v_mfma_f32_16x16x32_bf16 v[124:127], a[60:63], a[116:119], v[124:127]\n"
"v_mfma_f32_16x16x32_bf16 v[124:127], a[56:59], a[112:115], v[124:127]\n"
"v_mfma_f32_16x16x32_bf16 v[128:131], a[56:59], a[120:123], v[128:131]\n"
"v_mfma_f32_16x16x32_bf16 v[128:131], a[60:63], a[124:127], v[128:131]\n"
"v_mfma_f32_16x16x32_bf16 v[100:103], a[52:55], a[124:127], v[100:103]\n"
"v_mfma_f32_16x16x32_bf16 v[100:103], a[48:51], a[120:123], v[100:103]\n"
"v_mfma_f32_16x16x32_bf16 v[104:107], a[48:51], a[128:131], v[104:107]\n"
"v_mfma_f32_16x16x32_bf16 v[104:107], a[52:55], a[132:135], v[104:107]\n"
"global_load_dwordx4 a[40:43], v0, s[34:35] offset:-2048 sc1\n"
"v_mfma_f32_16x16x32_bf16 v[132:135], a[60:63], a[132:135], v[132:135]\n"
"v_mfma_f32_16x16x32_bf16 v[132:135], a[56:59], a[128:131], v[132:135]\n"
"v_mfma_f32_16x16x32_bf16 v[136:139], a[56:59], a[136:139], v[136:139]\n"
"v_mfma_f32_16x16x32_bf16 v[136:139], a[60:63], a[140:143], v[136:139]\n"
"s_add_u32 m0, s64, 8192\n"
"v_mfma_f32_16x16x32_bf16 v[108:111], a[52:55], a[140:143], v[108:111]\n"
"v_mfma_f32_16x16x32_bf16 v[108:111], a[48:51], a[136:139], v[108:111]\n"
"v_mfma_f32_16x16x32_bf16 v[112:115], a[48:51], a[144:147], v[112:115]\n"
"v_mfma_f32_16x16x32_bf16 v[112:115], a[52:55], a[148:151], v[112:115]\n"
"global_load_dwordx4 a[44:47], v0, s[34:35] offset:-1024 sc1\n"
"v_mfma_f32_16x16x32_bf16 v[140:143], a[60:63], a[148:151], v[140:143]\n"
"v_mfma_f32_16x16x32_bf16 v[140:143], a[56:59], a[144:147], v[140:143]\n"
"v_mfma_f32_16x16x32_bf16 a[12:15], a[56:59], a[152:155], a[12:15]\n"
"v_mfma_f32_16x16x32_bf16 a[12:15], a[60:63], a[156:159], a[12:15]\n"
"v_mfma_f32_16x16x32_bf16 a[8:11], a[52:55], a[156:159], a[8:11]\n"
"v_mfma_f32_16x16x32_bf16 a[8:11], a[48:51], a[152:155], a[8:11]\n"
"s_waitcnt vmcnt(12)\n"
"v_mfma_f32_16x16x32_bf16 v[144:147], a[64:67], a[96:99], v[144:147]\n"
"v_mfma_f32_16x16x32_bf16 v[144:147], a[68:71], a[100:103], v[144:147]\n"
"global_load_dwordx4 a[48:51], v0, s[34:35] offset:0 sc1\n"
"v_mfma_f32_16x16x32_bf16 v[172:175], a[76:79], a[100:103], v[172:175]\n"
"v_mfma_f32_16x16x32_bf16 v[172:175], a[72:75], a[96:99], v[172:175]\n"
"v_mfma_f32_16x16x32_bf16 v[176:179], a[72:75], a[104:107], v[176:179]\n"
"v_mfma_f32_16x16x32_bf16 v[176:179], a[76:79], a[108:111], v[176:179]\n"
"buffer_load_dwordx4 v0, s[48:51], s69 offen offset:0 sc1 lds\n"
"v_mfma_f32_16x16x32_bf16 v[148:151], a[68:71], a[108:111], v[148:151]\n"
"v_mfma_f32_16x16x32_bf16 v[148:151], a[64:67], a[104:107], v[148:151]\n"
"v_mfma_f32_16x16x32_bf16 v[152:155], a[64:67], a[112:115], v[152:155]\n"
"v_mfma_f32_16x16x32_bf16 v[152:155], a[68:71], a[116:119], v[152:155]\n"
"global_load_dwordx4 a[52:55], v0, s[34:35] offset:1024 sc1\n"
"v_mfma_f32_16x16x32_bf16 v[180:183], a[76:79], a[116:119], v[180:183]\n"
"v_mfma_f32_16x16x32_bf16 v[180:183], a[72:75], a[112:115], v[180:183]\n"
"v_mfma_f32_16x16x32_bf16 v[184:187], a[72:75], a[120:123], v[184:187]\n"
"v_mfma_f32_16x16x32_bf16 v[184:187], a[76:79], a[124:127], v[184:187]\n"
"v_mfma_f32_16x16x32_bf16 v[156:159], a[68:71], a[124:127], v[156:159]\n"
"v_mfma_f32_16x16x32_bf16 v[156:159], a[64:67], a[120:123], v[156:159]\n"
"v_mfma_f32_16x16x32_bf16 v[160:163], a[64:67], a[128:131], v[160:163]\n"
"v_mfma_f32_16x16x32_bf16 v[160:163], a[68:71], a[132:135], v[160:163]\n"
"global_load_dwordx4 a[56:59], v0, s[34:35] offset:2048 sc1\n"
"v_mfma_f32_16x16x32_bf16 v[188:191], a[76:79], a[132:135], v[188:191]\n"
"v_mfma_f32_16x16x32_bf16 v[188:191], a[72:75], a[128:131], v[188:191]\n"
"v_mfma_f32_16x16x32_bf16 v[192:195], a[72:75], a[136:139], v[192:195]\n"
"v_mfma_f32_16x16x32_bf16 v[192:195], a[76:79], a[140:143], v[192:195]\n"
"buffer_load_dwordx4 v0, s[48:51], s69 offen offset:1024 sc1 lds\n"
"v_mfma_f32_16x16x32_bf16 v[164:167], a[68:71], a[140:143], v[164:167]\n"
"v_mfma_f32_16x16x32_bf16 v[164:167], a[64:67], a[136:139], v[164:167]\n"
"v_mfma_f32_16x16x32_bf16 v[168:171], a[64:67], a[144:147], v[168:171]\n"
"v_mfma_f32_16x16x32_bf16 v[168:171], a[68:71], a[148:151], v[168:171]\n"
"global_load_dwordx4 a[60:63], v0, s[34:35] offset:3072 sc1\n"
"v_mfma_f32_16x16x32_bf16 v[196:199], a[76:79], a[148:151], v[196:199]\n"
"v_mfma_f32_16x16x32_bf16 v[196:199], a[72:75], a[144:147], v[196:199]\n"
"v_mfma_f32_16x16x32_bf16 a[20:23], a[72:75], a[152:155], a[20:23]\n"
"s_add_u32 s34, s34, 8192\n"
"v_mfma_f32_16x16x32_bf16 a[20:23], a[76:79], a[156:159], a[20:23]\n"
"v_mfma_f32_16x16x32_bf16 a[16:19], a[68:71], a[156:159], a[16:19]\n"
"v_mfma_f32_16x16x32_bf16 a[16:19], a[64:67], a[152:155], a[16:19]\n"
"s_waitcnt vmcnt(11)\n"
"v_mfma_f32_16x16x32_bf16 v[200:203], a[80:83], a[96:99], v[200:203]\n"
"v_mfma_f32_16x16x32_bf16 v[200:203], a[84:87], a[100:103], v[200:203]\n"
"global_load_dwordx4 a[64:67], v0, s[34:35] offset:-4096 sc1\n"
"v_mfma_f32_16x16x32_bf16 v[228:231], a[92:95], a[100:103], v[228:231]\n"
"ds_read_b128 a[100:103], v4 offset:17408\n"
"v_mfma_f32_16x16x32_bf16 v[228:231], a[88:91], a[96:99], v[228:231]\n"
"ds_read_b128 a[96:99], v4 offset:16384\n"
"v_mfma_f32_16x16x32_bf16 v[232:235], a[88:91], a[104:107], v[232:235]\n"
"buffer_load_dwordx4 v0, s[48:51], s69 offen offset:2048 sc1 lds\n"
"v_mfma_f32_16x16x32_bf16 v[232:235], a[92:95], a[108:111], v[232:235]\n"
"v_mfma_f32_16x16x32_bf16 v[204:207], a[84:87], a[108:111], v[204:207]\n"
"ds_read_b128 a[108:111], v4 offset:19456\n"
"v_mfma_f32_16x16x32_bf16 v[204:207], a[80:83], a[104:107], v[204:207]\n"
"ds_read_b128 a[104:107], v4 offset:18432\n"
"v_mfma_f32_16x16x32_bf16 v[208:211], a[80:83], a[112:115], v[208:211]\n"
"v_mfma_f32_16x16x32_bf16 v[208:211], a[84:87], a[116:119], v[208:211]\n"
"global_load_dwordx4 a[68:71], v0, s[34:35] offset:-3072 sc1\n"
"v_mfma_f32_16x16x32_bf16 v[236:239], a[92:95], a[116:119], v[236:239]\n"
"ds_read_b128 a[116:119], v4 offset:21504\n"
"v_mfma_f32_16x16x32_bf16 v[236:239], a[88:91], a[112:115], v[236:239]\n"
"ds_read_b128 a[112:115], v4 offset:20480\n"
"v_mfma_f32_16x16x32_bf16 v[240:243], a[88:91], a[120:123], v[240:243]\n"
"v_mfma_f32_16x16x32_bf16 v[240:243], a[92:95], a[124:127], v[240:243]\n"
"v_mfma_f32_16x16x32_bf16 v[212:215], a[84:87], a[124:127], v[212:215]\n"
"ds_read_b128 a[124:127], v4 offset:23552\n"
"v_mfma_f32_16x16x32_bf16 v[212:215], a[80:83], a[120:123], v[212:215]\n"
"ds_read_b128 a[120:123], v4 offset:22528\n"
"v_mfma_f32_16x16x32_bf16 v[216:219], a[80:83], a[128:131], v[216:219]\n"
"v_mfma_f32_16x16x32_bf16 v[216:219], a[84:87], a[132:135], v[216:219]\n"
"global_load_dwordx4 a[72:75], v0, s[34:35] offset:-2048 sc1\n"
"v_mfma_f32_16x16x32_bf16 v[244:247], a[92:95], a[132:135], v[244:247]\n"
"ds_read_b128 a[132:135], v4 offset:25600\n"
"v_mfma_f32_16x16x32_bf16 v[244:247], a[88:91], a[128:131], v[244:247]\n"
"ds_read_b128 a[128:131], v4 offset:24576\n"
"v_mfma_f32_16x16x32_bf16 v[248:251], a[88:91], a[136:139], v[248:251]\n"
"buffer_load_dwordx4 v0, s[48:51], s69 offen offset:3072 sc1 lds\n"
"v_mfma_f32_16x16x32_bf16 v[248:251], a[92:95], a[140:143], v[248:251]\n"
"v_mfma_f32_16x16x32_bf16 v[220:223], a[84:87], a[140:143], v[220:223]\n"
"ds_read_b128 a[140:143], v4 offset:27648\n"
"v_mfma_f32_16x16x32_bf16 v[220:223], a[80:83], a[136:139], v[220:223]\n"
"ds_read_b128 a[136:139], v4 offset:26624\n"
"v_mfma_f32_16x16x32_bf16 v[224:227], a[80:83], a[144:147], v[224:227]\n"
"v_mfma_f32_16x16x32_bf16 v[224:227], a[84:87], a[148:151], v[224:227]\n"
"global_load_dwordx4 a[76:79], v0, s[34:35] offset:-1024 sc1\n"
"v_mfma_f32_16x16x32_bf16 v[252:255], a[92:95], a[148:151], v[252:255]\n"
"ds_read_b128 a[148:151], v4 offset:29696\n"
"v_mfma_f32_16x16x32_bf16 v[252:255], a[88:91], a[144:147], v[252:255]\n"
"ds_read_b128 a[144:147], v4 offset:28672\n"
"v_mfma_f32_16x16x32_bf16 a[28:31], a[88:91], a[152:155], a[28:31]\n"
"v_mfma_f32_16x16x32_bf16 a[28:31], a[92:95], a[156:159], a[28:31]\n"
"v_mfma_f32_16x16x32_bf16 a[24:27], a[84:87], a[156:159], a[24:27]\n"
"ds_read_b128 a[156:159], v4 offset:31744\n"
"v_mfma_f32_16x16x32_bf16 a[24:27], a[80:83], a[152:155], a[24:27]\n"
"s_waitcnt vmcnt(12) lgkmcnt(11)\n"
"ds_read_b128 a[152:155], v4 offset:30720\n"
"v_mfma_f32_16x16x32_bf16 v[32:35], a[32:35], a[96:99], v[32:35]\n"
"v_mfma_f32_16x16x32_bf16 v[32:35], a[36:39], a[100:103], v[32:35]\n"
"global_load_dwordx4 a[80:83], v0, s[34:35] offset:0 sc1\n"
"v_mfma_f32_16x16x32_bf16 v[60:63], a[44:47], a[100:103], v[60:63]\n"
"v_mfma_f32_16x16x32_bf16 v[60:63], a[40:43], a[96:99], v[60:63]\n"
"v_mfma_f32_16x16x32_bf16 v[64:67], a[40:43], a[104:107], v[64:67]\n"
"s_add_u32 m0, s64, 12288\n"
"v_mfma_f32_16x16x32_bf16 v[64:67], a[44:47], a[108:111], v[64:67]\n"
"v_mfma_f32_16x16x32_bf16 v[36:39], a[36:39], a[108:111], v[36:39]\n"
"v_mfma_f32_16x16x32_bf16 v[36:39], a[32:35], a[104:107], v[36:39]\n"
"s_waitcnt lgkmcnt(8)\n"
"v_mfma_f32_16x16x32_bf16 v[40:43], a[32:35], a[112:115], v[40:43]\n"
"v_mfma_f32_16x16x32_bf16 v[40:43], a[36:39], a[116:119], v[40:43]\n"
"global_load_dwordx4 a[84:87], v0, s[34:35] offset:1024 sc1\n"
"v_mfma_f32_16x16x32_bf16 v[68:71], a[44:47], a[116:119], v[68:71]\n"
"v_mfma_f32_16x16x32_bf16 v[68:71], a[40:43], a[112:115], v[68:71]\n"
"v_mfma_f32_16x16x32_bf16 v[72:75], a[40:43], a[120:123], v[72:75]\n"
"v_mfma_f32_16x16x32_bf16 v[72:75], a[44:47], a[124:127], v[72:75]\n"
"v_mfma_f32_16x16x32_bf16 v[44:47], a[36:39], a[124:127], v[44:47]\n"
"v_mfma_f32_16x16x32_bf16 v[44:47], a[32:35], a[120:123], v[44:47]\n"
"s_waitcnt lgkmcnt(4)\n"
"v_mfma_f32_16x16x32_bf16 v[48:51], a[32:35], a[128:131], v[48:51]\n"
"v_mfma_f32_16x16x32_bf16 v[48:51], a[36:39], a[132:135], v[48:51]\n"
"global_load_dwordx4 a[88:91], v0, s[34:35] offset:2048 sc1\n"
"v_mfma_f32_16x16x32_bf16 v[76:79], a[44:47], a[132:135], v[76:79]\n"
"v_mfma_f32_16x16x32_bf16 v[76:79], a[40:43], a[128:131], v[76:79]\n"
"v_mfma_f32_16x16x32_bf16 v[80:83], a[40:43], a[136:139], v[80:83]\n"
"buffer_load_dwordx4 v1, s[48:51], s69 offen offset:0 sc1 lds\n"
"v_mfma_f32_16x16x32_bf16 v[80:83], a[44:47], a[140:143], v[80:83]\n"
"v_mfma_f32_16x16x32_bf16 v[52:55], a[36:39], a[140:143], v[52:55]\n"
"v_mfma_f32_16x16x32_bf16 v[52:55], a[32:35], a[136:139], v[52:55]\n"
"s_waitcnt lgkmcnt(0)\n"
"v_mfma_f32_16x16x32_bf16 v[56:59], a[32:35], a[144:147], v[56:59]\n"
"v_mfma_f32_16x16x32_bf16 v[56:59], a[36:39], a[148:151], v[56:59]\n"
"global_load_dwordx4 a[92:95], v0, s[34:35] offset:3072 sc1\n"
"v_mfma_f32_16x16x32_bf16 v[84:87], a[44:47], a[148:151], v[84:87]\n"
"v_mfma_f32_16x16x32_bf16 v[84:87], a[40:43], a[144:147], v[84:87]\n"
"v_mfma_f32_16x16x32_bf16 a[4:7], a[40:43], a[152:155], a[4:7]\n"
"s_add_u32 s34, s34, 8192\n"
"v_mfma_f32_16x16x32_bf16 a[4:7], a[44:47], a[156:159], a[4:7]\n"
"v_mfma_f32_16x16x32_bf16 a[0:3], a[36:39], a[156:159], a[0:3]\n"
"v_mfma_f32_16x16x32_bf16 a[0:3], a[32:35], a[152:155], a[0:3]\n"
"s_waitcnt vmcnt(11)\n"
"v_mfma_f32_16x16x32_bf16 v[88:91], a[48:51], a[96:99], v[88:91]\n"
"v_mfma_f32_16x16x32_bf16 v[88:91], a[52:55], a[100:103], v[88:91]\n"
"global_load_dwordx4 a[32:35], v0, s[34:35] offset:-4096 sc1\n"
"v_mfma_f32_16x16x32_bf16 v[116:119], a[60:63], a[100:103], v[116:119]\n"
"v_mfma_f32_16x16x32_bf16 v[116:119], a[56:59], a[96:99], v[116:119]\n"
"buffer_load_dwordx4 v1, s[48:51], s69 offen offset:1024 sc1 lds\n"
"v_mfma_f32_16x16x32_bf16 v[120:123], a[56:59], a[104:107], v[120:123]\n"
"v_mfma_f32_16x16x32_bf16 v[120:123], a[60:63], a[108:111], v[120:123]\n"
"v_mfma_f32_16x16x32_bf16 v[92:95], a[52:55], a[108:111], v[92:95]\n"
"v_mfma_f32_16x16x32_bf16 v[92:95], a[48:51], a[104:107], v[92:95]\n"
"v_mfma_f32_16x16x32_bf16 v[96:99], a[48:51], a[112:115], v[96:99]\n"
"v_mfma_f32_16x16x32_bf16 v[96:99], a[52:55], a[116:119], v[96:99]\n"
"global_load_dwordx4 a[36:39], v0, s[34:35] offset:-3072 sc1\n"
"v_mfma_f32_16x16x32_bf16 v[124:127], a[60:63], a[116:119], v[124:127]\n"
"v_mfma_f32_16x16x32_bf16 v[124:127], a[56:59], a[112:115], v[124:127]\n"
"v_mfma_f32_16x16x32_bf16 v[128:131], a[56:59], a[120:123], v[128:131]\n"
"v_mfma_f32_16x16x32_bf16 v[128:131], a[60:63], a[124:127], v[128:131]\n"
"v_mfma_f32_16x16x32_bf16 v[100:103], a[52:55], a[124:127], v[100:103]\n"
"v_mfma_f32_16x16x32_bf16 v[100:103], a[48:51], a[120:123], v[100:103]\n"
"v_mfma_f32_16x16x32_bf16 v[104:107], a[48:51], a[128:131], v[104:107]\n"
"v_mfma_f32_16x16x32_bf16 v[104:107], a[52:55], a[132:135], v[104:107]\n"
"global_load_dwordx4 a[40:43], v0, s[34:35] offset:-2048 sc1\n"
"v_mfma_f32_16x16x32_bf16 v[132:135], a[60:63], a[132:135], v[132:135]\n"
"v_mfma_f32_16x16x32_bf16 v[132:135], a[56:59], a[128:131], v[132:135]\n"
"buffer_load_dwordx4 v1, s[48:51], s69 offen offset:2048 sc1 lds\n"
"v_mfma_f32_16x16x32_bf16 v[136:139], a[56:59], a[136:139], v[136:139]\n"
"v_mfma_f32_16x16x32_bf16 v[136:139], a[60:63], a[140:143], v[136:139]\n"
"v_mfma_f32_16x16x32_bf16 v[108:111], a[52:55], a[140:143], v[108:111]\n"
"v_mfma_f32_16x16x32_bf16 v[108:111], a[48:51], a[136:139], v[108:111]\n"
"v_mfma_f32_16x16x32_bf16 v[112:115], a[48:51], a[144:147], v[112:115]\n"
"v_mfma_f32_16x16x32_bf16 v[112:115], a[52:55], a[148:151], v[112:115]\n"
"global_load_dwordx4 a[44:47], v0, s[34:35] offset:-1024 sc1\n"
"v_mfma_f32_16x16x32_bf16 v[140:143], a[60:63], a[148:151], v[140:143]\n"
"v_mfma_f32_16x16x32_bf16 v[140:143], a[56:59], a[144:147], v[140:143]\n"
"v_mfma_f32_16x16x32_bf16 a[12:15], a[56:59], a[152:155], a[12:15]\n"
"v_mfma_f32_16x16x32_bf16 a[12:15], a[60:63], a[156:159], a[12:15]\n"
"v_mfma_f32_16x16x32_bf16 a[8:11], a[52:55], a[156:159], a[8:11]\n"
"v_mfma_f32_16x16x32_bf16 a[8:11], a[48:51], a[152:155], a[8:11]\n"
"s_waitcnt vmcnt(11)\n"
"v_mfma_f32_16x16x32_bf16 v[144:147], a[64:67], a[96:99], v[144:147]\n"
"v_mfma_f32_16x16x32_bf16 v[144:147], a[68:71], a[100:103], v[144:147]\n"
"global_load_dwordx4 a[48:51], v0, s[34:35] offset:0 sc1\n"
"v_mfma_f32_16x16x32_bf16 v[172:175], a[76:79], a[100:103], v[172:175]\n"
"v_mfma_f32_16x16x32_bf16 v[172:175], a[72:75], a[96:99], v[172:175]\n"
"buffer_load_dwordx4 v1, s[48:51], s69 offen offset:3072 sc1 lds\n"
"v_mfma_f32_16x16x32_bf16 v[176:179], a[72:75], a[104:107], v[176:179]\n"
"v_mfma_f32_16x16x32_bf16 v[176:179], a[76:79], a[108:111], v[176:179]\n"
"v_mfma_f32_16x16x32_bf16 v[148:151], a[68:71], a[108:111], v[148:151]\n"
"v_mfma_f32_16x16x32_bf16 v[148:151], a[64:67], a[104:107], v[148:151]\n"
"v_mfma_f32_16x16x32_bf16 v[152:155], a[64:67], a[112:115], v[152:155]\n"
"v_lshlrev_b32 v16, 16, v18\n"
"v_mfma_f32_16x16x32_bf16 v[152:155], a[68:71], a[116:119], v[152:155]\n"
"global_load_dwordx4 a[52:55], v0, s[34:35] offset:1024 sc1\n"
"v_and_b32 v17, s63, v18\n"
"v_mfma_f32_16x16x32_bf16 v[180:183], a[76:79], a[116:119], v[180:183]\n"
"v_lshlrev_b32 v18, 16, v19\n"
"v_mfma_f32_16x16x32_bf16 v[180:183], a[72:75], a[112:115], v[180:183]\n"
"v_and_b32 v19, s63, v19\n"
"v_mfma_f32_16x16x32_bf16 v[184:187], a[72:75], a[120:123], v[184:187]\n"
"v_add_f32 v32, v32, v16\n"
"v_mfma_f32_16x16x32_bf16 v[184:187], a[76:79], a[124:127], v[184:187]\n"
"v_add_f32 v33, v33, v17\n"
"v_add_f32 v34, v34, v18\n"
"v_mfma_f32_16x16x32_bf16 v[156:159], a[68:71], a[124:127], v[156:159]\n"
"v_add_f32 v35, v35, v19\n"
"v_mfma_f32_16x16x32_bf16 v[156:159], a[64:67], a[120:123], v[156:159]\n"
"v_add_f32 v36, v36, v16\n"
"v_mfma_f32_16x16x32_bf16 v[160:163], a[64:67], a[128:131], v[160:163]\n"
"v_add_f32 v37, v37, v17\n"
"v_mfma_f32_16x16x32_bf16 v[160:163], a[68:71], a[132:135], v[160:163]\n"
"global_load_dwordx4 a[56:59], v0, s[34:35] offset:2048 sc1\n"
"v_add_f32 v38, v38, v18\n"
"v_mfma_f32_16x16x32_bf16 v[188:191], a[76:79], a[132:135], v[188:191]\n"
"v_add_f32 v39, v39, v19\n"
"v_mfma_f32_16x16x32_bf16 v[188:191], a[72:75], a[128:131], v[188:191]\n"
"v_add_f32 v40, v40, v16\n"
"v_mfma_f32_16x16x32_bf16 v[192:195], a[72:75], a[136:139], v[192:195]\n"
"v_add_f32 v41, v41, v17\n"
"v_mfma_f32_16x16x32_bf16 v[192:195], a[76:79], a[140:143], v[192:195]\n"
"v_add_f32 v42, v42, v18\n"
"v_mfma_f32_16x16x32_bf16 v[164:167], a[68:71], a[140:143], v[164:167]\n"
"v_add_f32 v43, v43, v19\n"
"v_mfma_f32_16x16x32_bf16 v[164:167], a[64:67], a[136:139], v[164:167]\n"
"v_add_f32 v44, v44, v16\n"
"v_add_f32 v45, v45, v17\n"
"v_mfma_f32_16x16x32_bf16 v[168:171], a[64:67], a[144:147], v[168:171]\n"
"v_add_f32 v46, v46, v18\n"
"v_mfma_f32_16x16x32_bf16 v[168:171], a[68:71], a[148:151], v[168:171]\n"
"global_load_dwordx4 a[60:63], v0, s[34:35] offset:3072 sc1\n"
"v_add_f32 v47, v47, v19\n"
"v_mfma_f32_16x16x32_bf16 v[196:199], a[76:79], a[148:151], v[196:199]\n"
"v_add_f32 v48, v48, v16\n"
"v_mfma_f32_16x16x32_bf16 v[196:199], a[72:75], a[144:147], v[196:199]\n"
"v_add_f32 v49, v49, v17\n"
"v_mfma_f32_16x16x32_bf16 a[20:23], a[72:75], a[152:155], a[20:23]\n"
"s_add_u32 s34, s34, 8192\n"
"v_add_f32 v50, v50, v18\n"
"v_mfma_f32_16x16x32_bf16 a[20:23], a[76:79], a[156:159], a[20:23]\n"
"v_add_f32 v51, v51, v19\n"
"v_mfma_f32_16x16x32_bf16 a[16:19], a[68:71], a[156:159], a[16:19]\n"
"v_add_f32 v52, v52, v16\n"
"v_mfma_f32_16x16x32_bf16 a[16:19], a[64:67], a[152:155], a[16:19]\n"
"s_waitcnt vmcnt(11)\n"
"v_add_f32 v53, v53, v17\n"
"v_mfma_f32_16x16x32_bf16 v[200:203], a[80:83], a[96:99], v[200:203]\n"
"v_add_f32 v54, v54, v18\n"
"v_add_f32 v55, v55, v19\n"
"v_mfma_f32_16x16x32_bf16 v[200:203], a[84:87], a[100:103], v[200:203]\n"
"global_load_dwordx4 a[64:67], v0, s[34:35] offset:-4096 sc1\n"
"v_add_f32 v56, v56, v16\n"
"v_mfma_f32_16x16x32_bf16 v[228:231], a[92:95], a[100:103], v[228:231]\n"
"ds_read_b128 a[100:103], v5 offset:17408\n"
"v_add_f32 v57, v57, v17\n"
"v_mfma_f32_16x16x32_bf16 v[228:231], a[88:91], a[96:99], v[228:231]\n"
"ds_read_b128 a[96:99], v5 offset:16384\n"
"v_add_f32 v58, v58, v18\n"
"v_mfma_f32_16x16x32_bf16 v[232:235], a[88:91], a[104:107], v[232:235]\n"
"v_add_f32 v59, v59, v19\n"
"v_mfma_f32_16x16x32_bf16 v[232:235], a[92:95], a[108:111], v[232:235]\n"
"v_accvgpr_read_b32 v12, a0\n"
"v_mfma_f32_16x16x32_bf16 v[204:207], a[84:87], a[108:111], v[204:207]\n"
"ds_read_b128 a[108:111], v5 offset:19456\n"
"v_accvgpr_read_b32 v13, a1\n"
"v_mfma_f32_16x16x32_bf16 v[204:207], a[80:83], a[104:107], v[204:207]\n"
"ds_read_b128 a[104:107], v5 offset:18432\n"
"v_accvgpr_read_b32 v14, a2\n"
"v_mfma_f32_16x16x32_bf16 v[208:211], a[80:83], a[112:115], v[208:211]\n"
"v_accvgpr_read_b32 v15, a3\n"
"v_mfma_f32_16x16x32_bf16 v[208:211], a[84:87], a[116:119], v[208:211]\n"
"global_load_dwordx4 a[68:71], v0, s[34:35] offset:-3072 sc1\n"
"v_add_f32 v12, v12, v16\n"
"v_mfma_f32_16x16x32_bf16 v[236:239], a[92:95], a[116:119], v[236:239]\n"
"ds_read_b128 a[116:119], v5 offset:21504\n"
"v_add_f32 v13, v13, v17\n"
"v_mfma_f32_16x16x32_bf16 v[236:239], a[88:91], a[112:115], v[236:239]\n"
"ds_read_b128 a[112:115], v5 offset:20480\n"
"v_add_f32 v14, v14, v18\n"
"v_mfma_f32_16x16x32_bf16 v[240:243], a[88:91], a[120:123], v[240:243]\n"
"v_add_f32 v15, v15, v19\n"
"v_accvgpr_write_b32 a0, v12\n"
"v_mfma_f32_16x16x32_bf16 v[240:243], a[92:95], a[124:127], v[240:243]\n"
"v_accvgpr_write_b32 a1, v13\n"
"v_mfma_f32_16x16x32_bf16 v[212:215], a[84:87], a[124:127], v[212:215]\n"
"ds_read_b128 a[124:127], v5 offset:23552\n"
"v_accvgpr_write_b32 a2, v14\n"
"v_mfma_f32_16x16x32_bf16 v[212:215], a[80:83], a[120:123], v[212:215]\n"
"ds_read_b128 a[120:123], v5 offset:22528\n"
"v_accvgpr_write_b32 a3, v15\n"
"v_mfma_f32_16x16x32_bf16 v[216:219], a[80:83], a[128:131], v[216:219]\n"
"v_mfma_f32_16x16x32_bf16 v[216:219], a[84:87], a[132:135], v[216:219]\n"
"global_load_dwordx4 a[72:75], v0, s[34:35] offset:-2048 sc1\n"
"v_lshrrev_b32 v16, 4, v0\n"
"v_mfma_f32_16x16x32_bf16 v[244:247], a[92:95], a[132:135], v[244:247]\n"
"ds_read_b128 a[132:135], v5 offset:25600\n"
"v_and_b32 v16, 0x30, v16\n"
"v_mfma_f32_16x16x32_bf16 v[244:247], a[88:91], a[128:131], v[244:247]\n"
"ds_read_b128 a[128:131], v5 offset:24576\n"
"buffer_load_dwordx2 v[18:19], v16, s[44:47], s61 offen offset:64\n"
"v_mfma_f32_16x16x32_bf16 v[248:251], a[88:91], a[136:139], v[248:251]\n"
"v_mfma_f32_16x16x32_bf16 v[248:251], a[92:95], a[140:143], v[248:251]\n"
"v_mfma_f32_16x16x32_bf16 v[220:223], a[84:87], a[140:143], v[220:223]\n"
"ds_read_b128 a[140:143], v5 offset:27648\n"
"v_mfma_f32_16x16x32_bf16 v[220:223], a[80:83], a[136:139], v[220:223]\n"
"ds_read_b128 a[136:139], v5 offset:26624\n"
"v_mfma_f32_16x16x32_bf16 v[224:227], a[80:83], a[144:147], v[224:227]\n"
"v_mfma_f32_16x16x32_bf16 v[224:227], a[84:87], a[148:151], v[224:227]\n"
"global_load_dwordx4 a[76:79], v0, s[34:35] offset:-1024 sc1\n"
"v_mfma_f32_16x16x32_bf16 v[252:255], a[92:95], a[148:151], v[252:255]\n"
"ds_read_b128 a[148:151], v5 offset:29696\n"
"v_mfma_f32_16x16x32_bf16 v[252:255], a[88:91], a[144:147], v[252:255]\n"
"ds_read_b128 a[144:147], v5 offset:28672\n"
"v_mfma_f32_16x16x32_bf16 a[28:31], a[88:91], a[152:155], a[28:31]\n"
"v_mfma_f32_16x16x32_bf16 a[28:31], a[92:95], a[156:159], a[28:31]\n"
"v_mfma_f32_16x16x32_bf16 a[24:27], a[84:87], a[156:159], a[24:27]\n"
"ds_read_b128 a[156:159], v5 offset:31744\n"
"buffer_load_dwordx2 v[8:9], off, s[56:59], %14 sc1\n"
"v_mfma_f32_16x16x32_bf16 a[24:27], a[80:83], a[152:155], a[24:27]\n"
"s_waitcnt vmcnt(11) lgkmcnt(11)\n"
"ds_read_b128 a[152:155], v5 offset:30720\n"
"v_mfma_f32_16x16x32_bf16 v[32:35], a[32:35], a[96:99], v[32:35]\n"
"v_mfma_f32_16x16x32_bf16 v[32:35], a[36:39], a[100:103], v[32:35]\n"
"global_load_dwordx4 a[80:83], v0, s[34:35] offset:0 sc1\n"
"v_mfma_f32_16x16x32_bf16 v[60:63], a[44:47], a[100:103], v[60:63]\n"
"v_mfma_f32_16x16x32_bf16 v[60:63], a[40:43], a[96:99], v[60:63]\n"
"v_mfma_f32_16x16x32_bf16 v[64:67], a[40:43], a[104:107], v[64:67]\n"
"v_mfma_f32_16x16x32_bf16 v[64:67], a[44:47], a[108:111], v[64:67]\n"
"v_mfma_f32_16x16x32_bf16 v[36:39], a[36:39], a[108:111], v[36:39]\n"
"v_mfma_f32_16x16x32_bf16 v[36:39], a[32:35], a[104:107], v[36:39]\n"
"s_waitcnt lgkmcnt(8)\n"
"v_mfma_f32_16x16x32_bf16 v[40:43], a[32:35], a[112:115], v[40:43]\n"
"v_mfma_f32_16x16x32_bf16 v[40:43], a[36:39], a[116:119], v[40:43]\n"
"global_load_dwordx4 a[84:87], v0, s[34:35] offset:1024 sc1\n"
"v_mfma_f32_16x16x32_bf16 v[68:71], a[44:47], a[116:119], v[68:71]\n"
"v_mfma_f32_16x16x32_bf16 v[68:71], a[40:43], a[112:115], v[68:71]\n"
"v_mfma_f32_16x16x32_bf16 v[72:75], a[40:43], a[120:123], v[72:75]\n"
"v_mfma_f32_16x16x32_bf16 v[72:75], a[44:47], a[124:127], v[72:75]\n"
"v_mfma_f32_16x16x32_bf16 v[44:47], a[36:39], a[124:127], v[44:47]\n"
"v_mfma_f32_16x16x32_bf16 v[44:47], a[32:35], a[120:123], v[44:47]\n"
"s_waitcnt lgkmcnt(4)\n"
"v_mfma_f32_16x16x32_bf16 v[48:51], a[32:35], a[128:131], v[48:51]\n"
"v_mfma_f32_16x16x32_bf16 v[48:51], a[36:39], a[132:135], v[48:51]\n"
"global_load_dwordx4 a[88:91], v0, s[34:35] offset:2048 sc1\n"
"v_mfma_f32_16x16x32_bf16 v[76:79], a[44:47], a[132:135], v[76:79]\n"
"v_mfma_f32_16x16x32_bf16 v[76:79], a[40:43], a[128:131], v[76:79]\n"
"v_mfma_f32_16x16x32_bf16 v[80:83], a[40:43], a[136:139], v[80:83]\n"
"v_mfma_f32_16x16x32_bf16 v[80:83], a[44:47], a[140:143], v[80:83]\n"
"v_mfma_f32_16x16x32_bf16 v[52:55], a[36:39], a[140:143], v[52:55]\n"
"v_mfma_f32_16x16x32_bf16 v[52:55], a[32:35], a[136:139], v[52:55]\n"
"s_waitcnt lgkmcnt(0)\n"
"v_mfma_f32_16x16x32_bf16 v[56:59], a[32:35], a[144:147], v[56:59]\n"
"v_mfma_f32_16x16x32_bf16 v[56:59], a[36:39], a[148:151], v[56:59]\n"
"global_load_dwordx4 a[92:95], v0, s[34:35] offset:3072 sc1\n"
"v_mfma_f32_16x16x32_bf16 v[84:87], a[44:47], a[148:151], v[84:87]\n"
"v_mfma_f32_16x16x32_bf16 v[84:87], a[40:43], a[144:147], v[84:87]\n"
"v_mfma_f32_16x16x32_bf16 a[4:7], a[40:43], a[152:155], a[4:7]\n"
"s_add_u32 s34, s34, 8192\n"
"v_mfma_f32_16x16x32_bf16 a[4:7], a[44:47], a[156:159], a[4:7]\n"
"v_mfma_f32_16x16x32_bf16 a[0:3], a[36:39], a[156:159], a[0:3]\n"
"v_mfma_f32_16x16x32_bf16 a[0:3], a[32:35], a[152:155], a[0:3]\n"
"s_waitcnt vmcnt(10)\n"
"v_mfma_f32_16x16x32_bf16 v[88:91], a[48:51], a[96:99], v[88:91]\n"
"v_mfma_f32_16x16x32_bf16 v[88:91], a[52:55], a[100:103], v[88:91]\n"
"global_load_dwordx4 a[32:35], v0, s[34:35] offset:-4096 sc1\n"
"v_mfma_f32_16x16x32_bf16 v[116:119], a[60:63], a[100:103], v[116:119]\n"
"v_mfma_f32_16x16x32_bf16 v[116:119], a[56:59], a[96:99], v[116:119]\n"
"v_mfma_f32_16x16x32_bf16 v[120:123], a[56:59], a[104:107], v[120:123]\n"
"v_mfma_f32_16x16x32_bf16 v[120:123], a[60:63], a[108:111], v[120:123]\n"
"v_mfma_f32_16x16x32_bf16 v[92:95], a[52:55], a[108:111], v[92:95]\n"
"v_mfma_f32_16x16x32_bf16 v[92:95], a[48:51], a[104:107], v[92:95]\n"
"v_mfma_f32_16x16x32_bf16 v[96:99], a[48:51], a[112:115], v[96:99]\n"
"v_mfma_f32_16x16x32_bf16 v[96:99], a[52:55], a[116:119], v[96:99]\n"
"global_load_dwordx4 a[36:39], v0, s[34:35] offset:-3072 sc1\n"
"v_mfma_f32_16x16x32_bf16 v[124:127], a[60:63], a[116:119], v[124:127]\n"
"v_mfma_f32_16x16x32_bf16 v[124:127], a[56:59], a[112:115], v[124:127]\n"
"v_mfma_f32_16x16x32_bf16 v[128:131], a[56:59], a[120:123], v[128:131]\n"
"v_mfma_f32_16x16x32_bf16 v[128:131], a[60:63], a[124:127], v[128:131]\n"
"v_mfma_f32_16x16x32_bf16 v[100:103], a[52:55], a[124:127], v[100:103]\n"
"v_mfma_f32_16x16x32_bf16 v[100:103], a[48:51], a[120:123], v[100:103]\n"
"v_mfma_f32_16x16x32_bf16 v[104:107], a[48:51], a[128:131], v[104:107]\n"
"v_mfma_f32_16x16x32_bf16 v[104:107], a[52:55], a[132:135], v[104:107]\n"
"global_load_dwordx4 a[40:43], v0, s[34:35] offset:-2048 sc1\n"
"v_mfma_f32_16x16x32_bf16 v[132:135], a[60:63], a[132:135], v[132:135]\n"
"v_mfma_f32_16x16x32_bf16 v[132:135], a[56:59], a[128:131], v[132:135]\n"
"v_mfma_f32_16x16x32_bf16 v[136:139], a[56:59], a[136:139], v[136:139]\n"
"v_mfma_f32_16x16x32_bf16 v[136:139], a[60:63], a[140:143], v[136:139]\n"
"v_mfma_f32_16x16x32_bf16 v[108:111], a[52:55], a[140:143], v[108:111]\n"
"v_mfma_f32_16x16x32_bf16 v[108:111], a[48:51], a[136:139], v[108:111]\n"
"v_mfma_f32_16x16x32_bf16 v[112:115], a[48:51], a[144:147], v[112:115]\n"
"v_mfma_f32_16x16x32_bf16 v[112:115], a[52:55], a[148:151], v[112:115]\n"
"global_load_dwordx4 a[44:47], v0, s[34:35] offset:-1024 sc1\n"
"v_mfma_f32_16x16x32_bf16 v[140:143], a[60:63], a[148:151], v[140:143]\n"
"v_mfma_f32_16x16x32_bf16 v[140:143], a[56:59], a[144:147], v[140:143]\n"
"v_mfma_f32_16x16x32_bf16 a[12:15], a[56:59], a[152:155], a[12:15]\n"
"v_mfma_f32_16x16x32_bf16 a[12:15], a[60:63], a[156:159], a[12:15]\n"
"v_mfma_f32_16x16x32_bf16 a[8:11], a[52:55], a[156:159], a[8:11]\n"
"v_mfma_f32_16x16x32_bf16 a[8:11], a[48:51], a[152:155], a[8:11]\n"
"s_waitcnt vmcnt(9)\n"
"v_mfma_f32_16x16x32_bf16 v[144:147], a[64:67], a[96:99], v[144:147]\n"
"v_mfma_f32_16x16x32_bf16 v[144:147], a[68:71], a[100:103], v[144:147]\n"
"global_load_dwordx4 a[48:51], v0, s[34:35] offset:0 sc1\n"
"v_mfma_f32_16x16x32_bf16 v[172:175], a[76:79], a[100:103], v[172:175]\n"
"v_mfma_f32_16x16x32_bf16 v[172:175], a[72:75], a[96:99], v[172:175]\n"
"v_mfma_f32_16x16x32_bf16 v[176:179], a[72:75], a[104:107], v[176:179]\n"
"v_mfma_f32_16x16x32_bf16 v[176:179], a[76:79], a[108:111], v[176:179]\n"
"v_mfma_f32_16x16x32_bf16 v[148:151], a[68:71], a[108:111], v[148:151]\n"
"v_mfma_f32_16x16x32_bf16 v[148:151], a[64:67], a[104:107], v[148:151]\n"
"v_mfma_f32_16x16x32_bf16 v[152:155], a[64:67], a[112:115], v[152:155]\n"
"v_lshlrev_b32 v20, 16, v22\n"
"v_mfma_f32_16x16x32_bf16 v[152:155], a[68:71], a[116:119], v[152:155]\n"
"global_load_dwordx4 a[52:55], v0, s[34:35] offset:1024 sc1\n"
"v_and_b32 v21, s63, v22\n"
"v_mfma_f32_16x16x32_bf16 v[180:183], a[76:79], a[116:119], v[180:183]\n"
"v_lshlrev_b32 v22, 16, v23\n"
"v_mfma_f32_16x16x32_bf16 v[180:183], a[72:75], a[112:115], v[180:183]\n"
"v_and_b32 v23, s63, v23\n"
"v_mfma_f32_16x16x32_bf16 v[184:187], a[72:75], a[120:123], v[184:187]\n"
"v_add_f32 v60, v60, v20\n"
"v_mfma_f32_16x16x32_bf16 v[184:187], a[76:79], a[124:127], v[184:187]\n"
"v_add_f32 v61, v61, v21\n"
"v_add_f32 v62, v62, v22\n"
"v_mfma_f32_16x16x32_bf16 v[156:159], a[68:71], a[124:127], v[156:159]\n"
"v_add_f32 v63, v63, v23\n"
"v_mfma_f32_16x16x32_bf16 v[156:159], a[64:67], a[120:123], v[156:159]\n"
"v_add_f32 v64, v64, v20\n"
"v_mfma_f32_16x16x32_bf16 v[160:163], a[64:67], a[128:131], v[160:163]\n"
"v_add_f32 v65, v65, v21\n"
"v_mfma_f32_16x16x32_bf16 v[160:163], a[68:71], a[132:135], v[160:163]\n"
"global_load_dwordx4 a[56:59], v0, s[34:35] offset:2048 sc1\n"
"v_add_f32 v66, v66, v22\n"
"v_mfma_f32_16x16x32_bf16 v[188:191], a[76:79], a[132:135], v[188:191]\n"
"v_add_f32 v67, v67, v23\n"
"v_mfma_f32_16x16x32_bf16 v[188:191], a[72:75], a[128:131], v[188:191]\n"
"v_add_f32 v68, v68, v20\n"
"v_mfma_f32_16x16x32_bf16 v[192:195], a[72:75], a[136:139], v[192:195]\n"
"v_add_f32 v69, v69, v21\n"
"v_mfma_f32_16x16x32_bf16 v[192:195], a[76:79], a[140:143], v[192:195]\n"
"v_add_f32 v70, v70, v22\n"
"v_mfma_f32_16x16x32_bf16 v[164:167], a[68:71], a[140:143], v[164:167]\n"
"v_add_f32 v71, v71, v23\n"
"v_mfma_f32_16x16x32_bf16 v[164:167], a[64:67], a[136:139], v[164:167]\n"
"v_add_f32 v72, v72, v20\n"
"v_add_f32 v73, v73, v21\n"
"v_mfma_f32_16x16x32_bf16 v[168:171], a[64:67], a[144:147], v[168:171]\n"
"v_add_f32 v74, v74, v22\n"
"v_mfma_f32_16x16x32_bf16 v[168:171], a[68:71], a[148:151], v[168:171]\n"
"global_load_dwordx4 a[60:63], v0, s[34:35] offset:3072 sc1\n"
"v_add_f32 v75, v75, v23\n"
"v_mfma_f32_16x16x32_bf16 v[196:199], a[76:79], a[148:151], v[196:199]\n"
"v_add_f32 v76, v76, v20\n"
"v_mfma_f32_16x16x32_bf16 v[196:199], a[72:75], a[144:147], v[196:199]\n"
"v_add_f32 v77, v77, v21\n"
"v_mfma_f32_16x16x32_bf16 a[20:23], a[72:75], a[152:155], a[20:23]\n"
"s_add_u32 s34, s34, 8192\n"
"v_add_f32 v78, v78, v22\n"
"v_mfma_f32_16x16x32_bf16 a[20:23], a[76:79], a[156:159], a[20:23]\n"
"v_add_f32 v79, v79, v23\n"
"v_mfma_f32_16x16x32_bf16 a[16:19], a[68:71], a[156:159], a[16:19]\n"
"v_add_f32 v80, v80, v20\n"
"v_mfma_f32_16x16x32_bf16 a[16:19], a[64:67], a[152:155], a[16:19]\n"
"s_waitcnt vmcnt(8)\n"
"v_add_f32 v81, v81, v21\n"
"v_mfma_f32_16x16x32_bf16 v[200:203], a[80:83], a[96:99], v[200:203]\n"
"v_add_f32 v82, v82, v22\n"
"v_add_f32 v83, v83, v23\n"
"v_mfma_f32_16x16x32_bf16 v[200:203], a[84:87], a[100:103], v[200:203]\n"
"global_load_dwordx4 a[64:67], v0, s[34:35] offset:-4096 sc1\n"
"v_add_f32 v84, v84, v20\n"
"v_mfma_f32_16x16x32_bf16 v[228:231], a[92:95], a[100:103], v[228:231]\n"
"ds_read_b128 a[100:103], v2 offset:1024\n"
"v_add_f32 v85, v85, v21\n"
"v_mfma_f32_16x16x32_bf16 v[228:231], a[88:91], a[96:99], v[228:231]\n"
"ds_read_b128 a[96:99], v2 offset:0\n"
"v_add_f32 v86, v86, v22\n"
"v_mfma_f32_16x16x32_bf16 v[232:235], a[88:91], a[104:107], v[232:235]\n"
"v_add_f32 v87, v87, v23\n"
"v_mfma_f32_16x16x32_bf16 v[232:235], a[92:95], a[108:111], v[232:235]\n"
"v_accvgpr_read_b32 v12, a4\n"
"v_mfma_f32_16x16x32_bf16 v[204:207], a[84:87], a[108:111], v[204:207]\n"
"ds_read_b128 a[108:111], v2 offset:3072\n"
"v_accvgpr_read_b32 v13, a5\n"
"v_mfma_f32_16x16x32_bf16 v[204:207], a[80:83], a[104:107], v[204:207]\n"
"ds_read_b128 a[104:107], v2 offset:2048\n"
"v_accvgpr_read_b32 v14, a6\n"
"v_mfma_f32_16x16x32_bf16 v[208:211], a[80:83], a[112:115], v[208:211]\n"
"v_accvgpr_read_b32 v15, a7\n"
"v_mfma_f32_16x16x32_bf16 v[208:211], a[84:87], a[116:119], v[208:211]\n"
"global_load_dwordx4 a[68:71], v0, s[34:35] offset:-3072 sc1\n"
"v_add_f32 v12, v12, v20\n"
"v_mfma_f32_16x16x32_bf16 v[236:239], a[92:95], a[116:119], v[236:239]\n"
"ds_read_b128 a[116:119], v2 offset:5120\n"
"v_add_f32 v13, v13, v21\n"
"v_mfma_f32_16x16x32_bf16 v[236:239], a[88:91], a[112:115], v[236:239]\n"
"ds_read_b128 a[112:115], v2 offset:4096\n"
"v_add_f32 v14, v14, v22\n"
"v_mfma_f32_16x16x32_bf16 v[240:243], a[88:91], a[120:123], v[240:243]\n"
"v_add_f32 v15, v15, v23\n"
"v_accvgpr_write_b32 a4, v12\n"
"v_mfma_f32_16x16x32_bf16 v[240:243], a[92:95], a[124:127], v[240:243]\n"
"v_accvgpr_write_b32 a5, v13\n"
"v_mfma_f32_16x16x32_bf16 v[212:215], a[84:87], a[124:127], v[212:215]\n"
"ds_read_b128 a[124:127], v2 offset:7168\n"
"v_accvgpr_write_b32 a6, v14\n"
"v_mfma_f32_16x16x32_bf16 v[212:215], a[80:83], a[120:123], v[212:215]\n"
"ds_read_b128 a[120:123], v2 offset:6144\n"
"v_accvgpr_write_b32 a7, v15\n"
"v_mfma_f32_16x16x32_bf16 v[216:219], a[80:83], a[128:131], v[216:219]\n"
"v_mfma_f32_16x16x32_bf16 v[216:219], a[84:87], a[132:135], v[216:219]\n"
"global_load_dwordx4 a[72:75], v0, s[34:35] offset:-2048 sc1\n"
"v_lshrrev_b32 v20, 4, v0\n"
"v_mfma_f32_16x16x32_bf16 v[244:247], a[92:95], a[132:135], v[244:247]\n"
"ds_read_b128 a[132:135], v2 offset:9216\n"
"v_and_b32 v20, 0x30, v20\n"
"v_mfma_f32_16x16x32_bf16 v[244:247], a[88:91], a[128:131], v[244:247]\n"
"ds_read_b128 a[128:131], v2 offset:8192\n"
"buffer_load_dwordx2 v[22:23], v20, s[44:47], s61 offen offset:72\n"
"v_mfma_f32_16x16x32_bf16 v[248:251], a[88:91], a[136:139], v[248:251]\n"
"v_mfma_f32_16x16x32_bf16 v[248:251], a[92:95], a[140:143], v[248:251]\n"
"v_mfma_f32_16x16x32_bf16 v[220:223], a[84:87], a[140:143], v[220:223]\n"
"ds_read_b128 a[140:143], v2 offset:11264\n"
"v_mfma_f32_16x16x32_bf16 v[220:223], a[80:83], a[136:139], v[220:223]\n"
"ds_read_b128 a[136:139], v2 offset:10240\n"
"v_mfma_f32_16x16x32_bf16 v[224:227], a[80:83], a[144:147], v[224:227]\n"
"v_mfma_f32_16x16x32_bf16 v[224:227], a[84:87], a[148:151], v[224:227]\n"
"global_load_dwordx4 a[76:79], v0, s[34:35] offset:-1024 sc1\n"
"v_mfma_f32_16x16x32_bf16 v[252:255], a[92:95], a[148:151], v[252:255]\n"
"ds_read_b128 a[148:151], v2 offset:13312\n"
"v_mfma_f32_16x16x32_bf16 v[252:255], a[88:91], a[144:147], v[252:255]\n"
"ds_read_b128 a[144:147], v2 offset:12288\n"
"v_mfma_f32_16x16x32_bf16 a[28:31], a[88:91], a[152:155], a[28:31]\n"
"v_mfma_f32_16x16x32_bf16 a[28:31], a[92:95], a[156:159], a[28:31]\n"
"v_mfma_f32_16x16x32_bf16 a[24:27], a[84:87], a[156:159], a[24:27]\n"
"ds_read_b128 a[156:159], v2 offset:15360\n"
"v_mfma_f32_16x16x32_bf16 a[24:27], a[80:83], a[152:155], a[24:27]\n"
"ds_read_b128 a[152:155], v2 offset:14336\n"
"s_waitcnt vmcnt(9) lgkmcnt(12)\n"
"v_mfma_f32_16x16x32_bf16 v[32:35], a[32:35], a[96:99], v[32:35]\n"
"s_add_u32 s48, s48, 131072\n"
"s_addc_u32 s49, s49, 0\n"
"v_mfma_f32_16x16x32_bf16 v[32:35], a[36:39], a[100:103], v[32:35]\n"
"global_load_dwordx4 a[80:83], v0, s[34:35] offset:0 sc1\n"
"v_mfma_f32_16x16x32_bf16 v[60:63], a[44:47], a[100:103], v[60:63]\n"
"s_barrier\n"
"v_mfma_f32_16x16x32_bf16 v[60:63], a[40:43], a[96:99], v[60:63]\n"
"v_mfma_f32_16x16x32_bf16 v[64:67], a[40:43], a[104:107], v[64:67]\n"
"v_mfma_f32_16x16x32_bf16 v[64:67], a[44:47], a[108:111], v[64:67]\n"
"v_mfma_f32_16x16x32_bf16 v[36:39], a[36:39], a[108:111], v[36:39]\n"
"v_subrev_u32 v8, s65, v8\n"
"v_mfma_f32_16x16x32_bf16 v[36:39], a[32:35], a[104:107], v[36:39]\n"
"s_waitcnt lgkmcnt(8)\n"
"v_xor_b32 v9, s79, v9\n"
"v_mfma_f32_16x16x32_bf16 v[40:43], a[32:35], a[112:115], v[40:43]\n"
"v_or_b32 v8, v8, v9\n"
"v_mfma_f32_16x16x32_bf16 v[40:43], a[36:39], a[116:119], v[40:43]\n"
"global_load_dwordx4 a[84:87], v0, s[34:35] offset:1024 sc1\n"
"v_cmp_lt_u32 vcc, 15, v8\n"
"v_mfma_f32_16x16x32_bf16 v[68:71], a[44:47], a[116:119], v[68:71]\n"
"s_cbranch_vccnz L_slow1\n"
"L_back1:\n"
"v_mfma_f32_16x16x32_bf16 v[68:71], a[40:43], a[112:115], v[68:71]\n"
"v_mfma_f32_16x16x32_bf16 v[72:75], a[40:43], a[120:123], v[72:75]\n"
"v_mfma_f32_16x16x32_bf16 v[72:75], a[44:47], a[124:127], v[72:75]\n"
"v_mfma_f32_16x16x32_bf16 v[44:47], a[36:39], a[124:127], v[44:47]\n"
"v_mfma_f32_16x16x32_bf16 v[44:47], a[32:35], a[120:123], v[44:47]\n"
"s_waitcnt lgkmcnt(4)\n"
"v_mfma_f32_16x16x32_bf16 v[48:51], a[32:35], a[128:131], v[48:51]\n"
"v_mfma_f32_16x16x32_bf16 v[48:51], a[36:39], a[132:135], v[48:51]\n"
"global_load_dwordx4 a[88:91], v0, s[34:35] offset:2048 sc1\n"
"v_mfma_f32_16x16x32_bf16 v[76:79], a[44:47], a[132:135], v[76:79]\n"
"v_mfma_f32_16x16x32_bf16 v[76:79], a[40:43], a[128:131], v[76:79]\n"
"s_add_u32 m0, s64, 16384\n"
"v_mfma_f32_16x16x32_bf16 v[80:83], a[40:43], a[136:139], v[80:83]\n"
"v_mfma_f32_16x16x32_bf16 v[80:83], a[44:47], a[140:143], v[80:83]\n"
"v_mfma_f32_16x16x32_bf16 v[52:55], a[36:39], a[140:143], v[52:55]\n"
"v_mfma_f32_16x16x32_bf16 v[52:55], a[32:35], a[136:139], v[52:55]\n"
"s_waitcnt lgkmcnt(0)\n"
"v_mfma_f32_16x16x32_bf16 v[56:59], a[32:35], a[144:147], v[56:59]\n"
"v_mfma_f32_16x16x32_bf16 v[56:59], a[36:39], a[148:151], v[56:59]\n"
"global_load_dwordx4 a[92:95], v0, s[34:35] offset:3072 sc1\n"
"v_mfma_f32_16x16x32_bf16 v[84:87], a[44:47], a[148:151], v[84:87]\n"
"v_mfma_f32_16x16x32_bf16 v[84:87], a[40:43], a[144:147], v[84:87]\n"
"v_mfma_f32_16x16x32_bf16 a[4:7], a[40:43], a[152:155], a[4:7]\n"
"s_add_u32 s34, s34, 8192\n"
"v_mfma_f32_16x16x32_bf16 a[4:7], a[44:47], a[156:159], a[4:7]\n"
"v_mfma_f32_16x16x32_bf16 a[0:3], a[36:39], a[156:159], a[0:3]\n"
"v_mfma_f32_16x16x32_bf16 a[0:3], a[32:35], a[152:155], a[0:3]\n"
"s_waitcnt vmcnt(9)\n"
"v_mfma_f32_16x16x32_bf16 v[88:91], a[48:51], a[96:99], v[88:91]\n"
"v_mfma_f32_16x16x32_bf16 v[88:91], a[52:55], a[100:103], v[88:91]\n"
"global_load_dwordx4 a[32:35], v0, s[34:35] offset:-4096 sc1\n"
"v_mfma_f32_16x16x32_bf16 v[116:119], a[60:63], a[100:103], v[116:119]\n"
"buffer_load_dwordx4 v0, s[48:51], s75 offen offset:0 sc1 lds\n"
"v_mfma_f32_16x16x32_bf16 v[116:119], a[56:59], a[96:99], v[116:119]\n"
"v_mfma_f32_16x16x32_bf16 v[120:123], a[56:59], a[104:107], v[120:123]\n"
"v_mfma_f32_16x16x32_bf16 v[120:123], a[60:63], a[108:111], v[120:123]\n"
"v_mfma_f32_16x16x32_bf16 v[92:95], a[52:55], a[108:111], v[92:95]\n"
"v_mfma_f32_16x16x32_bf16 v[92:95], a[48:51], a[104:107], v[92:95]\n"
"v_mfma_f32_16x16x32_bf16 v[96:99], a[48:51], a[112:115], v[96:99]\n"
"v_mfma_f32_16x16x32_bf16 v[96:99], a[52:55], a[116:119], v[96:99]\n"
"global_load_dwordx4 a[36:39], v0, s[34:35] offset:-3072 sc1\n"
"v_mfma_f32_16x16x32_bf16 v[124:127], a[60:63], a[116:119], v[124:127]\n"
"v_mfma_f32_16x16x32_bf16 v[124:127], a[56:59], a[112:115], v[124:127]\n"
"v_mfma_f32_16x16x32_bf16 v[128:131], a[56:59], a[120:123], v[128:131]\n"
"v_mfma_f32_16x16x32_bf16 v[128:131], a[60:63], a[124:127], v[128:131]\n"
"v_mfma_f32_16x16x32_bf16 v[100:103], a[52:55], a[124:127], v[100:103]\n"
"v_mfma_f32_16x16x32_bf16 v[100:103], a[48:51], a[120:123], v[100:103]\n"
"v_mfma_f32_16x16x32_bf16 v[104:107], a[48:51], a[128:131], v[104:107]\n"
"v_mfma_f32_16x16x32_bf16 v[104:107], a[52:55], a[132:135], v[104:107]\n"
"global_load_dwordx4 a[40:43], v0, s[34:35] offset:-2048 sc1\n"
"v_mfma_f32_16x16x32_bf16 v[132:135], a[60:63], a[132:135], v[132:135]\n"
"buffer_load_dwordx4 v0, s[48:51], s75 offen offset:1024 sc1 lds\n"
"v_mfma_f32_16x16x32_bf16 v[132:135], a[56:59], a[128:131], v[132:135]\n"
"v_mfma_f32_16x16x32_bf16 v[136:139], a[56:59], a[136:139], v[136:139]\n"
"v_mfma_f32_16x16x32_bf16 v[136:139], a[60:63], a[140:143], v[136:139]\n"
"v_mfma_f32_16x16x32_bf16 v[108:111], a[52:55], a[140:143], v[108:111]\n"
"v_mfma_f32_16x16x32_bf16 v[108:111], a[48:51], a[136:139], v[108:111]\n"
"v_mfma_f32_16x16x32_bf16 v[112:115], a[48:51], a[144:147], v[112:115]\n"
"v_mfma_f32_16x16x32_bf16 v[112:115], a[52:55], a[148:151], v[112:115]\n"
"global_load_dwordx4 a[44:47], v0, s[34:35] offset:-1024 sc1\n"
"v_mfma_f32_16x16x32_bf16 v[140:143], a[60:63], a[148:151], v[140:143]\n"
"v_mfma_f32_16x16x32_bf16 v[140:143], a[56:59], a[144:147], v[140:143]\n"
"v_mfma_f32_16x16x32_bf16 a[12:15], a[56:59], a[152:155], a[12:15]\n"
"v_mfma_f32_16x16x32_bf16 a[12:15], a[60:63], a[156:159], a[12:15]\n"
"v_mfma_f32_16x16x32_bf16 a[8:11], a[52:55], a[156:159], a[8:11]\n"
"v_mfma_f32_16x16x32_bf16 a[8:11], a[48:51], a[152:155], a[8:11]\n"
"s_waitcnt vmcnt(10)\n"
"v_mfma_f32_16x16x32_bf16 v[144:147], a[64:67], a[96:99], v[144:147]\n"
"v_mfma_f32_16x16x32_bf16 v[144:147], a[68:71], a[100:103], v[144:147]\n"
"global_load_dwordx4 a[48:51], v0, s[34:35] offset:0 sc1\n"
"v_mfma_f32_16x16x32_bf16 v[172:175], a[76:79], a[100:103], v[172:175]\n"
"v_mfma_f32_16x16x32_bf16 v[172:175], a[72:75], a[96:99], v[172:175]\n"
"buffer_load_dwordx4 v0, s[48:51], s75 offen offset:2048 sc1 lds\n"
"v_mfma_f32_16x16x32_bf16 v[176:179], a[72:75], a[104:107], v[176:179]\n"
"v_mfma_f32_16x16x32_bf16 v[176:179], a[76:79], a[108:111], v[176:179]\n"
"v_mfma_f32_16x16x32_bf16 v[148:151], a[68:71], a[108:111], v[148:151]\n"
"v_mfma_f32_16x16x32_bf16 v[148:151], a[64:67], a[104:107], v[148:151]\n"
"v_mfma_f32_16x16x32_bf16 v[152:155], a[64:67], a[112:115], v[152:155]\n"
"v_lshlrev_b32 v16, 16, v18\n"
"v_mfma_f32_16x16x32_bf16 v[152:155], a[68:71], a[116:119], v[152:155]\n"
"global_load_dwordx4 a[52:55], v0, s[34:35] offset:1024 sc1\n"
"v_and_b32 v17, s63, v18\n"
"v_mfma_f32_16x16x32_bf16 v[180:183], a[76:79], a[116:119], v[180:183]\n"
"v_lshlrev_b32 v18, 16, v19\n"
"v_mfma_f32_16x16x32_bf16 v[180:183], a[72:75], a[112:115], v[180:183]\n"
"v_and_b32 v19, s63, v19\n"
"v_mfma_f32_16x16x32_bf16 v[184:187], a[72:75], a[120:123], v[184:187]\n"
"v_add_f32 v88, v88, v16\n"
"v_mfma_f32_16x16x32_bf16 v[184:187], a[76:79], a[124:127], v[184:187]\n"
"v_add_f32 v89, v89, v17\n"
"v_add_f32 v90, v90, v18\n"
"v_mfma_f32_16x16x32_bf16 v[156:159], a[68:71], a[124:127], v[156:159]\n"
"v_add_f32 v91, v91, v19\n"
"v_mfma_f32_16x16x32_bf16 v[156:159], a[64:67], a[120:123], v[156:159]\n"
"v_add_f32 v92, v92, v16\n"
"v_mfma_f32_16x16x32_bf16 v[160:163], a[64:67], a[128:131], v[160:163]\n"
"v_add_f32 v93, v93, v17\n"
"v_mfma_f32_16x16x32_bf16 v[160:163], a[68:71], a[132:135], v[160:163]\n"
"global_load_dwordx4 a[56:59], v0, s[34:35] offset:2048 sc1\n"
"v_add_f32 v94, v94, v18\n"
"v_mfma_f32_16x16x32_bf16 v[188:191], a[76:79], a[132:135], v[188:191]\n"
"buffer_load_dwordx4 v0, s[48:51], s75 offen offset:3072 sc1 lds\n"
"v_mfma_f32_16x16x32_bf16 v[188:191], a[72:75], a[128:131], v[188:191]\n"
"v_add_f32 v95, v95, v19\n"
"v_add_f32 v96, v96, v16\n"
"v_mfma_f32_16x16x32_bf16 v[192:195], a[72:75], a[136:139], v[192:195]\n"
"v_add_f32 v97, v97, v17\n"
"v_mfma_f32_16x16x32_bf16 v[192:195], a[76:79], a[140:143], v[192:195]\n"
"v_add_f32 v98, v98, v18\n"
"v_mfma_f32_16x16x32_bf16 v[164:167], a[68:71], a[140:143], v[164:167]\n"
"v_add_f32 v99, v99, v19\n"
"v_mfma_f32_16x16x32_bf16 v[164:167], a[64:67], a[136:139], v[164:167]\n"
"v_add_f32 v100, v100, v16\n"
"v_add_f32 v101, v101, v17\n"
"v_mfma_f32_16x16x32_bf16 v[168:171], a[64:67], a[144:147], v[168:171]\n"
"v_add_f32 v102, v102, v18\n"
"v_mfma_f32_16x16x32_bf16 v[168:171], a[68:71], a[148:151], v[168:171]\n"
"global_load_dwordx4 a[60:63], v0, s[34:35] offset:3072 sc1\n"
"v_add_f32 v103, v103, v19\n"
"v_mfma_f32_16x16x32_bf16 v[196:199], a[76:79], a[148:151], v[196:199]\n"
"v_add_f32 v104, v104, v16\n"
"v_mfma_f32_16x16x32_bf16 v[196:199], a[72:75], a[144:147], v[196:199]\n"
"v_add_f32 v105, v105, v17\n"
"v_mfma_f32_16x16x32_bf16 a[20:23], a[72:75], a[152:155], a[20:23]\n"
"s_add_u32 s34, s34, 8192\n"
"v_add_f32 v106, v106, v18\n"
"v_mfma_f32_16x16x32_bf16 a[20:23], a[76:79], a[156:159], a[20:23]\n"
"v_add_f32 v107, v107, v19\n"
"v_mfma_f32_16x16x32_bf16 a[16:19], a[68:71], a[156:159], a[16:19]\n"
"v_add_f32 v108, v108, v16\n"
"v_mfma_f32_16x16x32_bf16 a[16:19], a[64:67], a[152:155], a[16:19]\n"
"s_waitcnt vmcnt(12)\n"
"v_add_f32 v109, v109, v17\n"
"v_mfma_f32_16x16x32_bf16 v[200:203], a[80:83], a[96:99], v[200:203]\n"
"v_add_f32 v110, v110, v18\n"
"v_add_f32 v111, v111, v19\n"
"v_mfma_f32_16x16x32_bf16 v[200:203], a[84:87], a[100:103], v[200:203]\n"
"global_load_dwordx4 a[64:67], v0, s[34:35] offset:-4096 sc1\n"
"v_add_f32 v112, v112, v16\n"
"v_mfma_f32_16x16x32_bf16 v[228:231], a[92:95], a[100:103], v[228:231]\n"
"ds_read_b128 a[100:103], v3 offset:1024\n"
"v_add_f32 v113, v113, v17\n"
"v_mfma_f32_16x16x32_bf16 v[228:231], a[88:91], a[96:99], v[228:231]\n"
"s_add_u32 m0, s64, 20480\n"
"ds_read_b128 a[96:99], v3 offset:0\n"
"v_add_f32 v114, v114, v18\n"
"v_mfma_f32_16x16x32_bf16 v[232:235], a[88:91], a[104:107], v[232:235]\n"
"v_add_f32 v115, v115, v19\n"
"v_mfma_f32_16x16x32_bf16 v[232:235], a[92:95], a[108:111], v[232:235]\n"
"v_accvgpr_read_b32 v12, a8\n"
"v_mfma_f32_16x16x32_bf16 v[204:207], a[84:87], a[108:111], v[204:207]\n"
"ds_read_b128 a[108:111], v3 offset:3072\n"
"v_accvgpr_read_b32 v13, a9\n"
"v_mfma_f32_16x16x32_bf16 v[204:207], a[80:83], a[104:107], v[204:207]\n"
"ds_read_b128 a[104:107], v3 offset:2048\n"
"v_accvgpr_read_b32 v14, a10\n"
"v_mfma_f32_16x16x32_bf16 v[208:211], a[80:83], a[112:115], v[208:211]\n"
"v_accvgpr_read_b32 v15, a11\n"
"v_mfma_f32_16x16x32_bf16 v[208:211], a[84:87], a[116:119], v[208:211]\n"
"global_load_dwordx4 a[68:71], v0, s[34:35] offset:-3072 sc1\n"
"v_add_f32 v12, v12, v16\n"
"v_mfma_f32_16x16x32_bf16 v[236:239], a[92:95], a[116:119], v[236:239]\n"
"ds_read_b128 a[116:119], v3 offset:5120\n"
"v_add_f32 v13, v13, v17\n"
"v_mfma_f32_16x16x32_bf16 v[236:239], a[88:91], a[112:115], v[236:239]\n"
"ds_read_b128 a[112:115], v3 offset:4096\n"
"v_add_f32 v14, v14, v18\n"
"v_mfma_f32_16x16x32_bf16 v[240:243], a[88:91], a[120:123], v[240:243]\n"
"v_add_f32 v15, v15, v19\n"
"v_accvgpr_write_b32 a8, v12\n"
"v_mfma_f32_16x16x32_bf16 v[240:243], a[92:95], a[124:127], v[240:243]\n"
"v_accvgpr_write_b32 a9, v13\n"
"v_mfma_f32_16x16x32_bf16 v[212:215], a[84:87], a[124:127], v[212:215]\n"
"ds_read_b128 a[124:127], v3 offset:7168\n"
"v_accvgpr_write_b32 a10, v14\n"
"v_mfma_f32_16x16x32_bf16 v[212:215], a[80:83], a[120:123], v[212:215]\n"
"ds_read_b128 a[120:123], v3 offset:6144\n"
"v_accvgpr_write_b32 a11, v15\n"
"v_mfma_f32_16x16x32_bf16 v[216:219], a[80:83], a[128:131], v[216:219]\n"
"v_mfma_f32_16x16x32_bf16 v[216:219], a[84:87], a[132:135], v[216:219]\n"
"global_load_dwordx4 a[72:75], v0, s[34:35] offset:-2048 sc1\n"
"v_mfma_f32_16x16x32_bf16 v[244:247], a[92:95], a[132:135], v[244:247]\n"
"ds_read_b128 a[132:135], v3 offset:9216\n"
"v_mfma_f32_16x16x32_bf16 v[244:247], a[88:91], a[128:131], v[244:247]\n"
"ds_read_b128 a[128:131], v3 offset:8192\n"
"v_mfma_f32_16x16x32_bf16 v[248:251], a[88:91], a[136:139], v[248:251]\n"
"buffer_load_dwordx4 v1, s[48:51], s75 offen offset:0 sc1 lds\n"
"v_mfma_f32_16x16x32_bf16 v[248:251], a[92:95], a[140:143], v[248:251]\n"
"v_mfma_f32_16x16x32_bf16 v[220:223], a[84:87], a[140:143], v[220:223]\n"
"ds_read_b128 a[140:143], v3 offset:11264\n"
"v_mfma_f32_16x16x32_bf16 v[220:223], a[80:83], a[136:139], v[220:223]\n"
"ds_read_b128 a[136:139], v3 offset:10240\n"
"v_mfma_f32_16x16x32_bf16 v[224:227], a[80:83], a[144:147], v[224:227]\n"
"v_mfma_f32_16x16x32_bf16 v[224:227], a[84:87], a[148:151], v[224:227]\n"
"global_load_dwordx4 a[76:79], v0, s[34:35] offset:-1024 sc1\n"
"v_mfma_f32_16x16x32_bf16 v[252:255], a[92:95], a[148:151], v[252:255]\n"
"ds_read_b128 a[148:151], v3 offset:13312\n"
"v_mfma_f32_16x16x32_bf16 v[252:255], a[88:91], a[144:147], v[252:255]\n"
"ds_read_b128 a[144:147], v3 offset:12288\n"
"v_mfma_f32_16x16x32_bf16 a[28:31], a[88:91], a[152:155], a[28:31]\n"
"v_mfma_f32_16x16x32_bf16 a[28:31], a[92:95], a[156:159], a[28:31]\n"
"v_mfma_f32_16x16x32_bf16 a[24:27], a[84:87], a[156:159], a[24:27]\n"
"ds_read_b128 a[156:159], v3 offset:15360\n"
"v_mfma_f32_16x16x32_bf16 a[24:27], a[80:83], a[152:155], a[24:27]\n"
"s_waitcnt vmcnt(11) lgkmcnt(11)\n"
"ds_read_b128 a[152:155], v3 offset:14336\n"
"v_mfma_f32_16x16x32_bf16 v[32:35], a[32:35], a[96:99], v[32:35]\n"
"v_mfma_f32_16x16x32_bf16 v[32:35], a[36:39], a[100:103], v[32:35]\n"
"global_load_dwordx4 a[80:83], v0, s[34:35] offset:0 sc1\n"
"v_mfma_f32_16x16x32_bf16 v[60:63], a[44:47], a[100:103], v[60:63]\n"
"v_mfma_f32_16x16x32_bf16 v[60:63], a[40:43], a[96:99], v[60:63]\n"
"buffer_load_dwordx4 v1, s[48:51], s75 offen offset:1024 sc1 lds\n"
"v_mfma_f32_16x16x32_bf16 v[64:67], a[40:43], a[104:107], v[64:67]\n"
"v_mfma_f32_16x16x32_bf16 v[64:67], a[44:47], a[108:111], v[64:67]\n"
"v_mfma_f32_16x16x32_bf16 v[36:39], a[36:39], a[108:111], v[36:39]\n"
"v_mfma_f32_16x16x32_bf16 v[36:39], a[32:35], a[104:107], v[36:39]\n"
"s_waitcnt lgkmcnt(8)\n"
"v_mfma_f32_16x16x32_bf16 v[40:43], a[32:35], a[112:115], v[40:43]\n"
"v_mfma_f32_16x16x32_bf16 v[40:43], a[36:39], a[116:119], v[40:43]\n"
"global_load_dwordx4 a[84:87], v0, s[34:35] offset:1024 sc1\n"
"v_mfma_f32_16x16x32_bf16 v[68:71], a[44:47], a[116:119], v[68:71]\n"
"v_mfma_f32_16x16x32_bf16 v[68:71], a[40:43], a[112:115], v[68:71]\n"
"v_mfma_f32_16x16x32_bf16 v[72:75], a[40:43], a[120:123], v[72:75]\n"
"v_mfma_f32_16x16x32_bf16 v[72:75], a[44:47], a[124:127], v[72:75]\n"
"v_mfma_f32_16x16x32_bf16 v[44:47], a[36:39], a[124:127], v[44:47]\n"
"v_mfma_f32_16x16x32_bf16 v[44:47], a[32:35], a[120:123], v[44:47]\n"
"s_waitcnt lgkmcnt(4)\n"
"v_mfma_f32_16x16x32_bf16 v[48:51], a[32:35], a[128:131], v[48:51]\n"
"v_mfma_f32_16x16x32_bf16 v[48:51], a[36:39], a[132:135], v[48:51]\n"
"global_load_dwordx4 a[88:91], v0, s[34:35] offset:2048 sc1\n"
"v_mfma_f32_16x16x32_bf16 v[76:79], a[44:47], a[132:135], v[76:79]\n"
"v_mfma_f32_16x16x32_bf16 v[76:79], a[40:43], a[128:131], v[76:79]\n"
"buffer_load_dwordx4 v1, s[48:51], s75 offen offset:2048 sc1 lds\n"
"v_mfma_f32_16x16x32_bf16 v[80:83], a[40:43], a[136:139], v[80:83]\n"
"v_mfma_f32_16x16x32_bf16 v[80:83], a[44:47], a[140:143], v[80:83]\n"
"v_mfma_f32_16x16x32_bf16 v[52:55], a[36:39], a[140:143], v[52:55]\n"
"v_mfma_f32_16x16x32_bf16 v[52:55], a[32:35], a[136:139], v[52:55]\n"
"s_waitcnt lgkmcnt(0)\n"
"v_mfma_f32_16x16x32_bf16 v[56:59], a[32:35], a[144:147], v[56:59]\n"
"v_mfma_f32_16x16x32_bf16 v[56:59], a[36:39], a[148:151], v[56:59]\n"
"global_load_dwordx4 a[92:95], v0, s[34:35] offset:3072 sc1\n"
"v_mfma_f32_16x16x32_bf16 v[84:87], a[44:47], a[148:151], v[84:87]\n"
"v_mfma_f32_16x16x32_bf16 v[84:87], a[40:43], a[144:147], v[84:87]\n"
"v_mfma_f32_16x16x32_bf16 a[4:7], a[40:43], a[152:155], a[4:7]\n"
"s_add_u32 s34, s34, 8192\n"
"v_mfma_f32_16x16x32_bf16 a[4:7], a[44:47], a[156:159], a[4:7]\n"
"v_mfma_f32_16x16x32_bf16 a[0:3], a[36:39], a[156:159], a[0:3]\n"
"v_mfma_f32_16x16x32_bf16 a[0:3], a[32:35], a[152:155], a[0:3]\n"
"s_waitcnt vmcnt(11)\n"
"v_mfma_f32_16x16x32_bf16 v[88:91], a[48:51], a[96:99], v[88:91]\n"
"v_mfma_f32_16x16x32_bf16 v[88:91], a[52:55], a[100:103], v[88:91]\n"
"global_load_dwordx4 a[32:35], v0, s[34:35] offset:-4096 sc1\n"
"v_mfma_f32_16x16x32_bf16 v[116:119], a[60:63], a[100:103], v[116:119]\n"
"v_mfma_f32_16x16x32_bf16 v[116:119], a[56:59], a[96:99], v[116:119]\n"
"buffer_load_dwordx4 v1, s[48:51], s75 offen offset:3072 sc1 lds\n"
"v_mfma_f32_16x16x32_bf16 v[120:123], a[56:59], a[104:107], v[120:123]\n"
"v_mfma_f32_16x16x32_bf16 v[120:123], a[60:63], a[108:111], v[120:123]\n"
"v_mfma_f32_16x16x32_bf16 v[92:95], a[52:55], a[108:111], v[92:95]\n"
"v_mfma_f32_16x16x32_bf16 v[92:95], a[48:51], a[104:107], v[92:95]\n"
"v_mfma_f32_16x16x32_bf16 v[96:99], a[48:51], a[112:115], v[96:99]\n"
"v_mfma_f32_16x16x32_bf16 v[96:99], a[52:55], a[116:119], v[96:99]\n"
"global_load_dwordx4 a[36:39], v0, s[34:35] offset:-3072 sc1\n"
"v_mfma_f32_16x16x32_bf16 v[124:127], a[60:63], a[116:119], v[124:127]\n"
"v_mfma_f32_16x16x32_bf16 v[124:127], a[56:59], a[112:115], v[124:127]\n"
"v_mfma_f32_16x16x32_bf16 v[128:131], a[56:59], a[120:123], v[128:131]\n"
"v_mfma_f32_16x16x32_bf16 v[128:131], a[60:63], a[124:127], v[128:131]\n"
"v_mfma_f32_16x16x32_bf16 v[100:103], a[52:55], a[124:127], v[100:103]\n"
"v_mfma_f32_16x16x32_bf16 v[100:103], a[48:51], a[120:123], v[100:103]\n"
"v_mfma_f32_16x16x32_bf16 v[104:107], a[48:51], a[128:131], v[104:107]\n"
"v_mfma_f32_16x16x32_bf16 v[104:107], a[52:55], a[132:135], v[104:107]\n"
"global_load_dwordx4 a[40:43], v0, s[34:35] offset:-2048 sc1\n"
"v_mfma_f32_16x16x32_bf16 v[132:135], a[60:63], a[132:135], v[132:135]\n"
"v_mfma_f32_16x16x32_bf16 v[132:135], a[56:59], a[128:131], v[132:135]\n"
"s_add_u32 m0, s64, 24576\n"
"v_mfma_f32_16x16x32_bf16 v[136:139], a[56:59], a[136:139], v[136:139]\n"
"v_mfma_f32_16x16x32_bf16 v[136:139], a[60:63], a[140:143], v[136:139]\n"
"v_mfma_f32_16x16x32_bf16 v[108:111], a[52:55], a[140:143], v[108:111]\n"
"v_mfma_f32_16x16x32_bf16 v[108:111], a[48:51], a[136:139], v[108:111]\n"
"v_mfma_f32_16x16x32_bf16 v[112:115], a[48:51], a[144:147], v[112:115]\n"
"v_mfma_f32_16x16x32_bf16 v[112:115], a[52:55], a[148:151], v[112:115]\n"
"global_load_dwordx4 a[44:47], v0, s[34:35] offset:-1024 sc1\n"
"v_mfma_f32_16x16x32_bf16 v[140:143], a[60:63], a[148:151], v[140:143]\n"
"v_mfma_f32_16x16x32_bf16 v[140:143], a[56:59], a[144:147], v[140:143]\n"
"v_mfma_f32_16x16x32_bf16 a[12:15], a[56:59], a[152:155], a[12:15]\n"
"v_mfma_f32_16x16x32_bf16 a[12:15], a[60:63], a[156:159], a[12:15]\n"
"v_mfma_f32_16x16x32_bf16 a[8:11], a[52:55], a[156:159], a[8:11]\n"
"v_mfma_f32_16x16x32_bf16 a[8:11], a[48:51], a[152:155], a[8:11]\n"
"s_waitcnt vmcnt(11)\n"
"v_mfma_f32_16x16x32_bf16 v[144:147], a[64:67], a[96:99], v[144:147]\n"
"v_mfma_f32_16x16x32_bf16 v[144:147], a[68:71], a[100:103], v[144:147]\n"
"global_load_dwordx4 a[48:51], v0, s[34:35] offset:0 sc1\n"
"v_mfma_f32_16x16x32_bf16 v[172:175], a[76:79], a[100:103], v[172:175]\n"
"v_mfma_f32_16x16x32_bf16 v[172:175], a[72:75], a[96:99], v[172:175]\n"
"buffer_load_dwordx4 v0, s[48:51], s69 offen offset:0 sc1 lds\n"
"v_mfma_f32_16x16x32_bf16 v[176:179], a[72:75], a[104:107], v[176:179]\n"
"v_mfma_f32_16x16x32_bf16 v[176:179], a[76:79], a[108:111], v[176:179]\n"
"v_mfma_f32_16x16x32_bf16 v[148:151], a[68:71], a[108:111], v[148:151]\n"
"v_mfma_f32_16x16x32_bf16 v[148:151], a[64:67], a[104:107], v[148:151]\n"
"v_mfma_f32_16x16x32_bf16 v[152:155], a[64:67], a[112:115], v[152:155]\n"
"v_lshlrev_b32 v20, 16, v22\n"
"v_mfma_f32_16x16x32_bf16 v[152:155], a[68:71], a[116:119], v[152:155]\n"
"global_load_dwordx4 a[52:55], v0, s[34:35] offset:1024 sc1\n"
"v_and_b32 v21, s63, v22\n"
"v_mfma_f32_16x16x32_bf16 v[180:183], a[76:79], a[116:119], v[180:183]\n"
"v_lshlrev_b32 v22, 16, v23\n"
"v_mfma_f32_16x16x32_bf16 v[180:183], a[72:75], a[112:115], v[180:183]\n"
"v_and_b32 v23, s63, v23\n"
"v_mfma_f32_16x16x32_bf16 v[184:187], a[72:75], a[120:123], v[184:187]\n"
"v_add_f32 v116, v116, v20\n"
"v_mfma_f32_16x16x32_bf16 v[184:187], a[76:79], a[124:127], v[184:187]\n"
"v_add_f32 v117, v117, v21\n"
"v_add_f32 v118, v118, v22\n"
"v_mfma_f32_16x16x32_bf16 v[156:159], a[68:71], a[124:127], v[156:159]\n"
"v_add_f32 v119, v119, v23\n"
"v_mfma_f32_16x16x32_bf16 v[156:159], a[64:67], a[120:123], v[156:159]\n"
"v_add_f32 v120, v120, v20\n"
"v_mfma_f32_16x16x32_bf16 v[160:163], a[64:67], a[128:131], v[160:163]\n"
"v_add_f32 v121, v121, v21\n"
"v_mfma_f32_16x16x32_bf16 v[160:163], a[68:71], a[132:135], v[160:163]\n"
"global_load_dwordx4 a[56:59], v0, s[34:35] offset:2048 sc1\n"
"v_add_f32 v122, v122, v22\n"
"v_mfma_f32_16x16x32_bf16 v[188:191], a[76:79], a[132:135], v[188:191]\n"
"v_add_f32 v123, v123, v23\n"
"v_add_f32 v124, v124, v20\n"
"v_mfma_f32_16x16x32_bf16 v[188:191], a[72:75], a[128:131], v[188:191]\n"
"buffer_load_dwordx4 v0, s[48:51], s69 offen offset:1024 sc1 lds\n"
"v_mfma_f32_16x16x32_bf16 v[192:195], a[72:75], a[136:139], v[192:195]\n"
"v_add_f32 v125, v125, v21\n"
"v_mfma_f32_16x16x32_bf16 v[192:195], a[76:79], a[140:143], v[192:195]\n"
"v_add_f32 v126, v126, v22\n"
"v_mfma_f32_16x16x32_bf16 v[164:167], a[68:71], a[140:143], v[164:167]\n"
"v_add_f32 v127, v127, v23\n"
"v_mfma_f32_16x16x32_bf16 v[164:167], a[64:67], a[136:139], v[164:167]\n"
"v_add_f32 v128, v128, v20\n"
"v_add_f32 v129, v129, v21\n"
"v_mfma_f32_16x16x32_bf16 v[168:171], a[64:67], a[144:147], v[168:171]\n"
"v_add_f32 v130, v130, v22\n"
"v_mfma_f32_16x16x32_bf16 v[168:171], a[68:71], a[148:151], v[168:171]\n"
"global_load_dwordx4 a[60:63], v0, s[34:35] offset:3072 sc1\n"
"v_add_f32 v131, v131, v23\n"
"v_mfma_f32_16x16x32_bf16 v[196:199], a[76:79], a[148:151], v[196:199]\n"
"v_add_f32 v132, v132, v20\n"
"v_mfma_f32_16x16x32_bf16 v[196:199], a[72:75], a[144:147], v[196:199]\n"
"v_add_f32 v133, v133, v21\n"
"v_mfma_f32_16x16x32_bf16 a[20:23], a[72:75], a[152:155], a[20:23]\n"
"s_add_u32 s34, s34, 8192\n"
"v_add_f32 v134, v134, v22\n"
"v_mfma_f32_16x16x32_bf16 a[20:23], a[76:79], a[156:159], a[20:23]\n"
"v_add_f32 v135, v135, v23\n"
"v_mfma_f32_16x16x32_bf16 a[16:19], a[68:71], a[156:159], a[16:19]\n"
"v_add_f32 v136, v136, v20\n"
"v_mfma_f32_16x16x32_bf16 a[16:19], a[64:67], a[152:155], a[16:19]\n"
"s_waitcnt vmcnt(11)\n"
"v_add_f32 v137, v137, v21\n"
"v_mfma_f32_16x16x32_bf16 v[200:203], a[80:83], a[96:99], v[200:203]\n"
"v_add_f32 v138, v138, v22\n"
"v_add_f32 v139, v139, v23\n"
"v_mfma_f32_16x16x32_bf16 v[200:203], a[84:87], a[100:103], v[200:203]\n"
"global_load_dwordx4 a[64:67], v0, s[34:35] offset:-4096 sc1\n"
"v_add_f32 v140, v140, v20\n"
"v_mfma_f32_16x16x32_bf16 v[228:231], a[92:95], a[100:103], v[228:231]\n"
"ds_read_b128 a[100:103], v4 offset:1024\n"
"v_add_f32 v141, v141, v21\n"
"v_mfma_f32_16x16x32_bf16 v[228:231], a[88:91], a[96:99], v[228:231]\n"
"ds_read_b128 a[96:99], v4 offset:0\n"
"v_add_f32 v142, v142, v22\n"
"v_mfma_f32_16x16x32_bf16 v[232:235], a[88:91], a[104:107], v[232:235]\n"
"buffer_load_dwordx4 v0, s[48:51], s69 offen offset:2048 sc1 lds\n"
"v_mfma_f32_16x16x32_bf16 v[232:235], a[92:95], a[108:111], v[232:235]\n"
"v_add_f32 v143, v143, v23\n"
"v_accvgpr_read_b32 v12, a12\n"
"v_mfma_f32_16x16x32_bf16 v[204:207], a[84:87], a[108:111], v[204:207]\n"
"ds_read_b128 a[108:111], v4 offset:3072\n"
"v_accvgpr_read_b32 v13, a13\n"
"v_mfma_f32_16x16x32_bf16 v[204:207], a[80:83], a[104:107], v[204:207]\n"
"ds_read_b128 a[104:107], v4 offset:2048\n"
"v_accvgpr_read_b32 v14, a14\n"
"v_mfma_f32_16x16x32_bf16 v[208:211], a[80:83], a[112:115], v[208:211]\n"
"v_accvgpr_read_b32 v15, a15\n"
"v_mfma_f32_16x16x32_bf16 v[208:211], a[84:87], a[116:119], v[208:211]\n"
"global_load_dwordx4 a[68:71], v0, s[34:35] offset:-3072 sc1\n"
"v_add_f32 v12, v12, v20\n"
"v_mfma_f32_16x16x32_bf16 v[236:239], a[92:95], a[116:119], v[236:239]\n"
"ds_read_b128 a[116:119], v4 offset:5120\n"
"v_add_f32 v13, v13, v21\n"
"v_mfma_f32_16x16x32_bf16 v[236:239], a[88:91], a[112:115], v[236:239]\n"
"ds_read_b128 a[112:115], v4 offset:4096\n"
"v_add_f32 v14, v14, v22\n"
"v_mfma_f32_16x16x32_bf16 v[240:243], a[88:91], a[120:123], v[240:243]\n"
"v_add_f32 v15, v15, v23\n"
"v_accvgpr_write_b32 a12, v12\n"
"v_mfma_f32_16x16x32_bf16 v[240:243], a[92:95], a[124:127], v[240:243]\n"
"v_accvgpr_write_b32 a13, v13\n"
"v_mfma_f32_16x16x32_bf16 v[212:215], a[84:87], a[124:127], v[212:215]\n"
"ds_read_b128 a[124:127], v4 offset:7168\n"
"v_accvgpr_write_b32 a14, v14\n"
"v_mfma_f32_16x16x32_bf16 v[212:215], a[80:83], a[120:123], v[212:215]\n"
"ds_read_b128 a[120:123], v4 offset:6144\n"
"v_accvgpr_write_b32 a15, v15\n"
"v_mfma_f32_16x16x32_bf16 v[216:219], a[80:83], a[128:131], v[216:219]\n"
"v_mfma_f32_16x16x32_bf16 v[216:219], a[84:87], a[132:135], v[216:219]\n"
"global_load_dwordx4 a[72:75], v0, s[34:35] offset:-2048 sc1\n"
"v_mfma_f32_16x16x32_bf16 v[244:247], a[92:95], a[132:135], v[244:247]\n"
"ds_read_b128 a[132:135], v4 offset:9216\n"
"v_mfma_f32_16x16x32_bf16 v[244:247], a[88:91], a[128:131], v[244:247]\n"
"ds_read_b128 a[128:131], v4 offset:8192\n"
"v_mfma_f32_16x16x32_bf16 v[248:251], a[88:91], a[136:139], v[248:251]\n"
"buffer_load_dwordx4 v0, s[48:51], s69 offen offset:3072 sc1 lds\n"
"v_mfma_f32_16x16x32_bf16 v[248:251], a[92:95], a[140:143], v[248:251]\n"
"v_mfma_f32_16x16x32_bf16 v[220:223], a[84:87], a[140:143], v[220:223]\n"
"ds_read_b128 a[140:143], v4 offset:11264\n"
"v_mfma_f32_16x16x32_bf16 v[220:223], a[80:83], a[136:139], v[220:223]\n"
"ds_read_b128 a[136:139], v4 offset:10240\n"
"v_mfma_f32_16x16x32_bf16 v[224:227], a[80:83], a[144:147], v[224:227]\n"
"v_mfma_f32_16x16x32_bf16 v[224:227], a[84:87], a[148:151], v[224:227]\n"
"global_load_dwordx4 a[76:79], v0, s[34:35] offset:-1024 sc1\n"
"v_mfma_f32_16x16x32_bf16 v[252:255], a[92:95], a[148:151], v[252:255]\n"
"ds_read_b128 a[148:151], v4 offset:13312\n"
"v_mfma_f32_16x16x32_bf16 v[252:255], a[88:91], a[144:147], v[252:255]\n"
"ds_read_b128 a[144:147], v4 offset:12288\n"
"v_mfma_f32_16x16x32_bf16 a[28:31], a[88:91], a[152:155], a[28:31]\n"
"v_mfma_f32_16x16x32_bf16 a[28:31], a[92:95], a[156:159], a[28:31]\n"
"v_mfma_f32_16x16x32_bf16 a[24:27], a[84:87], a[156:159], a[24:27]\n"
"ds_read_b128 a[156:159], v4 offset:15360\n"
"v_mfma_f32_16x16x32_bf16 a[24:27], a[80:83], a[152:155], a[24:27]\n"
"s_waitcnt vmcnt(12) lgkmcnt(11)\n"
"ds_read_b128 a[152:155], v4 offset:14336\n"
"v_mfma_f32_16x16x32_bf16 v[32:35], a[32:35], a[96:99], v[32:35]\n"
"v_mfma_f32_16x16x32_bf16 v[32:35], a[36:39], a[100:103], v[32:35]\n"
"global_load_dwordx4 a[80:83], v0, s[34:35] offset:0 sc1\n"
"v_lshrrev_b32 v16, 4, v0\n"
"v_mfma_f32_16x16x32_bf16 v[60:63], a[44:47], a[100:103], v[60:63]\n"
"v_and_b32 v16, 0x30, v16\n"
"v_mfma_f32_16x16x32_bf16 v[60:63], a[40:43], a[96:99], v[60:63]\n"
"s_add_u32 m0, s64, 28672\n"
"buffer_load_dwordx2 v[18:19], v16, s[44:47], s61 offen offset:128\n"
"v_mfma_f32_16x16x32_bf16 v[64:67], a[40:43], a[104:107], v[64:67]\n"
"v_mfma_f32_16x16x32_bf16 v[64:67], a[44:47], a[108:111], v[64:67]\n"
"v_mfma_f32_16x16x32_bf16 v[36:39], a[36:39], a[108:111], v[36:39]\n"
"buffer_load_dwordx4 v[12:15], v0, s[56:59], %25 offen sc1\n"
"v_mfma_f32_16x16x32_bf16 v[36:39], a[32:35], a[104:107], v[36:39]\n"
"s_waitcnt lgkmcnt(8)\n"
"v_mfma_f32_16x16x32_bf16 v[40:43], a[32:35], a[112:115], v[40:43]\n"
"v_mfma_f32_16x16x32_bf16 v[40:43], a[36:39], a[116:119], v[40:43]\n"
"global_load_dwordx4 a[84:87], v0, s[34:35] offset:1024 sc1\n"
"v_lshrrev_b32 v20, 4, v0\n"
"v_mfma_f32_16x16x32_bf16 v[68:71], a[44:47], a[116:119], v[68:71]\n"
"v_and_b32 v20, 0x30, v20\n"
"v_mfma_f32_16x16x32_bf16 v[68:71], a[40:43], a[112:115], v[68:71]\n"
"buffer_load_dwordx2 v[22:23], v20, s[44:47], s61 offen offset:136\n"
"v_mfma_f32_16x16x32_bf16 v[72:75], a[40:43], a[120:123], v[72:75]\n"
"v_mfma_f32_16x16x32_bf16 v[72:75], a[44:47], a[124:127], v[72:75]\n"
"v_mfma_f32_16x16x32_bf16 v[44:47], a[36:39], a[124:127], v[44:47]\n"
"v_mfma_f32_16x16x32_bf16 v[44:47], a[32:35], a[120:123], v[44:47]\n"
"s_waitcnt lgkmcnt(4)\n"
"v_mfma_f32_16x16x32_bf16 v[48:51], a[32:35], a[128:131], v[48:51]\n"
"v_mfma_f32_16x16x32_bf16 v[48:51], a[36:39], a[132:135], v[48:51]\n"
"global_load_dwordx4 a[88:91], v0, s[34:35] offset:2048 sc1\n"
"v_lshrrev_b32 v24, 4, v0\n"
"v_mfma_f32_16x16x32_bf16 v[76:79], a[44:47], a[132:135], v[76:79]\n"
"v_and_b32 v24, 0x30, v24\n"
"v_mfma_f32_16x16x32_bf16 v[76:79], a[40:43], a[128:131], v[76:79]\n"
"buffer_load_dwordx4 v1, s[48:51], s69 offen offset:0 sc1 lds\n"
"buffer_load_dwordx2 v[26:27], v24, s[44:47], s61 offen offset:192\n"
"v_mfma_f32_16x16x32_bf16 v[80:83], a[40:43], a[136:139], v[80:83]\n"
"v_mfma_f32_16x16x32_bf16 v[80:83], a[44:47], a[140:143], v[80:83]\n"
"v_mfma_f32_16x16x32_bf16 v[52:55], a[36:39], a[140:143], v[52:55]\n"
"v_mfma_f32_16x16x32_bf16 v[52:55], a[32:35], a[136:139], v[52:55]\n"
"s_waitcnt lgkmcnt(0)\n"
"v_mfma_f32_16x16x32_bf16 v[56:59], a[32:35], a[144:147], v[56:59]\n"
"v_mfma_f32_16x16x32_bf16 v[56:59], a[36:39], a[148:151], v[56:59]\n"
"global_load_dwordx4 a[92:95], v0, s[34:35] offset:3072 sc1\n"
"v_lshrrev_b32 v28, 4, v0\n"
"v_mfma_f32_16x16x32_bf16 v[84:87], a[44:47], a[148:151], v[84:87]\n"
"v_and_b32 v28, 0x30, v28\n"
"v_mfma_f32_16x16x32_bf16 v[84:87], a[40:43], a[144:147], v[84:87]\n"
"buffer_load_dwordx2 v[30:31], v28, s[44:47], s61 offen offset:200\n"
"v_mfma_f32_16x16x32_bf16 a[4:7], a[40:43], a[152:155], a[4:7]\n"
"s_add_u32 s34, s34, 8192\n"
"v_mfma_f32_16x16x32_bf16 a[4:7], a[44:47], a[156:159], a[4:7]\n"
"v_mfma_f32_16x16x32_bf16 a[0:3], a[36:39], a[156:159], a[0:3]\n"
"v_mfma_f32_16x16x32_bf16 a[0:3], a[32:35], a[152:155], a[0:3]\n"
"s_waitcnt vmcnt(16)\n"
"v_mfma_f32_16x16x32_bf16 v[88:91], a[48:51], a[96:99], v[88:91]\n"
"v_mfma_f32_16x16x32_bf16 v[88:91], a[52:55], a[100:103], v[88:91]\n"
"global_load_dwordx4 a[32:35], v0, s[34:35] offset:-4096 sc1\n"
"v_mfma_f32_16x16x32_bf16 v[116:119], a[60:63], a[100:103], v[116:119]\n"
"v_mfma_f32_16x16x32_bf16 v[116:119], a[56:59], a[96:99], v[116:119]\n"
"buffer_load_dwordx4 v1, s[48:51], s69 offen offset:1024 sc1 lds\n"
"v_mfma_f32_16x16x32_bf16 v[120:123], a[56:59], a[104:107], v[120:123]\n"
"v_mfma_f32_16x16x32_bf16 v[120:123], a[60:63], a[108:111], v[120:123]\n"
"v_mfma_f32_16x16x32_bf16 v[92:95], a[52:55], a[108:111], v[92:95]\n"
"v_mfma_f32_16x16x32_bf16 v[92:95], a[48:51], a[104:107], v[92:95]\n"
"v_mfma_f32_16x16x32_bf16 v[96:99], a[48:51], a[112:115], v[96:99]\n"
"v_mfma_f32_16x16x32_bf16 v[96:99], a[52:55], a[116:119], v[96:99]\n"
"global_load_dwordx4 a[36:39], v0, s[34:35] offset:-3072 sc1\n"
"v_mfma_f32_16x16x32_bf16 v[124:127], a[60:63], a[116:119], v[124:127]\n"
"v_mfma_f32_16x16x32_bf16 v[124:127], a[56:59], a[112:115], v[124:127]\n"
"v_mfma_f32_16x16x32_bf16 v[128:131], a[56:59], a[120:123], v[128:131]\n"
"v_mfma_f32_16x16x32_bf16 v[128:131], a[60:63], a[124:127], v[128:131]\n"
"v_mfma_f32_16x16x32_bf16 v[100:103], a[52:55], a[124:127], v[100:103]\n"
"v_mfma_f32_16x16x32_bf16 v[100:103], a[48:51], a[120:123], v[100:103]\n"
"v_mfma_f32_16x16x32_bf16 v[104:107], a[48:51], a[128:131], v[104:107]\n"
"v_mfma_f32_16x16x32_bf16 v[104:107], a[52:55], a[132:135], v[104:107]\n"
"global_load_dwordx4 a[40:43], v0, s[34:35] offset:-2048 sc1\n"
"v_mfma_f32_16x16x32_bf16 v[132:135], a[60:63], a[132:135], v[132:135]\n"
"v_mfma_f32_16x16x32_bf16 v[132:135], a[56:59], a[128:131], v[132:135]\n"
"buffer_load_dwordx4 v1, s[48:51], s69 offen offset:2048 sc1 lds\n"
"v_mfma_f32_16x16x32_bf16 v[136:139], a[56:59], a[136:139], v[136:139]\n"
"v_mfma_f32_16x16x32_bf16 v[136:139], a[60:63], a[140:143], v[136:139]\n"
"v_mfma_f32_16x16x32_bf16 v[108:111], a[52:55], a[140:143], v[108:111]\n"
"v_mfma_f32_16x16x32_bf16 v[108:111], a[48:51], a[136:139], v[108:111]\n"
"v_mfma_f32_16x16x32_bf16 v[112:115], a[48:51], a[144:147], v[112:115]\n"
"v_mfma_f32_16x16x32_bf16 v[112:115], a[52:55], a[148:151], v[112:115]\n"
"global_load_dwordx4 a[44:47], v0, s[34:35] offset:-1024 sc1\n"
"v_mfma_f32_16x16x32_bf16 v[140:143], a[60:63], a[148:151], v[140:143]\n"
"v_mfma_f32_16x16x32_bf16 v[140:143], a[56:59], a[144:147], v[140:143]\n"
"v_mfma_f32_16x16x32_bf16 a[12:15], a[56:59], a[152:155], a[12:15]\n"
"v_mfma_f32_16x16x32_bf16 a[12:15], a[60:63], a[156:159], a[12:15]\n"
"v_mfma_f32_16x16x32_bf16 a[8:11], a[52:55], a[156:159], a[8:11]\n"
"v_mfma_f32_16x16x32_bf16 a[8:11], a[48:51], a[152:155], a[8:11]\n"
"s_waitcnt vmcnt(16)\n"
"v_mfma_f32_16x16x32_bf16 v[144:147], a[64:67], a[96:99], v[144:147]\n"
"v_mfma_f32_16x16x32_bf16 v[144:147], a[68:71], a[100:103], v[144:147]\n"
"global_load_dwordx4 a[48:51], v0, s[34:35] offset:0 sc1\n"
"v_mfma_f32_16x16x32_bf16 v[172:175], a[76:79], a[100:103], v[172:175]\n"
"v_mfma_f32_16x16x32_bf16 v[172:175], a[72:75], a[96:99], v[172:175]\n"
"buffer_load_dwordx4 v1, s[48:51], s69 offen offset:3072 sc1 lds\n"
"v_mfma_f32_16x16x32_bf16 v[176:179], a[72:75], a[104:107], v[176:179]\n"
"v_mfma_f32_16x16x32_bf16 v[176:179], a[76:79], a[108:111], v[176:179]\n"
"v_mfma_f32_16x16x32_bf16 v[148:151], a[68:71], a[108:111], v[148:151]\n"
"v_mfma_f32_16x16x32_bf16 v[148:151], a[64:67], a[104:107], v[148:151]\n"
"v_mfma_f32_16x16x32_bf16 v[152:155], a[64:67], a[112:115], v[152:155]\n"
"v_mfma_f32_16x16x32_bf16 v[152:155], a[68:71], a[116:119], v[152:155]\n"
"global_load_dwordx4 a[52:55], v0, s[34:35] offset:1024 sc1\n"
"v_mfma_f32_16x16x32_bf16 v[180:183], a[76:79], a[116:119], v[180:183]\n"
"v_mfma_f32_16x16x32_bf16 v[180:183], a[72:75], a[112:115], v[180:183]\n"
"v_mfma_f32_16x16x32_bf16 v[184:187], a[72:75], a[120:123], v[184:187]\n"
"v_mfma_f32_16x16x32_bf16 v[184:187], a[76:79], a[124:127], v[184:187]\n"
"v_mfma_f32_16x16x32_bf16 v[156:159], a[68:71], a[124:127], v[156:159]\n"
"v_mfma_f32_16x16x32_bf16 v[156:159], a[64:67], a[120:123], v[156:159]\n"
"v_mfma_f32_16x16x32_bf16 v[160:163], a[64:67], a[128:131], v[160:163]\n"
"v_mfma_f32_16x16x32_bf16 v[160:163], a[68:71], a[132:135], v[160:163]\n"
"global_load_dwordx4 a[56:59], v0, s[34:35] offset:2048 sc1\n"
"v_mfma_f32_16x16x32_bf16 v[188:191], a[76:79], a[132:135], v[188:191]\n"
"v_mfma_f32_16x16x32_bf16 v[188:191], a[72:75], a[128:131], v[188:191]\n"
"v_mfma_f32_16x16x32_bf16 v[192:195], a[72:75], a[136:139], v[192:195]\n"
"v_mfma_f32_16x16x32_bf16 v[192:195], a[76:79], a[140:143], v[192:195]\n"
"v_mfma_f32_16x16x32_bf16 v[164:167], a[68:71], a[140:143], v[164:167]\n"
"v_mfma_f32_16x16x32_bf16 v[164:167], a[64:67], a[136:139], v[164:167]\n"
"v_mfma_f32_16x16x32_bf16 v[168:171], a[64:67], a[144:147], v[168:171]\n"
"v_mfma_f32_16x16x32_bf16 v[168:171], a[68:71], a[148:151], v[168:171]\n"
"global_load_dwordx4 a[60:63], v0, s[34:35] offset:3072 sc1\n"
"v_mfma_f32_16x16x32_bf16 v[196:199], a[76:79], a[148:151], v[196:199]\n"
"v_mfma_f32_16x16x32_bf16 v[196:199], a[72:75], a[144:147], v[196:199]\n"
"v_mfma_f32_16x16x32_bf16 a[20:23], a[72:75], a[152:155], a[20:23]\n"
"s_add_u32 s34, s34, 8192\n"
"v_mfma_f32_16x16x32_bf16 a[20:23], a[76:79], a[156:159], a[20:23]\n"
"v_mfma_f32_16x16x32_bf16 a[16:19], a[68:71], a[156:159], a[16:19]\n"
"v_mfma_f32_16x16x32_bf16 a[16:19], a[64:67], a[152:155], a[16:19]\n"
"s_waitcnt vmcnt(12)\n"
"v_mfma_f32_16x16x32_bf16 v[200:203], a[80:83], a[96:99], v[200:203]\n"
"v_mfma_f32_16x16x32_bf16 v[200:203], a[84:87], a[100:103], v[200:203]\n"
"global_load_dwordx4 a[64:67], v0, s[34:35] offset:-4096 sc1\n"
"v_mfma_f32_16x16x32_bf16 v[228:231], a[92:95], a[100:103], v[228:231]\n"
"ds_read_b128 a[100:103], v5 offset:1024\n"
"v_mfma_f32_16x16x32_bf16 v[228:231], a[88:91], a[96:99], v[228:231]\n"
"ds_read_b128 a[96:99], v5 offset:0\n"
"v_mfma_f32_16x16x32_bf16 v[232:235], a[88:91], a[104:107], v[232:235]\n"
"v_mfma_f32_16x16x32_bf16 v[232:235], a[92:95], a[108:111], v[232:235]\n"
"v_mfma_f32_16x16x32_bf16 v[204:207], a[84:87], a[108:111], v[204:207]\n"
"ds_read_b128 a[108:111], v5 offset:3072\n"
"v_mfma_f32_16x16x32_bf16 v[204:207], a[80:83], a[104:107], v[204:207]\n"
"ds_read_b128 a[104:107], v5 offset:2048\n"
"v_mfma_f32_16x16x32_bf16 v[208:211], a[80:83], a[112:115], v[208:211]\n"
"v_mfma_f32_16x16x32_bf16 v[208:211], a[84:87], a[116:119], v[208:211]\n"
"global_load_dwordx4 a[68:71], v0, s[34:35] offset:-3072 sc1\n"
"v_mfma_f32_16x16x32_bf16 v[236:239], a[92:95], a[116:119], v[236:239]\n"
"ds_read_b128 a[116:119], v5 offset:5120\n"
"v_mfma_f32_16x16x32_bf16 v[236:239], a[88:91], a[112:115], v[236:239]\n"
"ds_read_b128 a[112:115], v5 offset:4096\n"
"v_mfma_f32_16x16x32_bf16 v[240:243], a[88:91], a[120:123], v[240:243]\n"
"v_mfma_f32_16x16x32_bf16 v[240:243], a[92:95], a[124:127], v[240:243]\n"
"v_mfma_f32_16x16x32_bf16 v[212:215], a[84:87], a[124:127], v[212:215]\n"
"ds_read_b128 a[124:127], v5 offset:7168\n"
"v_mfma_f32_16x16x32_bf16 v[212:215], a[80:83], a[120:123], v[212:215]\n"
"ds_read_b128 a[120:123], v5 offset:6144\n"
"v_mfma_f32_16x16x32_bf16 v[216:219], a[80:83], a[128:131], v[216:219]\n"
"v_mfma_f32_16x16x32_bf16 v[216:219], a[84:87], a[132:135], v[216:219]\n"
"global_load_dwordx4 a[72:75], v0, s[34:35] offset:-2048 sc1\n"
"v_mfma_f32_16x16x32_bf16 v[244:247], a[92:95], a[132:135], v[244:247]\n"
"ds_read_b128 a[132:135], v5 offset:9216\n"
"v_mfma_f32_16x16x32_bf16 v[244:247], a[88:91], a[128:131], v[244:247]\n"
"ds_read_b128 a[128:131], v5 offset:8192\n"
"v_mfma_f32_16x16x32_bf16 v[248:251], a[88:91], a[136:139], v[248:251]\n"
"v_mfma_f32_16x16x32_bf16 v[248:251], a[92:95], a[140:143], v[248:251]\n"
"v_mfma_f32_16x16x32_bf16 v[220:223], a[84:87], a[140:143], v[220:223]\n"
"ds_read_b128 a[140:143], v5 offset:11264\n"
"v_mfma_f32_16x16x32_bf16 v[220:223], a[80:83], a[136:139], v[220:223]\n"
"ds_read_b128 a[136:139], v5 offset:10240\n"
"v_mfma_f32_16x16x32_bf16 v[224:227], a[80:83], a[144:147], v[224:227]\n"
"v_mfma_f32_16x16x32_bf16 v[224:227], a[84:87], a[148:151], v[224:227]\n"
"global_load_dwordx4 a[76:79], v0, s[34:35] offset:-1024 sc1\n"
"v_mfma_f32_16x16x32_bf16 v[252:255], a[92:95], a[148:151], v[252:255]\n"
"ds_read_b128 a[148:151], v5 offset:13312\n"
"v_mfma_f32_16x16x32_bf16 v[252:255], a[88:91], a[144:147], v[252:255]\n"
"ds_read_b128 a[144:147], v5 offset:12288\n"
"v_mfma_f32_16x16x32_bf16 a[28:31], a[88:91], a[152:155], a[28:31]\n"
"v_mfma_f32_16x16x32_bf16 a[28:31], a[92:95], a[156:159], a[28:31]\n"
"v_mfma_f32_16x16x32_bf16 a[24:27], a[84:87], a[156:159], a[24:27]\n"
"ds_read_b128 a[156:159], v5 offset:15360\n"
"v_mfma_f32_16x16x32_bf16 a[24:27], a[80:83], a[152:155], a[24:27]\n"
"s_waitcnt vmcnt(9) lgkmcnt(11)\n"
"ds_read_b128 a[152:155], v5 offset:14336\n"
"v_mfma_f32_16x16x32_bf16 v[32:35], a[32:35], a[96:99], v[32:35]\n"
"v_mfma_f32_16x16x32_bf16 v[32:35], a[36:39], a[100:103], v[32:35]\n"
"global_load_dwordx4 a[80:83], v0, s[34:35] offset:0 sc1\n"
"v_mfma_f32_16x16x32_bf16 v[60:63], a[44:47], a[100:103], v[60:63]\n"
"v_mfma_f32_16x16x32_bf16 v[60:63], a[40:43], a[96:99], v[60:63]\n"
"v_mfma_f32_16x16x32_bf16 v[64:67], a[40:43], a[104:107], v[64:67]\n"
"v_mfma_f32_16x16x32_bf16 v[64:67], a[44:47], a[108:111], v[64:67]\n"
"v_mfma_f32_16x16x32_bf16 v[36:39], a[36:39], a[108:111], v[36:39]\n"
"s_add_u32 s67, s78, 2\n"
"v_mfma_f32_16x16x32_bf16 v[36:39], a[32:35], a[104:107], v[36:39]\n"
"s_waitcnt lgkmcnt(8)\n"
"v_mfma_f32_16x16x32_bf16 v[40:43], a[32:35], a[112:115], v[40:43]\n"
"v_mfma_f32_16x16x32_bf16 v[40:43], a[36:39], a[116:119], v[40:43]\n"
"global_load_dwordx4 a[84:87], v0, s[34:35] offset:1024 sc1\n"
"v_mfma_f32_16x16x32_bf16 v[68:71], a[44:47], a[116:119], v[68:71]\n"
"v_mfma_f32_16x16x32_bf16 v[68:71], a[40:43], a[112:115], v[68:71]\n"
"v_mfma_f32_16x16x32_bf16 v[72:75], a[40:43], a[120:123], v[72:75]\n"
"v_mfma_f32_16x16x32_bf16 v[72:75], a[44:47], a[124:127], v[72:75]\n"
"v_mfma_f32_16x16x32_bf16 v[44:47], a[36:39], a[124:127], v[44:47]\n"
"v_mfma_f32_16x16x32_bf16 v[44:47], a[32:35], a[120:123], v[44:47]\n"
"s_waitcnt lgkmcnt(4)\n"
"v_mfma_f32_16x16x32_bf16 v[48:51], a[32:35], a[128:131], v[48:51]\n"
"s_lshr_b32 s66, 20, s62\n"
"v_mfma_f32_16x16x32_bf16 v[48:51], a[36:39], a[132:135], v[48:51]\n"
"global_load_dwordx4 a[88:91], v0, s[34:35] offset:2048 sc1\n"
"v_mfma_f32_16x16x32_bf16 v[76:79], a[44:47], a[132:135], v[76:79]\n"
"s_and_b32 s66, s66, 1\n"
"s_cselect_b32 s70, 4294574080, 0\n"
"s_cselect_b32 s71, -1, 0\n"
"s_cselect_b32 s66, 0, 131072\n"
"v_mfma_f32_16x16x32_bf16 v[76:79], a[40:43], a[128:131], v[76:79]\n"
"v_mfma_f32_16x16x32_bf16 v[80:83], a[40:43], a[136:139], v[80:83]\n"
"s_add_u32 s70, s70, s66\n"
"v_subrev_u32 v12, s67, v12\n"
"v_mfma_f32_16x16x32_bf16 v[80:83], a[44:47], a[140:143], v[80:83]\n"
"v_xor_b32 v13, s79, v13\n"
"v_mfma_f32_16x16x32_bf16 v[52:55], a[36:39], a[140:143], v[52:55]\n"
"v_subrev_u32 v14, s67, v14\n"
"v_mfma_f32_16x16x32_bf16 v[52:55], a[32:35], a[136:139], v[52:55]\n"
"s_waitcnt lgkmcnt(0)\n"
"v_xor_b32 v15, s79, v15\n"
"v_mfma_f32_16x16x32_bf16 v[56:59], a[32:35], a[144:147], v[56:59]\n"
"v_or_b32 v12, v12, v14\n"
"v_mfma_f32_16x16x32_bf16 v[56:59], a[36:39], a[148:151], v[56:59]\n"
"global_load_dwordx4 a[92:95], v0, s[34:35] offset:3072 sc1\n"
"v_or_b32 v13, v13, v15\n"
"v_mfma_f32_16x16x32_bf16 v[84:87], a[44:47], a[148:151], v[84:87]\n"
"v_or_b32 v12, v12, v13\n"
"v_mfma_f32_16x16x32_bf16 v[84:87], a[40:43], a[144:147], v[84:87]\n"
"v_cmp_lt_u32 vcc, 15, v12\n"
"v_mfma_f32_16x16x32_bf16 a[4:7], a[40:43], a[152:155], a[4:7]\n"
"s_add_u32 s34, s34, 8192\n"
"s_cbranch_vccnz L_slow2\n"
"L_back2:\n"
"v_mfma_f32_16x16x32_bf16 a[4:7], a[44:47], a[156:159], a[4:7]\n"
"v_mfma_f32_16x16x32_bf16 a[0:3], a[36:39], a[156:159], a[0:3]\n"
"v_mfma_f32_16x16x32_bf16 a[0:3], a[32:35], a[152:155], a[0:3]\n"
"s_waitcnt vmcnt(8)\n"
"v_mfma_f32_16x16x32_bf16 v[88:91], a[48:51], a[96:99], v[88:91]\n"
"v_mfma_f32_16x16x32_bf16 v[88:91], a[52:55], a[100:103], v[88:91]\n"
"global_load_dwordx4 a[32:35], v0, s[34:35] offset:-4096 sc1\n"
"v_mfma_f32_16x16x32_bf16 v[116:119], a[60:63], a[100:103], v[116:119]\n"
"v_mfma_f32_16x16x32_bf16 v[116:119], a[56:59], a[96:99], v[116:119]\n"
"v_mfma_f32_16x16x32_bf16 v[120:123], a[56:59], a[104:107], v[120:123]\n"
"v_mfma_f32_16x16x32_bf16 v[120:123], a[60:63], a[108:111], v[120:123]\n"
"v_mfma_f32_16x16x32_bf16 v[92:95], a[52:55], a[108:111], v[92:95]\n"
"v_mfma_f32_16x16x32_bf16 v[92:95], a[48:51], a[104:107], v[92:95]\n"
"v_mfma_f32_16x16x32_bf16 v[96:99], a[48:51], a[112:115], v[96:99]\n"
"v_mfma_f32_16x16x32_bf16 v[96:99], a[52:55], a[116:119], v[96:99]\n"
"global_load_dwordx4 a[36:39], v0, s[34:35] offset:-3072 sc1\n"
"v_mfma_f32_16x16x32_bf16 v[124:127], a[60:63], a[116:119], v[124:127]\n"
"v_mfma_f32_16x16x32_bf16 v[124:127], a[56:59], a[112:115], v[124:127]\n"
"v_mfma_f32_16x16x32_bf16 v[128:131], a[56:59], a[120:123], v[128:131]\n"
"v_mfma_f32_16x16x32_bf16 v[128:131], a[60:63], a[124:127], v[128:131]\n"
"v_mfma_f32_16x16x32_bf16 v[100:103], a[52:55], a[124:127], v[100:103]\n"
"v_mfma_f32_16x16x32_bf16 v[100:103], a[48:51], a[120:123], v[100:103]\n"
"v_mfma_f32_16x16x32_bf16 v[104:107], a[48:51], a[128:131], v[104:107]\n"
"v_mfma_f32_16x16x32_bf16 v[104:107], a[52:55], a[132:135], v[104:107]\n"
"global_load_dwordx4 a[40:43], v0, s[34:35] offset:-2048 sc1\n"
"v_mfma_f32_16x16x32_bf16 v[132:135], a[60:63], a[132:135], v[132:135]\n"
"v_mfma_f32_16x16x32_bf16 v[132:135], a[56:59], a[128:131], v[132:135]\n"
"v_mfma_f32_16x16x32_bf16 v[136:139], a[56:59], a[136:139], v[136:139]\n"
"v_mfma_f32_16x16x32_bf16 v[136:139], a[60:63], a[140:143], v[136:139]\n"
"v_mfma_f32_16x16x32_bf16 v[108:111], a[52:55], a[140:143], v[108:111]\n"
"v_mfma_f32_16x16x32_bf16 v[108:111], a[48:51], a[136:139], v[108:111]\n"
"v_mfma_f32_16x16x32_bf16 v[112:115], a[48:51], a[144:147], v[112:115]\n"
"v_mfma_f32_16x16x32_bf16 v[112:115], a[52:55], a[148:151], v[112:115]\n"
"global_load_dwordx4 a[44:47], v0, s[34:35] offset:-1024 sc1\n"
"v_mfma_f32_16x16x32_bf16 v[140:143], a[60:63], a[148:151], v[140:143]\n"
"v_mfma_f32_16x16x32_bf16 v[140:143], a[56:59], a[144:147], v[140:143]\n"
"v_mfma_f32_16x16x32_bf16 a[12:15], a[56:59], a[152:155], a[12:15]\n"
"v_mfma_f32_16x16x32_bf16 a[12:15], a[60:63], a[156:159], a[12:15]\n"
"v_mfma_f32_16x16x32_bf16 a[8:11], a[52:55], a[156:159], a[8:11]\n"
"v_mfma_f32_16x16x32_bf16 a[8:11], a[48:51], a[152:155], a[8:11]\n"
"s_waitcnt vmcnt(8)\n"
"v_mfma_f32_16x16x32_bf16 v[144:147], a[64:67], a[96:99], v[144:147]\n"
"v_mfma_f32_16x16x32_bf16 v[144:147], a[68:71], a[100:103], v[144:147]\n"
"global_load_dwordx4 a[48:51], v0, s[34:35] offset:0 sc1\n"
"v_mfma_f32_16x16x32_bf16 v[172:175], a[76:79], a[100:103], v[172:175]\n"
"v_mfma_f32_16x16x32_bf16 v[172:175], a[72:75], a[96:99], v[172:175]\n"
"v_mfma_f32_16x16x32_bf16 v[176:179], a[72:75], a[104:107], v[176:179]\n"
"v_mfma_f32_16x16x32_bf16 v[176:179], a[76:79], a[108:111], v[176:179]\n"
"v_mfma_f32_16x16x32_bf16 v[148:151], a[68:71], a[108:111], v[148:151]\n"
"v_mfma_f32_16x16x32_bf16 v[148:151], a[64:67], a[104:107], v[148:151]\n"
"v_mfma_f32_16x16x32_bf16 v[152:155], a[64:67], a[112:115], v[152:155]\n"
"v_mfma_f32_16x16x32_bf16 v[152:155], a[68:71], a[116:119], v[152:155]\n"
"global_load_dwordx4 a[52:55], v0, s[34:35] offset:1024 sc1\n"
"v_mfma_f32_16x16x32_bf16 v[180:183], a[76:79], a[116:119], v[180:183]\n"
"v_mfma_f32_16x16x32_bf16 v[180:183], a[72:75], a[112:115], v[180:183]\n"
"v_mfma_f32_16x16x32_bf16 v[184:187], a[72:75], a[120:123], v[184:187]\n"
"v_mfma_f32_16x16x32_bf16 v[184:187], a[76:79], a[124:127], v[184:187]\n"
"v_mfma_f32_16x16x32_bf16 v[156:159], a[68:71], a[124:127], v[156:159]\n"
"v_mfma_f32_16x16x32_bf16 v[156:159], a[64:67], a[120:123], v[156:159]\n"
"v_mfma_f32_16x16x32_bf16 v[160:163], a[64:67], a[128:131], v[160:163]\n"
"v_mfma_f32_16x16x32_bf16 v[160:163], a[68:71], a[132:135], v[160:163]\n"
"global_load_dwordx4 a[56:59], v0, s[34:35] offset:2048 sc1\n"
"v_mfma_f32_16x16x32_bf16 v[188:191], a[76:79], a[132:135], v[188:191]\n"
"v_mfma_f32_16x16x32_bf16 v[188:191], a[72:75], a[128:131], v[188:191]\n"
"v_mfma_f32_16x16x32_bf16 v[192:195], a[72:75], a[136:139], v[192:195]\n"
"v_mfma_f32_16x16x32_bf16 v[192:195], a[76:79], a[140:143], v[192:195]\n"
"v_mfma_f32_16x16x32_bf16 v[164:167], a[68:71], a[140:143], v[164:167]\n"
"v_mfma_f32_16x16x32_bf16 v[164:167], a[64:67], a[136:139], v[164:167]\n"
"v_mfma_f32_16x16x32_bf16 v[168:171], a[64:67], a[144:147], v[168:171]\n"
"v_mfma_f32_16x16x32_bf16 v[168:171], a[68:71], a[148:151], v[168:171]\n"
"global_load_dwordx4 a[60:63], v0, s[34:35] offset:3072 sc1\n"
"v_mfma_f32_16x16x32_bf16 v[196:199], a[76:79], a[148:151], v[196:199]\n"
"v_mfma_f32_16x16x32_bf16 v[196:199], a[72:75], a[144:147], v[196:199]\n"
"v_mfma_f32_16x16x32_bf16 a[20:23], a[72:75], a[152:155], a[20:23]\n"
"s_add_u32 s34, s34, 8192\n"
"v_mfma_f32_16x16x32_bf16 a[20:23], a[76:79], a[156:159], a[20:23]\n"
"v_mfma_f32_16x16x32_bf16 a[16:19], a[68:71], a[156:159], a[16:19]\n"
"v_mfma_f32_16x16x32_bf16 a[16:19], a[64:67], a[152:155], a[16:19]\n"
"s_waitcnt vmcnt(8)\n"
"v_mfma_f32_16x16x32_bf16 v[200:203], a[80:83], a[96:99], v[200:203]\n"
"v_mfma_f32_16x16x32_bf16 v[200:203], a[84:87], a[100:103], v[200:203]\n"
"global_load_dwordx4 a[64:67], v0, s[34:35] offset:-4096 sc1\n"
"v_mfma_f32_16x16x32_bf16 v[228:231], a[92:95], a[100:103], v[228:231]\n"
"ds_read_b128 a[100:103], v2 offset:17408\n"
"v_mfma_f32_16x16x32_bf16 v[228:231], a[88:91], a[96:99], v[228:231]\n"
"ds_read_b128 a[96:99], v2 offset:16384\n"
"v_mfma_f32_16x16x32_bf16 v[232:235], a[88:91], a[104:107], v[232:235]\n"
"v_mfma_f32_16x16x32_bf16 v[232:235], a[92:95], a[108:111], v[232:235]\n"
"v_mfma_f32_16x16x32_bf16 v[204:207], a[84:87], a[108:111], v[204:207]\n"
"ds_read_b128 a[108:111], v2 offset:19456\n"
"v_mfma_f32_16x16x32_bf16 v[204:207], a[80:83], a[104:107], v[204:207]\n"
"ds_read_b128 a[104:107], v2 offset:18432\n"
"v_mfma_f32_16x16x32_bf16 v[208:211], a[80:83], a[112:115], v[208:211]\n"
"v_mfma_f32_16x16x32_bf16 v[208:211], a[84:87], a[116:119], v[208:211]\n"
"global_load_dwordx4 a[68:71], v0, s[34:35] offset:-3072 sc1\n"
"v_mfma_f32_16x16x32_bf16 v[236:239], a[92:95], a[116:119], v[236:239]\n"
"ds_read_b128 a[116:119], v2 offset:21504\n"
"v_mfma_f32_16x16x32_bf16 v[236:239], a[88:91], a[112:115], v[236:239]\n"
"ds_read_b128 a[112:115], v2 offset:20480\n"
"v_mfma_f32_16x16x32_bf16 v[240:243], a[88:91], a[120:123], v[240:243]\n"
"v_mfma_f32_16x16x32_bf16 v[240:243], a[92:95], a[124:127], v[240:243]\n"
"v_mfma_f32_16x16x32_bf16 v[212:215], a[84:87], a[124:127], v[212:215]\n"
"ds_read_b128 a[124:127], v2 offset:23552\n"
"v_mfma_f32_16x16x32_bf16 v[212:215], a[80:83], a[120:123], v[212:215]\n"
"ds_read_b128 a[120:123], v2 offset:22528\n"
"v_mfma_f32_16x16x32_bf16 v[216:219], a[80:83], a[128:131], v[216:219]\n"
"v_mfma_f32_16x16x32_bf16 v[216:219], a[84:87], a[132:135], v[216:219]\n"
"global_load_dwordx4 a[72:75], v0, s[34:35] offset:-2048 sc1\n"
"v_mfma_f32_16x16x32_bf16 v[244:247], a[92:95], a[132:135], v[244:247]\n"
"ds_read_b128 a[132:135], v2 offset:25600\n"
"v_mfma_f32_16x16x32_bf16 v[244:247], a[88:91], a[128:131], v[244:247]\n"
"ds_read_b128 a[128:131], v2 offset:24576\n"
"v_mfma_f32_16x16x32_bf16 v[248:251], a[88:91], a[136:139], v[248:251]\n"
"v_mfma_f32_16x16x32_bf16 v[248:251], a[92:95], a[140:143], v[248:251]\n"
"v_mfma_f32_16x16x32_bf16 v[220:223], a[84:87], a[140:143], v[220:223]\n"
"ds_read_b128 a[140:143], v2 offset:27648\n"
"v_mfma_f32_16x16x32_bf16 v[220:223], a[80:83], a[136:139], v[220:223]\n"
"ds_read_b128 a[136:139], v2 offset:26624\n"
"v_mfma_f32_16x16x32_bf16 v[224:227], a[80:83], a[144:147], v[224:227]\n"
"v_mfma_f32_16x16x32_bf16 v[224:227], a[84:87], a[148:151], v[224:227]\n"
"global_load_dwordx4 a[76:79], v0, s[34:35] offset:-1024 sc1\n"
"v_mfma_f32_16x16x32_bf16 v[252:255], a[92:95], a[148:151], v[252:255]\n"
"ds_read_b128 a[148:151], v2 offset:29696\n"
"v_mfma_f32_16x16x32_bf16 v[252:255], a[88:91], a[144:147], v[252:255]\n"
"ds_read_b128 a[144:147], v2 offset:28672\n"
"v_mfma_f32_16x16x32_bf16 a[28:31], a[88:91], a[152:155], a[28:31]\n"
"v_mfma_f32_16x16x32_bf16 a[28:31], a[92:95], a[156:159], a[28:31]\n"
"v_mfma_f32_16x16x32_bf16 a[24:27], a[84:87], a[156:159], a[24:27]\n"
"ds_read_b128 a[156:159], v2 offset:31744\n"
"v_mfma_f32_16x16x32_bf16 a[24:27], a[80:83], a[152:155], a[24:27]\n"
"ds_read_b128 a[152:155], v2 offset:30720\n"
"L_P5:\n"
"s_waitcnt vmcnt(8) lgkmcnt(12)\n"
"v_mfma_f32_16x16x32_bf16 v[32:35], a[32:35], a[96:99], v[32:35]\n"
"v_mfma_f32_16x16x32_bf16 v[32:35], a[36:39], a[100:103], v[32:35]\n"
"global_load_dwordx4 a[80:83], v0, s[34:35] offset:0 sc1\n"
"v_mfma_f32_16x16x32_bf16 v[60:63], a[44:47], a[100:103], v[60:63]\n"
"s_barrier\n"
"v_mfma_f32_16x16x32_bf16 v[60:63], a[40:43], a[96:99], v[60:63]\n"
"s_add_u32 s48, s48, s70\n"
"s_addc_u32 s49, s49, s71\n"
"v_mfma_f32_16x16x32_bf16 v[64:67], a[40:43], a[104:107], v[64:67]\n"
"v_mfma_f32_16x16x32_bf16 v[64:67], a[44:47], a[108:111], v[64:67]\n"
"v_mfma_f32_16x16x32_bf16 v[36:39], a[36:39], a[108:111], v[36:39]\n"
"s_add_u32 s52, s52, s70\n"
"s_addc_u32 s53, s53, s71\n"
"v_mfma_f32_16x16x32_bf16 v[36:39], a[32:35], a[104:107], v[36:39]\n"
"s_waitcnt lgkmcnt(8)\n"
"v_mfma_f32_16x16x32_bf16 v[40:43], a[32:35], a[112:115], v[40:43]\n"
"s_add_u32 s54, s54, s70\n"
"s_addc_u32 s55, s55, s71\n"
"v_mfma_f32_16x16x32_bf16 v[40:43], a[36:39], a[116:119], v[40:43]\n"
"global_load_dwordx4 a[84:87], v0, s[34:35] offset:1024 sc1\n"
"v_mfma_f32_16x16x32_bf16 v[68:71], a[44:47], a[116:119], v[68:71]\n"
"v_mfma_f32_16x16x32_bf16 v[68:71], a[40:43], a[112:115], v[68:71]\n"
"v_mfma_f32_16x16x32_bf16 v[72:75], a[40:43], a[120:123], v[72:75]\n"
"v_mfma_f32_16x16x32_bf16 v[72:75], a[44:47], a[124:127], v[72:75]\n"
"s_add_u32 s61, s61, 2048\n"
"v_mfma_f32_16x16x32_bf16 v[44:47], a[36:39], a[124:127], v[44:47]\n"
"v_mfma_f32_16x16x32_bf16 v[44:47], a[32:35], a[120:123], v[44:47]\n"
"s_waitcnt lgkmcnt(4)\n"
"v_mfma_f32_16x16x32_bf16 v[48:51], a[32:35], a[128:131], v[48:51]\n"
"s_waitcnt vmcnt(8)\n"
"v_lshlrev_b32 v16, 16, v18\n"
"v_mfma_f32_16x16x32_bf16 v[48:51], a[36:39], a[132:135], v[48:51]\n"
"global_load_dwordx4 a[88:91], v0, s[34:35] offset:2048 sc1\n"
"v_and_b32 v17, s63, v18\n"
"v_mfma_f32_16x16x32_bf16 v[76:79], a[44:47], a[132:135], v[76:79]\n"
"v_lshlrev_b32 v18, 16, v19\n"
"v_mfma_f32_16x16x32_bf16 v[76:79], a[40:43], a[128:131], v[76:79]\n"
"v_and_b32 v19, s63, v19\n"
"v_mfma_f32_16x16x32_bf16 v[80:83], a[40:43], a[136:139], v[80:83]\n"
"v_add_f32 v144, v144, v16\n"
"v_add_f32 v145, v145, v17\n"
"v_mfma_f32_16x16x32_bf16 v[80:83], a[44:47], a[140:143], v[80:83]\n"
"v_add_f32 v146, v146, v18\n"
"v_mfma_f32_16x16x32_bf16 v[52:55], a[36:39], a[140:143], v[52:55]\n"
"v_add_f32 v147, v147, v19\n"
"v_mfma_f32_16x16x32_bf16 v[52:55], a[32:35], a[136:139], v[52:55]\n"
"s_waitcnt lgkmcnt(0)\n"
"v_add_f32 v148, v148, v16\n"
"v_mfma_f32_16x16x32_bf16 v[56:59], a[32:35], a[144:147], v[56:59]\n"
"v_add_f32 v149, v149, v17\n"
"v_mfma_f32_16x16x32_bf16 v[56:59], a[36:39], a[148:151], v[56:59]\n"
"global_load_dwordx4 a[92:95], v0, s[34:35] offset:3072 sc1\n"
"v_add_f32 v150, v150, v18\n"
"v_mfma_f32_16x16x32_bf16 v[84:87], a[44:47], a[148:151], v[84:87]\n"
"v_add_f32 v151, v151, v19\n"
"v_mfma_f32_16x16x32_bf16 v[84:87], a[40:43], a[144:147], v[84:87]\n"
"v_add_f32 v152, v152, v16\n"
"v_mfma_f32_16x16x32_bf16 a[4:7], a[40:43], a[152:155], a[4:7]\n"
"s_add_u32 s34, s34, 8192\n"
"v_add_f32 v153, v153, v17\n"
"v_mfma_f32_16x16x32_bf16 a[4:7], a[44:47], a[156:159], a[4:7]\n"
"v_add_f32 v154, v154, v18\n"
"v_mfma_f32_16x16x32_bf16 a[0:3], a[36:39], a[156:159], a[0:3]\n"
"v_add_f32 v155, v155, v19\n"
"v_add_f32 v156, v156, v16\n"
"v_mfma_f32_16x16x32_bf16 a[0:3], a[32:35], a[152:155], a[0:3]\n"
"s_waitcnt vmcnt(8)\n"
"v_add_f32 v157, v157, v17\n"
"v_mfma_f32_16x16x32_bf16 v[88:91], a[48:51], a[96:99], v[88:91]\n"
"v_add_f32 v158, v158, v18\n"
"v_mfma_f32_16x16x32_bf16 v[88:91], a[52:55], a[100:103], v[88:91]\n"
"global_load_dwordx4 a[32:35], v0, s[34:35] offset:-4096 sc1\n"
"v_add_f32 v159, v159, v19\n"
"v_mfma_f32_16x16x32_bf16 v[116:119], a[60:63], a[100:103], v[116:119]\n"
"ds_read_b128 a[100:103], v3 offset:17408\n"
"v_add_f32 v160, v160, v16\n"
"v_mfma_f32_16x16x32_bf16 v[116:119], a[56:59], a[96:99], v[116:119]\n"
"ds_read_b128 a[96:99], v3 offset:16384\n"
"v_add_f32 v161, v161, v17\n"
"v_mfma_f32_16x16x32_bf16 v[120:123], a[56:59], a[104:107], v[120:123]\n"
"v_add_f32 v162, v162, v18\n"
"v_mfma_f32_16x16x32_bf16 v[120:123], a[60:63], a[108:111], v[120:123]\n"
"v_add_f32 v163, v163, v19\n"
"v_mfma_f32_16x16x32_bf16 v[92:95], a[52:55], a[108:111], v[92:95]\n"
"ds_read_b128 a[108:111], v3 offset:19456\n"
"v_add_f32 v164, v164, v16\n"
"v_mfma_f32_16x16x32_bf16 v[92:95], a[48:51], a[104:107], v[92:95]\n"
"ds_read_b128 a[104:107], v3 offset:18432\n"
"v_add_f32 v165, v165, v17\n"
"v_mfma_f32_16x16x32_bf16 v[96:99], a[48:51], a[112:115], v[96:99]\n"
"v_add_f32 v166, v166, v18\n"
"v_add_f32 v167, v167, v19\n"
"v_mfma_f32_16x16x32_bf16 v[96:99], a[52:55], a[116:119], v[96:99]\n"
"global_load_dwordx4 a[36:39], v0, s[34:35] offset:-3072 sc1\n"
"v_add_f32 v168, v168, v16\n"
"v_mfma_f32_16x16x32_bf16 v[124:127], a[60:63], a[116:119], v[124:127]\n"
"ds_read_b128 a[116:119], v3 offset:21504\n"
"v_add_f32 v169, v169, v17\n"
"v_mfma_f32_16x16x32_bf16 v[124:127], a[56:59], a[112:115], v[124:127]\n"
"ds_read_b128 a[112:115], v3 offset:20480\n"
"v_add_f32 v170, v170, v18\n"
"v_mfma_f32_16x16x32_bf16 v[128:131], a[56:59], a[120:123], v[128:131]\n"
"v_add_f32 v171, v171, v19\n"
"v_mfma_f32_16x16x32_bf16 v[128:131], a[60:63], a[124:127], v[128:131]\n"
"v_accvgpr_read_b32 v12, a16\n"
"v_mfma_f32_16x16x32_bf16 v[100:103], a[52:55], a[124:127], v[100:103]\n"
"ds_read_b128 a[124:127], v3 offset:23552\n"
"v_accvgpr_read_b32 v13, a17\n"
"v_mfma_f32_16x16x32_bf16 v[100:103], a[48:51], a[120:123], v[100:103]\n"
"ds_read_b128 a[120:123], v3 offset:22528\n"
"v_accvgpr_read_b32 v14, a18\n"
"v_mfma_f32_16x16x32_bf16 v[104:107], a[48:51], a[128:131], v[104:107]\n"
"v_accvgpr_read_b32 v15, a19\n"
"v_add_f32 v12, v12, v16\n"
"v_mfma_f32_16x16x32_bf16 v[104:107], a[52:55], a[132:135], v[104:107]\n"
"global_load_dwordx4 a[40:43], v0, s[34:35] offset:-2048 sc1\n"
"v_add_f32 v13, v13, v17\n"
"v_mfma_f32_16x16x32_bf16 v[132:135], a[60:63], a[132:135], v[132:135]\n"
"ds_read_b128 a[132:135], v3 offset:25600\n"
"v_add_f32 v14, v14, v18\n"
"v_mfma_f32_16x16x32_bf16 v[132:135], a[56:59], a[128:131], v[132:135]\n"
"ds_read_b128 a[128:131], v3 offset:24576\n"
"v_add_f32 v15, v15, v19\n"
"v_mfma_f32_16x16x32_bf16 v[136:139], a[56:59], a[136:139], v[136:139]\n"
"v_accvgpr_write_b32 a16, v12\n"
"v_mfma_f32_16x16x32_bf16 v[136:139], a[60:63], a[140:143], v[136:139]\n"
"v_accvgpr_write_b32 a17, v13\n"
"v_mfma_f32_16x16x32_bf16 v[108:111], a[52:55], a[140:143], v[108:111]\n"
"ds_read_b128 a[140:143], v3 offset:27648\n"
"v_accvgpr_write_b32 a18, v14\n"
"v_mfma_f32_16x16x32_bf16 v[108:111], a[48:51], a[136:139], v[108:111]\n"
"ds_read_b128 a[136:139], v3 offset:26624\n"
"v_accvgpr_write_b32 a19, v15\n"
"v_mfma_f32_16x16x32_bf16 v[112:115], a[48:51], a[144:147], v[112:115]\n"
"v_lshlrev_b32 v20, 16, v22\n"
"v_and_b32 v21, s63, v22\n"
"v_mfma_f32_16x16x32_bf16 v[112:115], a[52:55], a[148:151], v[112:115]\n"
"global_load_dwordx4 a[44:47], v0, s[34:35] offset:-1024 sc1\n"
"v_lshlrev_b32 v22, 16, v23\n"
"v_mfma_f32_16x16x32_bf16 v[140:143], a[60:63], a[148:151], v[140:143]\n"
"ds_read_b128 a[148:151], v3 offset:29696\n"
"v_and_b32 v23, s63, v23\n"
"v_mfma_f32_16x16x32_bf16 v[140:143], a[56:59], a[144:147], v[140:143]\n"
"ds_read_b128 a[144:147], v3 offset:28672\n"
"v_add_f32 v172, v172, v20\n"
"v_mfma_f32_16x16x32_bf16 a[12:15], a[56:59], a[152:155], a[12:15]\n"
"v_add_f32 v173, v173, v21\n"
"v_mfma_f32_16x16x32_bf16 a[12:15], a[60:63], a[156:159], a[12:15]\n"
"v_add_f32 v174, v174, v22\n"
"v_mfma_f32_16x16x32_bf16 a[8:11], a[52:55], a[156:159], a[8:11]\n"
"ds_read_b128 a[156:159], v3 offset:31744\n"
"v_add_f32 v175, v175, v23\n"
"v_mfma_f32_16x16x32_bf16 a[8:11], a[48:51], a[152:155], a[8:11]\n"
"s_waitcnt vmcnt(8) lgkmcnt(11)\n"
"ds_read_b128 a[152:155], v3 offset:30720\n"
"v_add_f32 v176, v176, v20\n"
"v_mfma_f32_16x16x32_bf16 v[32:35], a[64:67], a[96:99], v[32:35]\n"
"v_add_f32 v177, v177, v21\n"
"v_mfma_f32_16x16x32_bf16 v[32:35], a[68:71], a[100:103], v[32:35]\n"
"global_load_dwordx4 a[48:51], v0, s[34:35] offset:0 sc1\n"
"v_add_f32 v178, v178, v22\n"
"v_mfma_f32_16x16x32_bf16 v[60:63], a[76:79], a[100:103], v[60:63]\n"
"v_add_f32 v179, v179, v23\n"
"v_mfma_f32_16x16x32_bf16 v[60:63], a[72:75], a[96:99], v[60:63]\n"
"v_add_f32 v180, v180, v20\n"
"v_add_f32 v181, v181, v21\n"
"v_mfma_f32_16x16x32_bf16 v[64:67], a[72:75], a[104:107], v[64:67]\n"
"v_add_f32 v182, v182, v22\n"
"v_mfma_f32_16x16x32_bf16 v[64:67], a[76:79], a[108:111], v[64:67]\n"
"v_add_f32 v183, v183, v23\n"
"v_mfma_f32_16x16x32_bf16 v[36:39], a[68:71], a[108:111], v[36:39]\n"
"v_add_f32 v184, v184, v20\n"
"v_mfma_f32_16x16x32_bf16 v[36:39], a[64:67], a[104:107], v[36:39]\n"
"s_waitcnt lgkmcnt(8)\n"
"v_add_f32 v185, v185, v21\n"
"v_mfma_f32_16x16x32_bf16 v[40:43], a[64:67], a[112:115], v[40:43]\n"
"v_add_f32 v186, v186, v22\n"
"v_mfma_f32_16x16x32_bf16 v[40:43], a[68:71], a[116:119], v[40:43]\n"
"global_load_dwordx4 a[52:55], v0, s[34:35] offset:1024 sc1\n"
"v_add_f32 v187, v187, v23\n"
"v_mfma_f32_16x16x32_bf16 v[68:71], a[76:79], a[116:119], v[68:71]\n"
"v_add_f32 v188, v188, v20\n"
"v_mfma_f32_16x16x32_bf16 v[68:71], a[72:75], a[112:115], v[68:71]\n"
"v_add_f32 v189, v189, v21\n"
"v_mfma_f32_16x16x32_bf16 v[72:75], a[72:75], a[120:123], v[72:75]\n"
"v_add_f32 v190, v190, v22\n"
"v_add_f32 v191, v191, v23\n"
"v_mfma_f32_16x16x32_bf16 v[72:75], a[76:79], a[124:127], v[72:75]\n"
"v_add_f32 v192, v192, v20\n"
"v_mfma_f32_16x16x32_bf16 v[44:47], a[68:71], a[124:127], v[44:47]\n"
"v_add_f32 v193, v193, v21\n"
"v_mfma_f32_16x16x32_bf16 v[44:47], a[64:67], a[120:123], v[44:47]\n"
"s_waitcnt lgkmcnt(4)\n"
"v_add_f32 v194, v194, v22\n"
"v_mfma_f32_16x16x32_bf16 v[48:51], a[64:67], a[128:131], v[48:51]\n"
"v_add_f32 v195, v195, v23\n"
"v_mfma_f32_16x16x32_bf16 v[48:51], a[68:71], a[132:135], v[48:51]\n"
"global_load_dwordx4 a[56:59], v0, s[34:35] offset:2048 sc1\n"
"v_add_f32 v196, v196, v20\n"
"v_mfma_f32_16x16x32_bf16 v[76:79], a[76:79], a[132:135], v[76:79]\n"
"v_add_f32 v197, v197, v21\n"
"v_mfma_f32_16x16x32_bf16 v[76:79], a[72:75], a[128:131], v[76:79]\n"
"v_add_f32 v198, v198, v22\n"
"v_mfma_f32_16x16x32_bf16 v[80:83], a[72:75], a[136:139], v[80:83]\n"
"v_add_f32 v199, v199, v23\n"
"v_mfma_f32_16x16x32_bf16 v[80:83], a[76:79], a[140:143], v[80:83]\n"
"v_accvgpr_read_b32 v12, a20\n"
"v_mfma_f32_16x16x32_bf16 v[52:55], a[68:71], a[140:143], v[52:55]\n"
"v_accvgpr_read_b32 v13, a21\n"
"v_accvgpr_read_b32 v14, a22\n"
"v_mfma_f32_16x16x32_bf16 v[52:55], a[64:67], a[136:139], v[52:55]\n"
"s_waitcnt lgkmcnt(0)\n"
"v_accvgpr_read_b32 v15, a23\n"
"v_mfma_f32_16x16x32_bf16 v[56:59], a[64:67], a[144:147], v[56:59]\n"
"v_add_f32 v12, v12, v20\n"
"v_mfma_f32_16x16x32_bf16 v[56:59], a[68:71], a[148:151], v[56:59]\n"
"global_load_dwordx4 a[60:63], v0, s[34:35] offset:3072 sc1\n"
"v_add_f32 v13, v13, v21\n"
"v_mfma_f32_16x16x32_bf16 v[84:87], a[76:79], a[148:151], v[84:87]\n"
"v_add_f32 v14, v14, v22\n"
"v_mfma_f32_16x16x32_bf16 v[84:87], a[72:75], a[144:147], v[84:87]\n"
"v_add_f32 v15, v15, v23\n"
"v_mfma_f32_16x16x32_bf16 a[4:7], a[72:75], a[152:155], a[4:7]\n"
"s_add_u32 s34, s34, 8192\n"
"v_accvgpr_write_b32 a20, v12\n"
"v_mfma_f32_16x16x32_bf16 a[4:7], a[76:79], a[156:159], a[4:7]\n"
"v_accvgpr_write_b32 a21, v13\n"
"v_mfma_f32_16x16x32_bf16 a[0:3], a[68:71], a[156:159], a[0:3]\n"
"v_accvgpr_write_b32 a22, v14\n"
"v_mfma_f32_16x16x32_bf16 a[0:3], a[64:67], a[152:155], a[0:3]\n"
"s_waitcnt vmcnt(8)\n"
"v_accvgpr_write_b32 a23, v15\n"
"v_lshlrev_b32 v24, 16, v26\n"
"v_mfma_f32_16x16x32_bf16 v[88:91], a[80:83], a[96:99], v[88:91]\n"
"v_and_b32 v25, s63, v26\n"
"v_mfma_f32_16x16x32_bf16 v[88:91], a[84:87], a[100:103], v[88:91]\n"
"global_load_dwordx4 a[64:67], v0, s[34:35] offset:-4096 sc1\n"
"v_lshlrev_b32 v26, 16, v27\n"
"v_mfma_f32_16x16x32_bf16 v[116:119], a[92:95], a[100:103], v[116:119]\n"
"ds_read_b128 a[100:103], v4 offset:17408\n"
"v_and_b32 v27, s63, v27\n"
"v_mfma_f32_16x16x32_bf16 v[116:119], a[88:91], a[96:99], v[116:119]\n"
"ds_read_b128 a[96:99], v4 offset:16384\n"
"v_add_f32 v200, v200, v24\n"
"v_mfma_f32_16x16x32_bf16 v[120:123], a[88:91], a[104:107], v[120:123]\n"
"v_add_f32 v201, v201, v25\n"
"v_mfma_f32_16x16x32_bf16 v[120:123], a[92:95], a[108:111], v[120:123]\n"
"v_add_f32 v202, v202, v26\n"
"v_mfma_f32_16x16x32_bf16 v[92:95], a[84:87], a[108:111], v[92:95]\n"
"ds_read_b128 a[108:111], v4 offset:19456\n"
"v_add_f32 v203, v203, v27\n"
"v_mfma_f32_16x16x32_bf16 v[92:95], a[80:83], a[104:107], v[92:95]\n"
"ds_read_b128 a[104:107], v4 offset:18432\n"
"v_add_f32 v204, v204, v24\n"
"v_mfma_f32_16x16x32_bf16 v[96:99], a[80:83], a[112:115], v[96:99]\n"
"v_add_f32 v205, v205, v25\n"
"v_add_f32 v206, v206, v26\n"
"v_mfma_f32_16x16x32_bf16 v[96:99], a[84:87], a[116:119], v[96:99]\n"
"global_load_dwordx4 a[68:71], v0, s[34:35] offset:-3072 sc1\n"
"v_add_f32 v207, v207, v27\n"
"v_mfma_f32_16x16x32_bf16 v[124:127], a[92:95], a[116:119], v[124:127]\n"
"ds_read_b128 a[116:119], v4 offset:21504\n"
"v_add_f32 v208, v208, v24\n"
"v_mfma_f32_16x16x32_bf16 v[124:127], a[88:91], a[112:115], v[124:127]\n"
"ds_read_b128 a[112:115], v4 offset:20480\n"
"v_add_f32 v209, v209, v25\n"
"v_mfma_f32_16x16x32_bf16 v[128:131], a[88:91], a[120:123], v[128:131]\n"
"v_add_f32 v210, v210, v26\n"
"v_mfma_f32_16x16x32_bf16 v[128:131], a[92:95], a[124:127], v[128:131]\n"
"v_add_f32 v211, v211, v27\n"
"v_mfma_f32_16x16x32_bf16 v[100:103], a[84:87], a[124:127], v[100:103]\n"
"ds_read_b128 a[124:127], v4 offset:23552\n"
"v_add_f32 v212, v212, v24\n"
"v_mfma_f32_16x16x32_bf16 v[100:103], a[80:83], a[120:123], v[100:103]\n"
"ds_read_b128 a[120:123], v4 offset:22528\n"
"v_add_f32 v213, v213, v25\n"
"v_mfma_f32_16x16x32_bf16 v[104:107], a[80:83], a[128:131], v[104:107]\n"
"v_add_f32 v214, v214, v26\n"
"v_add_f32 v215, v215, v27\n"
"v_mfma_f32_16x16x32_bf16 v[104:107], a[84:87], a[132:135], v[104:107]\n"
"global_load_dwordx4 a[72:75], v0, s[34:35] offset:-2048 sc1\n"
"v_add_f32 v216, v216, v24\n"
"v_mfma_f32_16x16x32_bf16 v[132:135], a[92:95], a[132:135], v[132:135]\n"
"ds_read_b128 a[132:135], v4 offset:25600\n"
"v_add_f32 v217, v217, v25\n"
"v_mfma_f32_16x16x32_bf16 v[132:135], a[88:91], a[128:131], v[132:135]\n"
"ds_read_b128 a[128:131], v4 offset:24576\n"
"v_add_f32 v218, v218, v26\n"
"v_mfma_f32_16x16x32_bf16 v[136:139], a[88:91], a[136:139], v[136:139]\n"
"v_add_f32 v219, v219, v27\n"
"v_mfma_f32_16x16x32_bf16 v[136:139], a[92:95], a[140:143], v[136:139]\n"
"v_add_f32 v220, v220, v24\n"
"v_mfma_f32_16x16x32_bf16 v[108:111], a[84:87], a[140:143], v[108:111]\n"
"ds_read_b128 a[140:143], v4 offset:27648\n"
"v_add_f32 v221, v221, v25\n"
"v_mfma_f32_16x16x32_bf16 v[108:111], a[80:83], a[136:139], v[108:111]\n"
"ds_read_b128 a[136:139], v4 offset:26624\n"
"v_add_f32 v222, v222, v26\n"
"v_mfma_f32_16x16x32_bf16 v[112:115], a[80:83], a[144:147], v[112:115]\n"
"v_add_f32 v223, v223, v27\n"
"v_mfma_f32_16x16x32_bf16 v[112:115], a[84:87], a[148:151], v[112:115]\n"
"global_load_dwordx4 a[76:79], v0, s[34:35] offset:-1024 sc1\n"
"v_add_f32 v224, v224, v24\n"
"v_mfma_f32_16x16x32_bf16 v[140:143], a[92:95], a[148:151], v[140:143]\n"
"ds_read_b128 a[148:151], v4 offset:29696\n"
"v_add_f32 v225, v225, v25\n"
"v_mfma_f32_16x16x32_bf16 v[140:143], a[88:91], a[144:147], v[140:143]\n"
"ds_read_b128 a[144:147], v4 offset:28672\n"
"v_add_f32 v226, v226, v26\n"
"v_mfma_f32_16x16x32_bf16 a[12:15], a[88:91], a[152:155], a[12:15]\n"
"v_add_f32 v227, v227, v27\n"
"v_accvgpr_read_b32 v12, a24\n"
"v_mfma_f32_16x16x32_bf16 a[12:15], a[92:95], a[156:159], a[12:15]\n"
"v_accvgpr_read_b32 v13, a25\n"
"v_mfma_f32_16x16x32_bf16 a[8:11], a[84:87], a[156:159], a[8:11]\n"
"ds_read_b128 a[156:159], v4 offset:31744\n"
"v_accvgpr_read_b32 v14, a26\n"
"v_mfma_f32_16x16x32_bf16 a[8:11], a[80:83], a[152:155], a[8:11]\n"
"s_waitcnt vmcnt(8) lgkmcnt(11)\n"
"ds_read_b128 a[152:155], v4 offset:30720\n"
"v_accvgpr_read_b32 v15, a27\n"
"v_mfma_f32_16x16x32_bf16 v[32:35], a[32:35], a[96:99], v[32:35]\n"
"v_add_f32 v12, v12, v24\n"
"v_mfma_f32_16x16x32_bf16 v[32:35], a[36:39], a[100:103], v[32:35]\n"
"global_load_dwordx4 a[80:83], v0, s[34:35] offset:0 sc1\n"
"v_add_f32 v13, v13, v25\n"
"v_mfma_f32_16x16x32_bf16 v[60:63], a[44:47], a[100:103], v[60:63]\n"
"v_add_f32 v14, v14, v26\n"
"v_mfma_f32_16x16x32_bf16 v[60:63], a[40:43], a[96:99], v[60:63]\n"
"v_add_f32 v15, v15, v27\n"
"v_mfma_f32_16x16x32_bf16 v[64:67], a[40:43], a[104:107], v[64:67]\n"
"v_accvgpr_write_b32 a24, v12\n"
"v_accvgpr_write_b32 a25, v13\n"
"v_mfma_f32_16x16x32_bf16 v[64:67], a[44:47], a[108:111], v[64:67]\n"
"v_accvgpr_write_b32 a26, v14\n"
"v_mfma_f32_16x16x32_bf16 v[36:39], a[36:39], a[108:111], v[36:39]\n"
"v_accvgpr_write_b32 a27, v15\n"
"v_mfma_f32_16x16x32_bf16 v[36:39], a[32:35], a[104:107], v[36:39]\n"
"s_waitcnt lgkmcnt(8)\n"
"v_lshlrev_b32 v28, 16, v30\n"
"v_mfma_f32_16x16x32_bf16 v[40:43], a[32:35], a[112:115], v[40:43]\n"
"v_and_b32 v29, s63, v30\n"
"v_mfma_f32_16x16x32_bf16 v[40:43], a[36:39], a[116:119], v[40:43]\n"
"global_load_dwordx4 a[84:87], v0, s[34:35] offset:1024 sc1\n"
"v_lshlrev_b32 v30, 16, v31\n"
"v_mfma_f32_16x16x32_bf16 v[68:71], a[44:47], a[116:119], v[68:71]\n"
"v_and_b32 v31, s63, v31\n"
"v_mfma_f32_16x16x32_bf16 v[68:71], a[40:43], a[112:115], v[68:71]\n"
"v_add_f32 v228, v228, v28\n"
"v_mfma_f32_16x16x32_bf16 v[72:75], a[40:43], a[120:123], v[72:75]\n"
"v_add_f32 v229, v229, v29\n"
"v_mfma_f32_16x16x32_bf16 v[72:75], a[44:47], a[124:127], v[72:75]\n"
"v_add_f32 v230, v230, v30\n"
"v_mfma_f32_16x16x32_bf16 v[44:47], a[36:39], a[124:127], v[44:47]\n"
"v_add_f32 v231, v231, v31\n"
"v_add_f32 v232, v232, v28\n"
"v_mfma_f32_16x16x32_bf16 v[44:47], a[32:35], a[120:123], v[44:47]\n"
"s_waitcnt lgkmcnt(4)\n"
"v_add_f32 v233, v233, v29\n"
"v_mfma_f32_16x16x32_bf16 v[48:51], a[32:35], a[128:131], v[48:51]\n"
"v_add_f32 v234, v234, v30\n"
"v_mfma_f32_16x16x32_bf16 v[48:51], a[36:39], a[132:135], v[48:51]\n"
"global_load_dwordx4 a[88:91], v0, s[34:35] offset:2048 sc1\n"
"v_add_f32 v235, v235, v31\n"
"v_mfma_f32_16x16x32_bf16 v[76:79], a[44:47], a[132:135], v[76:79]\n"
"v_add_f32 v236, v236, v28\n"
"v_mfma_f32_16x16x32_bf16 v[76:79], a[40:43], a[128:131], v[76:79]\n"
"v_add_f32 v237, v237, v29\n"
"v_mfma_f32_16x16x32_bf16 v[80:83], a[40:43], a[136:139], v[80:83]\n"
"v_add_f32 v238, v238, v30\n"
"v_mfma_f32_16x16x32_bf16 v[80:83], a[44:47], a[140:143], v[80:83]\n"
"v_add_f32 v239, v239, v31\n"
"v_mfma_f32_16x16x32_bf16 v[52:55], a[36:39], a[140:143], v[52:55]\n"
"v_add_f32 v240, v240, v28\n"
"v_mfma_f32_16x16x32_bf16 v[52:55], a[32:35], a[136:139], v[52:55]\n"
"s_waitcnt lgkmcnt(0)\n"
"v_add_f32 v241, v241, v29\n"
"v_add_f32 v242, v242, v30\n"
"v_mfma_f32_16x16x32_bf16 v[56:59], a[32:35], a[144:147], v[56:59]\n"
"v_add_f32 v243, v243, v31\n"
"v_mfma_f32_16x16x32_bf16 v[56:59], a[36:39], a[148:151], v[56:59]\n"
"global_load_dwordx4 a[92:95], v0, s[34:35] offset:3072 sc1\n"
"v_add_f32 v244, v244, v28\n"
"v_mfma_f32_16x16x32_bf16 v[84:87], a[44:47], a[148:151], v[84:87]\n"
"v_add_f32 v245, v245, v29\n"
"v_mfma_f32_16x16x32_bf16 v[84:87], a[40:43], a[144:147], v[84:87]\n"
"v_add_f32 v246, v246, v30\n"
"v_mfma_f32_16x16x32_bf16 a[4:7], a[40:43], a[152:155], a[4:7]\n"
"s_add_u32 s34, s34, 8192\n"
"v_add_f32 v247, v247, v31\n"
"v_mfma_f32_16x16x32_bf16 a[4:7], a[44:47], a[156:159], a[4:7]\n"
"v_add_f32 v248, v248, v28\n"
"v_mfma_f32_16x16x32_bf16 a[0:3], a[36:39], a[156:159], a[0:3]\n"
"v_add_f32 v249, v249, v29\n"
"v_mfma_f32_16x16x32_bf16 a[0:3], a[32:35], a[152:155], a[0:3]\n"
"s_waitcnt vmcnt(8)\n"
"v_add_f32 v250, v250, v30\n"
"v_mfma_f32_16x16x32_bf16 v[88:91], a[48:51], a[96:99], v[88:91]\n"
"v_add_f32 v251, v251, v31\n"
"v_add_f32 v252, v252, v28\n"
"v_mfma_f32_16x16x32_bf16 v[88:91], a[52:55], a[100:103], v[88:91]\n"
"global_load_dwordx4 a[32:35], v0, s[34:35] offset:-4096 sc1\n"
"v_add_f32 v253, v253, v29\n"
"v_mfma_f32_16x16x32_bf16 v[116:119], a[60:63], a[100:103], v[116:119]\n"
"ds_read_b128 a[100:103], v5 offset:17408\n"
"v_add_f32 v254, v254, v30\n"
"v_mfma_f32_16x16x32_bf16 v[116:119], a[56:59], a[96:99], v[116:119]\n"
"ds_read_b128 a[96:99], v5 offset:16384\n"
"v_add_f32 v255, v255, v31\n"
"v_mfma_f32_16x16x32_bf16 v[120:123], a[56:59], a[104:107], v[120:123]\n"
"v_accvgpr_read_b32 v12, a28\n"
"v_mfma_f32_16x16x32_bf16 v[120:123], a[60:63], a[108:111], v[120:123]\n"
"v_accvgpr_read_b32 v13, a29\n"
"v_mfma_f32_16x16x32_bf16 v[92:95], a[52:55], a[108:111], v[92:95]\n"
"ds_read_b128 a[108:111], v5 offset:19456\n"
"v_accvgpr_read_b32 v14, a30\n"
"v_mfma_f32_16x16x32_bf16 v[92:95], a[48:51], a[104:107], v[92:95]\n"
"ds_read_b128 a[104:107], v5 offset:18432\n"
"v_accvgpr_read_b32 v15, a31\n"
"v_mfma_f32_16x16x32_bf16 v[96:99], a[48:51], a[112:115], v[96:99]\n"
"v_add_f32 v12, v12, v28\n"
"v_add_f32 v13, v13, v29\n"
"v_mfma_f32_16x16x32_bf16 v[96:99], a[52:55], a[116:119], v[96:99]\n"
"global_load_dwordx4 a[36:39], v0, s[34:35] offset:-3072 sc1\n"
"v_add_f32 v14, v14, v30\n"
"v_mfma_f32_16x16x32_bf16 v[124:127], a[60:63], a[116:119], v[124:127]\n"
"ds_read_b128 a[116:119], v5 offset:21504\n"
"v_add_f32 v15, v15, v31\n"
"v_mfma_f32_16x16x32_bf16 v[124:127], a[56:59], a[112:115], v[124:127]\n"
"ds_read_b128 a[112:115], v5 offset:20480\n"
"v_accvgpr_write_b32 a28, v12\n"
"v_mfma_f32_16x16x32_bf16 v[128:131], a[56:59], a[120:123], v[128:131]\n"
"v_accvgpr_write_b32 a29, v13\n"
"v_mfma_f32_16x16x32_bf16 v[128:131], a[60:63], a[124:127], v[128:131]\n"
"v_accvgpr_write_b32 a30, v14\n"
"v_mfma_f32_16x16x32_bf16 v[100:103], a[52:55], a[124:127], v[100:103]\n"
"ds_read_b128 a[124:127], v5 offset:23552\n"
"v_accvgpr_write_b32 a31, v15\n"
"v_mfma_f32_16x16x32_bf16 v[100:103], a[48:51], a[120:123], v[100:103]\n"
"ds_read_b128 a[120:123], v5 offset:22528\n"
"v_mfma_f32_16x16x32_bf16 v[104:107], a[48:51], a[128:131], v[104:107]\n"
"v_mfma_f32_16x16x32_bf16 v[104:107], a[52:55], a[132:135], v[104:107]\n"
"global_load_dwordx4 a[40:43], v0, s[34:35] offset:-2048 sc1\n"
"v_mfma_f32_16x16x32_bf16 v[132:135], a[60:63], a[132:135], v[132:135]\n"
"ds_read_b128 a[132:135], v5 offset:25600\n"
"v_mfma_f32_16x16x32_bf16 v[132:135], a[56:59], a[128:131], v[132:135]\n"
"ds_read_b128 a[128:131], v5 offset:24576\n"
"v_mfma_f32_16x16x32_bf16 v[136:139], a[56:59], a[136:139], v[136:139]\n"
"ds_read_b128 v[16:19], v7 offset:32768\n"
"v_mfma_f32_16x16x32_bf16 v[136:139], a[60:63], a[140:143], v[136:139]\n"
"v_mfma_f32_16x16x32_bf16 v[108:111], a[52:55], a[140:143], v[108:111]\n"
"ds_read_b128 a[140:143], v5 offset:27648\n"
"v_mfma_f32_16x16x32_bf16 v[108:111], a[48:51], a[136:139], v[108:111]\n"
"ds_read_b128 a[136:139], v5 offset:26624\n"
"v_mfma_f32_16x16x32_bf16 v[112:115], a[48:51], a[144:147], v[112:115]\n"
"v_mfma_f32_16x16x32_bf16 v[112:115], a[52:55], a[148:151], v[112:115]\n"
"global_load_dwordx4 a[44:47], v0, s[34:35] offset:-1024 sc1\n"
"v_mfma_f32_16x16x32_bf16 v[140:143], a[60:63], a[148:151], v[140:143]\n"
"ds_read_b128 a[148:151], v5 offset:29696\n"
"v_mfma_f32_16x16x32_bf16 v[140:143], a[56:59], a[144:147], v[140:143]\n"
"ds_read_b128 a[144:147], v5 offset:28672\n"
"v_mfma_f32_16x16x32_bf16 a[12:15], a[56:59], a[152:155], a[12:15]\n"
"v_mfma_f32_16x16x32_bf16 a[12:15], a[60:63], a[156:159], a[12:15]\n"
"v_mfma_f32_16x16x32_bf16 a[8:11], a[52:55], a[156:159], a[8:11]\n"
"s_cmp_eq_u32 s62, 5\n"
"ds_read_b128 a[156:159], v5 offset:31744\n"
"v_mfma_f32_16x16x32_bf16 a[8:11], a[48:51], a[152:155], a[8:11]\n"
"ds_read_b128 a[152:155], v5 offset:30720\n"
"s_cbranch_scc1 L_last\n"
"s_waitcnt vmcnt(8) lgkmcnt(13)\n"
"v_mfma_f32_16x16x32_bf16 v[32:35], a[64:67], a[96:99], v[32:35]\n"
"v_mfma_f32_16x16x32_bf16 v[32:35], a[68:71], a[100:103], v[32:35]\n"
"global_load_dwordx4 a[48:51], v0, s[34:35] offset:0 sc1\n"
"v_mfma_f32_16x16x32_bf16 v[60:63], a[76:79], a[100:103], v[60:63]\n"
"v_mfma_f32_16x16x32_bf16 v[60:63], a[72:75], a[96:99], v[60:63]\n"
"v_mfma_f32_16x16x32_bf16 v[64:67], a[72:75], a[104:107], v[64:67]\n"
"v_mfma_f32_16x16x32_bf16 v[64:67], a[76:79], a[108:111], v[64:67]\n"
"v_mfma_f32_16x16x32_bf16 v[36:39], a[68:71], a[108:111], v[36:39]\n"
"s_waitcnt lgkmcnt(6)\n"
"v_lshlrev_b32 v12, 16, v16\n"
"v_mfma_f32_16x16x32_bf16 v[36:39], a[64:67], a[104:107], v[36:39]\n"
"v_and_b32 v13, s63, v16\n"
"v_lshlrev_b32 v14, 16, v17\n"
"v_mfma_f32_16x16x32_bf16 v[40:43], a[64:67], a[112:115], v[40:43]\n"
"v_and_b32 v15, s63, v17\n"
"v_mfma_f32_16x16x32_bf16 v[40:43], a[68:71], a[116:119], v[40:43]\n"
"global_load_dwordx4 a[52:55], v0, s[34:35] offset:1024 sc1\n"
"ds_read_b128 v[20:23], v7 offset:34816\n"
"v_mfma_f32_16x16x32_bf16 v[68:71], a[76:79], a[116:119], v[68:71]\n"
"v_mul_f32 v12, v12, v32\n"
"v_mul_f32 v13, v13, v33\n"
"v_mfma_f32_16x16x32_bf16 v[68:71], a[72:75], a[112:115], v[68:71]\n"
"v_mul_f32 v14, v14, v34\n"
"v_mfma_f32_16x16x32_bf16 v[72:75], a[72:75], a[120:123], v[72:75]\n"
"v_mul_f32 v15, v15, v35\n"
"v_cvt_pk_bf16_f32 v24, v12, v13\n"
"v_mfma_f32_16x16x32_bf16 v[72:75], a[76:79], a[124:127], v[72:75]\n"
"v_cvt_pk_bf16_f32 v25, v14, v15\n"
"v_mfma_f32_16x16x32_bf16 v[44:47], a[68:71], a[124:127], v[44:47]\n"
"ds_write_b64 v2, v[24:25] offset:0\n"
"v_mfma_f32_16x16x32_bf16 v[44:47], a[64:67], a[120:123], v[44:47]\n"
"s_waitcnt lgkmcnt(6)\n"
"v_lshlrev_b32 v12, 16, v18\n"
"v_and_b32 v13, s63, v18\n"
"v_mfma_f32_16x16x32_bf16 v[48:51], a[64:67], a[128:131], v[48:51]\n"
"v_lshlrev_b32 v14, 16, v19\n"
"v_mfma_f32_16x16x32_bf16 v[48:51], a[68:71], a[132:135], v[48:51]\n"
"global_load_dwordx4 a[56:59], v0, s[34:35] offset:2048 sc1\n"
"v_and_b32 v15, s63, v19\n"
"v_mfma_f32_16x16x32_bf16 v[76:79], a[76:79], a[132:135], v[76:79]\n"
"v_mul_f32 v12, v12, v60\n"
"v_mul_f32 v13, v13, v61\n"
"v_mfma_f32_16x16x32_bf16 v[76:79], a[72:75], a[128:131], v[76:79]\n"
"v_mul_f32 v14, v14, v62\n"
"v_mfma_f32_16x16x32_bf16 v[80:83], a[72:75], a[136:139], v[80:83]\n"
"v_mul_f32 v15, v15, v63\n"
"v_cvt_pk_bf16_f32 v26, v12, v13\n"
"v_mfma_f32_16x16x32_bf16 v[80:83], a[76:79], a[140:143], v[80:83]\n"
"v_cvt_pk_bf16_f32 v27, v14, v15\n"
"v_mfma_f32_16x16x32_bf16 v[52:55], a[68:71], a[140:143], v[52:55]\n"
"ds_write_b64 v2, v[26:27] offset:8\n"
"v_mfma_f32_16x16x32_bf16 v[52:55], a[64:67], a[136:139], v[52:55]\n"
"s_waitcnt lgkmcnt(2)\n"
"v_lshlrev_b32 v12, 16, v20\n"
"v_and_b32 v13, s63, v20\n"
"v_mfma_f32_16x16x32_bf16 v[56:59], a[64:67], a[144:147], v[56:59]\n"
"v_lshlrev_b32 v14, 16, v21\n"
"v_mfma_f32_16x16x32_bf16 v[56:59], a[68:71], a[148:151], v[56:59]\n"
"global_load_dwordx4 a[60:63], v0, s[34:35] offset:3072 sc1\n"
"v_and_b32 v15, s63, v21\n"
"v_mfma_f32_16x16x32_bf16 v[84:87], a[76:79], a[148:151], v[84:87]\n"
"v_xor_b32 v16, 64, v7\n"
"v_mfma_f32_16x16x32_bf16 v[84:87], a[72:75], a[144:147], v[84:87]\n"
"ds_read_b128 v[16:19], v16 offset:32768\n"
"v_mul_f32 v12, v12, v36\n"
"v_mfma_f32_16x16x32_bf16 a[4:7], a[72:75], a[152:155], a[4:7]\n"
"s_add_u32 s34, s34, 8192\n"
"v_mul_f32 v13, v13, v37\n"
"v_mul_f32 v14, v14, v38\n"
"v_mfma_f32_16x16x32_bf16 a[4:7], a[76:79], a[156:159], a[4:7]\n"
"v_mul_f32 v15, v15, v39\n"
"v_mfma_f32_16x16x32_bf16 a[0:3], a[68:71], a[156:159], a[0:3]\n"
"v_cvt_pk_bf16_f32 v28, v12, v13\n"
"v_cvt_pk_bf16_f32 v29, v14, v15\n"
"v_mfma_f32_16x16x32_bf16 a[0:3], a[64:67], a[152:155], a[0:3]\n"
"s_waitcnt vmcnt(8)\n"
"ds_write_b64 v2, v[28:29] offset:2048\n"
"v_mfma_f32_16x16x32_bf16 v[88:91], a[80:83], a[96:99], v[88:91]\n"
"v_lshlrev_b32 v12, 16, v22\n"
"v_and_b32 v13, s63, v22\n"
"v_mfma_f32_16x16x32_bf16 v[88:91], a[84:87], a[100:103], v[88:91]\n"
"global_load_dwordx4 a[64:67], v0, s[34:35] offset:-4096 sc1\n"
"v_lshlrev_b32 v14, 16, v23\n"
"v_mfma_f32_16x16x32_bf16 v[116:119], a[92:95], a[100:103], v[116:119]\n"
"ds_read_b128 a[100:103], v2 offset:17408\n"
"v_and_b32 v15, s63, v23\n"
"v_mfma_f32_16x16x32_bf16 v[116:119], a[88:91], a[96:99], v[116:119]\n"
"ds_read_b128 a[96:99], v2 offset:16384\n"
"v_mul_f32 v12, v12, v64\n"
"v_mfma_f32_16x16x32_bf16 v[120:123], a[88:91], a[104:107], v[120:123]\n"
"v_mul_f32 v13, v13, v65\n"
"v_mul_f32 v14, v14, v66\n"
"v_mfma_f32_16x16x32_bf16 v[120:123], a[92:95], a[108:111], v[120:123]\n"
"v_mul_f32 v15, v15, v67\n"
"v_mfma_f32_16x16x32_bf16 v[92:95], a[84:87], a[108:111], v[92:95]\n"
"ds_read_b128 a[108:111], v2 offset:19456\n"
"v_cvt_pk_bf16_f32 v30, v12, v13\n"
"v_mfma_f32_16x16x32_bf16 v[92:95], a[80:83], a[104:107], v[92:95]\n"
"ds_read_b128 a[104:107], v2 offset:18432\n"
"v_cvt_pk_bf16_f32 v31, v14, v15\n"
"v_mfma_f32_16x16x32_bf16 v[96:99], a[80:83], a[112:115], v[96:99]\n"
"s_waitcnt lgkmcnt(5)\n"
"global_store_dwordx4 v0, v[28:31], s[52:53] offset:-2048 sc1\n"
"ds_write_b64 v2, v[30:31] offset:2056\n"
"v_lshlrev_b32 v12, 16, v16\n"
"v_mfma_f32_16x16x32_bf16 v[96:99], a[84:87], a[116:119], v[96:99]\n"
"global_load_dwordx4 a[68:71], v0, s[34:35] offset:-3072 sc1\n"
"v_and_b32 v13, s63, v16\n"
"v_mfma_f32_16x16x32_bf16 v[124:127], a[92:95], a[116:119], v[124:127]\n"
"ds_read_b128 a[116:119], v2 offset:21504\n"
"v_lshlrev_b32 v14, 16, v17\n"
"v_mfma_f32_16x16x32_bf16 v[124:127], a[88:91], a[112:115], v[124:127]\n"
"ds_read_b128 a[112:115], v2 offset:20480\n"
"v_and_b32 v15, s63, v17\n"
"v_mfma_f32_16x16x32_bf16 v[128:131], a[88:91], a[120:123], v[128:131]\n"
"v_xor_b32 v20, 64, v7\n"
"v_mfma_f32_16x16x32_bf16 v[128:131], a[92:95], a[124:127], v[128:131]\n"
"ds_read_b128 v[20:23], v20 offset:34816\n"
"v_mul_f32 v12, v12, v88\n"
"v_mfma_f32_16x16x32_bf16 v[100:103], a[84:87], a[124:127], v[100:103]\n"
"ds_read_b128 a[124:127], v2 offset:23552\n"
"v_mul_f32 v13, v13, v89\n"
"v_mfma_f32_16x16x32_bf16 v[100:103], a[80:83], a[120:123], v[100:103]\n"
"ds_read_b128 a[120:123], v2 offset:22528\n"
"v_mul_f32 v14, v14, v90\n"
"v_mfma_f32_16x16x32_bf16 v[104:107], a[80:83], a[128:131], v[104:107]\n"
"v_mul_f32 v15, v15, v91\n"
"v_cvt_pk_bf16_f32 v24, v12, v13\n"
"v_mfma_f32_16x16x32_bf16 v[104:107], a[84:87], a[132:135], v[104:107]\n"
"global_load_dwordx4 a[72:75], v0, s[34:35] offset:-2048 sc1\n"
"v_cvt_pk_bf16_f32 v25, v14, v15\n"
"v_mfma_f32_16x16x32_bf16 v[132:135], a[92:95], a[132:135], v[132:135]\n"
"ds_read_b128 a[132:135], v2 offset:25600\n"
"v_mfma_f32_16x16x32_bf16 v[132:135], a[88:91], a[128:131], v[132:135]\n"
"ds_read_b128 a[128:131], v2 offset:24576\n"
"v_mfma_f32_16x16x32_bf16 v[136:139], a[88:91], a[136:139], v[136:139]\n"
"ds_write_b64 v2, v[24:25] offset:1024\n"
"v_mfma_f32_16x16x32_bf16 v[136:139], a[92:95], a[140:143], v[136:139]\n"
"v_lshlrev_b32 v12, 16, v18\n"
"v_and_b32 v13, s63, v18\n"
"v_mfma_f32_16x16x32_bf16 v[108:111], a[84:87], a[140:143], v[108:111]\n"
"ds_read_b128 a[140:143], v2 offset:27648\n"
"v_lshlrev_b32 v14, 16, v19\n"
"v_mfma_f32_16x16x32_bf16 v[108:111], a[80:83], a[136:139], v[108:111]\n"
"ds_read_b128 a[136:139], v2 offset:26624\n"
"v_and_b32 v15, s63, v19\n"
"v_mfma_f32_16x16x32_bf16 v[112:115], a[80:83], a[144:147], v[112:115]\n"
"v_mul_f32 v12, v12, v116\n"
"v_mul_f32 v13, v13, v117\n"
"v_mfma_f32_16x16x32_bf16 v[112:115], a[84:87], a[148:151], v[112:115]\n"
"global_load_dwordx4 a[76:79], v0, s[34:35] offset:-1024 sc1\n"
"v_mul_f32 v14, v14, v118\n"
"v_mfma_f32_16x16x32_bf16 v[140:143], a[92:95], a[148:151], v[140:143]\n"
"ds_read_b128 a[148:151], v2 offset:29696\n"
"v_mul_f32 v15, v15, v119\n"
"v_mfma_f32_16x16x32_bf16 v[140:143], a[88:91], a[144:147], v[140:143]\n"
"ds_read_b128 a[144:147], v2 offset:28672\n"
"v_cvt_pk_bf16_f32 v26, v12, v13\n"
"v_mfma_f32_16x16x32_bf16 a[12:15], a[88:91], a[152:155], a[12:15]\n"
"v_cvt_pk_bf16_f32 v27, v14, v15\n"
"v_mfma_f32_16x16x32_bf16 a[12:15], a[92:95], a[156:159], a[12:15]\n"
"s_waitcnt lgkmcnt(9)\n"
"global_store_dwordx4 v0, v[24:27], s[52:53] offset:-3072 sc1\n"
"ds_write_b64 v2, v[26:27] offset:1032\n"
"v_lshlrev_b32 v12, 16, v20\n"
"v_mfma_f32_16x16x32_bf16 a[8:11], a[84:87], a[156:159], a[8:11]\n"
"ds_read_b128 a[156:159], v2 offset:31744\n"
"v_and_b32 v13, s63, v20\n"
"v_mfma_f32_16x16x32_bf16 a[8:11], a[80:83], a[152:155], a[8:11]\n"
"s_waitcnt vmcnt(10)\n"
"ds_read_b128 a[152:155], v2 offset:30720\n"
"v_lshlrev_b32 v14, 16, v21\n"
"v_mfma_f32_16x16x32_bf16 v[144:147], a[32:35], a[96:99], v[144:147]\n"
"ds_read_b128 v[16:19], v7 offset:36864\n"
"v_and_b32 v15, s63, v21\n"
"v_mfma_f32_16x16x32_bf16 v[144:147], a[36:39], a[100:103], v[144:147]\n"
"global_load_dwordx4 a[80:83], v0, s[34:35] offset:0 sc1\n"
"v_mul_f32 v12, v12, v92\n"
"v_mfma_f32_16x16x32_bf16 v[172:175], a[44:47], a[100:103], v[172:175]\n"
"v_mul_f32 v13, v13, v93\n"
"v_mul_f32 v14, v14, v94\n"
"v_mfma_f32_16x16x32_bf16 v[172:175], a[40:43], a[96:99], v[172:175]\n"
"v_mul_f32 v15, v15, v95\n"
"v_cvt_pk_bf16_f32 v28, v12, v13\n"
"v_mfma_f32_16x16x32_bf16 v[176:179], a[40:43], a[104:107], v[176:179]\n"
"v_cvt_pk_bf16_f32 v29, v14, v15\n"
"v_mfma_f32_16x16x32_bf16 v[176:179], a[44:47], a[108:111], v[176:179]\n"
"ds_write_b64 v2, v[28:29] offset:3072\n"
"v_mfma_f32_16x16x32_bf16 v[148:151], a[36:39], a[108:111], v[148:151]\n"
"v_lshlrev_b32 v12, 16, v22\n"
"v_and_b32 v13, s63, v22\n"
"v_mfma_f32_16x16x32_bf16 v[148:151], a[32:35], a[104:107], v[148:151]\n"
"s_waitcnt lgkmcnt(12)\n"
"v_lshlrev_b32 v14, 16, v23\n"
"v_and_b32 v15, s63, v23\n"
"v_mfma_f32_16x16x32_bf16 v[152:155], a[32:35], a[112:115], v[152:155]\n"
"v_mul_f32 v12, v12, v120\n"
"v_mul_f32 v13, v13, v121\n"
"v_mfma_f32_16x16x32_bf16 v[152:155], a[36:39], a[116:119], v[152:155]\n"
"global_load_dwordx4 a[84:87], v0, s[34:35] offset:1024 sc1\n"
"v_mul_f32 v14, v14, v122\n"
"v_mfma_f32_16x16x32_bf16 v[180:183], a[44:47], a[116:119], v[180:183]\n"
"v_mul_f32 v15, v15, v123\n"
"v_cvt_pk_bf16_f32 v30, v12, v13\n"
"v_mfma_f32_16x16x32_bf16 v[180:183], a[40:43], a[112:115], v[180:183]\n"
"v_cvt_pk_bf16_f32 v31, v14, v15\n"
"v_mfma_f32_16x16x32_bf16 v[184:187], a[40:43], a[120:123], v[184:187]\n"
"global_store_dwordx4 v0, v[28:31], s[52:53] offset:-1024 sc1\n"
"ds_write_b64 v2, v[30:31] offset:3080\n"
"v_mfma_f32_16x16x32_bf16 v[184:187], a[44:47], a[124:127], v[184:187]\n"
"s_waitcnt lgkmcnt(2)\n"
"v_lshlrev_b32 v12, 16, v16\n"
"v_and_b32 v13, s63, v16\n"
"v_mfma_f32_16x16x32_bf16 v[156:159], a[36:39], a[124:127], v[156:159]\n"
"v_lshlrev_b32 v14, 16, v17\n"
"v_mfma_f32_16x16x32_bf16 v[156:159], a[32:35], a[120:123], v[156:159]\n"
"v_and_b32 v15, s63, v17\n"
"v_xor_b32 v20, 64, v7\n"
"v_mfma_f32_16x16x32_bf16 v[160:163], a[32:35], a[128:131], v[160:163]\n"
"ds_read_b128 v[20:23], v20 offset:36864\n"
"v_mfma_f32_16x16x32_bf16 v[160:163], a[36:39], a[132:135], v[160:163]\n"
"global_load_dwordx4 a[88:91], v0, s[34:35] offset:2048 sc1\n"
"v_mul_f32 v12, v12, v40\n"
"v_mfma_f32_16x16x32_bf16 v[188:191], a[44:47], a[132:135], v[188:191]\n"
"v_mul_f32 v13, v13, v41\n"
"v_mul_f32 v14, v14, v42\n"
"v_mfma_f32_16x16x32_bf16 v[188:191], a[40:43], a[128:131], v[188:191]\n"
"v_mul_f32 v15, v15, v43\n"
"v_mfma_f32_16x16x32_bf16 v[192:195], a[40:43], a[136:139], v[192:195]\n"
"v_cvt_pk_bf16_f32 v24, v12, v13\n"
"v_cvt_pk_bf16_f32 v25, v14, v15\n"
"v_mfma_f32_16x16x32_bf16 v[192:195], a[44:47], a[140:143], v[192:195]\n"
"ds_write_b64 v2, v[24:25] offset:4096\n"
"v_mfma_f32_16x16x32_bf16 v[164:167], a[36:39], a[140:143], v[164:167]\n"
"v_lshlrev_b32 v12, 16, v18\n"
"v_mfma_f32_16x16x32_bf16 v[164:167], a[32:35], a[136:139], v[164:167]\n"
"v_and_b32 v13, s63, v18\n"
"v_lshlrev_b32 v14, 16, v19\n"
"v_mfma_f32_16x16x32_bf16 v[168:171], a[32:35], a[144:147], v[168:171]\n"
"v_and_b32 v15, s63, v19\n"
"v_mfma_f32_16x16x32_bf16 v[168:171], a[36:39], a[148:151], v[168:171]\n"
"global_load_dwordx4 a[92:95], v0, s[34:35] offset:3072 sc1\n"
"v_mul_f32 v12, v12, v68\n"
"v_mfma_f32_16x16x32_bf16 v[196:199], a[44:47], a[148:151], v[196:199]\n"
"v_mul_f32 v13, v13, v69\n"
"v_mul_f32 v14, v14, v70\n"
"v_mfma_f32_16x16x32_bf16 v[196:199], a[40:43], a[144:147], v[196:199]\n"
"v_mul_f32 v15, v15, v71\n"
"v_mfma_f32_16x16x32_bf16 a[20:23], a[40:43], a[152:155], a[20:23]\n"
"s_add_u32 s34, s34, 8192\n"
"v_cvt_pk_bf16_f32 v26, v12, v13\n"
"v_cvt_pk_bf16_f32 v27, v14, v15\n"
"v_mfma_f32_16x16x32_bf16 a[20:23], a[44:47], a[156:159], a[20:23]\n"
"global_store_dwordx4 v0, v[24:27], s[52:53] offset:0 sc1\n"
"ds_write_b64 v2, v[26:27] offset:4104\n"
"v_mfma_f32_16x16x32_bf16 a[16:19], a[36:39], a[156:159], a[16:19]\n"
"s_waitcnt lgkmcnt(2)\n"
"v_lshlrev_b32 v12, 16, v20\n"
"v_and_b32 v13, s63, v20\n"
"v_mfma_f32_16x16x32_bf16 a[16:19], a[32:35], a[152:155], a[16:19]\n"
"s_waitcnt vmcnt(12)\n"
"v_lshlrev_b32 v14, 16, v21\n"
"v_and_b32 v15, s63, v21\n"
"v_mfma_f32_16x16x32_bf16 v[200:203], a[48:51], a[96:99], v[200:203]\n"
"ds_read_b128 v[16:19], v7 offset:38912\n"
"v_mul_f32 v12, v12, v96\n"
"v_mfma_f32_16x16x32_bf16 v[200:203], a[52:55], a[100:103], v[200:203]\n"
"global_load_dwordx4 a[32:35], v0, s[34:35] offset:-4096 sc1\n"
"v_mul_f32 v13, v13, v97\n"
"v_mfma_f32_16x16x32_bf16 v[228:231], a[60:63], a[100:103], v[228:231]\n"
"ds_read_b128 a[100:103], v3 offset:17408\n"
"v_mul_f32 v14, v14, v98\n"
"v_mfma_f32_16x16x32_bf16 v[228:231], a[56:59], a[96:99], v[228:231]\n"
"ds_read_b128 a[96:99], v3 offset:16384\n"
"v_mul_f32 v15, v15, v99\n"
"v_mfma_f32_16x16x32_bf16 v[232:235], a[56:59], a[104:107], v[232:235]\n"
"v_cvt_pk_bf16_f32 v28, v12, v13\n"
"v_cvt_pk_bf16_f32 v29, v14, v15\n"
"v_mfma_f32_16x16x32_bf16 v[232:235], a[60:63], a[108:111], v[232:235]\n"
"ds_write_b64 v2, v[28:29] offset:5120\n"
"v_mfma_f32_16x16x32_bf16 v[204:207], a[52:55], a[108:111], v[204:207]\n"
"ds_read_b128 a[108:111], v3 offset:19456\n"
"v_mfma_f32_16x16x32_bf16 v[204:207], a[48:51], a[104:107], v[204:207]\n"
"ds_read_b128 a[104:107], v3 offset:18432\n"
"v_mfma_f32_16x16x32_bf16 v[208:211], a[48:51], a[112:115], v[208:211]\n"
"v_lshlrev_b32 v12, 16, v22\n"
"v_and_b32 v13, s63, v22\n"
"v_mfma_f32_16x16x32_bf16 v[208:211], a[52:55], a[116:119], v[208:211]\n"
"global_load_dwordx4 a[36:39], v0, s[34:35] offset:-3072 sc1\n"
"v_lshlrev_b32 v14, 16, v23\n"
"v_mfma_f32_16x16x32_bf16 v[236:239], a[60:63], a[116:119], v[236:239]\n"
"ds_read_b128 a[116:119], v3 offset:21504\n"
"v_and_b32 v15, s63, v23\n"
"v_mfma_f32_16x16x32_bf16 v[236:239], a[56:59], a[112:115], v[236:239]\n"
"ds_read_b128 a[112:115], v3 offset:20480\n"
"v_mul_f32 v12, v12, v124\n"
"v_mfma_f32_16x16x32_bf16 v[240:243], a[56:59], a[120:123], v[240:243]\n"
"v_mul_f32 v13, v13, v125\n"
"v_mul_f32 v14, v14, v126\n"
"v_mfma_f32_16x16x32_bf16 v[240:243], a[60:63], a[124:127], v[240:243]\n"
"v_mul_f32 v15, v15, v127\n"
"v_cvt_pk_bf16_f32 v30, v12, v13\n"
"v_mfma_f32_16x16x32_bf16 v[212:215], a[52:55], a[124:127], v[212:215]\n"
"ds_read_b128 a[124:127], v3 offset:23552\n"
"v_cvt_pk_bf16_f32 v31, v14, v15\n"
"v_mfma_f32_16x16x32_bf16 v[212:215], a[48:51], a[120:123], v[212:215]\n"
"ds_read_b128 a[120:123], v3 offset:22528\n"
"v_mfma_f32_16x16x32_bf16 v[216:219], a[48:51], a[128:131], v[216:219]\n"
"ds_write_b64 v2, v[30:31] offset:5128\n"
"v_mfma_f32_16x16x32_bf16 v[216:219], a[52:55], a[132:135], v[216:219]\n"
"s_waitcnt lgkmcnt(10)\n"
"global_load_dwordx4 a[40:43], v0, s[34:35] offset:-2048 sc1\n"
"v_lshlrev_b32 v12, 16, v16\n"
"v_mfma_f32_16x16x32_bf16 v[244:247], a[60:63], a[132:135], v[244:247]\n"
"ds_read_b128 a[132:135], v3 offset:25600\n"
"v_and_b32 v13, s63, v16\n"
"v_mfma_f32_16x16x32_bf16 v[244:247], a[56:59], a[128:131], v[244:247]\n"
"ds_read_b128 a[128:131], v3 offset:24576\n"
"v_lshlrev_b32 v14, 16, v17\n"
"v_mfma_f32_16x16x32_bf16 v[248:251], a[56:59], a[136:139], v[248:251]\n"
"v_and_b32 v15, s63, v17\n"
"v_xor_b32 v20, 64, v7\n"
"v_mfma_f32_16x16x32_bf16 v[248:251], a[60:63], a[140:143], v[248:251]\n"
"ds_read_b128 v[20:23], v20 offset:38912\n"
"v_mul_f32 v12, v12, v44\n"
"v_mfma_f32_16x16x32_bf16 v[220:223], a[52:55], a[140:143], v[220:223]\n"
"ds_read_b128 a[140:143], v3 offset:27648\n"
"v_mul_f32 v13, v13, v45\n"
"v_mfma_f32_16x16x32_bf16 v[220:223], a[48:51], a[136:139], v[220:223]\n"
"ds_read_b128 a[136:139], v3 offset:26624\n"
"v_mul_f32 v14, v14, v46\n"
"v_mfma_f32_16x16x32_bf16 v[224:227], a[48:51], a[144:147], v[224:227]\n"
"v_mul_f32 v15, v15, v47\n"
"v_cvt_pk_bf16_f32 v24, v12, v13\n"
"v_mfma_f32_16x16x32_bf16 v[224:227], a[52:55], a[148:151], v[224:227]\n"
"global_load_dwordx4 a[44:47], v0, s[34:35] offset:-1024 sc1\n"
"v_cvt_pk_bf16_f32 v25, v14, v15\n"
"v_mfma_f32_16x16x32_bf16 v[252:255], a[60:63], a[148:151], v[252:255]\n"
"ds_read_b128 a[148:151], v3 offset:29696\n"
"v_mfma_f32_16x16x32_bf16 v[252:255], a[56:59], a[144:147], v[252:255]\n"
"ds_read_b128 a[144:147], v3 offset:28672\n"
"v_mfma_f32_16x16x32_bf16 a[28:31], a[56:59], a[152:155], a[28:31]\n"
"ds_write_b64 v2, v[24:25] offset:6144\n"
"v_mfma_f32_16x16x32_bf16 a[28:31], a[60:63], a[156:159], a[28:31]\n"
"v_lshlrev_b32 v12, 16, v18\n"
"v_and_b32 v13, s63, v18\n"
"v_mfma_f32_16x16x32_bf16 a[24:27], a[52:55], a[156:159], a[24:27]\n"
"ds_read_b128 a[156:159], v3 offset:31744\n"
"v_lshlrev_b32 v14, 16, v19\n"
"v_mfma_f32_16x16x32_bf16 a[24:27], a[48:51], a[152:155], a[24:27]\n"
"s_waitcnt vmcnt(11) lgkmcnt(14)\n"
"ds_read_b128 a[152:155], v3 offset:30720\n"
"v_and_b32 v15, s63, v19\n"
"v_mfma_f32_16x16x32_bf16 v[144:147], a[64:67], a[96:99], v[144:147]\n"
"v_mul_f32 v12, v12, v72\n"
"v_mul_f32 v13, v13, v73\n"
"v_mfma_f32_16x16x32_bf16 v[144:147], a[68:71], a[100:103], v[144:147]\n"
"global_load_dwordx4 a[48:51], v0, s[34:35] offset:0 sc1\n"
"v_mul_f32 v14, v14, v74\n"
"v_mfma_f32_16x16x32_bf16 v[172:175], a[76:79], a[100:103], v[172:175]\n"
"v_mul_f32 v15, v15, v75\n"
"v_cvt_pk_bf16_f32 v26, v12, v13\n"
"v_mfma_f32_16x16x32_bf16 v[172:175], a[72:75], a[96:99], v[172:175]\n"
"v_cvt_pk_bf16_f32 v27, v14, v15\n"
"v_mfma_f32_16x16x32_bf16 v[176:179], a[72:75], a[104:107], v[176:179]\n"
"s_waitcnt lgkmcnt(7)\n"
"global_store_dwordx4 v0, v[24:27], s[52:53] offset:2048 sc1\n"
"ds_write_b64 v2, v[26:27] offset:6152\n"
"v_lshlrev_b32 v12, 16, v20\n"
"v_mfma_f32_16x16x32_bf16 v[176:179], a[76:79], a[108:111], v[176:179]\n"
"v_and_b32 v13, s63, v20\n"
"v_lshlrev_b32 v14, 16, v21\n"
"v_mfma_f32_16x16x32_bf16 v[148:151], a[68:71], a[108:111], v[148:151]\n"
"v_and_b32 v15, s63, v21\n"
"v_mul_f32 v12, v12, v100\n"
"v_mfma_f32_16x16x32_bf16 v[148:151], a[64:67], a[104:107], v[148:151]\n"
"v_mul_f32 v13, v13, v101\n"
"v_mul_f32 v14, v14, v102\n"
"v_mfma_f32_16x16x32_bf16 v[152:155], a[64:67], a[112:115], v[152:155]\n"
"v_mul_f32 v15, v15, v103\n"
"v_cvt_pk_bf16_f32 v28, v12, v13\n"
"v_mfma_f32_16x16x32_bf16 v[152:155], a[68:71], a[116:119], v[152:155]\n"
"global_load_dwordx4 a[52:55], v0, s[34:35] offset:1024 sc1\n"
"v_cvt_pk_bf16_f32 v29, v14, v15\n"
"v_mfma_f32_16x16x32_bf16 v[180:183], a[76:79], a[116:119], v[180:183]\n"
"ds_write_b64 v2, v[28:29] offset:7168\n"
"v_mfma_f32_16x16x32_bf16 v[180:183], a[72:75], a[112:115], v[180:183]\n"
"v_lshlrev_b32 v12, 16, v22\n"
"v_and_b32 v13, s63, v22\n"
"v_mfma_f32_16x16x32_bf16 v[184:187], a[72:75], a[120:123], v[184:187]\n"
"v_lshlrev_b32 v14, 16, v23\n"
"v_and_b32 v15, s63, v23\n"
"v_mfma_f32_16x16x32_bf16 v[184:187], a[76:79], a[124:127], v[184:187]\n"
"v_mul_f32 v12, v12, v128\n"
"v_mul_f32 v13, v13, v129\n"
"v_mfma_f32_16x16x32_bf16 v[156:159], a[68:71], a[124:127], v[156:159]\n"
"v_mul_f32 v14, v14, v130\n"
"v_mfma_f32_16x16x32_bf16 v[156:159], a[64:67], a[120:123], v[156:159]\n"
"s_waitcnt lgkmcnt(7)\n"
"v_mul_f32 v15, v15, v131\n"
"v_mfma_f32_16x16x32_bf16 v[160:163], a[64:67], a[128:131], v[160:163]\n"
"v_cvt_pk_bf16_f32 v30, v12, v13\n"
"v_cvt_pk_bf16_f32 v31, v14, v15\n"
"v_mfma_f32_16x16x32_bf16 v[160:163], a[68:71], a[132:135], v[160:163]\n"
"global_load_dwordx4 a[56:59], v0, s[34:35] offset:2048 sc1\n"
"v_mfma_f32_16x16x32_bf16 v[188:191], a[76:79], a[132:135], v[188:191]\n"
"global_store_dwordx4 v0, v[28:31], s[52:53] offset:3072 sc1\n"
"ds_write_b64 v2, v[30:31] offset:7176\n"
"v_mfma_f32_16x16x32_bf16 v[188:191], a[72:75], a[128:131], v[188:191]\n"
"v_accvgpr_read_b32 v16, a160\n"
"v_accvgpr_read_b32 v17, a161\n"
"v_mfma_f32_16x16x32_bf16 v[192:195], a[72:75], a[136:139], v[192:195]\n"
"v_accvgpr_read_b32 v18, a162\n"
"v_mfma_f32_16x16x32_bf16 v[192:195], a[76:79], a[140:143], v[192:195]\n"
"v_accvgpr_read_b32 v19, a163\n"
"v_lshlrev_b32 v12, 16, v16\n"
"v_mfma_f32_16x16x32_bf16 v[164:167], a[68:71], a[140:143], v[164:167]\n"
"v_and_b32 v13, s63, v16\n"
"v_mfma_f32_16x16x32_bf16 v[164:167], a[64:67], a[136:139], v[164:167]\n"
"s_waitcnt lgkmcnt(3)\n"
"v_lshlrev_b32 v14, 16, v17\n"
"v_mfma_f32_16x16x32_bf16 v[168:171], a[64:67], a[144:147], v[168:171]\n"
"v_and_b32 v15, s63, v17\n"
"v_mul_f32 v12, v12, v48\n"
"v_mfma_f32_16x16x32_bf16 v[168:171], a[68:71], a[148:151], v[168:171]\n"
"global_load_dwordx4 a[60:63], v0, s[34:35] offset:3072 sc1\n"
"v_mul_f32 v13, v13, v49\n"
"v_mfma_f32_16x16x32_bf16 v[196:199], a[76:79], a[148:151], v[196:199]\n"
"v_mul_f32 v14, v14, v50\n"
"v_mfma_f32_16x16x32_bf16 v[196:199], a[72:75], a[144:147], v[196:199]\n"
"v_mul_f32 v15, v15, v51\n"
"v_cvt_pk_bf16_f32 v24, v12, v13\n"
"v_mfma_f32_16x16x32_bf16 a[20:23], a[72:75], a[152:155], a[20:23]\n"
"s_add_u32 s34, s34, 8192\n"
"v_cvt_pk_bf16_f32 v25, v14, v15\n"
"v_mfma_f32_16x16x32_bf16 a[20:23], a[76:79], a[156:159], a[20:23]\n"
"ds_write_b64 v2, v[24:25] offset:8192\n"
"v_mfma_f32_16x16x32_bf16 a[16:19], a[68:71], a[156:159], a[16:19]\n"
"v_lshlrev_b32 v12, 16, v18\n"
"v_and_b32 v13, s63, v18\n"
"v_mfma_f32_16x16x32_bf16 a[16:19], a[64:67], a[152:155], a[16:19]\n"
"s_waitcnt vmcnt(11)\n"
"v_lshlrev_b32 v14, 16, v19\n"
"v_mfma_f32_16x16x32_bf16 v[200:203], a[80:83], a[96:99], v[200:203]\n"
"v_and_b32 v15, s63, v19\n"
"v_mul_f32 v12, v12, v76\n"
"v_mfma_f32_16x16x32_bf16 v[200:203], a[84:87], a[100:103], v[200:203]\n"
"global_load_dwordx4 a[64:67], v0, s[34:35] offset:-4096 sc1\n"
"v_mul_f32 v13, v13, v77\n"
"v_mfma_f32_16x16x32_bf16 v[228:231], a[92:95], a[100:103], v[228:231]\n"
"ds_read_b128 a[100:103], v4 offset:17408\n"
"v_mul_f32 v14, v14, v78\n"
"v_mfma_f32_16x16x32_bf16 v[228:231], a[88:91], a[96:99], v[228:231]\n"
"ds_read_b128 a[96:99], v4 offset:16384\n"
"v_mul_f32 v15, v15, v79\n"
"v_mfma_f32_16x16x32_bf16 v[232:235], a[88:91], a[104:107], v[232:235]\n"
"v_cvt_pk_bf16_f32 v26, v12, v13\n"
"v_cvt_pk_bf16_f32 v27, v14, v15\n"
"v_mfma_f32_16x16x32_bf16 v[232:235], a[92:95], a[108:111], v[232:235]\n"
"global_store_dwordx4 v0, v[24:27], s[54:55] offset:-4096 sc1\n"
"ds_write_b64 v2, v[26:27] offset:8200\n"
"v_accvgpr_read_b32 v16, a164\n"
"v_mfma_f32_16x16x32_bf16 v[204:207], a[84:87], a[108:111], v[204:207]\n"
"ds_read_b128 a[108:111], v4 offset:19456\n"
"v_accvgpr_read_b32 v17, a165\n"
"v_mfma_f32_16x16x32_bf16 v[204:207], a[80:83], a[104:107], v[204:207]\n"
"ds_read_b128 a[104:107], v4 offset:18432\n"
"v_accvgpr_read_b32 v18, a166\n"
"v_mfma_f32_16x16x32_bf16 v[208:211], a[80:83], a[112:115], v[208:211]\n"
"v_accvgpr_read_b32 v19, a167\n"
"v_lshlrev_b32 v12, 16, v16\n"
"v_mfma_f32_16x16x32_bf16 v[208:211], a[84:87], a[116:119], v[208:211]\n"
"global_load_dwordx4 a[68:71], v0, s[34:35] offset:-3072 sc1\n"
"v_and_b32 v13, s63, v16\n"
"v_mfma_f32_16x16x32_bf16 v[236:239], a[92:95], a[116:119], v[236:239]\n"
"ds_read_b128 a[116:119], v4 offset:21504\n"
"v_lshlrev_b32 v14, 16, v17\n"
"v_mfma_f32_16x16x32_bf16 v[236:239], a[88:91], a[112:115], v[236:239]\n"
"ds_read_b128 a[112:115], v4 offset:20480\n"
"v_and_b32 v15, s63, v17\n"
"v_mfma_f32_16x16x32_bf16 v[240:243], a[88:91], a[120:123], v[240:243]\n"
"v_mul_f32 v12, v12, v104\n"
"v_mul_f32 v13, v13, v105\n"
"v_mfma_f32_16x16x32_bf16 v[240:243], a[92:95], a[124:127], v[240:243]\n"
"v_mul_f32 v14, v14, v106\n"
"v_mul_f32 v15, v15, v107\n"
"v_mfma_f32_16x16x32_bf16 v[212:215], a[84:87], a[124:127], v[212:215]\n"
"ds_read_b128 a[124:127], v4 offset:23552\n"
"v_cvt_pk_bf16_f32 v28, v12, v13\n"
"v_mfma_f32_16x16x32_bf16 v[212:215], a[80:83], a[120:123], v[212:215]\n"
"ds_read_b128 a[120:123], v4 offset:22528\n"
"v_cvt_pk_bf16_f32 v29, v14, v15\n"
"v_mfma_f32_16x16x32_bf16 v[216:219], a[80:83], a[128:131], v[216:219]\n"
"ds_write_b64 v2, v[28:29] offset:9216\n"
"v_mfma_f32_16x16x32_bf16 v[216:219], a[84:87], a[132:135], v[216:219]\n"
"global_load_dwordx4 a[72:75], v0, s[34:35] offset:-2048 sc1\n"
"v_lshlrev_b32 v12, 16, v18\n"
"v_mfma_f32_16x16x32_bf16 v[244:247], a[92:95], a[132:135], v[244:247]\n"
"ds_read_b128 a[132:135], v4 offset:25600\n"
"v_and_b32 v13, s63, v18\n"
"v_mfma_f32_16x16x32_bf16 v[244:247], a[88:91], a[128:131], v[244:247]\n"
"ds_read_b128 a[128:131], v4 offset:24576\n"
"v_lshlrev_b32 v14, 16, v19\n"
"v_mfma_f32_16x16x32_bf16 v[248:251], a[88:91], a[136:139], v[248:251]\n"
"v_and_b32 v15, s63, v19\n"
"v_mul_f32 v12, v12, v132\n"
"v_mfma_f32_16x16x32_bf16 v[248:251], a[92:95], a[140:143], v[248:251]\n"
"v_mul_f32 v13, v13, v133\n"
"v_mul_f32 v14, v14, v134\n"
"v_mfma_f32_16x16x32_bf16 v[220:223], a[84:87], a[140:143], v[220:223]\n"
"ds_read_b128 a[140:143], v4 offset:27648\n"
"v_mul_f32 v15, v15, v135\n"
"v_mfma_f32_16x16x32_bf16 v[220:223], a[80:83], a[136:139], v[220:223]\n"
"ds_read_b128 a[136:139], v4 offset:26624\n"
"v_cvt_pk_bf16_f32 v30, v12, v13\n"
"v_mfma_f32_16x16x32_bf16 v[224:227], a[80:83], a[144:147], v[224:227]\n"
"v_cvt_pk_bf16_f32 v31, v14, v15\n"
"v_mfma_f32_16x16x32_bf16 v[224:227], a[84:87], a[148:151], v[224:227]\n"
"global_load_dwordx4 a[76:79], v0, s[34:35] offset:-1024 sc1\n"
"v_mfma_f32_16x16x32_bf16 v[252:255], a[92:95], a[148:151], v[252:255]\n"
"global_store_dwordx4 v0, v[28:31], s[54:55] offset:-3072 sc1\n"
"ds_read_b128 a[148:151], v4 offset:29696\n"
"ds_write_b64 v2, v[30:31] offset:9224\n"
"v_mfma_f32_16x16x32_bf16 v[252:255], a[88:91], a[144:147], v[252:255]\n"
"ds_read_b128 a[144:147], v4 offset:28672\n"
"v_accvgpr_read_b32 v16, a168\n"
"v_mfma_f32_16x16x32_bf16 a[28:31], a[88:91], a[152:155], a[28:31]\n"
"v_accvgpr_read_b32 v17, a169\n"
"v_accvgpr_read_b32 v18, a170\n"
"v_mfma_f32_16x16x32_bf16 a[28:31], a[92:95], a[156:159], a[28:31]\n"
"v_accvgpr_read_b32 v19, a171\n"
"v_lshlrev_b32 v12, 16, v16\n"
"v_mfma_f32_16x16x32_bf16 a[24:27], a[84:87], a[156:159], a[24:27]\n"
"ds_read_b128 a[156:159], v4 offset:31744\n"
"v_and_b32 v13, s63, v16\n"
"v_mfma_f32_16x16x32_bf16 a[24:27], a[80:83], a[152:155], a[24:27]\n"
"s_waitcnt vmcnt(12) lgkmcnt(13)\n"
"ds_read_b128 a[152:155], v4 offset:30720\n"
"v_lshlrev_b32 v14, 16, v17\n"
"v_mfma_f32_16x16x32_bf16 v[144:147], a[32:35], a[96:99], v[144:147]\n"
"v_and_b32 v15, s63, v17\n"
"v_mul_f32 v12, v12, v52\n"
"v_mfma_f32_16x16x32_bf16 v[144:147], a[36:39], a[100:103], v[144:147]\n"
"global_load_dwordx4 a[80:83], v0, s[34:35] offset:0 sc1\n"
"v_mul_f32 v13, v13, v53\n"
"v_mfma_f32_16x16x32_bf16 v[172:175], a[44:47], a[100:103], v[172:175]\n"
"v_mul_f32 v14, v14, v54\n"
"v_mul_f32 v15, v15, v55\n"
"v_mfma_f32_16x16x32_bf16 v[172:175], a[40:43], a[96:99], v[172:175]\n"
"v_cvt_pk_bf16_f32 v24, v12, v13\n"
"v_cvt_pk_bf16_f32 v25, v14, v15\n"
"v_mfma_f32_16x16x32_bf16 v[176:179], a[40:43], a[104:107], v[176:179]\n"
"ds_write_b64 v2, v[24:25] offset:10240\n"
"v_mfma_f32_16x16x32_bf16 v[176:179], a[44:47], a[108:111], v[176:179]\n"
"v_lshlrev_b32 v12, 16, v18\n"
"v_mfma_f32_16x16x32_bf16 v[148:151], a[36:39], a[108:111], v[148:151]\n"
"v_and_b32 v13, s63, v18\n"
"v_lshlrev_b32 v14, 16, v19\n"
"v_mfma_f32_16x16x32_bf16 v[148:151], a[32:35], a[104:107], v[148:151]\n"
"s_waitcnt lgkmcnt(11)\n"
"v_and_b32 v15, s63, v19\n"
"v_mfma_f32_16x16x32_bf16 v[152:155], a[32:35], a[112:115], v[152:155]\n"
"v_mul_f32 v12, v12, v80\n"
"v_mul_f32 v13, v13, v81\n"
"v_mfma_f32_16x16x32_bf16 v[152:155], a[36:39], a[116:119], v[152:155]\n"
"global_load_dwordx4 a[84:87], v0, s[34:35] offset:1024 sc1\n"
"v_mul_f32 v14, v14, v82\n"
"v_mfma_f32_16x16x32_bf16 v[180:183], a[44:47], a[116:119], v[180:183]\n"
"v_mul_f32 v15, v15, v83\n"
"v_mfma_f32_16x16x32_bf16 v[180:183], a[40:43], a[112:115], v[180:183]\n"
"v_cvt_pk_bf16_f32 v26, v12, v13\n"
"v_cvt_pk_bf16_f32 v27, v14, v15\n"
"v_mfma_f32_16x16x32_bf16 v[184:187], a[40:43], a[120:123], v[184:187]\n"
"ds_write_b64 v2, v[26:27] offset:10248\n"
"v_mfma_f32_16x16x32_bf16 v[184:187], a[44:47], a[124:127], v[184:187]\n"
"v_accvgpr_read_b32 v16, a172\n"
"v_mfma_f32_16x16x32_bf16 v[156:159], a[36:39], a[124:127], v[156:159]\n"
"v_accvgpr_read_b32 v17, a173\n"
"v_accvgpr_read_b32 v18, a174\n"
"v_mfma_f32_16x16x32_bf16 v[156:159], a[32:35], a[120:123], v[156:159]\n"
"s_waitcnt lgkmcnt(7)\n"
"v_accvgpr_read_b32 v19, a175\n"
"v_mfma_f32_16x16x32_bf16 v[160:163], a[32:35], a[128:131], v[160:163]\n"
"v_lshlrev_b32 v12, 16, v16\n"
"v_and_b32 v13, s63, v16\n"
"v_mfma_f32_16x16x32_bf16 v[160:163], a[36:39], a[132:135], v[160:163]\n"
"global_load_dwordx4 a[88:91], v0, s[34:35] offset:2048 sc1\n"
"v_lshlrev_b32 v14, 16, v17\n"
"v_mfma_f32_16x16x32_bf16 v[188:191], a[44:47], a[132:135], v[188:191]\n"
"v_and_b32 v15, s63, v17\n"
"v_mfma_f32_16x16x32_bf16 v[188:191], a[40:43], a[128:131], v[188:191]\n"
"v_mul_f32 v12, v12, v108\n"
"v_mul_f32 v13, v13, v109\n"
"v_mfma_f32_16x16x32_bf16 v[192:195], a[40:43], a[136:139], v[192:195]\n"
"v_mul_f32 v14, v14, v110\n"
"v_mfma_f32_16x16x32_bf16 v[192:195], a[44:47], a[140:143], v[192:195]\n"
"v_mul_f32 v15, v15, v111\n"
"v_mfma_f32_16x16x32_bf16 v[164:167], a[36:39], a[140:143], v[164:167]\n"
"v_cvt_pk_bf16_f32 v28, v12, v13\n"
"v_cvt_pk_bf16_f32 v29, v14, v15\n"
"v_mfma_f32_16x16x32_bf16 v[164:167], a[32:35], a[136:139], v[164:167]\n"
"s_waitcnt lgkmcnt(2)\n"
"ds_write_b64 v2, v[28:29] offset:11264\n"
"v_mfma_f32_16x16x32_bf16 v[168:171], a[32:35], a[144:147], v[168:171]\n"
"v_lshlrev_b32 v12, 16, v18\n"
"v_and_b32 v13, s63, v18\n"
"v_mfma_f32_16x16x32_bf16 v[168:171], a[36:39], a[148:151], v[168:171]\n"
"global_load_dwordx4 a[92:95], v0, s[34:35] offset:3072 sc1\n"
"v_lshlrev_b32 v14, 16, v19\n"
"v_mfma_f32_16x16x32_bf16 v[196:199], a[44:47], a[148:151], v[196:199]\n"
"v_and_b32 v15, s63, v19\n"
"v_mfma_f32_16x16x32_bf16 v[196:199], a[40:43], a[144:147], v[196:199]\n"
"v_mul_f32 v12, v12, v136\n"
"v_mul_f32 v13, v13, v137\n"
"v_mfma_f32_16x16x32_bf16 a[20:23], a[40:43], a[152:155], a[20:23]\n"
"s_add_u32 s34, s34, 8192\n"
"v_mul_f32 v14, v14, v138\n"
"v_mfma_f32_16x16x32_bf16 a[20:23], a[44:47], a[156:159], a[20:23]\n"
"v_mul_f32 v15, v15, v139\n"
"v_mfma_f32_16x16x32_bf16 a[16:19], a[36:39], a[156:159], a[16:19]\n"
"v_cvt_pk_bf16_f32 v30, v12, v13\n"
"v_cvt_pk_bf16_f32 v31, v14, v15\n"
"v_mfma_f32_16x16x32_bf16 a[16:19], a[32:35], a[152:155], a[16:19]\n"
"s_waitcnt vmcnt(10)\n"
"global_store_dwordx4 v0, v[28:31], s[54:55] offset:-1024 sc1\n"
"ds_write_b64 v2, v[30:31] offset:11272\n"
"v_mfma_f32_16x16x32_bf16 v[200:203], a[48:51], a[96:99], v[200:203]\n"
"v_accvgpr_read_b32 v16, a176\n"
"v_mfma_f32_16x16x32_bf16 v[200:203], a[52:55], a[100:103], v[200:203]\n"
"global_load_dwordx4 a[32:35], v0, s[34:35] offset:-4096 sc1\n"
"v_accvgpr_read_b32 v17, a177\n"
"v_mfma_f32_16x16x32_bf16 v[228:231], a[60:63], a[100:103], v[228:231]\n"
"ds_read_b128 a[100:103], v5 offset:17408\n"
"v_accvgpr_read_b32 v18, a178\n"
"v_mfma_f32_16x16x32_bf16 v[228:231], a[56:59], a[96:99], v[228:231]\n"
"ds_read_b128 a[96:99], v5 offset:16384\n"
"v_accvgpr_read_b32 v19, a179\n"
"v_mfma_f32_16x16x32_bf16 v[232:235], a[56:59], a[104:107], v[232:235]\n"
"v_lshlrev_b32 v12, 16, v16\n"
"v_and_b32 v13, s63, v16\n"
"v_mfma_f32_16x16x32_bf16 v[232:235], a[60:63], a[108:111], v[232:235]\n"
"v_lshlrev_b32 v14, 16, v17\n"
"v_and_b32 v15, s63, v17\n"
"v_mfma_f32_16x16x32_bf16 v[204:207], a[52:55], a[108:111], v[204:207]\n"
"ds_read_b128 a[108:111], v5 offset:19456\n"
"v_mul_f32 v12, v12, v56\n"
"v_mfma_f32_16x16x32_bf16 v[204:207], a[48:51], a[104:107], v[204:207]\n"
"ds_read_b128 a[104:107], v5 offset:18432\n"
"v_mul_f32 v13, v13, v57\n"
"v_mfma_f32_16x16x32_bf16 v[208:211], a[48:51], a[112:115], v[208:211]\n"
"v_mul_f32 v14, v14, v58\n"
"v_mul_f32 v15, v15, v59\n"
"v_mfma_f32_16x16x32_bf16 v[208:211], a[52:55], a[116:119], v[208:211]\n"
"global_load_dwordx4 a[36:39], v0, s[34:35] offset:-3072 sc1\n"
"v_cvt_pk_bf16_f32 v24, v12, v13\n"
"v_mfma_f32_16x16x32_bf16 v[236:239], a[60:63], a[116:119], v[236:239]\n"
"ds_read_b128 a[116:119], v5 offset:21504\n"
"v_cvt_pk_bf16_f32 v25, v14, v15\n"
"v_mfma_f32_16x16x32_bf16 v[236:239], a[56:59], a[112:115], v[236:239]\n"
"ds_read_b128 a[112:115], v5 offset:20480\n"
"v_mfma_f32_16x16x32_bf16 v[240:243], a[56:59], a[120:123], v[240:243]\n"
"ds_write_b64 v2, v[24:25] offset:12288\n"
"v_mfma_f32_16x16x32_bf16 v[240:243], a[60:63], a[124:127], v[240:243]\n"
"v_lshlrev_b32 v12, 16, v18\n"
"v_and_b32 v13, s63, v18\n"
"v_mfma_f32_16x16x32_bf16 v[212:215], a[52:55], a[124:127], v[212:215]\n"
"ds_read_b128 a[124:127], v5 offset:23552\n"
"v_lshlrev_b32 v14, 16, v19\n"
"v_mfma_f32_16x16x32_bf16 v[212:215], a[48:51], a[120:123], v[212:215]\n"
"ds_read_b128 a[120:123], v5 offset:22528\n"
"v_and_b32 v15, s63, v19\n"
"v_mfma_f32_16x16x32_bf16 v[216:219], a[48:51], a[128:131], v[216:219]\n"
"v_mul_f32 v12, v12, v84\n"
"v_mul_f32 v13, v13, v85\n"
"v_mfma_f32_16x16x32_bf16 v[216:219], a[52:55], a[132:135], v[216:219]\n"
"global_load_dwordx4 a[40:43], v0, s[34:35] offset:-2048 sc1\n"
"v_mul_f32 v14, v14, v86\n"
"v_mfma_f32_16x16x32_bf16 v[244:247], a[60:63], a[132:135], v[244:247]\n"
"ds_read_b128 a[132:135], v5 offset:25600\n"
"v_mul_f32 v15, v15, v87\n"
"v_mfma_f32_16x16x32_bf16 v[244:247], a[56:59], a[128:131], v[244:247]\n"
"ds_read_b128 a[128:131], v5 offset:24576\n"
"v_cvt_pk_bf16_f32 v26, v12, v13\n"
"v_mfma_f32_16x16x32_bf16 v[248:251], a[56:59], a[136:139], v[248:251]\n"
"v_cvt_pk_bf16_f32 v27, v14, v15\n"
"v_mfma_f32_16x16x32_bf16 v[248:251], a[60:63], a[140:143], v[248:251]\n"
"global_store_dwordx4 v0, v[24:27], s[54:55] offset:0 sc1\n"
"ds_write_b64 v2, v[26:27] offset:12296\n"
"v_accvgpr_read_b32 v16, a180\n"
"v_mfma_f32_16x16x32_bf16 v[220:223], a[52:55], a[140:143], v[220:223]\n"
"ds_read_b128 a[140:143], v5 offset:27648\n"
"v_accvgpr_read_b32 v17, a181\n"
"v_mfma_f32_16x16x32_bf16 v[220:223], a[48:51], a[136:139], v[220:223]\n"
"ds_read_b128 a[136:139], v5 offset:26624\n"
"v_accvgpr_read_b32 v18, a182\n"
"v_mfma_f32_16x16x32_bf16 v[224:227], a[48:51], a[144:147], v[224:227]\n"
"v_accvgpr_read_b32 v19, a183\n"
"v_lshlrev_b32 v12, 16, v16\n"
"v_mfma_f32_16x16x32_bf16 v[224:227], a[52:55], a[148:151], v[224:227]\n"
"global_load_dwordx4 a[44:47], v0, s[34:35] offset:-1024 sc1\n"
"v_and_b32 v13, s63, v16\n"
"v_mfma_f32_16x16x32_bf16 v[252:255], a[60:63], a[148:151], v[252:255]\n"
"ds_read_b128 a[148:151], v5 offset:29696\n"
"v_lshlrev_b32 v14, 16, v17\n"
"v_mfma_f32_16x16x32_bf16 v[252:255], a[56:59], a[144:147], v[252:255]\n"
"ds_read_b128 a[144:147], v5 offset:28672\n"
"v_and_b32 v15, s63, v17\n"
"v_mfma_f32_16x16x32_bf16 a[28:31], a[56:59], a[152:155], a[28:31]\n"
"v_mul_f32 v12, v12, v112\n"
"v_mul_f32 v13, v13, v113\n"
"v_mfma_f32_16x16x32_bf16 a[28:31], a[60:63], a[156:159], a[28:31]\n"
"v_mul_f32 v14, v14, v114\n"
"v_mul_f32 v15, v15, v115\n"
"v_mfma_f32_16x16x32_bf16 a[24:27], a[52:55], a[156:159], a[24:27]\n"
"ds_read_b128 a[156:159], v5 offset:31744\n"
"v_cvt_pk_bf16_f32 v28, v12, v13\n"
"v_mfma_f32_16x16x32_bf16 a[24:27], a[48:51], a[152:155], a[24:27]\n"
"s_waitcnt vmcnt(11) lgkmcnt(13)\n"
"ds_read_b128 a[152:155], v5 offset:30720\n"
"v_cvt_pk_bf16_f32 v29, v14, v15\n"
"v_mfma_f32_16x16x32_bf16 v[144:147], a[64:67], a[96:99], v[144:147]\n"
"ds_write_b64 v2, v[28:29] offset:13312\n"
"v_mfma_f32_16x16x32_bf16 v[144:147], a[68:71], a[100:103], v[144:147]\n"
"global_load_dwordx4 a[48:51], v0, s[34:35] offset:0 sc1\n"
"v_lshlrev_b32 v12, 16, v18\n"
"v_mfma_f32_16x16x32_bf16 v[172:175], a[76:79], a[100:103], v[172:175]\n"
"v_and_b32 v13, s63, v18\n"
"v_lshlrev_b32 v14, 16, v19\n"
"v_mfma_f32_16x16x32_bf16 v[172:175], a[72:75], a[96:99], v[172:175]\n"
"v_and_b32 v15, s63, v19\n"
"v_mul_f32 v12, v12, v140\n"
"v_mfma_f32_16x16x32_bf16 v[176:179], a[72:75], a[104:107], v[176:179]\n"
"v_mul_f32 v13, v13, v141\n"
"v_mul_f32 v14, v14, v142\n"
"v_mfma_f32_16x16x32_bf16 v[176:179], a[76:79], a[108:111], v[176:179]\n"
"v_mul_f32 v15, v15, v143\n"
"v_cvt_pk_bf16_f32 v30, v12, v13\n"
"v_mfma_f32_16x16x32_bf16 v[148:151], a[68:71], a[108:111], v[148:151]\n"
"v_cvt_pk_bf16_f32 v31, v14, v15\n"
"v_mfma_f32_16x16x32_bf16 v[148:151], a[64:67], a[104:107], v[148:151]\n"
"s_waitcnt lgkmcnt(10)\n"
"global_store_dwordx4 v0, v[28:31], s[54:55] offset:1024 sc1\n"
"ds_write_b64 v2, v[30:31] offset:13320\n"
"v_mfma_f32_16x16x32_bf16 v[152:155], a[64:67], a[112:115], v[152:155]\n"
"v_accvgpr_read_b32 v16, a184\n"
"v_accvgpr_read_b32 v17, a185\n"
"v_mfma_f32_16x16x32_bf16 v[152:155], a[68:71], a[116:119], v[152:155]\n"
"global_load_dwordx4 a[52:55], v0, s[34:35] offset:1024 sc1\n"
"v_accvgpr_read_b32 v18, a186\n"
"v_mfma_f32_16x16x32_bf16 v[180:183], a[76:79], a[116:119], v[180:183]\n"
"v_accvgpr_read_b32 v19, a187\n"
"v_mfma_f32_16x16x32_bf16 v[180:183], a[72:75], a[112:115], v[180:183]\n"
"v_lshlrev_b32 v12, 16, v16\n"
"v_and_b32 v13, s63, v16\n"
"v_mfma_f32_16x16x32_bf16 v[184:187], a[72:75], a[120:123], v[184:187]\n"
"v_lshlrev_b32 v14, 16, v17\n"
"v_mfma_f32_16x16x32_bf16 v[184:187], a[76:79], a[124:127], v[184:187]\n"
"v_and_b32 v15, s63, v17\n"
"v_mfma_f32_16x16x32_bf16 v[156:159], a[68:71], a[124:127], v[156:159]\n"
"v_accvgpr_read_b32 v10, a0\n"
"v_accvgpr_read_b32 v11, a1\n"
"v_mfma_f32_16x16x32_bf16 v[156:159], a[64:67], a[120:123], v[156:159]\n"
"s_waitcnt lgkmcnt(6)\n"
"v_mul_f32 v12, v12, v10\n"
"v_mfma_f32_16x16x32_bf16 v[160:163], a[64:67], a[128:131], v[160:163]\n"
"v_mul_f32 v13, v13, v11\n"
"v_accvgpr_read_b32 v10, a2\n"
"v_mfma_f32_16x16x32_bf16 v[160:163], a[68:71], a[132:135], v[160:163]\n"
"global_load_dwordx4 a[56:59], v0, s[34:35] offset:2048 sc1\n"
"v_accvgpr_read_b32 v11, a3\n"
"v_mfma_f32_16x16x32_bf16 v[188:191], a[76:79], a[132:135], v[188:191]\n"
"v_mul_f32 v14, v14, v10\n"
"v_mfma_f32_16x16x32_bf16 v[188:191], a[72:75], a[128:131], v[188:191]\n"
"v_mul_f32 v15, v15, v11\n"
"v_mfma_f32_16x16x32_bf16 v[192:195], a[72:75], a[136:139], v[192:195]\n"
"v_cvt_pk_bf16_f32 v24, v12, v13\n"
"v_cvt_pk_bf16_f32 v25, v14, v15\n"
"v_mfma_f32_16x16x32_bf16 v[192:195], a[76:79], a[140:143], v[192:195]\n"
"ds_write_b64 v2, v[24:25] offset:14336\n"
"v_mfma_f32_16x16x32_bf16 v[164:167], a[68:71], a[140:143], v[164:167]\n"
"v_lshlrev_b32 v12, 16, v18\n"
"v_and_b32 v13, s63, v18\n"
"v_mfma_f32_16x16x32_bf16 v[164:167], a[64:67], a[136:139], v[164:167]\n"
"s_waitcnt lgkmcnt(3)\n"
"v_lshlrev_b32 v14, 16, v19\n"
"v_mfma_f32_16x16x32_bf16 v[168:171], a[64:67], a[144:147], v[168:171]\n"
"v_and_b32 v15, s63, v19\n"
"v_accvgpr_read_b32 v10, a4\n"
"v_mfma_f32_16x16x32_bf16 v[168:171], a[68:71], a[148:151], v[168:171]\n"
"global_load_dwordx4 a[60:63], v0, s[34:35] offset:3072 sc1\n"
"v_accvgpr_read_b32 v11, a5\n"
"v_mfma_f32_16x16x32_bf16 v[196:199], a[76:79], a[148:151], v[196:199]\n"
"v_mul_f32 v12, v12, v10\n"
"v_mfma_f32_16x16x32_bf16 v[196:199], a[72:75], a[144:147], v[196:199]\n"
"v_mul_f32 v13, v13, v11\n"
"v_mfma_f32_16x16x32_bf16 a[20:23], a[72:75], a[152:155], a[20:23]\n"
"s_add_u32 s34, s34, 8192\n"
"v_accvgpr_read_b32 v10, a6\n"
"v_accvgpr_read_b32 v11, a7\n"
"v_mfma_f32_16x16x32_bf16 a[20:23], a[76:79], a[156:159], a[20:23]\n"
"v_mul_f32 v14, v14, v10\n"
"v_mul_f32 v15, v15, v11\n"
"v_mfma_f32_16x16x32_bf16 a[16:19], a[68:71], a[156:159], a[16:19]\n"
"v_cvt_pk_bf16_f32 v26, v12, v13\n"
"v_cvt_pk_bf16_f32 v27, v14, v15\n"
"v_mfma_f32_16x16x32_bf16 a[16:19], a[64:67], a[152:155], a[16:19]\n"
"s_waitcnt vmcnt(11)\n"
"global_store_dwordx4 v0, v[24:27], s[54:55] offset:2048 sc1\n"
"ds_write_b64 v2, v[26:27] offset:14344\n"
"v_mfma_f32_16x16x32_bf16 v[200:203], a[80:83], a[96:99], v[200:203]\n"
"v_accvgpr_read_b32 v16, a188\n"
"v_accvgpr_read_b32 v17, a189\n"
"v_mfma_f32_16x16x32_bf16 v[200:203], a[84:87], a[100:103], v[200:203]\n"
"global_load_dwordx4 a[64:67], v0, s[34:35] offset:-4096 sc1\n"
"v_accvgpr_read_b32 v18, a190\n"
"v_mfma_f32_16x16x32_bf16 v[228:231], a[92:95], a[100:103], v[228:231]\n"
"ds_read_b128 a[100:103], v2 offset:1024\n"
"v_accvgpr_read_b32 v19, a191\n"
"v_mfma_f32_16x16x32_bf16 v[228:231], a[88:91], a[96:99], v[228:231]\n"
"ds_read_b128 a[96:99], v2 offset:0\n"
"v_lshlrev_b32 v12, 16, v16\n"
"v_mfma_f32_16x16x32_bf16 v[232:235], a[88:91], a[104:107], v[232:235]\n"
"v_and_b32 v13, s63, v16\n"
"v_lshlrev_b32 v14, 16, v17\n"
"v_mfma_f32_16x16x32_bf16 v[232:235], a[92:95], a[108:111], v[232:235]\n"
"v_and_b32 v15, s63, v17\n"
"v_accvgpr_read_b32 v10, a8\n"
"v_mfma_f32_16x16x32_bf16 v[204:207], a[84:87], a[108:111], v[204:207]\n"
"ds_read_b128 a[108:111], v2 offset:3072\n"
"v_accvgpr_read_b32 v11, a9\n"
"v_mfma_f32_16x16x32_bf16 v[204:207], a[80:83], a[104:107], v[204:207]\n"
"ds_read_b128 a[104:107], v2 offset:2048\n"
"v_mul_f32 v12, v12, v10\n"
"v_mfma_f32_16x16x32_bf16 v[208:211], a[80:83], a[112:115], v[208:211]\n"
"v_mul_f32 v13, v13, v11\n"
"v_accvgpr_read_b32 v10, a10\n"
"v_mfma_f32_16x16x32_bf16 v[208:211], a[84:87], a[116:119], v[208:211]\n"
"global_load_dwordx4 a[68:71], v0, s[34:35] offset:-3072 sc1\n"
"v_accvgpr_read_b32 v11, a11\n"
"v_mfma_f32_16x16x32_bf16 v[236:239], a[92:95], a[116:119], v[236:239]\n"
"ds_read_b128 a[116:119], v2 offset:5120\n"
"v_mul_f32 v14, v14, v10\n"
"v_mfma_f32_16x16x32_bf16 v[236:239], a[88:91], a[112:115], v[236:239]\n"
"ds_read_b128 a[112:115], v2 offset:4096\n"
"v_mul_f32 v15, v15, v11\n"
"v_mfma_f32_16x16x32_bf16 v[240:243], a[88:91], a[120:123], v[240:243]\n"
"v_cvt_pk_bf16_f32 v28, v12, v13\n"
"v_cvt_pk_bf16_f32 v29, v14, v15\n"
"v_mfma_f32_16x16x32_bf16 v[240:243], a[92:95], a[124:127], v[240:243]\n"
"ds_write_b64 v2, v[28:29] offset:15360\n"
"v_mfma_f32_16x16x32_bf16 v[212:215], a[84:87], a[124:127], v[212:215]\n"
"ds_read_b128 a[124:127], v2 offset:7168\n"
"v_mfma_f32_16x16x32_bf16 v[212:215], a[80:83], a[120:123], v[212:215]\n"
"ds_read_b128 a[120:123], v2 offset:6144\n"
"v_lshlrev_b32 v12, 16, v18\n"
"v_mfma_f32_16x16x32_bf16 v[216:219], a[80:83], a[128:131], v[216:219]\n"
"v_and_b32 v13, s63, v18\n"
"v_lshlrev_b32 v14, 16, v19\n"
"v_mfma_f32_16x16x32_bf16 v[216:219], a[84:87], a[132:135], v[216:219]\n"
"global_load_dwordx4 a[72:75], v0, s[34:35] offset:-2048 sc1\n"
"v_and_b32 v15, s63, v19\n"
"v_mfma_f32_16x16x32_bf16 v[244:247], a[92:95], a[132:135], v[244:247]\n"
"ds_read_b128 a[132:135], v2 offset:9216\n"
"v_accvgpr_read_b32 v10, a12\n"
"v_mfma_f32_16x16x32_bf16 v[244:247], a[88:91], a[128:131], v[244:247]\n"
"ds_read_b128 a[128:131], v2 offset:8192\n"
"v_accvgpr_read_b32 v11, a13\n"
"v_mfma_f32_16x16x32_bf16 v[248:251], a[88:91], a[136:139], v[248:251]\n"
"v_mul_f32 v12, v12, v10\n"
"v_mul_f32 v13, v13, v11\n"
"v_mfma_f32_16x16x32_bf16 v[248:251], a[92:95], a[140:143], v[248:251]\n"
"v_accvgpr_read_b32 v10, a14\n"
"v_accvgpr_read_b32 v11, a15\n"
"v_mfma_f32_16x16x32_bf16 v[220:223], a[84:87], a[140:143], v[220:223]\n"
"ds_read_b128 a[140:143], v2 offset:11264\n"
"v_mul_f32 v14, v14, v10\n"
"v_mfma_f32_16x16x32_bf16 v[220:223], a[80:83], a[136:139], v[220:223]\n"
"ds_read_b128 a[136:139], v2 offset:10240\n"
"v_mul_f32 v15, v15, v11\n"
"v_mfma_f32_16x16x32_bf16 v[224:227], a[80:83], a[144:147], v[224:227]\n"
"v_cvt_pk_bf16_f32 v30, v12, v13\n"
"v_mfma_f32_16x16x32_bf16 v[224:227], a[84:87], a[148:151], v[224:227]\n"
"global_load_dwordx4 a[76:79], v0, s[34:35] offset:-1024 sc1\n"
"v_cvt_pk_bf16_f32 v31, v14, v15\n"
"v_mfma_f32_16x16x32_bf16 v[252:255], a[92:95], a[148:151], v[252:255]\n"
"global_store_dwordx4 v0, v[28:31], s[54:55] offset:3072 sc1\n"
"ds_read_b128 a[148:151], v2 offset:13312\n"
"ds_write_b64 v2, v[30:31] offset:15368\n"
"v_mfma_f32_16x16x32_bf16 v[252:255], a[88:91], a[144:147], v[252:255]\n"
"ds_read_b128 a[144:147], v2 offset:12288\n"
"v_mfma_f32_16x16x32_bf16 a[28:31], a[88:91], a[152:155], a[28:31]\n"
"s_add_u32 s62, s62, 1\n"
"v_mfma_f32_16x16x32_bf16 a[28:31], a[92:95], a[156:159], a[28:31]\n"
"v_mfma_f32_16x16x32_bf16 a[24:27], a[84:87], a[156:159], a[24:27]\n"
"ds_read_b128 a[156:159], v2 offset:15360\n"
"v_mfma_f32_16x16x32_bf16 a[24:27], a[80:83], a[152:155], a[24:27]\n"
"ds_read_b128 a[152:155], v2 offset:14336\n"
"s_branch L_layer\n"
"L_last:\n"
"s_waitcnt vmcnt(8) lgkmcnt(13)\n"
"v_mfma_f32_16x16x32_bf16 v[32:35], a[64:67], a[96:99], v[32:35]\n"
"v_mfma_f32_16x16x32_bf16 v[32:35], a[68:71], a[100:103], v[32:35]\n"
"global_load_dwordx4 a[48:51], v0, s[34:35] offset:0 sc1\n"
"v_mfma_f32_16x16x32_bf16 v[60:63], a[76:79], a[100:103], v[60:63]\n"
"v_mfma_f32_16x16x32_bf16 v[60:63], a[72:75], a[96:99], v[60:63]\n"
"v_mfma_f32_16x16x32_bf16 v[64:67], a[72:75], a[104:107], v[64:67]\n"
"v_mfma_f32_16x16x32_bf16 v[64:67], a[76:79], a[108:111], v[64:67]\n"
"v_mfma_f32_16x16x32_bf16 v[36:39], a[68:71], a[108:111], v[36:39]\n"
"s_mov_b32 s52, %4\n"
"s_and_b32 s53, %5, 0xffff\n"
"v_mfma_f32_16x16x32_bf16 v[36:39], a[64:67], a[104:107], v[36:39]\n"
"s_mov_b32 s54, 260352\n"
"s_waitcnt lgkmcnt(9)\n"
"v_mfma_f32_16x16x32_bf16 v[40:43], a[64:67], a[112:115], v[40:43]\n"
"s_mov_b32 s55, 0x20000\n"
"v_xor_b32 v8, 64, v7\n"
"v_mfma_f32_16x16x32_bf16 v[40:43], a[68:71], a[116:119], v[40:43]\n"
"global_load_dwordx4 a[52:55], v0, s[34:35] offset:1024 sc1\n"
"v_mfma_f32_16x16x32_bf16 v[68:71], a[76:79], a[116:119], v[68:71]\n"
"s_waitcnt lgkmcnt(6)\n"
"v_lshlrev_b32 v12, 16, v16\n"
"v_and_b32 v13, s63, v16\n"
"v_mfma_f32_16x16x32_bf16 v[68:71], a[72:75], a[112:115], v[68:71]\n"
"v_lshlrev_b32 v14, 16, v17\n"
"v_mfma_f32_16x16x32_bf16 v[72:75], a[72:75], a[120:123], v[72:75]\n"
"ds_read_b128 v[20:23], v7 offset:34816\n"
"v_and_b32 v15, s63, v17\n"
"v_mfma_f32_16x16x32_bf16 v[72:75], a[76:79], a[124:127], v[72:75]\n"
"v_mul_f32 v12, v12, v32\n"
"v_mfma_f32_16x16x32_bf16 v[44:47], a[68:71], a[124:127], v[44:47]\n"
"v_mul_f32 v13, v13, v33\n"
"v_mfma_f32_16x16x32_bf16 v[44:47], a[64:67], a[120:123], v[44:47]\n"
"s_waitcnt lgkmcnt(5)\n"
"v_mul_f32 v14, v14, v34\n"
"v_mul_f32 v15, v15, v35\n"
"v_mfma_f32_16x16x32_bf16 v[48:51], a[64:67], a[128:131], v[48:51]\n"
"v_cvt_pk_bf16_f32 v24, v12, v13\n"
"v_mfma_f32_16x16x32_bf16 v[48:51], a[68:71], a[132:135], v[48:51]\n"
"global_load_dwordx4 a[56:59], v0, s[34:35] offset:2048 sc1\n"
"v_cvt_pk_bf16_f32 v25, v14, v15\n"
"v_mfma_f32_16x16x32_bf16 v[76:79], a[76:79], a[132:135], v[76:79]\n"
"v_lshlrev_b32 v12, 16, v18\n"
"v_and_b32 v13, s63, v18\n"
"v_mfma_f32_16x16x32_bf16 v[76:79], a[72:75], a[128:131], v[76:79]\n"
"v_lshlrev_b32 v14, 16, v19\n"
"v_mfma_f32_16x16x32_bf16 v[80:83], a[72:75], a[136:139], v[80:83]\n"
"v_and_b32 v15, s63, v19\n"
"v_mul_f32 v12, v12, v60\n"
"v_mfma_f32_16x16x32_bf16 v[80:83], a[76:79], a[140:143], v[80:83]\n"
"v_mul_f32 v13, v13, v61\n"
"v_mfma_f32_16x16x32_bf16 v[52:55], a[68:71], a[140:143], v[52:55]\n"
"v_mul_f32 v14, v14, v62\n"
"v_mul_f32 v15, v15, v63\n"
"v_mfma_f32_16x16x32_bf16 v[52:55], a[64:67], a[136:139], v[52:55]\n"
"s_waitcnt lgkmcnt(1)\n"
"v_cvt_pk_bf16_f32 v26, v12, v13\n"
"v_cvt_pk_bf16_f32 v27, v14, v15\n"
"v_mfma_f32_16x16x32_bf16 v[56:59], a[64:67], a[144:147], v[56:59]\n"
"ds_write_b128 v7, v[24:27] offset:0\n"
"v_mfma_f32_16x16x32_bf16 v[56:59], a[68:71], a[148:151], v[56:59]\n"
"global_load_dwordx4 a[60:63], v0, s[34:35] offset:3072 sc1\n"
"v_mfma_f32_16x16x32_bf16 v[84:87], a[76:79], a[148:151], v[84:87]\n"
"s_waitcnt lgkmcnt(1)\n"
"v_lshlrev_b32 v12, 16, v20\n"
"v_and_b32 v13, s63, v20\n"
"v_mfma_f32_16x16x32_bf16 v[84:87], a[72:75], a[144:147], v[84:87]\n"
"v_lshlrev_b32 v14, 16, v21\n"
"v_mfma_f32_16x16x32_bf16 a[4:7], a[72:75], a[152:155], a[4:7]\n"
"s_add_u32 s34, s34, 8192\n"
"v_and_b32 v15, s63, v21\n"
"v_xor_b32 v16, 64, v7\n"
"v_mfma_f32_16x16x32_bf16 a[4:7], a[76:79], a[156:159], a[4:7]\n"
"ds_read_b128 v[16:19], v16 offset:32768\n"
"v_mul_f32 v12, v12, v36\n"
"v_mfma_f32_16x16x32_bf16 a[0:3], a[68:71], a[156:159], a[0:3]\n"
"v_mul_f32 v13, v13, v37\n"
"v_mul_f32 v14, v14, v38\n"
"v_mfma_f32_16x16x32_bf16 a[0:3], a[64:67], a[152:155], a[0:3]\n"
"s_waitcnt vmcnt(8)\n"
"v_mul_f32 v15, v15, v39\n"
"v_cvt_pk_bf16_f32 v28, v12, v13\n"
"v_mfma_f32_16x16x32_bf16 v[88:91], a[80:83], a[96:99], v[88:91]\n"
"v_cvt_pk_bf16_f32 v29, v14, v15\n"
"v_lshlrev_b32 v12, 16, v22\n"
"v_mfma_f32_16x16x32_bf16 v[88:91], a[84:87], a[100:103], v[88:91]\n"
"global_load_dwordx4 a[64:67], v0, s[34:35] offset:-4096 sc1\n"
"v_and_b32 v13, s63, v22\n"
"v_mfma_f32_16x16x32_bf16 v[116:119], a[92:95], a[100:103], v[116:119]\n"
"ds_read_b128 a[100:103], v2 offset:17408\n"
"v_lshlrev_b32 v14, 16, v23\n"
"v_mfma_f32_16x16x32_bf16 v[116:119], a[88:91], a[96:99], v[116:119]\n"
"ds_read_b128 a[96:99], v2 offset:16384\n"
"v_and_b32 v15, s63, v23\n"
"v_mfma_f32_16x16x32_bf16 v[120:123], a[88:91], a[104:107], v[120:123]\n"
"v_mul_f32 v12, v12, v64\n"
"v_mul_f32 v13, v13, v65\n"
"v_mfma_f32_16x16x32_bf16 v[120:123], a[92:95], a[108:111], v[120:123]\n"
"v_mul_f32 v14, v14, v66\n"
"v_mul_f32 v15, v15, v67\n"
"v_mfma_f32_16x16x32_bf16 v[92:95], a[84:87], a[108:111], v[92:95]\n"
"ds_read_b128 a[108:111], v2 offset:19456\n"
"v_cvt_pk_bf16_f32 v30, v12, v13\n"
"v_mfma_f32_16x16x32_bf16 v[92:95], a[80:83], a[104:107], v[92:95]\n"
"ds_read_b128 a[104:107], v2 offset:18432\n"
"v_cvt_pk_bf16_f32 v31, v14, v15\n"
"v_mfma_f32_16x16x32_bf16 v[96:99], a[80:83], a[112:115], v[96:99]\n"
"ds_write_b128 v7, v[28:31] offset:2048\n"
"v_mfma_f32_16x16x32_bf16 v[96:99], a[84:87], a[116:119], v[96:99]\n"
"s_waitcnt lgkmcnt(5)\n"
"global_load_dwordx4 a[68:71], v0, s[34:35] offset:-3072 sc1\n"
"v_lshlrev_b32 v12, 16, v16\n"
"v_mfma_f32_16x16x32_bf16 v[124:127], a[92:95], a[116:119], v[124:127]\n"
"ds_read_b128 a[116:119], v2 offset:21504\n"
"v_and_b32 v13, s63, v16\n"
"v_mfma_f32_16x16x32_bf16 v[124:127], a[88:91], a[112:115], v[124:127]\n"
"ds_read_b128 a[112:115], v2 offset:20480\n"
"v_lshlrev_b32 v14, 16, v17\n"
"v_mfma_f32_16x16x32_bf16 v[128:131], a[88:91], a[120:123], v[128:131]\n"
"v_and_b32 v15, s63, v17\n"
"v_xor_b32 v20, 64, v7\n"
"v_mfma_f32_16x16x32_bf16 v[128:131], a[92:95], a[124:127], v[128:131]\n"
"ds_read_b128 v[20:23], v20 offset:34816\n"
"v_mfma_f32_16x16x32_bf16 v[100:103], a[84:87], a[124:127], v[100:103]\n"
"ds_read_b128 a[124:127], v2 offset:23552\n"
"v_mfma_f32_16x16x32_bf16 v[100:103], a[80:83], a[120:123], v[100:103]\n"
"ds_read_b128 a[120:123], v2 offset:22528\n"
"v_mul_f32 v12, v12, v88\n"
"v_mfma_f32_16x16x32_bf16 v[104:107], a[80:83], a[128:131], v[104:107]\n"
"v_mul_f32 v13, v13, v89\n"
"v_mul_f32 v14, v14, v90\n"
"v_mfma_f32_16x16x32_bf16 v[104:107], a[84:87], a[132:135], v[104:107]\n"
"global_load_dwordx4 a[72:75], v0, s[34:35] offset:-2048 sc1\n"
"v_mul_f32 v15, v15, v91\n"
"v_mfma_f32_16x16x32_bf16 v[132:135], a[92:95], a[132:135], v[132:135]\n"
"ds_read_b128 a[132:135], v2 offset:25600\n"
"v_cvt_pk_bf16_f32 v24, v12, v13\n"
"v_mfma_f32_16x16x32_bf16 v[132:135], a[88:91], a[128:131], v[132:135]\n"
"ds_read_b128 a[128:131], v2 offset:24576\n"
"v_cvt_pk_bf16_f32 v25, v14, v15\n"
"v_mfma_f32_16x16x32_bf16 v[136:139], a[88:91], a[136:139], v[136:139]\n"
"v_lshlrev_b32 v12, 16, v18\n"
"v_and_b32 v13, s63, v18\n"
"v_mfma_f32_16x16x32_bf16 v[136:139], a[92:95], a[140:143], v[136:139]\n"
"v_lshlrev_b32 v14, 16, v19\n"
"v_and_b32 v15, s63, v19\n"
"v_mfma_f32_16x16x32_bf16 v[108:111], a[84:87], a[140:143], v[108:111]\n"
"ds_read_b128 a[140:143], v2 offset:27648\n"
"v_mul_f32 v12, v12, v116\n"
"v_mfma_f32_16x16x32_bf16 v[108:111], a[80:83], a[136:139], v[108:111]\n"
"ds_read_b128 a[136:139], v2 offset:26624\n"
"v_mul_f32 v13, v13, v117\n"
"v_mfma_f32_16x16x32_bf16 v[112:115], a[80:83], a[144:147], v[112:115]\n"
"v_mul_f32 v14, v14, v118\n"
"v_mul_f32 v15, v15, v119\n"
"v_mfma_f32_16x16x32_bf16 v[112:115], a[84:87], a[148:151], v[112:115]\n"
"global_load_dwordx4 a[76:79], v0, s[34:35] offset:-1024 sc1\n"
"v_cvt_pk_bf16_f32 v26, v12, v13\n"
"v_mfma_f32_16x16x32_bf16 v[140:143], a[92:95], a[148:151], v[140:143]\n"
"ds_read_b128 a[148:151], v2 offset:29696\n"
"v_cvt_pk_bf16_f32 v27, v14, v15\n"
"v_mfma_f32_16x16x32_bf16 v[140:143], a[88:91], a[144:147], v[140:143]\n"
"ds_read_b128 a[144:147], v2 offset:28672\n"
"v_mfma_f32_16x16x32_bf16 a[12:15], a[88:91], a[152:155], a[12:15]\n"
"ds_write_b128 v8, v[24:27] offset:0\n"
"v_mfma_f32_16x16x32_bf16 a[12:15], a[92:95], a[156:159], a[12:15]\n"
"ds_read_b128 v[32:35], v2 offset:0\n"
"v_mfma_f32_16x16x32_bf16 a[8:11], a[84:87], a[156:159], a[8:11]\n"
"ds_read_b128 a[156:159], v2 offset:31744\n"
"v_mfma_f32_16x16x32_bf16 a[8:11], a[80:83], a[152:155], a[8:11]\n"
"s_waitcnt vmcnt(8) lgkmcnt(15)\n"
"ds_read_b128 a[152:155], v2 offset:30720\n"
"v_mfma_f32_16x16x32_bf16 v[144:147], a[32:35], a[96:99], v[144:147]\n"
"s_waitcnt lgkmcnt(12)\n"
"ds_read_b128 v[60:63], v2 offset:1024\n"
"v_lshlrev_b32 v12, 16, v20\n"
"v_mfma_f32_16x16x32_bf16 v[144:147], a[36:39], a[100:103], v[144:147]\n"
"global_load_dwordx4 a[80:83], v0, s[34:35] offset:0 sc1\n"
"v_and_b32 v13, s63, v20\n"
"v_mfma_f32_16x16x32_bf16 v[172:175], a[44:47], a[100:103], v[172:175]\n"
"v_lshlrev_b32 v14, 16, v21\n"
"v_and_b32 v15, s63, v21\n"
"v_mfma_f32_16x16x32_bf16 v[172:175], a[40:43], a[96:99], v[172:175]\n"
"ds_read_b128 v[16:19], v7 offset:36864\n"
"v_mul_f32 v12, v12, v92\n"
"v_mfma_f32_16x16x32_bf16 v[176:179], a[40:43], a[104:107], v[176:179]\n"
"v_mul_f32 v13, v13, v93\n"
"v_mul_f32 v14, v14, v94\n"
"v_mfma_f32_16x16x32_bf16 v[176:179], a[44:47], a[108:111], v[176:179]\n"
"v_mul_f32 v15, v15, v95\n"
"v_cvt_pk_bf16_f32 v28, v12, v13\n"
"v_mfma_f32_16x16x32_bf16 v[148:151], a[36:39], a[108:111], v[148:151]\n"
"v_cvt_pk_bf16_f32 v29, v14, v15\n"
"v_lshlrev_b32 v12, 16, v22\n"
"v_mfma_f32_16x16x32_bf16 v[148:151], a[32:35], a[104:107], v[148:151]\n"
"s_waitcnt lgkmcnt(12)\n"
"v_and_b32 v13, s63, v22\n"
"v_lshlrev_b32 v14, 16, v23\n"
"v_mfma_f32_16x16x32_bf16 v[152:155], a[32:35], a[112:115], v[152:155]\n"
"v_and_b32 v15, s63, v23\n"
"v_mul_f32 v12, v12, v120\n"
"v_mfma_f32_16x16x32_bf16 v[152:155], a[36:39], a[116:119], v[152:155]\n"
"global_load_dwordx4 a[84:87], v0, s[34:35] offset:1024 sc1\n"
"v_mul_f32 v13, v13, v121\n"
"v_mfma_f32_16x16x32_bf16 v[180:183], a[44:47], a[116:119], v[180:183]\n"
"v_mul_f32 v14, v14, v122\n"
"v_mul_f32 v15, v15, v123\n"
"v_mfma_f32_16x16x32_bf16 v[180:183], a[40:43], a[112:115], v[180:183]\n"
"v_cvt_pk_bf16_f32 v30, v12, v13\n"
"v_mfma_f32_16x16x32_bf16 v[184:187], a[40:43], a[120:123], v[184:187]\n"
"v_cvt_pk_bf16_f32 v31, v14, v15\n"
"v_mfma_f32_16x16x32_bf16 v[184:187], a[44:47], a[124:127], v[184:187]\n"
"ds_write_b128 v8, v[28:31] offset:2048\n"
"v_mfma_f32_16x16x32_bf16 v[156:159], a[36:39], a[124:127], v[156:159]\n"
"s_waitcnt lgkmcnt(5)\n"
"buffer_store_dwordx4 v[32:35], v6, s[52:55], s80 offen offset:0 sc1\n"
"ds_read_b128 v[36:39], v2 offset:2048\n"
"ds_read_b128 v[64:67], v2 offset:3072\n"
"v_mfma_f32_16x16x32_bf16 v[156:159], a[32:35], a[120:123], v[156:159]\n"
"s_waitcnt lgkmcnt(3)\n"
"buffer_store_dwordx4 v[60:63], v6, s[52:55], s81 offen offset:1024 sc1\n"
"v_lshlrev_b32 v12, 16, v16\n"
"v_and_b32 v13, s63, v16\n"
"v_lshlrev_b32 v14, 16, v17\n"
"v_mfma_f32_16x16x32_bf16 v[160:163], a[32:35], a[128:131], v[160:163]\n"
"v_and_b32 v15, s63, v17\n"
"v_mfma_f32_16x16x32_bf16 v[160:163], a[36:39], a[132:135], v[160:163]\n"
"global_load_dwordx4 a[88:91], v0, s[34:35] offset:2048 sc1\n"
"v_mfma_f32_16x16x32_bf16 v[188:191], a[44:47], a[132:135], v[188:191]\n"
"v_xor_b32 v20, 64, v7\n"
"v_mfma_f32_16x16x32_bf16 v[188:191], a[40:43], a[128:131], v[188:191]\n"
"ds_read_b128 v[20:23], v20 offset:36864\n"
"v_mfma_f32_16x16x32_bf16 v[192:195], a[40:43], a[136:139], v[192:195]\n"
"v_mul_f32 v12, v12, v40\n"
"v_mfma_f32_16x16x32_bf16 v[192:195], a[44:47], a[140:143], v[192:195]\n"
"v_mul_f32 v13, v13, v41\n"
"v_mul_f32 v14, v14, v42\n"
"v_mfma_f32_16x16x32_bf16 v[164:167], a[36:39], a[140:143], v[164:167]\n"
"v_mul_f32 v15, v15, v43\n"
"v_mfma_f32_16x16x32_bf16 v[164:167], a[32:35], a[136:139], v[164:167]\n"
"v_cvt_pk_bf16_f32 v24, v12, v13\n"
"v_mfma_f32_16x16x32_bf16 v[168:171], a[32:35], a[144:147], v[168:171]\n"
"v_cvt_pk_bf16_f32 v25, v14, v15\n"
"v_lshlrev_b32 v12, 16, v18\n"
"v_mfma_f32_16x16x32_bf16 v[168:171], a[36:39], a[148:151], v[168:171]\n"
"global_load_dwordx4 a[92:95], v0, s[34:35] offset:3072 sc1\n"
"v_and_b32 v13, s63, v18\n"
"v_mfma_f32_16x16x32_bf16 v[196:199], a[44:47], a[148:151], v[196:199]\n"
"v_lshlrev_b32 v14, 16, v19\n"
"v_and_b32 v15, s63, v19\n"
"v_mfma_f32_16x16x32_bf16 v[196:199], a[40:43], a[144:147], v[196:199]\n"
"v_mul_f32 v12, v12, v68\n"
"v_mul_f32 v13, v13, v69\n"
"v_mfma_f32_16x16x32_bf16 a[20:23], a[40:43], a[152:155], a[20:23]\n"
"s_add_u32 s34, s34, 8192\n"
"v_mul_f32 v14, v14, v70\n"
"v_mul_f32 v15, v15, v71\n"
"v_mfma_f32_16x16x32_bf16 a[20:23], a[44:47], a[156:159], a[20:23]\n"
"v_cvt_pk_bf16_f32 v26, v12, v13\n"
"v_cvt_pk_bf16_f32 v27, v14, v15\n"
"v_mfma_f32_16x16x32_bf16 a[16:19], a[36:39], a[156:159], a[16:19]\n"
"ds_write_b128 v7, v[24:27] offset:4096\n"
"v_mfma_f32_16x16x32_bf16 a[16:19], a[32:35], a[152:155], a[16:19]\n"
"s_waitcnt lgkmcnt(3)\n"
"buffer_store_dwordx4 v[36:39], v6, s[52:55], s82 offen offset:2048 sc1\n"
"s_waitcnt vmcnt(11)\n"
"v_mfma_f32_16x16x32_bf16 v[200:203], a[48:51], a[96:99], v[200:203]\n"
"s_waitcnt lgkmcnt(1)\n"
"buffer_store_dwordx4 v[64:67], v6, s[52:55], s83 offen offset:3072 sc1\n"
"v_lshlrev_b32 v12, 16, v20\n"
"v_and_b32 v13, s63, v20\n"
"v_lshlrev_b32 v14, 16, v21\n"
"v_mfma_f32_16x16x32_bf16 v[200:203], a[52:55], a[100:103], v[200:203]\n"
"global_load_dwordx4 a[32:35], v0, s[34:35] offset:-4096 sc1\n"
"v_mfma_f32_16x16x32_bf16 v[228:231], a[60:63], a[100:103], v[228:231]\n"
"ds_read_b128 a[100:103], v3 offset:17408\n"
"v_mfma_f32_16x16x32_bf16 v[228:231], a[56:59], a[96:99], v[228:231]\n"
"ds_read_b128 a[96:99], v3 offset:16384\n"
"v_and_b32 v15, s63, v21\n"
"v_mfma_f32_16x16x32_bf16 v[232:235], a[56:59], a[104:107], v[232:235]\n"
"ds_read_b128 v[16:19], v7 offset:38912\n"
"v_mul_f32 v12, v12, v96\n"
"v_mfma_f32_16x16x32_bf16 v[232:235], a[60:63], a[108:111], v[232:235]\n"
"v_mul_f32 v13, v13, v97\n"
"v_mul_f32 v14, v14, v98\n"
"v_mfma_f32_16x16x32_bf16 v[204:207], a[52:55], a[108:111], v[204:207]\n"
"ds_read_b128 a[108:111], v3 offset:19456\n"
"v_mul_f32 v15, v15, v99\n"
"v_mfma_f32_16x16x32_bf16 v[204:207], a[48:51], a[104:107], v[204:207]\n"
"ds_read_b128 a[104:107], v3 offset:18432\n"
"v_cvt_pk_bf16_f32 v28, v12, v13\n"
"v_mfma_f32_16x16x32_bf16 v[208:211], a[48:51], a[112:115], v[208:211]\n"
"v_cvt_pk_bf16_f32 v29, v14, v15\n"
"v_lshlrev_b32 v12, 16, v22\n"
"v_mfma_f32_16x16x32_bf16 v[208:211], a[52:55], a[116:119], v[208:211]\n"
"global_load_dwordx4 a[36:39], v0, s[34:35] offset:-3072 sc1\n"
"v_and_b32 v13, s63, v22\n"
"v_mfma_f32_16x16x32_bf16 v[236:239], a[60:63], a[116:119], v[236:239]\n"
"ds_read_b128 a[116:119], v3 offset:21504\n"
"v_lshlrev_b32 v14, 16, v23\n"
"v_mfma_f32_16x16x32_bf16 v[236:239], a[56:59], a[112:115], v[236:239]\n"
"ds_read_b128 a[112:115], v3 offset:20480\n"
"v_and_b32 v15, s63, v23\n"
"v_mfma_f32_16x16x32_bf16 v[240:243], a[56:59], a[120:123], v[240:243]\n"
"v_mul_f32 v12, v12, v124\n"
"v_mul_f32 v13, v13, v125\n"
"v_mfma_f32_16x16x32_bf16 v[240:243], a[60:63], a[124:127], v[240:243]\n"
"v_mul_f32 v14, v14, v126\n"
"v_mul_f32 v15, v15, v127\n"
"v_mfma_f32_16x16x32_bf16 v[212:215], a[52:55], a[124:127], v[212:215]\n"
"ds_read_b128 a[124:127], v3 offset:23552\n"
"v_cvt_pk_bf16_f32 v30, v12, v13\n"
"v_mfma_f32_16x16x32_bf16 v[212:215], a[48:51], a[120:123], v[212:215]\n"
"ds_read_b128 a[120:123], v3 offset:22528\n"
"v_cvt_pk_bf16_f32 v31, v14, v15\n"
"v_mfma_f32_16x16x32_bf16 v[216:219], a[48:51], a[128:131], v[216:219]\n"
"ds_write_b128 v8, v[28:31] offset:4096\n"
"v_mfma_f32_16x16x32_bf16 v[216:219], a[52:55], a[132:135], v[216:219]\n"
"global_load_dwordx4 a[40:43], v0, s[34:35] offset:-2048 sc1\n"
"ds_read_b128 v[40:43], v2 offset:4096\n"
"v_mfma_f32_16x16x32_bf16 v[244:247], a[60:63], a[132:135], v[244:247]\n"
"ds_read_b128 a[132:135], v3 offset:25600\n"
"v_mfma_f32_16x16x32_bf16 v[244:247], a[56:59], a[128:131], v[244:247]\n"
"ds_read_b128 a[128:131], v3 offset:24576\n"
"v_mfma_f32_16x16x32_bf16 v[248:251], a[56:59], a[136:139], v[248:251]\n"
"s_waitcnt lgkmcnt(10)\n"
"ds_read_b128 v[68:71], v2 offset:5120\n"
"v_lshlrev_b32 v12, 16, v16\n"
"v_mfma_f32_16x16x32_bf16 v[248:251], a[60:63], a[140:143], v[248:251]\n"
"v_and_b32 v13, s63, v16\n"
"v_lshlrev_b32 v14, 16, v17\n"
"v_mfma_f32_16x16x32_bf16 v[220:223], a[52:55], a[140:143], v[220:223]\n"
"ds_read_b128 a[140:143], v3 offset:27648\n"
"v_and_b32 v15, s63, v17\n"
"v_mfma_f32_16x16x32_bf16 v[220:223], a[48:51], a[136:139], v[220:223]\n"
"ds_read_b128 a[136:139], v3 offset:26624\n"
"v_xor_b32 v20, 64, v7\n"
"v_mfma_f32_16x16x32_bf16 v[224:227], a[48:51], a[144:147], v[224:227]\n"
"ds_read_b128 v[20:23], v20 offset:38912\n"
"v_mul_f32 v12, v12, v44\n"
"v_mfma_f32_16x16x32_bf16 v[224:227], a[52:55], a[148:151], v[224:227]\n"
"global_load_dwordx4 a[44:47], v0, s[34:35] offset:-1024 sc1\n"
"v_mul_f32 v13, v13, v45\n"
"v_mfma_f32_16x16x32_bf16 v[252:255], a[60:63], a[148:151], v[252:255]\n"
"ds_read_b128 a[148:151], v3 offset:29696\n"
"v_mul_f32 v14, v14, v46\n"
"v_mfma_f32_16x16x32_bf16 v[252:255], a[56:59], a[144:147], v[252:255]\n"
"ds_read_b128 a[144:147], v3 offset:28672\n"
"v_mul_f32 v15, v15, v47\n"
"v_mfma_f32_16x16x32_bf16 a[28:31], a[56:59], a[152:155], a[28:31]\n"
"v_cvt_pk_bf16_f32 v24, v12, v13\n"
"v_cvt_pk_bf16_f32 v25, v14, v15\n"
"v_mfma_f32_16x16x32_bf16 a[28:31], a[60:63], a[156:159], a[28:31]\n"
"v_lshlrev_b32 v12, 16, v18\n"
"v_and_b32 v13, s63, v18\n"
"v_mfma_f32_16x16x32_bf16 a[24:27], a[52:55], a[156:159], a[24:27]\n"
"ds_read_b128 a[156:159], v3 offset:31744\n"
"v_lshlrev_b32 v14, 16, v19\n"
"v_mfma_f32_16x16x32_bf16 a[24:27], a[48:51], a[152:155], a[24:27]\n"
"s_waitcnt vmcnt(12) lgkmcnt(15)\n"
"ds_read_b128 a[152:155], v3 offset:30720\n"
"v_and_b32 v15, s63, v19\n"
"v_mfma_f32_16x16x32_bf16 v[144:147], a[64:67], a[96:99], v[144:147]\n"
"v_mul_f32 v12, v12, v72\n"
"v_mul_f32 v13, v13, v73\n"
"v_mfma_f32_16x16x32_bf16 v[144:147], a[68:71], a[100:103], v[144:147]\n"
"global_load_dwordx4 a[48:51], v0, s[34:35] offset:0 sc1\n"
"v_mul_f32 v14, v14, v74\n"
"v_mfma_f32_16x16x32_bf16 v[172:175], a[76:79], a[100:103], v[172:175]\n"
"v_mul_f32 v15, v15, v75\n"
"v_cvt_pk_bf16_f32 v26, v12, v13\n"
"v_mfma_f32_16x16x32_bf16 v[172:175], a[72:75], a[96:99], v[172:175]\n"
"v_cvt_pk_bf16_f32 v27, v14, v15\n"
"v_mfma_f32_16x16x32_bf16 v[176:179], a[72:75], a[104:107], v[176:179]\n"
"ds_write_b128 v7, v[24:27] offset:6144\n"
"v_mfma_f32_16x16x32_bf16 v[176:179], a[76:79], a[108:111], v[176:179]\n"
"s_waitcnt lgkmcnt(11)\n"
"buffer_store_dwordx4 v[40:43], v6, s[52:55], s84 offen offset:0 sc1\n"
"v_mfma_f32_16x16x32_bf16 v[148:151], a[68:71], a[108:111], v[148:151]\n"
"s_waitcnt lgkmcnt(5)\n"
"buffer_store_dwordx4 v[68:71], v6, s[52:55], s85 offen offset:1024 sc1\n"
"v_lshlrev_b32 v12, 16, v20\n"
"v_and_b32 v13, s63, v20\n"
"v_lshlrev_b32 v14, 16, v21\n"
"v_mfma_f32_16x16x32_bf16 v[148:151], a[64:67], a[104:107], v[148:151]\n"
"v_and_b32 v15, s63, v21\n"
"v_mfma_f32_16x16x32_bf16 v[152:155], a[64:67], a[112:115], v[152:155]\n"
"v_mul_f32 v12, v12, v100\n"
"v_mfma_f32_16x16x32_bf16 v[152:155], a[68:71], a[116:119], v[152:155]\n"
"global_load_dwordx4 a[52:55], v0, s[34:35] offset:1024 sc1\n"
"v_mul_f32 v13, v13, v101\n"
"v_mfma_f32_16x16x32_bf16 v[180:183], a[76:79], a[116:119], v[180:183]\n"
"v_mul_f32 v14, v14, v102\n"
"v_mfma_f32_16x16x32_bf16 v[180:183], a[72:75], a[112:115], v[180:183]\n"
"v_mul_f32 v15, v15, v103\n"
"v_cvt_pk_bf16_f32 v28, v12, v13\n"
"v_mfma_f32_16x16x32_bf16 v[184:187], a[72:75], a[120:123], v[184:187]\n"
"v_cvt_pk_bf16_f32 v29, v14, v15\n"
"v_mfma_f32_16x16x32_bf16 v[184:187], a[76:79], a[124:127], v[184:187]\n"
"v_lshlrev_b32 v12, 16, v22\n"
"v_mfma_f32_16x16x32_bf16 v[156:159], a[68:71], a[124:127], v[156:159]\n"
"v_and_b32 v13, s63, v22\n"
"v_lshlrev_b32 v14, 16, v23\n"
"v_mfma_f32_16x16x32_bf16 v[156:159], a[64:67], a[120:123], v[156:159]\n"
"v_and_b32 v15, s63, v23\n"
"v_mfma_f32_16x16x32_bf16 v[160:163], a[64:67], a[128:131], v[160:163]\n"
"v_mul_f32 v12, v12, v128\n"
"v_mul_f32 v13, v13, v129\n"
"v_mfma_f32_16x16x32_bf16 v[160:163], a[68:71], a[132:135], v[160:163]\n"
"global_load_dwordx4 a[56:59], v0, s[34:35] offset:2048 sc1\n"
"v_mul_f32 v14, v14, v130\n"
"v_mfma_f32_16x16x32_bf16 v[188:191], a[76:79], a[132:135], v[188:191]\n"
"v_mul_f32 v15, v15, v131\n"
"v_mfma_f32_16x16x32_bf16 v[188:191], a[72:75], a[128:131], v[188:191]\n"
"v_cvt_pk_bf16_f32 v30, v12, v13\n"
"v_cvt_pk_bf16_f32 v31, v14, v15\n"
"v_mfma_f32_16x16x32_bf16 v[192:195], a[72:75], a[136:139], v[192:195]\n"
"ds_write_b128 v8, v[28:31] offset:6144\n"
"v_mfma_f32_16x16x32_bf16 v[192:195], a[76:79], a[140:143], v[192:195]\n"
"ds_read_b128 v[44:47], v2 offset:6144\n"
"v_mfma_f32_16x16x32_bf16 v[164:167], a[68:71], a[140:143], v[164:167]\n"
"ds_read_b128 v[72:75], v2 offset:7168\n"
"v_accvgpr_read_b32 v16, a160\n"
"v_mfma_f32_16x16x32_bf16 v[164:167], a[64:67], a[136:139], v[164:167]\n"
"s_waitcnt lgkmcnt(4)\n"
"v_accvgpr_read_b32 v17, a161\n"
"v_mfma_f32_16x16x32_bf16 v[168:171], a[64:67], a[144:147], v[168:171]\n"
"v_accvgpr_read_b32 v18, a162\n"
"v_mfma_f32_16x16x32_bf16 v[168:171], a[68:71], a[148:151], v[168:171]\n"
"global_load_dwordx4 a[60:63], v0, s[34:35] offset:3072 sc1\n"
"v_accvgpr_read_b32 v19, a163\n"
"v_mfma_f32_16x16x32_bf16 v[196:199], a[76:79], a[148:151], v[196:199]\n"
"v_lshlrev_b32 v12, 16, v16\n"
"v_and_b32 v13, s63, v16\n"
"v_mfma_f32_16x16x32_bf16 v[196:199], a[72:75], a[144:147], v[196:199]\n"
"v_lshlrev_b32 v14, 16, v17\n"
"v_mfma_f32_16x16x32_bf16 a[20:23], a[72:75], a[152:155], a[20:23]\n"
"s_add_u32 s34, s34, 8192\n"
"v_and_b32 v15, s63, v17\n"
"v_mul_f32 v12, v12, v48\n"
"v_mfma_f32_16x16x32_bf16 a[20:23], a[76:79], a[156:159], a[20:23]\n"
"v_mul_f32 v13, v13, v49\n"
"v_mfma_f32_16x16x32_bf16 a[16:19], a[68:71], a[156:159], a[16:19]\n"
"v_mul_f32 v14, v14, v50\n"
"v_mul_f32 v15, v15, v51\n"
"v_mfma_f32_16x16x32_bf16 a[16:19], a[64:67], a[152:155], a[16:19]\n"
"s_waitcnt vmcnt(12)\n"
"v_cvt_pk_bf16_f32 v24, v12, v13\n"
"v_mfma_f32_16x16x32_bf16 v[200:203], a[80:83], a[96:99], v[200:203]\n"
"v_cvt_pk_bf16_f32 v25, v14, v15\n"
"v_lshlrev_b32 v12, 16, v18\n"
"v_mfma_f32_16x16x32_bf16 v[200:203], a[84:87], a[100:103], v[200:203]\n"
"global_load_dwordx4 a[64:67], v0, s[34:35] offset:-4096 sc1\n"
"v_and_b32 v13, s63, v18\n"
"v_mfma_f32_16x16x32_bf16 v[228:231], a[92:95], a[100:103], v[228:231]\n"
"ds_read_b128 a[100:103], v4 offset:17408\n"
"v_lshlrev_b32 v14, 16, v19\n"
"v_mfma_f32_16x16x32_bf16 v[228:231], a[88:91], a[96:99], v[228:231]\n"
"ds_read_b128 a[96:99], v4 offset:16384\n"
"v_and_b32 v15, s63, v19\n"
"v_mfma_f32_16x16x32_bf16 v[232:235], a[88:91], a[104:107], v[232:235]\n"
"v_mul_f32 v12, v12, v76\n"
"v_mul_f32 v13, v13, v77\n"
"v_mfma_f32_16x16x32_bf16 v[232:235], a[92:95], a[108:111], v[232:235]\n"
"v_mul_f32 v14, v14, v78\n"
"v_mul_f32 v15, v15, v79\n"
"v_mfma_f32_16x16x32_bf16 v[204:207], a[84:87], a[108:111], v[204:207]\n"
"ds_read_b128 a[108:111], v4 offset:19456\n"
"v_cvt_pk_bf16_f32 v26, v12, v13\n"
"v_mfma_f32_16x16x32_bf16 v[204:207], a[80:83], a[104:107], v[204:207]\n"
"ds_read_b128 a[104:107], v4 offset:18432\n"
"v_cvt_pk_bf16_f32 v27, v14, v15\n"
"v_mfma_f32_16x16x32_bf16 v[208:211], a[80:83], a[112:115], v[208:211]\n"
"ds_write_b128 v7, v[24:27] offset:8192\n"
"v_mfma_f32_16x16x32_bf16 v[208:211], a[84:87], a[116:119], v[208:211]\n"
"global_load_dwordx4 a[68:71], v0, s[34:35] offset:-3072 sc1\n"
"v_mfma_f32_16x16x32_bf16 v[236:239], a[92:95], a[116:119], v[236:239]\n"
"s_waitcnt lgkmcnt(6)\n"
"buffer_store_dwordx4 v[44:47], v6, s[52:55], s86 offen offset:2048 sc1\n"
"ds_read_b128 a[116:119], v4 offset:21504\n"
"v_mfma_f32_16x16x32_bf16 v[236:239], a[88:91], a[112:115], v[236:239]\n"
"s_waitcnt lgkmcnt(6)\n"
"buffer_store_dwordx4 v[72:75], v6, s[52:55], s87 offen offset:3072 sc1\n"
"ds_read_b128 a[112:115], v4 offset:20480\n"
"v_accvgpr_read_b32 v16, a164\n"
"v_accvgpr_read_b32 v17, a165\n"
"v_mfma_f32_16x16x32_bf16 v[240:243], a[88:91], a[120:123], v[240:243]\n"
"v_accvgpr_read_b32 v18, a166\n"
"v_accvgpr_read_b32 v19, a167\n"
"v_mfma_f32_16x16x32_bf16 v[240:243], a[92:95], a[124:127], v[240:243]\n"
"v_lshlrev_b32 v12, 16, v16\n"
"v_and_b32 v13, s63, v16\n"
"v_mfma_f32_16x16x32_bf16 v[212:215], a[84:87], a[124:127], v[212:215]\n"
"ds_read_b128 a[124:127], v4 offset:23552\n"
"v_lshlrev_b32 v14, 16, v17\n"
"v_mfma_f32_16x16x32_bf16 v[212:215], a[80:83], a[120:123], v[212:215]\n"
"ds_read_b128 a[120:123], v4 offset:22528\n"
"v_and_b32 v15, s63, v17\n"
"v_mfma_f32_16x16x32_bf16 v[216:219], a[80:83], a[128:131], v[216:219]\n"
"v_mul_f32 v12, v12, v104\n"
"v_mul_f32 v13, v13, v105\n"
"v_mfma_f32_16x16x32_bf16 v[216:219], a[84:87], a[132:135], v[216:219]\n"
"global_load_dwordx4 a[72:75], v0, s[34:35] offset:-2048 sc1\n"
"v_mul_f32 v14, v14, v106\n"
"v_mfma_f32_16x16x32_bf16 v[244:247], a[92:95], a[132:135], v[244:247]\n"
"ds_read_b128 a[132:135], v4 offset:25600\n"
"v_mul_f32 v15, v15, v107\n"
"v_mfma_f32_16x16x32_bf16 v[244:247], a[88:91], a[128:131], v[244:247]\n"
"ds_read_b128 a[128:131], v4 offset:24576\n"
"v_cvt_pk_bf16_f32 v28, v12, v13\n"
"v_mfma_f32_16x16x32_bf16 v[248:251], a[88:91], a[136:139], v[248:251]\n"
"v_cvt_pk_bf16_f32 v29, v14, v15\n"
"v_lshlrev_b32 v12, 16, v18\n"
"v_mfma_f32_16x16x32_bf16 v[248:251], a[92:95], a[140:143], v[248:251]\n"
"v_and_b32 v13, s63, v18\n"
"v_lshlrev_b32 v14, 16, v19\n"
"v_mfma_f32_16x16x32_bf16 v[220:223], a[84:87], a[140:143], v[220:223]\n"
"ds_read_b128 a[140:143], v4 offset:27648\n"
"v_and_b32 v15, s63, v19\n"
"v_mfma_f32_16x16x32_bf16 v[220:223], a[80:83], a[136:139], v[220:223]\n"
"ds_read_b128 a[136:139], v4 offset:26624\n"
"v_mul_f32 v12, v12, v132\n"
"v_mfma_f32_16x16x32_bf16 v[224:227], a[80:83], a[144:147], v[224:227]\n"
"v_mul_f32 v13, v13, v133\n"
"v_mul_f32 v14, v14, v134\n"
"v_mfma_f32_16x16x32_bf16 v[224:227], a[84:87], a[148:151], v[224:227]\n"
"global_load_dwordx4 a[76:79], v0, s[34:35] offset:-1024 sc1\n"
"v_mul_f32 v15, v15, v135\n"
"v_mfma_f32_16x16x32_bf16 v[252:255], a[92:95], a[148:151], v[252:255]\n"
"ds_read_b128 a[148:151], v4 offset:29696\n"
"v_cvt_pk_bf16_f32 v30, v12, v13\n"
"v_mfma_f32_16x16x32_bf16 v[252:255], a[88:91], a[144:147], v[252:255]\n"
"ds_read_b128 a[144:147], v4 offset:28672\n"
"v_cvt_pk_bf16_f32 v31, v14, v15\n"
"v_mfma_f32_16x16x32_bf16 a[28:31], a[88:91], a[152:155], a[28:31]\n"
"ds_write_b128 v8, v[28:31] offset:8192\n"
"v_mfma_f32_16x16x32_bf16 a[28:31], a[92:95], a[156:159], a[28:31]\n"
"ds_read_b128 v[48:51], v2 offset:8192\n"
"v_mfma_f32_16x16x32_bf16 a[24:27], a[84:87], a[156:159], a[24:27]\n"
"ds_read_b128 a[156:159], v4 offset:31744\n"
"v_mfma_f32_16x16x32_bf16 a[24:27], a[80:83], a[152:155], a[24:27]\n"
"s_waitcnt vmcnt(12) lgkmcnt(14)\n"
"ds_read_b128 a[152:155], v4 offset:30720\n"
"v_mfma_f32_16x16x32_bf16 v[144:147], a[32:35], a[96:99], v[144:147]\n"
"ds_read_b128 v[76:79], v2 offset:9216\n"
"v_accvgpr_read_b32 v16, a168\n"
"v_mfma_f32_16x16x32_bf16 v[144:147], a[36:39], a[100:103], v[144:147]\n"
"global_load_dwordx4 a[80:83], v0, s[34:35] offset:0 sc1\n"
"v_accvgpr_read_b32 v17, a169\n"
"v_mfma_f32_16x16x32_bf16 v[172:175], a[44:47], a[100:103], v[172:175]\n"
"v_accvgpr_read_b32 v18, a170\n"
"v_accvgpr_read_b32 v19, a171\n"
"v_mfma_f32_16x16x32_bf16 v[172:175], a[40:43], a[96:99], v[172:175]\n"
"v_lshlrev_b32 v12, 16, v16\n"
"v_and_b32 v13, s63, v16\n"
"v_mfma_f32_16x16x32_bf16 v[176:179], a[40:43], a[104:107], v[176:179]\n"
"v_lshlrev_b32 v14, 16, v17\n"
"v_and_b32 v15, s63, v17\n"
"v_mfma_f32_16x16x32_bf16 v[176:179], a[44:47], a[108:111], v[176:179]\n"
"v_mul_f32 v12, v12, v52\n"
"v_mul_f32 v13, v13, v53\n"
"v_mfma_f32_16x16x32_bf16 v[148:151], a[36:39], a[108:111], v[148:151]\n"
"v_mul_f32 v14, v14, v54\n"
"v_mul_f32 v15, v15, v55\n"
"v_mfma_f32_16x16x32_bf16 v[148:151], a[32:35], a[104:107], v[148:151]\n"
"s_waitcnt lgkmcnt(11)\n"
"v_cvt_pk_bf16_f32 v24, v12, v13\n"
"v_cvt_pk_bf16_f32 v25, v14, v15\n"
"v_mfma_f32_16x16x32_bf16 v[152:155], a[32:35], a[112:115], v[152:155]\n"
"v_lshlrev_b32 v12, 16, v18\n"
"v_mfma_f32_16x16x32_bf16 v[152:155], a[36:39], a[116:119], v[152:155]\n"
"global_load_dwordx4 a[84:87], v0, s[34:35] offset:1024 sc1\n"
"v_and_b32 v13, s63, v18\n"
"v_mfma_f32_16x16x32_bf16 v[180:183], a[44:47], a[116:119], v[180:183]\n"
"v_lshlrev_b32 v14, 16, v19\n"
"v_and_b32 v15, s63, v19\n"
"v_mfma_f32_16x16x32_bf16 v[180:183], a[40:43], a[112:115], v[180:183]\n"
"v_mul_f32 v12, v12, v80\n"
"v_mfma_f32_16x16x32_bf16 v[184:187], a[40:43], a[120:123], v[184:187]\n"
"v_mul_f32 v13, v13, v81\n"
"v_mul_f32 v14, v14, v82\n"
"v_mfma_f32_16x16x32_bf16 v[184:187], a[44:47], a[124:127], v[184:187]\n"
"v_mul_f32 v15, v15, v83\n"
"v_mfma_f32_16x16x32_bf16 v[156:159], a[36:39], a[124:127], v[156:159]\n"
"v_cvt_pk_bf16_f32 v26, v12, v13\n"
"v_cvt_pk_bf16_f32 v27, v14, v15\n"
"v_mfma_f32_16x16x32_bf16 v[156:159], a[32:35], a[120:123], v[156:159]\n"
"s_waitcnt lgkmcnt(7)\n"
"ds_write_b128 v7, v[24:27] offset:10240\n"
"v_mfma_f32_16x16x32_bf16 v[160:163], a[32:35], a[128:131], v[160:163]\n"
"s_waitcnt lgkmcnt(4)\n"
"buffer_store_dwordx4 v[48:51], v6, s[52:55], s88 offen offset:0 sc1\n"
"v_mfma_f32_16x16x32_bf16 v[160:163], a[36:39], a[132:135], v[160:163]\n"
"global_load_dwordx4 a[88:91], v0, s[34:35] offset:2048 sc1\n"
"v_mfma_f32_16x16x32_bf16 v[188:191], a[44:47], a[132:135], v[188:191]\n"
"s_waitcnt lgkmcnt(1)\n"
"buffer_store_dwordx4 v[76:79], v6, s[52:55], s89 offen offset:1024 sc1\n"
"v_accvgpr_read_b32 v16, a172\n"
"v_accvgpr_read_b32 v17, a173\n"
"v_accvgpr_read_b32 v18, a174\n"
"v_mfma_f32_16x16x32_bf16 v[188:191], a[40:43], a[128:131], v[188:191]\n"
"v_accvgpr_read_b32 v19, a175\n"
"v_mfma_f32_16x16x32_bf16 v[192:195], a[40:43], a[136:139], v[192:195]\n"
"v_lshlrev_b32 v12, 16, v16\n"
"v_mfma_f32_16x16x32_bf16 v[192:195], a[44:47], a[140:143], v[192:195]\n"
"v_and_b32 v13, s63, v16\n"
"v_mfma_f32_16x16x32_bf16 v[164:167], a[36:39], a[140:143], v[164:167]\n"
"v_lshlrev_b32 v14, 16, v17\n"
"v_mfma_f32_16x16x32_bf16 v[164:167], a[32:35], a[136:139], v[164:167]\n"
"v_and_b32 v15, s63, v17\n"
"v_mul_f32 v12, v12, v108\n"
"v_mfma_f32_16x16x32_bf16 v[168:171], a[32:35], a[144:147], v[168:171]\n"
"v_mul_f32 v13, v13, v109\n"
"v_mul_f32 v14, v14, v110\n"
"v_mfma_f32_16x16x32_bf16 v[168:171], a[36:39], a[148:151], v[168:171]\n"
"global_load_dwordx4 a[92:95], v0, s[34:35] offset:3072 sc1\n"
"v_mul_f32 v15, v15, v111\n"
"v_mfma_f32_16x16x32_bf16 v[196:199], a[44:47], a[148:151], v[196:199]\n"
"v_cvt_pk_bf16_f32 v28, v12, v13\n"
"v_cvt_pk_bf16_f32 v29, v14, v15\n"
"v_mfma_f32_16x16x32_bf16 v[196:199], a[40:43], a[144:147], v[196:199]\n"
"v_lshlrev_b32 v12, 16, v18\n"
"v_and_b32 v13, s63, v18\n"
"v_mfma_f32_16x16x32_bf16 a[20:23], a[40:43], a[152:155], a[20:23]\n"
"s_add_u32 s34, s34, 8192\n"
"v_lshlrev_b32 v14, 16, v19\n"
"v_and_b32 v15, s63, v19\n"
"v_mfma_f32_16x16x32_bf16 a[20:23], a[44:47], a[156:159], a[20:23]\n"
"v_mul_f32 v12, v12, v136\n"
"v_mul_f32 v13, v13, v137\n"
"v_mfma_f32_16x16x32_bf16 a[16:19], a[36:39], a[156:159], a[16:19]\n"
"v_mul_f32 v14, v14, v138\n"
"v_mul_f32 v15, v15, v139\n"
"v_mfma_f32_16x16x32_bf16 a[16:19], a[32:35], a[152:155], a[16:19]\n"
"s_waitcnt vmcnt(12)\n"
"v_cvt_pk_bf16_f32 v30, v12, v13\n"
"v_cvt_pk_bf16_f32 v31, v14, v15\n"
"v_mfma_f32_16x16x32_bf16 v[200:203], a[48:51], a[96:99], v[200:203]\n"
"ds_write_b128 v8, v[28:31] offset:10240\n"
"v_mfma_f32_16x16x32_bf16 v[200:203], a[52:55], a[100:103], v[200:203]\n"
"ds_read_b128 v[52:55], v2 offset:10240\n"
"v_mfma_f32_16x16x32_bf16 v[228:231], a[60:63], a[100:103], v[228:231]\n"
"ds_read_b128 a[100:103], v5 offset:17408\n"
"v_mfma_f32_16x16x32_bf16 v[228:231], a[56:59], a[96:99], v[228:231]\n"
"ds_read_b128 a[96:99], v5 offset:16384\n"
"v_mfma_f32_16x16x32_bf16 v[232:235], a[56:59], a[104:107], v[232:235]\n"
"ds_read_b128 v[80:83], v2 offset:11264\n"
"v_mfma_f32_16x16x32_bf16 v[232:235], a[60:63], a[108:111], v[232:235]\n"
"v_accvgpr_read_b32 v16, a176\n"
"v_mfma_f32_16x16x32_bf16 v[204:207], a[52:55], a[108:111], v[204:207]\n"
"ds_read_b128 a[108:111], v5 offset:19456\n"
"v_accvgpr_read_b32 v17, a177\n"
"v_mfma_f32_16x16x32_bf16 v[204:207], a[48:51], a[104:107], v[204:207]\n"
"ds_read_b128 a[104:107], v5 offset:18432\n"
"v_accvgpr_read_b32 v18, a178\n"
"v_mfma_f32_16x16x32_bf16 v[208:211], a[48:51], a[112:115], v[208:211]\n"
"v_accvgpr_read_b32 v19, a179\n"
"v_lshlrev_b32 v12, 16, v16\n"
"v_mfma_f32_16x16x32_bf16 v[208:211], a[52:55], a[116:119], v[208:211]\n"
"v_and_b32 v13, s63, v16\n"
"v_lshlrev_b32 v14, 16, v17\n"
"v_mfma_f32_16x16x32_bf16 v[236:239], a[60:63], a[116:119], v[236:239]\n"
"ds_read_b128 a[116:119], v5 offset:21504\n"
"v_and_b32 v15, s63, v17\n"
"v_mfma_f32_16x16x32_bf16 v[236:239], a[56:59], a[112:115], v[236:239]\n"
"ds_read_b128 a[112:115], v5 offset:20480\n"
"v_mul_f32 v12, v12, v56\n"
"v_mfma_f32_16x16x32_bf16 v[240:243], a[56:59], a[120:123], v[240:243]\n"
"v_mul_f32 v13, v13, v57\n"
"v_mfma_f32_16x16x32_bf16 v[240:243], a[60:63], a[124:127], v[240:243]\n"
"v_mul_f32 v14, v14, v58\n"
"v_mul_f32 v15, v15, v59\n"
"v_mfma_f32_16x16x32_bf16 v[212:215], a[52:55], a[124:127], v[212:215]\n"
"ds_read_b128 a[124:127], v5 offset:23552\n"
"v_cvt_pk_bf16_f32 v24, v12, v13\n"
"v_mfma_f32_16x16x32_bf16 v[212:215], a[48:51], a[120:123], v[212:215]\n"
"ds_read_b128 a[120:123], v5 offset:22528\n"
"v_cvt_pk_bf16_f32 v25, v14, v15\n"
"v_mfma_f32_16x16x32_bf16 v[216:219], a[48:51], a[128:131], v[216:219]\n"
"v_lshlrev_b32 v12, 16, v18\n"
"v_and_b32 v13, s63, v18\n"
"v_mfma_f32_16x16x32_bf16 v[216:219], a[52:55], a[132:135], v[216:219]\n"
"v_lshlrev_b32 v14, 16, v19\n"
"v_and_b32 v15, s63, v19\n"
"v_mfma_f32_16x16x32_bf16 v[244:247], a[60:63], a[132:135], v[244:247]\n"
"ds_read_b128 a[132:135], v5 offset:25600\n"
"v_mul_f32 v12, v12, v84\n"
"v_mfma_f32_16x16x32_bf16 v[244:247], a[56:59], a[128:131], v[244:247]\n"
"ds_read_b128 a[128:131], v5 offset:24576\n"
"v_mul_f32 v13, v13, v85\n"
"v_mfma_f32_16x16x32_bf16 v[248:251], a[56:59], a[136:139], v[248:251]\n"
"v_mul_f32 v14, v14, v86\n"
"v_mfma_f32_16x16x32_bf16 v[248:251], a[60:63], a[140:143], v[248:251]\n"
"v_mul_f32 v15, v15, v87\n"
"v_cvt_pk_bf16_f32 v26, v12, v13\n"
"v_mfma_f32_16x16x32_bf16 v[220:223], a[52:55], a[140:143], v[220:223]\n"
"ds_read_b128 a[140:143], v5 offset:27648\n"
"v_cvt_pk_bf16_f32 v27, v14, v15\n"
"v_mfma_f32_16x16x32_bf16 v[220:223], a[48:51], a[136:139], v[220:223]\n"
"ds_read_b128 a[136:139], v5 offset:26624\n"
"v_mfma_f32_16x16x32_bf16 v[224:227], a[48:51], a[144:147], v[224:227]\n"
"ds_write_b128 v7, v[24:27] offset:12288\n"
"v_mfma_f32_16x16x32_bf16 v[224:227], a[52:55], a[148:151], v[224:227]\n"
"s_waitcnt lgkmcnt(14)\n"
"buffer_store_dwordx4 v[52:55], v6, s[52:55], s90 offen offset:2048 sc1\n"
"v_mfma_f32_16x16x32_bf16 v[252:255], a[60:63], a[148:151], v[252:255]\n"
"s_waitcnt lgkmcnt(11)\n"
"buffer_store_dwordx4 v[80:83], v6, s[52:55], s91 offen offset:3072 sc1\n"
"ds_read_b128 a[148:151], v5 offset:29696\n"
"v_accvgpr_read_b32 v16, a180\n"
"v_accvgpr_read_b32 v17, a181\n"
"v_mfma_f32_16x16x32_bf16 v[252:255], a[56:59], a[144:147], v[252:255]\n"
"ds_read_b128 a[144:147], v5 offset:28672\n"
"v_accvgpr_read_b32 v18, a182\n"
"v_mfma_f32_16x16x32_bf16 a[28:31], a[56:59], a[152:155], a[28:31]\n"
"v_accvgpr_read_b32 v19, a183\n"
"v_lshlrev_b32 v12, 16, v16\n"
"v_mfma_f32_16x16x32_bf16 a[28:31], a[60:63], a[156:159], a[28:31]\n"
"v_and_b32 v13, s63, v16\n"
"v_lshlrev_b32 v14, 16, v17\n"
"v_mfma_f32_16x16x32_bf16 a[24:27], a[52:55], a[156:159], a[24:27]\n"
"ds_read_b128 a[156:159], v5 offset:31744\n"
"v_and_b32 v15, s63, v17\n"
"v_mfma_f32_16x16x32_bf16 a[24:27], a[48:51], a[152:155], a[24:27]\n"
"s_waitcnt vmcnt(8) lgkmcnt(12)\n"
"ds_read_b128 a[152:155], v5 offset:30720\n"
"v_mul_f32 v12, v12, v112\n"
"v_mfma_f32_16x16x32_bf16 v[144:147], a[64:67], a[96:99], v[144:147]\n"
"v_mul_f32 v13, v13, v113\n"
"v_mul_f32 v14, v14, v114\n"
"v_mfma_f32_16x16x32_bf16 v[144:147], a[68:71], a[100:103], v[144:147]\n"
"v_mul_f32 v15, v15, v115\n"
"v_mfma_f32_16x16x32_bf16 v[172:175], a[76:79], a[100:103], v[172:175]\n"
"v_cvt_pk_bf16_f32 v28, v12, v13\n"
"v_mfma_f32_16x16x32_bf16 v[172:175], a[72:75], a[96:99], v[172:175]\n"
"v_cvt_pk_bf16_f32 v29, v14, v15\n"
"v_lshlrev_b32 v12, 16, v18\n"
"v_mfma_f32_16x16x32_bf16 v[176:179], a[72:75], a[104:107], v[176:179]\n"
"v_and_b32 v13, s63, v18\n"
"v_mfma_f32_16x16x32_bf16 v[176:179], a[76:79], a[108:111], v[176:179]\n"
"v_lshlrev_b32 v14, 16, v19\n"
"v_and_b32 v15, s63, v19\n"
"v_mfma_f32_16x16x32_bf16 v[148:151], a[68:71], a[108:111], v[148:151]\n"
"v_mul_f32 v12, v12, v140\n"
"v_mfma_f32_16x16x32_bf16 v[148:151], a[64:67], a[104:107], v[148:151]\n"
"s_waitcnt lgkmcnt(9)\n"
"v_mul_f32 v13, v13, v141\n"
"v_mfma_f32_16x16x32_bf16 v[152:155], a[64:67], a[112:115], v[152:155]\n"
"v_mul_f32 v14, v14, v142\n"
"v_mul_f32 v15, v15, v143\n"
"v_mfma_f32_16x16x32_bf16 v[152:155], a[68:71], a[116:119], v[152:155]\n"
"v_cvt_pk_bf16_f32 v30, v12, v13\n"
"v_cvt_pk_bf16_f32 v31, v14, v15\n"
"v_mfma_f32_16x16x32_bf16 v[180:183], a[76:79], a[116:119], v[180:183]\n"
"ds_write_b128 v8, v[28:31] offset:12288\n"
"v_mfma_f32_16x16x32_bf16 v[180:183], a[72:75], a[112:115], v[180:183]\n"
"ds_read_b128 v[56:59], v2 offset:12288\n"
"v_mfma_f32_16x16x32_bf16 v[184:187], a[72:75], a[120:123], v[184:187]\n"
"ds_read_b128 v[84:87], v2 offset:13312\n"
"v_mfma_f32_16x16x32_bf16 v[184:187], a[76:79], a[124:127], v[184:187]\n"
"v_accvgpr_read_b32 v16, a184\n"
"v_accvgpr_read_b32 v17, a185\n"
"v_mfma_f32_16x16x32_bf16 v[156:159], a[68:71], a[124:127], v[156:159]\n"
"v_accvgpr_read_b32 v18, a186\n"
"v_mfma_f32_16x16x32_bf16 v[156:159], a[64:67], a[120:123], v[156:159]\n"
"s_waitcnt lgkmcnt(8)\n"
"v_accvgpr_read_b32 v19, a187\n"
"v_mfma_f32_16x16x32_bf16 v[160:163], a[64:67], a[128:131], v[160:163]\n"
"v_lshlrev_b32 v12, 16, v16\n"
"v_and_b32 v13, s63, v16\n"
"v_mfma_f32_16x16x32_bf16 v[160:163], a[68:71], a[132:135], v[160:163]\n"
"v_lshlrev_b32 v14, 16, v17\n"
"v_mfma_f32_16x16x32_bf16 v[188:191], a[76:79], a[132:135], v[188:191]\n"
"v_and_b32 v15, s63, v17\n"
"v_mfma_f32_16x16x32_bf16 v[188:191], a[72:75], a[128:131], v[188:191]\n"
"v_accvgpr_read_b32 v10, a0\n"
"v_accvgpr_read_b32 v11, a1\n"
"v_mfma_f32_16x16x32_bf16 v[192:195], a[72:75], a[136:139], v[192:195]\n"
"v_mul_f32 v12, v12, v10\n"
"v_mfma_f32_16x16x32_bf16 v[192:195], a[76:79], a[140:143], v[192:195]\n"
"v_mul_f32 v13, v13, v11\n"
"v_mfma_f32_16x16x32_bf16 v[164:167], a[68:71], a[140:143], v[164:167]\n"
"v_accvgpr_read_b32 v10, a2\n"
"v_accvgpr_read_b32 v11, a3\n"
"v_mfma_f32_16x16x32_bf16 v[164:167], a[64:67], a[136:139], v[164:167]\n"
"s_waitcnt lgkmcnt(3)\n"
"v_mul_f32 v14, v14, v10\n"
"v_mfma_f32_16x16x32_bf16 v[168:171], a[64:67], a[144:147], v[168:171]\n"
"v_mul_f32 v15, v15, v11\n"
"v_cvt_pk_bf16_f32 v24, v12, v13\n"
"v_mfma_f32_16x16x32_bf16 v[168:171], a[68:71], a[148:151], v[168:171]\n"
"v_cvt_pk_bf16_f32 v25, v14, v15\n"
"v_mfma_f32_16x16x32_bf16 v[196:199], a[76:79], a[148:151], v[196:199]\n"
"v_lshlrev_b32 v12, 16, v18\n"
"v_mfma_f32_16x16x32_bf16 v[196:199], a[72:75], a[144:147], v[196:199]\n"
"v_and_b32 v13, s63, v18\n"
"v_lshlrev_b32 v14, 16, v19\n"
"v_mfma_f32_16x16x32_bf16 a[20:23], a[72:75], a[152:155], a[20:23]\n"
"v_and_b32 v15, s63, v19\n"
"v_mfma_f32_16x16x32_bf16 a[20:23], a[76:79], a[156:159], a[20:23]\n"
"v_accvgpr_read_b32 v10, a4\n"
"v_mfma_f32_16x16x32_bf16 a[16:19], a[68:71], a[156:159], a[16:19]\n"
"v_accvgpr_read_b32 v11, a5\n"
"v_mul_f32 v12, v12, v10\n"
"v_mfma_f32_16x16x32_bf16 a[16:19], a[64:67], a[152:155], a[16:19]\n"
"s_waitcnt vmcnt(2)\n"
"v_mul_f32 v13, v13, v11\n"
"v_mfma_f32_16x16x32_bf16 v[200:203], a[80:83], a[96:99], v[200:203]\n"
"v_accvgpr_read_b32 v10, a6\n"
"v_accvgpr_read_b32 v11, a7\n"
"v_mfma_f32_16x16x32_bf16 v[200:203], a[84:87], a[100:103], v[200:203]\n"
"v_mul_f32 v14, v14, v10\n"
"v_mfma_f32_16x16x32_bf16 v[228:231], a[92:95], a[100:103], v[228:231]\n"
"v_mul_f32 v15, v15, v11\n"
"v_mfma_f32_16x16x32_bf16 v[228:231], a[88:91], a[96:99], v[228:231]\n"
"v_cvt_pk_bf16_f32 v26, v12, v13\n"
"v_mfma_f32_16x16x32_bf16 v[232:235], a[88:91], a[104:107], v[232:235]\n"
"v_cvt_pk_bf16_f32 v27, v14, v15\n"
"v_mfma_f32_16x16x32_bf16 v[232:235], a[92:95], a[108:111], v[232:235]\n"
"ds_write_b128 v7, v[24:27] offset:14336\n"
"v_mfma_f32_16x16x32_bf16 v[204:207], a[84:87], a[108:111], v[204:207]\n"
"s_waitcnt lgkmcnt(2)\n"
"buffer_store_dwordx4 v[56:59], v6, s[52:55], s92 offen offset:0 sc1\n"
"v_mfma_f32_16x16x32_bf16 v[204:207], a[80:83], a[104:107], v[204:207]\n"
"s_waitcnt lgkmcnt(1)\n"
"buffer_store_dwordx4 v[84:87], v6, s[52:55], s93 offen offset:1024 sc1\n"
"v_accvgpr_read_b32 v16, a188\n"
"v_accvgpr_read_b32 v17, a189\n"
"v_accvgpr_read_b32 v18, a190\n"
"v_mfma_f32_16x16x32_bf16 v[208:211], a[80:83], a[112:115], v[208:211]\n"
"v_accvgpr_read_b32 v19, a191\n"
"v_mfma_f32_16x16x32_bf16 v[208:211], a[84:87], a[116:119], v[208:211]\n"
"v_lshlrev_b32 v12, 16, v16\n"
"v_mfma_f32_16x16x32_bf16 v[236:239], a[92:95], a[116:119], v[236:239]\n"
"v_and_b32 v13, s63, v16\n"
"v_mfma_f32_16x16x32_bf16 v[236:239], a[88:91], a[112:115], v[236:239]\n"
"v_lshlrev_b32 v14, 16, v17\n"
"v_and_b32 v15, s63, v17\n"
"v_mfma_f32_16x16x32_bf16 v[240:243], a[88:91], a[120:123], v[240:243]\n"
"v_accvgpr_read_b32 v10, a8\n"
"v_mfma_f32_16x16x32_bf16 v[240:243], a[92:95], a[124:127], v[240:243]\n"
"v_accvgpr_read_b32 v11, a9\n"
"v_mfma_f32_16x16x32_bf16 v[212:215], a[84:87], a[124:127], v[212:215]\n"
"v_mul_f32 v12, v12, v10\n"
"v_mul_f32 v13, v13, v11\n"
"v_mfma_f32_16x16x32_bf16 v[212:215], a[80:83], a[120:123], v[212:215]\n"
"v_accvgpr_read_b32 v10, a10\n"
"v_mfma_f32_16x16x32_bf16 v[216:219], a[80:83], a[128:131], v[216:219]\n"
"v_accvgpr_read_b32 v11, a11\n"
"v_mfma_f32_16x16x32_bf16 v[216:219], a[84:87], a[132:135], v[216:219]\n"
"v_mul_f32 v14, v14, v10\n"
"v_mul_f32 v15, v15, v11\n"
"v_mfma_f32_16x16x32_bf16 v[244:247], a[92:95], a[132:135], v[244:247]\n"
"v_cvt_pk_bf16_f32 v28, v12, v13\n"
"v_mfma_f32_16x16x32_bf16 v[244:247], a[88:91], a[128:131], v[244:247]\n"
"v_cvt_pk_bf16_f32 v29, v14, v15\n"
"v_lshlrev_b32 v12, 16, v18\n"
"v_mfma_f32_16x16x32_bf16 v[248:251], a[88:91], a[136:139], v[248:251]\n"
"v_and_b32 v13, s63, v18\n"
"v_mfma_f32_16x16x32_bf16 v[248:251], a[92:95], a[140:143], v[248:251]\n"
"v_lshlrev_b32 v14, 16, v19\n"
"v_mfma_f32_16x16x32_bf16 v[220:223], a[84:87], a[140:143], v[220:223]\n"
"v_and_b32 v15, s63, v19\n"
"v_accvgpr_read_b32 v10, a12\n"
"v_mfma_f32_16x16x32_bf16 v[220:223], a[80:83], a[136:139], v[220:223]\n"
"v_accvgpr_read_b32 v11, a13\n"
"v_mfma_f32_16x16x32_bf16 v[224:227], a[80:83], a[144:147], v[224:227]\n"
"v_mul_f32 v12, v12, v10\n"
"v_mfma_f32_16x16x32_bf16 v[224:227], a[84:87], a[148:151], v[224:227]\n"
"v_mul_f32 v13, v13, v11\n"
"v_accvgpr_read_b32 v10, a14\n"
"v_mfma_f32_16x16x32_bf16 v[252:255], a[92:95], a[148:151], v[252:255]\n"
"v_accvgpr_read_b32 v11, a15\n"
"v_mfma_f32_16x16x32_bf16 v[252:255], a[88:91], a[144:147], v[252:255]\n"
"v_mul_f32 v14, v14, v10\n"
"v_mul_f32 v15, v15, v11\n"
"v_mfma_f32_16x16x32_bf16 a[28:31], a[88:91], a[152:155], a[28:31]\n"
"s_add_u32 s62, s62, 1\n"
"v_cvt_pk_bf16_f32 v30, v12, v13\n"
"v_cvt_pk_bf16_f32 v31, v14, v15\n"
"v_mfma_f32_16x16x32_bf16 a[28:31], a[92:95], a[156:159], a[28:31]\n"
"ds_write_b128 v8, v[28:31] offset:14336\n"
"v_mfma_f32_16x16x32_bf16 a[24:27], a[84:87], a[156:159], a[24:27]\n"
"ds_read_b128 v[52:55], v2 offset:14336\n"
"v_mfma_f32_16x16x32_bf16 a[24:27], a[80:83], a[152:155], a[24:27]\n"
"ds_read_b128 v[80:83], v2 offset:15360\n"
"s_nop 7\n"
"v_accvgpr_read_b32 v16, a192\n"
"v_accvgpr_read_b32 v17, a193\n"
"v_accvgpr_read_b32 v18, a194\n"
"v_accvgpr_read_b32 v19, a195\n"
"v_lshlrev_b32 v12, 16, v16\n"
"v_and_b32 v13, s63, v16\n"
"v_lshlrev_b32 v14, 16, v17\n"
"v_and_b32 v15, s63, v17\n"
"v_pk_mul_f32 v[12:13], v[12:13], v[144:145]\n"
"v_pk_mul_f32 v[14:15], v[14:15], v[146:147]\n"
"v_cvt_pk_bf16_f32 v24, v12, v13\n"
"v_cvt_pk_bf16_f32 v25, v14, v15\n"
"v_lshlrev_b32 v12, 16, v18\n"
"v_and_b32 v13, s63, v18\n"
"v_lshlrev_b32 v14, 16, v19\n"
"v_and_b32 v15, s63, v19\n"
"v_pk_mul_f32 v[12:13], v[12:13], v[172:173]\n"
"v_pk_mul_f32 v[14:15], v[14:15], v[174:175]\n"
"v_cvt_pk_bf16_f32 v26, v12, v13\n"
"v_cvt_pk_bf16_f32 v27, v14, v15\n"
"ds_write_b128 v7, v[24:27] offset:0\n"
"s_waitcnt lgkmcnt(2)\n"
"buffer_store_dwordx4 v[52:55], v6, s[52:55], s94 offen offset:2048 sc1\n"
"s_waitcnt lgkmcnt(1)\n"
"buffer_store_dwordx4 v[80:83], v6, s[52:55], s95 offen offset:3072 sc1\n"
"v_accvgpr_read_b32 v16, a196\n"
"v_accvgpr_read_b32 v17, a197\n"
"v_accvgpr_read_b32 v18, a198\n"
"v_accvgpr_read_b32 v19, a199\n"
"v_lshlrev_b32 v12, 16, v16\n"
"v_and_b32 v13, s63, v16\n"
"v_lshlrev_b32 v14, 16, v17\n"
"v_and_b32 v15, s63, v17\n"
"v_pk_mul_f32 v[12:13], v[12:13], v[200:201]\n"
"v_pk_mul_f32 v[14:15], v[14:15], v[202:203]\n"
"v_cvt_pk_bf16_f32 v28, v12, v13\n"
"v_cvt_pk_bf16_f32 v29, v14, v15\n"
"v_lshlrev_b32 v12, 16, v18\n"
"v_and_b32 v13, s63, v18\n"
"v_lshlrev_b32 v14, 16, v19\n"
"v_and_b32 v15, s63, v19\n"
"v_pk_mul_f32 v[12:13], v[12:13], v[228:229]\n"
"v_pk_mul_f32 v[14:15], v[14:15], v[230:231]\n"
"v_cvt_pk_bf16_f32 v30, v12, v13\n"
"v_cvt_pk_bf16_f32 v31, v14, v15\n"
"ds_write_b128 v8, v[28:31] offset:0\n"
"ds_read_b128 v[144:147], v2 offset:0\n"
"ds_read_b128 v[172:175], v2 offset:1024\n"
"v_accvgpr_read_b32 v16, a200\n"
"v_accvgpr_read_b32 v17, a201\n"
"v_accvgpr_read_b32 v18, a202\n"
"v_accvgpr_read_b32 v19, a203\n"
"v_lshlrev_b32 v12, 16, v16\n"
"v_and_b32 v13, s63, v16\n"
"v_lshlrev_b32 v14, 16, v17\n"
"v_and_b32 v15, s63, v17\n"
"v_pk_mul_f32 v[12:13], v[12:13], v[148:149]\n"
"v_pk_mul_f32 v[14:15], v[14:15], v[150:151]\n"
"v_cvt_pk_bf16_f32 v24, v12, v13\n"
"v_cvt_pk_bf16_f32 v25, v14, v15\n"
"v_lshlrev_b32 v12, 16, v18\n"
"v_and_b32 v13, s63, v18\n"
"v_lshlrev_b32 v14, 16, v19\n"
"v_and_b32 v15, s63, v19\n"
"v_pk_mul_f32 v[12:13], v[12:13], v[176:177]\n"
"v_pk_mul_f32 v[14:15], v[14:15], v[178:179]\n"
"v_cvt_pk_bf16_f32 v26, v12, v13\n"
"v_cvt_pk_bf16_f32 v27, v14, v15\n"
"ds_write_b128 v7, v[24:27] offset:2048\n"
"s_waitcnt lgkmcnt(2)\n"
"buffer_store_dwordx4 v[144:147], v6, s[52:55], s80 offen offset:128 sc1\n"
"s_waitcnt lgkmcnt(1)\n"
"buffer_store_dwordx4 v[172:175], v6, s[52:55], s81 offen offset:1152 sc1\n"
"v_accvgpr_read_b32 v16, a204\n"
"v_accvgpr_read_b32 v17, a205\n"
"v_accvgpr_read_b32 v18, a206\n"
"v_accvgpr_read_b32 v19, a207\n"
"v_lshlrev_b32 v12, 16, v16\n"
"v_and_b32 v13, s63, v16\n"
"v_lshlrev_b32 v14, 16, v17\n"
"v_and_b32 v15, s63, v17\n"
"v_pk_mul_f32 v[12:13], v[12:13], v[204:205]\n"
"v_pk_mul_f32 v[14:15], v[14:15], v[206:207]\n"
"v_cvt_pk_bf16_f32 v28, v12, v13\n"
"v_cvt_pk_bf16_f32 v29, v14, v15\n"
"v_lshlrev_b32 v12, 16, v18\n"
"v_and_b32 v13, s63, v18\n"
"v_lshlrev_b32 v14, 16, v19\n"
"v_and_b32 v15, s63, v19\n"
"v_pk_mul_f32 v[12:13], v[12:13], v[232:233]\n"
"v_pk_mul_f32 v[14:15], v[14:15], v[234:235]\n"
"v_cvt_pk_bf16_f32 v30, v12, v13\n"
"v_cvt_pk_bf16_f32 v31, v14, v15\n"
"ds_write_b128 v8, v[28:31] offset:2048\n"
"ds_read_b128 v[148:151], v2 offset:2048\n"
"ds_read_b128 v[176:179], v2 offset:3072\n"
"v_accvgpr_read_b32 v16, a208\n"
"v_accvgpr_read_b32 v17, a209\n"
"v_accvgpr_read_b32 v18, a210\n"
"v_accvgpr_read_b32 v19, a211\n"
"v_lshlrev_b32 v12, 16, v16\n"
"v_and_b32 v13, s63, v16\n"
"v_lshlrev_b32 v14, 16, v17\n"
"v_and_b32 v15, s63, v17\n"
"v_pk_mul_f32 v[12:13], v[12:13], v[152:153]\n"
"v_pk_mul_f32 v[14:15], v[14:15], v[154:155]\n"
"v_cvt_pk_bf16_f32 v24, v12, v13\n"
"v_cvt_pk_bf16_f32 v25, v14, v15\n"
"v_lshlrev_b32 v12, 16, v18\n"
"v_and_b32 v13, s63, v18\n"
"v_lshlrev_b32 v14, 16, v19\n"
"v_and_b32 v15, s63, v19\n"
"v_pk_mul_f32 v[12:13], v[12:13], v[180:181]\n"
"v_pk_mul_f32 v[14:15], v[14:15], v[182:183]\n"
"v_cvt_pk_bf16_f32 v26, v12, v13\n"
"v_cvt_pk_bf16_f32 v27, v14, v15\n"
"ds_write_b128 v7, v[24:27] offset:4096\n"
"s_waitcnt lgkmcnt(2)\n"
"buffer_store_dwordx4 v[148:151], v6, s[52:55], s82 offen offset:2176 sc1\n"
"s_waitcnt lgkmcnt(1)\n"
"buffer_store_dwordx4 v[176:179], v6, s[52:55], s83 offen offset:3200 sc1\n"
"v_accvgpr_read_b32 v16, a212\n"
"v_accvgpr_read_b32 v17, a213\n"
"v_accvgpr_read_b32 v18, a214\n"
"v_accvgpr_read_b32 v19, a215\n"
"v_lshlrev_b32 v12, 16, v16\n"
"v_and_b32 v13, s63, v16\n"
"v_lshlrev_b32 v14, 16, v17\n"
"v_and_b32 v15, s63, v17\n"
"v_pk_mul_f32 v[12:13], v[12:13], v[208:209]\n"
"v_pk_mul_f32 v[14:15], v[14:15], v[210:211]\n"
"v_cvt_pk_bf16_f32 v28, v12, v13\n"
"v_cvt_pk_bf16_f32 v29, v14, v15\n"
"v_lshlrev_b32 v12, 16, v18\n"
"v_and_b32 v13, s63, v18\n"
"v_lshlrev_b32 v14, 16, v19\n"
"v_and_b32 v15, s63, v19\n"
"v_pk_mul_f32 v[12:13], v[12:13], v[236:237]\n"
"v_pk_mul_f32 v[14:15], v[14:15], v[238:239]\n"
"v_cvt_pk_bf16_f32 v30, v12, v13\n"
"v_cvt_pk_bf16_f32 v31, v14, v15\n"
"ds_write_b128 v8, v[28:31] offset:4096\n"
"ds_read_b128 v[152:155], v2 offset:4096\n"
"ds_read_b128 v[180:183], v2 offset:5120\n"
"v_accvgpr_read_b32 v16, a216\n"
"v_accvgpr_read_b32 v17, a217\n"
"v_accvgpr_read_b32 v18, a218\n"
"v_accvgpr_read_b32 v19, a219\n"
"v_lshlrev_b32 v12, 16, v16\n"
"v_and_b32 v13, s63, v16\n"
"v_lshlrev_b32 v14, 16, v17\n"
"v_and_b32 v15, s63, v17\n"
"v_pk_mul_f32 v[12:13], v[12:13], v[156:157]\n"
"v_pk_mul_f32 v[14:15], v[14:15], v[158:159]\n"
"v_cvt_pk_bf16_f32 v24, v12, v13\n"
"v_cvt_pk_bf16_f32 v25, v14, v15\n"
"v_lshlrev_b32 v12, 16, v18\n"
"v_and_b32 v13, s63, v18\n"
"v_lshlrev_b32 v14, 16, v19\n"
"v_and_b32 v15, s63, v19\n"
"v_pk_mul_f32 v[12:13], v[12:13], v[184:185]\n"
"v_pk_mul_f32 v[14:15], v[14:15], v[186:187]\n"
"v_cvt_pk_bf16_f32 v26, v12, v13\n"
"v_cvt_pk_bf16_f32 v27, v14, v15\n"
"ds_write_b128 v7, v[24:27] offset:6144\n"
"s_waitcnt lgkmcnt(2)\n"
"buffer_store_dwordx4 v[152:155], v6, s[52:55], s84 offen offset:128 sc1\n"
"s_waitcnt lgkmcnt(1)\n"
"buffer_store_dwordx4 v[180:183], v6, s[52:55], s85 offen offset:1152 sc1\n"
"v_accvgpr_read_b32 v16, a220\n"
"v_accvgpr_read_b32 v17, a221\n"
"v_accvgpr_read_b32 v18, a222\n"
"v_accvgpr_read_b32 v19, a223\n"
"v_lshlrev_b32 v12, 16, v16\n"
"v_and_b32 v13, s63, v16\n"
"v_lshlrev_b32 v14, 16, v17\n"
"v_and_b32 v15, s63, v17\n"
"v_pk_mul_f32 v[12:13], v[12:13], v[212:213]\n"
"v_pk_mul_f32 v[14:15], v[14:15], v[214:215]\n"
"v_cvt_pk_bf16_f32 v28, v12, v13\n"
"v_cvt_pk_bf16_f32 v29, v14, v15\n"
"v_lshlrev_b32 v12, 16, v18\n"
"v_and_b32 v13, s63, v18\n"
"v_lshlrev_b32 v14, 16, v19\n"
"v_and_b32 v15, s63, v19\n"
"v_pk_mul_f32 v[12:13], v[12:13], v[240:241]\n"
"v_pk_mul_f32 v[14:15], v[14:15], v[242:243]\n"
"v_cvt_pk_bf16_f32 v30, v12, v13\n"
"v_cvt_pk_bf16_f32 v31, v14, v15\n"
"ds_write_b128 v8, v[28:31] offset:6144\n"
"ds_read_b128 v[156:159], v2 offset:6144\n"
"ds_read_b128 v[184:187], v2 offset:7168\n"
"v_accvgpr_read_b32 v16, a224\n"
"v_accvgpr_read_b32 v17, a225\n"
"v_accvgpr_read_b32 v18, a226\n"
"v_accvgpr_read_b32 v19, a227\n"
"v_lshlrev_b32 v12, 16, v16\n"
"v_and_b32 v13, s63, v16\n"
"v_lshlrev_b32 v14, 16, v17\n"
"v_and_b32 v15, s63, v17\n"
"v_pk_mul_f32 v[12:13], v[12:13], v[160:161]\n"
"v_pk_mul_f32 v[14:15], v[14:15], v[162:163]\n"
"v_cvt_pk_bf16_f32 v24, v12, v13\n"
"v_cvt_pk_bf16_f32 v25, v14, v15\n"
"v_lshlrev_b32 v12, 16, v18\n"
"v_and_b32 v13, s63, v18\n"
"v_lshlrev_b32 v14, 16, v19\n"
"v_and_b32 v15, s63, v19\n"
"v_pk_mul_f32 v[12:13], v[12:13], v[188:189]\n"
"v_pk_mul_f32 v[14:15], v[14:15], v[190:191]\n"
"v_cvt_pk_bf16_f32 v26, v12, v13\n"
"v_cvt_pk_bf16_f32 v27, v14, v15\n"
"ds_write_b128 v7, v[24:27] offset:8192\n"
"s_waitcnt lgkmcnt(2)\n"
"buffer_store_dwordx4 v[156:159], v6, s[52:55], s86 offen offset:2176 sc1\n"
"s_waitcnt lgkmcnt(1)\n"
"buffer_store_dwordx4 v[184:187], v6, s[52:55], s87 offen offset:3200 sc1\n"
"v_accvgpr_read_b32 v16, a228\n"
"v_accvgpr_read_b32 v17, a229\n"
"v_accvgpr_read_b32 v18, a230\n"
"v_accvgpr_read_b32 v19, a231\n"
"v_lshlrev_b32 v12, 16, v16\n"
"v_and_b32 v13, s63, v16\n"
"v_lshlrev_b32 v14, 16, v17\n"
"v_and_b32 v15, s63, v17\n"
"v_pk_mul_f32 v[12:13], v[12:13], v[216:217]\n"
"v_pk_mul_f32 v[14:15], v[14:15], v[218:219]\n"
"v_cvt_pk_bf16_f32 v28, v12, v13\n"
"v_cvt_pk_bf16_f32 v29, v14, v15\n"
"v_lshlrev_b32 v12, 16, v18\n"
"v_and_b32 v13, s63, v18\n"
"v_lshlrev_b32 v14, 16, v19\n"
"v_and_b32 v15, s63, v19\n"
"v_pk_mul_f32 v[12:13], v[12:13], v[244:245]\n"
"v_pk_mul_f32 v[14:15], v[14:15], v[246:247]\n"
"v_cvt_pk_bf16_f32 v30, v12, v13\n"
"v_cvt_pk_bf16_f32 v31, v14, v15\n"
"ds_write_b128 v8, v[28:31] offset:8192\n"
"ds_read_b128 v[160:163], v2 offset:8192\n"
"ds_read_b128 v[188:191], v2 offset:9216\n"
"v_accvgpr_read_b32 v16, a232\n"
"v_accvgpr_read_b32 v17, a233\n"
"v_accvgpr_read_b32 v18, a234\n"
"v_accvgpr_read_b32 v19, a235\n"
"v_lshlrev_b32 v12, 16, v16\n"
"v_and_b32 v13, s63, v16\n"
"v_lshlrev_b32 v14, 16, v17\n"
"v_and_b32 v15, s63, v17\n"
"v_pk_mul_f32 v[12:13], v[12:13], v[164:165]\n"
"v_pk_mul_f32 v[14:15], v[14:15], v[166:167]\n"
"v_cvt_pk_bf16_f32 v24, v12, v13\n"
"v_cvt_pk_bf16_f32 v25, v14, v15\n"
"v_lshlrev_b32 v12, 16, v18\n"
"v_and_b32 v13, s63, v18\n"
"v_lshlrev_b32 v14, 16, v19\n"
"v_and_b32 v15, s63, v19\n"
"v_pk_mul_f32 v[12:13], v[12:13], v[192:193]\n"
"v_pk_mul_f32 v[14:15], v[14:15], v[194:195]\n"
"v_cvt_pk_bf16_f32 v26, v12, v13\n"
"v_cvt_pk_bf16_f32 v27, v14, v15\n"
"ds_write_b128 v7, v[24:27] offset:10240\n"
"s_waitcnt lgkmcnt(2)\n"
"buffer_store_dwordx4 v[160:163], v6, s[52:55], s88 offen offset:128 sc1\n"
"s_waitcnt lgkmcnt(1)\n"
"buffer_store_dwordx4 v[188:191], v6, s[52:55], s89 offen offset:1152 sc1\n"
"v_accvgpr_read_b32 v16, a236\n"
"v_accvgpr_read_b32 v17, a237\n"
"v_accvgpr_read_b32 v18, a238\n"
"v_accvgpr_read_b32 v19, a239\n"
"v_lshlrev_b32 v12, 16, v16\n"
"v_and_b32 v13, s63, v16\n"
"v_lshlrev_b32 v14, 16, v17\n"
"v_and_b32 v15, s63, v17\n"
"v_pk_mul_f32 v[12:13], v[12:13], v[220:221]\n"
"v_pk_mul_f32 v[14:15], v[14:15], v[222:223]\n"
"v_cvt_pk_bf16_f32 v28, v12, v13\n"
"v_cvt_pk_bf16_f32 v29, v14, v15\n"
"v_lshlrev_b32 v12, 16, v18\n"
"v_and_b32 v13, s63, v18\n"
"v_lshlrev_b32 v14, 16, v19\n"
"v_and_b32 v15, s63, v19\n"
"v_pk_mul_f32 v[12:13], v[12:13], v[248:249]\n"
"v_pk_mul_f32 v[14:15], v[14:15], v[250:251]\n"
"v_cvt_pk_bf16_f32 v30, v12, v13\n"
"v_cvt_pk_bf16_f32 v31, v14, v15\n"
"ds_write_b128 v8, v[28:31] offset:10240\n"
"ds_read_b128 v[164:167], v2 offset:10240\n"
"ds_read_b128 v[192:195], v2 offset:11264\n"
"v_accvgpr_read_b32 v16, a240\n"
"v_accvgpr_read_b32 v17, a241\n"
"v_accvgpr_read_b32 v18, a242\n"
"v_accvgpr_read_b32 v19, a243\n"
"v_lshlrev_b32 v12, 16, v16\n"
"v_and_b32 v13, s63, v16\n"
"v_lshlrev_b32 v14, 16, v17\n"
"v_and_b32 v15, s63, v17\n"
"v_pk_mul_f32 v[12:13], v[12:13], v[168:169]\n"
"v_pk_mul_f32 v[14:15], v[14:15], v[170:171]\n"
"v_cvt_pk_bf16_f32 v24, v12, v13\n"
"v_cvt_pk_bf16_f32 v25, v14, v15\n"
"v_lshlrev_b32 v12, 16, v18\n"
"v_and_b32 v13, s63, v18\n"
"v_lshlrev_b32 v14, 16, v19\n"
"v_and_b32 v15, s63, v19\n"
"v_pk_mul_f32 v[12:13], v[12:13], v[196:197]\n"
"v_pk_mul_f32 v[14:15], v[14:15], v[198:199]\n"
"v_cvt_pk_bf16_f32 v26, v12, v13\n"
"v_cvt_pk_bf16_f32 v27, v14, v15\n"
"ds_write_b128 v7, v[24:27] offset:12288\n"
"s_waitcnt lgkmcnt(2)\n"
"buffer_store_dwordx4 v[164:167], v6, s[52:55], s90 offen offset:2176 sc1\n"
"s_waitcnt lgkmcnt(1)\n"
"buffer_store_dwordx4 v[192:195], v6, s[52:55], s91 offen offset:3200 sc1\n"
"v_accvgpr_read_b32 v16, a244\n"
"v_accvgpr_read_b32 v17, a245\n"
"v_accvgpr_read_b32 v18, a246\n"
"v_accvgpr_read_b32 v19, a247\n"
"v_lshlrev_b32 v12, 16, v16\n"
"v_and_b32 v13, s63, v16\n"
"v_lshlrev_b32 v14, 16, v17\n"
"v_and_b32 v15, s63, v17\n"
"v_pk_mul_f32 v[12:13], v[12:13], v[224:225]\n"
"v_pk_mul_f32 v[14:15], v[14:15], v[226:227]\n"
"v_cvt_pk_bf16_f32 v28, v12, v13\n"
"v_cvt_pk_bf16_f32 v29, v14, v15\n"
"v_lshlrev_b32 v12, 16, v18\n"
"v_and_b32 v13, s63, v18\n"
"v_lshlrev_b32 v14, 16, v19\n"
"v_and_b32 v15, s63, v19\n"
"v_pk_mul_f32 v[12:13], v[12:13], v[252:253]\n"
"v_pk_mul_f32 v[14:15], v[14:15], v[254:255]\n"
"v_cvt_pk_bf16_f32 v30, v12, v13\n"
"v_cvt_pk_bf16_f32 v31, v14, v15\n"
"ds_write_b128 v8, v[28:31] offset:12288\n"
"ds_read_b128 v[168:171], v2 offset:12288\n"
"ds_read_b128 v[196:199], v2 offset:13312\n"
"v_accvgpr_read_b32 v16, a248\n"
"v_accvgpr_read_b32 v17, a249\n"
"v_accvgpr_read_b32 v18, a250\n"
"v_accvgpr_read_b32 v19, a251\n"
"v_lshlrev_b32 v12, 16, v16\n"
"v_and_b32 v13, s63, v16\n"
"v_lshlrev_b32 v14, 16, v17\n"
"v_and_b32 v15, s63, v17\n"
"v_accvgpr_read_b32 v10, a16\n"
"v_accvgpr_read_b32 v11, a17\n"
"v_mul_f32 v12, v12, v10\n"
"v_mul_f32 v13, v13, v11\n"
"v_accvgpr_read_b32 v10, a18\n"
"v_accvgpr_read_b32 v11, a19\n"
"v_mul_f32 v14, v14, v10\n"
"v_mul_f32 v15, v15, v11\n"
"v_cvt_pk_bf16_f32 v24, v12, v13\n"
"v_cvt_pk_bf16_f32 v25, v14, v15\n"
"v_lshlrev_b32 v12, 16, v18\n"
"v_and_b32 v13, s63, v18\n"
"v_lshlrev_b32 v14, 16, v19\n"
"v_and_b32 v15, s63, v19\n"
"v_accvgpr_read_b32 v10, a20\n"
"v_accvgpr_read_b32 v11, a21\n"
"v_mul_f32 v12, v12, v10\n"
"v_mul_f32 v13, v13, v11\n"
"v_accvgpr_read_b32 v10, a22\n"
"v_accvgpr_read_b32 v11, a23\n"
"v_mul_f32 v14, v14, v10\n"
"v_mul_f32 v15, v15, v11\n"
"v_cvt_pk_bf16_f32 v26, v12, v13\n"
"v_cvt_pk_bf16_f32 v27, v14, v15\n"
"ds_write_b128 v7, v[24:27] offset:14336\n"
"s_waitcnt lgkmcnt(2)\n"
"buffer_store_dwordx4 v[168:171], v6, s[52:55], s92 offen offset:128 sc1\n"
"s_waitcnt lgkmcnt(1)\n"
"buffer_store_dwordx4 v[196:199], v6, s[52:55], s93 offen offset:1152 sc1\n"
"v_accvgpr_read_b32 v16, a252\n"
"v_accvgpr_read_b32 v17, a253\n"
"v_accvgpr_read_b32 v18, a254\n"
"v_accvgpr_read_b32 v19, a255\n"
"v_lshlrev_b32 v12, 16, v16\n"
"v_and_b32 v13, s63, v16\n"
"v_lshlrev_b32 v14, 16, v17\n"
"v_and_b32 v15, s63, v17\n"
"v_accvgpr_read_b32 v10, a24\n"
"v_accvgpr_read_b32 v11, a25\n"
"v_mul_f32 v12, v12, v10\n"
"v_mul_f32 v13, v13, v11\n"
"v_accvgpr_read_b32 v10, a26\n"
"v_accvgpr_read_b32 v11, a27\n"
"v_mul_f32 v14, v14, v10\n"
"v_mul_f32 v15, v15, v11\n"
"v_cvt_pk_bf16_f32 v28, v12, v13\n"
"v_cvt_pk_bf16_f32 v29, v14, v15\n"
"v_lshlrev_b32 v12, 16, v18\n"
"v_and_b32 v13, s63, v18\n"
"v_lshlrev_b32 v14, 16, v19\n"
"v_and_b32 v15, s63, v19\n"
"v_accvgpr_read_b32 v10, a28\n"
"v_accvgpr_read_b32 v11, a29\n"
"v_mul_f32 v12, v12, v10\n"
"v_mul_f32 v13, v13, v11\n"
"v_accvgpr_read_b32 v10, a30\n"
"v_accvgpr_read_b32 v11, a31\n"
"v_mul_f32 v14, v14, v10\n"
"v_mul_f32 v15, v15, v11\n"
"v_cvt_pk_bf16_f32 v30, v12, v13\n"
"v_cvt_pk_bf16_f32 v31, v14, v15\n"
"ds_write_b128 v8, v[28:31] offset:14336\n"
"ds_read_b128 v[164:167], v2 offset:14336\n"
"ds_read_b128 v[192:195], v2 offset:15360\n"
"s_waitcnt lgkmcnt(1)\n"
"buffer_store_dwordx4 v[164:167], v6, s[52:55], s94 offen offset:2176 sc1\n"
"s_waitcnt lgkmcnt(0)\n"
"buffer_store_dwordx4 v[192:195], v6, s[52:55], s95 offen offset:3200 sc1\n"
"s_or_b32 s66, %26, %16\n"
"s_cbranch_scc1 L_end\n"
"v_mov_b32 v10, s78\n"
"v_mov_b32 v11, s79\n"
"s_mov_b32 s66, 139264\n"
"buffer_store_dwordx2 v[10:11], off, s[56:59], s66 sc1\n"
"s_branch L_end\n"
"L_slow0:\n"
"s_cmp_eq_u32 s68, 0\n"
"s_cbranch_scc1 L_back0\n"
"s_sub_u32 s68, s68, 1\n"
"s_sleep 2\n"
"buffer_load_dwordx2 v[8:9], off, s[56:59], %14 sc1\n"
"s_waitcnt vmcnt(0)\n"
"v_subrev_u32 v8, s65, v8\n"
"v_xor_b32 v9, s79, v9\n"
"v_or_b32 v8, v8, v9\n"
"v_cmp_lt_u32 vcc, 15, v8\n"
"s_nop 4\n"
"s_cbranch_vccnz L_slow0\n"
"s_branch L_back0\n"
"L_slow1:\n"
"s_cmp_eq_u32 s68, 0\n"
"s_cbranch_scc1 L_back1\n"
"s_sub_u32 s68, s68, 1\n"
"s_sleep 2\n"
"buffer_load_dwordx2 v[8:9], off, s[56:59], %14 sc1\n"
"s_waitcnt vmcnt(0)\n"
"v_subrev_u32 v8, s65, v8\n"
"v_xor_b32 v9, s79, v9\n"
"v_or_b32 v8, v8, v9\n"
"v_cmp_lt_u32 vcc, 15, v8\n"
"s_nop 4\n"
"s_cbranch_vccnz L_slow1\n"
"s_branch L_back1\n"
"L_slow2:\n"
"s_cmp_eq_u32 s68, 0\n"
"s_cbranch_scc1 L_back2\n"
"s_sub_u32 s68, s68, 1\n"
"s_sleep 2\n"
"buffer_load_dwordx4 v[12:15], v0, s[56:59], %25 offen sc1\n"
"s_waitcnt vmcnt(0)\n"
"v_subrev_u32 v12, s67, v12\n"
"v_xor_b32 v13, s79, v13\n"
"v_subrev_u32 v14, s67, v14\n"
"v_xor_b32 v15, s79, v15\n"
"v_or_b32 v12, v12, v14\n"
"v_or_b32 v13, v13, v15\n"
"v_or_b32 v12, v12, v13\n"
"v_cmp_lt_u32 vcc, 15, v12\n"
"s_nop 4\n"
"s_cbranch_vccnz L_slow2\n"
"s_branch L_back2\n"
"L_slow3:\n"
"s_cmp_eq_u32 s68, 0\n"
"s_cbranch_scc1 L_back3\n"
"s_sub_u32 s68, s68, 1\n"
"s_sleep 2\n"
"buffer_load_dwordx4 v[24:27], v0, s[56:59], %25 offen sc1\n"
"s_waitcnt vmcnt(0)\n"
"v_subrev_u32 v24, s70, v24\n"
"v_xor_b32 v25, s79, v25\n"
"v_subrev_u32 v26, s70, v26\n"
"v_xor_b32 v27, s79, v27\n"
"v_or_b32 v24, v24, v26\n"
"v_or_b32 v25, v25, v27\n"
"v_or_b32 v24, v24, v25\n"
"v_cmp_lt_u32 vcc, 15, v24\n"
"s_nop 4\n"
"s_cbranch_vccnz L_slow3\n"
"s_branch L_back3\n"
"L_end:\n"
"s_waitcnt vmcnt(0) lgkmcnt(0)\n"
    :: LO(wsb), HI(wsb), LO(xo), HI(xo), LO(ob), HI(ob), LO(wr), HI(wr), LO(bb), HI(bb), "s"(wo), "s"(eo), "s"(ep), "s"(fo), "s"(fp),
       "s"(ldsa), "s"(w), "s"(col), "v"(threadIdx.x), "s"(db), "s"(rs1), "s"(rs2), "s"(rd1), "s"(rn1), "s"(wfo), "s"(wfa), "s"(b)
    : "memory", "scc", "vcc",
"v1","v2","v3","v4","v5","v6","v7","v8","v9","v10","v11","v12","v13","v14","v15","v16","v17","v18","v19","v20","v21","v22","v23","v24","v25","v26","v27","v28","v29","v30","v31","v32","v33","v34","v35","v36","v37","v38","v39","v40","v41","v42","v43","v44","v45","v46","v47","v48","v49","v50","v51","v52","v53","v54","v55","v56","v57","v58","v59","v60","v61","v62","v63","v64","v65","v66","v67","v68","v69","v70","v71","v72","v73","v74","v75","v76","v77","v78","v79","v80","v81","v82","v83","v84","v85","v86","v87","v88","v89","v90","v91","v92","v93","v94","v95","v96","v97","v98","v99","v100","v101","v102","v103","v104","v105","v106","v107","v108","v109","v110","v111","v112","v113","v114","v115","v116","v117","v118","v119","v120","v121","v122","v123","v124","v125","v126","v127","v128","v129","v130","v131","v132","v133","v134","v135","v136","v137","v138","v139","v140","v141","v142","v143","v144","v145","v146","v147","v148","v149","v150","v151","v152","v153","v154","v155","v156","v157","v158","v159","v160","v161","v162","v163","v164","v165","v166","v167","v168","v169","v170","v171","v172","v173","v174","v175","v176","v177","v178","v179","v180","v181","v182","v183","v184","v185","v186","v187","v188","v189","v190","v191","v192","v193","v194","v195","v196","v197","v198","v199","v200","v201","v202","v203","v204","v205","v206","v207","v208","v209","v210","v211","v212","v213","v214","v215","v216","v217","v218","v219","v220","v221","v222","v223","v224","v225","v226","v227","v228","v229","v230","v231","v232","v233","v234","v235","v236","v237","v238","v239","v240","v241","v242","v243","v244","v245","v246","v247","v248","v249","v250","v251","v252","v253","v254","v255",
"a0","a1","a2","a3","a4","a5","a6","a7","a8","a9","a10","a11","a12","a13","a14","a15","a16","a17","a18","a19","a20","a21","a22","a23","a24","a25","a26","a27","a28","a29","a30","a31","a32","a33","a34","a35","a36","a37","a38","a39","a40","a41","a42","a43","a44","a45","a46","a47","a48","a49","a50","a51","a52","a53","a54","a55","a56","a57","a58","a59","a60","a61","a62","a63","a64","a65","a66","a67","a68","a69","a70","a71","a72","a73","a74","a75","a76","a77","a78","a79","a80","a81","a82","a83","a84","a85","a86","a87","a88","a89","a90","a91","a92","a93","a94","a95","a96","a97","a98","a99","a100","a101","a102","a103","a104","a105","a106","a107","a108","a109","a110","a111","a112","a113","a114","a115","a116","a117","a118","a119","a120","a121","a122","a123","a124","a125","a126","a127","a128","a129","a130","a131","a132","a133","a134","a135","a136","a137","a138","a139","a140","a141","a142","a143","a144","a145","a146","a147","a148","a149","a150","a151","a152","a153","a154","a155","a156","a157","a158","a159","a160","a161","a162","a163","a164","a165","a166","a167","a168","a169","a170","a171","a172","a173","a174","a175","a176","a177","a178","a179","a180","a181","a182","a183","a184","a185","a186","a187","a188","a189","a190","a191","a192","a193","a194","a195","a196","a197","a198","a199","a200","a201","a202","a203","a204","a205","a206","a207","a208","a209","a210","a211","a212","a213","a214","a215","a216","a217","a218","a219","a220","a221","a222","a223","a224","a225","a226","a227","a228","a229","a230","a231","a232","a233","a234","a235","a236","a237","a238","a239","a240","a241","a242","a243","a244","a245","a246","a247","a248","a249","a250","a251","a252","a253","a254","a255",
"s33","s34","s35","s36","s37","s38","s39","s40","s41","s42","s43","s44","s45","s46","s47","s48","s49","s50","s51","s52","s53","s54","s55","s56","s57","s58","s59","s60","s61","s62","s63","s64","s65","s66","s67","s68","s69","s70","s71","s72","s73","s74","s75","s76","s77","s78","s79","s80","s81","s82","s83","s84","s85","s86","s87","s88","s89","s90","s91","s92","s93","s94","s95","s96","s97","s98","s99");
}

extern "C" void kernel_launch(void* const* d_in, const int* in_sizes, int n_in,
                              void* d_out, int out_size, void* d_ws, size_t ws_size,
                              hipStream_t stream) {
  const unsigned short* x0 = (const unsigned short*)d_in[0];
  const unsigned short* ws = (const unsigned short*)d_in[1];
  const unsigned short* bs = (const unsigned short*)d_in[2];
  dcn_cross_kernel<<<256, 256, 0, stream>>>(x0, ws, bs, (unsigned char*)d_ws, (unsigned short*)d_out);
}
```

```cpp
#include <hip/hip_runtime.h>


constexpr int kD = 1024;
constexpr unsigned kWFlagOff = 131072, kWOff = 1u << 20, kWStream = 200 * 8192, kXchOff = 32u << 20, kDbgOff = 224u << 20;

__device__ __forceinline__ unsigned unit_src(unsigned v, unsigned h, unsigned w) {
  const unsigned l = v >> 5, u = v & 31;
  const unsigned split = ((0xff0000ffu >> u) & 1) - 1;
  const unsigned part = l ? u >> 4 : 0u, g = (0xff00ff00u >> u) & 1;
  const unsigned c = u >> (split & 1), jh = (u >> (~split & 2)) & 1;
  const unsigned kc = 8 * (h ^ part) + 2 * ((w + c) & 3) + g;
  return (l * 1024 + 64 * kc) * 2048 + jh * 128;
}

__global__ __launch_bounds__(256, 1)
void dcn_cross_kernel(const unsigned short* __restrict__ x0, const unsigned short* __restrict__ ws, const unsigned short* __restrict__ bs,
                      unsigned char* __restrict__ wsp, unsigned short* __restrict__ out) {
  __shared__ __attribute__((aligned(16))) unsigned char lds[163840];
  const unsigned b = blockIdx.x;
  constexpr unsigned kPx = 0 ? 1u : 8u;
  const unsigned h = 0 ? (b & 1) : ((b >> 3) & 1), rb = 0 ? (b >> 1) : ((b >> 4) * 8 + (b & 7));
  const unsigned w = __builtin_amdgcn_readfirstlane(threadIdx.x >> 6);
  const unsigned col = (512 * h + 128 * w) * 2;
  const unsigned long long wsb = (unsigned long long)wsp;
  const unsigned long long xo = (unsigned long long)(x0 + (size_t)rb * 128 * kD);
  const unsigned long long ob = (unsigned long long)(out + (size_t)rb * 128 * kD) + col;
  const unsigned long long wr = (unsigned long long)ws + col;
  const unsigned long long bb = (unsigned long long)bs;
  const unsigned wo = ((unsigned)wsb > ~(15u << 20) ? 16u << 20 : kWOff) + (h * 4 + w) * kWStream;
  const unsigned xb = kXchOff + ((0u - (unsigned)wsb) & 0x1fffffu);
  const unsigned eo = xb + b * 524288 + w * 32768, ep = xb + (b ^ kPx) * 524288 + w * 32768;
  const unsigned fo = b * 512 + w * 128, fp = (b ^ kPx) * 512 + w * 128;
  const unsigned db = kDbgOff + (b * 4 + w) * 512;
  const unsigned v1 = 24 + (rb >> 1), v2 = 64 + (rb >> 1), hk = rb & 1;
  const unsigned rs1 = v1 < 64 ? __builtin_amdgcn_readfirstlane(unit_src(v1, h, w)) + hk * 65536 : (1 - h) << 20, rd1 = v1 < 64 ? v1 * 8192 + hk * 1024 : 192 * 8192;
  const unsigned rs2 = __builtin_amdgcn_readfirstlane(unit_src(v2, h, w)) + hk * 65536, rn1 = v2 * 8192 + hk * 1024;
  const unsigned wfa = kWFlagOff + (4 * h + w) * 1024, wfo = wfa + rb * 8;
  const unsigned ldsa = (unsigned)(size_t)(__attribute__((address_space(3))) unsigned char*)lds;
#define LO(x) "s"((unsigned)(x))
#define HI(x) "s"((unsigned)((x) >> 32))
  asm volatile(
"L_main_entry:\n"
"v_and_b32 v12, 63, %18\n"
"v_lshrrev_b32 v13, 3, v12\n"
"v_and_b32 v15, 7, v12\n"
"v_lshlrev_b32 v10, 11, v13\n"
"s_andn2_b32 s60, 1024, %17\n"
"s_lshl_b32 s75, s60, 10\n"
"s_mov_b32 s52, %6\n"
"s_and_b32 s53, %7, 0xffff\n"
"s_mov_b32 s54, 12581120\n"
"s_mov_b32 s55, 0x20000\n"
"v_xor_b32 v6, v15, v13\n"
"v_lshl_add_u32 v6, v6, 4, v10\n"
"s_mov_b32 s48, %2\n"
"s_and_b32 s49, %3, 0xffff\n"
"s_mov_b32 s50, 65536\n"
"s_mov_b32 s51, 0x20000\n"
"s_lshl_b32 s66, %16, 16\n"
"s_add_u32 s48, s48, s66\n"
"s_addc_u32 s49, s49, 0\n"
"s_lshl_b32 s72, %16, 12\n"
"s_add_u32 s72, s72, %15\n"
"s_add_u32 m0, s72, 16384\n"
"s_add_u32 s67, s60, 0\n"
"buffer_load_dwordx4 v6, s[48:51], s67 offen offset:0  lds\n"
"s_add_u32 s67, s60, 15360\n"
"buffer_load_dwordx4 v6, s[48:51], s67 offen offset:1024  lds\n"
"s_add_u32 s67, s60, 30720\n"
"buffer_load_dwordx4 v6, s[48:51], s67 offen offset:2048  lds\n"
"s_add_u32 s67, s60, 46080\n"
"buffer_load_dwordx4 v6, s[48:51], s67 offen offset:3072  lds\n"
"s_mul_i32 s73, %16, 40960\n"
"s_add_u32 s64, s73, %15\n"
"v_lshrrev_b32 v11, 2, v12\n"
"v_and_b32 v11, 4, v11\n"
"v_xor_b32 v11, v11, v15\n"
"v_lshl_add_u32 v11, v11, 4, v10\n"
"s_add_u32 m0, s64, 0\n"
"s_add_u32 s67, s75, 0\n"
"buffer_load_dwordx4 v11, s[52:55], s67 offen offset:0 lds\n"
"s_add_u32 s67, s75, 15360\n"
"buffer_load_dwordx4 v11, s[52:55], s67 offen offset:1024 lds\n"
"s_add_u32 s67, s75, 30720\n"
"buffer_load_dwordx4 v11, s[52:55], s67 offen offset:2048 lds\n"
"s_add_u32 s67, s75, 46080\n"
"buffer_load_dwordx4 v11, s[52:55], s67 offen offset:3072 lds\n"
"s_add_u32 m0, s64, 4096\n"
"s_add_u32 s67, s75, 65536\n"
"buffer_load_dwordx4 v11, s[52:55], s67 offen offset:0 lds\n"
"s_add_u32 s67, s75, 80896\n"
"buffer_load_dwordx4 v11, s[52:55], s67 offen offset:1024 lds\n"
"s_add_u32 s67, s75, 96256\n"
"buffer_load_dwordx4 v11, s[52:55], s67 offen offset:2048 lds\n"
"s_add_u32 s67, s75, 111616\n"
"buffer_load_dwordx4 v11, s[52:55], s67 offen offset:3072 lds\n"
"s_mov_b32 s44, %8\n"
"s_and_b32 s45, %9, 0xffff\n"
"s_mov_b32 s46, 12288\n"
"s_mov_b32 s47, 0x20000\n"
"v_lshlrev_b32 v0, 4, v12\n"
"s_mov_b32 s61, %17\n"
"v_lshrrev_b32 v16, 4, v0\n"
"v_and_b32 v16, 0x30, v16\n"
"buffer_load_dwordx2 v[18:19], v16, s[44:47], s61 offen offset:0\n"
"v_lshrrev_b32 v20, 4, v0\n"
"v_and_b32 v20, 0x30, v20\n"
"buffer_load_dwordx2 v[22:23], v20, s[44:47], s61 offen offset:8\n"
"v_lshrrev_b32 v24, 4, v0\n"
"v_and_b32 v24, 0x30, v24\n"
"buffer_load_dwordx2 v[26:27], v24, s[44:47], s61 offen offset:64\n"
"v_lshrrev_b32 v28, 4, v0\n"
"v_and_b32 v28, 0x30, v28\n"
"buffer_load_dwordx2 v[30:31], v28, s[44:47], s61 offen offset:72\n"
"s_getpc_b64 s[96:97]\n"
"L_pc:\n"
"s_lshr_b32 s66, %26, 3\n"
"s_lshl_b32 s66, s66, 2\n"
"s_add_u32 s66, s66, %16\n"
"s_mul_i32 s66, s66, 640\n"
"s_and_b32 s97, s97, 0xffff\n"
"s_mov_b32 s98, L_end-L_pc\n"
"s_mov_b32 s99, 0x20000\n"
"s_min_u32 s66, s66, L_end-L_pc-640\n"
"v_min_u32 v9, 9, v12\n"
"v_lshlrev_b32 v9, 6, v9\n"
"buffer_load_dword a252, v9, s[96:99], s66 offen\n"
"s_lshr_b32 s65, %26, 4\n"
"s_lshl_b32 s65, s65, 2\n"
"s_add_u32 s65, s65, %16\n"
"s_lshl_b32 s65, s65, 11\n"
"s_lshl_b32 s67, %16, 8\n"
"s_sub_u32 s65, s65, s67\n"
"v_and_b32 v14, 1, v13\n"
"v_lshlrev_b32 v14, 17, v14\n"
"v_lshl_add_u32 v14, v15, 7, v14\n"
"s_add_u32 s70, s75, 131072\n"
"s_add_u32 s70, s70, s65\n"
"buffer_load_dword a252, v14, s[52:55], s70 offen\n"
"s_add_u32 m0, s64, 8192\n"
"s_add_u32 s67, s75, 128\n"
"buffer_load_dwordx4 v11, s[52:55], s67 offen offset:0 lds\n"
"s_add_u32 s67, s75, 15488\n"
"buffer_load_dwordx4 v11, s[52:55], s67 offen offset:1024 lds\n"
"s_add_u32 s67, s75, 30848\n"
"buffer_load_dwordx4 v11, s[52:55], s67 offen offset:2048 lds\n"
"s_add_u32 s67, s75, 46208\n"
"buffer_load_dwordx4 v11, s[52:55], s67 offen offset:3072 lds\n"
"s_add_u32 m0, s64, 12288\n"
"s_add_u32 s67, s75, 65664\n"
"buffer_load_dwordx4 v11, s[52:55], s67 offen offset:0 lds\n"
"s_add_u32 s67, s75, 81024\n"
"buffer_load_dwordx4 v11, s[52:55], s67 offen offset:1024 lds\n"
"s_add_u32 s67, s75, 96384\n"
"buffer_load_dwordx4 v11, s[52:55], s67 offen offset:2048 lds\n"
"s_add_u32 s67, s75, 111744\n"
"buffer_load_dwordx4 v11, s[52:55], s67 offen offset:3072 lds\n"
"s_add_u32 m0, s72, 57344\n"
"s_add_u32 s67, s60, 128\n"
"buffer_load_dwordx4 v6, s[48:51], s67 offen offset:0  lds\n"
"s_add_u32 s67, s60, 15488\n"
"buffer_load_dwordx4 v6, s[48:51], s67 offen offset:1024  lds\n"
"s_add_u32 s67, s60, 30848\n"
"buffer_load_dwordx4 v6, s[48:51], s67 offen offset:2048  lds\n"
"s_add_u32 s67, s60, 46208\n"
"buffer_load_dwordx4 v6, s[48:51], s67 offen offset:3072  lds\n"
"s_mov_b32 s56, %0\n"
"s_and_b32 s57, %1, 0xffff\n"
"s_mov_b32 s58, 171966464\n"
"s_mov_b32 s59, 0x20000\n"
"s_mov_b32 s67, 139264\n"
"buffer_load_dwordx2 v[252:253], off, s[56:59], s67 sc1\n"
"v_add_u32 v1, 4096, v0\n"
"s_add_u32 s36, s56, %10\n"
"s_addc_u32 s37, s57, 0\n"
"s_mov_b32 s38, 1572864\n"
"s_mov_b32 s39, 0x20000\n"
"s_add_u32 s34, s36, 200704\n"
"s_mov_b32 s35, s37\n"
"v_and_b32 v13, 15, v12\n"
"v_lshrrev_b32 v10, 4, v12\n"
"v_xor_b32 v15, v15, v10\n"
"v_lshlrev_b32 v7, 7, v13\n"
"v_lshl_add_u32 v7, v15, 4, v7\n"
"v_add_u32 v7, s64, v7\n"
"v_xor_b32 v8, 64, v7\n"
"v_bfe_u32 v13, v12, 2, 2\n"
"v_and_b32 v15, 3, v12\n"
"v_bfe_u32 v9, v12, 3, 1\n"
"v_lshl_add_u32 v15, v9, 2, v15\n"
"v_lshl_add_u32 v13, v10, 3, v13\n"
"v_lshlrev_b32 v13, 7, v13\n"
"v_lshl_add_u32 v9, v15, 4, v13\n"
"v_add_u32 v9, s64, v9\n"
"v_xor_b32 v15, 64, v9\n"
"s_mov_b32 s40, %2\n"
"s_and_b32 s41, %3, 0xffff\n"
"s_mov_b32 s42, 260352\n"
"s_mov_b32 s43, 0x20000\n"
"s_add_u32 s40, s40, %17\n"
"s_addc_u32 s41, s41, 0\n"
"s_mov_b32 s80, 0\n"
"s_mov_b32 s81, 15360\n"
"s_mov_b32 s82, 30720\n"
"s_mov_b32 s83, 46080\n"
"s_mov_b32 s84, 65536\n"
"s_mov_b32 s85, 80896\n"
"s_mov_b32 s86, 96256\n"
"s_mov_b32 s87, 111616\n"
"s_mov_b32 s88, 131072\n"
"s_mov_b32 s89, 146432\n"
"s_mov_b32 s90, 161792\n"
"s_mov_b32 s91, 177152\n"
"s_mov_b32 s92, 196608\n"
"s_mov_b32 s93, 211968\n"
"s_mov_b32 s94, 227328\n"
"s_mov_b32 s95, 242688\n"
"s_mov_b32 s62, 0\n"
"s_mov_b32 s63, 0xffff0000\n"
"s_mov_b32 s68, 0x8000\n"
"s_add_u32 s66, %16, 0\n"
"s_and_b32 s66, s66, 3\n"
"s_mul_i32 s66, s66, 40960\n"
"s_add_u32 s66, s66, %15\n"
"v_add_u32 v2, s66, v0\n"
"s_add_u32 s66, %16, 1\n"
"s_and_b32 s66, s66, 3\n"
"s_mul_i32 s66, s66, 40960\n"
"s_add_u32 s66, s66, %15\n"
"v_add_u32 v3, s66, v0\n"
"s_add_u32 s66, %16, 2\n"
"s_and_b32 s66, s66, 3\n"
"s_mul_i32 s66, s66, 40960\n"
"s_add_u32 s66, s66, %15\n"
"v_add_u32 v4, s66, v0\n"
"s_add_u32 s66, %16, 3\n"
"s_and_b32 s66, s66, 3\n"
"s_mul_i32 s66, s66, 40960\n"
"s_add_u32 s66, s66, %15\n"
"v_add_u32 v5, s66, v0\n"
"s_sub_u32 s66, 0, s73\n"
"v_add_u32 v12, s66, v7\n"
"v_add_u32 v13, s66, v8\n"
"s_waitcnt vmcnt(27)\n"
"s_barrier\n"
"ds_read_b128 a[100:103], v13 offset:16384\n"
"ds_read_b128 a[96:99], v12 offset:16384\n"
"ds_read_b128 a[108:111], v13 offset:18432\n"
"ds_read_b128 a[104:107], v12 offset:18432\n"
"ds_read_b128 a[116:119], v13 offset:20480\n"
"ds_read_b128 a[112:115], v12 offset:20480\n"
"ds_read_b128 a[124:127], v13 offset:22528\n"
"ds_read_b128 a[120:123], v12 offset:22528\n"
"ds_read_b128 a[132:135], v13 offset:24576\n"
"ds_read_b128 a[128:131], v12 offset:24576\n"
"ds_read_b128 a[140:143], v13 offset:26624\n"
"ds_read_b128 a[136:139], v12 offset:26624\n"
"ds_read_b128 a[148:151], v13 offset:28672\n"
"ds_read_b128 a[144:147], v12 offset:28672\n"
"ds_read_b128 a[156:159], v13 offset:30720\n"
"ds_read_b128 a[152:155], v12 offset:30720\n"
"s_waitcnt vmcnt(19)\n"
"ds_read_b64_tr_b16 a[32:33], v9 offset:0\n"
"ds_read_b64_tr_b16 a[34:35], v9 offset:512\n"
"ds_read_b64_tr_b16 a[36:37], v9 offset:4096\n"
"ds_read_b64_tr_b16 a[38:39], v9 offset:4608\n"
"ds_read_b64_tr_b16 a[40:41], v9 offset:8\n"
"ds_read_b64_tr_b16 a[42:43], v9 offset:520\n"
"ds_read_b64_tr_b16 a[44:45], v9 offset:4104\n"
"ds_read_b64_tr_b16 a[46:47], v9 offset:4616\n"
"ds_read_b64_tr_b16 a[48:49], v15 offset:0\n"
"ds_read_b64_tr_b16 a[50:51], v15 offset:512\n"
"ds_read_b64_tr_b16 a[52:53], v15 offset:4096\n"
"ds_read_b64_tr_b16 a[54:55], v15 offset:4608\n"
"ds_read_b64_tr_b16 a[56:57], v15 offset:8\n"
"ds_read_b64_tr_b16 a[58:59], v15 offset:520\n"
"ds_read_b64_tr_b16 a[60:61], v15 offset:4104\n"
"ds_read_b64_tr_b16 a[62:63], v15 offset:4616\n"
"s_waitcnt vmcnt(15)\n"
"v_lshlrev_b32 v16, 16, v18\n"
"v_and_b32 v17, s63, v18\n"
"v_lshlrev_b32 v18, 16, v19\n"
"v_and_b32 v19, s63, v19\n"
"v_add_f32 v16, 1.0, v16\n"
"v_add_f32 v17, 1.0, v17\n"
"v_add_f32 v18, 1.0, v18\n"
"v_add_f32 v19, 1.0, v19\n"
"v_lshlrev_b32 v20, 16, v22\n"
"v_and_b32 v21, s63, v22\n"
"v_lshlrev_b32 v22, 16, v23\n"
"v_and_b32 v23, s63, v23\n"
"v_add_f32 v20, 1.0, v20\n"
"v_add_f32 v21, 1.0, v21\n"
"v_add_f32 v22, 1.0, v22\n"
"v_add_f32 v23, 1.0, v23\n"
"v_lshlrev_b32 v24, 16, v26\n"
"v_and_b32 v25, s63, v26\n"
"v_lshlrev_b32 v26, 16, v27\n"
"v_and_b32 v27, s63, v27\n"
"v_add_f32 v24, 1.0, v24\n"
"v_add_f32 v25, 1.0, v25\n"
"v_add_f32 v26, 1.0, v26\n"
"v_add_f32 v27, 1.0, v27\n"
"v_lshlrev_b32 v28, 16, v30\n"
"v_and_b32 v29, s63, v30\n"
"v_lshlrev_b32 v30, 16, v31\n"
"v_and_b32 v31, s63, v31\n"
"v_add_f32 v28, 1.0, v28\n"
"v_add_f32 v29, 1.0, v29\n"
"v_add_f32 v30, 1.0, v30\n"
"v_add_f32 v31, 1.0, v31\n"
"v_accvgpr_write_b32 a0, v16\n"
"v_accvgpr_write_b32 a1, v17\n"
"v_accvgpr_write_b32 a2, v18\n"
"v_accvgpr_write_b32 a3, v19\n"
"v_accvgpr_write_b32 a4, v20\n"
"v_accvgpr_write_b32 a5, v21\n"
"v_accvgpr_write_b32 a6, v22\n"
"v_accvgpr_write_b32 a7, v23\n"
"v_accvgpr_write_b32 a8, v24\n"
"v_accvgpr_write_b32 a9, v25\n"
"v_accvgpr_write_b32 a10, v26\n"
"v_accvgpr_write_b32 a11, v27\n"
"v_accvgpr_write_b32 a12, v28\n"
"v_accvgpr_write_b32 a13, v29\n"
"v_accvgpr_write_b32 a14, v30\n"
"v_accvgpr_write_b32 a15, v31\n"
"s_nop 1\n"
"s_waitcnt lgkmcnt(8)\n"
"v_mfma_f32_16x16x32_bf16 v[32:35], a[32:35], a[96:99], v[16:19]\n"
"s_add_u32 s70, s75, 393216\n"
"v_mfma_f32_16x16x32_bf16 v[32:35], a[36:39], a[100:103], v[32:35]\n"
"s_add_u32 s70, s70, s65\n"
"v_mfma_f32_16x16x32_bf16 v[60:63], a[44:47], a[100:103], v[20:23]\n"
"buffer_load_dword a252, v14, s[52:55], s70 offen\n"
"v_mfma_f32_16x16x32_bf16 v[60:63], a[40:43], a[96:99], v[60:63]\n"
"s_add_u32 m0, s64, 32768\n"
"v_mfma_f32_16x16x32_bf16 v[64:67], a[40:43], a[104:107], v[20:23]\n"
"s_add_u32 s67, s75, 131072\n"
"v_mfma_f32_16x16x32_bf16 v[64:67], a[44:47], a[108:111], v[64:67]\n"
"buffer_load_dwordx4 v11, s[52:55], s67 offen offset:0 lds\n"
"v_mfma_f32_16x16x32_bf16 v[36:39], a[36:39], a[108:111], v[16:19]\n"
"s_add_u32 s67, s75, 146432\n"
"v_mfma_f32_16x16x32_bf16 v[36:39], a[32:35], a[104:107], v[36:39]\n"
"v_mfma_f32_16x16x32_bf16 v[40:43], a[32:35], a[112:115], v[16:19]\n"
"s_waitcnt lgkmcnt(0)\n"
"buffer_load_dwordx4 v11, s[52:55], s67 offen offset:1024 lds\n"
"v_mfma_f32_16x16x32_bf16 v[40:43], a[36:39], a[116:119], v[40:43]\n"
"v_mfma_f32_16x16x32_bf16 v[68:71], a[44:47], a[116:119], v[20:23]\n"
"s_add_u32 s67, s75, 161792\n"
"v_mfma_f32_16x16x32_bf16 v[68:71], a[40:43], a[112:115], v[68:71]\n"
"v_mfma_f32_16x16x32_bf16 v[72:75], a[40:43], a[120:123], v[20:23]\n"
"buffer_load_dwordx4 v11, s[52:55], s67 offen offset:2048 lds\n"
"v_mfma_f32_16x16x32_bf16 v[72:75], a[44:47], a[124:127], v[72:75]\n"
"v_mfma_f32_16x16x32_bf16 v[44:47], a[36:39], a[124:127], v[16:19]\n"
"s_add_u32 s67, s75, 177152\n"
"v_mfma_f32_16x16x32_bf16 v[44:47], a[32:35], a[120:123], v[44:47]\n"
"v_mfma_f32_16x16x32_bf16 v[48:51], a[32:35], a[128:131], v[16:19]\n"
"buffer_load_dwordx4 v11, s[52:55], s67 offen offset:3072 lds\n"
"v_mfma_f32_16x16x32_bf16 v[48:51], a[36:39], a[132:135], v[48:51]\n"
"v_mfma_f32_16x16x32_bf16 v[76:79], a[44:47], a[132:135], v[20:23]\n"
"s_add_u32 m0, s64, 36864\n"
"v_mfma_f32_16x16x32_bf16 v[76:79], a[40:43], a[128:131], v[76:79]\n"
"v_mfma_f32_16x16x32_bf16 v[80:83], a[40:43], a[136:139], v[20:23]\n"
"s_add_u32 s67, s75, 196608\n"
"v_mfma_f32_16x16x32_bf16 v[80:83], a[44:47], a[140:143], v[80:83]\n"
"v_mfma_f32_16x16x32_bf16 v[52:55], a[36:39], a[140:143], v[16:19]\n"
"buffer_load_dwordx4 v11, s[52:55], s67 offen offset:0 lds\n"
"v_mfma_f32_16x16x32_bf16 v[52:55], a[32:35], a[136:139], v[52:55]\n"
"v_mfma_f32_16x16x32_bf16 v[56:59], a[32:35], a[144:147], v[16:19]\n"
"s_add_u32 s67, s75, 211968\n"
"v_mfma_f32_16x16x32_bf16 v[56:59], a[36:39], a[148:151], v[56:59]\n"
"v_mfma_f32_16x16x32_bf16 v[84:87], a[44:47], a[148:151], v[20:23]\n"
"buffer_load_dwordx4 v11, s[52:55], s67 offen offset:1024 lds\n"
"v_mfma_f32_16x16x32_bf16 v[84:87], a[40:43], a[144:147], v[84:87]\n"
"v_mfma_f32_16x16x32_bf16 a[4:7], a[40:43], a[152:155], a[4:7]\n"
"s_add_u32 s67, s75, 227328\n"
"v_mfma_f32_16x16x32_bf16 a[4:7], a[44:47], a[156:159], a[4:7]\n"
"v_mfma_f32_16x16x32_bf16 a[0:3], a[36:39], a[156:159], a[0:3]\n"
"buffer_load_dwordx4 v11, s[52:55], s67 offen offset:2048 lds\n"
"v_mfma_f32_16x16x32_bf16 a[0:3], a[32:35], a[152:155], a[0:3]\n"
"v_mfma_f32_16x16x32_bf16 v[88:91], a[48:51], a[96:99], v[24:27]\n"
"s_add_u32 s67, s75, 242688\n"
"v_mfma_f32_16x16x32_bf16 v[88:91], a[52:55], a[100:103], v[88:91]\n"
"v_mfma_f32_16x16x32_bf16 v[116:119], a[60:63], a[100:103], v[28:31]\n"
"v_mfma_f32_16x16x32_bf16 v[116:119], a[56:59], a[96:99], v[116:119]\n"
"buffer_load_dwordx4 v11, s[52:55], s67 offen offset:3072 lds\n"
"v_mfma_f32_16x16x32_bf16 v[120:123], a[56:59], a[104:107], v[28:31]\n"
"s_add_u32 m0, s64, 0\n"
"v_mfma_f32_16x16x32_bf16 v[120:123], a[60:63], a[108:111], v[120:123]\n"
"v_mfma_f32_16x16x32_bf16 v[92:95], a[52:55], a[108:111], v[24:27]\n"
"s_add_u32 s67, s75, 131200\n"
"v_mfma_f32_16x16x32_bf16 v[92:95], a[48:51], a[104:107], v[92:95]\n"
"v_mfma_f32_16x16x32_bf16 v[96:99], a[48:51], a[112:115], v[24:27]\n"
"buffer_load_dwordx4 v11, s[52:55], s67 offen offset:0 lds\n"
"v_mfma_f32_16x16x32_bf16 v[96:99], a[52:55], a[116:119], v[96:99]\n"
"v_mfma_f32_16x16x32_bf16 v[124:127], a[60:63], a[116:119], v[28:31]\n"
"s_add_u32 s67, s75, 146560\n"
"v_mfma_f32_16x16x32_bf16 v[124:127], a[56:59], a[112:115], v[124:127]\n"
"buffer_load_dwordx4 v11, s[52:55], s67 offen offset:1024 lds\n"
"v_mfma_f32_16x16x32_bf16 v[128:131], a[56:59], a[120:123], v[28:31]\n"
"s_waitcnt vmcnt(16)\n"
"ds_read_b64_tr_b16 a[64:65], v9 offset:8192\n"
"v_mfma_f32_16x16x32_bf16 v[128:131], a[60:63], a[124:127], v[128:131]\n"
"s_add_u32 s67, s75, 161920\n"
"ds_read_b64_tr_b16 a[66:67], v9 offset:8704\n"
"v_mfma_f32_16x16x32_bf16 v[100:103], a[52:55], a[124:127], v[24:27]\n"
"ds_read_b64_tr_b16 a[68:69], v9 offset:12288\n"
"v_mfma_f32_16x16x32_bf16 v[100:103], a[48:51], a[120:123], v[100:103]\n"
"ds_read_b64_tr_b16 a[70:71], v9 offset:12800\n"
"v_mfma_f32_16x16x32_bf16 v[104:107], a[48:51], a[128:131], v[24:27]\n"
"ds_read_b64_tr_b16 a[72:73], v9 offset:8200\n"
"v_mfma_f32_16x16x32_bf16 v[104:107], a[52:55], a[132:135], v[104:107]\n"
"ds_read_b64_tr_b16 a[74:75], v9 offset:8712\n"
"buffer_load_dwordx4 v11, s[52:55], s67 offen offset:2048 lds\n"
"v_mfma_f32_16x16x32_bf16 v[132:135], a[60:63], a[132:135], v[28:31]\n"
"ds_read_b64_tr_b16 a[76:77], v9 offset:12296\n"
"v_mfma_f32_16x16x32_bf16 v[132:135], a[56:59], a[128:131], v[132:135]\n"
"s_add_u32 s67, s75, 177280\n"
"ds_read_b64_tr_b16 a[78:79], v9 offset:12808\n"
"v_mfma_f32_16x16x32_bf16 v[136:139], a[56:59], a[136:139], v[28:31]\n"
"buffer_load_dwordx4 v11, s[52:55], s67 offen offset:3072 lds\n"
"v_mfma_f32_16x16x32_bf16 v[136:139], a[60:63], a[140:143], v[136:139]\n"
"ds_read_b64_tr_b16 a[80:81], v15 offset:8192\n"
"v_mfma_f32_16x16x32_bf16 v[108:111], a[52:55], a[140:143], v[24:27]\n"
"s_add_u32 m0, s64, 4096\n"
"ds_read_b64_tr_b16 a[82:83], v15 offset:8704\n"
"v_mfma_f32_16x16x32_bf16 v[108:111], a[48:51], a[136:139], v[108:111]\n"
"ds_read_b64_tr_b16 a[84:85], v15 offset:12288\n"
"v_mfma_f32_16x16x32_bf16 v[112:115], a[48:51], a[144:147], v[24:27]\n"
"s_add_u32 s67, s75, 196736\n"
"ds_read_b64_tr_b16 a[86:87], v15 offset:12800\n"
"v_mfma_f32_16x16x32_bf16 v[112:115], a[52:55], a[148:151], v[112:115]\n"
"ds_read_b64_tr_b16 a[88:89], v15 offset:8200\n"
"v_mfma_f32_16x16x32_bf16 v[140:143], a[60:63], a[148:151], v[28:31]\n"
"ds_read_b64_tr_b16 a[90:91], v15 offset:8712\n"
"buffer_load_dwordx4 v11, s[52:55], s67 offen offset:0 lds\n"
"v_mfma_f32_16x16x32_bf16 v[140:143], a[56:59], a[144:147], v[140:143]\n"
"ds_read_b64_tr_b16 a[92:93], v15 offset:12296\n"
"v_mfma_f32_16x16x32_bf16 a[12:15], a[56:59], a[152:155], a[12:15]\n"
"s_add_u32 s67, s75, 212096\n"
"ds_read_b64_tr_b16 a[94:95], v15 offset:12808\n"
"v_mfma_f32_16x16x32_bf16 a[12:15], a[60:63], a[156:159], a[12:15]\n"
"v_mfma_f32_16x16x32_bf16 a[8:11], a[52:55], a[156:159], a[8:11]\n"
"buffer_load_dwordx4 v11, s[52:55], s67 offen offset:1024 lds\n"
"v_mfma_f32_16x16x32_bf16 a[8:11], a[48:51], a[152:155], a[8:11]\n"
"s_waitcnt lgkmcnt(8)\n"
"v_mfma_f32_16x16x32_bf16 v[144:147], a[64:67], a[96:99], 1.0\n"
"s_add_u32 s67, s75, 227456\n"
"v_mfma_f32_16x16x32_bf16 v[144:147], a[68:71], a[100:103], v[144:147]\n"
"v_mfma_f32_16x16x32_bf16 v[172:175], a[76:79], a[100:103], 1.0\n"
"s_waitcnt vmcnt(15) lgkmcnt(0)\n"
"v_readfirstlane_b32 s78, v252\n"
"v_readfirstlane_b32 s79, v253\n"
"v_mfma_f32_16x16x32_bf16 v[172:175], a[72:75], a[96:99], v[172:175]\n"
"buffer_load_dwordx4 v11, s[52:55], s67 offen offset:2048 lds\n"
"v_mfma_f32_16x16x32_bf16 v[176:179], a[72:75], a[104:107], 1.0\n"
"v_mfma_f32_16x16x32_bf16 v[176:179], a[76:79], a[108:111], v[176:179]\n"
"s_add_u32 s67, s75, 242816\n"
"v_mfma_f32_16x16x32_bf16 v[148:151], a[68:71], a[108:111], 1.0\n"
"s_add_u32 s78, s78, 16\n"
"v_mfma_f32_16x16x32_bf16 v[148:151], a[64:67], a[104:107], v[148:151]\n"
"s_add_u32 s79, s79, 16\n"
"v_mfma_f32_16x16x32_bf16 v[152:155], a[64:67], a[112:115], 1.0\n"
"s_sub_u32 s66, 40960, s73\n"
"v_mfma_f32_16x16x32_bf16 v[152:155], a[68:71], a[116:119], v[152:155]\n"
"buffer_load_dwordx4 v11, s[52:55], s67 offen offset:3072 lds\n"
"v_mfma_f32_16x16x32_bf16 v[180:183], a[76:79], a[116:119], 1.0\n"
"s_add_u32 m0, s72, 98304\n"
"v_add_u32 v12, s66, v7\n"
"v_mfma_f32_16x16x32_bf16 v[180:183], a[72:75], a[112:115], v[180:183]\n"
"v_mfma_f32_16x16x32_bf16 v[184:187], a[72:75], a[120:123], 1.0\n"
"s_add_u32 s67, s60, 256\n"
"v_add_u32 v13, s66, v8\n"
"v_mfma_f32_16x16x32_bf16 v[184:187], a[76:79], a[124:127], v[184:187]\n"
"buffer_load_dwordx4 v6, s[48:51], s67 offen offset:0  lds\n"
"v_mfma_f32_16x16x32_bf16 v[156:159], a[68:71], a[124:127], 1.0\n"
"v_mfma_f32_16x16x32_bf16 v[156:159], a[64:67], a[120:123], v[156:159]\n"
"s_add_u32 s67, s60, 15616\n"
"v_mfma_f32_16x16x32_bf16 v[160:163], a[64:67], a[128:131], 1.0\n"
"v_mfma_f32_16x16x32_bf16 v[160:163], a[68:71], a[132:135], v[160:163]\n"
"buffer_load_dwordx4 v6, s[48:51], s67 offen offset:1024  lds\n"
"v_mfma_f32_16x16x32_bf16 v[188:191], a[76:79], a[132:135], 1.0\n"
"v_mfma_f32_16x16x32_bf16 v[188:191], a[72:75], a[128:131], v[188:191]\n"
"s_add_u32 s67, s60, 30976\n"
"v_mfma_f32_16x16x32_bf16 v[192:195], a[72:75], a[136:139], 1.0\n"
"v_mfma_f32_16x16x32_bf16 v[192:195], a[76:79], a[140:143], v[192:195]\n"
"buffer_load_dwordx4 v6, s[48:51], s67 offen offset:2048  lds\n"
"v_mfma_f32_16x16x32_bf16 v[164:167], a[68:71], a[140:143], 1.0\n"
"v_mfma_f32_16x16x32_bf16 v[164:167], a[64:67], a[136:139], v[164:167]\n"
"s_add_u32 s67, s60, 46336\n"
"v_mfma_f32_16x16x32_bf16 v[168:171], a[64:67], a[144:147], 1.0\n"
"v_mfma_f32_16x16x32_bf16 v[168:171], a[68:71], a[148:151], v[168:171]\n"
"v_mfma_f32_16x16x32_bf16 v[196:199], a[76:79], a[148:151], 1.0\n"
"buffer_load_dwordx4 v6, s[48:51], s67 offen offset:3072  lds\n"
"v_mfma_f32_16x16x32_bf16 v[196:199], a[72:75], a[144:147], v[196:199]\n"
"s_add_u32 m0, s72, 139264\n"
"v_mfma_f32_16x16x32_bf16 a[20:23], a[72:75], a[152:155], 1.0\n"
"v_mfma_f32_16x16x32_bf16 a[20:23], a[76:79], a[156:159], a[20:23]\n"
"s_add_u32 s67, %20, 0\n"
"v_mfma_f32_16x16x32_bf16 a[16:19], a[68:71], a[156:159], 1.0\n"
"buffer_load_dwordx4 v11, s[52:55], s67 offen offset:0 nt lds\n"
"v_mfma_f32_16x16x32_bf16 a[16:19], a[64:67], a[152:155], a[16:19]\n"
"s_barrier\n"
"s_waitcnt vmcnt(13)\n"
"ds_read_b64_tr_b16 a[32:33], v9 offset:32768\n"
"v_mfma_f32_16x16x32_bf16 v[200:203], a[80:83], a[96:99], 1.0\n"
"s_add_u32 s67, %20, 15360\n"
"ds_read_b64_tr_b16 a[34:35], v9 offset:33280\n"
"v_mfma_f32_16x16x32_bf16 v[200:203], a[84:87], a[100:103], v[200:203]\n"
"ds_read_b64_tr_b16 a[36:37], v9 offset:36864\n"
"v_mfma_f32_16x16x32_bf16 v[228:231], a[92:95], a[100:103], 1.0\n"
"ds_read_b128 a[100:103], v13 offset:16384\n"
"v_mfma_f32_16x16x32_bf16 v[228:231], a[88:91], a[96:99], v[228:231]\n"
"ds_read_b128 a[96:99], v12 offset:16384\n"
"buffer_load_dwordx4 v11, s[52:55], s67 offen offset:1024 nt lds\n"
"v_mfma_f32_16x16x32_bf16 v[232:235], a[88:91], a[104:107], 1.0\n"
"ds_read_b64_tr_b16 a[38:39], v9 offset:37376\n"
"v_mfma_f32_16x16x32_bf16 v[232:235], a[92:95], a[108:111], v[232:235]\n"
"s_add_u32 s67, %20, 30720\n"
"ds_read_b64_tr_b16 a[40:41], v9 offset:32776\n"
"v_mfma_f32_16x16x32_bf16 v[204:207], a[84:87], a[108:111], 1.0\n"
"ds_read_b128 a[108:111], v13 offset:18432\n"
"v_mfma_f32_16x16x32_bf16 v[204:207], a[80:83], a[104:107], v[204:207]\n"
"ds_read_b128 a[104:107], v12 offset:18432\n"
"v_mfma_f32_16x16x32_bf16 v[208:211], a[80:83], a[112:115], 1.0\n"
"ds_read_b64_tr_b16 a[42:43], v9 offset:33288\n"
"buffer_load_dwordx4 v11, s[52:55], s67 offen offset:2048 nt lds\n"
"v_mfma_f32_16x16x32_bf16 v[208:211], a[84:87], a[116:119], v[208:211]\n"
"ds_read_b64_tr_b16 a[44:45], v9 offset:36872\n"
"v_mfma_f32_16x16x32_bf16 v[236:239], a[92:95], a[116:119], 1.0\n"
"s_add_u32 s67, %20, 46080\n"
"ds_read_b128 a[116:119], v13 offset:20480\n"
"v_mfma_f32_16x16x32_bf16 v[236:239], a[88:91], a[112:115], v[236:239]\n"
"ds_read_b128 a[112:115], v12 offset:20480\n"
"v_mfma_f32_16x16x32_bf16 v[240:243], a[88:91], a[120:123], 1.0\n"
"ds_read_b64_tr_b16 a[46:47], v9 offset:37384\n"
"buffer_load_dwordx4 v11, s[52:55], s67 offen offset:3072 nt lds\n"
"v_mfma_f32_16x16x32_bf16 v[240:243], a[92:95], a[124:127], v[240:243]\n"
"ds_read_b64_tr_b16 a[48:49], v15 offset:32768\n"
"v_mfma_f32_16x16x32_bf16 v[212:215], a[84:87], a[124:127], 1.0\n"
"ds_read_b128 a[124:127], v13 offset:22528\n"
"v_mfma_f32_16x16x32_bf16 v[212:215], a[80:83], a[120:123], v[212:215]\n"
"ds_read_b128 a[120:123], v12 offset:22528\n"
"v_mfma_f32_16x16x32_bf16 v[216:219], a[80:83], a[128:131], 1.0\n"
"s_add_u32 m0, s64, 8192\n"
"ds_read_b64_tr_b16 a[50:51], v15 offset:33280\n"
"v_mfma_f32_16x16x32_bf16 v[216:219], a[84:87], a[132:135], v[216:219]\n"
"ds_read_b64_tr_b16 a[52:53], v15 offset:36864\n"
"v_mfma_f32_16x16x32_bf16 v[244:247], a[92:95], a[132:135], 1.0\n"
"ds_read_b128 a[132:135], v13 offset:24576\n"
"v_mfma_f32_16x16x32_bf16 v[244:247], a[88:91], a[128:131], v[244:247]\n"
"ds_read_b128 a[128:131], v12 offset:24576\n"
"v_mfma_f32_16x16x32_bf16 v[248:251], a[88:91], a[136:139], 1.0\n"
"ds_read_b64_tr_b16 a[54:55], v15 offset:37376\n"
"v_mfma_f32_16x16x32_bf16 v[248:251], a[92:95], a[140:143], v[248:251]\n"
"ds_read_b64_tr_b16 a[56:57], v15 offset:32776\n"
"v_mfma_f32_16x16x32_bf16 v[220:223], a[84:87], a[140:143], 1.0\n"
"ds_read_b128 a[140:143], v13 offset:26624\n"
"v_mfma_f32_16x16x32_bf16 v[220:223], a[80:83], a[136:139], v[220:223]\n"
"s_add_u32 s67, s75, 262144\n"
"ds_read_b128 a[136:139], v12 offset:26624\n"
"v_mfma_f32_16x16x32_bf16 v[224:227], a[80:83], a[144:147], 1.0\n"
"ds_read_b64_tr_b16 a[58:59], v15 offset:33288\n"
"v_mfma_f32_16x16x32_bf16 v[224:227], a[84:87], a[148:151], v[224:227]\n"
"ds_read_b64_tr_b16 a[60:61], v15 offset:36872\n"
"v_mfma_f32_16x16x32_bf16 v[252:255], a[92:95], a[148:151], 1.0\n"
"ds_read_b128 a[148:151], v13 offset:28672\n"
"v_mfma_f32_16x16x32_bf16 v[252:255], a[88:91], a[144:147], v[252:255]\n"
"ds_read_b128 a[144:147], v12 offset:28672\n"
"ds_read_b64_tr_b16 a[62:63], v15 offset:37384\n"
"v_mfma_f32_16x16x32_bf16 a[28:31], a[88:91], a[152:155], 1.0\n"
"v_mfma_f32_16x16x32_bf16 a[28:31], a[92:95], a[156:159], a[28:31]\n"
"v_mfma_f32_16x16x32_bf16 a[24:27], a[84:87], a[156:159], 1.0\n"
"ds_read_b128 a[156:159], v13 offset:30720\n"
"v_mfma_f32_16x16x32_bf16 a[24:27], a[80:83], a[152:155], a[24:27]\n"
"s_waitcnt lgkmcnt(15)\n"
"ds_read_b128 a[152:155], v12 offset:30720\n"
"v_mfma_f32_16x16x32_bf16 v[32:35], a[32:35], a[96:99], v[32:35]\n"
"buffer_load_dwordx4 v11, s[52:55], s67 offen offset:0 lds\n"
"v_mfma_f32_16x16x32_bf16 v[32:35], a[36:39], a[100:103], v[32:35]\n"
"s_waitcnt lgkmcnt(2)\n"
"v_mfma_f32_16x16x32_bf16 v[60:63], a[44:47], a[100:103], v[60:63]\n"
"s_add_u32 s67, s75, 277504\n"
"v_mfma_f32_16x16x32_bf16 v[60:63], a[40:43], a[96:99], v[60:63]\n"
"v_mfma_f32_16x16x32_bf16 v[64:67], a[40:43], a[104:107], v[64:67]\n"
"buffer_load_dwordx4 v11, s[52:55], s67 offen offset:1024 lds\n"
"v_mfma_f32_16x16x32_bf16 v[64:67], a[44:47], a[108:111], v[64:67]\n"
"v_mfma_f32_16x16x32_bf16 v[36:39], a[36:39], a[108:111], v[36:39]\n"
"s_add_u32 s67, s75, 292864\n"
"v_mfma_f32_16x16x32_bf16 v[36:39], a[32:35], a[104:107], v[36:39]\n"
"v_mfma_f32_16x16x32_bf16 v[40:43], a[32:35], a[112:115], v[40:43]\n"
"v_mfma_f32_16x16x32_bf16 v[40:43], a[36:39], a[116:119], v[40:43]\n"
"buffer_load_dwordx4 v11, s[52:55], s67 offen offset:2048 lds\n"
"v_mfma_f32_16x16x32_bf16 v[68:71], a[44:47], a[116:119], v[68:71]\n"
"v_mfma_f32_16x16x32_bf16 v[68:71], a[40:43], a[112:115], v[68:71]\n"
"s_add_u32 s67, s75, 308224\n"
"v_mfma_f32_16x16x32_bf16 v[72:75], a[40:43], a[120:123], v[72:75]\n"
"v_mfma_f32_16x16x32_bf16 v[72:75], a[44:47], a[124:127], v[72:75]\n"
"v_mfma_f32_16x16x32_bf16 v[44:47], a[36:39], a[124:127], v[44:47]\n"
"v_mov_b32 v16, -1\n"
"v_mfma_f32_16x16x32_bf16 v[44:47], a[32:35], a[120:123], v[44:47]\n"
"buffer_load_dwordx4 v11, s[52:55], s67 offen offset:3072 lds\n"
"v_mfma_f32_16x16x32_bf16 v[48:51], a[32:35], a[128:131], v[48:51]\n"
"s_add_u32 m0, s64, 12288\n"
"v_mfma_f32_16x16x32_bf16 v[48:51], a[36:39], a[132:135], v[48:51]\n"
"s_add_u32 s67, s75, 327680\n"
"v_lshrrev_b32 v17, 2, v0\n"
"v_mfma_f32_16x16x32_bf16 v[76:79], a[44:47], a[132:135], v[76:79]\n"
"s_waitcnt vmcnt(12)\n"
"ds_read_b64_tr_b16 a[64:65], v9 offset:0\n"
"v_mfma_f32_16x16x32_bf16 v[76:79], a[40:43], a[128:131], v[76:79]\n"
"ds_read_b64_tr_b16 a[66:67], v9 offset:512\n"
"buffer_load_dwordx4 v11, s[52:55], s67 offen offset:0 lds\n"
"v_mfma_f32_16x16x32_bf16 v[80:83], a[40:43], a[136:139], v[80:83]\n"
"ds_read_b64_tr_b16 a[68:69], v9 offset:4096\n"
"v_mfma_f32_16x16x32_bf16 v[80:83], a[44:47], a[140:143], v[80:83]\n"
"s_add_u32 s67, s75, 343040\n"
"ds_read_b64_tr_b16 a[70:71], v9 offset:4608\n"
"v_mfma_f32_16x16x32_bf16 v[52:55], a[36:39], a[140:143], v[52:55]\n"
"ds_read_b64_tr_b16 a[72:73], v9 offset:8\n"
"v_mfma_f32_16x16x32_bf16 v[52:55], a[32:35], a[136:139], v[52:55]\n"
"s_waitcnt lgkmcnt(5)\n"
"ds_read_b64_tr_b16 a[74:75], v9 offset:520\n"
"buffer_load_dwordx4 v11, s[52:55], s67 offen offset:1024 lds\n"
"v_mfma_f32_16x16x32_bf16 v[56:59], a[32:35], a[144:147], v[56:59]\n"
"s_add_u32 s67, s75, 358400\n"
"ds_read_b64_tr_b16 a[76:77], v9 offset:4104\n"
"v_mfma_f32_16x16x32_bf16 v[56:59], a[36:39], a[148:151], v[56:59]\n"
"s_add_u32 s69, %11, 8192\n"
"ds_read_b64_tr_b16 a[78:79], v9 offset:4616\n"
"v_mfma_f32_16x16x32_bf16 v[84:87], a[44:47], a[148:151], v[84:87]\n"
"buffer_load_dwordx4 v11, s[52:55], s67 offen offset:2048 lds\n"
"v_mfma_f32_16x16x32_bf16 v[84:87], a[40:43], a[144:147], v[84:87]\n"
"ds_read_b64_tr_b16 a[80:81], v15 offset:0\n"
"v_mfma_f32_16x16x32_bf16 a[4:7], a[40:43], a[152:155], a[4:7]\n"
"s_add_u32 s67, s75, 373760\n"
"ds_read_b64_tr_b16 a[82:83], v15 offset:512\n"
"v_mfma_f32_16x16x32_bf16 a[4:7], a[44:47], a[156:159], a[4:7]\n"
"ds_read_b64_tr_b16 a[84:85], v15 offset:4096\n"
"v_mfma_f32_16x16x32_bf16 a[0:3], a[36:39], a[156:159], a[0:3]\n"
"ds_read_b64_tr_b16 a[86:87], v15 offset:4608\n"
"v_mfma_f32_16x16x32_bf16 a[0:3], a[32:35], a[152:155], a[0:3]\n"
"ds_read_b64_tr_b16 a[88:89], v15 offset:8\n"
"v_mfma_f32_16x16x32_bf16 v[88:91], a[48:51], a[96:99], v[88:91]\n"
"ds_read_b64_tr_b16 a[90:91], v15 offset:520\n"
"buffer_load_dwordx4 v11, s[52:55], s67 offen offset:3072 lds\n"
"v_mfma_f32_16x16x32_bf16 v[88:91], a[52:55], a[100:103], v[88:91]\n"
"ds_read_b64_tr_b16 a[92:93], v15 offset:4104\n"
"v_mfma_f32_16x16x32_bf16 v[116:119], a[60:63], a[100:103], v[116:119]\n"
"s_add_u32 m0, s64, 0\n"
"ds_read_b64_tr_b16 a[94:95], v15 offset:4616\n"
"v_mfma_f32_16x16x32_bf16 v[116:119], a[56:59], a[96:99], v[116:119]\n"
"v_mfma_f32_16x16x32_bf16 v[120:123], a[56:59], a[104:107], v[120:123]\n"
"v_mfma_f32_16x16x32_bf16 v[120:123], a[60:63], a[108:111], v[120:123]\n"
"v_mfma_f32_16x16x32_bf16 v[92:95], a[52:55], a[108:111], v[92:95]\n"
"s_add_u32 s67, s75, 262272\n"
"v_mfma_f32_16x16x32_bf16 v[92:95], a[48:51], a[104:107], v[92:95]\n"
"v_mfma_f32_16x16x32_bf16 v[96:99], a[48:51], a[112:115], v[96:99]\n"
"s_waitcnt lgkmcnt(0)\n"
"v_mfma_f32_16x16x32_bf16 v[96:99], a[52:55], a[116:119], v[96:99]\n"
"buffer_load_dwordx4 v11, s[52:55], s67 offen offset:0 lds\n"
"v_mfma_f32_16x16x32_bf16 v[124:127], a[60:63], a[116:119], v[124:127]\n"
"v_mfma_f32_16x16x32_bf16 v[124:127], a[56:59], a[112:115], v[124:127]\n"
"s_add_u32 s67, s75, 277632\n"
"v_mfma_f32_16x16x32_bf16 v[128:131], a[56:59], a[120:123], v[128:131]\n"
"v_mfma_f32_16x16x32_bf16 v[128:131], a[60:63], a[124:127], v[128:131]\n"
"buffer_load_dwordx4 v11, s[52:55], s67 offen offset:1024 lds\n"
"v_mfma_f32_16x16x32_bf16 v[100:103], a[52:55], a[124:127], v[100:103]\n"
"v_mfma_f32_16x16x32_bf16 v[100:103], a[48:51], a[120:123], v[100:103]\n"
"s_add_u32 s67, s75, 292992\n"
"v_mfma_f32_16x16x32_bf16 v[104:107], a[48:51], a[128:131], v[104:107]\n"
"v_mfma_f32_16x16x32_bf16 v[104:107], a[52:55], a[132:135], v[104:107]\n"
"v_mfma_f32_16x16x32_bf16 v[132:135], a[60:63], a[132:135], v[132:135]\n"
"buffer_load_dwordx4 v11, s[52:55], s67 offen offset:2048 lds\n"
"v_mfma_f32_16x16x32_bf16 v[132:135], a[56:59], a[128:131], v[132:135]\n"
"v_mfma_f32_16x16x32_bf16 v[136:139], a[56:59], a[136:139], v[136:139]\n"
"s_add_u32 s67, s75, 308352\n"
"v_mfma_f32_16x16x32_bf16 v[136:139], a[60:63], a[140:143], v[136:139]\n"
"v_mfma_f32_16x16x32_bf16 v[108:111], a[52:55], a[140:143], v[108:111]\n"
"buffer_load_dwordx4 v11, s[52:55], s67 offen offset:3072 lds\n"
"v_mfma_f32_16x16x32_bf16 v[108:111], a[48:51], a[136:139], v[108:111]\n"
"v_mfma_f32_16x16x32_bf16 v[112:115], a[48:51], a[144:147], v[112:115]\n"
"s_add_u32 m0, s64, 4096\n"
"v_mfma_f32_16x16x32_bf16 v[112:115], a[52:55], a[148:151], v[112:115]\n"
"s_sub_u32 s71, s72, s64\n"
"v_mfma_f32_16x16x32_bf16 v[140:143], a[60:63], a[148:151], v[140:143]\n"
"s_add_u32 s67, s75, 327808\n"
"v_mfma_f32_16x16x32_bf16 v[140:143], a[56:59], a[144:147], v[140:143]\n"
"buffer_load_dwordx4 v11, s[52:55], s67 offen offset:0 lds\n"
"v_mfma_f32_16x16x32_bf16 a[12:15], a[56:59], a[152:155], a[12:15]\n"
"s_add_u32 s71, s71, 139264\n"
"v_mfma_f32_16x16x32_bf16 a[12:15], a[60:63], a[156:159], a[12:15]\n"
"s_add_u32 s67, s75, 343168\n"
"v_mfma_f32_16x16x32_bf16 a[8:11], a[52:55], a[156:159], a[8:11]\n"
"v_add_u32 v20, s71, v9\n"
"v_mfma_f32_16x16x32_bf16 a[8:11], a[48:51], a[152:155], a[8:11]\n"
"v_mfma_f32_16x16x32_bf16 v[144:147], a[64:67], a[96:99], v[144:147]\n"
"buffer_load_dwordx4 v11, s[52:55], s67 offen offset:1024 lds\n"
"v_mfma_f32_16x16x32_bf16 v[144:147], a[68:71], a[100:103], v[144:147]\n"
"v_add_u32 v21, s71, v15\n"
"v_mfma_f32_16x16x32_bf16 v[172:175], a[76:79], a[100:103], v[172:175]\n"
"s_add_u32 s67, s75, 358528\n"
"v_mfma_f32_16x16x32_bf16 v[172:175], a[72:75], a[96:99], v[172:175]\n"
"v_mfma_f32_16x16x32_bf16 v[176:179], a[72:75], a[104:107], v[176:179]\n"
"s_waitcnt vmcnt(14)\n"
"ds_read_b64_tr_b16 a[160:161], v20 offset:0\n"
"v_mfma_f32_16x16x32_bf16 v[176:179], a[76:79], a[108:111], v[176:179]\n"
"buffer_load_dwordx4 v11, s[52:55], s67 offen offset:2048 lds\n"
"v_mfma_f32_16x16x32_bf16 v[148:151], a[68:71], a[108:111], v[148:151]\n"
"ds_read_b64_tr_b16 a[162:163], v20 offset:512\n"
"v_mfma_f32_16x16x32_bf16 v[148:151], a[64:67], a[104:107], v[148:151]\n"
"s_add_u32 s67, s75, 373888\n"
"ds_read_b64_tr_b16 a[164:165], v20 offset:8\n"
"v_mfma_f32_16x16x32_bf16 v[152:155], a[64:67], a[112:115], v[152:155]\n"
"s_sub_u32 s66, 81920, s73\n"
"ds_read_b64_tr_b16 a[166:167], v20 offset:520\n"
"v_mfma_f32_16x16x32_bf16 v[152:155], a[68:71], a[116:119], v[152:155]\n"
"ds_read_b64_tr_b16 a[168:169], v21 offset:0\n"
"buffer_load_dwordx4 v11, s[52:55], s67 offen offset:3072 lds\n"
"v_mfma_f32_16x16x32_bf16 v[180:183], a[76:79], a[116:119], v[180:183]\n"
"v_add_u32 v12, s66, v7\n"
"v_mfma_f32_16x16x32_bf16 v[180:183], a[72:75], a[112:115], v[180:183]\n"
"ds_read_b64_tr_b16 a[170:171], v21 offset:512\n"
"v_mfma_f32_16x16x32_bf16 v[184:187], a[72:75], a[120:123], v[184:187]\n"
"s_add_u32 m0, s72, 139264\n"
"v_add_u32 v13, s66, v8\n"
"v_mfma_f32_16x16x32_bf16 v[184:187], a[76:79], a[124:127], v[184:187]\n"
"ds_read_b64_tr_b16 a[172:173], v21 offset:8\n"
"v_mfma_f32_16x16x32_bf16 v[156:159], a[68:71], a[124:127], v[156:159]\n"
"ds_read_b64_tr_b16 a[174:175], v21 offset:520\n"
"v_mfma_f32_16x16x32_bf16 v[156:159], a[64:67], a[120:123], v[156:159]\n"
"v_mfma_f32_16x16x32_bf16 v[160:163], a[64:67], a[128:131], v[160:163]\n"
"s_add_u32 s67, s60, 384\n"
"v_mfma_f32_16x16x32_bf16 v[160:163], a[68:71], a[132:135], v[160:163]\n"
"v_mfma_f32_16x16x32_bf16 v[188:191], a[76:79], a[132:135], v[188:191]\n"
"v_mfma_f32_16x16x32_bf16 v[188:191], a[72:75], a[128:131], v[188:191]\n"
"v_mfma_f32_16x16x32_bf16 v[192:195], a[72:75], a[136:139], v[192:195]\n"
"s_waitcnt lgkmcnt(0)\n"
"v_mfma_f32_16x16x32_bf16 v[192:195], a[76:79], a[140:143], v[192:195]\n"
"buffer_load_dwordx4 v6, s[48:51], s67 offen offset:0  lds\n"
"v_mfma_f32_16x16x32_bf16 v[164:167], a[68:71], a[140:143], v[164:167]\n"
"v_mfma_f32_16x16x32_bf16 v[164:167], a[64:67], a[136:139], v[164:167]\n"
"s_add_u32 s67, s60, 15744\n"
"v_mfma_f32_16x16x32_bf16 v[168:171], a[64:67], a[144:147], v[168:171]\n"
"v_mfma_f32_16x16x32_bf16 v[168:171], a[68:71], a[148:151], v[168:171]\n"
"v_mfma_f32_16x16x32_bf16 v[196:199], a[76:79], a[148:151], v[196:199]\n"
"buffer_load_dwordx4 v6, s[48:51], s67 offen offset:1024  lds\n"
"v_mfma_f32_16x16x32_bf16 v[196:199], a[72:75], a[144:147], v[196:199]\n"
"v_mfma_f32_16x16x32_bf16 a[20:23], a[72:75], a[152:155], a[20:23]\n"
"s_add_u32 s67, s60, 31104\n"
"v_mfma_f32_16x16x32_bf16 a[20:23], a[76:79], a[156:159], a[20:23]\n"
"v_mfma_f32_16x16x32_bf16 a[16:19], a[68:71], a[156:159], a[16:19]\n"
"buffer_load_dwordx4 v6, s[48:51], s67 offen offset:2048  lds\n"
"v_mfma_f32_16x16x32_bf16 a[16:19], a[64:67], a[152:155], a[16:19]\n"
"s_barrier\n"
"s_waitcnt vmcnt(11)\n"
"ds_read_b64_tr_b16 a[32:33], v9 offset:8192\n"
"v_mfma_f32_16x16x32_bf16 v[200:203], a[80:83], a[96:99], v[200:203]\n"
"s_add_u32 s67, s60, 46464\n"
"ds_read_b64_tr_b16 a[34:35], v9 offset:8704\n"
"v_mfma_f32_16x16x32_bf16 v[200:203], a[84:87], a[100:103], v[200:203]\n"
"ds_read_b64_tr_b16 a[36:37], v9 offset:12288\n"
"v_mfma_f32_16x16x32_bf16 v[228:231], a[92:95], a[100:103], v[228:231]\n"
"ds_read_b128 a[100:103], v13 offset:16384\n"
"v_mfma_f32_16x16x32_bf16 v[228:231], a[88:91], a[96:99], v[228:231]\n"
"ds_read_b128 a[96:99], v12 offset:16384\n"
"buffer_load_dwordx4 v6, s[48:51], s67 offen offset:3072  lds\n"
"v_mfma_f32_16x16x32_bf16 v[232:235], a[88:91], a[104:107], v[232:235]\n"
"ds_read_b64_tr_b16 a[38:39], v9 offset:12800\n"
"v_mfma_f32_16x16x32_bf16 v[232:235], a[92:95], a[108:111], v[232:235]\n"
"s_add_u32 s70, s75, 655360\n"
"ds_read_b64_tr_b16 a[40:41], v9 offset:8200\n"
"v_mfma_f32_16x16x32_bf16 v[204:207], a[84:87], a[108:111], v[204:207]\n"
"ds_read_b128 a[108:111], v13 offset:18432\n"
"v_mfma_f32_16x16x32_bf16 v[204:207], a[80:83], a[104:107], v[204:207]\n"
"s_add_u32 s70, s70, s65\n"
"ds_read_b128 a[104:107], v12 offset:18432\n"
"v_mfma_f32_16x16x32_bf16 v[208:211], a[80:83], a[112:115], v[208:211]\n"
"ds_read_b64_tr_b16 a[42:43], v9 offset:8712\n"
"v_mfma_f32_16x16x32_bf16 v[208:211], a[84:87], a[116:119], v[208:211]\n"
"ds_read_b64_tr_b16 a[44:45], v9 offset:12296\n"
"buffer_load_dword v23, v14, s[52:55], s70 offen\n"
"v_mfma_f32_16x16x32_bf16 v[236:239], a[92:95], a[116:119], v[236:239]\n"
"ds_read_b128 a[116:119], v13 offset:20480\n"
"v_mfma_f32_16x16x32_bf16 v[236:239], a[88:91], a[112:115], v[236:239]\n"
"buffer_store_dwordx4 a[160:163], v0, s[36:39], %22 offen offset:0 sc1\n"
"ds_read_b128 a[112:115], v12 offset:20480\n"
"v_mfma_f32_16x16x32_bf16 v[240:243], a[88:91], a[120:123], v[240:243]\n"
"ds_read_b64_tr_b16 a[46:47], v9 offset:12808\n"
"v_mfma_f32_16x16x32_bf16 v[240:243], a[92:95], a[124:127], v[240:243]\n"
"ds_read_b64_tr_b16 a[48:49], v15 offset:8192\n"
"v_mfma_f32_16x16x32_bf16 v[212:215], a[84:87], a[124:127], v[212:215]\n"
"ds_read_b128 a[124:127], v13 offset:22528\n"
"v_mfma_f32_16x16x32_bf16 v[212:215], a[80:83], a[120:123], v[212:215]\n"
"ds_read_b128 a[120:123], v12 offset:22528\n"
"v_mfma_f32_16x16x32_bf16 v[216:219], a[80:83], a[128:131], v[216:219]\n"
"s_add_u32 m0, s64, 8192\n"
"ds_read_b64_tr_b16 a[50:51], v15 offset:8704\n"
"v_mfma_f32_16x16x32_bf16 v[216:219], a[84:87], a[132:135], v[216:219]\n"
"ds_read_b64_tr_b16 a[52:53], v15 offset:12288\n"
"v_mfma_f32_16x16x32_bf16 v[244:247], a[92:95], a[132:135], v[244:247]\n"
"ds_read_b128 a[132:135], v13 offset:24576\n"
"v_mfma_f32_16x16x32_bf16 v[244:247], a[88:91], a[128:131], v[244:247]\n"
"ds_read_b128 a[128:131], v12 offset:24576\n"
"v_mfma_f32_16x16x32_bf16 v[248:251], a[88:91], a[136:139], v[248:251]\n"
"ds_read_b64_tr_b16 a[54:55], v15 offset:12800\n"
"v_mfma_f32_16x16x32_bf16 v[248:251], a[92:95], a[140:143], v[248:251]\n"
"ds_read_b64_tr_b16 a[56:57], v15 offset:8200\n"
"v_mfma_f32_16x16x32_bf16 v[220:223], a[84:87], a[140:143], v[220:223]\n"
"ds_read_b128 a[140:143], v13 offset:26624\n"
"v_mfma_f32_16x16x32_bf16 v[220:223], a[80:83], a[136:139], v[220:223]\n"
"ds_read_b128 a[136:139], v12 offset:26624\n"
"v_mfma_f32_16x16x32_bf16 v[224:227], a[80:83], a[144:147], v[224:227]\n"
"s_add_u32 s67, s75, 393216\n"
"ds_read_b64_tr_b16 a[58:59], v15 offset:8712\n"
"v_mfma_f32_16x16x32_bf16 v[224:227], a[84:87], a[148:151], v[224:227]\n"
"ds_read_b64_tr_b16 a[60:61], v15 offset:12296\n"
"v_mfma_f32_16x16x32_bf16 v[252:255], a[92:95], a[148:151], v[252:255]\n"
"ds_read_b128 a[148:151], v13 offset:28672\n"
"v_mfma_f32_16x16x32_bf16 v[252:255], a[88:91], a[144:147], v[252:255]\n"
"ds_read_b128 a[144:147], v12 offset:28672\n"
"ds_read_b64_tr_b16 a[62:63], v15 offset:12808\n"
"v_mfma_f32_16x16x32_bf16 a[28:31], a[88:91], a[152:155], a[28:31]\n"
"v_mfma_f32_16x16x32_bf16 a[28:31], a[92:95], a[156:159], a[28:31]\n"
"v_mfma_f32_16x16x32_bf16 a[24:27], a[84:87], a[156:159], a[24:27]\n"
"ds_read_b128 a[156:159], v13 offset:30720\n"
"v_mfma_f32_16x16x32_bf16 a[24:27], a[80:83], a[152:155], a[24:27]\n"
"s_waitcnt lgkmcnt(15)\n"
"ds_read_b128 a[152:155], v12 offset:30720\n"
"v_mfma_f32_16x16x32_bf16 v[32:35], a[32:35], a[96:99], v[32:35]\n"
"v_mfma_f32_16x16x32_bf16 v[32:35], a[36:39], a[100:103], v[32:35]\n"
"s_waitcnt lgkmcnt(2)\n"
"v_mfma_f32_16x16x32_bf16 v[60:63], a[44:47], a[100:103], v[60:63]\n"
"buffer_load_dwordx4 v11, s[52:55], s67 offen offset:0 lds\n"
"v_mfma_f32_16x16x32_bf16 v[60:63], a[40:43], a[96:99], v[60:63]\n"
"v_mfma_f32_16x16x32_bf16 v[64:67], a[40:43], a[104:107], v[64:67]\n"
"s_add_u32 s67, s75, 408576\n"
"v_mfma_f32_16x16x32_bf16 v[64:67], a[44:47], a[108:111], v[64:67]\n"
"v_mfma_f32_16x16x32_bf16 v[36:39], a[36:39], a[108:111], v[36:39]\n"
"v_mfma_f32_16x16x32_bf16 v[36:39], a[32:35], a[104:107], v[36:39]\n"
"buffer_load_dwordx4 v11, s[52:55], s67 offen offset:1024 lds\n"
"v_mfma_f32_16x16x32_bf16 v[40:43], a[32:35], a[112:115], v[40:43]\n"
"v_mfma_f32_16x16x32_bf16 v[40:43], a[36:39], a[116:119], v[40:43]\n"
"s_add_u32 s67, s75, 423936\n"
"v_mfma_f32_16x16x32_bf16 v[68:71], a[44:47], a[116:119], v[68:71]\n"
"v_mfma_f32_16x16x32_bf16 v[68:71], a[40:43], a[112:115], v[68:71]\n"
"v_mfma_f32_16x16x32_bf16 v[72:75], a[40:43], a[120:123], v[72:75]\n"
"buffer_load_dwordx4 v11, s[52:55], s67 offen offset:2048 lds\n"
"v_mfma_f32_16x16x32_bf16 v[72:75], a[44:47], a[124:127], v[72:75]\n"
"v_mfma_f32_16x16x32_bf16 v[44:47], a[36:39], a[124:127], v[44:47]\n"
"s_add_u32 s67, s75, 439296\n"
"v_mfma_f32_16x16x32_bf16 v[44:47], a[32:35], a[120:123], v[44:47]\n"
"v_mfma_f32_16x16x32_bf16 v[48:51], a[32:35], a[128:131], v[48:51]\n"
"buffer_load_dwordx4 v11, s[52:55], s67 offen offset:3072 lds\n"
"v_mfma_f32_16x16x32_bf16 v[48:51], a[36:39], a[132:135], v[48:51]\n"
"s_add_u32 m0, s64, 12288\n"
"v_mfma_f32_16x16x32_bf16 v[76:79], a[44:47], a[132:135], v[76:79]\n"
"s_waitcnt vmcnt(10)\n"
"ds_read_b64_tr_b16 a[64:65], v9 offset:0\n"
"v_mfma_f32_16x16x32_bf16 v[76:79], a[40:43], a[128:131], v[76:79]\n"
"ds_read_b64_tr_b16 a[66:67], v9 offset:512\n"
"v_mfma_f32_16x16x32_bf16 v[80:83], a[40:43], a[136:139], v[80:83]\n"
"s_add_u32 s67, s75, 458752\n"
"ds_read_b64_tr_b16 a[68:69], v9 offset:4096\n"
"v_mfma_f32_16x16x32_bf16 v[80:83], a[44:47], a[140:143], v[80:83]\n"
"ds_read_b64_tr_b16 a[70:71], v9 offset:4608\n"
"v_mfma_f32_16x16x32_bf16 v[52:55], a[36:39], a[140:143], v[52:55]\n"
"ds_read_b64_tr_b16 a[72:73], v9 offset:8\n"
"buffer_load_dwordx4 v11, s[52:55], s67 offen offset:0 lds\n"
"v_mfma_f32_16x16x32_bf16 v[52:55], a[32:35], a[136:139], v[52:55]\n"
"s_waitcnt lgkmcnt(5)\n"
"ds_read_b64_tr_b16 a[74:75], v9 offset:520\n"
"v_mfma_f32_16x16x32_bf16 v[56:59], a[32:35], a[144:147], v[56:59]\n"
"s_add_u32 s67, s75, 474112\n"
"ds_read_b64_tr_b16 a[76:77], v9 offset:4104\n"
"v_mfma_f32_16x16x32_bf16 v[56:59], a[36:39], a[148:151], v[56:59]\n"
"ds_read_b64_tr_b16 a[78:79], v9 offset:4616\n"
"v_mfma_f32_16x16x32_bf16 v[84:87], a[44:47], a[148:151], v[84:87]\n"
"buffer_load_dwordx4 v11, s[52:55], s67 offen offset:1024 lds\n"
"v_mfma_f32_16x16x32_bf16 v[84:87], a[40:43], a[144:147], v[84:87]\n"
"ds_read_b64_tr_b16 a[80:81], v15 offset:0\n"
"v_mfma_f32_16x16x32_bf16 a[4:7], a[40:43], a[152:155], a[4:7]\n"
"s_add_u32 s67, s75, 489472\n"
"ds_read_b64_tr_b16 a[82:83], v15 offset:512\n"
"v_mfma_f32_16x16x32_bf16 a[4:7], a[44:47], a[156:159], a[4:7]\n"
"ds_read_b64_tr_b16 a[84:85], v15 offset:4096\n"
"v_mfma_f32_16x16x32_bf16 a[0:3], a[36:39], a[156:159], a[0:3]\n"
"ds_read_b64_tr_b16 a[86:87], v15 offset:4608\n"
"buffer_load_dwordx4 v11, s[52:55], s67 offen offset:2048 lds\n"
"v_mfma_f32_16x16x32_bf16 a[0:3], a[32:35], a[152:155], a[0:3]\n"
"ds_read_b64_tr_b16 a[88:89], v15 offset:8\n"
"v_mfma_f32_16x16x32_bf16 v[88:91], a[48:51], a[96:99], v[88:91]\n"
"s_add_u32 s67, s75, 504832\n"
"ds_read_b64_tr_b16 a[90:91], v15 offset:520\n"
"v_mfma_f32_16x16x32_bf16 v[88:91], a[52:55], a[100:103], v[88:91]\n"
"ds_read_b64_tr_b16 a[92:93], v15 offset:4104\n"
"v_mfma_f32_16x16x32_bf16 v[116:119], a[60:63], a[100:103], v[116:119]\n"
"ds_read_b64_tr_b16 a[94:95], v15 offset:4616\n"
"v_mfma_f32_16x16x32_bf16 v[116:119], a[56:59], a[96:99], v[116:119]\n"
"buffer_load_dwordx4 v11, s[52:55], s67 offen offset:3072 lds\n"
"v_mfma_f32_16x16x32_bf16 v[120:123], a[56:59], a[104:107], v[120:123]\n"
"v_mfma_f32_16x16x32_bf16 v[120:123], a[60:63], a[108:111], v[120:123]\n"
"s_add_u32 m0, s64, 0\n"
"v_mfma_f32_16x16x32_bf16 v[92:95], a[52:55], a[108:111], v[92:95]\n"
"v_mfma_f32_16x16x32_bf16 v[92:95], a[48:51], a[104:107], v[92:95]\n"
"s_add_u32 s67, s75, 393344\n"
"v_mfma_f32_16x16x32_bf16 v[96:99], a[48:51], a[112:115], v[96:99]\n"
"s_waitcnt lgkmcnt(0)\n"
"v_mfma_f32_16x16x32_bf16 v[96:99], a[52:55], a[116:119], v[96:99]\n"
"buffer_load_dwordx4 v11, s[52:55], s67 offen offset:0 lds\n"
"v_mfma_f32_16x16x32_bf16 v[124:127], a[60:63], a[116:119], v[124:127]\n"
"v_mfma_f32_16x16x32_bf16 v[124:127], a[56:59], a[112:115], v[124:127]\n"
"s_add_u32 s67, s75, 408704\n"
"v_mfma_f32_16x16x32_bf16 v[128:131], a[56:59], a[120:123], v[128:131]\n"
"v_mfma_f32_16x16x32_bf16 v[128:131], a[60:63], a[124:127], v[128:131]\n"
"v_mfma_f32_16x16x32_bf16 v[100:103], a[52:55], a[124:127], v[100:103]\n"
"buffer_load_dwordx4 v11, s[52:55], s67 offen offset:1024 lds\n"
"v_mfma_f32_16x16x32_bf16 v[100:103], a[48:51], a[120:123], v[100:103]\n"
"v_mfma_f32_16x16x32_bf16 v[104:107], a[48:51], a[128:131], v[104:107]\n"
"s_add_u32 s67, s75, 424064\n"
"v_mfma_f32_16x16x32_bf16 v[104:107], a[52:55], a[132:135], v[104:107]\n"
"v_mfma_f32_16x16x32_bf16 v[132:135], a[60:63], a[132:135], v[132:135]\n"
"v_mfma_f32_16x16x32_bf16 v[132:135], a[56:59], a[128:131], v[132:135]\n"
"buffer_load_dwordx4 v11, s[52:55], s67 offen offset:2048 lds\n"
"v_mfma_f32_16x16x32_bf16 v[136:139], a[56:59], a[136:139], v[136:139]\n"
"v_mfma_f32_16x16x32_bf16 v[136:139], a[60:63], a[140:143], v[136:139]\n"
"s_add_u32 s67, s75, 439424\n"
"v_mfma_f32_16x16x32_bf16 v[108:111], a[52:55], a[140:143], v[108:111]\n"
"v_mfma_f32_16x16x32_bf16 v[108:111], a[48:51], a[136:139], v[108:111]\n"
"buffer_load_dwordx4 v11, s[52:55], s67 offen offset:3072 lds\n"
"v_mfma_f32_16x16x32_bf16 v[112:115], a[48:51], a[144:147], v[112:115]\n"
"v_mfma_f32_16x16x32_bf16 v[112:115], a[52:55], a[148:151], v[112:115]\n"
"s_add_u32 m0, s64, 4096\n"
"v_mfma_f32_16x16x32_bf16 v[140:143], a[60:63], a[148:151], v[140:143]\n"
"v_mfma_f32_16x16x32_bf16 v[140:143], a[56:59], a[144:147], v[140:143]\n"
"s_add_u32 s67, s75, 458880\n"
"v_mfma_f32_16x16x32_bf16 a[12:15], a[56:59], a[152:155], a[12:15]\n"
"v_mfma_f32_16x16x32_bf16 a[12:15], a[60:63], a[156:159], a[12:15]\n"
"buffer_load_dwordx4 v11, s[52:55], s67 offen offset:0 lds\n"
"v_mfma_f32_16x16x32_bf16 a[8:11], a[52:55], a[156:159], a[8:11]\n"
"v_mfma_f32_16x16x32_bf16 a[8:11], a[48:51], a[152:155], a[8:11]\n"
"s_add_u32 s67, s75, 474240\n"
"v_mfma_f32_16x16x32_bf16 v[144:147], a[64:67], a[96:99], v[144:147]\n"
"v_mfma_f32_16x16x32_bf16 v[144:147], a[68:71], a[100:103], v[144:147]\n"
"buffer_load_dwordx4 v11, s[52:55], s67 offen offset:1024 lds\n"
"v_mfma_f32_16x16x32_bf16 v[172:175], a[76:79], a[100:103], v[172:175]\n"
"v_mfma_f32_16x16x32_bf16 v[172:175], a[72:75], a[96:99], v[172:175]\n"
"s_add_u32 s67, s75, 489600\n"
"v_mfma_f32_16x16x32_bf16 v[176:179], a[72:75], a[104:107], v[176:179]\n"
"v_mfma_f32_16x16x32_bf16 v[176:179], a[76:79], a[108:111], v[176:179]\n"
"buffer_load_dwordx4 v11, s[52:55], s67 offen offset:2048 lds\n"
"v_mfma_f32_16x16x32_bf16 v[148:151], a[68:71], a[108:111], v[148:151]\n"
"v_mfma_f32_16x16x32_bf16 v[148:151], a[64:67], a[104:107], v[148:151]\n"
"s_add_u32 s67, s75, 504960\n"
"v_mfma_f32_16x16x32_bf16 v[152:155], a[64:67], a[112:115], v[152:155]\n"
"s_sub_u32 s66, 122880, s73\n"
"v_mfma_f32_16x16x32_bf16 v[152:155], a[68:71], a[116:119], v[152:155]\n"
"v_mfma_f32_16x16x32_bf16 v[180:183], a[76:79], a[116:119], v[180:183]\n"
"v_add_u32 v12, s66, v7\n"
"v_mfma_f32_16x16x32_bf16 v[180:183], a[72:75], a[112:115], v[180:183]\n"
"buffer_load_dwordx4 v11, s[52:55], s67 offen offset:3072 lds\n"
"v_mfma_f32_16x16x32_bf16 v[184:187], a[72:75], a[120:123], v[184:187]\n"
"s_add_u32 m0, s72, 16384\n"
"v_add_u32 v13, s66, v8\n"
"v_mfma_f32_16x16x32_bf16 v[184:187], a[76:79], a[124:127], v[184:187]\n"
"v_mfma_f32_16x16x32_bf16 v[156:159], a[68:71], a[124:127], v[156:159]\n"
"s_add_u32 s67, s60, 512\n"
"v_mfma_f32_16x16x32_bf16 v[156:159], a[64:67], a[120:123], v[156:159]\n"
"buffer_load_dwordx4 v6, s[48:51], s67 offen offset:0  lds\n"
"v_mfma_f32_16x16x32_bf16 v[160:163], a[64:67], a[128:131], v[160:163]\n"
"v_mfma_f32_16x16x32_bf16 v[160:163], a[68:71], a[132:135], v[160:163]\n"
"s_add_u32 s67, s60, 15872\n"
"v_mfma_f32_16x16x32_bf16 v[188:191], a[76:79], a[132:135], v[188:191]\n"
"v_mfma_f32_16x16x32_bf16 v[188:191], a[72:75], a[128:131], v[188:191]\n"
"buffer_load_dwordx4 v6, s[48:51], s67 offen offset:1024  lds\n"
"v_mfma_f32_16x16x32_bf16 v[192:195], a[72:75], a[136:139], v[192:195]\n"
"v_mfma_f32_16x16x32_bf16 v[192:195], a[76:79], a[140:143], v[192:195]\n"
"s_add_u32 s67, s60, 31232\n"
"v_mfma_f32_16x16x32_bf16 v[164:167], a[68:71], a[140:143], v[164:167]\n"
"v_mfma_f32_16x16x32_bf16 v[164:167], a[64:67], a[136:139], v[164:167]\n"
"buffer_load_dwordx4 v6, s[48:51], s67 offen offset:2048  lds\n"
"v_mfma_f32_16x16x32_bf16 v[168:171], a[64:67], a[144:147], v[168:171]\n"
"v_mfma_f32_16x16x32_bf16 v[168:171], a[68:71], a[148:151], v[168:171]\n"
"s_add_u32 s67, s60, 46592\n"
"v_mfma_f32_16x16x32_bf16 v[196:199], a[76:79], a[148:151], v[196:199]\n"
"v_mfma_f32_16x16x32_bf16 v[196:199], a[72:75], a[144:147], v[196:199]\n"
"v_mfma_f32_16x16x32_bf16 a[20:23], a[72:75], a[152:155], a[20:23]\n"
"buffer_load_dwordx4 v6, s[48:51], s67 offen offset:3072  lds\n"
"v_mfma_f32_16x16x32_bf16 a[20:23], a[76:79], a[156:159], a[20:23]\n"
"v_mfma_f32_16x16x32_bf16 a[16:19], a[68:71], a[156:159], a[16:19]\n"
"s_add_u32 s70, s75, 786432\n"
"s_add_u32 s70, s70, s65\n"
"v_mfma_f32_16x16x32_bf16 a[16:19], a[64:67], a[152:155], a[16:19]\n"
"s_waitcnt vmcnt(22)\n"
"s_barrier\n"
"v_mfma_f32_16x16x32_bf16 v[200:203], a[80:83], a[96:99], v[200:203]\n"
"s_waitcnt vmcnt(12)\n"
"ds_read_b64_tr_b16 a[32:33], v9 offset:8192\n"
"v_mfma_f32_16x16x32_bf16 v[200:203], a[84:87], a[100:103], v[200:203]\n"
"ds_read_b64_tr_b16 a[34:35], v9 offset:8704\n"
"v_mfma_f32_16x16x32_bf16 v[228:231], a[92:95], a[100:103], v[228:231]\n"
"ds_read_b128 a[100:103], v13 offset:16384\n"
"buffer_load_dword v23, v14, s[52:55], s70 offen\n"
"v_mfma_f32_16x16x32_bf16 v[228:231], a[88:91], a[96:99], v[228:231]\n"
"ds_read_b128 a[96:99], v12 offset:16384\n"
"v_mfma_f32_16x16x32_bf16 v[232:235], a[88:91], a[104:107], v[232:235]\n"
"ds_read_b64_tr_b16 a[36:37], v9 offset:12288\n"
"v_mfma_f32_16x16x32_bf16 v[232:235], a[92:95], a[108:111], v[232:235]\n"
"ds_read_b64_tr_b16 a[38:39], v9 offset:12800\n"
"v_mfma_f32_16x16x32_bf16 v[204:207], a[84:87], a[108:111], v[204:207]\n"
"ds_read_b128 a[108:111], v13 offset:18432\n"
"v_mfma_f32_16x16x32_bf16 v[204:207], a[80:83], a[104:107], v[204:207]\n"
"buffer_store_dwordx4 a[164:167], v0, s[36:39], %22 offen offset:2048 sc1\n"
"ds_read_b128 a[104:107], v12 offset:18432\n"
"ds_read_b64_tr_b16 a[40:41], v9 offset:8200\n"
"v_mfma_f32_16x16x32_bf16 v[208:211], a[80:83], a[112:115], v[208:211]\n"
"ds_read_b64_tr_b16 a[42:43], v9 offset:8712\n"
"v_mfma_f32_16x16x32_bf16 v[208:211], a[84:87], a[116:119], v[208:211]\n"
"ds_read_b64_tr_b16 a[44:45], v9 offset:12296\n"
"v_mfma_f32_16x16x32_bf16 v[236:239], a[92:95], a[116:119], v[236:239]\n"
"ds_read_b128 a[116:119], v13 offset:20480\n"
"v_mfma_f32_16x16x32_bf16 v[236:239], a[88:91], a[112:115], v[236:239]\n"
"ds_read_b128 a[112:115], v12 offset:20480\n"
"v_mfma_f32_16x16x32_bf16 v[240:243], a[88:91], a[120:123], v[240:243]\n"
"s_add_u32 m0, s64, 8192\n"
"ds_read_b64_tr_b16 a[46:47], v9 offset:12808\n"
"v_mfma_f32_16x16x32_bf16 v[240:243], a[92:95], a[124:127], v[240:243]\n"
"ds_read_b64_tr_b16 a[48:49], v15 offset:8192\n"
"v_mfma_f32_16x16x32_bf16 v[212:215], a[84:87], a[124:127], v[212:215]\n"
"ds_read_b128 a[124:127], v13 offset:22528\n"
"v_mfma_f32_16x16x32_bf16 v[212:215], a[80:83], a[120:123], v[212:215]\n"
"ds_read_b128 a[120:123], v12 offset:22528\n"
"v_mfma_f32_16x16x32_bf16 v[216:219], a[80:83], a[128:131], v[216:219]\n"
"ds_read_b64_tr_b16 a[50:51], v15 offset:8704\n"
"v_mfma_f32_16x16x32_bf16 v[216:219], a[84:87], a[132:135], v[216:219]\n"
"ds_read_b64_tr_b16 a[52:53], v15 offset:12288\n"
"v_mfma_f32_16x16x32_bf16 v[244:247], a[92:95], a[132:135], v[244:247]\n"
"ds_read_b128 a[132:135], v13 offset:24576\n"
"v_mfma_f32_16x16x32_bf16 v[244:247], a[88:91], a[128:131], v[244:247]\n"
"ds_read_b128 a[128:131], v12 offset:24576\n"
"v_mfma_f32_16x16x32_bf16 v[248:251], a[88:91], a[136:139], v[248:251]\n"
"ds_read_b64_tr_b16 a[54:55], v15 offset:12800\n"
"v_mfma_f32_16x16x32_bf16 v[248:251], a[92:95], a[140:143], v[248:251]\n"
"ds_read_b64_tr_b16 a[56:57], v15 offset:8200\n"
"v_mfma_f32_16x16x32_bf16 v[220:223], a[84:87], a[140:143], v[220:223]\n"
"s_add_u32 s67, s75, 524288\n"
"ds_read_b128 a[140:143], v13 offset:26624\n"
"v_mfma_f32_16x16x32_bf16 v[220:223], a[80:83], a[136:139], v[220:223]\n"
"ds_read_b128 a[136:139], v12 offset:26624\n"
"v_mfma_f32_16x16x32_bf16 v[224:227], a[80:83], a[144:147], v[224:227]\n"
"ds_read_b64_tr_b16 a[58:59], v15 offset:8712\n"
"v_mfma_f32_16x16x32_bf16 v[224:227], a[84:87], a[148:151], v[224:227]\n"
"ds_read_b64_tr_b16 a[60:61], v15 offset:12296\n"
"v_mfma_f32_16x16x32_bf16 v[252:255], a[92:95], a[148:151], v[252:255]\n"
"ds_read_b128 a[148:151], v13 offset:28672\n"
"v_mfma_f32_16x16x32_bf16 v[252:255], a[88:91], a[144:147], v[252:255]\n"
"ds_read_b128 a[144:147], v12 offset:28672\n"
"ds_read_b64_tr_b16 a[62:63], v15 offset:12808\n"
"v_mfma_f32_16x16x32_bf16 a[28:31], a[88:91], a[152:155], a[28:31]\n"
"v_mfma_f32_16x16x32_bf16 a[28:31], a[92:95], a[156:159], a[28:31]\n"
"v_mfma_f32_16x16x32_bf16 a[24:27], a[84:87], a[156:159], a[24:27]\n"
"ds_read_b128 a[156:159], v13 offset:30720\n"
"v_mfma_f32_16x16x32_bf16 a[24:27], a[80:83], a[152:155], a[24:27]\n"
"s_waitcnt lgkmcnt(15)\n"
"ds_read_b128 a[152:155], v12 offset:30720\n"
"v_mfma_f32_16x16x32_bf16 v[32:35], a[32:35], a[96:99], v[32:35]\n"
"v_mfma_f32_16x16x32_bf16 v[32:35], a[36:39], a[100:103], v[32:35]\n"
"s_waitcnt lgkmcnt(2)\n"
"v_mfma_f32_16x16x32_bf16 v[60:63], a[44:47], a[100:103], v[60:63]\n"
"buffer_load_dwordx4 v11, s[52:55], s67 offen offset:0 lds\n"
"v_mfma_f32_16x16x32_bf16 v[60:63], a[40:43], a[96:99], v[60:63]\n"
"v_mfma_f32_16x16x32_bf16 v[64:67], a[40:43], a[104:107], v[64:67]\n"
"s_add_u32 s67, s75, 539648\n"
"v_mfma_f32_16x16x32_bf16 v[64:67], a[44:47], a[108:111], v[64:67]\n"
"v_mfma_f32_16x16x32_bf16 v[36:39], a[36:39], a[108:111], v[36:39]\n"
"v_mfma_f32_16x16x32_bf16 v[36:39], a[32:35], a[104:107], v[36:39]\n"
"buffer_load_dwordx4 v11, s[52:55], s67 offen offset:1024 lds\n"
"v_mfma_f32_16x16x32_bf16 v[40:43], a[32:35], a[112:115], v[40:43]\n"
"v_mfma_f32_16x16x32_bf16 v[40:43], a[36:39], a[116:119], v[40:43]\n"
"s_add_u32 s67, s75, 555008\n"
"v_mfma_f32_16x16x32_bf16 v[68:71], a[44:47], a[116:119], v[68:71]\n"
"v_mfma_f32_16x16x32_bf16 v[68:71], a[40:43], a[112:115], v[68:71]\n"
"buffer_load_dwordx4 v11, s[52:55], s67 offen offset:2048 lds\n"
"v_mfma_f32_16x16x32_bf16 v[72:75], a[40:43], a[120:123], v[72:75]\n"
"v_mfma_f32_16x16x32_bf16 v[72:75], a[44:47], a[124:127], v[72:75]\n"
"s_add_u32 s67, s75, 570368\n"
"v_mfma_f32_16x16x32_bf16 v[44:47], a[36:39], a[124:127], v[44:47]\n"
"v_mfma_f32_16x16x32_bf16 v[44:47], a[32:35], a[120:123], v[44:47]\n"
"v_mfma_f32_16x16x32_bf16 v[48:51], a[32:35], a[128:131], v[48:51]\n"
"buffer_load_dwordx4 v11, s[52:55], s67 offen offset:3072 lds\n"
"v_mfma_f32_16x16x32_bf16 v[48:51], a[36:39], a[132:135], v[48:51]\n"
"s_add_u32 m0, s64, 12288\n"
"v_mfma_f32_16x16x32_bf16 v[76:79], a[44:47], a[132:135], v[76:79]\n"
"s_waitcnt vmcnt(10)\n"
"ds_read_b64_tr_b16 a[64:65], v9 offset:0\n"
"v_mfma_f32_16x16x32_bf16 v[76:79], a[40:43], a[128:131], v[76:79]\n"
"ds_read_b64_tr_b16 a[66:67], v9 offset:512\n"
"v_mfma_f32_16x16x32_bf16 v[80:83], a[40:43], a[136:139], v[80:83]\n"
"s_add_u32 s67, s75, 589824\n"
"ds_read_b64_tr_b16 a[68:69], v9 offset:4096\n"
"v_mfma_f32_16x16x32_bf16 v[80:83], a[44:47], a[140:143], v[80:83]\n"
"ds_read_b64_tr_b16 a[70:71], v9 offset:4608\n"
"v_mfma_f32_16x16x32_bf16 v[52:55], a[36:39], a[140:143], v[52:55]\n"
"ds_read_b64_tr_b16 a[72:73], v9 offset:8\n"
"buffer_load_dwordx4 v11, s[52:55], s67 offen offset:0 lds\n"
"v_mfma_f32_16x16x32_bf16 v[52:55], a[32:35], a[136:139], v[52:55]\n"
"s_waitcnt lgkmcnt(5)\n"
"ds_read_b64_tr_b16 a[74:75], v9 offset:520\n"
"v_mfma_f32_16x16x32_bf16 v[56:59], a[32:35], a[144:147], v[56:59]\n"
"s_add_u32 s67, s75, 605184\n"
"ds_read_b64_tr_b16 a[76:77], v9 offset:4104\n"
"v_mfma_f32_16x16x32_bf16 v[56:59], a[36:39], a[148:151], v[56:59]\n"
"ds_read_b64_tr_b16 a[78:79], v9 offset:4616\n"
"v_mfma_f32_16x16x32_bf16 v[84:87], a[44:47], a[148:151], v[84:87]\n"
"buffer_load_dwordx4 v11, s[52:55], s67 offen offset:1024 lds\n"
"v_mfma_f32_16x16x32_bf16 v[84:87], a[40:43], a[144:147], v[84:87]\n"
"ds_read_b64_tr_b16 a[80:81], v15 offset:0\n"
"v_mfma_f32_16x16x32_bf16 a[4:7], a[40:43], a[152:155], a[4:7]\n"
"s_add_u32 s67, s75, 620544\n"
"ds_read_b64_tr_b16 a[82:83], v15 offset:512\n"
"v_mfma_f32_16x16x32_bf16 a[4:7], a[44:47], a[156:159], a[4:7]\n"
"ds_read_b64_tr_b16 a[84:85], v15 offset:4096\n"
"v_mfma_f32_16x16x32_bf16 a[0:3], a[36:39], a[156:159], a[0:3]\n"
"ds_read_b64_tr_b16 a[86:87], v15 offset:4608\n"
"buffer_load_dwordx4 v11, s[52:55], s67 offen offset:2048 lds\n"
"v_mfma_f32_16x16x32_bf16 a[0:3], a[32:35], a[152:155], a[0:3]\n"
"ds_read_b64_tr_b16 a[88:89], v15 offset:8\n"
"v_mfma_f32_16x16x32_bf16 v[88:91], a[48:51], a[96:99], v[88:91]\n"
"s_add_u32 s67, s75, 635904\n"
"ds_read_b64_tr_b16 a[90:91], v15 offset:520\n"
"v_mfma_f32_16x16x32_bf16 v[88:91], a[52:55], a[100:103], v[88:91]\n"
"ds_read_b64_tr_b16 a[92:93], v15 offset:4104\n"
"v_mfma_f32_16x16x32_bf16 v[116:119], a[60:63], a[100:103], v[116:119]\n"
"ds_read_b64_tr_b16 a[94:95], v15 offset:4616\n"
"v_mfma_f32_16x16x32_bf16 v[116:119], a[56:59], a[96:99], v[116:119]\n"
"buffer_load_dwordx4 v11, s[52:55], s67 offen offset:3072 lds\n"
"v_mfma_f32_16x16x32_bf16 v[120:123], a[56:59], a[104:107], v[120:123]\n"
"v_mfma_f32_16x16x32_bf16 v[120:123], a[60:63], a[108:111], v[120:123]\n"
"s_add_u32 m0, s64, 0\n"
"v_mfma_f32_16x16x32_bf16 v[92:95], a[52:55], a[108:111], v[92:95]\n"
"v_mfma_f32_16x16x32_bf16 v[92:95], a[48:51], a[104:107], v[92:95]\n"
"s_add_u32 s67, s75, 524416\n"
"v_mfma_f32_16x16x32_bf16 v[96:99], a[48:51], a[112:115], v[96:99]\n"
"s_waitcnt lgkmcnt(0)\n"
"v_mfma_f32_16x16x32_bf16 v[96:99], a[52:55], a[116:119], v[96:99]\n"
"buffer_load_dwordx4 v11, s[52:55], s67 offen offset:0 lds\n"
"v_mfma_f32_16x16x32_bf16 v[124:127], a[60:63], a[116:119], v[124:127]\n"
"v_mfma_f32_16x16x32_bf16 v[124:127], a[56:59], a[112:115], v[124:127]\n"
"s_add_u32 s67, s75, 539776\n"
"v_mfma_f32_16x16x32_bf16 v[128:131], a[56:59], a[120:123], v[128:131]\n"
"v_mfma_f32_16x16x32_bf16 v[128:131], a[60:63], a[124:127], v[128:131]\n"
"v_mfma_f32_16x16x32_bf16 v[100:103], a[52:55], a[124:127], v[100:103]\n"
"buffer_load_dwordx4 v11, s[52:55], s67 offen offset:1024 lds\n"
"v_mfma_f32_16x16x32_bf16 v[100:103], a[48:51], a[120:123], v[100:103]\n"
"v_mfma_f32_16x16x32_bf16 v[104:107], a[48:51], a[128:131], v[104:107]\n"
"s_add_u32 s67, s75, 555136\n"
"v_mfma_f32_16x16x32_bf16 v[104:107], a[52:55], a[132:135], v[104:107]\n"
"v_mfma_f32_16x16x32_bf16 v[132:135], a[60:63], a[132:135], v[132:135]\n"
"buffer_load_dwordx4 v11, s[52:55], s67 offen offset:2048 lds\n"
"v_mfma_f32_16x16x32_bf16 v[132:135], a[56:59], a[128:131], v[132:135]\n"
"v_mfma_f32_16x16x32_bf16 v[136:139], a[56:59], a[136:139], v[136:139]\n"
"s_add_u32 s67, s75, 570496\n"
"v_mfma_f32_16x16x32_bf16 v[136:139], a[60:63], a[140:143], v[136:139]\n"
"v_mfma_f32_16x16x32_bf16 v[108:111], a[52:55], a[140:143], v[108:111]\n"
"v_mfma_f32_16x16x32_bf16 v[108:111], a[48:51], a[136:139], v[108:111]\n"
"buffer_load_dwordx4 v11, s[52:55], s67 offen offset:3072 lds\n"
"v_mfma_f32_16x16x32_bf16 v[112:115], a[48:51], a[144:147], v[112:115]\n"
"s_add_u32 m0, s64, 4096\n"
"v_mfma_f32_16x16x32_bf16 v[112:115], a[52:55], a[148:151], v[112:115]\n"
"v_mfma_f32_16x16x32_bf16 v[140:143], a[60:63], a[148:151], v[140:143]\n"
"s_add_u32 s67, s75, 589952\n"
"v_mfma_f32_16x16x32_bf16 v[140:143], a[56:59], a[144:147], v[140:143]\n"
"v_mfma_f32_16x16x32_bf16 a[12:15], a[56:59], a[152:155], a[12:15]\n"
"buffer_load_dwordx4 v11, s[52:55], s67 offen offset:0 lds\n"
"v_mfma_f32_16x16x32_bf16 a[12:15], a[60:63], a[156:159], a[12:15]\n"
"v_mfma_f32_16x16x32_bf16 a[8:11], a[52:55], a[156:159], a[8:11]\n"
"s_add_u32 s67, s75, 605312\n"
"v_mfma_f32_16x16x32_bf16 a[8:11], a[48:51], a[152:155], a[8:11]\n"
"v_mfma_f32_16x16x32_bf16 v[144:147], a[64:67], a[96:99], v[144:147]\n"
"buffer_load_dwordx4 v11, s[52:55], s67 offen offset:1024 lds\n"
"v_mfma_f32_16x16x32_bf16 v[144:147], a[68:71], a[100:103], v[144:147]\n"
"v_mfma_f32_16x16x32_bf16 v[172:175], a[76:79], a[100:103], v[172:175]\n"
"s_add_u32 s67, s75, 620672\n"
"v_mfma_f32_16x16x32_bf16 v[172:175], a[72:75], a[96:99], v[172:175]\n"
"v_mfma_f32_16x16x32_bf16 v[176:179], a[72:75], a[104:107], v[176:179]\n"
"v_mfma_f32_16x16x32_bf16 v[176:179], a[76:79], a[108:111], v[176:179]\n"
"buffer_load_dwordx4 v11, s[52:55], s67 offen offset:2048 lds\n"
"v_mfma_f32_16x16x32_bf16 v[148:151], a[68:71], a[108:111], v[148:151]\n"
"v_mfma_f32_16x16x32_bf16 v[148:151], a[64:67], a[104:107], v[148:151]\n"
"s_add_u32 s67, s75, 636032\n"
"v_mfma_f32_16x16x32_bf16 v[152:155], a[64:67], a[112:115], v[152:155]\n"
"s_sub_u32 s66, 0, s73\n"
"v_mfma_f32_16x16x32_bf16 v[152:155], a[68:71], a[116:119], v[152:155]\n"
"buffer_load_dwordx4 v11, s[52:55], s67 offen offset:3072 lds\n"
"v_mfma_f32_16x16x32_bf16 v[180:183], a[76:79], a[116:119], v[180:183]\n"
"v_add_u32 v12, s66, v7\n"
"v_mfma_f32_16x16x32_bf16 v[180:183], a[72:75], a[112:115], v[180:183]\n"
"s_add_u32 m0, s72, 57344\n"
"v_mfma_f32_16x16x32_bf16 v[184:187], a[72:75], a[120:123], v[184:187]\n"
"v_add_u32 v13, s66, v8\n"
"v_mfma_f32_16x16x32_bf16 v[184:187], a[76:79], a[124:127], v[184:187]\n"
"s_add_u32 s67, s60, 640\n"
"v_mfma_f32_16x16x32_bf16 v[156:159], a[68:71], a[124:127], v[156:159]\n"
"v_mfma_f32_16x16x32_bf16 v[156:159], a[64:67], a[120:123], v[156:159]\n"
"buffer_load_dwordx4 v6, s[48:51], s67 offen offset:0  lds\n"
"v_mfma_f32_16x16x32_bf16 v[160:163], a[64:67], a[128:131], v[160:163]\n"
"v_mfma_f32_16x16x32_bf16 v[160:163], a[68:71], a[132:135], v[160:163]\n"
"s_add_u32 s67, s60, 16000\n"
"v_mfma_f32_16x16x32_bf16 v[188:191], a[76:79], a[132:135], v[188:191]\n"
"v_mfma_f32_16x16x32_bf16 v[188:191], a[72:75], a[128:131], v[188:191]\n"
"buffer_load_dwordx4 v6, s[48:51], s67 offen offset:1024  lds\n"
"v_mfma_f32_16x16x32_bf16 v[192:195], a[72:75], a[136:139], v[192:195]\n"
"v_mfma_f32_16x16x32_bf16 v[192:195], a[76:79], a[140:143], v[192:195]\n"
"s_add_u32 s67, s60, 31360\n"
"v_mfma_f32_16x16x32_bf16 v[164:167], a[68:71], a[140:143], v[164:167]\n"
"v_mfma_f32_16x16x32_bf16 v[164:167], a[64:67], a[136:139], v[164:167]\n"
"buffer_load_dwordx4 v6, s[48:51], s67 offen offset:2048  lds\n"
"v_mfma_f32_16x16x32_bf16 v[168:171], a[64:67], a[144:147], v[168:171]\n"
"v_mfma_f32_16x16x32_bf16 v[168:171], a[68:71], a[148:151], v[168:171]\n"
"s_add_u32 s67, s60, 46720\n"
"v_mfma_f32_16x16x32_bf16 v[196:199], a[76:79], a[148:151], v[196:199]\n"
"v_mfma_f32_16x16x32_bf16 v[196:199], a[72:75], a[144:147], v[196:199]\n"
"v_mfma_f32_16x16x32_bf16 a[20:23], a[72:75], a[152:155], a[20:23]\n"
"buffer_load_dwordx4 v6, s[48:51], s67 offen offset:3072  lds\n"
"v_mfma_f32_16x16x32_bf16 a[20:23], a[76:79], a[156:159], a[20:23]\n"
"s_sub_u32 s70, 1048576, s75\n"
"v_and_b32 v22, 0x20000, v14\n"
"v_mfma_f32_16x16x32_bf16 a[16:19], a[68:71], a[156:159], a[16:19]\n"
"v_add_u32 v14, v22, v14\n"
"v_mfma_f32_16x16x32_bf16 a[16:19], a[64:67], a[152:155], a[16:19]\n"
"s_waitcnt vmcnt(22)\n"
"s_barrier\n"
"v_mfma_f32_16x16x32_bf16 v[200:203], a[80:83], a[96:99], v[200:203]\n"
"s_waitcnt vmcnt(12)\n"
"ds_read_b64_tr_b16 a[32:33], v9 offset:8192\n"
"v_mfma_f32_16x16x32_bf16 v[200:203], a[84:87], a[100:103], v[200:203]\n"
"s_add_u32 s70, s70, s65\n"
"ds_read_b64_tr_b16 a[34:35], v9 offset:8704\n"
"buffer_load_dword v23, v14, s[52:55], s70 offen\n"
"v_mfma_f32_16x16x32_bf16 v[228:231], a[92:95], a[100:103], v[228:231]\n"
"ds_read_b128 a[100:103], v13 offset:16384\n"
"v_mfma_f32_16x16x32_bf16 v[228:231], a[88:91], a[96:99], v[228:231]\n"
"ds_read_b128 a[96:99], v12 offset:16384\n"
"v_mfma_f32_16x16x32_bf16 v[232:235], a[88:91], a[104:107], v[232:235]\n"
"ds_read_b64_tr_b16 a[36:37], v9 offset:12288\n"
"v_mfma_f32_16x16x32_bf16 v[232:235], a[92:95], a[108:111], v[232:235]\n"
"ds_read_b64_tr_b16 a[38:39], v9 offset:12800\n"
"v_mfma_f32_16x16x32_bf16 v[204:207], a[84:87], a[108:111], v[204:207]\n"
"ds_read_b128 a[108:111], v13 offset:18432\n"
"v_mfma_f32_16x16x32_bf16 v[204:207], a[80:83], a[104:107], v[204:207]\n"
"buffer_store_dwordx4 a[168:171], v1, s[36:39], %22 offen offset:0 sc1\n"
"ds_read_b128 a[104:107], v12 offset:18432\n"
"ds_read_b64_tr_b16 a[40:41], v9 offset:8200\n"
"v_mfma_f32_16x16x32_bf16 v[208:211], a[80:83], a[112:115], v[208:211]\n"
"ds_read_b64_tr_b16 a[42:43], v9 offset:8712\n"
"v_mfma_f32_16x16x32_bf16 v[208:211], a[84:87], a[116:119], v[208:211]\n"
"ds_read_b64_tr_b16 a[44:45], v9 offset:12296\n"
"v_mfma_f32_16x16x32_bf16 v[236:239], a[92:95], a[116:119], v[236:239]\n"
"ds_read_b128 a[116:119], v13 offset:20480\n"
"v_mfma_f32_16x16x32_bf16 v[236:239], a[88:91], a[112:115], v[236:239]\n"
"ds_read_b128 a[112:115], v12 offset:20480\n"
"v_mfma_f32_16x16x32_bf16 v[240:243], a[88:91], a[120:123], v[240:243]\n"
"s_add_u32 m0, s64, 8192\n"
"ds_read_b64_tr_b16 a[46:47], v9 offset:12808\n"
"v_mfma_f32_16x16x32_bf16 v[240:243], a[92:95], a[124:127], v[240:243]\n"
"ds_read_b64_tr_b16 a[48:49], v15 offset:8192\n"
"v_mfma_f32_16x16x32_bf16 v[212:215], a[84:87], a[124:127], v[212:215]\n"
"ds_read_b128 a[124:127], v13 offset:22528\n"
"v_mfma_f32_16x16x32_bf16 v[212:215], a[80:83], a[120:123], v[212:215]\n"
"ds_read_b128 a[120:123], v12 offset:22528\n"
"v_mfma_f32_16x16x32_bf16 v[216:219], a[80:83], a[128:131], v[216:219]\n"
"ds_read_b64_tr_b16 a[50:51], v15 offset:8704\n"
"v_mfma_f32_16x16x32_bf16 v[216:219], a[84:87], a[132:135], v[216:219]\n"
"ds_read_b64_tr_b16 a[52:53], v15 offset:12288\n"
"v_mfma_f32_16x16x32_bf16 v[244:247], a[92:95], a[132:135], v[244:247]\n"
"ds_read_b128 a[132:135], v13 offset:24576\n"
"v_mfma_f32_16x16x32_bf16 v[244:247], a[88:91], a[128:131], v[244:247]\n"
"ds_read_b128 a[128:131], v12 offset:24576\n"
"v_mfma_f32_16x16x32_bf16 v[248:251], a[88:91], a[136:139], v[248:251]\n"
"ds_read_b64_tr_b16 a[54:55], v15 offset:12800\n"
"v_mfma_f32_16x16x32_bf16 v[248:251], a[92:95], a[140:143], v[248:251]\n"
"ds_read_b64_tr_b16 a[56:57], v15 offset:8200\n"
"v_mfma_f32_16x16x32_bf16 v[220:223], a[84:87], a[140:143], v[220:223]\n"
"s_add_u32 s67, s75, 655360\n"
"ds_read_b128 a[140:143], v13 offset:26624\n"
"v_mfma_f32_16x16x32_bf16 v[220:223], a[80:83], a[136:139], v[220:223]\n"
"ds_read_b128 a[136:139], v12 offset:26624\n"
"v_mfma_f32_16x16x32_bf16 v[224:227], a[80:83], a[144:147], v[224:227]\n"
"ds_read_b64_tr_b16 a[58:59], v15 offset:8712\n"
"v_mfma_f32_16x16x32_bf16 v[224:227], a[84:87], a[148:151], v[224:227]\n"
"ds_read_b64_tr_b16 a[60:61], v15 offset:12296\n"
"v_mfma_f32_16x16x32_bf16 v[252:255], a[92:95], a[148:151], v[252:255]\n"
"ds_read_b128 a[148:151], v13 offset:28672\n"
"v_mfma_f32_16x16x32_bf16 v[252:255], a[88:91], a[144:147], v[252:255]\n"
"ds_read_b128 a[144:147], v12 offset:28672\n"
"ds_read_b64_tr_b16 a[62:63], v15 offset:12808\n"
"v_mfma_f32_16x16x32_bf16 a[28:31], a[88:91], a[152:155], a[28:31]\n"
"v_mfma_f32_16x16x32_bf16 a[28:31], a[92:95], a[156:159], a[28:31]\n"
"v_mfma_f32_16x16x32_bf16 a[24:27], a[84:87], a[156:159], a[24:27]\n"
"ds_read_b128 a[156:159], v13 offset:30720\n"
"v_mfma_f32_16x16x32_bf16 a[24:27], a[80:83], a[152:155], a[24:27]\n"
"s_waitcnt lgkmcnt(15)\n"
"ds_read_b128 a[152:155], v12 offset:30720\n"
"v_mfma_f32_16x16x32_bf16 v[32:35], a[32:35], a[96:99], v[32:35]\n"
"v_mfma_f32_16x16x32_bf16 v[32:35], a[36:39], a[100:103], v[32:35]\n"
"s_waitcnt lgkmcnt(2)\n"
"v_mfma_f32_16x16x32_bf16 v[60:63], a[44:47], a[100:103], v[60:63]\n"
"buffer_load_dwordx4 v11, s[52:55], s67 offen offset:0 lds\n"
"v_mfma_f32_16x16x32_bf16 v[60:63], a[40:43], a[96:99], v[60:63]\n"
"v_mfma_f32_16x16x32_bf16 v[64:67], a[40:43], a[104:107], v[64:67]\n"
"s_add_u32 s67, s75, 670720\n"
"v_mfma_f32_16x16x32_bf16 v[64:67], a[44:47], a[108:111], v[64:67]\n"
"v_mfma_f32_16x16x32_bf16 v[36:39], a[36:39], a[108:111], v[36:39]\n"
"buffer_load_dwordx4 v11, s[52:55], s67 offen offset:1024 lds\n"
"v_mfma_f32_16x16x32_bf16 v[36:39], a[32:35], a[104:107], v[36:39]\n"
"v_mfma_f32_16x16x32_bf16 v[40:43], a[32:35], a[112:115], v[40:43]\n"
"s_add_u32 s67, s75, 686080\n"
"v_mfma_f32_16x16x32_bf16 v[40:43], a[36:39], a[116:119], v[40:43]\n"
"v_mfma_f32_16x16x32_bf16 v[68:71], a[44:47], a[116:119], v[68:71]\n"
"v_mfma_f32_16x16x32_bf16 v[68:71], a[40:43], a[112:115], v[68:71]\n"
"buffer_load_dwordx4 v11, s[52:55], s67 offen offset:2048 lds\n"
"v_mfma_f32_16x16x32_bf16 v[72:75], a[40:43], a[120:123], v[72:75]\n"
"v_mfma_f32_16x16x32_bf16 v[72:75], a[44:47], a[124:127], v[72:75]\n"
"s_add_u32 s67, s75, 701440\n"
"v_mfma_f32_16x16x32_bf16 v[44:47], a[36:39], a[124:127], v[44:47]\n"
"v_mfma_f32_16x16x32_bf16 v[44:47], a[32:35], a[120:123], v[44:47]\n"
"v_mfma_f32_16x16x32_bf16 v[48:51], a[32:35], a[128:131], v[48:51]\n"
"buffer_load_dwordx4 v11, s[52:55], s67 offen offset:3072 lds\n"
"v_mfma_f32_16x16x32_bf16 v[48:51], a[36:39], a[132:135], v[48:51]\n"
"s_add_u32 m0, s64, 12288\n"
"v_mfma_f32_16x16x32_bf16 v[76:79], a[44:47], a[132:135], v[76:79]\n"
"s_waitcnt vmcnt(10)\n"
"ds_read_b64_tr_b16 a[64:65], v9 offset:0\n"
"v_mfma_f32_16x16x32_bf16 v[76:79], a[40:43], a[128:131], v[76:79]\n"
"s_add_u32 s67, s75, 720896\n"
"ds_read_b64_tr_b16 a[66:67], v9 offset:512\n"
"v_mfma_f32_16x16x32_bf16 v[80:83], a[40:43], a[136:139], v[80:83]\n"
"ds_read_b64_tr_b16 a[68:69], v9 offset:4096\n"
"v_mfma_f32_16x16x32_bf16 v[80:83], a[44:47], a[140:143], v[80:83]\n"
"ds_read_b64_tr_b16 a[70:71], v9 offset:4608\n"
"buffer_load_dwordx4 v11, s[52:55], s67 offen offset:0 lds\n"
"v_mfma_f32_16x16x32_bf16 v[52:55], a[36:39], a[140:143], v[52:55]\n"
"s_add_u32 s67, s75, 736256\n"
"ds_read_b64_tr_b16 a[72:73], v9 offset:8\n"
"v_mfma_f32_16x16x32_bf16 v[52:55], a[32:35], a[136:139], v[52:55]\n"
"s_waitcnt lgkmcnt(5)\n"
"ds_read_b64_tr_b16 a[74:75], v9 offset:520\n"
"v_mfma_f32_16x16x32_bf16 v[56:59], a[32:35], a[144:147], v[56:59]\n"
"ds_read_b64_tr_b16 a[76:77], v9 offset:4104\n"
"v_mfma_f32_16x16x32_bf16 v[56:59], a[36:39], a[148:151], v[56:59]\n"
"ds_read_b64_tr_b16 a[78:79], v9 offset:4616\n"
"v_mfma_f32_16x16x32_bf16 v[84:87], a[44:47], a[148:151], v[84:87]\n"
"buffer_load_dwordx4 v11, s[52:55], s67 offen offset:1024 lds\n"
"v_mfma_f32_16x16x32_bf16 v[84:87], a[40:43], a[144:147], v[84:87]\n"
"ds_read_b64_tr_b16 a[80:81], v15 offset:0\n"
"v_mfma_f32_16x16x32_bf16 a[4:7], a[40:43], a[152:155], a[4:7]\n"
"s_add_u32 s67, s75, 751616\n"
"ds_read_b64_tr_b16 a[82:83], v15 offset:512\n"
"v_mfma_f32_16x16x32_bf16 a[4:7], a[44:47], a[156:159], a[4:7]\n"
"ds_read_b64_tr_b16 a[84:85], v15 offset:4096\n"
"v_mfma_f32_16x16x32_bf16 a[0:3], a[36:39], a[156:159], a[0:3]\n"
"ds_read_b64_tr_b16 a[86:87], v15 offset:4608\n"
"buffer_load_dwordx4 v11, s[52:55], s67 offen offset:2048 lds\n"
"v_mfma_f32_16x16x32_bf16 a[0:3], a[32:35], a[152:155], a[0:3]\n"
"ds_read_b64_tr_b16 a[88:89], v15 offset:8\n"
"v_mfma_f32_16x16x32_bf16 v[88:91], a[48:51], a[96:99], v[88:91]\n"
"s_add_u32 s67, s75, 766976\n"
"ds_read_b64_tr_b16 a[90:91], v15 offset:520\n"
"v_mfma_f32_16x16x32_bf16 v[88:91], a[52:55], a[100:103], v[88:91]\n"
"ds_read_b64_tr_b16 a[92:93], v15 offset:4104\n"
"v_mfma_f32_16x16x32_bf16 v[116:119], a[60:63], a[100:103], v[116:119]\n"
"ds_read_b64_tr_b16 a[94:95], v15 offset:4616\n"
"v_mfma_f32_16x16x32_bf16 v[116:119], a[56:59], a[96:99], v[116:119]\n"
"buffer_load_dwordx4 v11, s[52:55], s67 offen offset:3072 lds\n"
"v_mfma_f32_16x16x32_bf16 v[120:123], a[56:59], a[104:107], v[120:123]\n"
"v_mfma_f32_16x16x32_bf16 v[120:123], a[60:63], a[108:111], v[120:123]\n"
"s_add_u32 m0, s64, 0\n"
"v_mfma_f32_16x16x32_bf16 v[92:95], a[52:55], a[108:111], v[92:95]\n"
"v_mfma_f32_16x16x32_bf16 v[92:95], a[48:51], a[104:107], v[92:95]\n"
"s_add_u32 s67, s75, 655488\n"
"v_mfma_f32_16x16x32_bf16 v[96:99], a[48:51], a[112:115], v[96:99]\n"
"s_waitcnt lgkmcnt(0)\n"
"v_mfma_f32_16x16x32_bf16 v[96:99], a[52:55], a[116:119], v[96:99]\n"
"buffer_load_dwordx4 v11, s[52:55], s67 offen offset:0 lds\n"
"v_mfma_f32_16x16x32_bf16 v[124:127], a[60:63], a[116:119], v[124:127]\n"
"v_mfma_f32_16x16x32_bf16 v[124:127], a[56:59], a[112:115], v[124:127]\n"
"s_add_u32 s67, s75, 670848\n"
"v_mfma_f32_16x16x32_bf16 v[128:131], a[56:59], a[120:123], v[128:131]\n"
"v_mfma_f32_16x16x32_bf16 v[128:131], a[60:63], a[124:127], v[128:131]\n"
"buffer_load_dwordx4 v11, s[52:55], s67 offen offset:1024 lds\n"
"v_mfma_f32_16x16x32_bf16 v[100:103], a[52:55], a[124:127], v[100:103]\n"
"v_mfma_f32_16x16x32_bf16 v[100:103], a[48:51], a[120:123], v[100:103]\n"
"s_add_u32 s67, s75, 686208\n"
"v_mfma_f32_16x16x32_bf16 v[104:107], a[48:51], a[128:131], v[104:107]\n"
"v_mfma_f32_16x16x32_bf16 v[104:107], a[52:55], a[132:135], v[104:107]\n"
"v_mfma_f32_16x16x32_bf16 v[132:135], a[60:63], a[132:135], v[132:135]\n"
"buffer_load_dwordx4 v11, s[52:55], s67 offen offset:2048 lds\n"
"v_mfma_f32_16x16x32_bf16 v[132:135], a[56:59], a[128:131], v[132:135]\n"
"v_mfma_f32_16x16x32_bf16 v[136:139], a[56:59], a[136:139], v[136:139]\n"
"s_add_u32 s67, s75, 701568\n"
"v_mfma_f32_16x16x32_bf16 v[136:139], a[60:63], a[140:143], v[136:139]\n"
"v_mfma_f32_16x16x32_bf16 v[108:111], a[52:55], a[140:143], v[108:111]\n"
"buffer_load_dwordx4 v11, s[52:55], s67 offen offset:3072 lds\n"
"v_mfma_f32_16x16x32_bf16 v[108:111], a[48:51], a[136:139], v[108:111]\n"
"v_mfma_f32_16x16x32_bf16 v[112:115], a[48:51], a[144:147], v[112:115]\n"
"s_add_u32 m0, s64, 4096\n"
"v_mfma_f32_16x16x32_bf16 v[112:115], a[52:55], a[148:151], v[112:115]\n"
"v_mfma_f32_16x16x32_bf16 v[140:143], a[60:63], a[148:151], v[140:143]\n"
"s_add_u32 s67, s75, 721024\n"
"v_mfma_f32_16x16x32_bf16 v[140:143], a[56:59], a[144:147], v[140:143]\n"
"v_mfma_f32_16x16x32_bf16 a[12:15], a[56:59], a[152:155], a[12:15]\n"
"buffer_load_dwordx4 v11, s[52:55], s67 offen offset:0 lds\n"
"v_mfma_f32_16x16x32_bf16 a[12:15], a[60:63], a[156:159], a[12:15]\n"
"v_mfma_f32_16x16x32_bf16 a[8:11], a[52:55], a[156:159], a[8:11]\n"
"s_add_u32 s67, s75, 736384\n"
"v_mfma_f32_16x16x32_bf16 a[8:11], a[48:51], a[152:155], a[8:11]\n"
"v_mfma_f32_16x16x32_bf16 v[144:147], a[64:67], a[96:99], v[144:147]\n"
"buffer_load_dwordx4 v11, s[52:55], s67 offen offset:1024 lds\n"
"v_mfma_f32_16x16x32_bf16 v[144:147], a[68:71], a[100:103], v[144:147]\n"
"v_mfma_f32_16x16x32_bf16 v[172:175], a[76:79], a[100:103], v[172:175]\n"
"s_add_u32 s67, s75, 751744\n"
"v_mfma_f32_16x16x32_bf16 v[172:175], a[72:75], a[96:99], v[172:175]\n"
"v_mfma_f32_16x16x32_bf16 v[176:179], a[72:75], a[104:107], v[176:179]\n"
"buffer_load_dwordx4 v11, s[52:55], s67 offen offset:2048 lds\n"
"v_mfma_f32_16x16x32_bf16 v[176:179], a[76:79], a[108:111], v[176:179]\n"
"v_mfma_f32_16x16x32_bf16 v[148:151], a[68:71], a[108:111], v[148:151]\n"
"s_add_u32 s67, s75, 767104\n"
"v_mfma_f32_16x16x32_bf16 v[148:151], a[64:67], a[104:107], v[148:151]\n"
"v_mfma_f32_16x16x32_bf16 v[152:155], a[64:67], a[112:115], v[152:155]\n"
"s_sub_u32 s66, 40960, s73\n"
"v_mfma_f32_16x16x32_bf16 v[152:155], a[68:71], a[116:119], v[152:155]\n"
"buffer_load_dwordx4 v11, s[52:55], s67 offen offset:3072 lds\n"
"v_mfma_f32_16x16x32_bf16 v[180:183], a[76:79], a[116:119], v[180:183]\n"
"s_add_u32 m0, s72, 98304\n"
"v_add_u32 v12, s66, v7\n"
"v_mfma_f32_16x16x32_bf16 v[180:183], a[72:75], a[112:115], v[180:183]\n"
"v_mfma_f32_16x16x32_bf16 v[184:187], a[72:75], a[120:123], v[184:187]\n"
"s_add_u32 s67, s60, 768\n"
"v_add_u32 v13, s66, v8\n"
"v_mfma_f32_16x16x32_bf16 v[184:187], a[76:79], a[124:127], v[184:187]\n"
"v_mfma_f32_16x16x32_bf16 v[156:159], a[68:71], a[124:127], v[156:159]\n"
"buffer_load_dwordx4 v6, s[48:51], s67 offen offset:0  lds\n"
"v_mfma_f32_16x16x32_bf16 v[156:159], a[64:67], a[120:123], v[156:159]\n"
"v_mfma_f32_16x16x32_bf16 v[160:163], a[64:67], a[128:131], v[160:163]\n"
"s_add_u32 s67, s60, 16128\n"
"v_mfma_f32_16x16x32_bf16 v[160:163], a[68:71], a[132:135], v[160:163]\n"
"v_mfma_f32_16x16x32_bf16 v[188:191], a[76:79], a[132:135], v[188:191]\n"
"buffer_load_dwordx4 v6, s[48:51], s67 offen offset:1024  lds\n"
"v_mfma_f32_16x16x32_bf16 v[188:191], a[72:75], a[128:131], v[188:191]\n"
"v_mfma_f32_16x16x32_bf16 v[192:195], a[72:75], a[136:139], v[192:195]\n"
"s_add_u32 s67, s60, 31488\n"
"v_mfma_f32_16x16x32_bf16 v[192:195], a[76:79], a[140:143], v[192:195]\n"
"v_mfma_f32_16x16x32_bf16 v[164:167], a[68:71], a[140:143], v[164:167]\n"
"v_mfma_f32_16x16x32_bf16 v[164:167], a[64:67], a[136:139], v[164:167]\n"
"buffer_load_dwordx4 v6, s[48:51], s67 offen offset:2048  lds\n"
"v_mfma_f32_16x16x32_bf16 v[168:171], a[64:67], a[144:147], v[168:171]\n"
"v_mfma_f32_16x16x32_bf16 v[168:171], a[68:71], a[148:151], v[168:171]\n"
"s_add_u32 s67, s60, 46848\n"
"v_mfma_f32_16x16x32_bf16 v[196:199], a[76:79], a[148:151], v[196:199]\n"
"v_mfma_f32_16x16x32_bf16 v[196:199], a[72:75], a[144:147], v[196:199]\n"
"buffer_load_dwordx4 v6, s[48:51], s67 offen offset:3072  lds\n"
"v_mfma_f32_16x16x32_bf16 a[20:23], a[72:75], a[152:155], a[20:23]\n"
"v_mfma_f32_16x16x32_bf16 a[20:23], a[76:79], a[156:159], a[20:23]\n"
"s_sub_u32 s70, 1572864, s75\n"
"v_mfma_f32_16x16x32_bf16 a[16:19], a[68:71], a[156:159], a[16:19]\n"
"s_add_u32 s70, s70, s65\n"
"v_mfma_f32_16x16x32_bf16 a[16:19], a[64:67], a[152:155], a[16:19]\n"
"s_waitcnt vmcnt(22)\n"
"s_barrier\n"
"v_mfma_f32_16x16x32_bf16 v[200:203], a[80:83], a[96:99], v[200:203]\n"
"s_waitcnt vmcnt(12)\n"
"ds_read_b64_tr_b16 a[32:33], v9 offset:8192\n"
"v_mfma_f32_16x16x32_bf16 v[200:203], a[84:87], a[100:103], v[200:203]\n"
"ds_read_b64_tr_b16 a[34:35], v9 offset:8704\n"
"buffer_load_dword v23, v14, s[52:55], s70 offen\n"
"v_mfma_f32_16x16x32_bf16 v[228:231], a[92:95], a[100:103], v[228:231]\n"
"ds_read_b128 a[100:103], v13 offset:16384\n"
"v_mfma_f32_16x16x32_bf16 v[228:231], a[88:91], a[96:99], v[228:231]\n"
"ds_read_b128 a[96:99], v12 offset:16384\n"
"v_mfma_f32_16x16x32_bf16 v[232:235], a[88:91], a[104:107], v[232:235]\n"
"ds_read_b64_tr_b16 a[36:37], v9 offset:12288\n"
"v_mfma_f32_16x16x32_bf16 v[232:235], a[92:95], a[108:111], v[232:235]\n"
"ds_read_b64_tr_b16 a[38:39], v9 offset:12800\n"
"v_mfma_f32_16x16x32_bf16 v[204:207], a[84:87], a[108:111], v[204:207]\n"
"ds_read_b128 a[108:111], v13 offset:18432\n"
"v_mfma_f32_16x16x32_bf16 v[204:207], a[80:83], a[104:107], v[204:207]\n"
"buffer_store_dwordx4 a[172:175], v1, s[36:39], %22 offen offset:2048 sc1\n"
"ds_read_b128 a[104:107], v12 offset:18432\n"
"ds_read_b64_tr_b16 a[40:41], v9 offset:8200\n"
"v_mfma_f32_16x16x32_bf16 v[208:211], a[80:83], a[112:115], v[208:211]\n"
"ds_read_b64_tr_b16 a[42:43], v9 offset:8712\n"
"v_mfma_f32_16x16x32_bf16 v[208:211], a[84:87], a[116:119], v[208:211]\n"
"ds_read_b64_tr_b16 a[44:45], v9 offset:12296\n"
"v_mfma_f32_16x16x32_bf16 v[236:239], a[92:95], a[116:119], v[236:239]\n"
"ds_read_b128 a[116:119], v13 offset:20480\n"
"v_mfma_f32_16x16x32_bf16 v[236:239], a[88:91], a[112:115], v[236:239]\n"
"ds_read_b128 a[112:115], v12 offset:20480\n"
"v_mfma_f32_16x16x32_bf16 v[240:243], a[88:91], a[120:123], v[240:243]\n"
"s_add_u32 m0, s64, 8192\n"
"ds_read_b64_tr_b16 a[46:47], v9 offset:12808\n"
"v_mfma_f32_16x16x32_bf16 v[240:243], a[92:95], a[124:127], v[240:243]\n"
"ds_read_b64_tr_b16 a[48:49], v15 offset:8192\n"
"v_mfma_f32_16x16x32_bf16 v[212:215], a[84:87], a[124:127], v[212:215]\n"
"ds_read_b128 a[124:127], v13 offset:22528\n"
"v_mfma_f32_16x16x32_bf16 v[212:215], a[80:83], a[120:123], v[212:215]\n"
"ds_read_b128 a[120:123], v12 offset:22528\n"
"v_mfma_f32_16x16x32_bf16 v[216:219], a[80:83], a[128:131], v[216:219]\n"
"ds_read_b64_tr_b16 a[50:51], v15 offset:8704\n"
"v_mfma_f32_16x16x32_bf16 v[216:219], a[84:87], a[132:135], v[216:219]\n"
"ds_read_b64_tr_b16 a[52:53], v15 offset:12288\n"
"v_mfma_f32_16x16x32_bf16 v[244:247], a[92:95], a[132:135], v[244:247]\n"
"ds_read_b128 a[132:135], v13 offset:24576\n"
"v_mfma_f32_16x16x32_bf16 v[244:247], a[88:91], a[128:131], v[244:247]\n"
"ds_read_b128 a[128:131], v12 offset:24576\n"
"v_mfma_f32_16x16x32_bf16 v[248:251], a[88:91], a[136:139], v[248:251]\n"
"ds_read_b64_tr_b16 a[54:55], v15 offset:12800\n"
"v_mfma_f32_16x16x32_bf16 v[248:251], a[92:95], a[140:143], v[248:251]\n"
"ds_read_b64_tr_b16 a[56:57], v15 offset:8200\n"
"v_mfma_f32_16x16x32_bf16 v[220:223], a[84:87], a[140:143], v[220:223]\n"
"s_add_u32 s67, s75, 786432\n"
"ds_read_b128 a[140:143], v13 offset:26624\n"
"v_mfma_f32_16x16x32_bf16 v[220:223], a[80:83], a[136:139], v[220:223]\n"
"ds_read_b128 a[136:139], v12 offset:26624\n"
"v_mfma_f32_16x16x32_bf16 v[224:227], a[80:83], a[144:147], v[224:227]\n"
"ds_read_b64_tr_b16 a[58:59], v15 offset:8712\n"
"v_mfma_f32_16x16x32_bf16 v[224:227], a[84:87], a[148:151], v[224:227]\n"
"ds_read_b64_tr_b16 a[60:61], v15 offset:12296\n"
"v_mfma_f32_16x16x32_bf16 v[252:255], a[92:95], a[148:151], v[252:255]\n"
"ds_read_b128 a[148:151], v13 offset:28672\n"
"v_mfma_f32_16x16x32_bf16 v[252:255], a[88:91], a[144:147], v[252:255]\n"
"ds_read_b128 a[144:147], v12 offset:28672\n"
"ds_read_b64_tr_b16 a[62:63], v15 offset:12808\n"
"v_mfma_f32_16x16x32_bf16 a[28:31], a[88:91], a[152:155], a[28:31]\n"
"v_mfma_f32_16x16x32_bf16 a[28:31], a[92:95], a[156:159], a[28:31]\n"
"v_mfma_f32_16x16x32_bf16 a[24:27], a[84:87], a[156:159], a[24:27]\n"
"ds_read_b128 a[156:159], v13 offset:30720\n"
"v_mfma_f32_16x16x32_bf16 a[24:27], a[80:83], a[152:155], a[24:27]\n"
"s_waitcnt lgkmcnt(15)\n"
"ds_read_b128 a[152:155], v12 offset:30720\n"
"v_mfma_f32_16x16x32_bf16 v[32:35], a[32:35], a[96:99], v[32:35]\n"
"v_mfma_f32_16x16x32_bf16 v[32:35], a[36:39], a[100:103], v[32:35]\n"
"s_waitcnt lgkmcnt(2)\n"
"v_mfma_f32_16x16x32_bf16 v[60:63], a[44:47], a[100:103], v[60:63]\n"
"buffer_load_dwordx4 v11, s[52:55], s67 offen offset:0 lds\n"
"v_mfma_f32_16x16x32_bf16 v[60:63], a[40:43], a[96:99], v[60:63]\n"
"v_mfma_f32_16x16x32_bf16 v[64:67], a[40:43], a[104:107], v[64:67]\n"
"s_add_u32 s67, s75, 801792\n"
"v_mfma_f32_16x16x32_bf16 v[64:67], a[44:47], a[108:111], v[64:67]\n"
"v_mfma_f32_16x16x32_bf16 v[36:39], a[36:39], a[108:111], v[36:39]\n"
"v_mfma_f32_16x16x32_bf16 v[36:39], a[32:35], a[104:107], v[36:39]\n"
"buffer_load_dwordx4 v11, s[52:55], s67 offen offset:1024 lds\n"
"v_mfma_f32_16x16x32_bf16 v[40:43], a[32:35], a[112:115], v[40:43]\n"
"v_mfma_f32_16x16x32_bf16 v[40:43], a[36:39], a[116:119], v[40:43]\n"
"s_add_u32 s67, s75, 817152\n"
"v_mfma_f32_16x16x32_bf16 v[68:71], a[44:47], a[116:119], v[68:71]\n"
"v_mfma_f32_16x16x32_bf16 v[68:71], a[40:43], a[112:115], v[68:71]\n"
"v_mfma_f32_16x16x32_bf16 v[72:75], a[40:43], a[120:123], v[72:75]\n"
"buffer_load_dwordx4 v11, s[52:55], s67 offen offset:2048 lds\n"
"v_mfma_f32_16x16x32_bf16 v[72:75], a[44:47], a[124:127], v[72:75]\n"
"v_mfma_f32_16x16x32_bf16 v[44:47], a[36:39], a[124:127], v[44:47]\n"
"s_add_u32 s67, s75, 832512\n"
"v_mfma_f32_16x16x32_bf16 v[44:47], a[32:35], a[120:123], v[44:47]\n"
"v_mfma_f32_16x16x32_bf16 v[48:51], a[32:35], a[128:131], v[48:51]\n"
"buffer_load_dwordx4 v11, s[52:55], s67 offen offset:3072 lds\n"
"v_mfma_f32_16x16x32_bf16 v[48:51], a[36:39], a[132:135], v[48:51]\n"
"s_add_u32 m0, s64, 12288\n"
"v_mfma_f32_16x16x32_bf16 v[76:79], a[44:47], a[132:135], v[76:79]\n"
"s_waitcnt vmcnt(10)\n"
"ds_read_b64_tr_b16 a[64:65], v9 offset:0\n"
"v_mfma_f32_16x16x32_bf16 v[76:79], a[40:43], a[128:131], v[76:79]\n"
"ds_read_b64_tr_b16 a[66:67], v9 offset:512\n"
"v_mfma_f32_16x16x32_bf16 v[80:83], a[40:43], a[136:139], v[80:83]\n"
"s_add_u32 s67, s75, 851968\n"
"ds_read_b64_tr_b16 a[68:69], v9 offset:4096\n"
"v_mfma_f32_16x16x32_bf16 v[80:83], a[44:47], a[140:143], v[80:83]\n"
"ds_read_b64_tr_b16 a[70:71], v9 offset:4608\n"
"v_mfma_f32_16x16x32_bf16 v[52:55], a[36:39], a[140:143], v[52:55]\n"
"ds_read_b64_tr_b16 a[72:73], v9 offset:8\n"
"buffer_load_dwordx4 v11, s[52:55], s67 offen offset:0 lds\n"
"v_mfma_f32_16x16x32_bf16 v[52:55], a[32:35], a[136:139], v[52:55]\n"
"s_waitcnt lgkmcnt(5)\n"
"ds_read_b64_tr_b16 a[74:75], v9 offset:520\n"
"v_mfma_f32_16x16x32_bf16 v[56:59], a[32:35], a[144:147], v[56:59]\n"
"s_add_u32 s67, s75, 867328\n"
"ds_read_b64_tr_b16 a[76:77], v9 offset:4104\n"
"v_mfma_f32_16x16x32_bf16 v[56:59], a[36:39], a[148:151], v[56:59]\n"
"ds_read_b64_tr_b16 a[78:79], v9 offset:4616\n"
"v_mfma_f32_16x16x32_bf16 v[84:87], a[44:47], a[148:151], v[84:87]\n"
"buffer_load_dwordx4 v11, s[52:55], s67 offen offset:1024 lds\n"
"v_mfma_f32_16x16x32_bf16 v[84:87], a[40:43], a[144:147], v[84:87]\n"
"ds_read_b64_tr_b16 a[80:81], v15 offset:0\n"
"v_mfma_f32_16x16x32_bf16 a[4:7], a[40:43], a[152:155], a[4:7]\n"
"s_add_u32 s67, s75, 882688\n"
"ds_read_b64_tr_b16 a[82:83], v15 offset:512\n"
"v_mfma_f32_16x16x32_bf16 a[4:7], a[44:47], a[156:159], a[4:7]\n"
"ds_read_b64_tr_b16 a[84:85], v15 offset:4096\n"
"v_mfma_f32_16x16x32_bf16 a[0:3], a[36:39], a[156:159], a[0:3]\n"
"ds_read_b64_tr_b16 a[86:87], v15 offset:4608\n"
"buffer_load_dwordx4 v11, s[52:55], s67 offen offset:2048 lds\n"
"v_mfma_f32_16x16x32_bf16 a[0:3], a[32:35], a[152:155], a[0:3]\n"
"ds_read_b64_tr_b16 a[88:89], v15 offset:8\n"
"v_mfma_f32_16x16x32_bf16 v[88:91], a[48:51], a[96:99], v[88:91]\n"
"s_add_u32 s67, s75, 898048\n"
"ds_read_b64_tr_b16 a[90:91], v15 offset:520\n"
"v_mfma_f32_16x16x32_bf16 v[88:91], a[52:55], a[100:103], v[88:91]\n"
"ds_read_b64_tr_b16 a[92:93], v15 offset:4104\n"
"v_mfma_f32_16x16x32_bf16 v[116:119], a[60:63], a[100:103], v[116:119]\n"
"ds_read_b64_tr_b16 a[94:95], v15 offset:4616\n"
"v_mfma_f32_16x16x32_bf16 v[116:119], a[56:59], a[96:99], v[116:119]\n"
"buffer_load_dwordx4 v11, s[52:55], s67 offen offset:3072 lds\n"
"v_mfma_f32_16x16x32_bf16 v[120:123], a[56:59], a[104:107], v[120:123]\n"
"v_mfma_f32_16x16x32_bf16 v[120:123], a[60:63], a[108:111], v[120:123]\n"
"s_add_u32 m0, s64, 0\n"
"v_mfma_f32_16x16x32_bf16 v[92:95], a[52:55], a[108:111], v[92:95]\n"
"v_mfma_f32_16x16x32_bf16 v[92:95], a[48:51], a[104:107], v[92:95]\n"
"s_add_u32 s67, s75, 786560\n"
"v_mfma_f32_16x16x32_bf16 v[96:99], a[48:51], a[112:115], v[96:99]\n"
"s_waitcnt lgkmcnt(0)\n"
"v_mfma_f32_16x16x32_bf16 v[96:99], a[52:55], a[116:119], v[96:99]\n"
"buffer_load_dwordx4 v11, s[52:55], s67 offen offset:0 lds\n"
"v_mfma_f32_16x16x32_bf16 v[124:127], a[60:63], a[116:119], v[124:127]\n"
"v_mfma_f32_16x16x32_bf16 v[124:127], a[56:59], a[112:115], v[124:127]\n"
"s_add_u32 s67, s75, 801920\n"
"v_mfma_f32_16x16x32_bf16 v[128:131], a[56:59], a[120:123], v[128:131]\n"
"v_mfma_f32_16x16x32_bf16 v[128:131], a[60:63], a[124:127], v[128:131]\n"
"v_mfma_f32_16x16x32_bf16 v[100:103], a[52:55], a[124:127], v[100:103]\n"
"buffer_load_dwordx4 v11, s[52:55], s67 offen offset:1024 lds\n"
"v_mfma_f32_16x16x32_bf16 v[100:103], a[48:51], a[120:123], v[100:103]\n"
"v_mfma_f32_16x16x32_bf16 v[104:107], a[48:51], a[128:131], v[104:107]\n"
"s_add_u32 s67, s75, 817280\n"
"v_mfma_f32_16x16x32_bf16 v[104:107], a[52:55], a[132:135], v[104:107]\n"
"v_mfma_f32_16x16x32_bf16 v[132:135], a[60:63], a[132:135], v[132:135]\n"
"buffer_load_dwordx4 v11, s[52:55], s67 offen offset:2048 lds\n"
"v_mfma_f32_16x16x32_bf16 v[132:135], a[56:59], a[128:131], v[132:135]\n"
"v_mfma_f32_16x16x32_bf16 v[136:139], a[56:59], a[136:139], v[136:139]\n"
"s_add_u32 s67, s75, 832640\n"
"v_mfma_f32_16x16x32_bf16 v[136:139], a[60:63], a[140:143], v[136:139]\n"
"v_mfma_f32_16x16x32_bf16 v[108:111], a[52:55], a[140:143], v[108:111]\n"
"v_mfma_f32_16x16x32_bf16 v[108:111], a[48:51], a[136:139], v[108:111]\n"
"buffer_load_dwordx4 v11, s[52:55], s67 offen offset:3072 lds\n"
"v_mfma_f32_16x16x32_bf16 v[112:115], a[48:51], a[144:147], v[112:115]\n"
"v_mfma_f32_16x16x32_bf16 v[112:115], a[52:55], a[148:151], v[112:115]\n"
"s_add_u32 m0, s64, 4096\n"
"v_mfma_f32_16x16x32_bf16 v[140:143], a[60:63], a[148:151], v[140:143]\n"
"v_mfma_f32_16x16x32_bf16 v[140:143], a[56:59], a[144:147], v[140:143]\n"
"s_add_u32 s67, s75, 852096\n"
"v_mfma_f32_16x16x32_bf16 a[12:15], a[56:59], a[152:155], a[12:15]\n"
"v_mfma_f32_16x16x32_bf16 a[12:15], a[60:63], a[156:159], a[12:15]\n"
"buffer_load_dwordx4 v11, s[52:55], s67 offen offset:0 lds\n"
"v_mfma_f32_16x16x32_bf16 a[8:11], a[52:55], a[156:159], a[8:11]\n"
"v_mfma_f32_16x16x32_bf16 a[8:11], a[48:51], a[152:155], a[8:11]\n"
"s_add_u32 s67, s75, 867456\n"
"v_mfma_f32_16x16x32_bf16 v[144:147], a[64:67], a[96:99], v[144:147]\n"
"v_mfma_f32_16x16x32_bf16 v[144:147], a[68:71], a[100:103], v[144:147]\n"
"buffer_load_dwordx4 v11, s[52:55], s67 offen offset:1024 lds\n"
"v_mfma_f32_16x16x32_bf16 v[172:175], a[76:79], a[100:103], v[172:175]\n"
"v_mfma_f32_16x16x32_bf16 v[172:175], a[72:75], a[96:99], v[172:175]\n"
"s_add_u32 s67, s75, 882816\n"
"v_mfma_f32_16x16x32_bf16 v[176:179], a[72:75], a[104:107], v[176:179]\n"
"v_mfma_f32_16x16x32_bf16 v[176:179], a[76:79], a[108:111], v[176:179]\n"
"buffer_load_dwordx4 v11, s[52:55], s67 offen offset:2048 lds\n"
"v_mfma_f32_16x16x32_bf16 v[148:151], a[68:71], a[108:111], v[148:151]\n"
"v_mfma_f32_16x16x32_bf16 v[148:151], a[64:67], a[104:107], v[148:151]\n"
"s_add_u32 s67, s75, 898176\n"
"v_mfma_f32_16x16x32_bf16 v[152:155], a[64:67], a[112:115], v[152:155]\n"
"s_sub_u32 s66, 81920, s73\n"
"v_mfma_f32_16x16x32_bf16 v[152:155], a[68:71], a[116:119], v[152:155]\n"
"buffer_load_dwordx4 v11, s[52:55], s67 offen offset:3072 lds\n"
"v_mfma_f32_16x16x32_bf16 v[180:183], a[76:79], a[116:119], v[180:183]\n"
"v_add_u32 v12, s66, v7\n"
"v_mfma_f32_16x16x32_bf16 v[180:183], a[72:75], a[112:115], v[180:183]\n"
"s_add_u32 m0, s72, 139264\n"
"v_mfma_f32_16x16x32_bf16 v[184:187], a[72:75], a[120:123], v[184:187]\n"
"v_add_u32 v13, s66, v8\n"
"v_mfma_f32_16x16x32_bf16 v[184:187], a[76:79], a[124:127], v[184:187]\n"
"s_add_u32 s67, s60, 896\n"
"v_mfma_f32_16x16x32_bf16 v[156:159], a[68:71], a[124:127], v[156:159]\n"
"v_mfma_f32_16x16x32_bf16 v[156:159], a[64:67], a[120:123], v[156:159]\n"
"buffer_load_dwordx4 v6, s[48:51], s67 offen offset:0  lds\n"
"v_mfma_f32_16x16x32_bf16 v[160:163], a[64:67], a[128:131], v[160:163]\n"
"v_mfma_f32_16x16x32_bf16 v[160:163], a[68:71], a[132:135], v[160:163]\n"
"s_add_u32 s67, s60, 16256\n"
"v_mfma_f32_16x16x32_bf16 v[188:191], a[76:79], a[132:135], v[188:191]\n"
"v_mfma_f32_16x16x32_bf16 v[188:191], a[72:75], a[128:131], v[188:191]\n"
"buffer_load_dwordx4 v6, s[48:51], s67 offen offset:1024  lds\n"
"v_mfma_f32_16x16x32_bf16 v[192:195], a[72:75], a[136:139], v[192:195]\n"
"v_mfma_f32_16x16x32_bf16 v[192:195], a[76:79], a[140:143], v[192:195]\n"
"s_add_u32 s67, s60, 31616\n"
"v_mfma_f32_16x16x32_bf16 v[164:167], a[68:71], a[140:143], v[164:167]\n"
"v_mfma_f32_16x16x32_bf16 v[164:167], a[64:67], a[136:139], v[164:167]\n"
"buffer_load_dwordx4 v6, s[48:51], s67 offen offset:2048  lds\n"
"v_mfma_f32_16x16x32_bf16 v[168:171], a[64:67], a[144:147], v[168:171]\n"
"v_mfma_f32_16x16x32_bf16 v[168:171], a[68:71], a[148:151], v[168:171]\n"
"s_add_u32 s67, s60, 46976\n"
"v_mfma_f32_16x16x32_bf16 v[196:199], a[76:79], a[148:151], v[196:199]\n"
"v_mfma_f32_16x16x32_bf16 v[196:199], a[72:75], a[144:147], v[196:199]\n"
"v_mfma_f32_16x16x32_bf16 a[20:23], a[72:75], a[152:155], a[20:23]\n"
"buffer_load_dwordx4 v6, s[48:51], s67 offen offset:3072  lds\n"
"v_mfma_f32_16x16x32_bf16 a[20:23], a[76:79], a[156:159], a[20:23]\n"
"v_mfma_f32_16x16x32_bf16 a[16:19], a[68:71], a[156:159], a[16:19]\n"
"s_add_u32 m0, s64, 32640\n"
"v_mfma_f32_16x16x32_bf16 a[16:19], a[64:67], a[152:155], a[16:19]\n"
"s_waitcnt vmcnt(22)\n"
"s_barrier\n"
"v_mfma_f32_16x16x32_bf16 v[200:203], a[80:83], a[96:99], v[200:203]\n"
"s_waitcnt vmcnt(12)\n"
"ds_read_b64_tr_b16 a[32:33], v9 offset:8192\n"
"buffer_load_dwordx4 v6, s[40:43], s80 offen offset:128  lds\n"
"v_mfma_f32_16x16x32_bf16 v[200:203], a[84:87], a[100:103], v[200:203]\n"
"ds_read_b64_tr_b16 a[34:35], v9 offset:8704\n"
"v_mfma_f32_16x16x32_bf16 v[228:231], a[92:95], a[100:103], v[228:231]\n"
"ds_read_b128 a[100:103], v13 offset:16384\n"
"v_mfma_f32_16x16x32_bf16 v[228:231], a[88:91], a[96:99], v[228:231]\n"
"ds_read_b128 a[96:99], v12 offset:16384\n"
"v_mfma_f32_16x16x32_bf16 v[232:235], a[88:91], a[104:107], v[232:235]\n"
"ds_read_b64_tr_b16 a[36:37], v9 offset:12288\n"
"buffer_load_dwordx4 v6, s[40:43], s81 offen offset:1152  lds\n"
"v_mfma_f32_16x16x32_bf16 v[232:235], a[92:95], a[108:111], v[232:235]\n"
"ds_read_b64_tr_b16 a[38:39], v9 offset:12800\n"
"v_mfma_f32_16x16x32_bf16 v[204:207], a[84:87], a[108:111], v[204:207]\n"
"ds_read_b128 a[108:111], v13 offset:18432\n"
"v_mfma_f32_16x16x32_bf16 v[204:207], a[80:83], a[104:107], v[204:207]\n"
"ds_read_b128 a[104:107], v12 offset:18432\n"
"ds_read_b64_tr_b16 a[40:41], v9 offset:8200\n"
"v_mfma_f32_16x16x32_bf16 v[208:211], a[80:83], a[112:115], v[208:211]\n"
"ds_read_b64_tr_b16 a[42:43], v9 offset:8712\n"
"v_mfma_f32_16x16x32_bf16 v[208:211], a[84:87], a[116:119], v[208:211]\n"
"ds_read_b64_tr_b16 a[44:45], v9 offset:12296\n"
"buffer_load_dwordx4 v6, s[40:43], s82 offen offset:2176  lds\n"
"v_mfma_f32_16x16x32_bf16 v[236:239], a[92:95], a[116:119], v[236:239]\n"
"ds_read_b128 a[116:119], v13 offset:20480\n"
"v_mfma_f32_16x16x32_bf16 v[236:239], a[88:91], a[112:115], v[236:239]\n"
"ds_read_b128 a[112:115], v12 offset:20480\n"
"v_mfma_f32_16x16x32_bf16 v[240:243], a[88:91], a[120:123], v[240:243]\n"
"ds_read_b64_tr_b16 a[46:47], v9 offset:12808\n"
"v_mfma_f32_16x16x32_bf16 v[240:243], a[92:95], a[124:127], v[240:243]\n"
"ds_read_b64_tr_b16 a[48:49], v15 offset:8192\n"
"buffer_load_dwordx4 v6, s[40:43], s83 offen offset:3200  lds\n"
"v_mfma_f32_16x16x32_bf16 v[212:215], a[84:87], a[124:127], v[212:215]\n"
"ds_read_b128 a[124:127], v13 offset:22528\n"
"v_mfma_f32_16x16x32_bf16 v[212:215], a[80:83], a[120:123], v[212:215]\n"
"ds_read_b128 a[120:123], v12 offset:22528\n"
"v_mfma_f32_16x16x32_bf16 v[216:219], a[80:83], a[128:131], v[216:219]\n"
"ds_read_b64_tr_b16 a[50:51], v15 offset:8704\n"
"v_mfma_f32_16x16x32_bf16 v[216:219], a[84:87], a[132:135], v[216:219]\n"
"ds_read_b64_tr_b16 a[52:53], v15 offset:12288\n"
"v_mfma_f32_16x16x32_bf16 v[244:247], a[92:95], a[132:135], v[244:247]\n"
"ds_read_b128 a[132:135], v13 offset:24576\n"
"buffer_store_dword v16, v17, s[56:59], %11 offen sc1\n"
"v_mfma_f32_16x16x32_bf16 v[244:247], a[88:91], a[128:131], v[244:247]\n"
"ds_read_b128 a[128:131], v12 offset:24576\n"
"v_mfma_f32_16x16x32_bf16 v[248:251], a[88:91], a[136:139], v[248:251]\n"
"ds_read_b64_tr_b16 a[54:55], v15 offset:12800\n"
"v_mfma_f32_16x16x32_bf16 v[248:251], a[92:95], a[140:143], v[248:251]\n"
"ds_read_b64_tr_b16 a[56:57], v15 offset:8200\n"
"v_mfma_f32_16x16x32_bf16 v[220:223], a[84:87], a[140:143], v[220:223]\n"
"ds_read_b128 a[140:143], v13 offset:26624\n"
"buffer_store_dword v16, v17, s[56:59], s69 offen sc1\n"
"v_mfma_f32_16x16x32_bf16 v[220:223], a[80:83], a[136:139], v[220:223]\n"
"ds_read_b128 a[136:139], v12 offset:26624\n"
"v_mfma_f32_16x16x32_bf16 v[224:227], a[80:83], a[144:147], v[224:227]\n"
"s_add_u32 s66, s78, 1\n"
"ds_read_b64_tr_b16 a[58:59], v15 offset:8712\n"
"v_mfma_f32_16x16x32_bf16 v[224:227], a[84:87], a[148:151], v[224:227]\n"
"s_add_u32 m0, s64, 8192\n"
"ds_read_b64_tr_b16 a[60:61], v15 offset:12296\n"
"v_mfma_f32_16x16x32_bf16 v[252:255], a[92:95], a[148:151], v[252:255]\n"
"ds_read_b128 a[148:151], v13 offset:28672\n"
"v_mov_b32 v28, s66\n"
"v_mfma_f32_16x16x32_bf16 v[252:255], a[88:91], a[144:147], v[252:255]\n"
"ds_read_b128 a[144:147], v12 offset:28672\n"
"ds_read_b64_tr_b16 a[62:63], v15 offset:12808\n"
"v_mov_b32 v29, s79\n"
"v_mfma_f32_16x16x32_bf16 a[28:31], a[88:91], a[152:155], a[28:31]\n"
"v_mfma_f32_16x16x32_bf16 a[28:31], a[92:95], a[156:159], a[28:31]\n"
"buffer_store_dwordx2 v[28:29], off, s[56:59], %24 sc1\n"
"s_add_u32 s67, s75, 917504\n"
"v_mfma_f32_16x16x32_bf16 a[24:27], a[84:87], a[156:159], a[24:27]\n"
"ds_read_b128 a[156:159], v13 offset:30720\n"
"v_mfma_f32_16x16x32_bf16 a[24:27], a[80:83], a[152:155], a[24:27]\n"
"s_waitcnt lgkmcnt(15)\n"
"ds_read_b128 a[152:155], v12 offset:30720\n"
"v_mfma_f32_16x16x32_bf16 v[32:35], a[32:35], a[96:99], v[32:35]\n"
"v_mfma_f32_16x16x32_bf16 v[32:35], a[36:39], a[100:103], v[32:35]\n"
"s_waitcnt lgkmcnt(2)\n"
"L_H6:\n"
"v_mfma_f32_16x16x32_bf16 v[60:63], a[44:47], a[100:103], v[60:63]\n"
"buffer_load_dwordx4 v11, s[52:55], s67 offen offset:0 lds\n"
"v_mfma_f32_16x16x32_bf16 v[60:63], a[40:43], a[96:99], v[60:63]\n"
"v_mfma_f32_16x16x32_bf16 v[64:67], a[40:43], a[104:107], v[64:67]\n"
"s_add_u32 s67, s75, 932864\n"
"v_mfma_f32_16x16x32_bf16 v[64:67], a[44:47], a[108:111], v[64:67]\n"
"v_mfma_f32_16x16x32_bf16 v[36:39], a[36:39], a[108:111], v[36:39]\n"
"v_mfma_f32_16x16x32_bf16 v[36:39], a[32:35], a[104:107], v[36:39]\n"
"buffer_load_dwordx4 v11, s[52:55], s67 offen offset:1024 lds\n"
"v_mfma_f32_16x16x32_bf16 v[40:43], a[32:35], a[112:115], v[40:43]\n"
"v_mfma_f32_16x16x32_bf16 v[40:43], a[36:39], a[116:119], v[40:43]\n"
"s_add_u32 s67, s75, 948224\n"
"v_mfma_f32_16x16x32_bf16 v[68:71], a[44:47], a[116:119], v[68:71]\n"
"v_mfma_f32_16x16x32_bf16 v[68:71], a[40:43], a[112:115], v[68:71]\n"
"buffer_load_dwordx4 v11, s[52:55], s67 offen offset:2048 lds\n"
"v_mfma_f32_16x16x32_bf16 v[72:75], a[40:43], a[120:123], v[72:75]\n"
"v_mfma_f32_16x16x32_bf16 v[72:75], a[44:47], a[124:127], v[72:75]\n"
"s_add_u32 s67, s75, 963584\n"
"v_mfma_f32_16x16x32_bf16 v[44:47], a[36:39], a[124:127], v[44:47]\n"
"v_mfma_f32_16x16x32_bf16 v[44:47], a[32:35], a[120:123], v[44:47]\n"
"v_mfma_f32_16x16x32_bf16 v[48:51], a[32:35], a[128:131], v[48:51]\n"
"buffer_load_dwordx4 v11, s[52:55], s67 offen offset:3072 lds\n"
"v_mfma_f32_16x16x32_bf16 v[48:51], a[36:39], a[132:135], v[48:51]\n"
"s_add_u32 m0, s64, 12288\n"
"v_mfma_f32_16x16x32_bf16 v[76:79], a[44:47], a[132:135], v[76:79]\n"
"s_waitcnt vmcnt(15)\n"
"ds_read_b64_tr_b16 a[64:65], v9 offset:0\n"
"v_mfma_f32_16x16x32_bf16 v[76:79], a[40:43], a[128:131], v[76:79]\n"
"s_add_u32 s67, s75, 983040\n"
"ds_read_b64_tr_b16 a[66:67], v9 offset:512\n"
"v_mfma_f32_16x16x32_bf16 v[80:83], a[40:43], a[136:139], v[80:83]\n"
"ds_read_b64_tr_b16 a[68:69], v9 offset:4096\n"
"v_mfma_f32_16x16x32_bf16 v[80:83], a[44:47], a[140:143], v[80:83]\n"
"ds_read_b64_tr_b16 a[70:71], v9 offset:4608\n"
"buffer_load_dwordx4 v11, s[52:55], s67 offen offset:0 lds\n"
"v_mfma_f32_16x16x32_bf16 v[52:55], a[36:39], a[140:143], v[52:55]\n"
"s_add_u32 s67, s75, 998400\n"
"ds_read_b64_tr_b16 a[72:73], v9 offset:8\n"
"v_mfma_f32_16x16x32_bf16 v[52:55], a[32:35], a[136:139], v[52:55]\n"
"s_waitcnt lgkmcnt(5)\n"
"ds_read_b64_tr_b16 a[74:75], v9 offset:520\n"
"v_mfma_f32_16x16x32_bf16 v[56:59], a[32:35], a[144:147], v[56:59]\n"
"ds_read_b64_tr_b16 a[76:77], v9 offset:4104\n"
"v_mfma_f32_16x16x32_bf16 v[56:59], a[36:39], a[148:151], v[56:59]\n"
"ds_read_b64_tr_b16 a[78:79], v9 offset:4616\n"
"v_mfma_f32_16x16x32_bf16 v[84:87], a[44:47], a[148:151], v[84:87]\n"
"buffer_load_dwordx4 v11, s[52:55], s67 offen offset:1024 lds\n"
"v_mfma_f32_16x16x32_bf16 v[84:87], a[40:43], a[144:147], v[84:87]\n"
"ds_read_b64_tr_b16 a[80:81], v15 offset:0\n"
"v_mfma_f32_16x16x32_bf16 a[4:7], a[40:43], a[152:155], a[4:7]\n"
"s_add_u32 s67, s75, 1013760\n"
"ds_read_b64_tr_b16 a[82:83], v15 offset:512\n"
"v_mfma_f32_16x16x32_bf16 a[4:7], a[44:47], a[156:159], a[4:7]\n"
"ds_read_b64_tr_b16 a[84:85], v15 offset:4096\n"
"v_mfma_f32_16x16x32_bf16 a[0:3], a[36:39], a[156:159], a[0:3]\n"
"ds_read_b64_tr_b16 a[86:87], v15 offset:4608\n"
"buffer_load_dwordx4 v11, s[52:55], s67 offen offset:2048 lds\n"
"v_mfma_f32_16x16x32_bf16 a[0:3], a[32:35], a[152:155], a[0:3]\n"
"ds_read_b64_tr_b16 a[88:89], v15 offset:8\n"
"v_mfma_f32_16x16x32_bf16 v[88:91], a[48:51], a[96:99], v[88:91]\n"
"s_add_u32 s67, s75, 1029120\n"
"ds_read_b64_tr_b16 a[90:91], v15 offset:520\n"
"v_mfma_f32_16x16x32_bf16 v[88:91], a[52:55], a[100:103], v[88:91]\n"
"ds_read_b64_tr_b16 a[92:93], v15 offset:4104\n"
"v_mfma_f32_16x16x32_bf16 v[116:119], a[60:63], a[100:103], v[116:119]\n"
"ds_read_b64_tr_b16 a[94:95], v15 offset:4616\n"
"v_mfma_f32_16x16x32_bf16 v[116:119], a[56:59], a[96:99], v[116:119]\n"
"buffer_load_dwordx4 v11, s[52:55], s67 offen offset:3072 lds\n"
"v_mfma_f32_16x16x32_bf16 v[120:123], a[56:59], a[104:107], v[120:123]\n"
"v_mfma_f32_16x16x32_bf16 v[120:123], a[60:63], a[108:111], v[120:123]\n"
"s_add_u32 m0, s64, 0\n"
"v_mfma_f32_16x16x32_bf16 v[92:95], a[52:55], a[108:111], v[92:95]\n"
"v_mfma_f32_16x16x32_bf16 v[92:95], a[48:51], a[104:107], v[92:95]\n"
"s_add_u32 s67, s75, 917632\n"
"v_mfma_f32_16x16x32_bf16 v[96:99], a[48:51], a[112:115], v[96:99]\n"
"s_waitcnt lgkmcnt(0)\n"
"v_mfma_f32_16x16x32_bf16 v[96:99], a[52:55], a[116:119], v[96:99]\n"
"buffer_load_dwordx4 v11, s[52:55], s67 offen offset:0 lds\n"
"v_mfma_f32_16x16x32_bf16 v[124:127], a[60:63], a[116:119], v[124:127]\n"
"v_mfma_f32_16x16x32_bf16 v[124:127], a[56:59], a[112:115], v[124:127]\n"
"s_add_u32 s67, s75, 932992\n"
"v_mfma_f32_16x16x32_bf16 v[128:131], a[56:59], a[120:123], v[128:131]\n"
"v_mfma_f32_16x16x32_bf16 v[128:131], a[60:63], a[124:127], v[128:131]\n"
"buffer_load_dwordx4 v11, s[52:55], s67 offen offset:1024 lds\n"
"v_mfma_f32_16x16x32_bf16 v[100:103], a[52:55], a[124:127], v[100:103]\n"
"v_mfma_f32_16x16x32_bf16 v[100:103], a[48:51], a[120:123], v[100:103]\n"
"s_add_u32 s67, s75, 948352\n"
"v_mfma_f32_16x16x32_bf16 v[104:107], a[48:51], a[128:131], v[104:107]\n"
"v_mfma_f32_16x16x32_bf16 v[104:107], a[52:55], a[132:135], v[104:107]\n"
"v_mfma_f32_16x16x32_bf16 v[132:135], a[60:63], a[132:135], v[132:135]\n"
"buffer_load_dwordx4 v11, s[52:55], s67 offen offset:2048 lds\n"
"v_mfma_f32_16x16x32_bf16 v[132:135], a[56:59], a[128:131], v[132:135]\n"
"v_mfma_f32_16x16x32_bf16 v[136:139], a[56:59], a[136:139], v[136:139]\n"
"s_add_u32 s67, s75, 963712\n"
"v_mfma_f32_16x16x32_bf16 v[136:139], a[60:63], a[140:143], v[136:139]\n"
"v_mfma_f32_16x16x32_bf16 v[108:111], a[52:55], a[140:143], v[108:111]\n"
"v_mfma_f32_16x16x32_bf16 v[108:111], a[48:51], a[136:139], v[108:111]\n"
"buffer_load_dwordx4 v11, s[52:55], s67 offen offset:3072 lds\n"
"v_mfma_f32_16x16x32_bf16 v[112:115], a[48:51], a[144:147], v[112:115]\n"
"s_add_u32 m0, s64, 4096\n"
"v_mfma_f32_16x16x32_bf16 v[112:115], a[52:55], a[148:151], v[112:115]\n"
"v_mfma_f32_16x16x32_bf16 v[140:143], a[60:63], a[148:151], v[140:143]\n"
"s_add_u32 s67, s75, 983168\n"
"v_mfma_f32_16x16x32_bf16 v[140:143], a[56:59], a[144:147], v[140:143]\n"
"v_mfma_f32_16x16x32_bf16 a[12:15], a[56:59], a[152:155], a[12:15]\n"
"buffer_load_dwordx4 v11, s[52:55], s67 offen offset:0 lds\n"
"v_mfma_f32_16x16x32_bf16 a[12:15], a[60:63], a[156:159], a[12:15]\n"
"s_sub_u32 s71, s64, s64\n"
"v_mfma_f32_16x16x32_bf16 a[8:11], a[52:55], a[156:159], a[8:11]\n"
"s_add_u32 s67, s75, 998528\n"
"v_mfma_f32_16x16x32_bf16 a[8:11], a[48:51], a[152:155], a[8:11]\n"
"v_mfma_f32_16x16x32_bf16 v[144:147], a[64:67], a[96:99], v[144:147]\n"
"s_add_u32 s71, s71, 32768\n"
"v_mfma_f32_16x16x32_bf16 v[144:147], a[68:71], a[100:103], v[144:147]\n"
"buffer_load_dwordx4 v11, s[52:55], s67 offen offset:1024 lds\n"
"v_mfma_f32_16x16x32_bf16 v[172:175], a[76:79], a[100:103], v[172:175]\n"
"v_add_u32 v20, s71, v7\n"
"v_mfma_f32_16x16x32_bf16 v[172:175], a[72:75], a[96:99], v[172:175]\n"
"s_add_u32 s67, s75, 1013888\n"
"v_mfma_f32_16x16x32_bf16 v[176:179], a[72:75], a[104:107], v[176:179]\n"
"buffer_load_dwordx4 v11, s[52:55], s67 offen offset:2048 lds\n"
"v_mfma_f32_16x16x32_bf16 v[176:179], a[76:79], a[108:111], v[176:179]\n"
"v_add_u32 v21, s71, v8\n"
"v_mfma_f32_16x16x32_bf16 v[148:151], a[68:71], a[108:111], v[148:151]\n"
"s_add_u32 s67, s75, 1029248\n"
"v_mfma_f32_16x16x32_bf16 v[148:151], a[64:67], a[104:107], v[148:151]\n"
"v_mfma_f32_16x16x32_bf16 v[152:155], a[64:67], a[112:115], v[152:155]\n"
"s_sub_u32 s66, 122880, s73\n"
"v_mfma_f32_16x16x32_bf16 v[152:155], a[68:71], a[116:119], v[152:155]\n"
"s_waitcnt vmcnt(18)\n"
"ds_read_b128 a[192:195], v20 offset:0\n"
"buffer_load_dwordx4 v11, s[52:55], s67 offen offset:3072 lds\n"
"v_mfma_f32_16x16x32_bf16 v[180:183], a[76:79], a[116:119], v[180:183]\n"
"ds_read_b128 a[196:199], v21 offset:0\n"
"v_add_u32 v12, s66, v7\n"
"v_mfma_f32_16x16x32_bf16 v[180:183], a[72:75], a[112:115], v[180:183]\n"
"s_add_u32 m0, s72, 57344\n"
"ds_read_b128 a[200:203], v20 offset:2048\n"
"v_mfma_f32_16x16x32_bf16 v[184:187], a[72:75], a[120:123], v[184:187]\n"
"v_add_u32 v13, s66, v8\n"
"v_mfma_f32_16x16x32_bf16 v[184:187], a[76:79], a[124:127], v[184:187]\n"
"s_add_u32 s67, %21, 0\n"
"ds_read_b128 a[204:207], v21 offset:2048\n"
"v_mfma_f32_16x16x32_bf16 v[156:159], a[68:71], a[124:127], v[156:159]\n"
"v_mfma_f32_16x16x32_bf16 v[156:159], a[64:67], a[120:123], v[156:159]\n"
"buffer_load_dwordx4 v11, s[52:55], s67 offen offset:0 nt lds\n"
"v_mfma_f32_16x16x32_bf16 v[160:163], a[64:67], a[128:131], v[160:163]\n"
"v_mfma_f32_16x16x32_bf16 v[160:163], a[68:71], a[132:135], v[160:163]\n"
"s_add_u32 s67, %21, 15360\n"
"v_mfma_f32_16x16x32_bf16 v[188:191], a[76:79], a[132:135], v[188:191]\n"
"v_mfma_f32_16x16x32_bf16 v[188:191], a[72:75], a[128:131], v[188:191]\n"
"s_waitcnt lgkmcnt(0)\n"
"buffer_load_dwordx4 v11, s[52:55], s67 offen offset:1024 nt lds\n"
"v_mfma_f32_16x16x32_bf16 v[192:195], a[72:75], a[136:139], v[192:195]\n"
"v_mfma_f32_16x16x32_bf16 v[192:195], a[76:79], a[140:143], v[192:195]\n"
"s_add_u32 s67, %21, 30720\n"
"v_mfma_f32_16x16x32_bf16 v[164:167], a[68:71], a[140:143], v[164:167]\n"
"v_mfma_f32_16x16x32_bf16 v[164:167], a[64:67], a[136:139], v[164:167]\n"
"buffer_load_dwordx4 v11, s[52:55], s67 offen offset:2048 nt lds\n"
"v_mfma_f32_16x16x32_bf16 v[168:171], a[64:67], a[144:147], v[168:171]\n"
"v_mfma_f32_16x16x32_bf16 v[168:171], a[68:71], a[148:151], v[168:171]\n"
"s_add_u32 s67, %21, 46080\n"
"v_mfma_f32_16x16x32_bf16 v[196:199], a[76:79], a[148:151], v[196:199]\n"
"v_mfma_f32_16x16x32_bf16 v[196:199], a[72:75], a[144:147], v[196:199]\n"
"buffer_load_dwordx4 v11, s[52:55], s67 offen offset:3072 nt lds\n"
"v_mfma_f32_16x16x32_bf16 a[20:23], a[72:75], a[152:155], a[20:23]\n"
"s_cmp_lt_u32 %16, 2\n"
"s_cselect_b32 s67, 16384, 0\n"
"v_mfma_f32_16x16x32_bf16 a[20:23], a[76:79], a[156:159], a[20:23]\n"
"v_mfma_f32_16x16x32_bf16 a[16:19], a[68:71], a[156:159], a[16:19]\n"
"s_add_u32 s67, s67, s72\n"
"s_add_u32 m0, s67, 16384\n"
"v_mfma_f32_16x16x32_bf16 a[16:19], a[64:67], a[152:155], a[16:19]\n"
"s_barrier\n"
"s_waitcnt vmcnt(12)\n"
"ds_read_b64_tr_b16 a[32:33], v9 offset:8192\n"
"v_mfma_f32_16x16x32_bf16 v[200:203], a[80:83], a[96:99], v[200:203]\n"
"s_sub_u32 s67, 1024, s60\n"
"ds_read_b64_tr_b16 a[34:35], v9 offset:8704\n"
"v_mfma_f32_16x16x32_bf16 v[200:203], a[84:87], a[100:103], v[200:203]\n"
"ds_read_b64_tr_b16 a[36:37], v9 offset:12288\n"
"v_mfma_f32_16x16x32_bf16 v[228:231], a[92:95], a[100:103], v[228:231]\n"
"ds_read_b128 a[100:103], v13 offset:16384\n"
"buffer_load_dwordx4 v6, s[48:51], s67 offen offset:0  lds\n"
"v_mfma_f32_16x16x32_bf16 v[228:231], a[88:91], a[96:99], v[228:231]\n"
"ds_read_b128 a[96:99], v12 offset:16384\n"
"v_mfma_f32_16x16x32_bf16 v[232:235], a[88:91], a[104:107], v[232:235]\n"
"s_sub_u32 s67, 16384, s60\n"
"ds_read_b64_tr_b16 a[38:39], v9 offset:12800\n"
"v_mfma_f32_16x16x32_bf16 v[232:235], a[92:95], a[108:111], v[232:235]\n"
"ds_read_b64_tr_b16 a[40:41], v9 offset:8200\n"
"v_mfma_f32_16x16x32_bf16 v[204:207], a[84:87], a[108:111], v[204:207]\n"
"ds_read_b128 a[108:111], v13 offset:18432\n"
"buffer_load_dwordx4 v6, s[48:51], s67 offen offset:1024  lds\n"
"v_mfma_f32_16x16x32_bf16 v[204:207], a[80:83], a[104:107], v[204:207]\n"
"ds_read_b128 a[104:107], v12 offset:18432\n"
"v_mfma_f32_16x16x32_bf16 v[208:211], a[80:83], a[112:115], v[208:211]\n"
"s_sub_u32 s67, 31744, s60\n"
"ds_read_b64_tr_b16 a[42:43], v9 offset:8712\n"
"v_mfma_f32_16x16x32_bf16 v[208:211], a[84:87], a[116:119], v[208:211]\n"
"ds_read_b64_tr_b16 a[44:45], v9 offset:12296\n"
"v_mfma_f32_16x16x32_bf16 v[236:239], a[92:95], a[116:119], v[236:239]\n"
"ds_read_b128 a[116:119], v13 offset:20480\n"
"buffer_load_dwordx4 v6, s[48:51], s67 offen offset:2048  lds\n"
"v_mfma_f32_16x16x32_bf16 v[236:239], a[88:91], a[112:115], v[236:239]\n"
"ds_read_b128 a[112:115], v12 offset:20480\n"
"v_mfma_f32_16x16x32_bf16 v[240:243], a[88:91], a[120:123], v[240:243]\n"
"s_sub_u32 s67, 47104, s60\n"
"ds_read_b64_tr_b16 a[46:47], v9 offset:12808\n"
"v_mfma_f32_16x16x32_bf16 v[240:243], a[92:95], a[124:127], v[240:243]\n"
"ds_read_b64_tr_b16 a[48:49], v15 offset:8192\n"
"v_mfma_f32_16x16x32_bf16 v[212:215], a[84:87], a[124:127], v[212:215]\n"
"ds_read_b128 a[124:127], v13 offset:22528\n"
"v_mfma_f32_16x16x32_bf16 v[212:215], a[80:83], a[120:123], v[212:215]\n"
"ds_read_b128 a[120:123], v12 offset:22528\n"
"buffer_load_dwordx4 v6, s[48:51], s67 offen offset:3072  lds\n"
"v_mfma_f32_16x16x32_bf16 v[216:219], a[80:83], a[128:131], v[216:219]\n"
"ds_read_b64_tr_b16 a[50:51], v15 offset:8704\n"
"v_mfma_f32_16x16x32_bf16 v[216:219], a[84:87], a[132:135], v[216:219]\n"
"ds_read_b64_tr_b16 a[52:53], v15 offset:12288\n"
"v_mfma_f32_16x16x32_bf16 v[244:247], a[92:95], a[132:135], v[244:247]\n"
"ds_read_b128 a[132:135], v13 offset:24576\n"
"v_mfma_f32_16x16x32_bf16 v[244:247], a[88:91], a[128:131], v[244:247]\n"
"s_add_u32 m0, s64, 8192\n"
"ds_read_b128 a[128:131], v12 offset:24576\n"
"v_mfma_f32_16x16x32_bf16 v[248:251], a[88:91], a[136:139], v[248:251]\n"
"ds_read_b64_tr_b16 a[54:55], v15 offset:12800\n"
"v_mfma_f32_16x16x32_bf16 v[248:251], a[92:95], a[140:143], v[248:251]\n"
"ds_read_b64_tr_b16 a[56:57], v15 offset:8200\n"
"v_mfma_f32_16x16x32_bf16 v[220:223], a[84:87], a[140:143], v[220:223]\n"
"ds_read_b128 a[140:143], v13 offset:26624\n"
"v_mfma_f32_16x16x32_bf16 v[220:223], a[80:83], a[136:139], v[220:223]\n"
"ds_read_b128 a[136:139], v12 offset:26624\n"
"v_mfma_f32_16x16x32_bf16 v[224:227], a[80:83], a[144:147], v[224:227]\n"
"ds_read_b64_tr_b16 a[58:59], v15 offset:8712\n"
"v_mfma_f32_16x16x32_bf16 v[224:227], a[84:87], a[148:151], v[224:227]\n"
"ds_read_b64_tr_b16 a[60:61], v15 offset:12296\n"
"v_mfma_f32_16x16x32_bf16 v[252:255], a[92:95], a[148:151], v[252:255]\n"
"s_sub_u32 s67, 1048576, s75\n"
"ds_read_b128 a[148:151], v13 offset:28672\n"
"v_mfma_f32_16x16x32_bf16 v[252:255], a[88:91], a[144:147], v[252:255]\n"
"ds_read_b128 a[144:147], v12 offset:28672\n"
"ds_read_b64_tr_b16 a[62:63], v15 offset:12808\n"
"v_mfma_f32_16x16x32_bf16 a[28:31], a[88:91], a[152:155], a[28:31]\n"
"v_mfma_f32_16x16x32_bf16 a[28:31], a[92:95], a[156:159], a[28:31]\n"
"v_mfma_f32_16x16x32_bf16 a[24:27], a[84:87], a[156:159], a[24:27]\n"
"ds_read_b128 a[156:159], v13 offset:30720\n"
"v_mfma_f32_16x16x32_bf16 a[24:27], a[80:83], a[152:155], a[24:27]\n"
"s_waitcnt lgkmcnt(15)\n"
"ds_read_b128 a[152:155], v12 offset:30720\n"
"v_mfma_f32_16x16x32_bf16 v[32:35], a[32:35], a[96:99], v[32:35]\n"
"v_mfma_f32_16x16x32_bf16 v[32:35], a[36:39], a[100:103], v[32:35]\n"
"s_waitcnt lgkmcnt(2)\n"
"v_mfma_f32_16x16x32_bf16 v[60:63], a[44:47], a[100:103], v[60:63]\n"
"buffer_load_dwordx4 v11, s[52:55], s67 offen offset:0 lds\n"
"v_mfma_f32_16x16x32_bf16 v[60:63], a[40:43], a[96:99], v[60:63]\n"
"v_mfma_f32_16x16x32_bf16 v[64:67], a[40:43], a[104:107], v[64:67]\n"
"s_sub_u32 s67, 1063936, s75\n"
"v_mfma_f32_16x16x32_bf16 v[64:67], a[44:47], a[108:111], v[64:67]\n"
"v_mfma_f32_16x16x32_bf16 v[36:39], a[36:39], a[108:111], v[36:39]\n"
"v_mfma_f32_16x16x32_bf16 v[36:39], a[32:35], a[104:107], v[36:39]\n"
"buffer_load_dwordx4 v11, s[52:55], s67 offen offset:1024 lds\n"
"v_mfma_f32_16x16x32_bf16 v[40:43], a[32:35], a[112:115], v[40:43]\n"
"v_mfma_f32_16x16x32_bf16 v[40:43], a[36:39], a[116:119], v[40:43]\n"
"s_sub_u32 s67, 1079296, s75\n"
"v_mfma_f32_16x16x32_bf16 v[68:71], a[44:47], a[116:119], v[68:71]\n"
"v_mfma_f32_16x16x32_bf16 v[68:71], a[40:43], a[112:115], v[68:71]\n"
"v_mfma_f32_16x16x32_bf16 v[72:75], a[40:43], a[120:123], v[72:75]\n"
"buffer_load_dwordx4 v11, s[52:55], s67 offen offset:2048 lds\n"
"v_mfma_f32_16x16x32_bf16 v[72:75], a[44:47], a[124:127], v[72:75]\n"
"v_mfma_f32_16x16x32_bf16 v[44:47], a[36:39], a[124:127], v[44:47]\n"
"s_sub_u32 s67, 1094656, s75\n"
"v_mfma_f32_16x16x32_bf16 v[44:47], a[32:35], a[120:123], v[44:47]\n"
"v_mfma_f32_16x16x32_bf16 v[48:51], a[32:35], a[128:131], v[48:51]\n"
"buffer_load_dwordx4 v11, s[52:55], s67 offen offset:3072 lds\n"
"v_mfma_f32_16x16x32_bf16 v[48:51], a[36:39], a[132:135], v[48:51]\n"
"s_add_u32 m0, s64, 12288\n"
"v_mfma_f32_16x16x32_bf16 v[76:79], a[44:47], a[132:135], v[76:79]\n"
"s_waitcnt vmcnt(12)\n"
"ds_read_b64_tr_b16 a[64:65], v9 offset:0\n"
"v_mfma_f32_16x16x32_bf16 v[76:79], a[40:43], a[128:131], v[76:79]\n"
"ds_read_b64_tr_b16 a[66:67], v9 offset:512\n"
"v_mfma_f32_16x16x32_bf16 v[80:83], a[40:43], a[136:139], v[80:83]\n"
"s_sub_u32 s67, 1114112, s75\n"
"ds_read_b64_tr_b16 a[68:69], v9 offset:4096\n"
"v_mfma_f32_16x16x32_bf16 v[80:83], a[44:47], a[140:143], v[80:83]\n"
"ds_read_b64_tr_b16 a[70:71], v9 offset:4608\n"
"v_mfma_f32_16x16x32_bf16 v[52:55], a[36:39], a[140:143], v[52:55]\n"
"ds_read_b64_tr_b16 a[72:73], v9 offset:8\n"
"buffer_load_dwordx4 v11, s[52:55], s67 offen offset:0 lds\n"
"v_mfma_f32_16x16x32_bf16 v[52:55], a[32:35], a[136:139], v[52:55]\n"
"s_waitcnt lgkmcnt(5)\n"
"ds_read_b64_tr_b16 a[74:75], v9 offset:520\n"
"v_mfma_f32_16x16x32_bf16 v[56:59], a[32:35], a[144:147], v[56:59]\n"
"s_sub_u32 s67, 1129472, s75\n"
"ds_read_b64_tr_b16 a[76:77], v9 offset:4104\n"
"v_mfma_f32_16x16x32_bf16 v[56:59], a[36:39], a[148:151], v[56:59]\n"
"ds_read_b64_tr_b16 a[78:79], v9 offset:4616\n"
"v_mfma_f32_16x16x32_bf16 v[84:87], a[44:47], a[148:151], v[84:87]\n"
"buffer_load_dwordx4 v11, s[52:55], s67 offen offset:1024 lds\n"
"v_mfma_f32_16x16x32_bf16 v[84:87], a[40:43], a[144:147], v[84:87]\n"
"ds_read_b64_tr_b16 a[80:81], v15 offset:0\n"
"v_mfma_f32_16x16x32_bf16 a[4:7], a[40:43], a[152:155], a[4:7]\n"
"s_sub_u32 s67, 1144832, s75\n"
"ds_read_b64_tr_b16 a[82:83], v15 offset:512\n"
"v_mfma_f32_16x16x32_bf16 a[4:7], a[44:47], a[156:159], a[4:7]\n"
"ds_read_b64_tr_b16 a[84:85], v15 offset:4096\n"
"v_mfma_f32_16x16x32_bf16 a[0:3], a[36:39], a[156:159], a[0:3]\n"
"s_add_u32 s96, s56, %11\n"
"ds_read_b64_tr_b16 a[86:87], v15 offset:4608\n"
"s_addc_u32 s97, s57, 0\n"
"v_mfma_f32_16x16x32_bf16 a[0:3], a[32:35], a[152:155], a[0:3]\n"
"ds_read_b64_tr_b16 a[88:89], v15 offset:8\n"
"buffer_load_dwordx4 v11, s[52:55], s67 offen offset:2048 lds\n"
"v_mfma_f32_16x16x32_bf16 v[88:91], a[48:51], a[96:99], v[88:91]\n"
"s_mov_b32 s69, 0\n"
"ds_read_b64_tr_b16 a[90:91], v15 offset:520\n"
"v_mfma_f32_16x16x32_bf16 v[88:91], a[52:55], a[100:103], v[88:91]\n"
"s_sub_u32 s67, 1160192, s75\n"
"ds_read_b64_tr_b16 a[92:93], v15 offset:4104\n"
"v_mfma_f32_16x16x32_bf16 v[116:119], a[60:63], a[100:103], v[116:119]\n"
"ds_read_b64_tr_b16 a[94:95], v15 offset:4616\n"
"v_mfma_f32_16x16x32_bf16 v[116:119], a[56:59], a[96:99], v[116:119]\n"
"buffer_load_dwordx4 v11, s[52:55], s67 offen offset:3072 lds\n"
"v_mfma_f32_16x16x32_bf16 v[120:123], a[56:59], a[104:107], v[120:123]\n"
"v_mfma_f32_16x16x32_bf16 v[120:123], a[60:63], a[108:111], v[120:123]\n"
"s_add_u32 m0, s64, 0\n"
"v_mfma_f32_16x16x32_bf16 v[92:95], a[52:55], a[108:111], v[92:95]\n"
"v_mfma_f32_16x16x32_bf16 v[92:95], a[48:51], a[104:107], v[92:95]\n"
"s_sub_u32 s67, 1048704, s75\n"
"v_mfma_f32_16x16x32_bf16 v[96:99], a[48:51], a[112:115], v[96:99]\n"
"s_waitcnt lgkmcnt(0)\n"
"v_mfma_f32_16x16x32_bf16 v[96:99], a[52:55], a[116:119], v[96:99]\n"
"s_mov_b32 s98, 0\n"
"v_mfma_f32_16x16x32_bf16 v[124:127], a[60:63], a[116:119], v[124:127]\n"
"s_load_dword s98, s[96:97], 0 glc\n"
"buffer_load_dwordx4 v11, s[52:55], s67 offen offset:0 lds\n"
"v_mfma_f32_16x16x32_bf16 v[124:127], a[56:59], a[112:115], v[124:127]\n"
"v_mfma_f32_16x16x32_bf16 v[128:131], a[56:59], a[120:123], v[128:131]\n"
"s_sub_u32 s67, 1064064, s75\n"
"v_mfma_f32_16x16x32_bf16 v[128:131], a[60:63], a[124:127], v[128:131]\n"
"v_mfma_f32_16x16x32_bf16 v[100:103], a[52:55], a[124:127], v[100:103]\n"
"buffer_load_dwordx4 v11, s[52:55], s67 offen offset:1024 lds\n"
"v_mfma_f32_16x16x32_bf16 v[100:103], a[48:51], a[120:123], v[100:103]\n"
"v_mfma_f32_16x16x32_bf16 v[104:107], a[48:51], a[128:131], v[104:107]\n"
"s_sub_u32 s67, 1079424, s75\n"
"v_mfma_f32_16x16x32_bf16 v[104:107], a[52:55], a[132:135], v[104:107]\n"
"v_mfma_f32_16x16x32_bf16 v[132:135], a[60:63], a[132:135], v[132:135]\n"
"buffer_load_dwordx4 v11, s[52:55], s67 offen offset:2048 lds\n"
"v_mfma_f32_16x16x32_bf16 v[132:135], a[56:59], a[128:131], v[132:135]\n"
"v_mfma_f32_16x16x32_bf16 v[136:139], a[56:59], a[136:139], v[136:139]\n"
"s_sub_u32 s67, 1094784, s75\n"
"v_mfma_f32_16x16x32_bf16 v[136:139], a[60:63], a[140:143], v[136:139]\n"
"v_mfma_f32_16x16x32_bf16 v[108:111], a[52:55], a[140:143], v[108:111]\n"
"buffer_load_dwordx4 v11, s[52:55], s67 offen offset:3072 lds\n"
"v_mfma_f32_16x16x32_bf16 v[108:111], a[48:51], a[136:139], v[108:111]\n"
"s_add_u32 s69, s69, s98\n"
"v_mfma_f32_16x16x32_bf16 v[112:115], a[48:51], a[144:147], v[112:115]\n"
"s_add_u32 s69, s69, s98\n"
"s_add_u32 m0, s64, 4096\n"
"v_mfma_f32_16x16x32_bf16 v[112:115], a[52:55], a[148:151], v[112:115]\n"
"v_mfma_f32_16x16x32_bf16 v[140:143], a[60:63], a[148:151], v[140:143]\n"
"s_add_u32 s69, s69, s98\n"
"s_sub_u32 s67, 1114240, s75\n"
"v_mfma_f32_16x16x32_bf16 v[140:143], a[56:59], a[144:147], v[140:143]\n"
"s_add_u32 s69, s69, s98\n"
"v_mfma_f32_16x16x32_bf16 a[12:15], a[56:59], a[152:155], a[12:15]\n"
"s_add_u32 s69, s69, s98\n"
"v_mfma_f32_16x16x32_bf16 a[12:15], a[60:63], a[156:159], a[12:15]\n"
"buffer_load_dwordx4 v11, s[52:55], s67 offen offset:0 lds\n"
"v_mfma_f32_16x16x32_bf16 a[8:11], a[52:55], a[156:159], a[8:11]\n"
"s_add_u32 s69, s69, s98\n"
"v_mfma_f32_16x16x32_bf16 a[8:11], a[48:51], a[152:155], a[8:11]\n"
"s_add_u32 s69, s69, s98\n"
"s_sub_u32 s67, 1129600, s75\n"
"v_mfma_f32_16x16x32_bf16 v[144:147], a[64:67], a[96:99], v[144:147]\n"
"s_add_u32 s69, s69, s98\n"
"v_mfma_f32_16x16x32_bf16 v[144:147], a[68:71], a[100:103], v[144:147]\n"
"s_sub_u32 s71, s72, s64\n"
"buffer_load_dwordx4 v11, s[52:55], s67 offen offset:1024 lds\n"
"v_mfma_f32_16x16x32_bf16 v[172:175], a[76:79], a[100:103], v[172:175]\n"
"s_add_u32 s69, s69, s98\n"
"v_mfma_f32_16x16x32_bf16 v[172:175], a[72:75], a[96:99], v[172:175]\n"
"s_add_u32 s69, s69, s98\n"
"s_sub_u32 s67, 1144960, s75\n"
"v_mfma_f32_16x16x32_bf16 v[176:179], a[72:75], a[104:107], v[176:179]\n"
"s_add_u32 s69, s69, s98\n"
"s_add_u32 s71, s71, 57344\n"
"v_mfma_f32_16x16x32_bf16 v[176:179], a[76:79], a[108:111], v[176:179]\n"
"buffer_load_dwordx4 v11, s[52:55], s67 offen offset:2048 lds\n"
"v_mfma_f32_16x16x32_bf16 v[148:151], a[68:71], a[108:111], v[148:151]\n"
"s_add_u32 s69, s69, s98\n"
"v_add_u32 v20, s71, v9\n"
"v_mfma_f32_16x16x32_bf16 v[148:151], a[64:67], a[104:107], v[148:151]\n"
"s_add_u32 s69, s69, s98\n"
"s_sub_u32 s67, 1160320, s75\n"
"v_mfma_f32_16x16x32_bf16 v[152:155], a[64:67], a[112:115], v[152:155]\n"
"s_sub_u32 s66, 0, s73\n"
"s_add_u32 s69, s69, s98\n"
"v_mfma_f32_16x16x32_bf16 v[152:155], a[68:71], a[116:119], v[152:155]\n"
"buffer_load_dwordx4 v11, s[52:55], s67 offen offset:3072 lds\n"
"v_add_u32 v21, s71, v15\n"
"v_mfma_f32_16x16x32_bf16 v[180:183], a[76:79], a[116:119], v[180:183]\n"
"s_add_u32 s69, s69, s98\n"
"v_add_u32 v12, s66, v7\n"
"s_waitcnt lgkmcnt(0)\n"
"v_mfma_f32_16x16x32_bf16 v[180:183], a[72:75], a[112:115], v[180:183]\n"
"s_cmp_lt_u32 %16, 2\n"
"s_cselect_b32 s67, 16384, 0\n"
"v_mfma_f32_16x16x32_bf16 v[184:187], a[72:75], a[120:123], v[184:187]\n"
"v_add_u32 v13, s66, v8\n"
"v_mfma_f32_16x16x32_bf16 v[184:187], a[76:79], a[124:127], v[184:187]\n"
"s_waitcnt vmcnt(20)\n"
"ds_read_b64_tr_b16 a[176:177], v20 offset:0\n"
"v_mfma_f32_16x16x32_bf16 v[156:159], a[68:71], a[124:127], v[156:159]\n"
"ds_read_b64_tr_b16 a[178:179], v20 offset:512\n"
"v_mfma_f32_16x16x32_bf16 v[156:159], a[64:67], a[120:123], v[156:159]\n"
"ds_read_b64_tr_b16 a[180:181], v20 offset:8\n"
"v_mfma_f32_16x16x32_bf16 v[160:163], a[64:67], a[128:131], v[160:163]\n"
"s_add_u32 s67, s67, s72\n"
"ds_read_b64_tr_b16 a[182:183], v20 offset:520\n"
"v_mfma_f32_16x16x32_bf16 v[160:163], a[68:71], a[132:135], v[160:163]\n"
"v_mfma_f32_16x16x32_bf16 v[188:191], a[76:79], a[132:135], v[188:191]\n"
"ds_read_b64_tr_b16 a[184:185], v21 offset:0\n"
"v_mfma_f32_16x16x32_bf16 v[188:191], a[72:75], a[128:131], v[188:191]\n"
"ds_read_b64_tr_b16 a[186:187], v21 offset:512\n"
"v_mfma_f32_16x16x32_bf16 v[192:195], a[72:75], a[136:139], v[192:195]\n"
"s_add_u32 m0, s67, 57344\n"
"ds_read_b64_tr_b16 a[188:189], v21 offset:8\n"
"v_mfma_f32_16x16x32_bf16 v[192:195], a[76:79], a[140:143], v[192:195]\n"
"v_mfma_f32_16x16x32_bf16 v[164:167], a[68:71], a[140:143], v[164:167]\n"
"ds_read_b64_tr_b16 a[190:191], v21 offset:520\n"
"v_mfma_f32_16x16x32_bf16 v[164:167], a[64:67], a[136:139], v[164:167]\n"
"v_mfma_f32_16x16x32_bf16 v[168:171], a[64:67], a[144:147], v[168:171]\n"
"s_sub_u32 s67, 1280, s60\n"
"v_mfma_f32_16x16x32_bf16 v[168:171], a[68:71], a[148:151], v[168:171]\n"
"v_mfma_f32_16x16x32_bf16 v[196:199], a[76:79], a[148:151], v[196:199]\n"
"v_mfma_f32_16x16x32_bf16 v[196:199], a[72:75], a[144:147], v[196:199]\n"
"v_mfma_f32_16x16x32_bf16 a[20:23], a[72:75], a[152:155], a[20:23]\n"
"s_waitcnt lgkmcnt(0)\n"
"v_mfma_f32_16x16x32_bf16 a[20:23], a[76:79], a[156:159], a[20:23]\n"
"buffer_load_dwordx4 v6, s[48:51], s67 offen offset:0  lds\n"
"v_mfma_f32_16x16x32_bf16 a[16:19], a[68:71], a[156:159], a[16:19]\n"
"s_sub_u32 s67, 16640, s60\n"
"v_mfma_f32_16x16x32_bf16 a[16:19], a[64:67], a[152:155], a[16:19]\n"
"s_waitcnt vmcnt(17)\n"
"s_barrier\n"
"v_mfma_f32_16x16x32_bf16 v[200:203], a[80:83], a[96:99], v[200:203]\n"
"s_waitcnt vmcnt(9)\n"
"ds_read_b64_tr_b16 a[32:33], v9 offset:8192\n"
"v_mfma_f32_16x16x32_bf16 v[200:203], a[84:87], a[100:103], v[200:203]\n"
"ds_read_b64_tr_b16 a[34:35], v9 offset:8704\n"
"v_mfma_f32_16x16x32_bf16 v[228:231], a[92:95], a[100:103], v[228:231]\n"
"ds_read_b128 a[100:103], v13 offset:32768\n"
"buffer_load_dwordx4 v6, s[48:51], s67 offen offset:1024  lds\n"
"v_mfma_f32_16x16x32_bf16 v[228:231], a[88:91], a[96:99], v[228:231]\n"
"ds_read_b128 a[96:99], v12 offset:32768\n"
"v_mfma_f32_16x16x32_bf16 v[232:235], a[88:91], a[104:107], v[232:235]\n"
"s_sub_u32 s67, 32000, s60\n"
"ds_read_b64_tr_b16 a[36:37], v9 offset:12288\n"
"v_mfma_f32_16x16x32_bf16 v[232:235], a[92:95], a[108:111], v[232:235]\n"
"ds_read_b64_tr_b16 a[38:39], v9 offset:12800\n"
"v_mfma_f32_16x16x32_bf16 v[204:207], a[84:87], a[108:111], v[204:207]\n"
"ds_read_b128 a[108:111], v13 offset:34816\n"
"buffer_load_dwordx4 v6, s[48:51], s67 offen offset:2048  lds\n"
"v_mfma_f32_16x16x32_bf16 v[204:207], a[80:83], a[104:107], v[204:207]\n"
"ds_read_b128 a[104:107], v12 offset:34816\n"
"ds_read_b64_tr_b16 a[40:41], v9 offset:8200\n"
"v_mfma_f32_16x16x32_bf16 v[208:211], a[80:83], a[112:115], v[208:211]\n"
"s_sub_u32 s67, 47360, s60\n"
"ds_read_b64_tr_b16 a[42:43], v9 offset:8712\n"
"v_mfma_f32_16x16x32_bf16 v[208:211], a[84:87], a[116:119], v[208:211]\n"
"ds_read_b64_tr_b16 a[44:45], v9 offset:12296\n"
"v_mfma_f32_16x16x32_bf16 v[236:239], a[92:95], a[116:119], v[236:239]\n"
"ds_read_b128 a[116:119], v13 offset:36864\n"
"v_mfma_f32_16x16x32_bf16 v[236:239], a[88:91], a[112:115], v[236:239]\n"
"ds_read_b128 a[112:115], v12 offset:36864\n"
"buffer_load_dwordx4 v6, s[48:51], s67 offen offset:3072  lds\n"
"v_mfma_f32_16x16x32_bf16 v[240:243], a[88:91], a[120:123], v[240:243]\n"
"ds_read_b64_tr_b16 a[46:47], v9 offset:12808\n"
"v_mfma_f32_16x16x32_bf16 v[240:243], a[92:95], a[124:127], v[240:243]\n"
"s_add_u32 m0, s64, 20352\n"
"ds_read_b64_tr_b16 a[48:49], v15 offset:8192\n"
"v_mfma_f32_16x16x32_bf16 v[212:215], a[84:87], a[124:127], v[212:215]\n"
"ds_read_b128 a[124:127], v13 offset:38912\n"
"v_mfma_f32_16x16x32_bf16 v[212:215], a[80:83], a[120:123], v[212:215]\n"
"ds_read_b128 a[120:123], v12 offset:38912\n"
"buffer_load_dwordx4 v6, s[40:43], s84 offen offset:128  lds\n"
"v_mfma_f32_16x16x32_bf16 v[216:219], a[80:83], a[128:131], v[216:219]\n"
"ds_read_b64_tr_b16 a[50:51], v15 offset:8704\n"
"v_mfma_f32_16x16x32_bf16 v[216:219], a[84:87], a[132:135], v[216:219]\n"
"ds_read_b64_tr_b16 a[52:53], v15 offset:12288\n"
"v_mfma_f32_16x16x32_bf16 v[244:247], a[92:95], a[132:135], v[244:247]\n"
"ds_read_b128 a[132:135], v13 offset:24576\n"
"v_mfma_f32_16x16x32_bf16 v[244:247], a[88:91], a[128:131], v[244:247]\n"
"ds_read_b128 a[128:131], v12 offset:24576\n"
"buffer_load_dwordx4 v6, s[40:43], s85 offen offset:1152  lds\n"
"v_mfma_f32_16x16x32_bf16 v[248:251], a[88:91], a[136:139], v[248:251]\n"
"ds_read_b64_tr_b16 a[54:55], v15 offset:12800\n"
"v_mfma_f32_16x16x32_bf16 v[248:251], a[92:95], a[140:143], v[248:251]\n"
"ds_read_b64_tr_b16 a[56:57], v15 offset:8200\n"
"v_mfma_f32_16x16x32_bf16 v[220:223], a[84:87], a[140:143], v[220:223]\n"
"ds_read_b128 a[140:143], v13 offset:26624\n"
"v_mfma_f32_16x16x32_bf16 v[220:223], a[80:83], a[136:139], v[220:223]\n"
"ds_read_b128 a[136:139], v12 offset:26624\n"
"v_mfma_f32_16x16x32_bf16 v[224:227], a[80:83], a[144:147], v[224:227]\n"
"ds_read_b64_tr_b16 a[58:59], v15 offset:8712\n"
"buffer_load_dwordx4 v6, s[40:43], s86 offen offset:2176  lds\n"
"v_mfma_f32_16x16x32_bf16 v[224:227], a[84:87], a[148:151], v[224:227]\n"
"ds_read_b64_tr_b16 a[60:61], v15 offset:12296\n"
"v_mfma_f32_16x16x32_bf16 v[252:255], a[92:95], a[148:151], v[252:255]\n"
"ds_read_b128 a[148:151], v13 offset:28672\n"
"v_mfma_f32_16x16x32_bf16 v[252:255], a[88:91], a[144:147], v[252:255]\n"
"ds_read_b128 a[144:147], v12 offset:28672\n"
"ds_read_b64_tr_b16 a[62:63], v15 offset:12808\n"
"v_mfma_f32_16x16x32_bf16 a[28:31], a[88:91], a[152:155], a[28:31]\n"
"buffer_load_dwordx4 v6, s[40:43], s87 offen offset:3200  lds\n"
"v_mfma_f32_16x16x32_bf16 a[28:31], a[92:95], a[156:159], a[28:31]\n"
"v_mfma_f32_16x16x32_bf16 a[24:27], a[84:87], a[156:159], a[24:27]\n"
"ds_read_b128 a[156:159], v13 offset:30720\n"
"v_mfma_f32_16x16x32_bf16 a[24:27], a[80:83], a[152:155], a[24:27]\n"
"s_waitcnt lgkmcnt(15)\n"
"ds_read_b128 a[152:155], v12 offset:30720\n"
"v_mfma_f32_16x16x32_bf16 v[32:35], a[32:35], a[96:99], v[32:35]\n"
"v_mfma_f32_16x16x32_bf16 v[32:35], a[36:39], a[100:103], v[32:35]\n"
"s_waitcnt lgkmcnt(2)\n"
"buffer_store_dwordx4 a[176:179], v0, s[36:39], %23 offen offset:0 sc1\n"
"v_mfma_f32_16x16x32_bf16 v[60:63], a[44:47], a[100:103], v[60:63]\n"
"v_mfma_f32_16x16x32_bf16 v[60:63], a[40:43], a[96:99], v[60:63]\n"
"s_add_u32 m0, s64, 8192\n"
"v_mfma_f32_16x16x32_bf16 v[64:67], a[40:43], a[104:107], v[64:67]\n"
"v_mfma_f32_16x16x32_bf16 v[64:67], a[44:47], a[108:111], v[64:67]\n"
"s_sub_u32 s67, 1310720, s75\n"
"v_mfma_f32_16x16x32_bf16 v[36:39], a[36:39], a[108:111], v[36:39]\n"
"v_mfma_f32_16x16x32_bf16 v[36:39], a[32:35], a[104:107], v[36:39]\n"
"buffer_load_dwordx4 v11, s[52:55], s67 offen offset:0 lds\n"
"v_mfma_f32_16x16x32_bf16 v[40:43], a[32:35], a[112:115], v[40:43]\n"
"v_mfma_f32_16x16x32_bf16 v[40:43], a[36:39], a[116:119], v[40:43]\n"
"s_sub_u32 s67, 1326080, s75\n"
"v_mfma_f32_16x16x32_bf16 v[68:71], a[44:47], a[116:119], v[68:71]\n"
"v_mfma_f32_16x16x32_bf16 v[68:71], a[40:43], a[112:115], v[68:71]\n"
"buffer_load_dwordx4 v11, s[52:55], s67 offen offset:1024 lds\n"
"v_mfma_f32_16x16x32_bf16 v[72:75], a[40:43], a[120:123], v[72:75]\n"
"v_mfma_f32_16x16x32_bf16 v[72:75], a[44:47], a[124:127], v[72:75]\n"
"s_sub_u32 s67, 1341440, s75\n"
"v_mfma_f32_16x16x32_bf16 v[44:47], a[36:39], a[124:127], v[44:47]\n"
"v_mfma_f32_16x16x32_bf16 v[44:47], a[32:35], a[120:123], v[44:47]\n"
"v_mfma_f32_16x16x32_bf16 v[48:51], a[32:35], a[128:131], v[48:51]\n"
"buffer_load_dwordx4 v11, s[52:55], s67 offen offset:2048 lds\n"
"v_mfma_f32_16x16x32_bf16 v[48:51], a[36:39], a[132:135], v[48:51]\n"
"s_sub_u32 s67, 1356800, s75\n"
"v_mfma_f32_16x16x32_bf16 v[76:79], a[44:47], a[132:135], v[76:79]\n"
"s_waitcnt vmcnt(12)\n"
"ds_read_b64_tr_b16 a[64:65], v9 offset:0\n"
"v_mfma_f32_16x16x32_bf16 v[76:79], a[40:43], a[128:131], v[76:79]\n"
"ds_read_b64_tr_b16 a[66:67], v9 offset:512\n"
"v_mfma_f32_16x16x32_bf16 v[80:83], a[40:43], a[136:139], v[80:83]\n"
"ds_read_b64_tr_b16 a[68:69], v9 offset:4096\n"
"buffer_load_dwordx4 v11, s[52:55], s67 offen offset:3072 lds\n"
"v_mfma_f32_16x16x32_bf16 v[80:83], a[44:47], a[140:143], v[80:83]\n"
"ds_read_b64_tr_b16 a[70:71], v9 offset:4608\n"
"v_mfma_f32_16x16x32_bf16 v[52:55], a[36:39], a[140:143], v[52:55]\n"
"s_add_u32 m0, s64, 12288\n"
"ds_read_b64_tr_b16 a[72:73], v9 offset:8\n"
"v_mfma_f32_16x16x32_bf16 v[52:55], a[32:35], a[136:139], v[52:55]\n"
"s_waitcnt lgkmcnt(5)\n"
"ds_read_b64_tr_b16 a[74:75], v9 offset:520\n"
"v_mfma_f32_16x16x32_bf16 v[56:59], a[32:35], a[144:147], v[56:59]\n"
"s_sub_u32 s67, 1376256, s75\n"
"ds_read_b64_tr_b16 a[76:77], v9 offset:4104\n"
"v_mfma_f32_16x16x32_bf16 v[56:59], a[36:39], a[148:151], v[56:59]\n"
"ds_read_b64_tr_b16 a[78:79], v9 offset:4616\n"
"v_mfma_f32_16x16x32_bf16 v[84:87], a[44:47], a[148:151], v[84:87]\n"
"buffer_load_dwordx4 v11, s[52:55], s67 offen offset:0 lds\n"
"v_mfma_f32_16x16x32_bf16 v[84:87], a[40:43], a[144:147], v[84:87]\n"
"ds_read_b64_tr_b16 a[80:81], v15 offset:0\n"
"v_mfma_f32_16x16x32_bf16 a[4:7], a[40:43], a[152:155], a[4:7]\n"
"s_sub_u32 s67, 1391616, s75\n"
"ds_read_b64_tr_b16 a[82:83], v15 offset:512\n"
"v_mfma_f32_16x16x32_bf16 a[4:7], a[44:47], a[156:159], a[4:7]\n"
"ds_read_b64_tr_b16 a[84:85], v15 offset:4096\n"
"v_mfma_f32_16x16x32_bf16 a[0:3], a[36:39], a[156:159], a[0:3]\n"
"ds_read_b64_tr_b16 a[86:87], v15 offset:4608\n"
"buffer_load_dwordx4 v11, s[52:55], s67 offen offset:1024 lds\n"
"v_mfma_f32_16x16x32_bf16 a[0:3], a[32:35], a[152:155], a[0:3]\n"
"ds_read_b64_tr_b16 a[88:89], v15 offset:8\n"
"v_mfma_f32_16x16x32_bf16 v[88:91], a[48:51], a[96:99], v[88:91]\n"
"s_sub_u32 s67, 1406976, s75\n"
"ds_read_b64_tr_b16 a[90:91], v15 offset:520\n"
"v_mfma_f32_16x16x32_bf16 v[88:91], a[52:55], a[100:103], v[88:91]\n"
"ds_read_b64_tr_b16 a[92:93], v15 offset:4104\n"
"v_mfma_f32_16x16x32_bf16 v[116:119], a[60:63], a[100:103], v[116:119]\n"
"ds_read_b64_tr_b16 a[94:95], v15 offset:4616\n"
"v_mfma_f32_16x16x32_bf16 v[116:119], a[56:59], a[96:99], v[116:119]\n"
"buffer_load_dwordx4 v11, s[52:55], s67 offen offset:2048 lds\n"
"v_mfma_f32_16x16x32_bf16 v[120:123], a[56:59], a[104:107], v[120:123]\n"
"v_mfma_f32_16x16x32_bf16 v[120:123], a[60:63], a[108:111], v[120:123]\n"
"s_sub_u32 s67, 1422336, s75\n"
"v_mfma_f32_16x16x32_bf16 v[92:95], a[52:55], a[108:111], v[92:95]\n"
"v_mfma_f32_16x16x32_bf16 v[92:95], a[48:51], a[104:107], v[92:95]\n"
"buffer_load_dwordx4 v11, s[52:55], s67 offen offset:3072 lds\n"
"v_mfma_f32_16x16x32_bf16 v[96:99], a[48:51], a[112:115], v[96:99]\n"
"s_waitcnt lgkmcnt(0)\n"
"s_add_u32 m0, s64, 0\n"
"v_mfma_f32_16x16x32_bf16 v[96:99], a[52:55], a[116:119], v[96:99]\n"
"s_mov_b32 s98, 0\n"
"v_mfma_f32_16x16x32_bf16 v[124:127], a[60:63], a[116:119], v[124:127]\n"
"s_load_dword s98, s[96:97], 8192 glc\n"
"s_sub_u32 s67, 1310848, s75\n"
"v_mfma_f32_16x16x32_bf16 v[124:127], a[56:59], a[112:115], v[124:127]\n"
"v_mfma_f32_16x16x32_bf16 v[128:131], a[56:59], a[120:123], v[128:131]\n"
"buffer_load_dwordx4 v11, s[52:55], s67 offen offset:0 lds\n"
"v_mfma_f32_16x16x32_bf16 v[128:131], a[60:63], a[124:127], v[128:131]\n"
"v_mfma_f32_16x16x32_bf16 v[100:103], a[52:55], a[124:127], v[100:103]\n"
"s_sub_u32 s67, 1326208, s75\n"
"v_mfma_f32_16x16x32_bf16 v[100:103], a[48:51], a[120:123], v[100:103]\n"
"v_mfma_f32_16x16x32_bf16 v[104:107], a[48:51], a[128:131], v[104:107]\n"
"buffer_load_dwordx4 v11, s[52:55], s67 offen offset:1024 lds\n"
"v_mfma_f32_16x16x32_bf16 v[104:107], a[52:55], a[132:135], v[104:107]\n"
"v_mfma_f32_16x16x32_bf16 v[132:135], a[60:63], a[132:135], v[132:135]\n"
"s_sub_u32 s67, 1341568, s75\n"
"v_mfma_f32_16x16x32_bf16 v[132:135], a[56:59], a[128:131], v[132:135]\n"
"v_mfma_f32_16x16x32_bf16 v[136:139], a[56:59], a[136:139], v[136:139]\n"
"buffer_load_dwordx4 v11, s[52:55], s67 offen offset:2048 lds\n"
"v_mfma_f32_16x16x32_bf16 v[136:139], a[60:63], a[140:143], v[136:139]\n"
"v_mfma_f32_16x16x32_bf16 v[108:111], a[52:55], a[140:143], v[108:111]\n"
"s_sub_u32 s67, 1356928, s75\n"
"v_mfma_f32_16x16x32_bf16 v[108:111], a[48:51], a[136:139], v[108:111]\n"
"s_sub_u32 s69, s69, s98\n"
"v_mfma_f32_16x16x32_bf16 v[112:115], a[48:51], a[144:147], v[112:115]\n"
"s_sub_u32 s69, s69, s98\n"
"v_mfma_f32_16x16x32_bf16 v[112:115], a[52:55], a[148:151], v[112:115]\n"
"buffer_load_dwordx4 v11, s[52:55], s67 offen offset:3072 lds\n"
"v_mfma_f32_16x16x32_bf16 v[140:143], a[60:63], a[148:151], v[140:143]\n"
"s_sub_u32 s69, s69, s98\n"
"v_mfma_f32_16x16x32_bf16 v[140:143], a[56:59], a[144:147], v[140:143]\n"
"s_sub_u32 s69, s69, s98\n"
"s_add_u32 m0, s64, 4096\n"
"v_mfma_f32_16x16x32_bf16 a[12:15], a[56:59], a[152:155], a[12:15]\n"
"s_sub_u32 s69, s69, s98\n"
"s_sub_u32 s67, 1376384, s75\n"
"v_mfma_f32_16x16x32_bf16 a[12:15], a[60:63], a[156:159], a[12:15]\n"
"buffer_load_dwordx4 v11, s[52:55], s67 offen offset:0 lds\n"
"v_mfma_f32_16x16x32_bf16 a[8:11], a[52:55], a[156:159], a[8:11]\n"
"s_sub_u32 s69, s69, s98\n"
"v_mfma_f32_16x16x32_bf16 a[8:11], a[48:51], a[152:155], a[8:11]\n"
"s_sub_u32 s69, s69, s98\n"
"s_sub_u32 s67, 1391744, s75\n"
"v_mfma_f32_16x16x32_bf16 v[144:147], a[64:67], a[96:99], v[144:147]\n"
"s_sub_u32 s69, s69, s98\n"
"v_mfma_f32_16x16x32_bf16 v[144:147], a[68:71], a[100:103], v[144:147]\n"
"buffer_load_dwordx4 v11, s[52:55], s67 offen offset:1024 lds\n"
"v_mfma_f32_16x16x32_bf16 v[172:175], a[76:79], a[100:103], v[172:175]\n"
"s_sub_u32 s69, s69, s98\n"
"v_mfma_f32_16x16x32_bf16 v[172:175], a[72:75], a[96:99], v[172:175]\n"
"s_sub_u32 s69, s69, s98\n"
"s_sub_u32 s67, 1407104, s75\n"
"v_mfma_f32_16x16x32_bf16 v[176:179], a[72:75], a[104:107], v[176:179]\n"
"s_sub_u32 s69, s69, s98\n"
"v_mfma_f32_16x16x32_bf16 v[176:179], a[76:79], a[108:111], v[176:179]\n"
"s_sub_u32 s71, s64, s64\n"
"v_mfma_f32_16x16x32_bf16 v[148:151], a[68:71], a[108:111], v[148:151]\n"
"s_sub_u32 s69, s69, s98\n"
"buffer_load_dwordx4 v11, s[52:55], s67 offen offset:2048 lds\n"
"v_mfma_f32_16x16x32_bf16 v[148:151], a[64:67], a[104:107], v[148:151]\n"
"s_sub_u32 s69, s69, s98\n"
"s_add_u32 s71, s71, 20480\n"
"v_mfma_f32_16x16x32_bf16 v[152:155], a[64:67], a[112:115], v[152:155]\n"
"s_sub_u32 s66, 40960, s73\n"
"s_sub_u32 s69, s69, s98\n"
"v_mfma_f32_16x16x32_bf16 v[152:155], a[68:71], a[116:119], v[152:155]\n"
"s_sub_u32 s67, 1422464, s75\n"
"v_mfma_f32_16x16x32_bf16 v[180:183], a[76:79], a[116:119], v[180:183]\n"
"s_sub_u32 s69, s69, s98\n"
"v_add_u32 v12, s66, v7\n"
"s_waitcnt lgkmcnt(0)\n"
"v_mfma_f32_16x16x32_bf16 v[180:183], a[72:75], a[112:115], v[180:183]\n"
"buffer_load_dwordx4 v11, s[52:55], s67 offen offset:3072 lds\n"
"v_add_u32 v20, s71, v7\n"
"v_mfma_f32_16x16x32_bf16 v[184:187], a[72:75], a[120:123], v[184:187]\n"
"s_cmp_lt_u32 %16, 2\n"
"s_cselect_b32 s67, 16384, 0\n"
"v_add_u32 v13, s66, v8\n"
"v_mfma_f32_16x16x32_bf16 v[184:187], a[76:79], a[124:127], v[184:187]\n"
"s_add_u32 s67, s67, s72\n"
"v_add_u32 v21, s71, v8\n"
"v_mfma_f32_16x16x32_bf16 v[156:159], a[68:71], a[124:127], v[156:159]\n"
"v_mfma_f32_16x16x32_bf16 v[156:159], a[64:67], a[120:123], v[156:159]\n"
"s_add_u32 m0, s67, 98304\n"
"s_sub_u32 s67, 1536, s60\n"
"v_mfma_f32_16x16x32_bf16 v[160:163], a[64:67], a[128:131], v[160:163]\n"
"s_waitcnt vmcnt(17)\n"
"ds_read_b128 a[208:211], v20 offset:0\n"
"v_mfma_f32_16x16x32_bf16 v[160:163], a[68:71], a[132:135], v[160:163]\n"
"buffer_load_dwordx4 v6, s[48:51], s67 offen offset:0  lds\n"
"v_mfma_f32_16x16x32_bf16 v[188:191], a[76:79], a[132:135], v[188:191]\n"
"ds_read_b128 a[212:215], v21 offset:0\n"
"v_mfma_f32_16x16x32_bf16 v[188:191], a[72:75], a[128:131], v[188:191]\n"
"s_sub_u32 s67, 16896, s60\n"
"ds_read_b128 a[216:219], v20 offset:2048\n"
"v_mfma_f32_16x16x32_bf16 v[192:195], a[72:75], a[136:139], v[192:195]\n"
"v_mfma_f32_16x16x32_bf16 v[192:195], a[76:79], a[140:143], v[192:195]\n"
"ds_read_b128 a[220:223], v21 offset:2048\n"
"v_mfma_f32_16x16x32_bf16 v[164:167], a[68:71], a[140:143], v[164:167]\n"
"buffer_load_dwordx4 v6, s[48:51], s67 offen offset:1024  lds\n"
"v_mfma_f32_16x16x32_bf16 v[164:167], a[64:67], a[136:139], v[164:167]\n"
"v_mfma_f32_16x16x32_bf16 v[168:171], a[64:67], a[144:147], v[168:171]\n"
"s_sub_u32 s67, 32256, s60\n"
"v_mfma_f32_16x16x32_bf16 v[168:171], a[68:71], a[148:151], v[168:171]\n"
"v_mfma_f32_16x16x32_bf16 v[196:199], a[76:79], a[148:151], v[196:199]\n"
"buffer_load_dwordx4 v6, s[48:51], s67 offen offset:2048  lds\n"
"v_mfma_f32_16x16x32_bf16 v[196:199], a[72:75], a[144:147], v[196:199]\n"
"s_waitcnt lgkmcnt(0)\n"
"v_mfma_f32_16x16x32_bf16 a[20:23], a[72:75], a[152:155], a[20:23]\n"
"s_sub_u32 s67, 47616, s60\n"
"v_mfma_f32_16x16x32_bf16 a[20:23], a[76:79], a[156:159], a[20:23]\n"
"v_mfma_f32_16x16x32_bf16 a[16:19], a[68:71], a[156:159], a[16:19]\n"
"buffer_load_dwordx4 v6, s[48:51], s67 offen offset:3072  lds\n"
"v_mfma_f32_16x16x32_bf16 a[16:19], a[64:67], a[152:155], a[16:19]\n"
"s_barrier\n"
"s_waitcnt vmcnt(12)\n"
"ds_read_b64_tr_b16 a[32:33], v9 offset:8192\n"
"v_mfma_f32_16x16x32_bf16 v[200:203], a[80:83], a[96:99], v[200:203]\n"
"s_add_u32 m0, s64, 16384\n"
"ds_read_b64_tr_b16 a[34:35], v9 offset:8704\n"
"v_mfma_f32_16x16x32_bf16 v[200:203], a[84:87], a[100:103], v[200:203]\n"
"s_add_u32 s67, %21, 4194304\n"
"ds_read_b64_tr_b16 a[36:37], v9 offset:12288\n"
"v_mfma_f32_16x16x32_bf16 v[228:231], a[92:95], a[100:103], v[228:231]\n"
"ds_read_b128 a[100:103], v13 offset:32768\n"
"v_mfma_f32_16x16x32_bf16 v[228:231], a[88:91], a[96:99], v[228:231]\n"
"ds_read_b128 a[96:99], v12 offset:32768\n"
"buffer_load_dwordx4 v11, s[52:55], s67 offen offset:0 nt lds\n"
"v_mfma_f32_16x16x32_bf16 v[232:235], a[88:91], a[104:107], v[232:235]\n"
"ds_read_b64_tr_b16 a[38:39], v9 offset:12800\n"
"v_mfma_f32_16x16x32_bf16 v[232:235], a[92:95], a[108:111], v[232:235]\n"
"s_add_u32 s67, %21, 4209664\n"
"ds_read_b64_tr_b16 a[40:41], v9 offset:8200\n"
"v_mfma_f32_16x16x32_bf16 v[204:207], a[84:87], a[108:111], v[204:207]\n"
"ds_read_b128 a[108:111], v13 offset:34816\n"
"v_mfma_f32_16x16x32_bf16 v[204:207], a[80:83], a[104:107], v[204:207]\n"
"ds_read_b128 a[104:107], v12 offset:34816\n"
"buffer_load_dwordx4 v11, s[52:55], s67 offen offset:1024 nt lds\n"
"v_mfma_f32_16x16x32_bf16 v[208:211], a[80:83], a[112:115], v[208:211]\n"
"ds_read_b64_tr_b16 a[42:43], v9 offset:8712\n"
"v_mfma_f32_16x16x32_bf16 v[208:211], a[84:87], a[116:119], v[208:211]\n"
"s_add_u32 s67, %21, 4225024\n"
"ds_read_b64_tr_b16 a[44:45], v9 offset:12296\n"
"v_mfma_f32_16x16x32_bf16 v[236:239], a[92:95], a[116:119], v[236:239]\n"
"ds_read_b128 a[116:119], v13 offset:36864\n"
"v_mfma_f32_16x16x32_bf16 v[236:239], a[88:91], a[112:115], v[236:239]\n"
"ds_read_b128 a[112:115], v12 offset:36864\n"
"v_mfma_f32_16x16x32_bf16 v[240:243], a[88:91], a[120:123], v[240:243]\n"
"ds_read_b64_tr_b16 a[46:47], v9 offset:12808\n"
"buffer_load_dwordx4 v11, s[52:55], s67 offen offset:2048 nt lds\n"
"v_mfma_f32_16x16x32_bf16 v[240:243], a[92:95], a[124:127], v[240:243]\n"
"ds_read_b64_tr_b16 a[48:49], v15 offset:8192\n"
"v_mfma_f32_16x16x32_bf16 v[212:215], a[84:87], a[124:127], v[212:215]\n"
"s_add_u32 s67, %21, 4240384\n"
"ds_read_b128 a[124:127], v13 offset:38912\n"
"v_mfma_f32_16x16x32_bf16 v[212:215], a[80:83], a[120:123], v[212:215]\n"
"ds_read_b128 a[120:123], v12 offset:38912\n"
"v_mfma_f32_16x16x32_bf16 v[216:219], a[80:83], a[128:131], v[216:219]\n"
"ds_read_b64_tr_b16 a[50:51], v15 offset:8704\n"
"buffer_load_dwordx4 v11, s[52:55], s67 offen offset:3072 nt lds\n"
"v_mfma_f32_16x16x32_bf16 v[216:219], a[84:87], a[132:135], v[216:219]\n"
"ds_read_b64_tr_b16 a[52:53], v15 offset:12288\n"
"v_mfma_f32_16x16x32_bf16 v[244:247], a[92:95], a[132:135], v[244:247]\n"
"ds_read_b128 a[132:135], v13 offset:24576\n"
"v_mfma_f32_16x16x32_bf16 v[244:247], a[88:91], a[128:131], v[244:247]\n"
"ds_read_b128 a[128:131], v12 offset:24576\n"
"v_mfma_f32_16x16x32_bf16 v[248:251], a[88:91], a[136:139], v[248:251]\n"
"s_add_u32 m0, s64, 8192\n"
"ds_read_b64_tr_b16 a[54:55], v15 offset:12800\n"
"v_mfma_f32_16x16x32_bf16 v[248:251], a[92:95], a[140:143], v[248:251]\n"
"ds_read_b64_tr_b16 a[56:57], v15 offset:8200\n"
"v_mfma_f32_16x16x32_bf16 v[220:223], a[84:87], a[140:143], v[220:223]\n"
"ds_read_b128 a[140:143], v13 offset:26624\n"
"v_mfma_f32_16x16x32_bf16 v[220:223], a[80:83], a[136:139], v[220:223]\n"
"ds_read_b128 a[136:139], v12 offset:26624\n"
"v_mfma_f32_16x16x32_bf16 v[224:227], a[80:83], a[144:147], v[224:227]\n"
"ds_read_b64_tr_b16 a[58:59], v15 offset:8712\n"
"v_mfma_f32_16x16x32_bf16 v[224:227], a[84:87], a[148:151], v[224:227]\n"
"ds_read_b64_tr_b16 a[60:61], v15 offset:12296\n"
"v_mfma_f32_16x16x32_bf16 v[252:255], a[92:95], a[148:151], v[252:255]\n"
"s_sub_u32 s67, 1572864, s75\n"
"ds_read_b128 a[148:151], v13 offset:28672\n"
"v_mfma_f32_16x16x32_bf16 v[252:255], a[88:91], a[144:147], v[252:255]\n"
"ds_read_b128 a[144:147], v12 offset:28672\n"
"ds_read_b64_tr_b16 a[62:63], v15 offset:12808\n"
"v_mfma_f32_16x16x32_bf16 a[28:31], a[88:91], a[152:155], a[28:31]\n"
"v_mfma_f32_16x16x32_bf16 a[28:31], a[92:95], a[156:159], a[28:31]\n"
"v_mfma_f32_16x16x32_bf16 a[24:27], a[84:87], a[156:159], a[24:27]\n"
"ds_read_b128 a[156:159], v13 offset:30720\n"
"v_mfma_f32_16x16x32_bf16 a[24:27], a[80:83], a[152:155], a[24:27]\n"
"s_waitcnt lgkmcnt(15)\n"
"ds_read_b128 a[152:155], v12 offset:30720\n"
"v_mfma_f32_16x16x32_bf16 v[32:35], a[32:35], a[96:99], v[32:35]\n"
"v_mfma_f32_16x16x32_bf16 v[32:35], a[36:39], a[100:103], v[32:35]\n"
"s_waitcnt lgkmcnt(2)\n"
"v_mfma_f32_16x16x32_bf16 v[60:63], a[44:47], a[100:103], v[60:63]\n"
"buffer_load_dwordx4 v11, s[52:55], s67 offen offset:0 lds\n"
"v_mfma_f32_16x16x32_bf16 v[60:63], a[40:43], a[96:99], v[60:63]\n"
"v_mfma_f32_16x16x32_bf16 v[64:67], a[40:43], a[104:107], v[64:67]\n"
"s_sub_u32 s67, 1588224, s75\n"
"v_mfma_f32_16x16x32_bf16 v[64:67], a[44:47], a[108:111], v[64:67]\n"
"v_mfma_f32_16x16x32_bf16 v[36:39], a[36:39], a[108:111], v[36:39]\n"
"buffer_load_dwordx4 v11, s[52:55], s67 offen offset:1024 lds\n"
"v_mfma_f32_16x16x32_bf16 v[36:39], a[32:35], a[104:107], v[36:39]\n"
"v_mfma_f32_16x16x32_bf16 v[40:43], a[32:35], a[112:115], v[40:43]\n"
"s_sub_u32 s67, 1603584, s75\n"
"v_mfma_f32_16x16x32_bf16 v[40:43], a[36:39], a[116:119], v[40:43]\n"
"v_mfma_f32_16x16x32_bf16 v[68:71], a[44:47], a[116:119], v[68:71]\n"
"v_mfma_f32_16x16x32_bf16 v[68:71], a[40:43], a[112:115], v[68:71]\n"
"buffer_load_dwordx4 v11, s[52:55], s67 offen offset:2048 lds\n"
"v_mfma_f32_16x16x32_bf16 v[72:75], a[40:43], a[120:123], v[72:75]\n"
"v_mfma_f32_16x16x32_bf16 v[72:75], a[44:47], a[124:127], v[72:75]\n"
"s_sub_u32 s67, 1618944, s75\n"
"v_mfma_f32_16x16x32_bf16 v[44:47], a[36:39], a[124:127], v[44:47]\n"
"v_mfma_f32_16x16x32_bf16 v[44:47], a[32:35], a[120:123], v[44:47]\n"
"v_mfma_f32_16x16x32_bf16 v[48:51], a[32:35], a[128:131], v[48:51]\n"
"buffer_load_dwordx4 v11, s[52:55], s67 offen offset:3072 lds\n"
"v_mfma_f32_16x16x32_bf16 v[48:51], a[36:39], a[132:135], v[48:51]\n"
"s_add_u32 m0, s64, 12288\n"
"v_mfma_f32_16x16x32_bf16 v[76:79], a[44:47], a[132:135], v[76:79]\n"
"s_waitcnt vmcnt(12)\n"
"ds_read_b64_tr_b16 a[64:65], v9 offset:0\n"
"v_mfma_f32_16x16x32_bf16 v[76:79], a[40:43], a[128:131], v[76:79]\n"
"s_sub_u32 s67, 1638400, s75\n"
"ds_read_b64_tr_b16 a[66:67], v9 offset:512\n"
"v_mfma_f32_16x16x32_bf16 v[80:83], a[40:43], a[136:139], v[80:83]\n"
"ds_read_b64_tr_b16 a[68:69], v9 offset:4096\n"
"v_mfma_f32_16x16x32_bf16 v[80:83], a[44:47], a[140:143], v[80:83]\n"
"ds_read_b64_tr_b16 a[70:71], v9 offset:4608\n"
"buffer_load_dwordx4 v11, s[52:55], s67 offen offset:0 lds\n"
"v_mfma_f32_16x16x32_bf16 v[52:55], a[36:39], a[140:143], v[52:55]\n"
"s_sub_u32 s67, 1653760, s75\n"
"ds_read_b64_tr_b16 a[72:73], v9 offset:8\n"
"v_mfma_f32_16x16x32_bf16 v[52:55], a[32:35], a[136:139], v[52:55]\n"
"s_waitcnt lgkmcnt(5)\n"
"ds_read_b64_tr_b16 a[74:75], v9 offset:520\n"
"v_mfma_f32_16x16x32_bf16 v[56:59], a[32:35], a[144:147], v[56:59]\n"
"ds_read_b64_tr_b16 a[76:77], v9 offset:4104\n"
"v_mfma_f32_16x16x32_bf16 v[56:59], a[36:39], a[148:151], v[56:59]\n"
"ds_read_b64_tr_b16 a[78:79], v9 offset:4616\n"
"v_mfma_f32_16x16x32_bf16 v[84:87], a[44:47], a[148:151], v[84:87]\n"
"buffer_load_dwordx4 v11, s[52:55], s67 offen offset:1024 lds\n"
"v_mfma_f32_16x16x32_bf16 v[84:87], a[40:43], a[144:147], v[84:87]\n"
"ds_read_b64_tr_b16 a[80:81], v15 offset:0\n"
"v_mfma_f32_16x16x32_bf16 a[4:7], a[40:43], a[152:155], a[4:7]\n"
"s_sub_u32 s67, 1669120, s75\n"
"ds_read_b64_tr_b16 a[82:83], v15 offset:512\n"
"v_mfma_f32_16x16x32_bf16 a[4:7], a[44:47], a[156:159], a[4:7]\n"
"ds_read_b64_tr_b16 a[84:85], v15 offset:4096\n"
"v_mfma_f32_16x16x32_bf16 a[0:3], a[36:39], a[156:159], a[0:3]\n"
"ds_read_b64_tr_b16 a[86:87], v15 offset:4608\n"
"buffer_load_dwordx4 v11, s[52:55], s67 offen offset:2048 lds\n"
"v_mfma_f32_16x16x32_bf16 a[0:3], a[32:35], a[152:155], a[0:3]\n"
"ds_read_b64_tr_b16 a[88:89], v15 offset:8\n"
"v_mfma_f32_16x16x32_bf16 v[88:91], a[48:51], a[96:99], v[88:91]\n"
"s_sub_u32 s67, 1684480, s75\n"
"ds_read_b64_tr_b16 a[90:91], v15 offset:520\n"
"v_mfma_f32_16x16x32_bf16 v[88:91], a[52:55], a[100:103], v[88:91]\n"
"ds_read_b64_tr_b16 a[92:93], v15 offset:4104\n"
"v_mfma_f32_16x16x32_bf16 v[116:119], a[60:63], a[100:103], v[116:119]\n"
"ds_read_b64_tr_b16 a[94:95], v15 offset:4616\n"
"v_mfma_f32_16x16x32_bf16 v[116:119], a[56:59], a[96:99], v[116:119]\n"
"buffer_load_dwordx4 v11, s[52:55], s67 offen offset:3072 lds\n"
"v_mfma_f32_16x16x32_bf16 v[120:123], a[56:59], a[104:107], v[120:123]\n"
"v_mfma_f32_16x16x32_bf16 v[120:123], a[60:63], a[108:111], v[120:123]\n"
"s_add_u32 m0, s64, 0\n"
"v_mfma_f32_16x16x32_bf16 v[92:95], a[52:55], a[108:111], v[92:95]\n"
"v_mfma_f32_16x16x32_bf16 v[92:95], a[48:51], a[104:107], v[92:95]\n"
"s_sub_u32 s67, 1572992, s75\n"
"v_mfma_f32_16x16x32_bf16 v[96:99], a[48:51], a[112:115], v[96:99]\n"
"s_waitcnt lgkmcnt(0)\n"
"v_mfma_f32_16x16x32_bf16 v[96:99], a[52:55], a[116:119], v[96:99]\n"
"s_mov_b32 s98, 0\n"
"v_mfma_f32_16x16x32_bf16 v[124:127], a[60:63], a[116:119], v[124:127]\n"
"s_load_dword s98, s[96:97], 8320 glc\n"
"buffer_load_dwordx4 v11, s[52:55], s67 offen offset:0 lds\n"
"v_mfma_f32_16x16x32_bf16 v[124:127], a[56:59], a[112:115], v[124:127]\n"
"v_mfma_f32_16x16x32_bf16 v[128:131], a[56:59], a[120:123], v[128:131]\n"
"s_sub_u32 s67, 1588352, s75\n"
"v_mfma_f32_16x16x32_bf16 v[128:131], a[60:63], a[124:127], v[128:131]\n"
"v_mfma_f32_16x16x32_bf16 v[100:103], a[52:55], a[124:127], v[100:103]\n"
"buffer_load_dwordx4 v11, s[52:55], s67 offen offset:1024 lds\n"
"v_mfma_f32_16x16x32_bf16 v[100:103], a[48:51], a[120:123], v[100:103]\n"
"v_mfma_f32_16x16x32_bf16 v[104:107], a[48:51], a[128:131], v[104:107]\n"
"s_sub_u32 s67, 1603712, s75\n"
"v_mfma_f32_16x16x32_bf16 v[104:107], a[52:55], a[132:135], v[104:107]\n"
"v_mfma_f32_16x16x32_bf16 v[132:135], a[60:63], a[132:135], v[132:135]\n"
"v_mfma_f32_16x16x32_bf16 v[132:135], a[56:59], a[128:131], v[132:135]\n"
"buffer_load_dwordx4 v11, s[52:55], s67 offen offset:2048 lds\n"
"v_mfma_f32_16x16x32_bf16 v[136:139], a[56:59], a[136:139], v[136:139]\n"
"v_mfma_f32_16x16x32_bf16 v[136:139], a[60:63], a[140:143], v[136:139]\n"
"s_sub_u32 s67, 1619072, s75\n"
"v_mfma_f32_16x16x32_bf16 v[108:111], a[52:55], a[140:143], v[108:111]\n"
"buffer_load_dwordx4 v11, s[52:55], s67 offen offset:3072 lds\n"
"v_mfma_f32_16x16x32_bf16 v[108:111], a[48:51], a[136:139], v[108:111]\n"
"s_sub_u32 s69, s69, s98\n"
"v_mfma_f32_16x16x32_bf16 v[112:115], a[48:51], a[144:147], v[112:115]\n"
"s_sub_u32 s69, s69, s98\n"
"s_add_u32 m0, s64, 4096\n"
"v_mfma_f32_16x16x32_bf16 v[112:115], a[52:55], a[148:151], v[112:115]\n"
"v_mfma_f32_16x16x32_bf16 v[140:143], a[60:63], a[148:151], v[140:143]\n"
"s_sub_u32 s69, s69, s98\n"
"s_sub_u32 s67, 1638528, s75\n"
"v_mfma_f32_16x16x32_bf16 v[140:143], a[56:59], a[144:147], v[140:143]\n"
"s_sub_u32 s69, s69, s98\n"
"v_mfma_f32_16x16x32_bf16 a[12:15], a[56:59], a[152:155], a[12:15]\n"
"s_sub_u32 s69, s69, s98\n"
"v_mfma_f32_16x16x32_bf16 a[12:15], a[60:63], a[156:159], a[12:15]\n"
"buffer_load_dwordx4 v11, s[52:55], s67 offen offset:0 lds\n"
"v_mfma_f32_16x16x32_bf16 a[8:11], a[52:55], a[156:159], a[8:11]\n"
"s_sub_u32 s69, s69, s98\n"
"v_mfma_f32_16x16x32_bf16 a[8:11], a[48:51], a[152:155], a[8:11]\n"
"s_sub_u32 s69, s69, s98\n"
"s_sub_u32 s67, 1653888, s75\n"
"v_mfma_f32_16x16x32_bf16 v[144:147], a[64:67], a[96:99], v[144:147]\n"
"s_sub_u32 s69, s69, s98\n"
"v_mfma_f32_16x16x32_bf16 v[144:147], a[68:71], a[100:103], v[144:147]\n"
"buffer_load_dwordx4 v11, s[52:55], s67 offen offset:1024 lds\n"
"v_mfma_f32_16x16x32_bf16 v[172:175], a[76:79], a[100:103], v[172:175]\n"
"s_sub_u32 s69, s69, s98\n"
"v_mfma_f32_16x16x32_bf16 v[172:175], a[72:75], a[96:99], v[172:175]\n"
"s_sub_u32 s69, s69, s98\n"
"s_sub_u32 s67, 1669248, s75\n"
"v_mfma_f32_16x16x32_bf16 v[176:179], a[72:75], a[104:107], v[176:179]\n"
"s_sub_u32 s69, s69, s98\n"
"v_mfma_f32_16x16x32_bf16 v[176:179], a[76:79], a[108:111], v[176:179]\n"
"s_sub_u32 s71, s64, s64\n"
"buffer_load_dwordx4 v11, s[52:55], s67 offen offset:2048 lds\n"
"v_mfma_f32_16x16x32_bf16 v[148:151], a[68:71], a[108:111], v[148:151]\n"
"s_sub_u32 s69, s69, s98\n"
"s_sub_u32 s67, 1684608, s75\n"
"v_mfma_f32_16x16x32_bf16 v[148:151], a[64:67], a[104:107], v[148:151]\n"
"s_sub_u32 s69, s69, s98\n"
"s_add_u32 s71, s71, 16384\n"
"v_mfma_f32_16x16x32_bf16 v[152:155], a[64:67], a[112:115], v[152:155]\n"
"s_sub_u32 s66, 81920, s73\n"
"s_sub_u32 s69, s69, s98\n"
"v_mfma_f32_16x16x32_bf16 v[152:155], a[68:71], a[116:119], v[152:155]\n"
"buffer_load_dwordx4 v11, s[52:55], s67 offen offset:3072 lds\n"
"v_mfma_f32_16x16x32_bf16 v[180:183], a[76:79], a[116:119], v[180:183]\n"
"s_sub_u32 s69, s69, s98\n"
"v_add_u32 v12, s66, v7\n"
"s_waitcnt lgkmcnt(0)\n"
"v_mfma_f32_16x16x32_bf16 v[180:183], a[72:75], a[112:115], v[180:183]\n"
"s_cmp_lt_u32 %16, 2\n"
"s_cselect_b32 s67, 16384, 0\n"
"v_add_u32 v20, s71, v9\n"
"v_mfma_f32_16x16x32_bf16 v[184:187], a[72:75], a[120:123], v[184:187]\n"
"s_add_u32 s67, s67, s72\n"
"v_add_u32 v13, s66, v8\n"
"v_mfma_f32_16x16x32_bf16 v[184:187], a[76:79], a[124:127], v[184:187]\n"
"s_add_u32 m0, s67, 139264\n"
"v_add_u32 v21, s71, v15\n"
"v_mfma_f32_16x16x32_bf16 v[156:159], a[68:71], a[124:127], v[156:159]\n"
"s_sub_u32 s67, 1792, s60\n"
"v_mfma_f32_16x16x32_bf16 v[156:159], a[64:67], a[120:123], v[156:159]\n"
"buffer_load_dwordx4 v6, s[48:51], s67 offen offset:0  lds\n"
"v_mfma_f32_16x16x32_bf16 v[160:163], a[64:67], a[128:131], v[160:163]\n"
"s_waitcnt vmcnt(17)\n"
"ds_read_b64_tr_b16 a[160:161], v20 offset:0\n"
"v_mfma_f32_16x16x32_bf16 v[160:163], a[68:71], a[132:135], v[160:163]\n"
"s_sub_u32 s67, 17152, s60\n"
"v_mfma_f32_16x16x32_bf16 v[188:191], a[76:79], a[132:135], v[188:191]\n"
"ds_read_b64_tr_b16 a[162:163], v20 offset:512\n"
"v_mfma_f32_16x16x32_bf16 v[188:191], a[72:75], a[128:131], v[188:191]\n"
"ds_read_b64_tr_b16 a[164:165], v20 offset:8\n"
"buffer_load_dwordx4 v6, s[48:51], s67 offen offset:1024  lds\n"
"v_mfma_f32_16x16x32_bf16 v[192:195], a[72:75], a[136:139], v[192:195]\n"
"ds_read_b64_tr_b16 a[166:167], v20 offset:520\n"
"v_mfma_f32_16x16x32_bf16 v[192:195], a[76:79], a[140:143], v[192:195]\n"
"s_sub_u32 s67, 32512, s60\n"
"v_mfma_f32_16x16x32_bf16 v[164:167], a[68:71], a[140:143], v[164:167]\n"
"ds_read_b64_tr_b16 a[168:169], v21 offset:0\n"
"v_mfma_f32_16x16x32_bf16 v[164:167], a[64:67], a[136:139], v[164:167]\n"
"ds_read_b64_tr_b16 a[170:171], v21 offset:512\n"
"buffer_load_dwordx4 v6, s[48:51], s67 offen offset:2048  lds\n"
"v_mfma_f32_16x16x32_bf16 v[168:171], a[64:67], a[144:147], v[168:171]\n"
"ds_read_b64_tr_b16 a[172:173], v21 offset:8\n"
"v_mfma_f32_16x16x32_bf16 v[168:171], a[68:71], a[148:151], v[168:171]\n"
"s_sub_u32 s67, 47872, s60\n"
"v_mfma_f32_16x16x32_bf16 v[196:199], a[76:79], a[148:151], v[196:199]\n"
"ds_read_b64_tr_b16 a[174:175], v21 offset:520\n"
"v_mfma_f32_16x16x32_bf16 v[196:199], a[72:75], a[144:147], v[196:199]\n"
"v_mfma_f32_16x16x32_bf16 a[20:23], a[72:75], a[152:155], a[20:23]\n"
"buffer_load_dwordx4 v6, s[48:51], s67 offen offset:3072  lds\n"
"v_mfma_f32_16x16x32_bf16 a[20:23], a[76:79], a[156:159], a[20:23]\n"
"v_mfma_f32_16x16x32_bf16 a[16:19], a[68:71], a[156:159], a[16:19]\n"
"s_add_u32 m0, s64, 20352\n"
"v_mfma_f32_16x16x32_bf16 a[16:19], a[64:67], a[152:155], a[16:19]\n"
"s_barrier\n"
"s_waitcnt vmcnt(12)\n"
"ds_read_b64_tr_b16 a[32:33], v9 offset:8192\n"
"v_mfma_f32_16x16x32_bf16 v[200:203], a[80:83], a[96:99], v[200:203]\n"
"s_waitcnt lgkmcnt(1)\n"
"ds_read_b64_tr_b16 a[34:35], v9 offset:8704\n"
"v_mfma_f32_16x16x32_bf16 v[200:203], a[84:87], a[100:103], v[200:203]\n"
"ds_read_b64_tr_b16 a[36:37], v9 offset:12288\n"
"buffer_load_dwordx4 v6, s[40:43], s88 offen offset:128  lds\n"
"v_mfma_f32_16x16x32_bf16 v[228:231], a[92:95], a[100:103], v[228:231]\n"
"ds_read_b128 a[100:103], v13 offset:32768\n"
"v_mfma_f32_16x16x32_bf16 v[228:231], a[88:91], a[96:99], v[228:231]\n"
"ds_read_b128 a[96:99], v12 offset:32768\n"
"v_mfma_f32_16x16x32_bf16 v[232:235], a[88:91], a[104:107], v[232:235]\n"
"ds_read_b64_tr_b16 a[38:39], v9 offset:12800\n"
"buffer_load_dwordx4 v6, s[40:43], s89 offen offset:1152  lds\n"
"v_mfma_f32_16x16x32_bf16 v[232:235], a[92:95], a[108:111], v[232:235]\n"
"ds_read_b64_tr_b16 a[40:41], v9 offset:8200\n"
"v_mfma_f32_16x16x32_bf16 v[204:207], a[84:87], a[108:111], v[204:207]\n"
"ds_read_b128 a[108:111], v13 offset:34816\n"
"v_mfma_f32_16x16x32_bf16 v[204:207], a[80:83], a[104:107], v[204:207]\n"
"ds_read_b128 a[104:107], v12 offset:34816\n"
"v_mfma_f32_16x16x32_bf16 v[208:211], a[80:83], a[112:115], v[208:211]\n"
"ds_read_b64_tr_b16 a[42:43], v9 offset:8712\n"
"v_mfma_f32_16x16x32_bf16 v[208:211], a[84:87], a[116:119], v[208:211]\n"
"ds_read_b64_tr_b16 a[44:45], v9 offset:12296\n"
"buffer_load_dwordx4 v6, s[40:43], s90 offen offset:2176  lds\n"
"v_mfma_f32_16x16x32_bf16 v[236:239], a[92:95], a[116:119], v[236:239]\n"
"ds_read_b128 a[116:119], v13 offset:36864\n"
"v_mfma_f32_16x16x32_bf16 v[236:239], a[88:91], a[112:115], v[236:239]\n"
"ds_read_b128 a[112:115], v12 offset:36864\n"
"v_mfma_f32_16x16x32_bf16 v[240:243], a[88:91], a[120:123], v[240:243]\n"
"ds_read_b64_tr_b16 a[46:47], v9 offset:12808\n"
"v_mfma_f32_16x16x32_bf16 v[240:243], a[92:95], a[124:127], v[240:243]\n"
"ds_read_b64_tr_b16 a[48:49], v15 offset:8192\n"
"buffer_load_dwordx4 v6, s[40:43], s91 offen offset:3200  lds\n"
"v_mfma_f32_16x16x32_bf16 v[212:215], a[84:87], a[124:127], v[212:215]\n"
"ds_read_b128 a[124:127], v13 offset:38912\n"
"v_mfma_f32_16x16x32_bf16 v[212:215], a[80:83], a[120:123], v[212:215]\n"
"ds_read_b128 a[120:123], v12 offset:38912\n"
"v_mfma_f32_16x16x32_bf16 v[216:219], a[80:83], a[128:131], v[216:219]\n"
"ds_read_b64_tr_b16 a[50:51], v15 offset:8704\n"
"v_mfma_f32_16x16x32_bf16 v[216:219], a[84:87], a[132:135], v[216:219]\n"
"ds_read_b64_tr_b16 a[52:53], v15 offset:12288\n"
"v_mfma_f32_16x16x32_bf16 v[244:247], a[92:95], a[132:135], v[244:247]\n"
"buffer_store_dwordx4 a[180:183], v0, s[36:39], %23 offen offset:2048 sc1\n"
"ds_read_b128 a[132:135], v13 offset:24576\n"
"s_getpc_b64 s[70:71]\n"
"L_c10_:\n"
"s_add_u32 s70, s70, L_P5-L_c10_\n"
"s_addc_u32 s71, s71, 0\n"
"v_mfma_f32_16x16x32_bf16 v[244:247], a[88:91], a[128:131], v[244:247]\n"
"s_lshr_b32 s66, %26, 3\n"
"ds_read_b128 a[128:131], v12 offset:24576\n"
"v_mfma_f32_16x16x32_bf16 v[248:251], a[88:91], a[136:139], v[248:251]\n"
"s_lshl2_add_u32 s66, s66, %16\n"
"ds_read_b64_tr_b16 a[54:55], v15 offset:12800\n"
"v_mfma_f32_16x16x32_bf16 v[248:251], a[92:95], a[140:143], v[248:251]\n"
"s_add_u32 m0, s64, 8192\n"
"ds_read_b64_tr_b16 a[56:57], v15 offset:8200\n"
"v_mfma_f32_16x16x32_bf16 v[220:223], a[84:87], a[140:143], v[220:223]\n"
"s_mul_i32 s66, s66, 256\n"
"ds_read_b128 a[140:143], v13 offset:26624\n"
"v_mfma_f32_16x16x32_bf16 v[220:223], a[80:83], a[136:139], v[220:223]\n"
"s_min_u32 s66, s66, L_end-L_P5-256\n"
"ds_read_b128 a[136:139], v12 offset:26624\n"
"v_mfma_f32_16x16x32_bf16 v[224:227], a[80:83], a[144:147], v[224:227]\n"
"ds_read_b64_tr_b16 a[58:59], v15 offset:8712\n"
"v_min_u32 v22, 16, v0\n"
"v_mfma_f32_16x16x32_bf16 v[224:227], a[84:87], a[148:151], v[224:227]\n"
"ds_read_b64_tr_b16 a[60:61], v15 offset:12296\n"
"v_mfma_f32_16x16x32_bf16 v[252:255], a[92:95], a[148:151], v[252:255]\n"
"ds_read_b128 a[148:151], v13 offset:28672\n"
"v_mfma_f32_16x16x32_bf16 v[252:255], a[88:91], a[144:147], v[252:255]\n"
"ds_read_b128 a[144:147], v12 offset:28672\n"
"s_sub_u32 s67, 1835008, s75\n"
"ds_read_b64_tr_b16 a[62:63], v15 offset:12808\n"
"v_mfma_f32_16x16x32_bf16 a[28:31], a[88:91], a[152:155], a[28:31]\n"
"v_lshl_add_u32 v22, v22, 3, s66\n"
"v_mfma_f32_16x16x32_bf16 a[28:31], a[92:95], a[156:159], a[28:31]\n"
"global_load_dword v23, v22, s[70:71]\n"
"v_mfma_f32_16x16x32_bf16 a[24:27], a[84:87], a[156:159], a[24:27]\n"
"ds_read_b128 a[156:159], v13 offset:30720\n"
"v_mfma_f32_16x16x32_bf16 a[24:27], a[80:83], a[152:155], a[24:27]\n"
"s_waitcnt lgkmcnt(15)\n"
"ds_read_b128 a[152:155], v12 offset:30720\n"
"v_mfma_f32_16x16x32_bf16 v[32:35], a[32:35], a[96:99], v[32:35]\n"
"v_mfma_f32_16x16x32_bf16 v[32:35], a[36:39], a[100:103], v[32:35]\n"
"s_waitcnt lgkmcnt(2)\n"
"v_mfma_f32_16x16x32_bf16 v[60:63], a[44:47], a[100:103], v[60:63]\n"
"buffer_load_dwordx4 v11, s[52:55], s67 offen offset:0 lds\n"
"v_mfma_f32_16x16x32_bf16 v[60:63], a[40:43], a[96:99], v[60:63]\n"
"v_mfma_f32_16x16x32_bf16 v[64:67], a[40:43], a[104:107], v[64:67]\n"
"s_sub_u32 s67, 1850368, s75\n"
"v_mfma_f32_16x16x32_bf16 v[64:67], a[44:47], a[108:111], v[64:67]\n"
"v_mfma_f32_16x16x32_bf16 v[36:39], a[36:39], a[108:111], v[36:39]\n"
"buffer_load_dwordx4 v11, s[52:55], s67 offen offset:1024 lds\n"
"v_mfma_f32_16x16x32_bf16 v[36:39], a[32:35], a[104:107], v[36:39]\n"
"v_mfma_f32_16x16x32_bf16 v[40:43], a[32:35], a[112:115], v[40:43]\n"
"s_sub_u32 s67, 1865728, s75\n"
"v_mfma_f32_16x16x32_bf16 v[40:43], a[36:39], a[116:119], v[40:43]\n"
"v_mfma_f32_16x16x32_bf16 v[68:71], a[44:47], a[116:119], v[68:71]\n"
"v_mfma_f32_16x16x32_bf16 v[68:71], a[40:43], a[112:115], v[68:71]\n"
"buffer_load_dwordx4 v11, s[52:55], s67 offen offset:2048 lds\n"
"v_mfma_f32_16x16x32_bf16 v[72:75], a[40:43], a[120:123], v[72:75]\n"
"v_mfma_f32_16x16x32_bf16 v[72:75], a[44:47], a[124:127], v[72:75]\n"
"s_sub_u32 s67, 1881088, s75\n"
"v_mfma_f32_16x16x32_bf16 v[44:47], a[36:39], a[124:127], v[44:47]\n"
"v_mfma_f32_16x16x32_bf16 v[44:47], a[32:35], a[120:123], v[44:47]\n"
"buffer_load_dwordx4 v11, s[52:55], s67 offen offset:3072 lds\n"
"v_mfma_f32_16x16x32_bf16 v[48:51], a[32:35], a[128:131], v[48:51]\n"
"v_mfma_f32_16x16x32_bf16 v[48:51], a[36:39], a[132:135], v[48:51]\n"
"s_add_u32 m0, s64, 12288\n"
"v_mfma_f32_16x16x32_bf16 v[76:79], a[44:47], a[132:135], v[76:79]\n"
"s_waitcnt vmcnt(14)\n"
"ds_read_b64_tr_b16 a[64:65], v9 offset:0\n"
"v_mfma_f32_16x16x32_bf16 v[76:79], a[40:43], a[128:131], v[76:79]\n"
"s_sub_u32 s67, 1900544, s75\n"
"ds_read_b64_tr_b16 a[66:67], v9 offset:512\n"
"v_mfma_f32_16x16x32_bf16 v[80:83], a[40:43], a[136:139], v[80:83]\n"
"ds_read_b64_tr_b16 a[68:69], v9 offset:4096\n"
"v_mfma_f32_16x16x32_bf16 v[80:83], a[44:47], a[140:143], v[80:83]\n"
"ds_read_b64_tr_b16 a[70:71], v9 offset:4608\n"
"buffer_load_dwordx4 v11, s[52:55], s67 offen offset:0 lds\n"
"v_mfma_f32_16x16x32_bf16 v[52:55], a[36:39], a[140:143], v[52:55]\n"
"s_sub_u32 s67, 1915904, s75\n"
"ds_read_b64_tr_b16 a[72:73], v9 offset:8\n"
"v_mfma_f32_16x16x32_bf16 v[52:55], a[32:35], a[136:139], v[52:55]\n"
"s_waitcnt lgkmcnt(5)\n"
"ds_read_b64_tr_b16 a[74:75], v9 offset:520\n"
"v_mfma_f32_16x16x32_bf16 v[56:59], a[32:35], a[144:147], v[56:59]\n"
"ds_read_b64_tr_b16 a[76:77], v9 offset:4104\n"
"v_mfma_f32_16x16x32_bf16 v[56:59], a[36:39], a[148:151], v[56:59]\n"
"ds_read_b64_tr_b16 a[78:79], v9 offset:4616\n"
"v_mfma_f32_16x16x32_bf16 v[84:87], a[44:47], a[148:151], v[84:87]\n"
"buffer_load_dwordx4 v11, s[52:55], s67 offen offset:1024 lds\n"
"v_mfma_f32_16x16x32_bf16 v[84:87], a[40:43], a[144:147], v[84:87]\n"
"ds_read_b64_tr_b16 a[80:81], v15 offset:0\n"
"v_mfma_f32_16x16x32_bf16 a[4:7], a[40:43], a[152:155], a[4:7]\n"
"s_sub_u32 s67, 1931264, s75\n"
"ds_read_b64_tr_b16 a[82:83], v15 offset:512\n"
"v_mfma_f32_16x16x32_bf16 a[4:7], a[44:47], a[156:159], a[4:7]\n"
"ds_read_b64_tr_b16 a[84:85], v15 offset:4096\n"
"v_mfma_f32_16x16x32_bf16 a[0:3], a[36:39], a[156:159], a[0:3]\n"
"ds_read_b64_tr_b16 a[86:87], v15 offset:4608\n"
"buffer_load_dwordx4 v11, s[52:55], s67 offen offset:2048 lds\n"
"v_mfma_f32_16x16x32_bf16 a[0:3], a[32:35], a[152:155], a[0:3]\n"
"ds_read_b64_tr_b16 a[88:89], v15 offset:8\n"
"v_mfma_f32_16x16x32_bf16 v[88:91], a[48:51], a[96:99], v[88:91]\n"
"s_sub_u32 s67, 1946624, s75\n"
"ds_read_b64_tr_b16 a[90:91], v15 offset:520\n"
"v_mfma_f32_16x16x32_bf16 v[88:91], a[52:55], a[100:103], v[88:91]\n"
"ds_read_b64_tr_b16 a[92:93], v15 offset:4104\n"
"v_mfma_f32_16x16x32_bf16 v[116:119], a[60:63], a[100:103], v[116:119]\n"
"ds_read_b64_tr_b16 a[94:95], v15 offset:4616\n"
"v_mfma_f32_16x16x32_bf16 v[116:119], a[56:59], a[96:99], v[116:119]\n"
"buffer_load_dwordx4 v11, s[52:55], s67 offen offset:3072 lds\n"
"v_mfma_f32_16x16x32_bf16 v[120:123], a[56:59], a[104:107], v[120:123]\n"
"v_mfma_f32_16x16x32_bf16 v[120:123], a[60:63], a[108:111], v[120:123]\n"
"s_add_u32 m0, s64, 16256\n"
"v_mfma_f32_16x16x32_bf16 v[92:95], a[52:55], a[108:111], v[92:95]\n"
"v_mfma_f32_16x16x32_bf16 v[92:95], a[48:51], a[104:107], v[92:95]\n"
"buffer_load_dwordx4 v6, s[40:43], s92 offen offset:128  lds\n"
"v_mfma_f32_16x16x32_bf16 v[96:99], a[48:51], a[112:115], v[96:99]\n"
"s_waitcnt lgkmcnt(0)\n"
"v_mfma_f32_16x16x32_bf16 v[96:99], a[52:55], a[116:119], v[96:99]\n"
"s_mov_b32 s98, 0\n"
"v_mfma_f32_16x16x32_bf16 v[124:127], a[60:63], a[116:119], v[124:127]\n"
"s_load_dword s98, s[96:97], 128 glc\n"
"buffer_load_dwordx4 v6, s[40:43], s93 offen offset:1152  lds\n"
"v_mfma_f32_16x16x32_bf16 v[124:127], a[56:59], a[112:115], v[124:127]\n"
"v_mfma_f32_16x16x32_bf16 v[128:131], a[56:59], a[120:123], v[128:131]\n"
"v_mfma_f32_16x16x32_bf16 v[128:131], a[60:63], a[124:127], v[128:131]\n"
"v_mfma_f32_16x16x32_bf16 v[100:103], a[52:55], a[124:127], v[100:103]\n"
"v_mfma_f32_16x16x32_bf16 v[100:103], a[48:51], a[120:123], v[100:103]\n"
"buffer_load_dwordx4 v6, s[40:43], s94 offen offset:2176  lds\n"
"v_mfma_f32_16x16x32_bf16 v[104:107], a[48:51], a[128:131], v[104:107]\n"
"v_mfma_f32_16x16x32_bf16 v[104:107], a[52:55], a[132:135], v[104:107]\n"
"v_mfma_f32_16x16x32_bf16 v[132:135], a[60:63], a[132:135], v[132:135]\n"
"v_mfma_f32_16x16x32_bf16 v[132:135], a[56:59], a[128:131], v[132:135]\n"
"buffer_load_dwordx4 v6, s[40:43], s95 offen offset:3200  lds\n"
"v_mfma_f32_16x16x32_bf16 v[136:139], a[56:59], a[136:139], v[136:139]\n"
"v_mfma_f32_16x16x32_bf16 v[136:139], a[60:63], a[140:143], v[136:139]\n"
"s_add_u32 m0, s64, 0\n"
"v_mfma_f32_16x16x32_bf16 v[108:111], a[52:55], a[140:143], v[108:111]\n"
"v_mfma_f32_16x16x32_bf16 v[108:111], a[48:51], a[136:139], v[108:111]\n"
"s_add_u32 s69, s69, s98\n"
"s_sub_u32 s67, 1835136, s75\n"
"v_mfma_f32_16x16x32_bf16 v[112:115], a[48:51], a[144:147], v[112:115]\n"
"s_add_u32 s69, s69, s98\n"
"v_mfma_f32_16x16x32_bf16 v[112:115], a[52:55], a[148:151], v[112:115]\n"
"buffer_load_dwordx4 v11, s[52:55], s67 offen offset:0 lds\n"
"v_mfma_f32_16x16x32_bf16 v[140:143], a[60:63], a[148:151], v[140:143]\n"
"s_add_u32 s69, s69, s98\n"
"buffer_load_dwordx4 v[24:27], v0, s[56:59], %25 offen sc1\n"
"v_mfma_f32_16x16x32_bf16 v[140:143], a[56:59], a[144:147], v[140:143]\n"
"s_add_u32 s69, s69, s98\n"
"s_sub_u32 s67, 1850496, s75\n"
"v_mfma_f32_16x16x32_bf16 a[12:15], a[56:59], a[152:155], a[12:15]\n"
"s_add_u32 s69, s69, s98\n"
"v_mfma_f32_16x16x32_bf16 a[12:15], a[60:63], a[156:159], a[12:15]\n"
"buffer_load_dwordx4 v11, s[52:55], s67 offen offset:1024 lds\n"
"v_mfma_f32_16x16x32_bf16 a[8:11], a[52:55], a[156:159], a[8:11]\n"
"s_add_u32 s69, s69, s98\n"
"v_mfma_f32_16x16x32_bf16 a[8:11], a[48:51], a[152:155], a[8:11]\n"
"s_add_u32 s69, s69, s98\n"
"s_sub_u32 s67, 1865856, s75\n"
"v_mfma_f32_16x16x32_bf16 v[144:147], a[64:67], a[96:99], v[144:147]\n"
"s_add_u32 s69, s69, s98\n"
"v_mfma_f32_16x16x32_bf16 v[144:147], a[68:71], a[100:103], v[144:147]\n"
"buffer_load_dwordx4 v11, s[52:55], s67 offen offset:2048 lds\n"
"v_mfma_f32_16x16x32_bf16 v[172:175], a[76:79], a[100:103], v[172:175]\n"
"s_add_u32 s69, s69, s98\n"
"v_mfma_f32_16x16x32_bf16 v[172:175], a[72:75], a[96:99], v[172:175]\n"
"s_add_u32 s69, s69, s98\n"
"s_sub_u32 s67, 1881216, s75\n"
"v_mfma_f32_16x16x32_bf16 v[176:179], a[72:75], a[104:107], v[176:179]\n"
"s_add_u32 s69, s69, s98\n"
"v_mfma_f32_16x16x32_bf16 v[176:179], a[76:79], a[108:111], v[176:179]\n"
"s_sub_u32 s71, s64, s64\n"
"v_mfma_f32_16x16x32_bf16 v[148:151], a[68:71], a[108:111], v[148:151]\n"
"s_add_u32 s69, s69, s98\n"
"buffer_load_dwordx4 v11, s[52:55], s67 offen offset:3072 lds\n"
"v_mfma_f32_16x16x32_bf16 v[148:151], a[64:67], a[104:107], v[148:151]\n"
"s_add_u32 s69, s69, s98\n"
"s_add_u32 s71, s71, 20480\n"
"v_mfma_f32_16x16x32_bf16 v[152:155], a[64:67], a[112:115], v[152:155]\n"
"s_sub_u32 s66, 122880, s73\n"
"s_add_u32 s69, s69, s98\n"
"v_mfma_f32_16x16x32_bf16 v[152:155], a[68:71], a[116:119], v[152:155]\n"
"s_add_u32 m0, s64, 4096\n"
"s_sub_u32 s67, 1900672, s75\n"
"v_mfma_f32_16x16x32_bf16 v[180:183], a[76:79], a[116:119], v[180:183]\n"
"s_add_u32 s69, s69, s98\n"
"v_add_u32 v12, s66, v7\n"
"s_waitcnt lgkmcnt(0)\n"
"v_mfma_f32_16x16x32_bf16 v[180:183], a[72:75], a[112:115], v[180:183]\n"
"v_add_u32 v20, s71, v7\n"
"v_mfma_f32_16x16x32_bf16 v[184:187], a[72:75], a[120:123], v[184:187]\n"
"buffer_load_dwordx4 v11, s[52:55], s67 offen offset:0 lds\n"
"v_add_u32 v13, s66, v8\n"
"v_mfma_f32_16x16x32_bf16 v[184:187], a[76:79], a[124:127], v[184:187]\n"
"v_add_u32 v21, s71, v8\n"
"v_mfma_f32_16x16x32_bf16 v[156:159], a[68:71], a[124:127], v[156:159]\n"
"s_sub_u32 s67, 1916032, s75\n"
"v_mfma_f32_16x16x32_bf16 v[156:159], a[64:67], a[120:123], v[156:159]\n"
"v_mfma_f32_16x16x32_bf16 v[160:163], a[64:67], a[128:131], v[160:163]\n"
"s_waitcnt vmcnt(20)\n"
"ds_read_b128 a[224:227], v20 offset:0\n"
"v_mfma_f32_16x16x32_bf16 v[160:163], a[68:71], a[132:135], v[160:163]\n"
"buffer_load_dwordx4 v11, s[52:55], s67 offen offset:1024 lds\n"
"v_mfma_f32_16x16x32_bf16 v[188:191], a[76:79], a[132:135], v[188:191]\n"
"ds_read_b128 a[228:231], v21 offset:0\n"
"v_mfma_f32_16x16x32_bf16 v[188:191], a[72:75], a[128:131], v[188:191]\n"
"s_sub_u32 s67, 1931392, s75\n"
"ds_read_b128 a[232:235], v20 offset:2048\n"
"v_mfma_f32_16x16x32_bf16 v[192:195], a[72:75], a[136:139], v[192:195]\n"
"buffer_load_dwordx4 v11, s[52:55], s67 offen offset:2048 lds\n"
"v_mfma_f32_16x16x32_bf16 v[192:195], a[76:79], a[140:143], v[192:195]\n"
"ds_read_b128 a[236:239], v21 offset:2048\n"
"v_mfma_f32_16x16x32_bf16 v[164:167], a[68:71], a[140:143], v[164:167]\n"
"s_sub_u32 s67, 1946752, s75\n"
"v_mfma_f32_16x16x32_bf16 v[164:167], a[64:67], a[136:139], v[164:167]\n"
"v_mfma_f32_16x16x32_bf16 v[168:171], a[64:67], a[144:147], v[168:171]\n"
"v_mfma_f32_16x16x32_bf16 v[168:171], a[68:71], a[148:151], v[168:171]\n"
"buffer_load_dwordx4 v11, s[52:55], s67 offen offset:3072 lds\n"
"v_mfma_f32_16x16x32_bf16 v[196:199], a[76:79], a[148:151], v[196:199]\n"
"v_mfma_f32_16x16x32_bf16 v[196:199], a[72:75], a[144:147], v[196:199]\n"
"s_waitcnt lgkmcnt(0)\n"
"v_mfma_f32_16x16x32_bf16 a[20:23], a[72:75], a[152:155], a[20:23]\n"
"v_mfma_f32_16x16x32_bf16 a[20:23], a[76:79], a[156:159], a[20:23]\n"
"v_mfma_f32_16x16x32_bf16 a[16:19], a[68:71], a[156:159], a[16:19]\n"
"v_mfma_f32_16x16x32_bf16 a[16:19], a[64:67], a[152:155], a[16:19]\n"
"s_barrier\n"
"s_waitcnt vmcnt(13)\n"
"ds_read_b64_tr_b16 a[32:33], v9 offset:8192\n"
"v_mfma_f32_16x16x32_bf16 v[200:203], a[80:83], a[96:99], v[200:203]\n"
"buffer_store_dwordx4 a[184:187], v1, s[36:39], %23 offen offset:0 sc1\n"
"ds_read_b64_tr_b16 a[34:35], v9 offset:8704\n"
"v_mfma_f32_16x16x32_bf16 v[200:203], a[84:87], a[100:103], v[200:203]\n"
"ds_read_b64_tr_b16 a[36:37], v9 offset:12288\n"
"v_mfma_f32_16x16x32_bf16 v[228:231], a[92:95], a[100:103], v[228:231]\n"
"ds_read_b128 a[100:103], v13 offset:32768\n"
"v_mfma_f32_16x16x32_bf16 v[228:231], a[88:91], a[96:99], v[228:231]\n"
"ds_read_b128 a[96:99], v12 offset:32768\n"
"v_mfma_f32_16x16x32_bf16 v[232:235], a[88:91], a[104:107], v[232:235]\n"
"ds_read_b64_tr_b16 a[38:39], v9 offset:12800\n"
"v_mfma_f32_16x16x32_bf16 v[232:235], a[92:95], a[108:111], v[232:235]\n"
"ds_read_b64_tr_b16 a[40:41], v9 offset:8200\n"
"v_mfma_f32_16x16x32_bf16 v[204:207], a[84:87], a[108:111], v[204:207]\n"
"ds_read_b128 a[108:111], v13 offset:34816\n"
"v_mfma_f32_16x16x32_bf16 v[204:207], a[80:83], a[104:107], v[204:207]\n"
"ds_read_b128 a[104:107], v12 offset:34816\n"
"v_mfma_f32_16x16x32_bf16 v[208:211], a[80:83], a[112:115], v[208:211]\n"
"ds_read_b64_tr_b16 a[42:43], v9 offset:8712\n"
"v_mfma_f32_16x16x32_bf16 v[208:211], a[84:87], a[116:119], v[208:211]\n"
"ds_read_b64_tr_b16 a[44:45], v9 offset:12296\n"
"v_mfma_f32_16x16x32_bf16 v[236:239], a[92:95], a[116:119], v[236:239]\n"
"ds_read_b128 a[116:119], v13 offset:36864\n"
"v_mfma_f32_16x16x32_bf16 v[236:239], a[88:91], a[112:115], v[236:239]\n"
"buffer_store_dwordx4 a[188:191], v1, s[36:39], %23 offen offset:2048 sc1\n"
"ds_read_b128 a[112:115], v12 offset:36864\n"
"v_mfma_f32_16x16x32_bf16 v[240:243], a[88:91], a[120:123], v[240:243]\n"
"ds_read_b64_tr_b16 a[46:47], v9 offset:12808\n"
"v_mfma_f32_16x16x32_bf16 v[240:243], a[92:95], a[124:127], v[240:243]\n"
"ds_read_b64_tr_b16 a[48:49], v15 offset:8192\n"
"v_mfma_f32_16x16x32_bf16 v[212:215], a[84:87], a[124:127], v[212:215]\n"
"ds_read_b128 a[124:127], v13 offset:38912\n"
"v_mfma_f32_16x16x32_bf16 v[212:215], a[80:83], a[120:123], v[212:215]\n"
"s_add_u32 s74, %23, 524288\n"
"ds_read_b128 a[120:123], v12 offset:38912\n"
"v_mfma_f32_16x16x32_bf16 v[216:219], a[80:83], a[128:131], v[216:219]\n"
"ds_read_b64_tr_b16 a[50:51], v15 offset:8704\n"
"v_mfma_f32_16x16x32_bf16 v[216:219], a[84:87], a[132:135], v[216:219]\n"
"ds_read_b64_tr_b16 a[52:53], v15 offset:12288\n"
"v_mfma_f32_16x16x32_bf16 v[244:247], a[92:95], a[132:135], v[244:247]\n"
"ds_read_b128 a[132:135], v13 offset:24576\n"
"v_mfma_f32_16x16x32_bf16 v[244:247], a[88:91], a[128:131], v[244:247]\n"
"ds_read_b128 a[128:131], v12 offset:24576\n"
"v_mfma_f32_16x16x32_bf16 v[248:251], a[88:91], a[136:139], v[248:251]\n"
"ds_read_b64_tr_b16 a[54:55], v15 offset:12800\n"
"v_mfma_f32_16x16x32_bf16 v[248:251], a[92:95], a[140:143], v[248:251]\n"
"ds_read_b64_tr_b16 a[56:57], v15 offset:8200\n"
"v_mfma_f32_16x16x32_bf16 v[220:223], a[84:87], a[140:143], v[220:223]\n"
"ds_read_b128 a[140:143], v13 offset:26624\n"
"v_mfma_f32_16x16x32_bf16 v[220:223], a[80:83], a[136:139], v[220:223]\n"
"ds_read_b128 a[136:139], v12 offset:26624\n"
"v_mfma_f32_16x16x32_bf16 v[224:227], a[80:83], a[144:147], v[224:227]\n"
"buffer_store_dwordx4 a[160:163], v0, s[36:39], s74 offen offset:0 sc1\n"
"ds_read_b64_tr_b16 a[58:59], v15 offset:8712\n"
"v_mfma_f32_16x16x32_bf16 v[224:227], a[84:87], a[148:151], v[224:227]\n"
"ds_read_b64_tr_b16 a[60:61], v15 offset:12296\n"
"v_mfma_f32_16x16x32_bf16 v[252:255], a[92:95], a[148:151], v[252:255]\n"
"ds_read_b128 a[148:151], v13 offset:28672\n"
"v_mfma_f32_16x16x32_bf16 v[252:255], a[88:91], a[144:147], v[252:255]\n"
"ds_read_b128 a[144:147], v12 offset:28672\n"
"s_sub_u32 s71, s64, s64\n"
"ds_read_b64_tr_b16 a[62:63], v15 offset:12808\n"
"v_mfma_f32_16x16x32_bf16 a[28:31], a[88:91], a[152:155], a[28:31]\n"
"v_mfma_f32_16x16x32_bf16 a[28:31], a[92:95], a[156:159], a[28:31]\n"
"v_mfma_f32_16x16x32_bf16 a[24:27], a[84:87], a[156:159], a[24:27]\n"
"s_add_u32 s71, s71, 16384\n"
"ds_read_b128 a[156:159], v13 offset:30720\n"
"v_mfma_f32_16x16x32_bf16 a[24:27], a[80:83], a[152:155], a[24:27]\n"
"s_waitcnt lgkmcnt(15)\n"
"ds_read_b128 a[152:155], v12 offset:30720\n"
"v_mfma_f32_16x16x32_bf16 v[32:35], a[32:35], a[96:99], v[32:35]\n"
"v_add_u32 v20, s71, v7\n"
"v_mfma_f32_16x16x32_bf16 v[32:35], a[36:39], a[100:103], v[32:35]\n"
"s_waitcnt lgkmcnt(2)\n"
"v_mfma_f32_16x16x32_bf16 v[60:63], a[44:47], a[100:103], v[60:63]\n"
"v_mfma_f32_16x16x32_bf16 v[60:63], a[40:43], a[96:99], v[60:63]\n"
"v_add_u32 v21, s71, v8\n"
"v_mfma_f32_16x16x32_bf16 v[64:67], a[40:43], a[104:107], v[64:67]\n"
"buffer_store_dwordx4 a[164:167], v0, s[36:39], s74 offen offset:2048 sc1\n"
"v_mfma_f32_16x16x32_bf16 v[64:67], a[44:47], a[108:111], v[64:67]\n"
"v_mfma_f32_16x16x32_bf16 v[36:39], a[36:39], a[108:111], v[36:39]\n"
"s_waitcnt vmcnt(13)\n"
"ds_read_b128 a[240:243], v20 offset:0\n"
"v_mfma_f32_16x16x32_bf16 v[36:39], a[32:35], a[104:107], v[36:39]\n"
"v_mfma_f32_16x16x32_bf16 v[40:43], a[32:35], a[112:115], v[40:43]\n"
"ds_read_b128 a[244:247], v21 offset:0\n"
"v_mfma_f32_16x16x32_bf16 v[40:43], a[36:39], a[116:119], v[40:43]\n"
"ds_read_b128 a[248:251], v20 offset:2048\n"
"v_mfma_f32_16x16x32_bf16 v[68:71], a[44:47], a[116:119], v[68:71]\n"
"s_add_u32 s70, s78, 1\n"
"v_mfma_f32_16x16x32_bf16 v[68:71], a[40:43], a[112:115], v[68:71]\n"
"ds_read_b128 a[252:255], v21 offset:2048\n"
"v_mfma_f32_16x16x32_bf16 v[72:75], a[40:43], a[120:123], v[72:75]\n"
"v_mov_b32 v16, s69\n"
"v_mfma_f32_16x16x32_bf16 v[72:75], a[44:47], a[124:127], v[72:75]\n"
"v_mfma_f32_16x16x32_bf16 v[44:47], a[36:39], a[124:127], v[44:47]\n"
"v_mfma_f32_16x16x32_bf16 v[44:47], a[32:35], a[120:123], v[44:47]\n"
"v_mfma_f32_16x16x32_bf16 v[48:51], a[32:35], a[128:131], v[48:51]\n"
"v_mfma_f32_16x16x32_bf16 v[48:51], a[36:39], a[132:135], v[48:51]\n"
"s_waitcnt lgkmcnt(0)\n"
"buffer_store_dwordx4 a[168:171], v1, s[36:39], s74 offen offset:0 sc1\n"
"ds_write_b32 v2, v16 offset:8192\n"
"v_mfma_f32_16x16x32_bf16 v[76:79], a[44:47], a[132:135], v[76:79]\n"
"s_waitcnt vmcnt(5)\n"
"ds_read_b64_tr_b16 a[64:65], v9 offset:0\n"
"v_mfma_f32_16x16x32_bf16 v[76:79], a[40:43], a[128:131], v[76:79]\n"
"ds_read_b64_tr_b16 a[66:67], v9 offset:512\n"
"v_mfma_f32_16x16x32_bf16 v[80:83], a[40:43], a[136:139], v[80:83]\n"
"ds_read_b64_tr_b16 a[68:69], v9 offset:4096\n"
"v_mfma_f32_16x16x32_bf16 v[80:83], a[44:47], a[140:143], v[80:83]\n"
"ds_read_b64_tr_b16 a[70:71], v9 offset:4608\n"
"v_mfma_f32_16x16x32_bf16 v[52:55], a[36:39], a[140:143], v[52:55]\n"
"ds_read_b64_tr_b16 a[72:73], v9 offset:8\n"
"v_subrev_u32 v24, s70, v24\n"
"v_mfma_f32_16x16x32_bf16 v[52:55], a[32:35], a[136:139], v[52:55]\n"
"ds_read_b64_tr_b16 a[74:75], v9 offset:520\n"
"v_xor_b32 v25, s79, v25\n"
"v_mfma_f32_16x16x32_bf16 v[56:59], a[32:35], a[144:147], v[56:59]\n"
"ds_read_b64_tr_b16 a[76:77], v9 offset:4104\n"
"v_subrev_u32 v26, s70, v26\n"
"v_mfma_f32_16x16x32_bf16 v[56:59], a[36:39], a[148:151], v[56:59]\n"
"ds_read_b64_tr_b16 a[78:79], v9 offset:4616\n"
"v_xor_b32 v27, s79, v27\n"
"v_mfma_f32_16x16x32_bf16 v[84:87], a[44:47], a[148:151], v[84:87]\n"
"ds_read_b64_tr_b16 a[80:81], v15 offset:0\n"
"v_or_b32 v24, v24, v26\n"
"v_mfma_f32_16x16x32_bf16 v[84:87], a[40:43], a[144:147], v[84:87]\n"
"ds_read_b64_tr_b16 a[82:83], v15 offset:512\n"
"v_or_b32 v25, v25, v27\n"
"v_mfma_f32_16x16x32_bf16 a[4:7], a[40:43], a[152:155], a[4:7]\n"
"ds_read_b64_tr_b16 a[84:85], v15 offset:4096\n"
"v_or_b32 v24, v24, v25\n"
"v_mfma_f32_16x16x32_bf16 a[4:7], a[44:47], a[156:159], a[4:7]\n"
"ds_read_b64_tr_b16 a[86:87], v15 offset:4608\n"
"v_cmp_lt_u32 vcc, 15, v24\n"
"v_mfma_f32_16x16x32_bf16 a[0:3], a[36:39], a[156:159], a[0:3]\n"
"buffer_store_dwordx4 a[172:175], v1, s[36:39], s74 offen offset:2048 sc1\n"
"ds_read_b64_tr_b16 a[88:89], v15 offset:8\n"
"s_cbranch_vccnz L_slow3\n"
"L_back3:\n"
"v_mfma_f32_16x16x32_bf16 a[0:3], a[32:35], a[152:155], a[0:3]\n"
"ds_read_b64_tr_b16 a[90:91], v15 offset:520\n"
"v_mfma_f32_16x16x32_bf16 v[88:91], a[48:51], a[96:99], v[88:91]\n"
"s_waitcnt lgkmcnt(14)\n"
"ds_read_b64_tr_b16 a[92:93], v15 offset:4104\n"
"v_mfma_f32_16x16x32_bf16 v[88:91], a[52:55], a[100:103], v[88:91]\n"
"global_load_dwordx4 a[32:35], v0, s[34:35] offset:-4096 sc1\n"
"s_barrier\n"
"v_mfma_f32_16x16x32_bf16 v[116:119], a[60:63], a[100:103], v[116:119]\n"
"ds_read_b64_tr_b16 a[94:95], v15 offset:4616\n"
"v_mfma_f32_16x16x32_bf16 v[116:119], a[56:59], a[96:99], v[116:119]\n"
"ds_write_b64 v2, a[192:193] offset:16384\n"
"v_mfma_f32_16x16x32_bf16 v[120:123], a[56:59], a[104:107], v[120:123]\n"
"ds_write_b64 v2, a[194:195] offset:16392\n"
"v_mfma_f32_16x16x32_bf16 v[120:123], a[60:63], a[108:111], v[120:123]\n"
"ds_read_b32 v17, v3 offset:8192\n"
"v_mfma_f32_16x16x32_bf16 v[92:95], a[52:55], a[108:111], v[92:95]\n"
"ds_read_b128 a[160:163], v7 offset:24576\n"
"v_mfma_f32_16x16x32_bf16 v[92:95], a[48:51], a[104:107], v[92:95]\n"
"ds_read_b32 v18, v4 offset:8192\n"
"v_mfma_f32_16x16x32_bf16 v[96:99], a[48:51], a[112:115], v[96:99]\n"
"s_waitcnt lgkmcnt(5)\n"
"ds_read_b128 a[164:167], v8 offset:24576\n"
"v_mfma_f32_16x16x32_bf16 v[96:99], a[52:55], a[116:119], v[96:99]\n"
"global_load_dwordx4 a[36:39], v0, s[34:35] offset:-3072 sc1\n"
"v_mfma_f32_16x16x32_bf16 v[124:127], a[60:63], a[116:119], v[124:127]\n"
"ds_read_b128 a[168:171], v7 offset:26624\n"
"v_mfma_f32_16x16x32_bf16 v[124:127], a[56:59], a[112:115], v[124:127]\n"
"ds_read_b128 a[172:175], v8 offset:26624\n"
"v_mfma_f32_16x16x32_bf16 v[128:131], a[56:59], a[120:123], v[128:131]\n"
"ds_read_b32 v19, v5 offset:8192\n"
"v_mfma_f32_16x16x32_bf16 v[128:131], a[60:63], a[124:127], v[128:131]\n"
"ds_read_b128 a[176:179], v7 offset:28672\n"
"v_mfma_f32_16x16x32_bf16 v[100:103], a[52:55], a[124:127], v[100:103]\n"
"ds_read_b128 a[180:183], v8 offset:28672\n"
"v_mfma_f32_16x16x32_bf16 v[100:103], a[48:51], a[120:123], v[100:103]\n"
"ds_write_b64 v2, a[196:197] offset:17408\n"
"v_mfma_f32_16x16x32_bf16 v[104:107], a[48:51], a[128:131], v[104:107]\n"
"ds_read_b128 a[184:187], v7 offset:30720\n"
"v_mfma_f32_16x16x32_bf16 v[104:107], a[52:55], a[132:135], v[104:107]\n"
"global_load_dwordx4 a[40:43], v0, s[34:35] offset:-2048 sc1\n"
"v_mfma_f32_16x16x32_bf16 v[132:135], a[60:63], a[132:135], v[132:135]\n"
"ds_read_b128 a[188:191], v8 offset:30720\n"
"v_mfma_f32_16x16x32_bf16 v[132:135], a[56:59], a[128:131], v[132:135]\n"
"ds_write_b64 v2, a[198:199] offset:17416\n"
"v_mfma_f32_16x16x32_bf16 v[136:139], a[56:59], a[136:139], v[136:139]\n"
"ds_write_b64 v2, a[200:201] offset:18432\n"
"v_mfma_f32_16x16x32_bf16 v[136:139], a[60:63], a[140:143], v[136:139]\n"
"ds_write_b64 v2, a[202:203] offset:18440\n"
"v_mfma_f32_16x16x32_bf16 v[108:111], a[52:55], a[140:143], v[108:111]\n"
"ds_write_b64 v2, a[204:205] offset:19456\n"
"v_mfma_f32_16x16x32_bf16 v[108:111], a[48:51], a[136:139], v[108:111]\n"
"s_waitcnt lgkmcnt(9)\n"
"v_add_u32 v16, v16, v17\n"
"v_mfma_f32_16x16x32_bf16 v[112:115], a[48:51], a[144:147], v[112:115]\n"
"ds_write_b64 v2, a[206:207] offset:19464\n"
"v_mfma_f32_16x16x32_bf16 v[112:115], a[52:55], a[148:151], v[112:115]\n"
"global_load_dwordx4 a[44:47], v0, s[34:35] offset:-1024 sc1\n"
"v_mfma_f32_16x16x32_bf16 v[140:143], a[60:63], a[148:151], v[140:143]\n"
"ds_write_b64 v2, a[208:209] offset:20480\n"
"v_mfma_f32_16x16x32_bf16 v[140:143], a[56:59], a[144:147], v[140:143]\n"
"v_add3_u32 v16, v16, v18, v19\n"
"v_mfma_f32_16x16x32_bf16 a[12:15], a[56:59], a[152:155], a[12:15]\n"
"ds_write_b64 v2, a[210:211] offset:20488\n"
"v_mfma_f32_16x16x32_bf16 a[12:15], a[60:63], a[156:159], a[12:15]\n"
"ds_write_b64 v2, a[212:213] offset:21504\n"
"v_mfma_f32_16x16x32_bf16 a[8:11], a[52:55], a[156:159], a[8:11]\n"
"ds_write_b64 v2, a[214:215] offset:21512\n"
"v_mfma_f32_16x16x32_bf16 a[8:11], a[48:51], a[152:155], a[8:11]\n"
"v_readfirstlane_b32 s69, v16\n"
"v_mfma_f32_16x16x32_bf16 v[144:147], a[64:67], a[96:99], v[144:147]\n"
"ds_write_b64 v2, a[216:217] offset:22528\n"
"v_mfma_f32_16x16x32_bf16 v[144:147], a[68:71], a[100:103], v[144:147]\n"
"global_load_dwordx4 a[48:51], v0, s[34:35] offset:0 sc1\n"
"v_mfma_f32_16x16x32_bf16 v[172:175], a[76:79], a[100:103], v[172:175]\n"
"ds_write_b64 v2, a[218:219] offset:22536\n"
"v_mfma_f32_16x16x32_bf16 v[172:175], a[72:75], a[96:99], v[172:175]\n"
"ds_write_b64 v2, a[220:221] offset:23552\n"
"v_mfma_f32_16x16x32_bf16 v[176:179], a[72:75], a[104:107], v[176:179]\n"
"ds_write_b64 v2, a[222:223] offset:23560\n"
"v_mfma_f32_16x16x32_bf16 v[176:179], a[76:79], a[108:111], v[176:179]\n"
"ds_write_b64 v2, a[224:225] offset:24576\n"
"v_mfma_f32_16x16x32_bf16 v[148:151], a[68:71], a[108:111], v[148:151]\n"
"s_cmp_le_i32 s69, -4\n"
"s_cselect_b32 s66, 2, 1\n"
"v_mfma_f32_16x16x32_bf16 v[148:151], a[64:67], a[104:107], v[148:151]\n"
"ds_write_b64 v2, a[226:227] offset:24584\n"
"v_mfma_f32_16x16x32_bf16 v[152:155], a[64:67], a[112:115], v[152:155]\n"
"s_cmp_ge_i32 s69, 4\n"
"s_cselect_b32 s66, 3, s66\n"
"v_mfma_f32_16x16x32_bf16 v[152:155], a[68:71], a[116:119], v[152:155]\n"
"global_load_dwordx4 a[52:55], v0, s[34:35] offset:1024 sc1\n"
"v_mfma_f32_16x16x32_bf16 v[180:183], a[76:79], a[116:119], v[180:183]\n"
"s_lshl_b32 s73, s66, 13\n"
"s_xor_b32 s60, s79, s66\n"
"v_mfma_f32_16x16x32_bf16 v[180:183], a[72:75], a[112:115], v[180:183]\n"
"ds_write_b64 v2, a[228:229] offset:25600\n"
"v_mfma_f32_16x16x32_bf16 v[184:187], a[72:75], a[120:123], v[184:187]\n"
"s_cmp_eq_u32 s66, 3\n"
"s_cselect_b32 s72, 8192, 0\n"
"v_mfma_f32_16x16x32_bf16 v[184:187], a[76:79], a[124:127], v[184:187]\n"
"ds_write_b64 v2, a[230:231] offset:25608\n"
"v_mfma_f32_16x16x32_bf16 v[156:159], a[68:71], a[124:127], v[156:159]\n"
"s_add_u32 s52, s56, %11\n"
"ds_write_b64 v2, a[232:233] offset:26624\n"
"s_addc_u32 s53, s57, 0\n"
"v_mfma_f32_16x16x32_bf16 v[156:159], a[64:67], a[120:123], v[156:159]\n"
"ds_write_b64 v2, a[234:235] offset:26632\n"
"v_mfma_f32_16x16x32_bf16 v[160:163], a[64:67], a[128:131], v[160:163]\n"
"s_add_u32 s52, s52, 4096\n"
"ds_write_b64 v2, a[236:237] offset:27648\n"
"s_addc_u32 s53, s53, 0\n"
"v_mfma_f32_16x16x32_bf16 v[160:163], a[68:71], a[132:135], v[160:163]\n"
"global_load_dwordx4 a[56:59], v0, s[34:35] offset:2048 sc1\n"
"v_mfma_f32_16x16x32_bf16 v[188:191], a[76:79], a[132:135], v[188:191]\n"
"s_add_u32 s54, s52, s73\n"
"ds_write_b64 v2, a[238:239] offset:27656\n"
"s_addc_u32 s55, s53, 0\n"
"v_mfma_f32_16x16x32_bf16 v[188:191], a[72:75], a[128:131], v[188:191]\n"
"ds_write_b64 v2, a[240:241] offset:28672\n"
"v_mfma_f32_16x16x32_bf16 v[192:195], a[72:75], a[136:139], v[192:195]\n"
"s_add_u32 s52, s52, s72\n"
"ds_write_b64 v2, a[242:243] offset:28680\n"
"s_addc_u32 s53, s53, 0\n"
"v_mfma_f32_16x16x32_bf16 v[192:195], a[76:79], a[140:143], v[192:195]\n"
"ds_write_b64 v2, a[244:245] offset:29696\n"
"v_mfma_f32_16x16x32_bf16 v[164:167], a[68:71], a[140:143], v[164:167]\n"
"s_add_u32 s48, s56, %12\n"
"ds_write_b64 v2, a[246:247] offset:29704\n"
"s_addc_u32 s49, s57, 0\n"
"v_mfma_f32_16x16x32_bf16 v[164:167], a[64:67], a[136:139], v[164:167]\n"
"s_add_u32 s74, s78, 2\n"
"v_mfma_f32_16x16x32_bf16 v[168:171], a[64:67], a[144:147], v[168:171]\n"
"s_mov_b32 s50, 32768\n"
"ds_write_b64 v2, a[248:249] offset:30720\n"
"v_mfma_f32_16x16x32_bf16 v[168:171], a[68:71], a[148:151], v[168:171]\n"
"global_load_dwordx4 a[60:63], v0, s[34:35] offset:3072 sc1\n"
"v_mov_b32 v28, s74\n"
"v_mfma_f32_16x16x32_bf16 v[196:199], a[76:79], a[148:151], v[196:199]\n"
"s_mov_b32 s70, 0\n"
"v_mov_b32 v29, s79\n"
"v_mfma_f32_16x16x32_bf16 v[196:199], a[72:75], a[144:147], v[196:199]\n"
"ds_write_b64 v2, a[250:251] offset:30728\n"
"v_mfma_f32_16x16x32_bf16 a[20:23], a[72:75], a[152:155], a[20:23]\n"
"s_waitcnt vmcnt(8)\n"
"buffer_store_dwordx2 v[28:29], off, s[56:59], %24 sc1\n"
"s_add_u32 s34, s34, 8192\n"
"s_mov_b32 s71, 0\n"
"v_mfma_f32_16x16x32_bf16 a[20:23], a[76:79], a[156:159], a[20:23]\n"
"ds_write_b64 v2, a[252:253] offset:31744\n"
"v_mfma_f32_16x16x32_bf16 a[16:19], a[68:71], a[156:159], a[16:19]\n"
"s_mov_b32 s65, s78\n"
"ds_write_b64 v2, a[254:255] offset:31752\n"
"v_mfma_f32_16x16x32_bf16 a[16:19], a[64:67], a[152:155], a[16:19]\n"
"v_mfma_f32_16x16x32_bf16 v[200:203], a[80:83], a[96:99], v[200:203]\n"
"v_lshrrev_b32 v16, 4, v0\n"
"v_mfma_f32_16x16x32_bf16 v[200:203], a[84:87], a[100:103], v[200:203]\n"
"global_load_dwordx4 a[64:67], v0, s[34:35] offset:-4096 sc1\n"
"v_mfma_f32_16x16x32_bf16 v[228:231], a[92:95], a[100:103], v[228:231]\n"
"ds_read_b128 a[100:103], v2 offset:17408\n"
"v_mfma_f32_16x16x32_bf16 v[228:231], a[88:91], a[96:99], v[228:231]\n"
"ds_read_b128 a[96:99], v2 offset:16384\n"
"v_mfma_f32_16x16x32_bf16 v[232:235], a[88:91], a[104:107], v[232:235]\n"
"v_and_b32 v16, 0x30, v16\n"
"v_mfma_f32_16x16x32_bf16 v[232:235], a[92:95], a[108:111], v[232:235]\n"
"buffer_load_dwordx2 v[18:19], v16, s[44:47], s61 offen offset:128\n"
"v_mfma_f32_16x16x32_bf16 v[204:207], a[84:87], a[108:111], v[204:207]\n"
"ds_read_b128 a[108:111], v2 offset:19456\n"
"v_mfma_f32_16x16x32_bf16 v[204:207], a[80:83], a[104:107], v[204:207]\n"
"ds_read_b128 a[104:107], v2 offset:18432\n"
"v_mfma_f32_16x16x32_bf16 v[208:211], a[80:83], a[112:115], v[208:211]\n"
"v_lshrrev_b32 v20, 4, v0\n"
"v_mfma_f32_16x16x32_bf16 v[208:211], a[84:87], a[116:119], v[208:211]\n"
"global_load_dwordx4 a[68:71], v0, s[34:35] offset:-3072 sc1\n"
"v_mfma_f32_16x16x32_bf16 v[236:239], a[92:95], a[116:119], v[236:239]\n"
"ds_read_b128 a[116:119], v2 offset:21504\n"
"v_mfma_f32_16x16x32_bf16 v[236:239], a[88:91], a[112:115], v[236:239]\n"
"ds_read_b128 a[112:115], v2 offset:20480\n"
"v_mfma_f32_16x16x32_bf16 v[240:243], a[88:91], a[120:123], v[240:243]\n"
"v_and_b32 v20, 0x30, v20\n"
"v_mfma_f32_16x16x32_bf16 v[240:243], a[92:95], a[124:127], v[240:243]\n"
"buffer_load_dwordx2 v[22:23], v20, s[44:47], s61 offen offset:136\n"
"v_mfma_f32_16x16x32_bf16 v[212:215], a[84:87], a[124:127], v[212:215]\n"
"ds_read_b128 a[124:127], v2 offset:23552\n"
"v_mfma_f32_16x16x32_bf16 v[212:215], a[80:83], a[120:123], v[212:215]\n"
"ds_read_b128 a[120:123], v2 offset:22528\n"
"v_mfma_f32_16x16x32_bf16 v[216:219], a[80:83], a[128:131], v[216:219]\n"
"v_lshrrev_b32 v24, 4, v0\n"
"v_mfma_f32_16x16x32_bf16 v[216:219], a[84:87], a[132:135], v[216:219]\n"
"global_load_dwordx4 a[72:75], v0, s[34:35] offset:-2048 sc1\n"
"v_mfma_f32_16x16x32_bf16 v[244:247], a[92:95], a[132:135], v[244:247]\n"
"ds_read_b128 a[132:135], v2 offset:25600\n"
"v_and_b32 v24, 0x30, v24\n"
"v_mfma_f32_16x16x32_bf16 v[244:247], a[88:91], a[128:131], v[244:247]\n"
"ds_read_b128 a[128:131], v2 offset:24576\n"
"v_mfma_f32_16x16x32_bf16 v[248:251], a[88:91], a[136:139], v[248:251]\n"
"buffer_load_dwordx2 v[26:27], v24, s[44:47], s61 offen offset:192\n"
"v_mfma_f32_16x16x32_bf16 v[248:251], a[92:95], a[140:143], v[248:251]\n"
"v_lshrrev_b32 v28, 4, v0\n"
"v_mfma_f32_16x16x32_bf16 v[220:223], a[84:87], a[140:143], v[220:223]\n"
"ds_read_b128 a[140:143], v2 offset:27648\n"
"v_mfma_f32_16x16x32_bf16 v[220:223], a[80:83], a[136:139], v[220:223]\n"
"ds_read_b128 a[136:139], v2 offset:26624\n"
"v_mfma_f32_16x16x32_bf16 v[224:227], a[80:83], a[144:147], v[224:227]\n"
"v_and_b32 v28, 0x30, v28\n"
"v_mfma_f32_16x16x32_bf16 v[224:227], a[84:87], a[148:151], v[224:227]\n"
"global_load_dwordx4 a[76:79], v0, s[34:35] offset:-1024 sc1\n"
"v_mfma_f32_16x16x32_bf16 v[252:255], a[92:95], a[148:151], v[252:255]\n"
"ds_read_b128 a[148:151], v2 offset:29696\n"
"v_mfma_f32_16x16x32_bf16 v[252:255], a[88:91], a[144:147], v[252:255]\n"
"ds_read_b128 a[144:147], v2 offset:28672\n"
"v_mfma_f32_16x16x32_bf16 a[28:31], a[88:91], a[152:155], a[28:31]\n"
"buffer_load_dwordx2 v[30:31], v28, s[44:47], s61 offen offset:200\n"
"v_mfma_f32_16x16x32_bf16 a[28:31], a[92:95], a[156:159], a[28:31]\n"
"v_mfma_f32_16x16x32_bf16 a[24:27], a[84:87], a[156:159], a[24:27]\n"
"ds_read_b128 a[156:159], v2 offset:31744\n"
"v_mfma_f32_16x16x32_bf16 a[24:27], a[80:83], a[152:155], a[24:27]\n"
"s_waitcnt lgkmcnt(15)\n"
"ds_read_b128 a[152:155], v2 offset:30720\n"
"L_P5:\n"
"s_waitcnt vmcnt(8) lgkmcnt(12)\n"
"v_mfma_f32_16x16x32_bf16 v[32:35], a[32:35], a[96:99], v[32:35]\n"
"v_mfma_f32_16x16x32_bf16 v[32:35], a[36:39], a[100:103], v[32:35]\n"
"global_load_dwordx4 a[80:83], v0, s[34:35] offset:0 sc1\n"
"v_mfma_f32_16x16x32_bf16 v[60:63], a[44:47], a[100:103], v[60:63]\n"
"s_barrier\n"
"v_mfma_f32_16x16x32_bf16 v[60:63], a[40:43], a[96:99], v[60:63]\n"
"s_add_u32 s48, s48, s70\n"
"s_addc_u32 s49, s49, s71\n"
"v_mfma_f32_16x16x32_bf16 v[64:67], a[40:43], a[104:107], v[64:67]\n"
"v_mfma_f32_16x16x32_bf16 v[64:67], a[44:47], a[108:111], v[64:67]\n"
"v_mfma_f32_16x16x32_bf16 v[36:39], a[36:39], a[108:111], v[36:39]\n"
"s_add_u32 s52, s52, s70\n"
"s_addc_u32 s53, s53, s71\n"
"v_mfma_f32_16x16x32_bf16 v[36:39], a[32:35], a[104:107], v[36:39]\n"
"s_waitcnt lgkmcnt(8)\n"
"v_mfma_f32_16x16x32_bf16 v[40:43], a[32:35], a[112:115], v[40:43]\n"
"s_add_u32 s54, s54, s70\n"
"s_addc_u32 s55, s55, s71\n"
"v_mfma_f32_16x16x32_bf16 v[40:43], a[36:39], a[116:119], v[40:43]\n"
"global_load_dwordx4 a[84:87], v0, s[34:35] offset:1024 sc1\n"
"v_mfma_f32_16x16x32_bf16 v[68:71], a[44:47], a[116:119], v[68:71]\n"
"v_mfma_f32_16x16x32_bf16 v[68:71], a[40:43], a[112:115], v[68:71]\n"
"v_mfma_f32_16x16x32_bf16 v[72:75], a[40:43], a[120:123], v[72:75]\n"
"v_mfma_f32_16x16x32_bf16 v[72:75], a[44:47], a[124:127], v[72:75]\n"
"s_add_u32 s61, s61, 2048\n"
"v_mfma_f32_16x16x32_bf16 v[44:47], a[36:39], a[124:127], v[44:47]\n"
"v_mfma_f32_16x16x32_bf16 v[44:47], a[32:35], a[120:123], v[44:47]\n"
"s_waitcnt lgkmcnt(4)\n"
"v_mfma_f32_16x16x32_bf16 v[48:51], a[32:35], a[128:131], v[48:51]\n"
"s_waitcnt vmcnt(8)\n"
"v_lshlrev_b32 v16, 16, v18\n"
"v_mfma_f32_16x16x32_bf16 v[48:51], a[36:39], a[132:135], v[48:51]\n"
"global_load_dwordx4 a[88:91], v0, s[34:35] offset:2048 sc1\n"
"v_and_b32 v17, s63, v18\n"
"v_mfma_f32_16x16x32_bf16 v[76:79], a[44:47], a[132:135], v[76:79]\n"
"v_lshlrev_b32 v18, 16, v19\n"
"v_mfma_f32_16x16x32_bf16 v[76:79], a[40:43], a[128:131], v[76:79]\n"
"v_and_b32 v19, s63, v19\n"
"v_mfma_f32_16x16x32_bf16 v[80:83], a[40:43], a[136:139], v[80:83]\n"
"v_add_f32 v144, v144, v16\n"
"v_add_f32 v145, v145, v17\n"
"v_mfma_f32_16x16x32_bf16 v[80:83], a[44:47], a[140:143], v[80:83]\n"
"v_add_f32 v146, v146, v18\n"
"v_mfma_f32_16x16x32_bf16 v[52:55], a[36:39], a[140:143], v[52:55]\n"
"v_add_f32 v147, v147, v19\n"
"v_mfma_f32_16x16x32_bf16 v[52:55], a[32:35], a[136:139], v[52:55]\n"
"s_waitcnt lgkmcnt(0)\n"
"v_add_f32 v148, v148, v16\n"
"v_mfma_f32_16x16x32_bf16 v[56:59], a[32:35], a[144:147], v[56:59]\n"
"v_add_f32 v149, v149, v17\n"
"v_mfma_f32_16x16x32_bf16 v[56:59], a[36:39], a[148:151], v[56:59]\n"
"global_load_dwordx4 a[92:95], v0, s[34:35] offset:3072 sc1\n"
"v_add_f32 v150, v150, v18\n"
"v_mfma_f32_16x16x32_bf16 v[84:87], a[44:47], a[148:151], v[84:87]\n"
"v_add_f32 v151, v151, v19\n"
"v_mfma_f32_16x16x32_bf16 v[84:87], a[40:43], a[144:147], v[84:87]\n"
"v_add_f32 v152, v152, v16\n"
"v_mfma_f32_16x16x32_bf16 a[4:7], a[40:43], a[152:155], a[4:7]\n"
"s_add_u32 s34, s34, 8192\n"
"v_add_f32 v153, v153, v17\n"
"v_mfma_f32_16x16x32_bf16 a[4:7], a[44:47], a[156:159], a[4:7]\n"
"v_add_f32 v154, v154, v18\n"
"v_mfma_f32_16x16x32_bf16 a[0:3], a[36:39], a[156:159], a[0:3]\n"
"v_add_f32 v155, v155, v19\n"
"v_add_f32 v156, v156, v16\n"
"v_mfma_f32_16x16x32_bf16 a[0:3], a[32:35], a[152:155], a[0:3]\n"
"s_waitcnt vmcnt(8)\n"
"v_add_f32 v157, v157, v17\n"
"v_mfma_f32_16x16x32_bf16 v[88:91], a[48:51], a[96:99], v[88:91]\n"
"v_add_f32 v158, v158, v18\n"
"v_mfma_f32_16x16x32_bf16 v[88:91], a[52:55], a[100:103], v[88:91]\n"
"global_load_dwordx4 a[32:35], v0, s[34:35] offset:-4096 sc1\n"
"v_add_f32 v159, v159, v19\n"
"v_mfma_f32_16x16x32_bf16 v[116:119], a[60:63], a[100:103], v[116:119]\n"
"ds_read_b128 a[100:103], v3 offset:17408\n"
"v_add_f32 v160, v160, v16\n"
"v_mfma_f32_16x16x32_bf16 v[116:119], a[56:59], a[96:99], v[116:119]\n"
"ds_read_b128 a[96:99], v3 offset:16384\n"
"v_add_f32 v161, v161, v17\n"
"v_mfma_f32_16x16x32_bf16 v[120:123], a[56:59], a[104:107], v[120:123]\n"
"v_add_f32 v162, v162, v18\n"
"v_mfma_f32_16x16x32_bf16 v[120:123], a[60:63], a[108:111], v[120:123]\n"
"v_add_f32 v163, v163, v19\n"
"v_mfma_f32_16x16x32_bf16 v[92:95], a[52:55], a[108:111], v[92:95]\n"
"ds_read_b128 a[108:111], v3 offset:19456\n"
"v_add_f32 v164, v164, v16\n"
"v_mfma_f32_16x16x32_bf16 v[92:95], a[48:51], a[104:107], v[92:95]\n"
"ds_read_b128 a[104:107], v3 offset:18432\n"
"v_add_f32 v165, v165, v17\n"
"v_mfma_f32_16x16x32_bf16 v[96:99], a[48:51], a[112:115], v[96:99]\n"
"v_add_f32 v166, v166, v18\n"
"v_add_f32 v167, v167, v19\n"
"v_mfma_f32_16x16x32_bf16 v[96:99], a[52:55], a[116:119], v[96:99]\n"
"global_load_dwordx4 a[36:39], v0, s[34:35] offset:-3072 sc1\n"
"v_add_f32 v168, v168, v16\n"
"v_mfma_f32_16x16x32_bf16 v[124:127], a[60:63], a[116:119], v[124:127]\n"
"ds_read_b128 a[116:119], v3 offset:21504\n"
"v_add_f32 v169, v169, v17\n"
"v_mfma_f32_16x16x32_bf16 v[124:127], a[56:59], a[112:115], v[124:127]\n"
"ds_read_b128 a[112:115], v3 offset:20480\n"
"v_add_f32 v170, v170, v18\n"
"v_mfma_f32_16x16x32_bf16 v[128:131], a[56:59], a[120:123], v[128:131]\n"
"v_add_f32 v171, v171, v19\n"
"v_mfma_f32_16x16x32_bf16 v[128:131], a[60:63], a[124:127], v[128:131]\n"
"v_accvgpr_read_b32 v12, a16\n"
"v_mfma_f32_16x16x32_bf16 v[100:103], a[52:55], a[124:127], v[100:103]\n"
"ds_read_b128 a[124:127], v3 offset:23552\n"
"v_accvgpr_read_b32 v13, a17\n"
"v_mfma_f32_16x16x32_bf16 v[100:103], a[48:51], a[120:123], v[100:103]\n"
"ds_read_b128 a[120:123], v3 offset:22528\n"
"v_accvgpr_read_b32 v14, a18\n"
"v_mfma_f32_16x16x32_bf16 v[104:107], a[48:51], a[128:131], v[104:107]\n"
"v_accvgpr_read_b32 v15, a19\n"
"v_add_f32 v12, v12, v16\n"
"v_mfma_f32_16x16x32_bf16 v[104:107], a[52:55], a[132:135], v[104:107]\n"
"global_load_dwordx4 a[40:43], v0, s[34:35] offset:-2048 sc1\n"
"v_add_f32 v13, v13, v17\n"
"v_mfma_f32_16x16x32_bf16 v[132:135], a[60:63], a[132:135], v[132:135]\n"
"ds_read_b128 a[132:135], v3 offset:25600\n"
"v_add_f32 v14, v14, v18\n"
"v_mfma_f32_16x16x32_bf16 v[132:135], a[56:59], a[128:131], v[132:135]\n"
"ds_read_b128 a[128:131], v3 offset:24576\n"
"v_add_f32 v15, v15, v19\n"
"v_mfma_f32_16x16x32_bf16 v[136:139], a[56:59], a[136:139], v[136:139]\n"
"v_accvgpr_write_b32 a16, v12\n"
"v_mfma_f32_16x16x32_bf16 v[136:139], a[60:63], a[140:143], v[136:139]\n"
"v_accvgpr_write_b32 a17, v13\n"
"v_mfma_f32_16x16x32_bf16 v[108:111], a[52:55], a[140:143], v[108:111]\n"
"ds_read_b128 a[140:143], v3 offset:27648\n"
"v_accvgpr_write_b32 a18, v14\n"
"v_mfma_f32_16x16x32_bf16 v[108:111], a[48:51], a[136:139], v[108:111]\n"
"ds_read_b128 a[136:139], v3 offset:26624\n"
"v_accvgpr_write_b32 a19, v15\n"
"v_mfma_f32_16x16x32_bf16 v[112:115], a[48:51], a[144:147], v[112:115]\n"
"v_lshlrev_b32 v20, 16, v22\n"
"v_and_b32 v21, s63, v22\n"
"v_mfma_f32_16x16x32_bf16 v[112:115], a[52:55], a[148:151], v[112:115]\n"
"global_load_dwordx4 a[44:47], v0, s[34:35] offset:-1024 sc1\n"
"v_lshlrev_b32 v22, 16, v23\n"
"v_mfma_f32_16x16x32_bf16 v[140:143], a[60:63], a[148:151], v[140:143]\n"
"ds_read_b128 a[148:151], v3 offset:29696\n"
"v_and_b32 v23, s63, v23\n"
"v_mfma_f32_16x16x32_bf16 v[140:143], a[56:59], a[144:147], v[140:143]\n"
"ds_read_b128 a[144:147], v3 offset:28672\n"
"v_add_f32 v172, v172, v20\n"
"v_mfma_f32_16x16x32_bf16 a[12:15], a[56:59], a[152:155], a[12:15]\n"
"v_add_f32 v173, v173, v21\n"
"v_mfma_f32_16x16x32_bf16 a[12:15], a[60:63], a[156:159], a[12:15]\n"
"v_add_f32 v174, v174, v22\n"
"v_mfma_f32_16x16x32_bf16 a[8:11], a[52:55], a[156:159], a[8:11]\n"
"ds_read_b128 a[156:159], v3 offset:31744\n"
"v_add_f32 v175, v175, v23\n"
"v_mfma_f32_16x16x32_bf16 a[8:11], a[48:51], a[152:155], a[8:11]\n"
"s_waitcnt vmcnt(8) lgkmcnt(11)\n"
"ds_read_b128 a[152:155], v3 offset:30720\n"
"v_add_f32 v176, v176, v20\n"
"v_mfma_f32_16x16x32_bf16 v[32:35], a[64:67], a[96:99], v[32:35]\n"
"v_add_f32 v177, v177, v21\n"
"v_mfma_f32_16x16x32_bf16 v[32:35], a[68:71], a[100:103], v[32:35]\n"
"global_load_dwordx4 a[48:51], v0, s[34:35] offset:0 sc1\n"
"v_add_f32 v178, v178, v22\n"
"v_mfma_f32_16x16x32_bf16 v[60:63], a[76:79], a[100:103], v[60:63]\n"
"v_add_f32 v179, v179, v23\n"
"v_mfma_f32_16x16x32_bf16 v[60:63], a[72:75], a[96:99], v[60:63]\n"
"v_add_f32 v180, v180, v20\n"
"v_add_f32 v181, v181, v21\n"
"v_mfma_f32_16x16x32_bf16 v[64:67], a[72:75], a[104:107], v[64:67]\n"
"v_add_f32 v182, v182, v22\n"
"v_mfma_f32_16x16x32_bf16 v[64:67], a[76:79], a[108:111], v[64:67]\n"
"v_add_f32 v183, v183, v23\n"
"v_mfma_f32_16x16x32_bf16 v[36:39], a[68:71], a[108:111], v[36:39]\n"
"v_add_f32 v184, v184, v20\n"
"v_mfma_f32_16x16x32_bf16 v[36:39], a[64:67], a[104:107], v[36:39]\n"
"s_waitcnt lgkmcnt(8)\n"
"v_add_f32 v185, v185, v21\n"
"v_mfma_f32_16x16x32_bf16 v[40:43], a[64:67], a[112:115], v[40:43]\n"
"v_add_f32 v186, v186, v22\n"
"v_mfma_f32_16x16x32_bf16 v[40:43], a[68:71], a[116:119], v[40:43]\n"
"global_load_dwordx4 a[52:55], v0, s[34:35] offset:1024 sc1\n"
"v_add_f32 v187, v187, v23\n"
"v_mfma_f32_16x16x32_bf16 v[68:71], a[76:79], a[116:119], v[68:71]\n"
"v_add_f32 v188, v188, v20\n"
"v_mfma_f32_16x16x32_bf16 v[68:71], a[72:75], a[112:115], v[68:71]\n"
"v_add_f32 v189, v189, v21\n"
"v_mfma_f32_16x16x32_bf16 v[72:75], a[72:75], a[120:123], v[72:75]\n"
"v_add_f32 v190, v190, v22\n"
"v_add_f32 v191, v191, v23\n"
"v_mfma_f32_16x16x32_bf16 v[72:75], a[76:79], a[124:127], v[72:75]\n"
"v_add_f32 v192, v192, v20\n"
"v_mfma_f32_16x16x32_bf16 v[44:47], a[68:71], a[124:127], v[44:47]\n"
"v_add_f32 v193, v193, v21\n"
"v_mfma_f32_16x16x32_bf16 v[44:47], a[64:67], a[120:123], v[44:47]\n"
"s_waitcnt lgkmcnt(4)\n"
"v_add_f32 v194, v194, v22\n"
"v_mfma_f32_16x16x32_bf16 v[48:51], a[64:67], a[128:131], v[48:51]\n"
"v_add_f32 v195, v195, v23\n"
"v_mfma_f32_16x16x32_bf16 v[48:51], a[68:71], a[132:135], v[48:51]\n"
"global_load_dwordx4 a[56:59], v0, s[34:35] offset:2048 sc1\n"
"v_add_f32 v196, v196, v20\n"
"v_mfma_f32_16x16x32_bf16 v[76:79], a[76:79], a[132:135], v[76:79]\n"
"v_add_f32 v197, v197, v21\n"
"v_mfma_f32_16x16x32_bf16 v[76:79], a[72:75], a[128:131], v[76:79]\n"
"v_add_f32 v198, v198, v22\n"
"v_mfma_f32_16x16x32_bf16 v[80:83], a[72:75], a[136:139], v[80:83]\n"
"v_add_f32 v199, v199, v23\n"
"v_mfma_f32_16x16x32_bf16 v[80:83], a[76:79], a[140:143], v[80:83]\n"
"v_accvgpr_read_b32 v12, a20\n"
"v_mfma_f32_16x16x32_bf16 v[52:55], a[68:71], a[140:143], v[52:55]\n"
"v_accvgpr_read_b32 v13, a21\n"
"v_accvgpr_read_b32 v14, a22\n"
"v_mfma_f32_16x16x32_bf16 v[52:55], a[64:67], a[136:139], v[52:55]\n"
"s_waitcnt lgkmcnt(0)\n"
"v_accvgpr_read_b32 v15, a23\n"
"v_mfma_f32_16x16x32_bf16 v[56:59], a[64:67], a[144:147], v[56:59]\n"
"v_add_f32 v12, v12, v20\n"
"v_mfma_f32_16x16x32_bf16 v[56:59], a[68:71], a[148:151], v[56:59]\n"
"global_load_dwordx4 a[60:63], v0, s[34:35] offset:3072 sc1\n"
"v_add_f32 v13, v13, v21\n"
"v_mfma_f32_16x16x32_bf16 v[84:87], a[76:79], a[148:151], v[84:87]\n"
"v_add_f32 v14, v14, v22\n"
"v_mfma_f32_16x16x32_bf16 v[84:87], a[72:75], a[144:147], v[84:87]\n"
"v_add_f32 v15, v15, v23\n"
"v_mfma_f32_16x16x32_bf16 a[4:7], a[72:75], a[152:155], a[4:7]\n"
"s_add_u32 s34, s34, 8192\n"
"v_accvgpr_write_b32 a20, v12\n"
"v_mfma_f32_16x16x32_bf16 a[4:7], a[76:79], a[156:159], a[4:7]\n"
"v_accvgpr_write_b32 a21, v13\n"
"v_mfma_f32_16x16x32_bf16 a[0:3], a[68:71], a[156:159], a[0:3]\n"
"v_accvgpr_write_b32 a22, v14\n"
"v_mfma_f32_16x16x32_bf16 a[0:3], a[64:67], a[152:155], a[0:3]\n"
"s_waitcnt vmcnt(8)\n"
"v_accvgpr_write_b32 a23, v15\n"
"v_lshlrev_b32 v24, 16, v26\n"
"v_mfma_f32_16x16x32_bf16 v[88:91], a[80:83], a[96:99], v[88:91]\n"
"v_and_b32 v25, s63, v26\n"
"v_mfma_f32_16x16x32_bf16 v[88:91], a[84:87], a[100:103], v[88:91]\n"
"global_load_dwordx4 a[64:67], v0, s[34:35] offset:-4096 sc1\n"
"v_lshlrev_b32 v26, 16, v27\n"
"v_mfma_f32_16x16x32_bf16 v[116:119], a[92:95], a[100:103], v[116:119]\n"
"ds_read_b128 a[100:103], v4 offset:17408\n"
"v_and_b32 v27, s63, v27\n"
"v_mfma_f32_16x16x32_bf16 v[116:119], a[88:91], a[96:99], v[116:119]\n"
"ds_read_b128 a[96:99], v4 offset:16384\n"
"v_add_f32 v200, v200, v24\n"
"v_mfma_f32_16x16x32_bf16 v[120:123], a[88:91], a[104:107], v[120:123]\n"
"v_add_f32 v201, v201, v25\n"
"v_mfma_f32_16x16x32_bf16 v[120:123], a[92:95], a[108:111], v[120:123]\n"
"v_add_f32 v202, v202, v26\n"
"v_mfma_f32_16x16x32_bf16 v[92:95], a[84:87], a[108:111], v[92:95]\n"
"ds_read_b128 a[108:111], v4 offset:19456\n"
"v_add_f32 v203, v203, v27\n"
"v_mfma_f32_16x16x32_bf16 v[92:95], a[80:83], a[104:107], v[92:95]\n"
"ds_read_b128 a[104:107], v4 offset:18432\n"
"v_add_f32 v204, v204, v24\n"
"v_mfma_f32_16x16x32_bf16 v[96:99], a[80:83], a[112:115], v[96:99]\n"
"v_add_f32 v205, v205, v25\n"
"v_add_f32 v206, v206, v26\n"
"v_mfma_f32_16x16x32_bf16 v[96:99], a[84:87], a[116:119], v[96:99]\n"
"global_load_dwordx4 a[68:71], v0, s[34:35] offset:-3072 sc1\n"
"v_add_f32 v207, v207, v27\n"
"v_mfma_f32_16x16x32_bf16 v[124:127], a[92:95], a[116:119], v[124:127]\n"
"ds_read_b128 a[116:119], v4 offset:21504\n"
"v_add_f32 v208, v208, v24\n"
"v_mfma_f32_16x16x32_bf16 v[124:127], a[88:91], a[112:115], v[124:127]\n"
"ds_read_b128 a[112:115], v4 offset:20480\n"
"v_add_f32 v209, v209, v25\n"
"v_mfma_f32_16x16x32_bf16 v[128:131], a[88:91], a[120:123], v[128:131]\n"
"v_add_f32 v210, v210, v26\n"
"v_mfma_f32_16x16x32_bf16 v[128:131], a[92:95], a[124:127], v[128:131]\n"
"v_add_f32 v211, v211, v27\n"
"v_mfma_f32_16x16x32_bf16 v[100:103], a[84:87], a[124:127], v[100:103]\n"
"ds_read_b128 a[124:127], v4 offset:23552\n"
"v_add_f32 v212, v212, v24\n"
"v_mfma_f32_16x16x32_bf16 v[100:103], a[80:83], a[120:123], v[100:103]\n"
"ds_read_b128 a[120:123], v4 offset:22528\n"
"v_add_f32 v213, v213, v25\n"
"v_mfma_f32_16x16x32_bf16 v[104:107], a[80:83], a[128:131], v[104:107]\n"
"v_add_f32 v214, v214, v26\n"
"v_add_f32 v215, v215, v27\n"
"v_mfma_f32_16x16x32_bf16 v[104:107], a[84:87], a[132:135], v[104:107]\n"
"global_load_dwordx4 a[72:75], v0, s[34:35] offset:-2048 sc1\n"
"v_add_f32 v216, v216, v24\n"
"v_mfma_f32_16x16x32_bf16 v[132:135], a[92:95], a[132:135], v[132:135]\n"
"ds_read_b128 a[132:135], v4 offset:25600\n"
"v_add_f32 v217, v217, v25\n"
"v_mfma_f32_16x16x32_bf16 v[132:135], a[88:91], a[128:131], v[132:135]\n"
"ds_read_b128 a[128:131], v4 offset:24576\n"
"v_add_f32 v218, v218, v26\n"
"v_mfma_f32_16x16x32_bf16 v[136:139], a[88:91], a[136:139], v[136:139]\n"
"v_add_f32 v219, v219, v27\n"
"v_mfma_f32_16x16x32_bf16 v[136:139], a[92:95], a[140:143], v[136:139]\n"
"v_add_f32 v220, v220, v24\n"
"v_mfma_f32_16x16x32_bf16 v[108:111], a[84:87], a[140:143], v[108:111]\n"
"ds_read_b128 a[140:143], v4 offset:27648\n"
"v_add_f32 v221, v221, v25\n"
"v_mfma_f32_16x16x32_bf16 v[108:111], a[80:83], a[136:139], v[108:111]\n"
"ds_read_b128 a[136:139], v4 offset:26624\n"
"v_add_f32 v222, v222, v26\n"
"v_mfma_f32_16x16x32_bf16 v[112:115], a[80:83], a[144:147], v[112:115]\n"
"v_add_f32 v223, v223, v27\n"
"v_mfma_f32_16x16x32_bf16 v[112:115], a[84:87], a[148:151], v[112:115]\n"
"global_load_dwordx4 a[76:79], v0, s[34:35] offset:-1024 sc1\n"
"v_add_f32 v224, v224, v24\n"
"v_mfma_f32_16x16x32_bf16 v[140:143], a[92:95], a[148:151], v[140:143]\n"
"ds_read_b128 a[148:151], v4 offset:29696\n"
"v_add_f32 v225, v225, v25\n"
"v_mfma_f32_16x16x32_bf16 v[140:143], a[88:91], a[144:147], v[140:143]\n"
"ds_read_b128 a[144:147], v4 offset:28672\n"
"v_add_f32 v226, v226, v26\n"
"v_mfma_f32_16x16x32_bf16 a[12:15], a[88:91], a[152:155], a[12:15]\n"
"v_add_f32 v227, v227, v27\n"
"v_accvgpr_read_b32 v12, a24\n"
"v_mfma_f32_16x16x32_bf16 a[12:15], a[92:95], a[156:159], a[12:15]\n"
"v_accvgpr_read_b32 v13, a25\n"
"v_mfma_f32_16x16x32_bf16 a[8:11], a[84:87], a[156:159], a[8:11]\n"
"ds_read_b128 a[156:159], v4 offset:31744\n"
"v_accvgpr_read_b32 v14, a26\n"
"v_mfma_f32_16x16x32_bf16 a[8:11], a[80:83], a[152:155], a[8:11]\n"
"s_waitcnt vmcnt(8) lgkmcnt(11)\n"
"ds_read_b128 a[152:155], v4 offset:30720\n"
"v_accvgpr_read_b32 v15, a27\n"
"v_mfma_f32_16x16x32_bf16 v[32:35], a[32:35], a[96:99], v[32:35]\n"
"v_add_f32 v12, v12, v24\n"
"v_mfma_f32_16x16x32_bf16 v[32:35], a[36:39], a[100:103], v[32:35]\n"
"global_load_dwordx4 a[80:83], v0, s[34:35] offset:0 sc1\n"
"v_add_f32 v13, v13, v25\n"
"v_mfma_f32_16x16x32_bf16 v[60:63], a[44:47], a[100:103], v[60:63]\n"
"v_add_f32 v14, v14, v26\n"
"v_mfma_f32_16x16x32_bf16 v[60:63], a[40:43], a[96:99], v[60:63]\n"
"v_add_f32 v15, v15, v27\n"
"v_mfma_f32_16x16x32_bf16 v[64:67], a[40:43], a[104:107], v[64:67]\n"
"v_accvgpr_write_b32 a24, v12\n"
"v_accvgpr_write_b32 a25, v13\n"
"v_mfma_f32_16x16x32_bf16 v[64:67], a[44:47], a[108:111], v[64:67]\n"
"v_accvgpr_write_b32 a26, v14\n"
"v_mfma_f32_16x16x32_bf16 v[36:39], a[36:39], a[108:111], v[36:39]\n"
"v_accvgpr_write_b32 a27, v15\n"
"v_mfma_f32_16x16x32_bf16 v[36:39], a[32:35], a[104:107], v[36:39]\n"
"s_waitcnt lgkmcnt(8)\n"
"v_lshlrev_b32 v28, 16, v30\n"
"v_mfma_f32_16x16x32_bf16 v[40:43], a[32:35], a[112:115], v[40:43]\n"
"v_and_b32 v29, s63, v30\n"
"v_mfma_f32_16x16x32_bf16 v[40:43], a[36:39], a[116:119], v[40:43]\n"
"global_load_dwordx4 a[84:87], v0, s[34:35] offset:1024 sc1\n"
"v_lshlrev_b32 v30, 16, v31\n"
"v_mfma_f32_16x16x32_bf16 v[68:71], a[44:47], a[116:119], v[68:71]\n"
"v_and_b32 v31, s63, v31\n"
"v_mfma_f32_16x16x32_bf16 v[68:71], a[40:43], a[112:115], v[68:71]\n"
"v_add_f32 v228, v228, v28\n"
"v_mfma_f32_16x16x32_bf16 v[72:75], a[40:43], a[120:123], v[72:75]\n"
"v_add_f32 v229, v229, v29\n"
"v_mfma_f32_16x16x32_bf16 v[72:75], a[44:47], a[124:127], v[72:75]\n"
"v_add_f32 v230, v230, v30\n"
"v_mfma_f32_16x16x32_bf16 v[44:47], a[36:39], a[124:127], v[44:47]\n"
"v_add_f32 v231, v231, v31\n"
"v_add_f32 v232, v232, v28\n"
"v_mfma_f32_16x16x32_bf16 v[44:47], a[32:35], a[120:123], v[44:47]\n"
"s_waitcnt lgkmcnt(4)\n"
"v_add_f32 v233, v233, v29\n"
"v_mfma_f32_16x16x32_bf16 v[48:51], a[32:35], a[128:131], v[48:51]\n"
"v_add_f32 v234, v234, v30\n"
"v_mfma_f32_16x16x32_bf16 v[48:51], a[36:39], a[132:135], v[48:51]\n"
"global_load_dwordx4 a[88:91], v0, s[34:35] offset:2048 sc1\n"
"v_add_f32 v235, v235, v31\n"
"v_mfma_f32_16x16x32_bf16 v[76:79], a[44:47], a[132:135], v[76:79]\n"
"v_add_f32 v236, v236, v28\n"
"v_mfma_f32_16x16x32_bf16 v[76:79], a[40:43], a[128:131], v[76:79]\n"
"v_add_f32 v237, v237, v29\n"
"v_mfma_f32_16x16x32_bf16 v[80:83], a[40:43], a[136:139], v[80:83]\n"
"v_add_f32 v238, v238, v30\n"
"v_mfma_f32_16x16x32_bf16 v[80:83], a[44:47], a[140:143], v[80:83]\n"
"v_add_f32 v239, v239, v31\n"
"v_mfma_f32_16x16x32_bf16 v[52:55], a[36:39], a[140:143], v[52:55]\n"
"v_add_f32 v240, v240, v28\n"
"v_mfma_f32_16x16x32_bf16 v[52:55], a[32:35], a[136:139], v[52:55]\n"
"s_waitcnt lgkmcnt(0)\n"
"v_add_f32 v241, v241, v29\n"
"v_add_f32 v242, v242, v30\n"
"v_mfma_f32_16x16x32_bf16 v[56:59], a[32:35], a[144:147], v[56:59]\n"
"v_add_f32 v243, v243, v31\n"
"v_mfma_f32_16x16x32_bf16 v[56:59], a[36:39], a[148:151], v[56:59]\n"
"global_load_dwordx4 a[92:95], v0, s[34:35] offset:3072 sc1\n"
"v_add_f32 v244, v244, v28\n"
"v_mfma_f32_16x16x32_bf16 v[84:87], a[44:47], a[148:151], v[84:87]\n"
"v_add_f32 v245, v245, v29\n"
"v_mfma_f32_16x16x32_bf16 v[84:87], a[40:43], a[144:147], v[84:87]\n"
"v_add_f32 v246, v246, v30\n"
"v_mfma_f32_16x16x32_bf16 a[4:7], a[40:43], a[152:155], a[4:7]\n"
"s_add_u32 s34, s34, 8192\n"
"v_add_f32 v247, v247, v31\n"
"v_mfma_f32_16x16x32_bf16 a[4:7], a[44:47], a[156:159], a[4:7]\n"
"v_add_f32 v248, v248, v28\n"
"v_mfma_f32_16x16x32_bf16 a[0:3], a[36:39], a[156:159], a[0:3]\n"
"v_add_f32 v249, v249, v29\n"
"v_mfma_f32_16x16x32_bf16 a[0:3], a[32:35], a[152:155], a[0:3]\n"
"s_waitcnt vmcnt(8)\n"
"v_add_f32 v250, v250, v30\n"
"v_mfma_f32_16x16x32_bf16 v[88:91], a[48:51], a[96:99], v[88:91]\n"
"v_add_f32 v251, v251, v31\n"
"v_add_f32 v252, v252, v28\n"
"v_mfma_f32_16x16x32_bf16 v[88:91], a[52:55], a[100:103], v[88:91]\n"
"global_load_dwordx4 a[32:35], v0, s[34:35] offset:-4096 sc1\n"
"v_add_f32 v253, v253, v29\n"
"v_mfma_f32_16x16x32_bf16 v[116:119], a[60:63], a[100:103], v[116:119]\n"
"ds_read_b128 a[100:103], v5 offset:17408\n"
"v_add_f32 v254, v254, v30\n"
"v_mfma_f32_16x16x32_bf16 v[116:119], a[56:59], a[96:99], v[116:119]\n"
"ds_read_b128 a[96:99], v5 offset:16384\n"
"v_add_f32 v255, v255, v31\n"
"v_mfma_f32_16x16x32_bf16 v[120:123], a[56:59], a[104:107], v[120:123]\n"
"v_accvgpr_read_b32 v12, a28\n"
"v_mfma_f32_16x16x32_bf16 v[120:123], a[60:63], a[108:111], v[120:123]\n"
"v_accvgpr_read_b32 v13, a29\n"
"v_mfma_f32_16x16x32_bf16 v[92:95], a[52:55], a[108:111], v[92:95]\n"
"ds_read_b128 a[108:111], v5 offset:19456\n"
"v_accvgpr_read_b32 v14, a30\n"
"v_mfma_f32_16x16x32_bf16 v[92:95], a[48:51], a[104:107], v[92:95]\n"
"ds_read_b128 a[104:107], v5 offset:18432\n"
"v_accvgpr_read_b32 v15, a31\n"
"v_mfma_f32_16x16x32_bf16 v[96:99], a[48:51], a[112:115], v[96:99]\n"
"v_add_f32 v12, v12, v28\n"
"v_add_f32 v13, v13, v29\n"
"v_mfma_f32_16x16x32_bf16 v[96:99], a[52:55], a[116:119], v[96:99]\n"
"global_load_dwordx4 a[36:39], v0, s[34:35] offset:-3072 sc1\n"
"v_add_f32 v14, v14, v30\n"
"v_mfma_f32_16x16x32_bf16 v[124:127], a[60:63], a[116:119], v[124:127]\n"
"ds_read_b128 a[116:119], v5 offset:21504\n"
"v_add_f32 v15, v15, v31\n"
"v_mfma_f32_16x16x32_bf16 v[124:127], a[56:59], a[112:115], v[124:127]\n"
"ds_read_b128 a[112:115], v5 offset:20480\n"
"v_accvgpr_write_b32 a28, v12\n"
"v_mfma_f32_16x16x32_bf16 v[128:131], a[56:59], a[120:123], v[128:131]\n"
"v_accvgpr_write_b32 a29, v13\n"
"v_mfma_f32_16x16x32_bf16 v[128:131], a[60:63], a[124:127], v[128:131]\n"
"v_accvgpr_write_b32 a30, v14\n"
"v_mfma_f32_16x16x32_bf16 v[100:103], a[52:55], a[124:127], v[100:103]\n"
"ds_read_b128 a[124:127], v5 offset:23552\n"
"v_accvgpr_write_b32 a31, v15\n"
"v_mfma_f32_16x16x32_bf16 v[100:103], a[48:51], a[120:123], v[100:103]\n"
"ds_read_b128 a[120:123], v5 offset:22528\n"
"v_mfma_f32_16x16x32_bf16 v[104:107], a[48:51], a[128:131], v[104:107]\n"
"v_mfma_f32_16x16x32_bf16 v[104:107], a[52:55], a[132:135], v[104:107]\n"
"global_load_dwordx4 a[40:43], v0, s[34:35] offset:-2048 sc1\n"
"v_mfma_f32_16x16x32_bf16 v[132:135], a[60:63], a[132:135], v[132:135]\n"
"ds_read_b128 a[132:135], v5 offset:25600\n"
"v_mfma_f32_16x16x32_bf16 v[132:135], a[56:59], a[128:131], v[132:135]\n"
"ds_read_b128 a[128:131], v5 offset:24576\n"
"v_mfma_f32_16x16x32_bf16 v[136:139], a[56:59], a[136:139], v[136:139]\n"
"ds_read_b128 v[16:19], v7 offset:32768\n"
"v_mfma_f32_16x16x32_bf16 v[136:139], a[60:63], a[140:143], v[136:139]\n"
"v_mfma_f32_16x16x32_bf16 v[108:111], a[52:55], a[140:143], v[108:111]\n"
"ds_read_b128 a[140:143], v5 offset:27648\n"
"v_mfma_f32_16x16x32_bf16 v[108:111], a[48:51], a[136:139], v[108:111]\n"
"ds_read_b128 a[136:139], v5 offset:26624\n"
"v_mfma_f32_16x16x32_bf16 v[112:115], a[48:51], a[144:147], v[112:115]\n"
"v_mfma_f32_16x16x32_bf16 v[112:115], a[52:55], a[148:151], v[112:115]\n"
"global_load_dwordx4 a[44:47], v0, s[34:35] offset:-1024 sc1\n"
"v_mfma_f32_16x16x32_bf16 v[140:143], a[60:63], a[148:151], v[140:143]\n"
"ds_read_b128 a[148:151], v5 offset:29696\n"
"v_mfma_f32_16x16x32_bf16 v[140:143], a[56:59], a[144:147], v[140:143]\n"
"ds_read_b128 a[144:147], v5 offset:28672\n"
"v_mfma_f32_16x16x32_bf16 a[12:15], a[56:59], a[152:155], a[12:15]\n"
"v_mfma_f32_16x16x32_bf16 a[12:15], a[60:63], a[156:159], a[12:15]\n"
"v_mfma_f32_16x16x32_bf16 a[8:11], a[52:55], a[156:159], a[8:11]\n"
"ds_read_b128 a[156:159], v5 offset:31744\n"
"v_mfma_f32_16x16x32_bf16 a[8:11], a[48:51], a[152:155], a[8:11]\n"
"ds_read_b128 a[152:155], v5 offset:30720\n"
"s_waitcnt vmcnt(8) lgkmcnt(13)\n"
"v_mfma_f32_16x16x32_bf16 v[32:35], a[64:67], a[96:99], v[32:35]\n"
"v_mfma_f32_16x16x32_bf16 v[32:35], a[68:71], a[100:103], v[32:35]\n"
"global_load_dwordx4 a[48:51], v0, s[34:35] offset:0 sc1\n"
"v_mfma_f32_16x16x32_bf16 v[60:63], a[76:79], a[100:103], v[60:63]\n"
"v_mfma_f32_16x16x32_bf16 v[60:63], a[72:75], a[96:99], v[60:63]\n"
"v_mfma_f32_16x16x32_bf16 v[64:67], a[72:75], a[104:107], v[64:67]\n"
"v_mfma_f32_16x16x32_bf16 v[64:67], a[76:79], a[108:111], v[64:67]\n"
"v_mfma_f32_16x16x32_bf16 v[36:39], a[68:71], a[108:111], v[36:39]\n"
"s_waitcnt lgkmcnt(6)\n"
"v_lshlrev_b32 v12, 16, v16\n"
"v_mfma_f32_16x16x32_bf16 v[36:39], a[64:67], a[104:107], v[36:39]\n"
"v_and_b32 v13, s63, v16\n"
"v_lshlrev_b32 v14, 16, v17\n"
"v_mfma_f32_16x16x32_bf16 v[40:43], a[64:67], a[112:115], v[40:43]\n"
"v_and_b32 v15, s63, v17\n"
"v_mfma_f32_16x16x32_bf16 v[40:43], a[68:71], a[116:119], v[40:43]\n"
"global_load_dwordx4 a[52:55], v0, s[34:35] offset:1024 sc1\n"
"ds_read_b128 v[20:23], v7 offset:34816\n"
"v_mfma_f32_16x16x32_bf16 v[68:71], a[76:79], a[116:119], v[68:71]\n"
"v_mul_f32 v12, v12, v32\n"
"v_mul_f32 v13, v13, v33\n"
"v_mfma_f32_16x16x32_bf16 v[68:71], a[72:75], a[112:115], v[68:71]\n"
"v_mul_f32 v14, v14, v34\n"
"v_mfma_f32_16x16x32_bf16 v[72:75], a[72:75], a[120:123], v[72:75]\n"
"v_mul_f32 v15, v15, v35\n"
"v_cvt_pk_bf16_f32 v24, v12, v13\n"
"v_mfma_f32_16x16x32_bf16 v[72:75], a[76:79], a[124:127], v[72:75]\n"
"v_cvt_pk_bf16_f32 v25, v14, v15\n"
"v_mfma_f32_16x16x32_bf16 v[44:47], a[68:71], a[124:127], v[44:47]\n"
"ds_write_b64 v2, v[24:25] offset:0\n"
"v_mfma_f32_16x16x32_bf16 v[44:47], a[64:67], a[120:123], v[44:47]\n"
"s_waitcnt lgkmcnt(6)\n"
"v_lshlrev_b32 v12, 16, v18\n"
"v_and_b32 v13, s63, v18\n"
"v_mfma_f32_16x16x32_bf16 v[48:51], a[64:67], a[128:131], v[48:51]\n"
"v_lshlrev_b32 v14, 16, v19\n"
"v_mfma_f32_16x16x32_bf16 v[48:51], a[68:71], a[132:135], v[48:51]\n"
"global_load_dwordx4 a[56:59], v0, s[34:35] offset:2048 sc1\n"
"v_and_b32 v15, s63, v19\n"
"v_mfma_f32_16x16x32_bf16 v[76:79], a[76:79], a[132:135], v[76:79]\n"
"v_mul_f32 v12, v12, v60\n"
"v_mul_f32 v13, v13, v61\n"
"v_mfma_f32_16x16x32_bf16 v[76:79], a[72:75], a[128:131], v[76:79]\n"
"v_mul_f32 v14, v14, v62\n"
"v_mfma_f32_16x16x32_bf16 v[80:83], a[72:75], a[136:139], v[80:83]\n"
"v_mul_f32 v15, v15, v63\n"
"v_mfma_f32_16x16x32_bf16 v[80:83], a[76:79], a[140:143], v[80:83]\n"
"v_cvt_pk_bf16_f32 v26, v12, v13\n"
"v_cvt_pk_bf16_f32 v27, v14, v15\n"
"v_mfma_f32_16x16x32_bf16 v[52:55], a[68:71], a[140:143], v[52:55]\n"
"ds_write_b64 v2, v[26:27] offset:8\n"
"v_mfma_f32_16x16x32_bf16 v[52:55], a[64:67], a[136:139], v[52:55]\n"
"s_waitcnt lgkmcnt(2)\n"
"v_lshlrev_b32 v12, 16, v20\n"
"v_and_b32 v13, s63, v20\n"
"v_mfma_f32_16x16x32_bf16 v[56:59], a[64:67], a[144:147], v[56:59]\n"
"v_lshlrev_b32 v14, 16, v21\n"
"v_mfma_f32_16x16x32_bf16 v[56:59], a[68:71], a[148:151], v[56:59]\n"
"global_load_dwordx4 a[60:63], v0, s[34:35] offset:3072 sc1\n"
"v_and_b32 v15, s63, v21\n"
"v_mfma_f32_16x16x32_bf16 v[84:87], a[76:79], a[148:151], v[84:87]\n"
"v_xor_b32 v16, 64, v7\n"
"v_mfma_f32_16x16x32_bf16 v[84:87], a[72:75], a[144:147], v[84:87]\n"
"ds_read_b128 v[16:19], v16 offset:32768\n"
"v_mul_f32 v12, v12, v36\n"
"v_mfma_f32_16x16x32_bf16 a[4:7], a[72:75], a[152:155], a[4:7]\n"
"s_add_u32 s34, s34, 8192\n"
"v_mul_f32 v13, v13, v37\n"
"v_mfma_f32_16x16x32_bf16 a[4:7], a[76:79], a[156:159], a[4:7]\n"
"v_mul_f32 v14, v14, v38\n"
"v_mul_f32 v15, v15, v39\n"
"v_mfma_f32_16x16x32_bf16 a[0:3], a[68:71], a[156:159], a[0:3]\n"
"v_cvt_pk_bf16_f32 v28, v12, v13\n"
"v_cvt_pk_bf16_f32 v29, v14, v15\n"
"v_mfma_f32_16x16x32_bf16 a[0:3], a[64:67], a[152:155], a[0:3]\n"
"s_waitcnt vmcnt(8)\n"
"ds_write_b64 v2, v[28:29] offset:2048\n"
"v_mfma_f32_16x16x32_bf16 v[88:91], a[80:83], a[96:99], v[88:91]\n"
"v_lshlrev_b32 v12, 16, v22\n"
"v_mfma_f32_16x16x32_bf16 v[88:91], a[84:87], a[100:103], v[88:91]\n"
"global_load_dwordx4 a[64:67], v0, s[34:35] offset:-4096 sc1\n"
"v_and_b32 v13, s63, v22\n"
"v_mfma_f32_16x16x32_bf16 v[116:119], a[92:95], a[100:103], v[116:119]\n"
"ds_read_b128 a[100:103], v2 offset:17408\n"
"v_lshlrev_b32 v14, 16, v23\n"
"v_mfma_f32_16x16x32_bf16 v[116:119], a[88:91], a[96:99], v[116:119]\n"
"ds_read_b128 a[96:99], v2 offset:16384\n"
"v_and_b32 v15, s63, v23\n"
"v_mfma_f32_16x16x32_bf16 v[120:123], a[88:91], a[104:107], v[120:123]\n"
"v_mul_f32 v12, v12, v64\n"
"v_mul_f32 v13, v13, v65\n"
"v_mfma_f32_16x16x32_bf16 v[120:123], a[92:95], a[108:111], v[120:123]\n"
"v_mul_f32 v14, v14, v66\n"
"v_mul_f32 v15, v15, v67\n"
"v_mfma_f32_16x16x32_bf16 v[92:95], a[84:87], a[108:111], v[92:95]\n"
"ds_read_b128 a[108:111], v2 offset:19456\n"
"v_cvt_pk_bf16_f32 v30, v12, v13\n"
"v_mfma_f32_16x16x32_bf16 v[92:95], a[80:83], a[104:107], v[92:95]\n"
"ds_read_b128 a[104:107], v2 offset:18432\n"
"v_cvt_pk_bf16_f32 v31, v14, v15\n"
"v_mfma_f32_16x16x32_bf16 v[96:99], a[80:83], a[112:115], v[96:99]\n"
"global_store_dwordx4 v0, v[28:31], s[52:53] offset:-2048 sc1\n"
"ds_write_b64 v2, v[30:31] offset:2056\n"
"v_mfma_f32_16x16x32_bf16 v[96:99], a[84:87], a[116:119], v[96:99]\n"
"s_waitcnt lgkmcnt(6)\n"
"global_load_dwordx4 a[68:71], v0, s[34:35] offset:-3072 sc1\n"
"v_lshlrev_b32 v12, 16, v16\n"
"v_mfma_f32_16x16x32_bf16 v[124:127], a[92:95], a[116:119], v[124:127]\n"
"ds_read_b128 a[116:119], v2 offset:21504\n"
"v_and_b32 v13, s63, v16\n"
"v_mfma_f32_16x16x32_bf16 v[124:127], a[88:91], a[112:115], v[124:127]\n"
"ds_read_b128 a[112:115], v2 offset:20480\n"
"v_lshlrev_b32 v14, 16, v17\n"
"v_mfma_f32_16x16x32_bf16 v[128:131], a[88:91], a[120:123], v[128:131]\n"
"v_and_b32 v15, s63, v17\n"
"v_xor_b32 v20, 64, v7\n"
"v_mfma_f32_16x16x32_bf16 v[128:131], a[92:95], a[124:127], v[128:131]\n"
"ds_read_b128 v[20:23], v20 offset:34816\n"
"v_mul_f32 v12, v12, v88\n"
"v_mfma_f32_16x16x32_bf16 v[100:103], a[84:87], a[124:127], v[100:103]\n"
"ds_read_b128 a[124:127], v2 offset:23552\n"
"v_mul_f32 v13, v13, v89\n"
"v_mfma_f32_16x16x32_bf16 v[100:103], a[80:83], a[120:123], v[100:103]\n"
"ds_read_b128 a[120:123], v2 offset:22528\n"
"v_mul_f32 v14, v14, v90\n"
"v_mfma_f32_16x16x32_bf16 v[104:107], a[80:83], a[128:131], v[104:107]\n"
"v_mul_f32 v15, v15, v91\n"
"v_cvt_pk_bf16_f32 v24, v12, v13\n"
"v_mfma_f32_16x16x32_bf16 v[104:107], a[84:87], a[132:135], v[104:107]\n"
"global_load_dwordx4 a[72:75], v0, s[34:35] offset:-2048 sc1\n"
"v_cvt_pk_bf16_f32 v25, v14, v15\n"
"v_mfma_f32_16x16x32_bf16 v[132:135], a[92:95], a[132:135], v[132:135]\n"
"ds_read_b128 a[132:135], v2 offset:25600\n"
"v_mfma_f32_16x16x32_bf16 v[132:135], a[88:91], a[128:131], v[132:135]\n"
"ds_read_b128 a[128:131], v2 offset:24576\n"
"v_mfma_f32_16x16x32_bf16 v[136:139], a[88:91], a[136:139], v[136:139]\n"
"ds_write_b64 v2, v[24:25] offset:1024\n"
"v_mfma_f32_16x16x32_bf16 v[136:139], a[92:95], a[140:143], v[136:139]\n"
"v_lshlrev_b32 v12, 16, v18\n"
"v_and_b32 v13, s63, v18\n"
"v_mfma_f32_16x16x32_bf16 v[108:111], a[84:87], a[140:143], v[108:111]\n"
"ds_read_b128 a[140:143], v2 offset:27648\n"
"v_lshlrev_b32 v14, 16, v19\n"
"v_mfma_f32_16x16x32_bf16 v[108:111], a[80:83], a[136:139], v[108:111]\n"
"ds_read_b128 a[136:139], v2 offset:26624\n"
"v_and_b32 v15, s63, v19\n"
"v_mfma_f32_16x16x32_bf16 v[112:115], a[80:83], a[144:147], v[112:115]\n"
"v_mul_f32 v12, v12, v116\n"
"v_mul_f32 v13, v13, v117\n"
"v_mfma_f32_16x16x32_bf16 v[112:115], a[84:87], a[148:151], v[112:115]\n"
"global_load_dwordx4 a[76:79], v0, s[34:35] offset:-1024 sc1\n"
"v_mul_f32 v14, v14, v118\n"
"v_mfma_f32_16x16x32_bf16 v[140:143], a[92:95], a[148:151], v[140:143]\n"
"ds_read_b128 a[148:151], v2 offset:29696\n"
"v_mul_f32 v15, v15, v119\n"
"v_mfma_f32_16x16x32_bf16 v[140:143], a[88:91], a[144:147], v[140:143]\n"
"ds_read_b128 a[144:147], v2 offset:28672\n"
"v_cvt_pk_bf16_f32 v26, v12, v13\n"
"v_mfma_f32_16x16x32_bf16 a[12:15], a[88:91], a[152:155], a[12:15]\n"
"v_cvt_pk_bf16_f32 v27, v14, v15\n"
"v_mfma_f32_16x16x32_bf16 a[12:15], a[92:95], a[156:159], a[12:15]\n"
"s_waitcnt lgkmcnt(9)\n"
"global_store_dwordx4 v0, v[24:27], s[52:53] offset:-3072 sc1\n"
"ds_write_b64 v2, v[26:27] offset:1032\n"
"v_lshlrev_b32 v12, 16, v20\n"
"v_mfma_f32_16x16x32_bf16 a[8:11], a[84:87], a[156:159], a[8:11]\n"
"ds_read_b128 a[156:159], v2 offset:31744\n"
"v_and_b32 v13, s63, v20\n"
"v_mfma_f32_16x16x32_bf16 a[8:11], a[80:83], a[152:155], a[8:11]\n"
"s_waitcnt vmcnt(10)\n"
"ds_read_b128 a[152:155], v2 offset:30720\n"
"v_lshlrev_b32 v14, 16, v21\n"
"v_mfma_f32_16x16x32_bf16 v[144:147], a[32:35], a[96:99], v[144:147]\n"
"ds_read_b128 v[16:19], v7 offset:36864\n"
"v_and_b32 v15, s63, v21\n"
"v_mfma_f32_16x16x32_bf16 v[144:147], a[36:39], a[100:103], v[144:147]\n"
"global_load_dwordx4 a[80:83], v0, s[34:35] offset:0 sc1\n"
"v_mul_f32 v12, v12, v92\n"
"v_mfma_f32_16x16x32_bf16 v[172:175], a[44:47], a[100:103], v[172:175]\n"
"v_mul_f32 v13, v13, v93\n"
"v_mul_f32 v14, v14, v94\n"
"v_mfma_f32_16x16x32_bf16 v[172:175], a[40:43], a[96:99], v[172:175]\n"
"v_mul_f32 v15, v15, v95\n"
"v_cvt_pk_bf16_f32 v28, v12, v13\n"
"v_mfma_f32_16x16x32_bf16 v[176:179], a[40:43], a[104:107], v[176:179]\n"
"v_cvt_pk_bf16_f32 v29, v14, v15\n"
"v_mfma_f32_16x16x32_bf16 v[176:179], a[44:47], a[108:111], v[176:179]\n"
"ds_write_b64 v2, v[28:29] offset:3072\n"
"v_mfma_f32_16x16x32_bf16 v[148:151], a[36:39], a[108:111], v[148:151]\n"
"v_lshlrev_b32 v12, 16, v22\n"
"v_and_b32 v13, s63, v22\n"
"v_mfma_f32_16x16x32_bf16 v[148:151], a[32:35], a[104:107], v[148:151]\n"
"s_waitcnt lgkmcnt(12)\n"
"v_lshlrev_b32 v14, 16, v23\n"
"v_and_b32 v15, s63, v23\n"
"v_mfma_f32_16x16x32_bf16 v[152:155], a[32:35], a[112:115], v[152:155]\n"
"v_mul_f32 v12, v12, v120\n"
"v_mul_f32 v13, v13, v121\n"
"v_mfma_f32_16x16x32_bf16 v[152:155], a[36:39], a[116:119], v[152:155]\n"
"global_load_dwordx4 a[84:87], v0, s[34:35] offset:1024 sc1\n"
"v_mul_f32 v14, v14, v122\n"
"v_mfma_f32_16x16x32_bf16 v[180:183], a[44:47], a[116:119], v[180:183]\n"
"v_mul_f32 v15, v15, v123\n"
"v_cvt_pk_bf16_f32 v30, v12, v13\n"
"v_mfma_f32_16x16x32_bf16 v[180:183], a[40:43], a[112:115], v[180:183]\n"
"v_cvt_pk_bf16_f32 v31, v14, v15\n"
"v_mfma_f32_16x16x32_bf16 v[184:187], a[40:43], a[120:123], v[184:187]\n"
"ds_write_b64 v2, v[30:31] offset:3080\n"
"v_mfma_f32_16x16x32_bf16 v[184:187], a[44:47], a[124:127], v[184:187]\n"
"s_waitcnt lgkmcnt(2)\n"
"v_lshlrev_b32 v12, 16, v16\n"
"v_and_b32 v13, s63, v16\n"
"v_mfma_f32_16x16x32_bf16 v[156:159], a[36:39], a[124:127], v[156:159]\n"
"v_lshlrev_b32 v14, 16, v17\n"
"v_and_b32 v15, s63, v17\n"
"v_mfma_f32_16x16x32_bf16 v[156:159], a[32:35], a[120:123], v[156:159]\n"
"v_xor_b32 v20, 64, v7\n"
"v_mfma_f32_16x16x32_bf16 v[160:163], a[32:35], a[128:131], v[160:163]\n"
"ds_read_b128 v[20:23], v20 offset:36864\n"
"v_mul_f32 v12, v12, v40\n"
"v_mfma_f32_16x16x32_bf16 v[160:163], a[36:39], a[132:135], v[160:163]\n"
"global_load_dwordx4 a[88:91], v0, s[34:35] offset:2048 sc1\n"
"v_mul_f32 v13, v13, v41\n"
"v_mfma_f32_16x16x32_bf16 v[188:191], a[44:47], a[132:135], v[188:191]\n"
"v_mul_f32 v14, v14, v42\n"
"v_mfma_f32_16x16x32_bf16 v[188:191], a[40:43], a[128:131], v[188:191]\n"
"v_mul_f32 v15, v15, v43\n"
"v_cvt_pk_bf16_f32 v24, v12, v13\n"
"v_mfma_f32_16x16x32_bf16 v[192:195], a[40:43], a[136:139], v[192:195]\n"
"v_cvt_pk_bf16_f32 v25, v14, v15\n"
"v_mfma_f32_16x16x32_bf16 v[192:195], a[44:47], a[140:143], v[192:195]\n"
"ds_write_b64 v2, v[24:25] offset:4096\n"
"v_mfma_f32_16x16x32_bf16 v[164:167], a[36:39], a[140:143], v[164:167]\n"
"v_lshlrev_b32 v12, 16, v18\n"
"v_and_b32 v13, s63, v18\n"
"v_mfma_f32_16x16x32_bf16 v[164:167], a[32:35], a[136:139], v[164:167]\n"
"v_lshlrev_b32 v14, 16, v19\n"
"v_mfma_f32_16x16x32_bf16 v[168:171], a[32:35], a[144:147], v[168:171]\n"
"v_and_b32 v15, s63, v19\n"
"v_mul_f32 v12, v12, v68\n"
"v_mfma_f32_16x16x32_bf16 v[168:171], a[36:39], a[148:151], v[168:171]\n"
"global_load_dwordx4 a[92:95], v0, s[34:35] offset:3072 sc1\n"
"v_mul_f32 v13, v13, v69\n"
"v_mfma_f32_16x16x32_bf16 v[196:199], a[44:47], a[148:151], v[196:199]\n"
"v_mul_f32 v14, v14, v70\n"
"v_mfma_f32_16x16x32_bf16 v[196:199], a[40:43], a[144:147], v[196:199]\n"
"v_mul_f32 v15, v15, v71\n"
"v_mfma_f32_16x16x32_bf16 a[20:23], a[40:43], a[152:155], a[20:23]\n"
"s_add_u32 s34, s34, 8192\n"
"v_cvt_pk_bf16_f32 v26, v12, v13\n"
"v_cvt_pk_bf16_f32 v27, v14, v15\n"
"v_mfma_f32_16x16x32_bf16 a[20:23], a[44:47], a[156:159], a[20:23]\n"
"global_store_dwordx4 v0, v[24:27], s[52:53] offset:0 sc1\n"
"ds_write_b64 v2, v[26:27] offset:4104\n"
"v_mfma_f32_16x16x32_bf16 a[16:19], a[36:39], a[156:159], a[16:19]\n"
"s_waitcnt lgkmcnt(2)\n"
"v_lshlrev_b32 v12, 16, v20\n"
"v_and_b32 v13, s63, v20\n"
"v_mfma_f32_16x16x32_bf16 a[16:19], a[32:35], a[152:155], a[16:19]\n"
"s_waitcnt vmcnt(11)\n"
"v_lshlrev_b32 v14, 16, v21\n"
"v_and_b32 v15, s63, v21\n"
"v_mfma_f32_16x16x32_bf16 v[200:203], a[48:51], a[96:99], v[200:203]\n"
"ds_read_b128 v[16:19], v7 offset:38912\n"
"v_mul_f32 v12, v12, v96\n"
"v_mfma_f32_16x16x32_bf16 v[200:203], a[52:55], a[100:103], v[200:203]\n"
"global_load_dwordx4 a[32:35], v0, s[34:35] offset:-4096 sc1\n"
"v_mul_f32 v13, v13, v97\n"
"v_mfma_f32_16x16x32_bf16 v[228:231], a[60:63], a[100:103], v[228:231]\n"
"ds_read_b128 a[100:103], v3 offset:17408\n"
"v_mul_f32 v14, v14, v98\n"
"v_mfma_f32_16x16x32_bf16 v[228:231], a[56:59], a[96:99], v[228:231]\n"
"ds_read_b128 a[96:99], v3 offset:16384\n"
"v_mul_f32 v15, v15, v99\n"
"v_mfma_f32_16x16x32_bf16 v[232:235], a[56:59], a[104:107], v[232:235]\n"
"v_cvt_pk_bf16_f32 v28, v12, v13\n"
"v_cvt_pk_bf16_f32 v29, v14, v15\n"
"v_mfma_f32_16x16x32_bf16 v[232:235], a[60:63], a[108:111], v[232:235]\n"
"ds_write_b64 v2, v[28:29] offset:5120\n"
"v_mfma_f32_16x16x32_bf16 v[204:207], a[52:55], a[108:111], v[204:207]\n"
"ds_read_b128 a[108:111], v3 offset:19456\n"
"v_mfma_f32_16x16x32_bf16 v[204:207], a[48:51], a[104:107], v[204:207]\n"
"ds_read_b128 a[104:107], v3 offset:18432\n"
"v_lshlrev_b32 v12, 16, v22\n"
"v_mfma_f32_16x16x32_bf16 v[208:211], a[48:51], a[112:115], v[208:211]\n"
"v_and_b32 v13, s63, v22\n"
"v_lshlrev_b32 v14, 16, v23\n"
"v_mfma_f32_16x16x32_bf16 v[208:211], a[52:55], a[116:119], v[208:211]\n"
"global_load_dwordx4 a[36:39], v0, s[34:35] offset:-3072 sc1\n"
"v_and_b32 v15, s63, v23\n"
"v_mfma_f32_16x16x32_bf16 v[236:239], a[60:63], a[116:119], v[236:239]\n"
"ds_read_b128 a[116:119], v3 offset:21504\n"
"v_mul_f32 v12, v12, v124\n"
"v_mfma_f32_16x16x32_bf16 v[236:239], a[56:59], a[112:115], v[236:239]\n"
"ds_read_b128 a[112:115], v3 offset:20480\n"
"v_mul_f32 v13, v13, v125\n"
"v_mfma_f32_16x16x32_bf16 v[240:243], a[56:59], a[120:123], v[240:243]\n"
"v_mul_f32 v14, v14, v126\n"
"v_mul_f32 v15, v15, v127\n"
"v_mfma_f32_16x16x32_bf16 v[240:243], a[60:63], a[124:127], v[240:243]\n"
"v_cvt_pk_bf16_f32 v30, v12, v13\n"
"v_mfma_f32_16x16x32_bf16 v[212:215], a[52:55], a[124:127], v[212:215]\n"
"ds_read_b128 a[124:127], v3 offset:23552\n"
"v_cvt_pk_bf16_f32 v31, v14, v15\n"
"v_mfma_f32_16x16x32_bf16 v[212:215], a[48:51], a[120:123], v[212:215]\n"
"global_store_dwordx4 v0, v[28:31], s[52:53] offset:1024 sc1\n"
"ds_read_b128 a[120:123], v3 offset:22528\n"
"ds_write_b64 v2, v[30:31] offset:5128\n"
"v_mfma_f32_16x16x32_bf16 v[216:219], a[48:51], a[128:131], v[216:219]\n"
"s_waitcnt lgkmcnt(10)\n"
"v_lshlrev_b32 v12, 16, v16\n"
"v_and_b32 v13, s63, v16\n"
"v_mfma_f32_16x16x32_bf16 v[216:219], a[52:55], a[132:135], v[216:219]\n"
"global_load_dwordx4 a[40:43], v0, s[34:35] offset:-2048 sc1\n"
"v_lshlrev_b32 v14, 16, v17\n"
"v_mfma_f32_16x16x32_bf16 v[244:247], a[60:63], a[132:135], v[244:247]\n"
"ds_read_b128 a[132:135], v3 offset:25600\n"
"v_and_b32 v15, s63, v17\n"
"v_mfma_f32_16x16x32_bf16 v[244:247], a[56:59], a[128:131], v[244:247]\n"
"ds_read_b128 a[128:131], v3 offset:24576\n"
"v_xor_b32 v20, 64, v7\n"
"v_mfma_f32_16x16x32_bf16 v[248:251], a[56:59], a[136:139], v[248:251]\n"
"ds_read_b128 v[20:23], v20 offset:38912\n"
"v_mul_f32 v12, v12, v44\n"
"v_mfma_f32_16x16x32_bf16 v[248:251], a[60:63], a[140:143], v[248:251]\n"
"v_mul_f32 v13, v13, v45\n"
"v_mfma_f32_16x16x32_bf16 v[220:223], a[52:55], a[140:143], v[220:223]\n"
"ds_read_b128 a[140:143], v3 offset:27648\n"
"v_mul_f32 v14, v14, v46\n"
"v_mfma_f32_16x16x32_bf16 v[220:223], a[48:51], a[136:139], v[220:223]\n"
"ds_read_b128 a[136:139], v3 offset:26624\n"
"v_mul_f32 v15, v15, v47\n"
"v_mfma_f32_16x16x32_bf16 v[224:227], a[48:51], a[144:147], v[224:227]\n"
"v_cvt_pk_bf16_f32 v24, v12, v13\n"
"v_cvt_pk_bf16_f32 v25, v14, v15\n"
"v_mfma_f32_16x16x32_bf16 v[224:227], a[52:55], a[148:151], v[224:227]\n"
"global_load_dwordx4 a[44:47], v0, s[34:35] offset:-1024 sc1\n"
"v_mfma_f32_16x16x32_bf16 v[252:255], a[60:63], a[148:151], v[252:255]\n"
"ds_read_b128 a[148:151], v3 offset:29696\n"
"v_mfma_f32_16x16x32_bf16 v[252:255], a[56:59], a[144:147], v[252:255]\n"
"ds_read_b128 a[144:147], v3 offset:28672\n"
"v_mfma_f32_16x16x32_bf16 a[28:31], a[56:59], a[152:155], a[28:31]\n"
"ds_write_b64 v2, v[24:25] offset:6144\n"
"v_mfma_f32_16x16x32_bf16 a[28:31], a[60:63], a[156:159], a[28:31]\n"
"v_lshlrev_b32 v12, 16, v18\n"
"v_and_b32 v13, s63, v18\n"
"v_mfma_f32_16x16x32_bf16 a[24:27], a[52:55], a[156:159], a[24:27]\n"
"ds_read_b128 a[156:159], v3 offset:31744\n"
"v_lshlrev_b32 v14, 16, v19\n"
"v_mfma_f32_16x16x32_bf16 a[24:27], a[48:51], a[152:155], a[24:27]\n"
"s_waitcnt vmcnt(11) lgkmcnt(14)\n"
"ds_read_b128 a[152:155], v3 offset:30720\n"
"v_and_b32 v15, s63, v19\n"
"v_mfma_f32_16x16x32_bf16 v[144:147], a[64:67], a[96:99], v[144:147]\n"
"v_mul_f32 v12, v12, v72\n"
"v_mul_f32 v13, v13, v73\n"
"v_mfma_f32_16x16x32_bf16 v[144:147], a[68:71], a[100:103], v[144:147]\n"
"global_load_dwordx4 a[48:51], v0, s[34:35] offset:0 sc1\n"
"v_mul_f32 v14, v14, v74\n"
"v_mfma_f32_16x16x32_bf16 v[172:175], a[76:79], a[100:103], v[172:175]\n"
"v_mul_f32 v15, v15, v75\n"
"v_cvt_pk_bf16_f32 v26, v12, v13\n"
"v_mfma_f32_16x16x32_bf16 v[172:175], a[72:75], a[96:99], v[172:175]\n"
"v_cvt_pk_bf16_f32 v27, v14, v15\n"
"v_mfma_f32_16x16x32_bf16 v[176:179], a[72:75], a[104:107], v[176:179]\n"
"ds_write_b64 v2, v[26:27] offset:6152\n"
"v_mfma_f32_16x16x32_bf16 v[176:179], a[76:79], a[108:111], v[176:179]\n"
"s_waitcnt lgkmcnt(8)\n"
"v_lshlrev_b32 v12, 16, v20\n"
"v_and_b32 v13, s63, v20\n"
"v_mfma_f32_16x16x32_bf16 v[148:151], a[68:71], a[108:111], v[148:151]\n"
"v_lshlrev_b32 v14, 16, v21\n"
"v_and_b32 v15, s63, v21\n"
"v_mfma_f32_16x16x32_bf16 v[148:151], a[64:67], a[104:107], v[148:151]\n"
"v_mul_f32 v12, v12, v100\n"
"v_mul_f32 v13, v13, v101\n"
"v_mfma_f32_16x16x32_bf16 v[152:155], a[64:67], a[112:115], v[152:155]\n"
"v_mul_f32 v14, v14, v102\n"
"v_mul_f32 v15, v15, v103\n"
"v_mfma_f32_16x16x32_bf16 v[152:155], a[68:71], a[116:119], v[152:155]\n"
"global_load_dwordx4 a[52:55], v0, s[34:35] offset:1024 sc1\n"
"v_cvt_pk_bf16_f32 v28, v12, v13\n"
"v_mfma_f32_16x16x32_bf16 v[180:183], a[76:79], a[116:119], v[180:183]\n"
"v_cvt_pk_bf16_f32 v29, v14, v15\n"
"v_mfma_f32_16x16x32_bf16 v[180:183], a[72:75], a[112:115], v[180:183]\n"
"ds_write_b64 v2, v[28:29] offset:7168\n"
"v_mfma_f32_16x16x32_bf16 v[184:187], a[72:75], a[120:123], v[184:187]\n"
"v_lshlrev_b32 v12, 16, v22\n"
"v_and_b32 v13, s63, v22\n"
"v_mfma_f32_16x16x32_bf16 v[184:187], a[76:79], a[124:127], v[184:187]\n"
"v_lshlrev_b32 v14, 16, v23\n"
"v_and_b32 v15, s63, v23\n"
"v_mfma_f32_16x16x32_bf16 v[156:159], a[68:71], a[124:127], v[156:159]\n"
"v_mul_f32 v12, v12, v128\n"
"v_mul_f32 v13, v13, v129\n"
"v_mfma_f32_16x16x32_bf16 v[156:159], a[64:67], a[120:123], v[156:159]\n"
"s_waitcnt lgkmcnt(7)\n"
"v_mul_f32 v14, v14, v130\n"
"v_mul_f32 v15, v15, v131\n"
"v_mfma_f32_16x16x32_bf16 v[160:163], a[64:67], a[128:131], v[160:163]\n"
"v_cvt_pk_bf16_f32 v30, v12, v13\n"
"v_cvt_pk_bf16_f32 v31, v14, v15\n"
"v_mfma_f32_16x16x32_bf16 v[160:163], a[68:71], a[132:135], v[160:163]\n"
"global_load_dwordx4 a[56:59], v0, s[34:35] offset:2048 sc1\n"
"v_mfma_f32_16x16x32_bf16 v[188:191], a[76:79], a[132:135], v[188:191]\n"
"global_store_dwordx4 v0, v[28:31], s[52:53] offset:3072 sc1\n"
"ds_write_b64 v2, v[30:31] offset:7176\n"
"v_accvgpr_read_b32 v16, a160\n"
"v_mfma_f32_16x16x32_bf16 v[188:191], a[72:75], a[128:131], v[188:191]\n"
"v_accvgpr_read_b32 v17, a161\n"
"v_mfma_f32_16x16x32_bf16 v[192:195], a[72:75], a[136:139], v[192:195]\n"
"v_accvgpr_read_b32 v18, a162\n"
"v_mfma_f32_16x16x32_bf16 v[192:195], a[76:79], a[140:143], v[192:195]\n"
"v_accvgpr_read_b32 v19, a163\n"
"v_lshlrev_b32 v12, 16, v16\n"
"v_mfma_f32_16x16x32_bf16 v[164:167], a[68:71], a[140:143], v[164:167]\n"
"v_and_b32 v13, s63, v16\n"
"v_mfma_f32_16x16x32_bf16 v[164:167], a[64:67], a[136:139], v[164:167]\n"
"s_waitcnt lgkmcnt(3)\n"
"v_lshlrev_b32 v14, 16, v17\n"
"v_mfma_f32_16x16x32_bf16 v[168:171], a[64:67], a[144:147], v[168:171]\n"
"v_and_b32 v15, s63, v17\n"
"v_mul_f32 v12, v12, v48\n"
"v_mfma_f32_16x16x32_bf16 v[168:171], a[68:71], a[148:151], v[168:171]\n"
"global_load_dwordx4 a[60:63], v0, s[34:35] offset:3072 sc1\n"
"v_mul_f32 v13, v13, v49\n"
"v_mfma_f32_16x16x32_bf16 v[196:199], a[76:79], a[148:151], v[196:199]\n"
"v_mul_f32 v14, v14, v50\n"
"v_mfma_f32_16x16x32_bf16 v[196:199], a[72:75], a[144:147], v[196:199]\n"
"v_mul_f32 v15, v15, v51\n"
"v_cvt_pk_bf16_f32 v24, v12, v13\n"
"v_mfma_f32_16x16x32_bf16 a[20:23], a[72:75], a[152:155], a[20:23]\n"
"s_add_u32 s34, s34, 8192\n"
"v_cvt_pk_bf16_f32 v25, v14, v15\n"
"v_mfma_f32_16x16x32_bf16 a[20:23], a[76:79], a[156:159], a[20:23]\n"
"ds_write_b64 v2, v[24:25] offset:8192\n"
"v_mfma_f32_16x16x32_bf16 a[16:19], a[68:71], a[156:159], a[16:19]\n"
"v_lshlrev_b32 v12, 16, v18\n"
"v_and_b32 v13, s63, v18\n"
"v_mfma_f32_16x16x32_bf16 a[16:19], a[64:67], a[152:155], a[16:19]\n"
"s_waitcnt vmcnt(11)\n"
"v_lshlrev_b32 v14, 16, v19\n"
"v_mfma_f32_16x16x32_bf16 v[200:203], a[80:83], a[96:99], v[200:203]\n"
"v_and_b32 v15, s63, v19\n"
"v_mul_f32 v12, v12, v76\n"
"v_mfma_f32_16x16x32_bf16 v[200:203], a[84:87], a[100:103], v[200:203]\n"
"global_load_dwordx4 a[64:67], v0, s[34:35] offset:-4096 sc1\n"
"v_mul_f32 v13, v13, v77\n"
"v_mfma_f32_16x16x32_bf16 v[228:231], a[92:95], a[100:103], v[228:231]\n"
"ds_read_b128 a[100:103], v4 offset:17408\n"
"v_mul_f32 v14, v14, v78\n"
"v_mfma_f32_16x16x32_bf16 v[228:231], a[88:91], a[96:99], v[228:231]\n"
"ds_read_b128 a[96:99], v4 offset:16384\n"
"v_mul_f32 v15, v15, v79\n"
"v_mfma_f32_16x16x32_bf16 v[232:235], a[88:91], a[104:107], v[232:235]\n"
"v_cvt_pk_bf16_f32 v26, v12, v13\n"
"v_cvt_pk_bf16_f32 v27, v14, v15\n"
"v_mfma_f32_16x16x32_bf16 v[232:235], a[92:95], a[108:111], v[232:235]\n"
"global_store_dwordx4 v0, v[24:27], s[54:55] offset:-4096 sc1\n"
"ds_write_b64 v2, v[26:27] offset:8200\n"
"v_accvgpr_read_b32 v16, a164\n"
"v_mfma_f32_16x16x32_bf16 v[204:207], a[84:87], a[108:111], v[204:207]\n"
"ds_read_b128 a[108:111], v4 offset:19456\n"
"v_accvgpr_read_b32 v17, a165\n"
"v_mfma_f32_16x16x32_bf16 v[204:207], a[80:83], a[104:107], v[204:207]\n"
"ds_read_b128 a[104:107], v4 offset:18432\n"
"v_accvgpr_read_b32 v18, a166\n"
"v_mfma_f32_16x16x32_bf16 v[208:211], a[80:83], a[112:115], v[208:211]\n"
"v_accvgpr_read_b32 v19, a167\n"
"v_lshlrev_b32 v12, 16, v16\n"
"v_mfma_f32_16x16x32_bf16 v[208:211], a[84:87], a[116:119], v[208:211]\n"
"global_load_dwordx4 a[68:71], v0, s[34:35] offset:-3072 sc1\n"
"v_and_b32 v13, s63, v16\n"
"v_mfma_f32_16x16x32_bf16 v[236:239], a[92:95], a[116:119], v[236:239]\n"
"ds_read_b128 a[116:119], v4 offset:21504\n"
"v_lshlrev_b32 v14, 16, v17\n"
"v_mfma_f32_16x16x32_bf16 v[236:239], a[88:91], a[112:115], v[236:239]\n"
"ds_read_b128 a[112:115], v4 offset:20480\n"
"v_and_b32 v15, s63, v17\n"
"v_mfma_f32_16x16x32_bf16 v[240:243], a[88:91], a[120:123], v[240:243]\n"
"v_mul_f32 v12, v12, v104\n"
"v_mul_f32 v13, v13, v105\n"
"v_mfma_f32_16x16x32_bf16 v[240:243], a[92:95], a[124:127], v[240:243]\n"
"v_mul_f32 v14, v14, v106\n"
"v_mul_f32 v15, v15, v107\n"
"v_mfma_f32_16x16x32_bf16 v[212:215], a[84:87], a[124:127], v[212:215]\n"
"ds_read_b128 a[124:127], v4 offset:23552\n"
"v_cvt_pk_bf16_f32 v28, v12, v13\n"
"v_mfma_f32_16x16x32_bf16 v[212:215], a[80:83], a[120:123], v[212:215]\n"
"ds_read_b128 a[120:123], v4 offset:22528\n"
"v_cvt_pk_bf16_f32 v29, v14, v15\n"
"v_mfma_f32_16x16x32_bf16 v[216:219], a[80:83], a[128:131], v[216:219]\n"
"ds_write_b64 v2, v[28:29] offset:9216\n"
"v_mfma_f32_16x16x32_bf16 v[216:219], a[84:87], a[132:135], v[216:219]\n"
"global_load_dwordx4 a[72:75], v0, s[34:35] offset:-2048 sc1\n"
"v_lshlrev_b32 v12, 16, v18\n"
"v_mfma_f32_16x16x32_bf16 v[244:247], a[92:95], a[132:135], v[244:247]\n"
"ds_read_b128 a[132:135], v4 offset:25600\n"
"v_and_b32 v13, s63, v18\n"
"v_mfma_f32_16x16x32_bf16 v[244:247], a[88:91], a[128:131], v[244:247]\n"
"ds_read_b128 a[128:131], v4 offset:24576\n"
"v_lshlrev_b32 v14, 16, v19\n"
"v_mfma_f32_16x16x32_bf16 v[248:251], a[88:91], a[136:139], v[248:251]\n"
"v_and_b32 v15, s63, v19\n"
"v_mul_f32 v12, v12, v132\n"
"v_mfma_f32_16x16x32_bf16 v[248:251], a[92:95], a[140:143], v[248:251]\n"
"v_mul_f32 v13, v13, v133\n"
"v_mul_f32 v14, v14, v134\n"
"v_mfma_f32_16x16x32_bf16 v[220:223], a[84:87], a[140:143], v[220:223]\n"
"ds_read_b128 a[140:143], v4 offset:27648\n"
"v_mul_f32 v15, v15, v135\n"
"v_mfma_f32_16x16x32_bf16 v[220:223], a[80:83], a[136:139], v[220:223]\n"
"ds_read_b128 a[136:139], v4 offset:26624\n"
"v_cvt_pk_bf16_f32 v30, v12, v13\n"
"v_mfma_f32_16x16x32_bf16 v[224:227], a[80:83], a[144:147], v[224:227]\n"
"v_cvt_pk_bf16_f32 v31, v14, v15\n"
"v_mfma_f32_16x16x32_bf16 v[224:227], a[84:87], a[148:151], v[224:227]\n"
"global_load_dwordx4 a[76:79], v0, s[34:35] offset:-1024 sc1\n"
"v_mfma_f32_16x16x32_bf16 v[252:255], a[92:95], a[148:151], v[252:255]\n"
"ds_read_b128 a[148:151], v4 offset:29696\n"
"v_mfma_f32_16x16x32_bf16 v[252:255], a[88:91], a[144:147], v[252:255]\n"
"ds_read_b128 a[144:147], v4 offset:28672\n"
"v_mfma_f32_16x16x32_bf16 a[28:31], a[88:91], a[152:155], a[28:31]\n"
"ds_write_b64 v2, v[30:31] offset:9224\n"
"v_mfma_f32_16x16x32_bf16 a[28:31], a[92:95], a[156:159], a[28:31]\n"
"v_accvgpr_read_b32 v16, a168\n"
"v_accvgpr_read_b32 v17, a169\n"
"v_mfma_f32_16x16x32_bf16 a[24:27], a[84:87], a[156:159], a[24:27]\n"
"ds_read_b128 a[156:159], v4 offset:31744\n"
"v_accvgpr_read_b32 v18, a170\n"
"v_mfma_f32_16x16x32_bf16 a[24:27], a[80:83], a[152:155], a[24:27]\n"
"s_waitcnt vmcnt(10) lgkmcnt(13)\n"
"ds_read_b128 a[152:155], v4 offset:30720\n"
"v_accvgpr_read_b32 v19, a171\n"
"v_mfma_f32_16x16x32_bf16 v[144:147], a[32:35], a[96:99], v[144:147]\n"
"v_lshlrev_b32 v12, 16, v16\n"
"v_and_b32 v13, s63, v16\n"
"v_mfma_f32_16x16x32_bf16 v[144:147], a[36:39], a[100:103], v[144:147]\n"
"global_load_dwordx4 a[80:83], v0, s[34:35] offset:0 sc1\n"
"v_lshlrev_b32 v14, 16, v17\n"
"v_mfma_f32_16x16x32_bf16 v[172:175], a[44:47], a[100:103], v[172:175]\n"
"v_and_b32 v15, s63, v17\n"
"v_mul_f32 v12, v12, v52\n"
"v_mfma_f32_16x16x32_bf16 v[172:175], a[40:43], a[96:99], v[172:175]\n"
"v_mul_f32 v13, v13, v53\n"
"v_mul_f32 v14, v14, v54\n"
"v_mfma_f32_16x16x32_bf16 v[176:179], a[40:43], a[104:107], v[176:179]\n"
"v_mul_f32 v15, v15, v55\n"
"v_cvt_pk_bf16_f32 v24, v12, v13\n"
"v_mfma_f32_16x16x32_bf16 v[176:179], a[44:47], a[108:111], v[176:179]\n"
"v_cvt_pk_bf16_f32 v25, v14, v15\n"
"v_mfma_f32_16x16x32_bf16 v[148:151], a[36:39], a[108:111], v[148:151]\n"
"ds_write_b64 v2, v[24:25] offset:10240\n"
"v_mfma_f32_16x16x32_bf16 v[148:151], a[32:35], a[104:107], v[148:151]\n"
"s_waitcnt lgkmcnt(11)\n"
"v_lshlrev_b32 v12, 16, v18\n"
"v_and_b32 v13, s63, v18\n"
"v_mfma_f32_16x16x32_bf16 v[152:155], a[32:35], a[112:115], v[152:155]\n"
"v_lshlrev_b32 v14, 16, v19\n"
"v_and_b32 v15, s63, v19\n"
"v_mfma_f32_16x16x32_bf16 v[152:155], a[36:39], a[116:119], v[152:155]\n"
"global_load_dwordx4 a[84:87], v0, s[34:35] offset:1024 sc1\n"
"v_mul_f32 v12, v12, v80\n"
"v_mfma_f32_16x16x32_bf16 v[180:183], a[44:47], a[116:119], v[180:183]\n"
"v_mul_f32 v13, v13, v81\n"
"v_mul_f32 v14, v14, v82\n"
"v_mfma_f32_16x16x32_bf16 v[180:183], a[40:43], a[112:115], v[180:183]\n"
"v_mul_f32 v15, v15, v83\n"
"v_cvt_pk_bf16_f32 v26, v12, v13\n"
"v_mfma_f32_16x16x32_bf16 v[184:187], a[40:43], a[120:123], v[184:187]\n"
"v_cvt_pk_bf16_f32 v27, v14, v15\n"
"v_mfma_f32_16x16x32_bf16 v[184:187], a[44:47], a[124:127], v[184:187]\n"
"global_store_dwordx4 v0, v[24:27], s[54:55] offset:-2048 sc1\n"
"ds_write_b64 v2, v[26:27] offset:10248\n"
"v_accvgpr_read_b32 v16, a172\n"
"v_mfma_f32_16x16x32_bf16 v[156:159], a[36:39], a[124:127], v[156:159]\n"
"v_accvgpr_read_b32 v17, a173\n"
"v_accvgpr_read_b32 v18, a174\n"
"v_mfma_f32_16x16x32_bf16 v[156:159], a[32:35], a[120:123], v[156:159]\n"
"s_waitcnt lgkmcnt(7)\n"
"v_accvgpr_read_b32 v19, a175\n"
"v_mfma_f32_16x16x32_bf16 v[160:163], a[32:35], a[128:131], v[160:163]\n"
"v_lshlrev_b32 v12, 16, v16\n"
"v_mfma_f32_16x16x32_bf16 v[160:163], a[36:39], a[132:135], v[160:163]\n"
"global_load_dwordx4 a[88:91], v0, s[34:35] offset:2048 sc1\n"
"v_and_b32 v13, s63, v16\n"
"v_mfma_f32_16x16x32_bf16 v[188:191], a[44:47], a[132:135], v[188:191]\n"
"v_lshlrev_b32 v14, 16, v17\n"
"v_and_b32 v15, s63, v17\n"
"v_mfma_f32_16x16x32_bf16 v[188:191], a[40:43], a[128:131], v[188:191]\n"
"v_mul_f32 v12, v12, v108\n"
"v_mfma_f32_16x16x32_bf16 v[192:195], a[40:43], a[136:139], v[192:195]\n"
"v_mul_f32 v13, v13, v109\n"
"v_mul_f32 v14, v14, v110\n"
"v_mfma_f32_16x16x32_bf16 v[192:195], a[44:47], a[140:143], v[192:195]\n"
"v_mul_f32 v15, v15, v111\n"
"v_mfma_f32_16x16x32_bf16 v[164:167], a[36:39], a[140:143], v[164:167]\n"
"v_cvt_pk_bf16_f32 v28, v12, v13\n"
"v_cvt_pk_bf16_f32 v29, v14, v15\n"
"v_mfma_f32_16x16x32_bf16 v[164:167], a[32:35], a[136:139], v[164:167]\n"
"s_waitcnt lgkmcnt(2)\n"
"ds_write_b64 v2, v[28:29] offset:11264\n"
"v_mfma_f32_16x16x32_bf16 v[168:171], a[32:35], a[144:147], v[168:171]\n"
"v_lshlrev_b32 v12, 16, v18\n"
"v_mfma_f32_16x16x32_bf16 v[168:171], a[36:39], a[148:151], v[168:171]\n"
"global_load_dwordx4 a[92:95], v0, s[34:35] offset:3072 sc1\n"
"v_and_b32 v13, s63, v18\n"
"v_mfma_f32_16x16x32_bf16 v[196:199], a[44:47], a[148:151], v[196:199]\n"
"v_lshlrev_b32 v14, 16, v19\n"
"v_and_b32 v15, s63, v19\n"
"v_mfma_f32_16x16x32_bf16 v[196:199], a[40:43], a[144:147], v[196:199]\n"
"v_mul_f32 v12, v12, v136\n"
"v_mul_f32 v13, v13, v137\n"
"v_mfma_f32_16x16x32_bf16 a[20:23], a[40:43], a[152:155], a[20:23]\n"
"s_add_u32 s34, s34, 8192\n"
"v_mul_f32 v14, v14, v138\n"
"v_mul_f32 v15, v15, v139\n"
"v_mfma_f32_16x16x32_bf16 a[20:23], a[44:47], a[156:159], a[20:23]\n"
"v_cvt_pk_bf16_f32 v30, v12, v13\n"
"v_cvt_pk_bf16_f32 v31, v14, v15\n"
"v_mfma_f32_16x16x32_bf16 a[16:19], a[36:39], a[156:159], a[16:19]\n"
"global_store_dwordx4 v0, v[28:31], s[54:55] offset:-1024 sc1\n"
"ds_write_b64 v2, v[30:31] offset:11272\n"
"v_accvgpr_read_b32 v16, a176\n"
"v_mfma_f32_16x16x32_bf16 a[16:19], a[32:35], a[152:155], a[16:19]\n"
"s_waitcnt vmcnt(11)\n"
"v_accvgpr_read_b32 v17, a177\n"
"v_accvgpr_read_b32 v18, a178\n"
"v_mfma_f32_16x16x32_bf16 v[200:203], a[48:51], a[96:99], v[200:203]\n"
"v_accvgpr_read_b32 v19, a179\n"
"v_lshlrev_b32 v12, 16, v16\n"
"v_mfma_f32_16x16x32_bf16 v[200:203], a[52:55], a[100:103], v[200:203]\n"
"global_load_dwordx4 a[32:35], v0, s[34:35] offset:-4096 sc1\n"
"v_and_b32 v13, s63, v16\n"
"v_mfma_f32_16x16x32_bf16 v[228:231], a[60:63], a[100:103], v[228:231]\n"
"ds_read_b128 a[100:103], v5 offset:17408\n"
"v_lshlrev_b32 v14, 16, v17\n"
"v_mfma_f32_16x16x32_bf16 v[228:231], a[56:59], a[96:99], v[228:231]\n"
"ds_read_b128 a[96:99], v5 offset:16384\n"
"v_and_b32 v15, s63, v17\n"
"v_mfma_f32_16x16x32_bf16 v[232:235], a[56:59], a[104:107], v[232:235]\n"
"v_mul_f32 v12, v12, v56\n"
"v_mul_f32 v13, v13, v57\n"
"v_mfma_f32_16x16x32_bf16 v[232:235], a[60:63], a[108:111], v[232:235]\n"
"v_mul_f32 v14, v14, v58\n"
"v_mul_f32 v15, v15, v59\n"
"v_mfma_f32_16x16x32_bf16 v[204:207], a[52:55], a[108:111], v[204:207]\n"
"ds_read_b128 a[108:111], v5 offset:19456\n"
"v_cvt_pk_bf16_f32 v24, v12, v13\n"
"v_mfma_f32_16x16x32_bf16 v[204:207], a[48:51], a[104:107], v[204:207]\n"
"ds_read_b128 a[104:107], v5 offset:18432\n"
"v_cvt_pk_bf16_f32 v25, v14, v15\n"
"v_mfma_f32_16x16x32_bf16 v[208:211], a[48:51], a[112:115], v[208:211]\n"
"ds_write_b64 v2, v[24:25] offset:12288\n"
"v_mfma_f32_16x16x32_bf16 v[208:211], a[52:55], a[116:119], v[208:211]\n"
"global_load_dwordx4 a[36:39], v0, s[34:35] offset:-3072 sc1\n"
"v_mfma_f32_16x16x32_bf16 v[236:239], a[60:63], a[116:119], v[236:239]\n"
"ds_read_b128 a[116:119], v5 offset:21504\n"
"v_mfma_f32_16x16x32_bf16 v[236:239], a[56:59], a[112:115], v[236:239]\n"
"ds_read_b128 a[112:115], v5 offset:20480\n"
"v_mfma_f32_16x16x32_bf16 v[240:243], a[56:59], a[120:123], v[240:243]\n"
"v_lshlrev_b32 v12, 16, v18\n"
"v_mfma_f32_16x16x32_bf16 v[240:243], a[60:63], a[124:127], v[240:243]\n"
"v_and_b32 v13, s63, v18\n"
"v_lshlrev_b32 v14, 16, v19\n"
"v_mfma_f32_16x16x32_bf16 v[212:215], a[52:55], a[124:127], v[212:215]\n"
"ds_read_b128 a[124:127], v5 offset:23552\n"
"v_and_b32 v15, s63, v19\n"
"v_mfma_f32_16x16x32_bf16 v[212:215], a[48:51], a[120:123], v[212:215]\n"
"ds_read_b128 a[120:123], v5 offset:22528\n"
"v_mul_f32 v12, v12, v84\n"
"v_mfma_f32_16x16x32_bf16 v[216:219], a[48:51], a[128:131], v[216:219]\n"
"v_mul_f32 v13, v13, v85\n"
"v_mul_f32 v14, v14, v86\n"
"v_mfma_f32_16x16x32_bf16 v[216:219], a[52:55], a[132:135], v[216:219]\n"
"global_load_dwordx4 a[40:43], v0, s[34:35] offset:-2048 sc1\n"
"v_mul_f32 v15, v15, v87\n"
"v_mfma_f32_16x16x32_bf16 v[244:247], a[60:63], a[132:135], v[244:247]\n"
"ds_read_b128 a[132:135], v5 offset:25600\n"
"v_cvt_pk_bf16_f32 v26, v12, v13\n"
"v_mfma_f32_16x16x32_bf16 v[244:247], a[56:59], a[128:131], v[244:247]\n"
"ds_read_b128 a[128:131], v5 offset:24576\n"
"v_cvt_pk_bf16_f32 v27, v14, v15\n"
"v_mfma_f32_16x16x32_bf16 v[248:251], a[56:59], a[136:139], v[248:251]\n"
"ds_write_b64 v2, v[26:27] offset:12296\n"
"v_mfma_f32_16x16x32_bf16 v[248:251], a[60:63], a[140:143], v[248:251]\n"
"v_accvgpr_read_b32 v16, a180\n"
"v_accvgpr_read_b32 v17, a181\n"
"v_mfma_f32_16x16x32_bf16 v[220:223], a[52:55], a[140:143], v[220:223]\n"
"ds_read_b128 a[140:143], v5 offset:27648\n"
"v_accvgpr_read_b32 v18, a182\n"
"v_mfma_f32_16x16x32_bf16 v[220:223], a[48:51], a[136:139], v[220:223]\n"
"ds_read_b128 a[136:139], v5 offset:26624\n"
"v_accvgpr_read_b32 v19, a183\n"
"v_mfma_f32_16x16x32_bf16 v[224:227], a[48:51], a[144:147], v[224:227]\n"
"v_lshlrev_b32 v12, 16, v16\n"
"v_and_b32 v13, s63, v16\n"
"v_mfma_f32_16x16x32_bf16 v[224:227], a[52:55], a[148:151], v[224:227]\n"
"global_load_dwordx4 a[44:47], v0, s[34:35] offset:-1024 sc1\n"
"v_lshlrev_b32 v14, 16, v17\n"
"v_mfma_f32_16x16x32_bf16 v[252:255], a[60:63], a[148:151], v[252:255]\n"
"ds_read_b128 a[148:151], v5 offset:29696\n"
"v_and_b32 v15, s63, v17\n"
"v_mfma_f32_16x16x32_bf16 v[252:255], a[56:59], a[144:147], v[252:255]\n"
"ds_read_b128 a[144:147], v5 offset:28672\n"
"v_mul_f32 v12, v12, v112\n"
"v_mfma_f32_16x16x32_bf16 a[28:31], a[56:59], a[152:155], a[28:31]\n"
"v_mul_f32 v13, v13, v113\n"
"v_mul_f32 v14, v14, v114\n"
"v_mfma_f32_16x16x32_bf16 a[28:31], a[60:63], a[156:159], a[28:31]\n"
"v_mul_f32 v15, v15, v115\n"
"v_cvt_pk_bf16_f32 v28, v12, v13\n"
"v_mfma_f32_16x16x32_bf16 a[24:27], a[52:55], a[156:159], a[24:27]\n"
"ds_read_b128 a[156:159], v5 offset:31744\n"
"v_cvt_pk_bf16_f32 v29, v14, v15\n"
"v_mfma_f32_16x16x32_bf16 a[24:27], a[48:51], a[152:155], a[24:27]\n"
"s_waitcnt vmcnt(10) lgkmcnt(13)\n"
"ds_read_b128 a[152:155], v5 offset:30720\n"
"v_mfma_f32_16x16x32_bf16 v[144:147], a[64:67], a[96:99], v[144:147]\n"
"ds_write_b64 v2, v[28:29] offset:13312\n"
"v_mfma_f32_16x16x32_bf16 v[144:147], a[68:71], a[100:103], v[144:147]\n"
"global_load_dwordx4 a[48:51], v0, s[34:35] offset:0 sc1\n"
"v_lshlrev_b32 v12, 16, v18\n"
"v_mfma_f32_16x16x32_bf16 v[172:175], a[76:79], a[100:103], v[172:175]\n"
"v_and_b32 v13, s63, v18\n"
"v_lshlrev_b32 v14, 16, v19\n"
"v_mfma_f32_16x16x32_bf16 v[172:175], a[72:75], a[96:99], v[172:175]\n"
"v_and_b32 v15, s63, v19\n"
"v_mul_f32 v12, v12, v140\n"
"v_mfma_f32_16x16x32_bf16 v[176:179], a[72:75], a[104:107], v[176:179]\n"
"v_mul_f32 v13, v13, v141\n"
"v_mul_f32 v14, v14, v142\n"
"v_mfma_f32_16x16x32_bf16 v[176:179], a[76:79], a[108:111], v[176:179]\n"
"v_mul_f32 v15, v15, v143\n"
"v_cvt_pk_bf16_f32 v30, v12, v13\n"
"v_mfma_f32_16x16x32_bf16 v[148:151], a[68:71], a[108:111], v[148:151]\n"
"v_cvt_pk_bf16_f32 v31, v14, v15\n"
"v_mfma_f32_16x16x32_bf16 v[148:151], a[64:67], a[104:107], v[148:151]\n"
"s_waitcnt lgkmcnt(10)\n"
"global_store_dwordx4 v0, v[28:31], s[54:55] offset:1024 sc1\n"
"ds_write_b64 v2, v[30:31] offset:13320\n"
"v_mfma_f32_16x16x32_bf16 v[152:155], a[64:67], a[112:115], v[152:155]\n"
"v_accvgpr_read_b32 v16, a184\n"
"v_accvgpr_read_b32 v17, a185\n"
"v_mfma_f32_16x16x32_bf16 v[152:155], a[68:71], a[116:119], v[152:155]\n"
"global_load_dwordx4 a[52:55], v0, s[34:35] offset:1024 sc1\n"
"v_accvgpr_read_b32 v18, a186\n"
"v_mfma_f32_16x16x32_bf16 v[180:183], a[76:79], a[116:119], v[180:183]\n"
"v_accvgpr_read_b32 v19, a187\n"
"v_mfma_f32_16x16x32_bf16 v[180:183], a[72:75], a[112:115], v[180:183]\n"
"v_lshlrev_b32 v12, 16, v16\n"
"v_and_b32 v13, s63, v16\n"
"v_mfma_f32_16x16x32_bf16 v[184:187], a[72:75], a[120:123], v[184:187]\n"
"v_lshlrev_b32 v14, 16, v17\n"
"v_mfma_f32_16x16x32_bf16 v[184:187], a[76:79], a[124:127], v[184:187]\n"
"v_and_b32 v15, s63, v17\n"
"v_accvgpr_read_b32 v10, a0\n"
"v_mfma_f32_16x16x32_bf16 v[156:159], a[68:71], a[124:127], v[156:159]\n"
"v_accvgpr_read_b32 v11, a1\n"
"v_mfma_f32_16x16x32_bf16 v[156:159], a[64:67], a[120:123], v[156:159]\n"
"s_waitcnt lgkmcnt(6)\n"
"v_mul_f32 v12, v12, v10\n"
"v_mfma_f32_16x16x32_bf16 v[160:163], a[64:67], a[128:131], v[160:163]\n"
"v_mul_f32 v13, v13, v11\n"
"v_accvgpr_read_b32 v10, a2\n"
"v_mfma_f32_16x16x32_bf16 v[160:163], a[68:71], a[132:135], v[160:163]\n"
"global_load_dwordx4 a[56:59], v0, s[34:35] offset:2048 sc1\n"
"v_accvgpr_read_b32 v11, a3\n"
"v_mfma_f32_16x16x32_bf16 v[188:191], a[76:79], a[132:135], v[188:191]\n"
"v_mul_f32 v14, v14, v10\n"
"v_mfma_f32_16x16x32_bf16 v[188:191], a[72:75], a[128:131], v[188:191]\n"
"v_mul_f32 v15, v15, v11\n"
"v_cvt_pk_bf16_f32 v24, v12, v13\n"
"v_mfma_f32_16x16x32_bf16 v[192:195], a[72:75], a[136:139], v[192:195]\n"
"v_cvt_pk_bf16_f32 v25, v14, v15\n"
"v_mfma_f32_16x16x32_bf16 v[192:195], a[76:79], a[140:143], v[192:195]\n"
"ds_write_b64 v2, v[24:25] offset:14336\n"
"v_mfma_f32_16x16x32_bf16 v[164:167], a[68:71], a[140:143], v[164:167]\n"
"v_lshlrev_b32 v12, 16, v18\n"
"v_and_b32 v13, s63, v18\n"
"v_mfma_f32_16x16x32_bf16 v[164:167], a[64:67], a[136:139], v[164:167]\n"
"s_waitcnt lgkmcnt(3)\n"
"v_lshlrev_b32 v14, 16, v19\n"
"v_mfma_f32_16x16x32_bf16 v[168:171], a[64:67], a[144:147], v[168:171]\n"
"v_and_b32 v15, s63, v19\n"
"v_accvgpr_read_b32 v10, a4\n"
"v_mfma_f32_16x16x32_bf16 v[168:171], a[68:71], a[148:151], v[168:171]\n"
"global_load_dwordx4 a[60:63], v0, s[34:35] offset:3072 sc1\n"
"v_accvgpr_read_b32 v11, a5\n"
"v_mfma_f32_16x16x32_bf16 v[196:199], a[76:79], a[148:151], v[196:199]\n"
"v_mul_f32 v12, v12, v10\n"
"v_mfma_f32_16x16x32_bf16 v[196:199], a[72:75], a[144:147], v[196:199]\n"
"v_mul_f32 v13, v13, v11\n"
"v_accvgpr_read_b32 v10, a6\n"
"v_mfma_f32_16x16x32_bf16 a[20:23], a[72:75], a[152:155], a[20:23]\n"
"s_add_u32 s34, s34, 8192\n"
"v_accvgpr_read_b32 v11, a7\n"
"v_mfma_f32_16x16x32_bf16 a[20:23], a[76:79], a[156:159], a[20:23]\n"
"v_mul_f32 v14, v14, v10\n"
"v_mul_f32 v15, v15, v11\n"
"v_mfma_f32_16x16x32_bf16 a[16:19], a[68:71], a[156:159], a[16:19]\n"
"v_cvt_pk_bf16_f32 v26, v12, v13\n"
"v_cvt_pk_bf16_f32 v27, v14, v15\n"
"v_mfma_f32_16x16x32_bf16 a[16:19], a[64:67], a[152:155], a[16:19]\n"
"s_waitcnt vmcnt(10)\n"
"global_store_dwordx4 v0, v[24:27], s[54:55] offset:2048 sc1\n"
"ds_write_b64 v2, v[26:27] offset:14344\n"
"v_mfma_f32_16x16x32_bf16 v[200:203], a[80:83], a[96:99], v[200:203]\n"
"v_accvgpr_read_b32 v16, a188\n"
"v_accvgpr_read_b32 v17, a189\n"
"v_mfma_f32_16x16x32_bf16 v[200:203], a[84:87], a[100:103], v[200:203]\n"
"global_load_dwordx4 a[64:67], v0, s[34:35] offset:-4096 sc1\n"
"v_accvgpr_read_b32 v18, a190\n"
"v_mfma_f32_16x16x32_bf16 v[228:231], a[92:95], a[100:103], v[228:231]\n"
"ds_read_b128 a[100:103], v2 offset:1024\n"
"v_accvgpr_read_b32 v19, a191\n"
"v_mfma_f32_16x16x32_bf16 v[228:231], a[88:91], a[96:99], v[228:231]\n"
"ds_read_b128 a[96:99], v2 offset:0\n"
"v_lshlrev_b32 v12, 16, v16\n"
"v_mfma_f32_16x16x32_bf16 v[232:235], a[88:91], a[104:107], v[232:235]\n"
"v_and_b32 v13, s63, v16\n"
"v_lshlrev_b32 v14, 16, v17\n"
"v_mfma_f32_16x16x32_bf16 v[232:235], a[92:95], a[108:111], v[232:235]\n"
"v_and_b32 v15, s63, v17\n"
"v_accvgpr_read_b32 v10, a8\n"
"v_mfma_f32_16x16x32_bf16 v[204:207], a[84:87], a[108:111], v[204:207]\n"
"ds_read_b128 a[108:111], v2 offset:3072\n"
"v_accvgpr_read_b32 v11, a9\n"
"v_mfma_f32_16x16x32_bf16 v[204:207], a[80:83], a[104:107], v[204:207]\n"
"ds_read_b128 a[104:107], v2 offset:2048\n"
"v_mul_f32 v12, v12, v10\n"
"v_mfma_f32_16x16x32_bf16 v[208:211], a[80:83], a[112:115], v[208:211]\n"
"v_mul_f32 v13, v13, v11\n"
"v_accvgpr_read_b32 v10, a10\n"
"v_mfma_f32_16x16x32_bf16 v[208:211], a[84:87], a[116:119], v[208:211]\n"
"global_load_dwordx4 a[68:71], v0, s[34:35] offset:-3072 sc1\n"
"v_accvgpr_read_b32 v11, a11\n"
"v_mfma_f32_16x16x32_bf16 v[236:239], a[92:95], a[116:119], v[236:239]\n"
"ds_read_b128 a[116:119], v2 offset:5120\n"
"v_mul_f32 v14, v14, v10\n"
"v_mfma_f32_16x16x32_bf16 v[236:239], a[88:91], a[112:115], v[236:239]\n"
"ds_read_b128 a[112:115], v2 offset:4096\n"
"v_mul_f32 v15, v15, v11\n"
"v_mfma_f32_16x16x32_bf16 v[240:243], a[88:91], a[120:123], v[240:243]\n"
"v_cvt_pk_bf16_f32 v28, v12, v13\n"
"v_cvt_pk_bf16_f32 v29, v14, v15\n"
"v_mfma_f32_16x16x32_bf16 v[240:243], a[92:95], a[124:127], v[240:243]\n"
"ds_write_b64 v2, v[28:29] offset:15360\n"
"v_mfma_f32_16x16x32_bf16 v[212:215], a[84:87], a[124:127], v[212:215]\n"
"ds_read_b128 a[124:127], v2 offset:7168\n"
"v_mfma_f32_16x16x32_bf16 v[212:215], a[80:83], a[120:123], v[212:215]\n"
"ds_read_b128 a[120:123], v2 offset:6144\n"
"v_lshlrev_b32 v12, 16, v18\n"
"v_mfma_f32_16x16x32_bf16 v[216:219], a[80:83], a[128:131], v[216:219]\n"
"v_and_b32 v13, s63, v18\n"
"v_lshlrev_b32 v14, 16, v19\n"
"v_mfma_f32_16x16x32_bf16 v[216:219], a[84:87], a[132:135], v[216:219]\n"
"global_load_dwordx4 a[72:75], v0, s[34:35] offset:-2048 sc1\n"
"v_and_b32 v15, s63, v19\n"
"v_mfma_f32_16x16x32_bf16 v[244:247], a[92:95], a[132:135], v[244:247]\n"
"ds_read_b128 a[132:135], v2 offset:9216\n"
"v_accvgpr_read_b32 v10, a12\n"
"v_mfma_f32_16x16x32_bf16 v[244:247], a[88:91], a[128:131], v[244:247]\n"
"ds_read_b128 a[128:131], v2 offset:8192\n"
"v_accvgpr_read_b32 v11, a13\n"
"v_mfma_f32_16x16x32_bf16 v[248:251], a[88:91], a[136:139], v[248:251]\n"
"v_mul_f32 v12, v12, v10\n"
"v_mul_f32 v13, v13, v11\n"
"v_mfma_f32_16x16x32_bf16 v[248:251], a[92:95], a[140:143], v[248:251]\n"
"v_accvgpr_read_b32 v10, a14\n"
"v_accvgpr_read_b32 v11, a15\n"
"v_mfma_f32_16x16x32_bf16 v[220:223], a[84:87], a[140:143], v[220:223]\n"
"ds_read_b128 a[140:143], v2 offset:11264\n"
"v_mul_f32 v14, v14, v10\n"
"v_mfma_f32_16x16x32_bf16 v[220:223], a[80:83], a[136:139], v[220:223]\n"
"ds_read_b128 a[136:139], v2 offset:10240\n"
"v_mul_f32 v15, v15, v11\n"
"v_mfma_f32_16x16x32_bf16 v[224:227], a[80:83], a[144:147], v[224:227]\n"
"v_cvt_pk_bf16_f32 v30, v12, v13\n"
"v_mfma_f32_16x16x32_bf16 v[224:227], a[84:87], a[148:151], v[224:227]\n"
"global_load_dwordx4 a[76:79], v0, s[34:35] offset:-1024 sc1\n"
"v_cvt_pk_bf16_f32 v31, v14, v15\n"
"v_mfma_f32_16x16x32_bf16 v[252:255], a[92:95], a[148:151], v[252:255]\n"
"global_store_dwordx4 v0, v[28:31], s[54:55] offset:3072 sc1\n"
"ds_read_b128 a[148:151], v2 offset:13312\n"
"ds_write_b64 v2, v[30:31] offset:15368\n"
"v_mfma_f32_16x16x32_bf16 v[252:255], a[88:91], a[144:147], v[252:255]\n"
"ds_read_b128 a[144:147], v2 offset:12288\n"
"v_mfma_f32_16x16x32_bf16 a[28:31], a[88:91], a[152:155], a[28:31]\n"
"s_add_u32 s62, s62, 1\n"
"v_mfma_f32_16x16x32_bf16 a[28:31], a[92:95], a[156:159], a[28:31]\n"
"v_mfma_f32_16x16x32_bf16 a[24:27], a[84:87], a[156:159], a[24:27]\n"
"ds_read_b128 a[156:159], v2 offset:15360\n"
"v_mfma_f32_16x16x32_bf16 a[24:27], a[80:83], a[152:155], a[24:27]\n"
"ds_read_b128 a[152:155], v2 offset:14336\n"
"L_layer:\n"
"s_waitcnt vmcnt(11) lgkmcnt(14)\n"
"v_mfma_f32_16x16x32_bf16 v[32:35], a[32:35], a[96:99], v[32:35]\n"
"s_mov_b64 s[96:97], s[52:53]\n"
"v_accvgpr_read_b32 v16, a192\n"
"v_accvgpr_read_b32 v17, a193\n"
"v_mfma_f32_16x16x32_bf16 v[32:35], a[36:39], a[100:103], v[32:35]\n"
"global_load_dwordx4 a[80:83], v0, s[34:35] offset:0 sc1\n"
"v_accvgpr_read_b32 v18, a194\n"
"v_mfma_f32_16x16x32_bf16 v[60:63], a[44:47], a[100:103], v[60:63]\n"
"s_mov_b64 s[98:99], s[54:55]\n"
"v_accvgpr_read_b32 v19, a195\n"
"v_mfma_f32_16x16x32_bf16 v[60:63], a[40:43], a[96:99], v[60:63]\n"
"v_lshlrev_b32 v12, 16, v16\n"
"v_and_b32 v13, s63, v16\n"
"v_mfma_f32_16x16x32_bf16 v[64:67], a[40:43], a[104:107], v[64:67]\n"
"s_add_u32 s52, s52, 131072\n"
"s_addc_u32 s53, s53, 0\n"
"v_lshlrev_b32 v14, 16, v17\n"
"v_mfma_f32_16x16x32_bf16 v[64:67], a[44:47], a[108:111], v[64:67]\n"
"v_and_b32 v15, s63, v17\n"
"v_mul_f32 v12, v12, v144\n"
"v_mfma_f32_16x16x32_bf16 v[36:39], a[36:39], a[108:111], v[36:39]\n"
"s_add_u32 s54, s54, 131072\n"
"s_addc_u32 s55, s55, 0\n"
"v_mul_f32 v13, v13, v145\n"
"v_mfma_f32_16x16x32_bf16 v[36:39], a[32:35], a[104:107], v[36:39]\n"
"s_waitcnt lgkmcnt(9)\n"
"v_mul_f32 v14, v14, v146\n"
"v_mul_f32 v15, v15, v147\n"
"v_mfma_f32_16x16x32_bf16 v[40:43], a[32:35], a[112:115], v[40:43]\n"
"ds_read_b128 v[20:23], v2 offset:0\n"
"v_cvt_pk_bf16_f32 v24, v12, v13\n"
"v_mfma_f32_16x16x32_bf16 v[40:43], a[36:39], a[116:119], v[40:43]\n"
"global_load_dwordx4 a[84:87], v0, s[34:35] offset:1024 sc1\n"
"v_cvt_pk_bf16_f32 v25, v14, v15\n"
"v_mfma_f32_16x16x32_bf16 v[68:71], a[44:47], a[116:119], v[68:71]\n"
"ds_write_b64 v2, v[24:25] offset:16384\n"
"v_mfma_f32_16x16x32_bf16 v[68:71], a[40:43], a[112:115], v[68:71]\n"
"v_lshlrev_b32 v12, 16, v18\n"
"v_and_b32 v13, s63, v18\n"
"v_mfma_f32_16x16x32_bf16 v[72:75], a[40:43], a[120:123], v[72:75]\n"
"s_waitcnt lgkmcnt(5)\n"
"s_barrier\n"
"v_mfma_f32_16x16x32_bf16 v[72:75], a[44:47], a[124:127], v[72:75]\n"
"v_lshlrev_b32 v14, 16, v19\n"
"v_and_b32 v15, s63, v19\n"
"v_mfma_f32_16x16x32_bf16 v[44:47], a[36:39], a[124:127], v[44:47]\n"
"v_mul_f32 v12, v12, v172\n"
"v_mul_f32 v13, v13, v173\n"
"v_mfma_f32_16x16x32_bf16 v[44:47], a[32:35], a[120:123], v[44:47]\n"
"v_mul_f32 v14, v14, v174\n"
"v_mfma_f32_16x16x32_bf16 v[48:51], a[32:35], a[128:131], v[48:51]\n"
"v_mul_f32 v15, v15, v175\n"
"v_mfma_f32_16x16x32_bf16 v[48:51], a[36:39], a[132:135], v[48:51]\n"
"global_load_dwordx4 a[88:91], v0, s[34:35] offset:2048 sc1\n"
"v_mfma_f32_16x16x32_bf16 v[76:79], a[44:47], a[132:135], v[76:79]\n"
"v_cvt_pk_bf16_f32 v26, v12, v13\n"
"v_mfma_f32_16x16x32_bf16 v[76:79], a[40:43], a[128:131], v[76:79]\n"
"v_cvt_pk_bf16_f32 v27, v14, v15\n"
"v_mfma_f32_16x16x32_bf16 v[80:83], a[40:43], a[136:139], v[80:83]\n"
"s_waitcnt lgkmcnt(1)\n"
"global_store_dwordx4 v0, v[20:23], s[96:97] offset:-4096 sc1\n"
"ds_write_b64 v2, v[26:27] offset:16392\n"
"v_accvgpr_read_b32 v16, a196\n"
"v_mfma_f32_16x16x32_bf16 v[80:83], a[44:47], a[140:143], v[80:83]\n"
"v_accvgpr_read_b32 v17, a197\n"
"v_accvgpr_read_b32 v18, a198\n"
"v_mfma_f32_16x16x32_bf16 v[52:55], a[36:39], a[140:143], v[52:55]\n"
"v_accvgpr_read_b32 v19, a199\n"
"v_lshlrev_b32 v12, 16, v16\n"
"v_mfma_f32_16x16x32_bf16 v[52:55], a[32:35], a[136:139], v[52:55]\n"
"v_and_b32 v13, s63, v16\n"
"v_lshlrev_b32 v14, 16, v17\n"
"v_mfma_f32_16x16x32_bf16 v[56:59], a[32:35], a[144:147], v[56:59]\n"
"v_and_b32 v15, s63, v17\n"
"v_mul_f32 v12, v12, v200\n"
"v_mfma_f32_16x16x32_bf16 v[56:59], a[36:39], a[148:151], v[56:59]\n"
"global_load_dwordx4 a[92:95], v0, s[34:35] offset:3072 sc1\n"
"v_mul_f32 v13, v13, v201\n"
"v_mfma_f32_16x16x32_bf16 v[84:87], a[44:47], a[148:151], v[84:87]\n"
"v_mul_f32 v14, v14, v202\n"
"v_mul_f32 v15, v15, v203\n"
"v_mfma_f32_16x16x32_bf16 v[84:87], a[40:43], a[144:147], v[84:87]\n"
"v_cvt_pk_bf16_f32 v28, v12, v13\n"
"v_cvt_pk_bf16_f32 v29, v14, v15\n"
"v_mfma_f32_16x16x32_bf16 a[4:7], a[40:43], a[152:155], a[4:7]\n"
"s_add_u32 s34, s34, 8192\n"
"ds_write_b64 v2, v[28:29] offset:17408\n"
"v_mfma_f32_16x16x32_bf16 a[4:7], a[44:47], a[156:159], a[4:7]\n"
"v_lshlrev_b32 v12, 16, v18\n"
"v_mfma_f32_16x16x32_bf16 a[0:3], a[36:39], a[156:159], a[0:3]\n"
"v_and_b32 v13, s63, v18\n"
"v_lshlrev_b32 v14, 16, v19\n"
"v_mfma_f32_16x16x32_bf16 a[0:3], a[32:35], a[152:155], a[0:3]\n"
"s_waitcnt vmcnt(11)\n"
"v_and_b32 v15, s63, v19\n"
"v_mul_f32 v12, v12, v228\n"
"v_mfma_f32_16x16x32_bf16 v[88:91], a[48:51], a[96:99], v[88:91]\n"
"v_mul_f32 v13, v13, v229\n"
"v_mul_f32 v14, v14, v230\n"
"v_mfma_f32_16x16x32_bf16 v[88:91], a[52:55], a[100:103], v[88:91]\n"
"global_load_dwordx4 a[32:35], v0, s[34:35] offset:-4096 sc1\n"
"v_mul_f32 v15, v15, v231\n"
"v_mfma_f32_16x16x32_bf16 v[116:119], a[60:63], a[100:103], v[116:119]\n"
"ds_read_b128 a[100:103], v3 offset:1024\n"
"v_cvt_pk_bf16_f32 v30, v12, v13\n"
"v_mfma_f32_16x16x32_bf16 v[116:119], a[56:59], a[96:99], v[116:119]\n"
"ds_read_b128 a[96:99], v3 offset:0\n"
"v_cvt_pk_bf16_f32 v31, v14, v15\n"
"v_mfma_f32_16x16x32_bf16 v[120:123], a[56:59], a[104:107], v[120:123]\n"
"ds_write_b64 v2, v[30:31] offset:17416\n"
"v_mfma_f32_16x16x32_bf16 v[120:123], a[60:63], a[108:111], v[120:123]\n"
"ds_read_b128 v[20:23], v2 offset:3072\n"
"v_accvgpr_read_b32 v16, a200\n"
"v_mfma_f32_16x16x32_bf16 v[92:95], a[52:55], a[108:111], v[92:95]\n"
"ds_read_b128 a[108:111], v3 offset:3072\n"
"v_accvgpr_read_b32 v17, a201\n"
"v_mfma_f32_16x16x32_bf16 v[92:95], a[48:51], a[104:107], v[92:95]\n"
"ds_read_b128 a[104:107], v3 offset:2048\n"
"v_accvgpr_read_b32 v18, a202\n"
"v_mfma_f32_16x16x32_bf16 v[96:99], a[48:51], a[112:115], v[96:99]\n"
"v_accvgpr_read_b32 v19, a203\n"
"v_lshlrev_b32 v12, 16, v16\n"
"v_mfma_f32_16x16x32_bf16 v[96:99], a[52:55], a[116:119], v[96:99]\n"
"global_load_dwordx4 a[36:39], v0, s[34:35] offset:-3072 sc1\n"
"v_and_b32 v13, s63, v16\n"
"v_mfma_f32_16x16x32_bf16 v[124:127], a[60:63], a[116:119], v[124:127]\n"
"ds_read_b128 a[116:119], v3 offset:5120\n"
"v_lshlrev_b32 v14, 16, v17\n"
"v_mfma_f32_16x16x32_bf16 v[124:127], a[56:59], a[112:115], v[124:127]\n"
"ds_read_b128 a[112:115], v3 offset:4096\n"
"v_and_b32 v15, s63, v17\n"
"v_mfma_f32_16x16x32_bf16 v[128:131], a[56:59], a[120:123], v[128:131]\n"
"v_mul_f32 v12, v12, v148\n"
"v_mul_f32 v13, v13, v149\n"
"v_mfma_f32_16x16x32_bf16 v[128:131], a[60:63], a[124:127], v[128:131]\n"
"v_mul_f32 v14, v14, v150\n"
"v_mul_f32 v15, v15, v151\n"
"v_mfma_f32_16x16x32_bf16 v[100:103], a[52:55], a[124:127], v[100:103]\n"
"ds_read_b128 a[124:127], v3 offset:7168\n"
"v_cvt_pk_bf16_f32 v24, v12, v13\n"
"v_mfma_f32_16x16x32_bf16 v[100:103], a[48:51], a[120:123], v[100:103]\n"
"ds_read_b128 a[120:123], v3 offset:6144\n"
"v_cvt_pk_bf16_f32 v25, v14, v15\n"
"v_mfma_f32_16x16x32_bf16 v[104:107], a[48:51], a[128:131], v[104:107]\n"
"s_waitcnt lgkmcnt(6)\n"
"global_store_dwordx4 v0, v[20:23], s[96:97] offset:-1024 sc1\n"
"ds_write_b64 v2, v[24:25] offset:18432\n"
"v_lshlrev_b32 v12, 16, v18\n"
"v_mfma_f32_16x16x32_bf16 v[104:107], a[52:55], a[132:135], v[104:107]\n"
"global_load_dwordx4 a[40:43], v0, s[34:35] offset:-2048 sc1\n"
"v_and_b32 v13, s63, v18\n"
"v_mfma_f32_16x16x32_bf16 v[132:135], a[60:63], a[132:135], v[132:135]\n"
"ds_read_b128 a[132:135], v3 offset:9216\n"
"v_lshlrev_b32 v14, 16, v19\n"
"v_mfma_f32_16x16x32_bf16 v[132:135], a[56:59], a[128:131], v[132:135]\n"
"ds_read_b128 a[128:131], v3 offset:8192\n"
"v_and_b32 v15, s63, v19\n"
"v_mfma_f32_16x16x32_bf16 v[136:139], a[56:59], a[136:139], v[136:139]\n"
"v_mul_f32 v12, v12, v176\n"
"v_mul_f32 v13, v13, v177\n"
"v_mfma_f32_16x16x32_bf16 v[136:139], a[60:63], a[140:143], v[136:139]\n"
"v_mul_f32 v14, v14, v178\n"
"v_mul_f32 v15, v15, v179\n"
"v_mfma_f32_16x16x32_bf16 v[108:111], a[52:55], a[140:143], v[108:111]\n"
"ds_read_b128 a[140:143], v3 offset:11264\n"
"v_cvt_pk_bf16_f32 v26, v12, v13\n"
"v_mfma_f32_16x16x32_bf16 v[108:111], a[48:51], a[136:139], v[108:111]\n"
"ds_read_b128 a[136:139], v3 offset:10240\n"
"v_cvt_pk_bf16_f32 v27, v14, v15\n"
"v_mfma_f32_16x16x32_bf16 v[112:115], a[48:51], a[144:147], v[112:115]\n"
"ds_write_b64 v2, v[26:27] offset:18440\n"
"v_mfma_f32_16x16x32_bf16 v[112:115], a[52:55], a[148:151], v[112:115]\n"
"global_load_dwordx4 a[44:47], v0, s[34:35] offset:-1024 sc1\n"
"v_accvgpr_read_b32 v16, a204\n"
"v_mfma_f32_16x16x32_bf16 v[140:143], a[60:63], a[148:151], v[140:143]\n"
"ds_read_b128 a[148:151], v3 offset:13312\n"
"v_accvgpr_read_b32 v17, a205\n"
"v_mfma_f32_16x16x32_bf16 v[140:143], a[56:59], a[144:147], v[140:143]\n"
"ds_read_b128 a[144:147], v3 offset:12288\n"
"v_accvgpr_read_b32 v18, a206\n"
"v_mfma_f32_16x16x32_bf16 a[12:15], a[56:59], a[152:155], a[12:15]\n"
"v_accvgpr_read_b32 v19, a207\n"
"v_lshlrev_b32 v12, 16, v16\n"
"v_mfma_f32_16x16x32_bf16 a[12:15], a[60:63], a[156:159], a[12:15]\n"
"v_and_b32 v13, s63, v16\n"
"v_lshlrev_b32 v14, 16, v17\n"
"v_mfma_f32_16x16x32_bf16 a[8:11], a[52:55], a[156:159], a[8:11]\n"
"ds_read_b128 a[156:159], v3 offset:15360\n"
"v_and_b32 v15, s63, v17\n"
"v_mfma_f32_16x16x32_bf16 a[8:11], a[48:51], a[152:155], a[8:11]\n"
"s_waitcnt vmcnt(11) lgkmcnt(13)\n"
"ds_read_b128 a[152:155], v3 offset:14336\n"
"v_mul_f32 v12, v12, v204\n"
"v_mfma_f32_16x16x32_bf16 v[32:35], a[64:67], a[96:99], v[32:35]\n"
"ds_read_b128 v[20:23], v2 offset:6144\n"
"v_mul_f32 v13, v13, v205\n"
"v_mfma_f32_16x16x32_bf16 v[32:35], a[68:71], a[100:103], v[32:35]\n"
"global_load_dwordx4 a[48:51], v0, s[34:35] offset:0 sc1\n"
"v_mul_f32 v14, v14, v206\n"
"v_mfma_f32_16x16x32_bf16 v[60:63], a[76:79], a[100:103], v[60:63]\n"
"v_mul_f32 v15, v15, v207\n"
"v_cvt_pk_bf16_f32 v28, v12, v13\n"
"v_mfma_f32_16x16x32_bf16 v[60:63], a[72:75], a[96:99], v[60:63]\n"
"v_cvt_pk_bf16_f32 v29, v14, v15\n"
"v_mfma_f32_16x16x32_bf16 v[64:67], a[72:75], a[104:107], v[64:67]\n"
"ds_write_b64 v2, v[28:29] offset:19456\n"
"v_mfma_f32_16x16x32_bf16 v[64:67], a[76:79], a[108:111], v[64:67]\n"
"v_lshlrev_b32 v12, 16, v18\n"
"v_and_b32 v13, s63, v18\n"
"v_mfma_f32_16x16x32_bf16 v[36:39], a[68:71], a[108:111], v[36:39]\n"
"v_lshlrev_b32 v14, 16, v19\n"
"v_and_b32 v15, s63, v19\n"
"v_mfma_f32_16x16x32_bf16 v[36:39], a[64:67], a[104:107], v[36:39]\n"
"s_waitcnt lgkmcnt(12)\n"
"v_mul_f32 v12, v12, v232\n"
"v_mul_f32 v13, v13, v233\n"
"v_mfma_f32_16x16x32_bf16 v[40:43], a[64:67], a[112:115], v[40:43]\n"
"v_mul_f32 v14, v14, v234\n"
"v_mul_f32 v15, v15, v235\n"
"v_mfma_f32_16x16x32_bf16 v[40:43], a[68:71], a[116:119], v[40:43]\n"
"global_load_dwordx4 a[52:55], v0, s[34:35] offset:1024 sc1\n"
"v_mfma_f32_16x16x32_bf16 v[68:71], a[76:79], a[116:119], v[68:71]\n"
"v_cvt_pk_bf16_f32 v30, v12, v13\n"
"v_mfma_f32_16x16x32_bf16 v[68:71], a[72:75], a[112:115], v[68:71]\n"
"v_cvt_pk_bf16_f32 v31, v14, v15\n"
"v_mfma_f32_16x16x32_bf16 v[72:75], a[72:75], a[120:123], v[72:75]\n"
"s_waitcnt lgkmcnt(1)\n"
"global_store_dwordx4 v0, v[20:23], s[96:97] offset:2048 sc1\n"
"ds_write_b64 v2, v[30:31] offset:19464\n"
"v_accvgpr_read_b32 v16, a208\n"
"v_mfma_f32_16x16x32_bf16 v[72:75], a[76:79], a[124:127], v[72:75]\n"
"v_accvgpr_read_b32 v17, a209\n"
"v_accvgpr_read_b32 v18, a210\n"
"v_mfma_f32_16x16x32_bf16 v[44:47], a[68:71], a[124:127], v[44:47]\n"
"v_accvgpr_read_b32 v19, a211\n"
"v_lshlrev_b32 v12, 16, v16\n"
"v_mfma_f32_16x16x32_bf16 v[44:47], a[64:67], a[120:123], v[44:47]\n"
"v_and_b32 v13, s63, v16\n"
"v_lshlrev_b32 v14, 16, v17\n"
"v_mfma_f32_16x16x32_bf16 v[48:51], a[64:67], a[128:131], v[48:51]\n"
"v_and_b32 v15, s63, v17\n"
"v_mul_f32 v12, v12, v152\n"
"v_mfma_f32_16x16x32_bf16 v[48:51], a[68:71], a[132:135], v[48:51]\n"
"global_load_dwordx4 a[56:59], v0, s[34:35] offset:2048 sc1\n"
"v_mul_f32 v13, v13, v153\n"
"v_mfma_f32_16x16x32_bf16 v[76:79], a[76:79], a[132:135], v[76:79]\n"
"v_mul_f32 v14, v14, v154\n"
"v_mfma_f32_16x16x32_bf16 v[76:79], a[72:75], a[128:131], v[76:79]\n"
"v_mul_f32 v15, v15, v155\n"
"v_cvt_pk_bf16_f32 v24, v12, v13\n"
"v_mfma_f32_16x16x32_bf16 v[80:83], a[72:75], a[136:139], v[80:83]\n"
"v_cvt_pk_bf16_f32 v25, v14, v15\n"
"v_mfma_f32_16x16x32_bf16 v[80:83], a[76:79], a[140:143], v[80:83]\n"
"ds_write_b64 v2, v[24:25] offset:20480\n"
"v_mfma_f32_16x16x32_bf16 v[52:55], a[68:71], a[140:143], v[52:55]\n"
"v_lshlrev_b32 v12, 16, v18\n"
"v_and_b32 v13, s63, v18\n"
"v_mfma_f32_16x16x32_bf16 v[52:55], a[64:67], a[136:139], v[52:55]\n"
"v_lshlrev_b32 v14, 16, v19\n"
"v_mfma_f32_16x16x32_bf16 v[56:59], a[64:67], a[144:147], v[56:59]\n"
"v_and_b32 v15, s63, v19\n"
"v_mul_f32 v12, v12, v180\n"
"v_mfma_f32_16x16x32_bf16 v[56:59], a[68:71], a[148:151], v[56:59]\n"
"global_load_dwordx4 a[60:63], v0, s[34:35] offset:3072 sc1\n"
"v_mul_f32 v13, v13, v181\n"
"v_mfma_f32_16x16x32_bf16 v[84:87], a[76:79], a[148:151], v[84:87]\n"
"v_mul_f32 v14, v14, v182\n"
"v_mul_f32 v15, v15, v183\n"
"v_mfma_f32_16x16x32_bf16 v[84:87], a[72:75], a[144:147], v[84:87]\n"
"v_cvt_pk_bf16_f32 v26, v12, v13\n"
"v_cvt_pk_bf16_f32 v27, v14, v15\n"
"v_mfma_f32_16x16x32_bf16 a[4:7], a[72:75], a[152:155], a[4:7]\n"
"s_add_u32 s34, s34, 8192\n"
"ds_write_b64 v2, v[26:27] offset:20488\n"
"v_mfma_f32_16x16x32_bf16 a[4:7], a[76:79], a[156:159], a[4:7]\n"
"ds_read_b128 v[20:23], v2 offset:9216\n"
"v_mfma_f32_16x16x32_bf16 a[0:3], a[68:71], a[156:159], a[0:3]\n"
"v_accvgpr_read_b32 v16, a212\n"
"v_accvgpr_read_b32 v17, a213\n"
"v_mfma_f32_16x16x32_bf16 a[0:3], a[64:67], a[152:155], a[0:3]\n"
"s_waitcnt vmcnt(10)\n"
"v_accvgpr_read_b32 v18, a214\n"
"v_mfma_f32_16x16x32_bf16 v[88:91], a[80:83], a[96:99], v[88:91]\n"
"v_accvgpr_read_b32 v19, a215\n"
"v_lshlrev_b32 v12, 16, v16\n"
"v_mfma_f32_16x16x32_bf16 v[88:91], a[84:87], a[100:103], v[88:91]\n"
"global_load_dwordx4 a[64:67], v0, s[34:35] offset:-4096 sc1\n"
"v_and_b32 v13, s63, v16\n"
"v_mfma_f32_16x16x32_bf16 v[116:119], a[92:95], a[100:103], v[116:119]\n"
"ds_read_b128 a[100:103], v4 offset:1024\n"
"v_lshlrev_b32 v14, 16, v17\n"
"v_mfma_f32_16x16x32_bf16 v[116:119], a[88:91], a[96:99], v[116:119]\n"
"ds_read_b128 a[96:99], v4 offset:0\n"
"v_and_b32 v15, s63, v17\n"
"v_mfma_f32_16x16x32_bf16 v[120:123], a[88:91], a[104:107], v[120:123]\n"
"v_mul_f32 v12, v12, v208\n"
"v_mul_f32 v13, v13, v209\n"
"v_mfma_f32_16x16x32_bf16 v[120:123], a[92:95], a[108:111], v[120:123]\n"
"v_mul_f32 v14, v14, v210\n"
"v_mul_f32 v15, v15, v211\n"
"v_mfma_f32_16x16x32_bf16 v[92:95], a[84:87], a[108:111], v[92:95]\n"
"ds_read_b128 a[108:111], v4 offset:3072\n"
"v_cvt_pk_bf16_f32 v28, v12, v13\n"
"v_mfma_f32_16x16x32_bf16 v[92:95], a[80:83], a[104:107], v[92:95]\n"
"ds_read_b128 a[104:107], v4 offset:2048\n"
"v_cvt_pk_bf16_f32 v29, v14, v15\n"
"v_mfma_f32_16x16x32_bf16 v[96:99], a[80:83], a[112:115], v[96:99]\n"
"s_waitcnt lgkmcnt(4)\n"
"global_store_dwordx4 v0, v[20:23], s[98:99] offset:-3072 sc1\n"
"ds_write_b64 v2, v[28:29] offset:21504\n"
"v_lshlrev_b32 v12, 16, v18\n"
"v_mfma_f32_16x16x32_bf16 v[96:99], a[84:87], a[116:119], v[96:99]\n"
"global_load_dwordx4 a[68:71], v0, s[34:35] offset:-3072 sc1\n"
"v_and_b32 v13, s63, v18\n"
"v_mfma_f32_16x16x32_bf16 v[124:127], a[92:95], a[116:119], v[124:127]\n"
"ds_read_b128 a[116:119], v4 offset:5120\n"
"v_lshlrev_b32 v14, 16, v19\n"
"v_mfma_f32_16x16x32_bf16 v[124:127], a[88:91], a[112:115], v[124:127]\n"
"ds_read_b128 a[112:115], v4 offset:4096\n"
"v_and_b32 v15, s63, v19\n"
"v_mfma_f32_16x16x32_bf16 v[128:131], a[88:91], a[120:123], v[128:131]\n"
"v_mul_f32 v12, v12, v236\n"
"v_mul_f32 v13, v13, v237\n"
"v_mfma_f32_16x16x32_bf16 v[128:131], a[92:95], a[124:127], v[128:131]\n"
"v_mul_f32 v14, v14, v238\n"
"v_mul_f32 v15, v15, v239\n"
"v_mfma_f32_16x16x32_bf16 v[100:103], a[84:87], a[124:127], v[100:103]\n"
"ds_read_b128 a[124:127], v4 offset:7168\n"
"v_cvt_pk_bf16_f32 v30, v12, v13\n"
"v_mfma_f32_16x16x32_bf16 v[100:103], a[80:83], a[120:123], v[100:103]\n"
"ds_read_b128 a[120:123], v4 offset:6144\n"
"v_cvt_pk_bf16_f32 v31, v14, v15\n"
"v_mfma_f32_16x16x32_bf16 v[104:107], a[80:83], a[128:131], v[104:107]\n"
"ds_write_b64 v2, v[30:31] offset:21512\n"
"v_mfma_f32_16x16x32_bf16 v[104:107], a[84:87], a[132:135], v[104:107]\n"
"global_load_dwordx4 a[72:75], v0, s[34:35] offset:-2048 sc1\n"
"v_accvgpr_read_b32 v16, a216\n"
"v_mfma_f32_16x16x32_bf16 v[132:135], a[92:95], a[132:135], v[132:135]\n"
"ds_read_b128 a[132:135], v4 offset:9216\n"
"v_accvgpr_read_b32 v17, a217\n"
"v_mfma_f32_16x16x32_bf16 v[132:135], a[88:91], a[128:131], v[132:135]\n"
"ds_read_b128 a[128:131], v4 offset:8192\n"
"v_accvgpr_read_b32 v18, a218\n"
"v_mfma_f32_16x16x32_bf16 v[136:139], a[88:91], a[136:139], v[136:139]\n"
"v_accvgpr_read_b32 v19, a219\n"
"v_lshlrev_b32 v12, 16, v16\n"
"v_mfma_f32_16x16x32_bf16 v[136:139], a[92:95], a[140:143], v[136:139]\n"
"v_and_b32 v13, s63, v16\n"
"v_lshlrev_b32 v14, 16, v17\n"
"v_mfma_f32_16x16x32_bf16 v[108:111], a[84:87], a[140:143], v[108:111]\n"
"ds_read_b128 a[140:143], v4 offset:11264\n"
"v_and_b32 v15, s63, v17\n"
"v_mfma_f32_16x16x32_bf16 v[108:111], a[80:83], a[136:139], v[108:111]\n"
"ds_read_b128 a[136:139], v4 offset:10240\n"
"v_mul_f32 v12, v12, v156\n"
"v_mfma_f32_16x16x32_bf16 v[112:115], a[80:83], a[144:147], v[112:115]\n"
"ds_read_b128 v[20:23], v2 offset:12288\n"
"v_mul_f32 v13, v13, v157\n"
"v_mfma_f32_16x16x32_bf16 v[112:115], a[84:87], a[148:151], v[112:115]\n"
"global_load_dwordx4 a[76:79], v0, s[34:35] offset:-1024 sc1\n"
"v_mul_f32 v14, v14, v158\n"
"v_mfma_f32_16x16x32_bf16 v[140:143], a[92:95], a[148:151], v[140:143]\n"
"ds_read_b128 a[148:151], v4 offset:13312\n"
"v_mul_f32 v15, v15, v159\n"
"v_mfma_f32_16x16x32_bf16 v[140:143], a[88:91], a[144:147], v[140:143]\n"
"ds_read_b128 a[144:147], v4 offset:12288\n"
"v_cvt_pk_bf16_f32 v24, v12, v13\n"
"v_mfma_f32_16x16x32_bf16 a[12:15], a[88:91], a[152:155], a[12:15]\n"
"v_cvt_pk_bf16_f32 v25, v14, v15\n"
"v_mfma_f32_16x16x32_bf16 a[12:15], a[92:95], a[156:159], a[12:15]\n"
"ds_write_b64 v2, v[24:25] offset:22528\n"
"v_mfma_f32_16x16x32_bf16 a[8:11], a[84:87], a[156:159], a[8:11]\n"
"ds_read_b128 a[156:159], v4 offset:15360\n"
"v_lshlrev_b32 v12, 16, v18\n"
"v_mfma_f32_16x16x32_bf16 a[8:11], a[80:83], a[152:155], a[8:11]\n"
"s_waitcnt vmcnt(10) lgkmcnt(15)\n"
"ds_read_b128 a[152:155], v4 offset:14336\n"
"v_and_b32 v13, s63, v18\n"
"v_mfma_f32_16x16x32_bf16 v[32:35], a[32:35], a[96:99], v[32:35]\n"
"v_lshlrev_b32 v14, 16, v19\n"
"v_and_b32 v15, s63, v19\n"
"v_mfma_f32_16x16x32_bf16 v[32:35], a[36:39], a[100:103], v[32:35]\n"
"global_load_dwordx4 a[80:83], v0, s[34:35] offset:0 sc1\n"
"v_mfma_f32_16x16x32_bf16 v[60:63], a[44:47], a[100:103], v[60:63]\n"
"s_add_u32 s65, s65, 1\n"
"v_mul_f32 v12, v12, v184\n"
"v_mfma_f32_16x16x32_bf16 v[60:63], a[40:43], a[96:99], v[60:63]\n"
"v_mul_f32 v13, v13, v185\n"
"v_mfma_f32_16x16x32_bf16 v[64:67], a[40:43], a[104:107], v[64:67]\n"
"s_waitcnt lgkmcnt(5)\n"
"global_store_dwordx4 v0, v[20:23], s[98:99] offset:0 sc1\n"
"v_mul_f32 v14, v14, v186\n"
"v_mul_f32 v15, v15, v187\n"
"v_cvt_pk_bf16_f32 v26, v12, v13\n"
"v_cvt_pk_bf16_f32 v27, v14, v15\n"
"v_mfma_f32_16x16x32_bf16 v[64:67], a[44:47], a[108:111], v[64:67]\n"
"ds_write_b64 v2, v[26:27] offset:22536\n"
"v_mfma_f32_16x16x32_bf16 v[36:39], a[36:39], a[108:111], v[36:39]\n"
"v_accvgpr_read_b32 v16, a220\n"
"v_accvgpr_read_b32 v17, a221\n"
"v_mfma_f32_16x16x32_bf16 v[36:39], a[32:35], a[104:107], v[36:39]\n"
"v_accvgpr_read_b32 v18, a222\n"
"v_accvgpr_read_b32 v19, a223\n"
"v_mfma_f32_16x16x32_bf16 v[40:43], a[32:35], a[112:115], v[40:43]\n"
"v_lshlrev_b32 v12, 16, v16\n"
"v_and_b32 v13, s63, v16\n"
"v_mfma_f32_16x16x32_bf16 v[40:43], a[36:39], a[116:119], v[40:43]\n"
"global_load_dwordx4 a[84:87], v0, s[34:35] offset:1024 sc1\n"
"v_lshlrev_b32 v14, 16, v17\n"
"v_mfma_f32_16x16x32_bf16 v[68:71], a[44:47], a[116:119], v[68:71]\n"
"v_and_b32 v15, s63, v17\n"
"v_mul_f32 v12, v12, v212\n"
"v_mfma_f32_16x16x32_bf16 v[68:71], a[40:43], a[112:115], v[68:71]\n"
"v_mul_f32 v13, v13, v213\n"
"v_mul_f32 v14, v14, v214\n"
"v_mfma_f32_16x16x32_bf16 v[72:75], a[40:43], a[120:123], v[72:75]\n"
"v_mul_f32 v15, v15, v215\n"
"v_cvt_pk_bf16_f32 v28, v12, v13\n"
"v_mfma_f32_16x16x32_bf16 v[72:75], a[44:47], a[124:127], v[72:75]\n"
"v_cvt_pk_bf16_f32 v29, v14, v15\n"
"v_mfma_f32_16x16x32_bf16 v[44:47], a[36:39], a[124:127], v[44:47]\n"
"ds_write_b64 v2, v[28:29] offset:23552\n"
"v_mfma_f32_16x16x32_bf16 v[44:47], a[32:35], a[120:123], v[44:47]\n"
"v_lshlrev_b32 v12, 16, v18\n"
"v_and_b32 v13, s63, v18\n"
"v_mfma_f32_16x16x32_bf16 v[48:51], a[32:35], a[128:131], v[48:51]\n"
"v_lshlrev_b32 v14, 16, v19\n"
"v_and_b32 v15, s63, v19\n"
"v_mfma_f32_16x16x32_bf16 v[48:51], a[36:39], a[132:135], v[48:51]\n"
"global_load_dwordx4 a[88:91], v0, s[34:35] offset:2048 sc1\n"
"v_mul_f32 v12, v12, v240\n"
"v_mfma_f32_16x16x32_bf16 v[76:79], a[44:47], a[132:135], v[76:79]\n"
"v_mul_f32 v13, v13, v241\n"
"v_mul_f32 v14, v14, v242\n"
"v_mfma_f32_16x16x32_bf16 v[76:79], a[40:43], a[128:131], v[76:79]\n"
"v_mul_f32 v15, v15, v243\n"
"v_mfma_f32_16x16x32_bf16 v[80:83], a[40:43], a[136:139], v[80:83]\n"
"v_cvt_pk_bf16_f32 v30, v12, v13\n"
"v_cvt_pk_bf16_f32 v31, v14, v15\n"
"v_mfma_f32_16x16x32_bf16 v[80:83], a[44:47], a[140:143], v[80:83]\n"
"ds_write_b64 v2, v[30:31] offset:23560\n"
"v_mfma_f32_16x16x32_bf16 v[52:55], a[36:39], a[140:143], v[52:55]\n"
"v_accvgpr_read_b32 v16, a224\n"
"v_mfma_f32_16x16x32_bf16 v[52:55], a[32:35], a[136:139], v[52:55]\n"
"s_waitcnt lgkmcnt(3)\n"
"v_accvgpr_read_b32 v17, a225\n"
"v_accvgpr_read_b32 v18, a226\n"
"v_mfma_f32_16x16x32_bf16 v[56:59], a[32:35], a[144:147], v[56:59]\n"
"v_accvgpr_read_b32 v19, a227\n"
"v_mfma_f32_16x16x32_bf16 v[56:59], a[36:39], a[148:151], v[56:59]\n"
"global_load_dwordx4 a[92:95], v0, s[34:35] offset:3072 sc1\n"
"v_lshlrev_b32 v12, 16, v16\n"
"v_mfma_f32_16x16x32_bf16 v[84:87], a[44:47], a[148:151], v[84:87]\n"
"v_and_b32 v13, s63, v16\n"
"v_lshlrev_b32 v14, 16, v17\n"
"v_mfma_f32_16x16x32_bf16 v[84:87], a[40:43], a[144:147], v[84:87]\n"
"v_and_b32 v15, s63, v17\n"
"v_mfma_f32_16x16x32_bf16 a[4:7], a[40:43], a[152:155], a[4:7]\n"
"s_add_u32 s34, s34, 8192\n"
"v_mul_f32 v12, v12, v160\n"
"v_mul_f32 v13, v13, v161\n"
"v_mfma_f32_16x16x32_bf16 a[4:7], a[44:47], a[156:159], a[4:7]\n"
"v_mul_f32 v14, v14, v162\n"
"v_mul_f32 v15, v15, v163\n"
"v_mfma_f32_16x16x32_bf16 a[0:3], a[36:39], a[156:159], a[0:3]\n"
"v_cvt_pk_bf16_f32 v24, v12, v13\n"
"v_cvt_pk_bf16_f32 v25, v14, v15\n"
"v_mfma_f32_16x16x32_bf16 a[0:3], a[32:35], a[152:155], a[0:3]\n"
"s_waitcnt vmcnt(10)\n"
"ds_write_b64 v2, v[24:25] offset:24576\n"
"v_mfma_f32_16x16x32_bf16 v[88:91], a[48:51], a[96:99], v[88:91]\n"
"v_lshlrev_b32 v12, 16, v18\n"
"v_mfma_f32_16x16x32_bf16 v[88:91], a[52:55], a[100:103], v[88:91]\n"
"global_load_dwordx4 a[32:35], v0, s[34:35] offset:-4096 sc1\n"
"v_and_b32 v13, s63, v18\n"
"v_mfma_f32_16x16x32_bf16 v[116:119], a[60:63], a[100:103], v[116:119]\n"
"ds_read_b128 a[100:103], v5 offset:1024\n"
"v_lshlrev_b32 v14, 16, v19\n"
"v_mfma_f32_16x16x32_bf16 v[116:119], a[56:59], a[96:99], v[116:119]\n"
"ds_read_b128 a[96:99], v5 offset:0\n"
"v_and_b32 v15, s63, v19\n"
"v_mfma_f32_16x16x32_bf16 v[120:123], a[56:59], a[104:107], v[120:123]\n"
"v_mul_f32 v12, v12, v188\n"
"v_mul_f32 v13, v13, v189\n"
"v_mfma_f32_16x16x32_bf16 v[120:123], a[60:63], a[108:111], v[120:123]\n"
"v_mul_f32 v14, v14, v190\n"
"v_mul_f32 v15, v15, v191\n"
"v_mfma_f32_16x16x32_bf16 v[92:95], a[52:55], a[108:111], v[92:95]\n"
"ds_read_b128 a[108:111], v5 offset:3072\n"
"v_cvt_pk_bf16_f32 v26, v12, v13\n"
"v_mfma_f32_16x16x32_bf16 v[92:95], a[48:51], a[104:107], v[92:95]\n"
"ds_read_b128 a[104:107], v5 offset:2048\n"
"v_cvt_pk_bf16_f32 v27, v14, v15\n"
"v_mfma_f32_16x16x32_bf16 v[96:99], a[48:51], a[112:115], v[96:99]\n"
"ds_write_b64 v2, v[26:27] offset:24584\n"
"v_mfma_f32_16x16x32_bf16 v[96:99], a[52:55], a[116:119], v[96:99]\n"
"global_load_dwordx4 a[36:39], v0, s[34:35] offset:-3072 sc1\n"
"v_accvgpr_read_b32 v16, a228\n"
"v_mfma_f32_16x16x32_bf16 v[124:127], a[60:63], a[116:119], v[124:127]\n"
"ds_read_b128 a[116:119], v5 offset:5120\n"
"v_accvgpr_read_b32 v17, a229\n"
"v_mfma_f32_16x16x32_bf16 v[124:127], a[56:59], a[112:115], v[124:127]\n"
"ds_read_b128 a[112:115], v5 offset:4096\n"
"v_accvgpr_read_b32 v18, a230\n"
"v_mfma_f32_16x16x32_bf16 v[128:131], a[56:59], a[120:123], v[128:131]\n"
"v_accvgpr_read_b32 v19, a231\n"
"v_lshlrev_b32 v12, 16, v16\n"
"v_mfma_f32_16x16x32_bf16 v[128:131], a[60:63], a[124:127], v[128:131]\n"
"v_and_b32 v13, s63, v16\n"
"v_lshlrev_b32 v14, 16, v17\n"
"v_mfma_f32_16x16x32_bf16 v[100:103], a[52:55], a[124:127], v[100:103]\n"
"ds_read_b128 a[124:127], v5 offset:7168\n"
"v_and_b32 v15, s63, v17\n"
"v_mfma_f32_16x16x32_bf16 v[100:103], a[48:51], a[120:123], v[100:103]\n"
"ds_read_b128 a[120:123], v5 offset:6144\n"
"v_mul_f32 v12, v12, v216\n"
"v_mfma_f32_16x16x32_bf16 v[104:107], a[48:51], a[128:131], v[104:107]\n"
"v_mul_f32 v13, v13, v217\n"
"v_mul_f32 v14, v14, v218\n"
"v_mfma_f32_16x16x32_bf16 v[104:107], a[52:55], a[132:135], v[104:107]\n"
"global_load_dwordx4 a[40:43], v0, s[34:35] offset:-2048 sc1\n"
"v_mul_f32 v15, v15, v219\n"
"v_mfma_f32_16x16x32_bf16 v[132:135], a[60:63], a[132:135], v[132:135]\n"
"ds_read_b128 a[132:135], v5 offset:9216\n"
"v_cvt_pk_bf16_f32 v28, v12, v13\n"
"v_mfma_f32_16x16x32_bf16 v[132:135], a[56:59], a[128:131], v[132:135]\n"
"ds_read_b128 a[128:131], v5 offset:8192\n"
"v_cvt_pk_bf16_f32 v29, v14, v15\n"
"v_mfma_f32_16x16x32_bf16 v[136:139], a[56:59], a[136:139], v[136:139]\n"
"ds_write_b64 v2, v[28:29] offset:25600\n"
"v_mfma_f32_16x16x32_bf16 v[136:139], a[60:63], a[140:143], v[136:139]\n"
"v_lshlrev_b32 v12, 16, v18\n"
"v_and_b32 v13, s63, v18\n"
"v_mfma_f32_16x16x32_bf16 v[108:111], a[52:55], a[140:143], v[108:111]\n"
"ds_read_b128 a[140:143], v5 offset:11264\n"
"v_lshlrev_b32 v14, 16, v19\n"
"v_mfma_f32_16x16x32_bf16 v[108:111], a[48:51], a[136:139], v[108:111]\n"
"ds_read_b128 a[136:139], v5 offset:10240\n"
"v_and_b32 v15, s63, v19\n"
"v_mfma_f32_16x16x32_bf16 v[112:115], a[48:51], a[144:147], v[112:115]\n"
"v_mul_f32 v12, v12, v244\n"
"v_mul_f32 v13, v13, v245\n"
"v_mfma_f32_16x16x32_bf16 v[112:115], a[52:55], a[148:151], v[112:115]\n"
"global_load_dwordx4 a[44:47], v0, s[34:35] offset:-1024 sc1\n"
"v_mul_f32 v14, v14, v246\n"
"v_mfma_f32_16x16x32_bf16 v[140:143], a[60:63], a[148:151], v[140:143]\n"
"ds_read_b128 a[148:151], v5 offset:13312\n"
"v_mul_f32 v15, v15, v247\n"
"v_mfma_f32_16x16x32_bf16 v[140:143], a[56:59], a[144:147], v[140:143]\n"
"ds_read_b128 a[144:147], v5 offset:12288\n"
"v_cvt_pk_bf16_f32 v30, v12, v13\n"
"v_mfma_f32_16x16x32_bf16 a[12:15], a[56:59], a[152:155], a[12:15]\n"
"v_cvt_pk_bf16_f32 v31, v14, v15\n"
"v_mfma_f32_16x16x32_bf16 a[12:15], a[60:63], a[156:159], a[12:15]\n"
"global_store_dwordx4 v0, v[28:31], s[54:55] offset:-3072 sc1\n"
"ds_write_b64 v2, v[30:31] offset:25608\n"
"v_accvgpr_read_b32 v16, a232\n"
"v_mfma_f32_16x16x32_bf16 a[8:11], a[52:55], a[156:159], a[8:11]\n"
"ds_read_b128 a[156:159], v5 offset:15360\n"
"v_accvgpr_read_b32 v17, a233\n"
"v_mfma_f32_16x16x32_bf16 a[8:11], a[48:51], a[152:155], a[8:11]\n"
"s_waitcnt vmcnt(10) lgkmcnt(14)\n"
"ds_read_b128 a[152:155], v5 offset:14336\n"
"v_accvgpr_read_b32 v18, a234\n"
"v_mfma_f32_16x16x32_bf16 v[32:35], a[64:67], a[96:99], v[32:35]\n"
"v_accvgpr_read_b32 v19, a235\n"
"v_lshlrev_b32 v12, 16, v16\n"
"v_mfma_f32_16x16x32_bf16 v[32:35], a[68:71], a[100:103], v[32:35]\n"
"global_load_dwordx4 a[48:51], v0, s[34:35] offset:0 sc1\n"
"v_and_b32 v13, s63, v16\n"
"v_mfma_f32_16x16x32_bf16 v[60:63], a[76:79], a[100:103], v[60:63]\n"
"v_lshlrev_b32 v14, 16, v17\n"
"v_and_b32 v15, s63, v17\n"
"v_mfma_f32_16x16x32_bf16 v[60:63], a[72:75], a[96:99], v[60:63]\n"
"v_mul_f32 v12, v12, v164\n"
"v_mul_f32 v13, v13, v165\n"
"v_mfma_f32_16x16x32_bf16 v[64:67], a[72:75], a[104:107], v[64:67]\n"
"v_mul_f32 v14, v14, v166\n"
"v_mul_f32 v15, v15, v167\n"
"v_mfma_f32_16x16x32_bf16 v[64:67], a[76:79], a[108:111], v[64:67]\n"
"v_cvt_pk_bf16_f32 v24, v12, v13\n"
"v_cvt_pk_bf16_f32 v25, v14, v15\n"
"v_mfma_f32_16x16x32_bf16 v[36:39], a[68:71], a[108:111], v[36:39]\n"
"ds_write_b64 v2, v[24:25] offset:26624\n"
"v_mfma_f32_16x16x32_bf16 v[36:39], a[64:67], a[104:107], v[36:39]\n"
"s_waitcnt lgkmcnt(11)\n"
"v_lshlrev_b32 v12, 16, v18\n"
"v_mfma_f32_16x16x32_bf16 v[40:43], a[64:67], a[112:115], v[40:43]\n"
"v_and_b32 v13, s63, v18\n"
"v_lshlrev_b32 v14, 16, v19\n"
"v_mfma_f32_16x16x32_bf16 v[40:43], a[68:71], a[116:119], v[40:43]\n"
"global_load_dwordx4 a[52:55], v0, s[34:35] offset:1024 sc1\n"
"v_and_b32 v15, s63, v19\n"
"v_mfma_f32_16x16x32_bf16 v[68:71], a[76:79], a[116:119], v[68:71]\n"
"v_mul_f32 v12, v12, v192\n"
"v_mul_f32 v13, v13, v193\n"
"v_mfma_f32_16x16x32_bf16 v[68:71], a[72:75], a[112:115], v[68:71]\n"
"v_mul_f32 v14, v14, v194\n"
"v_mfma_f32_16x16x32_bf16 v[72:75], a[72:75], a[120:123], v[72:75]\n"
"v_mul_f32 v15, v15, v195\n"
"v_mfma_f32_16x16x32_bf16 v[72:75], a[76:79], a[124:127], v[72:75]\n"
"v_cvt_pk_bf16_f32 v26, v12, v13\n"
"v_cvt_pk_bf16_f32 v27, v14, v15\n"
"v_mfma_f32_16x16x32_bf16 v[44:47], a[68:71], a[124:127], v[44:47]\n"
"ds_write_b64 v2, v[26:27] offset:26632\n"
"v_mfma_f32_16x16x32_bf16 v[44:47], a[64:67], a[120:123], v[44:47]\n"
"s_waitcnt lgkmcnt(7)\n"
"v_accvgpr_read_b32 v16, a236\n"
"v_accvgpr_read_b32 v17, a237\n"
"v_mfma_f32_16x16x32_bf16 v[48:51], a[64:67], a[128:131], v[48:51]\n"
"v_accvgpr_read_b32 v18, a238\n"
"v_mfma_f32_16x16x32_bf16 v[48:51], a[68:71], a[132:135], v[48:51]\n"
"global_load_dwordx4 a[56:59], v0, s[34:35] offset:2048 sc1\n"
"v_accvgpr_read_b32 v19, a239\n"
"v_mfma_f32_16x16x32_bf16 v[76:79], a[76:79], a[132:135], v[76:79]\n"
"v_lshlrev_b32 v12, 16, v16\n"
"v_and_b32 v13, s63, v16\n"
"v_mfma_f32_16x16x32_bf16 v[76:79], a[72:75], a[128:131], v[76:79]\n"
"v_lshlrev_b32 v14, 16, v17\n"
"v_mfma_f32_16x16x32_bf16 v[80:83], a[72:75], a[136:139], v[80:83]\n"
"v_and_b32 v15, s63, v17\n"
"v_mfma_f32_16x16x32_bf16 v[80:83], a[76:79], a[140:143], v[80:83]\n"
"v_mul_f32 v12, v12, v220\n"
"v_mul_f32 v13, v13, v221\n"
"v_mfma_f32_16x16x32_bf16 v[52:55], a[68:71], a[140:143], v[52:55]\n"
"v_mul_f32 v14, v14, v222\n"
"v_mul_f32 v15, v15, v223\n"
"v_mfma_f32_16x16x32_bf16 v[52:55], a[64:67], a[136:139], v[52:55]\n"
"s_waitcnt lgkmcnt(2)\n"
"v_cvt_pk_bf16_f32 v28, v12, v13\n"
"v_cvt_pk_bf16_f32 v29, v14, v15\n"
"v_mfma_f32_16x16x32_bf16 v[56:59], a[64:67], a[144:147], v[56:59]\n"
"ds_write_b64 v2, v[28:29] offset:27648\n"
"v_mfma_f32_16x16x32_bf16 v[56:59], a[68:71], a[148:151], v[56:59]\n"
"global_load_dwordx4 a[60:63], v0, s[34:35] offset:3072 sc1\n"
"v_mfma_f32_16x16x32_bf16 v[84:87], a[76:79], a[148:151], v[84:87]\n"
"v_lshlrev_b32 v12, 16, v18\n"
"v_and_b32 v13, s63, v18\n"
"v_mfma_f32_16x16x32_bf16 v[84:87], a[72:75], a[144:147], v[84:87]\n"
"v_lshlrev_b32 v14, 16, v19\n"
"v_mfma_f32_16x16x32_bf16 a[4:7], a[72:75], a[152:155], a[4:7]\n"
"s_add_u32 s34, s34, 8192\n"
"v_and_b32 v15, s63, v19\n"
"v_mul_f32 v12, v12, v248\n"
"v_mfma_f32_16x16x32_bf16 a[4:7], a[76:79], a[156:159], a[4:7]\n"
"v_mul_f32 v13, v13, v249\n"
"v_mfma_f32_16x16x32_bf16 a[0:3], a[68:71], a[156:159], a[0:3]\n"
"v_mul_f32 v14, v14, v250\n"
"v_mul_f32 v15, v15, v251\n"
"v_mfma_f32_16x16x32_bf16 a[0:3], a[64:67], a[152:155], a[0:3]\n"
"s_waitcnt vmcnt(9)\n"
"v_cvt_pk_bf16_f32 v30, v12, v13\n"
"v_cvt_pk_bf16_f32 v31, v14, v15\n"
"v_mfma_f32_16x16x32_bf16 v[88:91], a[80:83], a[96:99], v[88:91]\n"
"global_store_dwordx4 v0, v[28:31], s[54:55] offset:-1024 sc1\n"
"ds_write_b64 v2, v[30:31] offset:27656\n"
"v_mfma_f32_16x16x32_bf16 v[88:91], a[84:87], a[100:103], v[88:91]\n"
"global_load_dwordx4 a[64:67], v0, s[34:35] offset:-4096 sc1\n"
"v_accvgpr_read_b32 v16, a240\n"
"v_mfma_f32_16x16x32_bf16 v[116:119], a[92:95], a[100:103], v[116:119]\n"
"ds_read_b128 a[100:103], v2 offset:1024\n"
"v_accvgpr_read_b32 v17, a241\n"
"v_mfma_f32_16x16x32_bf16 v[116:119], a[88:91], a[96:99], v[116:119]\n"
"ds_read_b128 a[96:99], v2 offset:0\n"
"v_accvgpr_read_b32 v18, a242\n"
"v_mfma_f32_16x16x32_bf16 v[120:123], a[88:91], a[104:107], v[120:123]\n"
"v_accvgpr_read_b32 v19, a243\n"
"v_lshlrev_b32 v12, 16, v16\n"
"v_mfma_f32_16x16x32_bf16 v[120:123], a[92:95], a[108:111], v[120:123]\n"
"v_and_b32 v13, s63, v16\n"
"v_lshlrev_b32 v14, 16, v17\n"
"v_mfma_f32_16x16x32_bf16 v[92:95], a[84:87], a[108:111], v[92:95]\n"
"ds_read_b128 a[108:111], v2 offset:3072\n"
"v_and_b32 v15, s63, v17\n"
"v_mfma_f32_16x16x32_bf16 v[92:95], a[80:83], a[104:107], v[92:95]\n"
"ds_read_b128 a[104:107], v2 offset:2048\n"
"v_mul_f32 v12, v12, v168\n"
"v_mfma_f32_16x16x32_bf16 v[96:99], a[80:83], a[112:115], v[96:99]\n"
"v_mul_f32 v13, v13, v169\n"
"v_mul_f32 v14, v14, v170\n"
"v_mfma_f32_16x16x32_bf16 v[96:99], a[84:87], a[116:119], v[96:99]\n"
"global_load_dwordx4 a[68:71], v0, s[34:35] offset:-3072 sc1\n"
"v_mul_f32 v15, v15, v171\n"
"v_mfma_f32_16x16x32_bf16 v[124:127], a[92:95], a[116:119], v[124:127]\n"
"ds_read_b128 a[116:119], v2 offset:5120\n"
"v_cvt_pk_bf16_f32 v24, v12, v13\n"
"v_mfma_f32_16x16x32_bf16 v[124:127], a[88:91], a[112:115], v[124:127]\n"
"ds_read_b128 a[112:115], v2 offset:4096\n"
"v_cvt_pk_bf16_f32 v25, v14, v15\n"
"v_mfma_f32_16x16x32_bf16 v[128:131], a[88:91], a[120:123], v[128:131]\n"
"ds_write_b64 v2, v[24:25] offset:28672\n"
"v_mfma_f32_16x16x32_bf16 v[128:131], a[92:95], a[124:127], v[128:131]\n"
"v_lshlrev_b32 v12, 16, v18\n"
"v_and_b32 v13, s63, v18\n"
"v_mfma_f32_16x16x32_bf16 v[100:103], a[84:87], a[124:127], v[100:103]\n"
"ds_read_b128 a[124:127], v2 offset:7168\n"
"v_lshlrev_b32 v14, 16, v19\n"
"v_mfma_f32_16x16x32_bf16 v[100:103], a[80:83], a[120:123], v[100:103]\n"
"ds_read_b128 a[120:123], v2 offset:6144\n"
"v_and_b32 v15, s63, v19\n"
"v_mfma_f32_16x16x32_bf16 v[104:107], a[80:83], a[128:131], v[104:107]\n"
"v_mul_f32 v12, v12, v196\n"
"v_mul_f32 v13, v13, v197\n"
"v_mfma_f32_16x16x32_bf16 v[104:107], a[84:87], a[132:135], v[104:107]\n"
"global_load_dwordx4 a[72:75], v0, s[34:35] offset:-2048 sc1\n"
"v_mul_f32 v14, v14, v198\n"
"v_mfma_f32_16x16x32_bf16 v[132:135], a[92:95], a[132:135], v[132:135]\n"
"ds_read_b128 a[132:135], v2 offset:9216\n"
"v_mul_f32 v15, v15, v199\n"
"v_mfma_f32_16x16x32_bf16 v[132:135], a[88:91], a[128:131], v[132:135]\n"
"ds_read_b128 a[128:131], v2 offset:8192\n"
"v_cvt_pk_bf16_f32 v26, v12, v13\n"
"v_mfma_f32_16x16x32_bf16 v[136:139], a[88:91], a[136:139], v[136:139]\n"
"v_cvt_pk_bf16_f32 v27, v14, v15\n"
"v_mfma_f32_16x16x32_bf16 v[136:139], a[92:95], a[140:143], v[136:139]\n"
"global_store_dwordx4 v0, v[24:27], s[54:55] offset:0 sc1\n"
"ds_write_b64 v2, v[26:27] offset:28680\n"
"v_accvgpr_read_b32 v16, a248\n"
"v_mfma_f32_16x16x32_bf16 v[108:111], a[84:87], a[140:143], v[108:111]\n"
"ds_read_b128 a[140:143], v2 offset:11264\n"
"v_accvgpr_read_b32 v17, a249\n"
"v_mfma_f32_16x16x32_bf16 v[108:111], a[80:83], a[136:139], v[108:111]\n"
"ds_read_b128 a[136:139], v2 offset:10240\n"
"v_accvgpr_read_b32 v18, a250\n"
"v_mfma_f32_16x16x32_bf16 v[112:115], a[80:83], a[144:147], v[112:115]\n"
"v_accvgpr_read_b32 v19, a251\n"
"v_lshlrev_b32 v12, 16, v16\n"
"v_mfma_f32_16x16x32_bf16 v[112:115], a[84:87], a[148:151], v[112:115]\n"
"global_load_dwordx4 a[76:79], v0, s[34:35] offset:-1024 sc1\n"
"v_and_b32 v13, s63, v16\n"
"v_mfma_f32_16x16x32_bf16 v[140:143], a[92:95], a[148:151], v[140:143]\n"
"ds_read_b128 a[148:151], v2 offset:13312\n"
"v_lshlrev_b32 v14, 16, v17\n"
"v_mfma_f32_16x16x32_bf16 v[140:143], a[88:91], a[144:147], v[140:143]\n"
"ds_read_b128 a[144:147], v2 offset:12288\n"
"v_and_b32 v15, s63, v17\n"
"v_mfma_f32_16x16x32_bf16 a[12:15], a[88:91], a[152:155], a[12:15]\n"
"v_accvgpr_read_b32 v10, a16\n"
"v_accvgpr_read_b32 v11, a17\n"
"v_mfma_f32_16x16x32_bf16 a[12:15], a[92:95], a[156:159], a[12:15]\n"
"v_mul_f32 v12, v12, v10\n"
"v_mul_f32 v13, v13, v11\n"
"v_mfma_f32_16x16x32_bf16 a[8:11], a[84:87], a[156:159], a[8:11]\n"
"ds_read_b128 a[156:159], v2 offset:15360\n"
"v_accvgpr_read_b32 v10, a18\n"
"v_mfma_f32_16x16x32_bf16 a[8:11], a[80:83], a[152:155], a[8:11]\n"
"s_waitcnt vmcnt(11) lgkmcnt(13)\n"
"ds_read_b128 a[152:155], v2 offset:14336\n"
"v_accvgpr_read_b32 v11, a19\n"
"v_mfma_f32_16x16x32_bf16 v[144:147], a[32:35], a[96:99], v[144:147]\n"
"v_mul_f32 v14, v14, v10\n"
"v_mul_f32 v15, v15, v11\n"
"v_mfma_f32_16x16x32_bf16 v[144:147], a[36:39], a[100:103], v[144:147]\n"
"global_load_dwordx4 a[80:83], v0, s[34:35] offset:0 sc1\n"
"v_cvt_pk_bf16_f32 v28, v12, v13\n"
"v_mfma_f32_16x16x32_bf16 v[172:175], a[44:47], a[100:103], v[172:175]\n"
"v_cvt_pk_bf16_f32 v29, v14, v15\n"
"v_mfma_f32_16x16x32_bf16 v[172:175], a[40:43], a[96:99], v[172:175]\n"
"ds_write_b64 v2, v[28:29] offset:30720\n"
"v_mfma_f32_16x16x32_bf16 v[176:179], a[40:43], a[104:107], v[176:179]\n"
"v_lshlrev_b32 v12, 16, v18\n"
"v_and_b32 v13, s63, v18\n"
"v_mfma_f32_16x16x32_bf16 v[176:179], a[44:47], a[108:111], v[176:179]\n"
"v_lshlrev_b32 v14, 16, v19\n"
"v_mfma_f32_16x16x32_bf16 v[148:151], a[36:39], a[108:111], v[148:151]\n"
"v_and_b32 v15, s63, v19\n"
"v_accvgpr_read_b32 v10, a20\n"
"v_mfma_f32_16x16x32_bf16 v[148:151], a[32:35], a[104:107], v[148:151]\n"
"s_waitcnt lgkmcnt(10)\n"
"v_accvgpr_read_b32 v11, a21\n"
"v_mfma_f32_16x16x32_bf16 v[152:155], a[32:35], a[112:115], v[152:155]\n"
"v_mul_f32 v12, v12, v10\n"
"v_mul_f32 v13, v13, v11\n"
"v_mfma_f32_16x16x32_bf16 v[152:155], a[36:39], a[116:119], v[152:155]\n"
"global_load_dwordx4 a[84:87], v0, s[34:35] offset:1024 sc1\n"
"v_accvgpr_read_b32 v10, a22\n"
"v_mfma_f32_16x16x32_bf16 v[180:183], a[44:47], a[116:119], v[180:183]\n"
"v_accvgpr_read_b32 v11, a23\n"
"v_mfma_f32_16x16x32_bf16 v[180:183], a[40:43], a[112:115], v[180:183]\n"
"v_mul_f32 v14, v14, v10\n"
"v_mul_f32 v15, v15, v11\n"
"v_mfma_f32_16x16x32_bf16 v[184:187], a[40:43], a[120:123], v[184:187]\n"
"v_cvt_pk_bf16_f32 v30, v12, v13\n"
"v_mfma_f32_16x16x32_bf16 v[184:187], a[44:47], a[124:127], v[184:187]\n"
"v_cvt_pk_bf16_f32 v31, v14, v15\n"
"v_mfma_f32_16x16x32_bf16 v[156:159], a[36:39], a[124:127], v[156:159]\n"
"global_store_dwordx4 v0, v[28:31], s[54:55] offset:2048 sc1\n"
"ds_write_b64 v2, v[30:31] offset:30728\n"
"v_accvgpr_read_b32 v16, a244\n"
"v_mfma_f32_16x16x32_bf16 v[156:159], a[32:35], a[120:123], v[156:159]\n"
"s_waitcnt lgkmcnt(6)\n"
"v_accvgpr_read_b32 v17, a245\n"
"v_accvgpr_read_b32 v18, a246\n"
"v_mfma_f32_16x16x32_bf16 v[160:163], a[32:35], a[128:131], v[160:163]\n"
"v_accvgpr_read_b32 v19, a247\n"
"v_lshlrev_b32 v12, 16, v16\n"
"v_mfma_f32_16x16x32_bf16 v[160:163], a[36:39], a[132:135], v[160:163]\n"
"global_load_dwordx4 a[88:91], v0, s[34:35] offset:2048 sc1\n"
"v_and_b32 v13, s63, v16\n"
"v_mfma_f32_16x16x32_bf16 v[188:191], a[44:47], a[132:135], v[188:191]\n"
"v_lshlrev_b32 v14, 16, v17\n"
"v_and_b32 v15, s63, v17\n"
"v_mfma_f32_16x16x32_bf16 v[188:191], a[40:43], a[128:131], v[188:191]\n"
"v_mul_f32 v12, v12, v224\n"
"v_mul_f32 v13, v13, v225\n"
"v_mfma_f32_16x16x32_bf16 v[192:195], a[40:43], a[136:139], v[192:195]\n"
"v_mul_f32 v14, v14, v226\n"
"v_mul_f32 v15, v15, v227\n"
"v_mfma_f32_16x16x32_bf16 v[192:195], a[44:47], a[140:143], v[192:195]\n"
"v_cvt_pk_bf16_f32 v24, v12, v13\n"
"v_cvt_pk_bf16_f32 v25, v14, v15\n"
"v_mfma_f32_16x16x32_bf16 v[164:167], a[36:39], a[140:143], v[164:167]\n"
"ds_write_b64 v2, v[24:25] offset:29696\n"
"v_mfma_f32_16x16x32_bf16 v[164:167], a[32:35], a[136:139], v[164:167]\n"
"s_waitcnt lgkmcnt(3)\n"
"v_mov_b32 v8, s65\n"
"v_mfma_f32_16x16x32_bf16 v[168:171], a[32:35], a[144:147], v[168:171]\n"
"v_mov_b32 v9, s60\n"
"v_mfma_f32_16x16x32_bf16 v[168:171], a[36:39], a[148:151], v[168:171]\n"
"global_load_dwordx4 a[92:95], v0, s[34:35] offset:3072 sc1\n"
"v_lshlrev_b32 v12, 16, v18\n"
"v_mfma_f32_16x16x32_bf16 v[196:199], a[44:47], a[148:151], v[196:199]\n"
"buffer_store_dwordx2 v[8:9], off, s[56:59], %13 sc1\n"
"v_and_b32 v13, s63, v18\n"
"v_mfma_f32_16x16x32_bf16 v[196:199], a[40:43], a[144:147], v[196:199]\n"
"v_lshlrev_b32 v14, 16, v19\n"
"v_and_b32 v15, s63, v19\n"
"v_mfma_f32_16x16x32_bf16 a[20:23], a[40:43], a[152:155], a[20:23]\n"
"s_add_u32 s34, s34, 8192\n"
"v_mul_f32 v12, v12, v252\n"
"v_mul_f32 v13, v13, v253\n"
"v_mfma_f32_16x16x32_bf16 a[20:23], a[44:47], a[156:159], a[20:23]\n"
"v_mul_f32 v14, v14, v254\n"
"v_mul_f32 v15, v15, v255\n"
"v_mfma_f32_16x16x32_bf16 a[16:19], a[36:39], a[156:159], a[16:19]\n"
"v_cvt_pk_bf16_f32 v26, v12, v13\n"
"v_cvt_pk_bf16_f32 v27, v14, v15\n"
"v_mfma_f32_16x16x32_bf16 a[16:19], a[32:35], a[152:155], a[16:19]\n"
"s_waitcnt vmcnt(12)\n"
"ds_write_b64 v2, v[26:27] offset:29704\n"
"v_mfma_f32_16x16x32_bf16 v[200:203], a[48:51], a[96:99], v[200:203]\n"
"v_accvgpr_read_b32 v16, a252\n"
"v_accvgpr_read_b32 v17, a253\n"
"v_mfma_f32_16x16x32_bf16 v[200:203], a[52:55], a[100:103], v[200:203]\n"
"global_load_dwordx4 a[32:35], v0, s[34:35] offset:-4096 sc1\n"
"v_accvgpr_read_b32 v18, a254\n"
"v_mfma_f32_16x16x32_bf16 v[228:231], a[60:63], a[100:103], v[228:231]\n"
"ds_read_b128 a[100:103], v3 offset:1024\n"
"v_accvgpr_read_b32 v19, a255\n"
"v_mfma_f32_16x16x32_bf16 v[228:231], a[56:59], a[96:99], v[228:231]\n"
"ds_read_b128 a[96:99], v3 offset:0\n"
"v_lshlrev_b32 v12, 16, v16\n"
"v_mfma_f32_16x16x32_bf16 v[232:235], a[56:59], a[104:107], v[232:235]\n"
"v_and_b32 v13, s63, v16\n"
"v_lshlrev_b32 v14, 16, v17\n"
"v_mfma_f32_16x16x32_bf16 v[232:235], a[60:63], a[108:111], v[232:235]\n"
"v_and_b32 v15, s63, v17\n"
"v_accvgpr_read_b32 v10, a24\n"
"v_mfma_f32_16x16x32_bf16 v[204:207], a[52:55], a[108:111], v[204:207]\n"
"ds_read_b128 a[108:111], v3 offset:3072\n"
"v_accvgpr_read_b32 v11, a25\n"
"v_mfma_f32_16x16x32_bf16 v[204:207], a[48:51], a[104:107], v[204:207]\n"
"ds_read_b128 a[104:107], v3 offset:2048\n"
"v_mul_f32 v12, v12, v10\n"
"v_mfma_f32_16x16x32_bf16 v[208:211], a[48:51], a[112:115], v[208:211]\n"
"v_mul_f32 v13, v13, v11\n"
"v_accvgpr_read_b32 v10, a26\n"
"v_mfma_f32_16x16x32_bf16 v[208:211], a[52:55], a[116:119], v[208:211]\n"
"global_load_dwordx4 a[36:39], v0, s[34:35] offset:-3072 sc1\n"
"v_accvgpr_read_b32 v11, a27\n"
"v_mfma_f32_16x16x32_bf16 v[236:239], a[60:63], a[116:119], v[236:239]\n"
"ds_read_b128 a[116:119], v3 offset:5120\n"
"v_mul_f32 v14, v14, v10\n"
"v_mfma_f32_16x16x32_bf16 v[236:239], a[56:59], a[112:115], v[236:239]\n"
"ds_read_b128 a[112:115], v3 offset:4096\n"
"v_mul_f32 v15, v15, v11\n"
"v_mfma_f32_16x16x32_bf16 v[240:243], a[56:59], a[120:123], v[240:243]\n"
"v_cvt_pk_bf16_f32 v28, v12, v13\n"
"v_cvt_pk_bf16_f32 v29, v14, v15\n"
"v_mfma_f32_16x16x32_bf16 v[240:243], a[60:63], a[124:127], v[240:243]\n"
"ds_write_b64 v2, v[28:29] offset:31744\n"
"v_mfma_f32_16x16x32_bf16 v[212:215], a[52:55], a[124:127], v[212:215]\n"
"ds_read_b128 a[124:127], v3 offset:7168\n"
"v_mfma_f32_16x16x32_bf16 v[212:215], a[48:51], a[120:123], v[212:215]\n"
"ds_read_b128 a[120:123], v3 offset:6144\n"
"v_lshlrev_b32 v12, 16, v18\n"
"v_mfma_f32_16x16x32_bf16 v[216:219], a[48:51], a[128:131], v[216:219]\n"
"v_and_b32 v13, s63, v18\n"
"v_lshlrev_b32 v14, 16, v19\n"
"v_mfma_f32_16x16x32_bf16 v[216:219], a[52:55], a[132:135], v[216:219]\n"
"global_load_dwordx4 a[40:43], v0, s[34:35] offset:-2048 sc1\n"
"v_and_b32 v15, s63, v19\n"
"v_mfma_f32_16x16x32_bf16 v[244:247], a[60:63], a[132:135], v[244:247]\n"
"ds_read_b128 a[132:135], v3 offset:9216\n"
"v_accvgpr_read_b32 v10, a28\n"
"v_mfma_f32_16x16x32_bf16 v[244:247], a[56:59], a[128:131], v[244:247]\n"
"ds_read_b128 a[128:131], v3 offset:8192\n"
"v_accvgpr_read_b32 v11, a29\n"
"v_mfma_f32_16x16x32_bf16 v[248:251], a[56:59], a[136:139], v[248:251]\n"
"v_mul_f32 v12, v12, v10\n"
"v_mul_f32 v13, v13, v11\n"
"v_mfma_f32_16x16x32_bf16 v[248:251], a[60:63], a[140:143], v[248:251]\n"
"v_accvgpr_read_b32 v10, a30\n"
"v_mfma_f32_16x16x32_bf16 v[220:223], a[52:55], a[140:143], v[220:223]\n"
"ds_read_b128 a[140:143], v3 offset:11264\n"
"v_accvgpr_read_b32 v11, a31\n"
"v_mfma_f32_16x16x32_bf16 v[220:223], a[48:51], a[136:139], v[220:223]\n"
"ds_read_b128 a[136:139], v3 offset:10240\n"
"v_mul_f32 v14, v14, v10\n"
"v_mfma_f32_16x16x32_bf16 v[224:227], a[48:51], a[144:147], v[224:227]\n"
"v_mul_f32 v15, v15, v11\n"
"v_cvt_pk_bf16_f32 v30, v12, v13\n"
"v_mfma_f32_16x16x32_bf16 v[224:227], a[52:55], a[148:151], v[224:227]\n"
"global_load_dwordx4 a[44:47], v0, s[34:35] offset:-1024 sc1\n"
"v_cvt_pk_bf16_f32 v31, v14, v15\n"
"v_mfma_f32_16x16x32_bf16 v[252:255], a[60:63], a[148:151], v[252:255]\n"
"global_store_dwordx4 v0, v[28:31], s[54:55] offset:3072 sc1\n"
"ds_read_b128 a[148:151], v3 offset:13312\n"
"ds_write_b64 v2, v[30:31] offset:31752\n"
"v_mfma_f32_16x16x32_bf16 v[252:255], a[56:59], a[144:147], v[252:255]\n"
"ds_read_b128 a[144:147], v3 offset:12288\n"
"v_mfma_f32_16x16x32_bf16 a[28:31], a[56:59], a[152:155], a[28:31]\n"
"v_mfma_f32_16x16x32_bf16 a[28:31], a[60:63], a[156:159], a[28:31]\n"
"v_mfma_f32_16x16x32_bf16 a[24:27], a[52:55], a[156:159], a[24:27]\n"
"ds_read_b128 a[156:159], v3 offset:15360\n"
"v_mfma_f32_16x16x32_bf16 a[24:27], a[48:51], a[152:155], a[24:27]\n"
"s_waitcnt vmcnt(11) lgkmcnt(13)\n"
"ds_read_b128 a[152:155], v3 offset:14336\n"
"v_mfma_f32_16x16x32_bf16 v[144:147], a[64:67], a[96:99], v[144:147]\n"
"ds_read_b128 v[24:27], v2 offset:16384\n"
"v_mfma_f32_16x16x32_bf16 v[144:147], a[68:71], a[100:103], v[144:147]\n"
"global_load_dwordx4 a[48:51], v0, s[34:35] offset:0 sc1\n"
"v_mfma_f32_16x16x32_bf16 v[172:175], a[76:79], a[100:103], v[172:175]\n"
"v_mfma_f32_16x16x32_bf16 v[172:175], a[72:75], a[96:99], v[172:175]\n"
"v_mfma_f32_16x16x32_bf16 v[176:179], a[72:75], a[104:107], v[176:179]\n"
"v_mfma_f32_16x16x32_bf16 v[176:179], a[76:79], a[108:111], v[176:179]\n"
"v_mfma_f32_16x16x32_bf16 v[148:151], a[68:71], a[108:111], v[148:151]\n"
"v_mfma_f32_16x16x32_bf16 v[148:151], a[64:67], a[104:107], v[148:151]\n"
"s_waitcnt lgkmcnt(10)\n"
"v_mfma_f32_16x16x32_bf16 v[152:155], a[64:67], a[112:115], v[152:155]\n"
"v_mfma_f32_16x16x32_bf16 v[152:155], a[68:71], a[116:119], v[152:155]\n"
"global_load_dwordx4 a[52:55], v0, s[34:35] offset:1024 sc1\n"
"v_mfma_f32_16x16x32_bf16 v[180:183], a[76:79], a[116:119], v[180:183]\n"
"v_mfma_f32_16x16x32_bf16 v[180:183], a[72:75], a[112:115], v[180:183]\n"
"v_mfma_f32_16x16x32_bf16 v[184:187], a[72:75], a[120:123], v[184:187]\n"
"s_waitcnt lgkmcnt(0)\n"
"global_store_dwordx4 v0, v[24:27], s[52:53] offset:-4096 sc1\n"
"v_mfma_f32_16x16x32_bf16 v[184:187], a[76:79], a[124:127], v[184:187]\n"
"v_mfma_f32_16x16x32_bf16 v[156:159], a[68:71], a[124:127], v[156:159]\n"
"v_mfma_f32_16x16x32_bf16 v[156:159], a[64:67], a[120:123], v[156:159]\n"
"v_mfma_f32_16x16x32_bf16 v[160:163], a[64:67], a[128:131], v[160:163]\n"
"v_mfma_f32_16x16x32_bf16 v[160:163], a[68:71], a[132:135], v[160:163]\n"
"global_load_dwordx4 a[56:59], v0, s[34:35] offset:2048 sc1\n"
"v_mfma_f32_16x16x32_bf16 v[188:191], a[76:79], a[132:135], v[188:191]\n"
"v_mfma_f32_16x16x32_bf16 v[188:191], a[72:75], a[128:131], v[188:191]\n"
"v_mfma_f32_16x16x32_bf16 v[192:195], a[72:75], a[136:139], v[192:195]\n"
"v_mfma_f32_16x16x32_bf16 v[192:195], a[76:79], a[140:143], v[192:195]\n"
"v_mfma_f32_16x16x32_bf16 v[164:167], a[68:71], a[140:143], v[164:167]\n"
"v_mfma_f32_16x16x32_bf16 v[164:167], a[64:67], a[136:139], v[164:167]\n"
"v_mfma_f32_16x16x32_bf16 v[168:171], a[64:67], a[144:147], v[168:171]\n"
"v_mfma_f32_16x16x32_bf16 v[168:171], a[68:71], a[148:151], v[168:171]\n"
"global_load_dwordx4 a[60:63], v0, s[34:35] offset:3072 sc1\n"
"v_mfma_f32_16x16x32_bf16 v[196:199], a[76:79], a[148:151], v[196:199]\n"
"v_mfma_f32_16x16x32_bf16 v[196:199], a[72:75], a[144:147], v[196:199]\n"
"v_mfma_f32_16x16x32_bf16 a[20:23], a[72:75], a[152:155], a[20:23]\n"
"s_add_u32 s34, s34, 8192\n"
"v_mfma_f32_16x16x32_bf16 a[20:23], a[76:79], a[156:159], a[20:23]\n"
"v_mfma_f32_16x16x32_bf16 a[16:19], a[68:71], a[156:159], a[16:19]\n"
"v_mfma_f32_16x16x32_bf16 a[16:19], a[64:67], a[152:155], a[16:19]\n"
"s_waitcnt vmcnt(11)\n"
"ds_read_b128 v[28:31], v2 offset:17408\n"
"v_mfma_f32_16x16x32_bf16 v[200:203], a[80:83], a[96:99], v[200:203]\n"
"v_mfma_f32_16x16x32_bf16 v[200:203], a[84:87], a[100:103], v[200:203]\n"
"global_load_dwordx4 a[64:67], v0, s[34:35] offset:-4096 sc1\n"
"v_mfma_f32_16x16x32_bf16 v[228:231], a[92:95], a[100:103], v[228:231]\n"
"ds_read_b128 a[100:103], v4 offset:1024\n"
"v_mfma_f32_16x16x32_bf16 v[228:231], a[88:91], a[96:99], v[228:231]\n"
"ds_read_b128 a[96:99], v4 offset:0\n"
"v_mfma_f32_16x16x32_bf16 v[232:235], a[88:91], a[104:107], v[232:235]\n"
"v_mfma_f32_16x16x32_bf16 v[232:235], a[92:95], a[108:111], v[232:235]\n"
"v_mfma_f32_16x16x32_bf16 v[204:207], a[84:87], a[108:111], v[204:207]\n"
"ds_read_b128 a[108:111], v4 offset:3072\n"
"v_mfma_f32_16x16x32_bf16 v[204:207], a[80:83], a[104:107], v[204:207]\n"
"ds_read_b128 a[104:107], v4 offset:2048\n"
"v_mfma_f32_16x16x32_bf16 v[208:211], a[80:83], a[112:115], v[208:211]\n"
"v_mfma_f32_16x16x32_bf16 v[208:211], a[84:87], a[116:119], v[208:211]\n"
"global_load_dwordx4 a[68:71], v0, s[34:35] offset:-3072 sc1\n"
"v_mfma_f32_16x16x32_bf16 v[236:239], a[92:95], a[116:119], v[236:239]\n"
"s_waitcnt lgkmcnt(4)\n"
"global_store_dwordx4 v0, v[28:31], s[52:53] offset:-3072 sc1\n"
"ds_read_b128 a[116:119], v4 offset:5120\n"
"v_mfma_f32_16x16x32_bf16 v[236:239], a[88:91], a[112:115], v[236:239]\n"
"ds_read_b128 a[112:115], v4 offset:4096\n"
"v_mfma_f32_16x16x32_bf16 v[240:243], a[88:91], a[120:123], v[240:243]\n"
"v_mfma_f32_16x16x32_bf16 v[240:243], a[92:95], a[124:127], v[240:243]\n"
"v_mfma_f32_16x16x32_bf16 v[212:215], a[84:87], a[124:127], v[212:215]\n"
"ds_read_b128 a[124:127], v4 offset:7168\n"
"v_mfma_f32_16x16x32_bf16 v[212:215], a[80:83], a[120:123], v[212:215]\n"
"ds_read_b128 a[120:123], v4 offset:6144\n"
"v_mfma_f32_16x16x32_bf16 v[216:219], a[80:83], a[128:131], v[216:219]\n"
"v_mfma_f32_16x16x32_bf16 v[216:219], a[84:87], a[132:135], v[216:219]\n"
"global_load_dwordx4 a[72:75], v0, s[34:35] offset:-2048 sc1\n"
"v_mfma_f32_16x16x32_bf16 v[244:247], a[92:95], a[132:135], v[244:247]\n"
"ds_read_b128 a[132:135], v4 offset:9216\n"
"v_mfma_f32_16x16x32_bf16 v[244:247], a[88:91], a[128:131], v[244:247]\n"
"ds_read_b128 a[128:131], v4 offset:8192\n"
"v_mfma_f32_16x16x32_bf16 v[248:251], a[88:91], a[136:139], v[248:251]\n"
"v_mfma_f32_16x16x32_bf16 v[248:251], a[92:95], a[140:143], v[248:251]\n"
"v_mfma_f32_16x16x32_bf16 v[220:223], a[84:87], a[140:143], v[220:223]\n"
"ds_read_b128 a[140:143], v4 offset:11264\n"
"v_mfma_f32_16x16x32_bf16 v[220:223], a[80:83], a[136:139], v[220:223]\n"
"ds_read_b128 a[136:139], v4 offset:10240\n"
"v_mfma_f32_16x16x32_bf16 v[224:227], a[80:83], a[144:147], v[224:227]\n"
"v_mfma_f32_16x16x32_bf16 v[224:227], a[84:87], a[148:151], v[224:227]\n"
"global_load_dwordx4 a[76:79], v0, s[34:35] offset:-1024 sc1\n"
"v_mfma_f32_16x16x32_bf16 v[252:255], a[92:95], a[148:151], v[252:255]\n"
"ds_read_b128 a[148:151], v4 offset:13312\n"
"v_mfma_f32_16x16x32_bf16 v[252:255], a[88:91], a[144:147], v[252:255]\n"
"ds_read_b128 a[144:147], v4 offset:12288\n"
"v_mfma_f32_16x16x32_bf16 a[28:31], a[88:91], a[152:155], a[28:31]\n"
"v_mfma_f32_16x16x32_bf16 a[28:31], a[92:95], a[156:159], a[28:31]\n"
"ds_read_b128 v[24:27], v2 offset:18432\n"
"v_mfma_f32_16x16x32_bf16 a[24:27], a[84:87], a[156:159], a[24:27]\n"
"ds_read_b128 a[156:159], v4 offset:15360\n"
"v_mfma_f32_16x16x32_bf16 a[24:27], a[80:83], a[152:155], a[24:27]\n"
"s_waitcnt vmcnt(11) lgkmcnt(12)\n"
"ds_read_b128 a[152:155], v4 offset:14336\n"
"v_mfma_f32_16x16x32_bf16 v[144:147], a[32:35], a[96:99], v[144:147]\n"
"v_mfma_f32_16x16x32_bf16 v[144:147], a[36:39], a[100:103], v[144:147]\n"
"global_load_dwordx4 a[80:83], v0, s[34:35] offset:0 sc1\n"
"v_mfma_f32_16x16x32_bf16 v[172:175], a[44:47], a[100:103], v[172:175]\n"
"v_mfma_f32_16x16x32_bf16 v[172:175], a[40:43], a[96:99], v[172:175]\n"
"v_mfma_f32_16x16x32_bf16 v[176:179], a[40:43], a[104:107], v[176:179]\n"
"v_mfma_f32_16x16x32_bf16 v[176:179], a[44:47], a[108:111], v[176:179]\n"
"v_mfma_f32_16x16x32_bf16 v[148:151], a[36:39], a[108:111], v[148:151]\n"
"v_mfma_f32_16x16x32_bf16 v[148:151], a[32:35], a[104:107], v[148:151]\n"
"s_waitcnt lgkmcnt(9)\n"
"v_mfma_f32_16x16x32_bf16 v[152:155], a[32:35], a[112:115], v[152:155]\n"
"s_waitcnt lgkmcnt(2)\n"
"global_store_dwordx4 v0, v[24:27], s[52:53] offset:-2048 sc1\n"
"v_mfma_f32_16x16x32_bf16 v[152:155], a[36:39], a[116:119], v[152:155]\n"
"global_load_dwordx4 a[84:87], v0, s[34:35] offset:1024 sc1\n"
"v_mfma_f32_16x16x32_bf16 v[180:183], a[44:47], a[116:119], v[180:183]\n"
"v_mfma_f32_16x16x32_bf16 v[180:183], a[40:43], a[112:115], v[180:183]\n"
"v_mfma_f32_16x16x32_bf16 v[184:187], a[40:43], a[120:123], v[184:187]\n"
"v_mfma_f32_16x16x32_bf16 v[184:187], a[44:47], a[124:127], v[184:187]\n"
"v_mfma_f32_16x16x32_bf16 v[156:159], a[36:39], a[124:127], v[156:159]\n"
"v_mfma_f32_16x16x32_bf16 v[156:159], a[32:35], a[120:123], v[156:159]\n"
"v_mfma_f32_16x16x32_bf16 v[160:163], a[32:35], a[128:131], v[160:163]\n"
"v_mfma_f32_16x16x32_bf16 v[160:163], a[36:39], a[132:135], v[160:163]\n"
"global_load_dwordx4 a[88:91], v0, s[34:35] offset:2048 sc1\n"
"v_mfma_f32_16x16x32_bf16 v[188:191], a[44:47], a[132:135], v[188:191]\n"
"v_mfma_f32_16x16x32_bf16 v[188:191], a[40:43], a[128:131], v[188:191]\n"
"v_mfma_f32_16x16x32_bf16 v[192:195], a[40:43], a[136:139], v[192:195]\n"
"v_mfma_f32_16x16x32_bf16 v[192:195], a[44:47], a[140:143], v[192:195]\n"
"v_mfma_f32_16x16x32_bf16 v[164:167], a[36:39], a[140:143], v[164:167]\n"
"v_mfma_f32_16x16x32_bf16 v[164:167], a[32:35], a[136:139], v[164:167]\n"
"s_waitcnt lgkmcnt(0)\n"
"v_mfma_f32_16x16x32_bf16 v[168:171], a[32:35], a[144:147], v[168:171]\n"
"v_mfma_f32_16x16x32_bf16 v[168:171], a[36:39], a[148:151], v[168:171]\n"
"global_load_dwordx4 a[92:95], v0, s[34:35] offset:3072 sc1\n"
"v_mfma_f32_16x16x32_bf16 v[196:199], a[44:47], a[148:151], v[196:199]\n"
"v_mfma_f32_16x16x32_bf16 v[196:199], a[40:43], a[144:147], v[196:199]\n"
"ds_read_b128 v[28:31], v2 offset:19456\n"
"v_mfma_f32_16x16x32_bf16 a[20:23], a[40:43], a[152:155], a[20:23]\n"
"s_add_u32 s34, s34, 8192\n"
"v_mfma_f32_16x16x32_bf16 a[20:23], a[44:47], a[156:159], a[20:23]\n"
"v_mfma_f32_16x16x32_bf16 a[16:19], a[36:39], a[156:159], a[16:19]\n"
"v_mfma_f32_16x16x32_bf16 a[16:19], a[32:35], a[152:155], a[16:19]\n"
"s_waitcnt vmcnt(10)\n"
"v_mfma_f32_16x16x32_bf16 v[200:203], a[48:51], a[96:99], v[200:203]\n"
"v_mfma_f32_16x16x32_bf16 v[200:203], a[52:55], a[100:103], v[200:203]\n"
"global_load_dwordx4 a[32:35], v0, s[34:35] offset:-4096 sc1\n"
"v_mfma_f32_16x16x32_bf16 v[228:231], a[60:63], a[100:103], v[228:231]\n"
"ds_read_b128 a[100:103], v5 offset:1024\n"
"v_mfma_f32_16x16x32_bf16 v[228:231], a[56:59], a[96:99], v[228:231]\n"
"ds_read_b128 a[96:99], v5 offset:0\n"
"v_mfma_f32_16x16x32_bf16 v[232:235], a[56:59], a[104:107], v[232:235]\n"
"v_mfma_f32_16x16x32_bf16 v[232:235], a[60:63], a[108:111], v[232:235]\n"
"v_mfma_f32_16x16x32_bf16 v[204:207], a[52:55], a[108:111], v[204:207]\n"
"s_waitcnt lgkmcnt(2)\n"
"global_store_dwordx4 v0, v[28:31], s[52:53] offset:-1024 sc1\n"
"ds_read_b128 a[108:111], v5 offset:3072\n"
"v_mfma_f32_16x16x32_bf16 v[204:207], a[48:51], a[104:107], v[204:207]\n"
"ds_read_b128 a[104:107], v5 offset:2048\n"
"v_mfma_f32_16x16x32_bf16 v[208:211], a[48:51], a[112:115], v[208:211]\n"
"v_mfma_f32_16x16x32_bf16 v[208:211], a[52:55], a[116:119], v[208:211]\n"
"global_load_dwordx4 a[36:39], v0, s[34:35] offset:-3072 sc1\n"
"v_mfma_f32_16x16x32_bf16 v[236:239], a[60:63], a[116:119], v[236:239]\n"
"ds_read_b128 a[116:119], v5 offset:5120\n"
"v_mfma_f32_16x16x32_bf16 v[236:239], a[56:59], a[112:115], v[236:239]\n"
"ds_read_b128 a[112:115], v5 offset:4096\n"
"v_mfma_f32_16x16x32_bf16 v[240:243], a[56:59], a[120:123], v[240:243]\n"
"v_mfma_f32_16x16x32_bf16 v[240:243], a[60:63], a[124:127], v[240:243]\n"
"v_mfma_f32_16x16x32_bf16 v[212:215], a[52:55], a[124:127], v[212:215]\n"
"ds_read_b128 a[124:127], v5 offset:7168\n"
"v_mfma_f32_16x16x32_bf16 v[212:215], a[48:51], a[120:123], v[212:215]\n"
"ds_read_b128 a[120:123], v5 offset:6144\n"
"v_mfma_f32_16x16x32_bf16 v[216:219], a[48:51], a[128:131], v[216:219]\n"
"v_mfma_f32_16x16x32_bf16 v[216:219], a[52:55], a[132:135], v[216:219]\n"
"global_load_dwordx4 a[40:43], v0, s[34:35] offset:-2048 sc1\n"
"v_mfma_f32_16x16x32_bf16 v[244:247], a[60:63], a[132:135], v[244:247]\n"
"ds_read_b128 a[132:135], v5 offset:9216\n"
"v_mfma_f32_16x16x32_bf16 v[244:247], a[56:59], a[128:131], v[244:247]\n"
"ds_read_b128 a[128:131], v5 offset:8192\n"
"v_mfma_f32_16x16x32_bf16 v[248:251], a[56:59], a[136:139], v[248:251]\n"
"v_mfma_f32_16x16x32_bf16 v[248:251], a[60:63], a[140:143], v[248:251]\n"
"v_mfma_f32_16x16x32_bf16 v[220:223], a[52:55], a[140:143], v[220:223]\n"
"ds_read_b128 a[140:143], v5 offset:11264\n"
"v_mfma_f32_16x16x32_bf16 v[220:223], a[48:51], a[136:139], v[220:223]\n"
"ds_read_b128 a[136:139], v5 offset:10240\n"
"v_mfma_f32_16x16x32_bf16 v[224:227], a[48:51], a[144:147], v[224:227]\n"
"ds_read_b128 v[24:27], v2 offset:20480\n"
"v_mfma_f32_16x16x32_bf16 v[224:227], a[52:55], a[148:151], v[224:227]\n"
"global_load_dwordx4 a[44:47], v0, s[34:35] offset:-1024 sc1\n"
"v_mfma_f32_16x16x32_bf16 v[252:255], a[60:63], a[148:151], v[252:255]\n"
"ds_read_b128 a[148:151], v5 offset:13312\n"
"v_mfma_f32_16x16x32_bf16 v[252:255], a[56:59], a[144:147], v[252:255]\n"
"ds_read_b128 a[144:147], v5 offset:12288\n"
"v_mfma_f32_16x16x32_bf16 a[28:31], a[56:59], a[152:155], a[28:31]\n"
"v_mfma_f32_16x16x32_bf16 a[28:31], a[60:63], a[156:159], a[28:31]\n"
"v_mfma_f32_16x16x32_bf16 a[24:27], a[52:55], a[156:159], a[24:27]\n"
"ds_read_b128 a[156:159], v5 offset:15360\n"
"v_mfma_f32_16x16x32_bf16 a[24:27], a[48:51], a[152:155], a[24:27]\n"
"s_waitcnt vmcnt(10) lgkmcnt(12)\n"
"ds_read_b128 a[152:155], v5 offset:14336\n"
"v_mfma_f32_16x16x32_bf16 v[144:147], a[64:67], a[96:99], v[144:147]\n"
"v_mfma_f32_16x16x32_bf16 v[144:147], a[68:71], a[100:103], v[144:147]\n"
"global_load_dwordx4 a[48:51], v0, s[34:35] offset:0 sc1\n"
"v_mfma_f32_16x16x32_bf16 v[172:175], a[76:79], a[100:103], v[172:175]\n"
"v_mfma_f32_16x16x32_bf16 v[172:175], a[72:75], a[96:99], v[172:175]\n"
"v_mfma_f32_16x16x32_bf16 v[176:179], a[72:75], a[104:107], v[176:179]\n"
"s_waitcnt lgkmcnt(4)\n"
"global_store_dwordx4 v0, v[24:27], s[52:53] offset:0 sc1\n"
"v_mfma_f32_16x16x32_bf16 v[176:179], a[76:79], a[108:111], v[176:179]\n"
"v_mfma_f32_16x16x32_bf16 v[148:151], a[68:71], a[108:111], v[148:151]\n"
"v_mfma_f32_16x16x32_bf16 v[148:151], a[64:67], a[104:107], v[148:151]\n"
"v_mfma_f32_16x16x32_bf16 v[152:155], a[64:67], a[112:115], v[152:155]\n"
"v_mfma_f32_16x16x32_bf16 v[152:155], a[68:71], a[116:119], v[152:155]\n"
"global_load_dwordx4 a[52:55], v0, s[34:35] offset:1024 sc1\n"
"v_mfma_f32_16x16x32_bf16 v[180:183], a[76:79], a[116:119], v[180:183]\n"
"v_mfma_f32_16x16x32_bf16 v[180:183], a[72:75], a[112:115], v[180:183]\n"
"v_mfma_f32_16x16x32_bf16 v[184:187], a[72:75], a[120:123], v[184:187]\n"
"v_mfma_f32_16x16x32_bf16 v[184:187], a[76:79], a[124:127], v[184:187]\n"
"v_mfma_f32_16x16x32_bf16 v[156:159], a[68:71], a[124:127], v[156:159]\n"
"v_mfma_f32_16x16x32_bf16 v[156:159], a[64:67], a[120:123], v[156:159]\n"
"v_mfma_f32_16x16x32_bf16 v[160:163], a[64:67], a[128:131], v[160:163]\n"
"v_mfma_f32_16x16x32_bf16 v[160:163], a[68:71], a[132:135], v[160:163]\n"
"global_load_dwordx4 a[56:59], v0, s[34:35] offset:2048 sc1\n"
"v_mfma_f32_16x16x32_bf16 v[188:191], a[76:79], a[132:135], v[188:191]\n"
"v_mfma_f32_16x16x32_bf16 v[188:191], a[72:75], a[128:131], v[188:191]\n"
"v_mfma_f32_16x16x32_bf16 v[192:195], a[72:75], a[136:139], v[192:195]\n"
"v_mfma_f32_16x16x32_bf16 v[192:195], a[76:79], a[140:143], v[192:195]\n"
"v_mfma_f32_16x16x32_bf16 v[164:167], a[68:71], a[140:143], v[164:167]\n"
"v_mfma_f32_16x16x32_bf16 v[164:167], a[64:67], a[136:139], v[164:167]\n"
"s_waitcnt lgkmcnt(0)\n"
"ds_read_b128 v[28:31], v2 offset:21504\n"
"v_mfma_f32_16x16x32_bf16 v[168:171], a[64:67], a[144:147], v[168:171]\n"
"v_mfma_f32_16x16x32_bf16 v[168:171], a[68:71], a[148:151], v[168:171]\n"
"global_load_dwordx4 a[60:63], v0, s[34:35] offset:3072 sc1\n"
"v_mfma_f32_16x16x32_bf16 v[196:199], a[76:79], a[148:151], v[196:199]\n"
"v_mfma_f32_16x16x32_bf16 v[196:199], a[72:75], a[144:147], v[196:199]\n"
"v_mfma_f32_16x16x32_bf16 a[20:23], a[72:75], a[152:155], a[20:23]\n"
"s_add_u32 s34, s34, 8192\n"
"v_mfma_f32_16x16x32_bf16 a[20:23], a[76:79], a[156:159], a[20:23]\n"
"v_mfma_f32_16x16x32_bf16 a[16:19], a[68:71], a[156:159], a[16:19]\n"
"v_mfma_f32_16x16x32_bf16 a[16:19], a[64:67], a[152:155], a[16:19]\n"
"s_waitcnt vmcnt(10)\n"
"v_mfma_f32_16x16x32_bf16 v[200:203], a[80:83], a[96:99], v[200:203]\n"
"v_mfma_f32_16x16x32_bf16 v[200:203], a[84:87], a[100:103], v[200:203]\n"
"global_load_dwordx4 a[64:67], v0, s[34:35] offset:-4096 sc1\n"
"v_mfma_f32_16x16x32_bf16 v[228:231], a[92:95], a[100:103], v[228:231]\n"
"s_waitcnt lgkmcnt(0)\n"
"global_store_dwordx4 v0, v[28:31], s[52:53] offset:1024 sc1\n"
"ds_read_b128 a[100:103], v2 offset:17408\n"
"buffer_load_dwordx2 v[8:9], off, s[56:59], %14 sc1\n"
"v_mfma_f32_16x16x32_bf16 v[228:231], a[88:91], a[96:99], v[228:231]\n"
"ds_read_b128 a[96:99], v2 offset:16384\n"
"v_mfma_f32_16x16x32_bf16 v[232:235], a[88:91], a[104:107], v[232:235]\n"
"v_mfma_f32_16x16x32_bf16 v[232:235], a[92:95], a[108:111], v[232:235]\n"
"v_mfma_f32_16x16x32_bf16 v[204:207], a[84:87], a[108:111], v[204:207]\n"
"ds_read_b128 a[108:111], v2 offset:19456\n"
"v_mfma_f32_16x16x32_bf16 v[204:207], a[80:83], a[104:107], v[204:207]\n"
"ds_read_b128 a[104:107], v2 offset:18432\n"
"v_mfma_f32_16x16x32_bf16 v[208:211], a[80:83], a[112:115], v[208:211]\n"
"v_mfma_f32_16x16x32_bf16 v[208:211], a[84:87], a[116:119], v[208:211]\n"
"global_load_dwordx4 a[68:71], v0, s[34:35] offset:-3072 sc1\n"
"v_mfma_f32_16x16x32_bf16 v[236:239], a[92:95], a[116:119], v[236:239]\n"
"ds_read_b128 a[116:119], v2 offset:21504\n"
"v_mfma_f32_16x16x32_bf16 v[236:239], a[88:91], a[112:115], v[236:239]\n"
"ds_read_b128 a[112:115], v2 offset:20480\n"
"v_mfma_f32_16x16x32_bf16 v[240:243], a[88:91], a[120:123], v[240:243]\n"
"v_mfma_f32_16x16x32_bf16 v[240:243], a[92:95], a[124:127], v[240:243]\n"
"v_mfma_f32_16x16x32_bf16 v[212:215], a[84:87], a[124:127], v[212:215]\n"
"ds_read_b128 a[124:127], v2 offset:23552\n"
"v_mfma_f32_16x16x32_bf16 v[212:215], a[80:83], a[120:123], v[212:215]\n"
"ds_read_b128 a[120:123], v2 offset:22528\n"
"v_mfma_f32_16x16x32_bf16 v[216:219], a[80:83], a[128:131], v[216:219]\n"
"v_mfma_f32_16x16x32_bf16 v[216:219], a[84:87], a[132:135], v[216:219]\n"
"global_load_dwordx4 a[72:75], v0, s[34:35] offset:-2048 sc1\n"
"v_mfma_f32_16x16x32_bf16 v[244:247], a[92:95], a[132:135], v[244:247]\n"
"ds_read_b128 a[132:135], v2 offset:25600\n"
"v_mfma_f32_16x16x32_bf16 v[244:247], a[88:91], a[128:131], v[244:247]\n"
"ds_read_b128 a[128:131], v2 offset:24576\n"
"v_mfma_f32_16x16x32_bf16 v[248:251], a[88:91], a[136:139], v[248:251]\n"
"v_mfma_f32_16x16x32_bf16 v[248:251], a[92:95], a[140:143], v[248:251]\n"
"ds_read_b128 v[24:27], v2 offset:22528\n"
"v_mfma_f32_16x16x32_bf16 v[220:223], a[84:87], a[140:143], v[220:223]\n"
"ds_read_b128 a[140:143], v2 offset:27648\n"
"v_mfma_f32_16x16x32_bf16 v[220:223], a[80:83], a[136:139], v[220:223]\n"
"ds_read_b128 a[136:139], v2 offset:26624\n"
"v_mfma_f32_16x16x32_bf16 v[224:227], a[80:83], a[144:147], v[224:227]\n"
"v_mfma_f32_16x16x32_bf16 v[224:227], a[84:87], a[148:151], v[224:227]\n"
"global_load_dwordx4 a[76:79], v0, s[34:35] offset:-1024 sc1\n"
"v_mfma_f32_16x16x32_bf16 v[252:255], a[92:95], a[148:151], v[252:255]\n"
"ds_read_b128 a[148:151], v2 offset:29696\n"
"v_mfma_f32_16x16x32_bf16 v[252:255], a[88:91], a[144:147], v[252:255]\n"
"ds_read_b128 a[144:147], v2 offset:28672\n"
"v_mfma_f32_16x16x32_bf16 a[28:31], a[88:91], a[152:155], a[28:31]\n"
"v_mfma_f32_16x16x32_bf16 a[28:31], a[92:95], a[156:159], a[28:31]\n"
"v_mfma_f32_16x16x32_bf16 a[24:27], a[84:87], a[156:159], a[24:27]\n"
"ds_read_b128 a[156:159], v2 offset:31744\n"
"v_mfma_f32_16x16x32_bf16 a[24:27], a[80:83], a[152:155], a[24:27]\n"
"s_waitcnt vmcnt(11) lgkmcnt(12)\n"
"ds_read_b128 a[152:155], v2 offset:30720\n"
"v_mfma_f32_16x16x32_bf16 v[32:35], a[32:35], a[96:99], v[32:35]\n"
"s_waitcnt lgkmcnt(6)\n"
"global_store_dwordx4 v0, v[24:27], s[52:53] offset:2048 sc1\n"
"v_mfma_f32_16x16x32_bf16 v[32:35], a[36:39], a[100:103], v[32:35]\n"
"global_load_dwordx4 a[80:83], v0, s[34:35] offset:0 sc1\n"
"L_P3:\n"
"v_mfma_f32_16x16x32_bf16 v[60:63], a[44:47], a[100:103], v[60:63]\n"
"s_barrier\n"
"v_mfma_f32_16x16x32_bf16 v[60:63], a[40:43], a[96:99], v[60:63]\n"
"v_mfma_f32_16x16x32_bf16 v[64:67], a[40:43], a[104:107], v[64:67]\n"
"v_mfma_f32_16x16x32_bf16 v[64:67], a[44:47], a[108:111], v[64:67]\n"
"v_mfma_f32_16x16x32_bf16 v[36:39], a[36:39], a[108:111], v[36:39]\n"
"v_mfma_f32_16x16x32_bf16 v[36:39], a[32:35], a[104:107], v[36:39]\n"
"v_mfma_f32_16x16x32_bf16 v[40:43], a[32:35], a[112:115], v[40:43]\n"
"v_mfma_f32_16x16x32_bf16 v[40:43], a[36:39], a[116:119], v[40:43]\n"
"global_load_dwordx4 a[84:87], v0, s[34:35] offset:1024 sc1\n"
"v_mfma_f32_16x16x32_bf16 v[68:71], a[44:47], a[116:119], v[68:71]\n"
"v_mfma_f32_16x16x32_bf16 v[68:71], a[40:43], a[112:115], v[68:71]\n"
"v_mfma_f32_16x16x32_bf16 v[72:75], a[40:43], a[120:123], v[72:75]\n"
"v_mfma_f32_16x16x32_bf16 v[72:75], a[44:47], a[124:127], v[72:75]\n"
"v_mfma_f32_16x16x32_bf16 v[44:47], a[36:39], a[124:127], v[44:47]\n"
"v_mfma_f32_16x16x32_bf16 v[44:47], a[32:35], a[120:123], v[44:47]\n"
"s_waitcnt lgkmcnt(4)\n"
"v_mfma_f32_16x16x32_bf16 v[48:51], a[32:35], a[128:131], v[48:51]\n"
"v_mfma_f32_16x16x32_bf16 v[48:51], a[36:39], a[132:135], v[48:51]\n"
"global_load_dwordx4 a[88:91], v0, s[34:35] offset:2048 sc1\n"
"v_lshrrev_b32 v16, 4, v0\n"
"v_mfma_f32_16x16x32_bf16 v[76:79], a[44:47], a[132:135], v[76:79]\n"
"v_and_b32 v16, 0x30, v16\n"
"v_mfma_f32_16x16x32_bf16 v[76:79], a[40:43], a[128:131], v[76:79]\n"
"ds_read_b128 v[28:31], v2 offset:23552\n"
"buffer_load_dwordx2 v[18:19], v16, s[44:47], s61 offen offset:0\n"
"v_mfma_f32_16x16x32_bf16 v[80:83], a[40:43], a[136:139], v[80:83]\n"
"v_mfma_f32_16x16x32_bf16 v[80:83], a[44:47], a[140:143], v[80:83]\n"
"v_mfma_f32_16x16x32_bf16 v[52:55], a[36:39], a[140:143], v[52:55]\n"
"v_mfma_f32_16x16x32_bf16 v[52:55], a[32:35], a[136:139], v[52:55]\n"
"s_waitcnt lgkmcnt(1)\n"
"v_mfma_f32_16x16x32_bf16 v[56:59], a[32:35], a[144:147], v[56:59]\n"
"v_mfma_f32_16x16x32_bf16 v[56:59], a[36:39], a[148:151], v[56:59]\n"
"global_load_dwordx4 a[92:95], v0, s[34:35] offset:3072 sc1\n"
"v_lshrrev_b32 v20, 4, v0\n"
"v_mfma_f32_16x16x32_bf16 v[84:87], a[44:47], a[148:151], v[84:87]\n"
"v_and_b32 v20, 0x30, v20\n"
"v_mfma_f32_16x16x32_bf16 v[84:87], a[40:43], a[144:147], v[84:87]\n"
"buffer_load_dwordx2 v[22:23], v20, s[44:47], s61 offen offset:8\n"
"v_mfma_f32_16x16x32_bf16 a[4:7], a[40:43], a[152:155], a[4:7]\n"
"s_add_u32 s34, s34, 8192\n"
"v_mfma_f32_16x16x32_bf16 a[4:7], a[44:47], a[156:159], a[4:7]\n"
"v_mfma_f32_16x16x32_bf16 a[0:3], a[36:39], a[156:159], a[0:3]\n"
"s_waitcnt lgkmcnt(0)\n"
"global_store_dwordx4 v0, v[28:31], s[52:53] offset:3072 sc1\n"
"v_mfma_f32_16x16x32_bf16 a[0:3], a[32:35], a[152:155], a[0:3]\n"
"s_waitcnt vmcnt(14)\n"
"v_mfma_f32_16x16x32_bf16 v[88:91], a[48:51], a[96:99], v[88:91]\n"
"v_mfma_f32_16x16x32_bf16 v[88:91], a[52:55], a[100:103], v[88:91]\n"
"global_load_dwordx4 a[32:35], v0, s[34:35] offset:-4096 sc1\n"
"v_mfma_f32_16x16x32_bf16 v[116:119], a[60:63], a[100:103], v[116:119]\n"
"v_mfma_f32_16x16x32_bf16 v[116:119], a[56:59], a[96:99], v[116:119]\n"
"v_mfma_f32_16x16x32_bf16 v[120:123], a[56:59], a[104:107], v[120:123]\n"
"v_mfma_f32_16x16x32_bf16 v[120:123], a[60:63], a[108:111], v[120:123]\n"
"v_mfma_f32_16x16x32_bf16 v[92:95], a[52:55], a[108:111], v[92:95]\n"
"v_mfma_f32_16x16x32_bf16 v[92:95], a[48:51], a[104:107], v[92:95]\n"
"v_mfma_f32_16x16x32_bf16 v[96:99], a[48:51], a[112:115], v[96:99]\n"
"v_mfma_f32_16x16x32_bf16 v[96:99], a[52:55], a[116:119], v[96:99]\n"
"global_load_dwordx4 a[36:39], v0, s[34:35] offset:-3072 sc1\n"
"v_mfma_f32_16x16x32_bf16 v[124:127], a[60:63], a[116:119], v[124:127]\n"
"v_mfma_f32_16x16x32_bf16 v[124:127], a[56:59], a[112:115], v[124:127]\n"
"v_mfma_f32_16x16x32_bf16 v[128:131], a[56:59], a[120:123], v[128:131]\n"
"v_mfma_f32_16x16x32_bf16 v[128:131], a[60:63], a[124:127], v[128:131]\n"
"v_mfma_f32_16x16x32_bf16 v[100:103], a[52:55], a[124:127], v[100:103]\n"
"v_mfma_f32_16x16x32_bf16 v[100:103], a[48:51], a[120:123], v[100:103]\n"
"v_mfma_f32_16x16x32_bf16 v[104:107], a[48:51], a[128:131], v[104:107]\n"
"ds_read_b128 v[24:27], v2 offset:24576\n"
"v_mfma_f32_16x16x32_bf16 v[104:107], a[52:55], a[132:135], v[104:107]\n"
"global_load_dwordx4 a[40:43], v0, s[34:35] offset:-2048 sc1\n"
"v_mfma_f32_16x16x32_bf16 v[132:135], a[60:63], a[132:135], v[132:135]\n"
"v_mfma_f32_16x16x32_bf16 v[132:135], a[56:59], a[128:131], v[132:135]\n"
"v_mfma_f32_16x16x32_bf16 v[136:139], a[56:59], a[136:139], v[136:139]\n"
"v_mfma_f32_16x16x32_bf16 v[136:139], a[60:63], a[140:143], v[136:139]\n"
"v_mfma_f32_16x16x32_bf16 v[108:111], a[52:55], a[140:143], v[108:111]\n"
"v_mfma_f32_16x16x32_bf16 v[108:111], a[48:51], a[136:139], v[108:111]\n"
"v_mfma_f32_16x16x32_bf16 v[112:115], a[48:51], a[144:147], v[112:115]\n"
"v_mfma_f32_16x16x32_bf16 v[112:115], a[52:55], a[148:151], v[112:115]\n"
"global_load_dwordx4 a[44:47], v0, s[34:35] offset:-1024 sc1\n"
"v_mfma_f32_16x16x32_bf16 v[140:143], a[60:63], a[148:151], v[140:143]\n"
"v_mfma_f32_16x16x32_bf16 v[140:143], a[56:59], a[144:147], v[140:143]\n"
"v_mfma_f32_16x16x32_bf16 a[12:15], a[56:59], a[152:155], a[12:15]\n"
"s_waitcnt lgkmcnt(0)\n"
"global_store_dwordx4 v0, v[24:27], s[54:55] offset:-4096 sc1\n"
"v_mfma_f32_16x16x32_bf16 a[12:15], a[60:63], a[156:159], a[12:15]\n"
"v_mfma_f32_16x16x32_bf16 a[8:11], a[52:55], a[156:159], a[8:11]\n"
"v_mfma_f32_16x16x32_bf16 a[8:11], a[48:51], a[152:155], a[8:11]\n"
"s_waitcnt vmcnt(13)\n"
"v_mfma_f32_16x16x32_bf16 v[144:147], a[64:67], a[96:99], v[144:147]\n"
"v_mfma_f32_16x16x32_bf16 v[144:147], a[68:71], a[100:103], v[144:147]\n"
"global_load_dwordx4 a[48:51], v0, s[34:35] offset:0 sc1\n"
"v_mfma_f32_16x16x32_bf16 v[172:175], a[76:79], a[100:103], v[172:175]\n"
"v_mfma_f32_16x16x32_bf16 v[172:175], a[72:75], a[96:99], v[172:175]\n"
"v_mfma_f32_16x16x32_bf16 v[176:179], a[72:75], a[104:107], v[176:179]\n"
"v_mfma_f32_16x16x32_bf16 v[176:179], a[76:79], a[108:111], v[176:179]\n"
"v_mfma_f32_16x16x32_bf16 v[148:151], a[68:71], a[108:111], v[148:151]\n"
"v_mfma_f32_16x16x32_bf16 v[148:151], a[64:67], a[104:107], v[148:151]\n"
"v_mfma_f32_16x16x32_bf16 v[152:155], a[64:67], a[112:115], v[152:155]\n"
"v_mfma_f32_16x16x32_bf16 v[152:155], a[68:71], a[116:119], v[152:155]\n"
"global_load_dwordx4 a[52:55], v0, s[34:35] offset:1024 sc1\n"
"v_mfma_f32_16x16x32_bf16 v[180:183], a[76:79], a[116:119], v[180:183]\n"
"v_mfma_f32_16x16x32_bf16 v[180:183], a[72:75], a[112:115], v[180:183]\n"
"v_mfma_f32_16x16x32_bf16 v[184:187], a[72:75], a[120:123], v[184:187]\n"
"v_mfma_f32_16x16x32_bf16 v[184:187], a[76:79], a[124:127], v[184:187]\n"
"v_mfma_f32_16x16x32_bf16 v[156:159], a[68:71], a[124:127], v[156:159]\n"
"v_mfma_f32_16x16x32_bf16 v[156:159], a[64:67], a[120:123], v[156:159]\n"
"ds_read_b128 v[28:31], v2 offset:26624\n"
"v_mfma_f32_16x16x32_bf16 v[160:163], a[64:67], a[128:131], v[160:163]\n"
"v_mfma_f32_16x16x32_bf16 v[160:163], a[68:71], a[132:135], v[160:163]\n"
"global_load_dwordx4 a[56:59], v0, s[34:35] offset:2048 sc1\n"
"v_mfma_f32_16x16x32_bf16 v[188:191], a[76:79], a[132:135], v[188:191]\n"
"v_mfma_f32_16x16x32_bf16 v[188:191], a[72:75], a[128:131], v[188:191]\n"
"v_mfma_f32_16x16x32_bf16 v[192:195], a[72:75], a[136:139], v[192:195]\n"
"v_mfma_f32_16x16x32_bf16 v[192:195], a[76:79], a[140:143], v[192:195]\n"
"v_mfma_f32_16x16x32_bf16 v[164:167], a[68:71], a[140:143], v[164:167]\n"
"v_mfma_f32_16x16x32_bf16 v[164:167], a[64:67], a[136:139], v[164:167]\n"
"v_mfma_f32_16x16x32_bf16 v[168:171], a[64:67], a[144:147], v[168:171]\n"
"v_mfma_f32_16x16x32_bf16 v[168:171], a[68:71], a[148:151], v[168:171]\n"
"global_load_dwordx4 a[60:63], v0, s[34:35] offset:3072 sc1\n"
"v_mfma_f32_16x16x32_bf16 v[196:199], a[76:79], a[148:151], v[196:199]\n"
"s_waitcnt lgkmcnt(0)\n"
"global_store_dwordx4 v0, v[28:31], s[54:55] offset:-2048 sc1\n"
"v_mfma_f32_16x16x32_bf16 v[196:199], a[72:75], a[144:147], v[196:199]\n"
"v_mfma_f32_16x16x32_bf16 a[20:23], a[72:75], a[152:155], a[20:23]\n"
"s_add_u32 s34, s34, 8192\n"
"v_mfma_f32_16x16x32_bf16 a[20:23], a[76:79], a[156:159], a[20:23]\n"
"v_mfma_f32_16x16x32_bf16 a[16:19], a[68:71], a[156:159], a[16:19]\n"
"v_mfma_f32_16x16x32_bf16 a[16:19], a[64:67], a[152:155], a[16:19]\n"
"s_waitcnt vmcnt(12)\n"
"v_mfma_f32_16x16x32_bf16 v[200:203], a[80:83], a[96:99], v[200:203]\n"
"v_mfma_f32_16x16x32_bf16 v[200:203], a[84:87], a[100:103], v[200:203]\n"
"global_load_dwordx4 a[64:67], v0, s[34:35] offset:-4096 sc1\n"
"v_mfma_f32_16x16x32_bf16 v[228:231], a[92:95], a[100:103], v[228:231]\n"
"ds_read_b128 a[100:103], v3 offset:17408\n"
"v_mfma_f32_16x16x32_bf16 v[228:231], a[88:91], a[96:99], v[228:231]\n"
"ds_read_b128 a[96:99], v3 offset:16384\n"
"v_mfma_f32_16x16x32_bf16 v[232:235], a[88:91], a[104:107], v[232:235]\n"
"v_mfma_f32_16x16x32_bf16 v[232:235], a[92:95], a[108:111], v[232:235]\n"
"v_mfma_f32_16x16x32_bf16 v[204:207], a[84:87], a[108:111], v[204:207]\n"
"ds_read_b128 a[108:111], v3 offset:19456\n"
"v_mfma_f32_16x16x32_bf16 v[204:207], a[80:83], a[104:107], v[204:207]\n"
"ds_read_b128 a[104:107], v3 offset:18432\n"
"v_mfma_f32_16x16x32_bf16 v[208:211], a[80:83], a[112:115], v[208:211]\n"
"v_mfma_f32_16x16x32_bf16 v[208:211], a[84:87], a[116:119], v[208:211]\n"
"global_load_dwordx4 a[68:71], v0, s[34:35] offset:-3072 sc1\n"
"v_mfma_f32_16x16x32_bf16 v[236:239], a[92:95], a[116:119], v[236:239]\n"
"ds_read_b128 a[116:119], v3 offset:21504\n"
"v_mfma_f32_16x16x32_bf16 v[236:239], a[88:91], a[112:115], v[236:239]\n"
"ds_read_b128 a[112:115], v3 offset:20480\n"
"v_mfma_f32_16x16x32_bf16 v[240:243], a[88:91], a[120:123], v[240:243]\n"
"v_mfma_f32_16x16x32_bf16 v[240:243], a[92:95], a[124:127], v[240:243]\n"
"ds_read_b128 v[24:27], v2 offset:29696\n"
"v_mfma_f32_16x16x32_bf16 v[212:215], a[84:87], a[124:127], v[212:215]\n"
"ds_read_b128 a[124:127], v3 offset:23552\n"
"v_mfma_f32_16x16x32_bf16 v[212:215], a[80:83], a[120:123], v[212:215]\n"
"ds_read_b128 a[120:123], v3 offset:22528\n"
"v_mfma_f32_16x16x32_bf16 v[216:219], a[80:83], a[128:131], v[216:219]\n"
"v_mfma_f32_16x16x32_bf16 v[216:219], a[84:87], a[132:135], v[216:219]\n"
"global_load_dwordx4 a[72:75], v0, s[34:35] offset:-2048 sc1\n"
"v_mfma_f32_16x16x32_bf16 v[244:247], a[92:95], a[132:135], v[244:247]\n"
"ds_read_b128 a[132:135], v3 offset:25600\n"
"v_mfma_f32_16x16x32_bf16 v[244:247], a[88:91], a[128:131], v[244:247]\n"
"ds_read_b128 a[128:131], v3 offset:24576\n"
"v_mfma_f32_16x16x32_bf16 v[248:251], a[88:91], a[136:139], v[248:251]\n"
"v_subrev_u32 v8, s65, v8\n"
"v_mfma_f32_16x16x32_bf16 v[248:251], a[92:95], a[140:143], v[248:251]\n"
"v_xor_b32 v9, s79, v9\n"
"v_mfma_f32_16x16x32_bf16 v[220:223], a[84:87], a[140:143], v[220:223]\n"
"ds_read_b128 a[140:143], v3 offset:27648\n"
"v_or_b32 v8, v8, v9\n"
"v_mfma_f32_16x16x32_bf16 v[220:223], a[80:83], a[136:139], v[220:223]\n"
"ds_read_b128 a[136:139], v3 offset:26624\n"
"v_cmp_lt_u32 vcc, 15, v8\n"
"v_mfma_f32_16x16x32_bf16 v[224:227], a[80:83], a[144:147], v[224:227]\n"
"s_waitcnt lgkmcnt(6)\n"
"global_store_dwordx4 v0, v[24:27], s[54:55] offset:1024 sc1\n"
"s_cbranch_vccnz L_slow0\n"
"L_back0:\n"
"v_mfma_f32_16x16x32_bf16 v[224:227], a[84:87], a[148:151], v[224:227]\n"
"global_load_dwordx4 a[76:79], v0, s[34:35] offset:-1024 sc1\n"
"v_readfirstlane_b32 s66, v9\n"
"v_mfma_f32_16x16x32_bf16 v[252:255], a[92:95], a[148:151], v[252:255]\n"
"ds_read_b128 a[148:151], v3 offset:29696\n"
"v_mfma_f32_16x16x32_bf16 v[252:255], a[88:91], a[144:147], v[252:255]\n"
"s_and_b32 s66, s66, 3\n"
"ds_read_b128 a[144:147], v3 offset:28672\n"
"v_mfma_f32_16x16x32_bf16 a[28:31], a[88:91], a[152:155], a[28:31]\n"
"s_lshl_b32 s69, s66, 13\n"
"v_mfma_f32_16x16x32_bf16 a[28:31], a[92:95], a[156:159], a[28:31]\n"
"s_cmp_eq_u32 s66, 3\n"
"s_cselect_b32 s75, 8192, 0\n"
"v_mfma_f32_16x16x32_bf16 a[24:27], a[84:87], a[156:159], a[24:27]\n"
"ds_read_b128 a[156:159], v3 offset:31744\n"
"v_mfma_f32_16x16x32_bf16 a[24:27], a[80:83], a[152:155], a[24:27]\n"
"s_waitcnt vmcnt(11)\n"
"ds_read_b128 a[152:155], v3 offset:30720\n"
"v_mfma_f32_16x16x32_bf16 v[32:35], a[32:35], a[96:99], v[32:35]\n"
"v_mfma_f32_16x16x32_bf16 v[32:35], a[36:39], a[100:103], v[32:35]\n"
"global_load_dwordx4 a[80:83], v0, s[34:35] offset:0 sc1\n"
"v_mfma_f32_16x16x32_bf16 v[60:63], a[44:47], a[100:103], v[60:63]\n"
"v_mfma_f32_16x16x32_bf16 v[60:63], a[40:43], a[96:99], v[60:63]\n"
"v_mfma_f32_16x16x32_bf16 v[64:67], a[40:43], a[104:107], v[64:67]\n"
"v_mfma_f32_16x16x32_bf16 v[64:67], a[44:47], a[108:111], v[64:67]\n"
"v_mfma_f32_16x16x32_bf16 v[36:39], a[36:39], a[108:111], v[36:39]\n"
"s_add_u32 m0, s64, 0\n"
"v_mfma_f32_16x16x32_bf16 v[36:39], a[32:35], a[104:107], v[36:39]\n"
"s_waitcnt lgkmcnt(8)\n"
"v_mfma_f32_16x16x32_bf16 v[40:43], a[32:35], a[112:115], v[40:43]\n"
"v_mfma_f32_16x16x32_bf16 v[40:43], a[36:39], a[116:119], v[40:43]\n"
"global_load_dwordx4 a[84:87], v0, s[34:35] offset:1024 sc1\n"
"v_mfma_f32_16x16x32_bf16 v[68:71], a[44:47], a[116:119], v[68:71]\n"
"v_mfma_f32_16x16x32_bf16 v[68:71], a[40:43], a[112:115], v[68:71]\n"
"v_mfma_f32_16x16x32_bf16 v[72:75], a[40:43], a[120:123], v[72:75]\n"
"v_mfma_f32_16x16x32_bf16 v[72:75], a[44:47], a[124:127], v[72:75]\n"
"v_mfma_f32_16x16x32_bf16 v[44:47], a[36:39], a[124:127], v[44:47]\n"
"v_mfma_f32_16x16x32_bf16 v[44:47], a[32:35], a[120:123], v[44:47]\n"
"s_waitcnt lgkmcnt(4)\n"
"v_mfma_f32_16x16x32_bf16 v[48:51], a[32:35], a[128:131], v[48:51]\n"
"v_mfma_f32_16x16x32_bf16 v[48:51], a[36:39], a[132:135], v[48:51]\n"
"global_load_dwordx4 a[88:91], v0, s[34:35] offset:2048 sc1\n"
"v_mfma_f32_16x16x32_bf16 v[76:79], a[44:47], a[132:135], v[76:79]\n"
"v_mfma_f32_16x16x32_bf16 v[76:79], a[40:43], a[128:131], v[76:79]\n"
"v_mfma_f32_16x16x32_bf16 v[80:83], a[40:43], a[136:139], v[80:83]\n"
"v_mfma_f32_16x16x32_bf16 v[80:83], a[44:47], a[140:143], v[80:83]\n"
"buffer_load_dwordx4 v0, s[48:51], s75 offen offset:0 sc1 lds\n"
"v_mfma_f32_16x16x32_bf16 v[52:55], a[36:39], a[140:143], v[52:55]\n"
"v_mfma_f32_16x16x32_bf16 v[52:55], a[32:35], a[136:139], v[52:55]\n"
"s_waitcnt lgkmcnt(0)\n"
"v_mfma_f32_16x16x32_bf16 v[56:59], a[32:35], a[144:147], v[56:59]\n"
"v_mfma_f32_16x16x32_bf16 v[56:59], a[36:39], a[148:151], v[56:59]\n"
"global_load_dwordx4 a[92:95], v0, s[34:35] offset:3072 sc1\n"
"v_mfma_f32_16x16x32_bf16 v[84:87], a[44:47], a[148:151], v[84:87]\n"
"v_mfma_f32_16x16x32_bf16 v[84:87], a[40:43], a[144:147], v[84:87]\n"
"v_mfma_f32_16x16x32_bf16 a[4:7], a[40:43], a[152:155], a[4:7]\n"
"s_add_u32 s34, s34, 8192\n"
"v_mfma_f32_16x16x32_bf16 a[4:7], a[44:47], a[156:159], a[4:7]\n"
"v_mfma_f32_16x16x32_bf16 a[0:3], a[36:39], a[156:159], a[0:3]\n"
"v_mfma_f32_16x16x32_bf16 a[0:3], a[32:35], a[152:155], a[0:3]\n"
"s_waitcnt vmcnt(11)\n"
"v_mfma_f32_16x16x32_bf16 v[88:91], a[48:51], a[96:99], v[88:91]\n"
"v_mfma_f32_16x16x32_bf16 v[88:91], a[52:55], a[100:103], v[88:91]\n"
"global_load_dwordx4 a[32:35], v0, s[34:35] offset:-4096 sc1\n"
"v_mfma_f32_16x16x32_bf16 v[116:119], a[60:63], a[100:103], v[116:119]\n"
"v_mfma_f32_16x16x32_bf16 v[116:119], a[56:59], a[96:99], v[116:119]\n"
"buffer_load_dwordx4 v0, s[48:51], s75 offen offset:1024 sc1 lds\n"
"v_mfma_f32_16x16x32_bf16 v[120:123], a[56:59], a[104:107], v[120:123]\n"
"v_mfma_f32_16x16x32_bf16 v[120:123], a[60:63], a[108:111], v[120:123]\n"
"v_mfma_f32_16x16x32_bf16 v[92:95], a[52:55], a[108:111], v[92:95]\n"
"v_mfma_f32_16x16x32_bf16 v[92:95], a[48:51], a[104:107], v[92:95]\n"
"v_mfma_f32_16x16x32_bf16 v[96:99], a[48:51], a[112:115], v[96:99]\n"
"v_mfma_f32_16x16x32_bf16 v[96:99], a[52:55], a[116:119], v[96:99]\n"
"global_load_dwordx4 a[36:39], v0, s[34:35] offset:-3072 sc1\n"
"v_mfma_f32_16x16x32_bf16 v[124:127], a[60:63], a[116:119], v[124:127]\n"
"v_mfma_f32_16x16x32_bf16 v[124:127], a[56:59], a[112:115], v[124:127]\n"
"v_mfma_f32_16x16x32_bf16 v[128:131], a[56:59], a[120:123], v[128:131]\n"
"v_mfma_f32_16x16x32_bf16 v[128:131], a[60:63], a[124:127], v[128:131]\n"
"v_mfma_f32_16x16x32_bf16 v[100:103], a[52:55], a[124:127], v[100:103]\n"
"v_mfma_f32_16x16x32_bf16 v[100:103], a[48:51], a[120:123], v[100:103]\n"
"v_mfma_f32_16x16x32_bf16 v[104:107], a[48:51], a[128:131], v[104:107]\n"
"v_mfma_f32_16x16x32_bf16 v[104:107], a[52:55], a[132:135], v[104:107]\n"
"global_load_dwordx4 a[40:43], v0, s[34:35] offset:-2048 sc1\n"
"v_mfma_f32_16x16x32_bf16 v[132:135], a[60:63], a[132:135], v[132:135]\n"
"buffer_load_dwordx4 v0, s[48:51], s75 offen offset:2048 sc1 lds\n"
"v_mfma_f32_16x16x32_bf16 v[132:135], a[56:59], a[128:131], v[132:135]\n"
"v_mfma_f32_16x16x32_bf16 v[136:139], a[56:59], a[136:139], v[136:139]\n"
"v_mfma_f32_16x16x32_bf16 v[136:139], a[60:63], a[140:143], v[136:139]\n"
"v_mfma_f32_16x16x32_bf16 v[108:111], a[52:55], a[140:143], v[108:111]\n"
"v_mfma_f32_16x16x32_bf16 v[108:111], a[48:51], a[136:139], v[108:111]\n"
"v_mfma_f32_16x16x32_bf16 v[112:115], a[48:51], a[144:147], v[112:115]\n"
"v_mfma_f32_16x16x32_bf16 v[112:115], a[52:55], a[148:151], v[112:115]\n"
"global_load_dwordx4 a[44:47], v0, s[34:35] offset:-1024 sc1\n"
"v_mfma_f32_16x16x32_bf16 v[140:143], a[60:63], a[148:151], v[140:143]\n"
"v_mfma_f32_16x16x32_bf16 v[140:143], a[56:59], a[144:147], v[140:143]\n"
"v_mfma_f32_16x16x32_bf16 a[12:15], a[56:59], a[152:155], a[12:15]\n"
"v_mfma_f32_16x16x32_bf16 a[12:15], a[60:63], a[156:159], a[12:15]\n"
"v_mfma_f32_16x16x32_bf16 a[8:11], a[52:55], a[156:159], a[8:11]\n"
"v_mfma_f32_16x16x32_bf16 a[8:11], a[48:51], a[152:155], a[8:11]\n"
"s_waitcnt vmcnt(11)\n"
"v_mfma_f32_16x16x32_bf16 v[144:147], a[64:67], a[96:99], v[144:147]\n"
"buffer_load_dwordx4 v0, s[48:51], s75 offen offset:3072 sc1 lds\n"
"v_mfma_f32_16x16x32_bf16 v[144:147], a[68:71], a[100:103], v[144:147]\n"
"global_load_dwordx4 a[48:51], v0, s[34:35] offset:0 sc1\n"
"v_mfma_f32_16x16x32_bf16 v[172:175], a[76:79], a[100:103], v[172:175]\n"
"v_mfma_f32_16x16x32_bf16 v[172:175], a[72:75], a[96:99], v[172:175]\n"
"v_mfma_f32_16x16x32_bf16 v[176:179], a[72:75], a[104:107], v[176:179]\n"
"v_mfma_f32_16x16x32_bf16 v[176:179], a[76:79], a[108:111], v[176:179]\n"
"v_mfma_f32_16x16x32_bf16 v[148:151], a[68:71], a[108:111], v[148:151]\n"
"v_mfma_f32_16x16x32_bf16 v[148:151], a[64:67], a[104:107], v[148:151]\n"
"v_mfma_f32_16x16x32_bf16 v[152:155], a[64:67], a[112:115], v[152:155]\n"
"v_mfma_f32_16x16x32_bf16 v[152:155], a[68:71], a[116:119], v[152:155]\n"
"global_load_dwordx4 a[52:55], v0, s[34:35] offset:1024 sc1\n"
"v_mfma_f32_16x16x32_bf16 v[180:183], a[76:79], a[116:119], v[180:183]\n"
"v_mfma_f32_16x16x32_bf16 v[180:183], a[72:75], a[112:115], v[180:183]\n"
"v_mfma_f32_16x16x32_bf16 v[184:187], a[72:75], a[120:123], v[184:187]\n"
"v_mfma_f32_16x16x32_bf16 v[184:187], a[76:79], a[124:127], v[184:187]\n"
"v_mfma_f32_16x16x32_bf16 v[156:159], a[68:71], a[124:127], v[156:159]\n"
"v_mfma_f32_16x16x32_bf16 v[156:159], a[64:67], a[120:123], v[156:159]\n"
"s_add_u32 m0, s64, 4096\n"
"v_mfma_f32_16x16x32_bf16 v[160:163], a[64:67], a[128:131], v[160:163]\n"
"v_mfma_f32_16x16x32_bf16 v[160:163], a[68:71], a[132:135], v[160:163]\n"
"global_load_dwordx4 a[56:59], v0, s[34:35] offset:2048 sc1\n"
"v_mfma_f32_16x16x32_bf16 v[188:191], a[76:79], a[132:135], v[188:191]\n"
"v_mfma_f32_16x16x32_bf16 v[188:191], a[72:75], a[128:131], v[188:191]\n"
"v_mfma_f32_16x16x32_bf16 v[192:195], a[72:75], a[136:139], v[192:195]\n"
"v_mfma_f32_16x16x32_bf16 v[192:195], a[76:79], a[140:143], v[192:195]\n"
"v_mfma_f32_16x16x32_bf16 v[164:167], a[68:71], a[140:143], v[164:167]\n"
"v_mfma_f32_16x16x32_bf16 v[164:167], a[64:67], a[136:139], v[164:167]\n"
"v_mfma_f32_16x16x32_bf16 v[168:171], a[64:67], a[144:147], v[168:171]\n"
"v_mfma_f32_16x16x32_bf16 v[168:171], a[68:71], a[148:151], v[168:171]\n"
"global_load_dwordx4 a[60:63], v0, s[34:35] offset:3072 sc1\n"
"v_mfma_f32_16x16x32_bf16 v[196:199], a[76:79], a[148:151], v[196:199]\n"
"v_mfma_f32_16x16x32_bf16 v[196:199], a[72:75], a[144:147], v[196:199]\n"
"v_mfma_f32_16x16x32_bf16 a[20:23], a[72:75], a[152:155], a[20:23]\n"
"s_add_u32 s34, s34, 8192\n"
"v_mfma_f32_16x16x32_bf16 a[20:23], a[76:79], a[156:159], a[20:23]\n"
"v_mfma_f32_16x16x32_bf16 a[16:19], a[68:71], a[156:159], a[16:19]\n"
"buffer_load_dwordx4 v1, s[48:51], s75 offen offset:0 sc1 lds\n"
"v_mfma_f32_16x16x32_bf16 a[16:19], a[64:67], a[152:155], a[16:19]\n"
"s_waitcnt vmcnt(12)\n"
"v_mfma_f32_16x16x32_bf16 v[200:203], a[80:83], a[96:99], v[200:203]\n"
"v_mfma_f32_16x16x32_bf16 v[200:203], a[84:87], a[100:103], v[200:203]\n"
"global_load_dwordx4 a[64:67], v0, s[34:35] offset:-4096 sc1\n"
"v_mfma_f32_16x16x32_bf16 v[228:231], a[92:95], a[100:103], v[228:231]\n"
"ds_read_b128 a[100:103], v4 offset:17408\n"
"v_mfma_f32_16x16x32_bf16 v[228:231], a[88:91], a[96:99], v[228:231]\n"
"ds_read_b128 a[96:99], v4 offset:16384\n"
"v_mfma_f32_16x16x32_bf16 v[232:235], a[88:91], a[104:107], v[232:235]\n"
"v_mfma_f32_16x16x32_bf16 v[232:235], a[92:95], a[108:111], v[232:235]\n"
"v_mfma_f32_16x16x32_bf16 v[204:207], a[84:87], a[108:111], v[204:207]\n"
"ds_read_b128 a[108:111], v4 offset:19456\n"
"v_mfma_f32_16x16x32_bf16 v[204:207], a[80:83], a[104:107], v[204:207]\n"
"ds_read_b128 a[104:107], v4 offset:18432\n"
"v_mfma_f32_16x16x32_bf16 v[208:211], a[80:83], a[112:115], v[208:211]\n"
"v_mfma_f32_16x16x32_bf16 v[208:211], a[84:87], a[116:119], v[208:211]\n"
"global_load_dwordx4 a[68:71], v0, s[34:35] offset:-3072 sc1\n"
"v_mfma_f32_16x16x32_bf16 v[236:239], a[92:95], a[116:119], v[236:239]\n"
"ds_read_b128 a[116:119], v4 offset:21504\n"
"v_mfma_f32_16x16x32_bf16 v[236:239], a[88:91], a[112:115], v[236:239]\n"
"ds_read_b128 a[112:115], v4 offset:20480\n"
"v_mfma_f32_16x16x32_bf16 v[240:243], a[88:91], a[120:123], v[240:243]\n"
"v_mfma_f32_16x16x32_bf16 v[240:243], a[92:95], a[124:127], v[240:243]\n"
"buffer_load_dwordx4 v1, s[48:51], s75 offen offset:1024 sc1 lds\n"
"v_mfma_f32_16x16x32_bf16 v[212:215], a[84:87], a[124:127], v[212:215]\n"
"ds_read_b128 a[124:127], v4 offset:23552\n"
"v_mfma_f32_16x16x32_bf16 v[212:215], a[80:83], a[120:123], v[212:215]\n"
"ds_read_b128 a[120:123], v4 offset:22528\n"
"v_mfma_f32_16x16x32_bf16 v[216:219], a[80:83], a[128:131], v[216:219]\n"
"v_mfma_f32_16x16x32_bf16 v[216:219], a[84:87], a[132:135], v[216:219]\n"
"global_load_dwordx4 a[72:75], v0, s[34:35] offset:-2048 sc1\n"
"v_mfma_f32_16x16x32_bf16 v[244:247], a[92:95], a[132:135], v[244:247]\n"
"ds_read_b128 a[132:135], v4 offset:25600\n"
"v_mfma_f32_16x16x32_bf16 v[244:247], a[88:91], a[128:131], v[244:247]\n"
"ds_read_b128 a[128:131], v4 offset:24576\n"
"v_mfma_f32_16x16x32_bf16 v[248:251], a[88:91], a[136:139], v[248:251]\n"
"v_mfma_f32_16x16x32_bf16 v[248:251], a[92:95], a[140:143], v[248:251]\n"
"v_mfma_f32_16x16x32_bf16 v[220:223], a[84:87], a[140:143], v[220:223]\n"
"ds_read_b128 a[140:143], v4 offset:27648\n"
"v_mfma_f32_16x16x32_bf16 v[220:223], a[80:83], a[136:139], v[220:223]\n"
"ds_read_b128 a[136:139], v4 offset:26624\n"
"v_mfma_f32_16x16x32_bf16 v[224:227], a[80:83], a[144:147], v[224:227]\n"
"v_mfma_f32_16x16x32_bf16 v[224:227], a[84:87], a[148:151], v[224:227]\n"
"global_load_dwordx4 a[76:79], v0, s[34:35] offset:-1024 sc1\n"
"v_mfma_f32_16x16x32_bf16 v[252:255], a[92:95], a[148:151], v[252:255]\n"
"ds_read_b128 a[148:151], v4 offset:29696\n"
"v_mfma_f32_16x16x32_bf16 v[252:255], a[88:91], a[144:147], v[252:255]\n"
"ds_read_b128 a[144:147], v4 offset:28672\n"
"v_mfma_f32_16x16x32_bf16 a[28:31], a[88:91], a[152:155], a[28:31]\n"
"buffer_load_dwordx4 v1, s[48:51], s75 offen offset:2048 sc1 lds\n"
"v_mfma_f32_16x16x32_bf16 a[28:31], a[92:95], a[156:159], a[28:31]\n"
"v_mfma_f32_16x16x32_bf16 a[24:27], a[84:87], a[156:159], a[24:27]\n"
"ds_read_b128 a[156:159], v4 offset:31744\n"
"v_mfma_f32_16x16x32_bf16 a[24:27], a[80:83], a[152:155], a[24:27]\n"
"s_waitcnt vmcnt(12) lgkmcnt(11)\n"
"ds_read_b128 a[152:155], v4 offset:30720\n"
"v_mfma_f32_16x16x32_bf16 v[32:35], a[32:35], a[96:99], v[32:35]\n"
"v_mfma_f32_16x16x32_bf16 v[32:35], a[36:39], a[100:103], v[32:35]\n"
"global_load_dwordx4 a[80:83], v0, s[34:35] offset:0 sc1\n"
"v_mfma_f32_16x16x32_bf16 v[60:63], a[44:47], a[100:103], v[60:63]\n"
"v_mfma_f32_16x16x32_bf16 v[60:63], a[40:43], a[96:99], v[60:63]\n"
"v_mfma_f32_16x16x32_bf16 v[64:67], a[40:43], a[104:107], v[64:67]\n"
"v_mfma_f32_16x16x32_bf16 v[64:67], a[44:47], a[108:111], v[64:67]\n"
"v_mfma_f32_16x16x32_bf16 v[36:39], a[36:39], a[108:111], v[36:39]\n"
"v_mfma_f32_16x16x32_bf16 v[36:39], a[32:35], a[104:107], v[36:39]\n"
"s_waitcnt lgkmcnt(8)\n"
"v_mfma_f32_16x16x32_bf16 v[40:43], a[32:35], a[112:115], v[40:43]\n"
"v_mfma_f32_16x16x32_bf16 v[40:43], a[36:39], a[116:119], v[40:43]\n"
"global_load_dwordx4 a[84:87], v0, s[34:35] offset:1024 sc1\n"
"s_add_u32 s65, s65, 1\n"
"v_mfma_f32_16x16x32_bf16 v[68:71], a[44:47], a[116:119], v[68:71]\n"
"buffer_load_dwordx4 v1, s[48:51], s75 offen offset:3072 sc1 lds\n"
"v_mfma_f32_16x16x32_bf16 v[68:71], a[40:43], a[112:115], v[68:71]\n"
"v_mov_b32 v8, s65\n"
"v_mfma_f32_16x16x32_bf16 v[72:75], a[40:43], a[120:123], v[72:75]\n"
"v_mov_b32 v9, s60\n"
"v_mfma_f32_16x16x32_bf16 v[72:75], a[44:47], a[124:127], v[72:75]\n"
"v_mfma_f32_16x16x32_bf16 v[44:47], a[36:39], a[124:127], v[44:47]\n"
"buffer_store_dwordx2 v[8:9], off, s[56:59], %13 sc1\n"
"v_mfma_f32_16x16x32_bf16 v[44:47], a[32:35], a[120:123], v[44:47]\n"
"s_waitcnt lgkmcnt(4)\n"
"v_mfma_f32_16x16x32_bf16 v[48:51], a[32:35], a[128:131], v[48:51]\n"
"v_mfma_f32_16x16x32_bf16 v[48:51], a[36:39], a[132:135], v[48:51]\n"
"global_load_dwordx4 a[88:91], v0, s[34:35] offset:2048 sc1\n"
"v_mfma_f32_16x16x32_bf16 v[76:79], a[44:47], a[132:135], v[76:79]\n"
"v_mfma_f32_16x16x32_bf16 v[76:79], a[40:43], a[128:131], v[76:79]\n"
"v_mfma_f32_16x16x32_bf16 v[80:83], a[40:43], a[136:139], v[80:83]\n"
"v_mfma_f32_16x16x32_bf16 v[80:83], a[44:47], a[140:143], v[80:83]\n"
"v_mfma_f32_16x16x32_bf16 v[52:55], a[36:39], a[140:143], v[52:55]\n"
"v_mfma_f32_16x16x32_bf16 v[52:55], a[32:35], a[136:139], v[52:55]\n"
"s_waitcnt lgkmcnt(0)\n"
"v_mfma_f32_16x16x32_bf16 v[56:59], a[32:35], a[144:147], v[56:59]\n"
"v_mfma_f32_16x16x32_bf16 v[56:59], a[36:39], a[148:151], v[56:59]\n"
"s_add_u32 m0, s64, 8192\n"
"global_load_dwordx4 a[92:95], v0, s[34:35] offset:3072 sc1\n"
"v_mfma_f32_16x16x32_bf16 v[84:87], a[44:47], a[148:151], v[84:87]\n"
"v_mfma_f32_16x16x32_bf16 v[84:87], a[40:43], a[144:147], v[84:87]\n"
"v_mfma_f32_16x16x32_bf16 a[4:7], a[40:43], a[152:155], a[4:7]\n"
"s_add_u32 s34, s34, 8192\n"
"v_mfma_f32_16x16x32_bf16 a[4:7], a[44:47], a[156:159], a[4:7]\n"
"v_mfma_f32_16x16x32_bf16 a[0:3], a[36:39], a[156:159], a[0:3]\n"
"v_mfma_f32_16x16x32_bf16 a[0:3], a[32:35], a[152:155], a[0:3]\n"
"s_waitcnt vmcnt(13)\n"
"v_mfma_f32_16x16x32_bf16 v[88:91], a[48:51], a[96:99], v[88:91]\n"
"v_mfma_f32_16x16x32_bf16 v[88:91], a[52:55], a[100:103], v[88:91]\n"
"global_load_dwordx4 a[32:35], v0, s[34:35] offset:-4096 sc1\n"
"v_mfma_f32_16x16x32_bf16 v[116:119], a[60:63], a[100:103], v[116:119]\n"
"v_mfma_f32_16x16x32_bf16 v[116:119], a[56:59], a[96:99], v[116:119]\n"
"v_mfma_f32_16x16x32_bf16 v[120:123], a[56:59], a[104:107], v[120:123]\n"
"v_mfma_f32_16x16x32_bf16 v[120:123], a[60:63], a[108:111], v[120:123]\n"
"v_mfma_f32_16x16x32_bf16 v[92:95], a[52:55], a[108:111], v[92:95]\n"
"v_mfma_f32_16x16x32_bf16 v[92:95], a[48:51], a[104:107], v[92:95]\n"
"v_mfma_f32_16x16x32_bf16 v[96:99], a[48:51], a[112:115], v[96:99]\n"
"buffer_load_dwordx4 v0, s[48:51], s69 offen offset:0 sc1 lds\n"
"v_mfma_f32_16x16x32_bf16 v[96:99], a[52:55], a[116:119], v[96:99]\n"
"global_load_dwordx4 a[36:39], v0, s[34:35] offset:-3072 sc1\n"
"v_mfma_f32_16x16x32_bf16 v[124:127], a[60:63], a[116:119], v[124:127]\n"
"v_mfma_f32_16x16x32_bf16 v[124:127], a[56:59], a[112:115], v[124:127]\n"
"v_mfma_f32_16x16x32_bf16 v[128:131], a[56:59], a[120:123], v[128:131]\n"
"v_mfma_f32_16x16x32_bf16 v[128:131], a[60:63], a[124:127], v[128:131]\n"
"v_mfma_f32_16x16x32_bf16 v[100:103], a[52:55], a[124:127], v[100:103]\n"
"v_mfma_f32_16x16x32_bf16 v[100:103], a[48:51], a[120:123], v[100:103]\n"
"v_mfma_f32_16x16x32_bf16 v[104:107], a[48:51], a[128:131], v[104:107]\n"
"v_mfma_f32_16x16x32_bf16 v[104:107], a[52:55], a[132:135], v[104:107]\n"
"global_load_dwordx4 a[40:43], v0, s[34:35] offset:-2048 sc1\n"
"v_mfma_f32_16x16x32_bf16 v[132:135], a[60:63], a[132:135], v[132:135]\n"
"v_mfma_f32_16x16x32_bf16 v[132:135], a[56:59], a[128:131], v[132:135]\n"
"v_mfma_f32_16x16x32_bf16 v[136:139], a[56:59], a[136:139], v[136:139]\n"
"v_mfma_f32_16x16x32_bf16 v[136:139], a[60:63], a[140:143], v[136:139]\n"
"v_mfma_f32_16x16x32_bf16 v[108:111], a[52:55], a[140:143], v[108:111]\n"
"buffer_load_dwordx4 v0, s[48:51], s69 offen offset:1024 sc1 lds\n"
"v_mfma_f32_16x16x32_bf16 v[108:111], a[48:51], a[136:139], v[108:111]\n"
"v_mfma_f32_16x16x32_bf16 v[112:115], a[48:51], a[144:147], v[112:115]\n"
"v_mfma_f32_16x16x32_bf16 v[112:115], a[52:55], a[148:151], v[112:115]\n"
"global_load_dwordx4 a[44:47], v0, s[34:35] offset:-1024 sc1\n"
"v_mfma_f32_16x16x32_bf16 v[140:143], a[60:63], a[148:151], v[140:143]\n"
"v_mfma_f32_16x16x32_bf16 v[140:143], a[56:59], a[144:147], v[140:143]\n"
"v_mfma_f32_16x16x32_bf16 a[12:15], a[56:59], a[152:155], a[12:15]\n"
"v_mfma_f32_16x16x32_bf16 a[12:15], a[60:63], a[156:159], a[12:15]\n"
"v_mfma_f32_16x16x32_bf16 a[8:11], a[52:55], a[156:159], a[8:11]\n"
"v_mfma_f32_16x16x32_bf16 a[8:11], a[48:51], a[152:155], a[8:11]\n"
"s_waitcnt vmcnt(13)\n"
"v_mfma_f32_16x16x32_bf16 v[144:147], a[64:67], a[96:99], v[144:147]\n"
"v_mfma_f32_16x16x32_bf16 v[144:147], a[68:71], a[100:103], v[144:147]\n"
"global_load_dwordx4 a[48:51], v0, s[34:35] offset:0 sc1\n"
"v_mfma_f32_16x16x32_bf16 v[172:175], a[76:79], a[100:103], v[172:175]\n"
"v_mfma_f32_16x16x32_bf16 v[172:175], a[72:75], a[96:99], v[172:175]\n"
"v_mfma_f32_16x16x32_bf16 v[176:179], a[72:75], a[104:107], v[176:179]\n"
"v_mfma_f32_16x16x32_bf16 v[176:179], a[76:79], a[108:111], v[176:179]\n"
"buffer_load_dwordx4 v0, s[48:51], s69 offen offset:2048 sc1 lds\n"
"v_mfma_f32_16x16x32_bf16 v[148:151], a[68:71], a[108:111], v[148:151]\n"
"v_mfma_f32_16x16x32_bf16 v[148:151], a[64:67], a[104:107], v[148:151]\n"
"v_mfma_f32_16x16x32_bf16 v[152:155], a[64:67], a[112:115], v[152:155]\n"
"v_lshlrev_b32 v16, 16, v18\n"
"v_mfma_f32_16x16x32_bf16 v[152:155], a[68:71], a[116:119], v[152:155]\n"
"global_load_dwordx4 a[52:55], v0, s[34:35] offset:1024 sc1\n"
"v_and_b32 v17, s63, v18\n"
"v_mfma_f32_16x16x32_bf16 v[180:183], a[76:79], a[116:119], v[180:183]\n"
"v_lshlrev_b32 v18, 16, v19\n"
"v_mfma_f32_16x16x32_bf16 v[180:183], a[72:75], a[112:115], v[180:183]\n"
"v_and_b32 v19, s63, v19\n"
"v_mfma_f32_16x16x32_bf16 v[184:187], a[72:75], a[120:123], v[184:187]\n"
"v_add_f32 v32, v32, v16\n"
"v_mfma_f32_16x16x32_bf16 v[184:187], a[76:79], a[124:127], v[184:187]\n"
"v_add_f32 v33, v33, v17\n"
"v_add_f32 v34, v34, v18\n"
"v_mfma_f32_16x16x32_bf16 v[156:159], a[68:71], a[124:127], v[156:159]\n"
"v_add_f32 v35, v35, v19\n"
"v_mfma_f32_16x16x32_bf16 v[156:159], a[64:67], a[120:123], v[156:159]\n"
"v_add_f32 v36, v36, v16\n"
"v_mfma_f32_16x16x32_bf16 v[160:163], a[64:67], a[128:131], v[160:163]\n"
"v_add_f32 v37, v37, v17\n"
"v_mfma_f32_16x16x32_bf16 v[160:163], a[68:71], a[132:135], v[160:163]\n"
"global_load_dwordx4 a[56:59], v0, s[34:35] offset:2048 sc1\n"
"v_add_f32 v38, v38, v18\n"
"v_mfma_f32_16x16x32_bf16 v[188:191], a[76:79], a[132:135], v[188:191]\n"
"v_add_f32 v39, v39, v19\n"
"v_mfma_f32_16x16x32_bf16 v[188:191], a[72:75], a[128:131], v[188:191]\n"
"v_add_f32 v40, v40, v16\n"
"v_add_f32 v41, v41, v17\n"
"v_mfma_f32_16x16x32_bf16 v[192:195], a[72:75], a[136:139], v[192:195]\n"
"buffer_load_dwordx4 v0, s[48:51], s69 offen offset:3072 sc1 lds\n"
"v_mfma_f32_16x16x32_bf16 v[192:195], a[76:79], a[140:143], v[192:195]\n"
"v_add_f32 v42, v42, v18\n"
"v_mfma_f32_16x16x32_bf16 v[164:167], a[68:71], a[140:143], v[164:167]\n"
"v_add_f32 v43, v43, v19\n"
"v_mfma_f32_16x16x32_bf16 v[164:167], a[64:67], a[136:139], v[164:167]\n"
"v_add_f32 v44, v44, v16\n"
"v_add_f32 v45, v45, v17\n"
"v_mfma_f32_16x16x32_bf16 v[168:171], a[64:67], a[144:147], v[168:171]\n"
"v_add_f32 v46, v46, v18\n"
"v_mfma_f32_16x16x32_bf16 v[168:171], a[68:71], a[148:151], v[168:171]\n"
"global_load_dwordx4 a[60:63], v0, s[34:35] offset:3072 sc1\n"
"v_add_f32 v47, v47, v19\n"
"v_mfma_f32_16x16x32_bf16 v[196:199], a[76:79], a[148:151], v[196:199]\n"
"v_add_f32 v48, v48, v16\n"
"v_mfma_f32_16x16x32_bf16 v[196:199], a[72:75], a[144:147], v[196:199]\n"
"v_add_f32 v49, v49, v17\n"
"v_mfma_f32_16x16x32_bf16 a[20:23], a[72:75], a[152:155], a[20:23]\n"
"s_add_u32 s34, s34, 8192\n"
"v_add_f32 v50, v50, v18\n"
"v_mfma_f32_16x16x32_bf16 a[20:23], a[76:79], a[156:159], a[20:23]\n"
"v_add_f32 v51, v51, v19\n"
"v_mfma_f32_16x16x32_bf16 a[16:19], a[68:71], a[156:159], a[16:19]\n"
"v_add_f32 v52, v52, v16\n"
"v_mfma_f32_16x16x32_bf16 a[16:19], a[64:67], a[152:155], a[16:19]\n"
"s_waitcnt vmcnt(12)\n"
"v_add_f32 v53, v53, v17\n"
"v_mfma_f32_16x16x32_bf16 v[200:203], a[80:83], a[96:99], v[200:203]\n"
"v_add_f32 v54, v54, v18\n"
"v_add_f32 v55, v55, v19\n"
"v_mfma_f32_16x16x32_bf16 v[200:203], a[84:87], a[100:103], v[200:203]\n"
"global_load_dwordx4 a[64:67], v0, s[34:35] offset:-4096 sc1\n"
"v_add_f32 v56, v56, v16\n"
"v_mfma_f32_16x16x32_bf16 v[228:231], a[92:95], a[100:103], v[228:231]\n"
"s_add_u32 m0, s64, 12288\n"
"ds_read_b128 a[100:103], v5 offset:17408\n"
"v_add_f32 v57, v57, v17\n"
"v_mfma_f32_16x16x32_bf16 v[228:231], a[88:91], a[96:99], v[228:231]\n"
"ds_read_b128 a[96:99], v5 offset:16384\n"
"v_add_f32 v58, v58, v18\n"
"v_mfma_f32_16x16x32_bf16 v[232:235], a[88:91], a[104:107], v[232:235]\n"
"v_add_f32 v59, v59, v19\n"
"v_mfma_f32_16x16x32_bf16 v[232:235], a[92:95], a[108:111], v[232:235]\n"
"v_accvgpr_read_b32 v12, a0\n"
"v_mfma_f32_16x16x32_bf16 v[204:207], a[84:87], a[108:111], v[204:207]\n"
"ds_read_b128 a[108:111], v5 offset:19456\n"
"v_accvgpr_read_b32 v13, a1\n"
"v_mfma_f32_16x16x32_bf16 v[204:207], a[80:83], a[104:107], v[204:207]\n"
"ds_read_b128 a[104:107], v5 offset:18432\n"
"v_accvgpr_read_b32 v14, a2\n"
"v_mfma_f32_16x16x32_bf16 v[208:211], a[80:83], a[112:115], v[208:211]\n"
"v_accvgpr_read_b32 v15, a3\n"
"v_mfma_f32_16x16x32_bf16 v[208:211], a[84:87], a[116:119], v[208:211]\n"
"global_load_dwordx4 a[68:71], v0, s[34:35] offset:-3072 sc1\n"
"v_add_f32 v12, v12, v16\n"
"v_mfma_f32_16x16x32_bf16 v[236:239], a[92:95], a[116:119], v[236:239]\n"
"ds_read_b128 a[116:119], v5 offset:21504\n"
"v_add_f32 v13, v13, v17\n"
"v_mfma_f32_16x16x32_bf16 v[236:239], a[88:91], a[112:115], v[236:239]\n"
"ds_read_b128 a[112:115], v5 offset:20480\n"
"v_add_f32 v14, v14, v18\n"
"v_mfma_f32_16x16x32_bf16 v[240:243], a[88:91], a[120:123], v[240:243]\n"
"v_add_f32 v15, v15, v19\n"
"v_accvgpr_write_b32 a0, v12\n"
"v_mfma_f32_16x16x32_bf16 v[240:243], a[92:95], a[124:127], v[240:243]\n"
"v_accvgpr_write_b32 a1, v13\n"
"v_mfma_f32_16x16x32_bf16 v[212:215], a[84:87], a[124:127], v[212:215]\n"
"ds_read_b128 a[124:127], v5 offset:23552\n"
"v_accvgpr_write_b32 a2, v14\n"
"v_mfma_f32_16x16x32_bf16 v[212:215], a[80:83], a[120:123], v[212:215]\n"
"ds_read_b128 a[120:123], v5 offset:22528\n"
"v_accvgpr_write_b32 a3, v15\n"
"v_mfma_f32_16x16x32_bf16 v[216:219], a[80:83], a[128:131], v[216:219]\n"
"buffer_load_dwordx4 v1, s[48:51], s69 offen offset:0 sc1 lds\n"
"v_mfma_f32_16x16x32_bf16 v[216:219], a[84:87], a[132:135], v[216:219]\n"
"global_load_dwordx4 a[72:75], v0, s[34:35] offset:-2048 sc1\n"
"v_lshrrev_b32 v16, 4, v0\n"
"v_mfma_f32_16x16x32_bf16 v[244:247], a[92:95], a[132:135], v[244:247]\n"
"ds_read_b128 a[132:135], v5 offset:25600\n"
"v_and_b32 v16, 0x30, v16\n"
"v_mfma_f32_16x16x32_bf16 v[244:247], a[88:91], a[128:131], v[244:247]\n"
"ds_read_b128 a[128:131], v5 offset:24576\n"
"buffer_load_dwordx2 v[18:19], v16, s[44:47], s61 offen offset:64\n"
"v_mfma_f32_16x16x32_bf16 v[248:251], a[88:91], a[136:139], v[248:251]\n"
"s_getpc_b64 s[96:97]\n"
"L_c3_:\n"
"s_add_u32 s96, s96, L_last-L_c3_\n"
"s_addc_u32 s97, s97, 0\n"
"v_mfma_f32_16x16x32_bf16 v[248:251], a[92:95], a[140:143], v[248:251]\n"
"s_lshr_b32 s66, %26, 3\n"
"v_mfma_f32_16x16x32_bf16 v[220:223], a[84:87], a[140:143], v[220:223]\n"
"s_lshl2_add_u32 s66, s66, %16\n"
"ds_read_b128 a[140:143], v5 offset:27648\n"
"v_mfma_f32_16x16x32_bf16 v[220:223], a[80:83], a[136:139], v[220:223]\n"
"ds_read_b128 a[136:139], v5 offset:26624\n"
"v_mfma_f32_16x16x32_bf16 v[224:227], a[80:83], a[144:147], v[224:227]\n"
"s_mul_i32 s66, s66, 256\n"
"v_mfma_f32_16x16x32_bf16 v[224:227], a[84:87], a[148:151], v[224:227]\n"
"global_load_dwordx4 a[76:79], v0, s[34:35] offset:-1024 sc1\n"
"v_mfma_f32_16x16x32_bf16 v[252:255], a[92:95], a[148:151], v[252:255]\n"
"ds_read_b128 a[148:151], v5 offset:29696\n"
"v_mfma_f32_16x16x32_bf16 v[252:255], a[88:91], a[144:147], v[252:255]\n"
"s_min_u32 s66, s66, L_end-L_last-256\n"
"ds_read_b128 a[144:147], v5 offset:28672\n"
"v_mfma_f32_16x16x32_bf16 a[28:31], a[88:91], a[152:155], a[28:31]\n"
"v_mfma_f32_16x16x32_bf16 a[28:31], a[92:95], a[156:159], a[28:31]\n"
"v_min_u32 v11, 16, v0\n"
"v_mfma_f32_16x16x32_bf16 a[24:27], a[84:87], a[156:159], a[24:27]\n"
"ds_read_b128 a[156:159], v5 offset:31744\n"
"buffer_load_dwordx2 v[8:9], off, s[56:59], %14 sc1\n"
"v_mfma_f32_16x16x32_bf16 a[24:27], a[80:83], a[152:155], a[24:27]\n"
"s_waitcnt vmcnt(13) lgkmcnt(11)\n"
"ds_read_b128 a[152:155], v5 offset:30720\n"
"v_mfma_f32_16x16x32_bf16 v[32:35], a[32:35], a[96:99], v[32:35]\n"
"buffer_load_dwordx4 v1, s[48:51], s69 offen offset:1024 sc1 lds\n"
"v_mfma_f32_16x16x32_bf16 v[32:35], a[36:39], a[100:103], v[32:35]\n"
"global_load_dwordx4 a[80:83], v0, s[34:35] offset:0 sc1\n"
"v_mfma_f32_16x16x32_bf16 v[60:63], a[44:47], a[100:103], v[60:63]\n"
"v_lshl_add_u32 v11, v11, 3, s66\n"
"v_mfma_f32_16x16x32_bf16 v[60:63], a[40:43], a[96:99], v[60:63]\n"
"global_load_dword v10, v11, s[96:97]\n"
"v_mfma_f32_16x16x32_bf16 v[64:67], a[40:43], a[104:107], v[64:67]\n"
"v_mfma_f32_16x16x32_bf16 v[64:67], a[44:47], a[108:111], v[64:67]\n"
"v_mfma_f32_16x16x32_bf16 v[36:39], a[36:39], a[108:111], v[36:39]\n"
"v_mfma_f32_16x16x32_bf16 v[36:39], a[32:35], a[104:107], v[36:39]\n"
"s_waitcnt lgkmcnt(8)\n"
"v_mfma_f32_16x16x32_bf16 v[40:43], a[32:35], a[112:115], v[40:43]\n"
"v_mfma_f32_16x16x32_bf16 v[40:43], a[36:39], a[116:119], v[40:43]\n"
"global_load_dwordx4 a[84:87], v0, s[34:35] offset:1024 sc1\n"
"v_mfma_f32_16x16x32_bf16 v[68:71], a[44:47], a[116:119], v[68:71]\n"
"v_mfma_f32_16x16x32_bf16 v[68:71], a[40:43], a[112:115], v[68:71]\n"
"v_mfma_f32_16x16x32_bf16 v[72:75], a[40:43], a[120:123], v[72:75]\n"
"v_mfma_f32_16x16x32_bf16 v[72:75], a[44:47], a[124:127], v[72:75]\n"
"v_mfma_f32_16x16x32_bf16 v[44:47], a[36:39], a[124:127], v[44:47]\n"
"buffer_load_dwordx4 v1, s[48:51], s69 offen offset:2048 sc1 lds\n"
"v_mfma_f32_16x16x32_bf16 v[44:47], a[32:35], a[120:123], v[44:47]\n"
"s_waitcnt lgkmcnt(4)\n"
"v_mfma_f32_16x16x32_bf16 v[48:51], a[32:35], a[128:131], v[48:51]\n"
"v_mfma_f32_16x16x32_bf16 v[48:51], a[36:39], a[132:135], v[48:51]\n"
"global_load_dwordx4 a[88:91], v0, s[34:35] offset:2048 sc1\n"
"v_mfma_f32_16x16x32_bf16 v[76:79], a[44:47], a[132:135], v[76:79]\n"
"v_mfma_f32_16x16x32_bf16 v[76:79], a[40:43], a[128:131], v[76:79]\n"
"v_mfma_f32_16x16x32_bf16 v[80:83], a[40:43], a[136:139], v[80:83]\n"
"v_mfma_f32_16x16x32_bf16 v[80:83], a[44:47], a[140:143], v[80:83]\n"
"v_mfma_f32_16x16x32_bf16 v[52:55], a[36:39], a[140:143], v[52:55]\n"
"v_mfma_f32_16x16x32_bf16 v[52:55], a[32:35], a[136:139], v[52:55]\n"
"s_waitcnt lgkmcnt(0)\n"
"v_mfma_f32_16x16x32_bf16 v[56:59], a[32:35], a[144:147], v[56:59]\n"
"v_mfma_f32_16x16x32_bf16 v[56:59], a[36:39], a[148:151], v[56:59]\n"
"global_load_dwordx4 a[92:95], v0, s[34:35] offset:3072 sc1\n"
"v_mfma_f32_16x16x32_bf16 v[84:87], a[44:47], a[148:151], v[84:87]\n"
"v_mfma_f32_16x16x32_bf16 v[84:87], a[40:43], a[144:147], v[84:87]\n"
"v_mfma_f32_16x16x32_bf16 a[4:7], a[40:43], a[152:155], a[4:7]\n"
"s_add_u32 s34, s34, 8192\n"
"v_mfma_f32_16x16x32_bf16 a[4:7], a[44:47], a[156:159], a[4:7]\n"
"buffer_load_dwordx4 v1, s[48:51], s69 offen offset:3072 sc1 lds\n"
"v_mfma_f32_16x16x32_bf16 a[0:3], a[36:39], a[156:159], a[0:3]\n"
"v_mfma_f32_16x16x32_bf16 a[0:3], a[32:35], a[152:155], a[0:3]\n"
"s_waitcnt vmcnt(15)\n"
"v_mfma_f32_16x16x32_bf16 v[88:91], a[48:51], a[96:99], v[88:91]\n"
"v_mfma_f32_16x16x32_bf16 v[88:91], a[52:55], a[100:103], v[88:91]\n"
"global_load_dwordx4 a[32:35], v0, s[34:35] offset:-4096 sc1\n"
"v_mfma_f32_16x16x32_bf16 v[116:119], a[60:63], a[100:103], v[116:119]\n"
"v_mfma_f32_16x16x32_bf16 v[116:119], a[56:59], a[96:99], v[116:119]\n"
"v_mfma_f32_16x16x32_bf16 v[120:123], a[56:59], a[104:107], v[120:123]\n"
"v_mfma_f32_16x16x32_bf16 v[120:123], a[60:63], a[108:111], v[120:123]\n"
"v_mfma_f32_16x16x32_bf16 v[92:95], a[52:55], a[108:111], v[92:95]\n"
"v_mfma_f32_16x16x32_bf16 v[92:95], a[48:51], a[104:107], v[92:95]\n"
"v_mfma_f32_16x16x32_bf16 v[96:99], a[48:51], a[112:115], v[96:99]\n"
"v_mfma_f32_16x16x32_bf16 v[96:99], a[52:55], a[116:119], v[96:99]\n"
"global_load_dwordx4 a[36:39], v0, s[34:35] offset:-3072 sc1\n"
"v_mfma_f32_16x16x32_bf16 v[124:127], a[60:63], a[116:119], v[124:127]\n"
"v_mfma_f32_16x16x32_bf16 v[124:127], a[56:59], a[112:115], v[124:127]\n"
"v_mfma_f32_16x16x32_bf16 v[128:131], a[56:59], a[120:123], v[128:131]\n"
"v_mfma_f32_16x16x32_bf16 v[128:131], a[60:63], a[124:127], v[128:131]\n"
"v_mfma_f32_16x16x32_bf16 v[100:103], a[52:55], a[124:127], v[100:103]\n"
"v_mfma_f32_16x16x32_bf16 v[100:103], a[48:51], a[120:123], v[100:103]\n"
"v_mfma_f32_16x16x32_bf16 v[104:107], a[48:51], a[128:131], v[104:107]\n"
"v_mfma_f32_16x16x32_bf16 v[104:107], a[52:55], a[132:135], v[104:107]\n"
"global_load_dwordx4 a[40:43], v0, s[34:35] offset:-2048 sc1\n"
"v_mfma_f32_16x16x32_bf16 v[132:135], a[60:63], a[132:135], v[132:135]\n"
"v_mfma_f32_16x16x32_bf16 v[132:135], a[56:59], a[128:131], v[132:135]\n"
"v_mfma_f32_16x16x32_bf16 v[136:139], a[56:59], a[136:139], v[136:139]\n"
"v_mfma_f32_16x16x32_bf16 v[136:139], a[60:63], a[140:143], v[136:139]\n"
"v_mfma_f32_16x16x32_bf16 v[108:111], a[52:55], a[140:143], v[108:111]\n"
"v_mfma_f32_16x16x32_bf16 v[108:111], a[48:51], a[136:139], v[108:111]\n"
"v_mfma_f32_16x16x32_bf16 v[112:115], a[48:51], a[144:147], v[112:115]\n"
"v_mfma_f32_16x16x32_bf16 v[112:115], a[52:55], a[148:151], v[112:115]\n"
"global_load_dwordx4 a[44:47], v0, s[34:35] offset:-1024 sc1\n"
"v_mfma_f32_16x16x32_bf16 v[140:143], a[60:63], a[148:151], v[140:143]\n"
"v_mfma_f32_16x16x32_bf16 v[140:143], a[56:59], a[144:147], v[140:143]\n"
"v_mfma_f32_16x16x32_bf16 a[12:15], a[56:59], a[152:155], a[12:15]\n"
"v_mfma_f32_16x16x32_bf16 a[12:15], a[60:63], a[156:159], a[12:15]\n"
"v_mfma_f32_16x16x32_bf16 a[8:11], a[52:55], a[156:159], a[8:11]\n"
"v_mfma_f32_16x16x32_bf16 a[8:11], a[48:51], a[152:155], a[8:11]\n"
"s_waitcnt vmcnt(13)\n"
"v_mfma_f32_16x16x32_bf16 v[144:147], a[64:67], a[96:99], v[144:147]\n"
"v_mfma_f32_16x16x32_bf16 v[144:147], a[68:71], a[100:103], v[144:147]\n"
"global_load_dwordx4 a[48:51], v0, s[34:35] offset:0 sc1\n"
"v_mfma_f32_16x16x32_bf16 v[172:175], a[76:79], a[100:103], v[172:175]\n"
"v_mfma_f32_16x16x32_bf16 v[172:175], a[72:75], a[96:99], v[172:175]\n"
"v_mfma_f32_16x16x32_bf16 v[176:179], a[72:75], a[104:107], v[176:179]\n"
"v_mfma_f32_16x16x32_bf16 v[176:179], a[76:79], a[108:111], v[176:179]\n"
"v_mfma_f32_16x16x32_bf16 v[148:151], a[68:71], a[108:111], v[148:151]\n"
"v_mfma_f32_16x16x32_bf16 v[148:151], a[64:67], a[104:107], v[148:151]\n"
"v_mfma_f32_16x16x32_bf16 v[152:155], a[64:67], a[112:115], v[152:155]\n"
"v_lshlrev_b32 v20, 16, v22\n"
"v_mfma_f32_16x16x32_bf16 v[152:155], a[68:71], a[116:119], v[152:155]\n"
"global_load_dwordx4 a[52:55], v0, s[34:35] offset:1024 sc1\n"
"v_and_b32 v21, s63, v22\n"
"v_mfma_f32_16x16x32_bf16 v[180:183], a[76:79], a[116:119], v[180:183]\n"
"v_lshlrev_b32 v22, 16, v23\n"
"v_mfma_f32_16x16x32_bf16 v[180:183], a[72:75], a[112:115], v[180:183]\n"
"v_and_b32 v23, s63, v23\n"
"v_mfma_f32_16x16x32_bf16 v[184:187], a[72:75], a[120:123], v[184:187]\n"
"v_add_f32 v60, v60, v20\n"
"v_mfma_f32_16x16x32_bf16 v[184:187], a[76:79], a[124:127], v[184:187]\n"
"v_add_f32 v61, v61, v21\n"
"v_add_f32 v62, v62, v22\n"
"v_mfma_f32_16x16x32_bf16 v[156:159], a[68:71], a[124:127], v[156:159]\n"
"v_add_f32 v63, v63, v23\n"
"v_mfma_f32_16x16x32_bf16 v[156:159], a[64:67], a[120:123], v[156:159]\n"
"v_add_f32 v64, v64, v20\n"
"v_mfma_f32_16x16x32_bf16 v[160:163], a[64:67], a[128:131], v[160:163]\n"
"v_add_f32 v65, v65, v21\n"
"v_mfma_f32_16x16x32_bf16 v[160:163], a[68:71], a[132:135], v[160:163]\n"
"global_load_dwordx4 a[56:59], v0, s[34:35] offset:2048 sc1\n"
"v_add_f32 v66, v66, v22\n"
"v_mfma_f32_16x16x32_bf16 v[188:191], a[76:79], a[132:135], v[188:191]\n"
"v_add_f32 v67, v67, v23\n"
"v_mfma_f32_16x16x32_bf16 v[188:191], a[72:75], a[128:131], v[188:191]\n"
"v_add_f32 v68, v68, v20\n"
"v_mfma_f32_16x16x32_bf16 v[192:195], a[72:75], a[136:139], v[192:195]\n"
"v_add_f32 v69, v69, v21\n"
"v_mfma_f32_16x16x32_bf16 v[192:195], a[76:79], a[140:143], v[192:195]\n"
"v_add_f32 v70, v70, v22\n"
"v_mfma_f32_16x16x32_bf16 v[164:167], a[68:71], a[140:143], v[164:167]\n"
"v_add_f32 v71, v71, v23\n"
"v_mfma_f32_16x16x32_bf16 v[164:167], a[64:67], a[136:139], v[164:167]\n"
"v_add_f32 v72, v72, v20\n"
"v_add_f32 v73, v73, v21\n"
"v_mfma_f32_16x16x32_bf16 v[168:171], a[64:67], a[144:147], v[168:171]\n"
"v_add_f32 v74, v74, v22\n"
"v_mfma_f32_16x16x32_bf16 v[168:171], a[68:71], a[148:151], v[168:171]\n"
"global_load_dwordx4 a[60:63], v0, s[34:35] offset:3072 sc1\n"
"v_add_f32 v75, v75, v23\n"
"v_mfma_f32_16x16x32_bf16 v[196:199], a[76:79], a[148:151], v[196:199]\n"
"v_add_f32 v76, v76, v20\n"
"v_mfma_f32_16x16x32_bf16 v[196:199], a[72:75], a[144:147], v[196:199]\n"
"v_add_f32 v77, v77, v21\n"
"v_mfma_f32_16x16x32_bf16 a[20:23], a[72:75], a[152:155], a[20:23]\n"
"s_add_u32 s34, s34, 8192\n"
"v_add_f32 v78, v78, v22\n"
"v_mfma_f32_16x16x32_bf16 a[20:23], a[76:79], a[156:159], a[20:23]\n"
"v_add_f32 v79, v79, v23\n"
"v_mfma_f32_16x16x32_bf16 a[16:19], a[68:71], a[156:159], a[16:19]\n"
"v_add_f32 v80, v80, v20\n"
"v_mfma_f32_16x16x32_bf16 a[16:19], a[64:67], a[152:155], a[16:19]\n"
"s_waitcnt vmcnt(9)\n"
"v_add_f32 v81, v81, v21\n"
"v_mfma_f32_16x16x32_bf16 v[200:203], a[80:83], a[96:99], v[200:203]\n"
"v_add_f32 v82, v82, v22\n"
"v_add_f32 v83, v83, v23\n"
"v_mfma_f32_16x16x32_bf16 v[200:203], a[84:87], a[100:103], v[200:203]\n"
"global_load_dwordx4 a[64:67], v0, s[34:35] offset:-4096 sc1\n"
"v_add_f32 v84, v84, v20\n"
"v_mfma_f32_16x16x32_bf16 v[228:231], a[92:95], a[100:103], v[228:231]\n"
"s_waitcnt vmcnt(9)\n"
"ds_read_b128 a[100:103], v2 offset:1024\n"
"v_add_f32 v85, v85, v21\n"
"v_mfma_f32_16x16x32_bf16 v[228:231], a[88:91], a[96:99], v[228:231]\n"
"ds_read_b128 a[96:99], v2 offset:0\n"
"v_add_f32 v86, v86, v22\n"
"v_mfma_f32_16x16x32_bf16 v[232:235], a[88:91], a[104:107], v[232:235]\n"
"v_add_f32 v87, v87, v23\n"
"v_mfma_f32_16x16x32_bf16 v[232:235], a[92:95], a[108:111], v[232:235]\n"
"v_accvgpr_read_b32 v12, a4\n"
"v_mfma_f32_16x16x32_bf16 v[204:207], a[84:87], a[108:111], v[204:207]\n"
"ds_read_b128 a[108:111], v2 offset:3072\n"
"v_accvgpr_read_b32 v13, a5\n"
"v_mfma_f32_16x16x32_bf16 v[204:207], a[80:83], a[104:107], v[204:207]\n"
"ds_read_b128 a[104:107], v2 offset:2048\n"
"v_accvgpr_read_b32 v14, a6\n"
"v_mfma_f32_16x16x32_bf16 v[208:211], a[80:83], a[112:115], v[208:211]\n"
"v_accvgpr_read_b32 v15, a7\n"
"v_mfma_f32_16x16x32_bf16 v[208:211], a[84:87], a[116:119], v[208:211]\n"
"global_load_dwordx4 a[68:71], v0, s[34:35] offset:-3072 sc1\n"
"v_add_f32 v12, v12, v20\n"
"v_mfma_f32_16x16x32_bf16 v[236:239], a[92:95], a[116:119], v[236:239]\n"
"ds_read_b128 a[116:119], v2 offset:5120\n"
"v_add_f32 v13, v13, v21\n"
"v_mfma_f32_16x16x32_bf16 v[236:239], a[88:91], a[112:115], v[236:239]\n"
"ds_read_b128 a[112:115], v2 offset:4096\n"
"v_add_f32 v14, v14, v22\n"
"v_mfma_f32_16x16x32_bf16 v[240:243], a[88:91], a[120:123], v[240:243]\n"
"v_add_f32 v15, v15, v23\n"
"v_accvgpr_write_b32 a4, v12\n"
"v_mfma_f32_16x16x32_bf16 v[240:243], a[92:95], a[124:127], v[240:243]\n"
"v_accvgpr_write_b32 a5, v13\n"
"v_mfma_f32_16x16x32_bf16 v[212:215], a[84:87], a[124:127], v[212:215]\n"
"ds_read_b128 a[124:127], v2 offset:7168\n"
"v_accvgpr_write_b32 a6, v14\n"
"v_mfma_f32_16x16x32_bf16 v[212:215], a[80:83], a[120:123], v[212:215]\n"
"ds_read_b128 a[120:123], v2 offset:6144\n"
"v_accvgpr_write_b32 a7, v15\n"
"v_mfma_f32_16x16x32_bf16 v[216:219], a[80:83], a[128:131], v[216:219]\n"
"v_mfma_f32_16x16x32_bf16 v[216:219], a[84:87], a[132:135], v[216:219]\n"
"global_load_dwordx4 a[72:75], v0, s[34:35] offset:-2048 sc1\n"
"v_lshrrev_b32 v20, 4, v0\n"
"v_mfma_f32_16x16x32_bf16 v[244:247], a[92:95], a[132:135], v[244:247]\n"
"ds_read_b128 a[132:135], v2 offset:9216\n"
"v_and_b32 v20, 0x30, v20\n"
"v_mfma_f32_16x16x32_bf16 v[244:247], a[88:91], a[128:131], v[244:247]\n"
"ds_read_b128 a[128:131], v2 offset:8192\n"
"buffer_load_dwordx2 v[22:23], v20, s[44:47], s61 offen offset:72\n"
"v_mfma_f32_16x16x32_bf16 v[248:251], a[88:91], a[136:139], v[248:251]\n"
"v_mfma_f32_16x16x32_bf16 v[248:251], a[92:95], a[140:143], v[248:251]\n"
"v_mfma_f32_16x16x32_bf16 v[220:223], a[84:87], a[140:143], v[220:223]\n"
"ds_read_b128 a[140:143], v2 offset:11264\n"
"v_mfma_f32_16x16x32_bf16 v[220:223], a[80:83], a[136:139], v[220:223]\n"
"ds_read_b128 a[136:139], v2 offset:10240\n"
"v_mfma_f32_16x16x32_bf16 v[224:227], a[80:83], a[144:147], v[224:227]\n"
"v_mfma_f32_16x16x32_bf16 v[224:227], a[84:87], a[148:151], v[224:227]\n"
"global_load_dwordx4 a[76:79], v0, s[34:35] offset:-1024 sc1\n"
"v_mfma_f32_16x16x32_bf16 v[252:255], a[92:95], a[148:151], v[252:255]\n"
"ds_read_b128 a[148:151], v2 offset:13312\n"
"v_mfma_f32_16x16x32_bf16 v[252:255], a[88:91], a[144:147], v[252:255]\n"
"ds_read_b128 a[144:147], v2 offset:12288\n"
"v_mfma_f32_16x16x32_bf16 a[28:31], a[88:91], a[152:155], a[28:31]\n"
"v_mfma_f32_16x16x32_bf16 a[28:31], a[92:95], a[156:159], a[28:31]\n"
"v_mfma_f32_16x16x32_bf16 a[24:27], a[84:87], a[156:159], a[24:27]\n"
"s_cmp_eq_u32 s62, 5\n"
"ds_read_b128 a[156:159], v2 offset:15360\n"
"v_mfma_f32_16x16x32_bf16 a[24:27], a[80:83], a[152:155], a[24:27]\n"
"ds_read_b128 a[152:155], v2 offset:14336\n"
"s_cbranch_scc1 L_last\n"
"s_waitcnt vmcnt(9) lgkmcnt(12)\n"
"v_mfma_f32_16x16x32_bf16 v[32:35], a[32:35], a[96:99], v[32:35]\n"
"s_add_u32 s48, s48, 131072\n"
"s_addc_u32 s49, s49, 0\n"
"v_mfma_f32_16x16x32_bf16 v[32:35], a[36:39], a[100:103], v[32:35]\n"
"global_load_dwordx4 a[80:83], v0, s[34:35] offset:0 sc1\n"
"L_P4:\n"
"v_mfma_f32_16x16x32_bf16 v[60:63], a[44:47], a[100:103], v[60:63]\n"
"s_barrier\n"
"v_mfma_f32_16x16x32_bf16 v[60:63], a[40:43], a[96:99], v[60:63]\n"
"v_mfma_f32_16x16x32_bf16 v[64:67], a[40:43], a[104:107], v[64:67]\n"
"v_mfma_f32_16x16x32_bf16 v[64:67], a[44:47], a[108:111], v[64:67]\n"
"v_mfma_f32_16x16x32_bf16 v[36:39], a[36:39], a[108:111], v[36:39]\n"
"v_subrev_u32 v8, s65, v8\n"
"v_mfma_f32_16x16x32_bf16 v[36:39], a[32:35], a[104:107], v[36:39]\n"
"s_waitcnt lgkmcnt(8)\n"
"v_xor_b32 v9, s79, v9\n"
"v_mfma_f32_16x16x32_bf16 v[40:43], a[32:35], a[112:115], v[40:43]\n"
"v_or_b32 v8, v8, v9\n"
"v_mfma_f32_16x16x32_bf16 v[40:43], a[36:39], a[116:119], v[40:43]\n"
"global_load_dwordx4 a[84:87], v0, s[34:35] offset:1024 sc1\n"
"v_cmp_lt_u32 vcc, 15, v8\n"
"v_mfma_f32_16x16x32_bf16 v[68:71], a[44:47], a[116:119], v[68:71]\n"
"s_cbranch_vccnz L_slow1\n"
"L_back1:\n"
"v_mfma_f32_16x16x32_bf16 v[68:71], a[40:43], a[112:115], v[68:71]\n"
"v_mfma_f32_16x16x32_bf16 v[72:75], a[40:43], a[120:123], v[72:75]\n"
"v_mfma_f32_16x16x32_bf16 v[72:75], a[44:47], a[124:127], v[72:75]\n"
"v_mfma_f32_16x16x32_bf16 v[44:47], a[36:39], a[124:127], v[44:47]\n"
"v_mfma_f32_16x16x32_bf16 v[44:47], a[32:35], a[120:123], v[44:47]\n"
"s_waitcnt lgkmcnt(4)\n"
"v_mfma_f32_16x16x32_bf16 v[48:51], a[32:35], a[128:131], v[48:51]\n"
"v_mfma_f32_16x16x32_bf16 v[48:51], a[36:39], a[132:135], v[48:51]\n"
"global_load_dwordx4 a[88:91], v0, s[34:35] offset:2048 sc1\n"
"v_mfma_f32_16x16x32_bf16 v[76:79], a[44:47], a[132:135], v[76:79]\n"
"v_mfma_f32_16x16x32_bf16 v[76:79], a[40:43], a[128:131], v[76:79]\n"
"s_add_u32 m0, s64, 16384\n"
"v_mfma_f32_16x16x32_bf16 v[80:83], a[40:43], a[136:139], v[80:83]\n"
"v_mfma_f32_16x16x32_bf16 v[80:83], a[44:47], a[140:143], v[80:83]\n"
"v_mfma_f32_16x16x32_bf16 v[52:55], a[36:39], a[140:143], v[52:55]\n"
"v_mfma_f32_16x16x32_bf16 v[52:55], a[32:35], a[136:139], v[52:55]\n"
"s_waitcnt lgkmcnt(0)\n"
"v_mfma_f32_16x16x32_bf16 v[56:59], a[32:35], a[144:147], v[56:59]\n"
"v_mfma_f32_16x16x32_bf16 v[56:59], a[36:39], a[148:151], v[56:59]\n"
"global_load_dwordx4 a[92:95], v0, s[34:35] offset:3072 sc1\n"
"v_mfma_f32_16x16x32_bf16 v[84:87], a[44:47], a[148:151], v[84:87]\n"
"v_mfma_f32_16x16x32_bf16 v[84:87], a[40:43], a[144:147], v[84:87]\n"
"v_mfma_f32_16x16x32_bf16 a[4:7], a[40:43], a[152:155], a[4:7]\n"
"s_add_u32 s34, s34, 8192\n"
"v_mfma_f32_16x16x32_bf16 a[4:7], a[44:47], a[156:159], a[4:7]\n"
"v_mfma_f32_16x16x32_bf16 a[0:3], a[36:39], a[156:159], a[0:3]\n"
"v_mfma_f32_16x16x32_bf16 a[0:3], a[32:35], a[152:155], a[0:3]\n"
"s_waitcnt vmcnt(9)\n"
"v_mfma_f32_16x16x32_bf16 v[88:91], a[48:51], a[96:99], v[88:91]\n"
"v_mfma_f32_16x16x32_bf16 v[88:91], a[52:55], a[100:103], v[88:91]\n"
"global_load_dwordx4 a[32:35], v0, s[34:35] offset:-4096 sc1\n"
"v_mfma_f32_16x16x32_bf16 v[116:119], a[60:63], a[100:103], v[116:119]\n"
"buffer_load_dwordx4 v0, s[48:51], s75 offen offset:0 sc1 lds\n"
"v_mfma_f32_16x16x32_bf16 v[116:119], a[56:59], a[96:99], v[116:119]\n"
"v_mfma_f32_16x16x32_bf16 v[120:123], a[56:59], a[104:107], v[120:123]\n"
"v_mfma_f32_16x16x32_bf16 v[120:123], a[60:63], a[108:111], v[120:123]\n"
"v_mfma_f32_16x16x32_bf16 v[92:95], a[52:55], a[108:111], v[92:95]\n"
"v_mfma_f32_16x16x32_bf16 v[92:95], a[48:51], a[104:107], v[92:95]\n"
"v_mfma_f32_16x16x32_bf16 v[96:99], a[48:51], a[112:115], v[96:99]\n"
"v_mfma_f32_16x16x32_bf16 v[96:99], a[52:55], a[116:119], v[96:99]\n"
"global_load_dwordx4 a[36:39], v0, s[34:35] offset:-3072 sc1\n"
"v_mfma_f32_16x16x32_bf16 v[124:127], a[60:63], a[116:119], v[124:127]\n"
"v_mfma_f32_16x16x32_bf16 v[124:127], a[56:59], a[112:115], v[124:127]\n"
"v_mfma_f32_16x16x32_bf16 v[128:131], a[56:59], a[120:123], v[128:131]\n"
"v_mfma_f32_16x16x32_bf16 v[128:131], a[60:63], a[124:127], v[128:131]\n"
"v_mfma_f32_16x16x32_bf16 v[100:103], a[52:55], a[124:127], v[100:103]\n"
"v_mfma_f32_16x16x32_bf16 v[100:103], a[48:51], a[120:123], v[100:103]\n"
"v_mfma_f32_16x16x32_bf16 v[104:107], a[48:51], a[128:131], v[104:107]\n"
"v_mfma_f32_16x16x32_bf16 v[104:107], a[52:55], a[132:135], v[104:107]\n"
"global_load_dwordx4 a[40:43], v0, s[34:35] offset:-2048 sc1\n"
"v_mfma_f32_16x16x32_bf16 v[132:135], a[60:63], a[132:135], v[132:135]\n"
"buffer_load_dwordx4 v0, s[48:51], s75 offen offset:1024 sc1 lds\n"
"v_mfma_f32_16x16x32_bf16 v[132:135], a[56:59], a[128:131], v[132:135]\n"
"v_mfma_f32_16x16x32_bf16 v[136:139], a[56:59], a[136:139], v[136:139]\n"
"v_mfma_f32_16x16x32_bf16 v[136:139], a[60:63], a[140:143], v[136:139]\n"
"v_mfma_f32_16x16x32_bf16 v[108:111], a[52:55], a[140:143], v[108:111]\n"
"v_mfma_f32_16x16x32_bf16 v[108:111], a[48:51], a[136:139], v[108:111]\n"
"v_mfma_f32_16x16x32_bf16 v[112:115], a[48:51], a[144:147], v[112:115]\n"
"v_mfma_f32_16x16x32_bf16 v[112:115], a[52:55], a[148:151], v[112:115]\n"
"global_load_dwordx4 a[44:47], v0, s[34:35] offset:-1024 sc1\n"
"v_mfma_f32_16x16x32_bf16 v[140:143], a[60:63], a[148:151], v[140:143]\n"
"v_mfma_f32_16x16x32_bf16 v[140:143], a[56:59], a[144:147], v[140:143]\n"
"v_mfma_f32_16x16x32_bf16 a[12:15], a[56:59], a[152:155], a[12:15]\n"
"v_mfma_f32_16x16x32_bf16 a[12:15], a[60:63], a[156:159], a[12:15]\n"
"v_mfma_f32_16x16x32_bf16 a[8:11], a[52:55], a[156:159], a[8:11]\n"
"v_mfma_f32_16x16x32_bf16 a[8:11], a[48:51], a[152:155], a[8:11]\n"
"s_waitcnt vmcnt(10)\n"
"v_mfma_f32_16x16x32_bf16 v[144:147], a[64:67], a[96:99], v[144:147]\n"
"v_mfma_f32_16x16x32_bf16 v[144:147], a[68:71], a[100:103], v[144:147]\n"
"global_load_dwordx4 a[48:51], v0, s[34:35] offset:0 sc1\n"
"v_mfma_f32_16x16x32_bf16 v[172:175], a[76:79], a[100:103], v[172:175]\n"
"v_mfma_f32_16x16x32_bf16 v[172:175], a[72:75], a[96:99], v[172:175]\n"
"buffer_load_dwordx4 v0, s[48:51], s75 offen offset:2048 sc1 lds\n"
"v_mfma_f32_16x16x32_bf16 v[176:179], a[72:75], a[104:107], v[176:179]\n"
"v_mfma_f32_16x16x32_bf16 v[176:179], a[76:79], a[108:111], v[176:179]\n"
"v_mfma_f32_16x16x32_bf16 v[148:151], a[68:71], a[108:111], v[148:151]\n"
"v_mfma_f32_16x16x32_bf16 v[148:151], a[64:67], a[104:107], v[148:151]\n"
"v_mfma_f32_16x16x32_bf16 v[152:155], a[64:67], a[112:115], v[152:155]\n"
"v_lshlrev_b32 v16, 16, v18\n"
"v_mfma_f32_16x16x32_bf16 v[152:155], a[68:71], a[116:119], v[152:155]\n"
"global_load_dwordx4 a[52:55], v0, s[34:35] offset:1024 sc1\n"
"v_and_b32 v17, s63, v18\n"
"v_mfma_f32_16x16x32_bf16 v[180:183], a[76:79], a[116:119], v[180:183]\n"
"v_lshlrev_b32 v18, 16, v19\n"
"v_mfma_f32_16x16x32_bf16 v[180:183], a[72:75], a[112:115], v[180:183]\n"
"v_and_b32 v19, s63, v19\n"
"v_mfma_f32_16x16x32_bf16 v[184:187], a[72:75], a[120:123], v[184:187]\n"
"v_add_f32 v88, v88, v16\n"
"v_mfma_f32_16x16x32_bf16 v[184:187], a[76:79], a[124:127], v[184:187]\n"
"v_add_f32 v89, v89, v17\n"
"v_add_f32 v90, v90, v18\n"
"v_mfma_f32_16x16x32_bf16 v[156:159], a[68:71], a[124:127], v[156:159]\n"
"v_add_f32 v91, v91, v19\n"
"v_mfma_f32_16x16x32_bf16 v[156:159], a[64:67], a[120:123], v[156:159]\n"
"v_add_f32 v92, v92, v16\n"
"v_mfma_f32_16x16x32_bf16 v[160:163], a[64:67], a[128:131], v[160:163]\n"
"v_add_f32 v93, v93, v17\n"
"v_mfma_f32_16x16x32_bf16 v[160:163], a[68:71], a[132:135], v[160:163]\n"
"global_load_dwordx4 a[56:59], v0, s[34:35] offset:2048 sc1\n"
"v_add_f32 v94, v94, v18\n"
"v_mfma_f32_16x16x32_bf16 v[188:191], a[76:79], a[132:135], v[188:191]\n"
"buffer_load_dwordx4 v0, s[48:51], s75 offen offset:3072 sc1 lds\n"
"v_mfma_f32_16x16x32_bf16 v[188:191], a[72:75], a[128:131], v[188:191]\n"
"v_add_f32 v95, v95, v19\n"
"v_add_f32 v96, v96, v16\n"
"v_mfma_f32_16x16x32_bf16 v[192:195], a[72:75], a[136:139], v[192:195]\n"
"v_add_f32 v97, v97, v17\n"
"v_mfma_f32_16x16x32_bf16 v[192:195], a[76:79], a[140:143], v[192:195]\n"
"v_add_f32 v98, v98, v18\n"
"v_mfma_f32_16x16x32_bf16 v[164:167], a[68:71], a[140:143], v[164:167]\n"
"v_add_f32 v99, v99, v19\n"
"v_mfma_f32_16x16x32_bf16 v[164:167], a[64:67], a[136:139], v[164:167]\n"
"v_add_f32 v100, v100, v16\n"
"v_add_f32 v101, v101, v17\n"
"v_mfma_f32_16x16x32_bf16 v[168:171], a[64:67], a[144:147], v[168:171]\n"
"v_add_f32 v102, v102, v18\n"
"v_mfma_f32_16x16x32_bf16 v[168:171], a[68:71], a[148:151], v[168:171]\n"
"global_load_dwordx4 a[60:63], v0, s[34:35] offset:3072 sc1\n"
"v_add_f32 v103, v103, v19\n"
"v_mfma_f32_16x16x32_bf16 v[196:199], a[76:79], a[148:151], v[196:199]\n"
"v_add_f32 v104, v104, v16\n"
"v_mfma_f32_16x16x32_bf16 v[196:199], a[72:75], a[144:147], v[196:199]\n"
"v_add_f32 v105, v105, v17\n"
"v_mfma_f32_16x16x32_bf16 a[20:23], a[72:75], a[152:155], a[20:23]\n"
"s_add_u32 s34, s34, 8192\n"
"v_add_f32 v106, v106, v18\n"
"v_mfma_f32_16x16x32_bf16 a[20:23], a[76:79], a[156:159], a[20:23]\n"
"v_add_f32 v107, v107, v19\n"
"v_mfma_f32_16x16x32_bf16 a[16:19], a[68:71], a[156:159], a[16:19]\n"
"v_add_f32 v108, v108, v16\n"
"v_mfma_f32_16x16x32_bf16 a[16:19], a[64:67], a[152:155], a[16:19]\n"
"s_waitcnt vmcnt(12)\n"
"v_add_f32 v109, v109, v17\n"
"v_mfma_f32_16x16x32_bf16 v[200:203], a[80:83], a[96:99], v[200:203]\n"
"v_add_f32 v110, v110, v18\n"
"v_add_f32 v111, v111, v19\n"
"v_mfma_f32_16x16x32_bf16 v[200:203], a[84:87], a[100:103], v[200:203]\n"
"global_load_dwordx4 a[64:67], v0, s[34:35] offset:-4096 sc1\n"
"v_add_f32 v112, v112, v16\n"
"v_mfma_f32_16x16x32_bf16 v[228:231], a[92:95], a[100:103], v[228:231]\n"
"ds_read_b128 a[100:103], v3 offset:1024\n"
"v_add_f32 v113, v113, v17\n"
"v_mfma_f32_16x16x32_bf16 v[228:231], a[88:91], a[96:99], v[228:231]\n"
"s_add_u32 m0, s64, 20480\n"
"ds_read_b128 a[96:99], v3 offset:0\n"
"v_add_f32 v114, v114, v18\n"
"v_mfma_f32_16x16x32_bf16 v[232:235], a[88:91], a[104:107], v[232:235]\n"
"v_add_f32 v115, v115, v19\n"
"v_mfma_f32_16x16x32_bf16 v[232:235], a[92:95], a[108:111], v[232:235]\n"
"v_accvgpr_read_b32 v12, a8\n"
"v_mfma_f32_16x16x32_bf16 v[204:207], a[84:87], a[108:111], v[204:207]\n"
"ds_read_b128 a[108:111], v3 offset:3072\n"
"v_accvgpr_read_b32 v13, a9\n"
"v_mfma_f32_16x16x32_bf16 v[204:207], a[80:83], a[104:107], v[204:207]\n"
"ds_read_b128 a[104:107], v3 offset:2048\n"
"v_accvgpr_read_b32 v14, a10\n"
"v_mfma_f32_16x16x32_bf16 v[208:211], a[80:83], a[112:115], v[208:211]\n"
"v_accvgpr_read_b32 v15, a11\n"
"v_mfma_f32_16x16x32_bf16 v[208:211], a[84:87], a[116:119], v[208:211]\n"
"global_load_dwordx4 a[68:71], v0, s[34:35] offset:-3072 sc1\n"
"v_add_f32 v12, v12, v16\n"
"v_mfma_f32_16x16x32_bf16 v[236:239], a[92:95], a[116:119], v[236:239]\n"
"ds_read_b128 a[116:119], v3 offset:5120\n"
"v_add_f32 v13, v13, v17\n"
"v_mfma_f32_16x16x32_bf16 v[236:239], a[88:91], a[112:115], v[236:239]\n"
"ds_read_b128 a[112:115], v3 offset:4096\n"
"v_add_f32 v14, v14, v18\n"
"v_mfma_f32_16x16x32_bf16 v[240:243], a[88:91], a[120:123], v[240:243]\n"
"v_add_f32 v15, v15, v19\n"
"v_accvgpr_write_b32 a8, v12\n"
"v_mfma_f32_16x16x32_bf16 v[240:243], a[92:95], a[124:127], v[240:243]\n"
"v_accvgpr_write_b32 a9, v13\n"
"v_mfma_f32_16x16x32_bf16 v[212:215], a[84:87], a[124:127], v[212:215]\n"
"ds_read_b128 a[124:127], v3 offset:7168\n"
"v_accvgpr_write_b32 a10, v14\n"
"v_mfma_f32_16x16x32_bf16 v[212:215], a[80:83], a[120:123], v[212:215]\n"
"ds_read_b128 a[120:123], v3 offset:6144\n"
"v_accvgpr_write_b32 a11, v15\n"
"v_mfma_f32_16x16x32_bf16 v[216:219], a[80:83], a[128:131], v[216:219]\n"
"v_mfma_f32_16x16x32_bf16 v[216:219], a[84:87], a[132:135], v[216:219]\n"
"global_load_dwordx4 a[72:75], v0, s[34:35] offset:-2048 sc1\n"
"v_mfma_f32_16x16x32_bf16 v[244:247], a[92:95], a[132:135], v[244:247]\n"
"ds_read_b128 a[132:135], v3 offset:9216\n"
"v_mfma_f32_16x16x32_bf16 v[244:247], a[88:91], a[128:131], v[244:247]\n"
"ds_read_b128 a[128:131], v3 offset:8192\n"
"v_mfma_f32_16x16x32_bf16 v[248:251], a[88:91], a[136:139], v[248:251]\n"
"buffer_load_dwordx4 v1, s[48:51], s75 offen offset:0 sc1 lds\n"
"v_mfma_f32_16x16x32_bf16 v[248:251], a[92:95], a[140:143], v[248:251]\n"
"v_mfma_f32_16x16x32_bf16 v[220:223], a[84:87], a[140:143], v[220:223]\n"
"ds_read_b128 a[140:143], v3 offset:11264\n"
"v_mfma_f32_16x16x32_bf16 v[220:223], a[80:83], a[136:139], v[220:223]\n"
"ds_read_b128 a[136:139], v3 offset:10240\n"
"v_mfma_f32_16x16x32_bf16 v[224:227], a[80:83], a[144:147], v[224:227]\n"
"v_mfma_f32_16x16x32_bf16 v[224:227], a[84:87], a[148:151], v[224:227]\n"
"global_load_dwordx4 a[76:79], v0, s[34:35] offset:-1024 sc1\n"
"v_mfma_f32_16x16x32_bf16 v[252:255], a[92:95], a[148:151], v[252:255]\n"
"ds_read_b128 a[148:151], v3 offset:13312\n"
"v_mfma_f32_16x16x32_bf16 v[252:255], a[88:91], a[144:147], v[252:255]\n"
"ds_read_b128 a[144:147], v3 offset:12288\n"
"v_mfma_f32_16x16x32_bf16 a[28:31], a[88:91], a[152:155], a[28:31]\n"
"v_mfma_f32_16x16x32_bf16 a[28:31], a[92:95], a[156:159], a[28:31]\n"
"v_mfma_f32_16x16x32_bf16 a[24:27], a[84:87], a[156:159], a[24:27]\n"
"ds_read_b128 a[156:159], v3 offset:15360\n"
"v_mfma_f32_16x16x32_bf16 a[24:27], a[80:83], a[152:155], a[24:27]\n"
"s_waitcnt vmcnt(11) lgkmcnt(11)\n"
"ds_read_b128 a[152:155], v3 offset:14336\n"
"v_mfma_f32_16x16x32_bf16 v[32:35], a[32:35], a[96:99], v[32:35]\n"
"v_mfma_f32_16x16x32_bf16 v[32:35], a[36:39], a[100:103], v[32:35]\n"
"global_load_dwordx4 a[80:83], v0, s[34:35] offset:0 sc1\n"
"v_mfma_f32_16x16x32_bf16 v[60:63], a[44:47], a[100:103], v[60:63]\n"
"v_mfma_f32_16x16x32_bf16 v[60:63], a[40:43], a[96:99], v[60:63]\n"
"buffer_load_dwordx4 v1, s[48:51], s75 offen offset:1024 sc1 lds\n"
"v_mfma_f32_16x16x32_bf16 v[64:67], a[40:43], a[104:107], v[64:67]\n"
"v_mfma_f32_16x16x32_bf16 v[64:67], a[44:47], a[108:111], v[64:67]\n"
"v_mfma_f32_16x16x32_bf16 v[36:39], a[36:39], a[108:111], v[36:39]\n"
"v_mfma_f32_16x16x32_bf16 v[36:39], a[32:35], a[104:107], v[36:39]\n"
"s_waitcnt lgkmcnt(8)\n"
"v_mfma_f32_16x16x32_bf16 v[40:43], a[32:35], a[112:115], v[40:43]\n"
"v_mfma_f32_16x16x32_bf16 v[40:43], a[36:39], a[116:119], v[40:43]\n"
"global_load_dwordx4 a[84:87], v0, s[34:35] offset:1024 sc1\n"
"v_mfma_f32_16x16x32_bf16 v[68:71], a[44:47], a[116:119], v[68:71]\n"
"v_mfma_f32_16x16x32_bf16 v[68:71], a[40:43], a[112:115], v[68:71]\n"
"v_mfma_f32_16x16x32_bf16 v[72:75], a[40:43], a[120:123], v[72:75]\n"
"v_mfma_f32_16x16x32_bf16 v[72:75], a[44:47], a[124:127], v[72:75]\n"
"v_mfma_f32_16x16x32_bf16 v[44:47], a[36:39], a[124:127], v[44:47]\n"
"v_mfma_f32_16x16x32_bf16 v[44:47], a[32:35], a[120:123], v[44:47]\n"
"s_waitcnt lgkmcnt(4)\n"
"v_mfma_f32_16x16x32_bf16 v[48:51], a[32:35], a[128:131], v[48:51]\n"
"v_mfma_f32_16x16x32_bf16 v[48:51], a[36:39], a[132:135], v[48:51]\n"
"global_load_dwordx4 a[88:91], v0, s[34:35] offset:2048 sc1\n"
"v_mfma_f32_16x16x32_bf16 v[76:79], a[44:47], a[132:135], v[76:79]\n"
"v_mfma_f32_16x16x32_bf16 v[76:79], a[40:43], a[128:131], v[76:79]\n"
"buffer_load_dwordx4 v1, s[48:51], s75 offen offset:2048 sc1 lds\n"
"v_mfma_f32_16x16x32_bf16 v[80:83], a[40:43], a[136:139], v[80:83]\n"
"v_mfma_f32_16x16x32_bf16 v[80:83], a[44:47], a[140:143], v[80:83]\n"
"v_mfma_f32_16x16x32_bf16 v[52:55], a[36:39], a[140:143], v[52:55]\n"
"v_mfma_f32_16x16x32_bf16 v[52:55], a[32:35], a[136:139], v[52:55]\n"
"s_waitcnt lgkmcnt(0)\n"
"v_mfma_f32_16x16x32_bf16 v[56:59], a[32:35], a[144:147], v[56:59]\n"
"v_mfma_f32_16x16x32_bf16 v[56:59], a[36:39], a[148:151], v[56:59]\n"
"global_load_dwordx4 a[92:95], v0, s[34:35] offset:3072 sc1\n"
"v_mfma_f32_16x16x32_bf16 v[84:87], a[44:47], a[148:151], v[84:87]\n"
"v_mfma_f32_16x16x32_bf16 v[84:87], a[40:43], a[144:147], v[84:87]\n"
"v_mfma_f32_16x16x32_bf16 a[4:7], a[40:43], a[152:155], a[4:7]\n"
"s_add_u32 s34, s34, 8192\n"
"v_mfma_f32_16x16x32_bf16 a[4:7], a[44:47], a[156:159], a[4:7]\n"
"v_mfma_f32_16x16x32_bf16 a[0:3], a[36:39], a[156:159], a[0:3]\n"
"v_mfma_f32_16x16x32_bf16 a[0:3], a[32:35], a[152:155], a[0:3]\n"
"s_waitcnt vmcnt(11)\n"
"v_mfma_f32_16x16x32_bf16 v[88:91], a[48:51], a[96:99], v[88:91]\n"
"v_mfma_f32_16x16x32_bf16 v[88:91], a[52:55], a[100:103], v[88:91]\n"
"global_load_dwordx4 a[32:35], v0, s[34:35] offset:-4096 sc1\n"
"v_mfma_f32_16x16x32_bf16 v[116:119], a[60:63], a[100:103], v[116:119]\n"
"v_mfma_f32_16x16x32_bf16 v[116:119], a[56:59], a[96:99], v[116:119]\n"
"buffer_load_dwordx4 v1, s[48:51], s75 offen offset:3072 sc1 lds\n"
"v_mfma_f32_16x16x32_bf16 v[120:123], a[56:59], a[104:107], v[120:123]\n"
"v_mfma_f32_16x16x32_bf16 v[120:123], a[60:63], a[108:111], v[120:123]\n"
"v_mfma_f32_16x16x32_bf16 v[92:95], a[52:55], a[108:111], v[92:95]\n"
"v_mfma_f32_16x16x32_bf16 v[92:95], a[48:51], a[104:107], v[92:95]\n"
"v_mfma_f32_16x16x32_bf16 v[96:99], a[48:51], a[112:115], v[96:99]\n"
"v_mfma_f32_16x16x32_bf16 v[96:99], a[52:55], a[116:119], v[96:99]\n"
"global_load_dwordx4 a[36:39], v0, s[34:35] offset:-3072 sc1\n"
"v_mfma_f32_16x16x32_bf16 v[124:127], a[60:63], a[116:119], v[124:127]\n"
"v_mfma_f32_16x16x32_bf16 v[124:127], a[56:59], a[112:115], v[124:127]\n"
"v_mfma_f32_16x16x32_bf16 v[128:131], a[56:59], a[120:123], v[128:131]\n"
"v_mfma_f32_16x16x32_bf16 v[128:131], a[60:63], a[124:127], v[128:131]\n"
"v_mfma_f32_16x16x32_bf16 v[100:103], a[52:55], a[124:127], v[100:103]\n"
"v_mfma_f32_16x16x32_bf16 v[100:103], a[48:51], a[120:123], v[100:103]\n"
"v_mfma_f32_16x16x32_bf16 v[104:107], a[48:51], a[128:131], v[104:107]\n"
"v_mfma_f32_16x16x32_bf16 v[104:107], a[52:55], a[132:135], v[104:107]\n"
"global_load_dwordx4 a[40:43], v0, s[34:35] offset:-2048 sc1\n"
"v_mfma_f32_16x16x32_bf16 v[132:135], a[60:63], a[132:135], v[132:135]\n"
"v_mfma_f32_16x16x32_bf16 v[132:135], a[56:59], a[128:131], v[132:135]\n"
"s_add_u32 m0, s64, 24576\n"
"v_mfma_f32_16x16x32_bf16 v[136:139], a[56:59], a[136:139], v[136:139]\n"
"v_mfma_f32_16x16x32_bf16 v[136:139], a[60:63], a[140:143], v[136:139]\n"
"v_mfma_f32_16x16x32_bf16 v[108:111], a[52:55], a[140:143], v[108:111]\n"
"v_mfma_f32_16x16x32_bf16 v[108:111], a[48:51], a[136:139], v[108:111]\n"
"v_mfma_f32_16x16x32_bf16 v[112:115], a[48:51], a[144:147], v[112:115]\n"
"v_mfma_f32_16x16x32_bf16 v[112:115], a[52:55], a[148:151], v[112:115]\n"
"global_load_dwordx4 a[44:47], v0, s[34:35] offset:-1024 sc1\n"
"v_mfma_f32_16x16x32_bf16 v[140:143], a[60:63], a[148:151], v[140:143]\n"
"v_mfma_f32_16x16x32_bf16 v[140:143], a[56:59], a[144:147], v[140:143]\n"
"v_mfma_f32_16x16x32_bf16 a[12:15], a[56:59], a[152:155], a[12:15]\n"
"v_mfma_f32_16x16x32_bf16 a[12:15], a[60:63], a[156:159], a[12:15]\n"
"v_mfma_f32_16x16x32_bf16 a[8:11], a[52:55], a[156:159], a[8:11]\n"
"v_mfma_f32_16x16x32_bf16 a[8:11], a[48:51], a[152:155], a[8:11]\n"
"s_waitcnt vmcnt(11)\n"
"v_mfma_f32_16x16x32_bf16 v[144:147], a[64:67], a[96:99], v[144:147]\n"
"v_mfma_f32_16x16x32_bf16 v[144:147], a[68:71], a[100:103], v[144:147]\n"
"global_load_dwordx4 a[48:51], v0, s[34:35] offset:0 sc1\n"
"v_mfma_f32_16x16x32_bf16 v[172:175], a[76:79], a[100:103], v[172:175]\n"
"v_mfma_f32_16x16x32_bf16 v[172:175], a[72:75], a[96:99], v[172:175]\n"
"buffer_load_dwordx4 v0, s[48:51], s69 offen offset:0 sc1 lds\n"
"v_mfma_f32_16x16x32_bf16 v[176:179], a[72:75], a[104:107], v[176:179]\n"
"v_mfma_f32_16x16x32_bf16 v[176:179], a[76:79], a[108:111], v[176:179]\n"
"v_mfma_f32_16x16x32_bf16 v[148:151], a[68:71], a[108:111], v[148:151]\n"
"v_mfma_f32_16x16x32_bf16 v[148:151], a[64:67], a[104:107], v[148:151]\n"
"v_mfma_f32_16x16x32_bf16 v[152:155], a[64:67], a[112:115], v[152:155]\n"
"v_lshlrev_b32 v20, 16, v22\n"
"v_mfma_f32_16x16x32_bf16 v[152:155], a[68:71], a[116:119], v[152:155]\n"
"global_load_dwordx4 a[52:55], v0, s[34:35] offset:1024 sc1\n"
"v_and_b32 v21, s63, v22\n"
"v_mfma_f32_16x16x32_bf16 v[180:183], a[76:79], a[116:119], v[180:183]\n"
"v_lshlrev_b32 v22, 16, v23\n"
"v_mfma_f32_16x16x32_bf16 v[180:183], a[72:75], a[112:115], v[180:183]\n"
"v_and_b32 v23, s63, v23\n"
"v_mfma_f32_16x16x32_bf16 v[184:187], a[72:75], a[120:123], v[184:187]\n"
"v_add_f32 v116, v116, v20\n"
"v_mfma_f32_16x16x32_bf16 v[184:187], a[76:79], a[124:127], v[184:187]\n"
"v_add_f32 v117, v117, v21\n"
"v_add_f32 v118, v118, v22\n"
"v_mfma_f32_16x16x32_bf16 v[156:159], a[68:71], a[124:127], v[156:159]\n"
"v_add_f32 v119, v119, v23\n"
"v_mfma_f32_16x16x32_bf16 v[156:159], a[64:67], a[120:123], v[156:159]\n"
"v_add_f32 v120, v120, v20\n"
"v_mfma_f32_16x16x32_bf16 v[160:163], a[64:67], a[128:131], v[160:163]\n"
"v_add_f32 v121, v121, v21\n"
"v_mfma_f32_16x16x32_bf16 v[160:163], a[68:71], a[132:135], v[160:163]\n"
"global_load_dwordx4 a[56:59], v0, s[34:35] offset:2048 sc1\n"
"v_add_f32 v122, v122, v22\n"
"v_mfma_f32_16x16x32_bf16 v[188:191], a[76:79], a[132:135], v[188:191]\n"
"v_add_f32 v123, v123, v23\n"
"v_add_f32 v124, v124, v20\n"
"v_mfma_f32_16x16x32_bf16 v[188:191], a[72:75], a[128:131], v[188:191]\n"
"buffer_load_dwordx4 v0, s[48:51], s69 offen offset:1024 sc1 lds\n"
"v_mfma_f32_16x16x32_bf16 v[192:195], a[72:75], a[136:139], v[192:195]\n"
"v_add_f32 v125, v125, v21\n"
"v_mfma_f32_16x16x32_bf16 v[192:195], a[76:79], a[140:143], v[192:195]\n"
"v_add_f32 v126, v126, v22\n"
"v_mfma_f32_16x16x32_bf16 v[164:167], a[68:71], a[140:143], v[164:167]\n"
"v_add_f32 v127, v127, v23\n"
"v_mfma_f32_16x16x32_bf16 v[164:167], a[64:67], a[136:139], v[164:167]\n"
"v_add_f32 v128, v128, v20\n"
"v_add_f32 v129, v129, v21\n"
"v_mfma_f32_16x16x32_bf16 v[168:171], a[64:67], a[144:147], v[168:171]\n"
"v_add_f32 v130, v130, v22\n"
"v_mfma_f32_16x16x32_bf16 v[168:171], a[68:71], a[148:151], v[168:171]\n"
"global_load_dwordx4 a[60:63], v0, s[34:35] offset:3072 sc1\n"
"v_add_f32 v131, v131, v23\n"
"v_mfma_f32_16x16x32_bf16 v[196:199], a[76:79], a[148:151], v[196:199]\n"
"v_add_f32 v132, v132, v20\n"
"v_mfma_f32_16x16x32_bf16 v[196:199], a[72:75], a[144:147], v[196:199]\n"
"v_add_f32 v133, v133, v21\n"
"v_mfma_f32_16x16x32_bf16 a[20:23], a[72:75], a[152:155], a[20:23]\n"
"s_add_u32 s34, s34, 8192\n"
"v_add_f32 v134, v134, v22\n"
"v_mfma_f32_16x16x32_bf16 a[20:23], a[76:79], a[156:159], a[20:23]\n"
"v_add_f32 v135, v135, v23\n"
"v_mfma_f32_16x16x32_bf16 a[16:19], a[68:71], a[156:159], a[16:19]\n"
"v_add_f32 v136, v136, v20\n"
"v_mfma_f32_16x16x32_bf16 a[16:19], a[64:67], a[152:155], a[16:19]\n"
"s_waitcnt vmcnt(11)\n"
"v_add_f32 v137, v137, v21\n"
"v_mfma_f32_16x16x32_bf16 v[200:203], a[80:83], a[96:99], v[200:203]\n"
"v_add_f32 v138, v138, v22\n"
"v_add_f32 v139, v139, v23\n"
"v_mfma_f32_16x16x32_bf16 v[200:203], a[84:87], a[100:103], v[200:203]\n"
"global_load_dwordx4 a[64:67], v0, s[34:35] offset:-4096 sc1\n"
"v_add_f32 v140, v140, v20\n"
"v_mfma_f32_16x16x32_bf16 v[228:231], a[92:95], a[100:103], v[228:231]\n"
"ds_read_b128 a[100:103], v4 offset:1024\n"
"v_add_f32 v141, v141, v21\n"
"v_mfma_f32_16x16x32_bf16 v[228:231], a[88:91], a[96:99], v[228:231]\n"
"ds_read_b128 a[96:99], v4 offset:0\n"
"v_add_f32 v142, v142, v22\n"
"v_mfma_f32_16x16x32_bf16 v[232:235], a[88:91], a[104:107], v[232:235]\n"
"buffer_load_dwordx4 v0, s[48:51], s69 offen offset:2048 sc1 lds\n"
"v_mfma_f32_16x16x32_bf16 v[232:235], a[92:95], a[108:111], v[232:235]\n"
"v_add_f32 v143, v143, v23\n"
"v_accvgpr_read_b32 v12, a12\n"
"v_mfma_f32_16x16x32_bf16 v[204:207], a[84:87], a[108:111], v[204:207]\n"
"ds_read_b128 a[108:111], v4 offset:3072\n"
"v_accvgpr_read_b32 v13, a13\n"
"v_mfma_f32_16x16x32_bf16 v[204:207], a[80:83], a[104:107], v[204:207]\n"
"ds_read_b128 a[104:107], v4 offset:2048\n"
"v_accvgpr_read_b32 v14, a14\n"
"v_mfma_f32_16x16x32_bf16 v[208:211], a[80:83], a[112:115], v[208:211]\n"
"v_accvgpr_read_b32 v15, a15\n"
"v_mfma_f32_16x16x32_bf16 v[208:211], a[84:87], a[116:119], v[208:211]\n"
"global_load_dwordx4 a[68:71], v0, s[34:35] offset:-3072 sc1\n"
"v_add_f32 v12, v12, v20\n"
"v_mfma_f32_16x16x32_bf16 v[236:239], a[92:95], a[116:119], v[236:239]\n"
"ds_read_b128 a[116:119], v4 offset:5120\n"
"v_add_f32 v13, v13, v21\n"
"v_mfma_f32_16x16x32_bf16 v[236:239], a[88:91], a[112:115], v[236:239]\n"
"ds_read_b128 a[112:115], v4 offset:4096\n"
"v_add_f32 v14, v14, v22\n"
"v_mfma_f32_16x16x32_bf16 v[240:243], a[88:91], a[120:123], v[240:243]\n"
"v_add_f32 v15, v15, v23\n"
"v_accvgpr_write_b32 a12, v12\n"
"v_mfma_f32_16x16x32_bf16 v[240:243], a[92:95], a[124:127], v[240:243]\n"
"v_accvgpr_write_b32 a13, v13\n"
"v_mfma_f32_16x16x32_bf16 v[212:215], a[84:87], a[124:127], v[212:215]\n"
"ds_read_b128 a[124:127], v4 offset:7168\n"
"v_accvgpr_write_b32 a14, v14\n"
"v_mfma_f32_16x16x32_bf16 v[212:215], a[80:83], a[120:123], v[212:215]\n"
"ds_read_b128 a[120:123], v4 offset:6144\n"
"v_accvgpr_write_b32 a15, v15\n"
"v_mfma_f32_16x16x32_bf16 v[216:219], a[80:83], a[128:131], v[216:219]\n"
"v_mfma_f32_16x16x32_bf16 v[216:219], a[84:87], a[132:135], v[216:219]\n"
"global_load_dwordx4 a[72:75], v0, s[34:35] offset:-2048 sc1\n"
"v_mfma_f32_16x16x32_bf16 v[244:247], a[92:95], a[132:135], v[244:247]\n"
"ds_read_b128 a[132:135], v4 offset:9216\n"
"v_mfma_f32_16x16x32_bf16 v[244:247], a[88:91], a[128:131], v[244:247]\n"
"ds_read_b128 a[128:131], v4 offset:8192\n"
"v_mfma_f32_16x16x32_bf16 v[248:251], a[88:91], a[136:139], v[248:251]\n"
"buffer_load_dwordx4 v0, s[48:51], s69 offen offset:3072 sc1 lds\n"
"v_mfma_f32_16x16x32_bf16 v[248:251], a[92:95], a[140:143], v[248:251]\n"
"v_mfma_f32_16x16x32_bf16 v[220:223], a[84:87], a[140:143], v[220:223]\n"
"ds_read_b128 a[140:143], v4 offset:11264\n"
"v_mfma_f32_16x16x32_bf16 v[220:223], a[80:83], a[136:139], v[220:223]\n"
"ds_read_b128 a[136:139], v4 offset:10240\n"
"v_mfma_f32_16x16x32_bf16 v[224:227], a[80:83], a[144:147], v[224:227]\n"
"v_mfma_f32_16x16x32_bf16 v[224:227], a[84:87], a[148:151], v[224:227]\n"
"global_load_dwordx4 a[76:79], v0, s[34:35] offset:-1024 sc1\n"
"v_mfma_f32_16x16x32_bf16 v[252:255], a[92:95], a[148:151], v[252:255]\n"
"ds_read_b128 a[148:151], v4 offset:13312\n"
"v_mfma_f32_16x16x32_bf16 v[252:255], a[88:91], a[144:147], v[252:255]\n"
"ds_read_b128 a[144:147], v4 offset:12288\n"
"v_mfma_f32_16x16x32_bf16 a[28:31], a[88:91], a[152:155], a[28:31]\n"
"v_mfma_f32_16x16x32_bf16 a[28:31], a[92:95], a[156:159], a[28:31]\n"
"v_mfma_f32_16x16x32_bf16 a[24:27], a[84:87], a[156:159], a[24:27]\n"
"ds_read_b128 a[156:159], v4 offset:15360\n"
"v_mfma_f32_16x16x32_bf16 a[24:27], a[80:83], a[152:155], a[24:27]\n"
"s_waitcnt vmcnt(12) lgkmcnt(11)\n"
"ds_read_b128 a[152:155], v4 offset:14336\n"
"v_mfma_f32_16x16x32_bf16 v[32:35], a[32:35], a[96:99], v[32:35]\n"
"v_mfma_f32_16x16x32_bf16 v[32:35], a[36:39], a[100:103], v[32:35]\n"
"global_load_dwordx4 a[80:83], v0, s[34:35] offset:0 sc1\n"
"v_lshrrev_b32 v16, 4, v0\n"
"v_mfma_f32_16x16x32_bf16 v[60:63], a[44:47], a[100:103], v[60:63]\n"
"v_and_b32 v16, 0x30, v16\n"
"v_mfma_f32_16x16x32_bf16 v[60:63], a[40:43], a[96:99], v[60:63]\n"
"s_add_u32 m0, s64, 28672\n"
"buffer_load_dwordx2 v[18:19], v16, s[44:47], s61 offen offset:128\n"
"v_mfma_f32_16x16x32_bf16 v[64:67], a[40:43], a[104:107], v[64:67]\n"
"v_mfma_f32_16x16x32_bf16 v[64:67], a[44:47], a[108:111], v[64:67]\n"
"v_mfma_f32_16x16x32_bf16 v[36:39], a[36:39], a[108:111], v[36:39]\n"
"buffer_load_dwordx4 v[12:15], v0, s[56:59], %25 offen sc1\n"
"v_mfma_f32_16x16x32_bf16 v[36:39], a[32:35], a[104:107], v[36:39]\n"
"s_waitcnt lgkmcnt(8)\n"
"v_mfma_f32_16x16x32_bf16 v[40:43], a[32:35], a[112:115], v[40:43]\n"
"v_mfma_f32_16x16x32_bf16 v[40:43], a[36:39], a[116:119], v[40:43]\n"
"global_load_dwordx4 a[84:87], v0, s[34:35] offset:1024 sc1\n"
"v_lshrrev_b32 v20, 4, v0\n"
"v_mfma_f32_16x16x32_bf16 v[68:71], a[44:47], a[116:119], v[68:71]\n"
"v_and_b32 v20, 0x30, v20\n"
"v_mfma_f32_16x16x32_bf16 v[68:71], a[40:43], a[112:115], v[68:71]\n"
"buffer_load_dwordx2 v[22:23], v20, s[44:47], s61 offen offset:136\n"
"v_mfma_f32_16x16x32_bf16 v[72:75], a[40:43], a[120:123], v[72:75]\n"
"v_mfma_f32_16x16x32_bf16 v[72:75], a[44:47], a[124:127], v[72:75]\n"
"v_mfma_f32_16x16x32_bf16 v[44:47], a[36:39], a[124:127], v[44:47]\n"
"v_mfma_f32_16x16x32_bf16 v[44:47], a[32:35], a[120:123], v[44:47]\n"
"s_waitcnt lgkmcnt(4)\n"
"v_mfma_f32_16x16x32_bf16 v[48:51], a[32:35], a[128:131], v[48:51]\n"
"v_mfma_f32_16x16x32_bf16 v[48:51], a[36:39], a[132:135], v[48:51]\n"
"global_load_dwordx4 a[88:91], v0, s[34:35] offset:2048 sc1\n"
"v_lshrrev_b32 v24, 4, v0\n"
"v_mfma_f32_16x16x32_bf16 v[76:79], a[44:47], a[132:135], v[76:79]\n"
"v_and_b32 v24, 0x30, v24\n"
"v_mfma_f32_16x16x32_bf16 v[76:79], a[40:43], a[128:131], v[76:79]\n"
"buffer_load_dwordx4 v1, s[48:51], s69 offen offset:0 sc1 lds\n"
"buffer_load_dwordx2 v[26:27], v24, s[44:47], s61 offen offset:192\n"
"v_mfma_f32_16x16x32_bf16 v[80:83], a[40:43], a[136:139], v[80:83]\n"
"v_mfma_f32_16x16x32_bf16 v[80:83], a[44:47], a[140:143], v[80:83]\n"
"v_mfma_f32_16x16x32_bf16 v[52:55], a[36:39], a[140:143], v[52:55]\n"
"v_mfma_f32_16x16x32_bf16 v[52:55], a[32:35], a[136:139], v[52:55]\n"
"s_waitcnt lgkmcnt(0)\n"
"v_mfma_f32_16x16x32_bf16 v[56:59], a[32:35], a[144:147], v[56:59]\n"
"v_mfma_f32_16x16x32_bf16 v[56:59], a[36:39], a[148:151], v[56:59]\n"
"global_load_dwordx4 a[92:95], v0, s[34:35] offset:3072 sc1\n"
"v_lshrrev_b32 v28, 4, v0\n"
"v_mfma_f32_16x16x32_bf16 v[84:87], a[44:47], a[148:151], v[84:87]\n"
"v_and_b32 v28, 0x30, v28\n"
"v_mfma_f32_16x16x32_bf16 v[84:87], a[40:43], a[144:147], v[84:87]\n"
"buffer_load_dwordx2 v[30:31], v28, s[44:47], s61 offen offset:200\n"
"v_mfma_f32_16x16x32_bf16 a[4:7], a[40:43], a[152:155], a[4:7]\n"
"s_add_u32 s34, s34, 8192\n"
"v_mfma_f32_16x16x32_bf16 a[4:7], a[44:47], a[156:159], a[4:7]\n"
"v_mfma_f32_16x16x32_bf16 a[0:3], a[36:39], a[156:159], a[0:3]\n"
"v_mfma_f32_16x16x32_bf16 a[0:3], a[32:35], a[152:155], a[0:3]\n"
"s_waitcnt vmcnt(16)\n"
"v_mfma_f32_16x16x32_bf16 v[88:91], a[48:51], a[96:99], v[88:91]\n"
"v_mfma_f32_16x16x32_bf16 v[88:91], a[52:55], a[100:103], v[88:91]\n"
"global_load_dwordx4 a[32:35], v0, s[34:35] offset:-4096 sc1\n"
"v_mfma_f32_16x16x32_bf16 v[116:119], a[60:63], a[100:103], v[116:119]\n"
"v_mfma_f32_16x16x32_bf16 v[116:119], a[56:59], a[96:99], v[116:119]\n"
"buffer_load_dwordx4 v1, s[48:51], s69 offen offset:1024 sc1 lds\n"
"v_mfma_f32_16x16x32_bf16 v[120:123], a[56:59], a[104:107], v[120:123]\n"
"v_mfma_f32_16x16x32_bf16 v[120:123], a[60:63], a[108:111], v[120:123]\n"
"v_mfma_f32_16x16x32_bf16 v[92:95], a[52:55], a[108:111], v[92:95]\n"
"v_mfma_f32_16x16x32_bf16 v[92:95], a[48:51], a[104:107], v[92:95]\n"
"v_mfma_f32_16x16x32_bf16 v[96:99], a[48:51], a[112:115], v[96:99]\n"
"v_mfma_f32_16x16x32_bf16 v[96:99], a[52:55], a[116:119], v[96:99]\n"
"global_load_dwordx4 a[36:39], v0, s[34:35] offset:-3072 sc1\n"
"v_mfma_f32_16x16x32_bf16 v[124:127], a[60:63], a[116:119], v[124:127]\n"
"v_mfma_f32_16x16x32_bf16 v[124:127], a[56:59], a[112:115], v[124:127]\n"
"v_mfma_f32_16x16x32_bf16 v[128:131], a[56:59], a[120:123], v[128:131]\n"
"v_mfma_f32_16x16x32_bf16 v[128:131], a[60:63], a[124:127], v[128:131]\n"
"v_mfma_f32_16x16x32_bf16 v[100:103], a[52:55], a[124:127], v[100:103]\n"
"v_mfma_f32_16x16x32_bf16 v[100:103], a[48:51], a[120:123], v[100:103]\n"
"v_mfma_f32_16x16x32_bf16 v[104:107], a[48:51], a[128:131], v[104:107]\n"
"v_mfma_f32_16x16x32_bf16 v[104:107], a[52:55], a[132:135], v[104:107]\n"
"global_load_dwordx4 a[40:43], v0, s[34:35] offset:-2048 sc1\n"
"v_mfma_f32_16x16x32_bf16 v[132:135], a[60:63], a[132:135], v[132:135]\n"
"v_mfma_f32_16x16x32_bf16 v[132:135], a[56:59], a[128:131], v[132:135]\n"
"buffer_load_dwordx4 v1, s[48:51], s69 offen offset:2048 sc1 lds\n"
"v_mfma_f32_16x16x32_bf16 v[136:139], a[56:59], a[136:139], v[136:139]\n"
"v_mfma_f32_16x16x32_bf16 v[136:139], a[60:63], a[140:143], v[136:139]\n"
"v_mfma_f32_16x16x32_bf16 v[108:111], a[52:55], a[140:143], v[108:111]\n"
"v_mfma_f32_16x16x32_bf16 v[108:111], a[48:51], a[136:139], v[108:111]\n"
"v_mfma_f32_16x16x32_bf16 v[112:115], a[48:51], a[144:147], v[112:115]\n"
"v_mfma_f32_16x16x32_bf16 v[112:115], a[52:55], a[148:151], v[112:115]\n"
"global_load_dwordx4 a[44:47], v0, s[34:35] offset:-1024 sc1\n"
"v_mfma_f32_16x16x32_bf16 v[140:143], a[60:63], a[148:151], v[140:143]\n"
"v_mfma_f32_16x16x32_bf16 v[140:143], a[56:59], a[144:147], v[140:143]\n"
"v_mfma_f32_16x16x32_bf16 a[12:15], a[56:59], a[152:155], a[12:15]\n"
"v_mfma_f32_16x16x32_bf16 a[12:15], a[60:63], a[156:159], a[12:15]\n"
"v_mfma_f32_16x16x32_bf16 a[8:11], a[52:55], a[156:159], a[8:11]\n"
"v_mfma_f32_16x16x32_bf16 a[8:11], a[48:51], a[152:155], a[8:11]\n"
"s_waitcnt vmcnt(16)\n"
"v_mfma_f32_16x16x32_bf16 v[144:147], a[64:67], a[96:99], v[144:147]\n"
"v_mfma_f32_16x16x32_bf16 v[144:147], a[68:71], a[100:103], v[144:147]\n"
"global_load_dwordx4 a[48:51], v0, s[34:35] offset:0 sc1\n"
"v_mfma_f32_16x16x32_bf16 v[172:175], a[76:79], a[100:103], v[172:175]\n"
"v_mfma_f32_16x16x32_bf16 v[172:175], a[72:75], a[96:99], v[172:175]\n"
"buffer_load_dwordx4 v1, s[48:51], s69 offen offset:3072 sc1 lds\n"
"v_mfma_f32_16x16x32_bf16 v[176:179], a[72:75], a[104:107], v[176:179]\n"
"v_mfma_f32_16x16x32_bf16 v[176:179], a[76:79], a[108:111], v[176:179]\n"
"v_mfma_f32_16x16x32_bf16 v[148:151], a[68:71], a[108:111], v[148:151]\n"
"v_mfma_f32_16x16x32_bf16 v[148:151], a[64:67], a[104:107], v[148:151]\n"
"v_mfma_f32_16x16x32_bf16 v[152:155], a[64:67], a[112:115], v[152:155]\n"
"v_mfma_f32_16x16x32_bf16 v[152:155], a[68:71], a[116:119], v[152:155]\n"
"global_load_dwordx4 a[52:55], v0, s[34:35] offset:1024 sc1\n"
"v_mfma_f32_16x16x32_bf16 v[180:183], a[76:79], a[116:119], v[180:183]\n"
"v_mfma_f32_16x16x32_bf16 v[180:183], a[72:75], a[112:115], v[180:183]\n"
"v_mfma_f32_16x16x32_bf16 v[184:187], a[72:75], a[120:123], v[184:187]\n"
"v_mfma_f32_16x16x32_bf16 v[184:187], a[76:79], a[124:127], v[184:187]\n"
"v_mfma_f32_16x16x32_bf16 v[156:159], a[68:71], a[124:127], v[156:159]\n"
"v_mfma_f32_16x16x32_bf16 v[156:159], a[64:67], a[120:123], v[156:159]\n"
"v_mfma_f32_16x16x32_bf16 v[160:163], a[64:67], a[128:131], v[160:163]\n"
"v_mfma_f32_16x16x32_bf16 v[160:163], a[68:71], a[132:135], v[160:163]\n"
"global_load_dwordx4 a[56:59], v0, s[34:35] offset:2048 sc1\n"
"v_mfma_f32_16x16x32_bf16 v[188:191], a[76:79], a[132:135], v[188:191]\n"
"v_mfma_f32_16x16x32_bf16 v[188:191], a[72:75], a[128:131], v[188:191]\n"
"v_mfma_f32_16x16x32_bf16 v[192:195], a[72:75], a[136:139], v[192:195]\n"
"v_mfma_f32_16x16x32_bf16 v[192:195], a[76:79], a[140:143], v[192:195]\n"
"v_mfma_f32_16x16x32_bf16 v[164:167], a[68:71], a[140:143], v[164:167]\n"
"v_mfma_f32_16x16x32_bf16 v[164:167], a[64:67], a[136:139], v[164:167]\n"
"v_mfma_f32_16x16x32_bf16 v[168:171], a[64:67], a[144:147], v[168:171]\n"
"v_mfma_f32_16x16x32_bf16 v[168:171], a[68:71], a[148:151], v[168:171]\n"
"global_load_dwordx4 a[60:63], v0, s[34:35] offset:3072 sc1\n"
"v_mfma_f32_16x16x32_bf16 v[196:199], a[76:79], a[148:151], v[196:199]\n"
"v_mfma_f32_16x16x32_bf16 v[196:199], a[72:75], a[144:147], v[196:199]\n"
"v_mfma_f32_16x16x32_bf16 a[20:23], a[72:75], a[152:155], a[20:23]\n"
"s_add_u32 s34, s34, 8192\n"
"v_mfma_f32_16x16x32_bf16 a[20:23], a[76:79], a[156:159], a[20:23]\n"
"v_mfma_f32_16x16x32_bf16 a[16:19], a[68:71], a[156:159], a[16:19]\n"
"v_mfma_f32_16x16x32_bf16 a[16:19], a[64:67], a[152:155], a[16:19]\n"
"s_waitcnt vmcnt(12)\n"
"v_mfma_f32_16x16x32_bf16 v[200:203], a[80:83], a[96:99], v[200:203]\n"
"v_mfma_f32_16x16x32_bf16 v[200:203], a[84:87], a[100:103], v[200:203]\n"
"global_load_dwordx4 a[64:67], v0, s[34:35] offset:-4096 sc1\n"
"v_mfma_f32_16x16x32_bf16 v[228:231], a[92:95], a[100:103], v[228:231]\n"
"ds_read_b128 a[100:103], v5 offset:1024\n"
"v_mfma_f32_16x16x32_bf16 v[228:231], a[88:91], a[96:99], v[228:231]\n"
"ds_read_b128 a[96:99], v5 offset:0\n"
"v_mfma_f32_16x16x32_bf16 v[232:235], a[88:91], a[104:107], v[232:235]\n"
"v_mfma_f32_16x16x32_bf16 v[232:235], a[92:95], a[108:111], v[232:235]\n"
"v_mfma_f32_16x16x32_bf16 v[204:207], a[84:87], a[108:111], v[204:207]\n"
"ds_read_b128 a[108:111], v5 offset:3072\n"
"v_mfma_f32_16x16x32_bf16 v[204:207], a[80:83], a[104:107], v[204:207]\n"
"ds_read_b128 a[104:107], v5 offset:2048\n"
"v_mfma_f32_16x16x32_bf16 v[208:211], a[80:83], a[112:115], v[208:211]\n"
"v_mfma_f32_16x16x32_bf16 v[208:211], a[84:87], a[116:119], v[208:211]\n"
"global_load_dwordx4 a[68:71], v0, s[34:35] offset:-3072 sc1\n"
"v_mfma_f32_16x16x32_bf16 v[236:239], a[92:95], a[116:119], v[236:239]\n"
"ds_read_b128 a[116:119], v5 offset:5120\n"
"v_mfma_f32_16x16x32_bf16 v[236:239], a[88:91], a[112:115], v[236:239]\n"
"ds_read_b128 a[112:115], v5 offset:4096\n"
"v_mfma_f32_16x16x32_bf16 v[240:243], a[88:91], a[120:123], v[240:243]\n"
"v_mfma_f32_16x16x32_bf16 v[240:243], a[92:95], a[124:127], v[240:243]\n"
"v_mfma_f32_16x16x32_bf16 v[212:215], a[84:87], a[124:127], v[212:215]\n"
"ds_read_b128 a[124:127], v5 offset:7168\n"
"v_mfma_f32_16x16x32_bf16 v[212:215], a[80:83], a[120:123], v[212:215]\n"
"ds_read_b128 a[120:123], v5 offset:6144\n"
"v_mfma_f32_16x16x32_bf16 v[216:219], a[80:83], a[128:131], v[216:219]\n"
"v_mfma_f32_16x16x32_bf16 v[216:219], a[84:87], a[132:135], v[216:219]\n"
"global_load_dwordx4 a[72:75], v0, s[34:35] offset:-2048 sc1\n"
"v_mfma_f32_16x16x32_bf16 v[244:247], a[92:95], a[132:135], v[244:247]\n"
"ds_read_b128 a[132:135], v5 offset:9216\n"
"v_mfma_f32_16x16x32_bf16 v[244:247], a[88:91], a[128:131], v[244:247]\n"
"ds_read_b128 a[128:131], v5 offset:8192\n"
"v_mfma_f32_16x16x32_bf16 v[248:251], a[88:91], a[136:139], v[248:251]\n"
"v_mfma_f32_16x16x32_bf16 v[248:251], a[92:95], a[140:143], v[248:251]\n"
"v_mfma_f32_16x16x32_bf16 v[220:223], a[84:87], a[140:143], v[220:223]\n"
"ds_read_b128 a[140:143], v5 offset:11264\n"
"v_mfma_f32_16x16x32_bf16 v[220:223], a[80:83], a[136:139], v[220:223]\n"
"ds_read_b128 a[136:139], v5 offset:10240\n"
"v_mfma_f32_16x16x32_bf16 v[224:227], a[80:83], a[144:147], v[224:227]\n"
"v_mfma_f32_16x16x32_bf16 v[224:227], a[84:87], a[148:151], v[224:227]\n"
"global_load_dwordx4 a[76:79], v0, s[34:35] offset:-1024 sc1\n"
"v_mfma_f32_16x16x32_bf16 v[252:255], a[92:95], a[148:151], v[252:255]\n"
"ds_read_b128 a[148:151], v5 offset:13312\n"
"v_mfma_f32_16x16x32_bf16 v[252:255], a[88:91], a[144:147], v[252:255]\n"
"ds_read_b128 a[144:147], v5 offset:12288\n"
"v_mfma_f32_16x16x32_bf16 a[28:31], a[88:91], a[152:155], a[28:31]\n"
"v_mfma_f32_16x16x32_bf16 a[28:31], a[92:95], a[156:159], a[28:31]\n"
"v_mfma_f32_16x16x32_bf16 a[24:27], a[84:87], a[156:159], a[24:27]\n"
"ds_read_b128 a[156:159], v5 offset:15360\n"
"v_mfma_f32_16x16x32_bf16 a[24:27], a[80:83], a[152:155], a[24:27]\n"
"s_waitcnt vmcnt(9) lgkmcnt(11)\n"
"ds_read_b128 a[152:155], v5 offset:14336\n"
"v_mfma_f32_16x16x32_bf16 v[32:35], a[32:35], a[96:99], v[32:35]\n"
"v_mfma_f32_16x16x32_bf16 v[32:35], a[36:39], a[100:103], v[32:35]\n"
"global_load_dwordx4 a[80:83], v0, s[34:35] offset:0 sc1\n"
"v_mfma_f32_16x16x32_bf16 v[60:63], a[44:47], a[100:103], v[60:63]\n"
"v_mfma_f32_16x16x32_bf16 v[60:63], a[40:43], a[96:99], v[60:63]\n"
"v_mfma_f32_16x16x32_bf16 v[64:67], a[40:43], a[104:107], v[64:67]\n"
"v_mfma_f32_16x16x32_bf16 v[64:67], a[44:47], a[108:111], v[64:67]\n"
"v_mfma_f32_16x16x32_bf16 v[36:39], a[36:39], a[108:111], v[36:39]\n"
"s_add_u32 s67, s78, 2\n"
"v_mfma_f32_16x16x32_bf16 v[36:39], a[32:35], a[104:107], v[36:39]\n"
"s_waitcnt lgkmcnt(8)\n"
"v_mfma_f32_16x16x32_bf16 v[40:43], a[32:35], a[112:115], v[40:43]\n"
"v_mfma_f32_16x16x32_bf16 v[40:43], a[36:39], a[116:119], v[40:43]\n"
"global_load_dwordx4 a[84:87], v0, s[34:35] offset:1024 sc1\n"
"v_mfma_f32_16x16x32_bf16 v[68:71], a[44:47], a[116:119], v[68:71]\n"
"v_mfma_f32_16x16x32_bf16 v[68:71], a[40:43], a[112:115], v[68:71]\n"
"v_mfma_f32_16x16x32_bf16 v[72:75], a[40:43], a[120:123], v[72:75]\n"
"v_mfma_f32_16x16x32_bf16 v[72:75], a[44:47], a[124:127], v[72:75]\n"
"v_mfma_f32_16x16x32_bf16 v[44:47], a[36:39], a[124:127], v[44:47]\n"
"v_mfma_f32_16x16x32_bf16 v[44:47], a[32:35], a[120:123], v[44:47]\n"
"s_waitcnt lgkmcnt(4)\n"
"v_mfma_f32_16x16x32_bf16 v[48:51], a[32:35], a[128:131], v[48:51]\n"
"s_lshr_b32 s66, 20, s62\n"
"v_mfma_f32_16x16x32_bf16 v[48:51], a[36:39], a[132:135], v[48:51]\n"
"global_load_dwordx4 a[88:91], v0, s[34:35] offset:2048 sc1\n"
"v_mfma_f32_16x16x32_bf16 v[76:79], a[44:47], a[132:135], v[76:79]\n"
"s_and_b32 s66, s66, 1\n"
"s_cselect_b32 s70, 4294574080, 0\n"
"s_cselect_b32 s71, -1, 0\n"
"s_cselect_b32 s66, 0, 131072\n"
"v_mfma_f32_16x16x32_bf16 v[76:79], a[40:43], a[128:131], v[76:79]\n"
"v_mfma_f32_16x16x32_bf16 v[80:83], a[40:43], a[136:139], v[80:83]\n"
"s_add_u32 s70, s70, s66\n"
"v_subrev_u32 v12, s67, v12\n"
"v_mfma_f32_16x16x32_bf16 v[80:83], a[44:47], a[140:143], v[80:83]\n"
"v_xor_b32 v13, s79, v13\n"
"v_mfma_f32_16x16x32_bf16 v[52:55], a[36:39], a[140:143], v[52:55]\n"
"v_subrev_u32 v14, s67, v14\n"
"v_mfma_f32_16x16x32_bf16 v[52:55], a[32:35], a[136:139], v[52:55]\n"
"s_waitcnt lgkmcnt(0)\n"
"v_xor_b32 v15, s79, v15\n"
"v_mfma_f32_16x16x32_bf16 v[56:59], a[32:35], a[144:147], v[56:59]\n"
"v_or_b32 v12, v12, v14\n"
"v_mfma_f32_16x16x32_bf16 v[56:59], a[36:39], a[148:151], v[56:59]\n"
"global_load_dwordx4 a[92:95], v0, s[34:35] offset:3072 sc1\n"
"v_or_b32 v13, v13, v15\n"
"v_mfma_f32_16x16x32_bf16 v[84:87], a[44:47], a[148:151], v[84:87]\n"
"v_or_b32 v12, v12, v13\n"
"v_mfma_f32_16x16x32_bf16 v[84:87], a[40:43], a[144:147], v[84:87]\n"
"v_cmp_lt_u32 vcc, 15, v12\n"
"v_mfma_f32_16x16x32_bf16 a[4:7], a[40:43], a[152:155], a[4:7]\n"
"s_add_u32 s34, s34, 8192\n"
"s_cbranch_vccnz L_slow2\n"
"L_back2:\n"
"v_mfma_f32_16x16x32_bf16 a[4:7], a[44:47], a[156:159], a[4:7]\n"
"v_mfma_f32_16x16x32_bf16 a[0:3], a[36:39], a[156:159], a[0:3]\n"
"v_mfma_f32_16x16x32_bf16 a[0:3], a[32:35], a[152:155], a[0:3]\n"
"s_waitcnt vmcnt(8)\n"
"v_mfma_f32_16x16x32_bf16 v[88:91], a[48:51], a[96:99], v[88:91]\n"
"v_mfma_f32_16x16x32_bf16 v[88:91], a[52:55], a[100:103], v[88:91]\n"
"global_load_dwordx4 a[32:35], v0, s[34:35] offset:-4096 sc1\n"
"v_mfma_f32_16x16x32_bf16 v[116:119], a[60:63], a[100:103], v[116:119]\n"
"v_mfma_f32_16x16x32_bf16 v[116:119], a[56:59], a[96:99], v[116:119]\n"
"v_mfma_f32_16x16x32_bf16 v[120:123], a[56:59], a[104:107], v[120:123]\n"
"v_mfma_f32_16x16x32_bf16 v[120:123], a[60:63], a[108:111], v[120:123]\n"
"v_mfma_f32_16x16x32_bf16 v[92:95], a[52:55], a[108:111], v[92:95]\n"
"v_mfma_f32_16x16x32_bf16 v[92:95], a[48:51], a[104:107], v[92:95]\n"
"v_mfma_f32_16x16x32_bf16 v[96:99], a[48:51], a[112:115], v[96:99]\n"
"v_mfma_f32_16x16x32_bf16 v[96:99], a[52:55], a[116:119], v[96:99]\n"
"global_load_dwordx4 a[36:39], v0, s[34:35] offset:-3072 sc1\n"
"v_mfma_f32_16x16x32_bf16 v[124:127], a[60:63], a[116:119], v[124:127]\n"
"v_mfma_f32_16x16x32_bf16 v[124:127], a[56:59], a[112:115], v[124:127]\n"
"v_mfma_f32_16x16x32_bf16 v[128:131], a[56:59], a[120:123], v[128:131]\n"
"v_mfma_f32_16x16x32_bf16 v[128:131], a[60:63], a[124:127], v[128:131]\n"
"v_mfma_f32_16x16x32_bf16 v[100:103], a[52:55], a[124:127], v[100:103]\n"
"v_mfma_f32_16x16x32_bf16 v[100:103], a[48:51], a[120:123], v[100:103]\n"
"v_mfma_f32_16x16x32_bf16 v[104:107], a[48:51], a[128:131], v[104:107]\n"
"v_mfma_f32_16x16x32_bf16 v[104:107], a[52:55], a[132:135], v[104:107]\n"
"global_load_dwordx4 a[40:43], v0, s[34:35] offset:-2048 sc1\n"
"v_mfma_f32_16x16x32_bf16 v[132:135], a[60:63], a[132:135], v[132:135]\n"
"v_mfma_f32_16x16x32_bf16 v[132:135], a[56:59], a[128:131], v[132:135]\n"
"v_mfma_f32_16x16x32_bf16 v[136:139], a[56:59], a[136:139], v[136:139]\n"
"v_mfma_f32_16x16x32_bf16 v[136:139], a[60:63], a[140:143], v[136:139]\n"
"v_mfma_f32_16x16x32_bf16 v[108:111], a[52:55], a[140:143], v[108:111]\n"
"v_mfma_f32_16x16x32_bf16 v[108:111], a[48:51], a[136:139], v[108:111]\n"
"v_mfma_f32_16x16x32_bf16 v[112:115], a[48:51], a[144:147], v[112:115]\n"
"v_mfma_f32_16x16x32_bf16 v[112:115], a[52:55], a[148:151], v[112:115]\n"
"global_load_dwordx4 a[44:47], v0, s[34:35] offset:-1024 sc1\n"
"v_mfma_f32_16x16x32_bf16 v[140:143], a[60:63], a[148:151], v[140:143]\n"
"v_mfma_f32_16x16x32_bf16 v[140:143], a[56:59], a[144:147], v[140:143]\n"
"v_mfma_f32_16x16x32_bf16 a[12:15], a[56:59], a[152:155], a[12:15]\n"
"v_mfma_f32_16x16x32_bf16 a[12:15], a[60:63], a[156:159], a[12:15]\n"
"v_mfma_f32_16x16x32_bf16 a[8:11], a[52:55], a[156:159], a[8:11]\n"
"v_mfma_f32_16x16x32_bf16 a[8:11], a[48:51], a[152:155], a[8:11]\n"
"s_waitcnt vmcnt(8)\n"
"v_mfma_f32_16x16x32_bf16 v[144:147], a[64:67], a[96:99], v[144:147]\n"
"v_mfma_f32_16x16x32_bf16 v[144:147], a[68:71], a[100:103], v[144:147]\n"
"global_load_dwordx4 a[48:51], v0, s[34:35] offset:0 sc1\n"
"v_mfma_f32_16x16x32_bf16 v[172:175], a[76:79], a[100:103], v[172:175]\n"
"v_mfma_f32_16x16x32_bf16 v[172:175], a[72:75], a[96:99], v[172:175]\n"
"v_mfma_f32_16x16x32_bf16 v[176:179], a[72:75], a[104:107], v[176:179]\n"
"v_mfma_f32_16x16x32_bf16 v[176:179], a[76:79], a[108:111], v[176:179]\n"
"v_mfma_f32_16x16x32_bf16 v[148:151], a[68:71], a[108:111], v[148:151]\n"
"v_mfma_f32_16x16x32_bf16 v[148:151], a[64:67], a[104:107], v[148:151]\n"
"v_mfma_f32_16x16x32_bf16 v[152:155], a[64:67], a[112:115], v[152:155]\n"
"v_mfma_f32_16x16x32_bf16 v[152:155], a[68:71], a[116:119], v[152:155]\n"
"global_load_dwordx4 a[52:55], v0, s[34:35] offset:1024 sc1\n"
"v_mfma_f32_16x16x32_bf16 v[180:183], a[76:79], a[116:119], v[180:183]\n"
"v_mfma_f32_16x16x32_bf16 v[180:183], a[72:75], a[112:115], v[180:183]\n"
"v_mfma_f32_16x16x32_bf16 v[184:187], a[72:75], a[120:123], v[184:187]\n"
"v_mfma_f32_16x16x32_bf16 v[184:187], a[76:79], a[124:127], v[184:187]\n"
"v_mfma_f32_16x16x32_bf16 v[156:159], a[68:71], a[124:127], v[156:159]\n"
"v_mfma_f32_16x16x32_bf16 v[156:159], a[64:67], a[120:123], v[156:159]\n"
"v_mfma_f32_16x16x32_bf16 v[160:163], a[64:67], a[128:131], v[160:163]\n"
"v_mfma_f32_16x16x32_bf16 v[160:163], a[68:71], a[132:135], v[160:163]\n"
"global_load_dwordx4 a[56:59], v0, s[34:35] offset:2048 sc1\n"
"v_mfma_f32_16x16x32_bf16 v[188:191], a[76:79], a[132:135], v[188:191]\n"
"v_mfma_f32_16x16x32_bf16 v[188:191], a[72:75], a[128:131], v[188:191]\n"
"v_mfma_f32_16x16x32_bf16 v[192:195], a[72:75], a[136:139], v[192:195]\n"
"v_mfma_f32_16x16x32_bf16 v[192:195], a[76:79], a[140:143], v[192:195]\n"
"v_mfma_f32_16x16x32_bf16 v[164:167], a[68:71], a[140:143], v[164:167]\n"
"v_mfma_f32_16x16x32_bf16 v[164:167], a[64:67], a[136:139], v[164:167]\n"
"v_mfma_f32_16x16x32_bf16 v[168:171], a[64:67], a[144:147], v[168:171]\n"
"v_mfma_f32_16x16x32_bf16 v[168:171], a[68:71], a[148:151], v[168:171]\n"
"global_load_dwordx4 a[60:63], v0, s[34:35] offset:3072 sc1\n"
"v_mfma_f32_16x16x32_bf16 v[196:199], a[76:79], a[148:151], v[196:199]\n"
"v_mfma_f32_16x16x32_bf16 v[196:199], a[72:75], a[144:147], v[196:199]\n"
"v_mfma_f32_16x16x32_bf16 a[20:23], a[72:75], a[152:155], a[20:23]\n"
"s_add_u32 s34, s34, 8192\n"
"v_mfma_f32_16x16x32_bf16 a[20:23], a[76:79], a[156:159], a[20:23]\n"
"v_mfma_f32_16x16x32_bf16 a[16:19], a[68:71], a[156:159], a[16:19]\n"
"v_mfma_f32_16x16x32_bf16 a[16:19], a[64:67], a[152:155], a[16:19]\n"
"s_waitcnt vmcnt(8)\n"
"v_mfma_f32_16x16x32_bf16 v[200:203], a[80:83], a[96:99], v[200:203]\n"
"v_mfma_f32_16x16x32_bf16 v[200:203], a[84:87], a[100:103], v[200:203]\n"
"global_load_dwordx4 a[64:67], v0, s[34:35] offset:-4096 sc1\n"
"v_mfma_f32_16x16x32_bf16 v[228:231], a[92:95], a[100:103], v[228:231]\n"
"ds_read_b128 a[100:103], v2 offset:17408\n"
"v_mfma_f32_16x16x32_bf16 v[228:231], a[88:91], a[96:99], v[228:231]\n"
"ds_read_b128 a[96:99], v2 offset:16384\n"
"v_mfma_f32_16x16x32_bf16 v[232:235], a[88:91], a[104:107], v[232:235]\n"
"v_mfma_f32_16x16x32_bf16 v[232:235], a[92:95], a[108:111], v[232:235]\n"
"v_mfma_f32_16x16x32_bf16 v[204:207], a[84:87], a[108:111], v[204:207]\n"
"ds_read_b128 a[108:111], v2 offset:19456\n"
"v_mfma_f32_16x16x32_bf16 v[204:207], a[80:83], a[104:107], v[204:207]\n"
"ds_read_b128 a[104:107], v2 offset:18432\n"
"v_mfma_f32_16x16x32_bf16 v[208:211], a[80:83], a[112:115], v[208:211]\n"
"v_mfma_f32_16x16x32_bf16 v[208:211], a[84:87], a[116:119], v[208:211]\n"
"global_load_dwordx4 a[68:71], v0, s[34:35] offset:-3072 sc1\n"
"v_mfma_f32_16x16x32_bf16 v[236:239], a[92:95], a[116:119], v[236:239]\n"
"ds_read_b128 a[116:119], v2 offset:21504\n"
"v_mfma_f32_16x16x32_bf16 v[236:239], a[88:91], a[112:115], v[236:239]\n"
"ds_read_b128 a[112:115], v2 offset:20480\n"
"v_mfma_f32_16x16x32_bf16 v[240:243], a[88:91], a[120:123], v[240:243]\n"
"v_mfma_f32_16x16x32_bf16 v[240:243], a[92:95], a[124:127], v[240:243]\n"
"v_mfma_f32_16x16x32_bf16 v[212:215], a[84:87], a[124:127], v[212:215]\n"
"ds_read_b128 a[124:127], v2 offset:23552\n"
"v_mfma_f32_16x16x32_bf16 v[212:215], a[80:83], a[120:123], v[212:215]\n"
"ds_read_b128 a[120:123], v2 offset:22528\n"
"v_mfma_f32_16x16x32_bf16 v[216:219], a[80:83], a[128:131], v[216:219]\n"
"v_mfma_f32_16x16x32_bf16 v[216:219], a[84:87], a[132:135], v[216:219]\n"
"global_load_dwordx4 a[72:75], v0, s[34:35] offset:-2048 sc1\n"
"v_mfma_f32_16x16x32_bf16 v[244:247], a[92:95], a[132:135], v[244:247]\n"
"ds_read_b128 a[132:135], v2 offset:25600\n"
"v_mfma_f32_16x16x32_bf16 v[244:247], a[88:91], a[128:131], v[244:247]\n"
"ds_read_b128 a[128:131], v2 offset:24576\n"
"v_mfma_f32_16x16x32_bf16 v[248:251], a[88:91], a[136:139], v[248:251]\n"
"v_mfma_f32_16x16x32_bf16 v[248:251], a[92:95], a[140:143], v[248:251]\n"
"v_mfma_f32_16x16x32_bf16 v[220:223], a[84:87], a[140:143], v[220:223]\n"
"ds_read_b128 a[140:143], v2 offset:27648\n"
"v_mfma_f32_16x16x32_bf16 v[220:223], a[80:83], a[136:139], v[220:223]\n"
"ds_read_b128 a[136:139], v2 offset:26624\n"
"v_mfma_f32_16x16x32_bf16 v[224:227], a[80:83], a[144:147], v[224:227]\n"
"v_mfma_f32_16x16x32_bf16 v[224:227], a[84:87], a[148:151], v[224:227]\n"
"global_load_dwordx4 a[76:79], v0, s[34:35] offset:-1024 sc1\n"
"v_mfma_f32_16x16x32_bf16 v[252:255], a[92:95], a[148:151], v[252:255]\n"
"ds_read_b128 a[148:151], v2 offset:29696\n"
"v_mfma_f32_16x16x32_bf16 v[252:255], a[88:91], a[144:147], v[252:255]\n"
"ds_read_b128 a[144:147], v2 offset:28672\n"
"v_mfma_f32_16x16x32_bf16 a[28:31], a[88:91], a[152:155], a[28:31]\n"
"v_mfma_f32_16x16x32_bf16 a[28:31], a[92:95], a[156:159], a[28:31]\n"
"v_mfma_f32_16x16x32_bf16 a[24:27], a[84:87], a[156:159], a[24:27]\n"
"ds_read_b128 a[156:159], v2 offset:31744\n"
"v_mfma_f32_16x16x32_bf16 a[24:27], a[80:83], a[152:155], a[24:27]\n"
"ds_read_b128 a[152:155], v2 offset:30720\n"
"s_branch L_P5\n"
"L_last:\n"
"s_waitcnt vmcnt(9) lgkmcnt(12)\n"
"v_mfma_f32_16x16x32_bf16 v[32:35], a[32:35], a[96:99], v[32:35]\n"
"s_add_u32 s34, s36, 1454080\n"
"s_add_u32 s48, s48, 131072\n"
"s_addc_u32 s49, s49, 0\n"
"v_mfma_f32_16x16x32_bf16 v[32:35], a[36:39], a[100:103], v[32:35]\n"
"global_load_dwordx4 a[80:83], v0, s[34:35] offset:0 sc1\n"
"v_mfma_f32_16x16x32_bf16 v[60:63], a[44:47], a[100:103], v[60:63]\n"
"s_barrier\n"
"v_mfma_f32_16x16x32_bf16 v[60:63], a[40:43], a[96:99], v[60:63]\n"
"v_mfma_f32_16x16x32_bf16 v[64:67], a[40:43], a[104:107], v[64:67]\n"
"v_mfma_f32_16x16x32_bf16 v[64:67], a[44:47], a[108:111], v[64:67]\n"
"v_mfma_f32_16x16x32_bf16 v[36:39], a[36:39], a[108:111], v[36:39]\n"
"v_subrev_u32 v8, s65, v8\n"
"v_mfma_f32_16x16x32_bf16 v[36:39], a[32:35], a[104:107], v[36:39]\n"
"s_waitcnt lgkmcnt(8)\n"
"v_xor_b32 v9, s79, v9\n"
"v_mfma_f32_16x16x32_bf16 v[40:43], a[32:35], a[112:115], v[40:43]\n"
"v_or_b32 v8, v8, v9\n"
"v_mfma_f32_16x16x32_bf16 v[40:43], a[36:39], a[116:119], v[40:43]\n"
"global_load_dwordx4 a[84:87], v0, s[34:35] offset:1024 sc1\n"
"v_cmp_lt_u32 vcc, 15, v8\n"
"v_mfma_f32_16x16x32_bf16 v[68:71], a[44:47], a[116:119], v[68:71]\n"
"s_cbranch_vccnz L_slow4\n"
"L_back4:\n"
"v_mfma_f32_16x16x32_bf16 v[68:71], a[40:43], a[112:115], v[68:71]\n"
"v_mfma_f32_16x16x32_bf16 v[72:75], a[40:43], a[120:123], v[72:75]\n"
"v_mfma_f32_16x16x32_bf16 v[72:75], a[44:47], a[124:127], v[72:75]\n"
"v_mfma_f32_16x16x32_bf16 v[44:47], a[36:39], a[124:127], v[44:47]\n"
"v_mfma_f32_16x16x32_bf16 v[44:47], a[32:35], a[120:123], v[44:47]\n"
"s_add_u32 m0, s64, 16384\n"
"s_waitcnt lgkmcnt(4)\n"
"v_mfma_f32_16x16x32_bf16 v[48:51], a[32:35], a[128:131], v[48:51]\n"
"v_mfma_f32_16x16x32_bf16 v[48:51], a[36:39], a[132:135], v[48:51]\n"
"global_load_dwordx4 a[88:91], v0, s[34:35] offset:2048 sc1\n"
"v_mfma_f32_16x16x32_bf16 v[76:79], a[44:47], a[132:135], v[76:79]\n"
"v_mfma_f32_16x16x32_bf16 v[76:79], a[40:43], a[128:131], v[76:79]\n"
"v_mfma_f32_16x16x32_bf16 v[80:83], a[40:43], a[136:139], v[80:83]\n"
"s_mov_b32 s97, s37\n"
"v_mfma_f32_16x16x32_bf16 v[80:83], a[44:47], a[140:143], v[80:83]\n"
"v_mfma_f32_16x16x32_bf16 v[52:55], a[36:39], a[140:143], v[52:55]\n"
"v_mfma_f32_16x16x32_bf16 v[52:55], a[32:35], a[136:139], v[52:55]\n"
"s_waitcnt lgkmcnt(0)\n"
"v_mfma_f32_16x16x32_bf16 v[56:59], a[32:35], a[144:147], v[56:59]\n"
"buffer_load_dwordx4 v0, s[48:51], s75 offen offset:0 sc1 lds\n"
"v_mfma_f32_16x16x32_bf16 v[56:59], a[36:39], a[148:151], v[56:59]\n"
"global_load_dwordx4 a[92:95], v0, s[34:35] offset:3072 sc1\n"
"v_mfma_f32_16x16x32_bf16 v[84:87], a[44:47], a[148:151], v[84:87]\n"
"v_mfma_f32_16x16x32_bf16 v[84:87], a[40:43], a[144:147], v[84:87]\n"
"v_mfma_f32_16x16x32_bf16 a[4:7], a[40:43], a[152:155], a[4:7]\n"
"v_mfma_f32_16x16x32_bf16 a[4:7], a[44:47], a[156:159], a[4:7]\n"
"v_mfma_f32_16x16x32_bf16 a[0:3], a[36:39], a[156:159], a[0:3]\n"
"v_mfma_f32_16x16x32_bf16 a[0:3], a[32:35], a[152:155], a[0:3]\n"
"s_waitcnt vmcnt(10)\n"
"v_mfma_f32_16x16x32_bf16 v[88:91], a[48:51], a[96:99], v[88:91]\n"
"s_add_u32 s34, s36, 1458176\n"
"v_mfma_f32_16x16x32_bf16 v[88:91], a[52:55], a[100:103], v[88:91]\n"
"global_load_dwordx4 a[32:35], v0, s[34:35] offset:0 sc1\n"
"v_mfma_f32_16x16x32_bf16 v[116:119], a[60:63], a[100:103], v[116:119]\n"
"buffer_load_dwordx4 v0, s[48:51], s75 offen offset:1024 sc1 lds\n"
"v_mfma_f32_16x16x32_bf16 v[116:119], a[56:59], a[96:99], v[116:119]\n"
"v_mfma_f32_16x16x32_bf16 v[120:123], a[56:59], a[104:107], v[120:123]\n"
"v_mfma_f32_16x16x32_bf16 v[120:123], a[60:63], a[108:111], v[120:123]\n"
"v_mfma_f32_16x16x32_bf16 v[92:95], a[52:55], a[108:111], v[92:95]\n"
"v_mfma_f32_16x16x32_bf16 v[92:95], a[48:51], a[104:107], v[92:95]\n"
"v_mfma_f32_16x16x32_bf16 v[96:99], a[48:51], a[112:115], v[96:99]\n"
"v_mfma_f32_16x16x32_bf16 v[96:99], a[52:55], a[116:119], v[96:99]\n"
"global_load_dwordx4 a[36:39], v0, s[34:35] offset:1024 sc1\n"
"v_mfma_f32_16x16x32_bf16 v[124:127], a[60:63], a[116:119], v[124:127]\n"
"v_mfma_f32_16x16x32_bf16 v[124:127], a[56:59], a[112:115], v[124:127]\n"
"v_mfma_f32_16x16x32_bf16 v[128:131], a[56:59], a[120:123], v[128:131]\n"
"buffer_load_dwordx4 v0, s[48:51], s75 offen offset:2048 sc1 lds\n"
"v_mfma_f32_16x16x32_bf16 v[128:131], a[60:63], a[124:127], v[128:131]\n"
"v_mfma_f32_16x16x32_bf16 v[100:103], a[52:55], a[124:127], v[100:103]\n"
"v_mfma_f32_16x16x32_bf16 v[100:103], a[48:51], a[120:123], v[100:103]\n"
"v_mfma_f32_16x16x32_bf16 v[104:107], a[48:51], a[128:131], v[104:107]\n"
"v_mfma_f32_16x16x32_bf16 v[104:107], a[52:55], a[132:135], v[104:107]\n"
"global_load_dwordx4 a[40:43], v0, s[34:35] offset:2048 sc1\n"
"v_mfma_f32_16x16x32_bf16 v[132:135], a[60:63], a[132:135], v[132:135]\n"
"v_mfma_f32_16x16x32_bf16 v[132:135], a[56:59], a[128:131], v[132:135]\n"
"v_mfma_f32_16x16x32_bf16 v[136:139], a[56:59], a[136:139], v[136:139]\n"
"v_mfma_f32_16x16x32_bf16 v[136:139], a[60:63], a[140:143], v[136:139]\n"
"buffer_load_dwordx4 v0, s[48:51], s75 offen offset:3072 sc1 lds\n"
"v_mfma_f32_16x16x32_bf16 v[108:111], a[52:55], a[140:143], v[108:111]\n"
"v_mfma_f32_16x16x32_bf16 v[108:111], a[48:51], a[136:139], v[108:111]\n"
"v_mfma_f32_16x16x32_bf16 v[112:115], a[48:51], a[144:147], v[112:115]\n"
"v_mfma_f32_16x16x32_bf16 v[112:115], a[52:55], a[148:151], v[112:115]\n"
"global_load_dwordx4 a[44:47], v0, s[34:35] offset:3072 sc1\n"
"v_mfma_f32_16x16x32_bf16 v[140:143], a[60:63], a[148:151], v[140:143]\n"
"v_mfma_f32_16x16x32_bf16 v[140:143], a[56:59], a[144:147], v[140:143]\n"
"v_mfma_f32_16x16x32_bf16 a[12:15], a[56:59], a[152:155], a[12:15]\n"
"v_mfma_f32_16x16x32_bf16 a[12:15], a[60:63], a[156:159], a[12:15]\n"
"v_mfma_f32_16x16x32_bf16 a[8:11], a[52:55], a[156:159], a[8:11]\n"
"v_mfma_f32_16x16x32_bf16 a[8:11], a[48:51], a[152:155], a[8:11]\n"
"s_add_u32 m0, s64, 20480\n"
"s_waitcnt vmcnt(12)\n"
"v_mfma_f32_16x16x32_bf16 v[144:147], a[64:67], a[96:99], v[144:147]\n"
"s_add_u32 s34, s36, 1462272\n"
"v_mfma_f32_16x16x32_bf16 v[144:147], a[68:71], a[100:103], v[144:147]\n"
"global_load_dwordx4 a[48:51], v0, s[34:35] offset:0 sc1\n"
"v_mfma_f32_16x16x32_bf16 v[172:175], a[76:79], a[100:103], v[172:175]\n"
"v_mfma_f32_16x16x32_bf16 v[172:175], a[72:75], a[96:99], v[172:175]\n"
"v_mfma_f32_16x16x32_bf16 v[176:179], a[72:75], a[104:107], v[176:179]\n"
"v_mfma_f32_16x16x32_bf16 v[176:179], a[76:79], a[108:111], v[176:179]\n"
"v_mfma_f32_16x16x32_bf16 v[148:151], a[68:71], a[108:111], v[148:151]\n"
"v_lshlrev_b32 v16, 16, v18\n"
"v_and_b32 v17, s63, v18\n"
"v_mfma_f32_16x16x32_bf16 v[148:151], a[64:67], a[104:107], v[148:151]\n"
"v_lshlrev_b32 v18, 16, v19\n"
"v_and_b32 v19, s63, v19\n"
"v_mfma_f32_16x16x32_bf16 v[152:155], a[64:67], a[112:115], v[152:155]\n"
"buffer_load_dwordx4 v1, s[48:51], s75 offen offset:0 sc1 lds\n"
"v_mfma_f32_16x16x32_bf16 v[152:155], a[68:71], a[116:119], v[152:155]\n"
"global_load_dwordx4 a[52:55], v0, s[34:35] offset:1024 sc1\n"
"v_add_f32 v88, v88, v16\n"
"v_mfma_f32_16x16x32_bf16 v[180:183], a[76:79], a[116:119], v[180:183]\n"
"v_add_f32 v89, v89, v17\n"
"v_add_f32 v90, v90, v18\n"
"v_mfma_f32_16x16x32_bf16 v[180:183], a[72:75], a[112:115], v[180:183]\n"
"v_add_f32 v91, v91, v19\n"
"v_add_f32 v92, v92, v16\n"
"v_mfma_f32_16x16x32_bf16 v[184:187], a[72:75], a[120:123], v[184:187]\n"
"v_add_f32 v93, v93, v17\n"
"v_add_f32 v94, v94, v18\n"
"v_mfma_f32_16x16x32_bf16 v[184:187], a[76:79], a[124:127], v[184:187]\n"
"v_add_f32 v95, v95, v19\n"
"v_add_f32 v96, v96, v16\n"
"v_mfma_f32_16x16x32_bf16 v[156:159], a[68:71], a[124:127], v[156:159]\n"
"v_add_f32 v97, v97, v17\n"
"v_add_f32 v98, v98, v18\n"
"v_mfma_f32_16x16x32_bf16 v[156:159], a[64:67], a[120:123], v[156:159]\n"
"v_add_f32 v99, v99, v19\n"
"v_add_f32 v100, v100, v16\n"
"v_mfma_f32_16x16x32_bf16 v[160:163], a[64:67], a[128:131], v[160:163]\n"
"v_add_f32 v101, v101, v17\n"
"v_add_f32 v102, v102, v18\n"
"v_mfma_f32_16x16x32_bf16 v[160:163], a[68:71], a[132:135], v[160:163]\n"
"global_load_dwordx4 a[56:59], v0, s[34:35] offset:2048 sc1\n"
"v_add_f32 v103, v103, v19\n"
"v_mfma_f32_16x16x32_bf16 v[188:191], a[76:79], a[132:135], v[188:191]\n"
"buffer_load_dwordx4 v1, s[48:51], s75 offen offset:1024 sc1 lds\n"
"v_mfma_f32_16x16x32_bf16 v[188:191], a[72:75], a[128:131], v[188:191]\n"
"v_add_f32 v104, v104, v16\n"
"v_add_f32 v105, v105, v17\n"
"v_mfma_f32_16x16x32_bf16 v[192:195], a[72:75], a[136:139], v[192:195]\n"
"v_add_f32 v106, v106, v18\n"
"v_add_f32 v107, v107, v19\n"
"v_mfma_f32_16x16x32_bf16 v[192:195], a[76:79], a[140:143], v[192:195]\n"
"v_add_f32 v108, v108, v16\n"
"v_add_f32 v109, v109, v17\n"
"v_mfma_f32_16x16x32_bf16 v[164:167], a[68:71], a[140:143], v[164:167]\n"
"v_add_f32 v110, v110, v18\n"
"v_add_f32 v111, v111, v19\n"
"v_mfma_f32_16x16x32_bf16 v[164:167], a[64:67], a[136:139], v[164:167]\n"
"v_add_f32 v112, v112, v16\n"
"v_add_f32 v113, v113, v17\n"
"v_mfma_f32_16x16x32_bf16 v[168:171], a[64:67], a[144:147], v[168:171]\n"
"v_add_f32 v114, v114, v18\n"
"v_add_f32 v115, v115, v19\n"
"v_mfma_f32_16x16x32_bf16 v[168:171], a[68:71], a[148:151], v[168:171]\n"
"global_load_dwordx4 a[60:63], v0, s[34:35] offset:3072 sc1\n"
"v_accvgpr_read_b32 v12, a8\n"
"v_mfma_f32_16x16x32_bf16 v[196:199], a[76:79], a[148:151], v[196:199]\n"
"v_accvgpr_read_b32 v13, a9\n"
"v_accvgpr_read_b32 v14, a10\n"
"v_mfma_f32_16x16x32_bf16 v[196:199], a[72:75], a[144:147], v[196:199]\n"
"buffer_load_dwordx4 v1, s[48:51], s75 offen offset:2048 sc1 lds\n"
"v_mfma_f32_16x16x32_bf16 a[20:23], a[72:75], a[152:155], a[20:23]\n"
"v_accvgpr_read_b32 v15, a11\n"
"v_add_f32 v12, v12, v16\n"
"v_mfma_f32_16x16x32_bf16 a[20:23], a[76:79], a[156:159], a[20:23]\n"
"v_add_f32 v13, v13, v17\n"
"v_add_f32 v14, v14, v18\n"
"v_mfma_f32_16x16x32_bf16 a[16:19], a[68:71], a[156:159], a[16:19]\n"
"v_add_f32 v15, v15, v19\n"
"v_accvgpr_write_b32 a8, v12\n"
"v_mfma_f32_16x16x32_bf16 a[16:19], a[64:67], a[152:155], a[16:19]\n"
"s_waitcnt vmcnt(14)\n"
"v_accvgpr_write_b32 a9, v13\n"
"v_accvgpr_write_b32 a10, v14\n"
"v_accvgpr_write_b32 a11, v15\n"
"v_mfma_f32_16x16x32_bf16 v[200:203], a[80:83], a[96:99], v[200:203]\n"
"s_add_u32 s34, s36, 1474560\n"
"v_lshlrev_b32 v20, 16, v22\n"
"v_and_b32 v21, s63, v22\n"
"v_mfma_f32_16x16x32_bf16 v[200:203], a[84:87], a[100:103], v[200:203]\n"
"global_load_dwordx4 a[64:67], v0, s[34:35] offset:0 sc1\n"
"v_lshlrev_b32 v22, 16, v23\n"
"v_mfma_f32_16x16x32_bf16 v[228:231], a[92:95], a[100:103], v[228:231]\n"
"ds_read_b128 a[100:103], v3 offset:1024\n"
"v_and_b32 v23, s63, v23\n"
"v_mfma_f32_16x16x32_bf16 v[228:231], a[88:91], a[96:99], v[228:231]\n"
"ds_read_b128 a[96:99], v3 offset:0\n"
"v_add_f32 v116, v116, v20\n"
"v_mfma_f32_16x16x32_bf16 v[232:235], a[88:91], a[104:107], v[232:235]\n"
"v_add_f32 v117, v117, v21\n"
"v_add_f32 v118, v118, v22\n"
"v_mfma_f32_16x16x32_bf16 v[232:235], a[92:95], a[108:111], v[232:235]\n"
"buffer_load_dwordx4 v1, s[48:51], s75 offen offset:3072 sc1 lds\n"
"v_mfma_f32_16x16x32_bf16 v[204:207], a[84:87], a[108:111], v[204:207]\n"
"ds_read_b128 a[108:111], v3 offset:3072\n"
"v_add_f32 v119, v119, v23\n"
"v_add_f32 v120, v120, v20\n"
"v_mfma_f32_16x16x32_bf16 v[204:207], a[80:83], a[104:107], v[204:207]\n"
"ds_read_b128 a[104:107], v3 offset:2048\n"
"v_add_f32 v121, v121, v21\n"
"v_mfma_f32_16x16x32_bf16 v[208:211], a[80:83], a[112:115], v[208:211]\n"
"v_add_f32 v122, v122, v22\n"
"v_add_f32 v123, v123, v23\n"
"v_mfma_f32_16x16x32_bf16 v[208:211], a[84:87], a[116:119], v[208:211]\n"
"global_load_dwordx4 a[68:71], v0, s[34:35] offset:1024 sc1\n"
"v_add_f32 v124, v124, v20\n"
"v_add_f32 v125, v125, v21\n"
"v_mfma_f32_16x16x32_bf16 v[236:239], a[92:95], a[116:119], v[236:239]\n"
"ds_read_b128 a[116:119], v3 offset:5120\n"
"v_add_f32 v126, v126, v22\n"
"v_mfma_f32_16x16x32_bf16 v[236:239], a[88:91], a[112:115], v[236:239]\n"
"ds_read_b128 a[112:115], v3 offset:4096\n"
"v_add_f32 v127, v127, v23\n"
"v_mfma_f32_16x16x32_bf16 v[240:243], a[88:91], a[120:123], v[240:243]\n"
"v_add_f32 v128, v128, v20\n"
"v_add_f32 v129, v129, v21\n"
"v_mfma_f32_16x16x32_bf16 v[240:243], a[92:95], a[124:127], v[240:243]\n"
"v_add_f32 v130, v130, v22\n"
"v_add_f32 v131, v131, v23\n"
"v_mfma_f32_16x16x32_bf16 v[212:215], a[84:87], a[124:127], v[212:215]\n"
"s_add_u32 m0, s64, 24576\n"
"ds_read_b128 a[124:127], v3 offset:7168\n"
"v_add_f32 v132, v132, v20\n"
"v_add_f32 v133, v133, v21\n"
"v_mfma_f32_16x16x32_bf16 v[212:215], a[80:83], a[120:123], v[212:215]\n"
"ds_read_b128 a[120:123], v3 offset:6144\n"
"v_add_f32 v134, v134, v22\n"
"v_mfma_f32_16x16x32_bf16 v[216:219], a[80:83], a[128:131], v[216:219]\n"
"v_add_f32 v135, v135, v23\n"
"v_add_f32 v136, v136, v20\n"
"v_mfma_f32_16x16x32_bf16 v[216:219], a[84:87], a[132:135], v[216:219]\n"
"global_load_dwordx4 a[72:75], v0, s[34:35] offset:2048 sc1\n"
"v_add_f32 v137, v137, v21\n"
"v_mfma_f32_16x16x32_bf16 v[244:247], a[92:95], a[132:135], v[244:247]\n"
"ds_read_b128 a[132:135], v3 offset:9216\n"
"v_add_f32 v138, v138, v22\n"
"v_add_f32 v139, v139, v23\n"
"v_mfma_f32_16x16x32_bf16 v[244:247], a[88:91], a[128:131], v[244:247]\n"
"ds_read_b128 a[128:131], v3 offset:8192\n"
"v_add_f32 v140, v140, v20\n"
"v_mfma_f32_16x16x32_bf16 v[248:251], a[88:91], a[136:139], v[248:251]\n"
"v_add_f32 v141, v141, v21\n"
"v_add_f32 v142, v142, v22\n"
"v_mfma_f32_16x16x32_bf16 v[248:251], a[92:95], a[140:143], v[248:251]\n"
"v_add_f32 v143, v143, v23\n"
"v_accvgpr_read_b32 v12, a12\n"
"v_mfma_f32_16x16x32_bf16 v[220:223], a[84:87], a[140:143], v[220:223]\n"
"ds_read_b128 a[140:143], v3 offset:11264\n"
"v_accvgpr_read_b32 v13, a13\n"
"v_mfma_f32_16x16x32_bf16 v[220:223], a[80:83], a[136:139], v[220:223]\n"
"ds_read_b128 a[136:139], v3 offset:10240\n"
"v_accvgpr_read_b32 v14, a14\n"
"v_accvgpr_read_b32 v15, a15\n"
"v_mfma_f32_16x16x32_bf16 v[224:227], a[80:83], a[144:147], v[224:227]\n"
"buffer_load_dwordx4 v0, s[48:51], s69 offen offset:0 sc1 lds\n"
"v_add_f32 v12, v12, v20\n"
"v_mfma_f32_16x16x32_bf16 v[224:227], a[84:87], a[148:151], v[224:227]\n"
"global_load_dwordx4 a[76:79], v0, s[34:35] offset:3072 sc1\n"
"v_add_f32 v13, v13, v21\n"
"v_add_f32 v14, v14, v22\n"
"v_mfma_f32_16x16x32_bf16 v[252:255], a[92:95], a[148:151], v[252:255]\n"
"ds_read_b128 a[148:151], v3 offset:13312\n"
"v_add_f32 v15, v15, v23\n"
"v_accvgpr_write_b32 a12, v12\n"
"v_mfma_f32_16x16x32_bf16 v[252:255], a[88:91], a[144:147], v[252:255]\n"
"ds_read_b128 a[144:147], v3 offset:12288\n"
"v_accvgpr_write_b32 a13, v13\n"
"v_mfma_f32_16x16x32_bf16 a[28:31], a[88:91], a[152:155], a[28:31]\n"
"v_accvgpr_write_b32 a14, v14\n"
"v_accvgpr_write_b32 a15, v15\n"
"v_mfma_f32_16x16x32_bf16 a[28:31], a[92:95], a[156:159], a[28:31]\n"
"v_mfma_f32_16x16x32_bf16 a[24:27], a[84:87], a[156:159], a[24:27]\n"
"ds_read_b128 a[156:159], v3 offset:15360\n"
"v_mfma_f32_16x16x32_bf16 a[24:27], a[80:83], a[152:155], a[24:27]\n"
"s_waitcnt vmcnt(13) lgkmcnt(11)\n"
"ds_read_b128 a[152:155], v3 offset:14336\n"
"v_mfma_f32_16x16x32_bf16 v[32:35], a[32:35], a[96:99], v[32:35]\n"
"s_add_u32 s34, s36, 1478656\n"
"v_mfma_f32_16x16x32_bf16 v[32:35], a[36:39], a[100:103], v[32:35]\n"
"global_load_dwordx4 a[80:83], v0, s[34:35] offset:0 sc1\n"
"v_mfma_f32_16x16x32_bf16 v[60:63], a[44:47], a[100:103], v[60:63]\n"
"buffer_load_dwordx4 v0, s[48:51], s69 offen offset:1024 sc1 lds\n"
"v_mfma_f32_16x16x32_bf16 v[60:63], a[40:43], a[96:99], v[60:63]\n"
"v_lshrrev_b32 v16, 4, v0\n"
"v_mfma_f32_16x16x32_bf16 v[64:67], a[40:43], a[104:107], v[64:67]\n"
"v_mfma_f32_16x16x32_bf16 v[64:67], a[44:47], a[108:111], v[64:67]\n"
"v_mfma_f32_16x16x32_bf16 v[36:39], a[36:39], a[108:111], v[36:39]\n"
"v_mfma_f32_16x16x32_bf16 v[36:39], a[32:35], a[104:107], v[36:39]\n"
"s_waitcnt lgkmcnt(8)\n"
"v_and_b32 v16, 0x30, v16\n"
"v_mfma_f32_16x16x32_bf16 v[40:43], a[32:35], a[112:115], v[40:43]\n"
"v_mfma_f32_16x16x32_bf16 v[40:43], a[36:39], a[116:119], v[40:43]\n"
"global_load_dwordx4 a[84:87], v0, s[34:35] offset:1024 sc1\n"
"v_mfma_f32_16x16x32_bf16 v[68:71], a[44:47], a[116:119], v[68:71]\n"
"buffer_load_dwordx2 v[18:19], v16, s[44:47], s61 offen offset:128\n"
"v_mfma_f32_16x16x32_bf16 v[68:71], a[40:43], a[112:115], v[68:71]\n"
"buffer_load_dwordx4 v0, s[48:51], s69 offen offset:2048 sc1 lds\n"
"v_mfma_f32_16x16x32_bf16 v[72:75], a[40:43], a[120:123], v[72:75]\n"
"v_mfma_f32_16x16x32_bf16 v[72:75], a[44:47], a[124:127], v[72:75]\n"
"v_lshrrev_b32 v20, 4, v0\n"
"v_mfma_f32_16x16x32_bf16 v[44:47], a[36:39], a[124:127], v[44:47]\n"
"v_mfma_f32_16x16x32_bf16 v[44:47], a[32:35], a[120:123], v[44:47]\n"
"s_waitcnt lgkmcnt(4)\n"
"v_mfma_f32_16x16x32_bf16 v[48:51], a[32:35], a[128:131], v[48:51]\n"
"v_and_b32 v20, 0x30, v20\n"
"v_mfma_f32_16x16x32_bf16 v[48:51], a[36:39], a[132:135], v[48:51]\n"
"global_load_dwordx4 a[88:91], v0, s[34:35] offset:2048 sc1\n"
"v_mfma_f32_16x16x32_bf16 v[76:79], a[44:47], a[132:135], v[76:79]\n"
"v_mfma_f32_16x16x32_bf16 v[76:79], a[40:43], a[128:131], v[76:79]\n"
"v_mfma_f32_16x16x32_bf16 v[80:83], a[40:43], a[136:139], v[80:83]\n"
"buffer_load_dwordx4 v0, s[48:51], s69 offen offset:3072 sc1 lds\n"
"v_mfma_f32_16x16x32_bf16 v[80:83], a[44:47], a[140:143], v[80:83]\n"
"buffer_load_dwordx2 v[22:23], v20, s[44:47], s61 offen offset:136\n"
"v_mfma_f32_16x16x32_bf16 v[52:55], a[36:39], a[140:143], v[52:55]\n"
"v_mfma_f32_16x16x32_bf16 v[52:55], a[32:35], a[136:139], v[52:55]\n"
"s_waitcnt lgkmcnt(0)\n"
"v_mfma_f32_16x16x32_bf16 v[56:59], a[32:35], a[144:147], v[56:59]\n"
"v_lshrrev_b32 v24, 4, v0\n"
"v_mfma_f32_16x16x32_bf16 v[56:59], a[36:39], a[148:151], v[56:59]\n"
"global_load_dwordx4 a[92:95], v0, s[34:35] offset:3072 sc1\n"
"v_mfma_f32_16x16x32_bf16 v[84:87], a[44:47], a[148:151], v[84:87]\n"
"v_mfma_f32_16x16x32_bf16 v[84:87], a[40:43], a[144:147], v[84:87]\n"
"v_and_b32 v24, 0x30, v24\n"
"v_mfma_f32_16x16x32_bf16 a[4:7], a[40:43], a[152:155], a[4:7]\n"
"v_mfma_f32_16x16x32_bf16 a[4:7], a[44:47], a[156:159], a[4:7]\n"
"v_mfma_f32_16x16x32_bf16 a[0:3], a[36:39], a[156:159], a[0:3]\n"
"s_add_u32 m0, s64, 28672\n"
"v_mfma_f32_16x16x32_bf16 a[0:3], a[32:35], a[152:155], a[0:3]\n"
"s_waitcnt vmcnt(16)\n"
"buffer_load_dwordx2 v[26:27], v24, s[44:47], s61 offen offset:192\n"
"v_mfma_f32_16x16x32_bf16 v[88:91], a[48:51], a[96:99], v[88:91]\n"
"s_add_u32 s34, s36, 1490944\n"
"v_lshrrev_b32 v28, 4, v0\n"
"v_mfma_f32_16x16x32_bf16 v[88:91], a[52:55], a[100:103], v[88:91]\n"
"global_load_dwordx4 a[32:35], v0, s[34:35] offset:0 sc1\n"
"v_mfma_f32_16x16x32_bf16 v[116:119], a[60:63], a[100:103], v[116:119]\n"
"ds_read_b128 a[100:103], v4 offset:1024\n"
"v_mfma_f32_16x16x32_bf16 v[116:119], a[56:59], a[96:99], v[116:119]\n"
"ds_read_b128 a[96:99], v4 offset:0\n"
"v_mfma_f32_16x16x32_bf16 v[120:123], a[56:59], a[104:107], v[120:123]\n"
"v_and_b32 v28, 0x30, v28\n"
"v_mfma_f32_16x16x32_bf16 v[120:123], a[60:63], a[108:111], v[120:123]\n"
"buffer_load_dwordx2 v[30:31], v28, s[44:47], s61 offen offset:200\n"
"v_mfma_f32_16x16x32_bf16 v[92:95], a[52:55], a[108:111], v[92:95]\n"
"ds_read_b128 a[108:111], v4 offset:3072\n"
"v_mfma_f32_16x16x32_bf16 v[92:95], a[48:51], a[104:107], v[92:95]\n"
"ds_read_b128 a[104:107], v4 offset:2048\n"
"v_mfma_f32_16x16x32_bf16 v[96:99], a[48:51], a[112:115], v[96:99]\n"
"buffer_load_dwordx4 v1, s[48:51], s69 offen offset:0 sc1 lds\n"
"v_mfma_f32_16x16x32_bf16 v[96:99], a[52:55], a[116:119], v[96:99]\n"
"global_load_dwordx4 a[36:39], v0, s[34:35] offset:1024 sc1\n"
"v_mfma_f32_16x16x32_bf16 v[124:127], a[60:63], a[116:119], v[124:127]\n"
"ds_read_b128 a[116:119], v4 offset:5120\n"
"v_mfma_f32_16x16x32_bf16 v[124:127], a[56:59], a[112:115], v[124:127]\n"
"ds_read_b128 a[112:115], v4 offset:4096\n"
"v_mfma_f32_16x16x32_bf16 v[128:131], a[56:59], a[120:123], v[128:131]\n"
"v_mfma_f32_16x16x32_bf16 v[128:131], a[60:63], a[124:127], v[128:131]\n"
"v_mfma_f32_16x16x32_bf16 v[100:103], a[52:55], a[124:127], v[100:103]\n"
"ds_read_b128 a[124:127], v4 offset:7168\n"
"v_mfma_f32_16x16x32_bf16 v[100:103], a[48:51], a[120:123], v[100:103]\n"
"ds_read_b128 a[120:123], v4 offset:6144\n"
"v_mfma_f32_16x16x32_bf16 v[104:107], a[48:51], a[128:131], v[104:107]\n"
"buffer_load_dwordx4 v1, s[48:51], s69 offen offset:1024 sc1 lds\n"
"v_mfma_f32_16x16x32_bf16 v[104:107], a[52:55], a[132:135], v[104:107]\n"
"global_load_dwordx4 a[40:43], v0, s[34:35] offset:2048 sc1\n"
"v_mfma_f32_16x16x32_bf16 v[132:135], a[60:63], a[132:135], v[132:135]\n"
"ds_read_b128 a[132:135], v4 offset:9216\n"
"v_mfma_f32_16x16x32_bf16 v[132:135], a[56:59], a[128:131], v[132:135]\n"
"ds_read_b128 a[128:131], v4 offset:8192\n"
"v_mfma_f32_16x16x32_bf16 v[136:139], a[56:59], a[136:139], v[136:139]\n"
"v_mfma_f32_16x16x32_bf16 v[136:139], a[60:63], a[140:143], v[136:139]\n"
"v_mfma_f32_16x16x32_bf16 v[108:111], a[52:55], a[140:143], v[108:111]\n"
"ds_read_b128 a[140:143], v4 offset:11264\n"
"v_mfma_f32_16x16x32_bf16 v[108:111], a[48:51], a[136:139], v[108:111]\n"
"ds_read_b128 a[136:139], v4 offset:10240\n"
"v_mfma_f32_16x16x32_bf16 v[112:115], a[48:51], a[144:147], v[112:115]\n"
"v_mfma_f32_16x16x32_bf16 v[112:115], a[52:55], a[148:151], v[112:115]\n"
"global_load_dwordx4 a[44:47], v0, s[34:35] offset:3072 sc1\n"
"v_mfma_f32_16x16x32_bf16 v[140:143], a[60:63], a[148:151], v[140:143]\n"
"ds_read_b128 a[148:151], v4 offset:13312\n"
"v_mfma_f32_16x16x32_bf16 v[140:143], a[56:59], a[144:147], v[140:143]\n"
"ds_read_b128 a[144:147], v4 offset:12288\n"
"v_mfma_f32_16x16x32_bf16 a[12:15], a[56:59], a[152:155], a[12:15]\n"
"buffer_load_dwordx4 v1, s[48:51], s69 offen offset:2048 sc1 lds\n"
"v_mfma_f32_16x16x32_bf16 a[12:15], a[60:63], a[156:159], a[12:15]\n"
"v_mfma_f32_16x16x32_bf16 a[8:11], a[52:55], a[156:159], a[8:11]\n"
"ds_read_b128 a[156:159], v4 offset:15360\n"
"v_mfma_f32_16x16x32_bf16 a[8:11], a[48:51], a[152:155], a[8:11]\n"
"s_waitcnt vmcnt(18) lgkmcnt(11)\n"
"ds_read_b128 a[152:155], v4 offset:14336\n"
"v_mfma_f32_16x16x32_bf16 v[32:35], a[64:67], a[96:99], v[32:35]\n"
"s_add_u32 s34, s36, 1495040\n"
"v_mfma_f32_16x16x32_bf16 v[32:35], a[68:71], a[100:103], v[32:35]\n"
"global_load_dwordx4 a[48:51], v0, s[34:35] offset:0 sc1\n"
"v_mfma_f32_16x16x32_bf16 v[60:63], a[76:79], a[100:103], v[60:63]\n"
"v_mfma_f32_16x16x32_bf16 v[60:63], a[72:75], a[96:99], v[60:63]\n"
"v_mfma_f32_16x16x32_bf16 v[64:67], a[72:75], a[104:107], v[64:67]\n"
"buffer_load_dwordx4 v1, s[48:51], s69 offen offset:3072 sc1 lds\n"
"v_mfma_f32_16x16x32_bf16 v[64:67], a[76:79], a[108:111], v[64:67]\n"
"v_mfma_f32_16x16x32_bf16 v[36:39], a[68:71], a[108:111], v[36:39]\n"
"v_mfma_f32_16x16x32_bf16 v[36:39], a[64:67], a[104:107], v[36:39]\n"
"s_waitcnt lgkmcnt(8)\n"
"v_mfma_f32_16x16x32_bf16 v[40:43], a[64:67], a[112:115], v[40:43]\n"
"v_mfma_f32_16x16x32_bf16 v[40:43], a[68:71], a[116:119], v[40:43]\n"
"global_load_dwordx4 a[52:55], v0, s[34:35] offset:1024 sc1\n"
"v_mfma_f32_16x16x32_bf16 v[68:71], a[76:79], a[116:119], v[68:71]\n"
"v_mfma_f32_16x16x32_bf16 v[68:71], a[72:75], a[112:115], v[68:71]\n"
"v_mfma_f32_16x16x32_bf16 v[72:75], a[72:75], a[120:123], v[72:75]\n"
"v_mfma_f32_16x16x32_bf16 v[72:75], a[76:79], a[124:127], v[72:75]\n"
"v_mfma_f32_16x16x32_bf16 v[44:47], a[68:71], a[124:127], v[44:47]\n"
"v_mfma_f32_16x16x32_bf16 v[44:47], a[64:67], a[120:123], v[44:47]\n"
"s_waitcnt lgkmcnt(4)\n"
"v_mfma_f32_16x16x32_bf16 v[48:51], a[64:67], a[128:131], v[48:51]\n"
"v_mfma_f32_16x16x32_bf16 v[48:51], a[68:71], a[132:135], v[48:51]\n"
"global_load_dwordx4 a[56:59], v0, s[34:35] offset:2048 sc1\n"
"v_mfma_f32_16x16x32_bf16 v[76:79], a[76:79], a[132:135], v[76:79]\n"
"v_mfma_f32_16x16x32_bf16 v[76:79], a[72:75], a[128:131], v[76:79]\n"
"v_mfma_f32_16x16x32_bf16 v[80:83], a[72:75], a[136:139], v[80:83]\n"
"v_mfma_f32_16x16x32_bf16 v[80:83], a[76:79], a[140:143], v[80:83]\n"
"v_mfma_f32_16x16x32_bf16 v[52:55], a[68:71], a[140:143], v[52:55]\n"
"v_mfma_f32_16x16x32_bf16 v[52:55], a[64:67], a[136:139], v[52:55]\n"
"s_waitcnt lgkmcnt(0)\n"
"v_mfma_f32_16x16x32_bf16 v[56:59], a[64:67], a[144:147], v[56:59]\n"
"v_mfma_f32_16x16x32_bf16 v[56:59], a[68:71], a[148:151], v[56:59]\n"
"global_load_dwordx4 a[60:63], v0, s[34:35] offset:3072 sc1\n"
"v_mfma_f32_16x16x32_bf16 v[84:87], a[76:79], a[148:151], v[84:87]\n"
"v_mfma_f32_16x16x32_bf16 v[84:87], a[72:75], a[144:147], v[84:87]\n"
"v_mfma_f32_16x16x32_bf16 a[4:7], a[72:75], a[152:155], a[4:7]\n"
"v_mfma_f32_16x16x32_bf16 a[4:7], a[76:79], a[156:159], a[4:7]\n"
"v_mfma_f32_16x16x32_bf16 a[0:3], a[68:71], a[156:159], a[0:3]\n"
"v_mfma_f32_16x16x32_bf16 a[0:3], a[64:67], a[152:155], a[0:3]\n"
"s_waitcnt vmcnt(14)\n"
"v_mfma_f32_16x16x32_bf16 v[88:91], a[80:83], a[96:99], v[88:91]\n"
"s_add_u32 s34, s36, 1507328\n"
"v_mfma_f32_16x16x32_bf16 v[88:91], a[84:87], a[100:103], v[88:91]\n"
"global_load_dwordx4 a[64:67], v0, s[34:35] offset:0 sc1\n"
"v_mfma_f32_16x16x32_bf16 v[116:119], a[92:95], a[100:103], v[116:119]\n"
"ds_read_b128 a[100:103], v5 offset:1024\n"
"v_mfma_f32_16x16x32_bf16 v[116:119], a[88:91], a[96:99], v[116:119]\n"
"ds_read_b128 a[96:99], v5 offset:0\n"
"v_mfma_f32_16x16x32_bf16 v[120:123], a[88:91], a[104:107], v[120:123]\n"
"v_mfma_f32_16x16x32_bf16 v[120:123], a[92:95], a[108:111], v[120:123]\n"
"v_mfma_f32_16x16x32_bf16 v[92:95], a[84:87], a[108:111], v[92:95]\n"
"ds_read_b128 a[108:111], v5 offset:3072\n"
"v_mfma_f32_16x16x32_bf16 v[92:95], a[80:83], a[104:107], v[92:95]\n"
"ds_read_b128 a[104:107], v5 offset:2048\n"
"v_mfma_f32_16x16x32_bf16 v[96:99], a[80:83], a[112:115], v[96:99]\n"
"v_mfma_f32_16x16x32_bf16 v[96:99], a[84:87], a[116:119], v[96:99]\n"
"global_load_dwordx4 a[68:71], v0, s[34:35] offset:1024 sc1\n"
"v_mfma_f32_16x16x32_bf16 v[124:127], a[92:95], a[116:119], v[124:127]\n"
"ds_read_b128 a[116:119], v5 offset:5120\n"
"v_mfma_f32_16x16x32_bf16 v[124:127], a[88:91], a[112:115], v[124:127]\n"
"ds_read_b128 a[112:115], v5 offset:4096\n"
"v_mfma_f32_16x16x32_bf16 v[128:131], a[88:91], a[120:123], v[128:131]\n"
"v_mfma_f32_16x16x32_bf16 v[128:131], a[92:95], a[124:127], v[128:131]\n"
"v_mfma_f32_16x16x32_bf16 v[100:103], a[84:87], a[124:127], v[100:103]\n"
"ds_read_b128 a[124:127], v5 offset:7168\n"
"v_mfma_f32_16x16x32_bf16 v[100:103], a[80:83], a[120:123], v[100:103]\n"
"ds_read_b128 a[120:123], v5 offset:6144\n"
"v_mfma_f32_16x16x32_bf16 v[104:107], a[80:83], a[128:131], v[104:107]\n"
"v_mfma_f32_16x16x32_bf16 v[104:107], a[84:87], a[132:135], v[104:107]\n"
"global_load_dwordx4 a[72:75], v0, s[34:35] offset:2048 sc1\n"
"v_mfma_f32_16x16x32_bf16 v[132:135], a[92:95], a[132:135], v[132:135]\n"
"ds_read_b128 a[132:135], v5 offset:9216\n"
"v_mfma_f32_16x16x32_bf16 v[132:135], a[88:91], a[128:131], v[132:135]\n"
"ds_read_b128 a[128:131], v5 offset:8192\n"
"v_mfma_f32_16x16x32_bf16 v[136:139], a[88:91], a[136:139], v[136:139]\n"
"v_mfma_f32_16x16x32_bf16 v[136:139], a[92:95], a[140:143], v[136:139]\n"
"v_mfma_f32_16x16x32_bf16 v[108:111], a[84:87], a[140:143], v[108:111]\n"
"ds_read_b128 a[140:143], v5 offset:11264\n"
"v_mfma_f32_16x16x32_bf16 v[108:111], a[80:83], a[136:139], v[108:111]\n"
"ds_read_b128 a[136:139], v5 offset:10240\n"
"v_mfma_f32_16x16x32_bf16 v[112:115], a[80:83], a[144:147], v[112:115]\n"
"v_mfma_f32_16x16x32_bf16 v[112:115], a[84:87], a[148:151], v[112:115]\n"
"global_load_dwordx4 a[76:79], v0, s[34:35] offset:3072 sc1\n"
"v_mfma_f32_16x16x32_bf16 v[140:143], a[92:95], a[148:151], v[140:143]\n"
"ds_read_b128 a[148:151], v5 offset:13312\n"
"v_mfma_f32_16x16x32_bf16 v[140:143], a[88:91], a[144:147], v[140:143]\n"
"ds_read_b128 a[144:147], v5 offset:12288\n"
"v_mfma_f32_16x16x32_bf16 a[12:15], a[88:91], a[152:155], a[12:15]\n"
"v_mfma_f32_16x16x32_bf16 a[12:15], a[92:95], a[156:159], a[12:15]\n"
"v_mfma_f32_16x16x32_bf16 a[8:11], a[84:87], a[156:159], a[8:11]\n"
"ds_read_b128 a[156:159], v5 offset:15360\n"
"v_mfma_f32_16x16x32_bf16 a[8:11], a[80:83], a[152:155], a[8:11]\n"
"s_waitcnt vmcnt(10) lgkmcnt(11)\n"
"ds_read_b128 a[152:155], v5 offset:14336\n"
"v_mfma_f32_16x16x32_bf16 v[32:35], a[32:35], a[96:99], v[32:35]\n"
"s_add_u32 s34, s36, 1511424\n"
"v_mfma_f32_16x16x32_bf16 v[32:35], a[36:39], a[100:103], v[32:35]\n"
"global_load_dwordx4 a[80:83], v0, s[34:35] offset:0 sc1\n"
"v_mfma_f32_16x16x32_bf16 v[60:63], a[44:47], a[100:103], v[60:63]\n"
"v_mfma_f32_16x16x32_bf16 v[60:63], a[40:43], a[96:99], v[60:63]\n"
"v_mfma_f32_16x16x32_bf16 v[64:67], a[40:43], a[104:107], v[64:67]\n"
"v_mfma_f32_16x16x32_bf16 v[64:67], a[44:47], a[108:111], v[64:67]\n"
"v_mfma_f32_16x16x32_bf16 v[36:39], a[36:39], a[108:111], v[36:39]\n"
"v_mfma_f32_16x16x32_bf16 v[36:39], a[32:35], a[104:107], v[36:39]\n"
"s_waitcnt lgkmcnt(8)\n"
"v_mfma_f32_16x16x32_bf16 v[40:43], a[32:35], a[112:115], v[40:43]\n"
"v_mfma_f32_16x16x32_bf16 v[40:43], a[36:39], a[116:119], v[40:43]\n"
"global_load_dwordx4 a[84:87], v0, s[34:35] offset:1024 sc1\n"
"v_mfma_f32_16x16x32_bf16 v[68:71], a[44:47], a[116:119], v[68:71]\n"
"v_mfma_f32_16x16x32_bf16 v[68:71], a[40:43], a[112:115], v[68:71]\n"
"v_mfma_f32_16x16x32_bf16 v[72:75], a[40:43], a[120:123], v[72:75]\n"
"v_mfma_f32_16x16x32_bf16 v[72:75], a[44:47], a[124:127], v[72:75]\n"
"v_mfma_f32_16x16x32_bf16 v[44:47], a[36:39], a[124:127], v[44:47]\n"
"v_mfma_f32_16x16x32_bf16 v[44:47], a[32:35], a[120:123], v[44:47]\n"
"s_waitcnt lgkmcnt(4)\n"
"v_mfma_f32_16x16x32_bf16 v[48:51], a[32:35], a[128:131], v[48:51]\n"
"v_mfma_f32_16x16x32_bf16 v[48:51], a[36:39], a[132:135], v[48:51]\n"
"global_load_dwordx4 a[88:91], v0, s[34:35] offset:2048 sc1\n"
"v_mfma_f32_16x16x32_bf16 v[76:79], a[44:47], a[132:135], v[76:79]\n"
"v_mfma_f32_16x16x32_bf16 v[76:79], a[40:43], a[128:131], v[76:79]\n"
"v_mfma_f32_16x16x32_bf16 v[80:83], a[40:43], a[136:139], v[80:83]\n"
"v_mfma_f32_16x16x32_bf16 v[80:83], a[44:47], a[140:143], v[80:83]\n"
"v_mfma_f32_16x16x32_bf16 v[52:55], a[36:39], a[140:143], v[52:55]\n"
"v_mfma_f32_16x16x32_bf16 v[52:55], a[32:35], a[136:139], v[52:55]\n"
"s_waitcnt lgkmcnt(0)\n"
"v_mfma_f32_16x16x32_bf16 v[56:59], a[32:35], a[144:147], v[56:59]\n"
"v_mfma_f32_16x16x32_bf16 v[56:59], a[36:39], a[148:151], v[56:59]\n"
"global_load_dwordx4 a[92:95], v0, s[34:35] offset:3072 sc1\n"
"v_mfma_f32_16x16x32_bf16 v[84:87], a[44:47], a[148:151], v[84:87]\n"
"v_mfma_f32_16x16x32_bf16 v[84:87], a[40:43], a[144:147], v[84:87]\n"
"v_mfma_f32_16x16x32_bf16 a[4:7], a[40:43], a[152:155], a[4:7]\n"
"v_mfma_f32_16x16x32_bf16 a[4:7], a[44:47], a[156:159], a[4:7]\n"
"v_mfma_f32_16x16x32_bf16 a[0:3], a[36:39], a[156:159], a[0:3]\n"
"v_mfma_f32_16x16x32_bf16 a[0:3], a[32:35], a[152:155], a[0:3]\n"
"s_waitcnt vmcnt(8)\n"
"v_mfma_f32_16x16x32_bf16 v[88:91], a[48:51], a[96:99], v[88:91]\n"
"s_add_u32 s34, s36, 1515520\n"
"v_mfma_f32_16x16x32_bf16 v[88:91], a[52:55], a[100:103], v[88:91]\n"
"global_load_dwordx4 a[32:35], v0, s[34:35] offset:0 sc1\n"
"v_mfma_f32_16x16x32_bf16 v[116:119], a[60:63], a[100:103], v[116:119]\n"
"ds_read_b128 a[100:103], v2 offset:17408\n"
"v_mfma_f32_16x16x32_bf16 v[116:119], a[56:59], a[96:99], v[116:119]\n"
"ds_read_b128 a[96:99], v2 offset:16384\n"
"v_mfma_f32_16x16x32_bf16 v[120:123], a[56:59], a[104:107], v[120:123]\n"
"v_mfma_f32_16x16x32_bf16 v[120:123], a[60:63], a[108:111], v[120:123]\n"
"v_mfma_f32_16x16x32_bf16 v[92:95], a[52:55], a[108:111], v[92:95]\n"
"ds_read_b128 a[108:111], v2 offset:19456\n"
"v_mfma_f32_16x16x32_bf16 v[92:95], a[48:51], a[104:107], v[92:95]\n"
"ds_read_b128 a[104:107], v2 offset:18432\n"
"v_mfma_f32_16x16x32_bf16 v[96:99], a[48:51], a[112:115], v[96:99]\n"
"v_mfma_f32_16x16x32_bf16 v[96:99], a[52:55], a[116:119], v[96:99]\n"
"global_load_dwordx4 a[36:39], v0, s[34:35] offset:1024 sc1\n"
"v_mfma_f32_16x16x32_bf16 v[124:127], a[60:63], a[116:119], v[124:127]\n"
"ds_read_b128 a[116:119], v2 offset:21504\n"
"v_mfma_f32_16x16x32_bf16 v[124:127], a[56:59], a[112:115], v[124:127]\n"
"ds_read_b128 a[112:115], v2 offset:20480\n"
"v_mfma_f32_16x16x32_bf16 v[128:131], a[56:59], a[120:123], v[128:131]\n"
"v_mfma_f32_16x16x32_bf16 v[128:131], a[60:63], a[124:127], v[128:131]\n"
"v_mfma_f32_16x16x32_bf16 v[100:103], a[52:55], a[124:127], v[100:103]\n"
"ds_read_b128 a[124:127], v2 offset:23552\n"
"v_mfma_f32_16x16x32_bf16 v[100:103], a[48:51], a[120:123], v[100:103]\n"
"ds_read_b128 a[120:123], v2 offset:22528\n"
"v_mfma_f32_16x16x32_bf16 v[104:107], a[48:51], a[128:131], v[104:107]\n"
"v_mfma_f32_16x16x32_bf16 v[104:107], a[52:55], a[132:135], v[104:107]\n"
"global_load_dwordx4 a[40:43], v0, s[34:35] offset:2048 sc1\n"
"v_mfma_f32_16x16x32_bf16 v[132:135], a[60:63], a[132:135], v[132:135]\n"
"ds_read_b128 a[132:135], v2 offset:25600\n"
"v_mfma_f32_16x16x32_bf16 v[132:135], a[56:59], a[128:131], v[132:135]\n"
"ds_read_b128 a[128:131], v2 offset:24576\n"
"v_mfma_f32_16x16x32_bf16 v[136:139], a[56:59], a[136:139], v[136:139]\n"
"v_mfma_f32_16x16x32_bf16 v[136:139], a[60:63], a[140:143], v[136:139]\n"
"v_mfma_f32_16x16x32_bf16 v[108:111], a[52:55], a[140:143], v[108:111]\n"
"ds_read_b128 a[140:143], v2 offset:27648\n"
"v_mfma_f32_16x16x32_bf16 v[108:111], a[48:51], a[136:139], v[108:111]\n"
"ds_read_b128 a[136:139], v2 offset:26624\n"
"v_mfma_f32_16x16x32_bf16 v[112:115], a[48:51], a[144:147], v[112:115]\n"
"v_mfma_f32_16x16x32_bf16 v[112:115], a[52:55], a[148:151], v[112:115]\n"
"global_load_dwordx4 a[44:47], v0, s[34:35] offset:3072 sc1\n"
"v_mfma_f32_16x16x32_bf16 v[140:143], a[60:63], a[148:151], v[140:143]\n"
"ds_read_b128 a[148:151], v2 offset:29696\n"
"v_mfma_f32_16x16x32_bf16 v[140:143], a[56:59], a[144:147], v[140:143]\n"
"ds_read_b128 a[144:147], v2 offset:28672\n"
"v_mfma_f32_16x16x32_bf16 a[12:15], a[56:59], a[152:155], a[12:15]\n"
"v_mfma_f32_16x16x32_bf16 a[12:15], a[60:63], a[156:159], a[12:15]\n"
"v_mfma_f32_16x16x32_bf16 a[8:11], a[52:55], a[156:159], a[8:11]\n"
"ds_read_b128 a[156:159], v2 offset:31744\n"
"v_mfma_f32_16x16x32_bf16 a[8:11], a[48:51], a[152:155], a[8:11]\n"
"s_waitcnt vmcnt(8) lgkmcnt(11)\n"
"ds_read_b128 a[152:155], v2 offset:30720\n"
"v_mfma_f32_16x16x32_bf16 v[32:35], a[64:67], a[96:99], v[32:35]\n"
"s_add_u32 s34, s36, 1519616\n"
"v_mfma_f32_16x16x32_bf16 v[32:35], a[68:71], a[100:103], v[32:35]\n"
"global_load_dwordx4 a[48:51], v0, s[34:35] offset:0 sc1\n"
"v_mfma_f32_16x16x32_bf16 v[60:63], a[76:79], a[100:103], v[60:63]\n"
"s_barrier\n"
"v_mfma_f32_16x16x32_bf16 v[60:63], a[72:75], a[96:99], v[60:63]\n"
"v_mfma_f32_16x16x32_bf16 v[64:67], a[72:75], a[104:107], v[64:67]\n"
"v_mfma_f32_16x16x32_bf16 v[64:67], a[76:79], a[108:111], v[64:67]\n"
"v_mfma_f32_16x16x32_bf16 v[36:39], a[68:71], a[108:111], v[36:39]\n"
"v_mfma_f32_16x16x32_bf16 v[36:39], a[64:67], a[104:107], v[36:39]\n"
"s_waitcnt lgkmcnt(8)\n"
"v_mfma_f32_16x16x32_bf16 v[40:43], a[64:67], a[112:115], v[40:43]\n"
"v_mfma_f32_16x16x32_bf16 v[40:43], a[68:71], a[116:119], v[40:43]\n"
"global_load_dwordx4 a[52:55], v0, s[34:35] offset:1024 sc1\n"
"v_mfma_f32_16x16x32_bf16 v[68:71], a[76:79], a[116:119], v[68:71]\n"
"v_mfma_f32_16x16x32_bf16 v[68:71], a[72:75], a[112:115], v[68:71]\n"
"v_mfma_f32_16x16x32_bf16 v[72:75], a[72:75], a[120:123], v[72:75]\n"
"v_mfma_f32_16x16x32_bf16 v[72:75], a[76:79], a[124:127], v[72:75]\n"
"v_mfma_f32_16x16x32_bf16 v[44:47], a[68:71], a[124:127], v[44:47]\n"
"v_mfma_f32_16x16x32_bf16 v[44:47], a[64:67], a[120:123], v[44:47]\n"
"s_waitcnt lgkmcnt(4)\n"
"v_mfma_f32_16x16x32_bf16 v[48:51], a[64:67], a[128:131], v[48:51]\n"
"v_mfma_f32_16x16x32_bf16 v[48:51], a[68:71], a[132:135], v[48:51]\n"
"global_load_dwordx4 a[56:59], v0, s[34:35] offset:2048 sc1\n"
"v_mfma_f32_16x16x32_bf16 v[76:79], a[76:79], a[132:135], v[76:79]\n"
"v_mfma_f32_16x16x32_bf16 v[76:79], a[72:75], a[128:131], v[76:79]\n"
"v_mfma_f32_16x16x32_bf16 v[80:83], a[72:75], a[136:139], v[80:83]\n"
"v_lshlrev_b32 v16, 16, v18\n"
"v_mfma_f32_16x16x32_bf16 v[80:83], a[76:79], a[140:143], v[80:83]\n"
"v_and_b32 v17, s63, v18\n"
"v_mfma_f32_16x16x32_bf16 v[52:55], a[68:71], a[140:143], v[52:55]\n"
"v_lshlrev_b32 v18, 16, v19\n"
"v_mfma_f32_16x16x32_bf16 v[52:55], a[64:67], a[136:139], v[52:55]\n"
"s_waitcnt lgkmcnt(0)\n"
"v_and_b32 v19, s63, v19\n"
"v_add_f32 v144, v144, v16\n"
"v_mfma_f32_16x16x32_bf16 v[56:59], a[64:67], a[144:147], v[56:59]\n"
"v_add_f32 v145, v145, v17\n"
"v_mfma_f32_16x16x32_bf16 v[56:59], a[68:71], a[148:151], v[56:59]\n"
"global_load_dwordx4 a[60:63], v0, s[34:35] offset:3072 sc1\n"
"v_add_f32 v146, v146, v18\n"
"v_mfma_f32_16x16x32_bf16 v[84:87], a[76:79], a[148:151], v[84:87]\n"
"v_add_f32 v147, v147, v19\n"
"v_mfma_f32_16x16x32_bf16 v[84:87], a[72:75], a[144:147], v[84:87]\n"
"v_add_f32 v148, v148, v16\n"
"v_mfma_f32_16x16x32_bf16 a[4:7], a[72:75], a[152:155], a[4:7]\n"
"v_add_f32 v149, v149, v17\n"
"v_mfma_f32_16x16x32_bf16 a[4:7], a[76:79], a[156:159], a[4:7]\n"
"v_add_f32 v150, v150, v18\n"
"v_mfma_f32_16x16x32_bf16 a[0:3], a[68:71], a[156:159], a[0:3]\n"
"v_add_f32 v151, v151, v19\n"
"v_mfma_f32_16x16x32_bf16 a[0:3], a[64:67], a[152:155], a[0:3]\n"
"s_waitcnt vmcnt(8)\n"
"v_add_f32 v152, v152, v16\n"
"v_add_f32 v153, v153, v17\n"
"v_mfma_f32_16x16x32_bf16 v[88:91], a[80:83], a[96:99], v[88:91]\n"
"s_add_u32 s34, s36, 1523712\n"
"v_add_f32 v154, v154, v18\n"
"v_mfma_f32_16x16x32_bf16 v[88:91], a[84:87], a[100:103], v[88:91]\n"
"global_load_dwordx4 a[64:67], v0, s[34:35] offset:0 sc1\n"
"v_add_f32 v155, v155, v19\n"
"v_mfma_f32_16x16x32_bf16 v[116:119], a[92:95], a[100:103], v[116:119]\n"
"ds_read_b128 a[100:103], v3 offset:17408\n"
"v_add_f32 v156, v156, v16\n"
"v_mfma_f32_16x16x32_bf16 v[116:119], a[88:91], a[96:99], v[116:119]\n"
"ds_read_b128 a[96:99], v3 offset:16384\n"
"v_add_f32 v157, v157, v17\n"
"v_mfma_f32_16x16x32_bf16 v[120:123], a[88:91], a[104:107], v[120:123]\n"
"v_add_f32 v158, v158, v18\n"
"v_mfma_f32_16x16x32_bf16 v[120:123], a[92:95], a[108:111], v[120:123]\n"
"v_add_f32 v159, v159, v19\n"
"v_add_f32 v160, v160, v16\n"
"v_mfma_f32_16x16x32_bf16 v[92:95], a[84:87], a[108:111], v[92:95]\n"
"ds_read_b128 a[108:111], v3 offset:19456\n"
"v_add_f32 v161, v161, v17\n"
"v_mfma_f32_16x16x32_bf16 v[92:95], a[80:83], a[104:107], v[92:95]\n"
"ds_read_b128 a[104:107], v3 offset:18432\n"
"v_add_f32 v162, v162, v18\n"
"v_mfma_f32_16x16x32_bf16 v[96:99], a[80:83], a[112:115], v[96:99]\n"
"v_add_f32 v163, v163, v19\n"
"v_mfma_f32_16x16x32_bf16 v[96:99], a[84:87], a[116:119], v[96:99]\n"
"global_load_dwordx4 a[68:71], v0, s[34:35] offset:1024 sc1\n"
"v_add_f32 v164, v164, v16\n"
"v_mfma_f32_16x16x32_bf16 v[124:127], a[92:95], a[116:119], v[124:127]\n"
"ds_read_b128 a[116:119], v3 offset:21504\n"
"v_add_f32 v165, v165, v17\n"
"v_mfma_f32_16x16x32_bf16 v[124:127], a[88:91], a[112:115], v[124:127]\n"
"ds_read_b128 a[112:115], v3 offset:20480\n"
"v_add_f32 v166, v166, v18\n"
"v_mfma_f32_16x16x32_bf16 v[128:131], a[88:91], a[120:123], v[128:131]\n"
"v_add_f32 v167, v167, v19\n"
"v_mfma_f32_16x16x32_bf16 v[128:131], a[92:95], a[124:127], v[128:131]\n"
"v_add_f32 v168, v168, v16\n"
"v_add_f32 v169, v169, v17\n"
"v_mfma_f32_16x16x32_bf16 v[100:103], a[84:87], a[124:127], v[100:103]\n"
"ds_read_b128 a[124:127], v3 offset:23552\n"
"v_add_f32 v170, v170, v18\n"
"v_mfma_f32_16x16x32_bf16 v[100:103], a[80:83], a[120:123], v[100:103]\n"
"ds_read_b128 a[120:123], v3 offset:22528\n"
"v_add_f32 v171, v171, v19\n"
"v_mfma_f32_16x16x32_bf16 v[104:107], a[80:83], a[128:131], v[104:107]\n"
"v_accvgpr_read_b32 v12, a16\n"
"v_mfma_f32_16x16x32_bf16 v[104:107], a[84:87], a[132:135], v[104:107]\n"
"global_load_dwordx4 a[72:75], v0, s[34:35] offset:2048 sc1\n"
"v_accvgpr_read_b32 v13, a17\n"
"v_mfma_f32_16x16x32_bf16 v[132:135], a[92:95], a[132:135], v[132:135]\n"
"ds_read_b128 a[132:135], v3 offset:25600\n"
"v_accvgpr_read_b32 v14, a18\n"
"v_mfma_f32_16x16x32_bf16 v[132:135], a[88:91], a[128:131], v[132:135]\n"
"ds_read_b128 a[128:131], v3 offset:24576\n"
"v_accvgpr_read_b32 v15, a19\n"
"v_mfma_f32_16x16x32_bf16 v[136:139], a[88:91], a[136:139], v[136:139]\n"
"v_add_f32 v12, v12, v16\n"
"v_mfma_f32_16x16x32_bf16 v[136:139], a[92:95], a[140:143], v[136:139]\n"
"v_add_f32 v13, v13, v17\n"
"v_add_f32 v14, v14, v18\n"
"v_mfma_f32_16x16x32_bf16 v[108:111], a[84:87], a[140:143], v[108:111]\n"
"ds_read_b128 a[140:143], v3 offset:27648\n"
"v_add_f32 v15, v15, v19\n"
"v_mfma_f32_16x16x32_bf16 v[108:111], a[80:83], a[136:139], v[108:111]\n"
"ds_read_b128 a[136:139], v3 offset:26624\n"
"v_accvgpr_write_b32 a16, v12\n"
"v_mfma_f32_16x16x32_bf16 v[112:115], a[80:83], a[144:147], v[112:115]\n"
"v_accvgpr_write_b32 a17, v13\n"
"v_mfma_f32_16x16x32_bf16 v[112:115], a[84:87], a[148:151], v[112:115]\n"
"global_load_dwordx4 a[76:79], v0, s[34:35] offset:3072 sc1\n"
"v_accvgpr_write_b32 a18, v14\n"
"v_mfma_f32_16x16x32_bf16 v[140:143], a[92:95], a[148:151], v[140:143]\n"
"ds_read_b128 a[148:151], v3 offset:29696\n"
"v_accvgpr_write_b32 a19, v15\n"
"v_mfma_f32_16x16x32_bf16 v[140:143], a[88:91], a[144:147], v[140:143]\n"
"ds_read_b128 a[144:147], v3 offset:28672\n"
"v_lshlrev_b32 v20, 16, v22\n"
"v_mfma_f32_16x16x32_bf16 a[12:15], a[88:91], a[152:155], a[12:15]\n"
"v_and_b32 v21, s63, v22\n"
"v_lshlrev_b32 v22, 16, v23\n"
"v_mfma_f32_16x16x32_bf16 a[12:15], a[92:95], a[156:159], a[12:15]\n"
"v_and_b32 v23, s63, v23\n"
"v_mfma_f32_16x16x32_bf16 a[8:11], a[84:87], a[156:159], a[8:11]\n"
"ds_read_b128 a[156:159], v3 offset:31744\n"
"v_add_f32 v172, v172, v20\n"
"v_mfma_f32_16x16x32_bf16 a[8:11], a[80:83], a[152:155], a[8:11]\n"
"s_waitcnt vmcnt(8) lgkmcnt(11)\n"
"ds_read_b128 a[152:155], v3 offset:30720\n"
"v_add_f32 v173, v173, v21\n"
"v_mfma_f32_16x16x32_bf16 v[32:35], a[32:35], a[96:99], v[32:35]\n"
"s_add_u32 s34, s36, 1527808\n"
"v_add_f32 v174, v174, v22\n"
"v_mfma_f32_16x16x32_bf16 v[32:35], a[36:39], a[100:103], v[32:35]\n"
"global_load_dwordx4 a[80:83], v0, s[34:35] offset:0 sc1\n"
"v_add_f32 v175, v175, v23\n"
"v_mfma_f32_16x16x32_bf16 v[60:63], a[44:47], a[100:103], v[60:63]\n"
"v_add_f32 v176, v176, v20\n"
"v_mfma_f32_16x16x32_bf16 v[60:63], a[40:43], a[96:99], v[60:63]\n"
"v_add_f32 v177, v177, v21\n"
"v_add_f32 v178, v178, v22\n"
"v_mfma_f32_16x16x32_bf16 v[64:67], a[40:43], a[104:107], v[64:67]\n"
"v_add_f32 v179, v179, v23\n"
"v_mfma_f32_16x16x32_bf16 v[64:67], a[44:47], a[108:111], v[64:67]\n"
"v_add_f32 v180, v180, v20\n"
"v_mfma_f32_16x16x32_bf16 v[36:39], a[36:39], a[108:111], v[36:39]\n"
"v_add_f32 v181, v181, v21\n"
"v_mfma_f32_16x16x32_bf16 v[36:39], a[32:35], a[104:107], v[36:39]\n"
"s_waitcnt lgkmcnt(8)\n"
"v_add_f32 v182, v182, v22\n"
"v_mfma_f32_16x16x32_bf16 v[40:43], a[32:35], a[112:115], v[40:43]\n"
"v_add_f32 v183, v183, v23\n"
"v_mfma_f32_16x16x32_bf16 v[40:43], a[36:39], a[116:119], v[40:43]\n"
"global_load_dwordx4 a[84:87], v0, s[34:35] offset:1024 sc1\n"
"v_add_f32 v184, v184, v20\n"
"v_mfma_f32_16x16x32_bf16 v[68:71], a[44:47], a[116:119], v[68:71]\n"
"v_add_f32 v185, v185, v21\n"
"v_mfma_f32_16x16x32_bf16 v[68:71], a[40:43], a[112:115], v[68:71]\n"
"v_add_f32 v186, v186, v22\n"
"v_add_f32 v187, v187, v23\n"
"v_mfma_f32_16x16x32_bf16 v[72:75], a[40:43], a[120:123], v[72:75]\n"
"v_add_f32 v188, v188, v20\n"
"v_mfma_f32_16x16x32_bf16 v[72:75], a[44:47], a[124:127], v[72:75]\n"
"v_add_f32 v189, v189, v21\n"
"v_mfma_f32_16x16x32_bf16 v[44:47], a[36:39], a[124:127], v[44:47]\n"
"v_add_f32 v190, v190, v22\n"
"v_mfma_f32_16x16x32_bf16 v[44:47], a[32:35], a[120:123], v[44:47]\n"
"s_waitcnt lgkmcnt(4)\n"
"v_add_f32 v191, v191, v23\n"
"v_mfma_f32_16x16x32_bf16 v[48:51], a[32:35], a[128:131], v[48:51]\n"
"v_add_f32 v192, v192, v20\n"
"v_mfma_f32_16x16x32_bf16 v[48:51], a[36:39], a[132:135], v[48:51]\n"
"global_load_dwordx4 a[88:91], v0, s[34:35] offset:2048 sc1\n"
"v_add_f32 v193, v193, v21\n"
"v_mfma_f32_16x16x32_bf16 v[76:79], a[44:47], a[132:135], v[76:79]\n"
"v_add_f32 v194, v194, v22\n"
"v_add_f32 v195, v195, v23\n"
"v_mfma_f32_16x16x32_bf16 v[76:79], a[40:43], a[128:131], v[76:79]\n"
"v_add_f32 v196, v196, v20\n"
"v_mfma_f32_16x16x32_bf16 v[80:83], a[40:43], a[136:139], v[80:83]\n"
"v_add_f32 v197, v197, v21\n"
"v_mfma_f32_16x16x32_bf16 v[80:83], a[44:47], a[140:143], v[80:83]\n"
"v_add_f32 v198, v198, v22\n"
"v_mfma_f32_16x16x32_bf16 v[52:55], a[36:39], a[140:143], v[52:55]\n"
"v_add_f32 v199, v199, v23\n"
"v_mfma_f32_16x16x32_bf16 v[52:55], a[32:35], a[136:139], v[52:55]\n"
"s_waitcnt lgkmcnt(0)\n"
"v_accvgpr_read_b32 v12, a20\n"
"v_mfma_f32_16x16x32_bf16 v[56:59], a[32:35], a[144:147], v[56:59]\n"
"v_accvgpr_read_b32 v13, a21\n"
"v_mfma_f32_16x16x32_bf16 v[56:59], a[36:39], a[148:151], v[56:59]\n"
"global_load_dwordx4 a[92:95], v0, s[34:35] offset:3072 sc1\n"
"v_accvgpr_read_b32 v14, a22\n"
"v_mfma_f32_16x16x32_bf16 v[84:87], a[44:47], a[148:151], v[84:87]\n"
"v_accvgpr_read_b32 v15, a23\n"
"v_add_f32 v12, v12, v20\n"
"v_mfma_f32_16x16x32_bf16 v[84:87], a[40:43], a[144:147], v[84:87]\n"
"v_add_f32 v13, v13, v21\n"
"v_mfma_f32_16x16x32_bf16 a[4:7], a[40:43], a[152:155], a[4:7]\n"
"v_add_f32 v14, v14, v22\n"
"v_mfma_f32_16x16x32_bf16 a[4:7], a[44:47], a[156:159], a[4:7]\n"
"v_add_f32 v15, v15, v23\n"
"v_mfma_f32_16x16x32_bf16 a[0:3], a[36:39], a[156:159], a[0:3]\n"
"v_accvgpr_write_b32 a20, v12\n"
"v_mfma_f32_16x16x32_bf16 a[0:3], a[32:35], a[152:155], a[0:3]\n"
"s_waitcnt vmcnt(8)\n"
"v_accvgpr_write_b32 a21, v13\n"
"v_mfma_f32_16x16x32_bf16 v[88:91], a[48:51], a[96:99], v[88:91]\n"
"s_add_u32 s34, s36, 1531904\n"
"v_accvgpr_write_b32 a22, v14\n"
"v_accvgpr_write_b32 a23, v15\n"
"v_mfma_f32_16x16x32_bf16 v[88:91], a[52:55], a[100:103], v[88:91]\n"
"global_load_dwordx4 a[32:35], v0, s[34:35] offset:0 sc1\n"
"v_lshlrev_b32 v24, 16, v26\n"
"v_mfma_f32_16x16x32_bf16 v[116:119], a[60:63], a[100:103], v[116:119]\n"
"ds_read_b128 a[100:103], v4 offset:17408\n"
"v_and_b32 v25, s63, v26\n"
"v_mfma_f32_16x16x32_bf16 v[116:119], a[56:59], a[96:99], v[116:119]\n"
"ds_read_b128 a[96:99], v4 offset:16384\n"
"v_lshlrev_b32 v26, 16, v27\n"
"v_mfma_f32_16x16x32_bf16 v[120:123], a[56:59], a[104:107], v[120:123]\n"
"v_and_b32 v27, s63, v27\n"
"v_mfma_f32_16x16x32_bf16 v[120:123], a[60:63], a[108:111], v[120:123]\n"
"v_add_f32 v200, v200, v24\n"
"v_mfma_f32_16x16x32_bf16 v[92:95], a[52:55], a[108:111], v[92:95]\n"
"ds_read_b128 a[108:111], v4 offset:19456\n"
"v_add_f32 v201, v201, v25\n"
"v_mfma_f32_16x16x32_bf16 v[92:95], a[48:51], a[104:107], v[92:95]\n"
"ds_read_b128 a[104:107], v4 offset:18432\n"
"v_add_f32 v202, v202, v26\n"
"v_mfma_f32_16x16x32_bf16 v[96:99], a[48:51], a[112:115], v[96:99]\n"
"v_add_f32 v203, v203, v27\n"
"v_add_f32 v204, v204, v24\n"
"v_mfma_f32_16x16x32_bf16 v[96:99], a[52:55], a[116:119], v[96:99]\n"
"global_load_dwordx4 a[36:39], v0, s[34:35] offset:1024 sc1\n"
"v_add_f32 v205, v205, v25\n"
"v_mfma_f32_16x16x32_bf16 v[124:127], a[60:63], a[116:119], v[124:127]\n"
"ds_read_b128 a[116:119], v4 offset:21504\n"
"v_add_f32 v206, v206, v26\n"
"v_mfma_f32_16x16x32_bf16 v[124:127], a[56:59], a[112:115], v[124:127]\n"
"ds_read_b128 a[112:115], v4 offset:20480\n"
"v_add_f32 v207, v207, v27\n"
"v_mfma_f32_16x16x32_bf16 v[128:131], a[56:59], a[120:123], v[128:131]\n"
"v_add_f32 v208, v208, v24\n"
"v_mfma_f32_16x16x32_bf16 v[128:131], a[60:63], a[124:127], v[128:131]\n"
"v_add_f32 v209, v209, v25\n"
"v_mfma_f32_16x16x32_bf16 v[100:103], a[52:55], a[124:127], v[100:103]\n"
"ds_read_b128 a[124:127], v4 offset:23552\n"
"v_add_f32 v210, v210, v26\n"
"v_mfma_f32_16x16x32_bf16 v[100:103], a[48:51], a[120:123], v[100:103]\n"
"ds_read_b128 a[120:123], v4 offset:22528\n"
"v_add_f32 v211, v211, v27\n"
"v_mfma_f32_16x16x32_bf16 v[104:107], a[48:51], a[128:131], v[104:107]\n"
"v_add_f32 v212, v212, v24\n"
"v_add_f32 v213, v213, v25\n"
"v_mfma_f32_16x16x32_bf16 v[104:107], a[52:55], a[132:135], v[104:107]\n"
"global_load_dwordx4 a[40:43], v0, s[34:35] offset:2048 sc1\n"
"v_add_f32 v214, v214, v26\n"
"v_mfma_f32_16x16x32_bf16 v[132:135], a[60:63], a[132:135], v[132:135]\n"
"ds_read_b128 a[132:135], v4 offset:25600\n"
"v_add_f32 v215, v215, v27\n"
"v_mfma_f32_16x16x32_bf16 v[132:135], a[56:59], a[128:131], v[132:135]\n"
"ds_read_b128 a[128:131], v4 offset:24576\n"
"v_add_f32 v216, v216, v24\n"
"v_mfma_f32_16x16x32_bf16 v[136:139], a[56:59], a[136:139], v[136:139]\n"
"v_add_f32 v217, v217, v25\n"
"v_mfma_f32_16x16x32_bf16 v[136:139], a[60:63], a[140:143], v[136:139]\n"
"v_add_f32 v218, v218, v26\n"
"v_mfma_f32_16x16x32_bf16 v[108:111], a[52:55], a[140:143], v[108:111]\n"
"ds_read_b128 a[140:143], v4 offset:27648\n"
"v_add_f32 v219, v219, v27\n"
"v_mfma_f32_16x16x32_bf16 v[108:111], a[48:51], a[136:139], v[108:111]\n"
"ds_read_b128 a[136:139], v4 offset:26624\n"
"v_add_f32 v220, v220, v24\n"
"v_mfma_f32_16x16x32_bf16 v[112:115], a[48:51], a[144:147], v[112:115]\n"
"v_add_f32 v221, v221, v25\n"
"v_add_f32 v222, v222, v26\n"
"v_mfma_f32_16x16x32_bf16 v[112:115], a[52:55], a[148:151], v[112:115]\n"
"global_load_dwordx4 a[44:47], v0, s[34:35] offset:3072 sc1\n"
"v_add_f32 v223, v223, v27\n"
"v_mfma_f32_16x16x32_bf16 v[140:143], a[60:63], a[148:151], v[140:143]\n"
"ds_read_b128 a[148:151], v4 offset:29696\n"
"v_add_f32 v224, v224, v24\n"
"v_mfma_f32_16x16x32_bf16 v[140:143], a[56:59], a[144:147], v[140:143]\n"
"ds_read_b128 a[144:147], v4 offset:28672\n"
"v_add_f32 v225, v225, v25\n"
"v_mfma_f32_16x16x32_bf16 a[12:15], a[56:59], a[152:155], a[12:15]\n"
"v_add_f32 v226, v226, v26\n"
"v_mfma_f32_16x16x32_bf16 a[12:15], a[60:63], a[156:159], a[12:15]\n"
"v_add_f32 v227, v227, v27\n"
"v_mfma_f32_16x16x32_bf16 a[8:11], a[52:55], a[156:159], a[8:11]\n"
"ds_read_b128 a[156:159], v4 offset:31744\n"
"v_accvgpr_read_b32 v12, a24\n"
"v_mfma_f32_16x16x32_bf16 a[8:11], a[48:51], a[152:155], a[8:11]\n"
"s_waitcnt vmcnt(8) lgkmcnt(11)\n"
"ds_read_b128 a[152:155], v4 offset:30720\n"
"v_accvgpr_read_b32 v13, a25\n"
"v_mfma_f32_16x16x32_bf16 v[32:35], a[64:67], a[96:99], v[32:35]\n"
"s_add_u32 s34, s36, 1536000\n"
"v_accvgpr_read_b32 v14, a26\n"
"v_accvgpr_read_b32 v15, a27\n"
"v_mfma_f32_16x16x32_bf16 v[32:35], a[68:71], a[100:103], v[32:35]\n"
"global_load_dwordx4 a[48:51], v0, s[34:35] offset:0 sc1\n"
"v_add_f32 v12, v12, v24\n"
"v_mfma_f32_16x16x32_bf16 v[60:63], a[76:79], a[100:103], v[60:63]\n"
"v_add_f32 v13, v13, v25\n"
"v_mfma_f32_16x16x32_bf16 v[60:63], a[72:75], a[96:99], v[60:63]\n"
"v_add_f32 v14, v14, v26\n"
"v_mfma_f32_16x16x32_bf16 v[64:67], a[72:75], a[104:107], v[64:67]\n"
"v_add_f32 v15, v15, v27\n"
"v_mfma_f32_16x16x32_bf16 v[64:67], a[76:79], a[108:111], v[64:67]\n"
"v_accvgpr_write_b32 a24, v12\n"
"v_mfma_f32_16x16x32_bf16 v[36:39], a[68:71], a[108:111], v[36:39]\n"
"v_accvgpr_write_b32 a25, v13\n"
"v_accvgpr_write_b32 a26, v14\n"
"v_mfma_f32_16x16x32_bf16 v[36:39], a[64:67], a[104:107], v[36:39]\n"
"s_waitcnt lgkmcnt(8)\n"
"v_accvgpr_write_b32 a27, v15\n"
"v_mfma_f32_16x16x32_bf16 v[40:43], a[64:67], a[112:115], v[40:43]\n"
"v_lshlrev_b32 v28, 16, v30\n"
"v_mfma_f32_16x16x32_bf16 v[40:43], a[68:71], a[116:119], v[40:43]\n"
"global_load_dwordx4 a[52:55], v0, s[34:35] offset:1024 sc1\n"
"v_and_b32 v29, s63, v30\n"
"v_mfma_f32_16x16x32_bf16 v[68:71], a[76:79], a[116:119], v[68:71]\n"
"v_lshlrev_b32 v30, 16, v31\n"
"v_mfma_f32_16x16x32_bf16 v[68:71], a[72:75], a[112:115], v[68:71]\n"
"v_and_b32 v31, s63, v31\n"
"v_mfma_f32_16x16x32_bf16 v[72:75], a[72:75], a[120:123], v[72:75]\n"
"v_add_f32 v228, v228, v28\n"
"v_mfma_f32_16x16x32_bf16 v[72:75], a[76:79], a[124:127], v[72:75]\n"
"v_add_f32 v229, v229, v29\n"
"v_add_f32 v230, v230, v30\n"
"v_mfma_f32_16x16x32_bf16 v[44:47], a[68:71], a[124:127], v[44:47]\n"
"v_add_f32 v231, v231, v31\n"
"v_mfma_f32_16x16x32_bf16 v[44:47], a[64:67], a[120:123], v[44:47]\n"
"s_waitcnt lgkmcnt(4)\n"
"v_add_f32 v232, v232, v28\n"
"v_mfma_f32_16x16x32_bf16 v[48:51], a[64:67], a[128:131], v[48:51]\n"
"v_add_f32 v233, v233, v29\n"
"v_mfma_f32_16x16x32_bf16 v[48:51], a[68:71], a[132:135], v[48:51]\n"
"global_load_dwordx4 a[56:59], v0, s[34:35] offset:2048 sc1\n"
"v_add_f32 v234, v234, v30\n"
"v_mfma_f32_16x16x32_bf16 v[76:79], a[76:79], a[132:135], v[76:79]\n"
"v_add_f32 v235, v235, v31\n"
"v_mfma_f32_16x16x32_bf16 v[76:79], a[72:75], a[128:131], v[76:79]\n"
"v_add_f32 v236, v236, v28\n"
"v_mfma_f32_16x16x32_bf16 v[80:83], a[72:75], a[136:139], v[80:83]\n"
"v_add_f32 v237, v237, v29\n"
"v_mfma_f32_16x16x32_bf16 v[80:83], a[76:79], a[140:143], v[80:83]\n"
"v_add_f32 v238, v238, v30\n"
"v_add_f32 v239, v239, v31\n"
"v_mfma_f32_16x16x32_bf16 v[52:55], a[68:71], a[140:143], v[52:55]\n"
"v_add_f32 v240, v240, v28\n"
"v_mfma_f32_16x16x32_bf16 v[52:55], a[64:67], a[136:139], v[52:55]\n"
"s_waitcnt lgkmcnt(0)\n"
"v_add_f32 v241, v241, v29\n"
"v_mfma_f32_16x16x32_bf16 v[56:59], a[64:67], a[144:147], v[56:59]\n"
"v_add_f32 v242, v242, v30\n"
"v_mfma_f32_16x16x32_bf16 v[56:59], a[68:71], a[148:151], v[56:59]\n"
"global_load_dwordx4 a[60:63], v0, s[34:35] offset:3072 sc1\n"
"v_add_f32 v243, v243, v31\n"
"v_mfma_f32_16x16x32_bf16 v[84:87], a[76:79], a[148:151], v[84:87]\n"
"v_add_f32 v244, v244, v28\n"
"v_mfma_f32_16x16x32_bf16 v[84:87], a[72:75], a[144:147], v[84:87]\n"
"v_add_f32 v245, v245, v29\n"
"v_mfma_f32_16x16x32_bf16 a[4:7], a[72:75], a[152:155], a[4:7]\n"
"v_add_f32 v246, v246, v30\n"
"v_add_f32 v247, v247, v31\n"
"v_mfma_f32_16x16x32_bf16 a[4:7], a[76:79], a[156:159], a[4:7]\n"
"v_add_f32 v248, v248, v28\n"
"v_mfma_f32_16x16x32_bf16 a[0:3], a[68:71], a[156:159], a[0:3]\n"
"v_add_f32 v249, v249, v29\n"
"v_mfma_f32_16x16x32_bf16 a[0:3], a[64:67], a[152:155], a[0:3]\n"
"s_waitcnt vmcnt(8)\n"
"v_add_f32 v250, v250, v30\n"
"v_mfma_f32_16x16x32_bf16 v[88:91], a[80:83], a[96:99], v[88:91]\n"
"s_add_u32 s34, s36, 1466368\n"
"v_add_f32 v251, v251, v31\n"
"v_mfma_f32_16x16x32_bf16 v[88:91], a[84:87], a[100:103], v[88:91]\n"
"global_load_dwordx4 a[64:67], v0, s[34:35] offset:0 sc1\n"
"v_add_f32 v252, v252, v28\n"
"v_mfma_f32_16x16x32_bf16 v[116:119], a[92:95], a[100:103], v[116:119]\n"
"ds_read_b128 a[100:103], v5 offset:17408\n"
"v_add_f32 v253, v253, v29\n"
"v_mfma_f32_16x16x32_bf16 v[116:119], a[88:91], a[96:99], v[116:119]\n"
"ds_read_b128 a[96:99], v5 offset:16384\n"
"v_add_f32 v254, v254, v30\n"
"v_mfma_f32_16x16x32_bf16 v[120:123], a[88:91], a[104:107], v[120:123]\n"
"v_add_f32 v255, v255, v31\n"
"v_accvgpr_read_b32 v12, a28\n"
"v_mfma_f32_16x16x32_bf16 v[120:123], a[92:95], a[108:111], v[120:123]\n"
"v_accvgpr_read_b32 v13, a29\n"
"v_mfma_f32_16x16x32_bf16 v[92:95], a[84:87], a[108:111], v[92:95]\n"
"ds_read_b128 a[108:111], v5 offset:19456\n"
"v_accvgpr_read_b32 v14, a30\n"
"v_mfma_f32_16x16x32_bf16 v[92:95], a[80:83], a[104:107], v[92:95]\n"
"ds_read_b128 a[104:107], v5 offset:18432\n"
"v_accvgpr_read_b32 v15, a31\n"
"v_mfma_f32_16x16x32_bf16 v[96:99], a[80:83], a[112:115], v[96:99]\n"
"v_add_f32 v12, v12, v28\n"
"v_mfma_f32_16x16x32_bf16 v[96:99], a[84:87], a[116:119], v[96:99]\n"
"global_load_dwordx4 a[68:71], v0, s[34:35] offset:1024 sc1\n"
"v_add_f32 v13, v13, v29\n"
"v_mfma_f32_16x16x32_bf16 v[124:127], a[92:95], a[116:119], v[124:127]\n"
"ds_read_b128 a[116:119], v5 offset:21504\n"
"v_add_f32 v14, v14, v30\n"
"v_mfma_f32_16x16x32_bf16 v[124:127], a[88:91], a[112:115], v[124:127]\n"
"ds_read_b128 a[112:115], v5 offset:20480\n"
"v_add_f32 v15, v15, v31\n"
"v_mfma_f32_16x16x32_bf16 v[128:131], a[88:91], a[120:123], v[128:131]\n"
"v_accvgpr_write_b32 a28, v12\n"
"v_accvgpr_write_b32 a29, v13\n"
"v_mfma_f32_16x16x32_bf16 v[128:131], a[92:95], a[124:127], v[128:131]\n"
"v_accvgpr_write_b32 a30, v14\n"
"v_mfma_f32_16x16x32_bf16 v[100:103], a[84:87], a[124:127], v[100:103]\n"
"ds_read_b128 a[124:127], v5 offset:23552\n"
"v_accvgpr_write_b32 a31, v15\n"
"v_mfma_f32_16x16x32_bf16 v[100:103], a[80:83], a[120:123], v[100:103]\n"
"ds_read_b128 a[120:123], v5 offset:22528\n"
"v_mfma_f32_16x16x32_bf16 v[104:107], a[80:83], a[128:131], v[104:107]\n"
"v_mfma_f32_16x16x32_bf16 v[104:107], a[84:87], a[132:135], v[104:107]\n"
"global_load_dwordx4 a[72:75], v0, s[34:35] offset:2048 sc1\n"
"v_mfma_f32_16x16x32_bf16 v[132:135], a[92:95], a[132:135], v[132:135]\n"
"ds_read_b128 a[132:135], v5 offset:25600\n"
"v_mfma_f32_16x16x32_bf16 v[132:135], a[88:91], a[128:131], v[132:135]\n"
"ds_read_b128 a[128:131], v5 offset:24576\n"
"v_mfma_f32_16x16x32_bf16 v[136:139], a[88:91], a[136:139], v[136:139]\n"
"ds_read_b128 v[16:19], v7 offset:32768\n"
"v_mfma_f32_16x16x32_bf16 v[136:139], a[92:95], a[140:143], v[136:139]\n"
"v_mfma_f32_16x16x32_bf16 v[108:111], a[84:87], a[140:143], v[108:111]\n"
"ds_read_b128 a[140:143], v5 offset:27648\n"
"v_mfma_f32_16x16x32_bf16 v[108:111], a[80:83], a[136:139], v[108:111]\n"
"ds_read_b128 a[136:139], v5 offset:26624\n"
"v_mfma_f32_16x16x32_bf16 v[112:115], a[80:83], a[144:147], v[112:115]\n"
"v_mfma_f32_16x16x32_bf16 v[112:115], a[84:87], a[148:151], v[112:115]\n"
"global_load_dwordx4 a[76:79], v0, s[34:35] offset:3072 sc1\n"
"v_mfma_f32_16x16x32_bf16 v[140:143], a[92:95], a[148:151], v[140:143]\n"
"ds_read_b128 a[148:151], v5 offset:29696\n"
"v_mfma_f32_16x16x32_bf16 v[140:143], a[88:91], a[144:147], v[140:143]\n"
"ds_read_b128 a[144:147], v5 offset:28672\n"
"v_mfma_f32_16x16x32_bf16 a[12:15], a[88:91], a[152:155], a[12:15]\n"
"v_mfma_f32_16x16x32_bf16 a[12:15], a[92:95], a[156:159], a[12:15]\n"
"v_mfma_f32_16x16x32_bf16 a[8:11], a[84:87], a[156:159], a[8:11]\n"
"ds_read_b128 a[156:159], v5 offset:31744\n"
"v_mfma_f32_16x16x32_bf16 a[8:11], a[80:83], a[152:155], a[8:11]\n"
"s_waitcnt vmcnt(8) lgkmcnt(12)\n"
"ds_read_b128 a[152:155], v5 offset:30720\n"
"v_mfma_f32_16x16x32_bf16 v[32:35], a[32:35], a[96:99], v[32:35]\n"
"s_add_u32 s34, s36, 1470464\n"
"v_mfma_f32_16x16x32_bf16 v[32:35], a[36:39], a[100:103], v[32:35]\n"
"global_load_dwordx4 a[80:83], v0, s[34:35] offset:0 sc1\n"
"v_mfma_f32_16x16x32_bf16 v[60:63], a[44:47], a[100:103], v[60:63]\n"
"v_mfma_f32_16x16x32_bf16 v[60:63], a[40:43], a[96:99], v[60:63]\n"
"v_mfma_f32_16x16x32_bf16 v[64:67], a[40:43], a[104:107], v[64:67]\n"
"v_mfma_f32_16x16x32_bf16 v[64:67], a[44:47], a[108:111], v[64:67]\n"
"v_mfma_f32_16x16x32_bf16 v[36:39], a[36:39], a[108:111], v[36:39]\n"
"v_mfma_f32_16x16x32_bf16 v[36:39], a[32:35], a[104:107], v[36:39]\n"
"s_waitcnt lgkmcnt(9)\n"
"v_mfma_f32_16x16x32_bf16 v[40:43], a[32:35], a[112:115], v[40:43]\n"
"s_mov_b32 s52, %4\n"
"s_and_b32 s53, %5, 0xffff\n"
"v_mfma_f32_16x16x32_bf16 v[40:43], a[36:39], a[116:119], v[40:43]\n"
"global_load_dwordx4 a[84:87], v0, s[34:35] offset:1024 sc1\n"
"v_mfma_f32_16x16x32_bf16 v[68:71], a[44:47], a[116:119], v[68:71]\n"
"s_mov_b32 s54, 260352\n"
"s_mov_b32 s55, 0x20000\n"
"v_mfma_f32_16x16x32_bf16 v[68:71], a[40:43], a[112:115], v[68:71]\n"
"v_xor_b32 v8, 64, v7\n"
"v_mfma_f32_16x16x32_bf16 v[72:75], a[40:43], a[120:123], v[72:75]\n"
"s_waitcnt lgkmcnt(6)\n"
"v_lshlrev_b32 v12, 16, v16\n"
"v_mfma_f32_16x16x32_bf16 v[72:75], a[44:47], a[124:127], v[72:75]\n"
"v_and_b32 v13, s63, v16\n"
"v_lshlrev_b32 v14, 16, v17\n"
"v_mfma_f32_16x16x32_bf16 v[44:47], a[36:39], a[124:127], v[44:47]\n"
"v_and_b32 v15, s63, v17\n"
"v_mfma_f32_16x16x32_bf16 v[44:47], a[32:35], a[120:123], v[44:47]\n"
"s_waitcnt lgkmcnt(4)\n"
"v_xor_b32 v20, 64, v7\n"
"v_mfma_f32_16x16x32_bf16 v[48:51], a[32:35], a[128:131], v[48:51]\n"
"ds_read_b128 v[20:23], v20 offset:32768\n"
"v_mul_f32 v12, v12, v32\n"
"v_mfma_f32_16x16x32_bf16 v[48:51], a[36:39], a[132:135], v[48:51]\n"
"global_load_dwordx4 a[88:91], v0, s[34:35] offset:2048 sc1\n"
"v_mul_f32 v13, v13, v33\n"
"v_mfma_f32_16x16x32_bf16 v[76:79], a[44:47], a[132:135], v[76:79]\n"
"v_mul_f32 v14, v14, v34\n"
"v_mfma_f32_16x16x32_bf16 v[76:79], a[40:43], a[128:131], v[76:79]\n"
"v_mul_f32 v15, v15, v35\n"
"v_cvt_pk_bf16_f32 v24, v12, v13\n"
"v_mfma_f32_16x16x32_bf16 v[80:83], a[40:43], a[136:139], v[80:83]\n"
"v_cvt_pk_bf16_f32 v25, v14, v15\n"
"v_mfma_f32_16x16x32_bf16 v[80:83], a[44:47], a[140:143], v[80:83]\n"
"ds_write_b64 v7, v[24:25] offset:32768\n"
"v_mfma_f32_16x16x32_bf16 v[52:55], a[36:39], a[140:143], v[52:55]\n"
"v_lshlrev_b32 v12, 16, v18\n"
"v_mfma_f32_16x16x32_bf16 v[52:55], a[32:35], a[136:139], v[52:55]\n"
"s_waitcnt lgkmcnt(2)\n"
"v_and_b32 v13, s63, v18\n"
"v_lshlrev_b32 v14, 16, v19\n"
"v_mfma_f32_16x16x32_bf16 v[56:59], a[32:35], a[144:147], v[56:59]\n"
"v_and_b32 v15, s63, v19\n"
"v_mfma_f32_16x16x32_bf16 v[56:59], a[36:39], a[148:151], v[56:59]\n"
"global_load_dwordx4 a[92:95], v0, s[34:35] offset:3072 sc1\n"
"v_mul_f32 v12, v12, v60\n"
"v_mfma_f32_16x16x32_bf16 v[84:87], a[44:47], a[148:151], v[84:87]\n"
"v_mul_f32 v13, v13, v61\n"
"v_mul_f32 v14, v14, v62\n"
"v_mfma_f32_16x16x32_bf16 v[84:87], a[40:43], a[144:147], v[84:87]\n"
"v_mul_f32 v15, v15, v63\n"
"v_mfma_f32_16x16x32_bf16 a[4:7], a[40:43], a[152:155], a[4:7]\n"
"v_cvt_pk_bf16_f32 v26, v12, v13\n"
"v_mfma_f32_16x16x32_bf16 a[4:7], a[44:47], a[156:159], a[4:7]\n"
"v_cvt_pk_bf16_f32 v27, v14, v15\n"
"v_mfma_f32_16x16x32_bf16 a[0:3], a[36:39], a[156:159], a[0:3]\n"
"ds_write_b64 v7, v[26:27] offset:32776\n"
"v_mfma_f32_16x16x32_bf16 a[0:3], a[32:35], a[152:155], a[0:3]\n"
"s_waitcnt vmcnt(8) lgkmcnt(2)\n"
"v_lshlrev_b32 v12, 16, v20\n"
"v_mfma_f32_16x16x32_bf16 v[88:91], a[48:51], a[96:99], v[88:91]\n"
"s_add_u32 s34, s36, 1482752\n"
"v_and_b32 v13, s63, v20\n"
"v_mfma_f32_16x16x32_bf16 v[88:91], a[52:55], a[100:103], v[88:91]\n"
"global_load_dwordx4 a[32:35], v0, s[34:35] offset:0 sc1\n"
"v_mfma_f32_16x16x32_bf16 v[116:119], a[60:63], a[100:103], v[116:119]\n"
"ds_read_b128 a[100:103], v3 offset:1024\n"
"v_mfma_f32_16x16x32_bf16 v[116:119], a[56:59], a[96:99], v[116:119]\n"
"ds_read_b128 a[96:99], v3 offset:0\n"
"v_mfma_f32_16x16x32_bf16 v[120:123], a[56:59], a[104:107], v[120:123]\n"
"v_lshlrev_b32 v14, 16, v21\n"
"v_mfma_f32_16x16x32_bf16 v[120:123], a[60:63], a[108:111], v[120:123]\n"
"v_and_b32 v15, s63, v21\n"
"v_mfma_f32_16x16x32_bf16 v[92:95], a[52:55], a[108:111], v[92:95]\n"
"ds_read_b128 a[108:111], v3 offset:3072\n"
"v_mfma_f32_16x16x32_bf16 v[92:95], a[48:51], a[104:107], v[92:95]\n"
"ds_read_b128 a[104:107], v3 offset:2048\n"
"v_mfma_f32_16x16x32_bf16 v[96:99], a[48:51], a[112:115], v[96:99]\n"
"ds_read_b128 v[16:19], v7 offset:34816\n"
"v_mfma_f32_16x16x32_bf16 v[96:99], a[52:55], a[116:119], v[96:99]\n"
"global_load_dwordx4 a[36:39], v0, s[34:35] offset:1024 sc1\n"
"v_mul_f32 v12, v12, v88\n"
"v_mul_f32 v13, v13, v89\n"
"v_mfma_f32_16x16x32_bf16 v[124:127], a[60:63], a[116:119], v[124:127]\n"
"ds_read_b128 a[116:119], v3 offset:5120\n"
"v_mul_f32 v14, v14, v90\n"
"v_mul_f32 v15, v15, v91\n"
"v_mfma_f32_16x16x32_bf16 v[124:127], a[56:59], a[112:115], v[124:127]\n"
"ds_read_b128 a[112:115], v3 offset:4096\n"
"v_cvt_pk_bf16_f32 v28, v12, v13\n"
"v_cvt_pk_bf16_f32 v29, v14, v15\n"
"v_mfma_f32_16x16x32_bf16 v[128:131], a[56:59], a[120:123], v[128:131]\n"
"ds_write_b64 v8, v[28:29] offset:32768\n"
"v_mfma_f32_16x16x32_bf16 v[128:131], a[60:63], a[124:127], v[128:131]\n"
"v_lshlrev_b32 v12, 16, v22\n"
"v_and_b32 v13, s63, v22\n"
"v_lshlrev_b32 v14, 16, v23\n"
"v_mfma_f32_16x16x32_bf16 v[100:103], a[52:55], a[124:127], v[100:103]\n"
"ds_read_b128 a[124:127], v3 offset:7168\n"
"v_and_b32 v15, s63, v23\n"
"v_mul_f32 v12, v12, v116\n"
"v_mfma_f32_16x16x32_bf16 v[100:103], a[48:51], a[120:123], v[100:103]\n"
"ds_read_b128 a[120:123], v3 offset:6144\n"
"v_mul_f32 v13, v13, v117\n"
"v_mfma_f32_16x16x32_bf16 v[104:107], a[48:51], a[128:131], v[104:107]\n"
"v_mul_f32 v14, v14, v118\n"
"v_mul_f32 v15, v15, v119\n"
"v_mfma_f32_16x16x32_bf16 v[104:107], a[52:55], a[132:135], v[104:107]\n"
"global_load_dwordx4 a[40:43], v0, s[34:35] offset:2048 sc1\n"
"v_cvt_pk_bf16_f32 v30, v12, v13\n"
"v_mfma_f32_16x16x32_bf16 v[132:135], a[60:63], a[132:135], v[132:135]\n"
"ds_read_b128 a[132:135], v3 offset:9216\n"
"v_cvt_pk_bf16_f32 v31, v14, v15\n"
"v_mfma_f32_16x16x32_bf16 v[132:135], a[56:59], a[128:131], v[132:135]\n"
"ds_read_b128 a[128:131], v3 offset:8192\n"
"ds_write_b64 v8, v[30:31] offset:32776\n"
"v_mfma_f32_16x16x32_bf16 v[136:139], a[56:59], a[136:139], v[136:139]\n"
"ds_read_b128 v[32:35], v2 offset:32768\n"
"v_mfma_f32_16x16x32_bf16 v[136:139], a[60:63], a[140:143], v[136:139]\n"
"s_add_u32 s96, s36, 1552384\n"
"ds_read_b128 v[60:63], v2 offset:33792\n"
"v_mfma_f32_16x16x32_bf16 v[108:111], a[52:55], a[140:143], v[108:111]\n"
"s_waitcnt lgkmcnt(10)\n"
"ds_read_b128 a[140:143], v3 offset:11264\n"
"v_lshlrev_b32 v12, 16, v16\n"
"v_mfma_f32_16x16x32_bf16 v[108:111], a[48:51], a[136:139], v[108:111]\n"
"ds_read_b128 a[136:139], v3 offset:10240\n"
"v_and_b32 v13, s63, v16\n"
"v_mfma_f32_16x16x32_bf16 v[112:115], a[48:51], a[144:147], v[112:115]\n"
"global_load_dwordx4 v[88:91], v0, s[96:97] offset:-4096 sc1\n"
"v_lshlrev_b32 v14, 16, v17\n"
"v_mfma_f32_16x16x32_bf16 v[112:115], a[52:55], a[148:151], v[112:115]\n"
"global_load_dwordx4 a[44:47], v0, s[34:35] offset:3072 sc1\n"
"v_and_b32 v15, s63, v17\n"
"v_mfma_f32_16x16x32_bf16 v[140:143], a[60:63], a[148:151], v[140:143]\n"
"ds_read_b128 a[148:151], v3 offset:13312\n"
"v_xor_b32 v20, 64, v7\n"
"v_mfma_f32_16x16x32_bf16 v[140:143], a[56:59], a[144:147], v[140:143]\n"
"ds_read_b128 a[144:147], v3 offset:12288\n"
"v_mfma_f32_16x16x32_bf16 a[12:15], a[56:59], a[152:155], a[12:15]\n"
"ds_read_b128 v[20:23], v20 offset:34816\n"
"v_mul_f32 v12, v12, v36\n"
"v_mfma_f32_16x16x32_bf16 a[12:15], a[60:63], a[156:159], a[12:15]\n"
"s_waitcnt lgkmcnt(6)\n"
"buffer_store_dwordx4 v[32:35], v6, s[52:55], s80 offen offset:0 sc1\n"
"v_mul_f32 v13, v13, v37\n"
"v_mul_f32 v14, v14, v38\n"
"v_mul_f32 v15, v15, v39\n"
"v_mfma_f32_16x16x32_bf16 a[8:11], a[52:55], a[156:159], a[8:11]\n"
"ds_read_b128 a[156:159], v3 offset:15360\n"
"v_cvt_pk_bf16_f32 v24, v12, v13\n"
"v_mfma_f32_16x16x32_bf16 a[8:11], a[48:51], a[152:155], a[8:11]\n"
"s_waitcnt vmcnt(10)\n"
"ds_read_b128 a[152:155], v3 offset:14336\n"
"v_cvt_pk_bf16_f32 v25, v14, v15\n"
"v_mfma_f32_16x16x32_bf16 v[144:147], a[64:67], a[96:99], v[144:147]\n"
"s_add_u32 s34, s36, 1486848\n"
"ds_write_b64 v7, v[24:25] offset:34816\n"
"v_mfma_f32_16x16x32_bf16 v[144:147], a[68:71], a[100:103], v[144:147]\n"
"global_load_dwordx4 a[48:51], v0, s[34:35] offset:0 sc1\n"
"v_lshlrev_b32 v12, 16, v18\n"
"v_mfma_f32_16x16x32_bf16 v[172:175], a[76:79], a[100:103], v[172:175]\n"
"global_load_dwordx4 v[116:119], v0, s[96:97] offset:-3072 sc1\n"
"v_and_b32 v13, s63, v18\n"
"v_mfma_f32_16x16x32_bf16 v[172:175], a[72:75], a[96:99], v[172:175]\n"
"v_lshlrev_b32 v14, 16, v19\n"
"v_and_b32 v15, s63, v19\n"
"v_mfma_f32_16x16x32_bf16 v[176:179], a[72:75], a[104:107], v[176:179]\n"
"v_mul_f32 v12, v12, v64\n"
"v_mul_f32 v13, v13, v65\n"
"v_mfma_f32_16x16x32_bf16 v[176:179], a[76:79], a[108:111], v[176:179]\n"
"v_mul_f32 v14, v14, v66\n"
"v_mul_f32 v15, v15, v67\n"
"v_mfma_f32_16x16x32_bf16 v[148:151], a[68:71], a[108:111], v[148:151]\n"
"v_cvt_pk_bf16_f32 v26, v12, v13\n"
"v_cvt_pk_bf16_f32 v27, v14, v15\n"
"v_mfma_f32_16x16x32_bf16 v[148:151], a[64:67], a[104:107], v[148:151]\n"
"ds_write_b64 v7, v[26:27] offset:34824\n"
"v_mfma_f32_16x16x32_bf16 v[152:155], a[64:67], a[112:115], v[152:155]\n"
"s_waitcnt lgkmcnt(4)\n"
"v_lshlrev_b32 v12, 16, v20\n"
"v_and_b32 v13, s63, v20\n"
"v_mfma_f32_16x16x32_bf16 v[152:155], a[68:71], a[116:119], v[152:155]\n"
"global_load_dwordx4 a[52:55], v0, s[34:35] offset:1024 sc1\n"
"v_lshlrev_b32 v14, 16, v21\n"
"v_mfma_f32_16x16x32_bf16 v[180:183], a[76:79], a[116:119], v[180:183]\n"
"ds_read_b128 v[16:19], v7 offset:36864\n"
"v_and_b32 v15, s63, v21\n"
"v_mfma_f32_16x16x32_bf16 v[180:183], a[72:75], a[112:115], v[180:183]\n"
"v_mul_f32 v12, v12, v92\n"
"v_mul_f32 v13, v13, v93\n"
"v_mfma_f32_16x16x32_bf16 v[184:187], a[72:75], a[120:123], v[184:187]\n"
"v_mul_f32 v14, v14, v94\n"
"v_mul_f32 v15, v15, v95\n"
"v_mfma_f32_16x16x32_bf16 v[184:187], a[76:79], a[124:127], v[184:187]\n"
"v_cvt_pk_bf16_f32 v28, v12, v13\n"
"v_cvt_pk_bf16_f32 v29, v14, v15\n"
"v_mfma_f32_16x16x32_bf16 v[156:159], a[68:71], a[124:127], v[156:159]\n"
"ds_write_b64 v8, v[28:29] offset:34816\n"
"v_mfma_f32_16x16x32_bf16 v[156:159], a[64:67], a[120:123], v[156:159]\n"
"v_lshlrev_b32 v12, 16, v22\n"
"v_and_b32 v13, s63, v22\n"
"v_mfma_f32_16x16x32_bf16 v[160:163], a[64:67], a[128:131], v[160:163]\n"
"v_lshlrev_b32 v14, 16, v23\n"
"v_and_b32 v15, s63, v23\n"
"v_mfma_f32_16x16x32_bf16 v[160:163], a[68:71], a[132:135], v[160:163]\n"
"global_load_dwordx4 a[56:59], v0, s[34:35] offset:2048 sc1\n"
"v_mul_f32 v12, v12, v120\n"
"v_mfma_f32_16x16x32_bf16 v[188:191], a[76:79], a[132:135], v[188:191]\n"
"v_mul_f32 v13, v13, v121\n"
"v_mul_f32 v14, v14, v122\n"
"v_mfma_f32_16x16x32_bf16 v[188:191], a[72:75], a[128:131], v[188:191]\n"
"v_mul_f32 v15, v15, v123\n"
"v_cvt_pk_bf16_f32 v30, v12, v13\n"
"v_mfma_f32_16x16x32_bf16 v[192:195], a[72:75], a[136:139], v[192:195]\n"
"v_cvt_pk_bf16_f32 v31, v14, v15\n"
"v_mfma_f32_16x16x32_bf16 v[192:195], a[76:79], a[140:143], v[192:195]\n"
"ds_write_b64 v8, v[30:31] offset:34824\n"
"v_mfma_f32_16x16x32_bf16 v[164:167], a[68:71], a[140:143], v[164:167]\n"
"ds_read_b128 v[36:39], v2 offset:34816\n"
"v_mfma_f32_16x16x32_bf16 v[164:167], a[64:67], a[136:139], v[164:167]\n"
"s_waitcnt lgkmcnt(3)\n"
"ds_read_b128 v[64:67], v2 offset:35840\n"
"v_lshlrev_b32 v12, 16, v16\n"
"v_mfma_f32_16x16x32_bf16 v[168:171], a[64:67], a[144:147], v[168:171]\n"
"buffer_store_dwordx4 v[60:63], v6, s[52:55], s81 offen offset:1024 sc1\n"
"v_and_b32 v13, s63, v16\n"
"v_lshlrev_b32 v14, 16, v17\n"
"v_and_b32 v15, s63, v17\n"
"v_xor_b32 v20, 64, v7\n"
"v_mfma_f32_16x16x32_bf16 v[168:171], a[68:71], a[148:151], v[168:171]\n"
"global_load_dwordx4 a[60:63], v0, s[34:35] offset:3072 sc1\n"
"ds_read_b128 v[20:23], v20 offset:36864\n"
"v_mfma_f32_16x16x32_bf16 v[196:199], a[76:79], a[148:151], v[196:199]\n"
"v_mul_f32 v12, v12, v40\n"
"v_mul_f32 v13, v13, v41\n"
"v_mfma_f32_16x16x32_bf16 v[196:199], a[72:75], a[144:147], v[196:199]\n"
"v_mul_f32 v14, v14, v42\n"
"v_mul_f32 v15, v15, v43\n"
"v_mfma_f32_16x16x32_bf16 a[20:23], a[72:75], a[152:155], a[20:23]\n"
"v_cvt_pk_bf16_f32 v24, v12, v13\n"
"v_cvt_pk_bf16_f32 v25, v14, v15\n"
"v_mfma_f32_16x16x32_bf16 a[20:23], a[76:79], a[156:159], a[20:23]\n"
"ds_write_b64 v7, v[24:25] offset:36864\n"
"v_mfma_f32_16x16x32_bf16 a[16:19], a[68:71], a[156:159], a[16:19]\n"
"global_load_dwordx4 v[92:95], v0, s[96:97] offset:-2048 sc1\n"
"v_lshlrev_b32 v12, 16, v18\n"
"v_mfma_f32_16x16x32_bf16 a[16:19], a[64:67], a[152:155], a[16:19]\n"
"s_waitcnt vmcnt(13)\n"
"v_and_b32 v13, s63, v18\n"
"v_lshlrev_b32 v14, 16, v19\n"
"v_mfma_f32_16x16x32_bf16 v[200:203], a[80:83], a[96:99], v[200:203]\n"
"s_add_u32 s34, s36, 1499136\n"
"v_and_b32 v15, s63, v19\n"
"v_mul_f32 v12, v12, v68\n"
"v_mfma_f32_16x16x32_bf16 v[200:203], a[84:87], a[100:103], v[200:203]\n"
"global_load_dwordx4 a[64:67], v0, s[34:35] offset:0 sc1\n"
"v_mul_f32 v13, v13, v69\n"
"v_mfma_f32_16x16x32_bf16 v[228:231], a[92:95], a[100:103], v[228:231]\n"
"ds_read_b128 a[100:103], v4 offset:1024\n"
"v_mul_f32 v14, v14, v70\n"
"v_mfma_f32_16x16x32_bf16 v[228:231], a[88:91], a[96:99], v[228:231]\n"
"ds_read_b128 a[96:99], v4 offset:0\n"
"v_mul_f32 v15, v15, v71\n"
"v_mfma_f32_16x16x32_bf16 v[232:235], a[88:91], a[104:107], v[232:235]\n"
"v_cvt_pk_bf16_f32 v26, v12, v13\n"
"v_cvt_pk_bf16_f32 v27, v14, v15\n"
"v_mfma_f32_16x16x32_bf16 v[232:235], a[92:95], a[108:111], v[232:235]\n"
"ds_write_b64 v7, v[26:27] offset:36872\n"
"v_mfma_f32_16x16x32_bf16 v[204:207], a[84:87], a[108:111], v[204:207]\n"
"ds_read_b128 a[108:111], v4 offset:3072\n"
"v_mfma_f32_16x16x32_bf16 v[204:207], a[80:83], a[104:107], v[204:207]\n"
"ds_read_b128 a[104:107], v4 offset:2048\n"
"v_mfma_f32_16x16x32_bf16 v[208:211], a[80:83], a[112:115], v[208:211]\n"
"global_load_dwordx4 v[120:123], v0, s[96:97] offset:-1024 sc1\n"
"v_mfma_f32_16x16x32_bf16 v[208:211], a[84:87], a[116:119], v[208:211]\n"
"global_load_dwordx4 a[68:71], v0, s[34:35] offset:1024 sc1\n"
"v_mfma_f32_16x16x32_bf16 v[236:239], a[92:95], a[116:119], v[236:239]\n"
"s_waitcnt lgkmcnt(6)\n"
"ds_read_b128 a[116:119], v4 offset:5120\n"
"v_lshlrev_b32 v12, 16, v20\n"
"v_mfma_f32_16x16x32_bf16 v[236:239], a[88:91], a[112:115], v[236:239]\n"
"ds_read_b128 a[112:115], v4 offset:4096\n"
"v_and_b32 v13, s63, v20\n"
"v_mfma_f32_16x16x32_bf16 v[240:243], a[88:91], a[120:123], v[240:243]\n"
"v_lshlrev_b32 v14, 16, v21\n"
"v_and_b32 v15, s63, v21\n"
"v_mfma_f32_16x16x32_bf16 v[240:243], a[92:95], a[124:127], v[240:243]\n"
"ds_read_b128 v[16:19], v7 offset:38912\n"
"v_mul_f32 v12, v12, v96\n"
"v_mfma_f32_16x16x32_bf16 v[212:215], a[84:87], a[124:127], v[212:215]\n"
"ds_read_b128 a[124:127], v4 offset:7168\n"
"v_mul_f32 v13, v13, v97\n"
"v_mfma_f32_16x16x32_bf16 v[212:215], a[80:83], a[120:123], v[212:215]\n"
"ds_read_b128 a[120:123], v4 offset:6144\n"
"v_mul_f32 v14, v14, v98\n"
"v_mfma_f32_16x16x32_bf16 v[216:219], a[80:83], a[128:131], v[216:219]\n"
"v_mul_f32 v15, v15, v99\n"
"v_mfma_f32_16x16x32_bf16 v[216:219], a[84:87], a[132:135], v[216:219]\n"
"global_load_dwordx4 a[72:75], v0, s[34:35] offset:2048 sc1\n"
"v_cvt_pk_bf16_f32 v28, v12, v13\n"
"v_mfma_f32_16x16x32_bf16 v[244:247], a[92:95], a[132:135], v[244:247]\n"
"ds_read_b128 a[132:135], v4 offset:9216\n"
"global_load_dwordx4 v[32:35], v0, s[96:97] offset:0 sc1\n"
"v_mfma_f32_16x16x32_bf16 v[244:247], a[88:91], a[128:131], v[244:247]\n"
"ds_read_b128 a[128:131], v4 offset:8192\n"
"v_mfma_f32_16x16x32_bf16 v[248:251], a[88:91], a[136:139], v[248:251]\n"
"v_cvt_pk_bf16_f32 v29, v14, v15\n"
"v_mfma_f32_16x16x32_bf16 v[248:251], a[92:95], a[140:143], v[248:251]\n"
"buffer_store_dwordx4 v[36:39], v6, s[52:55], s82 offen offset:2048 sc1\n"
"ds_write_b64 v8, v[28:29] offset:36864\n"
"v_mfma_f32_16x16x32_bf16 v[220:223], a[84:87], a[140:143], v[220:223]\n"
"ds_read_b128 a[140:143], v4 offset:11264\n"
"v_lshlrev_b32 v12, 16, v22\n"
"v_mfma_f32_16x16x32_bf16 v[220:223], a[80:83], a[136:139], v[220:223]\n"
"ds_read_b128 a[136:139], v4 offset:10240\n"
"v_and_b32 v13, s63, v22\n"
"v_mfma_f32_16x16x32_bf16 v[224:227], a[80:83], a[144:147], v[224:227]\n"
"v_lshlrev_b32 v14, 16, v23\n"
"v_and_b32 v15, s63, v23\n"
"v_mfma_f32_16x16x32_bf16 v[224:227], a[84:87], a[148:151], v[224:227]\n"
"global_load_dwordx4 a[76:79], v0, s[34:35] offset:3072 sc1\n"
"v_mul_f32 v12, v12, v124\n"
"v_mfma_f32_16x16x32_bf16 v[252:255], a[92:95], a[148:151], v[252:255]\n"
"ds_read_b128 a[148:151], v4 offset:13312\n"
"v_mul_f32 v13, v13, v125\n"
"v_mfma_f32_16x16x32_bf16 v[252:255], a[88:91], a[144:147], v[252:255]\n"
"ds_read_b128 a[144:147], v4 offset:12288\n"
"v_mul_f32 v14, v14, v126\n"
"v_mfma_f32_16x16x32_bf16 a[28:31], a[88:91], a[152:155], a[28:31]\n"
"global_load_dwordx4 v[60:63], v0, s[96:97] offset:1024 sc1\n"
"v_mul_f32 v15, v15, v127\n"
"v_mfma_f32_16x16x32_bf16 a[28:31], a[92:95], a[156:159], a[28:31]\n"
"v_cvt_pk_bf16_f32 v30, v12, v13\n"
"v_cvt_pk_bf16_f32 v31, v14, v15\n"
"v_mfma_f32_16x16x32_bf16 a[24:27], a[84:87], a[156:159], a[24:27]\n"
"ds_read_b128 a[156:159], v4 offset:15360\n"
"v_mfma_f32_16x16x32_bf16 a[24:27], a[80:83], a[152:155], a[24:27]\n"
"s_waitcnt vmcnt(16) lgkmcnt(13)\n"
"ds_read_b128 a[152:155], v4 offset:14336\n"
"v_mfma_f32_16x16x32_bf16 v[144:147], a[32:35], a[96:99], v[144:147]\n"
"s_add_u32 s34, s36, 1503232\n"
"ds_write_b64 v8, v[30:31] offset:36872\n"
"v_mfma_f32_16x16x32_bf16 v[144:147], a[36:39], a[100:103], v[144:147]\n"
"global_load_dwordx4 a[80:83], v0, s[34:35] offset:0 sc1\n"
"ds_read_b128 v[40:43], v2 offset:36864\n"
"v_mfma_f32_16x16x32_bf16 v[172:175], a[44:47], a[100:103], v[172:175]\n"
"s_waitcnt lgkmcnt(13)\n"
"ds_read_b128 v[68:71], v2 offset:37888\n"
"v_lshlrev_b32 v12, 16, v16\n"
"v_mfma_f32_16x16x32_bf16 v[172:175], a[40:43], a[96:99], v[172:175]\n"
"v_and_b32 v13, s63, v16\n"
"v_lshlrev_b32 v14, 16, v17\n"
"v_mfma_f32_16x16x32_bf16 v[176:179], a[40:43], a[104:107], v[176:179]\n"
"v_and_b32 v15, s63, v17\n"
"v_xor_b32 v20, 64, v7\n"
"v_mfma_f32_16x16x32_bf16 v[176:179], a[44:47], a[108:111], v[176:179]\n"
"ds_read_b128 v[20:23], v20 offset:38912\n"
"v_mul_f32 v12, v12, v44\n"
"v_mfma_f32_16x16x32_bf16 v[148:151], a[36:39], a[108:111], v[148:151]\n"
"v_mul_f32 v13, v13, v45\n"
"v_mul_f32 v14, v14, v46\n"
"v_mfma_f32_16x16x32_bf16 v[148:151], a[32:35], a[104:107], v[148:151]\n"
"s_waitcnt lgkmcnt(13)\n"
"global_load_dwordx4 v[96:99], v0, s[96:97] offset:2048 sc1\n"
"v_mul_f32 v15, v15, v47\n"
"v_mfma_f32_16x16x32_bf16 v[152:155], a[32:35], a[112:115], v[152:155]\n"
"v_cvt_pk_bf16_f32 v24, v12, v13\n"
"v_cvt_pk_bf16_f32 v25, v14, v15\n"
"v_mfma_f32_16x16x32_bf16 v[152:155], a[36:39], a[116:119], v[152:155]\n"
"global_load_dwordx4 a[84:87], v0, s[34:35] offset:1024 sc1\n"
"v_mfma_f32_16x16x32_bf16 v[180:183], a[44:47], a[116:119], v[180:183]\n"
"ds_write_b64 v7, v[24:25] offset:38912\n"
"v_mfma_f32_16x16x32_bf16 v[180:183], a[40:43], a[112:115], v[180:183]\n"
"v_lshlrev_b32 v12, 16, v18\n"
"v_and_b32 v13, s63, v18\n"
"v_mfma_f32_16x16x32_bf16 v[184:187], a[40:43], a[120:123], v[184:187]\n"
"v_lshlrev_b32 v14, 16, v19\n"
"v_and_b32 v15, s63, v19\n"
"v_mfma_f32_16x16x32_bf16 v[184:187], a[44:47], a[124:127], v[184:187]\n"
"v_mul_f32 v12, v12, v72\n"
"v_mul_f32 v13, v13, v73\n"
"v_mfma_f32_16x16x32_bf16 v[156:159], a[36:39], a[124:127], v[156:159]\n"
"v_mul_f32 v14, v14, v74\n"
"v_mul_f32 v15, v15, v75\n"
"v_mfma_f32_16x16x32_bf16 v[156:159], a[32:35], a[120:123], v[156:159]\n"
"s_waitcnt lgkmcnt(9)\n"
"v_cvt_pk_bf16_f32 v26, v12, v13\n"
"v_cvt_pk_bf16_f32 v27, v14, v15\n"
"v_mfma_f32_16x16x32_bf16 v[160:163], a[32:35], a[128:131], v[160:163]\n"
"buffer_store_dwordx4 v[64:67], v6, s[52:55], s83 offen offset:3072 sc1\n"
"ds_write_b64 v7, v[26:27] offset:38920\n"
"v_mfma_f32_16x16x32_bf16 v[160:163], a[36:39], a[132:135], v[160:163]\n"
"s_waitcnt lgkmcnt(2)\n"
"global_load_dwordx4 a[88:91], v0, s[34:35] offset:2048 sc1\n"
"v_lshlrev_b32 v12, 16, v20\n"
"v_mfma_f32_16x16x32_bf16 v[188:191], a[44:47], a[132:135], v[188:191]\n"
"global_load_dwordx4 v[124:127], v0, s[96:97] offset:3072 sc1\n"
"v_and_b32 v13, s63, v20\n"
"v_mfma_f32_16x16x32_bf16 v[188:191], a[40:43], a[128:131], v[188:191]\n"
"v_lshlrev_b32 v14, 16, v21\n"
"v_and_b32 v15, s63, v21\n"
"v_mfma_f32_16x16x32_bf16 v[192:195], a[40:43], a[136:139], v[192:195]\n"
"v_mul_f32 v12, v12, v100\n"
"v_mul_f32 v13, v13, v101\n"
"v_mfma_f32_16x16x32_bf16 v[192:195], a[44:47], a[140:143], v[192:195]\n"
"v_mul_f32 v14, v14, v102\n"
"v_mul_f32 v15, v15, v103\n"
"v_mfma_f32_16x16x32_bf16 v[164:167], a[36:39], a[140:143], v[164:167]\n"
"v_cvt_pk_bf16_f32 v28, v12, v13\n"
"v_cvt_pk_bf16_f32 v29, v14, v15\n"
"v_mfma_f32_16x16x32_bf16 v[164:167], a[32:35], a[136:139], v[164:167]\n"
"ds_write_b64 v8, v[28:29] offset:38912\n"
"v_mfma_f32_16x16x32_bf16 v[168:171], a[32:35], a[144:147], v[168:171]\n"
"v_lshlrev_b32 v12, 16, v22\n"
"v_mfma_f32_16x16x32_bf16 v[168:171], a[36:39], a[148:151], v[168:171]\n"
"global_load_dwordx4 a[92:95], v0, s[34:35] offset:3072 sc1\n"
"v_and_b32 v13, s63, v22\n"
"v_mfma_f32_16x16x32_bf16 v[196:199], a[44:47], a[148:151], v[196:199]\n"
"v_lshlrev_b32 v14, 16, v23\n"
"v_and_b32 v15, s63, v23\n"
"v_mfma_f32_16x16x32_bf16 v[196:199], a[40:43], a[144:147], v[196:199]\n"
"v_mul_f32 v12, v12, v128\n"
"v_mfma_f32_16x16x32_bf16 a[20:23], a[40:43], a[152:155], a[20:23]\n"
"v_mul_f32 v13, v13, v129\n"
"v_mfma_f32_16x16x32_bf16 a[20:23], a[44:47], a[156:159], a[20:23]\n"
"v_mul_f32 v14, v14, v130\n"
"v_mul_f32 v15, v15, v131\n"
"v_mfma_f32_16x16x32_bf16 a[16:19], a[36:39], a[156:159], a[16:19]\n"
"v_cvt_pk_bf16_f32 v30, v12, v13\n"
"v_cvt_pk_bf16_f32 v31, v14, v15\n"
"v_mfma_f32_16x16x32_bf16 a[16:19], a[32:35], a[152:155], a[16:19]\n"
"s_waitcnt vmcnt(16)\n"
"ds_write_b64 v8, v[30:31] offset:38920\n"
"v_mfma_f32_16x16x32_bf16 v[200:203], a[48:51], a[96:99], v[200:203]\n"
"s_add_u32 s34, s36, 1540096\n"
"ds_read_b128 v[44:47], v2 offset:38912\n"
"v_mfma_f32_16x16x32_bf16 v[200:203], a[52:55], a[100:103], v[200:203]\n"
"global_load_dwordx4 a[32:35], v0, s[34:35] offset:0 sc1\n"
"ds_read_b128 v[72:75], v2 offset:39936\n"
"v_mfma_f32_16x16x32_bf16 v[228:231], a[60:63], a[100:103], v[228:231]\n"
"ds_read_b128 a[100:103], v5 offset:1024\n"
"v_accvgpr_read_b32 v16, a160\n"
"v_mfma_f32_16x16x32_bf16 v[228:231], a[56:59], a[96:99], v[228:231]\n"
"ds_read_b128 a[96:99], v5 offset:0\n"
"v_accvgpr_read_b32 v17, a161\n"
"v_mfma_f32_16x16x32_bf16 v[232:235], a[56:59], a[104:107], v[232:235]\n"
"v_accvgpr_read_b32 v18, a162\n"
"v_accvgpr_read_b32 v19, a163\n"
"v_mfma_f32_16x16x32_bf16 v[232:235], a[60:63], a[108:111], v[232:235]\n"
"v_lshlrev_b32 v12, 16, v16\n"
"v_mfma_f32_16x16x32_bf16 v[204:207], a[52:55], a[108:111], v[204:207]\n"
"ds_read_b128 a[108:111], v5 offset:3072\n"
"v_and_b32 v13, s63, v16\n"
"v_mfma_f32_16x16x32_bf16 v[204:207], a[48:51], a[104:107], v[204:207]\n"
"ds_read_b128 a[104:107], v5 offset:2048\n"
"v_lshlrev_b32 v14, 16, v17\n"
"v_mfma_f32_16x16x32_bf16 v[208:211], a[48:51], a[112:115], v[208:211]\n"
"v_and_b32 v15, s63, v17\n"
"v_mul_f32 v12, v12, v48\n"
"v_mfma_f32_16x16x32_bf16 v[208:211], a[52:55], a[116:119], v[208:211]\n"
"global_load_dwordx4 a[36:39], v0, s[34:35] offset:1024 sc1\n"
"v_mul_f32 v13, v13, v49\n"
"v_mfma_f32_16x16x32_bf16 v[236:239], a[60:63], a[116:119], v[236:239]\n"
"ds_read_b128 a[116:119], v5 offset:5120\n"
"v_mul_f32 v14, v14, v50\n"
"v_mfma_f32_16x16x32_bf16 v[236:239], a[56:59], a[112:115], v[236:239]\n"
"ds_read_b128 a[112:115], v5 offset:4096\n"
"v_mul_f32 v15, v15, v51\n"
"v_mfma_f32_16x16x32_bf16 v[240:243], a[56:59], a[120:123], v[240:243]\n"
"v_cvt_pk_bf16_f32 v24, v12, v13\n"
"v_cvt_pk_bf16_f32 v25, v14, v15\n"
"v_mfma_f32_16x16x32_bf16 v[240:243], a[60:63], a[124:127], v[240:243]\n"
"buffer_store_dwordx4 v[40:43], v6, s[52:55], s84 offen offset:0 sc1\n"
"ds_write_b64 v7, v[24:25] offset:32768\n"
"v_mfma_f32_16x16x32_bf16 v[212:215], a[52:55], a[124:127], v[212:215]\n"
"ds_read_b128 a[124:127], v5 offset:7168\n"
"v_lshlrev_b32 v12, 16, v18\n"
"v_mfma_f32_16x16x32_bf16 v[212:215], a[48:51], a[120:123], v[212:215]\n"
"ds_read_b128 a[120:123], v5 offset:6144\n"
"v_and_b32 v13, s63, v18\n"
"v_mfma_f32_16x16x32_bf16 v[216:219], a[48:51], a[128:131], v[216:219]\n"
"v_lshlrev_b32 v14, 16, v19\n"
"v_and_b32 v15, s63, v19\n"
"v_mfma_f32_16x16x32_bf16 v[216:219], a[52:55], a[132:135], v[216:219]\n"
"global_load_dwordx4 a[40:43], v0, s[34:35] offset:2048 sc1\n"
"v_mul_f32 v12, v12, v76\n"
"v_mfma_f32_16x16x32_bf16 v[244:247], a[60:63], a[132:135], v[244:247]\n"
"s_add_u32 s96, s36, 1560576\n"
"ds_read_b128 a[132:135], v5 offset:9216\n"
"v_mul_f32 v13, v13, v77\n"
"v_mfma_f32_16x16x32_bf16 v[244:247], a[56:59], a[128:131], v[244:247]\n"
"ds_read_b128 a[128:131], v5 offset:8192\n"
"v_mul_f32 v14, v14, v78\n"
"v_mfma_f32_16x16x32_bf16 v[248:251], a[56:59], a[136:139], v[248:251]\n"
"global_load_dwordx4 v[100:103], v0, s[96:97] offset:-4096 sc1\n"
"v_mul_f32 v15, v15, v79\n"
"v_mfma_f32_16x16x32_bf16 v[248:251], a[60:63], a[140:143], v[248:251]\n"
"v_cvt_pk_bf16_f32 v26, v12, v13\n"
"v_cvt_pk_bf16_f32 v27, v14, v15\n"
"v_mfma_f32_16x16x32_bf16 v[220:223], a[52:55], a[140:143], v[220:223]\n"
"ds_read_b128 a[140:143], v5 offset:11264\n"
"v_mfma_f32_16x16x32_bf16 v[220:223], a[48:51], a[136:139], v[220:223]\n"
"ds_read_b128 a[136:139], v5 offset:10240\n"
"v_mfma_f32_16x16x32_bf16 v[224:227], a[48:51], a[144:147], v[224:227]\n"
"ds_write_b64 v7, v[26:27] offset:32776\n"
"v_mfma_f32_16x16x32_bf16 v[224:227], a[52:55], a[148:151], v[224:227]\n"
"global_load_dwordx4 a[44:47], v0, s[34:35] offset:3072 sc1\n"
"v_accvgpr_read_b32 v16, a164\n"
"v_mfma_f32_16x16x32_bf16 v[252:255], a[60:63], a[148:151], v[252:255]\n"
"ds_read_b128 a[148:151], v5 offset:13312\n"
"v_accvgpr_read_b32 v17, a165\n"
"v_mfma_f32_16x16x32_bf16 v[252:255], a[56:59], a[144:147], v[252:255]\n"
"ds_read_b128 a[144:147], v5 offset:12288\n"
"v_accvgpr_read_b32 v18, a166\n"
"v_mfma_f32_16x16x32_bf16 a[28:31], a[56:59], a[152:155], a[28:31]\n"
"v_accvgpr_read_b32 v19, a167\n"
"v_lshlrev_b32 v12, 16, v16\n"
"v_mfma_f32_16x16x32_bf16 a[28:31], a[60:63], a[156:159], a[28:31]\n"
"global_load_dwordx4 v[128:131], v0, s[96:97] offset:-3072 sc1\n"
"v_and_b32 v13, s63, v16\n"
"v_mfma_f32_16x16x32_bf16 a[24:27], a[52:55], a[156:159], a[24:27]\n"
"ds_read_b128 a[156:159], v5 offset:15360\n"
"v_lshlrev_b32 v14, 16, v17\n"
"v_mfma_f32_16x16x32_bf16 a[24:27], a[48:51], a[152:155], a[24:27]\n"
"s_waitcnt vmcnt(15) lgkmcnt(13)\n"
"ds_read_b128 a[152:155], v5 offset:14336\n"
"v_and_b32 v15, s63, v17\n"
"v_mfma_f32_16x16x32_bf16 v[144:147], a[64:67], a[96:99], v[144:147]\n"
"s_add_u32 s34, s36, 1544192\n"
"v_mul_f32 v12, v12, v104\n"
"v_mul_f32 v13, v13, v105\n"
"v_mfma_f32_16x16x32_bf16 v[144:147], a[68:71], a[100:103], v[144:147]\n"
"global_load_dwordx4 a[48:51], v0, s[34:35] offset:0 sc1\n"
"v_mul_f32 v14, v14, v106\n"
"v_mfma_f32_16x16x32_bf16 v[172:175], a[76:79], a[100:103], v[172:175]\n"
"v_mul_f32 v15, v15, v107\n"
"v_cvt_pk_bf16_f32 v28, v12, v13\n"
"v_mfma_f32_16x16x32_bf16 v[172:175], a[72:75], a[96:99], v[172:175]\n"
"v_cvt_pk_bf16_f32 v29, v14, v15\n"
"v_mfma_f32_16x16x32_bf16 v[176:179], a[72:75], a[104:107], v[176:179]\n"
"ds_write_b64 v8, v[28:29] offset:32768\n"
"v_mfma_f32_16x16x32_bf16 v[176:179], a[76:79], a[108:111], v[176:179]\n"
"v_lshlrev_b32 v12, 16, v18\n"
"v_and_b32 v13, s63, v18\n"
"v_mfma_f32_16x16x32_bf16 v[148:151], a[68:71], a[108:111], v[148:151]\n"
"v_lshlrev_b32 v14, 16, v19\n"
"v_and_b32 v15, s63, v19\n"
"v_mfma_f32_16x16x32_bf16 v[148:151], a[64:67], a[104:107], v[148:151]\n"
"s_waitcnt lgkmcnt(10)\n"
"v_mul_f32 v12, v12, v132\n"
"v_mul_f32 v13, v13, v133\n"
"v_mfma_f32_16x16x32_bf16 v[152:155], a[64:67], a[112:115], v[152:155]\n"
"buffer_store_dwordx4 v[68:71], v6, s[52:55], s85 offen offset:1024 sc1\n"
"global_load_dwordx4 v[36:39], v0, s[96:97] offset:-2048 sc1\n"
"v_mul_f32 v14, v14, v134\n"
"v_mul_f32 v15, v15, v135\n"
"v_mfma_f32_16x16x32_bf16 v[152:155], a[68:71], a[116:119], v[152:155]\n"
"global_load_dwordx4 a[52:55], v0, s[34:35] offset:1024 sc1\n"
"v_cvt_pk_bf16_f32 v30, v12, v13\n"
"v_mfma_f32_16x16x32_bf16 v[180:183], a[76:79], a[116:119], v[180:183]\n"
"v_cvt_pk_bf16_f32 v31, v14, v15\n"
"v_mfma_f32_16x16x32_bf16 v[180:183], a[72:75], a[112:115], v[180:183]\n"
"ds_write_b64 v8, v[30:31] offset:32776\n"
"v_mfma_f32_16x16x32_bf16 v[184:187], a[72:75], a[120:123], v[184:187]\n"
"ds_read_b128 v[48:51], v2 offset:32768\n"
"v_mfma_f32_16x16x32_bf16 v[184:187], a[76:79], a[124:127], v[184:187]\n"
"ds_read_b128 v[76:79], v2 offset:33792\n"
"v_accvgpr_read_b32 v16, a168\n"
"v_mfma_f32_16x16x32_bf16 v[156:159], a[68:71], a[124:127], v[156:159]\n"
"v_accvgpr_read_b32 v17, a169\n"
"v_accvgpr_read_b32 v18, a170\n"
"v_mfma_f32_16x16x32_bf16 v[156:159], a[64:67], a[120:123], v[156:159]\n"
"s_waitcnt lgkmcnt(9)\n"
"v_accvgpr_read_b32 v19, a171\n"
"v_lshlrev_b32 v12, 16, v16\n"
"v_mfma_f32_16x16x32_bf16 v[160:163], a[64:67], a[128:131], v[160:163]\n"
"v_and_b32 v13, s63, v16\n"
"v_lshlrev_b32 v14, 16, v17\n"
"v_mfma_f32_16x16x32_bf16 v[160:163], a[68:71], a[132:135], v[160:163]\n"
"global_load_dwordx4 a[56:59], v0, s[34:35] offset:2048 sc1\n"
"v_and_b32 v15, s63, v17\n"
"v_mfma_f32_16x16x32_bf16 v[188:191], a[76:79], a[132:135], v[188:191]\n"
"global_load_dwordx4 v[64:67], v0, s[96:97] offset:-1024 sc1\n"
"v_mul_f32 v12, v12, v52\n"
"v_mfma_f32_16x16x32_bf16 v[188:191], a[72:75], a[128:131], v[188:191]\n"
"v_mul_f32 v13, v13, v53\n"
"v_mul_f32 v14, v14, v54\n"
"v_mfma_f32_16x16x32_bf16 v[192:195], a[72:75], a[136:139], v[192:195]\n"
"v_mul_f32 v15, v15, v55\n"
"v_mfma_f32_16x16x32_bf16 v[192:195], a[76:79], a[140:143], v[192:195]\n"
"v_cvt_pk_bf16_f32 v24, v12, v13\n"
"v_cvt_pk_bf16_f32 v25, v14, v15\n"
"v_mfma_f32_16x16x32_bf16 v[164:167], a[68:71], a[140:143], v[164:167]\n"
"ds_write_b64 v7, v[24:25] offset:34816\n"
"v_mfma_f32_16x16x32_bf16 v[164:167], a[64:67], a[136:139], v[164:167]\n"
"s_waitcnt lgkmcnt(5)\n"
"v_lshlrev_b32 v12, 16, v18\n"
"v_and_b32 v13, s63, v18\n"
"v_mfma_f32_16x16x32_bf16 v[168:171], a[64:67], a[144:147], v[168:171]\n"
"v_lshlrev_b32 v14, 16, v19\n"
"v_and_b32 v15, s63, v19\n"
"v_mfma_f32_16x16x32_bf16 v[168:171], a[68:71], a[148:151], v[168:171]\n"
"global_load_dwordx4 a[60:63], v0, s[34:35] offset:3072 sc1\n"
"v_mul_f32 v12, v12, v80\n"
"v_mfma_f32_16x16x32_bf16 v[196:199], a[76:79], a[148:151], v[196:199]\n"
"v_mul_f32 v13, v13, v81\n"
"v_mul_f32 v14, v14, v82\n"
"v_mfma_f32_16x16x32_bf16 v[196:199], a[72:75], a[144:147], v[196:199]\n"
"v_mul_f32 v15, v15, v83\n"
"v_cvt_pk_bf16_f32 v26, v12, v13\n"
"v_mfma_f32_16x16x32_bf16 a[20:23], a[72:75], a[152:155], a[20:23]\n"
"global_load_dwordx4 v[104:107], v0, s[96:97] offset:0 sc1\n"
"v_cvt_pk_bf16_f32 v27, v14, v15\n"
"v_mfma_f32_16x16x32_bf16 a[20:23], a[76:79], a[156:159], a[20:23]\n"
"ds_write_b64 v7, v[26:27] offset:34824\n"
"v_mfma_f32_16x16x32_bf16 a[16:19], a[68:71], a[156:159], a[16:19]\n"
"v_accvgpr_read_b32 v16, a172\n"
"v_mfma_f32_16x16x32_bf16 a[16:19], a[64:67], a[152:155], a[16:19]\n"
"s_waitcnt vmcnt(15)\n"
"v_accvgpr_read_b32 v17, a173\n"
"v_accvgpr_read_b32 v18, a174\n"
"v_mfma_f32_16x16x32_bf16 v[200:203], a[80:83], a[96:99], v[200:203]\n"
"s_add_u32 s34, s36, 1564672\n"
"v_accvgpr_read_b32 v19, a175\n"
"v_lshlrev_b32 v12, 16, v16\n"
"v_mfma_f32_16x16x32_bf16 v[200:203], a[84:87], a[100:103], v[200:203]\n"
"global_load_dwordx4 a[64:67], v0, s[34:35] offset:0 sc1\n"
"v_and_b32 v13, s63, v16\n"
"v_mfma_f32_16x16x32_bf16 v[228:231], a[92:95], a[100:103], v[228:231]\n"
"ds_read_b128 a[100:103], v2 offset:17408\n"
"v_lshlrev_b32 v14, 16, v17\n"
"v_mfma_f32_16x16x32_bf16 v[228:231], a[88:91], a[96:99], v[228:231]\n"
"ds_read_b128 a[96:99], v2 offset:16384\n"
"v_and_b32 v15, s63, v17\n"
"v_mfma_f32_16x16x32_bf16 v[232:235], a[88:91], a[104:107], v[232:235]\n"
"v_mul_f32 v12, v12, v108\n"
"v_mul_f32 v13, v13, v109\n"
"v_mfma_f32_16x16x32_bf16 v[232:235], a[92:95], a[108:111], v[232:235]\n"
"buffer_store_dwordx4 v[44:47], v6, s[52:55], s86 offen offset:2048 sc1\n"
"global_load_dwordx4 v[132:135], v0, s[96:97] offset:1024 sc1\n"
"v_mul_f32 v14, v14, v110\n"
"v_mul_f32 v15, v15, v111\n"
"v_mfma_f32_16x16x32_bf16 v[204:207], a[84:87], a[108:111], v[204:207]\n"
"ds_read_b128 a[108:111], v2 offset:19456\n"
"v_cvt_pk_bf16_f32 v28, v12, v13\n"
"v_mfma_f32_16x16x32_bf16 v[204:207], a[80:83], a[104:107], v[204:207]\n"
"ds_read_b128 a[104:107], v2 offset:18432\n"
"v_cvt_pk_bf16_f32 v29, v14, v15\n"
"v_mfma_f32_16x16x32_bf16 v[208:211], a[80:83], a[112:115], v[208:211]\n"
"ds_write_b64 v8, v[28:29] offset:34816\n"
"v_mfma_f32_16x16x32_bf16 v[208:211], a[84:87], a[116:119], v[208:211]\n"
"global_load_dwordx4 a[68:71], v0, s[34:35] offset:1024 sc1\n"
"v_mfma_f32_16x16x32_bf16 v[236:239], a[92:95], a[116:119], v[236:239]\n"
"ds_read_b128 a[116:119], v2 offset:21504\n"
"v_mfma_f32_16x16x32_bf16 v[236:239], a[88:91], a[112:115], v[236:239]\n"
"ds_read_b128 a[112:115], v2 offset:20480\n"
"v_lshlrev_b32 v12, 16, v18\n"
"v_mfma_f32_16x16x32_bf16 v[240:243], a[88:91], a[120:123], v[240:243]\n"
"v_and_b32 v13, s63, v18\n"
"v_mfma_f32_16x16x32_bf16 v[240:243], a[92:95], a[124:127], v[240:243]\n"
"v_lshlrev_b32 v14, 16, v19\n"
"v_and_b32 v15, s63, v19\n"
"v_mfma_f32_16x16x32_bf16 v[212:215], a[84:87], a[124:127], v[212:215]\n"
"ds_read_b128 a[124:127], v2 offset:23552\n"
"v_mul_f32 v12, v12, v136\n"
"v_mfma_f32_16x16x32_bf16 v[212:215], a[80:83], a[120:123], v[212:215]\n"
"ds_read_b128 a[120:123], v2 offset:22528\n"
"v_mul_f32 v13, v13, v137\n"
"v_mfma_f32_16x16x32_bf16 v[216:219], a[80:83], a[128:131], v[216:219]\n"
"v_mul_f32 v14, v14, v138\n"
"v_mul_f32 v15, v15, v139\n"
"v_mfma_f32_16x16x32_bf16 v[216:219], a[84:87], a[132:135], v[216:219]\n"
"global_load_dwordx4 a[72:75], v0, s[34:35] offset:2048 sc1\n"
"v_cvt_pk_bf16_f32 v30, v12, v13\n"
"v_mfma_f32_16x16x32_bf16 v[244:247], a[92:95], a[132:135], v[244:247]\n"
"ds_read_b128 a[132:135], v2 offset:25600\n"
"v_cvt_pk_bf16_f32 v31, v14, v15\n"
"v_mfma_f32_16x16x32_bf16 v[244:247], a[88:91], a[128:131], v[244:247]\n"
"ds_read_b128 a[128:131], v2 offset:24576\n"
"v_mfma_f32_16x16x32_bf16 v[248:251], a[88:91], a[136:139], v[248:251]\n"
"ds_write_b64 v8, v[30:31] offset:34824\n"
"v_mfma_f32_16x16x32_bf16 v[248:251], a[92:95], a[140:143], v[248:251]\n"
"ds_read_b128 v[52:55], v2 offset:34816\n"
"v_mfma_f32_16x16x32_bf16 v[220:223], a[84:87], a[140:143], v[220:223]\n"
"ds_read_b128 a[140:143], v2 offset:27648\n"
"v_mfma_f32_16x16x32_bf16 v[220:223], a[80:83], a[136:139], v[220:223]\n"
"ds_read_b128 a[136:139], v2 offset:26624\n"
"v_mfma_f32_16x16x32_bf16 v[224:227], a[80:83], a[144:147], v[224:227]\n"
"ds_read_b128 v[80:83], v2 offset:35840\n"
"v_accvgpr_read_b32 v16, a176\n"
"v_mfma_f32_16x16x32_bf16 v[224:227], a[84:87], a[148:151], v[224:227]\n"
"global_load_dwordx4 a[76:79], v0, s[34:35] offset:3072 sc1\n"
"v_accvgpr_read_b32 v17, a177\n"
"v_mfma_f32_16x16x32_bf16 v[252:255], a[92:95], a[148:151], v[252:255]\n"
"ds_read_b128 a[148:151], v2 offset:29696\n"
"v_accvgpr_read_b32 v18, a178\n"
"v_mfma_f32_16x16x32_bf16 v[252:255], a[88:91], a[144:147], v[252:255]\n"
"ds_read_b128 a[144:147], v2 offset:28672\n"
"v_accvgpr_read_b32 v19, a179\n"
"v_mfma_f32_16x16x32_bf16 a[28:31], a[88:91], a[152:155], a[28:31]\n"
"v_lshlrev_b32 v12, 16, v16\n"
"v_and_b32 v13, s63, v16\n"
"v_mfma_f32_16x16x32_bf16 a[28:31], a[92:95], a[156:159], a[28:31]\n"
"v_lshlrev_b32 v14, 16, v17\n"
"v_and_b32 v15, s63, v17\n"
"v_mfma_f32_16x16x32_bf16 a[24:27], a[84:87], a[156:159], a[24:27]\n"
"ds_read_b128 a[156:159], v2 offset:31744\n"
"v_mul_f32 v12, v12, v56\n"
"v_mfma_f32_16x16x32_bf16 a[24:27], a[80:83], a[152:155], a[24:27]\n"
"s_waitcnt vmcnt(15) lgkmcnt(15)\n"
"ds_read_b128 a[152:155], v2 offset:30720\n"
"v_mul_f32 v13, v13, v57\n"
"v_mfma_f32_16x16x32_bf16 v[144:147], a[32:35], a[96:99], v[144:147]\n"
"buffer_store_dwordx4 v[72:75], v6, s[52:55], s87 offen offset:3072 sc1\n"
"s_add_u32 s34, s36, 1568768\n"
"v_mul_f32 v14, v14, v58\n"
"v_mul_f32 v15, v15, v59\n"
"v_mfma_f32_16x16x32_bf16 v[144:147], a[36:39], a[100:103], v[144:147]\n"
"global_load_dwordx4 a[80:83], v0, s[34:35] offset:0 sc1\n"
"v_cvt_pk_bf16_f32 v24, v12, v13\n"
"v_mfma_f32_16x16x32_bf16 v[172:175], a[44:47], a[100:103], v[172:175]\n"
"v_cvt_pk_bf16_f32 v25, v14, v15\n"
"v_mfma_f32_16x16x32_bf16 v[172:175], a[40:43], a[96:99], v[172:175]\n"
"ds_write_b64 v7, v[24:25] offset:36864\n"
"v_mfma_f32_16x16x32_bf16 v[176:179], a[40:43], a[104:107], v[176:179]\n"
"global_load_dwordx4 a[84:87], v0, s[34:35] offset:1024 sc1\n"
"v_lshlrev_b32 v12, 16, v18\n"
"v_mfma_f32_16x16x32_bf16 v[176:179], a[44:47], a[108:111], v[176:179]\n"
"global_load_dwordx4 v[108:111], v0, s[96:97] offset:2048 sc1\n"
"v_and_b32 v13, s63, v18\n"
"v_mfma_f32_16x16x32_bf16 v[148:151], a[36:39], a[108:111], v[148:151]\n"
"v_lshlrev_b32 v14, 16, v19\n"
"v_and_b32 v15, s63, v19\n"
"v_mfma_f32_16x16x32_bf16 v[148:151], a[32:35], a[104:107], v[148:151]\n"
"s_waitcnt lgkmcnt(12)\n"
"global_load_dwordx4 a[88:91], v0, s[34:35] offset:2048 sc1\n"
"v_mul_f32 v12, v12, v84\n"
"v_mfma_f32_16x16x32_bf16 v[152:155], a[32:35], a[112:115], v[152:155]\n"
"v_mul_f32 v13, v13, v85\n"
"v_mul_f32 v14, v14, v86\n"
"v_mfma_f32_16x16x32_bf16 v[152:155], a[36:39], a[116:119], v[152:155]\n"
"v_mul_f32 v15, v15, v87\n"
"v_cvt_pk_bf16_f32 v26, v12, v13\n"
"v_mfma_f32_16x16x32_bf16 v[180:183], a[44:47], a[116:119], v[180:183]\n"
"global_load_dwordx4 a[92:95], v0, s[34:35] offset:3072 sc1\n"
"v_cvt_pk_bf16_f32 v27, v14, v15\n"
"v_mfma_f32_16x16x32_bf16 v[180:183], a[40:43], a[112:115], v[180:183]\n"
"ds_write_b64 v7, v[26:27] offset:36872\n"
"v_mfma_f32_16x16x32_bf16 v[184:187], a[40:43], a[120:123], v[184:187]\n"
"v_accvgpr_read_b32 v16, a180\n"
"v_accvgpr_read_b32 v17, a181\n"
"v_mfma_f32_16x16x32_bf16 v[184:187], a[44:47], a[124:127], v[184:187]\n"
"v_accvgpr_read_b32 v18, a182\n"
"v_accvgpr_read_b32 v19, a183\n"
"v_mfma_f32_16x16x32_bf16 v[156:159], a[36:39], a[124:127], v[156:159]\n"
"global_load_dwordx4 v[136:139], v0, s[96:97] offset:3072 sc1\n"
"v_lshlrev_b32 v12, 16, v16\n"
"v_mfma_f32_16x16x32_bf16 v[156:159], a[32:35], a[120:123], v[156:159]\n"
"s_waitcnt lgkmcnt(7)\n"
"v_and_b32 v13, s63, v16\n"
"v_lshlrev_b32 v14, 16, v17\n"
"v_mfma_f32_16x16x32_bf16 v[160:163], a[32:35], a[128:131], v[160:163]\n"
"v_and_b32 v15, s63, v17\n"
"v_mul_f32 v12, v12, v112\n"
"v_mfma_f32_16x16x32_bf16 v[160:163], a[36:39], a[132:135], v[160:163]\n"
"v_mul_f32 v13, v13, v113\n"
"v_mul_f32 v14, v14, v114\n"
"v_mfma_f32_16x16x32_bf16 v[188:191], a[44:47], a[132:135], v[188:191]\n"
"v_mul_f32 v15, v15, v115\n"
"v_mfma_f32_16x16x32_bf16 v[188:191], a[40:43], a[128:131], v[188:191]\n"
"v_cvt_pk_bf16_f32 v28, v12, v13\n"
"v_cvt_pk_bf16_f32 v29, v14, v15\n"
"v_mfma_f32_16x16x32_bf16 v[192:195], a[40:43], a[136:139], v[192:195]\n"
"ds_write_b64 v8, v[28:29] offset:36864\n"
"v_mfma_f32_16x16x32_bf16 v[192:195], a[44:47], a[140:143], v[192:195]\n"
"v_lshlrev_b32 v12, 16, v18\n"
"v_mfma_f32_16x16x32_bf16 v[164:167], a[36:39], a[140:143], v[164:167]\n"
"v_and_b32 v13, s63, v18\n"
"v_lshlrev_b32 v14, 16, v19\n"
"v_mfma_f32_16x16x32_bf16 v[164:167], a[32:35], a[136:139], v[164:167]\n"
"s_waitcnt lgkmcnt(3)\n"
"v_and_b32 v15, s63, v19\n"
"v_mfma_f32_16x16x32_bf16 v[168:171], a[32:35], a[144:147], v[168:171]\n"
"v_mul_f32 v12, v12, v140\n"
"v_mfma_f32_16x16x32_bf16 v[168:171], a[36:39], a[148:151], v[168:171]\n"
"v_mul_f32 v13, v13, v141\n"
"v_mfma_f32_16x16x32_bf16 v[196:199], a[44:47], a[148:151], v[196:199]\n"
"v_mul_f32 v14, v14, v142\n"
"v_mul_f32 v15, v15, v143\n"
"v_mfma_f32_16x16x32_bf16 v[196:199], a[40:43], a[144:147], v[196:199]\n"
"v_cvt_pk_bf16_f32 v30, v12, v13\n"
"v_mfma_f32_16x16x32_bf16 a[20:23], a[40:43], a[152:155], a[20:23]\n"
"v_cvt_pk_bf16_f32 v31, v14, v15\n"
"v_mfma_f32_16x16x32_bf16 a[20:23], a[44:47], a[156:159], a[20:23]\n"
"buffer_store_dwordx4 v[48:51], v6, s[52:55], s88 offen offset:0 sc1\n"
"ds_write_b64 v8, v[30:31] offset:36872\n"
"v_mfma_f32_16x16x32_bf16 a[16:19], a[36:39], a[156:159], a[16:19]\n"
"ds_read_b128 v[56:59], v2 offset:36864\n"
"v_mfma_f32_16x16x32_bf16 a[16:19], a[32:35], a[152:155], a[16:19]\n"
"s_waitcnt vmcnt(15)\n"
"ds_read_b128 v[84:87], v2 offset:37888\n"
"v_accvgpr_read_b32 v16, a184\n"
"v_mfma_f32_16x16x32_bf16 v[200:203], a[48:51], a[96:99], v[200:203]\n"
"v_accvgpr_read_b32 v17, a185\n"
"v_accvgpr_read_b32 v18, a186\n"
"v_mfma_f32_16x16x32_bf16 v[200:203], a[52:55], a[100:103], v[200:203]\n"
"v_accvgpr_read_b32 v19, a187\n"
"v_mfma_f32_16x16x32_bf16 v[228:231], a[60:63], a[100:103], v[228:231]\n"
"ds_read_b128 a[100:103], v3 offset:17408\n"
"v_lshlrev_b32 v12, 16, v16\n"
"v_mfma_f32_16x16x32_bf16 v[228:231], a[56:59], a[96:99], v[228:231]\n"
"ds_read_b128 a[96:99], v3 offset:16384\n"
"v_and_b32 v13, s63, v16\n"
"v_mfma_f32_16x16x32_bf16 v[232:235], a[56:59], a[104:107], v[232:235]\n"
"v_lshlrev_b32 v14, 16, v17\n"
"v_and_b32 v15, s63, v17\n"
"v_mfma_f32_16x16x32_bf16 v[232:235], a[60:63], a[108:111], v[232:235]\n"
"v_accvgpr_read_b32 v10, a0\n"
"v_mfma_f32_16x16x32_bf16 v[204:207], a[52:55], a[108:111], v[204:207]\n"
"ds_read_b128 a[108:111], v4 offset:17408\n"
"v_accvgpr_read_b32 v11, a1\n"
"v_mfma_f32_16x16x32_bf16 v[204:207], a[48:51], a[104:107], v[204:207]\n"
"ds_read_b128 a[104:107], v4 offset:16384\n"
"v_mul_f32 v12, v12, v10\n"
"v_mfma_f32_16x16x32_bf16 v[208:211], a[48:51], a[112:115], v[208:211]\n"
"v_mul_f32 v13, v13, v11\n"
"v_accvgpr_read_b32 v10, a2\n"
"v_mfma_f32_16x16x32_bf16 v[208:211], a[52:55], a[116:119], v[208:211]\n"
"v_accvgpr_read_b32 v11, a3\n"
"v_mul_f32 v14, v14, v10\n"
"v_mfma_f32_16x16x32_bf16 v[236:239], a[60:63], a[116:119], v[236:239]\n"
"ds_read_b128 a[116:119], v5 offset:17408\n"
"v_mul_f32 v15, v15, v11\n"
"v_mfma_f32_16x16x32_bf16 v[236:239], a[56:59], a[112:115], v[236:239]\n"
"ds_read_b128 a[112:115], v5 offset:16384\n"
"v_cvt_pk_bf16_f32 v24, v12, v13\n"
"v_mfma_f32_16x16x32_bf16 v[240:243], a[56:59], a[120:123], v[240:243]\n"
"v_cvt_pk_bf16_f32 v25, v14, v15\n"
"v_mfma_f32_16x16x32_bf16 v[240:243], a[60:63], a[124:127], v[240:243]\n"
"ds_write_b64 v7, v[24:25] offset:38912\n"
"v_mfma_f32_16x16x32_bf16 v[212:215], a[52:55], a[124:127], v[212:215]\n"
"ds_read_b128 a[124:127], v3 offset:19456\n"
"v_lshlrev_b32 v12, 16, v18\n"
"v_mfma_f32_16x16x32_bf16 v[212:215], a[48:51], a[120:123], v[212:215]\n"
"ds_read_b128 a[120:123], v3 offset:18432\n"
"v_and_b32 v13, s63, v18\n"
"v_mfma_f32_16x16x32_bf16 v[216:219], a[48:51], a[128:131], v[216:219]\n"
"v_lshlrev_b32 v14, 16, v19\n"
"v_and_b32 v15, s63, v19\n"
"v_mfma_f32_16x16x32_bf16 v[216:219], a[52:55], a[132:135], v[216:219]\n"
"v_accvgpr_read_b32 v10, a4\n"
"v_accvgpr_read_b32 v11, a5\n"
"v_mfma_f32_16x16x32_bf16 v[244:247], a[60:63], a[132:135], v[244:247]\n"
"ds_read_b128 a[132:135], v4 offset:19456\n"
"v_mul_f32 v12, v12, v10\n"
"v_mfma_f32_16x16x32_bf16 v[244:247], a[56:59], a[128:131], v[244:247]\n"
"ds_read_b128 a[128:131], v4 offset:18432\n"
"v_mul_f32 v13, v13, v11\n"
"v_mfma_f32_16x16x32_bf16 v[248:251], a[56:59], a[136:139], v[248:251]\n"
"v_accvgpr_read_b32 v10, a6\n"
"v_mfma_f32_16x16x32_bf16 v[248:251], a[60:63], a[140:143], v[248:251]\n"
"v_accvgpr_read_b32 v11, a7\n"
"v_mul_f32 v14, v14, v10\n"
"v_mfma_f32_16x16x32_bf16 v[220:223], a[52:55], a[140:143], v[220:223]\n"
"ds_read_b128 a[140:143], v5 offset:19456\n"
"v_mul_f32 v15, v15, v11\n"
"v_mfma_f32_16x16x32_bf16 v[220:223], a[48:51], a[136:139], v[220:223]\n"
"ds_read_b128 a[136:139], v5 offset:18432\n"
"v_cvt_pk_bf16_f32 v26, v12, v13\n"
"v_mfma_f32_16x16x32_bf16 v[224:227], a[48:51], a[144:147], v[224:227]\n"
"v_cvt_pk_bf16_f32 v27, v14, v15\n"
"v_mfma_f32_16x16x32_bf16 v[224:227], a[52:55], a[148:151], v[224:227]\n"
"buffer_store_dwordx4 v[76:79], v6, s[52:55], s89 offen offset:1024 sc1\n"
"ds_write_b64 v7, v[26:27] offset:38920\n"
"v_mfma_f32_16x16x32_bf16 v[252:255], a[60:63], a[148:151], v[252:255]\n"
"ds_read_b128 a[148:151], v3 offset:21504\n"
"v_accvgpr_read_b32 v16, a188\n"
"v_mfma_f32_16x16x32_bf16 v[252:255], a[56:59], a[144:147], v[252:255]\n"
"ds_read_b128 a[144:147], v3 offset:20480\n"
"v_accvgpr_read_b32 v17, a189\n"
"v_mfma_f32_16x16x32_bf16 a[28:31], a[56:59], a[152:155], a[28:31]\n"
"v_accvgpr_read_b32 v18, a190\n"
"v_accvgpr_read_b32 v19, a191\n"
"v_mfma_f32_16x16x32_bf16 a[28:31], a[60:63], a[156:159], a[28:31]\n"
"v_lshlrev_b32 v12, 16, v16\n"
"v_and_b32 v13, s63, v16\n"
"v_mfma_f32_16x16x32_bf16 a[24:27], a[52:55], a[156:159], a[24:27]\n"
"ds_read_b128 a[156:159], v4 offset:21504\n"
"v_lshlrev_b32 v14, 16, v17\n"
"v_mfma_f32_16x16x32_bf16 a[24:27], a[48:51], a[152:155], a[24:27]\n"
"s_waitcnt lgkmcnt(15)\n"
"ds_read_b128 a[152:155], v4 offset:20480\n"
"v_and_b32 v15, s63, v17\n"
"v_mfma_f32_16x16x32_bf16 v[144:147], v[88:91], a[96:99], v[144:147]\n"
"v_accvgpr_read_b32 v10, a8\n"
"v_accvgpr_read_b32 v11, a9\n"
"v_mfma_f32_16x16x32_bf16 v[144:147], v[116:119], a[100:103], v[144:147]\n"
"v_mul_f32 v12, v12, v10\n"
"v_mul_f32 v13, v13, v11\n"
"v_mfma_f32_16x16x32_bf16 v[172:175], v[120:123], a[100:103], v[172:175]\n"
"v_accvgpr_read_b32 v10, a10\n"
"v_accvgpr_read_b32 v11, a11\n"
"v_mfma_f32_16x16x32_bf16 v[172:175], v[92:95], a[96:99], v[172:175]\n"
"buffer_store_dwordx4 v[52:55], v6, s[52:55], s90 offen offset:2048 sc1\n"
"v_mul_f32 v14, v14, v10\n"
"v_mul_f32 v15, v15, v11\n"
"v_cvt_pk_bf16_f32 v28, v12, v13\n"
"v_cvt_pk_bf16_f32 v29, v14, v15\n"
"v_mfma_f32_16x16x32_bf16 v[200:203], v[32:35], a[96:99], v[200:203]\n"
"ds_write_b64 v8, v[28:29] offset:38912\n"
"v_mfma_f32_16x16x32_bf16 v[200:203], v[60:63], a[100:103], v[200:203]\n"
"v_lshlrev_b32 v12, 16, v18\n"
"v_and_b32 v13, s63, v18\n"
"v_mfma_f32_16x16x32_bf16 v[228:231], v[124:127], a[100:103], v[228:231]\n"
"ds_read_b128 a[100:103], v5 offset:21504\n"
"v_lshlrev_b32 v14, 16, v19\n"
"v_mfma_f32_16x16x32_bf16 v[228:231], v[96:99], a[96:99], v[228:231]\n"
"s_waitcnt vmcnt(3) lgkmcnt(15)\n"
"ds_read_b128 a[96:99], v5 offset:20480\n"
"v_and_b32 v15, s63, v19\n"
"v_mfma_f32_16x16x32_bf16 v[228:231], v[108:111], a[104:107], v[228:231]\n"
"v_accvgpr_read_b32 v10, a12\n"
"v_accvgpr_read_b32 v11, a13\n"
"v_mfma_f32_16x16x32_bf16 v[228:231], v[136:139], a[108:111], v[228:231]\n"
"v_mul_f32 v12, v12, v10\n"
"v_mul_f32 v13, v13, v11\n"
"v_mfma_f32_16x16x32_bf16 v[200:203], v[132:135], a[108:111], v[200:203]\n"
"v_accvgpr_read_b32 v10, a14\n"
"v_accvgpr_read_b32 v11, a15\n"
"v_mfma_f32_16x16x32_bf16 v[200:203], v[104:107], a[104:107], v[200:203]\n"
"v_mul_f32 v14, v14, v10\n"
"v_mul_f32 v15, v15, v11\n"
"v_mfma_f32_16x16x32_bf16 v[172:175], v[36:39], a[104:107], v[172:175]\n"
"v_cvt_pk_bf16_f32 v30, v12, v13\n"
"v_cvt_pk_bf16_f32 v31, v14, v15\n"
"v_mfma_f32_16x16x32_bf16 v[172:175], v[64:67], a[108:111], v[172:175]\n"
"buffer_store_dwordx4 v[80:83], v6, s[52:55], s91 offen offset:3072 sc1\n"
"ds_write_b64 v8, v[30:31] offset:38920\n"
"v_mfma_f32_16x16x32_bf16 v[144:147], v[128:131], a[108:111], v[144:147]\n"
"ds_read_b128 a[108:111], v3 offset:23552\n"
"v_mfma_f32_16x16x32_bf16 v[144:147], v[100:103], a[104:107], v[144:147]\n"
"s_waitcnt lgkmcnt(15)\n"
"ds_read_b128 a[104:107], v3 offset:22528\n"
"v_mfma_f32_16x16x32_bf16 v[144:147], a[64:67], a[112:115], v[144:147]\n"
"v_mfma_f32_16x16x32_bf16 v[144:147], a[68:71], a[116:119], v[144:147]\n"
"v_mfma_f32_16x16x32_bf16 v[172:175], a[76:79], a[116:119], v[172:175]\n"
"v_mfma_f32_16x16x32_bf16 v[172:175], a[72:75], a[112:115], v[172:175]\n"
"ds_read_b128 v[112:115], v2 offset:38912\n"
"v_mfma_f32_16x16x32_bf16 v[200:203], a[80:83], a[112:115], v[200:203]\n"
"ds_read_b128 v[140:143], v2 offset:39936\n"
"v_mfma_f32_16x16x32_bf16 v[200:203], a[84:87], a[116:119], v[200:203]\n"
"v_accvgpr_read_b32 v16, a192\n"
"v_accvgpr_read_b32 v17, a193\n"
"v_mfma_f32_16x16x32_bf16 v[228:231], a[92:95], a[116:119], v[228:231]\n"
"ds_read_b128 a[116:119], v4 offset:23552\n"
"v_accvgpr_read_b32 v18, a194\n"
"v_mfma_f32_16x16x32_bf16 v[228:231], a[88:91], a[112:115], v[228:231]\n"
"s_waitcnt lgkmcnt(15)\n"
"buffer_store_dwordx4 v[56:59], v6, s[52:55], s92 offen offset:0 sc1\n"
"ds_read_b128 a[112:115], v4 offset:22528\n"
"v_accvgpr_read_b32 v19, a195\n"
"v_lshlrev_b32 v12, 16, v16\n"
"v_mfma_f32_16x16x32_bf16 v[232:235], v[96:99], a[120:123], v[232:235]\n"
"v_and_b32 v13, s63, v16\n"
"v_mfma_f32_16x16x32_bf16 v[232:235], v[124:127], a[124:127], v[232:235]\n"
"v_lshlrev_b32 v14, 16, v17\n"
"v_and_b32 v15, s63, v17\n"
"v_mfma_f32_16x16x32_bf16 v[204:207], v[60:63], a[124:127], v[204:207]\n"
"v_mul_f32 v12, v12, v144\n"
"v_mul_f32 v13, v13, v145\n"
"v_mfma_f32_16x16x32_bf16 v[204:207], v[32:35], a[120:123], v[204:207]\n"
"v_mul_f32 v14, v14, v146\n"
"v_mul_f32 v15, v15, v147\n"
"v_mfma_f32_16x16x32_bf16 v[176:179], v[92:95], a[120:123], v[176:179]\n"
"v_cvt_pk_bf16_f32 v24, v12, v13\n"
"v_cvt_pk_bf16_f32 v25, v14, v15\n"
"v_mfma_f32_16x16x32_bf16 v[176:179], v[120:123], a[124:127], v[176:179]\n"
"ds_write_b64 v7, v[24:25] offset:32768\n"
"v_mfma_f32_16x16x32_bf16 v[148:151], v[116:119], a[124:127], v[148:151]\n"
"ds_read_b128 a[124:127], v5 offset:23552\n"
"v_lshlrev_b32 v12, 16, v18\n"
"v_mfma_f32_16x16x32_bf16 v[148:151], v[88:91], a[120:123], v[148:151]\n"
"ds_read_b128 a[120:123], v5 offset:22528\n"
"v_and_b32 v13, s63, v18\n"
"v_mfma_f32_16x16x32_bf16 v[148:151], v[100:103], a[128:131], v[148:151]\n"
"v_lshlrev_b32 v14, 16, v19\n"
"v_mfma_f32_16x16x32_bf16 v[148:151], v[128:131], a[132:135], v[148:151]\n"
"buffer_store_dwordx4 v[84:87], v6, s[52:55], s93 offen offset:1024 sc1\n"
"v_and_b32 v15, s63, v19\n"
"v_mul_f32 v12, v12, v172\n"
"v_mul_f32 v13, v13, v173\n"
"v_mfma_f32_16x16x32_bf16 v[176:179], v[64:67], a[132:135], v[176:179]\n"
"v_mul_f32 v14, v14, v174\n"
"v_mul_f32 v15, v15, v175\n"
"v_mfma_f32_16x16x32_bf16 v[176:179], v[36:39], a[128:131], v[176:179]\n"
"v_cvt_pk_bf16_f32 v26, v12, v13\n"
"v_cvt_pk_bf16_f32 v27, v14, v15\n"
"v_mfma_f32_16x16x32_bf16 v[204:207], v[104:107], a[128:131], v[204:207]\n"
"ds_write_b64 v7, v[26:27] offset:32776\n"
"v_mfma_f32_16x16x32_bf16 v[204:207], v[132:135], a[132:135], v[204:207]\n"
"v_accvgpr_read_b32 v16, a196\n"
"v_mfma_f32_16x16x32_bf16 v[232:235], v[136:139], a[132:135], v[232:235]\n"
"ds_read_b128 a[132:135], v3 offset:25600\n"
"v_accvgpr_read_b32 v17, a197\n"
"v_mfma_f32_16x16x32_bf16 v[232:235], v[108:111], a[128:131], v[232:235]\n"
"s_waitcnt lgkmcnt(15)\n"
"ds_read_b128 a[128:131], v3 offset:24576\n"
"v_accvgpr_read_b32 v18, a198\n"
"v_mfma_f32_16x16x32_bf16 v[232:235], a[88:91], a[136:139], v[232:235]\n"
"v_accvgpr_read_b32 v19, a199\n"
"v_lshlrev_b32 v12, 16, v16\n"
"v_mfma_f32_16x16x32_bf16 v[232:235], a[92:95], a[140:143], v[232:235]\n"
"v_and_b32 v13, s63, v16\n"
"v_lshlrev_b32 v14, 16, v17\n"
"v_mfma_f32_16x16x32_bf16 v[204:207], a[84:87], a[140:143], v[204:207]\n"
"v_and_b32 v15, s63, v17\n"
"v_mul_f32 v12, v12, v200\n"
"v_mfma_f32_16x16x32_bf16 v[204:207], a[80:83], a[136:139], v[204:207]\n"
"s_waitcnt lgkmcnt(9)\n"
"buffer_store_dwordx4 v[112:115], v6, s[52:55], s94 offen offset:2048 sc1\n"
"v_mul_f32 v13, v13, v201\n"
"v_mul_f32 v14, v14, v202\n"
"v_mul_f32 v15, v15, v203\n"
"v_mfma_f32_16x16x32_bf16 v[176:179], a[72:75], a[136:139], v[176:179]\n"
"v_cvt_pk_bf16_f32 v28, v12, v13\n"
"v_cvt_pk_bf16_f32 v29, v14, v15\n"
"v_mfma_f32_16x16x32_bf16 v[176:179], a[76:79], a[140:143], v[176:179]\n"
"ds_write_b64 v8, v[28:29] offset:32768\n"
"v_mfma_f32_16x16x32_bf16 v[148:151], a[68:71], a[140:143], v[148:151]\n"
"ds_read_b128 a[140:143], v4 offset:25600\n"
"v_mfma_f32_16x16x32_bf16 v[148:151], a[64:67], a[136:139], v[148:151]\n"
"ds_read_b128 a[136:139], v4 offset:24576\n"
"v_lshlrev_b32 v12, 16, v18\n"
"v_mfma_f32_16x16x32_bf16 v[152:155], v[88:91], a[144:147], v[152:155]\n"
"v_and_b32 v13, s63, v18\n"
"v_lshlrev_b32 v14, 16, v19\n"
"v_mfma_f32_16x16x32_bf16 v[152:155], v[116:119], a[148:151], v[152:155]\n"
"v_and_b32 v15, s63, v19\n"
"v_mul_f32 v12, v12, v228\n"
"v_mfma_f32_16x16x32_bf16 v[180:183], v[120:123], a[148:151], v[180:183]\n"
"v_mul_f32 v13, v13, v229\n"
"v_mfma_f32_16x16x32_bf16 v[180:183], v[92:95], a[144:147], v[180:183]\n"
"v_mul_f32 v14, v14, v230\n"
"v_mul_f32 v15, v15, v231\n"
"v_mfma_f32_16x16x32_bf16 v[208:211], v[32:35], a[144:147], v[208:211]\n"
"v_cvt_pk_bf16_f32 v30, v12, v13\n"
"v_cvt_pk_bf16_f32 v31, v14, v15\n"
"v_mfma_f32_16x16x32_bf16 v[208:211], v[60:63], a[148:151], v[208:211]\n"
"s_waitcnt lgkmcnt(11)\n"
"buffer_store_dwordx4 v[140:143], v6, s[52:55], s95 offen offset:3072 sc1\n"
"ds_write_b64 v8, v[30:31] offset:32776\n"
"v_mfma_f32_16x16x32_bf16 v[236:239], v[124:127], a[148:151], v[236:239]\n"
"ds_read_b128 a[148:151], v5 offset:25600\n"
"v_mfma_f32_16x16x32_bf16 v[236:239], v[96:99], a[144:147], v[236:239]\n"
"ds_read_b128 a[144:147], v5 offset:24576\n"
"v_mfma_f32_16x16x32_bf16 v[236:239], v[108:111], a[152:155], v[236:239]\n"
"ds_read_b128 v[144:147], v2 offset:32768\n"
"v_mfma_f32_16x16x32_bf16 v[236:239], v[136:139], a[156:159], v[236:239]\n"
"ds_read_b128 v[172:175], v2 offset:33792\n"
"v_accvgpr_read_b32 v16, a200\n"
"v_mfma_f32_16x16x32_bf16 v[208:211], v[132:135], a[156:159], v[208:211]\n"
"v_accvgpr_read_b32 v17, a201\n"
"v_accvgpr_read_b32 v18, a202\n"
"v_mfma_f32_16x16x32_bf16 v[208:211], v[104:107], a[152:155], v[208:211]\n"
"v_accvgpr_read_b32 v19, a203\n"
"v_lshlrev_b32 v12, 16, v16\n"
"v_mfma_f32_16x16x32_bf16 v[180:183], v[36:39], a[152:155], v[180:183]\n"
"v_and_b32 v13, s63, v16\n"
"v_lshlrev_b32 v14, 16, v17\n"
"v_mfma_f32_16x16x32_bf16 v[180:183], v[64:67], a[156:159], v[180:183]\n"
"v_and_b32 v15, s63, v17\n"
"v_mul_f32 v12, v12, v148\n"
"v_mfma_f32_16x16x32_bf16 v[152:155], v[128:131], a[156:159], v[152:155]\n"
"ds_read_b128 a[156:159], v3 offset:27648\n"
"v_mul_f32 v13, v13, v149\n"
"v_mfma_f32_16x16x32_bf16 v[152:155], v[100:103], a[152:155], v[152:155]\n"
"ds_read_b128 a[152:155], v3 offset:26624\n"
"v_mul_f32 v14, v14, v150\n"
"v_mfma_f32_16x16x32_bf16 v[152:155], a[64:67], a[96:99], v[152:155]\n"
"v_mul_f32 v15, v15, v151\n"
"v_cvt_pk_bf16_f32 v24, v12, v13\n"
"v_mfma_f32_16x16x32_bf16 v[152:155], a[68:71], a[100:103], v[152:155]\n"
"v_cvt_pk_bf16_f32 v25, v14, v15\n"
"v_mfma_f32_16x16x32_bf16 v[180:183], a[76:79], a[100:103], v[180:183]\n"
"s_waitcnt lgkmcnt(3)\n"
"buffer_store_dwordx4 v[144:147], v6, s[52:55], s80 offen offset:128 sc1\n"
"ds_write_b64 v7, v[24:25] offset:34816\n"
"v_mfma_f32_16x16x32_bf16 v[180:183], a[72:75], a[96:99], v[180:183]\n"
"v_lshlrev_b32 v12, 16, v18\n"
"v_and_b32 v13, s63, v18\n"
"v_mfma_f32_16x16x32_bf16 v[208:211], a[80:83], a[96:99], v[208:211]\n"
"v_lshlrev_b32 v14, 16, v19\n"
"v_and_b32 v15, s63, v19\n"
"v_mfma_f32_16x16x32_bf16 v[208:211], a[84:87], a[100:103], v[208:211]\n"
"v_mul_f32 v12, v12, v176\n"
"v_mul_f32 v13, v13, v177\n"
"v_mfma_f32_16x16x32_bf16 v[236:239], a[92:95], a[100:103], v[236:239]\n"
"ds_read_b128 a[100:103], v4 offset:27648\n"
"v_mul_f32 v14, v14, v178\n"
"v_mfma_f32_16x16x32_bf16 v[236:239], a[88:91], a[96:99], v[236:239]\n"
"ds_read_b128 a[96:99], v4 offset:26624\n"
"v_mul_f32 v15, v15, v179\n"
"v_mfma_f32_16x16x32_bf16 v[240:243], v[96:99], a[104:107], v[240:243]\n"
"v_cvt_pk_bf16_f32 v26, v12, v13\n"
"v_cvt_pk_bf16_f32 v27, v14, v15\n"
"v_mfma_f32_16x16x32_bf16 v[240:243], v[124:127], a[108:111], v[240:243]\n"
"ds_write_b64 v7, v[26:27] offset:34824\n"
"v_mfma_f32_16x16x32_bf16 v[212:215], v[60:63], a[108:111], v[212:215]\n"
"v_accvgpr_read_b32 v16, a204\n"
"v_accvgpr_read_b32 v17, a205\n"
"v_mfma_f32_16x16x32_bf16 v[212:215], v[32:35], a[104:107], v[212:215]\n"
"v_accvgpr_read_b32 v18, a206\n"
"v_accvgpr_read_b32 v19, a207\n"
"v_mfma_f32_16x16x32_bf16 v[184:187], v[92:95], a[104:107], v[184:187]\n"
"s_waitcnt lgkmcnt(6)\n"
"buffer_store_dwordx4 v[172:175], v6, s[52:55], s81 offen offset:1152 sc1\n"
"v_lshlrev_b32 v12, 16, v16\n"
"v_and_b32 v13, s63, v16\n"
"v_lshlrev_b32 v14, 16, v17\n"
"v_mfma_f32_16x16x32_bf16 v[184:187], v[120:123], a[108:111], v[184:187]\n"
"v_and_b32 v15, s63, v17\n"
"v_mul_f32 v12, v12, v204\n"
"v_mfma_f32_16x16x32_bf16 v[156:159], v[116:119], a[108:111], v[156:159]\n"
"ds_read_b128 a[108:111], v5 offset:27648\n"
"v_mul_f32 v13, v13, v205\n"
"v_mfma_f32_16x16x32_bf16 v[156:159], v[88:91], a[104:107], v[156:159]\n"
"ds_read_b128 a[104:107], v5 offset:26624\n"
"v_mul_f32 v14, v14, v206\n"
"v_mfma_f32_16x16x32_bf16 v[156:159], v[100:103], a[112:115], v[156:159]\n"
"v_mul_f32 v15, v15, v207\n"
"v_cvt_pk_bf16_f32 v28, v12, v13\n"
"v_mfma_f32_16x16x32_bf16 v[156:159], v[128:131], a[116:119], v[156:159]\n"
"v_cvt_pk_bf16_f32 v29, v14, v15\n"
"v_mfma_f32_16x16x32_bf16 v[184:187], v[64:67], a[116:119], v[184:187]\n"
"ds_write_b64 v8, v[28:29] offset:34816\n"
"v_mfma_f32_16x16x32_bf16 v[184:187], v[36:39], a[112:115], v[184:187]\n"
"v_lshlrev_b32 v12, 16, v18\n"
"v_and_b32 v13, s63, v18\n"
"v_mfma_f32_16x16x32_bf16 v[212:215], v[104:107], a[112:115], v[212:215]\n"
"v_lshlrev_b32 v14, 16, v19\n"
"v_and_b32 v15, s63, v19\n"
"v_mfma_f32_16x16x32_bf16 v[212:215], v[132:135], a[116:119], v[212:215]\n"
"v_mul_f32 v12, v12, v232\n"
"v_mul_f32 v13, v13, v233\n"
"v_mfma_f32_16x16x32_bf16 v[240:243], v[136:139], a[116:119], v[240:243]\n"
"ds_read_b128 a[116:119], v3 offset:29696\n"
"v_mul_f32 v14, v14, v234\n"
"v_mfma_f32_16x16x32_bf16 v[240:243], v[108:111], a[112:115], v[240:243]\n"
"ds_read_b128 a[112:115], v3 offset:28672\n"
"v_mul_f32 v15, v15, v235\n"
"v_mfma_f32_16x16x32_bf16 v[240:243], a[88:91], a[120:123], v[240:243]\n"
"v_cvt_pk_bf16_f32 v30, v12, v13\n"
"v_cvt_pk_bf16_f32 v31, v14, v15\n"
"v_mfma_f32_16x16x32_bf16 v[240:243], a[92:95], a[124:127], v[240:243]\n"
"ds_write_b64 v8, v[30:31] offset:34824\n"
"v_mfma_f32_16x16x32_bf16 v[212:215], a[84:87], a[124:127], v[212:215]\n"
"ds_read_b128 v[148:151], v2 offset:34816\n"
"v_mfma_f32_16x16x32_bf16 v[212:215], a[80:83], a[120:123], v[212:215]\n"
"ds_read_b128 v[176:179], v2 offset:35840\n"
"v_accvgpr_read_b32 v16, a208\n"
"v_mfma_f32_16x16x32_bf16 v[184:187], a[72:75], a[120:123], v[184:187]\n"
"v_accvgpr_read_b32 v17, a209\n"
"v_accvgpr_read_b32 v18, a210\n"
"v_mfma_f32_16x16x32_bf16 v[184:187], a[76:79], a[124:127], v[184:187]\n"
"v_accvgpr_read_b32 v19, a211\n"
"v_lshlrev_b32 v12, 16, v16\n"
"v_mfma_f32_16x16x32_bf16 v[156:159], a[68:71], a[124:127], v[156:159]\n"
"ds_read_b128 a[124:127], v4 offset:29696\n"
"v_and_b32 v13, s63, v16\n"
"v_mfma_f32_16x16x32_bf16 v[156:159], a[64:67], a[120:123], v[156:159]\n"
"ds_read_b128 a[120:123], v4 offset:28672\n"
"v_lshlrev_b32 v14, 16, v17\n"
"v_mfma_f32_16x16x32_bf16 v[160:163], v[88:91], a[128:131], v[160:163]\n"
"v_and_b32 v15, s63, v17\n"
"v_mul_f32 v12, v12, v152\n"
"v_mfma_f32_16x16x32_bf16 v[160:163], v[116:119], a[132:135], v[160:163]\n"
"v_mul_f32 v13, v13, v153\n"
"v_mul_f32 v14, v14, v154\n"
"v_mfma_f32_16x16x32_bf16 v[188:191], v[120:123], a[132:135], v[188:191]\n"
"v_mul_f32 v15, v15, v155\n"
"v_cvt_pk_bf16_f32 v24, v12, v13\n"
"v_mfma_f32_16x16x32_bf16 v[188:191], v[92:95], a[128:131], v[188:191]\n"
"v_cvt_pk_bf16_f32 v25, v14, v15\n"
"v_mfma_f32_16x16x32_bf16 v[216:219], v[32:35], a[128:131], v[216:219]\n"
"s_waitcnt lgkmcnt(3)\n"
"buffer_store_dwordx4 v[148:151], v6, s[52:55], s82 offen offset:2176 sc1\n"
"ds_write_b64 v7, v[24:25] offset:36864\n"
"v_mfma_f32_16x16x32_bf16 v[216:219], v[60:63], a[132:135], v[216:219]\n"
"v_lshlrev_b32 v12, 16, v18\n"
"v_and_b32 v13, s63, v18\n"
"v_mfma_f32_16x16x32_bf16 v[244:247], v[124:127], a[132:135], v[244:247]\n"
"ds_read_b128 a[132:135], v5 offset:29696\n"
"v_lshlrev_b32 v14, 16, v19\n"
"v_mfma_f32_16x16x32_bf16 v[244:247], v[96:99], a[128:131], v[244:247]\n"
"ds_read_b128 a[128:131], v5 offset:28672\n"
"v_and_b32 v15, s63, v19\n"
"v_mfma_f32_16x16x32_bf16 v[244:247], v[108:111], a[136:139], v[244:247]\n"
"v_mul_f32 v12, v12, v180\n"
"v_mul_f32 v13, v13, v181\n"
"v_mfma_f32_16x16x32_bf16 v[244:247], v[136:139], a[140:143], v[244:247]\n"
"v_mul_f32 v14, v14, v182\n"
"v_mul_f32 v15, v15, v183\n"
"v_mfma_f32_16x16x32_bf16 v[216:219], v[132:135], a[140:143], v[216:219]\n"
"v_cvt_pk_bf16_f32 v26, v12, v13\n"
"v_cvt_pk_bf16_f32 v27, v14, v15\n"
"v_mfma_f32_16x16x32_bf16 v[216:219], v[104:107], a[136:139], v[216:219]\n"
"ds_write_b64 v7, v[26:27] offset:36872\n"
"v_mfma_f32_16x16x32_bf16 v[188:191], v[36:39], a[136:139], v[188:191]\n"
"v_accvgpr_read_b32 v16, a212\n"
"v_accvgpr_read_b32 v17, a213\n"
"v_mfma_f32_16x16x32_bf16 v[188:191], v[64:67], a[140:143], v[188:191]\n"
"v_accvgpr_read_b32 v18, a214\n"
"v_accvgpr_read_b32 v19, a215\n"
"v_mfma_f32_16x16x32_bf16 v[160:163], v[128:131], a[140:143], v[160:163]\n"
"s_waitcnt lgkmcnt(6)\n"
"buffer_store_dwordx4 v[176:179], v6, s[52:55], s83 offen offset:3200 sc1\n"
"ds_read_b128 a[140:143], v3 offset:31744\n"
"v_lshlrev_b32 v12, 16, v16\n"
"v_and_b32 v13, s63, v16\n"
"v_mfma_f32_16x16x32_bf16 v[160:163], v[100:103], a[136:139], v[160:163]\n"
"ds_read_b128 a[136:139], v3 offset:30720\n"
"v_lshlrev_b32 v14, 16, v17\n"
"v_mfma_f32_16x16x32_bf16 v[160:163], a[64:67], a[144:147], v[160:163]\n"
"v_and_b32 v15, s63, v17\n"
"v_mul_f32 v12, v12, v208\n"
"v_mfma_f32_16x16x32_bf16 v[160:163], a[68:71], a[148:151], v[160:163]\n"
"v_mul_f32 v13, v13, v209\n"
"v_mul_f32 v14, v14, v210\n"
"v_mfma_f32_16x16x32_bf16 v[188:191], a[76:79], a[148:151], v[188:191]\n"
"v_mul_f32 v15, v15, v211\n"
"v_cvt_pk_bf16_f32 v28, v12, v13\n"
"v_mfma_f32_16x16x32_bf16 v[188:191], a[72:75], a[144:147], v[188:191]\n"
"v_cvt_pk_bf16_f32 v29, v14, v15\n"
"v_mfma_f32_16x16x32_bf16 v[216:219], a[80:83], a[144:147], v[216:219]\n"
"ds_write_b64 v8, v[28:29] offset:36864\n"
"v_mfma_f32_16x16x32_bf16 v[216:219], a[84:87], a[148:151], v[216:219]\n"
"v_lshlrev_b32 v12, 16, v18\n"
"v_and_b32 v13, s63, v18\n"
"v_mfma_f32_16x16x32_bf16 v[244:247], a[92:95], a[148:151], v[244:247]\n"
"ds_read_b128 a[148:151], v4 offset:31744\n"
"v_lshlrev_b32 v14, 16, v19\n"
"v_mfma_f32_16x16x32_bf16 v[244:247], a[88:91], a[144:147], v[244:247]\n"
"ds_read_b128 a[144:147], v4 offset:30720\n"
"v_and_b32 v15, s63, v19\n"
"v_mfma_f32_16x16x32_bf16 v[248:251], v[96:99], a[152:155], v[248:251]\n"
"v_mul_f32 v12, v12, v236\n"
"v_mul_f32 v13, v13, v237\n"
"v_mfma_f32_16x16x32_bf16 v[248:251], v[124:127], a[156:159], v[248:251]\n"
"v_mul_f32 v14, v14, v238\n"
"v_mul_f32 v15, v15, v239\n"
"v_mfma_f32_16x16x32_bf16 v[220:223], v[60:63], a[156:159], v[220:223]\n"
"v_cvt_pk_bf16_f32 v30, v12, v13\n"
"v_cvt_pk_bf16_f32 v31, v14, v15\n"
"v_mfma_f32_16x16x32_bf16 v[220:223], v[32:35], a[152:155], v[220:223]\n"
"ds_write_b64 v8, v[30:31] offset:36872\n"
"v_mfma_f32_16x16x32_bf16 v[192:195], v[92:95], a[152:155], v[192:195]\n"
"ds_read_b128 v[152:155], v2 offset:36864\n"
"v_mfma_f32_16x16x32_bf16 v[192:195], v[120:123], a[156:159], v[192:195]\n"
"ds_read_b128 v[180:183], v2 offset:37888\n"
"v_accvgpr_read_b32 v16, a216\n"
"v_mfma_f32_16x16x32_bf16 v[164:167], v[116:119], a[156:159], v[164:167]\n"
"ds_read_b128 a[156:159], v5 offset:31744\n"
"v_accvgpr_read_b32 v17, a217\n"
"v_mfma_f32_16x16x32_bf16 v[164:167], v[88:91], a[152:155], v[164:167]\n"
"ds_read_b128 a[152:155], v5 offset:30720\n"
"v_accvgpr_read_b32 v18, a218\n"
"v_mfma_f32_16x16x32_bf16 v[164:167], v[100:103], a[96:99], v[164:167]\n"
"v_accvgpr_read_b32 v19, a219\n"
"v_lshlrev_b32 v12, 16, v16\n"
"v_mfma_f32_16x16x32_bf16 v[164:167], v[128:131], a[100:103], v[164:167]\n"
"v_and_b32 v13, s63, v16\n"
"v_lshlrev_b32 v14, 16, v17\n"
"v_mfma_f32_16x16x32_bf16 v[192:195], v[64:67], a[100:103], v[192:195]\n"
"v_and_b32 v15, s63, v17\n"
"v_mul_f32 v12, v12, v156\n"
"v_mfma_f32_16x16x32_bf16 v[192:195], v[36:39], a[96:99], v[192:195]\n"
"v_mul_f32 v13, v13, v157\n"
"v_mul_f32 v14, v14, v158\n"
"v_mfma_f32_16x16x32_bf16 v[220:223], v[104:107], a[96:99], v[220:223]\n"
"v_mul_f32 v15, v15, v159\n"
"v_mfma_f32_16x16x32_bf16 v[220:223], v[132:135], a[100:103], v[220:223]\n"
"v_cvt_pk_bf16_f32 v24, v12, v13\n"
"v_mfma_f32_16x16x32_bf16 v[248:251], v[136:139], a[100:103], v[248:251]\n"
"v_cvt_pk_bf16_f32 v25, v14, v15\n"
"v_mfma_f32_16x16x32_bf16 v[248:251], v[108:111], a[96:99], v[248:251]\n"
"s_waitcnt lgkmcnt(3)\n"
"buffer_store_dwordx4 v[152:155], v6, s[52:55], s84 offen offset:128 sc1\n"
"ds_write_b64 v7, v[24:25] offset:38912\n"
"v_mfma_f32_16x16x32_bf16 v[248:251], a[88:91], a[104:107], v[248:251]\n"
"v_lshlrev_b32 v12, 16, v18\n"
"v_and_b32 v13, s63, v18\n"
"v_mfma_f32_16x16x32_bf16 v[248:251], a[92:95], a[108:111], v[248:251]\n"
"v_lshlrev_b32 v14, 16, v19\n"
"v_mfma_f32_16x16x32_bf16 v[220:223], a[84:87], a[108:111], v[220:223]\n"
"v_and_b32 v15, s63, v19\n"
"v_mfma_f32_16x16x32_bf16 v[220:223], a[80:83], a[104:107], v[220:223]\n"
"v_mul_f32 v12, v12, v184\n"
"v_mfma_f32_16x16x32_bf16 v[192:195], a[72:75], a[104:107], v[192:195]\n"
"v_mul_f32 v13, v13, v185\n"
"v_mfma_f32_16x16x32_bf16 v[192:195], a[76:79], a[108:111], v[192:195]\n"
"v_mul_f32 v14, v14, v186\n"
"v_mfma_f32_16x16x32_bf16 v[164:167], a[68:71], a[108:111], v[164:167]\n"
"v_mul_f32 v15, v15, v187\n"
"v_mfma_f32_16x16x32_bf16 v[164:167], a[64:67], a[104:107], v[164:167]\n"
"v_cvt_pk_bf16_f32 v26, v12, v13\n"
"v_mfma_f32_16x16x32_bf16 v[168:171], v[88:91], a[112:115], v[168:171]\n"
"v_cvt_pk_bf16_f32 v27, v14, v15\n"
"v_mfma_f32_16x16x32_bf16 v[168:171], v[116:119], a[116:119], v[168:171]\n"
"s_waitcnt lgkmcnt(3)\n"
"buffer_store_dwordx4 v[180:183], v6, s[52:55], s85 offen offset:1152 sc1\n"
"ds_write_b64 v7, v[26:27] offset:38920\n"
"v_mfma_f32_16x16x32_bf16 v[196:199], v[120:123], a[116:119], v[196:199]\n"
"v_accvgpr_read_b32 v16, a220\n"
"v_accvgpr_read_b32 v17, a221\n"
"v_mfma_f32_16x16x32_bf16 v[196:199], v[92:95], a[112:115], v[196:199]\n"
"v_accvgpr_read_b32 v18, a222\n"
"v_accvgpr_read_b32 v19, a223\n"
"v_mfma_f32_16x16x32_bf16 v[224:227], v[32:35], a[112:115], v[224:227]\n"
"v_lshlrev_b32 v12, 16, v16\n"
"v_and_b32 v13, s63, v16\n"
"v_mfma_f32_16x16x32_bf16 v[224:227], v[60:63], a[116:119], v[224:227]\n"
"v_lshlrev_b32 v14, 16, v17\n"
"v_and_b32 v15, s63, v17\n"
"v_mfma_f32_16x16x32_bf16 v[252:255], v[124:127], a[116:119], v[252:255]\n"
"v_mul_f32 v12, v12, v212\n"
"v_mul_f32 v13, v13, v213\n"
"v_mfma_f32_16x16x32_bf16 v[252:255], v[96:99], a[112:115], v[252:255]\n"
"v_mul_f32 v14, v14, v214\n"
"v_mul_f32 v15, v15, v215\n"
"v_mfma_f32_16x16x32_bf16 v[252:255], v[108:111], a[120:123], v[252:255]\n"
"v_cvt_pk_bf16_f32 v28, v12, v13\n"
"v_cvt_pk_bf16_f32 v29, v14, v15\n"
"v_mfma_f32_16x16x32_bf16 v[252:255], v[136:139], a[124:127], v[252:255]\n"
"ds_write_b64 v8, v[28:29] offset:38912\n"
"v_mfma_f32_16x16x32_bf16 v[224:227], v[132:135], a[124:127], v[224:227]\n"
"v_lshlrev_b32 v12, 16, v18\n"
"v_and_b32 v13, s63, v18\n"
"v_mfma_f32_16x16x32_bf16 v[224:227], v[104:107], a[120:123], v[224:227]\n"
"v_lshlrev_b32 v14, 16, v19\n"
"v_and_b32 v15, s63, v19\n"
"v_mfma_f32_16x16x32_bf16 v[196:199], v[36:39], a[120:123], v[196:199]\n"
"v_mul_f32 v12, v12, v240\n"
"v_mul_f32 v13, v13, v241\n"
"v_mfma_f32_16x16x32_bf16 v[196:199], v[64:67], a[124:127], v[196:199]\n"
"v_mul_f32 v14, v14, v242\n"
"v_mul_f32 v15, v15, v243\n"
"v_mfma_f32_16x16x32_bf16 v[168:171], v[128:131], a[124:127], v[168:171]\n"
"v_cvt_pk_bf16_f32 v30, v12, v13\n"
"v_cvt_pk_bf16_f32 v31, v14, v15\n"
"v_mfma_f32_16x16x32_bf16 v[168:171], v[100:103], a[120:123], v[168:171]\n"
"ds_write_b64 v8, v[30:31] offset:38920\n"
"v_mfma_f32_16x16x32_bf16 v[168:171], a[64:67], a[128:131], v[168:171]\n"
"ds_read_b128 v[156:159], v2 offset:38912\n"
"v_mfma_f32_16x16x32_bf16 v[168:171], a[68:71], a[132:135], v[168:171]\n"
"ds_read_b128 v[184:187], v2 offset:39936\n"
"v_accvgpr_read_b32 v16, a224\n"
"v_mfma_f32_16x16x32_bf16 v[196:199], a[76:79], a[132:135], v[196:199]\n"
"v_accvgpr_read_b32 v17, a225\n"
"v_accvgpr_read_b32 v18, a226\n"
"v_mfma_f32_16x16x32_bf16 v[196:199], a[72:75], a[128:131], v[196:199]\n"
"v_accvgpr_read_b32 v19, a227\n"
"v_lshlrev_b32 v12, 16, v16\n"
"v_mfma_f32_16x16x32_bf16 v[224:227], a[80:83], a[128:131], v[224:227]\n"
"v_and_b32 v13, s63, v16\n"
"v_lshlrev_b32 v14, 16, v17\n"
"v_mfma_f32_16x16x32_bf16 v[224:227], a[84:87], a[132:135], v[224:227]\n"
"v_and_b32 v15, s63, v17\n"
"v_mul_f32 v12, v12, v160\n"
"v_mfma_f32_16x16x32_bf16 v[252:255], a[92:95], a[132:135], v[252:255]\n"
"v_mul_f32 v13, v13, v161\n"
"v_mul_f32 v14, v14, v162\n"
"v_mfma_f32_16x16x32_bf16 v[252:255], a[88:91], a[128:131], v[252:255]\n"
"v_mul_f32 v15, v15, v163\n"
"v_cvt_pk_bf16_f32 v24, v12, v13\n"
"v_mfma_f32_16x16x32_bf16 a[28:31], v[96:99], a[136:139], a[28:31]\n"
"v_cvt_pk_bf16_f32 v25, v14, v15\n"
"v_mfma_f32_16x16x32_bf16 a[28:31], v[124:127], a[140:143], a[28:31]\n"
"s_waitcnt lgkmcnt(1)\n"
"buffer_store_dwordx4 v[156:159], v6, s[52:55], s86 offen offset:2176 sc1\n"
"ds_write_b64 v7, v[24:25] offset:32768\n"
"v_mfma_f32_16x16x32_bf16 a[24:27], v[60:63], a[140:143], a[24:27]\n"
"v_lshlrev_b32 v12, 16, v18\n"
"v_and_b32 v13, s63, v18\n"
"v_mfma_f32_16x16x32_bf16 a[24:27], v[32:35], a[136:139], a[24:27]\n"
"v_lshlrev_b32 v14, 16, v19\n"
"v_and_b32 v15, s63, v19\n"
"v_mfma_f32_16x16x32_bf16 a[20:23], v[92:95], a[136:139], a[20:23]\n"
"v_mul_f32 v12, v12, v188\n"
"v_mul_f32 v13, v13, v189\n"
"v_mfma_f32_16x16x32_bf16 a[20:23], v[120:123], a[140:143], a[20:23]\n"
"v_mul_f32 v14, v14, v190\n"
"v_mul_f32 v15, v15, v191\n"
"v_mfma_f32_16x16x32_bf16 a[16:19], v[116:119], a[140:143], a[16:19]\n"
"v_cvt_pk_bf16_f32 v26, v12, v13\n"
"v_cvt_pk_bf16_f32 v27, v14, v15\n"
"v_mfma_f32_16x16x32_bf16 a[16:19], v[88:91], a[136:139], a[16:19]\n"
"ds_write_b64 v7, v[26:27] offset:32776\n"
"v_mfma_f32_16x16x32_bf16 a[16:19], v[100:103], a[144:147], a[16:19]\n"
"v_accvgpr_read_b32 v16, a228\n"
"v_accvgpr_read_b32 v17, a229\n"
"v_mfma_f32_16x16x32_bf16 a[16:19], v[128:131], a[148:151], a[16:19]\n"
"v_accvgpr_read_b32 v18, a230\n"
"v_accvgpr_read_b32 v19, a231\n"
"v_mfma_f32_16x16x32_bf16 a[20:23], v[64:67], a[148:151], a[20:23]\n"
"v_lshlrev_b32 v12, 16, v16\n"
"v_and_b32 v13, s63, v16\n"
"v_mfma_f32_16x16x32_bf16 a[20:23], v[36:39], a[144:147], a[20:23]\n"
"s_waitcnt lgkmcnt(2)\n"
"buffer_store_dwordx4 v[184:187], v6, s[52:55], s87 offen offset:3200 sc1\n"
"v_lshlrev_b32 v14, 16, v17\n"
"v_and_b32 v15, s63, v17\n"
"v_mul_f32 v12, v12, v216\n"
"v_mfma_f32_16x16x32_bf16 a[24:27], v[104:107], a[144:147], a[24:27]\n"
"v_mul_f32 v13, v13, v217\n"
"v_mul_f32 v14, v14, v218\n"
"v_mfma_f32_16x16x32_bf16 a[24:27], v[132:135], a[148:151], a[24:27]\n"
"v_mul_f32 v15, v15, v219\n"
"v_mfma_f32_16x16x32_bf16 a[28:31], v[136:139], a[148:151], a[28:31]\n"
"v_cvt_pk_bf16_f32 v28, v12, v13\n"
"v_cvt_pk_bf16_f32 v29, v14, v15\n"
"v_mfma_f32_16x16x32_bf16 a[28:31], v[108:111], a[144:147], a[28:31]\n"
"ds_write_b64 v8, v[28:29] offset:32768\n"
"v_mfma_f32_16x16x32_bf16 a[28:31], a[88:91], a[152:155], a[28:31]\n"
"v_lshlrev_b32 v12, 16, v18\n"
"v_and_b32 v13, s63, v18\n"
"v_mfma_f32_16x16x32_bf16 a[28:31], a[92:95], a[156:159], a[28:31]\n"
"v_lshlrev_b32 v14, 16, v19\n"
"v_and_b32 v15, s63, v19\n"
"v_mfma_f32_16x16x32_bf16 a[24:27], a[84:87], a[156:159], a[24:27]\n"
"v_mul_f32 v12, v12, v244\n"
"v_mul_f32 v13, v13, v245\n"
"v_mfma_f32_16x16x32_bf16 a[24:27], a[80:83], a[152:155], a[24:27]\n"
"v_mul_f32 v14, v14, v246\n"
"v_mul_f32 v15, v15, v247\n"
"v_mfma_f32_16x16x32_bf16 a[20:23], a[72:75], a[152:155], a[20:23]\n"
"s_add_u32 s62, s62, 1\n"
"v_cvt_pk_bf16_f32 v30, v12, v13\n"
"v_cvt_pk_bf16_f32 v31, v14, v15\n"
"v_mfma_f32_16x16x32_bf16 a[20:23], a[76:79], a[156:159], a[20:23]\n"
"ds_write_b64 v8, v[30:31] offset:32776\n"
"v_mfma_f32_16x16x32_bf16 a[16:19], a[68:71], a[156:159], a[16:19]\n"
"ds_read_b128 v[160:163], v2 offset:32768\n"
"v_mfma_f32_16x16x32_bf16 a[16:19], a[64:67], a[152:155], a[16:19]\n"
"ds_read_b128 v[188:191], v2 offset:33792\n"
"s_nop 7\n"
"v_accvgpr_read_b32 v16, a232\n"
"v_accvgpr_read_b32 v17, a233\n"
"v_accvgpr_read_b32 v18, a234\n"
"v_accvgpr_read_b32 v19, a235\n"
"v_lshlrev_b32 v12, 16, v16\n"
"v_and_b32 v13, s63, v16\n"
"v_lshlrev_b32 v14, 16, v17\n"
"v_and_b32 v15, s63, v17\n"
"v_pk_mul_f32 v[12:13], v[12:13], v[164:165]\n"
"v_pk_mul_f32 v[14:15], v[14:15], v[166:167]\n"
"v_cvt_pk_bf16_f32 v24, v12, v13\n"
"v_cvt_pk_bf16_f32 v25, v14, v15\n"
"v_lshlrev_b32 v12, 16, v18\n"
"v_and_b32 v13, s63, v18\n"
"v_lshlrev_b32 v14, 16, v19\n"
"v_and_b32 v15, s63, v19\n"
"v_pk_mul_f32 v[12:13], v[12:13], v[192:193]\n"
"v_pk_mul_f32 v[14:15], v[14:15], v[194:195]\n"
"v_cvt_pk_bf16_f32 v26, v12, v13\n"
"v_cvt_pk_bf16_f32 v27, v14, v15\n"
"ds_write_b128 v7, v[24:27] offset:34816\n"
"s_waitcnt lgkmcnt(2)\n"
"buffer_store_dwordx4 v[160:163], v6, s[52:55], s88 offen offset:128 sc1\n"
"s_waitcnt lgkmcnt(1)\n"
"buffer_store_dwordx4 v[188:191], v6, s[52:55], s89 offen offset:1152 sc1\n"
"v_accvgpr_read_b32 v16, a236\n"
"v_accvgpr_read_b32 v17, a237\n"
"v_accvgpr_read_b32 v18, a238\n"
"v_accvgpr_read_b32 v19, a239\n"
"v_lshlrev_b32 v12, 16, v16\n"
"v_and_b32 v13, s63, v16\n"
"v_lshlrev_b32 v14, 16, v17\n"
"v_and_b32 v15, s63, v17\n"
"v_pk_mul_f32 v[12:13], v[12:13], v[220:221]\n"
"v_pk_mul_f32 v[14:15], v[14:15], v[222:223]\n"
"v_cvt_pk_bf16_f32 v28, v12, v13\n"
"v_cvt_pk_bf16_f32 v29, v14, v15\n"
"v_lshlrev_b32 v12, 16, v18\n"
"v_and_b32 v13, s63, v18\n"
"v_lshlrev_b32 v14, 16, v19\n"
"v_and_b32 v15, s63, v19\n"
"v_pk_mul_f32 v[12:13], v[12:13], v[248:249]\n"
"v_pk_mul_f32 v[14:15], v[14:15], v[250:251]\n"
"v_cvt_pk_bf16_f32 v30, v12, v13\n"
"v_cvt_pk_bf16_f32 v31, v14, v15\n"
"ds_write_b128 v8, v[28:31] offset:34816\n"
"ds_read_b128 v[164:167], v2 offset:34816\n"
"ds_read_b128 v[192:195], v2 offset:35840\n"
"v_accvgpr_read_b32 v16, a240\n"
"v_accvgpr_read_b32 v17, a241\n"
"v_accvgpr_read_b32 v18, a242\n"
"v_accvgpr_read_b32 v19, a243\n"
"v_lshlrev_b32 v12, 16, v16\n"
"v_and_b32 v13, s63, v16\n"
"v_lshlrev_b32 v14, 16, v17\n"
"v_and_b32 v15, s63, v17\n"
"v_pk_mul_f32 v[12:13], v[12:13], v[168:169]\n"
"v_pk_mul_f32 v[14:15], v[14:15], v[170:171]\n"
"v_cvt_pk_bf16_f32 v24, v12, v13\n"
"v_cvt_pk_bf16_f32 v25, v14, v15\n"
"v_lshlrev_b32 v12, 16, v18\n"
"v_and_b32 v13, s63, v18\n"
"v_lshlrev_b32 v14, 16, v19\n"
"v_and_b32 v15, s63, v19\n"
"v_pk_mul_f32 v[12:13], v[12:13], v[196:197]\n"
"v_pk_mul_f32 v[14:15], v[14:15], v[198:199]\n"
"v_cvt_pk_bf16_f32 v26, v12, v13\n"
"v_cvt_pk_bf16_f32 v27, v14, v15\n"
"ds_write_b128 v7, v[24:27] offset:36864\n"
"s_waitcnt lgkmcnt(2)\n"
"buffer_store_dwordx4 v[164:167], v6, s[52:55], s90 offen offset:2176 sc1\n"
"s_waitcnt lgkmcnt(1)\n"
"buffer_store_dwordx4 v[192:195], v6, s[52:55], s91 offen offset:3200 sc1\n"
"v_accvgpr_read_b32 v16, a244\n"
"v_accvgpr_read_b32 v17, a245\n"
"v_accvgpr_read_b32 v18, a246\n"
"v_accvgpr_read_b32 v19, a247\n"
"v_lshlrev_b32 v12, 16, v16\n"
"v_and_b32 v13, s63, v16\n"
"v_lshlrev_b32 v14, 16, v17\n"
"v_and_b32 v15, s63, v17\n"
"v_pk_mul_f32 v[12:13], v[12:13], v[224:225]\n"
"v_pk_mul_f32 v[14:15], v[14:15], v[226:227]\n"
"v_cvt_pk_bf16_f32 v28, v12, v13\n"
"v_cvt_pk_bf16_f32 v29, v14, v15\n"
"v_lshlrev_b32 v12, 16, v18\n"
"v_and_b32 v13, s63, v18\n"
"v_lshlrev_b32 v14, 16, v19\n"
"v_and_b32 v15, s63, v19\n"
"v_pk_mul_f32 v[12:13], v[12:13], v[252:253]\n"
"v_pk_mul_f32 v[14:15], v[14:15], v[254:255]\n"
"v_cvt_pk_bf16_f32 v30, v12, v13\n"
"v_cvt_pk_bf16_f32 v31, v14, v15\n"
"ds_write_b128 v8, v[28:31] offset:36864\n"
"ds_read_b128 v[168:171], v2 offset:36864\n"
"ds_read_b128 v[196:199], v2 offset:37888\n"
"v_accvgpr_read_b32 v16, a248\n"
"v_accvgpr_read_b32 v17, a249\n"
"v_accvgpr_read_b32 v18, a250\n"
"v_accvgpr_read_b32 v19, a251\n"
"v_lshlrev_b32 v12, 16, v16\n"
"v_and_b32 v13, s63, v16\n"
"v_lshlrev_b32 v14, 16, v17\n"
"v_and_b32 v15, s63, v17\n"
"v_accvgpr_read_b32 v10, a16\n"
"v_accvgpr_read_b32 v11, a17\n"
"v_mul_f32 v12, v12, v10\n"
"v_mul_f32 v13, v13, v11\n"
"v_accvgpr_read_b32 v10, a18\n"
"v_accvgpr_read_b32 v11, a19\n"
"v_mul_f32 v14, v14, v10\n"
"v_mul_f32 v15, v15, v11\n"
"v_cvt_pk_bf16_f32 v24, v12, v13\n"
"v_cvt_pk_bf16_f32 v25, v14, v15\n"
"v_lshlrev_b32 v12, 16, v18\n"
"v_and_b32 v13, s63, v18\n"
"v_lshlrev_b32 v14, 16, v19\n"
"v_and_b32 v15, s63, v19\n"
"v_accvgpr_read_b32 v10, a20\n"
"v_accvgpr_read_b32 v11, a21\n"
"v_mul_f32 v12, v12, v10\n"
"v_mul_f32 v13, v13, v11\n"
"v_accvgpr_read_b32 v10, a22\n"
"v_accvgpr_read_b32 v11, a23\n"
"v_mul_f32 v14, v14, v10\n"
"v_mul_f32 v15, v15, v11\n"
"v_cvt_pk_bf16_f32 v26, v12, v13\n"
"v_cvt_pk_bf16_f32 v27, v14, v15\n"
"ds_write_b128 v7, v[24:27] offset:38912\n"
"s_waitcnt lgkmcnt(2)\n"
"buffer_store_dwordx4 v[168:171], v6, s[52:55], s92 offen offset:128 sc1\n"
"s_waitcnt lgkmcnt(1)\n"
"buffer_store_dwordx4 v[196:199], v6, s[52:55], s93 offen offset:1152 sc1\n"
"v_accvgpr_read_b32 v16, a252\n"
"v_accvgpr_read_b32 v17, a253\n"
"v_accvgpr_read_b32 v18, a254\n"
"v_accvgpr_read_b32 v19, a255\n"
"v_lshlrev_b32 v12, 16, v16\n"
"v_and_b32 v13, s63, v16\n"
"v_lshlrev_b32 v14, 16, v17\n"
"v_and_b32 v15, s63, v17\n"
"v_accvgpr_read_b32 v10, a24\n"
"v_accvgpr_read_b32 v11, a25\n"
"v_mul_f32 v12, v12, v10\n"
"v_mul_f32 v13, v13, v11\n"
"v_accvgpr_read_b32 v10, a26\n"
"v_accvgpr_read_b32 v11, a27\n"
"v_mul_f32 v14, v14, v10\n"
"v_mul_f32 v15, v15, v11\n"
"v_cvt_pk_bf16_f32 v28, v12, v13\n"
"v_cvt_pk_bf16_f32 v29, v14, v15\n"
"v_lshlrev_b32 v12, 16, v18\n"
"v_and_b32 v13, s63, v18\n"
"v_lshlrev_b32 v14, 16, v19\n"
"v_and_b32 v15, s63, v19\n"
"v_accvgpr_read_b32 v10, a28\n"
"v_accvgpr_read_b32 v11, a29\n"
"v_mul_f32 v12, v12, v10\n"
"v_mul_f32 v13, v13, v11\n"
"v_accvgpr_read_b32 v10, a30\n"
"v_accvgpr_read_b32 v11, a31\n"
"v_mul_f32 v14, v14, v10\n"
"v_mul_f32 v15, v15, v11\n"
"v_cvt_pk_bf16_f32 v30, v12, v13\n"
"v_cvt_pk_bf16_f32 v31, v14, v15\n"
"ds_write_b128 v8, v[28:31] offset:38912\n"
"ds_read_b128 v[224:227], v2 offset:38912\n"
"ds_read_b128 v[252:255], v2 offset:39936\n"
"s_waitcnt lgkmcnt(1)\n"
"buffer_store_dwordx4 v[224:227], v6, s[52:55], s94 offen offset:2176 sc1\n"
"s_waitcnt lgkmcnt(0)\n"
"buffer_store_dwordx4 v[252:255], v6, s[52:55], s95 offen offset:3200 sc1\n"
"s_or_b32 s66, %26, %16\n"
"s_cbranch_scc1 L_end\n"
"v_mov_b32 v10, s78\n"
"v_mov_b32 v11, s79\n"
"s_mov_b32 s66, 139264\n"
"buffer_store_dwordx2 v[10:11], off, s[56:59], s66 sc1\n"
"s_branch L_end\n"
"L_slow0:\n"
"s_cmp_eq_u32 s68, 0\n"
"s_cbranch_scc1 L_back0\n"
"s_sub_u32 s68, s68, 1\n"
"s_sleep 2\n"
"buffer_load_dwordx2 v[8:9], off, s[56:59], %14 sc1\n"
"s_waitcnt vmcnt(0)\n"
"v_subrev_u32 v8, s65, v8\n"
"v_xor_b32 v9, s79, v9\n"
"v_or_b32 v8, v8, v9\n"
"v_cmp_lt_u32 vcc, 15, v8\n"
"s_nop 4\n"
"s_cbranch_vccnz L_slow0\n"
"s_branch L_back0\n"
"L_slow1:\n"
"s_cmp_eq_u32 s68, 0\n"
"s_cbranch_scc1 L_back1\n"
"s_sub_u32 s68, s68, 1\n"
"s_sleep 2\n"
"buffer_load_dwordx2 v[8:9], off, s[56:59], %14 sc1\n"
"s_waitcnt vmcnt(0)\n"
"v_subrev_u32 v8, s65, v8\n"
"v_xor_b32 v9, s79, v9\n"
"v_or_b32 v8, v8, v9\n"
"v_cmp_lt_u32 vcc, 15, v8\n"
"s_nop 4\n"
"s_cbranch_vccnz L_slow1\n"
"s_branch L_back1\n"
"L_slow2:\n"
"s_cmp_eq_u32 s68, 0\n"
"s_cbranch_scc1 L_back2\n"
"s_sub_u32 s68, s68, 1\n"
"s_sleep 2\n"
"buffer_load_dwordx4 v[12:15], v0, s[56:59], %25 offen sc1\n"
"s_waitcnt vmcnt(0)\n"
"v_subrev_u32 v12, s67, v12\n"
"v_xor_b32 v13, s79, v13\n"
"v_subrev_u32 v14, s67, v14\n"
"v_xor_b32 v15, s79, v15\n"
"v_or_b32 v12, v12, v14\n"
"v_or_b32 v13, v13, v15\n"
"v_or_b32 v12, v12, v13\n"
"v_cmp_lt_u32 vcc, 15, v12\n"
"s_nop 4\n"
"s_cbranch_vccnz L_slow2\n"
"s_branch L_back2\n"
"L_slow3:\n"
"s_cmp_eq_u32 s68, 0\n"
"s_cbranch_scc1 L_back3\n"
"s_sub_u32 s68, s68, 1\n"
"s_sleep 2\n"
"buffer_load_dwordx4 v[24:27], v0, s[56:59], %25 offen sc1\n"
"s_waitcnt vmcnt(0)\n"
"v_subrev_u32 v24, s70, v24\n"
"v_xor_b32 v25, s79, v25\n"
"v_subrev_u32 v26, s70, v26\n"
"v_xor_b32 v27, s79, v27\n"
"v_or_b32 v24, v24, v26\n"
"v_or_b32 v25, v25, v27\n"
"v_or_b32 v24, v24, v25\n"
"v_cmp_lt_u32 vcc, 15, v24\n"
"s_nop 4\n"
"s_cbranch_vccnz L_slow3\n"
"s_branch L_back3\n"
"L_slow4:\n"
"s_cmp_eq_u32 s68, 0\n"
"s_cbranch_scc1 L_back4\n"
"s_sub_u32 s68, s68, 1\n"
"s_sleep 2\n"
"buffer_load_dwordx2 v[8:9], off, s[56:59], %14 sc1\n"
"s_waitcnt vmcnt(0)\n"
"v_subrev_u32 v8, s65, v8\n"
"v_xor_b32 v9, s79, v9\n"
"v_or_b32 v8, v8, v9\n"
"v_cmp_lt_u32 vcc, 15, v8\n"
"s_nop 4\n"
"s_cbranch_vccnz L_slow4\n"
"s_branch L_back4\n"
"L_end:\n"
"s_waitcnt vmcnt(0) lgkmcnt(0)\n"
    :: LO(wsb), HI(wsb), LO(xo), HI(xo), LO(ob), HI(ob), LO(wr), HI(wr), LO(bb), HI(bb), "s"(wo), "s"(eo), "s"(ep), "s"(fo), "s"(fp),
       "s"(ldsa), "s"(w), "s"(col), "v"(threadIdx.x), "s"(db), "s"(rs1), "s"(rs2), "s"(rd1), "s"(rn1), "s"(wfo), "s"(wfa), "s"(b)
    : "memory", "scc", "vcc",
"v1","v2","v3","v4","v5","v6","v7","v8","v9","v10","v11","v12","v13","v14","v15","v16","v17","v18","v19","v20","v21","v22","v23","v24","v25","v26","v27","v28","v29","v30","v31","v32","v33","v34","v35","v36","v37","v38","v39","v40","v41","v42","v43","v44","v45","v46","v47","v48","v49","v50","v51","v52","v53","v54","v55","v56","v57","v58","v59","v60","v61","v62","v63","v64","v65","v66","v67","v68","v69","v70","v71","v72","v73","v74","v75","v76","v77","v78","v79","v80","v81","v82","v83","v84","v85","v86","v87","v88","v89","v90","v91","v92","v93","v94","v95","v96","v97","v98","v99","v100","v101","v102","v103","v104","v105","v106","v107","v108","v109","v110","v111","v112","v113","v114","v115","v116","v117","v118","v119","v120","v121","v122","v123","v124","v125","v126","v127","v128","v129","v130","v131","v132","v133","v134","v135","v136","v137","v138","v139","v140","v141","v142","v143","v144","v145","v146","v147","v148","v149","v150","v151","v152","v153","v154","v155","v156","v157","v158","v159","v160","v161","v162","v163","v164","v165","v166","v167","v168","v169","v170","v171","v172","v173","v174","v175","v176","v177","v178","v179","v180","v181","v182","v183","v184","v185","v186","v187","v188","v189","v190","v191","v192","v193","v194","v195","v196","v197","v198","v199","v200","v201","v202","v203","v204","v205","v206","v207","v208","v209","v210","v211","v212","v213","v214","v215","v216","v217","v218","v219","v220","v221","v222","v223","v224","v225","v226","v227","v228","v229","v230","v231","v232","v233","v234","v235","v236","v237","v238","v239","v240","v241","v242","v243","v244","v245","v246","v247","v248","v249","v250","v251","v252","v253","v254","v255",
"a0","a1","a2","a3","a4","a5","a6","a7","a8","a9","a10","a11","a12","a13","a14","a15","a16","a17","a18","a19","a20","a21","a22","a23","a24","a25","a26","a27","a28","a29","a30","a31","a32","a33","a34","a35","a36","a37","a38","a39","a40","a41","a42","a43","a44","a45","a46","a47","a48","a49","a50","a51","a52","a53","a54","a55","a56","a57","a58","a59","a60","a61","a62","a63","a64","a65","a66","a67","a68","a69","a70","a71","a72","a73","a74","a75","a76","a77","a78","a79","a80","a81","a82","a83","a84","a85","a86","a87","a88","a89","a90","a91","a92","a93","a94","a95","a96","a97","a98","a99","a100","a101","a102","a103","a104","a105","a106","a107","a108","a109","a110","a111","a112","a113","a114","a115","a116","a117","a118","a119","a120","a121","a122","a123","a124","a125","a126","a127","a128","a129","a130","a131","a132","a133","a134","a135","a136","a137","a138","a139","a140","a141","a142","a143","a144","a145","a146","a147","a148","a149","a150","a151","a152","a153","a154","a155","a156","a157","a158","a159","a160","a161","a162","a163","a164","a165","a166","a167","a168","a169","a170","a171","a172","a173","a174","a175","a176","a177","a178","a179","a180","a181","a182","a183","a184","a185","a186","a187","a188","a189","a190","a191","a192","a193","a194","a195","a196","a197","a198","a199","a200","a201","a202","a203","a204","a205","a206","a207","a208","a209","a210","a211","a212","a213","a214","a215","a216","a217","a218","a219","a220","a221","a222","a223","a224","a225","a226","a227","a228","a229","a230","a231","a232","a233","a234","a235","a236","a237","a238","a239","a240","a241","a242","a243","a244","a245","a246","a247","a248","a249","a250","a251","a252","a253","a254","a255",
"s33","s34","s35","s36","s37","s38","s39","s40","s41","s42","s43","s44","s45","s46","s47","s48","s49","s50","s51","s52","s53","s54","s55","s56","s57","s58","s59","s60","s61","s62","s63","s64","s65","s66","s67","s68","s69","s70","s71","s72","s73","s74","s75","s76","s77","s78","s79","s80","s81","s82","s83","s84","s85","s86","s87","s88","s89","s90","s91","s92","s93","s94","s95","s96","s97","s98","s99");
}

extern "C" void kernel_launch(void* const* d_in, const int* in_sizes, int n_in,
                              void* d_out, int out_size, void* d_ws, size_t ws_size,
                              hipStream_t stream) {
  const unsigned short* x0 = (const unsigned short*)d_in[0];
  const unsigned short* ws = (const unsigned short*)d_in[1];
  const unsigned short* bs = (const unsigned short*)d_in[2];
  dcn_cross_kernel<<<256, 256, 0, stream>>>(x0, ws, bs, (unsigned char*)d_ws, (unsigned short*)d_out);
}
```

```cpp
#include <hip/hip_runtime.h>


constexpr int kD = 1024;
constexpr unsigned kWFlagOff = 131072, kWOff = 1u << 20, kWStream = 200 * 8192, kXchOff = 32u << 20, kDbgOff = 224u << 20;

__device__ __forceinline__ unsigned unit_src(unsigned v, unsigned h, unsigned w) {
  const unsigned l = v >> 5, u = v & 31;
  const unsigned split = ((0xff0000ffu >> u) & 1) - 1;
  const unsigned part = l ? u >> 4 : 0u, g = (0xff00ff00u >> u) & 1;
  const unsigned c = u >> (split & 1), jh = (u >> (~split & 2)) & 1;
  const unsigned kc = 8 * (h ^ part) + 2 * ((w + c) & 3) + g;
  return (l * 1024 + 64 * kc) * 2048 + jh * 128;
}

__global__ __launch_bounds__(256, 1)
void dcn_cross_kernel(const unsigned short* __restrict__ x0, const unsigned short* __restrict__ ws, const unsigned short* __restrict__ bs,
                      unsigned char* __restrict__ wsp, unsigned short* __restrict__ out) {
  __shared__ __attribute__((aligned(16))) unsigned char lds[163840];
  const unsigned b = blockIdx.x;
  constexpr unsigned kPx = 1 ? 1u : 8u;
  const unsigned h = 1 ? (b & 1) : ((b >> 3) & 1), rb = 1 ? (b >> 1) : ((b >> 4) * 8 + (b & 7));
  const unsigned w = __builtin_amdgcn_readfirstlane(threadIdx.x >> 6);
  unsigned col = (512 * h + 128 * w) * 2, rows = rb * 128 * kD * 2, oofs = rows + col;
  unsigned wo = (h * 4 + w) * kWStream, eo = kXchOff + b * 524288 + w * 32768, ep = kXchOff + (b ^ kPx) * 524288 + w * 32768;
  unsigned fo = b * 512 + w * 128, fp = (b ^ kPx) * 512 + w * 128;
  unsigned db = kDbgOff + (b * 4 + w) * 512;
  const unsigned v1 = 24 + (rb >> 1), v2 = 64 + (rb >> 1), hk = rb & 1, on = v1 < 64 ? ~0u : 0u;
  unsigned rs1 = ((unit_src(v1, h, w) + hk * 65536) & on) | (((1 - h) << 20) & ~on), rd1 = ((v1 * 8192 + hk * 1024) & on) | ((192 * 8192) & ~on);
  unsigned rs2 = unit_src(v2, h, w) + hk * 65536, rn1 = v2 * 8192 + hk * 1024;
  unsigned wfa = kWFlagOff + (4 * h + w) * 1024, wfo = wfa + rb * 8;
  asm volatile("" : "+s"(col), "+s"(rows), "+s"(oofs), "+s"(wo), "+s"(eo), "+s"(ep), "+s"(fo), "+s"(fp), "+s"(db), "+s"(rs1), "+s"(rd1), "+s"(rs2), "+s"(rn1), "+s"(wfa), "+s"(wfo));
  __builtin_amdgcn_sched_barrier(0);
  const unsigned long long wsb = (unsigned long long)wsp, bb = (unsigned long long)bs;
  const unsigned long long xo = (unsigned long long)x0 + rows, ob = (unsigned long long)out + oofs, wr = (unsigned long long)ws + col;
  wo += (unsigned)wsb > ~(15u << 20) ? 16u << 20 : kWOff;
  const unsigned xa = (0u - (unsigned)wsb) & 0x1fffffu;
  eo += xa; ep += xa;
  const unsigned ldsa = (unsigned)(size_t)(__attribute__((address_space(3))) unsigned char*)lds;
#define LO(x) "s"((unsigned)(x))
#define HI(x) "s"((unsigned)((x) >> 32))
  asm volatile(
"L_main_entry:\n"
"v_and_b32 v12, 63, %18\n"
"v_lshrrev_b32 v13, 3, v12\n"
"v_and_b32 v15, 7, v12\n"
"v_lshlrev_b32 v10, 11, v13\n"
"s_andn2_b32 s60, 1024, %17\n"
"s_lshl_b32 s75, s60, 10\n"
"s_mov_b32 s52, %6\n"
"s_and_b32 s53, %7, 0xffff\n"
"s_mov_b32 s54, 12581120\n"
"s_mov_b32 s55, 0x20000\n"
"v_xor_b32 v6, v15, v13\n"
"v_lshl_add_u32 v6, v6, 4, v10\n"
"s_mov_b32 s48, %2\n"
"s_and_b32 s49, %3, 0xffff\n"
"s_mov_b32 s50, 65536\n"
"s_mov_b32 s51, 0x20000\n"
"s_lshl_b32 s66, %16, 16\n"
"s_add_u32 s48, s48, s66\n"
"s_addc_u32 s49, s49, 0\n"
"s_lshl_b32 s72, %16, 12\n"
"s_add_u32 s72, s72, %15\n"
"s_add_u32 m0, s72, 16384\n"
"s_add_u32 s67, s60, 0\n"
"buffer_load_dwordx4 v6, s[48:51], s67 offen offset:0  lds\n"
"s_add_u32 s67, s60, 15360\n"
"buffer_load_dwordx4 v6, s[48:51], s67 offen offset:1024  lds\n"
"s_add_u32 s67, s60, 30720\n"
"buffer_load_dwordx4 v6, s[48:51], s67 offen offset:2048  lds\n"
"s_add_u32 s67, s60, 46080\n"
"buffer_load_dwordx4 v6, s[48:51], s67 offen offset:3072  lds\n"
"s_mul_i32 s73, %16, 40960\n"
"s_add_u32 s64, s73, %15\n"
"v_lshrrev_b32 v11, 2, v12\n"
"v_and_b32 v11, 4, v11\n"
"v_xor_b32 v11, v11, v15\n"
"v_lshl_add_u32 v11, v11, 4, v10\n"
"s_add_u32 m0, s64, 0\n"
"s_add_u32 s67, s75, 0\n"
"buffer_load_dwordx4 v11, s[52:55], s67 offen offset:0 lds\n"
"s_add_u32 s67, s75, 15360\n"
"buffer_load_dwordx4 v11, s[52:55], s67 offen offset:1024 lds\n"
"s_add_u32 s67, s75, 30720\n"
"buffer_load_dwordx4 v11, s[52:55], s67 offen offset:2048 lds\n"
"s_add_u32 s67, s75, 46080\n"
"buffer_load_dwordx4 v11, s[52:55], s67 offen offset:3072 lds\n"
"s_add_u32 m0, s64, 4096\n"
"s_add_u32 s67, s75, 65536\n"
"buffer_load_dwordx4 v11, s[52:55], s67 offen offset:0 lds\n"
"s_add_u32 s67, s75, 80896\n"
"buffer_load_dwordx4 v11, s[52:55], s67 offen offset:1024 lds\n"
"s_add_u32 s67, s75, 96256\n"
"buffer_load_dwordx4 v11, s[52:55], s67 offen offset:2048 lds\n"
"s_add_u32 s67, s75, 111616\n"
"buffer_load_dwordx4 v11, s[52:55], s67 offen offset:3072 lds\n"
"s_mov_b32 s44, %8\n"
"s_and_b32 s45, %9, 0xffff\n"
"s_mov_b32 s46, 12288\n"
"s_mov_b32 s47, 0x20000\n"
"v_lshlrev_b32 v0, 4, v12\n"
"s_mov_b32 s61, %17\n"
"v_lshrrev_b32 v16, 4, v0\n"
"v_and_b32 v16, 0x30, v16\n"
"buffer_load_dwordx2 v[18:19], v16, s[44:47], s61 offen offset:0\n"
"v_lshrrev_b32 v20, 4, v0\n"
"v_and_b32 v20, 0x30, v20\n"
"buffer_load_dwordx2 v[22:23], v20, s[44:47], s61 offen offset:8\n"
"v_lshrrev_b32 v24, 4, v0\n"
"v_and_b32 v24, 0x30, v24\n"
"buffer_load_dwordx2 v[26:27], v24, s[44:47], s61 offen offset:64\n"
"v_lshrrev_b32 v28, 4, v0\n"
"v_and_b32 v28, 0x30, v28\n"
"buffer_load_dwordx2 v[30:31], v28, s[44:47], s61 offen offset:72\n"
"s_getpc_b64 s[96:97]\n"
"L_pc:\n"
"s_lshr_b32 s66, %26, 3\n"
"s_lshl_b32 s66, s66, 2\n"
"s_add_u32 s66, s66, %16\n"
"s_mul_i32 s66, s66, 640\n"
"s_and_b32 s97, s97, 0xffff\n"
"s_mov_b32 s98, L_end-L_pc\n"
"s_mov_b32 s99, 0x20000\n"
"s_min_u32 s66, s66, L_end-L_pc-640\n"
"v_min_u32 v9, 9, v12\n"
"v_lshlrev_b32 v9, 6, v9\n"
"buffer_load_dword a252, v9, s[96:99], s66 offen\n"
"s_lshr_b32 s65, %26, 4\n"
"s_lshl_b32 s65, s65, 2\n"
"s_add_u32 s65, s65, %16\n"
"s_lshl_b32 s65, s65, 11\n"
"s_lshl_b32 s67, %16, 8\n"
"s_sub_u32 s65, s65, s67\n"
"v_and_b32 v14, 1, v13\n"
"v_lshlrev_b32 v14, 17, v14\n"
"v_lshl_add_u32 v14, v15, 7, v14\n"
"v_and_b32 v14, 0xfffffdff, v14\n"
"s_lshl_b32 s67, %26, 6\n"
"s_and_b32 s67, s67, 512\n"
"s_add_u32 s65, s65, s67\n"
"s_add_u32 s70, s75, 131072\n"
"s_add_u32 s70, s70, s65\n"
"buffer_load_dword a252, v14, s[52:55], s70 offen\n"
"s_add_u32 m0, s64, 8192\n"
"s_add_u32 s67, s75, 128\n"
"buffer_load_dwordx4 v11, s[52:55], s67 offen offset:0 lds\n"
"s_add_u32 s67, s75, 15488\n"
"buffer_load_dwordx4 v11, s[52:55], s67 offen offset:1024 lds\n"
"s_add_u32 s67, s75, 30848\n"
"buffer_load_dwordx4 v11, s[52:55], s67 offen offset:2048 lds\n"
"s_add_u32 s67, s75, 46208\n"
"buffer_load_dwordx4 v11, s[52:55], s67 offen offset:3072 lds\n"
"s_add_u32 m0, s64, 12288\n"
"s_add_u32 s67, s75, 65664\n"
"buffer_load_dwordx4 v11, s[52:55], s67 offen offset:0 lds\n"
"s_add_u32 s67, s75, 81024\n"
"buffer_load_dwordx4 v11, s[52:55], s67 offen offset:1024 lds\n"
"s_add_u32 s67, s75, 96384\n"
"buffer_load_dwordx4 v11, s[52:55], s67 offen offset:2048 lds\n"
"s_add_u32 s67, s75, 111744\n"
"buffer_load_dwordx4 v11, s[52:55], s67 offen offset:3072 lds\n"
"s_add_u32 m0, s72, 57344\n"
"s_add_u32 s67, s60, 128\n"
"buffer_load_dwordx4 v6, s[48:51], s67 offen offset:0  lds\n"
"s_add_u32 s67, s60, 15488\n"
"buffer_load_dwordx4 v6, s[48:51], s67 offen offset:1024  lds\n"
"s_add_u32 s67, s60, 30848\n"
"buffer_load_dwordx4 v6, s[48:51], s67 offen offset:2048  lds\n"
"s_add_u32 s67, s60, 46208\n"
"buffer_load_dwordx4 v6, s[48:51], s67 offen offset:3072  lds\n"
"s_mov_b32 s56, %0\n"
"s_and_b32 s57, %1, 0xffff\n"
"s_mov_b32 s58, 171966464\n"
"s_mov_b32 s59, 0x20000\n"
"s_mov_b32 s67, 139264\n"
"buffer_load_dwordx2 v[252:253], off, s[56:59], s67 sc1\n"
"v_add_u32 v1, 4096, v0\n"
"s_add_u32 s36, s56, %10\n"
"s_addc_u32 s37, s57, 0\n"
"s_mov_b32 s38, 1572864\n"
"s_mov_b32 s39, 0x20000\n"
"s_add_u32 s34, s36, 200704\n"
"s_mov_b32 s35, s37\n"
"v_and_b32 v13, 15, v12\n"
"v_lshrrev_b32 v10, 4, v12\n"
"v_xor_b32 v15, v15, v10\n"
"v_lshlrev_b32 v7, 7, v13\n"
"v_lshl_add_u32 v7, v15, 4, v7\n"
"v_add_u32 v7, s64, v7\n"
"v_xor_b32 v8, 64, v7\n"
"v_bfe_u32 v13, v12, 2, 2\n"
"v_and_b32 v15, 3, v12\n"
"v_bfe_u32 v9, v12, 3, 1\n"
"v_lshl_add_u32 v15, v9, 2, v15\n"
"v_lshl_add_u32 v13, v10, 3, v13\n"
"v_lshlrev_b32 v13, 7, v13\n"
"v_lshl_add_u32 v9, v15, 4, v13\n"
"v_add_u32 v9, s64, v9\n"
"v_xor_b32 v15, 64, v9\n"
"s_mov_b32 s40, %2\n"
"s_and_b32 s41, %3, 0xffff\n"
"s_mov_b32 s42, 260352\n"
"s_mov_b32 s43, 0x20000\n"
"s_add_u32 s40, s40, %17\n"
"s_addc_u32 s41, s41, 0\n"
"s_mov_b32 s80, 0\n"
"s_mov_b32 s81, 15360\n"
"s_mov_b32 s82, 30720\n"
"s_mov_b32 s83, 46080\n"
"s_mov_b32 s84, 65536\n"
"s_mov_b32 s85, 80896\n"
"s_mov_b32 s86, 96256\n"
"s_mov_b32 s87, 111616\n"
"s_mov_b32 s88, 131072\n"
"s_mov_b32 s89, 146432\n"
"s_mov_b32 s90, 161792\n"
"s_mov_b32 s91, 177152\n"
"s_mov_b32 s92, 196608\n"
"s_mov_b32 s93, 211968\n"
"s_mov_b32 s94, 227328\n"
"s_mov_b32 s95, 242688\n"
"s_mov_b32 s62, 0\n"
"s_mov_b32 s63, 0xffff0000\n"
"s_mov_b32 s68, 0x8000\n"
"s_add_u32 s66, %16, 0\n"
"s_and_b32 s66, s66, 3\n"
"s_mul_i32 s66, s66, 40960\n"
"s_add_u32 s66, s66, %15\n"
"v_add_u32 v2, s66, v0\n"
"s_add_u32 s66, %16, 1\n"
"s_and_b32 s66, s66, 3\n"
"s_mul_i32 s66, s66, 40960\n"
"s_add_u32 s66, s66, %15\n"
"v_add_u32 v3, s66, v0\n"
"s_add_u32 s66, %16, 2\n"
"s_and_b32 s66, s66, 3\n"
"s_mul_i32 s66, s66, 40960\n"
"s_add_u32 s66, s66, %15\n"
"v_add_u32 v4, s66, v0\n"
"s_add_u32 s66, %16, 3\n"
"s_and_b32 s66, s66, 3\n"
"s_mul_i32 s66, s66, 40960\n"
"s_add_u32 s66, s66, %15\n"
"v_add_u32 v5, s66, v0\n"
"s_sub_u32 s66, 0, s73\n"
"v_add_u32 v12, s66, v7\n"
"v_add_u32 v13, s66, v8\n"
"s_waitcnt vmcnt(27)\n"
"s_barrier\n"
"ds_read_b128 a[100:103], v13 offset:16384\n"
"ds_read_b128 a[96:99], v12 offset:16384\n"
"ds_read_b128 a[108:111], v13 offset:18432\n"
"ds_read_b128 a[104:107], v12 offset:18432\n"
"ds_read_b128 a[116:119], v13 offset:20480\n"
"ds_read_b128 a[112:115], v12 offset:20480\n"
"ds_read_b128 a[124:127], v13 offset:22528\n"
"ds_read_b128 a[120:123], v12 offset:22528\n"
"ds_read_b128 a[132:135], v13 offset:24576\n"
"ds_read_b128 a[128:131], v12 offset:24576\n"
"ds_read_b128 a[140:143], v13 offset:26624\n"
"ds_read_b128 a[136:139], v12 offset:26624\n"
"ds_read_b128 a[148:151], v13 offset:28672\n"
"ds_read_b128 a[144:147], v12 offset:28672\n"
"ds_read_b128 a[156:159], v13 offset:30720\n"
"ds_read_b128 a[152:155], v12 offset:30720\n"
"s_waitcnt vmcnt(19)\n"
"ds_read_b64_tr_b16 a[32:33], v9 offset:0\n"
"ds_read_b64_tr_b16 a[34:35], v9 offset:512\n"
"ds_read_b64_tr_b16 a[36:37], v9 offset:4096\n"
"ds_read_b64_tr_b16 a[38:39], v9 offset:4608\n"
"ds_read_b64_tr_b16 a[40:41], v9 offset:8\n"
"ds_read_b64_tr_b16 a[42:43], v9 offset:520\n"
"ds_read_b64_tr_b16 a[44:45], v9 offset:4104\n"
"ds_read_b64_tr_b16 a[46:47], v9 offset:4616\n"
"ds_read_b64_tr_b16 a[48:49], v15 offset:0\n"
"ds_read_b64_tr_b16 a[50:51], v15 offset:512\n"
"ds_read_b64_tr_b16 a[52:53], v15 offset:4096\n"
"ds_read_b64_tr_b16 a[54:55], v15 offset:4608\n"
"ds_read_b64_tr_b16 a[56:57], v15 offset:8\n"
"ds_read_b64_tr_b16 a[58:59], v15 offset:520\n"
"ds_read_b64_tr_b16 a[60:61], v15 offset:4104\n"
"ds_read_b64_tr_b16 a[62:63], v15 offset:4616\n"
"s_waitcnt vmcnt(15)\n"
"v_lshlrev_b32 v16, 16, v18\n"
"v_and_b32 v17, s63, v18\n"
"v_lshlrev_b32 v18, 16, v19\n"
"v_and_b32 v19, s63, v19\n"
"v_add_f32 v16, 1.0, v16\n"
"v_add_f32 v17, 1.0, v17\n"
"v_add_f32 v18, 1.0, v18\n"
"v_add_f32 v19, 1.0, v19\n"
"v_lshlrev_b32 v20, 16, v22\n"
"v_and_b32 v21, s63, v22\n"
"v_lshlrev_b32 v22, 16, v23\n"
"v_and_b32 v23, s63, v23\n"
"v_add_f32 v20, 1.0, v20\n"
"v_add_f32 v21, 1.0, v21\n"
"v_add_f32 v22, 1.0, v22\n"
"v_add_f32 v23, 1.0, v23\n"
"v_lshlrev_b32 v24, 16, v26\n"
"v_and_b32 v25, s63, v26\n"
"v_lshlrev_b32 v26, 16, v27\n"
"v_and_b32 v27, s63, v27\n"
"v_add_f32 v24, 1.0, v24\n"
"v_add_f32 v25, 1.0, v25\n"
"v_add_f32 v26, 1.0, v26\n"
"v_add_f32 v27, 1.0, v27\n"
"v_lshlrev_b32 v28, 16, v30\n"
"v_and_b32 v29, s63, v30\n"
"v_lshlrev_b32 v30, 16, v31\n"
"v_and_b32 v31, s63, v31\n"
"v_add_f32 v28, 1.0, v28\n"
"v_add_f32 v29, 1.0, v29\n"
"v_add_f32 v30, 1.0, v30\n"
"v_add_f32 v31, 1.0, v31\n"
"v_accvgpr_write_b32 a0, v16\n"
"v_accvgpr_write_b32 a1, v17\n"
"v_accvgpr_write_b32 a2, v18\n"
"v_accvgpr_write_b32 a3, v19\n"
"v_accvgpr_write_b32 a4, v20\n"
"v_accvgpr_write_b32 a5, v21\n"
"v_accvgpr_write_b32 a6, v22\n"
"v_accvgpr_write_b32 a7, v23\n"
"v_accvgpr_write_b32 a8, v24\n"
"v_accvgpr_write_b32 a9, v25\n"
"v_accvgpr_write_b32 a10, v26\n"
"v_accvgpr_write_b32 a11, v27\n"
"v_accvgpr_write_b32 a12, v28\n"
"v_accvgpr_write_b32 a13, v29\n"
"v_accvgpr_write_b32 a14, v30\n"
"v_accvgpr_write_b32 a15, v31\n"
"s_nop 1\n"
"s_waitcnt lgkmcnt(8)\n"
"v_mfma_f32_16x16x32_bf16 v[32:35], a[32:35], a[96:99], v[16:19]\n"
"s_add_u32 s70, s75, 393216\n"
"v_mfma_f32_16x16x32_bf16 v[32:35], a[36:39], a[100:103], v[32:35]\n"
"s_add_u32 s70, s70, s65\n"
"v_mfma_f32_16x16x32_bf16 v[60:63], a[44:47], a[100:103], v[20:23]\n"
"buffer_load_dword a252, v14, s[52:55], s70 offen\n"
"v_mfma_f32_16x16x32_bf16 v[60:63], a[40:43], a[96:99], v[60:63]\n"
"s_add_u32 m0, s64, 32768\n"
"v_mfma_f32_16x16x32_bf16 v[64:67], a[40:43], a[104:107], v[20:23]\n"
"s_add_u32 s67, s75, 131072\n"
"v_mfma_f32_16x16x32_bf16 v[64:67], a[44:47], a[108:111], v[64:67]\n"
"buffer_load_dwordx4 v11, s[52:55], s67 offen offset:0 lds\n"
"v_mfma_f32_16x16x32_bf16 v[36:39], a[36:39], a[108:111], v[16:19]\n"
"s_add_u32 s67, s75, 146432\n"
"v_mfma_f32_16x16x32_bf16 v[36:39], a[32:35], a[104:107], v[36:39]\n"
"v_mfma_f32_16x16x32_bf16 v[40:43], a[32:35], a[112:115], v[16:19]\n"
"s_waitcnt lgkmcnt(0)\n"
"buffer_load_dwordx4 v11, s[52:55], s67 offen offset:1024 lds\n"
"v_mfma_f32_16x16x32_bf16 v[40:43], a[36:39], a[116:119], v[40:43]\n"
"v_mfma_f32_16x16x32_bf16 v[68:71], a[44:47], a[116:119], v[20:23]\n"
"s_add_u32 s67, s75, 161792\n"
"v_mfma_f32_16x16x32_bf16 v[68:71], a[40:43], a[112:115], v[68:71]\n"
"v_mfma_f32_16x16x32_bf16 v[72:75], a[40:43], a[120:123], v[20:23]\n"
"buffer_load_dwordx4 v11, s[52:55], s67 offen offset:2048 lds\n"
"v_mfma_f32_16x16x32_bf16 v[72:75], a[44:47], a[124:127], v[72:75]\n"
"v_mfma_f32_16x16x32_bf16 v[44:47], a[36:39], a[124:127], v[16:19]\n"
"s_add_u32 s67, s75, 177152\n"
"v_mfma_f32_16x16x32_bf16 v[44:47], a[32:35], a[120:123], v[44:47]\n"
"v_mfma_f32_16x16x32_bf16 v[48:51], a[32:35], a[128:131], v[16:19]\n"
"buffer_load_dwordx4 v11, s[52:55], s67 offen offset:3072 lds\n"
"v_mfma_f32_16x16x32_bf16 v[48:51], a[36:39], a[132:135], v[48:51]\n"
"v_mfma_f32_16x16x32_bf16 v[76:79], a[44:47], a[132:135], v[20:23]\n"
"s_add_u32 m0, s64, 36864\n"
"v_mfma_f32_16x16x32_bf16 v[76:79], a[40:43], a[128:131], v[76:79]\n"
"v_mfma_f32_16x16x32_bf16 v[80:83], a[40:43], a[136:139], v[20:23]\n"
"s_add_u32 s67, s75, 196608\n"
"v_mfma_f32_16x16x32_bf16 v[80:83], a[44:47], a[140:143], v[80:83]\n"
"v_mfma_f32_16x16x32_bf16 v[52:55], a[36:39], a[140:143], v[16:19]\n"
"buffer_load_dwordx4 v11, s[52:55], s67 offen offset:0 lds\n"
"v_mfma_f32_16x16x32_bf16 v[52:55], a[32:35], a[136:139], v[52:55]\n"
"v_mfma_f32_16x16x32_bf16 v[56:59], a[32:35], a[144:147], v[16:19]\n"
"s_add_u32 s67, s75, 211968\n"
"v_mfma_f32_16x16x32_bf16 v[56:59], a[36:39], a[148:151], v[56:59]\n"
"v_mfma_f32_16x16x32_bf16 v[84:87], a[44:47], a[148:151], v[20:23]\n"
"buffer_load_dwordx4 v11, s[52:55], s67 offen offset:1024 lds\n"
"v_mfma_f32_16x16x32_bf16 v[84:87], a[40:43], a[144:147], v[84:87]\n"
"v_mfma_f32_16x16x32_bf16 a[4:7], a[40:43], a[152:155], a[4:7]\n"
"s_add_u32 s67, s75, 227328\n"
"v_mfma_f32_16x16x32_bf16 a[4:7], a[44:47], a[156:159], a[4:7]\n"
"v_mfma_f32_16x16x32_bf16 a[0:3], a[36:39], a[156:159], a[0:3]\n"
"buffer_load_dwordx4 v11, s[52:55], s67 offen offset:2048 lds\n"
"v_mfma_f32_16x16x32_bf16 a[0:3], a[32:35], a[152:155], a[0:3]\n"
"v_mfma_f32_16x16x32_bf16 v[88:91], a[48:51], a[96:99], v[24:27]\n"
"s_add_u32 s67, s75, 242688\n"
"v_mfma_f32_16x16x32_bf16 v[88:91], a[52:55], a[100:103], v[88:91]\n"
"v_mfma_f32_16x16x32_bf16 v[116:119], a[60:63], a[100:103], v[28:31]\n"
"v_mfma_f32_16x16x32_bf16 v[116:119], a[56:59], a[96:99], v[116:119]\n"
"buffer_load_dwordx4 v11, s[52:55], s67 offen offset:3072 lds\n"
"v_mfma_f32_16x16x32_bf16 v[120:123], a[56:59], a[104:107], v[28:31]\n"
"s_add_u32 m0, s64, 0\n"
"v_mfma_f32_16x16x32_bf16 v[120:123], a[60:63], a[108:111], v[120:123]\n"
"v_mfma_f32_16x16x32_bf16 v[92:95], a[52:55], a[108:111], v[24:27]\n"
"s_add_u32 s67, s75, 131200\n"
"v_mfma_f32_16x16x32_bf16 v[92:95], a[48:51], a[104:107], v[92:95]\n"
"v_mfma_f32_16x16x32_bf16 v[96:99], a[48:51], a[112:115], v[24:27]\n"
"buffer_load_dwordx4 v11, s[52:55], s67 offen offset:0 lds\n"
"v_mfma_f32_16x16x32_bf16 v[96:99], a[52:55], a[116:119], v[96:99]\n"
"v_mfma_f32_16x16x32_bf16 v[124:127], a[60:63], a[116:119], v[28:31]\n"
"s_add_u32 s67, s75, 146560\n"
"v_mfma_f32_16x16x32_bf16 v[124:127], a[56:59], a[112:115], v[124:127]\n"
"buffer_load_dwordx4 v11, s[52:55], s67 offen offset:1024 lds\n"
"v_mfma_f32_16x16x32_bf16 v[128:131], a[56:59], a[120:123], v[28:31]\n"
"s_waitcnt vmcnt(16)\n"
"ds_read_b64_tr_b16 a[64:65], v9 offset:8192\n"
"v_mfma_f32_16x16x32_bf16 v[128:131], a[60:63], a[124:127], v[128:131]\n"
"s_add_u32 s67, s75, 161920\n"
"ds_read_b64_tr_b16 a[66:67], v9 offset:8704\n"
"v_mfma_f32_16x16x32_bf16 v[100:103], a[52:55], a[124:127], v[24:27]\n"
"ds_read_b64_tr_b16 a[68:69], v9 offset:12288\n"
"v_mfma_f32_16x16x32_bf16 v[100:103], a[48:51], a[120:123], v[100:103]\n"
"ds_read_b64_tr_b16 a[70:71], v9 offset:12800\n"
"v_mfma_f32_16x16x32_bf16 v[104:107], a[48:51], a[128:131], v[24:27]\n"
"ds_read_b64_tr_b16 a[72:73], v9 offset:8200\n"
"v_mfma_f32_16x16x32_bf16 v[104:107], a[52:55], a[132:135], v[104:107]\n"
"ds_read_b64_tr_b16 a[74:75], v9 offset:8712\n"
"buffer_load_dwordx4 v11, s[52:55], s67 offen offset:2048 lds\n"
"v_mfma_f32_16x16x32_bf16 v[132:135], a[60:63], a[132:135], v[28:31]\n"
"ds_read_b64_tr_b16 a[76:77], v9 offset:12296\n"
"v_mfma_f32_16x16x32_bf16 v[132:135], a[56:59], a[128:131], v[132:135]\n"
"s_add_u32 s67, s75, 177280\n"
"ds_read_b64_tr_b16 a[78:79], v9 offset:12808\n"
"v_mfma_f32_16x16x32_bf16 v[136:139], a[56:59], a[136:139], v[28:31]\n"
"buffer_load_dwordx4 v11, s[52:55], s67 offen offset:3072 lds\n"
"v_mfma_f32_16x16x32_bf16 v[136:139], a[60:63], a[140:143], v[136:139]\n"
"ds_read_b64_tr_b16 a[80:81], v15 offset:8192\n"
"v_mfma_f32_16x16x32_bf16 v[108:111], a[52:55], a[140:143], v[24:27]\n"
"s_add_u32 m0, s64, 4096\n"
"ds_read_b64_tr_b16 a[82:83], v15 offset:8704\n"
"v_mfma_f32_16x16x32_bf16 v[108:111], a[48:51], a[136:139], v[108:111]\n"
"ds_read_b64_tr_b16 a[84:85], v15 offset:12288\n"
"v_mfma_f32_16x16x32_bf16 v[112:115], a[48:51], a[144:147], v[24:27]\n"
"s_add_u32 s67, s75, 196736\n"
"ds_read_b64_tr_b16 a[86:87], v15 offset:12800\n"
"v_mfma_f32_16x16x32_bf16 v[112:115], a[52:55], a[148:151], v[112:115]\n"
"ds_read_b64_tr_b16 a[88:89], v15 offset:8200\n"
"v_mfma_f32_16x16x32_bf16 v[140:143], a[60:63], a[148:151], v[28:31]\n"
"ds_read_b64_tr_b16 a[90:91], v15 offset:8712\n"
"buffer_load_dwordx4 v11, s[52:55], s67 offen offset:0 lds\n"
"v_mfma_f32_16x16x32_bf16 v[140:143], a[56:59], a[144:147], v[140:143]\n"
"ds_read_b64_tr_b16 a[92:93], v15 offset:12296\n"
"v_mfma_f32_16x16x32_bf16 a[12:15], a[56:59], a[152:155], a[12:15]\n"
"s_add_u32 s67, s75, 212096\n"
"ds_read_b64_tr_b16 a[94:95], v15 offset:12808\n"
"v_mfma_f32_16x16x32_bf16 a[12:15], a[60:63], a[156:159], a[12:15]\n"
"v_mfma_f32_16x16x32_bf16 a[8:11], a[52:55], a[156:159], a[8:11]\n"
"buffer_load_dwordx4 v11, s[52:55], s67 offen offset:1024 lds\n"
"v_mfma_f32_16x16x32_bf16 a[8:11], a[48:51], a[152:155], a[8:11]\n"
"s_waitcnt lgkmcnt(8)\n"
"v_mfma_f32_16x16x32_bf16 v[144:147], a[64:67], a[96:99], 1.0\n"
"s_add_u32 s67, s75, 227456\n"
"v_mfma_f32_16x16x32_bf16 v[144:147], a[68:71], a[100:103], v[144:147]\n"
"v_mfma_f32_16x16x32_bf16 v[172:175], a[76:79], a[100:103], 1.0\n"
"s_waitcnt vmcnt(15) lgkmcnt(0)\n"
"v_readfirstlane_b32 s78, v252\n"
"v_readfirstlane_b32 s79, v253\n"
"v_mfma_f32_16x16x32_bf16 v[172:175], a[72:75], a[96:99], v[172:175]\n"
"buffer_load_dwordx4 v11, s[52:55], s67 offen offset:2048 lds\n"
"v_mfma_f32_16x16x32_bf16 v[176:179], a[72:75], a[104:107], 1.0\n"
"v_mfma_f32_16x16x32_bf16 v[176:179], a[76:79], a[108:111], v[176:179]\n"
"s_add_u32 s67, s75, 242816\n"
"v_mfma_f32_16x16x32_bf16 v[148:151], a[68:71], a[108:111], 1.0\n"
"s_add_u32 s78, s78, 16\n"
"v_mfma_f32_16x16x32_bf16 v[148:151], a[64:67], a[104:107], v[148:151]\n"
"s_add_u32 s79, s79, 16\n"
"v_mfma_f32_16x16x32_bf16 v[152:155], a[64:67], a[112:115], 1.0\n"
"s_sub_u32 s66, 40960, s73\n"
"v_mfma_f32_16x16x32_bf16 v[152:155], a[68:71], a[116:119], v[152:155]\n"
"buffer_load_dwordx4 v11, s[52:55], s67 offen offset:3072 lds\n"
"v_mfma_f32_16x16x32_bf16 v[180:183], a[76:79], a[116:119], 1.0\n"
"s_add_u32 m0, s72, 98304\n"
"v_add_u32 v12, s66, v7\n"
"v_mfma_f32_16x16x32_bf16 v[180:183], a[72:75], a[112:115], v[180:183]\n"
"v_mfma_f32_16x16x32_bf16 v[184:187], a[72:75], a[120:123], 1.0\n"
"s_add_u32 s67, s60, 256\n"
"v_add_u32 v13, s66, v8\n"
"v_mfma_f32_16x16x32_bf16 v[184:187], a[76:79], a[124:127], v[184:187]\n"
"buffer_load_dwordx4 v6, s[48:51], s67 offen offset:0  lds\n"
"v_mfma_f32_16x16x32_bf16 v[156:159], a[68:71], a[124:127], 1.0\n"
"v_mfma_f32_16x16x32_bf16 v[156:159], a[64:67], a[120:123], v[156:159]\n"
"s_add_u32 s67, s60, 15616\n"
"v_mfma_f32_16x16x32_bf16 v[160:163], a[64:67], a[128:131], 1.0\n"
"v_mfma_f32_16x16x32_bf16 v[160:163], a[68:71], a[132:135], v[160:163]\n"
"buffer_load_dwordx4 v6, s[48:51], s67 offen offset:1024  lds\n"
"v_mfma_f32_16x16x32_bf16 v[188:191], a[76:79], a[132:135], 1.0\n"
"v_mfma_f32_16x16x32_bf16 v[188:191], a[72:75], a[128:131], v[188:191]\n"
"s_add_u32 s67, s60, 30976\n"
"v_mfma_f32_16x16x32_bf16 v[192:195], a[72:75], a[136:139], 1.0\n"
"v_mfma_f32_16x16x32_bf16 v[192:195], a[76:79], a[140:143], v[192:195]\n"
"buffer_load_dwordx4 v6, s[48:51], s67 offen offset:2048  lds\n"
"v_mfma_f32_16x16x32_bf16 v[164:167], a[68:71], a[140:143], 1.0\n"
"v_mfma_f32_16x16x32_bf16 v[164:167], a[64:67], a[136:139], v[164:167]\n"
"s_add_u32 s67, s60, 46336\n"
"v_mfma_f32_16x16x32_bf16 v[168:171], a[64:67], a[144:147], 1.0\n"
"v_mfma_f32_16x16x32_bf16 v[168:171], a[68:71], a[148:151], v[168:171]\n"
"v_mfma_f32_16x16x32_bf16 v[196:199], a[76:79], a[148:151], 1.0\n"
"buffer_load_dwordx4 v6, s[48:51], s67 offen offset:3072  lds\n"
"v_mfma_f32_16x16x32_bf16 v[196:199], a[72:75], a[144:147], v[196:199]\n"
"s_mov_b64 s[96:97], s[52:53]\n"
"v_mfma_f32_16x16x32_bf16 a[20:23], a[72:75], a[152:155], 1.0\n"
"s_cmp_lt_u32 %22, 1572864\n"
"s_cselect_b32 s98, s54, 0\n"
"s_cselect_b32 s38, s38, 0\n"
"v_mfma_f32_16x16x32_bf16 a[20:23], a[76:79], a[156:159], a[20:23]\n"
"s_mov_b32 s99, 0x20000\n"
"s_add_u32 m0, s72, 139264\n"
"s_add_u32 s67, %20, 0\n"
"v_mfma_f32_16x16x32_bf16 a[16:19], a[68:71], a[156:159], 1.0\n"
"buffer_load_dwordx4 v11, s[96:99], s67 offen offset:0  lds\n"
"v_mfma_f32_16x16x32_bf16 a[16:19], a[64:67], a[152:155], a[16:19]\n"
"s_barrier\n"
"s_waitcnt vmcnt(13)\n"
"ds_read_b64_tr_b16 a[32:33], v9 offset:32768\n"
"v_mfma_f32_16x16x32_bf16 v[200:203], a[80:83], a[96:99], 1.0\n"
"s_add_u32 s67, %20, 15360\n"
"ds_read_b64_tr_b16 a[34:35], v9 offset:33280\n"
"v_mfma_f32_16x16x32_bf16 v[200:203], a[84:87], a[100:103], v[200:203]\n"
"ds_read_b64_tr_b16 a[36:37], v9 offset:36864\n"
"v_mfma_f32_16x16x32_bf16 v[228:231], a[92:95], a[100:103], 1.0\n"
"ds_read_b128 a[100:103], v13 offset:16384\n"
"v_mfma_f32_16x16x32_bf16 v[228:231], a[88:91], a[96:99], v[228:231]\n"
"ds_read_b128 a[96:99], v12 offset:16384\n"
"buffer_load_dwordx4 v11, s[96:99], s67 offen offset:1024  lds\n"
"v_mfma_f32_16x16x32_bf16 v[232:235], a[88:91], a[104:107], 1.0\n"
"ds_read_b64_tr_b16 a[38:39], v9 offset:37376\n"
"v_mfma_f32_16x16x32_bf16 v[232:235], a[92:95], a[108:111], v[232:235]\n"
"s_add_u32 s67, %20, 30720\n"
"ds_read_b64_tr_b16 a[40:41], v9 offset:32776\n"
"v_mfma_f32_16x16x32_bf16 v[204:207], a[84:87], a[108:111], 1.0\n"
"ds_read_b128 a[108:111], v13 offset:18432\n"
"v_mfma_f32_16x16x32_bf16 v[204:207], a[80:83], a[104:107], v[204:207]\n"
"ds_read_b128 a[104:107], v12 offset:18432\n"
"v_mfma_f32_16x16x32_bf16 v[208:211], a[80:83], a[112:115], 1.0\n"
"ds_read_b64_tr_b16 a[42:43], v9 offset:33288\n"
"buffer_load_dwordx4 v11, s[96:99], s67 offen offset:2048  lds\n"
"v_mfma_f32_16x16x32_bf16 v[208:211], a[84:87], a[116:119], v[208:211]\n"
"ds_read_b64_tr_b16 a[44:45], v9 offset:36872\n"
"v_mfma_f32_16x16x32_bf16 v[236:239], a[92:95], a[116:119], 1.0\n"
"s_add_u32 s67, %20, 46080\n"
"ds_read_b128 a[116:119], v13 offset:20480\n"
"v_mfma_f32_16x16x32_bf16 v[236:239], a[88:91], a[112:115], v[236:239]\n"
"ds_read_b128 a[112:115], v12 offset:20480\n"
"v_mfma_f32_16x16x32_bf16 v[240:243], a[88:91], a[120:123], 1.0\n"
"ds_read_b64_tr_b16 a[46:47], v9 offset:37384\n"
"buffer_load_dwordx4 v11, s[96:99], s67 offen offset:3072  lds\n"
"v_mfma_f32_16x16x32_bf16 v[240:243], a[92:95], a[124:127], v[240:243]\n"
"ds_read_b64_tr_b16 a[48:49], v15 offset:32768\n"
"v_mfma_f32_16x16x32_bf16 v[212:215], a[84:87], a[124:127], 1.0\n"
"ds_read_b128 a[124:127], v13 offset:22528\n"
"v_mfma_f32_16x16x32_bf16 v[212:215], a[80:83], a[120:123], v[212:215]\n"
"ds_read_b128 a[120:123], v12 offset:22528\n"
"v_mfma_f32_16x16x32_bf16 v[216:219], a[80:83], a[128:131], 1.0\n"
"s_add_u32 m0, s64, 8192\n"
"ds_read_b64_tr_b16 a[50:51], v15 offset:33280\n"
"v_mfma_f32_16x16x32_bf16 v[216:219], a[84:87], a[132:135], v[216:219]\n"
"ds_read_b64_tr_b16 a[52:53], v15 offset:36864\n"
"v_mfma_f32_16x16x32_bf16 v[244:247], a[92:95], a[132:135], 1.0\n"
"ds_read_b128 a[132:135], v13 offset:24576\n"
"v_mfma_f32_16x16x32_bf16 v[244:247], a[88:91], a[128:131], v[244:247]\n"
"ds_read_b128 a[128:131], v12 offset:24576\n"
"v_mfma_f32_16x16x32_bf16 v[248:251], a[88:91], a[136:139], 1.0\n"
"ds_read_b64_tr_b16 a[54:55], v15 offset:37376\n"
"v_mfma_f32_16x16x32_bf16 v[248:251], a[92:95], a[140:143], v[248:251]\n"
"ds_read_b64_tr_b16 a[56:57], v15 offset:32776\n"
"v_mfma_f32_16x16x32_bf16 v[220:223], a[84:87], a[140:143], 1.0\n"
"ds_read_b128 a[140:143], v13 offset:26624\n"
"v_mfma_f32_16x16x32_bf16 v[220:223], a[80:83], a[136:139], v[220:223]\n"
"s_add_u32 s67, s75, 262144\n"
"ds_read_b128 a[136:139], v12 offset:26624\n"
"v_mfma_f32_16x16x32_bf16 v[224:227], a[80:83], a[144:147], 1.0\n"
"ds_read_b64_tr_b16 a[58:59], v15 offset:33288\n"
"v_mfma_f32_16x16x32_bf16 v[224:227], a[84:87], a[148:151], v[224:227]\n"
"ds_read_b64_tr_b16 a[60:61], v15 offset:36872\n"
"v_mfma_f32_16x16x32_bf16 v[252:255], a[92:95], a[148:151], 1.0\n"
"ds_read_b128 a[148:151], v13 offset:28672\n"
"v_mfma_f32_16x16x32_bf16 v[252:255], a[88:91], a[144:147], v[252:255]\n"
"ds_read_b128 a[144:147], v12 offset:28672\n"
"ds_read_b64_tr_b16 a[62:63], v15 offset:37384\n"
"v_mfma_f32_16x16x32_bf16 a[28:31], a[88:91], a[152:155], 1.0\n"
"v_mfma_f32_16x16x32_bf16 a[28:31], a[92:95], a[156:159], a[28:31]\n"
"v_mfma_f32_16x16x32_bf16 a[24:27], a[84:87], a[156:159], 1.0\n"
"ds_read_b128 a[156:159], v13 offset:30720\n"
"v_mfma_f32_16x16x32_bf16 a[24:27], a[80:83], a[152:155], a[24:27]\n"
"s_waitcnt lgkmcnt(15)\n"
"ds_read_b128 a[152:155], v12 offset:30720\n"
"v_mfma_f32_16x16x32_bf16 v[32:35], a[32:35], a[96:99], v[32:35]\n"
"buffer_load_dwordx4 v11, s[52:55], s67 offen offset:0 lds\n"
"v_mfma_f32_16x16x32_bf16 v[32:35], a[36:39], a[100:103], v[32:35]\n"
"s_waitcnt lgkmcnt(2)\n"
"v_mfma_f32_16x16x32_bf16 v[60:63], a[44:47], a[100:103], v[60:63]\n"
"s_add_u32 s67, s75, 277504\n"
"v_mfma_f32_16x16x32_bf16 v[60:63], a[40:43], a[96:99], v[60:63]\n"
"v_mfma_f32_16x16x32_bf16 v[64:67], a[40:43], a[104:107], v[64:67]\n"
"buffer_load_dwordx4 v11, s[52:55], s67 offen offset:1024 lds\n"
"v_mfma_f32_16x16x32_bf16 v[64:67], a[44:47], a[108:111], v[64:67]\n"
"v_mfma_f32_16x16x32_bf16 v[36:39], a[36:39], a[108:111], v[36:39]\n"
"s_add_u32 s67, s75, 292864\n"
"v_mfma_f32_16x16x32_bf16 v[36:39], a[32:35], a[104:107], v[36:39]\n"
"v_mfma_f32_16x16x32_bf16 v[40:43], a[32:35], a[112:115], v[40:43]\n"
"v_mfma_f32_16x16x32_bf16 v[40:43], a[36:39], a[116:119], v[40:43]\n"
"buffer_load_dwordx4 v11, s[52:55], s67 offen offset:2048 lds\n"
"v_mfma_f32_16x16x32_bf16 v[68:71], a[44:47], a[116:119], v[68:71]\n"
"v_mfma_f32_16x16x32_bf16 v[68:71], a[40:43], a[112:115], v[68:71]\n"
"s_add_u32 s67, s75, 308224\n"
"v_mfma_f32_16x16x32_bf16 v[72:75], a[40:43], a[120:123], v[72:75]\n"
"v_mfma_f32_16x16x32_bf16 v[72:75], a[44:47], a[124:127], v[72:75]\n"
"v_mfma_f32_16x16x32_bf16 v[44:47], a[36:39], a[124:127], v[44:47]\n"
"v_mov_b32 v16, -1\n"
"v_mfma_f32_16x16x32_bf16 v[44:47], a[32:35], a[120:123], v[44:47]\n"
"buffer_load_dwordx4 v11, s[52:55], s67 offen offset:3072 lds\n"
"v_mfma_f32_16x16x32_bf16 v[48:51], a[32:35], a[128:131], v[48:51]\n"
"s_add_u32 m0, s64, 12288\n"
"v_mfma_f32_16x16x32_bf16 v[48:51], a[36:39], a[132:135], v[48:51]\n"
"s_add_u32 s67, s75, 327680\n"
"v_lshrrev_b32 v17, 2, v0\n"
"v_mfma_f32_16x16x32_bf16 v[76:79], a[44:47], a[132:135], v[76:79]\n"
"s_waitcnt vmcnt(12)\n"
"ds_read_b64_tr_b16 a[64:65], v9 offset:0\n"
"v_mfma_f32_16x16x32_bf16 v[76:79], a[40:43], a[128:131], v[76:79]\n"
"ds_read_b64_tr_b16 a[66:67], v9 offset:512\n"
"buffer_load_dwordx4 v11, s[52:55], s67 offen offset:0 lds\n"
"v_mfma_f32_16x16x32_bf16 v[80:83], a[40:43], a[136:139], v[80:83]\n"
"ds_read_b64_tr_b16 a[68:69], v9 offset:4096\n"
"v_mfma_f32_16x16x32_bf16 v[80:83], a[44:47], a[140:143], v[80:83]\n"
"s_add_u32 s67, s75, 343040\n"
"ds_read_b64_tr_b16 a[70:71], v9 offset:4608\n"
"v_mfma_f32_16x16x32_bf16 v[52:55], a[36:39], a[140:143], v[52:55]\n"
"ds_read_b64_tr_b16 a[72:73], v9 offset:8\n"
"v_mfma_f32_16x16x32_bf16 v[52:55], a[32:35], a[136:139], v[52:55]\n"
"s_waitcnt lgkmcnt(5)\n"
"ds_read_b64_tr_b16 a[74:75], v9 offset:520\n"
"buffer_load_dwordx4 v11, s[52:55], s67 offen offset:1024 lds\n"
"v_mfma_f32_16x16x32_bf16 v[56:59], a[32:35], a[144:147], v[56:59]\n"
"s_add_u32 s67, s75, 358400\n"
"ds_read_b64_tr_b16 a[76:77], v9 offset:4104\n"
"v_mfma_f32_16x16x32_bf16 v[56:59], a[36:39], a[148:151], v[56:59]\n"
"s_add_u32 s69, %11, 8192\n"
"ds_read_b64_tr_b16 a[78:79], v9 offset:4616\n"
"v_mfma_f32_16x16x32_bf16 v[84:87], a[44:47], a[148:151], v[84:87]\n"
"buffer_load_dwordx4 v11, s[52:55], s67 offen offset:2048 lds\n"
"v_mfma_f32_16x16x32_bf16 v[84:87], a[40:43], a[144:147], v[84:87]\n"
"ds_read_b64_tr_b16 a[80:81], v15 offset:0\n"
"v_mfma_f32_16x16x32_bf16 a[4:7], a[40:43], a[152:155], a[4:7]\n"
"s_add_u32 s67, s75, 373760\n"
"ds_read_b64_tr_b16 a[82:83], v15 offset:512\n"
"v_mfma_f32_16x16x32_bf16 a[4:7], a[44:47], a[156:159], a[4:7]\n"
"ds_read_b64_tr_b16 a[84:85], v15 offset:4096\n"
"v_mfma_f32_16x16x32_bf16 a[0:3], a[36:39], a[156:159], a[0:3]\n"
"ds_read_b64_tr_b16 a[86:87], v15 offset:4608\n"
"v_mfma_f32_16x16x32_bf16 a[0:3], a[32:35], a[152:155], a[0:3]\n"
"ds_read_b64_tr_b16 a[88:89], v15 offset:8\n"
"v_mfma_f32_16x16x32_bf16 v[88:91], a[48:51], a[96:99], v[88:91]\n"
"ds_read_b64_tr_b16 a[90:91], v15 offset:520\n"
"buffer_load_dwordx4 v11, s[52:55], s67 offen offset:3072 lds\n"
"v_mfma_f32_16x16x32_bf16 v[88:91], a[52:55], a[100:103], v[88:91]\n"
"ds_read_b64_tr_b16 a[92:93], v15 offset:4104\n"
"v_mfma_f32_16x16x32_bf16 v[116:119], a[60:63], a[100:103], v[116:119]\n"
"s_add_u32 m0, s64, 0\n"
"ds_read_b64_tr_b16 a[94:95], v15 offset:4616\n"
"v_mfma_f32_16x16x32_bf16 v[116:119], a[56:59], a[96:99], v[116:119]\n"
"v_mfma_f32_16x16x32_bf16 v[120:123], a[56:59], a[104:107], v[120:123]\n"
"v_mfma_f32_16x16x32_bf16 v[120:123], a[60:63], a[108:111], v[120:123]\n"
"v_mfma_f32_16x16x32_bf16 v[92:95], a[52:55], a[108:111], v[92:95]\n"
"s_add_u32 s67, s75, 262272\n"
"v_mfma_f32_16x16x32_bf16 v[92:95], a[48:51], a[104:107], v[92:95]\n"
"v_mfma_f32_16x16x32_bf16 v[96:99], a[48:51], a[112:115], v[96:99]\n"
"s_waitcnt lgkmcnt(0)\n"
"v_mfma_f32_16x16x32_bf16 v[96:99], a[52:55], a[116:119], v[96:99]\n"
"buffer_load_dwordx4 v11, s[52:55], s67 offen offset:0 lds\n"
"v_mfma_f32_16x16x32_bf16 v[124:127], a[60:63], a[116:119], v[124:127]\n"
"v_mfma_f32_16x16x32_bf16 v[124:127], a[56:59], a[112:115], v[124:127]\n"
"s_add_u32 s67, s75, 277632\n"
"v_mfma_f32_16x16x32_bf16 v[128:131], a[56:59], a[120:123], v[128:131]\n"
"v_mfma_f32_16x16x32_bf16 v[128:131], a[60:63], a[124:127], v[128:131]\n"
"buffer_load_dwordx4 v11, s[52:55], s67 offen offset:1024 lds\n"
"v_mfma_f32_16x16x32_bf16 v[100:103], a[52:55], a[124:127], v[100:103]\n"
"v_mfma_f32_16x16x32_bf16 v[100:103], a[48:51], a[120:123], v[100:103]\n"
"s_add_u32 s67, s75, 292992\n"
"v_mfma_f32_16x16x32_bf16 v[104:107], a[48:51], a[128:131], v[104:107]\n"
"v_mfma_f32_16x16x32_bf16 v[104:107], a[52:55], a[132:135], v[104:107]\n"
"v_mfma_f32_16x16x32_bf16 v[132:135], a[60:63], a[132:135], v[132:135]\n"
"buffer_load_dwordx4 v11, s[52:55], s67 offen offset:2048 lds\n"
"v_mfma_f32_16x16x32_bf16 v[132:135], a[56:59], a[128:131], v[132:135]\n"
"v_mfma_f32_16x16x32_bf16 v[136:139], a[56:59], a[136:139], v[136:139]\n"
"s_add_u32 s67, s75, 308352\n"
"v_mfma_f32_16x16x32_bf16 v[136:139], a[60:63], a[140:143], v[136:139]\n"
"v_mfma_f32_16x16x32_bf16 v[108:111], a[52:55], a[140:143], v[108:111]\n"
"buffer_load_dwordx4 v11, s[52:55], s67 offen offset:3072 lds\n"
"v_mfma_f32_16x16x32_bf16 v[108:111], a[48:51], a[136:139], v[108:111]\n"
"v_mfma_f32_16x16x32_bf16 v[112:115], a[48:51], a[144:147], v[112:115]\n"
"s_add_u32 m0, s64, 4096\n"
"v_mfma_f32_16x16x32_bf16 v[112:115], a[52:55], a[148:151], v[112:115]\n"
"s_sub_u32 s71, s72, s64\n"
"v_mfma_f32_16x16x32_bf16 v[140:143], a[60:63], a[148:151], v[140:143]\n"
"s_add_u32 s67, s75, 327808\n"
"v_mfma_f32_16x16x32_bf16 v[140:143], a[56:59], a[144:147], v[140:143]\n"
"buffer_load_dwordx4 v11, s[52:55], s67 offen offset:0 lds\n"
"v_mfma_f32_16x16x32_bf16 a[12:15], a[56:59], a[152:155], a[12:15]\n"
"s_add_u32 s71, s71, 139264\n"
"v_mfma_f32_16x16x32_bf16 a[12:15], a[60:63], a[156:159], a[12:15]\n"
"s_add_u32 s67, s75, 343168\n"
"v_mfma_f32_16x16x32_bf16 a[8:11], a[52:55], a[156:159], a[8:11]\n"
"v_add_u32 v20, s71, v9\n"
"v_mfma_f32_16x16x32_bf16 a[8:11], a[48:51], a[152:155], a[8:11]\n"
"v_mfma_f32_16x16x32_bf16 v[144:147], a[64:67], a[96:99], v[144:147]\n"
"buffer_load_dwordx4 v11, s[52:55], s67 offen offset:1024 lds\n"
"v_mfma_f32_16x16x32_bf16 v[144:147], a[68:71], a[100:103], v[144:147]\n"
"v_add_u32 v21, s71, v15\n"
"v_mfma_f32_16x16x32_bf16 v[172:175], a[76:79], a[100:103], v[172:175]\n"
"s_add_u32 s67, s75, 358528\n"
"v_mfma_f32_16x16x32_bf16 v[172:175], a[72:75], a[96:99], v[172:175]\n"
"v_mfma_f32_16x16x32_bf16 v[176:179], a[72:75], a[104:107], v[176:179]\n"
"s_waitcnt vmcnt(14)\n"
"ds_read_b64_tr_b16 a[160:161], v20 offset:0\n"
"v_mfma_f32_16x16x32_bf16 v[176:179], a[76:79], a[108:111], v[176:179]\n"
"buffer_load_dwordx4 v11, s[52:55], s67 offen offset:2048 lds\n"
"v_mfma_f32_16x16x32_bf16 v[148:151], a[68:71], a[108:111], v[148:151]\n"
"ds_read_b64_tr_b16 a[162:163], v20 offset:512\n"
"v_mfma_f32_16x16x32_bf16 v[148:151], a[64:67], a[104:107], v[148:151]\n"
"s_add_u32 s67, s75, 373888\n"
"ds_read_b64_tr_b16 a[164:165], v20 offset:8\n"
"v_mfma_f32_16x16x32_bf16 v[152:155], a[64:67], a[112:115], v[152:155]\n"
"s_sub_u32 s66, 81920, s73\n"
"ds_read_b64_tr_b16 a[166:167], v20 offset:520\n"
"v_mfma_f32_16x16x32_bf16 v[152:155], a[68:71], a[116:119], v[152:155]\n"
"ds_read_b64_tr_b16 a[168:169], v21 offset:0\n"
"buffer_load_dwordx4 v11, s[52:55], s67 offen offset:3072 lds\n"
"v_mfma_f32_16x16x32_bf16 v[180:183], a[76:79], a[116:119], v[180:183]\n"
"v_add_u32 v12, s66, v7\n"
"v_mfma_f32_16x16x32_bf16 v[180:183], a[72:75], a[112:115], v[180:183]\n"
"ds_read_b64_tr_b16 a[170:171], v21 offset:512\n"
"v_mfma_f32_16x16x32_bf16 v[184:187], a[72:75], a[120:123], v[184:187]\n"
"s_add_u32 m0, s72, 139264\n"
"v_add_u32 v13, s66, v8\n"
"v_mfma_f32_16x16x32_bf16 v[184:187], a[76:79], a[124:127], v[184:187]\n"
"ds_read_b64_tr_b16 a[172:173], v21 offset:8\n"
"v_mfma_f32_16x16x32_bf16 v[156:159], a[68:71], a[124:127], v[156:159]\n"
"ds_read_b64_tr_b16 a[174:175], v21 offset:520\n"
"v_mfma_f32_16x16x32_bf16 v[156:159], a[64:67], a[120:123], v[156:159]\n"
"v_mfma_f32_16x16x32_bf16 v[160:163], a[64:67], a[128:131], v[160:163]\n"
"s_add_u32 s67, s60, 384\n"
"v_mfma_f32_16x16x32_bf16 v[160:163], a[68:71], a[132:135], v[160:163]\n"
"v_mfma_f32_16x16x32_bf16 v[188:191], a[76:79], a[132:135], v[188:191]\n"
"v_mfma_f32_16x16x32_bf16 v[188:191], a[72:75], a[128:131], v[188:191]\n"
"v_mfma_f32_16x16x32_bf16 v[192:195], a[72:75], a[136:139], v[192:195]\n"
"s_waitcnt lgkmcnt(0)\n"
"v_mfma_f32_16x16x32_bf16 v[192:195], a[76:79], a[140:143], v[192:195]\n"
"buffer_load_dwordx4 v6, s[48:51], s67 offen offset:0  lds\n"
"v_mfma_f32_16x16x32_bf16 v[164:167], a[68:71], a[140:143], v[164:167]\n"
"v_mfma_f32_16x16x32_bf16 v[164:167], a[64:67], a[136:139], v[164:167]\n"
"s_add_u32 s67, s60, 15744\n"
"v_mfma_f32_16x16x32_bf16 v[168:171], a[64:67], a[144:147], v[168:171]\n"
"v_mfma_f32_16x16x32_bf16 v[168:171], a[68:71], a[148:151], v[168:171]\n"
"v_mfma_f32_16x16x32_bf16 v[196:199], a[76:79], a[148:151], v[196:199]\n"
"buffer_load_dwordx4 v6, s[48:51], s67 offen offset:1024  lds\n"
"v_mfma_f32_16x16x32_bf16 v[196:199], a[72:75], a[144:147], v[196:199]\n"
"v_mfma_f32_16x16x32_bf16 a[20:23], a[72:75], a[152:155], a[20:23]\n"
"s_add_u32 s67, s60, 31104\n"
"v_mfma_f32_16x16x32_bf16 a[20:23], a[76:79], a[156:159], a[20:23]\n"
"v_mfma_f32_16x16x32_bf16 a[16:19], a[68:71], a[156:159], a[16:19]\n"
"buffer_load_dwordx4 v6, s[48:51], s67 offen offset:2048  lds\n"
"v_mfma_f32_16x16x32_bf16 a[16:19], a[64:67], a[152:155], a[16:19]\n"
"s_barrier\n"
"s_waitcnt vmcnt(11)\n"
"ds_read_b64_tr_b16 a[32:33], v9 offset:8192\n"
"v_mfma_f32_16x16x32_bf16 v[200:203], a[80:83], a[96:99], v[200:203]\n"
"s_add_u32 s67, s60, 46464\n"
"ds_read_b64_tr_b16 a[34:35], v9 offset:8704\n"
"v_mfma_f32_16x16x32_bf16 v[200:203], a[84:87], a[100:103], v[200:203]\n"
"ds_read_b64_tr_b16 a[36:37], v9 offset:12288\n"
"v_mfma_f32_16x16x32_bf16 v[228:231], a[92:95], a[100:103], v[228:231]\n"
"ds_read_b128 a[100:103], v13 offset:16384\n"
"v_mfma_f32_16x16x32_bf16 v[228:231], a[88:91], a[96:99], v[228:231]\n"
"ds_read_b128 a[96:99], v12 offset:16384\n"
"buffer_load_dwordx4 v6, s[48:51], s67 offen offset:3072  lds\n"
"v_mfma_f32_16x16x32_bf16 v[232:235], a[88:91], a[104:107], v[232:235]\n"
"ds_read_b64_tr_b16 a[38:39], v9 offset:12800\n"
"v_mfma_f32_16x16x32_bf16 v[232:235], a[92:95], a[108:111], v[232:235]\n"
"s_add_u32 s70, s75, 655360\n"
"ds_read_b64_tr_b16 a[40:41], v9 offset:8200\n"
"v_mfma_f32_16x16x32_bf16 v[204:207], a[84:87], a[108:111], v[204:207]\n"
"ds_read_b128 a[108:111], v13 offset:18432\n"
"v_mfma_f32_16x16x32_bf16 v[204:207], a[80:83], a[104:107], v[204:207]\n"
"s_add_u32 s70, s70, s65\n"
"ds_read_b128 a[104:107], v12 offset:18432\n"
"v_mfma_f32_16x16x32_bf16 v[208:211], a[80:83], a[112:115], v[208:211]\n"
"ds_read_b64_tr_b16 a[42:43], v9 offset:8712\n"
"v_mfma_f32_16x16x32_bf16 v[208:211], a[84:87], a[116:119], v[208:211]\n"
"ds_read_b64_tr_b16 a[44:45], v9 offset:12296\n"
"buffer_load_dword v23, v14, s[52:55], s70 offen\n"
"v_mfma_f32_16x16x32_bf16 v[236:239], a[92:95], a[116:119], v[236:239]\n"
"ds_read_b128 a[116:119], v13 offset:20480\n"
"v_mfma_f32_16x16x32_bf16 v[236:239], a[88:91], a[112:115], v[236:239]\n"
"buffer_store_dwordx4 a[160:163], v0, s[36:39], %22 offen offset:0 sc1\n"
"ds_read_b128 a[112:115], v12 offset:20480\n"
"v_mfma_f32_16x16x32_bf16 v[240:243], a[88:91], a[120:123], v[240:243]\n"
"ds_read_b64_tr_b16 a[46:47], v9 offset:12808\n"
"v_mfma_f32_16x16x32_bf16 v[240:243], a[92:95], a[124:127], v[240:243]\n"
"ds_read_b64_tr_b16 a[48:49], v15 offset:8192\n"
"v_mfma_f32_16x16x32_bf16 v[212:215], a[84:87], a[124:127], v[212:215]\n"
"ds_read_b128 a[124:127], v13 offset:22528\n"
"v_mfma_f32_16x16x32_bf16 v[212:215], a[80:83], a[120:123], v[212:215]\n"
"ds_read_b128 a[120:123], v12 offset:22528\n"
"v_mfma_f32_16x16x32_bf16 v[216:219], a[80:83], a[128:131], v[216:219]\n"
"s_add_u32 m0, s64, 8192\n"
"ds_read_b64_tr_b16 a[50:51], v15 offset:8704\n"
"v_mfma_f32_16x16x32_bf16 v[216:219], a[84:87], a[132:135], v[216:219]\n"
"ds_read_b64_tr_b16 a[52:53], v15 offset:12288\n"
"v_mfma_f32_16x16x32_bf16 v[244:247], a[92:95], a[132:135], v[244:247]\n"
"ds_read_b128 a[132:135], v13 offset:24576\n"
"v_mfma_f32_16x16x32_bf16 v[244:247], a[88:91], a[128:131], v[244:247]\n"
"ds_read_b128 a[128:131], v12 offset:24576\n"
"v_mfma_f32_16x16x32_bf16 v[248:251], a[88:91], a[136:139], v[248:251]\n"
"ds_read_b64_tr_b16 a[54:55], v15 offset:12800\n"
"v_mfma_f32_16x16x32_bf16 v[248:251], a[92:95], a[140:143], v[248:251]\n"
"ds_read_b64_tr_b16 a[56:57], v15 offset:8200\n"
"v_mfma_f32_16x16x32_bf16 v[220:223], a[84:87], a[140:143], v[220:223]\n"
"ds_read_b128 a[140:143], v13 offset:26624\n"
"v_mfma_f32_16x16x32_bf16 v[220:223], a[80:83], a[136:139], v[220:223]\n"
"ds_read_b128 a[136:139], v12 offset:26624\n"
"v_mfma_f32_16x16x32_bf16 v[224:227], a[80:83], a[144:147], v[224:227]\n"
"s_add_u32 s67, s75, 393216\n"
"ds_read_b64_tr_b16 a[58:59], v15 offset:8712\n"
"v_mfma_f32_16x16x32_bf16 v[224:227], a[84:87], a[148:151], v[224:227]\n"
"ds_read_b64_tr_b16 a[60:61], v15 offset:12296\n"
"v_mfma_f32_16x16x32_bf16 v[252:255], a[92:95], a[148:151], v[252:255]\n"
"ds_read_b128 a[148:151], v13 offset:28672\n"
"v_mfma_f32_16x16x32_bf16 v[252:255], a[88:91], a[144:147], v[252:255]\n"
"ds_read_b128 a[144:147], v12 offset:28672\n"
"ds_read_b64_tr_b16 a[62:63], v15 offset:12808\n"
"v_mfma_f32_16x16x32_bf16 a[28:31], a[88:91], a[152:155], a[28:31]\n"
"v_mfma_f32_16x16x32_bf16 a[28:31], a[92:95], a[156:159], a[28:31]\n"
"v_mfma_f32_16x16x32_bf16 a[24:27], a[84:87], a[156:159], a[24:27]\n"
"ds_read_b128 a[156:159], v13 offset:30720\n"
"v_mfma_f32_16x16x32_bf16 a[24:27], a[80:83], a[152:155], a[24:27]\n"
"s_waitcnt lgkmcnt(15)\n"
"ds_read_b128 a[152:155], v12 offset:30720\n"
"v_mfma_f32_16x16x32_bf16 v[32:35], a[32:35], a[96:99], v[32:35]\n"
"v_mfma_f32_16x16x32_bf16 v[32:35], a[36:39], a[100:103], v[32:35]\n"
"s_waitcnt lgkmcnt(2)\n"
"v_mfma_f32_16x16x32_bf16 v[60:63], a[44:47], a[100:103], v[60:63]\n"
"buffer_load_dwordx4 v11, s[52:55], s67 offen offset:0 lds\n"
"v_mfma_f32_16x16x32_bf16 v[60:63], a[40:43], a[96:99], v[60:63]\n"
"v_mfma_f32_16x16x32_bf16 v[64:67], a[40:43], a[104:107], v[64:67]\n"
"s_add_u32 s67, s75, 408576\n"
"v_mfma_f32_16x16x32_bf16 v[64:67], a[44:47], a[108:111], v[64:67]\n"
"v_mfma_f32_16x16x32_bf16 v[36:39], a[36:39], a[108:111], v[36:39]\n"
"v_mfma_f32_16x16x32_bf16 v[36:39], a[32:35], a[104:107], v[36:39]\n"
"buffer_load_dwordx4 v11, s[52:55], s67 offen offset:1024 lds\n"
"v_mfma_f32_16x16x32_bf16 v[40:43], a[32:35], a[112:115], v[40:43]\n"
"v_mfma_f32_16x16x32_bf16 v[40:43], a[36:39], a[116:119], v[40:43]\n"
"s_add_u32 s67, s75, 423936\n"
"v_mfma_f32_16x16x32_bf16 v[68:71], a[44:47], a[116:119], v[68:71]\n"
"v_mfma_f32_16x16x32_bf16 v[68:71], a[40:43], a[112:115], v[68:71]\n"
"v_mfma_f32_16x16x32_bf16 v[72:75], a[40:43], a[120:123], v[72:75]\n"
"buffer_load_dwordx4 v11, s[52:55], s67 offen offset:2048 lds\n"
"v_mfma_f32_16x16x32_bf16 v[72:75], a[44:47], a[124:127], v[72:75]\n"
"v_mfma_f32_16x16x32_bf16 v[44:47], a[36:39], a[124:127], v[44:47]\n"
"s_add_u32 s67, s75, 439296\n"
"v_mfma_f32_16x16x32_bf16 v[44:47], a[32:35], a[120:123], v[44:47]\n"
"v_mfma_f32_16x16x32_bf16 v[48:51], a[32:35], a[128:131], v[48:51]\n"
"buffer_load_dwordx4 v11, s[52:55], s67 offen offset:3072 lds\n"
"v_mfma_f32_16x16x32_bf16 v[48:51], a[36:39], a[132:135], v[48:51]\n"
"s_add_u32 m0, s64, 12288\n"
"v_mfma_f32_16x16x32_bf16 v[76:79], a[44:47], a[132:135], v[76:79]\n"
"s_waitcnt vmcnt(10)\n"
"ds_read_b64_tr_b16 a[64:65], v9 offset:0\n"
"v_mfma_f32_16x16x32_bf16 v[76:79], a[40:43], a[128:131], v[76:79]\n"
"ds_read_b64_tr_b16 a[66:67], v9 offset:512\n"
"v_mfma_f32_16x16x32_bf16 v[80:83], a[40:43], a[136:139], v[80:83]\n"
"s_add_u32 s67, s75, 458752\n"
"ds_read_b64_tr_b16 a[68:69], v9 offset:4096\n"
"v_mfma_f32_16x16x32_bf16 v[80:83], a[44:47], a[140:143], v[80:83]\n"
"ds_read_b64_tr_b16 a[70:71], v9 offset:4608\n"
"v_mfma_f32_16x16x32_bf16 v[52:55], a[36:39], a[140:143], v[52:55]\n"
"ds_read_b64_tr_b16 a[72:73], v9 offset:8\n"
"buffer_load_dwordx4 v11, s[52:55], s67 offen offset:0 lds\n"
"v_mfma_f32_16x16x32_bf16 v[52:55], a[32:35], a[136:139], v[52:55]\n"
"s_waitcnt lgkmcnt(5)\n"
"ds_read_b64_tr_b16 a[74:75], v9 offset:520\n"
"v_mfma_f32_16x16x32_bf16 v[56:59], a[32:35], a[144:147], v[56:59]\n"
"s_add_u32 s67, s75, 474112\n"
"ds_read_b64_tr_b16 a[76:77], v9 offset:4104\n"
"v_mfma_f32_16x16x32_bf16 v[56:59], a[36:39], a[148:151], v[56:59]\n"
"ds_read_b64_tr_b16 a[78:79], v9 offset:4616\n"
"v_mfma_f32_16x16x32_bf16 v[84:87], a[44:47], a[148:151], v[84:87]\n"
"buffer_load_dwordx4 v11, s[52:55], s67 offen offset:1024 lds\n"
"v_mfma_f32_16x16x32_bf16 v[84:87], a[40:43], a[144:147], v[84:87]\n"
"ds_read_b64_tr_b16 a[80:81], v15 offset:0\n"
"v_mfma_f32_16x16x32_bf16 a[4:7], a[40:43], a[152:155], a[4:7]\n"
"s_add_u32 s67, s75, 489472\n"
"ds_read_b64_tr_b16 a[82:83], v15 offset:512\n"
"v_mfma_f32_16x16x32_bf16 a[4:7], a[44:47], a[156:159], a[4:7]\n"
"ds_read_b64_tr_b16 a[84:85], v15 offset:4096\n"
"v_mfma_f32_16x16x32_bf16 a[0:3], a[36:39], a[156:159], a[0:3]\n"
"ds_read_b64_tr_b16 a[86:87], v15 offset:4608\n"
"buffer_load_dwordx4 v11, s[52:55], s67 offen offset:2048 lds\n"
"v_mfma_f32_16x16x32_bf16 a[0:3], a[32:35], a[152:155], a[0:3]\n"
"ds_read_b64_tr_b16 a[88:89], v15 offset:8\n"
"v_mfma_f32_16x16x32_bf16 v[88:91], a[48:51], a[96:99], v[88:91]\n"
"s_add_u32 s67, s75, 504832\n"
"ds_read_b64_tr_b16 a[90:91], v15 offset:520\n"
"v_mfma_f32_16x16x32_bf16 v[88:91], a[52:55], a[100:103], v[88:91]\n"
"ds_read_b64_tr_b16 a[92:93], v15 offset:4104\n"
"v_mfma_f32_16x16x32_bf16 v[116:119], a[60:63], a[100:103], v[116:119]\n"
"ds_read_b64_tr_b16 a[94:95], v15 offset:4616\n"
"v_mfma_f32_16x16x32_bf16 v[116:119], a[56:59], a[96:99], v[116:119]\n"
"buffer_load_dwordx4 v11, s[52:55], s67 offen offset:3072 lds\n"
"v_mfma_f32_16x16x32_bf16 v[120:123], a[56:59], a[104:107], v[120:123]\n"
"v_mfma_f32_16x16x32_bf16 v[120:123], a[60:63], a[108:111], v[120:123]\n"
"s_add_u32 m0, s64, 0\n"
"v_mfma_f32_16x16x32_bf16 v[92:95], a[52:55], a[108:111], v[92:95]\n"
"v_mfma_f32_16x16x32_bf16 v[92:95], a[48:51], a[104:107], v[92:95]\n"
"s_add_u32 s67, s75, 393344\n"
"v_mfma_f32_16x16x32_bf16 v[96:99], a[48:51], a[112:115], v[96:99]\n"
"s_waitcnt lgkmcnt(0)\n"
"v_mfma_f32_16x16x32_bf16 v[96:99], a[52:55], a[116:119], v[96:99]\n"
"buffer_load_dwordx4 v11, s[52:55], s67 offen offset:0 lds\n"
"v_mfma_f32_16x16x32_bf16 v[124:127], a[60:63], a[116:119], v[124:127]\n"
"v_mfma_f32_16x16x32_bf16 v[124:127], a[56:59], a[112:115], v[124:127]\n"
"s_add_u32 s67, s75, 408704\n"
"v_mfma_f32_16x16x32_bf16 v[128:131], a[56:59], a[120:123], v[128:131]\n"
"v_mfma_f32_16x16x32_bf16 v[128:131], a[60:63], a[124:127], v[128:131]\n"
"v_mfma_f32_16x16x32_bf16 v[100:103], a[52:55], a[124:127], v[100:103]\n"
"buffer_load_dwordx4 v11, s[52:55], s67 offen offset:1024 lds\n"
"v_mfma_f32_16x16x32_bf16 v[100:103], a[48:51], a[120:123], v[100:103]\n"
"v_mfma_f32_16x16x32_bf16 v[104:107], a[48:51], a[128:131], v[104:107]\n"
"s_add_u32 s67, s75, 424064\n"
"v_mfma_f32_16x16x32_bf16 v[104:107], a[52:55], a[132:135], v[104:107]\n"
"v_mfma_f32_16x16x32_bf16 v[132:135], a[60:63], a[132:135], v[132:135]\n"
"v_mfma_f32_16x16x32_bf16 v[132:135], a[56:59], a[128:131], v[132:135]\n"
"buffer_load_dwordx4 v11, s[52:55], s67 offen offset:2048 lds\n"
"v_mfma_f32_16x16x32_bf16 v[136:139], a[56:59], a[136:139], v[136:139]\n"
"v_mfma_f32_16x16x32_bf16 v[136:139], a[60:63], a[140:143], v[136:139]\n"
"s_add_u32 s67, s75, 439424\n"
"v_mfma_f32_16x16x32_bf16 v[108:111], a[52:55], a[140:143], v[108:111]\n"
"v_mfma_f32_16x16x32_bf16 v[108:111], a[48:51], a[136:139], v[108:111]\n"
"buffer_load_dwordx4 v11, s[52:55], s67 offen offset:3072 lds\n"
"v_mfma_f32_16x16x32_bf16 v[112:115], a[48:51], a[144:147], v[112:115]\n"
"v_mfma_f32_16x16x32_bf16 v[112:115], a[52:55], a[148:151], v[112:115]\n"
"s_add_u32 m0, s64, 4096\n"
"v_mfma_f32_16x16x32_bf16 v[140:143], a[60:63], a[148:151], v[140:143]\n"
"v_mfma_f32_16x16x32_bf16 v[140:143], a[56:59], a[144:147], v[140:143]\n"
"s_add_u32 s67, s75, 458880\n"
"v_mfma_f32_16x16x32_bf16 a[12:15], a[56:59], a[152:155], a[12:15]\n"
"v_mfma_f32_16x16x32_bf16 a[12:15], a[60:63], a[156:159], a[12:15]\n"
"buffer_load_dwordx4 v11, s[52:55], s67 offen offset:0 lds\n"
"v_mfma_f32_16x16x32_bf16 a[8:11], a[52:55], a[156:159], a[8:11]\n"
"v_mfma_f32_16x16x32_bf16 a[8:11], a[48:51], a[152:155], a[8:11]\n"
"s_add_u32 s67, s75, 474240\n"
"v_mfma_f32_16x16x32_bf16 v[144:147], a[64:67], a[96:99], v[144:147]\n"
"v_mfma_f32_16x16x32_bf16 v[144:147], a[68:71], a[100:103], v[144:147]\n"
"buffer_load_dwordx4 v11, s[52:55], s67 offen offset:1024 lds\n"
"v_mfma_f32_16x16x32_bf16 v[172:175], a[76:79], a[100:103], v[172:175]\n"
"v_mfma_f32_16x16x32_bf16 v[172:175], a[72:75], a[96:99], v[172:175]\n"
"s_add_u32 s67, s75, 489600\n"
"v_mfma_f32_16x16x32_bf16 v[176:179], a[72:75], a[104:107], v[176:179]\n"
"v_mfma_f32_16x16x32_bf16 v[176:179], a[76:79], a[108:111], v[176:179]\n"
"buffer_load_dwordx4 v11, s[52:55], s67 offen offset:2048 lds\n"
"v_mfma_f32_16x16x32_bf16 v[148:151], a[68:71], a[108:111], v[148:151]\n"
"v_mfma_f32_16x16x32_bf16 v[148:151], a[64:67], a[104:107], v[148:151]\n"
"s_add_u32 s67, s75, 504960\n"
"v_mfma_f32_16x16x32_bf16 v[152:155], a[64:67], a[112:115], v[152:155]\n"
"s_sub_u32 s66, 122880, s73\n"
"v_mfma_f32_16x16x32_bf16 v[152:155], a[68:71], a[116:119], v[152:155]\n"
"v_mfma_f32_16x16x32_bf16 v[180:183], a[76:79], a[116:119], v[180:183]\n"
"v_add_u32 v12, s66, v7\n"
"v_mfma_f32_16x16x32_bf16 v[180:183], a[72:75], a[112:115], v[180:183]\n"
"buffer_load_dwordx4 v11, s[52:55], s67 offen offset:3072 lds\n"
"v_mfma_f32_16x16x32_bf16 v[184:187], a[72:75], a[120:123], v[184:187]\n"
"s_add_u32 m0, s72, 16384\n"
"v_add_u32 v13, s66, v8\n"
"v_mfma_f32_16x16x32_bf16 v[184:187], a[76:79], a[124:127], v[184:187]\n"
"v_mfma_f32_16x16x32_bf16 v[156:159], a[68:71], a[124:127], v[156:159]\n"
"s_add_u32 s67, s60, 512\n"
"v_mfma_f32_16x16x32_bf16 v[156:159], a[64:67], a[120:123], v[156:159]\n"
"buffer_load_dwordx4 v6, s[48:51], s67 offen offset:0  lds\n"
"v_mfma_f32_16x16x32_bf16 v[160:163], a[64:67], a[128:131], v[160:163]\n"
"v_mfma_f32_16x16x32_bf16 v[160:163], a[68:71], a[132:135], v[160:163]\n"
"s_add_u32 s67, s60, 15872\n"
"v_mfma_f32_16x16x32_bf16 v[188:191], a[76:79], a[132:135], v[188:191]\n"
"v_mfma_f32_16x16x32_bf16 v[188:191], a[72:75], a[128:131], v[188:191]\n"
"buffer_load_dwordx4 v6, s[48:51], s67 offen offset:1024  lds\n"
"v_mfma_f32_16x16x32_bf16 v[192:195], a[72:75], a[136:139], v[192:195]\n"
"v_mfma_f32_16x16x32_bf16 v[192:195], a[76:79], a[140:143], v[192:195]\n"
"s_add_u32 s67, s60, 31232\n"
"v_mfma_f32_16x16x32_bf16 v[164:167], a[68:71], a[140:143], v[164:167]\n"
"v_mfma_f32_16x16x32_bf16 v[164:167], a[64:67], a[136:139], v[164:167]\n"
"buffer_load_dwordx4 v6, s[48:51], s67 offen offset:2048  lds\n"
"v_mfma_f32_16x16x32_bf16 v[168:171], a[64:67], a[144:147], v[168:171]\n"
"v_mfma_f32_16x16x32_bf16 v[168:171], a[68:71], a[148:151], v[168:171]\n"
"s_add_u32 s67, s60, 46592\n"
"v_mfma_f32_16x16x32_bf16 v[196:199], a[76:79], a[148:151], v[196:199]\n"
"v_mfma_f32_16x16x32_bf16 v[196:199], a[72:75], a[144:147], v[196:199]\n"
"v_mfma_f32_16x16x32_bf16 a[20:23], a[72:75], a[152:155], a[20:23]\n"
"buffer_load_dwordx4 v6, s[48:51], s67 offen offset:3072  lds\n"
"v_mfma_f32_16x16x32_bf16 a[20:23], a[76:79], a[156:159], a[20:23]\n"
"v_mfma_f32_16x16x32_bf16 a[16:19], a[68:71], a[156:159], a[16:19]\n"
"s_add_u32 s70, s75, 786432\n"
"s_add_u32 s70, s70, s65\n"
"v_mfma_f32_16x16x32_bf16 a[16:19], a[64:67], a[152:155], a[16:19]\n"
"s_waitcnt vmcnt(22)\n"
"s_barrier\n"
"v_mfma_f32_16x16x32_bf16 v[200:203], a[80:83], a[96:99], v[200:203]\n"
"s_waitcnt vmcnt(12)\n"
"ds_read_b64_tr_b16 a[32:33], v9 offset:8192\n"
"v_mfma_f32_16x16x32_bf16 v[200:203], a[84:87], a[100:103], v[200:203]\n"
"ds_read_b64_tr_b16 a[34:35], v9 offset:8704\n"
"v_mfma_f32_16x16x32_bf16 v[228:231], a[92:95], a[100:103], v[228:231]\n"
"ds_read_b128 a[100:103], v13 offset:16384\n"
"buffer_load_dword v23, v14, s[52:55], s70 offen\n"
"v_mfma_f32_16x16x32_bf16 v[228:231], a[88:91], a[96:99], v[228:231]\n"
"ds_read_b128 a[96:99], v12 offset:16384\n"
"v_mfma_f32_16x16x32_bf16 v[232:235], a[88:91], a[104:107], v[232:235]\n"
"ds_read_b64_tr_b16 a[36:37], v9 offset:12288\n"
"v_mfma_f32_16x16x32_bf16 v[232:235], a[92:95], a[108:111], v[232:235]\n"
"ds_read_b64_tr_b16 a[38:39], v9 offset:12800\n"
"v_mfma_f32_16x16x32_bf16 v[204:207], a[84:87], a[108:111], v[204:207]\n"
"ds_read_b128 a[108:111], v13 offset:18432\n"
"v_mfma_f32_16x16x32_bf16 v[204:207], a[80:83], a[104:107], v[204:207]\n"
"buffer_store_dwordx4 a[164:167], v0, s[36:39], %22 offen offset:2048 sc1\n"
"ds_read_b128 a[104:107], v12 offset:18432\n"
"ds_read_b64_tr_b16 a[40:41], v9 offset:8200\n"
"v_mfma_f32_16x16x32_bf16 v[208:211], a[80:83], a[112:115], v[208:211]\n"
"ds_read_b64_tr_b16 a[42:43], v9 offset:8712\n"
"v_mfma_f32_16x16x32_bf16 v[208:211], a[84:87], a[116:119], v[208:211]\n"
"ds_read_b64_tr_b16 a[44:45], v9 offset:12296\n"
"v_mfma_f32_16x16x32_bf16 v[236:239], a[92:95], a[116:119], v[236:239]\n"
"ds_read_b128 a[116:119], v13 offset:20480\n"
"v_mfma_f32_16x16x32_bf16 v[236:239], a[88:91], a[112:115], v[236:239]\n"
"ds_read_b128 a[112:115], v12 offset:20480\n"
"v_mfma_f32_16x16x32_bf16 v[240:243], a[88:91], a[120:123], v[240:243]\n"
"s_add_u32 m0, s64, 8192\n"
"ds_read_b64_tr_b16 a[46:47], v9 offset:12808\n"
"v_mfma_f32_16x16x32_bf16 v[240:243], a[92:95], a[124:127], v[240:243]\n"
"ds_read_b64_tr_b16 a[48:49], v15 offset:8192\n"
"v_mfma_f32_16x16x32_bf16 v[212:215], a[84:87], a[124:127], v[212:215]\n"
"ds_read_b128 a[124:127], v13 offset:22528\n"
"v_mfma_f32_16x16x32_bf16 v[212:215], a[80:83], a[120:123], v[212:215]\n"
"ds_read_b128 a[120:123], v12 offset:22528\n"
"v_mfma_f32_16x16x32_bf16 v[216:219], a[80:83], a[128:131], v[216:219]\n"
"ds_read_b64_tr_b16 a[50:51], v15 offset:8704\n"
"v_mfma_f32_16x16x32_bf16 v[216:219], a[84:87], a[132:135], v[216:219]\n"
"ds_read_b64_tr_b16 a[52:53], v15 offset:12288\n"
"v_mfma_f32_16x16x32_bf16 v[244:247], a[92:95], a[132:135], v[244:247]\n"
"ds_read_b128 a[132:135], v13 offset:24576\n"
"v_mfma_f32_16x16x32_bf16 v[244:247], a[88:91], a[128:131], v[244:247]\n"
"ds_read_b128 a[128:131], v12 offset:24576\n"
"v_mfma_f32_16x16x32_bf16 v[248:251], a[88:91], a[136:139], v[248:251]\n"
"ds_read_b64_tr_b16 a[54:55], v15 offset:12800\n"
"v_mfma_f32_16x16x32_bf16 v[248:251], a[92:95], a[140:143], v[248:251]\n"
"ds_read_b64_tr_b16 a[56:57], v15 offset:8200\n"
"v_mfma_f32_16x16x32_bf16 v[220:223], a[84:87], a[140:143], v[220:223]\n"
"s_add_u32 s67, s75, 524288\n"
"ds_read_b128 a[140:143], v13 offset:26624\n"
"v_mfma_f32_16x16x32_bf16 v[220:223], a[80:83], a[136:139], v[220:223]\n"
"ds_read_b128 a[136:139], v12 offset:26624\n"
"v_mfma_f32_16x16x32_bf16 v[224:227], a[80:83], a[144:147], v[224:227]\n"
"ds_read_b64_tr_b16 a[58:59], v15 offset:8712\n"
"v_mfma_f32_16x16x32_bf16 v[224:227], a[84:87], a[148:151], v[224:227]\n"
"ds_read_b64_tr_b16 a[60:61], v15 offset:12296\n"
"v_mfma_f32_16x16x32_bf16 v[252:255], a[92:95], a[148:151], v[252:255]\n"
"ds_read_b128 a[148:151], v13 offset:28672\n"
"v_mfma_f32_16x16x32_bf16 v[252:255], a[88:91], a[144:147], v[252:255]\n"
"ds_read_b128 a[144:147], v12 offset:28672\n"
"ds_read_b64_tr_b16 a[62:63], v15 offset:12808\n"
"v_mfma_f32_16x16x32_bf16 a[28:31], a[88:91], a[152:155], a[28:31]\n"
"v_mfma_f32_16x16x32_bf16 a[28:31], a[92:95], a[156:159], a[28:31]\n"
"v_mfma_f32_16x16x32_bf16 a[24:27], a[84:87], a[156:159], a[24:27]\n"
"ds_read_b128 a[156:159], v13 offset:30720\n"
"v_mfma_f32_16x16x32_bf16 a[24:27], a[80:83], a[152:155], a[24:27]\n"
"s_waitcnt lgkmcnt(15)\n"
"ds_read_b128 a[152:155], v12 offset:30720\n"
"v_mfma_f32_16x16x32_bf16 v[32:35], a[32:35], a[96:99], v[32:35]\n"
"v_mfma_f32_16x16x32_bf16 v[32:35], a[36:39], a[100:103], v[32:35]\n"
"s_waitcnt lgkmcnt(2)\n"
"v_mfma_f32_16x16x32_bf16 v[60:63], a[44:47], a[100:103], v[60:63]\n"
"buffer_load_dwordx4 v11, s[52:55], s67 offen offset:0 lds\n"
"v_mfma_f32_16x16x32_bf16 v[60:63], a[40:43], a[96:99], v[60:63]\n"
"v_mfma_f32_16x16x32_bf16 v[64:67], a[40:43], a[104:107], v[64:67]\n"
"s_add_u32 s67, s75, 539648\n"
"v_mfma_f32_16x16x32_bf16 v[64:67], a[44:47], a[108:111], v[64:67]\n"
"v_mfma_f32_16x16x32_bf16 v[36:39], a[36:39], a[108:111], v[36:39]\n"
"v_mfma_f32_16x16x32_bf16 v[36:39], a[32:35], a[104:107], v[36:39]\n"
"buffer_load_dwordx4 v11, s[52:55], s67 offen offset:1024 lds\n"
"v_mfma_f32_16x16x32_bf16 v[40:43], a[32:35], a[112:115], v[40:43]\n"
"v_mfma_f32_16x16x32_bf16 v[40:43], a[36:39], a[116:119], v[40:43]\n"
"s_add_u32 s67, s75, 555008\n"
"v_mfma_f32_16x16x32_bf16 v[68:71], a[44:47], a[116:119], v[68:71]\n"
"v_mfma_f32_16x16x32_bf16 v[68:71], a[40:43], a[112:115], v[68:71]\n"
"buffer_load_dwordx4 v11, s[52:55], s67 offen offset:2048 lds\n"
"v_mfma_f32_16x16x32_bf16 v[72:75], a[40:43], a[120:123], v[72:75]\n"
"v_mfma_f32_16x16x32_bf16 v[72:75], a[44:47], a[124:127], v[72:75]\n"
"s_add_u32 s67, s75, 570368\n"
"v_mfma_f32_16x16x32_bf16 v[44:47], a[36:39], a[124:127], v[44:47]\n"
"v_mfma_f32_16x16x32_bf16 v[44:47], a[32:35], a[120:123], v[44:47]\n"
"v_mfma_f32_16x16x32_bf16 v[48:51], a[32:35], a[128:131], v[48:51]\n"
"buffer_load_dwordx4 v11, s[52:55], s67 offen offset:3072 lds\n"
"v_mfma_f32_16x16x32_bf16 v[48:51], a[36:39], a[132:135], v[48:51]\n"
"s_add_u32 m0, s64, 12288\n"
"v_mfma_f32_16x16x32_bf16 v[76:79], a[44:47], a[132:135], v[76:79]\n"
"s_waitcnt vmcnt(10)\n"
"ds_read_b64_tr_b16 a[64:65], v9 offset:0\n"
"v_mfma_f32_16x16x32_bf16 v[76:79], a[40:43], a[128:131], v[76:79]\n"
"ds_read_b64_tr_b16 a[66:67], v9 offset:512\n"
"v_mfma_f32_16x16x32_bf16 v[80:83], a[40:43], a[136:139], v[80:83]\n"
"s_add_u32 s67, s75, 589824\n"
"ds_read_b64_tr_b16 a[68:69], v9 offset:4096\n"
"v_mfma_f32_16x16x32_bf16 v[80:83], a[44:47], a[140:143], v[80:83]\n"
"ds_read_b64_tr_b16 a[70:71], v9 offset:4608\n"
"v_mfma_f32_16x16x32_bf16 v[52:55], a[36:39], a[140:143], v[52:55]\n"
"ds_read_b64_tr_b16 a[72:73], v9 offset:8\n"
"buffer_load_dwordx4 v11, s[52:55], s67 offen offset:0 lds\n"
"v_mfma_f32_16x16x32_bf16 v[52:55], a[32:35], a[136:139], v[52:55]\n"
"s_waitcnt lgkmcnt(5)\n"
"ds_read_b64_tr_b16 a[74:75], v9 offset:520\n"
"v_mfma_f32_16x16x32_bf16 v[56:59], a[32:35], a[144:147], v[56:59]\n"
"s_add_u32 s67, s75, 605184\n"
"ds_read_b64_tr_b16 a[76:77], v9 offset:4104\n"
"v_mfma_f32_16x16x32_bf16 v[56:59], a[36:39], a[148:151], v[56:59]\n"
"ds_read_b64_tr_b16 a[78:79], v9 offset:4616\n"
"v_mfma_f32_16x16x32_bf16 v[84:87], a[44:47], a[148:151], v[84:87]\n"
"buffer_load_dwordx4 v11, s[52:55], s67 offen offset:1024 lds\n"
"v_mfma_f32_16x16x32_bf16 v[84:87], a[40:43], a[144:147], v[84:87]\n"
"ds_read_b64_tr_b16 a[80:81], v15 offset:0\n"
"v_mfma_f32_16x16x32_bf16 a[4:7], a[40:43], a[152:155], a[4:7]\n"
"s_add_u32 s67, s75, 620544\n"
"ds_read_b64_tr_b16 a[82:83], v15 offset:512\n"
"v_mfma_f32_16x16x32_bf16 a[4:7], a[44:47], a[156:159], a[4:7]\n"
"ds_read_b64_tr_b16 a[84:85], v15 offset:4096\n"
"v_mfma_f32_16x16x32_bf16 a[0:3], a[36:39], a[156:159], a[0:3]\n"
"ds_read_b64_tr_b16 a[86:87], v15 offset:4608\n"
"buffer_load_dwordx4 v11, s[52:55], s67 offen offset:2048 lds\n"
"v_mfma_f32_16x16x32_bf16 a[0:3], a[32:35], a[152:155], a[0:3]\n"
"ds_read_b64_tr_b16 a[88:89], v15 offset:8\n"
"v_mfma_f32_16x16x32_bf16 v[88:91], a[48:51], a[96:99], v[88:91]\n"
"s_add_u32 s67, s75, 635904\n"
"ds_read_b64_tr_b16 a[90:91], v15 offset:520\n"
"v_mfma_f32_16x16x32_bf16 v[88:91], a[52:55], a[100:103], v[88:91]\n"
"ds_read_b64_tr_b16 a[92:93], v15 offset:4104\n"
"v_mfma_f32_16x16x32_bf16 v[116:119], a[60:63], a[100:103], v[116:119]\n"
"ds_read_b64_tr_b16 a[94:95], v15 offset:4616\n"
"v_mfma_f32_16x16x32_bf16 v[116:119], a[56:59], a[96:99], v[116:119]\n"
"buffer_load_dwordx4 v11, s[52:55], s67 offen offset:3072 lds\n"
"v_mfma_f32_16x16x32_bf16 v[120:123], a[56:59], a[104:107], v[120:123]\n"
"v_mfma_f32_16x16x32_bf16 v[120:123], a[60:63], a[108:111], v[120:123]\n"
"s_add_u32 m0, s64, 0\n"
"v_mfma_f32_16x16x32_bf16 v[92:95], a[52:55], a[108:111], v[92:95]\n"
"v_mfma_f32_16x16x32_bf16 v[92:95], a[48:51], a[104:107], v[92:95]\n"
"s_add_u32 s67, s75, 524416\n"
"v_mfma_f32_16x16x32_bf16 v[96:99], a[48:51], a[112:115], v[96:99]\n"
"s_waitcnt lgkmcnt(0)\n"
"v_mfma_f32_16x16x32_bf16 v[96:99], a[52:55], a[116:119], v[96:99]\n"
"buffer_load_dwordx4 v11, s[52:55], s67 offen offset:0 lds\n"
"v_mfma_f32_16x16x32_bf16 v[124:127], a[60:63], a[116:119], v[124:127]\n"
"v_mfma_f32_16x16x32_bf16 v[124:127], a[56:59], a[112:115], v[124:127]\n"
"s_add_u32 s67, s75, 539776\n"
"v_mfma_f32_16x16x32_bf16 v[128:131], a[56:59], a[120:123], v[128:131]\n"
"v_mfma_f32_16x16x32_bf16 v[128:131], a[60:63], a[124:127], v[128:131]\n"
"v_mfma_f32_16x16x32_bf16 v[100:103], a[52:55], a[124:127], v[100:103]\n"
"buffer_load_dwordx4 v11, s[52:55], s67 offen offset:1024 lds\n"
"v_mfma_f32_16x16x32_bf16 v[100:103], a[48:51], a[120:123], v[100:103]\n"
"v_mfma_f32_16x16x32_bf16 v[104:107], a[48:51], a[128:131], v[104:107]\n"
"s_add_u32 s67, s75, 555136\n"
"v_mfma_f32_16x16x32_bf16 v[104:107], a[52:55], a[132:135], v[104:107]\n"
"v_mfma_f32_16x16x32_bf16 v[132:135], a[60:63], a[132:135], v[132:135]\n"
"buffer_load_dwordx4 v11, s[52:55], s67 offen offset:2048 lds\n"
"v_mfma_f32_16x16x32_bf16 v[132:135], a[56:59], a[128:131], v[132:135]\n"
"v_mfma_f32_16x16x32_bf16 v[136:139], a[56:59], a[136:139], v[136:139]\n"
"s_add_u32 s67, s75, 570496\n"
"v_mfma_f32_16x16x32_bf16 v[136:139], a[60:63], a[140:143], v[136:139]\n"
"v_mfma_f32_16x16x32_bf16 v[108:111], a[52:55], a[140:143], v[108:111]\n"
"v_mfma_f32_16x16x32_bf16 v[108:111], a[48:51], a[136:139], v[108:111]\n"
"buffer_load_dwordx4 v11, s[52:55], s67 offen offset:3072 lds\n"
"v_mfma_f32_16x16x32_bf16 v[112:115], a[48:51], a[144:147], v[112:115]\n"
"s_add_u32 m0, s64, 4096\n"
"v_mfma_f32_16x16x32_bf16 v[112:115], a[52:55], a[148:151], v[112:115]\n"
"v_mfma_f32_16x16x32_bf16 v[140:143], a[60:63], a[148:151], v[140:143]\n"
"s_add_u32 s67, s75, 589952\n"
"v_mfma_f32_16x16x32_bf16 v[140:143], a[56:59], a[144:147], v[140:143]\n"
"v_mfma_f32_16x16x32_bf16 a[12:15], a[56:59], a[152:155], a[12:15]\n"
"buffer_load_dwordx4 v11, s[52:55], s67 offen offset:0 lds\n"
"v_mfma_f32_16x16x32_bf16 a[12:15], a[60:63], a[156:159], a[12:15]\n"
"v_mfma_f32_16x16x32_bf16 a[8:11], a[52:55], a[156:159], a[8:11]\n"
"s_add_u32 s67, s75, 605312\n"
"v_mfma_f32_16x16x32_bf16 a[8:11], a[48:51], a[152:155], a[8:11]\n"
"v_mfma_f32_16x16x32_bf16 v[144:147], a[64:67], a[96:99], v[144:147]\n"
"buffer_load_dwordx4 v11, s[52:55], s67 offen offset:1024 lds\n"
"v_mfma_f32_16x16x32_bf16 v[144:147], a[68:71], a[100:103], v[144:147]\n"
"v_mfma_f32_16x16x32_bf16 v[172:175], a[76:79], a[100:103], v[172:175]\n"
"s_add_u32 s67, s75, 620672\n"
"v_mfma_f32_16x16x32_bf16 v[172:175], a[72:75], a[96:99], v[172:175]\n"
"v_mfma_f32_16x16x32_bf16 v[176:179], a[72:75], a[104:107], v[176:179]\n"
"v_mfma_f32_16x16x32_bf16 v[176:179], a[76:79], a[108:111], v[176:179]\n"
"buffer_load_dwordx4 v11, s[52:55], s67 offen offset:2048 lds\n"
"v_mfma_f32_16x16x32_bf16 v[148:151], a[68:71], a[108:111], v[148:151]\n"
"v_mfma_f32_16x16x32_bf16 v[148:151], a[64:67], a[104:107], v[148:151]\n"
"s_add_u32 s67, s75, 636032\n"
"v_mfma_f32_16x16x32_bf16 v[152:155], a[64:67], a[112:115], v[152:155]\n"
"s_sub_u32 s66, 0, s73\n"
"v_mfma_f32_16x16x32_bf16 v[152:155], a[68:71], a[116:119], v[152:155]\n"
"buffer_load_dwordx4 v11, s[52:55], s67 offen offset:3072 lds\n"
"v_mfma_f32_16x16x32_bf16 v[180:183], a[76:79], a[116:119], v[180:183]\n"
"v_add_u32 v12, s66, v7\n"
"v_mfma_f32_16x16x32_bf16 v[180:183], a[72:75], a[112:115], v[180:183]\n"
"s_add_u32 m0, s72, 57344\n"
"v_mfma_f32_16x16x32_bf16 v[184:187], a[72:75], a[120:123], v[184:187]\n"
"v_add_u32 v13, s66, v8\n"
"v_mfma_f32_16x16x32_bf16 v[184:187], a[76:79], a[124:127], v[184:187]\n"
"s_add_u32 s67, s60, 640\n"
"v_mfma_f32_16x16x32_bf16 v[156:159], a[68:71], a[124:127], v[156:159]\n"
"v_mfma_f32_16x16x32_bf16 v[156:159], a[64:67], a[120:123], v[156:159]\n"
"buffer_load_dwordx4 v6, s[48:51], s67 offen offset:0  lds\n"
"v_mfma_f32_16x16x32_bf16 v[160:163], a[64:67], a[128:131], v[160:163]\n"
"v_mfma_f32_16x16x32_bf16 v[160:163], a[68:71], a[132:135], v[160:163]\n"
"s_add_u32 s67, s60, 16000\n"
"v_mfma_f32_16x16x32_bf16 v[188:191], a[76:79], a[132:135], v[188:191]\n"
"v_mfma_f32_16x16x32_bf16 v[188:191], a[72:75], a[128:131], v[188:191]\n"
"buffer_load_dwordx4 v6, s[48:51], s67 offen offset:1024  lds\n"
"v_mfma_f32_16x16x32_bf16 v[192:195], a[72:75], a[136:139], v[192:195]\n"
"v_mfma_f32_16x16x32_bf16 v[192:195], a[76:79], a[140:143], v[192:195]\n"
"s_add_u32 s67, s60, 31360\n"
"v_mfma_f32_16x16x32_bf16 v[164:167], a[68:71], a[140:143], v[164:167]\n"
"v_mfma_f32_16x16x32_bf16 v[164:167], a[64:67], a[136:139], v[164:167]\n"
"buffer_load_dwordx4 v6, s[48:51], s67 offen offset:2048  lds\n"
"v_mfma_f32_16x16x32_bf16 v[168:171], a[64:67], a[144:147], v[168:171]\n"
"v_mfma_f32_16x16x32_bf16 v[168:171], a[68:71], a[148:151], v[168:171]\n"
"s_add_u32 s67, s60, 46720\n"
"v_mfma_f32_16x16x32_bf16 v[196:199], a[76:79], a[148:151], v[196:199]\n"
"v_mfma_f32_16x16x32_bf16 v[196:199], a[72:75], a[144:147], v[196:199]\n"
"v_mfma_f32_16x16x32_bf16 a[20:23], a[72:75], a[152:155], a[20:23]\n"
"buffer_load_dwordx4 v6, s[48:51], s67 offen offset:3072  lds\n"
"v_mfma_f32_16x16x32_bf16 a[20:23], a[76:79], a[156:159], a[20:23]\n"
"s_sub_u32 s70, 1048576, s75\n"
"v_and_b32 v22, 0x20000, v14\n"
"v_mfma_f32_16x16x32_bf16 a[16:19], a[68:71], a[156:159], a[16:19]\n"
"v_add_u32 v14, v22, v14\n"
"v_mfma_f32_16x16x32_bf16 a[16:19], a[64:67], a[152:155], a[16:19]\n"
"s_waitcnt vmcnt(22)\n"
"s_barrier\n"
"v_mfma_f32_16x16x32_bf16 v[200:203], a[80:83], a[96:99], v[200:203]\n"
"s_waitcnt vmcnt(12)\n"
"ds_read_b64_tr_b16 a[32:33], v9 offset:8192\n"
"v_mfma_f32_16x16x32_bf16 v[200:203], a[84:87], a[100:103], v[200:203]\n"
"s_add_u32 s70, s70, s65\n"
"ds_read_b64_tr_b16 a[34:35], v9 offset:8704\n"
"buffer_load_dword v23, v14, s[52:55], s70 offen\n"
"v_mfma_f32_16x16x32_bf16 v[228:231], a[92:95], a[100:103], v[228:231]\n"
"ds_read_b128 a[100:103], v13 offset:16384\n"
"v_mfma_f32_16x16x32_bf16 v[228:231], a[88:91], a[96:99], v[228:231]\n"
"ds_read_b128 a[96:99], v12 offset:16384\n"
"v_mfma_f32_16x16x32_bf16 v[232:235], a[88:91], a[104:107], v[232:235]\n"
"ds_read_b64_tr_b16 a[36:37], v9 offset:12288\n"
"v_mfma_f32_16x16x32_bf16 v[232:235], a[92:95], a[108:111], v[232:235]\n"
"ds_read_b64_tr_b16 a[38:39], v9 offset:12800\n"
"v_mfma_f32_16x16x32_bf16 v[204:207], a[84:87], a[108:111], v[204:207]\n"
"ds_read_b128 a[108:111], v13 offset:18432\n"
"v_mfma_f32_16x16x32_bf16 v[204:207], a[80:83], a[104:107], v[204:207]\n"
"ds_read_b128 a[104:107], v12 offset:18432\n"
"buffer_store_dwordx4 a[168:171], v1, s[36:39], %22 offen offset:0 sc1\n"
"ds_read_b64_tr_b16 a[40:41], v9 offset:8200\n"
"s_getpc_b64 s[96:97]\n"
"L_c10_:\n"
"s_add_u32 s96, s96, L_H6S-L_c10_\n"
"s_addc_u32 s97, s97, 0\n"
"v_mfma_f32_16x16x32_bf16 v[208:211], a[80:83], a[112:115], v[208:211]\n"
"s_lshr_b32 s66, %26, 3\n"
"ds_read_b64_tr_b16 a[42:43], v9 offset:8712\n"
"v_mfma_f32_16x16x32_bf16 v[208:211], a[84:87], a[116:119], v[208:211]\n"
"s_lshl2_add_u32 s66, s66, %16\n"
"ds_read_b64_tr_b16 a[44:45], v9 offset:12296\n"
"v_mfma_f32_16x16x32_bf16 v[236:239], a[92:95], a[116:119], v[236:239]\n"
"s_mul_i32 s66, s66, 128\n"
"ds_read_b128 a[116:119], v13 offset:20480\n"
"v_mfma_f32_16x16x32_bf16 v[236:239], a[88:91], a[112:115], v[236:239]\n"
"s_min_u32 s66, s66, L_headF-L_H6S-128\n"
"ds_read_b128 a[112:115], v12 offset:20480\n"
"v_mfma_f32_16x16x32_bf16 v[240:243], a[88:91], a[120:123], v[240:243]\n"
"s_add_u32 m0, s64, 8192\n"
"ds_read_b64_tr_b16 a[46:47], v9 offset:12808\n"
"v_mfma_f32_16x16x32_bf16 v[240:243], a[92:95], a[124:127], v[240:243]\n"
"ds_read_b64_tr_b16 a[48:49], v15 offset:8192\n"
"v_mfma_f32_16x16x32_bf16 v[212:215], a[84:87], a[124:127], v[212:215]\n"
"ds_read_b128 a[124:127], v13 offset:22528\n"
"v_min_u32 v22, 0, v0\n"
"v_mfma_f32_16x16x32_bf16 v[212:215], a[80:83], a[120:123], v[212:215]\n"
"ds_read_b128 a[120:123], v12 offset:22528\n"
"v_mfma_f32_16x16x32_bf16 v[216:219], a[80:83], a[128:131], v[216:219]\n"
"ds_read_b64_tr_b16 a[50:51], v15 offset:8704\n"
"v_mfma_f32_16x16x32_bf16 v[216:219], a[84:87], a[132:135], v[216:219]\n"
"ds_read_b64_tr_b16 a[52:53], v15 offset:12288\n"
"v_lshl_add_u32 v22, v22, 3, s66\n"
"v_mfma_f32_16x16x32_bf16 v[244:247], a[92:95], a[132:135], v[244:247]\n"
"ds_read_b128 a[132:135], v13 offset:24576\n"
"v_mfma_f32_16x16x32_bf16 v[244:247], a[88:91], a[128:131], v[244:247]\n"
"ds_read_b128 a[128:131], v12 offset:24576\n"
"global_load_dword v23, v22, s[96:97]\n"
"v_mfma_f32_16x16x32_bf16 v[248:251], a[88:91], a[136:139], v[248:251]\n"
"ds_read_b64_tr_b16 a[54:55], v15 offset:12800\n"
"v_mfma_f32_16x16x32_bf16 v[248:251], a[92:95], a[140:143], v[248:251]\n"
"ds_read_b64_tr_b16 a[56:57], v15 offset:8200\n"
"v_mfma_f32_16x16x32_bf16 v[220:223], a[84:87], a[140:143], v[220:223]\n"
"s_add_u32 s67, s75, 655360\n"
"ds_read_b128 a[140:143], v13 offset:26624\n"
"v_mfma_f32_16x16x32_bf16 v[220:223], a[80:83], a[136:139], v[220:223]\n"
"ds_read_b128 a[136:139], v12 offset:26624\n"
"v_mfma_f32_16x16x32_bf16 v[224:227], a[80:83], a[144:147], v[224:227]\n"
"ds_read_b64_tr_b16 a[58:59], v15 offset:8712\n"
"v_mfma_f32_16x16x32_bf16 v[224:227], a[84:87], a[148:151], v[224:227]\n"
"ds_read_b64_tr_b16 a[60:61], v15 offset:12296\n"
"v_mfma_f32_16x16x32_bf16 v[252:255], a[92:95], a[148:151], v[252:255]\n"
"ds_read_b128 a[148:151], v13 offset:28672\n"
"v_mfma_f32_16x16x32_bf16 v[252:255], a[88:91], a[144:147], v[252:255]\n"
"ds_read_b128 a[144:147], v12 offset:28672\n"
"ds_read_b64_tr_b16 a[62:63], v15 offset:12808\n"
"v_mfma_f32_16x16x32_bf16 a[28:31], a[88:91], a[152:155], a[28:31]\n"
"v_mfma_f32_16x16x32_bf16 a[28:31], a[92:95], a[156:159], a[28:31]\n"
"v_mfma_f32_16x16x32_bf16 a[24:27], a[84:87], a[156:159], a[24:27]\n"
"ds_read_b128 a[156:159], v13 offset:30720\n"
"v_mfma_f32_16x16x32_bf16 a[24:27], a[80:83], a[152:155], a[24:27]\n"
"s_waitcnt lgkmcnt(15)\n"
"ds_read_b128 a[152:155], v12 offset:30720\n"
"v_mfma_f32_16x16x32_bf16 v[32:35], a[32:35], a[96:99], v[32:35]\n"
"v_mfma_f32_16x16x32_bf16 v[32:35], a[36:39], a[100:103], v[32:35]\n"
"s_waitcnt lgkmcnt(2)\n"
"v_mfma_f32_16x16x32_bf16 v[60:63], a[44:47], a[100:103], v[60:63]\n"
"buffer_load_dwordx4 v11, s[52:55], s67 offen offset:0 lds\n"
"v_mfma_f32_16x16x32_bf16 v[60:63], a[40:43], a[96:99], v[60:63]\n"
"v_mfma_f32_16x16x32_bf16 v[64:67], a[40:43], a[104:107], v[64:67]\n"
"s_add_u32 s67, s75, 670720\n"
"v_mfma_f32_16x16x32_bf16 v[64:67], a[44:47], a[108:111], v[64:67]\n"
"v_mfma_f32_16x16x32_bf16 v[36:39], a[36:39], a[108:111], v[36:39]\n"
"buffer_load_dwordx4 v11, s[52:55], s67 offen offset:1024 lds\n"
"v_mfma_f32_16x16x32_bf16 v[36:39], a[32:35], a[104:107], v[36:39]\n"
"v_mfma_f32_16x16x32_bf16 v[40:43], a[32:35], a[112:115], v[40:43]\n"
"s_add_u32 s67, s75, 686080\n"
"v_mfma_f32_16x16x32_bf16 v[40:43], a[36:39], a[116:119], v[40:43]\n"
"v_mfma_f32_16x16x32_bf16 v[68:71], a[44:47], a[116:119], v[68:71]\n"
"v_mfma_f32_16x16x32_bf16 v[68:71], a[40:43], a[112:115], v[68:71]\n"
"buffer_load_dwordx4 v11, s[52:55], s67 offen offset:2048 lds\n"
"v_mfma_f32_16x16x32_bf16 v[72:75], a[40:43], a[120:123], v[72:75]\n"
"v_mfma_f32_16x16x32_bf16 v[72:75], a[44:47], a[124:127], v[72:75]\n"
"s_add_u32 s67, s75, 701440\n"
"v_mfma_f32_16x16x32_bf16 v[44:47], a[36:39], a[124:127], v[44:47]\n"
"v_mfma_f32_16x16x32_bf16 v[44:47], a[32:35], a[120:123], v[44:47]\n"
"v_mfma_f32_16x16x32_bf16 v[48:51], a[32:35], a[128:131], v[48:51]\n"
"buffer_load_dwordx4 v11, s[52:55], s67 offen offset:3072 lds\n"
"v_mfma_f32_16x16x32_bf16 v[48:51], a[36:39], a[132:135], v[48:51]\n"
"s_add_u32 m0, s64, 12288\n"
"v_mfma_f32_16x16x32_bf16 v[76:79], a[44:47], a[132:135], v[76:79]\n"
"s_waitcnt vmcnt(11)\n"
"ds_read_b64_tr_b16 a[64:65], v9 offset:0\n"
"v_mfma_f32_16x16x32_bf16 v[76:79], a[40:43], a[128:131], v[76:79]\n"
"s_add_u32 s67, s75, 720896\n"
"ds_read_b64_tr_b16 a[66:67], v9 offset:512\n"
"v_mfma_f32_16x16x32_bf16 v[80:83], a[40:43], a[136:139], v[80:83]\n"
"ds_read_b64_tr_b16 a[68:69], v9 offset:4096\n"
"v_mfma_f32_16x16x32_bf16 v[80:83], a[44:47], a[140:143], v[80:83]\n"
"ds_read_b64_tr_b16 a[70:71], v9 offset:4608\n"
"buffer_load_dwordx4 v11, s[52:55], s67 offen offset:0 lds\n"
"v_mfma_f32_16x16x32_bf16 v[52:55], a[36:39], a[140:143], v[52:55]\n"
"s_add_u32 s67, s75, 736256\n"
"ds_read_b64_tr_b16 a[72:73], v9 offset:8\n"
"v_mfma_f32_16x16x32_bf16 v[52:55], a[32:35], a[136:139], v[52:55]\n"
"s_waitcnt lgkmcnt(5)\n"
"ds_read_b64_tr_b16 a[74:75], v9 offset:520\n"
"v_mfma_f32_16x16x32_bf16 v[56:59], a[32:35], a[144:147], v[56:59]\n"
"ds_read_b64_tr_b16 a[76:77], v9 offset:4104\n"
"v_mfma_f32_16x16x32_bf16 v[56:59], a[36:39], a[148:151], v[56:59]\n"
"ds_read_b64_tr_b16 a[78:79], v9 offset:4616\n"
"v_mfma_f32_16x16x32_bf16 v[84:87], a[44:47], a[148:151], v[84:87]\n"
"buffer_load_dwordx4 v11, s[52:55], s67 offen offset:1024 lds\n"
"v_mfma_f32_16x16x32_bf16 v[84:87], a[40:43], a[144:147], v[84:87]\n"
"ds_read_b64_tr_b16 a[80:81], v15 offset:0\n"
"v_mfma_f32_16x16x32_bf16 a[4:7], a[40:43], a[152:155], a[4:7]\n"
"s_add_u32 s67, s75, 751616\n"
"ds_read_b64_tr_b16 a[82:83], v15 offset:512\n"
"v_mfma_f32_16x16x32_bf16 a[4:7], a[44:47], a[156:159], a[4:7]\n"
"ds_read_b64_tr_b16 a[84:85], v15 offset:4096\n"
"v_mfma_f32_16x16x32_bf16 a[0:3], a[36:39], a[156:159], a[0:3]\n"
"ds_read_b64_tr_b16 a[86:87], v15 offset:4608\n"
"buffer_load_dwordx4 v11, s[52:55], s67 offen offset:2048 lds\n"
"v_mfma_f32_16x16x32_bf16 a[0:3], a[32:35], a[152:155], a[0:3]\n"
"ds_read_b64_tr_b16 a[88:89], v15 offset:8\n"
"v_mfma_f32_16x16x32_bf16 v[88:91], a[48:51], a[96:99], v[88:91]\n"
"s_add_u32 s67, s75, 766976\n"
"ds_read_b64_tr_b16 a[90:91], v15 offset:520\n"
"v_mfma_f32_16x16x32_bf16 v[88:91], a[52:55], a[100:103], v[88:91]\n"
"ds_read_b64_tr_b16 a[92:93], v15 offset:4104\n"
"v_mfma_f32_16x16x32_bf16 v[116:119], a[60:63], a[100:103], v[116:119]\n"
"ds_read_b64_tr_b16 a[94:95], v15 offset:4616\n"
"v_mfma_f32_16x16x32_bf16 v[116:119], a[56:59], a[96:99], v[116:119]\n"
"buffer_load_dwordx4 v11, s[52:55], s67 offen offset:3072 lds\n"
"v_mfma_f32_16x16x32_bf16 v[120:123], a[56:59], a[104:107], v[120:123]\n"
"v_mfma_f32_16x16x32_bf16 v[120:123], a[60:63], a[108:111], v[120:123]\n"
"s_add_u32 m0, s64, 0\n"
"v_mfma_f32_16x16x32_bf16 v[92:95], a[52:55], a[108:111], v[92:95]\n"
"v_mfma_f32_16x16x32_bf16 v[92:95], a[48:51], a[104:107], v[92:95]\n"
"s_add_u32 s67, s75, 655488\n"
"v_mfma_f32_16x16x32_bf16 v[96:99], a[48:51], a[112:115], v[96:99]\n"
"s_waitcnt lgkmcnt(0)\n"
"v_mfma_f32_16x16x32_bf16 v[96:99], a[52:55], a[116:119], v[96:99]\n"
"buffer_load_dwordx4 v11, s[52:55], s67 offen offset:0 lds\n"
"v_mfma_f32_16x16x32_bf16 v[124:127], a[60:63], a[116:119], v[124:127]\n"
"v_mfma_f32_16x16x32_bf16 v[124:127], a[56:59], a[112:115], v[124:127]\n"
"s_add_u32 s67, s75, 670848\n"
"v_mfma_f32_16x16x32_bf16 v[128:131], a[56:59], a[120:123], v[128:131]\n"
"v_mfma_f32_16x16x32_bf16 v[128:131], a[60:63], a[124:127], v[128:131]\n"
"buffer_load_dwordx4 v11, s[52:55], s67 offen offset:1024 lds\n"
"v_mfma_f32_16x16x32_bf16 v[100:103], a[52:55], a[124:127], v[100:103]\n"
"v_mfma_f32_16x16x32_bf16 v[100:103], a[48:51], a[120:123], v[100:103]\n"
"s_add_u32 s67, s75, 686208\n"
"v_mfma_f32_16x16x32_bf16 v[104:107], a[48:51], a[128:131], v[104:107]\n"
"v_mfma_f32_16x16x32_bf16 v[104:107], a[52:55], a[132:135], v[104:107]\n"
"v_mfma_f32_16x16x32_bf16 v[132:135], a[60:63], a[132:135], v[132:135]\n"
"buffer_load_dwordx4 v11, s[52:55], s67 offen offset:2048 lds\n"
"v_mfma_f32_16x16x32_bf16 v[132:135], a[56:59], a[128:131], v[132:135]\n"
"v_mfma_f32_16x16x32_bf16 v[136:139], a[56:59], a[136:139], v[136:139]\n"
"s_add_u32 s67, s75, 701568\n"
"v_mfma_f32_16x16x32_bf16 v[136:139], a[60:63], a[140:143], v[136:139]\n"
"v_mfma_f32_16x16x32_bf16 v[108:111], a[52:55], a[140:143], v[108:111]\n"
"buffer_load_dwordx4 v11, s[52:55], s67 offen offset:3072 lds\n"
"v_mfma_f32_16x16x32_bf16 v[108:111], a[48:51], a[136:139], v[108:111]\n"
"v_mfma_f32_16x16x32_bf16 v[112:115], a[48:51], a[144:147], v[112:115]\n"
"s_add_u32 m0, s64, 4096\n"
"v_mfma_f32_16x16x32_bf16 v[112:115], a[52:55], a[148:151], v[112:115]\n"
"v_mfma_f32_16x16x32_bf16 v[140:143], a[60:63], a[148:151], v[140:143]\n"
"s_add_u32 s67, s75, 721024\n"
"v_mfma_f32_16x16x32_bf16 v[140:143], a[56:59], a[144:147], v[140:143]\n"
"v_mfma_f32_16x16x32_bf16 a[12:15], a[56:59], a[152:155], a[12:15]\n"
"buffer_load_dwordx4 v11, s[52:55], s67 offen offset:0 lds\n"
"v_mfma_f32_16x16x32_bf16 a[12:15], a[60:63], a[156:159], a[12:15]\n"
"v_mfma_f32_16x16x32_bf16 a[8:11], a[52:55], a[156:159], a[8:11]\n"
"s_add_u32 s67, s75, 736384\n"
"v_mfma_f32_16x16x32_bf16 a[8:11], a[48:51], a[152:155], a[8:11]\n"
"v_mfma_f32_16x16x32_bf16 v[144:147], a[64:67], a[96:99], v[144:147]\n"
"buffer_load_dwordx4 v11, s[52:55], s67 offen offset:1024 lds\n"
"v_mfma_f32_16x16x32_bf16 v[144:147], a[68:71], a[100:103], v[144:147]\n"
"v_mfma_f32_16x16x32_bf16 v[172:175], a[76:79], a[100:103], v[172:175]\n"
"s_add_u32 s67, s75, 751744\n"
"v_mfma_f32_16x16x32_bf16 v[172:175], a[72:75], a[96:99], v[172:175]\n"
"v_mfma_f32_16x16x32_bf16 v[176:179], a[72:75], a[104:107], v[176:179]\n"
"buffer_load_dwordx4 v11, s[52:55], s67 offen offset:2048 lds\n"
"v_mfma_f32_16x16x32_bf16 v[176:179], a[76:79], a[108:111], v[176:179]\n"
"v_mfma_f32_16x16x32_bf16 v[148:151], a[68:71], a[108:111], v[148:151]\n"
"s_add_u32 s67, s75, 767104\n"
"v_mfma_f32_16x16x32_bf16 v[148:151], a[64:67], a[104:107], v[148:151]\n"
"v_mfma_f32_16x16x32_bf16 v[152:155], a[64:67], a[112:115], v[152:155]\n"
"s_sub_u32 s66, 40960, s73\n"
"v_mfma_f32_16x16x32_bf16 v[152:155], a[68:71], a[116:119], v[152:155]\n"
"buffer_load_dwordx4 v11, s[52:55], s67 offen offset:3072 lds\n"
"v_mfma_f32_16x16x32_bf16 v[180:183], a[76:79], a[116:119], v[180:183]\n"
"s_add_u32 m0, s72, 98304\n"
"v_add_u32 v12, s66, v7\n"
"v_mfma_f32_16x16x32_bf16 v[180:183], a[72:75], a[112:115], v[180:183]\n"
"v_mfma_f32_16x16x32_bf16 v[184:187], a[72:75], a[120:123], v[184:187]\n"
"s_add_u32 s67, s60, 768\n"
"v_add_u32 v13, s66, v8\n"
"v_mfma_f32_16x16x32_bf16 v[184:187], a[76:79], a[124:127], v[184:187]\n"
"v_mfma_f32_16x16x32_bf16 v[156:159], a[68:71], a[124:127], v[156:159]\n"
"buffer_load_dwordx4 v6, s[48:51], s67 offen offset:0  lds\n"
"v_mfma_f32_16x16x32_bf16 v[156:159], a[64:67], a[120:123], v[156:159]\n"
"v_mfma_f32_16x16x32_bf16 v[160:163], a[64:67], a[128:131], v[160:163]\n"
"s_add_u32 s67, s60, 16128\n"
"v_mfma_f32_16x16x32_bf16 v[160:163], a[68:71], a[132:135], v[160:163]\n"
"v_mfma_f32_16x16x32_bf16 v[188:191], a[76:79], a[132:135], v[188:191]\n"
"buffer_load_dwordx4 v6, s[48:51], s67 offen offset:1024  lds\n"
"v_mfma_f32_16x16x32_bf16 v[188:191], a[72:75], a[128:131], v[188:191]\n"
"v_mfma_f32_16x16x32_bf16 v[192:195], a[72:75], a[136:139], v[192:195]\n"
"s_add_u32 s67, s60, 31488\n"
"v_mfma_f32_16x16x32_bf16 v[192:195], a[76:79], a[140:143], v[192:195]\n"
"v_mfma_f32_16x16x32_bf16 v[164:167], a[68:71], a[140:143], v[164:167]\n"
"v_mfma_f32_16x16x32_bf16 v[164:167], a[64:67], a[136:139], v[164:167]\n"
"buffer_load_dwordx4 v6, s[48:51], s67 offen offset:2048  lds\n"
"v_mfma_f32_16x16x32_bf16 v[168:171], a[64:67], a[144:147], v[168:171]\n"
"v_mfma_f32_16x16x32_bf16 v[168:171], a[68:71], a[148:151], v[168:171]\n"
"s_add_u32 s67, s60, 46848\n"
"v_mfma_f32_16x16x32_bf16 v[196:199], a[76:79], a[148:151], v[196:199]\n"
"v_mfma_f32_16x16x32_bf16 v[196:199], a[72:75], a[144:147], v[196:199]\n"
"buffer_load_dwordx4 v6, s[48:51], s67 offen offset:3072  lds\n"
"v_mfma_f32_16x16x32_bf16 a[20:23], a[72:75], a[152:155], a[20:23]\n"
"v_mfma_f32_16x16x32_bf16 a[20:23], a[76:79], a[156:159], a[20:23]\n"
"s_sub_u32 s70, 1572864, s75\n"
"v_mfma_f32_16x16x32_bf16 a[16:19], a[68:71], a[156:159], a[16:19]\n"
"s_add_u32 s70, s70, s65\n"
"v_mfma_f32_16x16x32_bf16 a[16:19], a[64:67], a[152:155], a[16:19]\n"
"s_waitcnt vmcnt(23)\n"
"s_barrier\n"
"v_mfma_f32_16x16x32_bf16 v[200:203], a[80:83], a[96:99], v[200:203]\n"
"s_waitcnt vmcnt(12)\n"
"ds_read_b64_tr_b16 a[32:33], v9 offset:8192\n"
"v_mfma_f32_16x16x32_bf16 v[200:203], a[84:87], a[100:103], v[200:203]\n"
"ds_read_b64_tr_b16 a[34:35], v9 offset:8704\n"
"buffer_load_dword v23, v14, s[52:55], s70 offen\n"
"v_mfma_f32_16x16x32_bf16 v[228:231], a[92:95], a[100:103], v[228:231]\n"
"ds_read_b128 a[100:103], v13 offset:16384\n"
"v_mfma_f32_16x16x32_bf16 v[228:231], a[88:91], a[96:99], v[228:231]\n"
"ds_read_b128 a[96:99], v12 offset:16384\n"
"v_mfma_f32_16x16x32_bf16 v[232:235], a[88:91], a[104:107], v[232:235]\n"
"ds_read_b64_tr_b16 a[36:37], v9 offset:12288\n"
"v_mfma_f32_16x16x32_bf16 v[232:235], a[92:95], a[108:111], v[232:235]\n"
"ds_read_b64_tr_b16 a[38:39], v9 offset:12800\n"
"v_mfma_f32_16x16x32_bf16 v[204:207], a[84:87], a[108:111], v[204:207]\n"
"ds_read_b128 a[108:111], v13 offset:18432\n"
"v_mfma_f32_16x16x32_bf16 v[204:207], a[80:83], a[104:107], v[204:207]\n"
"buffer_store_dwordx4 a[172:175], v1, s[36:39], %22 offen offset:2048 sc1\n"
"ds_read_b128 a[104:107], v12 offset:18432\n"
"ds_read_b64_tr_b16 a[40:41], v9 offset:8200\n"
"v_mfma_f32_16x16x32_bf16 v[208:211], a[80:83], a[112:115], v[208:211]\n"
"ds_read_b64_tr_b16 a[42:43], v9 offset:8712\n"
"v_mfma_f32_16x16x32_bf16 v[208:211], a[84:87], a[116:119], v[208:211]\n"
"ds_read_b64_tr_b16 a[44:45], v9 offset:12296\n"
"v_mfma_f32_16x16x32_bf16 v[236:239], a[92:95], a[116:119], v[236:239]\n"
"ds_read_b128 a[116:119], v13 offset:20480\n"
"v_mfma_f32_16x16x32_bf16 v[236:239], a[88:91], a[112:115], v[236:239]\n"
"ds_read_b128 a[112:115], v12 offset:20480\n"
"v_mfma_f32_16x16x32_bf16 v[240:243], a[88:91], a[120:123], v[240:243]\n"
"s_add_u32 m0, s64, 8192\n"
"ds_read_b64_tr_b16 a[46:47], v9 offset:12808\n"
"v_mfma_f32_16x16x32_bf16 v[240:243], a[92:95], a[124:127], v[240:243]\n"
"ds_read_b64_tr_b16 a[48:49], v15 offset:8192\n"
"v_mfma_f32_16x16x32_bf16 v[212:215], a[84:87], a[124:127], v[212:215]\n"
"ds_read_b128 a[124:127], v13 offset:22528\n"
"v_mfma_f32_16x16x32_bf16 v[212:215], a[80:83], a[120:123], v[212:215]\n"
"ds_read_b128 a[120:123], v12 offset:22528\n"
"v_mfma_f32_16x16x32_bf16 v[216:219], a[80:83], a[128:131], v[216:219]\n"
"ds_read_b64_tr_b16 a[50:51], v15 offset:8704\n"
"v_mfma_f32_16x16x32_bf16 v[216:219], a[84:87], a[132:135], v[216:219]\n"
"ds_read_b64_tr_b16 a[52:53], v15 offset:12288\n"
"v_mfma_f32_16x16x32_bf16 v[244:247], a[92:95], a[132:135], v[244:247]\n"
"ds_read_b128 a[132:135], v13 offset:24576\n"
"v_mfma_f32_16x16x32_bf16 v[244:247], a[88:91], a[128:131], v[244:247]\n"
"ds_read_b128 a[128:131], v12 offset:24576\n"
"v_mfma_f32_16x16x32_bf16 v[248:251], a[88:91], a[136:139], v[248:251]\n"
"ds_read_b64_tr_b16 a[54:55], v15 offset:12800\n"
"v_mfma_f32_16x16x32_bf16 v[248:251], a[92:95], a[140:143], v[248:251]\n"
"ds_read_b64_tr_b16 a[56:57], v15 offset:8200\n"
"v_mfma_f32_16x16x32_bf16 v[220:223], a[84:87], a[140:143], v[220:223]\n"
"s_add_u32 s67, s75, 786432\n"
"ds_read_b128 a[140:143], v13 offset:26624\n"
"v_mfma_f32_16x16x32_bf16 v[220:223], a[80:83], a[136:139], v[220:223]\n"
"ds_read_b128 a[136:139], v12 offset:26624\n"
"v_mfma_f32_16x16x32_bf16 v[224:227], a[80:83], a[144:147], v[224:227]\n"
"ds_read_b64_tr_b16 a[58:59], v15 offset:8712\n"
"v_mfma_f32_16x16x32_bf16 v[224:227], a[84:87], a[148:151], v[224:227]\n"
"ds_read_b64_tr_b16 a[60:61], v15 offset:12296\n"
"v_mfma_f32_16x16x32_bf16 v[252:255], a[92:95], a[148:151], v[252:255]\n"
"ds_read_b128 a[148:151], v13 offset:28672\n"
"v_mfma_f32_16x16x32_bf16 v[252:255], a[88:91], a[144:147], v[252:255]\n"
"ds_read_b128 a[144:147], v12 offset:28672\n"
"ds_read_b64_tr_b16 a[62:63], v15 offset:12808\n"
"v_mfma_f32_16x16x32_bf16 a[28:31], a[88:91], a[152:155], a[28:31]\n"
"v_mfma_f32_16x16x32_bf16 a[28:31], a[92:95], a[156:159], a[28:31]\n"
"v_mfma_f32_16x16x32_bf16 a[24:27], a[84:87], a[156:159], a[24:27]\n"
"ds_read_b128 a[156:159], v13 offset:30720\n"
"v_mfma_f32_16x16x32_bf16 a[24:27], a[80:83], a[152:155], a[24:27]\n"
"s_waitcnt lgkmcnt(15)\n"
"ds_read_b128 a[152:155], v12 offset:30720\n"
"v_mfma_f32_16x16x32_bf16 v[32:35], a[32:35], a[96:99], v[32:35]\n"
"v_mfma_f32_16x16x32_bf16 v[32:35], a[36:39], a[100:103], v[32:35]\n"
"s_waitcnt lgkmcnt(2)\n"
"v_mfma_f32_16x16x32_bf16 v[60:63], a[44:47], a[100:103], v[60:63]\n"
"buffer_load_dwordx4 v11, s[52:55], s67 offen offset:0 lds\n"
"v_mfma_f32_16x16x32_bf16 v[60:63], a[40:43], a[96:99], v[60:63]\n"
"v_mfma_f32_16x16x32_bf16 v[64:67], a[40:43], a[104:107], v[64:67]\n"
"s_add_u32 s67, s75, 801792\n"
"v_mfma_f32_16x16x32_bf16 v[64:67], a[44:47], a[108:111], v[64:67]\n"
"v_mfma_f32_16x16x32_bf16 v[36:39], a[36:39], a[108:111], v[36:39]\n"
"v_mfma_f32_16x16x32_bf16 v[36:39], a[32:35], a[104:107], v[36:39]\n"
"buffer_load_dwordx4 v11, s[52:55], s67 offen offset:1024 lds\n"
"v_mfma_f32_16x16x32_bf16 v[40:43], a[32:35], a[112:115], v[40:43]\n"
"v_mfma_f32_16x16x32_bf16 v[40:43], a[36:39], a[116:119], v[40:43]\n"
"s_add_u32 s67, s75, 817152\n"
"v_mfma_f32_16x16x32_bf16 v[68:71], a[44:47], a[116:119], v[68:71]\n"
"v_mfma_f32_16x16x32_bf16 v[68:71], a[40:43], a[112:115], v[68:71]\n"
"v_mfma_f32_16x16x32_bf16 v[72:75], a[40:43], a[120:123], v[72:75]\n"
"buffer_load_dwordx4 v11, s[52:55], s67 offen offset:2048 lds\n"
"v_mfma_f32_16x16x32_bf16 v[72:75], a[44:47], a[124:127], v[72:75]\n"
"v_mfma_f32_16x16x32_bf16 v[44:47], a[36:39], a[124:127], v[44:47]\n"
"s_add_u32 s67, s75, 832512\n"
"v_mfma_f32_16x16x32_bf16 v[44:47], a[32:35], a[120:123], v[44:47]\n"
"v_mfma_f32_16x16x32_bf16 v[48:51], a[32:35], a[128:131], v[48:51]\n"
"buffer_load_dwordx4 v11, s[52:55], s67 offen offset:3072 lds\n"
"v_mfma_f32_16x16x32_bf16 v[48:51], a[36:39], a[132:135], v[48:51]\n"
"s_add_u32 m0, s64, 12288\n"
"v_mfma_f32_16x16x32_bf16 v[76:79], a[44:47], a[132:135], v[76:79]\n"
"s_waitcnt vmcnt(10)\n"
"ds_read_b64_tr_b16 a[64:65], v9 offset:0\n"
"v_mfma_f32_16x16x32_bf16 v[76:79], a[40:43], a[128:131], v[76:79]\n"
"ds_read_b64_tr_b16 a[66:67], v9 offset:512\n"
"v_mfma_f32_16x16x32_bf16 v[80:83], a[40:43], a[136:139], v[80:83]\n"
"s_add_u32 s67, s75, 851968\n"
"ds_read_b64_tr_b16 a[68:69], v9 offset:4096\n"
"v_mfma_f32_16x16x32_bf16 v[80:83], a[44:47], a[140:143], v[80:83]\n"
"ds_read_b64_tr_b16 a[70:71], v9 offset:4608\n"
"v_mfma_f32_16x16x32_bf16 v[52:55], a[36:39], a[140:143], v[52:55]\n"
"ds_read_b64_tr_b16 a[72:73], v9 offset:8\n"
"buffer_load_dwordx4 v11, s[52:55], s67 offen offset:0 lds\n"
"v_mfma_f32_16x16x32_bf16 v[52:55], a[32:35], a[136:139], v[52:55]\n"
"s_waitcnt lgkmcnt(5)\n"
"ds_read_b64_tr_b16 a[74:75], v9 offset:520\n"
"v_mfma_f32_16x16x32_bf16 v[56:59], a[32:35], a[144:147], v[56:59]\n"
"s_add_u32 s67, s75, 867328\n"
"ds_read_b64_tr_b16 a[76:77], v9 offset:4104\n"
"v_mfma_f32_16x16x32_bf16 v[56:59], a[36:39], a[148:151], v[56:59]\n"
"ds_read_b64_tr_b16 a[78:79], v9 offset:4616\n"
"v_mfma_f32_16x16x32_bf16 v[84:87], a[44:47], a[148:151], v[84:87]\n"
"buffer_load_dwordx4 v11, s[52:55], s67 offen offset:1024 lds\n"
"v_mfma_f32_16x16x32_bf16 v[84:87], a[40:43], a[144:147], v[84:87]\n"
"ds_read_b64_tr_b16 a[80:81], v15 offset:0\n"
"v_mfma_f32_16x16x32_bf16 a[4:7], a[40:43], a[152:155], a[4:7]\n"
"s_add_u32 s67, s75, 882688\n"
"ds_read_b64_tr_b16 a[82:83], v15 offset:512\n"
"v_mfma_f32_16x16x32_bf16 a[4:7], a[44:47], a[156:159], a[4:7]\n"
"ds_read_b64_tr_b16 a[84:85], v15 offset:4096\n"
"v_mfma_f32_16x16x32_bf16 a[0:3], a[36:39], a[156:159], a[0:3]\n"
"s_add_u32 s96, s56, %11\n"
"ds_read_b64_tr_b16 a[86:87], v15 offset:4608\n"
"s_addc_u32 s97, s57, 0\n"
"v_mfma_f32_16x16x32_bf16 a[0:3], a[32:35], a[152:155], a[0:3]\n"
"ds_read_b64_tr_b16 a[88:89], v15 offset:8\n"
"buffer_load_dwordx4 v11, s[52:55], s67 offen offset:2048 lds\n"
"v_mfma_f32_16x16x32_bf16 v[88:91], a[48:51], a[96:99], v[88:91]\n"
"ds_read_b64_tr_b16 a[90:91], v15 offset:520\n"
"v_mfma_f32_16x16x32_bf16 v[88:91], a[52:55], a[100:103], v[88:91]\n"
"s_add_u32 s67, s75, 898048\n"
"ds_read_b64_tr_b16 a[92:93], v15 offset:4104\n"
"v_mfma_f32_16x16x32_bf16 v[116:119], a[60:63], a[100:103], v[116:119]\n"
"ds_read_b64_tr_b16 a[94:95], v15 offset:4616\n"
"v_mfma_f32_16x16x32_bf16 v[116:119], a[56:59], a[96:99], v[116:119]\n"
"buffer_load_dwordx4 v11, s[52:55], s67 offen offset:3072 lds\n"
"v_mfma_f32_16x16x32_bf16 v[120:123], a[56:59], a[104:107], v[120:123]\n"
"v_mfma_f32_16x16x32_bf16 v[120:123], a[60:63], a[108:111], v[120:123]\n"
"s_add_u32 m0, s64, 0\n"
"v_mfma_f32_16x16x32_bf16 v[92:95], a[52:55], a[108:111], v[92:95]\n"
"v_mfma_f32_16x16x32_bf16 v[92:95], a[48:51], a[104:107], v[92:95]\n"
"s_add_u32 s67, s75, 786560\n"
"v_mfma_f32_16x16x32_bf16 v[96:99], a[48:51], a[112:115], v[96:99]\n"
"s_waitcnt lgkmcnt(0)\n"
"v_mfma_f32_16x16x32_bf16 v[96:99], a[52:55], a[116:119], v[96:99]\n"
"buffer_load_dwordx4 v11, s[52:55], s67 offen offset:0 lds\n"
"v_mfma_f32_16x16x32_bf16 v[124:127], a[60:63], a[116:119], v[124:127]\n"
"s_load_dword s98, s[96:97], 512 glc\n"
"v_mfma_f32_16x16x32_bf16 v[124:127], a[56:59], a[112:115], v[124:127]\n"
"s_load_dword s99, s[96:97], 8704 glc\n"
"s_add_u32 s67, s75, 801920\n"
"v_mfma_f32_16x16x32_bf16 v[128:131], a[56:59], a[120:123], v[128:131]\n"
"v_mfma_f32_16x16x32_bf16 v[128:131], a[60:63], a[124:127], v[128:131]\n"
"v_mfma_f32_16x16x32_bf16 v[100:103], a[52:55], a[124:127], v[100:103]\n"
"buffer_load_dwordx4 v11, s[52:55], s67 offen offset:1024 lds\n"
"v_mfma_f32_16x16x32_bf16 v[100:103], a[48:51], a[120:123], v[100:103]\n"
"v_mfma_f32_16x16x32_bf16 v[104:107], a[48:51], a[128:131], v[104:107]\n"
"s_add_u32 s67, s75, 817280\n"
"v_mfma_f32_16x16x32_bf16 v[104:107], a[52:55], a[132:135], v[104:107]\n"
"v_mfma_f32_16x16x32_bf16 v[132:135], a[60:63], a[132:135], v[132:135]\n"
"buffer_load_dwordx4 v11, s[52:55], s67 offen offset:2048 lds\n"
"v_mfma_f32_16x16x32_bf16 v[132:135], a[56:59], a[128:131], v[132:135]\n"
"v_mfma_f32_16x16x32_bf16 v[136:139], a[56:59], a[136:139], v[136:139]\n"
"s_add_u32 s67, s75, 832640\n"
"v_mfma_f32_16x16x32_bf16 v[136:139], a[60:63], a[140:143], v[136:139]\n"
"v_mfma_f32_16x16x32_bf16 v[108:111], a[52:55], a[140:143], v[108:111]\n"
"v_mfma_f32_16x16x32_bf16 v[108:111], a[48:51], a[136:139], v[108:111]\n"
"buffer_load_dwordx4 v11, s[52:55], s67 offen offset:3072 lds\n"
"v_mfma_f32_16x16x32_bf16 v[112:115], a[48:51], a[144:147], v[112:115]\n"
"v_mfma_f32_16x16x32_bf16 v[112:115], a[52:55], a[148:151], v[112:115]\n"
"s_add_u32 m0, s64, 4096\n"
"v_mfma_f32_16x16x32_bf16 v[140:143], a[60:63], a[148:151], v[140:143]\n"
"v_mfma_f32_16x16x32_bf16 v[140:143], a[56:59], a[144:147], v[140:143]\n"
"s_add_u32 s67, s75, 852096\n"
"v_mfma_f32_16x16x32_bf16 a[12:15], a[56:59], a[152:155], a[12:15]\n"
"v_mfma_f32_16x16x32_bf16 a[12:15], a[60:63], a[156:159], a[12:15]\n"
"buffer_load_dwordx4 v11, s[52:55], s67 offen offset:0 lds\n"
"v_mfma_f32_16x16x32_bf16 a[8:11], a[52:55], a[156:159], a[8:11]\n"
"v_mfma_f32_16x16x32_bf16 a[8:11], a[48:51], a[152:155], a[8:11]\n"
"s_add_u32 s67, s75, 867456\n"
"v_mfma_f32_16x16x32_bf16 v[144:147], a[64:67], a[96:99], v[144:147]\n"
"v_mfma_f32_16x16x32_bf16 v[144:147], a[68:71], a[100:103], v[144:147]\n"
"buffer_load_dwordx4 v11, s[52:55], s67 offen offset:1024 lds\n"
"v_mfma_f32_16x16x32_bf16 v[172:175], a[76:79], a[100:103], v[172:175]\n"
"v_mfma_f32_16x16x32_bf16 v[172:175], a[72:75], a[96:99], v[172:175]\n"
"s_add_u32 s67, s75, 882816\n"
"v_mfma_f32_16x16x32_bf16 v[176:179], a[72:75], a[104:107], v[176:179]\n"
"v_mfma_f32_16x16x32_bf16 v[176:179], a[76:79], a[108:111], v[176:179]\n"
"buffer_load_dwordx4 v11, s[52:55], s67 offen offset:2048 lds\n"
"v_mfma_f32_16x16x32_bf16 v[148:151], a[68:71], a[108:111], v[148:151]\n"
"v_mfma_f32_16x16x32_bf16 v[148:151], a[64:67], a[104:107], v[148:151]\n"
"s_add_u32 s67, s75, 898176\n"
"v_mfma_f32_16x16x32_bf16 v[152:155], a[64:67], a[112:115], v[152:155]\n"
"s_sub_u32 s66, 81920, s73\n"
"v_mfma_f32_16x16x32_bf16 v[152:155], a[68:71], a[116:119], v[152:155]\n"
"buffer_load_dwordx4 v11, s[52:55], s67 offen offset:3072 lds\n"
"v_mfma_f32_16x16x32_bf16 v[180:183], a[76:79], a[116:119], v[180:183]\n"
"v_add_u32 v12, s66, v7\n"
"v_mfma_f32_16x16x32_bf16 v[180:183], a[72:75], a[112:115], v[180:183]\n"
"s_add_u32 m0, s72, 139264\n"
"v_mfma_f32_16x16x32_bf16 v[184:187], a[72:75], a[120:123], v[184:187]\n"
"v_add_u32 v13, s66, v8\n"
"v_mfma_f32_16x16x32_bf16 v[184:187], a[76:79], a[124:127], v[184:187]\n"
"s_add_u32 s67, s60, 896\n"
"v_mfma_f32_16x16x32_bf16 v[156:159], a[68:71], a[124:127], v[156:159]\n"
"v_mfma_f32_16x16x32_bf16 v[156:159], a[64:67], a[120:123], v[156:159]\n"
"buffer_load_dwordx4 v6, s[48:51], s67 offen offset:0  lds\n"
"v_mfma_f32_16x16x32_bf16 v[160:163], a[64:67], a[128:131], v[160:163]\n"
"v_mfma_f32_16x16x32_bf16 v[160:163], a[68:71], a[132:135], v[160:163]\n"
"s_add_u32 s67, s60, 16256\n"
"v_mfma_f32_16x16x32_bf16 v[188:191], a[76:79], a[132:135], v[188:191]\n"
"v_mfma_f32_16x16x32_bf16 v[188:191], a[72:75], a[128:131], v[188:191]\n"
"buffer_load_dwordx4 v6, s[48:51], s67 offen offset:1024  lds\n"
"v_mfma_f32_16x16x32_bf16 v[192:195], a[72:75], a[136:139], v[192:195]\n"
"v_mfma_f32_16x16x32_bf16 v[192:195], a[76:79], a[140:143], v[192:195]\n"
"s_add_u32 s67, s60, 31616\n"
"v_mfma_f32_16x16x32_bf16 v[164:167], a[68:71], a[140:143], v[164:167]\n"
"v_mfma_f32_16x16x32_bf16 v[164:167], a[64:67], a[136:139], v[164:167]\n"
"buffer_load_dwordx4 v6, s[48:51], s67 offen offset:2048  lds\n"
"v_mfma_f32_16x16x32_bf16 v[168:171], a[64:67], a[144:147], v[168:171]\n"
"v_mfma_f32_16x16x32_bf16 v[168:171], a[68:71], a[148:151], v[168:171]\n"
"s_add_u32 s67, s60, 46976\n"
"v_mfma_f32_16x16x32_bf16 v[196:199], a[76:79], a[148:151], v[196:199]\n"
"v_mfma_f32_16x16x32_bf16 v[196:199], a[72:75], a[144:147], v[196:199]\n"
"v_mfma_f32_16x16x32_bf16 a[20:23], a[72:75], a[152:155], a[20:23]\n"
"buffer_load_dwordx4 v6, s[48:51], s67 offen offset:3072  lds\n"
"v_mfma_f32_16x16x32_bf16 a[20:23], a[76:79], a[156:159], a[20:23]\n"
"s_waitcnt lgkmcnt(0)\n"
"v_mfma_f32_16x16x32_bf16 a[16:19], a[68:71], a[156:159], a[16:19]\n"
"s_add_u32 m0, s64, 32640\n"
"v_mfma_f32_16x16x32_bf16 a[16:19], a[64:67], a[152:155], a[16:19]\n"
"s_waitcnt vmcnt(22)\n"
"s_barrier\n"
"v_mfma_f32_16x16x32_bf16 v[200:203], a[80:83], a[96:99], v[200:203]\n"
"s_waitcnt vmcnt(12)\n"
"ds_read_b64_tr_b16 a[32:33], v9 offset:8192\n"
"buffer_load_dwordx4 v6, s[40:43], s80 offen offset:128  lds\n"
"v_mfma_f32_16x16x32_bf16 v[200:203], a[84:87], a[100:103], v[200:203]\n"
"ds_read_b64_tr_b16 a[34:35], v9 offset:8704\n"
"v_mfma_f32_16x16x32_bf16 v[228:231], a[92:95], a[100:103], v[228:231]\n"
"ds_read_b128 a[100:103], v13 offset:16384\n"
"v_mfma_f32_16x16x32_bf16 v[228:231], a[88:91], a[96:99], v[228:231]\n"
"ds_read_b128 a[96:99], v12 offset:16384\n"
"v_mfma_f32_16x16x32_bf16 v[232:235], a[88:91], a[104:107], v[232:235]\n"
"ds_read_b64_tr_b16 a[36:37], v9 offset:12288\n"
"buffer_load_dwordx4 v6, s[40:43], s81 offen offset:1152  lds\n"
"v_mfma_f32_16x16x32_bf16 v[232:235], a[92:95], a[108:111], v[232:235]\n"
"ds_read_b64_tr_b16 a[38:39], v9 offset:12800\n"
"v_mfma_f32_16x16x32_bf16 v[204:207], a[84:87], a[108:111], v[204:207]\n"
"ds_read_b128 a[108:111], v13 offset:18432\n"
"v_mfma_f32_16x16x32_bf16 v[204:207], a[80:83], a[104:107], v[204:207]\n"
"ds_read_b128 a[104:107], v12 offset:18432\n"
"ds_read_b64_tr_b16 a[40:41], v9 offset:8200\n"
"v_mfma_f32_16x16x32_bf16 v[208:211], a[80:83], a[112:115], v[208:211]\n"
"ds_read_b64_tr_b16 a[42:43], v9 offset:8712\n"
"v_mfma_f32_16x16x32_bf16 v[208:211], a[84:87], a[116:119], v[208:211]\n"
"ds_read_b64_tr_b16 a[44:45], v9 offset:12296\n"
"buffer_load_dwordx4 v6, s[40:43], s82 offen offset:2176  lds\n"
"v_mfma_f32_16x16x32_bf16 v[236:239], a[92:95], a[116:119], v[236:239]\n"
"ds_read_b128 a[116:119], v13 offset:20480\n"
"v_mfma_f32_16x16x32_bf16 v[236:239], a[88:91], a[112:115], v[236:239]\n"
"ds_read_b128 a[112:115], v12 offset:20480\n"
"v_mfma_f32_16x16x32_bf16 v[240:243], a[88:91], a[120:123], v[240:243]\n"
"ds_read_b64_tr_b16 a[46:47], v9 offset:12808\n"
"v_mfma_f32_16x16x32_bf16 v[240:243], a[92:95], a[124:127], v[240:243]\n"
"ds_read_b64_tr_b16 a[48:49], v15 offset:8192\n"
"buffer_load_dwordx4 v6, s[40:43], s83 offen offset:3200  lds\n"
"v_mfma_f32_16x16x32_bf16 v[212:215], a[84:87], a[124:127], v[212:215]\n"
"ds_read_b128 a[124:127], v13 offset:22528\n"
"v_mfma_f32_16x16x32_bf16 v[212:215], a[80:83], a[120:123], v[212:215]\n"
"ds_read_b128 a[120:123], v12 offset:22528\n"
"v_mfma_f32_16x16x32_bf16 v[216:219], a[80:83], a[128:131], v[216:219]\n"
"ds_read_b64_tr_b16 a[50:51], v15 offset:8704\n"
"v_mfma_f32_16x16x32_bf16 v[216:219], a[84:87], a[132:135], v[216:219]\n"
"ds_read_b64_tr_b16 a[52:53], v15 offset:12288\n"
"v_mfma_f32_16x16x32_bf16 v[244:247], a[92:95], a[132:135], v[244:247]\n"
"ds_read_b128 a[132:135], v13 offset:24576\n"
"buffer_store_dword v16, v17, s[56:59], %11 offen sc1\n"
"v_mfma_f32_16x16x32_bf16 v[244:247], a[88:91], a[128:131], v[244:247]\n"
"ds_read_b128 a[128:131], v12 offset:24576\n"
"v_mfma_f32_16x16x32_bf16 v[248:251], a[88:91], a[136:139], v[248:251]\n"
"ds_read_b64_tr_b16 a[54:55], v15 offset:12800\n"
"v_mfma_f32_16x16x32_bf16 v[248:251], a[92:95], a[140:143], v[248:251]\n"
"ds_read_b64_tr_b16 a[56:57], v15 offset:8200\n"
"v_mfma_f32_16x16x32_bf16 v[220:223], a[84:87], a[140:143], v[220:223]\n"
"ds_read_b128 a[140:143], v13 offset:26624\n"
"buffer_store_dword v16, v17, s[56:59], s69 offen sc1\n"
"v_mfma_f32_16x16x32_bf16 v[220:223], a[80:83], a[136:139], v[220:223]\n"
"ds_read_b128 a[136:139], v12 offset:26624\n"
"v_mfma_f32_16x16x32_bf16 v[224:227], a[80:83], a[144:147], v[224:227]\n"
"s_add_u32 s66, s78, 1\n"
"ds_read_b64_tr_b16 a[58:59], v15 offset:8712\n"
"v_mfma_f32_16x16x32_bf16 v[224:227], a[84:87], a[148:151], v[224:227]\n"
"s_add_u32 m0, s64, 8192\n"
"ds_read_b64_tr_b16 a[60:61], v15 offset:12296\n"
"v_mfma_f32_16x16x32_bf16 v[252:255], a[92:95], a[148:151], v[252:255]\n"
"ds_read_b128 a[148:151], v13 offset:28672\n"
"v_mov_b32 v28, s66\n"
"v_mfma_f32_16x16x32_bf16 v[252:255], a[88:91], a[144:147], v[252:255]\n"
"ds_read_b128 a[144:147], v12 offset:28672\n"
"ds_read_b64_tr_b16 a[62:63], v15 offset:12808\n"
"v_mov_b32 v29, s79\n"
"v_mfma_f32_16x16x32_bf16 a[28:31], a[88:91], a[152:155], a[28:31]\n"
"v_mfma_f32_16x16x32_bf16 a[28:31], a[92:95], a[156:159], a[28:31]\n"
"buffer_store_dwordx2 v[28:29], off, s[56:59], %24 sc1\n"
"s_add_u32 s67, s75, 917504\n"
"v_mfma_f32_16x16x32_bf16 a[24:27], a[84:87], a[156:159], a[24:27]\n"
"ds_read_b128 a[156:159], v13 offset:30720\n"
"v_mfma_f32_16x16x32_bf16 a[24:27], a[80:83], a[152:155], a[24:27]\n"
"s_waitcnt lgkmcnt(15)\n"
"ds_read_b128 a[152:155], v12 offset:30720\n"
"v_mfma_f32_16x16x32_bf16 v[32:35], a[32:35], a[96:99], v[32:35]\n"
"v_mfma_f32_16x16x32_bf16 v[32:35], a[36:39], a[100:103], v[32:35]\n"
"s_waitcnt lgkmcnt(2)\n"
"L_H6S:\n"
"v_mfma_f32_16x16x32_bf16 v[60:63], a[44:47], a[100:103], v[60:63]\n"
"buffer_load_dwordx4 v11, s[52:55], s67 offen offset:0 lds\n"
"v_mfma_f32_16x16x32_bf16 v[60:63], a[40:43], a[96:99], v[60:63]\n"
"v_mfma_f32_16x16x32_bf16 v[64:67], a[40:43], a[104:107], v[64:67]\n"
"s_add_u32 s67, s75, 932864\n"
"v_mfma_f32_16x16x32_bf16 v[64:67], a[44:47], a[108:111], v[64:67]\n"
"v_mfma_f32_16x16x32_bf16 v[36:39], a[36:39], a[108:111], v[36:39]\n"
"v_mfma_f32_16x16x32_bf16 v[36:39], a[32:35], a[104:107], v[36:39]\n"
"buffer_load_dwordx4 v11, s[52:55], s67 offen offset:1024 lds\n"
"v_mfma_f32_16x16x32_bf16 v[40:43], a[32:35], a[112:115], v[40:43]\n"
"v_mfma_f32_16x16x32_bf16 v[40:43], a[36:39], a[116:119], v[40:43]\n"
"s_add_u32 s67, s75, 948224\n"
"v_mfma_f32_16x16x32_bf16 v[68:71], a[44:47], a[116:119], v[68:71]\n"
"v_mfma_f32_16x16x32_bf16 v[68:71], a[40:43], a[112:115], v[68:71]\n"
"buffer_load_dwordx4 v11, s[52:55], s67 offen offset:2048 lds\n"
"v_mfma_f32_16x16x32_bf16 v[72:75], a[40:43], a[120:123], v[72:75]\n"
"v_mfma_f32_16x16x32_bf16 v[72:75], a[44:47], a[124:127], v[72:75]\n"
"s_add_u32 s67, s75, 963584\n"
"v_mfma_f32_16x16x32_bf16 v[44:47], a[36:39], a[124:127], v[44:47]\n"
"v_mfma_f32_16x16x32_bf16 v[44:47], a[32:35], a[120:123], v[44:47]\n"
"v_mfma_f32_16x16x32_bf16 v[48:51], a[32:35], a[128:131], v[48:51]\n"
"buffer_load_dwordx4 v11, s[52:55], s67 offen offset:3072 lds\n"
"v_mfma_f32_16x16x32_bf16 v[48:51], a[36:39], a[132:135], v[48:51]\n"
"s_add_u32 m0, s64, 12288\n"
"v_mfma_f32_16x16x32_bf16 v[76:79], a[44:47], a[132:135], v[76:79]\n"
"s_waitcnt vmcnt(15)\n"
"ds_read_b64_tr_b16 a[64:65], v9 offset:0\n"
"v_mfma_f32_16x16x32_bf16 v[76:79], a[40:43], a[128:131], v[76:79]\n"
"s_add_u32 s67, s75, 983040\n"
"ds_read_b64_tr_b16 a[66:67], v9 offset:512\n"
"v_mfma_f32_16x16x32_bf16 v[80:83], a[40:43], a[136:139], v[80:83]\n"
"ds_read_b64_tr_b16 a[68:69], v9 offset:4096\n"
"v_mfma_f32_16x16x32_bf16 v[80:83], a[44:47], a[140:143], v[80:83]\n"
"ds_read_b64_tr_b16 a[70:71], v9 offset:4608\n"
"buffer_load_dwordx4 v11, s[52:55], s67 offen offset:0 lds\n"
"v_mfma_f32_16x16x32_bf16 v[52:55], a[36:39], a[140:143], v[52:55]\n"
"s_add_u32 s67, s75, 998400\n"
"ds_read_b64_tr_b16 a[72:73], v9 offset:8\n"
"v_mfma_f32_16x16x32_bf16 v[52:55], a[32:35], a[136:139], v[52:55]\n"
"s_waitcnt lgkmcnt(5)\n"
"ds_read_b64_tr_b16 a[74:75], v9 offset:520\n"
"v_mfma_f32_16x16x32_bf16 v[56:59], a[32:35], a[144:147], v[56:59]\n"
"ds_read_b64_tr_b16 a[76:77], v9 offset:4104\n"
"v_mfma_f32_16x16x32_bf16 v[56:59], a[36:39], a[148:151], v[56:59]\n"
"ds_read_b64_tr_b16 a[78:79], v9 offset:4616\n"
"v_mfma_f32_16x16x32_bf16 v[84:87], a[44:47], a[148:151], v[84:87]\n"
"buffer_load_dwordx4 v11, s[52:55], s67 offen offset:1024 lds\n"
"v_mfma_f32_16x16x32_bf16 v[84:87], a[40:43], a[144:147], v[84:87]\n"
"ds_read_b64_tr_b16 a[80:81], v15 offset:0\n"
"v_mfma_f32_16x16x32_bf16 a[4:7], a[40:43], a[152:155], a[4:7]\n"
"s_add_u32 s67, s75, 1013760\n"
"ds_read_b64_tr_b16 a[82:83], v15 offset:512\n"
"v_mfma_f32_16x16x32_bf16 a[4:7], a[44:47], a[156:159], a[4:7]\n"
"ds_read_b64_tr_b16 a[84:85], v15 offset:4096\n"
"v_mfma_f32_16x16x32_bf16 a[0:3], a[36:39], a[156:159], a[0:3]\n"
"ds_read_b64_tr_b16 a[86:87], v15 offset:4608\n"
"buffer_load_dwordx4 v11, s[52:55], s67 offen offset:2048 lds\n"
"v_mfma_f32_16x16x32_bf16 a[0:3], a[32:35], a[152:155], a[0:3]\n"
"ds_read_b64_tr_b16 a[88:89], v15 offset:8\n"
"v_mfma_f32_16x16x32_bf16 v[88:91], a[48:51], a[96:99], v[88:91]\n"
"s_add_u32 s67, s75, 1029120\n"
"ds_read_b64_tr_b16 a[90:91], v15 offset:520\n"
"v_mfma_f32_16x16x32_bf16 v[88:91], a[52:55], a[100:103], v[88:91]\n"
"ds_read_b64_tr_b16 a[92:93], v15 offset:4104\n"
"v_mfma_f32_16x16x32_bf16 v[116:119], a[60:63], a[100:103], v[116:119]\n"
"ds_read_b64_tr_b16 a[94:95], v15 offset:4616\n"
"v_mfma_f32_16x16x32_bf16 v[116:119], a[56:59], a[96:99], v[116:119]\n"
"buffer_load_dwordx4 v11, s[52:55], s67 offen offset:3072 lds\n"
"v_mfma_f32_16x16x32_bf16 v[120:123], a[56:59], a[104:107], v[120:123]\n"
"v_mfma_f32_16x16x32_bf16 v[120:123], a[60:63], a[108:111], v[120:123]\n"
"s_add_u32 m0, s64, 0\n"
"v_mfma_f32_16x16x32_bf16 v[92:95], a[52:55], a[108:111], v[92:95]\n"
"v_mfma_f32_16x16x32_bf16 v[92:95], a[48:51], a[104:107], v[92:95]\n"
"s_add_u32 s67, s75, 917632\n"
"v_mfma_f32_16x16x32_bf16 v[96:99], a[48:51], a[112:115], v[96:99]\n"
"s_waitcnt lgkmcnt(0)\n"
"v_mfma_f32_16x16x32_bf16 v[96:99], a[52:55], a[116:119], v[96:99]\n"
"buffer_load_dwordx4 v11, s[52:55], s67 offen offset:0 lds\n"
"v_mfma_f32_16x16x32_bf16 v[124:127], a[60:63], a[116:119], v[124:127]\n"
"v_mfma_f32_16x16x32_bf16 v[124:127], a[56:59], a[112:115], v[124:127]\n"
"s_add_u32 s67, s75, 932992\n"
"v_mfma_f32_16x16x32_bf16 v[128:131], a[56:59], a[120:123], v[128:131]\n"
"v_mfma_f32_16x16x32_bf16 v[128:131], a[60:63], a[124:127], v[128:131]\n"
"buffer_load_dwordx4 v11, s[52:55], s67 offen offset:1024 lds\n"
"v_mfma_f32_16x16x32_bf16 v[100:103], a[52:55], a[124:127], v[100:103]\n"
"v_mfma_f32_16x16x32_bf16 v[100:103], a[48:51], a[120:123], v[100:103]\n"
"s_add_u32 s67, s75, 948352\n"
"v_mfma_f32_16x16x32_bf16 v[104:107], a[48:51], a[128:131], v[104:107]\n"
"v_mfma_f32_16x16x32_bf16 v[104:107], a[52:55], a[132:135], v[104:107]\n"
"v_mfma_f32_16x16x32_bf16 v[132:135], a[60:63], a[132:135], v[132:135]\n"
"buffer_load_dwordx4 v11, s[52:55], s67 offen offset:2048 lds\n"
"v_mfma_f32_16x16x32_bf16 v[132:135], a[56:59], a[128:131], v[132:135]\n"
"v_mfma_f32_16x16x32_bf16 v[136:139], a[56:59], a[136:139], v[136:139]\n"
"s_add_u32 s67, s75, 963712\n"
"v_mfma_f32_16x16x32_bf16 v[136:139], a[60:63], a[140:143], v[136:139]\n"
"v_mfma_f32_16x16x32_bf16 v[108:111], a[52:55], a[140:143], v[108:111]\n"
"v_mfma_f32_16x16x32_bf16 v[108:111], a[48:51], a[136:139], v[108:111]\n"
"buffer_load_dwordx4 v11, s[52:55], s67 offen offset:3072 lds\n"
"v_mfma_f32_16x16x32_bf16 v[112:115], a[48:51], a[144:147], v[112:115]\n"
"s_add_u32 m0, s64, 4096\n"
"v_mfma_f32_16x16x32_bf16 v[112:115], a[52:55], a[148:151], v[112:115]\n"
"v_mfma_f32_16x16x32_bf16 v[140:143], a[60:63], a[148:151], v[140:143]\n"
"s_add_u32 s67, s75, 983168\n"
"v_mfma_f32_16x16x32_bf16 v[140:143], a[56:59], a[144:147], v[140:143]\n"
"v_mfma_f32_16x16x32_bf16 a[12:15], a[56:59], a[152:155], a[12:15]\n"
"buffer_load_dwordx4 v11, s[52:55], s67 offen offset:0 lds\n"
"v_mfma_f32_16x16x32_bf16 a[12:15], a[60:63], a[156:159], a[12:15]\n"
"s_sub_u32 s71, s64, s64\n"
"v_mfma_f32_16x16x32_bf16 a[8:11], a[52:55], a[156:159], a[8:11]\n"
"s_add_u32 s67, s75, 998528\n"
"v_mfma_f32_16x16x32_bf16 a[8:11], a[48:51], a[152:155], a[8:11]\n"
"v_mfma_f32_16x16x32_bf16 v[144:147], a[64:67], a[96:99], v[144:147]\n"
"s_add_u32 s71, s71, 32768\n"
"v_mfma_f32_16x16x32_bf16 v[144:147], a[68:71], a[100:103], v[144:147]\n"
"buffer_load_dwordx4 v11, s[52:55], s67 offen offset:1024 lds\n"
"v_mfma_f32_16x16x32_bf16 v[172:175], a[76:79], a[100:103], v[172:175]\n"
"v_add_u32 v20, s71, v7\n"
"v_mfma_f32_16x16x32_bf16 v[172:175], a[72:75], a[96:99], v[172:175]\n"
"s_add_u32 s67, s75, 1013888\n"
"v_mfma_f32_16x16x32_bf16 v[176:179], a[72:75], a[104:107], v[176:179]\n"
"buffer_load_dwordx4 v11, s[52:55], s67 offen offset:2048 lds\n"
"v_mfma_f32_16x16x32_bf16 v[176:179], a[76:79], a[108:111], v[176:179]\n"
"v_add_u32 v21, s71, v8\n"
"v_mfma_f32_16x16x32_bf16 v[148:151], a[68:71], a[108:111], v[148:151]\n"
"s_add_u32 s67, s75, 1029248\n"
"v_mfma_f32_16x16x32_bf16 v[148:151], a[64:67], a[104:107], v[148:151]\n"
"v_mfma_f32_16x16x32_bf16 v[152:155], a[64:67], a[112:115], v[152:155]\n"
"s_sub_u32 s66, 122880, s73\n"
"v_mfma_f32_16x16x32_bf16 v[152:155], a[68:71], a[116:119], v[152:155]\n"
"s_waitcnt vmcnt(18)\n"
"ds_read_b128 a[192:195], v20 offset:0\n"
"buffer_load_dwordx4 v11, s[52:55], s67 offen offset:3072 lds\n"
"v_mfma_f32_16x16x32_bf16 v[180:183], a[76:79], a[116:119], v[180:183]\n"
"ds_read_b128 a[196:199], v21 offset:0\n"
"v_add_u32 v12, s66, v7\n"
"v_mfma_f32_16x16x32_bf16 v[180:183], a[72:75], a[112:115], v[180:183]\n"
"s_add_u32 m0, s72, 57344\n"
"ds_read_b128 a[200:203], v20 offset:2048\n"
"v_mfma_f32_16x16x32_bf16 v[184:187], a[72:75], a[120:123], v[184:187]\n"
"v_add_u32 v13, s66, v8\n"
"v_mfma_f32_16x16x32_bf16 v[184:187], a[76:79], a[124:127], v[184:187]\n"
"s_add_u32 s67, %21, 0\n"
"ds_read_b128 a[204:207], v21 offset:2048\n"
"v_mfma_f32_16x16x32_bf16 v[156:159], a[68:71], a[124:127], v[156:159]\n"
"v_mfma_f32_16x16x32_bf16 v[156:159], a[64:67], a[120:123], v[156:159]\n"
"buffer_load_dwordx4 v11, s[52:55], s67 offen offset:0  lds\n"
"v_mfma_f32_16x16x32_bf16 v[160:163], a[64:67], a[128:131], v[160:163]\n"
"v_mfma_f32_16x16x32_bf16 v[160:163], a[68:71], a[132:135], v[160:163]\n"
"s_add_u32 s67, %21, 15360\n"
"v_mfma_f32_16x16x32_bf16 v[188:191], a[76:79], a[132:135], v[188:191]\n"
"v_mfma_f32_16x16x32_bf16 v[188:191], a[72:75], a[128:131], v[188:191]\n"
"s_waitcnt lgkmcnt(0)\n"
"buffer_load_dwordx4 v11, s[52:55], s67 offen offset:1024  lds\n"
"v_mfma_f32_16x16x32_bf16 v[192:195], a[72:75], a[136:139], v[192:195]\n"
"v_mfma_f32_16x16x32_bf16 v[192:195], a[76:79], a[140:143], v[192:195]\n"
"s_add_u32 s67, %21, 30720\n"
"v_mfma_f32_16x16x32_bf16 v[164:167], a[68:71], a[140:143], v[164:167]\n"
"v_mfma_f32_16x16x32_bf16 v[164:167], a[64:67], a[136:139], v[164:167]\n"
"buffer_load_dwordx4 v11, s[52:55], s67 offen offset:2048  lds\n"
"v_mfma_f32_16x16x32_bf16 v[168:171], a[64:67], a[144:147], v[168:171]\n"
"v_mfma_f32_16x16x32_bf16 v[168:171], a[68:71], a[148:151], v[168:171]\n"
"s_add_u32 s67, %21, 46080\n"
"v_mfma_f32_16x16x32_bf16 v[196:199], a[76:79], a[148:151], v[196:199]\n"
"v_mfma_f32_16x16x32_bf16 v[196:199], a[72:75], a[144:147], v[196:199]\n"
"buffer_load_dwordx4 v11, s[52:55], s67 offen offset:3072  lds\n"
"v_mfma_f32_16x16x32_bf16 a[20:23], a[72:75], a[152:155], a[20:23]\n"
"s_cmp_lt_u32 %16, 2\n"
"s_cselect_b32 s67, 16384, 0\n"
"v_mfma_f32_16x16x32_bf16 a[20:23], a[76:79], a[156:159], a[20:23]\n"
"v_mfma_f32_16x16x32_bf16 a[16:19], a[68:71], a[156:159], a[16:19]\n"
"s_add_u32 s67, s67, s72\n"
"s_add_u32 m0, s67, 16384\n"
"v_mfma_f32_16x16x32_bf16 a[16:19], a[64:67], a[152:155], a[16:19]\n"
"s_barrier\n"
"s_waitcnt vmcnt(12)\n"
"ds_read_b64_tr_b16 a[32:33], v9 offset:8192\n"
"v_mfma_f32_16x16x32_bf16 v[200:203], a[80:83], a[96:99], v[200:203]\n"
"s_sub_u32 s67, 1024, s60\n"
"ds_read_b64_tr_b16 a[34:35], v9 offset:8704\n"
"v_mfma_f32_16x16x32_bf16 v[200:203], a[84:87], a[100:103], v[200:203]\n"
"ds_read_b64_tr_b16 a[36:37], v9 offset:12288\n"
"v_mfma_f32_16x16x32_bf16 v[228:231], a[92:95], a[100:103], v[228:231]\n"
"ds_read_b128 a[100:103], v13 offset:16384\n"
"buffer_load_dwordx4 v6, s[48:51], s67 offen offset:0  lds\n"
"v_mfma_f32_16x16x32_bf16 v[228:231], a[88:91], a[96:99], v[228:231]\n"
"ds_read_b128 a[96:99], v12 offset:16384\n"
"v_mfma_f32_16x16x32_bf16 v[232:235], a[88:91], a[104:107], v[232:235]\n"
"s_sub_u32 s67, 16384, s60\n"
"ds_read_b64_tr_b16 a[38:39], v9 offset:12800\n"
"v_mfma_f32_16x16x32_bf16 v[232:235], a[92:95], a[108:111], v[232:235]\n"
"ds_read_b64_tr_b16 a[40:41], v9 offset:8200\n"
"v_mfma_f32_16x16x32_bf16 v[204:207], a[84:87], a[108:111], v[204:207]\n"
"ds_read_b128 a[108:111], v13 offset:18432\n"
"buffer_load_dwordx4 v6, s[48:51], s67 offen offset:1024  lds\n"
"v_mfma_f32_16x16x32_bf16 v[204:207], a[80:83], a[104:107], v[204:207]\n"
"ds_read_b128 a[104:107], v12 offset:18432\n"
"v_mfma_f32_16x16x32_bf16 v[208:211], a[80:83], a[112:115], v[208:211]\n"
"s_sub_u32 s67, 31744, s60\n"
"ds_read_b64_tr_b16 a[42:43], v9 offset:8712\n"
"v_mfma_f32_16x16x32_bf16 v[208:211], a[84:87], a[116:119], v[208:211]\n"
"ds_read_b64_tr_b16 a[44:45], v9 offset:12296\n"
"v_mfma_f32_16x16x32_bf16 v[236:239], a[92:95], a[116:119], v[236:239]\n"
"ds_read_b128 a[116:119], v13 offset:20480\n"
"buffer_load_dwordx4 v6, s[48:51], s67 offen offset:2048  lds\n"
"v_mfma_f32_16x16x32_bf16 v[236:239], a[88:91], a[112:115], v[236:239]\n"
"ds_read_b128 a[112:115], v12 offset:20480\n"
"v_mfma_f32_16x16x32_bf16 v[240:243], a[88:91], a[120:123], v[240:243]\n"
"s_sub_u32 s67, 47104, s60\n"
"ds_read_b64_tr_b16 a[46:47], v9 offset:12808\n"
"v_mfma_f32_16x16x32_bf16 v[240:243], a[92:95], a[124:127], v[240:243]\n"
"ds_read_b64_tr_b16 a[48:49], v15 offset:8192\n"
"v_mfma_f32_16x16x32_bf16 v[212:215], a[84:87], a[124:127], v[212:215]\n"
"ds_read_b128 a[124:127], v13 offset:22528\n"
"v_mfma_f32_16x16x32_bf16 v[212:215], a[80:83], a[120:123], v[212:215]\n"
"ds_read_b128 a[120:123], v12 offset:22528\n"
"buffer_load_dwordx4 v6, s[48:51], s67 offen offset:3072  lds\n"
"v_mfma_f32_16x16x32_bf16 v[216:219], a[80:83], a[128:131], v[216:219]\n"
"ds_read_b64_tr_b16 a[50:51], v15 offset:8704\n"
"v_mfma_f32_16x16x32_bf16 v[216:219], a[84:87], a[132:135], v[216:219]\n"
"ds_read_b64_tr_b16 a[52:53], v15 offset:12288\n"
"v_mfma_f32_16x16x32_bf16 v[244:247], a[92:95], a[132:135], v[244:247]\n"
"ds_read_b128 a[132:135], v13 offset:24576\n"
"v_mfma_f32_16x16x32_bf16 v[244:247], a[88:91], a[128:131], v[244:247]\n"
"s_add_u32 m0, s64, 8192\n"
"ds_read_b128 a[128:131], v12 offset:24576\n"
"v_mfma_f32_16x16x32_bf16 v[248:251], a[88:91], a[136:139], v[248:251]\n"
"ds_read_b64_tr_b16 a[54:55], v15 offset:12800\n"
"v_mfma_f32_16x16x32_bf16 v[248:251], a[92:95], a[140:143], v[248:251]\n"
"ds_read_b64_tr_b16 a[56:57], v15 offset:8200\n"
"v_mfma_f32_16x16x32_bf16 v[220:223], a[84:87], a[140:143], v[220:223]\n"
"ds_read_b128 a[140:143], v13 offset:26624\n"
"v_mfma_f32_16x16x32_bf16 v[220:223], a[80:83], a[136:139], v[220:223]\n"
"ds_read_b128 a[136:139], v12 offset:26624\n"
"v_mfma_f32_16x16x32_bf16 v[224:227], a[80:83], a[144:147], v[224:227]\n"
"ds_read_b64_tr_b16 a[58:59], v15 offset:8712\n"
"v_mfma_f32_16x16x32_bf16 v[224:227], a[84:87], a[148:151], v[224:227]\n"
"ds_read_b64_tr_b16 a[60:61], v15 offset:12296\n"
"v_mfma_f32_16x16x32_bf16 v[252:255], a[92:95], a[148:151], v[252:255]\n"
"s_sub_u32 s67, 1048576, s75\n"
"ds_read_b128 a[148:151], v13 offset:28672\n"
"v_mfma_f32_16x16x32_bf16 v[252:255], a[88:91], a[144:147], v[252:255]\n"
"ds_read_b128 a[144:147], v12 offset:28672\n"
"ds_read_b64_tr_b16 a[62:63], v15 offset:12808\n"
"v_mfma_f32_16x16x32_bf16 a[28:31], a[88:91], a[152:155], a[28:31]\n"
"v_mfma_f32_16x16x32_bf16 a[28:31], a[92:95], a[156:159], a[28:31]\n"
"v_mfma_f32_16x16x32_bf16 a[24:27], a[84:87], a[156:159], a[24:27]\n"
"ds_read_b128 a[156:159], v13 offset:30720\n"
"v_mfma_f32_16x16x32_bf16 a[24:27], a[80:83], a[152:155], a[24:27]\n"
"s_waitcnt lgkmcnt(15)\n"
"ds_read_b128 a[152:155], v12 offset:30720\n"
"v_mfma_f32_16x16x32_bf16 v[32:35], a[32:35], a[96:99], v[32:35]\n"
"v_mfma_f32_16x16x32_bf16 v[32:35], a[36:39], a[100:103], v[32:35]\n"
"s_waitcnt lgkmcnt(2)\n"
"v_mfma_f32_16x16x32_bf16 v[60:63], a[44:47], a[100:103], v[60:63]\n"
"buffer_load_dwordx4 v11, s[52:55], s67 offen offset:0 lds\n"
"v_mfma_f32_16x16x32_bf16 v[60:63], a[40:43], a[96:99], v[60:63]\n"
"v_mfma_f32_16x16x32_bf16 v[64:67], a[40:43], a[104:107], v[64:67]\n"
"s_sub_u32 s67, 1063936, s75\n"
"v_mfma_f32_16x16x32_bf16 v[64:67], a[44:47], a[108:111], v[64:67]\n"
"v_mfma_f32_16x16x32_bf16 v[36:39], a[36:39], a[108:111], v[36:39]\n"
"v_mfma_f32_16x16x32_bf16 v[36:39], a[32:35], a[104:107], v[36:39]\n"
"buffer_load_dwordx4 v11, s[52:55], s67 offen offset:1024 lds\n"
"v_mfma_f32_16x16x32_bf16 v[40:43], a[32:35], a[112:115], v[40:43]\n"
"v_mfma_f32_16x16x32_bf16 v[40:43], a[36:39], a[116:119], v[40:43]\n"
"s_sub_u32 s67, 1079296, s75\n"
"v_mfma_f32_16x16x32_bf16 v[68:71], a[44:47], a[116:119], v[68:71]\n"
"v_mfma_f32_16x16x32_bf16 v[68:71], a[40:43], a[112:115], v[68:71]\n"
"v_mfma_f32_16x16x32_bf16 v[72:75], a[40:43], a[120:123], v[72:75]\n"
"buffer_load_dwordx4 v11, s[52:55], s67 offen offset:2048 lds\n"
"v_mfma_f32_16x16x32_bf16 v[72:75], a[44:47], a[124:127], v[72:75]\n"
"v_mfma_f32_16x16x32_bf16 v[44:47], a[36:39], a[124:127], v[44:47]\n"
"s_sub_u32 s67, 1094656, s75\n"
"v_mfma_f32_16x16x32_bf16 v[44:47], a[32:35], a[120:123], v[44:47]\n"
"v_mfma_f32_16x16x32_bf16 v[48:51], a[32:35], a[128:131], v[48:51]\n"
"buffer_load_dwordx4 v11, s[52:55], s67 offen offset:3072 lds\n"
"v_mfma_f32_16x16x32_bf16 v[48:51], a[36:39], a[132:135], v[48:51]\n"
"s_add_u32 m0, s64, 12288\n"
"v_mfma_f32_16x16x32_bf16 v[76:79], a[44:47], a[132:135], v[76:79]\n"
"s_waitcnt vmcnt(12)\n"
"ds_read_b64_tr_b16 a[64:65], v9 offset:0\n"
"v_mfma_f32_16x16x32_bf16 v[76:79], a[40:43], a[128:131], v[76:79]\n"
"ds_read_b64_tr_b16 a[66:67], v9 offset:512\n"
"v_mfma_f32_16x16x32_bf16 v[80:83], a[40:43], a[136:139], v[80:83]\n"
"s_sub_u32 s67, 1114112, s75\n"
"ds_read_b64_tr_b16 a[68:69], v9 offset:4096\n"
"v_mfma_f32_16x16x32_bf16 v[80:83], a[44:47], a[140:143], v[80:83]\n"
"ds_read_b64_tr_b16 a[70:71], v9 offset:4608\n"
"v_mfma_f32_16x16x32_bf16 v[52:55], a[36:39], a[140:143], v[52:55]\n"
"ds_read_b64_tr_b16 a[72:73], v9 offset:8\n"
"buffer_load_dwordx4 v11, s[52:55], s67 offen offset:0 lds\n"
"v_mfma_f32_16x16x32_bf16 v[52:55], a[32:35], a[136:139], v[52:55]\n"
"s_waitcnt lgkmcnt(5)\n"
"ds_read_b64_tr_b16 a[74:75], v9 offset:520\n"
"v_mfma_f32_16x16x32_bf16 v[56:59], a[32:35], a[144:147], v[56:59]\n"
"s_sub_u32 s67, 1129472, s75\n"
"ds_read_b64_tr_b16 a[76:77], v9 offset:4104\n"
"v_mfma_f32_16x16x32_bf16 v[56:59], a[36:39], a[148:151], v[56:59]\n"
"ds_read_b64_tr_b16 a[78:79], v9 offset:4616\n"
"v_mfma_f32_16x16x32_bf16 v[84:87], a[44:47], a[148:151], v[84:87]\n"
"buffer_load_dwordx4 v11, s[52:55], s67 offen offset:1024 lds\n"
"v_mfma_f32_16x16x32_bf16 v[84:87], a[40:43], a[144:147], v[84:87]\n"
"ds_read_b64_tr_b16 a[80:81], v15 offset:0\n"
"v_mfma_f32_16x16x32_bf16 a[4:7], a[40:43], a[152:155], a[4:7]\n"
"s_sub_u32 s67, 1144832, s75\n"
"ds_read_b64_tr_b16 a[82:83], v15 offset:512\n"
"v_mfma_f32_16x16x32_bf16 a[4:7], a[44:47], a[156:159], a[4:7]\n"
"ds_read_b64_tr_b16 a[84:85], v15 offset:4096\n"
"v_mfma_f32_16x16x32_bf16 a[0:3], a[36:39], a[156:159], a[0:3]\n"
"ds_read_b64_tr_b16 a[86:87], v15 offset:4608\n"
"buffer_load_dwordx4 v11, s[52:55], s67 offen offset:2048 lds\n"
"v_mfma_f32_16x16x32_bf16 a[0:3], a[32:35], a[152:155], a[0:3]\n"
"ds_read_b64_tr_b16 a[88:89], v15 offset:8\n"
"v_mfma_f32_16x16x32_bf16 v[88:91], a[48:51], a[96:99], v[88:91]\n"
"s_sub_u32 s67, 1160192, s75\n"
"ds_read_b64_tr_b16 a[90:91], v15 offset:520\n"
"v_mfma_f32_16x16x32_bf16 v[88:91], a[52:55], a[100:103], v[88:91]\n"
"ds_read_b64_tr_b16 a[92:93], v15 offset:4104\n"
"v_mfma_f32_16x16x32_bf16 v[116:119], a[60:63], a[100:103], v[116:119]\n"
"ds_read_b64_tr_b16 a[94:95], v15 offset:4616\n"
"v_mfma_f32_16x16x32_bf16 v[116:119], a[56:59], a[96:99], v[116:119]\n"
"buffer_load_dwordx4 v11, s[52:55], s67 offen offset:3072 lds\n"
"v_mfma_f32_16x16x32_bf16 v[120:123], a[56:59], a[104:107], v[120:123]\n"
"v_mfma_f32_16x16x32_bf16 v[120:123], a[60:63], a[108:111], v[120:123]\n"
"s_add_u32 m0, s64, 0\n"
"v_mfma_f32_16x16x32_bf16 v[92:95], a[52:55], a[108:111], v[92:95]\n"
"v_mfma_f32_16x16x32_bf16 v[92:95], a[48:51], a[104:107], v[92:95]\n"
"s_sub_u32 s67, 1048704, s75\n"
"v_mfma_f32_16x16x32_bf16 v[96:99], a[48:51], a[112:115], v[96:99]\n"
"s_waitcnt lgkmcnt(0)\n"
"v_mfma_f32_16x16x32_bf16 v[96:99], a[52:55], a[116:119], v[96:99]\n"
"buffer_load_dwordx4 v11, s[52:55], s67 offen offset:0 lds\n"
"v_mfma_f32_16x16x32_bf16 v[124:127], a[60:63], a[116:119], v[124:127]\n"
"v_mfma_f32_16x16x32_bf16 v[124:127], a[56:59], a[112:115], v[124:127]\n"
"s_sub_u32 s67, 1064064, s75\n"
"v_mfma_f32_16x16x32_bf16 v[128:131], a[56:59], a[120:123], v[128:131]\n"
"v_mfma_f32_16x16x32_bf16 v[128:131], a[60:63], a[124:127], v[128:131]\n"
"v_mfma_f32_16x16x32_bf16 v[100:103], a[52:55], a[124:127], v[100:103]\n"
"buffer_load_dwordx4 v11, s[52:55], s67 offen offset:1024 lds\n"
"v_mfma_f32_16x16x32_bf16 v[100:103], a[48:51], a[120:123], v[100:103]\n"
"v_mfma_f32_16x16x32_bf16 v[104:107], a[48:51], a[128:131], v[104:107]\n"
"s_sub_u32 s67, 1079424, s75\n"
"v_mfma_f32_16x16x32_bf16 v[104:107], a[52:55], a[132:135], v[104:107]\n"
"v_mfma_f32_16x16x32_bf16 v[132:135], a[60:63], a[132:135], v[132:135]\n"
"buffer_load_dwordx4 v11, s[52:55], s67 offen offset:2048 lds\n"
"v_mfma_f32_16x16x32_bf16 v[132:135], a[56:59], a[128:131], v[132:135]\n"
"v_mfma_f32_16x16x32_bf16 v[136:139], a[56:59], a[136:139], v[136:139]\n"
"s_sub_u32 s67, 1094784, s75\n"
"v_mfma_f32_16x16x32_bf16 v[136:139], a[60:63], a[140:143], v[136:139]\n"
"v_mfma_f32_16x16x32_bf16 v[108:111], a[52:55], a[140:143], v[108:111]\n"
"v_mfma_f32_16x16x32_bf16 v[108:111], a[48:51], a[136:139], v[108:111]\n"
"buffer_load_dwordx4 v11, s[52:55], s67 offen offset:3072 lds\n"
"v_mfma_f32_16x16x32_bf16 v[112:115], a[48:51], a[144:147], v[112:115]\n"
"v_mfma_f32_16x16x32_bf16 v[112:115], a[52:55], a[148:151], v[112:115]\n"
"s_add_u32 m0, s64, 4096\n"
"v_mfma_f32_16x16x32_bf16 v[140:143], a[60:63], a[148:151], v[140:143]\n"
"v_mfma_f32_16x16x32_bf16 v[140:143], a[56:59], a[144:147], v[140:143]\n"
"s_sub_u32 s67, 1114240, s75\n"
"v_mfma_f32_16x16x32_bf16 a[12:15], a[56:59], a[152:155], a[12:15]\n"
"v_mfma_f32_16x16x32_bf16 a[12:15], a[60:63], a[156:159], a[12:15]\n"
"buffer_load_dwordx4 v11, s[52:55], s67 offen offset:0 lds\n"
"v_mfma_f32_16x16x32_bf16 a[8:11], a[52:55], a[156:159], a[8:11]\n"
"v_mfma_f32_16x16x32_bf16 a[8:11], a[48:51], a[152:155], a[8:11]\n"
"s_sub_u32 s67, 1129600, s75\n"
"v_mfma_f32_16x16x32_bf16 v[144:147], a[64:67], a[96:99], v[144:147]\n"
"buffer_load_dwordx4 v11, s[52:55], s67 offen offset:1024 lds\n"
"v_mfma_f32_16x16x32_bf16 v[144:147], a[68:71], a[100:103], v[144:147]\n"
"s_sub_u32 s71, s72, s64\n"
"v_mfma_f32_16x16x32_bf16 v[172:175], a[76:79], a[100:103], v[172:175]\n"
"s_sub_u32 s67, 1144960, s75\n"
"v_mfma_f32_16x16x32_bf16 v[172:175], a[72:75], a[96:99], v[172:175]\n"
"v_mfma_f32_16x16x32_bf16 v[176:179], a[72:75], a[104:107], v[176:179]\n"
"s_add_u32 s71, s71, 57344\n"
"v_mfma_f32_16x16x32_bf16 v[176:179], a[76:79], a[108:111], v[176:179]\n"
"buffer_load_dwordx4 v11, s[52:55], s67 offen offset:2048 lds\n"
"v_mfma_f32_16x16x32_bf16 v[148:151], a[68:71], a[108:111], v[148:151]\n"
"v_add_u32 v20, s71, v9\n"
"v_mfma_f32_16x16x32_bf16 v[148:151], a[64:67], a[104:107], v[148:151]\n"
"s_sub_u32 s67, 1160320, s75\n"
"v_mfma_f32_16x16x32_bf16 v[152:155], a[64:67], a[112:115], v[152:155]\n"
"s_sub_u32 s66, 0, s73\n"
"v_mfma_f32_16x16x32_bf16 v[152:155], a[68:71], a[116:119], v[152:155]\n"
"buffer_load_dwordx4 v11, s[52:55], s67 offen offset:3072 lds\n"
"v_add_u32 v21, s71, v15\n"
"v_mfma_f32_16x16x32_bf16 v[180:183], a[76:79], a[116:119], v[180:183]\n"
"v_add_u32 v12, s66, v7\n"
"v_mfma_f32_16x16x32_bf16 v[180:183], a[72:75], a[112:115], v[180:183]\n"
"s_cmp_lt_u32 %16, 2\n"
"s_cselect_b32 s67, 16384, 0\n"
"v_mfma_f32_16x16x32_bf16 v[184:187], a[72:75], a[120:123], v[184:187]\n"
"v_add_u32 v13, s66, v8\n"
"v_mfma_f32_16x16x32_bf16 v[184:187], a[76:79], a[124:127], v[184:187]\n"
"s_waitcnt vmcnt(20)\n"
"ds_read_b64_tr_b16 a[176:177], v20 offset:0\n"
"v_mfma_f32_16x16x32_bf16 v[156:159], a[68:71], a[124:127], v[156:159]\n"
"ds_read_b64_tr_b16 a[178:179], v20 offset:512\n"
"v_mfma_f32_16x16x32_bf16 v[156:159], a[64:67], a[120:123], v[156:159]\n"
"ds_read_b64_tr_b16 a[180:181], v20 offset:8\n"
"v_mfma_f32_16x16x32_bf16 v[160:163], a[64:67], a[128:131], v[160:163]\n"
"s_add_u32 s67, s67, s72\n"
"ds_read_b64_tr_b16 a[182:183], v20 offset:520\n"
"v_mfma_f32_16x16x32_bf16 v[160:163], a[68:71], a[132:135], v[160:163]\n"
"v_mfma_f32_16x16x32_bf16 v[188:191], a[76:79], a[132:135], v[188:191]\n"
"ds_read_b64_tr_b16 a[184:185], v21 offset:0\n"
"v_mfma_f32_16x16x32_bf16 v[188:191], a[72:75], a[128:131], v[188:191]\n"
"ds_read_b64_tr_b16 a[186:187], v21 offset:512\n"
"v_mfma_f32_16x16x32_bf16 v[192:195], a[72:75], a[136:139], v[192:195]\n"
"s_add_u32 m0, s67, 57344\n"
"ds_read_b64_tr_b16 a[188:189], v21 offset:8\n"
"v_mfma_f32_16x16x32_bf16 v[192:195], a[76:79], a[140:143], v[192:195]\n"
"v_mfma_f32_16x16x32_bf16 v[164:167], a[68:71], a[140:143], v[164:167]\n"
"ds_read_b64_tr_b16 a[190:191], v21 offset:520\n"
"v_mfma_f32_16x16x32_bf16 v[164:167], a[64:67], a[136:139], v[164:167]\n"
"v_mfma_f32_16x16x32_bf16 v[168:171], a[64:67], a[144:147], v[168:171]\n"
"s_sub_u32 s67, 1280, s60\n"
"v_mfma_f32_16x16x32_bf16 v[168:171], a[68:71], a[148:151], v[168:171]\n"
"v_mfma_f32_16x16x32_bf16 v[196:199], a[76:79], a[148:151], v[196:199]\n"
"v_mfma_f32_16x16x32_bf16 v[196:199], a[72:75], a[144:147], v[196:199]\n"
"v_mfma_f32_16x16x32_bf16 a[20:23], a[72:75], a[152:155], a[20:23]\n"
"s_waitcnt lgkmcnt(0)\n"
"v_mfma_f32_16x16x32_bf16 a[20:23], a[76:79], a[156:159], a[20:23]\n"
"buffer_load_dwordx4 v6, s[48:51], s67 offen offset:0  lds\n"
"v_mfma_f32_16x16x32_bf16 a[16:19], a[68:71], a[156:159], a[16:19]\n"
"s_sub_u32 s67, 16640, s60\n"
"v_mfma_f32_16x16x32_bf16 a[16:19], a[64:67], a[152:155], a[16:19]\n"
"s_waitcnt vmcnt(17)\n"
"s_barrier\n"
"v_mfma_f32_16x16x32_bf16 v[200:203], a[80:83], a[96:99], v[200:203]\n"
"s_waitcnt vmcnt(9)\n"
"ds_read_b64_tr_b16 a[32:33], v9 offset:8192\n"
"v_mfma_f32_16x16x32_bf16 v[200:203], a[84:87], a[100:103], v[200:203]\n"
"ds_read_b64_tr_b16 a[34:35], v9 offset:8704\n"
"v_mfma_f32_16x16x32_bf16 v[228:231], a[92:95], a[100:103], v[228:231]\n"
"ds_read_b128 a[100:103], v13 offset:32768\n"
"buffer_load_dwordx4 v6, s[48:51], s67 offen offset:1024  lds\n"
"v_mfma_f32_16x16x32_bf16 v[228:231], a[88:91], a[96:99], v[228:231]\n"
"ds_read_b128 a[96:99], v12 offset:32768\n"
"v_mfma_f32_16x16x32_bf16 v[232:235], a[88:91], a[104:107], v[232:235]\n"
"s_sub_u32 s67, 32000, s60\n"
"ds_read_b64_tr_b16 a[36:37], v9 offset:12288\n"
"v_mfma_f32_16x16x32_bf16 v[232:235], a[92:95], a[108:111], v[232:235]\n"
"ds_read_b64_tr_b16 a[38:39], v9 offset:12800\n"
"v_mfma_f32_16x16x32_bf16 v[204:207], a[84:87], a[108:111], v[204:207]\n"
"ds_read_b128 a[108:111], v13 offset:34816\n"
"buffer_load_dwordx4 v6, s[48:51], s67 offen offset:2048  lds\n"
"v_mfma_f32_16x16x32_bf16 v[204:207], a[80:83], a[104:107], v[204:207]\n"
"ds_read_b128 a[104:107], v12 offset:34816\n"
"ds_read_b64_tr_b16 a[40:41], v9 offset:8200\n"
"v_mfma_f32_16x16x32_bf16 v[208:211], a[80:83], a[112:115], v[208:211]\n"
"s_sub_u32 s67, 47360, s60\n"
"ds_read_b64_tr_b16 a[42:43], v9 offset:8712\n"
"v_mfma_f32_16x16x32_bf16 v[208:211], a[84:87], a[116:119], v[208:211]\n"
"ds_read_b64_tr_b16 a[44:45], v9 offset:12296\n"
"v_mfma_f32_16x16x32_bf16 v[236:239], a[92:95], a[116:119], v[236:239]\n"
"ds_read_b128 a[116:119], v13 offset:36864\n"
"v_mfma_f32_16x16x32_bf16 v[236:239], a[88:91], a[112:115], v[236:239]\n"
"ds_read_b128 a[112:115], v12 offset:36864\n"
"buffer_load_dwordx4 v6, s[48:51], s67 offen offset:3072  lds\n"
"v_mfma_f32_16x16x32_bf16 v[240:243], a[88:91], a[120:123], v[240:243]\n"
"ds_read_b64_tr_b16 a[46:47], v9 offset:12808\n"
"v_mfma_f32_16x16x32_bf16 v[240:243], a[92:95], a[124:127], v[240:243]\n"
"s_add_u32 m0, s64, 20352\n"
"ds_read_b64_tr_b16 a[48:49], v15 offset:8192\n"
"v_mfma_f32_16x16x32_bf16 v[212:215], a[84:87], a[124:127], v[212:215]\n"
"ds_read_b128 a[124:127], v13 offset:38912\n"
"v_mfma_f32_16x16x32_bf16 v[212:215], a[80:83], a[120:123], v[212:215]\n"
"ds_read_b128 a[120:123], v12 offset:38912\n"
"buffer_load_dwordx4 v6, s[40:43], s84 offen offset:128  lds\n"
"v_mfma_f32_16x16x32_bf16 v[216:219], a[80:83], a[128:131], v[216:219]\n"
"ds_read_b64_tr_b16 a[50:51], v15 offset:8704\n"
"v_mfma_f32_16x16x32_bf16 v[216:219], a[84:87], a[132:135], v[216:219]\n"
"ds_read_b64_tr_b16 a[52:53], v15 offset:12288\n"
"v_mfma_f32_16x16x32_bf16 v[244:247], a[92:95], a[132:135], v[244:247]\n"
"ds_read_b128 a[132:135], v13 offset:24576\n"
"v_mfma_f32_16x16x32_bf16 v[244:247], a[88:91], a[128:131], v[244:247]\n"
"ds_read_b128 a[128:131], v12 offset:24576\n"
"buffer_load_dwordx4 v6, s[40:43], s85 offen offset:1152  lds\n"
"v_mfma_f32_16x16x32_bf16 v[248:251], a[88:91], a[136:139], v[248:251]\n"
"ds_read_b64_tr_b16 a[54:55], v15 offset:12800\n"
"v_mfma_f32_16x16x32_bf16 v[248:251], a[92:95], a[140:143], v[248:251]\n"
"ds_read_b64_tr_b16 a[56:57], v15 offset:8200\n"
"v_mfma_f32_16x16x32_bf16 v[220:223], a[84:87], a[140:143], v[220:223]\n"
"ds_read_b128 a[140:143], v13 offset:26624\n"
"v_mfma_f32_16x16x32_bf16 v[220:223], a[80:83], a[136:139], v[220:223]\n"
"ds_read_b128 a[136:139], v12 offset:26624\n"
"v_mfma_f32_16x16x32_bf16 v[224:227], a[80:83], a[144:147], v[224:227]\n"
"ds_read_b64_tr_b16 a[58:59], v15 offset:8712\n"
"buffer_load_dwordx4 v6, s[40:43], s86 offen offset:2176  lds\n"
"v_mfma_f32_16x16x32_bf16 v[224:227], a[84:87], a[148:151], v[224:227]\n"
"ds_read_b64_tr_b16 a[60:61], v15 offset:12296\n"
"v_mfma_f32_16x16x32_bf16 v[252:255], a[92:95], a[148:151], v[252:255]\n"
"ds_read_b128 a[148:151], v13 offset:28672\n"
"v_mfma_f32_16x16x32_bf16 v[252:255], a[88:91], a[144:147], v[252:255]\n"
"ds_read_b128 a[144:147], v12 offset:28672\n"
"ds_read_b64_tr_b16 a[62:63], v15 offset:12808\n"
"v_mfma_f32_16x16x32_bf16 a[28:31], a[88:91], a[152:155], a[28:31]\n"
"buffer_load_dwordx4 v6, s[40:43], s87 offen offset:3200  lds\n"
"v_mfma_f32_16x16x32_bf16 a[28:31], a[92:95], a[156:159], a[28:31]\n"
"v_mfma_f32_16x16x32_bf16 a[24:27], a[84:87], a[156:159], a[24:27]\n"
"s_mov_b32 s38, 1572864\n"
"ds_read_b128 a[156:159], v13 offset:30720\n"
"v_mfma_f32_16x16x32_bf16 a[24:27], a[80:83], a[152:155], a[24:27]\n"
"s_waitcnt lgkmcnt(15)\n"
"ds_read_b128 a[152:155], v12 offset:30720\n"
"v_mfma_f32_16x16x32_bf16 v[32:35], a[32:35], a[96:99], v[32:35]\n"
"v_mfma_f32_16x16x32_bf16 v[32:35], a[36:39], a[100:103], v[32:35]\n"
"s_waitcnt lgkmcnt(2)\n"
"v_mfma_f32_16x16x32_bf16 v[60:63], a[44:47], a[100:103], v[60:63]\n"
"buffer_store_dwordx4 a[176:179], v0, s[36:39], %23 offen offset:0 sc1\n"
"v_mfma_f32_16x16x32_bf16 v[60:63], a[40:43], a[96:99], v[60:63]\n"
"s_add_u32 m0, s64, 8192\n"
"v_mfma_f32_16x16x32_bf16 v[64:67], a[40:43], a[104:107], v[64:67]\n"
"v_mfma_f32_16x16x32_bf16 v[64:67], a[44:47], a[108:111], v[64:67]\n"
"s_sub_u32 s67, 1310720, s75\n"
"v_mfma_f32_16x16x32_bf16 v[36:39], a[36:39], a[108:111], v[36:39]\n"
"v_mfma_f32_16x16x32_bf16 v[36:39], a[32:35], a[104:107], v[36:39]\n"
"buffer_load_dwordx4 v11, s[52:55], s67 offen offset:0 lds\n"
"v_mfma_f32_16x16x32_bf16 v[40:43], a[32:35], a[112:115], v[40:43]\n"
"v_mfma_f32_16x16x32_bf16 v[40:43], a[36:39], a[116:119], v[40:43]\n"
"s_sub_u32 s67, 1326080, s75\n"
"v_mfma_f32_16x16x32_bf16 v[68:71], a[44:47], a[116:119], v[68:71]\n"
"v_mfma_f32_16x16x32_bf16 v[68:71], a[40:43], a[112:115], v[68:71]\n"
"buffer_load_dwordx4 v11, s[52:55], s67 offen offset:1024 lds\n"
"v_mfma_f32_16x16x32_bf16 v[72:75], a[40:43], a[120:123], v[72:75]\n"
"v_mfma_f32_16x16x32_bf16 v[72:75], a[44:47], a[124:127], v[72:75]\n"
"s_sub_u32 s67, 1341440, s75\n"
"v_mfma_f32_16x16x32_bf16 v[44:47], a[36:39], a[124:127], v[44:47]\n"
"v_mfma_f32_16x16x32_bf16 v[44:47], a[32:35], a[120:123], v[44:47]\n"
"v_mfma_f32_16x16x32_bf16 v[48:51], a[32:35], a[128:131], v[48:51]\n"
"buffer_load_dwordx4 v11, s[52:55], s67 offen offset:2048 lds\n"
"v_mfma_f32_16x16x32_bf16 v[48:51], a[36:39], a[132:135], v[48:51]\n"
"s_sub_u32 s67, 1356800, s75\n"
"v_mfma_f32_16x16x32_bf16 v[76:79], a[44:47], a[132:135], v[76:79]\n"
"s_waitcnt vmcnt(12)\n"
"ds_read_b64_tr_b16 a[64:65], v9 offset:0\n"
"v_mfma_f32_16x16x32_bf16 v[76:79], a[40:43], a[128:131], v[76:79]\n"
"ds_read_b64_tr_b16 a[66:67], v9 offset:512\n"
"v_mfma_f32_16x16x32_bf16 v[80:83], a[40:43], a[136:139], v[80:83]\n"
"ds_read_b64_tr_b16 a[68:69], v9 offset:4096\n"
"buffer_load_dwordx4 v11, s[52:55], s67 offen offset:3072 lds\n"
"v_mfma_f32_16x16x32_bf16 v[80:83], a[44:47], a[140:143], v[80:83]\n"
"ds_read_b64_tr_b16 a[70:71], v9 offset:4608\n"
"v_mfma_f32_16x16x32_bf16 v[52:55], a[36:39], a[140:143], v[52:55]\n"
"s_add_u32 m0, s64, 12288\n"
"ds_read_b64_tr_b16 a[72:73], v9 offset:8\n"
"v_mfma_f32_16x16x32_bf16 v[52:55], a[32:35], a[136:139], v[52:55]\n"
"s_waitcnt lgkmcnt(5)\n"
"ds_read_b64_tr_b16 a[74:75], v9 offset:520\n"
"v_mfma_f32_16x16x32_bf16 v[56:59], a[32:35], a[144:147], v[56:59]\n"
"s_sub_u32 s67, 1376256, s75\n"
"ds_read_b64_tr_b16 a[76:77], v9 offset:4104\n"
"v_mfma_f32_16x16x32_bf16 v[56:59], a[36:39], a[148:151], v[56:59]\n"
"ds_read_b64_tr_b16 a[78:79], v9 offset:4616\n"
"v_mfma_f32_16x16x32_bf16 v[84:87], a[44:47], a[148:151], v[84:87]\n"
"buffer_load_dwordx4 v11, s[52:55], s67 offen offset:0 lds\n"
"v_mfma_f32_16x16x32_bf16 v[84:87], a[40:43], a[144:147], v[84:87]\n"
"ds_read_b64_tr_b16 a[80:81], v15 offset:0\n"
"v_mfma_f32_16x16x32_bf16 a[4:7], a[40:43], a[152:155], a[4:7]\n"
"s_sub_u32 s67, 1391616, s75\n"
"ds_read_b64_tr_b16 a[82:83], v15 offset:512\n"
"v_mfma_f32_16x16x32_bf16 a[4:7], a[44:47], a[156:159], a[4:7]\n"
"ds_read_b64_tr_b16 a[84:85], v15 offset:4096\n"
"v_mfma_f32_16x16x32_bf16 a[0:3], a[36:39], a[156:159], a[0:3]\n"
"ds_read_b64_tr_b16 a[86:87], v15 offset:4608\n"
"buffer_load_dwordx4 v11, s[52:55], s67 offen offset:1024 lds\n"
"v_mfma_f32_16x16x32_bf16 a[0:3], a[32:35], a[152:155], a[0:3]\n"
"ds_read_b64_tr_b16 a[88:89], v15 offset:8\n"
"v_mfma_f32_16x16x32_bf16 v[88:91], a[48:51], a[96:99], v[88:91]\n"
"s_sub_u32 s67, 1406976, s75\n"
"ds_read_b64_tr_b16 a[90:91], v15 offset:520\n"
"v_mfma_f32_16x16x32_bf16 v[88:91], a[52:55], a[100:103], v[88:91]\n"
"ds_read_b64_tr_b16 a[92:93], v15 offset:4104\n"
"v_mfma_f32_16x16x32_bf16 v[116:119], a[60:63], a[100:103], v[116:119]\n"
"ds_read_b64_tr_b16 a[94:95], v15 offset:4616\n"
"v_mfma_f32_16x16x32_bf16 v[116:119], a[56:59], a[96:99], v[116:119]\n"
"buffer_load_dwordx4 v11, s[52:55], s67 offen offset:2048 lds\n"
"v_mfma_f32_16x16x32_bf16 v[120:123], a[56:59], a[104:107], v[120:123]\n"
"v_mfma_f32_16x16x32_bf16 v[120:123], a[60:63], a[108:111], v[120:123]\n"
"s_sub_u32 s67, 1422336, s75\n"
"v_mfma_f32_16x16x32_bf16 v[92:95], a[52:55], a[108:111], v[92:95]\n"
"v_mfma_f32_16x16x32_bf16 v[92:95], a[48:51], a[104:107], v[92:95]\n"
"buffer_load_dwordx4 v11, s[52:55], s67 offen offset:3072 lds\n"
"v_mfma_f32_16x16x32_bf16 v[96:99], a[48:51], a[112:115], v[96:99]\n"
"s_waitcnt lgkmcnt(0)\n"
"s_add_u32 m0, s64, 0\n"
"v_mfma_f32_16x16x32_bf16 v[96:99], a[52:55], a[116:119], v[96:99]\n"
"v_mfma_f32_16x16x32_bf16 v[124:127], a[60:63], a[116:119], v[124:127]\n"
"s_sub_u32 s67, 1310848, s75\n"
"v_mfma_f32_16x16x32_bf16 v[124:127], a[56:59], a[112:115], v[124:127]\n"
"v_mfma_f32_16x16x32_bf16 v[128:131], a[56:59], a[120:123], v[128:131]\n"
"buffer_load_dwordx4 v11, s[52:55], s67 offen offset:0 lds\n"
"v_mfma_f32_16x16x32_bf16 v[128:131], a[60:63], a[124:127], v[128:131]\n"
"v_mfma_f32_16x16x32_bf16 v[100:103], a[52:55], a[124:127], v[100:103]\n"
"s_sub_u32 s67, 1326208, s75\n"
"v_mfma_f32_16x16x32_bf16 v[100:103], a[48:51], a[120:123], v[100:103]\n"
"v_mfma_f32_16x16x32_bf16 v[104:107], a[48:51], a[128:131], v[104:107]\n"
"buffer_load_dwordx4 v11, s[52:55], s67 offen offset:1024 lds\n"
"v_mfma_f32_16x16x32_bf16 v[104:107], a[52:55], a[132:135], v[104:107]\n"
"v_mfma_f32_16x16x32_bf16 v[132:135], a[60:63], a[132:135], v[132:135]\n"
"s_sub_u32 s67, 1341568, s75\n"
"v_mfma_f32_16x16x32_bf16 v[132:135], a[56:59], a[128:131], v[132:135]\n"
"v_mfma_f32_16x16x32_bf16 v[136:139], a[56:59], a[136:139], v[136:139]\n"
"buffer_load_dwordx4 v11, s[52:55], s67 offen offset:2048 lds\n"
"v_mfma_f32_16x16x32_bf16 v[136:139], a[60:63], a[140:143], v[136:139]\n"
"v_mfma_f32_16x16x32_bf16 v[108:111], a[52:55], a[140:143], v[108:111]\n"
"s_sub_u32 s67, 1356928, s75\n"
"v_mfma_f32_16x16x32_bf16 v[108:111], a[48:51], a[136:139], v[108:111]\n"
"v_mfma_f32_16x16x32_bf16 v[112:115], a[48:51], a[144:147], v[112:115]\n"
"v_mfma_f32_16x16x32_bf16 v[112:115], a[52:55], a[148:151], v[112:115]\n"
"buffer_load_dwordx4 v11, s[52:55], s67 offen offset:3072 lds\n"
"v_mfma_f32_16x16x32_bf16 v[140:143], a[60:63], a[148:151], v[140:143]\n"
"v_mfma_f32_16x16x32_bf16 v[140:143], a[56:59], a[144:147], v[140:143]\n"
"s_add_u32 m0, s64, 4096\n"
"v_mfma_f32_16x16x32_bf16 a[12:15], a[56:59], a[152:155], a[12:15]\n"
"v_mfma_f32_16x16x32_bf16 a[12:15], a[60:63], a[156:159], a[12:15]\n"
"s_sub_u32 s67, 1376384, s75\n"
"v_mfma_f32_16x16x32_bf16 a[8:11], a[52:55], a[156:159], a[8:11]\n"
"v_mfma_f32_16x16x32_bf16 a[8:11], a[48:51], a[152:155], a[8:11]\n"
"buffer_load_dwordx4 v11, s[52:55], s67 offen offset:0 lds\n"
"v_mfma_f32_16x16x32_bf16 v[144:147], a[64:67], a[96:99], v[144:147]\n"
"v_mfma_f32_16x16x32_bf16 v[144:147], a[68:71], a[100:103], v[144:147]\n"
"s_sub_u32 s67, 1391744, s75\n"
"v_mfma_f32_16x16x32_bf16 v[172:175], a[76:79], a[100:103], v[172:175]\n"
"v_mfma_f32_16x16x32_bf16 v[172:175], a[72:75], a[96:99], v[172:175]\n"
"buffer_load_dwordx4 v11, s[52:55], s67 offen offset:1024 lds\n"
"v_mfma_f32_16x16x32_bf16 v[176:179], a[72:75], a[104:107], v[176:179]\n"
"v_mfma_f32_16x16x32_bf16 v[176:179], a[76:79], a[108:111], v[176:179]\n"
"s_sub_u32 s71, s64, s64\n"
"s_sub_u32 s67, 1407104, s75\n"
"v_mfma_f32_16x16x32_bf16 v[148:151], a[68:71], a[108:111], v[148:151]\n"
"v_mfma_f32_16x16x32_bf16 v[148:151], a[64:67], a[104:107], v[148:151]\n"
"buffer_load_dwordx4 v11, s[52:55], s67 offen offset:2048 lds\n"
"v_mfma_f32_16x16x32_bf16 v[152:155], a[64:67], a[112:115], v[152:155]\n"
"s_sub_u32 s66, 40960, s73\n"
"s_add_u32 s71, s71, 20480\n"
"v_mfma_f32_16x16x32_bf16 v[152:155], a[68:71], a[116:119], v[152:155]\n"
"s_sub_u32 s67, 1422464, s75\n"
"v_mfma_f32_16x16x32_bf16 v[180:183], a[76:79], a[116:119], v[180:183]\n"
"v_add_u32 v12, s66, v7\n"
"v_mfma_f32_16x16x32_bf16 v[180:183], a[72:75], a[112:115], v[180:183]\n"
"buffer_load_dwordx4 v11, s[52:55], s67 offen offset:3072 lds\n"
"v_add_u32 v20, s71, v7\n"
"v_mfma_f32_16x16x32_bf16 v[184:187], a[72:75], a[120:123], v[184:187]\n"
"s_cmp_lt_u32 %16, 2\n"
"s_cselect_b32 s67, 16384, 0\n"
"v_add_u32 v13, s66, v8\n"
"v_mfma_f32_16x16x32_bf16 v[184:187], a[76:79], a[124:127], v[184:187]\n"
"s_add_u32 s67, s67, s72\n"
"v_add_u32 v21, s71, v8\n"
"v_mfma_f32_16x16x32_bf16 v[156:159], a[68:71], a[124:127], v[156:159]\n"
"v_mfma_f32_16x16x32_bf16 v[156:159], a[64:67], a[120:123], v[156:159]\n"
"s_add_u32 m0, s67, 98304\n"
"s_sub_u32 s67, 1536, s60\n"
"v_mfma_f32_16x16x32_bf16 v[160:163], a[64:67], a[128:131], v[160:163]\n"
"s_waitcnt vmcnt(17)\n"
"ds_read_b128 a[208:211], v20 offset:0\n"
"v_mfma_f32_16x16x32_bf16 v[160:163], a[68:71], a[132:135], v[160:163]\n"
"buffer_load_dwordx4 v6, s[48:51], s67 offen offset:0  lds\n"
"v_mfma_f32_16x16x32_bf16 v[188:191], a[76:79], a[132:135], v[188:191]\n"
"ds_read_b128 a[212:215], v21 offset:0\n"
"v_mfma_f32_16x16x32_bf16 v[188:191], a[72:75], a[128:131], v[188:191]\n"
"s_sub_u32 s67, 16896, s60\n"
"ds_read_b128 a[216:219], v20 offset:2048\n"
"v_mfma_f32_16x16x32_bf16 v[192:195], a[72:75], a[136:139], v[192:195]\n"
"v_mfma_f32_16x16x32_bf16 v[192:195], a[76:79], a[140:143], v[192:195]\n"
"ds_read_b128 a[220:223], v21 offset:2048\n"
"v_mfma_f32_16x16x32_bf16 v[164:167], a[68:71], a[140:143], v[164:167]\n"
"buffer_load_dwordx4 v6, s[48:51], s67 offen offset:1024  lds\n"
"v_mfma_f32_16x16x32_bf16 v[164:167], a[64:67], a[136:139], v[164:167]\n"
"v_mfma_f32_16x16x32_bf16 v[168:171], a[64:67], a[144:147], v[168:171]\n"
"s_sub_u32 s67, 32256, s60\n"
"v_mfma_f32_16x16x32_bf16 v[168:171], a[68:71], a[148:151], v[168:171]\n"
"v_mfma_f32_16x16x32_bf16 v[196:199], a[76:79], a[148:151], v[196:199]\n"
"buffer_load_dwordx4 v6, s[48:51], s67 offen offset:2048  lds\n"
"v_mfma_f32_16x16x32_bf16 v[196:199], a[72:75], a[144:147], v[196:199]\n"
"s_waitcnt lgkmcnt(0)\n"
"v_mfma_f32_16x16x32_bf16 a[20:23], a[72:75], a[152:155], a[20:23]\n"
"s_sub_u32 s67, 47616, s60\n"
"v_mfma_f32_16x16x32_bf16 a[20:23], a[76:79], a[156:159], a[20:23]\n"
"v_mfma_f32_16x16x32_bf16 a[16:19], a[68:71], a[156:159], a[16:19]\n"
"buffer_load_dwordx4 v6, s[48:51], s67 offen offset:3072  lds\n"
"v_mfma_f32_16x16x32_bf16 a[16:19], a[64:67], a[152:155], a[16:19]\n"
"s_barrier\n"
"s_waitcnt vmcnt(12)\n"
"ds_read_b64_tr_b16 a[32:33], v9 offset:8192\n"
"v_mfma_f32_16x16x32_bf16 v[200:203], a[80:83], a[96:99], v[200:203]\n"
"s_add_u32 m0, s64, 16384\n"
"ds_read_b64_tr_b16 a[34:35], v9 offset:8704\n"
"v_mfma_f32_16x16x32_bf16 v[200:203], a[84:87], a[100:103], v[200:203]\n"
"s_add_u32 s67, %21, 4194304\n"
"ds_read_b64_tr_b16 a[36:37], v9 offset:12288\n"
"v_mfma_f32_16x16x32_bf16 v[228:231], a[92:95], a[100:103], v[228:231]\n"
"ds_read_b128 a[100:103], v13 offset:32768\n"
"v_mfma_f32_16x16x32_bf16 v[228:231], a[88:91], a[96:99], v[228:231]\n"
"ds_read_b128 a[96:99], v12 offset:32768\n"
"buffer_load_dwordx4 v11, s[52:55], s67 offen offset:0  lds\n"
"v_mfma_f32_16x16x32_bf16 v[232:235], a[88:91], a[104:107], v[232:235]\n"
"ds_read_b64_tr_b16 a[38:39], v9 offset:12800\n"
"v_mfma_f32_16x16x32_bf16 v[232:235], a[92:95], a[108:111], v[232:235]\n"
"s_add_u32 s67, %21, 4209664\n"
"ds_read_b64_tr_b16 a[40:41], v9 offset:8200\n"
"v_mfma_f32_16x16x32_bf16 v[204:207], a[84:87], a[108:111], v[204:207]\n"
"ds_read_b128 a[108:111], v13 offset:34816\n"
"v_mfma_f32_16x16x32_bf16 v[204:207], a[80:83], a[104:107], v[204:207]\n"
"ds_read_b128 a[104:107], v12 offset:34816\n"
"buffer_load_dwordx4 v11, s[52:55], s67 offen offset:1024  lds\n"
"v_mfma_f32_16x16x32_bf16 v[208:211], a[80:83], a[112:115], v[208:211]\n"
"ds_read_b64_tr_b16 a[42:43], v9 offset:8712\n"
"v_mfma_f32_16x16x32_bf16 v[208:211], a[84:87], a[116:119], v[208:211]\n"
"s_add_u32 s67, %21, 4225024\n"
"ds_read_b64_tr_b16 a[44:45], v9 offset:12296\n"
"v_mfma_f32_16x16x32_bf16 v[236:239], a[92:95], a[116:119], v[236:239]\n"
"ds_read_b128 a[116:119], v13 offset:36864\n"
"v_mfma_f32_16x16x32_bf16 v[236:239], a[88:91], a[112:115], v[236:239]\n"
"ds_read_b128 a[112:115], v12 offset:36864\n"
"v_mfma_f32_16x16x32_bf16 v[240:243], a[88:91], a[120:123], v[240:243]\n"
"ds_read_b64_tr_b16 a[46:47], v9 offset:12808\n"
"buffer_load_dwordx4 v11, s[52:55], s67 offen offset:2048  lds\n"
"v_mfma_f32_16x16x32_bf16 v[240:243], a[92:95], a[124:127], v[240:243]\n"
"ds_read_b64_tr_b16 a[48:49], v15 offset:8192\n"
"v_mfma_f32_16x16x32_bf16 v[212:215], a[84:87], a[124:127], v[212:215]\n"
"s_add_u32 s67, %21, 4240384\n"
"ds_read_b128 a[124:127], v13 offset:38912\n"
"v_mfma_f32_16x16x32_bf16 v[212:215], a[80:83], a[120:123], v[212:215]\n"
"ds_read_b128 a[120:123], v12 offset:38912\n"
"v_mfma_f32_16x16x32_bf16 v[216:219], a[80:83], a[128:131], v[216:219]\n"
"ds_read_b64_tr_b16 a[50:51], v15 offset:8704\n"
"buffer_load_dwordx4 v11, s[52:55], s67 offen offset:3072  lds\n"
"v_mfma_f32_16x16x32_bf16 v[216:219], a[84:87], a[132:135], v[216:219]\n"
"ds_read_b64_tr_b16 a[52:53], v15 offset:12288\n"
"v_mfma_f32_16x16x32_bf16 v[244:247], a[92:95], a[132:135], v[244:247]\n"
"ds_read_b128 a[132:135], v13 offset:24576\n"
"v_mfma_f32_16x16x32_bf16 v[244:247], a[88:91], a[128:131], v[244:247]\n"
"ds_read_b128 a[128:131], v12 offset:24576\n"
"v_mfma_f32_16x16x32_bf16 v[248:251], a[88:91], a[136:139], v[248:251]\n"
"s_add_u32 m0, s64, 8192\n"
"ds_read_b64_tr_b16 a[54:55], v15 offset:12800\n"
"v_mfma_f32_16x16x32_bf16 v[248:251], a[92:95], a[140:143], v[248:251]\n"
"ds_read_b64_tr_b16 a[56:57], v15 offset:8200\n"
"v_mfma_f32_16x16x32_bf16 v[220:223], a[84:87], a[140:143], v[220:223]\n"
"ds_read_b128 a[140:143], v13 offset:26624\n"
"v_mfma_f32_16x16x32_bf16 v[220:223], a[80:83], a[136:139], v[220:223]\n"
"ds_read_b128 a[136:139], v12 offset:26624\n"
"v_mfma_f32_16x16x32_bf16 v[224:227], a[80:83], a[144:147], v[224:227]\n"
"ds_read_b64_tr_b16 a[58:59], v15 offset:8712\n"
"v_mfma_f32_16x16x32_bf16 v[224:227], a[84:87], a[148:151], v[224:227]\n"
"ds_read_b64_tr_b16 a[60:61], v15 offset:12296\n"
"v_mfma_f32_16x16x32_bf16 v[252:255], a[92:95], a[148:151], v[252:255]\n"
"s_sub_u32 s67, 1572864, s75\n"
"ds_read_b128 a[148:151], v13 offset:28672\n"
"v_mfma_f32_16x16x32_bf16 v[252:255], a[88:91], a[144:147], v[252:255]\n"
"ds_read_b128 a[144:147], v12 offset:28672\n"
"ds_read_b64_tr_b16 a[62:63], v15 offset:12808\n"
"v_mfma_f32_16x16x32_bf16 a[28:31], a[88:91], a[152:155], a[28:31]\n"
"v_mfma_f32_16x16x32_bf16 a[28:31], a[92:95], a[156:159], a[28:31]\n"
"v_mfma_f32_16x16x32_bf16 a[24:27], a[84:87], a[156:159], a[24:27]\n"
"ds_read_b128 a[156:159], v13 offset:30720\n"
"v_mfma_f32_16x16x32_bf16 a[24:27], a[80:83], a[152:155], a[24:27]\n"
"s_waitcnt lgkmcnt(15)\n"
"ds_read_b128 a[152:155], v12 offset:30720\n"
"v_mfma_f32_16x16x32_bf16 v[32:35], a[32:35], a[96:99], v[32:35]\n"
"v_mfma_f32_16x16x32_bf16 v[32:35], a[36:39], a[100:103], v[32:35]\n"
"s_waitcnt lgkmcnt(2)\n"
"v_mfma_f32_16x16x32_bf16 v[60:63], a[44:47], a[100:103], v[60:63]\n"
"buffer_load_dwordx4 v11, s[52:55], s67 offen offset:0 lds\n"
"v_mfma_f32_16x16x32_bf16 v[60:63], a[40:43], a[96:99], v[60:63]\n"
"v_mfma_f32_16x16x32_bf16 v[64:67], a[40:43], a[104:107], v[64:67]\n"
"s_sub_u32 s67, 1588224, s75\n"
"v_mfma_f32_16x16x32_bf16 v[64:67], a[44:47], a[108:111], v[64:67]\n"
"v_mfma_f32_16x16x32_bf16 v[36:39], a[36:39], a[108:111], v[36:39]\n"
"buffer_load_dwordx4 v11, s[52:55], s67 offen offset:1024 lds\n"
"v_mfma_f32_16x16x32_bf16 v[36:39], a[32:35], a[104:107], v[36:39]\n"
"v_mfma_f32_16x16x32_bf16 v[40:43], a[32:35], a[112:115], v[40:43]\n"
"s_sub_u32 s67, 1603584, s75\n"
"v_mfma_f32_16x16x32_bf16 v[40:43], a[36:39], a[116:119], v[40:43]\n"
"v_mfma_f32_16x16x32_bf16 v[68:71], a[44:47], a[116:119], v[68:71]\n"
"v_mfma_f32_16x16x32_bf16 v[68:71], a[40:43], a[112:115], v[68:71]\n"
"buffer_load_dwordx4 v11, s[52:55], s67 offen offset:2048 lds\n"
"v_mfma_f32_16x16x32_bf16 v[72:75], a[40:43], a[120:123], v[72:75]\n"
"v_mfma_f32_16x16x32_bf16 v[72:75], a[44:47], a[124:127], v[72:75]\n"
"s_sub_u32 s67, 1618944, s75\n"
"v_mfma_f32_16x16x32_bf16 v[44:47], a[36:39], a[124:127], v[44:47]\n"
"v_mfma_f32_16x16x32_bf16 v[44:47], a[32:35], a[120:123], v[44:47]\n"
"v_mfma_f32_16x16x32_bf16 v[48:51], a[32:35], a[128:131], v[48:51]\n"
"buffer_load_dwordx4 v11, s[52:55], s67 offen offset:3072 lds\n"
"v_mfma_f32_16x16x32_bf16 v[48:51], a[36:39], a[132:135], v[48:51]\n"
"s_add_u32 m0, s64, 12288\n"
"v_mfma_f32_16x16x32_bf16 v[76:79], a[44:47], a[132:135], v[76:79]\n"
"s_waitcnt vmcnt(12)\n"
"ds_read_b64_tr_b16 a[64:65], v9 offset:0\n"
"v_mfma_f32_16x16x32_bf16 v[76:79], a[40:43], a[128:131], v[76:79]\n"
"s_sub_u32 s67, 1638400, s75\n"
"ds_read_b64_tr_b16 a[66:67], v9 offset:512\n"
"v_mfma_f32_16x16x32_bf16 v[80:83], a[40:43], a[136:139], v[80:83]\n"
"ds_read_b64_tr_b16 a[68:69], v9 offset:4096\n"
"v_mfma_f32_16x16x32_bf16 v[80:83], a[44:47], a[140:143], v[80:83]\n"
"ds_read_b64_tr_b16 a[70:71], v9 offset:4608\n"
"buffer_load_dwordx4 v11, s[52:55], s67 offen offset:0 lds\n"
"v_mfma_f32_16x16x32_bf16 v[52:55], a[36:39], a[140:143], v[52:55]\n"
"s_sub_u32 s67, 1653760, s75\n"
"ds_read_b64_tr_b16 a[72:73], v9 offset:8\n"
"v_mfma_f32_16x16x32_bf16 v[52:55], a[32:35], a[136:139], v[52:55]\n"
"s_waitcnt lgkmcnt(5)\n"
"ds_read_b64_tr_b16 a[74:75], v9 offset:520\n"
"v_mfma_f32_16x16x32_bf16 v[56:59], a[32:35], a[144:147], v[56:59]\n"
"ds_read_b64_tr_b16 a[76:77], v9 offset:4104\n"
"v_mfma_f32_16x16x32_bf16 v[56:59], a[36:39], a[148:151], v[56:59]\n"
"ds_read_b64_tr_b16 a[78:79], v9 offset:4616\n"
"v_mfma_f32_16x16x32_bf16 v[84:87], a[44:47], a[148:151], v[84:87]\n"
"buffer_load_dwordx4 v11, s[52:55], s67 offen offset:1024 lds\n"
"v_mfma_f32_16x16x32_bf16 v[84:87], a[40:43], a[144:147], v[84:87]\n"
"ds_read_b64_tr_b16 a[80:81], v15 offset:0\n"
"v_mfma_f32_16x16x32_bf16 a[4:7], a[40:43], a[152:155], a[4:7]\n"
"s_sub_u32 s67, 1669120, s75\n"
"ds_read_b64_tr_b16 a[82:83], v15 offset:512\n"
"v_mfma_f32_16x16x32_bf16 a[4:7], a[44:47], a[156:159], a[4:7]\n"
"ds_read_b64_tr_b16 a[84:85], v15 offset:4096\n"
"v_mfma_f32_16x16x32_bf16 a[0:3], a[36:39], a[156:159], a[0:3]\n"
"ds_read_b64_tr_b16 a[86:87], v15 offset:4608\n"
"buffer_load_dwordx4 v11, s[52:55], s67 offen offset:2048 lds\n"
"v_mfma_f32_16x16x32_bf16 a[0:3], a[32:35], a[152:155], a[0:3]\n"
"ds_read_b64_tr_b16 a[88:89], v15 offset:8\n"
"v_mfma_f32_16x16x32_bf16 v[88:91], a[48:51], a[96:99], v[88:91]\n"
"s_sub_u32 s67, 1684480, s75\n"
"ds_read_b64_tr_b16 a[90:91], v15 offset:520\n"
"v_mfma_f32_16x16x32_bf16 v[88:91], a[52:55], a[100:103], v[88:91]\n"
"ds_read_b64_tr_b16 a[92:93], v15 offset:4104\n"
"v_mfma_f32_16x16x32_bf16 v[116:119], a[60:63], a[100:103], v[116:119]\n"
"ds_read_b64_tr_b16 a[94:95], v15 offset:4616\n"
"v_mfma_f32_16x16x32_bf16 v[116:119], a[56:59], a[96:99], v[116:119]\n"
"buffer_load_dwordx4 v11, s[52:55], s67 offen offset:3072 lds\n"
"v_mfma_f32_16x16x32_bf16 v[120:123], a[56:59], a[104:107], v[120:123]\n"
"v_mfma_f32_16x16x32_bf16 v[120:123], a[60:63], a[108:111], v[120:123]\n"
"s_add_u32 m0, s64, 0\n"
"v_mfma_f32_16x16x32_bf16 v[92:95], a[52:55], a[108:111], v[92:95]\n"
"v_mfma_f32_16x16x32_bf16 v[92:95], a[48:51], a[104:107], v[92:95]\n"
"s_sub_u32 s67, 1572992, s75\n"
"v_mfma_f32_16x16x32_bf16 v[96:99], a[48:51], a[112:115], v[96:99]\n"
"s_waitcnt lgkmcnt(0)\n"
"v_mfma_f32_16x16x32_bf16 v[96:99], a[52:55], a[116:119], v[96:99]\n"
"buffer_load_dwordx4 v11, s[52:55], s67 offen offset:0 lds\n"
"v_mfma_f32_16x16x32_bf16 v[124:127], a[60:63], a[116:119], v[124:127]\n"
"v_mfma_f32_16x16x32_bf16 v[124:127], a[56:59], a[112:115], v[124:127]\n"
"s_sub_u32 s67, 1588352, s75\n"
"v_mfma_f32_16x16x32_bf16 v[128:131], a[56:59], a[120:123], v[128:131]\n"
"v_mfma_f32_16x16x32_bf16 v[128:131], a[60:63], a[124:127], v[128:131]\n"
"v_mfma_f32_16x16x32_bf16 v[100:103], a[52:55], a[124:127], v[100:103]\n"
"buffer_load_dwordx4 v11, s[52:55], s67 offen offset:1024 lds\n"
"v_mfma_f32_16x16x32_bf16 v[100:103], a[48:51], a[120:123], v[100:103]\n"
"v_mfma_f32_16x16x32_bf16 v[104:107], a[48:51], a[128:131], v[104:107]\n"
"s_sub_u32 s67, 1603712, s75\n"
"v_mfma_f32_16x16x32_bf16 v[104:107], a[52:55], a[132:135], v[104:107]\n"
"v_mfma_f32_16x16x32_bf16 v[132:135], a[60:63], a[132:135], v[132:135]\n"
"v_mfma_f32_16x16x32_bf16 v[132:135], a[56:59], a[128:131], v[132:135]\n"
"buffer_load_dwordx4 v11, s[52:55], s67 offen offset:2048 lds\n"
"v_mfma_f32_16x16x32_bf16 v[136:139], a[56:59], a[136:139], v[136:139]\n"
"v_mfma_f32_16x16x32_bf16 v[136:139], a[60:63], a[140:143], v[136:139]\n"
"s_sub_u32 s67, 1619072, s75\n"
"v_mfma_f32_16x16x32_bf16 v[108:111], a[52:55], a[140:143], v[108:111]\n"
"v_mfma_f32_16x16x32_bf16 v[108:111], a[48:51], a[136:139], v[108:111]\n"
"buffer_load_dwordx4 v11, s[52:55], s67 offen offset:3072 lds\n"
"v_mfma_f32_16x16x32_bf16 v[112:115], a[48:51], a[144:147], v[112:115]\n"
"v_mfma_f32_16x16x32_bf16 v[112:115], a[52:55], a[148:151], v[112:115]\n"
"s_add_u32 m0, s64, 4096\n"
"v_mfma_f32_16x16x32_bf16 v[140:143], a[60:63], a[148:151], v[140:143]\n"
"v_mfma_f32_16x16x32_bf16 v[140:143], a[56:59], a[144:147], v[140:143]\n"
"s_sub_u32 s67, 1638528, s75\n"
"v_mfma_f32_16x16x32_bf16 a[12:15], a[56:59], a[152:155], a[12:15]\n"
"v_mfma_f32_16x16x32_bf16 a[12:15], a[60:63], a[156:159], a[12:15]\n"
"buffer_load_dwordx4 v11, s[52:55], s67 offen offset:0 lds\n"
"v_mfma_f32_16x16x32_bf16 a[8:11], a[52:55], a[156:159], a[8:11]\n"
"v_mfma_f32_16x16x32_bf16 a[8:11], a[48:51], a[152:155], a[8:11]\n"
"s_sub_u32 s67, 1653888, s75\n"
"v_mfma_f32_16x16x32_bf16 v[144:147], a[64:67], a[96:99], v[144:147]\n"
"v_mfma_f32_16x16x32_bf16 v[144:147], a[68:71], a[100:103], v[144:147]\n"
"buffer_load_dwordx4 v11, s[52:55], s67 offen offset:1024 lds\n"
"v_mfma_f32_16x16x32_bf16 v[172:175], a[76:79], a[100:103], v[172:175]\n"
"v_mfma_f32_16x16x32_bf16 v[172:175], a[72:75], a[96:99], v[172:175]\n"
"s_sub_u32 s67, 1669248, s75\n"
"v_mfma_f32_16x16x32_bf16 v[176:179], a[72:75], a[104:107], v[176:179]\n"
"v_mfma_f32_16x16x32_bf16 v[176:179], a[76:79], a[108:111], v[176:179]\n"
"s_sub_u32 s71, s64, s64\n"
"v_mfma_f32_16x16x32_bf16 v[148:151], a[68:71], a[108:111], v[148:151]\n"
"buffer_load_dwordx4 v11, s[52:55], s67 offen offset:2048 lds\n"
"v_mfma_f32_16x16x32_bf16 v[148:151], a[64:67], a[104:107], v[148:151]\n"
"s_sub_u32 s67, 1684608, s75\n"
"v_mfma_f32_16x16x32_bf16 v[152:155], a[64:67], a[112:115], v[152:155]\n"
"s_sub_u32 s66, 81920, s73\n"
"s_add_u32 s71, s71, 16384\n"
"v_mfma_f32_16x16x32_bf16 v[152:155], a[68:71], a[116:119], v[152:155]\n"
"buffer_load_dwordx4 v11, s[52:55], s67 offen offset:3072 lds\n"
"v_mfma_f32_16x16x32_bf16 v[180:183], a[76:79], a[116:119], v[180:183]\n"
"s_cmp_lt_u32 %16, 2\n"
"s_cselect_b32 s67, 16384, 0\n"
"v_add_u32 v12, s66, v7\n"
"v_mfma_f32_16x16x32_bf16 v[180:183], a[72:75], a[112:115], v[180:183]\n"
"s_add_u32 s67, s67, s72\n"
"v_add_u32 v20, s71, v9\n"
"v_mfma_f32_16x16x32_bf16 v[184:187], a[72:75], a[120:123], v[184:187]\n"
"v_add_u32 v13, s66, v8\n"
"v_mfma_f32_16x16x32_bf16 v[184:187], a[76:79], a[124:127], v[184:187]\n"
"s_add_u32 m0, s67, 139264\n"
"v_add_u32 v21, s71, v15\n"
"v_mfma_f32_16x16x32_bf16 v[156:159], a[68:71], a[124:127], v[156:159]\n"
"s_sub_u32 s67, 1792, s60\n"
"v_mfma_f32_16x16x32_bf16 v[156:159], a[64:67], a[120:123], v[156:159]\n"
"buffer_load_dwordx4 v6, s[48:51], s67 offen offset:0  lds\n"
"v_mfma_f32_16x16x32_bf16 v[160:163], a[64:67], a[128:131], v[160:163]\n"
"s_waitcnt vmcnt(17)\n"
"ds_read_b64_tr_b16 a[160:161], v20 offset:0\n"
"v_mfma_f32_16x16x32_bf16 v[160:163], a[68:71], a[132:135], v[160:163]\n"
"s_sub_u32 s67, 17152, s60\n"
"v_mfma_f32_16x16x32_bf16 v[188:191], a[76:79], a[132:135], v[188:191]\n"
"ds_read_b64_tr_b16 a[162:163], v20 offset:512\n"
"v_mfma_f32_16x16x32_bf16 v[188:191], a[72:75], a[128:131], v[188:191]\n"
"ds_read_b64_tr_b16 a[164:165], v20 offset:8\n"
"buffer_load_dwordx4 v6, s[48:51], s67 offen offset:1024  lds\n"
"v_mfma_f32_16x16x32_bf16 v[192:195], a[72:75], a[136:139], v[192:195]\n"
"ds_read_b64_tr_b16 a[166:167], v20 offset:520\n"
"v_mfma_f32_16x16x32_bf16 v[192:195], a[76:79], a[140:143], v[192:195]\n"
"s_sub_u32 s67, 32512, s60\n"
"v_mfma_f32_16x16x32_bf16 v[164:167], a[68:71], a[140:143], v[164:167]\n"
"ds_read_b64_tr_b16 a[168:169], v21 offset:0\n"
"v_mfma_f32_16x16x32_bf16 v[164:167], a[64:67], a[136:139], v[164:167]\n"
"ds_read_b64_tr_b16 a[170:171], v21 offset:512\n"
"buffer_load_dwordx4 v6, s[48:51], s67 offen offset:2048  lds\n"
"v_mfma_f32_16x16x32_bf16 v[168:171], a[64:67], a[144:147], v[168:171]\n"
"ds_read_b64_tr_b16 a[172:173], v21 offset:8\n"
"v_mfma_f32_16x16x32_bf16 v[168:171], a[68:71], a[148:151], v[168:171]\n"
"s_sub_u32 s67, 47872, s60\n"
"v_mfma_f32_16x16x32_bf16 v[196:199], a[76:79], a[148:151], v[196:199]\n"
"ds_read_b64_tr_b16 a[174:175], v21 offset:520\n"
"v_mfma_f32_16x16x32_bf16 v[196:199], a[72:75], a[144:147], v[196:199]\n"
"v_mfma_f32_16x16x32_bf16 a[20:23], a[72:75], a[152:155], a[20:23]\n"
"buffer_load_dwordx4 v6, s[48:51], s67 offen offset:3072  lds\n"
"v_mfma_f32_16x16x32_bf16 a[20:23], a[76:79], a[156:159], a[20:23]\n"
"v_mfma_f32_16x16x32_bf16 a[16:19], a[68:71], a[156:159], a[16:19]\n"
"s_add_u32 m0, s64, 20352\n"
"v_mfma_f32_16x16x32_bf16 a[16:19], a[64:67], a[152:155], a[16:19]\n"
"s_barrier\n"
"s_waitcnt vmcnt(12)\n"
"ds_read_b64_tr_b16 a[32:33], v9 offset:8192\n"
"v_mfma_f32_16x16x32_bf16 v[200:203], a[80:83], a[96:99], v[200:203]\n"
"s_waitcnt lgkmcnt(1)\n"
"ds_read_b64_tr_b16 a[34:35], v9 offset:8704\n"
"v_mfma_f32_16x16x32_bf16 v[200:203], a[84:87], a[100:103], v[200:203]\n"
"ds_read_b64_tr_b16 a[36:37], v9 offset:12288\n"
"buffer_load_dwordx4 v6, s[40:43], s88 offen offset:128  lds\n"
"v_mfma_f32_16x16x32_bf16 v[228:231], a[92:95], a[100:103], v[228:231]\n"
"ds_read_b128 a[100:103], v13 offset:32768\n"
"v_mfma_f32_16x16x32_bf16 v[228:231], a[88:91], a[96:99], v[228:231]\n"
"ds_read_b128 a[96:99], v12 offset:32768\n"
"v_mfma_f32_16x16x32_bf16 v[232:235], a[88:91], a[104:107], v[232:235]\n"
"ds_read_b64_tr_b16 a[38:39], v9 offset:12800\n"
"buffer_load_dwordx4 v6, s[40:43], s89 offen offset:1152  lds\n"
"v_mfma_f32_16x16x32_bf16 v[232:235], a[92:95], a[108:111], v[232:235]\n"
"ds_read_b64_tr_b16 a[40:41], v9 offset:8200\n"
"v_mfma_f32_16x16x32_bf16 v[204:207], a[84:87], a[108:111], v[204:207]\n"
"ds_read_b128 a[108:111], v13 offset:34816\n"
"v_mfma_f32_16x16x32_bf16 v[204:207], a[80:83], a[104:107], v[204:207]\n"
"ds_read_b128 a[104:107], v12 offset:34816\n"
"v_mfma_f32_16x16x32_bf16 v[208:211], a[80:83], a[112:115], v[208:211]\n"
"ds_read_b64_tr_b16 a[42:43], v9 offset:8712\n"
"v_mfma_f32_16x16x32_bf16 v[208:211], a[84:87], a[116:119], v[208:211]\n"
"ds_read_b64_tr_b16 a[44:45], v9 offset:12296\n"
"buffer_load_dwordx4 v6, s[40:43], s90 offen offset:2176  lds\n"
"v_mfma_f32_16x16x32_bf16 v[236:239], a[92:95], a[116:119], v[236:239]\n"
"ds_read_b128 a[116:119], v13 offset:36864\n"
"v_mfma_f32_16x16x32_bf16 v[236:239], a[88:91], a[112:115], v[236:239]\n"
"ds_read_b128 a[112:115], v12 offset:36864\n"
"v_mfma_f32_16x16x32_bf16 v[240:243], a[88:91], a[120:123], v[240:243]\n"
"ds_read_b64_tr_b16 a[46:47], v9 offset:12808\n"
"v_mfma_f32_16x16x32_bf16 v[240:243], a[92:95], a[124:127], v[240:243]\n"
"ds_read_b64_tr_b16 a[48:49], v15 offset:8192\n"
"buffer_load_dwordx4 v6, s[40:43], s91 offen offset:3200  lds\n"
"v_mfma_f32_16x16x32_bf16 v[212:215], a[84:87], a[124:127], v[212:215]\n"
"ds_read_b128 a[124:127], v13 offset:38912\n"
"v_mfma_f32_16x16x32_bf16 v[212:215], a[80:83], a[120:123], v[212:215]\n"
"ds_read_b128 a[120:123], v12 offset:38912\n"
"v_mfma_f32_16x16x32_bf16 v[216:219], a[80:83], a[128:131], v[216:219]\n"
"ds_read_b64_tr_b16 a[50:51], v15 offset:8704\n"
"v_mfma_f32_16x16x32_bf16 v[216:219], a[84:87], a[132:135], v[216:219]\n"
"ds_read_b64_tr_b16 a[52:53], v15 offset:12288\n"
"v_mfma_f32_16x16x32_bf16 v[244:247], a[92:95], a[132:135], v[244:247]\n"
"buffer_store_dwordx4 a[180:183], v0, s[36:39], %23 offen offset:2048 sc1\n"
"ds_read_b128 a[132:135], v13 offset:24576\n"
"v_mfma_f32_16x16x32_bf16 v[244:247], a[88:91], a[128:131], v[244:247]\n"
"ds_read_b128 a[128:131], v12 offset:24576\n"
"v_mfma_f32_16x16x32_bf16 v[248:251], a[88:91], a[136:139], v[248:251]\n"
"ds_read_b64_tr_b16 a[54:55], v15 offset:12800\n"
"v_mfma_f32_16x16x32_bf16 v[248:251], a[92:95], a[140:143], v[248:251]\n"
"s_add_u32 m0, s64, 8192\n"
"ds_read_b64_tr_b16 a[56:57], v15 offset:8200\n"
"v_mfma_f32_16x16x32_bf16 v[220:223], a[84:87], a[140:143], v[220:223]\n"
"ds_read_b128 a[140:143], v13 offset:26624\n"
"v_mfma_f32_16x16x32_bf16 v[220:223], a[80:83], a[136:139], v[220:223]\n"
"ds_read_b128 a[136:139], v12 offset:26624\n"
"v_mfma_f32_16x16x32_bf16 v[224:227], a[80:83], a[144:147], v[224:227]\n"
"ds_read_b64_tr_b16 a[58:59], v15 offset:8712\n"
"v_mfma_f32_16x16x32_bf16 v[224:227], a[84:87], a[148:151], v[224:227]\n"
"ds_read_b64_tr_b16 a[60:61], v15 offset:12296\n"
"v_mfma_f32_16x16x32_bf16 v[252:255], a[92:95], a[148:151], v[252:255]\n"
"ds_read_b128 a[148:151], v13 offset:28672\n"
"v_mfma_f32_16x16x32_bf16 v[252:255], a[88:91], a[144:147], v[252:255]\n"
"ds_read_b128 a[144:147], v12 offset:28672\n"
"s_sub_u32 s67, 1835008, s75\n"
"ds_read_b64_tr_b16 a[62:63], v15 offset:12808\n"
"v_mfma_f32_16x16x32_bf16 a[28:31], a[88:91], a[152:155], a[28:31]\n"
"v_mfma_f32_16x16x32_bf16 a[28:31], a[92:95], a[156:159], a[28:31]\n"
"v_mfma_f32_16x16x32_bf16 a[24:27], a[84:87], a[156:159], a[24:27]\n"
"ds_read_b128 a[156:159], v13 offset:30720\n"
"v_mfma_f32_16x16x32_bf16 a[24:27], a[80:83], a[152:155], a[24:27]\n"
"s_waitcnt lgkmcnt(15)\n"
"ds_read_b128 a[152:155], v12 offset:30720\n"
"v_mfma_f32_16x16x32_bf16 v[32:35], a[32:35], a[96:99], v[32:35]\n"
"v_mfma_f32_16x16x32_bf16 v[32:35], a[36:39], a[100:103], v[32:35]\n"
"s_waitcnt lgkmcnt(2)\n"
"v_mfma_f32_16x16x32_bf16 v[60:63], a[44:47], a[100:103], v[60:63]\n"
"buffer_load_dwordx4 v11, s[52:55], s67 offen offset:0 lds\n"
"v_mfma_f32_16x16x32_bf16 v[60:63], a[40:43], a[96:99], v[60:63]\n"
"v_mfma_f32_16x16x32_bf16 v[64:67], a[40:43], a[104:107], v[64:67]\n"
"s_sub_u32 s67, 1850368, s75\n"
"v_mfma_f32_16x16x32_bf16 v[64:67], a[44:47], a[108:111], v[64:67]\n"
"v_mfma_f32_16x16x32_bf16 v[36:39], a[36:39], a[108:111], v[36:39]\n"
"buffer_load_dwordx4 v11, s[52:55], s67 offen offset:1024 lds\n"
"v_mfma_f32_16x16x32_bf16 v[36:39], a[32:35], a[104:107], v[36:39]\n"
"v_mfma_f32_16x16x32_bf16 v[40:43], a[32:35], a[112:115], v[40:43]\n"
"s_sub_u32 s67, 1865728, s75\n"
"v_mfma_f32_16x16x32_bf16 v[40:43], a[36:39], a[116:119], v[40:43]\n"
"v_mfma_f32_16x16x32_bf16 v[68:71], a[44:47], a[116:119], v[68:71]\n"
"v_mfma_f32_16x16x32_bf16 v[68:71], a[40:43], a[112:115], v[68:71]\n"
"buffer_load_dwordx4 v11, s[52:55], s67 offen offset:2048 lds\n"
"v_mfma_f32_16x16x32_bf16 v[72:75], a[40:43], a[120:123], v[72:75]\n"
"v_mfma_f32_16x16x32_bf16 v[72:75], a[44:47], a[124:127], v[72:75]\n"
"s_sub_u32 s67, 1881088, s75\n"
"v_mfma_f32_16x16x32_bf16 v[44:47], a[36:39], a[124:127], v[44:47]\n"
"v_mfma_f32_16x16x32_bf16 v[44:47], a[32:35], a[120:123], v[44:47]\n"
"buffer_load_dwordx4 v11, s[52:55], s67 offen offset:3072 lds\n"
"v_mfma_f32_16x16x32_bf16 v[48:51], a[32:35], a[128:131], v[48:51]\n"
"v_mfma_f32_16x16x32_bf16 v[48:51], a[36:39], a[132:135], v[48:51]\n"
"s_add_u32 m0, s64, 12288\n"
"v_mfma_f32_16x16x32_bf16 v[76:79], a[44:47], a[132:135], v[76:79]\n"
"s_waitcnt vmcnt(13)\n"
"ds_read_b64_tr_b16 a[64:65], v9 offset:0\n"
"v_mfma_f32_16x16x32_bf16 v[76:79], a[40:43], a[128:131], v[76:79]\n"
"s_sub_u32 s67, 1900544, s75\n"
"ds_read_b64_tr_b16 a[66:67], v9 offset:512\n"
"v_mfma_f32_16x16x32_bf16 v[80:83], a[40:43], a[136:139], v[80:83]\n"
"ds_read_b64_tr_b16 a[68:69], v9 offset:4096\n"
"v_mfma_f32_16x16x32_bf16 v[80:83], a[44:47], a[140:143], v[80:83]\n"
"ds_read_b64_tr_b16 a[70:71], v9 offset:4608\n"
"buffer_load_dwordx4 v11, s[52:55], s67 offen offset:0 lds\n"
"v_mfma_f32_16x16x32_bf16 v[52:55], a[36:39], a[140:143], v[52:55]\n"
"s_sub_u32 s67, 1915904, s75\n"
"ds_read_b64_tr_b16 a[72:73], v9 offset:8\n"
"v_mfma_f32_16x16x32_bf16 v[52:55], a[32:35], a[136:139], v[52:55]\n"
"s_waitcnt lgkmcnt(5)\n"
"ds_read_b64_tr_b16 a[74:75], v9 offset:520\n"
"v_mfma_f32_16x16x32_bf16 v[56:59], a[32:35], a[144:147], v[56:59]\n"
"ds_read_b64_tr_b16 a[76:77], v9 offset:4104\n"
"v_mfma_f32_16x16x32_bf16 v[56:59], a[36:39], a[148:151], v[56:59]\n"
"ds_read_b64_tr_b16 a[78:79], v9 offset:4616\n"
"v_mfma_f32_16x16x32_bf16 v[84:87], a[44:47], a[148:151], v[84:87]\n"
"buffer_load_dwordx4 v11, s[52:55], s67 offen offset:1024 lds\n"
"v_mfma_f32_16x16x32_bf16 v[84:87], a[40:43], a[144:147], v[84:87]\n"
"ds_read_b64_tr_b16 a[80:81], v15 offset:0\n"
"v_mfma_f32_16x16x32_bf16 a[4:7], a[40:43], a[152:155], a[4:7]\n"
"s_sub_u32 s67, 1931264, s75\n"
"ds_read_b64_tr_b16 a[82:83], v15 offset:512\n"
"v_mfma_f32_16x16x32_bf16 a[4:7], a[44:47], a[156:159], a[4:7]\n"
"ds_read_b64_tr_b16 a[84:85], v15 offset:4096\n"
"v_mfma_f32_16x16x32_bf16 a[0:3], a[36:39], a[156:159], a[0:3]\n"
"ds_read_b64_tr_b16 a[86:87], v15 offset:4608\n"
"buffer_load_dwordx4 v11, s[52:55], s67 offen offset:2048 lds\n"
"v_mfma_f32_16x16x32_bf16 a[0:3], a[32:35], a[152:155], a[0:3]\n"
"ds_read_b64_tr_b16 a[88:89], v15 offset:8\n"
"v_mfma_f32_16x16x32_bf16 v[88:91], a[48:51], a[96:99], v[88:91]\n"
"s_sub_u32 s67, 1946624, s75\n"
"ds_read_b64_tr_b16 a[90:91], v15 offset:520\n"
"v_mfma_f32_16x16x32_bf16 v[88:91], a[52:55], a[100:103], v[88:91]\n"
"ds_read_b64_tr_b16 a[92:93], v15 offset:4104\n"
"v_mfma_f32_16x16x32_bf16 v[116:119], a[60:63], a[100:103], v[116:119]\n"
"ds_read_b64_tr_b16 a[94:95], v15 offset:4616\n"
"v_mfma_f32_16x16x32_bf16 v[116:119], a[56:59], a[96:99], v[116:119]\n"
"buffer_load_dwordx4 v11, s[52:55], s67 offen offset:3072 lds\n"
"v_mfma_f32_16x16x32_bf16 v[120:123], a[56:59], a[104:107], v[120:123]\n"
"v_mfma_f32_16x16x32_bf16 v[120:123], a[60:63], a[108:111], v[120:123]\n"
"s_add_u32 m0, s64, 16256\n"
"v_mfma_f32_16x16x32_bf16 v[92:95], a[52:55], a[108:111], v[92:95]\n"
"v_mfma_f32_16x16x32_bf16 v[92:95], a[48:51], a[104:107], v[92:95]\n"
"buffer_load_dwordx4 v6, s[40:43], s92 offen offset:128  lds\n"
"v_mfma_f32_16x16x32_bf16 v[96:99], a[48:51], a[112:115], v[96:99]\n"
"s_waitcnt lgkmcnt(0)\n"
"v_mfma_f32_16x16x32_bf16 v[96:99], a[52:55], a[116:119], v[96:99]\n"
"v_mfma_f32_16x16x32_bf16 v[124:127], a[60:63], a[116:119], v[124:127]\n"
"buffer_load_dwordx4 v6, s[40:43], s93 offen offset:1152  lds\n"
"v_mfma_f32_16x16x32_bf16 v[124:127], a[56:59], a[112:115], v[124:127]\n"
"v_mfma_f32_16x16x32_bf16 v[128:131], a[56:59], a[120:123], v[128:131]\n"
"v_mfma_f32_16x16x32_bf16 v[128:131], a[60:63], a[124:127], v[128:131]\n"
"v_mfma_f32_16x16x32_bf16 v[100:103], a[52:55], a[124:127], v[100:103]\n"
"v_mfma_f32_16x16x32_bf16 v[100:103], a[48:51], a[120:123], v[100:103]\n"
"buffer_load_dwordx4 v6, s[40:43], s94 offen offset:2176  lds\n"
"v_mfma_f32_16x16x32_bf16 v[104:107], a[48:51], a[128:131], v[104:107]\n"
"v_mfma_f32_16x16x32_bf16 v[104:107], a[52:55], a[132:135], v[104:107]\n"
"v_mfma_f32_16x16x32_bf16 v[132:135], a[60:63], a[132:135], v[132:135]\n"
"v_mfma_f32_16x16x32_bf16 v[132:135], a[56:59], a[128:131], v[132:135]\n"
"buffer_load_dwordx4 v6, s[40:43], s95 offen offset:3200  lds\n"
"v_mfma_f32_16x16x32_bf16 v[136:139], a[56:59], a[136:139], v[136:139]\n"
"v_mfma_f32_16x16x32_bf16 v[136:139], a[60:63], a[140:143], v[136:139]\n"
"s_add_u32 m0, s64, 0\n"
"v_mfma_f32_16x16x32_bf16 v[108:111], a[52:55], a[140:143], v[108:111]\n"
"v_mfma_f32_16x16x32_bf16 v[108:111], a[48:51], a[136:139], v[108:111]\n"
"s_sub_u32 s67, 1835136, s75\n"
"v_mfma_f32_16x16x32_bf16 v[112:115], a[48:51], a[144:147], v[112:115]\n"
"v_mfma_f32_16x16x32_bf16 v[112:115], a[52:55], a[148:151], v[112:115]\n"
"buffer_load_dwordx4 v11, s[52:55], s67 offen offset:0 lds\n"
"v_mfma_f32_16x16x32_bf16 v[140:143], a[60:63], a[148:151], v[140:143]\n"
"buffer_load_dwordx4 v[24:27], v0, s[56:59], %25 offen sc1\n"
"v_mfma_f32_16x16x32_bf16 v[140:143], a[56:59], a[144:147], v[140:143]\n"
"s_sub_u32 s67, 1850496, s75\n"
"v_mfma_f32_16x16x32_bf16 a[12:15], a[56:59], a[152:155], a[12:15]\n"
"v_mfma_f32_16x16x32_bf16 a[12:15], a[60:63], a[156:159], a[12:15]\n"
"buffer_load_dwordx4 v11, s[52:55], s67 offen offset:1024 lds\n"
"v_mfma_f32_16x16x32_bf16 a[8:11], a[52:55], a[156:159], a[8:11]\n"
"v_mfma_f32_16x16x32_bf16 a[8:11], a[48:51], a[152:155], a[8:11]\n"
"s_sub_u32 s67, 1865856, s75\n"
"v_mfma_f32_16x16x32_bf16 v[144:147], a[64:67], a[96:99], v[144:147]\n"
"v_mfma_f32_16x16x32_bf16 v[144:147], a[68:71], a[100:103], v[144:147]\n"
"v_mfma_f32_16x16x32_bf16 v[172:175], a[76:79], a[100:103], v[172:175]\n"
"buffer_load_dwordx4 v11, s[52:55], s67 offen offset:2048 lds\n"
"v_mfma_f32_16x16x32_bf16 v[172:175], a[72:75], a[96:99], v[172:175]\n"
"v_mfma_f32_16x16x32_bf16 v[176:179], a[72:75], a[104:107], v[176:179]\n"
"s_sub_u32 s67, 1881216, s75\n"
"v_mfma_f32_16x16x32_bf16 v[176:179], a[76:79], a[108:111], v[176:179]\n"
"s_sub_u32 s71, s64, s64\n"
"v_mfma_f32_16x16x32_bf16 v[148:151], a[68:71], a[108:111], v[148:151]\n"
"v_mfma_f32_16x16x32_bf16 v[148:151], a[64:67], a[104:107], v[148:151]\n"
"buffer_load_dwordx4 v11, s[52:55], s67 offen offset:3072 lds\n"
"v_mfma_f32_16x16x32_bf16 v[152:155], a[64:67], a[112:115], v[152:155]\n"
"s_sub_u32 s66, 122880, s73\n"
"s_add_u32 s71, s71, 20480\n"
"v_mfma_f32_16x16x32_bf16 v[152:155], a[68:71], a[116:119], v[152:155]\n"
"s_add_u32 m0, s64, 4096\n"
"v_mfma_f32_16x16x32_bf16 v[180:183], a[76:79], a[116:119], v[180:183]\n"
"s_sub_u32 s67, 1900672, s75\n"
"v_add_u32 v12, s66, v7\n"
"v_mfma_f32_16x16x32_bf16 v[180:183], a[72:75], a[112:115], v[180:183]\n"
"v_add_u32 v20, s71, v7\n"
"v_mfma_f32_16x16x32_bf16 v[184:187], a[72:75], a[120:123], v[184:187]\n"
"buffer_load_dwordx4 v11, s[52:55], s67 offen offset:0 lds\n"
"v_add_u32 v13, s66, v8\n"
"v_mfma_f32_16x16x32_bf16 v[184:187], a[76:79], a[124:127], v[184:187]\n"
"v_add_u32 v21, s71, v8\n"
"v_mfma_f32_16x16x32_bf16 v[156:159], a[68:71], a[124:127], v[156:159]\n"
"s_sub_u32 s67, 1916032, s75\n"
"v_mfma_f32_16x16x32_bf16 v[156:159], a[64:67], a[120:123], v[156:159]\n"
"v_mfma_f32_16x16x32_bf16 v[160:163], a[64:67], a[128:131], v[160:163]\n"
"s_waitcnt vmcnt(19)\n"
"ds_read_b128 a[224:227], v20 offset:0\n"
"v_mfma_f32_16x16x32_bf16 v[160:163], a[68:71], a[132:135], v[160:163]\n"
"buffer_load_dwordx4 v11, s[52:55], s67 offen offset:1024 lds\n"
"v_mfma_f32_16x16x32_bf16 v[188:191], a[76:79], a[132:135], v[188:191]\n"
"ds_read_b128 a[228:231], v21 offset:0\n"
"v_mfma_f32_16x16x32_bf16 v[188:191], a[72:75], a[128:131], v[188:191]\n"
"s_sub_u32 s67, 1931392, s75\n"
"ds_read_b128 a[232:235], v20 offset:2048\n"
"v_mfma_f32_16x16x32_bf16 v[192:195], a[72:75], a[136:139], v[192:195]\n"
"buffer_load_dwordx4 v11, s[52:55], s67 offen offset:2048 lds\n"
"v_mfma_f32_16x16x32_bf16 v[192:195], a[76:79], a[140:143], v[192:195]\n"
"ds_read_b128 a[236:239], v21 offset:2048\n"
"v_mfma_f32_16x16x32_bf16 v[164:167], a[68:71], a[140:143], v[164:167]\n"
"s_sub_u32 s67, 1946752, s75\n"
"v_mfma_f32_16x16x32_bf16 v[164:167], a[64:67], a[136:139], v[164:167]\n"
"v_mfma_f32_16x16x32_bf16 v[168:171], a[64:67], a[144:147], v[168:171]\n"
"v_mfma_f32_16x16x32_bf16 v[168:171], a[68:71], a[148:151], v[168:171]\n"
"buffer_load_dwordx4 v11, s[52:55], s67 offen offset:3072 lds\n"
"v_mfma_f32_16x16x32_bf16 v[196:199], a[76:79], a[148:151], v[196:199]\n"
"v_mfma_f32_16x16x32_bf16 v[196:199], a[72:75], a[144:147], v[196:199]\n"
"s_waitcnt lgkmcnt(0)\n"
"v_mfma_f32_16x16x32_bf16 a[20:23], a[72:75], a[152:155], a[20:23]\n"
"v_mfma_f32_16x16x32_bf16 a[20:23], a[76:79], a[156:159], a[20:23]\n"
"v_mfma_f32_16x16x32_bf16 a[16:19], a[68:71], a[156:159], a[16:19]\n"
"v_mfma_f32_16x16x32_bf16 a[16:19], a[64:67], a[152:155], a[16:19]\n"
"s_barrier\n"
"s_waitcnt vmcnt(13)\n"
"ds_read_b64_tr_b16 a[32:33], v9 offset:8192\n"
"v_mfma_f32_16x16x32_bf16 v[200:203], a[80:83], a[96:99], v[200:203]\n"
"buffer_store_dwordx4 a[184:187], v1, s[36:39], %23 offen offset:0 sc1\n"
"ds_read_b64_tr_b16 a[34:35], v9 offset:8704\n"
"v_mfma_f32_16x16x32_bf16 v[200:203], a[84:87], a[100:103], v[200:203]\n"
"ds_read_b64_tr_b16 a[36:37], v9 offset:12288\n"
"v_mfma_f32_16x16x32_bf16 v[228:231], a[92:95], a[100:103], v[228:231]\n"
"ds_read_b128 a[100:103], v13 offset:32768\n"
"v_mfma_f32_16x16x32_bf16 v[228:231], a[88:91], a[96:99], v[228:231]\n"
"ds_read_b128 a[96:99], v12 offset:32768\n"
"v_mfma_f32_16x16x32_bf16 v[232:235], a[88:91], a[104:107], v[232:235]\n"
"ds_read_b64_tr_b16 a[38:39], v9 offset:12800\n"
"v_mfma_f32_16x16x32_bf16 v[232:235], a[92:95], a[108:111], v[232:235]\n"
"ds_read_b64_tr_b16 a[40:41], v9 offset:8200\n"
"v_mfma_f32_16x16x32_bf16 v[204:207], a[84:87], a[108:111], v[204:207]\n"
"ds_read_b128 a[108:111], v13 offset:34816\n"
"v_mfma_f32_16x16x32_bf16 v[204:207], a[80:83], a[104:107], v[204:207]\n"
"ds_read_b128 a[104:107], v12 offset:34816\n"
"v_mfma_f32_16x16x32_bf16 v[208:211], a[80:83], a[112:115], v[208:211]\n"
"ds_read_b64_tr_b16 a[42:43], v9 offset:8712\n"
"v_mfma_f32_16x16x32_bf16 v[208:211], a[84:87], a[116:119], v[208:211]\n"
"ds_read_b64_tr_b16 a[44:45], v9 offset:12296\n"
"v_mfma_f32_16x16x32_bf16 v[236:239], a[92:95], a[116:119], v[236:239]\n"
"ds_read_b128 a[116:119], v13 offset:36864\n"
"v_mfma_f32_16x16x32_bf16 v[236:239], a[88:91], a[112:115], v[236:239]\n"
"buffer_store_dwordx4 a[188:191], v1, s[36:39], %23 offen offset:2048 sc1\n"
"ds_read_b128 a[112:115], v12 offset:36864\n"
"s_getpc_b64 s[70:71]\n"
"L_c11_:\n"
"s_add_u32 s70, s70, L_P6-L_c11_\n"
"s_addc_u32 s71, s71, 0\n"
"v_mfma_f32_16x16x32_bf16 v[240:243], a[88:91], a[120:123], v[240:243]\n"
"s_lshr_b32 s66, %26, 3\n"
"ds_read_b64_tr_b16 a[46:47], v9 offset:12808\n"
"v_mfma_f32_16x16x32_bf16 v[240:243], a[92:95], a[124:127], v[240:243]\n"
"s_lshl2_add_u32 s66, s66, %16\n"
"ds_read_b64_tr_b16 a[48:49], v15 offset:8192\n"
"v_mfma_f32_16x16x32_bf16 v[212:215], a[84:87], a[124:127], v[212:215]\n"
"s_mul_i32 s66, s66, 256\n"
"ds_read_b128 a[124:127], v13 offset:38912\n"
"v_mfma_f32_16x16x32_bf16 v[212:215], a[80:83], a[120:123], v[212:215]\n"
"s_add_u32 s74, %23, 524288\n"
"ds_read_b128 a[120:123], v12 offset:38912\n"
"v_mfma_f32_16x16x32_bf16 v[216:219], a[80:83], a[128:131], v[216:219]\n"
"s_min_u32 s66, s66, L_headF-L_P6-256\n"
"ds_read_b64_tr_b16 a[50:51], v15 offset:8704\n"
"v_mfma_f32_16x16x32_bf16 v[216:219], a[84:87], a[132:135], v[216:219]\n"
"ds_read_b64_tr_b16 a[52:53], v15 offset:12288\n"
"v_mfma_f32_16x16x32_bf16 v[244:247], a[92:95], a[132:135], v[244:247]\n"
"ds_read_b128 a[132:135], v13 offset:24576\n"
"v_min_u32 v22, 16, v0\n"
"v_mfma_f32_16x16x32_bf16 v[244:247], a[88:91], a[128:131], v[244:247]\n"
"ds_read_b128 a[128:131], v12 offset:24576\n"
"v_mfma_f32_16x16x32_bf16 v[248:251], a[88:91], a[136:139], v[248:251]\n"
"ds_read_b64_tr_b16 a[54:55], v15 offset:12800\n"
"v_mfma_f32_16x16x32_bf16 v[248:251], a[92:95], a[140:143], v[248:251]\n"
"ds_read_b64_tr_b16 a[56:57], v15 offset:8200\n"
"v_lshl_add_u32 v22, v22, 3, s66\n"
"v_mfma_f32_16x16x32_bf16 v[220:223], a[84:87], a[140:143], v[220:223]\n"
"ds_read_b128 a[140:143], v13 offset:26624\n"
"v_mfma_f32_16x16x32_bf16 v[220:223], a[80:83], a[136:139], v[220:223]\n"
"ds_read_b128 a[136:139], v12 offset:26624\n"
"v_mfma_f32_16x16x32_bf16 v[224:227], a[80:83], a[144:147], v[224:227]\n"
"buffer_store_dwordx4 a[160:163], v0, s[36:39], s74 offen offset:0 sc1\n"
"ds_read_b64_tr_b16 a[58:59], v15 offset:8712\n"
"global_load_dword v23, v22, s[70:71]\n"
"v_mfma_f32_16x16x32_bf16 v[224:227], a[84:87], a[148:151], v[224:227]\n"
"ds_read_b64_tr_b16 a[60:61], v15 offset:12296\n"
"v_mfma_f32_16x16x32_bf16 v[252:255], a[92:95], a[148:151], v[252:255]\n"
"ds_read_b128 a[148:151], v13 offset:28672\n"
"v_mfma_f32_16x16x32_bf16 v[252:255], a[88:91], a[144:147], v[252:255]\n"
"ds_read_b128 a[144:147], v12 offset:28672\n"
"s_sub_u32 s71, s64, s64\n"
"ds_read_b64_tr_b16 a[62:63], v15 offset:12808\n"
"v_mfma_f32_16x16x32_bf16 a[28:31], a[88:91], a[152:155], a[28:31]\n"
"v_mfma_f32_16x16x32_bf16 a[28:31], a[92:95], a[156:159], a[28:31]\n"
"v_mfma_f32_16x16x32_bf16 a[24:27], a[84:87], a[156:159], a[24:27]\n"
"s_add_u32 s71, s71, 16384\n"
"ds_read_b128 a[156:159], v13 offset:30720\n"
"v_mfma_f32_16x16x32_bf16 a[24:27], a[80:83], a[152:155], a[24:27]\n"
"s_waitcnt lgkmcnt(15)\n"
"ds_read_b128 a[152:155], v12 offset:30720\n"
"v_mfma_f32_16x16x32_bf16 v[32:35], a[32:35], a[96:99], v[32:35]\n"
"v_add_u32 v20, s71, v7\n"
"v_mfma_f32_16x16x32_bf16 v[32:35], a[36:39], a[100:103], v[32:35]\n"
"s_waitcnt lgkmcnt(2)\n"
"v_mfma_f32_16x16x32_bf16 v[60:63], a[44:47], a[100:103], v[60:63]\n"
"v_mfma_f32_16x16x32_bf16 v[60:63], a[40:43], a[96:99], v[60:63]\n"
"v_add_u32 v21, s71, v8\n"
"v_mfma_f32_16x16x32_bf16 v[64:67], a[40:43], a[104:107], v[64:67]\n"
"buffer_store_dwordx4 a[164:167], v0, s[36:39], s74 offen offset:2048 sc1\n"
"v_mfma_f32_16x16x32_bf16 v[64:67], a[44:47], a[108:111], v[64:67]\n"
"v_mfma_f32_16x16x32_bf16 v[36:39], a[36:39], a[108:111], v[36:39]\n"
"s_waitcnt vmcnt(14)\n"
"ds_read_b128 a[240:243], v20 offset:0\n"
"v_mfma_f32_16x16x32_bf16 v[36:39], a[32:35], a[104:107], v[36:39]\n"
"v_mfma_f32_16x16x32_bf16 v[40:43], a[32:35], a[112:115], v[40:43]\n"
"ds_read_b128 a[244:247], v21 offset:0\n"
"v_mfma_f32_16x16x32_bf16 v[40:43], a[36:39], a[116:119], v[40:43]\n"
"ds_read_b128 a[248:251], v20 offset:2048\n"
"v_mfma_f32_16x16x32_bf16 v[68:71], a[44:47], a[116:119], v[68:71]\n"
"s_add_u32 s70, s78, 1\n"
"v_mfma_f32_16x16x32_bf16 v[68:71], a[40:43], a[112:115], v[68:71]\n"
"ds_read_b128 a[252:255], v21 offset:2048\n"
"v_mfma_f32_16x16x32_bf16 v[72:75], a[40:43], a[120:123], v[72:75]\n"
"v_mfma_f32_16x16x32_bf16 v[72:75], a[44:47], a[124:127], v[72:75]\n"
"v_mfma_f32_16x16x32_bf16 v[44:47], a[36:39], a[124:127], v[44:47]\n"
"v_mfma_f32_16x16x32_bf16 v[44:47], a[32:35], a[120:123], v[44:47]\n"
"v_mfma_f32_16x16x32_bf16 v[48:51], a[32:35], a[128:131], v[48:51]\n"
"v_mfma_f32_16x16x32_bf16 v[48:51], a[36:39], a[132:135], v[48:51]\n"
"s_waitcnt lgkmcnt(0)\n"
"buffer_store_dwordx4 a[168:171], v1, s[36:39], s74 offen offset:0 sc1\n"
"v_mfma_f32_16x16x32_bf16 v[76:79], a[44:47], a[132:135], v[76:79]\n"
"s_waitcnt vmcnt(6)\n"
"ds_read_b64_tr_b16 a[64:65], v9 offset:0\n"
"v_mfma_f32_16x16x32_bf16 v[76:79], a[40:43], a[128:131], v[76:79]\n"
"ds_read_b64_tr_b16 a[66:67], v9 offset:512\n"
"v_mfma_f32_16x16x32_bf16 v[80:83], a[40:43], a[136:139], v[80:83]\n"
"ds_read_b64_tr_b16 a[68:69], v9 offset:4096\n"
"v_mfma_f32_16x16x32_bf16 v[80:83], a[44:47], a[140:143], v[80:83]\n"
"ds_read_b64_tr_b16 a[70:71], v9 offset:4608\n"
"v_mfma_f32_16x16x32_bf16 v[52:55], a[36:39], a[140:143], v[52:55]\n"
"ds_read_b64_tr_b16 a[72:73], v9 offset:8\n"
"v_subrev_u32 v24, s70, v24\n"
"v_mfma_f32_16x16x32_bf16 v[52:55], a[32:35], a[136:139], v[52:55]\n"
"ds_read_b64_tr_b16 a[74:75], v9 offset:520\n"
"v_xor_b32 v25, s79, v25\n"
"v_mfma_f32_16x16x32_bf16 v[56:59], a[32:35], a[144:147], v[56:59]\n"
"ds_read_b64_tr_b16 a[76:77], v9 offset:4104\n"
"v_subrev_u32 v26, s70, v26\n"
"v_mfma_f32_16x16x32_bf16 v[56:59], a[36:39], a[148:151], v[56:59]\n"
"ds_read_b64_tr_b16 a[78:79], v9 offset:4616\n"
"v_xor_b32 v27, s79, v27\n"
"v_mfma_f32_16x16x32_bf16 v[84:87], a[44:47], a[148:151], v[84:87]\n"
"ds_read_b64_tr_b16 a[80:81], v15 offset:0\n"
"v_or_b32 v24, v24, v26\n"
"v_mfma_f32_16x16x32_bf16 v[84:87], a[40:43], a[144:147], v[84:87]\n"
"ds_read_b64_tr_b16 a[82:83], v15 offset:512\n"
"v_or_b32 v25, v25, v27\n"
"v_mfma_f32_16x16x32_bf16 a[4:7], a[40:43], a[152:155], a[4:7]\n"
"ds_read_b64_tr_b16 a[84:85], v15 offset:4096\n"
"v_or_b32 v24, v24, v25\n"
"v_mfma_f32_16x16x32_bf16 a[4:7], a[44:47], a[156:159], a[4:7]\n"
"ds_read_b64_tr_b16 a[86:87], v15 offset:4608\n"
"v_cmp_lt_u32 vcc, 15, v24\n"
"v_mfma_f32_16x16x32_bf16 a[0:3], a[36:39], a[156:159], a[0:3]\n"
"buffer_store_dwordx4 a[172:175], v1, s[36:39], s74 offen offset:2048 sc1\n"
"ds_read_b64_tr_b16 a[88:89], v15 offset:8\n"
"s_cbranch_vccnz L_slow3\n"
"L_back3:\n"
"v_mfma_f32_16x16x32_bf16 a[0:3], a[32:35], a[152:155], a[0:3]\n"
"ds_read_b64_tr_b16 a[90:91], v15 offset:520\n"
"v_mfma_f32_16x16x32_bf16 v[88:91], a[48:51], a[96:99], v[88:91]\n"
"ds_read_b64_tr_b16 a[92:93], v15 offset:4104\n"
"v_mfma_f32_16x16x32_bf16 v[88:91], a[52:55], a[100:103], v[88:91]\n"
"global_load_dwordx4 a[32:35], v0, s[34:35] offset:-4096 sc1\n"
"s_barrier\n"
"v_mfma_f32_16x16x32_bf16 v[116:119], a[60:63], a[100:103], v[116:119]\n"
"ds_read_b64_tr_b16 a[94:95], v15 offset:4616\n"
"v_mfma_f32_16x16x32_bf16 v[116:119], a[56:59], a[96:99], v[116:119]\n"
"ds_write_b64 v2, a[192:193] offset:16384\n"
"v_mfma_f32_16x16x32_bf16 v[120:123], a[56:59], a[104:107], v[120:123]\n"
"ds_write_b64 v2, a[194:195] offset:16392\n"
"v_mfma_f32_16x16x32_bf16 v[120:123], a[60:63], a[108:111], v[120:123]\n"
"ds_write_b64 v2, a[196:197] offset:17408\n"
"v_mfma_f32_16x16x32_bf16 v[92:95], a[52:55], a[108:111], v[92:95]\n"
"ds_read_b128 a[160:163], v7 offset:24576\n"
"v_mfma_f32_16x16x32_bf16 v[92:95], a[48:51], a[104:107], v[92:95]\n"
"ds_write_b64 v2, a[198:199] offset:17416\n"
"v_mfma_f32_16x16x32_bf16 v[96:99], a[48:51], a[112:115], v[96:99]\n"
"s_waitcnt lgkmcnt(5)\n"
"ds_read_b128 a[164:167], v8 offset:24576\n"
"v_mfma_f32_16x16x32_bf16 v[96:99], a[52:55], a[116:119], v[96:99]\n"
"global_load_dwordx4 a[36:39], v0, s[34:35] offset:-3072 sc1\n"
"v_mfma_f32_16x16x32_bf16 v[124:127], a[60:63], a[116:119], v[124:127]\n"
"ds_read_b128 a[168:171], v7 offset:26624\n"
"v_mfma_f32_16x16x32_bf16 v[124:127], a[56:59], a[112:115], v[124:127]\n"
"ds_read_b128 a[172:175], v8 offset:26624\n"
"v_mfma_f32_16x16x32_bf16 v[128:131], a[56:59], a[120:123], v[128:131]\n"
"ds_write_b64 v2, a[200:201] offset:18432\n"
"v_mfma_f32_16x16x32_bf16 v[128:131], a[60:63], a[124:127], v[128:131]\n"
"ds_read_b128 a[176:179], v7 offset:28672\n"
"v_mfma_f32_16x16x32_bf16 v[100:103], a[52:55], a[124:127], v[100:103]\n"
"ds_read_b128 a[180:183], v8 offset:28672\n"
"v_mfma_f32_16x16x32_bf16 v[100:103], a[48:51], a[120:123], v[100:103]\n"
"ds_write_b64 v2, a[202:203] offset:18440\n"
"v_mfma_f32_16x16x32_bf16 v[104:107], a[48:51], a[128:131], v[104:107]\n"
"ds_read_b128 a[184:187], v7 offset:30720\n"
"v_mfma_f32_16x16x32_bf16 v[104:107], a[52:55], a[132:135], v[104:107]\n"
"global_load_dwordx4 a[40:43], v0, s[34:35] offset:-2048 sc1\n"
"v_mfma_f32_16x16x32_bf16 v[132:135], a[60:63], a[132:135], v[132:135]\n"
"ds_read_b128 a[188:191], v8 offset:30720\n"
"v_mfma_f32_16x16x32_bf16 v[132:135], a[56:59], a[128:131], v[132:135]\n"
"ds_write_b64 v2, a[204:205] offset:19456\n"
"v_mfma_f32_16x16x32_bf16 v[136:139], a[56:59], a[136:139], v[136:139]\n"
"ds_write_b64 v2, a[206:207] offset:19464\n"
"v_mfma_f32_16x16x32_bf16 v[136:139], a[60:63], a[140:143], v[136:139]\n"
"ds_write_b64 v2, a[208:209] offset:20480\n"
"v_mfma_f32_16x16x32_bf16 v[108:111], a[52:55], a[140:143], v[108:111]\n"
"ds_write_b64 v2, a[210:211] offset:20488\n"
"v_mfma_f32_16x16x32_bf16 v[108:111], a[48:51], a[136:139], v[108:111]\n"
"ds_write_b64 v2, a[212:213] offset:21504\n"
"v_mfma_f32_16x16x32_bf16 v[112:115], a[48:51], a[144:147], v[112:115]\n"
"ds_write_b64 v2, a[214:215] offset:21512\n"
"v_mfma_f32_16x16x32_bf16 v[112:115], a[52:55], a[148:151], v[112:115]\n"
"global_load_dwordx4 a[44:47], v0, s[34:35] offset:-1024 sc1\n"
"v_mfma_f32_16x16x32_bf16 v[140:143], a[60:63], a[148:151], v[140:143]\n"
"ds_write_b64 v2, a[216:217] offset:22528\n"
"v_mfma_f32_16x16x32_bf16 v[140:143], a[56:59], a[144:147], v[140:143]\n"
"v_mfma_f32_16x16x32_bf16 a[12:15], a[56:59], a[152:155], a[12:15]\n"
"ds_write_b64 v2, a[218:219] offset:22536\n"
"v_mfma_f32_16x16x32_bf16 a[12:15], a[60:63], a[156:159], a[12:15]\n"
"v_mfma_f32_16x16x32_bf16 a[8:11], a[52:55], a[156:159], a[8:11]\n"
"ds_write_b64 v2, a[220:221] offset:23552\n"
"v_mfma_f32_16x16x32_bf16 a[8:11], a[48:51], a[152:155], a[8:11]\n"
"v_mfma_f32_16x16x32_bf16 v[144:147], a[64:67], a[96:99], v[144:147]\n"
"ds_write_b64 v2, a[222:223] offset:23560\n"
"v_mfma_f32_16x16x32_bf16 v[144:147], a[68:71], a[100:103], v[144:147]\n"
"global_load_dwordx4 a[48:51], v0, s[34:35] offset:0 sc1\n"
"v_mfma_f32_16x16x32_bf16 v[172:175], a[76:79], a[100:103], v[172:175]\n"
"ds_write_b64 v2, a[224:225] offset:24576\n"
"v_mfma_f32_16x16x32_bf16 v[172:175], a[72:75], a[96:99], v[172:175]\n"
"ds_write_b64 v2, a[226:227] offset:24584\n"
"v_mfma_f32_16x16x32_bf16 v[176:179], a[72:75], a[104:107], v[176:179]\n"
"v_mfma_f32_16x16x32_bf16 v[176:179], a[76:79], a[108:111], v[176:179]\n"
"ds_write_b64 v2, a[228:229] offset:25600\n"
"v_mfma_f32_16x16x32_bf16 v[148:151], a[68:71], a[108:111], v[148:151]\n"
"v_mfma_f32_16x16x32_bf16 v[148:151], a[64:67], a[104:107], v[148:151]\n"
"ds_write_b64 v2, a[230:231] offset:25608\n"
"v_mfma_f32_16x16x32_bf16 v[152:155], a[64:67], a[112:115], v[152:155]\n"
"ds_write_b64 v2, a[232:233] offset:26624\n"
"v_mfma_f32_16x16x32_bf16 v[152:155], a[68:71], a[116:119], v[152:155]\n"
"global_load_dwordx4 a[52:55], v0, s[34:35] offset:1024 sc1\n"
"v_mfma_f32_16x16x32_bf16 v[180:183], a[76:79], a[116:119], v[180:183]\n"
"v_mfma_f32_16x16x32_bf16 v[180:183], a[72:75], a[112:115], v[180:183]\n"
"ds_write_b64 v2, a[234:235] offset:26632\n"
"v_mfma_f32_16x16x32_bf16 v[184:187], a[72:75], a[120:123], v[184:187]\n"
"v_mfma_f32_16x16x32_bf16 v[184:187], a[76:79], a[124:127], v[184:187]\n"
"ds_write_b64 v2, a[236:237] offset:27648\n"
"v_mfma_f32_16x16x32_bf16 v[156:159], a[68:71], a[124:127], v[156:159]\n"
"v_mfma_f32_16x16x32_bf16 v[156:159], a[64:67], a[120:123], v[156:159]\n"
"ds_write_b64 v2, a[238:239] offset:27656\n"
"v_mfma_f32_16x16x32_bf16 v[160:163], a[64:67], a[128:131], v[160:163]\n"
"ds_write_b64 v2, a[240:241] offset:28672\n"
"v_mfma_f32_16x16x32_bf16 v[160:163], a[68:71], a[132:135], v[160:163]\n"
"global_load_dwordx4 a[56:59], v0, s[34:35] offset:2048 sc1\n"
"v_mfma_f32_16x16x32_bf16 v[188:191], a[76:79], a[132:135], v[188:191]\n"
"ds_write_b64 v2, a[242:243] offset:28680\n"
"v_mfma_f32_16x16x32_bf16 v[188:191], a[72:75], a[128:131], v[188:191]\n"
"v_mfma_f32_16x16x32_bf16 v[192:195], a[72:75], a[136:139], v[192:195]\n"
"ds_write_b64 v2, a[244:245] offset:29696\n"
"v_mfma_f32_16x16x32_bf16 v[192:195], a[76:79], a[140:143], v[192:195]\n"
"v_mfma_f32_16x16x32_bf16 v[164:167], a[68:71], a[140:143], v[164:167]\n"
"ds_write_b64 v2, a[246:247] offset:29704\n"
"v_mfma_f32_16x16x32_bf16 v[164:167], a[64:67], a[136:139], v[164:167]\n"
"s_add_u32 s74, s78, 2\n"
"v_mfma_f32_16x16x32_bf16 v[168:171], a[64:67], a[144:147], v[168:171]\n"
"ds_write_b64 v2, a[248:249] offset:30720\n"
"v_mfma_f32_16x16x32_bf16 v[168:171], a[68:71], a[148:151], v[168:171]\n"
"global_load_dwordx4 a[60:63], v0, s[34:35] offset:3072 sc1\n"
"v_mov_b32 v28, s74\n"
"v_mfma_f32_16x16x32_bf16 v[196:199], a[76:79], a[148:151], v[196:199]\n"
"v_mov_b32 v29, s79\n"
"v_mfma_f32_16x16x32_bf16 v[196:199], a[72:75], a[144:147], v[196:199]\n"
"ds_write_b64 v2, a[250:251] offset:30728\n"
"v_mfma_f32_16x16x32_bf16 a[20:23], a[72:75], a[152:155], a[20:23]\n"
"s_waitcnt vmcnt(8)\n"
"buffer_store_dwordx2 v[28:29], off, s[56:59], %24 sc1\n"
"s_add_u32 s34, s34, 8192\n"
"v_mfma_f32_16x16x32_bf16 a[20:23], a[76:79], a[156:159], a[20:23]\n"
"ds_write_b64 v2, a[252:253] offset:31744\n"
"v_mfma_f32_16x16x32_bf16 a[16:19], a[68:71], a[156:159], a[16:19]\n"
"ds_write_b64 v2, a[254:255] offset:31752\n"
"v_mfma_f32_16x16x32_bf16 a[16:19], a[64:67], a[152:155], a[16:19]\n"
"v_mfma_f32_16x16x32_bf16 v[200:203], a[80:83], a[96:99], v[200:203]\n"
"v_lshrrev_b32 v16, 4, v0\n"
"v_mfma_f32_16x16x32_bf16 v[200:203], a[84:87], a[100:103], v[200:203]\n"
"global_load_dwordx4 a[64:67], v0, s[34:35] offset:-4096 sc1\n"
"v_mfma_f32_16x16x32_bf16 v[228:231], a[92:95], a[100:103], v[228:231]\n"
"ds_read_b128 a[100:103], v2 offset:17408\n"
"v_mfma_f32_16x16x32_bf16 v[228:231], a[88:91], a[96:99], v[228:231]\n"
"ds_read_b128 a[96:99], v2 offset:16384\n"
"v_mfma_f32_16x16x32_bf16 v[232:235], a[88:91], a[104:107], v[232:235]\n"
"v_and_b32 v16, 0x30, v16\n"
"v_mfma_f32_16x16x32_bf16 v[232:235], a[92:95], a[108:111], v[232:235]\n"
"buffer_load_dwordx2 v[18:19], v16, s[44:47], s61 offen offset:128\n"
"v_mfma_f32_16x16x32_bf16 v[204:207], a[84:87], a[108:111], v[204:207]\n"
"ds_read_b128 a[108:111], v2 offset:19456\n"
"v_mfma_f32_16x16x32_bf16 v[204:207], a[80:83], a[104:107], v[204:207]\n"
"ds_read_b128 a[104:107], v2 offset:18432\n"
"v_mfma_f32_16x16x32_bf16 v[208:211], a[80:83], a[112:115], v[208:211]\n"
"v_lshrrev_b32 v20, 4, v0\n"
"v_mfma_f32_16x16x32_bf16 v[208:211], a[84:87], a[116:119], v[208:211]\n"
"global_load_dwordx4 a[68:71], v0, s[34:35] offset:-3072 sc1\n"
"v_mfma_f32_16x16x32_bf16 v[236:239], a[92:95], a[116:119], v[236:239]\n"
"ds_read_b128 a[116:119], v2 offset:21504\n"
"v_mfma_f32_16x16x32_bf16 v[236:239], a[88:91], a[112:115], v[236:239]\n"
"ds_read_b128 a[112:115], v2 offset:20480\n"
"v_mfma_f32_16x16x32_bf16 v[240:243], a[88:91], a[120:123], v[240:243]\n"
"v_and_b32 v20, 0x30, v20\n"
"v_mfma_f32_16x16x32_bf16 v[240:243], a[92:95], a[124:127], v[240:243]\n"
"buffer_load_dwordx2 v[22:23], v20, s[44:47], s61 offen offset:136\n"
"v_mfma_f32_16x16x32_bf16 v[212:215], a[84:87], a[124:127], v[212:215]\n"
"ds_read_b128 a[124:127], v2 offset:23552\n"
"v_mfma_f32_16x16x32_bf16 v[212:215], a[80:83], a[120:123], v[212:215]\n"
"ds_read_b128 a[120:123], v2 offset:22528\n"
"v_mfma_f32_16x16x32_bf16 v[216:219], a[80:83], a[128:131], v[216:219]\n"
"v_lshrrev_b32 v24, 4, v0\n"
"v_mfma_f32_16x16x32_bf16 v[216:219], a[84:87], a[132:135], v[216:219]\n"
"global_load_dwordx4 a[72:75], v0, s[34:35] offset:-2048 sc1\n"
"v_mfma_f32_16x16x32_bf16 v[244:247], a[92:95], a[132:135], v[244:247]\n"
"ds_read_b128 a[132:135], v2 offset:25600\n"
"v_and_b32 v24, 0x30, v24\n"
"v_mfma_f32_16x16x32_bf16 v[244:247], a[88:91], a[128:131], v[244:247]\n"
"ds_read_b128 a[128:131], v2 offset:24576\n"
"v_mfma_f32_16x16x32_bf16 v[248:251], a[88:91], a[136:139], v[248:251]\n"
"buffer_load_dwordx2 v[26:27], v24, s[44:47], s61 offen offset:192\n"
"v_mfma_f32_16x16x32_bf16 v[248:251], a[92:95], a[140:143], v[248:251]\n"
"v_lshrrev_b32 v28, 4, v0\n"
"v_mfma_f32_16x16x32_bf16 v[220:223], a[84:87], a[140:143], v[220:223]\n"
"ds_read_b128 a[140:143], v2 offset:27648\n"
"v_mfma_f32_16x16x32_bf16 v[220:223], a[80:83], a[136:139], v[220:223]\n"
"ds_read_b128 a[136:139], v2 offset:26624\n"
"v_mfma_f32_16x16x32_bf16 v[224:227], a[80:83], a[144:147], v[224:227]\n"
"v_and_b32 v28, 0x30, v28\n"
"v_mfma_f32_16x16x32_bf16 v[224:227], a[84:87], a[148:151], v[224:227]\n"
"global_load_dwordx4 a[76:79], v0, s[34:35] offset:-1024 sc1\n"
"v_mfma_f32_16x16x32_bf16 v[252:255], a[92:95], a[148:151], v[252:255]\n"
"ds_read_b128 a[148:151], v2 offset:29696\n"
"v_mfma_f32_16x16x32_bf16 v[252:255], a[88:91], a[144:147], v[252:255]\n"
"ds_read_b128 a[144:147], v2 offset:28672\n"
"v_mfma_f32_16x16x32_bf16 a[28:31], a[88:91], a[152:155], a[28:31]\n"
"buffer_load_dwordx2 v[30:31], v28, s[44:47], s61 offen offset:200\n"
"v_mfma_f32_16x16x32_bf16 a[28:31], a[92:95], a[156:159], a[28:31]\n"
"v_mfma_f32_16x16x32_bf16 a[24:27], a[84:87], a[156:159], a[24:27]\n"
"ds_read_b128 a[156:159], v2 offset:31744\n"
"v_mfma_f32_16x16x32_bf16 a[24:27], a[80:83], a[152:155], a[24:27]\n"
"s_waitcnt vmcnt(13) lgkmcnt(11)\n"
"ds_read_b128 a[152:155], v2 offset:30720\n"
"v_mfma_f32_16x16x32_bf16 v[32:35], a[32:35], a[96:99], v[32:35]\n"
"v_mfma_f32_16x16x32_bf16 v[32:35], a[36:39], a[100:103], v[32:35]\n"
"global_load_dwordx4 a[80:83], v0, s[34:35] offset:0 sc1\n"
"v_mfma_f32_16x16x32_bf16 v[60:63], a[44:47], a[100:103], v[60:63]\n"
"s_barrier\n"
"v_mfma_f32_16x16x32_bf16 v[60:63], a[40:43], a[96:99], v[60:63]\n"
"v_mfma_f32_16x16x32_bf16 v[64:67], a[40:43], a[104:107], v[64:67]\n"
"v_mfma_f32_16x16x32_bf16 v[64:67], a[44:47], a[108:111], v[64:67]\n"
"v_mfma_f32_16x16x32_bf16 v[36:39], a[36:39], a[108:111], v[36:39]\n"
"v_mfma_f32_16x16x32_bf16 v[36:39], a[32:35], a[104:107], v[36:39]\n"
"s_waitcnt lgkmcnt(8)\n"
"v_mfma_f32_16x16x32_bf16 v[40:43], a[32:35], a[112:115], v[40:43]\n"
"v_mfma_f32_16x16x32_bf16 v[40:43], a[36:39], a[116:119], v[40:43]\n"
"global_load_dwordx4 a[84:87], v0, s[34:35] offset:1024 sc1\n"
"v_mfma_f32_16x16x32_bf16 v[68:71], a[44:47], a[116:119], v[68:71]\n"
"s_mov_b32 s69, 0\n"
"v_mfma_f32_16x16x32_bf16 v[68:71], a[40:43], a[112:115], v[68:71]\n"
"v_mfma_f32_16x16x32_bf16 v[72:75], a[40:43], a[120:123], v[72:75]\n"
"v_mfma_f32_16x16x32_bf16 v[72:75], a[44:47], a[124:127], v[72:75]\n"
"s_add_u32 s61, s61, 2048\n"
"v_mfma_f32_16x16x32_bf16 v[44:47], a[36:39], a[124:127], v[44:47]\n"
"v_mfma_f32_16x16x32_bf16 v[44:47], a[32:35], a[120:123], v[44:47]\n"
"s_waitcnt lgkmcnt(4)\n"
"v_mfma_f32_16x16x32_bf16 v[48:51], a[32:35], a[128:131], v[48:51]\n"
"s_waitcnt vmcnt(8)\n"
"v_lshlrev_b32 v16, 16, v18\n"
"v_and_b32 v17, s63, v18\n"
"v_mfma_f32_16x16x32_bf16 v[48:51], a[36:39], a[132:135], v[48:51]\n"
"global_load_dwordx4 a[88:91], v0, s[34:35] offset:2048 sc1\n"
"v_lshlrev_b32 v18, 16, v19\n"
"v_mfma_f32_16x16x32_bf16 v[76:79], a[44:47], a[132:135], v[76:79]\n"
"v_and_b32 v19, s63, v19\n"
"v_add_f32 v144, v144, v16\n"
"v_mfma_f32_16x16x32_bf16 v[76:79], a[40:43], a[128:131], v[76:79]\n"
"v_add_f32 v145, v145, v17\n"
"v_add_f32 v146, v146, v18\n"
"v_mfma_f32_16x16x32_bf16 v[80:83], a[40:43], a[136:139], v[80:83]\n"
"v_add_f32 v147, v147, v19\n"
"v_add_f32 v148, v148, v16\n"
"v_mfma_f32_16x16x32_bf16 v[80:83], a[44:47], a[140:143], v[80:83]\n"
"v_add_f32 v149, v149, v17\n"
"v_add_f32 v150, v150, v18\n"
"v_mfma_f32_16x16x32_bf16 v[52:55], a[36:39], a[140:143], v[52:55]\n"
"v_add_f32 v151, v151, v19\n"
"v_add_f32 v152, v152, v16\n"
"v_mfma_f32_16x16x32_bf16 v[52:55], a[32:35], a[136:139], v[52:55]\n"
"s_waitcnt lgkmcnt(0)\n"
"v_add_f32 v153, v153, v17\n"
"v_add_f32 v154, v154, v18\n"
"v_mfma_f32_16x16x32_bf16 v[56:59], a[32:35], a[144:147], v[56:59]\n"
"s_mov_b32 s98, 0\n"
"v_add_f32 v155, v155, v19\n"
"v_add_f32 v156, v156, v16\n"
"v_mfma_f32_16x16x32_bf16 v[56:59], a[36:39], a[148:151], v[56:59]\n"
"global_load_dwordx4 a[92:95], v0, s[34:35] offset:3072 sc1\n"
"s_load_dword s98, s[96:97], 0 glc\n"
"v_mfma_f32_16x16x32_bf16 v[84:87], a[44:47], a[148:151], v[84:87]\n"
"v_add_f32 v157, v157, v17\n"
"v_add_f32 v158, v158, v18\n"
"v_mfma_f32_16x16x32_bf16 v[84:87], a[40:43], a[144:147], v[84:87]\n"
"v_add_f32 v159, v159, v19\n"
"v_add_f32 v160, v160, v16\n"
"v_mfma_f32_16x16x32_bf16 a[4:7], a[40:43], a[152:155], a[4:7]\n"
"s_add_u32 s34, s34, 8192\n"
"v_add_f32 v161, v161, v17\n"
"v_add_f32 v162, v162, v18\n"
"v_mfma_f32_16x16x32_bf16 a[4:7], a[44:47], a[156:159], a[4:7]\n"
"v_add_f32 v163, v163, v19\n"
"v_add_f32 v164, v164, v16\n"
"v_mfma_f32_16x16x32_bf16 a[0:3], a[36:39], a[156:159], a[0:3]\n"
"v_add_f32 v165, v165, v17\n"
"v_add_f32 v166, v166, v18\n"
"v_mfma_f32_16x16x32_bf16 a[0:3], a[32:35], a[152:155], a[0:3]\n"
"v_add_f32 v167, v167, v19\n"
"v_add_f32 v168, v168, v16\n"
"v_mfma_f32_16x16x32_bf16 v[88:91], a[48:51], a[96:99], v[88:91]\n"
"v_add_f32 v169, v169, v17\n"
"v_add_f32 v170, v170, v18\n"
"v_mfma_f32_16x16x32_bf16 v[88:91], a[52:55], a[100:103], v[88:91]\n"
"global_load_dwordx4 a[32:35], v0, s[34:35] offset:-4096 sc1\n"
"v_add_f32 v171, v171, v19\n"
"v_mfma_f32_16x16x32_bf16 v[116:119], a[60:63], a[100:103], v[116:119]\n"
"ds_read_b128 a[100:103], v3 offset:17408\n"
"v_accvgpr_read_b32 v12, a16\n"
"v_mfma_f32_16x16x32_bf16 v[116:119], a[56:59], a[96:99], v[116:119]\n"
"ds_read_b128 a[96:99], v3 offset:16384\n"
"v_accvgpr_read_b32 v13, a17\n"
"v_mfma_f32_16x16x32_bf16 v[120:123], a[56:59], a[104:107], v[120:123]\n"
"s_add_u32 s69, s69, s98\n"
"v_accvgpr_read_b32 v14, a18\n"
"v_accvgpr_read_b32 v15, a19\n"
"v_mfma_f32_16x16x32_bf16 v[120:123], a[60:63], a[108:111], v[120:123]\n"
"s_add_u32 s69, s69, s98\n"
"v_add_f32 v12, v12, v16\n"
"v_add_f32 v13, v13, v17\n"
"v_mfma_f32_16x16x32_bf16 v[92:95], a[52:55], a[108:111], v[92:95]\n"
"s_add_u32 s69, s69, s98\n"
"ds_read_b128 a[108:111], v3 offset:19456\n"
"v_add_f32 v14, v14, v18\n"
"v_mfma_f32_16x16x32_bf16 v[92:95], a[48:51], a[104:107], v[92:95]\n"
"s_add_u32 s69, s69, s98\n"
"ds_read_b128 a[104:107], v3 offset:18432\n"
"v_add_f32 v15, v15, v19\n"
"v_mfma_f32_16x16x32_bf16 v[96:99], a[48:51], a[112:115], v[96:99]\n"
"s_add_u32 s69, s69, s98\n"
"v_accvgpr_write_b32 a16, v12\n"
"v_accvgpr_write_b32 a17, v13\n"
"v_accvgpr_write_b32 a18, v14\n"
"v_mfma_f32_16x16x32_bf16 v[96:99], a[52:55], a[116:119], v[96:99]\n"
"s_add_u32 s69, s69, s98\n"
"s_waitcnt vmcnt(9)\n"
"global_load_dwordx4 a[36:39], v0, s[34:35] offset:-3072 sc1\n"
"v_accvgpr_write_b32 a19, v15\n"
"v_mfma_f32_16x16x32_bf16 v[124:127], a[60:63], a[116:119], v[124:127]\n"
"s_add_u32 s69, s69, s98\n"
"ds_read_b128 a[116:119], v3 offset:21504\n"
"v_lshlrev_b32 v20, 16, v22\n"
"v_mfma_f32_16x16x32_bf16 v[124:127], a[56:59], a[112:115], v[124:127]\n"
"s_add_u32 s69, s69, s98\n"
"ds_read_b128 a[112:115], v3 offset:20480\n"
"v_and_b32 v21, s63, v22\n"
"v_mfma_f32_16x16x32_bf16 v[128:131], a[56:59], a[120:123], v[128:131]\n"
"s_add_u32 s69, s69, s98\n"
"v_lshlrev_b32 v22, 16, v23\n"
"v_and_b32 v23, s63, v23\n"
"v_mfma_f32_16x16x32_bf16 v[128:131], a[60:63], a[124:127], v[128:131]\n"
"s_add_u32 s69, s69, s98\n"
"v_add_f32 v172, v172, v20\n"
"v_add_f32 v173, v173, v21\n"
"v_mfma_f32_16x16x32_bf16 v[100:103], a[52:55], a[124:127], v[100:103]\n"
"s_add_u32 s69, s69, s98\n"
"ds_read_b128 a[124:127], v3 offset:23552\n"
"v_add_f32 v174, v174, v22\n"
"v_mfma_f32_16x16x32_bf16 v[100:103], a[48:51], a[120:123], v[100:103]\n"
"s_add_u32 s69, s69, s98\n"
"ds_read_b128 a[120:123], v3 offset:22528\n"
"v_add_f32 v175, v175, v23\n"
"v_mfma_f32_16x16x32_bf16 v[104:107], a[48:51], a[128:131], v[104:107]\n"
"s_add_u32 s69, s69, s98\n"
"v_add_f32 v176, v176, v20\n"
"v_add_f32 v177, v177, v21\n"
"v_mfma_f32_16x16x32_bf16 v[104:107], a[52:55], a[132:135], v[104:107]\n"
"global_load_dwordx4 a[40:43], v0, s[34:35] offset:-2048 sc1\n"
"s_add_u32 s69, s69, s98\n"
"v_mfma_f32_16x16x32_bf16 v[132:135], a[60:63], a[132:135], v[132:135]\n"
"s_add_u32 s69, s69, s98\n"
"ds_read_b128 a[132:135], v3 offset:25600\n"
"v_add_f32 v178, v178, v22\n"
"v_mfma_f32_16x16x32_bf16 v[132:135], a[56:59], a[128:131], v[132:135]\n"
"s_add_u32 s69, s69, s98\n"
"ds_read_b128 a[128:131], v3 offset:24576\n"
"v_add_f32 v179, v179, v23\n"
"v_mfma_f32_16x16x32_bf16 v[136:139], a[56:59], a[136:139], v[136:139]\n"
"s_add_u32 s69, s69, s98\n"
"v_add_f32 v180, v180, v20\n"
"v_add_f32 v181, v181, v21\n"
"v_mfma_f32_16x16x32_bf16 v[136:139], a[60:63], a[140:143], v[136:139]\n"
"s_add_u32 s69, s69, s98\n"
"v_add_f32 v182, v182, v22\n"
"v_add_f32 v183, v183, v23\n"
"v_mfma_f32_16x16x32_bf16 v[108:111], a[52:55], a[140:143], v[108:111]\n"
"s_add_u32 s69, s69, s98\n"
"ds_read_b128 a[140:143], v3 offset:27648\n"
"v_add_f32 v184, v184, v20\n"
"v_mfma_f32_16x16x32_bf16 v[108:111], a[48:51], a[136:139], v[108:111]\n"
"s_add_u32 s69, s69, s98\n"
"ds_read_b128 a[136:139], v3 offset:26624\n"
"v_add_f32 v185, v185, v21\n"
"v_mfma_f32_16x16x32_bf16 v[112:115], a[48:51], a[144:147], v[112:115]\n"
"s_add_u32 s69, s69, s98\n"
"v_add_f32 v186, v186, v22\n"
"v_add_f32 v187, v187, v23\n"
"v_mfma_f32_16x16x32_bf16 v[112:115], a[52:55], a[148:151], v[112:115]\n"
"global_load_dwordx4 a[44:47], v0, s[34:35] offset:-1024 sc1\n"
"s_add_u32 s69, s69, s98\n"
"v_mfma_f32_16x16x32_bf16 v[140:143], a[60:63], a[148:151], v[140:143]\n"
"s_add_u32 s69, s69, s98\n"
"ds_read_b128 a[148:151], v3 offset:29696\n"
"v_add_f32 v188, v188, v20\n"
"v_mfma_f32_16x16x32_bf16 v[140:143], a[56:59], a[144:147], v[140:143]\n"
"s_add_u32 s69, s69, s98\n"
"ds_read_b128 a[144:147], v3 offset:28672\n"
"v_add_f32 v189, v189, v21\n"
"v_mfma_f32_16x16x32_bf16 a[12:15], a[56:59], a[152:155], a[12:15]\n"
"s_add_u32 s69, s69, s98\n"
"v_add_f32 v190, v190, v22\n"
"v_add_f32 v191, v191, v23\n"
"v_mfma_f32_16x16x32_bf16 a[12:15], a[60:63], a[156:159], a[12:15]\n"
"s_add_u32 s69, s69, s98\n"
"v_add_f32 v192, v192, v20\n"
"v_add_f32 v193, v193, v21\n"
"v_mfma_f32_16x16x32_bf16 a[8:11], a[52:55], a[156:159], a[8:11]\n"
"ds_read_b128 a[156:159], v3 offset:31744\n"
"v_add_f32 v194, v194, v22\n"
"v_mfma_f32_16x16x32_bf16 a[8:11], a[48:51], a[152:155], a[8:11]\n"
"s_waitcnt vmcnt(9) lgkmcnt(11)\n"
"ds_read_b128 a[152:155], v3 offset:30720\n"
"v_add_f32 v195, v195, v23\n"
"v_mfma_f32_16x16x32_bf16 v[32:35], a[64:67], a[96:99], v[32:35]\n"
"v_add_f32 v196, v196, v20\n"
"v_add_f32 v197, v197, v21\n"
"v_mfma_f32_16x16x32_bf16 v[32:35], a[68:71], a[100:103], v[32:35]\n"
"global_load_dwordx4 a[48:51], v0, s[34:35] offset:0 sc1\n"
"v_add_f32 v198, v198, v22\n"
"v_mfma_f32_16x16x32_bf16 v[60:63], a[76:79], a[100:103], v[60:63]\n"
"v_add_f32 v199, v199, v23\n"
"v_accvgpr_read_b32 v12, a20\n"
"v_mfma_f32_16x16x32_bf16 v[60:63], a[72:75], a[96:99], v[60:63]\n"
"v_accvgpr_read_b32 v13, a21\n"
"v_accvgpr_read_b32 v14, a22\n"
"v_mfma_f32_16x16x32_bf16 v[64:67], a[72:75], a[104:107], v[64:67]\n"
"v_accvgpr_read_b32 v15, a23\n"
"v_add_f32 v12, v12, v20\n"
"v_mfma_f32_16x16x32_bf16 v[64:67], a[76:79], a[108:111], v[64:67]\n"
"v_add_f32 v13, v13, v21\n"
"v_add_f32 v14, v14, v22\n"
"v_mfma_f32_16x16x32_bf16 v[36:39], a[68:71], a[108:111], v[36:39]\n"
"v_add_f32 v15, v15, v23\n"
"v_accvgpr_write_b32 a20, v12\n"
"v_mfma_f32_16x16x32_bf16 v[36:39], a[64:67], a[104:107], v[36:39]\n"
"s_waitcnt lgkmcnt(8)\n"
"v_accvgpr_write_b32 a21, v13\n"
"v_accvgpr_write_b32 a22, v14\n"
"v_mfma_f32_16x16x32_bf16 v[40:43], a[64:67], a[112:115], v[40:43]\n"
"v_accvgpr_write_b32 a23, v15\n"
"v_lshlrev_b32 v24, 16, v26\n"
"v_mfma_f32_16x16x32_bf16 v[40:43], a[68:71], a[116:119], v[40:43]\n"
"global_load_dwordx4 a[52:55], v0, s[34:35] offset:1024 sc1\n"
"v_and_b32 v25, s63, v26\n"
"v_mfma_f32_16x16x32_bf16 v[68:71], a[76:79], a[116:119], v[68:71]\n"
"v_lshlrev_b32 v26, 16, v27\n"
"v_and_b32 v27, s63, v27\n"
"v_mfma_f32_16x16x32_bf16 v[68:71], a[72:75], a[112:115], v[68:71]\n"
"v_add_f32 v200, v200, v24\n"
"v_add_f32 v201, v201, v25\n"
"v_mfma_f32_16x16x32_bf16 v[72:75], a[72:75], a[120:123], v[72:75]\n"
"v_add_f32 v202, v202, v26\n"
"v_add_f32 v203, v203, v27\n"
"v_mfma_f32_16x16x32_bf16 v[72:75], a[76:79], a[124:127], v[72:75]\n"
"v_add_f32 v204, v204, v24\n"
"v_add_f32 v205, v205, v25\n"
"v_mfma_f32_16x16x32_bf16 v[44:47], a[68:71], a[124:127], v[44:47]\n"
"v_add_f32 v206, v206, v26\n"
"v_add_f32 v207, v207, v27\n"
"v_mfma_f32_16x16x32_bf16 v[44:47], a[64:67], a[120:123], v[44:47]\n"
"s_waitcnt lgkmcnt(4)\n"
"v_add_f32 v208, v208, v24\n"
"v_add_f32 v209, v209, v25\n"
"v_mfma_f32_16x16x32_bf16 v[48:51], a[64:67], a[128:131], v[48:51]\n"
"v_add_f32 v210, v210, v26\n"
"v_add_f32 v211, v211, v27\n"
"v_mfma_f32_16x16x32_bf16 v[48:51], a[68:71], a[132:135], v[48:51]\n"
"global_load_dwordx4 a[56:59], v0, s[34:35] offset:2048 sc1\n"
"v_add_f32 v212, v212, v24\n"
"v_mfma_f32_16x16x32_bf16 v[76:79], a[76:79], a[132:135], v[76:79]\n"
"v_add_f32 v213, v213, v25\n"
"v_add_f32 v214, v214, v26\n"
"v_mfma_f32_16x16x32_bf16 v[76:79], a[72:75], a[128:131], v[76:79]\n"
"v_add_f32 v215, v215, v27\n"
"v_add_f32 v216, v216, v24\n"
"v_mfma_f32_16x16x32_bf16 v[80:83], a[72:75], a[136:139], v[80:83]\n"
"v_add_f32 v217, v217, v25\n"
"v_add_f32 v218, v218, v26\n"
"v_mfma_f32_16x16x32_bf16 v[80:83], a[76:79], a[140:143], v[80:83]\n"
"v_add_f32 v219, v219, v27\n"
"v_add_f32 v220, v220, v24\n"
"v_mfma_f32_16x16x32_bf16 v[52:55], a[68:71], a[140:143], v[52:55]\n"
"v_add_f32 v221, v221, v25\n"
"v_add_f32 v222, v222, v26\n"
"v_mfma_f32_16x16x32_bf16 v[52:55], a[64:67], a[136:139], v[52:55]\n"
"s_waitcnt lgkmcnt(0)\n"
"v_add_f32 v223, v223, v27\n"
"v_mfma_f32_16x16x32_bf16 v[56:59], a[64:67], a[144:147], v[56:59]\n"
"s_mov_b32 s98, 0\n"
"v_add_f32 v224, v224, v24\n"
"v_add_f32 v225, v225, v25\n"
"v_mfma_f32_16x16x32_bf16 v[56:59], a[68:71], a[148:151], v[56:59]\n"
"global_load_dwordx4 a[60:63], v0, s[34:35] offset:3072 sc1\n"
"s_load_dword s98, s[96:97], 8192 glc\n"
"v_mfma_f32_16x16x32_bf16 v[84:87], a[76:79], a[148:151], v[84:87]\n"
"v_add_f32 v226, v226, v26\n"
"v_add_f32 v227, v227, v27\n"
"v_mfma_f32_16x16x32_bf16 v[84:87], a[72:75], a[144:147], v[84:87]\n"
"v_accvgpr_read_b32 v12, a24\n"
"v_accvgpr_read_b32 v13, a25\n"
"v_mfma_f32_16x16x32_bf16 a[4:7], a[72:75], a[152:155], a[4:7]\n"
"s_add_u32 s34, s34, 8192\n"
"v_accvgpr_read_b32 v14, a26\n"
"v_accvgpr_read_b32 v15, a27\n"
"v_mfma_f32_16x16x32_bf16 a[4:7], a[76:79], a[156:159], a[4:7]\n"
"v_add_f32 v12, v12, v24\n"
"v_add_f32 v13, v13, v25\n"
"v_mfma_f32_16x16x32_bf16 a[0:3], a[68:71], a[156:159], a[0:3]\n"
"v_add_f32 v14, v14, v26\n"
"v_add_f32 v15, v15, v27\n"
"v_mfma_f32_16x16x32_bf16 a[0:3], a[64:67], a[152:155], a[0:3]\n"
"s_waitcnt vmcnt(8)\n"
"v_accvgpr_write_b32 a24, v12\n"
"v_accvgpr_write_b32 a25, v13\n"
"v_mfma_f32_16x16x32_bf16 v[88:91], a[80:83], a[96:99], v[88:91]\n"
"v_accvgpr_write_b32 a26, v14\n"
"v_accvgpr_write_b32 a27, v15\n"
"v_mfma_f32_16x16x32_bf16 v[88:91], a[84:87], a[100:103], v[88:91]\n"
"global_load_dwordx4 a[64:67], v0, s[34:35] offset:-4096 sc1\n"
"v_lshlrev_b32 v28, 16, v30\n"
"v_mfma_f32_16x16x32_bf16 v[116:119], a[92:95], a[100:103], v[116:119]\n"
"ds_read_b128 a[100:103], v4 offset:17408\n"
"v_and_b32 v29, s63, v30\n"
"v_mfma_f32_16x16x32_bf16 v[116:119], a[88:91], a[96:99], v[116:119]\n"
"ds_read_b128 a[96:99], v4 offset:16384\n"
"v_lshlrev_b32 v30, 16, v31\n"
"v_mfma_f32_16x16x32_bf16 v[120:123], a[88:91], a[104:107], v[120:123]\n"
"s_sub_u32 s69, s69, s98\n"
"v_and_b32 v31, s63, v31\n"
"v_add_f32 v228, v228, v28\n"
"v_mfma_f32_16x16x32_bf16 v[120:123], a[92:95], a[108:111], v[120:123]\n"
"s_sub_u32 s69, s69, s98\n"
"v_add_f32 v229, v229, v29\n"
"v_add_f32 v230, v230, v30\n"
"v_mfma_f32_16x16x32_bf16 v[92:95], a[84:87], a[108:111], v[92:95]\n"
"s_sub_u32 s69, s69, s98\n"
"ds_read_b128 a[108:111], v4 offset:19456\n"
"v_add_f32 v231, v231, v31\n"
"v_mfma_f32_16x16x32_bf16 v[92:95], a[80:83], a[104:107], v[92:95]\n"
"s_sub_u32 s69, s69, s98\n"
"ds_read_b128 a[104:107], v4 offset:18432\n"
"v_add_f32 v232, v232, v28\n"
"v_mfma_f32_16x16x32_bf16 v[96:99], a[80:83], a[112:115], v[96:99]\n"
"s_sub_u32 s69, s69, s98\n"
"v_add_f32 v233, v233, v29\n"
"v_add_f32 v234, v234, v30\n"
"v_mfma_f32_16x16x32_bf16 v[96:99], a[84:87], a[116:119], v[96:99]\n"
"global_load_dwordx4 a[68:71], v0, s[34:35] offset:-3072 sc1\n"
"s_sub_u32 s69, s69, s98\n"
"v_mfma_f32_16x16x32_bf16 v[124:127], a[92:95], a[116:119], v[124:127]\n"
"s_sub_u32 s69, s69, s98\n"
"ds_read_b128 a[116:119], v4 offset:21504\n"
"v_add_f32 v235, v235, v31\n"
"v_mfma_f32_16x16x32_bf16 v[124:127], a[88:91], a[112:115], v[124:127]\n"
"s_sub_u32 s69, s69, s98\n"
"ds_read_b128 a[112:115], v4 offset:20480\n"
"v_add_f32 v236, v236, v28\n"
"v_mfma_f32_16x16x32_bf16 v[128:131], a[88:91], a[120:123], v[128:131]\n"
"s_sub_u32 s69, s69, s98\n"
"v_add_f32 v237, v237, v29\n"
"v_add_f32 v238, v238, v30\n"
"v_mfma_f32_16x16x32_bf16 v[128:131], a[92:95], a[124:127], v[128:131]\n"
"s_sub_u32 s69, s69, s98\n"
"v_add_f32 v239, v239, v31\n"
"v_add_f32 v240, v240, v28\n"
"v_mfma_f32_16x16x32_bf16 v[100:103], a[84:87], a[124:127], v[100:103]\n"
"s_sub_u32 s69, s69, s98\n"
"ds_read_b128 a[124:127], v4 offset:23552\n"
"v_add_f32 v241, v241, v29\n"
"v_mfma_f32_16x16x32_bf16 v[100:103], a[80:83], a[120:123], v[100:103]\n"
"s_sub_u32 s69, s69, s98\n"
"ds_read_b128 a[120:123], v4 offset:22528\n"
"v_add_f32 v242, v242, v30\n"
"v_mfma_f32_16x16x32_bf16 v[104:107], a[80:83], a[128:131], v[104:107]\n"
"s_sub_u32 s69, s69, s98\n"
"v_add_f32 v243, v243, v31\n"
"v_add_f32 v244, v244, v28\n"
"v_mfma_f32_16x16x32_bf16 v[104:107], a[84:87], a[132:135], v[104:107]\n"
"global_load_dwordx4 a[72:75], v0, s[34:35] offset:-2048 sc1\n"
"s_sub_u32 s69, s69, s98\n"
"v_mfma_f32_16x16x32_bf16 v[132:135], a[92:95], a[132:135], v[132:135]\n"
"s_sub_u32 s69, s69, s98\n"
"ds_read_b128 a[132:135], v4 offset:25600\n"
"v_add_f32 v245, v245, v29\n"
"v_mfma_f32_16x16x32_bf16 v[132:135], a[88:91], a[128:131], v[132:135]\n"
"s_sub_u32 s69, s69, s98\n"
"ds_read_b128 a[128:131], v4 offset:24576\n"
"v_add_f32 v246, v246, v30\n"
"v_mfma_f32_16x16x32_bf16 v[136:139], a[88:91], a[136:139], v[136:139]\n"
"s_sub_u32 s69, s69, s98\n"
"v_add_f32 v247, v247, v31\n"
"v_add_f32 v248, v248, v28\n"
"v_mfma_f32_16x16x32_bf16 v[136:139], a[92:95], a[140:143], v[136:139]\n"
"s_sub_u32 s69, s69, s98\n"
"v_add_f32 v249, v249, v29\n"
"v_add_f32 v250, v250, v30\n"
"v_mfma_f32_16x16x32_bf16 v[108:111], a[84:87], a[140:143], v[108:111]\n"
"s_sub_u32 s69, s69, s98\n"
"ds_read_b128 a[140:143], v4 offset:27648\n"
"v_add_f32 v251, v251, v31\n"
"v_mfma_f32_16x16x32_bf16 v[108:111], a[80:83], a[136:139], v[108:111]\n"
"s_sub_u32 s69, s69, s98\n"
"ds_read_b128 a[136:139], v4 offset:26624\n"
"v_add_f32 v252, v252, v28\n"
"v_add_f32 v253, v253, v29\n"
"v_mfma_f32_16x16x32_bf16 v[112:115], a[80:83], a[144:147], v[112:115]\n"
"s_sub_u32 s69, s69, s98\n"
"v_add_f32 v254, v254, v30\n"
"v_add_f32 v255, v255, v31\n"
"v_mfma_f32_16x16x32_bf16 v[112:115], a[84:87], a[148:151], v[112:115]\n"
"global_load_dwordx4 a[76:79], v0, s[34:35] offset:-1024 sc1\n"
"s_sub_u32 s69, s69, s98\n"
"v_accvgpr_read_b32 v12, a28\n"
"v_mfma_f32_16x16x32_bf16 v[140:143], a[92:95], a[148:151], v[140:143]\n"
"s_sub_u32 s69, s69, s98\n"
"ds_read_b128 a[148:151], v4 offset:29696\n"
"v_accvgpr_read_b32 v13, a29\n"
"v_accvgpr_read_b32 v14, a30\n"
"v_mfma_f32_16x16x32_bf16 v[140:143], a[88:91], a[144:147], v[140:143]\n"
"s_sub_u32 s69, s69, s98\n"
"ds_read_b128 a[144:147], v4 offset:28672\n"
"v_accvgpr_read_b32 v15, a31\n"
"v_mfma_f32_16x16x32_bf16 a[12:15], a[88:91], a[152:155], a[12:15]\n"
"s_sub_u32 s69, s69, s98\n"
"v_add_f32 v12, v12, v28\n"
"v_add_f32 v13, v13, v29\n"
"v_mfma_f32_16x16x32_bf16 a[12:15], a[92:95], a[156:159], a[12:15]\n"
"s_sub_u32 s69, s69, s98\n"
"v_add_f32 v14, v14, v30\n"
"v_add_f32 v15, v15, v31\n"
"v_mfma_f32_16x16x32_bf16 a[8:11], a[84:87], a[156:159], a[8:11]\n"
"ds_read_b128 a[156:159], v4 offset:31744\n"
"v_accvgpr_write_b32 a28, v12\n"
"v_mfma_f32_16x16x32_bf16 a[8:11], a[80:83], a[152:155], a[8:11]\n"
"s_waitcnt vmcnt(8) lgkmcnt(11)\n"
"ds_read_b128 a[152:155], v4 offset:30720\n"
"v_accvgpr_write_b32 a29, v13\n"
"v_mfma_f32_16x16x32_bf16 v[32:35], a[32:35], a[96:99], v[32:35]\n"
"v_accvgpr_write_b32 a30, v14\n"
"v_accvgpr_write_b32 a31, v15\n"
"v_mfma_f32_16x16x32_bf16 v[32:35], a[36:39], a[100:103], v[32:35]\n"
"global_load_dwordx4 a[80:83], v0, s[34:35] offset:0 sc1\n"
"v_mfma_f32_16x16x32_bf16 v[60:63], a[44:47], a[100:103], v[60:63]\n"
"v_mfma_f32_16x16x32_bf16 v[60:63], a[40:43], a[96:99], v[60:63]\n"
"v_mfma_f32_16x16x32_bf16 v[64:67], a[40:43], a[104:107], v[64:67]\n"
"v_mfma_f32_16x16x32_bf16 v[64:67], a[44:47], a[108:111], v[64:67]\n"
"v_mov_b32 v16, s69\n"
"v_mfma_f32_16x16x32_bf16 v[36:39], a[36:39], a[108:111], v[36:39]\n"
"v_mfma_f32_16x16x32_bf16 v[36:39], a[32:35], a[104:107], v[36:39]\n"
"s_waitcnt lgkmcnt(8)\n"
"ds_write_b32 v2, v16 offset:8192\n"
"v_mfma_f32_16x16x32_bf16 v[40:43], a[32:35], a[112:115], v[40:43]\n"
"v_mfma_f32_16x16x32_bf16 v[40:43], a[36:39], a[116:119], v[40:43]\n"
"global_load_dwordx4 a[84:87], v0, s[34:35] offset:1024 sc1\n"
"v_mfma_f32_16x16x32_bf16 v[68:71], a[44:47], a[116:119], v[68:71]\n"
"v_mfma_f32_16x16x32_bf16 v[68:71], a[40:43], a[112:115], v[68:71]\n"
"v_mfma_f32_16x16x32_bf16 v[72:75], a[40:43], a[120:123], v[72:75]\n"
"v_mfma_f32_16x16x32_bf16 v[72:75], a[44:47], a[124:127], v[72:75]\n"
"v_mfma_f32_16x16x32_bf16 v[44:47], a[36:39], a[124:127], v[44:47]\n"
"s_mov_b32 s65, s78\n"
"v_mfma_f32_16x16x32_bf16 v[44:47], a[32:35], a[120:123], v[44:47]\n"
"s_waitcnt lgkmcnt(5)\n"
"v_mfma_f32_16x16x32_bf16 v[48:51], a[32:35], a[128:131], v[48:51]\n"
"v_mfma_f32_16x16x32_bf16 v[48:51], a[36:39], a[132:135], v[48:51]\n"
"global_load_dwordx4 a[88:91], v0, s[34:35] offset:2048 sc1\n"
"v_mfma_f32_16x16x32_bf16 v[76:79], a[44:47], a[132:135], v[76:79]\n"
"v_mfma_f32_16x16x32_bf16 v[76:79], a[40:43], a[128:131], v[76:79]\n"
"v_mfma_f32_16x16x32_bf16 v[80:83], a[40:43], a[136:139], v[80:83]\n"
"v_mfma_f32_16x16x32_bf16 v[80:83], a[44:47], a[140:143], v[80:83]\n"
"v_mfma_f32_16x16x32_bf16 v[52:55], a[36:39], a[140:143], v[52:55]\n"
"v_mfma_f32_16x16x32_bf16 v[52:55], a[32:35], a[136:139], v[52:55]\n"
"s_waitcnt lgkmcnt(0)\n"
"v_mfma_f32_16x16x32_bf16 v[56:59], a[32:35], a[144:147], v[56:59]\n"
"v_mfma_f32_16x16x32_bf16 v[56:59], a[36:39], a[148:151], v[56:59]\n"
"global_load_dwordx4 a[92:95], v0, s[34:35] offset:3072 sc1\n"
"s_barrier\n"
"v_mfma_f32_16x16x32_bf16 v[84:87], a[44:47], a[148:151], v[84:87]\n"
"ds_read_b32 v17, v3 offset:8192\n"
"v_mfma_f32_16x16x32_bf16 v[84:87], a[40:43], a[144:147], v[84:87]\n"
"v_mfma_f32_16x16x32_bf16 a[4:7], a[40:43], a[152:155], a[4:7]\n"
"s_add_u32 s34, s34, 8192\n"
"ds_read_b32 v18, v4 offset:8192\n"
"v_mfma_f32_16x16x32_bf16 a[4:7], a[44:47], a[156:159], a[4:7]\n"
"v_mfma_f32_16x16x32_bf16 a[0:3], a[36:39], a[156:159], a[0:3]\n"
"ds_read_b32 v19, v5 offset:8192\n"
"v_mfma_f32_16x16x32_bf16 a[0:3], a[32:35], a[152:155], a[0:3]\n"
"s_waitcnt vmcnt(8)\n"
"v_mfma_f32_16x16x32_bf16 v[88:91], a[48:51], a[96:99], v[88:91]\n"
"v_mfma_f32_16x16x32_bf16 v[88:91], a[52:55], a[100:103], v[88:91]\n"
"global_load_dwordx4 a[32:35], v0, s[34:35] offset:-4096 sc1\n"
"v_mfma_f32_16x16x32_bf16 v[116:119], a[60:63], a[100:103], v[116:119]\n"
"ds_read_b128 a[100:103], v5 offset:17408\n"
"v_mfma_f32_16x16x32_bf16 v[116:119], a[56:59], a[96:99], v[116:119]\n"
"ds_read_b128 a[96:99], v5 offset:16384\n"
"v_mfma_f32_16x16x32_bf16 v[120:123], a[56:59], a[104:107], v[120:123]\n"
"s_waitcnt lgkmcnt(2)\n"
"v_add_u32 v16, v16, v17\n"
"v_mfma_f32_16x16x32_bf16 v[120:123], a[60:63], a[108:111], v[120:123]\n"
"v_add3_u32 v16, v16, v18, v19\n"
"v_mfma_f32_16x16x32_bf16 v[92:95], a[52:55], a[108:111], v[92:95]\n"
"ds_read_b128 a[108:111], v5 offset:19456\n"
"v_mfma_f32_16x16x32_bf16 v[92:95], a[48:51], a[104:107], v[92:95]\n"
"ds_read_b128 a[104:107], v5 offset:18432\n"
"v_mfma_f32_16x16x32_bf16 v[96:99], a[48:51], a[112:115], v[96:99]\n"
"v_mfma_f32_16x16x32_bf16 v[96:99], a[52:55], a[116:119], v[96:99]\n"
"global_load_dwordx4 a[36:39], v0, s[34:35] offset:-3072 sc1\n"
"v_readfirstlane_b32 s69, v16\n"
"v_mfma_f32_16x16x32_bf16 v[124:127], a[60:63], a[116:119], v[124:127]\n"
"ds_read_b128 a[116:119], v5 offset:21504\n"
"v_mfma_f32_16x16x32_bf16 v[124:127], a[56:59], a[112:115], v[124:127]\n"
"ds_read_b128 a[112:115], v5 offset:20480\n"
"v_mfma_f32_16x16x32_bf16 v[128:131], a[56:59], a[120:123], v[128:131]\n"
"s_cmp_le_i32 s69, -4\n"
"s_cselect_b32 s66, 2, 1\n"
"v_mfma_f32_16x16x32_bf16 v[128:131], a[60:63], a[124:127], v[128:131]\n"
"v_mfma_f32_16x16x32_bf16 v[100:103], a[52:55], a[124:127], v[100:103]\n"
"s_cmp_ge_i32 s69, 4\n"
"ds_read_b128 a[124:127], v5 offset:23552\n"
"s_cselect_b32 s66, 3, s66\n"
"v_mfma_f32_16x16x32_bf16 v[100:103], a[48:51], a[120:123], v[100:103]\n"
"ds_read_b128 a[120:123], v5 offset:22528\n"
"v_mfma_f32_16x16x32_bf16 v[104:107], a[48:51], a[128:131], v[104:107]\n"
"s_lshl_b32 s73, s66, 13\n"
"s_xor_b32 s60, s79, s66\n"
"v_mfma_f32_16x16x32_bf16 v[104:107], a[52:55], a[132:135], v[104:107]\n"
"global_load_dwordx4 a[40:43], v0, s[34:35] offset:-2048 sc1\n"
"v_mfma_f32_16x16x32_bf16 v[132:135], a[60:63], a[132:135], v[132:135]\n"
"s_cmp_eq_u32 s66, 3\n"
"ds_read_b128 a[132:135], v5 offset:25600\n"
"s_cselect_b32 s72, 8192, 0\n"
"v_mfma_f32_16x16x32_bf16 v[132:135], a[56:59], a[128:131], v[132:135]\n"
"ds_read_b128 a[128:131], v5 offset:24576\n"
"v_mfma_f32_16x16x32_bf16 v[136:139], a[56:59], a[136:139], v[136:139]\n"
"s_add_u32 s52, s56, %11\n"
"s_addc_u32 s53, s57, 0\n"
"ds_read_b128 v[16:19], v7 offset:32768\n"
"v_mfma_f32_16x16x32_bf16 v[136:139], a[60:63], a[140:143], v[136:139]\n"
"v_mfma_f32_16x16x32_bf16 v[108:111], a[52:55], a[140:143], v[108:111]\n"
"s_add_u32 s52, s52, 4096\n"
"ds_read_b128 a[140:143], v5 offset:27648\n"
"s_addc_u32 s53, s53, 0\n"
"v_mfma_f32_16x16x32_bf16 v[108:111], a[48:51], a[136:139], v[108:111]\n"
"ds_read_b128 a[136:139], v5 offset:26624\n"
"v_mfma_f32_16x16x32_bf16 v[112:115], a[48:51], a[144:147], v[112:115]\n"
"s_add_u32 s54, s52, s73\n"
"s_addc_u32 s55, s53, 0\n"
"v_mfma_f32_16x16x32_bf16 v[112:115], a[52:55], a[148:151], v[112:115]\n"
"global_load_dwordx4 a[44:47], v0, s[34:35] offset:-1024 sc1\n"
"v_mfma_f32_16x16x32_bf16 v[140:143], a[60:63], a[148:151], v[140:143]\n"
"s_add_u32 s52, s52, s72\n"
"ds_read_b128 a[148:151], v5 offset:29696\n"
"s_addc_u32 s53, s53, 0\n"
"v_mfma_f32_16x16x32_bf16 v[140:143], a[56:59], a[144:147], v[140:143]\n"
"ds_read_b128 a[144:147], v5 offset:28672\n"
"v_mfma_f32_16x16x32_bf16 a[12:15], a[56:59], a[152:155], a[12:15]\n"
"s_add_u32 s48, s56, %12\n"
"s_addc_u32 s49, s57, 0\n"
"v_mfma_f32_16x16x32_bf16 a[12:15], a[60:63], a[156:159], a[12:15]\n"
"v_mfma_f32_16x16x32_bf16 a[8:11], a[52:55], a[156:159], a[8:11]\n"
"s_mov_b32 s50, 32768\n"
"ds_read_b128 a[156:159], v5 offset:31744\n"
"v_mfma_f32_16x16x32_bf16 a[8:11], a[48:51], a[152:155], a[8:11]\n"
"ds_read_b128 a[152:155], v5 offset:30720\n"
"L_P6:\n"
"s_waitcnt vmcnt(8) lgkmcnt(13)\n"
"v_mfma_f32_16x16x32_bf16 v[32:35], a[64:67], a[96:99], v[32:35]\n"
"v_mfma_f32_16x16x32_bf16 v[32:35], a[68:71], a[100:103], v[32:35]\n"
"global_load_dwordx4 a[48:51], v0, s[34:35] offset:0 sc1\n"
"v_mfma_f32_16x16x32_bf16 v[60:63], a[76:79], a[100:103], v[60:63]\n"
"v_mfma_f32_16x16x32_bf16 v[60:63], a[72:75], a[96:99], v[60:63]\n"
"v_mfma_f32_16x16x32_bf16 v[64:67], a[72:75], a[104:107], v[64:67]\n"
"v_mfma_f32_16x16x32_bf16 v[64:67], a[76:79], a[108:111], v[64:67]\n"
"v_mfma_f32_16x16x32_bf16 v[36:39], a[68:71], a[108:111], v[36:39]\n"
"s_waitcnt lgkmcnt(6)\n"
"v_lshlrev_b32 v12, 16, v16\n"
"v_mfma_f32_16x16x32_bf16 v[36:39], a[64:67], a[104:107], v[36:39]\n"
"v_and_b32 v13, s63, v16\n"
"v_lshlrev_b32 v14, 16, v17\n"
"v_mfma_f32_16x16x32_bf16 v[40:43], a[64:67], a[112:115], v[40:43]\n"
"v_and_b32 v15, s63, v17\n"
"v_mfma_f32_16x16x32_bf16 v[40:43], a[68:71], a[116:119], v[40:43]\n"
"global_load_dwordx4 a[52:55], v0, s[34:35] offset:1024 sc1\n"
"ds_read_b128 v[20:23], v7 offset:34816\n"
"v_mfma_f32_16x16x32_bf16 v[68:71], a[76:79], a[116:119], v[68:71]\n"
"v_mul_f32 v12, v12, v32\n"
"v_mul_f32 v13, v13, v33\n"
"v_mfma_f32_16x16x32_bf16 v[68:71], a[72:75], a[112:115], v[68:71]\n"
"v_mul_f32 v14, v14, v34\n"
"v_mfma_f32_16x16x32_bf16 v[72:75], a[72:75], a[120:123], v[72:75]\n"
"v_mul_f32 v15, v15, v35\n"
"v_mfma_f32_16x16x32_bf16 v[72:75], a[76:79], a[124:127], v[72:75]\n"
"v_cvt_pk_bf16_f32 v24, v12, v13\n"
"v_cvt_pk_bf16_f32 v25, v14, v15\n"
"v_mfma_f32_16x16x32_bf16 v[44:47], a[68:71], a[124:127], v[44:47]\n"
"ds_write_b64 v2, v[24:25] offset:0\n"
"v_mfma_f32_16x16x32_bf16 v[44:47], a[64:67], a[120:123], v[44:47]\n"
"s_waitcnt lgkmcnt(6)\n"
"v_lshlrev_b32 v12, 16, v18\n"
"v_mfma_f32_16x16x32_bf16 v[48:51], a[64:67], a[128:131], v[48:51]\n"
"v_and_b32 v13, s63, v18\n"
"v_lshlrev_b32 v14, 16, v19\n"
"v_mfma_f32_16x16x32_bf16 v[48:51], a[68:71], a[132:135], v[48:51]\n"
"global_load_dwordx4 a[56:59], v0, s[34:35] offset:2048 sc1\n"
"v_and_b32 v15, s63, v19\n"
"v_mfma_f32_16x16x32_bf16 v[76:79], a[76:79], a[132:135], v[76:79]\n"
"v_mul_f32 v12, v12, v60\n"
"v_mfma_f32_16x16x32_bf16 v[76:79], a[72:75], a[128:131], v[76:79]\n"
"v_mul_f32 v13, v13, v61\n"
"v_mul_f32 v14, v14, v62\n"
"v_mfma_f32_16x16x32_bf16 v[80:83], a[72:75], a[136:139], v[80:83]\n"
"v_mul_f32 v15, v15, v63\n"
"v_mfma_f32_16x16x32_bf16 v[80:83], a[76:79], a[140:143], v[80:83]\n"
"v_cvt_pk_bf16_f32 v26, v12, v13\n"
"v_cvt_pk_bf16_f32 v27, v14, v15\n"
"v_mfma_f32_16x16x32_bf16 v[52:55], a[68:71], a[140:143], v[52:55]\n"
"ds_write_b64 v2, v[26:27] offset:8\n"
"v_mfma_f32_16x16x32_bf16 v[52:55], a[64:67], a[136:139], v[52:55]\n"
"s_waitcnt lgkmcnt(2)\n"
"v_lshlrev_b32 v12, 16, v20\n"
"v_mfma_f32_16x16x32_bf16 v[56:59], a[64:67], a[144:147], v[56:59]\n"
"v_and_b32 v13, s63, v20\n"
"v_mfma_f32_16x16x32_bf16 v[56:59], a[68:71], a[148:151], v[56:59]\n"
"global_load_dwordx4 a[60:63], v0, s[34:35] offset:3072 sc1\n"
"v_lshlrev_b32 v14, 16, v21\n"
"v_mfma_f32_16x16x32_bf16 v[84:87], a[76:79], a[148:151], v[84:87]\n"
"v_and_b32 v15, s63, v21\n"
"v_xor_b32 v16, 64, v7\n"
"v_mfma_f32_16x16x32_bf16 v[84:87], a[72:75], a[144:147], v[84:87]\n"
"ds_read_b128 v[16:19], v16 offset:32768\n"
"v_mfma_f32_16x16x32_bf16 a[4:7], a[72:75], a[152:155], a[4:7]\n"
"s_add_u32 s34, s34, 8192\n"
"v_mul_f32 v12, v12, v36\n"
"v_mul_f32 v13, v13, v37\n"
"v_mfma_f32_16x16x32_bf16 a[4:7], a[76:79], a[156:159], a[4:7]\n"
"v_mul_f32 v14, v14, v38\n"
"v_mfma_f32_16x16x32_bf16 a[0:3], a[68:71], a[156:159], a[0:3]\n"
"v_mul_f32 v15, v15, v39\n"
"v_mfma_f32_16x16x32_bf16 a[0:3], a[64:67], a[152:155], a[0:3]\n"
"s_waitcnt vmcnt(8)\n"
"v_cvt_pk_bf16_f32 v28, v12, v13\n"
"v_cvt_pk_bf16_f32 v29, v14, v15\n"
"v_mfma_f32_16x16x32_bf16 v[88:91], a[80:83], a[96:99], v[88:91]\n"
"v_lshlrev_b32 v12, 16, v22\n"
"v_mfma_f32_16x16x32_bf16 v[88:91], a[84:87], a[100:103], v[88:91]\n"
"global_load_dwordx4 a[64:67], v0, s[34:35] offset:-4096 sc1\n"
"v_and_b32 v13, s63, v22\n"
"v_mfma_f32_16x16x32_bf16 v[116:119], a[92:95], a[100:103], v[116:119]\n"
"ds_read_b128 a[100:103], v2 offset:17408\n"
"v_lshlrev_b32 v14, 16, v23\n"
"v_mfma_f32_16x16x32_bf16 v[116:119], a[88:91], a[96:99], v[116:119]\n"
"ds_read_b128 a[96:99], v2 offset:16384\n"
"v_and_b32 v15, s63, v23\n"
"v_mfma_f32_16x16x32_bf16 v[120:123], a[88:91], a[104:107], v[120:123]\n"
"v_mul_f32 v12, v12, v64\n"
"v_mul_f32 v13, v13, v65\n"
"v_mfma_f32_16x16x32_bf16 v[120:123], a[92:95], a[108:111], v[120:123]\n"
"v_mul_f32 v14, v14, v66\n"
"v_mul_f32 v15, v15, v67\n"
"v_mfma_f32_16x16x32_bf16 v[92:95], a[84:87], a[108:111], v[92:95]\n"
"ds_read_b128 a[108:111], v2 offset:19456\n"
"v_cvt_pk_bf16_f32 v30, v12, v13\n"
"v_mfma_f32_16x16x32_bf16 v[92:95], a[80:83], a[104:107], v[92:95]\n"
"ds_read_b128 a[104:107], v2 offset:18432\n"
"v_cvt_pk_bf16_f32 v31, v14, v15\n"
"v_mfma_f32_16x16x32_bf16 v[96:99], a[80:83], a[112:115], v[96:99]\n"
"global_store_dwordx4 v0, v[28:31], s[52:53] offset:-2048 sc1\n"
"ds_write_b128 v2, v[28:31] offset:2048\n"
"v_mfma_f32_16x16x32_bf16 v[96:99], a[84:87], a[116:119], v[96:99]\n"
"s_waitcnt lgkmcnt(5)\n"
"global_load_dwordx4 a[68:71], v0, s[34:35] offset:-3072 sc1\n"
"v_lshlrev_b32 v12, 16, v16\n"
"v_mfma_f32_16x16x32_bf16 v[124:127], a[92:95], a[116:119], v[124:127]\n"
"ds_read_b128 a[116:119], v2 offset:21504\n"
"v_and_b32 v13, s63, v16\n"
"v_mfma_f32_16x16x32_bf16 v[124:127], a[88:91], a[112:115], v[124:127]\n"
"ds_read_b128 a[112:115], v2 offset:20480\n"
"v_lshlrev_b32 v14, 16, v17\n"
"v_mfma_f32_16x16x32_bf16 v[128:131], a[88:91], a[120:123], v[128:131]\n"
"v_and_b32 v15, s63, v17\n"
"v_xor_b32 v20, 64, v7\n"
"v_mfma_f32_16x16x32_bf16 v[128:131], a[92:95], a[124:127], v[128:131]\n"
"ds_read_b128 v[20:23], v20 offset:34816\n"
"v_mul_f32 v12, v12, v88\n"
"v_mfma_f32_16x16x32_bf16 v[100:103], a[84:87], a[124:127], v[100:103]\n"
"ds_read_b128 a[124:127], v2 offset:23552\n"
"v_mul_f32 v13, v13, v89\n"
"v_mfma_f32_16x16x32_bf16 v[100:103], a[80:83], a[120:123], v[100:103]\n"
"ds_read_b128 a[120:123], v2 offset:22528\n"
"v_mul_f32 v14, v14, v90\n"
"v_mfma_f32_16x16x32_bf16 v[104:107], a[80:83], a[128:131], v[104:107]\n"
"v_mul_f32 v15, v15, v91\n"
"v_cvt_pk_bf16_f32 v24, v12, v13\n"
"v_mfma_f32_16x16x32_bf16 v[104:107], a[84:87], a[132:135], v[104:107]\n"
"global_load_dwordx4 a[72:75], v0, s[34:35] offset:-2048 sc1\n"
"v_cvt_pk_bf16_f32 v25, v14, v15\n"
"v_mfma_f32_16x16x32_bf16 v[132:135], a[92:95], a[132:135], v[132:135]\n"
"ds_read_b128 a[132:135], v2 offset:25600\n"
"v_lshlrev_b32 v12, 16, v18\n"
"v_mfma_f32_16x16x32_bf16 v[132:135], a[88:91], a[128:131], v[132:135]\n"
"ds_read_b128 a[128:131], v2 offset:24576\n"
"v_and_b32 v13, s63, v18\n"
"v_mfma_f32_16x16x32_bf16 v[136:139], a[88:91], a[136:139], v[136:139]\n"
"v_lshlrev_b32 v14, 16, v19\n"
"v_and_b32 v15, s63, v19\n"
"v_mfma_f32_16x16x32_bf16 v[136:139], a[92:95], a[140:143], v[136:139]\n"
"v_mul_f32 v12, v12, v116\n"
"v_mfma_f32_16x16x32_bf16 v[108:111], a[84:87], a[140:143], v[108:111]\n"
"ds_read_b128 a[140:143], v2 offset:27648\n"
"v_mul_f32 v13, v13, v117\n"
"v_mfma_f32_16x16x32_bf16 v[108:111], a[80:83], a[136:139], v[108:111]\n"
"ds_read_b128 a[136:139], v2 offset:26624\n"
"v_mul_f32 v14, v14, v118\n"
"v_mfma_f32_16x16x32_bf16 v[112:115], a[80:83], a[144:147], v[112:115]\n"
"v_mul_f32 v15, v15, v119\n"
"v_cvt_pk_bf16_f32 v26, v12, v13\n"
"v_mfma_f32_16x16x32_bf16 v[112:115], a[84:87], a[148:151], v[112:115]\n"
"global_load_dwordx4 a[76:79], v0, s[34:35] offset:-1024 sc1\n"
"v_cvt_pk_bf16_f32 v27, v14, v15\n"
"v_mfma_f32_16x16x32_bf16 v[140:143], a[92:95], a[148:151], v[140:143]\n"
"ds_read_b128 a[148:151], v2 offset:29696\n"
"v_mfma_f32_16x16x32_bf16 v[140:143], a[88:91], a[144:147], v[140:143]\n"
"ds_read_b128 a[144:147], v2 offset:28672\n"
"v_mfma_f32_16x16x32_bf16 a[12:15], a[88:91], a[152:155], a[12:15]\n"
"global_store_dwordx4 v0, v[24:27], s[52:53] offset:-3072 sc1\n"
"ds_write_b128 v2, v[24:27] offset:1024\n"
"v_mfma_f32_16x16x32_bf16 a[12:15], a[92:95], a[156:159], a[12:15]\n"
"s_waitcnt lgkmcnt(9)\n"
"v_lshlrev_b32 v12, 16, v20\n"
"v_and_b32 v13, s63, v20\n"
"v_mfma_f32_16x16x32_bf16 a[8:11], a[84:87], a[156:159], a[8:11]\n"
"ds_read_b128 a[156:159], v2 offset:31744\n"
"v_lshlrev_b32 v14, 16, v21\n"
"v_mfma_f32_16x16x32_bf16 a[8:11], a[80:83], a[152:155], a[8:11]\n"
"s_waitcnt vmcnt(10)\n"
"ds_read_b128 a[152:155], v2 offset:30720\n"
"v_and_b32 v15, s63, v21\n"
"v_mfma_f32_16x16x32_bf16 v[144:147], a[32:35], a[96:99], v[144:147]\n"
"ds_read_b128 v[16:19], v7 offset:36864\n"
"v_mul_f32 v12, v12, v92\n"
"v_mfma_f32_16x16x32_bf16 v[144:147], a[36:39], a[100:103], v[144:147]\n"
"global_load_dwordx4 a[80:83], v0, s[34:35] offset:0 sc1\n"
"v_mul_f32 v13, v13, v93\n"
"v_mfma_f32_16x16x32_bf16 v[172:175], a[44:47], a[100:103], v[172:175]\n"
"v_mul_f32 v14, v14, v94\n"
"v_mul_f32 v15, v15, v95\n"
"v_mfma_f32_16x16x32_bf16 v[172:175], a[40:43], a[96:99], v[172:175]\n"
"v_cvt_pk_bf16_f32 v28, v12, v13\n"
"v_cvt_pk_bf16_f32 v29, v14, v15\n"
"v_mfma_f32_16x16x32_bf16 v[176:179], a[40:43], a[104:107], v[176:179]\n"
"ds_write_b64 v2, v[28:29] offset:3072\n"
"v_mfma_f32_16x16x32_bf16 v[176:179], a[44:47], a[108:111], v[176:179]\n"
"v_lshlrev_b32 v12, 16, v22\n"
"v_and_b32 v13, s63, v22\n"
"v_mfma_f32_16x16x32_bf16 v[148:151], a[36:39], a[108:111], v[148:151]\n"
"v_lshlrev_b32 v14, 16, v23\n"
"v_mfma_f32_16x16x32_bf16 v[148:151], a[32:35], a[104:107], v[148:151]\n"
"s_waitcnt lgkmcnt(11)\n"
"v_and_b32 v15, s63, v23\n"
"v_mul_f32 v12, v12, v120\n"
"v_mfma_f32_16x16x32_bf16 v[152:155], a[32:35], a[112:115], v[152:155]\n"
"v_mul_f32 v13, v13, v121\n"
"v_mfma_f32_16x16x32_bf16 v[152:155], a[36:39], a[116:119], v[152:155]\n"
"global_load_dwordx4 a[84:87], v0, s[34:35] offset:1024 sc1\n"
"v_mul_f32 v14, v14, v122\n"
"v_mfma_f32_16x16x32_bf16 v[180:183], a[44:47], a[116:119], v[180:183]\n"
"v_mul_f32 v15, v15, v123\n"
"v_cvt_pk_bf16_f32 v30, v12, v13\n"
"v_mfma_f32_16x16x32_bf16 v[180:183], a[40:43], a[112:115], v[180:183]\n"
"v_cvt_pk_bf16_f32 v31, v14, v15\n"
"v_mfma_f32_16x16x32_bf16 v[184:187], a[40:43], a[120:123], v[184:187]\n"
"ds_write_b64 v2, v[30:31] offset:3080\n"
"v_mfma_f32_16x16x32_bf16 v[184:187], a[44:47], a[124:127], v[184:187]\n"
"s_waitcnt lgkmcnt(2)\n"
"v_lshlrev_b32 v12, 16, v16\n"
"v_and_b32 v13, s63, v16\n"
"v_mfma_f32_16x16x32_bf16 v[156:159], a[36:39], a[124:127], v[156:159]\n"
"v_lshlrev_b32 v14, 16, v17\n"
"v_mfma_f32_16x16x32_bf16 v[156:159], a[32:35], a[120:123], v[156:159]\n"
"v_and_b32 v15, s63, v17\n"
"v_xor_b32 v20, 64, v7\n"
"v_mfma_f32_16x16x32_bf16 v[160:163], a[32:35], a[128:131], v[160:163]\n"
"ds_read_b128 v[20:23], v20 offset:36864\n"
"v_mfma_f32_16x16x32_bf16 v[160:163], a[36:39], a[132:135], v[160:163]\n"
"global_load_dwordx4 a[88:91], v0, s[34:35] offset:2048 sc1\n"
"v_mul_f32 v12, v12, v40\n"
"v_mfma_f32_16x16x32_bf16 v[188:191], a[44:47], a[132:135], v[188:191]\n"
"v_mul_f32 v13, v13, v41\n"
"v_mfma_f32_16x16x32_bf16 v[188:191], a[40:43], a[128:131], v[188:191]\n"
"v_mul_f32 v14, v14, v42\n"
"v_mul_f32 v15, v15, v43\n"
"v_mfma_f32_16x16x32_bf16 v[192:195], a[40:43], a[136:139], v[192:195]\n"
"v_cvt_pk_bf16_f32 v24, v12, v13\n"
"v_mfma_f32_16x16x32_bf16 v[192:195], a[44:47], a[140:143], v[192:195]\n"
"v_cvt_pk_bf16_f32 v25, v14, v15\n"
"v_mfma_f32_16x16x32_bf16 v[164:167], a[36:39], a[140:143], v[164:167]\n"
"v_lshlrev_b32 v12, 16, v18\n"
"v_and_b32 v13, s63, v18\n"
"v_mfma_f32_16x16x32_bf16 v[164:167], a[32:35], a[136:139], v[164:167]\n"
"v_lshlrev_b32 v14, 16, v19\n"
"v_mfma_f32_16x16x32_bf16 v[168:171], a[32:35], a[144:147], v[168:171]\n"
"v_and_b32 v15, s63, v19\n"
"v_mul_f32 v12, v12, v68\n"
"v_mfma_f32_16x16x32_bf16 v[168:171], a[36:39], a[148:151], v[168:171]\n"
"global_load_dwordx4 a[92:95], v0, s[34:35] offset:3072 sc1\n"
"v_mul_f32 v13, v13, v69\n"
"v_mfma_f32_16x16x32_bf16 v[196:199], a[44:47], a[148:151], v[196:199]\n"
"v_mul_f32 v14, v14, v70\n"
"v_mfma_f32_16x16x32_bf16 v[196:199], a[40:43], a[144:147], v[196:199]\n"
"v_mul_f32 v15, v15, v71\n"
"v_mfma_f32_16x16x32_bf16 a[20:23], a[40:43], a[152:155], a[20:23]\n"
"s_add_u32 s34, s34, 8192\n"
"v_cvt_pk_bf16_f32 v26, v12, v13\n"
"v_cvt_pk_bf16_f32 v27, v14, v15\n"
"v_mfma_f32_16x16x32_bf16 a[20:23], a[44:47], a[156:159], a[20:23]\n"
"global_store_dwordx4 v0, v[24:27], s[52:53] offset:0 sc1\n"
"ds_write_b128 v2, v[24:27] offset:4096\n"
"v_mfma_f32_16x16x32_bf16 a[16:19], a[36:39], a[156:159], a[16:19]\n"
"s_waitcnt lgkmcnt(1)\n"
"v_lshlrev_b32 v12, 16, v20\n"
"v_mfma_f32_16x16x32_bf16 a[16:19], a[32:35], a[152:155], a[16:19]\n"
"s_waitcnt vmcnt(11)\n"
"v_and_b32 v13, s63, v20\n"
"v_mfma_f32_16x16x32_bf16 v[200:203], a[48:51], a[96:99], v[200:203]\n"
"v_lshlrev_b32 v14, 16, v21\n"
"v_and_b32 v15, s63, v21\n"
"v_mfma_f32_16x16x32_bf16 v[200:203], a[52:55], a[100:103], v[200:203]\n"
"global_load_dwordx4 a[32:35], v0, s[34:35] offset:-4096 sc1\n"
"ds_read_b128 v[16:19], v7 offset:38912\n"
"v_mfma_f32_16x16x32_bf16 v[228:231], a[60:63], a[100:103], v[228:231]\n"
"ds_read_b128 a[100:103], v3 offset:17408\n"
"v_mul_f32 v12, v12, v96\n"
"v_mfma_f32_16x16x32_bf16 v[228:231], a[56:59], a[96:99], v[228:231]\n"
"ds_read_b128 a[96:99], v3 offset:16384\n"
"v_mul_f32 v13, v13, v97\n"
"v_mfma_f32_16x16x32_bf16 v[232:235], a[56:59], a[104:107], v[232:235]\n"
"v_mul_f32 v14, v14, v98\n"
"v_mfma_f32_16x16x32_bf16 v[232:235], a[60:63], a[108:111], v[232:235]\n"
"v_mul_f32 v15, v15, v99\n"
"v_cvt_pk_bf16_f32 v28, v12, v13\n"
"v_mfma_f32_16x16x32_bf16 v[204:207], a[52:55], a[108:111], v[204:207]\n"
"ds_read_b128 a[108:111], v3 offset:19456\n"
"v_cvt_pk_bf16_f32 v29, v14, v15\n"
"v_mfma_f32_16x16x32_bf16 v[204:207], a[48:51], a[104:107], v[204:207]\n"
"ds_read_b128 a[104:107], v3 offset:18432\n"
"v_lshlrev_b32 v12, 16, v22\n"
"v_mfma_f32_16x16x32_bf16 v[208:211], a[48:51], a[112:115], v[208:211]\n"
"v_and_b32 v13, s63, v22\n"
"v_lshlrev_b32 v14, 16, v23\n"
"v_mfma_f32_16x16x32_bf16 v[208:211], a[52:55], a[116:119], v[208:211]\n"
"global_load_dwordx4 a[36:39], v0, s[34:35] offset:-3072 sc1\n"
"v_and_b32 v15, s63, v23\n"
"v_mfma_f32_16x16x32_bf16 v[236:239], a[60:63], a[116:119], v[236:239]\n"
"ds_read_b128 a[116:119], v3 offset:21504\n"
"v_mul_f32 v12, v12, v124\n"
"v_mfma_f32_16x16x32_bf16 v[236:239], a[56:59], a[112:115], v[236:239]\n"
"ds_read_b128 a[112:115], v3 offset:20480\n"
"v_mul_f32 v13, v13, v125\n"
"v_mfma_f32_16x16x32_bf16 v[240:243], a[56:59], a[120:123], v[240:243]\n"
"v_mul_f32 v14, v14, v126\n"
"v_mul_f32 v15, v15, v127\n"
"v_mfma_f32_16x16x32_bf16 v[240:243], a[60:63], a[124:127], v[240:243]\n"
"v_cvt_pk_bf16_f32 v30, v12, v13\n"
"v_mfma_f32_16x16x32_bf16 v[212:215], a[52:55], a[124:127], v[212:215]\n"
"ds_read_b128 a[124:127], v3 offset:23552\n"
"v_cvt_pk_bf16_f32 v31, v14, v15\n"
"v_mfma_f32_16x16x32_bf16 v[212:215], a[48:51], a[120:123], v[212:215]\n"
"ds_read_b128 a[120:123], v3 offset:22528\n"
"v_mfma_f32_16x16x32_bf16 v[216:219], a[48:51], a[128:131], v[216:219]\n"
"global_store_dwordx4 v0, v[28:31], s[52:53] offset:1024 sc1\n"
"ds_write_b128 v2, v[28:31] offset:5120\n"
"v_mfma_f32_16x16x32_bf16 v[216:219], a[52:55], a[132:135], v[216:219]\n"
"s_waitcnt lgkmcnt(9)\n"
"global_load_dwordx4 a[40:43], v0, s[34:35] offset:-2048 sc1\n"
"v_lshlrev_b32 v12, 16, v16\n"
"v_mfma_f32_16x16x32_bf16 v[244:247], a[60:63], a[132:135], v[244:247]\n"
"ds_read_b128 a[132:135], v3 offset:25600\n"
"v_and_b32 v13, s63, v16\n"
"v_mfma_f32_16x16x32_bf16 v[244:247], a[56:59], a[128:131], v[244:247]\n"
"ds_read_b128 a[128:131], v3 offset:24576\n"
"v_lshlrev_b32 v14, 16, v17\n"
"v_mfma_f32_16x16x32_bf16 v[248:251], a[56:59], a[136:139], v[248:251]\n"
"v_and_b32 v15, s63, v17\n"
"v_xor_b32 v20, 64, v7\n"
"v_mfma_f32_16x16x32_bf16 v[248:251], a[60:63], a[140:143], v[248:251]\n"
"ds_read_b128 v[20:23], v20 offset:38912\n"
"v_mul_f32 v12, v12, v44\n"
"v_mfma_f32_16x16x32_bf16 v[220:223], a[52:55], a[140:143], v[220:223]\n"
"ds_read_b128 a[140:143], v3 offset:27648\n"
"v_mul_f32 v13, v13, v45\n"
"v_mfma_f32_16x16x32_bf16 v[220:223], a[48:51], a[136:139], v[220:223]\n"
"ds_read_b128 a[136:139], v3 offset:26624\n"
"v_mul_f32 v14, v14, v46\n"
"v_mfma_f32_16x16x32_bf16 v[224:227], a[48:51], a[144:147], v[224:227]\n"
"v_mul_f32 v15, v15, v47\n"
"v_cvt_pk_bf16_f32 v24, v12, v13\n"
"v_mfma_f32_16x16x32_bf16 v[224:227], a[52:55], a[148:151], v[224:227]\n"
"global_load_dwordx4 a[44:47], v0, s[34:35] offset:-1024 sc1\n"
"v_cvt_pk_bf16_f32 v25, v14, v15\n"
"v_mfma_f32_16x16x32_bf16 v[252:255], a[60:63], a[148:151], v[252:255]\n"
"ds_read_b128 a[148:151], v3 offset:29696\n"
"v_mfma_f32_16x16x32_bf16 v[252:255], a[56:59], a[144:147], v[252:255]\n"
"ds_read_b128 a[144:147], v3 offset:28672\n"
"v_mfma_f32_16x16x32_bf16 a[28:31], a[56:59], a[152:155], a[28:31]\n"
"ds_write_b64 v2, v[24:25] offset:6144\n"
"v_mfma_f32_16x16x32_bf16 a[28:31], a[60:63], a[156:159], a[28:31]\n"
"v_lshlrev_b32 v12, 16, v18\n"
"v_and_b32 v13, s63, v18\n"
"v_mfma_f32_16x16x32_bf16 a[24:27], a[52:55], a[156:159], a[24:27]\n"
"ds_read_b128 a[156:159], v3 offset:31744\n"
"v_lshlrev_b32 v14, 16, v19\n"
"v_mfma_f32_16x16x32_bf16 a[24:27], a[48:51], a[152:155], a[24:27]\n"
"s_waitcnt vmcnt(11) lgkmcnt(14)\n"
"ds_read_b128 a[152:155], v3 offset:30720\n"
"v_and_b32 v15, s63, v19\n"
"v_mfma_f32_16x16x32_bf16 v[144:147], a[64:67], a[96:99], v[144:147]\n"
"v_mul_f32 v12, v12, v72\n"
"v_mul_f32 v13, v13, v73\n"
"v_mfma_f32_16x16x32_bf16 v[144:147], a[68:71], a[100:103], v[144:147]\n"
"global_load_dwordx4 a[48:51], v0, s[34:35] offset:0 sc1\n"
"v_mul_f32 v14, v14, v74\n"
"v_mfma_f32_16x16x32_bf16 v[172:175], a[76:79], a[100:103], v[172:175]\n"
"v_mul_f32 v15, v15, v75\n"
"v_cvt_pk_bf16_f32 v26, v12, v13\n"
"v_mfma_f32_16x16x32_bf16 v[172:175], a[72:75], a[96:99], v[172:175]\n"
"v_cvt_pk_bf16_f32 v27, v14, v15\n"
"v_mfma_f32_16x16x32_bf16 v[176:179], a[72:75], a[104:107], v[176:179]\n"
"ds_write_b64 v2, v[26:27] offset:6152\n"
"v_mfma_f32_16x16x32_bf16 v[176:179], a[76:79], a[108:111], v[176:179]\n"
"s_waitcnt lgkmcnt(8)\n"
"v_lshlrev_b32 v12, 16, v20\n"
"v_and_b32 v13, s63, v20\n"
"v_mfma_f32_16x16x32_bf16 v[148:151], a[68:71], a[108:111], v[148:151]\n"
"v_lshlrev_b32 v14, 16, v21\n"
"v_and_b32 v15, s63, v21\n"
"v_mfma_f32_16x16x32_bf16 v[148:151], a[64:67], a[104:107], v[148:151]\n"
"v_mul_f32 v12, v12, v100\n"
"v_mul_f32 v13, v13, v101\n"
"v_mfma_f32_16x16x32_bf16 v[152:155], a[64:67], a[112:115], v[152:155]\n"
"v_mul_f32 v14, v14, v102\n"
"v_mul_f32 v15, v15, v103\n"
"v_mfma_f32_16x16x32_bf16 v[152:155], a[68:71], a[116:119], v[152:155]\n"
"global_load_dwordx4 a[52:55], v0, s[34:35] offset:1024 sc1\n"
"v_cvt_pk_bf16_f32 v28, v12, v13\n"
"v_mfma_f32_16x16x32_bf16 v[180:183], a[76:79], a[116:119], v[180:183]\n"
"v_cvt_pk_bf16_f32 v29, v14, v15\n"
"v_lshlrev_b32 v12, 16, v22\n"
"v_mfma_f32_16x16x32_bf16 v[180:183], a[72:75], a[112:115], v[180:183]\n"
"v_and_b32 v13, s63, v22\n"
"v_lshlrev_b32 v14, 16, v23\n"
"v_mfma_f32_16x16x32_bf16 v[184:187], a[72:75], a[120:123], v[184:187]\n"
"v_and_b32 v15, s63, v23\n"
"v_mul_f32 v12, v12, v128\n"
"v_mfma_f32_16x16x32_bf16 v[184:187], a[76:79], a[124:127], v[184:187]\n"
"v_mul_f32 v13, v13, v129\n"
"v_mfma_f32_16x16x32_bf16 v[156:159], a[68:71], a[124:127], v[156:159]\n"
"v_mul_f32 v14, v14, v130\n"
"v_mfma_f32_16x16x32_bf16 v[156:159], a[64:67], a[120:123], v[156:159]\n"
"s_waitcnt lgkmcnt(6)\n"
"v_mul_f32 v15, v15, v131\n"
"v_cvt_pk_bf16_f32 v30, v12, v13\n"
"v_mfma_f32_16x16x32_bf16 v[160:163], a[64:67], a[128:131], v[160:163]\n"
"v_cvt_pk_bf16_f32 v31, v14, v15\n"
"v_mfma_f32_16x16x32_bf16 v[160:163], a[68:71], a[132:135], v[160:163]\n"
"global_load_dwordx4 a[56:59], v0, s[34:35] offset:2048 sc1\n"
"v_mfma_f32_16x16x32_bf16 v[188:191], a[76:79], a[132:135], v[188:191]\n"
"global_store_dwordx4 v0, v[28:31], s[52:53] offset:3072 sc1\n"
"ds_write_b128 v2, v[28:31] offset:7168\n"
"v_mfma_f32_16x16x32_bf16 v[188:191], a[72:75], a[128:131], v[188:191]\n"
"v_accvgpr_read_b32 v16, a160\n"
"v_accvgpr_read_b32 v17, a161\n"
"v_mfma_f32_16x16x32_bf16 v[192:195], a[72:75], a[136:139], v[192:195]\n"
"v_accvgpr_read_b32 v18, a162\n"
"v_mfma_f32_16x16x32_bf16 v[192:195], a[76:79], a[140:143], v[192:195]\n"
"v_accvgpr_read_b32 v19, a163\n"
"v_lshlrev_b32 v12, 16, v16\n"
"v_mfma_f32_16x16x32_bf16 v[164:167], a[68:71], a[140:143], v[164:167]\n"
"v_and_b32 v13, s63, v16\n"
"v_mfma_f32_16x16x32_bf16 v[164:167], a[64:67], a[136:139], v[164:167]\n"
"s_waitcnt lgkmcnt(2)\n"
"v_lshlrev_b32 v14, 16, v17\n"
"v_mfma_f32_16x16x32_bf16 v[168:171], a[64:67], a[144:147], v[168:171]\n"
"v_and_b32 v15, s63, v17\n"
"v_mul_f32 v12, v12, v48\n"
"v_mfma_f32_16x16x32_bf16 v[168:171], a[68:71], a[148:151], v[168:171]\n"
"global_load_dwordx4 a[60:63], v0, s[34:35] offset:3072 sc1\n"
"v_mul_f32 v13, v13, v49\n"
"v_mfma_f32_16x16x32_bf16 v[196:199], a[76:79], a[148:151], v[196:199]\n"
"v_mul_f32 v14, v14, v50\n"
"v_mfma_f32_16x16x32_bf16 v[196:199], a[72:75], a[144:147], v[196:199]\n"
"v_mul_f32 v15, v15, v51\n"
"v_cvt_pk_bf16_f32 v24, v12, v13\n"
"v_mfma_f32_16x16x32_bf16 a[20:23], a[72:75], a[152:155], a[20:23]\n"
"s_add_u32 s34, s34, 8192\n"
"v_cvt_pk_bf16_f32 v25, v14, v15\n"
"v_mfma_f32_16x16x32_bf16 a[20:23], a[76:79], a[156:159], a[20:23]\n"
"v_lshlrev_b32 v12, 16, v18\n"
"v_mfma_f32_16x16x32_bf16 a[16:19], a[68:71], a[156:159], a[16:19]\n"
"v_and_b32 v13, s63, v18\n"
"v_lshlrev_b32 v14, 16, v19\n"
"v_mfma_f32_16x16x32_bf16 a[16:19], a[64:67], a[152:155], a[16:19]\n"
"s_waitcnt vmcnt(11)\n"
"v_and_b32 v15, s63, v19\n"
"v_mul_f32 v12, v12, v76\n"
"v_mfma_f32_16x16x32_bf16 v[200:203], a[80:83], a[96:99], v[200:203]\n"
"v_mul_f32 v13, v13, v77\n"
"v_mul_f32 v14, v14, v78\n"
"v_mfma_f32_16x16x32_bf16 v[200:203], a[84:87], a[100:103], v[200:203]\n"
"global_load_dwordx4 a[64:67], v0, s[34:35] offset:-4096 sc1\n"
"v_mul_f32 v15, v15, v79\n"
"v_mfma_f32_16x16x32_bf16 v[228:231], a[92:95], a[100:103], v[228:231]\n"
"ds_read_b128 a[100:103], v4 offset:17408\n"
"v_cvt_pk_bf16_f32 v26, v12, v13\n"
"v_mfma_f32_16x16x32_bf16 v[228:231], a[88:91], a[96:99], v[228:231]\n"
"ds_read_b128 a[96:99], v4 offset:16384\n"
"v_cvt_pk_bf16_f32 v27, v14, v15\n"
"v_mfma_f32_16x16x32_bf16 v[232:235], a[88:91], a[104:107], v[232:235]\n"
"global_store_dwordx4 v0, v[24:27], s[54:55] offset:-4096 sc1\n"
"ds_write_b128 v2, v[24:27] offset:8192\n"
"v_mfma_f32_16x16x32_bf16 v[232:235], a[92:95], a[108:111], v[232:235]\n"
"v_accvgpr_read_b32 v16, a164\n"
"v_mfma_f32_16x16x32_bf16 v[204:207], a[84:87], a[108:111], v[204:207]\n"
"ds_read_b128 a[108:111], v4 offset:19456\n"
"v_accvgpr_read_b32 v17, a165\n"
"v_mfma_f32_16x16x32_bf16 v[204:207], a[80:83], a[104:107], v[204:207]\n"
"ds_read_b128 a[104:107], v4 offset:18432\n"
"v_accvgpr_read_b32 v18, a166\n"
"v_mfma_f32_16x16x32_bf16 v[208:211], a[80:83], a[112:115], v[208:211]\n"
"v_accvgpr_read_b32 v19, a167\n"
"v_lshlrev_b32 v12, 16, v16\n"
"v_mfma_f32_16x16x32_bf16 v[208:211], a[84:87], a[116:119], v[208:211]\n"
"global_load_dwordx4 a[68:71], v0, s[34:35] offset:-3072 sc1\n"
"v_and_b32 v13, s63, v16\n"
"v_mfma_f32_16x16x32_bf16 v[236:239], a[92:95], a[116:119], v[236:239]\n"
"ds_read_b128 a[116:119], v4 offset:21504\n"
"v_lshlrev_b32 v14, 16, v17\n"
"v_mfma_f32_16x16x32_bf16 v[236:239], a[88:91], a[112:115], v[236:239]\n"
"ds_read_b128 a[112:115], v4 offset:20480\n"
"v_and_b32 v15, s63, v17\n"
"v_mfma_f32_16x16x32_bf16 v[240:243], a[88:91], a[120:123], v[240:243]\n"
"v_mul_f32 v12, v12, v104\n"
"v_mul_f32 v13, v13, v105\n"
"v_mfma_f32_16x16x32_bf16 v[240:243], a[92:95], a[124:127], v[240:243]\n"
"v_mul_f32 v14, v14, v106\n"
"v_mul_f32 v15, v15, v107\n"
"v_mfma_f32_16x16x32_bf16 v[212:215], a[84:87], a[124:127], v[212:215]\n"
"ds_read_b128 a[124:127], v4 offset:23552\n"
"v_cvt_pk_bf16_f32 v28, v12, v13\n"
"v_mfma_f32_16x16x32_bf16 v[212:215], a[80:83], a[120:123], v[212:215]\n"
"ds_read_b128 a[120:123], v4 offset:22528\n"
"v_cvt_pk_bf16_f32 v29, v14, v15\n"
"v_mfma_f32_16x16x32_bf16 v[216:219], a[80:83], a[128:131], v[216:219]\n"
"ds_write_b64 v2, v[28:29] offset:9216\n"
"v_mfma_f32_16x16x32_bf16 v[216:219], a[84:87], a[132:135], v[216:219]\n"
"global_load_dwordx4 a[72:75], v0, s[34:35] offset:-2048 sc1\n"
"v_lshlrev_b32 v12, 16, v18\n"
"v_mfma_f32_16x16x32_bf16 v[244:247], a[92:95], a[132:135], v[244:247]\n"
"ds_read_b128 a[132:135], v4 offset:25600\n"
"v_and_b32 v13, s63, v18\n"
"v_mfma_f32_16x16x32_bf16 v[244:247], a[88:91], a[128:131], v[244:247]\n"
"ds_read_b128 a[128:131], v4 offset:24576\n"
"v_lshlrev_b32 v14, 16, v19\n"
"v_mfma_f32_16x16x32_bf16 v[248:251], a[88:91], a[136:139], v[248:251]\n"
"v_and_b32 v15, s63, v19\n"
"v_mul_f32 v12, v12, v132\n"
"v_mfma_f32_16x16x32_bf16 v[248:251], a[92:95], a[140:143], v[248:251]\n"
"v_mul_f32 v13, v13, v133\n"
"v_mfma_f32_16x16x32_bf16 v[220:223], a[84:87], a[140:143], v[220:223]\n"
"ds_read_b128 a[140:143], v4 offset:27648\n"
"v_mul_f32 v14, v14, v134\n"
"v_mfma_f32_16x16x32_bf16 v[220:223], a[80:83], a[136:139], v[220:223]\n"
"ds_read_b128 a[136:139], v4 offset:26624\n"
"v_mul_f32 v15, v15, v135\n"
"v_mfma_f32_16x16x32_bf16 v[224:227], a[80:83], a[144:147], v[224:227]\n"
"v_cvt_pk_bf16_f32 v30, v12, v13\n"
"v_cvt_pk_bf16_f32 v31, v14, v15\n"
"v_mfma_f32_16x16x32_bf16 v[224:227], a[84:87], a[148:151], v[224:227]\n"
"global_load_dwordx4 a[76:79], v0, s[34:35] offset:-1024 sc1\n"
"v_mfma_f32_16x16x32_bf16 v[252:255], a[92:95], a[148:151], v[252:255]\n"
"ds_read_b128 a[148:151], v4 offset:29696\n"
"v_mfma_f32_16x16x32_bf16 v[252:255], a[88:91], a[144:147], v[252:255]\n"
"ds_read_b128 a[144:147], v4 offset:28672\n"
"v_mfma_f32_16x16x32_bf16 a[28:31], a[88:91], a[152:155], a[28:31]\n"
"ds_write_b64 v2, v[30:31] offset:9224\n"
"v_mfma_f32_16x16x32_bf16 a[28:31], a[92:95], a[156:159], a[28:31]\n"
"v_accvgpr_read_b32 v16, a168\n"
"v_accvgpr_read_b32 v17, a169\n"
"v_mfma_f32_16x16x32_bf16 a[24:27], a[84:87], a[156:159], a[24:27]\n"
"ds_read_b128 a[156:159], v4 offset:31744\n"
"v_accvgpr_read_b32 v18, a170\n"
"v_mfma_f32_16x16x32_bf16 a[24:27], a[80:83], a[152:155], a[24:27]\n"
"s_waitcnt vmcnt(10) lgkmcnt(13)\n"
"ds_read_b128 a[152:155], v4 offset:30720\n"
"v_accvgpr_read_b32 v19, a171\n"
"v_mfma_f32_16x16x32_bf16 v[144:147], a[32:35], a[96:99], v[144:147]\n"
"v_lshlrev_b32 v12, 16, v16\n"
"v_and_b32 v13, s63, v16\n"
"v_mfma_f32_16x16x32_bf16 v[144:147], a[36:39], a[100:103], v[144:147]\n"
"global_load_dwordx4 a[80:83], v0, s[34:35] offset:0 sc1\n"
"v_lshlrev_b32 v14, 16, v17\n"
"v_mfma_f32_16x16x32_bf16 v[172:175], a[44:47], a[100:103], v[172:175]\n"
"v_and_b32 v15, s63, v17\n"
"v_mul_f32 v12, v12, v52\n"
"v_mfma_f32_16x16x32_bf16 v[172:175], a[40:43], a[96:99], v[172:175]\n"
"v_mul_f32 v13, v13, v53\n"
"v_mul_f32 v14, v14, v54\n"
"v_mfma_f32_16x16x32_bf16 v[176:179], a[40:43], a[104:107], v[176:179]\n"
"v_mul_f32 v15, v15, v55\n"
"v_cvt_pk_bf16_f32 v24, v12, v13\n"
"v_mfma_f32_16x16x32_bf16 v[176:179], a[44:47], a[108:111], v[176:179]\n"
"v_cvt_pk_bf16_f32 v25, v14, v15\n"
"v_lshlrev_b32 v12, 16, v18\n"
"v_mfma_f32_16x16x32_bf16 v[148:151], a[36:39], a[108:111], v[148:151]\n"
"v_and_b32 v13, s63, v18\n"
"v_lshlrev_b32 v14, 16, v19\n"
"v_mfma_f32_16x16x32_bf16 v[148:151], a[32:35], a[104:107], v[148:151]\n"
"s_waitcnt lgkmcnt(10)\n"
"v_and_b32 v15, s63, v19\n"
"v_mul_f32 v12, v12, v80\n"
"v_mfma_f32_16x16x32_bf16 v[152:155], a[32:35], a[112:115], v[152:155]\n"
"v_mul_f32 v13, v13, v81\n"
"v_mul_f32 v14, v14, v82\n"
"v_mfma_f32_16x16x32_bf16 v[152:155], a[36:39], a[116:119], v[152:155]\n"
"global_load_dwordx4 a[84:87], v0, s[34:35] offset:1024 sc1\n"
"v_mul_f32 v15, v15, v83\n"
"v_mfma_f32_16x16x32_bf16 v[180:183], a[44:47], a[116:119], v[180:183]\n"
"v_cvt_pk_bf16_f32 v26, v12, v13\n"
"v_mfma_f32_16x16x32_bf16 v[180:183], a[40:43], a[112:115], v[180:183]\n"
"v_cvt_pk_bf16_f32 v27, v14, v15\n"
"v_mfma_f32_16x16x32_bf16 v[184:187], a[40:43], a[120:123], v[184:187]\n"
"global_store_dwordx4 v0, v[24:27], s[54:55] offset:-2048 sc1\n"
"ds_write_b128 v2, v[24:27] offset:10240\n"
"v_mfma_f32_16x16x32_bf16 v[184:187], a[44:47], a[124:127], v[184:187]\n"
"v_accvgpr_read_b32 v16, a172\n"
"v_mfma_f32_16x16x32_bf16 v[156:159], a[36:39], a[124:127], v[156:159]\n"
"v_accvgpr_read_b32 v17, a173\n"
"v_mfma_f32_16x16x32_bf16 v[156:159], a[32:35], a[120:123], v[156:159]\n"
"s_waitcnt lgkmcnt(6)\n"
"v_accvgpr_read_b32 v18, a174\n"
"v_accvgpr_read_b32 v19, a175\n"
"v_mfma_f32_16x16x32_bf16 v[160:163], a[32:35], a[128:131], v[160:163]\n"
"v_lshlrev_b32 v12, 16, v16\n"
"v_mfma_f32_16x16x32_bf16 v[160:163], a[36:39], a[132:135], v[160:163]\n"
"global_load_dwordx4 a[88:91], v0, s[34:35] offset:2048 sc1\n"
"v_and_b32 v13, s63, v16\n"
"v_mfma_f32_16x16x32_bf16 v[188:191], a[44:47], a[132:135], v[188:191]\n"
"v_lshlrev_b32 v14, 16, v17\n"
"v_and_b32 v15, s63, v17\n"
"v_mfma_f32_16x16x32_bf16 v[188:191], a[40:43], a[128:131], v[188:191]\n"
"v_mul_f32 v12, v12, v108\n"
"v_mfma_f32_16x16x32_bf16 v[192:195], a[40:43], a[136:139], v[192:195]\n"
"v_mul_f32 v13, v13, v109\n"
"v_mfma_f32_16x16x32_bf16 v[192:195], a[44:47], a[140:143], v[192:195]\n"
"v_mul_f32 v14, v14, v110\n"
"v_mul_f32 v15, v15, v111\n"
"v_mfma_f32_16x16x32_bf16 v[164:167], a[36:39], a[140:143], v[164:167]\n"
"v_cvt_pk_bf16_f32 v28, v12, v13\n"
"v_mfma_f32_16x16x32_bf16 v[164:167], a[32:35], a[136:139], v[164:167]\n"
"s_waitcnt lgkmcnt(1)\n"
"v_cvt_pk_bf16_f32 v29, v14, v15\n"
"v_mfma_f32_16x16x32_bf16 v[168:171], a[32:35], a[144:147], v[168:171]\n"
"v_lshlrev_b32 v12, 16, v18\n"
"v_and_b32 v13, s63, v18\n"
"v_mfma_f32_16x16x32_bf16 v[168:171], a[36:39], a[148:151], v[168:171]\n"
"global_load_dwordx4 a[92:95], v0, s[34:35] offset:3072 sc1\n"
"v_lshlrev_b32 v14, 16, v19\n"
"v_mfma_f32_16x16x32_bf16 v[196:199], a[44:47], a[148:151], v[196:199]\n"
"v_and_b32 v15, s63, v19\n"
"v_mfma_f32_16x16x32_bf16 v[196:199], a[40:43], a[144:147], v[196:199]\n"
"v_mul_f32 v12, v12, v136\n"
"v_mul_f32 v13, v13, v137\n"
"v_mfma_f32_16x16x32_bf16 a[20:23], a[40:43], a[152:155], a[20:23]\n"
"s_add_u32 s34, s34, 8192\n"
"v_mul_f32 v14, v14, v138\n"
"v_mfma_f32_16x16x32_bf16 a[20:23], a[44:47], a[156:159], a[20:23]\n"
"v_mul_f32 v15, v15, v139\n"
"v_mfma_f32_16x16x32_bf16 a[16:19], a[36:39], a[156:159], a[16:19]\n"
"v_cvt_pk_bf16_f32 v30, v12, v13\n"
"v_cvt_pk_bf16_f32 v31, v14, v15\n"
"v_mfma_f32_16x16x32_bf16 a[16:19], a[32:35], a[152:155], a[16:19]\n"
"s_waitcnt vmcnt(10)\n"
"global_store_dwordx4 v0, v[28:31], s[54:55] offset:-1024 sc1\n"
"ds_write_b128 v2, v[28:31] offset:11264\n"
"v_mfma_f32_16x16x32_bf16 v[200:203], a[48:51], a[96:99], v[200:203]\n"
"v_accvgpr_read_b32 v16, a176\n"
"v_mfma_f32_16x16x32_bf16 v[200:203], a[52:55], a[100:103], v[200:203]\n"
"global_load_dwordx4 a[32:35], v0, s[34:35] offset:-4096 sc1\n"
"v_accvgpr_read_b32 v17, a177\n"
"v_mfma_f32_16x16x32_bf16 v[228:231], a[60:63], a[100:103], v[228:231]\n"
"ds_read_b128 a[100:103], v5 offset:17408\n"
"v_accvgpr_read_b32 v18, a178\n"
"v_mfma_f32_16x16x32_bf16 v[228:231], a[56:59], a[96:99], v[228:231]\n"
"ds_read_b128 a[96:99], v5 offset:16384\n"
"v_accvgpr_read_b32 v19, a179\n"
"v_mfma_f32_16x16x32_bf16 v[232:235], a[56:59], a[104:107], v[232:235]\n"
"v_lshlrev_b32 v12, 16, v16\n"
"v_and_b32 v13, s63, v16\n"
"v_mfma_f32_16x16x32_bf16 v[232:235], a[60:63], a[108:111], v[232:235]\n"
"v_lshlrev_b32 v14, 16, v17\n"
"v_mfma_f32_16x16x32_bf16 v[204:207], a[52:55], a[108:111], v[204:207]\n"
"ds_read_b128 a[108:111], v5 offset:19456\n"
"v_and_b32 v15, s63, v17\n"
"v_mfma_f32_16x16x32_bf16 v[204:207], a[48:51], a[104:107], v[204:207]\n"
"ds_read_b128 a[104:107], v5 offset:18432\n"
"v_mul_f32 v12, v12, v56\n"
"v_mfma_f32_16x16x32_bf16 v[208:211], a[48:51], a[112:115], v[208:211]\n"
"v_mul_f32 v13, v13, v57\n"
"v_mul_f32 v14, v14, v58\n"
"v_mfma_f32_16x16x32_bf16 v[208:211], a[52:55], a[116:119], v[208:211]\n"
"global_load_dwordx4 a[36:39], v0, s[34:35] offset:-3072 sc1\n"
"v_mul_f32 v15, v15, v59\n"
"v_mfma_f32_16x16x32_bf16 v[236:239], a[60:63], a[116:119], v[236:239]\n"
"ds_read_b128 a[116:119], v5 offset:21504\n"
"v_cvt_pk_bf16_f32 v24, v12, v13\n"
"v_mfma_f32_16x16x32_bf16 v[236:239], a[56:59], a[112:115], v[236:239]\n"
"ds_read_b128 a[112:115], v5 offset:20480\n"
"v_cvt_pk_bf16_f32 v25, v14, v15\n"
"v_mfma_f32_16x16x32_bf16 v[240:243], a[56:59], a[120:123], v[240:243]\n"
"ds_write_b64 v2, v[24:25] offset:12288\n"
"v_mfma_f32_16x16x32_bf16 v[240:243], a[60:63], a[124:127], v[240:243]\n"
"v_lshlrev_b32 v12, 16, v18\n"
"v_and_b32 v13, s63, v18\n"
"v_mfma_f32_16x16x32_bf16 v[212:215], a[52:55], a[124:127], v[212:215]\n"
"ds_read_b128 a[124:127], v5 offset:23552\n"
"v_lshlrev_b32 v14, 16, v19\n"
"v_mfma_f32_16x16x32_bf16 v[212:215], a[48:51], a[120:123], v[212:215]\n"
"ds_read_b128 a[120:123], v5 offset:22528\n"
"v_and_b32 v15, s63, v19\n"
"v_mfma_f32_16x16x32_bf16 v[216:219], a[48:51], a[128:131], v[216:219]\n"
"v_mul_f32 v12, v12, v84\n"
"v_mul_f32 v13, v13, v85\n"
"v_mfma_f32_16x16x32_bf16 v[216:219], a[52:55], a[132:135], v[216:219]\n"
"global_load_dwordx4 a[40:43], v0, s[34:35] offset:-2048 sc1\n"
"v_mul_f32 v14, v14, v86\n"
"v_mfma_f32_16x16x32_bf16 v[244:247], a[60:63], a[132:135], v[244:247]\n"
"ds_read_b128 a[132:135], v5 offset:25600\n"
"v_mul_f32 v15, v15, v87\n"
"v_mfma_f32_16x16x32_bf16 v[244:247], a[56:59], a[128:131], v[244:247]\n"
"ds_read_b128 a[128:131], v5 offset:24576\n"
"v_cvt_pk_bf16_f32 v26, v12, v13\n"
"v_mfma_f32_16x16x32_bf16 v[248:251], a[56:59], a[136:139], v[248:251]\n"
"v_cvt_pk_bf16_f32 v27, v14, v15\n"
"v_mfma_f32_16x16x32_bf16 v[248:251], a[60:63], a[140:143], v[248:251]\n"
"ds_write_b64 v2, v[26:27] offset:12296\n"
"v_mfma_f32_16x16x32_bf16 v[220:223], a[52:55], a[140:143], v[220:223]\n"
"ds_read_b128 a[140:143], v5 offset:27648\n"
"v_accvgpr_read_b32 v16, a180\n"
"v_mfma_f32_16x16x32_bf16 v[220:223], a[48:51], a[136:139], v[220:223]\n"
"ds_read_b128 a[136:139], v5 offset:26624\n"
"v_accvgpr_read_b32 v17, a181\n"
"v_mfma_f32_16x16x32_bf16 v[224:227], a[48:51], a[144:147], v[224:227]\n"
"v_accvgpr_read_b32 v18, a182\n"
"v_accvgpr_read_b32 v19, a183\n"
"v_mfma_f32_16x16x32_bf16 v[224:227], a[52:55], a[148:151], v[224:227]\n"
"global_load_dwordx4 a[44:47], v0, s[34:35] offset:-1024 sc1\n"
"v_lshlrev_b32 v12, 16, v16\n"
"v_mfma_f32_16x16x32_bf16 v[252:255], a[60:63], a[148:151], v[252:255]\n"
"ds_read_b128 a[148:151], v5 offset:29696\n"
"v_and_b32 v13, s63, v16\n"
"v_mfma_f32_16x16x32_bf16 v[252:255], a[56:59], a[144:147], v[252:255]\n"
"ds_read_b128 a[144:147], v5 offset:28672\n"
"v_lshlrev_b32 v14, 16, v17\n"
"v_mfma_f32_16x16x32_bf16 a[28:31], a[56:59], a[152:155], a[28:31]\n"
"v_and_b32 v15, s63, v17\n"
"v_mul_f32 v12, v12, v112\n"
"v_mfma_f32_16x16x32_bf16 a[28:31], a[60:63], a[156:159], a[28:31]\n"
"v_mul_f32 v13, v13, v113\n"
"v_mul_f32 v14, v14, v114\n"
"v_mfma_f32_16x16x32_bf16 a[24:27], a[52:55], a[156:159], a[24:27]\n"
"ds_read_b128 a[156:159], v5 offset:31744\n"
"v_mul_f32 v15, v15, v115\n"
"v_mfma_f32_16x16x32_bf16 a[24:27], a[48:51], a[152:155], a[24:27]\n"
"s_waitcnt vmcnt(10) lgkmcnt(13)\n"
"ds_read_b128 a[152:155], v5 offset:30720\n"
"v_cvt_pk_bf16_f32 v28, v12, v13\n"
"v_mfma_f32_16x16x32_bf16 v[144:147], a[64:67], a[96:99], v[144:147]\n"
"v_cvt_pk_bf16_f32 v29, v14, v15\n"
"v_lshlrev_b32 v12, 16, v18\n"
"v_mfma_f32_16x16x32_bf16 v[144:147], a[68:71], a[100:103], v[144:147]\n"
"global_load_dwordx4 a[48:51], v0, s[34:35] offset:0 sc1\n"
"v_and_b32 v13, s63, v18\n"
"v_mfma_f32_16x16x32_bf16 v[172:175], a[76:79], a[100:103], v[172:175]\n"
"v_lshlrev_b32 v14, 16, v19\n"
"v_and_b32 v15, s63, v19\n"
"v_mfma_f32_16x16x32_bf16 v[172:175], a[72:75], a[96:99], v[172:175]\n"
"v_mul_f32 v12, v12, v140\n"
"v_mul_f32 v13, v13, v141\n"
"v_mfma_f32_16x16x32_bf16 v[176:179], a[72:75], a[104:107], v[176:179]\n"
"v_mul_f32 v14, v14, v142\n"
"v_mul_f32 v15, v15, v143\n"
"v_mfma_f32_16x16x32_bf16 v[176:179], a[76:79], a[108:111], v[176:179]\n"
"v_cvt_pk_bf16_f32 v30, v12, v13\n"
"v_mfma_f32_16x16x32_bf16 v[148:151], a[68:71], a[108:111], v[148:151]\n"
"v_cvt_pk_bf16_f32 v31, v14, v15\n"
"v_mfma_f32_16x16x32_bf16 v[148:151], a[64:67], a[104:107], v[148:151]\n"
"s_waitcnt lgkmcnt(9)\n"
"global_store_dwordx4 v0, v[28:31], s[54:55] offset:1024 sc1\n"
"ds_write_b128 v2, v[28:31] offset:13312\n"
"v_mfma_f32_16x16x32_bf16 v[152:155], a[64:67], a[112:115], v[152:155]\n"
"v_accvgpr_read_b32 v16, a184\n"
"v_accvgpr_read_b32 v17, a185\n"
"v_mfma_f32_16x16x32_bf16 v[152:155], a[68:71], a[116:119], v[152:155]\n"
"global_load_dwordx4 a[52:55], v0, s[34:35] offset:1024 sc1\n"
"v_accvgpr_read_b32 v18, a186\n"
"v_mfma_f32_16x16x32_bf16 v[180:183], a[76:79], a[116:119], v[180:183]\n"
"v_accvgpr_read_b32 v19, a187\n"
"v_mfma_f32_16x16x32_bf16 v[180:183], a[72:75], a[112:115], v[180:183]\n"
"v_lshlrev_b32 v12, 16, v16\n"
"v_mfma_f32_16x16x32_bf16 v[184:187], a[72:75], a[120:123], v[184:187]\n"
"v_and_b32 v13, s63, v16\n"
"v_lshlrev_b32 v14, 16, v17\n"
"v_mfma_f32_16x16x32_bf16 v[184:187], a[76:79], a[124:127], v[184:187]\n"
"v_and_b32 v15, s63, v17\n"
"v_mfma_f32_16x16x32_bf16 v[156:159], a[68:71], a[124:127], v[156:159]\n"
"v_accvgpr_read_b32 v10, a0\n"
"v_mfma_f32_16x16x32_bf16 v[156:159], a[64:67], a[120:123], v[156:159]\n"
"s_waitcnt lgkmcnt(5)\n"
"v_accvgpr_read_b32 v11, a1\n"
"v_mul_f32 v12, v12, v10\n"
"v_mfma_f32_16x16x32_bf16 v[160:163], a[64:67], a[128:131], v[160:163]\n"
"v_mul_f32 v13, v13, v11\n"
"v_mfma_f32_16x16x32_bf16 v[160:163], a[68:71], a[132:135], v[160:163]\n"
"global_load_dwordx4 a[56:59], v0, s[34:35] offset:2048 sc1\n"
"v_accvgpr_read_b32 v10, a2\n"
"v_mfma_f32_16x16x32_bf16 v[188:191], a[76:79], a[132:135], v[188:191]\n"
"v_accvgpr_read_b32 v11, a3\n"
"v_mul_f32 v14, v14, v10\n"
"v_mfma_f32_16x16x32_bf16 v[188:191], a[72:75], a[128:131], v[188:191]\n"
"v_mul_f32 v15, v15, v11\n"
"v_mfma_f32_16x16x32_bf16 v[192:195], a[72:75], a[136:139], v[192:195]\n"
"v_cvt_pk_bf16_f32 v24, v12, v13\n"
"v_mfma_f32_16x16x32_bf16 v[192:195], a[76:79], a[140:143], v[192:195]\n"
"v_cvt_pk_bf16_f32 v25, v14, v15\n"
"v_lshlrev_b32 v12, 16, v18\n"
"v_mfma_f32_16x16x32_bf16 v[164:167], a[68:71], a[140:143], v[164:167]\n"
"v_and_b32 v13, s63, v18\n"
"v_mfma_f32_16x16x32_bf16 v[164:167], a[64:67], a[136:139], v[164:167]\n"
"s_waitcnt lgkmcnt(1)\n"
"v_lshlrev_b32 v14, 16, v19\n"
"v_mfma_f32_16x16x32_bf16 v[168:171], a[64:67], a[144:147], v[168:171]\n"
"v_and_b32 v15, s63, v19\n"
"v_accvgpr_read_b32 v10, a4\n"
"v_mfma_f32_16x16x32_bf16 v[168:171], a[68:71], a[148:151], v[168:171]\n"
"global_load_dwordx4 a[60:63], v0, s[34:35] offset:3072 sc1\n"
"v_accvgpr_read_b32 v11, a5\n"
"v_mfma_f32_16x16x32_bf16 v[196:199], a[76:79], a[148:151], v[196:199]\n"
"v_mul_f32 v12, v12, v10\n"
"v_mfma_f32_16x16x32_bf16 v[196:199], a[72:75], a[144:147], v[196:199]\n"
"v_mul_f32 v13, v13, v11\n"
"v_accvgpr_read_b32 v10, a6\n"
"v_mfma_f32_16x16x32_bf16 a[20:23], a[72:75], a[152:155], a[20:23]\n"
"s_add_u32 s34, s34, 8192\n"
"v_accvgpr_read_b32 v11, a7\n"
"v_mfma_f32_16x16x32_bf16 a[20:23], a[76:79], a[156:159], a[20:23]\n"
"v_mul_f32 v14, v14, v10\n"
"v_mul_f32 v15, v15, v11\n"
"v_mfma_f32_16x16x32_bf16 a[16:19], a[68:71], a[156:159], a[16:19]\n"
"v_cvt_pk_bf16_f32 v26, v12, v13\n"
"v_cvt_pk_bf16_f32 v27, v14, v15\n"
"v_mfma_f32_16x16x32_bf16 a[16:19], a[64:67], a[152:155], a[16:19]\n"
"s_waitcnt vmcnt(10)\n"
"global_store_dwordx4 v0, v[24:27], s[54:55] offset:2048 sc1\n"
"ds_write_b128 v2, v[24:27] offset:14336\n"
"v_mfma_f32_16x16x32_bf16 v[200:203], a[80:83], a[96:99], v[200:203]\n"
"v_accvgpr_read_b32 v16, a188\n"
"v_mfma_f32_16x16x32_bf16 v[200:203], a[84:87], a[100:103], v[200:203]\n"
"global_load_dwordx4 a[64:67], v0, s[34:35] offset:-4096 sc1\n"
"v_accvgpr_read_b32 v17, a189\n"
"v_mfma_f32_16x16x32_bf16 v[228:231], a[92:95], a[100:103], v[228:231]\n"
"ds_read_b128 a[100:103], v2 offset:1024\n"
"v_accvgpr_read_b32 v18, a190\n"
"v_mfma_f32_16x16x32_bf16 v[228:231], a[88:91], a[96:99], v[228:231]\n"
"ds_read_b128 a[96:99], v2 offset:0\n"
"v_accvgpr_read_b32 v19, a191\n"
"v_mfma_f32_16x16x32_bf16 v[232:235], a[88:91], a[104:107], v[232:235]\n"
"v_lshlrev_b32 v12, 16, v16\n"
"v_and_b32 v13, s63, v16\n"
"v_mfma_f32_16x16x32_bf16 v[232:235], a[92:95], a[108:111], v[232:235]\n"
"v_lshlrev_b32 v14, 16, v17\n"
"v_and_b32 v15, s63, v17\n"
"v_mfma_f32_16x16x32_bf16 v[204:207], a[84:87], a[108:111], v[204:207]\n"
"ds_read_b128 a[108:111], v2 offset:3072\n"
"v_accvgpr_read_b32 v10, a8\n"
"v_mfma_f32_16x16x32_bf16 v[204:207], a[80:83], a[104:107], v[204:207]\n"
"ds_read_b128 a[104:107], v2 offset:2048\n"
"v_accvgpr_read_b32 v11, a9\n"
"v_mfma_f32_16x16x32_bf16 v[208:211], a[80:83], a[112:115], v[208:211]\n"
"v_mul_f32 v12, v12, v10\n"
"v_mul_f32 v13, v13, v11\n"
"v_mfma_f32_16x16x32_bf16 v[208:211], a[84:87], a[116:119], v[208:211]\n"
"global_load_dwordx4 a[68:71], v0, s[34:35] offset:-3072 sc1\n"
"v_accvgpr_read_b32 v10, a10\n"
"v_mfma_f32_16x16x32_bf16 v[236:239], a[92:95], a[116:119], v[236:239]\n"
"ds_read_b128 a[116:119], v2 offset:5120\n"
"v_accvgpr_read_b32 v11, a11\n"
"v_mfma_f32_16x16x32_bf16 v[236:239], a[88:91], a[112:115], v[236:239]\n"
"ds_read_b128 a[112:115], v2 offset:4096\n"
"v_mul_f32 v14, v14, v10\n"
"v_mfma_f32_16x16x32_bf16 v[240:243], a[88:91], a[120:123], v[240:243]\n"
"v_mul_f32 v15, v15, v11\n"
"v_cvt_pk_bf16_f32 v28, v12, v13\n"
"v_mfma_f32_16x16x32_bf16 v[240:243], a[92:95], a[124:127], v[240:243]\n"
"v_cvt_pk_bf16_f32 v29, v14, v15\n"
"v_lshlrev_b32 v12, 16, v18\n"
"v_mfma_f32_16x16x32_bf16 v[212:215], a[84:87], a[124:127], v[212:215]\n"
"ds_read_b128 a[124:127], v2 offset:7168\n"
"v_and_b32 v13, s63, v18\n"
"v_mfma_f32_16x16x32_bf16 v[212:215], a[80:83], a[120:123], v[212:215]\n"
"ds_read_b128 a[120:123], v2 offset:6144\n"
"v_lshlrev_b32 v14, 16, v19\n"
"v_mfma_f32_16x16x32_bf16 v[216:219], a[80:83], a[128:131], v[216:219]\n"
"v_and_b32 v15, s63, v19\n"
"v_accvgpr_read_b32 v10, a12\n"
"v_mfma_f32_16x16x32_bf16 v[216:219], a[84:87], a[132:135], v[216:219]\n"
"global_load_dwordx4 a[72:75], v0, s[34:35] offset:-2048 sc1\n"
"v_accvgpr_read_b32 v11, a13\n"
"v_mfma_f32_16x16x32_bf16 v[244:247], a[92:95], a[132:135], v[244:247]\n"
"ds_read_b128 a[132:135], v2 offset:9216\n"
"v_mul_f32 v12, v12, v10\n"
"v_mfma_f32_16x16x32_bf16 v[244:247], a[88:91], a[128:131], v[244:247]\n"
"ds_read_b128 a[128:131], v2 offset:8192\n"
"v_mul_f32 v13, v13, v11\n"
"v_mfma_f32_16x16x32_bf16 v[248:251], a[88:91], a[136:139], v[248:251]\n"
"v_accvgpr_read_b32 v10, a14\n"
"v_accvgpr_read_b32 v11, a15\n"
"v_mfma_f32_16x16x32_bf16 v[248:251], a[92:95], a[140:143], v[248:251]\n"
"v_mul_f32 v14, v14, v10\n"
"v_mul_f32 v15, v15, v11\n"
"v_mfma_f32_16x16x32_bf16 v[220:223], a[84:87], a[140:143], v[220:223]\n"
"ds_read_b128 a[140:143], v2 offset:11264\n"
"v_cvt_pk_bf16_f32 v30, v12, v13\n"
"v_mfma_f32_16x16x32_bf16 v[220:223], a[80:83], a[136:139], v[220:223]\n"
"ds_read_b128 a[136:139], v2 offset:10240\n"
"v_cvt_pk_bf16_f32 v31, v14, v15\n"
"v_mfma_f32_16x16x32_bf16 v[224:227], a[80:83], a[144:147], v[224:227]\n"
"global_store_dwordx4 v0, v[28:31], s[54:55] offset:3072 sc1\n"
"ds_write_b128 v2, v[28:31] offset:15360\n"
"v_mfma_f32_16x16x32_bf16 v[224:227], a[84:87], a[148:151], v[224:227]\n"
"global_load_dwordx4 a[76:79], v0, s[34:35] offset:-1024 sc1\n"
"v_mfma_f32_16x16x32_bf16 v[252:255], a[92:95], a[148:151], v[252:255]\n"
"ds_read_b128 a[148:151], v2 offset:13312\n"
"v_mfma_f32_16x16x32_bf16 v[252:255], a[88:91], a[144:147], v[252:255]\n"
"ds_read_b128 a[144:147], v2 offset:12288\n"
"v_mfma_f32_16x16x32_bf16 a[28:31], a[88:91], a[152:155], a[28:31]\n"
"s_add_u32 s62, s62, 1\n"
"v_mfma_f32_16x16x32_bf16 a[28:31], a[92:95], a[156:159], a[28:31]\n"
"v_mfma_f32_16x16x32_bf16 a[24:27], a[84:87], a[156:159], a[24:27]\n"
"ds_read_b128 a[156:159], v2 offset:15360\n"
"v_mfma_f32_16x16x32_bf16 a[24:27], a[80:83], a[152:155], a[24:27]\n"
"ds_read_b128 a[152:155], v2 offset:14336\n"
"L_layer:\n"
"s_waitcnt vmcnt(11) lgkmcnt(13)\n"
"v_mfma_f32_16x16x32_bf16 v[32:35], a[32:35], a[96:99], v[32:35]\n"
"s_mov_b64 s[96:97], s[52:53]\n"
"v_accvgpr_read_b32 v16, a192\n"
"v_accvgpr_read_b32 v17, a193\n"
"v_mfma_f32_16x16x32_bf16 v[32:35], a[36:39], a[100:103], v[32:35]\n"
"global_load_dwordx4 a[80:83], v0, s[34:35] offset:0 sc1\n"
"v_accvgpr_read_b32 v18, a194\n"
"v_mfma_f32_16x16x32_bf16 v[60:63], a[44:47], a[100:103], v[60:63]\n"
"s_mov_b64 s[98:99], s[54:55]\n"
"v_accvgpr_read_b32 v19, a195\n"
"v_mfma_f32_16x16x32_bf16 v[60:63], a[40:43], a[96:99], v[60:63]\n"
"v_lshlrev_b32 v12, 16, v16\n"
"v_and_b32 v13, s63, v16\n"
"v_mfma_f32_16x16x32_bf16 v[64:67], a[40:43], a[104:107], v[64:67]\n"
"s_add_u32 s52, s52, 131072\n"
"s_addc_u32 s53, s53, 0\n"
"v_lshlrev_b32 v14, 16, v17\n"
"v_mfma_f32_16x16x32_bf16 v[64:67], a[44:47], a[108:111], v[64:67]\n"
"v_and_b32 v15, s63, v17\n"
"v_mul_f32 v12, v12, v144\n"
"v_mfma_f32_16x16x32_bf16 v[36:39], a[36:39], a[108:111], v[36:39]\n"
"s_add_u32 s54, s54, 131072\n"
"s_addc_u32 s55, s55, 0\n"
"v_mul_f32 v13, v13, v145\n"
"v_mfma_f32_16x16x32_bf16 v[36:39], a[32:35], a[104:107], v[36:39]\n"
"s_waitcnt lgkmcnt(9)\n"
"v_mul_f32 v14, v14, v146\n"
"v_mul_f32 v15, v15, v147\n"
"v_mfma_f32_16x16x32_bf16 v[40:43], a[32:35], a[112:115], v[40:43]\n"
"ds_read_b128 v[20:23], v2 offset:0\n"
"v_cvt_pk_bf16_f32 v24, v12, v13\n"
"v_mfma_f32_16x16x32_bf16 v[40:43], a[36:39], a[116:119], v[40:43]\n"
"global_load_dwordx4 a[84:87], v0, s[34:35] offset:1024 sc1\n"
"v_cvt_pk_bf16_f32 v25, v14, v15\n"
"v_mfma_f32_16x16x32_bf16 v[68:71], a[44:47], a[116:119], v[68:71]\n"
"ds_write_b64 v2, v[24:25] offset:16384\n"
"v_mfma_f32_16x16x32_bf16 v[68:71], a[40:43], a[112:115], v[68:71]\n"
"v_lshlrev_b32 v12, 16, v18\n"
"v_and_b32 v13, s63, v18\n"
"v_mfma_f32_16x16x32_bf16 v[72:75], a[40:43], a[120:123], v[72:75]\n"
"s_waitcnt lgkmcnt(6)\n"
"s_barrier\n"
"v_mfma_f32_16x16x32_bf16 v[72:75], a[44:47], a[124:127], v[72:75]\n"
"v_lshlrev_b32 v14, 16, v19\n"
"v_and_b32 v15, s63, v19\n"
"v_mfma_f32_16x16x32_bf16 v[44:47], a[36:39], a[124:127], v[44:47]\n"
"v_mul_f32 v12, v12, v172\n"
"v_mul_f32 v13, v13, v173\n"
"v_mfma_f32_16x16x32_bf16 v[44:47], a[32:35], a[120:123], v[44:47]\n"
"v_mul_f32 v14, v14, v174\n"
"v_mfma_f32_16x16x32_bf16 v[48:51], a[32:35], a[128:131], v[48:51]\n"
"v_mul_f32 v15, v15, v175\n"
"v_mfma_f32_16x16x32_bf16 v[48:51], a[36:39], a[132:135], v[48:51]\n"
"global_load_dwordx4 a[88:91], v0, s[34:35] offset:2048 sc1\n"
"v_mfma_f32_16x16x32_bf16 v[76:79], a[44:47], a[132:135], v[76:79]\n"
"v_cvt_pk_bf16_f32 v26, v12, v13\n"
"v_mfma_f32_16x16x32_bf16 v[76:79], a[40:43], a[128:131], v[76:79]\n"
"v_cvt_pk_bf16_f32 v27, v14, v15\n"
"v_mfma_f32_16x16x32_bf16 v[80:83], a[40:43], a[136:139], v[80:83]\n"
"s_waitcnt lgkmcnt(1)\n"
"global_store_dwordx4 v0, v[20:23], s[96:97] offset:-4096 sc1\n"
"ds_write_b64 v2, v[26:27] offset:16392\n"
"v_accvgpr_read_b32 v16, a196\n"
"v_mfma_f32_16x16x32_bf16 v[80:83], a[44:47], a[140:143], v[80:83]\n"
"v_accvgpr_read_b32 v17, a197\n"
"v_accvgpr_read_b32 v18, a198\n"
"v_mfma_f32_16x16x32_bf16 v[52:55], a[36:39], a[140:143], v[52:55]\n"
"v_accvgpr_read_b32 v19, a199\n"
"v_lshlrev_b32 v12, 16, v16\n"
"v_mfma_f32_16x16x32_bf16 v[52:55], a[32:35], a[136:139], v[52:55]\n"
"v_and_b32 v13, s63, v16\n"
"v_lshlrev_b32 v14, 16, v17\n"
"v_mfma_f32_16x16x32_bf16 v[56:59], a[32:35], a[144:147], v[56:59]\n"
"v_and_b32 v15, s63, v17\n"
"v_mul_f32 v12, v12, v200\n"
"v_mfma_f32_16x16x32_bf16 v[56:59], a[36:39], a[148:151], v[56:59]\n"
"global_load_dwordx4 a[92:95], v0, s[34:35] offset:3072 sc1\n"
"v_mul_f32 v13, v13, v201\n"
"v_mfma_f32_16x16x32_bf16 v[84:87], a[44:47], a[148:151], v[84:87]\n"
"v_mul_f32 v14, v14, v202\n"
"v_mul_f32 v15, v15, v203\n"
"v_mfma_f32_16x16x32_bf16 v[84:87], a[40:43], a[144:147], v[84:87]\n"
"v_cvt_pk_bf16_f32 v28, v12, v13\n"
"v_cvt_pk_bf16_f32 v29, v14, v15\n"
"v_mfma_f32_16x16x32_bf16 a[4:7], a[40:43], a[152:155], a[4:7]\n"
"s_add_u32 s34, s34, 8192\n"
"ds_write_b64 v2, v[28:29] offset:17408\n"
"v_mfma_f32_16x16x32_bf16 a[4:7], a[44:47], a[156:159], a[4:7]\n"
"v_lshlrev_b32 v12, 16, v18\n"
"v_mfma_f32_16x16x32_bf16 a[0:3], a[36:39], a[156:159], a[0:3]\n"
"v_and_b32 v13, s63, v18\n"
"v_lshlrev_b32 v14, 16, v19\n"
"v_mfma_f32_16x16x32_bf16 a[0:3], a[32:35], a[152:155], a[0:3]\n"
"s_waitcnt vmcnt(11)\n"
"v_and_b32 v15, s63, v19\n"
"v_mul_f32 v12, v12, v228\n"
"v_mfma_f32_16x16x32_bf16 v[88:91], a[48:51], a[96:99], v[88:91]\n"
"v_mul_f32 v13, v13, v229\n"
"v_mul_f32 v14, v14, v230\n"
"v_mfma_f32_16x16x32_bf16 v[88:91], a[52:55], a[100:103], v[88:91]\n"
"global_load_dwordx4 a[32:35], v0, s[34:35] offset:-4096 sc1\n"
"v_mul_f32 v15, v15, v231\n"
"v_mfma_f32_16x16x32_bf16 v[116:119], a[60:63], a[100:103], v[116:119]\n"
"ds_read_b128 a[100:103], v3 offset:1024\n"
"v_cvt_pk_bf16_f32 v30, v12, v13\n"
"v_mfma_f32_16x16x32_bf16 v[116:119], a[56:59], a[96:99], v[116:119]\n"
"ds_read_b128 a[96:99], v3 offset:0\n"
"v_cvt_pk_bf16_f32 v31, v14, v15\n"
"v_mfma_f32_16x16x32_bf16 v[120:123], a[56:59], a[104:107], v[120:123]\n"
"ds_write_b64 v2, v[30:31] offset:17416\n"
"v_mfma_f32_16x16x32_bf16 v[120:123], a[60:63], a[108:111], v[120:123]\n"
"ds_read_b128 v[20:23], v2 offset:3072\n"
"v_accvgpr_read_b32 v16, a200\n"
"v_mfma_f32_16x16x32_bf16 v[92:95], a[52:55], a[108:111], v[92:95]\n"
"ds_read_b128 a[108:111], v3 offset:3072\n"
"v_accvgpr_read_b32 v17, a201\n"
"v_mfma_f32_16x16x32_bf16 v[92:95], a[48:51], a[104:107], v[92:95]\n"
"ds_read_b128 a[104:107], v3 offset:2048\n"
"v_accvgpr_read_b32 v18, a202\n"
"v_mfma_f32_16x16x32_bf16 v[96:99], a[48:51], a[112:115], v[96:99]\n"
"v_accvgpr_read_b32 v19, a203\n"
"v_lshlrev_b32 v12, 16, v16\n"
"v_mfma_f32_16x16x32_bf16 v[96:99], a[52:55], a[116:119], v[96:99]\n"
"global_load_dwordx4 a[36:39], v0, s[34:35] offset:-3072 sc1\n"
"v_and_b32 v13, s63, v16\n"
"v_mfma_f32_16x16x32_bf16 v[124:127], a[60:63], a[116:119], v[124:127]\n"
"ds_read_b128 a[116:119], v3 offset:5120\n"
"v_lshlrev_b32 v14, 16, v17\n"
"v_mfma_f32_16x16x32_bf16 v[124:127], a[56:59], a[112:115], v[124:127]\n"
"ds_read_b128 a[112:115], v3 offset:4096\n"
"v_and_b32 v15, s63, v17\n"
"v_mfma_f32_16x16x32_bf16 v[128:131], a[56:59], a[120:123], v[128:131]\n"
"v_mul_f32 v12, v12, v148\n"
"v_mul_f32 v13, v13, v149\n"
"v_mfma_f32_16x16x32_bf16 v[128:131], a[60:63], a[124:127], v[128:131]\n"
"v_mul_f32 v14, v14, v150\n"
"v_mul_f32 v15, v15, v151\n"
"v_mfma_f32_16x16x32_bf16 v[100:103], a[52:55], a[124:127], v[100:103]\n"
"ds_read_b128 a[124:127], v3 offset:7168\n"
"v_cvt_pk_bf16_f32 v24, v12, v13\n"
"v_mfma_f32_16x16x32_bf16 v[100:103], a[48:51], a[120:123], v[100:103]\n"
"ds_read_b128 a[120:123], v3 offset:6144\n"
"v_cvt_pk_bf16_f32 v25, v14, v15\n"
"v_mfma_f32_16x16x32_bf16 v[104:107], a[48:51], a[128:131], v[104:107]\n"
"s_waitcnt lgkmcnt(6)\n"
"global_store_dwordx4 v0, v[20:23], s[96:97] offset:-1024 sc1\n"
"ds_write_b64 v2, v[24:25] offset:18432\n"
"v_lshlrev_b32 v12, 16, v18\n"
"v_mfma_f32_16x16x32_bf16 v[104:107], a[52:55], a[132:135], v[104:107]\n"
"global_load_dwordx4 a[40:43], v0, s[34:35] offset:-2048 sc1\n"
"v_and_b32 v13, s63, v18\n"
"v_mfma_f32_16x16x32_bf16 v[132:135], a[60:63], a[132:135], v[132:135]\n"
"ds_read_b128 a[132:135], v3 offset:9216\n"
"v_lshlrev_b32 v14, 16, v19\n"
"v_mfma_f32_16x16x32_bf16 v[132:135], a[56:59], a[128:131], v[132:135]\n"
"ds_read_b128 a[128:131], v3 offset:8192\n"
"v_and_b32 v15, s63, v19\n"
"v_mfma_f32_16x16x32_bf16 v[136:139], a[56:59], a[136:139], v[136:139]\n"
"v_mul_f32 v12, v12, v176\n"
"v_mul_f32 v13, v13, v177\n"
"v_mfma_f32_16x16x32_bf16 v[136:139], a[60:63], a[140:143], v[136:139]\n"
"v_mul_f32 v14, v14, v178\n"
"v_mul_f32 v15, v15, v179\n"
"v_mfma_f32_16x16x32_bf16 v[108:111], a[52:55], a[140:143], v[108:111]\n"
"ds_read_b128 a[140:143], v3 offset:11264\n"
"v_cvt_pk_bf16_f32 v26, v12, v13\n"
"v_mfma_f32_16x16x32_bf16 v[108:111], a[48:51], a[136:139], v[108:111]\n"
"ds_read_b128 a[136:139], v3 offset:10240\n"
"v_cvt_pk_bf16_f32 v27, v14, v15\n"
"v_mfma_f32_16x16x32_bf16 v[112:115], a[48:51], a[144:147], v[112:115]\n"
"ds_write_b64 v2, v[26:27] offset:18440\n"
"v_mfma_f32_16x16x32_bf16 v[112:115], a[52:55], a[148:151], v[112:115]\n"
"global_load_dwordx4 a[44:47], v0, s[34:35] offset:-1024 sc1\n"
"v_accvgpr_read_b32 v16, a204\n"
"v_mfma_f32_16x16x32_bf16 v[140:143], a[60:63], a[148:151], v[140:143]\n"
"ds_read_b128 a[148:151], v3 offset:13312\n"
"v_accvgpr_read_b32 v17, a205\n"
"v_mfma_f32_16x16x32_bf16 v[140:143], a[56:59], a[144:147], v[140:143]\n"
"ds_read_b128 a[144:147], v3 offset:12288\n"
"v_accvgpr_read_b32 v18, a206\n"
"v_mfma_f32_16x16x32_bf16 a[12:15], a[56:59], a[152:155], a[12:15]\n"
"v_accvgpr_read_b32 v19, a207\n"
"v_lshlrev_b32 v12, 16, v16\n"
"v_mfma_f32_16x16x32_bf16 a[12:15], a[60:63], a[156:159], a[12:15]\n"
"v_and_b32 v13, s63, v16\n"
"v_lshlrev_b32 v14, 16, v17\n"
"v_mfma_f32_16x16x32_bf16 a[8:11], a[52:55], a[156:159], a[8:11]\n"
"ds_read_b128 a[156:159], v3 offset:15360\n"
"v_and_b32 v15, s63, v17\n"
"v_mfma_f32_16x16x32_bf16 a[8:11], a[48:51], a[152:155], a[8:11]\n"
"s_waitcnt vmcnt(10) lgkmcnt(13)\n"
"ds_read_b128 a[152:155], v3 offset:14336\n"
"v_mul_f32 v12, v12, v204\n"
"v_mfma_f32_16x16x32_bf16 v[32:35], a[64:67], a[96:99], v[32:35]\n"
"ds_read_b128 v[20:23], v2 offset:6144\n"
"v_mul_f32 v13, v13, v205\n"
"v_mfma_f32_16x16x32_bf16 v[32:35], a[68:71], a[100:103], v[32:35]\n"
"global_load_dwordx4 a[48:51], v0, s[34:35] offset:0 sc1\n"
"v_mul_f32 v14, v14, v206\n"
"v_mfma_f32_16x16x32_bf16 v[60:63], a[76:79], a[100:103], v[60:63]\n"
"v_mul_f32 v15, v15, v207\n"
"v_cvt_pk_bf16_f32 v28, v12, v13\n"
"v_mfma_f32_16x16x32_bf16 v[60:63], a[72:75], a[96:99], v[60:63]\n"
"v_cvt_pk_bf16_f32 v29, v14, v15\n"
"v_mfma_f32_16x16x32_bf16 v[64:67], a[72:75], a[104:107], v[64:67]\n"
"ds_write_b64 v2, v[28:29] offset:19456\n"
"v_mfma_f32_16x16x32_bf16 v[64:67], a[76:79], a[108:111], v[64:67]\n"
"v_lshlrev_b32 v12, 16, v18\n"
"v_and_b32 v13, s63, v18\n"
"v_mfma_f32_16x16x32_bf16 v[36:39], a[68:71], a[108:111], v[36:39]\n"
"v_lshlrev_b32 v14, 16, v19\n"
"v_and_b32 v15, s63, v19\n"
"v_mfma_f32_16x16x32_bf16 v[36:39], a[64:67], a[104:107], v[36:39]\n"
"s_waitcnt lgkmcnt(12)\n"
"v_mul_f32 v12, v12, v232\n"
"v_mul_f32 v13, v13, v233\n"
"v_mfma_f32_16x16x32_bf16 v[40:43], a[64:67], a[112:115], v[40:43]\n"
"v_mul_f32 v14, v14, v234\n"
"v_mfma_f32_16x16x32_bf16 v[40:43], a[68:71], a[116:119], v[40:43]\n"
"global_load_dwordx4 a[52:55], v0, s[34:35] offset:1024 sc1\n"
"v_mul_f32 v15, v15, v235\n"
"v_mfma_f32_16x16x32_bf16 v[68:71], a[76:79], a[116:119], v[68:71]\n"
"v_cvt_pk_bf16_f32 v30, v12, v13\n"
"v_mfma_f32_16x16x32_bf16 v[68:71], a[72:75], a[112:115], v[68:71]\n"
"v_cvt_pk_bf16_f32 v31, v14, v15\n"
"v_mfma_f32_16x16x32_bf16 v[72:75], a[72:75], a[120:123], v[72:75]\n"
"s_waitcnt lgkmcnt(1)\n"
"global_store_dwordx4 v0, v[20:23], s[96:97] offset:2048 sc1\n"
"ds_write_b64 v2, v[30:31] offset:19464\n"
"v_accvgpr_read_b32 v16, a208\n"
"v_mfma_f32_16x16x32_bf16 v[72:75], a[76:79], a[124:127], v[72:75]\n"
"v_accvgpr_read_b32 v17, a209\n"
"v_accvgpr_read_b32 v18, a210\n"
"v_mfma_f32_16x16x32_bf16 v[44:47], a[68:71], a[124:127], v[44:47]\n"
"v_accvgpr_read_b32 v19, a211\n"
"v_mfma_f32_16x16x32_bf16 v[44:47], a[64:67], a[120:123], v[44:47]\n"
"v_lshlrev_b32 v12, 16, v16\n"
"v_and_b32 v13, s63, v16\n"
"v_mfma_f32_16x16x32_bf16 v[48:51], a[64:67], a[128:131], v[48:51]\n"
"v_lshlrev_b32 v14, 16, v17\n"
"v_mfma_f32_16x16x32_bf16 v[48:51], a[68:71], a[132:135], v[48:51]\n"
"global_load_dwordx4 a[56:59], v0, s[34:35] offset:2048 sc1\n"
"v_and_b32 v15, s63, v17\n"
"v_mfma_f32_16x16x32_bf16 v[76:79], a[76:79], a[132:135], v[76:79]\n"
"v_mul_f32 v12, v12, v152\n"
"v_mul_f32 v13, v13, v153\n"
"v_mfma_f32_16x16x32_bf16 v[76:79], a[72:75], a[128:131], v[76:79]\n"
"v_mul_f32 v14, v14, v154\n"
"v_mul_f32 v15, v15, v155\n"
"v_mfma_f32_16x16x32_bf16 v[80:83], a[72:75], a[136:139], v[80:83]\n"
"v_cvt_pk_bf16_f32 v24, v12, v13\n"
"v_cvt_pk_bf16_f32 v25, v14, v15\n"
"v_mfma_f32_16x16x32_bf16 v[80:83], a[76:79], a[140:143], v[80:83]\n"
"ds_write_b64 v2, v[24:25] offset:20480\n"
"v_mfma_f32_16x16x32_bf16 v[52:55], a[68:71], a[140:143], v[52:55]\n"
"v_lshlrev_b32 v12, 16, v18\n"
"v_mfma_f32_16x16x32_bf16 v[52:55], a[64:67], a[136:139], v[52:55]\n"
"v_and_b32 v13, s63, v18\n"
"v_lshlrev_b32 v14, 16, v19\n"
"v_mfma_f32_16x16x32_bf16 v[56:59], a[64:67], a[144:147], v[56:59]\n"
"v_and_b32 v15, s63, v19\n"
"v_mul_f32 v12, v12, v180\n"
"v_mfma_f32_16x16x32_bf16 v[56:59], a[68:71], a[148:151], v[56:59]\n"
"global_load_dwordx4 a[60:63], v0, s[34:35] offset:3072 sc1\n"
"v_mul_f32 v13, v13, v181\n"
"v_mfma_f32_16x16x32_bf16 v[84:87], a[76:79], a[148:151], v[84:87]\n"
"v_mul_f32 v14, v14, v182\n"
"v_mul_f32 v15, v15, v183\n"
"v_mfma_f32_16x16x32_bf16 v[84:87], a[72:75], a[144:147], v[84:87]\n"
"v_cvt_pk_bf16_f32 v26, v12, v13\n"
"v_cvt_pk_bf16_f32 v27, v14, v15\n"
"v_mfma_f32_16x16x32_bf16 a[4:7], a[72:75], a[152:155], a[4:7]\n"
"s_add_u32 s34, s34, 8192\n"
"ds_write_b64 v2, v[26:27] offset:20488\n"
"v_mfma_f32_16x16x32_bf16 a[4:7], a[76:79], a[156:159], a[4:7]\n"
"ds_read_b128 v[20:23], v2 offset:9216\n"
"v_mfma_f32_16x16x32_bf16 a[0:3], a[68:71], a[156:159], a[0:3]\n"
"v_accvgpr_read_b32 v16, a212\n"
"v_mfma_f32_16x16x32_bf16 a[0:3], a[64:67], a[152:155], a[0:3]\n"
"s_waitcnt vmcnt(10)\n"
"v_accvgpr_read_b32 v17, a213\n"
"v_accvgpr_read_b32 v18, a214\n"
"v_mfma_f32_16x16x32_bf16 v[88:91], a[80:83], a[96:99], v[88:91]\n"
"v_accvgpr_read_b32 v19, a215\n"
"v_lshlrev_b32 v12, 16, v16\n"
"v_mfma_f32_16x16x32_bf16 v[88:91], a[84:87], a[100:103], v[88:91]\n"
"global_load_dwordx4 a[64:67], v0, s[34:35] offset:-4096 sc1\n"
"v_and_b32 v13, s63, v16\n"
"v_mfma_f32_16x16x32_bf16 v[116:119], a[92:95], a[100:103], v[116:119]\n"
"ds_read_b128 a[100:103], v4 offset:1024\n"
"v_lshlrev_b32 v14, 16, v17\n"
"v_mfma_f32_16x16x32_bf16 v[116:119], a[88:91], a[96:99], v[116:119]\n"
"ds_read_b128 a[96:99], v4 offset:0\n"
"v_and_b32 v15, s63, v17\n"
"v_mfma_f32_16x16x32_bf16 v[120:123], a[88:91], a[104:107], v[120:123]\n"
"v_mul_f32 v12, v12, v208\n"
"v_mul_f32 v13, v13, v209\n"
"v_mfma_f32_16x16x32_bf16 v[120:123], a[92:95], a[108:111], v[120:123]\n"
"v_mul_f32 v14, v14, v210\n"
"v_mul_f32 v15, v15, v211\n"
"v_mfma_f32_16x16x32_bf16 v[92:95], a[84:87], a[108:111], v[92:95]\n"
"ds_read_b128 a[108:111], v4 offset:3072\n"
"v_cvt_pk_bf16_f32 v28, v12, v13\n"
"v_mfma_f32_16x16x32_bf16 v[92:95], a[80:83], a[104:107], v[92:95]\n"
"ds_read_b128 a[104:107], v4 offset:2048\n"
"v_cvt_pk_bf16_f32 v29, v14, v15\n"
"v_mfma_f32_16x16x32_bf16 v[96:99], a[80:83], a[112:115], v[96:99]\n"
"s_waitcnt lgkmcnt(4)\n"
"global_store_dwordx4 v0, v[20:23], s[98:99] offset:-3072 sc1\n"
"ds_write_b64 v2, v[28:29] offset:21504\n"
"v_lshlrev_b32 v12, 16, v18\n"
"v_mfma_f32_16x16x32_bf16 v[96:99], a[84:87], a[116:119], v[96:99]\n"
"global_load_dwordx4 a[68:71], v0, s[34:35] offset:-3072 sc1\n"
"v_and_b32 v13, s63, v18\n"
"v_mfma_f32_16x16x32_bf16 v[124:127], a[92:95], a[116:119], v[124:127]\n"
"ds_read_b128 a[116:119], v4 offset:5120\n"
"v_lshlrev_b32 v14, 16, v19\n"
"v_mfma_f32_16x16x32_bf16 v[124:127], a[88:91], a[112:115], v[124:127]\n"
"ds_read_b128 a[112:115], v4 offset:4096\n"
"v_and_b32 v15, s63, v19\n"
"v_mfma_f32_16x16x32_bf16 v[128:131], a[88:91], a[120:123], v[128:131]\n"
"v_mul_f32 v12, v12, v236\n"
"v_mul_f32 v13, v13, v237\n"
"v_mfma_f32_16x16x32_bf16 v[128:131], a[92:95], a[124:127], v[128:131]\n"
"v_mul_f32 v14, v14, v238\n"
"v_mul_f32 v15, v15, v239\n"
"v_mfma_f32_16x16x32_bf16 v[100:103], a[84:87], a[124:127], v[100:103]\n"
"ds_read_b128 a[124:127], v4 offset:7168\n"
"v_cvt_pk_bf16_f32 v30, v12, v13\n"
"v_mfma_f32_16x16x32_bf16 v[100:103], a[80:83], a[120:123], v[100:103]\n"
"ds_read_b128 a[120:123], v4 offset:6144\n"
"v_cvt_pk_bf16_f32 v31, v14, v15\n"
"v_mfma_f32_16x16x32_bf16 v[104:107], a[80:83], a[128:131], v[104:107]\n"
"ds_write_b64 v2, v[30:31] offset:21512\n"
"v_mfma_f32_16x16x32_bf16 v[104:107], a[84:87], a[132:135], v[104:107]\n"
"global_load_dwordx4 a[72:75], v0, s[34:35] offset:-2048 sc1\n"
"v_accvgpr_read_b32 v16, a216\n"
"v_mfma_f32_16x16x32_bf16 v[132:135], a[92:95], a[132:135], v[132:135]\n"
"ds_read_b128 a[132:135], v4 offset:9216\n"
"v_accvgpr_read_b32 v17, a217\n"
"v_mfma_f32_16x16x32_bf16 v[132:135], a[88:91], a[128:131], v[132:135]\n"
"ds_read_b128 a[128:131], v4 offset:8192\n"
"v_accvgpr_read_b32 v18, a218\n"
"v_mfma_f32_16x16x32_bf16 v[136:139], a[88:91], a[136:139], v[136:139]\n"
"v_accvgpr_read_b32 v19, a219\n"
"v_mfma_f32_16x16x32_bf16 v[136:139], a[92:95], a[140:143], v[136:139]\n"
"v_lshlrev_b32 v12, 16, v16\n"
"v_and_b32 v13, s63, v16\n"
"v_mfma_f32_16x16x32_bf16 v[108:111], a[84:87], a[140:143], v[108:111]\n"
"ds_read_b128 a[140:143], v4 offset:11264\n"
"v_lshlrev_b32 v14, 16, v17\n"
"v_mfma_f32_16x16x32_bf16 v[108:111], a[80:83], a[136:139], v[108:111]\n"
"ds_read_b128 a[136:139], v4 offset:10240\n"
"v_and_b32 v15, s63, v17\n"
"v_mfma_f32_16x16x32_bf16 v[112:115], a[80:83], a[144:147], v[112:115]\n"
"ds_read_b128 v[20:23], v2 offset:12288\n"
"v_mul_f32 v12, v12, v156\n"
"v_mfma_f32_16x16x32_bf16 v[112:115], a[84:87], a[148:151], v[112:115]\n"
"global_load_dwordx4 a[76:79], v0, s[34:35] offset:-1024 sc1\n"
"v_mfma_f32_16x16x32_bf16 v[140:143], a[92:95], a[148:151], v[140:143]\n"
"ds_read_b128 a[148:151], v4 offset:13312\n"
"v_mfma_f32_16x16x32_bf16 v[140:143], a[88:91], a[144:147], v[140:143]\n"
"ds_read_b128 a[144:147], v4 offset:12288\n"
"v_mfma_f32_16x16x32_bf16 a[12:15], a[88:91], a[152:155], a[12:15]\n"
"v_mul_f32 v13, v13, v157\n"
"v_mfma_f32_16x16x32_bf16 a[12:15], a[92:95], a[156:159], a[12:15]\n"
"v_mul_f32 v14, v14, v158\n"
"v_mfma_f32_16x16x32_bf16 a[8:11], a[84:87], a[156:159], a[8:11]\n"
"ds_read_b128 a[156:159], v4 offset:15360\n"
"v_mfma_f32_16x16x32_bf16 a[8:11], a[80:83], a[152:155], a[8:11]\n"
"s_waitcnt vmcnt(10) lgkmcnt(14)\n"
"ds_read_b128 a[152:155], v4 offset:14336\n"
"v_mfma_f32_16x16x32_bf16 v[32:35], a[32:35], a[96:99], v[32:35]\n"
"v_mul_f32 v15, v15, v159\n"
"v_mfma_f32_16x16x32_bf16 v[32:35], a[36:39], a[100:103], v[32:35]\n"
"global_load_dwordx4 a[80:83], v0, s[34:35] offset:0 sc1\n"
"v_mfma_f32_16x16x32_bf16 v[60:63], a[44:47], a[100:103], v[60:63]\n"
"s_add_u32 s65, s65, 1\n"
"v_cvt_pk_bf16_f32 v24, v12, v13\n"
"v_mfma_f32_16x16x32_bf16 v[60:63], a[40:43], a[96:99], v[60:63]\n"
"v_cvt_pk_bf16_f32 v25, v14, v15\n"
"v_mfma_f32_16x16x32_bf16 v[64:67], a[40:43], a[104:107], v[64:67]\n"
"s_waitcnt lgkmcnt(4)\n"
"global_store_dwordx4 v0, v[20:23], s[98:99] offset:0 sc1\n"
"ds_write_b64 v2, v[24:25] offset:22528\n"
"v_lshlrev_b32 v12, 16, v18\n"
"v_mfma_f32_16x16x32_bf16 v[64:67], a[44:47], a[108:111], v[64:67]\n"
"v_and_b32 v13, s63, v18\n"
"v_lshlrev_b32 v14, 16, v19\n"
"v_mfma_f32_16x16x32_bf16 v[36:39], a[36:39], a[108:111], v[36:39]\n"
"v_and_b32 v15, s63, v19\n"
"v_mul_f32 v12, v12, v184\n"
"v_mfma_f32_16x16x32_bf16 v[36:39], a[32:35], a[104:107], v[36:39]\n"
"v_mul_f32 v13, v13, v185\n"
"v_mul_f32 v14, v14, v186\n"
"v_mfma_f32_16x16x32_bf16 v[40:43], a[32:35], a[112:115], v[40:43]\n"
"v_mul_f32 v15, v15, v187\n"
"v_cvt_pk_bf16_f32 v26, v12, v13\n"
"v_mfma_f32_16x16x32_bf16 v[40:43], a[36:39], a[116:119], v[40:43]\n"
"global_load_dwordx4 a[84:87], v0, s[34:35] offset:1024 sc1\n"
"v_cvt_pk_bf16_f32 v27, v14, v15\n"
"v_mfma_f32_16x16x32_bf16 v[68:71], a[44:47], a[116:119], v[68:71]\n"
"ds_write_b64 v2, v[26:27] offset:22536\n"
"v_mfma_f32_16x16x32_bf16 v[68:71], a[40:43], a[112:115], v[68:71]\n"
"v_accvgpr_read_b32 v16, a220\n"
"v_accvgpr_read_b32 v17, a221\n"
"v_mfma_f32_16x16x32_bf16 v[72:75], a[40:43], a[120:123], v[72:75]\n"
"v_accvgpr_read_b32 v18, a222\n"
"v_accvgpr_read_b32 v19, a223\n"
"v_mfma_f32_16x16x32_bf16 v[72:75], a[44:47], a[124:127], v[72:75]\n"
"v_lshlrev_b32 v12, 16, v16\n"
"v_and_b32 v13, s63, v16\n"
"v_mfma_f32_16x16x32_bf16 v[44:47], a[36:39], a[124:127], v[44:47]\n"
"v_lshlrev_b32 v14, 16, v17\n"
"v_and_b32 v15, s63, v17\n"
"v_mfma_f32_16x16x32_bf16 v[44:47], a[32:35], a[120:123], v[44:47]\n"
"v_mul_f32 v12, v12, v212\n"
"v_mul_f32 v13, v13, v213\n"
"v_mfma_f32_16x16x32_bf16 v[48:51], a[32:35], a[128:131], v[48:51]\n"
"v_mul_f32 v14, v14, v214\n"
"v_mul_f32 v15, v15, v215\n"
"v_mfma_f32_16x16x32_bf16 v[48:51], a[36:39], a[132:135], v[48:51]\n"
"global_load_dwordx4 a[88:91], v0, s[34:35] offset:2048 sc1\n"
"v_cvt_pk_bf16_f32 v28, v12, v13\n"
"v_mfma_f32_16x16x32_bf16 v[76:79], a[44:47], a[132:135], v[76:79]\n"
"v_cvt_pk_bf16_f32 v29, v14, v15\n"
"v_mfma_f32_16x16x32_bf16 v[76:79], a[40:43], a[128:131], v[76:79]\n"
"ds_write_b64 v2, v[28:29] offset:23552\n"
"v_mfma_f32_16x16x32_bf16 v[80:83], a[40:43], a[136:139], v[80:83]\n"
"v_lshlrev_b32 v12, 16, v18\n"
"v_and_b32 v13, s63, v18\n"
"v_mfma_f32_16x16x32_bf16 v[80:83], a[44:47], a[140:143], v[80:83]\n"
"v_lshlrev_b32 v14, 16, v19\n"
"v_and_b32 v15, s63, v19\n"
"v_mfma_f32_16x16x32_bf16 v[52:55], a[36:39], a[140:143], v[52:55]\n"
"v_mul_f32 v12, v12, v240\n"
"v_mul_f32 v13, v13, v241\n"
"v_mfma_f32_16x16x32_bf16 v[52:55], a[32:35], a[136:139], v[52:55]\n"
"s_waitcnt lgkmcnt(3)\n"
"v_mul_f32 v14, v14, v242\n"
"v_mul_f32 v15, v15, v243\n"
"v_mfma_f32_16x16x32_bf16 v[56:59], a[32:35], a[144:147], v[56:59]\n"
"v_cvt_pk_bf16_f32 v30, v12, v13\n"
"v_cvt_pk_bf16_f32 v31, v14, v15\n"
"v_mfma_f32_16x16x32_bf16 v[56:59], a[36:39], a[148:151], v[56:59]\n"
"global_load_dwordx4 a[92:95], v0, s[34:35] offset:3072 sc1\n"
"v_mfma_f32_16x16x32_bf16 v[84:87], a[44:47], a[148:151], v[84:87]\n"
"ds_write_b64 v2, v[30:31] offset:23560\n"
"v_mfma_f32_16x16x32_bf16 v[84:87], a[40:43], a[144:147], v[84:87]\n"
"v_accvgpr_read_b32 v16, a224\n"
"v_accvgpr_read_b32 v17, a225\n"
"v_mfma_f32_16x16x32_bf16 a[4:7], a[40:43], a[152:155], a[4:7]\n"
"s_add_u32 s34, s34, 8192\n"
"v_accvgpr_read_b32 v18, a226\n"
"v_accvgpr_read_b32 v19, a227\n"
"v_mfma_f32_16x16x32_bf16 a[4:7], a[44:47], a[156:159], a[4:7]\n"
"v_lshlrev_b32 v12, 16, v16\n"
"v_and_b32 v13, s63, v16\n"
"v_mfma_f32_16x16x32_bf16 a[0:3], a[36:39], a[156:159], a[0:3]\n"
"v_lshlrev_b32 v14, 16, v17\n"
"v_and_b32 v15, s63, v17\n"
"v_mfma_f32_16x16x32_bf16 a[0:3], a[32:35], a[152:155], a[0:3]\n"
"s_waitcnt vmcnt(10)\n"
"v_mul_f32 v12, v12, v160\n"
"v_mul_f32 v13, v13, v161\n"
"v_mfma_f32_16x16x32_bf16 v[88:91], a[48:51], a[96:99], v[88:91]\n"
"v_mul_f32 v14, v14, v162\n"
"v_mul_f32 v15, v15, v163\n"
"v_mfma_f32_16x16x32_bf16 v[88:91], a[52:55], a[100:103], v[88:91]\n"
"global_load_dwordx4 a[32:35], v0, s[34:35] offset:-4096 sc1\n"
"v_cvt_pk_bf16_f32 v24, v12, v13\n"
"v_mfma_f32_16x16x32_bf16 v[116:119], a[60:63], a[100:103], v[116:119]\n"
"ds_read_b128 a[100:103], v5 offset:1024\n"
"v_cvt_pk_bf16_f32 v25, v14, v15\n"
"v_mfma_f32_16x16x32_bf16 v[116:119], a[56:59], a[96:99], v[116:119]\n"
"ds_read_b128 a[96:99], v5 offset:0\n"
"v_mfma_f32_16x16x32_bf16 v[120:123], a[56:59], a[104:107], v[120:123]\n"
"ds_write_b64 v2, v[24:25] offset:24576\n"
"v_mfma_f32_16x16x32_bf16 v[120:123], a[60:63], a[108:111], v[120:123]\n"
"v_lshlrev_b32 v12, 16, v18\n"
"v_and_b32 v13, s63, v18\n"
"v_mfma_f32_16x16x32_bf16 v[92:95], a[52:55], a[108:111], v[92:95]\n"
"ds_read_b128 a[108:111], v5 offset:3072\n"
"v_lshlrev_b32 v14, 16, v19\n"
"v_mfma_f32_16x16x32_bf16 v[92:95], a[48:51], a[104:107], v[92:95]\n"
"ds_read_b128 a[104:107], v5 offset:2048\n"
"v_and_b32 v15, s63, v19\n"
"v_mfma_f32_16x16x32_bf16 v[96:99], a[48:51], a[112:115], v[96:99]\n"
"v_mul_f32 v12, v12, v188\n"
"v_mul_f32 v13, v13, v189\n"
"v_mfma_f32_16x16x32_bf16 v[96:99], a[52:55], a[116:119], v[96:99]\n"
"global_load_dwordx4 a[36:39], v0, s[34:35] offset:-3072 sc1\n"
"v_mul_f32 v14, v14, v190\n"
"v_mfma_f32_16x16x32_bf16 v[124:127], a[60:63], a[116:119], v[124:127]\n"
"ds_read_b128 a[116:119], v5 offset:5120\n"
"v_mul_f32 v15, v15, v191\n"
"v_mfma_f32_16x16x32_bf16 v[124:127], a[56:59], a[112:115], v[124:127]\n"
"ds_read_b128 a[112:115], v5 offset:4096\n"
"v_cvt_pk_bf16_f32 v26, v12, v13\n"
"v_mfma_f32_16x16x32_bf16 v[128:131], a[56:59], a[120:123], v[128:131]\n"
"v_cvt_pk_bf16_f32 v27, v14, v15\n"
"v_mfma_f32_16x16x32_bf16 v[128:131], a[60:63], a[124:127], v[128:131]\n"
"ds_write_b64 v2, v[26:27] offset:24584\n"
"v_mfma_f32_16x16x32_bf16 v[100:103], a[52:55], a[124:127], v[100:103]\n"
"ds_read_b128 a[124:127], v5 offset:7168\n"
"v_accvgpr_read_b32 v16, a228\n"
"v_mfma_f32_16x16x32_bf16 v[100:103], a[48:51], a[120:123], v[100:103]\n"
"ds_read_b128 a[120:123], v5 offset:6144\n"
"v_accvgpr_read_b32 v17, a229\n"
"v_mfma_f32_16x16x32_bf16 v[104:107], a[48:51], a[128:131], v[104:107]\n"
"v_accvgpr_read_b32 v18, a230\n"
"v_accvgpr_read_b32 v19, a231\n"
"v_mfma_f32_16x16x32_bf16 v[104:107], a[52:55], a[132:135], v[104:107]\n"
"global_load_dwordx4 a[40:43], v0, s[34:35] offset:-2048 sc1\n"
"v_lshlrev_b32 v12, 16, v16\n"
"v_mfma_f32_16x16x32_bf16 v[132:135], a[60:63], a[132:135], v[132:135]\n"
"ds_read_b128 a[132:135], v5 offset:9216\n"
"v_and_b32 v13, s63, v16\n"
"v_mfma_f32_16x16x32_bf16 v[132:135], a[56:59], a[128:131], v[132:135]\n"
"ds_read_b128 a[128:131], v5 offset:8192\n"
"v_lshlrev_b32 v14, 16, v17\n"
"v_mfma_f32_16x16x32_bf16 v[136:139], a[56:59], a[136:139], v[136:139]\n"
"v_and_b32 v15, s63, v17\n"
"v_mul_f32 v12, v12, v216\n"
"v_mfma_f32_16x16x32_bf16 v[136:139], a[60:63], a[140:143], v[136:139]\n"
"v_mul_f32 v13, v13, v217\n"
"v_mul_f32 v14, v14, v218\n"
"v_mfma_f32_16x16x32_bf16 v[108:111], a[52:55], a[140:143], v[108:111]\n"
"ds_read_b128 a[140:143], v5 offset:11264\n"
"v_mul_f32 v15, v15, v219\n"
"v_mfma_f32_16x16x32_bf16 v[108:111], a[48:51], a[136:139], v[108:111]\n"
"ds_read_b128 a[136:139], v5 offset:10240\n"
"v_cvt_pk_bf16_f32 v28, v12, v13\n"
"v_mfma_f32_16x16x32_bf16 v[112:115], a[48:51], a[144:147], v[112:115]\n"
"v_cvt_pk_bf16_f32 v29, v14, v15\n"
"v_lshlrev_b32 v12, 16, v18\n"
"v_mfma_f32_16x16x32_bf16 v[112:115], a[52:55], a[148:151], v[112:115]\n"
"global_load_dwordx4 a[44:47], v0, s[34:35] offset:-1024 sc1\n"
"v_and_b32 v13, s63, v18\n"
"v_mfma_f32_16x16x32_bf16 v[140:143], a[60:63], a[148:151], v[140:143]\n"
"ds_read_b128 a[148:151], v5 offset:13312\n"
"v_lshlrev_b32 v14, 16, v19\n"
"v_mfma_f32_16x16x32_bf16 v[140:143], a[56:59], a[144:147], v[140:143]\n"
"ds_read_b128 a[144:147], v5 offset:12288\n"
"v_and_b32 v15, s63, v19\n"
"v_mfma_f32_16x16x32_bf16 a[12:15], a[56:59], a[152:155], a[12:15]\n"
"v_mul_f32 v12, v12, v244\n"
"v_mul_f32 v13, v13, v245\n"
"v_mfma_f32_16x16x32_bf16 a[12:15], a[60:63], a[156:159], a[12:15]\n"
"v_mul_f32 v14, v14, v246\n"
"v_mul_f32 v15, v15, v247\n"
"v_mfma_f32_16x16x32_bf16 a[8:11], a[52:55], a[156:159], a[8:11]\n"
"ds_read_b128 a[156:159], v5 offset:15360\n"
"v_cvt_pk_bf16_f32 v30, v12, v13\n"
"v_mfma_f32_16x16x32_bf16 a[8:11], a[48:51], a[152:155], a[8:11]\n"
"s_waitcnt vmcnt(9) lgkmcnt(12)\n"
"ds_read_b128 a[152:155], v5 offset:14336\n"
"v_cvt_pk_bf16_f32 v31, v14, v15\n"
"v_mfma_f32_16x16x32_bf16 v[32:35], a[64:67], a[96:99], v[32:35]\n"
"global_store_dwordx4 v0, v[28:31], s[54:55] offset:-3072 sc1\n"
"ds_write_b128 v2, v[28:31] offset:25600\n"
"v_mfma_f32_16x16x32_bf16 v[32:35], a[68:71], a[100:103], v[32:35]\n"
"global_load_dwordx4 a[48:51], v0, s[34:35] offset:0 sc1\n"
"v_accvgpr_read_b32 v16, a232\n"
"v_mfma_f32_16x16x32_bf16 v[60:63], a[76:79], a[100:103], v[60:63]\n"
"v_accvgpr_read_b32 v17, a233\n"
"v_accvgpr_read_b32 v18, a234\n"
"v_mfma_f32_16x16x32_bf16 v[60:63], a[72:75], a[96:99], v[60:63]\n"
"v_accvgpr_read_b32 v19, a235\n"
"v_lshlrev_b32 v12, 16, v16\n"
"v_mfma_f32_16x16x32_bf16 v[64:67], a[72:75], a[104:107], v[64:67]\n"
"v_and_b32 v13, s63, v16\n"
"v_lshlrev_b32 v14, 16, v17\n"
"v_mfma_f32_16x16x32_bf16 v[64:67], a[76:79], a[108:111], v[64:67]\n"
"v_and_b32 v15, s63, v17\n"
"v_mul_f32 v12, v12, v164\n"
"v_mfma_f32_16x16x32_bf16 v[36:39], a[68:71], a[108:111], v[36:39]\n"
"v_mul_f32 v13, v13, v165\n"
"v_mul_f32 v14, v14, v166\n"
"v_mfma_f32_16x16x32_bf16 v[36:39], a[64:67], a[104:107], v[36:39]\n"
"s_waitcnt lgkmcnt(9)\n"
"v_mul_f32 v15, v15, v167\n"
"v_cvt_pk_bf16_f32 v24, v12, v13\n"
"v_mfma_f32_16x16x32_bf16 v[40:43], a[64:67], a[112:115], v[40:43]\n"
"v_cvt_pk_bf16_f32 v25, v14, v15\n"
"v_mfma_f32_16x16x32_bf16 v[40:43], a[68:71], a[116:119], v[40:43]\n"
"global_load_dwordx4 a[52:55], v0, s[34:35] offset:1024 sc1\n"
"v_mfma_f32_16x16x32_bf16 v[68:71], a[76:79], a[116:119], v[68:71]\n"
"ds_write_b64 v2, v[24:25] offset:26624\n"
"v_mfma_f32_16x16x32_bf16 v[68:71], a[72:75], a[112:115], v[68:71]\n"
"v_lshlrev_b32 v12, 16, v18\n"
"v_and_b32 v13, s63, v18\n"
"v_mfma_f32_16x16x32_bf16 v[72:75], a[72:75], a[120:123], v[72:75]\n"
"v_lshlrev_b32 v14, 16, v19\n"
"v_and_b32 v15, s63, v19\n"
"v_mfma_f32_16x16x32_bf16 v[72:75], a[76:79], a[124:127], v[72:75]\n"
"v_mul_f32 v12, v12, v192\n"
"v_mul_f32 v13, v13, v193\n"
"v_mfma_f32_16x16x32_bf16 v[44:47], a[68:71], a[124:127], v[44:47]\n"
"v_mul_f32 v14, v14, v194\n"
"v_mul_f32 v15, v15, v195\n"
"v_mfma_f32_16x16x32_bf16 v[44:47], a[64:67], a[120:123], v[44:47]\n"
"s_waitcnt lgkmcnt(6)\n"
"v_cvt_pk_bf16_f32 v26, v12, v13\n"
"v_cvt_pk_bf16_f32 v27, v14, v15\n"
"v_mfma_f32_16x16x32_bf16 v[48:51], a[64:67], a[128:131], v[48:51]\n"
"ds_write_b64 v2, v[26:27] offset:26632\n"
"v_mfma_f32_16x16x32_bf16 v[48:51], a[68:71], a[132:135], v[48:51]\n"
"global_load_dwordx4 a[56:59], v0, s[34:35] offset:2048 sc1\n"
"v_accvgpr_read_b32 v16, a236\n"
"v_mfma_f32_16x16x32_bf16 v[76:79], a[76:79], a[132:135], v[76:79]\n"
"v_accvgpr_read_b32 v17, a237\n"
"v_accvgpr_read_b32 v18, a238\n"
"v_mfma_f32_16x16x32_bf16 v[76:79], a[72:75], a[128:131], v[76:79]\n"
"v_accvgpr_read_b32 v19, a239\n"
"v_lshlrev_b32 v12, 16, v16\n"
"v_mfma_f32_16x16x32_bf16 v[80:83], a[72:75], a[136:139], v[80:83]\n"
"v_and_b32 v13, s63, v16\n"
"v_mfma_f32_16x16x32_bf16 v[80:83], a[76:79], a[140:143], v[80:83]\n"
"v_lshlrev_b32 v14, 16, v17\n"
"v_mfma_f32_16x16x32_bf16 v[52:55], a[68:71], a[140:143], v[52:55]\n"
"v_and_b32 v15, s63, v17\n"
"v_mul_f32 v12, v12, v220\n"
"v_mfma_f32_16x16x32_bf16 v[52:55], a[64:67], a[136:139], v[52:55]\n"
"s_waitcnt lgkmcnt(3)\n"
"v_mul_f32 v13, v13, v221\n"
"v_mfma_f32_16x16x32_bf16 v[56:59], a[64:67], a[144:147], v[56:59]\n"
"v_mul_f32 v14, v14, v222\n"
"v_mul_f32 v15, v15, v223\n"
"v_mfma_f32_16x16x32_bf16 v[56:59], a[68:71], a[148:151], v[56:59]\n"
"global_load_dwordx4 a[60:63], v0, s[34:35] offset:3072 sc1\n"
"v_cvt_pk_bf16_f32 v28, v12, v13\n"
"v_mfma_f32_16x16x32_bf16 v[84:87], a[76:79], a[148:151], v[84:87]\n"
"v_cvt_pk_bf16_f32 v29, v14, v15\n"
"v_mfma_f32_16x16x32_bf16 v[84:87], a[72:75], a[144:147], v[84:87]\n"
"v_lshlrev_b32 v12, 16, v18\n"
"v_and_b32 v13, s63, v18\n"
"v_mfma_f32_16x16x32_bf16 a[4:7], a[72:75], a[152:155], a[4:7]\n"
"s_add_u32 s34, s34, 8192\n"
"v_lshlrev_b32 v14, 16, v19\n"
"v_and_b32 v15, s63, v19\n"
"v_mfma_f32_16x16x32_bf16 a[4:7], a[76:79], a[156:159], a[4:7]\n"
"v_mul_f32 v12, v12, v248\n"
"v_mul_f32 v13, v13, v249\n"
"v_mfma_f32_16x16x32_bf16 a[0:3], a[68:71], a[156:159], a[0:3]\n"
"v_mul_f32 v14, v14, v250\n"
"v_mul_f32 v15, v15, v251\n"
"v_mfma_f32_16x16x32_bf16 a[0:3], a[64:67], a[152:155], a[0:3]\n"
"s_waitcnt vmcnt(9)\n"
"v_cvt_pk_bf16_f32 v30, v12, v13\n"
"v_cvt_pk_bf16_f32 v31, v14, v15\n"
"v_mfma_f32_16x16x32_bf16 v[88:91], a[80:83], a[96:99], v[88:91]\n"
"global_store_dwordx4 v0, v[28:31], s[54:55] offset:-1024 sc1\n"
"ds_write_b128 v2, v[28:31] offset:27648\n"
"v_mfma_f32_16x16x32_bf16 v[88:91], a[84:87], a[100:103], v[88:91]\n"
"global_load_dwordx4 a[64:67], v0, s[34:35] offset:-4096 sc1\n"
"v_mfma_f32_16x16x32_bf16 v[116:119], a[92:95], a[100:103], v[116:119]\n"
"ds_read_b128 a[100:103], v2 offset:1024\n"
"v_accvgpr_read_b32 v16, a240\n"
"v_mfma_f32_16x16x32_bf16 v[116:119], a[88:91], a[96:99], v[116:119]\n"
"ds_read_b128 a[96:99], v2 offset:0\n"
"v_accvgpr_read_b32 v17, a241\n"
"v_mfma_f32_16x16x32_bf16 v[120:123], a[88:91], a[104:107], v[120:123]\n"
"v_accvgpr_read_b32 v18, a242\n"
"v_accvgpr_read_b32 v19, a243\n"
"v_mfma_f32_16x16x32_bf16 v[120:123], a[92:95], a[108:111], v[120:123]\n"
"v_lshlrev_b32 v12, 16, v16\n"
"v_and_b32 v13, s63, v16\n"
"v_mfma_f32_16x16x32_bf16 v[92:95], a[84:87], a[108:111], v[92:95]\n"
"ds_read_b128 a[108:111], v2 offset:3072\n"
"v_lshlrev_b32 v14, 16, v17\n"
"v_mfma_f32_16x16x32_bf16 v[92:95], a[80:83], a[104:107], v[92:95]\n"
"ds_read_b128 a[104:107], v2 offset:2048\n"
"v_and_b32 v15, s63, v17\n"
"v_mfma_f32_16x16x32_bf16 v[96:99], a[80:83], a[112:115], v[96:99]\n"
"v_mul_f32 v12, v12, v168\n"
"v_mul_f32 v13, v13, v169\n"
"v_mfma_f32_16x16x32_bf16 v[96:99], a[84:87], a[116:119], v[96:99]\n"
"global_load_dwordx4 a[68:71], v0, s[34:35] offset:-3072 sc1\n"
"v_mul_f32 v14, v14, v170\n"
"v_mfma_f32_16x16x32_bf16 v[124:127], a[92:95], a[116:119], v[124:127]\n"
"ds_read_b128 a[116:119], v2 offset:5120\n"
"v_mul_f32 v15, v15, v171\n"
"v_mfma_f32_16x16x32_bf16 v[124:127], a[88:91], a[112:115], v[124:127]\n"
"ds_read_b128 a[112:115], v2 offset:4096\n"
"v_cvt_pk_bf16_f32 v24, v12, v13\n"
"v_mfma_f32_16x16x32_bf16 v[128:131], a[88:91], a[120:123], v[128:131]\n"
"v_cvt_pk_bf16_f32 v25, v14, v15\n"
"v_lshlrev_b32 v12, 16, v18\n"
"v_mfma_f32_16x16x32_bf16 v[128:131], a[92:95], a[124:127], v[128:131]\n"
"v_and_b32 v13, s63, v18\n"
"v_lshlrev_b32 v14, 16, v19\n"
"v_mfma_f32_16x16x32_bf16 v[100:103], a[84:87], a[124:127], v[100:103]\n"
"ds_read_b128 a[124:127], v2 offset:7168\n"
"v_and_b32 v15, s63, v19\n"
"v_mfma_f32_16x16x32_bf16 v[100:103], a[80:83], a[120:123], v[100:103]\n"
"ds_read_b128 a[120:123], v2 offset:6144\n"
"v_mul_f32 v12, v12, v196\n"
"v_mfma_f32_16x16x32_bf16 v[104:107], a[80:83], a[128:131], v[104:107]\n"
"v_mul_f32 v13, v13, v197\n"
"v_mul_f32 v14, v14, v198\n"
"v_mfma_f32_16x16x32_bf16 v[104:107], a[84:87], a[132:135], v[104:107]\n"
"global_load_dwordx4 a[72:75], v0, s[34:35] offset:-2048 sc1\n"
"v_mul_f32 v15, v15, v199\n"
"v_mfma_f32_16x16x32_bf16 v[132:135], a[92:95], a[132:135], v[132:135]\n"
"ds_read_b128 a[132:135], v2 offset:9216\n"
"v_cvt_pk_bf16_f32 v26, v12, v13\n"
"v_mfma_f32_16x16x32_bf16 v[132:135], a[88:91], a[128:131], v[132:135]\n"
"ds_read_b128 a[128:131], v2 offset:8192\n"
"v_cvt_pk_bf16_f32 v27, v14, v15\n"
"v_mfma_f32_16x16x32_bf16 v[136:139], a[88:91], a[136:139], v[136:139]\n"
"global_store_dwordx4 v0, v[24:27], s[54:55] offset:0 sc1\n"
"ds_write_b128 v2, v[24:27] offset:28672\n"
"v_mfma_f32_16x16x32_bf16 v[136:139], a[92:95], a[140:143], v[136:139]\n"
"v_accvgpr_read_b32 v16, a248\n"
"v_mfma_f32_16x16x32_bf16 v[108:111], a[84:87], a[140:143], v[108:111]\n"
"ds_read_b128 a[140:143], v2 offset:11264\n"
"v_mfma_f32_16x16x32_bf16 v[108:111], a[80:83], a[136:139], v[108:111]\n"
"ds_read_b128 a[136:139], v2 offset:10240\n"
"v_accvgpr_read_b32 v17, a249\n"
"v_mfma_f32_16x16x32_bf16 v[112:115], a[80:83], a[144:147], v[112:115]\n"
"v_accvgpr_read_b32 v18, a250\n"
"v_accvgpr_read_b32 v19, a251\n"
"v_mfma_f32_16x16x32_bf16 v[112:115], a[84:87], a[148:151], v[112:115]\n"
"global_load_dwordx4 a[76:79], v0, s[34:35] offset:-1024 sc1\n"
"v_lshlrev_b32 v12, 16, v16\n"
"v_mfma_f32_16x16x32_bf16 v[140:143], a[92:95], a[148:151], v[140:143]\n"
"ds_read_b128 a[148:151], v2 offset:13312\n"
"v_and_b32 v13, s63, v16\n"
"v_mfma_f32_16x16x32_bf16 v[140:143], a[88:91], a[144:147], v[140:143]\n"
"ds_read_b128 a[144:147], v2 offset:12288\n"
"v_lshlrev_b32 v14, 16, v17\n"
"v_mfma_f32_16x16x32_bf16 a[12:15], a[88:91], a[152:155], a[12:15]\n"
"v_and_b32 v15, s63, v17\n"
"v_accvgpr_read_b32 v10, a16\n"
"v_mfma_f32_16x16x32_bf16 a[12:15], a[92:95], a[156:159], a[12:15]\n"
"v_accvgpr_read_b32 v11, a17\n"
"v_mul_f32 v12, v12, v10\n"
"v_mfma_f32_16x16x32_bf16 a[8:11], a[84:87], a[156:159], a[8:11]\n"
"ds_read_b128 a[156:159], v2 offset:15360\n"
"v_mul_f32 v13, v13, v11\n"
"v_mfma_f32_16x16x32_bf16 a[8:11], a[80:83], a[152:155], a[8:11]\n"
"s_waitcnt vmcnt(11) lgkmcnt(12)\n"
"ds_read_b128 a[152:155], v2 offset:14336\n"
"v_accvgpr_read_b32 v10, a18\n"
"v_mfma_f32_16x16x32_bf16 v[144:147], a[32:35], a[96:99], v[144:147]\n"
"v_accvgpr_read_b32 v11, a19\n"
"v_mul_f32 v14, v14, v10\n"
"v_mfma_f32_16x16x32_bf16 v[144:147], a[36:39], a[100:103], v[144:147]\n"
"global_load_dwordx4 a[80:83], v0, s[34:35] offset:0 sc1\n"
"v_mul_f32 v15, v15, v11\n"
"v_mfma_f32_16x16x32_bf16 v[172:175], a[44:47], a[100:103], v[172:175]\n"
"v_cvt_pk_bf16_f32 v28, v12, v13\n"
"v_mfma_f32_16x16x32_bf16 v[172:175], a[40:43], a[96:99], v[172:175]\n"
"v_cvt_pk_bf16_f32 v29, v14, v15\n"
"v_lshlrev_b32 v12, 16, v18\n"
"v_mfma_f32_16x16x32_bf16 v[176:179], a[40:43], a[104:107], v[176:179]\n"
"v_and_b32 v13, s63, v18\n"
"v_mfma_f32_16x16x32_bf16 v[176:179], a[44:47], a[108:111], v[176:179]\n"
"v_lshlrev_b32 v14, 16, v19\n"
"v_mfma_f32_16x16x32_bf16 v[148:151], a[36:39], a[108:111], v[148:151]\n"
"v_and_b32 v15, s63, v19\n"
"v_accvgpr_read_b32 v10, a20\n"
"v_mfma_f32_16x16x32_bf16 v[148:151], a[32:35], a[104:107], v[148:151]\n"
"s_waitcnt lgkmcnt(9)\n"
"v_accvgpr_read_b32 v11, a21\n"
"v_mfma_f32_16x16x32_bf16 v[152:155], a[32:35], a[112:115], v[152:155]\n"
"v_mul_f32 v12, v12, v10\n"
"v_mul_f32 v13, v13, v11\n"
"v_mfma_f32_16x16x32_bf16 v[152:155], a[36:39], a[116:119], v[152:155]\n"
"global_load_dwordx4 a[84:87], v0, s[34:35] offset:1024 sc1\n"
"v_accvgpr_read_b32 v10, a22\n"
"v_mfma_f32_16x16x32_bf16 v[180:183], a[44:47], a[116:119], v[180:183]\n"
"v_accvgpr_read_b32 v11, a23\n"
"v_mfma_f32_16x16x32_bf16 v[180:183], a[40:43], a[112:115], v[180:183]\n"
"v_mul_f32 v14, v14, v10\n"
"v_mul_f32 v15, v15, v11\n"
"v_mfma_f32_16x16x32_bf16 v[184:187], a[40:43], a[120:123], v[184:187]\n"
"v_cvt_pk_bf16_f32 v30, v12, v13\n"
"v_cvt_pk_bf16_f32 v31, v14, v15\n"
"v_mfma_f32_16x16x32_bf16 v[184:187], a[44:47], a[124:127], v[184:187]\n"
"global_store_dwordx4 v0, v[28:31], s[54:55] offset:2048 sc1\n"
"ds_write_b128 v2, v[28:31] offset:30720\n"
"v_mfma_f32_16x16x32_bf16 v[156:159], a[36:39], a[124:127], v[156:159]\n"
"v_accvgpr_read_b32 v16, a244\n"
"v_mfma_f32_16x16x32_bf16 v[156:159], a[32:35], a[120:123], v[156:159]\n"
"s_waitcnt lgkmcnt(5)\n"
"v_accvgpr_read_b32 v17, a245\n"
"v_accvgpr_read_b32 v18, a246\n"
"v_mfma_f32_16x16x32_bf16 v[160:163], a[32:35], a[128:131], v[160:163]\n"
"v_accvgpr_read_b32 v19, a247\n"
"v_lshlrev_b32 v12, 16, v16\n"
"v_mfma_f32_16x16x32_bf16 v[160:163], a[36:39], a[132:135], v[160:163]\n"
"global_load_dwordx4 a[88:91], v0, s[34:35] offset:2048 sc1\n"
"v_and_b32 v13, s63, v16\n"
"v_mfma_f32_16x16x32_bf16 v[188:191], a[44:47], a[132:135], v[188:191]\n"
"v_lshlrev_b32 v14, 16, v17\n"
"v_and_b32 v15, s63, v17\n"
"v_mfma_f32_16x16x32_bf16 v[188:191], a[40:43], a[128:131], v[188:191]\n"
"v_mul_f32 v12, v12, v224\n"
"v_mul_f32 v13, v13, v225\n"
"v_mfma_f32_16x16x32_bf16 v[192:195], a[40:43], a[136:139], v[192:195]\n"
"v_mul_f32 v14, v14, v226\n"
"v_mul_f32 v15, v15, v227\n"
"v_mfma_f32_16x16x32_bf16 v[192:195], a[44:47], a[140:143], v[192:195]\n"
"v_cvt_pk_bf16_f32 v24, v12, v13\n"
"v_cvt_pk_bf16_f32 v25, v14, v15\n"
"v_mfma_f32_16x16x32_bf16 v[164:167], a[36:39], a[140:143], v[164:167]\n"
"ds_write_b64 v2, v[24:25] offset:29696\n"
"v_mfma_f32_16x16x32_bf16 v[164:167], a[32:35], a[136:139], v[164:167]\n"
"s_waitcnt lgkmcnt(2)\n"
"v_mov_b32 v8, s65\n"
"v_mfma_f32_16x16x32_bf16 v[168:171], a[32:35], a[144:147], v[168:171]\n"
"v_mov_b32 v9, s60\n"
"v_mfma_f32_16x16x32_bf16 v[168:171], a[36:39], a[148:151], v[168:171]\n"
"global_load_dwordx4 a[92:95], v0, s[34:35] offset:3072 sc1\n"
"v_lshlrev_b32 v12, 16, v18\n"
"v_mfma_f32_16x16x32_bf16 v[196:199], a[44:47], a[148:151], v[196:199]\n"
"buffer_store_dwordx2 v[8:9], off, s[56:59], %13 sc1\n"
"v_and_b32 v13, s63, v18\n"
"v_mfma_f32_16x16x32_bf16 v[196:199], a[40:43], a[144:147], v[196:199]\n"
"v_lshlrev_b32 v14, 16, v19\n"
"v_and_b32 v15, s63, v19\n"
"v_mfma_f32_16x16x32_bf16 a[20:23], a[40:43], a[152:155], a[20:23]\n"
"s_add_u32 s34, s34, 8192\n"
"v_mul_f32 v12, v12, v252\n"
"v_mul_f32 v13, v13, v253\n"
"v_mfma_f32_16x16x32_bf16 a[20:23], a[44:47], a[156:159], a[20:23]\n"
"v_mul_f32 v14, v14, v254\n"
"v_mul_f32 v15, v15, v255\n"
"v_mfma_f32_16x16x32_bf16 a[16:19], a[36:39], a[156:159], a[16:19]\n"
"v_cvt_pk_bf16_f32 v26, v12, v13\n"
"v_cvt_pk_bf16_f32 v27, v14, v15\n"
"v_mfma_f32_16x16x32_bf16 a[16:19], a[32:35], a[152:155], a[16:19]\n"
"s_waitcnt vmcnt(12)\n"
"ds_write_b64 v2, v[26:27] offset:29704\n"
"v_mfma_f32_16x16x32_bf16 v[200:203], a[48:51], a[96:99], v[200:203]\n"
"v_accvgpr_read_b32 v16, a252\n"
"v_mfma_f32_16x16x32_bf16 v[200:203], a[52:55], a[100:103], v[200:203]\n"
"global_load_dwordx4 a[32:35], v0, s[34:35] offset:-4096 sc1\n"
"v_accvgpr_read_b32 v17, a253\n"
"v_mfma_f32_16x16x32_bf16 v[228:231], a[60:63], a[100:103], v[228:231]\n"
"ds_read_b128 a[100:103], v3 offset:1024\n"
"v_accvgpr_read_b32 v18, a254\n"
"v_mfma_f32_16x16x32_bf16 v[228:231], a[56:59], a[96:99], v[228:231]\n"
"ds_read_b128 a[96:99], v3 offset:0\n"
"v_accvgpr_read_b32 v19, a255\n"
"v_mfma_f32_16x16x32_bf16 v[232:235], a[56:59], a[104:107], v[232:235]\n"
"v_lshlrev_b32 v12, 16, v16\n"
"v_and_b32 v13, s63, v16\n"
"v_mfma_f32_16x16x32_bf16 v[232:235], a[60:63], a[108:111], v[232:235]\n"
"v_lshlrev_b32 v14, 16, v17\n"
"v_and_b32 v15, s63, v17\n"
"v_mfma_f32_16x16x32_bf16 v[204:207], a[52:55], a[108:111], v[204:207]\n"
"ds_read_b128 a[108:111], v3 offset:3072\n"
"v_accvgpr_read_b32 v10, a24\n"
"v_mfma_f32_16x16x32_bf16 v[204:207], a[48:51], a[104:107], v[204:207]\n"
"ds_read_b128 a[104:107], v3 offset:2048\n"
"v_accvgpr_read_b32 v11, a25\n"
"v_mfma_f32_16x16x32_bf16 v[208:211], a[48:51], a[112:115], v[208:211]\n"
"v_mul_f32 v12, v12, v10\n"
"v_mul_f32 v13, v13, v11\n"
"v_mfma_f32_16x16x32_bf16 v[208:211], a[52:55], a[116:119], v[208:211]\n"
"global_load_dwordx4 a[36:39], v0, s[34:35] offset:-3072 sc1\n"
"v_accvgpr_read_b32 v10, a26\n"
"v_mfma_f32_16x16x32_bf16 v[236:239], a[60:63], a[116:119], v[236:239]\n"
"ds_read_b128 a[116:119], v3 offset:5120\n"
"v_accvgpr_read_b32 v11, a27\n"
"v_mfma_f32_16x16x32_bf16 v[236:239], a[56:59], a[112:115], v[236:239]\n"
"ds_read_b128 a[112:115], v3 offset:4096\n"
"v_mul_f32 v14, v14, v10\n"
"v_mfma_f32_16x16x32_bf16 v[240:243], a[56:59], a[120:123], v[240:243]\n"
"v_mul_f32 v15, v15, v11\n"
"v_cvt_pk_bf16_f32 v28, v12, v13\n"
"v_mfma_f32_16x16x32_bf16 v[240:243], a[60:63], a[124:127], v[240:243]\n"
"v_cvt_pk_bf16_f32 v29, v14, v15\n"
"v_lshlrev_b32 v12, 16, v18\n"
"v_mfma_f32_16x16x32_bf16 v[212:215], a[52:55], a[124:127], v[212:215]\n"
"ds_read_b128 a[124:127], v3 offset:7168\n"
"v_and_b32 v13, s63, v18\n"
"v_mfma_f32_16x16x32_bf16 v[212:215], a[48:51], a[120:123], v[212:215]\n"
"ds_read_b128 a[120:123], v3 offset:6144\n"
"v_lshlrev_b32 v14, 16, v19\n"
"v_mfma_f32_16x16x32_bf16 v[216:219], a[48:51], a[128:131], v[216:219]\n"
"v_and_b32 v15, s63, v19\n"
"v_accvgpr_read_b32 v10, a28\n"
"v_mfma_f32_16x16x32_bf16 v[216:219], a[52:55], a[132:135], v[216:219]\n"
"global_load_dwordx4 a[40:43], v0, s[34:35] offset:-2048 sc1\n"
"v_accvgpr_read_b32 v11, a29\n"
"v_mfma_f32_16x16x32_bf16 v[244:247], a[60:63], a[132:135], v[244:247]\n"
"ds_read_b128 a[132:135], v3 offset:9216\n"
"v_mul_f32 v12, v12, v10\n"
"v_mfma_f32_16x16x32_bf16 v[244:247], a[56:59], a[128:131], v[244:247]\n"
"ds_read_b128 a[128:131], v3 offset:8192\n"
"v_mul_f32 v13, v13, v11\n"
"v_mfma_f32_16x16x32_bf16 v[248:251], a[56:59], a[136:139], v[248:251]\n"
"v_accvgpr_read_b32 v10, a30\n"
"v_accvgpr_read_b32 v11, a31\n"
"v_mfma_f32_16x16x32_bf16 v[248:251], a[60:63], a[140:143], v[248:251]\n"
"v_mul_f32 v14, v14, v10\n"
"v_mul_f32 v15, v15, v11\n"
"v_mfma_f32_16x16x32_bf16 v[220:223], a[52:55], a[140:143], v[220:223]\n"
"ds_read_b128 a[140:143], v3 offset:11264\n"
"v_cvt_pk_bf16_f32 v30, v12, v13\n"
"v_mfma_f32_16x16x32_bf16 v[220:223], a[48:51], a[136:139], v[220:223]\n"
"ds_read_b128 a[136:139], v3 offset:10240\n"
"v_cvt_pk_bf16_f32 v31, v14, v15\n"
"v_mfma_f32_16x16x32_bf16 v[224:227], a[48:51], a[144:147], v[224:227]\n"
"global_store_dwordx4 v0, v[28:31], s[54:55] offset:3072 sc1\n"
"ds_write_b128 v2, v[28:31] offset:31744\n"
"v_mfma_f32_16x16x32_bf16 v[224:227], a[52:55], a[148:151], v[224:227]\n"
"global_load_dwordx4 a[44:47], v0, s[34:35] offset:-1024 sc1\n"
"v_mfma_f32_16x16x32_bf16 v[252:255], a[60:63], a[148:151], v[252:255]\n"
"ds_read_b128 a[148:151], v3 offset:13312\n"
"v_mfma_f32_16x16x32_bf16 v[252:255], a[56:59], a[144:147], v[252:255]\n"
"ds_read_b128 a[144:147], v3 offset:12288\n"
"v_mfma_f32_16x16x32_bf16 a[28:31], a[56:59], a[152:155], a[28:31]\n"
"v_mfma_f32_16x16x32_bf16 a[28:31], a[60:63], a[156:159], a[28:31]\n"
"v_mfma_f32_16x16x32_bf16 a[24:27], a[52:55], a[156:159], a[24:27]\n"
"ds_read_b128 a[156:159], v3 offset:15360\n"
"v_mfma_f32_16x16x32_bf16 a[24:27], a[48:51], a[152:155], a[24:27]\n"
"s_waitcnt vmcnt(11) lgkmcnt(12)\n"
"ds_read_b128 a[152:155], v3 offset:14336\n"
"v_mfma_f32_16x16x32_bf16 v[144:147], a[64:67], a[96:99], v[144:147]\n"
"ds_read_b128 v[24:27], v2 offset:16384\n"
"v_mfma_f32_16x16x32_bf16 v[144:147], a[68:71], a[100:103], v[144:147]\n"
"global_load_dwordx4 a[48:51], v0, s[34:35] offset:0 sc1\n"
"v_mfma_f32_16x16x32_bf16 v[172:175], a[76:79], a[100:103], v[172:175]\n"
"v_mfma_f32_16x16x32_bf16 v[172:175], a[72:75], a[96:99], v[172:175]\n"
"v_mfma_f32_16x16x32_bf16 v[176:179], a[72:75], a[104:107], v[176:179]\n"
"v_mfma_f32_16x16x32_bf16 v[176:179], a[76:79], a[108:111], v[176:179]\n"
"v_mfma_f32_16x16x32_bf16 v[148:151], a[68:71], a[108:111], v[148:151]\n"
"v_mfma_f32_16x16x32_bf16 v[148:151], a[64:67], a[104:107], v[148:151]\n"
"s_waitcnt lgkmcnt(10)\n"
"v_mfma_f32_16x16x32_bf16 v[152:155], a[64:67], a[112:115], v[152:155]\n"
"v_mfma_f32_16x16x32_bf16 v[152:155], a[68:71], a[116:119], v[152:155]\n"
"global_load_dwordx4 a[52:55], v0, s[34:35] offset:1024 sc1\n"
"v_mfma_f32_16x16x32_bf16 v[180:183], a[76:79], a[116:119], v[180:183]\n"
"v_mfma_f32_16x16x32_bf16 v[180:183], a[72:75], a[112:115], v[180:183]\n"
"v_mfma_f32_16x16x32_bf16 v[184:187], a[72:75], a[120:123], v[184:187]\n"
"s_waitcnt lgkmcnt(0)\n"
"global_store_dwordx4 v0, v[24:27], s[52:53] offset:-4096 sc1\n"
"v_mfma_f32_16x16x32_bf16 v[184:187], a[76:79], a[124:127], v[184:187]\n"
"v_mfma_f32_16x16x32_bf16 v[156:159], a[68:71], a[124:127], v[156:159]\n"
"v_mfma_f32_16x16x32_bf16 v[156:159], a[64:67], a[120:123], v[156:159]\n"
"v_mfma_f32_16x16x32_bf16 v[160:163], a[64:67], a[128:131], v[160:163]\n"
"v_mfma_f32_16x16x32_bf16 v[160:163], a[68:71], a[132:135], v[160:163]\n"
"global_load_dwordx4 a[56:59], v0, s[34:35] offset:2048 sc1\n"
"v_mfma_f32_16x16x32_bf16 v[188:191], a[76:79], a[132:135], v[188:191]\n"
"v_mfma_f32_16x16x32_bf16 v[188:191], a[72:75], a[128:131], v[188:191]\n"
"v_mfma_f32_16x16x32_bf16 v[192:195], a[72:75], a[136:139], v[192:195]\n"
"v_mfma_f32_16x16x32_bf16 v[192:195], a[76:79], a[140:143], v[192:195]\n"
"v_mfma_f32_16x16x32_bf16 v[164:167], a[68:71], a[140:143], v[164:167]\n"
"v_mfma_f32_16x16x32_bf16 v[164:167], a[64:67], a[136:139], v[164:167]\n"
"v_mfma_f32_16x16x32_bf16 v[168:171], a[64:67], a[144:147], v[168:171]\n"
"v_mfma_f32_16x16x32_bf16 v[168:171], a[68:71], a[148:151], v[168:171]\n"
"global_load_dwordx4 a[60:63], v0, s[34:35] offset:3072 sc1\n"
"v_mfma_f32_16x16x32_bf16 v[196:199], a[76:79], a[148:151], v[196:199]\n"
"v_mfma_f32_16x16x32_bf16 v[196:199], a[72:75], a[144:147], v[196:199]\n"
"v_mfma_f32_16x16x32_bf16 a[20:23], a[72:75], a[152:155], a[20:23]\n"
"s_add_u32 s34, s34, 8192\n"
"v_mfma_f32_16x16x32_bf16 a[20:23], a[76:79], a[156:159], a[20:23]\n"
"v_mfma_f32_16x16x32_bf16 a[16:19], a[68:71], a[156:159], a[16:19]\n"
"v_mfma_f32_16x16x32_bf16 a[16:19], a[64:67], a[152:155], a[16:19]\n"
"s_waitcnt vmcnt(11)\n"
"ds_read_b128 v[28:31], v2 offset:17408\n"
"v_mfma_f32_16x16x32_bf16 v[200:203], a[80:83], a[96:99], v[200:203]\n"
"v_mfma_f32_16x16x32_bf16 v[200:203], a[84:87], a[100:103], v[200:203]\n"
"global_load_dwordx4 a[64:67], v0, s[34:35] offset:-4096 sc1\n"
"v_mfma_f32_16x16x32_bf16 v[228:231], a[92:95], a[100:103], v[228:231]\n"
"ds_read_b128 a[100:103], v4 offset:1024\n"
"v_mfma_f32_16x16x32_bf16 v[228:231], a[88:91], a[96:99], v[228:231]\n"
"ds_read_b128 a[96:99], v4 offset:0\n"
"v_mfma_f32_16x16x32_bf16 v[232:235], a[88:91], a[104:107], v[232:235]\n"
"v_mfma_f32_16x16x32_bf16 v[232:235], a[92:95], a[108:111], v[232:235]\n"
"v_mfma_f32_16x16x32_bf16 v[204:207], a[84:87], a[108:111], v[204:207]\n"
"ds_read_b128 a[108:111], v4 offset:3072\n"
"v_mfma_f32_16x16x32_bf16 v[204:207], a[80:83], a[104:107], v[204:207]\n"
"ds_read_b128 a[104:107], v4 offset:2048\n"
"v_mfma_f32_16x16x32_bf16 v[208:211], a[80:83], a[112:115], v[208:211]\n"
"v_mfma_f32_16x16x32_bf16 v[208:211], a[84:87], a[116:119], v[208:211]\n"
"global_load_dwordx4 a[68:71], v0, s[34:35] offset:-3072 sc1\n"
"v_mfma_f32_16x16x32_bf16 v[236:239], a[92:95], a[116:119], v[236:239]\n"
"s_waitcnt lgkmcnt(4)\n"
"global_store_dwordx4 v0, v[28:31], s[52:53] offset:-3072 sc1\n"
"ds_read_b128 a[116:119], v4 offset:5120\n"
"v_mfma_f32_16x16x32_bf16 v[236:239], a[88:91], a[112:115], v[236:239]\n"
"ds_read_b128 a[112:115], v4 offset:4096\n"
"v_mfma_f32_16x16x32_bf16 v[240:243], a[88:91], a[120:123], v[240:243]\n"
"v_mfma_f32_16x16x32_bf16 v[240:243], a[92:95], a[124:127], v[240:243]\n"
"v_mfma_f32_16x16x32_bf16 v[212:215], a[84:87], a[124:127], v[212:215]\n"
"ds_read_b128 a[124:127], v4 offset:7168\n"
"v_mfma_f32_16x16x32_bf16 v[212:215], a[80:83], a[120:123], v[212:215]\n"
"ds_read_b128 a[120:123], v4 offset:6144\n"
"v_mfma_f32_16x16x32_bf16 v[216:219], a[80:83], a[128:131], v[216:219]\n"
"v_mfma_f32_16x16x32_bf16 v[216:219], a[84:87], a[132:135], v[216:219]\n"
"global_load_dwordx4 a[72:75], v0, s[34:35] offset:-2048 sc1\n"
"v_mfma_f32_16x16x32_bf16 v[244:247], a[92:95], a[132:135], v[244:247]\n"
"ds_read_b128 a[132:135], v4 offset:9216\n"
"v_mfma_f32_16x16x32_bf16 v[244:247], a[88:91], a[128:131], v[244:247]\n"
"ds_read_b128 a[128:131], v4 offset:8192\n"
"v_mfma_f32_16x16x32_bf16 v[248:251], a[88:91], a[136:139], v[248:251]\n"
"v_mfma_f32_16x16x32_bf16 v[248:251], a[92:95], a[140:143], v[248:251]\n"
"v_mfma_f32_16x16x32_bf16 v[220:223], a[84:87], a[140:143], v[220:223]\n"
"ds_read_b128 a[140:143], v4 offset:11264\n"
"v_mfma_f32_16x16x32_bf16 v[220:223], a[80:83], a[136:139], v[220:223]\n"
"ds_read_b128 a[136:139], v4 offset:10240\n"
"v_mfma_f32_16x16x32_bf16 v[224:227], a[80:83], a[144:147], v[224:227]\n"
"v_mfma_f32_16x16x32_bf16 v[224:227], a[84:87], a[148:151], v[224:227]\n"
"global_load_dwordx4 a[76:79], v0, s[34:35] offset:-1024 sc1\n"
"v_mfma_f32_16x16x32_bf16 v[252:255], a[92:95], a[148:151], v[252:255]\n"
"ds_read_b128 a[148:151], v4 offset:13312\n"
"v_mfma_f32_16x16x32_bf16 v[252:255], a[88:91], a[144:147], v[252:255]\n"
"ds_read_b128 a[144:147], v4 offset:12288\n"
"v_mfma_f32_16x16x32_bf16 a[28:31], a[88:91], a[152:155], a[28:31]\n"
"v_mfma_f32_16x16x32_bf16 a[28:31], a[92:95], a[156:159], a[28:31]\n"
"ds_read_b128 v[24:27], v2 offset:18432\n"
"v_mfma_f32_16x16x32_bf16 a[24:27], a[84:87], a[156:159], a[24:27]\n"
"ds_read_b128 a[156:159], v4 offset:15360\n"
"v_mfma_f32_16x16x32_bf16 a[24:27], a[80:83], a[152:155], a[24:27]\n"
"s_waitcnt vmcnt(10) lgkmcnt(12)\n"
"ds_read_b128 a[152:155], v4 offset:14336\n"
"v_mfma_f32_16x16x32_bf16 v[144:147], a[32:35], a[96:99], v[144:147]\n"
"v_mfma_f32_16x16x32_bf16 v[144:147], a[36:39], a[100:103], v[144:147]\n"
"global_load_dwordx4 a[80:83], v0, s[34:35] offset:0 sc1\n"
"v_mfma_f32_16x16x32_bf16 v[172:175], a[44:47], a[100:103], v[172:175]\n"
"v_mfma_f32_16x16x32_bf16 v[172:175], a[40:43], a[96:99], v[172:175]\n"
"v_mfma_f32_16x16x32_bf16 v[176:179], a[40:43], a[104:107], v[176:179]\n"
"v_mfma_f32_16x16x32_bf16 v[176:179], a[44:47], a[108:111], v[176:179]\n"
"v_mfma_f32_16x16x32_bf16 v[148:151], a[36:39], a[108:111], v[148:151]\n"
"v_mfma_f32_16x16x32_bf16 v[148:151], a[32:35], a[104:107], v[148:151]\n"
"s_waitcnt lgkmcnt(9)\n"
"v_mfma_f32_16x16x32_bf16 v[152:155], a[32:35], a[112:115], v[152:155]\n"
"s_waitcnt lgkmcnt(2)\n"
"global_store_dwordx4 v0, v[24:27], s[52:53] offset:-2048 sc1\n"
"v_mfma_f32_16x16x32_bf16 v[152:155], a[36:39], a[116:119], v[152:155]\n"
"global_load_dwordx4 a[84:87], v0, s[34:35] offset:1024 sc1\n"
"v_mfma_f32_16x16x32_bf16 v[180:183], a[44:47], a[116:119], v[180:183]\n"
"v_mfma_f32_16x16x32_bf16 v[180:183], a[40:43], a[112:115], v[180:183]\n"
"v_mfma_f32_16x16x32_bf16 v[184:187], a[40:43], a[120:123], v[184:187]\n"
"v_mfma_f32_16x16x32_bf16 v[184:187], a[44:47], a[124:127], v[184:187]\n"
"v_mfma_f32_16x16x32_bf16 v[156:159], a[36:39], a[124:127], v[156:159]\n"
"v_mfma_f32_16x16x32_bf16 v[156:159], a[32:35], a[120:123], v[156:159]\n"
"v_mfma_f32_16x16x32_bf16 v[160:163], a[32:35], a[128:131], v[160:163]\n"
"v_mfma_f32_16x16x32_bf16 v[160:163], a[36:39], a[132:135], v[160:163]\n"
"global_load_dwordx4 a[88:91], v0, s[34:35] offset:2048 sc1\n"
"v_mfma_f32_16x16x32_bf16 v[188:191], a[44:47], a[132:135], v[188:191]\n"
"v_mfma_f32_16x16x32_bf16 v[188:191], a[40:43], a[128:131], v[188:191]\n"
"v_mfma_f32_16x16x32_bf16 v[192:195], a[40:43], a[136:139], v[192:195]\n"
"v_mfma_f32_16x16x32_bf16 v[192:195], a[44:47], a[140:143], v[192:195]\n"
"v_mfma_f32_16x16x32_bf16 v[164:167], a[36:39], a[140:143], v[164:167]\n"
"v_mfma_f32_16x16x32_bf16 v[164:167], a[32:35], a[136:139], v[164:167]\n"
"s_waitcnt lgkmcnt(0)\n"
"v_mfma_f32_16x16x32_bf16 v[168:171], a[32:35], a[144:147], v[168:171]\n"
"v_mfma_f32_16x16x32_bf16 v[168:171], a[36:39], a[148:151], v[168:171]\n"
"global_load_dwordx4 a[92:95], v0, s[34:35] offset:3072 sc1\n"
"v_mfma_f32_16x16x32_bf16 v[196:199], a[44:47], a[148:151], v[196:199]\n"
"v_mfma_f32_16x16x32_bf16 v[196:199], a[40:43], a[144:147], v[196:199]\n"
"ds_read_b128 v[28:31], v2 offset:19456\n"
"v_mfma_f32_16x16x32_bf16 a[20:23], a[40:43], a[152:155], a[20:23]\n"
"s_add_u32 s34, s34, 8192\n"
"v_mfma_f32_16x16x32_bf16 a[20:23], a[44:47], a[156:159], a[20:23]\n"
"v_mfma_f32_16x16x32_bf16 a[16:19], a[36:39], a[156:159], a[16:19]\n"
"v_mfma_f32_16x16x32_bf16 a[16:19], a[32:35], a[152:155], a[16:19]\n"
"s_waitcnt vmcnt(10)\n"
"v_mfma_f32_16x16x32_bf16 v[200:203], a[48:51], a[96:99], v[200:203]\n"
"v_mfma_f32_16x16x32_bf16 v[200:203], a[52:55], a[100:103], v[200:203]\n"
"global_load_dwordx4 a[32:35], v0, s[34:35] offset:-4096 sc1\n"
"v_mfma_f32_16x16x32_bf16 v[228:231], a[60:63], a[100:103], v[228:231]\n"
"ds_read_b128 a[100:103], v5 offset:1024\n"
"v_mfma_f32_16x16x32_bf16 v[228:231], a[56:59], a[96:99], v[228:231]\n"
"ds_read_b128 a[96:99], v5 offset:0\n"
"v_mfma_f32_16x16x32_bf16 v[232:235], a[56:59], a[104:107], v[232:235]\n"
"v_mfma_f32_16x16x32_bf16 v[232:235], a[60:63], a[108:111], v[232:235]\n"
"v_mfma_f32_16x16x32_bf16 v[204:207], a[52:55], a[108:111], v[204:207]\n"
"s_waitcnt lgkmcnt(2)\n"
"global_store_dwordx4 v0, v[28:31], s[52:53] offset:-1024 sc1\n"
"ds_read_b128 a[108:111], v5 offset:3072\n"
"v_mfma_f32_16x16x32_bf16 v[204:207], a[48:51], a[104:107], v[204:207]\n"
"ds_read_b128 a[104:107], v5 offset:2048\n"
"v_mfma_f32_16x16x32_bf16 v[208:211], a[48:51], a[112:115], v[208:211]\n"
"v_mfma_f32_16x16x32_bf16 v[208:211], a[52:55], a[116:119], v[208:211]\n"
"global_load_dwordx4 a[36:39], v0, s[34:35] offset:-3072 sc1\n"
"v_mfma_f32_16x16x32_bf16 v[236:239], a[60:63], a[116:119], v[236:239]\n"
"ds_read_b128 a[116:119], v5 offset:5120\n"
"v_mfma_f32_16x16x32_bf16 v[236:239], a[56:59], a[112:115], v[236:239]\n"
"ds_read_b128 a[112:115], v5 offset:4096\n"
"v_mfma_f32_16x16x32_bf16 v[240:243], a[56:59], a[120:123], v[240:243]\n"
"v_mfma_f32_16x16x32_bf16 v[240:243], a[60:63], a[124:127], v[240:243]\n"
"v_mfma_f32_16x16x32_bf16 v[212:215], a[52:55], a[124:127], v[212:215]\n"
"ds_read_b128 a[124:127], v5 offset:7168\n"
"v_mfma_f32_16x16x32_bf16 v[212:215], a[48:51], a[120:123], v[212:215]\n"
"ds_read_b128 a[120:123], v5 offset:6144\n"
"v_mfma_f32_16x16x32_bf16 v[216:219], a[48:51], a[128:131], v[216:219]\n"
"v_mfma_f32_16x16x32_bf16 v[216:219], a[52:55], a[132:135], v[216:219]\n"
"global_load_dwordx4 a[40:43], v0, s[34:35] offset:-2048 sc1\n"
"v_mfma_f32_16x16x32_bf16 v[244:247], a[60:63], a[132:135], v[244:247]\n"
"ds_read_b128 a[132:135], v5 offset:9216\n"
"v_mfma_f32_16x16x32_bf16 v[244:247], a[56:59], a[128:131], v[244:247]\n"
"ds_read_b128 a[128:131], v5 offset:8192\n"
"v_mfma_f32_16x16x32_bf16 v[248:251], a[56:59], a[136:139], v[248:251]\n"
"v_mfma_f32_16x16x32_bf16 v[248:251], a[60:63], a[140:143], v[248:251]\n"
"v_mfma_f32_16x16x32_bf16 v[220:223], a[52:55], a[140:143], v[220:223]\n"
"ds_read_b128 a[140:143], v5 offset:11264\n"
"v_mfma_f32_16x16x32_bf16 v[220:223], a[48:51], a[136:139], v[220:223]\n"
"ds_read_b128 a[136:139], v5 offset:10240\n"
"v_mfma_f32_16x16x32_bf16 v[224:227], a[48:51], a[144:147], v[224:227]\n"
"ds_read_b128 v[24:27], v2 offset:20480\n"
"v_mfma_f32_16x16x32_bf16 v[224:227], a[52:55], a[148:151], v[224:227]\n"
"global_load_dwordx4 a[44:47], v0, s[34:35] offset:-1024 sc1\n"
"v_mfma_f32_16x16x32_bf16 v[252:255], a[60:63], a[148:151], v[252:255]\n"
"ds_read_b128 a[148:151], v5 offset:13312\n"
"v_mfma_f32_16x16x32_bf16 v[252:255], a[56:59], a[144:147], v[252:255]\n"
"ds_read_b128 a[144:147], v5 offset:12288\n"
"v_mfma_f32_16x16x32_bf16 a[28:31], a[56:59], a[152:155], a[28:31]\n"
"v_mfma_f32_16x16x32_bf16 a[28:31], a[60:63], a[156:159], a[28:31]\n"
"v_mfma_f32_16x16x32_bf16 a[24:27], a[52:55], a[156:159], a[24:27]\n"
"ds_read_b128 a[156:159], v5 offset:15360\n"
"v_mfma_f32_16x16x32_bf16 a[24:27], a[48:51], a[152:155], a[24:27]\n"
"s_waitcnt vmcnt(10) lgkmcnt(12)\n"
"ds_read_b128 a[152:155], v5 offset:14336\n"
"v_mfma_f32_16x16x32_bf16 v[144:147], a[64:67], a[96:99], v[144:147]\n"
"v_mfma_f32_16x16x32_bf16 v[144:147], a[68:71], a[100:103], v[144:147]\n"
"global_load_dwordx4 a[48:51], v0, s[34:35] offset:0 sc1\n"
"v_mfma_f32_16x16x32_bf16 v[172:175], a[76:79], a[100:103], v[172:175]\n"
"v_mfma_f32_16x16x32_bf16 v[172:175], a[72:75], a[96:99], v[172:175]\n"
"v_mfma_f32_16x16x32_bf16 v[176:179], a[72:75], a[104:107], v[176:179]\n"
"s_waitcnt lgkmcnt(4)\n"
"global_store_dwordx4 v0, v[24:27], s[52:53] offset:0 sc1\n"
"v_mfma_f32_16x16x32_bf16 v[176:179], a[76:79], a[108:111], v[176:179]\n"
"v_mfma_f32_16x16x32_bf16 v[148:151], a[68:71], a[108:111], v[148:151]\n"
"v_mfma_f32_16x16x32_bf16 v[148:151], a[64:67], a[104:107], v[148:151]\n"
"v_mfma_f32_16x16x32_bf16 v[152:155], a[64:67], a[112:115], v[152:155]\n"
"v_mfma_f32_16x16x32_bf16 v[152:155], a[68:71], a[116:119], v[152:155]\n"
"global_load_dwordx4 a[52:55], v0, s[34:35] offset:1024 sc1\n"
"v_mfma_f32_16x16x32_bf16 v[180:183], a[76:79], a[116:119], v[180:183]\n"
"v_mfma_f32_16x16x32_bf16 v[180:183], a[72:75], a[112:115], v[180:183]\n"
"v_mfma_f32_16x16x32_bf16 v[184:187], a[72:75], a[120:123], v[184:187]\n"
"v_mfma_f32_16x16x32_bf16 v[184:187], a[76:79], a[124:127], v[184:187]\n"
"v_mfma_f32_16x16x32_bf16 v[156:159], a[68:71], a[124:127], v[156:159]\n"
"v_mfma_f32_16x16x32_bf16 v[156:159], a[64:67], a[120:123], v[156:159]\n"
"v_mfma_f32_16x16x32_bf16 v[160:163], a[64:67], a[128:131], v[160:163]\n"
"v_mfma_f32_16x16x32_bf16 v[160:163], a[68:71], a[132:135], v[160:163]\n"
"global_load_dwordx4 a[56:59], v0, s[34:35] offset:2048 sc1\n"
"v_mfma_f32_16x16x32_bf16 v[188:191], a[76:79], a[132:135], v[188:191]\n"
"v_mfma_f32_16x16x32_bf16 v[188:191], a[72:75], a[128:131], v[188:191]\n"
"v_mfma_f32_16x16x32_bf16 v[192:195], a[72:75], a[136:139], v[192:195]\n"
"v_mfma_f32_16x16x32_bf16 v[192:195], a[76:79], a[140:143], v[192:195]\n"
"v_mfma_f32_16x16x32_bf16 v[164:167], a[68:71], a[140:143], v[164:167]\n"
"v_mfma_f32_16x16x32_bf16 v[164:167], a[64:67], a[136:139], v[164:167]\n"
"s_waitcnt lgkmcnt(0)\n"
"ds_read_b128 v[28:31], v2 offset:21504\n"
"v_mfma_f32_16x16x32_bf16 v[168:171], a[64:67], a[144:147], v[168:171]\n"
"v_mfma_f32_16x16x32_bf16 v[168:171], a[68:71], a[148:151], v[168:171]\n"
"global_load_dwordx4 a[60:63], v0, s[34:35] offset:3072 sc1\n"
"v_mfma_f32_16x16x32_bf16 v[196:199], a[76:79], a[148:151], v[196:199]\n"
"v_mfma_f32_16x16x32_bf16 v[196:199], a[72:75], a[144:147], v[196:199]\n"
"v_mfma_f32_16x16x32_bf16 a[20:23], a[72:75], a[152:155], a[20:23]\n"
"s_add_u32 s34, s34, 8192\n"
"v_mfma_f32_16x16x32_bf16 a[20:23], a[76:79], a[156:159], a[20:23]\n"
"v_mfma_f32_16x16x32_bf16 a[16:19], a[68:71], a[156:159], a[16:19]\n"
"v_mfma_f32_16x16x32_bf16 a[16:19], a[64:67], a[152:155], a[16:19]\n"
"s_waitcnt vmcnt(10)\n"
"v_mfma_f32_16x16x32_bf16 v[200:203], a[80:83], a[96:99], v[200:203]\n"
"v_mfma_f32_16x16x32_bf16 v[200:203], a[84:87], a[100:103], v[200:203]\n"
"global_load_dwordx4 a[64:67], v0, s[34:35] offset:-4096 sc1\n"
"v_mfma_f32_16x16x32_bf16 v[228:231], a[92:95], a[100:103], v[228:231]\n"
"s_waitcnt lgkmcnt(0)\n"
"global_store_dwordx4 v0, v[28:31], s[52:53] offset:1024 sc1\n"
"ds_read_b128 a[100:103], v2 offset:17408\n"
"buffer_load_dwordx2 v[8:9], off, s[56:59], %14 sc1\n"
"v_mfma_f32_16x16x32_bf16 v[228:231], a[88:91], a[96:99], v[228:231]\n"
"ds_read_b128 a[96:99], v2 offset:16384\n"
"v_mfma_f32_16x16x32_bf16 v[232:235], a[88:91], a[104:107], v[232:235]\n"
"v_mfma_f32_16x16x32_bf16 v[232:235], a[92:95], a[108:111], v[232:235]\n"
"v_mfma_f32_16x16x32_bf16 v[204:207], a[84:87], a[108:111], v[204:207]\n"
"ds_read_b128 a[108:111], v2 offset:19456\n"
"v_mfma_f32_16x16x32_bf16 v[204:207], a[80:83], a[104:107], v[204:207]\n"
"ds_read_b128 a[104:107], v2 offset:18432\n"
"v_mfma_f32_16x16x32_bf16 v[208:211], a[80:83], a[112:115], v[208:211]\n"
"v_mfma_f32_16x16x32_bf16 v[208:211], a[84:87], a[116:119], v[208:211]\n"
"global_load_dwordx4 a[68:71], v0, s[34:35] offset:-3072 sc1\n"
"v_mfma_f32_16x16x32_bf16 v[236:239], a[92:95], a[116:119], v[236:239]\n"
"ds_read_b128 a[116:119], v2 offset:21504\n"
"v_mfma_f32_16x16x32_bf16 v[236:239], a[88:91], a[112:115], v[236:239]\n"
"ds_read_b128 a[112:115], v2 offset:20480\n"
"v_mfma_f32_16x16x32_bf16 v[240:243], a[88:91], a[120:123], v[240:243]\n"
"v_mfma_f32_16x16x32_bf16 v[240:243], a[92:95], a[124:127], v[240:243]\n"
"v_mfma_f32_16x16x32_bf16 v[212:215], a[84:87], a[124:127], v[212:215]\n"
"ds_read_b128 a[124:127], v2 offset:23552\n"
"v_mfma_f32_16x16x32_bf16 v[212:215], a[80:83], a[120:123], v[212:215]\n"
"ds_read_b128 a[120:123], v2 offset:22528\n"
"v_mfma_f32_16x16x32_bf16 v[216:219], a[80:83], a[128:131], v[216:219]\n"
"v_mfma_f32_16x16x32_bf16 v[216:219], a[84:87], a[132:135], v[216:219]\n"
"global_load_dwordx4 a[72:75], v0, s[34:35] offset:-2048 sc1\n"
"v_mfma_f32_16x16x32_bf16 v[244:247], a[92:95], a[132:135], v[244:247]\n"
"ds_read_b128 a[132:135], v2 offset:25600\n"
"v_mfma_f32_16x16x32_bf16 v[244:247], a[88:91], a[128:131], v[244:247]\n"
"ds_read_b128 a[128:131], v2 offset:24576\n"
"v_mfma_f32_16x16x32_bf16 v[248:251], a[88:91], a[136:139], v[248:251]\n"
"v_mfma_f32_16x16x32_bf16 v[248:251], a[92:95], a[140:143], v[248:251]\n"
"ds_read_b128 v[24:27], v2 offset:22528\n"
"v_mfma_f32_16x16x32_bf16 v[220:223], a[84:87], a[140:143], v[220:223]\n"
"ds_read_b128 a[140:143], v2 offset:27648\n"
"v_mfma_f32_16x16x32_bf16 v[220:223], a[80:83], a[136:139], v[220:223]\n"
"ds_read_b128 a[136:139], v2 offset:26624\n"
"v_mfma_f32_16x16x32_bf16 v[224:227], a[80:83], a[144:147], v[224:227]\n"
"v_mfma_f32_16x16x32_bf16 v[224:227], a[84:87], a[148:151], v[224:227]\n"
"global_load_dwordx4 a[76:79], v0, s[34:35] offset:-1024 sc1\n"
"v_mfma_f32_16x16x32_bf16 v[252:255], a[92:95], a[148:151], v[252:255]\n"
"ds_read_b128 a[148:151], v2 offset:29696\n"
"v_mfma_f32_16x16x32_bf16 v[252:255], a[88:91], a[144:147], v[252:255]\n"
"ds_read_b128 a[144:147], v2 offset:28672\n"
"v_mfma_f32_16x16x32_bf16 a[28:31], a[88:91], a[152:155], a[28:31]\n"
"v_mfma_f32_16x16x32_bf16 a[28:31], a[92:95], a[156:159], a[28:31]\n"
"v_mfma_f32_16x16x32_bf16 a[24:27], a[84:87], a[156:159], a[24:27]\n"
"ds_read_b128 a[156:159], v2 offset:31744\n"
"v_mfma_f32_16x16x32_bf16 a[24:27], a[80:83], a[152:155], a[24:27]\n"
"s_waitcnt vmcnt(11) lgkmcnt(12)\n"
"ds_read_b128 a[152:155], v2 offset:30720\n"
"v_mfma_f32_16x16x32_bf16 v[32:35], a[32:35], a[96:99], v[32:35]\n"
"s_waitcnt lgkmcnt(6)\n"
"global_store_dwordx4 v0, v[24:27], s[52:53] offset:2048 sc1\n"
"v_mfma_f32_16x16x32_bf16 v[32:35], a[36:39], a[100:103], v[32:35]\n"
"global_load_dwordx4 a[80:83], v0, s[34:35] offset:0 sc1\n"
"L_P3:\n"
"v_mfma_f32_16x16x32_bf16 v[60:63], a[44:47], a[100:103], v[60:63]\n"
"s_barrier\n"
"v_mfma_f32_16x16x32_bf16 v[60:63], a[40:43], a[96:99], v[60:63]\n"
"v_mfma_f32_16x16x32_bf16 v[64:67], a[40:43], a[104:107], v[64:67]\n"
"v_mfma_f32_16x16x32_bf16 v[64:67], a[44:47], a[108:111], v[64:67]\n"
"v_mfma_f32_16x16x32_bf16 v[36:39], a[36:39], a[108:111], v[36:39]\n"
"v_mfma_f32_16x16x32_bf16 v[36:39], a[32:35], a[104:107], v[36:39]\n"
"v_mfma_f32_16x16x32_bf16 v[40:43], a[32:35], a[112:115], v[40:43]\n"
"v_mfma_f32_16x16x32_bf16 v[40:43], a[36:39], a[116:119], v[40:43]\n"
"global_load_dwordx4 a[84:87], v0, s[34:35] offset:1024 sc1\n"
"v_mfma_f32_16x16x32_bf16 v[68:71], a[44:47], a[116:119], v[68:71]\n"
"v_mfma_f32_16x16x32_bf16 v[68:71], a[40:43], a[112:115], v[68:71]\n"
"v_mfma_f32_16x16x32_bf16 v[72:75], a[40:43], a[120:123], v[72:75]\n"
"v_mfma_f32_16x16x32_bf16 v[72:75], a[44:47], a[124:127], v[72:75]\n"
"v_mfma_f32_16x16x32_bf16 v[44:47], a[36:39], a[124:127], v[44:47]\n"
"v_mfma_f32_16x16x32_bf16 v[44:47], a[32:35], a[120:123], v[44:47]\n"
"s_waitcnt lgkmcnt(4)\n"
"v_mfma_f32_16x16x32_bf16 v[48:51], a[32:35], a[128:131], v[48:51]\n"
"v_mfma_f32_16x16x32_bf16 v[48:51], a[36:39], a[132:135], v[48:51]\n"
"global_load_dwordx4 a[88:91], v0, s[34:35] offset:2048 sc1\n"
"v_lshrrev_b32 v16, 4, v0\n"
"v_mfma_f32_16x16x32_bf16 v[76:79], a[44:47], a[132:135], v[76:79]\n"
"v_and_b32 v16, 0x30, v16\n"
"v_mfma_f32_16x16x32_bf16 v[76:79], a[40:43], a[128:131], v[76:79]\n"
"ds_read_b128 v[28:31], v2 offset:23552\n"
"buffer_load_dwordx2 v[18:19], v16, s[44:47], s61 offen offset:0\n"
"v_mfma_f32_16x16x32_bf16 v[80:83], a[40:43], a[136:139], v[80:83]\n"
"v_mfma_f32_16x16x32_bf16 v[80:83], a[44:47], a[140:143], v[80:83]\n"
"v_mfma_f32_16x16x32_bf16 v[52:55], a[36:39], a[140:143], v[52:55]\n"
"v_mfma_f32_16x16x32_bf16 v[52:55], a[32:35], a[136:139], v[52:55]\n"
"s_waitcnt lgkmcnt(1)\n"
"v_mfma_f32_16x16x32_bf16 v[56:59], a[32:35], a[144:147], v[56:59]\n"
"v_mfma_f32_16x16x32_bf16 v[56:59], a[36:39], a[148:151], v[56:59]\n"
"global_load_dwordx4 a[92:95], v0, s[34:35] offset:3072 sc1\n"
"v_lshrrev_b32 v20, 4, v0\n"
"v_mfma_f32_16x16x32_bf16 v[84:87], a[44:47], a[148:151], v[84:87]\n"
"v_and_b32 v20, 0x30, v20\n"
"v_mfma_f32_16x16x32_bf16 v[84:87], a[40:43], a[144:147], v[84:87]\n"
"buffer_load_dwordx2 v[22:23], v20, s[44:47], s61 offen offset:8\n"
"v_mfma_f32_16x16x32_bf16 a[4:7], a[40:43], a[152:155], a[4:7]\n"
"s_add_u32 s34, s34, 8192\n"
"v_mfma_f32_16x16x32_bf16 a[4:7], a[44:47], a[156:159], a[4:7]\n"
"v_mfma_f32_16x16x32_bf16 a[0:3], a[36:39], a[156:159], a[0:3]\n"
"s_waitcnt lgkmcnt(0)\n"
"global_store_dwordx4 v0, v[28:31], s[52:53] offset:3072 sc1\n"
"v_mfma_f32_16x16x32_bf16 a[0:3], a[32:35], a[152:155], a[0:3]\n"
"s_waitcnt vmcnt(14)\n"
"v_mfma_f32_16x16x32_bf16 v[88:91], a[48:51], a[96:99], v[88:91]\n"
"v_mfma_f32_16x16x32_bf16 v[88:91], a[52:55], a[100:103], v[88:91]\n"
"global_load_dwordx4 a[32:35], v0, s[34:35] offset:-4096 sc1\n"
"v_mfma_f32_16x16x32_bf16 v[116:119], a[60:63], a[100:103], v[116:119]\n"
"v_mfma_f32_16x16x32_bf16 v[116:119], a[56:59], a[96:99], v[116:119]\n"
"v_mfma_f32_16x16x32_bf16 v[120:123], a[56:59], a[104:107], v[120:123]\n"
"v_mfma_f32_16x16x32_bf16 v[120:123], a[60:63], a[108:111], v[120:123]\n"
"v_mfma_f32_16x16x32_bf16 v[92:95], a[52:55], a[108:111], v[92:95]\n"
"v_mfma_f32_16x16x32_bf16 v[92:95], a[48:51], a[104:107], v[92:95]\n"
"v_mfma_f32_16x16x32_bf16 v[96:99], a[48:51], a[112:115], v[96:99]\n"
"v_mfma_f32_16x16x32_bf16 v[96:99], a[52:55], a[116:119], v[96:99]\n"
"global_load_dwordx4 a[36:39], v0, s[34:35] offset:-3072 sc1\n"
"v_mfma_f32_16x16x32_bf16 v[124:127], a[60:63], a[116:119], v[124:127]\n"
"v_mfma_f32_16x16x32_bf16 v[124:127], a[56:59], a[112:115], v[124:127]\n"
"v_mfma_f32_16x16x32_bf16 v[128:131], a[56:59], a[120:123], v[128:131]\n"
"v_mfma_f32_16x16x32_bf16 v[128:131], a[60:63], a[124:127], v[128:131]\n"
"v_mfma_f32_16x16x32_bf16 v[100:103], a[52:55], a[124:127], v[100:103]\n"
"v_mfma_f32_16x16x32_bf16 v[100:103], a[48:51], a[120:123], v[100:103]\n"
"v_mfma_f32_16x16x32_bf16 v[104:107], a[48:51], a[128:131], v[104:107]\n"
"ds_read_b128 v[24:27], v2 offset:24576\n"
"v_mfma_f32_16x16x32_bf16 v[104:107], a[52:55], a[132:135], v[104:107]\n"
"global_load_dwordx4 a[40:43], v0, s[34:35] offset:-2048 sc1\n"
"v_mfma_f32_16x16x32_bf16 v[132:135], a[60:63], a[132:135], v[132:135]\n"
"v_mfma_f32_16x16x32_bf16 v[132:135], a[56:59], a[128:131], v[132:135]\n"
"v_mfma_f32_16x16x32_bf16 v[136:139], a[56:59], a[136:139], v[136:139]\n"
"v_mfma_f32_16x16x32_bf16 v[136:139], a[60:63], a[140:143], v[136:139]\n"
"v_mfma_f32_16x16x32_bf16 v[108:111], a[52:55], a[140:143], v[108:111]\n"
"v_mfma_f32_16x16x32_bf16 v[108:111], a[48:51], a[136:139], v[108:111]\n"
"v_mfma_f32_16x16x32_bf16 v[112:115], a[48:51], a[144:147], v[112:115]\n"
"v_mfma_f32_16x16x32_bf16 v[112:115], a[52:55], a[148:151], v[112:115]\n"
"global_load_dwordx4 a[44:47], v0, s[34:35] offset:-1024 sc1\n"
"v_mfma_f32_16x16x32_bf16 v[140:143], a[60:63], a[148:151], v[140:143]\n"
"v_mfma_f32_16x16x32_bf16 v[140:143], a[56:59], a[144:147], v[140:143]\n"
"v_mfma_f32_16x16x32_bf16 a[12:15], a[56:59], a[152:155], a[12:15]\n"
"s_waitcnt lgkmcnt(0)\n"
"global_store_dwordx4 v0, v[24:27], s[54:55] offset:-4096 sc1\n"
"v_mfma_f32_16x16x32_bf16 a[12:15], a[60:63], a[156:159], a[12:15]\n"
"v_mfma_f32_16x16x32_bf16 a[8:11], a[52:55], a[156:159], a[8:11]\n"
"v_mfma_f32_16x16x32_bf16 a[8:11], a[48:51], a[152:155], a[8:11]\n"
"s_waitcnt vmcnt(13)\n"
"v_mfma_f32_16x16x32_bf16 v[144:147], a[64:67], a[96:99], v[144:147]\n"
"v_mfma_f32_16x16x32_bf16 v[144:147], a[68:71], a[100:103], v[144:147]\n"
"global_load_dwordx4 a[48:51], v0, s[34:35] offset:0 sc1\n"
"v_mfma_f32_16x16x32_bf16 v[172:175], a[76:79], a[100:103], v[172:175]\n"
"v_mfma_f32_16x16x32_bf16 v[172:175], a[72:75], a[96:99], v[172:175]\n"
"v_mfma_f32_16x16x32_bf16 v[176:179], a[72:75], a[104:107], v[176:179]\n"
"v_mfma_f32_16x16x32_bf16 v[176:179], a[76:79], a[108:111], v[176:179]\n"
"v_mfma_f32_16x16x32_bf16 v[148:151], a[68:71], a[108:111], v[148:151]\n"
"v_mfma_f32_16x16x32_bf16 v[148:151], a[64:67], a[104:107], v[148:151]\n"
"v_mfma_f32_16x16x32_bf16 v[152:155], a[64:67], a[112:115], v[152:155]\n"
"v_mfma_f32_16x16x32_bf16 v[152:155], a[68:71], a[116:119], v[152:155]\n"
"global_load_dwordx4 a[52:55], v0, s[34:35] offset:1024 sc1\n"
"v_mfma_f32_16x16x32_bf16 v[180:183], a[76:79], a[116:119], v[180:183]\n"
"v_mfma_f32_16x16x32_bf16 v[180:183], a[72:75], a[112:115], v[180:183]\n"
"v_mfma_f32_16x16x32_bf16 v[184:187], a[72:75], a[120:123], v[184:187]\n"
"v_mfma_f32_16x16x32_bf16 v[184:187], a[76:79], a[124:127], v[184:187]\n"
"v_mfma_f32_16x16x32_bf16 v[156:159], a[68:71], a[124:127], v[156:159]\n"
"v_mfma_f32_16x16x32_bf16 v[156:159], a[64:67], a[120:123], v[156:159]\n"
"ds_read_b128 v[28:31], v2 offset:26624\n"
"v_mfma_f32_16x16x32_bf16 v[160:163], a[64:67], a[128:131], v[160:163]\n"
"v_mfma_f32_16x16x32_bf16 v[160:163], a[68:71], a[132:135], v[160:163]\n"
"global_load_dwordx4 a[56:59], v0, s[34:35] offset:2048 sc1\n"
"v_mfma_f32_16x16x32_bf16 v[188:191], a[76:79], a[132:135], v[188:191]\n"
"v_mfma_f32_16x16x32_bf16 v[188:191], a[72:75], a[128:131], v[188:191]\n"
"v_mfma_f32_16x16x32_bf16 v[192:195], a[72:75], a[136:139], v[192:195]\n"
"v_mfma_f32_16x16x32_bf16 v[192:195], a[76:79], a[140:143], v[192:195]\n"
"v_mfma_f32_16x16x32_bf16 v[164:167], a[68:71], a[140:143], v[164:167]\n"
"v_mfma_f32_16x16x32_bf16 v[164:167], a[64:67], a[136:139], v[164:167]\n"
"v_mfma_f32_16x16x32_bf16 v[168:171], a[64:67], a[144:147], v[168:171]\n"
"v_mfma_f32_16x16x32_bf16 v[168:171], a[68:71], a[148:151], v[168:171]\n"
"global_load_dwordx4 a[60:63], v0, s[34:35] offset:3072 sc1\n"
"v_mfma_f32_16x16x32_bf16 v[196:199], a[76:79], a[148:151], v[196:199]\n"
"s_waitcnt lgkmcnt(0)\n"
"global_store_dwordx4 v0, v[28:31], s[54:55] offset:-2048 sc1\n"
"v_mfma_f32_16x16x32_bf16 v[196:199], a[72:75], a[144:147], v[196:199]\n"
"v_mfma_f32_16x16x32_bf16 a[20:23], a[72:75], a[152:155], a[20:23]\n"
"s_add_u32 s34, s34, 8192\n"
"v_mfma_f32_16x16x32_bf16 a[20:23], a[76:79], a[156:159], a[20:23]\n"
"v_mfma_f32_16x16x32_bf16 a[16:19], a[68:71], a[156:159], a[16:19]\n"
"v_mfma_f32_16x16x32_bf16 a[16:19], a[64:67], a[152:155], a[16:19]\n"
"s_waitcnt vmcnt(12)\n"
"v_mfma_f32_16x16x32_bf16 v[200:203], a[80:83], a[96:99], v[200:203]\n"
"v_mfma_f32_16x16x32_bf16 v[200:203], a[84:87], a[100:103], v[200:203]\n"
"global_load_dwordx4 a[64:67], v0, s[34:35] offset:-4096 sc1\n"
"v_mfma_f32_16x16x32_bf16 v[228:231], a[92:95], a[100:103], v[228:231]\n"
"ds_read_b128 a[100:103], v3 offset:17408\n"
"v_mfma_f32_16x16x32_bf16 v[228:231], a[88:91], a[96:99], v[228:231]\n"
"ds_read_b128 a[96:99], v3 offset:16384\n"
"v_mfma_f32_16x16x32_bf16 v[232:235], a[88:91], a[104:107], v[232:235]\n"
"v_mfma_f32_16x16x32_bf16 v[232:235], a[92:95], a[108:111], v[232:235]\n"
"v_mfma_f32_16x16x32_bf16 v[204:207], a[84:87], a[108:111], v[204:207]\n"
"ds_read_b128 a[108:111], v3 offset:19456\n"
"v_mfma_f32_16x16x32_bf16 v[204:207], a[80:83], a[104:107], v[204:207]\n"
"ds_read_b128 a[104:107], v3 offset:18432\n"
"v_mfma_f32_16x16x32_bf16 v[208:211], a[80:83], a[112:115], v[208:211]\n"
"v_mfma_f32_16x16x32_bf16 v[208:211], a[84:87], a[116:119], v[208:211]\n"
"global_load_dwordx4 a[68:71], v0, s[34:35] offset:-3072 sc1\n"
"v_mfma_f32_16x16x32_bf16 v[236:239], a[92:95], a[116:119], v[236:239]\n"
"ds_read_b128 a[116:119], v3 offset:21504\n"
"v_mfma_f32_16x16x32_bf16 v[236:239], a[88:91], a[112:115], v[236:239]\n"
"ds_read_b128 a[112:115], v3 offset:20480\n"
"v_mfma_f32_16x16x32_bf16 v[240:243], a[88:91], a[120:123], v[240:243]\n"
"v_mfma_f32_16x16x32_bf16 v[240:243], a[92:95], a[124:127], v[240:243]\n"
"ds_read_b128 v[24:27], v2 offset:29696\n"
"v_mfma_f32_16x16x32_bf16 v[212:215], a[84:87], a[124:127], v[212:215]\n"
"ds_read_b128 a[124:127], v3 offset:23552\n"
"v_mfma_f32_16x16x32_bf16 v[212:215], a[80:83], a[120:123], v[212:215]\n"
"ds_read_b128 a[120:123], v3 offset:22528\n"
"v_mfma_f32_16x16x32_bf16 v[216:219], a[80:83], a[128:131], v[216:219]\n"
"v_mfma_f32_16x16x32_bf16 v[216:219], a[84:87], a[132:135], v[216:219]\n"
"global_load_dwordx4 a[72:75], v0, s[34:35] offset:-2048 sc1\n"
"v_mfma_f32_16x16x32_bf16 v[244:247], a[92:95], a[132:135], v[244:247]\n"
"ds_read_b128 a[132:135], v3 offset:25600\n"
"v_mfma_f32_16x16x32_bf16 v[244:247], a[88:91], a[128:131], v[244:247]\n"
"ds_read_b128 a[128:131], v3 offset:24576\n"
"v_mfma_f32_16x16x32_bf16 v[248:251], a[88:91], a[136:139], v[248:251]\n"
"v_subrev_u32 v8, s65, v8\n"
"v_mfma_f32_16x16x32_bf16 v[248:251], a[92:95], a[140:143], v[248:251]\n"
"v_xor_b32 v9, s79, v9\n"
"v_mfma_f32_16x16x32_bf16 v[220:223], a[84:87], a[140:143], v[220:223]\n"
"ds_read_b128 a[140:143], v3 offset:27648\n"
"v_or_b32 v8, v8, v9\n"
"v_mfma_f32_16x16x32_bf16 v[220:223], a[80:83], a[136:139], v[220:223]\n"
"ds_read_b128 a[136:139], v3 offset:26624\n"
"v_cmp_lt_u32 vcc, 15, v8\n"
"v_mfma_f32_16x16x32_bf16 v[224:227], a[80:83], a[144:147], v[224:227]\n"
"s_waitcnt lgkmcnt(6)\n"
"global_store_dwordx4 v0, v[24:27], s[54:55] offset:1024 sc1\n"
"s_cbranch_vccnz L_slow0\n"
"L_back0:\n"
"v_mfma_f32_16x16x32_bf16 v[224:227], a[84:87], a[148:151], v[224:227]\n"
"global_load_dwordx4 a[76:79], v0, s[34:35] offset:-1024 sc1\n"
"v_readfirstlane_b32 s66, v9\n"
"v_mfma_f32_16x16x32_bf16 v[252:255], a[92:95], a[148:151], v[252:255]\n"
"ds_read_b128 a[148:151], v3 offset:29696\n"
"v_mfma_f32_16x16x32_bf16 v[252:255], a[88:91], a[144:147], v[252:255]\n"
"s_and_b32 s66, s66, 3\n"
"ds_read_b128 a[144:147], v3 offset:28672\n"
"v_mfma_f32_16x16x32_bf16 a[28:31], a[88:91], a[152:155], a[28:31]\n"
"s_lshl_b32 s69, s66, 13\n"
"v_mfma_f32_16x16x32_bf16 a[28:31], a[92:95], a[156:159], a[28:31]\n"
"s_cmp_eq_u32 s66, 3\n"
"s_cselect_b32 s75, 8192, 0\n"
"v_mfma_f32_16x16x32_bf16 a[24:27], a[84:87], a[156:159], a[24:27]\n"
"ds_read_b128 a[156:159], v3 offset:31744\n"
"v_mfma_f32_16x16x32_bf16 a[24:27], a[80:83], a[152:155], a[24:27]\n"
"s_waitcnt vmcnt(11)\n"
"ds_read_b128 a[152:155], v3 offset:30720\n"
"v_mfma_f32_16x16x32_bf16 v[32:35], a[32:35], a[96:99], v[32:35]\n"
"v_mfma_f32_16x16x32_bf16 v[32:35], a[36:39], a[100:103], v[32:35]\n"
"global_load_dwordx4 a[80:83], v0, s[34:35] offset:0 sc1\n"
"v_mfma_f32_16x16x32_bf16 v[60:63], a[44:47], a[100:103], v[60:63]\n"
"v_mfma_f32_16x16x32_bf16 v[60:63], a[40:43], a[96:99], v[60:63]\n"
"v_mfma_f32_16x16x32_bf16 v[64:67], a[40:43], a[104:107], v[64:67]\n"
"v_mfma_f32_16x16x32_bf16 v[64:67], a[44:47], a[108:111], v[64:67]\n"
"v_mfma_f32_16x16x32_bf16 v[36:39], a[36:39], a[108:111], v[36:39]\n"
"s_add_u32 m0, s64, 0\n"
"v_mfma_f32_16x16x32_bf16 v[36:39], a[32:35], a[104:107], v[36:39]\n"
"s_waitcnt lgkmcnt(8)\n"
"v_mfma_f32_16x16x32_bf16 v[40:43], a[32:35], a[112:115], v[40:43]\n"
"v_mfma_f32_16x16x32_bf16 v[40:43], a[36:39], a[116:119], v[40:43]\n"
"global_load_dwordx4 a[84:87], v0, s[34:35] offset:1024 sc1\n"
"v_mfma_f32_16x16x32_bf16 v[68:71], a[44:47], a[116:119], v[68:71]\n"
"v_mfma_f32_16x16x32_bf16 v[68:71], a[40:43], a[112:115], v[68:71]\n"
"v_mfma_f32_16x16x32_bf16 v[72:75], a[40:43], a[120:123], v[72:75]\n"
"v_mfma_f32_16x16x32_bf16 v[72:75], a[44:47], a[124:127], v[72:75]\n"
"v_mfma_f32_16x16x32_bf16 v[44:47], a[36:39], a[124:127], v[44:47]\n"
"v_mfma_f32_16x16x32_bf16 v[44:47], a[32:35], a[120:123], v[44:47]\n"
"s_waitcnt lgkmcnt(4)\n"
"v_mfma_f32_16x16x32_bf16 v[48:51], a[32:35], a[128:131], v[48:51]\n"
"v_mfma_f32_16x16x32_bf16 v[48:51], a[36:39], a[132:135], v[48:51]\n"
"global_load_dwordx4 a[88:91], v0, s[34:35] offset:2048 sc1\n"
"v_mfma_f32_16x16x32_bf16 v[76:79], a[44:47], a[132:135], v[76:79]\n"
"v_mfma_f32_16x16x32_bf16 v[76:79], a[40:43], a[128:131], v[76:79]\n"
"v_mfma_f32_16x16x32_bf16 v[80:83], a[40:43], a[136:139], v[80:83]\n"
"v_mfma_f32_16x16x32_bf16 v[80:83], a[44:47], a[140:143], v[80:83]\n"
"buffer_load_dwordx4 v0, s[48:51], s75 offen offset:0 sc1 lds\n"
"v_mfma_f32_16x16x32_bf16 v[52:55], a[36:39], a[140:143], v[52:55]\n"
"v_mfma_f32_16x16x32_bf16 v[52:55], a[32:35], a[136:139], v[52:55]\n"
"s_waitcnt lgkmcnt(0)\n"
"v_mfma_f32_16x16x32_bf16 v[56:59], a[32:35], a[144:147], v[56:59]\n"
"v_mfma_f32_16x16x32_bf16 v[56:59], a[36:39], a[148:151], v[56:59]\n"
"global_load_dwordx4 a[92:95], v0, s[34:35] offset:3072 sc1\n"
"v_mfma_f32_16x16x32_bf16 v[84:87], a[44:47], a[148:151], v[84:87]\n"
"v_mfma_f32_16x16x32_bf16 v[84:87], a[40:43], a[144:147], v[84:87]\n"
"v_mfma_f32_16x16x32_bf16 a[4:7], a[40:43], a[152:155], a[4:7]\n"
"s_add_u32 s34, s34, 8192\n"
"v_mfma_f32_16x16x32_bf16 a[4:7], a[44:47], a[156:159], a[4:7]\n"
"v_mfma_f32_16x16x32_bf16 a[0:3], a[36:39], a[156:159], a[0:3]\n"
"v_mfma_f32_16x16x32_bf16 a[0:3], a[32:35], a[152:155], a[0:3]\n"
"s_waitcnt vmcnt(11)\n"
"v_mfma_f32_16x16x32_bf16 v[88:91], a[48:51], a[96:99], v[88:91]\n"
"v_mfma_f32_16x16x32_bf16 v[88:91], a[52:55], a[100:103], v[88:91]\n"
"global_load_dwordx4 a[32:35], v0, s[34:35] offset:-4096 sc1\n"
"v_mfma_f32_16x16x32_bf16 v[116:119], a[60:63], a[100:103], v[116:119]\n"
"v_mfma_f32_16x16x32_bf16 v[116:119], a[56:59], a[96:99], v[116:119]\n"
"buffer_load_dwordx4 v0, s[48:51], s75 offen offset:1024 sc1 lds\n"
"v_mfma_f32_16x16x32_bf16 v[120:123], a[56:59], a[104:107], v[120:123]\n"
"v_mfma_f32_16x16x32_bf16 v[120:123], a[60:63], a[108:111], v[120:123]\n"
"v_mfma_f32_16x16x32_bf16 v[92:95], a[52:55], a[108:111], v[92:95]\n"
"v_mfma_f32_16x16x32_bf16 v[92:95], a[48:51], a[104:107], v[92:95]\n"
"v_mfma_f32_16x16x32_bf16 v[96:99], a[48:51], a[112:115], v[96:99]\n"
"v_mfma_f32_16x16x32_bf16 v[96:99], a[52:55], a[116:119], v[96:99]\n"
"global_load_dwordx4 a[36:39], v0, s[34:35] offset:-3072 sc1\n"
"v_mfma_f32_16x16x32_bf16 v[124:127], a[60:63], a[116:119], v[124:127]\n"
"v_mfma_f32_16x16x32_bf16 v[124:127], a[56:59], a[112:115], v[124:127]\n"
"v_mfma_f32_16x16x32_bf16 v[128:131], a[56:59], a[120:123], v[128:131]\n"
"v_mfma_f32_16x16x32_bf16 v[128:131], a[60:63], a[124:127], v[128:131]\n"
"v_mfma_f32_16x16x32_bf16 v[100:103], a[52:55], a[124:127], v[100:103]\n"
"v_mfma_f32_16x16x32_bf16 v[100:103], a[48:51], a[120:123], v[100:103]\n"
"v_mfma_f32_16x16x32_bf16 v[104:107], a[48:51], a[128:131], v[104:107]\n"
"v_mfma_f32_16x16x32_bf16 v[104:107], a[52:55], a[132:135], v[104:107]\n"
"global_load_dwordx4 a[40:43], v0, s[34:35] offset:-2048 sc1\n"
"v_mfma_f32_16x16x32_bf16 v[132:135], a[60:63], a[132:135], v[132:135]\n"
"buffer_load_dwordx4 v0, s[48:51], s75 offen offset:2048 sc1 lds\n"
"v_mfma_f32_16x16x32_bf16 v[132:135], a[56:59], a[128:131], v[132:135]\n"
"v_mfma_f32_16x16x32_bf16 v[136:139], a[56:59], a[136:139], v[136:139]\n"
"v_mfma_f32_16x16x32_bf16 v[136:139], a[60:63], a[140:143], v[136:139]\n"
"v_mfma_f32_16x16x32_bf16 v[108:111], a[52:55], a[140:143], v[108:111]\n"
"v_mfma_f32_16x16x32_bf16 v[108:111], a[48:51], a[136:139], v[108:111]\n"
"v_mfma_f32_16x16x32_bf16 v[112:115], a[48:51], a[144:147], v[112:115]\n"
"v_mfma_f32_16x16x32_bf16 v[112:115], a[52:55], a[148:151], v[112:115]\n"
"global_load_dwordx4 a[44:47], v0, s[34:35] offset:-1024 sc1\n"
"v_mfma_f32_16x16x32_bf16 v[140:143], a[60:63], a[148:151], v[140:143]\n"
"v_mfma_f32_16x16x32_bf16 v[140:143], a[56:59], a[144:147], v[140:143]\n"
"v_mfma_f32_16x16x32_bf16 a[12:15], a[56:59], a[152:155], a[12:15]\n"
"v_mfma_f32_16x16x32_bf16 a[12:15], a[60:63], a[156:159], a[12:15]\n"
"v_mfma_f32_16x16x32_bf16 a[8:11], a[52:55], a[156:159], a[8:11]\n"
"v_mfma_f32_16x16x32_bf16 a[8:11], a[48:51], a[152:155], a[8:11]\n"
"s_waitcnt vmcnt(11)\n"
"v_mfma_f32_16x16x32_bf16 v[144:147], a[64:67], a[96:99], v[144:147]\n"
"buffer_load_dwordx4 v0, s[48:51], s75 offen offset:3072 sc1 lds\n"
"v_mfma_f32_16x16x32_bf16 v[144:147], a[68:71], a[100:103], v[144:147]\n"
"global_load_dwordx4 a[48:51], v0, s[34:35] offset:0 sc1\n"
"v_mfma_f32_16x16x32_bf16 v[172:175], a[76:79], a[100:103], v[172:175]\n"
"v_mfma_f32_16x16x32_bf16 v[172:175], a[72:75], a[96:99], v[172:175]\n"
"v_mfma_f32_16x16x32_bf16 v[176:179], a[72:75], a[104:107], v[176:179]\n"
"v_mfma_f32_16x16x32_bf16 v[176:179], a[76:79], a[108:111], v[176:179]\n"
"v_mfma_f32_16x16x32_bf16 v[148:151], a[68:71], a[108:111], v[148:151]\n"
"v_mfma_f32_16x16x32_bf16 v[148:151], a[64:67], a[104:107], v[148:151]\n"
"v_mfma_f32_16x16x32_bf16 v[152:155], a[64:67], a[112:115], v[152:155]\n"
"v_mfma_f32_16x16x32_bf16 v[152:155], a[68:71], a[116:119], v[152:155]\n"
"global_load_dwordx4 a[52:55], v0, s[34:35] offset:1024 sc1\n"
"v_mfma_f32_16x16x32_bf16 v[180:183], a[76:79], a[116:119], v[180:183]\n"
"v_mfma_f32_16x16x32_bf16 v[180:183], a[72:75], a[112:115], v[180:183]\n"
"v_mfma_f32_16x16x32_bf16 v[184:187], a[72:75], a[120:123], v[184:187]\n"
"v_mfma_f32_16x16x32_bf16 v[184:187], a[76:79], a[124:127], v[184:187]\n"
"v_mfma_f32_16x16x32_bf16 v[156:159], a[68:71], a[124:127], v[156:159]\n"
"v_mfma_f32_16x16x32_bf16 v[156:159], a[64:67], a[120:123], v[156:159]\n"
"s_add_u32 m0, s64, 4096\n"
"v_mfma_f32_16x16x32_bf16 v[160:163], a[64:67], a[128:131], v[160:163]\n"
"v_mfma_f32_16x16x32_bf16 v[160:163], a[68:71], a[132:135], v[160:163]\n"
"global_load_dwordx4 a[56:59], v0, s[34:35] offset:2048 sc1\n"
"v_mfma_f32_16x16x32_bf16 v[188:191], a[76:79], a[132:135], v[188:191]\n"
"v_mfma_f32_16x16x32_bf16 v[188:191], a[72:75], a[128:131], v[188:191]\n"
"v_mfma_f32_16x16x32_bf16 v[192:195], a[72:75], a[136:139], v[192:195]\n"
"v_mfma_f32_16x16x32_bf16 v[192:195], a[76:79], a[140:143], v[192:195]\n"
"v_mfma_f32_16x16x32_bf16 v[164:167], a[68:71], a[140:143], v[164:167]\n"
"v_mfma_f32_16x16x32_bf16 v[164:167], a[64:67], a[136:139], v[164:167]\n"
"v_mfma_f32_16x16x32_bf16 v[168:171], a[64:67], a[144:147], v[168:171]\n"
"v_mfma_f32_16x16x32_bf16 v[168:171], a[68:71], a[148:151], v[168:171]\n"
"global_load_dwordx4 a[60:63], v0, s[34:35] offset:3072 sc1\n"
"v_mfma_f32_16x16x32_bf16 v[196:199], a[76:79], a[148:151], v[196:199]\n"
"v_mfma_f32_16x16x32_bf16 v[196:199], a[72:75], a[144:147], v[196:199]\n"
"v_mfma_f32_16x16x32_bf16 a[20:23], a[72:75], a[152:155], a[20:23]\n"
"s_add_u32 s34, s34, 8192\n"
"v_mfma_f32_16x16x32_bf16 a[20:23], a[76:79], a[156:159], a[20:23]\n"
"v_mfma_f32_16x16x32_bf16 a[16:19], a[68:71], a[156:159], a[16:19]\n"
"buffer_load_dwordx4 v1, s[48:51], s75 offen offset:0 sc1 lds\n"
"v_mfma_f32_16x16x32_bf16 a[16:19], a[64:67], a[152:155], a[16:19]\n"
"s_waitcnt vmcnt(12)\n"
"v_mfma_f32_16x16x32_bf16 v[200:203], a[80:83], a[96:99], v[200:203]\n"
"v_mfma_f32_16x16x32_bf16 v[200:203], a[84:87], a[100:103], v[200:203]\n"
"global_load_dwordx4 a[64:67], v0, s[34:35] offset:-4096 sc1\n"
"v_mfma_f32_16x16x32_bf16 v[228:231], a[92:95], a[100:103], v[228:231]\n"
"ds_read_b128 a[100:103], v4 offset:17408\n"
"v_mfma_f32_16x16x32_bf16 v[228:231], a[88:91], a[96:99], v[228:231]\n"
"ds_read_b128 a[96:99], v4 offset:16384\n"
"v_mfma_f32_16x16x32_bf16 v[232:235], a[88:91], a[104:107], v[232:235]\n"
"v_mfma_f32_16x16x32_bf16 v[232:235], a[92:95], a[108:111], v[232:235]\n"
"v_mfma_f32_16x16x32_bf16 v[204:207], a[84:87], a[108:111], v[204:207]\n"
"ds_read_b128 a[108:111], v4 offset:19456\n"
"v_mfma_f32_16x16x32_bf16 v[204:207], a[80:83], a[104:107], v[204:207]\n"
"ds_read_b128 a[104:107], v4 offset:18432\n"
"v_mfma_f32_16x16x32_bf16 v[208:211], a[80:83], a[112:115], v[208:211]\n"
"v_mfma_f32_16x16x32_bf16 v[208:211], a[84:87], a[116:119], v[208:211]\n"
"global_load_dwordx4 a[68:71], v0, s[34:35] offset:-3072 sc1\n"
"v_mfma_f32_16x16x32_bf16 v[236:239], a[92:95], a[116:119], v[236:239]\n"
"ds_read_b128 a[116:119], v4 offset:21504\n"
"v_mfma_f32_16x16x32_bf16 v[236:239], a[88:91], a[112:115], v[236:239]\n"
"ds_read_b128 a[112:115], v4 offset:20480\n"
"v_mfma_f32_16x16x32_bf16 v[240:243], a[88:91], a[120:123], v[240:243]\n"
"v_mfma_f32_16x16x32_bf16 v[240:243], a[92:95], a[124:127], v[240:243]\n"
"buffer_load_dwordx4 v1, s[48:51], s75 offen offset:1024 sc1 lds\n"
"v_mfma_f32_16x16x32_bf16 v[212:215], a[84:87], a[124:127], v[212:215]\n"
"ds_read_b128 a[124:127], v4 offset:23552\n"
"v_mfma_f32_16x16x32_bf16 v[212:215], a[80:83], a[120:123], v[212:215]\n"
"ds_read_b128 a[120:123], v4 offset:22528\n"
"v_mfma_f32_16x16x32_bf16 v[216:219], a[80:83], a[128:131], v[216:219]\n"
"v_mfma_f32_16x16x32_bf16 v[216:219], a[84:87], a[132:135], v[216:219]\n"
"global_load_dwordx4 a[72:75], v0, s[34:35] offset:-2048 sc1\n"
"v_mfma_f32_16x16x32_bf16 v[244:247], a[92:95], a[132:135], v[244:247]\n"
"ds_read_b128 a[132:135], v4 offset:25600\n"
"v_mfma_f32_16x16x32_bf16 v[244:247], a[88:91], a[128:131], v[244:247]\n"
"ds_read_b128 a[128:131], v4 offset:24576\n"
"v_mfma_f32_16x16x32_bf16 v[248:251], a[88:91], a[136:139], v[248:251]\n"
"v_mfma_f32_16x16x32_bf16 v[248:251], a[92:95], a[140:143], v[248:251]\n"
"v_mfma_f32_16x16x32_bf16 v[220:223], a[84:87], a[140:143], v[220:223]\n"
"ds_read_b128 a[140:143], v4 offset:27648\n"
"v_mfma_f32_16x16x32_bf16 v[220:223], a[80:83], a[136:139], v[220:223]\n"
"ds_read_b128 a[136:139], v4 offset:26624\n"
"v_mfma_f32_16x16x32_bf16 v[224:227], a[80:83], a[144:147], v[224:227]\n"
"v_mfma_f32_16x16x32_bf16 v[224:227], a[84:87], a[148:151], v[224:227]\n"
"global_load_dwordx4 a[76:79], v0, s[34:35] offset:-1024 sc1\n"
"v_mfma_f32_16x16x32_bf16 v[252:255], a[92:95], a[148:151], v[252:255]\n"
"ds_read_b128 a[148:151], v4 offset:29696\n"
"v_mfma_f32_16x16x32_bf16 v[252:255], a[88:91], a[144:147], v[252:255]\n"
"ds_read_b128 a[144:147], v4 offset:28672\n"
"v_mfma_f32_16x16x32_bf16 a[28:31], a[88:91], a[152:155], a[28:31]\n"
"buffer_load_dwordx4 v1, s[48:51], s75 offen offset:2048 sc1 lds\n"
"v_mfma_f32_16x16x32_bf16 a[28:31], a[92:95], a[156:159], a[28:31]\n"
"v_mfma_f32_16x16x32_bf16 a[24:27], a[84:87], a[156:159], a[24:27]\n"
"ds_read_b128 a[156:159], v4 offset:31744\n"
"v_mfma_f32_16x16x32_bf16 a[24:27], a[80:83], a[152:155], a[24:27]\n"
"s_waitcnt vmcnt(12) lgkmcnt(11)\n"
"ds_read_b128 a[152:155], v4 offset:30720\n"
"v_mfma_f32_16x16x32_bf16 v[32:35], a[32:35], a[96:99], v[32:35]\n"
"v_mfma_f32_16x16x32_bf16 v[32:35], a[36:39], a[100:103], v[32:35]\n"
"global_load_dwordx4 a[80:83], v0, s[34:35] offset:0 sc1\n"
"v_mfma_f32_16x16x32_bf16 v[60:63], a[44:47], a[100:103], v[60:63]\n"
"v_mfma_f32_16x16x32_bf16 v[60:63], a[40:43], a[96:99], v[60:63]\n"
"v_mfma_f32_16x16x32_bf16 v[64:67], a[40:43], a[104:107], v[64:67]\n"
"v_mfma_f32_16x16x32_bf16 v[64:67], a[44:47], a[108:111], v[64:67]\n"
"v_mfma_f32_16x16x32_bf16 v[36:39], a[36:39], a[108:111], v[36:39]\n"
"v_mfma_f32_16x16x32_bf16 v[36:39], a[32:35], a[104:107], v[36:39]\n"
"s_waitcnt lgkmcnt(8)\n"
"v_mfma_f32_16x16x32_bf16 v[40:43], a[32:35], a[112:115], v[40:43]\n"
"v_mfma_f32_16x16x32_bf16 v[40:43], a[36:39], a[116:119], v[40:43]\n"
"global_load_dwordx4 a[84:87], v0, s[34:35] offset:1024 sc1\n"
"s_add_u32 s65, s65, 1\n"
"v_mfma_f32_16x16x32_bf16 v[68:71], a[44:47], a[116:119], v[68:71]\n"
"buffer_load_dwordx4 v1, s[48:51], s75 offen offset:3072 sc1 lds\n"
"v_mfma_f32_16x16x32_bf16 v[68:71], a[40:43], a[112:115], v[68:71]\n"
"v_mov_b32 v8, s65\n"
"v_mfma_f32_16x16x32_bf16 v[72:75], a[40:43], a[120:123], v[72:75]\n"
"v_mov_b32 v9, s60\n"
"v_mfma_f32_16x16x32_bf16 v[72:75], a[44:47], a[124:127], v[72:75]\n"
"v_mfma_f32_16x16x32_bf16 v[44:47], a[36:39], a[124:127], v[44:47]\n"
"buffer_store_dwordx2 v[8:9], off, s[56:59], %13 sc1\n"
"v_mfma_f32_16x16x32_bf16 v[44:47], a[32:35], a[120:123], v[44:47]\n"
"s_waitcnt lgkmcnt(4)\n"
"v_mfma_f32_16x16x32_bf16 v[48:51], a[32:35], a[128:131], v[48:51]\n"
"v_mfma_f32_16x16x32_bf16 v[48:51], a[36:39], a[132:135], v[48:51]\n"
"global_load_dwordx4 a[88:91], v0, s[34:35] offset:2048 sc1\n"
"v_mfma_f32_16x16x32_bf16 v[76:79], a[44:47], a[132:135], v[76:79]\n"
"v_mfma_f32_16x16x32_bf16 v[76:79], a[40:43], a[128:131], v[76:79]\n"
"v_mfma_f32_16x16x32_bf16 v[80:83], a[40:43], a[136:139], v[80:83]\n"
"v_mfma_f32_16x16x32_bf16 v[80:83], a[44:47], a[140:143], v[80:83]\n"
"v_mfma_f32_16x16x32_bf16 v[52:55], a[36:39], a[140:143], v[52:55]\n"
"v_mfma_f32_16x16x32_bf16 v[52:55], a[32:35], a[136:139], v[52:55]\n"
"s_waitcnt lgkmcnt(0)\n"
"v_mfma_f32_16x16x32_bf16 v[56:59], a[32:35], a[144:147], v[56:59]\n"
"v_mfma_f32_16x16x32_bf16 v[56:59], a[36:39], a[148:151], v[56:59]\n"
"s_add_u32 m0, s64, 8192\n"
"global_load_dwordx4 a[92:95], v0, s[34:35] offset:3072 sc1\n"
"v_mfma_f32_16x16x32_bf16 v[84:87], a[44:47], a[148:151], v[84:87]\n"
"v_mfma_f32_16x16x32_bf16 v[84:87], a[40:43], a[144:147], v[84:87]\n"
"v_mfma_f32_16x16x32_bf16 a[4:7], a[40:43], a[152:155], a[4:7]\n"
"s_add_u32 s34, s34, 8192\n"
"v_mfma_f32_16x16x32_bf16 a[4:7], a[44:47], a[156:159], a[4:7]\n"
"v_mfma_f32_16x16x32_bf16 a[0:3], a[36:39], a[156:159], a[0:3]\n"
"v_mfma_f32_16x16x32_bf16 a[0:3], a[32:35], a[152:155], a[0:3]\n"
"s_waitcnt vmcnt(13)\n"
"v_mfma_f32_16x16x32_bf16 v[88:91], a[48:51], a[96:99], v[88:91]\n"
"v_mfma_f32_16x16x32_bf16 v[88:91], a[52:55], a[100:103], v[88:91]\n"
"global_load_dwordx4 a[32:35], v0, s[34:35] offset:-4096 sc1\n"
"v_mfma_f32_16x16x32_bf16 v[116:119], a[60:63], a[100:103], v[116:119]\n"
"v_mfma_f32_16x16x32_bf16 v[116:119], a[56:59], a[96:99], v[116:119]\n"
"v_mfma_f32_16x16x32_bf16 v[120:123], a[56:59], a[104:107], v[120:123]\n"
"v_mfma_f32_16x16x32_bf16 v[120:123], a[60:63], a[108:111], v[120:123]\n"
"v_mfma_f32_16x16x32_bf16 v[92:95], a[52:55], a[108:111], v[92:95]\n"
"v_mfma_f32_16x16x32_bf16 v[92:95], a[48:51], a[104:107], v[92:95]\n"
"v_mfma_f32_16x16x32_bf16 v[96:99], a[48:51], a[112:115], v[96:99]\n"
"buffer_load_dwordx4 v0, s[48:51], s69 offen offset:0 sc1 lds\n"
"v_mfma_f32_16x16x32_bf16 v[96:99], a[52:55], a[116:119], v[96:99]\n"
"global_load_dwordx4 a[36:39], v0, s[34:35] offset:-3072 sc1\n"
"v_mfma_f32_16x16x32_bf16 v[124:127], a[60:63], a[116:119], v[124:127]\n"
"v_mfma_f32_16x16x32_bf16 v[124:127], a[56:59], a[112:115], v[124:127]\n"
"v_mfma_f32_16x16x32_bf16 v[128:131], a[56:59], a[120:123], v[128:131]\n"
"v_mfma_f32_16x16x32_bf16 v[128:131], a[60:63], a[124:127], v[128:131]\n"
"v_mfma_f32_16x16x32_bf16 v[100:103], a[52:55], a[124:127], v[100:103]\n"
"v_mfma_f32_16x16x32_bf16 v[100:103], a[48:51], a[120:123], v[100:103]\n"
"v_mfma_f32_16x16x32_bf16 v[104:107], a[48:51], a[128:131], v[104:107]\n"
"v_mfma_f32_16x16x32_bf16 v[104:107], a[52:55], a[132:135], v[104:107]\n"
"global_load_dwordx4 a[40:43], v0, s[34:35] offset:-2048 sc1\n"
"v_mfma_f32_16x16x32_bf16 v[132:135], a[60:63], a[132:135], v[132:135]\n"
"v_mfma_f32_16x16x32_bf16 v[132:135], a[56:59], a[128:131], v[132:135]\n"
"v_mfma_f32_16x16x32_bf16 v[136:139], a[56:59], a[136:139], v[136:139]\n"
"v_mfma_f32_16x16x32_bf16 v[136:139], a[60:63], a[140:143], v[136:139]\n"
"v_mfma_f32_16x16x32_bf16 v[108:111], a[52:55], a[140:143], v[108:111]\n"
"buffer_load_dwordx4 v0, s[48:51], s69 offen offset:1024 sc1 lds\n"
"v_mfma_f32_16x16x32_bf16 v[108:111], a[48:51], a[136:139], v[108:111]\n"
"v_mfma_f32_16x16x32_bf16 v[112:115], a[48:51], a[144:147], v[112:115]\n"
"v_mfma_f32_16x16x32_bf16 v[112:115], a[52:55], a[148:151], v[112:115]\n"
"global_load_dwordx4 a[44:47], v0, s[34:35] offset:-1024 sc1\n"
"v_mfma_f32_16x16x32_bf16 v[140:143], a[60:63], a[148:151], v[140:143]\n"
"v_mfma_f32_16x16x32_bf16 v[140:143], a[56:59], a[144:147], v[140:143]\n"
"v_mfma_f32_16x16x32_bf16 a[12:15], a[56:59], a[152:155], a[12:15]\n"
"v_mfma_f32_16x16x32_bf16 a[12:15], a[60:63], a[156:159], a[12:15]\n"
"v_mfma_f32_16x16x32_bf16 a[8:11], a[52:55], a[156:159], a[8:11]\n"
"v_mfma_f32_16x16x32_bf16 a[8:11], a[48:51], a[152:155], a[8:11]\n"
"s_waitcnt vmcnt(13)\n"
"v_mfma_f32_16x16x32_bf16 v[144:147], a[64:67], a[96:99], v[144:147]\n"
"v_mfma_f32_16x16x32_bf16 v[144:147], a[68:71], a[100:103], v[144:147]\n"
"global_load_dwordx4 a[48:51], v0, s[34:35] offset:0 sc1\n"
"v_mfma_f32_16x16x32_bf16 v[172:175], a[76:79], a[100:103], v[172:175]\n"
"v_mfma_f32_16x16x32_bf16 v[172:175], a[72:75], a[96:99], v[172:175]\n"
"v_mfma_f32_16x16x32_bf16 v[176:179], a[72:75], a[104:107], v[176:179]\n"
"v_mfma_f32_16x16x32_bf16 v[176:179], a[76:79], a[108:111], v[176:179]\n"
"buffer_load_dwordx4 v0, s[48:51], s69 offen offset:2048 sc1 lds\n"
"v_mfma_f32_16x16x32_bf16 v[148:151], a[68:71], a[108:111], v[148:151]\n"
"v_mfma_f32_16x16x32_bf16 v[148:151], a[64:67], a[104:107], v[148:151]\n"
"v_mfma_f32_16x16x32_bf16 v[152:155], a[64:67], a[112:115], v[152:155]\n"
"v_lshlrev_b32 v16, 16, v18\n"
"v_mfma_f32_16x16x32_bf16 v[152:155], a[68:71], a[116:119], v[152:155]\n"
"global_load_dwordx4 a[52:55], v0, s[34:35] offset:1024 sc1\n"
"v_and_b32 v17, s63, v18\n"
"v_mfma_f32_16x16x32_bf16 v[180:183], a[76:79], a[116:119], v[180:183]\n"
"v_lshlrev_b32 v18, 16, v19\n"
"v_mfma_f32_16x16x32_bf16 v[180:183], a[72:75], a[112:115], v[180:183]\n"
"v_and_b32 v19, s63, v19\n"
"v_mfma_f32_16x16x32_bf16 v[184:187], a[72:75], a[120:123], v[184:187]\n"
"v_add_f32 v32, v32, v16\n"
"v_mfma_f32_16x16x32_bf16 v[184:187], a[76:79], a[124:127], v[184:187]\n"
"v_add_f32 v33, v33, v17\n"
"v_add_f32 v34, v34, v18\n"
"v_mfma_f32_16x16x32_bf16 v[156:159], a[68:71], a[124:127], v[156:159]\n"
"v_add_f32 v35, v35, v19\n"
"v_mfma_f32_16x16x32_bf16 v[156:159], a[64:67], a[120:123], v[156:159]\n"
"v_add_f32 v36, v36, v16\n"
"v_mfma_f32_16x16x32_bf16 v[160:163], a[64:67], a[128:131], v[160:163]\n"
"v_add_f32 v37, v37, v17\n"
"v_mfma_f32_16x16x32_bf16 v[160:163], a[68:71], a[132:135], v[160:163]\n"
"global_load_dwordx4 a[56:59], v0, s[34:35] offset:2048 sc1\n"
"v_add_f32 v38, v38, v18\n"
"v_mfma_f32_16x16x32_bf16 v[188:191], a[76:79], a[132:135], v[188:191]\n"
"v_add_f32 v39, v39, v19\n"
"v_mfma_f32_16x16x32_bf16 v[188:191], a[72:75], a[128:131], v[188:191]\n"
"v_add_f32 v40, v40, v16\n"
"v_add_f32 v41, v41, v17\n"
"v_mfma_f32_16x16x32_bf16 v[192:195], a[72:75], a[136:139], v[192:195]\n"
"buffer_load_dwordx4 v0, s[48:51], s69 offen offset:3072 sc1 lds\n"
"v_mfma_f32_16x16x32_bf16 v[192:195], a[76:79], a[140:143], v[192:195]\n"
"v_add_f32 v42, v42, v18\n"
"v_mfma_f32_16x16x32_bf16 v[164:167], a[68:71], a[140:143], v[164:167]\n"
"v_add_f32 v43, v43, v19\n"
"v_mfma_f32_16x16x32_bf16 v[164:167], a[64:67], a[136:139], v[164:167]\n"
"v_add_f32 v44, v44, v16\n"
"v_add_f32 v45, v45, v17\n"
"v_mfma_f32_16x16x32_bf16 v[168:171], a[64:67], a[144:147], v[168:171]\n"
"v_add_f32 v46, v46, v18\n"
"v_mfma_f32_16x16x32_bf16 v[168:171], a[68:71], a[148:151], v[168:171]\n"
"global_load_dwordx4 a[60:63], v0, s[34:35] offset:3072 sc1\n"
"v_add_f32 v47, v47, v19\n"
"v_mfma_f32_16x16x32_bf16 v[196:199], a[76:79], a[148:151], v[196:199]\n"
"v_add_f32 v48, v48, v16\n"
"v_mfma_f32_16x16x32_bf16 v[196:199], a[72:75], a[144:147], v[196:199]\n"
"v_add_f32 v49, v49, v17\n"
"v_mfma_f32_16x16x32_bf16 a[20:23], a[72:75], a[152:155], a[20:23]\n"
"s_add_u32 s34, s34, 8192\n"
"v_add_f32 v50, v50, v18\n"
"v_mfma_f32_16x16x32_bf16 a[20:23], a[76:79], a[156:159], a[20:23]\n"
"v_add_f32 v51, v51, v19\n"
"v_mfma_f32_16x16x32_bf16 a[16:19], a[68:71], a[156:159], a[16:19]\n"
"v_add_f32 v52, v52, v16\n"
"v_mfma_f32_16x16x32_bf16 a[16:19], a[64:67], a[152:155], a[16:19]\n"
"s_waitcnt vmcnt(12)\n"
"v_add_f32 v53, v53, v17\n"
"v_mfma_f32_16x16x32_bf16 v[200:203], a[80:83], a[96:99], v[200:203]\n"
"v_add_f32 v54, v54, v18\n"
"v_add_f32 v55, v55, v19\n"
"v_mfma_f32_16x16x32_bf16 v[200:203], a[84:87], a[100:103], v[200:203]\n"
"global_load_dwordx4 a[64:67], v0, s[34:35] offset:-4096 sc1\n"
"v_add_f32 v56, v56, v16\n"
"v_mfma_f32_16x16x32_bf16 v[228:231], a[92:95], a[100:103], v[228:231]\n"
"s_add_u32 m0, s64, 12288\n"
"ds_read_b128 a[100:103], v5 offset:17408\n"
"v_add_f32 v57, v57, v17\n"
"v_mfma_f32_16x16x32_bf16 v[228:231], a[88:91], a[96:99], v[228:231]\n"
"ds_read_b128 a[96:99], v5 offset:16384\n"
"v_add_f32 v58, v58, v18\n"
"v_mfma_f32_16x16x32_bf16 v[232:235], a[88:91], a[104:107], v[232:235]\n"
"v_add_f32 v59, v59, v19\n"
"v_mfma_f32_16x16x32_bf16 v[232:235], a[92:95], a[108:111], v[232:235]\n"
"v_accvgpr_read_b32 v12, a0\n"
"v_mfma_f32_16x16x32_bf16 v[204:207], a[84:87], a[108:111], v[204:207]\n"
"ds_read_b128 a[108:111], v5 offset:19456\n"
"v_accvgpr_read_b32 v13, a1\n"
"v_mfma_f32_16x16x32_bf16 v[204:207], a[80:83], a[104:107], v[204:207]\n"
"ds_read_b128 a[104:107], v5 offset:18432\n"
"v_accvgpr_read_b32 v14, a2\n"
"v_mfma_f32_16x16x32_bf16 v[208:211], a[80:83], a[112:115], v[208:211]\n"
"v_accvgpr_read_b32 v15, a3\n"
"v_mfma_f32_16x16x32_bf16 v[208:211], a[84:87], a[116:119], v[208:211]\n"
"global_load_dwordx4 a[68:71], v0, s[34:35] offset:-3072 sc1\n"
"v_add_f32 v12, v12, v16\n"
"v_mfma_f32_16x16x32_bf16 v[236:239], a[92:95], a[116:119], v[236:239]\n"
"ds_read_b128 a[116:119], v5 offset:21504\n"
"v_add_f32 v13, v13, v17\n"
"v_mfma_f32_16x16x32_bf16 v[236:239], a[88:91], a[112:115], v[236:239]\n"
"ds_read_b128 a[112:115], v5 offset:20480\n"
"v_add_f32 v14, v14, v18\n"
"v_mfma_f32_16x16x32_bf16 v[240:243], a[88:91], a[120:123], v[240:243]\n"
"v_add_f32 v15, v15, v19\n"
"v_accvgpr_write_b32 a0, v12\n"
"v_mfma_f32_16x16x32_bf16 v[240:243], a[92:95], a[124:127], v[240:243]\n"
"v_accvgpr_write_b32 a1, v13\n"
"v_mfma_f32_16x16x32_bf16 v[212:215], a[84:87], a[124:127], v[212:215]\n"
"ds_read_b128 a[124:127], v5 offset:23552\n"
"v_accvgpr_write_b32 a2, v14\n"
"v_mfma_f32_16x16x32_bf16 v[212:215], a[80:83], a[120:123], v[212:215]\n"
"ds_read_b128 a[120:123], v5 offset:22528\n"
"v_accvgpr_write_b32 a3, v15\n"
"v_mfma_f32_16x16x32_bf16 v[216:219], a[80:83], a[128:131], v[216:219]\n"
"buffer_load_dwordx4 v1, s[48:51], s69 offen offset:0 sc1 lds\n"
"v_mfma_f32_16x16x32_bf16 v[216:219], a[84:87], a[132:135], v[216:219]\n"
"global_load_dwordx4 a[72:75], v0, s[34:35] offset:-2048 sc1\n"
"v_lshrrev_b32 v16, 4, v0\n"
"v_mfma_f32_16x16x32_bf16 v[244:247], a[92:95], a[132:135], v[244:247]\n"
"ds_read_b128 a[132:135], v5 offset:25600\n"
"v_and_b32 v16, 0x30, v16\n"
"v_mfma_f32_16x16x32_bf16 v[244:247], a[88:91], a[128:131], v[244:247]\n"
"ds_read_b128 a[128:131], v5 offset:24576\n"
"buffer_load_dwordx2 v[18:19], v16, s[44:47], s61 offen offset:64\n"
"v_mfma_f32_16x16x32_bf16 v[248:251], a[88:91], a[136:139], v[248:251]\n"
"s_getpc_b64 s[96:97]\n"
"L_c3_:\n"
"s_add_u32 s96, s96, L_last-L_c3_\n"
"s_addc_u32 s97, s97, 0\n"
"v_mfma_f32_16x16x32_bf16 v[248:251], a[92:95], a[140:143], v[248:251]\n"
"s_lshr_b32 s66, %26, 3\n"
"v_mfma_f32_16x16x32_bf16 v[220:223], a[84:87], a[140:143], v[220:223]\n"
"s_lshl2_add_u32 s66, s66, %16\n"
"ds_read_b128 a[140:143], v5 offset:27648\n"
"v_mfma_f32_16x16x32_bf16 v[220:223], a[80:83], a[136:139], v[220:223]\n"
"ds_read_b128 a[136:139], v5 offset:26624\n"
"v_mfma_f32_16x16x32_bf16 v[224:227], a[80:83], a[144:147], v[224:227]\n"
"s_mul_i32 s66, s66, 256\n"
"v_mfma_f32_16x16x32_bf16 v[224:227], a[84:87], a[148:151], v[224:227]\n"
"global_load_dwordx4 a[76:79], v0, s[34:35] offset:-1024 sc1\n"
"v_mfma_f32_16x16x32_bf16 v[252:255], a[92:95], a[148:151], v[252:255]\n"
"ds_read_b128 a[148:151], v5 offset:29696\n"
"v_mfma_f32_16x16x32_bf16 v[252:255], a[88:91], a[144:147], v[252:255]\n"
"s_min_u32 s66, s66, L_headF-L_last-256\n"
"ds_read_b128 a[144:147], v5 offset:28672\n"
"v_mfma_f32_16x16x32_bf16 a[28:31], a[88:91], a[152:155], a[28:31]\n"
"v_mfma_f32_16x16x32_bf16 a[28:31], a[92:95], a[156:159], a[28:31]\n"
"v_min_u32 v11, 16, v0\n"
"v_mfma_f32_16x16x32_bf16 a[24:27], a[84:87], a[156:159], a[24:27]\n"
"ds_read_b128 a[156:159], v5 offset:31744\n"
"buffer_load_dwordx2 v[8:9], off, s[56:59], %14 sc1\n"
"v_mfma_f32_16x16x32_bf16 a[24:27], a[80:83], a[152:155], a[24:27]\n"
"s_waitcnt vmcnt(13) lgkmcnt(11)\n"
"ds_read_b128 a[152:155], v5 offset:30720\n"
"v_mfma_f32_16x16x32_bf16 v[32:35], a[32:35], a[96:99], v[32:35]\n"
"buffer_load_dwordx4 v1, s[48:51], s69 offen offset:1024 sc1 lds\n"
"v_mfma_f32_16x16x32_bf16 v[32:35], a[36:39], a[100:103], v[32:35]\n"
"global_load_dwordx4 a[80:83], v0, s[34:35] offset:0 sc1\n"
"v_mfma_f32_16x16x32_bf16 v[60:63], a[44:47], a[100:103], v[60:63]\n"
"v_lshl_add_u32 v11, v11, 3, s66\n"
"v_mfma_f32_16x16x32_bf16 v[60:63], a[40:43], a[96:99], v[60:63]\n"
"global_load_dword v10, v11, s[96:97]\n"
"v_mfma_f32_16x16x32_bf16 v[64:67], a[40:43], a[104:107], v[64:67]\n"
"v_mfma_f32_16x16x32_bf16 v[64:67], a[44:47], a[108:111], v[64:67]\n"
"v_mfma_f32_16x16x32_bf16 v[36:39], a[36:39], a[108:111], v[36:39]\n"
"v_mfma_f32_16x16x32_bf16 v[36:39], a[32:35], a[104:107], v[36:39]\n"
"s_waitcnt lgkmcnt(8)\n"
"v_mfma_f32_16x16x32_bf16 v[40:43], a[32:35], a[112:115], v[40:43]\n"
"v_mfma_f32_16x16x32_bf16 v[40:43], a[36:39], a[116:119], v[40:43]\n"
"global_load_dwordx4 a[84:87], v0, s[34:35] offset:1024 sc1\n"
"v_mfma_f32_16x16x32_bf16 v[68:71], a[44:47], a[116:119], v[68:71]\n"
"v_mfma_f32_16x16x32_bf16 v[68:71], a[40:43], a[112:115], v[68:71]\n"
"v_mfma_f32_16x16x32_bf16 v[72:75], a[40:43], a[120:123], v[72:75]\n"
"v_mfma_f32_16x16x32_bf16 v[72:75], a[44:47], a[124:127], v[72:75]\n"
"v_mfma_f32_16x16x32_bf16 v[44:47], a[36:39], a[124:127], v[44:47]\n"
"buffer_load_dwordx4 v1, s[48:51], s69 offen offset:2048 sc1 lds\n"
"v_mfma_f32_16x16x32_bf16 v[44:47], a[32:35], a[120:123], v[44:47]\n"
"s_waitcnt lgkmcnt(4)\n"
"v_mfma_f32_16x16x32_bf16 v[48:51], a[32:35], a[128:131], v[48:51]\n"
"v_mfma_f32_16x16x32_bf16 v[48:51], a[36:39], a[132:135], v[48:51]\n"
"global_load_dwordx4 a[88:91], v0, s[34:35] offset:2048 sc1\n"
"v_mfma_f32_16x16x32_bf16 v[76:79], a[44:47], a[132:135], v[76:79]\n"
"v_mfma_f32_16x16x32_bf16 v[76:79], a[40:43], a[128:131], v[76:79]\n"
"v_mfma_f32_16x16x32_bf16 v[80:83], a[40:43], a[136:139], v[80:83]\n"
"v_mfma_f32_16x16x32_bf16 v[80:83], a[44:47], a[140:143], v[80:83]\n"
"v_mfma_f32_16x16x32_bf16 v[52:55], a[36:39], a[140:143], v[52:55]\n"
"v_mfma_f32_16x16x32_bf16 v[52:55], a[32:35], a[136:139], v[52:55]\n"
"s_waitcnt lgkmcnt(0)\n"
"v_mfma_f32_16x16x32_bf16 v[56:59], a[32:35], a[144:147], v[56:59]\n"
"v_mfma_f32_16x16x32_bf16 v[56:59], a[36:39], a[148:151], v[56:59]\n"
"global_load_dwordx4 a[92:95], v0, s[34:35] offset:3072 sc1\n"
"v_mfma_f32_16x16x32_bf16 v[84:87], a[44:47], a[148:151], v[84:87]\n"
"v_mfma_f32_16x16x32_bf16 v[84:87], a[40:43], a[144:147], v[84:87]\n"
"v_mfma_f32_16x16x32_bf16 a[4:7], a[40:43], a[152:155], a[4:7]\n"
"s_add_u32 s34, s34, 8192\n"
"v_mfma_f32_16x16x32_bf16 a[4:7], a[44:47], a[156:159], a[4:7]\n"
"buffer_load_dwordx4 v1, s[48:51], s69 offen offset:3072 sc1 lds\n"
"v_mfma_f32_16x16x32_bf16 a[0:3], a[36:39], a[156:159], a[0:3]\n"
"v_mfma_f32_16x16x32_bf16 a[0:3], a[32:35], a[152:155], a[0:3]\n"
"s_waitcnt vmcnt(15)\n"
"v_mfma_f32_16x16x32_bf16 v[88:91], a[48:51], a[96:99], v[88:91]\n"
"v_mfma_f32_16x16x32_bf16 v[88:91], a[52:55], a[100:103], v[88:91]\n"
"global_load_dwordx4 a[32:35], v0, s[34:35] offset:-4096 sc1\n"
"v_mfma_f32_16x16x32_bf16 v[116:119], a[60:63], a[100:103], v[116:119]\n"
"v_mfma_f32_16x16x32_bf16 v[116:119], a[56:59], a[96:99], v[116:119]\n"
"v_mfma_f32_16x16x32_bf16 v[120:123], a[56:59], a[104:107], v[120:123]\n"
"v_mfma_f32_16x16x32_bf16 v[120:123], a[60:63], a[108:111], v[120:123]\n"
"v_mfma_f32_16x16x32_bf16 v[92:95], a[52:55], a[108:111], v[92:95]\n"
"v_mfma_f32_16x16x32_bf16 v[92:95], a[48:51], a[104:107], v[92:95]\n"
"v_mfma_f32_16x16x32_bf16 v[96:99], a[48:51], a[112:115], v[96:99]\n"
"v_mfma_f32_16x16x32_bf16 v[96:99], a[52:55], a[116:119], v[96:99]\n"
"global_load_dwordx4 a[36:39], v0, s[34:35] offset:-3072 sc1\n"
"v_mfma_f32_16x16x32_bf16 v[124:127], a[60:63], a[116:119], v[124:127]\n"
"v_mfma_f32_16x16x32_bf16 v[124:127], a[56:59], a[112:115], v[124:127]\n"
"v_mfma_f32_16x16x32_bf16 v[128:131], a[56:59], a[120:123], v[128:131]\n"
"v_mfma_f32_16x16x32_bf16 v[128:131], a[60:63], a[124:127], v[128:131]\n"
"v_mfma_f32_16x16x32_bf16 v[100:103], a[52:55], a[124:127], v[100:103]\n"
"v_mfma_f32_16x16x32_bf16 v[100:103], a[48:51], a[120:123], v[100:103]\n"
"v_mfma_f32_16x16x32_bf16 v[104:107], a[48:51], a[128:131], v[104:107]\n"
"v_mfma_f32_16x16x32_bf16 v[104:107], a[52:55], a[132:135], v[104:107]\n"
"global_load_dwordx4 a[40:43], v0, s[34:35] offset:-2048 sc1\n"
"v_mfma_f32_16x16x32_bf16 v[132:135], a[60:63], a[132:135], v[132:135]\n"
"v_mfma_f32_16x16x32_bf16 v[132:135], a[56:59], a[128:131], v[132:135]\n"
"v_mfma_f32_16x16x32_bf16 v[136:139], a[56:59], a[136:139], v[136:139]\n"
"v_mfma_f32_16x16x32_bf16 v[136:139], a[60:63], a[140:143], v[136:139]\n"
"v_mfma_f32_16x16x32_bf16 v[108:111], a[52:55], a[140:143], v[108:111]\n"
"v_mfma_f32_16x16x32_bf16 v[108:111], a[48:51], a[136:139], v[108:111]\n"
"v_mfma_f32_16x16x32_bf16 v[112:115], a[48:51], a[144:147], v[112:115]\n"
"v_mfma_f32_16x16x32_bf16 v[112:115], a[52:55], a[148:151], v[112:115]\n"
"global_load_dwordx4 a[44:47], v0, s[34:35] offset:-1024 sc1\n"
"v_mfma_f32_16x16x32_bf16 v[140:143], a[60:63], a[148:151], v[140:143]\n"
"v_mfma_f32_16x16x32_bf16 v[140:143], a[56:59], a[144:147], v[140:143]\n"
"v_mfma_f32_16x16x32_bf16 a[12:15], a[56:59], a[152:155], a[12:15]\n"
"v_mfma_f32_16x16x32_bf16 a[12:15], a[60:63], a[156:159], a[12:15]\n"
"v_mfma_f32_16x16x32_bf16 a[8:11], a[52:55], a[156:159], a[8:11]\n"
"v_mfma_f32_16x16x32_bf16 a[8:11], a[48:51], a[152:155], a[8:11]\n"
"s_waitcnt vmcnt(13)\n"
"v_mfma_f32_16x16x32_bf16 v[144:147], a[64:67], a[96:99], v[144:147]\n"
"v_mfma_f32_16x16x32_bf16 v[144:147], a[68:71], a[100:103], v[144:147]\n"
"global_load_dwordx4 a[48:51], v0, s[34:35] offset:0 sc1\n"
"v_mfma_f32_16x16x32_bf16 v[172:175], a[76:79], a[100:103], v[172:175]\n"
"v_mfma_f32_16x16x32_bf16 v[172:175], a[72:75], a[96:99], v[172:175]\n"
"v_mfma_f32_16x16x32_bf16 v[176:179], a[72:75], a[104:107], v[176:179]\n"
"v_mfma_f32_16x16x32_bf16 v[176:179], a[76:79], a[108:111], v[176:179]\n"
"v_mfma_f32_16x16x32_bf16 v[148:151], a[68:71], a[108:111], v[148:151]\n"
"v_mfma_f32_16x16x32_bf16 v[148:151], a[64:67], a[104:107], v[148:151]\n"
"v_mfma_f32_16x16x32_bf16 v[152:155], a[64:67], a[112:115], v[152:155]\n"
"v_lshlrev_b32 v20, 16, v22\n"
"v_mfma_f32_16x16x32_bf16 v[152:155], a[68:71], a[116:119], v[152:155]\n"
"global_load_dwordx4 a[52:55], v0, s[34:35] offset:1024 sc1\n"
"v_and_b32 v21, s63, v22\n"
"v_mfma_f32_16x16x32_bf16 v[180:183], a[76:79], a[116:119], v[180:183]\n"
"v_lshlrev_b32 v22, 16, v23\n"
"v_mfma_f32_16x16x32_bf16 v[180:183], a[72:75], a[112:115], v[180:183]\n"
"v_and_b32 v23, s63, v23\n"
"v_mfma_f32_16x16x32_bf16 v[184:187], a[72:75], a[120:123], v[184:187]\n"
"v_add_f32 v60, v60, v20\n"
"v_mfma_f32_16x16x32_bf16 v[184:187], a[76:79], a[124:127], v[184:187]\n"
"v_add_f32 v61, v61, v21\n"
"v_add_f32 v62, v62, v22\n"
"v_mfma_f32_16x16x32_bf16 v[156:159], a[68:71], a[124:127], v[156:159]\n"
"v_add_f32 v63, v63, v23\n"
"v_mfma_f32_16x16x32_bf16 v[156:159], a[64:67], a[120:123], v[156:159]\n"
"v_add_f32 v64, v64, v20\n"
"v_mfma_f32_16x16x32_bf16 v[160:163], a[64:67], a[128:131], v[160:163]\n"
"v_add_f32 v65, v65, v21\n"
"v_mfma_f32_16x16x32_bf16 v[160:163], a[68:71], a[132:135], v[160:163]\n"
"global_load_dwordx4 a[56:59], v0, s[34:35] offset:2048 sc1\n"
"v_add_f32 v66, v66, v22\n"
"v_mfma_f32_16x16x32_bf16 v[188:191], a[76:79], a[132:135], v[188:191]\n"
"v_add_f32 v67, v67, v23\n"
"v_mfma_f32_16x16x32_bf16 v[188:191], a[72:75], a[128:131], v[188:191]\n"
"v_add_f32 v68, v68, v20\n"
"v_mfma_f32_16x16x32_bf16 v[192:195], a[72:75], a[136:139], v[192:195]\n"
"v_add_f32 v69, v69, v21\n"
"v_mfma_f32_16x16x32_bf16 v[192:195], a[76:79], a[140:143], v[192:195]\n"
"v_add_f32 v70, v70, v22\n"
"v_mfma_f32_16x16x32_bf16 v[164:167], a[68:71], a[140:143], v[164:167]\n"
"v_add_f32 v71, v71, v23\n"
"v_mfma_f32_16x16x32_bf16 v[164:167], a[64:67], a[136:139], v[164:167]\n"
"v_add_f32 v72, v72, v20\n"
"v_add_f32 v73, v73, v21\n"
"v_mfma_f32_16x16x32_bf16 v[168:171], a[64:67], a[144:147], v[168:171]\n"
"v_add_f32 v74, v74, v22\n"
"v_mfma_f32_16x16x32_bf16 v[168:171], a[68:71], a[148:151], v[168:171]\n"
"global_load_dwordx4 a[60:63], v0, s[34:35] offset:3072 sc1\n"
"v_add_f32 v75, v75, v23\n"
"v_mfma_f32_16x16x32_bf16 v[196:199], a[76:79], a[148:151], v[196:199]\n"
"v_add_f32 v76, v76, v20\n"
"v_mfma_f32_16x16x32_bf16 v[196:199], a[72:75], a[144:147], v[196:199]\n"
"v_add_f32 v77, v77, v21\n"
"v_mfma_f32_16x16x32_bf16 a[20:23], a[72:75], a[152:155], a[20:23]\n"
"s_add_u32 s34, s34, 8192\n"
"v_add_f32 v78, v78, v22\n"
"v_mfma_f32_16x16x32_bf16 a[20:23], a[76:79], a[156:159], a[20:23]\n"
"v_add_f32 v79, v79, v23\n"
"v_mfma_f32_16x16x32_bf16 a[16:19], a[68:71], a[156:159], a[16:19]\n"
"v_add_f32 v80, v80, v20\n"
"v_mfma_f32_16x16x32_bf16 a[16:19], a[64:67], a[152:155], a[16:19]\n"
"s_waitcnt vmcnt(9)\n"
"v_add_f32 v81, v81, v21\n"
"v_mfma_f32_16x16x32_bf16 v[200:203], a[80:83], a[96:99], v[200:203]\n"
"v_add_f32 v82, v82, v22\n"
"v_add_f32 v83, v83, v23\n"
"v_mfma_f32_16x16x32_bf16 v[200:203], a[84:87], a[100:103], v[200:203]\n"
"global_load_dwordx4 a[64:67], v0, s[34:35] offset:-4096 sc1\n"
"v_add_f32 v84, v84, v20\n"
"v_mfma_f32_16x16x32_bf16 v[228:231], a[92:95], a[100:103], v[228:231]\n"
"s_waitcnt vmcnt(9)\n"
"ds_read_b128 a[100:103], v2 offset:1024\n"
"v_add_f32 v85, v85, v21\n"
"v_mfma_f32_16x16x32_bf16 v[228:231], a[88:91], a[96:99], v[228:231]\n"
"ds_read_b128 a[96:99], v2 offset:0\n"
"v_add_f32 v86, v86, v22\n"
"v_mfma_f32_16x16x32_bf16 v[232:235], a[88:91], a[104:107], v[232:235]\n"
"v_add_f32 v87, v87, v23\n"
"v_mfma_f32_16x16x32_bf16 v[232:235], a[92:95], a[108:111], v[232:235]\n"
"v_accvgpr_read_b32 v12, a4\n"
"v_mfma_f32_16x16x32_bf16 v[204:207], a[84:87], a[108:111], v[204:207]\n"
"ds_read_b128 a[108:111], v2 offset:3072\n"
"v_accvgpr_read_b32 v13, a5\n"
"v_mfma_f32_16x16x32_bf16 v[204:207], a[80:83], a[104:107], v[204:207]\n"
"ds_read_b128 a[104:107], v2 offset:2048\n"
"v_accvgpr_read_b32 v14, a6\n"
"v_mfma_f32_16x16x32_bf16 v[208:211], a[80:83], a[112:115], v[208:211]\n"
"v_accvgpr_read_b32 v15, a7\n"
"v_mfma_f32_16x16x32_bf16 v[208:211], a[84:87], a[116:119], v[208:211]\n"
"global_load_dwordx4 a[68:71], v0, s[34:35] offset:-3072 sc1\n"
"v_add_f32 v12, v12, v20\n"
"v_mfma_f32_16x16x32_bf16 v[236:239], a[92:95], a[116:119], v[236:239]\n"
"ds_read_b128 a[116:119], v2 offset:5120\n"
"v_add_f32 v13, v13, v21\n"
"v_mfma_f32_16x16x32_bf16 v[236:239], a[88:91], a[112:115], v[236:239]\n"
"ds_read_b128 a[112:115], v2 offset:4096\n"
"v_add_f32 v14, v14, v22\n"
"v_mfma_f32_16x16x32_bf16 v[240:243], a[88:91], a[120:123], v[240:243]\n"
"v_add_f32 v15, v15, v23\n"
"v_accvgpr_write_b32 a4, v12\n"
"v_mfma_f32_16x16x32_bf16 v[240:243], a[92:95], a[124:127], v[240:243]\n"
"v_accvgpr_write_b32 a5, v13\n"
"v_mfma_f32_16x16x32_bf16 v[212:215], a[84:87], a[124:127], v[212:215]\n"
"ds_read_b128 a[124:127], v2 offset:7168\n"
"v_accvgpr_write_b32 a6, v14\n"
"v_mfma_f32_16x16x32_bf16 v[212:215], a[80:83], a[120:123], v[212:215]\n"
"ds_read_b128 a[120:123], v2 offset:6144\n"
"v_accvgpr_write_b32 a7, v15\n"
"v_mfma_f32_16x16x32_bf16 v[216:219], a[80:83], a[128:131], v[216:219]\n"
"v_mfma_f32_16x16x32_bf16 v[216:219], a[84:87], a[132:135], v[216:219]\n"
"global_load_dwordx4 a[72:75], v0, s[34:35] offset:-2048 sc1\n"
"v_lshrrev_b32 v20, 4, v0\n"
"v_mfma_f32_16x16x32_bf16 v[244:247], a[92:95], a[132:135], v[244:247]\n"
"ds_read_b128 a[132:135], v2 offset:9216\n"
"v_and_b32 v20, 0x30, v20\n"
"v_mfma_f32_16x16x32_bf16 v[244:247], a[88:91], a[128:131], v[244:247]\n"
"ds_read_b128 a[128:131], v2 offset:8192\n"
"buffer_load_dwordx2 v[22:23], v20, s[44:47], s61 offen offset:72\n"
"v_mfma_f32_16x16x32_bf16 v[248:251], a[88:91], a[136:139], v[248:251]\n"
"v_mfma_f32_16x16x32_bf16 v[248:251], a[92:95], a[140:143], v[248:251]\n"
"v_mfma_f32_16x16x32_bf16 v[220:223], a[84:87], a[140:143], v[220:223]\n"
"ds_read_b128 a[140:143], v2 offset:11264\n"
"v_mfma_f32_16x16x32_bf16 v[220:223], a[80:83], a[136:139], v[220:223]\n"
"ds_read_b128 a[136:139], v2 offset:10240\n"
"v_mfma_f32_16x16x32_bf16 v[224:227], a[80:83], a[144:147], v[224:227]\n"
"v_mfma_f32_16x16x32_bf16 v[224:227], a[84:87], a[148:151], v[224:227]\n"
"global_load_dwordx4 a[76:79], v0, s[34:35] offset:-1024 sc1\n"
"v_mfma_f32_16x16x32_bf16 v[252:255], a[92:95], a[148:151], v[252:255]\n"
"ds_read_b128 a[148:151], v2 offset:13312\n"
"v_mfma_f32_16x16x32_bf16 v[252:255], a[88:91], a[144:147], v[252:255]\n"
"ds_read_b128 a[144:147], v2 offset:12288\n"
"v_mfma_f32_16x16x32_bf16 a[28:31], a[88:91], a[152:155], a[28:31]\n"
"v_mfma_f32_16x16x32_bf16 a[28:31], a[92:95], a[156:159], a[28:31]\n"
"v_mfma_f32_16x16x32_bf16 a[24:27], a[84:87], a[156:159], a[24:27]\n"
"s_cmp_eq_u32 s62, 5\n"
"ds_read_b128 a[156:159], v2 offset:15360\n"
"v_mfma_f32_16x16x32_bf16 a[24:27], a[80:83], a[152:155], a[24:27]\n"
"ds_read_b128 a[152:155], v2 offset:14336\n"
"s_cbranch_scc1 L_last\n"
"s_waitcnt vmcnt(9) lgkmcnt(12)\n"
"v_mfma_f32_16x16x32_bf16 v[32:35], a[32:35], a[96:99], v[32:35]\n"
"s_add_u32 s48, s48, 131072\n"
"s_addc_u32 s49, s49, 0\n"
"v_mfma_f32_16x16x32_bf16 v[32:35], a[36:39], a[100:103], v[32:35]\n"
"global_load_dwordx4 a[80:83], v0, s[34:35] offset:0 sc1\n"
"L_P4:\n"
"v_mfma_f32_16x16x32_bf16 v[60:63], a[44:47], a[100:103], v[60:63]\n"
"s_barrier\n"
"v_mfma_f32_16x16x32_bf16 v[60:63], a[40:43], a[96:99], v[60:63]\n"
"v_mfma_f32_16x16x32_bf16 v[64:67], a[40:43], a[104:107], v[64:67]\n"
"v_mfma_f32_16x16x32_bf16 v[64:67], a[44:47], a[108:111], v[64:67]\n"
"v_mfma_f32_16x16x32_bf16 v[36:39], a[36:39], a[108:111], v[36:39]\n"
"v_subrev_u32 v8, s65, v8\n"
"v_mfma_f32_16x16x32_bf16 v[36:39], a[32:35], a[104:107], v[36:39]\n"
"s_waitcnt lgkmcnt(8)\n"
"v_xor_b32 v9, s79, v9\n"
"v_mfma_f32_16x16x32_bf16 v[40:43], a[32:35], a[112:115], v[40:43]\n"
"v_or_b32 v8, v8, v9\n"
"v_mfma_f32_16x16x32_bf16 v[40:43], a[36:39], a[116:119], v[40:43]\n"
"global_load_dwordx4 a[84:87], v0, s[34:35] offset:1024 sc1\n"
"v_cmp_lt_u32 vcc, 15, v8\n"
"v_mfma_f32_16x16x32_bf16 v[68:71], a[44:47], a[116:119], v[68:71]\n"
"s_cbranch_vccnz L_slow1\n"
"L_back1:\n"
"v_mfma_f32_16x16x32_bf16 v[68:71], a[40:43], a[112:115], v[68:71]\n"
"v_mfma_f32_16x16x32_bf16 v[72:75], a[40:43], a[120:123], v[72:75]\n"
"v_mfma_f32_16x16x32_bf16 v[72:75], a[44:47], a[124:127], v[72:75]\n"
"v_mfma_f32_16x16x32_bf16 v[44:47], a[36:39], a[124:127], v[44:47]\n"
"v_mfma_f32_16x16x32_bf16 v[44:47], a[32:35], a[120:123], v[44:47]\n"
"s_waitcnt lgkmcnt(4)\n"
"v_mfma_f32_16x16x32_bf16 v[48:51], a[32:35], a[128:131], v[48:51]\n"
"v_mfma_f32_16x16x32_bf16 v[48:51], a[36:39], a[132:135], v[48:51]\n"
"global_load_dwordx4 a[88:91], v0, s[34:35] offset:2048 sc1\n"
"v_mfma_f32_16x16x32_bf16 v[76:79], a[44:47], a[132:135], v[76:79]\n"
"v_mfma_f32_16x16x32_bf16 v[76:79], a[40:43], a[128:131], v[76:79]\n"
"s_add_u32 m0, s64, 16384\n"
"v_mfma_f32_16x16x32_bf16 v[80:83], a[40:43], a[136:139], v[80:83]\n"
"v_mfma_f32_16x16x32_bf16 v[80:83], a[44:47], a[140:143], v[80:83]\n"
"v_mfma_f32_16x16x32_bf16 v[52:55], a[36:39], a[140:143], v[52:55]\n"
"v_mfma_f32_16x16x32_bf16 v[52:55], a[32:35], a[136:139], v[52:55]\n"
"s_waitcnt lgkmcnt(0)\n"
"v_mfma_f32_16x16x32_bf16 v[56:59], a[32:35], a[144:147], v[56:59]\n"
"v_mfma_f32_16x16x32_bf16 v[56:59], a[36:39], a[148:151], v[56:59]\n"
"global_load_dwordx4 a[92:95], v0, s[34:35] offset:3072 sc1\n"
"v_mfma_f32_16x16x32_bf16 v[84:87], a[44:47], a[148:151], v[84:87]\n"
"v_mfma_f32_16x16x32_bf16 v[84:87], a[40:43], a[144:147], v[84:87]\n"
"v_mfma_f32_16x16x32_bf16 a[4:7], a[40:43], a[152:155], a[4:7]\n"
"s_add_u32 s34, s34, 8192\n"
"v_mfma_f32_16x16x32_bf16 a[4:7], a[44:47], a[156:159], a[4:7]\n"
"v_mfma_f32_16x16x32_bf16 a[0:3], a[36:39], a[156:159], a[0:3]\n"
"v_mfma_f32_16x16x32_bf16 a[0:3], a[32:35], a[152:155], a[0:3]\n"
"s_waitcnt vmcnt(9)\n"
"v_mfma_f32_16x16x32_bf16 v[88:91], a[48:51], a[96:99], v[88:91]\n"
"v_mfma_f32_16x16x32_bf16 v[88:91], a[52:55], a[100:103], v[88:91]\n"
"global_load_dwordx4 a[32:35], v0, s[34:35] offset:-4096 sc1\n"
"v_mfma_f32_16x16x32_bf16 v[116:119], a[60:63], a[100:103], v[116:119]\n"
"buffer_load_dwordx4 v0, s[48:51], s75 offen offset:0 sc1 lds\n"
"v_mfma_f32_16x16x32_bf16 v[116:119], a[56:59], a[96:99], v[116:119]\n"
"v_mfma_f32_16x16x32_bf16 v[120:123], a[56:59], a[104:107], v[120:123]\n"
"v_mfma_f32_16x16x32_bf16 v[120:123], a[60:63], a[108:111], v[120:123]\n"
"v_mfma_f32_16x16x32_bf16 v[92:95], a[52:55], a[108:111], v[92:95]\n"
"v_mfma_f32_16x16x32_bf16 v[92:95], a[48:51], a[104:107], v[92:95]\n"
"v_mfma_f32_16x16x32_bf16 v[96:99], a[48:51], a[112:115], v[96:99]\n"
"v_mfma_f32_16x16x32_bf16 v[96:99], a[52:55], a[116:119], v[96:99]\n"
"global_load_dwordx4 a[36:39], v0, s[34:35] offset:-3072 sc1\n"
"v_mfma_f32_16x16x32_bf16 v[124:127], a[60:63], a[116:119], v[124:127]\n"
"v_mfma_f32_16x16x32_bf16 v[124:127], a[56:59], a[112:115], v[124:127]\n"
"v_mfma_f32_16x16x32_bf16 v[128:131], a[56:59], a[120:123], v[128:131]\n"
"v_mfma_f32_16x16x32_bf16 v[128:131], a[60:63], a[124:127], v[128:131]\n"
"v_mfma_f32_16x16x32_bf16 v[100:103], a[52:55], a[124:127], v[100:103]\n"
"v_mfma_f32_16x16x32_bf16 v[100:103], a[48:51], a[120:123], v[100:103]\n"
"v_mfma_f32_16x16x32_bf16 v[104:107], a[48:51], a[128:131], v[104:107]\n"
"v_mfma_f32_16x16x32_bf16 v[104:107], a[52:55], a[132:135], v[104:107]\n"
"global_load_dwordx4 a[40:43], v0, s[34:35] offset:-2048 sc1\n"
"v_mfma_f32_16x16x32_bf16 v[132:135], a[60:63], a[132:135], v[132:135]\n"
"buffer_load_dwordx4 v0, s[48:51], s75 offen offset:1024 sc1 lds\n"
"v_mfma_f32_16x16x32_bf16 v[132:135], a[56:59], a[128:131], v[132:135]\n"
"v_mfma_f32_16x16x32_bf16 v[136:139], a[56:59], a[136:139], v[136:139]\n"
"v_mfma_f32_16x16x32_bf16 v[136:139], a[60:63], a[140:143], v[136:139]\n"
"v_mfma_f32_16x16x32_bf16 v[108:111], a[52:55], a[140:143], v[108:111]\n"
"v_mfma_f32_16x16x32_bf16 v[108:111], a[48:51], a[136:139], v[108:111]\n"
"v_mfma_f32_16x16x32_bf16 v[112:115], a[48:51], a[144:147], v[112:115]\n"
"v_mfma_f32_16x16x32_bf16 v[112:115], a[52:55], a[148:151], v[112:115]\n"
"global_load_dwordx4 a[44:47], v0, s[34:35] offset:-1024 sc1\n"
"v_mfma_f32_16x16x32_bf16 v[140:143], a[60:63], a[148:151], v[140:143]\n"
"v_mfma_f32_16x16x32_bf16 v[140:143], a[56:59], a[144:147], v[140:143]\n"
"v_mfma_f32_16x16x32_bf16 a[12:15], a[56:59], a[152:155], a[12:15]\n"
"v_mfma_f32_16x16x32_bf16 a[12:15], a[60:63], a[156:159], a[12:15]\n"
"v_mfma_f32_16x16x32_bf16 a[8:11], a[52:55], a[156:159], a[8:11]\n"
"v_mfma_f32_16x16x32_bf16 a[8:11], a[48:51], a[152:155], a[8:11]\n"
"s_waitcnt vmcnt(10)\n"
"v_mfma_f32_16x16x32_bf16 v[144:147], a[64:67], a[96:99], v[144:147]\n"
"v_mfma_f32_16x16x32_bf16 v[144:147], a[68:71], a[100:103], v[144:147]\n"
"global_load_dwordx4 a[48:51], v0, s[34:35] offset:0 sc1\n"
"v_mfma_f32_16x16x32_bf16 v[172:175], a[76:79], a[100:103], v[172:175]\n"
"v_mfma_f32_16x16x32_bf16 v[172:175], a[72:75], a[96:99], v[172:175]\n"
"buffer_load_dwordx4 v0, s[48:51], s75 offen offset:2048 sc1 lds\n"
"v_mfma_f32_16x16x32_bf16 v[176:179], a[72:75], a[104:107], v[176:179]\n"
"v_mfma_f32_16x16x32_bf16 v[176:179], a[76:79], a[108:111], v[176:179]\n"
"v_mfma_f32_16x16x32_bf16 v[148:151], a[68:71], a[108:111], v[148:151]\n"
"v_mfma_f32_16x16x32_bf16 v[148:151], a[64:67], a[104:107], v[148:151]\n"
"v_mfma_f32_16x16x32_bf16 v[152:155], a[64:67], a[112:115], v[152:155]\n"
"v_lshlrev_b32 v16, 16, v18\n"
"v_mfma_f32_16x16x32_bf16 v[152:155], a[68:71], a[116:119], v[152:155]\n"
"global_load_dwordx4 a[52:55], v0, s[34:35] offset:1024 sc1\n"
"v_and_b32 v17, s63, v18\n"
"v_mfma_f32_16x16x32_bf16 v[180:183], a[76:79], a[116:119], v[180:183]\n"
"v_lshlrev_b32 v18, 16, v19\n"
"v_mfma_f32_16x16x32_bf16 v[180:183], a[72:75], a[112:115], v[180:183]\n"
"v_and_b32 v19, s63, v19\n"
"v_mfma_f32_16x16x32_bf16 v[184:187], a[72:75], a[120:123], v[184:187]\n"
"v_add_f32 v88, v88, v16\n"
"v_mfma_f32_16x16x32_bf16 v[184:187], a[76:79], a[124:127], v[184:187]\n"
"v_add_f32 v89, v89, v17\n"
"v_add_f32 v90, v90, v18\n"
"v_mfma_f32_16x16x32_bf16 v[156:159], a[68:71], a[124:127], v[156:159]\n"
"v_add_f32 v91, v91, v19\n"
"v_mfma_f32_16x16x32_bf16 v[156:159], a[64:67], a[120:123], v[156:159]\n"
"v_add_f32 v92, v92, v16\n"
"v_mfma_f32_16x16x32_bf16 v[160:163], a[64:67], a[128:131], v[160:163]\n"
"v_add_f32 v93, v93, v17\n"
"v_mfma_f32_16x16x32_bf16 v[160:163], a[68:71], a[132:135], v[160:163]\n"
"global_load_dwordx4 a[56:59], v0, s[34:35] offset:2048 sc1\n"
"v_add_f32 v94, v94, v18\n"
"v_mfma_f32_16x16x32_bf16 v[188:191], a[76:79], a[132:135], v[188:191]\n"
"buffer_load_dwordx4 v0, s[48:51], s75 offen offset:3072 sc1 lds\n"
"v_mfma_f32_16x16x32_bf16 v[188:191], a[72:75], a[128:131], v[188:191]\n"
"v_add_f32 v95, v95, v19\n"
"v_add_f32 v96, v96, v16\n"
"v_mfma_f32_16x16x32_bf16 v[192:195], a[72:75], a[136:139], v[192:195]\n"
"v_add_f32 v97, v97, v17\n"
"v_mfma_f32_16x16x32_bf16 v[192:195], a[76:79], a[140:143], v[192:195]\n"
"v_add_f32 v98, v98, v18\n"
"v_mfma_f32_16x16x32_bf16 v[164:167], a[68:71], a[140:143], v[164:167]\n"
"v_add_f32 v99, v99, v19\n"
"v_mfma_f32_16x16x32_bf16 v[164:167], a[64:67], a[136:139], v[164:167]\n"
"v_add_f32 v100, v100, v16\n"
"v_add_f32 v101, v101, v17\n"
"v_mfma_f32_16x16x32_bf16 v[168:171], a[64:67], a[144:147], v[168:171]\n"
"v_add_f32 v102, v102, v18\n"
"v_mfma_f32_16x16x32_bf16 v[168:171], a[68:71], a[148:151], v[168:171]\n"
"global_load_dwordx4 a[60:63], v0, s[34:35] offset:3072 sc1\n"
"v_add_f32 v103, v103, v19\n"
"v_mfma_f32_16x16x32_bf16 v[196:199], a[76:79], a[148:151], v[196:199]\n"
"v_add_f32 v104, v104, v16\n"
"v_mfma_f32_16x16x32_bf16 v[196:199], a[72:75], a[144:147], v[196:199]\n"
"v_add_f32 v105, v105, v17\n"
"v_mfma_f32_16x16x32_bf16 a[20:23], a[72:75], a[152:155], a[20:23]\n"
"s_add_u32 s34, s34, 8192\n"
"v_add_f32 v106, v106, v18\n"
"v_mfma_f32_16x16x32_bf16 a[20:23], a[76:79], a[156:159], a[20:23]\n"
"v_add_f32 v107, v107, v19\n"
"v_mfma_f32_16x16x32_bf16 a[16:19], a[68:71], a[156:159], a[16:19]\n"
"v_add_f32 v108, v108, v16\n"
"v_mfma_f32_16x16x32_bf16 a[16:19], a[64:67], a[152:155], a[16:19]\n"
"s_waitcnt vmcnt(12)\n"
"v_add_f32 v109, v109, v17\n"
"v_mfma_f32_16x16x32_bf16 v[200:203], a[80:83], a[96:99], v[200:203]\n"
"v_add_f32 v110, v110, v18\n"
"v_add_f32 v111, v111, v19\n"
"v_mfma_f32_16x16x32_bf16 v[200:203], a[84:87], a[100:103], v[200:203]\n"
"global_load_dwordx4 a[64:67], v0, s[34:35] offset:-4096 sc1\n"
"v_add_f32 v112, v112, v16\n"
"v_mfma_f32_16x16x32_bf16 v[228:231], a[92:95], a[100:103], v[228:231]\n"
"ds_read_b128 a[100:103], v3 offset:1024\n"
"v_add_f32 v113, v113, v17\n"
"v_mfma_f32_16x16x32_bf16 v[228:231], a[88:91], a[96:99], v[228:231]\n"
"s_add_u32 m0, s64, 20480\n"
"ds_read_b128 a[96:99], v3 offset:0\n"
"v_add_f32 v114, v114, v18\n"
"v_mfma_f32_16x16x32_bf16 v[232:235], a[88:91], a[104:107], v[232:235]\n"
"v_add_f32 v115, v115, v19\n"
"v_mfma_f32_16x16x32_bf16 v[232:235], a[92:95], a[108:111], v[232:235]\n"
"v_accvgpr_read_b32 v12, a8\n"
"v_mfma_f32_16x16x32_bf16 v[204:207], a[84:87], a[108:111], v[204:207]\n"
"ds_read_b128 a[108:111], v3 offset:3072\n"
"v_accvgpr_read_b32 v13, a9\n"
"v_mfma_f32_16x16x32_bf16 v[204:207], a[80:83], a[104:107], v[204:207]\n"
"ds_read_b128 a[104:107], v3 offset:2048\n"
"v_accvgpr_read_b32 v14, a10\n"
"v_mfma_f32_16x16x32_bf16 v[208:211], a[80:83], a[112:115], v[208:211]\n"
"v_accvgpr_read_b32 v15, a11\n"
"v_mfma_f32_16x16x32_bf16 v[208:211], a[84:87], a[116:119], v[208:211]\n"
"global_load_dwordx4 a[68:71], v0, s[34:35] offset:-3072 sc1\n"
"v_add_f32 v12, v12, v16\n"
"v_mfma_f32_16x16x32_bf16 v[236:239], a[92:95], a[116:119], v[236:239]\n"
"ds_read_b128 a[116:119], v3 offset:5120\n"
"v_add_f32 v13, v13, v17\n"
"v_mfma_f32_16x16x32_bf16 v[236:239], a[88:91], a[112:115], v[236:239]\n"
"ds_read_b128 a[112:115], v3 offset:4096\n"
"v_add_f32 v14, v14, v18\n"
"v_mfma_f32_16x16x32_bf16 v[240:243], a[88:91], a[120:123], v[240:243]\n"
"v_add_f32 v15, v15, v19\n"
"v_accvgpr_write_b32 a8, v12\n"
"v_mfma_f32_16x16x32_bf16 v[240:243], a[92:95], a[124:127], v[240:243]\n"
"v_accvgpr_write_b32 a9, v13\n"
"v_mfma_f32_16x16x32_bf16 v[212:215], a[84:87], a[124:127], v[212:215]\n"
"ds_read_b128 a[124:127], v3 offset:7168\n"
"v_accvgpr_write_b32 a10, v14\n"
"v_mfma_f32_16x16x32_bf16 v[212:215], a[80:83], a[120:123], v[212:215]\n"
"ds_read_b128 a[120:123], v3 offset:6144\n"
"v_accvgpr_write_b32 a11, v15\n"
"v_mfma_f32_16x16x32_bf16 v[216:219], a[80:83], a[128:131], v[216:219]\n"
"v_mfma_f32_16x16x32_bf16 v[216:219], a[84:87], a[132:135], v[216:219]\n"
"global_load_dwordx4 a[72:75], v0, s[34:35] offset:-2048 sc1\n"
"v_mfma_f32_16x16x32_bf16 v[244:247], a[92:95], a[132:135], v[244:247]\n"
"ds_read_b128 a[132:135], v3 offset:9216\n"
"v_mfma_f32_16x16x32_bf16 v[244:247], a[88:91], a[128:131], v[244:247]\n"
"ds_read_b128 a[128:131], v3 offset:8192\n"
"v_mfma_f32_16x16x32_bf16 v[248:251], a[88:91], a[136:139], v[248:251]\n"
"buffer_load_dwordx4 v1, s[48:51], s75 offen offset:0 sc1 lds\n"
"v_mfma_f32_16x16x32_bf16 v[248:251], a[92:95], a[140:143], v[248:251]\n"
"v_mfma_f32_16x16x32_bf16 v[220:223], a[84:87], a[140:143], v[220:223]\n"
"ds_read_b128 a[140:143], v3 offset:11264\n"
"v_mfma_f32_16x16x32_bf16 v[220:223], a[80:83], a[136:139], v[220:223]\n"
"ds_read_b128 a[136:139], v3 offset:10240\n"
"v_mfma_f32_16x16x32_bf16 v[224:227], a[80:83], a[144:147], v[224:227]\n"
"v_mfma_f32_16x16x32_bf16 v[224:227], a[84:87], a[148:151], v[224:227]\n"
"global_load_dwordx4 a[76:79], v0, s[34:35] offset:-1024 sc1\n"
"v_mfma_f32_16x16x32_bf16 v[252:255], a[92:95], a[148:151], v[252:255]\n"
"ds_read_b128 a[148:151], v3 offset:13312\n"
"v_mfma_f32_16x16x32_bf16 v[252:255], a[88:91], a[144:147], v[252:255]\n"
"ds_read_b128 a[144:147], v3 offset:12288\n"
"v_mfma_f32_16x16x32_bf16 a[28:31], a[88:91], a[152:155], a[28:31]\n"
"v_mfma_f32_16x16x32_bf16 a[28:31], a[92:95], a[156:159], a[28:31]\n"
"v_mfma_f32_16x16x32_bf16 a[24:27], a[84:87], a[156:159], a[24:27]\n"
"ds_read_b128 a[156:159], v3 offset:15360\n"
"v_mfma_f32_16x16x32_bf16 a[24:27], a[80:83], a[152:155], a[24:27]\n"
"s_waitcnt vmcnt(11) lgkmcnt(11)\n"
"ds_read_b128 a[152:155], v3 offset:14336\n"
"v_mfma_f32_16x16x32_bf16 v[32:35], a[32:35], a[96:99], v[32:35]\n"
"v_mfma_f32_16x16x32_bf16 v[32:35], a[36:39], a[100:103], v[32:35]\n"
"global_load_dwordx4 a[80:83], v0, s[34:35] offset:0 sc1\n"
"v_mfma_f32_16x16x32_bf16 v[60:63], a[44:47], a[100:103], v[60:63]\n"
"v_mfma_f32_16x16x32_bf16 v[60:63], a[40:43], a[96:99], v[60:63]\n"
"buffer_load_dwordx4 v1, s[48:51], s75 offen offset:1024 sc1 lds\n"
"v_mfma_f32_16x16x32_bf16 v[64:67], a[40:43], a[104:107], v[64:67]\n"
"v_mfma_f32_16x16x32_bf16 v[64:67], a[44:47], a[108:111], v[64:67]\n"
"v_mfma_f32_16x16x32_bf16 v[36:39], a[36:39], a[108:111], v[36:39]\n"
"v_mfma_f32_16x16x32_bf16 v[36:39], a[32:35], a[104:107], v[36:39]\n"
"s_waitcnt lgkmcnt(8)\n"
"v_mfma_f32_16x16x32_bf16 v[40:43], a[32:35], a[112:115], v[40:43]\n"
"v_mfma_f32_16x16x32_bf16 v[40:43], a[36:39], a[116:119], v[40:43]\n"
"global_load_dwordx4 a[84:87], v0, s[34:35] offset:1024 sc1\n"
"v_mfma_f32_16x16x32_bf16 v[68:71], a[44:47], a[116:119], v[68:71]\n"
"v_mfma_f32_16x16x32_bf16 v[68:71], a[40:43], a[112:115], v[68:71]\n"
"v_mfma_f32_16x16x32_bf16 v[72:75], a[40:43], a[120:123], v[72:75]\n"
"v_mfma_f32_16x16x32_bf16 v[72:75], a[44:47], a[124:127], v[72:75]\n"
"v_mfma_f32_16x16x32_bf16 v[44:47], a[36:39], a[124:127], v[44:47]\n"
"v_mfma_f32_16x16x32_bf16 v[44:47], a[32:35], a[120:123], v[44:47]\n"
"s_waitcnt lgkmcnt(4)\n"
"v_mfma_f32_16x16x32_bf16 v[48:51], a[32:35], a[128:131], v[48:51]\n"
"v_mfma_f32_16x16x32_bf16 v[48:51], a[36:39], a[132:135], v[48:51]\n"
"global_load_dwordx4 a[88:91], v0, s[34:35] offset:2048 sc1\n"
"v_mfma_f32_16x16x32_bf16 v[76:79], a[44:47], a[132:135], v[76:79]\n"
"v_mfma_f32_16x16x32_bf16 v[76:79], a[40:43], a[128:131], v[76:79]\n"
"buffer_load_dwordx4 v1, s[48:51], s75 offen offset:2048 sc1 lds\n"
"v_mfma_f32_16x16x32_bf16 v[80:83], a[40:43], a[136:139], v[80:83]\n"
"v_mfma_f32_16x16x32_bf16 v[80:83], a[44:47], a[140:143], v[80:83]\n"
"v_mfma_f32_16x16x32_bf16 v[52:55], a[36:39], a[140:143], v[52:55]\n"
"v_mfma_f32_16x16x32_bf16 v[52:55], a[32:35], a[136:139], v[52:55]\n"
"s_waitcnt lgkmcnt(0)\n"
"v_mfma_f32_16x16x32_bf16 v[56:59], a[32:35], a[144:147], v[56:59]\n"
"v_mfma_f32_16x16x32_bf16 v[56:59], a[36:39], a[148:151], v[56:59]\n"
"global_load_dwordx4 a[92:95], v0, s[34:35] offset:3072 sc1\n"
"v_mfma_f32_16x16x32_bf16 v[84:87], a[44:47], a[148:151], v[84:87]\n"
"v_mfma_f32_16x16x32_bf16 v[84:87], a[40:43], a[144:147], v[84:87]\n"
"v_mfma_f32_16x16x32_bf16 a[4:7], a[40:43], a[152:155], a[4:7]\n"
"s_add_u32 s34, s34, 8192\n"
"v_mfma_f32_16x16x32_bf16 a[4:7], a[44:47], a[156:159], a[4:7]\n"
"v_mfma_f32_16x16x32_bf16 a[0:3], a[36:39], a[156:159], a[0:3]\n"
"v_mfma_f32_16x16x32_bf16 a[0:3], a[32:35], a[152:155], a[0:3]\n"
"s_waitcnt vmcnt(11)\n"
"v_mfma_f32_16x16x32_bf16 v[88:91], a[48:51], a[96:99], v[88:91]\n"
"v_mfma_f32_16x16x32_bf16 v[88:91], a[52:55], a[100:103], v[88:91]\n"
"global_load_dwordx4 a[32:35], v0, s[34:35] offset:-4096 sc1\n"
"v_mfma_f32_16x16x32_bf16 v[116:119], a[60:63], a[100:103], v[116:119]\n"
"v_mfma_f32_16x16x32_bf16 v[116:119], a[56:59], a[96:99], v[116:119]\n"
"buffer_load_dwordx4 v1, s[48:51], s75 offen offset:3072 sc1 lds\n"
"v_mfma_f32_16x16x32_bf16 v[120:123], a[56:59], a[104:107], v[120:123]\n"
"v_mfma_f32_16x16x32_bf16 v[120:123], a[60:63], a[108:111], v[120:123]\n"
"v_mfma_f32_16x16x32_bf16 v[92:95], a[52:55], a[108:111], v[92:95]\n"
"v_mfma_f32_16x16x32_bf16 v[92:95], a[48:51], a[104:107], v[92:95]\n"
"v_mfma_f32_16x16x32_bf16 v[96:99], a[48:51], a[112:115], v[96:99]\n"
"v_mfma_f32_16x16x32_bf16 v[96:99], a[52:55], a[116:119], v[96:99]\n"
"global_load_dwordx4 a[36:39], v0, s[34:35] offset:-3072 sc1\n"
"v_mfma_f32_16x16x32_bf16 v[124:127], a[60:63], a[116:119], v[124:127]\n"
"v_mfma_f32_16x16x32_bf16 v[124:127], a[56:59], a[112:115], v[124:127]\n"
"v_mfma_f32_16x16x32_bf16 v[128:131], a[56:59], a[120:123], v[128:131]\n"
"v_mfma_f32_16x16x32_bf16 v[128:131], a[60:63], a[124:127], v[128:131]\n"
"v_mfma_f32_16x16x32_bf16 v[100:103], a[52:55], a[124:127], v[100:103]\n"
"v_mfma_f32_16x16x32_bf16 v[100:103], a[48:51], a[120:123], v[100:103]\n"
"v_mfma_f32_16x16x32_bf16 v[104:107], a[48:51], a[128:131], v[104:107]\n"
"v_mfma_f32_16x16x32_bf16 v[104:107], a[52:55], a[132:135], v[104:107]\n"
"global_load_dwordx4 a[40:43], v0, s[34:35] offset:-2048 sc1\n"
"v_mfma_f32_16x16x32_bf16 v[132:135], a[60:63], a[132:135], v[132:135]\n"
"v_mfma_f32_16x16x32_bf16 v[132:135], a[56:59], a[128:131], v[132:135]\n"
"s_add_u32 m0, s64, 24576\n"
"v_mfma_f32_16x16x32_bf16 v[136:139], a[56:59], a[136:139], v[136:139]\n"
"v_mfma_f32_16x16x32_bf16 v[136:139], a[60:63], a[140:143], v[136:139]\n"
"v_mfma_f32_16x16x32_bf16 v[108:111], a[52:55], a[140:143], v[108:111]\n"
"v_mfma_f32_16x16x32_bf16 v[108:111], a[48:51], a[136:139], v[108:111]\n"
"v_mfma_f32_16x16x32_bf16 v[112:115], a[48:51], a[144:147], v[112:115]\n"
"v_mfma_f32_16x16x32_bf16 v[112:115], a[52:55], a[148:151], v[112:115]\n"
"global_load_dwordx4 a[44:47], v0, s[34:35] offset:-1024 sc1\n"
"v_mfma_f32_16x16x32_bf16 v[140:143], a[60:63], a[148:151], v[140:143]\n"
"v_mfma_f32_16x16x32_bf16 v[140:143], a[56:59], a[144:147], v[140:143]\n"
"v_mfma_f32_16x16x32_bf16 a[12:15], a[56:59], a[152:155], a[12:15]\n"
"v_mfma_f32_16x16x32_bf16 a[12:15], a[60:63], a[156:159], a[12:15]\n"
"v_mfma_f32_16x16x32_bf16 a[8:11], a[52:55], a[156:159], a[8:11]\n"
"v_mfma_f32_16x16x32_bf16 a[8:11], a[48:51], a[152:155], a[8:11]\n"
"s_waitcnt vmcnt(11)\n"
"v_mfma_f32_16x16x32_bf16 v[144:147], a[64:67], a[96:99], v[144:147]\n"
"v_mfma_f32_16x16x32_bf16 v[144:147], a[68:71], a[100:103], v[144:147]\n"
"global_load_dwordx4 a[48:51], v0, s[34:35] offset:0 sc1\n"
"v_mfma_f32_16x16x32_bf16 v[172:175], a[76:79], a[100:103], v[172:175]\n"
"v_mfma_f32_16x16x32_bf16 v[172:175], a[72:75], a[96:99], v[172:175]\n"
"buffer_load_dwordx4 v0, s[48:51], s69 offen offset:0 sc1 lds\n"
"v_mfma_f32_16x16x32_bf16 v[176:179], a[72:75], a[104:107], v[176:179]\n"
"v_mfma_f32_16x16x32_bf16 v[176:179], a[76:79], a[108:111], v[176:179]\n"
"v_mfma_f32_16x16x32_bf16 v[148:151], a[68:71], a[108:111], v[148:151]\n"
"v_mfma_f32_16x16x32_bf16 v[148:151], a[64:67], a[104:107], v[148:151]\n"
"v_mfma_f32_16x16x32_bf16 v[152:155], a[64:67], a[112:115], v[152:155]\n"
"v_lshlrev_b32 v20, 16, v22\n"
"v_mfma_f32_16x16x32_bf16 v[152:155], a[68:71], a[116:119], v[152:155]\n"
"global_load_dwordx4 a[52:55], v0, s[34:35] offset:1024 sc1\n"
"v_and_b32 v21, s63, v22\n"
"v_mfma_f32_16x16x32_bf16 v[180:183], a[76:79], a[116:119], v[180:183]\n"
"v_lshlrev_b32 v22, 16, v23\n"
"v_mfma_f32_16x16x32_bf16 v[180:183], a[72:75], a[112:115], v[180:183]\n"
"v_and_b32 v23, s63, v23\n"
"v_mfma_f32_16x16x32_bf16 v[184:187], a[72:75], a[120:123], v[184:187]\n"
"v_add_f32 v116, v116, v20\n"
"v_mfma_f32_16x16x32_bf16 v[184:187], a[76:79], a[124:127], v[184:187]\n"
"v_add_f32 v117, v117, v21\n"
"v_add_f32 v118, v118, v22\n"
"v_mfma_f32_16x16x32_bf16 v[156:159], a[68:71], a[124:127], v[156:159]\n"
"v_add_f32 v119, v119, v23\n"
"v_mfma_f32_16x16x32_bf16 v[156:159], a[64:67], a[120:123], v[156:159]\n"
"v_add_f32 v120, v120, v20\n"
"v_mfma_f32_16x16x32_bf16 v[160:163], a[64:67], a[128:131], v[160:163]\n"
"v_add_f32 v121, v121, v21\n"
"v_mfma_f32_16x16x32_bf16 v[160:163], a[68:71], a[132:135], v[160:163]\n"
"global_load_dwordx4 a[56:59], v0, s[34:35] offset:2048 sc1\n"
"v_add_f32 v122, v122, v22\n"
"v_mfma_f32_16x16x32_bf16 v[188:191], a[76:79], a[132:135], v[188:191]\n"
"v_add_f32 v123, v123, v23\n"
"v_add_f32 v124, v124, v20\n"
"v_mfma_f32_16x16x32_bf16 v[188:191], a[72:75], a[128:131], v[188:191]\n"
"buffer_load_dwordx4 v0, s[48:51], s69 offen offset:1024 sc1 lds\n"
"v_mfma_f32_16x16x32_bf16 v[192:195], a[72:75], a[136:139], v[192:195]\n"
"v_add_f32 v125, v125, v21\n"
"v_mfma_f32_16x16x32_bf16 v[192:195], a[76:79], a[140:143], v[192:195]\n"
"v_add_f32 v126, v126, v22\n"
"v_mfma_f32_16x16x32_bf16 v[164:167], a[68:71], a[140:143], v[164:167]\n"
"v_add_f32 v127, v127, v23\n"
"v_mfma_f32_16x16x32_bf16 v[164:167], a[64:67], a[136:139], v[164:167]\n"
"v_add_f32 v128, v128, v20\n"
"v_add_f32 v129, v129, v21\n"
"v_mfma_f32_16x16x32_bf16 v[168:171], a[64:67], a[144:147], v[168:171]\n"
"v_add_f32 v130, v130, v22\n"
"v_mfma_f32_16x16x32_bf16 v[168:171], a[68:71], a[148:151], v[168:171]\n"
"global_load_dwordx4 a[60:63], v0, s[34:35] offset:3072 sc1\n"
"v_add_f32 v131, v131, v23\n"
"v_mfma_f32_16x16x32_bf16 v[196:199], a[76:79], a[148:151], v[196:199]\n"
"v_add_f32 v132, v132, v20\n"
"v_mfma_f32_16x16x32_bf16 v[196:199], a[72:75], a[144:147], v[196:199]\n"
"v_add_f32 v133, v133, v21\n"
"v_mfma_f32_16x16x32_bf16 a[20:23], a[72:75], a[152:155], a[20:23]\n"
"s_add_u32 s34, s34, 8192\n"
"v_add_f32 v134, v134, v22\n"
"v_mfma_f32_16x16x32_bf16 a[20:23], a[76:79], a[156:159], a[20:23]\n"
"v_add_f32 v135, v135, v23\n"
"v_mfma_f32_16x16x32_bf16 a[16:19], a[68:71], a[156:159], a[16:19]\n"
"v_add_f32 v136, v136, v20\n"
"v_mfma_f32_16x16x32_bf16 a[16:19], a[64:67], a[152:155], a[16:19]\n"
"s_waitcnt vmcnt(11)\n"
"v_add_f32 v137, v137, v21\n"
"v_mfma_f32_16x16x32_bf16 v[200:203], a[80:83], a[96:99], v[200:203]\n"
"v_add_f32 v138, v138, v22\n"
"v_add_f32 v139, v139, v23\n"
"v_mfma_f32_16x16x32_bf16 v[200:203], a[84:87], a[100:103], v[200:203]\n"
"global_load_dwordx4 a[64:67], v0, s[34:35] offset:-4096 sc1\n"
"v_add_f32 v140, v140, v20\n"
"v_mfma_f32_16x16x32_bf16 v[228:231], a[92:95], a[100:103], v[228:231]\n"
"ds_read_b128 a[100:103], v4 offset:1024\n"
"v_add_f32 v141, v141, v21\n"
"v_mfma_f32_16x16x32_bf16 v[228:231], a[88:91], a[96:99], v[228:231]\n"
"ds_read_b128 a[96:99], v4 offset:0\n"
"v_add_f32 v142, v142, v22\n"
"v_mfma_f32_16x16x32_bf16 v[232:235], a[88:91], a[104:107], v[232:235]\n"
"buffer_load_dwordx4 v0, s[48:51], s69 offen offset:2048 sc1 lds\n"
"v_mfma_f32_16x16x32_bf16 v[232:235], a[92:95], a[108:111], v[232:235]\n"
"v_add_f32 v143, v143, v23\n"
"v_accvgpr_read_b32 v12, a12\n"
"v_mfma_f32_16x16x32_bf16 v[204:207], a[84:87], a[108:111], v[204:207]\n"
"ds_read_b128 a[108:111], v4 offset:3072\n"
"v_accvgpr_read_b32 v13, a13\n"
"v_mfma_f32_16x16x32_bf16 v[204:207], a[80:83], a[104:107], v[204:207]\n"
"ds_read_b128 a[104:107], v4 offset:2048\n"
"v_accvgpr_read_b32 v14, a14\n"
"v_mfma_f32_16x16x32_bf16 v[208:211], a[80:83], a[112:115], v[208:211]\n"
"v_accvgpr_read_b32 v15, a15\n"
"v_mfma_f32_16x16x32_bf16 v[208:211], a[84:87], a[116:119], v[208:211]\n"
"global_load_dwordx4 a[68:71], v0, s[34:35] offset:-3072 sc1\n"
"v_add_f32 v12, v12, v20\n"
"v_mfma_f32_16x16x32_bf16 v[236:239], a[92:95], a[116:119], v[236:239]\n"
"ds_read_b128 a[116:119], v4 offset:5120\n"
"v_add_f32 v13, v13, v21\n"
"v_mfma_f32_16x16x32_bf16 v[236:239], a[88:91], a[112:115], v[236:239]\n"
"ds_read_b128 a[112:115], v4 offset:4096\n"
"v_add_f32 v14, v14, v22\n"
"v_mfma_f32_16x16x32_bf16 v[240:243], a[88:91], a[120:123], v[240:243]\n"
"v_add_f32 v15, v15, v23\n"
"v_accvgpr_write_b32 a12, v12\n"
"v_mfma_f32_16x16x32_bf16 v[240:243], a[92:95], a[124:127], v[240:243]\n"
"v_accvgpr_write_b32 a13, v13\n"
"v_mfma_f32_16x16x32_bf16 v[212:215], a[84:87], a[124:127], v[212:215]\n"
"ds_read_b128 a[124:127], v4 offset:7168\n"
"v_accvgpr_write_b32 a14, v14\n"
"v_mfma_f32_16x16x32_bf16 v[212:215], a[80:83], a[120:123], v[212:215]\n"
"ds_read_b128 a[120:123], v4 offset:6144\n"
"v_accvgpr_write_b32 a15, v15\n"
"v_mfma_f32_16x16x32_bf16 v[216:219], a[80:83], a[128:131], v[216:219]\n"
"v_mfma_f32_16x16x32_bf16 v[216:219], a[84:87], a[132:135], v[216:219]\n"
"global_load_dwordx4 a[72:75], v0, s[34:35] offset:-2048 sc1\n"
"v_mfma_f32_16x16x32_bf16 v[244:247], a[92:95], a[132:135], v[244:247]\n"
"ds_read_b128 a[132:135], v4 offset:9216\n"
"v_mfma_f32_16x16x32_bf16 v[244:247], a[88:91], a[128:131], v[244:247]\n"
"ds_read_b128 a[128:131], v4 offset:8192\n"
"v_mfma_f32_16x16x32_bf16 v[248:251], a[88:91], a[136:139], v[248:251]\n"
"buffer_load_dwordx4 v0, s[48:51], s69 offen offset:3072 sc1 lds\n"
"v_mfma_f32_16x16x32_bf16 v[248:251], a[92:95], a[140:143], v[248:251]\n"
"v_mfma_f32_16x16x32_bf16 v[220:223], a[84:87], a[140:143], v[220:223]\n"
"ds_read_b128 a[140:143], v4 offset:11264\n"
"v_mfma_f32_16x16x32_bf16 v[220:223], a[80:83], a[136:139], v[220:223]\n"
"ds_read_b128 a[136:139], v4 offset:10240\n"
"v_mfma_f32_16x16x32_bf16 v[224:227], a[80:83], a[144:147], v[224:227]\n"
"v_mfma_f32_16x16x32_bf16 v[224:227], a[84:87], a[148:151], v[224:227]\n"
"global_load_dwordx4 a[76:79], v0, s[34:35] offset:-1024 sc1\n"
"v_mfma_f32_16x16x32_bf16 v[252:255], a[92:95], a[148:151], v[252:255]\n"
"ds_read_b128 a[148:151], v4 offset:13312\n"
"v_mfma_f32_16x16x32_bf16 v[252:255], a[88:91], a[144:147], v[252:255]\n"
"ds_read_b128 a[144:147], v4 offset:12288\n"
"v_mfma_f32_16x16x32_bf16 a[28:31], a[88:91], a[152:155], a[28:31]\n"
"v_mfma_f32_16x16x32_bf16 a[28:31], a[92:95], a[156:159], a[28:31]\n"
"v_mfma_f32_16x16x32_bf16 a[24:27], a[84:87], a[156:159], a[24:27]\n"
"ds_read_b128 a[156:159], v4 offset:15360\n"
"v_mfma_f32_16x16x32_bf16 a[24:27], a[80:83], a[152:155], a[24:27]\n"
"s_waitcnt vmcnt(12) lgkmcnt(11)\n"
"ds_read_b128 a[152:155], v4 offset:14336\n"
"v_mfma_f32_16x16x32_bf16 v[32:35], a[32:35], a[96:99], v[32:35]\n"
"v_mfma_f32_16x16x32_bf16 v[32:35], a[36:39], a[100:103], v[32:35]\n"
"global_load_dwordx4 a[80:83], v0, s[34:35] offset:0 sc1\n"
"v_lshrrev_b32 v16, 4, v0\n"
"v_mfma_f32_16x16x32_bf16 v[60:63], a[44:47], a[100:103], v[60:63]\n"
"v_and_b32 v16, 0x30, v16\n"
"v_mfma_f32_16x16x32_bf16 v[60:63], a[40:43], a[96:99], v[60:63]\n"
"s_add_u32 m0, s64, 28672\n"
"buffer_load_dwordx2 v[18:19], v16, s[44:47], s61 offen offset:128\n"
"v_mfma_f32_16x16x32_bf16 v[64:67], a[40:43], a[104:107], v[64:67]\n"
"v_mfma_f32_16x16x32_bf16 v[64:67], a[44:47], a[108:111], v[64:67]\n"
"v_mfma_f32_16x16x32_bf16 v[36:39], a[36:39], a[108:111], v[36:39]\n"
"buffer_load_dwordx4 v[12:15], v0, s[56:59], %25 offen sc1\n"
"v_mfma_f32_16x16x32_bf16 v[36:39], a[32:35], a[104:107], v[36:39]\n"
"s_waitcnt lgkmcnt(8)\n"
"v_mfma_f32_16x16x32_bf16 v[40:43], a[32:35], a[112:115], v[40:43]\n"
"v_mfma_f32_16x16x32_bf16 v[40:43], a[36:39], a[116:119], v[40:43]\n"
"global_load_dwordx4 a[84:87], v0, s[34:35] offset:1024 sc1\n"
"v_lshrrev_b32 v20, 4, v0\n"
"v_mfma_f32_16x16x32_bf16 v[68:71], a[44:47], a[116:119], v[68:71]\n"
"v_and_b32 v20, 0x30, v20\n"
"v_mfma_f32_16x16x32_bf16 v[68:71], a[40:43], a[112:115], v[68:71]\n"
"buffer_load_dwordx2 v[22:23], v20, s[44:47], s61 offen offset:136\n"
"v_mfma_f32_16x16x32_bf16 v[72:75], a[40:43], a[120:123], v[72:75]\n"
"v_mfma_f32_16x16x32_bf16 v[72:75], a[44:47], a[124:127], v[72:75]\n"
"v_mfma_f32_16x16x32_bf16 v[44:47], a[36:39], a[124:127], v[44:47]\n"
"v_mfma_f32_16x16x32_bf16 v[44:47], a[32:35], a[120:123], v[44:47]\n"
"s_waitcnt lgkmcnt(4)\n"
"v_mfma_f32_16x16x32_bf16 v[48:51], a[32:35], a[128:131], v[48:51]\n"
"v_mfma_f32_16x16x32_bf16 v[48:51], a[36:39], a[132:135], v[48:51]\n"
"global_load_dwordx4 a[88:91], v0, s[34:35] offset:2048 sc1\n"
"v_lshrrev_b32 v24, 4, v0\n"
"v_mfma_f32_16x16x32_bf16 v[76:79], a[44:47], a[132:135], v[76:79]\n"
"v_and_b32 v24, 0x30, v24\n"
"v_mfma_f32_16x16x32_bf16 v[76:79], a[40:43], a[128:131], v[76:79]\n"
"buffer_load_dwordx4 v1, s[48:51], s69 offen offset:0 sc1 lds\n"
"buffer_load_dwordx2 v[26:27], v24, s[44:47], s61 offen offset:192\n"
"v_mfma_f32_16x16x32_bf16 v[80:83], a[40:43], a[136:139], v[80:83]\n"
"v_mfma_f32_16x16x32_bf16 v[80:83], a[44:47], a[140:143], v[80:83]\n"
"v_mfma_f32_16x16x32_bf16 v[52:55], a[36:39], a[140:143], v[52:55]\n"
"v_mfma_f32_16x16x32_bf16 v[52:55], a[32:35], a[136:139], v[52:55]\n"
"s_waitcnt lgkmcnt(0)\n"
"v_mfma_f32_16x16x32_bf16 v[56:59], a[32:35], a[144:147], v[56:59]\n"
"v_mfma_f32_16x16x32_bf16 v[56:59], a[36:39], a[148:151], v[56:59]\n"
"global_load_dwordx4 a[92:95], v0, s[34:35] offset:3072 sc1\n"
"v_lshrrev_b32 v28, 4, v0\n"
"v_mfma_f32_16x16x32_bf16 v[84:87], a[44:47], a[148:151], v[84:87]\n"
"v_and_b32 v28, 0x30, v28\n"
"v_mfma_f32_16x16x32_bf16 v[84:87], a[40:43], a[144:147], v[84:87]\n"
"buffer_load_dwordx2 v[30:31], v28, s[44:47], s61 offen offset:200\n"
"v_mfma_f32_16x16x32_bf16 a[4:7], a[40:43], a[152:155], a[4:7]\n"
"s_add_u32 s34, s34, 8192\n"
"v_mfma_f32_16x16x32_bf16 a[4:7], a[44:47], a[156:159], a[4:7]\n"
"v_mfma_f32_16x16x32_bf16 a[0:3], a[36:39], a[156:159], a[0:3]\n"
"v_mfma_f32_16x16x32_bf16 a[0:3], a[32:35], a[152:155], a[0:3]\n"
"s_waitcnt vmcnt(16)\n"
"v_mfma_f32_16x16x32_bf16 v[88:91], a[48:51], a[96:99], v[88:91]\n"
"v_mfma_f32_16x16x32_bf16 v[88:91], a[52:55], a[100:103], v[88:91]\n"
"global_load_dwordx4 a[32:35], v0, s[34:35] offset:-4096 sc1\n"
"v_mfma_f32_16x16x32_bf16 v[116:119], a[60:63], a[100:103], v[116:119]\n"
"v_mfma_f32_16x16x32_bf16 v[116:119], a[56:59], a[96:99], v[116:119]\n"
"buffer_load_dwordx4 v1, s[48:51], s69 offen offset:1024 sc1 lds\n"
"v_mfma_f32_16x16x32_bf16 v[120:123], a[56:59], a[104:107], v[120:123]\n"
"v_mfma_f32_16x16x32_bf16 v[120:123], a[60:63], a[108:111], v[120:123]\n"
"v_mfma_f32_16x16x32_bf16 v[92:95], a[52:55], a[108:111], v[92:95]\n"
"v_mfma_f32_16x16x32_bf16 v[92:95], a[48:51], a[104:107], v[92:95]\n"
"v_mfma_f32_16x16x32_bf16 v[96:99], a[48:51], a[112:115], v[96:99]\n"
"v_mfma_f32_16x16x32_bf16 v[96:99], a[52:55], a[116:119], v[96:99]\n"
"global_load_dwordx4 a[36:39], v0, s[34:35] offset:-3072 sc1\n"
"v_mfma_f32_16x16x32_bf16 v[124:127], a[60:63], a[116:119], v[124:127]\n"
"v_mfma_f32_16x16x32_bf16 v[124:127], a[56:59], a[112:115], v[124:127]\n"
"v_mfma_f32_16x16x32_bf16 v[128:131], a[56:59], a[120:123], v[128:131]\n"
"v_mfma_f32_16x16x32_bf16 v[128:131], a[60:63], a[124:127], v[128:131]\n"
"v_mfma_f32_16x16x32_bf16 v[100:103], a[52:55], a[124:127], v[100:103]\n"
"v_mfma_f32_16x16x32_bf16 v[100:103], a[48:51], a[120:123], v[100:103]\n"
"v_mfma_f32_16x16x32_bf16 v[104:107], a[48:51], a[128:131], v[104:107]\n"
"v_mfma_f32_16x16x32_bf16 v[104:107], a[52:55], a[132:135], v[104:107]\n"
"global_load_dwordx4 a[40:43], v0, s[34:35] offset:-2048 sc1\n"
"v_mfma_f32_16x16x32_bf16 v[132:135], a[60:63], a[132:135], v[132:135]\n"
"v_mfma_f32_16x16x32_bf16 v[132:135], a[56:59], a[128:131], v[132:135]\n"
"buffer_load_dwordx4 v1, s[48:51], s69 offen offset:2048 sc1 lds\n"
"v_mfma_f32_16x16x32_bf16 v[136:139], a[56:59], a[136:139], v[136:139]\n"
"v_mfma_f32_16x16x32_bf16 v[136:139], a[60:63], a[140:143], v[136:139]\n"
"v_mfma_f32_16x16x32_bf16 v[108:111], a[52:55], a[140:143], v[108:111]\n"
"v_mfma_f32_16x16x32_bf16 v[108:111], a[48:51], a[136:139], v[108:111]\n"
"v_mfma_f32_16x16x32_bf16 v[112:115], a[48:51], a[144:147], v[112:115]\n"
"v_mfma_f32_16x16x32_bf16 v[112:115], a[52:55], a[148:151], v[112:115]\n"
"global_load_dwordx4 a[44:47], v0, s[34:35] offset:-1024 sc1\n"
"v_mfma_f32_16x16x32_bf16 v[140:143], a[60:63], a[148:151], v[140:143]\n"
"v_mfma_f32_16x16x32_bf16 v[140:143], a[56:59], a[144:147], v[140:143]\n"
"v_mfma_f32_16x16x32_bf16 a[12:15], a[56:59], a[152:155], a[12:15]\n"
"v_mfma_f32_16x16x32_bf16 a[12:15], a[60:63], a[156:159], a[12:15]\n"
"v_mfma_f32_16x16x32_bf16 a[8:11], a[52:55], a[156:159], a[8:11]\n"
"v_mfma_f32_16x16x32_bf16 a[8:11], a[48:51], a[152:155], a[8:11]\n"
"s_waitcnt vmcnt(16)\n"
"v_mfma_f32_16x16x32_bf16 v[144:147], a[64:67], a[96:99], v[144:147]\n"
"v_mfma_f32_16x16x32_bf16 v[144:147], a[68:71], a[100:103], v[144:147]\n"
"global_load_dwordx4 a[48:51], v0, s[34:35] offset:0 sc1\n"
"v_mfma_f32_16x16x32_bf16 v[172:175], a[76:79], a[100:103], v[172:175]\n"
"v_mfma_f32_16x16x32_bf16 v[172:175], a[72:75], a[96:99], v[172:175]\n"
"buffer_load_dwordx4 v1, s[48:51], s69 offen offset:3072 sc1 lds\n"
"v_mfma_f32_16x16x32_bf16 v[176:179], a[72:75], a[104:107], v[176:179]\n"
"v_mfma_f32_16x16x32_bf16 v[176:179], a[76:79], a[108:111], v[176:179]\n"
"v_mfma_f32_16x16x32_bf16 v[148:151], a[68:71], a[108:111], v[148:151]\n"
"v_mfma_f32_16x16x32_bf16 v[148:151], a[64:67], a[104:107], v[148:151]\n"
"v_mfma_f32_16x16x32_bf16 v[152:155], a[64:67], a[112:115], v[152:155]\n"
"v_mfma_f32_16x16x32_bf16 v[152:155], a[68:71], a[116:119], v[152:155]\n"
"global_load_dwordx4 a[52:55], v0, s[34:35] offset:1024 sc1\n"
"v_mfma_f32_16x16x32_bf16 v[180:183], a[76:79], a[116:119], v[180:183]\n"
"v_mfma_f32_16x16x32_bf16 v[180:183], a[72:75], a[112:115], v[180:183]\n"
"v_mfma_f32_16x16x32_bf16 v[184:187], a[72:75], a[120:123], v[184:187]\n"
"v_mfma_f32_16x16x32_bf16 v[184:187], a[76:79], a[124:127], v[184:187]\n"
"v_mfma_f32_16x16x32_bf16 v[156:159], a[68:71], a[124:127], v[156:159]\n"
"v_mfma_f32_16x16x32_bf16 v[156:159], a[64:67], a[120:123], v[156:159]\n"
"v_mfma_f32_16x16x32_bf16 v[160:163], a[64:67], a[128:131], v[160:163]\n"
"v_mfma_f32_16x16x32_bf16 v[160:163], a[68:71], a[132:135], v[160:163]\n"
"global_load_dwordx4 a[56:59], v0, s[34:35] offset:2048 sc1\n"
"v_mfma_f32_16x16x32_bf16 v[188:191], a[76:79], a[132:135], v[188:191]\n"
"v_mfma_f32_16x16x32_bf16 v[188:191], a[72:75], a[128:131], v[188:191]\n"
"v_mfma_f32_16x16x32_bf16 v[192:195], a[72:75], a[136:139], v[192:195]\n"
"v_mfma_f32_16x16x32_bf16 v[192:195], a[76:79], a[140:143], v[192:195]\n"
"v_mfma_f32_16x16x32_bf16 v[164:167], a[68:71], a[140:143], v[164:167]\n"
"v_mfma_f32_16x16x32_bf16 v[164:167], a[64:67], a[136:139], v[164:167]\n"
"v_mfma_f32_16x16x32_bf16 v[168:171], a[64:67], a[144:147], v[168:171]\n"
"v_mfma_f32_16x16x32_bf16 v[168:171], a[68:71], a[148:151], v[168:171]\n"
"global_load_dwordx4 a[60:63], v0, s[34:35] offset:3072 sc1\n"
"v_mfma_f32_16x16x32_bf16 v[196:199], a[76:79], a[148:151], v[196:199]\n"
"v_mfma_f32_16x16x32_bf16 v[196:199], a[72:75], a[144:147], v[196:199]\n"
"v_mfma_f32_16x16x32_bf16 a[20:23], a[72:75], a[152:155], a[20:23]\n"
"s_add_u32 s34, s34, 8192\n"
"v_mfma_f32_16x16x32_bf16 a[20:23], a[76:79], a[156:159], a[20:23]\n"
"v_mfma_f32_16x16x32_bf16 a[16:19], a[68:71], a[156:159], a[16:19]\n"
"v_mfma_f32_16x16x32_bf16 a[16:19], a[64:67], a[152:155], a[16:19]\n"
"s_waitcnt vmcnt(12)\n"
"v_mfma_f32_16x16x32_bf16 v[200:203], a[80:83], a[96:99], v[200:203]\n"
"v_mfma_f32_16x16x32_bf16 v[200:203], a[84:87], a[100:103], v[200:203]\n"
"global_load_dwordx4 a[64:67], v0, s[34:35] offset:-4096 sc1\n"
"v_mfma_f32_16x16x32_bf16 v[228:231], a[92:95], a[100:103], v[228:231]\n"
"ds_read_b128 a[100:103], v5 offset:1024\n"
"v_mfma_f32_16x16x32_bf16 v[228:231], a[88:91], a[96:99], v[228:231]\n"
"ds_read_b128 a[96:99], v5 offset:0\n"
"v_mfma_f32_16x16x32_bf16 v[232:235], a[88:91], a[104:107], v[232:235]\n"
"v_mfma_f32_16x16x32_bf16 v[232:235], a[92:95], a[108:111], v[232:235]\n"
"v_mfma_f32_16x16x32_bf16 v[204:207], a[84:87], a[108:111], v[204:207]\n"
"ds_read_b128 a[108:111], v5 offset:3072\n"
"v_mfma_f32_16x16x32_bf16 v[204:207], a[80:83], a[104:107], v[204:207]\n"
"ds_read_b128 a[104:107], v5 offset:2048\n"
"v_mfma_f32_16x16x32_bf16 v[208:211], a[80:83], a[112:115], v[208:211]\n"
"v_mfma_f32_16x16x32_bf16 v[208:211], a[84:87], a[116:119], v[208:211]\n"
"global_load_dwordx4 a[68:71], v0, s[34:35] offset:-3072 sc1\n"
"v_mfma_f32_16x16x32_bf16 v[236:239], a[92:95], a[116:119], v[236:239]\n"
"ds_read_b128 a[116:119], v5 offset:5120\n"
"v_mfma_f32_16x16x32_bf16 v[236:239], a[88:91], a[112:115], v[236:239]\n"
"ds_read_b128 a[112:115], v5 offset:4096\n"
"v_mfma_f32_16x16x32_bf16 v[240:243], a[88:91], a[120:123], v[240:243]\n"
"v_mfma_f32_16x16x32_bf16 v[240:243], a[92:95], a[124:127], v[240:243]\n"
"v_mfma_f32_16x16x32_bf16 v[212:215], a[84:87], a[124:127], v[212:215]\n"
"ds_read_b128 a[124:127], v5 offset:7168\n"
"v_mfma_f32_16x16x32_bf16 v[212:215], a[80:83], a[120:123], v[212:215]\n"
"ds_read_b128 a[120:123], v5 offset:6144\n"
"v_mfma_f32_16x16x32_bf16 v[216:219], a[80:83], a[128:131], v[216:219]\n"
"v_mfma_f32_16x16x32_bf16 v[216:219], a[84:87], a[132:135], v[216:219]\n"
"global_load_dwordx4 a[72:75], v0, s[34:35] offset:-2048 sc1\n"
"v_mfma_f32_16x16x32_bf16 v[244:247], a[92:95], a[132:135], v[244:247]\n"
"ds_read_b128 a[132:135], v5 offset:9216\n"
"v_mfma_f32_16x16x32_bf16 v[244:247], a[88:91], a[128:131], v[244:247]\n"
"ds_read_b128 a[128:131], v5 offset:8192\n"
"v_mfma_f32_16x16x32_bf16 v[248:251], a[88:91], a[136:139], v[248:251]\n"
"v_mfma_f32_16x16x32_bf16 v[248:251], a[92:95], a[140:143], v[248:251]\n"
"v_mfma_f32_16x16x32_bf16 v[220:223], a[84:87], a[140:143], v[220:223]\n"
"ds_read_b128 a[140:143], v5 offset:11264\n"
"v_mfma_f32_16x16x32_bf16 v[220:223], a[80:83], a[136:139], v[220:223]\n"
"ds_read_b128 a[136:139], v5 offset:10240\n"
"v_mfma_f32_16x16x32_bf16 v[224:227], a[80:83], a[144:147], v[224:227]\n"
"v_mfma_f32_16x16x32_bf16 v[224:227], a[84:87], a[148:151], v[224:227]\n"
"global_load_dwordx4 a[76:79], v0, s[34:35] offset:-1024 sc1\n"
"v_mfma_f32_16x16x32_bf16 v[252:255], a[92:95], a[148:151], v[252:255]\n"
"ds_read_b128 a[148:151], v5 offset:13312\n"
"v_mfma_f32_16x16x32_bf16 v[252:255], a[88:91], a[144:147], v[252:255]\n"
"ds_read_b128 a[144:147], v5 offset:12288\n"
"v_mfma_f32_16x16x32_bf16 a[28:31], a[88:91], a[152:155], a[28:31]\n"
"v_mfma_f32_16x16x32_bf16 a[28:31], a[92:95], a[156:159], a[28:31]\n"
"v_mfma_f32_16x16x32_bf16 a[24:27], a[84:87], a[156:159], a[24:27]\n"
"ds_read_b128 a[156:159], v5 offset:15360\n"
"v_mfma_f32_16x16x32_bf16 a[24:27], a[80:83], a[152:155], a[24:27]\n"
"s_waitcnt vmcnt(9) lgkmcnt(11)\n"
"ds_read_b128 a[152:155], v5 offset:14336\n"
"v_mfma_f32_16x16x32_bf16 v[32:35], a[32:35], a[96:99], v[32:35]\n"
"v_mfma_f32_16x16x32_bf16 v[32:35], a[36:39], a[100:103], v[32:35]\n"
"global_load_dwordx4 a[80:83], v0, s[34:35] offset:0 sc1\n"
"v_mfma_f32_16x16x32_bf16 v[60:63], a[44:47], a[100:103], v[60:63]\n"
"v_mfma_f32_16x16x32_bf16 v[60:63], a[40:43], a[96:99], v[60:63]\n"
"v_mfma_f32_16x16x32_bf16 v[64:67], a[40:43], a[104:107], v[64:67]\n"
"v_mfma_f32_16x16x32_bf16 v[64:67], a[44:47], a[108:111], v[64:67]\n"
"v_mfma_f32_16x16x32_bf16 v[36:39], a[36:39], a[108:111], v[36:39]\n"
"s_add_u32 s67, s78, 2\n"
"v_mfma_f32_16x16x32_bf16 v[36:39], a[32:35], a[104:107], v[36:39]\n"
"s_waitcnt lgkmcnt(8)\n"
"v_mfma_f32_16x16x32_bf16 v[40:43], a[32:35], a[112:115], v[40:43]\n"
"v_mfma_f32_16x16x32_bf16 v[40:43], a[36:39], a[116:119], v[40:43]\n"
"global_load_dwordx4 a[84:87], v0, s[34:35] offset:1024 sc1\n"
"v_mfma_f32_16x16x32_bf16 v[68:71], a[44:47], a[116:119], v[68:71]\n"
"v_mfma_f32_16x16x32_bf16 v[68:71], a[40:43], a[112:115], v[68:71]\n"
"v_mfma_f32_16x16x32_bf16 v[72:75], a[40:43], a[120:123], v[72:75]\n"
"v_mfma_f32_16x16x32_bf16 v[72:75], a[44:47], a[124:127], v[72:75]\n"
"v_mfma_f32_16x16x32_bf16 v[44:47], a[36:39], a[124:127], v[44:47]\n"
"v_mfma_f32_16x16x32_bf16 v[44:47], a[32:35], a[120:123], v[44:47]\n"
"s_waitcnt lgkmcnt(4)\n"
"v_mfma_f32_16x16x32_bf16 v[48:51], a[32:35], a[128:131], v[48:51]\n"
"s_lshr_b32 s66, 20, s62\n"
"v_mfma_f32_16x16x32_bf16 v[48:51], a[36:39], a[132:135], v[48:51]\n"
"global_load_dwordx4 a[88:91], v0, s[34:35] offset:2048 sc1\n"
"v_mfma_f32_16x16x32_bf16 v[76:79], a[44:47], a[132:135], v[76:79]\n"
"s_and_b32 s66, s66, 1\n"
"s_cselect_b32 s70, 4294574080, 0\n"
"s_cselect_b32 s71, -1, 0\n"
"s_cselect_b32 s66, 0, 131072\n"
"v_mfma_f32_16x16x32_bf16 v[76:79], a[40:43], a[128:131], v[76:79]\n"
"v_mfma_f32_16x16x32_bf16 v[80:83], a[40:43], a[136:139], v[80:83]\n"
"s_add_u32 s70, s70, s66\n"
"v_subrev_u32 v12, s67, v12\n"
"v_mfma_f32_16x16x32_bf16 v[80:83], a[44:47], a[140:143], v[80:83]\n"
"v_xor_b32 v13, s79, v13\n"
"v_mfma_f32_16x16x32_bf16 v[52:55], a[36:39], a[140:143], v[52:55]\n"
"v_subrev_u32 v14, s67, v14\n"
"v_mfma_f32_16x16x32_bf16 v[52:55], a[32:35], a[136:139], v[52:55]\n"
"s_waitcnt lgkmcnt(0)\n"
"v_xor_b32 v15, s79, v15\n"
"v_mfma_f32_16x16x32_bf16 v[56:59], a[32:35], a[144:147], v[56:59]\n"
"v_or_b32 v12, v12, v14\n"
"v_mfma_f32_16x16x32_bf16 v[56:59], a[36:39], a[148:151], v[56:59]\n"
"global_load_dwordx4 a[92:95], v0, s[34:35] offset:3072 sc1\n"
"v_or_b32 v13, v13, v15\n"
"v_mfma_f32_16x16x32_bf16 v[84:87], a[44:47], a[148:151], v[84:87]\n"
"v_or_b32 v12, v12, v13\n"
"v_mfma_f32_16x16x32_bf16 v[84:87], a[40:43], a[144:147], v[84:87]\n"
"v_cmp_lt_u32 vcc, 15, v12\n"
"v_mfma_f32_16x16x32_bf16 a[4:7], a[40:43], a[152:155], a[4:7]\n"
"s_add_u32 s34, s34, 8192\n"
"s_cbranch_vccnz L_slow2\n"
"L_back2:\n"
"v_mfma_f32_16x16x32_bf16 a[4:7], a[44:47], a[156:159], a[4:7]\n"
"v_mfma_f32_16x16x32_bf16 a[0:3], a[36:39], a[156:159], a[0:3]\n"
"v_mfma_f32_16x16x32_bf16 a[0:3], a[32:35], a[152:155], a[0:3]\n"
"s_waitcnt vmcnt(8)\n"
"v_mfma_f32_16x16x32_bf16 v[88:91], a[48:51], a[96:99], v[88:91]\n"
"v_mfma_f32_16x16x32_bf16 v[88:91], a[52:55], a[100:103], v[88:91]\n"
"global_load_dwordx4 a[32:35], v0, s[34:35] offset:-4096 sc1\n"
"v_mfma_f32_16x16x32_bf16 v[116:119], a[60:63], a[100:103], v[116:119]\n"
"v_mfma_f32_16x16x32_bf16 v[116:119], a[56:59], a[96:99], v[116:119]\n"
"v_mfma_f32_16x16x32_bf16 v[120:123], a[56:59], a[104:107], v[120:123]\n"
"v_mfma_f32_16x16x32_bf16 v[120:123], a[60:63], a[108:111], v[120:123]\n"
"v_mfma_f32_16x16x32_bf16 v[92:95], a[52:55], a[108:111], v[92:95]\n"
"v_mfma_f32_16x16x32_bf16 v[92:95], a[48:51], a[104:107], v[92:95]\n"
"v_mfma_f32_16x16x32_bf16 v[96:99], a[48:51], a[112:115], v[96:99]\n"
"v_mfma_f32_16x16x32_bf16 v[96:99], a[52:55], a[116:119], v[96:99]\n"
"global_load_dwordx4 a[36:39], v0, s[34:35] offset:-3072 sc1\n"
"v_mfma_f32_16x16x32_bf16 v[124:127], a[60:63], a[116:119], v[124:127]\n"
"v_mfma_f32_16x16x32_bf16 v[124:127], a[56:59], a[112:115], v[124:127]\n"
"v_mfma_f32_16x16x32_bf16 v[128:131], a[56:59], a[120:123], v[128:131]\n"
"v_mfma_f32_16x16x32_bf16 v[128:131], a[60:63], a[124:127], v[128:131]\n"
"v_mfma_f32_16x16x32_bf16 v[100:103], a[52:55], a[124:127], v[100:103]\n"
"v_mfma_f32_16x16x32_bf16 v[100:103], a[48:51], a[120:123], v[100:103]\n"
"v_mfma_f32_16x16x32_bf16 v[104:107], a[48:51], a[128:131], v[104:107]\n"
"v_mfma_f32_16x16x32_bf16 v[104:107], a[52:55], a[132:135], v[104:107]\n"
"global_load_dwordx4 a[40:43], v0, s[34:35] offset:-2048 sc1\n"
"v_mfma_f32_16x16x32_bf16 v[132:135], a[60:63], a[132:135], v[132:135]\n"
"v_mfma_f32_16x16x32_bf16 v[132:135], a[56:59], a[128:131], v[132:135]\n"
"v_mfma_f32_16x16x32_bf16 v[136:139], a[56:59], a[136:139], v[136:139]\n"
"v_mfma_f32_16x16x32_bf16 v[136:139], a[60:63], a[140:143], v[136:139]\n"
"v_mfma_f32_16x16x32_bf16 v[108:111], a[52:55], a[140:143], v[108:111]\n"
"v_mfma_f32_16x16x32_bf16 v[108:111], a[48:51], a[136:139], v[108:111]\n"
"v_mfma_f32_16x16x32_bf16 v[112:115], a[48:51], a[144:147], v[112:115]\n"
"v_mfma_f32_16x16x32_bf16 v[112:115], a[52:55], a[148:151], v[112:115]\n"
"global_load_dwordx4 a[44:47], v0, s[34:35] offset:-1024 sc1\n"
"v_mfma_f32_16x16x32_bf16 v[140:143], a[60:63], a[148:151], v[140:143]\n"
"v_mfma_f32_16x16x32_bf16 v[140:143], a[56:59], a[144:147], v[140:143]\n"
"v_mfma_f32_16x16x32_bf16 a[12:15], a[56:59], a[152:155], a[12:15]\n"
"v_mfma_f32_16x16x32_bf16 a[12:15], a[60:63], a[156:159], a[12:15]\n"
"v_mfma_f32_16x16x32_bf16 a[8:11], a[52:55], a[156:159], a[8:11]\n"
"v_mfma_f32_16x16x32_bf16 a[8:11], a[48:51], a[152:155], a[8:11]\n"
"s_waitcnt vmcnt(8)\n"
"v_mfma_f32_16x16x32_bf16 v[144:147], a[64:67], a[96:99], v[144:147]\n"
"v_mfma_f32_16x16x32_bf16 v[144:147], a[68:71], a[100:103], v[144:147]\n"
"global_load_dwordx4 a[48:51], v0, s[34:35] offset:0 sc1\n"
"v_mfma_f32_16x16x32_bf16 v[172:175], a[76:79], a[100:103], v[172:175]\n"
"v_mfma_f32_16x16x32_bf16 v[172:175], a[72:75], a[96:99], v[172:175]\n"
"v_mfma_f32_16x16x32_bf16 v[176:179], a[72:75], a[104:107], v[176:179]\n"
"v_mfma_f32_16x16x32_bf16 v[176:179], a[76:79], a[108:111], v[176:179]\n"
"v_mfma_f32_16x16x32_bf16 v[148:151], a[68:71], a[108:111], v[148:151]\n"
"v_mfma_f32_16x16x32_bf16 v[148:151], a[64:67], a[104:107], v[148:151]\n"
"v_mfma_f32_16x16x32_bf16 v[152:155], a[64:67], a[112:115], v[152:155]\n"
"v_mfma_f32_16x16x32_bf16 v[152:155], a[68:71], a[116:119], v[152:155]\n"
"global_load_dwordx4 a[52:55], v0, s[34:35] offset:1024 sc1\n"
"v_mfma_f32_16x16x32_bf16 v[180:183], a[76:79], a[116:119], v[180:183]\n"
"v_mfma_f32_16x16x32_bf16 v[180:183], a[72:75], a[112:115], v[180:183]\n"
"v_mfma_f32_16x16x32_bf16 v[184:187], a[72:75], a[120:123], v[184:187]\n"
"v_mfma_f32_16x16x32_bf16 v[184:187], a[76:79], a[124:127], v[184:187]\n"
"v_mfma_f32_16x16x32_bf16 v[156:159], a[68:71], a[124:127], v[156:159]\n"
"v_mfma_f32_16x16x32_bf16 v[156:159], a[64:67], a[120:123], v[156:159]\n"
"v_mfma_f32_16x16x32_bf16 v[160:163], a[64:67], a[128:131], v[160:163]\n"
"v_mfma_f32_16x16x32_bf16 v[160:163], a[68:71], a[132:135], v[160:163]\n"
"global_load_dwordx4 a[56:59], v0, s[34:35] offset:2048 sc1\n"
"v_mfma_f32_16x16x32_bf16 v[188:191], a[76:79], a[132:135], v[188:191]\n"
"v_mfma_f32_16x16x32_bf16 v[188:191], a[72:75], a[128:131], v[188:191]\n"
"v_mfma_f32_16x16x32_bf16 v[192:195], a[72:75], a[136:139], v[192:195]\n"
"v_mfma_f32_16x16x32_bf16 v[192:195], a[76:79], a[140:143], v[192:195]\n"
"v_mfma_f32_16x16x32_bf16 v[164:167], a[68:71], a[140:143], v[164:167]\n"
"v_mfma_f32_16x16x32_bf16 v[164:167], a[64:67], a[136:139], v[164:167]\n"
"v_mfma_f32_16x16x32_bf16 v[168:171], a[64:67], a[144:147], v[168:171]\n"
"v_mfma_f32_16x16x32_bf16 v[168:171], a[68:71], a[148:151], v[168:171]\n"
"global_load_dwordx4 a[60:63], v0, s[34:35] offset:3072 sc1\n"
"v_mfma_f32_16x16x32_bf16 v[196:199], a[76:79], a[148:151], v[196:199]\n"
"v_mfma_f32_16x16x32_bf16 v[196:199], a[72:75], a[144:147], v[196:199]\n"
"v_mfma_f32_16x16x32_bf16 a[20:23], a[72:75], a[152:155], a[20:23]\n"
"s_add_u32 s34, s34, 8192\n"
"v_mfma_f32_16x16x32_bf16 a[20:23], a[76:79], a[156:159], a[20:23]\n"
"v_mfma_f32_16x16x32_bf16 a[16:19], a[68:71], a[156:159], a[16:19]\n"
"v_mfma_f32_16x16x32_bf16 a[16:19], a[64:67], a[152:155], a[16:19]\n"
"s_waitcnt vmcnt(8)\n"
"v_mfma_f32_16x16x32_bf16 v[200:203], a[80:83], a[96:99], v[200:203]\n"
"v_mfma_f32_16x16x32_bf16 v[200:203], a[84:87], a[100:103], v[200:203]\n"
"global_load_dwordx4 a[64:67], v0, s[34:35] offset:-4096 sc1\n"
"v_mfma_f32_16x16x32_bf16 v[228:231], a[92:95], a[100:103], v[228:231]\n"
"ds_read_b128 a[100:103], v2 offset:17408\n"
"v_mfma_f32_16x16x32_bf16 v[228:231], a[88:91], a[96:99], v[228:231]\n"
"ds_read_b128 a[96:99], v2 offset:16384\n"
"v_mfma_f32_16x16x32_bf16 v[232:235], a[88:91], a[104:107], v[232:235]\n"
"v_mfma_f32_16x16x32_bf16 v[232:235], a[92:95], a[108:111], v[232:235]\n"
"v_mfma_f32_16x16x32_bf16 v[204:207], a[84:87], a[108:111], v[204:207]\n"
"ds_read_b128 a[108:111], v2 offset:19456\n"
"v_mfma_f32_16x16x32_bf16 v[204:207], a[80:83], a[104:107], v[204:207]\n"
"ds_read_b128 a[104:107], v2 offset:18432\n"
"v_mfma_f32_16x16x32_bf16 v[208:211], a[80:83], a[112:115], v[208:211]\n"
"v_mfma_f32_16x16x32_bf16 v[208:211], a[84:87], a[116:119], v[208:211]\n"
"global_load_dwordx4 a[68:71], v0, s[34:35] offset:-3072 sc1\n"
"v_mfma_f32_16x16x32_bf16 v[236:239], a[92:95], a[116:119], v[236:239]\n"
"ds_read_b128 a[116:119], v2 offset:21504\n"
"v_mfma_f32_16x16x32_bf16 v[236:239], a[88:91], a[112:115], v[236:239]\n"
"ds_read_b128 a[112:115], v2 offset:20480\n"
"v_mfma_f32_16x16x32_bf16 v[240:243], a[88:91], a[120:123], v[240:243]\n"
"v_mfma_f32_16x16x32_bf16 v[240:243], a[92:95], a[124:127], v[240:243]\n"
"v_mfma_f32_16x16x32_bf16 v[212:215], a[84:87], a[124:127], v[212:215]\n"
"ds_read_b128 a[124:127], v2 offset:23552\n"
"v_mfma_f32_16x16x32_bf16 v[212:215], a[80:83], a[120:123], v[212:215]\n"
"ds_read_b128 a[120:123], v2 offset:22528\n"
"v_mfma_f32_16x16x32_bf16 v[216:219], a[80:83], a[128:131], v[216:219]\n"
"v_mfma_f32_16x16x32_bf16 v[216:219], a[84:87], a[132:135], v[216:219]\n"
"global_load_dwordx4 a[72:75], v0, s[34:35] offset:-2048 sc1\n"
"v_mfma_f32_16x16x32_bf16 v[244:247], a[92:95], a[132:135], v[244:247]\n"
"ds_read_b128 a[132:135], v2 offset:25600\n"
"v_mfma_f32_16x16x32_bf16 v[244:247], a[88:91], a[128:131], v[244:247]\n"
"ds_read_b128 a[128:131], v2 offset:24576\n"
"v_mfma_f32_16x16x32_bf16 v[248:251], a[88:91], a[136:139], v[248:251]\n"
"v_mfma_f32_16x16x32_bf16 v[248:251], a[92:95], a[140:143], v[248:251]\n"
"v_mfma_f32_16x16x32_bf16 v[220:223], a[84:87], a[140:143], v[220:223]\n"
"ds_read_b128 a[140:143], v2 offset:27648\n"
"v_mfma_f32_16x16x32_bf16 v[220:223], a[80:83], a[136:139], v[220:223]\n"
"ds_read_b128 a[136:139], v2 offset:26624\n"
"v_mfma_f32_16x16x32_bf16 v[224:227], a[80:83], a[144:147], v[224:227]\n"
"v_mfma_f32_16x16x32_bf16 v[224:227], a[84:87], a[148:151], v[224:227]\n"
"global_load_dwordx4 a[76:79], v0, s[34:35] offset:-1024 sc1\n"
"v_mfma_f32_16x16x32_bf16 v[252:255], a[92:95], a[148:151], v[252:255]\n"
"ds_read_b128 a[148:151], v2 offset:29696\n"
"v_mfma_f32_16x16x32_bf16 v[252:255], a[88:91], a[144:147], v[252:255]\n"
"ds_read_b128 a[144:147], v2 offset:28672\n"
"v_mfma_f32_16x16x32_bf16 a[28:31], a[88:91], a[152:155], a[28:31]\n"
"v_mfma_f32_16x16x32_bf16 a[28:31], a[92:95], a[156:159], a[28:31]\n"
"v_mfma_f32_16x16x32_bf16 a[24:27], a[84:87], a[156:159], a[24:27]\n"
"ds_read_b128 a[156:159], v2 offset:31744\n"
"v_mfma_f32_16x16x32_bf16 a[24:27], a[80:83], a[152:155], a[24:27]\n"
"ds_read_b128 a[152:155], v2 offset:30720\n"
"L_P5:\n"
"s_waitcnt vmcnt(8) lgkmcnt(12)\n"
"v_mfma_f32_16x16x32_bf16 v[32:35], a[32:35], a[96:99], v[32:35]\n"
"v_mfma_f32_16x16x32_bf16 v[32:35], a[36:39], a[100:103], v[32:35]\n"
"global_load_dwordx4 a[80:83], v0, s[34:35] offset:0 sc1\n"
"v_mfma_f32_16x16x32_bf16 v[60:63], a[44:47], a[100:103], v[60:63]\n"
"s_barrier\n"
"v_mfma_f32_16x16x32_bf16 v[60:63], a[40:43], a[96:99], v[60:63]\n"
"s_add_u32 s48, s48, s70\n"
"s_addc_u32 s49, s49, s71\n"
"v_mfma_f32_16x16x32_bf16 v[64:67], a[40:43], a[104:107], v[64:67]\n"
"v_mfma_f32_16x16x32_bf16 v[64:67], a[44:47], a[108:111], v[64:67]\n"
"v_mfma_f32_16x16x32_bf16 v[36:39], a[36:39], a[108:111], v[36:39]\n"
"s_add_u32 s52, s52, s70\n"
"s_addc_u32 s53, s53, s71\n"
"v_mfma_f32_16x16x32_bf16 v[36:39], a[32:35], a[104:107], v[36:39]\n"
"s_waitcnt lgkmcnt(8)\n"
"v_mfma_f32_16x16x32_bf16 v[40:43], a[32:35], a[112:115], v[40:43]\n"
"s_add_u32 s54, s54, s70\n"
"s_addc_u32 s55, s55, s71\n"
"v_mfma_f32_16x16x32_bf16 v[40:43], a[36:39], a[116:119], v[40:43]\n"
"global_load_dwordx4 a[84:87], v0, s[34:35] offset:1024 sc1\n"
"v_mfma_f32_16x16x32_bf16 v[68:71], a[44:47], a[116:119], v[68:71]\n"
"v_mfma_f32_16x16x32_bf16 v[68:71], a[40:43], a[112:115], v[68:71]\n"
"v_mfma_f32_16x16x32_bf16 v[72:75], a[40:43], a[120:123], v[72:75]\n"
"v_mfma_f32_16x16x32_bf16 v[72:75], a[44:47], a[124:127], v[72:75]\n"
"s_add_u32 s61, s61, 2048\n"
"v_mfma_f32_16x16x32_bf16 v[44:47], a[36:39], a[124:127], v[44:47]\n"
"v_mfma_f32_16x16x32_bf16 v[44:47], a[32:35], a[120:123], v[44:47]\n"
"s_waitcnt lgkmcnt(4)\n"
"v_mfma_f32_16x16x32_bf16 v[48:51], a[32:35], a[128:131], v[48:51]\n"
"v_lshlrev_b32 v16, 16, v18\n"
"v_mfma_f32_16x16x32_bf16 v[48:51], a[36:39], a[132:135], v[48:51]\n"
"global_load_dwordx4 a[88:91], v0, s[34:35] offset:2048 sc1\n"
"v_and_b32 v17, s63, v18\n"
"v_mfma_f32_16x16x32_bf16 v[76:79], a[44:47], a[132:135], v[76:79]\n"
"v_lshlrev_b32 v18, 16, v19\n"
"v_mfma_f32_16x16x32_bf16 v[76:79], a[40:43], a[128:131], v[76:79]\n"
"v_and_b32 v19, s63, v19\n"
"v_mfma_f32_16x16x32_bf16 v[80:83], a[40:43], a[136:139], v[80:83]\n"
"v_add_f32 v144, v144, v16\n"
"v_add_f32 v145, v145, v17\n"
"v_mfma_f32_16x16x32_bf16 v[80:83], a[44:47], a[140:143], v[80:83]\n"
"v_add_f32 v146, v146, v18\n"
"v_mfma_f32_16x16x32_bf16 v[52:55], a[36:39], a[140:143], v[52:55]\n"
"v_add_f32 v147, v147, v19\n"
"v_mfma_f32_16x16x32_bf16 v[52:55], a[32:35], a[136:139], v[52:55]\n"
"s_waitcnt lgkmcnt(0)\n"
"v_add_f32 v148, v148, v16\n"
"v_mfma_f32_16x16x32_bf16 v[56:59], a[32:35], a[144:147], v[56:59]\n"
"v_add_f32 v149, v149, v17\n"
"v_mfma_f32_16x16x32_bf16 v[56:59], a[36:39], a[148:151], v[56:59]\n"
"global_load_dwordx4 a[92:95], v0, s[34:35] offset:3072 sc1\n"
"v_add_f32 v150, v150, v18\n"
"v_mfma_f32_16x16x32_bf16 v[84:87], a[44:47], a[148:151], v[84:87]\n"
"v_add_f32 v151, v151, v19\n"
"v_mfma_f32_16x16x32_bf16 v[84:87], a[40:43], a[144:147], v[84:87]\n"
"v_add_f32 v152, v152, v16\n"
"v_mfma_f32_16x16x32_bf16 a[4:7], a[40:43], a[152:155], a[4:7]\n"
"s_add_u32 s34, s34, 8192\n"
"v_add_f32 v153, v153, v17\n"
"v_mfma_f32_16x16x32_bf16 a[4:7], a[44:47], a[156:159], a[4:7]\n"
"v_add_f32 v154, v154, v18\n"
"v_mfma_f32_16x16x32_bf16 a[0:3], a[36:39], a[156:159], a[0:3]\n"
"v_add_f32 v155, v155, v19\n"
"v_add_f32 v156, v156, v16\n"
"v_mfma_f32_16x16x32_bf16 a[0:3], a[32:35], a[152:155], a[0:3]\n"
"s_waitcnt vmcnt(8)\n"
"v_add_f32 v157, v157, v17\n"
"v_mfma_f32_16x16x32_bf16 v[88:91], a[48:51], a[96:99], v[88:91]\n"
"v_add_f32 v158, v158, v18\n"
"v_mfma_f32_16x16x32_bf16 v[88:91], a[52:55], a[100:103], v[88:91]\n"
"global_load_dwordx4 a[32:35], v0, s[34:35] offset:-4096 sc1\n"
"v_add_f32 v159, v159, v19\n"
"v_mfma_f32_16x16x32_bf16 v[116:119], a[60:63], a[100:103], v[116:119]\n"
"ds_read_b128 a[100:103], v3 offset:17408\n"
"v_add_f32 v160, v160, v16\n"
"v_mfma_f32_16x16x32_bf16 v[116:119], a[56:59], a[96:99], v[116:119]\n"
"ds_read_b128 a[96:99], v3 offset:16384\n"
"v_add_f32 v161, v161, v17\n"
"v_mfma_f32_16x16x32_bf16 v[120:123], a[56:59], a[104:107], v[120:123]\n"
"v_add_f32 v162, v162, v18\n"
"v_mfma_f32_16x16x32_bf16 v[120:123], a[60:63], a[108:111], v[120:123]\n"
"v_add_f32 v163, v163, v19\n"
"v_mfma_f32_16x16x32_bf16 v[92:95], a[52:55], a[108:111], v[92:95]\n"
"ds_read_b128 a[108:111], v3 offset:19456\n"
"v_add_f32 v164, v164, v16\n"
"v_mfma_f32_16x16x32_bf16 v[92:95], a[48:51], a[104:107], v[92:95]\n"
"ds_read_b128 a[104:107], v3 offset:18432\n"
"v_add_f32 v165, v165, v17\n"
"v_mfma_f32_16x16x32_bf16 v[96:99], a[48:51], a[112:115], v[96:99]\n"
"v_add_f32 v166, v166, v18\n"
"v_add_f32 v167, v167, v19\n"
"v_mfma_f32_16x16x32_bf16 v[96:99], a[52:55], a[116:119], v[96:99]\n"
"global_load_dwordx4 a[36:39], v0, s[34:35] offset:-3072 sc1\n"
"v_add_f32 v168, v168, v16\n"
"v_mfma_f32_16x16x32_bf16 v[124:127], a[60:63], a[116:119], v[124:127]\n"
"ds_read_b128 a[116:119], v3 offset:21504\n"
"v_add_f32 v169, v169, v17\n"
"v_mfma_f32_16x16x32_bf16 v[124:127], a[56:59], a[112:115], v[124:127]\n"
"ds_read_b128 a[112:115], v3 offset:20480\n"
"v_add_f32 v170, v170, v18\n"
"v_mfma_f32_16x16x32_bf16 v[128:131], a[56:59], a[120:123], v[128:131]\n"
"v_add_f32 v171, v171, v19\n"
"v_mfma_f32_16x16x32_bf16 v[128:131], a[60:63], a[124:127], v[128:131]\n"
"v_accvgpr_read_b32 v12, a16\n"
"v_mfma_f32_16x16x32_bf16 v[100:103], a[52:55], a[124:127], v[100:103]\n"
"ds_read_b128 a[124:127], v3 offset:23552\n"
"v_accvgpr_read_b32 v13, a17\n"
"v_mfma_f32_16x16x32_bf16 v[100:103], a[48:51], a[120:123], v[100:103]\n"
"ds_read_b128 a[120:123], v3 offset:22528\n"
"v_accvgpr_read_b32 v14, a18\n"
"v_mfma_f32_16x16x32_bf16 v[104:107], a[48:51], a[128:131], v[104:107]\n"
"v_accvgpr_read_b32 v15, a19\n"
"v_add_f32 v12, v12, v16\n"
"v_mfma_f32_16x16x32_bf16 v[104:107], a[52:55], a[132:135], v[104:107]\n"
"global_load_dwordx4 a[40:43], v0, s[34:35] offset:-2048 sc1\n"
"v_add_f32 v13, v13, v17\n"
"v_mfma_f32_16x16x32_bf16 v[132:135], a[60:63], a[132:135], v[132:135]\n"
"ds_read_b128 a[132:135], v3 offset:25600\n"
"v_add_f32 v14, v14, v18\n"
"v_mfma_f32_16x16x32_bf16 v[132:135], a[56:59], a[128:131], v[132:135]\n"
"ds_read_b128 a[128:131], v3 offset:24576\n"
"v_add_f32 v15, v15, v19\n"
"v_mfma_f32_16x16x32_bf16 v[136:139], a[56:59], a[136:139], v[136:139]\n"
"v_accvgpr_write_b32 a16, v12\n"
"v_mfma_f32_16x16x32_bf16 v[136:139], a[60:63], a[140:143], v[136:139]\n"
"v_accvgpr_write_b32 a17, v13\n"
"v_mfma_f32_16x16x32_bf16 v[108:111], a[52:55], a[140:143], v[108:111]\n"
"ds_read_b128 a[140:143], v3 offset:27648\n"
"v_accvgpr_write_b32 a18, v14\n"
"v_mfma_f32_16x16x32_bf16 v[108:111], a[48:51], a[136:139], v[108:111]\n"
"ds_read_b128 a[136:139], v3 offset:26624\n"
"v_accvgpr_write_b32 a19, v15\n"
"v_mfma_f32_16x16x32_bf16 v[112:115], a[48:51], a[144:147], v[112:115]\n"
"v_lshlrev_b32 v20, 16, v22\n"
"v_and_b32 v21, s63, v22\n"
"v_mfma_f32_16x16x32_bf16 v[112:115], a[52:55], a[148:151], v[112:115]\n"
"global_load_dwordx4 a[44:47], v0, s[34:35] offset:-1024 sc1\n"
"v_lshlrev_b32 v22, 16, v23\n"
"v_mfma_f32_16x16x32_bf16 v[140:143], a[60:63], a[148:151], v[140:143]\n"
"ds_read_b128 a[148:151], v3 offset:29696\n"
"v_and_b32 v23, s63, v23\n"
"v_mfma_f32_16x16x32_bf16 v[140:143], a[56:59], a[144:147], v[140:143]\n"
"ds_read_b128 a[144:147], v3 offset:28672\n"
"v_add_f32 v172, v172, v20\n"
"v_mfma_f32_16x16x32_bf16 a[12:15], a[56:59], a[152:155], a[12:15]\n"
"v_add_f32 v173, v173, v21\n"
"v_mfma_f32_16x16x32_bf16 a[12:15], a[60:63], a[156:159], a[12:15]\n"
"v_add_f32 v174, v174, v22\n"
"v_mfma_f32_16x16x32_bf16 a[8:11], a[52:55], a[156:159], a[8:11]\n"
"ds_read_b128 a[156:159], v3 offset:31744\n"
"v_add_f32 v175, v175, v23\n"
"v_mfma_f32_16x16x32_bf16 a[8:11], a[48:51], a[152:155], a[8:11]\n"
"s_waitcnt vmcnt(8) lgkmcnt(11)\n"
"ds_read_b128 a[152:155], v3 offset:30720\n"
"v_add_f32 v176, v176, v20\n"
"v_mfma_f32_16x16x32_bf16 v[32:35], a[64:67], a[96:99], v[32:35]\n"
"v_add_f32 v177, v177, v21\n"
"v_mfma_f32_16x16x32_bf16 v[32:35], a[68:71], a[100:103], v[32:35]\n"
"global_load_dwordx4 a[48:51], v0, s[34:35] offset:0 sc1\n"
"v_add_f32 v178, v178, v22\n"
"v_mfma_f32_16x16x32_bf16 v[60:63], a[76:79], a[100:103], v[60:63]\n"
"v_add_f32 v179, v179, v23\n"
"v_mfma_f32_16x16x32_bf16 v[60:63], a[72:75], a[96:99], v[60:63]\n"
"v_add_f32 v180, v180, v20\n"
"v_add_f32 v181, v181, v21\n"
"v_mfma_f32_16x16x32_bf16 v[64:67], a[72:75], a[104:107], v[64:67]\n"
"v_add_f32 v182, v182, v22\n"
"v_mfma_f32_16x16x32_bf16 v[64:67], a[76:79], a[108:111], v[64:67]\n"
"v_add_f32 v183, v183, v23\n"
"v_mfma_f32_16x16x32_bf16 v[36:39], a[68:71], a[108:111], v[36:39]\n"
"v_add_f32 v184, v184, v20\n"
"v_mfma_f32_16x16x32_bf16 v[36:39], a[64:67], a[104:107], v[36:39]\n"
"s_waitcnt lgkmcnt(8)\n"
"v_add_f32 v185, v185, v21\n"
"v_mfma_f32_16x16x32_bf16 v[40:43], a[64:67], a[112:115], v[40:43]\n"
"v_add_f32 v186, v186, v22\n"
"v_mfma_f32_16x16x32_bf16 v[40:43], a[68:71], a[116:119], v[40:43]\n"
"global_load_dwordx4 a[52:55], v0, s[34:35] offset:1024 sc1\n"
"v_add_f32 v187, v187, v23\n"
"v_mfma_f32_16x16x32_bf16 v[68:71], a[76:79], a[116:119], v[68:71]\n"
"v_add_f32 v188, v188, v20\n"
"v_mfma_f32_16x16x32_bf16 v[68:71], a[72:75], a[112:115], v[68:71]\n"
"v_add_f32 v189, v189, v21\n"
"v_mfma_f32_16x16x32_bf16 v[72:75], a[72:75], a[120:123], v[72:75]\n"
"v_add_f32 v190, v190, v22\n"
"v_add_f32 v191, v191, v23\n"
"v_mfma_f32_16x16x32_bf16 v[72:75], a[76:79], a[124:127], v[72:75]\n"
"v_add_f32 v192, v192, v20\n"
"v_mfma_f32_16x16x32_bf16 v[44:47], a[68:71], a[124:127], v[44:47]\n"
"v_add_f32 v193, v193, v21\n"
"v_mfma_f32_16x16x32_bf16 v[44:47], a[64:67], a[120:123], v[44:47]\n"
"s_waitcnt lgkmcnt(4)\n"
"v_add_f32 v194, v194, v22\n"
"v_mfma_f32_16x16x32_bf16 v[48:51], a[64:67], a[128:131], v[48:51]\n"
"v_add_f32 v195, v195, v23\n"
"v_mfma_f32_16x16x32_bf16 v[48:51], a[68:71], a[132:135], v[48:51]\n"
"global_load_dwordx4 a[56:59], v0, s[34:35] offset:2048 sc1\n"
"v_add_f32 v196, v196, v20\n"
"v_mfma_f32_16x16x32_bf16 v[76:79], a[76:79], a[132:135], v[76:79]\n"
"v_add_f32 v197, v197, v21\n"
"v_mfma_f32_16x16x32_bf16 v[76:79], a[72:75], a[128:131], v[76:79]\n"
"v_add_f32 v198, v198, v22\n"
"v_mfma_f32_16x16x32_bf16 v[80:83], a[72:75], a[136:139], v[80:83]\n"
"v_add_f32 v199, v199, v23\n"
"v_mfma_f32_16x16x32_bf16 v[80:83], a[76:79], a[140:143], v[80:83]\n"
"v_accvgpr_read_b32 v12, a20\n"
"v_mfma_f32_16x16x32_bf16 v[52:55], a[68:71], a[140:143], v[52:55]\n"
"v_accvgpr_read_b32 v13, a21\n"
"v_accvgpr_read_b32 v14, a22\n"
"v_mfma_f32_16x16x32_bf16 v[52:55], a[64:67], a[136:139], v[52:55]\n"
"s_waitcnt lgkmcnt(0)\n"
"v_accvgpr_read_b32 v15, a23\n"
"v_mfma_f32_16x16x32_bf16 v[56:59], a[64:67], a[144:147], v[56:59]\n"
"v_add_f32 v12, v12, v20\n"
"v_mfma_f32_16x16x32_bf16 v[56:59], a[68:71], a[148:151], v[56:59]\n"
"global_load_dwordx4 a[60:63], v0, s[34:35] offset:3072 sc1\n"
"v_add_f32 v13, v13, v21\n"
"v_mfma_f32_16x16x32_bf16 v[84:87], a[76:79], a[148:151], v[84:87]\n"
"v_add_f32 v14, v14, v22\n"
"v_mfma_f32_16x16x32_bf16 v[84:87], a[72:75], a[144:147], v[84:87]\n"
"v_add_f32 v15, v15, v23\n"
"v_mfma_f32_16x16x32_bf16 a[4:7], a[72:75], a[152:155], a[4:7]\n"
"s_add_u32 s34, s34, 8192\n"
"v_accvgpr_write_b32 a20, v12\n"
"v_mfma_f32_16x16x32_bf16 a[4:7], a[76:79], a[156:159], a[4:7]\n"
"v_accvgpr_write_b32 a21, v13\n"
"v_mfma_f32_16x16x32_bf16 a[0:3], a[68:71], a[156:159], a[0:3]\n"
"v_accvgpr_write_b32 a22, v14\n"
"v_mfma_f32_16x16x32_bf16 a[0:3], a[64:67], a[152:155], a[0:3]\n"
"s_waitcnt vmcnt(8)\n"
"v_accvgpr_write_b32 a23, v15\n"
"v_lshlrev_b32 v24, 16, v26\n"
"v_mfma_f32_16x16x32_bf16 v[88:91], a[80:83], a[96:99], v[88:91]\n"
"v_and_b32 v25, s63, v26\n"
"v_mfma_f32_16x16x32_bf16 v[88:91], a[84:87], a[100:103], v[88:91]\n"
"global_load_dwordx4 a[64:67], v0, s[34:35] offset:-4096 sc1\n"
"v_lshlrev_b32 v26, 16, v27\n"
"v_mfma_f32_16x16x32_bf16 v[116:119], a[92:95], a[100:103], v[116:119]\n"
"ds_read_b128 a[100:103], v4 offset:17408\n"
"v_and_b32 v27, s63, v27\n"
"v_mfma_f32_16x16x32_bf16 v[116:119], a[88:91], a[96:99], v[116:119]\n"
"ds_read_b128 a[96:99], v4 offset:16384\n"
"v_add_f32 v200, v200, v24\n"
"v_mfma_f32_16x16x32_bf16 v[120:123], a[88:91], a[104:107], v[120:123]\n"
"v_add_f32 v201, v201, v25\n"
"v_mfma_f32_16x16x32_bf16 v[120:123], a[92:95], a[108:111], v[120:123]\n"
"v_add_f32 v202, v202, v26\n"
"v_mfma_f32_16x16x32_bf16 v[92:95], a[84:87], a[108:111], v[92:95]\n"
"ds_read_b128 a[108:111], v4 offset:19456\n"
"v_add_f32 v203, v203, v27\n"
"v_mfma_f32_16x16x32_bf16 v[92:95], a[80:83], a[104:107], v[92:95]\n"
"ds_read_b128 a[104:107], v4 offset:18432\n"
"v_add_f32 v204, v204, v24\n"
"v_mfma_f32_16x16x32_bf16 v[96:99], a[80:83], a[112:115], v[96:99]\n"
"v_add_f32 v205, v205, v25\n"
"v_add_f32 v206, v206, v26\n"
"v_mfma_f32_16x16x32_bf16 v[96:99], a[84:87], a[116:119], v[96:99]\n"
"global_load_dwordx4 a[68:71], v0, s[34:35] offset:-3072 sc1\n"
"v_add_f32 v207, v207, v27\n"
"v_mfma_f32_16x16x32_bf16 v[124:127], a[92:95], a[116:119], v[124:127]\n"
"ds_read_b128 a[116:119], v4 offset:21504\n"
"v_add_f32 v208, v208, v24\n"
"v_mfma_f32_16x16x32_bf16 v[124:127], a[88:91], a[112:115], v[124:127]\n"
"ds_read_b128 a[112:115], v4 offset:20480\n"
"v_add_f32 v209, v209, v25\n"
"v_mfma_f32_16x16x32_bf16 v[128:131], a[88:91], a[120:123], v[128:131]\n"
"v_add_f32 v210, v210, v26\n"
"v_mfma_f32_16x16x32_bf16 v[128:131], a[92:95], a[124:127], v[128:131]\n"
"v_add_f32 v211, v211, v27\n"
"v_mfma_f32_16x16x32_bf16 v[100:103], a[84:87], a[124:127], v[100:103]\n"
"ds_read_b128 a[124:127], v4 offset:23552\n"
"v_add_f32 v212, v212, v24\n"
"v_mfma_f32_16x16x32_bf16 v[100:103], a[80:83], a[120:123], v[100:103]\n"
"ds_read_b128 a[120:123], v4 offset:22528\n"
"v_add_f32 v213, v213, v25\n"
"v_mfma_f32_16x16x32_bf16 v[104:107], a[80:83], a[128:131], v[104:107]\n"
"v_add_f32 v214, v214, v26\n"
"v_add_f32 v215, v215, v27\n"
"v_mfma_f32_16x16x32_bf16 v[104:107], a[84:87], a[132:135], v[104:107]\n"
"global_load_dwordx4 a[72:75], v0, s[34:35] offset:-2048 sc1\n"
"v_add_f32 v216, v216, v24\n"
"v_mfma_f32_16x16x32_bf16 v[132:135], a[92:95], a[132:135], v[132:135]\n"
"ds_read_b128 a[132:135], v4 offset:25600\n"
"v_add_f32 v217, v217, v25\n"
"v_mfma_f32_16x16x32_bf16 v[132:135], a[88:91], a[128:131], v[132:135]\n"
"ds_read_b128 a[128:131], v4 offset:24576\n"
"v_add_f32 v218, v218, v26\n"
"v_mfma_f32_16x16x32_bf16 v[136:139], a[88:91], a[136:139], v[136:139]\n"
"v_add_f32 v219, v219, v27\n"
"v_mfma_f32_16x16x32_bf16 v[136:139], a[92:95], a[140:143], v[136:139]\n"
"v_add_f32 v220, v220, v24\n"
"v_mfma_f32_16x16x32_bf16 v[108:111], a[84:87], a[140:143], v[108:111]\n"
"ds_read_b128 a[140:143], v4 offset:27648\n"
"v_add_f32 v221, v221, v25\n"
"v_mfma_f32_16x16x32_bf16 v[108:111], a[80:83], a[136:139], v[108:111]\n"
"ds_read_b128 a[136:139], v4 offset:26624\n"
"v_add_f32 v222, v222, v26\n"
"v_mfma_f32_16x16x32_bf16 v[112:115], a[80:83], a[144:147], v[112:115]\n"
"v_add_f32 v223, v223, v27\n"
"v_mfma_f32_16x16x32_bf16 v[112:115], a[84:87], a[148:151], v[112:115]\n"
"global_load_dwordx4 a[76:79], v0, s[34:35] offset:-1024 sc1\n"
"v_add_f32 v224, v224, v24\n"
"v_mfma_f32_16x16x32_bf16 v[140:143], a[92:95], a[148:151], v[140:143]\n"
"ds_read_b128 a[148:151], v4 offset:29696\n"
"v_add_f32 v225, v225, v25\n"
"v_mfma_f32_16x16x32_bf16 v[140:143], a[88:91], a[144:147], v[140:143]\n"
"ds_read_b128 a[144:147], v4 offset:28672\n"
"v_add_f32 v226, v226, v26\n"
"v_mfma_f32_16x16x32_bf16 a[12:15], a[88:91], a[152:155], a[12:15]\n"
"v_add_f32 v227, v227, v27\n"
"v_accvgpr_read_b32 v12, a24\n"
"v_mfma_f32_16x16x32_bf16 a[12:15], a[92:95], a[156:159], a[12:15]\n"
"v_accvgpr_read_b32 v13, a25\n"
"v_mfma_f32_16x16x32_bf16 a[8:11], a[84:87], a[156:159], a[8:11]\n"
"ds_read_b128 a[156:159], v4 offset:31744\n"
"v_accvgpr_read_b32 v14, a26\n"
"v_mfma_f32_16x16x32_bf16 a[8:11], a[80:83], a[152:155], a[8:11]\n"
"s_waitcnt vmcnt(8) lgkmcnt(11)\n"
"ds_read_b128 a[152:155], v4 offset:30720\n"
"v_accvgpr_read_b32 v15, a27\n"
"v_mfma_f32_16x16x32_bf16 v[32:35], a[32:35], a[96:99], v[32:35]\n"
"v_add_f32 v12, v12, v24\n"
"v_mfma_f32_16x16x32_bf16 v[32:35], a[36:39], a[100:103], v[32:35]\n"
"global_load_dwordx4 a[80:83], v0, s[34:35] offset:0 sc1\n"
"v_add_f32 v13, v13, v25\n"
"v_mfma_f32_16x16x32_bf16 v[60:63], a[44:47], a[100:103], v[60:63]\n"
"v_add_f32 v14, v14, v26\n"
"v_mfma_f32_16x16x32_bf16 v[60:63], a[40:43], a[96:99], v[60:63]\n"
"v_add_f32 v15, v15, v27\n"
"v_mfma_f32_16x16x32_bf16 v[64:67], a[40:43], a[104:107], v[64:67]\n"
"v_accvgpr_write_b32 a24, v12\n"
"v_accvgpr_write_b32 a25, v13\n"
"v_mfma_f32_16x16x32_bf16 v[64:67], a[44:47], a[108:111], v[64:67]\n"
"v_accvgpr_write_b32 a26, v14\n"
"v_mfma_f32_16x16x32_bf16 v[36:39], a[36:39], a[108:111], v[36:39]\n"
"v_accvgpr_write_b32 a27, v15\n"
"v_mfma_f32_16x16x32_bf16 v[36:39], a[32:35], a[104:107], v[36:39]\n"
"s_waitcnt lgkmcnt(8)\n"
"v_lshlrev_b32 v28, 16, v30\n"
"v_mfma_f32_16x16x32_bf16 v[40:43], a[32:35], a[112:115], v[40:43]\n"
"v_and_b32 v29, s63, v30\n"
"v_mfma_f32_16x16x32_bf16 v[40:43], a[36:39], a[116:119], v[40:43]\n"
"global_load_dwordx4 a[84:87], v0, s[34:35] offset:1024 sc1\n"
"v_lshlrev_b32 v30, 16, v31\n"
"v_mfma_f32_16x16x32_bf16 v[68:71], a[44:47], a[116:119], v[68:71]\n"
"v_and_b32 v31, s63, v31\n"
"v_mfma_f32_16x16x32_bf16 v[68:71], a[40:43], a[112:115], v[68:71]\n"
"v_add_f32 v228, v228, v28\n"
"v_mfma_f32_16x16x32_bf16 v[72:75], a[40:43], a[120:123], v[72:75]\n"
"v_add_f32 v229, v229, v29\n"
"v_mfma_f32_16x16x32_bf16 v[72:75], a[44:47], a[124:127], v[72:75]\n"
"v_add_f32 v230, v230, v30\n"
"v_mfma_f32_16x16x32_bf16 v[44:47], a[36:39], a[124:127], v[44:47]\n"
"v_add_f32 v231, v231, v31\n"
"v_add_f32 v232, v232, v28\n"
"v_mfma_f32_16x16x32_bf16 v[44:47], a[32:35], a[120:123], v[44:47]\n"
"s_waitcnt lgkmcnt(4)\n"
"v_add_f32 v233, v233, v29\n"
"v_mfma_f32_16x16x32_bf16 v[48:51], a[32:35], a[128:131], v[48:51]\n"
"v_add_f32 v234, v234, v30\n"
"v_mfma_f32_16x16x32_bf16 v[48:51], a[36:39], a[132:135], v[48:51]\n"
"global_load_dwordx4 a[88:91], v0, s[34:35] offset:2048 sc1\n"
"v_add_f32 v235, v235, v31\n"
"v_mfma_f32_16x16x32_bf16 v[76:79], a[44:47], a[132:135], v[76:79]\n"
"v_add_f32 v236, v236, v28\n"
"v_mfma_f32_16x16x32_bf16 v[76:79], a[40:43], a[128:131], v[76:79]\n"
"v_add_f32 v237, v237, v29\n"
"v_mfma_f32_16x16x32_bf16 v[80:83], a[40:43], a[136:139], v[80:83]\n"
"v_add_f32 v238, v238, v30\n"
"v_mfma_f32_16x16x32_bf16 v[80:83], a[44:47], a[140:143], v[80:83]\n"
"v_add_f32 v239, v239, v31\n"
"v_mfma_f32_16x16x32_bf16 v[52:55], a[36:39], a[140:143], v[52:55]\n"
"v_add_f32 v240, v240, v28\n"
"v_mfma_f32_16x16x32_bf16 v[52:55], a[32:35], a[136:139], v[52:55]\n"
"s_waitcnt lgkmcnt(0)\n"
"v_add_f32 v241, v241, v29\n"
"v_add_f32 v242, v242, v30\n"
"v_mfma_f32_16x16x32_bf16 v[56:59], a[32:35], a[144:147], v[56:59]\n"
"v_add_f32 v243, v243, v31\n"
"v_mfma_f32_16x16x32_bf16 v[56:59], a[36:39], a[148:151], v[56:59]\n"
"global_load_dwordx4 a[92:95], v0, s[34:35] offset:3072 sc1\n"
"v_add_f32 v244, v244, v28\n"
"v_mfma_f32_16x16x32_bf16 v[84:87], a[44:47], a[148:151], v[84:87]\n"
"v_add_f32 v245, v245, v29\n"
"v_mfma_f32_16x16x32_bf16 v[84:87], a[40:43], a[144:147], v[84:87]\n"
"v_add_f32 v246, v246, v30\n"
"v_mfma_f32_16x16x32_bf16 a[4:7], a[40:43], a[152:155], a[4:7]\n"
"s_add_u32 s34, s34, 8192\n"
"v_add_f32 v247, v247, v31\n"
"v_mfma_f32_16x16x32_bf16 a[4:7], a[44:47], a[156:159], a[4:7]\n"
"v_add_f32 v248, v248, v28\n"
"v_mfma_f32_16x16x32_bf16 a[0:3], a[36:39], a[156:159], a[0:3]\n"
"v_add_f32 v249, v249, v29\n"
"v_mfma_f32_16x16x32_bf16 a[0:3], a[32:35], a[152:155], a[0:3]\n"
"s_waitcnt vmcnt(8)\n"
"v_add_f32 v250, v250, v30\n"
"v_mfma_f32_16x16x32_bf16 v[88:91], a[48:51], a[96:99], v[88:91]\n"
"v_add_f32 v251, v251, v31\n"
"v_add_f32 v252, v252, v28\n"
"v_mfma_f32_16x16x32_bf16 v[88:91], a[52:55], a[100:103], v[88:91]\n"
"global_load_dwordx4 a[32:35], v0, s[34:35] offset:-4096 sc1\n"
"v_add_f32 v253, v253, v29\n"
"v_mfma_f32_16x16x32_bf16 v[116:119], a[60:63], a[100:103], v[116:119]\n"
"ds_read_b128 a[100:103], v5 offset:17408\n"
"v_add_f32 v254, v254, v30\n"
"v_mfma_f32_16x16x32_bf16 v[116:119], a[56:59], a[96:99], v[116:119]\n"
"ds_read_b128 a[96:99], v5 offset:16384\n"
"v_add_f32 v255, v255, v31\n"
"v_mfma_f32_16x16x32_bf16 v[120:123], a[56:59], a[104:107], v[120:123]\n"
"v_accvgpr_read_b32 v12, a28\n"
"v_mfma_f32_16x16x32_bf16 v[120:123], a[60:63], a[108:111], v[120:123]\n"
"v_accvgpr_read_b32 v13, a29\n"
"v_mfma_f32_16x16x32_bf16 v[92:95], a[52:55], a[108:111], v[92:95]\n"
"ds_read_b128 a[108:111], v5 offset:19456\n"
"v_accvgpr_read_b32 v14, a30\n"
"v_mfma_f32_16x16x32_bf16 v[92:95], a[48:51], a[104:107], v[92:95]\n"
"ds_read_b128 a[104:107], v5 offset:18432\n"
"v_accvgpr_read_b32 v15, a31\n"
"v_mfma_f32_16x16x32_bf16 v[96:99], a[48:51], a[112:115], v[96:99]\n"
"v_add_f32 v12, v12, v28\n"
"v_add_f32 v13, v13, v29\n"
"v_mfma_f32_16x16x32_bf16 v[96:99], a[52:55], a[116:119], v[96:99]\n"
"global_load_dwordx4 a[36:39], v0, s[34:35] offset:-3072 sc1\n"
"v_add_f32 v14, v14, v30\n"
"v_mfma_f32_16x16x32_bf16 v[124:127], a[60:63], a[116:119], v[124:127]\n"
"ds_read_b128 a[116:119], v5 offset:21504\n"
"v_add_f32 v15, v15, v31\n"
"v_mfma_f32_16x16x32_bf16 v[124:127], a[56:59], a[112:115], v[124:127]\n"
"ds_read_b128 a[112:115], v5 offset:20480\n"
"v_accvgpr_write_b32 a28, v12\n"
"v_mfma_f32_16x16x32_bf16 v[128:131], a[56:59], a[120:123], v[128:131]\n"
"v_accvgpr_write_b32 a29, v13\n"
"v_mfma_f32_16x16x32_bf16 v[128:131], a[60:63], a[124:127], v[128:131]\n"
"v_accvgpr_write_b32 a30, v14\n"
"v_mfma_f32_16x16x32_bf16 v[100:103], a[52:55], a[124:127], v[100:103]\n"
"ds_read_b128 a[124:127], v5 offset:23552\n"
"v_accvgpr_write_b32 a31, v15\n"
"v_mfma_f32_16x16x32_bf16 v[100:103], a[48:51], a[120:123], v[100:103]\n"
"ds_read_b128 a[120:123], v5 offset:22528\n"
"v_mfma_f32_16x16x32_bf16 v[104:107], a[48:51], a[128:131], v[104:107]\n"
"v_mfma_f32_16x16x32_bf16 v[104:107], a[52:55], a[132:135], v[104:107]\n"
"global_load_dwordx4 a[40:43], v0, s[34:35] offset:-2048 sc1\n"
"v_mfma_f32_16x16x32_bf16 v[132:135], a[60:63], a[132:135], v[132:135]\n"
"ds_read_b128 a[132:135], v5 offset:25600\n"
"v_mfma_f32_16x16x32_bf16 v[132:135], a[56:59], a[128:131], v[132:135]\n"
"ds_read_b128 a[128:131], v5 offset:24576\n"
"v_mfma_f32_16x16x32_bf16 v[136:139], a[56:59], a[136:139], v[136:139]\n"
"ds_read_b128 v[16:19], v7 offset:32768\n"
"v_mfma_f32_16x16x32_bf16 v[136:139], a[60:63], a[140:143], v[136:139]\n"
"v_mfma_f32_16x16x32_bf16 v[108:111], a[52:55], a[140:143], v[108:111]\n"
"ds_read_b128 a[140:143], v5 offset:27648\n"
"v_mfma_f32_16x16x32_bf16 v[108:111], a[48:51], a[136:139], v[108:111]\n"
"ds_read_b128 a[136:139], v5 offset:26624\n"
"v_mfma_f32_16x16x32_bf16 v[112:115], a[48:51], a[144:147], v[112:115]\n"
"v_mfma_f32_16x16x32_bf16 v[112:115], a[52:55], a[148:151], v[112:115]\n"
"global_load_dwordx4 a[44:47], v0, s[34:35] offset:-1024 sc1\n"
"v_mfma_f32_16x16x32_bf16 v[140:143], a[60:63], a[148:151], v[140:143]\n"
"ds_read_b128 a[148:151], v5 offset:29696\n"
"v_mfma_f32_16x16x32_bf16 v[140:143], a[56:59], a[144:147], v[140:143]\n"
"ds_read_b128 a[144:147], v5 offset:28672\n"
"v_mfma_f32_16x16x32_bf16 a[12:15], a[56:59], a[152:155], a[12:15]\n"
"v_mfma_f32_16x16x32_bf16 a[12:15], a[60:63], a[156:159], a[12:15]\n"
"v_mfma_f32_16x16x32_bf16 a[8:11], a[52:55], a[156:159], a[8:11]\n"
"ds_read_b128 a[156:159], v5 offset:31744\n"
"v_mfma_f32_16x16x32_bf16 a[8:11], a[48:51], a[152:155], a[8:11]\n"
"ds_read_b128 a[152:155], v5 offset:30720\n"
"s_branch L_P6\n"
"L_last:\n"
"s_waitcnt vmcnt(9) lgkmcnt(12)\n"
"v_mfma_f32_16x16x32_bf16 v[32:35], a[32:35], a[96:99], v[32:35]\n"
"s_add_u32 s34, s36, 1454080\n"
"s_add_u32 s48, s48, 131072\n"
"s_addc_u32 s49, s49, 0\n"
"v_mfma_f32_16x16x32_bf16 v[32:35], a[36:39], a[100:103], v[32:35]\n"
"global_load_dwordx4 a[80:83], v0, s[34:35] offset:0 sc1\n"
"v_mfma_f32_16x16x32_bf16 v[60:63], a[44:47], a[100:103], v[60:63]\n"
"s_barrier\n"
"v_mfma_f32_16x16x32_bf16 v[60:63], a[40:43], a[96:99], v[60:63]\n"
"v_mfma_f32_16x16x32_bf16 v[64:67], a[40:43], a[104:107], v[64:67]\n"
"v_mfma_f32_16x16x32_bf16 v[64:67], a[44:47], a[108:111], v[64:67]\n"
"v_mfma_f32_16x16x32_bf16 v[36:39], a[36:39], a[108:111], v[36:39]\n"
"v_subrev_u32 v8, s65, v8\n"
"v_mfma_f32_16x16x32_bf16 v[36:39], a[32:35], a[104:107], v[36:39]\n"
"s_waitcnt lgkmcnt(8)\n"
"v_xor_b32 v9, s79, v9\n"
"v_mfma_f32_16x16x32_bf16 v[40:43], a[32:35], a[112:115], v[40:43]\n"
"v_or_b32 v8, v8, v9\n"
"v_mfma_f32_16x16x32_bf16 v[40:43], a[36:39], a[116:119], v[40:43]\n"
"global_load_dwordx4 a[84:87], v0, s[34:35] offset:1024 sc1\n"
"v_cmp_lt_u32 vcc, 15, v8\n"
"v_mfma_f32_16x16x32_bf16 v[68:71], a[44:47], a[116:119], v[68:71]\n"
"s_cbranch_vccnz L_slow4\n"
"L_back4:\n"
"v_mfma_f32_16x16x32_bf16 v[68:71], a[40:43], a[112:115], v[68:71]\n"
"v_mfma_f32_16x16x32_bf16 v[72:75], a[40:43], a[120:123], v[72:75]\n"
"v_mfma_f32_16x16x32_bf16 v[72:75], a[44:47], a[124:127], v[72:75]\n"
"v_mfma_f32_16x16x32_bf16 v[44:47], a[36:39], a[124:127], v[44:47]\n"
"v_mfma_f32_16x16x32_bf16 v[44:47], a[32:35], a[120:123], v[44:47]\n"
"s_add_u32 m0, s64, 16384\n"
"s_waitcnt lgkmcnt(4)\n"
"v_mfma_f32_16x16x32_bf16 v[48:51], a[32:35], a[128:131], v[48:51]\n"
"v_mfma_f32_16x16x32_bf16 v[48:51], a[36:39], a[132:135], v[48:51]\n"
"global_load_dwordx4 a[88:91], v0, s[34:35] offset:2048 sc1\n"
"v_mfma_f32_16x16x32_bf16 v[76:79], a[44:47], a[132:135], v[76:79]\n"
"v_mfma_f32_16x16x32_bf16 v[76:79], a[40:43], a[128:131], v[76:79]\n"
"v_mfma_f32_16x16x32_bf16 v[80:83], a[40:43], a[136:139], v[80:83]\n"
"s_mov_b32 s97, s37\n"
"v_mfma_f32_16x16x32_bf16 v[80:83], a[44:47], a[140:143], v[80:83]\n"
"v_mfma_f32_16x16x32_bf16 v[52:55], a[36:39], a[140:143], v[52:55]\n"
"v_mfma_f32_16x16x32_bf16 v[52:55], a[32:35], a[136:139], v[52:55]\n"
"s_waitcnt lgkmcnt(0)\n"
"v_mfma_f32_16x16x32_bf16 v[56:59], a[32:35], a[144:147], v[56:59]\n"
"buffer_load_dwordx4 v0, s[48:51], s75 offen offset:0 sc1 lds\n"
"v_mfma_f32_16x16x32_bf16 v[56:59], a[36:39], a[148:151], v[56:59]\n"
"global_load_dwordx4 a[92:95], v0, s[34:35] offset:3072 sc1\n"
"v_mfma_f32_16x16x32_bf16 v[84:87], a[44:47], a[148:151], v[84:87]\n"
"v_mfma_f32_16x16x32_bf16 v[84:87], a[40:43], a[144:147], v[84:87]\n"
"v_mfma_f32_16x16x32_bf16 a[4:7], a[40:43], a[152:155], a[4:7]\n"
"v_mfma_f32_16x16x32_bf16 a[4:7], a[44:47], a[156:159], a[4:7]\n"
"v_mfma_f32_16x16x32_bf16 a[0:3], a[36:39], a[156:159], a[0:3]\n"
"v_mfma_f32_16x16x32_bf16 a[0:3], a[32:35], a[152:155], a[0:3]\n"
"s_waitcnt vmcnt(10)\n"
"v_mfma_f32_16x16x32_bf16 v[88:91], a[48:51], a[96:99], v[88:91]\n"
"s_add_u32 s34, s36, 1458176\n"
"v_mfma_f32_16x16x32_bf16 v[88:91], a[52:55], a[100:103], v[88:91]\n"
"global_load_dwordx4 a[32:35], v0, s[34:35] offset:0 sc1\n"
"v_mfma_f32_16x16x32_bf16 v[116:119], a[60:63], a[100:103], v[116:119]\n"
"buffer_load_dwordx4 v0, s[48:51], s75 offen offset:1024 sc1 lds\n"
"v_mfma_f32_16x16x32_bf16 v[116:119], a[56:59], a[96:99], v[116:119]\n"
"v_mfma_f32_16x16x32_bf16 v[120:123], a[56:59], a[104:107], v[120:123]\n"
"v_mfma_f32_16x16x32_bf16 v[120:123], a[60:63], a[108:111], v[120:123]\n"
"v_mfma_f32_16x16x32_bf16 v[92:95], a[52:55], a[108:111], v[92:95]\n"
"v_mfma_f32_16x16x32_bf16 v[92:95], a[48:51], a[104:107], v[92:95]\n"
"v_mfma_f32_16x16x32_bf16 v[96:99], a[48:51], a[112:115], v[96:99]\n"
"v_mfma_f32_16x16x32_bf16 v[96:99], a[52:55], a[116:119], v[96:99]\n"
"global_load_dwordx4 a[36:39], v0, s[34:35] offset:1024 sc1\n"
"v_mfma_f32_16x16x32_bf16 v[124:127], a[60:63], a[116:119], v[124:127]\n"
"v_mfma_f32_16x16x32_bf16 v[124:127], a[56:59], a[112:115], v[124:127]\n"
"v_mfma_f32_16x16x32_bf16 v[128:131], a[56:59], a[120:123], v[128:131]\n"
"buffer_load_dwordx4 v0, s[48:51], s75 offen offset:2048 sc1 lds\n"
"v_mfma_f32_16x16x32_bf16 v[128:131], a[60:63], a[124:127], v[128:131]\n"
"v_mfma_f32_16x16x32_bf16 v[100:103], a[52:55], a[124:127], v[100:103]\n"
"v_mfma_f32_16x16x32_bf16 v[100:103], a[48:51], a[120:123], v[100:103]\n"
"v_mfma_f32_16x16x32_bf16 v[104:107], a[48:51], a[128:131], v[104:107]\n"
"v_mfma_f32_16x16x32_bf16 v[104:107], a[52:55], a[132:135], v[104:107]\n"
"global_load_dwordx4 a[40:43], v0, s[34:35] offset:2048 sc1\n"
"v_mfma_f32_16x16x32_bf16 v[132:135], a[60:63], a[132:135], v[132:135]\n"
"v_mfma_f32_16x16x32_bf16 v[132:135], a[56:59], a[128:131], v[132:135]\n"
"v_mfma_f32_16x16x32_bf16 v[136:139], a[56:59], a[136:139], v[136:139]\n"
"v_mfma_f32_16x16x32_bf16 v[136:139], a[60:63], a[140:143], v[136:139]\n"
"buffer_load_dwordx4 v0, s[48:51], s75 offen offset:3072 sc1 lds\n"
"v_mfma_f32_16x16x32_bf16 v[108:111], a[52:55], a[140:143], v[108:111]\n"
"v_mfma_f32_16x16x32_bf16 v[108:111], a[48:51], a[136:139], v[108:111]\n"
"v_mfma_f32_16x16x32_bf16 v[112:115], a[48:51], a[144:147], v[112:115]\n"
"v_mfma_f32_16x16x32_bf16 v[112:115], a[52:55], a[148:151], v[112:115]\n"
"global_load_dwordx4 a[44:47], v0, s[34:35] offset:3072 sc1\n"
"v_mfma_f32_16x16x32_bf16 v[140:143], a[60:63], a[148:151], v[140:143]\n"
"v_mfma_f32_16x16x32_bf16 v[140:143], a[56:59], a[144:147], v[140:143]\n"
"v_mfma_f32_16x16x32_bf16 a[12:15], a[56:59], a[152:155], a[12:15]\n"
"v_mfma_f32_16x16x32_bf16 a[12:15], a[60:63], a[156:159], a[12:15]\n"
"v_mfma_f32_16x16x32_bf16 a[8:11], a[52:55], a[156:159], a[8:11]\n"
"v_mfma_f32_16x16x32_bf16 a[8:11], a[48:51], a[152:155], a[8:11]\n"
"s_add_u32 m0, s64, 20480\n"
"s_waitcnt vmcnt(12)\n"
"v_mfma_f32_16x16x32_bf16 v[144:147], a[64:67], a[96:99], v[144:147]\n"
"s_add_u32 s34, s36, 1462272\n"
"v_mfma_f32_16x16x32_bf16 v[144:147], a[68:71], a[100:103], v[144:147]\n"
"global_load_dwordx4 a[48:51], v0, s[34:35] offset:0 sc1\n"
"v_mfma_f32_16x16x32_bf16 v[172:175], a[76:79], a[100:103], v[172:175]\n"
"v_mfma_f32_16x16x32_bf16 v[172:175], a[72:75], a[96:99], v[172:175]\n"
"v_mfma_f32_16x16x32_bf16 v[176:179], a[72:75], a[104:107], v[176:179]\n"
"v_mfma_f32_16x16x32_bf16 v[176:179], a[76:79], a[108:111], v[176:179]\n"
"v_mfma_f32_16x16x32_bf16 v[148:151], a[68:71], a[108:111], v[148:151]\n"
"v_lshlrev_b32 v16, 16, v18\n"
"v_and_b32 v17, s63, v18\n"
"v_mfma_f32_16x16x32_bf16 v[148:151], a[64:67], a[104:107], v[148:151]\n"
"v_lshlrev_b32 v18, 16, v19\n"
"v_and_b32 v19, s63, v19\n"
"v_mfma_f32_16x16x32_bf16 v[152:155], a[64:67], a[112:115], v[152:155]\n"
"buffer_load_dwordx4 v1, s[48:51], s75 offen offset:0 sc1 lds\n"
"v_mfma_f32_16x16x32_bf16 v[152:155], a[68:71], a[116:119], v[152:155]\n"
"global_load_dwordx4 a[52:55], v0, s[34:35] offset:1024 sc1\n"
"v_add_f32 v88, v88, v16\n"
"v_mfma_f32_16x16x32_bf16 v[180:183], a[76:79], a[116:119], v[180:183]\n"
"v_add_f32 v89, v89, v17\n"
"v_add_f32 v90, v90, v18\n"
"v_mfma_f32_16x16x32_bf16 v[180:183], a[72:75], a[112:115], v[180:183]\n"
"v_add_f32 v91, v91, v19\n"
"v_add_f32 v92, v92, v16\n"
"v_mfma_f32_16x16x32_bf16 v[184:187], a[72:75], a[120:123], v[184:187]\n"
"v_add_f32 v93, v93, v17\n"
"v_add_f32 v94, v94, v18\n"
"v_mfma_f32_16x16x32_bf16 v[184:187], a[76:79], a[124:127], v[184:187]\n"
"v_add_f32 v95, v95, v19\n"
"v_add_f32 v96, v96, v16\n"
"v_mfma_f32_16x16x32_bf16 v[156:159], a[68:71], a[124:127], v[156:159]\n"
"v_add_f32 v97, v97, v17\n"
"v_add_f32 v98, v98, v18\n"
"v_mfma_f32_16x16x32_bf16 v[156:159], a[64:67], a[120:123], v[156:159]\n"
"v_add_f32 v99, v99, v19\n"
"v_add_f32 v100, v100, v16\n"
"v_mfma_f32_16x16x32_bf16 v[160:163], a[64:67], a[128:131], v[160:163]\n"
"v_add_f32 v101, v101, v17\n"
"v_add_f32 v102, v102, v18\n"
"v_mfma_f32_16x16x32_bf16 v[160:163], a[68:71], a[132:135], v[160:163]\n"
"global_load_dwordx4 a[56:59], v0, s[34:35] offset:2048 sc1\n"
"v_add_f32 v103, v103, v19\n"
"v_mfma_f32_16x16x32_bf16 v[188:191], a[76:79], a[132:135], v[188:191]\n"
"buffer_load_dwordx4 v1, s[48:51], s75 offen offset:1024 sc1 lds\n"
"v_mfma_f32_16x16x32_bf16 v[188:191], a[72:75], a[128:131], v[188:191]\n"
"v_add_f32 v104, v104, v16\n"
"v_add_f32 v105, v105, v17\n"
"v_mfma_f32_16x16x32_bf16 v[192:195], a[72:75], a[136:139], v[192:195]\n"
"v_add_f32 v106, v106, v18\n"
"v_add_f32 v107, v107, v19\n"
"v_mfma_f32_16x16x32_bf16 v[192:195], a[76:79], a[140:143], v[192:195]\n"
"v_add_f32 v108, v108, v16\n"
"v_add_f32 v109, v109, v17\n"
"v_mfma_f32_16x16x32_bf16 v[164:167], a[68:71], a[140:143], v[164:167]\n"
"v_add_f32 v110, v110, v18\n"
"v_add_f32 v111, v111, v19\n"
"v_mfma_f32_16x16x32_bf16 v[164:167], a[64:67], a[136:139], v[164:167]\n"
"v_add_f32 v112, v112, v16\n"
"v_add_f32 v113, v113, v17\n"
"v_mfma_f32_16x16x32_bf16 v[168:171], a[64:67], a[144:147], v[168:171]\n"
"v_add_f32 v114, v114, v18\n"
"v_add_f32 v115, v115, v19\n"
"v_mfma_f32_16x16x32_bf16 v[168:171], a[68:71], a[148:151], v[168:171]\n"
"global_load_dwordx4 a[60:63], v0, s[34:35] offset:3072 sc1\n"
"v_accvgpr_read_b32 v12, a8\n"
"v_mfma_f32_16x16x32_bf16 v[196:199], a[76:79], a[148:151], v[196:199]\n"
"v_accvgpr_read_b32 v13, a9\n"
"v_accvgpr_read_b32 v14, a10\n"
"v_mfma_f32_16x16x32_bf16 v[196:199], a[72:75], a[144:147], v[196:199]\n"
"buffer_load_dwordx4 v1, s[48:51], s75 offen offset:2048 sc1 lds\n"
"v_mfma_f32_16x16x32_bf16 a[20:23], a[72:75], a[152:155], a[20:23]\n"
"v_accvgpr_read_b32 v15, a11\n"
"v_add_f32 v12, v12, v16\n"
"v_mfma_f32_16x16x32_bf16 a[20:23], a[76:79], a[156:159], a[20:23]\n"
"v_add_f32 v13, v13, v17\n"
"v_add_f32 v14, v14, v18\n"
"v_mfma_f32_16x16x32_bf16 a[16:19], a[68:71], a[156:159], a[16:19]\n"
"v_add_f32 v15, v15, v19\n"
"v_accvgpr_write_b32 a8, v12\n"
"v_mfma_f32_16x16x32_bf16 a[16:19], a[64:67], a[152:155], a[16:19]\n"
"s_waitcnt vmcnt(14)\n"
"v_accvgpr_write_b32 a9, v13\n"
"v_accvgpr_write_b32 a10, v14\n"
"v_accvgpr_write_b32 a11, v15\n"
"v_mfma_f32_16x16x32_bf16 v[200:203], a[80:83], a[96:99], v[200:203]\n"
"s_add_u32 s34, s36, 1474560\n"
"v_lshlrev_b32 v20, 16, v22\n"
"v_and_b32 v21, s63, v22\n"
"v_mfma_f32_16x16x32_bf16 v[200:203], a[84:87], a[100:103], v[200:203]\n"
"global_load_dwordx4 a[64:67], v0, s[34:35] offset:0 sc1\n"
"v_lshlrev_b32 v22, 16, v23\n"
"v_mfma_f32_16x16x32_bf16 v[228:231], a[92:95], a[100:103], v[228:231]\n"
"ds_read_b128 a[100:103], v3 offset:1024\n"
"v_and_b32 v23, s63, v23\n"
"v_mfma_f32_16x16x32_bf16 v[228:231], a[88:91], a[96:99], v[228:231]\n"
"ds_read_b128 a[96:99], v3 offset:0\n"
"v_add_f32 v116, v116, v20\n"
"v_mfma_f32_16x16x32_bf16 v[232:235], a[88:91], a[104:107], v[232:235]\n"
"v_add_f32 v117, v117, v21\n"
"v_add_f32 v118, v118, v22\n"
"v_mfma_f32_16x16x32_bf16 v[232:235], a[92:95], a[108:111], v[232:235]\n"
"buffer_load_dwordx4 v1, s[48:51], s75 offen offset:3072 sc1 lds\n"
"v_mfma_f32_16x16x32_bf16 v[204:207], a[84:87], a[108:111], v[204:207]\n"
"ds_read_b128 a[108:111], v3 offset:3072\n"
"v_add_f32 v119, v119, v23\n"
"v_add_f32 v120, v120, v20\n"
"v_mfma_f32_16x16x32_bf16 v[204:207], a[80:83], a[104:107], v[204:207]\n"
"ds_read_b128 a[104:107], v3 offset:2048\n"
"v_add_f32 v121, v121, v21\n"
"v_mfma_f32_16x16x32_bf16 v[208:211], a[80:83], a[112:115], v[208:211]\n"
"v_add_f32 v122, v122, v22\n"
"v_add_f32 v123, v123, v23\n"
"v_mfma_f32_16x16x32_bf16 v[208:211], a[84:87], a[116:119], v[208:211]\n"
"global_load_dwordx4 a[68:71], v0, s[34:35] offset:1024 sc1\n"
"v_add_f32 v124, v124, v20\n"
"v_add_f32 v125, v125, v21\n"
"v_mfma_f32_16x16x32_bf16 v[236:239], a[92:95], a[116:119], v[236:239]\n"
"ds_read_b128 a[116:119], v3 offset:5120\n"
"v_add_f32 v126, v126, v22\n"
"v_mfma_f32_16x16x32_bf16 v[236:239], a[88:91], a[112:115], v[236:239]\n"
"ds_read_b128 a[112:115], v3 offset:4096\n"
"v_add_f32 v127, v127, v23\n"
"v_mfma_f32_16x16x32_bf16 v[240:243], a[88:91], a[120:123], v[240:243]\n"
"v_add_f32 v128, v128, v20\n"
"v_add_f32 v129, v129, v21\n"
"v_mfma_f32_16x16x32_bf16 v[240:243], a[92:95], a[124:127], v[240:243]\n"
"v_add_f32 v130, v130, v22\n"
"v_add_f32 v131, v131, v23\n"
"v_mfma_f32_16x16x32_bf16 v[212:215], a[84:87], a[124:127], v[212:215]\n"
"s_add_u32 m0, s64, 24576\n"
"ds_read_b128 a[124:127], v3 offset:7168\n"
"v_add_f32 v132, v132, v20\n"
"v_add_f32 v133, v133, v21\n"
"v_mfma_f32_16x16x32_bf16 v[212:215], a[80:83], a[120:123], v[212:215]\n"
"ds_read_b128 a[120:123], v3 offset:6144\n"
"v_add_f32 v134, v134, v22\n"
"v_mfma_f32_16x16x32_bf16 v[216:219], a[80:83], a[128:131], v[216:219]\n"
"v_add_f32 v135, v135, v23\n"
"v_add_f32 v136, v136, v20\n"
"v_mfma_f32_16x16x32_bf16 v[216:219], a[84:87], a[132:135], v[216:219]\n"
"global_load_dwordx4 a[72:75], v0, s[34:35] offset:2048 sc1\n"
"v_add_f32 v137, v137, v21\n"
"v_mfma_f32_16x16x32_bf16 v[244:247], a[92:95], a[132:135], v[244:247]\n"
"ds_read_b128 a[132:135], v3 offset:9216\n"
"v_add_f32 v138, v138, v22\n"
"v_add_f32 v139, v139, v23\n"
"v_mfma_f32_16x16x32_bf16 v[244:247], a[88:91], a[128:131], v[244:247]\n"
"ds_read_b128 a[128:131], v3 offset:8192\n"
"v_add_f32 v140, v140, v20\n"
"v_mfma_f32_16x16x32_bf16 v[248:251], a[88:91], a[136:139], v[248:251]\n"
"v_add_f32 v141, v141, v21\n"
"v_add_f32 v142, v142, v22\n"
"v_mfma_f32_16x16x32_bf16 v[248:251], a[92:95], a[140:143], v[248:251]\n"
"v_add_f32 v143, v143, v23\n"
"v_accvgpr_read_b32 v12, a12\n"
"v_mfma_f32_16x16x32_bf16 v[220:223], a[84:87], a[140:143], v[220:223]\n"
"ds_read_b128 a[140:143], v3 offset:11264\n"
"v_accvgpr_read_b32 v13, a13\n"
"v_mfma_f32_16x16x32_bf16 v[220:223], a[80:83], a[136:139], v[220:223]\n"
"ds_read_b128 a[136:139], v3 offset:10240\n"
"v_accvgpr_read_b32 v14, a14\n"
"v_accvgpr_read_b32 v15, a15\n"
"v_mfma_f32_16x16x32_bf16 v[224:227], a[80:83], a[144:147], v[224:227]\n"
"buffer_load_dwordx4 v0, s[48:51], s69 offen offset:0 sc1 lds\n"
"v_add_f32 v12, v12, v20\n"
"v_mfma_f32_16x16x32_bf16 v[224:227], a[84:87], a[148:151], v[224:227]\n"
"global_load_dwordx4 a[76:79], v0, s[34:35] offset:3072 sc1\n"
"v_add_f32 v13, v13, v21\n"
"v_add_f32 v14, v14, v22\n"
"v_mfma_f32_16x16x32_bf16 v[252:255], a[92:95], a[148:151], v[252:255]\n"
"ds_read_b128 a[148:151], v3 offset:13312\n"
"v_add_f32 v15, v15, v23\n"
"v_accvgpr_write_b32 a12, v12\n"
"v_mfma_f32_16x16x32_bf16 v[252:255], a[88:91], a[144:147], v[252:255]\n"
"ds_read_b128 a[144:147], v3 offset:12288\n"
"v_accvgpr_write_b32 a13, v13\n"
"v_mfma_f32_16x16x32_bf16 a[28:31], a[88:91], a[152:155], a[28:31]\n"
"v_accvgpr_write_b32 a14, v14\n"
"v_accvgpr_write_b32 a15, v15\n"
"v_mfma_f32_16x16x32_bf16 a[28:31], a[92:95], a[156:159], a[28:31]\n"
"v_mfma_f32_16x16x32_bf16 a[24:27], a[84:87], a[156:159], a[24:27]\n"
"ds_read_b128 a[156:159], v3 offset:15360\n"
"v_mfma_f32_16x16x32_bf16 a[24:27], a[80:83], a[152:155], a[24:27]\n"
"s_waitcnt vmcnt(13) lgkmcnt(11)\n"
"ds_read_b128 a[152:155], v3 offset:14336\n"
"v_mfma_f32_16x16x32_bf16 v[32:35], a[32:35], a[96:99], v[32:35]\n"
"s_add_u32 s34, s36, 1478656\n"
"v_mfma_f32_16x16x32_bf16 v[32:35], a[36:39], a[100:103], v[32:35]\n"
"global_load_dwordx4 a[80:83], v0, s[34:35] offset:0 sc1\n"
"v_mfma_f32_16x16x32_bf16 v[60:63], a[44:47], a[100:103], v[60:63]\n"
"buffer_load_dwordx4 v0, s[48:51], s69 offen offset:1024 sc1 lds\n"
"v_mfma_f32_16x16x32_bf16 v[60:63], a[40:43], a[96:99], v[60:63]\n"
"v_lshrrev_b32 v16, 4, v0\n"
"v_mfma_f32_16x16x32_bf16 v[64:67], a[40:43], a[104:107], v[64:67]\n"
"v_mfma_f32_16x16x32_bf16 v[64:67], a[44:47], a[108:111], v[64:67]\n"
"v_mfma_f32_16x16x32_bf16 v[36:39], a[36:39], a[108:111], v[36:39]\n"
"v_mfma_f32_16x16x32_bf16 v[36:39], a[32:35], a[104:107], v[36:39]\n"
"s_waitcnt lgkmcnt(8)\n"
"v_and_b32 v16, 0x30, v16\n"
"v_mfma_f32_16x16x32_bf16 v[40:43], a[32:35], a[112:115], v[40:43]\n"
"v_mfma_f32_16x16x32_bf16 v[40:43], a[36:39], a[116:119], v[40:43]\n"
"global_load_dwordx4 a[84:87], v0, s[34:35] offset:1024 sc1\n"
"v_mfma_f32_16x16x32_bf16 v[68:71], a[44:47], a[116:119], v[68:71]\n"
"buffer_load_dwordx2 v[18:19], v16, s[44:47], s61 offen offset:128\n"
"v_mfma_f32_16x16x32_bf16 v[68:71], a[40:43], a[112:115], v[68:71]\n"
"buffer_load_dwordx4 v0, s[48:51], s69 offen offset:2048 sc1 lds\n"
"v_mfma_f32_16x16x32_bf16 v[72:75], a[40:43], a[120:123], v[72:75]\n"
"v_mfma_f32_16x16x32_bf16 v[72:75], a[44:47], a[124:127], v[72:75]\n"
"v_lshrrev_b32 v20, 4, v0\n"
"v_mfma_f32_16x16x32_bf16 v[44:47], a[36:39], a[124:127], v[44:47]\n"
"v_mfma_f32_16x16x32_bf16 v[44:47], a[32:35], a[120:123], v[44:47]\n"
"s_waitcnt lgkmcnt(4)\n"
"v_mfma_f32_16x16x32_bf16 v[48:51], a[32:35], a[128:131], v[48:51]\n"
"v_and_b32 v20, 0x30, v20\n"
"v_mfma_f32_16x16x32_bf16 v[48:51], a[36:39], a[132:135], v[48:51]\n"
"global_load_dwordx4 a[88:91], v0, s[34:35] offset:2048 sc1\n"
"v_mfma_f32_16x16x32_bf16 v[76:79], a[44:47], a[132:135], v[76:79]\n"
"v_mfma_f32_16x16x32_bf16 v[76:79], a[40:43], a[128:131], v[76:79]\n"
"v_mfma_f32_16x16x32_bf16 v[80:83], a[40:43], a[136:139], v[80:83]\n"
"buffer_load_dwordx4 v0, s[48:51], s69 offen offset:3072 sc1 lds\n"
"v_mfma_f32_16x16x32_bf16 v[80:83], a[44:47], a[140:143], v[80:83]\n"
"buffer_load_dwordx2 v[22:23], v20, s[44:47], s61 offen offset:136\n"
"v_mfma_f32_16x16x32_bf16 v[52:55], a[36:39], a[140:143], v[52:55]\n"
"v_mfma_f32_16x16x32_bf16 v[52:55], a[32:35], a[136:139], v[52:55]\n"
"s_waitcnt lgkmcnt(0)\n"
"v_mfma_f32_16x16x32_bf16 v[56:59], a[32:35], a[144:147], v[56:59]\n"
"v_lshrrev_b32 v24, 4, v0\n"
"v_mfma_f32_16x16x32_bf16 v[56:59], a[36:39], a[148:151], v[56:59]\n"
"global_load_dwordx4 a[92:95], v0, s[34:35] offset:3072 sc1\n"
"v_mfma_f32_16x16x32_bf16 v[84:87], a[44:47], a[148:151], v[84:87]\n"
"v_mfma_f32_16x16x32_bf16 v[84:87], a[40:43], a[144:147], v[84:87]\n"
"v_and_b32 v24, 0x30, v24\n"
"v_mfma_f32_16x16x32_bf16 a[4:7], a[40:43], a[152:155], a[4:7]\n"
"v_mfma_f32_16x16x32_bf16 a[4:7], a[44:47], a[156:159], a[4:7]\n"
"v_mfma_f32_16x16x32_bf16 a[0:3], a[36:39], a[156:159], a[0:3]\n"
"s_add_u32 m0, s64, 28672\n"
"v_mfma_f32_16x16x32_bf16 a[0:3], a[32:35], a[152:155], a[0:3]\n"
"s_waitcnt vmcnt(16)\n"
"buffer_load_dwordx2 v[26:27], v24, s[44:47], s61 offen offset:192\n"
"v_mfma_f32_16x16x32_bf16 v[88:91], a[48:51], a[96:99], v[88:91]\n"
"s_add_u32 s34, s36, 1490944\n"
"v_lshrrev_b32 v28, 4, v0\n"
"v_mfma_f32_16x16x32_bf16 v[88:91], a[52:55], a[100:103], v[88:91]\n"
"global_load_dwordx4 a[32:35], v0, s[34:35] offset:0 sc1\n"
"v_mfma_f32_16x16x32_bf16 v[116:119], a[60:63], a[100:103], v[116:119]\n"
"ds_read_b128 a[100:103], v4 offset:1024\n"
"v_mfma_f32_16x16x32_bf16 v[116:119], a[56:59], a[96:99], v[116:119]\n"
"ds_read_b128 a[96:99], v4 offset:0\n"
"v_mfma_f32_16x16x32_bf16 v[120:123], a[56:59], a[104:107], v[120:123]\n"
"v_and_b32 v28, 0x30, v28\n"
"v_mfma_f32_16x16x32_bf16 v[120:123], a[60:63], a[108:111], v[120:123]\n"
"buffer_load_dwordx2 v[30:31], v28, s[44:47], s61 offen offset:200\n"
"v_mfma_f32_16x16x32_bf16 v[92:95], a[52:55], a[108:111], v[92:95]\n"
"ds_read_b128 a[108:111], v4 offset:3072\n"
"v_mfma_f32_16x16x32_bf16 v[92:95], a[48:51], a[104:107], v[92:95]\n"
"ds_read_b128 a[104:107], v4 offset:2048\n"
"v_mfma_f32_16x16x32_bf16 v[96:99], a[48:51], a[112:115], v[96:99]\n"
"buffer_load_dwordx4 v1, s[48:51], s69 offen offset:0 sc1 lds\n"
"v_mfma_f32_16x16x32_bf16 v[96:99], a[52:55], a[116:119], v[96:99]\n"
"global_load_dwordx4 a[36:39], v0, s[34:35] offset:1024 sc1\n"
"v_mfma_f32_16x16x32_bf16 v[124:127], a[60:63], a[116:119], v[124:127]\n"
"ds_read_b128 a[116:119], v4 offset:5120\n"
"v_mfma_f32_16x16x32_bf16 v[124:127], a[56:59], a[112:115], v[124:127]\n"
"ds_read_b128 a[112:115], v4 offset:4096\n"
"v_mfma_f32_16x16x32_bf16 v[128:131], a[56:59], a[120:123], v[128:131]\n"
"v_mfma_f32_16x16x32_bf16 v[128:131], a[60:63], a[124:127], v[128:131]\n"
"v_mfma_f32_16x16x32_bf16 v[100:103], a[52:55], a[124:127], v[100:103]\n"
"ds_read_b128 a[124:127], v4 offset:7168\n"
"v_mfma_f32_16x16x32_bf16 v[100:103], a[48:51], a[120:123], v[100:103]\n"
"ds_read_b128 a[120:123], v4 offset:6144\n"
"v_mfma_f32_16x16x32_bf16 v[104:107], a[48:51], a[128:131], v[104:107]\n"
"buffer_load_dwordx4 v1, s[48:51], s69 offen offset:1024 sc1 lds\n"
"v_mfma_f32_16x16x32_bf16 v[104:107], a[52:55], a[132:135], v[104:107]\n"
"global_load_dwordx4 a[40:43], v0, s[34:35] offset:2048 sc1\n"
"v_mfma_f32_16x16x32_bf16 v[132:135], a[60:63], a[132:135], v[132:135]\n"
"ds_read_b128 a[132:135], v4 offset:9216\n"
"v_mfma_f32_16x16x32_bf16 v[132:135], a[56:59], a[128:131], v[132:135]\n"
"ds_read_b128 a[128:131], v4 offset:8192\n"
"v_mfma_f32_16x16x32_bf16 v[136:139], a[56:59], a[136:139], v[136:139]\n"
"v_mfma_f32_16x16x32_bf16 v[136:139], a[60:63], a[140:143], v[136:139]\n"
"v_mfma_f32_16x16x32_bf16 v[108:111], a[52:55], a[140:143], v[108:111]\n"
"ds_read_b128 a[140:143], v4 offset:11264\n"
"v_mfma_f32_16x16x32_bf16 v[108:111], a[48:51], a[136:139], v[108:111]\n"
"ds_read_b128 a[136:139], v4 offset:10240\n"
"v_mfma_f32_16x16x32_bf16 v[112:115], a[48:51], a[144:147], v[112:115]\n"
"v_mfma_f32_16x16x32_bf16 v[112:115], a[52:55], a[148:151], v[112:115]\n"
"global_load_dwordx4 a[44:47], v0, s[34:35] offset:3072 sc1\n"
"v_mfma_f32_16x16x32_bf16 v[140:143], a[60:63], a[148:151], v[140:143]\n"
"ds_read_b128 a[148:151], v4 offset:13312\n"
"v_mfma_f32_16x16x32_bf16 v[140:143], a[56:59], a[144:147], v[140:143]\n"
"ds_read_b128 a[144:147], v4 offset:12288\n"
"v_mfma_f32_16x16x32_bf16 a[12:15], a[56:59], a[152:155], a[12:15]\n"
"buffer_load_dwordx4 v1, s[48:51], s69 offen offset:2048 sc1 lds\n"
"v_mfma_f32_16x16x32_bf16 a[12:15], a[60:63], a[156:159], a[12:15]\n"
"v_mfma_f32_16x16x32_bf16 a[8:11], a[52:55], a[156:159], a[8:11]\n"
"ds_read_b128 a[156:159], v4 offset:15360\n"
"v_mfma_f32_16x16x32_bf16 a[8:11], a[48:51], a[152:155], a[8:11]\n"
"s_waitcnt vmcnt(18) lgkmcnt(11)\n"
"ds_read_b128 a[152:155], v4 offset:14336\n"
"v_mfma_f32_16x16x32_bf16 v[32:35], a[64:67], a[96:99], v[32:35]\n"
"s_add_u32 s34, s36, 1495040\n"
"v_mfma_f32_16x16x32_bf16 v[32:35], a[68:71], a[100:103], v[32:35]\n"
"global_load_dwordx4 a[48:51], v0, s[34:35] offset:0 sc1\n"
"v_mfma_f32_16x16x32_bf16 v[60:63], a[76:79], a[100:103], v[60:63]\n"
"v_mfma_f32_16x16x32_bf16 v[60:63], a[72:75], a[96:99], v[60:63]\n"
"v_mfma_f32_16x16x32_bf16 v[64:67], a[72:75], a[104:107], v[64:67]\n"
"buffer_load_dwordx4 v1, s[48:51], s69 offen offset:3072 sc1 lds\n"
"v_mfma_f32_16x16x32_bf16 v[64:67], a[76:79], a[108:111], v[64:67]\n"
"v_mfma_f32_16x16x32_bf16 v[36:39], a[68:71], a[108:111], v[36:39]\n"
"v_mfma_f32_16x16x32_bf16 v[36:39], a[64:67], a[104:107], v[36:39]\n"
"s_waitcnt lgkmcnt(8)\n"
"v_mfma_f32_16x16x32_bf16 v[40:43], a[64:67], a[112:115], v[40:43]\n"
"v_mfma_f32_16x16x32_bf16 v[40:43], a[68:71], a[116:119], v[40:43]\n"
"global_load_dwordx4 a[52:55], v0, s[34:35] offset:1024 sc1\n"
"v_mfma_f32_16x16x32_bf16 v[68:71], a[76:79], a[116:119], v[68:71]\n"
"v_mfma_f32_16x16x32_bf16 v[68:71], a[72:75], a[112:115], v[68:71]\n"
"v_mfma_f32_16x16x32_bf16 v[72:75], a[72:75], a[120:123], v[72:75]\n"
"v_mfma_f32_16x16x32_bf16 v[72:75], a[76:79], a[124:127], v[72:75]\n"
"v_mfma_f32_16x16x32_bf16 v[44:47], a[68:71], a[124:127], v[44:47]\n"
"v_mfma_f32_16x16x32_bf16 v[44:47], a[64:67], a[120:123], v[44:47]\n"
"s_waitcnt lgkmcnt(4)\n"
"v_mfma_f32_16x16x32_bf16 v[48:51], a[64:67], a[128:131], v[48:51]\n"
"v_mfma_f32_16x16x32_bf16 v[48:51], a[68:71], a[132:135], v[48:51]\n"
"global_load_dwordx4 a[56:59], v0, s[34:35] offset:2048 sc1\n"
"v_mfma_f32_16x16x32_bf16 v[76:79], a[76:79], a[132:135], v[76:79]\n"
"v_mfma_f32_16x16x32_bf16 v[76:79], a[72:75], a[128:131], v[76:79]\n"
"v_mfma_f32_16x16x32_bf16 v[80:83], a[72:75], a[136:139], v[80:83]\n"
"v_mfma_f32_16x16x32_bf16 v[80:83], a[76:79], a[140:143], v[80:83]\n"
"v_mfma_f32_16x16x32_bf16 v[52:55], a[68:71], a[140:143], v[52:55]\n"
"v_mfma_f32_16x16x32_bf16 v[52:55], a[64:67], a[136:139], v[52:55]\n"
"s_waitcnt lgkmcnt(0)\n"
"v_mfma_f32_16x16x32_bf16 v[56:59], a[64:67], a[144:147], v[56:59]\n"
"v_mfma_f32_16x16x32_bf16 v[56:59], a[68:71], a[148:151], v[56:59]\n"
"global_load_dwordx4 a[60:63], v0, s[34:35] offset:3072 sc1\n"
"v_mfma_f32_16x16x32_bf16 v[84:87], a[76:79], a[148:151], v[84:87]\n"
"v_mfma_f32_16x16x32_bf16 v[84:87], a[72:75], a[144:147], v[84:87]\n"
"v_mfma_f32_16x16x32_bf16 a[4:7], a[72:75], a[152:155], a[4:7]\n"
"v_mfma_f32_16x16x32_bf16 a[4:7], a[76:79], a[156:159], a[4:7]\n"
"v_mfma_f32_16x16x32_bf16 a[0:3], a[68:71], a[156:159], a[0:3]\n"
"v_mfma_f32_16x16x32_bf16 a[0:3], a[64:67], a[152:155], a[0:3]\n"
"s_waitcnt vmcnt(14)\n"
"v_mfma_f32_16x16x32_bf16 v[88:91], a[80:83], a[96:99], v[88:91]\n"
"s_add_u32 s34, s36, 1507328\n"
"v_mfma_f32_16x16x32_bf16 v[88:91], a[84:87], a[100:103], v[88:91]\n"
"global_load_dwordx4 a[64:67], v0, s[34:35] offset:0 sc1\n"
"v_mfma_f32_16x16x32_bf16 v[116:119], a[92:95], a[100:103], v[116:119]\n"
"ds_read_b128 a[100:103], v5 offset:1024\n"
"v_mfma_f32_16x16x32_bf16 v[116:119], a[88:91], a[96:99], v[116:119]\n"
"ds_read_b128 a[96:99], v5 offset:0\n"
"v_mfma_f32_16x16x32_bf16 v[120:123], a[88:91], a[104:107], v[120:123]\n"
"v_mfma_f32_16x16x32_bf16 v[120:123], a[92:95], a[108:111], v[120:123]\n"
"v_mfma_f32_16x16x32_bf16 v[92:95], a[84:87], a[108:111], v[92:95]\n"
"ds_read_b128 a[108:111], v5 offset:3072\n"
"v_mfma_f32_16x16x32_bf16 v[92:95], a[80:83], a[104:107], v[92:95]\n"
"ds_read_b128 a[104:107], v5 offset:2048\n"
"v_mfma_f32_16x16x32_bf16 v[96:99], a[80:83], a[112:115], v[96:99]\n"
"v_mfma_f32_16x16x32_bf16 v[96:99], a[84:87], a[116:119], v[96:99]\n"
"global_load_dwordx4 a[68:71], v0, s[34:35] offset:1024 sc1\n"
"v_mfma_f32_16x16x32_bf16 v[124:127], a[92:95], a[116:119], v[124:127]\n"
"ds_read_b128 a[116:119], v5 offset:5120\n"
"v_mfma_f32_16x16x32_bf16 v[124:127], a[88:91], a[112:115], v[124:127]\n"
"ds_read_b128 a[112:115], v5 offset:4096\n"
"v_mfma_f32_16x16x32_bf16 v[128:131], a[88:91], a[120:123], v[128:131]\n"
"v_mfma_f32_16x16x32_bf16 v[128:131], a[92:95], a[124:127], v[128:131]\n"
"v_mfma_f32_16x16x32_bf16 v[100:103], a[84:87], a[124:127], v[100:103]\n"
"ds_read_b128 a[124:127], v5 offset:7168\n"
"v_mfma_f32_16x16x32_bf16 v[100:103], a[80:83], a[120:123], v[100:103]\n"
"ds_read_b128 a[120:123], v5 offset:6144\n"
"v_mfma_f32_16x16x32_bf16 v[104:107], a[80:83], a[128:131], v[104:107]\n"
"v_mfma_f32_16x16x32_bf16 v[104:107], a[84:87], a[132:135], v[104:107]\n"
"global_load_dwordx4 a[72:75], v0, s[34:35] offset:2048 sc1\n"
"v_mfma_f32_16x16x32_bf16 v[132:135], a[92:95], a[132:135], v[132:135]\n"
"ds_read_b128 a[132:135], v5 offset:9216\n"
"v_mfma_f32_16x16x32_bf16 v[132:135], a[88:91], a[128:131], v[132:135]\n"
"ds_read_b128 a[128:131], v5 offset:8192\n"
"v_mfma_f32_16x16x32_bf16 v[136:139], a[88:91], a[136:139], v[136:139]\n"
"v_mfma_f32_16x16x32_bf16 v[136:139], a[92:95], a[140:143], v[136:139]\n"
"v_mfma_f32_16x16x32_bf16 v[108:111], a[84:87], a[140:143], v[108:111]\n"
"ds_read_b128 a[140:143], v5 offset:11264\n"
"v_mfma_f32_16x16x32_bf16 v[108:111], a[80:83], a[136:139], v[108:111]\n"
"ds_read_b128 a[136:139], v5 offset:10240\n"
"v_mfma_f32_16x16x32_bf16 v[112:115], a[80:83], a[144:147], v[112:115]\n"
"v_mfma_f32_16x16x32_bf16 v[112:115], a[84:87], a[148:151], v[112:115]\n"
"global_load_dwordx4 a[76:79], v0, s[34:35] offset:3072 sc1\n"
"v_mfma_f32_16x16x32_bf16 v[140:143], a[92:95], a[148:151], v[140:143]\n"
"ds_read_b128 a[148:151], v5 offset:13312\n"
"v_mfma_f32_16x16x32_bf16 v[140:143], a[88:91], a[144:147], v[140:143]\n"
"ds_read_b128 a[144:147], v5 offset:12288\n"
"v_mfma_f32_16x16x32_bf16 a[12:15], a[88:91], a[152:155], a[12:15]\n"
"v_mfma_f32_16x16x32_bf16 a[12:15], a[92:95], a[156:159], a[12:15]\n"
"v_mfma_f32_16x16x32_bf16 a[8:11], a[84:87], a[156:159], a[8:11]\n"
"ds_read_b128 a[156:159], v5 offset:15360\n"
"v_mfma_f32_16x16x32_bf16 a[8:11], a[80:83], a[152:155], a[8:11]\n"
"s_waitcnt vmcnt(10) lgkmcnt(11)\n"
"ds_read_b128 a[152:155], v5 offset:14336\n"
"v_mfma_f32_16x16x32_bf16 v[32:35], a[32:35], a[96:99], v[32:35]\n"
"s_add_u32 s34, s36, 1511424\n"
"v_mfma_f32_16x16x32_bf16 v[32:35], a[36:39], a[100:103], v[32:35]\n"
"global_load_dwordx4 a[80:83], v0, s[34:35] offset:0 sc1\n"
"v_mfma_f32_16x16x32_bf16 v[60:63], a[44:47], a[100:103], v[60:63]\n"
"v_mfma_f32_16x16x32_bf16 v[60:63], a[40:43], a[96:99], v[60:63]\n"
"v_mfma_f32_16x16x32_bf16 v[64:67], a[40:43], a[104:107], v[64:67]\n"
"v_mfma_f32_16x16x32_bf16 v[64:67], a[44:47], a[108:111], v[64:67]\n"
"v_mfma_f32_16x16x32_bf16 v[36:39], a[36:39], a[108:111], v[36:39]\n"
"v_mfma_f32_16x16x32_bf16 v[36:39], a[32:35], a[104:107], v[36:39]\n"
"s_waitcnt lgkmcnt(8)\n"
"v_mfma_f32_16x16x32_bf16 v[40:43], a[32:35], a[112:115], v[40:43]\n"
"v_mfma_f32_16x16x32_bf16 v[40:43], a[36:39], a[116:119], v[40:43]\n"
"global_load_dwordx4 a[84:87], v0, s[34:35] offset:1024 sc1\n"
"v_mfma_f32_16x16x32_bf16 v[68:71], a[44:47], a[116:119], v[68:71]\n"
"v_mfma_f32_16x16x32_bf16 v[68:71], a[40:43], a[112:115], v[68:71]\n"
"v_mfma_f32_16x16x32_bf16 v[72:75], a[40:43], a[120:123], v[72:75]\n"
"v_mfma_f32_16x16x32_bf16 v[72:75], a[44:47], a[124:127], v[72:75]\n"
"v_mfma_f32_16x16x32_bf16 v[44:47], a[36:39], a[124:127], v[44:47]\n"
"v_mfma_f32_16x16x32_bf16 v[44:47], a[32:35], a[120:123], v[44:47]\n"
"s_waitcnt lgkmcnt(4)\n"
"v_mfma_f32_16x16x32_bf16 v[48:51], a[32:35], a[128:131], v[48:51]\n"
"v_mfma_f32_16x16x32_bf16 v[48:51], a[36:39], a[132:135], v[48:51]\n"
"global_load_dwordx4 a[88:91], v0, s[34:35] offset:2048 sc1\n"
"v_mfma_f32_16x16x32_bf16 v[76:79], a[44:47], a[132:135], v[76:79]\n"
"v_mfma_f32_16x16x32_bf16 v[76:79], a[40:43], a[128:131], v[76:79]\n"
"v_mfma_f32_16x16x32_bf16 v[80:83], a[40:43], a[136:139], v[80:83]\n"
"v_mfma_f32_16x16x32_bf16 v[80:83], a[44:47], a[140:143], v[80:83]\n"
"v_mfma_f32_16x16x32_bf16 v[52:55], a[36:39], a[140:143], v[52:55]\n"
"v_mfma_f32_16x16x32_bf16 v[52:55], a[32:35], a[136:139], v[52:55]\n"
"s_waitcnt lgkmcnt(0)\n"
"v_mfma_f32_16x16x32_bf16 v[56:59], a[32:35], a[144:147], v[56:59]\n"
"v_mfma_f32_16x16x32_bf16 v[56:59], a[36:39], a[148:151], v[56:59]\n"
"global_load_dwordx4 a[92:95], v0, s[34:35] offset:3072 sc1\n"
"v_mfma_f32_16x16x32_bf16 v[84:87], a[44:47], a[148:151], v[84:87]\n"
"v_mfma_f32_16x16x32_bf16 v[84:87], a[40:43], a[144:147], v[84:87]\n"
"v_mfma_f32_16x16x32_bf16 a[4:7], a[40:43], a[152:155], a[4:7]\n"
"v_mfma_f32_16x16x32_bf16 a[4:7], a[44:47], a[156:159], a[4:7]\n"
"v_mfma_f32_16x16x32_bf16 a[0:3], a[36:39], a[156:159], a[0:3]\n"
"v_mfma_f32_16x16x32_bf16 a[0:3], a[32:35], a[152:155], a[0:3]\n"
"s_waitcnt vmcnt(8)\n"
"v_mfma_f32_16x16x32_bf16 v[88:91], a[48:51], a[96:99], v[88:91]\n"
"s_add_u32 s34, s36, 1515520\n"
"v_mfma_f32_16x16x32_bf16 v[88:91], a[52:55], a[100:103], v[88:91]\n"
"global_load_dwordx4 a[32:35], v0, s[34:35] offset:0 sc1\n"
"v_mfma_f32_16x16x32_bf16 v[116:119], a[60:63], a[100:103], v[116:119]\n"
"ds_read_b128 a[100:103], v2 offset:17408\n"
"v_mfma_f32_16x16x32_bf16 v[116:119], a[56:59], a[96:99], v[116:119]\n"
"ds_read_b128 a[96:99], v2 offset:16384\n"
"v_mfma_f32_16x16x32_bf16 v[120:123], a[56:59], a[104:107], v[120:123]\n"
"v_mfma_f32_16x16x32_bf16 v[120:123], a[60:63], a[108:111], v[120:123]\n"
"v_mfma_f32_16x16x32_bf16 v[92:95], a[52:55], a[108:111], v[92:95]\n"
"ds_read_b128 a[108:111], v2 offset:19456\n"
"v_mfma_f32_16x16x32_bf16 v[92:95], a[48:51], a[104:107], v[92:95]\n"
"ds_read_b128 a[104:107], v2 offset:18432\n"
"v_mfma_f32_16x16x32_bf16 v[96:99], a[48:51], a[112:115], v[96:99]\n"
"v_mfma_f32_16x16x32_bf16 v[96:99], a[52:55], a[116:119], v[96:99]\n"
"global_load_dwordx4 a[36:39], v0, s[34:35] offset:1024 sc1\n"
"v_mfma_f32_16x16x32_bf16 v[124:127], a[60:63], a[116:119], v[124:127]\n"
"ds_read_b128 a[116:119], v2 offset:21504\n"
"v_mfma_f32_16x16x32_bf16 v[124:127], a[56:59], a[112:115], v[124:127]\n"
"ds_read_b128 a[112:115], v2 offset:20480\n"
"v_mfma_f32_16x16x32_bf16 v[128:131], a[56:59], a[120:123], v[128:131]\n"
"v_mfma_f32_16x16x32_bf16 v[128:131], a[60:63], a[124:127], v[128:131]\n"
"v_mfma_f32_16x16x32_bf16 v[100:103], a[52:55], a[124:127], v[100:103]\n"
"ds_read_b128 a[124:127], v2 offset:23552\n"
"v_mfma_f32_16x16x32_bf16 v[100:103], a[48:51], a[120:123], v[100:103]\n"
"ds_read_b128 a[120:123], v2 offset:22528\n"
"v_mfma_f32_16x16x32_bf16 v[104:107], a[48:51], a[128:131], v[104:107]\n"
"v_mfma_f32_16x16x32_bf16 v[104:107], a[52:55], a[132:135], v[104:107]\n"
"global_load_dwordx4 a[40:43], v0, s[34:35] offset:2048 sc1\n"
"v_mfma_f32_16x16x32_bf16 v[132:135], a[60:63], a[132:135], v[132:135]\n"
"ds_read_b128 a[132:135], v2 offset:25600\n"
"v_mfma_f32_16x16x32_bf16 v[132:135], a[56:59], a[128:131], v[132:135]\n"
"ds_read_b128 a[128:131], v2 offset:24576\n"
"v_mfma_f32_16x16x32_bf16 v[136:139], a[56:59], a[136:139], v[136:139]\n"
"v_mfma_f32_16x16x32_bf16 v[136:139], a[60:63], a[140:143], v[136:139]\n"
"v_mfma_f32_16x16x32_bf16 v[108:111], a[52:55], a[140:143], v[108:111]\n"
"ds_read_b128 a[140:143], v2 offset:27648\n"
"v_mfma_f32_16x16x32_bf16 v[108:111], a[48:51], a[136:139], v[108:111]\n"
"ds_read_b128 a[136:139], v2 offset:26624\n"
"v_mfma_f32_16x16x32_bf16 v[112:115], a[48:51], a[144:147], v[112:115]\n"
"v_mfma_f32_16x16x32_bf16 v[112:115], a[52:55], a[148:151], v[112:115]\n"
"global_load_dwordx4 a[44:47], v0, s[34:35] offset:3072 sc1\n"
"v_mfma_f32_16x16x32_bf16 v[140:143], a[60:63], a[148:151], v[140:143]\n"
"ds_read_b128 a[148:151], v2 offset:29696\n"
"v_mfma_f32_16x16x32_bf16 v[140:143], a[56:59], a[144:147], v[140:143]\n"
"ds_read_b128 a[144:147], v2 offset:28672\n"
"v_mfma_f32_16x16x32_bf16 a[12:15], a[56:59], a[152:155], a[12:15]\n"
"v_mfma_f32_16x16x32_bf16 a[12:15], a[60:63], a[156:159], a[12:15]\n"
"v_mfma_f32_16x16x32_bf16 a[8:11], a[52:55], a[156:159], a[8:11]\n"
"ds_read_b128 a[156:159], v2 offset:31744\n"
"v_mfma_f32_16x16x32_bf16 a[8:11], a[48:51], a[152:155], a[8:11]\n"
"s_waitcnt vmcnt(8) lgkmcnt(11)\n"
"ds_read_b128 a[152:155], v2 offset:30720\n"
"v_mfma_f32_16x16x32_bf16 v[32:35], a[64:67], a[96:99], v[32:35]\n"
"s_add_u32 s34, s36, 1519616\n"
"v_mfma_f32_16x16x32_bf16 v[32:35], a[68:71], a[100:103], v[32:35]\n"
"global_load_dwordx4 a[48:51], v0, s[34:35] offset:0 sc1\n"
"v_mfma_f32_16x16x32_bf16 v[60:63], a[76:79], a[100:103], v[60:63]\n"
"s_barrier\n"
"v_mfma_f32_16x16x32_bf16 v[60:63], a[72:75], a[96:99], v[60:63]\n"
"v_mfma_f32_16x16x32_bf16 v[64:67], a[72:75], a[104:107], v[64:67]\n"
"v_mfma_f32_16x16x32_bf16 v[64:67], a[76:79], a[108:111], v[64:67]\n"
"v_mfma_f32_16x16x32_bf16 v[36:39], a[68:71], a[108:111], v[36:39]\n"
"v_mfma_f32_16x16x32_bf16 v[36:39], a[64:67], a[104:107], v[36:39]\n"
"s_waitcnt lgkmcnt(8)\n"
"v_mfma_f32_16x16x32_bf16 v[40:43], a[64:67], a[112:115], v[40:43]\n"
"v_mfma_f32_16x16x32_bf16 v[40:43], a[68:71], a[116:119], v[40:43]\n"
"global_load_dwordx4 a[52:55], v0, s[34:35] offset:1024 sc1\n"
"v_mfma_f32_16x16x32_bf16 v[68:71], a[76:79], a[116:119], v[68:71]\n"
"v_mfma_f32_16x16x32_bf16 v[68:71], a[72:75], a[112:115], v[68:71]\n"
"v_mfma_f32_16x16x32_bf16 v[72:75], a[72:75], a[120:123], v[72:75]\n"
"v_mfma_f32_16x16x32_bf16 v[72:75], a[76:79], a[124:127], v[72:75]\n"
"v_mfma_f32_16x16x32_bf16 v[44:47], a[68:71], a[124:127], v[44:47]\n"
"v_mfma_f32_16x16x32_bf16 v[44:47], a[64:67], a[120:123], v[44:47]\n"
"s_waitcnt lgkmcnt(4)\n"
"v_mfma_f32_16x16x32_bf16 v[48:51], a[64:67], a[128:131], v[48:51]\n"
"v_mfma_f32_16x16x32_bf16 v[48:51], a[68:71], a[132:135], v[48:51]\n"
"global_load_dwordx4 a[56:59], v0, s[34:35] offset:2048 sc1\n"
"v_mfma_f32_16x16x32_bf16 v[76:79], a[76:79], a[132:135], v[76:79]\n"
"v_mfma_f32_16x16x32_bf16 v[76:79], a[72:75], a[128:131], v[76:79]\n"
"v_mfma_f32_16x16x32_bf16 v[80:83], a[72:75], a[136:139], v[80:83]\n"
"v_lshlrev_b32 v16, 16, v18\n"
"v_mfma_f32_16x16x32_bf16 v[80:83], a[76:79], a[140:143], v[80:83]\n"
"v_and_b32 v17, s63, v18\n"
"v_mfma_f32_16x16x32_bf16 v[52:55], a[68:71], a[140:143], v[52:55]\n"
"v_lshlrev_b32 v18, 16, v19\n"
"v_mfma_f32_16x16x32_bf16 v[52:55], a[64:67], a[136:139], v[52:55]\n"
"s_waitcnt lgkmcnt(0)\n"
"v_and_b32 v19, s63, v19\n"
"v_add_f32 v144, v144, v16\n"
"v_mfma_f32_16x16x32_bf16 v[56:59], a[64:67], a[144:147], v[56:59]\n"
"v_add_f32 v145, v145, v17\n"
"v_mfma_f32_16x16x32_bf16 v[56:59], a[68:71], a[148:151], v[56:59]\n"
"global_load_dwordx4 a[60:63], v0, s[34:35] offset:3072 sc1\n"
"v_add_f32 v146, v146, v18\n"
"v_mfma_f32_16x16x32_bf16 v[84:87], a[76:79], a[148:151], v[84:87]\n"
"v_add_f32 v147, v147, v19\n"
"v_mfma_f32_16x16x32_bf16 v[84:87], a[72:75], a[144:147], v[84:87]\n"
"v_add_f32 v148, v148, v16\n"
"v_mfma_f32_16x16x32_bf16 a[4:7], a[72:75], a[152:155], a[4:7]\n"
"v_add_f32 v149, v149, v17\n"
"v_mfma_f32_16x16x32_bf16 a[4:7], a[76:79], a[156:159], a[4:7]\n"
"v_add_f32 v150, v150, v18\n"
"v_mfma_f32_16x16x32_bf16 a[0:3], a[68:71], a[156:159], a[0:3]\n"
"v_add_f32 v151, v151, v19\n"
"v_mfma_f32_16x16x32_bf16 a[0:3], a[64:67], a[152:155], a[0:3]\n"
"s_waitcnt vmcnt(8)\n"
"v_add_f32 v152, v152, v16\n"
"v_add_f32 v153, v153, v17\n"
"v_mfma_f32_16x16x32_bf16 v[88:91], a[80:83], a[96:99], v[88:91]\n"
"s_add_u32 s34, s36, 1523712\n"
"v_add_f32 v154, v154, v18\n"
"v_mfma_f32_16x16x32_bf16 v[88:91], a[84:87], a[100:103], v[88:91]\n"
"global_load_dwordx4 a[64:67], v0, s[34:35] offset:0 sc1\n"
"v_add_f32 v155, v155, v19\n"
"v_mfma_f32_16x16x32_bf16 v[116:119], a[92:95], a[100:103], v[116:119]\n"
"ds_read_b128 a[100:103], v3 offset:17408\n"
"v_add_f32 v156, v156, v16\n"
"v_mfma_f32_16x16x32_bf16 v[116:119], a[88:91], a[96:99], v[116:119]\n"
"ds_read_b128 a[96:99], v3 offset:16384\n"
"v_add_f32 v157, v157, v17\n"
"v_mfma_f32_16x16x32_bf16 v[120:123], a[88:91], a[104:107], v[120:123]\n"
"v_add_f32 v158, v158, v18\n"
"v_mfma_f32_16x16x32_bf16 v[120:123], a[92:95], a[108:111], v[120:123]\n"
"v_add_f32 v159, v159, v19\n"
"v_add_f32 v160, v160, v16\n"
"v_mfma_f32_16x16x32_bf16 v[92:95], a[84:87], a[108:111], v[92:95]\n"
"ds_read_b128 a[108:111], v3 offset:19456\n"
"v_add_f32 v161, v161, v17\n"
"v_mfma_f32_16x16x32_bf16 v[92:95], a[80:83], a[104:107], v[92:95]\n"
"ds_read_b128 a[104:107], v3 offset:18432\n"
"v_add_f32 v162, v162, v18\n"
"v_mfma_f32_16x16x32_bf16 v[96:99], a[80:83], a[112:115], v[96:99]\n"
"v_add_f32 v163, v163, v19\n"
"v_mfma_f32_16x16x32_bf16 v[96:99], a[84:87], a[116:119], v[96:99]\n"
"global_load_dwordx4 a[68:71], v0, s[34:35] offset:1024 sc1\n"
"v_add_f32 v164, v164, v16\n"
"v_mfma_f32_16x16x32_bf16 v[124:127], a[92:95], a[116:119], v[124:127]\n"
"ds_read_b128 a[116:119], v3 offset:21504\n"
"v_add_f32 v165, v165, v17\n"
"v_mfma_f32_16x16x32_bf16 v[124:127], a[88:91], a[112:115], v[124:127]\n"
"ds_read_b128 a[112:115], v3 offset:20480\n"
"v_add_f32 v166, v166, v18\n"
"v_mfma_f32_16x16x32_bf16 v[128:131], a[88:91], a[120:123], v[128:131]\n"
"v_add_f32 v167, v167, v19\n"
"v_mfma_f32_16x16x32_bf16 v[128:131], a[92:95], a[124:127], v[128:131]\n"
"v_add_f32 v168, v168, v16\n"
"v_add_f32 v169, v169, v17\n"
"v_mfma_f32_16x16x32_bf16 v[100:103], a[84:87], a[124:127], v[100:103]\n"
"ds_read_b128 a[124:127], v3 offset:23552\n"
"v_add_f32 v170, v170, v18\n"
"v_mfma_f32_16x16x32_bf16 v[100:103], a[80:83], a[120:123], v[100:103]\n"
"ds_read_b128 a[120:123], v3 offset:22528\n"
"v_add_f32 v171, v171, v19\n"
"v_mfma_f32_16x16x32_bf16 v[104:107], a[80:83], a[128:131], v[104:107]\n"
"v_accvgpr_read_b32 v12, a16\n"
"v_mfma_f32_16x16x32_bf16 v[104:107], a[84:87], a[132:135], v[104:107]\n"
"global_load_dwordx4 a[72:75], v0, s[34:35] offset:2048 sc1\n"
"v_accvgpr_read_b32 v13, a17\n"
"v_mfma_f32_16x16x32_bf16 v[132:135], a[92:95], a[132:135], v[132:135]\n"
"ds_read_b128 a[132:135], v3 offset:25600\n"
"v_accvgpr_read_b32 v14, a18\n"
"v_mfma_f32_16x16x32_bf16 v[132:135], a[88:91], a[128:131], v[132:135]\n"
"ds_read_b128 a[128:131], v3 offset:24576\n"
"v_accvgpr_read_b32 v15, a19\n"
"v_mfma_f32_16x16x32_bf16 v[136:139], a[88:91], a[136:139], v[136:139]\n"
"v_add_f32 v12, v12, v16\n"
"v_mfma_f32_16x16x32_bf16 v[136:139], a[92:95], a[140:143], v[136:139]\n"
"v_add_f32 v13, v13, v17\n"
"v_add_f32 v14, v14, v18\n"
"v_mfma_f32_16x16x32_bf16 v[108:111], a[84:87], a[140:143], v[108:111]\n"
"ds_read_b128 a[140:143], v3 offset:27648\n"
"v_add_f32 v15, v15, v19\n"
"v_mfma_f32_16x16x32_bf16 v[108:111], a[80:83], a[136:139], v[108:111]\n"
"ds_read_b128 a[136:139], v3 offset:26624\n"
"v_accvgpr_write_b32 a16, v12\n"
"v_mfma_f32_16x16x32_bf16 v[112:115], a[80:83], a[144:147], v[112:115]\n"
"v_accvgpr_write_b32 a17, v13\n"
"v_mfma_f32_16x16x32_bf16 v[112:115], a[84:87], a[148:151], v[112:115]\n"
"global_load_dwordx4 a[76:79], v0, s[34:35] offset:3072 sc1\n"
"v_accvgpr_write_b32 a18, v14\n"
"v_mfma_f32_16x16x32_bf16 v[140:143], a[92:95], a[148:151], v[140:143]\n"
"ds_read_b128 a[148:151], v3 offset:29696\n"
"v_accvgpr_write_b32 a19, v15\n"
"v_mfma_f32_16x16x32_bf16 v[140:143], a[88:91], a[144:147], v[140:143]\n"
"ds_read_b128 a[144:147], v3 offset:28672\n"
"v_lshlrev_b32 v20, 16, v22\n"
"v_mfma_f32_16x16x32_bf16 a[12:15], a[88:91], a[152:155], a[12:15]\n"
"v_and_b32 v21, s63, v22\n"
"v_lshlrev_b32 v22, 16, v23\n"
"v_mfma_f32_16x16x32_bf16 a[12:15], a[92:95], a[156:159], a[12:15]\n"
"v_and_b32 v23, s63, v23\n"
"v_mfma_f32_16x16x32_bf16 a[8:11], a[84:87], a[156:159], a[8:11]\n"
"ds_read_b128 a[156:159], v3 offset:31744\n"
"v_add_f32 v172, v172, v20\n"
"v_mfma_f32_16x16x32_bf16 a[8:11], a[80:83], a[152:155], a[8:11]\n"
"s_waitcnt vmcnt(8) lgkmcnt(11)\n"
"ds_read_b128 a[152:155], v3 offset:30720\n"
"v_add_f32 v173, v173, v21\n"
"v_mfma_f32_16x16x32_bf16 v[32:35], a[32:35], a[96:99], v[32:35]\n"
"s_add_u32 s34, s36, 1527808\n"
"v_add_f32 v174, v174, v22\n"
"v_mfma_f32_16x16x32_bf16 v[32:35], a[36:39], a[100:103], v[32:35]\n"
"global_load_dwordx4 a[80:83], v0, s[34:35] offset:0 sc1\n"
"v_add_f32 v175, v175, v23\n"
"v_mfma_f32_16x16x32_bf16 v[60:63], a[44:47], a[100:103], v[60:63]\n"
"v_add_f32 v176, v176, v20\n"
"v_mfma_f32_16x16x32_bf16 v[60:63], a[40:43], a[96:99], v[60:63]\n"
"v_add_f32 v177, v177, v21\n"
"v_add_f32 v178, v178, v22\n"
"v_mfma_f32_16x16x32_bf16 v[64:67], a[40:43], a[104:107], v[64:67]\n"
"v_add_f32 v179, v179, v23\n"
"v_mfma_f32_16x16x32_bf16 v[64:67], a[44:47], a[108:111], v[64:67]\n"
"v_add_f32 v180, v180, v20\n"
"v_mfma_f32_16x16x32_bf16 v[36:39], a[36:39], a[108:111], v[36:39]\n"
"v_add_f32 v181, v181, v21\n"
"v_mfma_f32_16x16x32_bf16 v[36:39], a[32:35], a[104:107], v[36:39]\n"
"s_waitcnt lgkmcnt(8)\n"
"v_add_f32 v182, v182, v22\n"
"v_mfma_f32_16x16x32_bf16 v[40:43], a[32:35], a[112:115], v[40:43]\n"
"v_add_f32 v183, v183, v23\n"
"v_mfma_f32_16x16x32_bf16 v[40:43], a[36:39], a[116:119], v[40:43]\n"
"global_load_dwordx4 a[84:87], v0, s[34:35] offset:1024 sc1\n"
"v_add_f32 v184, v184, v20\n"
"v_mfma_f32_16x16x32_bf16 v[68:71], a[44:47], a[116:119], v[68:71]\n"
"v_add_f32 v185, v185, v21\n"
"v_mfma_f32_16x16x32_bf16 v[68:71], a[40:43], a[112:115], v[68:71]\n"
"v_add_f32 v186, v186, v22\n"
"v_add_f32 v187, v187, v23\n"
"v_mfma_f32_16x16x32_bf16 v[72:75], a[40:43], a[120:123], v[72:75]\n"
"v_add_f32 v188, v188, v20\n"
"v_mfma_f32_16x16x32_bf16 v[72:75], a[44:47], a[124:127], v[72:75]\n"
"v_add_f32 v189, v189, v21\n"
"v_mfma_f32_16x16x32_bf16 v[44:47], a[36:39], a[124:127], v[44:47]\n"
"v_add_f32 v190, v190, v22\n"
"v_mfma_f32_16x16x32_bf16 v[44:47], a[32:35], a[120:123], v[44:47]\n"
"s_waitcnt lgkmcnt(4)\n"
"v_add_f32 v191, v191, v23\n"
"v_mfma_f32_16x16x32_bf16 v[48:51], a[32:35], a[128:131], v[48:51]\n"
"v_add_f32 v192, v192, v20\n"
"v_mfma_f32_16x16x32_bf16 v[48:51], a[36:39], a[132:135], v[48:51]\n"
"global_load_dwordx4 a[88:91], v0, s[34:35] offset:2048 sc1\n"
"v_add_f32 v193, v193, v21\n"
"v_mfma_f32_16x16x32_bf16 v[76:79], a[44:47], a[132:135], v[76:79]\n"
"v_add_f32 v194, v194, v22\n"
"v_add_f32 v195, v195, v23\n"
"v_mfma_f32_16x16x32_bf16 v[76:79], a[40:43], a[128:131], v[76:79]\n"
"v_add_f32 v196, v196, v20\n"
"v_mfma_f32_16x16x32_bf16 v[80:83], a[40:43], a[136:139], v[80:83]\n"
"v_add_f32 v197, v197, v21\n"
"v_mfma_f32_16x16x32_bf16 v[80:83], a[44:47], a[140:143], v[80:83]\n"
"v_add_f32 v198, v198, v22\n"
"v_mfma_f32_16x16x32_bf16 v[52:55], a[36:39], a[140:143], v[52:55]\n"
"v_add_f32 v199, v199, v23\n"
"v_mfma_f32_16x16x32_bf16 v[52:55], a[32:35], a[136:139], v[52:55]\n"
"s_waitcnt lgkmcnt(0)\n"
"v_accvgpr_read_b32 v12, a20\n"
"v_mfma_f32_16x16x32_bf16 v[56:59], a[32:35], a[144:147], v[56:59]\n"
"v_accvgpr_read_b32 v13, a21\n"
"v_mfma_f32_16x16x32_bf16 v[56:59], a[36:39], a[148:151], v[56:59]\n"
"global_load_dwordx4 a[92:95], v0, s[34:35] offset:3072 sc1\n"
"v_accvgpr_read_b32 v14, a22\n"
"v_mfma_f32_16x16x32_bf16 v[84:87], a[44:47], a[148:151], v[84:87]\n"
"v_accvgpr_read_b32 v15, a23\n"
"v_add_f32 v12, v12, v20\n"
"v_mfma_f32_16x16x32_bf16 v[84:87], a[40:43], a[144:147], v[84:87]\n"
"v_add_f32 v13, v13, v21\n"
"v_mfma_f32_16x16x32_bf16 a[4:7], a[40:43], a[152:155], a[4:7]\n"
"v_add_f32 v14, v14, v22\n"
"v_mfma_f32_16x16x32_bf16 a[4:7], a[44:47], a[156:159], a[4:7]\n"
"v_add_f32 v15, v15, v23\n"
"v_mfma_f32_16x16x32_bf16 a[0:3], a[36:39], a[156:159], a[0:3]\n"
"v_accvgpr_write_b32 a20, v12\n"
"v_mfma_f32_16x16x32_bf16 a[0:3], a[32:35], a[152:155], a[0:3]\n"
"s_waitcnt vmcnt(8)\n"
"v_accvgpr_write_b32 a21, v13\n"
"v_mfma_f32_16x16x32_bf16 v[88:91], a[48:51], a[96:99], v[88:91]\n"
"s_add_u32 s34, s36, 1531904\n"
"v_accvgpr_write_b32 a22, v14\n"
"v_accvgpr_write_b32 a23, v15\n"
"v_mfma_f32_16x16x32_bf16 v[88:91], a[52:55], a[100:103], v[88:91]\n"
"global_load_dwordx4 a[32:35], v0, s[34:35] offset:0 sc1\n"
"v_lshlrev_b32 v24, 16, v26\n"
"v_mfma_f32_16x16x32_bf16 v[116:119], a[60:63], a[100:103], v[116:119]\n"
"ds_read_b128 a[100:103], v4 offset:17408\n"
"v_and_b32 v25, s63, v26\n"
"v_mfma_f32_16x16x32_bf16 v[116:119], a[56:59], a[96:99], v[116:119]\n"
"ds_read_b128 a[96:99], v4 offset:16384\n"
"v_lshlrev_b32 v26, 16, v27\n"
"v_mfma_f32_16x16x32_bf16 v[120:123], a[56:59], a[104:107], v[120:123]\n"
"v_and_b32 v27, s63, v27\n"
"v_mfma_f32_16x16x32_bf16 v[120:123], a[60:63], a[108:111], v[120:123]\n"
"v_add_f32 v200, v200, v24\n"
"v_mfma_f32_16x16x32_bf16 v[92:95], a[52:55], a[108:111], v[92:95]\n"
"ds_read_b128 a[108:111], v4 offset:19456\n"
"v_add_f32 v201, v201, v25\n"
"v_mfma_f32_16x16x32_bf16 v[92:95], a[48:51], a[104:107], v[92:95]\n"
"ds_read_b128 a[104:107], v4 offset:18432\n"
"v_add_f32 v202, v202, v26\n"
"v_mfma_f32_16x16x32_bf16 v[96:99], a[48:51], a[112:115], v[96:99]\n"
"v_add_f32 v203, v203, v27\n"
"v_add_f32 v204, v204, v24\n"
"v_mfma_f32_16x16x32_bf16 v[96:99], a[52:55], a[116:119], v[96:99]\n"
"global_load_dwordx4 a[36:39], v0, s[34:35] offset:1024 sc1\n"
"v_add_f32 v205, v205, v25\n"
"v_mfma_f32_16x16x32_bf16 v[124:127], a[60:63], a[116:119], v[124:127]\n"
"ds_read_b128 a[116:119], v4 offset:21504\n"
"v_add_f32 v206, v206, v26\n"
"v_mfma_f32_16x16x32_bf16 v[124:127], a[56:59], a[112:115], v[124:127]\n"
"ds_read_b128 a[112:115], v4 offset:20480\n"
"v_add_f32 v207, v207, v27\n"
"v_mfma_f32_16x16x32_bf16 v[128:131], a[56:59], a[120:123], v[128:131]\n"
"v_add_f32 v208, v208, v24\n"
"v_mfma_f32_16x16x32_bf16 v[128:131], a[60:63], a[124:127], v[128:131]\n"
"v_add_f32 v209, v209, v25\n"
"v_mfma_f32_16x16x32_bf16 v[100:103], a[52:55], a[124:127], v[100:103]\n"
"ds_read_b128 a[124:127], v4 offset:23552\n"
"v_add_f32 v210, v210, v26\n"
"v_mfma_f32_16x16x32_bf16 v[100:103], a[48:51], a[120:123], v[100:103]\n"
"ds_read_b128 a[120:123], v4 offset:22528\n"
"v_add_f32 v211, v211, v27\n"
"v_mfma_f32_16x16x32_bf16 v[104:107], a[48:51], a[128:131], v[104:107]\n"
"v_add_f32 v212, v212, v24\n"
"v_add_f32 v213, v213, v25\n"
"v_mfma_f32_16x16x32_bf16 v[104:107], a[52:55], a[132:135], v[104:107]\n"
"global_load_dwordx4 a[40:43], v0, s[34:35] offset:2048 sc1\n"
"v_add_f32 v214, v214, v26\n"
"v_mfma_f32_16x16x32_bf16 v[132:135], a[60:63], a[132:135], v[132:135]\n"
"ds_read_b128 a[132:135], v4 offset:25600\n"
"v_add_f32 v215, v215, v27\n"
"v_mfma_f32_16x16x32_bf16 v[132:135], a[56:59], a[128:131], v[132:135]\n"
"ds_read_b128 a[128:131], v4 offset:24576\n"
"v_add_f32 v216, v216, v24\n"
"v_mfma_f32_16x16x32_bf16 v[136:139], a[56:59], a[136:139], v[136:139]\n"
"v_add_f32 v217, v217, v25\n"
"v_mfma_f32_16x16x32_bf16 v[136:139], a[60:63], a[140:143], v[136:139]\n"
"v_add_f32 v218, v218, v26\n"
"v_mfma_f32_16x16x32_bf16 v[108:111], a[52:55], a[140:143], v[108:111]\n"
"ds_read_b128 a[140:143], v4 offset:27648\n"
"v_add_f32 v219, v219, v27\n"
"v_mfma_f32_16x16x32_bf16 v[108:111], a[48:51], a[136:139], v[108:111]\n"
"ds_read_b128 a[136:139], v4 offset:26624\n"
"v_add_f32 v220, v220, v24\n"
"v_mfma_f32_16x16x32_bf16 v[112:115], a[48:51], a[144:147], v[112:115]\n"
"v_add_f32 v221, v221, v25\n"
"v_add_f32 v222, v222, v26\n"
"v_mfma_f32_16x16x32_bf16 v[112:115], a[52:55], a[148:151], v[112:115]\n"
"global_load_dwordx4 a[44:47], v0, s[34:35] offset:3072 sc1\n"
"v_add_f32 v223, v223, v27\n"
"v_mfma_f32_16x16x32_bf16 v[140:143], a[60:63], a[148:151], v[140:143]\n"
"ds_read_b128 a[148:151], v4 offset:29696\n"
"v_add_f32 v224, v224, v24\n"
"v_mfma_f32_16x16x32_bf16 v[140:143], a[56:59], a[144:147], v[140:143]\n"
"ds_read_b128 a[144:147], v4 offset:28672\n"
"v_add_f32 v225, v225, v25\n"
"v_mfma_f32_16x16x32_bf16 a[12:15], a[56:59], a[152:155], a[12:15]\n"
"v_add_f32 v226, v226, v26\n"
"v_mfma_f32_16x16x32_bf16 a[12:15], a[60:63], a[156:159], a[12:15]\n"
"v_add_f32 v227, v227, v27\n"
"v_mfma_f32_16x16x32_bf16 a[8:11], a[52:55], a[156:159], a[8:11]\n"
"ds_read_b128 a[156:159], v4 offset:31744\n"
"v_accvgpr_read_b32 v12, a24\n"
"v_mfma_f32_16x16x32_bf16 a[8:11], a[48:51], a[152:155], a[8:11]\n"
"s_waitcnt vmcnt(8) lgkmcnt(11)\n"
"ds_read_b128 a[152:155], v4 offset:30720\n"
"v_accvgpr_read_b32 v13, a25\n"
"v_mfma_f32_16x16x32_bf16 v[32:35], a[64:67], a[96:99], v[32:35]\n"
"s_add_u32 s34, s36, 1536000\n"
"v_accvgpr_read_b32 v14, a26\n"
"v_accvgpr_read_b32 v15, a27\n"
"v_mfma_f32_16x16x32_bf16 v[32:35], a[68:71], a[100:103], v[32:35]\n"
"global_load_dwordx4 a[48:51], v0, s[34:35] offset:0 sc1\n"
"v_add_f32 v12, v12, v24\n"
"v_mfma_f32_16x16x32_bf16 v[60:63], a[76:79], a[100:103], v[60:63]\n"
"v_add_f32 v13, v13, v25\n"
"v_mfma_f32_16x16x32_bf16 v[60:63], a[72:75], a[96:99], v[60:63]\n"
"v_add_f32 v14, v14, v26\n"
"v_mfma_f32_16x16x32_bf16 v[64:67], a[72:75], a[104:107], v[64:67]\n"
"v_add_f32 v15, v15, v27\n"
"v_mfma_f32_16x16x32_bf16 v[64:67], a[76:79], a[108:111], v[64:67]\n"
"v_accvgpr_write_b32 a24, v12\n"
"v_mfma_f32_16x16x32_bf16 v[36:39], a[68:71], a[108:111], v[36:39]\n"
"v_accvgpr_write_b32 a25, v13\n"
"v_accvgpr_write_b32 a26, v14\n"
"v_mfma_f32_16x16x32_bf16 v[36:39], a[64:67], a[104:107], v[36:39]\n"
"s_waitcnt lgkmcnt(8)\n"
"v_accvgpr_write_b32 a27, v15\n"
"v_mfma_f32_16x16x32_bf16 v[40:43], a[64:67], a[112:115], v[40:43]\n"
"v_lshlrev_b32 v28, 16, v30\n"
"v_mfma_f32_16x16x32_bf16 v[40:43], a[68:71], a[116:119], v[40:43]\n"
"global_load_dwordx4 a[52:55], v0, s[34:35] offset:1024 sc1\n"
"v_and_b32 v29, s63, v30\n"
"v_mfma_f32_16x16x32_bf16 v[68:71], a[76:79], a[116:119], v[68:71]\n"
"v_lshlrev_b32 v30, 16, v31\n"
"v_mfma_f32_16x16x32_bf16 v[68:71], a[72:75], a[112:115], v[68:71]\n"
"v_and_b32 v31, s63, v31\n"
"v_mfma_f32_16x16x32_bf16 v[72:75], a[72:75], a[120:123], v[72:75]\n"
"v_add_f32 v228, v228, v28\n"
"v_mfma_f32_16x16x32_bf16 v[72:75], a[76:79], a[124:127], v[72:75]\n"
"v_add_f32 v229, v229, v29\n"
"v_add_f32 v230, v230, v30\n"
"v_mfma_f32_16x16x32_bf16 v[44:47], a[68:71], a[124:127], v[44:47]\n"
"v_add_f32 v231, v231, v31\n"
"v_mfma_f32_16x16x32_bf16 v[44:47], a[64:67], a[120:123], v[44:47]\n"
"s_waitcnt lgkmcnt(4)\n"
"v_add_f32 v232, v232, v28\n"
"v_mfma_f32_16x16x32_bf16 v[48:51], a[64:67], a[128:131], v[48:51]\n"
"v_add_f32 v233, v233, v29\n"
"v_mfma_f32_16x16x32_bf16 v[48:51], a[68:71], a[132:135], v[48:51]\n"
"global_load_dwordx4 a[56:59], v0, s[34:35] offset:2048 sc1\n"
"v_add_f32 v234, v234, v30\n"
"v_mfma_f32_16x16x32_bf16 v[76:79], a[76:79], a[132:135], v[76:79]\n"
"v_add_f32 v235, v235, v31\n"
"v_mfma_f32_16x16x32_bf16 v[76:79], a[72:75], a[128:131], v[76:79]\n"
"v_add_f32 v236, v236, v28\n"
"v_mfma_f32_16x16x32_bf16 v[80:83], a[72:75], a[136:139], v[80:83]\n"
"v_add_f32 v237, v237, v29\n"
"v_mfma_f32_16x16x32_bf16 v[80:83], a[76:79], a[140:143], v[80:83]\n"
"v_add_f32 v238, v238, v30\n"
"v_add_f32 v239, v239, v31\n"
"v_mfma_f32_16x16x32_bf16 v[52:55], a[68:71], a[140:143], v[52:55]\n"
"v_add_f32 v240, v240, v28\n"
"v_mfma_f32_16x16x32_bf16 v[52:55], a[64:67], a[136:139], v[52:55]\n"
"s_waitcnt lgkmcnt(0)\n"
"v_add_f32 v241, v241, v29\n"
"v_mfma_f32_16x16x32_bf16 v[56:59], a[64:67], a[144:147], v[56:59]\n"
"v_add_f32 v242, v242, v30\n"
"v_mfma_f32_16x16x32_bf16 v[56:59], a[68:71], a[148:151], v[56:59]\n"
"global_load_dwordx4 a[60:63], v0, s[34:35] offset:3072 sc1\n"
"v_add_f32 v243, v243, v31\n"
"v_mfma_f32_16x16x32_bf16 v[84:87], a[76:79], a[148:151], v[84:87]\n"
"v_add_f32 v244, v244, v28\n"
"v_mfma_f32_16x16x32_bf16 v[84:87], a[72:75], a[144:147], v[84:87]\n"
"v_add_f32 v245, v245, v29\n"
"v_mfma_f32_16x16x32_bf16 a[4:7], a[72:75], a[152:155], a[4:7]\n"
"v_add_f32 v246, v246, v30\n"
"v_add_f32 v247, v247, v31\n"
"v_mfma_f32_16x16x32_bf16 a[4:7], a[76:79], a[156:159], a[4:7]\n"
"v_add_f32 v248, v248, v28\n"
"v_mfma_f32_16x16x32_bf16 a[0:3], a[68:71], a[156:159], a[0:3]\n"
"v_add_f32 v249, v249, v29\n"
"v_mfma_f32_16x16x32_bf16 a[0:3], a[64:67], a[152:155], a[0:3]\n"
"s_waitcnt vmcnt(8)\n"
"v_add_f32 v250, v250, v30\n"
"v_mfma_f32_16x16x32_bf16 v[88:91], a[80:83], a[96:99], v[88:91]\n"
"s_add_u32 s34, s36, 1466368\n"
"v_add_f32 v251, v251, v31\n"
"v_mfma_f32_16x16x32_bf16 v[88:91], a[84:87], a[100:103], v[88:91]\n"
"global_load_dwordx4 a[64:67], v0, s[34:35] offset:0 sc1\n"
"v_add_f32 v252, v252, v28\n"
"v_mfma_f32_16x16x32_bf16 v[116:119], a[92:95], a[100:103], v[116:119]\n"
"ds_read_b128 a[100:103], v5 offset:17408\n"
"v_add_f32 v253, v253, v29\n"
"v_mfma_f32_16x16x32_bf16 v[116:119], a[88:91], a[96:99], v[116:119]\n"
"ds_read_b128 a[96:99], v5 offset:16384\n"
"v_add_f32 v254, v254, v30\n"
"v_mfma_f32_16x16x32_bf16 v[120:123], a[88:91], a[104:107], v[120:123]\n"
"v_add_f32 v255, v255, v31\n"
"v_accvgpr_read_b32 v12, a28\n"
"v_mfma_f32_16x16x32_bf16 v[120:123], a[92:95], a[108:111], v[120:123]\n"
"v_accvgpr_read_b32 v13, a29\n"
"v_mfma_f32_16x16x32_bf16 v[92:95], a[84:87], a[108:111], v[92:95]\n"
"ds_read_b128 a[108:111], v5 offset:19456\n"
"v_accvgpr_read_b32 v14, a30\n"
"v_mfma_f32_16x16x32_bf16 v[92:95], a[80:83], a[104:107], v[92:95]\n"
"ds_read_b128 a[104:107], v5 offset:18432\n"
"v_accvgpr_read_b32 v15, a31\n"
"v_mfma_f32_16x16x32_bf16 v[96:99], a[80:83], a[112:115], v[96:99]\n"
"v_add_f32 v12, v12, v28\n"
"v_mfma_f32_16x16x32_bf16 v[96:99], a[84:87], a[116:119], v[96:99]\n"
"global_load_dwordx4 a[68:71], v0, s[34:35] offset:1024 sc1\n"
"v_add_f32 v13, v13, v29\n"
"v_mfma_f32_16x16x32_bf16 v[124:127], a[92:95], a[116:119], v[124:127]\n"
"ds_read_b128 a[116:119], v5 offset:21504\n"
"v_add_f32 v14, v14, v30\n"
"v_mfma_f32_16x16x32_bf16 v[124:127], a[88:91], a[112:115], v[124:127]\n"
"ds_read_b128 a[112:115], v5 offset:20480\n"
"v_add_f32 v15, v15, v31\n"
"v_mfma_f32_16x16x32_bf16 v[128:131], a[88:91], a[120:123], v[128:131]\n"
"v_accvgpr_write_b32 a28, v12\n"
"v_accvgpr_write_b32 a29, v13\n"
"v_mfma_f32_16x16x32_bf16 v[128:131], a[92:95], a[124:127], v[128:131]\n"
"v_accvgpr_write_b32 a30, v14\n"
"v_mfma_f32_16x16x32_bf16 v[100:103], a[84:87], a[124:127], v[100:103]\n"
"ds_read_b128 a[124:127], v5 offset:23552\n"
"v_accvgpr_write_b32 a31, v15\n"
"v_mfma_f32_16x16x32_bf16 v[100:103], a[80:83], a[120:123], v[100:103]\n"
"ds_read_b128 a[120:123], v5 offset:22528\n"
"v_mfma_f32_16x16x32_bf16 v[104:107], a[80:83], a[128:131], v[104:107]\n"
"v_mfma_f32_16x16x32_bf16 v[104:107], a[84:87], a[132:135], v[104:107]\n"
"global_load_dwordx4 a[72:75], v0, s[34:35] offset:2048 sc1\n"
"v_mfma_f32_16x16x32_bf16 v[132:135], a[92:95], a[132:135], v[132:135]\n"
"ds_read_b128 a[132:135], v5 offset:25600\n"
"v_mfma_f32_16x16x32_bf16 v[132:135], a[88:91], a[128:131], v[132:135]\n"
"ds_read_b128 a[128:131], v5 offset:24576\n"
"v_mfma_f32_16x16x32_bf16 v[136:139], a[88:91], a[136:139], v[136:139]\n"
"ds_read_b128 v[16:19], v7 offset:32768\n"
"v_mfma_f32_16x16x32_bf16 v[136:139], a[92:95], a[140:143], v[136:139]\n"
"v_mfma_f32_16x16x32_bf16 v[108:111], a[84:87], a[140:143], v[108:111]\n"
"ds_read_b128 a[140:143], v5 offset:27648\n"
"v_mfma_f32_16x16x32_bf16 v[108:111], a[80:83], a[136:139], v[108:111]\n"
"ds_read_b128 a[136:139], v5 offset:26624\n"
"v_mfma_f32_16x16x32_bf16 v[112:115], a[80:83], a[144:147], v[112:115]\n"
"v_mfma_f32_16x16x32_bf16 v[112:115], a[84:87], a[148:151], v[112:115]\n"
"global_load_dwordx4 a[76:79], v0, s[34:35] offset:3072 sc1\n"
"v_mfma_f32_16x16x32_bf16 v[140:143], a[92:95], a[148:151], v[140:143]\n"
"ds_read_b128 a[148:151], v5 offset:29696\n"
"v_mfma_f32_16x16x32_bf16 v[140:143], a[88:91], a[144:147], v[140:143]\n"
"ds_read_b128 a[144:147], v5 offset:28672\n"
"v_mfma_f32_16x16x32_bf16 a[12:15], a[88:91], a[152:155], a[12:15]\n"
"v_mfma_f32_16x16x32_bf16 a[12:15], a[92:95], a[156:159], a[12:15]\n"
"v_mfma_f32_16x16x32_bf16 a[8:11], a[84:87], a[156:159], a[8:11]\n"
"ds_read_b128 a[156:159], v5 offset:31744\n"
"v_mfma_f32_16x16x32_bf16 a[8:11], a[80:83], a[152:155], a[8:11]\n"
"s_waitcnt vmcnt(8) lgkmcnt(12)\n"
"ds_read_b128 a[152:155], v5 offset:30720\n"
"v_mfma_f32_16x16x32_bf16 v[32:35], a[32:35], a[96:99], v[32:35]\n"
"s_add_u32 s34, s36, 1470464\n"
"v_mfma_f32_16x16x32_bf16 v[32:35], a[36:39], a[100:103], v[32:35]\n"
"global_load_dwordx4 a[80:83], v0, s[34:35] offset:0 sc1\n"
"v_mfma_f32_16x16x32_bf16 v[60:63], a[44:47], a[100:103], v[60:63]\n"
"v_mfma_f32_16x16x32_bf16 v[60:63], a[40:43], a[96:99], v[60:63]\n"
"v_mfma_f32_16x16x32_bf16 v[64:67], a[40:43], a[104:107], v[64:67]\n"
"v_mfma_f32_16x16x32_bf16 v[64:67], a[44:47], a[108:111], v[64:67]\n"
"v_mfma_f32_16x16x32_bf16 v[36:39], a[36:39], a[108:111], v[36:39]\n"
"v_mfma_f32_16x16x32_bf16 v[36:39], a[32:35], a[104:107], v[36:39]\n"
"s_waitcnt lgkmcnt(9)\n"
"v_mfma_f32_16x16x32_bf16 v[40:43], a[32:35], a[112:115], v[40:43]\n"
"s_mov_b32 s52, %4\n"
"s_and_b32 s53, %5, 0xffff\n"
"v_mfma_f32_16x16x32_bf16 v[40:43], a[36:39], a[116:119], v[40:43]\n"
"global_load_dwordx4 a[84:87], v0, s[34:35] offset:1024 sc1\n"
"v_mfma_f32_16x16x32_bf16 v[68:71], a[44:47], a[116:119], v[68:71]\n"
"s_mov_b32 s54, 260352\n"
"s_mov_b32 s55, 0x20000\n"
"v_mfma_f32_16x16x32_bf16 v[68:71], a[40:43], a[112:115], v[68:71]\n"
"v_xor_b32 v8, 64, v7\n"
"v_mfma_f32_16x16x32_bf16 v[72:75], a[40:43], a[120:123], v[72:75]\n"
"s_waitcnt lgkmcnt(6)\n"
"v_lshlrev_b32 v12, 16, v16\n"
"v_mfma_f32_16x16x32_bf16 v[72:75], a[44:47], a[124:127], v[72:75]\n"
"v_and_b32 v13, s63, v16\n"
"v_lshlrev_b32 v14, 16, v17\n"
"v_mfma_f32_16x16x32_bf16 v[44:47], a[36:39], a[124:127], v[44:47]\n"
"v_and_b32 v15, s63, v17\n"
"v_mfma_f32_16x16x32_bf16 v[44:47], a[32:35], a[120:123], v[44:47]\n"
"s_waitcnt lgkmcnt(4)\n"
"v_xor_b32 v20, 64, v7\n"
"v_mfma_f32_16x16x32_bf16 v[48:51], a[32:35], a[128:131], v[48:51]\n"
"ds_read_b128 v[20:23], v20 offset:32768\n"
"v_mul_f32 v12, v12, v32\n"
"v_mfma_f32_16x16x32_bf16 v[48:51], a[36:39], a[132:135], v[48:51]\n"
"global_load_dwordx4 a[88:91], v0, s[34:35] offset:2048 sc1\n"
"v_mul_f32 v13, v13, v33\n"
"v_mfma_f32_16x16x32_bf16 v[76:79], a[44:47], a[132:135], v[76:79]\n"
"v_mul_f32 v14, v14, v34\n"
"v_mfma_f32_16x16x32_bf16 v[76:79], a[40:43], a[128:131], v[76:79]\n"
"v_mul_f32 v15, v15, v35\n"
"v_cvt_pk_bf16_f32 v24, v12, v13\n"
"v_mfma_f32_16x16x32_bf16 v[80:83], a[40:43], a[136:139], v[80:83]\n"
"v_cvt_pk_bf16_f32 v25, v14, v15\n"
"v_mfma_f32_16x16x32_bf16 v[80:83], a[44:47], a[140:143], v[80:83]\n"
"ds_write_b64 v7, v[24:25] offset:32768\n"
"v_mfma_f32_16x16x32_bf16 v[52:55], a[36:39], a[140:143], v[52:55]\n"
"v_lshlrev_b32 v12, 16, v18\n"
"v_mfma_f32_16x16x32_bf16 v[52:55], a[32:35], a[136:139], v[52:55]\n"
"s_waitcnt lgkmcnt(2)\n"
"v_and_b32 v13, s63, v18\n"
"v_lshlrev_b32 v14, 16, v19\n"
"v_mfma_f32_16x16x32_bf16 v[56:59], a[32:35], a[144:147], v[56:59]\n"
"v_and_b32 v15, s63, v19\n"
"v_mfma_f32_16x16x32_bf16 v[56:59], a[36:39], a[148:151], v[56:59]\n"
"global_load_dwordx4 a[92:95], v0, s[34:35] offset:3072 sc1\n"
"v_mul_f32 v12, v12, v60\n"
"v_mfma_f32_16x16x32_bf16 v[84:87], a[44:47], a[148:151], v[84:87]\n"
"v_mul_f32 v13, v13, v61\n"
"v_mul_f32 v14, v14, v62\n"
"v_mfma_f32_16x16x32_bf16 v[84:87], a[40:43], a[144:147], v[84:87]\n"
"v_mul_f32 v15, v15, v63\n"
"v_mfma_f32_16x16x32_bf16 a[4:7], a[40:43], a[152:155], a[4:7]\n"
"v_cvt_pk_bf16_f32 v26, v12, v13\n"
"v_mfma_f32_16x16x32_bf16 a[4:7], a[44:47], a[156:159], a[4:7]\n"
"v_cvt_pk_bf16_f32 v27, v14, v15\n"
"v_mfma_f32_16x16x32_bf16 a[0:3], a[36:39], a[156:159], a[0:3]\n"
"ds_write_b64 v7, v[26:27] offset:32776\n"
"v_mfma_f32_16x16x32_bf16 a[0:3], a[32:35], a[152:155], a[0:3]\n"
"s_waitcnt vmcnt(8) lgkmcnt(2)\n"
"v_lshlrev_b32 v12, 16, v20\n"
"v_mfma_f32_16x16x32_bf16 v[88:91], a[48:51], a[96:99], v[88:91]\n"
"s_add_u32 s34, s36, 1482752\n"
"v_and_b32 v13, s63, v20\n"
"v_mfma_f32_16x16x32_bf16 v[88:91], a[52:55], a[100:103], v[88:91]\n"
"global_load_dwordx4 a[32:35], v0, s[34:35] offset:0 sc1\n"
"v_mfma_f32_16x16x32_bf16 v[116:119], a[60:63], a[100:103], v[116:119]\n"
"ds_read_b128 a[100:103], v3 offset:1024\n"
"v_mfma_f32_16x16x32_bf16 v[116:119], a[56:59], a[96:99], v[116:119]\n"
"ds_read_b128 a[96:99], v3 offset:0\n"
"v_mfma_f32_16x16x32_bf16 v[120:123], a[56:59], a[104:107], v[120:123]\n"
"v_lshlrev_b32 v14, 16, v21\n"
"v_mfma_f32_16x16x32_bf16 v[120:123], a[60:63], a[108:111], v[120:123]\n"
"v_and_b32 v15, s63, v21\n"
"v_mfma_f32_16x16x32_bf16 v[92:95], a[52:55], a[108:111], v[92:95]\n"
"ds_read_b128 a[108:111], v3 offset:3072\n"
"v_mfma_f32_16x16x32_bf16 v[92:95], a[48:51], a[104:107], v[92:95]\n"
"ds_read_b128 a[104:107], v3 offset:2048\n"
"v_mfma_f32_16x16x32_bf16 v[96:99], a[48:51], a[112:115], v[96:99]\n"
"ds_read_b128 v[16:19], v7 offset:34816\n"
"v_mfma_f32_16x16x32_bf16 v[96:99], a[52:55], a[116:119], v[96:99]\n"
"global_load_dwordx4 a[36:39], v0, s[34:35] offset:1024 sc1\n"
"v_mul_f32 v12, v12, v88\n"
"v_mul_f32 v13, v13, v89\n"
"v_mfma_f32_16x16x32_bf16 v[124:127], a[60:63], a[116:119], v[124:127]\n"
"ds_read_b128 a[116:119], v3 offset:5120\n"
"v_mul_f32 v14, v14, v90\n"
"v_mul_f32 v15, v15, v91\n"
"v_mfma_f32_16x16x32_bf16 v[124:127], a[56:59], a[112:115], v[124:127]\n"
"ds_read_b128 a[112:115], v3 offset:4096\n"
"v_cvt_pk_bf16_f32 v28, v12, v13\n"
"v_cvt_pk_bf16_f32 v29, v14, v15\n"
"v_mfma_f32_16x16x32_bf16 v[128:131], a[56:59], a[120:123], v[128:131]\n"
"ds_write_b64 v8, v[28:29] offset:32768\n"
"v_mfma_f32_16x16x32_bf16 v[128:131], a[60:63], a[124:127], v[128:131]\n"
"v_lshlrev_b32 v12, 16, v22\n"
"v_and_b32 v13, s63, v22\n"
"v_lshlrev_b32 v14, 16, v23\n"
"v_mfma_f32_16x16x32_bf16 v[100:103], a[52:55], a[124:127], v[100:103]\n"
"ds_read_b128 a[124:127], v3 offset:7168\n"
"v_and_b32 v15, s63, v23\n"
"v_mul_f32 v12, v12, v116\n"
"v_mfma_f32_16x16x32_bf16 v[100:103], a[48:51], a[120:123], v[100:103]\n"
"ds_read_b128 a[120:123], v3 offset:6144\n"
"v_mul_f32 v13, v13, v117\n"
"v_mfma_f32_16x16x32_bf16 v[104:107], a[48:51], a[128:131], v[104:107]\n"
"v_mul_f32 v14, v14, v118\n"
"v_mul_f32 v15, v15, v119\n"
"v_mfma_f32_16x16x32_bf16 v[104:107], a[52:55], a[132:135], v[104:107]\n"
"global_load_dwordx4 a[40:43], v0, s[34:35] offset:2048 sc1\n"
"v_cvt_pk_bf16_f32 v30, v12, v13\n"
"v_mfma_f32_16x16x32_bf16 v[132:135], a[60:63], a[132:135], v[132:135]\n"
"ds_read_b128 a[132:135], v3 offset:9216\n"
"v_cvt_pk_bf16_f32 v31, v14, v15\n"
"v_mfma_f32_16x16x32_bf16 v[132:135], a[56:59], a[128:131], v[132:135]\n"
"ds_read_b128 a[128:131], v3 offset:8192\n"
"ds_write_b64 v8, v[30:31] offset:32776\n"
"v_mfma_f32_16x16x32_bf16 v[136:139], a[56:59], a[136:139], v[136:139]\n"
"ds_read_b128 v[32:35], v2 offset:32768\n"
"v_mfma_f32_16x16x32_bf16 v[136:139], a[60:63], a[140:143], v[136:139]\n"
"s_add_u32 s96, s36, 1552384\n"
"ds_read_b128 v[60:63], v2 offset:33792\n"
"v_mfma_f32_16x16x32_bf16 v[108:111], a[52:55], a[140:143], v[108:111]\n"
"s_waitcnt lgkmcnt(10)\n"
"ds_read_b128 a[140:143], v3 offset:11264\n"
"v_lshlrev_b32 v12, 16, v16\n"
"v_mfma_f32_16x16x32_bf16 v[108:111], a[48:51], a[136:139], v[108:111]\n"
"ds_read_b128 a[136:139], v3 offset:10240\n"
"v_and_b32 v13, s63, v16\n"
"v_mfma_f32_16x16x32_bf16 v[112:115], a[48:51], a[144:147], v[112:115]\n"
"global_load_dwordx4 v[88:91], v0, s[96:97] offset:-4096 sc1\n"
"v_lshlrev_b32 v14, 16, v17\n"
"v_mfma_f32_16x16x32_bf16 v[112:115], a[52:55], a[148:151], v[112:115]\n"
"global_load_dwordx4 a[44:47], v0, s[34:35] offset:3072 sc1\n"
"v_and_b32 v15, s63, v17\n"
"v_mfma_f32_16x16x32_bf16 v[140:143], a[60:63], a[148:151], v[140:143]\n"
"ds_read_b128 a[148:151], v3 offset:13312\n"
"v_xor_b32 v20, 64, v7\n"
"v_mfma_f32_16x16x32_bf16 v[140:143], a[56:59], a[144:147], v[140:143]\n"
"ds_read_b128 a[144:147], v3 offset:12288\n"
"v_mfma_f32_16x16x32_bf16 a[12:15], a[56:59], a[152:155], a[12:15]\n"
"ds_read_b128 v[20:23], v20 offset:34816\n"
"v_mul_f32 v12, v12, v36\n"
"v_mfma_f32_16x16x32_bf16 a[12:15], a[60:63], a[156:159], a[12:15]\n"
"s_waitcnt lgkmcnt(6)\n"
"buffer_store_dwordx4 v[32:35], v6, s[52:55], s80 offen offset:0 sc1\n"
"v_mul_f32 v13, v13, v37\n"
"v_mul_f32 v14, v14, v38\n"
"v_mul_f32 v15, v15, v39\n"
"v_mfma_f32_16x16x32_bf16 a[8:11], a[52:55], a[156:159], a[8:11]\n"
"ds_read_b128 a[156:159], v3 offset:15360\n"
"v_cvt_pk_bf16_f32 v24, v12, v13\n"
"v_mfma_f32_16x16x32_bf16 a[8:11], a[48:51], a[152:155], a[8:11]\n"
"s_waitcnt vmcnt(10)\n"
"ds_read_b128 a[152:155], v3 offset:14336\n"
"v_cvt_pk_bf16_f32 v25, v14, v15\n"
"v_mfma_f32_16x16x32_bf16 v[144:147], a[64:67], a[96:99], v[144:147]\n"
"s_add_u32 s34, s36, 1486848\n"
"ds_write_b64 v7, v[24:25] offset:34816\n"
"v_mfma_f32_16x16x32_bf16 v[144:147], a[68:71], a[100:103], v[144:147]\n"
"global_load_dwordx4 a[48:51], v0, s[34:35] offset:0 sc1\n"
"v_lshlrev_b32 v12, 16, v18\n"
"v_mfma_f32_16x16x32_bf16 v[172:175], a[76:79], a[100:103], v[172:175]\n"
"global_load_dwordx4 v[116:119], v0, s[96:97] offset:-3072 sc1\n"
"v_and_b32 v13, s63, v18\n"
"v_mfma_f32_16x16x32_bf16 v[172:175], a[72:75], a[96:99], v[172:175]\n"
"v_lshlrev_b32 v14, 16, v19\n"
"v_and_b32 v15, s63, v19\n"
"v_mfma_f32_16x16x32_bf16 v[176:179], a[72:75], a[104:107], v[176:179]\n"
"v_mul_f32 v12, v12, v64\n"
"v_mul_f32 v13, v13, v65\n"
"v_mfma_f32_16x16x32_bf16 v[176:179], a[76:79], a[108:111], v[176:179]\n"
"v_mul_f32 v14, v14, v66\n"
"v_mul_f32 v15, v15, v67\n"
"v_mfma_f32_16x16x32_bf16 v[148:151], a[68:71], a[108:111], v[148:151]\n"
"v_cvt_pk_bf16_f32 v26, v12, v13\n"
"v_cvt_pk_bf16_f32 v27, v14, v15\n"
"v_mfma_f32_16x16x32_bf16 v[148:151], a[64:67], a[104:107], v[148:151]\n"
"ds_write_b64 v7, v[26:27] offset:34824\n"
"v_mfma_f32_16x16x32_bf16 v[152:155], a[64:67], a[112:115], v[152:155]\n"
"s_waitcnt lgkmcnt(4)\n"
"v_lshlrev_b32 v12, 16, v20\n"
"v_and_b32 v13, s63, v20\n"
"v_mfma_f32_16x16x32_bf16 v[152:155], a[68:71], a[116:119], v[152:155]\n"
"global_load_dwordx4 a[52:55], v0, s[34:35] offset:1024 sc1\n"
"v_lshlrev_b32 v14, 16, v21\n"
"v_mfma_f32_16x16x32_bf16 v[180:183], a[76:79], a[116:119], v[180:183]\n"
"ds_read_b128 v[16:19], v7 offset:36864\n"
"v_and_b32 v15, s63, v21\n"
"v_mfma_f32_16x16x32_bf16 v[180:183], a[72:75], a[112:115], v[180:183]\n"
"v_mul_f32 v12, v12, v92\n"
"v_mul_f32 v13, v13, v93\n"
"v_mfma_f32_16x16x32_bf16 v[184:187], a[72:75], a[120:123], v[184:187]\n"
"v_mul_f32 v14, v14, v94\n"
"v_mul_f32 v15, v15, v95\n"
"v_mfma_f32_16x16x32_bf16 v[184:187], a[76:79], a[124:127], v[184:187]\n"
"v_cvt_pk_bf16_f32 v28, v12, v13\n"
"v_cvt_pk_bf16_f32 v29, v14, v15\n"
"v_mfma_f32_16x16x32_bf16 v[156:159], a[68:71], a[124:127], v[156:159]\n"
"ds_write_b64 v8, v[28:29] offset:34816\n"
"v_mfma_f32_16x16x32_bf16 v[156:159], a[64:67], a[120:123], v[156:159]\n"
"v_lshlrev_b32 v12, 16, v22\n"
"v_and_b32 v13, s63, v22\n"
"v_mfma_f32_16x16x32_bf16 v[160:163], a[64:67], a[128:131], v[160:163]\n"
"v_lshlrev_b32 v14, 16, v23\n"
"v_and_b32 v15, s63, v23\n"
"v_mfma_f32_16x16x32_bf16 v[160:163], a[68:71], a[132:135], v[160:163]\n"
"global_load_dwordx4 a[56:59], v0, s[34:35] offset:2048 sc1\n"
"v_mul_f32 v12, v12, v120\n"
"v_mfma_f32_16x16x32_bf16 v[188:191], a[76:79], a[132:135], v[188:191]\n"
"v_mul_f32 v13, v13, v121\n"
"v_mul_f32 v14, v14, v122\n"
"v_mfma_f32_16x16x32_bf16 v[188:191], a[72:75], a[128:131], v[188:191]\n"
"v_mul_f32 v15, v15, v123\n"
"v_cvt_pk_bf16_f32 v30, v12, v13\n"
"v_mfma_f32_16x16x32_bf16 v[192:195], a[72:75], a[136:139], v[192:195]\n"
"v_cvt_pk_bf16_f32 v31, v14, v15\n"
"v_mfma_f32_16x16x32_bf16 v[192:195], a[76:79], a[140:143], v[192:195]\n"
"ds_write_b64 v8, v[30:31] offset:34824\n"
"v_mfma_f32_16x16x32_bf16 v[164:167], a[68:71], a[140:143], v[164:167]\n"
"ds_read_b128 v[36:39], v2 offset:34816\n"
"v_mfma_f32_16x16x32_bf16 v[164:167], a[64:67], a[136:139], v[164:167]\n"
"s_waitcnt lgkmcnt(3)\n"
"ds_read_b128 v[64:67], v2 offset:35840\n"
"v_lshlrev_b32 v12, 16, v16\n"
"v_mfma_f32_16x16x32_bf16 v[168:171], a[64:67], a[144:147], v[168:171]\n"
"buffer_store_dwordx4 v[60:63], v6, s[52:55], s81 offen offset:1024 sc1\n"
"v_and_b32 v13, s63, v16\n"
"v_lshlrev_b32 v14, 16, v17\n"
"v_and_b32 v15, s63, v17\n"
"v_xor_b32 v20, 64, v7\n"
"v_mfma_f32_16x16x32_bf16 v[168:171], a[68:71], a[148:151], v[168:171]\n"
"global_load_dwordx4 a[60:63], v0, s[34:35] offset:3072 sc1\n"
"ds_read_b128 v[20:23], v20 offset:36864\n"
"v_mfma_f32_16x16x32_bf16 v[196:199], a[76:79], a[148:151], v[196:199]\n"
"v_mul_f32 v12, v12, v40\n"
"v_mul_f32 v13, v13, v41\n"
"v_mfma_f32_16x16x32_bf16 v[196:199], a[72:75], a[144:147], v[196:199]\n"
"v_mul_f32 v14, v14, v42\n"
"v_mul_f32 v15, v15, v43\n"
"v_mfma_f32_16x16x32_bf16 a[20:23], a[72:75], a[152:155], a[20:23]\n"
"v_cvt_pk_bf16_f32 v24, v12, v13\n"
"v_cvt_pk_bf16_f32 v25, v14, v15\n"
"v_mfma_f32_16x16x32_bf16 a[20:23], a[76:79], a[156:159], a[20:23]\n"
"ds_write_b64 v7, v[24:25] offset:36864\n"
"v_mfma_f32_16x16x32_bf16 a[16:19], a[68:71], a[156:159], a[16:19]\n"
"global_load_dwordx4 v[92:95], v0, s[96:97] offset:-2048 sc1\n"
"v_lshlrev_b32 v12, 16, v18\n"
"v_mfma_f32_16x16x32_bf16 a[16:19], a[64:67], a[152:155], a[16:19]\n"
"s_waitcnt vmcnt(13)\n"
"v_and_b32 v13, s63, v18\n"
"v_lshlrev_b32 v14, 16, v19\n"
"v_mfma_f32_16x16x32_bf16 v[200:203], a[80:83], a[96:99], v[200:203]\n"
"s_add_u32 s34, s36, 1499136\n"
"v_and_b32 v15, s63, v19\n"
"v_mul_f32 v12, v12, v68\n"
"v_mfma_f32_16x16x32_bf16 v[200:203], a[84:87], a[100:103], v[200:203]\n"
"global_load_dwordx4 a[64:67], v0, s[34:35] offset:0 sc1\n"
"v_mul_f32 v13, v13, v69\n"
"v_mfma_f32_16x16x32_bf16 v[228:231], a[92:95], a[100:103], v[228:231]\n"
"ds_read_b128 a[100:103], v4 offset:1024\n"
"v_mul_f32 v14, v14, v70\n"
"v_mfma_f32_16x16x32_bf16 v[228:231], a[88:91], a[96:99], v[228:231]\n"
"ds_read_b128 a[96:99], v4 offset:0\n"
"v_mul_f32 v15, v15, v71\n"
"v_mfma_f32_16x16x32_bf16 v[232:235], a[88:91], a[104:107], v[232:235]\n"
"v_cvt_pk_bf16_f32 v26, v12, v13\n"
"v_cvt_pk_bf16_f32 v27, v14, v15\n"
"v_mfma_f32_16x16x32_bf16 v[232:235], a[92:95], a[108:111], v[232:235]\n"
"ds_write_b64 v7, v[26:27] offset:36872\n"
"v_mfma_f32_16x16x32_bf16 v[204:207], a[84:87], a[108:111], v[204:207]\n"
"ds_read_b128 a[108:111], v4 offset:3072\n"
"v_mfma_f32_16x16x32_bf16 v[204:207], a[80:83], a[104:107], v[204:207]\n"
"ds_read_b128 a[104:107], v4 offset:2048\n"
"v_mfma_f32_16x16x32_bf16 v[208:211], a[80:83], a[112:115], v[208:211]\n"
"global_load_dwordx4 v[120:123], v0, s[96:97] offset:-1024 sc1\n"
"v_mfma_f32_16x16x32_bf16 v[208:211], a[84:87], a[116:119], v[208:211]\n"
"global_load_dwordx4 a[68:71], v0, s[34:35] offset:1024 sc1\n"
"v_mfma_f32_16x16x32_bf16 v[236:239], a[92:95], a[116:119], v[236:239]\n"
"s_waitcnt lgkmcnt(6)\n"
"ds_read_b128 a[116:119], v4 offset:5120\n"
"v_lshlrev_b32 v12, 16, v20\n"
"v_mfma_f32_16x16x32_bf16 v[236:239], a[88:91], a[112:115], v[236:239]\n"
"ds_read_b128 a[112:115], v4 offset:4096\n"
"v_and_b32 v13, s63, v20\n"
"v_mfma_f32_16x16x32_bf16 v[240:243], a[88:91], a[120:123], v[240:243]\n"
"v_lshlrev_b32 v14, 16, v21\n"
"v_and_b32 v15, s63, v21\n"
"v_mfma_f32_16x16x32_bf16 v[240:243], a[92:95], a[124:127], v[240:243]\n"
"ds_read_b128 v[16:19], v7 offset:38912\n"
"v_mul_f32 v12, v12, v96\n"
"v_mfma_f32_16x16x32_bf16 v[212:215], a[84:87], a[124:127], v[212:215]\n"
"ds_read_b128 a[124:127], v4 offset:7168\n"
"v_mul_f32 v13, v13, v97\n"
"v_mfma_f32_16x16x32_bf16 v[212:215], a[80:83], a[120:123], v[212:215]\n"
"ds_read_b128 a[120:123], v4 offset:6144\n"
"v_mul_f32 v14, v14, v98\n"
"v_mfma_f32_16x16x32_bf16 v[216:219], a[80:83], a[128:131], v[216:219]\n"
"v_mul_f32 v15, v15, v99\n"
"v_mfma_f32_16x16x32_bf16 v[216:219], a[84:87], a[132:135], v[216:219]\n"
"global_load_dwordx4 a[72:75], v0, s[34:35] offset:2048 sc1\n"
"v_cvt_pk_bf16_f32 v28, v12, v13\n"
"v_mfma_f32_16x16x32_bf16 v[244:247], a[92:95], a[132:135], v[244:247]\n"
"ds_read_b128 a[132:135], v4 offset:9216\n"
"global_load_dwordx4 v[32:35], v0, s[96:97] offset:0 sc1\n"
"v_mfma_f32_16x16x32_bf16 v[244:247], a[88:91], a[128:131], v[244:247]\n"
"ds_read_b128 a[128:131], v4 offset:8192\n"
"v_mfma_f32_16x16x32_bf16 v[248:251], a[88:91], a[136:139], v[248:251]\n"
"v_cvt_pk_bf16_f32 v29, v14, v15\n"
"v_mfma_f32_16x16x32_bf16 v[248:251], a[92:95], a[140:143], v[248:251]\n"
"buffer_store_dwordx4 v[36:39], v6, s[52:55], s82 offen offset:2048 sc1\n"
"ds_write_b64 v8, v[28:29] offset:36864\n"
"v_mfma_f32_16x16x32_bf16 v[220:223], a[84:87], a[140:143], v[220:223]\n"
"ds_read_b128 a[140:143], v4 offset:11264\n"
"v_lshlrev_b32 v12, 16, v22\n"
"v_mfma_f32_16x16x32_bf16 v[220:223], a[80:83], a[136:139], v[220:223]\n"
"ds_read_b128 a[136:139], v4 offset:10240\n"
"v_and_b32 v13, s63, v22\n"
"v_mfma_f32_16x16x32_bf16 v[224:227], a[80:83], a[144:147], v[224:227]\n"
"v_lshlrev_b32 v14, 16, v23\n"
"v_and_b32 v15, s63, v23\n"
"v_mfma_f32_16x16x32_bf16 v[224:227], a[84:87], a[148:151], v[224:227]\n"
"global_load_dwordx4 a[76:79], v0, s[34:35] offset:3072 sc1\n"
"v_mul_f32 v12, v12, v124\n"
"v_mfma_f32_16x16x32_bf16 v[252:255], a[92:95], a[148:151], v[252:255]\n"
"ds_read_b128 a[148:151], v4 offset:13312\n"
"v_mul_f32 v13, v13, v125\n"
"v_mfma_f32_16x16x32_bf16 v[252:255], a[88:91], a[144:147], v[252:255]\n"
"ds_read_b128 a[144:147], v4 offset:12288\n"
"v_mul_f32 v14, v14, v126\n"
"v_mfma_f32_16x16x32_bf16 a[28:31], a[88:91], a[152:155], a[28:31]\n"
"global_load_dwordx4 v[60:63], v0, s[96:97] offset:1024 sc1\n"
"v_mul_f32 v15, v15, v127\n"
"v_mfma_f32_16x16x32_bf16 a[28:31], a[92:95], a[156:159], a[28:31]\n"
"v_cvt_pk_bf16_f32 v30, v12, v13\n"
"v_cvt_pk_bf16_f32 v31, v14, v15\n"
"v_mfma_f32_16x16x32_bf16 a[24:27], a[84:87], a[156:159], a[24:27]\n"
"ds_read_b128 a[156:159], v4 offset:15360\n"
"v_mfma_f32_16x16x32_bf16 a[24:27], a[80:83], a[152:155], a[24:27]\n"
"s_waitcnt vmcnt(16) lgkmcnt(13)\n"
"ds_read_b128 a[152:155], v4 offset:14336\n"
"v_mfma_f32_16x16x32_bf16 v[144:147], a[32:35], a[96:99], v[144:147]\n"
"s_add_u32 s34, s36, 1503232\n"
"ds_write_b64 v8, v[30:31] offset:36872\n"
"v_mfma_f32_16x16x32_bf16 v[144:147], a[36:39], a[100:103], v[144:147]\n"
"global_load_dwordx4 a[80:83], v0, s[34:35] offset:0 sc1\n"
"ds_read_b128 v[40:43], v2 offset:36864\n"
"v_mfma_f32_16x16x32_bf16 v[172:175], a[44:47], a[100:103], v[172:175]\n"
"s_waitcnt lgkmcnt(13)\n"
"ds_read_b128 v[68:71], v2 offset:37888\n"
"v_lshlrev_b32 v12, 16, v16\n"
"v_mfma_f32_16x16x32_bf16 v[172:175], a[40:43], a[96:99], v[172:175]\n"
"v_and_b32 v13, s63, v16\n"
"v_lshlrev_b32 v14, 16, v17\n"
"v_mfma_f32_16x16x32_bf16 v[176:179], a[40:43], a[104:107], v[176:179]\n"
"v_and_b32 v15, s63, v17\n"
"v_xor_b32 v20, 64, v7\n"
"v_mfma_f32_16x16x32_bf16 v[176:179], a[44:47], a[108:111], v[176:179]\n"
"ds_read_b128 v[20:23], v20 offset:38912\n"
"v_mul_f32 v12, v12, v44\n"
"v_mfma_f32_16x16x32_bf16 v[148:151], a[36:39], a[108:111], v[148:151]\n"
"v_mul_f32 v13, v13, v45\n"
"v_mul_f32 v14, v14, v46\n"
"v_mfma_f32_16x16x32_bf16 v[148:151], a[32:35], a[104:107], v[148:151]\n"
"s_waitcnt lgkmcnt(13)\n"
"global_load_dwordx4 v[96:99], v0, s[96:97] offset:2048 sc1\n"
"v_mul_f32 v15, v15, v47\n"
"v_mfma_f32_16x16x32_bf16 v[152:155], a[32:35], a[112:115], v[152:155]\n"
"v_cvt_pk_bf16_f32 v24, v12, v13\n"
"v_cvt_pk_bf16_f32 v25, v14, v15\n"
"v_mfma_f32_16x16x32_bf16 v[152:155], a[36:39], a[116:119], v[152:155]\n"
"global_load_dwordx4 a[84:87], v0, s[34:35] offset:1024 sc1\n"
"v_mfma_f32_16x16x32_bf16 v[180:183], a[44:47], a[116:119], v[180:183]\n"
"ds_write_b64 v7, v[24:25] offset:38912\n"
"v_mfma_f32_16x16x32_bf16 v[180:183], a[40:43], a[112:115], v[180:183]\n"
"v_lshlrev_b32 v12, 16, v18\n"
"v_and_b32 v13, s63, v18\n"
"v_mfma_f32_16x16x32_bf16 v[184:187], a[40:43], a[120:123], v[184:187]\n"
"v_lshlrev_b32 v14, 16, v19\n"
"v_and_b32 v15, s63, v19\n"
"v_mfma_f32_16x16x32_bf16 v[184:187], a[44:47], a[124:127], v[184:187]\n"
"v_mul_f32 v12, v12, v72\n"
"v_mul_f32 v13, v13, v73\n"
"v_mfma_f32_16x16x32_bf16 v[156:159], a[36:39], a[124:127], v[156:159]\n"
"v_mul_f32 v14, v14, v74\n"
"v_mul_f32 v15, v15, v75\n"
"v_mfma_f32_16x16x32_bf16 v[156:159], a[32:35], a[120:123], v[156:159]\n"
"s_waitcnt lgkmcnt(9)\n"
"v_cvt_pk_bf16_f32 v26, v12, v13\n"
"v_cvt_pk_bf16_f32 v27, v14, v15\n"
"v_mfma_f32_16x16x32_bf16 v[160:163], a[32:35], a[128:131], v[160:163]\n"
"buffer_store_dwordx4 v[64:67], v6, s[52:55], s83 offen offset:3072 sc1\n"
"ds_write_b64 v7, v[26:27] offset:38920\n"
"v_mfma_f32_16x16x32_bf16 v[160:163], a[36:39], a[132:135], v[160:163]\n"
"s_waitcnt lgkmcnt(2)\n"
"global_load_dwordx4 a[88:91], v0, s[34:35] offset:2048 sc1\n"
"v_lshlrev_b32 v12, 16, v20\n"
"v_mfma_f32_16x16x32_bf16 v[188:191], a[44:47], a[132:135], v[188:191]\n"
"global_load_dwordx4 v[124:127], v0, s[96:97] offset:3072 sc1\n"
"v_and_b32 v13, s63, v20\n"
"v_mfma_f32_16x16x32_bf16 v[188:191], a[40:43], a[128:131], v[188:191]\n"
"v_lshlrev_b32 v14, 16, v21\n"
"v_and_b32 v15, s63, v21\n"
"v_mfma_f32_16x16x32_bf16 v[192:195], a[40:43], a[136:139], v[192:195]\n"
"v_mul_f32 v12, v12, v100\n"
"v_mul_f32 v13, v13, v101\n"
"v_mfma_f32_16x16x32_bf16 v[192:195], a[44:47], a[140:143], v[192:195]\n"
"v_mul_f32 v14, v14, v102\n"
"v_mul_f32 v15, v15, v103\n"
"v_mfma_f32_16x16x32_bf16 v[164:167], a[36:39], a[140:143], v[164:167]\n"
"v_cvt_pk_bf16_f32 v28, v12, v13\n"
"v_cvt_pk_bf16_f32 v29, v14, v15\n"
"v_mfma_f32_16x16x32_bf16 v[164:167], a[32:35], a[136:139], v[164:167]\n"
"ds_write_b64 v8, v[28:29] offset:38912\n"
"v_mfma_f32_16x16x32_bf16 v[168:171], a[32:35], a[144:147], v[168:171]\n"
"v_lshlrev_b32 v12, 16, v22\n"
"v_mfma_f32_16x16x32_bf16 v[168:171], a[36:39], a[148:151], v[168:171]\n"
"global_load_dwordx4 a[92:95], v0, s[34:35] offset:3072 sc1\n"
"v_and_b32 v13, s63, v22\n"
"v_mfma_f32_16x16x32_bf16 v[196:199], a[44:47], a[148:151], v[196:199]\n"
"v_lshlrev_b32 v14, 16, v23\n"
"v_and_b32 v15, s63, v23\n"
"v_mfma_f32_16x16x32_bf16 v[196:199], a[40:43], a[144:147], v[196:199]\n"
"v_mul_f32 v12, v12, v128\n"
"v_mfma_f32_16x16x32_bf16 a[20:23], a[40:43], a[152:155], a[20:23]\n"
"v_mul_f32 v13, v13, v129\n"
"v_mfma_f32_16x16x32_bf16 a[20:23], a[44:47], a[156:159], a[20:23]\n"
"v_mul_f32 v14, v14, v130\n"
"v_mul_f32 v15, v15, v131\n"
"v_mfma_f32_16x16x32_bf16 a[16:19], a[36:39], a[156:159], a[16:19]\n"
"v_cvt_pk_bf16_f32 v30, v12, v13\n"
"v_cvt_pk_bf16_f32 v31, v14, v15\n"
"v_mfma_f32_16x16x32_bf16 a[16:19], a[32:35], a[152:155], a[16:19]\n"
"s_waitcnt vmcnt(16)\n"
"ds_write_b64 v8, v[30:31] offset:38920\n"
"v_mfma_f32_16x16x32_bf16 v[200:203], a[48:51], a[96:99], v[200:203]\n"
"s_add_u32 s34, s36, 1540096\n"
"ds_read_b128 v[44:47], v2 offset:38912\n"
"v_mfma_f32_16x16x32_bf16 v[200:203], a[52:55], a[100:103], v[200:203]\n"
"global_load_dwordx4 a[32:35], v0, s[34:35] offset:0 sc1\n"
"ds_read_b128 v[72:75], v2 offset:39936\n"
"v_mfma_f32_16x16x32_bf16 v[228:231], a[60:63], a[100:103], v[228:231]\n"
"ds_read_b128 a[100:103], v5 offset:1024\n"
"v_accvgpr_read_b32 v16, a160\n"
"v_mfma_f32_16x16x32_bf16 v[228:231], a[56:59], a[96:99], v[228:231]\n"
"ds_read_b128 a[96:99], v5 offset:0\n"
"v_accvgpr_read_b32 v17, a161\n"
"v_mfma_f32_16x16x32_bf16 v[232:235], a[56:59], a[104:107], v[232:235]\n"
"v_accvgpr_read_b32 v18, a162\n"
"v_accvgpr_read_b32 v19, a163\n"
"v_mfma_f32_16x16x32_bf16 v[232:235], a[60:63], a[108:111], v[232:235]\n"
"v_lshlrev_b32 v12, 16, v16\n"
"v_mfma_f32_16x16x32_bf16 v[204:207], a[52:55], a[108:111], v[204:207]\n"
"ds_read_b128 a[108:111], v5 offset:3072\n"
"v_and_b32 v13, s63, v16\n"
"v_mfma_f32_16x16x32_bf16 v[204:207], a[48:51], a[104:107], v[204:207]\n"
"ds_read_b128 a[104:107], v5 offset:2048\n"
"v_lshlrev_b32 v14, 16, v17\n"
"v_mfma_f32_16x16x32_bf16 v[208:211], a[48:51], a[112:115], v[208:211]\n"
"v_and_b32 v15, s63, v17\n"
"v_mul_f32 v12, v12, v48\n"
"v_mfma_f32_16x16x32_bf16 v[208:211], a[52:55], a[116:119], v[208:211]\n"
"global_load_dwordx4 a[36:39], v0, s[34:35] offset:1024 sc1\n"
"v_mul_f32 v13, v13, v49\n"
"v_mfma_f32_16x16x32_bf16 v[236:239], a[60:63], a[116:119], v[236:239]\n"
"ds_read_b128 a[116:119], v5 offset:5120\n"
"v_mul_f32 v14, v14, v50\n"
"v_mfma_f32_16x16x32_bf16 v[236:239], a[56:59], a[112:115], v[236:239]\n"
"ds_read_b128 a[112:115], v5 offset:4096\n"
"v_mul_f32 v15, v15, v51\n"
"v_mfma_f32_16x16x32_bf16 v[240:243], a[56:59], a[120:123], v[240:243]\n"
"v_cvt_pk_bf16_f32 v24, v12, v13\n"
"v_cvt_pk_bf16_f32 v25, v14, v15\n"
"v_mfma_f32_16x16x32_bf16 v[240:243], a[60:63], a[124:127], v[240:243]\n"
"buffer_store_dwordx4 v[40:43], v6, s[52:55], s84 offen offset:0 sc1\n"
"ds_write_b64 v7, v[24:25] offset:32768\n"
"v_mfma_f32_16x16x32_bf16 v[212:215], a[52:55], a[124:127], v[212:215]\n"
"ds_read_b128 a[124:127], v5 offset:7168\n"
"v_lshlrev_b32 v12, 16, v18\n"
"v_mfma_f32_16x16x32_bf16 v[212:215], a[48:51], a[120:123], v[212:215]\n"
"ds_read_b128 a[120:123], v5 offset:6144\n"
"v_and_b32 v13, s63, v18\n"
"v_mfma_f32_16x16x32_bf16 v[216:219], a[48:51], a[128:131], v[216:219]\n"
"v_lshlrev_b32 v14, 16, v19\n"
"v_and_b32 v15, s63, v19\n"
"v_mfma_f32_16x16x32_bf16 v[216:219], a[52:55], a[132:135], v[216:219]\n"
"global_load_dwordx4 a[40:43], v0, s[34:35] offset:2048 sc1\n"
"v_mul_f32 v12, v12, v76\n"
"v_mfma_f32_16x16x32_bf16 v[244:247], a[60:63], a[132:135], v[244:247]\n"
"s_add_u32 s96, s36, 1560576\n"
"ds_read_b128 a[132:135], v5 offset:9216\n"
"v_mul_f32 v13, v13, v77\n"
"v_mfma_f32_16x16x32_bf16 v[244:247], a[56:59], a[128:131], v[244:247]\n"
"ds_read_b128 a[128:131], v5 offset:8192\n"
"v_mul_f32 v14, v14, v78\n"
"v_mfma_f32_16x16x32_bf16 v[248:251], a[56:59], a[136:139], v[248:251]\n"
"global_load_dwordx4 v[100:103], v0, s[96:97] offset:-4096 sc1\n"
"v_mul_f32 v15, v15, v79\n"
"v_mfma_f32_16x16x32_bf16 v[248:251], a[60:63], a[140:143], v[248:251]\n"
"v_cvt_pk_bf16_f32 v26, v12, v13\n"
"v_cvt_pk_bf16_f32 v27, v14, v15\n"
"v_mfma_f32_16x16x32_bf16 v[220:223], a[52:55], a[140:143], v[220:223]\n"
"ds_read_b128 a[140:143], v5 offset:11264\n"
"v_mfma_f32_16x16x32_bf16 v[220:223], a[48:51], a[136:139], v[220:223]\n"
"ds_read_b128 a[136:139], v5 offset:10240\n"
"v_mfma_f32_16x16x32_bf16 v[224:227], a[48:51], a[144:147], v[224:227]\n"
"ds_write_b64 v7, v[26:27] offset:32776\n"
"v_mfma_f32_16x16x32_bf16 v[224:227], a[52:55], a[148:151], v[224:227]\n"
"global_load_dwordx4 a[44:47], v0, s[34:35] offset:3072 sc1\n"
"v_accvgpr_read_b32 v16, a164\n"
"v_mfma_f32_16x16x32_bf16 v[252:255], a[60:63], a[148:151], v[252:255]\n"
"ds_read_b128 a[148:151], v5 offset:13312\n"
"v_accvgpr_read_b32 v17, a165\n"
"v_mfma_f32_16x16x32_bf16 v[252:255], a[56:59], a[144:147], v[252:255]\n"
"ds_read_b128 a[144:147], v5 offset:12288\n"
"v_accvgpr_read_b32 v18, a166\n"
"v_mfma_f32_16x16x32_bf16 a[28:31], a[56:59], a[152:155], a[28:31]\n"
"v_accvgpr_read_b32 v19, a167\n"
"v_lshlrev_b32 v12, 16, v16\n"
"v_mfma_f32_16x16x32_bf16 a[28:31], a[60:63], a[156:159], a[28:31]\n"
"global_load_dwordx4 v[128:131], v0, s[96:97] offset:-3072 sc1\n"
"v_and_b32 v13, s63, v16\n"
"v_mfma_f32_16x16x32_bf16 a[24:27], a[52:55], a[156:159], a[24:27]\n"
"ds_read_b128 a[156:159], v5 offset:15360\n"
"v_lshlrev_b32 v14, 16, v17\n"
"v_mfma_f32_16x16x32_bf16 a[24:27], a[48:51], a[152:155], a[24:27]\n"
"s_waitcnt vmcnt(15) lgkmcnt(13)\n"
"ds_read_b128 a[152:155], v5 offset:14336\n"
"v_and_b32 v15, s63, v17\n"
"v_mfma_f32_16x16x32_bf16 v[144:147], a[64:67], a[96:99], v[144:147]\n"
"s_add_u32 s34, s36, 1544192\n"
"v_mul_f32 v12, v12, v104\n"
"v_mul_f32 v13, v13, v105\n"
"v_mfma_f32_16x16x32_bf16 v[144:147], a[68:71], a[100:103], v[144:147]\n"
"global_load_dwordx4 a[48:51], v0, s[34:35] offset:0 sc1\n"
"v_mul_f32 v14, v14, v106\n"
"v_mfma_f32_16x16x32_bf16 v[172:175], a[76:79], a[100:103], v[172:175]\n"
"v_mul_f32 v15, v15, v107\n"
"v_cvt_pk_bf16_f32 v28, v12, v13\n"
"v_mfma_f32_16x16x32_bf16 v[172:175], a[72:75], a[96:99], v[172:175]\n"
"v_cvt_pk_bf16_f32 v29, v14, v15\n"
"v_mfma_f32_16x16x32_bf16 v[176:179], a[72:75], a[104:107], v[176:179]\n"
"ds_write_b64 v8, v[28:29] offset:32768\n"
"v_mfma_f32_16x16x32_bf16 v[176:179], a[76:79], a[108:111], v[176:179]\n"
"v_lshlrev_b32 v12, 16, v18\n"
"v_and_b32 v13, s63, v18\n"
"v_mfma_f32_16x16x32_bf16 v[148:151], a[68:71], a[108:111], v[148:151]\n"
"v_lshlrev_b32 v14, 16, v19\n"
"v_and_b32 v15, s63, v19\n"
"v_mfma_f32_16x16x32_bf16 v[148:151], a[64:67], a[104:107], v[148:151]\n"
"s_waitcnt lgkmcnt(10)\n"
"v_mul_f32 v12, v12, v132\n"
"v_mul_f32 v13, v13, v133\n"
"v_mfma_f32_16x16x32_bf16 v[152:155], a[64:67], a[112:115], v[152:155]\n"
"buffer_store_dwordx4 v[68:71], v6, s[52:55], s85 offen offset:1024 sc1\n"
"global_load_dwordx4 v[36:39], v0, s[96:97] offset:-2048 sc1\n"
"v_mul_f32 v14, v14, v134\n"
"v_mul_f32 v15, v15, v135\n"
"v_mfma_f32_16x16x32_bf16 v[152:155], a[68:71], a[116:119], v[152:155]\n"
"global_load_dwordx4 a[52:55], v0, s[34:35] offset:1024 sc1\n"
"v_cvt_pk_bf16_f32 v30, v12, v13\n"
"v_mfma_f32_16x16x32_bf16 v[180:183], a[76:79], a[116:119], v[180:183]\n"
"v_cvt_pk_bf16_f32 v31, v14, v15\n"
"v_mfma_f32_16x16x32_bf16 v[180:183], a[72:75], a[112:115], v[180:183]\n"
"ds_write_b64 v8, v[30:31] offset:32776\n"
"v_mfma_f32_16x16x32_bf16 v[184:187], a[72:75], a[120:123], v[184:187]\n"
"ds_read_b128 v[48:51], v2 offset:32768\n"
"v_mfma_f32_16x16x32_bf16 v[184:187], a[76:79], a[124:127], v[184:187]\n"
"ds_read_b128 v[76:79], v2 offset:33792\n"
"v_accvgpr_read_b32 v16, a168\n"
"v_mfma_f32_16x16x32_bf16 v[156:159], a[68:71], a[124:127], v[156:159]\n"
"v_accvgpr_read_b32 v17, a169\n"
"v_accvgpr_read_b32 v18, a170\n"
"v_mfma_f32_16x16x32_bf16 v[156:159], a[64:67], a[120:123], v[156:159]\n"
"s_waitcnt lgkmcnt(9)\n"
"v_accvgpr_read_b32 v19, a171\n"
"v_lshlrev_b32 v12, 16, v16\n"
"v_mfma_f32_16x16x32_bf16 v[160:163], a[64:67], a[128:131], v[160:163]\n"
"v_and_b32 v13, s63, v16\n"
"v_lshlrev_b32 v14, 16, v17\n"
"v_mfma_f32_16x16x32_bf16 v[160:163], a[68:71], a[132:135], v[160:163]\n"
"global_load_dwordx4 a[56:59], v0, s[34:35] offset:2048 sc1\n"
"v_and_b32 v15, s63, v17\n"
"v_mfma_f32_16x16x32_bf16 v[188:191], a[76:79], a[132:135], v[188:191]\n"
"global_load_dwordx4 v[64:67], v0, s[96:97] offset:-1024 sc1\n"
"v_mul_f32 v12, v12, v52\n"
"v_mfma_f32_16x16x32_bf16 v[188:191], a[72:75], a[128:131], v[188:191]\n"
"v_mul_f32 v13, v13, v53\n"
"v_mul_f32 v14, v14, v54\n"
"v_mfma_f32_16x16x32_bf16 v[192:195], a[72:75], a[136:139], v[192:195]\n"
"v_mul_f32 v15, v15, v55\n"
"v_mfma_f32_16x16x32_bf16 v[192:195], a[76:79], a[140:143], v[192:195]\n"
"v_cvt_pk_bf16_f32 v24, v12, v13\n"
"v_cvt_pk_bf16_f32 v25, v14, v15\n"
"v_mfma_f32_16x16x32_bf16 v[164:167], a[68:71], a[140:143], v[164:167]\n"
"ds_write_b64 v7, v[24:25] offset:34816\n"
"v_mfma_f32_16x16x32_bf16 v[164:167], a[64:67], a[136:139], v[164:167]\n"
"s_waitcnt lgkmcnt(5)\n"
"v_lshlrev_b32 v12, 16, v18\n"
"v_and_b32 v13, s63, v18\n"
"v_mfma_f32_16x16x32_bf16 v[168:171], a[64:67], a[144:147], v[168:171]\n"
"v_lshlrev_b32 v14, 16, v19\n"
"v_and_b32 v15, s63, v19\n"
"v_mfma_f32_16x16x32_bf16 v[168:171], a[68:71], a[148:151], v[168:171]\n"
"global_load_dwordx4 a[60:63], v0, s[34:35] offset:3072 sc1\n"
"v_mul_f32 v12, v12, v80\n"
"v_mfma_f32_16x16x32_bf16 v[196:199], a[76:79], a[148:151], v[196:199]\n"
"v_mul_f32 v13, v13, v81\n"
"v_mul_f32 v14, v14, v82\n"
"v_mfma_f32_16x16x32_bf16 v[196:199], a[72:75], a[144:147], v[196:199]\n"
"v_mul_f32 v15, v15, v83\n"
"v_cvt_pk_bf16_f32 v26, v12, v13\n"
"v_mfma_f32_16x16x32_bf16 a[20:23], a[72:75], a[152:155], a[20:23]\n"
"global_load_dwordx4 v[104:107], v0, s[96:97] offset:0 sc1\n"
"v_cvt_pk_bf16_f32 v27, v14, v15\n"
"v_mfma_f32_16x16x32_bf16 a[20:23], a[76:79], a[156:159], a[20:23]\n"
"ds_write_b64 v7, v[26:27] offset:34824\n"
"v_mfma_f32_16x16x32_bf16 a[16:19], a[68:71], a[156:159], a[16:19]\n"
"v_accvgpr_read_b32 v16, a172\n"
"v_mfma_f32_16x16x32_bf16 a[16:19], a[64:67], a[152:155], a[16:19]\n"
"s_waitcnt vmcnt(15)\n"
"v_accvgpr_read_b32 v17, a173\n"
"v_accvgpr_read_b32 v18, a174\n"
"v_mfma_f32_16x16x32_bf16 v[200:203], a[80:83], a[96:99], v[200:203]\n"
"s_add_u32 s34, s36, 1564672\n"
"v_accvgpr_read_b32 v19, a175\n"
"v_lshlrev_b32 v12, 16, v16\n"
"v_mfma_f32_16x16x32_bf16 v[200:203], a[84:87], a[100:103], v[200:203]\n"
"global_load_dwordx4 a[64:67], v0, s[34:35] offset:0 sc1\n"
"v_and_b32 v13, s63, v16\n"
"v_mfma_f32_16x16x32_bf16 v[228:231], a[92:95], a[100:103], v[228:231]\n"
"ds_read_b128 a[100:103], v2 offset:17408\n"
"v_lshlrev_b32 v14, 16, v17\n"
"v_mfma_f32_16x16x32_bf16 v[228:231], a[88:91], a[96:99], v[228:231]\n"
"ds_read_b128 a[96:99], v2 offset:16384\n"
"v_and_b32 v15, s63, v17\n"
"v_mfma_f32_16x16x32_bf16 v[232:235], a[88:91], a[104:107], v[232:235]\n"
"v_mul_f32 v12, v12, v108\n"
"v_mul_f32 v13, v13, v109\n"
"v_mfma_f32_16x16x32_bf16 v[232:235], a[92:95], a[108:111], v[232:235]\n"
"buffer_store_dwordx4 v[44:47], v6, s[52:55], s86 offen offset:2048 sc1\n"
"global_load_dwordx4 v[132:135], v0, s[96:97] offset:1024 sc1\n"
"v_mul_f32 v14, v14, v110\n"
"v_mul_f32 v15, v15, v111\n"
"v_mfma_f32_16x16x32_bf16 v[204:207], a[84:87], a[108:111], v[204:207]\n"
"ds_read_b128 a[108:111], v2 offset:19456\n"
"v_cvt_pk_bf16_f32 v28, v12, v13\n"
"v_mfma_f32_16x16x32_bf16 v[204:207], a[80:83], a[104:107], v[204:207]\n"
"ds_read_b128 a[104:107], v2 offset:18432\n"
"v_cvt_pk_bf16_f32 v29, v14, v15\n"
"v_mfma_f32_16x16x32_bf16 v[208:211], a[80:83], a[112:115], v[208:211]\n"
"ds_write_b64 v8, v[28:29] offset:34816\n"
"v_mfma_f32_16x16x32_bf16 v[208:211], a[84:87], a[116:119], v[208:211]\n"
"global_load_dwordx4 a[68:71], v0, s[34:35] offset:1024 sc1\n"
"v_mfma_f32_16x16x32_bf16 v[236:239], a[92:95], a[116:119], v[236:239]\n"
"ds_read_b128 a[116:119], v2 offset:21504\n"
"v_mfma_f32_16x16x32_bf16 v[236:239], a[88:91], a[112:115], v[236:239]\n"
"ds_read_b128 a[112:115], v2 offset:20480\n"
"v_lshlrev_b32 v12, 16, v18\n"
"v_mfma_f32_16x16x32_bf16 v[240:243], a[88:91], a[120:123], v[240:243]\n"
"v_and_b32 v13, s63, v18\n"
"v_mfma_f32_16x16x32_bf16 v[240:243], a[92:95], a[124:127], v[240:243]\n"
"v_lshlrev_b32 v14, 16, v19\n"
"v_and_b32 v15, s63, v19\n"
"v_mfma_f32_16x16x32_bf16 v[212:215], a[84:87], a[124:127], v[212:215]\n"
"ds_read_b128 a[124:127], v2 offset:23552\n"
"v_mul_f32 v12, v12, v136\n"
"v_mfma_f32_16x16x32_bf16 v[212:215], a[80:83], a[120:123], v[212:215]\n"
"ds_read_b128 a[120:123], v2 offset:22528\n"
"v_mul_f32 v13, v13, v137\n"
"v_mfma_f32_16x16x32_bf16 v[216:219], a[80:83], a[128:131], v[216:219]\n"
"v_mul_f32 v14, v14, v138\n"
"v_mul_f32 v15, v15, v139\n"
"v_mfma_f32_16x16x32_bf16 v[216:219], a[84:87], a[132:135], v[216:219]\n"
"global_load_dwordx4 a[72:75], v0, s[34:35] offset:2048 sc1\n"
"v_cvt_pk_bf16_f32 v30, v12, v13\n"
"v_mfma_f32_16x16x32_bf16 v[244:247], a[92:95], a[132:135], v[244:247]\n"
"ds_read_b128 a[132:135], v2 offset:25600\n"
"v_cvt_pk_bf16_f32 v31, v14, v15\n"
"v_mfma_f32_16x16x32_bf16 v[244:247], a[88:91], a[128:131], v[244:247]\n"
"ds_read_b128 a[128:131], v2 offset:24576\n"
"v_mfma_f32_16x16x32_bf16 v[248:251], a[88:91], a[136:139], v[248:251]\n"
"ds_write_b64 v8, v[30:31] offset:34824\n"
"v_mfma_f32_16x16x32_bf16 v[248:251], a[92:95], a[140:143], v[248:251]\n"
"ds_read_b128 v[52:55], v2 offset:34816\n"
"v_mfma_f32_16x16x32_bf16 v[220:223], a[84:87], a[140:143], v[220:223]\n"
"ds_read_b128 a[140:143], v2 offset:27648\n"
"v_mfma_f32_16x16x32_bf16 v[220:223], a[80:83], a[136:139], v[220:223]\n"
"ds_read_b128 a[136:139], v2 offset:26624\n"
"v_mfma_f32_16x16x32_bf16 v[224:227], a[80:83], a[144:147], v[224:227]\n"
"ds_read_b128 v[80:83], v2 offset:35840\n"
"v_accvgpr_read_b32 v16, a176\n"
"v_mfma_f32_16x16x32_bf16 v[224:227], a[84:87], a[148:151], v[224:227]\n"
"global_load_dwordx4 a[76:79], v0, s[34:35] offset:3072 sc1\n"
"v_accvgpr_read_b32 v17, a177\n"
"v_mfma_f32_16x16x32_bf16 v[252:255], a[92:95], a[148:151], v[252:255]\n"
"ds_read_b128 a[148:151], v2 offset:29696\n"
"v_accvgpr_read_b32 v18, a178\n"
"v_mfma_f32_16x16x32_bf16 v[252:255], a[88:91], a[144:147], v[252:255]\n"
"ds_read_b128 a[144:147], v2 offset:28672\n"
"v_accvgpr_read_b32 v19, a179\n"
"v_mfma_f32_16x16x32_bf16 a[28:31], a[88:91], a[152:155], a[28:31]\n"
"v_lshlrev_b32 v12, 16, v16\n"
"v_and_b32 v13, s63, v16\n"
"v_mfma_f32_16x16x32_bf16 a[28:31], a[92:95], a[156:159], a[28:31]\n"
"v_lshlrev_b32 v14, 16, v17\n"
"v_and_b32 v15, s63, v17\n"
"v_mfma_f32_16x16x32_bf16 a[24:27], a[84:87], a[156:159], a[24:27]\n"
"ds_read_b128 a[156:159], v2 offset:31744\n"
"v_mul_f32 v12, v12, v56\n"
"v_mfma_f32_16x16x32_bf16 a[24:27], a[80:83], a[152:155], a[24:27]\n"
"s_waitcnt vmcnt(15) lgkmcnt(15)\n"
"ds_read_b128 a[152:155], v2 offset:30720\n"
"v_mul_f32 v13, v13, v57\n"
"v_mfma_f32_16x16x32_bf16 v[144:147], a[32:35], a[96:99], v[144:147]\n"
"buffer_store_dwordx4 v[72:75], v6, s[52:55], s87 offen offset:3072 sc1\n"
"s_add_u32 s34, s36, 1568768\n"
"v_mul_f32 v14, v14, v58\n"
"v_mul_f32 v15, v15, v59\n"
"v_mfma_f32_16x16x32_bf16 v[144:147], a[36:39], a[100:103], v[144:147]\n"
"global_load_dwordx4 a[80:83], v0, s[34:35] offset:0 sc1\n"
"v_cvt_pk_bf16_f32 v24, v12, v13\n"
"v_mfma_f32_16x16x32_bf16 v[172:175], a[44:47], a[100:103], v[172:175]\n"
"v_cvt_pk_bf16_f32 v25, v14, v15\n"
"v_mfma_f32_16x16x32_bf16 v[172:175], a[40:43], a[96:99], v[172:175]\n"
"ds_write_b64 v7, v[24:25] offset:36864\n"
"v_mfma_f32_16x16x32_bf16 v[176:179], a[40:43], a[104:107], v[176:179]\n"
"global_load_dwordx4 a[84:87], v0, s[34:35] offset:1024 sc1\n"
"v_lshlrev_b32 v12, 16, v18\n"
"v_mfma_f32_16x16x32_bf16 v[176:179], a[44:47], a[108:111], v[176:179]\n"
"global_load_dwordx4 v[108:111], v0, s[96:97] offset:2048 sc1\n"
"v_and_b32 v13, s63, v18\n"
"v_mfma_f32_16x16x32_bf16 v[148:151], a[36:39], a[108:111], v[148:151]\n"
"v_lshlrev_b32 v14, 16, v19\n"
"v_and_b32 v15, s63, v19\n"
"v_mfma_f32_16x16x32_bf16 v[148:151], a[32:35], a[104:107], v[148:151]\n"
"s_waitcnt lgkmcnt(12)\n"
"global_load_dwordx4 a[88:91], v0, s[34:35] offset:2048 sc1\n"
"v_mul_f32 v12, v12, v84\n"
"v_mfma_f32_16x16x32_bf16 v[152:155], a[32:35], a[112:115], v[152:155]\n"
"v_mul_f32 v13, v13, v85\n"
"v_mul_f32 v14, v14, v86\n"
"v_mfma_f32_16x16x32_bf16 v[152:155], a[36:39], a[116:119], v[152:155]\n"
"v_mul_f32 v15, v15, v87\n"
"v_cvt_pk_bf16_f32 v26, v12, v13\n"
"v_mfma_f32_16x16x32_bf16 v[180:183], a[44:47], a[116:119], v[180:183]\n"
"global_load_dwordx4 a[92:95], v0, s[34:35] offset:3072 sc1\n"
"v_cvt_pk_bf16_f32 v27, v14, v15\n"
"v_mfma_f32_16x16x32_bf16 v[180:183], a[40:43], a[112:115], v[180:183]\n"
"ds_write_b64 v7, v[26:27] offset:36872\n"
"v_mfma_f32_16x16x32_bf16 v[184:187], a[40:43], a[120:123], v[184:187]\n"
"v_accvgpr_read_b32 v16, a180\n"
"v_accvgpr_read_b32 v17, a181\n"
"v_mfma_f32_16x16x32_bf16 v[184:187], a[44:47], a[124:127], v[184:187]\n"
"v_accvgpr_read_b32 v18, a182\n"
"v_accvgpr_read_b32 v19, a183\n"
"v_mfma_f32_16x16x32_bf16 v[156:159], a[36:39], a[124:127], v[156:159]\n"
"global_load_dwordx4 v[136:139], v0, s[96:97] offset:3072 sc1\n"
"v_lshlrev_b32 v12, 16, v16\n"
"v_mfma_f32_16x16x32_bf16 v[156:159], a[32:35], a[120:123], v[156:159]\n"
"s_waitcnt lgkmcnt(7)\n"
"v_and_b32 v13, s63, v16\n"
"v_lshlrev_b32 v14, 16, v17\n"
"v_mfma_f32_16x16x32_bf16 v[160:163], a[32:35], a[128:131], v[160:163]\n"
"v_and_b32 v15, s63, v17\n"
"v_mul_f32 v12, v12, v112\n"
"v_mfma_f32_16x16x32_bf16 v[160:163], a[36:39], a[132:135], v[160:163]\n"
"v_mul_f32 v13, v13, v113\n"
"v_mul_f32 v14, v14, v114\n"
"v_mfma_f32_16x16x32_bf16 v[188:191], a[44:47], a[132:135], v[188:191]\n"
"v_mul_f32 v15, v15, v115\n"
"v_mfma_f32_16x16x32_bf16 v[188:191], a[40:43], a[128:131], v[188:191]\n"
"v_cvt_pk_bf16_f32 v28, v12, v13\n"
"v_cvt_pk_bf16_f32 v29, v14, v15\n"
"v_mfma_f32_16x16x32_bf16 v[192:195], a[40:43], a[136:139], v[192:195]\n"
"ds_write_b64 v8, v[28:29] offset:36864\n"
"v_mfma_f32_16x16x32_bf16 v[192:195], a[44:47], a[140:143], v[192:195]\n"
"v_lshlrev_b32 v12, 16, v18\n"
"v_mfma_f32_16x16x32_bf16 v[164:167], a[36:39], a[140:143], v[164:167]\n"
"v_and_b32 v13, s63, v18\n"
"v_lshlrev_b32 v14, 16, v19\n"
"v_mfma_f32_16x16x32_bf16 v[164:167], a[32:35], a[136:139], v[164:167]\n"
"s_waitcnt lgkmcnt(3)\n"
"v_and_b32 v15, s63, v19\n"
"v_mfma_f32_16x16x32_bf16 v[168:171], a[32:35], a[144:147], v[168:171]\n"
"v_mul_f32 v12, v12, v140\n"
"v_mfma_f32_16x16x32_bf16 v[168:171], a[36:39], a[148:151], v[168:171]\n"
"v_mul_f32 v13, v13, v141\n"
"v_mfma_f32_16x16x32_bf16 v[196:199], a[44:47], a[148:151], v[196:199]\n"
"v_mul_f32 v14, v14, v142\n"
"v_mul_f32 v15, v15, v143\n"
"v_mfma_f32_16x16x32_bf16 v[196:199], a[40:43], a[144:147], v[196:199]\n"
"v_cvt_pk_bf16_f32 v30, v12, v13\n"
"v_mfma_f32_16x16x32_bf16 a[20:23], a[40:43], a[152:155], a[20:23]\n"
"v_cvt_pk_bf16_f32 v31, v14, v15\n"
"v_mfma_f32_16x16x32_bf16 a[20:23], a[44:47], a[156:159], a[20:23]\n"
"buffer_store_dwordx4 v[48:51], v6, s[52:55], s88 offen offset:0 sc1\n"
"ds_write_b64 v8, v[30:31] offset:36872\n"
"v_mfma_f32_16x16x32_bf16 a[16:19], a[36:39], a[156:159], a[16:19]\n"
"ds_read_b128 v[56:59], v2 offset:36864\n"
"v_mfma_f32_16x16x32_bf16 a[16:19], a[32:35], a[152:155], a[16:19]\n"
"s_waitcnt vmcnt(15)\n"
"ds_read_b128 v[84:87], v2 offset:37888\n"
"v_accvgpr_read_b32 v16, a184\n"
"v_mfma_f32_16x16x32_bf16 v[200:203], a[48:51], a[96:99], v[200:203]\n"
"v_accvgpr_read_b32 v17, a185\n"
"v_accvgpr_read_b32 v18, a186\n"
"v_mfma_f32_16x16x32_bf16 v[200:203], a[52:55], a[100:103], v[200:203]\n"
"v_accvgpr_read_b32 v19, a187\n"
"v_mfma_f32_16x16x32_bf16 v[228:231], a[60:63], a[100:103], v[228:231]\n"
"ds_read_b128 a[100:103], v3 offset:17408\n"
"v_lshlrev_b32 v12, 16, v16\n"
"v_mfma_f32_16x16x32_bf16 v[228:231], a[56:59], a[96:99], v[228:231]\n"
"ds_read_b128 a[96:99], v3 offset:16384\n"
"v_and_b32 v13, s63, v16\n"
"v_mfma_f32_16x16x32_bf16 v[232:235], a[56:59], a[104:107], v[232:235]\n"
"v_lshlrev_b32 v14, 16, v17\n"
"v_and_b32 v15, s63, v17\n"
"v_mfma_f32_16x16x32_bf16 v[232:235], a[60:63], a[108:111], v[232:235]\n"
"v_accvgpr_read_b32 v10, a0\n"
"v_mfma_f32_16x16x32_bf16 v[204:207], a[52:55], a[108:111], v[204:207]\n"
"ds_read_b128 a[108:111], v4 offset:17408\n"
"v_accvgpr_read_b32 v11, a1\n"
"v_mfma_f32_16x16x32_bf16 v[204:207], a[48:51], a[104:107], v[204:207]\n"
"ds_read_b128 a[104:107], v4 offset:16384\n"
"v_mul_f32 v12, v12, v10\n"
"v_mfma_f32_16x16x32_bf16 v[208:211], a[48:51], a[112:115], v[208:211]\n"
"v_mul_f32 v13, v13, v11\n"
"v_accvgpr_read_b32 v10, a2\n"
"v_mfma_f32_16x16x32_bf16 v[208:211], a[52:55], a[116:119], v[208:211]\n"
"v_accvgpr_read_b32 v11, a3\n"
"v_mul_f32 v14, v14, v10\n"
"v_mfma_f32_16x16x32_bf16 v[236:239], a[60:63], a[116:119], v[236:239]\n"
"ds_read_b128 a[116:119], v5 offset:17408\n"
"v_mul_f32 v15, v15, v11\n"
"v_mfma_f32_16x16x32_bf16 v[236:239], a[56:59], a[112:115], v[236:239]\n"
"ds_read_b128 a[112:115], v5 offset:16384\n"
"v_cvt_pk_bf16_f32 v24, v12, v13\n"
"v_mfma_f32_16x16x32_bf16 v[240:243], a[56:59], a[120:123], v[240:243]\n"
"v_cvt_pk_bf16_f32 v25, v14, v15\n"
"v_mfma_f32_16x16x32_bf16 v[240:243], a[60:63], a[124:127], v[240:243]\n"
"ds_write_b64 v7, v[24:25] offset:38912\n"
"v_mfma_f32_16x16x32_bf16 v[212:215], a[52:55], a[124:127], v[212:215]\n"
"ds_read_b128 a[124:127], v3 offset:19456\n"
"v_lshlrev_b32 v12, 16, v18\n"
"v_mfma_f32_16x16x32_bf16 v[212:215], a[48:51], a[120:123], v[212:215]\n"
"ds_read_b128 a[120:123], v3 offset:18432\n"
"v_and_b32 v13, s63, v18\n"
"v_mfma_f32_16x16x32_bf16 v[216:219], a[48:51], a[128:131], v[216:219]\n"
"v_lshlrev_b32 v14, 16, v19\n"
"v_and_b32 v15, s63, v19\n"
"v_mfma_f32_16x16x32_bf16 v[216:219], a[52:55], a[132:135], v[216:219]\n"
"v_accvgpr_read_b32 v10, a4\n"
"v_accvgpr_read_b32 v11, a5\n"
"v_mfma_f32_16x16x32_bf16 v[244:247], a[60:63], a[132:135], v[244:247]\n"
"ds_read_b128 a[132:135], v4 offset:19456\n"
"v_mul_f32 v12, v12, v10\n"
"v_mfma_f32_16x16x32_bf16 v[244:247], a[56:59], a[128:131], v[244:247]\n"
"ds_read_b128 a[128:131], v4 offset:18432\n"
"v_mul_f32 v13, v13, v11\n"
"v_mfma_f32_16x16x32_bf16 v[248:251], a[56:59], a[136:139], v[248:251]\n"
"v_accvgpr_read_b32 v10, a6\n"
"v_mfma_f32_16x16x32_bf16 v[248:251], a[60:63], a[140:143], v[248:251]\n"
"v_accvgpr_read_b32 v11, a7\n"
"v_mul_f32 v14, v14, v10\n"
"v_mfma_f32_16x16x32_bf16 v[220:223], a[52:55], a[140:143], v[220:223]\n"
"ds_read_b128 a[140:143], v5 offset:19456\n"
"v_mul_f32 v15, v15, v11\n"
"v_mfma_f32_16x16x32_bf16 v[220:223], a[48:51], a[136:139], v[220:223]\n"
"ds_read_b128 a[136:139], v5 offset:18432\n"
"v_cvt_pk_bf16_f32 v26, v12, v13\n"
"v_mfma_f32_16x16x32_bf16 v[224:227], a[48:51], a[144:147], v[224:227]\n"
"v_cvt_pk_bf16_f32 v27, v14, v15\n"
"v_mfma_f32_16x16x32_bf16 v[224:227], a[52:55], a[148:151], v[224:227]\n"
"buffer_store_dwordx4 v[76:79], v6, s[52:55], s89 offen offset:1024 sc1\n"
"ds_write_b64 v7, v[26:27] offset:38920\n"
"v_mfma_f32_16x16x32_bf16 v[252:255], a[60:63], a[148:151], v[252:255]\n"
"ds_read_b128 a[148:151], v3 offset:21504\n"
"v_accvgpr_read_b32 v16, a188\n"
"v_mfma_f32_16x16x32_bf16 v[252:255], a[56:59], a[144:147], v[252:255]\n"
"ds_read_b128 a[144:147], v3 offset:20480\n"
"v_accvgpr_read_b32 v17, a189\n"
"v_mfma_f32_16x16x32_bf16 a[28:31], a[56:59], a[152:155], a[28:31]\n"
"v_accvgpr_read_b32 v18, a190\n"
"v_accvgpr_read_b32 v19, a191\n"
"v_mfma_f32_16x16x32_bf16 a[28:31], a[60:63], a[156:159], a[28:31]\n"
"v_lshlrev_b32 v12, 16, v16\n"
"v_and_b32 v13, s63, v16\n"
"v_mfma_f32_16x16x32_bf16 a[24:27], a[52:55], a[156:159], a[24:27]\n"
"ds_read_b128 a[156:159], v4 offset:21504\n"
"v_lshlrev_b32 v14, 16, v17\n"
"v_mfma_f32_16x16x32_bf16 a[24:27], a[48:51], a[152:155], a[24:27]\n"
"s_waitcnt lgkmcnt(15)\n"
"ds_read_b128 a[152:155], v4 offset:20480\n"
"v_and_b32 v15, s63, v17\n"
"v_mfma_f32_16x16x32_bf16 v[144:147], v[88:91], a[96:99], v[144:147]\n"
"v_accvgpr_read_b32 v10, a8\n"
"v_accvgpr_read_b32 v11, a9\n"
"v_mfma_f32_16x16x32_bf16 v[144:147], v[116:119], a[100:103], v[144:147]\n"
"v_mul_f32 v12, v12, v10\n"
"v_mul_f32 v13, v13, v11\n"
"v_mfma_f32_16x16x32_bf16 v[172:175], v[120:123], a[100:103], v[172:175]\n"
"v_accvgpr_read_b32 v10, a10\n"
"v_accvgpr_read_b32 v11, a11\n"
"v_mfma_f32_16x16x32_bf16 v[172:175], v[92:95], a[96:99], v[172:175]\n"
"buffer_store_dwordx4 v[52:55], v6, s[52:55], s90 offen offset:2048 sc1\n"
"v_mul_f32 v14, v14, v10\n"
"v_mul_f32 v15, v15, v11\n"
"v_cvt_pk_bf16_f32 v28, v12, v13\n"
"v_cvt_pk_bf16_f32 v29, v14, v15\n"
"v_mfma_f32_16x16x32_bf16 v[200:203], v[32:35], a[96:99], v[200:203]\n"
"ds_write_b64 v8, v[28:29] offset:38912\n"
"v_mfma_f32_16x16x32_bf16 v[200:203], v[60:63], a[100:103], v[200:203]\n"
"v_lshlrev_b32 v12, 16, v18\n"
"v_and_b32 v13, s63, v18\n"
"v_mfma_f32_16x16x32_bf16 v[228:231], v[124:127], a[100:103], v[228:231]\n"
"ds_read_b128 a[100:103], v5 offset:21504\n"
"v_lshlrev_b32 v14, 16, v19\n"
"v_mfma_f32_16x16x32_bf16 v[228:231], v[96:99], a[96:99], v[228:231]\n"
"s_waitcnt vmcnt(3) lgkmcnt(15)\n"
"ds_read_b128 a[96:99], v5 offset:20480\n"
"v_and_b32 v15, s63, v19\n"
"v_mfma_f32_16x16x32_bf16 v[228:231], v[108:111], a[104:107], v[228:231]\n"
"v_accvgpr_read_b32 v10, a12\n"
"v_accvgpr_read_b32 v11, a13\n"
"v_mfma_f32_16x16x32_bf16 v[228:231], v[136:139], a[108:111], v[228:231]\n"
"v_mul_f32 v12, v12, v10\n"
"v_mul_f32 v13, v13, v11\n"
"v_mfma_f32_16x16x32_bf16 v[200:203], v[132:135], a[108:111], v[200:203]\n"
"v_accvgpr_read_b32 v10, a14\n"
"v_accvgpr_read_b32 v11, a15\n"
"v_mfma_f32_16x16x32_bf16 v[200:203], v[104:107], a[104:107], v[200:203]\n"
"v_mul_f32 v14, v14, v10\n"
"v_mul_f32 v15, v15, v11\n"
"v_mfma_f32_16x16x32_bf16 v[172:175], v[36:39], a[104:107], v[172:175]\n"
"v_cvt_pk_bf16_f32 v30, v12, v13\n"
"v_cvt_pk_bf16_f32 v31, v14, v15\n"
"v_mfma_f32_16x16x32_bf16 v[172:175], v[64:67], a[108:111], v[172:175]\n"
"buffer_store_dwordx4 v[80:83], v6, s[52:55], s91 offen offset:3072 sc1\n"
"ds_write_b64 v8, v[30:31] offset:38920\n"
"v_mfma_f32_16x16x32_bf16 v[144:147], v[128:131], a[108:111], v[144:147]\n"
"ds_read_b128 a[108:111], v3 offset:23552\n"
"v_mfma_f32_16x16x32_bf16 v[144:147], v[100:103], a[104:107], v[144:147]\n"
"s_waitcnt lgkmcnt(15)\n"
"ds_read_b128 a[104:107], v3 offset:22528\n"
"v_mfma_f32_16x16x32_bf16 v[144:147], a[64:67], a[112:115], v[144:147]\n"
"v_mfma_f32_16x16x32_bf16 v[144:147], a[68:71], a[116:119], v[144:147]\n"
"v_mfma_f32_16x16x32_bf16 v[172:175], a[76:79], a[116:119], v[172:175]\n"
"v_mfma_f32_16x16x32_bf16 v[172:175], a[72:75], a[112:115], v[172:175]\n"
"ds_read_b128 v[112:115], v2 offset:38912\n"
"v_mfma_f32_16x16x32_bf16 v[200:203], a[80:83], a[112:115], v[200:203]\n"
"ds_read_b128 v[140:143], v2 offset:39936\n"
"v_mfma_f32_16x16x32_bf16 v[200:203], a[84:87], a[116:119], v[200:203]\n"
"v_accvgpr_read_b32 v16, a192\n"
"v_accvgpr_read_b32 v17, a193\n"
"v_mfma_f32_16x16x32_bf16 v[228:231], a[92:95], a[116:119], v[228:231]\n"
"ds_read_b128 a[116:119], v4 offset:23552\n"
"v_accvgpr_read_b32 v18, a194\n"
"v_mfma_f32_16x16x32_bf16 v[228:231], a[88:91], a[112:115], v[228:231]\n"
"s_waitcnt lgkmcnt(15)\n"
"buffer_store_dwordx4 v[56:59], v6, s[52:55], s92 offen offset:0 sc1\n"
"ds_read_b128 a[112:115], v4 offset:22528\n"
"v_accvgpr_read_b32 v19, a195\n"
"v_lshlrev_b32 v12, 16, v16\n"
"v_mfma_f32_16x16x32_bf16 v[232:235], v[96:99], a[120:123], v[232:235]\n"
"v_and_b32 v13, s63, v16\n"
"v_mfma_f32_16x16x32_bf16 v[232:235], v[124:127], a[124:127], v[232:235]\n"
"v_lshlrev_b32 v14, 16, v17\n"
"v_and_b32 v15, s63, v17\n"
"v_mfma_f32_16x16x32_bf16 v[204:207], v[60:63], a[124:127], v[204:207]\n"
"v_mul_f32 v12, v12, v144\n"
"v_mul_f32 v13, v13, v145\n"
"v_mfma_f32_16x16x32_bf16 v[204:207], v[32:35], a[120:123], v[204:207]\n"
"v_mul_f32 v14, v14, v146\n"
"v_mul_f32 v15, v15, v147\n"
"v_mfma_f32_16x16x32_bf16 v[176:179], v[92:95], a[120:123], v[176:179]\n"
"v_cvt_pk_bf16_f32 v24, v12, v13\n"
"v_cvt_pk_bf16_f32 v25, v14, v15\n"
"v_mfma_f32_16x16x32_bf16 v[176:179], v[120:123], a[124:127], v[176:179]\n"
"ds_write_b64 v7, v[24:25] offset:32768\n"
"v_mfma_f32_16x16x32_bf16 v[148:151], v[116:119], a[124:127], v[148:151]\n"
"ds_read_b128 a[124:127], v5 offset:23552\n"
"v_lshlrev_b32 v12, 16, v18\n"
"v_mfma_f32_16x16x32_bf16 v[148:151], v[88:91], a[120:123], v[148:151]\n"
"ds_read_b128 a[120:123], v5 offset:22528\n"
"v_and_b32 v13, s63, v18\n"
"v_mfma_f32_16x16x32_bf16 v[148:151], v[100:103], a[128:131], v[148:151]\n"
"v_lshlrev_b32 v14, 16, v19\n"
"v_mfma_f32_16x16x32_bf16 v[148:151], v[128:131], a[132:135], v[148:151]\n"
"buffer_store_dwordx4 v[84:87], v6, s[52:55], s93 offen offset:1024 sc1\n"
"v_and_b32 v15, s63, v19\n"
"v_mul_f32 v12, v12, v172\n"
"v_mul_f32 v13, v13, v173\n"
"v_mfma_f32_16x16x32_bf16 v[176:179], v[64:67], a[132:135], v[176:179]\n"
"v_mul_f32 v14, v14, v174\n"
"v_mul_f32 v15, v15, v175\n"
"v_mfma_f32_16x16x32_bf16 v[176:179], v[36:39], a[128:131], v[176:179]\n"
"v_cvt_pk_bf16_f32 v26, v12, v13\n"
"v_cvt_pk_bf16_f32 v27, v14, v15\n"
"v_mfma_f32_16x16x32_bf16 v[204:207], v[104:107], a[128:131], v[204:207]\n"
"ds_write_b64 v7, v[26:27] offset:32776\n"
"v_mfma_f32_16x16x32_bf16 v[204:207], v[132:135], a[132:135], v[204:207]\n"
"v_accvgpr_read_b32 v16, a196\n"
"v_mfma_f32_16x16x32_bf16 v[232:235], v[136:139], a[132:135], v[232:235]\n"
"ds_read_b128 a[132:135], v3 offset:25600\n"
"v_accvgpr_read_b32 v17, a197\n"
"v_mfma_f32_16x16x32_bf16 v[232:235], v[108:111], a[128:131], v[232:235]\n"
"s_waitcnt lgkmcnt(15)\n"
"ds_read_b128 a[128:131], v3 offset:24576\n"
"v_accvgpr_read_b32 v18, a198\n"
"v_mfma_f32_16x16x32_bf16 v[232:235], a[88:91], a[136:139], v[232:235]\n"
"v_accvgpr_read_b32 v19, a199\n"
"v_lshlrev_b32 v12, 16, v16\n"
"v_mfma_f32_16x16x32_bf16 v[232:235], a[92:95], a[140:143], v[232:235]\n"
"v_and_b32 v13, s63, v16\n"
"v_lshlrev_b32 v14, 16, v17\n"
"v_mfma_f32_16x16x32_bf16 v[204:207], a[84:87], a[140:143], v[204:207]\n"
"v_and_b32 v15, s63, v17\n"
"v_mul_f32 v12, v12, v200\n"
"v_mfma_f32_16x16x32_bf16 v[204:207], a[80:83], a[136:139], v[204:207]\n"
"s_waitcnt lgkmcnt(9)\n"
"buffer_store_dwordx4 v[112:115], v6, s[52:55], s94 offen offset:2048 sc1\n"
"v_mul_f32 v13, v13, v201\n"
"v_mul_f32 v14, v14, v202\n"
"v_mul_f32 v15, v15, v203\n"
"v_mfma_f32_16x16x32_bf16 v[176:179], a[72:75], a[136:139], v[176:179]\n"
"v_cvt_pk_bf16_f32 v28, v12, v13\n"
"v_cvt_pk_bf16_f32 v29, v14, v15\n"
"v_mfma_f32_16x16x32_bf16 v[176:179], a[76:79], a[140:143], v[176:179]\n"
"ds_write_b64 v8, v[28:29] offset:32768\n"
"v_mfma_f32_16x16x32_bf16 v[148:151], a[68:71], a[140:143], v[148:151]\n"
"ds_read_b128 a[140:143], v4 offset:25600\n"
"v_mfma_f32_16x16x32_bf16 v[148:151], a[64:67], a[136:139], v[148:151]\n"
"ds_read_b128 a[136:139], v4 offset:24576\n"
"v_lshlrev_b32 v12, 16, v18\n"
"v_mfma_f32_16x16x32_bf16 v[152:155], v[88:91], a[144:147], v[152:155]\n"
"v_and_b32 v13, s63, v18\n"
"v_lshlrev_b32 v14, 16, v19\n"
"v_mfma_f32_16x16x32_bf16 v[152:155], v[116:119], a[148:151], v[152:155]\n"
"v_and_b32 v15, s63, v19\n"
"v_mul_f32 v12, v12, v228\n"
"v_mfma_f32_16x16x32_bf16 v[180:183], v[120:123], a[148:151], v[180:183]\n"
"v_mul_f32 v13, v13, v229\n"
"v_mfma_f32_16x16x32_bf16 v[180:183], v[92:95], a[144:147], v[180:183]\n"
"v_mul_f32 v14, v14, v230\n"
"v_mul_f32 v15, v15, v231\n"
"v_mfma_f32_16x16x32_bf16 v[208:211], v[32:35], a[144:147], v[208:211]\n"
"v_cvt_pk_bf16_f32 v30, v12, v13\n"
"v_cvt_pk_bf16_f32 v31, v14, v15\n"
"v_mfma_f32_16x16x32_bf16 v[208:211], v[60:63], a[148:151], v[208:211]\n"
"s_waitcnt lgkmcnt(11)\n"
"buffer_store_dwordx4 v[140:143], v6, s[52:55], s95 offen offset:3072 sc1\n"
"ds_write_b64 v8, v[30:31] offset:32776\n"
"v_mfma_f32_16x16x32_bf16 v[236:239], v[124:127], a[148:151], v[236:239]\n"
"ds_read_b128 a[148:151], v5 offset:25600\n"
"v_mfma_f32_16x16x32_bf16 v[236:239], v[96:99], a[144:147], v[236:239]\n"
"ds_read_b128 a[144:147], v5 offset:24576\n"
"v_mfma_f32_16x16x32_bf16 v[236:239], v[108:111], a[152:155], v[236:239]\n"
"ds_read_b128 v[144:147], v2 offset:32768\n"
"v_mfma_f32_16x16x32_bf16 v[236:239], v[136:139], a[156:159], v[236:239]\n"
"ds_read_b128 v[172:175], v2 offset:33792\n"
"v_accvgpr_read_b32 v16, a200\n"
"v_mfma_f32_16x16x32_bf16 v[208:211], v[132:135], a[156:159], v[208:211]\n"
"v_accvgpr_read_b32 v17, a201\n"
"v_accvgpr_read_b32 v18, a202\n"
"v_mfma_f32_16x16x32_bf16 v[208:211], v[104:107], a[152:155], v[208:211]\n"
"v_accvgpr_read_b32 v19, a203\n"
"v_lshlrev_b32 v12, 16, v16\n"
"v_mfma_f32_16x16x32_bf16 v[180:183], v[36:39], a[152:155], v[180:183]\n"
"v_and_b32 v13, s63, v16\n"
"v_lshlrev_b32 v14, 16, v17\n"
"v_mfma_f32_16x16x32_bf16 v[180:183], v[64:67], a[156:159], v[180:183]\n"
"v_and_b32 v15, s63, v17\n"
"v_mul_f32 v12, v12, v148\n"
"v_mfma_f32_16x16x32_bf16 v[152:155], v[128:131], a[156:159], v[152:155]\n"
"ds_read_b128 a[156:159], v3 offset:27648\n"
"v_mul_f32 v13, v13, v149\n"
"v_mfma_f32_16x16x32_bf16 v[152:155], v[100:103], a[152:155], v[152:155]\n"
"ds_read_b128 a[152:155], v3 offset:26624\n"
"v_mul_f32 v14, v14, v150\n"
"v_mfma_f32_16x16x32_bf16 v[152:155], a[64:67], a[96:99], v[152:155]\n"
"v_mul_f32 v15, v15, v151\n"
"v_cvt_pk_bf16_f32 v24, v12, v13\n"
"v_mfma_f32_16x16x32_bf16 v[152:155], a[68:71], a[100:103], v[152:155]\n"
"v_cvt_pk_bf16_f32 v25, v14, v15\n"
"v_mfma_f32_16x16x32_bf16 v[180:183], a[76:79], a[100:103], v[180:183]\n"
"s_waitcnt lgkmcnt(3)\n"
"buffer_store_dwordx4 v[144:147], v6, s[52:55], s80 offen offset:128 sc1\n"
"ds_write_b64 v7, v[24:25] offset:34816\n"
"v_mfma_f32_16x16x32_bf16 v[180:183], a[72:75], a[96:99], v[180:183]\n"
"v_lshlrev_b32 v12, 16, v18\n"
"v_and_b32 v13, s63, v18\n"
"v_mfma_f32_16x16x32_bf16 v[208:211], a[80:83], a[96:99], v[208:211]\n"
"v_lshlrev_b32 v14, 16, v19\n"
"v_and_b32 v15, s63, v19\n"
"v_mfma_f32_16x16x32_bf16 v[208:211], a[84:87], a[100:103], v[208:211]\n"
"v_mul_f32 v12, v12, v176\n"
"v_mul_f32 v13, v13, v177\n"
"v_mfma_f32_16x16x32_bf16 v[236:239], a[92:95], a[100:103], v[236:239]\n"
"ds_read_b128 a[100:103], v4 offset:27648\n"
"v_mul_f32 v14, v14, v178\n"
"v_mfma_f32_16x16x32_bf16 v[236:239], a[88:91], a[96:99], v[236:239]\n"
"ds_read_b128 a[96:99], v4 offset:26624\n"
"v_mul_f32 v15, v15, v179\n"
"v_mfma_f32_16x16x32_bf16 v[240:243], v[96:99], a[104:107], v[240:243]\n"
"v_cvt_pk_bf16_f32 v26, v12, v13\n"
"v_cvt_pk_bf16_f32 v27, v14, v15\n"
"v_mfma_f32_16x16x32_bf16 v[240:243], v[124:127], a[108:111], v[240:243]\n"
"ds_write_b64 v7, v[26:27] offset:34824\n"
"v_mfma_f32_16x16x32_bf16 v[212:215], v[60:63], a[108:111], v[212:215]\n"
"v_accvgpr_read_b32 v16, a204\n"
"v_accvgpr_read_b32 v17, a205\n"
"v_mfma_f32_16x16x32_bf16 v[212:215], v[32:35], a[104:107], v[212:215]\n"
"v_accvgpr_read_b32 v18, a206\n"
"v_accvgpr_read_b32 v19, a207\n"
"v_mfma_f32_16x16x32_bf16 v[184:187], v[92:95], a[104:107], v[184:187]\n"
"s_waitcnt lgkmcnt(6)\n"
"buffer_store_dwordx4 v[172:175], v6, s[52:55], s81 offen offset:1152 sc1\n"
"v_lshlrev_b32 v12, 16, v16\n"
"v_and_b32 v13, s63, v16\n"
"v_lshlrev_b32 v14, 16, v17\n"
"v_mfma_f32_16x16x32_bf16 v[184:187], v[120:123], a[108:111], v[184:187]\n"
"v_and_b32 v15, s63, v17\n"
"v_mul_f32 v12, v12, v204\n"
"v_mfma_f32_16x16x32_bf16 v[156:159], v[116:119], a[108:111], v[156:159]\n"
"ds_read_b128 a[108:111], v5 offset:27648\n"
"v_mul_f32 v13, v13, v205\n"
"v_mfma_f32_16x16x32_bf16 v[156:159], v[88:91], a[104:107], v[156:159]\n"
"ds_read_b128 a[104:107], v5 offset:26624\n"
"v_mul_f32 v14, v14, v206\n"
"v_mfma_f32_16x16x32_bf16 v[156:159], v[100:103], a[112:115], v[156:159]\n"
"v_mul_f32 v15, v15, v207\n"
"v_cvt_pk_bf16_f32 v28, v12, v13\n"
"v_mfma_f32_16x16x32_bf16 v[156:159], v[128:131], a[116:119], v[156:159]\n"
"v_cvt_pk_bf16_f32 v29, v14, v15\n"
"v_mfma_f32_16x16x32_bf16 v[184:187], v[64:67], a[116:119], v[184:187]\n"
"ds_write_b64 v8, v[28:29] offset:34816\n"
"v_mfma_f32_16x16x32_bf16 v[184:187], v[36:39], a[112:115], v[184:187]\n"
"v_lshlrev_b32 v12, 16, v18\n"
"v_and_b32 v13, s63, v18\n"
"v_mfma_f32_16x16x32_bf16 v[212:215], v[104:107], a[112:115], v[212:215]\n"
"v_lshlrev_b32 v14, 16, v19\n"
"v_and_b32 v15, s63, v19\n"
"v_mfma_f32_16x16x32_bf16 v[212:215], v[132:135], a[116:119], v[212:215]\n"
"v_mul_f32 v12, v12, v232\n"
"v_mul_f32 v13, v13, v233\n"
"v_mfma_f32_16x16x32_bf16 v[240:243], v[136:139], a[116:119], v[240:243]\n"
"ds_read_b128 a[116:119], v3 offset:29696\n"
"v_mul_f32 v14, v14, v234\n"
"v_mfma_f32_16x16x32_bf16 v[240:243], v[108:111], a[112:115], v[240:243]\n"
"ds_read_b128 a[112:115], v3 offset:28672\n"
"v_mul_f32 v15, v15, v235\n"
"v_mfma_f32_16x16x32_bf16 v[240:243], a[88:91], a[120:123], v[240:243]\n"
"v_cvt_pk_bf16_f32 v30, v12, v13\n"
"v_cvt_pk_bf16_f32 v31, v14, v15\n"
"v_mfma_f32_16x16x32_bf16 v[240:243], a[92:95], a[124:127], v[240:243]\n"
"ds_write_b64 v8, v[30:31] offset:34824\n"
"v_mfma_f32_16x16x32_bf16 v[212:215], a[84:87], a[124:127], v[212:215]\n"
"ds_read_b128 v[148:151], v2 offset:34816\n"
"v_mfma_f32_16x16x32_bf16 v[212:215], a[80:83], a[120:123], v[212:215]\n"
"ds_read_b128 v[176:179], v2 offset:35840\n"
"v_accvgpr_read_b32 v16, a208\n"
"v_mfma_f32_16x16x32_bf16 v[184:187], a[72:75], a[120:123], v[184:187]\n"
"v_accvgpr_read_b32 v17, a209\n"
"v_accvgpr_read_b32 v18, a210\n"
"v_mfma_f32_16x16x32_bf16 v[184:187], a[76:79], a[124:127], v[184:187]\n"
"v_accvgpr_read_b32 v19, a211\n"
"v_lshlrev_b32 v12, 16, v16\n"
"v_mfma_f32_16x16x32_bf16 v[156:159], a[68:71], a[124:127], v[156:159]\n"
"ds_read_b128 a[124:127], v4 offset:29696\n"
"v_and_b32 v13, s63, v16\n"
"v_mfma_f32_16x16x32_bf16 v[156:159], a[64:67], a[120:123], v[156:159]\n"
"ds_read_b128 a[120:123], v4 offset:28672\n"
"v_lshlrev_b32 v14, 16, v17\n"
"v_mfma_f32_16x16x32_bf16 v[160:163], v[88:91], a[128:131], v[160:163]\n"
"v_and_b32 v15, s63, v17\n"
"v_mul_f32 v12, v12, v152\n"
"v_mfma_f32_16x16x32_bf16 v[160:163], v[116:119], a[132:135], v[160:163]\n"
"v_mul_f32 v13, v13, v153\n"
"v_mul_f32 v14, v14, v154\n"
"v_mfma_f32_16x16x32_bf16 v[188:191], v[120:123], a[132:135], v[188:191]\n"
"v_mul_f32 v15, v15, v155\n"
"v_cvt_pk_bf16_f32 v24, v12, v13\n"
"v_mfma_f32_16x16x32_bf16 v[188:191], v[92:95], a[128:131], v[188:191]\n"
"v_cvt_pk_bf16_f32 v25, v14, v15\n"
"v_mfma_f32_16x16x32_bf16 v[216:219], v[32:35], a[128:131], v[216:219]\n"
"s_waitcnt lgkmcnt(3)\n"
"buffer_store_dwordx4 v[148:151], v6, s[52:55], s82 offen offset:2176 sc1\n"
"ds_write_b64 v7, v[24:25] offset:36864\n"
"v_mfma_f32_16x16x32_bf16 v[216:219], v[60:63], a[132:135], v[216:219]\n"
"v_lshlrev_b32 v12, 16, v18\n"
"v_and_b32 v13, s63, v18\n"
"v_mfma_f32_16x16x32_bf16 v[244:247], v[124:127], a[132:135], v[244:247]\n"
"ds_read_b128 a[132:135], v5 offset:29696\n"
"v_lshlrev_b32 v14, 16, v19\n"
"v_mfma_f32_16x16x32_bf16 v[244:247], v[96:99], a[128:131], v[244:247]\n"
"ds_read_b128 a[128:131], v5 offset:28672\n"
"v_and_b32 v15, s63, v19\n"
"v_mfma_f32_16x16x32_bf16 v[244:247], v[108:111], a[136:139], v[244:247]\n"
"v_mul_f32 v12, v12, v180\n"
"v_mul_f32 v13, v13, v181\n"
"v_mfma_f32_16x16x32_bf16 v[244:247], v[136:139], a[140:143], v[244:247]\n"
"v_mul_f32 v14, v14, v182\n"
"v_mul_f32 v15, v15, v183\n"
"v_mfma_f32_16x16x32_bf16 v[216:219], v[132:135], a[140:143], v[216:219]\n"
"v_cvt_pk_bf16_f32 v26, v12, v13\n"
"v_cvt_pk_bf16_f32 v27, v14, v15\n"
"v_mfma_f32_16x16x32_bf16 v[216:219], v[104:107], a[136:139], v[216:219]\n"
"ds_write_b64 v7, v[26:27] offset:36872\n"
"v_mfma_f32_16x16x32_bf16 v[188:191], v[36:39], a[136:139], v[188:191]\n"
"v_accvgpr_read_b32 v16, a212\n"
"v_accvgpr_read_b32 v17, a213\n"
"v_mfma_f32_16x16x32_bf16 v[188:191], v[64:67], a[140:143], v[188:191]\n"
"v_accvgpr_read_b32 v18, a214\n"
"v_accvgpr_read_b32 v19, a215\n"
"v_mfma_f32_16x16x32_bf16 v[160:163], v[128:131], a[140:143], v[160:163]\n"
"s_waitcnt lgkmcnt(6)\n"
"buffer_store_dwordx4 v[176:179], v6, s[52:55], s83 offen offset:3200 sc1\n"
"ds_read_b128 a[140:143], v3 offset:31744\n"
"v_lshlrev_b32 v12, 16, v16\n"
"v_and_b32 v13, s63, v16\n"
"v_mfma_f32_16x16x32_bf16 v[160:163], v[100:103], a[136:139], v[160:163]\n"
"ds_read_b128 a[136:139], v3 offset:30720\n"
"v_lshlrev_b32 v14, 16, v17\n"
"v_mfma_f32_16x16x32_bf16 v[160:163], a[64:67], a[144:147], v[160:163]\n"
"v_and_b32 v15, s63, v17\n"
"v_mul_f32 v12, v12, v208\n"
"v_mfma_f32_16x16x32_bf16 v[160:163], a[68:71], a[148:151], v[160:163]\n"
"v_mul_f32 v13, v13, v209\n"
"v_mul_f32 v14, v14, v210\n"
"v_mfma_f32_16x16x32_bf16 v[188:191], a[76:79], a[148:151], v[188:191]\n"
"v_mul_f32 v15, v15, v211\n"
"v_cvt_pk_bf16_f32 v28, v12, v13\n"
"v_mfma_f32_16x16x32_bf16 v[188:191], a[72:75], a[144:147], v[188:191]\n"
"v_cvt_pk_bf16_f32 v29, v14, v15\n"
"v_mfma_f32_16x16x32_bf16 v[216:219], a[80:83], a[144:147], v[216:219]\n"
"ds_write_b64 v8, v[28:29] offset:36864\n"
"v_mfma_f32_16x16x32_bf16 v[216:219], a[84:87], a[148:151], v[216:219]\n"
"v_lshlrev_b32 v12, 16, v18\n"
"v_and_b32 v13, s63, v18\n"
"v_mfma_f32_16x16x32_bf16 v[244:247], a[92:95], a[148:151], v[244:247]\n"
"ds_read_b128 a[148:151], v4 offset:31744\n"
"v_lshlrev_b32 v14, 16, v19\n"
"v_mfma_f32_16x16x32_bf16 v[244:247], a[88:91], a[144:147], v[244:247]\n"
"ds_read_b128 a[144:147], v4 offset:30720\n"
"v_and_b32 v15, s63, v19\n"
"v_mfma_f32_16x16x32_bf16 v[248:251], v[96:99], a[152:155], v[248:251]\n"
"v_mul_f32 v12, v12, v236\n"
"v_mul_f32 v13, v13, v237\n"
"v_mfma_f32_16x16x32_bf16 v[248:251], v[124:127], a[156:159], v[248:251]\n"
"v_mul_f32 v14, v14, v238\n"
"v_mul_f32 v15, v15, v239\n"
"v_mfma_f32_16x16x32_bf16 v[220:223], v[60:63], a[156:159], v[220:223]\n"
"v_cvt_pk_bf16_f32 v30, v12, v13\n"
"v_cvt_pk_bf16_f32 v31, v14, v15\n"
"v_mfma_f32_16x16x32_bf16 v[220:223], v[32:35], a[152:155], v[220:223]\n"
"ds_write_b64 v8, v[30:31] offset:36872\n"
"v_mfma_f32_16x16x32_bf16 v[192:195], v[92:95], a[152:155], v[192:195]\n"
"ds_read_b128 v[152:155], v2 offset:36864\n"
"v_mfma_f32_16x16x32_bf16 v[192:195], v[120:123], a[156:159], v[192:195]\n"
"ds_read_b128 v[180:183], v2 offset:37888\n"
"v_accvgpr_read_b32 v16, a216\n"
"v_mfma_f32_16x16x32_bf16 v[164:167], v[116:119], a[156:159], v[164:167]\n"
"ds_read_b128 a[156:159], v5 offset:31744\n"
"v_accvgpr_read_b32 v17, a217\n"
"v_mfma_f32_16x16x32_bf16 v[164:167], v[88:91], a[152:155], v[164:167]\n"
"ds_read_b128 a[152:155], v5 offset:30720\n"
"v_accvgpr_read_b32 v18, a218\n"
"v_mfma_f32_16x16x32_bf16 v[164:167], v[100:103], a[96:99], v[164:167]\n"
"v_accvgpr_read_b32 v19, a219\n"
"v_lshlrev_b32 v12, 16, v16\n"
"v_mfma_f32_16x16x32_bf16 v[164:167], v[128:131], a[100:103], v[164:167]\n"
"v_and_b32 v13, s63, v16\n"
"v_lshlrev_b32 v14, 16, v17\n"
"v_mfma_f32_16x16x32_bf16 v[192:195], v[64:67], a[100:103], v[192:195]\n"
"v_and_b32 v15, s63, v17\n"
"v_mul_f32 v12, v12, v156\n"
"v_mfma_f32_16x16x32_bf16 v[192:195], v[36:39], a[96:99], v[192:195]\n"
"v_mul_f32 v13, v13, v157\n"
"v_mul_f32 v14, v14, v158\n"
"v_mfma_f32_16x16x32_bf16 v[220:223], v[104:107], a[96:99], v[220:223]\n"
"v_mul_f32 v15, v15, v159\n"
"v_mfma_f32_16x16x32_bf16 v[220:223], v[132:135], a[100:103], v[220:223]\n"
"v_cvt_pk_bf16_f32 v24, v12, v13\n"
"v_mfma_f32_16x16x32_bf16 v[248:251], v[136:139], a[100:103], v[248:251]\n"
"v_cvt_pk_bf16_f32 v25, v14, v15\n"
"v_mfma_f32_16x16x32_bf16 v[248:251], v[108:111], a[96:99], v[248:251]\n"
"s_waitcnt lgkmcnt(3)\n"
"buffer_store_dwordx4 v[152:155], v6, s[52:55], s84 offen offset:128 sc1\n"
"ds_write_b64 v7, v[24:25] offset:38912\n"
"v_mfma_f32_16x16x32_bf16 v[248:251], a[88:91], a[104:107], v[248:251]\n"
"v_lshlrev_b32 v12, 16, v18\n"
"v_and_b32 v13, s63, v18\n"
"v_mfma_f32_16x16x32_bf16 v[248:251], a[92:95], a[108:111], v[248:251]\n"
"v_lshlrev_b32 v14, 16, v19\n"
"v_mfma_f32_16x16x32_bf16 v[220:223], a[84:87], a[108:111], v[220:223]\n"
"v_and_b32 v15, s63, v19\n"
"v_mfma_f32_16x16x32_bf16 v[220:223], a[80:83], a[104:107], v[220:223]\n"
"v_mul_f32 v12, v12, v184\n"
"v_mfma_f32_16x16x32_bf16 v[192:195], a[72:75], a[104:107], v[192:195]\n"
"v_mul_f32 v13, v13, v185\n"
"v_mfma_f32_16x16x32_bf16 v[192:195], a[76:79], a[108:111], v[192:195]\n"
"v_mul_f32 v14, v14, v186\n"
"v_mfma_f32_16x16x32_bf16 v[164:167], a[68:71], a[108:111], v[164:167]\n"
"v_mul_f32 v15, v15, v187\n"
"v_mfma_f32_16x16x32_bf16 v[164:167], a[64:67], a[104:107], v[164:167]\n"
"v_cvt_pk_bf16_f32 v26, v12, v13\n"
"v_mfma_f32_16x16x32_bf16 v[168:171], v[88:91], a[112:115], v[168:171]\n"
"v_cvt_pk_bf16_f32 v27, v14, v15\n"
"v_mfma_f32_16x16x32_bf16 v[168:171], v[116:119], a[116:119], v[168:171]\n"
"s_waitcnt lgkmcnt(3)\n"
"buffer_store_dwordx4 v[180:183], v6, s[52:55], s85 offen offset:1152 sc1\n"
"ds_write_b64 v7, v[26:27] offset:38920\n"
"v_mfma_f32_16x16x32_bf16 v[196:199], v[120:123], a[116:119], v[196:199]\n"
"v_accvgpr_read_b32 v16, a220\n"
"v_accvgpr_read_b32 v17, a221\n"
"v_mfma_f32_16x16x32_bf16 v[196:199], v[92:95], a[112:115], v[196:199]\n"
"v_accvgpr_read_b32 v18, a222\n"
"v_accvgpr_read_b32 v19, a223\n"
"v_mfma_f32_16x16x32_bf16 v[224:227], v[32:35], a[112:115], v[224:227]\n"
"v_lshlrev_b32 v12, 16, v16\n"
"v_and_b32 v13, s63, v16\n"
"v_mfma_f32_16x16x32_bf16 v[224:227], v[60:63], a[116:119], v[224:227]\n"
"v_lshlrev_b32 v14, 16, v17\n"
"v_and_b32 v15, s63, v17\n"
"v_mfma_f32_16x16x32_bf16 v[252:255], v[124:127], a[116:119], v[252:255]\n"
"v_mul_f32 v12, v12, v212\n"
"v_mul_f32 v13, v13, v213\n"
"v_mfma_f32_16x16x32_bf16 v[252:255], v[96:99], a[112:115], v[252:255]\n"
"v_mul_f32 v14, v14, v214\n"
"v_mul_f32 v15, v15, v215\n"
"v_mfma_f32_16x16x32_bf16 v[252:255], v[108:111], a[120:123], v[252:255]\n"
"v_cvt_pk_bf16_f32 v28, v12, v13\n"
"v_cvt_pk_bf16_f32 v29, v14, v15\n"
"v_mfma_f32_16x16x32_bf16 v[252:255], v[136:139], a[124:127], v[252:255]\n"
"ds_write_b64 v8, v[28:29] offset:38912\n"
"v_mfma_f32_16x16x32_bf16 v[224:227], v[132:135], a[124:127], v[224:227]\n"
"v_lshlrev_b32 v12, 16, v18\n"
"v_and_b32 v13, s63, v18\n"
"v_mfma_f32_16x16x32_bf16 v[224:227], v[104:107], a[120:123], v[224:227]\n"
"v_lshlrev_b32 v14, 16, v19\n"
"v_and_b32 v15, s63, v19\n"
"v_mfma_f32_16x16x32_bf16 v[196:199], v[36:39], a[120:123], v[196:199]\n"
"v_mul_f32 v12, v12, v240\n"
"v_mul_f32 v13, v13, v241\n"
"v_mfma_f32_16x16x32_bf16 v[196:199], v[64:67], a[124:127], v[196:199]\n"
"v_mul_f32 v14, v14, v242\n"
"v_mul_f32 v15, v15, v243\n"
"v_mfma_f32_16x16x32_bf16 v[168:171], v[128:131], a[124:127], v[168:171]\n"
"v_cvt_pk_bf16_f32 v30, v12, v13\n"
"v_cvt_pk_bf16_f32 v31, v14, v15\n"
"v_mfma_f32_16x16x32_bf16 v[168:171], v[100:103], a[120:123], v[168:171]\n"
"ds_write_b64 v8, v[30:31] offset:38920\n"
"v_mfma_f32_16x16x32_bf16 v[168:171], a[64:67], a[128:131], v[168:171]\n"
"ds_read_b128 v[156:159], v2 offset:38912\n"
"v_mfma_f32_16x16x32_bf16 v[168:171], a[68:71], a[132:135], v[168:171]\n"
"ds_read_b128 v[184:187], v2 offset:39936\n"
"v_accvgpr_read_b32 v16, a224\n"
"v_mfma_f32_16x16x32_bf16 v[196:199], a[76:79], a[132:135], v[196:199]\n"
"v_accvgpr_read_b32 v17, a225\n"
"v_accvgpr_read_b32 v18, a226\n"
"v_mfma_f32_16x16x32_bf16 v[196:199], a[72:75], a[128:131], v[196:199]\n"
"v_accvgpr_read_b32 v19, a227\n"
"v_lshlrev_b32 v12, 16, v16\n"
"v_mfma_f32_16x16x32_bf16 v[224:227], a[80:83], a[128:131], v[224:227]\n"
"v_and_b32 v13, s63, v16\n"
"v_lshlrev_b32 v14, 16, v17\n"
"v_mfma_f32_16x16x32_bf16 v[224:227], a[84:87], a[132:135], v[224:227]\n"
"v_and_b32 v15, s63, v17\n"
"v_mul_f32 v12, v12, v160\n"
"v_mfma_f32_16x16x32_bf16 v[252:255], a[92:95], a[132:135], v[252:255]\n"
"v_mul_f32 v13, v13, v161\n"
"v_mul_f32 v14, v14, v162\n"
"v_mfma_f32_16x16x32_bf16 v[252:255], a[88:91], a[128:131], v[252:255]\n"
"v_mul_f32 v15, v15, v163\n"
"v_cvt_pk_bf16_f32 v24, v12, v13\n"
"v_mfma_f32_16x16x32_bf16 a[28:31], v[96:99], a[136:139], a[28:31]\n"
"v_cvt_pk_bf16_f32 v25, v14, v15\n"
"v_mfma_f32_16x16x32_bf16 a[28:31], v[124:127], a[140:143], a[28:31]\n"
"s_waitcnt lgkmcnt(1)\n"
"buffer_store_dwordx4 v[156:159], v6, s[52:55], s86 offen offset:2176 sc1\n"
"ds_write_b64 v7, v[24:25] offset:32768\n"
"v_mfma_f32_16x16x32_bf16 a[24:27], v[60:63], a[140:143], a[24:27]\n"
"v_lshlrev_b32 v12, 16, v18\n"
"v_and_b32 v13, s63, v18\n"
"v_mfma_f32_16x16x32_bf16 a[24:27], v[32:35], a[136:139], a[24:27]\n"
"v_lshlrev_b32 v14, 16, v19\n"
"v_and_b32 v15, s63, v19\n"
"v_mfma_f32_16x16x32_bf16 a[20:23], v[92:95], a[136:139], a[20:23]\n"
"v_mul_f32 v12, v12, v188\n"
"v_mul_f32 v13, v13, v189\n"
"v_mfma_f32_16x16x32_bf16 a[20:23], v[120:123], a[140:143], a[20:23]\n"
"v_mul_f32 v14, v14, v190\n"
"v_mul_f32 v15, v15, v191\n"
"v_mfma_f32_16x16x32_bf16 a[16:19], v[116:119], a[140:143], a[16:19]\n"
"v_cvt_pk_bf16_f32 v26, v12, v13\n"
"v_cvt_pk_bf16_f32 v27, v14, v15\n"
"v_mfma_f32_16x16x32_bf16 a[16:19], v[88:91], a[136:139], a[16:19]\n"
"ds_write_b64 v7, v[26:27] offset:32776\n"
"v_mfma_f32_16x16x32_bf16 a[16:19], v[100:103], a[144:147], a[16:19]\n"
"v_accvgpr_read_b32 v16, a228\n"
"v_accvgpr_read_b32 v17, a229\n"
"v_mfma_f32_16x16x32_bf16 a[16:19], v[128:131], a[148:151], a[16:19]\n"
"v_accvgpr_read_b32 v18, a230\n"
"v_accvgpr_read_b32 v19, a231\n"
"v_mfma_f32_16x16x32_bf16 a[20:23], v[64:67], a[148:151], a[20:23]\n"
"v_lshlrev_b32 v12, 16, v16\n"
"v_and_b32 v13, s63, v16\n"
"v_mfma_f32_16x16x32_bf16 a[20:23], v[36:39], a[144:147], a[20:23]\n"
"s_waitcnt lgkmcnt(2)\n"
"buffer_store_dwordx4 v[184:187], v6, s[52:55], s87 offen offset:3200 sc1\n"
"v_lshlrev_b32 v14, 16, v17\n"
"v_and_b32 v15, s63, v17\n"
"v_mul_f32 v12, v12, v216\n"
"v_mfma_f32_16x16x32_bf16 a[24:27], v[104:107], a[144:147], a[24:27]\n"
"v_mul_f32 v13, v13, v217\n"
"v_mul_f32 v14, v14, v218\n"
"v_mfma_f32_16x16x32_bf16 a[24:27], v[132:135], a[148:151], a[24:27]\n"
"v_mul_f32 v15, v15, v219\n"
"v_mfma_f32_16x16x32_bf16 a[28:31], v[136:139], a[148:151], a[28:31]\n"
"v_cvt_pk_bf16_f32 v28, v12, v13\n"
"v_cvt_pk_bf16_f32 v29, v14, v15\n"
"v_mfma_f32_16x16x32_bf16 a[28:31], v[108:111], a[144:147], a[28:31]\n"
"ds_write_b64 v8, v[28:29] offset:32768\n"
"v_mfma_f32_16x16x32_bf16 a[28:31], a[88:91], a[152:155], a[28:31]\n"
"v_lshlrev_b32 v12, 16, v18\n"
"v_and_b32 v13, s63, v18\n"
"v_mfma_f32_16x16x32_bf16 a[28:31], a[92:95], a[156:159], a[28:31]\n"
"v_lshlrev_b32 v14, 16, v19\n"
"v_and_b32 v15, s63, v19\n"
"v_mfma_f32_16x16x32_bf16 a[24:27], a[84:87], a[156:159], a[24:27]\n"
"v_mul_f32 v12, v12, v244\n"
"v_mul_f32 v13, v13, v245\n"
"v_mfma_f32_16x16x32_bf16 a[24:27], a[80:83], a[152:155], a[24:27]\n"
"v_mul_f32 v14, v14, v246\n"
"v_mul_f32 v15, v15, v247\n"
"v_mfma_f32_16x16x32_bf16 a[20:23], a[72:75], a[152:155], a[20:23]\n"
"s_add_u32 s62, s62, 1\n"
"v_cvt_pk_bf16_f32 v30, v12, v13\n"
"v_cvt_pk_bf16_f32 v31, v14, v15\n"
"v_mfma_f32_16x16x32_bf16 a[20:23], a[76:79], a[156:159], a[20:23]\n"
"ds_write_b64 v8, v[30:31] offset:32776\n"
"v_mfma_f32_16x16x32_bf16 a[16:19], a[68:71], a[156:159], a[16:19]\n"
"ds_read_b128 v[160:163], v2 offset:32768\n"
"v_mfma_f32_16x16x32_bf16 a[16:19], a[64:67], a[152:155], a[16:19]\n"
"ds_read_b128 v[188:191], v2 offset:33792\n"
"s_nop 7\n"
"v_accvgpr_read_b32 v16, a232\n"
"v_accvgpr_read_b32 v17, a233\n"
"v_accvgpr_read_b32 v18, a234\n"
"v_accvgpr_read_b32 v19, a235\n"
"v_lshlrev_b32 v12, 16, v16\n"
"v_and_b32 v13, s63, v16\n"
"v_lshlrev_b32 v14, 16, v17\n"
"v_and_b32 v15, s63, v17\n"
"v_pk_mul_f32 v[12:13], v[12:13], v[164:165]\n"
"v_pk_mul_f32 v[14:15], v[14:15], v[166:167]\n"
"v_cvt_pk_bf16_f32 v24, v12, v13\n"
"v_cvt_pk_bf16_f32 v25, v14, v15\n"
"v_lshlrev_b32 v12, 16, v18\n"
"v_and_b32 v13, s63, v18\n"
"v_lshlrev_b32 v14, 16, v19\n"
"v_and_b32 v15, s63, v19\n"
"v_pk_mul_f32 v[12:13], v[12:13], v[192:193]\n"
"v_pk_mul_f32 v[14:15], v[14:15], v[194:195]\n"
"v_cvt_pk_bf16_f32 v26, v12, v13\n"
"v_cvt_pk_bf16_f32 v27, v14, v15\n"
"ds_write_b128 v7, v[24:27] offset:34816\n"
"s_waitcnt lgkmcnt(2)\n"
"buffer_store_dwordx4 v[160:163], v6, s[52:55], s88 offen offset:128 sc1\n"
"s_waitcnt lgkmcnt(1)\n"
"buffer_store_dwordx4 v[188:191], v6, s[52:55], s89 offen offset:1152 sc1\n"
"v_accvgpr_read_b32 v16, a236\n"
"v_accvgpr_read_b32 v17, a237\n"
"v_accvgpr_read_b32 v18, a238\n"
"v_accvgpr_read_b32 v19, a239\n"
"v_lshlrev_b32 v12, 16, v16\n"
"v_and_b32 v13, s63, v16\n"
"v_lshlrev_b32 v14, 16, v17\n"
"v_and_b32 v15, s63, v17\n"
"v_pk_mul_f32 v[12:13], v[12:13], v[220:221]\n"
"v_pk_mul_f32 v[14:15], v[14:15], v[222:223]\n"
"v_cvt_pk_bf16_f32 v28, v12, v13\n"
"v_cvt_pk_bf16_f32 v29, v14, v15\n"
"v_lshlrev_b32 v12, 16, v18\n"
"v_and_b32 v13, s63, v18\n"
"v_lshlrev_b32 v14, 16, v19\n"
"v_and_b32 v15, s63, v19\n"
"v_pk_mul_f32 v[12:13], v[12:13], v[248:249]\n"
"v_pk_mul_f32 v[14:15], v[14:15], v[250:251]\n"
"v_cvt_pk_bf16_f32 v30, v12, v13\n"
"v_cvt_pk_bf16_f32 v31, v14, v15\n"
"ds_write_b128 v8, v[28:31] offset:34816\n"
"ds_read_b128 v[164:167], v2 offset:34816\n"
"ds_read_b128 v[192:195], v2 offset:35840\n"
"v_accvgpr_read_b32 v16, a240\n"
"v_accvgpr_read_b32 v17, a241\n"
"v_accvgpr_read_b32 v18, a242\n"
"v_accvgpr_read_b32 v19, a243\n"
"v_lshlrev_b32 v12, 16, v16\n"
"v_and_b32 v13, s63, v16\n"
"v_lshlrev_b32 v14, 16, v17\n"
"v_and_b32 v15, s63, v17\n"
"v_pk_mul_f32 v[12:13], v[12:13], v[168:169]\n"
"v_pk_mul_f32 v[14:15], v[14:15], v[170:171]\n"
"v_cvt_pk_bf16_f32 v24, v12, v13\n"
"v_cvt_pk_bf16_f32 v25, v14, v15\n"
"v_lshlrev_b32 v12, 16, v18\n"
"v_and_b32 v13, s63, v18\n"
"v_lshlrev_b32 v14, 16, v19\n"
"v_and_b32 v15, s63, v19\n"
"v_pk_mul_f32 v[12:13], v[12:13], v[196:197]\n"
"v_pk_mul_f32 v[14:15], v[14:15], v[198:199]\n"
"v_cvt_pk_bf16_f32 v26, v12, v13\n"
"v_cvt_pk_bf16_f32 v27, v14, v15\n"
"ds_write_b128 v7, v[24:27] offset:36864\n"
"s_waitcnt lgkmcnt(2)\n"
"buffer_store_dwordx4 v[164:167], v6, s[52:55], s90 offen offset:2176 sc1\n"
"s_waitcnt lgkmcnt(1)\n"
"buffer_store_dwordx4 v[192:195], v6, s[52:55], s91 offen offset:3200 sc1\n"
"v_accvgpr_read_b32 v16, a244\n"
"v_accvgpr_read_b32 v17, a245\n"
"v_accvgpr_read_b32 v18, a246\n"
"v_accvgpr_read_b32 v19, a247\n"
"v_lshlrev_b32 v12, 16, v16\n"
"v_and_b32 v13, s63, v16\n"
"v_lshlrev_b32 v14, 16, v17\n"
"v_and_b32 v15, s63, v17\n"
"v_pk_mul_f32 v[12:13], v[12:13], v[224:225]\n"
"v_pk_mul_f32 v[14:15], v[14:15], v[226:227]\n"
"v_cvt_pk_bf16_f32 v28, v12, v13\n"
"v_cvt_pk_bf16_f32 v29, v14, v15\n"
"v_lshlrev_b32 v12, 16, v18\n"
"v_and_b32 v13, s63, v18\n"
"v_lshlrev_b32 v14, 16, v19\n"
"v_and_b32 v15, s63, v19\n"
"v_pk_mul_f32 v[12:13], v[12:13], v[252:253]\n"
"v_pk_mul_f32 v[14:15], v[14:15], v[254:255]\n"
"v_cvt_pk_bf16_f32 v30, v12, v13\n"
"v_cvt_pk_bf16_f32 v31, v14, v15\n"
"ds_write_b128 v8, v[28:31] offset:36864\n"
"ds_read_b128 v[168:171], v2 offset:36864\n"
"ds_read_b128 v[196:199], v2 offset:37888\n"
"v_accvgpr_read_b32 v16, a248\n"
"v_accvgpr_read_b32 v17, a249\n"
"v_accvgpr_read_b32 v18, a250\n"
"v_accvgpr_read_b32 v19, a251\n"
"v_lshlrev_b32 v12, 16, v16\n"
"v_and_b32 v13, s63, v16\n"
"v_lshlrev_b32 v14, 16, v17\n"
"v_and_b32 v15, s63, v17\n"
"v_accvgpr_read_b32 v10, a16\n"
"v_accvgpr_read_b32 v11, a17\n"
"v_mul_f32 v12, v12, v10\n"
"v_mul_f32 v13, v13, v11\n"
"v_accvgpr_read_b32 v10, a18\n"
"v_accvgpr_read_b32 v11, a19\n"
"v_mul_f32 v14, v14, v10\n"
"v_mul_f32 v15, v15, v11\n"
"v_cvt_pk_bf16_f32 v24, v12, v13\n"
"v_cvt_pk_bf16_f32 v25, v14, v15\n"
"v_lshlrev_b32 v12, 16, v18\n"
"v_and_b32 v13, s63, v18\n"
"v_lshlrev_b32 v14, 16, v19\n"
"v_and_b32 v15, s63, v19\n"
"v_accvgpr_read_b32 v10, a20\n"
"v_accvgpr_read_b32 v11, a21\n"
"v_mul_f32 v12, v12, v10\n"
"v_mul_f32 v13, v13, v11\n"
"v_accvgpr_read_b32 v10, a22\n"
"v_accvgpr_read_b32 v11, a23\n"
"v_mul_f32 v14, v14, v10\n"
"v_mul_f32 v15, v15, v11\n"
"v_cvt_pk_bf16_f32 v26, v12, v13\n"
"v_cvt_pk_bf16_f32 v27, v14, v15\n"
"ds_write_b128 v7, v[24:27] offset:38912\n"
"s_waitcnt lgkmcnt(2)\n"
"buffer_store_dwordx4 v[168:171], v6, s[52:55], s92 offen offset:128 sc1\n"
"s_waitcnt lgkmcnt(1)\n"
"buffer_store_dwordx4 v[196:199], v6, s[52:55], s93 offen offset:1152 sc1\n"
"v_accvgpr_read_b32 v16, a252\n"
"v_accvgpr_read_b32 v17, a253\n"
"v_accvgpr_read_b32 v18, a254\n"
"v_accvgpr_read_b32 v19, a255\n"
"v_lshlrev_b32 v12, 16, v16\n"
"v_and_b32 v13, s63, v16\n"
"v_lshlrev_b32 v14, 16, v17\n"
"v_and_b32 v15, s63, v17\n"
"v_accvgpr_read_b32 v10, a24\n"
"v_accvgpr_read_b32 v11, a25\n"
"v_mul_f32 v12, v12, v10\n"
"v_mul_f32 v13, v13, v11\n"
"v_accvgpr_read_b32 v10, a26\n"
"v_accvgpr_read_b32 v11, a27\n"
"v_mul_f32 v14, v14, v10\n"
"v_mul_f32 v15, v15, v11\n"
"v_cvt_pk_bf16_f32 v28, v12, v13\n"
"v_cvt_pk_bf16_f32 v29, v14, v15\n"
"v_lshlrev_b32 v12, 16, v18\n"
"v_and_b32 v13, s63, v18\n"
"v_lshlrev_b32 v14, 16, v19\n"
"v_and_b32 v15, s63, v19\n"
"v_accvgpr_read_b32 v10, a28\n"
"v_accvgpr_read_b32 v11, a29\n"
"v_mul_f32 v12, v12, v10\n"
"v_mul_f32 v13, v13, v11\n"
"v_accvgpr_read_b32 v10, a30\n"
"v_accvgpr_read_b32 v11, a31\n"
"v_mul_f32 v14, v14, v10\n"
"v_mul_f32 v15, v15, v11\n"
"v_cvt_pk_bf16_f32 v30, v12, v13\n"
"v_cvt_pk_bf16_f32 v31, v14, v15\n"
"ds_write_b128 v8, v[28:31] offset:38912\n"
"ds_read_b128 v[224:227], v2 offset:38912\n"
"ds_read_b128 v[252:255], v2 offset:39936\n"
"s_waitcnt lgkmcnt(1)\n"
"buffer_store_dwordx4 v[224:227], v6, s[52:55], s94 offen offset:2176 sc1\n"
"s_waitcnt lgkmcnt(0)\n"
"buffer_store_dwordx4 v[252:255], v6, s[52:55], s95 offen offset:3200 sc1\n"
"s_or_b32 s66, %26, %16\n"
"s_cbranch_scc1 L_end\n"
"v_mov_b32 v10, s78\n"
"v_mov_b32 v11, s79\n"
"s_mov_b32 s66, 139264\n"
"buffer_store_dwordx2 v[10:11], off, s[56:59], s66 sc1\n"
"s_branch L_end\n"
"L_slow0:\n"
"s_cmp_eq_u32 s68, 0\n"
"s_cbranch_scc1 L_back0\n"
"s_sub_u32 s68, s68, 1\n"
"s_sleep 2\n"
"buffer_load_dwordx2 v[8:9], off, s[56:59], %14 sc1\n"
"s_waitcnt vmcnt(0)\n"
"v_subrev_u32 v8, s65, v8\n"
"v_xor_b32 v9, s79, v9\n"
"v_or_b32 v8, v8, v9\n"
"v_cmp_lt_u32 vcc, 15, v8\n"
"s_nop 4\n"
"s_cbranch_vccnz L_slow0\n"
"s_branch L_back0\n"
"L_slow1:\n"
"s_cmp_eq_u32 s68, 0\n"
"s_cbranch_scc1 L_back1\n"
"s_sub_u32 s68, s68, 1\n"
"s_sleep 2\n"
"buffer_load_dwordx2 v[8:9], off, s[56:59], %14 sc1\n"
"s_waitcnt vmcnt(0)\n"
"v_subrev_u32 v8, s65, v8\n"
"v_xor_b32 v9, s79, v9\n"
"v_or_b32 v8, v8, v9\n"
"v_cmp_lt_u32 vcc, 15, v8\n"
"s_nop 4\n"
"s_cbranch_vccnz L_slow1\n"
"s_branch L_back1\n"
"L_slow2:\n"
"s_cmp_eq_u32 s68, 0\n"
"s_cbranch_scc1 L_back2\n"
"s_sub_u32 s68, s68, 1\n"
"s_sleep 2\n"
"buffer_load_dwordx4 v[12:15], v0, s[56:59], %25 offen sc1\n"
"s_waitcnt vmcnt(0)\n"
"v_subrev_u32 v12, s67, v12\n"
"v_xor_b32 v13, s79, v13\n"
"v_subrev_u32 v14, s67, v14\n"
"v_xor_b32 v15, s79, v15\n"
"v_or_b32 v12, v12, v14\n"
"v_or_b32 v13, v13, v15\n"
"v_or_b32 v12, v12, v13\n"
"v_cmp_lt_u32 vcc, 15, v12\n"
"s_nop 4\n"
"s_cbranch_vccnz L_slow2\n"
"s_branch L_back2\n"
"L_slow3:\n"
"s_cmp_eq_u32 s68, 0\n"
"s_cbranch_scc1 L_back3\n"
"s_sub_u32 s68, s68, 1\n"
"s_sleep 2\n"
"buffer_load_dwordx4 v[24:27], v0, s[56:59], %25 offen sc1\n"
"s_waitcnt vmcnt(0)\n"
"v_subrev_u32 v24, s70, v24\n"
"v_xor_b32 v25, s79, v25\n"
"v_subrev_u32 v26, s70, v26\n"
"v_xor_b32 v27, s79, v27\n"
"v_or_b32 v24, v24, v26\n"
"v_or_b32 v25, v25, v27\n"
"v_or_b32 v24, v24, v25\n"
"v_cmp_lt_u32 vcc, 15, v24\n"
"s_nop 4\n"
"s_cbranch_vccnz L_slow3\n"
"s_branch L_back3\n"
"L_slow4:\n"
"s_cmp_eq_u32 s68, 0\n"
"s_cbranch_scc1 L_back4\n"
"s_sub_u32 s68, s68, 1\n"
"s_sleep 2\n"
"buffer_load_dwordx2 v[8:9], off, s[56:59], %14 sc1\n"
"s_waitcnt vmcnt(0)\n"
"v_subrev_u32 v8, s65, v8\n"
"v_xor_b32 v9, s79, v9\n"
"v_or_b32 v8, v8, v9\n"
"v_cmp_lt_u32 vcc, 15, v8\n"
"s_nop 4\n"
"s_cbranch_vccnz L_slow4\n"
"s_branch L_back4\n"
"L_headF:\n"
"L_end:\n"
"s_waitcnt vmcnt(0) lgkmcnt(0)\n"
    :: LO(wsb), HI(wsb), LO(xo), HI(xo), LO(ob), HI(ob), LO(wr), HI(wr), LO(bb), HI(bb), "s"(wo), "s"(eo), "s"(ep), "s"(fo), "s"(fp),
       "s"(ldsa), "s"(w), "s"(col), "v"(threadIdx.x), "s"(db), "s"(rs1), "s"(rs2), "s"(rd1), "s"(rn1), "s"(wfo), "s"(wfa), "s"(b)
    : "memory", "scc", "vcc",
"v1","v2","v3","v4","v5","v6","v7","v8","v9","v10","v11","v12","v13","v14","v15","v16","v17","v18","v19","v20","v21","v22","v23","v24","v25","v26","v27","v28","v29","v30","v31","v32","v33","v34","v35","v36","v37","v38","v39","v40","v41","v42","v43","v44","v45","v46","v47","v48","v49","v50","v51","v52","v53","v54","v55","v56","v57","v58","v59","v60","v61","v62","v63","v64","v65","v66","v67","v68","v69","v70","v71","v72","v73","v74","v75","v76","v77","v78","v79","v80","v81","v82","v83","v84","v85","v86","v87","v88","v89","v90","v91","v92","v93","v94","v95","v96","v97","v98","v99","v100","v101","v102","v103","v104","v105","v106","v107","v108","v109","v110","v111","v112","v113","v114","v115","v116","v117","v118","v119","v120","v121","v122","v123","v124","v125","v126","v127","v128","v129","v130","v131","v132","v133","v134","v135","v136","v137","v138","v139","v140","v141","v142","v143","v144","v145","v146","v147","v148","v149","v150","v151","v152","v153","v154","v155","v156","v157","v158","v159","v160","v161","v162","v163","v164","v165","v166","v167","v168","v169","v170","v171","v172","v173","v174","v175","v176","v177","v178","v179","v180","v181","v182","v183","v184","v185","v186","v187","v188","v189","v190","v191","v192","v193","v194","v195","v196","v197","v198","v199","v200","v201","v202","v203","v204","v205","v206","v207","v208","v209","v210","v211","v212","v213","v214","v215","v216","v217","v218","v219","v220","v221","v222","v223","v224","v225","v226","v227","v228","v229","v230","v231","v232","v233","v234","v235","v236","v237","v238","v239","v240","v241","v242","v243","v244","v245","v246","v247","v248","v249","v250","v251","v252","v253","v254","v255",
"a0","a1","a2","a3","a4","a5","a6","a7","a8","a9","a10","a11","a12","a13","a14","a15","a16","a17","a18","a19","a20","a21","a22","a23","a24","a25","a26","a27","a28","a29","a30","a31","a32","a33","a34","a35","a36","a37","a38","a39","a40","a41","a42","a43","a44","a45","a46","a47","a48","a49","a50","a51","a52","a53","a54","a55","a56","a57","a58","a59","a60","a61","a62","a63","a64","a65","a66","a67","a68","a69","a70","a71","a72","a73","a74","a75","a76","a77","a78","a79","a80","a81","a82","a83","a84","a85","a86","a87","a88","a89","a90","a91","a92","a93","a94","a95","a96","a97","a98","a99","a100","a101","a102","a103","a104","a105","a106","a107","a108","a109","a110","a111","a112","a113","a114","a115","a116","a117","a118","a119","a120","a121","a122","a123","a124","a125","a126","a127","a128","a129","a130","a131","a132","a133","a134","a135","a136","a137","a138","a139","a140","a141","a142","a143","a144","a145","a146","a147","a148","a149","a150","a151","a152","a153","a154","a155","a156","a157","a158","a159","a160","a161","a162","a163","a164","a165","a166","a167","a168","a169","a170","a171","a172","a173","a174","a175","a176","a177","a178","a179","a180","a181","a182","a183","a184","a185","a186","a187","a188","a189","a190","a191","a192","a193","a194","a195","a196","a197","a198","a199","a200","a201","a202","a203","a204","a205","a206","a207","a208","a209","a210","a211","a212","a213","a214","a215","a216","a217","a218","a219","a220","a221","a222","a223","a224","a225","a226","a227","a228","a229","a230","a231","a232","a233","a234","a235","a236","a237","a238","a239","a240","a241","a242","a243","a244","a245","a246","a247","a248","a249","a250","a251","a252","a253","a254","a255",
"s33","s34","s35","s36","s37","s38","s39","s40","s41","s42","s43","s44","s45","s46","s47","s48","s49","s50","s51","s52","s53","s54","s55","s56","s57","s58","s59","s60","s61","s62","s63","s64","s65","s66","s67","s68","s69","s70","s71","s72","s73","s74","s75","s76","s77","s78","s79","s80","s81","s82","s83","s84","s85","s86","s87","s88","s89","s90","s91","s92","s93","s94","s95","s96","s97","s98","s99");
}

extern "C" void kernel_launch(void* const* d_in, const int* in_sizes, int n_in,
                              void* d_out, int out_size, void* d_ws, size_t ws_size,
                              hipStream_t stream) {
  const unsigned short* x0 = (const unsigned short*)d_in[0];
  const unsigned short* ws = (const unsigned short*)d_in[1];
  const unsigned short* bs = (const unsigned short*)d_in[2];
  dcn_cross_kernel<<<256, 256, 0, stream>>>(x0, ws, bs, (unsigned char*)d_ws, (unsigned short*)d_out);
}
```

```cpp
#include <hip/hip_runtime.h>


constexpr int kD = 1024;
constexpr unsigned kWFlagOff = 131072, kWOff = 1u << 20, kWStream = 200 * 8192, kXchOff = 32u << 20, kDbgOff = 224u << 20;

__device__ __forceinline__ unsigned unit_src(unsigned v, unsigned h, unsigned w) {
  const unsigned l = v >> 5, u = v & 31;
  const unsigned split = ((0xff0000ffu >> u) & 1) - 1;
  const unsigned part = l ? u >> 4 : 0u, g = (0xff00ff00u >> u) & 1;
  const unsigned c = u >> (split & 1), jh = (u >> (~split & 2)) & 1;
  const unsigned kc = 8 * (h ^ part) + 2 * ((w + c) & 3) + g;
  return (l * 1024 + 64 * kc) * 2048 + jh * 128;
}

__global__ __launch_bounds__(256, 1)
void dcn_cross_kernel(const unsigned short* __restrict__ x0, const unsigned short* __restrict__ ws, const unsigned short* __restrict__ bs,
                      unsigned char* __restrict__ wsp, unsigned short* __restrict__ out) {
  __shared__ __attribute__((aligned(16))) unsigned char lds[163840];
  const unsigned b = blockIdx.x;
  constexpr unsigned kPx = 1 ? 1u : 8u;
  const unsigned h = 1 ? (b & 1) : ((b >> 3) & 1), rb = 1 ? (b >> 1) : ((b >> 4) * 8 + (b & 7));
  const unsigned w = __builtin_amdgcn_readfirstlane(threadIdx.x >> 6);
  unsigned col = (512 * h + 128 * w) * 2, rows = rb * 128 * kD * 2, oofs = rows + col;
  unsigned wo = (h * 4 + w) * kWStream, eo = kXchOff + b * 524288 + w * 32768, ep = kXchOff + (b ^ kPx) * 524288 + w * 32768;
  unsigned fo = b * 512 + w * 128, fp = (b ^ kPx) * 512 + w * 128;
  unsigned db = kDbgOff + (b * 4 + w) * 512;
  const unsigned v1 = 24 + (rb >> 1), v2 = 64 + (rb >> 1), hk = rb & 1, on = v1 < 64 ? ~0u : 0u;
  unsigned rs1 = ((unit_src(v1, h, w) + hk * 65536) & on) | (((1 - h) << 20) & ~on), rd1 = ((v1 * 8192 + hk * 1024) & on) | ((192 * 8192) & ~on);
  unsigned rs2 = unit_src(v2, h, w) + hk * 65536, rn1 = v2 * 8192 + hk * 1024;
  unsigned wfa = kWFlagOff + (4 * h + w) * 1024, wfo = wfa + rb * 8;
  asm volatile("" : "+s"(col), "+s"(rows), "+s"(oofs), "+s"(wo), "+s"(eo), "+s"(ep), "+s"(fo), "+s"(fp), "+s"(db), "+s"(rs1), "+s"(rd1), "+s"(rs2), "+s"(rn1), "+s"(wfa), "+s"(wfo));
  __builtin_amdgcn_sched_barrier(0);
  const unsigned long long wsb = (unsigned long long)wsp, bb = (unsigned long long)bs;
  const unsigned long long xo = (unsigned long long)x0 + rows, ob = (unsigned long long)out + oofs, wr = (unsigned long long)ws + col;
  wo += (unsigned)wsb > ~(15u << 20) ? 16u << 20 : kWOff;
  const unsigned xa = (0u - (unsigned)wsb) & 0x1fffffu;
  eo += xa; ep += xa;
  const unsigned ldsa = (unsigned)(size_t)(__attribute__((address_space(3))) unsigned char*)lds;
#define LO(x) "s"((unsigned)(x))
#define HI(x) "s"((unsigned)((x) >> 32))
  asm volatile(
"L_main_entry:\n"
"v_and_b32 v12, 63, %18\n"
"v_lshrrev_b32 v13, 3, v12\n"
"v_and_b32 v15, 7, v12\n"
"v_lshlrev_b32 v10, 11, v13\n"
"s_andn2_b32 s60, 1024, %17\n"
"s_lshl_b32 s75, s60, 10\n"
"s_mov_b32 s52, %6\n"
"s_and_b32 s53, %7, 0xffff\n"
"s_mov_b32 s54, 12581120\n"
"s_mov_b32 s55, 0x20000\n"
"s_getpc_b64 s[96:97]\n"
"L_pc:\n"
"s_lshr_b32 s66, %26, 3\n"
"s_lshl_b32 s66, s66, 2\n"
"s_add_u32 s66, s66, %16\n"
"s_mul_i32 s66, s66, 640\n"
"s_and_b32 s97, s97, 0xffff\n"
"s_mov_b32 s98, L_end-L_pc\n"
"s_mov_b32 s99, 0x20000\n"
"s_min_u32 s66, s66, L_end-L_pc-640\n"
"v_min_u32 v9, 9, v12\n"
"v_lshlrev_b32 v9, 6, v9\n"
"buffer_load_dword a252, v9, s[96:99], s66 offen\n"
"v_xor_b32 v6, v15, v13\n"
"v_lshl_add_u32 v6, v6, 4, v10\n"
"s_mov_b32 s48, %2\n"
"s_and_b32 s49, %3, 0xffff\n"
"s_mov_b32 s50, 65536\n"
"s_mov_b32 s51, 0x20000\n"
"s_lshl_b32 s66, %16, 16\n"
"s_add_u32 s48, s48, s66\n"
"s_addc_u32 s49, s49, 0\n"
"s_lshl_b32 s72, %16, 12\n"
"s_add_u32 s72, s72, %15\n"
"s_add_u32 m0, s72, 16384\n"
"s_add_u32 s67, s60, 0\n"
"buffer_load_dwordx4 v6, s[48:51], s67 offen offset:0  lds\n"
"s_add_u32 s67, s60, 15360\n"
"buffer_load_dwordx4 v6, s[48:51], s67 offen offset:1024  lds\n"
"s_add_u32 s67, s60, 30720\n"
"buffer_load_dwordx4 v6, s[48:51], s67 offen offset:2048  lds\n"
"s_add_u32 s67, s60, 46080\n"
"buffer_load_dwordx4 v6, s[48:51], s67 offen offset:3072  lds\n"
"s_mul_i32 s73, %16, 40960\n"
"s_add_u32 s64, s73, %15\n"
"v_lshrrev_b32 v11, 2, v12\n"
"v_and_b32 v11, 4, v11\n"
"v_xor_b32 v11, v11, v15\n"
"v_lshl_add_u32 v11, v11, 4, v10\n"
"s_add_u32 m0, s64, 0\n"
"s_add_u32 s67, s75, 0\n"
"buffer_load_dwordx4 v11, s[52:55], s67 offen offset:0  lds\n"
"s_add_u32 s67, s75, 15360\n"
"buffer_load_dwordx4 v11, s[52:55], s67 offen offset:1024  lds\n"
"s_add_u32 s67, s75, 30720\n"
"buffer_load_dwordx4 v11, s[52:55], s67 offen offset:2048  lds\n"
"s_add_u32 s67, s75, 46080\n"
"buffer_load_dwordx4 v11, s[52:55], s67 offen offset:3072  lds\n"
"s_add_u32 m0, s64, 4096\n"
"s_add_u32 s67, s75, 65536\n"
"buffer_load_dwordx4 v11, s[52:55], s67 offen offset:0  lds\n"
"s_add_u32 s67, s75, 80896\n"
"buffer_load_dwordx4 v11, s[52:55], s67 offen offset:1024  lds\n"
"s_add_u32 s67, s75, 96256\n"
"buffer_load_dwordx4 v11, s[52:55], s67 offen offset:2048  lds\n"
"s_add_u32 s67, s75, 111616\n"
"buffer_load_dwordx4 v11, s[52:55], s67 offen offset:3072  lds\n"
"s_lshr_b32 s65, %26, 4\n"
"s_lshl_b32 s65, s65, 2\n"
"s_add_u32 s65, s65, %16\n"
"s_lshl_b32 s65, s65, 11\n"
"s_lshl_b32 s67, %16, 8\n"
"s_sub_u32 s65, s65, s67\n"
"v_and_b32 v14, 1, v13\n"
"v_lshlrev_b32 v14, 17, v14\n"
"v_lshl_add_u32 v14, v15, 7, v14\n"
"v_and_b32 v14, 0xfffffdff, v14\n"
"s_lshl_b32 s67, %26, 6\n"
"s_and_b32 s67, s67, 512\n"
"s_add_u32 s65, s65, s67\n"
"v_lshlrev_b32 v0, 4, v12\n"
"s_mov_b32 s56, %0\n"
"s_and_b32 s57, %1, 0xffff\n"
"s_mov_b32 s58, 171966464\n"
"s_mov_b32 s59, 0x20000\n"
"v_add_u32 v1, 4096, v0\n"
"s_add_u32 s36, s56, %10\n"
"s_addc_u32 s37, s57, 0\n"
"s_mov_b32 s38, 1572864\n"
"s_mov_b32 s39, 0x20000\n"
"s_add_u32 s34, s36, 200704\n"
"s_mov_b32 s35, s37\n"
"v_and_b32 v13, 15, v12\n"
"v_lshrrev_b32 v10, 4, v12\n"
"v_xor_b32 v15, v15, v10\n"
"v_lshlrev_b32 v7, 7, v13\n"
"v_lshl_add_u32 v7, v15, 4, v7\n"
"v_add_u32 v7, s64, v7\n"
"v_xor_b32 v8, 64, v7\n"
"v_bfe_u32 v13, v12, 2, 2\n"
"v_and_b32 v15, 3, v12\n"
"v_bfe_u32 v9, v12, 3, 1\n"
"v_lshl_add_u32 v15, v9, 2, v15\n"
"v_lshl_add_u32 v13, v10, 3, v13\n"
"v_lshlrev_b32 v13, 7, v13\n"
"v_lshl_add_u32 v9, v15, 4, v13\n"
"v_add_u32 v9, s64, v9\n"
"v_xor_b32 v15, 64, v9\n"
"s_mov_b32 s40, %2\n"
"s_and_b32 s41, %3, 0xffff\n"
"s_mov_b32 s42, 260352\n"
"s_mov_b32 s43, 0x20000\n"
"s_add_u32 s40, s40, %17\n"
"s_addc_u32 s41, s41, 0\n"
"s_mov_b32 s80, 0\n"
"s_mov_b32 s81, 15360\n"
"s_mov_b32 s82, 30720\n"
"s_mov_b32 s83, 46080\n"
"s_mov_b32 s84, 65536\n"
"s_mov_b32 s85, 80896\n"
"s_mov_b32 s86, 96256\n"
"s_mov_b32 s87, 111616\n"
"s_mov_b32 s88, 131072\n"
"s_mov_b32 s89, 146432\n"
"s_mov_b32 s90, 161792\n"
"s_mov_b32 s91, 177152\n"
"s_mov_b32 s92, 196608\n"
"s_mov_b32 s93, 211968\n"
"s_mov_b32 s94, 227328\n"
"s_mov_b32 s95, 242688\n"
"s_mov_b32 s62, 0\n"
"s_mov_b32 s63, 0xffff0000\n"
"s_mov_b32 s68, 0x8000\n"
"s_add_u32 s66, %16, 0\n"
"s_and_b32 s66, s66, 3\n"
"s_mul_i32 s66, s66, 40960\n"
"s_add_u32 s66, s66, %15\n"
"v_add_u32 v2, s66, v0\n"
"s_add_u32 s66, %16, 1\n"
"s_and_b32 s66, s66, 3\n"
"s_mul_i32 s66, s66, 40960\n"
"s_add_u32 s66, s66, %15\n"
"v_add_u32 v3, s66, v0\n"
"s_add_u32 s66, %16, 2\n"
"s_and_b32 s66, s66, 3\n"
"s_mul_i32 s66, s66, 40960\n"
"s_add_u32 s66, s66, %15\n"
"v_add_u32 v4, s66, v0\n"
"s_add_u32 s66, %16, 3\n"
"s_and_b32 s66, s66, 3\n"
"s_mul_i32 s66, s66, 40960\n"
"s_add_u32 s66, s66, %15\n"
"v_add_u32 v5, s66, v0\n"
"s_sub_u32 s66, 0, s73\n"
"v_add_u32 v12, s66, v7\n"
"v_add_u32 v13, s66, v8\n"
"s_mov_b32 s44, %8\n"
"s_and_b32 s45, %9, 0xffff\n"
"s_mov_b32 s46, 12288\n"
"s_mov_b32 s47, 0x20000\n"
"s_mov_b32 s61, %17\n"
"v_lshrrev_b32 v16, 4, v0\n"
"v_and_b32 v16, 0x30, v16\n"
"buffer_load_dwordx2 v[18:19], v16, s[44:47], s61 offen offset:0\n"
"v_lshrrev_b32 v20, 4, v0\n"
"v_and_b32 v20, 0x30, v20\n"
"buffer_load_dwordx2 v[22:23], v20, s[44:47], s61 offen offset:8\n"
"v_lshrrev_b32 v24, 4, v0\n"
"v_and_b32 v24, 0x30, v24\n"
"buffer_load_dwordx2 v[26:27], v24, s[44:47], s61 offen offset:64\n"
"v_lshrrev_b32 v28, 4, v0\n"
"v_and_b32 v28, 0x30, v28\n"
"buffer_load_dwordx2 v[30:31], v28, s[44:47], s61 offen offset:72\n"
"s_add_u32 s70, s75, 131072\n"
"s_add_u32 s70, s70, s65\n"
"buffer_load_dword a252, v14, s[52:55], s70 offen\n"
"s_mov_b32 s67, 139264\n"
"buffer_load_dwordx2 v[252:253], off, s[56:59], s67 sc1\n"
"s_waitcnt vmcnt(14)\n"
"s_barrier\n"
"ds_read_b128 a[100:103], v13 offset:16384\n"
"ds_read_b128 a[96:99], v12 offset:16384\n"
"ds_read_b128 a[108:111], v13 offset:18432\n"
"ds_read_b128 a[104:107], v12 offset:18432\n"
"ds_read_b128 a[116:119], v13 offset:20480\n"
"ds_read_b128 a[112:115], v12 offset:20480\n"
"ds_read_b128 a[124:127], v13 offset:22528\n"
"ds_read_b128 a[120:123], v12 offset:22528\n"
"ds_read_b128 a[132:135], v13 offset:24576\n"
"ds_read_b128 a[128:131], v12 offset:24576\n"
"ds_read_b128 a[140:143], v13 offset:26624\n"
"ds_read_b128 a[136:139], v12 offset:26624\n"
"ds_read_b128 a[148:151], v13 offset:28672\n"
"ds_read_b128 a[144:147], v12 offset:28672\n"
"ds_read_b128 a[156:159], v13 offset:30720\n"
"ds_read_b128 a[152:155], v12 offset:30720\n"
"s_add_u32 m0, s64, 8192\n"
"s_add_u32 s67, s75, 128\n"
"buffer_load_dwordx4 v11, s[52:55], s67 offen offset:0  lds\n"
"s_add_u32 s67, s75, 15488\n"
"buffer_load_dwordx4 v11, s[52:55], s67 offen offset:1024  lds\n"
"s_add_u32 s67, s75, 30848\n"
"buffer_load_dwordx4 v11, s[52:55], s67 offen offset:2048  lds\n"
"s_add_u32 s67, s75, 46208\n"
"buffer_load_dwordx4 v11, s[52:55], s67 offen offset:3072  lds\n"
"s_add_u32 m0, s64, 12288\n"
"s_add_u32 s67, s75, 65664\n"
"buffer_load_dwordx4 v11, s[52:55], s67 offen offset:0  lds\n"
"s_add_u32 s67, s75, 81024\n"
"buffer_load_dwordx4 v11, s[52:55], s67 offen offset:1024  lds\n"
"s_add_u32 s67, s75, 96384\n"
"buffer_load_dwordx4 v11, s[52:55], s67 offen offset:2048  lds\n"
"s_add_u32 s67, s75, 111744\n"
"buffer_load_dwordx4 v11, s[52:55], s67 offen offset:3072  lds\n"
"s_waitcnt vmcnt(14)\n"
"ds_read_b64_tr_b16 a[32:33], v9 offset:0\n"
"ds_read_b64_tr_b16 a[34:35], v9 offset:512\n"
"ds_read_b64_tr_b16 a[36:37], v9 offset:4096\n"
"ds_read_b64_tr_b16 a[38:39], v9 offset:4608\n"
"ds_read_b64_tr_b16 a[40:41], v9 offset:8\n"
"ds_read_b64_tr_b16 a[42:43], v9 offset:520\n"
"ds_read_b64_tr_b16 a[44:45], v9 offset:4104\n"
"ds_read_b64_tr_b16 a[46:47], v9 offset:4616\n"
"ds_read_b64_tr_b16 a[48:49], v15 offset:0\n"
"ds_read_b64_tr_b16 a[50:51], v15 offset:512\n"
"ds_read_b64_tr_b16 a[52:53], v15 offset:4096\n"
"ds_read_b64_tr_b16 a[54:55], v15 offset:4608\n"
"ds_read_b64_tr_b16 a[56:57], v15 offset:8\n"
"ds_read_b64_tr_b16 a[58:59], v15 offset:520\n"
"ds_read_b64_tr_b16 a[60:61], v15 offset:4104\n"
"ds_read_b64_tr_b16 a[62:63], v15 offset:4616\n"
"s_waitcnt vmcnt(10)\n"
"v_lshlrev_b32 v16, 16, v18\n"
"v_and_b32 v17, s63, v18\n"
"v_lshlrev_b32 v18, 16, v19\n"
"v_and_b32 v19, s63, v19\n"
"v_add_f32 v16, 1.0, v16\n"
"v_add_f32 v17, 1.0, v17\n"
"v_add_f32 v18, 1.0, v18\n"
"v_add_f32 v19, 1.0, v19\n"
"v_lshlrev_b32 v20, 16, v22\n"
"v_and_b32 v21, s63, v22\n"
"v_lshlrev_b32 v22, 16, v23\n"
"v_and_b32 v23, s63, v23\n"
"v_add_f32 v20, 1.0, v20\n"
"v_add_f32 v21, 1.0, v21\n"
"v_add_f32 v22, 1.0, v22\n"
"v_add_f32 v23, 1.0, v23\n"
"v_lshlrev_b32 v24, 16, v26\n"
"v_and_b32 v25, s63, v26\n"
"v_lshlrev_b32 v26, 16, v27\n"
"v_and_b32 v27, s63, v27\n"
"v_add_f32 v24, 1.0, v24\n"
"v_add_f32 v25, 1.0, v25\n"
"v_add_f32 v26, 1.0, v26\n"
"v_add_f32 v27, 1.0, v27\n"
"v_lshlrev_b32 v28, 16, v30\n"
"v_and_b32 v29, s63, v30\n"
"v_lshlrev_b32 v30, 16, v31\n"
"v_and_b32 v31, s63, v31\n"
"v_add_f32 v28, 1.0, v28\n"
"v_add_f32 v29, 1.0, v29\n"
"v_add_f32 v30, 1.0, v30\n"
"v_add_f32 v31, 1.0, v31\n"
"v_accvgpr_write_b32 a0, v16\n"
"v_accvgpr_write_b32 a1, v17\n"
"v_accvgpr_write_b32 a2, v18\n"
"v_accvgpr_write_b32 a3, v19\n"
"v_accvgpr_write_b32 a4, v20\n"
"v_accvgpr_write_b32 a5, v21\n"
"v_accvgpr_write_b32 a6, v22\n"
"v_accvgpr_write_b32 a7, v23\n"
"v_accvgpr_write_b32 a8, v24\n"
"v_accvgpr_write_b32 a9, v25\n"
"v_accvgpr_write_b32 a10, v26\n"
"v_accvgpr_write_b32 a11, v27\n"
"v_accvgpr_write_b32 a12, v28\n"
"v_accvgpr_write_b32 a13, v29\n"
"v_accvgpr_write_b32 a14, v30\n"
"v_accvgpr_write_b32 a15, v31\n"
"s_add_u32 m0, s72, 57344\n"
"s_add_u32 s67, s60, 128\n"
"buffer_load_dwordx4 v6, s[48:51], s67 offen offset:0  lds\n"
"s_add_u32 s67, s60, 15488\n"
"buffer_load_dwordx4 v6, s[48:51], s67 offen offset:1024  lds\n"
"s_add_u32 s67, s60, 30848\n"
"buffer_load_dwordx4 v6, s[48:51], s67 offen offset:2048  lds\n"
"s_add_u32 s67, s60, 46208\n"
"buffer_load_dwordx4 v6, s[48:51], s67 offen offset:3072  lds\n"
"s_bitcmp1_b32 %26, 0\n"
"s_cbranch_scc1 L_z\n"
"s_mov_b64 s[96:97], s[52:53]\n"
"s_cmp_lt_u32 %22, 1572864\n"
"s_cselect_b32 s98, s54, 0\n"
"s_cselect_b32 s38, 8192000, 0\n"
"s_mov_b32 s99, 0x20000\n"
"s_xor_b32 s71, %20, 0x100000\n"
"s_add_u32 s70, %22, 6553600\n"
"s_add_u32 m0, s72, 139264\n"
"s_add_u32 s67, %20, 0\n"
"buffer_load_dwordx4 v11, s[96:99], s67 offen offset:0  lds\n"
"s_add_u32 s67, %20, 15360\n"
"buffer_load_dwordx4 v11, s[96:99], s67 offen offset:1024  lds\n"
"s_add_u32 s67, %20, 30720\n"
"buffer_load_dwordx4 v11, s[96:99], s67 offen offset:2048  lds\n"
"s_add_u32 s67, %20, 46080\n"
"buffer_load_dwordx4 v11, s[96:99], s67 offen offset:3072  lds\n"
"s_add_u32 m0, s72, 98304\n"
"s_add_u32 s67, s71, 1024\n"
"buffer_load_dwordx4 v11, s[96:99], s67 offen offset:0  lds\n"
"s_add_u32 s67, s71, 16384\n"
"buffer_load_dwordx4 v11, s[96:99], s67 offen offset:1024  lds\n"
"s_add_u32 s67, s71, 31744\n"
"buffer_load_dwordx4 v11, s[96:99], s67 offen offset:2048  lds\n"
"s_add_u32 s67, s71, 47104\n"
"buffer_load_dwordx4 v11, s[96:99], s67 offen offset:3072  lds\n"
"s_sub_u32 s67, s72, s64\n"
"s_add_u32 s67, s67, 98304\n"
"v_add_u32 v40, s67, v9\n"
"v_add_u32 v41, s67, v15\n"
"s_sleep 36\n"
"s_waitcnt vmcnt(4)\n"
"ds_read_b64_tr_b16 a[160:161], v40 offset:40960\n"
"ds_read_b64_tr_b16 a[162:163], v40 offset:41472\n"
"ds_read_b64_tr_b16 a[164:165], v40 offset:40968\n"
"ds_read_b64_tr_b16 a[166:167], v40 offset:41480\n"
"ds_read_b64_tr_b16 a[168:169], v41 offset:40960\n"
"ds_read_b64_tr_b16 a[170:171], v41 offset:41472\n"
"ds_read_b64_tr_b16 a[172:173], v41 offset:40968\n"
"ds_read_b64_tr_b16 a[174:175], v41 offset:41480\n"
"s_waitcnt vmcnt(0)\n"
"ds_read_b64_tr_b16 a[176:177], v40 offset:0\n"
"ds_read_b64_tr_b16 a[178:179], v40 offset:512\n"
"ds_read_b64_tr_b16 a[180:181], v40 offset:8\n"
"ds_read_b64_tr_b16 a[182:183], v40 offset:520\n"
"ds_read_b64_tr_b16 a[184:185], v41 offset:0\n"
"ds_read_b64_tr_b16 a[186:187], v41 offset:512\n"
"ds_read_b64_tr_b16 a[188:189], v41 offset:8\n"
"ds_read_b64_tr_b16 a[190:191], v41 offset:520\n"
"s_waitcnt lgkmcnt(14)\n"
"buffer_store_dwordx4 a[160:163], v0, s[36:39], %22 offen offset:0 sc1\n"
"s_waitcnt lgkmcnt(12)\n"
"buffer_store_dwordx4 a[164:167], v0, s[36:39], %22 offen offset:2048 sc1\n"
"s_waitcnt lgkmcnt(10)\n"
"buffer_store_dwordx4 a[168:171], v1, s[36:39], %22 offen offset:0 sc1\n"
"s_waitcnt lgkmcnt(8)\n"
"buffer_store_dwordx4 a[172:175], v1, s[36:39], %22 offen offset:2048 sc1\n"
"s_waitcnt lgkmcnt(6)\n"
"buffer_store_dwordx4 a[176:179], v0, s[36:39], s70 offen offset:0 sc1\n"
"s_waitcnt lgkmcnt(4)\n"
"buffer_store_dwordx4 a[180:183], v0, s[36:39], s70 offen offset:2048 sc1\n"
"s_waitcnt lgkmcnt(2)\n"
"buffer_store_dwordx4 a[184:187], v1, s[36:39], s70 offen offset:0 sc1\n"
"s_waitcnt lgkmcnt(0)\n"
"buffer_store_dwordx4 a[188:191], v1, s[36:39], s70 offen offset:2048 sc1\n"
"L_z:\n"
"s_nop 1\n"
"s_waitcnt lgkmcnt(8)\n"
"v_mfma_f32_16x16x32_bf16 v[32:35], a[32:35], a[96:99], v[16:19]\n"
"s_add_u32 s70, s75, 393216\n"
"v_mfma_f32_16x16x32_bf16 v[32:35], a[36:39], a[100:103], v[32:35]\n"
"s_add_u32 s70, s70, s65\n"
"v_mfma_f32_16x16x32_bf16 v[60:63], a[44:47], a[100:103], v[20:23]\n"
"buffer_load_dword a252, v14, s[52:55], s70 offen\n"
"v_mfma_f32_16x16x32_bf16 v[60:63], a[40:43], a[96:99], v[60:63]\n"
"s_add_u32 m0, s64, 32768\n"
"v_mfma_f32_16x16x32_bf16 v[64:67], a[40:43], a[104:107], v[20:23]\n"
"s_add_u32 s67, s75, 131072\n"
"v_mfma_f32_16x16x32_bf16 v[64:67], a[44:47], a[108:111], v[64:67]\n"
"buffer_load_dwordx4 v11, s[52:55], s67 offen offset:0  lds\n"
"v_mfma_f32_16x16x32_bf16 v[36:39], a[36:39], a[108:111], v[16:19]\n"
"s_add_u32 s67, s75, 146432\n"
"v_mfma_f32_16x16x32_bf16 v[36:39], a[32:35], a[104:107], v[36:39]\n"
"v_mfma_f32_16x16x32_bf16 v[40:43], a[32:35], a[112:115], v[16:19]\n"
"s_waitcnt lgkmcnt(0)\n"
"buffer_load_dwordx4 v11, s[52:55], s67 offen offset:1024  lds\n"
"v_mfma_f32_16x16x32_bf16 v[40:43], a[36:39], a[116:119], v[40:43]\n"
"v_mfma_f32_16x16x32_bf16 v[68:71], a[44:47], a[116:119], v[20:23]\n"
"s_add_u32 s67, s75, 161792\n"
"v_mfma_f32_16x16x32_bf16 v[68:71], a[40:43], a[112:115], v[68:71]\n"
"v_mfma_f32_16x16x32_bf16 v[72:75], a[40:43], a[120:123], v[20:23]\n"
"buffer_load_dwordx4 v11, s[52:55], s67 offen offset:2048  lds\n"
"v_mfma_f32_16x16x32_bf16 v[72:75], a[44:47], a[124:127], v[72:75]\n"
"v_mfma_f32_16x16x32_bf16 v[44:47], a[36:39], a[124:127], v[16:19]\n"
"s_add_u32 s67, s75, 177152\n"
"v_mfma_f32_16x16x32_bf16 v[44:47], a[32:35], a[120:123], v[44:47]\n"
"v_mfma_f32_16x16x32_bf16 v[48:51], a[32:35], a[128:131], v[16:19]\n"
"buffer_load_dwordx4 v11, s[52:55], s67 offen offset:3072  lds\n"
"v_mfma_f32_16x16x32_bf16 v[48:51], a[36:39], a[132:135], v[48:51]\n"
"v_mfma_f32_16x16x32_bf16 v[76:79], a[44:47], a[132:135], v[20:23]\n"
"s_add_u32 m0, s64, 36864\n"
"v_mfma_f32_16x16x32_bf16 v[76:79], a[40:43], a[128:131], v[76:79]\n"
"v_mfma_f32_16x16x32_bf16 v[80:83], a[40:43], a[136:139], v[20:23]\n"
"s_add_u32 s67, s75, 196608\n"
"v_mfma_f32_16x16x32_bf16 v[80:83], a[44:47], a[140:143], v[80:83]\n"
"v_mfma_f32_16x16x32_bf16 v[52:55], a[36:39], a[140:143], v[16:19]\n"
"buffer_load_dwordx4 v11, s[52:55], s67 offen offset:0  lds\n"
"v_mfma_f32_16x16x32_bf16 v[52:55], a[32:35], a[136:139], v[52:55]\n"
"v_mfma_f32_16x16x32_bf16 v[56:59], a[32:35], a[144:147], v[16:19]\n"
"s_add_u32 s67, s75, 211968\n"
"v_mfma_f32_16x16x32_bf16 v[56:59], a[36:39], a[148:151], v[56:59]\n"
"v_mfma_f32_16x16x32_bf16 v[84:87], a[44:47], a[148:151], v[20:23]\n"
"buffer_load_dwordx4 v11, s[52:55], s67 offen offset:1024  lds\n"
"v_mfma_f32_16x16x32_bf16 v[84:87], a[40:43], a[144:147], v[84:87]\n"
"v_mfma_f32_16x16x32_bf16 a[4:7], a[40:43], a[152:155], a[4:7]\n"
"s_add_u32 s67, s75, 227328\n"
"v_mfma_f32_16x16x32_bf16 a[4:7], a[44:47], a[156:159], a[4:7]\n"
"v_mfma_f32_16x16x32_bf16 a[0:3], a[36:39], a[156:159], a[0:3]\n"
"buffer_load_dwordx4 v11, s[52:55], s67 offen offset:2048  lds\n"
"v_mfma_f32_16x16x32_bf16 a[0:3], a[32:35], a[152:155], a[0:3]\n"
"v_mfma_f32_16x16x32_bf16 v[88:91], a[48:51], a[96:99], v[24:27]\n"
"s_add_u32 s67, s75, 242688\n"
"v_mfma_f32_16x16x32_bf16 v[88:91], a[52:55], a[100:103], v[88:91]\n"
"v_mfma_f32_16x16x32_bf16 v[116:119], a[60:63], a[100:103], v[28:31]\n"
"v_mfma_f32_16x16x32_bf16 v[116:119], a[56:59], a[96:99], v[116:119]\n"
"buffer_load_dwordx4 v11, s[52:55], s67 offen offset:3072  lds\n"
"v_mfma_f32_16x16x32_bf16 v[120:123], a[56:59], a[104:107], v[28:31]\n"
"s_add_u32 m0, s64, 0\n"
"v_mfma_f32_16x16x32_bf16 v[120:123], a[60:63], a[108:111], v[120:123]\n"
"v_mfma_f32_16x16x32_bf16 v[92:95], a[52:55], a[108:111], v[24:27]\n"
"s_add_u32 s67, s75, 131200\n"
"v_mfma_f32_16x16x32_bf16 v[92:95], a[48:51], a[104:107], v[92:95]\n"
"v_mfma_f32_16x16x32_bf16 v[96:99], a[48:51], a[112:115], v[24:27]\n"
"buffer_load_dwordx4 v11, s[52:55], s67 offen offset:0  lds\n"
"v_mfma_f32_16x16x32_bf16 v[96:99], a[52:55], a[116:119], v[96:99]\n"
"v_mfma_f32_16x16x32_bf16 v[124:127], a[60:63], a[116:119], v[28:31]\n"
"s_add_u32 s67, s75, 146560\n"
"v_mfma_f32_16x16x32_bf16 v[124:127], a[56:59], a[112:115], v[124:127]\n"
"buffer_load_dwordx4 v11, s[52:55], s67 offen offset:1024  lds\n"
"v_mfma_f32_16x16x32_bf16 v[128:131], a[56:59], a[120:123], v[28:31]\n"
"s_waitcnt vmcnt(15)\n"
"ds_read_b64_tr_b16 a[64:65], v9 offset:8192\n"
"v_mfma_f32_16x16x32_bf16 v[128:131], a[60:63], a[124:127], v[128:131]\n"
"s_add_u32 s67, s75, 161920\n"
"ds_read_b64_tr_b16 a[66:67], v9 offset:8704\n"
"v_mfma_f32_16x16x32_bf16 v[100:103], a[52:55], a[124:127], v[24:27]\n"
"ds_read_b64_tr_b16 a[68:69], v9 offset:12288\n"
"v_mfma_f32_16x16x32_bf16 v[100:103], a[48:51], a[120:123], v[100:103]\n"
"ds_read_b64_tr_b16 a[70:71], v9 offset:12800\n"
"v_mfma_f32_16x16x32_bf16 v[104:107], a[48:51], a[128:131], v[24:27]\n"
"ds_read_b64_tr_b16 a[72:73], v9 offset:8200\n"
"v_mfma_f32_16x16x32_bf16 v[104:107], a[52:55], a[132:135], v[104:107]\n"
"ds_read_b64_tr_b16 a[74:75], v9 offset:8712\n"
"buffer_load_dwordx4 v11, s[52:55], s67 offen offset:2048  lds\n"
"v_mfma_f32_16x16x32_bf16 v[132:135], a[60:63], a[132:135], v[28:31]\n"
"ds_read_b64_tr_b16 a[76:77], v9 offset:12296\n"
"v_mfma_f32_16x16x32_bf16 v[132:135], a[56:59], a[128:131], v[132:135]\n"
"s_add_u32 s67, s75, 177280\n"
"ds_read_b64_tr_b16 a[78:79], v9 offset:12808\n"
"v_mfma_f32_16x16x32_bf16 v[136:139], a[56:59], a[136:139], v[28:31]\n"
"buffer_load_dwordx4 v11, s[52:55], s67 offen offset:3072  lds\n"
"v_mfma_f32_16x16x32_bf16 v[136:139], a[60:63], a[140:143], v[136:139]\n"
"ds_read_b64_tr_b16 a[80:81], v15 offset:8192\n"
"v_mfma_f32_16x16x32_bf16 v[108:111], a[52:55], a[140:143], v[24:27]\n"
"s_add_u32 m0, s64, 4096\n"
"ds_read_b64_tr_b16 a[82:83], v15 offset:8704\n"
"v_mfma_f32_16x16x32_bf16 v[108:111], a[48:51], a[136:139], v[108:111]\n"
"ds_read_b64_tr_b16 a[84:85], v15 offset:12288\n"
"v_mfma_f32_16x16x32_bf16 v[112:115], a[48:51], a[144:147], v[24:27]\n"
"s_add_u32 s67, s75, 196736\n"
"ds_read_b64_tr_b16 a[86:87], v15 offset:12800\n"
"v_mfma_f32_16x16x32_bf16 v[112:115], a[52:55], a[148:151], v[112:115]\n"
"ds_read_b64_tr_b16 a[88:89], v15 offset:8200\n"
"v_mfma_f32_16x16x32_bf16 v[140:143], a[60:63], a[148:151], v[28:31]\n"
"ds_read_b64_tr_b16 a[90:91], v15 offset:8712\n"
"buffer_load_dwordx4 v11, s[52:55], s67 offen offset:0  lds\n"
"v_mfma_f32_16x16x32_bf16 v[140:143], a[56:59], a[144:147], v[140:143]\n"
"ds_read_b64_tr_b16 a[92:93], v15 offset:12296\n"
"v_mfma_f32_16x16x32_bf16 a[12:15], a[56:59], a[152:155], a[12:15]\n"
"s_add_u32 s67, s75, 212096\n"
"ds_read_b64_tr_b16 a[94:95], v15 offset:12808\n"
"v_mfma_f32_16x16x32_bf16 a[12:15], a[60:63], a[156:159], a[12:15]\n"
"v_mfma_f32_16x16x32_bf16 a[8:11], a[52:55], a[156:159], a[8:11]\n"
"buffer_load_dwordx4 v11, s[52:55], s67 offen offset:1024  lds\n"
"v_mfma_f32_16x16x32_bf16 a[8:11], a[48:51], a[152:155], a[8:11]\n"
"s_waitcnt lgkmcnt(8)\n"
"v_mfma_f32_16x16x32_bf16 v[144:147], a[64:67], a[96:99], 1.0\n"
"s_add_u32 s67, s75, 227456\n"
"v_mfma_f32_16x16x32_bf16 v[144:147], a[68:71], a[100:103], v[144:147]\n"
"v_mfma_f32_16x16x32_bf16 v[172:175], a[76:79], a[100:103], 1.0\n"
"s_waitcnt lgkmcnt(0)\n"
"v_readfirstlane_b32 s78, v252\n"
"v_readfirstlane_b32 s79, v253\n"
"v_mfma_f32_16x16x32_bf16 v[172:175], a[72:75], a[96:99], v[172:175]\n"
"buffer_load_dwordx4 v11, s[52:55], s67 offen offset:2048  lds\n"
"v_mfma_f32_16x16x32_bf16 v[176:179], a[72:75], a[104:107], 1.0\n"
"v_mfma_f32_16x16x32_bf16 v[176:179], a[76:79], a[108:111], v[176:179]\n"
"s_add_u32 s67, s75, 242816\n"
"v_mfma_f32_16x16x32_bf16 v[148:151], a[68:71], a[108:111], 1.0\n"
"s_add_u32 s78, s78, 16\n"
"v_mfma_f32_16x16x32_bf16 v[148:151], a[64:67], a[104:107], v[148:151]\n"
"s_add_u32 s79, s79, 16\n"
"v_mfma_f32_16x16x32_bf16 v[152:155], a[64:67], a[112:115], 1.0\n"
"s_sub_u32 s66, 40960, s73\n"
"v_mfma_f32_16x16x32_bf16 v[152:155], a[68:71], a[116:119], v[152:155]\n"
"buffer_load_dwordx4 v11, s[52:55], s67 offen offset:3072  lds\n"
"v_mfma_f32_16x16x32_bf16 v[180:183], a[76:79], a[116:119], 1.0\n"
"s_add_u32 m0, s72, 98304\n"
"v_add_u32 v12, s66, v7\n"
"v_mfma_f32_16x16x32_bf16 v[180:183], a[72:75], a[112:115], v[180:183]\n"
"v_mfma_f32_16x16x32_bf16 v[184:187], a[72:75], a[120:123], 1.0\n"
"s_add_u32 s67, s60, 256\n"
"v_add_u32 v13, s66, v8\n"
"v_mfma_f32_16x16x32_bf16 v[184:187], a[76:79], a[124:127], v[184:187]\n"
"buffer_load_dwordx4 v6, s[48:51], s67 offen offset:0  lds\n"
"v_mfma_f32_16x16x32_bf16 v[156:159], a[68:71], a[124:127], 1.0\n"
"v_mfma_f32_16x16x32_bf16 v[156:159], a[64:67], a[120:123], v[156:159]\n"
"s_add_u32 s67, s60, 15616\n"
"v_mfma_f32_16x16x32_bf16 v[160:163], a[64:67], a[128:131], 1.0\n"
"v_mfma_f32_16x16x32_bf16 v[160:163], a[68:71], a[132:135], v[160:163]\n"
"buffer_load_dwordx4 v6, s[48:51], s67 offen offset:1024  lds\n"
"v_mfma_f32_16x16x32_bf16 v[188:191], a[76:79], a[132:135], 1.0\n"
"v_mfma_f32_16x16x32_bf16 v[188:191], a[72:75], a[128:131], v[188:191]\n"
"s_add_u32 s67, s60, 30976\n"
"v_mfma_f32_16x16x32_bf16 v[192:195], a[72:75], a[136:139], 1.0\n"
"v_mfma_f32_16x16x32_bf16 v[192:195], a[76:79], a[140:143], v[192:195]\n"
"buffer_load_dwordx4 v6, s[48:51], s67 offen offset:2048  lds\n"
"v_mfma_f32_16x16x32_bf16 v[164:167], a[68:71], a[140:143], 1.0\n"
"v_mfma_f32_16x16x32_bf16 v[164:167], a[64:67], a[136:139], v[164:167]\n"
"s_add_u32 s67, s60, 46336\n"
"v_mfma_f32_16x16x32_bf16 v[168:171], a[64:67], a[144:147], 1.0\n"
"v_mfma_f32_16x16x32_bf16 v[168:171], a[68:71], a[148:151], v[168:171]\n"
"v_mfma_f32_16x16x32_bf16 v[196:199], a[76:79], a[148:151], 1.0\n"
"buffer_load_dwordx4 v6, s[48:51], s67 offen offset:3072  lds\n"
"v_mfma_f32_16x16x32_bf16 v[196:199], a[72:75], a[144:147], v[196:199]\n"
"v_mfma_f32_16x16x32_bf16 a[20:23], a[72:75], a[152:155], 1.0\n"
"v_mfma_f32_16x16x32_bf16 a[20:23], a[76:79], a[156:159], a[20:23]\n"
"v_mfma_f32_16x16x32_bf16 a[16:19], a[68:71], a[156:159], 1.0\n"
"v_mfma_f32_16x16x32_bf16 a[16:19], a[64:67], a[152:155], a[16:19]\n"
"s_waitcnt vmcnt(21)\n"
"s_barrier\n"
"v_mfma_f32_16x16x32_bf16 v[200:203], a[80:83], a[96:99], 1.0\n"
"s_waitcnt vmcnt(12)\n"
"ds_read_b64_tr_b16 a[32:33], v9 offset:32768\n"
"v_mfma_f32_16x16x32_bf16 v[200:203], a[84:87], a[100:103], v[200:203]\n"
"s_add_u32 m0, s64, 8192\n"
"ds_read_b64_tr_b16 a[34:35], v9 offset:33280\n"
"v_mfma_f32_16x16x32_bf16 v[228:231], a[92:95], a[100:103], 1.0\n"
"ds_read_b128 a[100:103], v13 offset:16384\n"
"v_mfma_f32_16x16x32_bf16 v[228:231], a[88:91], a[96:99], v[228:231]\n"
"ds_read_b128 a[96:99], v12 offset:16384\n"
"v_mfma_f32_16x16x32_bf16 v[232:235], a[88:91], a[104:107], 1.0\n"
"ds_read_b64_tr_b16 a[36:37], v9 offset:36864\n"
"v_mfma_f32_16x16x32_bf16 v[232:235], a[92:95], a[108:111], v[232:235]\n"
"ds_read_b64_tr_b16 a[38:39], v9 offset:37376\n"
"v_mfma_f32_16x16x32_bf16 v[204:207], a[84:87], a[108:111], 1.0\n"
"ds_read_b128 a[108:111], v13 offset:18432\n"
"v_mfma_f32_16x16x32_bf16 v[204:207], a[80:83], a[104:107], v[204:207]\n"
"ds_read_b128 a[104:107], v12 offset:18432\n"
"ds_read_b64_tr_b16 a[40:41], v9 offset:32776\n"
"v_mfma_f32_16x16x32_bf16 v[208:211], a[80:83], a[112:115], 1.0\n"
"ds_read_b64_tr_b16 a[42:43], v9 offset:33288\n"
"v_mfma_f32_16x16x32_bf16 v[208:211], a[84:87], a[116:119], v[208:211]\n"
"ds_read_b64_tr_b16 a[44:45], v9 offset:36872\n"
"v_mfma_f32_16x16x32_bf16 v[236:239], a[92:95], a[116:119], 1.0\n"
"ds_read_b128 a[116:119], v13 offset:20480\n"
"v_mfma_f32_16x16x32_bf16 v[236:239], a[88:91], a[112:115], v[236:239]\n"
"ds_read_b128 a[112:115], v12 offset:20480\n"
"v_mfma_f32_16x16x32_bf16 v[240:243], a[88:91], a[120:123], 1.0\n"
"ds_read_b64_tr_b16 a[46:47], v9 offset:37384\n"
"v_mfma_f32_16x16x32_bf16 v[240:243], a[92:95], a[124:127], v[240:243]\n"
"ds_read_b64_tr_b16 a[48:49], v15 offset:32768\n"
"v_mfma_f32_16x16x32_bf16 v[212:215], a[84:87], a[124:127], 1.0\n"
"s_add_u32 s67, s75, 262144\n"
"ds_read_b128 a[124:127], v13 offset:22528\n"
"v_mfma_f32_16x16x32_bf16 v[212:215], a[80:83], a[120:123], v[212:215]\n"
"ds_read_b128 a[120:123], v12 offset:22528\n"
"v_mfma_f32_16x16x32_bf16 v[216:219], a[80:83], a[128:131], 1.0\n"
"ds_read_b64_tr_b16 a[50:51], v15 offset:33280\n"
"v_mfma_f32_16x16x32_bf16 v[216:219], a[84:87], a[132:135], v[216:219]\n"
"ds_read_b64_tr_b16 a[52:53], v15 offset:36864\n"
"v_mfma_f32_16x16x32_bf16 v[244:247], a[92:95], a[132:135], 1.0\n"
"ds_read_b128 a[132:135], v13 offset:24576\n"
"v_mfma_f32_16x16x32_bf16 v[244:247], a[88:91], a[128:131], v[244:247]\n"
"ds_read_b128 a[128:131], v12 offset:24576\n"
"v_mfma_f32_16x16x32_bf16 v[248:251], a[88:91], a[136:139], 1.0\n"
"ds_read_b64_tr_b16 a[54:55], v15 offset:37376\n"
"v_mfma_f32_16x16x32_bf16 v[248:251], a[92:95], a[140:143], v[248:251]\n"
"ds_read_b64_tr_b16 a[56:57], v15 offset:32776\n"
"v_mfma_f32_16x16x32_bf16 v[220:223], a[84:87], a[140:143], 1.0\n"
"ds_read_b128 a[140:143], v13 offset:26624\n"
"v_mfma_f32_16x16x32_bf16 v[220:223], a[80:83], a[136:139], v[220:223]\n"
"ds_read_b128 a[136:139], v12 offset:26624\n"
"v_mfma_f32_16x16x32_bf16 v[224:227], a[80:83], a[144:147], 1.0\n"
"ds_read_b64_tr_b16 a[58:59], v15 offset:33288\n"
"v_mfma_f32_16x16x32_bf16 v[224:227], a[84:87], a[148:151], v[224:227]\n"
"ds_read_b64_tr_b16 a[60:61], v15 offset:36872\n"
"v_mfma_f32_16x16x32_bf16 v[252:255], a[92:95], a[148:151], 1.0\n"
"ds_read_b128 a[148:151], v13 offset:28672\n"
"v_mfma_f32_16x16x32_bf16 v[252:255], a[88:91], a[144:147], v[252:255]\n"
"ds_read_b128 a[144:147], v12 offset:28672\n"
"ds_read_b64_tr_b16 a[62:63], v15 offset:37384\n"
"v_mfma_f32_16x16x32_bf16 a[28:31], a[88:91], a[152:155], 1.0\n"
"v_mfma_f32_16x16x32_bf16 a[28:31], a[92:95], a[156:159], a[28:31]\n"
"v_mfma_f32_16x16x32_bf16 a[24:27], a[84:87], a[156:159], 1.0\n"
"ds_read_b128 a[156:159], v13 offset:30720\n"
"v_mfma_f32_16x16x32_bf16 a[24:27], a[80:83], a[152:155], a[24:27]\n"
"s_waitcnt lgkmcnt(15)\n"
"ds_read_b128 a[152:155], v12 offset:30720\n"
"v_mfma_f32_16x16x32_bf16 v[32:35], a[32:35], a[96:99], v[32:35]\n"
"buffer_load_dwordx4 v11, s[52:55], s67 offen offset:0  lds\n"
"v_mfma_f32_16x16x32_bf16 v[32:35], a[36:39], a[100:103], v[32:35]\n"
"s_waitcnt lgkmcnt(2)\n"
"v_mfma_f32_16x16x32_bf16 v[60:63], a[44:47], a[100:103], v[60:63]\n"
"s_add_u32 s67, s75, 277504\n"
"v_mfma_f32_16x16x32_bf16 v[60:63], a[40:43], a[96:99], v[60:63]\n"
"v_mfma_f32_16x16x32_bf16 v[64:67], a[40:43], a[104:107], v[64:67]\n"
"v_mfma_f32_16x16x32_bf16 v[64:67], a[44:47], a[108:111], v[64:67]\n"
"buffer_load_dwordx4 v11, s[52:55], s67 offen offset:1024  lds\n"
"v_mfma_f32_16x16x32_bf16 v[36:39], a[36:39], a[108:111], v[36:39]\n"
"v_mfma_f32_16x16x32_bf16 v[36:39], a[32:35], a[104:107], v[36:39]\n"
"s_add_u32 s67, s75, 292864\n"
"v_mfma_f32_16x16x32_bf16 v[40:43], a[32:35], a[112:115], v[40:43]\n"
"v_mfma_f32_16x16x32_bf16 v[40:43], a[36:39], a[116:119], v[40:43]\n"
"v_mfma_f32_16x16x32_bf16 v[68:71], a[44:47], a[116:119], v[68:71]\n"
"buffer_load_dwordx4 v11, s[52:55], s67 offen offset:2048  lds\n"
"v_mfma_f32_16x16x32_bf16 v[68:71], a[40:43], a[112:115], v[68:71]\n"
"v_mfma_f32_16x16x32_bf16 v[72:75], a[40:43], a[120:123], v[72:75]\n"
"s_add_u32 s67, s75, 308224\n"
"v_mfma_f32_16x16x32_bf16 v[72:75], a[44:47], a[124:127], v[72:75]\n"
"v_mfma_f32_16x16x32_bf16 v[44:47], a[36:39], a[124:127], v[44:47]\n"
"v_mov_b32 v16, -1\n"
"v_mfma_f32_16x16x32_bf16 v[44:47], a[32:35], a[120:123], v[44:47]\n"
"buffer_load_dwordx4 v11, s[52:55], s67 offen offset:3072  lds\n"
"v_mfma_f32_16x16x32_bf16 v[48:51], a[32:35], a[128:131], v[48:51]\n"
"s_add_u32 m0, s64, 12288\n"
"v_mfma_f32_16x16x32_bf16 v[48:51], a[36:39], a[132:135], v[48:51]\n"
"v_mov_b32 v17, -1\n"
"v_mfma_f32_16x16x32_bf16 v[76:79], a[44:47], a[132:135], v[76:79]\n"
"s_waitcnt vmcnt(8)\n"
"ds_read_b64_tr_b16 a[64:65], v9 offset:0\n"
"v_mfma_f32_16x16x32_bf16 v[76:79], a[40:43], a[128:131], v[76:79]\n"
"s_add_u32 s67, s75, 327680\n"
"ds_read_b64_tr_b16 a[66:67], v9 offset:512\n"
"v_mfma_f32_16x16x32_bf16 v[80:83], a[40:43], a[136:139], v[80:83]\n"
"ds_read_b64_tr_b16 a[68:69], v9 offset:4096\n"
"buffer_load_dwordx4 v11, s[52:55], s67 offen offset:0  lds\n"
"v_mfma_f32_16x16x32_bf16 v[80:83], a[44:47], a[140:143], v[80:83]\n"
"ds_read_b64_tr_b16 a[70:71], v9 offset:4608\n"
"v_mfma_f32_16x16x32_bf16 v[52:55], a[36:39], a[140:143], v[52:55]\n"
"s_add_u32 s67, s75, 343040\n"
"ds_read_b64_tr_b16 a[72:73], v9 offset:8\n"
"v_mfma_f32_16x16x32_bf16 v[52:55], a[32:35], a[136:139], v[52:55]\n"
"s_waitcnt lgkmcnt(5)\n"
"ds_read_b64_tr_b16 a[74:75], v9 offset:520\n"
"v_mfma_f32_16x16x32_bf16 v[56:59], a[32:35], a[144:147], v[56:59]\n"
"ds_read_b64_tr_b16 a[76:77], v9 offset:4104\n"
"buffer_load_dwordx4 v11, s[52:55], s67 offen offset:1024  lds\n"
"v_mfma_f32_16x16x32_bf16 v[56:59], a[36:39], a[148:151], v[56:59]\n"
"s_add_u32 s67, s75, 358400\n"
"ds_read_b64_tr_b16 a[78:79], v9 offset:4616\n"
"v_mfma_f32_16x16x32_bf16 v[84:87], a[44:47], a[148:151], v[84:87]\n"
"s_add_u32 s69, %11, 8192\n"
"v_lshrrev_b32 v18, 1, v0\n"
"v_mfma_f32_16x16x32_bf16 v[84:87], a[40:43], a[144:147], v[84:87]\n"
"ds_read_b64_tr_b16 a[80:81], v15 offset:0\n"
"v_mfma_f32_16x16x32_bf16 a[4:7], a[40:43], a[152:155], a[4:7]\n"
"ds_read_b64_tr_b16 a[82:83], v15 offset:512\n"
"buffer_load_dwordx4 v11, s[52:55], s67 offen offset:2048  lds\n"
"v_mfma_f32_16x16x32_bf16 a[4:7], a[44:47], a[156:159], a[4:7]\n"
"ds_read_b64_tr_b16 a[84:85], v15 offset:4096\n"
"v_mfma_f32_16x16x32_bf16 a[0:3], a[36:39], a[156:159], a[0:3]\n"
"s_add_u32 s67, s75, 373760\n"
"ds_read_b64_tr_b16 a[86:87], v15 offset:4608\n"
"v_mfma_f32_16x16x32_bf16 a[0:3], a[32:35], a[152:155], a[0:3]\n"
"ds_read_b64_tr_b16 a[88:89], v15 offset:8\n"
"v_mfma_f32_16x16x32_bf16 v[88:91], a[48:51], a[96:99], v[88:91]\n"
"ds_read_b64_tr_b16 a[90:91], v15 offset:520\n"
"v_mfma_f32_16x16x32_bf16 v[88:91], a[52:55], a[100:103], v[88:91]\n"
"ds_read_b64_tr_b16 a[92:93], v15 offset:4104\n"
"buffer_load_dwordx4 v11, s[52:55], s67 offen offset:3072  lds\n"
"v_mfma_f32_16x16x32_bf16 v[116:119], a[60:63], a[100:103], v[116:119]\n"
"ds_read_b64_tr_b16 a[94:95], v15 offset:4616\n"
"v_mfma_f32_16x16x32_bf16 v[116:119], a[56:59], a[96:99], v[116:119]\n"
"s_add_u32 m0, s64, 0\n"
"v_mfma_f32_16x16x32_bf16 v[120:123], a[56:59], a[104:107], v[120:123]\n"
"v_mfma_f32_16x16x32_bf16 v[120:123], a[60:63], a[108:111], v[120:123]\n"
"v_mfma_f32_16x16x32_bf16 v[92:95], a[52:55], a[108:111], v[92:95]\n"
"s_add_u32 s67, s75, 262272\n"
"v_mfma_f32_16x16x32_bf16 v[92:95], a[48:51], a[104:107], v[92:95]\n"
"v_mfma_f32_16x16x32_bf16 v[96:99], a[48:51], a[112:115], v[96:99]\n"
"s_waitcnt lgkmcnt(0)\n"
"v_mfma_f32_16x16x32_bf16 v[96:99], a[52:55], a[116:119], v[96:99]\n"
"buffer_load_dwordx4 v11, s[52:55], s67 offen offset:0  lds\n"
"v_mfma_f32_16x16x32_bf16 v[124:127], a[60:63], a[116:119], v[124:127]\n"
"v_mfma_f32_16x16x32_bf16 v[124:127], a[56:59], a[112:115], v[124:127]\n"
"s_add_u32 s67, s75, 277632\n"
"v_mfma_f32_16x16x32_bf16 v[128:131], a[56:59], a[120:123], v[128:131]\n"
"v_mfma_f32_16x16x32_bf16 v[128:131], a[60:63], a[124:127], v[128:131]\n"
"v_mfma_f32_16x16x32_bf16 v[100:103], a[52:55], a[124:127], v[100:103]\n"
"buffer_load_dwordx4 v11, s[52:55], s67 offen offset:1024  lds\n"
"v_mfma_f32_16x16x32_bf16 v[100:103], a[48:51], a[120:123], v[100:103]\n"
"v_mfma_f32_16x16x32_bf16 v[104:107], a[48:51], a[128:131], v[104:107]\n"
"s_add_u32 s67, s75, 292992\n"
"v_mfma_f32_16x16x32_bf16 v[104:107], a[52:55], a[132:135], v[104:107]\n"
"v_mfma_f32_16x16x32_bf16 v[132:135], a[60:63], a[132:135], v[132:135]\n"
"buffer_load_dwordx4 v11, s[52:55], s67 offen offset:2048  lds\n"
"v_mfma_f32_16x16x32_bf16 v[132:135], a[56:59], a[128:131], v[132:135]\n"
"v_mfma_f32_16x16x32_bf16 v[136:139], a[56:59], a[136:139], v[136:139]\n"
"s_add_u32 s67, s75, 308352\n"
"v_mfma_f32_16x16x32_bf16 v[136:139], a[60:63], a[140:143], v[136:139]\n"
"v_mfma_f32_16x16x32_bf16 v[108:111], a[52:55], a[140:143], v[108:111]\n"
"v_mfma_f32_16x16x32_bf16 v[108:111], a[48:51], a[136:139], v[108:111]\n"
"buffer_load_dwordx4 v11, s[52:55], s67 offen offset:3072  lds\n"
"v_mfma_f32_16x16x32_bf16 v[112:115], a[48:51], a[144:147], v[112:115]\n"
"v_mfma_f32_16x16x32_bf16 v[112:115], a[52:55], a[148:151], v[112:115]\n"
"s_add_u32 m0, s64, 4096\n"
"v_mfma_f32_16x16x32_bf16 v[140:143], a[60:63], a[148:151], v[140:143]\n"
"v_mfma_f32_16x16x32_bf16 v[140:143], a[56:59], a[144:147], v[140:143]\n"
"s_add_u32 s67, s75, 327808\n"
"v_mfma_f32_16x16x32_bf16 a[12:15], a[56:59], a[152:155], a[12:15]\n"
"v_mfma_f32_16x16x32_bf16 a[12:15], a[60:63], a[156:159], a[12:15]\n"
"buffer_load_dwordx4 v11, s[52:55], s67 offen offset:0  lds\n"
"v_mfma_f32_16x16x32_bf16 a[8:11], a[52:55], a[156:159], a[8:11]\n"
"v_mfma_f32_16x16x32_bf16 a[8:11], a[48:51], a[152:155], a[8:11]\n"
"s_add_u32 s67, s75, 343168\n"
"v_mfma_f32_16x16x32_bf16 v[144:147], a[64:67], a[96:99], v[144:147]\n"
"v_mfma_f32_16x16x32_bf16 v[144:147], a[68:71], a[100:103], v[144:147]\n"
"buffer_load_dwordx4 v11, s[52:55], s67 offen offset:1024  lds\n"
"v_mfma_f32_16x16x32_bf16 v[172:175], a[76:79], a[100:103], v[172:175]\n"
"v_mfma_f32_16x16x32_bf16 v[172:175], a[72:75], a[96:99], v[172:175]\n"
"s_add_u32 s67, s75, 358528\n"
"v_mfma_f32_16x16x32_bf16 v[176:179], a[72:75], a[104:107], v[176:179]\n"
"v_mfma_f32_16x16x32_bf16 v[176:179], a[76:79], a[108:111], v[176:179]\n"
"buffer_load_dwordx4 v11, s[52:55], s67 offen offset:2048  lds\n"
"v_mfma_f32_16x16x32_bf16 v[148:151], a[68:71], a[108:111], v[148:151]\n"
"v_mfma_f32_16x16x32_bf16 v[148:151], a[64:67], a[104:107], v[148:151]\n"
"s_add_u32 s67, s75, 373888\n"
"v_mfma_f32_16x16x32_bf16 v[152:155], a[64:67], a[112:115], v[152:155]\n"
"s_sub_u32 s66, 81920, s73\n"
"v_mfma_f32_16x16x32_bf16 v[152:155], a[68:71], a[116:119], v[152:155]\n"
"buffer_load_dwordx4 v11, s[52:55], s67 offen offset:3072  lds\n"
"v_mfma_f32_16x16x32_bf16 v[180:183], a[76:79], a[116:119], v[180:183]\n"
"v_add_u32 v12, s66, v7\n"
"v_mfma_f32_16x16x32_bf16 v[180:183], a[72:75], a[112:115], v[180:183]\n"
"s_add_u32 m0, s72, 139264\n"
"v_mfma_f32_16x16x32_bf16 v[184:187], a[72:75], a[120:123], v[184:187]\n"
"v_add_u32 v13, s66, v8\n"
"v_mfma_f32_16x16x32_bf16 v[184:187], a[76:79], a[124:127], v[184:187]\n"
"s_add_u32 s67, s60, 384\n"
"v_mfma_f32_16x16x32_bf16 v[156:159], a[68:71], a[124:127], v[156:159]\n"
"v_mfma_f32_16x16x32_bf16 v[156:159], a[64:67], a[120:123], v[156:159]\n"
"buffer_load_dwordx4 v6, s[48:51], s67 offen offset:0  lds\n"
"v_mfma_f32_16x16x32_bf16 v[160:163], a[64:67], a[128:131], v[160:163]\n"
"v_mfma_f32_16x16x32_bf16 v[160:163], a[68:71], a[132:135], v[160:163]\n"
"s_add_u32 s67, s60, 15744\n"
"v_mfma_f32_16x16x32_bf16 v[188:191], a[76:79], a[132:135], v[188:191]\n"
"v_mfma_f32_16x16x32_bf16 v[188:191], a[72:75], a[128:131], v[188:191]\n"
"buffer_load_dwordx4 v6, s[48:51], s67 offen offset:1024  lds\n"
"v_mfma_f32_16x16x32_bf16 v[192:195], a[72:75], a[136:139], v[192:195]\n"
"v_mfma_f32_16x16x32_bf16 v[192:195], a[76:79], a[140:143], v[192:195]\n"
"s_add_u32 s67, s60, 31104\n"
"v_mfma_f32_16x16x32_bf16 v[164:167], a[68:71], a[140:143], v[164:167]\n"
"v_mfma_f32_16x16x32_bf16 v[164:167], a[64:67], a[136:139], v[164:167]\n"
"buffer_load_dwordx4 v6, s[48:51], s67 offen offset:2048  lds\n"
"v_mfma_f32_16x16x32_bf16 v[168:171], a[64:67], a[144:147], v[168:171]\n"
"v_mfma_f32_16x16x32_bf16 v[168:171], a[68:71], a[148:151], v[168:171]\n"
"s_add_u32 s67, s60, 46464\n"
"v_mfma_f32_16x16x32_bf16 v[196:199], a[76:79], a[148:151], v[196:199]\n"
"v_mfma_f32_16x16x32_bf16 v[196:199], a[72:75], a[144:147], v[196:199]\n"
"v_mfma_f32_16x16x32_bf16 a[20:23], a[72:75], a[152:155], a[20:23]\n"
"buffer_load_dwordx4 v6, s[48:51], s67 offen offset:3072  lds\n"
"v_mfma_f32_16x16x32_bf16 a[20:23], a[76:79], a[156:159], a[20:23]\n"
"s_add_u32 s70, s75, 655360\n"
"v_mfma_f32_16x16x32_bf16 a[16:19], a[68:71], a[156:159], a[16:19]\n"
"s_add_u32 s70, s70, s65\n"
"v_mfma_f32_16x16x32_bf16 a[16:19], a[64:67], a[152:155], a[16:19]\n"
"s_waitcnt vmcnt(20)\n"
"s_barrier\n"
"v_mfma_f32_16x16x32_bf16 v[200:203], a[80:83], a[96:99], v[200:203]\n"
"s_waitcnt vmcnt(12)\n"
"ds_read_b64_tr_b16 a[32:33], v9 offset:8192\n"
"v_mfma_f32_16x16x32_bf16 v[200:203], a[84:87], a[100:103], v[200:203]\n"
"ds_read_b64_tr_b16 a[34:35], v9 offset:8704\n"
"buffer_load_dword a252, v14, s[52:55], s70 offen\n"
"v_mfma_f32_16x16x32_bf16 v[228:231], a[92:95], a[100:103], v[228:231]\n"
"ds_read_b128 a[100:103], v13 offset:16384\n"
"v_mfma_f32_16x16x32_bf16 v[228:231], a[88:91], a[96:99], v[228:231]\n"
"ds_read_b128 a[96:99], v12 offset:16384\n"
"v_mfma_f32_16x16x32_bf16 v[232:235], a[88:91], a[104:107], v[232:235]\n"
"ds_read_b64_tr_b16 a[36:37], v9 offset:12288\n"
"v_mfma_f32_16x16x32_bf16 v[232:235], a[92:95], a[108:111], v[232:235]\n"
"ds_read_b64_tr_b16 a[38:39], v9 offset:12800\n"
"v_mfma_f32_16x16x32_bf16 v[204:207], a[84:87], a[108:111], v[204:207]\n"
"ds_read_b128 a[108:111], v13 offset:18432\n"
"v_mfma_f32_16x16x32_bf16 v[204:207], a[80:83], a[104:107], v[204:207]\n"
"ds_read_b128 a[104:107], v12 offset:18432\n"
"s_add_u32 m0, s64, 8192\n"
"ds_read_b64_tr_b16 a[40:41], v9 offset:8200\n"
"v_mfma_f32_16x16x32_bf16 v[208:211], a[80:83], a[112:115], v[208:211]\n"
"ds_read_b64_tr_b16 a[42:43], v9 offset:8712\n"
"v_mfma_f32_16x16x32_bf16 v[208:211], a[84:87], a[116:119], v[208:211]\n"
"ds_read_b64_tr_b16 a[44:45], v9 offset:12296\n"
"v_mfma_f32_16x16x32_bf16 v[236:239], a[92:95], a[116:119], v[236:239]\n"
"ds_read_b128 a[116:119], v13 offset:20480\n"
"v_mfma_f32_16x16x32_bf16 v[236:239], a[88:91], a[112:115], v[236:239]\n"
"ds_read_b128 a[112:115], v12 offset:20480\n"
"v_mfma_f32_16x16x32_bf16 v[240:243], a[88:91], a[120:123], v[240:243]\n"
"ds_read_b64_tr_b16 a[46:47], v9 offset:12808\n"
"v_mfma_f32_16x16x32_bf16 v[240:243], a[92:95], a[124:127], v[240:243]\n"
"ds_read_b64_tr_b16 a[48:49], v15 offset:8192\n"
"v_mfma_f32_16x16x32_bf16 v[212:215], a[84:87], a[124:127], v[212:215]\n"
"ds_read_b128 a[124:127], v13 offset:22528\n"
"v_mfma_f32_16x16x32_bf16 v[212:215], a[80:83], a[120:123], v[212:215]\n"
"ds_read_b128 a[120:123], v12 offset:22528\n"
"v_mfma_f32_16x16x32_bf16 v[216:219], a[80:83], a[128:131], v[216:219]\n"
"ds_read_b64_tr_b16 a[50:51], v15 offset:8704\n"
"v_mfma_f32_16x16x32_bf16 v[216:219], a[84:87], a[132:135], v[216:219]\n"
"ds_read_b64_tr_b16 a[52:53], v15 offset:12288\n"
"v_mfma_f32_16x16x32_bf16 v[244:247], a[92:95], a[132:135], v[244:247]\n"
"ds_read_b128 a[132:135], v13 offset:24576\n"
"v_mfma_f32_16x16x32_bf16 v[244:247], a[88:91], a[128:131], v[244:247]\n"
"s_add_u32 s67, s75, 393216\n"
"ds_read_b128 a[128:131], v12 offset:24576\n"
"v_mfma_f32_16x16x32_bf16 v[248:251], a[88:91], a[136:139], v[248:251]\n"
"ds_read_b64_tr_b16 a[54:55], v15 offset:12800\n"
"v_mfma_f32_16x16x32_bf16 v[248:251], a[92:95], a[140:143], v[248:251]\n"
"s_add_u32 s66, s78, 1\n"
"ds_read_b64_tr_b16 a[56:57], v15 offset:8200\n"
"v_mfma_f32_16x16x32_bf16 v[220:223], a[84:87], a[140:143], v[220:223]\n"
"ds_read_b128 a[140:143], v13 offset:26624\n"
"v_mfma_f32_16x16x32_bf16 v[220:223], a[80:83], a[136:139], v[220:223]\n"
"ds_read_b128 a[136:139], v12 offset:26624\n"
"v_mov_b32 v28, s66\n"
"v_mfma_f32_16x16x32_bf16 v[224:227], a[80:83], a[144:147], v[224:227]\n"
"ds_read_b64_tr_b16 a[58:59], v15 offset:8712\n"
"v_mov_b32 v29, s79\n"
"v_mfma_f32_16x16x32_bf16 v[224:227], a[84:87], a[148:151], v[224:227]\n"
"ds_read_b64_tr_b16 a[60:61], v15 offset:12296\n"
"v_mfma_f32_16x16x32_bf16 v[252:255], a[92:95], a[148:151], v[252:255]\n"
"buffer_store_dwordx2 v[28:29], off, s[56:59], %24 sc1\n"
"ds_read_b128 a[148:151], v13 offset:28672\n"
"v_mfma_f32_16x16x32_bf16 v[252:255], a[88:91], a[144:147], v[252:255]\n"
"ds_read_b128 a[144:147], v12 offset:28672\n"
"ds_read_b64_tr_b16 a[62:63], v15 offset:12808\n"
"v_mfma_f32_16x16x32_bf16 a[28:31], a[88:91], a[152:155], a[28:31]\n"
"v_mfma_f32_16x16x32_bf16 a[28:31], a[92:95], a[156:159], a[28:31]\n"
"v_mfma_f32_16x16x32_bf16 a[24:27], a[84:87], a[156:159], a[24:27]\n"
"ds_read_b128 a[156:159], v13 offset:30720\n"
"v_mfma_f32_16x16x32_bf16 a[24:27], a[80:83], a[152:155], a[24:27]\n"
"s_waitcnt lgkmcnt(15)\n"
"ds_read_b128 a[152:155], v12 offset:30720\n"
"v_mfma_f32_16x16x32_bf16 v[32:35], a[32:35], a[96:99], v[32:35]\n"
"v_mfma_f32_16x16x32_bf16 v[32:35], a[36:39], a[100:103], v[32:35]\n"
"s_waitcnt lgkmcnt(2)\n"
"v_mfma_f32_16x16x32_bf16 v[60:63], a[44:47], a[100:103], v[60:63]\n"
"buffer_load_dwordx4 v11, s[52:55], s67 offen offset:0  lds\n"
"v_mfma_f32_16x16x32_bf16 v[60:63], a[40:43], a[96:99], v[60:63]\n"
"v_mfma_f32_16x16x32_bf16 v[64:67], a[40:43], a[104:107], v[64:67]\n"
"s_add_u32 s67, s75, 408576\n"
"v_mfma_f32_16x16x32_bf16 v[64:67], a[44:47], a[108:111], v[64:67]\n"
"v_mfma_f32_16x16x32_bf16 v[36:39], a[36:39], a[108:111], v[36:39]\n"
"v_mfma_f32_16x16x32_bf16 v[36:39], a[32:35], a[104:107], v[36:39]\n"
"buffer_load_dwordx4 v11, s[52:55], s67 offen offset:1024  lds\n"
"v_mfma_f32_16x16x32_bf16 v[40:43], a[32:35], a[112:115], v[40:43]\n"
"v_mfma_f32_16x16x32_bf16 v[40:43], a[36:39], a[116:119], v[40:43]\n"
"s_add_u32 s67, s75, 423936\n"
"v_mfma_f32_16x16x32_bf16 v[68:71], a[44:47], a[116:119], v[68:71]\n"
"v_mfma_f32_16x16x32_bf16 v[68:71], a[40:43], a[112:115], v[68:71]\n"
"v_mfma_f32_16x16x32_bf16 v[72:75], a[40:43], a[120:123], v[72:75]\n"
"buffer_load_dwordx4 v11, s[52:55], s67 offen offset:2048  lds\n"
"v_mfma_f32_16x16x32_bf16 v[72:75], a[44:47], a[124:127], v[72:75]\n"
"v_mfma_f32_16x16x32_bf16 v[44:47], a[36:39], a[124:127], v[44:47]\n"
"s_add_u32 s67, s75, 439296\n"
"v_mfma_f32_16x16x32_bf16 v[44:47], a[32:35], a[120:123], v[44:47]\n"
"v_mfma_f32_16x16x32_bf16 v[48:51], a[32:35], a[128:131], v[48:51]\n"
"buffer_load_dwordx4 v11, s[52:55], s67 offen offset:3072  lds\n"
"v_mfma_f32_16x16x32_bf16 v[48:51], a[36:39], a[132:135], v[48:51]\n"
"s_add_u32 m0, s64, 12288\n"
"v_mfma_f32_16x16x32_bf16 v[76:79], a[44:47], a[132:135], v[76:79]\n"
"s_waitcnt vmcnt(10)\n"
"ds_read_b64_tr_b16 a[64:65], v9 offset:0\n"
"v_mfma_f32_16x16x32_bf16 v[76:79], a[40:43], a[128:131], v[76:79]\n"
"ds_read_b64_tr_b16 a[66:67], v9 offset:512\n"
"v_mfma_f32_16x16x32_bf16 v[80:83], a[40:43], a[136:139], v[80:83]\n"
"s_add_u32 s67, s75, 458752\n"
"ds_read_b64_tr_b16 a[68:69], v9 offset:4096\n"
"v_mfma_f32_16x16x32_bf16 v[80:83], a[44:47], a[140:143], v[80:83]\n"
"ds_read_b64_tr_b16 a[70:71], v9 offset:4608\n"
"v_mfma_f32_16x16x32_bf16 v[52:55], a[36:39], a[140:143], v[52:55]\n"
"ds_read_b64_tr_b16 a[72:73], v9 offset:8\n"
"buffer_load_dwordx4 v11, s[52:55], s67 offen offset:0  lds\n"
"v_mfma_f32_16x16x32_bf16 v[52:55], a[32:35], a[136:139], v[52:55]\n"
"s_waitcnt lgkmcnt(5)\n"
"ds_read_b64_tr_b16 a[74:75], v9 offset:520\n"
"v_mfma_f32_16x16x32_bf16 v[56:59], a[32:35], a[144:147], v[56:59]\n"
"s_add_u32 s67, s75, 474112\n"
"ds_read_b64_tr_b16 a[76:77], v9 offset:4104\n"
"v_mfma_f32_16x16x32_bf16 v[56:59], a[36:39], a[148:151], v[56:59]\n"
"ds_read_b64_tr_b16 a[78:79], v9 offset:4616\n"
"v_mfma_f32_16x16x32_bf16 v[84:87], a[44:47], a[148:151], v[84:87]\n"
"buffer_load_dwordx4 v11, s[52:55], s67 offen offset:1024  lds\n"
"v_mfma_f32_16x16x32_bf16 v[84:87], a[40:43], a[144:147], v[84:87]\n"
"ds_read_b64_tr_b16 a[80:81], v15 offset:0\n"
"v_mfma_f32_16x16x32_bf16 a[4:7], a[40:43], a[152:155], a[4:7]\n"
"s_add_u32 s67, s75, 489472\n"
"ds_read_b64_tr_b16 a[82:83], v15 offset:512\n"
"v_mfma_f32_16x16x32_bf16 a[4:7], a[44:47], a[156:159], a[4:7]\n"
"ds_read_b64_tr_b16 a[84:85], v15 offset:4096\n"
"v_mfma_f32_16x16x32_bf16 a[0:3], a[36:39], a[156:159], a[0:3]\n"
"ds_read_b64_tr_b16 a[86:87], v15 offset:4608\n"
"buffer_load_dwordx4 v11, s[52:55], s67 offen offset:2048  lds\n"
"v_mfma_f32_16x16x32_bf16 a[0:3], a[32:35], a[152:155], a[0:3]\n"
"ds_read_b64_tr_b16 a[88:89], v15 offset:8\n"
"v_mfma_f32_16x16x32_bf16 v[88:91], a[48:51], a[96:99], v[88:91]\n"
"s_add_u32 s67, s75, 504832\n"
"ds_read_b64_tr_b16 a[90:91], v15 offset:520\n"
"v_mfma_f32_16x16x32_bf16 v[88:91], a[52:55], a[100:103], v[88:91]\n"
"ds_read_b64_tr_b16 a[92:93], v15 offset:4104\n"
"v_mfma_f32_16x16x32_bf16 v[116:119], a[60:63], a[100:103], v[116:119]\n"
"ds_read_b64_tr_b16 a[94:95], v15 offset:4616\n"
"v_mfma_f32_16x16x32_bf16 v[116:119], a[56:59], a[96:99], v[116:119]\n"
"buffer_load_dwordx4 v11, s[52:55], s67 offen offset:3072  lds\n"
"v_mfma_f32_16x16x32_bf16 v[120:123], a[56:59], a[104:107], v[120:123]\n"
"v_mfma_f32_16x16x32_bf16 v[120:123], a[60:63], a[108:111], v[120:123]\n"
"s_add_u32 m0, s64, 0\n"
"v_mfma_f32_16x16x32_bf16 v[92:95], a[52:55], a[108:111], v[92:95]\n"
"v_mfma_f32_16x16x32_bf16 v[92:95], a[48:51], a[104:107], v[92:95]\n"
"s_add_u32 s67, s75, 393344\n"
"v_mfma_f32_16x16x32_bf16 v[96:99], a[48:51], a[112:115], v[96:99]\n"
"s_waitcnt lgkmcnt(0)\n"
"v_mfma_f32_16x16x32_bf16 v[96:99], a[52:55], a[116:119], v[96:99]\n"
"buffer_load_dwordx4 v11, s[52:55], s67 offen offset:0  lds\n"
"v_mfma_f32_16x16x32_bf16 v[124:127], a[60:63], a[116:119], v[124:127]\n"
"v_mfma_f32_16x16x32_bf16 v[124:127], a[56:59], a[112:115], v[124:127]\n"
"s_add_u32 s67, s75, 408704\n"
"v_mfma_f32_16x16x32_bf16 v[128:131], a[56:59], a[120:123], v[128:131]\n"
"v_mfma_f32_16x16x32_bf16 v[128:131], a[60:63], a[124:127], v[128:131]\n"
"v_mfma_f32_16x16x32_bf16 v[100:103], a[52:55], a[124:127], v[100:103]\n"
"buffer_load_dwordx4 v11, s[52:55], s67 offen offset:1024  lds\n"
"v_mfma_f32_16x16x32_bf16 v[100:103], a[48:51], a[120:123], v[100:103]\n"
"v_mfma_f32_16x16x32_bf16 v[104:107], a[48:51], a[128:131], v[104:107]\n"
"s_add_u32 s67, s75, 424064\n"
"v_mfma_f32_16x16x32_bf16 v[104:107], a[52:55], a[132:135], v[104:107]\n"
"v_mfma_f32_16x16x32_bf16 v[132:135], a[60:63], a[132:135], v[132:135]\n"
"v_mfma_f32_16x16x32_bf16 v[132:135], a[56:59], a[128:131], v[132:135]\n"
"buffer_load_dwordx4 v11, s[52:55], s67 offen offset:2048  lds\n"
"v_mfma_f32_16x16x32_bf16 v[136:139], a[56:59], a[136:139], v[136:139]\n"
"v_mfma_f32_16x16x32_bf16 v[136:139], a[60:63], a[140:143], v[136:139]\n"
"s_add_u32 s67, s75, 439424\n"
"v_mfma_f32_16x16x32_bf16 v[108:111], a[52:55], a[140:143], v[108:111]\n"
"v_mfma_f32_16x16x32_bf16 v[108:111], a[48:51], a[136:139], v[108:111]\n"
"buffer_load_dwordx4 v11, s[52:55], s67 offen offset:3072  lds\n"
"v_mfma_f32_16x16x32_bf16 v[112:115], a[48:51], a[144:147], v[112:115]\n"
"v_mfma_f32_16x16x32_bf16 v[112:115], a[52:55], a[148:151], v[112:115]\n"
"s_add_u32 m0, s64, 4096\n"
"v_mfma_f32_16x16x32_bf16 v[140:143], a[60:63], a[148:151], v[140:143]\n"
"v_mfma_f32_16x16x32_bf16 v[140:143], a[56:59], a[144:147], v[140:143]\n"
"s_add_u32 s67, s75, 458880\n"
"v_mfma_f32_16x16x32_bf16 a[12:15], a[56:59], a[152:155], a[12:15]\n"
"v_mfma_f32_16x16x32_bf16 a[12:15], a[60:63], a[156:159], a[12:15]\n"
"buffer_load_dwordx4 v11, s[52:55], s67 offen offset:0  lds\n"
"v_mfma_f32_16x16x32_bf16 a[8:11], a[52:55], a[156:159], a[8:11]\n"
"v_mfma_f32_16x16x32_bf16 a[8:11], a[48:51], a[152:155], a[8:11]\n"
"s_add_u32 s67, s75, 474240\n"
"v_mfma_f32_16x16x32_bf16 v[144:147], a[64:67], a[96:99], v[144:147]\n"
"v_mfma_f32_16x16x32_bf16 v[144:147], a[68:71], a[100:103], v[144:147]\n"
"buffer_load_dwordx4 v11, s[52:55], s67 offen offset:1024  lds\n"
"v_mfma_f32_16x16x32_bf16 v[172:175], a[76:79], a[100:103], v[172:175]\n"
"v_mfma_f32_16x16x32_bf16 v[172:175], a[72:75], a[96:99], v[172:175]\n"
"s_add_u32 s67, s75, 489600\n"
"v_mfma_f32_16x16x32_bf16 v[176:179], a[72:75], a[104:107], v[176:179]\n"
"v_mfma_f32_16x16x32_bf16 v[176:179], a[76:79], a[108:111], v[176:179]\n"
"buffer_load_dwordx4 v11, s[52:55], s67 offen offset:2048  lds\n"
"v_mfma_f32_16x16x32_bf16 v[148:151], a[68:71], a[108:111], v[148:151]\n"
"v_mfma_f32_16x16x32_bf16 v[148:151], a[64:67], a[104:107], v[148:151]\n"
"s_add_u32 s67, s75, 504960\n"
"v_mfma_f32_16x16x32_bf16 v[152:155], a[64:67], a[112:115], v[152:155]\n"
"s_sub_u32 s66, 122880, s73\n"
"v_mfma_f32_16x16x32_bf16 v[152:155], a[68:71], a[116:119], v[152:155]\n"
"v_mfma_f32_16x16x32_bf16 v[180:183], a[76:79], a[116:119], v[180:183]\n"
"v_add_u32 v12, s66, v7\n"
"v_mfma_f32_16x16x32_bf16 v[180:183], a[72:75], a[112:115], v[180:183]\n"
"buffer_load_dwordx4 v11, s[52:55], s67 offen offset:3072  lds\n"
"v_mfma_f32_16x16x32_bf16 v[184:187], a[72:75], a[120:123], v[184:187]\n"
"s_add_u32 m0, s72, 16384\n"
"v_add_u32 v13, s66, v8\n"
"v_mfma_f32_16x16x32_bf16 v[184:187], a[76:79], a[124:127], v[184:187]\n"
"v_mfma_f32_16x16x32_bf16 v[156:159], a[68:71], a[124:127], v[156:159]\n"
"s_add_u32 s67, s60, 512\n"
"v_mfma_f32_16x16x32_bf16 v[156:159], a[64:67], a[120:123], v[156:159]\n"
"buffer_load_dwordx4 v6, s[48:51], s67 offen offset:0  lds\n"
"v_mfma_f32_16x16x32_bf16 v[160:163], a[64:67], a[128:131], v[160:163]\n"
"v_mfma_f32_16x16x32_bf16 v[160:163], a[68:71], a[132:135], v[160:163]\n"
"s_add_u32 s67, s60, 15872\n"
"v_mfma_f32_16x16x32_bf16 v[188:191], a[76:79], a[132:135], v[188:191]\n"
"v_mfma_f32_16x16x32_bf16 v[188:191], a[72:75], a[128:131], v[188:191]\n"
"buffer_load_dwordx4 v6, s[48:51], s67 offen offset:1024  lds\n"
"v_mfma_f32_16x16x32_bf16 v[192:195], a[72:75], a[136:139], v[192:195]\n"
"v_mfma_f32_16x16x32_bf16 v[192:195], a[76:79], a[140:143], v[192:195]\n"
"s_add_u32 s67, s60, 31232\n"
"v_mfma_f32_16x16x32_bf16 v[164:167], a[68:71], a[140:143], v[164:167]\n"
"v_mfma_f32_16x16x32_bf16 v[164:167], a[64:67], a[136:139], v[164:167]\n"
"buffer_load_dwordx4 v6, s[48:51], s67 offen offset:2048  lds\n"
"v_mfma_f32_16x16x32_bf16 v[168:171], a[64:67], a[144:147], v[168:171]\n"
"v_mfma_f32_16x16x32_bf16 v[168:171], a[68:71], a[148:151], v[168:171]\n"
"s_add_u32 s67, s60, 46592\n"
"v_mfma_f32_16x16x32_bf16 v[196:199], a[76:79], a[148:151], v[196:199]\n"
"v_mfma_f32_16x16x32_bf16 v[196:199], a[72:75], a[144:147], v[196:199]\n"
"v_mfma_f32_16x16x32_bf16 a[20:23], a[72:75], a[152:155], a[20:23]\n"
"buffer_load_dwordx4 v6, s[48:51], s67 offen offset:3072  lds\n"
"v_mfma_f32_16x16x32_bf16 a[20:23], a[76:79], a[156:159], a[20:23]\n"
"v_mfma_f32_16x16x32_bf16 a[16:19], a[68:71], a[156:159], a[16:19]\n"
"s_add_u32 s70, s75, 786432\n"
"s_add_u32 s70, s70, s65\n"
"v_mfma_f32_16x16x32_bf16 a[16:19], a[64:67], a[152:155], a[16:19]\n"
"s_waitcnt vmcnt(22)\n"
"s_barrier\n"
"v_mfma_f32_16x16x32_bf16 v[200:203], a[80:83], a[96:99], v[200:203]\n"
"s_waitcnt vmcnt(12)\n"
"ds_read_b64_tr_b16 a[32:33], v9 offset:8192\n"
"v_mfma_f32_16x16x32_bf16 v[200:203], a[84:87], a[100:103], v[200:203]\n"
"ds_read_b64_tr_b16 a[34:35], v9 offset:8704\n"
"v_mfma_f32_16x16x32_bf16 v[228:231], a[92:95], a[100:103], v[228:231]\n"
"ds_read_b128 a[100:103], v13 offset:16384\n"
"buffer_load_dword a252, v14, s[52:55], s70 offen\n"
"v_mfma_f32_16x16x32_bf16 v[228:231], a[88:91], a[96:99], v[228:231]\n"
"ds_read_b128 a[96:99], v12 offset:16384\n"
"v_mfma_f32_16x16x32_bf16 v[232:235], a[88:91], a[104:107], v[232:235]\n"
"ds_read_b64_tr_b16 a[36:37], v9 offset:12288\n"
"v_mfma_f32_16x16x32_bf16 v[232:235], a[92:95], a[108:111], v[232:235]\n"
"ds_read_b64_tr_b16 a[38:39], v9 offset:12800\n"
"v_mfma_f32_16x16x32_bf16 v[204:207], a[84:87], a[108:111], v[204:207]\n"
"ds_read_b128 a[108:111], v13 offset:18432\n"
"v_mfma_f32_16x16x32_bf16 v[204:207], a[80:83], a[104:107], v[204:207]\n"
"ds_read_b128 a[104:107], v12 offset:18432\n"
"ds_read_b64_tr_b16 a[40:41], v9 offset:8200\n"
"v_mfma_f32_16x16x32_bf16 v[208:211], a[80:83], a[112:115], v[208:211]\n"
"s_add_u32 m0, s64, 8192\n"
"ds_read_b64_tr_b16 a[42:43], v9 offset:8712\n"
"v_mfma_f32_16x16x32_bf16 v[208:211], a[84:87], a[116:119], v[208:211]\n"
"ds_read_b64_tr_b16 a[44:45], v9 offset:12296\n"
"v_mfma_f32_16x16x32_bf16 v[236:239], a[92:95], a[116:119], v[236:239]\n"
"ds_read_b128 a[116:119], v13 offset:20480\n"
"v_mfma_f32_16x16x32_bf16 v[236:239], a[88:91], a[112:115], v[236:239]\n"
"ds_read_b128 a[112:115], v12 offset:20480\n"
"v_mfma_f32_16x16x32_bf16 v[240:243], a[88:91], a[120:123], v[240:243]\n"
"ds_read_b64_tr_b16 a[46:47], v9 offset:12808\n"
"v_mfma_f32_16x16x32_bf16 v[240:243], a[92:95], a[124:127], v[240:243]\n"
"ds_read_b64_tr_b16 a[48:49], v15 offset:8192\n"
"v_mfma_f32_16x16x32_bf16 v[212:215], a[84:87], a[124:127], v[212:215]\n"
"ds_read_b128 a[124:127], v13 offset:22528\n"
"v_mfma_f32_16x16x32_bf16 v[212:215], a[80:83], a[120:123], v[212:215]\n"
"ds_read_b128 a[120:123], v12 offset:22528\n"
"v_mfma_f32_16x16x32_bf16 v[216:219], a[80:83], a[128:131], v[216:219]\n"
"ds_read_b64_tr_b16 a[50:51], v15 offset:8704\n"
"v_mfma_f32_16x16x32_bf16 v[216:219], a[84:87], a[132:135], v[216:219]\n"
"ds_read_b64_tr_b16 a[52:53], v15 offset:12288\n"
"v_mfma_f32_16x16x32_bf16 v[244:247], a[92:95], a[132:135], v[244:247]\n"
"ds_read_b128 a[132:135], v13 offset:24576\n"
"v_mfma_f32_16x16x32_bf16 v[244:247], a[88:91], a[128:131], v[244:247]\n"
"s_add_u32 s67, s75, 524288\n"
"ds_read_b128 a[128:131], v12 offset:24576\n"
"v_mfma_f32_16x16x32_bf16 v[248:251], a[88:91], a[136:139], v[248:251]\n"
"ds_read_b64_tr_b16 a[54:55], v15 offset:12800\n"
"v_mfma_f32_16x16x32_bf16 v[248:251], a[92:95], a[140:143], v[248:251]\n"
"ds_read_b64_tr_b16 a[56:57], v15 offset:8200\n"
"v_mfma_f32_16x16x32_bf16 v[220:223], a[84:87], a[140:143], v[220:223]\n"
"ds_read_b128 a[140:143], v13 offset:26624\n"
"v_mfma_f32_16x16x32_bf16 v[220:223], a[80:83], a[136:139], v[220:223]\n"
"ds_read_b128 a[136:139], v12 offset:26624\n"
"v_mfma_f32_16x16x32_bf16 v[224:227], a[80:83], a[144:147], v[224:227]\n"
"ds_read_b64_tr_b16 a[58:59], v15 offset:8712\n"
"v_mfma_f32_16x16x32_bf16 v[224:227], a[84:87], a[148:151], v[224:227]\n"
"ds_read_b64_tr_b16 a[60:61], v15 offset:12296\n"
"v_mfma_f32_16x16x32_bf16 v[252:255], a[92:95], a[148:151], v[252:255]\n"
"ds_read_b128 a[148:151], v13 offset:28672\n"
"v_mfma_f32_16x16x32_bf16 v[252:255], a[88:91], a[144:147], v[252:255]\n"
"ds_read_b128 a[144:147], v12 offset:28672\n"
"ds_read_b64_tr_b16 a[62:63], v15 offset:12808\n"
"v_mfma_f32_16x16x32_bf16 a[28:31], a[88:91], a[152:155], a[28:31]\n"
"v_mfma_f32_16x16x32_bf16 a[28:31], a[92:95], a[156:159], a[28:31]\n"
"v_mfma_f32_16x16x32_bf16 a[24:27], a[84:87], a[156:159], a[24:27]\n"
"ds_read_b128 a[156:159], v13 offset:30720\n"
"v_mfma_f32_16x16x32_bf16 a[24:27], a[80:83], a[152:155], a[24:27]\n"
"s_waitcnt lgkmcnt(15)\n"
"ds_read_b128 a[152:155], v12 offset:30720\n"
"v_mfma_f32_16x16x32_bf16 v[32:35], a[32:35], a[96:99], v[32:35]\n"
"v_mfma_f32_16x16x32_bf16 v[32:35], a[36:39], a[100:103], v[32:35]\n"
"s_waitcnt lgkmcnt(2)\n"
"v_mfma_f32_16x16x32_bf16 v[60:63], a[44:47], a[100:103], v[60:63]\n"
"buffer_load_dwordx4 v11, s[52:55], s67 offen offset:0  lds\n"
"v_mfma_f32_16x16x32_bf16 v[60:63], a[40:43], a[96:99], v[60:63]\n"
"v_mfma_f32_16x16x32_bf16 v[64:67], a[40:43], a[104:107], v[64:67]\n"
"s_add_u32 s67, s75, 539648\n"
"v_mfma_f32_16x16x32_bf16 v[64:67], a[44:47], a[108:111], v[64:67]\n"
"v_mfma_f32_16x16x32_bf16 v[36:39], a[36:39], a[108:111], v[36:39]\n"
"buffer_load_dwordx4 v11, s[52:55], s67 offen offset:1024  lds\n"
"v_mfma_f32_16x16x32_bf16 v[36:39], a[32:35], a[104:107], v[36:39]\n"
"v_mfma_f32_16x16x32_bf16 v[40:43], a[32:35], a[112:115], v[40:43]\n"
"s_add_u32 s67, s75, 555008\n"
"v_mfma_f32_16x16x32_bf16 v[40:43], a[36:39], a[116:119], v[40:43]\n"
"v_mfma_f32_16x16x32_bf16 v[68:71], a[44:47], a[116:119], v[68:71]\n"
"v_mfma_f32_16x16x32_bf16 v[68:71], a[40:43], a[112:115], v[68:71]\n"
"buffer_load_dwordx4 v11, s[52:55], s67 offen offset:2048  lds\n"
"v_mfma_f32_16x16x32_bf16 v[72:75], a[40:43], a[120:123], v[72:75]\n"
"v_mfma_f32_16x16x32_bf16 v[72:75], a[44:47], a[124:127], v[72:75]\n"
"s_add_u32 s67, s75, 570368\n"
"v_mfma_f32_16x16x32_bf16 v[44:47], a[36:39], a[124:127], v[44:47]\n"
"v_mfma_f32_16x16x32_bf16 v[44:47], a[32:35], a[120:123], v[44:47]\n"
"buffer_load_dwordx4 v11, s[52:55], s67 offen offset:3072  lds\n"
"v_mfma_f32_16x16x32_bf16 v[48:51], a[32:35], a[128:131], v[48:51]\n"
"v_mfma_f32_16x16x32_bf16 v[48:51], a[36:39], a[132:135], v[48:51]\n"
"s_add_u32 m0, s64, 12288\n"
"v_mfma_f32_16x16x32_bf16 v[76:79], a[44:47], a[132:135], v[76:79]\n"
"s_waitcnt vmcnt(9)\n"
"ds_read_b64_tr_b16 a[64:65], v9 offset:0\n"
"v_mfma_f32_16x16x32_bf16 v[76:79], a[40:43], a[128:131], v[76:79]\n"
"s_add_u32 s67, s75, 589824\n"
"ds_read_b64_tr_b16 a[66:67], v9 offset:512\n"
"v_mfma_f32_16x16x32_bf16 v[80:83], a[40:43], a[136:139], v[80:83]\n"
"ds_read_b64_tr_b16 a[68:69], v9 offset:4096\n"
"v_mfma_f32_16x16x32_bf16 v[80:83], a[44:47], a[140:143], v[80:83]\n"
"ds_read_b64_tr_b16 a[70:71], v9 offset:4608\n"
"buffer_load_dwordx4 v11, s[52:55], s67 offen offset:0  lds\n"
"v_mfma_f32_16x16x32_bf16 v[52:55], a[36:39], a[140:143], v[52:55]\n"
"s_add_u32 s67, s75, 605184\n"
"ds_read_b64_tr_b16 a[72:73], v9 offset:8\n"
"v_mfma_f32_16x16x32_bf16 v[52:55], a[32:35], a[136:139], v[52:55]\n"
"s_waitcnt lgkmcnt(5)\n"
"ds_read_b64_tr_b16 a[74:75], v9 offset:520\n"
"v_mfma_f32_16x16x32_bf16 v[56:59], a[32:35], a[144:147], v[56:59]\n"
"ds_read_b64_tr_b16 a[76:77], v9 offset:4104\n"
"v_mfma_f32_16x16x32_bf16 v[56:59], a[36:39], a[148:151], v[56:59]\n"
"ds_read_b64_tr_b16 a[78:79], v9 offset:4616\n"
"v_mfma_f32_16x16x32_bf16 v[84:87], a[44:47], a[148:151], v[84:87]\n"
"buffer_load_dwordx4 v11, s[52:55], s67 offen offset:1024  lds\n"
"v_mfma_f32_16x16x32_bf16 v[84:87], a[40:43], a[144:147], v[84:87]\n"
"ds_read_b64_tr_b16 a[80:81], v15 offset:0\n"
"v_mfma_f32_16x16x32_bf16 a[4:7], a[40:43], a[152:155], a[4:7]\n"
"s_add_u32 s67, s75, 620544\n"
"ds_read_b64_tr_b16 a[82:83], v15 offset:512\n"
"v_mfma_f32_16x16x32_bf16 a[4:7], a[44:47], a[156:159], a[4:7]\n"
"ds_read_b64_tr_b16 a[84:85], v15 offset:4096\n"
"v_mfma_f32_16x16x32_bf16 a[0:3], a[36:39], a[156:159], a[0:3]\n"
"ds_read_b64_tr_b16 a[86:87], v15 offset:4608\n"
"buffer_load_dwordx4 v11, s[52:55], s67 offen offset:2048  lds\n"
"v_mfma_f32_16x16x32_bf16 a[0:3], a[32:35], a[152:155], a[0:3]\n"
"ds_read_b64_tr_b16 a[88:89], v15 offset:8\n"
"v_mfma_f32_16x16x32_bf16 v[88:91], a[48:51], a[96:99], v[88:91]\n"
"s_add_u32 s67, s75, 635904\n"
"ds_read_b64_tr_b16 a[90:91], v15 offset:520\n"
"v_mfma_f32_16x16x32_bf16 v[88:91], a[52:55], a[100:103], v[88:91]\n"
"ds_read_b64_tr_b16 a[92:93], v15 offset:4104\n"
"v_mfma_f32_16x16x32_bf16 v[116:119], a[60:63], a[100:103], v[116:119]\n"
"ds_read_b64_tr_b16 a[94:95], v15 offset:4616\n"
"v_mfma_f32_16x16x32_bf16 v[116:119], a[56:59], a[96:99], v[116:119]\n"
"buffer_load_dwordx4 v11, s[52:55], s67 offen offset:3072  lds\n"
"v_mfma_f32_16x16x32_bf16 v[120:123], a[56:59], a[104:107], v[120:123]\n"
"v_mfma_f32_16x16x32_bf16 v[120:123], a[60:63], a[108:111], v[120:123]\n"
"s_add_u32 m0, s64, 0\n"
"v_mfma_f32_16x16x32_bf16 v[92:95], a[52:55], a[108:111], v[92:95]\n"
"v_mfma_f32_16x16x32_bf16 v[92:95], a[48:51], a[104:107], v[92:95]\n"
"s_add_u32 s67, s75, 524416\n"
"v_mfma_f32_16x16x32_bf16 v[96:99], a[48:51], a[112:115], v[96:99]\n"
"s_waitcnt lgkmcnt(0)\n"
"v_mfma_f32_16x16x32_bf16 v[96:99], a[52:55], a[116:119], v[96:99]\n"
"buffer_load_dwordx4 v11, s[52:55], s67 offen offset:0  lds\n"
"v_mfma_f32_16x16x32_bf16 v[124:127], a[60:63], a[116:119], v[124:127]\n"
"v_mfma_f32_16x16x32_bf16 v[124:127], a[56:59], a[112:115], v[124:127]\n"
"s_add_u32 s67, s75, 539776\n"
"v_mfma_f32_16x16x32_bf16 v[128:131], a[56:59], a[120:123], v[128:131]\n"
"v_mfma_f32_16x16x32_bf16 v[128:131], a[60:63], a[124:127], v[128:131]\n"
"v_mfma_f32_16x16x32_bf16 v[100:103], a[52:55], a[124:127], v[100:103]\n"
"buffer_load_dwordx4 v11, s[52:55], s67 offen offset:1024  lds\n"
"v_mfma_f32_16x16x32_bf16 v[100:103], a[48:51], a[120:123], v[100:103]\n"
"v_mfma_f32_16x16x32_bf16 v[104:107], a[48:51], a[128:131], v[104:107]\n"
"s_add_u32 s67, s75, 555136\n"
"v_mfma_f32_16x16x32_bf16 v[104:107], a[52:55], a[132:135], v[104:107]\n"
"v_mfma_f32_16x16x32_bf16 v[132:135], a[60:63], a[132:135], v[132:135]\n"
"v_mfma_f32_16x16x32_bf16 v[132:135], a[56:59], a[128:131], v[132:135]\n"
"buffer_load_dwordx4 v11, s[52:55], s67 offen offset:2048  lds\n"
"v_mfma_f32_16x16x32_bf16 v[136:139], a[56:59], a[136:139], v[136:139]\n"
"v_mfma_f32_16x16x32_bf16 v[136:139], a[60:63], a[140:143], v[136:139]\n"
"s_add_u32 s67, s75, 570496\n"
"v_mfma_f32_16x16x32_bf16 v[108:111], a[52:55], a[140:143], v[108:111]\n"
"v_mfma_f32_16x16x32_bf16 v[108:111], a[48:51], a[136:139], v[108:111]\n"
"buffer_load_dwordx4 v11, s[52:55], s67 offen offset:3072  lds\n"
"v_mfma_f32_16x16x32_bf16 v[112:115], a[48:51], a[144:147], v[112:115]\n"
"v_mfma_f32_16x16x32_bf16 v[112:115], a[52:55], a[148:151], v[112:115]\n"
"s_add_u32 m0, s64, 4096\n"
"v_mfma_f32_16x16x32_bf16 v[140:143], a[60:63], a[148:151], v[140:143]\n"
"v_mfma_f32_16x16x32_bf16 v[140:143], a[56:59], a[144:147], v[140:143]\n"
"s_add_u32 s67, s75, 589952\n"
"v_mfma_f32_16x16x32_bf16 a[12:15], a[56:59], a[152:155], a[12:15]\n"
"v_mfma_f32_16x16x32_bf16 a[12:15], a[60:63], a[156:159], a[12:15]\n"
"buffer_load_dwordx4 v11, s[52:55], s67 offen offset:0  lds\n"
"v_mfma_f32_16x16x32_bf16 a[8:11], a[52:55], a[156:159], a[8:11]\n"
"v_mfma_f32_16x16x32_bf16 a[8:11], a[48:51], a[152:155], a[8:11]\n"
"s_add_u32 s67, s75, 605312\n"
"v_mfma_f32_16x16x32_bf16 v[144:147], a[64:67], a[96:99], v[144:147]\n"
"v_mfma_f32_16x16x32_bf16 v[144:147], a[68:71], a[100:103], v[144:147]\n"
"buffer_load_dwordx4 v11, s[52:55], s67 offen offset:1024  lds\n"
"v_mfma_f32_16x16x32_bf16 v[172:175], a[76:79], a[100:103], v[172:175]\n"
"v_mfma_f32_16x16x32_bf16 v[172:175], a[72:75], a[96:99], v[172:175]\n"
"s_add_u32 s67, s75, 620672\n"
"v_mfma_f32_16x16x32_bf16 v[176:179], a[72:75], a[104:107], v[176:179]\n"
"v_mfma_f32_16x16x32_bf16 v[176:179], a[76:79], a[108:111], v[176:179]\n"
"buffer_load_dwordx4 v11, s[52:55], s67 offen offset:2048  lds\n"
"v_mfma_f32_16x16x32_bf16 v[148:151], a[68:71], a[108:111], v[148:151]\n"
"v_mfma_f32_16x16x32_bf16 v[148:151], a[64:67], a[104:107], v[148:151]\n"
"s_add_u32 s67, s75, 636032\n"
"v_mfma_f32_16x16x32_bf16 v[152:155], a[64:67], a[112:115], v[152:155]\n"
"s_sub_u32 s66, 0, s73\n"
"v_mfma_f32_16x16x32_bf16 v[152:155], a[68:71], a[116:119], v[152:155]\n"
"v_mfma_f32_16x16x32_bf16 v[180:183], a[76:79], a[116:119], v[180:183]\n"
"v_add_u32 v12, s66, v7\n"
"v_mfma_f32_16x16x32_bf16 v[180:183], a[72:75], a[112:115], v[180:183]\n"
"buffer_load_dwordx4 v11, s[52:55], s67 offen offset:3072  lds\n"
"v_mfma_f32_16x16x32_bf16 v[184:187], a[72:75], a[120:123], v[184:187]\n"
"s_add_u32 m0, s72, 57344\n"
"v_add_u32 v13, s66, v8\n"
"v_mfma_f32_16x16x32_bf16 v[184:187], a[76:79], a[124:127], v[184:187]\n"
"v_mfma_f32_16x16x32_bf16 v[156:159], a[68:71], a[124:127], v[156:159]\n"
"s_add_u32 s67, s60, 640\n"
"v_mfma_f32_16x16x32_bf16 v[156:159], a[64:67], a[120:123], v[156:159]\n"
"buffer_load_dwordx4 v6, s[48:51], s67 offen offset:0  lds\n"
"v_mfma_f32_16x16x32_bf16 v[160:163], a[64:67], a[128:131], v[160:163]\n"
"v_mfma_f32_16x16x32_bf16 v[160:163], a[68:71], a[132:135], v[160:163]\n"
"s_add_u32 s67, s60, 16000\n"
"v_mfma_f32_16x16x32_bf16 v[188:191], a[76:79], a[132:135], v[188:191]\n"
"v_mfma_f32_16x16x32_bf16 v[188:191], a[72:75], a[128:131], v[188:191]\n"
"buffer_load_dwordx4 v6, s[48:51], s67 offen offset:1024  lds\n"
"v_mfma_f32_16x16x32_bf16 v[192:195], a[72:75], a[136:139], v[192:195]\n"
"v_mfma_f32_16x16x32_bf16 v[192:195], a[76:79], a[140:143], v[192:195]\n"
"s_add_u32 s67, s60, 31360\n"
"v_mfma_f32_16x16x32_bf16 v[164:167], a[68:71], a[140:143], v[164:167]\n"
"v_mfma_f32_16x16x32_bf16 v[164:167], a[64:67], a[136:139], v[164:167]\n"
"buffer_load_dwordx4 v6, s[48:51], s67 offen offset:2048  lds\n"
"v_mfma_f32_16x16x32_bf16 v[168:171], a[64:67], a[144:147], v[168:171]\n"
"v_mfma_f32_16x16x32_bf16 v[168:171], a[68:71], a[148:151], v[168:171]\n"
"s_add_u32 s67, s60, 46720\n"
"v_mfma_f32_16x16x32_bf16 v[196:199], a[76:79], a[148:151], v[196:199]\n"
"v_mfma_f32_16x16x32_bf16 v[196:199], a[72:75], a[144:147], v[196:199]\n"
"v_mfma_f32_16x16x32_bf16 a[20:23], a[72:75], a[152:155], a[20:23]\n"
"buffer_load_dwordx4 v6, s[48:51], s67 offen offset:3072  lds\n"
"v_mfma_f32_16x16x32_bf16 a[20:23], a[76:79], a[156:159], a[20:23]\n"
"s_sub_u32 s70, 1048576, s75\n"
"v_and_b32 v22, 0x20000, v14\n"
"v_mfma_f32_16x16x32_bf16 a[16:19], a[68:71], a[156:159], a[16:19]\n"
"v_add_u32 v14, v22, v14\n"
"v_mfma_f32_16x16x32_bf16 a[16:19], a[64:67], a[152:155], a[16:19]\n"
"s_waitcnt vmcnt(21)\n"
"s_barrier\n"
"v_mfma_f32_16x16x32_bf16 v[200:203], a[80:83], a[96:99], v[200:203]\n"
"s_waitcnt vmcnt(12)\n"
"ds_read_b64_tr_b16 a[32:33], v9 offset:8192\n"
"v_mfma_f32_16x16x32_bf16 v[200:203], a[84:87], a[100:103], v[200:203]\n"
"s_add_u32 s70, s70, s65\n"
"ds_read_b64_tr_b16 a[34:35], v9 offset:8704\n"
"s_getpc_b64 s[96:97]\n"
"L_c10_:\n"
"s_add_u32 s96, s96, L_H6S-L_c10_\n"
"s_addc_u32 s97, s97, 0\n"
"v_mfma_f32_16x16x32_bf16 v[228:231], a[92:95], a[100:103], v[228:231]\n"
"s_lshr_b32 s66, %26, 3\n"
"ds_read_b128 a[100:103], v13 offset:16384\n"
"buffer_load_dword a252, v14, s[52:55], s70 offen\n"
"v_mfma_f32_16x16x32_bf16 v[228:231], a[88:91], a[96:99], v[228:231]\n"
"s_lshl2_add_u32 s66, s66, %16\n"
"ds_read_b128 a[96:99], v12 offset:16384\n"
"v_mfma_f32_16x16x32_bf16 v[232:235], a[88:91], a[104:107], v[232:235]\n"
"s_mul_i32 s66, s66, 128\n"
"ds_read_b64_tr_b16 a[36:37], v9 offset:12288\n"
"v_mfma_f32_16x16x32_bf16 v[232:235], a[92:95], a[108:111], v[232:235]\n"
"s_min_u32 s66, s66, L_headF-L_H6S-128\n"
"ds_read_b64_tr_b16 a[38:39], v9 offset:12800\n"
"v_min_u32 v22, 0, v0\n"
"v_mfma_f32_16x16x32_bf16 v[204:207], a[84:87], a[108:111], v[204:207]\n"
"ds_read_b128 a[108:111], v13 offset:18432\n"
"v_lshl_add_u32 v22, v22, 3, s66\n"
"v_mfma_f32_16x16x32_bf16 v[204:207], a[80:83], a[104:107], v[204:207]\n"
"ds_read_b128 a[104:107], v12 offset:18432\n"
"global_load_dword v23, v22, s[96:97]\n"
"ds_read_b64_tr_b16 a[40:41], v9 offset:8200\n"
"v_mfma_f32_16x16x32_bf16 v[208:211], a[80:83], a[112:115], v[208:211]\n"
"s_add_u32 m0, s64, 8192\n"
"ds_read_b64_tr_b16 a[42:43], v9 offset:8712\n"
"v_mfma_f32_16x16x32_bf16 v[208:211], a[84:87], a[116:119], v[208:211]\n"
"ds_read_b64_tr_b16 a[44:45], v9 offset:12296\n"
"v_mfma_f32_16x16x32_bf16 v[236:239], a[92:95], a[116:119], v[236:239]\n"
"ds_read_b128 a[116:119], v13 offset:20480\n"
"v_mfma_f32_16x16x32_bf16 v[236:239], a[88:91], a[112:115], v[236:239]\n"
"ds_read_b128 a[112:115], v12 offset:20480\n"
"v_mfma_f32_16x16x32_bf16 v[240:243], a[88:91], a[120:123], v[240:243]\n"
"ds_read_b64_tr_b16 a[46:47], v9 offset:12808\n"
"v_mfma_f32_16x16x32_bf16 v[240:243], a[92:95], a[124:127], v[240:243]\n"
"ds_read_b64_tr_b16 a[48:49], v15 offset:8192\n"
"v_mfma_f32_16x16x32_bf16 v[212:215], a[84:87], a[124:127], v[212:215]\n"
"ds_read_b128 a[124:127], v13 offset:22528\n"
"v_mfma_f32_16x16x32_bf16 v[212:215], a[80:83], a[120:123], v[212:215]\n"
"ds_read_b128 a[120:123], v12 offset:22528\n"
"v_mfma_f32_16x16x32_bf16 v[216:219], a[80:83], a[128:131], v[216:219]\n"
"ds_read_b64_tr_b16 a[50:51], v15 offset:8704\n"
"v_mfma_f32_16x16x32_bf16 v[216:219], a[84:87], a[132:135], v[216:219]\n"
"ds_read_b64_tr_b16 a[52:53], v15 offset:12288\n"
"v_mfma_f32_16x16x32_bf16 v[244:247], a[92:95], a[132:135], v[244:247]\n"
"ds_read_b128 a[132:135], v13 offset:24576\n"
"v_mfma_f32_16x16x32_bf16 v[244:247], a[88:91], a[128:131], v[244:247]\n"
"s_add_u32 s67, s75, 655360\n"
"ds_read_b128 a[128:131], v12 offset:24576\n"
"v_mfma_f32_16x16x32_bf16 v[248:251], a[88:91], a[136:139], v[248:251]\n"
"ds_read_b64_tr_b16 a[54:55], v15 offset:12800\n"
"v_mfma_f32_16x16x32_bf16 v[248:251], a[92:95], a[140:143], v[248:251]\n"
"ds_read_b64_tr_b16 a[56:57], v15 offset:8200\n"
"v_mfma_f32_16x16x32_bf16 v[220:223], a[84:87], a[140:143], v[220:223]\n"
"ds_read_b128 a[140:143], v13 offset:26624\n"
"v_mfma_f32_16x16x32_bf16 v[220:223], a[80:83], a[136:139], v[220:223]\n"
"ds_read_b128 a[136:139], v12 offset:26624\n"
"v_mfma_f32_16x16x32_bf16 v[224:227], a[80:83], a[144:147], v[224:227]\n"
"ds_read_b64_tr_b16 a[58:59], v15 offset:8712\n"
"v_mfma_f32_16x16x32_bf16 v[224:227], a[84:87], a[148:151], v[224:227]\n"
"ds_read_b64_tr_b16 a[60:61], v15 offset:12296\n"
"v_mfma_f32_16x16x32_bf16 v[252:255], a[92:95], a[148:151], v[252:255]\n"
"ds_read_b128 a[148:151], v13 offset:28672\n"
"v_mfma_f32_16x16x32_bf16 v[252:255], a[88:91], a[144:147], v[252:255]\n"
"ds_read_b128 a[144:147], v12 offset:28672\n"
"ds_read_b64_tr_b16 a[62:63], v15 offset:12808\n"
"v_mfma_f32_16x16x32_bf16 a[28:31], a[88:91], a[152:155], a[28:31]\n"
"v_mfma_f32_16x16x32_bf16 a[28:31], a[92:95], a[156:159], a[28:31]\n"
"v_mfma_f32_16x16x32_bf16 a[24:27], a[84:87], a[156:159], a[24:27]\n"
"ds_read_b128 a[156:159], v13 offset:30720\n"
"v_mfma_f32_16x16x32_bf16 a[24:27], a[80:83], a[152:155], a[24:27]\n"
"s_waitcnt lgkmcnt(15)\n"
"ds_read_b128 a[152:155], v12 offset:30720\n"
"v_mfma_f32_16x16x32_bf16 v[32:35], a[32:35], a[96:99], v[32:35]\n"
"v_mfma_f32_16x16x32_bf16 v[32:35], a[36:39], a[100:103], v[32:35]\n"
"s_waitcnt lgkmcnt(2)\n"
"v_mfma_f32_16x16x32_bf16 v[60:63], a[44:47], a[100:103], v[60:63]\n"
"buffer_load_dwordx4 v11, s[52:55], s67 offen offset:0  lds\n"
"v_mfma_f32_16x16x32_bf16 v[60:63], a[40:43], a[96:99], v[60:63]\n"
"v_mfma_f32_16x16x32_bf16 v[64:67], a[40:43], a[104:107], v[64:67]\n"
"s_add_u32 s67, s75, 670720\n"
"v_mfma_f32_16x16x32_bf16 v[64:67], a[44:47], a[108:111], v[64:67]\n"
"v_mfma_f32_16x16x32_bf16 v[36:39], a[36:39], a[108:111], v[36:39]\n"
"buffer_load_dwordx4 v11, s[52:55], s67 offen offset:1024  lds\n"
"v_mfma_f32_16x16x32_bf16 v[36:39], a[32:35], a[104:107], v[36:39]\n"
"v_mfma_f32_16x16x32_bf16 v[40:43], a[32:35], a[112:115], v[40:43]\n"
"s_add_u32 s67, s75, 686080\n"
"v_mfma_f32_16x16x32_bf16 v[40:43], a[36:39], a[116:119], v[40:43]\n"
"v_mfma_f32_16x16x32_bf16 v[68:71], a[44:47], a[116:119], v[68:71]\n"
"v_mfma_f32_16x16x32_bf16 v[68:71], a[40:43], a[112:115], v[68:71]\n"
"buffer_load_dwordx4 v11, s[52:55], s67 offen offset:2048  lds\n"
"v_mfma_f32_16x16x32_bf16 v[72:75], a[40:43], a[120:123], v[72:75]\n"
"v_mfma_f32_16x16x32_bf16 v[72:75], a[44:47], a[124:127], v[72:75]\n"
"s_add_u32 s67, s75, 701440\n"
"v_mfma_f32_16x16x32_bf16 v[44:47], a[36:39], a[124:127], v[44:47]\n"
"v_mfma_f32_16x16x32_bf16 v[44:47], a[32:35], a[120:123], v[44:47]\n"
"buffer_load_dwordx4 v11, s[52:55], s67 offen offset:3072  lds\n"
"v_mfma_f32_16x16x32_bf16 v[48:51], a[32:35], a[128:131], v[48:51]\n"
"v_mfma_f32_16x16x32_bf16 v[48:51], a[36:39], a[132:135], v[48:51]\n"
"s_add_u32 m0, s64, 12288\n"
"v_mfma_f32_16x16x32_bf16 v[76:79], a[44:47], a[132:135], v[76:79]\n"
"s_waitcnt vmcnt(10)\n"
"ds_read_b64_tr_b16 a[64:65], v9 offset:0\n"
"v_mfma_f32_16x16x32_bf16 v[76:79], a[40:43], a[128:131], v[76:79]\n"
"s_add_u32 s67, s75, 720896\n"
"ds_read_b64_tr_b16 a[66:67], v9 offset:512\n"
"v_mfma_f32_16x16x32_bf16 v[80:83], a[40:43], a[136:139], v[80:83]\n"
"ds_read_b64_tr_b16 a[68:69], v9 offset:4096\n"
"v_mfma_f32_16x16x32_bf16 v[80:83], a[44:47], a[140:143], v[80:83]\n"
"ds_read_b64_tr_b16 a[70:71], v9 offset:4608\n"
"buffer_load_dwordx4 v11, s[52:55], s67 offen offset:0  lds\n"
"v_mfma_f32_16x16x32_bf16 v[52:55], a[36:39], a[140:143], v[52:55]\n"
"s_add_u32 s67, s75, 736256\n"
"ds_read_b64_tr_b16 a[72:73], v9 offset:8\n"
"v_mfma_f32_16x16x32_bf16 v[52:55], a[32:35], a[136:139], v[52:55]\n"
"s_waitcnt lgkmcnt(5)\n"
"ds_read_b64_tr_b16 a[74:75], v9 offset:520\n"
"v_mfma_f32_16x16x32_bf16 v[56:59], a[32:35], a[144:147], v[56:59]\n"
"ds_read_b64_tr_b16 a[76:77], v9 offset:4104\n"
"v_mfma_f32_16x16x32_bf16 v[56:59], a[36:39], a[148:151], v[56:59]\n"
"ds_read_b64_tr_b16 a[78:79], v9 offset:4616\n"
"v_mfma_f32_16x16x32_bf16 v[84:87], a[44:47], a[148:151], v[84:87]\n"
"buffer_load_dwordx4 v11, s[52:55], s67 offen offset:1024  lds\n"
"v_mfma_f32_16x16x32_bf16 v[84:87], a[40:43], a[144:147], v[84:87]\n"
"ds_read_b64_tr_b16 a[80:81], v15 offset:0\n"
"v_mfma_f32_16x16x32_bf16 a[4:7], a[40:43], a[152:155], a[4:7]\n"
"s_add_u32 s67, s75, 751616\n"
"ds_read_b64_tr_b16 a[82:83], v15 offset:512\n"
"v_mfma_f32_16x16x32_bf16 a[4:7], a[44:47], a[156:159], a[4:7]\n"
"ds_read_b64_tr_b16 a[84:85], v15 offset:4096\n"
"v_mfma_f32_16x16x32_bf16 a[0:3], a[36:39], a[156:159], a[0:3]\n"
"ds_read_b64_tr_b16 a[86:87], v15 offset:4608\n"
"buffer_load_dwordx4 v11, s[52:55], s67 offen offset:2048  lds\n"
"v_mfma_f32_16x16x32_bf16 a[0:3], a[32:35], a[152:155], a[0:3]\n"
"ds_read_b64_tr_b16 a[88:89], v15 offset:8\n"
"v_mfma_f32_16x16x32_bf16 v[88:91], a[48:51], a[96:99], v[88:91]\n"
"s_add_u32 s67, s75, 766976\n"
"ds_read_b64_tr_b16 a[90:91], v15 offset:520\n"
"v_mfma_f32_16x16x32_bf16 v[88:91], a[52:55], a[100:103], v[88:91]\n"
"ds_read_b64_tr_b16 a[92:93], v15 offset:4104\n"
"v_mfma_f32_16x16x32_bf16 v[116:119], a[60:63], a[100:103], v[116:119]\n"
"ds_read_b64_tr_b16 a[94:95], v15 offset:4616\n"
"v_mfma_f32_16x16x32_bf16 v[116:119], a[56:59], a[96:99], v[116:119]\n"
"buffer_load_dwordx4 v11, s[52:55], s67 offen offset:3072  lds\n"
"v_mfma_f32_16x16x32_bf16 v[120:123], a[56:59], a[104:107], v[120:123]\n"
"v_mfma_f32_16x16x32_bf16 v[120:123], a[60:63], a[108:111], v[120:123]\n"
"s_add_u32 m0, s64, 0\n"
"v_mfma_f32_16x16x32_bf16 v[92:95], a[52:55], a[108:111], v[92:95]\n"
"v_mfma_f32_16x16x32_bf16 v[92:95], a[48:51], a[104:107], v[92:95]\n"
"s_add_u32 s67, s75, 655488\n"
"v_mfma_f32_16x16x32_bf16 v[96:99], a[48:51], a[112:115], v[96:99]\n"
"s_waitcnt lgkmcnt(0)\n"
"v_mfma_f32_16x16x32_bf16 v[96:99], a[52:55], a[116:119], v[96:99]\n"
"buffer_load_dwordx4 v11, s[52:55], s67 offen offset:0  lds\n"
"v_mfma_f32_16x16x32_bf16 v[124:127], a[60:63], a[116:119], v[124:127]\n"
"v_mfma_f32_16x16x32_bf16 v[124:127], a[56:59], a[112:115], v[124:127]\n"
"s_add_u32 s67, s75, 670848\n"
"v_mfma_f32_16x16x32_bf16 v[128:131], a[56:59], a[120:123], v[128:131]\n"
"v_mfma_f32_16x16x32_bf16 v[128:131], a[60:63], a[124:127], v[128:131]\n"
"v_mfma_f32_16x16x32_bf16 v[100:103], a[52:55], a[124:127], v[100:103]\n"
"buffer_load_dwordx4 v11, s[52:55], s67 offen offset:1024  lds\n"
"v_mfma_f32_16x16x32_bf16 v[100:103], a[48:51], a[120:123], v[100:103]\n"
"v_mfma_f32_16x16x32_bf16 v[104:107], a[48:51], a[128:131], v[104:107]\n"
"s_add_u32 s67, s75, 686208\n"
"v_mfma_f32_16x16x32_bf16 v[104:107], a[52:55], a[132:135], v[104:107]\n"
"v_mfma_f32_16x16x32_bf16 v[132:135], a[60:63], a[132:135], v[132:135]\n"
"v_mfma_f32_16x16x32_bf16 v[132:135], a[56:59], a[128:131], v[132:135]\n"
"buffer_load_dwordx4 v11, s[52:55], s67 offen offset:2048  lds\n"
"v_mfma_f32_16x16x32_bf16 v[136:139], a[56:59], a[136:139], v[136:139]\n"
"v_mfma_f32_16x16x32_bf16 v[136:139], a[60:63], a[140:143], v[136:139]\n"
"s_add_u32 s67, s75, 701568\n"
"v_mfma_f32_16x16x32_bf16 v[108:111], a[52:55], a[140:143], v[108:111]\n"
"v_mfma_f32_16x16x32_bf16 v[108:111], a[48:51], a[136:139], v[108:111]\n"
"buffer_load_dwordx4 v11, s[52:55], s67 offen offset:3072  lds\n"
"v_mfma_f32_16x16x32_bf16 v[112:115], a[48:51], a[144:147], v[112:115]\n"
"v_mfma_f32_16x16x32_bf16 v[112:115], a[52:55], a[148:151], v[112:115]\n"
"s_add_u32 m0, s64, 4096\n"
"v_mfma_f32_16x16x32_bf16 v[140:143], a[60:63], a[148:151], v[140:143]\n"
"v_mfma_f32_16x16x32_bf16 v[140:143], a[56:59], a[144:147], v[140:143]\n"
"s_add_u32 s67, s75, 721024\n"
"v_mfma_f32_16x16x32_bf16 a[12:15], a[56:59], a[152:155], a[12:15]\n"
"v_mfma_f32_16x16x32_bf16 a[12:15], a[60:63], a[156:159], a[12:15]\n"
"buffer_load_dwordx4 v11, s[52:55], s67 offen offset:0  lds\n"
"v_mfma_f32_16x16x32_bf16 a[8:11], a[52:55], a[156:159], a[8:11]\n"
"v_mfma_f32_16x16x32_bf16 a[8:11], a[48:51], a[152:155], a[8:11]\n"
"s_add_u32 s67, s75, 736384\n"
"v_mfma_f32_16x16x32_bf16 v[144:147], a[64:67], a[96:99], v[144:147]\n"
"v_mfma_f32_16x16x32_bf16 v[144:147], a[68:71], a[100:103], v[144:147]\n"
"buffer_load_dwordx4 v11, s[52:55], s67 offen offset:1024  lds\n"
"v_mfma_f32_16x16x32_bf16 v[172:175], a[76:79], a[100:103], v[172:175]\n"
"v_mfma_f32_16x16x32_bf16 v[172:175], a[72:75], a[96:99], v[172:175]\n"
"s_add_u32 s67, s75, 751744\n"
"v_mfma_f32_16x16x32_bf16 v[176:179], a[72:75], a[104:107], v[176:179]\n"
"v_mfma_f32_16x16x32_bf16 v[176:179], a[76:79], a[108:111], v[176:179]\n"
"buffer_load_dwordx4 v11, s[52:55], s67 offen offset:2048  lds\n"
"v_mfma_f32_16x16x32_bf16 v[148:151], a[68:71], a[108:111], v[148:151]\n"
"v_mfma_f32_16x16x32_bf16 v[148:151], a[64:67], a[104:107], v[148:151]\n"
"s_add_u32 s67, s75, 767104\n"
"v_mfma_f32_16x16x32_bf16 v[152:155], a[64:67], a[112:115], v[152:155]\n"
"s_sub_u32 s66, 40960, s73\n"
"v_mfma_f32_16x16x32_bf16 v[152:155], a[68:71], a[116:119], v[152:155]\n"
"v_mfma_f32_16x16x32_bf16 v[180:183], a[76:79], a[116:119], v[180:183]\n"
"v_add_u32 v12, s66, v7\n"
"v_mfma_f32_16x16x32_bf16 v[180:183], a[72:75], a[112:115], v[180:183]\n"
"buffer_load_dwordx4 v11, s[52:55], s67 offen offset:3072  lds\n"
"v_mfma_f32_16x16x32_bf16 v[184:187], a[72:75], a[120:123], v[184:187]\n"
"s_add_u32 m0, s72, 98304\n"
"v_add_u32 v13, s66, v8\n"
"v_mfma_f32_16x16x32_bf16 v[184:187], a[76:79], a[124:127], v[184:187]\n"
"v_mfma_f32_16x16x32_bf16 v[156:159], a[68:71], a[124:127], v[156:159]\n"
"s_add_u32 s67, s60, 768\n"
"v_mfma_f32_16x16x32_bf16 v[156:159], a[64:67], a[120:123], v[156:159]\n"
"buffer_load_dwordx4 v6, s[48:51], s67 offen offset:0  lds\n"
"v_mfma_f32_16x16x32_bf16 v[160:163], a[64:67], a[128:131], v[160:163]\n"
"v_mfma_f32_16x16x32_bf16 v[160:163], a[68:71], a[132:135], v[160:163]\n"
"s_add_u32 s67, s60, 16128\n"
"v_mfma_f32_16x16x32_bf16 v[188:191], a[76:79], a[132:135], v[188:191]\n"
"v_mfma_f32_16x16x32_bf16 v[188:191], a[72:75], a[128:131], v[188:191]\n"
"buffer_load_dwordx4 v6, s[48:51], s67 offen offset:1024  lds\n"
"v_mfma_f32_16x16x32_bf16 v[192:195], a[72:75], a[136:139], v[192:195]\n"
"v_mfma_f32_16x16x32_bf16 v[192:195], a[76:79], a[140:143], v[192:195]\n"
"s_add_u32 s67, s60, 31488\n"
"v_mfma_f32_16x16x32_bf16 v[164:167], a[68:71], a[140:143], v[164:167]\n"
"v_mfma_f32_16x16x32_bf16 v[164:167], a[64:67], a[136:139], v[164:167]\n"
"buffer_load_dwordx4 v6, s[48:51], s67 offen offset:2048  lds\n"
"v_mfma_f32_16x16x32_bf16 v[168:171], a[64:67], a[144:147], v[168:171]\n"
"v_mfma_f32_16x16x32_bf16 v[168:171], a[68:71], a[148:151], v[168:171]\n"
"s_add_u32 s67, s60, 46848\n"
"v_mfma_f32_16x16x32_bf16 v[196:199], a[76:79], a[148:151], v[196:199]\n"
"v_mfma_f32_16x16x32_bf16 v[196:199], a[72:75], a[144:147], v[196:199]\n"
"v_mfma_f32_16x16x32_bf16 a[20:23], a[72:75], a[152:155], a[20:23]\n"
"buffer_load_dwordx4 v6, s[48:51], s67 offen offset:3072  lds\n"
"v_mfma_f32_16x16x32_bf16 a[20:23], a[76:79], a[156:159], a[20:23]\n"
"v_mfma_f32_16x16x32_bf16 a[16:19], a[68:71], a[156:159], a[16:19]\n"
"s_sub_u32 s70, 1572864, s75\n"
"s_add_u32 s70, s70, s65\n"
"v_mfma_f32_16x16x32_bf16 a[16:19], a[64:67], a[152:155], a[16:19]\n"
"s_waitcnt vmcnt(22)\n"
"s_barrier\n"
"v_mfma_f32_16x16x32_bf16 v[200:203], a[80:83], a[96:99], v[200:203]\n"
"s_waitcnt vmcnt(12)\n"
"ds_read_b64_tr_b16 a[32:33], v9 offset:8192\n"
"v_mfma_f32_16x16x32_bf16 v[200:203], a[84:87], a[100:103], v[200:203]\n"
"ds_read_b64_tr_b16 a[34:35], v9 offset:8704\n"
"v_mfma_f32_16x16x32_bf16 v[228:231], a[92:95], a[100:103], v[228:231]\n"
"ds_read_b128 a[100:103], v13 offset:16384\n"
"buffer_load_dword a252, v14, s[52:55], s70 offen\n"
"v_mfma_f32_16x16x32_bf16 v[228:231], a[88:91], a[96:99], v[228:231]\n"
"ds_read_b128 a[96:99], v12 offset:16384\n"
"v_mfma_f32_16x16x32_bf16 v[232:235], a[88:91], a[104:107], v[232:235]\n"
"buffer_store_dwordx2 v[16:17], v18, s[56:59], %11 offen sc1\n"
"ds_read_b64_tr_b16 a[36:37], v9 offset:12288\n"
"v_mfma_f32_16x16x32_bf16 v[232:235], a[92:95], a[108:111], v[232:235]\n"
"ds_read_b64_tr_b16 a[38:39], v9 offset:12800\n"
"v_mfma_f32_16x16x32_bf16 v[204:207], a[84:87], a[108:111], v[204:207]\n"
"ds_read_b128 a[108:111], v13 offset:18432\n"
"v_mfma_f32_16x16x32_bf16 v[204:207], a[80:83], a[104:107], v[204:207]\n"
"ds_read_b128 a[104:107], v12 offset:18432\n"
"buffer_store_dwordx2 v[16:17], v18, s[56:59], s69 offen sc1\n"
"ds_read_b64_tr_b16 a[40:41], v9 offset:8200\n"
"v_mfma_f32_16x16x32_bf16 v[208:211], a[80:83], a[112:115], v[208:211]\n"
"ds_read_b64_tr_b16 a[42:43], v9 offset:8712\n"
"v_mfma_f32_16x16x32_bf16 v[208:211], a[84:87], a[116:119], v[208:211]\n"
"ds_read_b64_tr_b16 a[44:45], v9 offset:12296\n"
"v_mfma_f32_16x16x32_bf16 v[236:239], a[92:95], a[116:119], v[236:239]\n"
"ds_read_b128 a[116:119], v13 offset:20480\n"
"v_mfma_f32_16x16x32_bf16 v[236:239], a[88:91], a[112:115], v[236:239]\n"
"ds_read_b128 a[112:115], v12 offset:20480\n"
"v_mfma_f32_16x16x32_bf16 v[240:243], a[88:91], a[120:123], v[240:243]\n"
"s_add_u32 m0, s64, 8192\n"
"ds_read_b64_tr_b16 a[46:47], v9 offset:12808\n"
"v_mfma_f32_16x16x32_bf16 v[240:243], a[92:95], a[124:127], v[240:243]\n"
"ds_read_b64_tr_b16 a[48:49], v15 offset:8192\n"
"v_mfma_f32_16x16x32_bf16 v[212:215], a[84:87], a[124:127], v[212:215]\n"
"ds_read_b128 a[124:127], v13 offset:22528\n"
"v_mfma_f32_16x16x32_bf16 v[212:215], a[80:83], a[120:123], v[212:215]\n"
"ds_read_b128 a[120:123], v12 offset:22528\n"
"v_mfma_f32_16x16x32_bf16 v[216:219], a[80:83], a[128:131], v[216:219]\n"
"ds_read_b64_tr_b16 a[50:51], v15 offset:8704\n"
"v_mfma_f32_16x16x32_bf16 v[216:219], a[84:87], a[132:135], v[216:219]\n"
"ds_read_b64_tr_b16 a[52:53], v15 offset:12288\n"
"v_mfma_f32_16x16x32_bf16 v[244:247], a[92:95], a[132:135], v[244:247]\n"
"ds_read_b128 a[132:135], v13 offset:24576\n"
"v_mfma_f32_16x16x32_bf16 v[244:247], a[88:91], a[128:131], v[244:247]\n"
"ds_read_b128 a[128:131], v12 offset:24576\n"
"v_mfma_f32_16x16x32_bf16 v[248:251], a[88:91], a[136:139], v[248:251]\n"
"ds_read_b64_tr_b16 a[54:55], v15 offset:12800\n"
"v_mfma_f32_16x16x32_bf16 v[248:251], a[92:95], a[140:143], v[248:251]\n"
"ds_read_b64_tr_b16 a[56:57], v15 offset:8200\n"
"v_mfma_f32_16x16x32_bf16 v[220:223], a[84:87], a[140:143], v[220:223]\n"
"s_add_u32 s67, s75, 786432\n"
"ds_read_b128 a[140:143], v13 offset:26624\n"
"v_mfma_f32_16x16x32_bf16 v[220:223], a[80:83], a[136:139], v[220:223]\n"
"ds_read_b128 a[136:139], v12 offset:26624\n"
"v_mfma_f32_16x16x32_bf16 v[224:227], a[80:83], a[144:147], v[224:227]\n"
"ds_read_b64_tr_b16 a[58:59], v15 offset:8712\n"
"v_mfma_f32_16x16x32_bf16 v[224:227], a[84:87], a[148:151], v[224:227]\n"
"ds_read_b64_tr_b16 a[60:61], v15 offset:12296\n"
"v_mfma_f32_16x16x32_bf16 v[252:255], a[92:95], a[148:151], v[252:255]\n"
"ds_read_b128 a[148:151], v13 offset:28672\n"
"v_mfma_f32_16x16x32_bf16 v[252:255], a[88:91], a[144:147], v[252:255]\n"
"ds_read_b128 a[144:147], v12 offset:28672\n"
"ds_read_b64_tr_b16 a[62:63], v15 offset:12808\n"
"v_mfma_f32_16x16x32_bf16 a[28:31], a[88:91], a[152:155], a[28:31]\n"
"v_mfma_f32_16x16x32_bf16 a[28:31], a[92:95], a[156:159], a[28:31]\n"
"v_mfma_f32_16x16x32_bf16 a[24:27], a[84:87], a[156:159], a[24:27]\n"
"ds_read_b128 a[156:159], v13 offset:30720\n"
"v_mfma_f32_16x16x32_bf16 a[24:27], a[80:83], a[152:155], a[24:27]\n"
"s_waitcnt lgkmcnt(15)\n"
"ds_read_b128 a[152:155], v12 offset:30720\n"
"v_mfma_f32_16x16x32_bf16 v[32:35], a[32:35], a[96:99], v[32:35]\n"
"v_mfma_f32_16x16x32_bf16 v[32:35], a[36:39], a[100:103], v[32:35]\n"
"s_waitcnt lgkmcnt(2)\n"
"v_mfma_f32_16x16x32_bf16 v[60:63], a[44:47], a[100:103], v[60:63]\n"
"buffer_load_dwordx4 v11, s[52:55], s67 offen offset:0  lds\n"
"v_mfma_f32_16x16x32_bf16 v[60:63], a[40:43], a[96:99], v[60:63]\n"
"v_mfma_f32_16x16x32_bf16 v[64:67], a[40:43], a[104:107], v[64:67]\n"
"s_add_u32 s67, s75, 801792\n"
"v_mfma_f32_16x16x32_bf16 v[64:67], a[44:47], a[108:111], v[64:67]\n"
"v_mfma_f32_16x16x32_bf16 v[36:39], a[36:39], a[108:111], v[36:39]\n"
"buffer_load_dwordx4 v11, s[52:55], s67 offen offset:1024  lds\n"
"v_mfma_f32_16x16x32_bf16 v[36:39], a[32:35], a[104:107], v[36:39]\n"
"v_mfma_f32_16x16x32_bf16 v[40:43], a[32:35], a[112:115], v[40:43]\n"
"s_add_u32 s67, s75, 817152\n"
"v_mfma_f32_16x16x32_bf16 v[40:43], a[36:39], a[116:119], v[40:43]\n"
"v_mfma_f32_16x16x32_bf16 v[68:71], a[44:47], a[116:119], v[68:71]\n"
"s_mov_b32 s31, 0\n"
"v_mfma_f32_16x16x32_bf16 v[68:71], a[40:43], a[112:115], v[68:71]\n"
"buffer_load_dwordx4 v11, s[52:55], s67 offen offset:2048  lds\n"
"v_mfma_f32_16x16x32_bf16 v[72:75], a[40:43], a[120:123], v[72:75]\n"
"s_mov_b32 s32, 0\n"
"v_mfma_f32_16x16x32_bf16 v[72:75], a[44:47], a[124:127], v[72:75]\n"
"s_add_u32 s67, s75, 832512\n"
"v_mfma_f32_16x16x32_bf16 v[44:47], a[36:39], a[124:127], v[44:47]\n"
"v_mfma_f32_16x16x32_bf16 v[44:47], a[32:35], a[120:123], v[44:47]\n"
"v_mfma_f32_16x16x32_bf16 v[48:51], a[32:35], a[128:131], v[48:51]\n"
"buffer_load_dwordx4 v11, s[52:55], s67 offen offset:3072  lds\n"
"v_mfma_f32_16x16x32_bf16 v[48:51], a[36:39], a[132:135], v[48:51]\n"
"s_add_u32 m0, s64, 12288\n"
"v_mfma_f32_16x16x32_bf16 v[76:79], a[44:47], a[132:135], v[76:79]\n"
"s_waitcnt vmcnt(11)\n"
"ds_read_b64_tr_b16 a[64:65], v9 offset:0\n"
"v_mfma_f32_16x16x32_bf16 v[76:79], a[40:43], a[128:131], v[76:79]\n"
"s_add_u32 s67, s75, 851968\n"
"ds_read_b64_tr_b16 a[66:67], v9 offset:512\n"
"v_mfma_f32_16x16x32_bf16 v[80:83], a[40:43], a[136:139], v[80:83]\n"
"ds_read_b64_tr_b16 a[68:69], v9 offset:4096\n"
"v_mfma_f32_16x16x32_bf16 v[80:83], a[44:47], a[140:143], v[80:83]\n"
"ds_read_b64_tr_b16 a[70:71], v9 offset:4608\n"
"buffer_load_dwordx4 v11, s[52:55], s67 offen offset:0  lds\n"
"v_mfma_f32_16x16x32_bf16 v[52:55], a[36:39], a[140:143], v[52:55]\n"
"s_add_u32 s67, s75, 867328\n"
"ds_read_b64_tr_b16 a[72:73], v9 offset:8\n"
"v_mfma_f32_16x16x32_bf16 v[52:55], a[32:35], a[136:139], v[52:55]\n"
"s_waitcnt lgkmcnt(5)\n"
"ds_read_b64_tr_b16 a[74:75], v9 offset:520\n"
"v_mfma_f32_16x16x32_bf16 v[56:59], a[32:35], a[144:147], v[56:59]\n"
"ds_read_b64_tr_b16 a[76:77], v9 offset:4104\n"
"v_mfma_f32_16x16x32_bf16 v[56:59], a[36:39], a[148:151], v[56:59]\n"
"ds_read_b64_tr_b16 a[78:79], v9 offset:4616\n"
"v_mfma_f32_16x16x32_bf16 v[84:87], a[44:47], a[148:151], v[84:87]\n"
"buffer_load_dwordx4 v11, s[52:55], s67 offen offset:1024  lds\n"
"v_mfma_f32_16x16x32_bf16 v[84:87], a[40:43], a[144:147], v[84:87]\n"
"ds_read_b64_tr_b16 a[80:81], v15 offset:0\n"
"v_mfma_f32_16x16x32_bf16 a[4:7], a[40:43], a[152:155], a[4:7]\n"
"s_add_u32 s67, s75, 882688\n"
"ds_read_b64_tr_b16 a[82:83], v15 offset:512\n"
"v_mfma_f32_16x16x32_bf16 a[4:7], a[44:47], a[156:159], a[4:7]\n"
"ds_read_b64_tr_b16 a[84:85], v15 offset:4096\n"
"buffer_load_dwordx4 v11, s[52:55], s67 offen offset:2048  lds\n"
"v_mfma_f32_16x16x32_bf16 a[0:3], a[36:39], a[156:159], a[0:3]\n"
"s_add_u32 s96, s56, %11\n"
"ds_read_b64_tr_b16 a[86:87], v15 offset:4608\n"
"s_addc_u32 s97, s57, 0\n"
"v_mfma_f32_16x16x32_bf16 a[0:3], a[32:35], a[152:155], a[0:3]\n"
"ds_read_b64_tr_b16 a[88:89], v15 offset:8\n"
"v_mfma_f32_16x16x32_bf16 v[88:91], a[48:51], a[96:99], v[88:91]\n"
"s_add_u32 s67, s75, 898048\n"
"ds_read_b64_tr_b16 a[90:91], v15 offset:520\n"
"v_mfma_f32_16x16x32_bf16 v[88:91], a[52:55], a[100:103], v[88:91]\n"
"ds_read_b64_tr_b16 a[92:93], v15 offset:4104\n"
"v_mfma_f32_16x16x32_bf16 v[116:119], a[60:63], a[100:103], v[116:119]\n"
"ds_read_b64_tr_b16 a[94:95], v15 offset:4616\n"
"v_mfma_f32_16x16x32_bf16 v[116:119], a[56:59], a[96:99], v[116:119]\n"
"buffer_load_dwordx4 v11, s[52:55], s67 offen offset:3072  lds\n"
"v_mfma_f32_16x16x32_bf16 v[120:123], a[56:59], a[104:107], v[120:123]\n"
"v_mfma_f32_16x16x32_bf16 v[120:123], a[60:63], a[108:111], v[120:123]\n"
"s_add_u32 m0, s64, 0\n"
"v_mfma_f32_16x16x32_bf16 v[92:95], a[52:55], a[108:111], v[92:95]\n"
"v_mfma_f32_16x16x32_bf16 v[92:95], a[48:51], a[104:107], v[92:95]\n"
"s_add_u32 s67, s75, 786560\n"
"v_mfma_f32_16x16x32_bf16 v[96:99], a[48:51], a[112:115], v[96:99]\n"
"s_waitcnt lgkmcnt(0)\n"
"v_mfma_f32_16x16x32_bf16 v[96:99], a[52:55], a[116:119], v[96:99]\n"
"buffer_load_dwordx4 v11, s[52:55], s67 offen offset:0  lds\n"
"v_mfma_f32_16x16x32_bf16 v[124:127], a[60:63], a[116:119], v[124:127]\n"
"s_load_dword s98, s[96:97], 512 glc\n"
"v_mfma_f32_16x16x32_bf16 v[124:127], a[56:59], a[112:115], v[124:127]\n"
"s_load_dword s99, s[96:97], 8704 glc\n"
"s_add_u32 s67, s75, 801920\n"
"v_mfma_f32_16x16x32_bf16 v[128:131], a[56:59], a[120:123], v[128:131]\n"
"v_mfma_f32_16x16x32_bf16 v[128:131], a[60:63], a[124:127], v[128:131]\n"
"buffer_load_dwordx4 v11, s[52:55], s67 offen offset:1024  lds\n"
"v_mfma_f32_16x16x32_bf16 v[100:103], a[52:55], a[124:127], v[100:103]\n"
"v_mfma_f32_16x16x32_bf16 v[100:103], a[48:51], a[120:123], v[100:103]\n"
"s_add_u32 s67, s75, 817280\n"
"v_mfma_f32_16x16x32_bf16 v[104:107], a[48:51], a[128:131], v[104:107]\n"
"v_mfma_f32_16x16x32_bf16 v[104:107], a[52:55], a[132:135], v[104:107]\n"
"v_mfma_f32_16x16x32_bf16 v[132:135], a[60:63], a[132:135], v[132:135]\n"
"buffer_load_dwordx4 v11, s[52:55], s67 offen offset:2048  lds\n"
"v_mfma_f32_16x16x32_bf16 v[132:135], a[56:59], a[128:131], v[132:135]\n"
"v_mfma_f32_16x16x32_bf16 v[136:139], a[56:59], a[136:139], v[136:139]\n"
"s_add_u32 s67, s75, 832640\n"
"v_mfma_f32_16x16x32_bf16 v[136:139], a[60:63], a[140:143], v[136:139]\n"
"v_mfma_f32_16x16x32_bf16 v[108:111], a[52:55], a[140:143], v[108:111]\n"
"buffer_load_dwordx4 v11, s[52:55], s67 offen offset:3072  lds\n"
"v_mfma_f32_16x16x32_bf16 v[108:111], a[48:51], a[136:139], v[108:111]\n"
"v_mfma_f32_16x16x32_bf16 v[112:115], a[48:51], a[144:147], v[112:115]\n"
"s_add_u32 m0, s64, 4096\n"
"v_mfma_f32_16x16x32_bf16 v[112:115], a[52:55], a[148:151], v[112:115]\n"
"v_mfma_f32_16x16x32_bf16 v[140:143], a[60:63], a[148:151], v[140:143]\n"
"s_add_u32 s67, s75, 852096\n"
"v_mfma_f32_16x16x32_bf16 v[140:143], a[56:59], a[144:147], v[140:143]\n"
"v_mfma_f32_16x16x32_bf16 a[12:15], a[56:59], a[152:155], a[12:15]\n"
"buffer_load_dwordx4 v11, s[52:55], s67 offen offset:0  lds\n"
"v_mfma_f32_16x16x32_bf16 a[12:15], a[60:63], a[156:159], a[12:15]\n"
"v_mfma_f32_16x16x32_bf16 a[8:11], a[52:55], a[156:159], a[8:11]\n"
"s_add_u32 s67, s75, 867456\n"
"v_mfma_f32_16x16x32_bf16 a[8:11], a[48:51], a[152:155], a[8:11]\n"
"v_mfma_f32_16x16x32_bf16 v[144:147], a[64:67], a[96:99], v[144:147]\n"
"buffer_load_dwordx4 v11, s[52:55], s67 offen offset:1024  lds\n"
"v_mfma_f32_16x16x32_bf16 v[144:147], a[68:71], a[100:103], v[144:147]\n"
"v_mfma_f32_16x16x32_bf16 v[172:175], a[76:79], a[100:103], v[172:175]\n"
"s_add_u32 s67, s75, 882816\n"
"v_mfma_f32_16x16x32_bf16 v[172:175], a[72:75], a[96:99], v[172:175]\n"
"v_mfma_f32_16x16x32_bf16 v[176:179], a[72:75], a[104:107], v[176:179]\n"
"buffer_load_dwordx4 v11, s[52:55], s67 offen offset:2048  lds\n"
"v_mfma_f32_16x16x32_bf16 v[176:179], a[76:79], a[108:111], v[176:179]\n"
"v_mfma_f32_16x16x32_bf16 v[148:151], a[68:71], a[108:111], v[148:151]\n"
"s_add_u32 s67, s75, 898176\n"
"v_mfma_f32_16x16x32_bf16 v[148:151], a[64:67], a[104:107], v[148:151]\n"
"v_mfma_f32_16x16x32_bf16 v[152:155], a[64:67], a[112:115], v[152:155]\n"
"s_sub_u32 s66, 81920, s73\n"
"v_mfma_f32_16x16x32_bf16 v[152:155], a[68:71], a[116:119], v[152:155]\n"
"buffer_load_dwordx4 v11, s[52:55], s67 offen offset:3072  lds\n"
"v_mfma_f32_16x16x32_bf16 v[180:183], a[76:79], a[116:119], v[180:183]\n"
"s_add_u32 m0, s72, 139264\n"
"v_add_u32 v12, s66, v7\n"
"v_mfma_f32_16x16x32_bf16 v[180:183], a[72:75], a[112:115], v[180:183]\n"
"v_mfma_f32_16x16x32_bf16 v[184:187], a[72:75], a[120:123], v[184:187]\n"
"s_add_u32 s67, s60, 896\n"
"v_add_u32 v13, s66, v8\n"
"v_mfma_f32_16x16x32_bf16 v[184:187], a[76:79], a[124:127], v[184:187]\n"
"v_mfma_f32_16x16x32_bf16 v[156:159], a[68:71], a[124:127], v[156:159]\n"
"buffer_load_dwordx4 v6, s[48:51], s67 offen offset:0  lds\n"
"v_mfma_f32_16x16x32_bf16 v[156:159], a[64:67], a[120:123], v[156:159]\n"
"v_mfma_f32_16x16x32_bf16 v[160:163], a[64:67], a[128:131], v[160:163]\n"
"s_add_u32 s67, s60, 16256\n"
"v_mfma_f32_16x16x32_bf16 v[160:163], a[68:71], a[132:135], v[160:163]\n"
"v_mfma_f32_16x16x32_bf16 v[188:191], a[76:79], a[132:135], v[188:191]\n"
"buffer_load_dwordx4 v6, s[48:51], s67 offen offset:1024  lds\n"
"v_mfma_f32_16x16x32_bf16 v[188:191], a[72:75], a[128:131], v[188:191]\n"
"v_mfma_f32_16x16x32_bf16 v[192:195], a[72:75], a[136:139], v[192:195]\n"
"s_add_u32 s67, s60, 31616\n"
"v_mfma_f32_16x16x32_bf16 v[192:195], a[76:79], a[140:143], v[192:195]\n"
"v_mfma_f32_16x16x32_bf16 v[164:167], a[68:71], a[140:143], v[164:167]\n"
"v_mfma_f32_16x16x32_bf16 v[164:167], a[64:67], a[136:139], v[164:167]\n"
"buffer_load_dwordx4 v6, s[48:51], s67 offen offset:2048  lds\n"
"v_mfma_f32_16x16x32_bf16 v[168:171], a[64:67], a[144:147], v[168:171]\n"
"v_mfma_f32_16x16x32_bf16 v[168:171], a[68:71], a[148:151], v[168:171]\n"
"s_add_u32 s67, s60, 46976\n"
"v_mfma_f32_16x16x32_bf16 v[196:199], a[76:79], a[148:151], v[196:199]\n"
"v_mfma_f32_16x16x32_bf16 v[196:199], a[72:75], a[144:147], v[196:199]\n"
"buffer_load_dwordx4 v6, s[48:51], s67 offen offset:3072  lds\n"
"v_mfma_f32_16x16x32_bf16 a[20:23], a[72:75], a[152:155], a[20:23]\n"
"v_mfma_f32_16x16x32_bf16 a[20:23], a[76:79], a[156:159], a[20:23]\n"
"s_waitcnt lgkmcnt(0)\n"
"s_add_u32 m0, s64, 32640\n"
"v_mfma_f32_16x16x32_bf16 a[16:19], a[68:71], a[156:159], a[16:19]\n"
"v_mfma_f32_16x16x32_bf16 a[16:19], a[64:67], a[152:155], a[16:19]\n"
"s_waitcnt vmcnt(23)\n"
"s_barrier\n"
"v_mfma_f32_16x16x32_bf16 v[200:203], a[80:83], a[96:99], v[200:203]\n"
"s_waitcnt vmcnt(12)\n"
"ds_read_b64_tr_b16 a[32:33], v9 offset:8192\n"
"buffer_load_dwordx4 v6, s[40:43], s80 offen offset:128  lds\n"
"v_mfma_f32_16x16x32_bf16 v[200:203], a[84:87], a[100:103], v[200:203]\n"
"ds_read_b64_tr_b16 a[34:35], v9 offset:8704\n"
"v_mfma_f32_16x16x32_bf16 v[228:231], a[92:95], a[100:103], v[228:231]\n"
"ds_read_b128 a[100:103], v13 offset:16384\n"
"v_mfma_f32_16x16x32_bf16 v[228:231], a[88:91], a[96:99], v[228:231]\n"
"ds_read_b128 a[96:99], v12 offset:16384\n"
"buffer_load_dwordx4 v6, s[40:43], s81 offen offset:1152  lds\n"
"v_mfma_f32_16x16x32_bf16 v[232:235], a[88:91], a[104:107], v[232:235]\n"
"ds_read_b64_tr_b16 a[36:37], v9 offset:12288\n"
"v_mfma_f32_16x16x32_bf16 v[232:235], a[92:95], a[108:111], v[232:235]\n"
"ds_read_b64_tr_b16 a[38:39], v9 offset:12800\n"
"v_mfma_f32_16x16x32_bf16 v[204:207], a[84:87], a[108:111], v[204:207]\n"
"ds_read_b128 a[108:111], v13 offset:18432\n"
"v_mfma_f32_16x16x32_bf16 v[204:207], a[80:83], a[104:107], v[204:207]\n"
"ds_read_b128 a[104:107], v12 offset:18432\n"
"ds_read_b64_tr_b16 a[40:41], v9 offset:8200\n"
"v_mfma_f32_16x16x32_bf16 v[208:211], a[80:83], a[112:115], v[208:211]\n"
"ds_read_b64_tr_b16 a[42:43], v9 offset:8712\n"
"buffer_load_dwordx4 v6, s[40:43], s82 offen offset:2176  lds\n"
"v_mfma_f32_16x16x32_bf16 v[208:211], a[84:87], a[116:119], v[208:211]\n"
"ds_read_b64_tr_b16 a[44:45], v9 offset:12296\n"
"v_mfma_f32_16x16x32_bf16 v[236:239], a[92:95], a[116:119], v[236:239]\n"
"ds_read_b128 a[116:119], v13 offset:20480\n"
"v_mfma_f32_16x16x32_bf16 v[236:239], a[88:91], a[112:115], v[236:239]\n"
"ds_read_b128 a[112:115], v12 offset:20480\n"
"v_mfma_f32_16x16x32_bf16 v[240:243], a[88:91], a[120:123], v[240:243]\n"
"ds_read_b64_tr_b16 a[46:47], v9 offset:12808\n"
"v_mfma_f32_16x16x32_bf16 v[240:243], a[92:95], a[124:127], v[240:243]\n"
"ds_read_b64_tr_b16 a[48:49], v15 offset:8192\n"
"buffer_load_dwordx4 v6, s[40:43], s83 offen offset:3200  lds\n"
"v_mfma_f32_16x16x32_bf16 v[212:215], a[84:87], a[124:127], v[212:215]\n"
"ds_read_b128 a[124:127], v13 offset:22528\n"
"v_mfma_f32_16x16x32_bf16 v[212:215], a[80:83], a[120:123], v[212:215]\n"
"ds_read_b128 a[120:123], v12 offset:22528\n"
"v_mfma_f32_16x16x32_bf16 v[216:219], a[80:83], a[128:131], v[216:219]\n"
"ds_read_b64_tr_b16 a[50:51], v15 offset:8704\n"
"v_mfma_f32_16x16x32_bf16 v[216:219], a[84:87], a[132:135], v[216:219]\n"
"s_add_u32 m0, s64, 8192\n"
"ds_read_b64_tr_b16 a[52:53], v15 offset:12288\n"
"v_mfma_f32_16x16x32_bf16 v[244:247], a[92:95], a[132:135], v[244:247]\n"
"ds_read_b128 a[132:135], v13 offset:24576\n"
"v_mfma_f32_16x16x32_bf16 v[244:247], a[88:91], a[128:131], v[244:247]\n"
"ds_read_b128 a[128:131], v12 offset:24576\n"
"v_mfma_f32_16x16x32_bf16 v[248:251], a[88:91], a[136:139], v[248:251]\n"
"ds_read_b64_tr_b16 a[54:55], v15 offset:12800\n"
"v_mfma_f32_16x16x32_bf16 v[248:251], a[92:95], a[140:143], v[248:251]\n"
"ds_read_b64_tr_b16 a[56:57], v15 offset:8200\n"
"v_mfma_f32_16x16x32_bf16 v[220:223], a[84:87], a[140:143], v[220:223]\n"
"ds_read_b128 a[140:143], v13 offset:26624\n"
"v_mfma_f32_16x16x32_bf16 v[220:223], a[80:83], a[136:139], v[220:223]\n"
"ds_read_b128 a[136:139], v12 offset:26624\n"
"v_mfma_f32_16x16x32_bf16 v[224:227], a[80:83], a[144:147], v[224:227]\n"
"ds_read_b64_tr_b16 a[58:59], v15 offset:8712\n"
"v_mfma_f32_16x16x32_bf16 v[224:227], a[84:87], a[148:151], v[224:227]\n"
"s_add_u32 s67, s75, 917504\n"
"ds_read_b64_tr_b16 a[60:61], v15 offset:12296\n"
"v_mfma_f32_16x16x32_bf16 v[252:255], a[92:95], a[148:151], v[252:255]\n"
"ds_read_b128 a[148:151], v13 offset:28672\n"
"v_mfma_f32_16x16x32_bf16 v[252:255], a[88:91], a[144:147], v[252:255]\n"
"ds_read_b128 a[144:147], v12 offset:28672\n"
"ds_read_b64_tr_b16 a[62:63], v15 offset:12808\n"
"v_mfma_f32_16x16x32_bf16 a[28:31], a[88:91], a[152:155], a[28:31]\n"
"v_mfma_f32_16x16x32_bf16 a[28:31], a[92:95], a[156:159], a[28:31]\n"
"v_mfma_f32_16x16x32_bf16 a[24:27], a[84:87], a[156:159], a[24:27]\n"
"ds_read_b128 a[156:159], v13 offset:30720\n"
"v_mfma_f32_16x16x32_bf16 a[24:27], a[80:83], a[152:155], a[24:27]\n"
"s_waitcnt lgkmcnt(15)\n"
"ds_read_b128 a[152:155], v12 offset:30720\n"
"v_mfma_f32_16x16x32_bf16 v[32:35], a[32:35], a[96:99], v[32:35]\n"
"v_mfma_f32_16x16x32_bf16 v[32:35], a[36:39], a[100:103], v[32:35]\n"
"s_waitcnt lgkmcnt(2)\n"
"L_H6S:\n"
"v_mfma_f32_16x16x32_bf16 v[60:63], a[44:47], a[100:103], v[60:63]\n"
"buffer_load_dwordx4 v11, s[52:55], s67 offen offset:0  lds\n"
"v_mfma_f32_16x16x32_bf16 v[60:63], a[40:43], a[96:99], v[60:63]\n"
"v_mfma_f32_16x16x32_bf16 v[64:67], a[40:43], a[104:107], v[64:67]\n"
"s_add_u32 s67, s75, 932864\n"
"v_mfma_f32_16x16x32_bf16 v[64:67], a[44:47], a[108:111], v[64:67]\n"
"v_mfma_f32_16x16x32_bf16 v[36:39], a[36:39], a[108:111], v[36:39]\n"
"v_mfma_f32_16x16x32_bf16 v[36:39], a[32:35], a[104:107], v[36:39]\n"
"buffer_load_dwordx4 v11, s[52:55], s67 offen offset:1024  lds\n"
"v_mfma_f32_16x16x32_bf16 v[40:43], a[32:35], a[112:115], v[40:43]\n"
"v_mfma_f32_16x16x32_bf16 v[40:43], a[36:39], a[116:119], v[40:43]\n"
"s_add_u32 s67, s75, 948224\n"
"v_mfma_f32_16x16x32_bf16 v[68:71], a[44:47], a[116:119], v[68:71]\n"
"v_mfma_f32_16x16x32_bf16 v[68:71], a[40:43], a[112:115], v[68:71]\n"
"buffer_load_dwordx4 v11, s[52:55], s67 offen offset:2048  lds\n"
"v_mfma_f32_16x16x32_bf16 v[72:75], a[40:43], a[120:123], v[72:75]\n"
"v_mfma_f32_16x16x32_bf16 v[72:75], a[44:47], a[124:127], v[72:75]\n"
"s_add_u32 s67, s75, 963584\n"
"v_mfma_f32_16x16x32_bf16 v[44:47], a[36:39], a[124:127], v[44:47]\n"
"v_mfma_f32_16x16x32_bf16 v[44:47], a[32:35], a[120:123], v[44:47]\n"
"v_mfma_f32_16x16x32_bf16 v[48:51], a[32:35], a[128:131], v[48:51]\n"
"buffer_load_dwordx4 v11, s[52:55], s67 offen offset:3072  lds\n"
"v_mfma_f32_16x16x32_bf16 v[48:51], a[36:39], a[132:135], v[48:51]\n"
"s_add_u32 m0, s64, 12288\n"
"v_mfma_f32_16x16x32_bf16 v[76:79], a[44:47], a[132:135], v[76:79]\n"
"s_waitcnt vmcnt(12)\n"
"ds_read_b64_tr_b16 a[64:65], v9 offset:0\n"
"v_mfma_f32_16x16x32_bf16 v[76:79], a[40:43], a[128:131], v[76:79]\n"
"ds_read_b64_tr_b16 a[66:67], v9 offset:512\n"
"v_mfma_f32_16x16x32_bf16 v[80:83], a[40:43], a[136:139], v[80:83]\n"
"s_add_u32 s67, s75, 983040\n"
"ds_read_b64_tr_b16 a[68:69], v9 offset:4096\n"
"v_mfma_f32_16x16x32_bf16 v[80:83], a[44:47], a[140:143], v[80:83]\n"
"ds_read_b64_tr_b16 a[70:71], v9 offset:4608\n"
"v_mfma_f32_16x16x32_bf16 v[52:55], a[36:39], a[140:143], v[52:55]\n"
"ds_read_b64_tr_b16 a[72:73], v9 offset:8\n"
"buffer_load_dwordx4 v11, s[52:55], s67 offen offset:0  lds\n"
"v_mfma_f32_16x16x32_bf16 v[52:55], a[32:35], a[136:139], v[52:55]\n"
"s_waitcnt lgkmcnt(5)\n"
"ds_read_b64_tr_b16 a[74:75], v9 offset:520\n"
"v_mfma_f32_16x16x32_bf16 v[56:59], a[32:35], a[144:147], v[56:59]\n"
"s_add_u32 s67, s75, 998400\n"
"ds_read_b64_tr_b16 a[76:77], v9 offset:4104\n"
"v_mfma_f32_16x16x32_bf16 v[56:59], a[36:39], a[148:151], v[56:59]\n"
"ds_read_b64_tr_b16 a[78:79], v9 offset:4616\n"
"v_mfma_f32_16x16x32_bf16 v[84:87], a[44:47], a[148:151], v[84:87]\n"
"buffer_load_dwordx4 v11, s[52:55], s67 offen offset:1024  lds\n"
"v_mfma_f32_16x16x32_bf16 v[84:87], a[40:43], a[144:147], v[84:87]\n"
"ds_read_b64_tr_b16 a[80:81], v15 offset:0\n"
"v_mfma_f32_16x16x32_bf16 a[4:7], a[40:43], a[152:155], a[4:7]\n"
"s_add_u32 s67, s75, 1013760\n"
"ds_read_b64_tr_b16 a[82:83], v15 offset:512\n"
"v_mfma_f32_16x16x32_bf16 a[4:7], a[44:47], a[156:159], a[4:7]\n"
"ds_read_b64_tr_b16 a[84:85], v15 offset:4096\n"
"v_mfma_f32_16x16x32_bf16 a[0:3], a[36:39], a[156:159], a[0:3]\n"
"ds_read_b64_tr_b16 a[86:87], v15 offset:4608\n"
"buffer_load_dwordx4 v11, s[52:55], s67 offen offset:2048  lds\n"
"v_mfma_f32_16x16x32_bf16 a[0:3], a[32:35], a[152:155], a[0:3]\n"
"ds_read_b64_tr_b16 a[88:89], v15 offset:8\n"
"v_mfma_f32_16x16x32_bf16 v[88:91], a[48:51], a[96:99], v[88:91]\n"
"s_add_u32 s67, s75, 1029120\n"
"ds_read_b64_tr_b16 a[90:91], v15 offset:520\n"
"v_mfma_f32_16x16x32_bf16 v[88:91], a[52:55], a[100:103], v[88:91]\n"
"ds_read_b64_tr_b16 a[92:93], v15 offset:4104\n"
"v_mfma_f32_16x16x32_bf16 v[116:119], a[60:63], a[100:103], v[116:119]\n"
"ds_read_b64_tr_b16 a[94:95], v15 offset:4616\n"
"v_mfma_f32_16x16x32_bf16 v[116:119], a[56:59], a[96:99], v[116:119]\n"
"buffer_load_dwordx4 v11, s[52:55], s67 offen offset:3072  lds\n"
"v_mfma_f32_16x16x32_bf16 v[120:123], a[56:59], a[104:107], v[120:123]\n"
"v_mfma_f32_16x16x32_bf16 v[120:123], a[60:63], a[108:111], v[120:123]\n"
"s_add_u32 m0, s64, 0\n"
"v_mfma_f32_16x16x32_bf16 v[92:95], a[52:55], a[108:111], v[92:95]\n"
"v_mfma_f32_16x16x32_bf16 v[92:95], a[48:51], a[104:107], v[92:95]\n"
"s_add_u32 s67, s75, 917632\n"
"v_mfma_f32_16x16x32_bf16 v[96:99], a[48:51], a[112:115], v[96:99]\n"
"s_waitcnt lgkmcnt(0)\n"
"s_mov_b32 s99, 0\n"
"v_mfma_f32_16x16x32_bf16 v[96:99], a[52:55], a[116:119], v[96:99]\n"
"s_mov_b32 s98, 0\n"
"buffer_load_dwordx4 v11, s[52:55], s67 offen offset:0  lds\n"
"v_mfma_f32_16x16x32_bf16 v[124:127], a[60:63], a[116:119], v[124:127]\n"
"s_memtime s[76:77]\n"
"s_load_dword s98, s[96:97], 8192 glc\n"
"v_mfma_f32_16x16x32_bf16 v[124:127], a[56:59], a[112:115], v[124:127]\n"
"s_add_u32 s67, s75, 932992\n"
"v_mfma_f32_16x16x32_bf16 v[128:131], a[56:59], a[120:123], v[128:131]\n"
"v_mfma_f32_16x16x32_bf16 v[128:131], a[60:63], a[124:127], v[128:131]\n"
"v_mfma_f32_16x16x32_bf16 v[100:103], a[52:55], a[124:127], v[100:103]\n"
"buffer_load_dwordx4 v11, s[52:55], s67 offen offset:1024  lds\n"
"v_mfma_f32_16x16x32_bf16 v[100:103], a[48:51], a[120:123], v[100:103]\n"
"v_mfma_f32_16x16x32_bf16 v[104:107], a[48:51], a[128:131], v[104:107]\n"
"s_add_u32 s67, s75, 948352\n"
"v_mfma_f32_16x16x32_bf16 v[104:107], a[52:55], a[132:135], v[104:107]\n"
"v_mfma_f32_16x16x32_bf16 v[132:135], a[60:63], a[132:135], v[132:135]\n"
"buffer_load_dwordx4 v11, s[52:55], s67 offen offset:2048  lds\n"
"v_mfma_f32_16x16x32_bf16 v[132:135], a[56:59], a[128:131], v[132:135]\n"
"s_mov_b32 s30, s76\n"
"v_mfma_f32_16x16x32_bf16 v[136:139], a[56:59], a[136:139], v[136:139]\n"
"s_add_u32 s67, s75, 963712\n"
"v_mfma_f32_16x16x32_bf16 v[136:139], a[60:63], a[140:143], v[136:139]\n"
"v_mfma_f32_16x16x32_bf16 v[108:111], a[52:55], a[140:143], v[108:111]\n"
"s_sub_u32 s99, s99, s98\n"
"s_sub_u32 s99, s99, s98\n"
"v_mfma_f32_16x16x32_bf16 v[108:111], a[48:51], a[136:139], v[108:111]\n"
"s_sub_u32 s99, s99, s98\n"
"buffer_load_dwordx4 v11, s[52:55], s67 offen offset:3072  lds\n"
"v_mfma_f32_16x16x32_bf16 v[112:115], a[48:51], a[144:147], v[112:115]\n"
"s_sub_u32 s99, s99, s98\n"
"s_add_u32 m0, s64, 4096\n"
"v_mfma_f32_16x16x32_bf16 v[112:115], a[52:55], a[148:151], v[112:115]\n"
"v_mfma_f32_16x16x32_bf16 v[140:143], a[60:63], a[148:151], v[140:143]\n"
"s_sub_u32 s99, s99, s98\n"
"s_sub_u32 s99, s99, s98\n"
"v_mfma_f32_16x16x32_bf16 v[140:143], a[56:59], a[144:147], v[140:143]\n"
"s_sub_u32 s99, s99, s98\n"
"s_add_u32 s67, s75, 983168\n"
"v_mfma_f32_16x16x32_bf16 a[12:15], a[56:59], a[152:155], a[12:15]\n"
"s_sub_u32 s99, s99, s98\n"
"buffer_load_dwordx4 v11, s[52:55], s67 offen offset:0  lds\n"
"v_mfma_f32_16x16x32_bf16 a[12:15], a[60:63], a[156:159], a[12:15]\n"
"s_add_u32 s67, s75, 998528\n"
"v_mfma_f32_16x16x32_bf16 a[8:11], a[52:55], a[156:159], a[8:11]\n"
"s_sub_u32 s99, s99, s98\n"
"s_sub_u32 s99, s99, s98\n"
"v_mfma_f32_16x16x32_bf16 a[8:11], a[48:51], a[152:155], a[8:11]\n"
"s_sub_u32 s99, s99, s98\n"
"v_mfma_f32_16x16x32_bf16 v[144:147], a[64:67], a[96:99], v[144:147]\n"
"s_sub_u32 s99, s99, s98\n"
"v_mfma_f32_16x16x32_bf16 v[144:147], a[68:71], a[100:103], v[144:147]\n"
"buffer_load_dwordx4 v11, s[52:55], s67 offen offset:1024  lds\n"
"v_mfma_f32_16x16x32_bf16 v[172:175], a[76:79], a[100:103], v[172:175]\n"
"s_sub_u32 s99, s99, s98\n"
"s_sub_u32 s99, s99, s98\n"
"v_mfma_f32_16x16x32_bf16 v[172:175], a[72:75], a[96:99], v[172:175]\n"
"s_sub_u32 s99, s99, s98\n"
"s_add_u32 s67, s75, 1013888\n"
"v_mfma_f32_16x16x32_bf16 v[176:179], a[72:75], a[104:107], v[176:179]\n"
"s_sub_u32 s99, s99, s98\n"
"v_mfma_f32_16x16x32_bf16 v[176:179], a[76:79], a[108:111], v[176:179]\n"
"s_sub_u32 s71, s64, s64\n"
"buffer_load_dwordx4 v11, s[52:55], s67 offen offset:2048  lds\n"
"v_mfma_f32_16x16x32_bf16 v[148:151], a[68:71], a[108:111], v[148:151]\n"
"s_sub_u32 s99, s99, s98\n"
"s_sub_u32 s99, s99, s98\n"
"v_mfma_f32_16x16x32_bf16 v[148:151], a[64:67], a[104:107], v[148:151]\n"
"s_sub_u32 s99, s99, s98\n"
"s_add_u32 s71, s71, 32768\n"
"s_add_u32 s67, s75, 1029248\n"
"v_mfma_f32_16x16x32_bf16 v[152:155], a[64:67], a[112:115], v[152:155]\n"
"s_sub_u32 s66, 122880, s73\n"
"s_sub_u32 s99, s99, s98\n"
"v_mfma_f32_16x16x32_bf16 v[152:155], a[68:71], a[116:119], v[152:155]\n"
"buffer_load_dwordx4 v11, s[52:55], s67 offen offset:3072  lds\n"
"v_mfma_f32_16x16x32_bf16 v[180:183], a[76:79], a[116:119], v[180:183]\n"
"s_sub_u32 s99, s99, s98\n"
"v_add_u32 v12, s66, v7\n"
"s_sub_u32 s99, s99, s98\n"
"v_mfma_f32_16x16x32_bf16 v[180:183], a[72:75], a[112:115], v[180:183]\n"
"s_sub_u32 s99, s99, s98\n"
"v_add_u32 v20, s71, v7\n"
"v_mfma_f32_16x16x32_bf16 v[184:187], a[72:75], a[120:123], v[184:187]\n"
"s_sub_u32 s99, s99, s98\n"
"v_add_u32 v13, s66, v8\n"
"v_mfma_f32_16x16x32_bf16 v[184:187], a[76:79], a[124:127], v[184:187]\n"
"s_memtime s[76:77]\n"
"v_add_u32 v21, s71, v8\n"
"v_mfma_f32_16x16x32_bf16 v[156:159], a[68:71], a[124:127], v[156:159]\n"
"s_add_u32 m0, s72, 57344\n"
"s_add_u32 s67, %21, 0\n"
"v_mfma_f32_16x16x32_bf16 v[156:159], a[64:67], a[120:123], v[156:159]\n"
"s_waitcnt lgkmcnt(0)\n"
"buffer_load_dwordx4 v11, s[52:55], s67 offen offset:0  lds\n"
"v_mfma_f32_16x16x32_bf16 v[160:163], a[64:67], a[128:131], v[160:163]\n"
"s_sub_u32 s30, s76, s30\n"
"s_waitcnt vmcnt(17)\n"
"v_mfma_f32_16x16x32_bf16 v[160:163], a[68:71], a[132:135], v[160:163]\n"
"s_cmp_lt_u32 s30, 620\n"
"s_cselect_b32 s99, s99, 0\n"
"v_mfma_f32_16x16x32_bf16 v[188:191], a[76:79], a[132:135], v[188:191]\n"
"s_max_u32 s32, s32, s99\n"
"ds_read_b128 a[192:195], v20 offset:0\n"
"v_mfma_f32_16x16x32_bf16 v[188:191], a[72:75], a[128:131], v[188:191]\n"
"s_add_u32 s67, %21, 15360\n"
"ds_read_b128 a[196:199], v21 offset:0\n"
"buffer_load_dwordx4 v11, s[52:55], s67 offen offset:1024  lds\n"
"v_mfma_f32_16x16x32_bf16 v[192:195], a[72:75], a[136:139], v[192:195]\n"
"ds_read_b128 a[200:203], v20 offset:2048\n"
"v_mfma_f32_16x16x32_bf16 v[192:195], a[76:79], a[140:143], v[192:195]\n"
"s_add_u32 s67, %21, 30720\n"
"ds_read_b128 a[204:207], v21 offset:2048\n"
"v_mfma_f32_16x16x32_bf16 v[164:167], a[68:71], a[140:143], v[164:167]\n"
"v_mfma_f32_16x16x32_bf16 v[164:167], a[64:67], a[136:139], v[164:167]\n"
"buffer_load_dwordx4 v11, s[52:55], s67 offen offset:2048  lds\n"
"v_mfma_f32_16x16x32_bf16 v[168:171], a[64:67], a[144:147], v[168:171]\n"
"v_mfma_f32_16x16x32_bf16 v[168:171], a[68:71], a[148:151], v[168:171]\n"
"s_add_u32 s67, %21, 46080\n"
"v_mfma_f32_16x16x32_bf16 v[196:199], a[76:79], a[148:151], v[196:199]\n"
"v_mfma_f32_16x16x32_bf16 v[196:199], a[72:75], a[144:147], v[196:199]\n"
"s_waitcnt lgkmcnt(0)\n"
"v_mfma_f32_16x16x32_bf16 a[20:23], a[72:75], a[152:155], a[20:23]\n"
"buffer_load_dwordx4 v11, s[52:55], s67 offen offset:3072  lds\n"
"v_mfma_f32_16x16x32_bf16 a[20:23], a[76:79], a[156:159], a[20:23]\n"
"s_cmp_lt_u32 %16, 2\n"
"s_cselect_b32 s67, 16384, 0\n"
"v_mfma_f32_16x16x32_bf16 a[16:19], a[68:71], a[156:159], a[16:19]\n"
"s_add_u32 s67, s67, s72\n"
"v_mfma_f32_16x16x32_bf16 a[16:19], a[64:67], a[152:155], a[16:19]\n"
"s_barrier\n"
"s_waitcnt vmcnt(12)\n"
"ds_read_b64_tr_b16 a[32:33], v9 offset:8192\n"
"v_mfma_f32_16x16x32_bf16 v[200:203], a[80:83], a[96:99], v[200:203]\n"
"s_add_u32 m0, s67, 16384\n"
"ds_read_b64_tr_b16 a[34:35], v9 offset:8704\n"
"v_mfma_f32_16x16x32_bf16 v[200:203], a[84:87], a[100:103], v[200:203]\n"
"s_sub_u32 s67, 1024, s60\n"
"ds_read_b64_tr_b16 a[36:37], v9 offset:12288\n"
"v_mfma_f32_16x16x32_bf16 v[228:231], a[92:95], a[100:103], v[228:231]\n"
"ds_read_b128 a[100:103], v13 offset:16384\n"
"buffer_load_dwordx4 v6, s[48:51], s67 offen offset:0  lds\n"
"v_mfma_f32_16x16x32_bf16 v[228:231], a[88:91], a[96:99], v[228:231]\n"
"ds_read_b128 a[96:99], v12 offset:16384\n"
"v_mfma_f32_16x16x32_bf16 v[232:235], a[88:91], a[104:107], v[232:235]\n"
"s_sub_u32 s67, 16384, s60\n"
"ds_read_b64_tr_b16 a[38:39], v9 offset:12800\n"
"v_mfma_f32_16x16x32_bf16 v[232:235], a[92:95], a[108:111], v[232:235]\n"
"ds_read_b64_tr_b16 a[40:41], v9 offset:8200\n"
"v_mfma_f32_16x16x32_bf16 v[204:207], a[84:87], a[108:111], v[204:207]\n"
"ds_read_b128 a[108:111], v13 offset:18432\n"
"buffer_load_dwordx4 v6, s[48:51], s67 offen offset:1024  lds\n"
"v_mfma_f32_16x16x32_bf16 v[204:207], a[80:83], a[104:107], v[204:207]\n"
"ds_read_b128 a[104:107], v12 offset:18432\n"
"v_mfma_f32_16x16x32_bf16 v[208:211], a[80:83], a[112:115], v[208:211]\n"
"s_sub_u32 s67, 31744, s60\n"
"ds_read_b64_tr_b16 a[42:43], v9 offset:8712\n"
"v_mfma_f32_16x16x32_bf16 v[208:211], a[84:87], a[116:119], v[208:211]\n"
"ds_read_b64_tr_b16 a[44:45], v9 offset:12296\n"
"v_mfma_f32_16x16x32_bf16 v[236:239], a[92:95], a[116:119], v[236:239]\n"
"ds_read_b128 a[116:119], v13 offset:20480\n"
"buffer_load_dwordx4 v6, s[48:51], s67 offen offset:2048  lds\n"
"v_mfma_f32_16x16x32_bf16 v[236:239], a[88:91], a[112:115], v[236:239]\n"
"ds_read_b128 a[112:115], v12 offset:20480\n"
"v_mfma_f32_16x16x32_bf16 v[240:243], a[88:91], a[120:123], v[240:243]\n"
"s_sub_u32 s67, 47104, s60\n"
"ds_read_b64_tr_b16 a[46:47], v9 offset:12808\n"
"v_mfma_f32_16x16x32_bf16 v[240:243], a[92:95], a[124:127], v[240:243]\n"
"ds_read_b64_tr_b16 a[48:49], v15 offset:8192\n"
"v_mfma_f32_16x16x32_bf16 v[212:215], a[84:87], a[124:127], v[212:215]\n"
"ds_read_b128 a[124:127], v13 offset:22528\n"
"v_mfma_f32_16x16x32_bf16 v[212:215], a[80:83], a[120:123], v[212:215]\n"
"ds_read_b128 a[120:123], v12 offset:22528\n"
"buffer_load_dwordx4 v6, s[48:51], s67 offen offset:3072  lds\n"
"v_mfma_f32_16x16x32_bf16 v[216:219], a[80:83], a[128:131], v[216:219]\n"
"ds_read_b64_tr_b16 a[50:51], v15 offset:8704\n"
"v_mfma_f32_16x16x32_bf16 v[216:219], a[84:87], a[132:135], v[216:219]\n"
"ds_read_b64_tr_b16 a[52:53], v15 offset:12288\n"
"v_mfma_f32_16x16x32_bf16 v[244:247], a[92:95], a[132:135], v[244:247]\n"
"ds_read_b128 a[132:135], v13 offset:24576\n"
"v_mfma_f32_16x16x32_bf16 v[244:247], a[88:91], a[128:131], v[244:247]\n"
"s_add_u32 m0, s64, 8192\n"
"ds_read_b128 a[128:131], v12 offset:24576\n"
"v_mfma_f32_16x16x32_bf16 v[248:251], a[88:91], a[136:139], v[248:251]\n"
"ds_read_b64_tr_b16 a[54:55], v15 offset:12800\n"
"v_mfma_f32_16x16x32_bf16 v[248:251], a[92:95], a[140:143], v[248:251]\n"
"ds_read_b64_tr_b16 a[56:57], v15 offset:8200\n"
"v_mfma_f32_16x16x32_bf16 v[220:223], a[84:87], a[140:143], v[220:223]\n"
"ds_read_b128 a[140:143], v13 offset:26624\n"
"v_mfma_f32_16x16x32_bf16 v[220:223], a[80:83], a[136:139], v[220:223]\n"
"ds_read_b128 a[136:139], v12 offset:26624\n"
"v_mfma_f32_16x16x32_bf16 v[224:227], a[80:83], a[144:147], v[224:227]\n"
"ds_read_b64_tr_b16 a[58:59], v15 offset:8712\n"
"v_mfma_f32_16x16x32_bf16 v[224:227], a[84:87], a[148:151], v[224:227]\n"
"ds_read_b64_tr_b16 a[60:61], v15 offset:12296\n"
"v_mfma_f32_16x16x32_bf16 v[252:255], a[92:95], a[148:151], v[252:255]\n"
"s_sub_u32 s67, 1048576, s75\n"
"ds_read_b128 a[148:151], v13 offset:28672\n"
"v_mfma_f32_16x16x32_bf16 v[252:255], a[88:91], a[144:147], v[252:255]\n"
"ds_read_b128 a[144:147], v12 offset:28672\n"
"ds_read_b64_tr_b16 a[62:63], v15 offset:12808\n"
"v_mfma_f32_16x16x32_bf16 a[28:31], a[88:91], a[152:155], a[28:31]\n"
"v_mfma_f32_16x16x32_bf16 a[28:31], a[92:95], a[156:159], a[28:31]\n"
"v_mfma_f32_16x16x32_bf16 a[24:27], a[84:87], a[156:159], a[24:27]\n"
"ds_read_b128 a[156:159], v13 offset:30720\n"
"v_mfma_f32_16x16x32_bf16 a[24:27], a[80:83], a[152:155], a[24:27]\n"
"s_waitcnt lgkmcnt(15)\n"
"ds_read_b128 a[152:155], v12 offset:30720\n"
"v_mfma_f32_16x16x32_bf16 v[32:35], a[32:35], a[96:99], v[32:35]\n"
"v_mfma_f32_16x16x32_bf16 v[32:35], a[36:39], a[100:103], v[32:35]\n"
"s_waitcnt lgkmcnt(2)\n"
"v_mfma_f32_16x16x32_bf16 v[60:63], a[44:47], a[100:103], v[60:63]\n"
"buffer_load_dwordx4 v11, s[52:55], s67 offen offset:0  lds\n"
"v_mfma_f32_16x16x32_bf16 v[60:63], a[40:43], a[96:99], v[60:63]\n"
"v_mfma_f32_16x16x32_bf16 v[64:67], a[40:43], a[104:107], v[64:67]\n"
"s_sub_u32 s67, 1063936, s75\n"
"v_mfma_f32_16x16x32_bf16 v[64:67], a[44:47], a[108:111], v[64:67]\n"
"v_mfma_f32_16x16x32_bf16 v[36:39], a[36:39], a[108:111], v[36:39]\n"
"buffer_load_dwordx4 v11, s[52:55], s67 offen offset:1024  lds\n"
"v_mfma_f32_16x16x32_bf16 v[36:39], a[32:35], a[104:107], v[36:39]\n"
"v_mfma_f32_16x16x32_bf16 v[40:43], a[32:35], a[112:115], v[40:43]\n"
"s_sub_u32 s67, 1079296, s75\n"
"v_mfma_f32_16x16x32_bf16 v[40:43], a[36:39], a[116:119], v[40:43]\n"
"v_mfma_f32_16x16x32_bf16 v[68:71], a[44:47], a[116:119], v[68:71]\n"
"v_mfma_f32_16x16x32_bf16 v[68:71], a[40:43], a[112:115], v[68:71]\n"
"buffer_load_dwordx4 v11, s[52:55], s67 offen offset:2048  lds\n"
"v_mfma_f32_16x16x32_bf16 v[72:75], a[40:43], a[120:123], v[72:75]\n"
"v_mfma_f32_16x16x32_bf16 v[72:75], a[44:47], a[124:127], v[72:75]\n"
"s_sub_u32 s67, 1094656, s75\n"
"v_mfma_f32_16x16x32_bf16 v[44:47], a[36:39], a[124:127], v[44:47]\n"
"v_mfma_f32_16x16x32_bf16 v[44:47], a[32:35], a[120:123], v[44:47]\n"
"buffer_load_dwordx4 v11, s[52:55], s67 offen offset:3072  lds\n"
"v_mfma_f32_16x16x32_bf16 v[48:51], a[32:35], a[128:131], v[48:51]\n"
"v_mfma_f32_16x16x32_bf16 v[48:51], a[36:39], a[132:135], v[48:51]\n"
"s_add_u32 m0, s64, 12288\n"
"v_mfma_f32_16x16x32_bf16 v[76:79], a[44:47], a[132:135], v[76:79]\n"
"s_waitcnt vmcnt(12)\n"
"ds_read_b64_tr_b16 a[64:65], v9 offset:0\n"
"v_mfma_f32_16x16x32_bf16 v[76:79], a[40:43], a[128:131], v[76:79]\n"
"s_sub_u32 s67, 1114112, s75\n"
"ds_read_b64_tr_b16 a[66:67], v9 offset:512\n"
"v_mfma_f32_16x16x32_bf16 v[80:83], a[40:43], a[136:139], v[80:83]\n"
"ds_read_b64_tr_b16 a[68:69], v9 offset:4096\n"
"v_mfma_f32_16x16x32_bf16 v[80:83], a[44:47], a[140:143], v[80:83]\n"
"ds_read_b64_tr_b16 a[70:71], v9 offset:4608\n"
"buffer_load_dwordx4 v11, s[52:55], s67 offen offset:0  lds\n"
"v_mfma_f32_16x16x32_bf16 v[52:55], a[36:39], a[140:143], v[52:55]\n"
"s_sub_u32 s67, 1129472, s75\n"
"ds_read_b64_tr_b16 a[72:73], v9 offset:8\n"
"v_mfma_f32_16x16x32_bf16 v[52:55], a[32:35], a[136:139], v[52:55]\n"
"s_waitcnt lgkmcnt(5)\n"
"ds_read_b64_tr_b16 a[74:75], v9 offset:520\n"
"v_mfma_f32_16x16x32_bf16 v[56:59], a[32:35], a[144:147], v[56:59]\n"
"ds_read_b64_tr_b16 a[76:77], v9 offset:4104\n"
"v_mfma_f32_16x16x32_bf16 v[56:59], a[36:39], a[148:151], v[56:59]\n"
"ds_read_b64_tr_b16 a[78:79], v9 offset:4616\n"
"v_mfma_f32_16x16x32_bf16 v[84:87], a[44:47], a[148:151], v[84:87]\n"
"buffer_load_dwordx4 v11, s[52:55], s67 offen offset:1024  lds\n"
"v_mfma_f32_16x16x32_bf16 v[84:87], a[40:43], a[144:147], v[84:87]\n"
"ds_read_b64_tr_b16 a[80:81], v15 offset:0\n"
"v_mfma_f32_16x16x32_bf16 a[4:7], a[40:43], a[152:155], a[4:7]\n"
"s_sub_u32 s67, 1144832, s75\n"
"ds_read_b64_tr_b16 a[82:83], v15 offset:512\n"
"v_mfma_f32_16x16x32_bf16 a[4:7], a[44:47], a[156:159], a[4:7]\n"
"ds_read_b64_tr_b16 a[84:85], v15 offset:4096\n"
"v_mfma_f32_16x16x32_bf16 a[0:3], a[36:39], a[156:159], a[0:3]\n"
"ds_read_b64_tr_b16 a[86:87], v15 offset:4608\n"
"buffer_load_dwordx4 v11, s[52:55], s67 offen offset:2048  lds\n"
"v_mfma_f32_16x16x32_bf16 a[0:3], a[32:35], a[152:155], a[0:3]\n"
"ds_read_b64_tr_b16 a[88:89], v15 offset:8\n"
"v_mfma_f32_16x16x32_bf16 v[88:91], a[48:51], a[96:99], v[88:91]\n"
"s_sub_u32 s67, 1160192, s75\n"
"ds_read_b64_tr_b16 a[90:91], v15 offset:520\n"
"v_mfma_f32_16x16x32_bf16 v[88:91], a[52:55], a[100:103], v[88:91]\n"
"ds_read_b64_tr_b16 a[92:93], v15 offset:4104\n"
"v_mfma_f32_16x16x32_bf16 v[116:119], a[60:63], a[100:103], v[116:119]\n"
"ds_read_b64_tr_b16 a[94:95], v15 offset:4616\n"
"v_mfma_f32_16x16x32_bf16 v[116:119], a[56:59], a[96:99], v[116:119]\n"
"buffer_load_dwordx4 v11, s[52:55], s67 offen offset:3072  lds\n"
"v_mfma_f32_16x16x32_bf16 v[120:123], a[56:59], a[104:107], v[120:123]\n"
"v_mfma_f32_16x16x32_bf16 v[120:123], a[60:63], a[108:111], v[120:123]\n"
"s_add_u32 m0, s64, 0\n"
"v_mfma_f32_16x16x32_bf16 v[92:95], a[52:55], a[108:111], v[92:95]\n"
"v_mfma_f32_16x16x32_bf16 v[92:95], a[48:51], a[104:107], v[92:95]\n"
"s_sub_u32 s67, 1048704, s75\n"
"v_mfma_f32_16x16x32_bf16 v[96:99], a[48:51], a[112:115], v[96:99]\n"
"s_waitcnt lgkmcnt(0)\n"
"v_mfma_f32_16x16x32_bf16 v[96:99], a[52:55], a[116:119], v[96:99]\n"
"buffer_load_dwordx4 v11, s[52:55], s67 offen offset:0  lds\n"
"v_mfma_f32_16x16x32_bf16 v[124:127], a[60:63], a[116:119], v[124:127]\n"
"v_mfma_f32_16x16x32_bf16 v[124:127], a[56:59], a[112:115], v[124:127]\n"
"s_sub_u32 s67, 1064064, s75\n"
"v_mfma_f32_16x16x32_bf16 v[128:131], a[56:59], a[120:123], v[128:131]\n"
"v_mfma_f32_16x16x32_bf16 v[128:131], a[60:63], a[124:127], v[128:131]\n"
"v_mfma_f32_16x16x32_bf16 v[100:103], a[52:55], a[124:127], v[100:103]\n"
"buffer_load_dwordx4 v11, s[52:55], s67 offen offset:1024  lds\n"
"v_mfma_f32_16x16x32_bf16 v[100:103], a[48:51], a[120:123], v[100:103]\n"
"v_mfma_f32_16x16x32_bf16 v[104:107], a[48:51], a[128:131], v[104:107]\n"
"s_sub_u32 s67, 1079424, s75\n"
"v_mfma_f32_16x16x32_bf16 v[104:107], a[52:55], a[132:135], v[104:107]\n"
"v_mfma_f32_16x16x32_bf16 v[132:135], a[60:63], a[132:135], v[132:135]\n"
"v_mfma_f32_16x16x32_bf16 v[132:135], a[56:59], a[128:131], v[132:135]\n"
"buffer_load_dwordx4 v11, s[52:55], s67 offen offset:2048  lds\n"
"v_mfma_f32_16x16x32_bf16 v[136:139], a[56:59], a[136:139], v[136:139]\n"
"v_mfma_f32_16x16x32_bf16 v[136:139], a[60:63], a[140:143], v[136:139]\n"
"s_sub_u32 s67, 1094784, s75\n"
"v_mfma_f32_16x16x32_bf16 v[108:111], a[52:55], a[140:143], v[108:111]\n"
"v_mfma_f32_16x16x32_bf16 v[108:111], a[48:51], a[136:139], v[108:111]\n"
"buffer_load_dwordx4 v11, s[52:55], s67 offen offset:3072  lds\n"
"v_mfma_f32_16x16x32_bf16 v[112:115], a[48:51], a[144:147], v[112:115]\n"
"v_mfma_f32_16x16x32_bf16 v[112:115], a[52:55], a[148:151], v[112:115]\n"
"s_add_u32 m0, s64, 4096\n"
"v_mfma_f32_16x16x32_bf16 v[140:143], a[60:63], a[148:151], v[140:143]\n"
"v_mfma_f32_16x16x32_bf16 v[140:143], a[56:59], a[144:147], v[140:143]\n"
"s_sub_u32 s67, 1114240, s75\n"
"v_mfma_f32_16x16x32_bf16 a[12:15], a[56:59], a[152:155], a[12:15]\n"
"v_mfma_f32_16x16x32_bf16 a[12:15], a[60:63], a[156:159], a[12:15]\n"
"buffer_load_dwordx4 v11, s[52:55], s67 offen offset:0  lds\n"
"v_mfma_f32_16x16x32_bf16 a[8:11], a[52:55], a[156:159], a[8:11]\n"
"v_mfma_f32_16x16x32_bf16 a[8:11], a[48:51], a[152:155], a[8:11]\n"
"s_sub_u32 s67, 1129600, s75\n"
"v_mfma_f32_16x16x32_bf16 v[144:147], a[64:67], a[96:99], v[144:147]\n"
"buffer_load_dwordx4 v11, s[52:55], s67 offen offset:1024  lds\n"
"v_mfma_f32_16x16x32_bf16 v[144:147], a[68:71], a[100:103], v[144:147]\n"
"s_sub_u32 s71, s72, s64\n"
"v_mfma_f32_16x16x32_bf16 v[172:175], a[76:79], a[100:103], v[172:175]\n"
"s_sub_u32 s67, 1144960, s75\n"
"v_mfma_f32_16x16x32_bf16 v[172:175], a[72:75], a[96:99], v[172:175]\n"
"v_mfma_f32_16x16x32_bf16 v[176:179], a[72:75], a[104:107], v[176:179]\n"
"s_add_u32 s71, s71, 57344\n"
"v_mfma_f32_16x16x32_bf16 v[176:179], a[76:79], a[108:111], v[176:179]\n"
"buffer_load_dwordx4 v11, s[52:55], s67 offen offset:2048  lds\n"
"v_mfma_f32_16x16x32_bf16 v[148:151], a[68:71], a[108:111], v[148:151]\n"
"v_add_u32 v20, s71, v9\n"
"v_mfma_f32_16x16x32_bf16 v[148:151], a[64:67], a[104:107], v[148:151]\n"
"s_sub_u32 s67, 1160320, s75\n"
"v_mfma_f32_16x16x32_bf16 v[152:155], a[64:67], a[112:115], v[152:155]\n"
"s_sub_u32 s66, 0, s73\n"
"v_mfma_f32_16x16x32_bf16 v[152:155], a[68:71], a[116:119], v[152:155]\n"
"v_add_u32 v21, s71, v15\n"
"v_mfma_f32_16x16x32_bf16 v[180:183], a[76:79], a[116:119], v[180:183]\n"
"v_add_u32 v12, s66, v7\n"
"v_mfma_f32_16x16x32_bf16 v[180:183], a[72:75], a[112:115], v[180:183]\n"
"buffer_load_dwordx4 v11, s[52:55], s67 offen offset:3072  lds\n"
"v_mfma_f32_16x16x32_bf16 v[184:187], a[72:75], a[120:123], v[184:187]\n"
"v_add_u32 v13, s66, v8\n"
"v_mfma_f32_16x16x32_bf16 v[184:187], a[76:79], a[124:127], v[184:187]\n"
"s_waitcnt vmcnt(20)\n"
"ds_read_b64_tr_b16 a[160:161], v20 offset:0\n"
"v_mfma_f32_16x16x32_bf16 v[156:159], a[68:71], a[124:127], v[156:159]\n"
"ds_read_b64_tr_b16 a[162:163], v20 offset:512\n"
"v_mfma_f32_16x16x32_bf16 v[156:159], a[64:67], a[120:123], v[156:159]\n"
"ds_read_b64_tr_b16 a[164:165], v20 offset:8\n"
"v_mfma_f32_16x16x32_bf16 v[160:163], a[64:67], a[128:131], v[160:163]\n"
"ds_read_b64_tr_b16 a[166:167], v20 offset:520\n"
"v_mfma_f32_16x16x32_bf16 v[160:163], a[68:71], a[132:135], v[160:163]\n"
"s_cmp_lt_u32 %16, 2\n"
"s_cselect_b32 s67, 16384, 0\n"
"v_mfma_f32_16x16x32_bf16 v[188:191], a[76:79], a[132:135], v[188:191]\n"
"s_add_u32 s67, s67, s72\n"
"ds_read_b64_tr_b16 a[168:169], v21 offset:0\n"
"v_mfma_f32_16x16x32_bf16 v[188:191], a[72:75], a[128:131], v[188:191]\n"
"ds_read_b64_tr_b16 a[170:171], v21 offset:512\n"
"v_mfma_f32_16x16x32_bf16 v[192:195], a[72:75], a[136:139], v[192:195]\n"
"ds_read_b64_tr_b16 a[172:173], v21 offset:8\n"
"v_mfma_f32_16x16x32_bf16 v[192:195], a[76:79], a[140:143], v[192:195]\n"
"s_add_u32 m0, s67, 57344\n"
"v_mfma_f32_16x16x32_bf16 v[164:167], a[68:71], a[140:143], v[164:167]\n"
"ds_read_b64_tr_b16 a[174:175], v21 offset:520\n"
"v_mfma_f32_16x16x32_bf16 v[164:167], a[64:67], a[136:139], v[164:167]\n"
"v_mfma_f32_16x16x32_bf16 v[168:171], a[64:67], a[144:147], v[168:171]\n"
"v_mfma_f32_16x16x32_bf16 v[168:171], a[68:71], a[148:151], v[168:171]\n"
"s_sub_u32 s67, 1280, s60\n"
"v_mfma_f32_16x16x32_bf16 v[196:199], a[76:79], a[148:151], v[196:199]\n"
"v_mfma_f32_16x16x32_bf16 v[196:199], a[72:75], a[144:147], v[196:199]\n"
"v_mfma_f32_16x16x32_bf16 a[20:23], a[72:75], a[152:155], a[20:23]\n"
"s_waitcnt lgkmcnt(0)\n"
"v_mfma_f32_16x16x32_bf16 a[20:23], a[76:79], a[156:159], a[20:23]\n"
"buffer_load_dwordx4 v6, s[48:51], s67 offen offset:0  lds\n"
"v_mfma_f32_16x16x32_bf16 a[16:19], a[68:71], a[156:159], a[16:19]\n"
"s_sub_u32 s67, 16640, s60\n"
"v_mfma_f32_16x16x32_bf16 a[16:19], a[64:67], a[152:155], a[16:19]\n"
"s_waitcnt vmcnt(17)\n"
"s_barrier\n"
"v_mfma_f32_16x16x32_bf16 v[200:203], a[80:83], a[96:99], v[200:203]\n"
"s_waitcnt vmcnt(9)\n"
"ds_read_b64_tr_b16 a[32:33], v9 offset:8192\n"
"v_mfma_f32_16x16x32_bf16 v[200:203], a[84:87], a[100:103], v[200:203]\n"
"ds_read_b64_tr_b16 a[34:35], v9 offset:8704\n"
"v_mfma_f32_16x16x32_bf16 v[228:231], a[92:95], a[100:103], v[228:231]\n"
"ds_read_b128 a[100:103], v13 offset:32768\n"
"buffer_load_dwordx4 v6, s[48:51], s67 offen offset:1024  lds\n"
"v_mfma_f32_16x16x32_bf16 v[228:231], a[88:91], a[96:99], v[228:231]\n"
"ds_read_b128 a[96:99], v12 offset:32768\n"
"v_mfma_f32_16x16x32_bf16 v[232:235], a[88:91], a[104:107], v[232:235]\n"
"s_sub_u32 s67, 32000, s60\n"
"ds_read_b64_tr_b16 a[36:37], v9 offset:12288\n"
"v_mfma_f32_16x16x32_bf16 v[232:235], a[92:95], a[108:111], v[232:235]\n"
"ds_read_b64_tr_b16 a[38:39], v9 offset:12800\n"
"v_mfma_f32_16x16x32_bf16 v[204:207], a[84:87], a[108:111], v[204:207]\n"
"ds_read_b128 a[108:111], v13 offset:34816\n"
"buffer_load_dwordx4 v6, s[48:51], s67 offen offset:2048  lds\n"
"v_mfma_f32_16x16x32_bf16 v[204:207], a[80:83], a[104:107], v[204:207]\n"
"ds_read_b128 a[104:107], v12 offset:34816\n"
"ds_read_b64_tr_b16 a[40:41], v9 offset:8200\n"
"v_mfma_f32_16x16x32_bf16 v[208:211], a[80:83], a[112:115], v[208:211]\n"
"s_sub_u32 s67, 47360, s60\n"
"ds_read_b64_tr_b16 a[42:43], v9 offset:8712\n"
"v_mfma_f32_16x16x32_bf16 v[208:211], a[84:87], a[116:119], v[208:211]\n"
"ds_read_b64_tr_b16 a[44:45], v9 offset:12296\n"
"v_mfma_f32_16x16x32_bf16 v[236:239], a[92:95], a[116:119], v[236:239]\n"
"ds_read_b128 a[116:119], v13 offset:36864\n"
"v_mfma_f32_16x16x32_bf16 v[236:239], a[88:91], a[112:115], v[236:239]\n"
"ds_read_b128 a[112:115], v12 offset:36864\n"
"buffer_load_dwordx4 v6, s[48:51], s67 offen offset:3072  lds\n"
"v_mfma_f32_16x16x32_bf16 v[240:243], a[88:91], a[120:123], v[240:243]\n"
"ds_read_b64_tr_b16 a[46:47], v9 offset:12808\n"
"v_mfma_f32_16x16x32_bf16 v[240:243], a[92:95], a[124:127], v[240:243]\n"
"s_add_u32 m0, s64, 20352\n"
"ds_read_b64_tr_b16 a[48:49], v15 offset:8192\n"
"v_mfma_f32_16x16x32_bf16 v[212:215], a[84:87], a[124:127], v[212:215]\n"
"ds_read_b128 a[124:127], v13 offset:38912\n"
"v_mfma_f32_16x16x32_bf16 v[212:215], a[80:83], a[120:123], v[212:215]\n"
"ds_read_b128 a[120:123], v12 offset:38912\n"
"v_mfma_f32_16x16x32_bf16 v[216:219], a[80:83], a[128:131], v[216:219]\n"
"ds_read_b64_tr_b16 a[50:51], v15 offset:8704\n"
"buffer_load_dwordx4 v6, s[40:43], s84 offen offset:128  lds\n"
"v_mfma_f32_16x16x32_bf16 v[216:219], a[84:87], a[132:135], v[216:219]\n"
"ds_read_b64_tr_b16 a[52:53], v15 offset:12288\n"
"v_mfma_f32_16x16x32_bf16 v[244:247], a[92:95], a[132:135], v[244:247]\n"
"ds_read_b128 a[132:135], v13 offset:24576\n"
"v_mfma_f32_16x16x32_bf16 v[244:247], a[88:91], a[128:131], v[244:247]\n"
"ds_read_b128 a[128:131], v12 offset:24576\n"
"buffer_load_dwordx4 v6, s[40:43], s85 offen offset:1152  lds\n"
"v_mfma_f32_16x16x32_bf16 v[248:251], a[88:91], a[136:139], v[248:251]\n"
"ds_read_b64_tr_b16 a[54:55], v15 offset:12800\n"
"v_mfma_f32_16x16x32_bf16 v[248:251], a[92:95], a[140:143], v[248:251]\n"
"ds_read_b64_tr_b16 a[56:57], v15 offset:8200\n"
"v_mfma_f32_16x16x32_bf16 v[220:223], a[84:87], a[140:143], v[220:223]\n"
"ds_read_b128 a[140:143], v13 offset:26624\n"
"v_mfma_f32_16x16x32_bf16 v[220:223], a[80:83], a[136:139], v[220:223]\n"
"ds_read_b128 a[136:139], v12 offset:26624\n"
"v_mfma_f32_16x16x32_bf16 v[224:227], a[80:83], a[144:147], v[224:227]\n"
"ds_read_b64_tr_b16 a[58:59], v15 offset:8712\n"
"buffer_load_dwordx4 v6, s[40:43], s86 offen offset:2176  lds\n"
"v_mfma_f32_16x16x32_bf16 v[224:227], a[84:87], a[148:151], v[224:227]\n"
"ds_read_b64_tr_b16 a[60:61], v15 offset:12296\n"
"v_mfma_f32_16x16x32_bf16 v[252:255], a[92:95], a[148:151], v[252:255]\n"
"ds_read_b128 a[148:151], v13 offset:28672\n"
"v_mfma_f32_16x16x32_bf16 v[252:255], a[88:91], a[144:147], v[252:255]\n"
"ds_read_b128 a[144:147], v12 offset:28672\n"
"ds_read_b64_tr_b16 a[62:63], v15 offset:12808\n"
"v_mfma_f32_16x16x32_bf16 a[28:31], a[88:91], a[152:155], a[28:31]\n"
"buffer_load_dwordx4 v6, s[40:43], s87 offen offset:3200  lds\n"
"v_mfma_f32_16x16x32_bf16 a[28:31], a[92:95], a[156:159], a[28:31]\n"
"v_mfma_f32_16x16x32_bf16 a[24:27], a[84:87], a[156:159], a[24:27]\n"
"s_mov_b32 s38, 1572864\n"
"ds_read_b128 a[156:159], v13 offset:30720\n"
"v_mfma_f32_16x16x32_bf16 a[24:27], a[80:83], a[152:155], a[24:27]\n"
"s_waitcnt lgkmcnt(15)\n"
"ds_read_b128 a[152:155], v12 offset:30720\n"
"v_mfma_f32_16x16x32_bf16 v[32:35], a[32:35], a[96:99], v[32:35]\n"
"v_mfma_f32_16x16x32_bf16 v[32:35], a[36:39], a[100:103], v[32:35]\n"
"s_waitcnt lgkmcnt(2)\n"
"v_mfma_f32_16x16x32_bf16 v[60:63], a[44:47], a[100:103], v[60:63]\n"
"buffer_store_dwordx4 a[160:163], v0, s[36:39], %23 offen offset:0 sc1\n"
"v_mfma_f32_16x16x32_bf16 v[60:63], a[40:43], a[96:99], v[60:63]\n"
"v_mfma_f32_16x16x32_bf16 v[64:67], a[40:43], a[104:107], v[64:67]\n"
"s_add_u32 m0, s64, 8192\n"
"v_mfma_f32_16x16x32_bf16 v[64:67], a[44:47], a[108:111], v[64:67]\n"
"v_mfma_f32_16x16x32_bf16 v[36:39], a[36:39], a[108:111], v[36:39]\n"
"s_sub_u32 s67, 1310720, s75\n"
"v_mfma_f32_16x16x32_bf16 v[36:39], a[32:35], a[104:107], v[36:39]\n"
"v_mfma_f32_16x16x32_bf16 v[40:43], a[32:35], a[112:115], v[40:43]\n"
"buffer_load_dwordx4 v11, s[52:55], s67 offen offset:0  lds\n"
"v_mfma_f32_16x16x32_bf16 v[40:43], a[36:39], a[116:119], v[40:43]\n"
"v_mfma_f32_16x16x32_bf16 v[68:71], a[44:47], a[116:119], v[68:71]\n"
"s_sub_u32 s67, 1326080, s75\n"
"v_mfma_f32_16x16x32_bf16 v[68:71], a[40:43], a[112:115], v[68:71]\n"
"v_mfma_f32_16x16x32_bf16 v[72:75], a[40:43], a[120:123], v[72:75]\n"
"buffer_load_dwordx4 v11, s[52:55], s67 offen offset:1024  lds\n"
"v_mfma_f32_16x16x32_bf16 v[72:75], a[44:47], a[124:127], v[72:75]\n"
"v_mfma_f32_16x16x32_bf16 v[44:47], a[36:39], a[124:127], v[44:47]\n"
"s_sub_u32 s67, 1341440, s75\n"
"v_mfma_f32_16x16x32_bf16 v[44:47], a[32:35], a[120:123], v[44:47]\n"
"v_mfma_f32_16x16x32_bf16 v[48:51], a[32:35], a[128:131], v[48:51]\n"
"buffer_load_dwordx4 v11, s[52:55], s67 offen offset:2048  lds\n"
"v_mfma_f32_16x16x32_bf16 v[48:51], a[36:39], a[132:135], v[48:51]\n"
"s_sub_u32 s67, 1356800, s75\n"
"v_mfma_f32_16x16x32_bf16 v[76:79], a[44:47], a[132:135], v[76:79]\n"
"s_waitcnt vmcnt(12)\n"
"ds_read_b64_tr_b16 a[64:65], v9 offset:0\n"
"v_mfma_f32_16x16x32_bf16 v[76:79], a[40:43], a[128:131], v[76:79]\n"
"ds_read_b64_tr_b16 a[66:67], v9 offset:512\n"
"v_mfma_f32_16x16x32_bf16 v[80:83], a[40:43], a[136:139], v[80:83]\n"
"ds_read_b64_tr_b16 a[68:69], v9 offset:4096\n"
"buffer_load_dwordx4 v11, s[52:55], s67 offen offset:3072  lds\n"
"v_mfma_f32_16x16x32_bf16 v[80:83], a[44:47], a[140:143], v[80:83]\n"
"ds_read_b64_tr_b16 a[70:71], v9 offset:4608\n"
"v_mfma_f32_16x16x32_bf16 v[52:55], a[36:39], a[140:143], v[52:55]\n"
"s_add_u32 m0, s64, 12288\n"
"ds_read_b64_tr_b16 a[72:73], v9 offset:8\n"
"v_mfma_f32_16x16x32_bf16 v[52:55], a[32:35], a[136:139], v[52:55]\n"
"s_waitcnt lgkmcnt(5)\n"
"ds_read_b64_tr_b16 a[74:75], v9 offset:520\n"
"v_mfma_f32_16x16x32_bf16 v[56:59], a[32:35], a[144:147], v[56:59]\n"
"s_sub_u32 s67, 1376256, s75\n"
"ds_read_b64_tr_b16 a[76:77], v9 offset:4104\n"
"v_mfma_f32_16x16x32_bf16 v[56:59], a[36:39], a[148:151], v[56:59]\n"
"ds_read_b64_tr_b16 a[78:79], v9 offset:4616\n"
"v_mfma_f32_16x16x32_bf16 v[84:87], a[44:47], a[148:151], v[84:87]\n"
"buffer_load_dwordx4 v11, s[52:55], s67 offen offset:0  lds\n"
"v_mfma_f32_16x16x32_bf16 v[84:87], a[40:43], a[144:147], v[84:87]\n"
"ds_read_b64_tr_b16 a[80:81], v15 offset:0\n"
"v_mfma_f32_16x16x32_bf16 a[4:7], a[40:43], a[152:155], a[4:7]\n"
"s_sub_u32 s67, 1391616, s75\n"
"ds_read_b64_tr_b16 a[82:83], v15 offset:512\n"
"v_mfma_f32_16x16x32_bf16 a[4:7], a[44:47], a[156:159], a[4:7]\n"
"ds_read_b64_tr_b16 a[84:85], v15 offset:4096\n"
"v_mfma_f32_16x16x32_bf16 a[0:3], a[36:39], a[156:159], a[0:3]\n"
"ds_read_b64_tr_b16 a[86:87], v15 offset:4608\n"
"buffer_load_dwordx4 v11, s[52:55], s67 offen offset:1024  lds\n"
"v_mfma_f32_16x16x32_bf16 a[0:3], a[32:35], a[152:155], a[0:3]\n"
"ds_read_b64_tr_b16 a[88:89], v15 offset:8\n"
"v_mfma_f32_16x16x32_bf16 v[88:91], a[48:51], a[96:99], v[88:91]\n"
"s_sub_u32 s67, 1406976, s75\n"
"ds_read_b64_tr_b16 a[90:91], v15 offset:520\n"
"v_mfma_f32_16x16x32_bf16 v[88:91], a[52:55], a[100:103], v[88:91]\n"
"ds_read_b64_tr_b16 a[92:93], v15 offset:4104\n"
"v_mfma_f32_16x16x32_bf16 v[116:119], a[60:63], a[100:103], v[116:119]\n"
"ds_read_b64_tr_b16 a[94:95], v15 offset:4616\n"
"v_mfma_f32_16x16x32_bf16 v[116:119], a[56:59], a[96:99], v[116:119]\n"
"buffer_load_dwordx4 v11, s[52:55], s67 offen offset:2048  lds\n"
"v_mfma_f32_16x16x32_bf16 v[120:123], a[56:59], a[104:107], v[120:123]\n"
"v_mfma_f32_16x16x32_bf16 v[120:123], a[60:63], a[108:111], v[120:123]\n"
"s_sub_u32 s67, 1422336, s75\n"
"v_mfma_f32_16x16x32_bf16 v[92:95], a[52:55], a[108:111], v[92:95]\n"
"v_mfma_f32_16x16x32_bf16 v[92:95], a[48:51], a[104:107], v[92:95]\n"
"buffer_load_dwordx4 v11, s[52:55], s67 offen offset:3072  lds\n"
"v_mfma_f32_16x16x32_bf16 v[96:99], a[48:51], a[112:115], v[96:99]\n"
"s_waitcnt lgkmcnt(0)\n"
"s_mov_b32 s99, 0\n"
"v_mfma_f32_16x16x32_bf16 v[96:99], a[52:55], a[116:119], v[96:99]\n"
"s_mov_b32 s98, 0\n"
"s_add_u32 m0, s64, 0\n"
"v_mfma_f32_16x16x32_bf16 v[124:127], a[60:63], a[116:119], v[124:127]\n"
"s_memtime s[76:77]\n"
"s_load_dword s98, s[96:97], 0 glc\n"
"v_mfma_f32_16x16x32_bf16 v[124:127], a[56:59], a[112:115], v[124:127]\n"
"s_sub_u32 s67, 1310848, s75\n"
"v_mfma_f32_16x16x32_bf16 v[128:131], a[56:59], a[120:123], v[128:131]\n"
"buffer_load_dwordx4 v11, s[52:55], s67 offen offset:0  lds\n"
"v_mfma_f32_16x16x32_bf16 v[128:131], a[60:63], a[124:127], v[128:131]\n"
"v_mfma_f32_16x16x32_bf16 v[100:103], a[52:55], a[124:127], v[100:103]\n"
"s_sub_u32 s67, 1326208, s75\n"
"v_mfma_f32_16x16x32_bf16 v[100:103], a[48:51], a[120:123], v[100:103]\n"
"v_mfma_f32_16x16x32_bf16 v[104:107], a[48:51], a[128:131], v[104:107]\n"
"buffer_load_dwordx4 v11, s[52:55], s67 offen offset:1024  lds\n"
"v_mfma_f32_16x16x32_bf16 v[104:107], a[52:55], a[132:135], v[104:107]\n"
"v_mfma_f32_16x16x32_bf16 v[132:135], a[60:63], a[132:135], v[132:135]\n"
"s_sub_u32 s67, 1341568, s75\n"
"v_mfma_f32_16x16x32_bf16 v[132:135], a[56:59], a[128:131], v[132:135]\n"
"s_mov_b32 s30, s76\n"
"v_mfma_f32_16x16x32_bf16 v[136:139], a[56:59], a[136:139], v[136:139]\n"
"v_mfma_f32_16x16x32_bf16 v[136:139], a[60:63], a[140:143], v[136:139]\n"
"buffer_load_dwordx4 v11, s[52:55], s67 offen offset:2048  lds\n"
"v_mfma_f32_16x16x32_bf16 v[108:111], a[52:55], a[140:143], v[108:111]\n"
"s_sub_u32 s99, s99, s98\n"
"s_sub_u32 s99, s99, s98\n"
"v_mfma_f32_16x16x32_bf16 v[108:111], a[48:51], a[136:139], v[108:111]\n"
"s_sub_u32 s99, s99, s98\n"
"s_sub_u32 s67, 1356928, s75\n"
"v_mfma_f32_16x16x32_bf16 v[112:115], a[48:51], a[144:147], v[112:115]\n"
"s_sub_u32 s99, s99, s98\n"
"v_mfma_f32_16x16x32_bf16 v[112:115], a[52:55], a[148:151], v[112:115]\n"
"buffer_load_dwordx4 v11, s[52:55], s67 offen offset:3072  lds\n"
"v_mfma_f32_16x16x32_bf16 v[140:143], a[60:63], a[148:151], v[140:143]\n"
"s_sub_u32 s99, s99, s98\n"
"s_sub_u32 s99, s99, s98\n"
"v_mfma_f32_16x16x32_bf16 v[140:143], a[56:59], a[144:147], v[140:143]\n"
"s_sub_u32 s99, s99, s98\n"
"s_add_u32 m0, s64, 4096\n"
"v_mfma_f32_16x16x32_bf16 a[12:15], a[56:59], a[152:155], a[12:15]\n"
"s_sub_u32 s99, s99, s98\n"
"s_sub_u32 s67, 1376384, s75\n"
"v_mfma_f32_16x16x32_bf16 a[12:15], a[60:63], a[156:159], a[12:15]\n"
"buffer_load_dwordx4 v11, s[52:55], s67 offen offset:0  lds\n"
"v_mfma_f32_16x16x32_bf16 a[8:11], a[52:55], a[156:159], a[8:11]\n"
"s_sub_u32 s99, s99, s98\n"
"s_sub_u32 s99, s99, s98\n"
"v_mfma_f32_16x16x32_bf16 a[8:11], a[48:51], a[152:155], a[8:11]\n"
"s_sub_u32 s99, s99, s98\n"
"s_sub_u32 s67, 1391744, s75\n"
"v_mfma_f32_16x16x32_bf16 v[144:147], a[64:67], a[96:99], v[144:147]\n"
"s_sub_u32 s99, s99, s98\n"
"v_mfma_f32_16x16x32_bf16 v[144:147], a[68:71], a[100:103], v[144:147]\n"
"buffer_load_dwordx4 v11, s[52:55], s67 offen offset:1024  lds\n"
"v_mfma_f32_16x16x32_bf16 v[172:175], a[76:79], a[100:103], v[172:175]\n"
"s_sub_u32 s99, s99, s98\n"
"s_sub_u32 s99, s99, s98\n"
"v_mfma_f32_16x16x32_bf16 v[172:175], a[72:75], a[96:99], v[172:175]\n"
"s_sub_u32 s99, s99, s98\n"
"s_sub_u32 s67, 1407104, s75\n"
"v_mfma_f32_16x16x32_bf16 v[176:179], a[72:75], a[104:107], v[176:179]\n"
"s_sub_u32 s99, s99, s98\n"
"v_mfma_f32_16x16x32_bf16 v[176:179], a[76:79], a[108:111], v[176:179]\n"
"v_mfma_f32_16x16x32_bf16 v[148:151], a[68:71], a[108:111], v[148:151]\n"
"s_sub_u32 s99, s99, s98\n"
"s_sub_u32 s99, s99, s98\n"
"v_mfma_f32_16x16x32_bf16 v[148:151], a[64:67], a[104:107], v[148:151]\n"
"s_sub_u32 s99, s99, s98\n"
"s_sub_u32 s71, s64, s64\n"
"buffer_load_dwordx4 v11, s[52:55], s67 offen offset:2048  lds\n"
"v_mfma_f32_16x16x32_bf16 v[152:155], a[64:67], a[112:115], v[152:155]\n"
"s_sub_u32 s66, 40960, s73\n"
"s_sub_u32 s99, s99, s98\n"
"v_mfma_f32_16x16x32_bf16 v[152:155], a[68:71], a[116:119], v[152:155]\n"
"s_add_u32 s71, s71, 20480\n"
"s_sub_u32 s67, 1422464, s75\n"
"v_mfma_f32_16x16x32_bf16 v[180:183], a[76:79], a[116:119], v[180:183]\n"
"s_sub_u32 s99, s99, s98\n"
"v_add_u32 v12, s66, v7\n"
"s_sub_u32 s99, s99, s98\n"
"v_mfma_f32_16x16x32_bf16 v[180:183], a[72:75], a[112:115], v[180:183]\n"
"s_sub_u32 s99, s99, s98\n"
"v_mfma_f32_16x16x32_bf16 v[184:187], a[72:75], a[120:123], v[184:187]\n"
"s_sub_u32 s99, s99, s98\n"
"v_add_u32 v13, s66, v8\n"
"v_mfma_f32_16x16x32_bf16 v[184:187], a[76:79], a[124:127], v[184:187]\n"
"s_memtime s[76:77]\n"
"buffer_load_dwordx4 v11, s[52:55], s67 offen offset:3072  lds\n"
"v_add_u32 v20, s71, v7\n"
"v_mfma_f32_16x16x32_bf16 v[156:159], a[68:71], a[124:127], v[156:159]\n"
"s_cmp_lt_u32 %16, 2\n"
"s_cselect_b32 s67, 16384, 0\n"
"s_add_u32 s67, s67, s72\n"
"v_mfma_f32_16x16x32_bf16 v[156:159], a[64:67], a[120:123], v[156:159]\n"
"s_waitcnt lgkmcnt(0)\n"
"v_add_u32 v21, s71, v8\n"
"s_add_u32 m0, s67, 98304\n"
"s_sub_u32 s67, 1536, s60\n"
"v_mfma_f32_16x16x32_bf16 v[160:163], a[64:67], a[128:131], v[160:163]\n"
"s_sub_u32 s30, s76, s30\n"
"buffer_load_dwordx4 v6, s[48:51], s67 offen offset:0  lds\n"
"v_mfma_f32_16x16x32_bf16 v[160:163], a[68:71], a[132:135], v[160:163]\n"
"s_cmp_lt_u32 s30, 620\n"
"s_cselect_b32 s99, s99, 0\n"
"v_mfma_f32_16x16x32_bf16 v[188:191], a[76:79], a[132:135], v[188:191]\n"
"s_max_u32 s31, s31, s99\n"
"s_sub_u32 s67, 16896, s60\n"
"v_mfma_f32_16x16x32_bf16 v[188:191], a[72:75], a[128:131], v[188:191]\n"
"s_waitcnt vmcnt(18)\n"
"ds_read_b128 a[208:211], v20 offset:0\n"
"v_mfma_f32_16x16x32_bf16 v[192:195], a[72:75], a[136:139], v[192:195]\n"
"ds_read_b128 a[212:215], v21 offset:0\n"
"v_mfma_f32_16x16x32_bf16 v[192:195], a[76:79], a[140:143], v[192:195]\n"
"ds_read_b128 a[216:219], v20 offset:2048\n"
"v_mfma_f32_16x16x32_bf16 v[164:167], a[68:71], a[140:143], v[164:167]\n"
"buffer_load_dwordx4 v6, s[48:51], s67 offen offset:1024  lds\n"
"v_mfma_f32_16x16x32_bf16 v[164:167], a[64:67], a[136:139], v[164:167]\n"
"ds_read_b128 a[220:223], v21 offset:2048\n"
"v_mfma_f32_16x16x32_bf16 v[168:171], a[64:67], a[144:147], v[168:171]\n"
"s_sub_u32 s67, 32256, s60\n"
"v_mfma_f32_16x16x32_bf16 v[168:171], a[68:71], a[148:151], v[168:171]\n"
"v_mfma_f32_16x16x32_bf16 v[196:199], a[76:79], a[148:151], v[196:199]\n"
"buffer_load_dwordx4 v6, s[48:51], s67 offen offset:2048  lds\n"
"v_mfma_f32_16x16x32_bf16 v[196:199], a[72:75], a[144:147], v[196:199]\n"
"v_mfma_f32_16x16x32_bf16 a[20:23], a[72:75], a[152:155], a[20:23]\n"
"s_sub_u32 s67, 47616, s60\n"
"v_mfma_f32_16x16x32_bf16 a[20:23], a[76:79], a[156:159], a[20:23]\n"
"s_waitcnt lgkmcnt(0)\n"
"v_mfma_f32_16x16x32_bf16 a[16:19], a[68:71], a[156:159], a[16:19]\n"
"buffer_load_dwordx4 v6, s[48:51], s67 offen offset:3072  lds\n"
"v_mfma_f32_16x16x32_bf16 a[16:19], a[64:67], a[152:155], a[16:19]\n"
"s_barrier\n"
"s_waitcnt vmcnt(12)\n"
"ds_read_b64_tr_b16 a[32:33], v9 offset:8192\n"
"v_mfma_f32_16x16x32_bf16 v[200:203], a[80:83], a[96:99], v[200:203]\n"
"s_add_u32 m0, s64, 16384\n"
"ds_read_b64_tr_b16 a[34:35], v9 offset:8704\n"
"v_mfma_f32_16x16x32_bf16 v[200:203], a[84:87], a[100:103], v[200:203]\n"
"ds_read_b64_tr_b16 a[36:37], v9 offset:12288\n"
"v_mfma_f32_16x16x32_bf16 v[228:231], a[92:95], a[100:103], v[228:231]\n"
"s_add_u32 s67, %21, 4194304\n"
"ds_read_b128 a[100:103], v13 offset:32768\n"
"v_mfma_f32_16x16x32_bf16 v[228:231], a[88:91], a[96:99], v[228:231]\n"
"ds_read_b128 a[96:99], v12 offset:32768\n"
"v_mfma_f32_16x16x32_bf16 v[232:235], a[88:91], a[104:107], v[232:235]\n"
"ds_read_b64_tr_b16 a[38:39], v9 offset:12800\n"
"buffer_load_dwordx4 v11, s[52:55], s67 offen offset:0  lds\n"
"v_mfma_f32_16x16x32_bf16 v[232:235], a[92:95], a[108:111], v[232:235]\n"
"ds_read_b64_tr_b16 a[40:41], v9 offset:8200\n"
"v_mfma_f32_16x16x32_bf16 v[204:207], a[84:87], a[108:111], v[204:207]\n"
"s_add_u32 s67, %21, 4209664\n"
"ds_read_b128 a[108:111], v13 offset:34816\n"
"v_mfma_f32_16x16x32_bf16 v[204:207], a[80:83], a[104:107], v[204:207]\n"
"ds_read_b128 a[104:107], v12 offset:34816\n"
"v_mfma_f32_16x16x32_bf16 v[208:211], a[80:83], a[112:115], v[208:211]\n"
"ds_read_b64_tr_b16 a[42:43], v9 offset:8712\n"
"buffer_load_dwordx4 v11, s[52:55], s67 offen offset:1024  lds\n"
"v_mfma_f32_16x16x32_bf16 v[208:211], a[84:87], a[116:119], v[208:211]\n"
"ds_read_b64_tr_b16 a[44:45], v9 offset:12296\n"
"v_mfma_f32_16x16x32_bf16 v[236:239], a[92:95], a[116:119], v[236:239]\n"
"s_add_u32 s67, %21, 4225024\n"
"ds_read_b128 a[116:119], v13 offset:36864\n"
"v_mfma_f32_16x16x32_bf16 v[236:239], a[88:91], a[112:115], v[236:239]\n"
"ds_read_b128 a[112:115], v12 offset:36864\n"
"v_mfma_f32_16x16x32_bf16 v[240:243], a[88:91], a[120:123], v[240:243]\n"
"ds_read_b64_tr_b16 a[46:47], v9 offset:12808\n"
"buffer_load_dwordx4 v11, s[52:55], s67 offen offset:2048  lds\n"
"v_mfma_f32_16x16x32_bf16 v[240:243], a[92:95], a[124:127], v[240:243]\n"
"ds_read_b64_tr_b16 a[48:49], v15 offset:8192\n"
"v_mfma_f32_16x16x32_bf16 v[212:215], a[84:87], a[124:127], v[212:215]\n"
"s_add_u32 s67, %21, 4240384\n"
"ds_read_b128 a[124:127], v13 offset:38912\n"
"v_mfma_f32_16x16x32_bf16 v[212:215], a[80:83], a[120:123], v[212:215]\n"
"ds_read_b128 a[120:123], v12 offset:38912\n"
"v_mfma_f32_16x16x32_bf16 v[216:219], a[80:83], a[128:131], v[216:219]\n"
"ds_read_b64_tr_b16 a[50:51], v15 offset:8704\n"
"buffer_load_dwordx4 v11, s[52:55], s67 offen offset:3072  lds\n"
"v_mfma_f32_16x16x32_bf16 v[216:219], a[84:87], a[132:135], v[216:219]\n"
"ds_read_b64_tr_b16 a[52:53], v15 offset:12288\n"
"v_mfma_f32_16x16x32_bf16 v[244:247], a[92:95], a[132:135], v[244:247]\n"
"ds_read_b128 a[132:135], v13 offset:24576\n"
"v_mfma_f32_16x16x32_bf16 v[244:247], a[88:91], a[128:131], v[244:247]\n"
"ds_read_b128 a[128:131], v12 offset:24576\n"
"v_mfma_f32_16x16x32_bf16 v[248:251], a[88:91], a[136:139], v[248:251]\n"
"s_add_u32 m0, s64, 8192\n"
"ds_read_b64_tr_b16 a[54:55], v15 offset:12800\n"
"v_mfma_f32_16x16x32_bf16 v[248:251], a[92:95], a[140:143], v[248:251]\n"
"ds_read_b64_tr_b16 a[56:57], v15 offset:8200\n"
"v_mfma_f32_16x16x32_bf16 v[220:223], a[84:87], a[140:143], v[220:223]\n"
"ds_read_b128 a[140:143], v13 offset:26624\n"
"v_mfma_f32_16x16x32_bf16 v[220:223], a[80:83], a[136:139], v[220:223]\n"
"ds_read_b128 a[136:139], v12 offset:26624\n"
"v_mfma_f32_16x16x32_bf16 v[224:227], a[80:83], a[144:147], v[224:227]\n"
"ds_read_b64_tr_b16 a[58:59], v15 offset:8712\n"
"v_mfma_f32_16x16x32_bf16 v[224:227], a[84:87], a[148:151], v[224:227]\n"
"ds_read_b64_tr_b16 a[60:61], v15 offset:12296\n"
"v_mfma_f32_16x16x32_bf16 v[252:255], a[92:95], a[148:151], v[252:255]\n"
"s_sub_u32 s67, 1572864, s75\n"
"ds_read_b128 a[148:151], v13 offset:28672\n"
"v_mfma_f32_16x16x32_bf16 v[252:255], a[88:91], a[144:147], v[252:255]\n"
"ds_read_b128 a[144:147], v12 offset:28672\n"
"ds_read_b64_tr_b16 a[62:63], v15 offset:12808\n"
"v_mfma_f32_16x16x32_bf16 a[28:31], a[88:91], a[152:155], a[28:31]\n"
"v_mfma_f32_16x16x32_bf16 a[28:31], a[92:95], a[156:159], a[28:31]\n"
"v_mfma_f32_16x16x32_bf16 a[24:27], a[84:87], a[156:159], a[24:27]\n"
"ds_read_b128 a[156:159], v13 offset:30720\n"
"v_mfma_f32_16x16x32_bf16 a[24:27], a[80:83], a[152:155], a[24:27]\n"
"s_waitcnt lgkmcnt(15)\n"
"ds_read_b128 a[152:155], v12 offset:30720\n"
"v_mfma_f32_16x16x32_bf16 v[32:35], a[32:35], a[96:99], v[32:35]\n"
"v_mfma_f32_16x16x32_bf16 v[32:35], a[36:39], a[100:103], v[32:35]\n"
"s_waitcnt lgkmcnt(2)\n"
"v_mfma_f32_16x16x32_bf16 v[60:63], a[44:47], a[100:103], v[60:63]\n"
"buffer_load_dwordx4 v11, s[52:55], s67 offen offset:0  lds\n"
"v_mfma_f32_16x16x32_bf16 v[60:63], a[40:43], a[96:99], v[60:63]\n"
"v_mfma_f32_16x16x32_bf16 v[64:67], a[40:43], a[104:107], v[64:67]\n"
"s_sub_u32 s67, 1588224, s75\n"
"v_mfma_f32_16x16x32_bf16 v[64:67], a[44:47], a[108:111], v[64:67]\n"
"v_mfma_f32_16x16x32_bf16 v[36:39], a[36:39], a[108:111], v[36:39]\n"
"v_mfma_f32_16x16x32_bf16 v[36:39], a[32:35], a[104:107], v[36:39]\n"
"buffer_load_dwordx4 v11, s[52:55], s67 offen offset:1024  lds\n"
"v_mfma_f32_16x16x32_bf16 v[40:43], a[32:35], a[112:115], v[40:43]\n"
"v_mfma_f32_16x16x32_bf16 v[40:43], a[36:39], a[116:119], v[40:43]\n"
"s_sub_u32 s67, 1603584, s75\n"
"v_mfma_f32_16x16x32_bf16 v[68:71], a[44:47], a[116:119], v[68:71]\n"
"v_mfma_f32_16x16x32_bf16 v[68:71], a[40:43], a[112:115], v[68:71]\n"
"buffer_load_dwordx4 v11, s[52:55], s67 offen offset:2048  lds\n"
"v_mfma_f32_16x16x32_bf16 v[72:75], a[40:43], a[120:123], v[72:75]\n"
"v_mfma_f32_16x16x32_bf16 v[72:75], a[44:47], a[124:127], v[72:75]\n"
"s_sub_u32 s67, 1618944, s75\n"
"v_mfma_f32_16x16x32_bf16 v[44:47], a[36:39], a[124:127], v[44:47]\n"
"v_mfma_f32_16x16x32_bf16 v[44:47], a[32:35], a[120:123], v[44:47]\n"
"v_mfma_f32_16x16x32_bf16 v[48:51], a[32:35], a[128:131], v[48:51]\n"
"buffer_load_dwordx4 v11, s[52:55], s67 offen offset:3072  lds\n"
"v_mfma_f32_16x16x32_bf16 v[48:51], a[36:39], a[132:135], v[48:51]\n"
"s_add_u32 m0, s64, 12288\n"
"v_mfma_f32_16x16x32_bf16 v[76:79], a[44:47], a[132:135], v[76:79]\n"
"s_waitcnt vmcnt(12)\n"
"ds_read_b64_tr_b16 a[64:65], v9 offset:0\n"
"v_mfma_f32_16x16x32_bf16 v[76:79], a[40:43], a[128:131], v[76:79]\n"
"s_sub_u32 s67, 1638400, s75\n"
"ds_read_b64_tr_b16 a[66:67], v9 offset:512\n"
"v_mfma_f32_16x16x32_bf16 v[80:83], a[40:43], a[136:139], v[80:83]\n"
"ds_read_b64_tr_b16 a[68:69], v9 offset:4096\n"
"v_mfma_f32_16x16x32_bf16 v[80:83], a[44:47], a[140:143], v[80:83]\n"
"ds_read_b64_tr_b16 a[70:71], v9 offset:4608\n"
"buffer_load_dwordx4 v11, s[52:55], s67 offen offset:0  lds\n"
"v_mfma_f32_16x16x32_bf16 v[52:55], a[36:39], a[140:143], v[52:55]\n"
"s_sub_u32 s67, 1653760, s75\n"
"ds_read_b64_tr_b16 a[72:73], v9 offset:8\n"
"v_mfma_f32_16x16x32_bf16 v[52:55], a[32:35], a[136:139], v[52:55]\n"
"s_waitcnt lgkmcnt(5)\n"
"ds_read_b64_tr_b16 a[74:75], v9 offset:520\n"
"v_mfma_f32_16x16x32_bf16 v[56:59], a[32:35], a[144:147], v[56:59]\n"
"ds_read_b64_tr_b16 a[76:77], v9 offset:4104\n"
"v_mfma_f32_16x16x32_bf16 v[56:59], a[36:39], a[148:151], v[56:59]\n"
"ds_read_b64_tr_b16 a[78:79], v9 offset:4616\n"
"v_mfma_f32_16x16x32_bf16 v[84:87], a[44:47], a[148:151], v[84:87]\n"
"buffer_load_dwordx4 v11, s[52:55], s67 offen offset:1024  lds\n"
"v_mfma_f32_16x16x32_bf16 v[84:87], a[40:43], a[144:147], v[84:87]\n"
"ds_read_b64_tr_b16 a[80:81], v15 offset:0\n"
"v_mfma_f32_16x16x32_bf16 a[4:7], a[40:43], a[152:155], a[4:7]\n"
"s_sub_u32 s67, 1669120, s75\n"
"ds_read_b64_tr_b16 a[82:83], v15 offset:512\n"
"v_mfma_f32_16x16x32_bf16 a[4:7], a[44:47], a[156:159], a[4:7]\n"
"ds_read_b64_tr_b16 a[84:85], v15 offset:4096\n"
"v_mfma_f32_16x16x32_bf16 a[0:3], a[36:39], a[156:159], a[0:3]\n"
"ds_read_b64_tr_b16 a[86:87], v15 offset:4608\n"
"buffer_load_dwordx4 v11, s[52:55], s67 offen offset:2048  lds\n"
"v_mfma_f32_16x16x32_bf16 a[0:3], a[32:35], a[152:155], a[0:3]\n"
"ds_read_b64_tr_b16 a[88:89], v15 offset:8\n"
"v_mfma_f32_16x16x32_bf16 v[88:91], a[48:51], a[96:99], v[88:91]\n"
"s_sub_u32 s67, 1684480, s75\n"
"ds_read_b64_tr_b16 a[90:91], v15 offset:520\n"
"v_mfma_f32_16x16x32_bf16 v[88:91], a[52:55], a[100:103], v[88:91]\n"
"ds_read_b64_tr_b16 a[92:93], v15 offset:4104\n"
"v_mfma_f32_16x16x32_bf16 v[116:119], a[60:63], a[100:103], v[116:119]\n"
"ds_read_b64_tr_b16 a[94:95], v15 offset:4616\n"
"v_mfma_f32_16x16x32_bf16 v[116:119], a[56:59], a[96:99], v[116:119]\n"
"buffer_load_dwordx4 v11, s[52:55], s67 offen offset:3072  lds\n"
"v_mfma_f32_16x16x32_bf16 v[120:123], a[56:59], a[104:107], v[120:123]\n"
"v_mfma_f32_16x16x32_bf16 v[120:123], a[60:63], a[108:111], v[120:123]\n"
"s_add_u32 m0, s64, 0\n"
"v_mfma_f32_16x16x32_bf16 v[92:95], a[52:55], a[108:111], v[92:95]\n"
"v_mfma_f32_16x16x32_bf16 v[92:95], a[48:51], a[104:107], v[92:95]\n"
"s_sub_u32 s67, 1572992, s75\n"
"v_mfma_f32_16x16x32_bf16 v[96:99], a[48:51], a[112:115], v[96:99]\n"
"s_waitcnt lgkmcnt(0)\n"
"s_mov_b32 s99, 0\n"
"v_mfma_f32_16x16x32_bf16 v[96:99], a[52:55], a[116:119], v[96:99]\n"
"s_mov_b32 s98, 0\n"
"buffer_load_dwordx4 v11, s[52:55], s67 offen offset:0  lds\n"
"v_mfma_f32_16x16x32_bf16 v[124:127], a[60:63], a[116:119], v[124:127]\n"
"s_memtime s[76:77]\n"
"s_load_dword s98, s[96:97], 8320 glc\n"
"v_mfma_f32_16x16x32_bf16 v[124:127], a[56:59], a[112:115], v[124:127]\n"
"s_sub_u32 s67, 1588352, s75\n"
"v_mfma_f32_16x16x32_bf16 v[128:131], a[56:59], a[120:123], v[128:131]\n"
"v_mfma_f32_16x16x32_bf16 v[128:131], a[60:63], a[124:127], v[128:131]\n"
"buffer_load_dwordx4 v11, s[52:55], s67 offen offset:1024  lds\n"
"v_mfma_f32_16x16x32_bf16 v[100:103], a[52:55], a[124:127], v[100:103]\n"
"v_mfma_f32_16x16x32_bf16 v[100:103], a[48:51], a[120:123], v[100:103]\n"
"s_sub_u32 s67, 1603712, s75\n"
"v_mfma_f32_16x16x32_bf16 v[104:107], a[48:51], a[128:131], v[104:107]\n"
"v_mfma_f32_16x16x32_bf16 v[104:107], a[52:55], a[132:135], v[104:107]\n"
"v_mfma_f32_16x16x32_bf16 v[132:135], a[60:63], a[132:135], v[132:135]\n"
"buffer_load_dwordx4 v11, s[52:55], s67 offen offset:2048  lds\n"
"v_mfma_f32_16x16x32_bf16 v[132:135], a[56:59], a[128:131], v[132:135]\n"
"s_mov_b32 s30, s76\n"
"v_mfma_f32_16x16x32_bf16 v[136:139], a[56:59], a[136:139], v[136:139]\n"
"s_sub_u32 s67, 1619072, s75\n"
"v_mfma_f32_16x16x32_bf16 v[136:139], a[60:63], a[140:143], v[136:139]\n"
"buffer_load_dwordx4 v11, s[52:55], s67 offen offset:3072  lds\n"
"v_mfma_f32_16x16x32_bf16 v[108:111], a[52:55], a[140:143], v[108:111]\n"
"s_sub_u32 s99, s99, s98\n"
"s_sub_u32 s99, s99, s98\n"
"v_mfma_f32_16x16x32_bf16 v[108:111], a[48:51], a[136:139], v[108:111]\n"
"s_sub_u32 s99, s99, s98\n"
"s_add_u32 m0, s64, 4096\n"
"v_mfma_f32_16x16x32_bf16 v[112:115], a[48:51], a[144:147], v[112:115]\n"
"s_sub_u32 s99, s99, s98\n"
"v_mfma_f32_16x16x32_bf16 v[112:115], a[52:55], a[148:151], v[112:115]\n"
"s_sub_u32 s67, 1638528, s75\n"
"v_mfma_f32_16x16x32_bf16 v[140:143], a[60:63], a[148:151], v[140:143]\n"
"s_sub_u32 s99, s99, s98\n"
"s_sub_u32 s99, s99, s98\n"
"v_mfma_f32_16x16x32_bf16 v[140:143], a[56:59], a[144:147], v[140:143]\n"
"s_sub_u32 s99, s99, s98\n"
"buffer_load_dwordx4 v11, s[52:55], s67 offen offset:0  lds\n"
"v_mfma_f32_16x16x32_bf16 a[12:15], a[56:59], a[152:155], a[12:15]\n"
"s_sub_u32 s99, s99, s98\n"
"v_mfma_f32_16x16x32_bf16 a[12:15], a[60:63], a[156:159], a[12:15]\n"
"s_sub_u32 s67, 1653888, s75\n"
"v_mfma_f32_16x16x32_bf16 a[8:11], a[52:55], a[156:159], a[8:11]\n"
"s_sub_u32 s99, s99, s98\n"
"s_sub_u32 s99, s99, s98\n"
"v_mfma_f32_16x16x32_bf16 a[8:11], a[48:51], a[152:155], a[8:11]\n"
"s_sub_u32 s99, s99, s98\n"
"v_mfma_f32_16x16x32_bf16 v[144:147], a[64:67], a[96:99], v[144:147]\n"
"s_sub_u32 s99, s99, s98\n"
"v_mfma_f32_16x16x32_bf16 v[144:147], a[68:71], a[100:103], v[144:147]\n"
"buffer_load_dwordx4 v11, s[52:55], s67 offen offset:1024  lds\n"
"v_mfma_f32_16x16x32_bf16 v[172:175], a[76:79], a[100:103], v[172:175]\n"
"s_sub_u32 s99, s99, s98\n"
"s_sub_u32 s99, s99, s98\n"
"v_mfma_f32_16x16x32_bf16 v[172:175], a[72:75], a[96:99], v[172:175]\n"
"s_sub_u32 s99, s99, s98\n"
"s_sub_u32 s67, 1669248, s75\n"
"v_mfma_f32_16x16x32_bf16 v[176:179], a[72:75], a[104:107], v[176:179]\n"
"s_sub_u32 s99, s99, s98\n"
"v_mfma_f32_16x16x32_bf16 v[176:179], a[76:79], a[108:111], v[176:179]\n"
"buffer_load_dwordx4 v11, s[52:55], s67 offen offset:2048  lds\n"
"v_mfma_f32_16x16x32_bf16 v[148:151], a[68:71], a[108:111], v[148:151]\n"
"s_sub_u32 s99, s99, s98\n"
"s_sub_u32 s99, s99, s98\n"
"v_mfma_f32_16x16x32_bf16 v[148:151], a[64:67], a[104:107], v[148:151]\n"
"s_sub_u32 s99, s99, s98\n"
"s_sub_u32 s71, s64, s64\n"
"s_sub_u32 s67, 1684608, s75\n"
"v_mfma_f32_16x16x32_bf16 v[152:155], a[64:67], a[112:115], v[152:155]\n"
"s_sub_u32 s66, 81920, s73\n"
"s_sub_u32 s99, s99, s98\n"
"v_mfma_f32_16x16x32_bf16 v[152:155], a[68:71], a[116:119], v[152:155]\n"
"s_add_u32 s71, s71, 16384\n"
"buffer_load_dwordx4 v11, s[52:55], s67 offen offset:3072  lds\n"
"v_mfma_f32_16x16x32_bf16 v[180:183], a[76:79], a[116:119], v[180:183]\n"
"s_sub_u32 s99, s99, s98\n"
"v_add_u32 v12, s66, v7\n"
"s_sub_u32 s99, s99, s98\n"
"v_mfma_f32_16x16x32_bf16 v[180:183], a[72:75], a[112:115], v[180:183]\n"
"s_sub_u32 s99, s99, s98\n"
"s_cmp_lt_u32 %16, 2\n"
"s_cselect_b32 s67, 16384, 0\n"
"s_add_u32 s67, s67, s72\n"
"v_mfma_f32_16x16x32_bf16 v[184:187], a[72:75], a[120:123], v[184:187]\n"
"s_sub_u32 s99, s99, s98\n"
"v_add_u32 v13, s66, v8\n"
"s_add_u32 m0, s67, 139264\n"
"v_mfma_f32_16x16x32_bf16 v[184:187], a[76:79], a[124:127], v[184:187]\n"
"s_memtime s[76:77]\n"
"v_add_u32 v20, s71, v9\n"
"s_sub_u32 s67, 1792, s60\n"
"v_mfma_f32_16x16x32_bf16 v[156:159], a[68:71], a[124:127], v[156:159]\n"
"buffer_load_dwordx4 v6, s[48:51], s67 offen offset:0  lds\n"
"v_mfma_f32_16x16x32_bf16 v[156:159], a[64:67], a[120:123], v[156:159]\n"
"s_waitcnt lgkmcnt(0)\n"
"v_add_u32 v21, s71, v15\n"
"v_mfma_f32_16x16x32_bf16 v[160:163], a[64:67], a[128:131], v[160:163]\n"
"s_sub_u32 s30, s76, s30\n"
"s_sub_u32 s67, 17152, s60\n"
"v_mfma_f32_16x16x32_bf16 v[160:163], a[68:71], a[132:135], v[160:163]\n"
"s_cmp_lt_u32 s30, 620\n"
"s_cselect_b32 s99, s99, 0\n"
"v_mfma_f32_16x16x32_bf16 v[188:191], a[76:79], a[132:135], v[188:191]\n"
"s_max_u32 s32, s32, s99\n"
"buffer_load_dwordx4 v6, s[48:51], s67 offen offset:1024  lds\n"
"v_mfma_f32_16x16x32_bf16 v[188:191], a[72:75], a[128:131], v[188:191]\n"
"s_waitcnt vmcnt(18)\n"
"ds_read_b64_tr_b16 a[176:177], v20 offset:0\n"
"v_mfma_f32_16x16x32_bf16 v[192:195], a[72:75], a[136:139], v[192:195]\n"
"s_sub_u32 s67, 32512, s60\n"
"ds_read_b64_tr_b16 a[178:179], v20 offset:512\n"
"v_mfma_f32_16x16x32_bf16 v[192:195], a[76:79], a[140:143], v[192:195]\n"
"ds_read_b64_tr_b16 a[180:181], v20 offset:8\n"
"v_mfma_f32_16x16x32_bf16 v[164:167], a[68:71], a[140:143], v[164:167]\n"
"ds_read_b64_tr_b16 a[182:183], v20 offset:520\n"
"v_mfma_f32_16x16x32_bf16 v[164:167], a[64:67], a[136:139], v[164:167]\n"
"buffer_load_dwordx4 v6, s[48:51], s67 offen offset:2048  lds\n"
"v_mfma_f32_16x16x32_bf16 v[168:171], a[64:67], a[144:147], v[168:171]\n"
"ds_read_b64_tr_b16 a[184:185], v21 offset:0\n"
"v_mfma_f32_16x16x32_bf16 v[168:171], a[68:71], a[148:151], v[168:171]\n"
"s_sub_u32 s67, 47872, s60\n"
"ds_read_b64_tr_b16 a[186:187], v21 offset:512\n"
"v_mfma_f32_16x16x32_bf16 v[196:199], a[76:79], a[148:151], v[196:199]\n"
"ds_read_b64_tr_b16 a[188:189], v21 offset:8\n"
"v_mfma_f32_16x16x32_bf16 v[196:199], a[72:75], a[144:147], v[196:199]\n"
"buffer_load_dwordx4 v6, s[48:51], s67 offen offset:3072  lds\n"
"v_mfma_f32_16x16x32_bf16 a[20:23], a[72:75], a[152:155], a[20:23]\n"
"ds_read_b64_tr_b16 a[190:191], v21 offset:520\n"
"v_mfma_f32_16x16x32_bf16 a[20:23], a[76:79], a[156:159], a[20:23]\n"
"s_add_u32 m0, s64, 20352\n"
"v_mfma_f32_16x16x32_bf16 a[16:19], a[68:71], a[156:159], a[16:19]\n"
"v_mfma_f32_16x16x32_bf16 a[16:19], a[64:67], a[152:155], a[16:19]\n"
"s_barrier\n"
"s_waitcnt vmcnt(12)\n"
"ds_read_b64_tr_b16 a[32:33], v9 offset:8192\n"
"v_mfma_f32_16x16x32_bf16 v[200:203], a[80:83], a[96:99], v[200:203]\n"
"ds_read_b64_tr_b16 a[34:35], v9 offset:8704\n"
"buffer_load_dwordx4 v6, s[40:43], s88 offen offset:128  lds\n"
"v_mfma_f32_16x16x32_bf16 v[200:203], a[84:87], a[100:103], v[200:203]\n"
"ds_read_b64_tr_b16 a[36:37], v9 offset:12288\n"
"v_mfma_f32_16x16x32_bf16 v[228:231], a[92:95], a[100:103], v[228:231]\n"
"s_waitcnt lgkmcnt(3)\n"
"ds_read_b128 a[100:103], v13 offset:32768\n"
"v_mfma_f32_16x16x32_bf16 v[228:231], a[88:91], a[96:99], v[228:231]\n"
"ds_read_b128 a[96:99], v12 offset:32768\n"
"buffer_load_dwordx4 v6, s[40:43], s89 offen offset:1152  lds\n"
"v_mfma_f32_16x16x32_bf16 v[232:235], a[88:91], a[104:107], v[232:235]\n"
"ds_read_b64_tr_b16 a[38:39], v9 offset:12800\n"
"v_mfma_f32_16x16x32_bf16 v[232:235], a[92:95], a[108:111], v[232:235]\n"
"ds_read_b64_tr_b16 a[40:41], v9 offset:8200\n"
"v_mfma_f32_16x16x32_bf16 v[204:207], a[84:87], a[108:111], v[204:207]\n"
"ds_read_b128 a[108:111], v13 offset:34816\n"
"v_mfma_f32_16x16x32_bf16 v[204:207], a[80:83], a[104:107], v[204:207]\n"
"ds_read_b128 a[104:107], v12 offset:34816\n"
"v_mfma_f32_16x16x32_bf16 v[208:211], a[80:83], a[112:115], v[208:211]\n"
"ds_read_b64_tr_b16 a[42:43], v9 offset:8712\n"
"buffer_load_dwordx4 v6, s[40:43], s90 offen offset:2176  lds\n"
"v_mfma_f32_16x16x32_bf16 v[208:211], a[84:87], a[116:119], v[208:211]\n"
"ds_read_b64_tr_b16 a[44:45], v9 offset:12296\n"
"v_mfma_f32_16x16x32_bf16 v[236:239], a[92:95], a[116:119], v[236:239]\n"
"ds_read_b128 a[116:119], v13 offset:36864\n"
"v_mfma_f32_16x16x32_bf16 v[236:239], a[88:91], a[112:115], v[236:239]\n"
"ds_read_b128 a[112:115], v12 offset:36864\n"
"v_mfma_f32_16x16x32_bf16 v[240:243], a[88:91], a[120:123], v[240:243]\n"
"ds_read_b64_tr_b16 a[46:47], v9 offset:12808\n"
"v_mfma_f32_16x16x32_bf16 v[240:243], a[92:95], a[124:127], v[240:243]\n"
"ds_read_b64_tr_b16 a[48:49], v15 offset:8192\n"
"buffer_load_dwordx4 v6, s[40:43], s91 offen offset:3200  lds\n"
"v_mfma_f32_16x16x32_bf16 v[212:215], a[84:87], a[124:127], v[212:215]\n"
"ds_read_b128 a[124:127], v13 offset:38912\n"
"v_mfma_f32_16x16x32_bf16 v[212:215], a[80:83], a[120:123], v[212:215]\n"
"ds_read_b128 a[120:123], v12 offset:38912\n"
"v_mfma_f32_16x16x32_bf16 v[216:219], a[80:83], a[128:131], v[216:219]\n"
"ds_read_b64_tr_b16 a[50:51], v15 offset:8704\n"
"v_mfma_f32_16x16x32_bf16 v[216:219], a[84:87], a[132:135], v[216:219]\n"
"buffer_store_dwordx4 a[164:167], v0, s[36:39], %23 offen offset:2048 sc1\n"
"ds_read_b64_tr_b16 a[52:53], v15 offset:12288\n"
"v_mfma_f32_16x16x32_bf16 v[244:247], a[92:95], a[132:135], v[244:247]\n"
"ds_read_b128 a[132:135], v13 offset:24576\n"
"v_mfma_f32_16x16x32_bf16 v[244:247], a[88:91], a[128:131], v[244:247]\n"
"ds_read_b128 a[128:131], v12 offset:24576\n"
"v_mfma_f32_16x16x32_bf16 v[248:251], a[88:91], a[136:139], v[248:251]\n"
"ds_read_b64_tr_b16 a[54:55], v15 offset:12800\n"
"v_mfma_f32_16x16x32_bf16 v[248:251], a[92:95], a[140:143], v[248:251]\n"
"s_add_u32 m0, s64, 8192\n"
"ds_read_b64_tr_b16 a[56:57], v15 offset:8200\n"
"v_mfma_f32_16x16x32_bf16 v[220:223], a[84:87], a[140:143], v[220:223]\n"
"ds_read_b128 a[140:143], v13 offset:26624\n"
"v_mfma_f32_16x16x32_bf16 v[220:223], a[80:83], a[136:139], v[220:223]\n"
"ds_read_b128 a[136:139], v12 offset:26624\n"
"v_mfma_f32_16x16x32_bf16 v[224:227], a[80:83], a[144:147], v[224:227]\n"
"ds_read_b64_tr_b16 a[58:59], v15 offset:8712\n"
"v_mfma_f32_16x16x32_bf16 v[224:227], a[84:87], a[148:151], v[224:227]\n"
"ds_read_b64_tr_b16 a[60:61], v15 offset:12296\n"
"v_mfma_f32_16x16x32_bf16 v[252:255], a[92:95], a[148:151], v[252:255]\n"
"ds_read_b128 a[148:151], v13 offset:28672\n"
"v_mfma_f32_16x16x32_bf16 v[252:255], a[88:91], a[144:147], v[252:255]\n"
"ds_read_b128 a[144:147], v12 offset:28672\n"
"s_sub_u32 s67, 1835008, s75\n"
"ds_read_b64_tr_b16 a[62:63], v15 offset:12808\n"
"v_mfma_f32_16x16x32_bf16 a[28:31], a[88:91], a[152:155], a[28:31]\n"
"v_mfma_f32_16x16x32_bf16 a[28:31], a[92:95], a[156:159], a[28:31]\n"
"v_mfma_f32_16x16x32_bf16 a[24:27], a[84:87], a[156:159], a[24:27]\n"
"ds_read_b128 a[156:159], v13 offset:30720\n"
"v_mfma_f32_16x16x32_bf16 a[24:27], a[80:83], a[152:155], a[24:27]\n"
"s_waitcnt lgkmcnt(15)\n"
"ds_read_b128 a[152:155], v12 offset:30720\n"
"v_mfma_f32_16x16x32_bf16 v[32:35], a[32:35], a[96:99], v[32:35]\n"
"v_mfma_f32_16x16x32_bf16 v[32:35], a[36:39], a[100:103], v[32:35]\n"
"s_waitcnt lgkmcnt(2)\n"
"v_mfma_f32_16x16x32_bf16 v[60:63], a[44:47], a[100:103], v[60:63]\n"
"buffer_load_dwordx4 v11, s[52:55], s67 offen offset:0  lds\n"
"v_mfma_f32_16x16x32_bf16 v[60:63], a[40:43], a[96:99], v[60:63]\n"
"v_mfma_f32_16x16x32_bf16 v[64:67], a[40:43], a[104:107], v[64:67]\n"
"s_sub_u32 s67, 1850368, s75\n"
"v_mfma_f32_16x16x32_bf16 v[64:67], a[44:47], a[108:111], v[64:67]\n"
"v_mfma_f32_16x16x32_bf16 v[36:39], a[36:39], a[108:111], v[36:39]\n"
"buffer_load_dwordx4 v11, s[52:55], s67 offen offset:1024  lds\n"
"v_mfma_f32_16x16x32_bf16 v[36:39], a[32:35], a[104:107], v[36:39]\n"
"v_mfma_f32_16x16x32_bf16 v[40:43], a[32:35], a[112:115], v[40:43]\n"
"s_sub_u32 s67, 1865728, s75\n"
"v_mfma_f32_16x16x32_bf16 v[40:43], a[36:39], a[116:119], v[40:43]\n"
"v_mfma_f32_16x16x32_bf16 v[68:71], a[44:47], a[116:119], v[68:71]\n"
"v_mfma_f32_16x16x32_bf16 v[68:71], a[40:43], a[112:115], v[68:71]\n"
"buffer_load_dwordx4 v11, s[52:55], s67 offen offset:2048  lds\n"
"v_mfma_f32_16x16x32_bf16 v[72:75], a[40:43], a[120:123], v[72:75]\n"
"v_mfma_f32_16x16x32_bf16 v[72:75], a[44:47], a[124:127], v[72:75]\n"
"s_sub_u32 s67, 1881088, s75\n"
"v_mfma_f32_16x16x32_bf16 v[44:47], a[36:39], a[124:127], v[44:47]\n"
"v_mfma_f32_16x16x32_bf16 v[44:47], a[32:35], a[120:123], v[44:47]\n"
"v_mfma_f32_16x16x32_bf16 v[48:51], a[32:35], a[128:131], v[48:51]\n"
"buffer_load_dwordx4 v11, s[52:55], s67 offen offset:3072  lds\n"
"v_mfma_f32_16x16x32_bf16 v[48:51], a[36:39], a[132:135], v[48:51]\n"
"s_add_u32 m0, s64, 12288\n"
"v_mfma_f32_16x16x32_bf16 v[76:79], a[44:47], a[132:135], v[76:79]\n"
"s_waitcnt vmcnt(13)\n"
"ds_read_b64_tr_b16 a[64:65], v9 offset:0\n"
"v_mfma_f32_16x16x32_bf16 v[76:79], a[40:43], a[128:131], v[76:79]\n"
"s_sub_u32 s67, 1900544, s75\n"
"ds_read_b64_tr_b16 a[66:67], v9 offset:512\n"
"v_mfma_f32_16x16x32_bf16 v[80:83], a[40:43], a[136:139], v[80:83]\n"
"ds_read_b64_tr_b16 a[68:69], v9 offset:4096\n"
"v_mfma_f32_16x16x32_bf16 v[80:83], a[44:47], a[140:143], v[80:83]\n"
"ds_read_b64_tr_b16 a[70:71], v9 offset:4608\n"
"buffer_load_dwordx4 v11, s[52:55], s67 offen offset:0  lds\n"
"v_mfma_f32_16x16x32_bf16 v[52:55], a[36:39], a[140:143], v[52:55]\n"
"s_sub_u32 s67, 1915904, s75\n"
"ds_read_b64_tr_b16 a[72:73], v9 offset:8\n"
"v_mfma_f32_16x16x32_bf16 v[52:55], a[32:35], a[136:139], v[52:55]\n"
"s_waitcnt lgkmcnt(5)\n"
"ds_read_b64_tr_b16 a[74:75], v9 offset:520\n"
"v_mfma_f32_16x16x32_bf16 v[56:59], a[32:35], a[144:147], v[56:59]\n"
"ds_read_b64_tr_b16 a[76:77], v9 offset:4104\n"
"v_mfma_f32_16x16x32_bf16 v[56:59], a[36:39], a[148:151], v[56:59]\n"
"ds_read_b64_tr_b16 a[78:79], v9 offset:4616\n"
"v_mfma_f32_16x16x32_bf16 v[84:87], a[44:47], a[148:151], v[84:87]\n"
"buffer_load_dwordx4 v11, s[52:55], s67 offen offset:1024  lds\n"
"v_mfma_f32_16x16x32_bf16 v[84:87], a[40:43], a[144:147], v[84:87]\n"
"ds_read_b64_tr_b16 a[80:81], v15 offset:0\n"
"v_mfma_f32_16x16x32_bf16 a[4:7], a[40:43], a[152:155], a[4:7]\n"
"s_sub_u32 s67, 1931264, s75\n"
"ds_read_b64_tr_b16 a[82:83], v15 offset:512\n"
"v_mfma_f32_16x16x32_bf16 a[4:7], a[44:47], a[156:159], a[4:7]\n"
"ds_read_b64_tr_b16 a[84:85], v15 offset:4096\n"
"v_mfma_f32_16x16x32_bf16 a[0:3], a[36:39], a[156:159], a[0:3]\n"
"ds_read_b64_tr_b16 a[86:87], v15 offset:4608\n"
"buffer_load_dwordx4 v11, s[52:55], s67 offen offset:2048  lds\n"
"v_mfma_f32_16x16x32_bf16 a[0:3], a[32:35], a[152:155], a[0:3]\n"
"ds_read_b64_tr_b16 a[88:89], v15 offset:8\n"
"v_mfma_f32_16x16x32_bf16 v[88:91], a[48:51], a[96:99], v[88:91]\n"
"s_sub_u32 s67, 1946624, s75\n"
"ds_read_b64_tr_b16 a[90:91], v15 offset:520\n"
"v_mfma_f32_16x16x32_bf16 v[88:91], a[52:55], a[100:103], v[88:91]\n"
"ds_read_b64_tr_b16 a[92:93], v15 offset:4104\n"
"v_mfma_f32_16x16x32_bf16 v[116:119], a[60:63], a[100:103], v[116:119]\n"
"ds_read_b64_tr_b16 a[94:95], v15 offset:4616\n"
"v_mfma_f32_16x16x32_bf16 v[116:119], a[56:59], a[96:99], v[116:119]\n"
"buffer_load_dwordx4 v11, s[52:55], s67 offen offset:3072  lds\n"
"v_mfma_f32_16x16x32_bf16 v[120:123], a[56:59], a[104:107], v[120:123]\n"
"v_mfma_f32_16x16x32_bf16 v[120:123], a[60:63], a[108:111], v[120:123]\n"
"s_add_u32 m0, s64, 16256\n"
"v_mfma_f32_16x16x32_bf16 v[92:95], a[52:55], a[108:111], v[92:95]\n"
"v_mfma_f32_16x16x32_bf16 v[92:95], a[48:51], a[104:107], v[92:95]\n"
"buffer_load_dwordx4 v6, s[40:43], s92 offen offset:128  lds\n"
"v_mfma_f32_16x16x32_bf16 v[96:99], a[48:51], a[112:115], v[96:99]\n"
"s_waitcnt lgkmcnt(0)\n"
"s_mov_b32 s99, 0\n"
"v_mfma_f32_16x16x32_bf16 v[96:99], a[52:55], a[116:119], v[96:99]\n"
"s_mov_b32 s98, 0\n"
"v_mfma_f32_16x16x32_bf16 v[124:127], a[60:63], a[116:119], v[124:127]\n"
"s_memtime s[76:77]\n"
"s_load_dword s98, s[96:97], 128 glc\n"
"v_mfma_f32_16x16x32_bf16 v[124:127], a[56:59], a[112:115], v[124:127]\n"
"buffer_load_dwordx4 v6, s[40:43], s93 offen offset:1152  lds\n"
"v_mfma_f32_16x16x32_bf16 v[128:131], a[56:59], a[120:123], v[128:131]\n"
"v_mfma_f32_16x16x32_bf16 v[128:131], a[60:63], a[124:127], v[128:131]\n"
"v_mfma_f32_16x16x32_bf16 v[100:103], a[52:55], a[124:127], v[100:103]\n"
"v_mfma_f32_16x16x32_bf16 v[100:103], a[48:51], a[120:123], v[100:103]\n"
"buffer_load_dwordx4 v6, s[40:43], s94 offen offset:2176  lds\n"
"v_mfma_f32_16x16x32_bf16 v[104:107], a[48:51], a[128:131], v[104:107]\n"
"v_mfma_f32_16x16x32_bf16 v[104:107], a[52:55], a[132:135], v[104:107]\n"
"v_mfma_f32_16x16x32_bf16 v[132:135], a[60:63], a[132:135], v[132:135]\n"
"v_mfma_f32_16x16x32_bf16 v[132:135], a[56:59], a[128:131], v[132:135]\n"
"s_mov_b32 s30, s76\n"
"v_mfma_f32_16x16x32_bf16 v[136:139], a[56:59], a[136:139], v[136:139]\n"
"buffer_load_dwordx4 v6, s[40:43], s95 offen offset:3200  lds\n"
"v_mfma_f32_16x16x32_bf16 v[136:139], a[60:63], a[140:143], v[136:139]\n"
"s_add_u32 m0, s64, 0\n"
"v_mfma_f32_16x16x32_bf16 v[108:111], a[52:55], a[140:143], v[108:111]\n"
"s_sub_u32 s99, s99, s98\n"
"s_sub_u32 s99, s99, s98\n"
"s_andn2_b32 s65, %25, 4096\n"
"v_mfma_f32_16x16x32_bf16 v[108:111], a[48:51], a[136:139], v[108:111]\n"
"s_sub_u32 s99, s99, s98\n"
"v_mfma_f32_16x16x32_bf16 v[112:115], a[48:51], a[144:147], v[112:115]\n"
"s_sub_u32 s99, s99, s98\n"
"s_sub_u32 s67, 1835136, s75\n"
"v_mfma_f32_16x16x32_bf16 v[112:115], a[52:55], a[148:151], v[112:115]\n"
"buffer_load_dwordx4 v11, s[52:55], s67 offen offset:0  lds\n"
"v_mfma_f32_16x16x32_bf16 v[140:143], a[60:63], a[148:151], v[140:143]\n"
"s_sub_u32 s99, s99, s98\n"
"s_sub_u32 s99, s99, s98\n"
"buffer_load_dwordx4 v[24:27], v0, s[56:59], s65 offen sc1\n"
"v_mfma_f32_16x16x32_bf16 v[140:143], a[56:59], a[144:147], v[140:143]\n"
"s_sub_u32 s99, s99, s98\n"
"s_sub_u32 s67, 1850496, s75\n"
"v_mfma_f32_16x16x32_bf16 a[12:15], a[56:59], a[152:155], a[12:15]\n"
"s_sub_u32 s99, s99, s98\n"
"v_mfma_f32_16x16x32_bf16 a[12:15], a[60:63], a[156:159], a[12:15]\n"
"buffer_load_dwordx4 v11, s[52:55], s67 offen offset:1024  lds\n"
"v_mfma_f32_16x16x32_bf16 a[8:11], a[52:55], a[156:159], a[8:11]\n"
"s_sub_u32 s99, s99, s98\n"
"s_sub_u32 s99, s99, s98\n"
"v_mfma_f32_16x16x32_bf16 a[8:11], a[48:51], a[152:155], a[8:11]\n"
"s_sub_u32 s99, s99, s98\n"
"s_sub_u32 s67, 1865856, s75\n"
"v_mfma_f32_16x16x32_bf16 v[144:147], a[64:67], a[96:99], v[144:147]\n"
"s_sub_u32 s99, s99, s98\n"
"v_mfma_f32_16x16x32_bf16 v[144:147], a[68:71], a[100:103], v[144:147]\n"
"buffer_load_dwordx4 v11, s[52:55], s67 offen offset:2048  lds\n"
"v_mfma_f32_16x16x32_bf16 v[172:175], a[76:79], a[100:103], v[172:175]\n"
"s_sub_u32 s99, s99, s98\n"
"s_sub_u32 s99, s99, s98\n"
"v_mfma_f32_16x16x32_bf16 v[172:175], a[72:75], a[96:99], v[172:175]\n"
"s_sub_u32 s99, s99, s98\n"
"s_sub_u32 s67, 1881216, s75\n"
"v_mfma_f32_16x16x32_bf16 v[176:179], a[72:75], a[104:107], v[176:179]\n"
"s_sub_u32 s99, s99, s98\n"
"v_mfma_f32_16x16x32_bf16 v[176:179], a[76:79], a[108:111], v[176:179]\n"
"v_mfma_f32_16x16x32_bf16 v[148:151], a[68:71], a[108:111], v[148:151]\n"
"s_sub_u32 s99, s99, s98\n"
"s_sub_u32 s99, s99, s98\n"
"v_mfma_f32_16x16x32_bf16 v[148:151], a[64:67], a[104:107], v[148:151]\n"
"s_sub_u32 s99, s99, s98\n"
"s_sub_u32 s71, s64, s64\n"
"buffer_load_dwordx4 v11, s[52:55], s67 offen offset:3072  lds\n"
"v_mfma_f32_16x16x32_bf16 v[152:155], a[64:67], a[112:115], v[152:155]\n"
"s_sub_u32 s66, 122880, s73\n"
"s_sub_u32 s99, s99, s98\n"
"s_add_u32 m0, s64, 4096\n"
"v_mfma_f32_16x16x32_bf16 v[152:155], a[68:71], a[116:119], v[152:155]\n"
"s_add_u32 s71, s71, 20480\n"
"s_sub_u32 s67, 1900672, s75\n"
"v_mfma_f32_16x16x32_bf16 v[180:183], a[76:79], a[116:119], v[180:183]\n"
"s_sub_u32 s99, s99, s98\n"
"v_add_u32 v12, s66, v7\n"
"s_sub_u32 s99, s99, s98\n"
"v_mfma_f32_16x16x32_bf16 v[180:183], a[72:75], a[112:115], v[180:183]\n"
"s_sub_u32 s99, s99, s98\n"
"buffer_load_dwordx4 v11, s[52:55], s67 offen offset:0  lds\n"
"v_mfma_f32_16x16x32_bf16 v[184:187], a[72:75], a[120:123], v[184:187]\n"
"s_sub_u32 s99, s99, s98\n"
"v_add_u32 v13, s66, v8\n"
"v_mfma_f32_16x16x32_bf16 v[184:187], a[76:79], a[124:127], v[184:187]\n"
"s_memtime s[76:77]\n"
"v_add_u32 v20, s71, v7\n"
"v_mfma_f32_16x16x32_bf16 v[156:159], a[68:71], a[124:127], v[156:159]\n"
"s_sub_u32 s67, 1916032, s75\n"
"v_mfma_f32_16x16x32_bf16 v[156:159], a[64:67], a[120:123], v[156:159]\n"
"s_waitcnt lgkmcnt(0)\n"
"v_add_u32 v21, s71, v8\n"
"v_mfma_f32_16x16x32_bf16 v[160:163], a[64:67], a[128:131], v[160:163]\n"
"s_sub_u32 s30, s76, s30\n"
"buffer_load_dwordx4 v11, s[52:55], s67 offen offset:1024  lds\n"
"v_mfma_f32_16x16x32_bf16 v[160:163], a[68:71], a[132:135], v[160:163]\n"
"s_cmp_lt_u32 s30, 620\n"
"s_cselect_b32 s99, s99, 0\n"
"v_mfma_f32_16x16x32_bf16 v[188:191], a[76:79], a[132:135], v[188:191]\n"
"s_max_u32 s31, s31, s99\n"
"s_sub_u32 s67, 1931392, s75\n"
"v_mfma_f32_16x16x32_bf16 v[188:191], a[72:75], a[128:131], v[188:191]\n"
"s_waitcnt vmcnt(20)\n"
"ds_read_b128 a[224:227], v20 offset:0\n"
"v_mfma_f32_16x16x32_bf16 v[192:195], a[72:75], a[136:139], v[192:195]\n"
"ds_read_b128 a[228:231], v21 offset:0\n"
"v_mfma_f32_16x16x32_bf16 v[192:195], a[76:79], a[140:143], v[192:195]\n"
"ds_read_b128 a[232:235], v20 offset:2048\n"
"v_mfma_f32_16x16x32_bf16 v[164:167], a[68:71], a[140:143], v[164:167]\n"
"buffer_load_dwordx4 v11, s[52:55], s67 offen offset:2048  lds\n"
"v_mfma_f32_16x16x32_bf16 v[164:167], a[64:67], a[136:139], v[164:167]\n"
"ds_read_b128 a[236:239], v21 offset:2048\n"
"v_mfma_f32_16x16x32_bf16 v[168:171], a[64:67], a[144:147], v[168:171]\n"
"s_sub_u32 s67, 1946752, s75\n"
"v_mfma_f32_16x16x32_bf16 v[168:171], a[68:71], a[148:151], v[168:171]\n"
"v_mfma_f32_16x16x32_bf16 v[196:199], a[76:79], a[148:151], v[196:199]\n"
"buffer_load_dwordx4 v11, s[52:55], s67 offen offset:3072  lds\n"
"v_mfma_f32_16x16x32_bf16 v[196:199], a[72:75], a[144:147], v[196:199]\n"
"v_mfma_f32_16x16x32_bf16 a[20:23], a[72:75], a[152:155], a[20:23]\n"
"v_mfma_f32_16x16x32_bf16 a[20:23], a[76:79], a[156:159], a[20:23]\n"
"s_waitcnt lgkmcnt(0)\n"
"v_mfma_f32_16x16x32_bf16 a[16:19], a[68:71], a[156:159], a[16:19]\n"
"v_mfma_f32_16x16x32_bf16 a[16:19], a[64:67], a[152:155], a[16:19]\n"
"s_barrier\n"
"s_waitcnt vmcnt(13)\n"
"ds_read_b64_tr_b16 a[32:33], v9 offset:8192\n"
"v_mfma_f32_16x16x32_bf16 v[200:203], a[80:83], a[96:99], v[200:203]\n"
"buffer_store_dwordx4 a[168:171], v1, s[36:39], %23 offen offset:0 sc1\n"
"ds_read_b64_tr_b16 a[34:35], v9 offset:8704\n"
"v_mfma_f32_16x16x32_bf16 v[200:203], a[84:87], a[100:103], v[200:203]\n"
"ds_read_b64_tr_b16 a[36:37], v9 offset:12288\n"
"v_mfma_f32_16x16x32_bf16 v[228:231], a[92:95], a[100:103], v[228:231]\n"
"ds_read_b128 a[100:103], v13 offset:32768\n"
"v_mfma_f32_16x16x32_bf16 v[228:231], a[88:91], a[96:99], v[228:231]\n"
"ds_read_b128 a[96:99], v12 offset:32768\n"
"v_mfma_f32_16x16x32_bf16 v[232:235], a[88:91], a[104:107], v[232:235]\n"
"ds_read_b64_tr_b16 a[38:39], v9 offset:12800\n"
"v_mfma_f32_16x16x32_bf16 v[232:235], a[92:95], a[108:111], v[232:235]\n"
"ds_read_b64_tr_b16 a[40:41], v9 offset:8200\n"
"v_mfma_f32_16x16x32_bf16 v[204:207], a[84:87], a[108:111], v[204:207]\n"
"ds_read_b128 a[108:111], v13 offset:34816\n"
"v_mfma_f32_16x16x32_bf16 v[204:207], a[80:83], a[104:107], v[204:207]\n"
"ds_read_b128 a[104:107], v12 offset:34816\n"
"v_mfma_f32_16x16x32_bf16 v[208:211], a[80:83], a[112:115], v[208:211]\n"
"ds_read_b64_tr_b16 a[42:43], v9 offset:8712\n"
"v_mfma_f32_16x16x32_bf16 v[208:211], a[84:87], a[116:119], v[208:211]\n"
"ds_read_b64_tr_b16 a[44:45], v9 offset:12296\n"
"v_mfma_f32_16x16x32_bf16 v[236:239], a[92:95], a[116:119], v[236:239]\n"
"ds_read_b128 a[116:119], v13 offset:36864\n"
"v_mfma_f32_16x16x32_bf16 v[236:239], a[88:91], a[112:115], v[236:239]\n"
"buffer_store_dwordx4 a[172:175], v1, s[36:39], %23 offen offset:2048 sc1\n"
"ds_read_b128 a[112:115], v12 offset:36864\n"
"s_getpc_b64 s[70:71]\n"
"L_c11_:\n"
"s_add_u32 s70, s70, L_P6-L_c11_\n"
"s_addc_u32 s71, s71, 0\n"
"v_mfma_f32_16x16x32_bf16 v[240:243], a[88:91], a[120:123], v[240:243]\n"
"s_lshr_b32 s66, %26, 3\n"
"ds_read_b64_tr_b16 a[46:47], v9 offset:12808\n"
"v_mfma_f32_16x16x32_bf16 v[240:243], a[92:95], a[124:127], v[240:243]\n"
"s_lshl2_add_u32 s66, s66, %16\n"
"ds_read_b64_tr_b16 a[48:49], v15 offset:8192\n"
"v_mfma_f32_16x16x32_bf16 v[212:215], a[84:87], a[124:127], v[212:215]\n"
"s_mul_i32 s66, s66, 256\n"
"ds_read_b128 a[124:127], v13 offset:38912\n"
"v_mfma_f32_16x16x32_bf16 v[212:215], a[80:83], a[120:123], v[212:215]\n"
"s_add_u32 s74, %23, 524288\n"
"ds_read_b128 a[120:123], v12 offset:38912\n"
"v_mfma_f32_16x16x32_bf16 v[216:219], a[80:83], a[128:131], v[216:219]\n"
"s_min_u32 s66, s66, L_headF-L_P6-256\n"
"ds_read_b64_tr_b16 a[50:51], v15 offset:8704\n"
"v_mfma_f32_16x16x32_bf16 v[216:219], a[84:87], a[132:135], v[216:219]\n"
"ds_read_b64_tr_b16 a[52:53], v15 offset:12288\n"
"v_mfma_f32_16x16x32_bf16 v[244:247], a[92:95], a[132:135], v[244:247]\n"
"ds_read_b128 a[132:135], v13 offset:24576\n"
"v_min_u32 v22, 16, v0\n"
"v_mfma_f32_16x16x32_bf16 v[244:247], a[88:91], a[128:131], v[244:247]\n"
"ds_read_b128 a[128:131], v12 offset:24576\n"
"v_mfma_f32_16x16x32_bf16 v[248:251], a[88:91], a[136:139], v[248:251]\n"
"ds_read_b64_tr_b16 a[54:55], v15 offset:12800\n"
"v_mfma_f32_16x16x32_bf16 v[248:251], a[92:95], a[140:143], v[248:251]\n"
"ds_read_b64_tr_b16 a[56:57], v15 offset:8200\n"
"v_lshl_add_u32 v22, v22, 3, s66\n"
"v_mfma_f32_16x16x32_bf16 v[220:223], a[84:87], a[140:143], v[220:223]\n"
"ds_read_b128 a[140:143], v13 offset:26624\n"
"v_mfma_f32_16x16x32_bf16 v[220:223], a[80:83], a[136:139], v[220:223]\n"
"ds_read_b128 a[136:139], v12 offset:26624\n"
"v_mfma_f32_16x16x32_bf16 v[224:227], a[80:83], a[144:147], v[224:227]\n"
"buffer_store_dwordx4 a[176:179], v0, s[36:39], s74 offen offset:0 sc1\n"
"ds_read_b64_tr_b16 a[58:59], v15 offset:8712\n"
"global_load_dword v23, v22, s[70:71]\n"
"v_mfma_f32_16x16x32_bf16 v[224:227], a[84:87], a[148:151], v[224:227]\n"
"ds_read_b64_tr_b16 a[60:61], v15 offset:12296\n"
"v_mfma_f32_16x16x32_bf16 v[252:255], a[92:95], a[148:151], v[252:255]\n"
"ds_read_b128 a[148:151], v13 offset:28672\n"
"v_mfma_f32_16x16x32_bf16 v[252:255], a[88:91], a[144:147], v[252:255]\n"
"ds_read_b128 a[144:147], v12 offset:28672\n"
"s_sub_u32 s71, s64, s64\n"
"ds_read_b64_tr_b16 a[62:63], v15 offset:12808\n"
"v_mfma_f32_16x16x32_bf16 a[28:31], a[88:91], a[152:155], a[28:31]\n"
"v_mfma_f32_16x16x32_bf16 a[28:31], a[92:95], a[156:159], a[28:31]\n"
"v_mfma_f32_16x16x32_bf16 a[24:27], a[84:87], a[156:159], a[24:27]\n"
"s_add_u32 s71, s71, 16384\n"
"ds_read_b128 a[156:159], v13 offset:30720\n"
"v_mfma_f32_16x16x32_bf16 a[24:27], a[80:83], a[152:155], a[24:27]\n"
"s_waitcnt lgkmcnt(15)\n"
"ds_read_b128 a[152:155], v12 offset:30720\n"
"v_mfma_f32_16x16x32_bf16 v[32:35], a[32:35], a[96:99], v[32:35]\n"
"v_add_u32 v20, s71, v7\n"
"v_mfma_f32_16x16x32_bf16 v[32:35], a[36:39], a[100:103], v[32:35]\n"
"s_waitcnt lgkmcnt(2)\n"
"v_mfma_f32_16x16x32_bf16 v[60:63], a[44:47], a[100:103], v[60:63]\n"
"v_mfma_f32_16x16x32_bf16 v[60:63], a[40:43], a[96:99], v[60:63]\n"
"v_add_u32 v21, s71, v8\n"
"v_mfma_f32_16x16x32_bf16 v[64:67], a[40:43], a[104:107], v[64:67]\n"
"v_mfma_f32_16x16x32_bf16 v[64:67], a[44:47], a[108:111], v[64:67]\n"
"buffer_store_dwordx4 a[180:183], v0, s[36:39], s74 offen offset:2048 sc1\n"
"v_mfma_f32_16x16x32_bf16 v[36:39], a[36:39], a[108:111], v[36:39]\n"
"s_waitcnt vmcnt(14)\n"
"ds_read_b128 a[240:243], v20 offset:0\n"
"v_mfma_f32_16x16x32_bf16 v[36:39], a[32:35], a[104:107], v[36:39]\n"
"v_mfma_f32_16x16x32_bf16 v[40:43], a[32:35], a[112:115], v[40:43]\n"
"ds_read_b128 a[244:247], v21 offset:0\n"
"v_mfma_f32_16x16x32_bf16 v[40:43], a[36:39], a[116:119], v[40:43]\n"
"ds_read_b128 a[248:251], v20 offset:2048\n"
"v_mfma_f32_16x16x32_bf16 v[68:71], a[44:47], a[116:119], v[68:71]\n"
"s_add_u32 s70, s78, 1\n"
"v_mfma_f32_16x16x32_bf16 v[68:71], a[40:43], a[112:115], v[68:71]\n"
"ds_read_b128 a[252:255], v21 offset:2048\n"
"v_mfma_f32_16x16x32_bf16 v[72:75], a[40:43], a[120:123], v[72:75]\n"
"v_mfma_f32_16x16x32_bf16 v[72:75], a[44:47], a[124:127], v[72:75]\n"
"v_mfma_f32_16x16x32_bf16 v[44:47], a[36:39], a[124:127], v[44:47]\n"
"v_mfma_f32_16x16x32_bf16 v[44:47], a[32:35], a[120:123], v[44:47]\n"
"v_mfma_f32_16x16x32_bf16 v[48:51], a[32:35], a[128:131], v[48:51]\n"
"v_mfma_f32_16x16x32_bf16 v[48:51], a[36:39], a[132:135], v[48:51]\n"
"s_waitcnt lgkmcnt(0)\n"
"buffer_store_dwordx4 a[184:187], v1, s[36:39], s74 offen offset:0 sc1\n"
"v_mfma_f32_16x16x32_bf16 v[76:79], a[44:47], a[132:135], v[76:79]\n"
"s_waitcnt vmcnt(6)\n"
"ds_read_b64_tr_b16 a[64:65], v9 offset:0\n"
"v_mfma_f32_16x16x32_bf16 v[76:79], a[40:43], a[128:131], v[76:79]\n"
"ds_read_b64_tr_b16 a[66:67], v9 offset:512\n"
"v_mfma_f32_16x16x32_bf16 v[80:83], a[40:43], a[136:139], v[80:83]\n"
"ds_read_b64_tr_b16 a[68:69], v9 offset:4096\n"
"v_mfma_f32_16x16x32_bf16 v[80:83], a[44:47], a[140:143], v[80:83]\n"
"ds_read_b64_tr_b16 a[70:71], v9 offset:4608\n"
"v_mfma_f32_16x16x32_bf16 v[52:55], a[36:39], a[140:143], v[52:55]\n"
"ds_read_b64_tr_b16 a[72:73], v9 offset:8\n"
"v_subrev_u32 v24, s70, v24\n"
"v_mfma_f32_16x16x32_bf16 v[52:55], a[32:35], a[136:139], v[52:55]\n"
"ds_read_b64_tr_b16 a[74:75], v9 offset:520\n"
"v_xor_b32 v25, s79, v25\n"
"v_mfma_f32_16x16x32_bf16 v[56:59], a[32:35], a[144:147], v[56:59]\n"
"ds_read_b64_tr_b16 a[76:77], v9 offset:4104\n"
"v_subrev_u32 v26, s70, v26\n"
"v_mfma_f32_16x16x32_bf16 v[56:59], a[36:39], a[148:151], v[56:59]\n"
"ds_read_b64_tr_b16 a[78:79], v9 offset:4616\n"
"v_xor_b32 v27, s79, v27\n"
"v_mfma_f32_16x16x32_bf16 v[84:87], a[44:47], a[148:151], v[84:87]\n"
"ds_read_b64_tr_b16 a[80:81], v15 offset:0\n"
"v_or_b32 v24, v24, v26\n"
"v_mfma_f32_16x16x32_bf16 v[84:87], a[40:43], a[144:147], v[84:87]\n"
"ds_read_b64_tr_b16 a[82:83], v15 offset:512\n"
"v_or_b32 v25, v25, v27\n"
"v_mfma_f32_16x16x32_bf16 a[4:7], a[40:43], a[152:155], a[4:7]\n"
"ds_read_b64_tr_b16 a[84:85], v15 offset:4096\n"
"v_or_b32 v24, v24, v25\n"
"v_mfma_f32_16x16x32_bf16 a[4:7], a[44:47], a[156:159], a[4:7]\n"
"ds_read_b64_tr_b16 a[86:87], v15 offset:4608\n"
"v_cmp_lt_u32 vcc, 15, v24\n"
"v_mfma_f32_16x16x32_bf16 a[0:3], a[36:39], a[156:159], a[0:3]\n"
"buffer_store_dwordx4 a[188:191], v1, s[36:39], s74 offen offset:2048 sc1\n"
"ds_read_b64_tr_b16 a[88:89], v15 offset:8\n"
"s_cbranch_vccnz L_slow3\n"
"L_back3:\n"
"v_mfma_f32_16x16x32_bf16 a[0:3], a[32:35], a[152:155], a[0:3]\n"
"ds_read_b64_tr_b16 a[90:91], v15 offset:520\n"
"v_mfma_f32_16x16x32_bf16 v[88:91], a[48:51], a[96:99], v[88:91]\n"
"ds_read_b64_tr_b16 a[92:93], v15 offset:4104\n"
"v_mfma_f32_16x16x32_bf16 v[88:91], a[52:55], a[100:103], v[88:91]\n"
"global_load_dwordx4 a[32:35], v0, s[34:35] offset:-4096 sc1\n"
"s_barrier\n"
"v_mfma_f32_16x16x32_bf16 v[116:119], a[60:63], a[100:103], v[116:119]\n"
"ds_read_b64_tr_b16 a[94:95], v15 offset:4616\n"
"v_mfma_f32_16x16x32_bf16 v[116:119], a[56:59], a[96:99], v[116:119]\n"
"ds_write_b64 v2, a[192:193] offset:16384\n"
"v_mfma_f32_16x16x32_bf16 v[120:123], a[56:59], a[104:107], v[120:123]\n"
"ds_write_b64 v2, a[194:195] offset:16392\n"
"v_mfma_f32_16x16x32_bf16 v[120:123], a[60:63], a[108:111], v[120:123]\n"
"ds_write_b64 v2, a[196:197] offset:17408\n"
"v_mfma_f32_16x16x32_bf16 v[92:95], a[52:55], a[108:111], v[92:95]\n"
"ds_read_b128 a[160:163], v7 offset:24576\n"
"v_mfma_f32_16x16x32_bf16 v[92:95], a[48:51], a[104:107], v[92:95]\n"
"ds_write_b64 v2, a[198:199] offset:17416\n"
"v_mfma_f32_16x16x32_bf16 v[96:99], a[48:51], a[112:115], v[96:99]\n"
"s_waitcnt lgkmcnt(5)\n"
"ds_read_b128 a[164:167], v8 offset:24576\n"
"v_mfma_f32_16x16x32_bf16 v[96:99], a[52:55], a[116:119], v[96:99]\n"
"global_load_dwordx4 a[36:39], v0, s[34:35] offset:-3072 sc1\n"
"v_mfma_f32_16x16x32_bf16 v[124:127], a[60:63], a[116:119], v[124:127]\n"
"ds_read_b128 a[168:171], v7 offset:26624\n"
"v_mfma_f32_16x16x32_bf16 v[124:127], a[56:59], a[112:115], v[124:127]\n"
"ds_read_b128 a[172:175], v8 offset:26624\n"
"v_mfma_f32_16x16x32_bf16 v[128:131], a[56:59], a[120:123], v[128:131]\n"
"ds_write_b64 v2, a[200:201] offset:18432\n"
"v_mfma_f32_16x16x32_bf16 v[128:131], a[60:63], a[124:127], v[128:131]\n"
"ds_read_b128 a[176:179], v7 offset:28672\n"
"v_mfma_f32_16x16x32_bf16 v[100:103], a[52:55], a[124:127], v[100:103]\n"
"ds_read_b128 a[180:183], v8 offset:28672\n"
"v_mfma_f32_16x16x32_bf16 v[100:103], a[48:51], a[120:123], v[100:103]\n"
"ds_write_b64 v2, a[202:203] offset:18440\n"
"v_mfma_f32_16x16x32_bf16 v[104:107], a[48:51], a[128:131], v[104:107]\n"
"ds_read_b128 a[184:187], v7 offset:30720\n"
"v_mfma_f32_16x16x32_bf16 v[104:107], a[52:55], a[132:135], v[104:107]\n"
"global_load_dwordx4 a[40:43], v0, s[34:35] offset:-2048 sc1\n"
"v_mfma_f32_16x16x32_bf16 v[132:135], a[60:63], a[132:135], v[132:135]\n"
"ds_read_b128 a[188:191], v8 offset:30720\n"
"v_mfma_f32_16x16x32_bf16 v[132:135], a[56:59], a[128:131], v[132:135]\n"
"ds_write_b64 v2, a[204:205] offset:19456\n"
"v_mfma_f32_16x16x32_bf16 v[136:139], a[56:59], a[136:139], v[136:139]\n"
"ds_write_b64 v2, a[206:207] offset:19464\n"
"v_mfma_f32_16x16x32_bf16 v[136:139], a[60:63], a[140:143], v[136:139]\n"
"ds_write_b64 v2, a[208:209] offset:20480\n"
"v_mfma_f32_16x16x32_bf16 v[108:111], a[52:55], a[140:143], v[108:111]\n"
"ds_write_b64 v2, a[210:211] offset:20488\n"
"v_mfma_f32_16x16x32_bf16 v[108:111], a[48:51], a[136:139], v[108:111]\n"
"ds_write_b64 v2, a[212:213] offset:21504\n"
"v_mfma_f32_16x16x32_bf16 v[112:115], a[48:51], a[144:147], v[112:115]\n"
"ds_write_b64 v2, a[214:215] offset:21512\n"
"v_mfma_f32_16x16x32_bf16 v[112:115], a[52:55], a[148:151], v[112:115]\n"
"global_load_dwordx4 a[44:47], v0, s[34:35] offset:-1024 sc1\n"
"v_mfma_f32_16x16x32_bf16 v[140:143], a[60:63], a[148:151], v[140:143]\n"
"ds_write_b64 v2, a[216:217] offset:22528\n"
"v_mfma_f32_16x16x32_bf16 v[140:143], a[56:59], a[144:147], v[140:143]\n"
"v_mfma_f32_16x16x32_bf16 a[12:15], a[56:59], a[152:155], a[12:15]\n"
"ds_write_b64 v2, a[218:219] offset:22536\n"
"v_mfma_f32_16x16x32_bf16 a[12:15], a[60:63], a[156:159], a[12:15]\n"
"v_mfma_f32_16x16x32_bf16 a[8:11], a[52:55], a[156:159], a[8:11]\n"
"ds_write_b64 v2, a[220:221] offset:23552\n"
"v_mfma_f32_16x16x32_bf16 a[8:11], a[48:51], a[152:155], a[8:11]\n"
"v_mfma_f32_16x16x32_bf16 v[144:147], a[64:67], a[96:99], v[144:147]\n"
"ds_write_b64 v2, a[222:223] offset:23560\n"
"v_mfma_f32_16x16x32_bf16 v[144:147], a[68:71], a[100:103], v[144:147]\n"
"global_load_dwordx4 a[48:51], v0, s[34:35] offset:0 sc1\n"
"v_mfma_f32_16x16x32_bf16 v[172:175], a[76:79], a[100:103], v[172:175]\n"
"ds_write_b64 v2, a[224:225] offset:24576\n"
"v_mfma_f32_16x16x32_bf16 v[172:175], a[72:75], a[96:99], v[172:175]\n"
"ds_write_b64 v2, a[226:227] offset:24584\n"
"v_mfma_f32_16x16x32_bf16 v[176:179], a[72:75], a[104:107], v[176:179]\n"
"v_mfma_f32_16x16x32_bf16 v[176:179], a[76:79], a[108:111], v[176:179]\n"
"ds_write_b64 v2, a[228:229] offset:25600\n"
"v_mfma_f32_16x16x32_bf16 v[148:151], a[68:71], a[108:111], v[148:151]\n"
"v_mfma_f32_16x16x32_bf16 v[148:151], a[64:67], a[104:107], v[148:151]\n"
"ds_write_b64 v2, a[230:231] offset:25608\n"
"v_mfma_f32_16x16x32_bf16 v[152:155], a[64:67], a[112:115], v[152:155]\n"
"ds_write_b64 v2, a[232:233] offset:26624\n"
"v_mfma_f32_16x16x32_bf16 v[152:155], a[68:71], a[116:119], v[152:155]\n"
"global_load_dwordx4 a[52:55], v0, s[34:35] offset:1024 sc1\n"
"v_mfma_f32_16x16x32_bf16 v[180:183], a[76:79], a[116:119], v[180:183]\n"
"v_mfma_f32_16x16x32_bf16 v[180:183], a[72:75], a[112:115], v[180:183]\n"
"ds_write_b64 v2, a[234:235] offset:26632\n"
"v_mfma_f32_16x16x32_bf16 v[184:187], a[72:75], a[120:123], v[184:187]\n"
"v_mfma_f32_16x16x32_bf16 v[184:187], a[76:79], a[124:127], v[184:187]\n"
"ds_write_b64 v2, a[236:237] offset:27648\n"
"v_mfma_f32_16x16x32_bf16 v[156:159], a[68:71], a[124:127], v[156:159]\n"
"v_mfma_f32_16x16x32_bf16 v[156:159], a[64:67], a[120:123], v[156:159]\n"
"ds_write_b64 v2, a[238:239] offset:27656\n"
"v_mfma_f32_16x16x32_bf16 v[160:163], a[64:67], a[128:131], v[160:163]\n"
"ds_write_b64 v2, a[240:241] offset:28672\n"
"v_mfma_f32_16x16x32_bf16 v[160:163], a[68:71], a[132:135], v[160:163]\n"
"global_load_dwordx4 a[56:59], v0, s[34:35] offset:2048 sc1\n"
"v_mfma_f32_16x16x32_bf16 v[188:191], a[76:79], a[132:135], v[188:191]\n"
"ds_write_b64 v2, a[242:243] offset:28680\n"
"v_mfma_f32_16x16x32_bf16 v[188:191], a[72:75], a[128:131], v[188:191]\n"
"v_mfma_f32_16x16x32_bf16 v[192:195], a[72:75], a[136:139], v[192:195]\n"
"ds_write_b64 v2, a[244:245] offset:29696\n"
"v_mfma_f32_16x16x32_bf16 v[192:195], a[76:79], a[140:143], v[192:195]\n"
"v_mfma_f32_16x16x32_bf16 v[164:167], a[68:71], a[140:143], v[164:167]\n"
"ds_write_b64 v2, a[246:247] offset:29704\n"
"v_mfma_f32_16x16x32_bf16 v[164:167], a[64:67], a[136:139], v[164:167]\n"
"s_add_u32 s74, s78, 2\n"
"v_mfma_f32_16x16x32_bf16 v[168:171], a[64:67], a[144:147], v[168:171]\n"
"ds_write_b64 v2, a[248:249] offset:30720\n"
"v_mfma_f32_16x16x32_bf16 v[168:171], a[68:71], a[148:151], v[168:171]\n"
"global_load_dwordx4 a[60:63], v0, s[34:35] offset:3072 sc1\n"
"v_mov_b32 v28, s74\n"
"v_mfma_f32_16x16x32_bf16 v[196:199], a[76:79], a[148:151], v[196:199]\n"
"v_mov_b32 v29, s79\n"
"v_mfma_f32_16x16x32_bf16 v[196:199], a[72:75], a[144:147], v[196:199]\n"
"ds_write_b64 v2, a[250:251] offset:30728\n"
"v_mfma_f32_16x16x32_bf16 a[20:23], a[72:75], a[152:155], a[20:23]\n"
"s_waitcnt vmcnt(8)\n"
"buffer_store_dwordx2 v[28:29], off, s[56:59], %24 sc1\n"
"s_add_u32 s34, s34, 8192\n"
"v_mfma_f32_16x16x32_bf16 a[20:23], a[76:79], a[156:159], a[20:23]\n"
"ds_write_b64 v2, a[252:253] offset:31744\n"
"v_mfma_f32_16x16x32_bf16 a[16:19], a[68:71], a[156:159], a[16:19]\n"
"ds_write_b64 v2, a[254:255] offset:31752\n"
"v_mfma_f32_16x16x32_bf16 a[16:19], a[64:67], a[152:155], a[16:19]\n"
"v_mfma_f32_16x16x32_bf16 v[200:203], a[80:83], a[96:99], v[200:203]\n"
"v_lshrrev_b32 v16, 4, v0\n"
"v_mfma_f32_16x16x32_bf16 v[200:203], a[84:87], a[100:103], v[200:203]\n"
"global_load_dwordx4 a[64:67], v0, s[34:35] offset:-4096 sc1\n"
"v_mfma_f32_16x16x32_bf16 v[228:231], a[92:95], a[100:103], v[228:231]\n"
"ds_read_b128 a[100:103], v2 offset:17408\n"
"v_mfma_f32_16x16x32_bf16 v[228:231], a[88:91], a[96:99], v[228:231]\n"
"ds_read_b128 a[96:99], v2 offset:16384\n"
"v_mfma_f32_16x16x32_bf16 v[232:235], a[88:91], a[104:107], v[232:235]\n"
"v_and_b32 v16, 0x30, v16\n"
"v_mfma_f32_16x16x32_bf16 v[232:235], a[92:95], a[108:111], v[232:235]\n"
"buffer_load_dwordx2 v[18:19], v16, s[44:47], s61 offen offset:128\n"
"v_mfma_f32_16x16x32_bf16 v[204:207], a[84:87], a[108:111], v[204:207]\n"
"ds_read_b128 a[108:111], v2 offset:19456\n"
"v_mfma_f32_16x16x32_bf16 v[204:207], a[80:83], a[104:107], v[204:207]\n"
"ds_read_b128 a[104:107], v2 offset:18432\n"
"v_mfma_f32_16x16x32_bf16 v[208:211], a[80:83], a[112:115], v[208:211]\n"
"v_lshrrev_b32 v20, 4, v0\n"
"v_mfma_f32_16x16x32_bf16 v[208:211], a[84:87], a[116:119], v[208:211]\n"
"global_load_dwordx4 a[68:71], v0, s[34:35] offset:-3072 sc1\n"
"v_mfma_f32_16x16x32_bf16 v[236:239], a[92:95], a[116:119], v[236:239]\n"
"ds_read_b128 a[116:119], v2 offset:21504\n"
"v_mfma_f32_16x16x32_bf16 v[236:239], a[88:91], a[112:115], v[236:239]\n"
"ds_read_b128 a[112:115], v2 offset:20480\n"
"v_mfma_f32_16x16x32_bf16 v[240:243], a[88:91], a[120:123], v[240:243]\n"
"v_and_b32 v20, 0x30, v20\n"
"v_mfma_f32_16x16x32_bf16 v[240:243], a[92:95], a[124:127], v[240:243]\n"
"buffer_load_dwordx2 v[22:23], v20, s[44:47], s61 offen offset:136\n"
"v_mfma_f32_16x16x32_bf16 v[212:215], a[84:87], a[124:127], v[212:215]\n"
"ds_read_b128 a[124:127], v2 offset:23552\n"
"v_mfma_f32_16x16x32_bf16 v[212:215], a[80:83], a[120:123], v[212:215]\n"
"ds_read_b128 a[120:123], v2 offset:22528\n"
"v_mfma_f32_16x16x32_bf16 v[216:219], a[80:83], a[128:131], v[216:219]\n"
"v_lshrrev_b32 v24, 4, v0\n"
"v_mfma_f32_16x16x32_bf16 v[216:219], a[84:87], a[132:135], v[216:219]\n"
"global_load_dwordx4 a[72:75], v0, s[34:35] offset:-2048 sc1\n"
"v_mfma_f32_16x16x32_bf16 v[244:247], a[92:95], a[132:135], v[244:247]\n"
"ds_read_b128 a[132:135], v2 offset:25600\n"
"v_and_b32 v24, 0x30, v24\n"
"v_mfma_f32_16x16x32_bf16 v[244:247], a[88:91], a[128:131], v[244:247]\n"
"ds_read_b128 a[128:131], v2 offset:24576\n"
"v_mfma_f32_16x16x32_bf16 v[248:251], a[88:91], a[136:139], v[248:251]\n"
"buffer_load_dwordx2 v[26:27], v24, s[44:47], s61 offen offset:192\n"
"v_mfma_f32_16x16x32_bf16 v[248:251], a[92:95], a[140:143], v[248:251]\n"
"v_lshrrev_b32 v28, 4, v0\n"
"v_mfma_f32_16x16x32_bf16 v[220:223], a[84:87], a[140:143], v[220:223]\n"
"ds_read_b128 a[140:143], v2 offset:27648\n"
"v_mfma_f32_16x16x32_bf16 v[220:223], a[80:83], a[136:139], v[220:223]\n"
"ds_read_b128 a[136:139], v2 offset:26624\n"
"v_mfma_f32_16x16x32_bf16 v[224:227], a[80:83], a[144:147], v[224:227]\n"
"v_and_b32 v28, 0x30, v28\n"
"v_mfma_f32_16x16x32_bf16 v[224:227], a[84:87], a[148:151], v[224:227]\n"
"global_load_dwordx4 a[76:79], v0, s[34:35] offset:-1024 sc1\n"
"v_mfma_f32_16x16x32_bf16 v[252:255], a[92:95], a[148:151], v[252:255]\n"
"ds_read_b128 a[148:151], v2 offset:29696\n"
"v_mfma_f32_16x16x32_bf16 v[252:255], a[88:91], a[144:147], v[252:255]\n"
"ds_read_b128 a[144:147], v2 offset:28672\n"
"v_mfma_f32_16x16x32_bf16 a[28:31], a[88:91], a[152:155], a[28:31]\n"
"buffer_load_dwordx2 v[30:31], v28, s[44:47], s61 offen offset:200\n"
"v_mfma_f32_16x16x32_bf16 a[28:31], a[92:95], a[156:159], a[28:31]\n"
"v_mfma_f32_16x16x32_bf16 a[24:27], a[84:87], a[156:159], a[24:27]\n"
"ds_read_b128 a[156:159], v2 offset:31744\n"
"v_mfma_f32_16x16x32_bf16 a[24:27], a[80:83], a[152:155], a[24:27]\n"
"s_waitcnt vmcnt(13) lgkmcnt(11)\n"
"ds_read_b128 a[152:155], v2 offset:30720\n"
"v_mfma_f32_16x16x32_bf16 v[32:35], a[32:35], a[96:99], v[32:35]\n"
"v_mfma_f32_16x16x32_bf16 v[32:35], a[36:39], a[100:103], v[32:35]\n"
"global_load_dwordx4 a[80:83], v0, s[34:35] offset:0 sc1\n"
"v_mfma_f32_16x16x32_bf16 v[60:63], a[44:47], a[100:103], v[60:63]\n"
"s_barrier\n"
"v_mfma_f32_16x16x32_bf16 v[60:63], a[40:43], a[96:99], v[60:63]\n"
"v_mfma_f32_16x16x32_bf16 v[64:67], a[40:43], a[104:107], v[64:67]\n"
"v_mfma_f32_16x16x32_bf16 v[64:67], a[44:47], a[108:111], v[64:67]\n"
"v_mfma_f32_16x16x32_bf16 v[36:39], a[36:39], a[108:111], v[36:39]\n"
"v_mfma_f32_16x16x32_bf16 v[36:39], a[32:35], a[104:107], v[36:39]\n"
"s_waitcnt lgkmcnt(8)\n"
"v_mfma_f32_16x16x32_bf16 v[40:43], a[32:35], a[112:115], v[40:43]\n"
"v_mfma_f32_16x16x32_bf16 v[40:43], a[36:39], a[116:119], v[40:43]\n"
"global_load_dwordx4 a[84:87], v0, s[34:35] offset:1024 sc1\n"
"v_mfma_f32_16x16x32_bf16 v[68:71], a[44:47], a[116:119], v[68:71]\n"
"v_mfma_f32_16x16x32_bf16 v[68:71], a[40:43], a[112:115], v[68:71]\n"
"v_mfma_f32_16x16x32_bf16 v[72:75], a[40:43], a[120:123], v[72:75]\n"
"v_mfma_f32_16x16x32_bf16 v[72:75], a[44:47], a[124:127], v[72:75]\n"
"s_add_u32 s61, s61, 2048\n"
"v_mfma_f32_16x16x32_bf16 v[44:47], a[36:39], a[124:127], v[44:47]\n"
"v_mfma_f32_16x16x32_bf16 v[44:47], a[32:35], a[120:123], v[44:47]\n"
"s_waitcnt lgkmcnt(4)\n"
"v_mfma_f32_16x16x32_bf16 v[48:51], a[32:35], a[128:131], v[48:51]\n"
"s_waitcnt vmcnt(8)\n"
"v_lshlrev_b32 v16, 16, v18\n"
"v_mfma_f32_16x16x32_bf16 v[48:51], a[36:39], a[132:135], v[48:51]\n"
"global_load_dwordx4 a[88:91], v0, s[34:35] offset:2048 sc1\n"
"v_and_b32 v17, s63, v18\n"
"v_mfma_f32_16x16x32_bf16 v[76:79], a[44:47], a[132:135], v[76:79]\n"
"v_lshlrev_b32 v18, 16, v19\n"
"v_and_b32 v19, s63, v19\n"
"v_mfma_f32_16x16x32_bf16 v[76:79], a[40:43], a[128:131], v[76:79]\n"
"v_add_f32 v144, v144, v16\n"
"v_mfma_f32_16x16x32_bf16 v[80:83], a[40:43], a[136:139], v[80:83]\n"
"v_add_f32 v145, v145, v17\n"
"v_add_f32 v146, v146, v18\n"
"v_mfma_f32_16x16x32_bf16 v[80:83], a[44:47], a[140:143], v[80:83]\n"
"v_add_f32 v147, v147, v19\n"
"v_mfma_f32_16x16x32_bf16 v[52:55], a[36:39], a[140:143], v[52:55]\n"
"v_add_f32 v148, v148, v16\n"
"v_mfma_f32_16x16x32_bf16 v[52:55], a[32:35], a[136:139], v[52:55]\n"
"s_mov_b32 s99, 0\n"
"v_add_f32 v149, v149, v17\n"
"s_waitcnt lgkmcnt(0)\n"
"v_mfma_f32_16x16x32_bf16 v[56:59], a[32:35], a[144:147], v[56:59]\n"
"s_mov_b32 s98, 0\n"
"v_add_f32 v150, v150, v18\n"
"v_add_f32 v151, v151, v19\n"
"v_mfma_f32_16x16x32_bf16 v[56:59], a[36:39], a[148:151], v[56:59]\n"
"global_load_dwordx4 a[92:95], v0, s[34:35] offset:3072 sc1\n"
"s_memtime s[76:77]\n"
"s_load_dword s98, s[96:97], 8448 glc\n"
"v_mfma_f32_16x16x32_bf16 v[84:87], a[44:47], a[148:151], v[84:87]\n"
"v_add_f32 v152, v152, v16\n"
"v_add_f32 v153, v153, v17\n"
"v_mfma_f32_16x16x32_bf16 v[84:87], a[40:43], a[144:147], v[84:87]\n"
"v_add_f32 v154, v154, v18\n"
"v_add_f32 v155, v155, v19\n"
"v_mfma_f32_16x16x32_bf16 a[4:7], a[40:43], a[152:155], a[4:7]\n"
"s_add_u32 s34, s34, 8192\n"
"v_add_f32 v156, v156, v16\n"
"v_mfma_f32_16x16x32_bf16 a[4:7], a[44:47], a[156:159], a[4:7]\n"
"v_add_f32 v157, v157, v17\n"
"v_mfma_f32_16x16x32_bf16 a[0:3], a[36:39], a[156:159], a[0:3]\n"
"v_add_f32 v158, v158, v18\n"
"v_add_f32 v159, v159, v19\n"
"v_mfma_f32_16x16x32_bf16 a[0:3], a[32:35], a[152:155], a[0:3]\n"
"v_add_f32 v160, v160, v16\n"
"v_mfma_f32_16x16x32_bf16 v[88:91], a[48:51], a[96:99], v[88:91]\n"
"v_add_f32 v161, v161, v17\n"
"v_add_f32 v162, v162, v18\n"
"v_mfma_f32_16x16x32_bf16 v[88:91], a[52:55], a[100:103], v[88:91]\n"
"global_load_dwordx4 a[32:35], v0, s[34:35] offset:-4096 sc1\n"
"v_add_f32 v163, v163, v19\n"
"v_mfma_f32_16x16x32_bf16 v[116:119], a[60:63], a[100:103], v[116:119]\n"
"s_mov_b32 s30, s76\n"
"ds_read_b128 a[100:103], v3 offset:17408\n"
"v_add_f32 v164, v164, v16\n"
"v_mfma_f32_16x16x32_bf16 v[116:119], a[56:59], a[96:99], v[116:119]\n"
"ds_read_b128 a[96:99], v3 offset:16384\n"
"v_add_f32 v165, v165, v17\n"
"v_mfma_f32_16x16x32_bf16 v[120:123], a[56:59], a[104:107], v[120:123]\n"
"s_sub_u32 s99, s99, s98\n"
"v_add_f32 v166, v166, v18\n"
"v_add_f32 v167, v167, v19\n"
"v_mfma_f32_16x16x32_bf16 v[120:123], a[60:63], a[108:111], v[120:123]\n"
"s_sub_u32 s99, s99, s98\n"
"v_add_f32 v168, v168, v16\n"
"v_add_f32 v169, v169, v17\n"
"v_mfma_f32_16x16x32_bf16 v[92:95], a[52:55], a[108:111], v[92:95]\n"
"s_sub_u32 s99, s99, s98\n"
"ds_read_b128 a[108:111], v3 offset:19456\n"
"v_add_f32 v170, v170, v18\n"
"v_mfma_f32_16x16x32_bf16 v[92:95], a[48:51], a[104:107], v[92:95]\n"
"s_sub_u32 s99, s99, s98\n"
"ds_read_b128 a[104:107], v3 offset:18432\n"
"v_add_f32 v171, v171, v19\n"
"v_mfma_f32_16x16x32_bf16 v[96:99], a[48:51], a[112:115], v[96:99]\n"
"s_sub_u32 s99, s99, s98\n"
"v_accvgpr_read_b32 v12, a16\n"
"v_accvgpr_read_b32 v13, a17\n"
"v_mfma_f32_16x16x32_bf16 v[96:99], a[52:55], a[116:119], v[96:99]\n"
"global_load_dwordx4 a[36:39], v0, s[34:35] offset:-3072 sc1\n"
"s_sub_u32 s99, s99, s98\n"
"v_mfma_f32_16x16x32_bf16 v[124:127], a[60:63], a[116:119], v[124:127]\n"
"s_sub_u32 s99, s99, s98\n"
"ds_read_b128 a[116:119], v3 offset:21504\n"
"v_accvgpr_read_b32 v14, a18\n"
"v_mfma_f32_16x16x32_bf16 v[124:127], a[56:59], a[112:115], v[124:127]\n"
"s_sub_u32 s99, s99, s98\n"
"ds_read_b128 a[112:115], v3 offset:20480\n"
"v_accvgpr_read_b32 v15, a19\n"
"v_mfma_f32_16x16x32_bf16 v[128:131], a[56:59], a[120:123], v[128:131]\n"
"s_sub_u32 s99, s99, s98\n"
"v_add_f32 v12, v12, v16\n"
"v_add_f32 v13, v13, v17\n"
"v_mfma_f32_16x16x32_bf16 v[128:131], a[60:63], a[124:127], v[128:131]\n"
"s_sub_u32 s99, s99, s98\n"
"v_add_f32 v14, v14, v18\n"
"v_add_f32 v15, v15, v19\n"
"v_mfma_f32_16x16x32_bf16 v[100:103], a[52:55], a[124:127], v[100:103]\n"
"s_sub_u32 s99, s99, s98\n"
"ds_read_b128 a[124:127], v3 offset:23552\n"
"v_accvgpr_write_b32 a16, v12\n"
"v_mfma_f32_16x16x32_bf16 v[100:103], a[48:51], a[120:123], v[100:103]\n"
"s_sub_u32 s99, s99, s98\n"
"ds_read_b128 a[120:123], v3 offset:22528\n"
"v_accvgpr_write_b32 a17, v13\n"
"v_mfma_f32_16x16x32_bf16 v[104:107], a[48:51], a[128:131], v[104:107]\n"
"s_sub_u32 s99, s99, s98\n"
"v_accvgpr_write_b32 a18, v14\n"
"v_accvgpr_write_b32 a19, v15\n"
"v_mfma_f32_16x16x32_bf16 v[104:107], a[52:55], a[132:135], v[104:107]\n"
"s_sub_u32 s99, s99, s98\n"
"s_waitcnt vmcnt(10)\n"
"global_load_dwordx4 a[40:43], v0, s[34:35] offset:-2048 sc1\n"
"v_mfma_f32_16x16x32_bf16 v[132:135], a[60:63], a[132:135], v[132:135]\n"
"s_sub_u32 s99, s99, s98\n"
"ds_read_b128 a[132:135], v3 offset:25600\n"
"v_lshlrev_b32 v20, 16, v22\n"
"v_mfma_f32_16x16x32_bf16 v[132:135], a[56:59], a[128:131], v[132:135]\n"
"s_sub_u32 s99, s99, s98\n"
"ds_read_b128 a[128:131], v3 offset:24576\n"
"v_and_b32 v21, s63, v22\n"
"v_mfma_f32_16x16x32_bf16 v[136:139], a[56:59], a[136:139], v[136:139]\n"
"s_sub_u32 s99, s99, s98\n"
"v_lshlrev_b32 v22, 16, v23\n"
"v_and_b32 v23, s63, v23\n"
"v_mfma_f32_16x16x32_bf16 v[136:139], a[60:63], a[140:143], v[136:139]\n"
"s_sub_u32 s99, s99, s98\n"
"v_add_f32 v172, v172, v20\n"
"v_add_f32 v173, v173, v21\n"
"v_mfma_f32_16x16x32_bf16 v[108:111], a[52:55], a[140:143], v[108:111]\n"
"s_sub_u32 s99, s99, s98\n"
"ds_read_b128 a[140:143], v3 offset:27648\n"
"v_add_f32 v174, v174, v22\n"
"v_mfma_f32_16x16x32_bf16 v[108:111], a[48:51], a[136:139], v[108:111]\n"
"s_sub_u32 s99, s99, s98\n"
"ds_read_b128 a[136:139], v3 offset:26624\n"
"v_add_f32 v175, v175, v23\n"
"v_mfma_f32_16x16x32_bf16 v[112:115], a[48:51], a[144:147], v[112:115]\n"
"s_sub_u32 s99, s99, s98\n"
"v_add_f32 v176, v176, v20\n"
"v_add_f32 v177, v177, v21\n"
"v_mfma_f32_16x16x32_bf16 v[112:115], a[52:55], a[148:151], v[112:115]\n"
"global_load_dwordx4 a[44:47], v0, s[34:35] offset:-1024 sc1\n"
"s_sub_u32 s99, s99, s98\n"
"v_mfma_f32_16x16x32_bf16 v[140:143], a[60:63], a[148:151], v[140:143]\n"
"s_sub_u32 s99, s99, s98\n"
"ds_read_b128 a[148:151], v3 offset:29696\n"
"v_add_f32 v178, v178, v22\n"
"v_mfma_f32_16x16x32_bf16 v[140:143], a[56:59], a[144:147], v[140:143]\n"
"s_sub_u32 s99, s99, s98\n"
"ds_read_b128 a[144:147], v3 offset:28672\n"
"v_add_f32 v179, v179, v23\n"
"v_mfma_f32_16x16x32_bf16 a[12:15], a[56:59], a[152:155], a[12:15]\n"
"s_memtime s[76:77]\n"
"v_add_f32 v180, v180, v20\n"
"v_add_f32 v181, v181, v21\n"
"v_mfma_f32_16x16x32_bf16 a[12:15], a[60:63], a[156:159], a[12:15]\n"
"v_add_f32 v182, v182, v22\n"
"v_add_f32 v183, v183, v23\n"
"v_mfma_f32_16x16x32_bf16 a[8:11], a[52:55], a[156:159], a[8:11]\n"
"ds_read_b128 a[156:159], v3 offset:31744\n"
"v_add_f32 v184, v184, v20\n"
"v_mfma_f32_16x16x32_bf16 a[8:11], a[48:51], a[152:155], a[8:11]\n"
"s_waitcnt vmcnt(9) lgkmcnt(11)\n"
"ds_read_b128 a[152:155], v3 offset:30720\n"
"v_add_f32 v185, v185, v21\n"
"v_mfma_f32_16x16x32_bf16 v[32:35], a[64:67], a[96:99], v[32:35]\n"
"v_add_f32 v186, v186, v22\n"
"v_add_f32 v187, v187, v23\n"
"v_mfma_f32_16x16x32_bf16 v[32:35], a[68:71], a[100:103], v[32:35]\n"
"global_load_dwordx4 a[48:51], v0, s[34:35] offset:0 sc1\n"
"v_add_f32 v188, v188, v20\n"
"v_mfma_f32_16x16x32_bf16 v[60:63], a[76:79], a[100:103], v[60:63]\n"
"v_add_f32 v189, v189, v21\n"
"v_mfma_f32_16x16x32_bf16 v[60:63], a[72:75], a[96:99], v[60:63]\n"
"v_add_f32 v190, v190, v22\n"
"v_add_f32 v191, v191, v23\n"
"v_mfma_f32_16x16x32_bf16 v[64:67], a[72:75], a[104:107], v[64:67]\n"
"v_add_f32 v192, v192, v20\n"
"v_mfma_f32_16x16x32_bf16 v[64:67], a[76:79], a[108:111], v[64:67]\n"
"v_add_f32 v193, v193, v21\n"
"v_mfma_f32_16x16x32_bf16 v[36:39], a[68:71], a[108:111], v[36:39]\n"
"v_add_f32 v194, v194, v22\n"
"v_add_f32 v195, v195, v23\n"
"v_mfma_f32_16x16x32_bf16 v[36:39], a[64:67], a[104:107], v[36:39]\n"
"s_waitcnt lgkmcnt(8)\n"
"v_add_f32 v196, v196, v20\n"
"v_mfma_f32_16x16x32_bf16 v[40:43], a[64:67], a[112:115], v[40:43]\n"
"v_add_f32 v197, v197, v21\n"
"v_mfma_f32_16x16x32_bf16 v[40:43], a[68:71], a[116:119], v[40:43]\n"
"global_load_dwordx4 a[52:55], v0, s[34:35] offset:1024 sc1\n"
"v_add_f32 v198, v198, v22\n"
"v_mfma_f32_16x16x32_bf16 v[68:71], a[76:79], a[116:119], v[68:71]\n"
"v_add_f32 v199, v199, v23\n"
"v_accvgpr_read_b32 v12, a20\n"
"v_mfma_f32_16x16x32_bf16 v[68:71], a[72:75], a[112:115], v[68:71]\n"
"v_accvgpr_read_b32 v13, a21\n"
"v_mfma_f32_16x16x32_bf16 v[72:75], a[72:75], a[120:123], v[72:75]\n"
"v_accvgpr_read_b32 v14, a22\n"
"v_accvgpr_read_b32 v15, a23\n"
"v_mfma_f32_16x16x32_bf16 v[72:75], a[76:79], a[124:127], v[72:75]\n"
"v_add_f32 v12, v12, v20\n"
"v_mfma_f32_16x16x32_bf16 v[44:47], a[68:71], a[124:127], v[44:47]\n"
"v_add_f32 v13, v13, v21\n"
"v_mfma_f32_16x16x32_bf16 v[44:47], a[64:67], a[120:123], v[44:47]\n"
"s_waitcnt lgkmcnt(4)\n"
"v_add_f32 v14, v14, v22\n"
"v_mfma_f32_16x16x32_bf16 v[48:51], a[64:67], a[128:131], v[48:51]\n"
"v_add_f32 v15, v15, v23\n"
"v_accvgpr_write_b32 a20, v12\n"
"v_mfma_f32_16x16x32_bf16 v[48:51], a[68:71], a[132:135], v[48:51]\n"
"global_load_dwordx4 a[56:59], v0, s[34:35] offset:2048 sc1\n"
"v_accvgpr_write_b32 a21, v13\n"
"v_mfma_f32_16x16x32_bf16 v[76:79], a[76:79], a[132:135], v[76:79]\n"
"s_waitcnt lgkmcnt(0)\n"
"v_accvgpr_write_b32 a22, v14\n"
"v_mfma_f32_16x16x32_bf16 v[76:79], a[72:75], a[128:131], v[76:79]\n"
"s_sub_u32 s30, s76, s30\n"
"v_accvgpr_write_b32 a23, v15\n"
"v_lshlrev_b32 v24, 16, v26\n"
"v_mfma_f32_16x16x32_bf16 v[80:83], a[72:75], a[136:139], v[80:83]\n"
"s_cmp_lt_u32 s30, 620\n"
"s_cselect_b32 s99, s99, 0\n"
"v_and_b32 v25, s63, v26\n"
"v_mfma_f32_16x16x32_bf16 v[80:83], a[76:79], a[140:143], v[80:83]\n"
"s_max_u32 s32, s32, s99\n"
"v_lshlrev_b32 v26, 16, v27\n"
"v_mfma_f32_16x16x32_bf16 v[52:55], a[68:71], a[140:143], v[52:55]\n"
"v_and_b32 v27, s63, v27\n"
"v_add_f32 v200, v200, v24\n"
"v_mfma_f32_16x16x32_bf16 v[52:55], a[64:67], a[136:139], v[52:55]\n"
"s_mov_b32 s99, 0\n"
"v_add_f32 v201, v201, v25\n"
"v_mfma_f32_16x16x32_bf16 v[56:59], a[64:67], a[144:147], v[56:59]\n"
"s_mov_b32 s98, 0\n"
"v_add_f32 v202, v202, v26\n"
"v_add_f32 v203, v203, v27\n"
"v_mfma_f32_16x16x32_bf16 v[56:59], a[68:71], a[148:151], v[56:59]\n"
"global_load_dwordx4 a[60:63], v0, s[34:35] offset:3072 sc1\n"
"s_memtime s[76:77]\n"
"s_load_dword s98, s[96:97], 256 glc\n"
"v_mfma_f32_16x16x32_bf16 v[84:87], a[76:79], a[148:151], v[84:87]\n"
"v_add_f32 v204, v204, v24\n"
"v_add_f32 v205, v205, v25\n"
"v_mfma_f32_16x16x32_bf16 v[84:87], a[72:75], a[144:147], v[84:87]\n"
"v_add_f32 v206, v206, v26\n"
"v_mfma_f32_16x16x32_bf16 a[4:7], a[72:75], a[152:155], a[4:7]\n"
"s_add_u32 s34, s34, 8192\n"
"v_add_f32 v207, v207, v27\n"
"v_mfma_f32_16x16x32_bf16 a[4:7], a[76:79], a[156:159], a[4:7]\n"
"v_add_f32 v208, v208, v24\n"
"v_add_f32 v209, v209, v25\n"
"v_mfma_f32_16x16x32_bf16 a[0:3], a[68:71], a[156:159], a[0:3]\n"
"v_add_f32 v210, v210, v26\n"
"v_mfma_f32_16x16x32_bf16 a[0:3], a[64:67], a[152:155], a[0:3]\n"
"s_waitcnt vmcnt(8)\n"
"v_add_f32 v211, v211, v27\n"
"v_mfma_f32_16x16x32_bf16 v[88:91], a[80:83], a[96:99], v[88:91]\n"
"v_add_f32 v212, v212, v24\n"
"v_add_f32 v213, v213, v25\n"
"v_mfma_f32_16x16x32_bf16 v[88:91], a[84:87], a[100:103], v[88:91]\n"
"global_load_dwordx4 a[64:67], v0, s[34:35] offset:-4096 sc1\n"
"v_add_f32 v214, v214, v26\n"
"v_mfma_f32_16x16x32_bf16 v[116:119], a[92:95], a[100:103], v[116:119]\n"
"s_mov_b32 s30, s76\n"
"ds_read_b128 a[100:103], v4 offset:17408\n"
"v_add_f32 v215, v215, v27\n"
"v_mfma_f32_16x16x32_bf16 v[116:119], a[88:91], a[96:99], v[116:119]\n"
"ds_read_b128 a[96:99], v4 offset:16384\n"
"v_add_f32 v216, v216, v24\n"
"v_mfma_f32_16x16x32_bf16 v[120:123], a[88:91], a[104:107], v[120:123]\n"
"s_sub_u32 s99, s99, s98\n"
"v_add_f32 v217, v217, v25\n"
"v_add_f32 v218, v218, v26\n"
"v_mfma_f32_16x16x32_bf16 v[120:123], a[92:95], a[108:111], v[120:123]\n"
"s_sub_u32 s99, s99, s98\n"
"v_add_f32 v219, v219, v27\n"
"v_add_f32 v220, v220, v24\n"
"v_mfma_f32_16x16x32_bf16 v[92:95], a[84:87], a[108:111], v[92:95]\n"
"s_sub_u32 s99, s99, s98\n"
"ds_read_b128 a[108:111], v4 offset:19456\n"
"v_add_f32 v221, v221, v25\n"
"v_mfma_f32_16x16x32_bf16 v[92:95], a[80:83], a[104:107], v[92:95]\n"
"s_sub_u32 s99, s99, s98\n"
"ds_read_b128 a[104:107], v4 offset:18432\n"
"v_add_f32 v222, v222, v26\n"
"v_mfma_f32_16x16x32_bf16 v[96:99], a[80:83], a[112:115], v[96:99]\n"
"s_sub_u32 s99, s99, s98\n"
"v_add_f32 v223, v223, v27\n"
"v_add_f32 v224, v224, v24\n"
"v_mfma_f32_16x16x32_bf16 v[96:99], a[84:87], a[116:119], v[96:99]\n"
"global_load_dwordx4 a[68:71], v0, s[34:35] offset:-3072 sc1\n"
"s_sub_u32 s99, s99, s98\n"
"v_mfma_f32_16x16x32_bf16 v[124:127], a[92:95], a[116:119], v[124:127]\n"
"s_sub_u32 s99, s99, s98\n"
"ds_read_b128 a[116:119], v4 offset:21504\n"
"v_add_f32 v225, v225, v25\n"
"v_mfma_f32_16x16x32_bf16 v[124:127], a[88:91], a[112:115], v[124:127]\n"
"s_sub_u32 s99, s99, s98\n"
"ds_read_b128 a[112:115], v4 offset:20480\n"
"v_add_f32 v226, v226, v26\n"
"v_mfma_f32_16x16x32_bf16 v[128:131], a[88:91], a[120:123], v[128:131]\n"
"s_sub_u32 s99, s99, s98\n"
"v_add_f32 v227, v227, v27\n"
"v_accvgpr_read_b32 v12, a24\n"
"v_mfma_f32_16x16x32_bf16 v[128:131], a[92:95], a[124:127], v[128:131]\n"
"s_sub_u32 s99, s99, s98\n"
"v_accvgpr_read_b32 v13, a25\n"
"v_accvgpr_read_b32 v14, a26\n"
"v_mfma_f32_16x16x32_bf16 v[100:103], a[84:87], a[124:127], v[100:103]\n"
"s_sub_u32 s99, s99, s98\n"
"ds_read_b128 a[124:127], v4 offset:23552\n"
"v_accvgpr_read_b32 v15, a27\n"
"v_mfma_f32_16x16x32_bf16 v[100:103], a[80:83], a[120:123], v[100:103]\n"
"s_sub_u32 s99, s99, s98\n"
"ds_read_b128 a[120:123], v4 offset:22528\n"
"v_add_f32 v12, v12, v24\n"
"v_mfma_f32_16x16x32_bf16 v[104:107], a[80:83], a[128:131], v[104:107]\n"
"s_sub_u32 s99, s99, s98\n"
"v_add_f32 v13, v13, v25\n"
"v_add_f32 v14, v14, v26\n"
"v_mfma_f32_16x16x32_bf16 v[104:107], a[84:87], a[132:135], v[104:107]\n"
"global_load_dwordx4 a[72:75], v0, s[34:35] offset:-2048 sc1\n"
"s_sub_u32 s99, s99, s98\n"
"v_mfma_f32_16x16x32_bf16 v[132:135], a[92:95], a[132:135], v[132:135]\n"
"s_sub_u32 s99, s99, s98\n"
"ds_read_b128 a[132:135], v4 offset:25600\n"
"v_add_f32 v15, v15, v27\n"
"v_mfma_f32_16x16x32_bf16 v[132:135], a[88:91], a[128:131], v[132:135]\n"
"s_sub_u32 s99, s99, s98\n"
"ds_read_b128 a[128:131], v4 offset:24576\n"
"v_accvgpr_write_b32 a24, v12\n"
"v_mfma_f32_16x16x32_bf16 v[136:139], a[88:91], a[136:139], v[136:139]\n"
"s_sub_u32 s99, s99, s98\n"
"v_accvgpr_write_b32 a25, v13\n"
"v_accvgpr_write_b32 a26, v14\n"
"v_mfma_f32_16x16x32_bf16 v[136:139], a[92:95], a[140:143], v[136:139]\n"
"s_sub_u32 s99, s99, s98\n"
"v_accvgpr_write_b32 a27, v15\n"
"v_lshlrev_b32 v28, 16, v30\n"
"v_mfma_f32_16x16x32_bf16 v[108:111], a[84:87], a[140:143], v[108:111]\n"
"s_sub_u32 s99, s99, s98\n"
"ds_read_b128 a[140:143], v4 offset:27648\n"
"v_and_b32 v29, s63, v30\n"
"v_mfma_f32_16x16x32_bf16 v[108:111], a[80:83], a[136:139], v[108:111]\n"
"s_sub_u32 s99, s99, s98\n"
"ds_read_b128 a[136:139], v4 offset:26624\n"
"v_lshlrev_b32 v30, 16, v31\n"
"v_mfma_f32_16x16x32_bf16 v[112:115], a[80:83], a[144:147], v[112:115]\n"
"s_sub_u32 s99, s99, s98\n"
"v_and_b32 v31, s63, v31\n"
"v_add_f32 v228, v228, v28\n"
"v_mfma_f32_16x16x32_bf16 v[112:115], a[84:87], a[148:151], v[112:115]\n"
"global_load_dwordx4 a[76:79], v0, s[34:35] offset:-1024 sc1\n"
"s_sub_u32 s99, s99, s98\n"
"v_mfma_f32_16x16x32_bf16 v[140:143], a[92:95], a[148:151], v[140:143]\n"
"s_sub_u32 s99, s99, s98\n"
"ds_read_b128 a[148:151], v4 offset:29696\n"
"v_add_f32 v229, v229, v29\n"
"v_mfma_f32_16x16x32_bf16 v[140:143], a[88:91], a[144:147], v[140:143]\n"
"s_sub_u32 s99, s99, s98\n"
"ds_read_b128 a[144:147], v4 offset:28672\n"
"v_add_f32 v230, v230, v30\n"
"v_mfma_f32_16x16x32_bf16 a[12:15], a[88:91], a[152:155], a[12:15]\n"
"s_memtime s[76:77]\n"
"v_add_f32 v231, v231, v31\n"
"v_add_f32 v232, v232, v28\n"
"v_mfma_f32_16x16x32_bf16 a[12:15], a[92:95], a[156:159], a[12:15]\n"
"v_add_f32 v233, v233, v29\n"
"v_add_f32 v234, v234, v30\n"
"v_mfma_f32_16x16x32_bf16 a[8:11], a[84:87], a[156:159], a[8:11]\n"
"ds_read_b128 a[156:159], v4 offset:31744\n"
"v_add_f32 v235, v235, v31\n"
"v_mfma_f32_16x16x32_bf16 a[8:11], a[80:83], a[152:155], a[8:11]\n"
"s_waitcnt vmcnt(8) lgkmcnt(11)\n"
"ds_read_b128 a[152:155], v4 offset:30720\n"
"v_add_f32 v236, v236, v28\n"
"v_mfma_f32_16x16x32_bf16 v[32:35], a[32:35], a[96:99], v[32:35]\n"
"v_add_f32 v237, v237, v29\n"
"v_add_f32 v238, v238, v30\n"
"v_mfma_f32_16x16x32_bf16 v[32:35], a[36:39], a[100:103], v[32:35]\n"
"global_load_dwordx4 a[80:83], v0, s[34:35] offset:0 sc1\n"
"v_add_f32 v239, v239, v31\n"
"v_mfma_f32_16x16x32_bf16 v[60:63], a[44:47], a[100:103], v[60:63]\n"
"v_add_f32 v240, v240, v28\n"
"v_add_f32 v241, v241, v29\n"
"v_mfma_f32_16x16x32_bf16 v[60:63], a[40:43], a[96:99], v[60:63]\n"
"v_add_f32 v242, v242, v30\n"
"v_mfma_f32_16x16x32_bf16 v[64:67], a[40:43], a[104:107], v[64:67]\n"
"v_add_f32 v243, v243, v31\n"
"v_add_f32 v244, v244, v28\n"
"v_mfma_f32_16x16x32_bf16 v[64:67], a[44:47], a[108:111], v[64:67]\n"
"v_add_f32 v245, v245, v29\n"
"v_mfma_f32_16x16x32_bf16 v[36:39], a[36:39], a[108:111], v[36:39]\n"
"v_add_f32 v246, v246, v30\n"
"v_mfma_f32_16x16x32_bf16 v[36:39], a[32:35], a[104:107], v[36:39]\n"
"s_waitcnt lgkmcnt(8)\n"
"v_add_f32 v247, v247, v31\n"
"v_mfma_f32_16x16x32_bf16 v[40:43], a[32:35], a[112:115], v[40:43]\n"
"v_add_f32 v248, v248, v28\n"
"v_add_f32 v249, v249, v29\n"
"v_mfma_f32_16x16x32_bf16 v[40:43], a[36:39], a[116:119], v[40:43]\n"
"global_load_dwordx4 a[84:87], v0, s[34:35] offset:1024 sc1\n"
"s_add_u32 s48, s56, %12\n"
"s_addc_u32 s49, s57, 0\n"
"v_mfma_f32_16x16x32_bf16 v[68:71], a[44:47], a[116:119], v[68:71]\n"
"v_add_f32 v250, v250, v30\n"
"v_add_f32 v251, v251, v31\n"
"v_mfma_f32_16x16x32_bf16 v[68:71], a[40:43], a[112:115], v[68:71]\n"
"s_mov_b32 s50, 32768\n"
"v_add_f32 v252, v252, v28\n"
"v_add_f32 v253, v253, v29\n"
"v_mfma_f32_16x16x32_bf16 v[72:75], a[40:43], a[120:123], v[72:75]\n"
"v_add_f32 v254, v254, v30\n"
"v_mfma_f32_16x16x32_bf16 v[72:75], a[44:47], a[124:127], v[72:75]\n"
"s_mov_b32 s65, s78\n"
"v_add_f32 v255, v255, v31\n"
"v_mfma_f32_16x16x32_bf16 v[44:47], a[36:39], a[124:127], v[44:47]\n"
"v_accvgpr_read_b32 v12, a28\n"
"v_accvgpr_read_b32 v13, a29\n"
"v_mfma_f32_16x16x32_bf16 v[44:47], a[32:35], a[120:123], v[44:47]\n"
"s_waitcnt lgkmcnt(4)\n"
"v_accvgpr_read_b32 v14, a30\n"
"v_mfma_f32_16x16x32_bf16 v[48:51], a[32:35], a[128:131], v[48:51]\n"
"v_accvgpr_read_b32 v15, a31\n"
"v_mfma_f32_16x16x32_bf16 v[48:51], a[36:39], a[132:135], v[48:51]\n"
"global_load_dwordx4 a[88:91], v0, s[34:35] offset:2048 sc1\n"
"v_add_f32 v12, v12, v28\n"
"v_mfma_f32_16x16x32_bf16 v[76:79], a[44:47], a[132:135], v[76:79]\n"
"s_waitcnt lgkmcnt(0)\n"
"v_add_f32 v13, v13, v29\n"
"v_add_f32 v14, v14, v30\n"
"v_mfma_f32_16x16x32_bf16 v[76:79], a[40:43], a[128:131], v[76:79]\n"
"s_sub_u32 s30, s76, s30\n"
"v_add_f32 v15, v15, v31\n"
"v_mfma_f32_16x16x32_bf16 v[80:83], a[40:43], a[136:139], v[80:83]\n"
"s_cmp_lt_u32 s30, 620\n"
"s_cselect_b32 s99, s99, 0\n"
"v_accvgpr_write_b32 a28, v12\n"
"v_mfma_f32_16x16x32_bf16 v[80:83], a[44:47], a[140:143], v[80:83]\n"
"s_max_u32 s31, s31, s99\n"
"v_accvgpr_write_b32 a29, v13\n"
"v_accvgpr_write_b32 a30, v14\n"
"v_mfma_f32_16x16x32_bf16 v[52:55], a[36:39], a[140:143], v[52:55]\n"
"s_cmp_eq_u32 s31, 0\n"
"s_cselect_b32 s31, 10, s31\n"
"v_accvgpr_write_b32 a31, v15\n"
"v_mfma_f32_16x16x32_bf16 v[52:55], a[32:35], a[136:139], v[52:55]\n"
"s_cmp_eq_u32 s32, 0\n"
"s_cselect_b32 s32, 10, s32\n"
"v_mfma_f32_16x16x32_bf16 v[56:59], a[32:35], a[144:147], v[56:59]\n"
"s_sub_u32 s69, s32, s31\n"
"v_mfma_f32_16x16x32_bf16 v[56:59], a[36:39], a[148:151], v[56:59]\n"
"global_load_dwordx4 a[92:95], v0, s[34:35] offset:3072 sc1\n"
"v_mov_b32 v20, s69\n"
"v_mfma_f32_16x16x32_bf16 v[84:87], a[44:47], a[148:151], v[84:87]\n"
"ds_write_b32 v2, v20 offset:8192\n"
"v_mfma_f32_16x16x32_bf16 v[84:87], a[40:43], a[144:147], v[84:87]\n"
"v_mfma_f32_16x16x32_bf16 a[4:7], a[40:43], a[152:155], a[4:7]\n"
"s_add_u32 s34, s34, 8192\n"
"v_mfma_f32_16x16x32_bf16 a[4:7], a[44:47], a[156:159], a[4:7]\n"
"v_mfma_f32_16x16x32_bf16 a[0:3], a[36:39], a[156:159], a[0:3]\n"
"v_mfma_f32_16x16x32_bf16 a[0:3], a[32:35], a[152:155], a[0:3]\n"
"s_waitcnt lgkmcnt(0)\n"
"s_barrier\n"
"s_waitcnt vmcnt(8)\n"
"v_mfma_f32_16x16x32_bf16 v[88:91], a[48:51], a[96:99], v[88:91]\n"
"ds_read_b32 v21, v3 offset:8192\n"
"v_mfma_f32_16x16x32_bf16 v[88:91], a[52:55], a[100:103], v[88:91]\n"
"global_load_dwordx4 a[32:35], v0, s[34:35] offset:-4096 sc1\n"
"v_mfma_f32_16x16x32_bf16 v[116:119], a[60:63], a[100:103], v[116:119]\n"
"ds_read_b128 a[100:103], v5 offset:17408\n"
"v_mfma_f32_16x16x32_bf16 v[116:119], a[56:59], a[96:99], v[116:119]\n"
"ds_read_b128 a[96:99], v5 offset:16384\n"
"v_mfma_f32_16x16x32_bf16 v[120:123], a[56:59], a[104:107], v[120:123]\n"
"ds_read_b32 v22, v4 offset:8192\n"
"v_mfma_f32_16x16x32_bf16 v[120:123], a[60:63], a[108:111], v[120:123]\n"
"ds_read_b32 v23, v5 offset:8192\n"
"v_mfma_f32_16x16x32_bf16 v[92:95], a[52:55], a[108:111], v[92:95]\n"
"ds_read_b128 a[108:111], v5 offset:19456\n"
"v_mfma_f32_16x16x32_bf16 v[92:95], a[48:51], a[104:107], v[92:95]\n"
"ds_read_b128 a[104:107], v5 offset:18432\n"
"v_mfma_f32_16x16x32_bf16 v[96:99], a[48:51], a[112:115], v[96:99]\n"
"v_mfma_f32_16x16x32_bf16 v[96:99], a[52:55], a[116:119], v[96:99]\n"
"global_load_dwordx4 a[36:39], v0, s[34:35] offset:-3072 sc1\n"
"v_mfma_f32_16x16x32_bf16 v[124:127], a[60:63], a[116:119], v[124:127]\n"
"s_waitcnt lgkmcnt(2)\n"
"ds_read_b128 a[116:119], v5 offset:21504\n"
"v_add_u32 v20, v20, v21\n"
"v_mfma_f32_16x16x32_bf16 v[124:127], a[56:59], a[112:115], v[124:127]\n"
"ds_read_b128 a[112:115], v5 offset:20480\n"
"v_mfma_f32_16x16x32_bf16 v[128:131], a[56:59], a[120:123], v[128:131]\n"
"v_add3_u32 v20, v20, v22, v23\n"
"v_mfma_f32_16x16x32_bf16 v[128:131], a[60:63], a[124:127], v[128:131]\n"
"v_mfma_f32_16x16x32_bf16 v[100:103], a[52:55], a[124:127], v[100:103]\n"
"ds_read_b128 a[124:127], v5 offset:23552\n"
"v_readfirstlane_b32 s69, v20\n"
"v_mfma_f32_16x16x32_bf16 v[100:103], a[48:51], a[120:123], v[100:103]\n"
"ds_read_b128 a[120:123], v5 offset:22528\n"
"v_mfma_f32_16x16x32_bf16 v[104:107], a[48:51], a[128:131], v[104:107]\n"
"v_mfma_f32_16x16x32_bf16 v[104:107], a[52:55], a[132:135], v[104:107]\n"
"s_cmp_le_i32 s69, -2\n"
"global_load_dwordx4 a[40:43], v0, s[34:35] offset:-2048 sc1\n"
"s_cselect_b32 s66, 2, 1\n"
"v_mfma_f32_16x16x32_bf16 v[132:135], a[60:63], a[132:135], v[132:135]\n"
"ds_read_b128 a[132:135], v5 offset:25600\n"
"v_mfma_f32_16x16x32_bf16 v[132:135], a[56:59], a[128:131], v[132:135]\n"
"s_cmp_ge_i32 s69, 2\n"
"ds_read_b128 a[128:131], v5 offset:24576\n"
"s_cselect_b32 s66, 3, s66\n"
"v_mfma_f32_16x16x32_bf16 v[136:139], a[56:59], a[136:139], v[136:139]\n"
"ds_read_b128 v[16:19], v7 offset:32768\n"
"v_mfma_f32_16x16x32_bf16 v[136:139], a[60:63], a[140:143], v[136:139]\n"
"s_lshl_b32 s73, s66, 13\n"
"s_xor_b32 s60, s79, s66\n"
"v_mfma_f32_16x16x32_bf16 v[108:111], a[52:55], a[140:143], v[108:111]\n"
"ds_read_b128 a[140:143], v5 offset:27648\n"
"v_mfma_f32_16x16x32_bf16 v[108:111], a[48:51], a[136:139], v[108:111]\n"
"s_cmp_eq_u32 s66, 3\n"
"ds_read_b128 a[136:139], v5 offset:26624\n"
"s_cselect_b32 s72, 8192, 0\n"
"v_mfma_f32_16x16x32_bf16 v[112:115], a[48:51], a[144:147], v[112:115]\n"
"s_abs_i32 s31, s69\n"
"v_mfma_f32_16x16x32_bf16 v[112:115], a[52:55], a[148:151], v[112:115]\n"
"global_load_dwordx4 a[44:47], v0, s[34:35] offset:-1024 sc1\n"
"s_add_u32 s52, s56, %11\n"
"s_addc_u32 s53, s57, 0\n"
"v_mfma_f32_16x16x32_bf16 v[140:143], a[60:63], a[148:151], v[140:143]\n"
"s_cmp_lt_u32 s31, 26\n"
"s_cselect_b32 s31, 2, 0\n"
"ds_read_b128 a[148:151], v5 offset:29696\n"
"v_mfma_f32_16x16x32_bf16 v[140:143], a[56:59], a[144:147], v[140:143]\n"
"s_add_u32 s52, s52, 4096\n"
"ds_read_b128 a[144:147], v5 offset:28672\n"
"s_addc_u32 s53, s53, 0\n"
"v_mfma_f32_16x16x32_bf16 a[12:15], a[56:59], a[152:155], a[12:15]\n"
"v_mfma_f32_16x16x32_bf16 a[12:15], a[60:63], a[156:159], a[12:15]\n"
"s_add_u32 s54, s52, s73\n"
"s_addc_u32 s55, s53, 0\n"
"v_mfma_f32_16x16x32_bf16 a[8:11], a[52:55], a[156:159], a[8:11]\n"
"ds_read_b128 a[156:159], v5 offset:31744\n"
"v_mfma_f32_16x16x32_bf16 a[8:11], a[48:51], a[152:155], a[8:11]\n"
"s_add_u32 s52, s52, s72\n"
"ds_read_b128 a[152:155], v5 offset:30720\n"
"s_addc_u32 s53, s53, 0\n"
"L_P6:\n"
"s_waitcnt vmcnt(8) lgkmcnt(13)\n"
"v_mfma_f32_16x16x32_bf16 v[32:35], a[64:67], a[96:99], v[32:35]\n"
"v_mfma_f32_16x16x32_bf16 v[32:35], a[68:71], a[100:103], v[32:35]\n"
"global_load_dwordx4 a[48:51], v0, s[34:35] offset:0 sc1\n"
"v_mfma_f32_16x16x32_bf16 v[60:63], a[76:79], a[100:103], v[60:63]\n"
"v_mfma_f32_16x16x32_bf16 v[60:63], a[72:75], a[96:99], v[60:63]\n"
"v_mfma_f32_16x16x32_bf16 v[64:67], a[72:75], a[104:107], v[64:67]\n"
"v_mfma_f32_16x16x32_bf16 v[64:67], a[76:79], a[108:111], v[64:67]\n"
"v_mfma_f32_16x16x32_bf16 v[36:39], a[68:71], a[108:111], v[36:39]\n"
"s_waitcnt lgkmcnt(6)\n"
"v_lshlrev_b32 v12, 16, v16\n"
"v_mfma_f32_16x16x32_bf16 v[36:39], a[64:67], a[104:107], v[36:39]\n"
"v_and_b32 v13, s63, v16\n"
"v_lshlrev_b32 v14, 16, v17\n"
"v_mfma_f32_16x16x32_bf16 v[40:43], a[64:67], a[112:115], v[40:43]\n"
"v_and_b32 v15, s63, v17\n"
"v_mfma_f32_16x16x32_bf16 v[40:43], a[68:71], a[116:119], v[40:43]\n"
"global_load_dwordx4 a[52:55], v0, s[34:35] offset:1024 sc1\n"
"ds_read_b128 v[20:23], v7 offset:34816\n"
"v_mfma_f32_16x16x32_bf16 v[68:71], a[76:79], a[116:119], v[68:71]\n"
"v_mul_f32 v12, v12, v32\n"
"v_mul_f32 v13, v13, v33\n"
"v_mfma_f32_16x16x32_bf16 v[68:71], a[72:75], a[112:115], v[68:71]\n"
"v_mul_f32 v14, v14, v34\n"
"v_mfma_f32_16x16x32_bf16 v[72:75], a[72:75], a[120:123], v[72:75]\n"
"v_mul_f32 v15, v15, v35\n"
"v_mfma_f32_16x16x32_bf16 v[72:75], a[76:79], a[124:127], v[72:75]\n"
"v_cvt_pk_bf16_f32 v24, v12, v13\n"
"v_cvt_pk_bf16_f32 v25, v14, v15\n"
"v_mfma_f32_16x16x32_bf16 v[44:47], a[68:71], a[124:127], v[44:47]\n"
"ds_write_b64 v2, v[24:25] offset:0\n"
"v_mfma_f32_16x16x32_bf16 v[44:47], a[64:67], a[120:123], v[44:47]\n"
"s_waitcnt lgkmcnt(6)\n"
"v_lshlrev_b32 v12, 16, v18\n"
"v_mfma_f32_16x16x32_bf16 v[48:51], a[64:67], a[128:131], v[48:51]\n"
"v_and_b32 v13, s63, v18\n"
"v_lshlrev_b32 v14, 16, v19\n"
"v_mfma_f32_16x16x32_bf16 v[48:51], a[68:71], a[132:135], v[48:51]\n"
"global_load_dwordx4 a[56:59], v0, s[34:35] offset:2048 sc1\n"
"v_and_b32 v15, s63, v19\n"
"v_mfma_f32_16x16x32_bf16 v[76:79], a[76:79], a[132:135], v[76:79]\n"
"v_mul_f32 v12, v12, v60\n"
"v_mfma_f32_16x16x32_bf16 v[76:79], a[72:75], a[128:131], v[76:79]\n"
"v_mul_f32 v13, v13, v61\n"
"v_mul_f32 v14, v14, v62\n"
"v_mfma_f32_16x16x32_bf16 v[80:83], a[72:75], a[136:139], v[80:83]\n"
"v_mul_f32 v15, v15, v63\n"
"v_mfma_f32_16x16x32_bf16 v[80:83], a[76:79], a[140:143], v[80:83]\n"
"v_cvt_pk_bf16_f32 v26, v12, v13\n"
"v_cvt_pk_bf16_f32 v27, v14, v15\n"
"v_mfma_f32_16x16x32_bf16 v[52:55], a[68:71], a[140:143], v[52:55]\n"
"ds_write_b64 v2, v[26:27] offset:8\n"
"v_mfma_f32_16x16x32_bf16 v[52:55], a[64:67], a[136:139], v[52:55]\n"
"s_waitcnt lgkmcnt(2)\n"
"v_lshlrev_b32 v12, 16, v20\n"
"v_mfma_f32_16x16x32_bf16 v[56:59], a[64:67], a[144:147], v[56:59]\n"
"v_and_b32 v13, s63, v20\n"
"v_mfma_f32_16x16x32_bf16 v[56:59], a[68:71], a[148:151], v[56:59]\n"
"global_load_dwordx4 a[60:63], v0, s[34:35] offset:3072 sc1\n"
"v_lshlrev_b32 v14, 16, v21\n"
"v_mfma_f32_16x16x32_bf16 v[84:87], a[76:79], a[148:151], v[84:87]\n"
"v_and_b32 v15, s63, v21\n"
"v_xor_b32 v16, 64, v7\n"
"v_mfma_f32_16x16x32_bf16 v[84:87], a[72:75], a[144:147], v[84:87]\n"
"ds_read_b128 v[16:19], v16 offset:32768\n"
"v_mfma_f32_16x16x32_bf16 a[4:7], a[72:75], a[152:155], a[4:7]\n"
"s_add_u32 s34, s34, 8192\n"
"v_mul_f32 v12, v12, v36\n"
"v_mul_f32 v13, v13, v37\n"
"v_mfma_f32_16x16x32_bf16 a[4:7], a[76:79], a[156:159], a[4:7]\n"
"v_mul_f32 v14, v14, v38\n"
"v_mfma_f32_16x16x32_bf16 a[0:3], a[68:71], a[156:159], a[0:3]\n"
"v_mul_f32 v15, v15, v39\n"
"v_mfma_f32_16x16x32_bf16 a[0:3], a[64:67], a[152:155], a[0:3]\n"
"s_waitcnt vmcnt(8)\n"
"v_cvt_pk_bf16_f32 v28, v12, v13\n"
"v_cvt_pk_bf16_f32 v29, v14, v15\n"
"v_mfma_f32_16x16x32_bf16 v[88:91], a[80:83], a[96:99], v[88:91]\n"
"v_lshlrev_b32 v12, 16, v22\n"
"v_mfma_f32_16x16x32_bf16 v[88:91], a[84:87], a[100:103], v[88:91]\n"
"global_load_dwordx4 a[64:67], v0, s[34:35] offset:-4096 sc1\n"
"v_and_b32 v13, s63, v22\n"
"v_mfma_f32_16x16x32_bf16 v[116:119], a[92:95], a[100:103], v[116:119]\n"
"ds_read_b128 a[100:103], v2 offset:17408\n"
"v_lshlrev_b32 v14, 16, v23\n"
"v_mfma_f32_16x16x32_bf16 v[116:119], a[88:91], a[96:99], v[116:119]\n"
"ds_read_b128 a[96:99], v2 offset:16384\n"
"v_and_b32 v15, s63, v23\n"
"v_mfma_f32_16x16x32_bf16 v[120:123], a[88:91], a[104:107], v[120:123]\n"
"v_mul_f32 v12, v12, v64\n"
"v_mul_f32 v13, v13, v65\n"
"v_mfma_f32_16x16x32_bf16 v[120:123], a[92:95], a[108:111], v[120:123]\n"
"v_mul_f32 v14, v14, v66\n"
"v_mul_f32 v15, v15, v67\n"
"v_mfma_f32_16x16x32_bf16 v[92:95], a[84:87], a[108:111], v[92:95]\n"
"ds_read_b128 a[108:111], v2 offset:19456\n"
"v_cvt_pk_bf16_f32 v30, v12, v13\n"
"v_mfma_f32_16x16x32_bf16 v[92:95], a[80:83], a[104:107], v[92:95]\n"
"ds_read_b128 a[104:107], v2 offset:18432\n"
"v_cvt_pk_bf16_f32 v31, v14, v15\n"
"v_mfma_f32_16x16x32_bf16 v[96:99], a[80:83], a[112:115], v[96:99]\n"
"global_store_dwordx4 v0, v[28:31], s[52:53] offset:-2048 sc1\n"
"ds_write_b128 v2, v[28:31] offset:2048\n"
"v_mfma_f32_16x16x32_bf16 v[96:99], a[84:87], a[116:119], v[96:99]\n"
"s_waitcnt lgkmcnt(5)\n"
"global_load_dwordx4 a[68:71], v0, s[34:35] offset:-3072 sc1\n"
"v_lshlrev_b32 v12, 16, v16\n"
"v_mfma_f32_16x16x32_bf16 v[124:127], a[92:95], a[116:119], v[124:127]\n"
"ds_read_b128 a[116:119], v2 offset:21504\n"
"v_and_b32 v13, s63, v16\n"
"v_mfma_f32_16x16x32_bf16 v[124:127], a[88:91], a[112:115], v[124:127]\n"
"ds_read_b128 a[112:115], v2 offset:20480\n"
"v_lshlrev_b32 v14, 16, v17\n"
"v_mfma_f32_16x16x32_bf16 v[128:131], a[88:91], a[120:123], v[128:131]\n"
"v_and_b32 v15, s63, v17\n"
"v_xor_b32 v20, 64, v7\n"
"v_mfma_f32_16x16x32_bf16 v[128:131], a[92:95], a[124:127], v[128:131]\n"
"ds_read_b128 v[20:23], v20 offset:34816\n"
"v_mul_f32 v12, v12, v88\n"
"v_mfma_f32_16x16x32_bf16 v[100:103], a[84:87], a[124:127], v[100:103]\n"
"ds_read_b128 a[124:127], v2 offset:23552\n"
"v_mul_f32 v13, v13, v89\n"
"v_mfma_f32_16x16x32_bf16 v[100:103], a[80:83], a[120:123], v[100:103]\n"
"ds_read_b128 a[120:123], v2 offset:22528\n"
"v_mul_f32 v14, v14, v90\n"
"v_mfma_f32_16x16x32_bf16 v[104:107], a[80:83], a[128:131], v[104:107]\n"
"v_mul_f32 v15, v15, v91\n"
"v_cvt_pk_bf16_f32 v24, v12, v13\n"
"v_mfma_f32_16x16x32_bf16 v[104:107], a[84:87], a[132:135], v[104:107]\n"
"global_load_dwordx4 a[72:75], v0, s[34:35] offset:-2048 sc1\n"
"v_cvt_pk_bf16_f32 v25, v14, v15\n"
"v_mfma_f32_16x16x32_bf16 v[132:135], a[92:95], a[132:135], v[132:135]\n"
"ds_read_b128 a[132:135], v2 offset:25600\n"
"v_lshlrev_b32 v12, 16, v18\n"
"v_mfma_f32_16x16x32_bf16 v[132:135], a[88:91], a[128:131], v[132:135]\n"
"ds_read_b128 a[128:131], v2 offset:24576\n"
"v_and_b32 v13, s63, v18\n"
"v_mfma_f32_16x16x32_bf16 v[136:139], a[88:91], a[136:139], v[136:139]\n"
"v_lshlrev_b32 v14, 16, v19\n"
"v_and_b32 v15, s63, v19\n"
"v_mfma_f32_16x16x32_bf16 v[136:139], a[92:95], a[140:143], v[136:139]\n"
"v_mul_f32 v12, v12, v116\n"
"v_mfma_f32_16x16x32_bf16 v[108:111], a[84:87], a[140:143], v[108:111]\n"
"ds_read_b128 a[140:143], v2 offset:27648\n"
"v_mul_f32 v13, v13, v117\n"
"v_mfma_f32_16x16x32_bf16 v[108:111], a[80:83], a[136:139], v[108:111]\n"
"ds_read_b128 a[136:139], v2 offset:26624\n"
"v_mul_f32 v14, v14, v118\n"
"v_mfma_f32_16x16x32_bf16 v[112:115], a[80:83], a[144:147], v[112:115]\n"
"v_mul_f32 v15, v15, v119\n"
"v_cvt_pk_bf16_f32 v26, v12, v13\n"
"v_mfma_f32_16x16x32_bf16 v[112:115], a[84:87], a[148:151], v[112:115]\n"
"global_load_dwordx4 a[76:79], v0, s[34:35] offset:-1024 sc1\n"
"v_cvt_pk_bf16_f32 v27, v14, v15\n"
"v_mfma_f32_16x16x32_bf16 v[140:143], a[92:95], a[148:151], v[140:143]\n"
"ds_read_b128 a[148:151], v2 offset:29696\n"
"v_mfma_f32_16x16x32_bf16 v[140:143], a[88:91], a[144:147], v[140:143]\n"
"ds_read_b128 a[144:147], v2 offset:28672\n"
"v_mfma_f32_16x16x32_bf16 a[12:15], a[88:91], a[152:155], a[12:15]\n"
"global_store_dwordx4 v0, v[24:27], s[52:53] offset:-3072 sc1\n"
"ds_write_b128 v2, v[24:27] offset:1024\n"
"v_mfma_f32_16x16x32_bf16 a[12:15], a[92:95], a[156:159], a[12:15]\n"
"s_waitcnt lgkmcnt(9)\n"
"v_lshlrev_b32 v12, 16, v20\n"
"v_and_b32 v13, s63, v20\n"
"v_mfma_f32_16x16x32_bf16 a[8:11], a[84:87], a[156:159], a[8:11]\n"
"ds_read_b128 a[156:159], v2 offset:31744\n"
"v_lshlrev_b32 v14, 16, v21\n"
"v_mfma_f32_16x16x32_bf16 a[8:11], a[80:83], a[152:155], a[8:11]\n"
"s_waitcnt vmcnt(10)\n"
"ds_read_b128 a[152:155], v2 offset:30720\n"
"v_and_b32 v15, s63, v21\n"
"v_mfma_f32_16x16x32_bf16 v[144:147], a[32:35], a[96:99], v[144:147]\n"
"ds_read_b128 v[16:19], v7 offset:36864\n"
"v_mul_f32 v12, v12, v92\n"
"v_mfma_f32_16x16x32_bf16 v[144:147], a[36:39], a[100:103], v[144:147]\n"
"global_load_dwordx4 a[80:83], v0, s[34:35] offset:0 sc1\n"
"v_mul_f32 v13, v13, v93\n"
"v_mfma_f32_16x16x32_bf16 v[172:175], a[44:47], a[100:103], v[172:175]\n"
"v_mul_f32 v14, v14, v94\n"
"v_mul_f32 v15, v15, v95\n"
"v_mfma_f32_16x16x32_bf16 v[172:175], a[40:43], a[96:99], v[172:175]\n"
"v_cvt_pk_bf16_f32 v28, v12, v13\n"
"v_cvt_pk_bf16_f32 v29, v14, v15\n"
"v_mfma_f32_16x16x32_bf16 v[176:179], a[40:43], a[104:107], v[176:179]\n"
"ds_write_b64 v2, v[28:29] offset:3072\n"
"v_mfma_f32_16x16x32_bf16 v[176:179], a[44:47], a[108:111], v[176:179]\n"
"v_lshlrev_b32 v12, 16, v22\n"
"v_and_b32 v13, s63, v22\n"
"v_mfma_f32_16x16x32_bf16 v[148:151], a[36:39], a[108:111], v[148:151]\n"
"v_lshlrev_b32 v14, 16, v23\n"
"v_mfma_f32_16x16x32_bf16 v[148:151], a[32:35], a[104:107], v[148:151]\n"
"s_waitcnt lgkmcnt(11)\n"
"v_and_b32 v15, s63, v23\n"
"v_mul_f32 v12, v12, v120\n"
"v_mfma_f32_16x16x32_bf16 v[152:155], a[32:35], a[112:115], v[152:155]\n"
"v_mul_f32 v13, v13, v121\n"
"v_mfma_f32_16x16x32_bf16 v[152:155], a[36:39], a[116:119], v[152:155]\n"
"global_load_dwordx4 a[84:87], v0, s[34:35] offset:1024 sc1\n"
"v_mul_f32 v14, v14, v122\n"
"v_mfma_f32_16x16x32_bf16 v[180:183], a[44:47], a[116:119], v[180:183]\n"
"v_mul_f32 v15, v15, v123\n"
"v_cvt_pk_bf16_f32 v30, v12, v13\n"
"v_mfma_f32_16x16x32_bf16 v[180:183], a[40:43], a[112:115], v[180:183]\n"
"v_cvt_pk_bf16_f32 v31, v14, v15\n"
"v_mfma_f32_16x16x32_bf16 v[184:187], a[40:43], a[120:123], v[184:187]\n"
"ds_write_b64 v2, v[30:31] offset:3080\n"
"v_mfma_f32_16x16x32_bf16 v[184:187], a[44:47], a[124:127], v[184:187]\n"
"s_waitcnt lgkmcnt(2)\n"
"v_lshlrev_b32 v12, 16, v16\n"
"v_and_b32 v13, s63, v16\n"
"v_mfma_f32_16x16x32_bf16 v[156:159], a[36:39], a[124:127], v[156:159]\n"
"v_lshlrev_b32 v14, 16, v17\n"
"v_mfma_f32_16x16x32_bf16 v[156:159], a[32:35], a[120:123], v[156:159]\n"
"v_and_b32 v15, s63, v17\n"
"v_xor_b32 v20, 64, v7\n"
"v_mfma_f32_16x16x32_bf16 v[160:163], a[32:35], a[128:131], v[160:163]\n"
"ds_read_b128 v[20:23], v20 offset:36864\n"
"v_mfma_f32_16x16x32_bf16 v[160:163], a[36:39], a[132:135], v[160:163]\n"
"global_load_dwordx4 a[88:91], v0, s[34:35] offset:2048 sc1\n"
"v_mul_f32 v12, v12, v40\n"
"v_mfma_f32_16x16x32_bf16 v[188:191], a[44:47], a[132:135], v[188:191]\n"
"v_mul_f32 v13, v13, v41\n"
"v_mfma_f32_16x16x32_bf16 v[188:191], a[40:43], a[128:131], v[188:191]\n"
"v_mul_f32 v14, v14, v42\n"
"v_mul_f32 v15, v15, v43\n"
"v_mfma_f32_16x16x32_bf16 v[192:195], a[40:43], a[136:139], v[192:195]\n"
"v_cvt_pk_bf16_f32 v24, v12, v13\n"
"v_mfma_f32_16x16x32_bf16 v[192:195], a[44:47], a[140:143], v[192:195]\n"
"v_cvt_pk_bf16_f32 v25, v14, v15\n"
"v_mfma_f32_16x16x32_bf16 v[164:167], a[36:39], a[140:143], v[164:167]\n"
"v_lshlrev_b32 v12, 16, v18\n"
"v_and_b32 v13, s63, v18\n"
"v_mfma_f32_16x16x32_bf16 v[164:167], a[32:35], a[136:139], v[164:167]\n"
"v_lshlrev_b32 v14, 16, v19\n"
"v_mfma_f32_16x16x32_bf16 v[168:171], a[32:35], a[144:147], v[168:171]\n"
"v_and_b32 v15, s63, v19\n"
"v_mul_f32 v12, v12, v68\n"
"v_mfma_f32_16x16x32_bf16 v[168:171], a[36:39], a[148:151], v[168:171]\n"
"global_load_dwordx4 a[92:95], v0, s[34:35] offset:3072 sc1\n"
"v_mul_f32 v13, v13, v69\n"
"v_mfma_f32_16x16x32_bf16 v[196:199], a[44:47], a[148:151], v[196:199]\n"
"v_mul_f32 v14, v14, v70\n"
"v_mfma_f32_16x16x32_bf16 v[196:199], a[40:43], a[144:147], v[196:199]\n"
"v_mul_f32 v15, v15, v71\n"
"v_mfma_f32_16x16x32_bf16 a[20:23], a[40:43], a[152:155], a[20:23]\n"
"s_add_u32 s34, s34, 8192\n"
"v_cvt_pk_bf16_f32 v26, v12, v13\n"
"v_cvt_pk_bf16_f32 v27, v14, v15\n"
"v_mfma_f32_16x16x32_bf16 a[20:23], a[44:47], a[156:159], a[20:23]\n"
"global_store_dwordx4 v0, v[24:27], s[52:53] offset:0 sc1\n"
"ds_write_b128 v2, v[24:27] offset:4096\n"
"v_mfma_f32_16x16x32_bf16 a[16:19], a[36:39], a[156:159], a[16:19]\n"
"s_waitcnt lgkmcnt(1)\n"
"v_lshlrev_b32 v12, 16, v20\n"
"v_mfma_f32_16x16x32_bf16 a[16:19], a[32:35], a[152:155], a[16:19]\n"
"s_waitcnt vmcnt(11)\n"
"v_and_b32 v13, s63, v20\n"
"v_mfma_f32_16x16x32_bf16 v[200:203], a[48:51], a[96:99], v[200:203]\n"
"v_lshlrev_b32 v14, 16, v21\n"
"v_and_b32 v15, s63, v21\n"
"v_mfma_f32_16x16x32_bf16 v[200:203], a[52:55], a[100:103], v[200:203]\n"
"global_load_dwordx4 a[32:35], v0, s[34:35] offset:-4096 sc1\n"
"ds_read_b128 v[16:19], v7 offset:38912\n"
"v_mfma_f32_16x16x32_bf16 v[228:231], a[60:63], a[100:103], v[228:231]\n"
"ds_read_b128 a[100:103], v3 offset:17408\n"
"v_mul_f32 v12, v12, v96\n"
"v_mfma_f32_16x16x32_bf16 v[228:231], a[56:59], a[96:99], v[228:231]\n"
"ds_read_b128 a[96:99], v3 offset:16384\n"
"v_mul_f32 v13, v13, v97\n"
"v_mfma_f32_16x16x32_bf16 v[232:235], a[56:59], a[104:107], v[232:235]\n"
"v_mul_f32 v14, v14, v98\n"
"v_mfma_f32_16x16x32_bf16 v[232:235], a[60:63], a[108:111], v[232:235]\n"
"v_mul_f32 v15, v15, v99\n"
"v_cvt_pk_bf16_f32 v28, v12, v13\n"
"v_mfma_f32_16x16x32_bf16 v[204:207], a[52:55], a[108:111], v[204:207]\n"
"ds_read_b128 a[108:111], v3 offset:19456\n"
"v_cvt_pk_bf16_f32 v29, v14, v15\n"
"v_mfma_f32_16x16x32_bf16 v[204:207], a[48:51], a[104:107], v[204:207]\n"
"ds_read_b128 a[104:107], v3 offset:18432\n"
"v_lshlrev_b32 v12, 16, v22\n"
"v_mfma_f32_16x16x32_bf16 v[208:211], a[48:51], a[112:115], v[208:211]\n"
"v_and_b32 v13, s63, v22\n"
"v_lshlrev_b32 v14, 16, v23\n"
"v_mfma_f32_16x16x32_bf16 v[208:211], a[52:55], a[116:119], v[208:211]\n"
"global_load_dwordx4 a[36:39], v0, s[34:35] offset:-3072 sc1\n"
"v_and_b32 v15, s63, v23\n"
"v_mfma_f32_16x16x32_bf16 v[236:239], a[60:63], a[116:119], v[236:239]\n"
"ds_read_b128 a[116:119], v3 offset:21504\n"
"v_mul_f32 v12, v12, v124\n"
"v_mfma_f32_16x16x32_bf16 v[236:239], a[56:59], a[112:115], v[236:239]\n"
"ds_read_b128 a[112:115], v3 offset:20480\n"
"v_mul_f32 v13, v13, v125\n"
"v_mfma_f32_16x16x32_bf16 v[240:243], a[56:59], a[120:123], v[240:243]\n"
"v_mul_f32 v14, v14, v126\n"
"v_mul_f32 v15, v15, v127\n"
"v_mfma_f32_16x16x32_bf16 v[240:243], a[60:63], a[124:127], v[240:243]\n"
"v_cvt_pk_bf16_f32 v30, v12, v13\n"
"v_mfma_f32_16x16x32_bf16 v[212:215], a[52:55], a[124:127], v[212:215]\n"
"ds_read_b128 a[124:127], v3 offset:23552\n"
"v_cvt_pk_bf16_f32 v31, v14, v15\n"
"v_mfma_f32_16x16x32_bf16 v[212:215], a[48:51], a[120:123], v[212:215]\n"
"ds_read_b128 a[120:123], v3 offset:22528\n"
"v_mfma_f32_16x16x32_bf16 v[216:219], a[48:51], a[128:131], v[216:219]\n"
"global_store_dwordx4 v0, v[28:31], s[52:53] offset:1024 sc1\n"
"ds_write_b128 v2, v[28:31] offset:5120\n"
"v_mfma_f32_16x16x32_bf16 v[216:219], a[52:55], a[132:135], v[216:219]\n"
"s_waitcnt lgkmcnt(9)\n"
"global_load_dwordx4 a[40:43], v0, s[34:35] offset:-2048 sc1\n"
"v_lshlrev_b32 v12, 16, v16\n"
"v_mfma_f32_16x16x32_bf16 v[244:247], a[60:63], a[132:135], v[244:247]\n"
"ds_read_b128 a[132:135], v3 offset:25600\n"
"v_and_b32 v13, s63, v16\n"
"v_mfma_f32_16x16x32_bf16 v[244:247], a[56:59], a[128:131], v[244:247]\n"
"ds_read_b128 a[128:131], v3 offset:24576\n"
"v_lshlrev_b32 v14, 16, v17\n"
"v_mfma_f32_16x16x32_bf16 v[248:251], a[56:59], a[136:139], v[248:251]\n"
"v_and_b32 v15, s63, v17\n"
"v_xor_b32 v20, 64, v7\n"
"v_mfma_f32_16x16x32_bf16 v[248:251], a[60:63], a[140:143], v[248:251]\n"
"ds_read_b128 v[20:23], v20 offset:38912\n"
"v_mul_f32 v12, v12, v44\n"
"v_mfma_f32_16x16x32_bf16 v[220:223], a[52:55], a[140:143], v[220:223]\n"
"ds_read_b128 a[140:143], v3 offset:27648\n"
"v_mul_f32 v13, v13, v45\n"
"v_mfma_f32_16x16x32_bf16 v[220:223], a[48:51], a[136:139], v[220:223]\n"
"ds_read_b128 a[136:139], v3 offset:26624\n"
"v_mul_f32 v14, v14, v46\n"
"v_mfma_f32_16x16x32_bf16 v[224:227], a[48:51], a[144:147], v[224:227]\n"
"v_mul_f32 v15, v15, v47\n"
"v_cvt_pk_bf16_f32 v24, v12, v13\n"
"v_mfma_f32_16x16x32_bf16 v[224:227], a[52:55], a[148:151], v[224:227]\n"
"global_load_dwordx4 a[44:47], v0, s[34:35] offset:-1024 sc1\n"
"v_cvt_pk_bf16_f32 v25, v14, v15\n"
"v_mfma_f32_16x16x32_bf16 v[252:255], a[60:63], a[148:151], v[252:255]\n"
"ds_read_b128 a[148:151], v3 offset:29696\n"
"v_mfma_f32_16x16x32_bf16 v[252:255], a[56:59], a[144:147], v[252:255]\n"
"ds_read_b128 a[144:147], v3 offset:28672\n"
"v_mfma_f32_16x16x32_bf16 a[28:31], a[56:59], a[152:155], a[28:31]\n"
"ds_write_b64 v2, v[24:25] offset:6144\n"
"v_mfma_f32_16x16x32_bf16 a[28:31], a[60:63], a[156:159], a[28:31]\n"
"v_lshlrev_b32 v12, 16, v18\n"
"v_and_b32 v13, s63, v18\n"
"v_mfma_f32_16x16x32_bf16 a[24:27], a[52:55], a[156:159], a[24:27]\n"
"ds_read_b128 a[156:159], v3 offset:31744\n"
"v_lshlrev_b32 v14, 16, v19\n"
"v_mfma_f32_16x16x32_bf16 a[24:27], a[48:51], a[152:155], a[24:27]\n"
"s_waitcnt vmcnt(11) lgkmcnt(14)\n"
"ds_read_b128 a[152:155], v3 offset:30720\n"
"v_and_b32 v15, s63, v19\n"
"v_mfma_f32_16x16x32_bf16 v[144:147], a[64:67], a[96:99], v[144:147]\n"
"v_mul_f32 v12, v12, v72\n"
"v_mul_f32 v13, v13, v73\n"
"v_mfma_f32_16x16x32_bf16 v[144:147], a[68:71], a[100:103], v[144:147]\n"
"global_load_dwordx4 a[48:51], v0, s[34:35] offset:0 sc1\n"
"v_mul_f32 v14, v14, v74\n"
"v_mfma_f32_16x16x32_bf16 v[172:175], a[76:79], a[100:103], v[172:175]\n"
"v_mul_f32 v15, v15, v75\n"
"v_cvt_pk_bf16_f32 v26, v12, v13\n"
"v_mfma_f32_16x16x32_bf16 v[172:175], a[72:75], a[96:99], v[172:175]\n"
"v_cvt_pk_bf16_f32 v27, v14, v15\n"
"v_mfma_f32_16x16x32_bf16 v[176:179], a[72:75], a[104:107], v[176:179]\n"
"ds_write_b64 v2, v[26:27] offset:6152\n"
"v_mfma_f32_16x16x32_bf16 v[176:179], a[76:79], a[108:111], v[176:179]\n"
"s_waitcnt lgkmcnt(8)\n"
"v_lshlrev_b32 v12, 16, v20\n"
"v_and_b32 v13, s63, v20\n"
"v_mfma_f32_16x16x32_bf16 v[148:151], a[68:71], a[108:111], v[148:151]\n"
"v_lshlrev_b32 v14, 16, v21\n"
"v_and_b32 v15, s63, v21\n"
"v_mfma_f32_16x16x32_bf16 v[148:151], a[64:67], a[104:107], v[148:151]\n"
"v_mul_f32 v12, v12, v100\n"
"v_mul_f32 v13, v13, v101\n"
"v_mfma_f32_16x16x32_bf16 v[152:155], a[64:67], a[112:115], v[152:155]\n"
"v_mul_f32 v14, v14, v102\n"
"v_mul_f32 v15, v15, v103\n"
"v_mfma_f32_16x16x32_bf16 v[152:155], a[68:71], a[116:119], v[152:155]\n"
"global_load_dwordx4 a[52:55], v0, s[34:35] offset:1024 sc1\n"
"v_cvt_pk_bf16_f32 v28, v12, v13\n"
"v_mfma_f32_16x16x32_bf16 v[180:183], a[76:79], a[116:119], v[180:183]\n"
"v_cvt_pk_bf16_f32 v29, v14, v15\n"
"v_lshlrev_b32 v12, 16, v22\n"
"v_mfma_f32_16x16x32_bf16 v[180:183], a[72:75], a[112:115], v[180:183]\n"
"v_and_b32 v13, s63, v22\n"
"v_lshlrev_b32 v14, 16, v23\n"
"v_mfma_f32_16x16x32_bf16 v[184:187], a[72:75], a[120:123], v[184:187]\n"
"v_and_b32 v15, s63, v23\n"
"v_mul_f32 v12, v12, v128\n"
"v_mfma_f32_16x16x32_bf16 v[184:187], a[76:79], a[124:127], v[184:187]\n"
"v_mul_f32 v13, v13, v129\n"
"v_mfma_f32_16x16x32_bf16 v[156:159], a[68:71], a[124:127], v[156:159]\n"
"v_mul_f32 v14, v14, v130\n"
"v_mfma_f32_16x16x32_bf16 v[156:159], a[64:67], a[120:123], v[156:159]\n"
"s_waitcnt lgkmcnt(6)\n"
"v_mul_f32 v15, v15, v131\n"
"v_cvt_pk_bf16_f32 v30, v12, v13\n"
"v_mfma_f32_16x16x32_bf16 v[160:163], a[64:67], a[128:131], v[160:163]\n"
"v_cvt_pk_bf16_f32 v31, v14, v15\n"
"v_mfma_f32_16x16x32_bf16 v[160:163], a[68:71], a[132:135], v[160:163]\n"
"global_load_dwordx4 a[56:59], v0, s[34:35] offset:2048 sc1\n"
"v_mfma_f32_16x16x32_bf16 v[188:191], a[76:79], a[132:135], v[188:191]\n"
"global_store_dwordx4 v0, v[28:31], s[52:53] offset:3072 sc1\n"
"ds_write_b128 v2, v[28:31] offset:7168\n"
"v_mfma_f32_16x16x32_bf16 v[188:191], a[72:75], a[128:131], v[188:191]\n"
"v_accvgpr_read_b32 v16, a160\n"
"v_accvgpr_read_b32 v17, a161\n"
"v_mfma_f32_16x16x32_bf16 v[192:195], a[72:75], a[136:139], v[192:195]\n"
"v_accvgpr_read_b32 v18, a162\n"
"v_mfma_f32_16x16x32_bf16 v[192:195], a[76:79], a[140:143], v[192:195]\n"
"v_accvgpr_read_b32 v19, a163\n"
"v_lshlrev_b32 v12, 16, v16\n"
"v_mfma_f32_16x16x32_bf16 v[164:167], a[68:71], a[140:143], v[164:167]\n"
"v_and_b32 v13, s63, v16\n"
"v_mfma_f32_16x16x32_bf16 v[164:167], a[64:67], a[136:139], v[164:167]\n"
"s_waitcnt lgkmcnt(2)\n"
"v_lshlrev_b32 v14, 16, v17\n"
"v_mfma_f32_16x16x32_bf16 v[168:171], a[64:67], a[144:147], v[168:171]\n"
"v_and_b32 v15, s63, v17\n"
"v_mul_f32 v12, v12, v48\n"
"v_mfma_f32_16x16x32_bf16 v[168:171], a[68:71], a[148:151], v[168:171]\n"
"global_load_dwordx4 a[60:63], v0, s[34:35] offset:3072 sc1\n"
"v_mul_f32 v13, v13, v49\n"
"v_mfma_f32_16x16x32_bf16 v[196:199], a[76:79], a[148:151], v[196:199]\n"
"v_mul_f32 v14, v14, v50\n"
"v_mfma_f32_16x16x32_bf16 v[196:199], a[72:75], a[144:147], v[196:199]\n"
"v_mul_f32 v15, v15, v51\n"
"v_cvt_pk_bf16_f32 v24, v12, v13\n"
"v_mfma_f32_16x16x32_bf16 a[20:23], a[72:75], a[152:155], a[20:23]\n"
"s_add_u32 s34, s34, 8192\n"
"v_cvt_pk_bf16_f32 v25, v14, v15\n"
"v_mfma_f32_16x16x32_bf16 a[20:23], a[76:79], a[156:159], a[20:23]\n"
"v_lshlrev_b32 v12, 16, v18\n"
"v_mfma_f32_16x16x32_bf16 a[16:19], a[68:71], a[156:159], a[16:19]\n"
"v_and_b32 v13, s63, v18\n"
"v_lshlrev_b32 v14, 16, v19\n"
"v_mfma_f32_16x16x32_bf16 a[16:19], a[64:67], a[152:155], a[16:19]\n"
"s_waitcnt vmcnt(11)\n"
"v_and_b32 v15, s63, v19\n"
"v_mul_f32 v12, v12, v76\n"
"v_mfma_f32_16x16x32_bf16 v[200:203], a[80:83], a[96:99], v[200:203]\n"
"v_mul_f32 v13, v13, v77\n"
"v_mul_f32 v14, v14, v78\n"
"v_mfma_f32_16x16x32_bf16 v[200:203], a[84:87], a[100:103], v[200:203]\n"
"global_load_dwordx4 a[64:67], v0, s[34:35] offset:-4096 sc1\n"
"v_mul_f32 v15, v15, v79\n"
"v_mfma_f32_16x16x32_bf16 v[228:231], a[92:95], a[100:103], v[228:231]\n"
"ds_read_b128 a[100:103], v4 offset:17408\n"
"v_cvt_pk_bf16_f32 v26, v12, v13\n"
"v_mfma_f32_16x16x32_bf16 v[228:231], a[88:91], a[96:99], v[228:231]\n"
"ds_read_b128 a[96:99], v4 offset:16384\n"
"v_cvt_pk_bf16_f32 v27, v14, v15\n"
"v_mfma_f32_16x16x32_bf16 v[232:235], a[88:91], a[104:107], v[232:235]\n"
"global_store_dwordx4 v0, v[24:27], s[54:55] offset:-4096 sc1\n"
"ds_write_b128 v2, v[24:27] offset:8192\n"
"v_mfma_f32_16x16x32_bf16 v[232:235], a[92:95], a[108:111], v[232:235]\n"
"v_accvgpr_read_b32 v16, a164\n"
"v_mfma_f32_16x16x32_bf16 v[204:207], a[84:87], a[108:111], v[204:207]\n"
"ds_read_b128 a[108:111], v4 offset:19456\n"
"v_accvgpr_read_b32 v17, a165\n"
"v_mfma_f32_16x16x32_bf16 v[204:207], a[80:83], a[104:107], v[204:207]\n"
"ds_read_b128 a[104:107], v4 offset:18432\n"
"v_accvgpr_read_b32 v18, a166\n"
"v_mfma_f32_16x16x32_bf16 v[208:211], a[80:83], a[112:115], v[208:211]\n"
"v_accvgpr_read_b32 v19, a167\n"
"v_lshlrev_b32 v12, 16, v16\n"
"v_mfma_f32_16x16x32_bf16 v[208:211], a[84:87], a[116:119], v[208:211]\n"
"global_load_dwordx4 a[68:71], v0, s[34:35] offset:-3072 sc1\n"
"v_and_b32 v13, s63, v16\n"
"v_mfma_f32_16x16x32_bf16 v[236:239], a[92:95], a[116:119], v[236:239]\n"
"ds_read_b128 a[116:119], v4 offset:21504\n"
"v_lshlrev_b32 v14, 16, v17\n"
"v_mfma_f32_16x16x32_bf16 v[236:239], a[88:91], a[112:115], v[236:239]\n"
"ds_read_b128 a[112:115], v4 offset:20480\n"
"v_and_b32 v15, s63, v17\n"
"v_mfma_f32_16x16x32_bf16 v[240:243], a[88:91], a[120:123], v[240:243]\n"
"v_mul_f32 v12, v12, v104\n"
"v_mul_f32 v13, v13, v105\n"
"v_mfma_f32_16x16x32_bf16 v[240:243], a[92:95], a[124:127], v[240:243]\n"
"v_mul_f32 v14, v14, v106\n"
"v_mul_f32 v15, v15, v107\n"
"v_mfma_f32_16x16x32_bf16 v[212:215], a[84:87], a[124:127], v[212:215]\n"
"ds_read_b128 a[124:127], v4 offset:23552\n"
"v_cvt_pk_bf16_f32 v28, v12, v13\n"
"v_mfma_f32_16x16x32_bf16 v[212:215], a[80:83], a[120:123], v[212:215]\n"
"ds_read_b128 a[120:123], v4 offset:22528\n"
"v_cvt_pk_bf16_f32 v29, v14, v15\n"
"v_mfma_f32_16x16x32_bf16 v[216:219], a[80:83], a[128:131], v[216:219]\n"
"ds_write_b64 v2, v[28:29] offset:9216\n"
"v_mfma_f32_16x16x32_bf16 v[216:219], a[84:87], a[132:135], v[216:219]\n"
"global_load_dwordx4 a[72:75], v0, s[34:35] offset:-2048 sc1\n"
"v_lshlrev_b32 v12, 16, v18\n"
"v_mfma_f32_16x16x32_bf16 v[244:247], a[92:95], a[132:135], v[244:247]\n"
"ds_read_b128 a[132:135], v4 offset:25600\n"
"v_and_b32 v13, s63, v18\n"
"v_mfma_f32_16x16x32_bf16 v[244:247], a[88:91], a[128:131], v[244:247]\n"
"ds_read_b128 a[128:131], v4 offset:24576\n"
"v_lshlrev_b32 v14, 16, v19\n"
"v_mfma_f32_16x16x32_bf16 v[248:251], a[88:91], a[136:139], v[248:251]\n"
"v_and_b32 v15, s63, v19\n"
"v_mul_f32 v12, v12, v132\n"
"v_mfma_f32_16x16x32_bf16 v[248:251], a[92:95], a[140:143], v[248:251]\n"
"v_mul_f32 v13, v13, v133\n"
"v_mfma_f32_16x16x32_bf16 v[220:223], a[84:87], a[140:143], v[220:223]\n"
"ds_read_b128 a[140:143], v4 offset:27648\n"
"v_mul_f32 v14, v14, v134\n"
"v_mfma_f32_16x16x32_bf16 v[220:223], a[80:83], a[136:139], v[220:223]\n"
"ds_read_b128 a[136:139], v4 offset:26624\n"
"v_mul_f32 v15, v15, v135\n"
"v_mfma_f32_16x16x32_bf16 v[224:227], a[80:83], a[144:147], v[224:227]\n"
"v_cvt_pk_bf16_f32 v30, v12, v13\n"
"v_cvt_pk_bf16_f32 v31, v14, v15\n"
"v_mfma_f32_16x16x32_bf16 v[224:227], a[84:87], a[148:151], v[224:227]\n"
"global_load_dwordx4 a[76:79], v0, s[34:35] offset:-1024 sc1\n"
"v_mfma_f32_16x16x32_bf16 v[252:255], a[92:95], a[148:151], v[252:255]\n"
"ds_read_b128 a[148:151], v4 offset:29696\n"
"v_mfma_f32_16x16x32_bf16 v[252:255], a[88:91], a[144:147], v[252:255]\n"
"ds_read_b128 a[144:147], v4 offset:28672\n"
"v_mfma_f32_16x16x32_bf16 a[28:31], a[88:91], a[152:155], a[28:31]\n"
"ds_write_b64 v2, v[30:31] offset:9224\n"
"v_mfma_f32_16x16x32_bf16 a[28:31], a[92:95], a[156:159], a[28:31]\n"
"v_accvgpr_read_b32 v16, a168\n"
"v_accvgpr_read_b32 v17, a169\n"
"v_mfma_f32_16x16x32_bf16 a[24:27], a[84:87], a[156:159], a[24:27]\n"
"ds_read_b128 a[156:159], v4 offset:31744\n"
"v_accvgpr_read_b32 v18, a170\n"
"v_mfma_f32_16x16x32_bf16 a[24:27], a[80:83], a[152:155], a[24:27]\n"
"s_waitcnt vmcnt(10) lgkmcnt(13)\n"
"ds_read_b128 a[152:155], v4 offset:30720\n"
"v_accvgpr_read_b32 v19, a171\n"
"v_mfma_f32_16x16x32_bf16 v[144:147], a[32:35], a[96:99], v[144:147]\n"
"v_lshlrev_b32 v12, 16, v16\n"
"v_and_b32 v13, s63, v16\n"
"v_mfma_f32_16x16x32_bf16 v[144:147], a[36:39], a[100:103], v[144:147]\n"
"global_load_dwordx4 a[80:83], v0, s[34:35] offset:0 sc1\n"
"v_lshlrev_b32 v14, 16, v17\n"
"v_mfma_f32_16x16x32_bf16 v[172:175], a[44:47], a[100:103], v[172:175]\n"
"v_and_b32 v15, s63, v17\n"
"v_mul_f32 v12, v12, v52\n"
"v_mfma_f32_16x16x32_bf16 v[172:175], a[40:43], a[96:99], v[172:175]\n"
"v_mul_f32 v13, v13, v53\n"
"v_mul_f32 v14, v14, v54\n"
"v_mfma_f32_16x16x32_bf16 v[176:179], a[40:43], a[104:107], v[176:179]\n"
"v_mul_f32 v15, v15, v55\n"
"v_cvt_pk_bf16_f32 v24, v12, v13\n"
"v_mfma_f32_16x16x32_bf16 v[176:179], a[44:47], a[108:111], v[176:179]\n"
"v_cvt_pk_bf16_f32 v25, v14, v15\n"
"v_lshlrev_b32 v12, 16, v18\n"
"v_mfma_f32_16x16x32_bf16 v[148:151], a[36:39], a[108:111], v[148:151]\n"
"v_and_b32 v13, s63, v18\n"
"v_lshlrev_b32 v14, 16, v19\n"
"v_mfma_f32_16x16x32_bf16 v[148:151], a[32:35], a[104:107], v[148:151]\n"
"s_waitcnt lgkmcnt(10)\n"
"v_and_b32 v15, s63, v19\n"
"v_mul_f32 v12, v12, v80\n"
"v_mfma_f32_16x16x32_bf16 v[152:155], a[32:35], a[112:115], v[152:155]\n"
"v_mul_f32 v13, v13, v81\n"
"v_mul_f32 v14, v14, v82\n"
"v_mfma_f32_16x16x32_bf16 v[152:155], a[36:39], a[116:119], v[152:155]\n"
"global_load_dwordx4 a[84:87], v0, s[34:35] offset:1024 sc1\n"
"v_mul_f32 v15, v15, v83\n"
"v_mfma_f32_16x16x32_bf16 v[180:183], a[44:47], a[116:119], v[180:183]\n"
"v_cvt_pk_bf16_f32 v26, v12, v13\n"
"v_mfma_f32_16x16x32_bf16 v[180:183], a[40:43], a[112:115], v[180:183]\n"
"v_cvt_pk_bf16_f32 v27, v14, v15\n"
"v_mfma_f32_16x16x32_bf16 v[184:187], a[40:43], a[120:123], v[184:187]\n"
"global_store_dwordx4 v0, v[24:27], s[54:55] offset:-2048 sc1\n"
"ds_write_b128 v2, v[24:27] offset:10240\n"
"v_mfma_f32_16x16x32_bf16 v[184:187], a[44:47], a[124:127], v[184:187]\n"
"v_accvgpr_read_b32 v16, a172\n"
"v_mfma_f32_16x16x32_bf16 v[156:159], a[36:39], a[124:127], v[156:159]\n"
"v_accvgpr_read_b32 v17, a173\n"
"v_mfma_f32_16x16x32_bf16 v[156:159], a[32:35], a[120:123], v[156:159]\n"
"s_waitcnt lgkmcnt(6)\n"
"v_accvgpr_read_b32 v18, a174\n"
"v_accvgpr_read_b32 v19, a175\n"
"v_mfma_f32_16x16x32_bf16 v[160:163], a[32:35], a[128:131], v[160:163]\n"
"v_lshlrev_b32 v12, 16, v16\n"
"v_mfma_f32_16x16x32_bf16 v[160:163], a[36:39], a[132:135], v[160:163]\n"
"global_load_dwordx4 a[88:91], v0, s[34:35] offset:2048 sc1\n"
"v_and_b32 v13, s63, v16\n"
"v_mfma_f32_16x16x32_bf16 v[188:191], a[44:47], a[132:135], v[188:191]\n"
"v_lshlrev_b32 v14, 16, v17\n"
"v_and_b32 v15, s63, v17\n"
"v_mfma_f32_16x16x32_bf16 v[188:191], a[40:43], a[128:131], v[188:191]\n"
"v_mul_f32 v12, v12, v108\n"
"v_mfma_f32_16x16x32_bf16 v[192:195], a[40:43], a[136:139], v[192:195]\n"
"v_mul_f32 v13, v13, v109\n"
"v_mfma_f32_16x16x32_bf16 v[192:195], a[44:47], a[140:143], v[192:195]\n"
"v_mul_f32 v14, v14, v110\n"
"v_mul_f32 v15, v15, v111\n"
"v_mfma_f32_16x16x32_bf16 v[164:167], a[36:39], a[140:143], v[164:167]\n"
"v_cvt_pk_bf16_f32 v28, v12, v13\n"
"v_mfma_f32_16x16x32_bf16 v[164:167], a[32:35], a[136:139], v[164:167]\n"
"s_waitcnt lgkmcnt(1)\n"
"v_cvt_pk_bf16_f32 v29, v14, v15\n"
"v_mfma_f32_16x16x32_bf16 v[168:171], a[32:35], a[144:147], v[168:171]\n"
"v_lshlrev_b32 v12, 16, v18\n"
"v_and_b32 v13, s63, v18\n"
"v_mfma_f32_16x16x32_bf16 v[168:171], a[36:39], a[148:151], v[168:171]\n"
"global_load_dwordx4 a[92:95], v0, s[34:35] offset:3072 sc1\n"
"v_lshlrev_b32 v14, 16, v19\n"
"v_mfma_f32_16x16x32_bf16 v[196:199], a[44:47], a[148:151], v[196:199]\n"
"v_and_b32 v15, s63, v19\n"
"v_mfma_f32_16x16x32_bf16 v[196:199], a[40:43], a[144:147], v[196:199]\n"
"v_mul_f32 v12, v12, v136\n"
"v_mul_f32 v13, v13, v137\n"
"v_mfma_f32_16x16x32_bf16 a[20:23], a[40:43], a[152:155], a[20:23]\n"
"s_add_u32 s34, s34, 8192\n"
"v_mul_f32 v14, v14, v138\n"
"v_mfma_f32_16x16x32_bf16 a[20:23], a[44:47], a[156:159], a[20:23]\n"
"v_mul_f32 v15, v15, v139\n"
"v_mfma_f32_16x16x32_bf16 a[16:19], a[36:39], a[156:159], a[16:19]\n"
"v_cvt_pk_bf16_f32 v30, v12, v13\n"
"v_cvt_pk_bf16_f32 v31, v14, v15\n"
"v_mfma_f32_16x16x32_bf16 a[16:19], a[32:35], a[152:155], a[16:19]\n"
"s_waitcnt vmcnt(10)\n"
"global_store_dwordx4 v0, v[28:31], s[54:55] offset:-1024 sc1\n"
"ds_write_b128 v2, v[28:31] offset:11264\n"
"v_mfma_f32_16x16x32_bf16 v[200:203], a[48:51], a[96:99], v[200:203]\n"
"v_accvgpr_read_b32 v16, a176\n"
"v_mfma_f32_16x16x32_bf16 v[200:203], a[52:55], a[100:103], v[200:203]\n"
"global_load_dwordx4 a[32:35], v0, s[34:35] offset:-4096 sc1\n"
"v_accvgpr_read_b32 v17, a177\n"
"v_mfma_f32_16x16x32_bf16 v[228:231], a[60:63], a[100:103], v[228:231]\n"
"ds_read_b128 a[100:103], v5 offset:17408\n"
"v_accvgpr_read_b32 v18, a178\n"
"v_mfma_f32_16x16x32_bf16 v[228:231], a[56:59], a[96:99], v[228:231]\n"
"ds_read_b128 a[96:99], v5 offset:16384\n"
"v_accvgpr_read_b32 v19, a179\n"
"v_mfma_f32_16x16x32_bf16 v[232:235], a[56:59], a[104:107], v[232:235]\n"
"v_lshlrev_b32 v12, 16, v16\n"
"v_and_b32 v13, s63, v16\n"
"v_mfma_f32_16x16x32_bf16 v[232:235], a[60:63], a[108:111], v[232:235]\n"
"v_lshlrev_b32 v14, 16, v17\n"
"v_mfma_f32_16x16x32_bf16 v[204:207], a[52:55], a[108:111], v[204:207]\n"
"ds_read_b128 a[108:111], v5 offset:19456\n"
"v_and_b32 v15, s63, v17\n"
"v_mfma_f32_16x16x32_bf16 v[204:207], a[48:51], a[104:107], v[204:207]\n"
"ds_read_b128 a[104:107], v5 offset:18432\n"
"v_mul_f32 v12, v12, v56\n"
"v_mfma_f32_16x16x32_bf16 v[208:211], a[48:51], a[112:115], v[208:211]\n"
"v_mul_f32 v13, v13, v57\n"
"v_mul_f32 v14, v14, v58\n"
"v_mfma_f32_16x16x32_bf16 v[208:211], a[52:55], a[116:119], v[208:211]\n"
"global_load_dwordx4 a[36:39], v0, s[34:35] offset:-3072 sc1\n"
"v_mul_f32 v15, v15, v59\n"
"v_mfma_f32_16x16x32_bf16 v[236:239], a[60:63], a[116:119], v[236:239]\n"
"ds_read_b128 a[116:119], v5 offset:21504\n"
"v_cvt_pk_bf16_f32 v24, v12, v13\n"
"v_mfma_f32_16x16x32_bf16 v[236:239], a[56:59], a[112:115], v[236:239]\n"
"ds_read_b128 a[112:115], v5 offset:20480\n"
"v_cvt_pk_bf16_f32 v25, v14, v15\n"
"v_mfma_f32_16x16x32_bf16 v[240:243], a[56:59], a[120:123], v[240:243]\n"
"ds_write_b64 v2, v[24:25] offset:12288\n"
"v_mfma_f32_16x16x32_bf16 v[240:243], a[60:63], a[124:127], v[240:243]\n"
"v_lshlrev_b32 v12, 16, v18\n"
"v_and_b32 v13, s63, v18\n"
"v_mfma_f32_16x16x32_bf16 v[212:215], a[52:55], a[124:127], v[212:215]\n"
"ds_read_b128 a[124:127], v5 offset:23552\n"
"v_lshlrev_b32 v14, 16, v19\n"
"v_mfma_f32_16x16x32_bf16 v[212:215], a[48:51], a[120:123], v[212:215]\n"
"ds_read_b128 a[120:123], v5 offset:22528\n"
"v_and_b32 v15, s63, v19\n"
"v_mfma_f32_16x16x32_bf16 v[216:219], a[48:51], a[128:131], v[216:219]\n"
"v_mul_f32 v12, v12, v84\n"
"v_mul_f32 v13, v13, v85\n"
"v_mfma_f32_16x16x32_bf16 v[216:219], a[52:55], a[132:135], v[216:219]\n"
"global_load_dwordx4 a[40:43], v0, s[34:35] offset:-2048 sc1\n"
"v_mul_f32 v14, v14, v86\n"
"v_mfma_f32_16x16x32_bf16 v[244:247], a[60:63], a[132:135], v[244:247]\n"
"ds_read_b128 a[132:135], v5 offset:25600\n"
"v_mul_f32 v15, v15, v87\n"
"v_mfma_f32_16x16x32_bf16 v[244:247], a[56:59], a[128:131], v[244:247]\n"
"ds_read_b128 a[128:131], v5 offset:24576\n"
"v_cvt_pk_bf16_f32 v26, v12, v13\n"
"v_mfma_f32_16x16x32_bf16 v[248:251], a[56:59], a[136:139], v[248:251]\n"
"v_cvt_pk_bf16_f32 v27, v14, v15\n"
"v_mfma_f32_16x16x32_bf16 v[248:251], a[60:63], a[140:143], v[248:251]\n"
"ds_write_b64 v2, v[26:27] offset:12296\n"
"v_mfma_f32_16x16x32_bf16 v[220:223], a[52:55], a[140:143], v[220:223]\n"
"ds_read_b128 a[140:143], v5 offset:27648\n"
"v_accvgpr_read_b32 v16, a180\n"
"v_mfma_f32_16x16x32_bf16 v[220:223], a[48:51], a[136:139], v[220:223]\n"
"ds_read_b128 a[136:139], v5 offset:26624\n"
"v_accvgpr_read_b32 v17, a181\n"
"v_mfma_f32_16x16x32_bf16 v[224:227], a[48:51], a[144:147], v[224:227]\n"
"v_accvgpr_read_b32 v18, a182\n"
"v_accvgpr_read_b32 v19, a183\n"
"v_mfma_f32_16x16x32_bf16 v[224:227], a[52:55], a[148:151], v[224:227]\n"
"global_load_dwordx4 a[44:47], v0, s[34:35] offset:-1024 sc1\n"
"v_lshlrev_b32 v12, 16, v16\n"
"v_mfma_f32_16x16x32_bf16 v[252:255], a[60:63], a[148:151], v[252:255]\n"
"ds_read_b128 a[148:151], v5 offset:29696\n"
"v_and_b32 v13, s63, v16\n"
"v_mfma_f32_16x16x32_bf16 v[252:255], a[56:59], a[144:147], v[252:255]\n"
"ds_read_b128 a[144:147], v5 offset:28672\n"
"v_lshlrev_b32 v14, 16, v17\n"
"v_mfma_f32_16x16x32_bf16 a[28:31], a[56:59], a[152:155], a[28:31]\n"
"v_and_b32 v15, s63, v17\n"
"v_mul_f32 v12, v12, v112\n"
"v_mfma_f32_16x16x32_bf16 a[28:31], a[60:63], a[156:159], a[28:31]\n"
"v_mul_f32 v13, v13, v113\n"
"v_mul_f32 v14, v14, v114\n"
"v_mfma_f32_16x16x32_bf16 a[24:27], a[52:55], a[156:159], a[24:27]\n"
"ds_read_b128 a[156:159], v5 offset:31744\n"
"v_mul_f32 v15, v15, v115\n"
"v_mfma_f32_16x16x32_bf16 a[24:27], a[48:51], a[152:155], a[24:27]\n"
"s_waitcnt vmcnt(10) lgkmcnt(13)\n"
"ds_read_b128 a[152:155], v5 offset:30720\n"
"v_cvt_pk_bf16_f32 v28, v12, v13\n"
"v_mfma_f32_16x16x32_bf16 v[144:147], a[64:67], a[96:99], v[144:147]\n"
"v_cvt_pk_bf16_f32 v29, v14, v15\n"
"v_lshlrev_b32 v12, 16, v18\n"
"v_mfma_f32_16x16x32_bf16 v[144:147], a[68:71], a[100:103], v[144:147]\n"
"global_load_dwordx4 a[48:51], v0, s[34:35] offset:0 sc1\n"
"v_and_b32 v13, s63, v18\n"
"v_mfma_f32_16x16x32_bf16 v[172:175], a[76:79], a[100:103], v[172:175]\n"
"v_lshlrev_b32 v14, 16, v19\n"
"v_and_b32 v15, s63, v19\n"
"v_mfma_f32_16x16x32_bf16 v[172:175], a[72:75], a[96:99], v[172:175]\n"
"v_mul_f32 v12, v12, v140\n"
"v_mul_f32 v13, v13, v141\n"
"v_mfma_f32_16x16x32_bf16 v[176:179], a[72:75], a[104:107], v[176:179]\n"
"v_mul_f32 v14, v14, v142\n"
"v_mul_f32 v15, v15, v143\n"
"v_mfma_f32_16x16x32_bf16 v[176:179], a[76:79], a[108:111], v[176:179]\n"
"v_cvt_pk_bf16_f32 v30, v12, v13\n"
"v_mfma_f32_16x16x32_bf16 v[148:151], a[68:71], a[108:111], v[148:151]\n"
"v_cvt_pk_bf16_f32 v31, v14, v15\n"
"v_mfma_f32_16x16x32_bf16 v[148:151], a[64:67], a[104:107], v[148:151]\n"
"s_waitcnt lgkmcnt(9)\n"
"global_store_dwordx4 v0, v[28:31], s[54:55] offset:1024 sc1\n"
"ds_write_b128 v2, v[28:31] offset:13312\n"
"v_mfma_f32_16x16x32_bf16 v[152:155], a[64:67], a[112:115], v[152:155]\n"
"v_accvgpr_read_b32 v16, a184\n"
"v_accvgpr_read_b32 v17, a185\n"
"v_mfma_f32_16x16x32_bf16 v[152:155], a[68:71], a[116:119], v[152:155]\n"
"global_load_dwordx4 a[52:55], v0, s[34:35] offset:1024 sc1\n"
"v_accvgpr_read_b32 v18, a186\n"
"v_mfma_f32_16x16x32_bf16 v[180:183], a[76:79], a[116:119], v[180:183]\n"
"v_accvgpr_read_b32 v19, a187\n"
"v_mfma_f32_16x16x32_bf16 v[180:183], a[72:75], a[112:115], v[180:183]\n"
"v_lshlrev_b32 v12, 16, v16\n"
"v_mfma_f32_16x16x32_bf16 v[184:187], a[72:75], a[120:123], v[184:187]\n"
"v_and_b32 v13, s63, v16\n"
"v_lshlrev_b32 v14, 16, v17\n"
"v_mfma_f32_16x16x32_bf16 v[184:187], a[76:79], a[124:127], v[184:187]\n"
"v_and_b32 v15, s63, v17\n"
"v_mfma_f32_16x16x32_bf16 v[156:159], a[68:71], a[124:127], v[156:159]\n"
"v_accvgpr_read_b32 v10, a0\n"
"v_mfma_f32_16x16x32_bf16 v[156:159], a[64:67], a[120:123], v[156:159]\n"
"s_waitcnt lgkmcnt(5)\n"
"v_accvgpr_read_b32 v11, a1\n"
"v_mul_f32 v12, v12, v10\n"
"v_mfma_f32_16x16x32_bf16 v[160:163], a[64:67], a[128:131], v[160:163]\n"
"v_mul_f32 v13, v13, v11\n"
"v_mfma_f32_16x16x32_bf16 v[160:163], a[68:71], a[132:135], v[160:163]\n"
"global_load_dwordx4 a[56:59], v0, s[34:35] offset:2048 sc1\n"
"v_accvgpr_read_b32 v10, a2\n"
"v_mfma_f32_16x16x32_bf16 v[188:191], a[76:79], a[132:135], v[188:191]\n"
"v_accvgpr_read_b32 v11, a3\n"
"v_mul_f32 v14, v14, v10\n"
"v_mfma_f32_16x16x32_bf16 v[188:191], a[72:75], a[128:131], v[188:191]\n"
"v_mul_f32 v15, v15, v11\n"
"v_mfma_f32_16x16x32_bf16 v[192:195], a[72:75], a[136:139], v[192:195]\n"
"v_cvt_pk_bf16_f32 v24, v12, v13\n"
"v_mfma_f32_16x16x32_bf16 v[192:195], a[76:79], a[140:143], v[192:195]\n"
"v_cvt_pk_bf16_f32 v25, v14, v15\n"
"v_lshlrev_b32 v12, 16, v18\n"
"v_mfma_f32_16x16x32_bf16 v[164:167], a[68:71], a[140:143], v[164:167]\n"
"v_and_b32 v13, s63, v18\n"
"v_mfma_f32_16x16x32_bf16 v[164:167], a[64:67], a[136:139], v[164:167]\n"
"s_waitcnt lgkmcnt(1)\n"
"v_lshlrev_b32 v14, 16, v19\n"
"v_mfma_f32_16x16x32_bf16 v[168:171], a[64:67], a[144:147], v[168:171]\n"
"v_and_b32 v15, s63, v19\n"
"v_accvgpr_read_b32 v10, a4\n"
"v_mfma_f32_16x16x32_bf16 v[168:171], a[68:71], a[148:151], v[168:171]\n"
"global_load_dwordx4 a[60:63], v0, s[34:35] offset:3072 sc1\n"
"v_accvgpr_read_b32 v11, a5\n"
"v_mfma_f32_16x16x32_bf16 v[196:199], a[76:79], a[148:151], v[196:199]\n"
"v_mul_f32 v12, v12, v10\n"
"v_mfma_f32_16x16x32_bf16 v[196:199], a[72:75], a[144:147], v[196:199]\n"
"v_mul_f32 v13, v13, v11\n"
"v_accvgpr_read_b32 v10, a6\n"
"v_mfma_f32_16x16x32_bf16 a[20:23], a[72:75], a[152:155], a[20:23]\n"
"s_add_u32 s34, s34, 8192\n"
"v_accvgpr_read_b32 v11, a7\n"
"v_mfma_f32_16x16x32_bf16 a[20:23], a[76:79], a[156:159], a[20:23]\n"
"v_mul_f32 v14, v14, v10\n"
"v_mul_f32 v15, v15, v11\n"
"v_mfma_f32_16x16x32_bf16 a[16:19], a[68:71], a[156:159], a[16:19]\n"
"v_cvt_pk_bf16_f32 v26, v12, v13\n"
"v_cvt_pk_bf16_f32 v27, v14, v15\n"
"v_mfma_f32_16x16x32_bf16 a[16:19], a[64:67], a[152:155], a[16:19]\n"
"s_waitcnt vmcnt(10)\n"
"global_store_dwordx4 v0, v[24:27], s[54:55] offset:2048 sc1\n"
"ds_write_b128 v2, v[24:27] offset:14336\n"
"v_mfma_f32_16x16x32_bf16 v[200:203], a[80:83], a[96:99], v[200:203]\n"
"v_accvgpr_read_b32 v16, a188\n"
"v_mfma_f32_16x16x32_bf16 v[200:203], a[84:87], a[100:103], v[200:203]\n"
"global_load_dwordx4 a[64:67], v0, s[34:35] offset:-4096 sc1\n"
"v_accvgpr_read_b32 v17, a189\n"
"v_mfma_f32_16x16x32_bf16 v[228:231], a[92:95], a[100:103], v[228:231]\n"
"ds_read_b128 a[100:103], v2 offset:1024\n"
"v_accvgpr_read_b32 v18, a190\n"
"v_mfma_f32_16x16x32_bf16 v[228:231], a[88:91], a[96:99], v[228:231]\n"
"ds_read_b128 a[96:99], v2 offset:0\n"
"v_accvgpr_read_b32 v19, a191\n"
"v_mfma_f32_16x16x32_bf16 v[232:235], a[88:91], a[104:107], v[232:235]\n"
"v_lshlrev_b32 v12, 16, v16\n"
"v_and_b32 v13, s63, v16\n"
"v_mfma_f32_16x16x32_bf16 v[232:235], a[92:95], a[108:111], v[232:235]\n"
"v_lshlrev_b32 v14, 16, v17\n"
"v_and_b32 v15, s63, v17\n"
"v_mfma_f32_16x16x32_bf16 v[204:207], a[84:87], a[108:111], v[204:207]\n"
"ds_read_b128 a[108:111], v2 offset:3072\n"
"v_accvgpr_read_b32 v10, a8\n"
"v_mfma_f32_16x16x32_bf16 v[204:207], a[80:83], a[104:107], v[204:207]\n"
"ds_read_b128 a[104:107], v2 offset:2048\n"
"v_accvgpr_read_b32 v11, a9\n"
"v_mfma_f32_16x16x32_bf16 v[208:211], a[80:83], a[112:115], v[208:211]\n"
"v_mul_f32 v12, v12, v10\n"
"v_mul_f32 v13, v13, v11\n"
"v_mfma_f32_16x16x32_bf16 v[208:211], a[84:87], a[116:119], v[208:211]\n"
"global_load_dwordx4 a[68:71], v0, s[34:35] offset:-3072 sc1\n"
"v_accvgpr_read_b32 v10, a10\n"
"v_mfma_f32_16x16x32_bf16 v[236:239], a[92:95], a[116:119], v[236:239]\n"
"ds_read_b128 a[116:119], v2 offset:5120\n"
"v_accvgpr_read_b32 v11, a11\n"
"v_mfma_f32_16x16x32_bf16 v[236:239], a[88:91], a[112:115], v[236:239]\n"
"ds_read_b128 a[112:115], v2 offset:4096\n"
"v_mul_f32 v14, v14, v10\n"
"v_mfma_f32_16x16x32_bf16 v[240:243], a[88:91], a[120:123], v[240:243]\n"
"v_mul_f32 v15, v15, v11\n"
"v_cvt_pk_bf16_f32 v28, v12, v13\n"
"v_mfma_f32_16x16x32_bf16 v[240:243], a[92:95], a[124:127], v[240:243]\n"
"v_cvt_pk_bf16_f32 v29, v14, v15\n"
"v_lshlrev_b32 v12, 16, v18\n"
"v_mfma_f32_16x16x32_bf16 v[212:215], a[84:87], a[124:127], v[212:215]\n"
"ds_read_b128 a[124:127], v2 offset:7168\n"
"v_and_b32 v13, s63, v18\n"
"v_mfma_f32_16x16x32_bf16 v[212:215], a[80:83], a[120:123], v[212:215]\n"
"ds_read_b128 a[120:123], v2 offset:6144\n"
"v_lshlrev_b32 v14, 16, v19\n"
"v_mfma_f32_16x16x32_bf16 v[216:219], a[80:83], a[128:131], v[216:219]\n"
"v_and_b32 v15, s63, v19\n"
"v_accvgpr_read_b32 v10, a12\n"
"v_mfma_f32_16x16x32_bf16 v[216:219], a[84:87], a[132:135], v[216:219]\n"
"global_load_dwordx4 a[72:75], v0, s[34:35] offset:-2048 sc1\n"
"v_accvgpr_read_b32 v11, a13\n"
"v_mfma_f32_16x16x32_bf16 v[244:247], a[92:95], a[132:135], v[244:247]\n"
"ds_read_b128 a[132:135], v2 offset:9216\n"
"v_mul_f32 v12, v12, v10\n"
"v_mfma_f32_16x16x32_bf16 v[244:247], a[88:91], a[128:131], v[244:247]\n"
"ds_read_b128 a[128:131], v2 offset:8192\n"
"v_mul_f32 v13, v13, v11\n"
"v_mfma_f32_16x16x32_bf16 v[248:251], a[88:91], a[136:139], v[248:251]\n"
"v_accvgpr_read_b32 v10, a14\n"
"v_accvgpr_read_b32 v11, a15\n"
"v_mfma_f32_16x16x32_bf16 v[248:251], a[92:95], a[140:143], v[248:251]\n"
"v_mul_f32 v14, v14, v10\n"
"v_mul_f32 v15, v15, v11\n"
"v_mfma_f32_16x16x32_bf16 v[220:223], a[84:87], a[140:143], v[220:223]\n"
"ds_read_b128 a[140:143], v2 offset:11264\n"
"v_cvt_pk_bf16_f32 v30, v12, v13\n"
"v_mfma_f32_16x16x32_bf16 v[220:223], a[80:83], a[136:139], v[220:223]\n"
"ds_read_b128 a[136:139], v2 offset:10240\n"
"v_cvt_pk_bf16_f32 v31, v14, v15\n"
"v_mfma_f32_16x16x32_bf16 v[224:227], a[80:83], a[144:147], v[224:227]\n"
"global_store_dwordx4 v0, v[28:31], s[54:55] offset:3072 sc1\n"
"ds_write_b128 v2, v[28:31] offset:15360\n"
"v_mfma_f32_16x16x32_bf16 v[224:227], a[84:87], a[148:151], v[224:227]\n"
"global_load_dwordx4 a[76:79], v0, s[34:35] offset:-1024 sc1\n"
"v_mfma_f32_16x16x32_bf16 v[252:255], a[92:95], a[148:151], v[252:255]\n"
"ds_read_b128 a[148:151], v2 offset:13312\n"
"v_mfma_f32_16x16x32_bf16 v[252:255], a[88:91], a[144:147], v[252:255]\n"
"ds_read_b128 a[144:147], v2 offset:12288\n"
"v_mfma_f32_16x16x32_bf16 a[28:31], a[88:91], a[152:155], a[28:31]\n"
"s_add_u32 s62, s62, 1\n"
"v_mfma_f32_16x16x32_bf16 a[28:31], a[92:95], a[156:159], a[28:31]\n"
"v_mfma_f32_16x16x32_bf16 a[24:27], a[84:87], a[156:159], a[24:27]\n"
"ds_read_b128 a[156:159], v2 offset:15360\n"
"v_mfma_f32_16x16x32_bf16 a[24:27], a[80:83], a[152:155], a[24:27]\n"
"ds_read_b128 a[152:155], v2 offset:14336\n"
"L_layer:\n"
"s_waitcnt vmcnt(11) lgkmcnt(13)\n"
"v_mfma_f32_16x16x32_bf16 v[32:35], a[32:35], a[96:99], v[32:35]\n"
"s_mov_b64 s[96:97], s[52:53]\n"
"v_accvgpr_read_b32 v16, a192\n"
"v_accvgpr_read_b32 v17, a193\n"
"v_mfma_f32_16x16x32_bf16 v[32:35], a[36:39], a[100:103], v[32:35]\n"
"global_load_dwordx4 a[80:83], v0, s[34:35] offset:0 sc1\n"
"v_accvgpr_read_b32 v18, a194\n"
"v_mfma_f32_16x16x32_bf16 v[60:63], a[44:47], a[100:103], v[60:63]\n"
"s_mov_b64 s[98:99], s[54:55]\n"
"v_accvgpr_read_b32 v19, a195\n"
"v_mfma_f32_16x16x32_bf16 v[60:63], a[40:43], a[96:99], v[60:63]\n"
"v_lshlrev_b32 v12, 16, v16\n"
"v_and_b32 v13, s63, v16\n"
"v_mfma_f32_16x16x32_bf16 v[64:67], a[40:43], a[104:107], v[64:67]\n"
"s_add_u32 s52, s52, 131072\n"
"s_addc_u32 s53, s53, 0\n"
"v_lshlrev_b32 v14, 16, v17\n"
"v_mfma_f32_16x16x32_bf16 v[64:67], a[44:47], a[108:111], v[64:67]\n"
"v_and_b32 v15, s63, v17\n"
"v_mul_f32 v12, v12, v144\n"
"v_mfma_f32_16x16x32_bf16 v[36:39], a[36:39], a[108:111], v[36:39]\n"
"s_add_u32 s54, s54, 131072\n"
"s_addc_u32 s55, s55, 0\n"
"v_mul_f32 v13, v13, v145\n"
"v_mfma_f32_16x16x32_bf16 v[36:39], a[32:35], a[104:107], v[36:39]\n"
"s_waitcnt lgkmcnt(9)\n"
"v_mul_f32 v14, v14, v146\n"
"v_mul_f32 v15, v15, v147\n"
"v_mfma_f32_16x16x32_bf16 v[40:43], a[32:35], a[112:115], v[40:43]\n"
"ds_read_b128 v[20:23], v2 offset:0\n"
"v_cvt_pk_bf16_f32 v24, v12, v13\n"
"v_mfma_f32_16x16x32_bf16 v[40:43], a[36:39], a[116:119], v[40:43]\n"
"global_load_dwordx4 a[84:87], v0, s[34:35] offset:1024 sc1\n"
"v_cvt_pk_bf16_f32 v25, v14, v15\n"
"v_mfma_f32_16x16x32_bf16 v[68:71], a[44:47], a[116:119], v[68:71]\n"
"ds_write_b64 v2, v[24:25] offset:16384\n"
"v_mfma_f32_16x16x32_bf16 v[68:71], a[40:43], a[112:115], v[68:71]\n"
"v_lshlrev_b32 v12, 16, v18\n"
"v_and_b32 v13, s63, v18\n"
"v_mfma_f32_16x16x32_bf16 v[72:75], a[40:43], a[120:123], v[72:75]\n"
"s_waitcnt lgkmcnt(6)\n"
"s_barrier\n"
"v_mfma_f32_16x16x32_bf16 v[72:75], a[44:47], a[124:127], v[72:75]\n"
"v_lshlrev_b32 v14, 16, v19\n"
"v_and_b32 v15, s63, v19\n"
"v_mfma_f32_16x16x32_bf16 v[44:47], a[36:39], a[124:127], v[44:47]\n"
"v_mul_f32 v12, v12, v172\n"
"v_mul_f32 v13, v13, v173\n"
"v_mfma_f32_16x16x32_bf16 v[44:47], a[32:35], a[120:123], v[44:47]\n"
"v_mul_f32 v14, v14, v174\n"
"v_mfma_f32_16x16x32_bf16 v[48:51], a[32:35], a[128:131], v[48:51]\n"
"v_mul_f32 v15, v15, v175\n"
"v_mfma_f32_16x16x32_bf16 v[48:51], a[36:39], a[132:135], v[48:51]\n"
"global_load_dwordx4 a[88:91], v0, s[34:35] offset:2048 sc1\n"
"v_mfma_f32_16x16x32_bf16 v[76:79], a[44:47], a[132:135], v[76:79]\n"
"v_cvt_pk_bf16_f32 v26, v12, v13\n"
"v_mfma_f32_16x16x32_bf16 v[76:79], a[40:43], a[128:131], v[76:79]\n"
"v_cvt_pk_bf16_f32 v27, v14, v15\n"
"v_mfma_f32_16x16x32_bf16 v[80:83], a[40:43], a[136:139], v[80:83]\n"
"s_waitcnt lgkmcnt(1)\n"
"global_store_dwordx4 v0, v[20:23], s[96:97] offset:-4096 sc1\n"
"ds_write_b64 v2, v[26:27] offset:16392\n"
"v_accvgpr_read_b32 v16, a196\n"
"v_mfma_f32_16x16x32_bf16 v[80:83], a[44:47], a[140:143], v[80:83]\n"
"v_accvgpr_read_b32 v17, a197\n"
"v_accvgpr_read_b32 v18, a198\n"
"v_mfma_f32_16x16x32_bf16 v[52:55], a[36:39], a[140:143], v[52:55]\n"
"v_accvgpr_read_b32 v19, a199\n"
"v_lshlrev_b32 v12, 16, v16\n"
"v_mfma_f32_16x16x32_bf16 v[52:55], a[32:35], a[136:139], v[52:55]\n"
"v_and_b32 v13, s63, v16\n"
"v_lshlrev_b32 v14, 16, v17\n"
"v_mfma_f32_16x16x32_bf16 v[56:59], a[32:35], a[144:147], v[56:59]\n"
"v_and_b32 v15, s63, v17\n"
"v_mul_f32 v12, v12, v200\n"
"v_mfma_f32_16x16x32_bf16 v[56:59], a[36:39], a[148:151], v[56:59]\n"
"global_load_dwordx4 a[92:95], v0, s[34:35] offset:3072 sc1\n"
"v_mul_f32 v13, v13, v201\n"
"v_mfma_f32_16x16x32_bf16 v[84:87], a[44:47], a[148:151], v[84:87]\n"
"v_mul_f32 v14, v14, v202\n"
"v_mul_f32 v15, v15, v203\n"
"v_mfma_f32_16x16x32_bf16 v[84:87], a[40:43], a[144:147], v[84:87]\n"
"v_cvt_pk_bf16_f32 v28, v12, v13\n"
"v_cvt_pk_bf16_f32 v29, v14, v15\n"
"v_mfma_f32_16x16x32_bf16 a[4:7], a[40:43], a[152:155], a[4:7]\n"
"s_add_u32 s34, s34, 8192\n"
"ds_write_b64 v2, v[28:29] offset:17408\n"
"v_mfma_f32_16x16x32_bf16 a[4:7], a[44:47], a[156:159], a[4:7]\n"
"v_lshlrev_b32 v12, 16, v18\n"
"v_mfma_f32_16x16x32_bf16 a[0:3], a[36:39], a[156:159], a[0:3]\n"
"v_and_b32 v13, s63, v18\n"
"v_lshlrev_b32 v14, 16, v19\n"
"v_mfma_f32_16x16x32_bf16 a[0:3], a[32:35], a[152:155], a[0:3]\n"
"s_waitcnt vmcnt(11)\n"
"v_and_b32 v15, s63, v19\n"
"v_mul_f32 v12, v12, v228\n"
"v_mfma_f32_16x16x32_bf16 v[88:91], a[48:51], a[96:99], v[88:91]\n"
"v_mul_f32 v13, v13, v229\n"
"v_mul_f32 v14, v14, v230\n"
"v_mfma_f32_16x16x32_bf16 v[88:91], a[52:55], a[100:103], v[88:91]\n"
"global_load_dwordx4 a[32:35], v0, s[34:35] offset:-4096 sc1\n"
"v_mul_f32 v15, v15, v231\n"
"v_mfma_f32_16x16x32_bf16 v[116:119], a[60:63], a[100:103], v[116:119]\n"
"ds_read_b128 a[100:103], v3 offset:1024\n"
"v_cvt_pk_bf16_f32 v30, v12, v13\n"
"v_mfma_f32_16x16x32_bf16 v[116:119], a[56:59], a[96:99], v[116:119]\n"
"ds_read_b128 a[96:99], v3 offset:0\n"
"v_cvt_pk_bf16_f32 v31, v14, v15\n"
"v_mfma_f32_16x16x32_bf16 v[120:123], a[56:59], a[104:107], v[120:123]\n"
"ds_write_b64 v2, v[30:31] offset:17416\n"
"v_mfma_f32_16x16x32_bf16 v[120:123], a[60:63], a[108:111], v[120:123]\n"
"ds_read_b128 v[20:23], v2 offset:3072\n"
"v_accvgpr_read_b32 v16, a200\n"
"v_mfma_f32_16x16x32_bf16 v[92:95], a[52:55], a[108:111], v[92:95]\n"
"ds_read_b128 a[108:111], v3 offset:3072\n"
"v_accvgpr_read_b32 v17, a201\n"
"v_mfma_f32_16x16x32_bf16 v[92:95], a[48:51], a[104:107], v[92:95]\n"
"ds_read_b128 a[104:107], v3 offset:2048\n"
"v_accvgpr_read_b32 v18, a202\n"
"v_mfma_f32_16x16x32_bf16 v[96:99], a[48:51], a[112:115], v[96:99]\n"
"v_accvgpr_read_b32 v19, a203\n"
"v_lshlrev_b32 v12, 16, v16\n"
"v_mfma_f32_16x16x32_bf16 v[96:99], a[52:55], a[116:119], v[96:99]\n"
"global_load_dwordx4 a[36:39], v0, s[34:35] offset:-3072 sc1\n"
"v_and_b32 v13, s63, v16\n"
"v_mfma_f32_16x16x32_bf16 v[124:127], a[60:63], a[116:119], v[124:127]\n"
"ds_read_b128 a[116:119], v3 offset:5120\n"
"v_lshlrev_b32 v14, 16, v17\n"
"v_mfma_f32_16x16x32_bf16 v[124:127], a[56:59], a[112:115], v[124:127]\n"
"ds_read_b128 a[112:115], v3 offset:4096\n"
"v_and_b32 v15, s63, v17\n"
"v_mfma_f32_16x16x32_bf16 v[128:131], a[56:59], a[120:123], v[128:131]\n"
"v_mul_f32 v12, v12, v148\n"
"v_mul_f32 v13, v13, v149\n"
"v_mfma_f32_16x16x32_bf16 v[128:131], a[60:63], a[124:127], v[128:131]\n"
"v_mul_f32 v14, v14, v150\n"
"v_mul_f32 v15, v15, v151\n"
"v_mfma_f32_16x16x32_bf16 v[100:103], a[52:55], a[124:127], v[100:103]\n"
"ds_read_b128 a[124:127], v3 offset:7168\n"
"v_cvt_pk_bf16_f32 v24, v12, v13\n"
"v_mfma_f32_16x16x32_bf16 v[100:103], a[48:51], a[120:123], v[100:103]\n"
"ds_read_b128 a[120:123], v3 offset:6144\n"
"v_cvt_pk_bf16_f32 v25, v14, v15\n"
"v_mfma_f32_16x16x32_bf16 v[104:107], a[48:51], a[128:131], v[104:107]\n"
"s_waitcnt lgkmcnt(6)\n"
"global_store_dwordx4 v0, v[20:23], s[96:97] offset:-1024 sc1\n"
"ds_write_b64 v2, v[24:25] offset:18432\n"
"v_lshlrev_b32 v12, 16, v18\n"
"v_mfma_f32_16x16x32_bf16 v[104:107], a[52:55], a[132:135], v[104:107]\n"
"global_load_dwordx4 a[40:43], v0, s[34:35] offset:-2048 sc1\n"
"v_and_b32 v13, s63, v18\n"
"v_mfma_f32_16x16x32_bf16 v[132:135], a[60:63], a[132:135], v[132:135]\n"
"ds_read_b128 a[132:135], v3 offset:9216\n"
"v_lshlrev_b32 v14, 16, v19\n"
"v_mfma_f32_16x16x32_bf16 v[132:135], a[56:59], a[128:131], v[132:135]\n"
"ds_read_b128 a[128:131], v3 offset:8192\n"
"v_and_b32 v15, s63, v19\n"
"v_mfma_f32_16x16x32_bf16 v[136:139], a[56:59], a[136:139], v[136:139]\n"
"v_mul_f32 v12, v12, v176\n"
"v_mul_f32 v13, v13, v177\n"
"v_mfma_f32_16x16x32_bf16 v[136:139], a[60:63], a[140:143], v[136:139]\n"
"v_mul_f32 v14, v14, v178\n"
"v_mul_f32 v15, v15, v179\n"
"v_mfma_f32_16x16x32_bf16 v[108:111], a[52:55], a[140:143], v[108:111]\n"
"ds_read_b128 a[140:143], v3 offset:11264\n"
"v_cvt_pk_bf16_f32 v26, v12, v13\n"
"v_mfma_f32_16x16x32_bf16 v[108:111], a[48:51], a[136:139], v[108:111]\n"
"ds_read_b128 a[136:139], v3 offset:10240\n"
"v_cvt_pk_bf16_f32 v27, v14, v15\n"
"v_mfma_f32_16x16x32_bf16 v[112:115], a[48:51], a[144:147], v[112:115]\n"
"ds_write_b64 v2, v[26:27] offset:18440\n"
"v_mfma_f32_16x16x32_bf16 v[112:115], a[52:55], a[148:151], v[112:115]\n"
"global_load_dwordx4 a[44:47], v0, s[34:35] offset:-1024 sc1\n"
"v_accvgpr_read_b32 v16, a204\n"
"v_mfma_f32_16x16x32_bf16 v[140:143], a[60:63], a[148:151], v[140:143]\n"
"ds_read_b128 a[148:151], v3 offset:13312\n"
"v_accvgpr_read_b32 v17, a205\n"
"v_mfma_f32_16x16x32_bf16 v[140:143], a[56:59], a[144:147], v[140:143]\n"
"ds_read_b128 a[144:147], v3 offset:12288\n"
"v_accvgpr_read_b32 v18, a206\n"
"v_mfma_f32_16x16x32_bf16 a[12:15], a[56:59], a[152:155], a[12:15]\n"
"v_accvgpr_read_b32 v19, a207\n"
"v_lshlrev_b32 v12, 16, v16\n"
"v_mfma_f32_16x16x32_bf16 a[12:15], a[60:63], a[156:159], a[12:15]\n"
"v_and_b32 v13, s63, v16\n"
"v_lshlrev_b32 v14, 16, v17\n"
"v_mfma_f32_16x16x32_bf16 a[8:11], a[52:55], a[156:159], a[8:11]\n"
"ds_read_b128 a[156:159], v3 offset:15360\n"
"v_and_b32 v15, s63, v17\n"
"v_mfma_f32_16x16x32_bf16 a[8:11], a[48:51], a[152:155], a[8:11]\n"
"s_waitcnt vmcnt(10) lgkmcnt(13)\n"
"ds_read_b128 a[152:155], v3 offset:14336\n"
"v_mul_f32 v12, v12, v204\n"
"v_mfma_f32_16x16x32_bf16 v[32:35], a[64:67], a[96:99], v[32:35]\n"
"ds_read_b128 v[20:23], v2 offset:6144\n"
"v_mul_f32 v13, v13, v205\n"
"v_mfma_f32_16x16x32_bf16 v[32:35], a[68:71], a[100:103], v[32:35]\n"
"global_load_dwordx4 a[48:51], v0, s[34:35] offset:0 sc1\n"
"v_mul_f32 v14, v14, v206\n"
"v_mfma_f32_16x16x32_bf16 v[60:63], a[76:79], a[100:103], v[60:63]\n"
"v_mul_f32 v15, v15, v207\n"
"v_cvt_pk_bf16_f32 v28, v12, v13\n"
"v_mfma_f32_16x16x32_bf16 v[60:63], a[72:75], a[96:99], v[60:63]\n"
"v_cvt_pk_bf16_f32 v29, v14, v15\n"
"v_mfma_f32_16x16x32_bf16 v[64:67], a[72:75], a[104:107], v[64:67]\n"
"ds_write_b64 v2, v[28:29] offset:19456\n"
"v_mfma_f32_16x16x32_bf16 v[64:67], a[76:79], a[108:111], v[64:67]\n"
"v_lshlrev_b32 v12, 16, v18\n"
"v_and_b32 v13, s63, v18\n"
"v_mfma_f32_16x16x32_bf16 v[36:39], a[68:71], a[108:111], v[36:39]\n"
"v_lshlrev_b32 v14, 16, v19\n"
"v_and_b32 v15, s63, v19\n"
"v_mfma_f32_16x16x32_bf16 v[36:39], a[64:67], a[104:107], v[36:39]\n"
"s_waitcnt lgkmcnt(12)\n"
"v_mul_f32 v12, v12, v232\n"
"v_mul_f32 v13, v13, v233\n"
"v_mfma_f32_16x16x32_bf16 v[40:43], a[64:67], a[112:115], v[40:43]\n"
"v_mul_f32 v14, v14, v234\n"
"v_mfma_f32_16x16x32_bf16 v[40:43], a[68:71], a[116:119], v[40:43]\n"
"global_load_dwordx4 a[52:55], v0, s[34:35] offset:1024 sc1\n"
"v_mul_f32 v15, v15, v235\n"
"v_mfma_f32_16x16x32_bf16 v[68:71], a[76:79], a[116:119], v[68:71]\n"
"v_cvt_pk_bf16_f32 v30, v12, v13\n"
"v_mfma_f32_16x16x32_bf16 v[68:71], a[72:75], a[112:115], v[68:71]\n"
"v_cvt_pk_bf16_f32 v31, v14, v15\n"
"v_mfma_f32_16x16x32_bf16 v[72:75], a[72:75], a[120:123], v[72:75]\n"
"s_waitcnt lgkmcnt(1)\n"
"global_store_dwordx4 v0, v[20:23], s[96:97] offset:2048 sc1\n"
"ds_write_b64 v2, v[30:31] offset:19464\n"
"v_accvgpr_read_b32 v16, a208\n"
"v_mfma_f32_16x16x32_bf16 v[72:75], a[76:79], a[124:127], v[72:75]\n"
"v_accvgpr_read_b32 v17, a209\n"
"v_accvgpr_read_b32 v18, a210\n"
"v_mfma_f32_16x16x32_bf16 v[44:47], a[68:71], a[124:127], v[44:47]\n"
"v_accvgpr_read_b32 v19, a211\n"
"v_mfma_f32_16x16x32_bf16 v[44:47], a[64:67], a[120:123], v[44:47]\n"
"v_lshlrev_b32 v12, 16, v16\n"
"v_and_b32 v13, s63, v16\n"
"v_mfma_f32_16x16x32_bf16 v[48:51], a[64:67], a[128:131], v[48:51]\n"
"v_lshlrev_b32 v14, 16, v17\n"
"v_mfma_f32_16x16x32_bf16 v[48:51], a[68:71], a[132:135], v[48:51]\n"
"global_load_dwordx4 a[56:59], v0, s[34:35] offset:2048 sc1\n"
"v_and_b32 v15, s63, v17\n"
"v_mfma_f32_16x16x32_bf16 v[76:79], a[76:79], a[132:135], v[76:79]\n"
"v_mul_f32 v12, v12, v152\n"
"v_mul_f32 v13, v13, v153\n"
"v_mfma_f32_16x16x32_bf16 v[76:79], a[72:75], a[128:131], v[76:79]\n"
"v_mul_f32 v14, v14, v154\n"
"v_mul_f32 v15, v15, v155\n"
"v_mfma_f32_16x16x32_bf16 v[80:83], a[72:75], a[136:139], v[80:83]\n"
"v_cvt_pk_bf16_f32 v24, v12, v13\n"
"v_cvt_pk_bf16_f32 v25, v14, v15\n"
"v_mfma_f32_16x16x32_bf16 v[80:83], a[76:79], a[140:143], v[80:83]\n"
"ds_write_b64 v2, v[24:25] offset:20480\n"
"v_mfma_f32_16x16x32_bf16 v[52:55], a[68:71], a[140:143], v[52:55]\n"
"v_lshlrev_b32 v12, 16, v18\n"
"v_mfma_f32_16x16x32_bf16 v[52:55], a[64:67], a[136:139], v[52:55]\n"
"v_and_b32 v13, s63, v18\n"
"v_lshlrev_b32 v14, 16, v19\n"
"v_mfma_f32_16x16x32_bf16 v[56:59], a[64:67], a[144:147], v[56:59]\n"
"v_and_b32 v15, s63, v19\n"
"v_mul_f32 v12, v12, v180\n"
"v_mfma_f32_16x16x32_bf16 v[56:59], a[68:71], a[148:151], v[56:59]\n"
"global_load_dwordx4 a[60:63], v0, s[34:35] offset:3072 sc1\n"
"v_mul_f32 v13, v13, v181\n"
"v_mfma_f32_16x16x32_bf16 v[84:87], a[76:79], a[148:151], v[84:87]\n"
"v_mul_f32 v14, v14, v182\n"
"v_mul_f32 v15, v15, v183\n"
"v_mfma_f32_16x16x32_bf16 v[84:87], a[72:75], a[144:147], v[84:87]\n"
"v_cvt_pk_bf16_f32 v26, v12, v13\n"
"v_cvt_pk_bf16_f32 v27, v14, v15\n"
"v_mfma_f32_16x16x32_bf16 a[4:7], a[72:75], a[152:155], a[4:7]\n"
"s_add_u32 s34, s34, 8192\n"
"ds_write_b64 v2, v[26:27] offset:20488\n"
"v_mfma_f32_16x16x32_bf16 a[4:7], a[76:79], a[156:159], a[4:7]\n"
"ds_read_b128 v[20:23], v2 offset:9216\n"
"v_mfma_f32_16x16x32_bf16 a[0:3], a[68:71], a[156:159], a[0:3]\n"
"v_accvgpr_read_b32 v16, a212\n"
"v_mfma_f32_16x16x32_bf16 a[0:3], a[64:67], a[152:155], a[0:3]\n"
"s_waitcnt vmcnt(10)\n"
"v_accvgpr_read_b32 v17, a213\n"
"v_accvgpr_read_b32 v18, a214\n"
"v_mfma_f32_16x16x32_bf16 v[88:91], a[80:83], a[96:99], v[88:91]\n"
"v_accvgpr_read_b32 v19, a215\n"
"v_lshlrev_b32 v12, 16, v16\n"
"v_mfma_f32_16x16x32_bf16 v[88:91], a[84:87], a[100:103], v[88:91]\n"
"global_load_dwordx4 a[64:67], v0, s[34:35] offset:-4096 sc1\n"
"v_and_b32 v13, s63, v16\n"
"v_mfma_f32_16x16x32_bf16 v[116:119], a[92:95], a[100:103], v[116:119]\n"
"ds_read_b128 a[100:103], v4 offset:1024\n"
"v_lshlrev_b32 v14, 16, v17\n"
"v_mfma_f32_16x16x32_bf16 v[116:119], a[88:91], a[96:99], v[116:119]\n"
"ds_read_b128 a[96:99], v4 offset:0\n"
"v_and_b32 v15, s63, v17\n"
"v_mfma_f32_16x16x32_bf16 v[120:123], a[88:91], a[104:107], v[120:123]\n"
"v_mul_f32 v12, v12, v208\n"
"v_mul_f32 v13, v13, v209\n"
"v_mfma_f32_16x16x32_bf16 v[120:123], a[92:95], a[108:111], v[120:123]\n"
"v_mul_f32 v14, v14, v210\n"
"v_mul_f32 v15, v15, v211\n"
"v_mfma_f32_16x16x32_bf16 v[92:95], a[84:87], a[108:111], v[92:95]\n"
"ds_read_b128 a[108:111], v4 offset:3072\n"
"v_cvt_pk_bf16_f32 v28, v12, v13\n"
"v_mfma_f32_16x16x32_bf16 v[92:95], a[80:83], a[104:107], v[92:95]\n"
"ds_read_b128 a[104:107], v4 offset:2048\n"
"v_cvt_pk_bf16_f32 v29, v14, v15\n"
"v_mfma_f32_16x16x32_bf16 v[96:99], a[80:83], a[112:115], v[96:99]\n"
"s_waitcnt lgkmcnt(4)\n"
"global_store_dwordx4 v0, v[20:23], s[98:99] offset:-3072 sc1\n"
"ds_write_b64 v2, v[28:29] offset:21504\n"
"v_lshlrev_b32 v12, 16, v18\n"
"v_mfma_f32_16x16x32_bf16 v[96:99], a[84:87], a[116:119], v[96:99]\n"
"global_load_dwordx4 a[68:71], v0, s[34:35] offset:-3072 sc1\n"
"v_and_b32 v13, s63, v18\n"
"v_mfma_f32_16x16x32_bf16 v[124:127], a[92:95], a[116:119], v[124:127]\n"
"ds_read_b128 a[116:119], v4 offset:5120\n"
"v_lshlrev_b32 v14, 16, v19\n"
"v_mfma_f32_16x16x32_bf16 v[124:127], a[88:91], a[112:115], v[124:127]\n"
"ds_read_b128 a[112:115], v4 offset:4096\n"
"v_and_b32 v15, s63, v19\n"
"v_mfma_f32_16x16x32_bf16 v[128:131], a[88:91], a[120:123], v[128:131]\n"
"v_mul_f32 v12, v12, v236\n"
"v_mul_f32 v13, v13, v237\n"
"v_mfma_f32_16x16x32_bf16 v[128:131], a[92:95], a[124:127], v[128:131]\n"
"v_mul_f32 v14, v14, v238\n"
"v_mul_f32 v15, v15, v239\n"
"v_mfma_f32_16x16x32_bf16 v[100:103], a[84:87], a[124:127], v[100:103]\n"
"ds_read_b128 a[124:127], v4 offset:7168\n"
"v_cvt_pk_bf16_f32 v30, v12, v13\n"
"v_mfma_f32_16x16x32_bf16 v[100:103], a[80:83], a[120:123], v[100:103]\n"
"ds_read_b128 a[120:123], v4 offset:6144\n"
"v_cvt_pk_bf16_f32 v31, v14, v15\n"
"v_mfma_f32_16x16x32_bf16 v[104:107], a[80:83], a[128:131], v[104:107]\n"
"ds_write_b64 v2, v[30:31] offset:21512\n"
"v_mfma_f32_16x16x32_bf16 v[104:107], a[84:87], a[132:135], v[104:107]\n"
"global_load_dwordx4 a[72:75], v0, s[34:35] offset:-2048 sc1\n"
"v_accvgpr_read_b32 v16, a216\n"
"v_mfma_f32_16x16x32_bf16 v[132:135], a[92:95], a[132:135], v[132:135]\n"
"ds_read_b128 a[132:135], v4 offset:9216\n"
"v_accvgpr_read_b32 v17, a217\n"
"v_mfma_f32_16x16x32_bf16 v[132:135], a[88:91], a[128:131], v[132:135]\n"
"ds_read_b128 a[128:131], v4 offset:8192\n"
"v_accvgpr_read_b32 v18, a218\n"
"v_mfma_f32_16x16x32_bf16 v[136:139], a[88:91], a[136:139], v[136:139]\n"
"v_accvgpr_read_b32 v19, a219\n"
"v_mfma_f32_16x16x32_bf16 v[136:139], a[92:95], a[140:143], v[136:139]\n"
"v_lshlrev_b32 v12, 16, v16\n"
"v_and_b32 v13, s63, v16\n"
"v_mfma_f32_16x16x32_bf16 v[108:111], a[84:87], a[140:143], v[108:111]\n"
"ds_read_b128 a[140:143], v4 offset:11264\n"
"v_lshlrev_b32 v14, 16, v17\n"
"v_mfma_f32_16x16x32_bf16 v[108:111], a[80:83], a[136:139], v[108:111]\n"
"ds_read_b128 a[136:139], v4 offset:10240\n"
"v_and_b32 v15, s63, v17\n"
"v_mfma_f32_16x16x32_bf16 v[112:115], a[80:83], a[144:147], v[112:115]\n"
"ds_read_b128 v[20:23], v2 offset:12288\n"
"v_mul_f32 v12, v12, v156\n"
"v_mfma_f32_16x16x32_bf16 v[112:115], a[84:87], a[148:151], v[112:115]\n"
"global_load_dwordx4 a[76:79], v0, s[34:35] offset:-1024 sc1\n"
"v_mfma_f32_16x16x32_bf16 v[140:143], a[92:95], a[148:151], v[140:143]\n"
"ds_read_b128 a[148:151], v4 offset:13312\n"
"v_mfma_f32_16x16x32_bf16 v[140:143], a[88:91], a[144:147], v[140:143]\n"
"ds_read_b128 a[144:147], v4 offset:12288\n"
"v_mfma_f32_16x16x32_bf16 a[12:15], a[88:91], a[152:155], a[12:15]\n"
"v_mul_f32 v13, v13, v157\n"
"v_mfma_f32_16x16x32_bf16 a[12:15], a[92:95], a[156:159], a[12:15]\n"
"v_mul_f32 v14, v14, v158\n"
"v_mfma_f32_16x16x32_bf16 a[8:11], a[84:87], a[156:159], a[8:11]\n"
"ds_read_b128 a[156:159], v4 offset:15360\n"
"v_mfma_f32_16x16x32_bf16 a[8:11], a[80:83], a[152:155], a[8:11]\n"
"s_waitcnt vmcnt(10) lgkmcnt(14)\n"
"ds_read_b128 a[152:155], v4 offset:14336\n"
"v_mfma_f32_16x16x32_bf16 v[32:35], a[32:35], a[96:99], v[32:35]\n"
"v_mul_f32 v15, v15, v159\n"
"v_mfma_f32_16x16x32_bf16 v[32:35], a[36:39], a[100:103], v[32:35]\n"
"global_load_dwordx4 a[80:83], v0, s[34:35] offset:0 sc1\n"
"v_mfma_f32_16x16x32_bf16 v[60:63], a[44:47], a[100:103], v[60:63]\n"
"s_add_u32 s65, s65, 1\n"
"v_cvt_pk_bf16_f32 v24, v12, v13\n"
"v_mfma_f32_16x16x32_bf16 v[60:63], a[40:43], a[96:99], v[60:63]\n"
"v_cvt_pk_bf16_f32 v25, v14, v15\n"
"v_mfma_f32_16x16x32_bf16 v[64:67], a[40:43], a[104:107], v[64:67]\n"
"s_waitcnt lgkmcnt(4)\n"
"global_store_dwordx4 v0, v[20:23], s[98:99] offset:0 sc1\n"
"ds_write_b64 v2, v[24:25] offset:22528\n"
"v_lshlrev_b32 v12, 16, v18\n"
"v_mfma_f32_16x16x32_bf16 v[64:67], a[44:47], a[108:111], v[64:67]\n"
"v_and_b32 v13, s63, v18\n"
"v_lshlrev_b32 v14, 16, v19\n"
"v_mfma_f32_16x16x32_bf16 v[36:39], a[36:39], a[108:111], v[36:39]\n"
"v_and_b32 v15, s63, v19\n"
"v_mul_f32 v12, v12, v184\n"
"v_mfma_f32_16x16x32_bf16 v[36:39], a[32:35], a[104:107], v[36:39]\n"
"v_mul_f32 v13, v13, v185\n"
"v_mul_f32 v14, v14, v186\n"
"v_mfma_f32_16x16x32_bf16 v[40:43], a[32:35], a[112:115], v[40:43]\n"
"v_mul_f32 v15, v15, v187\n"
"v_cvt_pk_bf16_f32 v26, v12, v13\n"
"v_mfma_f32_16x16x32_bf16 v[40:43], a[36:39], a[116:119], v[40:43]\n"
"global_load_dwordx4 a[84:87], v0, s[34:35] offset:1024 sc1\n"
"v_cvt_pk_bf16_f32 v27, v14, v15\n"
"v_mfma_f32_16x16x32_bf16 v[68:71], a[44:47], a[116:119], v[68:71]\n"
"ds_write_b64 v2, v[26:27] offset:22536\n"
"v_mfma_f32_16x16x32_bf16 v[68:71], a[40:43], a[112:115], v[68:71]\n"
"v_accvgpr_read_b32 v16, a220\n"
"v_accvgpr_read_b32 v17, a221\n"
"v_mfma_f32_16x16x32_bf16 v[72:75], a[40:43], a[120:123], v[72:75]\n"
"v_accvgpr_read_b32 v18, a222\n"
"v_accvgpr_read_b32 v19, a223\n"
"v_mfma_f32_16x16x32_bf16 v[72:75], a[44:47], a[124:127], v[72:75]\n"
"v_lshlrev_b32 v12, 16, v16\n"
"v_and_b32 v13, s63, v16\n"
"v_mfma_f32_16x16x32_bf16 v[44:47], a[36:39], a[124:127], v[44:47]\n"
"v_lshlrev_b32 v14, 16, v17\n"
"v_and_b32 v15, s63, v17\n"
"v_mfma_f32_16x16x32_bf16 v[44:47], a[32:35], a[120:123], v[44:47]\n"
"v_mul_f32 v12, v12, v212\n"
"v_mul_f32 v13, v13, v213\n"
"v_mfma_f32_16x16x32_bf16 v[48:51], a[32:35], a[128:131], v[48:51]\n"
"v_mul_f32 v14, v14, v214\n"
"v_mul_f32 v15, v15, v215\n"
"v_mfma_f32_16x16x32_bf16 v[48:51], a[36:39], a[132:135], v[48:51]\n"
"global_load_dwordx4 a[88:91], v0, s[34:35] offset:2048 sc1\n"
"v_cvt_pk_bf16_f32 v28, v12, v13\n"
"v_mfma_f32_16x16x32_bf16 v[76:79], a[44:47], a[132:135], v[76:79]\n"
"v_cvt_pk_bf16_f32 v29, v14, v15\n"
"v_mfma_f32_16x16x32_bf16 v[76:79], a[40:43], a[128:131], v[76:79]\n"
"ds_write_b64 v2, v[28:29] offset:23552\n"
"v_mfma_f32_16x16x32_bf16 v[80:83], a[40:43], a[136:139], v[80:83]\n"
"v_lshlrev_b32 v12, 16, v18\n"
"v_and_b32 v13, s63, v18\n"
"v_mfma_f32_16x16x32_bf16 v[80:83], a[44:47], a[140:143], v[80:83]\n"
"v_lshlrev_b32 v14, 16, v19\n"
"v_and_b32 v15, s63, v19\n"
"v_mfma_f32_16x16x32_bf16 v[52:55], a[36:39], a[140:143], v[52:55]\n"
"v_mul_f32 v12, v12, v240\n"
"v_mul_f32 v13, v13, v241\n"
"v_mfma_f32_16x16x32_bf16 v[52:55], a[32:35], a[136:139], v[52:55]\n"
"s_waitcnt lgkmcnt(3)\n"
"v_mul_f32 v14, v14, v242\n"
"v_mul_f32 v15, v15, v243\n"
"v_mfma_f32_16x16x32_bf16 v[56:59], a[32:35], a[144:147], v[56:59]\n"
"v_cvt_pk_bf16_f32 v30, v12, v13\n"
"v_cvt_pk_bf16_f32 v31, v14, v15\n"
"v_mfma_f32_16x16x32_bf16 v[56:59], a[36:39], a[148:151], v[56:59]\n"
"global_load_dwordx4 a[92:95], v0, s[34:35] offset:3072 sc1\n"
"v_mfma_f32_16x16x32_bf16 v[84:87], a[44:47], a[148:151], v[84:87]\n"
"ds_write_b64 v2, v[30:31] offset:23560\n"
"v_mfma_f32_16x16x32_bf16 v[84:87], a[40:43], a[144:147], v[84:87]\n"
"v_accvgpr_read_b32 v16, a224\n"
"v_accvgpr_read_b32 v17, a225\n"
"v_mfma_f32_16x16x32_bf16 a[4:7], a[40:43], a[152:155], a[4:7]\n"
"s_add_u32 s34, s34, 8192\n"
"v_accvgpr_read_b32 v18, a226\n"
"v_accvgpr_read_b32 v19, a227\n"
"v_mfma_f32_16x16x32_bf16 a[4:7], a[44:47], a[156:159], a[4:7]\n"
"v_lshlrev_b32 v12, 16, v16\n"
"v_and_b32 v13, s63, v16\n"
"v_mfma_f32_16x16x32_bf16 a[0:3], a[36:39], a[156:159], a[0:3]\n"
"v_lshlrev_b32 v14, 16, v17\n"
"v_and_b32 v15, s63, v17\n"
"v_mfma_f32_16x16x32_bf16 a[0:3], a[32:35], a[152:155], a[0:3]\n"
"s_waitcnt vmcnt(10)\n"
"v_mul_f32 v12, v12, v160\n"
"v_mul_f32 v13, v13, v161\n"
"v_mfma_f32_16x16x32_bf16 v[88:91], a[48:51], a[96:99], v[88:91]\n"
"v_mul_f32 v14, v14, v162\n"
"v_mul_f32 v15, v15, v163\n"
"v_mfma_f32_16x16x32_bf16 v[88:91], a[52:55], a[100:103], v[88:91]\n"
"global_load_dwordx4 a[32:35], v0, s[34:35] offset:-4096 sc1\n"
"v_cvt_pk_bf16_f32 v24, v12, v13\n"
"v_mfma_f32_16x16x32_bf16 v[116:119], a[60:63], a[100:103], v[116:119]\n"
"ds_read_b128 a[100:103], v5 offset:1024\n"
"v_cvt_pk_bf16_f32 v25, v14, v15\n"
"v_mfma_f32_16x16x32_bf16 v[116:119], a[56:59], a[96:99], v[116:119]\n"
"ds_read_b128 a[96:99], v5 offset:0\n"
"v_mfma_f32_16x16x32_bf16 v[120:123], a[56:59], a[104:107], v[120:123]\n"
"ds_write_b64 v2, v[24:25] offset:24576\n"
"v_mfma_f32_16x16x32_bf16 v[120:123], a[60:63], a[108:111], v[120:123]\n"
"v_lshlrev_b32 v12, 16, v18\n"
"v_and_b32 v13, s63, v18\n"
"v_mfma_f32_16x16x32_bf16 v[92:95], a[52:55], a[108:111], v[92:95]\n"
"ds_read_b128 a[108:111], v5 offset:3072\n"
"v_lshlrev_b32 v14, 16, v19\n"
"v_mfma_f32_16x16x32_bf16 v[92:95], a[48:51], a[104:107], v[92:95]\n"
"ds_read_b128 a[104:107], v5 offset:2048\n"
"v_and_b32 v15, s63, v19\n"
"v_mfma_f32_16x16x32_bf16 v[96:99], a[48:51], a[112:115], v[96:99]\n"
"v_mul_f32 v12, v12, v188\n"
"v_mul_f32 v13, v13, v189\n"
"v_mfma_f32_16x16x32_bf16 v[96:99], a[52:55], a[116:119], v[96:99]\n"
"global_load_dwordx4 a[36:39], v0, s[34:35] offset:-3072 sc1\n"
"v_mul_f32 v14, v14, v190\n"
"v_mfma_f32_16x16x32_bf16 v[124:127], a[60:63], a[116:119], v[124:127]\n"
"ds_read_b128 a[116:119], v5 offset:5120\n"
"v_mul_f32 v15, v15, v191\n"
"v_mfma_f32_16x16x32_bf16 v[124:127], a[56:59], a[112:115], v[124:127]\n"
"ds_read_b128 a[112:115], v5 offset:4096\n"
"v_cvt_pk_bf16_f32 v26, v12, v13\n"
"v_mfma_f32_16x16x32_bf16 v[128:131], a[56:59], a[120:123], v[128:131]\n"
"v_cvt_pk_bf16_f32 v27, v14, v15\n"
"v_mfma_f32_16x16x32_bf16 v[128:131], a[60:63], a[124:127], v[128:131]\n"
"ds_write_b64 v2, v[26:27] offset:24584\n"
"v_mfma_f32_16x16x32_bf16 v[100:103], a[52:55], a[124:127], v[100:103]\n"
"ds_read_b128 a[124:127], v5 offset:7168\n"
"v_accvgpr_read_b32 v16, a228\n"
"v_mfma_f32_16x16x32_bf16 v[100:103], a[48:51], a[120:123], v[100:103]\n"
"ds_read_b128 a[120:123], v5 offset:6144\n"
"v_accvgpr_read_b32 v17, a229\n"
"v_mfma_f32_16x16x32_bf16 v[104:107], a[48:51], a[128:131], v[104:107]\n"
"v_accvgpr_read_b32 v18, a230\n"
"v_accvgpr_read_b32 v19, a231\n"
"v_mfma_f32_16x16x32_bf16 v[104:107], a[52:55], a[132:135], v[104:107]\n"
"global_load_dwordx4 a[40:43], v0, s[34:35] offset:-2048 sc1\n"
"v_lshlrev_b32 v12, 16, v16\n"
"v_mfma_f32_16x16x32_bf16 v[132:135], a[60:63], a[132:135], v[132:135]\n"
"ds_read_b128 a[132:135], v5 offset:9216\n"
"v_and_b32 v13, s63, v16\n"
"v_mfma_f32_16x16x32_bf16 v[132:135], a[56:59], a[128:131], v[132:135]\n"
"ds_read_b128 a[128:131], v5 offset:8192\n"
"v_lshlrev_b32 v14, 16, v17\n"
"v_mfma_f32_16x16x32_bf16 v[136:139], a[56:59], a[136:139], v[136:139]\n"
"v_and_b32 v15, s63, v17\n"
"v_mul_f32 v12, v12, v216\n"
"v_mfma_f32_16x16x32_bf16 v[136:139], a[60:63], a[140:143], v[136:139]\n"
"v_mul_f32 v13, v13, v217\n"
"v_mul_f32 v14, v14, v218\n"
"v_mfma_f32_16x16x32_bf16 v[108:111], a[52:55], a[140:143], v[108:111]\n"
"ds_read_b128 a[140:143], v5 offset:11264\n"
"v_mul_f32 v15, v15, v219\n"
"v_mfma_f32_16x16x32_bf16 v[108:111], a[48:51], a[136:139], v[108:111]\n"
"ds_read_b128 a[136:139], v5 offset:10240\n"
"v_cvt_pk_bf16_f32 v28, v12, v13\n"
"v_mfma_f32_16x16x32_bf16 v[112:115], a[48:51], a[144:147], v[112:115]\n"
"v_cvt_pk_bf16_f32 v29, v14, v15\n"
"v_lshlrev_b32 v12, 16, v18\n"
"v_mfma_f32_16x16x32_bf16 v[112:115], a[52:55], a[148:151], v[112:115]\n"
"global_load_dwordx4 a[44:47], v0, s[34:35] offset:-1024 sc1\n"
"v_and_b32 v13, s63, v18\n"
"v_mfma_f32_16x16x32_bf16 v[140:143], a[60:63], a[148:151], v[140:143]\n"
"ds_read_b128 a[148:151], v5 offset:13312\n"
"v_lshlrev_b32 v14, 16, v19\n"
"v_mfma_f32_16x16x32_bf16 v[140:143], a[56:59], a[144:147], v[140:143]\n"
"ds_read_b128 a[144:147], v5 offset:12288\n"
"v_and_b32 v15, s63, v19\n"
"v_mfma_f32_16x16x32_bf16 a[12:15], a[56:59], a[152:155], a[12:15]\n"
"v_mul_f32 v12, v12, v244\n"
"v_mul_f32 v13, v13, v245\n"
"v_mfma_f32_16x16x32_bf16 a[12:15], a[60:63], a[156:159], a[12:15]\n"
"v_mul_f32 v14, v14, v246\n"
"v_mul_f32 v15, v15, v247\n"
"v_mfma_f32_16x16x32_bf16 a[8:11], a[52:55], a[156:159], a[8:11]\n"
"ds_read_b128 a[156:159], v5 offset:15360\n"
"v_cvt_pk_bf16_f32 v30, v12, v13\n"
"v_mfma_f32_16x16x32_bf16 a[8:11], a[48:51], a[152:155], a[8:11]\n"
"s_waitcnt vmcnt(9) lgkmcnt(12)\n"
"ds_read_b128 a[152:155], v5 offset:14336\n"
"v_cvt_pk_bf16_f32 v31, v14, v15\n"
"v_mfma_f32_16x16x32_bf16 v[32:35], a[64:67], a[96:99], v[32:35]\n"
"global_store_dwordx4 v0, v[28:31], s[54:55] offset:-3072 sc1\n"
"ds_write_b128 v2, v[28:31] offset:25600\n"
"v_mfma_f32_16x16x32_bf16 v[32:35], a[68:71], a[100:103], v[32:35]\n"
"global_load_dwordx4 a[48:51], v0, s[34:35] offset:0 sc1\n"
"v_accvgpr_read_b32 v16, a232\n"
"v_mfma_f32_16x16x32_bf16 v[60:63], a[76:79], a[100:103], v[60:63]\n"
"v_accvgpr_read_b32 v17, a233\n"
"v_accvgpr_read_b32 v18, a234\n"
"v_mfma_f32_16x16x32_bf16 v[60:63], a[72:75], a[96:99], v[60:63]\n"
"v_accvgpr_read_b32 v19, a235\n"
"v_lshlrev_b32 v12, 16, v16\n"
"v_mfma_f32_16x16x32_bf16 v[64:67], a[72:75], a[104:107], v[64:67]\n"
"v_and_b32 v13, s63, v16\n"
"v_lshlrev_b32 v14, 16, v17\n"
"v_mfma_f32_16x16x32_bf16 v[64:67], a[76:79], a[108:111], v[64:67]\n"
"v_and_b32 v15, s63, v17\n"
"v_mul_f32 v12, v12, v164\n"
"v_mfma_f32_16x16x32_bf16 v[36:39], a[68:71], a[108:111], v[36:39]\n"
"v_mul_f32 v13, v13, v165\n"
"v_mul_f32 v14, v14, v166\n"
"v_mfma_f32_16x16x32_bf16 v[36:39], a[64:67], a[104:107], v[36:39]\n"
"s_waitcnt lgkmcnt(9)\n"
"v_mul_f32 v15, v15, v167\n"
"v_cvt_pk_bf16_f32 v24, v12, v13\n"
"v_mfma_f32_16x16x32_bf16 v[40:43], a[64:67], a[112:115], v[40:43]\n"
"v_cvt_pk_bf16_f32 v25, v14, v15\n"
"v_mfma_f32_16x16x32_bf16 v[40:43], a[68:71], a[116:119], v[40:43]\n"
"global_load_dwordx4 a[52:55], v0, s[34:35] offset:1024 sc1\n"
"v_mfma_f32_16x16x32_bf16 v[68:71], a[76:79], a[116:119], v[68:71]\n"
"ds_write_b64 v2, v[24:25] offset:26624\n"
"v_mfma_f32_16x16x32_bf16 v[68:71], a[72:75], a[112:115], v[68:71]\n"
"v_lshlrev_b32 v12, 16, v18\n"
"v_and_b32 v13, s63, v18\n"
"v_mfma_f32_16x16x32_bf16 v[72:75], a[72:75], a[120:123], v[72:75]\n"
"v_lshlrev_b32 v14, 16, v19\n"
"v_and_b32 v15, s63, v19\n"
"v_mfma_f32_16x16x32_bf16 v[72:75], a[76:79], a[124:127], v[72:75]\n"
"v_mul_f32 v12, v12, v192\n"
"v_mul_f32 v13, v13, v193\n"
"v_mfma_f32_16x16x32_bf16 v[44:47], a[68:71], a[124:127], v[44:47]\n"
"v_mul_f32 v14, v14, v194\n"
"v_mul_f32 v15, v15, v195\n"
"v_mfma_f32_16x16x32_bf16 v[44:47], a[64:67], a[120:123], v[44:47]\n"
"s_waitcnt lgkmcnt(6)\n"
"v_cvt_pk_bf16_f32 v26, v12, v13\n"
"v_cvt_pk_bf16_f32 v27, v14, v15\n"
"v_mfma_f32_16x16x32_bf16 v[48:51], a[64:67], a[128:131], v[48:51]\n"
"ds_write_b64 v2, v[26:27] offset:26632\n"
"v_mfma_f32_16x16x32_bf16 v[48:51], a[68:71], a[132:135], v[48:51]\n"
"global_load_dwordx4 a[56:59], v0, s[34:35] offset:2048 sc1\n"
"v_accvgpr_read_b32 v16, a236\n"
"v_mfma_f32_16x16x32_bf16 v[76:79], a[76:79], a[132:135], v[76:79]\n"
"v_accvgpr_read_b32 v17, a237\n"
"v_accvgpr_read_b32 v18, a238\n"
"v_mfma_f32_16x16x32_bf16 v[76:79], a[72:75], a[128:131], v[76:79]\n"
"v_accvgpr_read_b32 v19, a239\n"
"v_lshlrev_b32 v12, 16, v16\n"
"v_mfma_f32_16x16x32_bf16 v[80:83], a[72:75], a[136:139], v[80:83]\n"
"v_and_b32 v13, s63, v16\n"
"v_mfma_f32_16x16x32_bf16 v[80:83], a[76:79], a[140:143], v[80:83]\n"
"v_lshlrev_b32 v14, 16, v17\n"
"v_mfma_f32_16x16x32_bf16 v[52:55], a[68:71], a[140:143], v[52:55]\n"
"v_and_b32 v15, s63, v17\n"
"v_mul_f32 v12, v12, v220\n"
"v_mfma_f32_16x16x32_bf16 v[52:55], a[64:67], a[136:139], v[52:55]\n"
"s_waitcnt lgkmcnt(3)\n"
"v_mul_f32 v13, v13, v221\n"
"v_mfma_f32_16x16x32_bf16 v[56:59], a[64:67], a[144:147], v[56:59]\n"
"v_mul_f32 v14, v14, v222\n"
"v_mul_f32 v15, v15, v223\n"
"v_mfma_f32_16x16x32_bf16 v[56:59], a[68:71], a[148:151], v[56:59]\n"
"global_load_dwordx4 a[60:63], v0, s[34:35] offset:3072 sc1\n"
"v_cvt_pk_bf16_f32 v28, v12, v13\n"
"v_mfma_f32_16x16x32_bf16 v[84:87], a[76:79], a[148:151], v[84:87]\n"
"v_cvt_pk_bf16_f32 v29, v14, v15\n"
"v_mfma_f32_16x16x32_bf16 v[84:87], a[72:75], a[144:147], v[84:87]\n"
"v_lshlrev_b32 v12, 16, v18\n"
"v_and_b32 v13, s63, v18\n"
"v_mfma_f32_16x16x32_bf16 a[4:7], a[72:75], a[152:155], a[4:7]\n"
"s_add_u32 s34, s34, 8192\n"
"v_lshlrev_b32 v14, 16, v19\n"
"v_and_b32 v15, s63, v19\n"
"v_mfma_f32_16x16x32_bf16 a[4:7], a[76:79], a[156:159], a[4:7]\n"
"v_mul_f32 v12, v12, v248\n"
"v_mul_f32 v13, v13, v249\n"
"v_mfma_f32_16x16x32_bf16 a[0:3], a[68:71], a[156:159], a[0:3]\n"
"v_mul_f32 v14, v14, v250\n"
"v_mul_f32 v15, v15, v251\n"
"v_mfma_f32_16x16x32_bf16 a[0:3], a[64:67], a[152:155], a[0:3]\n"
"s_waitcnt vmcnt(9)\n"
"v_cvt_pk_bf16_f32 v30, v12, v13\n"
"v_cvt_pk_bf16_f32 v31, v14, v15\n"
"v_mfma_f32_16x16x32_bf16 v[88:91], a[80:83], a[96:99], v[88:91]\n"
"global_store_dwordx4 v0, v[28:31], s[54:55] offset:-1024 sc1\n"
"ds_write_b128 v2, v[28:31] offset:27648\n"
"v_mfma_f32_16x16x32_bf16 v[88:91], a[84:87], a[100:103], v[88:91]\n"
"global_load_dwordx4 a[64:67], v0, s[34:35] offset:-4096 sc1\n"
"v_mfma_f32_16x16x32_bf16 v[116:119], a[92:95], a[100:103], v[116:119]\n"
"ds_read_b128 a[100:103], v2 offset:1024\n"
"v_accvgpr_read_b32 v16, a240\n"
"v_mfma_f32_16x16x32_bf16 v[116:119], a[88:91], a[96:99], v[116:119]\n"
"ds_read_b128 a[96:99], v2 offset:0\n"
"v_accvgpr_read_b32 v17, a241\n"
"v_mfma_f32_16x16x32_bf16 v[120:123], a[88:91], a[104:107], v[120:123]\n"
"v_accvgpr_read_b32 v18, a242\n"
"v_accvgpr_read_b32 v19, a243\n"
"v_mfma_f32_16x16x32_bf16 v[120:123], a[92:95], a[108:111], v[120:123]\n"
"v_lshlrev_b32 v12, 16, v16\n"
"v_and_b32 v13, s63, v16\n"
"v_mfma_f32_16x16x32_bf16 v[92:95], a[84:87], a[108:111], v[92:95]\n"
"ds_read_b128 a[108:111], v2 offset:3072\n"
"v_lshlrev_b32 v14, 16, v17\n"
"v_mfma_f32_16x16x32_bf16 v[92:95], a[80:83], a[104:107], v[92:95]\n"
"ds_read_b128 a[104:107], v2 offset:2048\n"
"v_and_b32 v15, s63, v17\n"
"v_mfma_f32_16x16x32_bf16 v[96:99], a[80:83], a[112:115], v[96:99]\n"
"v_mul_f32 v12, v12, v168\n"
"v_mul_f32 v13, v13, v169\n"
"v_mfma_f32_16x16x32_bf16 v[96:99], a[84:87], a[116:119], v[96:99]\n"
"global_load_dwordx4 a[68:71], v0, s[34:35] offset:-3072 sc1\n"
"v_mul_f32 v14, v14, v170\n"
"v_mfma_f32_16x16x32_bf16 v[124:127], a[92:95], a[116:119], v[124:127]\n"
"ds_read_b128 a[116:119], v2 offset:5120\n"
"v_mul_f32 v15, v15, v171\n"
"v_mfma_f32_16x16x32_bf16 v[124:127], a[88:91], a[112:115], v[124:127]\n"
"ds_read_b128 a[112:115], v2 offset:4096\n"
"v_cvt_pk_bf16_f32 v24, v12, v13\n"
"v_mfma_f32_16x16x32_bf16 v[128:131], a[88:91], a[120:123], v[128:131]\n"
"v_cvt_pk_bf16_f32 v25, v14, v15\n"
"v_lshlrev_b32 v12, 16, v18\n"
"v_mfma_f32_16x16x32_bf16 v[128:131], a[92:95], a[124:127], v[128:131]\n"
"v_and_b32 v13, s63, v18\n"
"v_lshlrev_b32 v14, 16, v19\n"
"v_mfma_f32_16x16x32_bf16 v[100:103], a[84:87], a[124:127], v[100:103]\n"
"ds_read_b128 a[124:127], v2 offset:7168\n"
"v_and_b32 v15, s63, v19\n"
"v_mfma_f32_16x16x32_bf16 v[100:103], a[80:83], a[120:123], v[100:103]\n"
"ds_read_b128 a[120:123], v2 offset:6144\n"
"v_mul_f32 v12, v12, v196\n"
"v_mfma_f32_16x16x32_bf16 v[104:107], a[80:83], a[128:131], v[104:107]\n"
"v_mul_f32 v13, v13, v197\n"
"v_mul_f32 v14, v14, v198\n"
"v_mfma_f32_16x16x32_bf16 v[104:107], a[84:87], a[132:135], v[104:107]\n"
"global_load_dwordx4 a[72:75], v0, s[34:35] offset:-2048 sc1\n"
"v_mul_f32 v15, v15, v199\n"
"v_mfma_f32_16x16x32_bf16 v[132:135], a[92:95], a[132:135], v[132:135]\n"
"ds_read_b128 a[132:135], v2 offset:9216\n"
"v_cvt_pk_bf16_f32 v26, v12, v13\n"
"v_mfma_f32_16x16x32_bf16 v[132:135], a[88:91], a[128:131], v[132:135]\n"
"ds_read_b128 a[128:131], v2 offset:8192\n"
"v_cvt_pk_bf16_f32 v27, v14, v15\n"
"v_mfma_f32_16x16x32_bf16 v[136:139], a[88:91], a[136:139], v[136:139]\n"
"global_store_dwordx4 v0, v[24:27], s[54:55] offset:0 sc1\n"
"ds_write_b128 v2, v[24:27] offset:28672\n"
"v_mfma_f32_16x16x32_bf16 v[136:139], a[92:95], a[140:143], v[136:139]\n"
"v_accvgpr_read_b32 v16, a248\n"
"v_mfma_f32_16x16x32_bf16 v[108:111], a[84:87], a[140:143], v[108:111]\n"
"ds_read_b128 a[140:143], v2 offset:11264\n"
"v_mfma_f32_16x16x32_bf16 v[108:111], a[80:83], a[136:139], v[108:111]\n"
"ds_read_b128 a[136:139], v2 offset:10240\n"
"v_accvgpr_read_b32 v17, a249\n"
"v_mfma_f32_16x16x32_bf16 v[112:115], a[80:83], a[144:147], v[112:115]\n"
"v_accvgpr_read_b32 v18, a250\n"
"v_accvgpr_read_b32 v19, a251\n"
"v_mfma_f32_16x16x32_bf16 v[112:115], a[84:87], a[148:151], v[112:115]\n"
"global_load_dwordx4 a[76:79], v0, s[34:35] offset:-1024 sc1\n"
"v_lshlrev_b32 v12, 16, v16\n"
"v_mfma_f32_16x16x32_bf16 v[140:143], a[92:95], a[148:151], v[140:143]\n"
"ds_read_b128 a[148:151], v2 offset:13312\n"
"v_and_b32 v13, s63, v16\n"
"v_mfma_f32_16x16x32_bf16 v[140:143], a[88:91], a[144:147], v[140:143]\n"
"ds_read_b128 a[144:147], v2 offset:12288\n"
"v_lshlrev_b32 v14, 16, v17\n"
"v_mfma_f32_16x16x32_bf16 a[12:15], a[88:91], a[152:155], a[12:15]\n"
"v_and_b32 v15, s63, v17\n"
"v_accvgpr_read_b32 v10, a16\n"
"v_mfma_f32_16x16x32_bf16 a[12:15], a[92:95], a[156:159], a[12:15]\n"
"v_accvgpr_read_b32 v11, a17\n"
"v_mul_f32 v12, v12, v10\n"
"v_mfma_f32_16x16x32_bf16 a[8:11], a[84:87], a[156:159], a[8:11]\n"
"ds_read_b128 a[156:159], v2 offset:15360\n"
"v_mul_f32 v13, v13, v11\n"
"v_mfma_f32_16x16x32_bf16 a[8:11], a[80:83], a[152:155], a[8:11]\n"
"s_waitcnt vmcnt(11) lgkmcnt(12)\n"
"ds_read_b128 a[152:155], v2 offset:14336\n"
"v_accvgpr_read_b32 v10, a18\n"
"v_mfma_f32_16x16x32_bf16 v[144:147], a[32:35], a[96:99], v[144:147]\n"
"v_accvgpr_read_b32 v11, a19\n"
"v_mul_f32 v14, v14, v10\n"
"v_mfma_f32_16x16x32_bf16 v[144:147], a[36:39], a[100:103], v[144:147]\n"
"global_load_dwordx4 a[80:83], v0, s[34:35] offset:0 sc1\n"
"v_mul_f32 v15, v15, v11\n"
"v_mfma_f32_16x16x32_bf16 v[172:175], a[44:47], a[100:103], v[172:175]\n"
"v_cvt_pk_bf16_f32 v28, v12, v13\n"
"v_mfma_f32_16x16x32_bf16 v[172:175], a[40:43], a[96:99], v[172:175]\n"
"v_cvt_pk_bf16_f32 v29, v14, v15\n"
"v_lshlrev_b32 v12, 16, v18\n"
"v_mfma_f32_16x16x32_bf16 v[176:179], a[40:43], a[104:107], v[176:179]\n"
"v_and_b32 v13, s63, v18\n"
"v_mfma_f32_16x16x32_bf16 v[176:179], a[44:47], a[108:111], v[176:179]\n"
"v_lshlrev_b32 v14, 16, v19\n"
"v_mfma_f32_16x16x32_bf16 v[148:151], a[36:39], a[108:111], v[148:151]\n"
"v_and_b32 v15, s63, v19\n"
"v_accvgpr_read_b32 v10, a20\n"
"v_mfma_f32_16x16x32_bf16 v[148:151], a[32:35], a[104:107], v[148:151]\n"
"s_waitcnt lgkmcnt(9)\n"
"v_accvgpr_read_b32 v11, a21\n"
"v_mfma_f32_16x16x32_bf16 v[152:155], a[32:35], a[112:115], v[152:155]\n"
"v_mul_f32 v12, v12, v10\n"
"v_mul_f32 v13, v13, v11\n"
"v_mfma_f32_16x16x32_bf16 v[152:155], a[36:39], a[116:119], v[152:155]\n"
"global_load_dwordx4 a[84:87], v0, s[34:35] offset:1024 sc1\n"
"v_accvgpr_read_b32 v10, a22\n"
"v_mfma_f32_16x16x32_bf16 v[180:183], a[44:47], a[116:119], v[180:183]\n"
"v_accvgpr_read_b32 v11, a23\n"
"v_mfma_f32_16x16x32_bf16 v[180:183], a[40:43], a[112:115], v[180:183]\n"
"v_mul_f32 v14, v14, v10\n"
"v_mul_f32 v15, v15, v11\n"
"v_mfma_f32_16x16x32_bf16 v[184:187], a[40:43], a[120:123], v[184:187]\n"
"v_cvt_pk_bf16_f32 v30, v12, v13\n"
"v_cvt_pk_bf16_f32 v31, v14, v15\n"
"v_mfma_f32_16x16x32_bf16 v[184:187], a[44:47], a[124:127], v[184:187]\n"
"global_store_dwordx4 v0, v[28:31], s[54:55] offset:2048 sc1\n"
"ds_write_b128 v2, v[28:31] offset:30720\n"
"v_mfma_f32_16x16x32_bf16 v[156:159], a[36:39], a[124:127], v[156:159]\n"
"v_accvgpr_read_b32 v16, a244\n"
"v_mfma_f32_16x16x32_bf16 v[156:159], a[32:35], a[120:123], v[156:159]\n"
"s_waitcnt lgkmcnt(5)\n"
"v_accvgpr_read_b32 v17, a245\n"
"v_accvgpr_read_b32 v18, a246\n"
"v_mfma_f32_16x16x32_bf16 v[160:163], a[32:35], a[128:131], v[160:163]\n"
"v_accvgpr_read_b32 v19, a247\n"
"v_lshlrev_b32 v12, 16, v16\n"
"v_mfma_f32_16x16x32_bf16 v[160:163], a[36:39], a[132:135], v[160:163]\n"
"global_load_dwordx4 a[88:91], v0, s[34:35] offset:2048 sc1\n"
"v_and_b32 v13, s63, v16\n"
"v_mfma_f32_16x16x32_bf16 v[188:191], a[44:47], a[132:135], v[188:191]\n"
"v_lshlrev_b32 v14, 16, v17\n"
"v_and_b32 v15, s63, v17\n"
"v_mfma_f32_16x16x32_bf16 v[188:191], a[40:43], a[128:131], v[188:191]\n"
"v_mul_f32 v12, v12, v224\n"
"v_mul_f32 v13, v13, v225\n"
"v_mfma_f32_16x16x32_bf16 v[192:195], a[40:43], a[136:139], v[192:195]\n"
"v_mul_f32 v14, v14, v226\n"
"v_mul_f32 v15, v15, v227\n"
"v_mfma_f32_16x16x32_bf16 v[192:195], a[44:47], a[140:143], v[192:195]\n"
"v_cvt_pk_bf16_f32 v24, v12, v13\n"
"v_cvt_pk_bf16_f32 v25, v14, v15\n"
"v_mfma_f32_16x16x32_bf16 v[164:167], a[36:39], a[140:143], v[164:167]\n"
"ds_write_b64 v2, v[24:25] offset:29696\n"
"v_mfma_f32_16x16x32_bf16 v[164:167], a[32:35], a[136:139], v[164:167]\n"
"s_waitcnt lgkmcnt(2)\n"
"v_mov_b32 v8, s65\n"
"v_mfma_f32_16x16x32_bf16 v[168:171], a[32:35], a[144:147], v[168:171]\n"
"v_mov_b32 v9, s60\n"
"v_mfma_f32_16x16x32_bf16 v[168:171], a[36:39], a[148:151], v[168:171]\n"
"global_load_dwordx4 a[92:95], v0, s[34:35] offset:3072 sc1\n"
"v_lshlrev_b32 v12, 16, v18\n"
"v_mfma_f32_16x16x32_bf16 v[196:199], a[44:47], a[148:151], v[196:199]\n"
"buffer_store_dwordx2 v[8:9], off, s[56:59], %13 sc1\n"
"v_and_b32 v13, s63, v18\n"
"v_mfma_f32_16x16x32_bf16 v[196:199], a[40:43], a[144:147], v[196:199]\n"
"v_lshlrev_b32 v14, 16, v19\n"
"v_and_b32 v15, s63, v19\n"
"v_mfma_f32_16x16x32_bf16 a[20:23], a[40:43], a[152:155], a[20:23]\n"
"s_add_u32 s34, s34, 8192\n"
"v_mul_f32 v12, v12, v252\n"
"v_mul_f32 v13, v13, v253\n"
"v_mfma_f32_16x16x32_bf16 a[20:23], a[44:47], a[156:159], a[20:23]\n"
"v_mul_f32 v14, v14, v254\n"
"v_mul_f32 v15, v15, v255\n"
"v_mfma_f32_16x16x32_bf16 a[16:19], a[36:39], a[156:159], a[16:19]\n"
"v_cvt_pk_bf16_f32 v26, v12, v13\n"
"v_cvt_pk_bf16_f32 v27, v14, v15\n"
"v_mfma_f32_16x16x32_bf16 a[16:19], a[32:35], a[152:155], a[16:19]\n"
"s_waitcnt vmcnt(12)\n"
"ds_write_b64 v2, v[26:27] offset:29704\n"
"v_mfma_f32_16x16x32_bf16 v[200:203], a[48:51], a[96:99], v[200:203]\n"
"v_accvgpr_read_b32 v16, a252\n"
"v_mfma_f32_16x16x32_bf16 v[200:203], a[52:55], a[100:103], v[200:203]\n"
"global_load_dwordx4 a[32:35], v0, s[34:35] offset:-4096 sc1\n"
"v_accvgpr_read_b32 v17, a253\n"
"v_mfma_f32_16x16x32_bf16 v[228:231], a[60:63], a[100:103], v[228:231]\n"
"ds_read_b128 a[100:103], v3 offset:1024\n"
"v_accvgpr_read_b32 v18, a254\n"
"v_mfma_f32_16x16x32_bf16 v[228:231], a[56:59], a[96:99], v[228:231]\n"
"ds_read_b128 a[96:99], v3 offset:0\n"
"v_accvgpr_read_b32 v19, a255\n"
"v_mfma_f32_16x16x32_bf16 v[232:235], a[56:59], a[104:107], v[232:235]\n"
"v_lshlrev_b32 v12, 16, v16\n"
"v_and_b32 v13, s63, v16\n"
"v_mfma_f32_16x16x32_bf16 v[232:235], a[60:63], a[108:111], v[232:235]\n"
"v_lshlrev_b32 v14, 16, v17\n"
"v_and_b32 v15, s63, v17\n"
"v_mfma_f32_16x16x32_bf16 v[204:207], a[52:55], a[108:111], v[204:207]\n"
"ds_read_b128 a[108:111], v3 offset:3072\n"
"v_accvgpr_read_b32 v10, a24\n"
"v_mfma_f32_16x16x32_bf16 v[204:207], a[48:51], a[104:107], v[204:207]\n"
"ds_read_b128 a[104:107], v3 offset:2048\n"
"v_accvgpr_read_b32 v11, a25\n"
"v_mfma_f32_16x16x32_bf16 v[208:211], a[48:51], a[112:115], v[208:211]\n"
"v_mul_f32 v12, v12, v10\n"
"v_mul_f32 v13, v13, v11\n"
"v_mfma_f32_16x16x32_bf16 v[208:211], a[52:55], a[116:119], v[208:211]\n"
"global_load_dwordx4 a[36:39], v0, s[34:35] offset:-3072 sc1\n"
"v_accvgpr_read_b32 v10, a26\n"
"v_mfma_f32_16x16x32_bf16 v[236:239], a[60:63], a[116:119], v[236:239]\n"
"ds_read_b128 a[116:119], v3 offset:5120\n"
"v_accvgpr_read_b32 v11, a27\n"
"v_mfma_f32_16x16x32_bf16 v[236:239], a[56:59], a[112:115], v[236:239]\n"
"ds_read_b128 a[112:115], v3 offset:4096\n"
"v_mul_f32 v14, v14, v10\n"
"v_mfma_f32_16x16x32_bf16 v[240:243], a[56:59], a[120:123], v[240:243]\n"
"v_mul_f32 v15, v15, v11\n"
"v_cvt_pk_bf16_f32 v28, v12, v13\n"
"v_mfma_f32_16x16x32_bf16 v[240:243], a[60:63], a[124:127], v[240:243]\n"
"v_cvt_pk_bf16_f32 v29, v14, v15\n"
"v_lshlrev_b32 v12, 16, v18\n"
"v_mfma_f32_16x16x32_bf16 v[212:215], a[52:55], a[124:127], v[212:215]\n"
"ds_read_b128 a[124:127], v3 offset:7168\n"
"v_and_b32 v13, s63, v18\n"
"v_mfma_f32_16x16x32_bf16 v[212:215], a[48:51], a[120:123], v[212:215]\n"
"ds_read_b128 a[120:123], v3 offset:6144\n"
"v_lshlrev_b32 v14, 16, v19\n"
"v_mfma_f32_16x16x32_bf16 v[216:219], a[48:51], a[128:131], v[216:219]\n"
"v_and_b32 v15, s63, v19\n"
"v_accvgpr_read_b32 v10, a28\n"
"v_mfma_f32_16x16x32_bf16 v[216:219], a[52:55], a[132:135], v[216:219]\n"
"global_load_dwordx4 a[40:43], v0, s[34:35] offset:-2048 sc1\n"
"v_accvgpr_read_b32 v11, a29\n"
"v_mfma_f32_16x16x32_bf16 v[244:247], a[60:63], a[132:135], v[244:247]\n"
"ds_read_b128 a[132:135], v3 offset:9216\n"
"v_mul_f32 v12, v12, v10\n"
"v_mfma_f32_16x16x32_bf16 v[244:247], a[56:59], a[128:131], v[244:247]\n"
"ds_read_b128 a[128:131], v3 offset:8192\n"
"v_mul_f32 v13, v13, v11\n"
"v_mfma_f32_16x16x32_bf16 v[248:251], a[56:59], a[136:139], v[248:251]\n"
"v_accvgpr_read_b32 v10, a30\n"
"v_accvgpr_read_b32 v11, a31\n"
"v_mfma_f32_16x16x32_bf16 v[248:251], a[60:63], a[140:143], v[248:251]\n"
"v_mul_f32 v14, v14, v10\n"
"v_mul_f32 v15, v15, v11\n"
"v_mfma_f32_16x16x32_bf16 v[220:223], a[52:55], a[140:143], v[220:223]\n"
"ds_read_b128 a[140:143], v3 offset:11264\n"
"v_cvt_pk_bf16_f32 v30, v12, v13\n"
"v_mfma_f32_16x16x32_bf16 v[220:223], a[48:51], a[136:139], v[220:223]\n"
"ds_read_b128 a[136:139], v3 offset:10240\n"
"v_cvt_pk_bf16_f32 v31, v14, v15\n"
"v_mfma_f32_16x16x32_bf16 v[224:227], a[48:51], a[144:147], v[224:227]\n"
"global_store_dwordx4 v0, v[28:31], s[54:55] offset:3072 sc1\n"
"ds_write_b128 v2, v[28:31] offset:31744\n"
"v_mfma_f32_16x16x32_bf16 v[224:227], a[52:55], a[148:151], v[224:227]\n"
"global_load_dwordx4 a[44:47], v0, s[34:35] offset:-1024 sc1\n"
"v_mfma_f32_16x16x32_bf16 v[252:255], a[60:63], a[148:151], v[252:255]\n"
"ds_read_b128 a[148:151], v3 offset:13312\n"
"v_mfma_f32_16x16x32_bf16 v[252:255], a[56:59], a[144:147], v[252:255]\n"
"ds_read_b128 a[144:147], v3 offset:12288\n"
"v_mfma_f32_16x16x32_bf16 a[28:31], a[56:59], a[152:155], a[28:31]\n"
"v_mfma_f32_16x16x32_bf16 a[28:31], a[60:63], a[156:159], a[28:31]\n"
"v_mfma_f32_16x16x32_bf16 a[24:27], a[52:55], a[156:159], a[24:27]\n"
"ds_read_b128 a[156:159], v3 offset:15360\n"
"v_mfma_f32_16x16x32_bf16 a[24:27], a[48:51], a[152:155], a[24:27]\n"
"s_waitcnt vmcnt(11) lgkmcnt(12)\n"
"ds_read_b128 a[152:155], v3 offset:14336\n"
"v_mfma_f32_16x16x32_bf16 v[144:147], a[64:67], a[96:99], v[144:147]\n"
"ds_read_b128 v[24:27], v2 offset:16384\n"
"v_mfma_f32_16x16x32_bf16 v[144:147], a[68:71], a[100:103], v[144:147]\n"
"global_load_dwordx4 a[48:51], v0, s[34:35] offset:0 sc1\n"
"v_mfma_f32_16x16x32_bf16 v[172:175], a[76:79], a[100:103], v[172:175]\n"
"v_mfma_f32_16x16x32_bf16 v[172:175], a[72:75], a[96:99], v[172:175]\n"
"v_mfma_f32_16x16x32_bf16 v[176:179], a[72:75], a[104:107], v[176:179]\n"
"v_mfma_f32_16x16x32_bf16 v[176:179], a[76:79], a[108:111], v[176:179]\n"
"v_mfma_f32_16x16x32_bf16 v[148:151], a[68:71], a[108:111], v[148:151]\n"
"v_mfma_f32_16x16x32_bf16 v[148:151], a[64:67], a[104:107], v[148:151]\n"
"s_waitcnt lgkmcnt(10)\n"
"v_mfma_f32_16x16x32_bf16 v[152:155], a[64:67], a[112:115], v[152:155]\n"
"v_mfma_f32_16x16x32_bf16 v[152:155], a[68:71], a[116:119], v[152:155]\n"
"global_load_dwordx4 a[52:55], v0, s[34:35] offset:1024 sc1\n"
"v_mfma_f32_16x16x32_bf16 v[180:183], a[76:79], a[116:119], v[180:183]\n"
"v_mfma_f32_16x16x32_bf16 v[180:183], a[72:75], a[112:115], v[180:183]\n"
"v_mfma_f32_16x16x32_bf16 v[184:187], a[72:75], a[120:123], v[184:187]\n"
"s_waitcnt lgkmcnt(0)\n"
"global_store_dwordx4 v0, v[24:27], s[52:53] offset:-4096 sc1\n"
"v_mfma_f32_16x16x32_bf16 v[184:187], a[76:79], a[124:127], v[184:187]\n"
"v_mfma_f32_16x16x32_bf16 v[156:159], a[68:71], a[124:127], v[156:159]\n"
"v_mfma_f32_16x16x32_bf16 v[156:159], a[64:67], a[120:123], v[156:159]\n"
"v_mfma_f32_16x16x32_bf16 v[160:163], a[64:67], a[128:131], v[160:163]\n"
"v_mfma_f32_16x16x32_bf16 v[160:163], a[68:71], a[132:135], v[160:163]\n"
"global_load_dwordx4 a[56:59], v0, s[34:35] offset:2048 sc1\n"
"v_mfma_f32_16x16x32_bf16 v[188:191], a[76:79], a[132:135], v[188:191]\n"
"v_mfma_f32_16x16x32_bf16 v[188:191], a[72:75], a[128:131], v[188:191]\n"
"v_mfma_f32_16x16x32_bf16 v[192:195], a[72:75], a[136:139], v[192:195]\n"
"v_mfma_f32_16x16x32_bf16 v[192:195], a[76:79], a[140:143], v[192:195]\n"
"v_mfma_f32_16x16x32_bf16 v[164:167], a[68:71], a[140:143], v[164:167]\n"
"v_mfma_f32_16x16x32_bf16 v[164:167], a[64:67], a[136:139], v[164:167]\n"
"v_mfma_f32_16x16x32_bf16 v[168:171], a[64:67], a[144:147], v[168:171]\n"
"v_mfma_f32_16x16x32_bf16 v[168:171], a[68:71], a[148:151], v[168:171]\n"
"global_load_dwordx4 a[60:63], v0, s[34:35] offset:3072 sc1\n"
"v_mfma_f32_16x16x32_bf16 v[196:199], a[76:79], a[148:151], v[196:199]\n"
"v_mfma_f32_16x16x32_bf16 v[196:199], a[72:75], a[144:147], v[196:199]\n"
"v_mfma_f32_16x16x32_bf16 a[20:23], a[72:75], a[152:155], a[20:23]\n"
"s_add_u32 s34, s34, 8192\n"
"v_mfma_f32_16x16x32_bf16 a[20:23], a[76:79], a[156:159], a[20:23]\n"
"v_mfma_f32_16x16x32_bf16 a[16:19], a[68:71], a[156:159], a[16:19]\n"
"v_mfma_f32_16x16x32_bf16 a[16:19], a[64:67], a[152:155], a[16:19]\n"
"s_waitcnt vmcnt(11)\n"
"ds_read_b128 v[28:31], v2 offset:17408\n"
"v_mfma_f32_16x16x32_bf16 v[200:203], a[80:83], a[96:99], v[200:203]\n"
"v_mfma_f32_16x16x32_bf16 v[200:203], a[84:87], a[100:103], v[200:203]\n"
"global_load_dwordx4 a[64:67], v0, s[34:35] offset:-4096 sc1\n"
"v_mfma_f32_16x16x32_bf16 v[228:231], a[92:95], a[100:103], v[228:231]\n"
"ds_read_b128 a[100:103], v4 offset:1024\n"
"v_mfma_f32_16x16x32_bf16 v[228:231], a[88:91], a[96:99], v[228:231]\n"
"ds_read_b128 a[96:99], v4 offset:0\n"
"v_mfma_f32_16x16x32_bf16 v[232:235], a[88:91], a[104:107], v[232:235]\n"
"v_mfma_f32_16x16x32_bf16 v[232:235], a[92:95], a[108:111], v[232:235]\n"
"v_mfma_f32_16x16x32_bf16 v[204:207], a[84:87], a[108:111], v[204:207]\n"
"ds_read_b128 a[108:111], v4 offset:3072\n"
"v_mfma_f32_16x16x32_bf16 v[204:207], a[80:83], a[104:107], v[204:207]\n"
"ds_read_b128 a[104:107], v4 offset:2048\n"
"v_mfma_f32_16x16x32_bf16 v[208:211], a[80:83], a[112:115], v[208:211]\n"
"v_mfma_f32_16x16x32_bf16 v[208:211], a[84:87], a[116:119], v[208:211]\n"
"global_load_dwordx4 a[68:71], v0, s[34:35] offset:-3072 sc1\n"
"v_mfma_f32_16x16x32_bf16 v[236:239], a[92:95], a[116:119], v[236:239]\n"
"s_waitcnt lgkmcnt(4)\n"
"global_store_dwordx4 v0, v[28:31], s[52:53] offset:-3072 sc1\n"
"ds_read_b128 a[116:119], v4 offset:5120\n"
"v_mfma_f32_16x16x32_bf16 v[236:239], a[88:91], a[112:115], v[236:239]\n"
"ds_read_b128 a[112:115], v4 offset:4096\n"
"v_mfma_f32_16x16x32_bf16 v[240:243], a[88:91], a[120:123], v[240:243]\n"
"v_mfma_f32_16x16x32_bf16 v[240:243], a[92:95], a[124:127], v[240:243]\n"
"v_mfma_f32_16x16x32_bf16 v[212:215], a[84:87], a[124:127], v[212:215]\n"
"ds_read_b128 a[124:127], v4 offset:7168\n"
"v_mfma_f32_16x16x32_bf16 v[212:215], a[80:83], a[120:123], v[212:215]\n"
"ds_read_b128 a[120:123], v4 offset:6144\n"
"v_mfma_f32_16x16x32_bf16 v[216:219], a[80:83], a[128:131], v[216:219]\n"
"v_mfma_f32_16x16x32_bf16 v[216:219], a[84:87], a[132:135], v[216:219]\n"
"global_load_dwordx4 a[72:75], v0, s[34:35] offset:-2048 sc1\n"
"v_mfma_f32_16x16x32_bf16 v[244:247], a[92:95], a[132:135], v[244:247]\n"
"ds_read_b128 a[132:135], v4 offset:9216\n"
"v_mfma_f32_16x16x32_bf16 v[244:247], a[88:91], a[128:131], v[244:247]\n"
"ds_read_b128 a[128:131], v4 offset:8192\n"
"v_mfma_f32_16x16x32_bf16 v[248:251], a[88:91], a[136:139], v[248:251]\n"
"v_mfma_f32_16x16x32_bf16 v[248:251], a[92:95], a[140:143], v[248:251]\n"
"v_mfma_f32_16x16x32_bf16 v[220:223], a[84:87], a[140:143], v[220:223]\n"
"ds_read_b128 a[140:143], v4 offset:11264\n"
"v_mfma_f32_16x16x32_bf16 v[220:223], a[80:83], a[136:139], v[220:223]\n"
"ds_read_b128 a[136:139], v4 offset:10240\n"
"v_mfma_f32_16x16x32_bf16 v[224:227], a[80:83], a[144:147], v[224:227]\n"
"v_mfma_f32_16x16x32_bf16 v[224:227], a[84:87], a[148:151], v[224:227]\n"
"global_load_dwordx4 a[76:79], v0, s[34:35] offset:-1024 sc1\n"
"v_mfma_f32_16x16x32_bf16 v[252:255], a[92:95], a[148:151], v[252:255]\n"
"ds_read_b128 a[148:151], v4 offset:13312\n"
"v_mfma_f32_16x16x32_bf16 v[252:255], a[88:91], a[144:147], v[252:255]\n"
"ds_read_b128 a[144:147], v4 offset:12288\n"
"v_mfma_f32_16x16x32_bf16 a[28:31], a[88:91], a[152:155], a[28:31]\n"
"v_mfma_f32_16x16x32_bf16 a[28:31], a[92:95], a[156:159], a[28:31]\n"
"ds_read_b128 v[24:27], v2 offset:18432\n"
"v_mfma_f32_16x16x32_bf16 a[24:27], a[84:87], a[156:159], a[24:27]\n"
"ds_read_b128 a[156:159], v4 offset:15360\n"
"v_mfma_f32_16x16x32_bf16 a[24:27], a[80:83], a[152:155], a[24:27]\n"
"s_waitcnt vmcnt(10) lgkmcnt(12)\n"
"ds_read_b128 a[152:155], v4 offset:14336\n"
"v_mfma_f32_16x16x32_bf16 v[144:147], a[32:35], a[96:99], v[144:147]\n"
"v_mfma_f32_16x16x32_bf16 v[144:147], a[36:39], a[100:103], v[144:147]\n"
"global_load_dwordx4 a[80:83], v0, s[34:35] offset:0 sc1\n"
"v_mfma_f32_16x16x32_bf16 v[172:175], a[44:47], a[100:103], v[172:175]\n"
"v_mfma_f32_16x16x32_bf16 v[172:175], a[40:43], a[96:99], v[172:175]\n"
"v_mfma_f32_16x16x32_bf16 v[176:179], a[40:43], a[104:107], v[176:179]\n"
"v_mfma_f32_16x16x32_bf16 v[176:179], a[44:47], a[108:111], v[176:179]\n"
"v_mfma_f32_16x16x32_bf16 v[148:151], a[36:39], a[108:111], v[148:151]\n"
"v_mfma_f32_16x16x32_bf16 v[148:151], a[32:35], a[104:107], v[148:151]\n"
"s_waitcnt lgkmcnt(9)\n"
"v_mfma_f32_16x16x32_bf16 v[152:155], a[32:35], a[112:115], v[152:155]\n"
"s_waitcnt lgkmcnt(2)\n"
"global_store_dwordx4 v0, v[24:27], s[52:53] offset:-2048 sc1\n"
"v_mfma_f32_16x16x32_bf16 v[152:155], a[36:39], a[116:119], v[152:155]\n"
"global_load_dwordx4 a[84:87], v0, s[34:35] offset:1024 sc1\n"
"v_mfma_f32_16x16x32_bf16 v[180:183], a[44:47], a[116:119], v[180:183]\n"
"v_mfma_f32_16x16x32_bf16 v[180:183], a[40:43], a[112:115], v[180:183]\n"
"v_mfma_f32_16x16x32_bf16 v[184:187], a[40:43], a[120:123], v[184:187]\n"
"v_mfma_f32_16x16x32_bf16 v[184:187], a[44:47], a[124:127], v[184:187]\n"
"v_mfma_f32_16x16x32_bf16 v[156:159], a[36:39], a[124:127], v[156:159]\n"
"v_mfma_f32_16x16x32_bf16 v[156:159], a[32:35], a[120:123], v[156:159]\n"
"v_mfma_f32_16x16x32_bf16 v[160:163], a[32:35], a[128:131], v[160:163]\n"
"v_mfma_f32_16x16x32_bf16 v[160:163], a[36:39], a[132:135], v[160:163]\n"
"global_load_dwordx4 a[88:91], v0, s[34:35] offset:2048 sc1\n"
"v_mfma_f32_16x16x32_bf16 v[188:191], a[44:47], a[132:135], v[188:191]\n"
"v_mfma_f32_16x16x32_bf16 v[188:191], a[40:43], a[128:131], v[188:191]\n"
"v_mfma_f32_16x16x32_bf16 v[192:195], a[40:43], a[136:139], v[192:195]\n"
"v_mfma_f32_16x16x32_bf16 v[192:195], a[44:47], a[140:143], v[192:195]\n"
"v_mfma_f32_16x16x32_bf16 v[164:167], a[36:39], a[140:143], v[164:167]\n"
"v_mfma_f32_16x16x32_bf16 v[164:167], a[32:35], a[136:139], v[164:167]\n"
"s_waitcnt lgkmcnt(0)\n"
"v_mfma_f32_16x16x32_bf16 v[168:171], a[32:35], a[144:147], v[168:171]\n"
"v_mfma_f32_16x16x32_bf16 v[168:171], a[36:39], a[148:151], v[168:171]\n"
"global_load_dwordx4 a[92:95], v0, s[34:35] offset:3072 sc1\n"
"v_mfma_f32_16x16x32_bf16 v[196:199], a[44:47], a[148:151], v[196:199]\n"
"v_mfma_f32_16x16x32_bf16 v[196:199], a[40:43], a[144:147], v[196:199]\n"
"ds_read_b128 v[28:31], v2 offset:19456\n"
"v_mfma_f32_16x16x32_bf16 a[20:23], a[40:43], a[152:155], a[20:23]\n"
"s_add_u32 s34, s34, 8192\n"
"v_mfma_f32_16x16x32_bf16 a[20:23], a[44:47], a[156:159], a[20:23]\n"
"v_mfma_f32_16x16x32_bf16 a[16:19], a[36:39], a[156:159], a[16:19]\n"
"v_mfma_f32_16x16x32_bf16 a[16:19], a[32:35], a[152:155], a[16:19]\n"
"s_waitcnt vmcnt(10)\n"
"v_mfma_f32_16x16x32_bf16 v[200:203], a[48:51], a[96:99], v[200:203]\n"
"v_mfma_f32_16x16x32_bf16 v[200:203], a[52:55], a[100:103], v[200:203]\n"
"global_load_dwordx4 a[32:35], v0, s[34:35] offset:-4096 sc1\n"
"v_mfma_f32_16x16x32_bf16 v[228:231], a[60:63], a[100:103], v[228:231]\n"
"ds_read_b128 a[100:103], v5 offset:1024\n"
"v_mfma_f32_16x16x32_bf16 v[228:231], a[56:59], a[96:99], v[228:231]\n"
"ds_read_b128 a[96:99], v5 offset:0\n"
"v_mfma_f32_16x16x32_bf16 v[232:235], a[56:59], a[104:107], v[232:235]\n"
"v_mfma_f32_16x16x32_bf16 v[232:235], a[60:63], a[108:111], v[232:235]\n"
"v_mfma_f32_16x16x32_bf16 v[204:207], a[52:55], a[108:111], v[204:207]\n"
"s_waitcnt lgkmcnt(2)\n"
"global_store_dwordx4 v0, v[28:31], s[52:53] offset:-1024 sc1\n"
"ds_read_b128 a[108:111], v5 offset:3072\n"
"v_mfma_f32_16x16x32_bf16 v[204:207], a[48:51], a[104:107], v[204:207]\n"
"ds_read_b128 a[104:107], v5 offset:2048\n"
"v_mfma_f32_16x16x32_bf16 v[208:211], a[48:51], a[112:115], v[208:211]\n"
"v_mfma_f32_16x16x32_bf16 v[208:211], a[52:55], a[116:119], v[208:211]\n"
"global_load_dwordx4 a[36:39], v0, s[34:35] offset:-3072 sc1\n"
"v_mfma_f32_16x16x32_bf16 v[236:239], a[60:63], a[116:119], v[236:239]\n"
"ds_read_b128 a[116:119], v5 offset:5120\n"
"v_mfma_f32_16x16x32_bf16 v[236:239], a[56:59], a[112:115], v[236:239]\n"
"ds_read_b128 a[112:115], v5 offset:4096\n"
"v_mfma_f32_16x16x32_bf16 v[240:243], a[56:59], a[120:123], v[240:243]\n"
"v_mfma_f32_16x16x32_bf16 v[240:243], a[60:63], a[124:127], v[240:243]\n"
"v_mfma_f32_16x16x32_bf16 v[212:215], a[52:55], a[124:127], v[212:215]\n"
"ds_read_b128 a[124:127], v5 offset:7168\n"
"v_mfma_f32_16x16x32_bf16 v[212:215], a[48:51], a[120:123], v[212:215]\n"
"ds_read_b128 a[120:123], v5 offset:6144\n"
"v_mfma_f32_16x16x32_bf16 v[216:219], a[48:51], a[128:131], v[216:219]\n"
"v_mfma_f32_16x16x32_bf16 v[216:219], a[52:55], a[132:135], v[216:219]\n"
"global_load_dwordx4 a[40:43], v0, s[34:35] offset:-2048 sc1\n"
"v_mfma_f32_16x16x32_bf16 v[244:247], a[60:63], a[132:135], v[244:247]\n"
"ds_read_b128 a[132:135], v5 offset:9216\n"
"v_mfma_f32_16x16x32_bf16 v[244:247], a[56:59], a[128:131], v[244:247]\n"
"ds_read_b128 a[128:131], v5 offset:8192\n"
"v_mfma_f32_16x16x32_bf16 v[248:251], a[56:59], a[136:139], v[248:251]\n"
"v_mfma_f32_16x16x32_bf16 v[248:251], a[60:63], a[140:143], v[248:251]\n"
"v_mfma_f32_16x16x32_bf16 v[220:223], a[52:55], a[140:143], v[220:223]\n"
"ds_read_b128 a[140:143], v5 offset:11264\n"
"v_mfma_f32_16x16x32_bf16 v[220:223], a[48:51], a[136:139], v[220:223]\n"
"ds_read_b128 a[136:139], v5 offset:10240\n"
"v_mfma_f32_16x16x32_bf16 v[224:227], a[48:51], a[144:147], v[224:227]\n"
"ds_read_b128 v[24:27], v2 offset:20480\n"
"v_mfma_f32_16x16x32_bf16 v[224:227], a[52:55], a[148:151], v[224:227]\n"
"global_load_dwordx4 a[44:47], v0, s[34:35] offset:-1024 sc1\n"
"v_mfma_f32_16x16x32_bf16 v[252:255], a[60:63], a[148:151], v[252:255]\n"
"ds_read_b128 a[148:151], v5 offset:13312\n"
"v_mfma_f32_16x16x32_bf16 v[252:255], a[56:59], a[144:147], v[252:255]\n"
"ds_read_b128 a[144:147], v5 offset:12288\n"
"v_mfma_f32_16x16x32_bf16 a[28:31], a[56:59], a[152:155], a[28:31]\n"
"v_mfma_f32_16x16x32_bf16 a[28:31], a[60:63], a[156:159], a[28:31]\n"
"v_mfma_f32_16x16x32_bf16 a[24:27], a[52:55], a[156:159], a[24:27]\n"
"ds_read_b128 a[156:159], v5 offset:15360\n"
"v_mfma_f32_16x16x32_bf16 a[24:27], a[48:51], a[152:155], a[24:27]\n"
"s_waitcnt vmcnt(10) lgkmcnt(12)\n"
"ds_read_b128 a[152:155], v5 offset:14336\n"
"v_mfma_f32_16x16x32_bf16 v[144:147], a[64:67], a[96:99], v[144:147]\n"
"v_mfma_f32_16x16x32_bf16 v[144:147], a[68:71], a[100:103], v[144:147]\n"
"global_load_dwordx4 a[48:51], v0, s[34:35] offset:0 sc1\n"
"v_mfma_f32_16x16x32_bf16 v[172:175], a[76:79], a[100:103], v[172:175]\n"
"v_mfma_f32_16x16x32_bf16 v[172:175], a[72:75], a[96:99], v[172:175]\n"
"v_mfma_f32_16x16x32_bf16 v[176:179], a[72:75], a[104:107], v[176:179]\n"
"s_waitcnt lgkmcnt(4)\n"
"global_store_dwordx4 v0, v[24:27], s[52:53] offset:0 sc1\n"
"v_mfma_f32_16x16x32_bf16 v[176:179], a[76:79], a[108:111], v[176:179]\n"
"v_mfma_f32_16x16x32_bf16 v[148:151], a[68:71], a[108:111], v[148:151]\n"
"v_mfma_f32_16x16x32_bf16 v[148:151], a[64:67], a[104:107], v[148:151]\n"
"v_mfma_f32_16x16x32_bf16 v[152:155], a[64:67], a[112:115], v[152:155]\n"
"v_mfma_f32_16x16x32_bf16 v[152:155], a[68:71], a[116:119], v[152:155]\n"
"global_load_dwordx4 a[52:55], v0, s[34:35] offset:1024 sc1\n"
"v_mfma_f32_16x16x32_bf16 v[180:183], a[76:79], a[116:119], v[180:183]\n"
"v_mfma_f32_16x16x32_bf16 v[180:183], a[72:75], a[112:115], v[180:183]\n"
"v_mfma_f32_16x16x32_bf16 v[184:187], a[72:75], a[120:123], v[184:187]\n"
"v_mfma_f32_16x16x32_bf16 v[184:187], a[76:79], a[124:127], v[184:187]\n"
"v_mfma_f32_16x16x32_bf16 v[156:159], a[68:71], a[124:127], v[156:159]\n"
"v_mfma_f32_16x16x32_bf16 v[156:159], a[64:67], a[120:123], v[156:159]\n"
"v_mfma_f32_16x16x32_bf16 v[160:163], a[64:67], a[128:131], v[160:163]\n"
"v_mfma_f32_16x16x32_bf16 v[160:163], a[68:71], a[132:135], v[160:163]\n"
"global_load_dwordx4 a[56:59], v0, s[34:35] offset:2048 sc1\n"
"v_mfma_f32_16x16x32_bf16 v[188:191], a[76:79], a[132:135], v[188:191]\n"
"v_mfma_f32_16x16x32_bf16 v[188:191], a[72:75], a[128:131], v[188:191]\n"
"v_mfma_f32_16x16x32_bf16 v[192:195], a[72:75], a[136:139], v[192:195]\n"
"v_mfma_f32_16x16x32_bf16 v[192:195], a[76:79], a[140:143], v[192:195]\n"
"v_mfma_f32_16x16x32_bf16 v[164:167], a[68:71], a[140:143], v[164:167]\n"
"v_mfma_f32_16x16x32_bf16 v[164:167], a[64:67], a[136:139], v[164:167]\n"
"s_waitcnt lgkmcnt(0)\n"
"ds_read_b128 v[28:31], v2 offset:21504\n"
"v_mfma_f32_16x16x32_bf16 v[168:171], a[64:67], a[144:147], v[168:171]\n"
"v_mfma_f32_16x16x32_bf16 v[168:171], a[68:71], a[148:151], v[168:171]\n"
"global_load_dwordx4 a[60:63], v0, s[34:35] offset:3072 sc1\n"
"v_mfma_f32_16x16x32_bf16 v[196:199], a[76:79], a[148:151], v[196:199]\n"
"v_mfma_f32_16x16x32_bf16 v[196:199], a[72:75], a[144:147], v[196:199]\n"
"v_mfma_f32_16x16x32_bf16 a[20:23], a[72:75], a[152:155], a[20:23]\n"
"s_add_u32 s34, s34, 8192\n"
"v_mfma_f32_16x16x32_bf16 a[20:23], a[76:79], a[156:159], a[20:23]\n"
"v_mfma_f32_16x16x32_bf16 a[16:19], a[68:71], a[156:159], a[16:19]\n"
"v_mfma_f32_16x16x32_bf16 a[16:19], a[64:67], a[152:155], a[16:19]\n"
"s_waitcnt vmcnt(10)\n"
"v_mfma_f32_16x16x32_bf16 v[200:203], a[80:83], a[96:99], v[200:203]\n"
"v_mfma_f32_16x16x32_bf16 v[200:203], a[84:87], a[100:103], v[200:203]\n"
"global_load_dwordx4 a[64:67], v0, s[34:35] offset:-4096 sc1\n"
"v_mfma_f32_16x16x32_bf16 v[228:231], a[92:95], a[100:103], v[228:231]\n"
"s_waitcnt lgkmcnt(0)\n"
"global_store_dwordx4 v0, v[28:31], s[52:53] offset:1024 sc1\n"
"ds_read_b128 a[100:103], v2 offset:17408\n"
"buffer_load_dwordx2 v[8:9], off, s[56:59], %14 sc1\n"
"v_mfma_f32_16x16x32_bf16 v[228:231], a[88:91], a[96:99], v[228:231]\n"
"ds_read_b128 a[96:99], v2 offset:16384\n"
"v_mfma_f32_16x16x32_bf16 v[232:235], a[88:91], a[104:107], v[232:235]\n"
"v_mfma_f32_16x16x32_bf16 v[232:235], a[92:95], a[108:111], v[232:235]\n"
"v_mfma_f32_16x16x32_bf16 v[204:207], a[84:87], a[108:111], v[204:207]\n"
"ds_read_b128 a[108:111], v2 offset:19456\n"
"v_mfma_f32_16x16x32_bf16 v[204:207], a[80:83], a[104:107], v[204:207]\n"
"ds_read_b128 a[104:107], v2 offset:18432\n"
"v_mfma_f32_16x16x32_bf16 v[208:211], a[80:83], a[112:115], v[208:211]\n"
"v_mfma_f32_16x16x32_bf16 v[208:211], a[84:87], a[116:119], v[208:211]\n"
"global_load_dwordx4 a[68:71], v0, s[34:35] offset:-3072 sc1\n"
"v_mfma_f32_16x16x32_bf16 v[236:239], a[92:95], a[116:119], v[236:239]\n"
"ds_read_b128 a[116:119], v2 offset:21504\n"
"v_mfma_f32_16x16x32_bf16 v[236:239], a[88:91], a[112:115], v[236:239]\n"
"ds_read_b128 a[112:115], v2 offset:20480\n"
"v_mfma_f32_16x16x32_bf16 v[240:243], a[88:91], a[120:123], v[240:243]\n"
"v_mfma_f32_16x16x32_bf16 v[240:243], a[92:95], a[124:127], v[240:243]\n"
"v_mfma_f32_16x16x32_bf16 v[212:215], a[84:87], a[124:127], v[212:215]\n"
"ds_read_b128 a[124:127], v2 offset:23552\n"
"v_mfma_f32_16x16x32_bf16 v[212:215], a[80:83], a[120:123], v[212:215]\n"
"ds_read_b128 a[120:123], v2 offset:22528\n"
"v_mfma_f32_16x16x32_bf16 v[216:219], a[80:83], a[128:131], v[216:219]\n"
"v_mfma_f32_16x16x32_bf16 v[216:219], a[84:87], a[132:135], v[216:219]\n"
"global_load_dwordx4 a[72:75], v0, s[34:35] offset:-2048 sc1\n"
"v_mfma_f32_16x16x32_bf16 v[244:247], a[92:95], a[132:135], v[244:247]\n"
"ds_read_b128 a[132:135], v2 offset:25600\n"
"v_mfma_f32_16x16x32_bf16 v[244:247], a[88:91], a[128:131], v[244:247]\n"
"ds_read_b128 a[128:131], v2 offset:24576\n"
"v_mfma_f32_16x16x32_bf16 v[248:251], a[88:91], a[136:139], v[248:251]\n"
"v_mfma_f32_16x16x32_bf16 v[248:251], a[92:95], a[140:143], v[248:251]\n"
"ds_read_b128 v[24:27], v2 offset:22528\n"
"v_mfma_f32_16x16x32_bf16 v[220:223], a[84:87], a[140:143], v[220:223]\n"
"ds_read_b128 a[140:143], v2 offset:27648\n"
"v_mfma_f32_16x16x32_bf16 v[220:223], a[80:83], a[136:139], v[220:223]\n"
"ds_read_b128 a[136:139], v2 offset:26624\n"
"v_mfma_f32_16x16x32_bf16 v[224:227], a[80:83], a[144:147], v[224:227]\n"
"v_mfma_f32_16x16x32_bf16 v[224:227], a[84:87], a[148:151], v[224:227]\n"
"global_load_dwordx4 a[76:79], v0, s[34:35] offset:-1024 sc1\n"
"v_mfma_f32_16x16x32_bf16 v[252:255], a[92:95], a[148:151], v[252:255]\n"
"ds_read_b128 a[148:151], v2 offset:29696\n"
"v_mfma_f32_16x16x32_bf16 v[252:255], a[88:91], a[144:147], v[252:255]\n"
"ds_read_b128 a[144:147], v2 offset:28672\n"
"v_mfma_f32_16x16x32_bf16 a[28:31], a[88:91], a[152:155], a[28:31]\n"
"v_mfma_f32_16x16x32_bf16 a[28:31], a[92:95], a[156:159], a[28:31]\n"
"v_mfma_f32_16x16x32_bf16 a[24:27], a[84:87], a[156:159], a[24:27]\n"
"ds_read_b128 a[156:159], v2 offset:31744\n"
"v_mfma_f32_16x16x32_bf16 a[24:27], a[80:83], a[152:155], a[24:27]\n"
"s_waitcnt vmcnt(11) lgkmcnt(12)\n"
"ds_read_b128 a[152:155], v2 offset:30720\n"
"v_mfma_f32_16x16x32_bf16 v[32:35], a[32:35], a[96:99], v[32:35]\n"
"s_waitcnt lgkmcnt(6)\n"
"global_store_dwordx4 v0, v[24:27], s[52:53] offset:2048 sc1\n"
"v_mfma_f32_16x16x32_bf16 v[32:35], a[36:39], a[100:103], v[32:35]\n"
"global_load_dwordx4 a[80:83], v0, s[34:35] offset:0 sc1\n"
"L_P3:\n"
"v_mfma_f32_16x16x32_bf16 v[60:63], a[44:47], a[100:103], v[60:63]\n"
"s_barrier\n"
"v_mfma_f32_16x16x32_bf16 v[60:63], a[40:43], a[96:99], v[60:63]\n"
"v_mfma_f32_16x16x32_bf16 v[64:67], a[40:43], a[104:107], v[64:67]\n"
"v_mfma_f32_16x16x32_bf16 v[64:67], a[44:47], a[108:111], v[64:67]\n"
"v_mfma_f32_16x16x32_bf16 v[36:39], a[36:39], a[108:111], v[36:39]\n"
"v_mfma_f32_16x16x32_bf16 v[36:39], a[32:35], a[104:107], v[36:39]\n"
"v_mfma_f32_16x16x32_bf16 v[40:43], a[32:35], a[112:115], v[40:43]\n"
"v_mfma_f32_16x16x32_bf16 v[40:43], a[36:39], a[116:119], v[40:43]\n"
"global_load_dwordx4 a[84:87], v0, s[34:35] offset:1024 sc1\n"
"v_mfma_f32_16x16x32_bf16 v[68:71], a[44:47], a[116:119], v[68:71]\n"
"v_mfma_f32_16x16x32_bf16 v[68:71], a[40:43], a[112:115], v[68:71]\n"
"v_mfma_f32_16x16x32_bf16 v[72:75], a[40:43], a[120:123], v[72:75]\n"
"v_mfma_f32_16x16x32_bf16 v[72:75], a[44:47], a[124:127], v[72:75]\n"
"v_mfma_f32_16x16x32_bf16 v[44:47], a[36:39], a[124:127], v[44:47]\n"
"v_mfma_f32_16x16x32_bf16 v[44:47], a[32:35], a[120:123], v[44:47]\n"
"s_waitcnt lgkmcnt(4)\n"
"v_mfma_f32_16x16x32_bf16 v[48:51], a[32:35], a[128:131], v[48:51]\n"
"v_mfma_f32_16x16x32_bf16 v[48:51], a[36:39], a[132:135], v[48:51]\n"
"global_load_dwordx4 a[88:91], v0, s[34:35] offset:2048 sc1\n"
"v_lshrrev_b32 v16, 4, v0\n"
"v_mfma_f32_16x16x32_bf16 v[76:79], a[44:47], a[132:135], v[76:79]\n"
"v_and_b32 v16, 0x30, v16\n"
"v_mfma_f32_16x16x32_bf16 v[76:79], a[40:43], a[128:131], v[76:79]\n"
"ds_read_b128 v[28:31], v2 offset:23552\n"
"buffer_load_dwordx2 v[18:19], v16, s[44:47], s61 offen offset:0\n"
"v_mfma_f32_16x16x32_bf16 v[80:83], a[40:43], a[136:139], v[80:83]\n"
"v_mfma_f32_16x16x32_bf16 v[80:83], a[44:47], a[140:143], v[80:83]\n"
"v_mfma_f32_16x16x32_bf16 v[52:55], a[36:39], a[140:143], v[52:55]\n"
"v_mfma_f32_16x16x32_bf16 v[52:55], a[32:35], a[136:139], v[52:55]\n"
"s_waitcnt lgkmcnt(1)\n"
"v_mfma_f32_16x16x32_bf16 v[56:59], a[32:35], a[144:147], v[56:59]\n"
"v_mfma_f32_16x16x32_bf16 v[56:59], a[36:39], a[148:151], v[56:59]\n"
"global_load_dwordx4 a[92:95], v0, s[34:35] offset:3072 sc1\n"
"v_lshrrev_b32 v20, 4, v0\n"
"v_mfma_f32_16x16x32_bf16 v[84:87], a[44:47], a[148:151], v[84:87]\n"
"v_and_b32 v20, 0x30, v20\n"
"v_mfma_f32_16x16x32_bf16 v[84:87], a[40:43], a[144:147], v[84:87]\n"
"buffer_load_dwordx2 v[22:23], v20, s[44:47], s61 offen offset:8\n"
"v_mfma_f32_16x16x32_bf16 a[4:7], a[40:43], a[152:155], a[4:7]\n"
"s_add_u32 s34, s34, 8192\n"
"v_mfma_f32_16x16x32_bf16 a[4:7], a[44:47], a[156:159], a[4:7]\n"
"v_mfma_f32_16x16x32_bf16 a[0:3], a[36:39], a[156:159], a[0:3]\n"
"s_waitcnt lgkmcnt(0)\n"
"global_store_dwordx4 v0, v[28:31], s[52:53] offset:3072 sc1\n"
"v_mfma_f32_16x16x32_bf16 a[0:3], a[32:35], a[152:155], a[0:3]\n"
"s_waitcnt vmcnt(14)\n"
"v_mfma_f32_16x16x32_bf16 v[88:91], a[48:51], a[96:99], v[88:91]\n"
"v_mfma_f32_16x16x32_bf16 v[88:91], a[52:55], a[100:103], v[88:91]\n"
"global_load_dwordx4 a[32:35], v0, s[34:35] offset:-4096 sc1\n"
"v_mfma_f32_16x16x32_bf16 v[116:119], a[60:63], a[100:103], v[116:119]\n"
"v_mfma_f32_16x16x32_bf16 v[116:119], a[56:59], a[96:99], v[116:119]\n"
"v_mfma_f32_16x16x32_bf16 v[120:123], a[56:59], a[104:107], v[120:123]\n"
"v_mfma_f32_16x16x32_bf16 v[120:123], a[60:63], a[108:111], v[120:123]\n"
"v_mfma_f32_16x16x32_bf16 v[92:95], a[52:55], a[108:111], v[92:95]\n"
"v_mfma_f32_16x16x32_bf16 v[92:95], a[48:51], a[104:107], v[92:95]\n"
"v_mfma_f32_16x16x32_bf16 v[96:99], a[48:51], a[112:115], v[96:99]\n"
"v_mfma_f32_16x16x32_bf16 v[96:99], a[52:55], a[116:119], v[96:99]\n"
"global_load_dwordx4 a[36:39], v0, s[34:35] offset:-3072 sc1\n"
"v_mfma_f32_16x16x32_bf16 v[124:127], a[60:63], a[116:119], v[124:127]\n"
"v_mfma_f32_16x16x32_bf16 v[124:127], a[56:59], a[112:115], v[124:127]\n"
"v_mfma_f32_16x16x32_bf16 v[128:131], a[56:59], a[120:123], v[128:131]\n"
"v_mfma_f32_16x16x32_bf16 v[128:131], a[60:63], a[124:127], v[128:131]\n"
"v_mfma_f32_16x16x32_bf16 v[100:103], a[52:55], a[124:127], v[100:103]\n"
"v_mfma_f32_16x16x32_bf16 v[100:103], a[48:51], a[120:123], v[100:103]\n"
"v_mfma_f32_16x16x32_bf16 v[104:107], a[48:51], a[128:131], v[104:107]\n"
"ds_read_b128 v[24:27], v2 offset:24576\n"
"v_mfma_f32_16x16x32_bf16 v[104:107], a[52:55], a[132:135], v[104:107]\n"
"global_load_dwordx4 a[40:43], v0, s[34:35] offset:-2048 sc1\n"
"v_mfma_f32_16x16x32_bf16 v[132:135], a[60:63], a[132:135], v[132:135]\n"
"v_mfma_f32_16x16x32_bf16 v[132:135], a[56:59], a[128:131], v[132:135]\n"
"v_mfma_f32_16x16x32_bf16 v[136:139], a[56:59], a[136:139], v[136:139]\n"
"v_mfma_f32_16x16x32_bf16 v[136:139], a[60:63], a[140:143], v[136:139]\n"
"v_mfma_f32_16x16x32_bf16 v[108:111], a[52:55], a[140:143], v[108:111]\n"
"v_mfma_f32_16x16x32_bf16 v[108:111], a[48:51], a[136:139], v[108:111]\n"
"v_mfma_f32_16x16x32_bf16 v[112:115], a[48:51], a[144:147], v[112:115]\n"
"v_mfma_f32_16x16x32_bf16 v[112:115], a[52:55], a[148:151], v[112:115]\n"
"global_load_dwordx4 a[44:47], v0, s[34:35] offset:-1024 sc1\n"
"v_mfma_f32_16x16x32_bf16 v[140:143], a[60:63], a[148:151], v[140:143]\n"
"v_mfma_f32_16x16x32_bf16 v[140:143], a[56:59], a[144:147], v[140:143]\n"
"v_mfma_f32_16x16x32_bf16 a[12:15], a[56:59], a[152:155], a[12:15]\n"
"s_waitcnt lgkmcnt(0)\n"
"global_store_dwordx4 v0, v[24:27], s[54:55] offset:-4096 sc1\n"
"v_mfma_f32_16x16x32_bf16 a[12:15], a[60:63], a[156:159], a[12:15]\n"
"v_mfma_f32_16x16x32_bf16 a[8:11], a[52:55], a[156:159], a[8:11]\n"
"v_mfma_f32_16x16x32_bf16 a[8:11], a[48:51], a[152:155], a[8:11]\n"
"s_waitcnt vmcnt(13)\n"
"v_mfma_f32_16x16x32_bf16 v[144:147], a[64:67], a[96:99], v[144:147]\n"
"v_mfma_f32_16x16x32_bf16 v[144:147], a[68:71], a[100:103], v[144:147]\n"
"global_load_dwordx4 a[48:51], v0, s[34:35] offset:0 sc1\n"
"v_mfma_f32_16x16x32_bf16 v[172:175], a[76:79], a[100:103], v[172:175]\n"
"v_mfma_f32_16x16x32_bf16 v[172:175], a[72:75], a[96:99], v[172:175]\n"
"v_mfma_f32_16x16x32_bf16 v[176:179], a[72:75], a[104:107], v[176:179]\n"
"v_mfma_f32_16x16x32_bf16 v[176:179], a[76:79], a[108:111], v[176:179]\n"
"v_mfma_f32_16x16x32_bf16 v[148:151], a[68:71], a[108:111], v[148:151]\n"
"v_mfma_f32_16x16x32_bf16 v[148:151], a[64:67], a[104:107], v[148:151]\n"
"v_mfma_f32_16x16x32_bf16 v[152:155], a[64:67], a[112:115], v[152:155]\n"
"v_mfma_f32_16x16x32_bf16 v[152:155], a[68:71], a[116:119], v[152:155]\n"
"global_load_dwordx4 a[52:55], v0, s[34:35] offset:1024 sc1\n"
"v_mfma_f32_16x16x32_bf16 v[180:183], a[76:79], a[116:119], v[180:183]\n"
"v_mfma_f32_16x16x32_bf16 v[180:183], a[72:75], a[112:115], v[180:183]\n"
"v_mfma_f32_16x16x32_bf16 v[184:187], a[72:75], a[120:123], v[184:187]\n"
"v_mfma_f32_16x16x32_bf16 v[184:187], a[76:79], a[124:127], v[184:187]\n"
"v_mfma_f32_16x16x32_bf16 v[156:159], a[68:71], a[124:127], v[156:159]\n"
"v_mfma_f32_16x16x32_bf16 v[156:159], a[64:67], a[120:123], v[156:159]\n"
"ds_read_b128 v[28:31], v2 offset:26624\n"
"v_mfma_f32_16x16x32_bf16 v[160:163], a[64:67], a[128:131], v[160:163]\n"
"v_mfma_f32_16x16x32_bf16 v[160:163], a[68:71], a[132:135], v[160:163]\n"
"global_load_dwordx4 a[56:59], v0, s[34:35] offset:2048 sc1\n"
"v_mfma_f32_16x16x32_bf16 v[188:191], a[76:79], a[132:135], v[188:191]\n"
"v_mfma_f32_16x16x32_bf16 v[188:191], a[72:75], a[128:131], v[188:191]\n"
"v_mfma_f32_16x16x32_bf16 v[192:195], a[72:75], a[136:139], v[192:195]\n"
"v_mfma_f32_16x16x32_bf16 v[192:195], a[76:79], a[140:143], v[192:195]\n"
"v_mfma_f32_16x16x32_bf16 v[164:167], a[68:71], a[140:143], v[164:167]\n"
"v_mfma_f32_16x16x32_bf16 v[164:167], a[64:67], a[136:139], v[164:167]\n"
"v_mfma_f32_16x16x32_bf16 v[168:171], a[64:67], a[144:147], v[168:171]\n"
"v_mfma_f32_16x16x32_bf16 v[168:171], a[68:71], a[148:151], v[168:171]\n"
"global_load_dwordx4 a[60:63], v0, s[34:35] offset:3072 sc1\n"
"v_mfma_f32_16x16x32_bf16 v[196:199], a[76:79], a[148:151], v[196:199]\n"
"s_waitcnt lgkmcnt(0)\n"
"global_store_dwordx4 v0, v[28:31], s[54:55] offset:-2048 sc1\n"
"v_mfma_f32_16x16x32_bf16 v[196:199], a[72:75], a[144:147], v[196:199]\n"
"v_mfma_f32_16x16x32_bf16 a[20:23], a[72:75], a[152:155], a[20:23]\n"
"s_add_u32 s34, s34, 8192\n"
"v_mfma_f32_16x16x32_bf16 a[20:23], a[76:79], a[156:159], a[20:23]\n"
"v_mfma_f32_16x16x32_bf16 a[16:19], a[68:71], a[156:159], a[16:19]\n"
"v_mfma_f32_16x16x32_bf16 a[16:19], a[64:67], a[152:155], a[16:19]\n"
"s_waitcnt vmcnt(12)\n"
"v_mfma_f32_16x16x32_bf16 v[200:203], a[80:83], a[96:99], v[200:203]\n"
"v_mfma_f32_16x16x32_bf16 v[200:203], a[84:87], a[100:103], v[200:203]\n"
"global_load_dwordx4 a[64:67], v0, s[34:35] offset:-4096 sc1\n"
"v_mfma_f32_16x16x32_bf16 v[228:231], a[92:95], a[100:103], v[228:231]\n"
"ds_read_b128 a[100:103], v3 offset:17408\n"
"v_mfma_f32_16x16x32_bf16 v[228:231], a[88:91], a[96:99], v[228:231]\n"
"ds_read_b128 a[96:99], v3 offset:16384\n"
"v_mfma_f32_16x16x32_bf16 v[232:235], a[88:91], a[104:107], v[232:235]\n"
"v_mfma_f32_16x16x32_bf16 v[232:235], a[92:95], a[108:111], v[232:235]\n"
"v_mfma_f32_16x16x32_bf16 v[204:207], a[84:87], a[108:111], v[204:207]\n"
"ds_read_b128 a[108:111], v3 offset:19456\n"
"v_mfma_f32_16x16x32_bf16 v[204:207], a[80:83], a[104:107], v[204:207]\n"
"ds_read_b128 a[104:107], v3 offset:18432\n"
"v_mfma_f32_16x16x32_bf16 v[208:211], a[80:83], a[112:115], v[208:211]\n"
"v_mfma_f32_16x16x32_bf16 v[208:211], a[84:87], a[116:119], v[208:211]\n"
"global_load_dwordx4 a[68:71], v0, s[34:35] offset:-3072 sc1\n"
"v_mfma_f32_16x16x32_bf16 v[236:239], a[92:95], a[116:119], v[236:239]\n"
"ds_read_b128 a[116:119], v3 offset:21504\n"
"v_mfma_f32_16x16x32_bf16 v[236:239], a[88:91], a[112:115], v[236:239]\n"
"ds_read_b128 a[112:115], v3 offset:20480\n"
"v_mfma_f32_16x16x32_bf16 v[240:243], a[88:91], a[120:123], v[240:243]\n"
"v_mfma_f32_16x16x32_bf16 v[240:243], a[92:95], a[124:127], v[240:243]\n"
"ds_read_b128 v[24:27], v2 offset:29696\n"
"v_mfma_f32_16x16x32_bf16 v[212:215], a[84:87], a[124:127], v[212:215]\n"
"ds_read_b128 a[124:127], v3 offset:23552\n"
"v_mfma_f32_16x16x32_bf16 v[212:215], a[80:83], a[120:123], v[212:215]\n"
"ds_read_b128 a[120:123], v3 offset:22528\n"
"v_mfma_f32_16x16x32_bf16 v[216:219], a[80:83], a[128:131], v[216:219]\n"
"v_mfma_f32_16x16x32_bf16 v[216:219], a[84:87], a[132:135], v[216:219]\n"
"global_load_dwordx4 a[72:75], v0, s[34:35] offset:-2048 sc1\n"
"v_mfma_f32_16x16x32_bf16 v[244:247], a[92:95], a[132:135], v[244:247]\n"
"ds_read_b128 a[132:135], v3 offset:25600\n"
"v_mfma_f32_16x16x32_bf16 v[244:247], a[88:91], a[128:131], v[244:247]\n"
"ds_read_b128 a[128:131], v3 offset:24576\n"
"v_mfma_f32_16x16x32_bf16 v[248:251], a[88:91], a[136:139], v[248:251]\n"
"v_subrev_u32 v8, s65, v8\n"
"v_mfma_f32_16x16x32_bf16 v[248:251], a[92:95], a[140:143], v[248:251]\n"
"v_xor_b32 v9, s79, v9\n"
"v_mfma_f32_16x16x32_bf16 v[220:223], a[84:87], a[140:143], v[220:223]\n"
"ds_read_b128 a[140:143], v3 offset:27648\n"
"v_or_b32 v8, v8, v9\n"
"v_mfma_f32_16x16x32_bf16 v[220:223], a[80:83], a[136:139], v[220:223]\n"
"ds_read_b128 a[136:139], v3 offset:26624\n"
"v_cmp_lt_u32 vcc, 15, v8\n"
"v_mfma_f32_16x16x32_bf16 v[224:227], a[80:83], a[144:147], v[224:227]\n"
"s_waitcnt lgkmcnt(6)\n"
"global_store_dwordx4 v0, v[24:27], s[54:55] offset:1024 sc1\n"
"s_cbranch_vccnz L_slow0\n"
"L_back0:\n"
"v_mfma_f32_16x16x32_bf16 v[224:227], a[84:87], a[148:151], v[224:227]\n"
"global_load_dwordx4 a[76:79], v0, s[34:35] offset:-1024 sc1\n"
"v_readfirstlane_b32 s66, v9\n"
"v_mfma_f32_16x16x32_bf16 v[252:255], a[92:95], a[148:151], v[252:255]\n"
"ds_read_b128 a[148:151], v3 offset:29696\n"
"v_mfma_f32_16x16x32_bf16 v[252:255], a[88:91], a[144:147], v[252:255]\n"
"s_and_b32 s66, s66, 3\n"
"ds_read_b128 a[144:147], v3 offset:28672\n"
"v_mfma_f32_16x16x32_bf16 a[28:31], a[88:91], a[152:155], a[28:31]\n"
"s_lshl_b32 s69, s66, 13\n"
"v_mfma_f32_16x16x32_bf16 a[28:31], a[92:95], a[156:159], a[28:31]\n"
"s_cmp_eq_u32 s66, 3\n"
"s_cselect_b32 s75, 8192, 0\n"
"v_mfma_f32_16x16x32_bf16 a[24:27], a[84:87], a[156:159], a[24:27]\n"
"s_and_b32 s30, s66, s31\n"
"ds_read_b128 a[156:159], v3 offset:31744\n"
"v_mfma_f32_16x16x32_bf16 a[24:27], a[80:83], a[152:155], a[24:27]\n"
"s_waitcnt vmcnt(11)\n"
"ds_read_b128 a[152:155], v3 offset:30720\n"
"v_mfma_f32_16x16x32_bf16 v[32:35], a[32:35], a[96:99], v[32:35]\n"
"s_xor_b32 s32, s79, s66\n"
"v_mfma_f32_16x16x32_bf16 v[32:35], a[36:39], a[100:103], v[32:35]\n"
"global_load_dwordx4 a[80:83], v0, s[34:35] offset:0 sc1\n"
"v_mfma_f32_16x16x32_bf16 v[60:63], a[44:47], a[100:103], v[60:63]\n"
"v_mfma_f32_16x16x32_bf16 v[60:63], a[40:43], a[96:99], v[60:63]\n"
"v_mfma_f32_16x16x32_bf16 v[64:67], a[40:43], a[104:107], v[64:67]\n"
"v_mfma_f32_16x16x32_bf16 v[64:67], a[44:47], a[108:111], v[64:67]\n"
"v_mfma_f32_16x16x32_bf16 v[36:39], a[36:39], a[108:111], v[36:39]\n"
"s_add_u32 m0, s64, 0\n"
"v_mfma_f32_16x16x32_bf16 v[36:39], a[32:35], a[104:107], v[36:39]\n"
"s_waitcnt lgkmcnt(8)\n"
"v_mfma_f32_16x16x32_bf16 v[40:43], a[32:35], a[112:115], v[40:43]\n"
"v_mfma_f32_16x16x32_bf16 v[40:43], a[36:39], a[116:119], v[40:43]\n"
"global_load_dwordx4 a[84:87], v0, s[34:35] offset:1024 sc1\n"
"v_mfma_f32_16x16x32_bf16 v[68:71], a[44:47], a[116:119], v[68:71]\n"
"v_mfma_f32_16x16x32_bf16 v[68:71], a[40:43], a[112:115], v[68:71]\n"
"v_mfma_f32_16x16x32_bf16 v[72:75], a[40:43], a[120:123], v[72:75]\n"
"v_mfma_f32_16x16x32_bf16 v[72:75], a[44:47], a[124:127], v[72:75]\n"
"v_mfma_f32_16x16x32_bf16 v[44:47], a[36:39], a[124:127], v[44:47]\n"
"v_mfma_f32_16x16x32_bf16 v[44:47], a[32:35], a[120:123], v[44:47]\n"
"s_waitcnt lgkmcnt(4)\n"
"v_mfma_f32_16x16x32_bf16 v[48:51], a[32:35], a[128:131], v[48:51]\n"
"v_mfma_f32_16x16x32_bf16 v[48:51], a[36:39], a[132:135], v[48:51]\n"
"global_load_dwordx4 a[88:91], v0, s[34:35] offset:2048 sc1\n"
"v_mfma_f32_16x16x32_bf16 v[76:79], a[44:47], a[132:135], v[76:79]\n"
"v_mfma_f32_16x16x32_bf16 v[76:79], a[40:43], a[128:131], v[76:79]\n"
"v_mfma_f32_16x16x32_bf16 v[80:83], a[40:43], a[136:139], v[80:83]\n"
"v_mfma_f32_16x16x32_bf16 v[80:83], a[44:47], a[140:143], v[80:83]\n"
"buffer_load_dwordx4 v0, s[48:51], s75 offen offset:0 sc1 lds\n"
"v_mfma_f32_16x16x32_bf16 v[52:55], a[36:39], a[140:143], v[52:55]\n"
"v_mfma_f32_16x16x32_bf16 v[52:55], a[32:35], a[136:139], v[52:55]\n"
"s_waitcnt lgkmcnt(0)\n"
"v_mfma_f32_16x16x32_bf16 v[56:59], a[32:35], a[144:147], v[56:59]\n"
"v_mfma_f32_16x16x32_bf16 v[56:59], a[36:39], a[148:151], v[56:59]\n"
"global_load_dwordx4 a[92:95], v0, s[34:35] offset:3072 sc1\n"
"v_mfma_f32_16x16x32_bf16 v[84:87], a[44:47], a[148:151], v[84:87]\n"
"v_mfma_f32_16x16x32_bf16 v[84:87], a[40:43], a[144:147], v[84:87]\n"
"v_mfma_f32_16x16x32_bf16 a[4:7], a[40:43], a[152:155], a[4:7]\n"
"s_add_u32 s34, s34, 8192\n"
"v_mfma_f32_16x16x32_bf16 a[4:7], a[44:47], a[156:159], a[4:7]\n"
"v_mfma_f32_16x16x32_bf16 a[0:3], a[36:39], a[156:159], a[0:3]\n"
"v_mfma_f32_16x16x32_bf16 a[0:3], a[32:35], a[152:155], a[0:3]\n"
"s_waitcnt vmcnt(11)\n"
"v_mfma_f32_16x16x32_bf16 v[88:91], a[48:51], a[96:99], v[88:91]\n"
"v_mfma_f32_16x16x32_bf16 v[88:91], a[52:55], a[100:103], v[88:91]\n"
"global_load_dwordx4 a[32:35], v0, s[34:35] offset:-4096 sc1\n"
"v_mfma_f32_16x16x32_bf16 v[116:119], a[60:63], a[100:103], v[116:119]\n"
"v_mfma_f32_16x16x32_bf16 v[116:119], a[56:59], a[96:99], v[116:119]\n"
"buffer_load_dwordx4 v0, s[48:51], s75 offen offset:1024 sc1 lds\n"
"v_mfma_f32_16x16x32_bf16 v[120:123], a[56:59], a[104:107], v[120:123]\n"
"v_mfma_f32_16x16x32_bf16 v[120:123], a[60:63], a[108:111], v[120:123]\n"
"v_mfma_f32_16x16x32_bf16 v[92:95], a[52:55], a[108:111], v[92:95]\n"
"v_mfma_f32_16x16x32_bf16 v[92:95], a[48:51], a[104:107], v[92:95]\n"
"v_mfma_f32_16x16x32_bf16 v[96:99], a[48:51], a[112:115], v[96:99]\n"
"v_mfma_f32_16x16x32_bf16 v[96:99], a[52:55], a[116:119], v[96:99]\n"
"global_load_dwordx4 a[36:39], v0, s[34:35] offset:-3072 sc1\n"
"v_mfma_f32_16x16x32_bf16 v[124:127], a[60:63], a[116:119], v[124:127]\n"
"v_mfma_f32_16x16x32_bf16 v[124:127], a[56:59], a[112:115], v[124:127]\n"
"v_mfma_f32_16x16x32_bf16 v[128:131], a[56:59], a[120:123], v[128:131]\n"
"v_mfma_f32_16x16x32_bf16 v[128:131], a[60:63], a[124:127], v[128:131]\n"
"v_mfma_f32_16x16x32_bf16 v[100:103], a[52:55], a[124:127], v[100:103]\n"
"v_mfma_f32_16x16x32_bf16 v[100:103], a[48:51], a[120:123], v[100:103]\n"
"v_mfma_f32_16x16x32_bf16 v[104:107], a[48:51], a[128:131], v[104:107]\n"
"v_mfma_f32_16x16x32_bf16 v[104:107], a[52:55], a[132:135], v[104:107]\n"
"global_load_dwordx4 a[40:43], v0, s[34:35] offset:-2048 sc1\n"
"v_mfma_f32_16x16x32_bf16 v[132:135], a[60:63], a[132:135], v[132:135]\n"
"buffer_load_dwordx4 v0, s[48:51], s75 offen offset:2048 sc1 lds\n"
"v_mfma_f32_16x16x32_bf16 v[132:135], a[56:59], a[128:131], v[132:135]\n"
"v_mfma_f32_16x16x32_bf16 v[136:139], a[56:59], a[136:139], v[136:139]\n"
"v_mfma_f32_16x16x32_bf16 v[136:139], a[60:63], a[140:143], v[136:139]\n"
"v_mfma_f32_16x16x32_bf16 v[108:111], a[52:55], a[140:143], v[108:111]\n"
"v_mfma_f32_16x16x32_bf16 v[108:111], a[48:51], a[136:139], v[108:111]\n"
"v_mfma_f32_16x16x32_bf16 v[112:115], a[48:51], a[144:147], v[112:115]\n"
"v_mfma_f32_16x16x32_bf16 v[112:115], a[52:55], a[148:151], v[112:115]\n"
"global_load_dwordx4 a[44:47], v0, s[34:35] offset:-1024 sc1\n"
"v_mfma_f32_16x16x32_bf16 v[140:143], a[60:63], a[148:151], v[140:143]\n"
"v_mfma_f32_16x16x32_bf16 v[140:143], a[56:59], a[144:147], v[140:143]\n"
"v_mfma_f32_16x16x32_bf16 a[12:15], a[56:59], a[152:155], a[12:15]\n"
"v_mfma_f32_16x16x32_bf16 a[12:15], a[60:63], a[156:159], a[12:15]\n"
"v_mfma_f32_16x16x32_bf16 a[8:11], a[52:55], a[156:159], a[8:11]\n"
"v_mfma_f32_16x16x32_bf16 a[8:11], a[48:51], a[152:155], a[8:11]\n"
"s_waitcnt vmcnt(11)\n"
"v_mfma_f32_16x16x32_bf16 v[144:147], a[64:67], a[96:99], v[144:147]\n"
"buffer_load_dwordx4 v0, s[48:51], s75 offen offset:3072 sc1 lds\n"
"v_mfma_f32_16x16x32_bf16 v[144:147], a[68:71], a[100:103], v[144:147]\n"
"global_load_dwordx4 a[48:51], v0, s[34:35] offset:0 sc1\n"
"v_mfma_f32_16x16x32_bf16 v[172:175], a[76:79], a[100:103], v[172:175]\n"
"v_mfma_f32_16x16x32_bf16 v[172:175], a[72:75], a[96:99], v[172:175]\n"
"v_mfma_f32_16x16x32_bf16 v[176:179], a[72:75], a[104:107], v[176:179]\n"
"v_mfma_f32_16x16x32_bf16 v[176:179], a[76:79], a[108:111], v[176:179]\n"
"v_mfma_f32_16x16x32_bf16 v[148:151], a[68:71], a[108:111], v[148:151]\n"
"v_mfma_f32_16x16x32_bf16 v[148:151], a[64:67], a[104:107], v[148:151]\n"
"v_mfma_f32_16x16x32_bf16 v[152:155], a[64:67], a[112:115], v[152:155]\n"
"v_mfma_f32_16x16x32_bf16 v[152:155], a[68:71], a[116:119], v[152:155]\n"
"global_load_dwordx4 a[52:55], v0, s[34:35] offset:1024 sc1\n"
"v_mfma_f32_16x16x32_bf16 v[180:183], a[76:79], a[116:119], v[180:183]\n"
"v_mfma_f32_16x16x32_bf16 v[180:183], a[72:75], a[112:115], v[180:183]\n"
"v_mfma_f32_16x16x32_bf16 v[184:187], a[72:75], a[120:123], v[184:187]\n"
"v_mfma_f32_16x16x32_bf16 v[184:187], a[76:79], a[124:127], v[184:187]\n"
"v_mfma_f32_16x16x32_bf16 v[156:159], a[68:71], a[124:127], v[156:159]\n"
"v_mfma_f32_16x16x32_bf16 v[156:159], a[64:67], a[120:123], v[156:159]\n"
"s_add_u32 m0, s64, 4096\n"
"v_mfma_f32_16x16x32_bf16 v[160:163], a[64:67], a[128:131], v[160:163]\n"
"v_mfma_f32_16x16x32_bf16 v[160:163], a[68:71], a[132:135], v[160:163]\n"
"global_load_dwordx4 a[56:59], v0, s[34:35] offset:2048 sc1\n"
"v_mfma_f32_16x16x32_bf16 v[188:191], a[76:79], a[132:135], v[188:191]\n"
"v_mfma_f32_16x16x32_bf16 v[188:191], a[72:75], a[128:131], v[188:191]\n"
"v_mfma_f32_16x16x32_bf16 v[192:195], a[72:75], a[136:139], v[192:195]\n"
"v_mfma_f32_16x16x32_bf16 v[192:195], a[76:79], a[140:143], v[192:195]\n"
"v_mfma_f32_16x16x32_bf16 v[164:167], a[68:71], a[140:143], v[164:167]\n"
"v_mfma_f32_16x16x32_bf16 v[164:167], a[64:67], a[136:139], v[164:167]\n"
"v_mfma_f32_16x16x32_bf16 v[168:171], a[64:67], a[144:147], v[168:171]\n"
"v_mfma_f32_16x16x32_bf16 v[168:171], a[68:71], a[148:151], v[168:171]\n"
"global_load_dwordx4 a[60:63], v0, s[34:35] offset:3072 sc1\n"
"v_mfma_f32_16x16x32_bf16 v[196:199], a[76:79], a[148:151], v[196:199]\n"
"v_mfma_f32_16x16x32_bf16 v[196:199], a[72:75], a[144:147], v[196:199]\n"
"v_mfma_f32_16x16x32_bf16 a[20:23], a[72:75], a[152:155], a[20:23]\n"
"s_add_u32 s34, s34, 8192\n"
"v_mfma_f32_16x16x32_bf16 a[20:23], a[76:79], a[156:159], a[20:23]\n"
"v_mfma_f32_16x16x32_bf16 a[16:19], a[68:71], a[156:159], a[16:19]\n"
"buffer_load_dwordx4 v1, s[48:51], s75 offen offset:0 sc1 lds\n"
"v_mfma_f32_16x16x32_bf16 a[16:19], a[64:67], a[152:155], a[16:19]\n"
"s_waitcnt vmcnt(12)\n"
"v_mfma_f32_16x16x32_bf16 v[200:203], a[80:83], a[96:99], v[200:203]\n"
"v_mfma_f32_16x16x32_bf16 v[200:203], a[84:87], a[100:103], v[200:203]\n"
"global_load_dwordx4 a[64:67], v0, s[34:35] offset:-4096 sc1\n"
"v_mfma_f32_16x16x32_bf16 v[228:231], a[92:95], a[100:103], v[228:231]\n"
"ds_read_b128 a[100:103], v4 offset:17408\n"
"v_mfma_f32_16x16x32_bf16 v[228:231], a[88:91], a[96:99], v[228:231]\n"
"ds_read_b128 a[96:99], v4 offset:16384\n"
"v_mfma_f32_16x16x32_bf16 v[232:235], a[88:91], a[104:107], v[232:235]\n"
"v_mfma_f32_16x16x32_bf16 v[232:235], a[92:95], a[108:111], v[232:235]\n"
"v_mfma_f32_16x16x32_bf16 v[204:207], a[84:87], a[108:111], v[204:207]\n"
"ds_read_b128 a[108:111], v4 offset:19456\n"
"v_mfma_f32_16x16x32_bf16 v[204:207], a[80:83], a[104:107], v[204:207]\n"
"ds_read_b128 a[104:107], v4 offset:18432\n"
"v_mfma_f32_16x16x32_bf16 v[208:211], a[80:83], a[112:115], v[208:211]\n"
"v_mfma_f32_16x16x32_bf16 v[208:211], a[84:87], a[116:119], v[208:211]\n"
"global_load_dwordx4 a[68:71], v0, s[34:35] offset:-3072 sc1\n"
"v_mfma_f32_16x16x32_bf16 v[236:239], a[92:95], a[116:119], v[236:239]\n"
"ds_read_b128 a[116:119], v4 offset:21504\n"
"v_mfma_f32_16x16x32_bf16 v[236:239], a[88:91], a[112:115], v[236:239]\n"
"ds_read_b128 a[112:115], v4 offset:20480\n"
"v_mfma_f32_16x16x32_bf16 v[240:243], a[88:91], a[120:123], v[240:243]\n"
"v_mfma_f32_16x16x32_bf16 v[240:243], a[92:95], a[124:127], v[240:243]\n"
"buffer_load_dwordx4 v1, s[48:51], s75 offen offset:1024 sc1 lds\n"
"v_mfma_f32_16x16x32_bf16 v[212:215], a[84:87], a[124:127], v[212:215]\n"
"ds_read_b128 a[124:127], v4 offset:23552\n"
"v_mfma_f32_16x16x32_bf16 v[212:215], a[80:83], a[120:123], v[212:215]\n"
"ds_read_b128 a[120:123], v4 offset:22528\n"
"v_mfma_f32_16x16x32_bf16 v[216:219], a[80:83], a[128:131], v[216:219]\n"
"v_mfma_f32_16x16x32_bf16 v[216:219], a[84:87], a[132:135], v[216:219]\n"
"global_load_dwordx4 a[72:75], v0, s[34:35] offset:-2048 sc1\n"
"v_mfma_f32_16x16x32_bf16 v[244:247], a[92:95], a[132:135], v[244:247]\n"
"ds_read_b128 a[132:135], v4 offset:25600\n"
"v_mfma_f32_16x16x32_bf16 v[244:247], a[88:91], a[128:131], v[244:247]\n"
"ds_read_b128 a[128:131], v4 offset:24576\n"
"v_mfma_f32_16x16x32_bf16 v[248:251], a[88:91], a[136:139], v[248:251]\n"
"v_mfma_f32_16x16x32_bf16 v[248:251], a[92:95], a[140:143], v[248:251]\n"
"v_mfma_f32_16x16x32_bf16 v[220:223], a[84:87], a[140:143], v[220:223]\n"
"ds_read_b128 a[140:143], v4 offset:27648\n"
"v_mfma_f32_16x16x32_bf16 v[220:223], a[80:83], a[136:139], v[220:223]\n"
"ds_read_b128 a[136:139], v4 offset:26624\n"
"v_mfma_f32_16x16x32_bf16 v[224:227], a[80:83], a[144:147], v[224:227]\n"
"v_mfma_f32_16x16x32_bf16 v[224:227], a[84:87], a[148:151], v[224:227]\n"
"global_load_dwordx4 a[76:79], v0, s[34:35] offset:-1024 sc1\n"
"v_mfma_f32_16x16x32_bf16 v[252:255], a[92:95], a[148:151], v[252:255]\n"
"ds_read_b128 a[148:151], v4 offset:29696\n"
"v_mfma_f32_16x16x32_bf16 v[252:255], a[88:91], a[144:147], v[252:255]\n"
"ds_read_b128 a[144:147], v4 offset:28672\n"
"v_mfma_f32_16x16x32_bf16 a[28:31], a[88:91], a[152:155], a[28:31]\n"
"buffer_load_dwordx4 v1, s[48:51], s75 offen offset:2048 sc1 lds\n"
"v_mfma_f32_16x16x32_bf16 a[28:31], a[92:95], a[156:159], a[28:31]\n"
"v_mfma_f32_16x16x32_bf16 a[24:27], a[84:87], a[156:159], a[24:27]\n"
"ds_read_b128 a[156:159], v4 offset:31744\n"
"v_mfma_f32_16x16x32_bf16 a[24:27], a[80:83], a[152:155], a[24:27]\n"
"s_waitcnt vmcnt(12) lgkmcnt(11)\n"
"ds_read_b128 a[152:155], v4 offset:30720\n"
"v_mfma_f32_16x16x32_bf16 v[32:35], a[32:35], a[96:99], v[32:35]\n"
"v_mfma_f32_16x16x32_bf16 v[32:35], a[36:39], a[100:103], v[32:35]\n"
"global_load_dwordx4 a[80:83], v0, s[34:35] offset:0 sc1\n"
"v_mfma_f32_16x16x32_bf16 v[60:63], a[44:47], a[100:103], v[60:63]\n"
"v_mfma_f32_16x16x32_bf16 v[60:63], a[40:43], a[96:99], v[60:63]\n"
"v_mfma_f32_16x16x32_bf16 v[64:67], a[40:43], a[104:107], v[64:67]\n"
"v_mfma_f32_16x16x32_bf16 v[64:67], a[44:47], a[108:111], v[64:67]\n"
"v_mfma_f32_16x16x32_bf16 v[36:39], a[36:39], a[108:111], v[36:39]\n"
"v_mfma_f32_16x16x32_bf16 v[36:39], a[32:35], a[104:107], v[36:39]\n"
"s_waitcnt lgkmcnt(8)\n"
"v_mfma_f32_16x16x32_bf16 v[40:43], a[32:35], a[112:115], v[40:43]\n"
"v_mfma_f32_16x16x32_bf16 v[40:43], a[36:39], a[116:119], v[40:43]\n"
"global_load_dwordx4 a[84:87], v0, s[34:35] offset:1024 sc1\n"
"s_add_u32 s65, s65, 1\n"
"v_mfma_f32_16x16x32_bf16 v[68:71], a[44:47], a[116:119], v[68:71]\n"
"buffer_load_dwordx4 v1, s[48:51], s75 offen offset:3072 sc1 lds\n"
"v_mfma_f32_16x16x32_bf16 v[68:71], a[40:43], a[112:115], v[68:71]\n"
"v_mov_b32 v8, s65\n"
"v_mfma_f32_16x16x32_bf16 v[72:75], a[40:43], a[120:123], v[72:75]\n"
"v_mov_b32 v9, s60\n"
"v_mfma_f32_16x16x32_bf16 v[72:75], a[44:47], a[124:127], v[72:75]\n"
"v_mfma_f32_16x16x32_bf16 v[44:47], a[36:39], a[124:127], v[44:47]\n"
"buffer_store_dwordx2 v[8:9], off, s[56:59], %13 sc1\n"
"v_mfma_f32_16x16x32_bf16 v[44:47], a[32:35], a[120:123], v[44:47]\n"
"s_waitcnt lgkmcnt(4)\n"
"v_mfma_f32_16x16x32_bf16 v[48:51], a[32:35], a[128:131], v[48:51]\n"
"v_mfma_f32_16x16x32_bf16 v[48:51], a[36:39], a[132:135], v[48:51]\n"
"global_load_dwordx4 a[88:91], v0, s[34:35] offset:2048 sc1\n"
"v_mfma_f32_16x16x32_bf16 v[76:79], a[44:47], a[132:135], v[76:79]\n"
"v_mfma_f32_16x16x32_bf16 v[76:79], a[40:43], a[128:131], v[76:79]\n"
"v_mfma_f32_16x16x32_bf16 v[80:83], a[40:43], a[136:139], v[80:83]\n"
"v_mfma_f32_16x16x32_bf16 v[80:83], a[44:47], a[140:143], v[80:83]\n"
"v_mfma_f32_16x16x32_bf16 v[52:55], a[36:39], a[140:143], v[52:55]\n"
"v_mfma_f32_16x16x32_bf16 v[52:55], a[32:35], a[136:139], v[52:55]\n"
"s_waitcnt lgkmcnt(0)\n"
"v_mfma_f32_16x16x32_bf16 v[56:59], a[32:35], a[144:147], v[56:59]\n"
"v_mfma_f32_16x16x32_bf16 v[56:59], a[36:39], a[148:151], v[56:59]\n"
"s_add_u32 m0, s64, 8192\n"
"global_load_dwordx4 a[92:95], v0, s[34:35] offset:3072 sc1\n"
"v_mfma_f32_16x16x32_bf16 v[84:87], a[44:47], a[148:151], v[84:87]\n"
"v_mfma_f32_16x16x32_bf16 v[84:87], a[40:43], a[144:147], v[84:87]\n"
"v_mfma_f32_16x16x32_bf16 a[4:7], a[40:43], a[152:155], a[4:7]\n"
"s_add_u32 s34, s34, 8192\n"
"v_mfma_f32_16x16x32_bf16 a[4:7], a[44:47], a[156:159], a[4:7]\n"
"v_mfma_f32_16x16x32_bf16 a[0:3], a[36:39], a[156:159], a[0:3]\n"
"v_mfma_f32_16x16x32_bf16 a[0:3], a[32:35], a[152:155], a[0:3]\n"
"s_waitcnt vmcnt(13)\n"
"v_mfma_f32_16x16x32_bf16 v[88:91], a[48:51], a[96:99], v[88:91]\n"
"v_mfma_f32_16x16x32_bf16 v[88:91], a[52:55], a[100:103], v[88:91]\n"
"global_load_dwordx4 a[32:35], v0, s[34:35] offset:-4096 sc1\n"
"v_mfma_f32_16x16x32_bf16 v[116:119], a[60:63], a[100:103], v[116:119]\n"
"v_mfma_f32_16x16x32_bf16 v[116:119], a[56:59], a[96:99], v[116:119]\n"
"v_mfma_f32_16x16x32_bf16 v[120:123], a[56:59], a[104:107], v[120:123]\n"
"v_mfma_f32_16x16x32_bf16 v[120:123], a[60:63], a[108:111], v[120:123]\n"
"v_mfma_f32_16x16x32_bf16 v[92:95], a[52:55], a[108:111], v[92:95]\n"
"v_mfma_f32_16x16x32_bf16 v[92:95], a[48:51], a[104:107], v[92:95]\n"
"v_mfma_f32_16x16x32_bf16 v[96:99], a[48:51], a[112:115], v[96:99]\n"
"buffer_load_dwordx4 v0, s[48:51], s69 offen offset:0 sc1 lds\n"
"v_mfma_f32_16x16x32_bf16 v[96:99], a[52:55], a[116:119], v[96:99]\n"
"global_load_dwordx4 a[36:39], v0, s[34:35] offset:-3072 sc1\n"
"v_mfma_f32_16x16x32_bf16 v[124:127], a[60:63], a[116:119], v[124:127]\n"
"v_mfma_f32_16x16x32_bf16 v[124:127], a[56:59], a[112:115], v[124:127]\n"
"v_mfma_f32_16x16x32_bf16 v[128:131], a[56:59], a[120:123], v[128:131]\n"
"v_mfma_f32_16x16x32_bf16 v[128:131], a[60:63], a[124:127], v[128:131]\n"
"v_mfma_f32_16x16x32_bf16 v[100:103], a[52:55], a[124:127], v[100:103]\n"
"v_mfma_f32_16x16x32_bf16 v[100:103], a[48:51], a[120:123], v[100:103]\n"
"v_mfma_f32_16x16x32_bf16 v[104:107], a[48:51], a[128:131], v[104:107]\n"
"v_mfma_f32_16x16x32_bf16 v[104:107], a[52:55], a[132:135], v[104:107]\n"
"global_load_dwordx4 a[40:43], v0, s[34:35] offset:-2048 sc1\n"
"v_mfma_f32_16x16x32_bf16 v[132:135], a[60:63], a[132:135], v[132:135]\n"
"v_mfma_f32_16x16x32_bf16 v[132:135], a[56:59], a[128:131], v[132:135]\n"
"v_mfma_f32_16x16x32_bf16 v[136:139], a[56:59], a[136:139], v[136:139]\n"
"v_mfma_f32_16x16x32_bf16 v[136:139], a[60:63], a[140:143], v[136:139]\n"
"v_mfma_f32_16x16x32_bf16 v[108:111], a[52:55], a[140:143], v[108:111]\n"
"buffer_load_dwordx4 v0, s[48:51], s69 offen offset:1024 sc1 lds\n"
"v_mfma_f32_16x16x32_bf16 v[108:111], a[48:51], a[136:139], v[108:111]\n"
"v_mfma_f32_16x16x32_bf16 v[112:115], a[48:51], a[144:147], v[112:115]\n"
"v_mfma_f32_16x16x32_bf16 v[112:115], a[52:55], a[148:151], v[112:115]\n"
"global_load_dwordx4 a[44:47], v0, s[34:35] offset:-1024 sc1\n"
"v_mfma_f32_16x16x32_bf16 v[140:143], a[60:63], a[148:151], v[140:143]\n"
"v_mfma_f32_16x16x32_bf16 v[140:143], a[56:59], a[144:147], v[140:143]\n"
"v_mfma_f32_16x16x32_bf16 a[12:15], a[56:59], a[152:155], a[12:15]\n"
"v_mfma_f32_16x16x32_bf16 a[12:15], a[60:63], a[156:159], a[12:15]\n"
"v_mfma_f32_16x16x32_bf16 a[8:11], a[52:55], a[156:159], a[8:11]\n"
"v_mfma_f32_16x16x32_bf16 a[8:11], a[48:51], a[152:155], a[8:11]\n"
"s_waitcnt vmcnt(13)\n"
"v_mfma_f32_16x16x32_bf16 v[144:147], a[64:67], a[96:99], v[144:147]\n"
"v_mfma_f32_16x16x32_bf16 v[144:147], a[68:71], a[100:103], v[144:147]\n"
"global_load_dwordx4 a[48:51], v0, s[34:35] offset:0 sc1\n"
"v_mfma_f32_16x16x32_bf16 v[172:175], a[76:79], a[100:103], v[172:175]\n"
"v_mfma_f32_16x16x32_bf16 v[172:175], a[72:75], a[96:99], v[172:175]\n"
"v_mfma_f32_16x16x32_bf16 v[176:179], a[72:75], a[104:107], v[176:179]\n"
"v_mfma_f32_16x16x32_bf16 v[176:179], a[76:79], a[108:111], v[176:179]\n"
"buffer_load_dwordx4 v0, s[48:51], s69 offen offset:2048 sc1 lds\n"
"v_mfma_f32_16x16x32_bf16 v[148:151], a[68:71], a[108:111], v[148:151]\n"
"v_mfma_f32_16x16x32_bf16 v[148:151], a[64:67], a[104:107], v[148:151]\n"
"v_mfma_f32_16x16x32_bf16 v[152:155], a[64:67], a[112:115], v[152:155]\n"
"v_lshlrev_b32 v16, 16, v18\n"
"v_mfma_f32_16x16x32_bf16 v[152:155], a[68:71], a[116:119], v[152:155]\n"
"global_load_dwordx4 a[52:55], v0, s[34:35] offset:1024 sc1\n"
"v_and_b32 v17, s63, v18\n"
"v_mfma_f32_16x16x32_bf16 v[180:183], a[76:79], a[116:119], v[180:183]\n"
"v_lshlrev_b32 v18, 16, v19\n"
"v_mfma_f32_16x16x32_bf16 v[180:183], a[72:75], a[112:115], v[180:183]\n"
"v_and_b32 v19, s63, v19\n"
"v_mfma_f32_16x16x32_bf16 v[184:187], a[72:75], a[120:123], v[184:187]\n"
"v_add_f32 v32, v32, v16\n"
"v_mfma_f32_16x16x32_bf16 v[184:187], a[76:79], a[124:127], v[184:187]\n"
"v_add_f32 v33, v33, v17\n"
"v_add_f32 v34, v34, v18\n"
"v_mfma_f32_16x16x32_bf16 v[156:159], a[68:71], a[124:127], v[156:159]\n"
"v_add_f32 v35, v35, v19\n"
"v_mfma_f32_16x16x32_bf16 v[156:159], a[64:67], a[120:123], v[156:159]\n"
"v_add_f32 v36, v36, v16\n"
"v_mfma_f32_16x16x32_bf16 v[160:163], a[64:67], a[128:131], v[160:163]\n"
"v_add_f32 v37, v37, v17\n"
"v_mfma_f32_16x16x32_bf16 v[160:163], a[68:71], a[132:135], v[160:163]\n"
"global_load_dwordx4 a[56:59], v0, s[34:35] offset:2048 sc1\n"
"v_add_f32 v38, v38, v18\n"
"v_mfma_f32_16x16x32_bf16 v[188:191], a[76:79], a[132:135], v[188:191]\n"
"v_add_f32 v39, v39, v19\n"
"v_mfma_f32_16x16x32_bf16 v[188:191], a[72:75], a[128:131], v[188:191]\n"
"v_add_f32 v40, v40, v16\n"
"v_add_f32 v41, v41, v17\n"
"v_mfma_f32_16x16x32_bf16 v[192:195], a[72:75], a[136:139], v[192:195]\n"
"buffer_load_dwordx4 v0, s[48:51], s69 offen offset:3072 sc1 lds\n"
"v_mfma_f32_16x16x32_bf16 v[192:195], a[76:79], a[140:143], v[192:195]\n"
"v_add_f32 v42, v42, v18\n"
"v_mfma_f32_16x16x32_bf16 v[164:167], a[68:71], a[140:143], v[164:167]\n"
"v_add_f32 v43, v43, v19\n"
"v_mfma_f32_16x16x32_bf16 v[164:167], a[64:67], a[136:139], v[164:167]\n"
"v_add_f32 v44, v44, v16\n"
"v_add_f32 v45, v45, v17\n"
"v_mfma_f32_16x16x32_bf16 v[168:171], a[64:67], a[144:147], v[168:171]\n"
"v_add_f32 v46, v46, v18\n"
"v_mfma_f32_16x16x32_bf16 v[168:171], a[68:71], a[148:151], v[168:171]\n"
"global_load_dwordx4 a[60:63], v0, s[34:35] offset:3072 sc1\n"
"v_add_f32 v47, v47, v19\n"
"v_mfma_f32_16x16x32_bf16 v[196:199], a[76:79], a[148:151], v[196:199]\n"
"v_add_f32 v48, v48, v16\n"
"v_mfma_f32_16x16x32_bf16 v[196:199], a[72:75], a[144:147], v[196:199]\n"
"v_add_f32 v49, v49, v17\n"
"v_mfma_f32_16x16x32_bf16 a[20:23], a[72:75], a[152:155], a[20:23]\n"
"s_add_u32 s34, s34, 8192\n"
"v_add_f32 v50, v50, v18\n"
"v_mfma_f32_16x16x32_bf16 a[20:23], a[76:79], a[156:159], a[20:23]\n"
"v_add_f32 v51, v51, v19\n"
"v_mfma_f32_16x16x32_bf16 a[16:19], a[68:71], a[156:159], a[16:19]\n"
"v_add_f32 v52, v52, v16\n"
"v_mfma_f32_16x16x32_bf16 a[16:19], a[64:67], a[152:155], a[16:19]\n"
"s_waitcnt vmcnt(12)\n"
"v_add_f32 v53, v53, v17\n"
"v_mfma_f32_16x16x32_bf16 v[200:203], a[80:83], a[96:99], v[200:203]\n"
"v_add_f32 v54, v54, v18\n"
"v_add_f32 v55, v55, v19\n"
"v_mfma_f32_16x16x32_bf16 v[200:203], a[84:87], a[100:103], v[200:203]\n"
"global_load_dwordx4 a[64:67], v0, s[34:35] offset:-4096 sc1\n"
"v_add_f32 v56, v56, v16\n"
"v_mfma_f32_16x16x32_bf16 v[228:231], a[92:95], a[100:103], v[228:231]\n"
"s_add_u32 m0, s64, 12288\n"
"ds_read_b128 a[100:103], v5 offset:17408\n"
"v_add_f32 v57, v57, v17\n"
"v_mfma_f32_16x16x32_bf16 v[228:231], a[88:91], a[96:99], v[228:231]\n"
"ds_read_b128 a[96:99], v5 offset:16384\n"
"v_add_f32 v58, v58, v18\n"
"v_mfma_f32_16x16x32_bf16 v[232:235], a[88:91], a[104:107], v[232:235]\n"
"v_add_f32 v59, v59, v19\n"
"v_mfma_f32_16x16x32_bf16 v[232:235], a[92:95], a[108:111], v[232:235]\n"
"v_accvgpr_read_b32 v12, a0\n"
"v_mfma_f32_16x16x32_bf16 v[204:207], a[84:87], a[108:111], v[204:207]\n"
"ds_read_b128 a[108:111], v5 offset:19456\n"
"v_accvgpr_read_b32 v13, a1\n"
"v_mfma_f32_16x16x32_bf16 v[204:207], a[80:83], a[104:107], v[204:207]\n"
"ds_read_b128 a[104:107], v5 offset:18432\n"
"v_accvgpr_read_b32 v14, a2\n"
"v_mfma_f32_16x16x32_bf16 v[208:211], a[80:83], a[112:115], v[208:211]\n"
"v_accvgpr_read_b32 v15, a3\n"
"v_mfma_f32_16x16x32_bf16 v[208:211], a[84:87], a[116:119], v[208:211]\n"
"global_load_dwordx4 a[68:71], v0, s[34:35] offset:-3072 sc1\n"
"v_add_f32 v12, v12, v16\n"
"v_mfma_f32_16x16x32_bf16 v[236:239], a[92:95], a[116:119], v[236:239]\n"
"ds_read_b128 a[116:119], v5 offset:21504\n"
"v_add_f32 v13, v13, v17\n"
"v_mfma_f32_16x16x32_bf16 v[236:239], a[88:91], a[112:115], v[236:239]\n"
"ds_read_b128 a[112:115], v5 offset:20480\n"
"v_add_f32 v14, v14, v18\n"
"v_mfma_f32_16x16x32_bf16 v[240:243], a[88:91], a[120:123], v[240:243]\n"
"v_add_f32 v15, v15, v19\n"
"v_accvgpr_write_b32 a0, v12\n"
"v_mfma_f32_16x16x32_bf16 v[240:243], a[92:95], a[124:127], v[240:243]\n"
"v_accvgpr_write_b32 a1, v13\n"
"v_mfma_f32_16x16x32_bf16 v[212:215], a[84:87], a[124:127], v[212:215]\n"
"ds_read_b128 a[124:127], v5 offset:23552\n"
"v_accvgpr_write_b32 a2, v14\n"
"v_mfma_f32_16x16x32_bf16 v[212:215], a[80:83], a[120:123], v[212:215]\n"
"ds_read_b128 a[120:123], v5 offset:22528\n"
"v_accvgpr_write_b32 a3, v15\n"
"v_mfma_f32_16x16x32_bf16 v[216:219], a[80:83], a[128:131], v[216:219]\n"
"buffer_load_dwordx4 v1, s[48:51], s69 offen offset:0 sc1 lds\n"
"v_mfma_f32_16x16x32_bf16 v[216:219], a[84:87], a[132:135], v[216:219]\n"
"global_load_dwordx4 a[72:75], v0, s[34:35] offset:-2048 sc1\n"
"v_lshrrev_b32 v16, 4, v0\n"
"v_mfma_f32_16x16x32_bf16 v[244:247], a[92:95], a[132:135], v[244:247]\n"
"ds_read_b128 a[132:135], v5 offset:25600\n"
"v_and_b32 v16, 0x30, v16\n"
"v_mfma_f32_16x16x32_bf16 v[244:247], a[88:91], a[128:131], v[244:247]\n"
"ds_read_b128 a[128:131], v5 offset:24576\n"
"buffer_load_dwordx2 v[18:19], v16, s[44:47], s61 offen offset:64\n"
"v_mfma_f32_16x16x32_bf16 v[248:251], a[88:91], a[136:139], v[248:251]\n"
"s_getpc_b64 s[96:97]\n"
"L_c3_:\n"
"s_add_u32 s96, s96, L_last-L_c3_\n"
"s_addc_u32 s97, s97, 0\n"
"v_mfma_f32_16x16x32_bf16 v[248:251], a[92:95], a[140:143], v[248:251]\n"
"s_lshr_b32 s66, %26, 3\n"
"v_mfma_f32_16x16x32_bf16 v[220:223], a[84:87], a[140:143], v[220:223]\n"
"s_lshl2_add_u32 s66, s66, %16\n"
"ds_read_b128 a[140:143], v5 offset:27648\n"
"v_mfma_f32_16x16x32_bf16 v[220:223], a[80:83], a[136:139], v[220:223]\n"
"ds_read_b128 a[136:139], v5 offset:26624\n"
"v_mfma_f32_16x16x32_bf16 v[224:227], a[80:83], a[144:147], v[224:227]\n"
"s_mul_i32 s66, s66, 256\n"
"v_mfma_f32_16x16x32_bf16 v[224:227], a[84:87], a[148:151], v[224:227]\n"
"global_load_dwordx4 a[76:79], v0, s[34:35] offset:-1024 sc1\n"
"v_mfma_f32_16x16x32_bf16 v[252:255], a[92:95], a[148:151], v[252:255]\n"
"ds_read_b128 a[148:151], v5 offset:29696\n"
"v_mfma_f32_16x16x32_bf16 v[252:255], a[88:91], a[144:147], v[252:255]\n"
"s_min_u32 s66, s66, L_headF-L_last-256\n"
"ds_read_b128 a[144:147], v5 offset:28672\n"
"v_mfma_f32_16x16x32_bf16 a[28:31], a[88:91], a[152:155], a[28:31]\n"
"v_mfma_f32_16x16x32_bf16 a[28:31], a[92:95], a[156:159], a[28:31]\n"
"v_min_u32 v11, 16, v0\n"
"v_mfma_f32_16x16x32_bf16 a[24:27], a[84:87], a[156:159], a[24:27]\n"
"ds_read_b128 a[156:159], v5 offset:31744\n"
"buffer_load_dwordx2 v[8:9], off, s[56:59], %14 sc1\n"
"v_mfma_f32_16x16x32_bf16 a[24:27], a[80:83], a[152:155], a[24:27]\n"
"s_waitcnt vmcnt(13) lgkmcnt(11)\n"
"ds_read_b128 a[152:155], v5 offset:30720\n"
"v_mfma_f32_16x16x32_bf16 v[32:35], a[32:35], a[96:99], v[32:35]\n"
"buffer_load_dwordx4 v1, s[48:51], s69 offen offset:1024 sc1 lds\n"
"v_mfma_f32_16x16x32_bf16 v[32:35], a[36:39], a[100:103], v[32:35]\n"
"global_load_dwordx4 a[80:83], v0, s[34:35] offset:0 sc1\n"
"v_mfma_f32_16x16x32_bf16 v[60:63], a[44:47], a[100:103], v[60:63]\n"
"v_lshl_add_u32 v11, v11, 3, s66\n"
"v_mfma_f32_16x16x32_bf16 v[60:63], a[40:43], a[96:99], v[60:63]\n"
"global_load_dword v10, v11, s[96:97]\n"
"v_mfma_f32_16x16x32_bf16 v[64:67], a[40:43], a[104:107], v[64:67]\n"
"v_mfma_f32_16x16x32_bf16 v[64:67], a[44:47], a[108:111], v[64:67]\n"
"v_mfma_f32_16x16x32_bf16 v[36:39], a[36:39], a[108:111], v[36:39]\n"
"v_mfma_f32_16x16x32_bf16 v[36:39], a[32:35], a[104:107], v[36:39]\n"
"s_waitcnt lgkmcnt(8)\n"
"v_mfma_f32_16x16x32_bf16 v[40:43], a[32:35], a[112:115], v[40:43]\n"
"v_mfma_f32_16x16x32_bf16 v[40:43], a[36:39], a[116:119], v[40:43]\n"
"global_load_dwordx4 a[84:87], v0, s[34:35] offset:1024 sc1\n"
"v_mfma_f32_16x16x32_bf16 v[68:71], a[44:47], a[116:119], v[68:71]\n"
"v_mfma_f32_16x16x32_bf16 v[68:71], a[40:43], a[112:115], v[68:71]\n"
"v_mfma_f32_16x16x32_bf16 v[72:75], a[40:43], a[120:123], v[72:75]\n"
"v_mfma_f32_16x16x32_bf16 v[72:75], a[44:47], a[124:127], v[72:75]\n"
"v_mfma_f32_16x16x32_bf16 v[44:47], a[36:39], a[124:127], v[44:47]\n"
"buffer_load_dwordx4 v1, s[48:51], s69 offen offset:2048 sc1 lds\n"
"v_mfma_f32_16x16x32_bf16 v[44:47], a[32:35], a[120:123], v[44:47]\n"
"s_waitcnt lgkmcnt(4)\n"
"v_mfma_f32_16x16x32_bf16 v[48:51], a[32:35], a[128:131], v[48:51]\n"
"v_mfma_f32_16x16x32_bf16 v[48:51], a[36:39], a[132:135], v[48:51]\n"
"global_load_dwordx4 a[88:91], v0, s[34:35] offset:2048 sc1\n"
"v_mfma_f32_16x16x32_bf16 v[76:79], a[44:47], a[132:135], v[76:79]\n"
"v_mfma_f32_16x16x32_bf16 v[76:79], a[40:43], a[128:131], v[76:79]\n"
"v_mfma_f32_16x16x32_bf16 v[80:83], a[40:43], a[136:139], v[80:83]\n"
"v_mfma_f32_16x16x32_bf16 v[80:83], a[44:47], a[140:143], v[80:83]\n"
"v_mfma_f32_16x16x32_bf16 v[52:55], a[36:39], a[140:143], v[52:55]\n"
"v_mfma_f32_16x16x32_bf16 v[52:55], a[32:35], a[136:139], v[52:55]\n"
"s_waitcnt lgkmcnt(0)\n"
"v_mfma_f32_16x16x32_bf16 v[56:59], a[32:35], a[144:147], v[56:59]\n"
"v_mfma_f32_16x16x32_bf16 v[56:59], a[36:39], a[148:151], v[56:59]\n"
"global_load_dwordx4 a[92:95], v0, s[34:35] offset:3072 sc1\n"
"v_mfma_f32_16x16x32_bf16 v[84:87], a[44:47], a[148:151], v[84:87]\n"
"v_mfma_f32_16x16x32_bf16 v[84:87], a[40:43], a[144:147], v[84:87]\n"
"v_mfma_f32_16x16x32_bf16 a[4:7], a[40:43], a[152:155], a[4:7]\n"
"s_add_u32 s34, s34, 8192\n"
"v_mfma_f32_16x16x32_bf16 a[4:7], a[44:47], a[156:159], a[4:7]\n"
"buffer_load_dwordx4 v1, s[48:51], s69 offen offset:3072 sc1 lds\n"
"v_mfma_f32_16x16x32_bf16 a[0:3], a[36:39], a[156:159], a[0:3]\n"
"v_mfma_f32_16x16x32_bf16 a[0:3], a[32:35], a[152:155], a[0:3]\n"
"s_waitcnt vmcnt(15)\n"
"v_mfma_f32_16x16x32_bf16 v[88:91], a[48:51], a[96:99], v[88:91]\n"
"v_mfma_f32_16x16x32_bf16 v[88:91], a[52:55], a[100:103], v[88:91]\n"
"global_load_dwordx4 a[32:35], v0, s[34:35] offset:-4096 sc1\n"
"v_mfma_f32_16x16x32_bf16 v[116:119], a[60:63], a[100:103], v[116:119]\n"
"v_mfma_f32_16x16x32_bf16 v[116:119], a[56:59], a[96:99], v[116:119]\n"
"v_mfma_f32_16x16x32_bf16 v[120:123], a[56:59], a[104:107], v[120:123]\n"
"v_mfma_f32_16x16x32_bf16 v[120:123], a[60:63], a[108:111], v[120:123]\n"
"v_mfma_f32_16x16x32_bf16 v[92:95], a[52:55], a[108:111], v[92:95]\n"
"v_mfma_f32_16x16x32_bf16 v[92:95], a[48:51], a[104:107], v[92:95]\n"
"v_mfma_f32_16x16x32_bf16 v[96:99], a[48:51], a[112:115], v[96:99]\n"
"v_mfma_f32_16x16x32_bf16 v[96:99], a[52:55], a[116:119], v[96:99]\n"
"global_load_dwordx4 a[36:39], v0, s[34:35] offset:-3072 sc1\n"
"v_mfma_f32_16x16x32_bf16 v[124:127], a[60:63], a[116:119], v[124:127]\n"
"v_mfma_f32_16x16x32_bf16 v[124:127], a[56:59], a[112:115], v[124:127]\n"
"v_mfma_f32_16x16x32_bf16 v[128:131], a[56:59], a[120:123], v[128:131]\n"
"v_mfma_f32_16x16x32_bf16 v[128:131], a[60:63], a[124:127], v[128:131]\n"
"v_mfma_f32_16x16x32_bf16 v[100:103], a[52:55], a[124:127], v[100:103]\n"
"v_mfma_f32_16x16x32_bf16 v[100:103], a[48:51], a[120:123], v[100:103]\n"
"v_mfma_f32_16x16x32_bf16 v[104:107], a[48:51], a[128:131], v[104:107]\n"
"v_mfma_f32_16x16x32_bf16 v[104:107], a[52:55], a[132:135], v[104:107]\n"
"global_load_dwordx4 a[40:43], v0, s[34:35] offset:-2048 sc1\n"
"v_mfma_f32_16x16x32_bf16 v[132:135], a[60:63], a[132:135], v[132:135]\n"
"v_mfma_f32_16x16x32_bf16 v[132:135], a[56:59], a[128:131], v[132:135]\n"
"v_mfma_f32_16x16x32_bf16 v[136:139], a[56:59], a[136:139], v[136:139]\n"
"v_mfma_f32_16x16x32_bf16 v[136:139], a[60:63], a[140:143], v[136:139]\n"
"v_mfma_f32_16x16x32_bf16 v[108:111], a[52:55], a[140:143], v[108:111]\n"
"v_mfma_f32_16x16x32_bf16 v[108:111], a[48:51], a[136:139], v[108:111]\n"
"v_mfma_f32_16x16x32_bf16 v[112:115], a[48:51], a[144:147], v[112:115]\n"
"v_mfma_f32_16x16x32_bf16 v[112:115], a[52:55], a[148:151], v[112:115]\n"
"global_load_dwordx4 a[44:47], v0, s[34:35] offset:-1024 sc1\n"
"v_mfma_f32_16x16x32_bf16 v[140:143], a[60:63], a[148:151], v[140:143]\n"
"v_mfma_f32_16x16x32_bf16 v[140:143], a[56:59], a[144:147], v[140:143]\n"
"v_mfma_f32_16x16x32_bf16 a[12:15], a[56:59], a[152:155], a[12:15]\n"
"v_mfma_f32_16x16x32_bf16 a[12:15], a[60:63], a[156:159], a[12:15]\n"
"v_mfma_f32_16x16x32_bf16 a[8:11], a[52:55], a[156:159], a[8:11]\n"
"v_mfma_f32_16x16x32_bf16 a[8:11], a[48:51], a[152:155], a[8:11]\n"
"s_waitcnt vmcnt(13)\n"
"v_mfma_f32_16x16x32_bf16 v[144:147], a[64:67], a[96:99], v[144:147]\n"
"v_mfma_f32_16x16x32_bf16 v[144:147], a[68:71], a[100:103], v[144:147]\n"
"global_load_dwordx4 a[48:51], v0, s[34:35] offset:0 sc1\n"
"v_mfma_f32_16x16x32_bf16 v[172:175], a[76:79], a[100:103], v[172:175]\n"
"v_mfma_f32_16x16x32_bf16 v[172:175], a[72:75], a[96:99], v[172:175]\n"
"v_mfma_f32_16x16x32_bf16 v[176:179], a[72:75], a[104:107], v[176:179]\n"
"v_mfma_f32_16x16x32_bf16 v[176:179], a[76:79], a[108:111], v[176:179]\n"
"v_mfma_f32_16x16x32_bf16 v[148:151], a[68:71], a[108:111], v[148:151]\n"
"v_mfma_f32_16x16x32_bf16 v[148:151], a[64:67], a[104:107], v[148:151]\n"
"v_mfma_f32_16x16x32_bf16 v[152:155], a[64:67], a[112:115], v[152:155]\n"
"v_lshlrev_b32 v20, 16, v22\n"
"v_mfma_f32_16x16x32_bf16 v[152:155], a[68:71], a[116:119], v[152:155]\n"
"global_load_dwordx4 a[52:55], v0, s[34:35] offset:1024 sc1\n"
"v_and_b32 v21, s63, v22\n"
"v_mfma_f32_16x16x32_bf16 v[180:183], a[76:79], a[116:119], v[180:183]\n"
"v_lshlrev_b32 v22, 16, v23\n"
"v_mfma_f32_16x16x32_bf16 v[180:183], a[72:75], a[112:115], v[180:183]\n"
"v_and_b32 v23, s63, v23\n"
"v_mfma_f32_16x16x32_bf16 v[184:187], a[72:75], a[120:123], v[184:187]\n"
"v_add_f32 v60, v60, v20\n"
"v_mfma_f32_16x16x32_bf16 v[184:187], a[76:79], a[124:127], v[184:187]\n"
"v_add_f32 v61, v61, v21\n"
"v_add_f32 v62, v62, v22\n"
"v_mfma_f32_16x16x32_bf16 v[156:159], a[68:71], a[124:127], v[156:159]\n"
"v_add_f32 v63, v63, v23\n"
"v_mfma_f32_16x16x32_bf16 v[156:159], a[64:67], a[120:123], v[156:159]\n"
"v_add_f32 v64, v64, v20\n"
"v_mfma_f32_16x16x32_bf16 v[160:163], a[64:67], a[128:131], v[160:163]\n"
"v_add_f32 v65, v65, v21\n"
"v_mfma_f32_16x16x32_bf16 v[160:163], a[68:71], a[132:135], v[160:163]\n"
"global_load_dwordx4 a[56:59], v0, s[34:35] offset:2048 sc1\n"
"v_add_f32 v66, v66, v22\n"
"v_mfma_f32_16x16x32_bf16 v[188:191], a[76:79], a[132:135], v[188:191]\n"
"v_add_f32 v67, v67, v23\n"
"v_mfma_f32_16x16x32_bf16 v[188:191], a[72:75], a[128:131], v[188:191]\n"
"v_add_f32 v68, v68, v20\n"
"v_mfma_f32_16x16x32_bf16 v[192:195], a[72:75], a[136:139], v[192:195]\n"
"v_add_f32 v69, v69, v21\n"
"v_mfma_f32_16x16x32_bf16 v[192:195], a[76:79], a[140:143], v[192:195]\n"
"v_add_f32 v70, v70, v22\n"
"v_mfma_f32_16x16x32_bf16 v[164:167], a[68:71], a[140:143], v[164:167]\n"
"v_add_f32 v71, v71, v23\n"
"v_mfma_f32_16x16x32_bf16 v[164:167], a[64:67], a[136:139], v[164:167]\n"
"v_add_f32 v72, v72, v20\n"
"v_add_f32 v73, v73, v21\n"
"v_mfma_f32_16x16x32_bf16 v[168:171], a[64:67], a[144:147], v[168:171]\n"
"v_add_f32 v74, v74, v22\n"
"v_mfma_f32_16x16x32_bf16 v[168:171], a[68:71], a[148:151], v[168:171]\n"
"global_load_dwordx4 a[60:63], v0, s[34:35] offset:3072 sc1\n"
"v_add_f32 v75, v75, v23\n"
"v_mfma_f32_16x16x32_bf16 v[196:199], a[76:79], a[148:151], v[196:199]\n"
"v_add_f32 v76, v76, v20\n"
"v_mfma_f32_16x16x32_bf16 v[196:199], a[72:75], a[144:147], v[196:199]\n"
"v_add_f32 v77, v77, v21\n"
"v_mfma_f32_16x16x32_bf16 a[20:23], a[72:75], a[152:155], a[20:23]\n"
"s_add_u32 s34, s34, 8192\n"
"v_add_f32 v78, v78, v22\n"
"v_mfma_f32_16x16x32_bf16 a[20:23], a[76:79], a[156:159], a[20:23]\n"
"v_add_f32 v79, v79, v23\n"
"v_mfma_f32_16x16x32_bf16 a[16:19], a[68:71], a[156:159], a[16:19]\n"
"v_add_f32 v80, v80, v20\n"
"v_mfma_f32_16x16x32_bf16 a[16:19], a[64:67], a[152:155], a[16:19]\n"
"s_waitcnt vmcnt(9)\n"
"v_add_f32 v81, v81, v21\n"
"v_mfma_f32_16x16x32_bf16 v[200:203], a[80:83], a[96:99], v[200:203]\n"
"v_add_f32 v82, v82, v22\n"
"v_add_f32 v83, v83, v23\n"
"v_mfma_f32_16x16x32_bf16 v[200:203], a[84:87], a[100:103], v[200:203]\n"
"global_load_dwordx4 a[64:67], v0, s[34:35] offset:-4096 sc1\n"
"v_add_f32 v84, v84, v20\n"
"v_mfma_f32_16x16x32_bf16 v[228:231], a[92:95], a[100:103], v[228:231]\n"
"s_waitcnt vmcnt(9)\n"
"ds_read_b128 a[100:103], v2 offset:1024\n"
"v_add_f32 v85, v85, v21\n"
"v_mfma_f32_16x16x32_bf16 v[228:231], a[88:91], a[96:99], v[228:231]\n"
"ds_read_b128 a[96:99], v2 offset:0\n"
"v_add_f32 v86, v86, v22\n"
"v_mfma_f32_16x16x32_bf16 v[232:235], a[88:91], a[104:107], v[232:235]\n"
"v_add_f32 v87, v87, v23\n"
"v_mfma_f32_16x16x32_bf16 v[232:235], a[92:95], a[108:111], v[232:235]\n"
"v_accvgpr_read_b32 v12, a4\n"
"v_mfma_f32_16x16x32_bf16 v[204:207], a[84:87], a[108:111], v[204:207]\n"
"ds_read_b128 a[108:111], v2 offset:3072\n"
"v_accvgpr_read_b32 v13, a5\n"
"v_mfma_f32_16x16x32_bf16 v[204:207], a[80:83], a[104:107], v[204:207]\n"
"ds_read_b128 a[104:107], v2 offset:2048\n"
"v_accvgpr_read_b32 v14, a6\n"
"v_mfma_f32_16x16x32_bf16 v[208:211], a[80:83], a[112:115], v[208:211]\n"
"v_accvgpr_read_b32 v15, a7\n"
"v_mfma_f32_16x16x32_bf16 v[208:211], a[84:87], a[116:119], v[208:211]\n"
"global_load_dwordx4 a[68:71], v0, s[34:35] offset:-3072 sc1\n"
"v_add_f32 v12, v12, v20\n"
"v_mfma_f32_16x16x32_bf16 v[236:239], a[92:95], a[116:119], v[236:239]\n"
"ds_read_b128 a[116:119], v2 offset:5120\n"
"v_add_f32 v13, v13, v21\n"
"v_mfma_f32_16x16x32_bf16 v[236:239], a[88:91], a[112:115], v[236:239]\n"
"ds_read_b128 a[112:115], v2 offset:4096\n"
"v_add_f32 v14, v14, v22\n"
"v_mfma_f32_16x16x32_bf16 v[240:243], a[88:91], a[120:123], v[240:243]\n"
"v_add_f32 v15, v15, v23\n"
"v_accvgpr_write_b32 a4, v12\n"
"v_mfma_f32_16x16x32_bf16 v[240:243], a[92:95], a[124:127], v[240:243]\n"
"v_accvgpr_write_b32 a5, v13\n"
"v_mfma_f32_16x16x32_bf16 v[212:215], a[84:87], a[124:127], v[212:215]\n"
"ds_read_b128 a[124:127], v2 offset:7168\n"
"v_accvgpr_write_b32 a6, v14\n"
"v_mfma_f32_16x16x32_bf16 v[212:215], a[80:83], a[120:123], v[212:215]\n"
"ds_read_b128 a[120:123], v2 offset:6144\n"
"v_accvgpr_write_b32 a7, v15\n"
"v_mfma_f32_16x16x32_bf16 v[216:219], a[80:83], a[128:131], v[216:219]\n"
"v_mfma_f32_16x16x32_bf16 v[216:219], a[84:87], a[132:135], v[216:219]\n"
"global_load_dwordx4 a[72:75], v0, s[34:35] offset:-2048 sc1\n"
"v_lshrrev_b32 v20, 4, v0\n"
"v_mfma_f32_16x16x32_bf16 v[244:247], a[92:95], a[132:135], v[244:247]\n"
"ds_read_b128 a[132:135], v2 offset:9216\n"
"v_and_b32 v20, 0x30, v20\n"
"v_mfma_f32_16x16x32_bf16 v[244:247], a[88:91], a[128:131], v[244:247]\n"
"ds_read_b128 a[128:131], v2 offset:8192\n"
"buffer_load_dwordx2 v[22:23], v20, s[44:47], s61 offen offset:72\n"
"v_mfma_f32_16x16x32_bf16 v[248:251], a[88:91], a[136:139], v[248:251]\n"
"v_mfma_f32_16x16x32_bf16 v[248:251], a[92:95], a[140:143], v[248:251]\n"
"v_mfma_f32_16x16x32_bf16 v[220:223], a[84:87], a[140:143], v[220:223]\n"
"ds_read_b128 a[140:143], v2 offset:11264\n"
"v_mfma_f32_16x16x32_bf16 v[220:223], a[80:83], a[136:139], v[220:223]\n"
"ds_read_b128 a[136:139], v2 offset:10240\n"
"v_mfma_f32_16x16x32_bf16 v[224:227], a[80:83], a[144:147], v[224:227]\n"
"v_mfma_f32_16x16x32_bf16 v[224:227], a[84:87], a[148:151], v[224:227]\n"
"global_load_dwordx4 a[76:79], v0, s[34:35] offset:-1024 sc1\n"
"v_mfma_f32_16x16x32_bf16 v[252:255], a[92:95], a[148:151], v[252:255]\n"
"ds_read_b128 a[148:151], v2 offset:13312\n"
"v_mfma_f32_16x16x32_bf16 v[252:255], a[88:91], a[144:147], v[252:255]\n"
"ds_read_b128 a[144:147], v2 offset:12288\n"
"v_mfma_f32_16x16x32_bf16 a[28:31], a[88:91], a[152:155], a[28:31]\n"
"v_mfma_f32_16x16x32_bf16 a[28:31], a[92:95], a[156:159], a[28:31]\n"
"v_mfma_f32_16x16x32_bf16 a[24:27], a[84:87], a[156:159], a[24:27]\n"
"s_cmp_eq_u32 s62, 5\n"
"ds_read_b128 a[156:159], v2 offset:15360\n"
"v_mfma_f32_16x16x32_bf16 a[24:27], a[80:83], a[152:155], a[24:27]\n"
"ds_read_b128 a[152:155], v2 offset:14336\n"
"s_cbranch_scc1 L_last\n"
"s_waitcnt vmcnt(9) lgkmcnt(12)\n"
"v_mfma_f32_16x16x32_bf16 v[32:35], a[32:35], a[96:99], v[32:35]\n"
"s_add_u32 s48, s48, 131072\n"
"s_addc_u32 s49, s49, 0\n"
"v_mfma_f32_16x16x32_bf16 v[32:35], a[36:39], a[100:103], v[32:35]\n"
"global_load_dwordx4 a[80:83], v0, s[34:35] offset:0 sc1\n"
"L_P4:\n"
"v_mfma_f32_16x16x32_bf16 v[60:63], a[44:47], a[100:103], v[60:63]\n"
"s_barrier\n"
"v_mfma_f32_16x16x32_bf16 v[60:63], a[40:43], a[96:99], v[60:63]\n"
"v_mfma_f32_16x16x32_bf16 v[64:67], a[40:43], a[104:107], v[64:67]\n"
"v_mfma_f32_16x16x32_bf16 v[64:67], a[44:47], a[108:111], v[64:67]\n"
"v_mfma_f32_16x16x32_bf16 v[36:39], a[36:39], a[108:111], v[36:39]\n"
"v_subrev_u32 v8, s65, v8\n"
"v_mfma_f32_16x16x32_bf16 v[36:39], a[32:35], a[104:107], v[36:39]\n"
"s_waitcnt lgkmcnt(8)\n"
"v_xor_b32 v9, s79, v9\n"
"v_mfma_f32_16x16x32_bf16 v[40:43], a[32:35], a[112:115], v[40:43]\n"
"v_or_b32 v8, v8, v9\n"
"v_mfma_f32_16x16x32_bf16 v[40:43], a[36:39], a[116:119], v[40:43]\n"
"global_load_dwordx4 a[84:87], v0, s[34:35] offset:1024 sc1\n"
"v_cmp_lt_u32 vcc, 15, v8\n"
"v_mfma_f32_16x16x32_bf16 v[68:71], a[44:47], a[116:119], v[68:71]\n"
"s_cbranch_vccnz L_slow1\n"
"L_back1:\n"
"v_mfma_f32_16x16x32_bf16 v[68:71], a[40:43], a[112:115], v[68:71]\n"
"v_mfma_f32_16x16x32_bf16 v[72:75], a[40:43], a[120:123], v[72:75]\n"
"v_mfma_f32_16x16x32_bf16 v[72:75], a[44:47], a[124:127], v[72:75]\n"
"v_mfma_f32_16x16x32_bf16 v[44:47], a[36:39], a[124:127], v[44:47]\n"
"v_mfma_f32_16x16x32_bf16 v[44:47], a[32:35], a[120:123], v[44:47]\n"
"s_waitcnt lgkmcnt(4)\n"
"v_mfma_f32_16x16x32_bf16 v[48:51], a[32:35], a[128:131], v[48:51]\n"
"v_mfma_f32_16x16x32_bf16 v[48:51], a[36:39], a[132:135], v[48:51]\n"
"global_load_dwordx4 a[88:91], v0, s[34:35] offset:2048 sc1\n"
"v_mfma_f32_16x16x32_bf16 v[76:79], a[44:47], a[132:135], v[76:79]\n"
"v_mfma_f32_16x16x32_bf16 v[76:79], a[40:43], a[128:131], v[76:79]\n"
"s_add_u32 m0, s64, 16384\n"
"v_mfma_f32_16x16x32_bf16 v[80:83], a[40:43], a[136:139], v[80:83]\n"
"v_mfma_f32_16x16x32_bf16 v[80:83], a[44:47], a[140:143], v[80:83]\n"
"v_mfma_f32_16x16x32_bf16 v[52:55], a[36:39], a[140:143], v[52:55]\n"
"v_mfma_f32_16x16x32_bf16 v[52:55], a[32:35], a[136:139], v[52:55]\n"
"s_waitcnt lgkmcnt(0)\n"
"v_mfma_f32_16x16x32_bf16 v[56:59], a[32:35], a[144:147], v[56:59]\n"
"v_mfma_f32_16x16x32_bf16 v[56:59], a[36:39], a[148:151], v[56:59]\n"
"global_load_dwordx4 a[92:95], v0, s[34:35] offset:3072 sc1\n"
"v_mfma_f32_16x16x32_bf16 v[84:87], a[44:47], a[148:151], v[84:87]\n"
"v_mfma_f32_16x16x32_bf16 v[84:87], a[40:43], a[144:147], v[84:87]\n"
"v_mfma_f32_16x16x32_bf16 a[4:7], a[40:43], a[152:155], a[4:7]\n"
"s_add_u32 s34, s34, 8192\n"
"v_mfma_f32_16x16x32_bf16 a[4:7], a[44:47], a[156:159], a[4:7]\n"
"v_mfma_f32_16x16x32_bf16 a[0:3], a[36:39], a[156:159], a[0:3]\n"
"v_mfma_f32_16x16x32_bf16 a[0:3], a[32:35], a[152:155], a[0:3]\n"
"s_waitcnt vmcnt(9)\n"
"v_mfma_f32_16x16x32_bf16 v[88:91], a[48:51], a[96:99], v[88:91]\n"
"v_mfma_f32_16x16x32_bf16 v[88:91], a[52:55], a[100:103], v[88:91]\n"
"global_load_dwordx4 a[32:35], v0, s[34:35] offset:-4096 sc1\n"
"v_mfma_f32_16x16x32_bf16 v[116:119], a[60:63], a[100:103], v[116:119]\n"
"buffer_load_dwordx4 v0, s[48:51], s75 offen offset:0 sc1 lds\n"
"v_mfma_f32_16x16x32_bf16 v[116:119], a[56:59], a[96:99], v[116:119]\n"
"v_mfma_f32_16x16x32_bf16 v[120:123], a[56:59], a[104:107], v[120:123]\n"
"v_mfma_f32_16x16x32_bf16 v[120:123], a[60:63], a[108:111], v[120:123]\n"
"v_mfma_f32_16x16x32_bf16 v[92:95], a[52:55], a[108:111], v[92:95]\n"
"v_mfma_f32_16x16x32_bf16 v[92:95], a[48:51], a[104:107], v[92:95]\n"
"v_mfma_f32_16x16x32_bf16 v[96:99], a[48:51], a[112:115], v[96:99]\n"
"v_mfma_f32_16x16x32_bf16 v[96:99], a[52:55], a[116:119], v[96:99]\n"
"global_load_dwordx4 a[36:39], v0, s[34:35] offset:-3072 sc1\n"
"v_mfma_f32_16x16x32_bf16 v[124:127], a[60:63], a[116:119], v[124:127]\n"
"v_mfma_f32_16x16x32_bf16 v[124:127], a[56:59], a[112:115], v[124:127]\n"
"v_mfma_f32_16x16x32_bf16 v[128:131], a[56:59], a[120:123], v[128:131]\n"
"v_mfma_f32_16x16x32_bf16 v[128:131], a[60:63], a[124:127], v[128:131]\n"
"v_mfma_f32_16x16x32_bf16 v[100:103], a[52:55], a[124:127], v[100:103]\n"
"v_mfma_f32_16x16x32_bf16 v[100:103], a[48:51], a[120:123], v[100:103]\n"
"v_mfma_f32_16x16x32_bf16 v[104:107], a[48:51], a[128:131], v[104:107]\n"
"v_mfma_f32_16x16x32_bf16 v[104:107], a[52:55], a[132:135], v[104:107]\n"
"global_load_dwordx4 a[40:43], v0, s[34:35] offset:-2048 sc1\n"
"v_mfma_f32_16x16x32_bf16 v[132:135], a[60:63], a[132:135], v[132:135]\n"
"buffer_load_dwordx4 v0, s[48:51], s75 offen offset:1024 sc1 lds\n"
"v_mfma_f32_16x16x32_bf16 v[132:135], a[56:59], a[128:131], v[132:135]\n"
"v_mfma_f32_16x16x32_bf16 v[136:139], a[56:59], a[136:139], v[136:139]\n"
"v_mfma_f32_16x16x32_bf16 v[136:139], a[60:63], a[140:143], v[136:139]\n"
"v_mfma_f32_16x16x32_bf16 v[108:111], a[52:55], a[140:143], v[108:111]\n"
"v_mfma_f32_16x16x32_bf16 v[108:111], a[48:51], a[136:139], v[108:111]\n"
"v_mfma_f32_16x16x32_bf16 v[112:115], a[48:51], a[144:147], v[112:115]\n"
"v_mfma_f32_16x16x32_bf16 v[112:115], a[52:55], a[148:151], v[112:115]\n"
"global_load_dwordx4 a[44:47], v0, s[34:35] offset:-1024 sc1\n"
"v_mfma_f32_16x16x32_bf16 v[140:143], a[60:63], a[148:151], v[140:143]\n"
"v_mfma_f32_16x16x32_bf16 v[140:143], a[56:59], a[144:147], v[140:143]\n"
"v_mfma_f32_16x16x32_bf16 a[12:15], a[56:59], a[152:155], a[12:15]\n"
"v_mfma_f32_16x16x32_bf16 a[12:15], a[60:63], a[156:159], a[12:15]\n"
"v_mfma_f32_16x16x32_bf16 a[8:11], a[52:55], a[156:159], a[8:11]\n"
"v_mfma_f32_16x16x32_bf16 a[8:11], a[48:51], a[152:155], a[8:11]\n"
"s_waitcnt vmcnt(10)\n"
"v_mfma_f32_16x16x32_bf16 v[144:147], a[64:67], a[96:99], v[144:147]\n"
"v_mfma_f32_16x16x32_bf16 v[144:147], a[68:71], a[100:103], v[144:147]\n"
"global_load_dwordx4 a[48:51], v0, s[34:35] offset:0 sc1\n"
"v_mfma_f32_16x16x32_bf16 v[172:175], a[76:79], a[100:103], v[172:175]\n"
"v_mfma_f32_16x16x32_bf16 v[172:175], a[72:75], a[96:99], v[172:175]\n"
"buffer_load_dwordx4 v0, s[48:51], s75 offen offset:2048 sc1 lds\n"
"v_mfma_f32_16x16x32_bf16 v[176:179], a[72:75], a[104:107], v[176:179]\n"
"v_mfma_f32_16x16x32_bf16 v[176:179], a[76:79], a[108:111], v[176:179]\n"
"v_mfma_f32_16x16x32_bf16 v[148:151], a[68:71], a[108:111], v[148:151]\n"
"v_mfma_f32_16x16x32_bf16 v[148:151], a[64:67], a[104:107], v[148:151]\n"
"v_mfma_f32_16x16x32_bf16 v[152:155], a[64:67], a[112:115], v[152:155]\n"
"v_lshlrev_b32 v16, 16, v18\n"
"v_mfma_f32_16x16x32_bf16 v[152:155], a[68:71], a[116:119], v[152:155]\n"
"global_load_dwordx4 a[52:55], v0, s[34:35] offset:1024 sc1\n"
"v_and_b32 v17, s63, v18\n"
"v_mfma_f32_16x16x32_bf16 v[180:183], a[76:79], a[116:119], v[180:183]\n"
"v_lshlrev_b32 v18, 16, v19\n"
"v_mfma_f32_16x16x32_bf16 v[180:183], a[72:75], a[112:115], v[180:183]\n"
"v_and_b32 v19, s63, v19\n"
"v_mfma_f32_16x16x32_bf16 v[184:187], a[72:75], a[120:123], v[184:187]\n"
"v_add_f32 v88, v88, v16\n"
"v_mfma_f32_16x16x32_bf16 v[184:187], a[76:79], a[124:127], v[184:187]\n"
"v_add_f32 v89, v89, v17\n"
"v_add_f32 v90, v90, v18\n"
"v_mfma_f32_16x16x32_bf16 v[156:159], a[68:71], a[124:127], v[156:159]\n"
"v_add_f32 v91, v91, v19\n"
"v_mfma_f32_16x16x32_bf16 v[156:159], a[64:67], a[120:123], v[156:159]\n"
"v_add_f32 v92, v92, v16\n"
"v_mfma_f32_16x16x32_bf16 v[160:163], a[64:67], a[128:131], v[160:163]\n"
"v_add_f32 v93, v93, v17\n"
"v_mfma_f32_16x16x32_bf16 v[160:163], a[68:71], a[132:135], v[160:163]\n"
"global_load_dwordx4 a[56:59], v0, s[34:35] offset:2048 sc1\n"
"v_add_f32 v94, v94, v18\n"
"v_mfma_f32_16x16x32_bf16 v[188:191], a[76:79], a[132:135], v[188:191]\n"
"buffer_load_dwordx4 v0, s[48:51], s75 offen offset:3072 sc1 lds\n"
"v_mfma_f32_16x16x32_bf16 v[188:191], a[72:75], a[128:131], v[188:191]\n"
"v_add_f32 v95, v95, v19\n"
"v_add_f32 v96, v96, v16\n"
"v_mfma_f32_16x16x32_bf16 v[192:195], a[72:75], a[136:139], v[192:195]\n"
"v_add_f32 v97, v97, v17\n"
"v_mfma_f32_16x16x32_bf16 v[192:195], a[76:79], a[140:143], v[192:195]\n"
"v_add_f32 v98, v98, v18\n"
"v_mfma_f32_16x16x32_bf16 v[164:167], a[68:71], a[140:143], v[164:167]\n"
"v_add_f32 v99, v99, v19\n"
"v_mfma_f32_16x16x32_bf16 v[164:167], a[64:67], a[136:139], v[164:167]\n"
"v_add_f32 v100, v100, v16\n"
"v_add_f32 v101, v101, v17\n"
"v_mfma_f32_16x16x32_bf16 v[168:171], a[64:67], a[144:147], v[168:171]\n"
"v_add_f32 v102, v102, v18\n"
"v_mfma_f32_16x16x32_bf16 v[168:171], a[68:71], a[148:151], v[168:171]\n"
"global_load_dwordx4 a[60:63], v0, s[34:35] offset:3072 sc1\n"
"v_add_f32 v103, v103, v19\n"
"v_mfma_f32_16x16x32_bf16 v[196:199], a[76:79], a[148:151], v[196:199]\n"
"v_add_f32 v104, v104, v16\n"
"v_mfma_f32_16x16x32_bf16 v[196:199], a[72:75], a[144:147], v[196:199]\n"
"v_add_f32 v105, v105, v17\n"
"v_mfma_f32_16x16x32_bf16 a[20:23], a[72:75], a[152:155], a[20:23]\n"
"s_add_u32 s34, s34, 8192\n"
"v_add_f32 v106, v106, v18\n"
"v_mfma_f32_16x16x32_bf16 a[20:23], a[76:79], a[156:159], a[20:23]\n"
"v_add_f32 v107, v107, v19\n"
"v_mfma_f32_16x16x32_bf16 a[16:19], a[68:71], a[156:159], a[16:19]\n"
"v_add_f32 v108, v108, v16\n"
"v_mfma_f32_16x16x32_bf16 a[16:19], a[64:67], a[152:155], a[16:19]\n"
"s_waitcnt vmcnt(12)\n"
"v_add_f32 v109, v109, v17\n"
"v_mfma_f32_16x16x32_bf16 v[200:203], a[80:83], a[96:99], v[200:203]\n"
"v_add_f32 v110, v110, v18\n"
"v_add_f32 v111, v111, v19\n"
"v_mfma_f32_16x16x32_bf16 v[200:203], a[84:87], a[100:103], v[200:203]\n"
"global_load_dwordx4 a[64:67], v0, s[34:35] offset:-4096 sc1\n"
"v_add_f32 v112, v112, v16\n"
"v_mfma_f32_16x16x32_bf16 v[228:231], a[92:95], a[100:103], v[228:231]\n"
"ds_read_b128 a[100:103], v3 offset:1024\n"
"v_add_f32 v113, v113, v17\n"
"v_mfma_f32_16x16x32_bf16 v[228:231], a[88:91], a[96:99], v[228:231]\n"
"s_add_u32 m0, s64, 20480\n"
"ds_read_b128 a[96:99], v3 offset:0\n"
"v_add_f32 v114, v114, v18\n"
"v_mfma_f32_16x16x32_bf16 v[232:235], a[88:91], a[104:107], v[232:235]\n"
"v_add_f32 v115, v115, v19\n"
"v_mfma_f32_16x16x32_bf16 v[232:235], a[92:95], a[108:111], v[232:235]\n"
"v_accvgpr_read_b32 v12, a8\n"
"v_mfma_f32_16x16x32_bf16 v[204:207], a[84:87], a[108:111], v[204:207]\n"
"ds_read_b128 a[108:111], v3 offset:3072\n"
"v_accvgpr_read_b32 v13, a9\n"
"v_mfma_f32_16x16x32_bf16 v[204:207], a[80:83], a[104:107], v[204:207]\n"
"ds_read_b128 a[104:107], v3 offset:2048\n"
"v_accvgpr_read_b32 v14, a10\n"
"v_mfma_f32_16x16x32_bf16 v[208:211], a[80:83], a[112:115], v[208:211]\n"
"v_accvgpr_read_b32 v15, a11\n"
"v_mfma_f32_16x16x32_bf16 v[208:211], a[84:87], a[116:119], v[208:211]\n"
"global_load_dwordx4 a[68:71], v0, s[34:35] offset:-3072 sc1\n"
"v_add_f32 v12, v12, v16\n"
"v_mfma_f32_16x16x32_bf16 v[236:239], a[92:95], a[116:119], v[236:239]\n"
"ds_read_b128 a[116:119], v3 offset:5120\n"
"v_add_f32 v13, v13, v17\n"
"v_mfma_f32_16x16x32_bf16 v[236:239], a[88:91], a[112:115], v[236:239]\n"
"ds_read_b128 a[112:115], v3 offset:4096\n"
"v_add_f32 v14, v14, v18\n"
"v_mfma_f32_16x16x32_bf16 v[240:243], a[88:91], a[120:123], v[240:243]\n"
"v_add_f32 v15, v15, v19\n"
"v_accvgpr_write_b32 a8, v12\n"
"v_mfma_f32_16x16x32_bf16 v[240:243], a[92:95], a[124:127], v[240:243]\n"
"v_accvgpr_write_b32 a9, v13\n"
"v_mfma_f32_16x16x32_bf16 v[212:215], a[84:87], a[124:127], v[212:215]\n"
"ds_read_b128 a[124:127], v3 offset:7168\n"
"v_accvgpr_write_b32 a10, v14\n"
"v_mfma_f32_16x16x32_bf16 v[212:215], a[80:83], a[120:123], v[212:215]\n"
"ds_read_b128 a[120:123], v3 offset:6144\n"
"v_accvgpr_write_b32 a11, v15\n"
"v_mfma_f32_16x16x32_bf16 v[216:219], a[80:83], a[128:131], v[216:219]\n"
"v_mfma_f32_16x16x32_bf16 v[216:219], a[84:87], a[132:135], v[216:219]\n"
"global_load_dwordx4 a[72:75], v0, s[34:35] offset:-2048 sc1\n"
"v_mfma_f32_16x16x32_bf16 v[244:247], a[92:95], a[132:135], v[244:247]\n"
"ds_read_b128 a[132:135], v3 offset:9216\n"
"v_mfma_f32_16x16x32_bf16 v[244:247], a[88:91], a[128:131], v[244:247]\n"
"ds_read_b128 a[128:131], v3 offset:8192\n"
"v_mfma_f32_16x16x32_bf16 v[248:251], a[88:91], a[136:139], v[248:251]\n"
"buffer_load_dwordx4 v1, s[48:51], s75 offen offset:0 sc1 lds\n"
"v_mfma_f32_16x16x32_bf16 v[248:251], a[92:95], a[140:143], v[248:251]\n"
"v_mfma_f32_16x16x32_bf16 v[220:223], a[84:87], a[140:143], v[220:223]\n"
"ds_read_b128 a[140:143], v3 offset:11264\n"
"v_mfma_f32_16x16x32_bf16 v[220:223], a[80:83], a[136:139], v[220:223]\n"
"ds_read_b128 a[136:139], v3 offset:10240\n"
"v_mfma_f32_16x16x32_bf16 v[224:227], a[80:83], a[144:147], v[224:227]\n"
"v_mfma_f32_16x16x32_bf16 v[224:227], a[84:87], a[148:151], v[224:227]\n"
"global_load_dwordx4 a[76:79], v0, s[34:35] offset:-1024 sc1\n"
"v_mfma_f32_16x16x32_bf16 v[252:255], a[92:95], a[148:151], v[252:255]\n"
"ds_read_b128 a[148:151], v3 offset:13312\n"
"v_mfma_f32_16x16x32_bf16 v[252:255], a[88:91], a[144:147], v[252:255]\n"
"ds_read_b128 a[144:147], v3 offset:12288\n"
"v_mfma_f32_16x16x32_bf16 a[28:31], a[88:91], a[152:155], a[28:31]\n"
"v_mfma_f32_16x16x32_bf16 a[28:31], a[92:95], a[156:159], a[28:31]\n"
"v_mfma_f32_16x16x32_bf16 a[24:27], a[84:87], a[156:159], a[24:27]\n"
"ds_read_b128 a[156:159], v3 offset:15360\n"
"v_mfma_f32_16x16x32_bf16 a[24:27], a[80:83], a[152:155], a[24:27]\n"
"s_waitcnt vmcnt(11) lgkmcnt(11)\n"
"ds_read_b128 a[152:155], v3 offset:14336\n"
"v_mfma_f32_16x16x32_bf16 v[32:35], a[32:35], a[96:99], v[32:35]\n"
"v_mfma_f32_16x16x32_bf16 v[32:35], a[36:39], a[100:103], v[32:35]\n"
"global_load_dwordx4 a[80:83], v0, s[34:35] offset:0 sc1\n"
"v_mfma_f32_16x16x32_bf16 v[60:63], a[44:47], a[100:103], v[60:63]\n"
"v_mfma_f32_16x16x32_bf16 v[60:63], a[40:43], a[96:99], v[60:63]\n"
"buffer_load_dwordx4 v1, s[48:51], s75 offen offset:1024 sc1 lds\n"
"v_mfma_f32_16x16x32_bf16 v[64:67], a[40:43], a[104:107], v[64:67]\n"
"v_mfma_f32_16x16x32_bf16 v[64:67], a[44:47], a[108:111], v[64:67]\n"
"v_mfma_f32_16x16x32_bf16 v[36:39], a[36:39], a[108:111], v[36:39]\n"
"v_mfma_f32_16x16x32_bf16 v[36:39], a[32:35], a[104:107], v[36:39]\n"
"s_waitcnt lgkmcnt(8)\n"
"v_mfma_f32_16x16x32_bf16 v[40:43], a[32:35], a[112:115], v[40:43]\n"
"v_mfma_f32_16x16x32_bf16 v[40:43], a[36:39], a[116:119], v[40:43]\n"
"global_load_dwordx4 a[84:87], v0, s[34:35] offset:1024 sc1\n"
"v_mfma_f32_16x16x32_bf16 v[68:71], a[44:47], a[116:119], v[68:71]\n"
"v_mfma_f32_16x16x32_bf16 v[68:71], a[40:43], a[112:115], v[68:71]\n"
"v_mfma_f32_16x16x32_bf16 v[72:75], a[40:43], a[120:123], v[72:75]\n"
"v_mfma_f32_16x16x32_bf16 v[72:75], a[44:47], a[124:127], v[72:75]\n"
"v_mfma_f32_16x16x32_bf16 v[44:47], a[36:39], a[124:127], v[44:47]\n"
"v_mfma_f32_16x16x32_bf16 v[44:47], a[32:35], a[120:123], v[44:47]\n"
"s_waitcnt lgkmcnt(4)\n"
"v_mfma_f32_16x16x32_bf16 v[48:51], a[32:35], a[128:131], v[48:51]\n"
"v_mfma_f32_16x16x32_bf16 v[48:51], a[36:39], a[132:135], v[48:51]\n"
"global_load_dwordx4 a[88:91], v0, s[34:35] offset:2048 sc1\n"
"v_mfma_f32_16x16x32_bf16 v[76:79], a[44:47], a[132:135], v[76:79]\n"
"v_mfma_f32_16x16x32_bf16 v[76:79], a[40:43], a[128:131], v[76:79]\n"
"buffer_load_dwordx4 v1, s[48:51], s75 offen offset:2048 sc1 lds\n"
"v_mfma_f32_16x16x32_bf16 v[80:83], a[40:43], a[136:139], v[80:83]\n"
"v_mfma_f32_16x16x32_bf16 v[80:83], a[44:47], a[140:143], v[80:83]\n"
"v_mfma_f32_16x16x32_bf16 v[52:55], a[36:39], a[140:143], v[52:55]\n"
"v_mfma_f32_16x16x32_bf16 v[52:55], a[32:35], a[136:139], v[52:55]\n"
"s_waitcnt lgkmcnt(0)\n"
"v_mfma_f32_16x16x32_bf16 v[56:59], a[32:35], a[144:147], v[56:59]\n"
"v_mfma_f32_16x16x32_bf16 v[56:59], a[36:39], a[148:151], v[56:59]\n"
"global_load_dwordx4 a[92:95], v0, s[34:35] offset:3072 sc1\n"
"v_mfma_f32_16x16x32_bf16 v[84:87], a[44:47], a[148:151], v[84:87]\n"
"v_mfma_f32_16x16x32_bf16 v[84:87], a[40:43], a[144:147], v[84:87]\n"
"v_mfma_f32_16x16x32_bf16 a[4:7], a[40:43], a[152:155], a[4:7]\n"
"s_add_u32 s34, s34, 8192\n"
"v_mfma_f32_16x16x32_bf16 a[4:7], a[44:47], a[156:159], a[4:7]\n"
"v_mfma_f32_16x16x32_bf16 a[0:3], a[36:39], a[156:159], a[0:3]\n"
"v_mfma_f32_16x16x32_bf16 a[0:3], a[32:35], a[152:155], a[0:3]\n"
"s_waitcnt vmcnt(11)\n"
"v_mfma_f32_16x16x32_bf16 v[88:91], a[48:51], a[96:99], v[88:91]\n"
"v_mfma_f32_16x16x32_bf16 v[88:91], a[52:55], a[100:103], v[88:91]\n"
"global_load_dwordx4 a[32:35], v0, s[34:35] offset:-4096 sc1\n"
"v_mfma_f32_16x16x32_bf16 v[116:119], a[60:63], a[100:103], v[116:119]\n"
"v_mfma_f32_16x16x32_bf16 v[116:119], a[56:59], a[96:99], v[116:119]\n"
"buffer_load_dwordx4 v1, s[48:51], s75 offen offset:3072 sc1 lds\n"
"v_mfma_f32_16x16x32_bf16 v[120:123], a[56:59], a[104:107], v[120:123]\n"
"v_mfma_f32_16x16x32_bf16 v[120:123], a[60:63], a[108:111], v[120:123]\n"
"v_mfma_f32_16x16x32_bf16 v[92:95], a[52:55], a[108:111], v[92:95]\n"
"v_mfma_f32_16x16x32_bf16 v[92:95], a[48:51], a[104:107], v[92:95]\n"
"v_mfma_f32_16x16x32_bf16 v[96:99], a[48:51], a[112:115], v[96:99]\n"
"v_mfma_f32_16x16x32_bf16 v[96:99], a[52:55], a[116:119], v[96:99]\n"
"global_load_dwordx4 a[36:39], v0, s[34:35] offset:-3072 sc1\n"
"v_mfma_f32_16x16x32_bf16 v[124:127], a[60:63], a[116:119], v[124:127]\n"
"v_mfma_f32_16x16x32_bf16 v[124:127], a[56:59], a[112:115], v[124:127]\n"
"v_mfma_f32_16x16x32_bf16 v[128:131], a[56:59], a[120:123], v[128:131]\n"
"v_mfma_f32_16x16x32_bf16 v[128:131], a[60:63], a[124:127], v[128:131]\n"
"v_mfma_f32_16x16x32_bf16 v[100:103], a[52:55], a[124:127], v[100:103]\n"
"v_mfma_f32_16x16x32_bf16 v[100:103], a[48:51], a[120:123], v[100:103]\n"
"v_mfma_f32_16x16x32_bf16 v[104:107], a[48:51], a[128:131], v[104:107]\n"
"v_mfma_f32_16x16x32_bf16 v[104:107], a[52:55], a[132:135], v[104:107]\n"
"global_load_dwordx4 a[40:43], v0, s[34:35] offset:-2048 sc1\n"
"v_mfma_f32_16x16x32_bf16 v[132:135], a[60:63], a[132:135], v[132:135]\n"
"v_mfma_f32_16x16x32_bf16 v[132:135], a[56:59], a[128:131], v[132:135]\n"
"s_add_u32 m0, s64, 24576\n"
"v_mfma_f32_16x16x32_bf16 v[136:139], a[56:59], a[136:139], v[136:139]\n"
"v_mfma_f32_16x16x32_bf16 v[136:139], a[60:63], a[140:143], v[136:139]\n"
"v_mfma_f32_16x16x32_bf16 v[108:111], a[52:55], a[140:143], v[108:111]\n"
"v_mfma_f32_16x16x32_bf16 v[108:111], a[48:51], a[136:139], v[108:111]\n"
"v_mfma_f32_16x16x32_bf16 v[112:115], a[48:51], a[144:147], v[112:115]\n"
"v_mfma_f32_16x16x32_bf16 v[112:115], a[52:55], a[148:151], v[112:115]\n"
"global_load_dwordx4 a[44:47], v0, s[34:35] offset:-1024 sc1\n"
"v_mfma_f32_16x16x32_bf16 v[140:143], a[60:63], a[148:151], v[140:143]\n"
"v_mfma_f32_16x16x32_bf16 v[140:143], a[56:59], a[144:147], v[140:143]\n"
"v_mfma_f32_16x16x32_bf16 a[12:15], a[56:59], a[152:155], a[12:15]\n"
"v_mfma_f32_16x16x32_bf16 a[12:15], a[60:63], a[156:159], a[12:15]\n"
"v_mfma_f32_16x16x32_bf16 a[8:11], a[52:55], a[156:159], a[8:11]\n"
"v_mfma_f32_16x16x32_bf16 a[8:11], a[48:51], a[152:155], a[8:11]\n"
"s_waitcnt vmcnt(11)\n"
"v_mfma_f32_16x16x32_bf16 v[144:147], a[64:67], a[96:99], v[144:147]\n"
"v_mfma_f32_16x16x32_bf16 v[144:147], a[68:71], a[100:103], v[144:147]\n"
"global_load_dwordx4 a[48:51], v0, s[34:35] offset:0 sc1\n"
"v_mfma_f32_16x16x32_bf16 v[172:175], a[76:79], a[100:103], v[172:175]\n"
"v_mfma_f32_16x16x32_bf16 v[172:175], a[72:75], a[96:99], v[172:175]\n"
"buffer_load_dwordx4 v0, s[48:51], s69 offen offset:0 sc1 lds\n"
"v_mfma_f32_16x16x32_bf16 v[176:179], a[72:75], a[104:107], v[176:179]\n"
"v_mfma_f32_16x16x32_bf16 v[176:179], a[76:79], a[108:111], v[176:179]\n"
"v_mfma_f32_16x16x32_bf16 v[148:151], a[68:71], a[108:111], v[148:151]\n"
"v_mfma_f32_16x16x32_bf16 v[148:151], a[64:67], a[104:107], v[148:151]\n"
"v_mfma_f32_16x16x32_bf16 v[152:155], a[64:67], a[112:115], v[152:155]\n"
"v_lshlrev_b32 v20, 16, v22\n"
"v_mfma_f32_16x16x32_bf16 v[152:155], a[68:71], a[116:119], v[152:155]\n"
"global_load_dwordx4 a[52:55], v0, s[34:35] offset:1024 sc1\n"
"v_and_b32 v21, s63, v22\n"
"v_mfma_f32_16x16x32_bf16 v[180:183], a[76:79], a[116:119], v[180:183]\n"
"v_lshlrev_b32 v22, 16, v23\n"
"v_mfma_f32_16x16x32_bf16 v[180:183], a[72:75], a[112:115], v[180:183]\n"
"v_and_b32 v23, s63, v23\n"
"v_mfma_f32_16x16x32_bf16 v[184:187], a[72:75], a[120:123], v[184:187]\n"
"v_add_f32 v116, v116, v20\n"
"v_mfma_f32_16x16x32_bf16 v[184:187], a[76:79], a[124:127], v[184:187]\n"
"v_add_f32 v117, v117, v21\n"
"v_add_f32 v118, v118, v22\n"
"v_mfma_f32_16x16x32_bf16 v[156:159], a[68:71], a[124:127], v[156:159]\n"
"v_add_f32 v119, v119, v23\n"
"v_mfma_f32_16x16x32_bf16 v[156:159], a[64:67], a[120:123], v[156:159]\n"
"v_add_f32 v120, v120, v20\n"
"v_mfma_f32_16x16x32_bf16 v[160:163], a[64:67], a[128:131], v[160:163]\n"
"v_add_f32 v121, v121, v21\n"
"v_mfma_f32_16x16x32_bf16 v[160:163], a[68:71], a[132:135], v[160:163]\n"
"global_load_dwordx4 a[56:59], v0, s[34:35] offset:2048 sc1\n"
"v_add_f32 v122, v122, v22\n"
"v_mfma_f32_16x16x32_bf16 v[188:191], a[76:79], a[132:135], v[188:191]\n"
"v_add_f32 v123, v123, v23\n"
"v_add_f32 v124, v124, v20\n"
"v_mfma_f32_16x16x32_bf16 v[188:191], a[72:75], a[128:131], v[188:191]\n"
"buffer_load_dwordx4 v0, s[48:51], s69 offen offset:1024 sc1 lds\n"
"v_mfma_f32_16x16x32_bf16 v[192:195], a[72:75], a[136:139], v[192:195]\n"
"v_add_f32 v125, v125, v21\n"
"v_mfma_f32_16x16x32_bf16 v[192:195], a[76:79], a[140:143], v[192:195]\n"
"v_add_f32 v126, v126, v22\n"
"v_mfma_f32_16x16x32_bf16 v[164:167], a[68:71], a[140:143], v[164:167]\n"
"v_add_f32 v127, v127, v23\n"
"v_mfma_f32_16x16x32_bf16 v[164:167], a[64:67], a[136:139], v[164:167]\n"
"v_add_f32 v128, v128, v20\n"
"v_add_f32 v129, v129, v21\n"
"v_mfma_f32_16x16x32_bf16 v[168:171], a[64:67], a[144:147], v[168:171]\n"
"v_add_f32 v130, v130, v22\n"
"v_mfma_f32_16x16x32_bf16 v[168:171], a[68:71], a[148:151], v[168:171]\n"
"global_load_dwordx4 a[60:63], v0, s[34:35] offset:3072 sc1\n"
"v_add_f32 v131, v131, v23\n"
"v_mfma_f32_16x16x32_bf16 v[196:199], a[76:79], a[148:151], v[196:199]\n"
"v_add_f32 v132, v132, v20\n"
"v_mfma_f32_16x16x32_bf16 v[196:199], a[72:75], a[144:147], v[196:199]\n"
"v_add_f32 v133, v133, v21\n"
"v_mfma_f32_16x16x32_bf16 a[20:23], a[72:75], a[152:155], a[20:23]\n"
"s_add_u32 s34, s34, 8192\n"
"v_add_f32 v134, v134, v22\n"
"v_mfma_f32_16x16x32_bf16 a[20:23], a[76:79], a[156:159], a[20:23]\n"
"v_add_f32 v135, v135, v23\n"
"v_mfma_f32_16x16x32_bf16 a[16:19], a[68:71], a[156:159], a[16:19]\n"
"v_add_f32 v136, v136, v20\n"
"v_mfma_f32_16x16x32_bf16 a[16:19], a[64:67], a[152:155], a[16:19]\n"
"s_waitcnt vmcnt(11)\n"
"v_add_f32 v137, v137, v21\n"
"v_mfma_f32_16x16x32_bf16 v[200:203], a[80:83], a[96:99], v[200:203]\n"
"v_add_f32 v138, v138, v22\n"
"v_add_f32 v139, v139, v23\n"
"v_mfma_f32_16x16x32_bf16 v[200:203], a[84:87], a[100:103], v[200:203]\n"
"global_load_dwordx4 a[64:67], v0, s[34:35] offset:-4096 sc1\n"
"v_add_f32 v140, v140, v20\n"
"v_mfma_f32_16x16x32_bf16 v[228:231], a[92:95], a[100:103], v[228:231]\n"
"ds_read_b128 a[100:103], v4 offset:1024\n"
"v_add_f32 v141, v141, v21\n"
"v_mfma_f32_16x16x32_bf16 v[228:231], a[88:91], a[96:99], v[228:231]\n"
"ds_read_b128 a[96:99], v4 offset:0\n"
"v_add_f32 v142, v142, v22\n"
"v_mfma_f32_16x16x32_bf16 v[232:235], a[88:91], a[104:107], v[232:235]\n"
"buffer_load_dwordx4 v0, s[48:51], s69 offen offset:2048 sc1 lds\n"
"v_mfma_f32_16x16x32_bf16 v[232:235], a[92:95], a[108:111], v[232:235]\n"
"v_add_f32 v143, v143, v23\n"
"v_accvgpr_read_b32 v12, a12\n"
"v_mfma_f32_16x16x32_bf16 v[204:207], a[84:87], a[108:111], v[204:207]\n"
"ds_read_b128 a[108:111], v4 offset:3072\n"
"v_accvgpr_read_b32 v13, a13\n"
"v_mfma_f32_16x16x32_bf16 v[204:207], a[80:83], a[104:107], v[204:207]\n"
"ds_read_b128 a[104:107], v4 offset:2048\n"
"v_accvgpr_read_b32 v14, a14\n"
"v_mfma_f32_16x16x32_bf16 v[208:211], a[80:83], a[112:115], v[208:211]\n"
"v_accvgpr_read_b32 v15, a15\n"
"v_mfma_f32_16x16x32_bf16 v[208:211], a[84:87], a[116:119], v[208:211]\n"
"global_load_dwordx4 a[68:71], v0, s[34:35] offset:-3072 sc1\n"
"v_add_f32 v12, v12, v20\n"
"v_mfma_f32_16x16x32_bf16 v[236:239], a[92:95], a[116:119], v[236:239]\n"
"ds_read_b128 a[116:119], v4 offset:5120\n"
"v_add_f32 v13, v13, v21\n"
"v_mfma_f32_16x16x32_bf16 v[236:239], a[88:91], a[112:115], v[236:239]\n"
"ds_read_b128 a[112:115], v4 offset:4096\n"
"v_add_f32 v14, v14, v22\n"
"v_mfma_f32_16x16x32_bf16 v[240:243], a[88:91], a[120:123], v[240:243]\n"
"v_add_f32 v15, v15, v23\n"
"v_accvgpr_write_b32 a12, v12\n"
"v_mfma_f32_16x16x32_bf16 v[240:243], a[92:95], a[124:127], v[240:243]\n"
"v_accvgpr_write_b32 a13, v13\n"
"v_mfma_f32_16x16x32_bf16 v[212:215], a[84:87], a[124:127], v[212:215]\n"
"ds_read_b128 a[124:127], v4 offset:7168\n"
"v_accvgpr_write_b32 a14, v14\n"
"v_mfma_f32_16x16x32_bf16 v[212:215], a[80:83], a[120:123], v[212:215]\n"
"ds_read_b128 a[120:123], v4 offset:6144\n"
"v_accvgpr_write_b32 a15, v15\n"
"v_mfma_f32_16x16x32_bf16 v[216:219], a[80:83], a[128:131], v[216:219]\n"
"v_mfma_f32_16x16x32_bf16 v[216:219], a[84:87], a[132:135], v[216:219]\n"
"global_load_dwordx4 a[72:75], v0, s[34:35] offset:-2048 sc1\n"
"v_mfma_f32_16x16x32_bf16 v[244:247], a[92:95], a[132:135], v[244:247]\n"
"ds_read_b128 a[132:135], v4 offset:9216\n"
"v_mfma_f32_16x16x32_bf16 v[244:247], a[88:91], a[128:131], v[244:247]\n"
"ds_read_b128 a[128:131], v4 offset:8192\n"
"v_mfma_f32_16x16x32_bf16 v[248:251], a[88:91], a[136:139], v[248:251]\n"
"buffer_load_dwordx4 v0, s[48:51], s69 offen offset:3072 sc1 lds\n"
"v_mfma_f32_16x16x32_bf16 v[248:251], a[92:95], a[140:143], v[248:251]\n"
"v_mfma_f32_16x16x32_bf16 v[220:223], a[84:87], a[140:143], v[220:223]\n"
"ds_read_b128 a[140:143], v4 offset:11264\n"
"v_mfma_f32_16x16x32_bf16 v[220:223], a[80:83], a[136:139], v[220:223]\n"
"ds_read_b128 a[136:139], v4 offset:10240\n"
"v_mfma_f32_16x16x32_bf16 v[224:227], a[80:83], a[144:147], v[224:227]\n"
"v_mfma_f32_16x16x32_bf16 v[224:227], a[84:87], a[148:151], v[224:227]\n"
"global_load_dwordx4 a[76:79], v0, s[34:35] offset:-1024 sc1\n"
"v_mfma_f32_16x16x32_bf16 v[252:255], a[92:95], a[148:151], v[252:255]\n"
"ds_read_b128 a[148:151], v4 offset:13312\n"
"v_mfma_f32_16x16x32_bf16 v[252:255], a[88:91], a[144:147], v[252:255]\n"
"ds_read_b128 a[144:147], v4 offset:12288\n"
"v_mfma_f32_16x16x32_bf16 a[28:31], a[88:91], a[152:155], a[28:31]\n"
"v_mfma_f32_16x16x32_bf16 a[28:31], a[92:95], a[156:159], a[28:31]\n"
"v_mfma_f32_16x16x32_bf16 a[24:27], a[84:87], a[156:159], a[24:27]\n"
"ds_read_b128 a[156:159], v4 offset:15360\n"
"v_mfma_f32_16x16x32_bf16 a[24:27], a[80:83], a[152:155], a[24:27]\n"
"s_waitcnt vmcnt(12) lgkmcnt(11)\n"
"ds_read_b128 a[152:155], v4 offset:14336\n"
"v_mfma_f32_16x16x32_bf16 v[32:35], a[32:35], a[96:99], v[32:35]\n"
"v_mfma_f32_16x16x32_bf16 v[32:35], a[36:39], a[100:103], v[32:35]\n"
"global_load_dwordx4 a[80:83], v0, s[34:35] offset:0 sc1\n"
"v_lshrrev_b32 v16, 4, v0\n"
"v_mfma_f32_16x16x32_bf16 v[60:63], a[44:47], a[100:103], v[60:63]\n"
"v_and_b32 v16, 0x30, v16\n"
"v_mfma_f32_16x16x32_bf16 v[60:63], a[40:43], a[96:99], v[60:63]\n"
"s_add_u32 m0, s64, 28672\n"
"buffer_load_dwordx2 v[18:19], v16, s[44:47], s61 offen offset:128\n"
"v_mfma_f32_16x16x32_bf16 v[64:67], a[40:43], a[104:107], v[64:67]\n"
"v_mfma_f32_16x16x32_bf16 v[64:67], a[44:47], a[108:111], v[64:67]\n"
"v_mfma_f32_16x16x32_bf16 v[36:39], a[36:39], a[108:111], v[36:39]\n"
"buffer_load_dwordx4 v[12:15], v0, s[56:59], %25 offen sc1\n"
"v_mfma_f32_16x16x32_bf16 v[36:39], a[32:35], a[104:107], v[36:39]\n"
"s_waitcnt lgkmcnt(8)\n"
"v_mfma_f32_16x16x32_bf16 v[40:43], a[32:35], a[112:115], v[40:43]\n"
"v_mfma_f32_16x16x32_bf16 v[40:43], a[36:39], a[116:119], v[40:43]\n"
"global_load_dwordx4 a[84:87], v0, s[34:35] offset:1024 sc1\n"
"v_lshrrev_b32 v20, 4, v0\n"
"v_mfma_f32_16x16x32_bf16 v[68:71], a[44:47], a[116:119], v[68:71]\n"
"v_and_b32 v20, 0x30, v20\n"
"v_mfma_f32_16x16x32_bf16 v[68:71], a[40:43], a[112:115], v[68:71]\n"
"buffer_load_dwordx2 v[22:23], v20, s[44:47], s61 offen offset:136\n"
"v_mfma_f32_16x16x32_bf16 v[72:75], a[40:43], a[120:123], v[72:75]\n"
"v_mfma_f32_16x16x32_bf16 v[72:75], a[44:47], a[124:127], v[72:75]\n"
"v_mfma_f32_16x16x32_bf16 v[44:47], a[36:39], a[124:127], v[44:47]\n"
"v_mfma_f32_16x16x32_bf16 v[44:47], a[32:35], a[120:123], v[44:47]\n"
"s_waitcnt lgkmcnt(4)\n"
"v_mfma_f32_16x16x32_bf16 v[48:51], a[32:35], a[128:131], v[48:51]\n"
"v_mfma_f32_16x16x32_bf16 v[48:51], a[36:39], a[132:135], v[48:51]\n"
"global_load_dwordx4 a[88:91], v0, s[34:35] offset:2048 sc1\n"
"v_lshrrev_b32 v24, 4, v0\n"
"v_mfma_f32_16x16x32_bf16 v[76:79], a[44:47], a[132:135], v[76:79]\n"
"v_and_b32 v24, 0x30, v24\n"
"v_mfma_f32_16x16x32_bf16 v[76:79], a[40:43], a[128:131], v[76:79]\n"
"buffer_load_dwordx4 v1, s[48:51], s69 offen offset:0 sc1 lds\n"
"buffer_load_dwordx2 v[26:27], v24, s[44:47], s61 offen offset:192\n"
"v_mfma_f32_16x16x32_bf16 v[80:83], a[40:43], a[136:139], v[80:83]\n"
"v_mfma_f32_16x16x32_bf16 v[80:83], a[44:47], a[140:143], v[80:83]\n"
"v_mfma_f32_16x16x32_bf16 v[52:55], a[36:39], a[140:143], v[52:55]\n"
"v_mfma_f32_16x16x32_bf16 v[52:55], a[32:35], a[136:139], v[52:55]\n"
"s_waitcnt lgkmcnt(0)\n"
"v_mfma_f32_16x16x32_bf16 v[56:59], a[32:35], a[144:147], v[56:59]\n"
"v_mfma_f32_16x16x32_bf16 v[56:59], a[36:39], a[148:151], v[56:59]\n"
"global_load_dwordx4 a[92:95], v0, s[34:35] offset:3072 sc1\n"
"v_lshrrev_b32 v28, 4, v0\n"
"v_mfma_f32_16x16x32_bf16 v[84:87], a[44:47], a[148:151], v[84:87]\n"
"v_and_b32 v28, 0x30, v28\n"
"v_mfma_f32_16x16x32_bf16 v[84:87], a[40:43], a[144:147], v[84:87]\n"
"buffer_load_dwordx2 v[30:31], v28, s[44:47], s61 offen offset:200\n"
"v_mfma_f32_16x16x32_bf16 a[4:7], a[40:43], a[152:155], a[4:7]\n"
"s_add_u32 s34, s34, 8192\n"
"v_mfma_f32_16x16x32_bf16 a[4:7], a[44:47], a[156:159], a[4:7]\n"
"v_mfma_f32_16x16x32_bf16 a[0:3], a[36:39], a[156:159], a[0:3]\n"
"v_mfma_f32_16x16x32_bf16 a[0:3], a[32:35], a[152:155], a[0:3]\n"
"s_waitcnt vmcnt(16)\n"
"v_mfma_f32_16x16x32_bf16 v[88:91], a[48:51], a[96:99], v[88:91]\n"
"v_mfma_f32_16x16x32_bf16 v[88:91], a[52:55], a[100:103], v[88:91]\n"
"global_load_dwordx4 a[32:35], v0, s[34:35] offset:-4096 sc1\n"
"v_mfma_f32_16x16x32_bf16 v[116:119], a[60:63], a[100:103], v[116:119]\n"
"v_mfma_f32_16x16x32_bf16 v[116:119], a[56:59], a[96:99], v[116:119]\n"
"buffer_load_dwordx4 v1, s[48:51], s69 offen offset:1024 sc1 lds\n"
"v_mfma_f32_16x16x32_bf16 v[120:123], a[56:59], a[104:107], v[120:123]\n"
"v_mfma_f32_16x16x32_bf16 v[120:123], a[60:63], a[108:111], v[120:123]\n"
"v_mfma_f32_16x16x32_bf16 v[92:95], a[52:55], a[108:111], v[92:95]\n"
"v_mfma_f32_16x16x32_bf16 v[92:95], a[48:51], a[104:107], v[92:95]\n"
"v_mfma_f32_16x16x32_bf16 v[96:99], a[48:51], a[112:115], v[96:99]\n"
"v_mfma_f32_16x16x32_bf16 v[96:99], a[52:55], a[116:119], v[96:99]\n"
"global_load_dwordx4 a[36:39], v0, s[34:35] offset:-3072 sc1\n"
"v_mfma_f32_16x16x32_bf16 v[124:127], a[60:63], a[116:119], v[124:127]\n"
"v_mfma_f32_16x16x32_bf16 v[124:127], a[56:59], a[112:115], v[124:127]\n"
"v_mfma_f32_16x16x32_bf16 v[128:131], a[56:59], a[120:123], v[128:131]\n"
"v_mfma_f32_16x16x32_bf16 v[128:131], a[60:63], a[124:127], v[128:131]\n"
"v_mfma_f32_16x16x32_bf16 v[100:103], a[52:55], a[124:127], v[100:103]\n"
"v_mfma_f32_16x16x32_bf16 v[100:103], a[48:51], a[120:123], v[100:103]\n"
"v_mfma_f32_16x16x32_bf16 v[104:107], a[48:51], a[128:131], v[104:107]\n"
"v_mfma_f32_16x16x32_bf16 v[104:107], a[52:55], a[132:135], v[104:107]\n"
"global_load_dwordx4 a[40:43], v0, s[34:35] offset:-2048 sc1\n"
"v_mfma_f32_16x16x32_bf16 v[132:135], a[60:63], a[132:135], v[132:135]\n"
"v_mfma_f32_16x16x32_bf16 v[132:135], a[56:59], a[128:131], v[132:135]\n"
"buffer_load_dwordx4 v1, s[48:51], s69 offen offset:2048 sc1 lds\n"
"v_mfma_f32_16x16x32_bf16 v[136:139], a[56:59], a[136:139], v[136:139]\n"
"v_mfma_f32_16x16x32_bf16 v[136:139], a[60:63], a[140:143], v[136:139]\n"
"v_mfma_f32_16x16x32_bf16 v[108:111], a[52:55], a[140:143], v[108:111]\n"
"v_mfma_f32_16x16x32_bf16 v[108:111], a[48:51], a[136:139], v[108:111]\n"
"v_mfma_f32_16x16x32_bf16 v[112:115], a[48:51], a[144:147], v[112:115]\n"
"v_mfma_f32_16x16x32_bf16 v[112:115], a[52:55], a[148:151], v[112:115]\n"
"global_load_dwordx4 a[44:47], v0, s[34:35] offset:-1024 sc1\n"
"v_mfma_f32_16x16x32_bf16 v[140:143], a[60:63], a[148:151], v[140:143]\n"
"v_mfma_f32_16x16x32_bf16 v[140:143], a[56:59], a[144:147], v[140:143]\n"
"v_mfma_f32_16x16x32_bf16 a[12:15], a[56:59], a[152:155], a[12:15]\n"
"v_mfma_f32_16x16x32_bf16 a[12:15], a[60:63], a[156:159], a[12:15]\n"
"v_mfma_f32_16x16x32_bf16 a[8:11], a[52:55], a[156:159], a[8:11]\n"
"v_mfma_f32_16x16x32_bf16 a[8:11], a[48:51], a[152:155], a[8:11]\n"
"s_waitcnt vmcnt(16)\n"
"v_mfma_f32_16x16x32_bf16 v[144:147], a[64:67], a[96:99], v[144:147]\n"
"v_mfma_f32_16x16x32_bf16 v[144:147], a[68:71], a[100:103], v[144:147]\n"
"global_load_dwordx4 a[48:51], v0, s[34:35] offset:0 sc1\n"
"v_mfma_f32_16x16x32_bf16 v[172:175], a[76:79], a[100:103], v[172:175]\n"
"v_mfma_f32_16x16x32_bf16 v[172:175], a[72:75], a[96:99], v[172:175]\n"
"buffer_load_dwordx4 v1, s[48:51], s69 offen offset:3072 sc1 lds\n"
"v_mfma_f32_16x16x32_bf16 v[176:179], a[72:75], a[104:107], v[176:179]\n"
"v_mfma_f32_16x16x32_bf16 v[176:179], a[76:79], a[108:111], v[176:179]\n"
"v_mfma_f32_16x16x32_bf16 v[148:151], a[68:71], a[108:111], v[148:151]\n"
"v_mfma_f32_16x16x32_bf16 v[148:151], a[64:67], a[104:107], v[148:151]\n"
"v_mfma_f32_16x16x32_bf16 v[152:155], a[64:67], a[112:115], v[152:155]\n"
"v_mfma_f32_16x16x32_bf16 v[152:155], a[68:71], a[116:119], v[152:155]\n"
"global_load_dwordx4 a[52:55], v0, s[34:35] offset:1024 sc1\n"
"v_mfma_f32_16x16x32_bf16 v[180:183], a[76:79], a[116:119], v[180:183]\n"
"v_mfma_f32_16x16x32_bf16 v[180:183], a[72:75], a[112:115], v[180:183]\n"
"v_mfma_f32_16x16x32_bf16 v[184:187], a[72:75], a[120:123], v[184:187]\n"
"v_mfma_f32_16x16x32_bf16 v[184:187], a[76:79], a[124:127], v[184:187]\n"
"v_mfma_f32_16x16x32_bf16 v[156:159], a[68:71], a[124:127], v[156:159]\n"
"v_mfma_f32_16x16x32_bf16 v[156:159], a[64:67], a[120:123], v[156:159]\n"
"v_mfma_f32_16x16x32_bf16 v[160:163], a[64:67], a[128:131], v[160:163]\n"
"v_mfma_f32_16x16x32_bf16 v[160:163], a[68:71], a[132:135], v[160:163]\n"
"global_load_dwordx4 a[56:59], v0, s[34:35] offset:2048 sc1\n"
"v_mfma_f32_16x16x32_bf16 v[188:191], a[76:79], a[132:135], v[188:191]\n"
"v_mfma_f32_16x16x32_bf16 v[188:191], a[72:75], a[128:131], v[188:191]\n"
"v_mfma_f32_16x16x32_bf16 v[192:195], a[72:75], a[136:139], v[192:195]\n"
"s_cmp_lg_u32 s30, 0\n"
"s_cselect_b32 s60, s32, s60\n"
"s_cselect_b32 s31, 0, s31\n"
"v_mfma_f32_16x16x32_bf16 v[192:195], a[76:79], a[140:143], v[192:195]\n"
"v_mfma_f32_16x16x32_bf16 v[164:167], a[68:71], a[140:143], v[164:167]\n"
"s_cmp_lg_u32 s30, 0\n"
"s_cselect_b32 s32, s75, s72\n"
"v_mfma_f32_16x16x32_bf16 v[164:167], a[64:67], a[136:139], v[164:167]\n"
"s_sub_u32 s52, s52, s72\n"
"s_subb_u32 s53, s53, 0\n"
"v_mfma_f32_16x16x32_bf16 v[168:171], a[64:67], a[144:147], v[168:171]\n"
"s_add_u32 s52, s52, s32\n"
"s_addc_u32 s53, s53, 0\n"
"v_mfma_f32_16x16x32_bf16 v[168:171], a[68:71], a[148:151], v[168:171]\n"
"global_load_dwordx4 a[60:63], v0, s[34:35] offset:3072 sc1\n"
"v_mfma_f32_16x16x32_bf16 v[196:199], a[76:79], a[148:151], v[196:199]\n"
"s_mov_b32 s72, s32\n"
"v_mfma_f32_16x16x32_bf16 v[196:199], a[72:75], a[144:147], v[196:199]\n"
"v_mfma_f32_16x16x32_bf16 a[20:23], a[72:75], a[152:155], a[20:23]\n"
"s_add_u32 s34, s34, 8192\n"
"v_mfma_f32_16x16x32_bf16 a[20:23], a[76:79], a[156:159], a[20:23]\n"
"v_mfma_f32_16x16x32_bf16 a[16:19], a[68:71], a[156:159], a[16:19]\n"
"s_cmp_lg_u32 s30, 0\n"
"s_cselect_b32 s32, s69, s73\n"
"v_mfma_f32_16x16x32_bf16 a[16:19], a[64:67], a[152:155], a[16:19]\n"
"s_sub_u32 s54, s54, s73\n"
"s_subb_u32 s55, s55, 0\n"
"s_waitcnt vmcnt(12)\n"
"v_mfma_f32_16x16x32_bf16 v[200:203], a[80:83], a[96:99], v[200:203]\n"
"s_add_u32 s54, s54, s32\n"
"s_addc_u32 s55, s55, 0\n"
"v_mfma_f32_16x16x32_bf16 v[200:203], a[84:87], a[100:103], v[200:203]\n"
"global_load_dwordx4 a[64:67], v0, s[34:35] offset:-4096 sc1\n"
"v_mfma_f32_16x16x32_bf16 v[228:231], a[92:95], a[100:103], v[228:231]\n"
"s_mov_b32 s73, s32\n"
"ds_read_b128 a[100:103], v5 offset:1024\n"
"v_mfma_f32_16x16x32_bf16 v[228:231], a[88:91], a[96:99], v[228:231]\n"
"ds_read_b128 a[96:99], v5 offset:0\n"
"v_mfma_f32_16x16x32_bf16 v[232:235], a[88:91], a[104:107], v[232:235]\n"
"v_mfma_f32_16x16x32_bf16 v[232:235], a[92:95], a[108:111], v[232:235]\n"
"v_mfma_f32_16x16x32_bf16 v[204:207], a[84:87], a[108:111], v[204:207]\n"
"ds_read_b128 a[108:111], v5 offset:3072\n"
"v_mfma_f32_16x16x32_bf16 v[204:207], a[80:83], a[104:107], v[204:207]\n"
"ds_read_b128 a[104:107], v5 offset:2048\n"
"v_mfma_f32_16x16x32_bf16 v[208:211], a[80:83], a[112:115], v[208:211]\n"
"v_mfma_f32_16x16x32_bf16 v[208:211], a[84:87], a[116:119], v[208:211]\n"
"global_load_dwordx4 a[68:71], v0, s[34:35] offset:-3072 sc1\n"
"v_mfma_f32_16x16x32_bf16 v[236:239], a[92:95], a[116:119], v[236:239]\n"
"ds_read_b128 a[116:119], v5 offset:5120\n"
"v_mfma_f32_16x16x32_bf16 v[236:239], a[88:91], a[112:115], v[236:239]\n"
"ds_read_b128 a[112:115], v5 offset:4096\n"
"v_mfma_f32_16x16x32_bf16 v[240:243], a[88:91], a[120:123], v[240:243]\n"
"v_mfma_f32_16x16x32_bf16 v[240:243], a[92:95], a[124:127], v[240:243]\n"
"v_mfma_f32_16x16x32_bf16 v[212:215], a[84:87], a[124:127], v[212:215]\n"
"ds_read_b128 a[124:127], v5 offset:7168\n"
"v_mfma_f32_16x16x32_bf16 v[212:215], a[80:83], a[120:123], v[212:215]\n"
"ds_read_b128 a[120:123], v5 offset:6144\n"
"v_mfma_f32_16x16x32_bf16 v[216:219], a[80:83], a[128:131], v[216:219]\n"
"v_mfma_f32_16x16x32_bf16 v[216:219], a[84:87], a[132:135], v[216:219]\n"
"global_load_dwordx4 a[72:75], v0, s[34:35] offset:-2048 sc1\n"
"v_mfma_f32_16x16x32_bf16 v[244:247], a[92:95], a[132:135], v[244:247]\n"
"ds_read_b128 a[132:135], v5 offset:9216\n"
"v_mfma_f32_16x16x32_bf16 v[244:247], a[88:91], a[128:131], v[244:247]\n"
"ds_read_b128 a[128:131], v5 offset:8192\n"
"v_mfma_f32_16x16x32_bf16 v[248:251], a[88:91], a[136:139], v[248:251]\n"
"v_mfma_f32_16x16x32_bf16 v[248:251], a[92:95], a[140:143], v[248:251]\n"
"v_mfma_f32_16x16x32_bf16 v[220:223], a[84:87], a[140:143], v[220:223]\n"
"ds_read_b128 a[140:143], v5 offset:11264\n"
"v_mfma_f32_16x16x32_bf16 v[220:223], a[80:83], a[136:139], v[220:223]\n"
"ds_read_b128 a[136:139], v5 offset:10240\n"
"v_mfma_f32_16x16x32_bf16 v[224:227], a[80:83], a[144:147], v[224:227]\n"
"v_mfma_f32_16x16x32_bf16 v[224:227], a[84:87], a[148:151], v[224:227]\n"
"global_load_dwordx4 a[76:79], v0, s[34:35] offset:-1024 sc1\n"
"v_mfma_f32_16x16x32_bf16 v[252:255], a[92:95], a[148:151], v[252:255]\n"
"ds_read_b128 a[148:151], v5 offset:13312\n"
"v_mfma_f32_16x16x32_bf16 v[252:255], a[88:91], a[144:147], v[252:255]\n"
"ds_read_b128 a[144:147], v5 offset:12288\n"
"v_mfma_f32_16x16x32_bf16 a[28:31], a[88:91], a[152:155], a[28:31]\n"
"v_mfma_f32_16x16x32_bf16 a[28:31], a[92:95], a[156:159], a[28:31]\n"
"v_mfma_f32_16x16x32_bf16 a[24:27], a[84:87], a[156:159], a[24:27]\n"
"ds_read_b128 a[156:159], v5 offset:15360\n"
"v_mfma_f32_16x16x32_bf16 a[24:27], a[80:83], a[152:155], a[24:27]\n"
"s_waitcnt vmcnt(9) lgkmcnt(11)\n"
"ds_read_b128 a[152:155], v5 offset:14336\n"
"v_mfma_f32_16x16x32_bf16 v[32:35], a[32:35], a[96:99], v[32:35]\n"
"v_mfma_f32_16x16x32_bf16 v[32:35], a[36:39], a[100:103], v[32:35]\n"
"global_load_dwordx4 a[80:83], v0, s[34:35] offset:0 sc1\n"
"v_mfma_f32_16x16x32_bf16 v[60:63], a[44:47], a[100:103], v[60:63]\n"
"v_mfma_f32_16x16x32_bf16 v[60:63], a[40:43], a[96:99], v[60:63]\n"
"v_mfma_f32_16x16x32_bf16 v[64:67], a[40:43], a[104:107], v[64:67]\n"
"v_mfma_f32_16x16x32_bf16 v[64:67], a[44:47], a[108:111], v[64:67]\n"
"v_mfma_f32_16x16x32_bf16 v[36:39], a[36:39], a[108:111], v[36:39]\n"
"s_add_u32 s67, s78, 2\n"
"v_mfma_f32_16x16x32_bf16 v[36:39], a[32:35], a[104:107], v[36:39]\n"
"s_waitcnt lgkmcnt(8)\n"
"v_mfma_f32_16x16x32_bf16 v[40:43], a[32:35], a[112:115], v[40:43]\n"
"v_mfma_f32_16x16x32_bf16 v[40:43], a[36:39], a[116:119], v[40:43]\n"
"global_load_dwordx4 a[84:87], v0, s[34:35] offset:1024 sc1\n"
"v_mfma_f32_16x16x32_bf16 v[68:71], a[44:47], a[116:119], v[68:71]\n"
"v_mfma_f32_16x16x32_bf16 v[68:71], a[40:43], a[112:115], v[68:71]\n"
"v_mfma_f32_16x16x32_bf16 v[72:75], a[40:43], a[120:123], v[72:75]\n"
"v_mfma_f32_16x16x32_bf16 v[72:75], a[44:47], a[124:127], v[72:75]\n"
"v_mfma_f32_16x16x32_bf16 v[44:47], a[36:39], a[124:127], v[44:47]\n"
"v_mfma_f32_16x16x32_bf16 v[44:47], a[32:35], a[120:123], v[44:47]\n"
"s_waitcnt lgkmcnt(4)\n"
"v_mfma_f32_16x16x32_bf16 v[48:51], a[32:35], a[128:131], v[48:51]\n"
"s_lshr_b32 s66, 20, s62\n"
"v_mfma_f32_16x16x32_bf16 v[48:51], a[36:39], a[132:135], v[48:51]\n"
"global_load_dwordx4 a[88:91], v0, s[34:35] offset:2048 sc1\n"
"v_mfma_f32_16x16x32_bf16 v[76:79], a[44:47], a[132:135], v[76:79]\n"
"s_and_b32 s66, s66, 1\n"
"s_cselect_b32 s70, 4294574080, 0\n"
"s_cselect_b32 s71, -1, 0\n"
"s_cselect_b32 s66, 0, 131072\n"
"v_mfma_f32_16x16x32_bf16 v[76:79], a[40:43], a[128:131], v[76:79]\n"
"v_mfma_f32_16x16x32_bf16 v[80:83], a[40:43], a[136:139], v[80:83]\n"
"s_add_u32 s70, s70, s66\n"
"v_subrev_u32 v12, s67, v12\n"
"v_mfma_f32_16x16x32_bf16 v[80:83], a[44:47], a[140:143], v[80:83]\n"
"v_xor_b32 v13, s79, v13\n"
"v_mfma_f32_16x16x32_bf16 v[52:55], a[36:39], a[140:143], v[52:55]\n"
"v_subrev_u32 v14, s67, v14\n"
"v_mfma_f32_16x16x32_bf16 v[52:55], a[32:35], a[136:139], v[52:55]\n"
"s_waitcnt lgkmcnt(0)\n"
"v_xor_b32 v15, s79, v15\n"
"v_mfma_f32_16x16x32_bf16 v[56:59], a[32:35], a[144:147], v[56:59]\n"
"v_or_b32 v12, v12, v14\n"
"v_mfma_f32_16x16x32_bf16 v[56:59], a[36:39], a[148:151], v[56:59]\n"
"global_load_dwordx4 a[92:95], v0, s[34:35] offset:3072 sc1\n"
"v_or_b32 v13, v13, v15\n"
"v_mfma_f32_16x16x32_bf16 v[84:87], a[44:47], a[148:151], v[84:87]\n"
"v_or_b32 v12, v12, v13\n"
"v_mfma_f32_16x16x32_bf16 v[84:87], a[40:43], a[144:147], v[84:87]\n"
"v_cmp_lt_u32 vcc, 15, v12\n"
"v_mfma_f32_16x16x32_bf16 a[4:7], a[40:43], a[152:155], a[4:7]\n"
"s_add_u32 s34, s34, 8192\n"
"s_cbranch_vccnz L_slow2\n"
"L_back2:\n"
"v_mfma_f32_16x16x32_bf16 a[4:7], a[44:47], a[156:159], a[4:7]\n"
"v_mfma_f32_16x16x32_bf16 a[0:3], a[36:39], a[156:159], a[0:3]\n"
"v_mfma_f32_16x16x32_bf16 a[0:3], a[32:35], a[152:155], a[0:3]\n"
"s_waitcnt vmcnt(8)\n"
"v_mfma_f32_16x16x32_bf16 v[88:91], a[48:51], a[96:99], v[88:91]\n"
"v_mfma_f32_16x16x32_bf16 v[88:91], a[52:55], a[100:103], v[88:91]\n"
"global_load_dwordx4 a[32:35], v0, s[34:35] offset:-4096 sc1\n"
"v_mfma_f32_16x16x32_bf16 v[116:119], a[60:63], a[100:103], v[116:119]\n"
"v_mfma_f32_16x16x32_bf16 v[116:119], a[56:59], a[96:99], v[116:119]\n"
"v_mfma_f32_16x16x32_bf16 v[120:123], a[56:59], a[104:107], v[120:123]\n"
"v_mfma_f32_16x16x32_bf16 v[120:123], a[60:63], a[108:111], v[120:123]\n"
"v_mfma_f32_16x16x32_bf16 v[92:95], a[52:55], a[108:111], v[92:95]\n"
"v_mfma_f32_16x16x32_bf16 v[92:95], a[48:51], a[104:107], v[92:95]\n"
"v_mfma_f32_16x16x32_bf16 v[96:99], a[48:51], a[112:115], v[96:99]\n"
"v_mfma_f32_16x16x32_bf16 v[96:99], a[52:55], a[116:119], v[96:99]\n"
"global_load_dwordx4 a[36:39], v0, s[34:35] offset:-3072 sc1\n"
"v_mfma_f32_16x16x32_bf16 v[124:127], a[60:63], a[116:119], v[124:127]\n"
"v_mfma_f32_16x16x32_bf16 v[124:127], a[56:59], a[112:115], v[124:127]\n"
"v_mfma_f32_16x16x32_bf16 v[128:131], a[56:59], a[120:123], v[128:131]\n"
"v_mfma_f32_16x16x32_bf16 v[128:131], a[60:63], a[124:127], v[128:131]\n"
"v_mfma_f32_16x16x32_bf16 v[100:103], a[52:55], a[124:127], v[100:103]\n"
"v_mfma_f32_16x16x32_bf16 v[100:103], a[48:51], a[120:123], v[100:103]\n"
"v_mfma_f32_16x16x32_bf16 v[104:107], a[48:51], a[128:131], v[104:107]\n"
"v_mfma_f32_16x16x32_bf16 v[104:107], a[52:55], a[132:135], v[104:107]\n"
"global_load_dwordx4 a[40:43], v0, s[34:35] offset:-2048 sc1\n"
"v_mfma_f32_16x16x32_bf16 v[132:135], a[60:63], a[132:135], v[132:135]\n"
"v_mfma_f32_16x16x32_bf16 v[132:135], a[56:59], a[128:131], v[132:135]\n"
"v_mfma_f32_16x16x32_bf16 v[136:139], a[56:59], a[136:139], v[136:139]\n"
"v_mfma_f32_16x16x32_bf16 v[136:139], a[60:63], a[140:143], v[136:139]\n"
"v_mfma_f32_16x16x32_bf16 v[108:111], a[52:55], a[140:143], v[108:111]\n"
"v_mfma_f32_16x16x32_bf16 v[108:111], a[48:51], a[136:139], v[108:111]\n"
"v_mfma_f32_16x16x32_bf16 v[112:115], a[48:51], a[144:147], v[112:115]\n"
"v_mfma_f32_16x16x32_bf16 v[112:115], a[52:55], a[148:151], v[112:115]\n"
"global_load_dwordx4 a[44:47], v0, s[34:35] offset:-1024 sc1\n"
"v_mfma_f32_16x16x32_bf16 v[140:143], a[60:63], a[148:151], v[140:143]\n"
"v_mfma_f32_16x16x32_bf16 v[140:143], a[56:59], a[144:147], v[140:143]\n"
"v_mfma_f32_16x16x32_bf16 a[12:15], a[56:59], a[152:155], a[12:15]\n"
"v_mfma_f32_16x16x32_bf16 a[12:15], a[60:63], a[156:159], a[12:15]\n"
"v_mfma_f32_16x16x32_bf16 a[8:11], a[52:55], a[156:159], a[8:11]\n"
"v_mfma_f32_16x16x32_bf16 a[8:11], a[48:51], a[152:155], a[8:11]\n"
"s_waitcnt vmcnt(8)\n"
"v_mfma_f32_16x16x32_bf16 v[144:147], a[64:67], a[96:99], v[144:147]\n"
"v_mfma_f32_16x16x32_bf16 v[144:147], a[68:71], a[100:103], v[144:147]\n"
"global_load_dwordx4 a[48:51], v0, s[34:35] offset:0 sc1\n"
"v_mfma_f32_16x16x32_bf16 v[172:175], a[76:79], a[100:103], v[172:175]\n"
"v_mfma_f32_16x16x32_bf16 v[172:175], a[72:75], a[96:99], v[172:175]\n"
"v_mfma_f32_16x16x32_bf16 v[176:179], a[72:75], a[104:107], v[176:179]\n"
"v_mfma_f32_16x16x32_bf16 v[176:179], a[76:79], a[108:111], v[176:179]\n"
"v_mfma_f32_16x16x32_bf16 v[148:151], a[68:71], a[108:111], v[148:151]\n"
"v_mfma_f32_16x16x32_bf16 v[148:151], a[64:67], a[104:107], v[148:151]\n"
"v_mfma_f32_16x16x32_bf16 v[152:155], a[64:67], a[112:115], v[152:155]\n"
"v_mfma_f32_16x16x32_bf16 v[152:155], a[68:71], a[116:119], v[152:155]\n"
"global_load_dwordx4 a[52:55], v0, s[34:35] offset:1024 sc1\n"
"v_mfma_f32_16x16x32_bf16 v[180:183], a[76:79], a[116:119], v[180:183]\n"
"v_mfma_f32_16x16x32_bf16 v[180:183], a[72:75], a[112:115], v[180:183]\n"
"v_mfma_f32_16x16x32_bf16 v[184:187], a[72:75], a[120:123], v[184:187]\n"
"v_mfma_f32_16x16x32_bf16 v[184:187], a[76:79], a[124:127], v[184:187]\n"
"v_mfma_f32_16x16x32_bf16 v[156:159], a[68:71], a[124:127], v[156:159]\n"
"v_mfma_f32_16x16x32_bf16 v[156:159], a[64:67], a[120:123], v[156:159]\n"
"v_mfma_f32_16x16x32_bf16 v[160:163], a[64:67], a[128:131], v[160:163]\n"
"v_mfma_f32_16x16x32_bf16 v[160:163], a[68:71], a[132:135], v[160:163]\n"
"global_load_dwordx4 a[56:59], v0, s[34:35] offset:2048 sc1\n"
"v_mfma_f32_16x16x32_bf16 v[188:191], a[76:79], a[132:135], v[188:191]\n"
"v_mfma_f32_16x16x32_bf16 v[188:191], a[72:75], a[128:131], v[188:191]\n"
"v_mfma_f32_16x16x32_bf16 v[192:195], a[72:75], a[136:139], v[192:195]\n"
"v_mfma_f32_16x16x32_bf16 v[192:195], a[76:79], a[140:143], v[192:195]\n"
"v_mfma_f32_16x16x32_bf16 v[164:167], a[68:71], a[140:143], v[164:167]\n"
"v_mfma_f32_16x16x32_bf16 v[164:167], a[64:67], a[136:139], v[164:167]\n"
"v_mfma_f32_16x16x32_bf16 v[168:171], a[64:67], a[144:147], v[168:171]\n"
"v_mfma_f32_16x16x32_bf16 v[168:171], a[68:71], a[148:151], v[168:171]\n"
"global_load_dwordx4 a[60:63], v0, s[34:35] offset:3072 sc1\n"
"v_mfma_f32_16x16x32_bf16 v[196:199], a[76:79], a[148:151], v[196:199]\n"
"v_mfma_f32_16x16x32_bf16 v[196:199], a[72:75], a[144:147], v[196:199]\n"
"v_mfma_f32_16x16x32_bf16 a[20:23], a[72:75], a[152:155], a[20:23]\n"
"s_add_u32 s34, s34, 8192\n"
"v_mfma_f32_16x16x32_bf16 a[20:23], a[76:79], a[156:159], a[20:23]\n"
"v_mfma_f32_16x16x32_bf16 a[16:19], a[68:71], a[156:159], a[16:19]\n"
"v_mfma_f32_16x16x32_bf16 a[16:19], a[64:67], a[152:155], a[16:19]\n"
"s_waitcnt vmcnt(8)\n"
"v_mfma_f32_16x16x32_bf16 v[200:203], a[80:83], a[96:99], v[200:203]\n"
"v_mfma_f32_16x16x32_bf16 v[200:203], a[84:87], a[100:103], v[200:203]\n"
"global_load_dwordx4 a[64:67], v0, s[34:35] offset:-4096 sc1\n"
"v_mfma_f32_16x16x32_bf16 v[228:231], a[92:95], a[100:103], v[228:231]\n"
"ds_read_b128 a[100:103], v2 offset:17408\n"
"v_mfma_f32_16x16x32_bf16 v[228:231], a[88:91], a[96:99], v[228:231]\n"
"ds_read_b128 a[96:99], v2 offset:16384\n"
"v_mfma_f32_16x16x32_bf16 v[232:235], a[88:91], a[104:107], v[232:235]\n"
"v_mfma_f32_16x16x32_bf16 v[232:235], a[92:95], a[108:111], v[232:235]\n"
"v_mfma_f32_16x16x32_bf16 v[204:207], a[84:87], a[108:111], v[204:207]\n"
"ds_read_b128 a[108:111], v2 offset:19456\n"
"v_mfma_f32_16x16x32_bf16 v[204:207], a[80:83], a[104:107], v[204:207]\n"
"ds_read_b128 a[104:107], v2 offset:18432\n"
"v_mfma_f32_16x16x32_bf16 v[208:211], a[80:83], a[112:115], v[208:211]\n"
"v_mfma_f32_16x16x32_bf16 v[208:211], a[84:87], a[116:119], v[208:211]\n"
"global_load_dwordx4 a[68:71], v0, s[34:35] offset:-3072 sc1\n"
"v_mfma_f32_16x16x32_bf16 v[236:239], a[92:95], a[116:119], v[236:239]\n"
"ds_read_b128 a[116:119], v2 offset:21504\n"
"v_mfma_f32_16x16x32_bf16 v[236:239], a[88:91], a[112:115], v[236:239]\n"
"ds_read_b128 a[112:115], v2 offset:20480\n"
"v_mfma_f32_16x16x32_bf16 v[240:243], a[88:91], a[120:123], v[240:243]\n"
"v_mfma_f32_16x16x32_bf16 v[240:243], a[92:95], a[124:127], v[240:243]\n"
"v_mfma_f32_16x16x32_bf16 v[212:215], a[84:87], a[124:127], v[212:215]\n"
"ds_read_b128 a[124:127], v2 offset:23552\n"
"v_mfma_f32_16x16x32_bf16 v[212:215], a[80:83], a[120:123], v[212:215]\n"
"ds_read_b128 a[120:123], v2 offset:22528\n"
"v_mfma_f32_16x16x32_bf16 v[216:219], a[80:83], a[128:131], v[216:219]\n"
"v_mfma_f32_16x16x32_bf16 v[216:219], a[84:87], a[132:135], v[216:219]\n"
"global_load_dwordx4 a[72:75], v0, s[34:35] offset:-2048 sc1\n"
"v_mfma_f32_16x16x32_bf16 v[244:247], a[92:95], a[132:135], v[244:247]\n"
"ds_read_b128 a[132:135], v2 offset:25600\n"
"v_mfma_f32_16x16x32_bf16 v[244:247], a[88:91], a[128:131], v[244:247]\n"
"ds_read_b128 a[128:131], v2 offset:24576\n"
"v_mfma_f32_16x16x32_bf16 v[248:251], a[88:91], a[136:139], v[248:251]\n"
"v_mfma_f32_16x16x32_bf16 v[248:251], a[92:95], a[140:143], v[248:251]\n"
"v_mfma_f32_16x16x32_bf16 v[220:223], a[84:87], a[140:143], v[220:223]\n"
"ds_read_b128 a[140:143], v2 offset:27648\n"
"v_mfma_f32_16x16x32_bf16 v[220:223], a[80:83], a[136:139], v[220:223]\n"
"ds_read_b128 a[136:139], v2 offset:26624\n"
"v_mfma_f32_16x16x32_bf16 v[224:227], a[80:83], a[144:147], v[224:227]\n"
"v_mfma_f32_16x16x32_bf16 v[224:227], a[84:87], a[148:151], v[224:227]\n"
"global_load_dwordx4 a[76:79], v0, s[34:35] offset:-1024 sc1\n"
"v_mfma_f32_16x16x32_bf16 v[252:255], a[92:95], a[148:151], v[252:255]\n"
"ds_read_b128 a[148:151], v2 offset:29696\n"
"v_mfma_f32_16x16x32_bf16 v[252:255], a[88:91], a[144:147], v[252:255]\n"
"ds_read_b128 a[144:147], v2 offset:28672\n"
"v_mfma_f32_16x16x32_bf16 a[28:31], a[88:91], a[152:155], a[28:31]\n"
"v_mfma_f32_16x16x32_bf16 a[28:31], a[92:95], a[156:159], a[28:31]\n"
"v_mfma_f32_16x16x32_bf16 a[24:27], a[84:87], a[156:159], a[24:27]\n"
"ds_read_b128 a[156:159], v2 offset:31744\n"
"v_mfma_f32_16x16x32_bf16 a[24:27], a[80:83], a[152:155], a[24:27]\n"
"ds_read_b128 a[152:155], v2 offset:30720\n"
"L_P5:\n"
"s_waitcnt vmcnt(8) lgkmcnt(12)\n"
"v_mfma_f32_16x16x32_bf16 v[32:35], a[32:35], a[96:99], v[32:35]\n"
"v_mfma_f32_16x16x32_bf16 v[32:35], a[36:39], a[100:103], v[32:35]\n"
"global_load_dwordx4 a[80:83], v0, s[34:35] offset:0 sc1\n"
"v_mfma_f32_16x16x32_bf16 v[60:63], a[44:47], a[100:103], v[60:63]\n"
"s_barrier\n"
"v_mfma_f32_16x16x32_bf16 v[60:63], a[40:43], a[96:99], v[60:63]\n"
"s_add_u32 s48, s48, s70\n"
"s_addc_u32 s49, s49, s71\n"
"v_mfma_f32_16x16x32_bf16 v[64:67], a[40:43], a[104:107], v[64:67]\n"
"v_mfma_f32_16x16x32_bf16 v[64:67], a[44:47], a[108:111], v[64:67]\n"
"v_mfma_f32_16x16x32_bf16 v[36:39], a[36:39], a[108:111], v[36:39]\n"
"s_add_u32 s52, s52, s70\n"
"s_addc_u32 s53, s53, s71\n"
"v_mfma_f32_16x16x32_bf16 v[36:39], a[32:35], a[104:107], v[36:39]\n"
"s_waitcnt lgkmcnt(8)\n"
"v_mfma_f32_16x16x32_bf16 v[40:43], a[32:35], a[112:115], v[40:43]\n"
"s_add_u32 s54, s54, s70\n"
"s_addc_u32 s55, s55, s71\n"
"v_mfma_f32_16x16x32_bf16 v[40:43], a[36:39], a[116:119], v[40:43]\n"
"global_load_dwordx4 a[84:87], v0, s[34:35] offset:1024 sc1\n"
"v_mfma_f32_16x16x32_bf16 v[68:71], a[44:47], a[116:119], v[68:71]\n"
"v_mfma_f32_16x16x32_bf16 v[68:71], a[40:43], a[112:115], v[68:71]\n"
"v_mfma_f32_16x16x32_bf16 v[72:75], a[40:43], a[120:123], v[72:75]\n"
"v_mfma_f32_16x16x32_bf16 v[72:75], a[44:47], a[124:127], v[72:75]\n"
"s_add_u32 s61, s61, 2048\n"
"v_mfma_f32_16x16x32_bf16 v[44:47], a[36:39], a[124:127], v[44:47]\n"
"v_mfma_f32_16x16x32_bf16 v[44:47], a[32:35], a[120:123], v[44:47]\n"
"s_waitcnt lgkmcnt(4)\n"
"v_mfma_f32_16x16x32_bf16 v[48:51], a[32:35], a[128:131], v[48:51]\n"
"v_lshlrev_b32 v16, 16, v18\n"
"v_mfma_f32_16x16x32_bf16 v[48:51], a[36:39], a[132:135], v[48:51]\n"
"global_load_dwordx4 a[88:91], v0, s[34:35] offset:2048 sc1\n"
"v_and_b32 v17, s63, v18\n"
"v_mfma_f32_16x16x32_bf16 v[76:79], a[44:47], a[132:135], v[76:79]\n"
"v_lshlrev_b32 v18, 16, v19\n"
"v_mfma_f32_16x16x32_bf16 v[76:79], a[40:43], a[128:131], v[76:79]\n"
"v_and_b32 v19, s63, v19\n"
"v_mfma_f32_16x16x32_bf16 v[80:83], a[40:43], a[136:139], v[80:83]\n"
"v_add_f32 v144, v144, v16\n"
"v_add_f32 v145, v145, v17\n"
"v_mfma_f32_16x16x32_bf16 v[80:83], a[44:47], a[140:143], v[80:83]\n"
"v_add_f32 v146, v146, v18\n"
"v_mfma_f32_16x16x32_bf16 v[52:55], a[36:39], a[140:143], v[52:55]\n"
"v_add_f32 v147, v147, v19\n"
"v_mfma_f32_16x16x32_bf16 v[52:55], a[32:35], a[136:139], v[52:55]\n"
"s_waitcnt lgkmcnt(0)\n"
"v_add_f32 v148, v148, v16\n"
"v_mfma_f32_16x16x32_bf16 v[56:59], a[32:35], a[144:147], v[56:59]\n"
"v_add_f32 v149, v149, v17\n"
"v_mfma_f32_16x16x32_bf16 v[56:59], a[36:39], a[148:151], v[56:59]\n"
"global_load_dwordx4 a[92:95], v0, s[34:35] offset:3072 sc1\n"
"v_add_f32 v150, v150, v18\n"
"v_mfma_f32_16x16x32_bf16 v[84:87], a[44:47], a[148:151], v[84:87]\n"
"v_add_f32 v151, v151, v19\n"
"v_mfma_f32_16x16x32_bf16 v[84:87], a[40:43], a[144:147], v[84:87]\n"
"v_add_f32 v152, v152, v16\n"
"v_mfma_f32_16x16x32_bf16 a[4:7], a[40:43], a[152:155], a[4:7]\n"
"s_add_u32 s34, s34, 8192\n"
"v_add_f32 v153, v153, v17\n"
"v_mfma_f32_16x16x32_bf16 a[4:7], a[44:47], a[156:159], a[4:7]\n"
"v_add_f32 v154, v154, v18\n"
"v_mfma_f32_16x16x32_bf16 a[0:3], a[36:39], a[156:159], a[0:3]\n"
"v_add_f32 v155, v155, v19\n"
"v_add_f32 v156, v156, v16\n"
"v_mfma_f32_16x16x32_bf16 a[0:3], a[32:35], a[152:155], a[0:3]\n"
"s_waitcnt vmcnt(8)\n"
"v_add_f32 v157, v157, v17\n"
"v_mfma_f32_16x16x32_bf16 v[88:91], a[48:51], a[96:99], v[88:91]\n"
"v_add_f32 v158, v158, v18\n"
"v_mfma_f32_16x16x32_bf16 v[88:91], a[52:55], a[100:103], v[88:91]\n"
"global_load_dwordx4 a[32:35], v0, s[34:35] offset:-4096 sc1\n"
"v_add_f32 v159, v159, v19\n"
"v_mfma_f32_16x16x32_bf16 v[116:119], a[60:63], a[100:103], v[116:119]\n"
"ds_read_b128 a[100:103], v3 offset:17408\n"
"v_add_f32 v160, v160, v16\n"
"v_mfma_f32_16x16x32_bf16 v[116:119], a[56:59], a[96:99], v[116:119]\n"
"ds_read_b128 a[96:99], v3 offset:16384\n"
"v_add_f32 v161, v161, v17\n"
"v_mfma_f32_16x16x32_bf16 v[120:123], a[56:59], a[104:107], v[120:123]\n"
"v_add_f32 v162, v162, v18\n"
"v_mfma_f32_16x16x32_bf16 v[120:123], a[60:63], a[108:111], v[120:123]\n"
"v_add_f32 v163, v163, v19\n"
"v_mfma_f32_16x16x32_bf16 v[92:95], a[52:55], a[108:111], v[92:95]\n"
"ds_read_b128 a[108:111], v3 offset:19456\n"
"v_add_f32 v164, v164, v16\n"
"v_mfma_f32_16x16x32_bf16 v[92:95], a[48:51], a[104:107], v[92:95]\n"
"ds_read_b128 a[104:107], v3 offset:18432\n"
"v_add_f32 v165, v165, v17\n"
"v_mfma_f32_16x16x32_bf16 v[96:99], a[48:51], a[112:115], v[96:99]\n"
"v_add_f32 v166, v166, v18\n"
"v_add_f32 v167, v167, v19\n"
"v_mfma_f32_16x16x32_bf16 v[96:99], a[52:55], a[116:119], v[96:99]\n"
"global_load_dwordx4 a[36:39], v0, s[34:35] offset:-3072 sc1\n"
"v_add_f32 v168, v168, v16\n"
"v_mfma_f32_16x16x32_bf16 v[124:127], a[60:63], a[116:119], v[124:127]\n"
"ds_read_b128 a[116:119], v3 offset:21504\n"
"v_add_f32 v169, v169, v17\n"
"v_mfma_f32_16x16x32_bf16 v[124:127], a[56:59], a[112:115], v[124:127]\n"
"ds_read_b128 a[112:115], v3 offset:20480\n"
"v_add_f32 v170, v170, v18\n"
"v_mfma_f32_16x16x32_bf16 v[128:131], a[56:59], a[120:123], v[128:131]\n"
"v_add_f32 v171, v171, v19\n"
"v_mfma_f32_16x16x32_bf16 v[128:131], a[60:63], a[124:127], v[128:131]\n"
"v_accvgpr_read_b32 v12, a16\n"
"v_mfma_f32_16x16x32_bf16 v[100:103], a[52:55], a[124:127], v[100:103]\n"
"ds_read_b128 a[124:127], v3 offset:23552\n"
"v_accvgpr_read_b32 v13, a17\n"
"v_mfma_f32_16x16x32_bf16 v[100:103], a[48:51], a[120:123], v[100:103]\n"
"ds_read_b128 a[120:123], v3 offset:22528\n"
"v_accvgpr_read_b32 v14, a18\n"
"v_mfma_f32_16x16x32_bf16 v[104:107], a[48:51], a[128:131], v[104:107]\n"
"v_accvgpr_read_b32 v15, a19\n"
"v_add_f32 v12, v12, v16\n"
"v_mfma_f32_16x16x32_bf16 v[104:107], a[52:55], a[132:135], v[104:107]\n"
"global_load_dwordx4 a[40:43], v0, s[34:35] offset:-2048 sc1\n"
"v_add_f32 v13, v13, v17\n"
"v_mfma_f32_16x16x32_bf16 v[132:135], a[60:63], a[132:135], v[132:135]\n"
"ds_read_b128 a[132:135], v3 offset:25600\n"
"v_add_f32 v14, v14, v18\n"
"v_mfma_f32_16x16x32_bf16 v[132:135], a[56:59], a[128:131], v[132:135]\n"
"ds_read_b128 a[128:131], v3 offset:24576\n"
"v_add_f32 v15, v15, v19\n"
"v_mfma_f32_16x16x32_bf16 v[136:139], a[56:59], a[136:139], v[136:139]\n"
"v_accvgpr_write_b32 a16, v12\n"
"v_mfma_f32_16x16x32_bf16 v[136:139], a[60:63], a[140:143], v[136:139]\n"
"v_accvgpr_write_b32 a17, v13\n"
"v_mfma_f32_16x16x32_bf16 v[108:111], a[52:55], a[140:143], v[108:111]\n"
"ds_read_b128 a[140:143], v3 offset:27648\n"
"v_accvgpr_write_b32 a18, v14\n"
"v_mfma_f32_16x16x32_bf16 v[108:111], a[48:51], a[136:139], v[108:111]\n"
"ds_read_b128 a[136:139], v3 offset:26624\n"
"v_accvgpr_write_b32 a19, v15\n"
"v_mfma_f32_16x16x32_bf16 v[112:115], a[48:51], a[144:147], v[112:115]\n"
"v_lshlrev_b32 v20, 16, v22\n"
"v_and_b32 v21, s63, v22\n"
"v_mfma_f32_16x16x32_bf16 v[112:115], a[52:55], a[148:151], v[112:115]\n"
"global_load_dwordx4 a[44:47], v0, s[34:35] offset:-1024 sc1\n"
"v_lshlrev_b32 v22, 16, v23\n"
"v_mfma_f32_16x16x32_bf16 v[140:143], a[60:63], a[148:151], v[140:143]\n"
"ds_read_b128 a[148:151], v3 offset:29696\n"
"v_and_b32 v23, s63, v23\n"
"v_mfma_f32_16x16x32_bf16 v[140:143], a[56:59], a[144:147], v[140:143]\n"
"ds_read_b128 a[144:147], v3 offset:28672\n"
"v_add_f32 v172, v172, v20\n"
"v_mfma_f32_16x16x32_bf16 a[12:15], a[56:59], a[152:155], a[12:15]\n"
"v_add_f32 v173, v173, v21\n"
"v_mfma_f32_16x16x32_bf16 a[12:15], a[60:63], a[156:159], a[12:15]\n"
"v_add_f32 v174, v174, v22\n"
"v_mfma_f32_16x16x32_bf16 a[8:11], a[52:55], a[156:159], a[8:11]\n"
"ds_read_b128 a[156:159], v3 offset:31744\n"
"v_add_f32 v175, v175, v23\n"
"v_mfma_f32_16x16x32_bf16 a[8:11], a[48:51], a[152:155], a[8:11]\n"
"s_waitcnt vmcnt(8) lgkmcnt(11)\n"
"ds_read_b128 a[152:155], v3 offset:30720\n"
"v_add_f32 v176, v176, v20\n"
"v_mfma_f32_16x16x32_bf16 v[32:35], a[64:67], a[96:99], v[32:35]\n"
"v_add_f32 v177, v177, v21\n"
"v_mfma_f32_16x16x32_bf16 v[32:35], a[68:71], a[100:103], v[32:35]\n"
"global_load_dwordx4 a[48:51], v0, s[34:35] offset:0 sc1\n"
"v_add_f32 v178, v178, v22\n"
"v_mfma_f32_16x16x32_bf16 v[60:63], a[76:79], a[100:103], v[60:63]\n"
"v_add_f32 v179, v179, v23\n"
"v_mfma_f32_16x16x32_bf16 v[60:63], a[72:75], a[96:99], v[60:63]\n"
"v_add_f32 v180, v180, v20\n"
"v_add_f32 v181, v181, v21\n"
"v_mfma_f32_16x16x32_bf16 v[64:67], a[72:75], a[104:107], v[64:67]\n"
"v_add_f32 v182, v182, v22\n"
"v_mfma_f32_16x16x32_bf16 v[64:67], a[76:79], a[108:111], v[64:67]\n"
"v_add_f32 v183, v183, v23\n"
"v_mfma_f32_16x16x32_bf16 v[36:39], a[68:71], a[108:111], v[36:39]\n"
"v_add_f32 v184, v184, v20\n"
"v_mfma_f32_16x16x32_bf16 v[36:39], a[64:67], a[104:107], v[36:39]\n"
"s_waitcnt lgkmcnt(8)\n"
"v_add_f32 v185, v185, v21\n"
"v_mfma_f32_16x16x32_bf16 v[40:43], a[64:67], a[112:115], v[40:43]\n"
"v_add_f32 v186, v186, v22\n"
"v_mfma_f32_16x16x32_bf16 v[40:43], a[68:71], a[116:119], v[40:43]\n"
"global_load_dwordx4 a[52:55], v0, s[34:35] offset:1024 sc1\n"
"v_add_f32 v187, v187, v23\n"
"v_mfma_f32_16x16x32_bf16 v[68:71], a[76:79], a[116:119], v[68:71]\n"
"v_add_f32 v188, v188, v20\n"
"v_mfma_f32_16x16x32_bf16 v[68:71], a[72:75], a[112:115], v[68:71]\n"
"v_add_f32 v189, v189, v21\n"
"v_mfma_f32_16x16x32_bf16 v[72:75], a[72:75], a[120:123], v[72:75]\n"
"v_add_f32 v190, v190, v22\n"
"v_add_f32 v191, v191, v23\n"
"v_mfma_f32_16x16x32_bf16 v[72:75], a[76:79], a[124:127], v[72:75]\n"
"v_add_f32 v192, v192, v20\n"
"v_mfma_f32_16x16x32_bf16 v[44:47], a[68:71], a[124:127], v[44:47]\n"
"v_add_f32 v193, v193, v21\n"
"v_mfma_f32_16x16x32_bf16 v[44:47], a[64:67], a[120:123], v[44:47]\n"
"s_waitcnt lgkmcnt(4)\n"
"v_add_f32 v194, v194, v22\n"
"v_mfma_f32_16x16x32_bf16 v[48:51], a[64:67], a[128:131], v[48:51]\n"
"v_add_f32 v195, v195, v23\n"
"v_mfma_f32_16x16x32_bf16 v[48:51], a[68:71], a[132:135], v[48:51]\n"
"global_load_dwordx4 a[56:59], v0, s[34:35] offset:2048 sc1\n"
"v_add_f32 v196, v196, v20\n"
"v_mfma_f32_16x16x32_bf16 v[76:79], a[76:79], a[132:135], v[76:79]\n"
"v_add_f32 v197, v197, v21\n"
"v_mfma_f32_16x16x32_bf16 v[76:79], a[72:75], a[128:131], v[76:79]\n"
"v_add_f32 v198, v198, v22\n"
"v_mfma_f32_16x16x32_bf16 v[80:83], a[72:75], a[136:139], v[80:83]\n"
"v_add_f32 v199, v199, v23\n"
"v_mfma_f32_16x16x32_bf16 v[80:83], a[76:79], a[140:143], v[80:83]\n"
"v_accvgpr_read_b32 v12, a20\n"
"v_mfma_f32_16x16x32_bf16 v[52:55], a[68:71], a[140:143], v[52:55]\n"
"v_accvgpr_read_b32 v13, a21\n"
"v_accvgpr_read_b32 v14, a22\n"
"v_mfma_f32_16x16x32_bf16 v[52:55], a[64:67], a[136:139], v[52:55]\n"
"s_waitcnt lgkmcnt(0)\n"
"v_accvgpr_read_b32 v15, a23\n"
"v_mfma_f32_16x16x32_bf16 v[56:59], a[64:67], a[144:147], v[56:59]\n"
"v_add_f32 v12, v12, v20\n"
"v_mfma_f32_16x16x32_bf16 v[56:59], a[68:71], a[148:151], v[56:59]\n"
"global_load_dwordx4 a[60:63], v0, s[34:35] offset:3072 sc1\n"
"v_add_f32 v13, v13, v21\n"
"v_mfma_f32_16x16x32_bf16 v[84:87], a[76:79], a[148:151], v[84:87]\n"
"v_add_f32 v14, v14, v22\n"
"v_mfma_f32_16x16x32_bf16 v[84:87], a[72:75], a[144:147], v[84:87]\n"
"v_add_f32 v15, v15, v23\n"
"v_mfma_f32_16x16x32_bf16 a[4:7], a[72:75], a[152:155], a[4:7]\n"
"s_add_u32 s34, s34, 8192\n"
"v_accvgpr_write_b32 a20, v12\n"
"v_mfma_f32_16x16x32_bf16 a[4:7], a[76:79], a[156:159], a[4:7]\n"
"v_accvgpr_write_b32 a21, v13\n"
"v_mfma_f32_16x16x32_bf16 a[0:3], a[68:71], a[156:159], a[0:3]\n"
"v_accvgpr_write_b32 a22, v14\n"
"v_mfma_f32_16x16x32_bf16 a[0:3], a[64:67], a[152:155], a[0:3]\n"
"s_waitcnt vmcnt(8)\n"
"v_accvgpr_write_b32 a23, v15\n"
"v_lshlrev_b32 v24, 16, v26\n"
"v_mfma_f32_16x16x32_bf16 v[88:91], a[80:83], a[96:99], v[88:91]\n"
"v_and_b32 v25, s63, v26\n"
"v_mfma_f32_16x16x32_bf16 v[88:91], a[84:87], a[100:103], v[88:91]\n"
"global_load_dwordx4 a[64:67], v0, s[34:35] offset:-4096 sc1\n"
"v_lshlrev_b32 v26, 16, v27\n"
"v_mfma_f32_16x16x32_bf16 v[116:119], a[92:95], a[100:103], v[116:119]\n"
"ds_read_b128 a[100:103], v4 offset:17408\n"
"v_and_b32 v27, s63, v27\n"
"v_mfma_f32_16x16x32_bf16 v[116:119], a[88:91], a[96:99], v[116:119]\n"
"ds_read_b128 a[96:99], v4 offset:16384\n"
"v_add_f32 v200, v200, v24\n"
"v_mfma_f32_16x16x32_bf16 v[120:123], a[88:91], a[104:107], v[120:123]\n"
"v_add_f32 v201, v201, v25\n"
"v_mfma_f32_16x16x32_bf16 v[120:123], a[92:95], a[108:111], v[120:123]\n"
"v_add_f32 v202, v202, v26\n"
"v_mfma_f32_16x16x32_bf16 v[92:95], a[84:87], a[108:111], v[92:95]\n"
"ds_read_b128 a[108:111], v4 offset:19456\n"
"v_add_f32 v203, v203, v27\n"
"v_mfma_f32_16x16x32_bf16 v[92:95], a[80:83], a[104:107], v[92:95]\n"
"ds_read_b128 a[104:107], v4 offset:18432\n"
"v_add_f32 v204, v204, v24\n"
"v_mfma_f32_16x16x32_bf16 v[96:99], a[80:83], a[112:115], v[96:99]\n"
"v_add_f32 v205, v205, v25\n"
"v_add_f32 v206, v206, v26\n"
"v_mfma_f32_16x16x32_bf16 v[96:99], a[84:87], a[116:119], v[96:99]\n"
"global_load_dwordx4 a[68:71], v0, s[34:35] offset:-3072 sc1\n"
"v_add_f32 v207, v207, v27\n"
"v_mfma_f32_16x16x32_bf16 v[124:127], a[92:95], a[116:119], v[124:127]\n"
"ds_read_b128 a[116:119], v4 offset:21504\n"
"v_add_f32 v208, v208, v24\n"
"v_mfma_f32_16x16x32_bf16 v[124:127], a[88:91], a[112:115], v[124:127]\n"
"ds_read_b128 a[112:115], v4 offset:20480\n"
"v_add_f32 v209, v209, v25\n"
"v_mfma_f32_16x16x32_bf16 v[128:131], a[88:91], a[120:123], v[128:131]\n"
"v_add_f32 v210, v210, v26\n"
"v_mfma_f32_16x16x32_bf16 v[128:131], a[92:95], a[124:127], v[128:131]\n"
"v_add_f32 v211, v211, v27\n"
"v_mfma_f32_16x16x32_bf16 v[100:103], a[84:87], a[124:127], v[100:103]\n"
"ds_read_b128 a[124:127], v4 offset:23552\n"
"v_add_f32 v212, v212, v24\n"
"v_mfma_f32_16x16x32_bf16 v[100:103], a[80:83], a[120:123], v[100:103]\n"
"ds_read_b128 a[120:123], v4 offset:22528\n"
"v_add_f32 v213, v213, v25\n"
"v_mfma_f32_16x16x32_bf16 v[104:107], a[80:83], a[128:131], v[104:107]\n"
"v_add_f32 v214, v214, v26\n"
"v_add_f32 v215, v215, v27\n"
"v_mfma_f32_16x16x32_bf16 v[104:107], a[84:87], a[132:135], v[104:107]\n"
"global_load_dwordx4 a[72:75], v0, s[34:35] offset:-2048 sc1\n"
"v_add_f32 v216, v216, v24\n"
"v_mfma_f32_16x16x32_bf16 v[132:135], a[92:95], a[132:135], v[132:135]\n"
"ds_read_b128 a[132:135], v4 offset:25600\n"
"v_add_f32 v217, v217, v25\n"
"v_mfma_f32_16x16x32_bf16 v[132:135], a[88:91], a[128:131], v[132:135]\n"
"ds_read_b128 a[128:131], v4 offset:24576\n"
"v_add_f32 v218, v218, v26\n"
"v_mfma_f32_16x16x32_bf16 v[136:139], a[88:91], a[136:139], v[136:139]\n"
"v_add_f32 v219, v219, v27\n"
"v_mfma_f32_16x16x32_bf16 v[136:139], a[92:95], a[140:143], v[136:139]\n"
"v_add_f32 v220, v220, v24\n"
"v_mfma_f32_16x16x32_bf16 v[108:111], a[84:87], a[140:143], v[108:111]\n"
"ds_read_b128 a[140:143], v4 offset:27648\n"
"v_add_f32 v221, v221, v25\n"
"v_mfma_f32_16x16x32_bf16 v[108:111], a[80:83], a[136:139], v[108:111]\n"
"ds_read_b128 a[136:139], v4 offset:26624\n"
"v_add_f32 v222, v222, v26\n"
"v_mfma_f32_16x16x32_bf16 v[112:115], a[80:83], a[144:147], v[112:115]\n"
"v_add_f32 v223, v223, v27\n"
"v_mfma_f32_16x16x32_bf16 v[112:115], a[84:87], a[148:151], v[112:115]\n"
"global_load_dwordx4 a[76:79], v0, s[34:35] offset:-1024 sc1\n"
"v_add_f32 v224, v224, v24\n"
"v_mfma_f32_16x16x32_bf16 v[140:143], a[92:95], a[148:151], v[140:143]\n"
"ds_read_b128 a[148:151], v4 offset:29696\n"
"v_add_f32 v225, v225, v25\n"
"v_mfma_f32_16x16x32_bf16 v[140:143], a[88:91], a[144:147], v[140:143]\n"
"ds_read_b128 a[144:147], v4 offset:28672\n"
"v_add_f32 v226, v226, v26\n"
"v_mfma_f32_16x16x32_bf16 a[12:15], a[88:91], a[152:155], a[12:15]\n"
"v_add_f32 v227, v227, v27\n"
"v_accvgpr_read_b32 v12, a24\n"
"v_mfma_f32_16x16x32_bf16 a[12:15], a[92:95], a[156:159], a[12:15]\n"
"v_accvgpr_read_b32 v13, a25\n"
"v_mfma_f32_16x16x32_bf16 a[8:11], a[84:87], a[156:159], a[8:11]\n"
"ds_read_b128 a[156:159], v4 offset:31744\n"
"v_accvgpr_read_b32 v14, a26\n"
"v_mfma_f32_16x16x32_bf16 a[8:11], a[80:83], a[152:155], a[8:11]\n"
"s_waitcnt vmcnt(8) lgkmcnt(11)\n"
"ds_read_b128 a[152:155], v4 offset:30720\n"
"v_accvgpr_read_b32 v15, a27\n"
"v_mfma_f32_16x16x32_bf16 v[32:35], a[32:35], a[96:99], v[32:35]\n"
"v_add_f32 v12, v12, v24\n"
"v_mfma_f32_16x16x32_bf16 v[32:35], a[36:39], a[100:103], v[32:35]\n"
"global_load_dwordx4 a[80:83], v0, s[34:35] offset:0 sc1\n"
"v_add_f32 v13, v13, v25\n"
"v_mfma_f32_16x16x32_bf16 v[60:63], a[44:47], a[100:103], v[60:63]\n"
"v_add_f32 v14, v14, v26\n"
"v_mfma_f32_16x16x32_bf16 v[60:63], a[40:43], a[96:99], v[60:63]\n"
"v_add_f32 v15, v15, v27\n"
"v_mfma_f32_16x16x32_bf16 v[64:67], a[40:43], a[104:107], v[64:67]\n"
"v_accvgpr_write_b32 a24, v12\n"
"v_accvgpr_write_b32 a25, v13\n"
"v_mfma_f32_16x16x32_bf16 v[64:67], a[44:47], a[108:111], v[64:67]\n"
"v_accvgpr_write_b32 a26, v14\n"
"v_mfma_f32_16x16x32_bf16 v[36:39], a[36:39], a[108:111], v[36:39]\n"
"v_accvgpr_write_b32 a27, v15\n"
"v_mfma_f32_16x16x32_bf16 v[36:39], a[32:35], a[104:107], v[36:39]\n"
"s_waitcnt lgkmcnt(8)\n"
"v_lshlrev_b32 v28, 16, v30\n"
"v_mfma_f32_16x16x32_bf16 v[40:43], a[32:35], a[112:115], v[40:43]\n"
"v_and_b32 v29, s63, v30\n"
"v_mfma_f32_16x16x32_bf16 v[40:43], a[36:39], a[116:119], v[40:43]\n"
"global_load_dwordx4 a[84:87], v0, s[34:35] offset:1024 sc1\n"
"v_lshlrev_b32 v30, 16, v31\n"
"v_mfma_f32_16x16x32_bf16 v[68:71], a[44:47], a[116:119], v[68:71]\n"
"v_and_b32 v31, s63, v31\n"
"v_mfma_f32_16x16x32_bf16 v[68:71], a[40:43], a[112:115], v[68:71]\n"
"v_add_f32 v228, v228, v28\n"
"v_mfma_f32_16x16x32_bf16 v[72:75], a[40:43], a[120:123], v[72:75]\n"
"v_add_f32 v229, v229, v29\n"
"v_mfma_f32_16x16x32_bf16 v[72:75], a[44:47], a[124:127], v[72:75]\n"
"v_add_f32 v230, v230, v30\n"
"v_mfma_f32_16x16x32_bf16 v[44:47], a[36:39], a[124:127], v[44:47]\n"
"v_add_f32 v231, v231, v31\n"
"v_add_f32 v232, v232, v28\n"
"v_mfma_f32_16x16x32_bf16 v[44:47], a[32:35], a[120:123], v[44:47]\n"
"s_waitcnt lgkmcnt(4)\n"
"v_add_f32 v233, v233, v29\n"
"v_mfma_f32_16x16x32_bf16 v[48:51], a[32:35], a[128:131], v[48:51]\n"
"v_add_f32 v234, v234, v30\n"
"v_mfma_f32_16x16x32_bf16 v[48:51], a[36:39], a[132:135], v[48:51]\n"
"global_load_dwordx4 a[88:91], v0, s[34:35] offset:2048 sc1\n"
"v_add_f32 v235, v235, v31\n"
"v_mfma_f32_16x16x32_bf16 v[76:79], a[44:47], a[132:135], v[76:79]\n"
"v_add_f32 v236, v236, v28\n"
"v_mfma_f32_16x16x32_bf16 v[76:79], a[40:43], a[128:131], v[76:79]\n"
"v_add_f32 v237, v237, v29\n"
"v_mfma_f32_16x16x32_bf16 v[80:83], a[40:43], a[136:139], v[80:83]\n"
"v_add_f32 v238, v238, v30\n"
"v_mfma_f32_16x16x32_bf16 v[80:83], a[44:47], a[140:143], v[80:83]\n"
"v_add_f32 v239, v239, v31\n"
"v_mfma_f32_16x16x32_bf16 v[52:55], a[36:39], a[140:143], v[52:55]\n"
"v_add_f32 v240, v240, v28\n"
"v_mfma_f32_16x16x32_bf16 v[52:55], a[32:35], a[136:139], v[52:55]\n"
"s_waitcnt lgkmcnt(0)\n"
"v_add_f32 v241, v241, v29\n"
"v_add_f32 v242, v242, v30\n"
"v_mfma_f32_16x16x32_bf16 v[56:59], a[32:35], a[144:147], v[56:59]\n"
"v_add_f32 v243, v243, v31\n"
"v_mfma_f32_16x16x32_bf16 v[56:59], a[36:39], a[148:151], v[56:59]\n"
"global_load_dwordx4 a[92:95], v0, s[34:35] offset:3072 sc1\n"
"v_add_f32 v244, v244, v28\n"
"v_mfma_f32_16x16x32_bf16 v[84:87], a[44:47], a[148:151], v[84:87]\n"
"v_add_f32 v245, v245, v29\n"
"v_mfma_f32_16x16x32_bf16 v[84:87], a[40:43], a[144:147], v[84:87]\n"
"v_add_f32 v246, v246, v30\n"
"v_mfma_f32_16x16x32_bf16 a[4:7], a[40:43], a[152:155], a[4:7]\n"
"s_add_u32 s34, s34, 8192\n"
"v_add_f32 v247, v247, v31\n"
"v_mfma_f32_16x16x32_bf16 a[4:7], a[44:47], a[156:159], a[4:7]\n"
"v_add_f32 v248, v248, v28\n"
"v_mfma_f32_16x16x32_bf16 a[0:3], a[36:39], a[156:159], a[0:3]\n"
"v_add_f32 v249, v249, v29\n"
"v_mfma_f32_16x16x32_bf16 a[0:3], a[32:35], a[152:155], a[0:3]\n"
"s_waitcnt vmcnt(8)\n"
"v_add_f32 v250, v250, v30\n"
"v_mfma_f32_16x16x32_bf16 v[88:91], a[48:51], a[96:99], v[88:91]\n"
"v_add_f32 v251, v251, v31\n"
"v_add_f32 v252, v252, v28\n"
"v_mfma_f32_16x16x32_bf16 v[88:91], a[52:55], a[100:103], v[88:91]\n"
"global_load_dwordx4 a[32:35], v0, s[34:35] offset:-4096 sc1\n"
"v_add_f32 v253, v253, v29\n"
"v_mfma_f32_16x16x32_bf16 v[116:119], a[60:63], a[100:103], v[116:119]\n"
"ds_read_b128 a[100:103], v5 offset:17408\n"
"v_add_f32 v254, v254, v30\n"
"v_mfma_f32_16x16x32_bf16 v[116:119], a[56:59], a[96:99], v[116:119]\n"
"ds_read_b128 a[96:99], v5 offset:16384\n"
"v_add_f32 v255, v255, v31\n"
"v_mfma_f32_16x16x32_bf16 v[120:123], a[56:59], a[104:107], v[120:123]\n"
"v_accvgpr_read_b32 v12, a28\n"
"v_mfma_f32_16x16x32_bf16 v[120:123], a[60:63], a[108:111], v[120:123]\n"
"v_accvgpr_read_b32 v13, a29\n"
"v_mfma_f32_16x16x32_bf16 v[92:95], a[52:55], a[108:111], v[92:95]\n"
"ds_read_b128 a[108:111], v5 offset:19456\n"
"v_accvgpr_read_b32 v14, a30\n"
"v_mfma_f32_16x16x32_bf16 v[92:95], a[48:51], a[104:107], v[92:95]\n"
"ds_read_b128 a[104:107], v5 offset:18432\n"
"v_accvgpr_read_b32 v15, a31\n"
"v_mfma_f32_16x16x32_bf16 v[96:99], a[48:51], a[112:115], v[96:99]\n"
"v_add_f32 v12, v12, v28\n"
"v_add_f32 v13, v13, v29\n"
"v_mfma_f32_16x16x32_bf16 v[96:99], a[52:55], a[116:119], v[96:99]\n"
"global_load_dwordx4 a[36:39], v0, s[34:35] offset:-3072 sc1\n"
"v_add_f32 v14, v14, v30\n"
"v_mfma_f32_16x16x32_bf16 v[124:127], a[60:63], a[116:119], v[124:127]\n"
"ds_read_b128 a[116:119], v5 offset:21504\n"
"v_add_f32 v15, v15, v31\n"
"v_mfma_f32_16x16x32_bf16 v[124:127], a[56:59], a[112:115], v[124:127]\n"
"ds_read_b128 a[112:115], v5 offset:20480\n"
"v_accvgpr_write_b32 a28, v12\n"
"v_mfma_f32_16x16x32_bf16 v[128:131], a[56:59], a[120:123], v[128:131]\n"
"v_accvgpr_write_b32 a29, v13\n"
"v_mfma_f32_16x16x32_bf16 v[128:131], a[60:63], a[124:127], v[128:131]\n"
"v_accvgpr_write_b32 a30, v14\n"
"v_mfma_f32_16x16x32_bf16 v[100:103], a[52:55], a[124:127], v[100:103]\n"
"ds_read_b128 a[124:127], v5 offset:23552\n"
"v_accvgpr_write_b32 a31, v15\n"
"v_mfma_f32_16x16x32_bf16 v[100:103], a[48:51], a[120:123], v[100:103]\n"
"ds_read_b128 a[120:123], v5 offset:22528\n"
"v_mfma_f32_16x16x32_bf16 v[104:107], a[48:51], a[128:131], v[104:107]\n"
"v_mfma_f32_16x16x32_bf16 v[104:107], a[52:55], a[132:135], v[104:107]\n"
"global_load_dwordx4 a[40:43], v0, s[34:35] offset:-2048 sc1\n"
"v_mfma_f32_16x16x32_bf16 v[132:135], a[60:63], a[132:135], v[132:135]\n"
"ds_read_b128 a[132:135], v5 offset:25600\n"
"v_mfma_f32_16x16x32_bf16 v[132:135], a[56:59], a[128:131], v[132:135]\n"
"ds_read_b128 a[128:131], v5 offset:24576\n"
"v_mfma_f32_16x16x32_bf16 v[136:139], a[56:59], a[136:139], v[136:139]\n"
"ds_read_b128 v[16:19], v7 offset:32768\n"
"v_mfma_f32_16x16x32_bf16 v[136:139], a[60:63], a[140:143], v[136:139]\n"
"v_mfma_f32_16x16x32_bf16 v[108:111], a[52:55], a[140:143], v[108:111]\n"
"ds_read_b128 a[140:143], v5 offset:27648\n"
"v_mfma_f32_16x16x32_bf16 v[108:111], a[48:51], a[136:139], v[108:111]\n"
"ds_read_b128 a[136:139], v5 offset:26624\n"
"v_mfma_f32_16x16x32_bf16 v[112:115], a[48:51], a[144:147], v[112:115]\n"
"v_mfma_f32_16x16x32_bf16 v[112:115], a[52:55], a[148:151], v[112:115]\n"
"global_load_dwordx4 a[44:47], v0, s[34:35] offset:-1024 sc1\n"
"v_mfma_f32_16x16x32_bf16 v[140:143], a[60:63], a[148:151], v[140:143]\n"
"ds_read_b128 a[148:151], v5 offset:29696\n"
"v_mfma_f32_16x16x32_bf16 v[140:143], a[56:59], a[144:147], v[140:143]\n"
"ds_read_b128 a[144:147], v5 offset:28672\n"
"v_mfma_f32_16x16x32_bf16 a[12:15], a[56:59], a[152:155], a[12:15]\n"
"v_mfma_f32_16x16x32_bf16 a[12:15], a[60:63], a[156:159], a[12:15]\n"
"v_mfma_f32_16x16x32_bf16 a[8:11], a[52:55], a[156:159], a[8:11]\n"
"ds_read_b128 a[156:159], v5 offset:31744\n"
"v_mfma_f32_16x16x32_bf16 a[8:11], a[48:51], a[152:155], a[8:11]\n"
"ds_read_b128 a[152:155], v5 offset:30720\n"
"s_branch L_P6\n"
"L_last:\n"
"s_waitcnt vmcnt(9) lgkmcnt(12)\n"
"v_mfma_f32_16x16x32_bf16 v[32:35], a[32:35], a[96:99], v[32:35]\n"
"s_add_u32 s34, s36, 1454080\n"
"s_add_u32 s48, s48, 131072\n"
"s_addc_u32 s49, s49, 0\n"
"v_mfma_f32_16x16x32_bf16 v[32:35], a[36:39], a[100:103], v[32:35]\n"
"global_load_dwordx4 a[80:83], v0, s[34:35] offset:0 sc1\n"
"v_mfma_f32_16x16x32_bf16 v[60:63], a[44:47], a[100:103], v[60:63]\n"
"s_barrier\n"
"v_mfma_f32_16x16x32_bf16 v[60:63], a[40:43], a[96:99], v[60:63]\n"
"v_mfma_f32_16x16x32_bf16 v[64:67], a[40:43], a[104:107], v[64:67]\n"
"v_mfma_f32_16x16x32_bf16 v[64:67], a[44:47], a[108:111], v[64:67]\n"
"v_mfma_f32_16x16x32_bf16 v[36:39], a[36:39], a[108:111], v[36:39]\n"
"v_subrev_u32 v8, s65, v8\n"
"v_mfma_f32_16x16x32_bf16 v[36:39], a[32:35], a[104:107], v[36:39]\n"
"s_waitcnt lgkmcnt(8)\n"
"v_xor_b32 v9, s79, v9\n"
"v_mfma_f32_16x16x32_bf16 v[40:43], a[32:35], a[112:115], v[40:43]\n"
"v_or_b32 v8, v8, v9\n"
"v_mfma_f32_16x16x32_bf16 v[40:43], a[36:39], a[116:119], v[40:43]\n"
"global_load_dwordx4 a[84:87], v0, s[34:35] offset:1024 sc1\n"
"v_cmp_lt_u32 vcc, 15, v8\n"
"v_mfma_f32_16x16x32_bf16 v[68:71], a[44:47], a[116:119], v[68:71]\n"
"s_cbranch_vccnz L_slow4\n"
"L_back4:\n"
"v_mfma_f32_16x16x32_bf16 v[68:71], a[40:43], a[112:115], v[68:71]\n"
"v_mfma_f32_16x16x32_bf16 v[72:75], a[40:43], a[120:123], v[72:75]\n"
"v_mfma_f32_16x16x32_bf16 v[72:75], a[44:47], a[124:127], v[72:75]\n"
"v_mfma_f32_16x16x32_bf16 v[44:47], a[36:39], a[124:127], v[44:47]\n"
"v_mfma_f32_16x16x32_bf16 v[44:47], a[32:35], a[120:123], v[44:47]\n"
"s_add_u32 m0, s64, 16384\n"
"s_waitcnt lgkmcnt(4)\n"
"v_mfma_f32_16x16x32_bf16 v[48:51], a[32:35], a[128:131], v[48:51]\n"
"v_mfma_f32_16x16x32_bf16 v[48:51], a[36:39], a[132:135], v[48:51]\n"
"global_load_dwordx4 a[88:91], v0, s[34:35] offset:2048 sc1\n"
"v_mfma_f32_16x16x32_bf16 v[76:79], a[44:47], a[132:135], v[76:79]\n"
"v_mfma_f32_16x16x32_bf16 v[76:79], a[40:43], a[128:131], v[76:79]\n"
"v_mfma_f32_16x16x32_bf16 v[80:83], a[40:43], a[136:139], v[80:83]\n"
"s_mov_b32 s97, s37\n"
"v_mfma_f32_16x16x32_bf16 v[80:83], a[44:47], a[140:143], v[80:83]\n"
"v_mfma_f32_16x16x32_bf16 v[52:55], a[36:39], a[140:143], v[52:55]\n"
"v_mfma_f32_16x16x32_bf16 v[52:55], a[32:35], a[136:139], v[52:55]\n"
"s_waitcnt lgkmcnt(0)\n"
"v_mfma_f32_16x16x32_bf16 v[56:59], a[32:35], a[144:147], v[56:59]\n"
"buffer_load_dwordx4 v0, s[48:51], s75 offen offset:0 sc1 lds\n"
"v_mfma_f32_16x16x32_bf16 v[56:59], a[36:39], a[148:151], v[56:59]\n"
"global_load_dwordx4 a[92:95], v0, s[34:35] offset:3072 sc1\n"
"v_mfma_f32_16x16x32_bf16 v[84:87], a[44:47], a[148:151], v[84:87]\n"
"v_mfma_f32_16x16x32_bf16 v[84:87], a[40:43], a[144:147], v[84:87]\n"
"v_mfma_f32_16x16x32_bf16 a[4:7], a[40:43], a[152:155], a[4:7]\n"
"v_mfma_f32_16x16x32_bf16 a[4:7], a[44:47], a[156:159], a[4:7]\n"
"v_mfma_f32_16x16x32_bf16 a[0:3], a[36:39], a[156:159], a[0:3]\n"
"v_mfma_f32_16x16x32_bf16 a[0:3], a[32:35], a[152:155], a[0:3]\n"
"s_waitcnt vmcnt(10)\n"
"v_mfma_f32_16x16x32_bf16 v[88:91], a[48:51], a[96:99], v[88:91]\n"
"s_add_u32 s34, s36, 1458176\n"
"v_mfma_f32_16x16x32_bf16 v[88:91], a[52:55], a[100:103], v[88:91]\n"
"global_load_dwordx4 a[32:35], v0, s[34:35] offset:0 sc1\n"
"v_mfma_f32_16x16x32_bf16 v[116:119], a[60:63], a[100:103], v[116:119]\n"
"buffer_load_dwordx4 v0, s[48:51], s75 offen offset:1024 sc1 lds\n"
"v_mfma_f32_16x16x32_bf16 v[116:119], a[56:59], a[96:99], v[116:119]\n"
"v_mfma_f32_16x16x32_bf16 v[120:123], a[56:59], a[104:107], v[120:123]\n"
"v_mfma_f32_16x16x32_bf16 v[120:123], a[60:63], a[108:111], v[120:123]\n"
"v_mfma_f32_16x16x32_bf16 v[92:95], a[52:55], a[108:111], v[92:95]\n"
"v_mfma_f32_16x16x32_bf16 v[92:95], a[48:51], a[104:107], v[92:95]\n"
"v_mfma_f32_16x16x32_bf16 v[96:99], a[48:51], a[112:115], v[96:99]\n"
"v_mfma_f32_16x16x32_bf16 v[96:99], a[52:55], a[116:119], v[96:99]\n"
"global_load_dwordx4 a[36:39], v0, s[34:35] offset:1024 sc1\n"
"v_mfma_f32_16x16x32_bf16 v[124:127], a[60:63], a[116:119], v[124:127]\n"
"v_mfma_f32_16x16x32_bf16 v[124:127], a[56:59], a[112:115], v[124:127]\n"
"v_mfma_f32_16x16x32_bf16 v[128:131], a[56:59], a[120:123], v[128:131]\n"
"buffer_load_dwordx4 v0, s[48:51], s75 offen offset:2048 sc1 lds\n"
"v_mfma_f32_16x16x32_bf16 v[128:131], a[60:63], a[124:127], v[128:131]\n"
"v_mfma_f32_16x16x32_bf16 v[100:103], a[52:55], a[124:127], v[100:103]\n"
"v_mfma_f32_16x16x32_bf16 v[100:103], a[48:51], a[120:123], v[100:103]\n"
"v_mfma_f32_16x16x32_bf16 v[104:107], a[48:51], a[128:131], v[104:107]\n"
"v_mfma_f32_16x16x32_bf16 v[104:107], a[52:55], a[132:135], v[104:107]\n"
"global_load_dwordx4 a[40:43], v0, s[34:35] offset:2048 sc1\n"
"v_mfma_f32_16x16x32_bf16 v[132:135], a[60:63], a[132:135], v[132:135]\n"
"v_mfma_f32_16x16x32_bf16 v[132:135], a[56:59], a[128:131], v[132:135]\n"
"v_mfma_f32_16x16x32_bf16 v[136:139], a[56:59], a[136:139], v[136:139]\n"
"v_mfma_f32_16x16x32_bf16 v[136:139], a[60:63], a[140:143], v[136:139]\n"
"buffer_load_dwordx4 v0, s[48:51], s75 offen offset:3072 sc1 lds\n"
"v_mfma_f32_16x16x32_bf16 v[108:111], a[52:55], a[140:143], v[108:111]\n"
"v_mfma_f32_16x16x32_bf16 v[108:111], a[48:51], a[136:139], v[108:111]\n"
"v_mfma_f32_16x16x32_bf16 v[112:115], a[48:51], a[144:147], v[112:115]\n"
"v_mfma_f32_16x16x32_bf16 v[112:115], a[52:55], a[148:151], v[112:115]\n"
"global_load_dwordx4 a[44:47], v0, s[34:35] offset:3072 sc1\n"
"v_mfma_f32_16x16x32_bf16 v[140:143], a[60:63], a[148:151], v[140:143]\n"
"v_mfma_f32_16x16x32_bf16 v[140:143], a[56:59], a[144:147], v[140:143]\n"
"v_mfma_f32_16x16x32_bf16 a[12:15], a[56:59], a[152:155], a[12:15]\n"
"v_mfma_f32_16x16x32_bf16 a[12:15], a[60:63], a[156:159], a[12:15]\n"
"v_mfma_f32_16x16x32_bf16 a[8:11], a[52:55], a[156:159], a[8:11]\n"
"v_mfma_f32_16x16x32_bf16 a[8:11], a[48:51], a[152:155], a[8:11]\n"
"s_add_u32 m0, s64, 20480\n"
"s_waitcnt vmcnt(12)\n"
"v_mfma_f32_16x16x32_bf16 v[144:147], a[64:67], a[96:99], v[144:147]\n"
"s_add_u32 s34, s36, 1462272\n"
"v_mfma_f32_16x16x32_bf16 v[144:147], a[68:71], a[100:103], v[144:147]\n"
"global_load_dwordx4 a[48:51], v0, s[34:35] offset:0 sc1\n"
"v_mfma_f32_16x16x32_bf16 v[172:175], a[76:79], a[100:103], v[172:175]\n"
"v_mfma_f32_16x16x32_bf16 v[172:175], a[72:75], a[96:99], v[172:175]\n"
"v_mfma_f32_16x16x32_bf16 v[176:179], a[72:75], a[104:107], v[176:179]\n"
"v_mfma_f32_16x16x32_bf16 v[176:179], a[76:79], a[108:111], v[176:179]\n"
"v_mfma_f32_16x16x32_bf16 v[148:151], a[68:71], a[108:111], v[148:151]\n"
"v_lshlrev_b32 v16, 16, v18\n"
"v_and_b32 v17, s63, v18\n"
"v_mfma_f32_16x16x32_bf16 v[148:151], a[64:67], a[104:107], v[148:151]\n"
"v_lshlrev_b32 v18, 16, v19\n"
"v_and_b32 v19, s63, v19\n"
"v_mfma_f32_16x16x32_bf16 v[152:155], a[64:67], a[112:115], v[152:155]\n"
"buffer_load_dwordx4 v1, s[48:51], s75 offen offset:0 sc1 lds\n"
"v_mfma_f32_16x16x32_bf16 v[152:155], a[68:71], a[116:119], v[152:155]\n"
"global_load_dwordx4 a[52:55], v0, s[34:35] offset:1024 sc1\n"
"v_add_f32 v88, v88, v16\n"
"v_mfma_f32_16x16x32_bf16 v[180:183], a[76:79], a[116:119], v[180:183]\n"
"v_add_f32 v89, v89, v17\n"
"v_add_f32 v90, v90, v18\n"
"v_mfma_f32_16x16x32_bf16 v[180:183], a[72:75], a[112:115], v[180:183]\n"
"v_add_f32 v91, v91, v19\n"
"v_add_f32 v92, v92, v16\n"
"v_mfma_f32_16x16x32_bf16 v[184:187], a[72:75], a[120:123], v[184:187]\n"
"v_add_f32 v93, v93, v17\n"
"v_add_f32 v94, v94, v18\n"
"v_mfma_f32_16x16x32_bf16 v[184:187], a[76:79], a[124:127], v[184:187]\n"
"v_add_f32 v95, v95, v19\n"
"v_add_f32 v96, v96, v16\n"
"v_mfma_f32_16x16x32_bf16 v[156:159], a[68:71], a[124:127], v[156:159]\n"
"v_add_f32 v97, v97, v17\n"
"v_add_f32 v98, v98, v18\n"
"v_mfma_f32_16x16x32_bf16 v[156:159], a[64:67], a[120:123], v[156:159]\n"
"v_add_f32 v99, v99, v19\n"
"v_add_f32 v100, v100, v16\n"
"v_mfma_f32_16x16x32_bf16 v[160:163], a[64:67], a[128:131], v[160:163]\n"
"v_add_f32 v101, v101, v17\n"
"v_add_f32 v102, v102, v18\n"
"v_mfma_f32_16x16x32_bf16 v[160:163], a[68:71], a[132:135], v[160:163]\n"
"global_load_dwordx4 a[56:59], v0, s[34:35] offset:2048 sc1\n"
"v_add_f32 v103, v103, v19\n"
"v_mfma_f32_16x16x32_bf16 v[188:191], a[76:79], a[132:135], v[188:191]\n"
"buffer_load_dwordx4 v1, s[48:51], s75 offen offset:1024 sc1 lds\n"
"v_mfma_f32_16x16x32_bf16 v[188:191], a[72:75], a[128:131], v[188:191]\n"
"v_add_f32 v104, v104, v16\n"
"v_add_f32 v105, v105, v17\n"
"v_mfma_f32_16x16x32_bf16 v[192:195], a[72:75], a[136:139], v[192:195]\n"
"v_add_f32 v106, v106, v18\n"
"v_add_f32 v107, v107, v19\n"
"v_mfma_f32_16x16x32_bf16 v[192:195], a[76:79], a[140:143], v[192:195]\n"
"v_add_f32 v108, v108, v16\n"
"v_add_f32 v109, v109, v17\n"
"v_mfma_f32_16x16x32_bf16 v[164:167], a[68:71], a[140:143], v[164:167]\n"
"v_add_f32 v110, v110, v18\n"
"v_add_f32 v111, v111, v19\n"
"v_mfma_f32_16x16x32_bf16 v[164:167], a[64:67], a[136:139], v[164:167]\n"
"v_add_f32 v112, v112, v16\n"
"v_add_f32 v113, v113, v17\n"
"v_mfma_f32_16x16x32_bf16 v[168:171], a[64:67], a[144:147], v[168:171]\n"
"v_add_f32 v114, v114, v18\n"
"v_add_f32 v115, v115, v19\n"
"v_mfma_f32_16x16x32_bf16 v[168:171], a[68:71], a[148:151], v[168:171]\n"
"global_load_dwordx4 a[60:63], v0, s[34:35] offset:3072 sc1\n"
"v_accvgpr_read_b32 v12, a8\n"
"v_mfma_f32_16x16x32_bf16 v[196:199], a[76:79], a[148:151], v[196:199]\n"
"v_accvgpr_read_b32 v13, a9\n"
"v_accvgpr_read_b32 v14, a10\n"
"v_mfma_f32_16x16x32_bf16 v[196:199], a[72:75], a[144:147], v[196:199]\n"
"buffer_load_dwordx4 v1, s[48:51], s75 offen offset:2048 sc1 lds\n"
"v_mfma_f32_16x16x32_bf16 a[20:23], a[72:75], a[152:155], a[20:23]\n"
"v_accvgpr_read_b32 v15, a11\n"
"v_add_f32 v12, v12, v16\n"
"v_mfma_f32_16x16x32_bf16 a[20:23], a[76:79], a[156:159], a[20:23]\n"
"v_add_f32 v13, v13, v17\n"
"v_add_f32 v14, v14, v18\n"
"v_mfma_f32_16x16x32_bf16 a[16:19], a[68:71], a[156:159], a[16:19]\n"
"v_add_f32 v15, v15, v19\n"
"v_accvgpr_write_b32 a8, v12\n"
"v_mfma_f32_16x16x32_bf16 a[16:19], a[64:67], a[152:155], a[16:19]\n"
"s_waitcnt vmcnt(14)\n"
"v_accvgpr_write_b32 a9, v13\n"
"v_accvgpr_write_b32 a10, v14\n"
"v_accvgpr_write_b32 a11, v15\n"
"v_mfma_f32_16x16x32_bf16 v[200:203], a[80:83], a[96:99], v[200:203]\n"
"s_add_u32 s34, s36, 1474560\n"
"v_lshlrev_b32 v20, 16, v22\n"
"v_and_b32 v21, s63, v22\n"
"v_mfma_f32_16x16x32_bf16 v[200:203], a[84:87], a[100:103], v[200:203]\n"
"global_load_dwordx4 a[64:67], v0, s[34:35] offset:0 sc1\n"
"v_lshlrev_b32 v22, 16, v23\n"
"v_mfma_f32_16x16x32_bf16 v[228:231], a[92:95], a[100:103], v[228:231]\n"
"ds_read_b128 a[100:103], v3 offset:1024\n"
"v_and_b32 v23, s63, v23\n"
"v_mfma_f32_16x16x32_bf16 v[228:231], a[88:91], a[96:99], v[228:231]\n"
"ds_read_b128 a[96:99], v3 offset:0\n"
"v_add_f32 v116, v116, v20\n"
"v_mfma_f32_16x16x32_bf16 v[232:235], a[88:91], a[104:107], v[232:235]\n"
"v_add_f32 v117, v117, v21\n"
"v_add_f32 v118, v118, v22\n"
"v_mfma_f32_16x16x32_bf16 v[232:235], a[92:95], a[108:111], v[232:235]\n"
"buffer_load_dwordx4 v1, s[48:51], s75 offen offset:3072 sc1 lds\n"
"v_mfma_f32_16x16x32_bf16 v[204:207], a[84:87], a[108:111], v[204:207]\n"
"ds_read_b128 a[108:111], v3 offset:3072\n"
"v_add_f32 v119, v119, v23\n"
"v_add_f32 v120, v120, v20\n"
"v_mfma_f32_16x16x32_bf16 v[204:207], a[80:83], a[104:107], v[204:207]\n"
"ds_read_b128 a[104:107], v3 offset:2048\n"
"v_add_f32 v121, v121, v21\n"
"v_mfma_f32_16x16x32_bf16 v[208:211], a[80:83], a[112:115], v[208:211]\n"
"v_add_f32 v122, v122, v22\n"
"v_add_f32 v123, v123, v23\n"
"v_mfma_f32_16x16x32_bf16 v[208:211], a[84:87], a[116:119], v[208:211]\n"
"global_load_dwordx4 a[68:71], v0, s[34:35] offset:1024 sc1\n"
"v_add_f32 v124, v124, v20\n"
"v_add_f32 v125, v125, v21\n"
"v_mfma_f32_16x16x32_bf16 v[236:239], a[92:95], a[116:119], v[236:239]\n"
"ds_read_b128 a[116:119], v3 offset:5120\n"
"v_add_f32 v126, v126, v22\n"
"v_mfma_f32_16x16x32_bf16 v[236:239], a[88:91], a[112:115], v[236:239]\n"
"ds_read_b128 a[112:115], v3 offset:4096\n"
"v_add_f32 v127, v127, v23\n"
"v_mfma_f32_16x16x32_bf16 v[240:243], a[88:91], a[120:123], v[240:243]\n"
"v_add_f32 v128, v128, v20\n"
"v_add_f32 v129, v129, v21\n"
"v_mfma_f32_16x16x32_bf16 v[240:243], a[92:95], a[124:127], v[240:243]\n"
"v_add_f32 v130, v130, v22\n"
"v_add_f32 v131, v131, v23\n"
"v_mfma_f32_16x16x32_bf16 v[212:215], a[84:87], a[124:127], v[212:215]\n"
"s_add_u32 m0, s64, 24576\n"
"ds_read_b128 a[124:127], v3 offset:7168\n"
"v_add_f32 v132, v132, v20\n"
"v_add_f32 v133, v133, v21\n"
"v_mfma_f32_16x16x32_bf16 v[212:215], a[80:83], a[120:123], v[212:215]\n"
"ds_read_b128 a[120:123], v3 offset:6144\n"
"v_add_f32 v134, v134, v22\n"
"v_mfma_f32_16x16x32_bf16 v[216:219], a[80:83], a[128:131], v[216:219]\n"
"v_add_f32 v135, v135, v23\n"
"v_add_f32 v136, v136, v20\n"
"v_mfma_f32_16x16x32_bf16 v[216:219], a[84:87], a[132:135], v[216:219]\n"
"global_load_dwordx4 a[72:75], v0, s[34:35] offset:2048 sc1\n"
"v_add_f32 v137, v137, v21\n"
"v_mfma_f32_16x16x32_bf16 v[244:247], a[92:95], a[132:135], v[244:247]\n"
"ds_read_b128 a[132:135], v3 offset:9216\n"
"v_add_f32 v138, v138, v22\n"
"v_add_f32 v139, v139, v23\n"
"v_mfma_f32_16x16x32_bf16 v[244:247], a[88:91], a[128:131], v[244:247]\n"
"ds_read_b128 a[128:131], v3 offset:8192\n"
"v_add_f32 v140, v140, v20\n"
"v_mfma_f32_16x16x32_bf16 v[248:251], a[88:91], a[136:139], v[248:251]\n"
"v_add_f32 v141, v141, v21\n"
"v_add_f32 v142, v142, v22\n"
"v_mfma_f32_16x16x32_bf16 v[248:251], a[92:95], a[140:143], v[248:251]\n"
"v_add_f32 v143, v143, v23\n"
"v_accvgpr_read_b32 v12, a12\n"
"v_mfma_f32_16x16x32_bf16 v[220:223], a[84:87], a[140:143], v[220:223]\n"
"ds_read_b128 a[140:143], v3 offset:11264\n"
"v_accvgpr_read_b32 v13, a13\n"
"v_mfma_f32_16x16x32_bf16 v[220:223], a[80:83], a[136:139], v[220:223]\n"
"ds_read_b128 a[136:139], v3 offset:10240\n"
"v_accvgpr_read_b32 v14, a14\n"
"v_accvgpr_read_b32 v15, a15\n"
"v_mfma_f32_16x16x32_bf16 v[224:227], a[80:83], a[144:147], v[224:227]\n"
"buffer_load_dwordx4 v0, s[48:51], s69 offen offset:0 sc1 lds\n"
"v_add_f32 v12, v12, v20\n"
"v_mfma_f32_16x16x32_bf16 v[224:227], a[84:87], a[148:151], v[224:227]\n"
"global_load_dwordx4 a[76:79], v0, s[34:35] offset:3072 sc1\n"
"v_add_f32 v13, v13, v21\n"
"v_add_f32 v14, v14, v22\n"
"v_mfma_f32_16x16x32_bf16 v[252:255], a[92:95], a[148:151], v[252:255]\n"
"ds_read_b128 a[148:151], v3 offset:13312\n"
"v_add_f32 v15, v15, v23\n"
"v_accvgpr_write_b32 a12, v12\n"
"v_mfma_f32_16x16x32_bf16 v[252:255], a[88:91], a[144:147], v[252:255]\n"
"ds_read_b128 a[144:147], v3 offset:12288\n"
"v_accvgpr_write_b32 a13, v13\n"
"v_mfma_f32_16x16x32_bf16 a[28:31], a[88:91], a[152:155], a[28:31]\n"
"v_accvgpr_write_b32 a14, v14\n"
"v_accvgpr_write_b32 a15, v15\n"
"v_mfma_f32_16x16x32_bf16 a[28:31], a[92:95], a[156:159], a[28:31]\n"
"v_mfma_f32_16x16x32_bf16 a[24:27], a[84:87], a[156:159], a[24:27]\n"
"ds_read_b128 a[156:159], v3 offset:15360\n"
"v_mfma_f32_16x16x32_bf16 a[24:27], a[80:83], a[152:155], a[24:27]\n"
"s_waitcnt vmcnt(13) lgkmcnt(11)\n"
"ds_read_b128 a[152:155], v3 offset:14336\n"
"v_mfma_f32_16x16x32_bf16 v[32:35], a[32:35], a[96:99], v[32:35]\n"
"s_add_u32 s34, s36, 1478656\n"
"v_mfma_f32_16x16x32_bf16 v[32:35], a[36:39], a[100:103], v[32:35]\n"
"global_load_dwordx4 a[80:83], v0, s[34:35] offset:0 sc1\n"
"v_mfma_f32_16x16x32_bf16 v[60:63], a[44:47], a[100:103], v[60:63]\n"
"buffer_load_dwordx4 v0, s[48:51], s69 offen offset:1024 sc1 lds\n"
"v_mfma_f32_16x16x32_bf16 v[60:63], a[40:43], a[96:99], v[60:63]\n"
"v_lshrrev_b32 v16, 4, v0\n"
"v_mfma_f32_16x16x32_bf16 v[64:67], a[40:43], a[104:107], v[64:67]\n"
"v_mfma_f32_16x16x32_bf16 v[64:67], a[44:47], a[108:111], v[64:67]\n"
"v_mfma_f32_16x16x32_bf16 v[36:39], a[36:39], a[108:111], v[36:39]\n"
"v_mfma_f32_16x16x32_bf16 v[36:39], a[32:35], a[104:107], v[36:39]\n"
"s_waitcnt lgkmcnt(8)\n"
"v_and_b32 v16, 0x30, v16\n"
"v_mfma_f32_16x16x32_bf16 v[40:43], a[32:35], a[112:115], v[40:43]\n"
"v_mfma_f32_16x16x32_bf16 v[40:43], a[36:39], a[116:119], v[40:43]\n"
"global_load_dwordx4 a[84:87], v0, s[34:35] offset:1024 sc1\n"
"v_mfma_f32_16x16x32_bf16 v[68:71], a[44:47], a[116:119], v[68:71]\n"
"buffer_load_dwordx2 v[18:19], v16, s[44:47], s61 offen offset:128\n"
"v_mfma_f32_16x16x32_bf16 v[68:71], a[40:43], a[112:115], v[68:71]\n"
"buffer_load_dwordx4 v0, s[48:51], s69 offen offset:2048 sc1 lds\n"
"v_mfma_f32_16x16x32_bf16 v[72:75], a[40:43], a[120:123], v[72:75]\n"
"v_mfma_f32_16x16x32_bf16 v[72:75], a[44:47], a[124:127], v[72:75]\n"
"v_lshrrev_b32 v20, 4, v0\n"
"v_mfma_f32_16x16x32_bf16 v[44:47], a[36:39], a[124:127], v[44:47]\n"
"v_mfma_f32_16x16x32_bf16 v[44:47], a[32:35], a[120:123], v[44:47]\n"
"s_waitcnt lgkmcnt(4)\n"
"v_mfma_f32_16x16x32_bf16 v[48:51], a[32:35], a[128:131], v[48:51]\n"
"v_and_b32 v20, 0x30, v20\n"
"v_mfma_f32_16x16x32_bf16 v[48:51], a[36:39], a[132:135], v[48:51]\n"
"global_load_dwordx4 a[88:91], v0, s[34:35] offset:2048 sc1\n"
"v_mfma_f32_16x16x32_bf16 v[76:79], a[44:47], a[132:135], v[76:79]\n"
"v_mfma_f32_16x16x32_bf16 v[76:79], a[40:43], a[128:131], v[76:79]\n"
"v_mfma_f32_16x16x32_bf16 v[80:83], a[40:43], a[136:139], v[80:83]\n"
"buffer_load_dwordx4 v0, s[48:51], s69 offen offset:3072 sc1 lds\n"
"v_mfma_f32_16x16x32_bf16 v[80:83], a[44:47], a[140:143], v[80:83]\n"
"buffer_load_dwordx2 v[22:23], v20, s[44:47], s61 offen offset:136\n"
"v_mfma_f32_16x16x32_bf16 v[52:55], a[36:39], a[140:143], v[52:55]\n"
"v_mfma_f32_16x16x32_bf16 v[52:55], a[32:35], a[136:139], v[52:55]\n"
"s_waitcnt lgkmcnt(0)\n"
"v_mfma_f32_16x16x32_bf16 v[56:59], a[32:35], a[144:147], v[56:59]\n"
"v_lshrrev_b32 v24, 4, v0\n"
"v_mfma_f32_16x16x32_bf16 v[56:59], a[36:39], a[148:151], v[56:59]\n"
"global_load_dwordx4 a[92:95], v0, s[34:35] offset:3072 sc1\n"
"v_mfma_f32_16x16x32_bf16 v[84:87], a[44:47], a[148:151], v[84:87]\n"
"v_mfma_f32_16x16x32_bf16 v[84:87], a[40:43], a[144:147], v[84:87]\n"
"v_and_b32 v24, 0x30, v24\n"
"v_mfma_f32_16x16x32_bf16 a[4:7], a[40:43], a[152:155], a[4:7]\n"
"v_mfma_f32_16x16x32_bf16 a[4:7], a[44:47], a[156:159], a[4:7]\n"
"v_mfma_f32_16x16x32_bf16 a[0:3], a[36:39], a[156:159], a[0:3]\n"
"s_add_u32 m0, s64, 28672\n"
"v_mfma_f32_16x16x32_bf16 a[0:3], a[32:35], a[152:155], a[0:3]\n"
"s_waitcnt vmcnt(16)\n"
"buffer_load_dwordx2 v[26:27], v24, s[44:47], s61 offen offset:192\n"
"v_mfma_f32_16x16x32_bf16 v[88:91], a[48:51], a[96:99], v[88:91]\n"
"s_add_u32 s34, s36, 1490944\n"
"v_lshrrev_b32 v28, 4, v0\n"
"v_mfma_f32_16x16x32_bf16 v[88:91], a[52:55], a[100:103], v[88:91]\n"
"global_load_dwordx4 a[32:35], v0, s[34:35] offset:0 sc1\n"
"v_mfma_f32_16x16x32_bf16 v[116:119], a[60:63], a[100:103], v[116:119]\n"
"ds_read_b128 a[100:103], v4 offset:1024\n"
"v_mfma_f32_16x16x32_bf16 v[116:119], a[56:59], a[96:99], v[116:119]\n"
"ds_read_b128 a[96:99], v4 offset:0\n"
"v_mfma_f32_16x16x32_bf16 v[120:123], a[56:59], a[104:107], v[120:123]\n"
"v_and_b32 v28, 0x30, v28\n"
"v_mfma_f32_16x16x32_bf16 v[120:123], a[60:63], a[108:111], v[120:123]\n"
"buffer_load_dwordx2 v[30:31], v28, s[44:47], s61 offen offset:200\n"
"v_mfma_f32_16x16x32_bf16 v[92:95], a[52:55], a[108:111], v[92:95]\n"
"ds_read_b128 a[108:111], v4 offset:3072\n"
"v_mfma_f32_16x16x32_bf16 v[92:95], a[48:51], a[104:107], v[92:95]\n"
"ds_read_b128 a[104:107], v4 offset:2048\n"
"v_mfma_f32_16x16x32_bf16 v[96:99], a[48:51], a[112:115], v[96:99]\n"
"buffer_load_dwordx4 v1, s[48:51], s69 offen offset:0 sc1 lds\n"
"v_mfma_f32_16x16x32_bf16 v[96:99], a[52:55], a[116:119], v[96:99]\n"
"global_load_dwordx4 a[36:39], v0, s[34:35] offset:1024 sc1\n"
"v_mfma_f32_16x16x32_bf16 v[124:127], a[60:63], a[116:119], v[124:127]\n"
"ds_read_b128 a[116:119], v4 offset:5120\n"
"v_mfma_f32_16x16x32_bf16 v[124:127], a[56:59], a[112:115], v[124:127]\n"
"ds_read_b128 a[112:115], v4 offset:4096\n"
"v_mfma_f32_16x16x32_bf16 v[128:131], a[56:59], a[120:123], v[128:131]\n"
"v_mfma_f32_16x16x32_bf16 v[128:131], a[60:63], a[124:127], v[128:131]\n"
"v_mfma_f32_16x16x32_bf16 v[100:103], a[52:55], a[124:127], v[100:103]\n"
"ds_read_b128 a[124:127], v4 offset:7168\n"
"v_mfma_f32_16x16x32_bf16 v[100:103], a[48:51], a[120:123], v[100:103]\n"
"ds_read_b128 a[120:123], v4 offset:6144\n"
"v_mfma_f32_16x16x32_bf16 v[104:107], a[48:51], a[128:131], v[104:107]\n"
"buffer_load_dwordx4 v1, s[48:51], s69 offen offset:1024 sc1 lds\n"
"v_mfma_f32_16x16x32_bf16 v[104:107], a[52:55], a[132:135], v[104:107]\n"
"global_load_dwordx4 a[40:43], v0, s[34:35] offset:2048 sc1\n"
"v_mfma_f32_16x16x32_bf16 v[132:135], a[60:63], a[132:135], v[132:135]\n"
"ds_read_b128 a[132:135], v4 offset:9216\n"
"v_mfma_f32_16x16x32_bf16 v[132:135], a[56:59], a[128:131], v[132:135]\n"
"ds_read_b128 a[128:131], v4 offset:8192\n"
"v_mfma_f32_16x16x32_bf16 v[136:139], a[56:59], a[136:139], v[136:139]\n"
"v_mfma_f32_16x16x32_bf16 v[136:139], a[60:63], a[140:143], v[136:139]\n"
"v_mfma_f32_16x16x32_bf16 v[108:111], a[52:55], a[140:143], v[108:111]\n"
"ds_read_b128 a[140:143], v4 offset:11264\n"
"v_mfma_f32_16x16x32_bf16 v[108:111], a[48:51], a[136:139], v[108:111]\n"
"ds_read_b128 a[136:139], v4 offset:10240\n"
"v_mfma_f32_16x16x32_bf16 v[112:115], a[48:51], a[144:147], v[112:115]\n"
"v_mfma_f32_16x16x32_bf16 v[112:115], a[52:55], a[148:151], v[112:115]\n"
"global_load_dwordx4 a[44:47], v0, s[34:35] offset:3072 sc1\n"
"v_mfma_f32_16x16x32_bf16 v[140:143], a[60:63], a[148:151], v[140:143]\n"
"ds_read_b128 a[148:151], v4 offset:13312\n"
"v_mfma_f32_16x16x32_bf16 v[140:143], a[56:59], a[144:147], v[140:143]\n"
"ds_read_b128 a[144:147], v4 offset:12288\n"
"v_mfma_f32_16x16x32_bf16 a[12:15], a[56:59], a[152:155], a[12:15]\n"
"buffer_load_dwordx4 v1, s[48:51], s69 offen offset:2048 sc1 lds\n"
"v_mfma_f32_16x16x32_bf16 a[12:15], a[60:63], a[156:159], a[12:15]\n"
"v_mfma_f32_16x16x32_bf16 a[8:11], a[52:55], a[156:159], a[8:11]\n"
"ds_read_b128 a[156:159], v4 offset:15360\n"
"v_mfma_f32_16x16x32_bf16 a[8:11], a[48:51], a[152:155], a[8:11]\n"
"s_waitcnt vmcnt(18) lgkmcnt(11)\n"
"ds_read_b128 a[152:155], v4 offset:14336\n"
"v_mfma_f32_16x16x32_bf16 v[32:35], a[64:67], a[96:99], v[32:35]\n"
"s_add_u32 s34, s36, 1495040\n"
"v_mfma_f32_16x16x32_bf16 v[32:35], a[68:71], a[100:103], v[32:35]\n"
"global_load_dwordx4 a[48:51], v0, s[34:35] offset:0 sc1\n"
"v_mfma_f32_16x16x32_bf16 v[60:63], a[76:79], a[100:103], v[60:63]\n"
"v_mfma_f32_16x16x32_bf16 v[60:63], a[72:75], a[96:99], v[60:63]\n"
"v_mfma_f32_16x16x32_bf16 v[64:67], a[72:75], a[104:107], v[64:67]\n"
"buffer_load_dwordx4 v1, s[48:51], s69 offen offset:3072 sc1 lds\n"
"v_mfma_f32_16x16x32_bf16 v[64:67], a[76:79], a[108:111], v[64:67]\n"
"v_mfma_f32_16x16x32_bf16 v[36:39], a[68:71], a[108:111], v[36:39]\n"
"v_mfma_f32_16x16x32_bf16 v[36:39], a[64:67], a[104:107], v[36:39]\n"
"s_waitcnt lgkmcnt(8)\n"
"v_mfma_f32_16x16x32_bf16 v[40:43], a[64:67], a[112:115], v[40:43]\n"
"v_mfma_f32_16x16x32_bf16 v[40:43], a[68:71], a[116:119], v[40:43]\n"
"global_load_dwordx4 a[52:55], v0, s[34:35] offset:1024 sc1\n"
"v_mfma_f32_16x16x32_bf16 v[68:71], a[76:79], a[116:119], v[68:71]\n"
"v_mfma_f32_16x16x32_bf16 v[68:71], a[72:75], a[112:115], v[68:71]\n"
"v_mfma_f32_16x16x32_bf16 v[72:75], a[72:75], a[120:123], v[72:75]\n"
"v_mfma_f32_16x16x32_bf16 v[72:75], a[76:79], a[124:127], v[72:75]\n"
"v_mfma_f32_16x16x32_bf16 v[44:47], a[68:71], a[124:127], v[44:47]\n"
"v_mfma_f32_16x16x32_bf16 v[44:47], a[64:67], a[120:123], v[44:47]\n"
"s_waitcnt lgkmcnt(4)\n"
"v_mfma_f32_16x16x32_bf16 v[48:51], a[64:67], a[128:131], v[48:51]\n"
"v_mfma_f32_16x16x32_bf16 v[48:51], a[68:71], a[132:135], v[48:51]\n"
"global_load_dwordx4 a[56:59], v0, s[34:35] offset:2048 sc1\n"
"v_mfma_f32_16x16x32_bf16 v[76:79], a[76:79], a[132:135], v[76:79]\n"
"v_mfma_f32_16x16x32_bf16 v[76:79], a[72:75], a[128:131], v[76:79]\n"
"v_mfma_f32_16x16x32_bf16 v[80:83], a[72:75], a[136:139], v[80:83]\n"
"v_mfma_f32_16x16x32_bf16 v[80:83], a[76:79], a[140:143], v[80:83]\n"
"v_mfma_f32_16x16x32_bf16 v[52:55], a[68:71], a[140:143], v[52:55]\n"
"v_mfma_f32_16x16x32_bf16 v[52:55], a[64:67], a[136:139], v[52:55]\n"
"s_waitcnt lgkmcnt(0)\n"
"v_mfma_f32_16x16x32_bf16 v[56:59], a[64:67], a[144:147], v[56:59]\n"
"v_mfma_f32_16x16x32_bf16 v[56:59], a[68:71], a[148:151], v[56:59]\n"
"global_load_dwordx4 a[60:63], v0, s[34:35] offset:3072 sc1\n"
"v_mfma_f32_16x16x32_bf16 v[84:87], a[76:79], a[148:151], v[84:87]\n"
"v_mfma_f32_16x16x32_bf16 v[84:87], a[72:75], a[144:147], v[84:87]\n"
"v_mfma_f32_16x16x32_bf16 a[4:7], a[72:75], a[152:155], a[4:7]\n"
"v_mfma_f32_16x16x32_bf16 a[4:7], a[76:79], a[156:159], a[4:7]\n"
"v_mfma_f32_16x16x32_bf16 a[0:3], a[68:71], a[156:159], a[0:3]\n"
"v_mfma_f32_16x16x32_bf16 a[0:3], a[64:67], a[152:155], a[0:3]\n"
"s_waitcnt vmcnt(14)\n"
"v_mfma_f32_16x16x32_bf16 v[88:91], a[80:83], a[96:99], v[88:91]\n"
"s_add_u32 s34, s36, 1507328\n"
"v_mfma_f32_16x16x32_bf16 v[88:91], a[84:87], a[100:103], v[88:91]\n"
"global_load_dwordx4 a[64:67], v0, s[34:35] offset:0 sc1\n"
"v_mfma_f32_16x16x32_bf16 v[116:119], a[92:95], a[100:103], v[116:119]\n"
"ds_read_b128 a[100:103], v5 offset:1024\n"
"v_mfma_f32_16x16x32_bf16 v[116:119], a[88:91], a[96:99], v[116:119]\n"
"ds_read_b128 a[96:99], v5 offset:0\n"
"v_mfma_f32_16x16x32_bf16 v[120:123], a[88:91], a[104:107], v[120:123]\n"
"v_mfma_f32_16x16x32_bf16 v[120:123], a[92:95], a[108:111], v[120:123]\n"
"v_mfma_f32_16x16x32_bf16 v[92:95], a[84:87], a[108:111], v[92:95]\n"
"ds_read_b128 a[108:111], v5 offset:3072\n"
"v_mfma_f32_16x16x32_bf16 v[92:95], a[80:83], a[104:107], v[92:95]\n"
"ds_read_b128 a[104:107], v5 offset:2048\n"
"v_mfma_f32_16x16x32_bf16 v[96:99], a[80:83], a[112:115], v[96:99]\n"
"v_mfma_f32_16x16x32_bf16 v[96:99], a[84:87], a[116:119], v[96:99]\n"
"global_load_dwordx4 a[68:71], v0, s[34:35] offset:1024 sc1\n"
"v_mfma_f32_16x16x32_bf16 v[124:127], a[92:95], a[116:119], v[124:127]\n"
"ds_read_b128 a[116:119], v5 offset:5120\n"
"v_mfma_f32_16x16x32_bf16 v[124:127], a[88:91], a[112:115], v[124:127]\n"
"ds_read_b128 a[112:115], v5 offset:4096\n"
"v_mfma_f32_16x16x32_bf16 v[128:131], a[88:91], a[120:123], v[128:131]\n"
"v_mfma_f32_16x16x32_bf16 v[128:131], a[92:95], a[124:127], v[128:131]\n"
"v_mfma_f32_16x16x32_bf16 v[100:103], a[84:87], a[124:127], v[100:103]\n"
"ds_read_b128 a[124:127], v5 offset:7168\n"
"v_mfma_f32_16x16x32_bf16 v[100:103], a[80:83], a[120:123], v[100:103]\n"
"ds_read_b128 a[120:123], v5 offset:6144\n"
"v_mfma_f32_16x16x32_bf16 v[104:107], a[80:83], a[128:131], v[104:107]\n"
"v_mfma_f32_16x16x32_bf16 v[104:107], a[84:87], a[132:135], v[104:107]\n"
"global_load_dwordx4 a[72:75], v0, s[34:35] offset:2048 sc1\n"
"v_mfma_f32_16x16x32_bf16 v[132:135], a[92:95], a[132:135], v[132:135]\n"
"ds_read_b128 a[132:135], v5 offset:9216\n"
"v_mfma_f32_16x16x32_bf16 v[132:135], a[88:91], a[128:131], v[132:135]\n"
"ds_read_b128 a[128:131], v5 offset:8192\n"
"v_mfma_f32_16x16x32_bf16 v[136:139], a[88:91], a[136:139], v[136:139]\n"
"v_mfma_f32_16x16x32_bf16 v[136:139], a[92:95], a[140:143], v[136:139]\n"
"v_mfma_f32_16x16x32_bf16 v[108:111], a[84:87], a[140:143], v[108:111]\n"
"ds_read_b128 a[140:143], v5 offset:11264\n"
"v_mfma_f32_16x16x32_bf16 v[108:111], a[80:83], a[136:139], v[108:111]\n"
"ds_read_b128 a[136:139], v5 offset:10240\n"
"v_mfma_f32_16x16x32_bf16 v[112:115], a[80:83], a[144:147], v[112:115]\n"
"v_mfma_f32_16x16x32_bf16 v[112:115], a[84:87], a[148:151], v[112:115]\n"
"global_load_dwordx4 a[76:79], v0, s[34:35] offset:3072 sc1\n"
"v_mfma_f32_16x16x32_bf16 v[140:143], a[92:95], a[148:151], v[140:143]\n"
"ds_read_b128 a[148:151], v5 offset:13312\n"
"v_mfma_f32_16x16x32_bf16 v[140:143], a[88:91], a[144:147], v[140:143]\n"
"ds_read_b128 a[144:147], v5 offset:12288\n"
"v_mfma_f32_16x16x32_bf16 a[12:15], a[88:91], a[152:155], a[12:15]\n"
"v_mfma_f32_16x16x32_bf16 a[12:15], a[92:95], a[156:159], a[12:15]\n"
"v_mfma_f32_16x16x32_bf16 a[8:11], a[84:87], a[156:159], a[8:11]\n"
"ds_read_b128 a[156:159], v5 offset:15360\n"
"v_mfma_f32_16x16x32_bf16 a[8:11], a[80:83], a[152:155], a[8:11]\n"
"s_waitcnt vmcnt(10) lgkmcnt(11)\n"
"ds_read_b128 a[152:155], v5 offset:14336\n"
"v_mfma_f32_16x16x32_bf16 v[32:35], a[32:35], a[96:99], v[32:35]\n"
"s_add_u32 s34, s36, 1511424\n"
"v_mfma_f32_16x16x32_bf16 v[32:35], a[36:39], a[100:103], v[32:35]\n"
"global_load_dwordx4 a[80:83], v0, s[34:35] offset:0 sc1\n"
"v_mfma_f32_16x16x32_bf16 v[60:63], a[44:47], a[100:103], v[60:63]\n"
"v_mfma_f32_16x16x32_bf16 v[60:63], a[40:43], a[96:99], v[60:63]\n"
"v_mfma_f32_16x16x32_bf16 v[64:67], a[40:43], a[104:107], v[64:67]\n"
"v_mfma_f32_16x16x32_bf16 v[64:67], a[44:47], a[108:111], v[64:67]\n"
"v_mfma_f32_16x16x32_bf16 v[36:39], a[36:39], a[108:111], v[36:39]\n"
"v_mfma_f32_16x16x32_bf16 v[36:39], a[32:35], a[104:107], v[36:39]\n"
"s_waitcnt lgkmcnt(8)\n"
"v_mfma_f32_16x16x32_bf16 v[40:43], a[32:35], a[112:115], v[40:43]\n"
"v_mfma_f32_16x16x32_bf16 v[40:43], a[36:39], a[116:119], v[40:43]\n"
"global_load_dwordx4 a[84:87], v0, s[34:35] offset:1024 sc1\n"
"v_mfma_f32_16x16x32_bf16 v[68:71], a[44:47], a[116:119], v[68:71]\n"
"v_mfma_f32_16x16x32_bf16 v[68:71], a[40:43], a[112:115], v[68:71]\n"
"v_mfma_f32_16x16x32_bf16 v[72:75], a[40:43], a[120:123], v[72:75]\n"
"v_mfma_f32_16x16x32_bf16 v[72:75], a[44:47], a[124:127], v[72:75]\n"
"v_mfma_f32_16x16x32_bf16 v[44:47], a[36:39], a[124:127], v[44:47]\n"
"v_mfma_f32_16x16x32_bf16 v[44:47], a[32:35], a[120:123], v[44:47]\n"
"s_waitcnt lgkmcnt(4)\n"
"v_mfma_f32_16x16x32_bf16 v[48:51], a[32:35], a[128:131], v[48:51]\n"
"v_mfma_f32_16x16x32_bf16 v[48:51], a[36:39], a[132:135], v[48:51]\n"
"global_load_dwordx4 a[88:91], v0, s[34:35] offset:2048 sc1\n"
"v_mfma_f32_16x16x32_bf16 v[76:79], a[44:47], a[132:135], v[76:79]\n"
"v_mfma_f32_16x16x32_bf16 v[76:79], a[40:43], a[128:131], v[76:79]\n"
"v_mfma_f32_16x16x32_bf16 v[80:83], a[40:43], a[136:139], v[80:83]\n"
"v_mfma_f32_16x16x32_bf16 v[80:83], a[44:47], a[140:143], v[80:83]\n"
"v_mfma_f32_16x16x32_bf16 v[52:55], a[36:39], a[140:143], v[52:55]\n"
"v_mfma_f32_16x16x32_bf16 v[52:55], a[32:35], a[136:139], v[52:55]\n"
"s_waitcnt lgkmcnt(0)\n"
"v_mfma_f32_16x16x32_bf16 v[56:59], a[32:35], a[144:147], v[56:59]\n"
"v_mfma_f32_16x16x32_bf16 v[56:59], a[36:39], a[148:151], v[56:59]\n"
"global_load_dwordx4 a[92:95], v0, s[34:35] offset:3072 sc1\n"
"v_mfma_f32_16x16x32_bf16 v[84:87], a[44:47], a[148:151], v[84:87]\n"
"v_mfma_f32_16x16x32_bf16 v[84:87], a[40:43], a[144:147], v[84:87]\n"
"v_mfma_f32_16x16x32_bf16 a[4:7], a[40:43], a[152:155], a[4:7]\n"
"v_mfma_f32_16x16x32_bf16 a[4:7], a[44:47], a[156:159], a[4:7]\n"
"v_mfma_f32_16x16x32_bf16 a[0:3], a[36:39], a[156:159], a[0:3]\n"
"v_mfma_f32_16x16x32_bf16 a[0:3], a[32:35], a[152:155], a[0:3]\n"
"s_waitcnt vmcnt(8)\n"
"v_mfma_f32_16x16x32_bf16 v[88:91], a[48:51], a[96:99], v[88:91]\n"
"s_add_u32 s34, s36, 1515520\n"
"v_mfma_f32_16x16x32_bf16 v[88:91], a[52:55], a[100:103], v[88:91]\n"
"global_load_dwordx4 a[32:35], v0, s[34:35] offset:0 sc1\n"
"v_mfma_f32_16x16x32_bf16 v[116:119], a[60:63], a[100:103], v[116:119]\n"
"ds_read_b128 a[100:103], v2 offset:17408\n"
"v_mfma_f32_16x16x32_bf16 v[116:119], a[56:59], a[96:99], v[116:119]\n"
"ds_read_b128 a[96:99], v2 offset:16384\n"
"v_mfma_f32_16x16x32_bf16 v[120:123], a[56:59], a[104:107], v[120:123]\n"
"v_mfma_f32_16x16x32_bf16 v[120:123], a[60:63], a[108:111], v[120:123]\n"
"v_mfma_f32_16x16x32_bf16 v[92:95], a[52:55], a[108:111], v[92:95]\n"
"ds_read_b128 a[108:111], v2 offset:19456\n"
"v_mfma_f32_16x16x32_bf16 v[92:95], a[48:51], a[104:107], v[92:95]\n"
"ds_read_b128 a[104:107], v2 offset:18432\n"
"v_mfma_f32_16x16x32_bf16 v[96:99], a[48:51], a[112:115], v[96:99]\n"
"v_mfma_f32_16x16x32_bf16 v[96:99], a[52:55], a[116:119], v[96:99]\n"
"global_load_dwordx4 a[36:39], v0, s[34:35] offset:1024 sc1\n"
"v_mfma_f32_16x16x32_bf16 v[124:127], a[60:63], a[116:119], v[124:127]\n"
"ds_read_b128 a[116:119], v2 offset:21504\n"
"v_mfma_f32_16x16x32_bf16 v[124:127], a[56:59], a[112:115], v[124:127]\n"
"ds_read_b128 a[112:115], v2 offset:20480\n"
"v_mfma_f32_16x16x32_bf16 v[128:131], a[56:59], a[120:123], v[128:131]\n"
"v_mfma_f32_16x16x32_bf16 v[128:131], a[60:63], a[124:127], v[128:131]\n"
"v_mfma_f32_16x16x32_bf16 v[100:103], a[52:55], a[124:127], v[100:103]\n"
"ds_read_b128 a[124:127], v2 offset:23552\n"
"v_mfma_f32_16x16x32_bf16 v[100:103], a[48:51], a[120:123], v[100:103]\n"
"ds_read_b128 a[120:123], v2 offset:22528\n"
"v_mfma_f32_16x16x32_bf16 v[104:107], a[48:51], a[128:131], v[104:107]\n"
"v_mfma_f32_16x16x32_bf16 v[104:107], a[52:55], a[132:135], v[104:107]\n"
"global_load_dwordx4 a[40:43], v0, s[34:35] offset:2048 sc1\n"
"v_mfma_f32_16x16x32_bf16 v[132:135], a[60:63], a[132:135], v[132:135]\n"
"ds_read_b128 a[132:135], v2 offset:25600\n"
"v_mfma_f32_16x16x32_bf16 v[132:135], a[56:59], a[128:131], v[132:135]\n"
"ds_read_b128 a[128:131], v2 offset:24576\n"
"v_mfma_f32_16x16x32_bf16 v[136:139], a[56:59], a[136:139], v[136:139]\n"
"v_mfma_f32_16x16x32_bf16 v[136:139], a[60:63], a[140:143], v[136:139]\n"
"v_mfma_f32_16x16x32_bf16 v[108:111], a[52:55], a[140:143], v[108:111]\n"
"ds_read_b128 a[140:143], v2 offset:27648\n"
"v_mfma_f32_16x16x32_bf16 v[108:111], a[48:51], a[136:139], v[108:111]\n"
"ds_read_b128 a[136:139], v2 offset:26624\n"
"v_mfma_f32_16x16x32_bf16 v[112:115], a[48:51], a[144:147], v[112:115]\n"
"v_mfma_f32_16x16x32_bf16 v[112:115], a[52:55], a[148:151], v[112:115]\n"
"global_load_dwordx4 a[44:47], v0, s[34:35] offset:3072 sc1\n"
"v_mfma_f32_16x16x32_bf16 v[140:143], a[60:63], a[148:151], v[140:143]\n"
"ds_read_b128 a[148:151], v2 offset:29696\n"
"v_mfma_f32_16x16x32_bf16 v[140:143], a[56:59], a[144:147], v[140:143]\n"
"ds_read_b128 a[144:147], v2 offset:28672\n"
"v_mfma_f32_16x16x32_bf16 a[12:15], a[56:59], a[152:155], a[12:15]\n"
"v_mfma_f32_16x16x32_bf16 a[12:15], a[60:63], a[156:159], a[12:15]\n"
"v_mfma_f32_16x16x32_bf16 a[8:11], a[52:55], a[156:159], a[8:11]\n"
"ds_read_b128 a[156:159], v2 offset:31744\n"
"v_mfma_f32_16x16x32_bf16 a[8:11], a[48:51], a[152:155], a[8:11]\n"
"s_waitcnt vmcnt(8) lgkmcnt(11)\n"
"ds_read_b128 a[152:155], v2 offset:30720\n"
"v_mfma_f32_16x16x32_bf16 v[32:35], a[64:67], a[96:99], v[32:35]\n"
"s_add_u32 s34, s36, 1519616\n"
"v_mfma_f32_16x16x32_bf16 v[32:35], a[68:71], a[100:103], v[32:35]\n"
"global_load_dwordx4 a[48:51], v0, s[34:35] offset:0 sc1\n"
"v_mfma_f32_16x16x32_bf16 v[60:63], a[76:79], a[100:103], v[60:63]\n"
"s_barrier\n"
"v_mfma_f32_16x16x32_bf16 v[60:63], a[72:75], a[96:99], v[60:63]\n"
"v_mfma_f32_16x16x32_bf16 v[64:67], a[72:75], a[104:107], v[64:67]\n"
"v_mfma_f32_16x16x32_bf16 v[64:67], a[76:79], a[108:111], v[64:67]\n"
"v_mfma_f32_16x16x32_bf16 v[36:39], a[68:71], a[108:111], v[36:39]\n"
"v_mfma_f32_16x16x32_bf16 v[36:39], a[64:67], a[104:107], v[36:39]\n"
"s_waitcnt lgkmcnt(8)\n"
"v_mfma_f32_16x16x32_bf16 v[40:43], a[64:67], a[112:115], v[40:43]\n"
"v_mfma_f32_16x16x32_bf16 v[40:43], a[68:71], a[116:119], v[40:43]\n"
"global_load_dwordx4 a[52:55], v0, s[34:35] offset:1024 sc1\n"
"v_mfma_f32_16x16x32_bf16 v[68:71], a[76:79], a[116:119], v[68:71]\n"
"v_mfma_f32_16x16x32_bf16 v[68:71], a[72:75], a[112:115], v[68:71]\n"
"v_mfma_f32_16x16x32_bf16 v[72:75], a[72:75], a[120:123], v[72:75]\n"
"v_mfma_f32_16x16x32_bf16 v[72:75], a[76:79], a[124:127], v[72:75]\n"
"v_mfma_f32_16x16x32_bf16 v[44:47], a[68:71], a[124:127], v[44:47]\n"
"v_mfma_f32_16x16x32_bf16 v[44:47], a[64:67], a[120:123], v[44:47]\n"
"s_waitcnt lgkmcnt(4)\n"
"v_mfma_f32_16x16x32_bf16 v[48:51], a[64:67], a[128:131], v[48:51]\n"
"v_mfma_f32_16x16x32_bf16 v[48:51], a[68:71], a[132:135], v[48:51]\n"
"global_load_dwordx4 a[56:59], v0, s[34:35] offset:2048 sc1\n"
"v_mfma_f32_16x16x32_bf16 v[76:79], a[76:79], a[132:135], v[76:79]\n"
"v_mfma_f32_16x16x32_bf16 v[76:79], a[72:75], a[128:131], v[76:79]\n"
"v_mfma_f32_16x16x32_bf16 v[80:83], a[72:75], a[136:139], v[80:83]\n"
"v_lshlrev_b32 v16, 16, v18\n"
"v_mfma_f32_16x16x32_bf16 v[80:83], a[76:79], a[140:143], v[80:83]\n"
"v_and_b32 v17, s63, v18\n"
"v_mfma_f32_16x16x32_bf16 v[52:55], a[68:71], a[140:143], v[52:55]\n"
"v_lshlrev_b32 v18, 16, v19\n"
"v_mfma_f32_16x16x32_bf16 v[52:55], a[64:67], a[136:139], v[52:55]\n"
"s_waitcnt lgkmcnt(0)\n"
"v_and_b32 v19, s63, v19\n"
"v_add_f32 v144, v144, v16\n"
"v_mfma_f32_16x16x32_bf16 v[56:59], a[64:67], a[144:147], v[56:59]\n"
"v_add_f32 v145, v145, v17\n"
"v_mfma_f32_16x16x32_bf16 v[56:59], a[68:71], a[148:151], v[56:59]\n"
"global_load_dwordx4 a[60:63], v0, s[34:35] offset:3072 sc1\n"
"v_add_f32 v146, v146, v18\n"
"v_mfma_f32_16x16x32_bf16 v[84:87], a[76:79], a[148:151], v[84:87]\n"
"v_add_f32 v147, v147, v19\n"
"v_mfma_f32_16x16x32_bf16 v[84:87], a[72:75], a[144:147], v[84:87]\n"
"v_add_f32 v148, v148, v16\n"
"v_mfma_f32_16x16x32_bf16 a[4:7], a[72:75], a[152:155], a[4:7]\n"
"v_add_f32 v149, v149, v17\n"
"v_mfma_f32_16x16x32_bf16 a[4:7], a[76:79], a[156:159], a[4:7]\n"
"v_add_f32 v150, v150, v18\n"
"v_mfma_f32_16x16x32_bf16 a[0:3], a[68:71], a[156:159], a[0:3]\n"
"v_add_f32 v151, v151, v19\n"
"v_mfma_f32_16x16x32_bf16 a[0:3], a[64:67], a[152:155], a[0:3]\n"
"s_waitcnt vmcnt(8)\n"
"v_add_f32 v152, v152, v16\n"
"v_add_f32 v153, v153, v17\n"
"v_mfma_f32_16x16x32_bf16 v[88:91], a[80:83], a[96:99], v[88:91]\n"
"s_add_u32 s34, s36, 1523712\n"
"v_add_f32 v154, v154, v18\n"
"v_mfma_f32_16x16x32_bf16 v[88:91], a[84:87], a[100:103], v[88:91]\n"
"global_load_dwordx4 a[64:67], v0, s[34:35] offset:0 sc1\n"
"v_add_f32 v155, v155, v19\n"
"v_mfma_f32_16x16x32_bf16 v[116:119], a[92:95], a[100:103], v[116:119]\n"
"ds_read_b128 a[100:103], v3 offset:17408\n"
"v_add_f32 v156, v156, v16\n"
"v_mfma_f32_16x16x32_bf16 v[116:119], a[88:91], a[96:99], v[116:119]\n"
"ds_read_b128 a[96:99], v3 offset:16384\n"
"v_add_f32 v157, v157, v17\n"
"v_mfma_f32_16x16x32_bf16 v[120:123], a[88:91], a[104:107], v[120:123]\n"
"v_add_f32 v158, v158, v18\n"
"v_mfma_f32_16x16x32_bf16 v[120:123], a[92:95], a[108:111], v[120:123]\n"
"v_add_f32 v159, v159, v19\n"
"v_add_f32 v160, v160, v16\n"
"v_mfma_f32_16x16x32_bf16 v[92:95], a[84:87], a[108:111], v[92:95]\n"
"ds_read_b128 a[108:111], v3 offset:19456\n"
"v_add_f32 v161, v161, v17\n"
"v_mfma_f32_16x16x32_bf16 v[92:95], a[80:83], a[104:107], v[92:95]\n"
"ds_read_b128 a[104:107], v3 offset:18432\n"
"v_add_f32 v162, v162, v18\n"
"v_mfma_f32_16x16x32_bf16 v[96:99], a[80:83], a[112:115], v[96:99]\n"
"v_add_f32 v163, v163, v19\n"
"v_mfma_f32_16x16x32_bf16 v[96:99], a[84:87], a[116:119], v[96:99]\n"
"global_load_dwordx4 a[68:71], v0, s[34:35] offset:1024 sc1\n"
"v_add_f32 v164, v164, v16\n"
"v_mfma_f32_16x16x32_bf16 v[124:127], a[92:95], a[116:119], v[124:127]\n"
"ds_read_b128 a[116:119], v3 offset:21504\n"
"v_add_f32 v165, v165, v17\n"
"v_mfma_f32_16x16x32_bf16 v[124:127], a[88:91], a[112:115], v[124:127]\n"
"ds_read_b128 a[112:115], v3 offset:20480\n"
"v_add_f32 v166, v166, v18\n"
"v_mfma_f32_16x16x32_bf16 v[128:131], a[88:91], a[120:123], v[128:131]\n"
"v_add_f32 v167, v167, v19\n"
"v_mfma_f32_16x16x32_bf16 v[128:131], a[92:95], a[124:127], v[128:131]\n"
"v_add_f32 v168, v168, v16\n"
"v_add_f32 v169, v169, v17\n"
"v_mfma_f32_16x16x32_bf16 v[100:103], a[84:87], a[124:127], v[100:103]\n"
"ds_read_b128 a[124:127], v3 offset:23552\n"
"v_add_f32 v170, v170, v18\n"
"v_mfma_f32_16x16x32_bf16 v[100:103], a[80:83], a[120:123], v[100:103]\n"
"ds_read_b128 a[120:123], v3 offset:22528\n"
"v_add_f32 v171, v171, v19\n"
"v_mfma_f32_16x16x32_bf16 v[104:107], a[80:83], a[128:131], v[104:107]\n"
"v_accvgpr_read_b32 v12, a16\n"
"v_mfma_f32_16x16x32_bf16 v[104:107], a[84:87], a[132:135], v[104:107]\n"
"global_load_dwordx4 a[72:75], v0, s[34:35] offset:2048 sc1\n"
"v_accvgpr_read_b32 v13, a17\n"
"v_mfma_f32_16x16x32_bf16 v[132:135], a[92:95], a[132:135], v[132:135]\n"
"ds_read_b128 a[132:135], v3 offset:25600\n"
"v_accvgpr_read_b32 v14, a18\n"
"v_mfma_f32_16x16x32_bf16 v[132:135], a[88:91], a[128:131], v[132:135]\n"
"ds_read_b128 a[128:131], v3 offset:24576\n"
"v_accvgpr_read_b32 v15, a19\n"
"v_mfma_f32_16x16x32_bf16 v[136:139], a[88:91], a[136:139], v[136:139]\n"
"v_add_f32 v12, v12, v16\n"
"v_mfma_f32_16x16x32_bf16 v[136:139], a[92:95], a[140:143], v[136:139]\n"
"v_add_f32 v13, v13, v17\n"
"v_add_f32 v14, v14, v18\n"
"v_mfma_f32_16x16x32_bf16 v[108:111], a[84:87], a[140:143], v[108:111]\n"
"ds_read_b128 a[140:143], v3 offset:27648\n"
"v_add_f32 v15, v15, v19\n"
"v_mfma_f32_16x16x32_bf16 v[108:111], a[80:83], a[136:139], v[108:111]\n"
"ds_read_b128 a[136:139], v3 offset:26624\n"
"v_accvgpr_write_b32 a16, v12\n"
"v_mfma_f32_16x16x32_bf16 v[112:115], a[80:83], a[144:147], v[112:115]\n"
"v_accvgpr_write_b32 a17, v13\n"
"v_mfma_f32_16x16x32_bf16 v[112:115], a[84:87], a[148:151], v[112:115]\n"
"global_load_dwordx4 a[76:79], v0, s[34:35] offset:3072 sc1\n"
"v_accvgpr_write_b32 a18, v14\n"
"v_mfma_f32_16x16x32_bf16 v[140:143], a[92:95], a[148:151], v[140:143]\n"
"ds_read_b128 a[148:151], v3 offset:29696\n"
"v_accvgpr_write_b32 a19, v15\n"
"v_mfma_f32_16x16x32_bf16 v[140:143], a[88:91], a[144:147], v[140:143]\n"
"ds_read_b128 a[144:147], v3 offset:28672\n"
"v_lshlrev_b32 v20, 16, v22\n"
"v_mfma_f32_16x16x32_bf16 a[12:15], a[88:91], a[152:155], a[12:15]\n"
"v_and_b32 v21, s63, v22\n"
"v_lshlrev_b32 v22, 16, v23\n"
"v_mfma_f32_16x16x32_bf16 a[12:15], a[92:95], a[156:159], a[12:15]\n"
"v_and_b32 v23, s63, v23\n"
"v_mfma_f32_16x16x32_bf16 a[8:11], a[84:87], a[156:159], a[8:11]\n"
"ds_read_b128 a[156:159], v3 offset:31744\n"
"v_add_f32 v172, v172, v20\n"
"v_mfma_f32_16x16x32_bf16 a[8:11], a[80:83], a[152:155], a[8:11]\n"
"s_waitcnt vmcnt(8) lgkmcnt(11)\n"
"ds_read_b128 a[152:155], v3 offset:30720\n"
"v_add_f32 v173, v173, v21\n"
"v_mfma_f32_16x16x32_bf16 v[32:35], a[32:35], a[96:99], v[32:35]\n"
"s_add_u32 s34, s36, 1527808\n"
"v_add_f32 v174, v174, v22\n"
"v_mfma_f32_16x16x32_bf16 v[32:35], a[36:39], a[100:103], v[32:35]\n"
"global_load_dwordx4 a[80:83], v0, s[34:35] offset:0 sc1\n"
"v_add_f32 v175, v175, v23\n"
"v_mfma_f32_16x16x32_bf16 v[60:63], a[44:47], a[100:103], v[60:63]\n"
"v_add_f32 v176, v176, v20\n"
"v_mfma_f32_16x16x32_bf16 v[60:63], a[40:43], a[96:99], v[60:63]\n"
"v_add_f32 v177, v177, v21\n"
"v_add_f32 v178, v178, v22\n"
"v_mfma_f32_16x16x32_bf16 v[64:67], a[40:43], a[104:107], v[64:67]\n"
"v_add_f32 v179, v179, v23\n"
"v_mfma_f32_16x16x32_bf16 v[64:67], a[44:47], a[108:111], v[64:67]\n"
"v_add_f32 v180, v180, v20\n"
"v_mfma_f32_16x16x32_bf16 v[36:39], a[36:39], a[108:111], v[36:39]\n"
"v_add_f32 v181, v181, v21\n"
"v_mfma_f32_16x16x32_bf16 v[36:39], a[32:35], a[104:107], v[36:39]\n"
"s_waitcnt lgkmcnt(8)\n"
"v_add_f32 v182, v182, v22\n"
"v_mfma_f32_16x16x32_bf16 v[40:43], a[32:35], a[112:115], v[40:43]\n"
"v_add_f32 v183, v183, v23\n"
"v_mfma_f32_16x16x32_bf16 v[40:43], a[36:39], a[116:119], v[40:43]\n"
"global_load_dwordx4 a[84:87], v0, s[34:35] offset:1024 sc1\n"
"v_add_f32 v184, v184, v20\n"
"v_mfma_f32_16x16x32_bf16 v[68:71], a[44:47], a[116:119], v[68:71]\n"
"v_add_f32 v185, v185, v21\n"
"v_mfma_f32_16x16x32_bf16 v[68:71], a[40:43], a[112:115], v[68:71]\n"
"v_add_f32 v186, v186, v22\n"
"v_add_f32 v187, v187, v23\n"
"v_mfma_f32_16x16x32_bf16 v[72:75], a[40:43], a[120:123], v[72:75]\n"
"v_add_f32 v188, v188, v20\n"
"v_mfma_f32_16x16x32_bf16 v[72:75], a[44:47], a[124:127], v[72:75]\n"
"v_add_f32 v189, v189, v21\n"
"v_mfma_f32_16x16x32_bf16 v[44:47], a[36:39], a[124:127], v[44:47]\n"
"v_add_f32 v190, v190, v22\n"
"v_mfma_f32_16x16x32_bf16 v[44:47], a[32:35], a[120:123], v[44:47]\n"
"s_waitcnt lgkmcnt(4)\n"
"v_add_f32 v191, v191, v23\n"
"v_mfma_f32_16x16x32_bf16 v[48:51], a[32:35], a[128:131], v[48:51]\n"
"v_add_f32 v192, v192, v20\n"
"v_mfma_f32_16x16x32_bf16 v[48:51], a[36:39], a[132:135], v[48:51]\n"
"global_load_dwordx4 a[88:91], v0, s[34:35] offset:2048 sc1\n"
"v_add_f32 v193, v193, v21\n"
"v_mfma_f32_16x16x32_bf16 v[76:79], a[44:47], a[132:135], v[76:79]\n"
"v_add_f32 v194, v194, v22\n"
"v_add_f32 v195, v195, v23\n"
"v_mfma_f32_16x16x32_bf16 v[76:79], a[40:43], a[128:131], v[76:79]\n"
"v_add_f32 v196, v196, v20\n"
"v_mfma_f32_16x16x32_bf16 v[80:83], a[40:43], a[136:139], v[80:83]\n"
"v_add_f32 v197, v197, v21\n"
"v_mfma_f32_16x16x32_bf16 v[80:83], a[44:47], a[140:143], v[80:83]\n"
"v_add_f32 v198, v198, v22\n"
"v_mfma_f32_16x16x32_bf16 v[52:55], a[36:39], a[140:143], v[52:55]\n"
"v_add_f32 v199, v199, v23\n"
"v_mfma_f32_16x16x32_bf16 v[52:55], a[32:35], a[136:139], v[52:55]\n"
"s_waitcnt lgkmcnt(0)\n"
"v_accvgpr_read_b32 v12, a20\n"
"v_mfma_f32_16x16x32_bf16 v[56:59], a[32:35], a[144:147], v[56:59]\n"
"v_accvgpr_read_b32 v13, a21\n"
"v_mfma_f32_16x16x32_bf16 v[56:59], a[36:39], a[148:151], v[56:59]\n"
"global_load_dwordx4 a[92:95], v0, s[34:35] offset:3072 sc1\n"
"v_accvgpr_read_b32 v14, a22\n"
"v_mfma_f32_16x16x32_bf16 v[84:87], a[44:47], a[148:151], v[84:87]\n"
"v_accvgpr_read_b32 v15, a23\n"
"v_add_f32 v12, v12, v20\n"
"v_mfma_f32_16x16x32_bf16 v[84:87], a[40:43], a[144:147], v[84:87]\n"
"v_add_f32 v13, v13, v21\n"
"v_mfma_f32_16x16x32_bf16 a[4:7], a[40:43], a[152:155], a[4:7]\n"
"v_add_f32 v14, v14, v22\n"
"v_mfma_f32_16x16x32_bf16 a[4:7], a[44:47], a[156:159], a[4:7]\n"
"v_add_f32 v15, v15, v23\n"
"v_mfma_f32_16x16x32_bf16 a[0:3], a[36:39], a[156:159], a[0:3]\n"
"v_accvgpr_write_b32 a20, v12\n"
"v_mfma_f32_16x16x32_bf16 a[0:3], a[32:35], a[152:155], a[0:3]\n"
"s_waitcnt vmcnt(8)\n"
"v_accvgpr_write_b32 a21, v13\n"
"v_mfma_f32_16x16x32_bf16 v[88:91], a[48:51], a[96:99], v[88:91]\n"
"s_add_u32 s34, s36, 1531904\n"
"v_accvgpr_write_b32 a22, v14\n"
"v_accvgpr_write_b32 a23, v15\n"
"v_mfma_f32_16x16x32_bf16 v[88:91], a[52:55], a[100:103], v[88:91]\n"
"global_load_dwordx4 a[32:35], v0, s[34:35] offset:0 sc1\n"
"v_lshlrev_b32 v24, 16, v26\n"
"v_mfma_f32_16x16x32_bf16 v[116:119], a[60:63], a[100:103], v[116:119]\n"
"ds_read_b128 a[100:103], v4 offset:17408\n"
"v_and_b32 v25, s63, v26\n"
"v_mfma_f32_16x16x32_bf16 v[116:119], a[56:59], a[96:99], v[116:119]\n"
"ds_read_b128 a[96:99], v4 offset:16384\n"
"v_lshlrev_b32 v26, 16, v27\n"
"v_mfma_f32_16x16x32_bf16 v[120:123], a[56:59], a[104:107], v[120:123]\n"
"v_and_b32 v27, s63, v27\n"
"v_mfma_f32_16x16x32_bf16 v[120:123], a[60:63], a[108:111], v[120:123]\n"
"v_add_f32 v200, v200, v24\n"
"v_mfma_f32_16x16x32_bf16 v[92:95], a[52:55], a[108:111], v[92:95]\n"
"ds_read_b128 a[108:111], v4 offset:19456\n"
"v_add_f32 v201, v201, v25\n"
"v_mfma_f32_16x16x32_bf16 v[92:95], a[48:51], a[104:107], v[92:95]\n"
"ds_read_b128 a[104:107], v4 offset:18432\n"
"v_add_f32 v202, v202, v26\n"
"v_mfma_f32_16x16x32_bf16 v[96:99], a[48:51], a[112:115], v[96:99]\n"
"v_add_f32 v203, v203, v27\n"
"v_add_f32 v204, v204, v24\n"
"v_mfma_f32_16x16x32_bf16 v[96:99], a[52:55], a[116:119], v[96:99]\n"
"global_load_dwordx4 a[36:39], v0, s[34:35] offset:1024 sc1\n"
"v_add_f32 v205, v205, v25\n"
"v_mfma_f32_16x16x32_bf16 v[124:127], a[60:63], a[116:119], v[124:127]\n"
"ds_read_b128 a[116:119], v4 offset:21504\n"
"v_add_f32 v206, v206, v26\n"
"v_mfma_f32_16x16x32_bf16 v[124:127], a[56:59], a[112:115], v[124:127]\n"
"ds_read_b128 a[112:115], v4 offset:20480\n"
"v_add_f32 v207, v207, v27\n"
"v_mfma_f32_16x16x32_bf16 v[128:131], a[56:59], a[120:123], v[128:131]\n"
"v_add_f32 v208, v208, v24\n"
"v_mfma_f32_16x16x32_bf16 v[128:131], a[60:63], a[124:127], v[128:131]\n"
"v_add_f32 v209, v209, v25\n"
"v_mfma_f32_16x16x32_bf16 v[100:103], a[52:55], a[124:127], v[100:103]\n"
"ds_read_b128 a[124:127], v4 offset:23552\n"
"v_add_f32 v210, v210, v26\n"
"v_mfma_f32_16x16x32_bf16 v[100:103], a[48:51], a[120:123], v[100:103]\n"
"ds_read_b128 a[120:123], v4 offset:22528\n"
"v_add_f32 v211, v211, v27\n"
"v_mfma_f32_16x16x32_bf16 v[104:107], a[48:51], a[128:131], v[104:107]\n"
"v_add_f32 v212, v212, v24\n"
"v_add_f32 v213, v213, v25\n"
"v_mfma_f32_16x16x32_bf16 v[104:107], a[52:55], a[132:135], v[104:107]\n"
"global_load_dwordx4 a[40:43], v0, s[34:35] offset:2048 sc1\n"
"v_add_f32 v214, v214, v26\n"
"v_mfma_f32_16x16x32_bf16 v[132:135], a[60:63], a[132:135], v[132:135]\n"
"ds_read_b128 a[132:135], v4 offset:25600\n"
"v_add_f32 v215, v215, v27\n"
"v_mfma_f32_16x16x32_bf16 v[132:135], a[56:59], a[128:131], v[132:135]\n"
"ds_read_b128 a[128:131], v4 offset:24576\n"
"v_add_f32 v216, v216, v24\n"
"v_mfma_f32_16x16x32_bf16 v[136:139], a[56:59], a[136:139], v[136:139]\n"
"v_add_f32 v217, v217, v25\n"
"v_mfma_f32_16x16x32_bf16 v[136:139], a[60:63], a[140:143], v[136:139]\n"
"v_add_f32 v218, v218, v26\n"
"v_mfma_f32_16x16x32_bf16 v[108:111], a[52:55], a[140:143], v[108:111]\n"
"ds_read_b128 a[140:143], v4 offset:27648\n"
"v_add_f32 v219, v219, v27\n"
"v_mfma_f32_16x16x32_bf16 v[108:111], a[48:51], a[136:139], v[108:111]\n"
"ds_read_b128 a[136:139], v4 offset:26624\n"
"v_add_f32 v220, v220, v24\n"
"v_mfma_f32_16x16x32_bf16 v[112:115], a[48:51], a[144:147], v[112:115]\n"
"v_add_f32 v221, v221, v25\n"
"v_add_f32 v222, v222, v26\n"
"v_mfma_f32_16x16x32_bf16 v[112:115], a[52:55], a[148:151], v[112:115]\n"
"global_load_dwordx4 a[44:47], v0, s[34:35] offset:3072 sc1\n"
"v_add_f32 v223, v223, v27\n"
"v_mfma_f32_16x16x32_bf16 v[140:143], a[60:63], a[148:151], v[140:143]\n"
"ds_read_b128 a[148:151], v4 offset:29696\n"
"v_add_f32 v224, v224, v24\n"
"v_mfma_f32_16x16x32_bf16 v[140:143], a[56:59], a[144:147], v[140:143]\n"
"ds_read_b128 a[144:147], v4 offset:28672\n"
"v_add_f32 v225, v225, v25\n"
"v_mfma_f32_16x16x32_bf16 a[12:15], a[56:59], a[152:155], a[12:15]\n"
"v_add_f32 v226, v226, v26\n"
"v_mfma_f32_16x16x32_bf16 a[12:15], a[60:63], a[156:159], a[12:15]\n"
"v_add_f32 v227, v227, v27\n"
"v_mfma_f32_16x16x32_bf16 a[8:11], a[52:55], a[156:159], a[8:11]\n"
"ds_read_b128 a[156:159], v4 offset:31744\n"
"v_accvgpr_read_b32 v12, a24\n"
"v_mfma_f32_16x16x32_bf16 a[8:11], a[48:51], a[152:155], a[8:11]\n"
"s_waitcnt vmcnt(8) lgkmcnt(11)\n"
"ds_read_b128 a[152:155], v4 offset:30720\n"
"v_accvgpr_read_b32 v13, a25\n"
"v_mfma_f32_16x16x32_bf16 v[32:35], a[64:67], a[96:99], v[32:35]\n"
"s_add_u32 s34, s36, 1536000\n"
"v_accvgpr_read_b32 v14, a26\n"
"v_accvgpr_read_b32 v15, a27\n"
"v_mfma_f32_16x16x32_bf16 v[32:35], a[68:71], a[100:103], v[32:35]\n"
"global_load_dwordx4 a[48:51], v0, s[34:35] offset:0 sc1\n"
"v_add_f32 v12, v12, v24\n"
"v_mfma_f32_16x16x32_bf16 v[60:63], a[76:79], a[100:103], v[60:63]\n"
"v_add_f32 v13, v13, v25\n"
"v_mfma_f32_16x16x32_bf16 v[60:63], a[72:75], a[96:99], v[60:63]\n"
"v_add_f32 v14, v14, v26\n"
"v_mfma_f32_16x16x32_bf16 v[64:67], a[72:75], a[104:107], v[64:67]\n"
"v_add_f32 v15, v15, v27\n"
"v_mfma_f32_16x16x32_bf16 v[64:67], a[76:79], a[108:111], v[64:67]\n"
"v_accvgpr_write_b32 a24, v12\n"
"v_mfma_f32_16x16x32_bf16 v[36:39], a[68:71], a[108:111], v[36:39]\n"
"v_accvgpr_write_b32 a25, v13\n"
"v_accvgpr_write_b32 a26, v14\n"
"v_mfma_f32_16x16x32_bf16 v[36:39], a[64:67], a[104:107], v[36:39]\n"
"s_waitcnt lgkmcnt(8)\n"
"v_accvgpr_write_b32 a27, v15\n"
"v_mfma_f32_16x16x32_bf16 v[40:43], a[64:67], a[112:115], v[40:43]\n"
"v_lshlrev_b32 v28, 16, v30\n"
"v_mfma_f32_16x16x32_bf16 v[40:43], a[68:71], a[116:119], v[40:43]\n"
"global_load_dwordx4 a[52:55], v0, s[34:35] offset:1024 sc1\n"
"v_and_b32 v29, s63, v30\n"
"v_mfma_f32_16x16x32_bf16 v[68:71], a[76:79], a[116:119], v[68:71]\n"
"v_lshlrev_b32 v30, 16, v31\n"
"v_mfma_f32_16x16x32_bf16 v[68:71], a[72:75], a[112:115], v[68:71]\n"
"v_and_b32 v31, s63, v31\n"
"v_mfma_f32_16x16x32_bf16 v[72:75], a[72:75], a[120:123], v[72:75]\n"
"v_add_f32 v228, v228, v28\n"
"v_mfma_f32_16x16x32_bf16 v[72:75], a[76:79], a[124:127], v[72:75]\n"
"v_add_f32 v229, v229, v29\n"
"v_add_f32 v230, v230, v30\n"
"v_mfma_f32_16x16x32_bf16 v[44:47], a[68:71], a[124:127], v[44:47]\n"
"v_add_f32 v231, v231, v31\n"
"v_mfma_f32_16x16x32_bf16 v[44:47], a[64:67], a[120:123], v[44:47]\n"
"s_waitcnt lgkmcnt(4)\n"
"v_add_f32 v232, v232, v28\n"
"v_mfma_f32_16x16x32_bf16 v[48:51], a[64:67], a[128:131], v[48:51]\n"
"v_add_f32 v233, v233, v29\n"
"v_mfma_f32_16x16x32_bf16 v[48:51], a[68:71], a[132:135], v[48:51]\n"
"global_load_dwordx4 a[56:59], v0, s[34:35] offset:2048 sc1\n"
"v_add_f32 v234, v234, v30\n"
"v_mfma_f32_16x16x32_bf16 v[76:79], a[76:79], a[132:135], v[76:79]\n"
"v_add_f32 v235, v235, v31\n"
"v_mfma_f32_16x16x32_bf16 v[76:79], a[72:75], a[128:131], v[76:79]\n"
"v_add_f32 v236, v236, v28\n"
"v_mfma_f32_16x16x32_bf16 v[80:83], a[72:75], a[136:139], v[80:83]\n"
"v_add_f32 v237, v237, v29\n"
"v_mfma_f32_16x16x32_bf16 v[80:83], a[76:79], a[140:143], v[80:83]\n"
"v_add_f32 v238, v238, v30\n"
"v_add_f32 v239, v239, v31\n"
"v_mfma_f32_16x16x32_bf16 v[52:55], a[68:71], a[140:143], v[52:55]\n"
"v_add_f32 v240, v240, v28\n"
"v_mfma_f32_16x16x32_bf16 v[52:55], a[64:67], a[136:139], v[52:55]\n"
"s_waitcnt lgkmcnt(0)\n"
"v_add_f32 v241, v241, v29\n"
"v_mfma_f32_16x16x32_bf16 v[56:59], a[64:67], a[144:147], v[56:59]\n"
"v_add_f32 v242, v242, v30\n"
"v_mfma_f32_16x16x32_bf16 v[56:59], a[68:71], a[148:151], v[56:59]\n"
"global_load_dwordx4 a[60:63], v0, s[34:35] offset:3072 sc1\n"
"v_add_f32 v243, v243, v31\n"
"v_mfma_f32_16x16x32_bf16 v[84:87], a[76:79], a[148:151], v[84:87]\n"
"v_add_f32 v244, v244, v28\n"
"v_mfma_f32_16x16x32_bf16 v[84:87], a[72:75], a[144:147], v[84:87]\n"
"v_add_f32 v245, v245, v29\n"
"v_mfma_f32_16x16x32_bf16 a[4:7], a[72:75], a[152:155], a[4:7]\n"
"v_add_f32 v246, v246, v30\n"
"v_add_f32 v247, v247, v31\n"
"v_mfma_f32_16x16x32_bf16 a[4:7], a[76:79], a[156:159], a[4:7]\n"
"v_add_f32 v248, v248, v28\n"
"v_mfma_f32_16x16x32_bf16 a[0:3], a[68:71], a[156:159], a[0:3]\n"
"v_add_f32 v249, v249, v29\n"
"v_mfma_f32_16x16x32_bf16 a[0:3], a[64:67], a[152:155], a[0:3]\n"
"s_waitcnt vmcnt(8)\n"
"v_add_f32 v250, v250, v30\n"
"v_mfma_f32_16x16x32_bf16 v[88:91], a[80:83], a[96:99], v[88:91]\n"
"s_add_u32 s34, s36, 1466368\n"
"v_add_f32 v251, v251, v31\n"
"v_mfma_f32_16x16x32_bf16 v[88:91], a[84:87], a[100:103], v[88:91]\n"
"global_load_dwordx4 a[64:67], v0, s[34:35] offset:0 sc1\n"
"v_add_f32 v252, v252, v28\n"
"v_mfma_f32_16x16x32_bf16 v[116:119], a[92:95], a[100:103], v[116:119]\n"
"ds_read_b128 a[100:103], v5 offset:17408\n"
"v_add_f32 v253, v253, v29\n"
"v_mfma_f32_16x16x32_bf16 v[116:119], a[88:91], a[96:99], v[116:119]\n"
"ds_read_b128 a[96:99], v5 offset:16384\n"
"v_add_f32 v254, v254, v30\n"
"v_mfma_f32_16x16x32_bf16 v[120:123], a[88:91], a[104:107], v[120:123]\n"
"v_add_f32 v255, v255, v31\n"
"v_accvgpr_read_b32 v12, a28\n"
"v_mfma_f32_16x16x32_bf16 v[120:123], a[92:95], a[108:111], v[120:123]\n"
"v_accvgpr_read_b32 v13, a29\n"
"v_mfma_f32_16x16x32_bf16 v[92:95], a[84:87], a[108:111], v[92:95]\n"
"ds_read_b128 a[108:111], v5 offset:19456\n"
"v_accvgpr_read_b32 v14, a30\n"
"v_mfma_f32_16x16x32_bf16 v[92:95], a[80:83], a[104:107], v[92:95]\n"
"ds_read_b128 a[104:107], v5 offset:18432\n"
"v_accvgpr_read_b32 v15, a31\n"
"v_mfma_f32_16x16x32_bf16 v[96:99], a[80:83], a[112:115], v[96:99]\n"
"v_add_f32 v12, v12, v28\n"
"v_mfma_f32_16x16x32_bf16 v[96:99], a[84:87], a[116:119], v[96:99]\n"
"global_load_dwordx4 a[68:71], v0, s[34:35] offset:1024 sc1\n"
"v_add_f32 v13, v13, v29\n"
"v_mfma_f32_16x16x32_bf16 v[124:127], a[92:95], a[116:119], v[124:127]\n"
"ds_read_b128 a[116:119], v5 offset:21504\n"
"v_add_f32 v14, v14, v30\n"
"v_mfma_f32_16x16x32_bf16 v[124:127], a[88:91], a[112:115], v[124:127]\n"
"ds_read_b128 a[112:115], v5 offset:20480\n"
"v_add_f32 v15, v15, v31\n"
"v_mfma_f32_16x16x32_bf16 v[128:131], a[88:91], a[120:123], v[128:131]\n"
"v_accvgpr_write_b32 a28, v12\n"
"v_accvgpr_write_b32 a29, v13\n"
"v_mfma_f32_16x16x32_bf16 v[128:131], a[92:95], a[124:127], v[128:131]\n"
"v_accvgpr_write_b32 a30, v14\n"
"v_mfma_f32_16x16x32_bf16 v[100:103], a[84:87], a[124:127], v[100:103]\n"
"ds_read_b128 a[124:127], v5 offset:23552\n"
"v_accvgpr_write_b32 a31, v15\n"
"v_mfma_f32_16x16x32_bf16 v[100:103], a[80:83], a[120:123], v[100:103]\n"
"ds_read_b128 a[120:123], v5 offset:22528\n"
"v_mfma_f32_16x16x32_bf16 v[104:107], a[80:83], a[128:131], v[104:107]\n"
"v_mfma_f32_16x16x32_bf16 v[104:107], a[84:87], a[132:135], v[104:107]\n"
"global_load_dwordx4 a[72:75], v0, s[34:35] offset:2048 sc1\n"
"v_mfma_f32_16x16x32_bf16 v[132:135], a[92:95], a[132:135], v[132:135]\n"
"ds_read_b128 a[132:135], v5 offset:25600\n"
"v_mfma_f32_16x16x32_bf16 v[132:135], a[88:91], a[128:131], v[132:135]\n"
"ds_read_b128 a[128:131], v5 offset:24576\n"
"v_mfma_f32_16x16x32_bf16 v[136:139], a[88:91], a[136:139], v[136:139]\n"
"ds_read_b128 v[16:19], v7 offset:32768\n"
"v_mfma_f32_16x16x32_bf16 v[136:139], a[92:95], a[140:143], v[136:139]\n"
"v_mfma_f32_16x16x32_bf16 v[108:111], a[84:87], a[140:143], v[108:111]\n"
"ds_read_b128 a[140:143], v5 offset:27648\n"
"v_mfma_f32_16x16x32_bf16 v[108:111], a[80:83], a[136:139], v[108:111]\n"
"ds_read_b128 a[136:139], v5 offset:26624\n"
"v_mfma_f32_16x16x32_bf16 v[112:115], a[80:83], a[144:147], v[112:115]\n"
"v_mfma_f32_16x16x32_bf16 v[112:115], a[84:87], a[148:151], v[112:115]\n"
"global_load_dwordx4 a[76:79], v0, s[34:35] offset:3072 sc1\n"
"v_mfma_f32_16x16x32_bf16 v[140:143], a[92:95], a[148:151], v[140:143]\n"
"ds_read_b128 a[148:151], v5 offset:29696\n"
"v_mfma_f32_16x16x32_bf16 v[140:143], a[88:91], a[144:147], v[140:143]\n"
"ds_read_b128 a[144:147], v5 offset:28672\n"
"v_mfma_f32_16x16x32_bf16 a[12:15], a[88:91], a[152:155], a[12:15]\n"
"v_mfma_f32_16x16x32_bf16 a[12:15], a[92:95], a[156:159], a[12:15]\n"
"v_mfma_f32_16x16x32_bf16 a[8:11], a[84:87], a[156:159], a[8:11]\n"
"ds_read_b128 a[156:159], v5 offset:31744\n"
"v_mfma_f32_16x16x32_bf16 a[8:11], a[80:83], a[152:155], a[8:11]\n"
"s_waitcnt vmcnt(8) lgkmcnt(12)\n"
"ds_read_b128 a[152:155], v5 offset:30720\n"
"v_mfma_f32_16x16x32_bf16 v[32:35], a[32:35], a[96:99], v[32:35]\n"
"s_add_u32 s34, s36, 1470464\n"
"v_mfma_f32_16x16x32_bf16 v[32:35], a[36:39], a[100:103], v[32:35]\n"
"global_load_dwordx4 a[80:83], v0, s[34:35] offset:0 sc1\n"
"v_mfma_f32_16x16x32_bf16 v[60:63], a[44:47], a[100:103], v[60:63]\n"
"v_mfma_f32_16x16x32_bf16 v[60:63], a[40:43], a[96:99], v[60:63]\n"
"v_mfma_f32_16x16x32_bf16 v[64:67], a[40:43], a[104:107], v[64:67]\n"
"v_mfma_f32_16x16x32_bf16 v[64:67], a[44:47], a[108:111], v[64:67]\n"
"v_mfma_f32_16x16x32_bf16 v[36:39], a[36:39], a[108:111], v[36:39]\n"
"v_mfma_f32_16x16x32_bf16 v[36:39], a[32:35], a[104:107], v[36:39]\n"
"s_waitcnt lgkmcnt(9)\n"
"v_mfma_f32_16x16x32_bf16 v[40:43], a[32:35], a[112:115], v[40:43]\n"
"s_mov_b32 s52, %4\n"
"s_and_b32 s53, %5, 0xffff\n"
"v_mfma_f32_16x16x32_bf16 v[40:43], a[36:39], a[116:119], v[40:43]\n"
"global_load_dwordx4 a[84:87], v0, s[34:35] offset:1024 sc1\n"
"v_mfma_f32_16x16x32_bf16 v[68:71], a[44:47], a[116:119], v[68:71]\n"
"s_mov_b32 s54, 260352\n"
"s_mov_b32 s55, 0x20000\n"
"v_mfma_f32_16x16x32_bf16 v[68:71], a[40:43], a[112:115], v[68:71]\n"
"v_xor_b32 v8, 64, v7\n"
"v_mfma_f32_16x16x32_bf16 v[72:75], a[40:43], a[120:123], v[72:75]\n"
"s_waitcnt lgkmcnt(6)\n"
"v_lshlrev_b32 v12, 16, v16\n"
"v_mfma_f32_16x16x32_bf16 v[72:75], a[44:47], a[124:127], v[72:75]\n"
"v_and_b32 v13, s63, v16\n"
"v_lshlrev_b32 v14, 16, v17\n"
"v_mfma_f32_16x16x32_bf16 v[44:47], a[36:39], a[124:127], v[44:47]\n"
"v_and_b32 v15, s63, v17\n"
"v_mfma_f32_16x16x32_bf16 v[44:47], a[32:35], a[120:123], v[44:47]\n"
"s_waitcnt lgkmcnt(4)\n"
"v_xor_b32 v20, 64, v7\n"
"v_mfma_f32_16x16x32_bf16 v[48:51], a[32:35], a[128:131], v[48:51]\n"
"ds_read_b128 v[20:23], v20 offset:32768\n"
"v_mul_f32 v12, v12, v32\n"
"v_mfma_f32_16x16x32_bf16 v[48:51], a[36:39], a[132:135], v[48:51]\n"
"global_load_dwordx4 a[88:91], v0, s[34:35] offset:2048 sc1\n"
"v_mul_f32 v13, v13, v33\n"
"v_mfma_f32_16x16x32_bf16 v[76:79], a[44:47], a[132:135], v[76:79]\n"
"v_mul_f32 v14, v14, v34\n"
"v_mfma_f32_16x16x32_bf16 v[76:79], a[40:43], a[128:131], v[76:79]\n"
"v_mul_f32 v15, v15, v35\n"
"v_cvt_pk_bf16_f32 v24, v12, v13\n"
"v_mfma_f32_16x16x32_bf16 v[80:83], a[40:43], a[136:139], v[80:83]\n"
"v_cvt_pk_bf16_f32 v25, v14, v15\n"
"v_mfma_f32_16x16x32_bf16 v[80:83], a[44:47], a[140:143], v[80:83]\n"
"ds_write_b64 v7, v[24:25] offset:32768\n"
"v_mfma_f32_16x16x32_bf16 v[52:55], a[36:39], a[140:143], v[52:55]\n"
"v_lshlrev_b32 v12, 16, v18\n"
"v_mfma_f32_16x16x32_bf16 v[52:55], a[32:35], a[136:139], v[52:55]\n"
"s_waitcnt lgkmcnt(2)\n"
"v_and_b32 v13, s63, v18\n"
"v_lshlrev_b32 v14, 16, v19\n"
"v_mfma_f32_16x16x32_bf16 v[56:59], a[32:35], a[144:147], v[56:59]\n"
"v_and_b32 v15, s63, v19\n"
"v_mfma_f32_16x16x32_bf16 v[56:59], a[36:39], a[148:151], v[56:59]\n"
"global_load_dwordx4 a[92:95], v0, s[34:35] offset:3072 sc1\n"
"v_mul_f32 v12, v12, v60\n"
"v_mfma_f32_16x16x32_bf16 v[84:87], a[44:47], a[148:151], v[84:87]\n"
"v_mul_f32 v13, v13, v61\n"
"v_mul_f32 v14, v14, v62\n"
"v_mfma_f32_16x16x32_bf16 v[84:87], a[40:43], a[144:147], v[84:87]\n"
"v_mul_f32 v15, v15, v63\n"
"v_mfma_f32_16x16x32_bf16 a[4:7], a[40:43], a[152:155], a[4:7]\n"
"v_cvt_pk_bf16_f32 v26, v12, v13\n"
"v_mfma_f32_16x16x32_bf16 a[4:7], a[44:47], a[156:159], a[4:7]\n"
"v_cvt_pk_bf16_f32 v27, v14, v15\n"
"v_mfma_f32_16x16x32_bf16 a[0:3], a[36:39], a[156:159], a[0:3]\n"
"ds_write_b64 v7, v[26:27] offset:32776\n"
"v_mfma_f32_16x16x32_bf16 a[0:3], a[32:35], a[152:155], a[0:3]\n"
"s_waitcnt vmcnt(8) lgkmcnt(2)\n"
"v_lshlrev_b32 v12, 16, v20\n"
"v_mfma_f32_16x16x32_bf16 v[88:91], a[48:51], a[96:99], v[88:91]\n"
"s_add_u32 s34, s36, 1482752\n"
"v_and_b32 v13, s63, v20\n"
"v_mfma_f32_16x16x32_bf16 v[88:91], a[52:55], a[100:103], v[88:91]\n"
"global_load_dwordx4 a[32:35], v0, s[34:35] offset:0 sc1\n"
"v_mfma_f32_16x16x32_bf16 v[116:119], a[60:63], a[100:103], v[116:119]\n"
"ds_read_b128 a[100:103], v3 offset:1024\n"
"v_mfma_f32_16x16x32_bf16 v[116:119], a[56:59], a[96:99], v[116:119]\n"
"ds_read_b128 a[96:99], v3 offset:0\n"
"v_mfma_f32_16x16x32_bf16 v[120:123], a[56:59], a[104:107], v[120:123]\n"
"v_lshlrev_b32 v14, 16, v21\n"
"v_mfma_f32_16x16x32_bf16 v[120:123], a[60:63], a[108:111], v[120:123]\n"
"v_and_b32 v15, s63, v21\n"
"v_mfma_f32_16x16x32_bf16 v[92:95], a[52:55], a[108:111], v[92:95]\n"
"ds_read_b128 a[108:111], v3 offset:3072\n"
"v_mfma_f32_16x16x32_bf16 v[92:95], a[48:51], a[104:107], v[92:95]\n"
"ds_read_b128 a[104:107], v3 offset:2048\n"
"v_mfma_f32_16x16x32_bf16 v[96:99], a[48:51], a[112:115], v[96:99]\n"
"ds_read_b128 v[16:19], v7 offset:34816\n"
"v_mfma_f32_16x16x32_bf16 v[96:99], a[52:55], a[116:119], v[96:99]\n"
"global_load_dwordx4 a[36:39], v0, s[34:35] offset:1024 sc1\n"
"v_mul_f32 v12, v12, v88\n"
"v_mul_f32 v13, v13, v89\n"
"v_mfma_f32_16x16x32_bf16 v[124:127], a[60:63], a[116:119], v[124:127]\n"
"ds_read_b128 a[116:119], v3 offset:5120\n"
"v_mul_f32 v14, v14, v90\n"
"v_mul_f32 v15, v15, v91\n"
"v_mfma_f32_16x16x32_bf16 v[124:127], a[56:59], a[112:115], v[124:127]\n"
"ds_read_b128 a[112:115], v3 offset:4096\n"
"v_cvt_pk_bf16_f32 v28, v12, v13\n"
"v_cvt_pk_bf16_f32 v29, v14, v15\n"
"v_mfma_f32_16x16x32_bf16 v[128:131], a[56:59], a[120:123], v[128:131]\n"
"ds_write_b64 v8, v[28:29] offset:32768\n"
"v_mfma_f32_16x16x32_bf16 v[128:131], a[60:63], a[124:127], v[128:131]\n"
"v_lshlrev_b32 v12, 16, v22\n"
"v_and_b32 v13, s63, v22\n"
"v_lshlrev_b32 v14, 16, v23\n"
"v_mfma_f32_16x16x32_bf16 v[100:103], a[52:55], a[124:127], v[100:103]\n"
"ds_read_b128 a[124:127], v3 offset:7168\n"
"v_and_b32 v15, s63, v23\n"
"v_mul_f32 v12, v12, v116\n"
"v_mfma_f32_16x16x32_bf16 v[100:103], a[48:51], a[120:123], v[100:103]\n"
"ds_read_b128 a[120:123], v3 offset:6144\n"
"v_mul_f32 v13, v13, v117\n"
"v_mfma_f32_16x16x32_bf16 v[104:107], a[48:51], a[128:131], v[104:107]\n"
"v_mul_f32 v14, v14, v118\n"
"v_mul_f32 v15, v15, v119\n"
"v_mfma_f32_16x16x32_bf16 v[104:107], a[52:55], a[132:135], v[104:107]\n"
"global_load_dwordx4 a[40:43], v0, s[34:35] offset:2048 sc1\n"
"v_cvt_pk_bf16_f32 v30, v12, v13\n"
"v_mfma_f32_16x16x32_bf16 v[132:135], a[60:63], a[132:135], v[132:135]\n"
"ds_read_b128 a[132:135], v3 offset:9216\n"
"v_cvt_pk_bf16_f32 v31, v14, v15\n"
"v_mfma_f32_16x16x32_bf16 v[132:135], a[56:59], a[128:131], v[132:135]\n"
"ds_read_b128 a[128:131], v3 offset:8192\n"
"ds_write_b64 v8, v[30:31] offset:32776\n"
"v_mfma_f32_16x16x32_bf16 v[136:139], a[56:59], a[136:139], v[136:139]\n"
"ds_read_b128 v[32:35], v2 offset:32768\n"
"v_mfma_f32_16x16x32_bf16 v[136:139], a[60:63], a[140:143], v[136:139]\n"
"s_add_u32 s96, s36, 1552384\n"
"ds_read_b128 v[60:63], v2 offset:33792\n"
"v_mfma_f32_16x16x32_bf16 v[108:111], a[52:55], a[140:143], v[108:111]\n"
"s_waitcnt lgkmcnt(10)\n"
"ds_read_b128 a[140:143], v3 offset:11264\n"
"v_lshlrev_b32 v12, 16, v16\n"
"v_mfma_f32_16x16x32_bf16 v[108:111], a[48:51], a[136:139], v[108:111]\n"
"ds_read_b128 a[136:139], v3 offset:10240\n"
"v_and_b32 v13, s63, v16\n"
"v_mfma_f32_16x16x32_bf16 v[112:115], a[48:51], a[144:147], v[112:115]\n"
"global_load_dwordx4 v[88:91], v0, s[96:97] offset:-4096 sc1\n"
"v_lshlrev_b32 v14, 16, v17\n"
"v_mfma_f32_16x16x32_bf16 v[112:115], a[52:55], a[148:151], v[112:115]\n"
"global_load_dwordx4 a[44:47], v0, s[34:35] offset:3072 sc1\n"
"v_and_b32 v15, s63, v17\n"
"v_mfma_f32_16x16x32_bf16 v[140:143], a[60:63], a[148:151], v[140:143]\n"
"ds_read_b128 a[148:151], v3 offset:13312\n"
"v_xor_b32 v20, 64, v7\n"
"v_mfma_f32_16x16x32_bf16 v[140:143], a[56:59], a[144:147], v[140:143]\n"
"ds_read_b128 a[144:147], v3 offset:12288\n"
"v_mfma_f32_16x16x32_bf16 a[12:15], a[56:59], a[152:155], a[12:15]\n"
"ds_read_b128 v[20:23], v20 offset:34816\n"
"v_mul_f32 v12, v12, v36\n"
"v_mfma_f32_16x16x32_bf16 a[12:15], a[60:63], a[156:159], a[12:15]\n"
"s_waitcnt lgkmcnt(6)\n"
"buffer_store_dwordx4 v[32:35], v6, s[52:55], s80 offen offset:0 sc1\n"
"v_mul_f32 v13, v13, v37\n"
"v_mul_f32 v14, v14, v38\n"
"v_mul_f32 v15, v15, v39\n"
"v_mfma_f32_16x16x32_bf16 a[8:11], a[52:55], a[156:159], a[8:11]\n"
"ds_read_b128 a[156:159], v3 offset:15360\n"
"v_cvt_pk_bf16_f32 v24, v12, v13\n"
"v_mfma_f32_16x16x32_bf16 a[8:11], a[48:51], a[152:155], a[8:11]\n"
"s_waitcnt vmcnt(10)\n"
"ds_read_b128 a[152:155], v3 offset:14336\n"
"v_cvt_pk_bf16_f32 v25, v14, v15\n"
"v_mfma_f32_16x16x32_bf16 v[144:147], a[64:67], a[96:99], v[144:147]\n"
"s_add_u32 s34, s36, 1486848\n"
"ds_write_b64 v7, v[24:25] offset:34816\n"
"v_mfma_f32_16x16x32_bf16 v[144:147], a[68:71], a[100:103], v[144:147]\n"
"global_load_dwordx4 a[48:51], v0, s[34:35] offset:0 sc1\n"
"v_lshlrev_b32 v12, 16, v18\n"
"v_mfma_f32_16x16x32_bf16 v[172:175], a[76:79], a[100:103], v[172:175]\n"
"global_load_dwordx4 v[116:119], v0, s[96:97] offset:-3072 sc1\n"
"v_and_b32 v13, s63, v18\n"
"v_mfma_f32_16x16x32_bf16 v[172:175], a[72:75], a[96:99], v[172:175]\n"
"v_lshlrev_b32 v14, 16, v19\n"
"v_and_b32 v15, s63, v19\n"
"v_mfma_f32_16x16x32_bf16 v[176:179], a[72:75], a[104:107], v[176:179]\n"
"v_mul_f32 v12, v12, v64\n"
"v_mul_f32 v13, v13, v65\n"
"v_mfma_f32_16x16x32_bf16 v[176:179], a[76:79], a[108:111], v[176:179]\n"
"v_mul_f32 v14, v14, v66\n"
"v_mul_f32 v15, v15, v67\n"
"v_mfma_f32_16x16x32_bf16 v[148:151], a[68:71], a[108:111], v[148:151]\n"
"v_cvt_pk_bf16_f32 v26, v12, v13\n"
"v_cvt_pk_bf16_f32 v27, v14, v15\n"
"v_mfma_f32_16x16x32_bf16 v[148:151], a[64:67], a[104:107], v[148:151]\n"
"ds_write_b64 v7, v[26:27] offset:34824\n"
"v_mfma_f32_16x16x32_bf16 v[152:155], a[64:67], a[112:115], v[152:155]\n"
"s_waitcnt lgkmcnt(4)\n"
"v_lshlrev_b32 v12, 16, v20\n"
"v_and_b32 v13, s63, v20\n"
"v_mfma_f32_16x16x32_bf16 v[152:155], a[68:71], a[116:119], v[152:155]\n"
"global_load_dwordx4 a[52:55], v0, s[34:35] offset:1024 sc1\n"
"v_lshlrev_b32 v14, 16, v21\n"
"v_mfma_f32_16x16x32_bf16 v[180:183], a[76:79], a[116:119], v[180:183]\n"
"ds_read_b128 v[16:19], v7 offset:36864\n"
"v_and_b32 v15, s63, v21\n"
"v_mfma_f32_16x16x32_bf16 v[180:183], a[72:75], a[112:115], v[180:183]\n"
"v_mul_f32 v12, v12, v92\n"
"v_mul_f32 v13, v13, v93\n"
"v_mfma_f32_16x16x32_bf16 v[184:187], a[72:75], a[120:123], v[184:187]\n"
"v_mul_f32 v14, v14, v94\n"
"v_mul_f32 v15, v15, v95\n"
"v_mfma_f32_16x16x32_bf16 v[184:187], a[76:79], a[124:127], v[184:187]\n"
"v_cvt_pk_bf16_f32 v28, v12, v13\n"
"v_cvt_pk_bf16_f32 v29, v14, v15\n"
"v_mfma_f32_16x16x32_bf16 v[156:159], a[68:71], a[124:127], v[156:159]\n"
"ds_write_b64 v8, v[28:29] offset:34816\n"
"v_mfma_f32_16x16x32_bf16 v[156:159], a[64:67], a[120:123], v[156:159]\n"
"v_lshlrev_b32 v12, 16, v22\n"
"v_and_b32 v13, s63, v22\n"
"v_mfma_f32_16x16x32_bf16 v[160:163], a[64:67], a[128:131], v[160:163]\n"
"v_lshlrev_b32 v14, 16, v23\n"
"v_and_b32 v15, s63, v23\n"
"v_mfma_f32_16x16x32_bf16 v[160:163], a[68:71], a[132:135], v[160:163]\n"
"global_load_dwordx4 a[56:59], v0, s[34:35] offset:2048 sc1\n"
"v_mul_f32 v12, v12, v120\n"
"v_mfma_f32_16x16x32_bf16 v[188:191], a[76:79], a[132:135], v[188:191]\n"
"v_mul_f32 v13, v13, v121\n"
"v_mul_f32 v14, v14, v122\n"
"v_mfma_f32_16x16x32_bf16 v[188:191], a[72:75], a[128:131], v[188:191]\n"
"v_mul_f32 v15, v15, v123\n"
"v_cvt_pk_bf16_f32 v30, v12, v13\n"
"v_mfma_f32_16x16x32_bf16 v[192:195], a[72:75], a[136:139], v[192:195]\n"
"v_cvt_pk_bf16_f32 v31, v14, v15\n"
"v_mfma_f32_16x16x32_bf16 v[192:195], a[76:79], a[140:143], v[192:195]\n"
"ds_write_b64 v8, v[30:31] offset:34824\n"
"v_mfma_f32_16x16x32_bf16 v[164:167], a[68:71], a[140:143], v[164:167]\n"
"ds_read_b128 v[36:39], v2 offset:34816\n"
"v_mfma_f32_16x16x32_bf16 v[164:167], a[64:67], a[136:139], v[164:167]\n"
"s_waitcnt lgkmcnt(3)\n"
"ds_read_b128 v[64:67], v2 offset:35840\n"
"v_lshlrev_b32 v12, 16, v16\n"
"v_mfma_f32_16x16x32_bf16 v[168:171], a[64:67], a[144:147], v[168:171]\n"
"buffer_store_dwordx4 v[60:63], v6, s[52:55], s81 offen offset:1024 sc1\n"
"v_and_b32 v13, s63, v16\n"
"v_lshlrev_b32 v14, 16, v17\n"
"v_and_b32 v15, s63, v17\n"
"v_xor_b32 v20, 64, v7\n"
"v_mfma_f32_16x16x32_bf16 v[168:171], a[68:71], a[148:151], v[168:171]\n"
"global_load_dwordx4 a[60:63], v0, s[34:35] offset:3072 sc1\n"
"ds_read_b128 v[20:23], v20 offset:36864\n"
"v_mfma_f32_16x16x32_bf16 v[196:199], a[76:79], a[148:151], v[196:199]\n"
"v_mul_f32 v12, v12, v40\n"
"v_mul_f32 v13, v13, v41\n"
"v_mfma_f32_16x16x32_bf16 v[196:199], a[72:75], a[144:147], v[196:199]\n"
"v_mul_f32 v14, v14, v42\n"
"v_mul_f32 v15, v15, v43\n"
"v_mfma_f32_16x16x32_bf16 a[20:23], a[72:75], a[152:155], a[20:23]\n"
"v_cvt_pk_bf16_f32 v24, v12, v13\n"
"v_cvt_pk_bf16_f32 v25, v14, v15\n"
"v_mfma_f32_16x16x32_bf16 a[20:23], a[76:79], a[156:159], a[20:23]\n"
"ds_write_b64 v7, v[24:25] offset:36864\n"
"v_mfma_f32_16x16x32_bf16 a[16:19], a[68:71], a[156:159], a[16:19]\n"
"global_load_dwordx4 v[92:95], v0, s[96:97] offset:-2048 sc1\n"
"v_lshlrev_b32 v12, 16, v18\n"
"v_mfma_f32_16x16x32_bf16 a[16:19], a[64:67], a[152:155], a[16:19]\n"
"s_waitcnt vmcnt(13)\n"
"v_and_b32 v13, s63, v18\n"
"v_lshlrev_b32 v14, 16, v19\n"
"v_mfma_f32_16x16x32_bf16 v[200:203], a[80:83], a[96:99], v[200:203]\n"
"s_add_u32 s34, s36, 1499136\n"
"v_and_b32 v15, s63, v19\n"
"v_mul_f32 v12, v12, v68\n"
"v_mfma_f32_16x16x32_bf16 v[200:203], a[84:87], a[100:103], v[200:203]\n"
"global_load_dwordx4 a[64:67], v0, s[34:35] offset:0 sc1\n"
"v_mul_f32 v13, v13, v69\n"
"v_mfma_f32_16x16x32_bf16 v[228:231], a[92:95], a[100:103], v[228:231]\n"
"ds_read_b128 a[100:103], v4 offset:1024\n"
"v_mul_f32 v14, v14, v70\n"
"v_mfma_f32_16x16x32_bf16 v[228:231], a[88:91], a[96:99], v[228:231]\n"
"ds_read_b128 a[96:99], v4 offset:0\n"
"v_mul_f32 v15, v15, v71\n"
"v_mfma_f32_16x16x32_bf16 v[232:235], a[88:91], a[104:107], v[232:235]\n"
"v_cvt_pk_bf16_f32 v26, v12, v13\n"
"v_cvt_pk_bf16_f32 v27, v14, v15\n"
"v_mfma_f32_16x16x32_bf16 v[232:235], a[92:95], a[108:111], v[232:235]\n"
"ds_write_b64 v7, v[26:27] offset:36872\n"
"v_mfma_f32_16x16x32_bf16 v[204:207], a[84:87], a[108:111], v[204:207]\n"
"ds_read_b128 a[108:111], v4 offset:3072\n"
"v_mfma_f32_16x16x32_bf16 v[204:207], a[80:83], a[104:107], v[204:207]\n"
"ds_read_b128 a[104:107], v4 offset:2048\n"
"v_mfma_f32_16x16x32_bf16 v[208:211], a[80:83], a[112:115], v[208:211]\n"
"global_load_dwordx4 v[120:123], v0, s[96:97] offset:-1024 sc1\n"
"v_mfma_f32_16x16x32_bf16 v[208:211], a[84:87], a[116:119], v[208:211]\n"
"global_load_dwordx4 a[68:71], v0, s[34:35] offset:1024 sc1\n"
"v_mfma_f32_16x16x32_bf16 v[236:239], a[92:95], a[116:119], v[236:239]\n"
"s_waitcnt lgkmcnt(6)\n"
"ds_read_b128 a[116:119], v4 offset:5120\n"
"v_lshlrev_b32 v12, 16, v20\n"
"v_mfma_f32_16x16x32_bf16 v[236:239], a[88:91], a[112:115], v[236:239]\n"
"ds_read_b128 a[112:115], v4 offset:4096\n"
"v_and_b32 v13, s63, v20\n"
"v_mfma_f32_16x16x32_bf16 v[240:243], a[88:91], a[120:123], v[240:243]\n"
"v_lshlrev_b32 v14, 16, v21\n"
"v_and_b32 v15, s63, v21\n"
"v_mfma_f32_16x16x32_bf16 v[240:243], a[92:95], a[124:127], v[240:243]\n"
"ds_read_b128 v[16:19], v7 offset:38912\n"
"v_mul_f32 v12, v12, v96\n"
"v_mfma_f32_16x16x32_bf16 v[212:215], a[84:87], a[124:127], v[212:215]\n"
"ds_read_b128 a[124:127], v4 offset:7168\n"
"v_mul_f32 v13, v13, v97\n"
"v_mfma_f32_16x16x32_bf16 v[212:215], a[80:83], a[120:123], v[212:215]\n"
"ds_read_b128 a[120:123], v4 offset:6144\n"
"v_mul_f32 v14, v14, v98\n"
"v_mfma_f32_16x16x32_bf16 v[216:219], a[80:83], a[128:131], v[216:219]\n"
"v_mul_f32 v15, v15, v99\n"
"v_mfma_f32_16x16x32_bf16 v[216:219], a[84:87], a[132:135], v[216:219]\n"
"global_load_dwordx4 a[72:75], v0, s[34:35] offset:2048 sc1\n"
"v_cvt_pk_bf16_f32 v28, v12, v13\n"
"v_mfma_f32_16x16x32_bf16 v[244:247], a[92:95], a[132:135], v[244:247]\n"
"ds_read_b128 a[132:135], v4 offset:9216\n"
"global_load_dwordx4 v[32:35], v0, s[96:97] offset:0 sc1\n"
"v_mfma_f32_16x16x32_bf16 v[244:247], a[88:91], a[128:131], v[244:247]\n"
"ds_read_b128 a[128:131], v4 offset:8192\n"
"v_mfma_f32_16x16x32_bf16 v[248:251], a[88:91], a[136:139], v[248:251]\n"
"v_cvt_pk_bf16_f32 v29, v14, v15\n"
"v_mfma_f32_16x16x32_bf16 v[248:251], a[92:95], a[140:143], v[248:251]\n"
"buffer_store_dwordx4 v[36:39], v6, s[52:55], s82 offen offset:2048 sc1\n"
"ds_write_b64 v8, v[28:29] offset:36864\n"
"v_mfma_f32_16x16x32_bf16 v[220:223], a[84:87], a[140:143], v[220:223]\n"
"ds_read_b128 a[140:143], v4 offset:11264\n"
"v_lshlrev_b32 v12, 16, v22\n"
"v_mfma_f32_16x16x32_bf16 v[220:223], a[80:83], a[136:139], v[220:223]\n"
"ds_read_b128 a[136:139], v4 offset:10240\n"
"v_and_b32 v13, s63, v22\n"
"v_mfma_f32_16x16x32_bf16 v[224:227], a[80:83], a[144:147], v[224:227]\n"
"v_lshlrev_b32 v14, 16, v23\n"
"v_and_b32 v15, s63, v23\n"
"v_mfma_f32_16x16x32_bf16 v[224:227], a[84:87], a[148:151], v[224:227]\n"
"global_load_dwordx4 a[76:79], v0, s[34:35] offset:3072 sc1\n"
"v_mul_f32 v12, v12, v124\n"
"v_mfma_f32_16x16x32_bf16 v[252:255], a[92:95], a[148:151], v[252:255]\n"
"ds_read_b128 a[148:151], v4 offset:13312\n"
"v_mul_f32 v13, v13, v125\n"
"v_mfma_f32_16x16x32_bf16 v[252:255], a[88:91], a[144:147], v[252:255]\n"
"ds_read_b128 a[144:147], v4 offset:12288\n"
"v_mul_f32 v14, v14, v126\n"
"v_mfma_f32_16x16x32_bf16 a[28:31], a[88:91], a[152:155], a[28:31]\n"
"global_load_dwordx4 v[60:63], v0, s[96:97] offset:1024 sc1\n"
"v_mul_f32 v15, v15, v127\n"
"v_mfma_f32_16x16x32_bf16 a[28:31], a[92:95], a[156:159], a[28:31]\n"
"v_cvt_pk_bf16_f32 v30, v12, v13\n"
"v_cvt_pk_bf16_f32 v31, v14, v15\n"
"v_mfma_f32_16x16x32_bf16 a[24:27], a[84:87], a[156:159], a[24:27]\n"
"ds_read_b128 a[156:159], v4 offset:15360\n"
"v_mfma_f32_16x16x32_bf16 a[24:27], a[80:83], a[152:155], a[24:27]\n"
"s_waitcnt vmcnt(16) lgkmcnt(13)\n"
"ds_read_b128 a[152:155], v4 offset:14336\n"
"v_mfma_f32_16x16x32_bf16 v[144:147], a[32:35], a[96:99], v[144:147]\n"
"s_add_u32 s34, s36, 1503232\n"
"ds_write_b64 v8, v[30:31] offset:36872\n"
"v_mfma_f32_16x16x32_bf16 v[144:147], a[36:39], a[100:103], v[144:147]\n"
"global_load_dwordx4 a[80:83], v0, s[34:35] offset:0 sc1\n"
"ds_read_b128 v[40:43], v2 offset:36864\n"
"v_mfma_f32_16x16x32_bf16 v[172:175], a[44:47], a[100:103], v[172:175]\n"
"s_waitcnt lgkmcnt(13)\n"
"ds_read_b128 v[68:71], v2 offset:37888\n"
"v_lshlrev_b32 v12, 16, v16\n"
"v_mfma_f32_16x16x32_bf16 v[172:175], a[40:43], a[96:99], v[172:175]\n"
"v_and_b32 v13, s63, v16\n"
"v_lshlrev_b32 v14, 16, v17\n"
"v_mfma_f32_16x16x32_bf16 v[176:179], a[40:43], a[104:107], v[176:179]\n"
"v_and_b32 v15, s63, v17\n"
"v_xor_b32 v20, 64, v7\n"
"v_mfma_f32_16x16x32_bf16 v[176:179], a[44:47], a[108:111], v[176:179]\n"
"ds_read_b128 v[20:23], v20 offset:38912\n"
"v_mul_f32 v12, v12, v44\n"
"v_mfma_f32_16x16x32_bf16 v[148:151], a[36:39], a[108:111], v[148:151]\n"
"v_mul_f32 v13, v13, v45\n"
"v_mul_f32 v14, v14, v46\n"
"v_mfma_f32_16x16x32_bf16 v[148:151], a[32:35], a[104:107], v[148:151]\n"
"s_waitcnt lgkmcnt(13)\n"
"global_load_dwordx4 v[96:99], v0, s[96:97] offset:2048 sc1\n"
"v_mul_f32 v15, v15, v47\n"
"v_mfma_f32_16x16x32_bf16 v[152:155], a[32:35], a[112:115], v[152:155]\n"
"v_cvt_pk_bf16_f32 v24, v12, v13\n"
"v_cvt_pk_bf16_f32 v25, v14, v15\n"
"v_mfma_f32_16x16x32_bf16 v[152:155], a[36:39], a[116:119], v[152:155]\n"
"global_load_dwordx4 a[84:87], v0, s[34:35] offset:1024 sc1\n"
"v_mfma_f32_16x16x32_bf16 v[180:183], a[44:47], a[116:119], v[180:183]\n"
"ds_write_b64 v7, v[24:25] offset:38912\n"
"v_mfma_f32_16x16x32_bf16 v[180:183], a[40:43], a[112:115], v[180:183]\n"
"v_lshlrev_b32 v12, 16, v18\n"
"v_and_b32 v13, s63, v18\n"
"v_mfma_f32_16x16x32_bf16 v[184:187], a[40:43], a[120:123], v[184:187]\n"
"v_lshlrev_b32 v14, 16, v19\n"
"v_and_b32 v15, s63, v19\n"
"v_mfma_f32_16x16x32_bf16 v[184:187], a[44:47], a[124:127], v[184:187]\n"
"v_mul_f32 v12, v12, v72\n"
"v_mul_f32 v13, v13, v73\n"
"v_mfma_f32_16x16x32_bf16 v[156:159], a[36:39], a[124:127], v[156:159]\n"
"v_mul_f32 v14, v14, v74\n"
"v_mul_f32 v15, v15, v75\n"
"v_mfma_f32_16x16x32_bf16 v[156:159], a[32:35], a[120:123], v[156:159]\n"
"s_waitcnt lgkmcnt(9)\n"
"v_cvt_pk_bf16_f32 v26, v12, v13\n"
"v_cvt_pk_bf16_f32 v27, v14, v15\n"
"v_mfma_f32_16x16x32_bf16 v[160:163], a[32:35], a[128:131], v[160:163]\n"
"buffer_store_dwordx4 v[64:67], v6, s[52:55], s83 offen offset:3072 sc1\n"
"ds_write_b64 v7, v[26:27] offset:38920\n"
"v_mfma_f32_16x16x32_bf16 v[160:163], a[36:39], a[132:135], v[160:163]\n"
"s_waitcnt lgkmcnt(2)\n"
"global_load_dwordx4 a[88:91], v0, s[34:35] offset:2048 sc1\n"
"v_lshlrev_b32 v12, 16, v20\n"
"v_mfma_f32_16x16x32_bf16 v[188:191], a[44:47], a[132:135], v[188:191]\n"
"global_load_dwordx4 v[124:127], v0, s[96:97] offset:3072 sc1\n"
"v_and_b32 v13, s63, v20\n"
"v_mfma_f32_16x16x32_bf16 v[188:191], a[40:43], a[128:131], v[188:191]\n"
"v_lshlrev_b32 v14, 16, v21\n"
"v_and_b32 v15, s63, v21\n"
"v_mfma_f32_16x16x32_bf16 v[192:195], a[40:43], a[136:139], v[192:195]\n"
"v_mul_f32 v12, v12, v100\n"
"v_mul_f32 v13, v13, v101\n"
"v_mfma_f32_16x16x32_bf16 v[192:195], a[44:47], a[140:143], v[192:195]\n"
"v_mul_f32 v14, v14, v102\n"
"v_mul_f32 v15, v15, v103\n"
"v_mfma_f32_16x16x32_bf16 v[164:167], a[36:39], a[140:143], v[164:167]\n"
"v_cvt_pk_bf16_f32 v28, v12, v13\n"
"v_cvt_pk_bf16_f32 v29, v14, v15\n"
"v_mfma_f32_16x16x32_bf16 v[164:167], a[32:35], a[136:139], v[164:167]\n"
"ds_write_b64 v8, v[28:29] offset:38912\n"
"v_mfma_f32_16x16x32_bf16 v[168:171], a[32:35], a[144:147], v[168:171]\n"
"v_lshlrev_b32 v12, 16, v22\n"
"v_mfma_f32_16x16x32_bf16 v[168:171], a[36:39], a[148:151], v[168:171]\n"
"global_load_dwordx4 a[92:95], v0, s[34:35] offset:3072 sc1\n"
"v_and_b32 v13, s63, v22\n"
"v_mfma_f32_16x16x32_bf16 v[196:199], a[44:47], a[148:151], v[196:199]\n"
"v_lshlrev_b32 v14, 16, v23\n"
"v_and_b32 v15, s63, v23\n"
"v_mfma_f32_16x16x32_bf16 v[196:199], a[40:43], a[144:147], v[196:199]\n"
"v_mul_f32 v12, v12, v128\n"
"v_mfma_f32_16x16x32_bf16 a[20:23], a[40:43], a[152:155], a[20:23]\n"
"v_mul_f32 v13, v13, v129\n"
"v_mfma_f32_16x16x32_bf16 a[20:23], a[44:47], a[156:159], a[20:23]\n"
"v_mul_f32 v14, v14, v130\n"
"v_mul_f32 v15, v15, v131\n"
"v_mfma_f32_16x16x32_bf16 a[16:19], a[36:39], a[156:159], a[16:19]\n"
"v_cvt_pk_bf16_f32 v30, v12, v13\n"
"v_cvt_pk_bf16_f32 v31, v14, v15\n"
"v_mfma_f32_16x16x32_bf16 a[16:19], a[32:35], a[152:155], a[16:19]\n"
"s_waitcnt vmcnt(16)\n"
"ds_write_b64 v8, v[30:31] offset:38920\n"
"v_mfma_f32_16x16x32_bf16 v[200:203], a[48:51], a[96:99], v[200:203]\n"
"s_add_u32 s34, s36, 1540096\n"
"ds_read_b128 v[44:47], v2 offset:38912\n"
"v_mfma_f32_16x16x32_bf16 v[200:203], a[52:55], a[100:103], v[200:203]\n"
"global_load_dwordx4 a[32:35], v0, s[34:35] offset:0 sc1\n"
"ds_read_b128 v[72:75], v2 offset:39936\n"
"v_mfma_f32_16x16x32_bf16 v[228:231], a[60:63], a[100:103], v[228:231]\n"
"ds_read_b128 a[100:103], v5 offset:1024\n"
"v_accvgpr_read_b32 v16, a160\n"
"v_mfma_f32_16x16x32_bf16 v[228:231], a[56:59], a[96:99], v[228:231]\n"
"ds_read_b128 a[96:99], v5 offset:0\n"
"v_accvgpr_read_b32 v17, a161\n"
"v_mfma_f32_16x16x32_bf16 v[232:235], a[56:59], a[104:107], v[232:235]\n"
"v_accvgpr_read_b32 v18, a162\n"
"v_accvgpr_read_b32 v19, a163\n"
"v_mfma_f32_16x16x32_bf16 v[232:235], a[60:63], a[108:111], v[232:235]\n"
"v_lshlrev_b32 v12, 16, v16\n"
"v_mfma_f32_16x16x32_bf16 v[204:207], a[52:55], a[108:111], v[204:207]\n"
"ds_read_b128 a[108:111], v5 offset:3072\n"
"v_and_b32 v13, s63, v16\n"
"v_mfma_f32_16x16x32_bf16 v[204:207], a[48:51], a[104:107], v[204:207]\n"
"ds_read_b128 a[104:107], v5 offset:2048\n"
"v_lshlrev_b32 v14, 16, v17\n"
"v_mfma_f32_16x16x32_bf16 v[208:211], a[48:51], a[112:115], v[208:211]\n"
"v_and_b32 v15, s63, v17\n"
"v_mul_f32 v12, v12, v48\n"
"v_mfma_f32_16x16x32_bf16 v[208:211], a[52:55], a[116:119], v[208:211]\n"
"global_load_dwordx4 a[36:39], v0, s[34:35] offset:1024 sc1\n"
"v_mul_f32 v13, v13, v49\n"
"v_mfma_f32_16x16x32_bf16 v[236:239], a[60:63], a[116:119], v[236:239]\n"
"ds_read_b128 a[116:119], v5 offset:5120\n"
"v_mul_f32 v14, v14, v50\n"
"v_mfma_f32_16x16x32_bf16 v[236:239], a[56:59], a[112:115], v[236:239]\n"
"ds_read_b128 a[112:115], v5 offset:4096\n"
"v_mul_f32 v15, v15, v51\n"
"v_mfma_f32_16x16x32_bf16 v[240:243], a[56:59], a[120:123], v[240:243]\n"
"v_cvt_pk_bf16_f32 v24, v12, v13\n"
"v_cvt_pk_bf16_f32 v25, v14, v15\n"
"v_mfma_f32_16x16x32_bf16 v[240:243], a[60:63], a[124:127], v[240:243]\n"
"buffer_store_dwordx4 v[40:43], v6, s[52:55], s84 offen offset:0 sc1\n"
"ds_write_b64 v7, v[24:25] offset:32768\n"
"v_mfma_f32_16x16x32_bf16 v[212:215], a[52:55], a[124:127], v[212:215]\n"
"ds_read_b128 a[124:127], v5 offset:7168\n"
"v_lshlrev_b32 v12, 16, v18\n"
"v_mfma_f32_16x16x32_bf16 v[212:215], a[48:51], a[120:123], v[212:215]\n"
"ds_read_b128 a[120:123], v5 offset:6144\n"
"v_and_b32 v13, s63, v18\n"
"v_mfma_f32_16x16x32_bf16 v[216:219], a[48:51], a[128:131], v[216:219]\n"
"v_lshlrev_b32 v14, 16, v19\n"
"v_and_b32 v15, s63, v19\n"
"v_mfma_f32_16x16x32_bf16 v[216:219], a[52:55], a[132:135], v[216:219]\n"
"global_load_dwordx4 a[40:43], v0, s[34:35] offset:2048 sc1\n"
"v_mul_f32 v12, v12, v76\n"
"v_mfma_f32_16x16x32_bf16 v[244:247], a[60:63], a[132:135], v[244:247]\n"
"s_add_u32 s96, s36, 1560576\n"
"ds_read_b128 a[132:135], v5 offset:9216\n"
"v_mul_f32 v13, v13, v77\n"
"v_mfma_f32_16x16x32_bf16 v[244:247], a[56:59], a[128:131], v[244:247]\n"
"ds_read_b128 a[128:131], v5 offset:8192\n"
"v_mul_f32 v14, v14, v78\n"
"v_mfma_f32_16x16x32_bf16 v[248:251], a[56:59], a[136:139], v[248:251]\n"
"global_load_dwordx4 v[100:103], v0, s[96:97] offset:-4096 sc1\n"
"v_mul_f32 v15, v15, v79\n"
"v_mfma_f32_16x16x32_bf16 v[248:251], a[60:63], a[140:143], v[248:251]\n"
"v_cvt_pk_bf16_f32 v26, v12, v13\n"
"v_cvt_pk_bf16_f32 v27, v14, v15\n"
"v_mfma_f32_16x16x32_bf16 v[220:223], a[52:55], a[140:143], v[220:223]\n"
"ds_read_b128 a[140:143], v5 offset:11264\n"
"v_mfma_f32_16x16x32_bf16 v[220:223], a[48:51], a[136:139], v[220:223]\n"
"ds_read_b128 a[136:139], v5 offset:10240\n"
"v_mfma_f32_16x16x32_bf16 v[224:227], a[48:51], a[144:147], v[224:227]\n"
"ds_write_b64 v7, v[26:27] offset:32776\n"
"v_mfma_f32_16x16x32_bf16 v[224:227], a[52:55], a[148:151], v[224:227]\n"
"global_load_dwordx4 a[44:47], v0, s[34:35] offset:3072 sc1\n"
"v_accvgpr_read_b32 v16, a164\n"
"v_mfma_f32_16x16x32_bf16 v[252:255], a[60:63], a[148:151], v[252:255]\n"
"ds_read_b128 a[148:151], v5 offset:13312\n"
"v_accvgpr_read_b32 v17, a165\n"
"v_mfma_f32_16x16x32_bf16 v[252:255], a[56:59], a[144:147], v[252:255]\n"
"ds_read_b128 a[144:147], v5 offset:12288\n"
"v_accvgpr_read_b32 v18, a166\n"
"v_mfma_f32_16x16x32_bf16 a[28:31], a[56:59], a[152:155], a[28:31]\n"
"v_accvgpr_read_b32 v19, a167\n"
"v_lshlrev_b32 v12, 16, v16\n"
"v_mfma_f32_16x16x32_bf16 a[28:31], a[60:63], a[156:159], a[28:31]\n"
"global_load_dwordx4 v[128:131], v0, s[96:97] offset:-3072 sc1\n"
"v_and_b32 v13, s63, v16\n"
"v_mfma_f32_16x16x32_bf16 a[24:27], a[52:55], a[156:159], a[24:27]\n"
"ds_read_b128 a[156:159], v5 offset:15360\n"
"v_lshlrev_b32 v14, 16, v17\n"
"v_mfma_f32_16x16x32_bf16 a[24:27], a[48:51], a[152:155], a[24:27]\n"
"s_waitcnt vmcnt(15) lgkmcnt(13)\n"
"ds_read_b128 a[152:155], v5 offset:14336\n"
"v_and_b32 v15, s63, v17\n"
"v_mfma_f32_16x16x32_bf16 v[144:147], a[64:67], a[96:99], v[144:147]\n"
"s_add_u32 s34, s36, 1544192\n"
"v_mul_f32 v12, v12, v104\n"
"v_mul_f32 v13, v13, v105\n"
"v_mfma_f32_16x16x32_bf16 v[144:147], a[68:71], a[100:103], v[144:147]\n"
"global_load_dwordx4 a[48:51], v0, s[34:35] offset:0 sc1\n"
"v_mul_f32 v14, v14, v106\n"
"v_mfma_f32_16x16x32_bf16 v[172:175], a[76:79], a[100:103], v[172:175]\n"
"v_mul_f32 v15, v15, v107\n"
"v_cvt_pk_bf16_f32 v28, v12, v13\n"
"v_mfma_f32_16x16x32_bf16 v[172:175], a[72:75], a[96:99], v[172:175]\n"
"v_cvt_pk_bf16_f32 v29, v14, v15\n"
"v_mfma_f32_16x16x32_bf16 v[176:179], a[72:75], a[104:107], v[176:179]\n"
"ds_write_b64 v8, v[28:29] offset:32768\n"
"v_mfma_f32_16x16x32_bf16 v[176:179], a[76:79], a[108:111], v[176:179]\n"
"v_lshlrev_b32 v12, 16, v18\n"
"v_and_b32 v13, s63, v18\n"
"v_mfma_f32_16x16x32_bf16 v[148:151], a[68:71], a[108:111], v[148:151]\n"
"v_lshlrev_b32 v14, 16, v19\n"
"v_and_b32 v15, s63, v19\n"
"v_mfma_f32_16x16x32_bf16 v[148:151], a[64:67], a[104:107], v[148:151]\n"
"s_waitcnt lgkmcnt(10)\n"
"v_mul_f32 v12, v12, v132\n"
"v_mul_f32 v13, v13, v133\n"
"v_mfma_f32_16x16x32_bf16 v[152:155], a[64:67], a[112:115], v[152:155]\n"
"buffer_store_dwordx4 v[68:71], v6, s[52:55], s85 offen offset:1024 sc1\n"
"global_load_dwordx4 v[36:39], v0, s[96:97] offset:-2048 sc1\n"
"v_mul_f32 v14, v14, v134\n"
"v_mul_f32 v15, v15, v135\n"
"v_mfma_f32_16x16x32_bf16 v[152:155], a[68:71], a[116:119], v[152:155]\n"
"global_load_dwordx4 a[52:55], v0, s[34:35] offset:1024 sc1\n"
"v_cvt_pk_bf16_f32 v30, v12, v13\n"
"v_mfma_f32_16x16x32_bf16 v[180:183], a[76:79], a[116:119], v[180:183]\n"
"v_cvt_pk_bf16_f32 v31, v14, v15\n"
"v_mfma_f32_16x16x32_bf16 v[180:183], a[72:75], a[112:115], v[180:183]\n"
"ds_write_b64 v8, v[30:31] offset:32776\n"
"v_mfma_f32_16x16x32_bf16 v[184:187], a[72:75], a[120:123], v[184:187]\n"
"ds_read_b128 v[48:51], v2 offset:32768\n"
"v_mfma_f32_16x16x32_bf16 v[184:187], a[76:79], a[124:127], v[184:187]\n"
"ds_read_b128 v[76:79], v2 offset:33792\n"
"v_accvgpr_read_b32 v16, a168\n"
"v_mfma_f32_16x16x32_bf16 v[156:159], a[68:71], a[124:127], v[156:159]\n"
"v_accvgpr_read_b32 v17, a169\n"
"v_accvgpr_read_b32 v18, a170\n"
"v_mfma_f32_16x16x32_bf16 v[156:159], a[64:67], a[120:123], v[156:159]\n"
"s_waitcnt lgkmcnt(9)\n"
"v_accvgpr_read_b32 v19, a171\n"
"v_lshlrev_b32 v12, 16, v16\n"
"v_mfma_f32_16x16x32_bf16 v[160:163], a[64:67], a[128:131], v[160:163]\n"
"v_and_b32 v13, s63, v16\n"
"v_lshlrev_b32 v14, 16, v17\n"
"v_mfma_f32_16x16x32_bf16 v[160:163], a[68:71], a[132:135], v[160:163]\n"
"global_load_dwordx4 a[56:59], v0, s[34:35] offset:2048 sc1\n"
"v_and_b32 v15, s63, v17\n"
"v_mfma_f32_16x16x32_bf16 v[188:191], a[76:79], a[132:135], v[188:191]\n"
"global_load_dwordx4 v[64:67], v0, s[96:97] offset:-1024 sc1\n"
"v_mul_f32 v12, v12, v52\n"
"v_mfma_f32_16x16x32_bf16 v[188:191], a[72:75], a[128:131], v[188:191]\n"
"v_mul_f32 v13, v13, v53\n"
"v_mul_f32 v14, v14, v54\n"
"v_mfma_f32_16x16x32_bf16 v[192:195], a[72:75], a[136:139], v[192:195]\n"
"v_mul_f32 v15, v15, v55\n"
"v_mfma_f32_16x16x32_bf16 v[192:195], a[76:79], a[140:143], v[192:195]\n"
"v_cvt_pk_bf16_f32 v24, v12, v13\n"
"v_cvt_pk_bf16_f32 v25, v14, v15\n"
"v_mfma_f32_16x16x32_bf16 v[164:167], a[68:71], a[140:143], v[164:167]\n"
"ds_write_b64 v7, v[24:25] offset:34816\n"
"v_mfma_f32_16x16x32_bf16 v[164:167], a[64:67], a[136:139], v[164:167]\n"
"s_waitcnt lgkmcnt(5)\n"
"v_lshlrev_b32 v12, 16, v18\n"
"v_and_b32 v13, s63, v18\n"
"v_mfma_f32_16x16x32_bf16 v[168:171], a[64:67], a[144:147], v[168:171]\n"
"v_lshlrev_b32 v14, 16, v19\n"
"v_and_b32 v15, s63, v19\n"
"v_mfma_f32_16x16x32_bf16 v[168:171], a[68:71], a[148:151], v[168:171]\n"
"global_load_dwordx4 a[60:63], v0, s[34:35] offset:3072 sc1\n"
"v_mul_f32 v12, v12, v80\n"
"v_mfma_f32_16x16x32_bf16 v[196:199], a[76:79], a[148:151], v[196:199]\n"
"v_mul_f32 v13, v13, v81\n"
"v_mul_f32 v14, v14, v82\n"
"v_mfma_f32_16x16x32_bf16 v[196:199], a[72:75], a[144:147], v[196:199]\n"
"v_mul_f32 v15, v15, v83\n"
"v_cvt_pk_bf16_f32 v26, v12, v13\n"
"v_mfma_f32_16x16x32_bf16 a[20:23], a[72:75], a[152:155], a[20:23]\n"
"global_load_dwordx4 v[104:107], v0, s[96:97] offset:0 sc1\n"
"v_cvt_pk_bf16_f32 v27, v14, v15\n"
"v_mfma_f32_16x16x32_bf16 a[20:23], a[76:79], a[156:159], a[20:23]\n"
"ds_write_b64 v7, v[26:27] offset:34824\n"
"v_mfma_f32_16x16x32_bf16 a[16:19], a[68:71], a[156:159], a[16:19]\n"
"v_accvgpr_read_b32 v16, a172\n"
"v_mfma_f32_16x16x32_bf16 a[16:19], a[64:67], a[152:155], a[16:19]\n"
"s_waitcnt vmcnt(15)\n"
"v_accvgpr_read_b32 v17, a173\n"
"v_accvgpr_read_b32 v18, a174\n"
"v_mfma_f32_16x16x32_bf16 v[200:203], a[80:83], a[96:99], v[200:203]\n"
"s_add_u32 s34, s36, 1564672\n"
"v_accvgpr_read_b32 v19, a175\n"
"v_lshlrev_b32 v12, 16, v16\n"
"v_mfma_f32_16x16x32_bf16 v[200:203], a[84:87], a[100:103], v[200:203]\n"
"global_load_dwordx4 a[64:67], v0, s[34:35] offset:0 sc1\n"
"v_and_b32 v13, s63, v16\n"
"v_mfma_f32_16x16x32_bf16 v[228:231], a[92:95], a[100:103], v[228:231]\n"
"ds_read_b128 a[100:103], v2 offset:17408\n"
"v_lshlrev_b32 v14, 16, v17\n"
"v_mfma_f32_16x16x32_bf16 v[228:231], a[88:91], a[96:99], v[228:231]\n"
"ds_read_b128 a[96:99], v2 offset:16384\n"
"v_and_b32 v15, s63, v17\n"
"v_mfma_f32_16x16x32_bf16 v[232:235], a[88:91], a[104:107], v[232:235]\n"
"v_mul_f32 v12, v12, v108\n"
"v_mul_f32 v13, v13, v109\n"
"v_mfma_f32_16x16x32_bf16 v[232:235], a[92:95], a[108:111], v[232:235]\n"
"buffer_store_dwordx4 v[44:47], v6, s[52:55], s86 offen offset:2048 sc1\n"
"global_load_dwordx4 v[132:135], v0, s[96:97] offset:1024 sc1\n"
"v_mul_f32 v14, v14, v110\n"
"v_mul_f32 v15, v15, v111\n"
"v_mfma_f32_16x16x32_bf16 v[204:207], a[84:87], a[108:111], v[204:207]\n"
"ds_read_b128 a[108:111], v2 offset:19456\n"
"v_cvt_pk_bf16_f32 v28, v12, v13\n"
"v_mfma_f32_16x16x32_bf16 v[204:207], a[80:83], a[104:107], v[204:207]\n"
"ds_read_b128 a[104:107], v2 offset:18432\n"
"v_cvt_pk_bf16_f32 v29, v14, v15\n"
"v_mfma_f32_16x16x32_bf16 v[208:211], a[80:83], a[112:115], v[208:211]\n"
"ds_write_b64 v8, v[28:29] offset:34816\n"
"v_mfma_f32_16x16x32_bf16 v[208:211], a[84:87], a[116:119], v[208:211]\n"
"global_load_dwordx4 a[68:71], v0, s[34:35] offset:1024 sc1\n"
"v_mfma_f32_16x16x32_bf16 v[236:239], a[92:95], a[116:119], v[236:239]\n"
"ds_read_b128 a[116:119], v2 offset:21504\n"
"v_mfma_f32_16x16x32_bf16 v[236:239], a[88:91], a[112:115], v[236:239]\n"
"ds_read_b128 a[112:115], v2 offset:20480\n"
"v_lshlrev_b32 v12, 16, v18\n"
"v_mfma_f32_16x16x32_bf16 v[240:243], a[88:91], a[120:123], v[240:243]\n"
"v_and_b32 v13, s63, v18\n"
"v_mfma_f32_16x16x32_bf16 v[240:243], a[92:95], a[124:127], v[240:243]\n"
"v_lshlrev_b32 v14, 16, v19\n"
"v_and_b32 v15, s63, v19\n"
"v_mfma_f32_16x16x32_bf16 v[212:215], a[84:87], a[124:127], v[212:215]\n"
"ds_read_b128 a[124:127], v2 offset:23552\n"
"v_mul_f32 v12, v12, v136\n"
"v_mfma_f32_16x16x32_bf16 v[212:215], a[80:83], a[120:123], v[212:215]\n"
"ds_read_b128 a[120:123], v2 offset:22528\n"
"v_mul_f32 v13, v13, v137\n"
"v_mfma_f32_16x16x32_bf16 v[216:219], a[80:83], a[128:131], v[216:219]\n"
"v_mul_f32 v14, v14, v138\n"
"v_mul_f32 v15, v15, v139\n"
"v_mfma_f32_16x16x32_bf16 v[216:219], a[84:87], a[132:135], v[216:219]\n"
"global_load_dwordx4 a[72:75], v0, s[34:35] offset:2048 sc1\n"
"v_cvt_pk_bf16_f32 v30, v12, v13\n"
"v_mfma_f32_16x16x32_bf16 v[244:247], a[92:95], a[132:135], v[244:247]\n"
"ds_read_b128 a[132:135], v2 offset:25600\n"
"v_cvt_pk_bf16_f32 v31, v14, v15\n"
"v_mfma_f32_16x16x32_bf16 v[244:247], a[88:91], a[128:131], v[244:247]\n"
"ds_read_b128 a[128:131], v2 offset:24576\n"
"v_mfma_f32_16x16x32_bf16 v[248:251], a[88:91], a[136:139], v[248:251]\n"
"ds_write_b64 v8, v[30:31] offset:34824\n"
"v_mfma_f32_16x16x32_bf16 v[248:251], a[92:95], a[140:143], v[248:251]\n"
"ds_read_b128 v[52:55], v2 offset:34816\n"
"v_mfma_f32_16x16x32_bf16 v[220:223], a[84:87], a[140:143], v[220:223]\n"
"ds_read_b128 a[140:143], v2 offset:27648\n"
"v_mfma_f32_16x16x32_bf16 v[220:223], a[80:83], a[136:139], v[220:223]\n"
"ds_read_b128 a[136:139], v2 offset:26624\n"
"v_mfma_f32_16x16x32_bf16 v[224:227], a[80:83], a[144:147], v[224:227]\n"
"ds_read_b128 v[80:83], v2 offset:35840\n"
"v_accvgpr_read_b32 v16, a176\n"
"v_mfma_f32_16x16x32_bf16 v[224:227], a[84:87], a[148:151], v[224:227]\n"
"global_load_dwordx4 a[76:79], v0, s[34:35] offset:3072 sc1\n"
"v_accvgpr_read_b32 v17, a177\n"
"v_mfma_f32_16x16x32_bf16 v[252:255], a[92:95], a[148:151], v[252:255]\n"
"ds_read_b128 a[148:151], v2 offset:29696\n"
"v_accvgpr_read_b32 v18, a178\n"
"v_mfma_f32_16x16x32_bf16 v[252:255], a[88:91], a[144:147], v[252:255]\n"
"ds_read_b128 a[144:147], v2 offset:28672\n"
"v_accvgpr_read_b32 v19, a179\n"
"v_mfma_f32_16x16x32_bf16 a[28:31], a[88:91], a[152:155], a[28:31]\n"
"v_lshlrev_b32 v12, 16, v16\n"
"v_and_b32 v13, s63, v16\n"
"v_mfma_f32_16x16x32_bf16 a[28:31], a[92:95], a[156:159], a[28:31]\n"
"v_lshlrev_b32 v14, 16, v17\n"
"v_and_b32 v15, s63, v17\n"
"v_mfma_f32_16x16x32_bf16 a[24:27], a[84:87], a[156:159], a[24:27]\n"
"ds_read_b128 a[156:159], v2 offset:31744\n"
"v_mul_f32 v12, v12, v56\n"
"v_mfma_f32_16x16x32_bf16 a[24:27], a[80:83], a[152:155], a[24:27]\n"
"s_waitcnt vmcnt(15) lgkmcnt(15)\n"
"ds_read_b128 a[152:155], v2 offset:30720\n"
"v_mul_f32 v13, v13, v57\n"
"v_mfma_f32_16x16x32_bf16 v[144:147], a[32:35], a[96:99], v[144:147]\n"
"buffer_store_dwordx4 v[72:75], v6, s[52:55], s87 offen offset:3072 sc1\n"
"s_add_u32 s34, s36, 1568768\n"
"v_mul_f32 v14, v14, v58\n"
"v_mul_f32 v15, v15, v59\n"
"v_mfma_f32_16x16x32_bf16 v[144:147], a[36:39], a[100:103], v[144:147]\n"
"global_load_dwordx4 a[80:83], v0, s[34:35] offset:0 sc1\n"
"v_cvt_pk_bf16_f32 v24, v12, v13\n"
"v_mfma_f32_16x16x32_bf16 v[172:175], a[44:47], a[100:103], v[172:175]\n"
"v_cvt_pk_bf16_f32 v25, v14, v15\n"
"v_mfma_f32_16x16x32_bf16 v[172:175], a[40:43], a[96:99], v[172:175]\n"
"ds_write_b64 v7, v[24:25] offset:36864\n"
"v_mfma_f32_16x16x32_bf16 v[176:179], a[40:43], a[104:107], v[176:179]\n"
"global_load_dwordx4 a[84:87], v0, s[34:35] offset:1024 sc1\n"
"v_lshlrev_b32 v12, 16, v18\n"
"v_mfma_f32_16x16x32_bf16 v[176:179], a[44:47], a[108:111], v[176:179]\n"
"global_load_dwordx4 v[108:111], v0, s[96:97] offset:2048 sc1\n"
"v_and_b32 v13, s63, v18\n"
"v_mfma_f32_16x16x32_bf16 v[148:151], a[36:39], a[108:111], v[148:151]\n"
"v_lshlrev_b32 v14, 16, v19\n"
"v_and_b32 v15, s63, v19\n"
"v_mfma_f32_16x16x32_bf16 v[148:151], a[32:35], a[104:107], v[148:151]\n"
"s_waitcnt lgkmcnt(12)\n"
"global_load_dwordx4 a[88:91], v0, s[34:35] offset:2048 sc1\n"
"v_mul_f32 v12, v12, v84\n"
"v_mfma_f32_16x16x32_bf16 v[152:155], a[32:35], a[112:115], v[152:155]\n"
"v_mul_f32 v13, v13, v85\n"
"v_mul_f32 v14, v14, v86\n"
"v_mfma_f32_16x16x32_bf16 v[152:155], a[36:39], a[116:119], v[152:155]\n"
"v_mul_f32 v15, v15, v87\n"
"v_cvt_pk_bf16_f32 v26, v12, v13\n"
"v_mfma_f32_16x16x32_bf16 v[180:183], a[44:47], a[116:119], v[180:183]\n"
"global_load_dwordx4 a[92:95], v0, s[34:35] offset:3072 sc1\n"
"v_cvt_pk_bf16_f32 v27, v14, v15\n"
"v_mfma_f32_16x16x32_bf16 v[180:183], a[40:43], a[112:115], v[180:183]\n"
"ds_write_b64 v7, v[26:27] offset:36872\n"
"v_mfma_f32_16x16x32_bf16 v[184:187], a[40:43], a[120:123], v[184:187]\n"
"v_accvgpr_read_b32 v16, a180\n"
"v_accvgpr_read_b32 v17, a181\n"
"v_mfma_f32_16x16x32_bf16 v[184:187], a[44:47], a[124:127], v[184:187]\n"
"v_accvgpr_read_b32 v18, a182\n"
"v_accvgpr_read_b32 v19, a183\n"
"v_mfma_f32_16x16x32_bf16 v[156:159], a[36:39], a[124:127], v[156:159]\n"
"global_load_dwordx4 v[136:139], v0, s[96:97] offset:3072 sc1\n"
"v_lshlrev_b32 v12, 16, v16\n"
"v_mfma_f32_16x16x32_bf16 v[156:159], a[32:35], a[120:123], v[156:159]\n"
"s_waitcnt lgkmcnt(7)\n"
"v_and_b32 v13, s63, v16\n"
"v_lshlrev_b32 v14, 16, v17\n"
"v_mfma_f32_16x16x32_bf16 v[160:163], a[32:35], a[128:131], v[160:163]\n"
"v_and_b32 v15, s63, v17\n"
"v_mul_f32 v12, v12, v112\n"
"v_mfma_f32_16x16x32_bf16 v[160:163], a[36:39], a[132:135], v[160:163]\n"
"v_mul_f32 v13, v13, v113\n"
"v_mul_f32 v14, v14, v114\n"
"v_mfma_f32_16x16x32_bf16 v[188:191], a[44:47], a[132:135], v[188:191]\n"
"v_mul_f32 v15, v15, v115\n"
"v_mfma_f32_16x16x32_bf16 v[188:191], a[40:43], a[128:131], v[188:191]\n"
"v_cvt_pk_bf16_f32 v28, v12, v13\n"
"v_cvt_pk_bf16_f32 v29, v14, v15\n"
"v_mfma_f32_16x16x32_bf16 v[192:195], a[40:43], a[136:139], v[192:195]\n"
"ds_write_b64 v8, v[28:29] offset:36864\n"
"v_mfma_f32_16x16x32_bf16 v[192:195], a[44:47], a[140:143], v[192:195]\n"
"v_lshlrev_b32 v12, 16, v18\n"
"v_mfma_f32_16x16x32_bf16 v[164:167], a[36:39], a[140:143], v[164:167]\n"
"v_and_b32 v13, s63, v18\n"
"v_lshlrev_b32 v14, 16, v19\n"
"v_mfma_f32_16x16x32_bf16 v[164:167], a[32:35], a[136:139], v[164:167]\n"
"s_waitcnt lgkmcnt(3)\n"
"v_and_b32 v15, s63, v19\n"
"v_mfma_f32_16x16x32_bf16 v[168:171], a[32:35], a[144:147], v[168:171]\n"
"v_mul_f32 v12, v12, v140\n"
"v_mfma_f32_16x16x32_bf16 v[168:171], a[36:39], a[148:151], v[168:171]\n"
"v_mul_f32 v13, v13, v141\n"
"v_mfma_f32_16x16x32_bf16 v[196:199], a[44:47], a[148:151], v[196:199]\n"
"v_mul_f32 v14, v14, v142\n"
"v_mul_f32 v15, v15, v143\n"
"v_mfma_f32_16x16x32_bf16 v[196:199], a[40:43], a[144:147], v[196:199]\n"
"v_cvt_pk_bf16_f32 v30, v12, v13\n"
"v_mfma_f32_16x16x32_bf16 a[20:23], a[40:43], a[152:155], a[20:23]\n"
"v_cvt_pk_bf16_f32 v31, v14, v15\n"
"v_mfma_f32_16x16x32_bf16 a[20:23], a[44:47], a[156:159], a[20:23]\n"
"buffer_store_dwordx4 v[48:51], v6, s[52:55], s88 offen offset:0 sc1\n"
"ds_write_b64 v8, v[30:31] offset:36872\n"
"v_mfma_f32_16x16x32_bf16 a[16:19], a[36:39], a[156:159], a[16:19]\n"
"ds_read_b128 v[56:59], v2 offset:36864\n"
"v_mfma_f32_16x16x32_bf16 a[16:19], a[32:35], a[152:155], a[16:19]\n"
"s_waitcnt vmcnt(15)\n"
"ds_read_b128 v[84:87], v2 offset:37888\n"
"v_accvgpr_read_b32 v16, a184\n"
"v_mfma_f32_16x16x32_bf16 v[200:203], a[48:51], a[96:99], v[200:203]\n"
"v_accvgpr_read_b32 v17, a185\n"
"v_accvgpr_read_b32 v18, a186\n"
"v_mfma_f32_16x16x32_bf16 v[200:203], a[52:55], a[100:103], v[200:203]\n"
"v_accvgpr_read_b32 v19, a187\n"
"v_mfma_f32_16x16x32_bf16 v[228:231], a[60:63], a[100:103], v[228:231]\n"
"ds_read_b128 a[100:103], v3 offset:17408\n"
"v_lshlrev_b32 v12, 16, v16\n"
"v_mfma_f32_16x16x32_bf16 v[228:231], a[56:59], a[96:99], v[228:231]\n"
"ds_read_b128 a[96:99], v3 offset:16384\n"
"v_and_b32 v13, s63, v16\n"
"v_mfma_f32_16x16x32_bf16 v[232:235], a[56:59], a[104:107], v[232:235]\n"
"v_lshlrev_b32 v14, 16, v17\n"
"v_and_b32 v15, s63, v17\n"
"v_mfma_f32_16x16x32_bf16 v[232:235], a[60:63], a[108:111], v[232:235]\n"
"v_accvgpr_read_b32 v10, a0\n"
"v_mfma_f32_16x16x32_bf16 v[204:207], a[52:55], a[108:111], v[204:207]\n"
"ds_read_b128 a[108:111], v4 offset:17408\n"
"v_accvgpr_read_b32 v11, a1\n"
"v_mfma_f32_16x16x32_bf16 v[204:207], a[48:51], a[104:107], v[204:207]\n"
"ds_read_b128 a[104:107], v4 offset:16384\n"
"v_mul_f32 v12, v12, v10\n"
"v_mfma_f32_16x16x32_bf16 v[208:211], a[48:51], a[112:115], v[208:211]\n"
"v_mul_f32 v13, v13, v11\n"
"v_accvgpr_read_b32 v10, a2\n"
"v_mfma_f32_16x16x32_bf16 v[208:211], a[52:55], a[116:119], v[208:211]\n"
"v_accvgpr_read_b32 v11, a3\n"
"v_mul_f32 v14, v14, v10\n"
"v_mfma_f32_16x16x32_bf16 v[236:239], a[60:63], a[116:119], v[236:239]\n"
"ds_read_b128 a[116:119], v5 offset:17408\n"
"v_mul_f32 v15, v15, v11\n"
"v_mfma_f32_16x16x32_bf16 v[236:239], a[56:59], a[112:115], v[236:239]\n"
"ds_read_b128 a[112:115], v5 offset:16384\n"
"v_cvt_pk_bf16_f32 v24, v12, v13\n"
"v_mfma_f32_16x16x32_bf16 v[240:243], a[56:59], a[120:123], v[240:243]\n"
"v_cvt_pk_bf16_f32 v25, v14, v15\n"
"v_mfma_f32_16x16x32_bf16 v[240:243], a[60:63], a[124:127], v[240:243]\n"
"ds_write_b64 v7, v[24:25] offset:38912\n"
"v_mfma_f32_16x16x32_bf16 v[212:215], a[52:55], a[124:127], v[212:215]\n"
"ds_read_b128 a[124:127], v3 offset:19456\n"
"v_lshlrev_b32 v12, 16, v18\n"
"v_mfma_f32_16x16x32_bf16 v[212:215], a[48:51], a[120:123], v[212:215]\n"
"ds_read_b128 a[120:123], v3 offset:18432\n"
"v_and_b32 v13, s63, v18\n"
"v_mfma_f32_16x16x32_bf16 v[216:219], a[48:51], a[128:131], v[216:219]\n"
"v_lshlrev_b32 v14, 16, v19\n"
"v_and_b32 v15, s63, v19\n"
"v_mfma_f32_16x16x32_bf16 v[216:219], a[52:55], a[132:135], v[216:219]\n"
"v_accvgpr_read_b32 v10, a4\n"
"v_accvgpr_read_b32 v11, a5\n"
"v_mfma_f32_16x16x32_bf16 v[244:247], a[60:63], a[132:135], v[244:247]\n"
"ds_read_b128 a[132:135], v4 offset:19456\n"
"v_mul_f32 v12, v12, v10\n"
"v_mfma_f32_16x16x32_bf16 v[244:247], a[56:59], a[128:131], v[244:247]\n"
"ds_read_b128 a[128:131], v4 offset:18432\n"
"v_mul_f32 v13, v13, v11\n"
"v_mfma_f32_16x16x32_bf16 v[248:251], a[56:59], a[136:139], v[248:251]\n"
"v_accvgpr_read_b32 v10, a6\n"
"v_mfma_f32_16x16x32_bf16 v[248:251], a[60:63], a[140:143], v[248:251]\n"
"v_accvgpr_read_b32 v11, a7\n"
"v_mul_f32 v14, v14, v10\n"
"v_mfma_f32_16x16x32_bf16 v[220:223], a[52:55], a[140:143], v[220:223]\n"
"ds_read_b128 a[140:143], v5 offset:19456\n"
"v_mul_f32 v15, v15, v11\n"
"v_mfma_f32_16x16x32_bf16 v[220:223], a[48:51], a[136:139], v[220:223]\n"
"ds_read_b128 a[136:139], v5 offset:18432\n"
"v_cvt_pk_bf16_f32 v26, v12, v13\n"
"v_mfma_f32_16x16x32_bf16 v[224:227], a[48:51], a[144:147], v[224:227]\n"
"v_cvt_pk_bf16_f32 v27, v14, v15\n"
"v_mfma_f32_16x16x32_bf16 v[224:227], a[52:55], a[148:151], v[224:227]\n"
"buffer_store_dwordx4 v[76:79], v6, s[52:55], s89 offen offset:1024 sc1\n"
"ds_write_b64 v7, v[26:27] offset:38920\n"
"v_mfma_f32_16x16x32_bf16 v[252:255], a[60:63], a[148:151], v[252:255]\n"
"ds_read_b128 a[148:151], v3 offset:21504\n"
"v_accvgpr_read_b32 v16, a188\n"
"v_mfma_f32_16x16x32_bf16 v[252:255], a[56:59], a[144:147], v[252:255]\n"
"ds_read_b128 a[144:147], v3 offset:20480\n"
"v_accvgpr_read_b32 v17, a189\n"
"v_mfma_f32_16x16x32_bf16 a[28:31], a[56:59], a[152:155], a[28:31]\n"
"v_accvgpr_read_b32 v18, a190\n"
"v_accvgpr_read_b32 v19, a191\n"
"v_mfma_f32_16x16x32_bf16 a[28:31], a[60:63], a[156:159], a[28:31]\n"
"v_lshlrev_b32 v12, 16, v16\n"
"v_and_b32 v13, s63, v16\n"
"v_mfma_f32_16x16x32_bf16 a[24:27], a[52:55], a[156:159], a[24:27]\n"
"ds_read_b128 a[156:159], v4 offset:21504\n"
"v_lshlrev_b32 v14, 16, v17\n"
"v_mfma_f32_16x16x32_bf16 a[24:27], a[48:51], a[152:155], a[24:27]\n"
"s_waitcnt lgkmcnt(15)\n"
"ds_read_b128 a[152:155], v4 offset:20480\n"
"v_and_b32 v15, s63, v17\n"
"v_mfma_f32_16x16x32_bf16 v[144:147], v[88:91], a[96:99], v[144:147]\n"
"v_accvgpr_read_b32 v10, a8\n"
"v_accvgpr_read_b32 v11, a9\n"
"v_mfma_f32_16x16x32_bf16 v[144:147], v[116:119], a[100:103], v[144:147]\n"
"v_mul_f32 v12, v12, v10\n"
"v_mul_f32 v13, v13, v11\n"
"v_mfma_f32_16x16x32_bf16 v[172:175], v[120:123], a[100:103], v[172:175]\n"
"v_accvgpr_read_b32 v10, a10\n"
"v_accvgpr_read_b32 v11, a11\n"
"v_mfma_f32_16x16x32_bf16 v[172:175], v[92:95], a[96:99], v[172:175]\n"
"buffer_store_dwordx4 v[52:55], v6, s[52:55], s90 offen offset:2048 sc1\n"
"v_mul_f32 v14, v14, v10\n"
"v_mul_f32 v15, v15, v11\n"
"v_cvt_pk_bf16_f32 v28, v12, v13\n"
"v_cvt_pk_bf16_f32 v29, v14, v15\n"
"v_mfma_f32_16x16x32_bf16 v[200:203], v[32:35], a[96:99], v[200:203]\n"
"ds_write_b64 v8, v[28:29] offset:38912\n"
"v_mfma_f32_16x16x32_bf16 v[200:203], v[60:63], a[100:103], v[200:203]\n"
"v_lshlrev_b32 v12, 16, v18\n"
"v_and_b32 v13, s63, v18\n"
"v_mfma_f32_16x16x32_bf16 v[228:231], v[124:127], a[100:103], v[228:231]\n"
"ds_read_b128 a[100:103], v5 offset:21504\n"
"v_lshlrev_b32 v14, 16, v19\n"
"v_mfma_f32_16x16x32_bf16 v[228:231], v[96:99], a[96:99], v[228:231]\n"
"s_waitcnt vmcnt(3) lgkmcnt(15)\n"
"ds_read_b128 a[96:99], v5 offset:20480\n"
"v_and_b32 v15, s63, v19\n"
"v_mfma_f32_16x16x32_bf16 v[228:231], v[108:111], a[104:107], v[228:231]\n"
"v_accvgpr_read_b32 v10, a12\n"
"v_accvgpr_read_b32 v11, a13\n"
"v_mfma_f32_16x16x32_bf16 v[228:231], v[136:139], a[108:111], v[228:231]\n"
"v_mul_f32 v12, v12, v10\n"
"v_mul_f32 v13, v13, v11\n"
"v_mfma_f32_16x16x32_bf16 v[200:203], v[132:135], a[108:111], v[200:203]\n"
"v_accvgpr_read_b32 v10, a14\n"
"v_accvgpr_read_b32 v11, a15\n"
"v_mfma_f32_16x16x32_bf16 v[200:203], v[104:107], a[104:107], v[200:203]\n"
"v_mul_f32 v14, v14, v10\n"
"v_mul_f32 v15, v15, v11\n"
"v_mfma_f32_16x16x32_bf16 v[172:175], v[36:39], a[104:107], v[172:175]\n"
"v_cvt_pk_bf16_f32 v30, v12, v13\n"
"v_cvt_pk_bf16_f32 v31, v14, v15\n"
"v_mfma_f32_16x16x32_bf16 v[172:175], v[64:67], a[108:111], v[172:175]\n"
"buffer_store_dwordx4 v[80:83], v6, s[52:55], s91 offen offset:3072 sc1\n"
"ds_write_b64 v8, v[30:31] offset:38920\n"
"v_mfma_f32_16x16x32_bf16 v[144:147], v[128:131], a[108:111], v[144:147]\n"
"ds_read_b128 a[108:111], v3 offset:23552\n"
"v_mfma_f32_16x16x32_bf16 v[144:147], v[100:103], a[104:107], v[144:147]\n"
"s_waitcnt lgkmcnt(15)\n"
"ds_read_b128 a[104:107], v3 offset:22528\n"
"v_mfma_f32_16x16x32_bf16 v[144:147], a[64:67], a[112:115], v[144:147]\n"
"v_mfma_f32_16x16x32_bf16 v[144:147], a[68:71], a[116:119], v[144:147]\n"
"v_mfma_f32_16x16x32_bf16 v[172:175], a[76:79], a[116:119], v[172:175]\n"
"v_mfma_f32_16x16x32_bf16 v[172:175], a[72:75], a[112:115], v[172:175]\n"
"ds_read_b128 v[112:115], v2 offset:38912\n"
"v_mfma_f32_16x16x32_bf16 v[200:203], a[80:83], a[112:115], v[200:203]\n"
"ds_read_b128 v[140:143], v2 offset:39936\n"
"v_mfma_f32_16x16x32_bf16 v[200:203], a[84:87], a[116:119], v[200:203]\n"
"v_accvgpr_read_b32 v16, a192\n"
"v_accvgpr_read_b32 v17, a193\n"
"v_mfma_f32_16x16x32_bf16 v[228:231], a[92:95], a[116:119], v[228:231]\n"
"ds_read_b128 a[116:119], v4 offset:23552\n"
"v_accvgpr_read_b32 v18, a194\n"
"v_mfma_f32_16x16x32_bf16 v[228:231], a[88:91], a[112:115], v[228:231]\n"
"s_waitcnt lgkmcnt(15)\n"
"buffer_store_dwordx4 v[56:59], v6, s[52:55], s92 offen offset:0 sc1\n"
"ds_read_b128 a[112:115], v4 offset:22528\n"
"v_accvgpr_read_b32 v19, a195\n"
"v_lshlrev_b32 v12, 16, v16\n"
"v_mfma_f32_16x16x32_bf16 v[232:235], v[96:99], a[120:123], v[232:235]\n"
"v_and_b32 v13, s63, v16\n"
"v_mfma_f32_16x16x32_bf16 v[232:235], v[124:127], a[124:127], v[232:235]\n"
"v_lshlrev_b32 v14, 16, v17\n"
"v_and_b32 v15, s63, v17\n"
"v_mfma_f32_16x16x32_bf16 v[204:207], v[60:63], a[124:127], v[204:207]\n"
"v_mul_f32 v12, v12, v144\n"
"v_mul_f32 v13, v13, v145\n"
"v_mfma_f32_16x16x32_bf16 v[204:207], v[32:35], a[120:123], v[204:207]\n"
"v_mul_f32 v14, v14, v146\n"
"v_mul_f32 v15, v15, v147\n"
"v_mfma_f32_16x16x32_bf16 v[176:179], v[92:95], a[120:123], v[176:179]\n"
"v_cvt_pk_bf16_f32 v24, v12, v13\n"
"v_cvt_pk_bf16_f32 v25, v14, v15\n"
"v_mfma_f32_16x16x32_bf16 v[176:179], v[120:123], a[124:127], v[176:179]\n"
"ds_write_b64 v7, v[24:25] offset:32768\n"
"v_mfma_f32_16x16x32_bf16 v[148:151], v[116:119], a[124:127], v[148:151]\n"
"ds_read_b128 a[124:127], v5 offset:23552\n"
"v_lshlrev_b32 v12, 16, v18\n"
"v_mfma_f32_16x16x32_bf16 v[148:151], v[88:91], a[120:123], v[148:151]\n"
"ds_read_b128 a[120:123], v5 offset:22528\n"
"v_and_b32 v13, s63, v18\n"
"v_mfma_f32_16x16x32_bf16 v[148:151], v[100:103], a[128:131], v[148:151]\n"
"v_lshlrev_b32 v14, 16, v19\n"
"v_mfma_f32_16x16x32_bf16 v[148:151], v[128:131], a[132:135], v[148:151]\n"
"buffer_store_dwordx4 v[84:87], v6, s[52:55], s93 offen offset:1024 sc1\n"
"v_and_b32 v15, s63, v19\n"
"v_mul_f32 v12, v12, v172\n"
"v_mul_f32 v13, v13, v173\n"
"v_mfma_f32_16x16x32_bf16 v[176:179], v[64:67], a[132:135], v[176:179]\n"
"v_mul_f32 v14, v14, v174\n"
"v_mul_f32 v15, v15, v175\n"
"v_mfma_f32_16x16x32_bf16 v[176:179], v[36:39], a[128:131], v[176:179]\n"
"v_cvt_pk_bf16_f32 v26, v12, v13\n"
"v_cvt_pk_bf16_f32 v27, v14, v15\n"
"v_mfma_f32_16x16x32_bf16 v[204:207], v[104:107], a[128:131], v[204:207]\n"
"ds_write_b64 v7, v[26:27] offset:32776\n"
"v_mfma_f32_16x16x32_bf16 v[204:207], v[132:135], a[132:135], v[204:207]\n"
"v_accvgpr_read_b32 v16, a196\n"
"v_mfma_f32_16x16x32_bf16 v[232:235], v[136:139], a[132:135], v[232:235]\n"
"ds_read_b128 a[132:135], v3 offset:25600\n"
"v_accvgpr_read_b32 v17, a197\n"
"v_mfma_f32_16x16x32_bf16 v[232:235], v[108:111], a[128:131], v[232:235]\n"
"s_waitcnt lgkmcnt(15)\n"
"ds_read_b128 a[128:131], v3 offset:24576\n"
"v_accvgpr_read_b32 v18, a198\n"
"v_mfma_f32_16x16x32_bf16 v[232:235], a[88:91], a[136:139], v[232:235]\n"
"v_accvgpr_read_b32 v19, a199\n"
"v_lshlrev_b32 v12, 16, v16\n"
"v_mfma_f32_16x16x32_bf16 v[232:235], a[92:95], a[140:143], v[232:235]\n"
"v_and_b32 v13, s63, v16\n"
"v_lshlrev_b32 v14, 16, v17\n"
"v_mfma_f32_16x16x32_bf16 v[204:207], a[84:87], a[140:143], v[204:207]\n"
"v_and_b32 v15, s63, v17\n"
"v_mul_f32 v12, v12, v200\n"
"v_mfma_f32_16x16x32_bf16 v[204:207], a[80:83], a[136:139], v[204:207]\n"
"s_waitcnt lgkmcnt(9)\n"
"buffer_store_dwordx4 v[112:115], v6, s[52:55], s94 offen offset:2048 sc1\n"
"v_mul_f32 v13, v13, v201\n"
"v_mul_f32 v14, v14, v202\n"
"v_mul_f32 v15, v15, v203\n"
"v_mfma_f32_16x16x32_bf16 v[176:179], a[72:75], a[136:139], v[176:179]\n"
"v_cvt_pk_bf16_f32 v28, v12, v13\n"
"v_cvt_pk_bf16_f32 v29, v14, v15\n"
"v_mfma_f32_16x16x32_bf16 v[176:179], a[76:79], a[140:143], v[176:179]\n"
"ds_write_b64 v8, v[28:29] offset:32768\n"
"v_mfma_f32_16x16x32_bf16 v[148:151], a[68:71], a[140:143], v[148:151]\n"
"ds_read_b128 a[140:143], v4 offset:25600\n"
"v_mfma_f32_16x16x32_bf16 v[148:151], a[64:67], a[136:139], v[148:151]\n"
"ds_read_b128 a[136:139], v4 offset:24576\n"
"v_lshlrev_b32 v12, 16, v18\n"
"v_mfma_f32_16x16x32_bf16 v[152:155], v[88:91], a[144:147], v[152:155]\n"
"v_and_b32 v13, s63, v18\n"
"v_lshlrev_b32 v14, 16, v19\n"
"v_mfma_f32_16x16x32_bf16 v[152:155], v[116:119], a[148:151], v[152:155]\n"
"v_and_b32 v15, s63, v19\n"
"v_mul_f32 v12, v12, v228\n"
"v_mfma_f32_16x16x32_bf16 v[180:183], v[120:123], a[148:151], v[180:183]\n"
"v_mul_f32 v13, v13, v229\n"
"v_mfma_f32_16x16x32_bf16 v[180:183], v[92:95], a[144:147], v[180:183]\n"
"v_mul_f32 v14, v14, v230\n"
"v_mul_f32 v15, v15, v231\n"
"v_mfma_f32_16x16x32_bf16 v[208:211], v[32:35], a[144:147], v[208:211]\n"
"v_cvt_pk_bf16_f32 v30, v12, v13\n"
"v_cvt_pk_bf16_f32 v31, v14, v15\n"
"v_mfma_f32_16x16x32_bf16 v[208:211], v[60:63], a[148:151], v[208:211]\n"
"s_waitcnt lgkmcnt(11)\n"
"buffer_store_dwordx4 v[140:143], v6, s[52:55], s95 offen offset:3072 sc1\n"
"ds_write_b64 v8, v[30:31] offset:32776\n"
"v_mfma_f32_16x16x32_bf16 v[236:239], v[124:127], a[148:151], v[236:239]\n"
"ds_read_b128 a[148:151], v5 offset:25600\n"
"v_mfma_f32_16x16x32_bf16 v[236:239], v[96:99], a[144:147], v[236:239]\n"
"ds_read_b128 a[144:147], v5 offset:24576\n"
"v_mfma_f32_16x16x32_bf16 v[236:239], v[108:111], a[152:155], v[236:239]\n"
"ds_read_b128 v[144:147], v2 offset:32768\n"
"v_mfma_f32_16x16x32_bf16 v[236:239], v[136:139], a[156:159], v[236:239]\n"
"ds_read_b128 v[172:175], v2 offset:33792\n"
"v_accvgpr_read_b32 v16, a200\n"
"v_mfma_f32_16x16x32_bf16 v[208:211], v[132:135], a[156:159], v[208:211]\n"
"v_accvgpr_read_b32 v17, a201\n"
"v_accvgpr_read_b32 v18, a202\n"
"v_mfma_f32_16x16x32_bf16 v[208:211], v[104:107], a[152:155], v[208:211]\n"
"v_accvgpr_read_b32 v19, a203\n"
"v_lshlrev_b32 v12, 16, v16\n"
"v_mfma_f32_16x16x32_bf16 v[180:183], v[36:39], a[152:155], v[180:183]\n"
"v_and_b32 v13, s63, v16\n"
"v_lshlrev_b32 v14, 16, v17\n"
"v_mfma_f32_16x16x32_bf16 v[180:183], v[64:67], a[156:159], v[180:183]\n"
"v_and_b32 v15, s63, v17\n"
"v_mul_f32 v12, v12, v148\n"
"v_mfma_f32_16x16x32_bf16 v[152:155], v[128:131], a[156:159], v[152:155]\n"
"ds_read_b128 a[156:159], v3 offset:27648\n"
"v_mul_f32 v13, v13, v149\n"
"v_mfma_f32_16x16x32_bf16 v[152:155], v[100:103], a[152:155], v[152:155]\n"
"ds_read_b128 a[152:155], v3 offset:26624\n"
"v_mul_f32 v14, v14, v150\n"
"v_mfma_f32_16x16x32_bf16 v[152:155], a[64:67], a[96:99], v[152:155]\n"
"v_mul_f32 v15, v15, v151\n"
"v_cvt_pk_bf16_f32 v24, v12, v13\n"
"v_mfma_f32_16x16x32_bf16 v[152:155], a[68:71], a[100:103], v[152:155]\n"
"v_cvt_pk_bf16_f32 v25, v14, v15\n"
"v_mfma_f32_16x16x32_bf16 v[180:183], a[76:79], a[100:103], v[180:183]\n"
"s_waitcnt lgkmcnt(3)\n"
"buffer_store_dwordx4 v[144:147], v6, s[52:55], s80 offen offset:128 sc1\n"
"ds_write_b64 v7, v[24:25] offset:34816\n"
"v_mfma_f32_16x16x32_bf16 v[180:183], a[72:75], a[96:99], v[180:183]\n"
"v_lshlrev_b32 v12, 16, v18\n"
"v_and_b32 v13, s63, v18\n"
"v_mfma_f32_16x16x32_bf16 v[208:211], a[80:83], a[96:99], v[208:211]\n"
"v_lshlrev_b32 v14, 16, v19\n"
"v_and_b32 v15, s63, v19\n"
"v_mfma_f32_16x16x32_bf16 v[208:211], a[84:87], a[100:103], v[208:211]\n"
"v_mul_f32 v12, v12, v176\n"
"v_mul_f32 v13, v13, v177\n"
"v_mfma_f32_16x16x32_bf16 v[236:239], a[92:95], a[100:103], v[236:239]\n"
"ds_read_b128 a[100:103], v4 offset:27648\n"
"v_mul_f32 v14, v14, v178\n"
"v_mfma_f32_16x16x32_bf16 v[236:239], a[88:91], a[96:99], v[236:239]\n"
"ds_read_b128 a[96:99], v4 offset:26624\n"
"v_mul_f32 v15, v15, v179\n"
"v_mfma_f32_16x16x32_bf16 v[240:243], v[96:99], a[104:107], v[240:243]\n"
"v_cvt_pk_bf16_f32 v26, v12, v13\n"
"v_cvt_pk_bf16_f32 v27, v14, v15\n"
"v_mfma_f32_16x16x32_bf16 v[240:243], v[124:127], a[108:111], v[240:243]\n"
"ds_write_b64 v7, v[26:27] offset:34824\n"
"v_mfma_f32_16x16x32_bf16 v[212:215], v[60:63], a[108:111], v[212:215]\n"
"v_accvgpr_read_b32 v16, a204\n"
"v_accvgpr_read_b32 v17, a205\n"
"v_mfma_f32_16x16x32_bf16 v[212:215], v[32:35], a[104:107], v[212:215]\n"
"v_accvgpr_read_b32 v18, a206\n"
"v_accvgpr_read_b32 v19, a207\n"
"v_mfma_f32_16x16x32_bf16 v[184:187], v[92:95], a[104:107], v[184:187]\n"
"s_waitcnt lgkmcnt(6)\n"
"buffer_store_dwordx4 v[172:175], v6, s[52:55], s81 offen offset:1152 sc1\n"
"v_lshlrev_b32 v12, 16, v16\n"
"v_and_b32 v13, s63, v16\n"
"v_lshlrev_b32 v14, 16, v17\n"
"v_mfma_f32_16x16x32_bf16 v[184:187], v[120:123], a[108:111], v[184:187]\n"
"v_and_b32 v15, s63, v17\n"
"v_mul_f32 v12, v12, v204\n"
"v_mfma_f32_16x16x32_bf16 v[156:159], v[116:119], a[108:111], v[156:159]\n"
"ds_read_b128 a[108:111], v5 offset:27648\n"
"v_mul_f32 v13, v13, v205\n"
"v_mfma_f32_16x16x32_bf16 v[156:159], v[88:91], a[104:107], v[156:159]\n"
"ds_read_b128 a[104:107], v5 offset:26624\n"
"v_mul_f32 v14, v14, v206\n"
"v_mfma_f32_16x16x32_bf16 v[156:159], v[100:103], a[112:115], v[156:159]\n"
"v_mul_f32 v15, v15, v207\n"
"v_cvt_pk_bf16_f32 v28, v12, v13\n"
"v_mfma_f32_16x16x32_bf16 v[156:159], v[128:131], a[116:119], v[156:159]\n"
"v_cvt_pk_bf16_f32 v29, v14, v15\n"
"v_mfma_f32_16x16x32_bf16 v[184:187], v[64:67], a[116:119], v[184:187]\n"
"ds_write_b64 v8, v[28:29] offset:34816\n"
"v_mfma_f32_16x16x32_bf16 v[184:187], v[36:39], a[112:115], v[184:187]\n"
"v_lshlrev_b32 v12, 16, v18\n"
"v_and_b32 v13, s63, v18\n"
"v_mfma_f32_16x16x32_bf16 v[212:215], v[104:107], a[112:115], v[212:215]\n"
"v_lshlrev_b32 v14, 16, v19\n"
"v_and_b32 v15, s63, v19\n"
"v_mfma_f32_16x16x32_bf16 v[212:215], v[132:135], a[116:119], v[212:215]\n"
"v_mul_f32 v12, v12, v232\n"
"v_mul_f32 v13, v13, v233\n"
"v_mfma_f32_16x16x32_bf16 v[240:243], v[136:139], a[116:119], v[240:243]\n"
"ds_read_b128 a[116:119], v3 offset:29696\n"
"v_mul_f32 v14, v14, v234\n"
"v_mfma_f32_16x16x32_bf16 v[240:243], v[108:111], a[112:115], v[240:243]\n"
"ds_read_b128 a[112:115], v3 offset:28672\n"
"v_mul_f32 v15, v15, v235\n"
"v_mfma_f32_16x16x32_bf16 v[240:243], a[88:91], a[120:123], v[240:243]\n"
"v_cvt_pk_bf16_f32 v30, v12, v13\n"
"v_cvt_pk_bf16_f32 v31, v14, v15\n"
"v_mfma_f32_16x16x32_bf16 v[240:243], a[92:95], a[124:127], v[240:243]\n"
"ds_write_b64 v8, v[30:31] offset:34824\n"
"v_mfma_f32_16x16x32_bf16 v[212:215], a[84:87], a[124:127], v[212:215]\n"
"ds_read_b128 v[148:151], v2 offset:34816\n"
"v_mfma_f32_16x16x32_bf16 v[212:215], a[80:83], a[120:123], v[212:215]\n"
"ds_read_b128 v[176:179], v2 offset:35840\n"
"v_accvgpr_read_b32 v16, a208\n"
"v_mfma_f32_16x16x32_bf16 v[184:187], a[72:75], a[120:123], v[184:187]\n"
"v_accvgpr_read_b32 v17, a209\n"
"v_accvgpr_read_b32 v18, a210\n"
"v_mfma_f32_16x16x32_bf16 v[184:187], a[76:79], a[124:127], v[184:187]\n"
"v_accvgpr_read_b32 v19, a211\n"
"v_lshlrev_b32 v12, 16, v16\n"
"v_mfma_f32_16x16x32_bf16 v[156:159], a[68:71], a[124:127], v[156:159]\n"
"ds_read_b128 a[124:127], v4 offset:29696\n"
"v_and_b32 v13, s63, v16\n"
"v_mfma_f32_16x16x32_bf16 v[156:159], a[64:67], a[120:123], v[156:159]\n"
"ds_read_b128 a[120:123], v4 offset:28672\n"
"v_lshlrev_b32 v14, 16, v17\n"
"v_mfma_f32_16x16x32_bf16 v[160:163], v[88:91], a[128:131], v[160:163]\n"
"v_and_b32 v15, s63, v17\n"
"v_mul_f32 v12, v12, v152\n"
"v_mfma_f32_16x16x32_bf16 v[160:163], v[116:119], a[132:135], v[160:163]\n"
"v_mul_f32 v13, v13, v153\n"
"v_mul_f32 v14, v14, v154\n"
"v_mfma_f32_16x16x32_bf16 v[188:191], v[120:123], a[132:135], v[188:191]\n"
"v_mul_f32 v15, v15, v155\n"
"v_cvt_pk_bf16_f32 v24, v12, v13\n"
"v_mfma_f32_16x16x32_bf16 v[188:191], v[92:95], a[128:131], v[188:191]\n"
"v_cvt_pk_bf16_f32 v25, v14, v15\n"
"v_mfma_f32_16x16x32_bf16 v[216:219], v[32:35], a[128:131], v[216:219]\n"
"s_waitcnt lgkmcnt(3)\n"
"buffer_store_dwordx4 v[148:151], v6, s[52:55], s82 offen offset:2176 sc1\n"
"ds_write_b64 v7, v[24:25] offset:36864\n"
"v_mfma_f32_16x16x32_bf16 v[216:219], v[60:63], a[132:135], v[216:219]\n"
"v_lshlrev_b32 v12, 16, v18\n"
"v_and_b32 v13, s63, v18\n"
"v_mfma_f32_16x16x32_bf16 v[244:247], v[124:127], a[132:135], v[244:247]\n"
"ds_read_b128 a[132:135], v5 offset:29696\n"
"v_lshlrev_b32 v14, 16, v19\n"
"v_mfma_f32_16x16x32_bf16 v[244:247], v[96:99], a[128:131], v[244:247]\n"
"ds_read_b128 a[128:131], v5 offset:28672\n"
"v_and_b32 v15, s63, v19\n"
"v_mfma_f32_16x16x32_bf16 v[244:247], v[108:111], a[136:139], v[244:247]\n"
"v_mul_f32 v12, v12, v180\n"
"v_mul_f32 v13, v13, v181\n"
"v_mfma_f32_16x16x32_bf16 v[244:247], v[136:139], a[140:143], v[244:247]\n"
"v_mul_f32 v14, v14, v182\n"
"v_mul_f32 v15, v15, v183\n"
"v_mfma_f32_16x16x32_bf16 v[216:219], v[132:135], a[140:143], v[216:219]\n"
"v_cvt_pk_bf16_f32 v26, v12, v13\n"
"v_cvt_pk_bf16_f32 v27, v14, v15\n"
"v_mfma_f32_16x16x32_bf16 v[216:219], v[104:107], a[136:139], v[216:219]\n"
"ds_write_b64 v7, v[26:27] offset:36872\n"
"v_mfma_f32_16x16x32_bf16 v[188:191], v[36:39], a[136:139], v[188:191]\n"
"v_accvgpr_read_b32 v16, a212\n"
"v_accvgpr_read_b32 v17, a213\n"
"v_mfma_f32_16x16x32_bf16 v[188:191], v[64:67], a[140:143], v[188:191]\n"
"v_accvgpr_read_b32 v18, a214\n"
"v_accvgpr_read_b32 v19, a215\n"
"v_mfma_f32_16x16x32_bf16 v[160:163], v[128:131], a[140:143], v[160:163]\n"
"s_waitcnt lgkmcnt(6)\n"
"buffer_store_dwordx4 v[176:179], v6, s[52:55], s83 offen offset:3200 sc1\n"
"ds_read_b128 a[140:143], v3 offset:31744\n"
"v_lshlrev_b32 v12, 16, v16\n"
"v_and_b32 v13, s63, v16\n"
"v_mfma_f32_16x16x32_bf16 v[160:163], v[100:103], a[136:139], v[160:163]\n"
"ds_read_b128 a[136:139], v3 offset:30720\n"
"v_lshlrev_b32 v14, 16, v17\n"
"v_mfma_f32_16x16x32_bf16 v[160:163], a[64:67], a[144:147], v[160:163]\n"
"v_and_b32 v15, s63, v17\n"
"v_mul_f32 v12, v12, v208\n"
"v_mfma_f32_16x16x32_bf16 v[160:163], a[68:71], a[148:151], v[160:163]\n"
"v_mul_f32 v13, v13, v209\n"
"v_mul_f32 v14, v14, v210\n"
"v_mfma_f32_16x16x32_bf16 v[188:191], a[76:79], a[148:151], v[188:191]\n"
"v_mul_f32 v15, v15, v211\n"
"v_cvt_pk_bf16_f32 v28, v12, v13\n"
"v_mfma_f32_16x16x32_bf16 v[188:191], a[72:75], a[144:147], v[188:191]\n"
"v_cvt_pk_bf16_f32 v29, v14, v15\n"
"v_mfma_f32_16x16x32_bf16 v[216:219], a[80:83], a[144:147], v[216:219]\n"
"ds_write_b64 v8, v[28:29] offset:36864\n"
"v_mfma_f32_16x16x32_bf16 v[216:219], a[84:87], a[148:151], v[216:219]\n"
"v_lshlrev_b32 v12, 16, v18\n"
"v_and_b32 v13, s63, v18\n"
"v_mfma_f32_16x16x32_bf16 v[244:247], a[92:95], a[148:151], v[244:247]\n"
"ds_read_b128 a[148:151], v4 offset:31744\n"
"v_lshlrev_b32 v14, 16, v19\n"
"v_mfma_f32_16x16x32_bf16 v[244:247], a[88:91], a[144:147], v[244:247]\n"
"ds_read_b128 a[144:147], v4 offset:30720\n"
"v_and_b32 v15, s63, v19\n"
"v_mfma_f32_16x16x32_bf16 v[248:251], v[96:99], a[152:155], v[248:251]\n"
"v_mul_f32 v12, v12, v236\n"
"v_mul_f32 v13, v13, v237\n"
"v_mfma_f32_16x16x32_bf16 v[248:251], v[124:127], a[156:159], v[248:251]\n"
"v_mul_f32 v14, v14, v238\n"
"v_mul_f32 v15, v15, v239\n"
"v_mfma_f32_16x16x32_bf16 v[220:223], v[60:63], a[156:159], v[220:223]\n"
"v_cvt_pk_bf16_f32 v30, v12, v13\n"
"v_cvt_pk_bf16_f32 v31, v14, v15\n"
"v_mfma_f32_16x16x32_bf16 v[220:223], v[32:35], a[152:155], v[220:223]\n"
"ds_write_b64 v8, v[30:31] offset:36872\n"
"v_mfma_f32_16x16x32_bf16 v[192:195], v[92:95], a[152:155], v[192:195]\n"
"ds_read_b128 v[152:155], v2 offset:36864\n"
"v_mfma_f32_16x16x32_bf16 v[192:195], v[120:123], a[156:159], v[192:195]\n"
"ds_read_b128 v[180:183], v2 offset:37888\n"
"v_accvgpr_read_b32 v16, a216\n"
"v_mfma_f32_16x16x32_bf16 v[164:167], v[116:119], a[156:159], v[164:167]\n"
"ds_read_b128 a[156:159], v5 offset:31744\n"
"v_accvgpr_read_b32 v17, a217\n"
"v_mfma_f32_16x16x32_bf16 v[164:167], v[88:91], a[152:155], v[164:167]\n"
"ds_read_b128 a[152:155], v5 offset:30720\n"
"v_accvgpr_read_b32 v18, a218\n"
"v_mfma_f32_16x16x32_bf16 v[164:167], v[100:103], a[96:99], v[164:167]\n"
"v_accvgpr_read_b32 v19, a219\n"
"v_lshlrev_b32 v12, 16, v16\n"
"v_mfma_f32_16x16x32_bf16 v[164:167], v[128:131], a[100:103], v[164:167]\n"
"v_and_b32 v13, s63, v16\n"
"v_lshlrev_b32 v14, 16, v17\n"
"v_mfma_f32_16x16x32_bf16 v[192:195], v[64:67], a[100:103], v[192:195]\n"
"v_and_b32 v15, s63, v17\n"
"v_mul_f32 v12, v12, v156\n"
"v_mfma_f32_16x16x32_bf16 v[192:195], v[36:39], a[96:99], v[192:195]\n"
"v_mul_f32 v13, v13, v157\n"
"v_mul_f32 v14, v14, v158\n"
"v_mfma_f32_16x16x32_bf16 v[220:223], v[104:107], a[96:99], v[220:223]\n"
"v_mul_f32 v15, v15, v159\n"
"v_mfma_f32_16x16x32_bf16 v[220:223], v[132:135], a[100:103], v[220:223]\n"
"v_cvt_pk_bf16_f32 v24, v12, v13\n"
"v_mfma_f32_16x16x32_bf16 v[248:251], v[136:139], a[100:103], v[248:251]\n"
"v_cvt_pk_bf16_f32 v25, v14, v15\n"
"v_mfma_f32_16x16x32_bf16 v[248:251], v[108:111], a[96:99], v[248:251]\n"
"s_waitcnt lgkmcnt(3)\n"
"buffer_store_dwordx4 v[152:155], v6, s[52:55], s84 offen offset:128 sc1\n"
"ds_write_b64 v7, v[24:25] offset:38912\n"
"v_mfma_f32_16x16x32_bf16 v[248:251], a[88:91], a[104:107], v[248:251]\n"
"v_lshlrev_b32 v12, 16, v18\n"
"v_and_b32 v13, s63, v18\n"
"v_mfma_f32_16x16x32_bf16 v[248:251], a[92:95], a[108:111], v[248:251]\n"
"v_lshlrev_b32 v14, 16, v19\n"
"v_mfma_f32_16x16x32_bf16 v[220:223], a[84:87], a[108:111], v[220:223]\n"
"v_and_b32 v15, s63, v19\n"
"v_mfma_f32_16x16x32_bf16 v[220:223], a[80:83], a[104:107], v[220:223]\n"
"v_mul_f32 v12, v12, v184\n"
"v_mfma_f32_16x16x32_bf16 v[192:195], a[72:75], a[104:107], v[192:195]\n"
"v_mul_f32 v13, v13, v185\n"
"v_mfma_f32_16x16x32_bf16 v[192:195], a[76:79], a[108:111], v[192:195]\n"
"v_mul_f32 v14, v14, v186\n"
"v_mfma_f32_16x16x32_bf16 v[164:167], a[68:71], a[108:111], v[164:167]\n"
"v_mul_f32 v15, v15, v187\n"
"v_mfma_f32_16x16x32_bf16 v[164:167], a[64:67], a[104:107], v[164:167]\n"
"v_cvt_pk_bf16_f32 v26, v12, v13\n"
"v_mfma_f32_16x16x32_bf16 v[168:171], v[88:91], a[112:115], v[168:171]\n"
"v_cvt_pk_bf16_f32 v27, v14, v15\n"
"v_mfma_f32_16x16x32_bf16 v[168:171], v[116:119], a[116:119], v[168:171]\n"
"s_waitcnt lgkmcnt(3)\n"
"buffer_store_dwordx4 v[180:183], v6, s[52:55], s85 offen offset:1152 sc1\n"
"ds_write_b64 v7, v[26:27] offset:38920\n"
"v_mfma_f32_16x16x32_bf16 v[196:199], v[120:123], a[116:119], v[196:199]\n"
"v_accvgpr_read_b32 v16, a220\n"
"v_accvgpr_read_b32 v17, a221\n"
"v_mfma_f32_16x16x32_bf16 v[196:199], v[92:95], a[112:115], v[196:199]\n"
"v_accvgpr_read_b32 v18, a222\n"
"v_accvgpr_read_b32 v19, a223\n"
"v_mfma_f32_16x16x32_bf16 v[224:227], v[32:35], a[112:115], v[224:227]\n"
"v_lshlrev_b32 v12, 16, v16\n"
"v_and_b32 v13, s63, v16\n"
"v_mfma_f32_16x16x32_bf16 v[224:227], v[60:63], a[116:119], v[224:227]\n"
"v_lshlrev_b32 v14, 16, v17\n"
"v_and_b32 v15, s63, v17\n"
"v_mfma_f32_16x16x32_bf16 v[252:255], v[124:127], a[116:119], v[252:255]\n"
"v_mul_f32 v12, v12, v212\n"
"v_mul_f32 v13, v13, v213\n"
"v_mfma_f32_16x16x32_bf16 v[252:255], v[96:99], a[112:115], v[252:255]\n"
"v_mul_f32 v14, v14, v214\n"
"v_mul_f32 v15, v15, v215\n"
"v_mfma_f32_16x16x32_bf16 v[252:255], v[108:111], a[120:123], v[252:255]\n"
"v_cvt_pk_bf16_f32 v28, v12, v13\n"
"v_cvt_pk_bf16_f32 v29, v14, v15\n"
"v_mfma_f32_16x16x32_bf16 v[252:255], v[136:139], a[124:127], v[252:255]\n"
"ds_write_b64 v8, v[28:29] offset:38912\n"
"v_mfma_f32_16x16x32_bf16 v[224:227], v[132:135], a[124:127], v[224:227]\n"
"v_lshlrev_b32 v12, 16, v18\n"
"v_and_b32 v13, s63, v18\n"
"v_mfma_f32_16x16x32_bf16 v[224:227], v[104:107], a[120:123], v[224:227]\n"
"v_lshlrev_b32 v14, 16, v19\n"
"v_and_b32 v15, s63, v19\n"
"v_mfma_f32_16x16x32_bf16 v[196:199], v[36:39], a[120:123], v[196:199]\n"
"v_mul_f32 v12, v12, v240\n"
"v_mul_f32 v13, v13, v241\n"
"v_mfma_f32_16x16x32_bf16 v[196:199], v[64:67], a[124:127], v[196:199]\n"
"v_mul_f32 v14, v14, v242\n"
"v_mul_f32 v15, v15, v243\n"
"v_mfma_f32_16x16x32_bf16 v[168:171], v[128:131], a[124:127], v[168:171]\n"
"v_cvt_pk_bf16_f32 v30, v12, v13\n"
"v_cvt_pk_bf16_f32 v31, v14, v15\n"
"v_mfma_f32_16x16x32_bf16 v[168:171], v[100:103], a[120:123], v[168:171]\n"
"ds_write_b64 v8, v[30:31] offset:38920\n"
"v_mfma_f32_16x16x32_bf16 v[168:171], a[64:67], a[128:131], v[168:171]\n"
"ds_read_b128 v[156:159], v2 offset:38912\n"
"v_mfma_f32_16x16x32_bf16 v[168:171], a[68:71], a[132:135], v[168:171]\n"
"ds_read_b128 v[184:187], v2 offset:39936\n"
"v_accvgpr_read_b32 v16, a224\n"
"v_mfma_f32_16x16x32_bf16 v[196:199], a[76:79], a[132:135], v[196:199]\n"
"v_accvgpr_read_b32 v17, a225\n"
"v_accvgpr_read_b32 v18, a226\n"
"v_mfma_f32_16x16x32_bf16 v[196:199], a[72:75], a[128:131], v[196:199]\n"
"v_accvgpr_read_b32 v19, a227\n"
"v_lshlrev_b32 v12, 16, v16\n"
"v_mfma_f32_16x16x32_bf16 v[224:227], a[80:83], a[128:131], v[224:227]\n"
"v_and_b32 v13, s63, v16\n"
"v_lshlrev_b32 v14, 16, v17\n"
"v_mfma_f32_16x16x32_bf16 v[224:227], a[84:87], a[132:135], v[224:227]\n"
"v_and_b32 v15, s63, v17\n"
"v_mul_f32 v12, v12, v160\n"
"v_mfma_f32_16x16x32_bf16 v[252:255], a[92:95], a[132:135], v[252:255]\n"
"v_mul_f32 v13, v13, v161\n"
"v_mul_f32 v14, v14, v162\n"
"v_mfma_f32_16x16x32_bf16 v[252:255], a[88:91], a[128:131], v[252:255]\n"
"v_mul_f32 v15, v15, v163\n"
"v_cvt_pk_bf16_f32 v24, v12, v13\n"
"v_mfma_f32_16x16x32_bf16 a[28:31], v[96:99], a[136:139], a[28:31]\n"
"v_cvt_pk_bf16_f32 v25, v14, v15\n"
"v_mfma_f32_16x16x32_bf16 a[28:31], v[124:127], a[140:143], a[28:31]\n"
"s_waitcnt lgkmcnt(1)\n"
"buffer_store_dwordx4 v[156:159], v6, s[52:55], s86 offen offset:2176 sc1\n"
"ds_write_b64 v7, v[24:25] offset:32768\n"
"v_mfma_f32_16x16x32_bf16 a[24:27], v[60:63], a[140:143], a[24:27]\n"
"v_lshlrev_b32 v12, 16, v18\n"
"v_and_b32 v13, s63, v18\n"
"v_mfma_f32_16x16x32_bf16 a[24:27], v[32:35], a[136:139], a[24:27]\n"
"v_lshlrev_b32 v14, 16, v19\n"
"v_and_b32 v15, s63, v19\n"
"v_mfma_f32_16x16x32_bf16 a[20:23], v[92:95], a[136:139], a[20:23]\n"
"v_mul_f32 v12, v12, v188\n"
"v_mul_f32 v13, v13, v189\n"
"v_mfma_f32_16x16x32_bf16 a[20:23], v[120:123], a[140:143], a[20:23]\n"
"v_mul_f32 v14, v14, v190\n"
"v_mul_f32 v15, v15, v191\n"
"v_mfma_f32_16x16x32_bf16 a[16:19], v[116:119], a[140:143], a[16:19]\n"
"v_cvt_pk_bf16_f32 v26, v12, v13\n"
"v_cvt_pk_bf16_f32 v27, v14, v15\n"
"v_mfma_f32_16x16x32_bf16 a[16:19], v[88:91], a[136:139], a[16:19]\n"
"ds_write_b64 v7, v[26:27] offset:32776\n"
"v_mfma_f32_16x16x32_bf16 a[16:19], v[100:103], a[144:147], a[16:19]\n"
"v_accvgpr_read_b32 v16, a228\n"
"v_accvgpr_read_b32 v17, a229\n"
"v_mfma_f32_16x16x32_bf16 a[16:19], v[128:131], a[148:151], a[16:19]\n"
"v_accvgpr_read_b32 v18, a230\n"
"v_accvgpr_read_b32 v19, a231\n"
"v_mfma_f32_16x16x32_bf16 a[20:23], v[64:67], a[148:151], a[20:23]\n"
"v_lshlrev_b32 v12, 16, v16\n"
"v_and_b32 v13, s63, v16\n"
"v_mfma_f32_16x16x32_bf16 a[20:23], v[36:39], a[144:147], a[20:23]\n"
"s_waitcnt lgkmcnt(2)\n"
"buffer_store_dwordx4 v[184:187], v6, s[52:55], s87 offen offset:3200 sc1\n"
"v_lshlrev_b32 v14, 16, v17\n"
"v_and_b32 v15, s63, v17\n"
"v_mul_f32 v12, v12, v216\n"
"v_mfma_f32_16x16x32_bf16 a[24:27], v[104:107], a[144:147], a[24:27]\n"
"v_mul_f32 v13, v13, v217\n"
"v_mul_f32 v14, v14, v218\n"
"v_mfma_f32_16x16x32_bf16 a[24:27], v[132:135], a[148:151], a[24:27]\n"
"v_mul_f32 v15, v15, v219\n"
"v_mfma_f32_16x16x32_bf16 a[28:31], v[136:139], a[148:151], a[28:31]\n"
"v_cvt_pk_bf16_f32 v28, v12, v13\n"
"v_cvt_pk_bf16_f32 v29, v14, v15\n"
"v_mfma_f32_16x16x32_bf16 a[28:31], v[108:111], a[144:147], a[28:31]\n"
"ds_write_b64 v8, v[28:29] offset:32768\n"
"v_mfma_f32_16x16x32_bf16 a[28:31], a[88:91], a[152:155], a[28:31]\n"
"v_lshlrev_b32 v12, 16, v18\n"
"v_and_b32 v13, s63, v18\n"
"v_mfma_f32_16x16x32_bf16 a[28:31], a[92:95], a[156:159], a[28:31]\n"
"v_lshlrev_b32 v14, 16, v19\n"
"v_and_b32 v15, s63, v19\n"
"v_mfma_f32_16x16x32_bf16 a[24:27], a[84:87], a[156:159], a[24:27]\n"
"v_mul_f32 v12, v12, v244\n"
"v_mul_f32 v13, v13, v245\n"
"v_mfma_f32_16x16x32_bf16 a[24:27], a[80:83], a[152:155], a[24:27]\n"
"v_mul_f32 v14, v14, v246\n"
"v_mul_f32 v15, v15, v247\n"
"v_mfma_f32_16x16x32_bf16 a[20:23], a[72:75], a[152:155], a[20:23]\n"
"s_add_u32 s62, s62, 1\n"
"v_cvt_pk_bf16_f32 v30, v12, v13\n"
"v_cvt_pk_bf16_f32 v31, v14, v15\n"
"v_mfma_f32_16x16x32_bf16 a[20:23], a[76:79], a[156:159], a[20:23]\n"
"ds_write_b64 v8, v[30:31] offset:32776\n"
"v_mfma_f32_16x16x32_bf16 a[16:19], a[68:71], a[156:159], a[16:19]\n"
"ds_read_b128 v[160:163], v2 offset:32768\n"
"v_mfma_f32_16x16x32_bf16 a[16:19], a[64:67], a[152:155], a[16:19]\n"
"ds_read_b128 v[188:191], v2 offset:33792\n"
"s_nop 7\n"
"v_accvgpr_read_b32 v16, a232\n"
"v_accvgpr_read_b32 v17, a233\n"
"v_accvgpr_read_b32 v18, a234\n"
"v_accvgpr_read_b32 v19, a235\n"
"v_lshlrev_b32 v12, 16, v16\n"
"v_and_b32 v13, s63, v16\n"
"v_lshlrev_b32 v14, 16, v17\n"
"v_and_b32 v15, s63, v17\n"
"v_pk_mul_f32 v[12:13], v[12:13], v[164:165]\n"
"v_pk_mul_f32 v[14:15], v[14:15], v[166:167]\n"
"v_cvt_pk_bf16_f32 v24, v12, v13\n"
"v_cvt_pk_bf16_f32 v25, v14, v15\n"
"v_lshlrev_b32 v12, 16, v18\n"
"v_and_b32 v13, s63, v18\n"
"v_lshlrev_b32 v14, 16, v19\n"
"v_and_b32 v15, s63, v19\n"
"v_pk_mul_f32 v[12:13], v[12:13], v[192:193]\n"
"v_pk_mul_f32 v[14:15], v[14:15], v[194:195]\n"
"v_cvt_pk_bf16_f32 v26, v12, v13\n"
"v_cvt_pk_bf16_f32 v27, v14, v15\n"
"ds_write_b128 v7, v[24:27] offset:34816\n"
"s_waitcnt lgkmcnt(2)\n"
"buffer_store_dwordx4 v[160:163], v6, s[52:55], s88 offen offset:128 sc1\n"
"s_waitcnt lgkmcnt(1)\n"
"buffer_store_dwordx4 v[188:191], v6, s[52:55], s89 offen offset:1152 sc1\n"
"v_accvgpr_read_b32 v16, a236\n"
"v_accvgpr_read_b32 v17, a237\n"
"v_accvgpr_read_b32 v18, a238\n"
"v_accvgpr_read_b32 v19, a239\n"
"v_lshlrev_b32 v12, 16, v16\n"
"v_and_b32 v13, s63, v16\n"
"v_lshlrev_b32 v14, 16, v17\n"
"v_and_b32 v15, s63, v17\n"
"v_pk_mul_f32 v[12:13], v[12:13], v[220:221]\n"
"v_pk_mul_f32 v[14:15], v[14:15], v[222:223]\n"
"v_cvt_pk_bf16_f32 v28, v12, v13\n"
"v_cvt_pk_bf16_f32 v29, v14, v15\n"
"v_lshlrev_b32 v12, 16, v18\n"
"v_and_b32 v13, s63, v18\n"
"v_lshlrev_b32 v14, 16, v19\n"
"v_and_b32 v15, s63, v19\n"
"v_pk_mul_f32 v[12:13], v[12:13], v[248:249]\n"
"v_pk_mul_f32 v[14:15], v[14:15], v[250:251]\n"
"v_cvt_pk_bf16_f32 v30, v12, v13\n"
"v_cvt_pk_bf16_f32 v31, v14, v15\n"
"ds_write_b128 v8, v[28:31] offset:34816\n"
"ds_read_b128 v[164:167], v2 offset:34816\n"
"ds_read_b128 v[192:195], v2 offset:35840\n"
"v_accvgpr_read_b32 v16, a240\n"
"v_accvgpr_read_b32 v17, a241\n"
"v_accvgpr_read_b32 v18, a242\n"
"v_accvgpr_read_b32 v19, a243\n"
"v_lshlrev_b32 v12, 16, v16\n"
"v_and_b32 v13, s63, v16\n"
"v_lshlrev_b32 v14, 16, v17\n"
"v_and_b32 v15, s63, v17\n"
"v_pk_mul_f32 v[12:13], v[12:13], v[168:169]\n"
"v_pk_mul_f32 v[14:15], v[14:15], v[170:171]\n"
"v_cvt_pk_bf16_f32 v24, v12, v13\n"
"v_cvt_pk_bf16_f32 v25, v14, v15\n"
"v_lshlrev_b32 v12, 16, v18\n"
"v_and_b32 v13, s63, v18\n"
"v_lshlrev_b32 v14, 16, v19\n"
"v_and_b32 v15, s63, v19\n"
"v_pk_mul_f32 v[12:13], v[12:13], v[196:197]\n"
"v_pk_mul_f32 v[14:15], v[14:15], v[198:199]\n"
"v_cvt_pk_bf16_f32 v26, v12, v13\n"
"v_cvt_pk_bf16_f32 v27, v14, v15\n"
"ds_write_b128 v7, v[24:27] offset:36864\n"
"s_waitcnt lgkmcnt(2)\n"
"buffer_store_dwordx4 v[164:167], v6, s[52:55], s90 offen offset:2176 sc1\n"
"s_waitcnt lgkmcnt(1)\n"
"buffer_store_dwordx4 v[192:195], v6, s[52:55], s91 offen offset:3200 sc1\n"
"v_accvgpr_read_b32 v16, a244\n"
"v_accvgpr_read_b32 v17, a245\n"
"v_accvgpr_read_b32 v18, a246\n"
"v_accvgpr_read_b32 v19, a247\n"
"v_lshlrev_b32 v12, 16, v16\n"
"v_and_b32 v13, s63, v16\n"
"v_lshlrev_b32 v14, 16, v17\n"
"v_and_b32 v15, s63, v17\n"
"v_pk_mul_f32 v[12:13], v[12:13], v[224:225]\n"
"v_pk_mul_f32 v[14:15], v[14:15], v[226:227]\n"
"v_cvt_pk_bf16_f32 v28, v12, v13\n"
"v_cvt_pk_bf16_f32 v29, v14, v15\n"
"v_lshlrev_b32 v12, 16, v18\n"
"v_and_b32 v13, s63, v18\n"
"v_lshlrev_b32 v14, 16, v19\n"
"v_and_b32 v15, s63, v19\n"
"v_pk_mul_f32 v[12:13], v[12:13], v[252:253]\n"
"v_pk_mul_f32 v[14:15], v[14:15], v[254:255]\n"
"v_cvt_pk_bf16_f32 v30, v12, v13\n"
"v_cvt_pk_bf16_f32 v31, v14, v15\n"
"ds_write_b128 v8, v[28:31] offset:36864\n"
"ds_read_b128 v[168:171], v2 offset:36864\n"
"ds_read_b128 v[196:199], v2 offset:37888\n"
"v_accvgpr_read_b32 v16, a248\n"
"v_accvgpr_read_b32 v17, a249\n"
"v_accvgpr_read_b32 v18, a250\n"
"v_accvgpr_read_b32 v19, a251\n"
"v_lshlrev_b32 v12, 16, v16\n"
"v_and_b32 v13, s63, v16\n"
"v_lshlrev_b32 v14, 16, v17\n"
"v_and_b32 v15, s63, v17\n"
"v_accvgpr_read_b32 v10, a16\n"
"v_accvgpr_read_b32 v11, a17\n"
"v_mul_f32 v12, v12, v10\n"
"v_mul_f32 v13, v13, v11\n"
"v_accvgpr_read_b32 v10, a18\n"
"v_accvgpr_read_b32 v11, a19\n"
"v_mul_f32 v14, v14, v10\n"
"v_mul_f32 v15, v15, v11\n"
"v_cvt_pk_bf16_f32 v24, v12, v13\n"
"v_cvt_pk_bf16_f32 v25, v14, v15\n"
"v_lshlrev_b32 v12, 16, v18\n"
"v_and_b32 v13, s63, v18\n"
"v_lshlrev_b32 v14, 16, v19\n"
"v_and_b32 v15, s63, v19\n"
"v_accvgpr_read_b32 v10, a20\n"
"v_accvgpr_read_b32 v11, a21\n"
"v_mul_f32 v12, v12, v10\n"
"v_mul_f32 v13, v13, v11\n"
"v_accvgpr_read_b32 v10, a22\n"
"v_accvgpr_read_b32 v11, a23\n"
"v_mul_f32 v14, v14, v10\n"
"v_mul_f32 v15, v15, v11\n"
"v_cvt_pk_bf16_f32 v26, v12, v13\n"
"v_cvt_pk_bf16_f32 v27, v14, v15\n"
"ds_write_b128 v7, v[24:27] offset:38912\n"
"s_waitcnt lgkmcnt(2)\n"
"buffer_store_dwordx4 v[168:171], v6, s[52:55], s92 offen offset:128 sc1\n"
"s_waitcnt lgkmcnt(1)\n"
"buffer_store_dwordx4 v[196:199], v6, s[52:55], s93 offen offset:1152 sc1\n"
"v_accvgpr_read_b32 v16, a252\n"
"v_accvgpr_read_b32 v17, a253\n"
"v_accvgpr_read_b32 v18, a254\n"
"v_accvgpr_read_b32 v19, a255\n"
"v_lshlrev_b32 v12, 16, v16\n"
"v_and_b32 v13, s63, v16\n"
"v_lshlrev_b32 v14, 16, v17\n"
"v_and_b32 v15, s63, v17\n"
"v_accvgpr_read_b32 v10, a24\n"
"v_accvgpr_read_b32 v11, a25\n"
"v_mul_f32 v12, v12, v10\n"
"v_mul_f32 v13, v13, v11\n"
"v_accvgpr_read_b32 v10, a26\n"
"v_accvgpr_read_b32 v11, a27\n"
"v_mul_f32 v14, v14, v10\n"
"v_mul_f32 v15, v15, v11\n"
"v_cvt_pk_bf16_f32 v28, v12, v13\n"
"v_cvt_pk_bf16_f32 v29, v14, v15\n"
"v_lshlrev_b32 v12, 16, v18\n"
"v_and_b32 v13, s63, v18\n"
"v_lshlrev_b32 v14, 16, v19\n"
"v_and_b32 v15, s63, v19\n"
"v_accvgpr_read_b32 v10, a28\n"
"v_accvgpr_read_b32 v11, a29\n"
"v_mul_f32 v12, v12, v10\n"
"v_mul_f32 v13, v13, v11\n"
"v_accvgpr_read_b32 v10, a30\n"
"v_accvgpr_read_b32 v11, a31\n"
"v_mul_f32 v14, v14, v10\n"
"v_mul_f32 v15, v15, v11\n"
"v_cvt_pk_bf16_f32 v30, v12, v13\n"
"v_cvt_pk_bf16_f32 v31, v14, v15\n"
"ds_write_b128 v8, v[28:31] offset:38912\n"
"ds_read_b128 v[224:227], v2 offset:38912\n"
"ds_read_b128 v[252:255], v2 offset:39936\n"
"s_waitcnt lgkmcnt(1)\n"
"buffer_store_dwordx4 v[224:227], v6, s[52:55], s94 offen offset:2176 sc1\n"
"s_waitcnt lgkmcnt(0)\n"
"buffer_store_dwordx4 v[252:255], v6, s[52:55], s95 offen offset:3200 sc1\n"
"s_or_b32 s66, %26, %16\n"
"s_cbranch_scc1 L_end\n"
"v_mov_b32 v10, s78\n"
"v_mov_b32 v11, s79\n"
"s_mov_b32 s66, 139264\n"
"buffer_store_dwordx2 v[10:11], off, s[56:59], s66 sc1\n"
"s_branch L_end\n"
"L_slow0:\n"
"s_cmp_eq_u32 s68, 0\n"
"s_cbranch_scc1 L_back0\n"
"s_sub_u32 s68, s68, 1\n"
"s_sleep 2\n"
"buffer_load_dwordx2 v[8:9], off, s[56:59], %14 sc1\n"
"s_waitcnt vmcnt(0)\n"
"v_subrev_u32 v8, s65, v8\n"
"v_xor_b32 v9, s79, v9\n"
"v_or_b32 v8, v8, v9\n"
"v_cmp_lt_u32 vcc, 15, v8\n"
"s_nop 4\n"
"s_cbranch_vccnz L_slow0\n"
"s_branch L_back0\n"
"L_slow1:\n"
"s_cmp_eq_u32 s68, 0\n"
"s_cbranch_scc1 L_back1\n"
"s_sub_u32 s68, s68, 1\n"
"s_sleep 2\n"
"buffer_load_dwordx2 v[8:9], off, s[56:59], %14 sc1\n"
"s_waitcnt vmcnt(0)\n"
"v_subrev_u32 v8, s65, v8\n"
"v_xor_b32 v9, s79, v9\n"
"v_or_b32 v8, v8, v9\n"
"v_cmp_lt_u32 vcc, 15, v8\n"
"s_nop 4\n"
"s_cbranch_vccnz L_slow1\n"
"s_branch L_back1\n"
"L_slow2:\n"
"s_cmp_eq_u32 s68, 0\n"
"s_cbranch_scc1 L_back2\n"
"s_sub_u32 s68, s68, 1\n"
"s_sleep 2\n"
"buffer_load_dwordx4 v[12:15], v0, s[56:59], %25 offen sc1\n"
"s_waitcnt vmcnt(0)\n"
"v_subrev_u32 v12, s67, v12\n"
"v_xor_b32 v13, s79, v13\n"
"v_subrev_u32 v14, s67, v14\n"
"v_xor_b32 v15, s79, v15\n"
"v_or_b32 v12, v12, v14\n"
"v_or_b32 v13, v13, v15\n"
"v_or_b32 v12, v12, v13\n"
"v_cmp_lt_u32 vcc, 15, v12\n"
"s_nop 4\n"
"s_cbranch_vccnz L_slow2\n"
"s_branch L_back2\n"
"L_slow3:\n"
"s_cmp_eq_u32 s68, 0\n"
"s_cbranch_scc1 L_back3\n"
"s_sub_u32 s68, s68, 1\n"
"s_sleep 2\n"
"buffer_load_dwordx4 v[24:27], v0, s[56:59], s65 offen sc1\n"
"s_waitcnt vmcnt(0)\n"
"v_subrev_u32 v24, s70, v24\n"
"v_xor_b32 v25, s79, v25\n"
"v_subrev_u32 v26, s70, v26\n"
"v_xor_b32 v27, s79, v27\n"
"v_or_b32 v24, v24, v26\n"
"v_or_b32 v25, v25, v27\n"
"v_or_b32 v24, v24, v25\n"
"v_cmp_lt_u32 vcc, 15, v24\n"
"s_nop 4\n"
"s_cbranch_vccnz L_slow3\n"
"s_branch L_back3\n"
"L_slow4:\n"
"s_cmp_eq_u32 s68, 0\n"
"s_cbranch_scc1 L_back4\n"
"s_sub_u32 s68, s68, 1\n"
"s_sleep 2\n"
"buffer_load_dwordx2 v[8:9], off, s[56:59], %14 sc1\n"
"s_waitcnt vmcnt(0)\n"
"v_subrev_u32 v8, s65, v8\n"
"v_xor_b32 v9, s79, v9\n"
"v_or_b32 v8, v8, v9\n"
"v_cmp_lt_u32 vcc, 15, v8\n"
"s_nop 4\n"
"s_cbranch_vccnz L_slow4\n"
"s_branch L_back4\n"
"L_headF:\n"
"L_end:\n"
"s_waitcnt vmcnt(0) lgkmcnt(0)\n"
    :: LO(wsb), HI(wsb), LO(xo), HI(xo), LO(ob), HI(ob), LO(wr), HI(wr), LO(bb), HI(bb), "s"(wo), "s"(eo), "s"(ep), "s"(fo), "s"(fp),
       "s"(ldsa), "s"(w), "s"(col), "v"(threadIdx.x), "s"(db), "s"(rs1), "s"(rs2), "s"(rd1), "s"(rn1), "s"(wfo), "s"(wfa), "s"(b)
    : "memory", "scc", "vcc",
"v1","v2","v3","v4","v5","v6","v7","v8","v9","v10","v11","v12","v13","v14","v15","v16","v17","v18","v19","v20","v21","v22","v23","v24","v25","v26","v27","v28","v29","v30","v31","v32","v33","v34","v35","v36","v37","v38","v39","v40","v41","v42","v43","v44","v45","v46","v47","v48","v49","v50","v51","v52","v53","v54","v55","v56","v57","v58","v59","v60","v61","v62","v63","v64","v65","v66","v67","v68","v69","v70","v71","v72","v73","v74","v75","v76","v77","v78","v79","v80","v81","v82","v83","v84","v85","v86","v87","v88","v89","v90","v91","v92","v93","v94","v95","v96","v97","v98","v99","v100","v101","v102","v103","v104","v105","v106","v107","v108","v109","v110","v111","v112","v113","v114","v115","v116","v117","v118","v119","v120","v121","v122","v123","v124","v125","v126","v127","v128","v129","v130","v131","v132","v133","v134","v135","v136","v137","v138","v139","v140","v141","v142","v143","v144","v145","v146","v147","v148","v149","v150","v151","v152","v153","v154","v155","v156","v157","v158","v159","v160","v161","v162","v163","v164","v165","v166","v167","v168","v169","v170","v171","v172","v173","v174","v175","v176","v177","v178","v179","v180","v181","v182","v183","v184","v185","v186","v187","v188","v189","v190","v191","v192","v193","v194","v195","v196","v197","v198","v199","v200","v201","v202","v203","v204","v205","v206","v207","v208","v209","v210","v211","v212","v213","v214","v215","v216","v217","v218","v219","v220","v221","v222","v223","v224","v225","v226","v227","v228","v229","v230","v231","v232","v233","v234","v235","v236","v237","v238","v239","v240","v241","v242","v243","v244","v245","v246","v247","v248","v249","v250","v251","v252","v253","v254","v255",
"a0","a1","a2","a3","a4","a5","a6","a7","a8","a9","a10","a11","a12","a13","a14","a15","a16","a17","a18","a19","a20","a21","a22","a23","a24","a25","a26","a27","a28","a29","a30","a31","a32","a33","a34","a35","a36","a37","a38","a39","a40","a41","a42","a43","a44","a45","a46","a47","a48","a49","a50","a51","a52","a53","a54","a55","a56","a57","a58","a59","a60","a61","a62","a63","a64","a65","a66","a67","a68","a69","a70","a71","a72","a73","a74","a75","a76","a77","a78","a79","a80","a81","a82","a83","a84","a85","a86","a87","a88","a89","a90","a91","a92","a93","a94","a95","a96","a97","a98","a99","a100","a101","a102","a103","a104","a105","a106","a107","a108","a109","a110","a111","a112","a113","a114","a115","a116","a117","a118","a119","a120","a121","a122","a123","a124","a125","a126","a127","a128","a129","a130","a131","a132","a133","a134","a135","a136","a137","a138","a139","a140","a141","a142","a143","a144","a145","a146","a147","a148","a149","a150","a151","a152","a153","a154","a155","a156","a157","a158","a159","a160","a161","a162","a163","a164","a165","a166","a167","a168","a169","a170","a171","a172","a173","a174","a175","a176","a177","a178","a179","a180","a181","a182","a183","a184","a185","a186","a187","a188","a189","a190","a191","a192","a193","a194","a195","a196","a197","a198","a199","a200","a201","a202","a203","a204","a205","a206","a207","a208","a209","a210","a211","a212","a213","a214","a215","a216","a217","a218","a219","a220","a221","a222","a223","a224","a225","a226","a227","a228","a229","a230","a231","a232","a233","a234","a235","a236","a237","a238","a239","a240","a241","a242","a243","a244","a245","a246","a247","a248","a249","a250","a251","a252","a253","a254","a255",
"s30","s31","s32","s33","s34","s35","s36","s37","s38","s39","s40","s41","s42","s43","s44","s45","s46","s47","s48","s49","s50","s51","s52","s53","s54","s55","s56","s57","s58","s59","s60","s61","s62","s63","s64","s65","s66","s67","s68","s69","s70","s71","s72","s73","s74","s75","s76","s77","s78","s79","s80","s81","s82","s83","s84","s85","s86","s87","s88","s89","s90","s91","s92","s93","s94","s95","s96","s97","s98","s99");
}

extern "C" void kernel_launch(void* const* d_in, const int* in_sizes, int n_in,
                              void* d_out, int out_size, void* d_ws, size_t ws_size,
                              hipStream_t stream) {
  const unsigned short* x0 = (const unsigned short*)d_in[0];
  const unsigned short* ws = (const unsigned short*)d_in[1];
  const unsigned short* bs = (const unsigned short*)d_in[2];
  dcn_cross_kernel<<<256, 256, 0, stream>>>(x0, ws, bs, (unsigned char*)d_ws, (unsigned short*)d_out);
}
```

```cpp
#include <hip/hip_runtime.h>


constexpr int kD = 1024;
constexpr unsigned kWFlagOff = 131072, kWOff = 1u << 20, kWStream = 200 * 8192, kXchOff = 32u << 20, kDbgOff = 224u << 20;

__device__ __forceinline__ unsigned unit_src(unsigned v, unsigned h, unsigned w) {
  const unsigned l = v >> 5, u = v & 31;
  const unsigned split = ((0xff0000ffu >> u) & 1) - 1;
  const unsigned part = l ? u >> 4 : 0u, g = (0xff00ff00u >> u) & 1;
  const unsigned c = u >> (split & 1), jh = (u >> (~split & 2)) & 1;
  const unsigned kc = 8 * (h ^ part) + 2 * ((w + c) & 3) + g;
  return (l * 1024 + 64 * kc) * 2048 + jh * 128;
}

__global__ __launch_bounds__(256, 1)
void dcn_cross_kernel(const unsigned short* __restrict__ x0, const unsigned short* __restrict__ ws, const unsigned short* __restrict__ bs,
                      unsigned char* __restrict__ wsp, unsigned short* __restrict__ out) {
  __shared__ __attribute__((aligned(16))) unsigned char lds[163840];
  const unsigned b = blockIdx.x;
  constexpr unsigned kPx = 1 ? 1u : 8u;
  const unsigned h = 1 ? (b & 1) : ((b >> 3) & 1), rb = 1 ? (b >> 1) : ((b >> 4) * 8 + (b & 7));
  const unsigned w = __builtin_amdgcn_readfirstlane(threadIdx.x >> 6);
  unsigned col = (512 * h + 128 * w) * 2, rows = rb * 128 * kD * 2, oofs = rows + col;
  unsigned wo = (h * 4 + w) * kWStream, eo = kXchOff + b * 524288 + w * 32768, ep = kXchOff + (b ^ kPx) * 524288 + w * 32768;
  unsigned fo = b * 512 + w * 128, fp = (b ^ kPx) * 512 + w * 128;
  unsigned db = kDbgOff + (b * 4 + w) * 512;
  const unsigned v1 = 24 + (rb >> 1), v2 = 64 + (rb >> 1), hk = rb & 1, on = v1 < 64 ? ~0u : 0u;
  unsigned rs1 = ((unit_src(v1, h, w) + hk * 65536) & on) | (((1 - h) << 20) & ~on), rd1 = ((v1 * 8192 + hk * 1024) & on) | ((192 * 8192) & ~on);
  unsigned rs2 = unit_src(v2, h, w) + hk * 65536, rn1 = v2 * 8192 + hk * 1024;
  unsigned wfa = kWFlagOff + (4 * h + w) * 1024, wfo = wfa + rb * 8;
  asm volatile("" : "+s"(col), "+s"(rows), "+s"(oofs), "+s"(wo), "+s"(eo), "+s"(ep), "+s"(fo), "+s"(fp), "+s"(db), "+s"(rs1), "+s"(rd1), "+s"(rs2), "+s"(rn1), "+s"(wfa), "+s"(wfo));
  __builtin_amdgcn_sched_barrier(0);
  const unsigned long long wsb = (unsigned long long)wsp, bb = (unsigned long long)bs;
  const unsigned long long xo = (unsigned long long)x0 + rows, ob = (unsigned long long)out + oofs, wr = (unsigned long long)ws + col;
  wo += (unsigned)wsb > ~(15u << 20) ? 16u << 20 : kWOff;
  const unsigned xa = (0u - (unsigned)wsb) & 0x1fffffu;
  eo += xa; ep += xa;
  const unsigned ldsa = (unsigned)(size_t)(__attribute__((address_space(3))) unsigned char*)lds;
#define LO(x) "s"((unsigned)(x))
#define HI(x) "s"((unsigned)((x) >> 32))
  asm volatile(
"L_main_entry:\n"
"v_and_b32 v12, 63, %18\n"
"v_lshrrev_b32 v13, 3, v12\n"
"v_and_b32 v15, 7, v12\n"
"v_lshlrev_b32 v10, 11, v13\n"
"s_andn2_b32 s60, 1024, %17\n"
"s_lshl_b32 s75, s60, 10\n"
"s_mov_b32 s52, %6\n"
"s_and_b32 s53, %7, 0xffff\n"
"s_mov_b32 s54, 12581120\n"
"s_mov_b32 s55, 0x20000\n"
"s_getpc_b64 s[96:97]\n"
"L_pc:\n"
"s_lshr_b32 s66, %26, 3\n"
"s_lshl_b32 s66, s66, 2\n"
"s_add_u32 s66, s66, %16\n"
"s_mul_i32 s66, s66, 640\n"
"s_and_b32 s97, s97, 0xffff\n"
"s_mov_b32 s98, L_end-L_pc\n"
"s_mov_b32 s99, 0x20000\n"
"s_min_u32 s66, s66, L_end-L_pc-640\n"
"v_min_u32 v9, 9, v12\n"
"v_lshlrev_b32 v9, 6, v9\n"
"buffer_load_dword a252, v9, s[96:99], s66 offen\n"
"v_xor_b32 v6, v15, v13\n"
"v_lshl_add_u32 v6, v6, 4, v10\n"
"s_mov_b32 s48, %2\n"
"s_and_b32 s49, %3, 0xffff\n"
"s_mov_b32 s50, 65536\n"
"s_mov_b32 s51, 0x20000\n"
"s_lshl_b32 s66, %16, 16\n"
"s_add_u32 s48, s48, s66\n"
"s_addc_u32 s49, s49, 0\n"
"s_lshl_b32 s72, %16, 12\n"
"s_add_u32 s72, s72, %15\n"
"s_add_u32 m0, s72, 16384\n"
"s_add_u32 s67, s60, 0\n"
"buffer_load_dwordx4 v6, s[48:51], s67 offen offset:0  lds\n"
"s_add_u32 s67, s60, 15360\n"
"buffer_load_dwordx4 v6, s[48:51], s67 offen offset:1024  lds\n"
"s_add_u32 s67, s60, 30720\n"
"buffer_load_dwordx4 v6, s[48:51], s67 offen offset:2048  lds\n"
"s_add_u32 s67, s60, 46080\n"
"buffer_load_dwordx4 v6, s[48:51], s67 offen offset:3072  lds\n"
"s_mul_i32 s73, %16, 40960\n"
"s_add_u32 s64, s73, %15\n"
"v_lshrrev_b32 v11, 2, v12\n"
"v_and_b32 v11, 4, v11\n"
"v_xor_b32 v11, v11, v15\n"
"v_lshl_add_u32 v11, v11, 4, v10\n"
"s_add_u32 m0, s64, 0\n"
"s_add_u32 s67, s75, 0\n"
"buffer_load_dwordx4 v11, s[52:55], s67 offen offset:0  lds\n"
"s_add_u32 s67, s75, 15360\n"
"buffer_load_dwordx4 v11, s[52:55], s67 offen offset:1024  lds\n"
"s_add_u32 s67, s75, 30720\n"
"buffer_load_dwordx4 v11, s[52:55], s67 offen offset:2048  lds\n"
"s_add_u32 s67, s75, 46080\n"
"buffer_load_dwordx4 v11, s[52:55], s67 offen offset:3072  lds\n"
"s_add_u32 m0, s64, 4096\n"
"s_add_u32 s67, s75, 65536\n"
"buffer_load_dwordx4 v11, s[52:55], s67 offen offset:0  lds\n"
"s_add_u32 s67, s75, 80896\n"
"buffer_load_dwordx4 v11, s[52:55], s67 offen offset:1024  lds\n"
"s_add_u32 s67, s75, 96256\n"
"buffer_load_dwordx4 v11, s[52:55], s67 offen offset:2048  lds\n"
"s_add_u32 s67, s75, 111616\n"
"buffer_load_dwordx4 v11, s[52:55], s67 offen offset:3072  lds\n"
"s_lshr_b32 s65, %26, 4\n"
"s_lshl_b32 s65, s65, 2\n"
"s_add_u32 s65, s65, %16\n"
"s_lshl_b32 s65, s65, 11\n"
"s_lshl_b32 s67, %16, 8\n"
"s_sub_u32 s65, s65, s67\n"
"v_and_b32 v14, 1, v13\n"
"v_lshlrev_b32 v14, 17, v14\n"
"v_lshl_add_u32 v14, v15, 7, v14\n"
"v_and_b32 v14, 0xfffffdff, v14\n"
"s_lshl_b32 s67, %26, 6\n"
"s_and_b32 s67, s67, 512\n"
"s_add_u32 s65, s65, s67\n"
"v_lshlrev_b32 v0, 4, v12\n"
"s_mov_b32 s56, %0\n"
"s_and_b32 s57, %1, 0xffff\n"
"s_mov_b32 s58, 171966464\n"
"s_mov_b32 s59, 0x20000\n"
"v_add_u32 v1, 4096, v0\n"
"s_add_u32 s36, s56, %10\n"
"s_addc_u32 s37, s57, 0\n"
"s_mov_b32 s38, 1572864\n"
"s_mov_b32 s39, 0x20000\n"
"s_add_u32 s34, s36, 200704\n"
"s_mov_b32 s35, s37\n"
"v_and_b32 v13, 15, v12\n"
"v_lshrrev_b32 v10, 4, v12\n"
"v_xor_b32 v15, v15, v10\n"
"v_lshlrev_b32 v7, 7, v13\n"
"v_lshl_add_u32 v7, v15, 4, v7\n"
"v_add_u32 v7, s64, v7\n"
"v_xor_b32 v8, 64, v7\n"
"v_bfe_u32 v13, v12, 2, 2\n"
"v_and_b32 v15, 3, v12\n"
"v_bfe_u32 v9, v12, 3, 1\n"
"v_lshl_add_u32 v15, v9, 2, v15\n"
"v_lshl_add_u32 v13, v10, 3, v13\n"
"v_lshlrev_b32 v13, 7, v13\n"
"v_lshl_add_u32 v9, v15, 4, v13\n"
"v_add_u32 v9, s64, v9\n"
"v_xor_b32 v15, 64, v9\n"
"s_mov_b32 s40, %2\n"
"s_and_b32 s41, %3, 0xffff\n"
"s_mov_b32 s42, 260352\n"
"s_mov_b32 s43, 0x20000\n"
"s_add_u32 s40, s40, %17\n"
"s_addc_u32 s41, s41, 0\n"
"s_mov_b32 s80, 0\n"
"s_mov_b32 s81, 15360\n"
"s_mov_b32 s82, 30720\n"
"s_mov_b32 s83, 46080\n"
"s_mov_b32 s84, 65536\n"
"s_mov_b32 s85, 80896\n"
"s_mov_b32 s86, 96256\n"
"s_mov_b32 s87, 111616\n"
"s_mov_b32 s88, 131072\n"
"s_mov_b32 s89, 146432\n"
"s_mov_b32 s90, 161792\n"
"s_mov_b32 s91, 177152\n"
"s_mov_b32 s92, 196608\n"
"s_mov_b32 s93, 211968\n"
"s_mov_b32 s94, 227328\n"
"s_mov_b32 s95, 242688\n"
"s_mov_b32 s62, 0\n"
"s_mov_b32 s63, 0xffff0000\n"
"s_mov_b32 s68, 0x8000\n"
"s_add_u32 s66, %16, 0\n"
"s_and_b32 s66, s66, 3\n"
"s_mul_i32 s66, s66, 40960\n"
"s_add_u32 s66, s66, %15\n"
"v_add_u32 v2, s66, v0\n"
"s_add_u32 s66, %16, 1\n"
"s_and_b32 s66, s66, 3\n"
"s_mul_i32 s66, s66, 40960\n"
"s_add_u32 s66, s66, %15\n"
"v_add_u32 v3, s66, v0\n"
"s_add_u32 s66, %16, 2\n"
"s_and_b32 s66, s66, 3\n"
"s_mul_i32 s66, s66, 40960\n"
"s_add_u32 s66, s66, %15\n"
"v_add_u32 v4, s66, v0\n"
"s_add_u32 s66, %16, 3\n"
"s_and_b32 s66, s66, 3\n"
"s_mul_i32 s66, s66, 40960\n"
"s_add_u32 s66, s66, %15\n"
"v_add_u32 v5, s66, v0\n"
"s_sub_u32 s66, 0, s73\n"
"v_add_u32 v12, s66, v7\n"
"v_add_u32 v13, s66, v8\n"
"s_mov_b32 s44, %8\n"
"s_and_b32 s45, %9, 0xffff\n"
"s_mov_b32 s46, 12288\n"
"s_mov_b32 s47, 0x20000\n"
"s_mov_b32 s61, %17\n"
"v_lshrrev_b32 v16, 4, v0\n"
"v_and_b32 v16, 0x30, v16\n"
"buffer_load_dwordx2 v[18:19], v16, s[44:47], s61 offen offset:0\n"
"v_lshrrev_b32 v20, 4, v0\n"
"v_and_b32 v20, 0x30, v20\n"
"buffer_load_dwordx2 v[22:23], v20, s[44:47], s61 offen offset:8\n"
"v_lshrrev_b32 v24, 4, v0\n"
"v_and_b32 v24, 0x30, v24\n"
"buffer_load_dwordx2 v[26:27], v24, s[44:47], s61 offen offset:64\n"
"v_lshrrev_b32 v28, 4, v0\n"
"v_and_b32 v28, 0x30, v28\n"
"buffer_load_dwordx2 v[30:31], v28, s[44:47], s61 offen offset:72\n"
"s_add_u32 s70, s75, 131072\n"
"s_add_u32 s70, s70, s65\n"
"buffer_load_dword a252, v14, s[52:55], s70 offen\n"
"s_mov_b32 s67, 139264\n"
"buffer_load_dwordx2 v[252:253], off, s[56:59], s67 sc1\n"
"s_waitcnt vmcnt(14)\n"
"s_barrier\n"
"ds_read_b128 a[100:103], v13 offset:16384\n"
"ds_read_b128 a[96:99], v12 offset:16384\n"
"ds_read_b128 a[108:111], v13 offset:18432\n"
"ds_read_b128 a[104:107], v12 offset:18432\n"
"ds_read_b128 a[116:119], v13 offset:20480\n"
"ds_read_b128 a[112:115], v12 offset:20480\n"
"ds_read_b128 a[124:127], v13 offset:22528\n"
"ds_read_b128 a[120:123], v12 offset:22528\n"
"ds_read_b128 a[132:135], v13 offset:24576\n"
"ds_read_b128 a[128:131], v12 offset:24576\n"
"ds_read_b128 a[140:143], v13 offset:26624\n"
"ds_read_b128 a[136:139], v12 offset:26624\n"
"ds_read_b128 a[148:151], v13 offset:28672\n"
"ds_read_b128 a[144:147], v12 offset:28672\n"
"ds_read_b128 a[156:159], v13 offset:30720\n"
"ds_read_b128 a[152:155], v12 offset:30720\n"
"s_add_u32 m0, s64, 8192\n"
"s_add_u32 s67, s75, 128\n"
"buffer_load_dwordx4 v11, s[52:55], s67 offen offset:0  lds\n"
"s_add_u32 s67, s75, 15488\n"
"buffer_load_dwordx4 v11, s[52:55], s67 offen offset:1024  lds\n"
"s_add_u32 s67, s75, 30848\n"
"buffer_load_dwordx4 v11, s[52:55], s67 offen offset:2048  lds\n"
"s_add_u32 s67, s75, 46208\n"
"buffer_load_dwordx4 v11, s[52:55], s67 offen offset:3072  lds\n"
"s_add_u32 m0, s64, 12288\n"
"s_add_u32 s67, s75, 65664\n"
"buffer_load_dwordx4 v11, s[52:55], s67 offen offset:0  lds\n"
"s_add_u32 s67, s75, 81024\n"
"buffer_load_dwordx4 v11, s[52:55], s67 offen offset:1024  lds\n"
"s_add_u32 s67, s75, 96384\n"
"buffer_load_dwordx4 v11, s[52:55], s67 offen offset:2048  lds\n"
"s_add_u32 s67, s75, 111744\n"
"buffer_load_dwordx4 v11, s[52:55], s67 offen offset:3072  lds\n"
"s_waitcnt vmcnt(14)\n"
"ds_read_b64_tr_b16 a[32:33], v9 offset:0\n"
"ds_read_b64_tr_b16 a[34:35], v9 offset:512\n"
"ds_read_b64_tr_b16 a[36:37], v9 offset:4096\n"
"ds_read_b64_tr_b16 a[38:39], v9 offset:4608\n"
"ds_read_b64_tr_b16 a[40:41], v9 offset:8\n"
"ds_read_b64_tr_b16 a[42:43], v9 offset:520\n"
"ds_read_b64_tr_b16 a[44:45], v9 offset:4104\n"
"ds_read_b64_tr_b16 a[46:47], v9 offset:4616\n"
"ds_read_b64_tr_b16 a[48:49], v15 offset:0\n"
"ds_read_b64_tr_b16 a[50:51], v15 offset:512\n"
"ds_read_b64_tr_b16 a[52:53], v15 offset:4096\n"
"ds_read_b64_tr_b16 a[54:55], v15 offset:4608\n"
"ds_read_b64_tr_b16 a[56:57], v15 offset:8\n"
"ds_read_b64_tr_b16 a[58:59], v15 offset:520\n"
"ds_read_b64_tr_b16 a[60:61], v15 offset:4104\n"
"ds_read_b64_tr_b16 a[62:63], v15 offset:4616\n"
"s_waitcnt vmcnt(10)\n"
"v_lshlrev_b32 v16, 16, v18\n"
"v_and_b32 v17, s63, v18\n"
"v_lshlrev_b32 v18, 16, v19\n"
"v_and_b32 v19, s63, v19\n"
"v_add_f32 v16, 1.0, v16\n"
"v_add_f32 v17, 1.0, v17\n"
"v_add_f32 v18, 1.0, v18\n"
"v_add_f32 v19, 1.0, v19\n"
"v_lshlrev_b32 v20, 16, v22\n"
"v_and_b32 v21, s63, v22\n"
"v_lshlrev_b32 v22, 16, v23\n"
"v_and_b32 v23, s63, v23\n"
"v_add_f32 v20, 1.0, v20\n"
"v_add_f32 v21, 1.0, v21\n"
"v_add_f32 v22, 1.0, v22\n"
"v_add_f32 v23, 1.0, v23\n"
"v_lshlrev_b32 v24, 16, v26\n"
"v_and_b32 v25, s63, v26\n"
"v_lshlrev_b32 v26, 16, v27\n"
"v_and_b32 v27, s63, v27\n"
"v_add_f32 v24, 1.0, v24\n"
"v_add_f32 v25, 1.0, v25\n"
"v_add_f32 v26, 1.0, v26\n"
"v_add_f32 v27, 1.0, v27\n"
"v_lshlrev_b32 v28, 16, v30\n"
"v_and_b32 v29, s63, v30\n"
"v_lshlrev_b32 v30, 16, v31\n"
"v_and_b32 v31, s63, v31\n"
"v_add_f32 v28, 1.0, v28\n"
"v_add_f32 v29, 1.0, v29\n"
"v_add_f32 v30, 1.0, v30\n"
"v_add_f32 v31, 1.0, v31\n"
"v_accvgpr_write_b32 a0, v16\n"
"v_accvgpr_write_b32 a1, v17\n"
"v_accvgpr_write_b32 a2, v18\n"
"v_accvgpr_write_b32 a3, v19\n"
"v_accvgpr_write_b32 a4, v20\n"
"v_accvgpr_write_b32 a5, v21\n"
"v_accvgpr_write_b32 a6, v22\n"
"v_accvgpr_write_b32 a7, v23\n"
"v_accvgpr_write_b32 a8, v24\n"
"v_accvgpr_write_b32 a9, v25\n"
"v_accvgpr_write_b32 a10, v26\n"
"v_accvgpr_write_b32 a11, v27\n"
"v_accvgpr_write_b32 a12, v28\n"
"v_accvgpr_write_b32 a13, v29\n"
"v_accvgpr_write_b32 a14, v30\n"
"v_accvgpr_write_b32 a15, v31\n"
"s_add_u32 m0, s72, 57344\n"
"s_add_u32 s67, s60, 128\n"
"buffer_load_dwordx4 v6, s[48:51], s67 offen offset:0  lds\n"
"s_add_u32 s67, s60, 15488\n"
"buffer_load_dwordx4 v6, s[48:51], s67 offen offset:1024  lds\n"
"s_add_u32 s67, s60, 30848\n"
"buffer_load_dwordx4 v6, s[48:51], s67 offen offset:2048  lds\n"
"s_add_u32 s67, s60, 46208\n"
"buffer_load_dwordx4 v6, s[48:51], s67 offen offset:3072  lds\n"
"s_bitcmp1_b32 %26, 0\n"
"s_cbranch_scc1 L_z\n"
"s_mov_b64 s[96:97], s[52:53]\n"
"s_cmp_lt_u32 %22, 1572864\n"
"s_cselect_b32 s98, s54, 0\n"
"s_cselect_b32 s38, 8192000, 0\n"
"s_mov_b32 s99, 0x20000\n"
"s_xor_b32 s71, %20, 0x100000\n"
"s_add_u32 s70, %22, 6553600\n"
"s_add_u32 m0, s72, 139264\n"
"s_add_u32 s67, %20, 0\n"
"buffer_load_dwordx4 v11, s[96:99], s67 offen offset:0  lds\n"
"s_add_u32 s67, %20, 15360\n"
"buffer_load_dwordx4 v11, s[96:99], s67 offen offset:1024  lds\n"
"s_add_u32 s67, %20, 30720\n"
"buffer_load_dwordx4 v11, s[96:99], s67 offen offset:2048  lds\n"
"s_add_u32 s67, %20, 46080\n"
"buffer_load_dwordx4 v11, s[96:99], s67 offen offset:3072  lds\n"
"s_add_u32 m0, s72, 98304\n"
"s_add_u32 s67, s71, 1024\n"
"buffer_load_dwordx4 v11, s[96:99], s67 offen offset:0  lds\n"
"s_add_u32 s67, s71, 16384\n"
"buffer_load_dwordx4 v11, s[96:99], s67 offen offset:1024  lds\n"
"s_add_u32 s67, s71, 31744\n"
"buffer_load_dwordx4 v11, s[96:99], s67 offen offset:2048  lds\n"
"s_add_u32 s67, s71, 47104\n"
"buffer_load_dwordx4 v11, s[96:99], s67 offen offset:3072  lds\n"
"s_sub_u32 s67, s72, s64\n"
"s_add_u32 s67, s67, 98304\n"
"v_add_u32 v40, s67, v9\n"
"v_add_u32 v41, s67, v15\n"
"s_sleep 36\n"
"s_waitcnt vmcnt(4)\n"
"ds_read_b64_tr_b16 a[160:161], v40 offset:40960\n"
"ds_read_b64_tr_b16 a[162:163], v40 offset:41472\n"
"ds_read_b64_tr_b16 a[164:165], v40 offset:40968\n"
"ds_read_b64_tr_b16 a[166:167], v40 offset:41480\n"
"ds_read_b64_tr_b16 a[168:169], v41 offset:40960\n"
"ds_read_b64_tr_b16 a[170:171], v41 offset:41472\n"
"ds_read_b64_tr_b16 a[172:173], v41 offset:40968\n"
"ds_read_b64_tr_b16 a[174:175], v41 offset:41480\n"
"s_waitcnt vmcnt(0)\n"
"ds_read_b64_tr_b16 a[176:177], v40 offset:0\n"
"ds_read_b64_tr_b16 a[178:179], v40 offset:512\n"
"ds_read_b64_tr_b16 a[180:181], v40 offset:8\n"
"ds_read_b64_tr_b16 a[182:183], v40 offset:520\n"
"ds_read_b64_tr_b16 a[184:185], v41 offset:0\n"
"ds_read_b64_tr_b16 a[186:187], v41 offset:512\n"
"ds_read_b64_tr_b16 a[188:189], v41 offset:8\n"
"ds_read_b64_tr_b16 a[190:191], v41 offset:520\n"
"s_waitcnt lgkmcnt(14)\n"
"buffer_store_dwordx4 a[160:163], v0, s[36:39], %22 offen offset:0 sc1\n"
"s_waitcnt lgkmcnt(12)\n"
"buffer_store_dwordx4 a[164:167], v0, s[36:39], %22 offen offset:2048 sc1\n"
"s_waitcnt lgkmcnt(10)\n"
"buffer_store_dwordx4 a[168:171], v1, s[36:39], %22 offen offset:0 sc1\n"
"s_waitcnt lgkmcnt(8)\n"
"buffer_store_dwordx4 a[172:175], v1, s[36:39], %22 offen offset:2048 sc1\n"
"s_waitcnt lgkmcnt(6)\n"
"buffer_store_dwordx4 a[176:179], v0, s[36:39], s70 offen offset:0 sc1\n"
"s_waitcnt lgkmcnt(4)\n"
"buffer_store_dwordx4 a[180:183], v0, s[36:39], s70 offen offset:2048 sc1\n"
"s_waitcnt lgkmcnt(2)\n"
"buffer_store_dwordx4 a[184:187], v1, s[36:39], s70 offen offset:0 sc1\n"
"s_waitcnt lgkmcnt(0)\n"
"buffer_store_dwordx4 a[188:191], v1, s[36:39], s70 offen offset:2048 sc1\n"
"L_z:\n"
"s_nop 1\n"
"s_waitcnt lgkmcnt(8)\n"
"v_mfma_f32_16x16x32_bf16 v[32:35], a[32:35], a[96:99], v[16:19]\n"
"s_add_u32 s70, s75, 393216\n"
"v_mfma_f32_16x16x32_bf16 v[32:35], a[36:39], a[100:103], v[32:35]\n"
"s_add_u32 s70, s70, s65\n"
"v_mfma_f32_16x16x32_bf16 v[60:63], a[44:47], a[100:103], v[20:23]\n"
"buffer_load_dword a252, v14, s[52:55], s70 offen\n"
"v_mfma_f32_16x16x32_bf16 v[60:63], a[40:43], a[96:99], v[60:63]\n"
"s_add_u32 m0, s64, 32768\n"
"v_mfma_f32_16x16x32_bf16 v[64:67], a[40:43], a[104:107], v[20:23]\n"
"s_add_u32 s67, s75, 131072\n"
"v_mfma_f32_16x16x32_bf16 v[64:67], a[44:47], a[108:111], v[64:67]\n"
"buffer_load_dwordx4 v11, s[52:55], s67 offen offset:0  lds\n"
"v_mfma_f32_16x16x32_bf16 v[36:39], a[36:39], a[108:111], v[16:19]\n"
"s_add_u32 s67, s75, 146432\n"
"v_mfma_f32_16x16x32_bf16 v[36:39], a[32:35], a[104:107], v[36:39]\n"
"v_mfma_f32_16x16x32_bf16 v[40:43], a[32:35], a[112:115], v[16:19]\n"
"s_waitcnt lgkmcnt(0)\n"
"buffer_load_dwordx4 v11, s[52:55], s67 offen offset:1024  lds\n"
"v_mfma_f32_16x16x32_bf16 v[40:43], a[36:39], a[116:119], v[40:43]\n"
"v_mfma_f32_16x16x32_bf16 v[68:71], a[44:47], a[116:119], v[20:23]\n"
"s_add_u32 s67, s75, 161792\n"
"v_mfma_f32_16x16x32_bf16 v[68:71], a[40:43], a[112:115], v[68:71]\n"
"v_mfma_f32_16x16x32_bf16 v[72:75], a[40:43], a[120:123], v[20:23]\n"
"buffer_load_dwordx4 v11, s[52:55], s67 offen offset:2048  lds\n"
"v_mfma_f32_16x16x32_bf16 v[72:75], a[44:47], a[124:127], v[72:75]\n"
"v_mfma_f32_16x16x32_bf16 v[44:47], a[36:39], a[124:127], v[16:19]\n"
"s_add_u32 s67, s75, 177152\n"
"v_mfma_f32_16x16x32_bf16 v[44:47], a[32:35], a[120:123], v[44:47]\n"
"v_mfma_f32_16x16x32_bf16 v[48:51], a[32:35], a[128:131], v[16:19]\n"
"buffer_load_dwordx4 v11, s[52:55], s67 offen offset:3072  lds\n"
"v_mfma_f32_16x16x32_bf16 v[48:51], a[36:39], a[132:135], v[48:51]\n"
"v_mfma_f32_16x16x32_bf16 v[76:79], a[44:47], a[132:135], v[20:23]\n"
"s_add_u32 m0, s64, 36864\n"
"v_mfma_f32_16x16x32_bf16 v[76:79], a[40:43], a[128:131], v[76:79]\n"
"v_mfma_f32_16x16x32_bf16 v[80:83], a[40:43], a[136:139], v[20:23]\n"
"s_add_u32 s67, s75, 196608\n"
"v_mfma_f32_16x16x32_bf16 v[80:83], a[44:47], a[140:143], v[80:83]\n"
"v_mfma_f32_16x16x32_bf16 v[52:55], a[36:39], a[140:143], v[16:19]\n"
"buffer_load_dwordx4 v11, s[52:55], s67 offen offset:0  lds\n"
"v_mfma_f32_16x16x32_bf16 v[52:55], a[32:35], a[136:139], v[52:55]\n"
"v_mfma_f32_16x16x32_bf16 v[56:59], a[32:35], a[144:147], v[16:19]\n"
"s_add_u32 s67, s75, 211968\n"
"v_mfma_f32_16x16x32_bf16 v[56:59], a[36:39], a[148:151], v[56:59]\n"
"v_mfma_f32_16x16x32_bf16 v[84:87], a[44:47], a[148:151], v[20:23]\n"
"buffer_load_dwordx4 v11, s[52:55], s67 offen offset:1024  lds\n"
"v_mfma_f32_16x16x32_bf16 v[84:87], a[40:43], a[144:147], v[84:87]\n"
"v_mfma_f32_16x16x32_bf16 a[4:7], a[40:43], a[152:155], a[4:7]\n"
"s_add_u32 s67, s75, 227328\n"
"v_mfma_f32_16x16x32_bf16 a[4:7], a[44:47], a[156:159], a[4:7]\n"
"v_mfma_f32_16x16x32_bf16 a[0:3], a[36:39], a[156:159], a[0:3]\n"
"buffer_load_dwordx4 v11, s[52:55], s67 offen offset:2048  lds\n"
"v_mfma_f32_16x16x32_bf16 a[0:3], a[32:35], a[152:155], a[0:3]\n"
"v_mfma_f32_16x16x32_bf16 v[88:91], a[48:51], a[96:99], v[24:27]\n"
"s_add_u32 s67, s75, 242688\n"
"v_mfma_f32_16x16x32_bf16 v[88:91], a[52:55], a[100:103], v[88:91]\n"
"v_mfma_f32_16x16x32_bf16 v[116:119], a[60:63], a[100:103], v[28:31]\n"
"v_mfma_f32_16x16x32_bf16 v[116:119], a[56:59], a[96:99], v[116:119]\n"
"buffer_load_dwordx4 v11, s[52:55], s67 offen offset:3072  lds\n"
"v_mfma_f32_16x16x32_bf16 v[120:123], a[56:59], a[104:107], v[28:31]\n"
"s_add_u32 m0, s64, 0\n"
"v_mfma_f32_16x16x32_bf16 v[120:123], a[60:63], a[108:111], v[120:123]\n"
"v_mfma_f32_16x16x32_bf16 v[92:95], a[52:55], a[108:111], v[24:27]\n"
"s_add_u32 s67, s75, 131200\n"
"v_mfma_f32_16x16x32_bf16 v[92:95], a[48:51], a[104:107], v[92:95]\n"
"v_mfma_f32_16x16x32_bf16 v[96:99], a[48:51], a[112:115], v[24:27]\n"
"buffer_load_dwordx4 v11, s[52:55], s67 offen offset:0  lds\n"
"v_mfma_f32_16x16x32_bf16 v[96:99], a[52:55], a[116:119], v[96:99]\n"
"v_mfma_f32_16x16x32_bf16 v[124:127], a[60:63], a[116:119], v[28:31]\n"
"s_add_u32 s67, s75, 146560\n"
"v_mfma_f32_16x16x32_bf16 v[124:127], a[56:59], a[112:115], v[124:127]\n"
"buffer_load_dwordx4 v11, s[52:55], s67 offen offset:1024  lds\n"
"v_mfma_f32_16x16x32_bf16 v[128:131], a[56:59], a[120:123], v[28:31]\n"
"s_waitcnt vmcnt(15)\n"
"ds_read_b64_tr_b16 a[64:65], v9 offset:8192\n"
"v_mfma_f32_16x16x32_bf16 v[128:131], a[60:63], a[124:127], v[128:131]\n"
"s_add_u32 s67, s75, 161920\n"
"ds_read_b64_tr_b16 a[66:67], v9 offset:8704\n"
"v_mfma_f32_16x16x32_bf16 v[100:103], a[52:55], a[124:127], v[24:27]\n"
"ds_read_b64_tr_b16 a[68:69], v9 offset:12288\n"
"v_mfma_f32_16x16x32_bf16 v[100:103], a[48:51], a[120:123], v[100:103]\n"
"ds_read_b64_tr_b16 a[70:71], v9 offset:12800\n"
"v_mfma_f32_16x16x32_bf16 v[104:107], a[48:51], a[128:131], v[24:27]\n"
"ds_read_b64_tr_b16 a[72:73], v9 offset:8200\n"
"v_mfma_f32_16x16x32_bf16 v[104:107], a[52:55], a[132:135], v[104:107]\n"
"ds_read_b64_tr_b16 a[74:75], v9 offset:8712\n"
"buffer_load_dwordx4 v11, s[52:55], s67 offen offset:2048  lds\n"
"v_mfma_f32_16x16x32_bf16 v[132:135], a[60:63], a[132:135], v[28:31]\n"
"ds_read_b64_tr_b16 a[76:77], v9 offset:12296\n"
"v_mfma_f32_16x16x32_bf16 v[132:135], a[56:59], a[128:131], v[132:135]\n"
"s_add_u32 s67, s75, 177280\n"
"ds_read_b64_tr_b16 a[78:79], v9 offset:12808\n"
"v_mfma_f32_16x16x32_bf16 v[136:139], a[56:59], a[136:139], v[28:31]\n"
"buffer_load_dwordx4 v11, s[52:55], s67 offen offset:3072  lds\n"
"v_mfma_f32_16x16x32_bf16 v[136:139], a[60:63], a[140:143], v[136:139]\n"
"ds_read_b64_tr_b16 a[80:81], v15 offset:8192\n"
"v_mfma_f32_16x16x32_bf16 v[108:111], a[52:55], a[140:143], v[24:27]\n"
"s_add_u32 m0, s64, 4096\n"
"ds_read_b64_tr_b16 a[82:83], v15 offset:8704\n"
"v_mfma_f32_16x16x32_bf16 v[108:111], a[48:51], a[136:139], v[108:111]\n"
"ds_read_b64_tr_b16 a[84:85], v15 offset:12288\n"
"v_mfma_f32_16x16x32_bf16 v[112:115], a[48:51], a[144:147], v[24:27]\n"
"s_add_u32 s67, s75, 196736\n"
"ds_read_b64_tr_b16 a[86:87], v15 offset:12800\n"
"v_mfma_f32_16x16x32_bf16 v[112:115], a[52:55], a[148:151], v[112:115]\n"
"ds_read_b64_tr_b16 a[88:89], v15 offset:8200\n"
"v_mfma_f32_16x16x32_bf16 v[140:143], a[60:63], a[148:151], v[28:31]\n"
"ds_read_b64_tr_b16 a[90:91], v15 offset:8712\n"
"buffer_load_dwordx4 v11, s[52:55], s67 offen offset:0  lds\n"
"v_mfma_f32_16x16x32_bf16 v[140:143], a[56:59], a[144:147], v[140:143]\n"
"ds_read_b64_tr_b16 a[92:93], v15 offset:12296\n"
"v_mfma_f32_16x16x32_bf16 a[12:15], a[56:59], a[152:155], a[12:15]\n"
"s_add_u32 s67, s75, 212096\n"
"ds_read_b64_tr_b16 a[94:95], v15 offset:12808\n"
"v_mfma_f32_16x16x32_bf16 a[12:15], a[60:63], a[156:159], a[12:15]\n"
"v_mfma_f32_16x16x32_bf16 a[8:11], a[52:55], a[156:159], a[8:11]\n"
"buffer_load_dwordx4 v11, s[52:55], s67 offen offset:1024  lds\n"
"v_mfma_f32_16x16x32_bf16 a[8:11], a[48:51], a[152:155], a[8:11]\n"
"s_waitcnt lgkmcnt(8)\n"
"v_mfma_f32_16x16x32_bf16 v[144:147], a[64:67], a[96:99], 1.0\n"
"s_add_u32 s67, s75, 227456\n"
"v_mfma_f32_16x16x32_bf16 v[144:147], a[68:71], a[100:103], v[144:147]\n"
"v_mfma_f32_16x16x32_bf16 v[172:175], a[76:79], a[100:103], 1.0\n"
"s_waitcnt lgkmcnt(0)\n"
"v_readfirstlane_b32 s78, v252\n"
"v_readfirstlane_b32 s79, v253\n"
"v_mfma_f32_16x16x32_bf16 v[172:175], a[72:75], a[96:99], v[172:175]\n"
"buffer_load_dwordx4 v11, s[52:55], s67 offen offset:2048  lds\n"
"v_mfma_f32_16x16x32_bf16 v[176:179], a[72:75], a[104:107], 1.0\n"
"v_mfma_f32_16x16x32_bf16 v[176:179], a[76:79], a[108:111], v[176:179]\n"
"s_add_u32 s67, s75, 242816\n"
"v_mfma_f32_16x16x32_bf16 v[148:151], a[68:71], a[108:111], 1.0\n"
"s_add_u32 s78, s78, 16\n"
"v_mfma_f32_16x16x32_bf16 v[148:151], a[64:67], a[104:107], v[148:151]\n"
"s_add_u32 s79, s79, 16\n"
"v_mfma_f32_16x16x32_bf16 v[152:155], a[64:67], a[112:115], 1.0\n"
"s_sub_u32 s66, 40960, s73\n"
"v_mfma_f32_16x16x32_bf16 v[152:155], a[68:71], a[116:119], v[152:155]\n"
"buffer_load_dwordx4 v11, s[52:55], s67 offen offset:3072  lds\n"
"v_mfma_f32_16x16x32_bf16 v[180:183], a[76:79], a[116:119], 1.0\n"
"s_add_u32 m0, s72, 98304\n"
"v_add_u32 v12, s66, v7\n"
"v_mfma_f32_16x16x32_bf16 v[180:183], a[72:75], a[112:115], v[180:183]\n"
"v_mfma_f32_16x16x32_bf16 v[184:187], a[72:75], a[120:123], 1.0\n"
"s_add_u32 s67, s60, 256\n"
"v_add_u32 v13, s66, v8\n"
"v_mfma_f32_16x16x32_bf16 v[184:187], a[76:79], a[124:127], v[184:187]\n"
"buffer_load_dwordx4 v6, s[48:51], s67 offen offset:0  lds\n"
"v_mfma_f32_16x16x32_bf16 v[156:159], a[68:71], a[124:127], 1.0\n"
"v_mfma_f32_16x16x32_bf16 v[156:159], a[64:67], a[120:123], v[156:159]\n"
"s_add_u32 s67, s60, 15616\n"
"v_mfma_f32_16x16x32_bf16 v[160:163], a[64:67], a[128:131], 1.0\n"
"v_mfma_f32_16x16x32_bf16 v[160:163], a[68:71], a[132:135], v[160:163]\n"
"buffer_load_dwordx4 v6, s[48:51], s67 offen offset:1024  lds\n"
"v_mfma_f32_16x16x32_bf16 v[188:191], a[76:79], a[132:135], 1.0\n"
"v_mfma_f32_16x16x32_bf16 v[188:191], a[72:75], a[128:131], v[188:191]\n"
"s_add_u32 s67, s60, 30976\n"
"v_mfma_f32_16x16x32_bf16 v[192:195], a[72:75], a[136:139], 1.0\n"
"v_mfma_f32_16x16x32_bf16 v[192:195], a[76:79], a[140:143], v[192:195]\n"
"buffer_load_dwordx4 v6, s[48:51], s67 offen offset:2048  lds\n"
"v_mfma_f32_16x16x32_bf16 v[164:167], a[68:71], a[140:143], 1.0\n"
"v_mfma_f32_16x16x32_bf16 v[164:167], a[64:67], a[136:139], v[164:167]\n"
"s_add_u32 s67, s60, 46336\n"
"v_mfma_f32_16x16x32_bf16 v[168:171], a[64:67], a[144:147], 1.0\n"
"v_mfma_f32_16x16x32_bf16 v[168:171], a[68:71], a[148:151], v[168:171]\n"
"v_mfma_f32_16x16x32_bf16 v[196:199], a[76:79], a[148:151], 1.0\n"
"buffer_load_dwordx4 v6, s[48:51], s67 offen offset:3072  lds\n"
"v_mfma_f32_16x16x32_bf16 v[196:199], a[72:75], a[144:147], v[196:199]\n"
"v_mfma_f32_16x16x32_bf16 a[20:23], a[72:75], a[152:155], 1.0\n"
"v_mfma_f32_16x16x32_bf16 a[20:23], a[76:79], a[156:159], a[20:23]\n"
"v_mfma_f32_16x16x32_bf16 a[16:19], a[68:71], a[156:159], 1.0\n"
"v_mfma_f32_16x16x32_bf16 a[16:19], a[64:67], a[152:155], a[16:19]\n"
"s_waitcnt vmcnt(21)\n"
"s_barrier\n"
"v_mfma_f32_16x16x32_bf16 v[200:203], a[80:83], a[96:99], 1.0\n"
"s_waitcnt vmcnt(12)\n"
"ds_read_b64_tr_b16 a[32:33], v9 offset:32768\n"
"v_mfma_f32_16x16x32_bf16 v[200:203], a[84:87], a[100:103], v[200:203]\n"
"s_add_u32 m0, s64, 8192\n"
"ds_read_b64_tr_b16 a[34:35], v9 offset:33280\n"
"v_mfma_f32_16x16x32_bf16 v[228:231], a[92:95], a[100:103], 1.0\n"
"ds_read_b128 a[100:103], v13 offset:16384\n"
"v_mfma_f32_16x16x32_bf16 v[228:231], a[88:91], a[96:99], v[228:231]\n"
"ds_read_b128 a[96:99], v12 offset:16384\n"
"v_mfma_f32_16x16x32_bf16 v[232:235], a[88:91], a[104:107], 1.0\n"
"ds_read_b64_tr_b16 a[36:37], v9 offset:36864\n"
"v_mfma_f32_16x16x32_bf16 v[232:235], a[92:95], a[108:111], v[232:235]\n"
"ds_read_b64_tr_b16 a[38:39], v9 offset:37376\n"
"v_mfma_f32_16x16x32_bf16 v[204:207], a[84:87], a[108:111], 1.0\n"
"ds_read_b128 a[108:111], v13 offset:18432\n"
"v_mfma_f32_16x16x32_bf16 v[204:207], a[80:83], a[104:107], v[204:207]\n"
"ds_read_b128 a[104:107], v12 offset:18432\n"
"ds_read_b64_tr_b16 a[40:41], v9 offset:32776\n"
"v_mfma_f32_16x16x32_bf16 v[208:211], a[80:83], a[112:115], 1.0\n"
"ds_read_b64_tr_b16 a[42:43], v9 offset:33288\n"
"v_mfma_f32_16x16x32_bf16 v[208:211], a[84:87], a[116:119], v[208:211]\n"
"ds_read_b64_tr_b16 a[44:45], v9 offset:36872\n"
"v_mfma_f32_16x16x32_bf16 v[236:239], a[92:95], a[116:119], 1.0\n"
"ds_read_b128 a[116:119], v13 offset:20480\n"
"v_mfma_f32_16x16x32_bf16 v[236:239], a[88:91], a[112:115], v[236:239]\n"
"ds_read_b128 a[112:115], v12 offset:20480\n"
"v_mfma_f32_16x16x32_bf16 v[240:243], a[88:91], a[120:123], 1.0\n"
"ds_read_b64_tr_b16 a[46:47], v9 offset:37384\n"
"v_mfma_f32_16x16x32_bf16 v[240:243], a[92:95], a[124:127], v[240:243]\n"
"ds_read_b64_tr_b16 a[48:49], v15 offset:32768\n"
"v_mfma_f32_16x16x32_bf16 v[212:215], a[84:87], a[124:127], 1.0\n"
"s_add_u32 s67, s75, 262144\n"
"ds_read_b128 a[124:127], v13 offset:22528\n"
"v_mfma_f32_16x16x32_bf16 v[212:215], a[80:83], a[120:123], v[212:215]\n"
"ds_read_b128 a[120:123], v12 offset:22528\n"
"v_mfma_f32_16x16x32_bf16 v[216:219], a[80:83], a[128:131], 1.0\n"
"ds_read_b64_tr_b16 a[50:51], v15 offset:33280\n"
"v_mfma_f32_16x16x32_bf16 v[216:219], a[84:87], a[132:135], v[216:219]\n"
"ds_read_b64_tr_b16 a[52:53], v15 offset:36864\n"
"v_mfma_f32_16x16x32_bf16 v[244:247], a[92:95], a[132:135], 1.0\n"
"ds_read_b128 a[132:135], v13 offset:24576\n"
"v_mfma_f32_16x16x32_bf16 v[244:247], a[88:91], a[128:131], v[244:247]\n"
"ds_read_b128 a[128:131], v12 offset:24576\n"
"v_mfma_f32_16x16x32_bf16 v[248:251], a[88:91], a[136:139], 1.0\n"
"ds_read_b64_tr_b16 a[54:55], v15 offset:37376\n"
"v_mfma_f32_16x16x32_bf16 v[248:251], a[92:95], a[140:143], v[248:251]\n"
"ds_read_b64_tr_b16 a[56:57], v15 offset:32776\n"
"v_mfma_f32_16x16x32_bf16 v[220:223], a[84:87], a[140:143], 1.0\n"
"ds_read_b128 a[140:143], v13 offset:26624\n"
"v_mfma_f32_16x16x32_bf16 v[220:223], a[80:83], a[136:139], v[220:223]\n"
"ds_read_b128 a[136:139], v12 offset:26624\n"
"v_mfma_f32_16x16x32_bf16 v[224:227], a[80:83], a[144:147], 1.0\n"
"ds_read_b64_tr_b16 a[58:59], v15 offset:33288\n"
"v_mfma_f32_16x16x32_bf16 v[224:227], a[84:87], a[148:151], v[224:227]\n"
"ds_read_b64_tr_b16 a[60:61], v15 offset:36872\n"
"v_mfma_f32_16x16x32_bf16 v[252:255], a[92:95], a[148:151], 1.0\n"
"ds_read_b128 a[148:151], v13 offset:28672\n"
"v_mfma_f32_16x16x32_bf16 v[252:255], a[88:91], a[144:147], v[252:255]\n"
"ds_read_b128 a[144:147], v12 offset:28672\n"
"ds_read_b64_tr_b16 a[62:63], v15 offset:37384\n"
"v_mfma_f32_16x16x32_bf16 a[28:31], a[88:91], a[152:155], 1.0\n"
"v_mfma_f32_16x16x32_bf16 a[28:31], a[92:95], a[156:159], a[28:31]\n"
"v_mfma_f32_16x16x32_bf16 a[24:27], a[84:87], a[156:159], 1.0\n"
"ds_read_b128 a[156:159], v13 offset:30720\n"
"v_mfma_f32_16x16x32_bf16 a[24:27], a[80:83], a[152:155], a[24:27]\n"
"s_waitcnt lgkmcnt(15)\n"
"ds_read_b128 a[152:155], v12 offset:30720\n"
"v_mfma_f32_16x16x32_bf16 v[32:35], a[32:35], a[96:99], v[32:35]\n"
"buffer_load_dwordx4 v11, s[52:55], s67 offen offset:0  lds\n"
"v_mfma_f32_16x16x32_bf16 v[32:35], a[36:39], a[100:103], v[32:35]\n"
"s_waitcnt lgkmcnt(2)\n"
"v_mfma_f32_16x16x32_bf16 v[60:63], a[44:47], a[100:103], v[60:63]\n"
"s_add_u32 s67, s75, 277504\n"
"v_mfma_f32_16x16x32_bf16 v[60:63], a[40:43], a[96:99], v[60:63]\n"
"v_mfma_f32_16x16x32_bf16 v[64:67], a[40:43], a[104:107], v[64:67]\n"
"v_mfma_f32_16x16x32_bf16 v[64:67], a[44:47], a[108:111], v[64:67]\n"
"buffer_load_dwordx4 v11, s[52:55], s67 offen offset:1024  lds\n"
"v_mfma_f32_16x16x32_bf16 v[36:39], a[36:39], a[108:111], v[36:39]\n"
"v_mfma_f32_16x16x32_bf16 v[36:39], a[32:35], a[104:107], v[36:39]\n"
"s_add_u32 s67, s75, 292864\n"
"v_mfma_f32_16x16x32_bf16 v[40:43], a[32:35], a[112:115], v[40:43]\n"
"v_mfma_f32_16x16x32_bf16 v[40:43], a[36:39], a[116:119], v[40:43]\n"
"v_mfma_f32_16x16x32_bf16 v[68:71], a[44:47], a[116:119], v[68:71]\n"
"buffer_load_dwordx4 v11, s[52:55], s67 offen offset:2048  lds\n"
"v_mfma_f32_16x16x32_bf16 v[68:71], a[40:43], a[112:115], v[68:71]\n"
"v_mfma_f32_16x16x32_bf16 v[72:75], a[40:43], a[120:123], v[72:75]\n"
"s_add_u32 s67, s75, 308224\n"
"v_mfma_f32_16x16x32_bf16 v[72:75], a[44:47], a[124:127], v[72:75]\n"
"v_mfma_f32_16x16x32_bf16 v[44:47], a[36:39], a[124:127], v[44:47]\n"
"v_mov_b32 v16, -1\n"
"v_mfma_f32_16x16x32_bf16 v[44:47], a[32:35], a[120:123], v[44:47]\n"
"buffer_load_dwordx4 v11, s[52:55], s67 offen offset:3072  lds\n"
"v_mfma_f32_16x16x32_bf16 v[48:51], a[32:35], a[128:131], v[48:51]\n"
"s_add_u32 m0, s64, 12288\n"
"v_mfma_f32_16x16x32_bf16 v[48:51], a[36:39], a[132:135], v[48:51]\n"
"v_mov_b32 v17, -1\n"
"v_mfma_f32_16x16x32_bf16 v[76:79], a[44:47], a[132:135], v[76:79]\n"
"s_waitcnt vmcnt(8)\n"
"ds_read_b64_tr_b16 a[64:65], v9 offset:0\n"
"v_mfma_f32_16x16x32_bf16 v[76:79], a[40:43], a[128:131], v[76:79]\n"
"s_add_u32 s67, s75, 327680\n"
"ds_read_b64_tr_b16 a[66:67], v9 offset:512\n"
"v_mfma_f32_16x16x32_bf16 v[80:83], a[40:43], a[136:139], v[80:83]\n"
"ds_read_b64_tr_b16 a[68:69], v9 offset:4096\n"
"buffer_load_dwordx4 v11, s[52:55], s67 offen offset:0  lds\n"
"v_mfma_f32_16x16x32_bf16 v[80:83], a[44:47], a[140:143], v[80:83]\n"
"ds_read_b64_tr_b16 a[70:71], v9 offset:4608\n"
"v_mfma_f32_16x16x32_bf16 v[52:55], a[36:39], a[140:143], v[52:55]\n"
"s_add_u32 s67, s75, 343040\n"
"ds_read_b64_tr_b16 a[72:73], v9 offset:8\n"
"v_mfma_f32_16x16x32_bf16 v[52:55], a[32:35], a[136:139], v[52:55]\n"
"s_waitcnt lgkmcnt(5)\n"
"ds_read_b64_tr_b16 a[74:75], v9 offset:520\n"
"v_mfma_f32_16x16x32_bf16 v[56:59], a[32:35], a[144:147], v[56:59]\n"
"ds_read_b64_tr_b16 a[76:77], v9 offset:4104\n"
"buffer_load_dwordx4 v11, s[52:55], s67 offen offset:1024  lds\n"
"v_mfma_f32_16x16x32_bf16 v[56:59], a[36:39], a[148:151], v[56:59]\n"
"s_add_u32 s67, s75, 358400\n"
"ds_read_b64_tr_b16 a[78:79], v9 offset:4616\n"
"v_mfma_f32_16x16x32_bf16 v[84:87], a[44:47], a[148:151], v[84:87]\n"
"s_add_u32 s69, %11, 8192\n"
"v_lshrrev_b32 v18, 1, v0\n"
"v_mfma_f32_16x16x32_bf16 v[84:87], a[40:43], a[144:147], v[84:87]\n"
"ds_read_b64_tr_b16 a[80:81], v15 offset:0\n"
"v_mfma_f32_16x16x32_bf16 a[4:7], a[40:43], a[152:155], a[4:7]\n"
"ds_read_b64_tr_b16 a[82:83], v15 offset:512\n"
"buffer_load_dwordx4 v11, s[52:55], s67 offen offset:2048  lds\n"
"v_mfma_f32_16x16x32_bf16 a[4:7], a[44:47], a[156:159], a[4:7]\n"
"ds_read_b64_tr_b16 a[84:85], v15 offset:4096\n"
"v_mfma_f32_16x16x32_bf16 a[0:3], a[36:39], a[156:159], a[0:3]\n"
"s_add_u32 s67, s75, 373760\n"
"ds_read_b64_tr_b16 a[86:87], v15 offset:4608\n"
"v_mfma_f32_16x16x32_bf16 a[0:3], a[32:35], a[152:155], a[0:3]\n"
"ds_read_b64_tr_b16 a[88:89], v15 offset:8\n"
"v_mfma_f32_16x16x32_bf16 v[88:91], a[48:51], a[96:99], v[88:91]\n"
"ds_read_b64_tr_b16 a[90:91], v15 offset:520\n"
"v_mfma_f32_16x16x32_bf16 v[88:91], a[52:55], a[100:103], v[88:91]\n"
"ds_read_b64_tr_b16 a[92:93], v15 offset:4104\n"
"buffer_load_dwordx4 v11, s[52:55], s67 offen offset:3072  lds\n"
"v_mfma_f32_16x16x32_bf16 v[116:119], a[60:63], a[100:103], v[116:119]\n"
"ds_read_b64_tr_b16 a[94:95], v15 offset:4616\n"
"v_mfma_f32_16x16x32_bf16 v[116:119], a[56:59], a[96:99], v[116:119]\n"
"s_add_u32 m0, s64, 0\n"
"v_mfma_f32_16x16x32_bf16 v[120:123], a[56:59], a[104:107], v[120:123]\n"
"v_mfma_f32_16x16x32_bf16 v[120:123], a[60:63], a[108:111], v[120:123]\n"
"v_mfma_f32_16x16x32_bf16 v[92:95], a[52:55], a[108:111], v[92:95]\n"
"s_add_u32 s67, s75, 262272\n"
"v_mfma_f32_16x16x32_bf16 v[92:95], a[48:51], a[104:107], v[92:95]\n"
"v_mfma_f32_16x16x32_bf16 v[96:99], a[48:51], a[112:115], v[96:99]\n"
"s_waitcnt lgkmcnt(0)\n"
"v_mfma_f32_16x16x32_bf16 v[96:99], a[52:55], a[116:119], v[96:99]\n"
"buffer_load_dwordx4 v11, s[52:55], s67 offen offset:0  lds\n"
"v_mfma_f32_16x16x32_bf16 v[124:127], a[60:63], a[116:119], v[124:127]\n"
"v_mfma_f32_16x16x32_bf16 v[124:127], a[56:59], a[112:115], v[124:127]\n"
"s_add_u32 s67, s75, 277632\n"
"v_mfma_f32_16x16x32_bf16 v[128:131], a[56:59], a[120:123], v[128:131]\n"
"v_mfma_f32_16x16x32_bf16 v[128:131], a[60:63], a[124:127], v[128:131]\n"
"v_mfma_f32_16x16x32_bf16 v[100:103], a[52:55], a[124:127], v[100:103]\n"
"buffer_load_dwordx4 v11, s[52:55], s67 offen offset:1024  lds\n"
"v_mfma_f32_16x16x32_bf16 v[100:103], a[48:51], a[120:123], v[100:103]\n"
"v_mfma_f32_16x16x32_bf16 v[104:107], a[48:51], a[128:131], v[104:107]\n"
"s_add_u32 s67, s75, 292992\n"
"v_mfma_f32_16x16x32_bf16 v[104:107], a[52:55], a[132:135], v[104:107]\n"
"v_mfma_f32_16x16x32_bf16 v[132:135], a[60:63], a[132:135], v[132:135]\n"
"buffer_load_dwordx4 v11, s[52:55], s67 offen offset:2048  lds\n"
"v_mfma_f32_16x16x32_bf16 v[132:135], a[56:59], a[128:131], v[132:135]\n"
"v_mfma_f32_16x16x32_bf16 v[136:139], a[56:59], a[136:139], v[136:139]\n"
"s_add_u32 s67, s75, 308352\n"
"v_mfma_f32_16x16x32_bf16 v[136:139], a[60:63], a[140:143], v[136:139]\n"
"v_mfma_f32_16x16x32_bf16 v[108:111], a[52:55], a[140:143], v[108:111]\n"
"v_mfma_f32_16x16x32_bf16 v[108:111], a[48:51], a[136:139], v[108:111]\n"
"buffer_load_dwordx4 v11, s[52:55], s67 offen offset:3072  lds\n"
"v_mfma_f32_16x16x32_bf16 v[112:115], a[48:51], a[144:147], v[112:115]\n"
"v_mfma_f32_16x16x32_bf16 v[112:115], a[52:55], a[148:151], v[112:115]\n"
"s_add_u32 m0, s64, 4096\n"
"v_mfma_f32_16x16x32_bf16 v[140:143], a[60:63], a[148:151], v[140:143]\n"
"v_mfma_f32_16x16x32_bf16 v[140:143], a[56:59], a[144:147], v[140:143]\n"
"s_add_u32 s67, s75, 327808\n"
"v_mfma_f32_16x16x32_bf16 a[12:15], a[56:59], a[152:155], a[12:15]\n"
"v_mfma_f32_16x16x32_bf16 a[12:15], a[60:63], a[156:159], a[12:15]\n"
"buffer_load_dwordx4 v11, s[52:55], s67 offen offset:0  lds\n"
"v_mfma_f32_16x16x32_bf16 a[8:11], a[52:55], a[156:159], a[8:11]\n"
"v_mfma_f32_16x16x32_bf16 a[8:11], a[48:51], a[152:155], a[8:11]\n"
"s_add_u32 s67, s75, 343168\n"
"v_mfma_f32_16x16x32_bf16 v[144:147], a[64:67], a[96:99], v[144:147]\n"
"v_mfma_f32_16x16x32_bf16 v[144:147], a[68:71], a[100:103], v[144:147]\n"
"buffer_load_dwordx4 v11, s[52:55], s67 offen offset:1024  lds\n"
"v_mfma_f32_16x16x32_bf16 v[172:175], a[76:79], a[100:103], v[172:175]\n"
"v_mfma_f32_16x16x32_bf16 v[172:175], a[72:75], a[96:99], v[172:175]\n"
"s_add_u32 s67, s75, 358528\n"
"v_mfma_f32_16x16x32_bf16 v[176:179], a[72:75], a[104:107], v[176:179]\n"
"v_mfma_f32_16x16x32_bf16 v[176:179], a[76:79], a[108:111], v[176:179]\n"
"buffer_load_dwordx4 v11, s[52:55], s67 offen offset:2048  lds\n"
"v_mfma_f32_16x16x32_bf16 v[148:151], a[68:71], a[108:111], v[148:151]\n"
"v_mfma_f32_16x16x32_bf16 v[148:151], a[64:67], a[104:107], v[148:151]\n"
"s_add_u32 s67, s75, 373888\n"
"v_mfma_f32_16x16x32_bf16 v[152:155], a[64:67], a[112:115], v[152:155]\n"
"s_sub_u32 s66, 81920, s73\n"
"v_mfma_f32_16x16x32_bf16 v[152:155], a[68:71], a[116:119], v[152:155]\n"
"buffer_load_dwordx4 v11, s[52:55], s67 offen offset:3072  lds\n"
"v_mfma_f32_16x16x32_bf16 v[180:183], a[76:79], a[116:119], v[180:183]\n"
"v_add_u32 v12, s66, v7\n"
"v_mfma_f32_16x16x32_bf16 v[180:183], a[72:75], a[112:115], v[180:183]\n"
"s_add_u32 m0, s72, 139264\n"
"v_mfma_f32_16x16x32_bf16 v[184:187], a[72:75], a[120:123], v[184:187]\n"
"v_add_u32 v13, s66, v8\n"
"v_mfma_f32_16x16x32_bf16 v[184:187], a[76:79], a[124:127], v[184:187]\n"
"s_add_u32 s67, s60, 384\n"
"v_mfma_f32_16x16x32_bf16 v[156:159], a[68:71], a[124:127], v[156:159]\n"
"v_mfma_f32_16x16x32_bf16 v[156:159], a[64:67], a[120:123], v[156:159]\n"
"buffer_load_dwordx4 v6, s[48:51], s67 offen offset:0  lds\n"
"v_mfma_f32_16x16x32_bf16 v[160:163], a[64:67], a[128:131], v[160:163]\n"
"v_mfma_f32_16x16x32_bf16 v[160:163], a[68:71], a[132:135], v[160:163]\n"
"s_add_u32 s67, s60, 15744\n"
"v_mfma_f32_16x16x32_bf16 v[188:191], a[76:79], a[132:135], v[188:191]\n"
"v_mfma_f32_16x16x32_bf16 v[188:191], a[72:75], a[128:131], v[188:191]\n"
"buffer_load_dwordx4 v6, s[48:51], s67 offen offset:1024  lds\n"
"v_mfma_f32_16x16x32_bf16 v[192:195], a[72:75], a[136:139], v[192:195]\n"
"v_mfma_f32_16x16x32_bf16 v[192:195], a[76:79], a[140:143], v[192:195]\n"
"s_add_u32 s67, s60, 31104\n"
"v_mfma_f32_16x16x32_bf16 v[164:167], a[68:71], a[140:143], v[164:167]\n"
"v_mfma_f32_16x16x32_bf16 v[164:167], a[64:67], a[136:139], v[164:167]\n"
"buffer_load_dwordx4 v6, s[48:51], s67 offen offset:2048  lds\n"
"v_mfma_f32_16x16x32_bf16 v[168:171], a[64:67], a[144:147], v[168:171]\n"
"v_mfma_f32_16x16x32_bf16 v[168:171], a[68:71], a[148:151], v[168:171]\n"
"s_add_u32 s67, s60, 46464\n"
"v_mfma_f32_16x16x32_bf16 v[196:199], a[76:79], a[148:151], v[196:199]\n"
"v_mfma_f32_16x16x32_bf16 v[196:199], a[72:75], a[144:147], v[196:199]\n"
"v_mfma_f32_16x16x32_bf16 a[20:23], a[72:75], a[152:155], a[20:23]\n"
"buffer_load_dwordx4 v6, s[48:51], s67 offen offset:3072  lds\n"
"v_mfma_f32_16x16x32_bf16 a[20:23], a[76:79], a[156:159], a[20:23]\n"
"s_add_u32 s70, s75, 655360\n"
"v_mfma_f32_16x16x32_bf16 a[16:19], a[68:71], a[156:159], a[16:19]\n"
"s_add_u32 s70, s70, s65\n"
"v_mfma_f32_16x16x32_bf16 a[16:19], a[64:67], a[152:155], a[16:19]\n"
"s_waitcnt vmcnt(20)\n"
"s_barrier\n"
"v_mfma_f32_16x16x32_bf16 v[200:203], a[80:83], a[96:99], v[200:203]\n"
"s_waitcnt vmcnt(12)\n"
"ds_read_b64_tr_b16 a[32:33], v9 offset:8192\n"
"v_mfma_f32_16x16x32_bf16 v[200:203], a[84:87], a[100:103], v[200:203]\n"
"ds_read_b64_tr_b16 a[34:35], v9 offset:8704\n"
"buffer_load_dword a252, v14, s[52:55], s70 offen\n"
"v_mfma_f32_16x16x32_bf16 v[228:231], a[92:95], a[100:103], v[228:231]\n"
"ds_read_b128 a[100:103], v13 offset:16384\n"
"v_mfma_f32_16x16x32_bf16 v[228:231], a[88:91], a[96:99], v[228:231]\n"
"ds_read_b128 a[96:99], v12 offset:16384\n"
"v_mfma_f32_16x16x32_bf16 v[232:235], a[88:91], a[104:107], v[232:235]\n"
"ds_read_b64_tr_b16 a[36:37], v9 offset:12288\n"
"v_mfma_f32_16x16x32_bf16 v[232:235], a[92:95], a[108:111], v[232:235]\n"
"ds_read_b64_tr_b16 a[38:39], v9 offset:12800\n"
"v_mfma_f32_16x16x32_bf16 v[204:207], a[84:87], a[108:111], v[204:207]\n"
"ds_read_b128 a[108:111], v13 offset:18432\n"
"v_mfma_f32_16x16x32_bf16 v[204:207], a[80:83], a[104:107], v[204:207]\n"
"ds_read_b128 a[104:107], v12 offset:18432\n"
"s_add_u32 m0, s64, 8192\n"
"ds_read_b64_tr_b16 a[40:41], v9 offset:8200\n"
"v_mfma_f32_16x16x32_bf16 v[208:211], a[80:83], a[112:115], v[208:211]\n"
"ds_read_b64_tr_b16 a[42:43], v9 offset:8712\n"
"v_mfma_f32_16x16x32_bf16 v[208:211], a[84:87], a[116:119], v[208:211]\n"
"ds_read_b64_tr_b16 a[44:45], v9 offset:12296\n"
"v_mfma_f32_16x16x32_bf16 v[236:239], a[92:95], a[116:119], v[236:239]\n"
"ds_read_b128 a[116:119], v13 offset:20480\n"
"v_mfma_f32_16x16x32_bf16 v[236:239], a[88:91], a[112:115], v[236:239]\n"
"ds_read_b128 a[112:115], v12 offset:20480\n"
"v_mfma_f32_16x16x32_bf16 v[240:243], a[88:91], a[120:123], v[240:243]\n"
"ds_read_b64_tr_b16 a[46:47], v9 offset:12808\n"
"v_mfma_f32_16x16x32_bf16 v[240:243], a[92:95], a[124:127], v[240:243]\n"
"ds_read_b64_tr_b16 a[48:49], v15 offset:8192\n"
"v_mfma_f32_16x16x32_bf16 v[212:215], a[84:87], a[124:127], v[212:215]\n"
"ds_read_b128 a[124:127], v13 offset:22528\n"
"v_mfma_f32_16x16x32_bf16 v[212:215], a[80:83], a[120:123], v[212:215]\n"
"ds_read_b128 a[120:123], v12 offset:22528\n"
"v_mfma_f32_16x16x32_bf16 v[216:219], a[80:83], a[128:131], v[216:219]\n"
"ds_read_b64_tr_b16 a[50:51], v15 offset:8704\n"
"v_mfma_f32_16x16x32_bf16 v[216:219], a[84:87], a[132:135], v[216:219]\n"
"ds_read_b64_tr_b16 a[52:53], v15 offset:12288\n"
"v_mfma_f32_16x16x32_bf16 v[244:247], a[92:95], a[132:135], v[244:247]\n"
"ds_read_b128 a[132:135], v13 offset:24576\n"
"v_mfma_f32_16x16x32_bf16 v[244:247], a[88:91], a[128:131], v[244:247]\n"
"s_add_u32 s67, s75, 393216\n"
"ds_read_b128 a[128:131], v12 offset:24576\n"
"v_mfma_f32_16x16x32_bf16 v[248:251], a[88:91], a[136:139], v[248:251]\n"
"ds_read_b64_tr_b16 a[54:55], v15 offset:12800\n"
"v_mfma_f32_16x16x32_bf16 v[248:251], a[92:95], a[140:143], v[248:251]\n"
"s_add_u32 s66, s78, 1\n"
"ds_read_b64_tr_b16 a[56:57], v15 offset:8200\n"
"v_mfma_f32_16x16x32_bf16 v[220:223], a[84:87], a[140:143], v[220:223]\n"
"ds_read_b128 a[140:143], v13 offset:26624\n"
"v_mfma_f32_16x16x32_bf16 v[220:223], a[80:83], a[136:139], v[220:223]\n"
"ds_read_b128 a[136:139], v12 offset:26624\n"
"v_mov_b32 v28, s66\n"
"v_mfma_f32_16x16x32_bf16 v[224:227], a[80:83], a[144:147], v[224:227]\n"
"ds_read_b64_tr_b16 a[58:59], v15 offset:8712\n"
"v_mov_b32 v29, s79\n"
"v_mfma_f32_16x16x32_bf16 v[224:227], a[84:87], a[148:151], v[224:227]\n"
"ds_read_b64_tr_b16 a[60:61], v15 offset:12296\n"
"v_mfma_f32_16x16x32_bf16 v[252:255], a[92:95], a[148:151], v[252:255]\n"
"buffer_store_dwordx2 v[28:29], off, s[56:59], %24 sc1\n"
"ds_read_b128 a[148:151], v13 offset:28672\n"
"v_mfma_f32_16x16x32_bf16 v[252:255], a[88:91], a[144:147], v[252:255]\n"
"ds_read_b128 a[144:147], v12 offset:28672\n"
"ds_read_b64_tr_b16 a[62:63], v15 offset:12808\n"
"v_mfma_f32_16x16x32_bf16 a[28:31], a[88:91], a[152:155], a[28:31]\n"
"v_mfma_f32_16x16x32_bf16 a[28:31], a[92:95], a[156:159], a[28:31]\n"
"v_mfma_f32_16x16x32_bf16 a[24:27], a[84:87], a[156:159], a[24:27]\n"
"ds_read_b128 a[156:159], v13 offset:30720\n"
"v_mfma_f32_16x16x32_bf16 a[24:27], a[80:83], a[152:155], a[24:27]\n"
"s_waitcnt lgkmcnt(15)\n"
"ds_read_b128 a[152:155], v12 offset:30720\n"
"v_mfma_f32_16x16x32_bf16 v[32:35], a[32:35], a[96:99], v[32:35]\n"
"v_mfma_f32_16x16x32_bf16 v[32:35], a[36:39], a[100:103], v[32:35]\n"
"s_waitcnt lgkmcnt(2)\n"
"v_mfma_f32_16x16x32_bf16 v[60:63], a[44:47], a[100:103], v[60:63]\n"
"buffer_load_dwordx4 v11, s[52:55], s67 offen offset:0  lds\n"
"v_mfma_f32_16x16x32_bf16 v[60:63], a[40:43], a[96:99], v[60:63]\n"
"v_mfma_f32_16x16x32_bf16 v[64:67], a[40:43], a[104:107], v[64:67]\n"
"s_add_u32 s67, s75, 408576\n"
"v_mfma_f32_16x16x32_bf16 v[64:67], a[44:47], a[108:111], v[64:67]\n"
"v_mfma_f32_16x16x32_bf16 v[36:39], a[36:39], a[108:111], v[36:39]\n"
"v_mfma_f32_16x16x32_bf16 v[36:39], a[32:35], a[104:107], v[36:39]\n"
"buffer_load_dwordx4 v11, s[52:55], s67 offen offset:1024  lds\n"
"v_mfma_f32_16x16x32_bf16 v[40:43], a[32:35], a[112:115], v[40:43]\n"
"v_mfma_f32_16x16x32_bf16 v[40:43], a[36:39], a[116:119], v[40:43]\n"
"s_add_u32 s67, s75, 423936\n"
"v_mfma_f32_16x16x32_bf16 v[68:71], a[44:47], a[116:119], v[68:71]\n"
"v_mfma_f32_16x16x32_bf16 v[68:71], a[40:43], a[112:115], v[68:71]\n"
"v_mfma_f32_16x16x32_bf16 v[72:75], a[40:43], a[120:123], v[72:75]\n"
"buffer_load_dwordx4 v11, s[52:55], s67 offen offset:2048  lds\n"
"v_mfma_f32_16x16x32_bf16 v[72:75], a[44:47], a[124:127], v[72:75]\n"
"v_mfma_f32_16x16x32_bf16 v[44:47], a[36:39], a[124:127], v[44:47]\n"
"s_add_u32 s67, s75, 439296\n"
"v_mfma_f32_16x16x32_bf16 v[44:47], a[32:35], a[120:123], v[44:47]\n"
"v_mfma_f32_16x16x32_bf16 v[48:51], a[32:35], a[128:131], v[48:51]\n"
"buffer_load_dwordx4 v11, s[52:55], s67 offen offset:3072  lds\n"
"v_mfma_f32_16x16x32_bf16 v[48:51], a[36:39], a[132:135], v[48:51]\n"
"s_add_u32 m0, s64, 12288\n"
"v_mfma_f32_16x16x32_bf16 v[76:79], a[44:47], a[132:135], v[76:79]\n"
"s_waitcnt vmcnt(10)\n"
"ds_read_b64_tr_b16 a[64:65], v9 offset:0\n"
"v_mfma_f32_16x16x32_bf16 v[76:79], a[40:43], a[128:131], v[76:79]\n"
"ds_read_b64_tr_b16 a[66:67], v9 offset:512\n"
"v_mfma_f32_16x16x32_bf16 v[80:83], a[40:43], a[136:139], v[80:83]\n"
"s_add_u32 s67, s75, 458752\n"
"ds_read_b64_tr_b16 a[68:69], v9 offset:4096\n"
"v_mfma_f32_16x16x32_bf16 v[80:83], a[44:47], a[140:143], v[80:83]\n"
"ds_read_b64_tr_b16 a[70:71], v9 offset:4608\n"
"v_mfma_f32_16x16x32_bf16 v[52:55], a[36:39], a[140:143], v[52:55]\n"
"ds_read_b64_tr_b16 a[72:73], v9 offset:8\n"
"buffer_load_dwordx4 v11, s[52:55], s67 offen offset:0  lds\n"
"v_mfma_f32_16x16x32_bf16 v[52:55], a[32:35], a[136:139], v[52:55]\n"
"s_waitcnt lgkmcnt(5)\n"
"ds_read_b64_tr_b16 a[74:75], v9 offset:520\n"
"v_mfma_f32_16x16x32_bf16 v[56:59], a[32:35], a[144:147], v[56:59]\n"
"s_add_u32 s67, s75, 474112\n"
"ds_read_b64_tr_b16 a[76:77], v9 offset:4104\n"
"v_mfma_f32_16x16x32_bf16 v[56:59], a[36:39], a[148:151], v[56:59]\n"
"ds_read_b64_tr_b16 a[78:79], v9 offset:4616\n"
"v_mfma_f32_16x16x32_bf16 v[84:87], a[44:47], a[148:151], v[84:87]\n"
"buffer_load_dwordx4 v11, s[52:55], s67 offen offset:1024  lds\n"
"v_mfma_f32_16x16x32_bf16 v[84:87], a[40:43], a[144:147], v[84:87]\n"
"ds_read_b64_tr_b16 a[80:81], v15 offset:0\n"
"v_mfma_f32_16x16x32_bf16 a[4:7], a[40:43], a[152:155], a[4:7]\n"
"s_add_u32 s67, s75, 489472\n"
"ds_read_b64_tr_b16 a[82:83], v15 offset:512\n"
"v_mfma_f32_16x16x32_bf16 a[4:7], a[44:47], a[156:159], a[4:7]\n"
"ds_read_b64_tr_b16 a[84:85], v15 offset:4096\n"
"v_mfma_f32_16x16x32_bf16 a[0:3], a[36:39], a[156:159], a[0:3]\n"
"ds_read_b64_tr_b16 a[86:87], v15 offset:4608\n"
"buffer_load_dwordx4 v11, s[52:55], s67 offen offset:2048  lds\n"
"v_mfma_f32_16x16x32_bf16 a[0:3], a[32:35], a[152:155], a[0:3]\n"
"ds_read_b64_tr_b16 a[88:89], v15 offset:8\n"
"v_mfma_f32_16x16x32_bf16 v[88:91], a[48:51], a[96:99], v[88:91]\n"
"s_add_u32 s67, s75, 504832\n"
"ds_read_b64_tr_b16 a[90:91], v15 offset:520\n"
"v_mfma_f32_16x16x32_bf16 v[88:91], a[52:55], a[100:103], v[88:91]\n"
"ds_read_b64_tr_b16 a[92:93], v15 offset:4104\n"
"v_mfma_f32_16x16x32_bf16 v[116:119], a[60:63], a[100:103], v[116:119]\n"
"ds_read_b64_tr_b16 a[94:95], v15 offset:4616\n"
"v_mfma_f32_16x16x32_bf16 v[116:119], a[56:59], a[96:99], v[116:119]\n"
"buffer_load_dwordx4 v11, s[52:55], s67 offen offset:3072  lds\n"
"v_mfma_f32_16x16x32_bf16 v[120:123], a[56:59], a[104:107], v[120:123]\n"
"v_mfma_f32_16x16x32_bf16 v[120:123], a[60:63], a[108:111], v[120:123]\n"
"s_add_u32 m0, s64, 0\n"
"v_mfma_f32_16x16x32_bf16 v[92:95], a[52:55], a[108:111], v[92:95]\n"
"v_mfma_f32_16x16x32_bf16 v[92:95], a[48:51], a[104:107], v[92:95]\n"
"s_add_u32 s67, s75, 393344\n"
"v_mfma_f32_16x16x32_bf16 v[96:99], a[48:51], a[112:115], v[96:99]\n"
"s_waitcnt lgkmcnt(0)\n"
"v_mfma_f32_16x16x32_bf16 v[96:99], a[52:55], a[116:119], v[96:99]\n"
"buffer_load_dwordx4 v11, s[52:55], s67 offen offset:0  lds\n"
"v_mfma_f32_16x16x32_bf16 v[124:127], a[60:63], a[116:119], v[124:127]\n"
"v_mfma_f32_16x16x32_bf16 v[124:127], a[56:59], a[112:115], v[124:127]\n"
"s_add_u32 s67, s75, 408704\n"
"v_mfma_f32_16x16x32_bf16 v[128:131], a[56:59], a[120:123], v[128:131]\n"
"v_mfma_f32_16x16x32_bf16 v[128:131], a[60:63], a[124:127], v[128:131]\n"
"v_mfma_f32_16x16x32_bf16 v[100:103], a[52:55], a[124:127], v[100:103]\n"
"buffer_load_dwordx4 v11, s[52:55], s67 offen offset:1024  lds\n"
"v_mfma_f32_16x16x32_bf16 v[100:103], a[48:51], a[120:123], v[100:103]\n"
"v_mfma_f32_16x16x32_bf16 v[104:107], a[48:51], a[128:131], v[104:107]\n"
"s_add_u32 s67, s75, 424064\n"
"v_mfma_f32_16x16x32_bf16 v[104:107], a[52:55], a[132:135], v[104:107]\n"
"v_mfma_f32_16x16x32_bf16 v[132:135], a[60:63], a[132:135], v[132:135]\n"
"v_mfma_f32_16x16x32_bf16 v[132:135], a[56:59], a[128:131], v[132:135]\n"
"buffer_load_dwordx4 v11, s[52:55], s67 offen offset:2048  lds\n"
"v_mfma_f32_16x16x32_bf16 v[136:139], a[56:59], a[136:139], v[136:139]\n"
"v_mfma_f32_16x16x32_bf16 v[136:139], a[60:63], a[140:143], v[136:139]\n"
"s_add_u32 s67, s75, 439424\n"
"v_mfma_f32_16x16x32_bf16 v[108:111], a[52:55], a[140:143], v[108:111]\n"
"v_mfma_f32_16x16x32_bf16 v[108:111], a[48:51], a[136:139], v[108:111]\n"
"buffer_load_dwordx4 v11, s[52:55], s67 offen offset:3072  lds\n"
"v_mfma_f32_16x16x32_bf16 v[112:115], a[48:51], a[144:147], v[112:115]\n"
"v_mfma_f32_16x16x32_bf16 v[112:115], a[52:55], a[148:151], v[112:115]\n"
"s_add_u32 m0, s64, 4096\n"
"v_mfma_f32_16x16x32_bf16 v[140:143], a[60:63], a[148:151], v[140:143]\n"
"v_mfma_f32_16x16x32_bf16 v[140:143], a[56:59], a[144:147], v[140:143]\n"
"s_add_u32 s67, s75, 458880\n"
"v_mfma_f32_16x16x32_bf16 a[12:15], a[56:59], a[152:155], a[12:15]\n"
"v_mfma_f32_16x16x32_bf16 a[12:15], a[60:63], a[156:159], a[12:15]\n"
"buffer_load_dwordx4 v11, s[52:55], s67 offen offset:0  lds\n"
"v_mfma_f32_16x16x32_bf16 a[8:11], a[52:55], a[156:159], a[8:11]\n"
"v_mfma_f32_16x16x32_bf16 a[8:11], a[48:51], a[152:155], a[8:11]\n"
"s_add_u32 s67, s75, 474240\n"
"v_mfma_f32_16x16x32_bf16 v[144:147], a[64:67], a[96:99], v[144:147]\n"
"v_mfma_f32_16x16x32_bf16 v[144:147], a[68:71], a[100:103], v[144:147]\n"
"buffer_load_dwordx4 v11, s[52:55], s67 offen offset:1024  lds\n"
"v_mfma_f32_16x16x32_bf16 v[172:175], a[76:79], a[100:103], v[172:175]\n"
"v_mfma_f32_16x16x32_bf16 v[172:175], a[72:75], a[96:99], v[172:175]\n"
"s_add_u32 s67, s75, 489600\n"
"v_mfma_f32_16x16x32_bf16 v[176:179], a[72:75], a[104:107], v[176:179]\n"
"v_mfma_f32_16x16x32_bf16 v[176:179], a[76:79], a[108:111], v[176:179]\n"
"buffer_load_dwordx4 v11, s[52:55], s67 offen offset:2048  lds\n"
"v_mfma_f32_16x16x32_bf16 v[148:151], a[68:71], a[108:111], v[148:151]\n"
"v_mfma_f32_16x16x32_bf16 v[148:151], a[64:67], a[104:107], v[148:151]\n"
"s_add_u32 s67, s75, 504960\n"
"v_mfma_f32_16x16x32_bf16 v[152:155], a[64:67], a[112:115], v[152:155]\n"
"s_sub_u32 s66, 122880, s73\n"
"v_mfma_f32_16x16x32_bf16 v[152:155], a[68:71], a[116:119], v[152:155]\n"
"v_mfma_f32_16x16x32_bf16 v[180:183], a[76:79], a[116:119], v[180:183]\n"
"v_add_u32 v12, s66, v7\n"
"v_mfma_f32_16x16x32_bf16 v[180:183], a[72:75], a[112:115], v[180:183]\n"
"buffer_load_dwordx4 v11, s[52:55], s67 offen offset:3072  lds\n"
"v_mfma_f32_16x16x32_bf16 v[184:187], a[72:75], a[120:123], v[184:187]\n"
"s_add_u32 m0, s72, 16384\n"
"v_add_u32 v13, s66, v8\n"
"v_mfma_f32_16x16x32_bf16 v[184:187], a[76:79], a[124:127], v[184:187]\n"
"v_mfma_f32_16x16x32_bf16 v[156:159], a[68:71], a[124:127], v[156:159]\n"
"s_add_u32 s67, s60, 512\n"
"v_mfma_f32_16x16x32_bf16 v[156:159], a[64:67], a[120:123], v[156:159]\n"
"buffer_load_dwordx4 v6, s[48:51], s67 offen offset:0  lds\n"
"v_mfma_f32_16x16x32_bf16 v[160:163], a[64:67], a[128:131], v[160:163]\n"
"v_mfma_f32_16x16x32_bf16 v[160:163], a[68:71], a[132:135], v[160:163]\n"
"s_add_u32 s67, s60, 15872\n"
"v_mfma_f32_16x16x32_bf16 v[188:191], a[76:79], a[132:135], v[188:191]\n"
"v_mfma_f32_16x16x32_bf16 v[188:191], a[72:75], a[128:131], v[188:191]\n"
"buffer_load_dwordx4 v6, s[48:51], s67 offen offset:1024  lds\n"
"v_mfma_f32_16x16x32_bf16 v[192:195], a[72:75], a[136:139], v[192:195]\n"
"v_mfma_f32_16x16x32_bf16 v[192:195], a[76:79], a[140:143], v[192:195]\n"
"s_add_u32 s67, s60, 31232\n"
"v_mfma_f32_16x16x32_bf16 v[164:167], a[68:71], a[140:143], v[164:167]\n"
"v_mfma_f32_16x16x32_bf16 v[164:167], a[64:67], a[136:139], v[164:167]\n"
"buffer_load_dwordx4 v6, s[48:51], s67 offen offset:2048  lds\n"
"v_mfma_f32_16x16x32_bf16 v[168:171], a[64:67], a[144:147], v[168:171]\n"
"v_mfma_f32_16x16x32_bf16 v[168:171], a[68:71], a[148:151], v[168:171]\n"
"s_add_u32 s67, s60, 46592\n"
"v_mfma_f32_16x16x32_bf16 v[196:199], a[76:79], a[148:151], v[196:199]\n"
"v_mfma_f32_16x16x32_bf16 v[196:199], a[72:75], a[144:147], v[196:199]\n"
"v_mfma_f32_16x16x32_bf16 a[20:23], a[72:75], a[152:155], a[20:23]\n"
"buffer_load_dwordx4 v6, s[48:51], s67 offen offset:3072  lds\n"
"v_mfma_f32_16x16x32_bf16 a[20:23], a[76:79], a[156:159], a[20:23]\n"
"v_mfma_f32_16x16x32_bf16 a[16:19], a[68:71], a[156:159], a[16:19]\n"
"s_add_u32 s70, s75, 786432\n"
"s_add_u32 s70, s70, s65\n"
"v_mfma_f32_16x16x32_bf16 a[16:19], a[64:67], a[152:155], a[16:19]\n"
"s_waitcnt vmcnt(22)\n"
"s_barrier\n"
"v_mfma_f32_16x16x32_bf16 v[200:203], a[80:83], a[96:99], v[200:203]\n"
"s_waitcnt vmcnt(12)\n"
"ds_read_b64_tr_b16 a[32:33], v9 offset:8192\n"
"v_mfma_f32_16x16x32_bf16 v[200:203], a[84:87], a[100:103], v[200:203]\n"
"ds_read_b64_tr_b16 a[34:35], v9 offset:8704\n"
"v_mfma_f32_16x16x32_bf16 v[228:231], a[92:95], a[100:103], v[228:231]\n"
"ds_read_b128 a[100:103], v13 offset:16384\n"
"buffer_load_dword a252, v14, s[52:55], s70 offen\n"
"v_mfma_f32_16x16x32_bf16 v[228:231], a[88:91], a[96:99], v[228:231]\n"
"ds_read_b128 a[96:99], v12 offset:16384\n"
"v_mfma_f32_16x16x32_bf16 v[232:235], a[88:91], a[104:107], v[232:235]\n"
"ds_read_b64_tr_b16 a[36:37], v9 offset:12288\n"
"v_mfma_f32_16x16x32_bf16 v[232:235], a[92:95], a[108:111], v[232:235]\n"
"ds_read_b64_tr_b16 a[38:39], v9 offset:12800\n"
"v_mfma_f32_16x16x32_bf16 v[204:207], a[84:87], a[108:111], v[204:207]\n"
"ds_read_b128 a[108:111], v13 offset:18432\n"
"v_mfma_f32_16x16x32_bf16 v[204:207], a[80:83], a[104:107], v[204:207]\n"
"ds_read_b128 a[104:107], v12 offset:18432\n"
"ds_read_b64_tr_b16 a[40:41], v9 offset:8200\n"
"v_mfma_f32_16x16x32_bf16 v[208:211], a[80:83], a[112:115], v[208:211]\n"
"s_add_u32 m0, s64, 8192\n"
"ds_read_b64_tr_b16 a[42:43], v9 offset:8712\n"
"v_mfma_f32_16x16x32_bf16 v[208:211], a[84:87], a[116:119], v[208:211]\n"
"ds_read_b64_tr_b16 a[44:45], v9 offset:12296\n"
"v_mfma_f32_16x16x32_bf16 v[236:239], a[92:95], a[116:119], v[236:239]\n"
"ds_read_b128 a[116:119], v13 offset:20480\n"
"v_mfma_f32_16x16x32_bf16 v[236:239], a[88:91], a[112:115], v[236:239]\n"
"ds_read_b128 a[112:115], v12 offset:20480\n"
"v_mfma_f32_16x16x32_bf16 v[240:243], a[88:91], a[120:123], v[240:243]\n"
"ds_read_b64_tr_b16 a[46:47], v9 offset:12808\n"
"v_mfma_f32_16x16x32_bf16 v[240:243], a[92:95], a[124:127], v[240:243]\n"
"ds_read_b64_tr_b16 a[48:49], v15 offset:8192\n"
"v_mfma_f32_16x16x32_bf16 v[212:215], a[84:87], a[124:127], v[212:215]\n"
"ds_read_b128 a[124:127], v13 offset:22528\n"
"v_mfma_f32_16x16x32_bf16 v[212:215], a[80:83], a[120:123], v[212:215]\n"
"ds_read_b128 a[120:123], v12 offset:22528\n"
"v_mfma_f32_16x16x32_bf16 v[216:219], a[80:83], a[128:131], v[216:219]\n"
"ds_read_b64_tr_b16 a[50:51], v15 offset:8704\n"
"v_mfma_f32_16x16x32_bf16 v[216:219], a[84:87], a[132:135], v[216:219]\n"
"ds_read_b64_tr_b16 a[52:53], v15 offset:12288\n"
"v_mfma_f32_16x16x32_bf16 v[244:247], a[92:95], a[132:135], v[244:247]\n"
"ds_read_b128 a[132:135], v13 offset:24576\n"
"v_mfma_f32_16x16x32_bf16 v[244:247], a[88:91], a[128:131], v[244:247]\n"
"s_add_u32 s67, s75, 524288\n"
"ds_read_b128 a[128:131], v12 offset:24576\n"
"v_mfma_f32_16x16x32_bf16 v[248:251], a[88:91], a[136:139], v[248:251]\n"
"ds_read_b64_tr_b16 a[54:55], v15 offset:12800\n"
"v_mfma_f32_16x16x32_bf16 v[248:251], a[92:95], a[140:143], v[248:251]\n"
"ds_read_b64_tr_b16 a[56:57], v15 offset:8200\n"
"v_mfma_f32_16x16x32_bf16 v[220:223], a[84:87], a[140:143], v[220:223]\n"
"ds_read_b128 a[140:143], v13 offset:26624\n"
"v_mfma_f32_16x16x32_bf16 v[220:223], a[80:83], a[136:139], v[220:223]\n"
"ds_read_b128 a[136:139], v12 offset:26624\n"
"v_mfma_f32_16x16x32_bf16 v[224:227], a[80:83], a[144:147], v[224:227]\n"
"ds_read_b64_tr_b16 a[58:59], v15 offset:8712\n"
"v_mfma_f32_16x16x32_bf16 v[224:227], a[84:87], a[148:151], v[224:227]\n"
"ds_read_b64_tr_b16 a[60:61], v15 offset:12296\n"
"v_mfma_f32_16x16x32_bf16 v[252:255], a[92:95], a[148:151], v[252:255]\n"
"ds_read_b128 a[148:151], v13 offset:28672\n"
"v_mfma_f32_16x16x32_bf16 v[252:255], a[88:91], a[144:147], v[252:255]\n"
"ds_read_b128 a[144:147], v12 offset:28672\n"
"ds_read_b64_tr_b16 a[62:63], v15 offset:12808\n"
"v_mfma_f32_16x16x32_bf16 a[28:31], a[88:91], a[152:155], a[28:31]\n"
"v_mfma_f32_16x16x32_bf16 a[28:31], a[92:95], a[156:159], a[28:31]\n"
"v_mfma_f32_16x16x32_bf16 a[24:27], a[84:87], a[156:159], a[24:27]\n"
"ds_read_b128 a[156:159], v13 offset:30720\n"
"v_mfma_f32_16x16x32_bf16 a[24:27], a[80:83], a[152:155], a[24:27]\n"
"s_waitcnt lgkmcnt(15)\n"
"ds_read_b128 a[152:155], v12 offset:30720\n"
"v_mfma_f32_16x16x32_bf16 v[32:35], a[32:35], a[96:99], v[32:35]\n"
"v_mfma_f32_16x16x32_bf16 v[32:35], a[36:39], a[100:103], v[32:35]\n"
"s_waitcnt lgkmcnt(2)\n"
"v_mfma_f32_16x16x32_bf16 v[60:63], a[44:47], a[100:103], v[60:63]\n"
"buffer_load_dwordx4 v11, s[52:55], s67 offen offset:0  lds\n"
"v_mfma_f32_16x16x32_bf16 v[60:63], a[40:43], a[96:99], v[60:63]\n"
"v_mfma_f32_16x16x32_bf16 v[64:67], a[40:43], a[104:107], v[64:67]\n"
"s_add_u32 s67, s75, 539648\n"
"v_mfma_f32_16x16x32_bf16 v[64:67], a[44:47], a[108:111], v[64:67]\n"
"v_mfma_f32_16x16x32_bf16 v[36:39], a[36:39], a[108:111], v[36:39]\n"
"buffer_load_dwordx4 v11, s[52:55], s67 offen offset:1024  lds\n"
"v_mfma_f32_16x16x32_bf16 v[36:39], a[32:35], a[104:107], v[36:39]\n"
"v_mfma_f32_16x16x32_bf16 v[40:43], a[32:35], a[112:115], v[40:43]\n"
"s_add_u32 s67, s75, 555008\n"
"v_mfma_f32_16x16x32_bf16 v[40:43], a[36:39], a[116:119], v[40:43]\n"
"v_mfma_f32_16x16x32_bf16 v[68:71], a[44:47], a[116:119], v[68:71]\n"
"v_mfma_f32_16x16x32_bf16 v[68:71], a[40:43], a[112:115], v[68:71]\n"
"buffer_load_dwordx4 v11, s[52:55], s67 offen offset:2048  lds\n"
"v_mfma_f32_16x16x32_bf16 v[72:75], a[40:43], a[120:123], v[72:75]\n"
"v_mfma_f32_16x16x32_bf16 v[72:75], a[44:47], a[124:127], v[72:75]\n"
"s_add_u32 s67, s75, 570368\n"
"v_mfma_f32_16x16x32_bf16 v[44:47], a[36:39], a[124:127], v[44:47]\n"
"v_mfma_f32_16x16x32_bf16 v[44:47], a[32:35], a[120:123], v[44:47]\n"
"buffer_load_dwordx4 v11, s[52:55], s67 offen offset:3072  lds\n"
"v_mfma_f32_16x16x32_bf16 v[48:51], a[32:35], a[128:131], v[48:51]\n"
"v_mfma_f32_16x16x32_bf16 v[48:51], a[36:39], a[132:135], v[48:51]\n"
"s_add_u32 m0, s64, 12288\n"
"v_mfma_f32_16x16x32_bf16 v[76:79], a[44:47], a[132:135], v[76:79]\n"
"s_waitcnt vmcnt(9)\n"
"ds_read_b64_tr_b16 a[64:65], v9 offset:0\n"
"v_mfma_f32_16x16x32_bf16 v[76:79], a[40:43], a[128:131], v[76:79]\n"
"s_add_u32 s67, s75, 589824\n"
"ds_read_b64_tr_b16 a[66:67], v9 offset:512\n"
"v_mfma_f32_16x16x32_bf16 v[80:83], a[40:43], a[136:139], v[80:83]\n"
"ds_read_b64_tr_b16 a[68:69], v9 offset:4096\n"
"v_mfma_f32_16x16x32_bf16 v[80:83], a[44:47], a[140:143], v[80:83]\n"
"ds_read_b64_tr_b16 a[70:71], v9 offset:4608\n"
"buffer_load_dwordx4 v11, s[52:55], s67 offen offset:0  lds\n"
"v_mfma_f32_16x16x32_bf16 v[52:55], a[36:39], a[140:143], v[52:55]\n"
"s_add_u32 s67, s75, 605184\n"
"ds_read_b64_tr_b16 a[72:73], v9 offset:8\n"
"v_mfma_f32_16x16x32_bf16 v[52:55], a[32:35], a[136:139], v[52:55]\n"
"s_waitcnt lgkmcnt(5)\n"
"ds_read_b64_tr_b16 a[74:75], v9 offset:520\n"
"v_mfma_f32_16x16x32_bf16 v[56:59], a[32:35], a[144:147], v[56:59]\n"
"ds_read_b64_tr_b16 a[76:77], v9 offset:4104\n"
"v_mfma_f32_16x16x32_bf16 v[56:59], a[36:39], a[148:151], v[56:59]\n"
"ds_read_b64_tr_b16 a[78:79], v9 offset:4616\n"
"v_mfma_f32_16x16x32_bf16 v[84:87], a[44:47], a[148:151], v[84:87]\n"
"buffer_load_dwordx4 v11, s[52:55], s67 offen offset:1024  lds\n"
"v_mfma_f32_16x16x32_bf16 v[84:87], a[40:43], a[144:147], v[84:87]\n"
"ds_read_b64_tr_b16 a[80:81], v15 offset:0\n"
"v_mfma_f32_16x16x32_bf16 a[4:7], a[40:43], a[152:155], a[4:7]\n"
"s_add_u32 s67, s75, 620544\n"
"ds_read_b64_tr_b16 a[82:83], v15 offset:512\n"
"v_mfma_f32_16x16x32_bf16 a[4:7], a[44:47], a[156:159], a[4:7]\n"
"ds_read_b64_tr_b16 a[84:85], v15 offset:4096\n"
"v_mfma_f32_16x16x32_bf16 a[0:3], a[36:39], a[156:159], a[0:3]\n"
"ds_read_b64_tr_b16 a[86:87], v15 offset:4608\n"
"buffer_load_dwordx4 v11, s[52:55], s67 offen offset:2048  lds\n"
"v_mfma_f32_16x16x32_bf16 a[0:3], a[32:35], a[152:155], a[0:3]\n"
"ds_read_b64_tr_b16 a[88:89], v15 offset:8\n"
"v_mfma_f32_16x16x32_bf16 v[88:91], a[48:51], a[96:99], v[88:91]\n"
"s_add_u32 s67, s75, 635904\n"
"ds_read_b64_tr_b16 a[90:91], v15 offset:520\n"
"v_mfma_f32_16x16x32_bf16 v[88:91], a[52:55], a[100:103], v[88:91]\n"
"ds_read_b64_tr_b16 a[92:93], v15 offset:4104\n"
"v_mfma_f32_16x16x32_bf16 v[116:119], a[60:63], a[100:103], v[116:119]\n"
"ds_read_b64_tr_b16 a[94:95], v15 offset:4616\n"
"v_mfma_f32_16x16x32_bf16 v[116:119], a[56:59], a[96:99], v[116:119]\n"
"buffer_load_dwordx4 v11, s[52:55], s67 offen offset:3072  lds\n"
"v_mfma_f32_16x16x32_bf16 v[120:123], a[56:59], a[104:107], v[120:123]\n"
"v_mfma_f32_16x16x32_bf16 v[120:123], a[60:63], a[108:111], v[120:123]\n"
"s_add_u32 m0, s64, 0\n"
"v_mfma_f32_16x16x32_bf16 v[92:95], a[52:55], a[108:111], v[92:95]\n"
"v_mfma_f32_16x16x32_bf16 v[92:95], a[48:51], a[104:107], v[92:95]\n"
"s_add_u32 s67, s75, 524416\n"
"v_mfma_f32_16x16x32_bf16 v[96:99], a[48:51], a[112:115], v[96:99]\n"
"s_waitcnt lgkmcnt(0)\n"
"v_mfma_f32_16x16x32_bf16 v[96:99], a[52:55], a[116:119], v[96:99]\n"
"buffer_load_dwordx4 v11, s[52:55], s67 offen offset:0  lds\n"
"v_mfma_f32_16x16x32_bf16 v[124:127], a[60:63], a[116:119], v[124:127]\n"
"v_mfma_f32_16x16x32_bf16 v[124:127], a[56:59], a[112:115], v[124:127]\n"
"s_add_u32 s67, s75, 539776\n"
"v_mfma_f32_16x16x32_bf16 v[128:131], a[56:59], a[120:123], v[128:131]\n"
"v_mfma_f32_16x16x32_bf16 v[128:131], a[60:63], a[124:127], v[128:131]\n"
"v_mfma_f32_16x16x32_bf16 v[100:103], a[52:55], a[124:127], v[100:103]\n"
"buffer_load_dwordx4 v11, s[52:55], s67 offen offset:1024  lds\n"
"v_mfma_f32_16x16x32_bf16 v[100:103], a[48:51], a[120:123], v[100:103]\n"
"v_mfma_f32_16x16x32_bf16 v[104:107], a[48:51], a[128:131], v[104:107]\n"
"s_add_u32 s67, s75, 555136\n"
"v_mfma_f32_16x16x32_bf16 v[104:107], a[52:55], a[132:135], v[104:107]\n"
"v_mfma_f32_16x16x32_bf16 v[132:135], a[60:63], a[132:135], v[132:135]\n"
"v_mfma_f32_16x16x32_bf16 v[132:135], a[56:59], a[128:131], v[132:135]\n"
"buffer_load_dwordx4 v11, s[52:55], s67 offen offset:2048  lds\n"
"v_mfma_f32_16x16x32_bf16 v[136:139], a[56:59], a[136:139], v[136:139]\n"
"v_mfma_f32_16x16x32_bf16 v[136:139], a[60:63], a[140:143], v[136:139]\n"
"s_add_u32 s67, s75, 570496\n"
"v_mfma_f32_16x16x32_bf16 v[108:111], a[52:55], a[140:143], v[108:111]\n"
"v_mfma_f32_16x16x32_bf16 v[108:111], a[48:51], a[136:139], v[108:111]\n"
"buffer_load_dwordx4 v11, s[52:55], s67 offen offset:3072  lds\n"
"v_mfma_f32_16x16x32_bf16 v[112:115], a[48:51], a[144:147], v[112:115]\n"
"v_mfma_f32_16x16x32_bf16 v[112:115], a[52:55], a[148:151], v[112:115]\n"
"s_add_u32 m0, s64, 4096\n"
"v_mfma_f32_16x16x32_bf16 v[140:143], a[60:63], a[148:151], v[140:143]\n"
"v_mfma_f32_16x16x32_bf16 v[140:143], a[56:59], a[144:147], v[140:143]\n"
"s_add_u32 s67, s75, 589952\n"
"v_mfma_f32_16x16x32_bf16 a[12:15], a[56:59], a[152:155], a[12:15]\n"
"v_mfma_f32_16x16x32_bf16 a[12:15], a[60:63], a[156:159], a[12:15]\n"
"buffer_load_dwordx4 v11, s[52:55], s67 offen offset:0  lds\n"
"v_mfma_f32_16x16x32_bf16 a[8:11], a[52:55], a[156:159], a[8:11]\n"
"v_mfma_f32_16x16x32_bf16 a[8:11], a[48:51], a[152:155], a[8:11]\n"
"s_add_u32 s67, s75, 605312\n"
"v_mfma_f32_16x16x32_bf16 v[144:147], a[64:67], a[96:99], v[144:147]\n"
"v_mfma_f32_16x16x32_bf16 v[144:147], a[68:71], a[100:103], v[144:147]\n"
"buffer_load_dwordx4 v11, s[52:55], s67 offen offset:1024  lds\n"
"v_mfma_f32_16x16x32_bf16 v[172:175], a[76:79], a[100:103], v[172:175]\n"
"v_mfma_f32_16x16x32_bf16 v[172:175], a[72:75], a[96:99], v[172:175]\n"
"s_add_u32 s67, s75, 620672\n"
"v_mfma_f32_16x16x32_bf16 v[176:179], a[72:75], a[104:107], v[176:179]\n"
"v_mfma_f32_16x16x32_bf16 v[176:179], a[76:79], a[108:111], v[176:179]\n"
"buffer_load_dwordx4 v11, s[52:55], s67 offen offset:2048  lds\n"
"v_mfma_f32_16x16x32_bf16 v[148:151], a[68:71], a[108:111], v[148:151]\n"
"v_mfma_f32_16x16x32_bf16 v[148:151], a[64:67], a[104:107], v[148:151]\n"
"s_add_u32 s67, s75, 636032\n"
"v_mfma_f32_16x16x32_bf16 v[152:155], a[64:67], a[112:115], v[152:155]\n"
"s_sub_u32 s66, 0, s73\n"
"v_mfma_f32_16x16x32_bf16 v[152:155], a[68:71], a[116:119], v[152:155]\n"
"v_mfma_f32_16x16x32_bf16 v[180:183], a[76:79], a[116:119], v[180:183]\n"
"v_add_u32 v12, s66, v7\n"
"v_mfma_f32_16x16x32_bf16 v[180:183], a[72:75], a[112:115], v[180:183]\n"
"buffer_load_dwordx4 v11, s[52:55], s67 offen offset:3072  lds\n"
"v_mfma_f32_16x16x32_bf16 v[184:187], a[72:75], a[120:123], v[184:187]\n"
"s_add_u32 m0, s72, 57344\n"
"v_add_u32 v13, s66, v8\n"
"v_mfma_f32_16x16x32_bf16 v[184:187], a[76:79], a[124:127], v[184:187]\n"
"v_mfma_f32_16x16x32_bf16 v[156:159], a[68:71], a[124:127], v[156:159]\n"
"s_add_u32 s67, s60, 640\n"
"v_mfma_f32_16x16x32_bf16 v[156:159], a[64:67], a[120:123], v[156:159]\n"
"buffer_load_dwordx4 v6, s[48:51], s67 offen offset:0  lds\n"
"v_mfma_f32_16x16x32_bf16 v[160:163], a[64:67], a[128:131], v[160:163]\n"
"v_mfma_f32_16x16x32_bf16 v[160:163], a[68:71], a[132:135], v[160:163]\n"
"s_add_u32 s67, s60, 16000\n"
"v_mfma_f32_16x16x32_bf16 v[188:191], a[76:79], a[132:135], v[188:191]\n"
"v_mfma_f32_16x16x32_bf16 v[188:191], a[72:75], a[128:131], v[188:191]\n"
"buffer_load_dwordx4 v6, s[48:51], s67 offen offset:1024  lds\n"
"v_mfma_f32_16x16x32_bf16 v[192:195], a[72:75], a[136:139], v[192:195]\n"
"v_mfma_f32_16x16x32_bf16 v[192:195], a[76:79], a[140:143], v[192:195]\n"
"s_add_u32 s67, s60, 31360\n"
"v_mfma_f32_16x16x32_bf16 v[164:167], a[68:71], a[140:143], v[164:167]\n"
"v_mfma_f32_16x16x32_bf16 v[164:167], a[64:67], a[136:139], v[164:167]\n"
"buffer_load_dwordx4 v6, s[48:51], s67 offen offset:2048  lds\n"
"v_mfma_f32_16x16x32_bf16 v[168:171], a[64:67], a[144:147], v[168:171]\n"
"v_mfma_f32_16x16x32_bf16 v[168:171], a[68:71], a[148:151], v[168:171]\n"
"s_add_u32 s67, s60, 46720\n"
"v_mfma_f32_16x16x32_bf16 v[196:199], a[76:79], a[148:151], v[196:199]\n"
"v_mfma_f32_16x16x32_bf16 v[196:199], a[72:75], a[144:147], v[196:199]\n"
"v_mfma_f32_16x16x32_bf16 a[20:23], a[72:75], a[152:155], a[20:23]\n"
"buffer_load_dwordx4 v6, s[48:51], s67 offen offset:3072  lds\n"
"v_mfma_f32_16x16x32_bf16 a[20:23], a[76:79], a[156:159], a[20:23]\n"
"s_sub_u32 s70, 1048576, s75\n"
"v_and_b32 v22, 0x20000, v14\n"
"v_mfma_f32_16x16x32_bf16 a[16:19], a[68:71], a[156:159], a[16:19]\n"
"v_add_u32 v14, v22, v14\n"
"v_mfma_f32_16x16x32_bf16 a[16:19], a[64:67], a[152:155], a[16:19]\n"
"s_waitcnt vmcnt(21)\n"
"s_barrier\n"
"v_mfma_f32_16x16x32_bf16 v[200:203], a[80:83], a[96:99], v[200:203]\n"
"s_waitcnt vmcnt(12)\n"
"ds_read_b64_tr_b16 a[32:33], v9 offset:8192\n"
"v_mfma_f32_16x16x32_bf16 v[200:203], a[84:87], a[100:103], v[200:203]\n"
"s_add_u32 s70, s70, s65\n"
"ds_read_b64_tr_b16 a[34:35], v9 offset:8704\n"
"s_getpc_b64 s[96:97]\n"
"L_c10_:\n"
"s_add_u32 s96, s96, L_H6S-L_c10_\n"
"s_addc_u32 s97, s97, 0\n"
"v_mfma_f32_16x16x32_bf16 v[228:231], a[92:95], a[100:103], v[228:231]\n"
"s_lshr_b32 s66, %26, 3\n"
"ds_read_b128 a[100:103], v13 offset:16384\n"
"buffer_load_dword a252, v14, s[52:55], s70 offen\n"
"v_mfma_f32_16x16x32_bf16 v[228:231], a[88:91], a[96:99], v[228:231]\n"
"s_lshl2_add_u32 s66, s66, %16\n"
"ds_read_b128 a[96:99], v12 offset:16384\n"
"v_mfma_f32_16x16x32_bf16 v[232:235], a[88:91], a[104:107], v[232:235]\n"
"s_mul_i32 s66, s66, 128\n"
"ds_read_b64_tr_b16 a[36:37], v9 offset:12288\n"
"v_mfma_f32_16x16x32_bf16 v[232:235], a[92:95], a[108:111], v[232:235]\n"
"s_min_u32 s66, s66, L_headF-L_H6S-128\n"
"ds_read_b64_tr_b16 a[38:39], v9 offset:12800\n"
"v_min_u32 v22, 0, v0\n"
"v_mfma_f32_16x16x32_bf16 v[204:207], a[84:87], a[108:111], v[204:207]\n"
"ds_read_b128 a[108:111], v13 offset:18432\n"
"v_lshl_add_u32 v22, v22, 3, s66\n"
"v_mfma_f32_16x16x32_bf16 v[204:207], a[80:83], a[104:107], v[204:207]\n"
"ds_read_b128 a[104:107], v12 offset:18432\n"
"global_load_dword v23, v22, s[96:97]\n"
"ds_read_b64_tr_b16 a[40:41], v9 offset:8200\n"
"v_mfma_f32_16x16x32_bf16 v[208:211], a[80:83], a[112:115], v[208:211]\n"
"s_add_u32 m0, s64, 8192\n"
"ds_read_b64_tr_b16 a[42:43], v9 offset:8712\n"
"v_mfma_f32_16x16x32_bf16 v[208:211], a[84:87], a[116:119], v[208:211]\n"
"ds_read_b64_tr_b16 a[44:45], v9 offset:12296\n"
"v_mfma_f32_16x16x32_bf16 v[236:239], a[92:95], a[116:119], v[236:239]\n"
"ds_read_b128 a[116:119], v13 offset:20480\n"
"v_mfma_f32_16x16x32_bf16 v[236:239], a[88:91], a[112:115], v[236:239]\n"
"ds_read_b128 a[112:115], v12 offset:20480\n"
"v_mfma_f32_16x16x32_bf16 v[240:243], a[88:91], a[120:123], v[240:243]\n"
"ds_read_b64_tr_b16 a[46:47], v9 offset:12808\n"
"v_mfma_f32_16x16x32_bf16 v[240:243], a[92:95], a[124:127], v[240:243]\n"
"ds_read_b64_tr_b16 a[48:49], v15 offset:8192\n"
"v_mfma_f32_16x16x32_bf16 v[212:215], a[84:87], a[124:127], v[212:215]\n"
"ds_read_b128 a[124:127], v13 offset:22528\n"
"v_mfma_f32_16x16x32_bf16 v[212:215], a[80:83], a[120:123], v[212:215]\n"
"ds_read_b128 a[120:123], v12 offset:22528\n"
"v_mfma_f32_16x16x32_bf16 v[216:219], a[80:83], a[128:131], v[216:219]\n"
"ds_read_b64_tr_b16 a[50:51], v15 offset:8704\n"
"v_mfma_f32_16x16x32_bf16 v[216:219], a[84:87], a[132:135], v[216:219]\n"
"ds_read_b64_tr_b16 a[52:53], v15 offset:12288\n"
"v_mfma_f32_16x16x32_bf16 v[244:247], a[92:95], a[132:135], v[244:247]\n"
"ds_read_b128 a[132:135], v13 offset:24576\n"
"v_mfma_f32_16x16x32_bf16 v[244:247], a[88:91], a[128:131], v[244:247]\n"
"s_add_u32 s67, s75, 655360\n"
"ds_read_b128 a[128:131], v12 offset:24576\n"
"v_mfma_f32_16x16x32_bf16 v[248:251], a[88:91], a[136:139], v[248:251]\n"
"ds_read_b64_tr_b16 a[54:55], v15 offset:12800\n"
"v_mfma_f32_16x16x32_bf16 v[248:251], a[92:95], a[140:143], v[248:251]\n"
"ds_read_b64_tr_b16 a[56:57], v15 offset:8200\n"
"v_mfma_f32_16x16x32_bf16 v[220:223], a[84:87], a[140:143], v[220:223]\n"
"ds_read_b128 a[140:143], v13 offset:26624\n"
"v_mfma_f32_16x16x32_bf16 v[220:223], a[80:83], a[136:139], v[220:223]\n"
"ds_read_b128 a[136:139], v12 offset:26624\n"
"v_mfma_f32_16x16x32_bf16 v[224:227], a[80:83], a[144:147], v[224:227]\n"
"ds_read_b64_tr_b16 a[58:59], v15 offset:8712\n"
"v_mfma_f32_16x16x32_bf16 v[224:227], a[84:87], a[148:151], v[224:227]\n"
"ds_read_b64_tr_b16 a[60:61], v15 offset:12296\n"
"v_mfma_f32_16x16x32_bf16 v[252:255], a[92:95], a[148:151], v[252:255]\n"
"ds_read_b128 a[148:151], v13 offset:28672\n"
"v_mfma_f32_16x16x32_bf16 v[252:255], a[88:91], a[144:147], v[252:255]\n"
"ds_read_b128 a[144:147], v12 offset:28672\n"
"ds_read_b64_tr_b16 a[62:63], v15 offset:12808\n"
"v_mfma_f32_16x16x32_bf16 a[28:31], a[88:91], a[152:155], a[28:31]\n"
"v_mfma_f32_16x16x32_bf16 a[28:31], a[92:95], a[156:159], a[28:31]\n"
"v_mfma_f32_16x16x32_bf16 a[24:27], a[84:87], a[156:159], a[24:27]\n"
"ds_read_b128 a[156:159], v13 offset:30720\n"
"v_mfma_f32_16x16x32_bf16 a[24:27], a[80:83], a[152:155], a[24:27]\n"
"s_waitcnt lgkmcnt(15)\n"
"ds_read_b128 a[152:155], v12 offset:30720\n"
"v_mfma_f32_16x16x32_bf16 v[32:35], a[32:35], a[96:99], v[32:35]\n"
"v_mfma_f32_16x16x32_bf16 v[32:35], a[36:39], a[100:103], v[32:35]\n"
"s_waitcnt lgkmcnt(2)\n"
"v_mfma_f32_16x16x32_bf16 v[60:63], a[44:47], a[100:103], v[60:63]\n"
"buffer_load_dwordx4 v11, s[52:55], s67 offen offset:0  lds\n"
"v_mfma_f32_16x16x32_bf16 v[60:63], a[40:43], a[96:99], v[60:63]\n"
"v_mfma_f32_16x16x32_bf16 v[64:67], a[40:43], a[104:107], v[64:67]\n"
"s_add_u32 s67, s75, 670720\n"
"v_mfma_f32_16x16x32_bf16 v[64:67], a[44:47], a[108:111], v[64:67]\n"
"v_mfma_f32_16x16x32_bf16 v[36:39], a[36:39], a[108:111], v[36:39]\n"
"buffer_load_dwordx4 v11, s[52:55], s67 offen offset:1024  lds\n"
"v_mfma_f32_16x16x32_bf16 v[36:39], a[32:35], a[104:107], v[36:39]\n"
"v_mfma_f32_16x16x32_bf16 v[40:43], a[32:35], a[112:115], v[40:43]\n"
"s_add_u32 s67, s75, 686080\n"
"v_mfma_f32_16x16x32_bf16 v[40:43], a[36:39], a[116:119], v[40:43]\n"
"v_mfma_f32_16x16x32_bf16 v[68:71], a[44:47], a[116:119], v[68:71]\n"
"v_mfma_f32_16x16x32_bf16 v[68:71], a[40:43], a[112:115], v[68:71]\n"
"buffer_load_dwordx4 v11, s[52:55], s67 offen offset:2048  lds\n"
"v_mfma_f32_16x16x32_bf16 v[72:75], a[40:43], a[120:123], v[72:75]\n"
"v_mfma_f32_16x16x32_bf16 v[72:75], a[44:47], a[124:127], v[72:75]\n"
"s_add_u32 s67, s75, 701440\n"
"v_mfma_f32_16x16x32_bf16 v[44:47], a[36:39], a[124:127], v[44:47]\n"
"v_mfma_f32_16x16x32_bf16 v[44:47], a[32:35], a[120:123], v[44:47]\n"
"buffer_load_dwordx4 v11, s[52:55], s67 offen offset:3072  lds\n"
"v_mfma_f32_16x16x32_bf16 v[48:51], a[32:35], a[128:131], v[48:51]\n"
"v_mfma_f32_16x16x32_bf16 v[48:51], a[36:39], a[132:135], v[48:51]\n"
"s_add_u32 m0, s64, 12288\n"
"v_mfma_f32_16x16x32_bf16 v[76:79], a[44:47], a[132:135], v[76:79]\n"
"s_waitcnt vmcnt(10)\n"
"ds_read_b64_tr_b16 a[64:65], v9 offset:0\n"
"v_mfma_f32_16x16x32_bf16 v[76:79], a[40:43], a[128:131], v[76:79]\n"
"s_add_u32 s67, s75, 720896\n"
"ds_read_b64_tr_b16 a[66:67], v9 offset:512\n"
"v_mfma_f32_16x16x32_bf16 v[80:83], a[40:43], a[136:139], v[80:83]\n"
"ds_read_b64_tr_b16 a[68:69], v9 offset:4096\n"
"v_mfma_f32_16x16x32_bf16 v[80:83], a[44:47], a[140:143], v[80:83]\n"
"ds_read_b64_tr_b16 a[70:71], v9 offset:4608\n"
"buffer_load_dwordx4 v11, s[52:55], s67 offen offset:0  lds\n"
"v_mfma_f32_16x16x32_bf16 v[52:55], a[36:39], a[140:143], v[52:55]\n"
"s_add_u32 s67, s75, 736256\n"
"ds_read_b64_tr_b16 a[72:73], v9 offset:8\n"
"v_mfma_f32_16x16x32_bf16 v[52:55], a[32:35], a[136:139], v[52:55]\n"
"s_waitcnt lgkmcnt(5)\n"
"ds_read_b64_tr_b16 a[74:75], v9 offset:520\n"
"v_mfma_f32_16x16x32_bf16 v[56:59], a[32:35], a[144:147], v[56:59]\n"
"ds_read_b64_tr_b16 a[76:77], v9 offset:4104\n"
"v_mfma_f32_16x16x32_bf16 v[56:59], a[36:39], a[148:151], v[56:59]\n"
"ds_read_b64_tr_b16 a[78:79], v9 offset:4616\n"
"v_mfma_f32_16x16x32_bf16 v[84:87], a[44:47], a[148:151], v[84:87]\n"
"buffer_load_dwordx4 v11, s[52:55], s67 offen offset:1024  lds\n"
"v_mfma_f32_16x16x32_bf16 v[84:87], a[40:43], a[144:147], v[84:87]\n"
"ds_read_b64_tr_b16 a[80:81], v15 offset:0\n"
"v_mfma_f32_16x16x32_bf16 a[4:7], a[40:43], a[152:155], a[4:7]\n"
"s_add_u32 s67, s75, 751616\n"
"ds_read_b64_tr_b16 a[82:83], v15 offset:512\n"
"v_mfma_f32_16x16x32_bf16 a[4:7], a[44:47], a[156:159], a[4:7]\n"
"ds_read_b64_tr_b16 a[84:85], v15 offset:4096\n"
"v_mfma_f32_16x16x32_bf16 a[0:3], a[36:39], a[156:159], a[0:3]\n"
"ds_read_b64_tr_b16 a[86:87], v15 offset:4608\n"
"buffer_load_dwordx4 v11, s[52:55], s67 offen offset:2048  lds\n"
"v_mfma_f32_16x16x32_bf16 a[0:3], a[32:35], a[152:155], a[0:3]\n"
"ds_read_b64_tr_b16 a[88:89], v15 offset:8\n"
"v_mfma_f32_16x16x32_bf16 v[88:91], a[48:51], a[96:99], v[88:91]\n"
"s_add_u32 s67, s75, 766976\n"
"ds_read_b64_tr_b16 a[90:91], v15 offset:520\n"
"v_mfma_f32_16x16x32_bf16 v[88:91], a[52:55], a[100:103], v[88:91]\n"
"ds_read_b64_tr_b16 a[92:93], v15 offset:4104\n"
"v_mfma_f32_16x16x32_bf16 v[116:119], a[60:63], a[100:103], v[116:119]\n"
"ds_read_b64_tr_b16 a[94:95], v15 offset:4616\n"
"v_mfma_f32_16x16x32_bf16 v[116:119], a[56:59], a[96:99], v[116:119]\n"
"buffer_load_dwordx4 v11, s[52:55], s67 offen offset:3072  lds\n"
"v_mfma_f32_16x16x32_bf16 v[120:123], a[56:59], a[104:107], v[120:123]\n"
"v_mfma_f32_16x16x32_bf16 v[120:123], a[60:63], a[108:111], v[120:123]\n"
"s_add_u32 m0, s64, 0\n"
"v_mfma_f32_16x16x32_bf16 v[92:95], a[52:55], a[108:111], v[92:95]\n"
"v_mfma_f32_16x16x32_bf16 v[92:95], a[48:51], a[104:107], v[92:95]\n"
"s_add_u32 s67, s75, 655488\n"
"v_mfma_f32_16x16x32_bf16 v[96:99], a[48:51], a[112:115], v[96:99]\n"
"s_waitcnt lgkmcnt(0)\n"
"v_mfma_f32_16x16x32_bf16 v[96:99], a[52:55], a[116:119], v[96:99]\n"
"buffer_load_dwordx4 v11, s[52:55], s67 offen offset:0  lds\n"
"v_mfma_f32_16x16x32_bf16 v[124:127], a[60:63], a[116:119], v[124:127]\n"
"v_mfma_f32_16x16x32_bf16 v[124:127], a[56:59], a[112:115], v[124:127]\n"
"s_add_u32 s67, s75, 670848\n"
"v_mfma_f32_16x16x32_bf16 v[128:131], a[56:59], a[120:123], v[128:131]\n"
"v_mfma_f32_16x16x32_bf16 v[128:131], a[60:63], a[124:127], v[128:131]\n"
"v_mfma_f32_16x16x32_bf16 v[100:103], a[52:55], a[124:127], v[100:103]\n"
"buffer_load_dwordx4 v11, s[52:55], s67 offen offset:1024  lds\n"
"v_mfma_f32_16x16x32_bf16 v[100:103], a[48:51], a[120:123], v[100:103]\n"
"v_mfma_f32_16x16x32_bf16 v[104:107], a[48:51], a[128:131], v[104:107]\n"
"s_add_u32 s67, s75, 686208\n"
"v_mfma_f32_16x16x32_bf16 v[104:107], a[52:55], a[132:135], v[104:107]\n"
"v_mfma_f32_16x16x32_bf16 v[132:135], a[60:63], a[132:135], v[132:135]\n"
"v_mfma_f32_16x16x32_bf16 v[132:135], a[56:59], a[128:131], v[132:135]\n"
"buffer_load_dwordx4 v11, s[52:55], s67 offen offset:2048  lds\n"
"v_mfma_f32_16x16x32_bf16 v[136:139], a[56:59], a[136:139], v[136:139]\n"
"v_mfma_f32_16x16x32_bf16 v[136:139], a[60:63], a[140:143], v[136:139]\n"
"s_add_u32 s67, s75, 701568\n"
"v_mfma_f32_16x16x32_bf16 v[108:111], a[52:55], a[140:143], v[108:111]\n"
"v_mfma_f32_16x16x32_bf16 v[108:111], a[48:51], a[136:139], v[108:111]\n"
"buffer_load_dwordx4 v11, s[52:55], s67 offen offset:3072  lds\n"
"v_mfma_f32_16x16x32_bf16 v[112:115], a[48:51], a[144:147], v[112:115]\n"
"v_mfma_f32_16x16x32_bf16 v[112:115], a[52:55], a[148:151], v[112:115]\n"
"s_add_u32 m0, s64, 4096\n"
"v_mfma_f32_16x16x32_bf16 v[140:143], a[60:63], a[148:151], v[140:143]\n"
"v_mfma_f32_16x16x32_bf16 v[140:143], a[56:59], a[144:147], v[140:143]\n"
"s_add_u32 s67, s75, 721024\n"
"v_mfma_f32_16x16x32_bf16 a[12:15], a[56:59], a[152:155], a[12:15]\n"
"v_mfma_f32_16x16x32_bf16 a[12:15], a[60:63], a[156:159], a[12:15]\n"
"buffer_load_dwordx4 v11, s[52:55], s67 offen offset:0  lds\n"
"v_mfma_f32_16x16x32_bf16 a[8:11], a[52:55], a[156:159], a[8:11]\n"
"v_mfma_f32_16x16x32_bf16 a[8:11], a[48:51], a[152:155], a[8:11]\n"
"s_add_u32 s67, s75, 736384\n"
"v_mfma_f32_16x16x32_bf16 v[144:147], a[64:67], a[96:99], v[144:147]\n"
"v_mfma_f32_16x16x32_bf16 v[144:147], a[68:71], a[100:103], v[144:147]\n"
"buffer_load_dwordx4 v11, s[52:55], s67 offen offset:1024  lds\n"
"v_mfma_f32_16x16x32_bf16 v[172:175], a[76:79], a[100:103], v[172:175]\n"
"v_mfma_f32_16x16x32_bf16 v[172:175], a[72:75], a[96:99], v[172:175]\n"
"s_add_u32 s67, s75, 751744\n"
"v_mfma_f32_16x16x32_bf16 v[176:179], a[72:75], a[104:107], v[176:179]\n"
"v_mfma_f32_16x16x32_bf16 v[176:179], a[76:79], a[108:111], v[176:179]\n"
"buffer_load_dwordx4 v11, s[52:55], s67 offen offset:2048  lds\n"
"v_mfma_f32_16x16x32_bf16 v[148:151], a[68:71], a[108:111], v[148:151]\n"
"v_mfma_f32_16x16x32_bf16 v[148:151], a[64:67], a[104:107], v[148:151]\n"
"s_add_u32 s67, s75, 767104\n"
"v_mfma_f32_16x16x32_bf16 v[152:155], a[64:67], a[112:115], v[152:155]\n"
"s_sub_u32 s66, 40960, s73\n"
"v_mfma_f32_16x16x32_bf16 v[152:155], a[68:71], a[116:119], v[152:155]\n"
"v_mfma_f32_16x16x32_bf16 v[180:183], a[76:79], a[116:119], v[180:183]\n"
"v_add_u32 v12, s66, v7\n"
"v_mfma_f32_16x16x32_bf16 v[180:183], a[72:75], a[112:115], v[180:183]\n"
"buffer_load_dwordx4 v11, s[52:55], s67 offen offset:3072  lds\n"
"v_mfma_f32_16x16x32_bf16 v[184:187], a[72:75], a[120:123], v[184:187]\n"
"s_add_u32 m0, s72, 98304\n"
"v_add_u32 v13, s66, v8\n"
"v_mfma_f32_16x16x32_bf16 v[184:187], a[76:79], a[124:127], v[184:187]\n"
"v_mfma_f32_16x16x32_bf16 v[156:159], a[68:71], a[124:127], v[156:159]\n"
"s_add_u32 s67, s60, 768\n"
"v_mfma_f32_16x16x32_bf16 v[156:159], a[64:67], a[120:123], v[156:159]\n"
"buffer_load_dwordx4 v6, s[48:51], s67 offen offset:0  lds\n"
"v_mfma_f32_16x16x32_bf16 v[160:163], a[64:67], a[128:131], v[160:163]\n"
"v_mfma_f32_16x16x32_bf16 v[160:163], a[68:71], a[132:135], v[160:163]\n"
"s_add_u32 s67, s60, 16128\n"
"v_mfma_f32_16x16x32_bf16 v[188:191], a[76:79], a[132:135], v[188:191]\n"
"v_mfma_f32_16x16x32_bf16 v[188:191], a[72:75], a[128:131], v[188:191]\n"
"buffer_load_dwordx4 v6, s[48:51], s67 offen offset:1024  lds\n"
"v_mfma_f32_16x16x32_bf16 v[192:195], a[72:75], a[136:139], v[192:195]\n"
"v_mfma_f32_16x16x32_bf16 v[192:195], a[76:79], a[140:143], v[192:195]\n"
"s_add_u32 s67, s60, 31488\n"
"v_mfma_f32_16x16x32_bf16 v[164:167], a[68:71], a[140:143], v[164:167]\n"
"v_mfma_f32_16x16x32_bf16 v[164:167], a[64:67], a[136:139], v[164:167]\n"
"buffer_load_dwordx4 v6, s[48:51], s67 offen offset:2048  lds\n"
"v_mfma_f32_16x16x32_bf16 v[168:171], a[64:67], a[144:147], v[168:171]\n"
"v_mfma_f32_16x16x32_bf16 v[168:171], a[68:71], a[148:151], v[168:171]\n"
"s_add_u32 s67, s60, 46848\n"
"v_mfma_f32_16x16x32_bf16 v[196:199], a[76:79], a[148:151], v[196:199]\n"
"v_mfma_f32_16x16x32_bf16 v[196:199], a[72:75], a[144:147], v[196:199]\n"
"v_mfma_f32_16x16x32_bf16 a[20:23], a[72:75], a[152:155], a[20:23]\n"
"buffer_load_dwordx4 v6, s[48:51], s67 offen offset:3072  lds\n"
"v_mfma_f32_16x16x32_bf16 a[20:23], a[76:79], a[156:159], a[20:23]\n"
"v_mfma_f32_16x16x32_bf16 a[16:19], a[68:71], a[156:159], a[16:19]\n"
"s_sub_u32 s70, 1572864, s75\n"
"s_add_u32 s70, s70, s65\n"
"v_mfma_f32_16x16x32_bf16 a[16:19], a[64:67], a[152:155], a[16:19]\n"
"s_waitcnt vmcnt(22)\n"
"s_barrier\n"
"v_mfma_f32_16x16x32_bf16 v[200:203], a[80:83], a[96:99], v[200:203]\n"
"s_waitcnt vmcnt(12)\n"
"ds_read_b64_tr_b16 a[32:33], v9 offset:8192\n"
"v_mfma_f32_16x16x32_bf16 v[200:203], a[84:87], a[100:103], v[200:203]\n"
"ds_read_b64_tr_b16 a[34:35], v9 offset:8704\n"
"v_mfma_f32_16x16x32_bf16 v[228:231], a[92:95], a[100:103], v[228:231]\n"
"ds_read_b128 a[100:103], v13 offset:16384\n"
"buffer_load_dword a252, v14, s[52:55], s70 offen\n"
"v_mfma_f32_16x16x32_bf16 v[228:231], a[88:91], a[96:99], v[228:231]\n"
"ds_read_b128 a[96:99], v12 offset:16384\n"
"v_mfma_f32_16x16x32_bf16 v[232:235], a[88:91], a[104:107], v[232:235]\n"
"buffer_store_dwordx2 v[16:17], v18, s[56:59], %11 offen sc1\n"
"ds_read_b64_tr_b16 a[36:37], v9 offset:12288\n"
"v_mfma_f32_16x16x32_bf16 v[232:235], a[92:95], a[108:111], v[232:235]\n"
"ds_read_b64_tr_b16 a[38:39], v9 offset:12800\n"
"v_mfma_f32_16x16x32_bf16 v[204:207], a[84:87], a[108:111], v[204:207]\n"
"ds_read_b128 a[108:111], v13 offset:18432\n"
"v_mfma_f32_16x16x32_bf16 v[204:207], a[80:83], a[104:107], v[204:207]\n"
"ds_read_b128 a[104:107], v12 offset:18432\n"
"buffer_store_dwordx2 v[16:17], v18, s[56:59], s69 offen sc1\n"
"ds_read_b64_tr_b16 a[40:41], v9 offset:8200\n"
"v_mfma_f32_16x16x32_bf16 v[208:211], a[80:83], a[112:115], v[208:211]\n"
"ds_read_b64_tr_b16 a[42:43], v9 offset:8712\n"
"v_mfma_f32_16x16x32_bf16 v[208:211], a[84:87], a[116:119], v[208:211]\n"
"ds_read_b64_tr_b16 a[44:45], v9 offset:12296\n"
"v_mfma_f32_16x16x32_bf16 v[236:239], a[92:95], a[116:119], v[236:239]\n"
"ds_read_b128 a[116:119], v13 offset:20480\n"
"v_mfma_f32_16x16x32_bf16 v[236:239], a[88:91], a[112:115], v[236:239]\n"
"ds_read_b128 a[112:115], v12 offset:20480\n"
"v_mfma_f32_16x16x32_bf16 v[240:243], a[88:91], a[120:123], v[240:243]\n"
"s_add_u32 m0, s64, 8192\n"
"ds_read_b64_tr_b16 a[46:47], v9 offset:12808\n"
"v_mfma_f32_16x16x32_bf16 v[240:243], a[92:95], a[124:127], v[240:243]\n"
"ds_read_b64_tr_b16 a[48:49], v15 offset:8192\n"
"v_mfma_f32_16x16x32_bf16 v[212:215], a[84:87], a[124:127], v[212:215]\n"
"ds_read_b128 a[124:127], v13 offset:22528\n"
"v_mfma_f32_16x16x32_bf16 v[212:215], a[80:83], a[120:123], v[212:215]\n"
"ds_read_b128 a[120:123], v12 offset:22528\n"
"v_mfma_f32_16x16x32_bf16 v[216:219], a[80:83], a[128:131], v[216:219]\n"
"ds_read_b64_tr_b16 a[50:51], v15 offset:8704\n"
"v_mfma_f32_16x16x32_bf16 v[216:219], a[84:87], a[132:135], v[216:219]\n"
"ds_read_b64_tr_b16 a[52:53], v15 offset:12288\n"
"v_mfma_f32_16x16x32_bf16 v[244:247], a[92:95], a[132:135], v[244:247]\n"
"ds_read_b128 a[132:135], v13 offset:24576\n"
"v_mfma_f32_16x16x32_bf16 v[244:247], a[88:91], a[128:131], v[244:247]\n"
"ds_read_b128 a[128:131], v12 offset:24576\n"
"v_mfma_f32_16x16x32_bf16 v[248:251], a[88:91], a[136:139], v[248:251]\n"
"ds_read_b64_tr_b16 a[54:55], v15 offset:12800\n"
"v_mfma_f32_16x16x32_bf16 v[248:251], a[92:95], a[140:143], v[248:251]\n"
"ds_read_b64_tr_b16 a[56:57], v15 offset:8200\n"
"v_mfma_f32_16x16x32_bf16 v[220:223], a[84:87], a[140:143], v[220:223]\n"
"s_add_u32 s67, s75, 786432\n"
"ds_read_b128 a[140:143], v13 offset:26624\n"
"v_mfma_f32_16x16x32_bf16 v[220:223], a[80:83], a[136:139], v[220:223]\n"
"ds_read_b128 a[136:139], v12 offset:26624\n"
"v_mfma_f32_16x16x32_bf16 v[224:227], a[80:83], a[144:147], v[224:227]\n"
"ds_read_b64_tr_b16 a[58:59], v15 offset:8712\n"
"v_mfma_f32_16x16x32_bf16 v[224:227], a[84:87], a[148:151], v[224:227]\n"
"ds_read_b64_tr_b16 a[60:61], v15 offset:12296\n"
"v_mfma_f32_16x16x32_bf16 v[252:255], a[92:95], a[148:151], v[252:255]\n"
"ds_read_b128 a[148:151], v13 offset:28672\n"
"v_mfma_f32_16x16x32_bf16 v[252:255], a[88:91], a[144:147], v[252:255]\n"
"ds_read_b128 a[144:147], v12 offset:28672\n"
"ds_read_b64_tr_b16 a[62:63], v15 offset:12808\n"
"v_mfma_f32_16x16x32_bf16 a[28:31], a[88:91], a[152:155], a[28:31]\n"
"v_mfma_f32_16x16x32_bf16 a[28:31], a[92:95], a[156:159], a[28:31]\n"
"v_mfma_f32_16x16x32_bf16 a[24:27], a[84:87], a[156:159], a[24:27]\n"
"ds_read_b128 a[156:159], v13 offset:30720\n"
"v_mfma_f32_16x16x32_bf16 a[24:27], a[80:83], a[152:155], a[24:27]\n"
"s_waitcnt lgkmcnt(15)\n"
"ds_read_b128 a[152:155], v12 offset:30720\n"
"v_mfma_f32_16x16x32_bf16 v[32:35], a[32:35], a[96:99], v[32:35]\n"
"v_mfma_f32_16x16x32_bf16 v[32:35], a[36:39], a[100:103], v[32:35]\n"
"s_waitcnt lgkmcnt(2)\n"
"v_mfma_f32_16x16x32_bf16 v[60:63], a[44:47], a[100:103], v[60:63]\n"
"buffer_load_dwordx4 v11, s[52:55], s67 offen offset:0  lds\n"
"v_mfma_f32_16x16x32_bf16 v[60:63], a[40:43], a[96:99], v[60:63]\n"
"v_mfma_f32_16x16x32_bf16 v[64:67], a[40:43], a[104:107], v[64:67]\n"
"s_add_u32 s67, s75, 801792\n"
"v_mfma_f32_16x16x32_bf16 v[64:67], a[44:47], a[108:111], v[64:67]\n"
"v_mfma_f32_16x16x32_bf16 v[36:39], a[36:39], a[108:111], v[36:39]\n"
"buffer_load_dwordx4 v11, s[52:55], s67 offen offset:1024  lds\n"
"v_mfma_f32_16x16x32_bf16 v[36:39], a[32:35], a[104:107], v[36:39]\n"
"v_mfma_f32_16x16x32_bf16 v[40:43], a[32:35], a[112:115], v[40:43]\n"
"s_add_u32 s67, s75, 817152\n"
"v_mfma_f32_16x16x32_bf16 v[40:43], a[36:39], a[116:119], v[40:43]\n"
"v_mfma_f32_16x16x32_bf16 v[68:71], a[44:47], a[116:119], v[68:71]\n"
"s_mov_b32 s30, 0\n"
"v_mfma_f32_16x16x32_bf16 v[68:71], a[40:43], a[112:115], v[68:71]\n"
"buffer_load_dwordx4 v11, s[52:55], s67 offen offset:2048  lds\n"
"v_mfma_f32_16x16x32_bf16 v[72:75], a[40:43], a[120:123], v[72:75]\n"
"s_mov_b32 s31, 0\n"
"v_mfma_f32_16x16x32_bf16 v[72:75], a[44:47], a[124:127], v[72:75]\n"
"s_add_u32 s67, s75, 832512\n"
"v_mfma_f32_16x16x32_bf16 v[44:47], a[36:39], a[124:127], v[44:47]\n"
"v_mfma_f32_16x16x32_bf16 v[44:47], a[32:35], a[120:123], v[44:47]\n"
"v_mfma_f32_16x16x32_bf16 v[48:51], a[32:35], a[128:131], v[48:51]\n"
"buffer_load_dwordx4 v11, s[52:55], s67 offen offset:3072  lds\n"
"v_mfma_f32_16x16x32_bf16 v[48:51], a[36:39], a[132:135], v[48:51]\n"
"s_add_u32 m0, s64, 12288\n"
"v_mfma_f32_16x16x32_bf16 v[76:79], a[44:47], a[132:135], v[76:79]\n"
"s_waitcnt vmcnt(11)\n"
"ds_read_b64_tr_b16 a[64:65], v9 offset:0\n"
"v_mfma_f32_16x16x32_bf16 v[76:79], a[40:43], a[128:131], v[76:79]\n"
"s_add_u32 s67, s75, 851968\n"
"ds_read_b64_tr_b16 a[66:67], v9 offset:512\n"
"v_mfma_f32_16x16x32_bf16 v[80:83], a[40:43], a[136:139], v[80:83]\n"
"ds_read_b64_tr_b16 a[68:69], v9 offset:4096\n"
"v_mfma_f32_16x16x32_bf16 v[80:83], a[44:47], a[140:143], v[80:83]\n"
"ds_read_b64_tr_b16 a[70:71], v9 offset:4608\n"
"buffer_load_dwordx4 v11, s[52:55], s67 offen offset:0  lds\n"
"v_mfma_f32_16x16x32_bf16 v[52:55], a[36:39], a[140:143], v[52:55]\n"
"s_add_u32 s67, s75, 867328\n"
"ds_read_b64_tr_b16 a[72:73], v9 offset:8\n"
"v_mfma_f32_16x16x32_bf16 v[52:55], a[32:35], a[136:139], v[52:55]\n"
"s_waitcnt lgkmcnt(5)\n"
"ds_read_b64_tr_b16 a[74:75], v9 offset:520\n"
"v_mfma_f32_16x16x32_bf16 v[56:59], a[32:35], a[144:147], v[56:59]\n"
"ds_read_b64_tr_b16 a[76:77], v9 offset:4104\n"
"v_mfma_f32_16x16x32_bf16 v[56:59], a[36:39], a[148:151], v[56:59]\n"
"ds_read_b64_tr_b16 a[78:79], v9 offset:4616\n"
"v_mfma_f32_16x16x32_bf16 v[84:87], a[44:47], a[148:151], v[84:87]\n"
"buffer_load_dwordx4 v11, s[52:55], s67 offen offset:1024  lds\n"
"v_mfma_f32_16x16x32_bf16 v[84:87], a[40:43], a[144:147], v[84:87]\n"
"ds_read_b64_tr_b16 a[80:81], v15 offset:0\n"
"v_mfma_f32_16x16x32_bf16 a[4:7], a[40:43], a[152:155], a[4:7]\n"
"s_add_u32 s67, s75, 882688\n"
"ds_read_b64_tr_b16 a[82:83], v15 offset:512\n"
"v_mfma_f32_16x16x32_bf16 a[4:7], a[44:47], a[156:159], a[4:7]\n"
"ds_read_b64_tr_b16 a[84:85], v15 offset:4096\n"
"buffer_load_dwordx4 v11, s[52:55], s67 offen offset:2048  lds\n"
"v_mfma_f32_16x16x32_bf16 a[0:3], a[36:39], a[156:159], a[0:3]\n"
"s_add_u32 s96, s56, %11\n"
"ds_read_b64_tr_b16 a[86:87], v15 offset:4608\n"
"s_addc_u32 s97, s57, 0\n"
"v_mfma_f32_16x16x32_bf16 a[0:3], a[32:35], a[152:155], a[0:3]\n"
"ds_read_b64_tr_b16 a[88:89], v15 offset:8\n"
"v_mfma_f32_16x16x32_bf16 v[88:91], a[48:51], a[96:99], v[88:91]\n"
"s_add_u32 s67, s75, 898048\n"
"ds_read_b64_tr_b16 a[90:91], v15 offset:520\n"
"v_mfma_f32_16x16x32_bf16 v[88:91], a[52:55], a[100:103], v[88:91]\n"
"ds_read_b64_tr_b16 a[92:93], v15 offset:4104\n"
"v_mfma_f32_16x16x32_bf16 v[116:119], a[60:63], a[100:103], v[116:119]\n"
"ds_read_b64_tr_b16 a[94:95], v15 offset:4616\n"
"v_mfma_f32_16x16x32_bf16 v[116:119], a[56:59], a[96:99], v[116:119]\n"
"buffer_load_dwordx4 v11, s[52:55], s67 offen offset:3072  lds\n"
"v_mfma_f32_16x16x32_bf16 v[120:123], a[56:59], a[104:107], v[120:123]\n"
"v_mfma_f32_16x16x32_bf16 v[120:123], a[60:63], a[108:111], v[120:123]\n"
"s_add_u32 m0, s64, 0\n"
"v_mfma_f32_16x16x32_bf16 v[92:95], a[52:55], a[108:111], v[92:95]\n"
"v_mfma_f32_16x16x32_bf16 v[92:95], a[48:51], a[104:107], v[92:95]\n"
"s_add_u32 s67, s75, 786560\n"
"v_mfma_f32_16x16x32_bf16 v[96:99], a[48:51], a[112:115], v[96:99]\n"
"s_waitcnt lgkmcnt(0)\n"
"v_mfma_f32_16x16x32_bf16 v[96:99], a[52:55], a[116:119], v[96:99]\n"
"buffer_load_dwordx4 v11, s[52:55], s67 offen offset:0  lds\n"
"v_mfma_f32_16x16x32_bf16 v[124:127], a[60:63], a[116:119], v[124:127]\n"
"s_load_dword s98, s[96:97], 512 glc\n"
"v_mfma_f32_16x16x32_bf16 v[124:127], a[56:59], a[112:115], v[124:127]\n"
"s_load_dword s99, s[96:97], 8704 glc\n"
"s_add_u32 s67, s75, 801920\n"
"v_mfma_f32_16x16x32_bf16 v[128:131], a[56:59], a[120:123], v[128:131]\n"
"v_mfma_f32_16x16x32_bf16 v[128:131], a[60:63], a[124:127], v[128:131]\n"
"buffer_load_dwordx4 v11, s[52:55], s67 offen offset:1024  lds\n"
"v_mfma_f32_16x16x32_bf16 v[100:103], a[52:55], a[124:127], v[100:103]\n"
"v_mfma_f32_16x16x32_bf16 v[100:103], a[48:51], a[120:123], v[100:103]\n"
"s_add_u32 s67, s75, 817280\n"
"v_mfma_f32_16x16x32_bf16 v[104:107], a[48:51], a[128:131], v[104:107]\n"
"v_mfma_f32_16x16x32_bf16 v[104:107], a[52:55], a[132:135], v[104:107]\n"
"v_mfma_f32_16x16x32_bf16 v[132:135], a[60:63], a[132:135], v[132:135]\n"
"buffer_load_dwordx4 v11, s[52:55], s67 offen offset:2048  lds\n"
"v_mfma_f32_16x16x32_bf16 v[132:135], a[56:59], a[128:131], v[132:135]\n"
"v_mfma_f32_16x16x32_bf16 v[136:139], a[56:59], a[136:139], v[136:139]\n"
"s_add_u32 s67, s75, 832640\n"
"v_mfma_f32_16x16x32_bf16 v[136:139], a[60:63], a[140:143], v[136:139]\n"
"v_mfma_f32_16x16x32_bf16 v[108:111], a[52:55], a[140:143], v[108:111]\n"
"buffer_load_dwordx4 v11, s[52:55], s67 offen offset:3072  lds\n"
"v_mfma_f32_16x16x32_bf16 v[108:111], a[48:51], a[136:139], v[108:111]\n"
"v_mfma_f32_16x16x32_bf16 v[112:115], a[48:51], a[144:147], v[112:115]\n"
"s_add_u32 m0, s64, 4096\n"
"v_mfma_f32_16x16x32_bf16 v[112:115], a[52:55], a[148:151], v[112:115]\n"
"v_mfma_f32_16x16x32_bf16 v[140:143], a[60:63], a[148:151], v[140:143]\n"
"s_add_u32 s67, s75, 852096\n"
"v_mfma_f32_16x16x32_bf16 v[140:143], a[56:59], a[144:147], v[140:143]\n"
"v_mfma_f32_16x16x32_bf16 a[12:15], a[56:59], a[152:155], a[12:15]\n"
"buffer_load_dwordx4 v11, s[52:55], s67 offen offset:0  lds\n"
"v_mfma_f32_16x16x32_bf16 a[12:15], a[60:63], a[156:159], a[12:15]\n"
"v_mfma_f32_16x16x32_bf16 a[8:11], a[52:55], a[156:159], a[8:11]\n"
"s_add_u32 s67, s75, 867456\n"
"v_mfma_f32_16x16x32_bf16 a[8:11], a[48:51], a[152:155], a[8:11]\n"
"v_mfma_f32_16x16x32_bf16 v[144:147], a[64:67], a[96:99], v[144:147]\n"
"buffer_load_dwordx4 v11, s[52:55], s67 offen offset:1024  lds\n"
"v_mfma_f32_16x16x32_bf16 v[144:147], a[68:71], a[100:103], v[144:147]\n"
"v_mfma_f32_16x16x32_bf16 v[172:175], a[76:79], a[100:103], v[172:175]\n"
"s_add_u32 s67, s75, 882816\n"
"v_mfma_f32_16x16x32_bf16 v[172:175], a[72:75], a[96:99], v[172:175]\n"
"v_mfma_f32_16x16x32_bf16 v[176:179], a[72:75], a[104:107], v[176:179]\n"
"buffer_load_dwordx4 v11, s[52:55], s67 offen offset:2048  lds\n"
"v_mfma_f32_16x16x32_bf16 v[176:179], a[76:79], a[108:111], v[176:179]\n"
"v_mfma_f32_16x16x32_bf16 v[148:151], a[68:71], a[108:111], v[148:151]\n"
"s_add_u32 s67, s75, 898176\n"
"v_mfma_f32_16x16x32_bf16 v[148:151], a[64:67], a[104:107], v[148:151]\n"
"v_mfma_f32_16x16x32_bf16 v[152:155], a[64:67], a[112:115], v[152:155]\n"
"s_sub_u32 s66, 81920, s73\n"
"v_mfma_f32_16x16x32_bf16 v[152:155], a[68:71], a[116:119], v[152:155]\n"
"buffer_load_dwordx4 v11, s[52:55], s67 offen offset:3072  lds\n"
"v_mfma_f32_16x16x32_bf16 v[180:183], a[76:79], a[116:119], v[180:183]\n"
"s_add_u32 m0, s72, 139264\n"
"v_add_u32 v12, s66, v7\n"
"v_mfma_f32_16x16x32_bf16 v[180:183], a[72:75], a[112:115], v[180:183]\n"
"v_mfma_f32_16x16x32_bf16 v[184:187], a[72:75], a[120:123], v[184:187]\n"
"s_add_u32 s67, s60, 896\n"
"v_add_u32 v13, s66, v8\n"
"v_mfma_f32_16x16x32_bf16 v[184:187], a[76:79], a[124:127], v[184:187]\n"
"v_mfma_f32_16x16x32_bf16 v[156:159], a[68:71], a[124:127], v[156:159]\n"
"buffer_load_dwordx4 v6, s[48:51], s67 offen offset:0  lds\n"
"v_mfma_f32_16x16x32_bf16 v[156:159], a[64:67], a[120:123], v[156:159]\n"
"v_mfma_f32_16x16x32_bf16 v[160:163], a[64:67], a[128:131], v[160:163]\n"
"s_add_u32 s67, s60, 16256\n"
"v_mfma_f32_16x16x32_bf16 v[160:163], a[68:71], a[132:135], v[160:163]\n"
"v_mfma_f32_16x16x32_bf16 v[188:191], a[76:79], a[132:135], v[188:191]\n"
"buffer_load_dwordx4 v6, s[48:51], s67 offen offset:1024  lds\n"
"v_mfma_f32_16x16x32_bf16 v[188:191], a[72:75], a[128:131], v[188:191]\n"
"v_mfma_f32_16x16x32_bf16 v[192:195], a[72:75], a[136:139], v[192:195]\n"
"s_add_u32 s67, s60, 31616\n"
"v_mfma_f32_16x16x32_bf16 v[192:195], a[76:79], a[140:143], v[192:195]\n"
"v_mfma_f32_16x16x32_bf16 v[164:167], a[68:71], a[140:143], v[164:167]\n"
"v_mfma_f32_16x16x32_bf16 v[164:167], a[64:67], a[136:139], v[164:167]\n"
"buffer_load_dwordx4 v6, s[48:51], s67 offen offset:2048  lds\n"
"v_mfma_f32_16x16x32_bf16 v[168:171], a[64:67], a[144:147], v[168:171]\n"
"v_mfma_f32_16x16x32_bf16 v[168:171], a[68:71], a[148:151], v[168:171]\n"
"s_add_u32 s67, s60, 46976\n"
"v_mfma_f32_16x16x32_bf16 v[196:199], a[76:79], a[148:151], v[196:199]\n"
"v_mfma_f32_16x16x32_bf16 v[196:199], a[72:75], a[144:147], v[196:199]\n"
"buffer_load_dwordx4 v6, s[48:51], s67 offen offset:3072  lds\n"
"v_mfma_f32_16x16x32_bf16 a[20:23], a[72:75], a[152:155], a[20:23]\n"
"v_mfma_f32_16x16x32_bf16 a[20:23], a[76:79], a[156:159], a[20:23]\n"
"s_waitcnt lgkmcnt(0)\n"
"s_add_u32 m0, s64, 32640\n"
"v_mfma_f32_16x16x32_bf16 a[16:19], a[68:71], a[156:159], a[16:19]\n"
"v_mfma_f32_16x16x32_bf16 a[16:19], a[64:67], a[152:155], a[16:19]\n"
"s_waitcnt vmcnt(23)\n"
"s_barrier\n"
"v_mfma_f32_16x16x32_bf16 v[200:203], a[80:83], a[96:99], v[200:203]\n"
"s_waitcnt vmcnt(12)\n"
"ds_read_b64_tr_b16 a[32:33], v9 offset:8192\n"
"buffer_load_dwordx4 v6, s[40:43], s80 offen offset:128  lds\n"
"v_mfma_f32_16x16x32_bf16 v[200:203], a[84:87], a[100:103], v[200:203]\n"
"ds_read_b64_tr_b16 a[34:35], v9 offset:8704\n"
"v_mfma_f32_16x16x32_bf16 v[228:231], a[92:95], a[100:103], v[228:231]\n"
"ds_read_b128 a[100:103], v13 offset:16384\n"
"v_mfma_f32_16x16x32_bf16 v[228:231], a[88:91], a[96:99], v[228:231]\n"
"ds_read_b128 a[96:99], v12 offset:16384\n"
"buffer_load_dwordx4 v6, s[40:43], s81 offen offset:1152  lds\n"
"v_mfma_f32_16x16x32_bf16 v[232:235], a[88:91], a[104:107], v[232:235]\n"
"ds_read_b64_tr_b16 a[36:37], v9 offset:12288\n"
"v_mfma_f32_16x16x32_bf16 v[232:235], a[92:95], a[108:111], v[232:235]\n"
"ds_read_b64_tr_b16 a[38:39], v9 offset:12800\n"
"v_mfma_f32_16x16x32_bf16 v[204:207], a[84:87], a[108:111], v[204:207]\n"
"ds_read_b128 a[108:111], v13 offset:18432\n"
"v_mfma_f32_16x16x32_bf16 v[204:207], a[80:83], a[104:107], v[204:207]\n"
"ds_read_b128 a[104:107], v12 offset:18432\n"
"ds_read_b64_tr_b16 a[40:41], v9 offset:8200\n"
"v_mfma_f32_16x16x32_bf16 v[208:211], a[80:83], a[112:115], v[208:211]\n"
"ds_read_b64_tr_b16 a[42:43], v9 offset:8712\n"
"buffer_load_dwordx4 v6, s[40:43], s82 offen offset:2176  lds\n"
"v_mfma_f32_16x16x32_bf16 v[208:211], a[84:87], a[116:119], v[208:211]\n"
"ds_read_b64_tr_b16 a[44:45], v9 offset:12296\n"
"v_mfma_f32_16x16x32_bf16 v[236:239], a[92:95], a[116:119], v[236:239]\n"
"ds_read_b128 a[116:119], v13 offset:20480\n"
"v_mfma_f32_16x16x32_bf16 v[236:239], a[88:91], a[112:115], v[236:239]\n"
"ds_read_b128 a[112:115], v12 offset:20480\n"
"v_mfma_f32_16x16x32_bf16 v[240:243], a[88:91], a[120:123], v[240:243]\n"
"ds_read_b64_tr_b16 a[46:47], v9 offset:12808\n"
"v_mfma_f32_16x16x32_bf16 v[240:243], a[92:95], a[124:127], v[240:243]\n"
"ds_read_b64_tr_b16 a[48:49], v15 offset:8192\n"
"buffer_load_dwordx4 v6, s[40:43], s83 offen offset:3200  lds\n"
"v_mfma_f32_16x16x32_bf16 v[212:215], a[84:87], a[124:127], v[212:215]\n"
"ds_read_b128 a[124:127], v13 offset:22528\n"
"v_mfma_f32_16x16x32_bf16 v[212:215], a[80:83], a[120:123], v[212:215]\n"
"ds_read_b128 a[120:123], v12 offset:22528\n"
"v_mfma_f32_16x16x32_bf16 v[216:219], a[80:83], a[128:131], v[216:219]\n"
"ds_read_b64_tr_b16 a[50:51], v15 offset:8704\n"
"v_mfma_f32_16x16x32_bf16 v[216:219], a[84:87], a[132:135], v[216:219]\n"
"s_add_u32 m0, s64, 8192\n"
"ds_read_b64_tr_b16 a[52:53], v15 offset:12288\n"
"v_mfma_f32_16x16x32_bf16 v[244:247], a[92:95], a[132:135], v[244:247]\n"
"ds_read_b128 a[132:135], v13 offset:24576\n"
"v_mfma_f32_16x16x32_bf16 v[244:247], a[88:91], a[128:131], v[244:247]\n"
"ds_read_b128 a[128:131], v12 offset:24576\n"
"v_mfma_f32_16x16x32_bf16 v[248:251], a[88:91], a[136:139], v[248:251]\n"
"ds_read_b64_tr_b16 a[54:55], v15 offset:12800\n"
"v_mfma_f32_16x16x32_bf16 v[248:251], a[92:95], a[140:143], v[248:251]\n"
"ds_read_b64_tr_b16 a[56:57], v15 offset:8200\n"
"v_mfma_f32_16x16x32_bf16 v[220:223], a[84:87], a[140:143], v[220:223]\n"
"ds_read_b128 a[140:143], v13 offset:26624\n"
"v_mfma_f32_16x16x32_bf16 v[220:223], a[80:83], a[136:139], v[220:223]\n"
"ds_read_b128 a[136:139], v12 offset:26624\n"
"v_mfma_f32_16x16x32_bf16 v[224:227], a[80:83], a[144:147], v[224:227]\n"
"ds_read_b64_tr_b16 a[58:59], v15 offset:8712\n"
"v_mfma_f32_16x16x32_bf16 v[224:227], a[84:87], a[148:151], v[224:227]\n"
"s_add_u32 s67, s75, 917504\n"
"ds_read_b64_tr_b16 a[60:61], v15 offset:12296\n"
"v_mfma_f32_16x16x32_bf16 v[252:255], a[92:95], a[148:151], v[252:255]\n"
"ds_read_b128 a[148:151], v13 offset:28672\n"
"v_mfma_f32_16x16x32_bf16 v[252:255], a[88:91], a[144:147], v[252:255]\n"
"ds_read_b128 a[144:147], v12 offset:28672\n"
"ds_read_b64_tr_b16 a[62:63], v15 offset:12808\n"
"v_mfma_f32_16x16x32_bf16 a[28:31], a[88:91], a[152:155], a[28:31]\n"
"v_mfma_f32_16x16x32_bf16 a[28:31], a[92:95], a[156:159], a[28:31]\n"
"v_mfma_f32_16x16x32_bf16 a[24:27], a[84:87], a[156:159], a[24:27]\n"
"ds_read_b128 a[156:159], v13 offset:30720\n"
"v_mfma_f32_16x16x32_bf16 a[24:27], a[80:83], a[152:155], a[24:27]\n"
"s_waitcnt lgkmcnt(15)\n"
"ds_read_b128 a[152:155], v12 offset:30720\n"
"v_mfma_f32_16x16x32_bf16 v[32:35], a[32:35], a[96:99], v[32:35]\n"
"v_mfma_f32_16x16x32_bf16 v[32:35], a[36:39], a[100:103], v[32:35]\n"
"s_waitcnt lgkmcnt(2)\n"
"L_H6S:\n"
"v_mfma_f32_16x16x32_bf16 v[60:63], a[44:47], a[100:103], v[60:63]\n"
"buffer_load_dwordx4 v11, s[52:55], s67 offen offset:0  lds\n"
"v_mfma_f32_16x16x32_bf16 v[60:63], a[40:43], a[96:99], v[60:63]\n"
"v_mfma_f32_16x16x32_bf16 v[64:67], a[40:43], a[104:107], v[64:67]\n"
"s_add_u32 s67, s75, 932864\n"
"v_mfma_f32_16x16x32_bf16 v[64:67], a[44:47], a[108:111], v[64:67]\n"
"v_mfma_f32_16x16x32_bf16 v[36:39], a[36:39], a[108:111], v[36:39]\n"
"v_mfma_f32_16x16x32_bf16 v[36:39], a[32:35], a[104:107], v[36:39]\n"
"buffer_load_dwordx4 v11, s[52:55], s67 offen offset:1024  lds\n"
"v_mfma_f32_16x16x32_bf16 v[40:43], a[32:35], a[112:115], v[40:43]\n"
"v_mfma_f32_16x16x32_bf16 v[40:43], a[36:39], a[116:119], v[40:43]\n"
"s_add_u32 s67, s75, 948224\n"
"v_mfma_f32_16x16x32_bf16 v[68:71], a[44:47], a[116:119], v[68:71]\n"
"v_mfma_f32_16x16x32_bf16 v[68:71], a[40:43], a[112:115], v[68:71]\n"
"buffer_load_dwordx4 v11, s[52:55], s67 offen offset:2048  lds\n"
"v_mfma_f32_16x16x32_bf16 v[72:75], a[40:43], a[120:123], v[72:75]\n"
"v_mfma_f32_16x16x32_bf16 v[72:75], a[44:47], a[124:127], v[72:75]\n"
"s_add_u32 s67, s75, 963584\n"
"v_mfma_f32_16x16x32_bf16 v[44:47], a[36:39], a[124:127], v[44:47]\n"
"v_mfma_f32_16x16x32_bf16 v[44:47], a[32:35], a[120:123], v[44:47]\n"
"v_mfma_f32_16x16x32_bf16 v[48:51], a[32:35], a[128:131], v[48:51]\n"
"buffer_load_dwordx4 v11, s[52:55], s67 offen offset:3072  lds\n"
"v_mfma_f32_16x16x32_bf16 v[48:51], a[36:39], a[132:135], v[48:51]\n"
"s_add_u32 m0, s64, 12288\n"
"v_mfma_f32_16x16x32_bf16 v[76:79], a[44:47], a[132:135], v[76:79]\n"
"s_waitcnt vmcnt(12)\n"
"ds_read_b64_tr_b16 a[64:65], v9 offset:0\n"
"v_mfma_f32_16x16x32_bf16 v[76:79], a[40:43], a[128:131], v[76:79]\n"
"ds_read_b64_tr_b16 a[66:67], v9 offset:512\n"
"v_mfma_f32_16x16x32_bf16 v[80:83], a[40:43], a[136:139], v[80:83]\n"
"s_add_u32 s67, s75, 983040\n"
"ds_read_b64_tr_b16 a[68:69], v9 offset:4096\n"
"v_mfma_f32_16x16x32_bf16 v[80:83], a[44:47], a[140:143], v[80:83]\n"
"ds_read_b64_tr_b16 a[70:71], v9 offset:4608\n"
"v_mfma_f32_16x16x32_bf16 v[52:55], a[36:39], a[140:143], v[52:55]\n"
"ds_read_b64_tr_b16 a[72:73], v9 offset:8\n"
"buffer_load_dwordx4 v11, s[52:55], s67 offen offset:0  lds\n"
"v_mfma_f32_16x16x32_bf16 v[52:55], a[32:35], a[136:139], v[52:55]\n"
"s_waitcnt lgkmcnt(5)\n"
"ds_read_b64_tr_b16 a[74:75], v9 offset:520\n"
"v_mfma_f32_16x16x32_bf16 v[56:59], a[32:35], a[144:147], v[56:59]\n"
"s_add_u32 s67, s75, 998400\n"
"ds_read_b64_tr_b16 a[76:77], v9 offset:4104\n"
"v_mfma_f32_16x16x32_bf16 v[56:59], a[36:39], a[148:151], v[56:59]\n"
"ds_read_b64_tr_b16 a[78:79], v9 offset:4616\n"
"v_mfma_f32_16x16x32_bf16 v[84:87], a[44:47], a[148:151], v[84:87]\n"
"buffer_load_dwordx4 v11, s[52:55], s67 offen offset:1024  lds\n"
"v_mfma_f32_16x16x32_bf16 v[84:87], a[40:43], a[144:147], v[84:87]\n"
"ds_read_b64_tr_b16 a[80:81], v15 offset:0\n"
"v_mfma_f32_16x16x32_bf16 a[4:7], a[40:43], a[152:155], a[4:7]\n"
"s_add_u32 s67, s75, 1013760\n"
"ds_read_b64_tr_b16 a[82:83], v15 offset:512\n"
"v_mfma_f32_16x16x32_bf16 a[4:7], a[44:47], a[156:159], a[4:7]\n"
"ds_read_b64_tr_b16 a[84:85], v15 offset:4096\n"
"v_mfma_f32_16x16x32_bf16 a[0:3], a[36:39], a[156:159], a[0:3]\n"
"ds_read_b64_tr_b16 a[86:87], v15 offset:4608\n"
"buffer_load_dwordx4 v11, s[52:55], s67 offen offset:2048  lds\n"
"v_mfma_f32_16x16x32_bf16 a[0:3], a[32:35], a[152:155], a[0:3]\n"
"ds_read_b64_tr_b16 a[88:89], v15 offset:8\n"
"v_mfma_f32_16x16x32_bf16 v[88:91], a[48:51], a[96:99], v[88:91]\n"
"s_add_u32 s67, s75, 1029120\n"
"ds_read_b64_tr_b16 a[90:91], v15 offset:520\n"
"v_mfma_f32_16x16x32_bf16 v[88:91], a[52:55], a[100:103], v[88:91]\n"
"ds_read_b64_tr_b16 a[92:93], v15 offset:4104\n"
"v_mfma_f32_16x16x32_bf16 v[116:119], a[60:63], a[100:103], v[116:119]\n"
"ds_read_b64_tr_b16 a[94:95], v15 offset:4616\n"
"v_mfma_f32_16x16x32_bf16 v[116:119], a[56:59], a[96:99], v[116:119]\n"
"buffer_load_dwordx4 v11, s[52:55], s67 offen offset:3072  lds\n"
"v_mfma_f32_16x16x32_bf16 v[120:123], a[56:59], a[104:107], v[120:123]\n"
"v_mfma_f32_16x16x32_bf16 v[120:123], a[60:63], a[108:111], v[120:123]\n"
"s_add_u32 m0, s64, 0\n"
"v_mfma_f32_16x16x32_bf16 v[92:95], a[52:55], a[108:111], v[92:95]\n"
"v_mfma_f32_16x16x32_bf16 v[92:95], a[48:51], a[104:107], v[92:95]\n"
"s_add_u32 s67, s75, 917632\n"
"v_mfma_f32_16x16x32_bf16 v[96:99], a[48:51], a[112:115], v[96:99]\n"
"s_waitcnt lgkmcnt(0)\n"
"s_mov_b32 s99, 0\n"
"v_mfma_f32_16x16x32_bf16 v[96:99], a[52:55], a[116:119], v[96:99]\n"
"s_mov_b32 s98, 0\n"
"buffer_load_dwordx4 v11, s[52:55], s67 offen offset:0  lds\n"
"v_mfma_f32_16x16x32_bf16 v[124:127], a[60:63], a[116:119], v[124:127]\n"
"s_memtime s[76:77]\n"
"s_load_dword s98, s[96:97], 8192 glc\n"
"v_mfma_f32_16x16x32_bf16 v[124:127], a[56:59], a[112:115], v[124:127]\n"
"s_add_u32 s67, s75, 932992\n"
"v_mfma_f32_16x16x32_bf16 v[128:131], a[56:59], a[120:123], v[128:131]\n"
"v_mfma_f32_16x16x32_bf16 v[128:131], a[60:63], a[124:127], v[128:131]\n"
"v_mfma_f32_16x16x32_bf16 v[100:103], a[52:55], a[124:127], v[100:103]\n"
"buffer_load_dwordx4 v11, s[52:55], s67 offen offset:1024  lds\n"
"v_mfma_f32_16x16x32_bf16 v[100:103], a[48:51], a[120:123], v[100:103]\n"
"v_mfma_f32_16x16x32_bf16 v[104:107], a[48:51], a[128:131], v[104:107]\n"
"s_add_u32 s67, s75, 948352\n"
"v_mfma_f32_16x16x32_bf16 v[104:107], a[52:55], a[132:135], v[104:107]\n"
"v_mfma_f32_16x16x32_bf16 v[132:135], a[60:63], a[132:135], v[132:135]\n"
"buffer_load_dwordx4 v11, s[52:55], s67 offen offset:2048  lds\n"
"v_mfma_f32_16x16x32_bf16 v[132:135], a[56:59], a[128:131], v[132:135]\n"
"s_mov_b32 s29, s76\n"
"v_mfma_f32_16x16x32_bf16 v[136:139], a[56:59], a[136:139], v[136:139]\n"
"s_add_u32 s67, s75, 963712\n"
"v_mfma_f32_16x16x32_bf16 v[136:139], a[60:63], a[140:143], v[136:139]\n"
"v_mfma_f32_16x16x32_bf16 v[108:111], a[52:55], a[140:143], v[108:111]\n"
"s_sub_u32 s99, s99, s98\n"
"s_sub_u32 s99, s99, s98\n"
"v_mfma_f32_16x16x32_bf16 v[108:111], a[48:51], a[136:139], v[108:111]\n"
"s_sub_u32 s99, s99, s98\n"
"buffer_load_dwordx4 v11, s[52:55], s67 offen offset:3072  lds\n"
"v_mfma_f32_16x16x32_bf16 v[112:115], a[48:51], a[144:147], v[112:115]\n"
"s_sub_u32 s99, s99, s98\n"
"s_add_u32 m0, s64, 4096\n"
"v_mfma_f32_16x16x32_bf16 v[112:115], a[52:55], a[148:151], v[112:115]\n"
"v_mfma_f32_16x16x32_bf16 v[140:143], a[60:63], a[148:151], v[140:143]\n"
"s_sub_u32 s99, s99, s98\n"
"s_sub_u32 s99, s99, s98\n"
"v_mfma_f32_16x16x32_bf16 v[140:143], a[56:59], a[144:147], v[140:143]\n"
"s_sub_u32 s99, s99, s98\n"
"s_add_u32 s67, s75, 983168\n"
"v_mfma_f32_16x16x32_bf16 a[12:15], a[56:59], a[152:155], a[12:15]\n"
"s_sub_u32 s99, s99, s98\n"
"buffer_load_dwordx4 v11, s[52:55], s67 offen offset:0  lds\n"
"v_mfma_f32_16x16x32_bf16 a[12:15], a[60:63], a[156:159], a[12:15]\n"
"s_add_u32 s67, s75, 998528\n"
"v_mfma_f32_16x16x32_bf16 a[8:11], a[52:55], a[156:159], a[8:11]\n"
"s_sub_u32 s99, s99, s98\n"
"s_sub_u32 s99, s99, s98\n"
"v_mfma_f32_16x16x32_bf16 a[8:11], a[48:51], a[152:155], a[8:11]\n"
"s_sub_u32 s99, s99, s98\n"
"v_mfma_f32_16x16x32_bf16 v[144:147], a[64:67], a[96:99], v[144:147]\n"
"s_sub_u32 s99, s99, s98\n"
"v_mfma_f32_16x16x32_bf16 v[144:147], a[68:71], a[100:103], v[144:147]\n"
"buffer_load_dwordx4 v11, s[52:55], s67 offen offset:1024  lds\n"
"v_mfma_f32_16x16x32_bf16 v[172:175], a[76:79], a[100:103], v[172:175]\n"
"s_sub_u32 s99, s99, s98\n"
"s_sub_u32 s99, s99, s98\n"
"v_mfma_f32_16x16x32_bf16 v[172:175], a[72:75], a[96:99], v[172:175]\n"
"s_sub_u32 s99, s99, s98\n"
"s_add_u32 s67, s75, 1013888\n"
"v_mfma_f32_16x16x32_bf16 v[176:179], a[72:75], a[104:107], v[176:179]\n"
"s_sub_u32 s99, s99, s98\n"
"v_mfma_f32_16x16x32_bf16 v[176:179], a[76:79], a[108:111], v[176:179]\n"
"s_sub_u32 s71, s64, s64\n"
"buffer_load_dwordx4 v11, s[52:55], s67 offen offset:2048  lds\n"
"v_mfma_f32_16x16x32_bf16 v[148:151], a[68:71], a[108:111], v[148:151]\n"
"s_sub_u32 s99, s99, s98\n"
"s_sub_u32 s99, s99, s98\n"
"v_mfma_f32_16x16x32_bf16 v[148:151], a[64:67], a[104:107], v[148:151]\n"
"s_sub_u32 s99, s99, s98\n"
"s_add_u32 s71, s71, 32768\n"
"s_add_u32 s67, s75, 1029248\n"
"v_mfma_f32_16x16x32_bf16 v[152:155], a[64:67], a[112:115], v[152:155]\n"
"s_sub_u32 s66, 122880, s73\n"
"s_sub_u32 s99, s99, s98\n"
"v_mfma_f32_16x16x32_bf16 v[152:155], a[68:71], a[116:119], v[152:155]\n"
"buffer_load_dwordx4 v11, s[52:55], s67 offen offset:3072  lds\n"
"v_mfma_f32_16x16x32_bf16 v[180:183], a[76:79], a[116:119], v[180:183]\n"
"s_sub_u32 s99, s99, s98\n"
"v_add_u32 v12, s66, v7\n"
"s_sub_u32 s99, s99, s98\n"
"v_mfma_f32_16x16x32_bf16 v[180:183], a[72:75], a[112:115], v[180:183]\n"
"s_sub_u32 s99, s99, s98\n"
"v_add_u32 v20, s71, v7\n"
"v_mfma_f32_16x16x32_bf16 v[184:187], a[72:75], a[120:123], v[184:187]\n"
"s_sub_u32 s99, s99, s98\n"
"v_add_u32 v13, s66, v8\n"
"v_mfma_f32_16x16x32_bf16 v[184:187], a[76:79], a[124:127], v[184:187]\n"
"s_memtime s[76:77]\n"
"v_add_u32 v21, s71, v8\n"
"v_mfma_f32_16x16x32_bf16 v[156:159], a[68:71], a[124:127], v[156:159]\n"
"s_add_u32 m0, s72, 57344\n"
"s_add_u32 s67, %21, 0\n"
"v_mfma_f32_16x16x32_bf16 v[156:159], a[64:67], a[120:123], v[156:159]\n"
"s_waitcnt lgkmcnt(0)\n"
"buffer_load_dwordx4 v11, s[52:55], s67 offen offset:0  lds\n"
"v_mfma_f32_16x16x32_bf16 v[160:163], a[64:67], a[128:131], v[160:163]\n"
"s_sub_u32 s29, s76, s29\n"
"s_waitcnt vmcnt(17)\n"
"v_mfma_f32_16x16x32_bf16 v[160:163], a[68:71], a[132:135], v[160:163]\n"
"s_cmp_lt_u32 s29, 620\n"
"s_cselect_b32 s99, s99, 0\n"
"v_mfma_f32_16x16x32_bf16 v[188:191], a[76:79], a[132:135], v[188:191]\n"
"s_max_u32 s31, s31, s99\n"
"ds_read_b128 a[192:195], v20 offset:0\n"
"v_mfma_f32_16x16x32_bf16 v[188:191], a[72:75], a[128:131], v[188:191]\n"
"s_add_u32 s67, %21, 15360\n"
"ds_read_b128 a[196:199], v21 offset:0\n"
"buffer_load_dwordx4 v11, s[52:55], s67 offen offset:1024  lds\n"
"v_mfma_f32_16x16x32_bf16 v[192:195], a[72:75], a[136:139], v[192:195]\n"
"ds_read_b128 a[200:203], v20 offset:2048\n"
"v_mfma_f32_16x16x32_bf16 v[192:195], a[76:79], a[140:143], v[192:195]\n"
"s_add_u32 s67, %21, 30720\n"
"ds_read_b128 a[204:207], v21 offset:2048\n"
"v_mfma_f32_16x16x32_bf16 v[164:167], a[68:71], a[140:143], v[164:167]\n"
"v_mfma_f32_16x16x32_bf16 v[164:167], a[64:67], a[136:139], v[164:167]\n"
"buffer_load_dwordx4 v11, s[52:55], s67 offen offset:2048  lds\n"
"v_mfma_f32_16x16x32_bf16 v[168:171], a[64:67], a[144:147], v[168:171]\n"
"v_mfma_f32_16x16x32_bf16 v[168:171], a[68:71], a[148:151], v[168:171]\n"
"s_add_u32 s67, %21, 46080\n"
"v_mfma_f32_16x16x32_bf16 v[196:199], a[76:79], a[148:151], v[196:199]\n"
"v_mfma_f32_16x16x32_bf16 v[196:199], a[72:75], a[144:147], v[196:199]\n"
"s_waitcnt lgkmcnt(0)\n"
"v_mfma_f32_16x16x32_bf16 a[20:23], a[72:75], a[152:155], a[20:23]\n"
"buffer_load_dwordx4 v11, s[52:55], s67 offen offset:3072  lds\n"
"v_mfma_f32_16x16x32_bf16 a[20:23], a[76:79], a[156:159], a[20:23]\n"
"s_cmp_lt_u32 %16, 2\n"
"s_cselect_b32 s67, 16384, 0\n"
"v_mfma_f32_16x16x32_bf16 a[16:19], a[68:71], a[156:159], a[16:19]\n"
"s_add_u32 s67, s67, s72\n"
"v_mfma_f32_16x16x32_bf16 a[16:19], a[64:67], a[152:155], a[16:19]\n"
"s_barrier\n"
"s_waitcnt vmcnt(12)\n"
"ds_read_b64_tr_b16 a[32:33], v9 offset:8192\n"
"v_mfma_f32_16x16x32_bf16 v[200:203], a[80:83], a[96:99], v[200:203]\n"
"s_add_u32 m0, s67, 16384\n"
"ds_read_b64_tr_b16 a[34:35], v9 offset:8704\n"
"v_mfma_f32_16x16x32_bf16 v[200:203], a[84:87], a[100:103], v[200:203]\n"
"s_sub_u32 s67, 1024, s60\n"
"ds_read_b64_tr_b16 a[36:37], v9 offset:12288\n"
"v_mfma_f32_16x16x32_bf16 v[228:231], a[92:95], a[100:103], v[228:231]\n"
"ds_read_b128 a[100:103], v13 offset:16384\n"
"buffer_load_dwordx4 v6, s[48:51], s67 offen offset:0  lds\n"
"v_mfma_f32_16x16x32_bf16 v[228:231], a[88:91], a[96:99], v[228:231]\n"
"ds_read_b128 a[96:99], v12 offset:16384\n"
"v_mfma_f32_16x16x32_bf16 v[232:235], a[88:91], a[104:107], v[232:235]\n"
"s_sub_u32 s67, 16384, s60\n"
"ds_read_b64_tr_b16 a[38:39], v9 offset:12800\n"
"v_mfma_f32_16x16x32_bf16 v[232:235], a[92:95], a[108:111], v[232:235]\n"
"ds_read_b64_tr_b16 a[40:41], v9 offset:8200\n"
"v_mfma_f32_16x16x32_bf16 v[204:207], a[84:87], a[108:111], v[204:207]\n"
"ds_read_b128 a[108:111], v13 offset:18432\n"
"buffer_load_dwordx4 v6, s[48:51], s67 offen offset:1024  lds\n"
"v_mfma_f32_16x16x32_bf16 v[204:207], a[80:83], a[104:107], v[204:207]\n"
"ds_read_b128 a[104:107], v12 offset:18432\n"
"v_mfma_f32_16x16x32_bf16 v[208:211], a[80:83], a[112:115], v[208:211]\n"
"s_sub_u32 s67, 31744, s60\n"
"ds_read_b64_tr_b16 a[42:43], v9 offset:8712\n"
"v_mfma_f32_16x16x32_bf16 v[208:211], a[84:87], a[116:119], v[208:211]\n"
"ds_read_b64_tr_b16 a[44:45], v9 offset:12296\n"
"v_mfma_f32_16x16x32_bf16 v[236:239], a[92:95], a[116:119], v[236:239]\n"
"ds_read_b128 a[116:119], v13 offset:20480\n"
"buffer_load_dwordx4 v6, s[48:51], s67 offen offset:2048  lds\n"
"v_mfma_f32_16x16x32_bf16 v[236:239], a[88:91], a[112:115], v[236:239]\n"
"ds_read_b128 a[112:115], v12 offset:20480\n"
"v_mfma_f32_16x16x32_bf16 v[240:243], a[88:91], a[120:123], v[240:243]\n"
"s_sub_u32 s67, 47104, s60\n"
"ds_read_b64_tr_b16 a[46:47], v9 offset:12808\n"
"v_mfma_f32_16x16x32_bf16 v[240:243], a[92:95], a[124:127], v[240:243]\n"
"ds_read_b64_tr_b16 a[48:49], v15 offset:8192\n"
"v_mfma_f32_16x16x32_bf16 v[212:215], a[84:87], a[124:127], v[212:215]\n"
"ds_read_b128 a[124:127], v13 offset:22528\n"
"v_mfma_f32_16x16x32_bf16 v[212:215], a[80:83], a[120:123], v[212:215]\n"
"ds_read_b128 a[120:123], v12 offset:22528\n"
"buffer_load_dwordx4 v6, s[48:51], s67 offen offset:3072  lds\n"
"v_mfma_f32_16x16x32_bf16 v[216:219], a[80:83], a[128:131], v[216:219]\n"
"ds_read_b64_tr_b16 a[50:51], v15 offset:8704\n"
"v_mfma_f32_16x16x32_bf16 v[216:219], a[84:87], a[132:135], v[216:219]\n"
"ds_read_b64_tr_b16 a[52:53], v15 offset:12288\n"
"v_mfma_f32_16x16x32_bf16 v[244:247], a[92:95], a[132:135], v[244:247]\n"
"ds_read_b128 a[132:135], v13 offset:24576\n"
"v_mfma_f32_16x16x32_bf16 v[244:247], a[88:91], a[128:131], v[244:247]\n"
"s_add_u32 m0, s64, 8192\n"
"ds_read_b128 a[128:131], v12 offset:24576\n"
"v_mfma_f32_16x16x32_bf16 v[248:251], a[88:91], a[136:139], v[248:251]\n"
"ds_read_b64_tr_b16 a[54:55], v15 offset:12800\n"
"v_mfma_f32_16x16x32_bf16 v[248:251], a[92:95], a[140:143], v[248:251]\n"
"ds_read_b64_tr_b16 a[56:57], v15 offset:8200\n"
"v_mfma_f32_16x16x32_bf16 v[220:223], a[84:87], a[140:143], v[220:223]\n"
"ds_read_b128 a[140:143], v13 offset:26624\n"
"v_mfma_f32_16x16x32_bf16 v[220:223], a[80:83], a[136:139], v[220:223]\n"
"ds_read_b128 a[136:139], v12 offset:26624\n"
"v_mfma_f32_16x16x32_bf16 v[224:227], a[80:83], a[144:147], v[224:227]\n"
"ds_read_b64_tr_b16 a[58:59], v15 offset:8712\n"
"v_mfma_f32_16x16x32_bf16 v[224:227], a[84:87], a[148:151], v[224:227]\n"
"ds_read_b64_tr_b16 a[60:61], v15 offset:12296\n"
"v_mfma_f32_16x16x32_bf16 v[252:255], a[92:95], a[148:151], v[252:255]\n"
"s_sub_u32 s67, 1048576, s75\n"
"ds_read_b128 a[148:151], v13 offset:28672\n"
"v_mfma_f32_16x16x32_bf16 v[252:255], a[88:91], a[144:147], v[252:255]\n"
"ds_read_b128 a[144:147], v12 offset:28672\n"
"ds_read_b64_tr_b16 a[62:63], v15 offset:12808\n"
"v_mfma_f32_16x16x32_bf16 a[28:31], a[88:91], a[152:155], a[28:31]\n"
"v_mfma_f32_16x16x32_bf16 a[28:31], a[92:95], a[156:159], a[28:31]\n"
"v_mfma_f32_16x16x32_bf16 a[24:27], a[84:87], a[156:159], a[24:27]\n"
"ds_read_b128 a[156:159], v13 offset:30720\n"
"v_mfma_f32_16x16x32_bf16 a[24:27], a[80:83], a[152:155], a[24:27]\n"
"s_waitcnt lgkmcnt(15)\n"
"ds_read_b128 a[152:155], v12 offset:30720\n"
"v_mfma_f32_16x16x32_bf16 v[32:35], a[32:35], a[96:99], v[32:35]\n"
"v_mfma_f32_16x16x32_bf16 v[32:35], a[36:39], a[100:103], v[32:35]\n"
"s_waitcnt lgkmcnt(2)\n"
"v_mfma_f32_16x16x32_bf16 v[60:63], a[44:47], a[100:103], v[60:63]\n"
"buffer_load_dwordx4 v11, s[52:55], s67 offen offset:0  lds\n"
"v_mfma_f32_16x16x32_bf16 v[60:63], a[40:43], a[96:99], v[60:63]\n"
"v_mfma_f32_16x16x32_bf16 v[64:67], a[40:43], a[104:107], v[64:67]\n"
"s_sub_u32 s67, 1063936, s75\n"
"v_mfma_f32_16x16x32_bf16 v[64:67], a[44:47], a[108:111], v[64:67]\n"
"v_mfma_f32_16x16x32_bf16 v[36:39], a[36:39], a[108:111], v[36:39]\n"
"buffer_load_dwordx4 v11, s[52:55], s67 offen offset:1024  lds\n"
"v_mfma_f32_16x16x32_bf16 v[36:39], a[32:35], a[104:107], v[36:39]\n"
"v_mfma_f32_16x16x32_bf16 v[40:43], a[32:35], a[112:115], v[40:43]\n"
"s_sub_u32 s67, 1079296, s75\n"
"v_mfma_f32_16x16x32_bf16 v[40:43], a[36:39], a[116:119], v[40:43]\n"
"v_mfma_f32_16x16x32_bf16 v[68:71], a[44:47], a[116:119], v[68:71]\n"
"v_mfma_f32_16x16x32_bf16 v[68:71], a[40:43], a[112:115], v[68:71]\n"
"buffer_load_dwordx4 v11, s[52:55], s67 offen offset:2048  lds\n"
"v_mfma_f32_16x16x32_bf16 v[72:75], a[40:43], a[120:123], v[72:75]\n"
"v_mfma_f32_16x16x32_bf16 v[72:75], a[44:47], a[124:127], v[72:75]\n"
"s_sub_u32 s67, 1094656, s75\n"
"v_mfma_f32_16x16x32_bf16 v[44:47], a[36:39], a[124:127], v[44:47]\n"
"v_mfma_f32_16x16x32_bf16 v[44:47], a[32:35], a[120:123], v[44:47]\n"
"buffer_load_dwordx4 v11, s[52:55], s67 offen offset:3072  lds\n"
"v_mfma_f32_16x16x32_bf16 v[48:51], a[32:35], a[128:131], v[48:51]\n"
"v_mfma_f32_16x16x32_bf16 v[48:51], a[36:39], a[132:135], v[48:51]\n"
"s_add_u32 m0, s64, 12288\n"
"v_mfma_f32_16x16x32_bf16 v[76:79], a[44:47], a[132:135], v[76:79]\n"
"s_waitcnt vmcnt(12)\n"
"ds_read_b64_tr_b16 a[64:65], v9 offset:0\n"
"v_mfma_f32_16x16x32_bf16 v[76:79], a[40:43], a[128:131], v[76:79]\n"
"s_sub_u32 s67, 1114112, s75\n"
"ds_read_b64_tr_b16 a[66:67], v9 offset:512\n"
"v_mfma_f32_16x16x32_bf16 v[80:83], a[40:43], a[136:139], v[80:83]\n"
"ds_read_b64_tr_b16 a[68:69], v9 offset:4096\n"
"v_mfma_f32_16x16x32_bf16 v[80:83], a[44:47], a[140:143], v[80:83]\n"
"ds_read_b64_tr_b16 a[70:71], v9 offset:4608\n"
"buffer_load_dwordx4 v11, s[52:55], s67 offen offset:0  lds\n"
"v_mfma_f32_16x16x32_bf16 v[52:55], a[36:39], a[140:143], v[52:55]\n"
"s_sub_u32 s67, 1129472, s75\n"
"ds_read_b64_tr_b16 a[72:73], v9 offset:8\n"
"v_mfma_f32_16x16x32_bf16 v[52:55], a[32:35], a[136:139], v[52:55]\n"
"s_waitcnt lgkmcnt(5)\n"
"ds_read_b64_tr_b16 a[74:75], v9 offset:520\n"
"v_mfma_f32_16x16x32_bf16 v[56:59], a[32:35], a[144:147], v[56:59]\n"
"ds_read_b64_tr_b16 a[76:77], v9 offset:4104\n"
"v_mfma_f32_16x16x32_bf16 v[56:59], a[36:39], a[148:151], v[56:59]\n"
"ds_read_b64_tr_b16 a[78:79], v9 offset:4616\n"
"v_mfma_f32_16x16x32_bf16 v[84:87], a[44:47], a[148:151], v[84:87]\n"
"buffer_load_dwordx4 v11, s[52:55], s67 offen offset:1024  lds\n"
"v_mfma_f32_16x16x32_bf16 v[84:87], a[40:43], a[144:147], v[84:87]\n"
"ds_read_b64_tr_b16 a[80:81], v15 offset:0\n"
"v_mfma_f32_16x16x32_bf16 a[4:7], a[40:43], a[152:155], a[4:7]\n"
"s_sub_u32 s67, 1144832, s75\n"
"ds_read_b64_tr_b16 a[82:83], v15 offset:512\n"
"v_mfma_f32_16x16x32_bf16 a[4:7], a[44:47], a[156:159], a[4:7]\n"
"ds_read_b64_tr_b16 a[84:85], v15 offset:4096\n"
"v_mfma_f32_16x16x32_bf16 a[0:3], a[36:39], a[156:159], a[0:3]\n"
"ds_read_b64_tr_b16 a[86:87], v15 offset:4608\n"
"buffer_load_dwordx4 v11, s[52:55], s67 offen offset:2048  lds\n"
"v_mfma_f32_16x16x32_bf16 a[0:3], a[32:35], a[152:155], a[0:3]\n"
"ds_read_b64_tr_b16 a[88:89], v15 offset:8\n"
"v_mfma_f32_16x16x32_bf16 v[88:91], a[48:51], a[96:99], v[88:91]\n"
"s_sub_u32 s67, 1160192, s75\n"
"ds_read_b64_tr_b16 a[90:91], v15 offset:520\n"
"v_mfma_f32_16x16x32_bf16 v[88:91], a[52:55], a[100:103], v[88:91]\n"
"ds_read_b64_tr_b16 a[92:93], v15 offset:4104\n"
"v_mfma_f32_16x16x32_bf16 v[116:119], a[60:63], a[100:103], v[116:119]\n"
"ds_read_b64_tr_b16 a[94:95], v15 offset:4616\n"
"v_mfma_f32_16x16x32_bf16 v[116:119], a[56:59], a[96:99], v[116:119]\n"
"buffer_load_dwordx4 v11, s[52:55], s67 offen offset:3072  lds\n"
"v_mfma_f32_16x16x32_bf16 v[120:123], a[56:59], a[104:107], v[120:123]\n"
"v_mfma_f32_16x16x32_bf16 v[120:123], a[60:63], a[108:111], v[120:123]\n"
"s_add_u32 m0, s64, 0\n"
"v_mfma_f32_16x16x32_bf16 v[92:95], a[52:55], a[108:111], v[92:95]\n"
"v_mfma_f32_16x16x32_bf16 v[92:95], a[48:51], a[104:107], v[92:95]\n"
"s_sub_u32 s67, 1048704, s75\n"
"v_mfma_f32_16x16x32_bf16 v[96:99], a[48:51], a[112:115], v[96:99]\n"
"s_waitcnt lgkmcnt(0)\n"
"v_mfma_f32_16x16x32_bf16 v[96:99], a[52:55], a[116:119], v[96:99]\n"
"buffer_load_dwordx4 v11, s[52:55], s67 offen offset:0  lds\n"
"v_mfma_f32_16x16x32_bf16 v[124:127], a[60:63], a[116:119], v[124:127]\n"
"v_mfma_f32_16x16x32_bf16 v[124:127], a[56:59], a[112:115], v[124:127]\n"
"s_sub_u32 s67, 1064064, s75\n"
"v_mfma_f32_16x16x32_bf16 v[128:131], a[56:59], a[120:123], v[128:131]\n"
"v_mfma_f32_16x16x32_bf16 v[128:131], a[60:63], a[124:127], v[128:131]\n"
"v_mfma_f32_16x16x32_bf16 v[100:103], a[52:55], a[124:127], v[100:103]\n"
"buffer_load_dwordx4 v11, s[52:55], s67 offen offset:1024  lds\n"
"v_mfma_f32_16x16x32_bf16 v[100:103], a[48:51], a[120:123], v[100:103]\n"
"v_mfma_f32_16x16x32_bf16 v[104:107], a[48:51], a[128:131], v[104:107]\n"
"s_sub_u32 s67, 1079424, s75\n"
"v_mfma_f32_16x16x32_bf16 v[104:107], a[52:55], a[132:135], v[104:107]\n"
"v_mfma_f32_16x16x32_bf16 v[132:135], a[60:63], a[132:135], v[132:135]\n"
"v_mfma_f32_16x16x32_bf16 v[132:135], a[56:59], a[128:131], v[132:135]\n"
"buffer_load_dwordx4 v11, s[52:55], s67 offen offset:2048  lds\n"
"v_mfma_f32_16x16x32_bf16 v[136:139], a[56:59], a[136:139], v[136:139]\n"
"v_mfma_f32_16x16x32_bf16 v[136:139], a[60:63], a[140:143], v[136:139]\n"
"s_sub_u32 s67, 1094784, s75\n"
"v_mfma_f32_16x16x32_bf16 v[108:111], a[52:55], a[140:143], v[108:111]\n"
"v_mfma_f32_16x16x32_bf16 v[108:111], a[48:51], a[136:139], v[108:111]\n"
"buffer_load_dwordx4 v11, s[52:55], s67 offen offset:3072  lds\n"
"v_mfma_f32_16x16x32_bf16 v[112:115], a[48:51], a[144:147], v[112:115]\n"
"v_mfma_f32_16x16x32_bf16 v[112:115], a[52:55], a[148:151], v[112:115]\n"
"s_add_u32 m0, s64, 4096\n"
"v_mfma_f32_16x16x32_bf16 v[140:143], a[60:63], a[148:151], v[140:143]\n"
"v_mfma_f32_16x16x32_bf16 v[140:143], a[56:59], a[144:147], v[140:143]\n"
"s_sub_u32 s67, 1114240, s75\n"
"v_mfma_f32_16x16x32_bf16 a[12:15], a[56:59], a[152:155], a[12:15]\n"
"v_mfma_f32_16x16x32_bf16 a[12:15], a[60:63], a[156:159], a[12:15]\n"
"buffer_load_dwordx4 v11, s[52:55], s67 offen offset:0  lds\n"
"v_mfma_f32_16x16x32_bf16 a[8:11], a[52:55], a[156:159], a[8:11]\n"
"v_mfma_f32_16x16x32_bf16 a[8:11], a[48:51], a[152:155], a[8:11]\n"
"s_sub_u32 s67, 1129600, s75\n"
"v_mfma_f32_16x16x32_bf16 v[144:147], a[64:67], a[96:99], v[144:147]\n"
"buffer_load_dwordx4 v11, s[52:55], s67 offen offset:1024  lds\n"
"v_mfma_f32_16x16x32_bf16 v[144:147], a[68:71], a[100:103], v[144:147]\n"
"s_sub_u32 s71, s72, s64\n"
"v_mfma_f32_16x16x32_bf16 v[172:175], a[76:79], a[100:103], v[172:175]\n"
"s_sub_u32 s67, 1144960, s75\n"
"v_mfma_f32_16x16x32_bf16 v[172:175], a[72:75], a[96:99], v[172:175]\n"
"v_mfma_f32_16x16x32_bf16 v[176:179], a[72:75], a[104:107], v[176:179]\n"
"s_add_u32 s71, s71, 57344\n"
"v_mfma_f32_16x16x32_bf16 v[176:179], a[76:79], a[108:111], v[176:179]\n"
"buffer_load_dwordx4 v11, s[52:55], s67 offen offset:2048  lds\n"
"v_mfma_f32_16x16x32_bf16 v[148:151], a[68:71], a[108:111], v[148:151]\n"
"v_add_u32 v20, s71, v9\n"
"v_mfma_f32_16x16x32_bf16 v[148:151], a[64:67], a[104:107], v[148:151]\n"
"s_sub_u32 s67, 1160320, s75\n"
"v_mfma_f32_16x16x32_bf16 v[152:155], a[64:67], a[112:115], v[152:155]\n"
"s_sub_u32 s66, 0, s73\n"
"v_mfma_f32_16x16x32_bf16 v[152:155], a[68:71], a[116:119], v[152:155]\n"
"v_add_u32 v21, s71, v15\n"
"v_mfma_f32_16x16x32_bf16 v[180:183], a[76:79], a[116:119], v[180:183]\n"
"v_add_u32 v12, s66, v7\n"
"v_mfma_f32_16x16x32_bf16 v[180:183], a[72:75], a[112:115], v[180:183]\n"
"buffer_load_dwordx4 v11, s[52:55], s67 offen offset:3072  lds\n"
"v_mfma_f32_16x16x32_bf16 v[184:187], a[72:75], a[120:123], v[184:187]\n"
"v_add_u32 v13, s66, v8\n"
"v_mfma_f32_16x16x32_bf16 v[184:187], a[76:79], a[124:127], v[184:187]\n"
"s_waitcnt vmcnt(20)\n"
"ds_read_b64_tr_b16 a[160:161], v20 offset:0\n"
"v_mfma_f32_16x16x32_bf16 v[156:159], a[68:71], a[124:127], v[156:159]\n"
"ds_read_b64_tr_b16 a[162:163], v20 offset:512\n"
"v_mfma_f32_16x16x32_bf16 v[156:159], a[64:67], a[120:123], v[156:159]\n"
"ds_read_b64_tr_b16 a[164:165], v20 offset:8\n"
"v_mfma_f32_16x16x32_bf16 v[160:163], a[64:67], a[128:131], v[160:163]\n"
"ds_read_b64_tr_b16 a[166:167], v20 offset:520\n"
"v_mfma_f32_16x16x32_bf16 v[160:163], a[68:71], a[132:135], v[160:163]\n"
"s_cmp_lt_u32 %16, 2\n"
"s_cselect_b32 s67, 16384, 0\n"
"v_mfma_f32_16x16x32_bf16 v[188:191], a[76:79], a[132:135], v[188:191]\n"
"s_add_u32 s67, s67, s72\n"
"ds_read_b64_tr_b16 a[168:169], v21 offset:0\n"
"v_mfma_f32_16x16x32_bf16 v[188:191], a[72:75], a[128:131], v[188:191]\n"
"ds_read_b64_tr_b16 a[170:171], v21 offset:512\n"
"v_mfma_f32_16x16x32_bf16 v[192:195], a[72:75], a[136:139], v[192:195]\n"
"ds_read_b64_tr_b16 a[172:173], v21 offset:8\n"
"v_mfma_f32_16x16x32_bf16 v[192:195], a[76:79], a[140:143], v[192:195]\n"
"s_add_u32 m0, s67, 57344\n"
"v_mfma_f32_16x16x32_bf16 v[164:167], a[68:71], a[140:143], v[164:167]\n"
"ds_read_b64_tr_b16 a[174:175], v21 offset:520\n"
"v_mfma_f32_16x16x32_bf16 v[164:167], a[64:67], a[136:139], v[164:167]\n"
"v_mfma_f32_16x16x32_bf16 v[168:171], a[64:67], a[144:147], v[168:171]\n"
"v_mfma_f32_16x16x32_bf16 v[168:171], a[68:71], a[148:151], v[168:171]\n"
"s_sub_u32 s67, 1280, s60\n"
"v_mfma_f32_16x16x32_bf16 v[196:199], a[76:79], a[148:151], v[196:199]\n"
"v_mfma_f32_16x16x32_bf16 v[196:199], a[72:75], a[144:147], v[196:199]\n"
"v_mfma_f32_16x16x32_bf16 a[20:23], a[72:75], a[152:155], a[20:23]\n"
"s_waitcnt lgkmcnt(0)\n"
"v_mfma_f32_16x16x32_bf16 a[20:23], a[76:79], a[156:159], a[20:23]\n"
"buffer_load_dwordx4 v6, s[48:51], s67 offen offset:0  lds\n"
"v_mfma_f32_16x16x32_bf16 a[16:19], a[68:71], a[156:159], a[16:19]\n"
"s_sub_u32 s67, 16640, s60\n"
"v_mfma_f32_16x16x32_bf16 a[16:19], a[64:67], a[152:155], a[16:19]\n"
"s_waitcnt vmcnt(17)\n"
"s_barrier\n"
"v_mfma_f32_16x16x32_bf16 v[200:203], a[80:83], a[96:99], v[200:203]\n"
"s_waitcnt vmcnt(9)\n"
"ds_read_b64_tr_b16 a[32:33], v9 offset:8192\n"
"v_mfma_f32_16x16x32_bf16 v[200:203], a[84:87], a[100:103], v[200:203]\n"
"ds_read_b64_tr_b16 a[34:35], v9 offset:8704\n"
"v_mfma_f32_16x16x32_bf16 v[228:231], a[92:95], a[100:103], v[228:231]\n"
"ds_read_b128 a[100:103], v13 offset:32768\n"
"buffer_load_dwordx4 v6, s[48:51], s67 offen offset:1024  lds\n"
"v_mfma_f32_16x16x32_bf16 v[228:231], a[88:91], a[96:99], v[228:231]\n"
"ds_read_b128 a[96:99], v12 offset:32768\n"
"v_mfma_f32_16x16x32_bf16 v[232:235], a[88:91], a[104:107], v[232:235]\n"
"s_sub_u32 s67, 32000, s60\n"
"ds_read_b64_tr_b16 a[36:37], v9 offset:12288\n"
"v_mfma_f32_16x16x32_bf16 v[232:235], a[92:95], a[108:111], v[232:235]\n"
"ds_read_b64_tr_b16 a[38:39], v9 offset:12800\n"
"v_mfma_f32_16x16x32_bf16 v[204:207], a[84:87], a[108:111], v[204:207]\n"
"ds_read_b128 a[108:111], v13 offset:34816\n"
"buffer_load_dwordx4 v6, s[48:51], s67 offen offset:2048  lds\n"
"v_mfma_f32_16x16x32_bf16 v[204:207], a[80:83], a[104:107], v[204:207]\n"
"ds_read_b128 a[104:107], v12 offset:34816\n"
"ds_read_b64_tr_b16 a[40:41], v9 offset:8200\n"
"v_mfma_f32_16x16x32_bf16 v[208:211], a[80:83], a[112:115], v[208:211]\n"
"s_sub_u32 s67, 47360, s60\n"
"ds_read_b64_tr_b16 a[42:43], v9 offset:8712\n"
"v_mfma_f32_16x16x32_bf16 v[208:211], a[84:87], a[116:119], v[208:211]\n"
"ds_read_b64_tr_b16 a[44:45], v9 offset:12296\n"
"v_mfma_f32_16x16x32_bf16 v[236:239], a[92:95], a[116:119], v[236:239]\n"
"ds_read_b128 a[116:119], v13 offset:36864\n"
"v_mfma_f32_16x16x32_bf16 v[236:239], a[88:91], a[112:115], v[236:239]\n"
"ds_read_b128 a[112:115], v12 offset:36864\n"
"buffer_load_dwordx4 v6, s[48:51], s67 offen offset:3072  lds\n"
"v_mfma_f32_16x16x32_bf16 v[240:243], a[88:91], a[120:123], v[240:243]\n"
"ds_read_b64_tr_b16 a[46:47], v9 offset:12808\n"
"v_mfma_f32_16x16x32_bf16 v[240:243], a[92:95], a[124:127], v[240:243]\n"
"s_add_u32 m0, s64, 20352\n"
"ds_read_b64_tr_b16 a[48:49], v15 offset:8192\n"
"v_mfma_f32_16x16x32_bf16 v[212:215], a[84:87], a[124:127], v[212:215]\n"
"ds_read_b128 a[124:127], v13 offset:38912\n"
"v_mfma_f32_16x16x32_bf16 v[212:215], a[80:83], a[120:123], v[212:215]\n"
"ds_read_b128 a[120:123], v12 offset:38912\n"
"v_mfma_f32_16x16x32_bf16 v[216:219], a[80:83], a[128:131], v[216:219]\n"
"ds_read_b64_tr_b16 a[50:51], v15 offset:8704\n"
"buffer_load_dwordx4 v6, s[40:43], s84 offen offset:128  lds\n"
"v_mfma_f32_16x16x32_bf16 v[216:219], a[84:87], a[132:135], v[216:219]\n"
"ds_read_b64_tr_b16 a[52:53], v15 offset:12288\n"
"v_mfma_f32_16x16x32_bf16 v[244:247], a[92:95], a[132:135], v[244:247]\n"
"ds_read_b128 a[132:135], v13 offset:24576\n"
"v_mfma_f32_16x16x32_bf16 v[244:247], a[88:91], a[128:131], v[244:247]\n"
"ds_read_b128 a[128:131], v12 offset:24576\n"
"buffer_load_dwordx4 v6, s[40:43], s85 offen offset:1152  lds\n"
"v_mfma_f32_16x16x32_bf16 v[248:251], a[88:91], a[136:139], v[248:251]\n"
"ds_read_b64_tr_b16 a[54:55], v15 offset:12800\n"
"v_mfma_f32_16x16x32_bf16 v[248:251], a[92:95], a[140:143], v[248:251]\n"
"ds_read_b64_tr_b16 a[56:57], v15 offset:8200\n"
"v_mfma_f32_16x16x32_bf16 v[220:223], a[84:87], a[140:143], v[220:223]\n"
"ds_read_b128 a[140:143], v13 offset:26624\n"
"v_mfma_f32_16x16x32_bf16 v[220:223], a[80:83], a[136:139], v[220:223]\n"
"ds_read_b128 a[136:139], v12 offset:26624\n"
"v_mfma_f32_16x16x32_bf16 v[224:227], a[80:83], a[144:147], v[224:227]\n"
"ds_read_b64_tr_b16 a[58:59], v15 offset:8712\n"
"buffer_load_dwordx4 v6, s[40:43], s86 offen offset:2176  lds\n"
"v_mfma_f32_16x16x32_bf16 v[224:227], a[84:87], a[148:151], v[224:227]\n"
"ds_read_b64_tr_b16 a[60:61], v15 offset:12296\n"
"v_mfma_f32_16x16x32_bf16 v[252:255], a[92:95], a[148:151], v[252:255]\n"
"ds_read_b128 a[148:151], v13 offset:28672\n"
"v_mfma_f32_16x16x32_bf16 v[252:255], a[88:91], a[144:147], v[252:255]\n"
"ds_read_b128 a[144:147], v12 offset:28672\n"
"ds_read_b64_tr_b16 a[62:63], v15 offset:12808\n"
"v_mfma_f32_16x16x32_bf16 a[28:31], a[88:91], a[152:155], a[28:31]\n"
"buffer_load_dwordx4 v6, s[40:43], s87 offen offset:3200  lds\n"
"v_mfma_f32_16x16x32_bf16 a[28:31], a[92:95], a[156:159], a[28:31]\n"
"v_mfma_f32_16x16x32_bf16 a[24:27], a[84:87], a[156:159], a[24:27]\n"
"s_mov_b32 s38, 1572864\n"
"ds_read_b128 a[156:159], v13 offset:30720\n"
"v_mfma_f32_16x16x32_bf16 a[24:27], a[80:83], a[152:155], a[24:27]\n"
"s_waitcnt lgkmcnt(15)\n"
"ds_read_b128 a[152:155], v12 offset:30720\n"
"v_mfma_f32_16x16x32_bf16 v[32:35], a[32:35], a[96:99], v[32:35]\n"
"v_mfma_f32_16x16x32_bf16 v[32:35], a[36:39], a[100:103], v[32:35]\n"
"s_waitcnt lgkmcnt(2)\n"
"v_mfma_f32_16x16x32_bf16 v[60:63], a[44:47], a[100:103], v[60:63]\n"
"buffer_store_dwordx4 a[160:163], v0, s[36:39], %23 offen offset:0 sc1\n"
"v_mfma_f32_16x16x32_bf16 v[60:63], a[40:43], a[96:99], v[60:63]\n"
"v_mfma_f32_16x16x32_bf16 v[64:67], a[40:43], a[104:107], v[64:67]\n"
"s_add_u32 m0, s64, 8192\n"
"v_mfma_f32_16x16x32_bf16 v[64:67], a[44:47], a[108:111], v[64:67]\n"
"v_mfma_f32_16x16x32_bf16 v[36:39], a[36:39], a[108:111], v[36:39]\n"
"s_sub_u32 s67, 1310720, s75\n"
"v_mfma_f32_16x16x32_bf16 v[36:39], a[32:35], a[104:107], v[36:39]\n"
"v_mfma_f32_16x16x32_bf16 v[40:43], a[32:35], a[112:115], v[40:43]\n"
"buffer_load_dwordx4 v11, s[52:55], s67 offen offset:0  lds\n"
"v_mfma_f32_16x16x32_bf16 v[40:43], a[36:39], a[116:119], v[40:43]\n"
"v_mfma_f32_16x16x32_bf16 v[68:71], a[44:47], a[116:119], v[68:71]\n"
"s_sub_u32 s67, 1326080, s75\n"
"v_mfma_f32_16x16x32_bf16 v[68:71], a[40:43], a[112:115], v[68:71]\n"
"v_mfma_f32_16x16x32_bf16 v[72:75], a[40:43], a[120:123], v[72:75]\n"
"buffer_load_dwordx4 v11, s[52:55], s67 offen offset:1024  lds\n"
"v_mfma_f32_16x16x32_bf16 v[72:75], a[44:47], a[124:127], v[72:75]\n"
"v_mfma_f32_16x16x32_bf16 v[44:47], a[36:39], a[124:127], v[44:47]\n"
"s_sub_u32 s67, 1341440, s75\n"
"v_mfma_f32_16x16x32_bf16 v[44:47], a[32:35], a[120:123], v[44:47]\n"
"v_mfma_f32_16x16x32_bf16 v[48:51], a[32:35], a[128:131], v[48:51]\n"
"buffer_load_dwordx4 v11, s[52:55], s67 offen offset:2048  lds\n"
"v_mfma_f32_16x16x32_bf16 v[48:51], a[36:39], a[132:135], v[48:51]\n"
"s_sub_u32 s67, 1356800, s75\n"
"v_mfma_f32_16x16x32_bf16 v[76:79], a[44:47], a[132:135], v[76:79]\n"
"s_waitcnt vmcnt(12)\n"
"ds_read_b64_tr_b16 a[64:65], v9 offset:0\n"
"v_mfma_f32_16x16x32_bf16 v[76:79], a[40:43], a[128:131], v[76:79]\n"
"ds_read_b64_tr_b16 a[66:67], v9 offset:512\n"
"v_mfma_f32_16x16x32_bf16 v[80:83], a[40:43], a[136:139], v[80:83]\n"
"ds_read_b64_tr_b16 a[68:69], v9 offset:4096\n"
"buffer_load_dwordx4 v11, s[52:55], s67 offen offset:3072  lds\n"
"v_mfma_f32_16x16x32_bf16 v[80:83], a[44:47], a[140:143], v[80:83]\n"
"ds_read_b64_tr_b16 a[70:71], v9 offset:4608\n"
"v_mfma_f32_16x16x32_bf16 v[52:55], a[36:39], a[140:143], v[52:55]\n"
"s_add_u32 m0, s64, 12288\n"
"ds_read_b64_tr_b16 a[72:73], v9 offset:8\n"
"v_mfma_f32_16x16x32_bf16 v[52:55], a[32:35], a[136:139], v[52:55]\n"
"s_waitcnt lgkmcnt(5)\n"
"ds_read_b64_tr_b16 a[74:75], v9 offset:520\n"
"v_mfma_f32_16x16x32_bf16 v[56:59], a[32:35], a[144:147], v[56:59]\n"
"s_sub_u32 s67, 1376256, s75\n"
"ds_read_b64_tr_b16 a[76:77], v9 offset:4104\n"
"v_mfma_f32_16x16x32_bf16 v[56:59], a[36:39], a[148:151], v[56:59]\n"
"ds_read_b64_tr_b16 a[78:79], v9 offset:4616\n"
"v_mfma_f32_16x16x32_bf16 v[84:87], a[44:47], a[148:151], v[84:87]\n"
"buffer_load_dwordx4 v11, s[52:55], s67 offen offset:0  lds\n"
"v_mfma_f32_16x16x32_bf16 v[84:87], a[40:43], a[144:147], v[84:87]\n"
"ds_read_b64_tr_b16 a[80:81], v15 offset:0\n"
"v_mfma_f32_16x16x32_bf16 a[4:7], a[40:43], a[152:155], a[4:7]\n"
"s_sub_u32 s67, 1391616, s75\n"
"ds_read_b64_tr_b16 a[82:83], v15 offset:512\n"
"v_mfma_f32_16x16x32_bf16 a[4:7], a[44:47], a[156:159], a[4:7]\n"
"ds_read_b64_tr_b16 a[84:85], v15 offset:4096\n"
"v_mfma_f32_16x16x32_bf16 a[0:3], a[36:39], a[156:159], a[0:3]\n"
"ds_read_b64_tr_b16 a[86:87], v15 offset:4608\n"
"buffer_load_dwordx4 v11, s[52:55], s67 offen offset:1024  lds\n"
"v_mfma_f32_16x16x32_bf16 a[0:3], a[32:35], a[152:155], a[0:3]\n"
"ds_read_b64_tr_b16 a[88:89], v15 offset:8\n"
"v_mfma_f32_16x16x32_bf16 v[88:91], a[48:51], a[96:99], v[88:91]\n"
"s_sub_u32 s67, 1406976, s75\n"
"ds_read_b64_tr_b16 a[90:91], v15 offset:520\n"
"v_mfma_f32_16x16x32_bf16 v[88:91], a[52:55], a[100:103], v[88:91]\n"
"ds_read_b64_tr_b16 a[92:93], v15 offset:4104\n"
"v_mfma_f32_16x16x32_bf16 v[116:119], a[60:63], a[100:103], v[116:119]\n"
"ds_read_b64_tr_b16 a[94:95], v15 offset:4616\n"
"v_mfma_f32_16x16x32_bf16 v[116:119], a[56:59], a[96:99], v[116:119]\n"
"buffer_load_dwordx4 v11, s[52:55], s67 offen offset:2048  lds\n"
"v_mfma_f32_16x16x32_bf16 v[120:123], a[56:59], a[104:107], v[120:123]\n"
"v_mfma_f32_16x16x32_bf16 v[120:123], a[60:63], a[108:111], v[120:123]\n"
"s_sub_u32 s67, 1422336, s75\n"
"v_mfma_f32_16x16x32_bf16 v[92:95], a[52:55], a[108:111], v[92:95]\n"
"v_mfma_f32_16x16x32_bf16 v[92:95], a[48:51], a[104:107], v[92:95]\n"
"buffer_load_dwordx4 v11, s[52:55], s67 offen offset:3072  lds\n"
"v_mfma_f32_16x16x32_bf16 v[96:99], a[48:51], a[112:115], v[96:99]\n"
"s_waitcnt lgkmcnt(0)\n"
"s_mov_b32 s99, 0\n"
"v_mfma_f32_16x16x32_bf16 v[96:99], a[52:55], a[116:119], v[96:99]\n"
"s_mov_b32 s98, 0\n"
"s_add_u32 m0, s64, 0\n"
"v_mfma_f32_16x16x32_bf16 v[124:127], a[60:63], a[116:119], v[124:127]\n"
"s_memtime s[76:77]\n"
"s_load_dword s98, s[96:97], 0 glc\n"
"v_mfma_f32_16x16x32_bf16 v[124:127], a[56:59], a[112:115], v[124:127]\n"
"s_sub_u32 s67, 1310848, s75\n"
"v_mfma_f32_16x16x32_bf16 v[128:131], a[56:59], a[120:123], v[128:131]\n"
"buffer_load_dwordx4 v11, s[52:55], s67 offen offset:0  lds\n"
"v_mfma_f32_16x16x32_bf16 v[128:131], a[60:63], a[124:127], v[128:131]\n"
"v_mfma_f32_16x16x32_bf16 v[100:103], a[52:55], a[124:127], v[100:103]\n"
"s_sub_u32 s67, 1326208, s75\n"
"v_mfma_f32_16x16x32_bf16 v[100:103], a[48:51], a[120:123], v[100:103]\n"
"v_mfma_f32_16x16x32_bf16 v[104:107], a[48:51], a[128:131], v[104:107]\n"
"buffer_load_dwordx4 v11, s[52:55], s67 offen offset:1024  lds\n"
"v_mfma_f32_16x16x32_bf16 v[104:107], a[52:55], a[132:135], v[104:107]\n"
"v_mfma_f32_16x16x32_bf16 v[132:135], a[60:63], a[132:135], v[132:135]\n"
"s_sub_u32 s67, 1341568, s75\n"
"v_mfma_f32_16x16x32_bf16 v[132:135], a[56:59], a[128:131], v[132:135]\n"
"s_mov_b32 s29, s76\n"
"v_mfma_f32_16x16x32_bf16 v[136:139], a[56:59], a[136:139], v[136:139]\n"
"v_mfma_f32_16x16x32_bf16 v[136:139], a[60:63], a[140:143], v[136:139]\n"
"buffer_load_dwordx4 v11, s[52:55], s67 offen offset:2048  lds\n"
"v_mfma_f32_16x16x32_bf16 v[108:111], a[52:55], a[140:143], v[108:111]\n"
"s_sub_u32 s99, s99, s98\n"
"s_sub_u32 s99, s99, s98\n"
"v_mfma_f32_16x16x32_bf16 v[108:111], a[48:51], a[136:139], v[108:111]\n"
"s_sub_u32 s99, s99, s98\n"
"s_sub_u32 s67, 1356928, s75\n"
"v_mfma_f32_16x16x32_bf16 v[112:115], a[48:51], a[144:147], v[112:115]\n"
"s_sub_u32 s99, s99, s98\n"
"v_mfma_f32_16x16x32_bf16 v[112:115], a[52:55], a[148:151], v[112:115]\n"
"buffer_load_dwordx4 v11, s[52:55], s67 offen offset:3072  lds\n"
"v_mfma_f32_16x16x32_bf16 v[140:143], a[60:63], a[148:151], v[140:143]\n"
"s_sub_u32 s99, s99, s98\n"
"s_sub_u32 s99, s99, s98\n"
"v_mfma_f32_16x16x32_bf16 v[140:143], a[56:59], a[144:147], v[140:143]\n"
"s_sub_u32 s99, s99, s98\n"
"s_add_u32 m0, s64, 4096\n"
"v_mfma_f32_16x16x32_bf16 a[12:15], a[56:59], a[152:155], a[12:15]\n"
"s_sub_u32 s99, s99, s98\n"
"s_sub_u32 s67, 1376384, s75\n"
"v_mfma_f32_16x16x32_bf16 a[12:15], a[60:63], a[156:159], a[12:15]\n"
"buffer_load_dwordx4 v11, s[52:55], s67 offen offset:0  lds\n"
"v_mfma_f32_16x16x32_bf16 a[8:11], a[52:55], a[156:159], a[8:11]\n"
"s_sub_u32 s99, s99, s98\n"
"s_sub_u32 s99, s99, s98\n"
"v_mfma_f32_16x16x32_bf16 a[8:11], a[48:51], a[152:155], a[8:11]\n"
"s_sub_u32 s99, s99, s98\n"
"s_sub_u32 s67, 1391744, s75\n"
"v_mfma_f32_16x16x32_bf16 v[144:147], a[64:67], a[96:99], v[144:147]\n"
"s_sub_u32 s99, s99, s98\n"
"v_mfma_f32_16x16x32_bf16 v[144:147], a[68:71], a[100:103], v[144:147]\n"
"buffer_load_dwordx4 v11, s[52:55], s67 offen offset:1024  lds\n"
"v_mfma_f32_16x16x32_bf16 v[172:175], a[76:79], a[100:103], v[172:175]\n"
"s_sub_u32 s99, s99, s98\n"
"s_sub_u32 s99, s99, s98\n"
"v_mfma_f32_16x16x32_bf16 v[172:175], a[72:75], a[96:99], v[172:175]\n"
"s_sub_u32 s99, s99, s98\n"
"s_sub_u32 s67, 1407104, s75\n"
"v_mfma_f32_16x16x32_bf16 v[176:179], a[72:75], a[104:107], v[176:179]\n"
"s_sub_u32 s99, s99, s98\n"
"v_mfma_f32_16x16x32_bf16 v[176:179], a[76:79], a[108:111], v[176:179]\n"
"v_mfma_f32_16x16x32_bf16 v[148:151], a[68:71], a[108:111], v[148:151]\n"
"s_sub_u32 s99, s99, s98\n"
"s_sub_u32 s99, s99, s98\n"
"v_mfma_f32_16x16x32_bf16 v[148:151], a[64:67], a[104:107], v[148:151]\n"
"s_sub_u32 s99, s99, s98\n"
"s_sub_u32 s71, s64, s64\n"
"buffer_load_dwordx4 v11, s[52:55], s67 offen offset:2048  lds\n"
"v_mfma_f32_16x16x32_bf16 v[152:155], a[64:67], a[112:115], v[152:155]\n"
"s_sub_u32 s66, 40960, s73\n"
"s_sub_u32 s99, s99, s98\n"
"v_mfma_f32_16x16x32_bf16 v[152:155], a[68:71], a[116:119], v[152:155]\n"
"s_add_u32 s71, s71, 20480\n"
"s_sub_u32 s67, 1422464, s75\n"
"v_mfma_f32_16x16x32_bf16 v[180:183], a[76:79], a[116:119], v[180:183]\n"
"s_sub_u32 s99, s99, s98\n"
"v_add_u32 v12, s66, v7\n"
"s_sub_u32 s99, s99, s98\n"
"v_mfma_f32_16x16x32_bf16 v[180:183], a[72:75], a[112:115], v[180:183]\n"
"s_sub_u32 s99, s99, s98\n"
"v_mfma_f32_16x16x32_bf16 v[184:187], a[72:75], a[120:123], v[184:187]\n"
"s_sub_u32 s99, s99, s98\n"
"v_add_u32 v13, s66, v8\n"
"v_mfma_f32_16x16x32_bf16 v[184:187], a[76:79], a[124:127], v[184:187]\n"
"s_memtime s[76:77]\n"
"buffer_load_dwordx4 v11, s[52:55], s67 offen offset:3072  lds\n"
"v_add_u32 v20, s71, v7\n"
"v_mfma_f32_16x16x32_bf16 v[156:159], a[68:71], a[124:127], v[156:159]\n"
"s_cmp_lt_u32 %16, 2\n"
"s_cselect_b32 s67, 16384, 0\n"
"s_add_u32 s67, s67, s72\n"
"v_mfma_f32_16x16x32_bf16 v[156:159], a[64:67], a[120:123], v[156:159]\n"
"s_waitcnt lgkmcnt(0)\n"
"v_add_u32 v21, s71, v8\n"
"s_add_u32 m0, s67, 98304\n"
"s_sub_u32 s67, 1536, s60\n"
"v_mfma_f32_16x16x32_bf16 v[160:163], a[64:67], a[128:131], v[160:163]\n"
"s_sub_u32 s29, s76, s29\n"
"buffer_load_dwordx4 v6, s[48:51], s67 offen offset:0  lds\n"
"v_mfma_f32_16x16x32_bf16 v[160:163], a[68:71], a[132:135], v[160:163]\n"
"s_cmp_lt_u32 s29, 620\n"
"s_cselect_b32 s99, s99, 0\n"
"v_mfma_f32_16x16x32_bf16 v[188:191], a[76:79], a[132:135], v[188:191]\n"
"s_max_u32 s30, s30, s99\n"
"s_sub_u32 s67, 16896, s60\n"
"v_mfma_f32_16x16x32_bf16 v[188:191], a[72:75], a[128:131], v[188:191]\n"
"s_waitcnt vmcnt(18)\n"
"ds_read_b128 a[208:211], v20 offset:0\n"
"v_mfma_f32_16x16x32_bf16 v[192:195], a[72:75], a[136:139], v[192:195]\n"
"ds_read_b128 a[212:215], v21 offset:0\n"
"v_mfma_f32_16x16x32_bf16 v[192:195], a[76:79], a[140:143], v[192:195]\n"
"ds_read_b128 a[216:219], v20 offset:2048\n"
"v_mfma_f32_16x16x32_bf16 v[164:167], a[68:71], a[140:143], v[164:167]\n"
"buffer_load_dwordx4 v6, s[48:51], s67 offen offset:1024  lds\n"
"v_mfma_f32_16x16x32_bf16 v[164:167], a[64:67], a[136:139], v[164:167]\n"
"ds_read_b128 a[220:223], v21 offset:2048\n"
"v_mfma_f32_16x16x32_bf16 v[168:171], a[64:67], a[144:147], v[168:171]\n"
"s_sub_u32 s67, 32256, s60\n"
"v_mfma_f32_16x16x32_bf16 v[168:171], a[68:71], a[148:151], v[168:171]\n"
"v_mfma_f32_16x16x32_bf16 v[196:199], a[76:79], a[148:151], v[196:199]\n"
"buffer_load_dwordx4 v6, s[48:51], s67 offen offset:2048  lds\n"
"v_mfma_f32_16x16x32_bf16 v[196:199], a[72:75], a[144:147], v[196:199]\n"
"v_mfma_f32_16x16x32_bf16 a[20:23], a[72:75], a[152:155], a[20:23]\n"
"s_sub_u32 s67, 47616, s60\n"
"v_mfma_f32_16x16x32_bf16 a[20:23], a[76:79], a[156:159], a[20:23]\n"
"s_waitcnt lgkmcnt(0)\n"
"v_mfma_f32_16x16x32_bf16 a[16:19], a[68:71], a[156:159], a[16:19]\n"
"buffer_load_dwordx4 v6, s[48:51], s67 offen offset:3072  lds\n"
"v_mfma_f32_16x16x32_bf16 a[16:19], a[64:67], a[152:155], a[16:19]\n"
"s_barrier\n"
"s_waitcnt vmcnt(12)\n"
"ds_read_b64_tr_b16 a[32:33], v9 offset:8192\n"
"v_mfma_f32_16x16x32_bf16 v[200:203], a[80:83], a[96:99], v[200:203]\n"
"s_add_u32 m0, s64, 16384\n"
"ds_read_b64_tr_b16 a[34:35], v9 offset:8704\n"
"v_mfma_f32_16x16x32_bf16 v[200:203], a[84:87], a[100:103], v[200:203]\n"
"ds_read_b64_tr_b16 a[36:37], v9 offset:12288\n"
"v_mfma_f32_16x16x32_bf16 v[228:231], a[92:95], a[100:103], v[228:231]\n"
"s_add_u32 s67, %21, 4194304\n"
"ds_read_b128 a[100:103], v13 offset:32768\n"
"v_mfma_f32_16x16x32_bf16 v[228:231], a[88:91], a[96:99], v[228:231]\n"
"ds_read_b128 a[96:99], v12 offset:32768\n"
"v_mfma_f32_16x16x32_bf16 v[232:235], a[88:91], a[104:107], v[232:235]\n"
"ds_read_b64_tr_b16 a[38:39], v9 offset:12800\n"
"buffer_load_dwordx4 v11, s[52:55], s67 offen offset:0  lds\n"
"v_mfma_f32_16x16x32_bf16 v[232:235], a[92:95], a[108:111], v[232:235]\n"
"ds_read_b64_tr_b16 a[40:41], v9 offset:8200\n"
"v_mfma_f32_16x16x32_bf16 v[204:207], a[84:87], a[108:111], v[204:207]\n"
"s_add_u32 s67, %21, 4209664\n"
"ds_read_b128 a[108:111], v13 offset:34816\n"
"v_mfma_f32_16x16x32_bf16 v[204:207], a[80:83], a[104:107], v[204:207]\n"
"ds_read_b128 a[104:107], v12 offset:34816\n"
"v_mfma_f32_16x16x32_bf16 v[208:211], a[80:83], a[112:115], v[208:211]\n"
"ds_read_b64_tr_b16 a[42:43], v9 offset:8712\n"
"buffer_load_dwordx4 v11, s[52:55], s67 offen offset:1024  lds\n"
"v_mfma_f32_16x16x32_bf16 v[208:211], a[84:87], a[116:119], v[208:211]\n"
"ds_read_b64_tr_b16 a[44:45], v9 offset:12296\n"
"v_mfma_f32_16x16x32_bf16 v[236:239], a[92:95], a[116:119], v[236:239]\n"
"s_add_u32 s67, %21, 4225024\n"
"ds_read_b128 a[116:119], v13 offset:36864\n"
"v_mfma_f32_16x16x32_bf16 v[236:239], a[88:91], a[112:115], v[236:239]\n"
"ds_read_b128 a[112:115], v12 offset:36864\n"
"v_mfma_f32_16x16x32_bf16 v[240:243], a[88:91], a[120:123], v[240:243]\n"
"ds_read_b64_tr_b16 a[46:47], v9 offset:12808\n"
"buffer_load_dwordx4 v11, s[52:55], s67 offen offset:2048  lds\n"
"v_mfma_f32_16x16x32_bf16 v[240:243], a[92:95], a[124:127], v[240:243]\n"
"ds_read_b64_tr_b16 a[48:49], v15 offset:8192\n"
"v_mfma_f32_16x16x32_bf16 v[212:215], a[84:87], a[124:127], v[212:215]\n"
"s_add_u32 s67, %21, 4240384\n"
"ds_read_b128 a[124:127], v13 offset:38912\n"
"v_mfma_f32_16x16x32_bf16 v[212:215], a[80:83], a[120:123], v[212:215]\n"
"ds_read_b128 a[120:123], v12 offset:38912\n"
"v_mfma_f32_16x16x32_bf16 v[216:219], a[80:83], a[128:131], v[216:219]\n"
"ds_read_b64_tr_b16 a[50:51], v15 offset:8704\n"
"buffer_load_dwordx4 v11, s[52:55], s67 offen offset:3072  lds\n"
"v_mfma_f32_16x16x32_bf16 v[216:219], a[84:87], a[132:135], v[216:219]\n"
"ds_read_b64_tr_b16 a[52:53], v15 offset:12288\n"
"v_mfma_f32_16x16x32_bf16 v[244:247], a[92:95], a[132:135], v[244:247]\n"
"ds_read_b128 a[132:135], v13 offset:24576\n"
"v_mfma_f32_16x16x32_bf16 v[244:247], a[88:91], a[128:131], v[244:247]\n"
"ds_read_b128 a[128:131], v12 offset:24576\n"
"v_mfma_f32_16x16x32_bf16 v[248:251], a[88:91], a[136:139], v[248:251]\n"
"s_add_u32 m0, s64, 8192\n"
"ds_read_b64_tr_b16 a[54:55], v15 offset:12800\n"
"v_mfma_f32_16x16x32_bf16 v[248:251], a[92:95], a[140:143], v[248:251]\n"
"ds_read_b64_tr_b16 a[56:57], v15 offset:8200\n"
"v_mfma_f32_16x16x32_bf16 v[220:223], a[84:87], a[140:143], v[220:223]\n"
"ds_read_b128 a[140:143], v13 offset:26624\n"
"v_mfma_f32_16x16x32_bf16 v[220:223], a[80:83], a[136:139], v[220:223]\n"
"ds_read_b128 a[136:139], v12 offset:26624\n"
"v_mfma_f32_16x16x32_bf16 v[224:227], a[80:83], a[144:147], v[224:227]\n"
"ds_read_b64_tr_b16 a[58:59], v15 offset:8712\n"
"v_mfma_f32_16x16x32_bf16 v[224:227], a[84:87], a[148:151], v[224:227]\n"
"ds_read_b64_tr_b16 a[60:61], v15 offset:12296\n"
"v_mfma_f32_16x16x32_bf16 v[252:255], a[92:95], a[148:151], v[252:255]\n"
"s_sub_u32 s67, 1572864, s75\n"
"ds_read_b128 a[148:151], v13 offset:28672\n"
"v_mfma_f32_16x16x32_bf16 v[252:255], a[88:91], a[144:147], v[252:255]\n"
"ds_read_b128 a[144:147], v12 offset:28672\n"
"ds_read_b64_tr_b16 a[62:63], v15 offset:12808\n"
"v_mfma_f32_16x16x32_bf16 a[28:31], a[88:91], a[152:155], a[28:31]\n"
"v_mfma_f32_16x16x32_bf16 a[28:31], a[92:95], a[156:159], a[28:31]\n"
"v_mfma_f32_16x16x32_bf16 a[24:27], a[84:87], a[156:159], a[24:27]\n"
"ds_read_b128 a[156:159], v13 offset:30720\n"
"v_mfma_f32_16x16x32_bf16 a[24:27], a[80:83], a[152:155], a[24:27]\n"
"s_waitcnt lgkmcnt(15)\n"
"ds_read_b128 a[152:155], v12 offset:30720\n"
"v_mfma_f32_16x16x32_bf16 v[32:35], a[32:35], a[96:99], v[32:35]\n"
"v_mfma_f32_16x16x32_bf16 v[32:35], a[36:39], a[100:103], v[32:35]\n"
"s_waitcnt lgkmcnt(2)\n"
"v_mfma_f32_16x16x32_bf16 v[60:63], a[44:47], a[100:103], v[60:63]\n"
"buffer_load_dwordx4 v11, s[52:55], s67 offen offset:0  lds\n"
"v_mfma_f32_16x16x32_bf16 v[60:63], a[40:43], a[96:99], v[60:63]\n"
"v_mfma_f32_16x16x32_bf16 v[64:67], a[40:43], a[104:107], v[64:67]\n"
"s_sub_u32 s67, 1588224, s75\n"
"v_mfma_f32_16x16x32_bf16 v[64:67], a[44:47], a[108:111], v[64:67]\n"
"v_mfma_f32_16x16x32_bf16 v[36:39], a[36:39], a[108:111], v[36:39]\n"
"v_mfma_f32_16x16x32_bf16 v[36:39], a[32:35], a[104:107], v[36:39]\n"
"buffer_load_dwordx4 v11, s[52:55], s67 offen offset:1024  lds\n"
"v_mfma_f32_16x16x32_bf16 v[40:43], a[32:35], a[112:115], v[40:43]\n"
"v_mfma_f32_16x16x32_bf16 v[40:43], a[36:39], a[116:119], v[40:43]\n"
"s_sub_u32 s67, 1603584, s75\n"
"v_mfma_f32_16x16x32_bf16 v[68:71], a[44:47], a[116:119], v[68:71]\n"
"v_mfma_f32_16x16x32_bf16 v[68:71], a[40:43], a[112:115], v[68:71]\n"
"buffer_load_dwordx4 v11, s[52:55], s67 offen offset:2048  lds\n"
"v_mfma_f32_16x16x32_bf16 v[72:75], a[40:43], a[120:123], v[72:75]\n"
"v_mfma_f32_16x16x32_bf16 v[72:75], a[44:47], a[124:127], v[72:75]\n"
"s_sub_u32 s67, 1618944, s75\n"
"v_mfma_f32_16x16x32_bf16 v[44:47], a[36:39], a[124:127], v[44:47]\n"
"v_mfma_f32_16x16x32_bf16 v[44:47], a[32:35], a[120:123], v[44:47]\n"
"v_mfma_f32_16x16x32_bf16 v[48:51], a[32:35], a[128:131], v[48:51]\n"
"buffer_load_dwordx4 v11, s[52:55], s67 offen offset:3072  lds\n"
"v_mfma_f32_16x16x32_bf16 v[48:51], a[36:39], a[132:135], v[48:51]\n"
"s_add_u32 m0, s64, 12288\n"
"v_mfma_f32_16x16x32_bf16 v[76:79], a[44:47], a[132:135], v[76:79]\n"
"s_waitcnt vmcnt(12)\n"
"ds_read_b64_tr_b16 a[64:65], v9 offset:0\n"
"v_mfma_f32_16x16x32_bf16 v[76:79], a[40:43], a[128:131], v[76:79]\n"
"s_sub_u32 s67, 1638400, s75\n"
"ds_read_b64_tr_b16 a[66:67], v9 offset:512\n"
"v_mfma_f32_16x16x32_bf16 v[80:83], a[40:43], a[136:139], v[80:83]\n"
"ds_read_b64_tr_b16 a[68:69], v9 offset:4096\n"
"v_mfma_f32_16x16x32_bf16 v[80:83], a[44:47], a[140:143], v[80:83]\n"
"ds_read_b64_tr_b16 a[70:71], v9 offset:4608\n"
"buffer_load_dwordx4 v11, s[52:55], s67 offen offset:0  lds\n"
"v_mfma_f32_16x16x32_bf16 v[52:55], a[36:39], a[140:143], v[52:55]\n"
"s_sub_u32 s67, 1653760, s75\n"
"ds_read_b64_tr_b16 a[72:73], v9 offset:8\n"
"v_mfma_f32_16x16x32_bf16 v[52:55], a[32:35], a[136:139], v[52:55]\n"
"s_waitcnt lgkmcnt(5)\n"
"ds_read_b64_tr_b16 a[74:75], v9 offset:520\n"
"v_mfma_f32_16x16x32_bf16 v[56:59], a[32:35], a[144:147], v[56:59]\n"
"ds_read_b64_tr_b16 a[76:77], v9 offset:4104\n"
"v_mfma_f32_16x16x32_bf16 v[56:59], a[36:39], a[148:151], v[56:59]\n"
"ds_read_b64_tr_b16 a[78:79], v9 offset:4616\n"
"v_mfma_f32_16x16x32_bf16 v[84:87], a[44:47], a[148:151], v[84:87]\n"
"buffer_load_dwordx4 v11, s[52:55], s67 offen offset:1024  lds\n"
"v_mfma_f32_16x16x32_bf16 v[84:87], a[40:43], a[144:147], v[84:87]\n"
"ds_read_b64_tr_b16 a[80:81], v15 offset:0\n"
"v_mfma_f32_16x16x32_bf16 a[4:7], a[40:43], a[152:155], a[4:7]\n"
"s_sub_u32 s67, 1669120, s75\n"
"ds_read_b64_tr_b16 a[82:83], v15 offset:512\n"
"v_mfma_f32_16x16x32_bf16 a[4:7], a[44:47], a[156:159], a[4:7]\n"
"ds_read_b64_tr_b16 a[84:85], v15 offset:4096\n"
"v_mfma_f32_16x16x32_bf16 a[0:3], a[36:39], a[156:159], a[0:3]\n"
"ds_read_b64_tr_b16 a[86:87], v15 offset:4608\n"
"buffer_load_dwordx4 v11, s[52:55], s67 offen offset:2048  lds\n"
"v_mfma_f32_16x16x32_bf16 a[0:3], a[32:35], a[152:155], a[0:3]\n"
"ds_read_b64_tr_b16 a[88:89], v15 offset:8\n"
"v_mfma_f32_16x16x32_bf16 v[88:91], a[48:51], a[96:99], v[88:91]\n"
"s_sub_u32 s67, 1684480, s75\n"
"ds_read_b64_tr_b16 a[90:91], v15 offset:520\n"
"v_mfma_f32_16x16x32_bf16 v[88:91], a[52:55], a[100:103], v[88:91]\n"
"ds_read_b64_tr_b16 a[92:93], v15 offset:4104\n"
"v_mfma_f32_16x16x32_bf16 v[116:119], a[60:63], a[100:103], v[116:119]\n"
"ds_read_b64_tr_b16 a[94:95], v15 offset:4616\n"
"v_mfma_f32_16x16x32_bf16 v[116:119], a[56:59], a[96:99], v[116:119]\n"
"buffer_load_dwordx4 v11, s[52:55], s67 offen offset:3072  lds\n"
"v_mfma_f32_16x16x32_bf16 v[120:123], a[56:59], a[104:107], v[120:123]\n"
"v_mfma_f32_16x16x32_bf16 v[120:123], a[60:63], a[108:111], v[120:123]\n"
"s_add_u32 m0, s64, 0\n"
"v_mfma_f32_16x16x32_bf16 v[92:95], a[52:55], a[108:111], v[92:95]\n"
"v_mfma_f32_16x16x32_bf16 v[92:95], a[48:51], a[104:107], v[92:95]\n"
"s_sub_u32 s67, 1572992, s75\n"
"v_mfma_f32_16x16x32_bf16 v[96:99], a[48:51], a[112:115], v[96:99]\n"
"s_waitcnt lgkmcnt(0)\n"
"s_mov_b32 s99, 0\n"
"v_mfma_f32_16x16x32_bf16 v[96:99], a[52:55], a[116:119], v[96:99]\n"
"s_mov_b32 s98, 0\n"
"buffer_load_dwordx4 v11, s[52:55], s67 offen offset:0  lds\n"
"v_mfma_f32_16x16x32_bf16 v[124:127], a[60:63], a[116:119], v[124:127]\n"
"s_memtime s[76:77]\n"
"s_load_dword s98, s[96:97], 8320 glc\n"
"v_mfma_f32_16x16x32_bf16 v[124:127], a[56:59], a[112:115], v[124:127]\n"
"s_sub_u32 s67, 1588352, s75\n"
"v_mfma_f32_16x16x32_bf16 v[128:131], a[56:59], a[120:123], v[128:131]\n"
"v_mfma_f32_16x16x32_bf16 v[128:131], a[60:63], a[124:127], v[128:131]\n"
"buffer_load_dwordx4 v11, s[52:55], s67 offen offset:1024  lds\n"
"v_mfma_f32_16x16x32_bf16 v[100:103], a[52:55], a[124:127], v[100:103]\n"
"v_mfma_f32_16x16x32_bf16 v[100:103], a[48:51], a[120:123], v[100:103]\n"
"s_sub_u32 s67, 1603712, s75\n"
"v_mfma_f32_16x16x32_bf16 v[104:107], a[48:51], a[128:131], v[104:107]\n"
"v_mfma_f32_16x16x32_bf16 v[104:107], a[52:55], a[132:135], v[104:107]\n"
"v_mfma_f32_16x16x32_bf16 v[132:135], a[60:63], a[132:135], v[132:135]\n"
"buffer_load_dwordx4 v11, s[52:55], s67 offen offset:2048  lds\n"
"v_mfma_f32_16x16x32_bf16 v[132:135], a[56:59], a[128:131], v[132:135]\n"
"s_mov_b32 s29, s76\n"
"v_mfma_f32_16x16x32_bf16 v[136:139], a[56:59], a[136:139], v[136:139]\n"
"s_sub_u32 s67, 1619072, s75\n"
"v_mfma_f32_16x16x32_bf16 v[136:139], a[60:63], a[140:143], v[136:139]\n"
"buffer_load_dwordx4 v11, s[52:55], s67 offen offset:3072  lds\n"
"v_mfma_f32_16x16x32_bf16 v[108:111], a[52:55], a[140:143], v[108:111]\n"
"s_sub_u32 s99, s99, s98\n"
"s_sub_u32 s99, s99, s98\n"
"v_mfma_f32_16x16x32_bf16 v[108:111], a[48:51], a[136:139], v[108:111]\n"
"s_sub_u32 s99, s99, s98\n"
"s_add_u32 m0, s64, 4096\n"
"v_mfma_f32_16x16x32_bf16 v[112:115], a[48:51], a[144:147], v[112:115]\n"
"s_sub_u32 s99, s99, s98\n"
"v_mfma_f32_16x16x32_bf16 v[112:115], a[52:55], a[148:151], v[112:115]\n"
"s_sub_u32 s67, 1638528, s75\n"
"v_mfma_f32_16x16x32_bf16 v[140:143], a[60:63], a[148:151], v[140:143]\n"
"s_sub_u32 s99, s99, s98\n"
"s_sub_u32 s99, s99, s98\n"
"v_mfma_f32_16x16x32_bf16 v[140:143], a[56:59], a[144:147], v[140:143]\n"
"s_sub_u32 s99, s99, s98\n"
"buffer_load_dwordx4 v11, s[52:55], s67 offen offset:0  lds\n"
"v_mfma_f32_16x16x32_bf16 a[12:15], a[56:59], a[152:155], a[12:15]\n"
"s_sub_u32 s99, s99, s98\n"
"v_mfma_f32_16x16x32_bf16 a[12:15], a[60:63], a[156:159], a[12:15]\n"
"s_sub_u32 s67, 1653888, s75\n"
"v_mfma_f32_16x16x32_bf16 a[8:11], a[52:55], a[156:159], a[8:11]\n"
"s_sub_u32 s99, s99, s98\n"
"s_sub_u32 s99, s99, s98\n"
"v_mfma_f32_16x16x32_bf16 a[8:11], a[48:51], a[152:155], a[8:11]\n"
"s_sub_u32 s99, s99, s98\n"
"v_mfma_f32_16x16x32_bf16 v[144:147], a[64:67], a[96:99], v[144:147]\n"
"s_sub_u32 s99, s99, s98\n"
"v_mfma_f32_16x16x32_bf16 v[144:147], a[68:71], a[100:103], v[144:147]\n"
"buffer_load_dwordx4 v11, s[52:55], s67 offen offset:1024  lds\n"
"v_mfma_f32_16x16x32_bf16 v[172:175], a[76:79], a[100:103], v[172:175]\n"
"s_sub_u32 s99, s99, s98\n"
"s_sub_u32 s99, s99, s98\n"
"v_mfma_f32_16x16x32_bf16 v[172:175], a[72:75], a[96:99], v[172:175]\n"
"s_sub_u32 s99, s99, s98\n"
"s_sub_u32 s67, 1669248, s75\n"
"v_mfma_f32_16x16x32_bf16 v[176:179], a[72:75], a[104:107], v[176:179]\n"
"s_sub_u32 s99, s99, s98\n"
"v_mfma_f32_16x16x32_bf16 v[176:179], a[76:79], a[108:111], v[176:179]\n"
"buffer_load_dwordx4 v11, s[52:55], s67 offen offset:2048  lds\n"
"v_mfma_f32_16x16x32_bf16 v[148:151], a[68:71], a[108:111], v[148:151]\n"
"s_sub_u32 s99, s99, s98\n"
"s_sub_u32 s99, s99, s98\n"
"v_mfma_f32_16x16x32_bf16 v[148:151], a[64:67], a[104:107], v[148:151]\n"
"s_sub_u32 s99, s99, s98\n"
"s_sub_u32 s71, s64, s64\n"
"s_sub_u32 s67, 1684608, s75\n"
"v_mfma_f32_16x16x32_bf16 v[152:155], a[64:67], a[112:115], v[152:155]\n"
"s_sub_u32 s66, 81920, s73\n"
"s_sub_u32 s99, s99, s98\n"
"v_mfma_f32_16x16x32_bf16 v[152:155], a[68:71], a[116:119], v[152:155]\n"
"s_add_u32 s71, s71, 16384\n"
"buffer_load_dwordx4 v11, s[52:55], s67 offen offset:3072  lds\n"
"v_mfma_f32_16x16x32_bf16 v[180:183], a[76:79], a[116:119], v[180:183]\n"
"s_sub_u32 s99, s99, s98\n"
"v_add_u32 v12, s66, v7\n"
"s_sub_u32 s99, s99, s98\n"
"v_mfma_f32_16x16x32_bf16 v[180:183], a[72:75], a[112:115], v[180:183]\n"
"s_sub_u32 s99, s99, s98\n"
"s_cmp_lt_u32 %16, 2\n"
"s_cselect_b32 s67, 16384, 0\n"
"s_add_u32 s67, s67, s72\n"
"v_mfma_f32_16x16x32_bf16 v[184:187], a[72:75], a[120:123], v[184:187]\n"
"s_sub_u32 s99, s99, s98\n"
"v_add_u32 v13, s66, v8\n"
"s_add_u32 m0, s67, 139264\n"
"v_mfma_f32_16x16x32_bf16 v[184:187], a[76:79], a[124:127], v[184:187]\n"
"s_memtime s[76:77]\n"
"v_add_u32 v20, s71, v9\n"
"s_sub_u32 s67, 1792, s60\n"
"v_mfma_f32_16x16x32_bf16 v[156:159], a[68:71], a[124:127], v[156:159]\n"
"buffer_load_dwordx4 v6, s[48:51], s67 offen offset:0  lds\n"
"v_mfma_f32_16x16x32_bf16 v[156:159], a[64:67], a[120:123], v[156:159]\n"
"s_waitcnt lgkmcnt(0)\n"
"v_add_u32 v21, s71, v15\n"
"v_mfma_f32_16x16x32_bf16 v[160:163], a[64:67], a[128:131], v[160:163]\n"
"s_sub_u32 s29, s76, s29\n"
"s_sub_u32 s67, 17152, s60\n"
"v_mfma_f32_16x16x32_bf16 v[160:163], a[68:71], a[132:135], v[160:163]\n"
"s_cmp_lt_u32 s29, 620\n"
"s_cselect_b32 s99, s99, 0\n"
"v_mfma_f32_16x16x32_bf16 v[188:191], a[76:79], a[132:135], v[188:191]\n"
"s_max_u32 s31, s31, s99\n"
"buffer_load_dwordx4 v6, s[48:51], s67 offen offset:1024  lds\n"
"v_mfma_f32_16x16x32_bf16 v[188:191], a[72:75], a[128:131], v[188:191]\n"
"s_waitcnt vmcnt(18)\n"
"ds_read_b64_tr_b16 a[176:177], v20 offset:0\n"
"v_mfma_f32_16x16x32_bf16 v[192:195], a[72:75], a[136:139], v[192:195]\n"
"s_sub_u32 s67, 32512, s60\n"
"ds_read_b64_tr_b16 a[178:179], v20 offset:512\n"
"v_mfma_f32_16x16x32_bf16 v[192:195], a[76:79], a[140:143], v[192:195]\n"
"ds_read_b64_tr_b16 a[180:181], v20 offset:8\n"
"v_mfma_f32_16x16x32_bf16 v[164:167], a[68:71], a[140:143], v[164:167]\n"
"ds_read_b64_tr_b16 a[182:183], v20 offset:520\n"
"v_mfma_f32_16x16x32_bf16 v[164:167], a[64:67], a[136:139], v[164:167]\n"
"buffer_load_dwordx4 v6, s[48:51], s67 offen offset:2048  lds\n"
"v_mfma_f32_16x16x32_bf16 v[168:171], a[64:67], a[144:147], v[168:171]\n"
"ds_read_b64_tr_b16 a[184:185], v21 offset:0\n"
"v_mfma_f32_16x16x32_bf16 v[168:171], a[68:71], a[148:151], v[168:171]\n"
"s_sub_u32 s67, 47872, s60\n"
"ds_read_b64_tr_b16 a[186:187], v21 offset:512\n"
"v_mfma_f32_16x16x32_bf16 v[196:199], a[76:79], a[148:151], v[196:199]\n"
"ds_read_b64_tr_b16 a[188:189], v21 offset:8\n"
"v_mfma_f32_16x16x32_bf16 v[196:199], a[72:75], a[144:147], v[196:199]\n"
"buffer_load_dwordx4 v6, s[48:51], s67 offen offset:3072  lds\n"
"v_mfma_f32_16x16x32_bf16 a[20:23], a[72:75], a[152:155], a[20:23]\n"
"ds_read_b64_tr_b16 a[190:191], v21 offset:520\n"
"v_mfma_f32_16x16x32_bf16 a[20:23], a[76:79], a[156:159], a[20:23]\n"
"s_add_u32 m0, s64, 20352\n"
"v_mfma_f32_16x16x32_bf16 a[16:19], a[68:71], a[156:159], a[16:19]\n"
"v_mfma_f32_16x16x32_bf16 a[16:19], a[64:67], a[152:155], a[16:19]\n"
"s_barrier\n"
"s_waitcnt vmcnt(12)\n"
"ds_read_b64_tr_b16 a[32:33], v9 offset:8192\n"
"v_mfma_f32_16x16x32_bf16 v[200:203], a[80:83], a[96:99], v[200:203]\n"
"ds_read_b64_tr_b16 a[34:35], v9 offset:8704\n"
"buffer_load_dwordx4 v6, s[40:43], s88 offen offset:128  lds\n"
"v_mfma_f32_16x16x32_bf16 v[200:203], a[84:87], a[100:103], v[200:203]\n"
"ds_read_b64_tr_b16 a[36:37], v9 offset:12288\n"
"v_mfma_f32_16x16x32_bf16 v[228:231], a[92:95], a[100:103], v[228:231]\n"
"s_waitcnt lgkmcnt(3)\n"
"ds_read_b128 a[100:103], v13 offset:32768\n"
"v_mfma_f32_16x16x32_bf16 v[228:231], a[88:91], a[96:99], v[228:231]\n"
"ds_read_b128 a[96:99], v12 offset:32768\n"
"buffer_load_dwordx4 v6, s[40:43], s89 offen offset:1152  lds\n"
"v_mfma_f32_16x16x32_bf16 v[232:235], a[88:91], a[104:107], v[232:235]\n"
"ds_read_b64_tr_b16 a[38:39], v9 offset:12800\n"
"v_mfma_f32_16x16x32_bf16 v[232:235], a[92:95], a[108:111], v[232:235]\n"
"ds_read_b64_tr_b16 a[40:41], v9 offset:8200\n"
"v_mfma_f32_16x16x32_bf16 v[204:207], a[84:87], a[108:111], v[204:207]\n"
"ds_read_b128 a[108:111], v13 offset:34816\n"
"v_mfma_f32_16x16x32_bf16 v[204:207], a[80:83], a[104:107], v[204:207]\n"
"ds_read_b128 a[104:107], v12 offset:34816\n"
"v_mfma_f32_16x16x32_bf16 v[208:211], a[80:83], a[112:115], v[208:211]\n"
"ds_read_b64_tr_b16 a[42:43], v9 offset:8712\n"
"buffer_load_dwordx4 v6, s[40:43], s90 offen offset:2176  lds\n"
"v_mfma_f32_16x16x32_bf16 v[208:211], a[84:87], a[116:119], v[208:211]\n"
"ds_read_b64_tr_b16 a[44:45], v9 offset:12296\n"
"v_mfma_f32_16x16x32_bf16 v[236:239], a[92:95], a[116:119], v[236:239]\n"
"ds_read_b128 a[116:119], v13 offset:36864\n"
"v_mfma_f32_16x16x32_bf16 v[236:239], a[88:91], a[112:115], v[236:239]\n"
"ds_read_b128 a[112:115], v12 offset:36864\n"
"v_mfma_f32_16x16x32_bf16 v[240:243], a[88:91], a[120:123], v[240:243]\n"
"ds_read_b64_tr_b16 a[46:47], v9 offset:12808\n"
"v_mfma_f32_16x16x32_bf16 v[240:243], a[92:95], a[124:127], v[240:243]\n"
"ds_read_b64_tr_b16 a[48:49], v15 offset:8192\n"
"buffer_load_dwordx4 v6, s[40:43], s91 offen offset:3200  lds\n"
"v_mfma_f32_16x16x32_bf16 v[212:215], a[84:87], a[124:127], v[212:215]\n"
"ds_read_b128 a[124:127], v13 offset:38912\n"
"v_mfma_f32_16x16x32_bf16 v[212:215], a[80:83], a[120:123], v[212:215]\n"
"ds_read_b128 a[120:123], v12 offset:38912\n"
"v_mfma_f32_16x16x32_bf16 v[216:219], a[80:83], a[128:131], v[216:219]\n"
"ds_read_b64_tr_b16 a[50:51], v15 offset:8704\n"
"v_mfma_f32_16x16x32_bf16 v[216:219], a[84:87], a[132:135], v[216:219]\n"
"buffer_store_dwordx4 a[164:167], v0, s[36:39], %23 offen offset:2048 sc1\n"
"ds_read_b64_tr_b16 a[52:53], v15 offset:12288\n"
"v_mfma_f32_16x16x32_bf16 v[244:247], a[92:95], a[132:135], v[244:247]\n"
"ds_read_b128 a[132:135], v13 offset:24576\n"
"v_mfma_f32_16x16x32_bf16 v[244:247], a[88:91], a[128:131], v[244:247]\n"
"ds_read_b128 a[128:131], v12 offset:24576\n"
"v_mfma_f32_16x16x32_bf16 v[248:251], a[88:91], a[136:139], v[248:251]\n"
"ds_read_b64_tr_b16 a[54:55], v15 offset:12800\n"
"v_mfma_f32_16x16x32_bf16 v[248:251], a[92:95], a[140:143], v[248:251]\n"
"s_add_u32 m0, s64, 8192\n"
"ds_read_b64_tr_b16 a[56:57], v15 offset:8200\n"
"v_mfma_f32_16x16x32_bf16 v[220:223], a[84:87], a[140:143], v[220:223]\n"
"ds_read_b128 a[140:143], v13 offset:26624\n"
"v_mfma_f32_16x16x32_bf16 v[220:223], a[80:83], a[136:139], v[220:223]\n"
"ds_read_b128 a[136:139], v12 offset:26624\n"
"v_mfma_f32_16x16x32_bf16 v[224:227], a[80:83], a[144:147], v[224:227]\n"
"ds_read_b64_tr_b16 a[58:59], v15 offset:8712\n"
"v_mfma_f32_16x16x32_bf16 v[224:227], a[84:87], a[148:151], v[224:227]\n"
"ds_read_b64_tr_b16 a[60:61], v15 offset:12296\n"
"v_mfma_f32_16x16x32_bf16 v[252:255], a[92:95], a[148:151], v[252:255]\n"
"ds_read_b128 a[148:151], v13 offset:28672\n"
"v_mfma_f32_16x16x32_bf16 v[252:255], a[88:91], a[144:147], v[252:255]\n"
"ds_read_b128 a[144:147], v12 offset:28672\n"
"s_sub_u32 s67, 1835008, s75\n"
"ds_read_b64_tr_b16 a[62:63], v15 offset:12808\n"
"v_mfma_f32_16x16x32_bf16 a[28:31], a[88:91], a[152:155], a[28:31]\n"
"v_mfma_f32_16x16x32_bf16 a[28:31], a[92:95], a[156:159], a[28:31]\n"
"v_mfma_f32_16x16x32_bf16 a[24:27], a[84:87], a[156:159], a[24:27]\n"
"ds_read_b128 a[156:159], v13 offset:30720\n"
"v_mfma_f32_16x16x32_bf16 a[24:27], a[80:83], a[152:155], a[24:27]\n"
"s_waitcnt lgkmcnt(15)\n"
"ds_read_b128 a[152:155], v12 offset:30720\n"
"v_mfma_f32_16x16x32_bf16 v[32:35], a[32:35], a[96:99], v[32:35]\n"
"v_mfma_f32_16x16x32_bf16 v[32:35], a[36:39], a[100:103], v[32:35]\n"
"s_waitcnt lgkmcnt(2)\n"
"v_mfma_f32_16x16x32_bf16 v[60:63], a[44:47], a[100:103], v[60:63]\n"
"buffer_load_dwordx4 v11, s[52:55], s67 offen offset:0  lds\n"
"v_mfma_f32_16x16x32_bf16 v[60:63], a[40:43], a[96:99], v[60:63]\n"
"v_mfma_f32_16x16x32_bf16 v[64:67], a[40:43], a[104:107], v[64:67]\n"
"s_sub_u32 s67, 1850368, s75\n"
"v_mfma_f32_16x16x32_bf16 v[64:67], a[44:47], a[108:111], v[64:67]\n"
"v_mfma_f32_16x16x32_bf16 v[36:39], a[36:39], a[108:111], v[36:39]\n"
"buffer_load_dwordx4 v11, s[52:55], s67 offen offset:1024  lds\n"
"v_mfma_f32_16x16x32_bf16 v[36:39], a[32:35], a[104:107], v[36:39]\n"
"v_mfma_f32_16x16x32_bf16 v[40:43], a[32:35], a[112:115], v[40:43]\n"
"s_sub_u32 s67, 1865728, s75\n"
"v_mfma_f32_16x16x32_bf16 v[40:43], a[36:39], a[116:119], v[40:43]\n"
"v_mfma_f32_16x16x32_bf16 v[68:71], a[44:47], a[116:119], v[68:71]\n"
"v_mfma_f32_16x16x32_bf16 v[68:71], a[40:43], a[112:115], v[68:71]\n"
"buffer_load_dwordx4 v11, s[52:55], s67 offen offset:2048  lds\n"
"v_mfma_f32_16x16x32_bf16 v[72:75], a[40:43], a[120:123], v[72:75]\n"
"v_mfma_f32_16x16x32_bf16 v[72:75], a[44:47], a[124:127], v[72:75]\n"
"s_sub_u32 s67, 1881088, s75\n"
"v_mfma_f32_16x16x32_bf16 v[44:47], a[36:39], a[124:127], v[44:47]\n"
"v_mfma_f32_16x16x32_bf16 v[44:47], a[32:35], a[120:123], v[44:47]\n"
"v_mfma_f32_16x16x32_bf16 v[48:51], a[32:35], a[128:131], v[48:51]\n"
"buffer_load_dwordx4 v11, s[52:55], s67 offen offset:3072  lds\n"
"v_mfma_f32_16x16x32_bf16 v[48:51], a[36:39], a[132:135], v[48:51]\n"
"s_add_u32 m0, s64, 12288\n"
"v_mfma_f32_16x16x32_bf16 v[76:79], a[44:47], a[132:135], v[76:79]\n"
"s_waitcnt vmcnt(13)\n"
"ds_read_b64_tr_b16 a[64:65], v9 offset:0\n"
"v_mfma_f32_16x16x32_bf16 v[76:79], a[40:43], a[128:131], v[76:79]\n"
"s_sub_u32 s67, 1900544, s75\n"
"ds_read_b64_tr_b16 a[66:67], v9 offset:512\n"
"v_mfma_f32_16x16x32_bf16 v[80:83], a[40:43], a[136:139], v[80:83]\n"
"ds_read_b64_tr_b16 a[68:69], v9 offset:4096\n"
"v_mfma_f32_16x16x32_bf16 v[80:83], a[44:47], a[140:143], v[80:83]\n"
"ds_read_b64_tr_b16 a[70:71], v9 offset:4608\n"
"buffer_load_dwordx4 v11, s[52:55], s67 offen offset:0  lds\n"
"v_mfma_f32_16x16x32_bf16 v[52:55], a[36:39], a[140:143], v[52:55]\n"
"s_sub_u32 s67, 1915904, s75\n"
"ds_read_b64_tr_b16 a[72:73], v9 offset:8\n"
"v_mfma_f32_16x16x32_bf16 v[52:55], a[32:35], a[136:139], v[52:55]\n"
"s_waitcnt lgkmcnt(5)\n"
"ds_read_b64_tr_b16 a[74:75], v9 offset:520\n"
"v_mfma_f32_16x16x32_bf16 v[56:59], a[32:35], a[144:147], v[56:59]\n"
"ds_read_b64_tr_b16 a[76:77], v9 offset:4104\n"
"v_mfma_f32_16x16x32_bf16 v[56:59], a[36:39], a[148:151], v[56:59]\n"
"ds_read_b64_tr_b16 a[78:79], v9 offset:4616\n"
"v_mfma_f32_16x16x32_bf16 v[84:87], a[44:47], a[148:151], v[84:87]\n"
"buffer_load_dwordx4 v11, s[52:55], s67 offen offset:1024  lds\n"
"v_mfma_f32_16x16x32_bf16 v[84:87], a[40:43], a[144:147], v[84:87]\n"
"ds_read_b64_tr_b16 a[80:81], v15 offset:0\n"
"v_mfma_f32_16x16x32_bf16 a[4:7], a[40:43], a[152:155], a[4:7]\n"
"s_sub_u32 s67, 1931264, s75\n"
"ds_read_b64_tr_b16 a[82:83], v15 offset:512\n"
"v_mfma_f32_16x16x32_bf16 a[4:7], a[44:47], a[156:159], a[4:7]\n"
"ds_read_b64_tr_b16 a[84:85], v15 offset:4096\n"
"v_mfma_f32_16x16x32_bf16 a[0:3], a[36:39], a[156:159], a[0:3]\n"
"ds_read_b64_tr_b16 a[86:87], v15 offset:4608\n"
"buffer_load_dwordx4 v11, s[52:55], s67 offen offset:2048  lds\n"
"v_mfma_f32_16x16x32_bf16 a[0:3], a[32:35], a[152:155], a[0:3]\n"
"ds_read_b64_tr_b16 a[88:89], v15 offset:8\n"
"v_mfma_f32_16x16x32_bf16 v[88:91], a[48:51], a[96:99], v[88:91]\n"
"s_sub_u32 s67, 1946624, s75\n"
"ds_read_b64_tr_b16 a[90:91], v15 offset:520\n"
"v_mfma_f32_16x16x32_bf16 v[88:91], a[52:55], a[100:103], v[88:91]\n"
"ds_read_b64_tr_b16 a[92:93], v15 offset:4104\n"
"v_mfma_f32_16x16x32_bf16 v[116:119], a[60:63], a[100:103], v[116:119]\n"
"ds_read_b64_tr_b16 a[94:95], v15 offset:4616\n"
"v_mfma_f32_16x16x32_bf16 v[116:119], a[56:59], a[96:99], v[116:119]\n"
"buffer_load_dwordx4 v11, s[52:55], s67 offen offset:3072  lds\n"
"v_mfma_f32_16x16x32_bf16 v[120:123], a[56:59], a[104:107], v[120:123]\n"
"v_mfma_f32_16x16x32_bf16 v[120:123], a[60:63], a[108:111], v[120:123]\n"
"s_add_u32 m0, s64, 16256\n"
"v_mfma_f32_16x16x32_bf16 v[92:95], a[52:55], a[108:111], v[92:95]\n"
"v_mfma_f32_16x16x32_bf16 v[92:95], a[48:51], a[104:107], v[92:95]\n"
"buffer_load_dwordx4 v6, s[40:43], s92 offen offset:128  lds\n"
"v_mfma_f32_16x16x32_bf16 v[96:99], a[48:51], a[112:115], v[96:99]\n"
"s_waitcnt lgkmcnt(0)\n"
"s_mov_b32 s99, 0\n"
"v_mfma_f32_16x16x32_bf16 v[96:99], a[52:55], a[116:119], v[96:99]\n"
"s_mov_b32 s98, 0\n"
"v_mfma_f32_16x16x32_bf16 v[124:127], a[60:63], a[116:119], v[124:127]\n"
"s_memtime s[76:77]\n"
"s_load_dword s98, s[96:97], 128 glc\n"
"v_mfma_f32_16x16x32_bf16 v[124:127], a[56:59], a[112:115], v[124:127]\n"
"buffer_load_dwordx4 v6, s[40:43], s93 offen offset:1152  lds\n"
"v_mfma_f32_16x16x32_bf16 v[128:131], a[56:59], a[120:123], v[128:131]\n"
"v_mfma_f32_16x16x32_bf16 v[128:131], a[60:63], a[124:127], v[128:131]\n"
"v_mfma_f32_16x16x32_bf16 v[100:103], a[52:55], a[124:127], v[100:103]\n"
"v_mfma_f32_16x16x32_bf16 v[100:103], a[48:51], a[120:123], v[100:103]\n"
"buffer_load_dwordx4 v6, s[40:43], s94 offen offset:2176  lds\n"
"v_mfma_f32_16x16x32_bf16 v[104:107], a[48:51], a[128:131], v[104:107]\n"
"v_mfma_f32_16x16x32_bf16 v[104:107], a[52:55], a[132:135], v[104:107]\n"
"v_mfma_f32_16x16x32_bf16 v[132:135], a[60:63], a[132:135], v[132:135]\n"
"v_mfma_f32_16x16x32_bf16 v[132:135], a[56:59], a[128:131], v[132:135]\n"
"s_mov_b32 s29, s76\n"
"v_mfma_f32_16x16x32_bf16 v[136:139], a[56:59], a[136:139], v[136:139]\n"
"buffer_load_dwordx4 v6, s[40:43], s95 offen offset:3200  lds\n"
"v_mfma_f32_16x16x32_bf16 v[136:139], a[60:63], a[140:143], v[136:139]\n"
"s_add_u32 m0, s64, 0\n"
"v_mfma_f32_16x16x32_bf16 v[108:111], a[52:55], a[140:143], v[108:111]\n"
"s_sub_u32 s99, s99, s98\n"
"s_sub_u32 s99, s99, s98\n"
"s_andn2_b32 s65, %25, 4096\n"
"v_mfma_f32_16x16x32_bf16 v[108:111], a[48:51], a[136:139], v[108:111]\n"
"s_sub_u32 s99, s99, s98\n"
"v_mfma_f32_16x16x32_bf16 v[112:115], a[48:51], a[144:147], v[112:115]\n"
"s_sub_u32 s99, s99, s98\n"
"s_sub_u32 s67, 1835136, s75\n"
"v_mfma_f32_16x16x32_bf16 v[112:115], a[52:55], a[148:151], v[112:115]\n"
"buffer_load_dwordx4 v11, s[52:55], s67 offen offset:0  lds\n"
"v_mfma_f32_16x16x32_bf16 v[140:143], a[60:63], a[148:151], v[140:143]\n"
"s_sub_u32 s99, s99, s98\n"
"s_sub_u32 s99, s99, s98\n"
"buffer_load_dwordx4 v[24:27], v0, s[56:59], s65 offen sc1\n"
"v_mfma_f32_16x16x32_bf16 v[140:143], a[56:59], a[144:147], v[140:143]\n"
"s_sub_u32 s99, s99, s98\n"
"s_sub_u32 s67, 1850496, s75\n"
"v_mfma_f32_16x16x32_bf16 a[12:15], a[56:59], a[152:155], a[12:15]\n"
"s_sub_u32 s99, s99, s98\n"
"v_mfma_f32_16x16x32_bf16 a[12:15], a[60:63], a[156:159], a[12:15]\n"
"buffer_load_dwordx4 v11, s[52:55], s67 offen offset:1024  lds\n"
"v_mfma_f32_16x16x32_bf16 a[8:11], a[52:55], a[156:159], a[8:11]\n"
"s_sub_u32 s99, s99, s98\n"
"s_sub_u32 s99, s99, s98\n"
"v_mfma_f32_16x16x32_bf16 a[8:11], a[48:51], a[152:155], a[8:11]\n"
"s_sub_u32 s99, s99, s98\n"
"s_sub_u32 s67, 1865856, s75\n"
"v_mfma_f32_16x16x32_bf16 v[144:147], a[64:67], a[96:99], v[144:147]\n"
"s_sub_u32 s99, s99, s98\n"
"v_mfma_f32_16x16x32_bf16 v[144:147], a[68:71], a[100:103], v[144:147]\n"
"buffer_load_dwordx4 v11, s[52:55], s67 offen offset:2048  lds\n"
"v_mfma_f32_16x16x32_bf16 v[172:175], a[76:79], a[100:103], v[172:175]\n"
"s_sub_u32 s99, s99, s98\n"
"s_sub_u32 s99, s99, s98\n"
"v_mfma_f32_16x16x32_bf16 v[172:175], a[72:75], a[96:99], v[172:175]\n"
"s_sub_u32 s99, s99, s98\n"
"s_sub_u32 s67, 1881216, s75\n"
"v_mfma_f32_16x16x32_bf16 v[176:179], a[72:75], a[104:107], v[176:179]\n"
"s_sub_u32 s99, s99, s98\n"
"v_mfma_f32_16x16x32_bf16 v[176:179], a[76:79], a[108:111], v[176:179]\n"
"v_mfma_f32_16x16x32_bf16 v[148:151], a[68:71], a[108:111], v[148:151]\n"
"s_sub_u32 s99, s99, s98\n"
"s_sub_u32 s99, s99, s98\n"
"v_mfma_f32_16x16x32_bf16 v[148:151], a[64:67], a[104:107], v[148:151]\n"
"s_sub_u32 s99, s99, s98\n"
"s_sub_u32 s71, s64, s64\n"
"buffer_load_dwordx4 v11, s[52:55], s67 offen offset:3072  lds\n"
"v_mfma_f32_16x16x32_bf16 v[152:155], a[64:67], a[112:115], v[152:155]\n"
"s_sub_u32 s66, 122880, s73\n"
"s_sub_u32 s99, s99, s98\n"
"s_add_u32 m0, s64, 4096\n"
"v_mfma_f32_16x16x32_bf16 v[152:155], a[68:71], a[116:119], v[152:155]\n"
"s_add_u32 s71, s71, 20480\n"
"s_sub_u32 s67, 1900672, s75\n"
"v_mfma_f32_16x16x32_bf16 v[180:183], a[76:79], a[116:119], v[180:183]\n"
"s_sub_u32 s99, s99, s98\n"
"v_add_u32 v12, s66, v7\n"
"s_sub_u32 s99, s99, s98\n"
"v_mfma_f32_16x16x32_bf16 v[180:183], a[72:75], a[112:115], v[180:183]\n"
"s_sub_u32 s99, s99, s98\n"
"buffer_load_dwordx4 v11, s[52:55], s67 offen offset:0  lds\n"
"v_mfma_f32_16x16x32_bf16 v[184:187], a[72:75], a[120:123], v[184:187]\n"
"s_sub_u32 s99, s99, s98\n"
"v_add_u32 v13, s66, v8\n"
"v_mfma_f32_16x16x32_bf16 v[184:187], a[76:79], a[124:127], v[184:187]\n"
"s_memtime s[76:77]\n"
"v_add_u32 v20, s71, v7\n"
"v_mfma_f32_16x16x32_bf16 v[156:159], a[68:71], a[124:127], v[156:159]\n"
"s_sub_u32 s67, 1916032, s75\n"
"v_mfma_f32_16x16x32_bf16 v[156:159], a[64:67], a[120:123], v[156:159]\n"
"s_waitcnt lgkmcnt(0)\n"
"v_add_u32 v21, s71, v8\n"
"v_mfma_f32_16x16x32_bf16 v[160:163], a[64:67], a[128:131], v[160:163]\n"
"s_sub_u32 s29, s76, s29\n"
"buffer_load_dwordx4 v11, s[52:55], s67 offen offset:1024  lds\n"
"v_mfma_f32_16x16x32_bf16 v[160:163], a[68:71], a[132:135], v[160:163]\n"
"s_cmp_lt_u32 s29, 620\n"
"s_cselect_b32 s99, s99, 0\n"
"v_mfma_f32_16x16x32_bf16 v[188:191], a[76:79], a[132:135], v[188:191]\n"
"s_max_u32 s30, s30, s99\n"
"s_sub_u32 s67, 1931392, s75\n"
"v_mfma_f32_16x16x32_bf16 v[188:191], a[72:75], a[128:131], v[188:191]\n"
"s_waitcnt vmcnt(20)\n"
"ds_read_b128 a[224:227], v20 offset:0\n"
"v_mfma_f32_16x16x32_bf16 v[192:195], a[72:75], a[136:139], v[192:195]\n"
"ds_read_b128 a[228:231], v21 offset:0\n"
"v_mfma_f32_16x16x32_bf16 v[192:195], a[76:79], a[140:143], v[192:195]\n"
"ds_read_b128 a[232:235], v20 offset:2048\n"
"v_mfma_f32_16x16x32_bf16 v[164:167], a[68:71], a[140:143], v[164:167]\n"
"buffer_load_dwordx4 v11, s[52:55], s67 offen offset:2048  lds\n"
"v_mfma_f32_16x16x32_bf16 v[164:167], a[64:67], a[136:139], v[164:167]\n"
"ds_read_b128 a[236:239], v21 offset:2048\n"
"v_mfma_f32_16x16x32_bf16 v[168:171], a[64:67], a[144:147], v[168:171]\n"
"s_sub_u32 s67, 1946752, s75\n"
"v_mfma_f32_16x16x32_bf16 v[168:171], a[68:71], a[148:151], v[168:171]\n"
"v_mfma_f32_16x16x32_bf16 v[196:199], a[76:79], a[148:151], v[196:199]\n"
"buffer_load_dwordx4 v11, s[52:55], s67 offen offset:3072  lds\n"
"v_mfma_f32_16x16x32_bf16 v[196:199], a[72:75], a[144:147], v[196:199]\n"
"v_mfma_f32_16x16x32_bf16 a[20:23], a[72:75], a[152:155], a[20:23]\n"
"v_mfma_f32_16x16x32_bf16 a[20:23], a[76:79], a[156:159], a[20:23]\n"
"s_waitcnt lgkmcnt(0)\n"
"v_mfma_f32_16x16x32_bf16 a[16:19], a[68:71], a[156:159], a[16:19]\n"
"v_mfma_f32_16x16x32_bf16 a[16:19], a[64:67], a[152:155], a[16:19]\n"
"s_barrier\n"
"s_waitcnt vmcnt(13)\n"
"ds_read_b64_tr_b16 a[32:33], v9 offset:8192\n"
"v_mfma_f32_16x16x32_bf16 v[200:203], a[80:83], a[96:99], v[200:203]\n"
"buffer_store_dwordx4 a[168:171], v1, s[36:39], %23 offen offset:0 sc1\n"
"ds_read_b64_tr_b16 a[34:35], v9 offset:8704\n"
"v_mfma_f32_16x16x32_bf16 v[200:203], a[84:87], a[100:103], v[200:203]\n"
"ds_read_b64_tr_b16 a[36:37], v9 offset:12288\n"
"v_mfma_f32_16x16x32_bf16 v[228:231], a[92:95], a[100:103], v[228:231]\n"
"ds_read_b128 a[100:103], v13 offset:32768\n"
"v_mfma_f32_16x16x32_bf16 v[228:231], a[88:91], a[96:99], v[228:231]\n"
"ds_read_b128 a[96:99], v12 offset:32768\n"
"v_mfma_f32_16x16x32_bf16 v[232:235], a[88:91], a[104:107], v[232:235]\n"
"ds_read_b64_tr_b16 a[38:39], v9 offset:12800\n"
"v_mfma_f32_16x16x32_bf16 v[232:235], a[92:95], a[108:111], v[232:235]\n"
"ds_read_b64_tr_b16 a[40:41], v9 offset:8200\n"
"v_mfma_f32_16x16x32_bf16 v[204:207], a[84:87], a[108:111], v[204:207]\n"
"ds_read_b128 a[108:111], v13 offset:34816\n"
"v_mfma_f32_16x16x32_bf16 v[204:207], a[80:83], a[104:107], v[204:207]\n"
"ds_read_b128 a[104:107], v12 offset:34816\n"
"v_mfma_f32_16x16x32_bf16 v[208:211], a[80:83], a[112:115], v[208:211]\n"
"ds_read_b64_tr_b16 a[42:43], v9 offset:8712\n"
"v_mfma_f32_16x16x32_bf16 v[208:211], a[84:87], a[116:119], v[208:211]\n"
"ds_read_b64_tr_b16 a[44:45], v9 offset:12296\n"
"v_mfma_f32_16x16x32_bf16 v[236:239], a[92:95], a[116:119], v[236:239]\n"
"ds_read_b128 a[116:119], v13 offset:36864\n"
"v_mfma_f32_16x16x32_bf16 v[236:239], a[88:91], a[112:115], v[236:239]\n"
"buffer_store_dwordx4 a[172:175], v1, s[36:39], %23 offen offset:2048 sc1\n"
"ds_read_b128 a[112:115], v12 offset:36864\n"
"s_getpc_b64 s[70:71]\n"
"L_c11_:\n"
"s_add_u32 s70, s70, L_P6-L_c11_\n"
"s_addc_u32 s71, s71, 0\n"
"v_mfma_f32_16x16x32_bf16 v[240:243], a[88:91], a[120:123], v[240:243]\n"
"s_lshr_b32 s66, %26, 3\n"
"ds_read_b64_tr_b16 a[46:47], v9 offset:12808\n"
"v_mfma_f32_16x16x32_bf16 v[240:243], a[92:95], a[124:127], v[240:243]\n"
"s_lshl2_add_u32 s66, s66, %16\n"
"ds_read_b64_tr_b16 a[48:49], v15 offset:8192\n"
"v_mfma_f32_16x16x32_bf16 v[212:215], a[84:87], a[124:127], v[212:215]\n"
"s_mul_i32 s66, s66, 256\n"
"ds_read_b128 a[124:127], v13 offset:38912\n"
"v_mfma_f32_16x16x32_bf16 v[212:215], a[80:83], a[120:123], v[212:215]\n"
"s_add_u32 s74, %23, 524288\n"
"ds_read_b128 a[120:123], v12 offset:38912\n"
"v_mfma_f32_16x16x32_bf16 v[216:219], a[80:83], a[128:131], v[216:219]\n"
"s_min_u32 s66, s66, L_headF-L_P6-256\n"
"ds_read_b64_tr_b16 a[50:51], v15 offset:8704\n"
"v_mfma_f32_16x16x32_bf16 v[216:219], a[84:87], a[132:135], v[216:219]\n"
"ds_read_b64_tr_b16 a[52:53], v15 offset:12288\n"
"v_mfma_f32_16x16x32_bf16 v[244:247], a[92:95], a[132:135], v[244:247]\n"
"ds_read_b128 a[132:135], v13 offset:24576\n"
"v_min_u32 v22, 16, v0\n"
"v_mfma_f32_16x16x32_bf16 v[244:247], a[88:91], a[128:131], v[244:247]\n"
"ds_read_b128 a[128:131], v12 offset:24576\n"
"v_mfma_f32_16x16x32_bf16 v[248:251], a[88:91], a[136:139], v[248:251]\n"
"ds_read_b64_tr_b16 a[54:55], v15 offset:12800\n"
"v_mfma_f32_16x16x32_bf16 v[248:251], a[92:95], a[140:143], v[248:251]\n"
"ds_read_b64_tr_b16 a[56:57], v15 offset:8200\n"
"v_lshl_add_u32 v22, v22, 3, s66\n"
"v_mfma_f32_16x16x32_bf16 v[220:223], a[84:87], a[140:143], v[220:223]\n"
"ds_read_b128 a[140:143], v13 offset:26624\n"
"v_mfma_f32_16x16x32_bf16 v[220:223], a[80:83], a[136:139], v[220:223]\n"
"ds_read_b128 a[136:139], v12 offset:26624\n"
"v_mfma_f32_16x16x32_bf16 v[224:227], a[80:83], a[144:147], v[224:227]\n"
"buffer_store_dwordx4 a[176:179], v0, s[36:39], s74 offen offset:0 sc1\n"
"ds_read_b64_tr_b16 a[58:59], v15 offset:8712\n"
"global_load_dword v23, v22, s[70:71]\n"
"v_mfma_f32_16x16x32_bf16 v[224:227], a[84:87], a[148:151], v[224:227]\n"
"ds_read_b64_tr_b16 a[60:61], v15 offset:12296\n"
"v_mfma_f32_16x16x32_bf16 v[252:255], a[92:95], a[148:151], v[252:255]\n"
"ds_read_b128 a[148:151], v13 offset:28672\n"
"v_mfma_f32_16x16x32_bf16 v[252:255], a[88:91], a[144:147], v[252:255]\n"
"ds_read_b128 a[144:147], v12 offset:28672\n"
"s_sub_u32 s71, s64, s64\n"
"ds_read_b64_tr_b16 a[62:63], v15 offset:12808\n"
"v_mfma_f32_16x16x32_bf16 a[28:31], a[88:91], a[152:155], a[28:31]\n"
"v_mfma_f32_16x16x32_bf16 a[28:31], a[92:95], a[156:159], a[28:31]\n"
"v_mfma_f32_16x16x32_bf16 a[24:27], a[84:87], a[156:159], a[24:27]\n"
"s_add_u32 s71, s71, 16384\n"
"ds_read_b128 a[156:159], v13 offset:30720\n"
"v_mfma_f32_16x16x32_bf16 a[24:27], a[80:83], a[152:155], a[24:27]\n"
"s_waitcnt lgkmcnt(15)\n"
"ds_read_b128 a[152:155], v12 offset:30720\n"
"v_mfma_f32_16x16x32_bf16 v[32:35], a[32:35], a[96:99], v[32:35]\n"
"v_add_u32 v20, s71, v7\n"
"v_mfma_f32_16x16x32_bf16 v[32:35], a[36:39], a[100:103], v[32:35]\n"
"s_waitcnt lgkmcnt(2)\n"
"v_mfma_f32_16x16x32_bf16 v[60:63], a[44:47], a[100:103], v[60:63]\n"
"v_mfma_f32_16x16x32_bf16 v[60:63], a[40:43], a[96:99], v[60:63]\n"
"v_add_u32 v21, s71, v8\n"
"v_mfma_f32_16x16x32_bf16 v[64:67], a[40:43], a[104:107], v[64:67]\n"
"v_mfma_f32_16x16x32_bf16 v[64:67], a[44:47], a[108:111], v[64:67]\n"
"buffer_store_dwordx4 a[180:183], v0, s[36:39], s74 offen offset:2048 sc1\n"
"v_mfma_f32_16x16x32_bf16 v[36:39], a[36:39], a[108:111], v[36:39]\n"
"s_waitcnt vmcnt(14)\n"
"ds_read_b128 a[240:243], v20 offset:0\n"
"v_mfma_f32_16x16x32_bf16 v[36:39], a[32:35], a[104:107], v[36:39]\n"
"v_mfma_f32_16x16x32_bf16 v[40:43], a[32:35], a[112:115], v[40:43]\n"
"ds_read_b128 a[244:247], v21 offset:0\n"
"v_mfma_f32_16x16x32_bf16 v[40:43], a[36:39], a[116:119], v[40:43]\n"
"ds_read_b128 a[248:251], v20 offset:2048\n"
"v_mfma_f32_16x16x32_bf16 v[68:71], a[44:47], a[116:119], v[68:71]\n"
"s_add_u32 s70, s78, 1\n"
"v_mfma_f32_16x16x32_bf16 v[68:71], a[40:43], a[112:115], v[68:71]\n"
"ds_read_b128 a[252:255], v21 offset:2048\n"
"v_mfma_f32_16x16x32_bf16 v[72:75], a[40:43], a[120:123], v[72:75]\n"
"v_mfma_f32_16x16x32_bf16 v[72:75], a[44:47], a[124:127], v[72:75]\n"
"v_mfma_f32_16x16x32_bf16 v[44:47], a[36:39], a[124:127], v[44:47]\n"
"v_mfma_f32_16x16x32_bf16 v[44:47], a[32:35], a[120:123], v[44:47]\n"
"v_mfma_f32_16x16x32_bf16 v[48:51], a[32:35], a[128:131], v[48:51]\n"
"v_mfma_f32_16x16x32_bf16 v[48:51], a[36:39], a[132:135], v[48:51]\n"
"s_waitcnt lgkmcnt(0)\n"
"buffer_store_dwordx4 a[184:187], v1, s[36:39], s74 offen offset:0 sc1\n"
"v_mfma_f32_16x16x32_bf16 v[76:79], a[44:47], a[132:135], v[76:79]\n"
"s_waitcnt vmcnt(6)\n"
"ds_read_b64_tr_b16 a[64:65], v9 offset:0\n"
"v_mfma_f32_16x16x32_bf16 v[76:79], a[40:43], a[128:131], v[76:79]\n"
"ds_read_b64_tr_b16 a[66:67], v9 offset:512\n"
"v_mfma_f32_16x16x32_bf16 v[80:83], a[40:43], a[136:139], v[80:83]\n"
"ds_read_b64_tr_b16 a[68:69], v9 offset:4096\n"
"v_mfma_f32_16x16x32_bf16 v[80:83], a[44:47], a[140:143], v[80:83]\n"
"ds_read_b64_tr_b16 a[70:71], v9 offset:4608\n"
"v_mfma_f32_16x16x32_bf16 v[52:55], a[36:39], a[140:143], v[52:55]\n"
"ds_read_b64_tr_b16 a[72:73], v9 offset:8\n"
"v_subrev_u32 v24, s70, v24\n"
"v_mfma_f32_16x16x32_bf16 v[52:55], a[32:35], a[136:139], v[52:55]\n"
"ds_read_b64_tr_b16 a[74:75], v9 offset:520\n"
"v_xor_b32 v25, s79, v25\n"
"v_mfma_f32_16x16x32_bf16 v[56:59], a[32:35], a[144:147], v[56:59]\n"
"ds_read_b64_tr_b16 a[76:77], v9 offset:4104\n"
"v_subrev_u32 v26, s70, v26\n"
"v_mfma_f32_16x16x32_bf16 v[56:59], a[36:39], a[148:151], v[56:59]\n"
"ds_read_b64_tr_b16 a[78:79], v9 offset:4616\n"
"v_xor_b32 v27, s79, v27\n"
"v_mfma_f32_16x16x32_bf16 v[84:87], a[44:47], a[148:151], v[84:87]\n"
"ds_read_b64_tr_b16 a[80:81], v15 offset:0\n"
"v_or_b32 v24, v24, v26\n"
"v_mfma_f32_16x16x32_bf16 v[84:87], a[40:43], a[144:147], v[84:87]\n"
"ds_read_b64_tr_b16 a[82:83], v15 offset:512\n"
"v_or_b32 v25, v25, v27\n"
"v_mfma_f32_16x16x32_bf16 a[4:7], a[40:43], a[152:155], a[4:7]\n"
"ds_read_b64_tr_b16 a[84:85], v15 offset:4096\n"
"v_or_b32 v24, v24, v25\n"
"v_mfma_f32_16x16x32_bf16 a[4:7], a[44:47], a[156:159], a[4:7]\n"
"ds_read_b64_tr_b16 a[86:87], v15 offset:4608\n"
"v_cmp_lt_u32 vcc, 15, v24\n"
"v_mfma_f32_16x16x32_bf16 a[0:3], a[36:39], a[156:159], a[0:3]\n"
"buffer_store_dwordx4 a[188:191], v1, s[36:39], s74 offen offset:2048 sc1\n"
"ds_read_b64_tr_b16 a[88:89], v15 offset:8\n"
"s_cbranch_vccnz L_slow3\n"
"L_back3:\n"
"v_mfma_f32_16x16x32_bf16 a[0:3], a[32:35], a[152:155], a[0:3]\n"
"ds_read_b64_tr_b16 a[90:91], v15 offset:520\n"
"v_mfma_f32_16x16x32_bf16 v[88:91], a[48:51], a[96:99], v[88:91]\n"
"ds_read_b64_tr_b16 a[92:93], v15 offset:4104\n"
"v_mfma_f32_16x16x32_bf16 v[88:91], a[52:55], a[100:103], v[88:91]\n"
"global_load_dwordx4 a[32:35], v0, s[34:35] offset:-4096 sc1\n"
"s_barrier\n"
"v_mfma_f32_16x16x32_bf16 v[116:119], a[60:63], a[100:103], v[116:119]\n"
"ds_read_b64_tr_b16 a[94:95], v15 offset:4616\n"
"v_mfma_f32_16x16x32_bf16 v[116:119], a[56:59], a[96:99], v[116:119]\n"
"ds_write_b64 v2, a[192:193] offset:16384\n"
"v_mfma_f32_16x16x32_bf16 v[120:123], a[56:59], a[104:107], v[120:123]\n"
"ds_write_b64 v2, a[194:195] offset:16392\n"
"v_mfma_f32_16x16x32_bf16 v[120:123], a[60:63], a[108:111], v[120:123]\n"
"ds_write_b64 v2, a[196:197] offset:17408\n"
"v_mfma_f32_16x16x32_bf16 v[92:95], a[52:55], a[108:111], v[92:95]\n"
"ds_read_b128 a[160:163], v7 offset:24576\n"
"v_mfma_f32_16x16x32_bf16 v[92:95], a[48:51], a[104:107], v[92:95]\n"
"ds_write_b64 v2, a[198:199] offset:17416\n"
"v_mfma_f32_16x16x32_bf16 v[96:99], a[48:51], a[112:115], v[96:99]\n"
"s_waitcnt lgkmcnt(5)\n"
"ds_read_b128 a[164:167], v8 offset:24576\n"
"v_mfma_f32_16x16x32_bf16 v[96:99], a[52:55], a[116:119], v[96:99]\n"
"global_load_dwordx4 a[36:39], v0, s[34:35] offset:-3072 sc1\n"
"v_mfma_f32_16x16x32_bf16 v[124:127], a[60:63], a[116:119], v[124:127]\n"
"ds_read_b128 a[168:171], v7 offset:26624\n"
"v_mfma_f32_16x16x32_bf16 v[124:127], a[56:59], a[112:115], v[124:127]\n"
"ds_read_b128 a[172:175], v8 offset:26624\n"
"v_mfma_f32_16x16x32_bf16 v[128:131], a[56:59], a[120:123], v[128:131]\n"
"ds_write_b64 v2, a[200:201] offset:18432\n"
"v_mfma_f32_16x16x32_bf16 v[128:131], a[60:63], a[124:127], v[128:131]\n"
"ds_read_b128 a[176:179], v7 offset:28672\n"
"v_mfma_f32_16x16x32_bf16 v[100:103], a[52:55], a[124:127], v[100:103]\n"
"ds_read_b128 a[180:183], v8 offset:28672\n"
"v_mfma_f32_16x16x32_bf16 v[100:103], a[48:51], a[120:123], v[100:103]\n"
"ds_write_b64 v2, a[202:203] offset:18440\n"
"v_mfma_f32_16x16x32_bf16 v[104:107], a[48:51], a[128:131], v[104:107]\n"
"ds_read_b128 a[184:187], v7 offset:30720\n"
"v_mfma_f32_16x16x32_bf16 v[104:107], a[52:55], a[132:135], v[104:107]\n"
"global_load_dwordx4 a[40:43], v0, s[34:35] offset:-2048 sc1\n"
"v_mfma_f32_16x16x32_bf16 v[132:135], a[60:63], a[132:135], v[132:135]\n"
"ds_read_b128 a[188:191], v8 offset:30720\n"
"v_mfma_f32_16x16x32_bf16 v[132:135], a[56:59], a[128:131], v[132:135]\n"
"ds_write_b64 v2, a[204:205] offset:19456\n"
"v_mfma_f32_16x16x32_bf16 v[136:139], a[56:59], a[136:139], v[136:139]\n"
"ds_write_b64 v2, a[206:207] offset:19464\n"
"v_mfma_f32_16x16x32_bf16 v[136:139], a[60:63], a[140:143], v[136:139]\n"
"ds_write_b64 v2, a[208:209] offset:20480\n"
"v_mfma_f32_16x16x32_bf16 v[108:111], a[52:55], a[140:143], v[108:111]\n"
"ds_write_b64 v2, a[210:211] offset:20488\n"
"v_mfma_f32_16x16x32_bf16 v[108:111], a[48:51], a[136:139], v[108:111]\n"
"ds_write_b64 v2, a[212:213] offset:21504\n"
"v_mfma_f32_16x16x32_bf16 v[112:115], a[48:51], a[144:147], v[112:115]\n"
"ds_write_b64 v2, a[214:215] offset:21512\n"
"v_mfma_f32_16x16x32_bf16 v[112:115], a[52:55], a[148:151], v[112:115]\n"
"global_load_dwordx4 a[44:47], v0, s[34:35] offset:-1024 sc1\n"
"v_mfma_f32_16x16x32_bf16 v[140:143], a[60:63], a[148:151], v[140:143]\n"
"ds_write_b64 v2, a[216:217] offset:22528\n"
"v_mfma_f32_16x16x32_bf16 v[140:143], a[56:59], a[144:147], v[140:143]\n"
"v_mfma_f32_16x16x32_bf16 a[12:15], a[56:59], a[152:155], a[12:15]\n"
"ds_write_b64 v2, a[218:219] offset:22536\n"
"v_mfma_f32_16x16x32_bf16 a[12:15], a[60:63], a[156:159], a[12:15]\n"
"v_mfma_f32_16x16x32_bf16 a[8:11], a[52:55], a[156:159], a[8:11]\n"
"ds_write_b64 v2, a[220:221] offset:23552\n"
"v_mfma_f32_16x16x32_bf16 a[8:11], a[48:51], a[152:155], a[8:11]\n"
"v_mfma_f32_16x16x32_bf16 v[144:147], a[64:67], a[96:99], v[144:147]\n"
"ds_write_b64 v2, a[222:223] offset:23560\n"
"v_mfma_f32_16x16x32_bf16 v[144:147], a[68:71], a[100:103], v[144:147]\n"
"global_load_dwordx4 a[48:51], v0, s[34:35] offset:0 sc1\n"
"v_mfma_f32_16x16x32_bf16 v[172:175], a[76:79], a[100:103], v[172:175]\n"
"ds_write_b64 v2, a[224:225] offset:24576\n"
"v_mfma_f32_16x16x32_bf16 v[172:175], a[72:75], a[96:99], v[172:175]\n"
"ds_write_b64 v2, a[226:227] offset:24584\n"
"v_mfma_f32_16x16x32_bf16 v[176:179], a[72:75], a[104:107], v[176:179]\n"
"v_mfma_f32_16x16x32_bf16 v[176:179], a[76:79], a[108:111], v[176:179]\n"
"ds_write_b64 v2, a[228:229] offset:25600\n"
"v_mfma_f32_16x16x32_bf16 v[148:151], a[68:71], a[108:111], v[148:151]\n"
"v_mfma_f32_16x16x32_bf16 v[148:151], a[64:67], a[104:107], v[148:151]\n"
"ds_write_b64 v2, a[230:231] offset:25608\n"
"v_mfma_f32_16x16x32_bf16 v[152:155], a[64:67], a[112:115], v[152:155]\n"
"ds_write_b64 v2, a[232:233] offset:26624\n"
"v_mfma_f32_16x16x32_bf16 v[152:155], a[68:71], a[116:119], v[152:155]\n"
"global_load_dwordx4 a[52:55], v0, s[34:35] offset:1024 sc1\n"
"v_mfma_f32_16x16x32_bf16 v[180:183], a[76:79], a[116:119], v[180:183]\n"
"v_mfma_f32_16x16x32_bf16 v[180:183], a[72:75], a[112:115], v[180:183]\n"
"ds_write_b64 v2, a[234:235] offset:26632\n"
"v_mfma_f32_16x16x32_bf16 v[184:187], a[72:75], a[120:123], v[184:187]\n"
"v_mfma_f32_16x16x32_bf16 v[184:187], a[76:79], a[124:127], v[184:187]\n"
"ds_write_b64 v2, a[236:237] offset:27648\n"
"v_mfma_f32_16x16x32_bf16 v[156:159], a[68:71], a[124:127], v[156:159]\n"
"v_mfma_f32_16x16x32_bf16 v[156:159], a[64:67], a[120:123], v[156:159]\n"
"ds_write_b64 v2, a[238:239] offset:27656\n"
"v_mfma_f32_16x16x32_bf16 v[160:163], a[64:67], a[128:131], v[160:163]\n"
"ds_write_b64 v2, a[240:241] offset:28672\n"
"v_mfma_f32_16x16x32_bf16 v[160:163], a[68:71], a[132:135], v[160:163]\n"
"global_load_dwordx4 a[56:59], v0, s[34:35] offset:2048 sc1\n"
"v_mfma_f32_16x16x32_bf16 v[188:191], a[76:79], a[132:135], v[188:191]\n"
"ds_write_b64 v2, a[242:243] offset:28680\n"
"v_mfma_f32_16x16x32_bf16 v[188:191], a[72:75], a[128:131], v[188:191]\n"
"v_mfma_f32_16x16x32_bf16 v[192:195], a[72:75], a[136:139], v[192:195]\n"
"ds_write_b64 v2, a[244:245] offset:29696\n"
"v_mfma_f32_16x16x32_bf16 v[192:195], a[76:79], a[140:143], v[192:195]\n"
"v_mfma_f32_16x16x32_bf16 v[164:167], a[68:71], a[140:143], v[164:167]\n"
"ds_write_b64 v2, a[246:247] offset:29704\n"
"v_mfma_f32_16x16x32_bf16 v[164:167], a[64:67], a[136:139], v[164:167]\n"
"s_add_u32 s74, s78, 2\n"
"v_mfma_f32_16x16x32_bf16 v[168:171], a[64:67], a[144:147], v[168:171]\n"
"ds_write_b64 v2, a[248:249] offset:30720\n"
"v_mfma_f32_16x16x32_bf16 v[168:171], a[68:71], a[148:151], v[168:171]\n"
"global_load_dwordx4 a[60:63], v0, s[34:35] offset:3072 sc1\n"
"v_mov_b32 v28, s74\n"
"v_mfma_f32_16x16x32_bf16 v[196:199], a[76:79], a[148:151], v[196:199]\n"
"v_mov_b32 v29, s79\n"
"v_mfma_f32_16x16x32_bf16 v[196:199], a[72:75], a[144:147], v[196:199]\n"
"ds_write_b64 v2, a[250:251] offset:30728\n"
"v_mfma_f32_16x16x32_bf16 a[20:23], a[72:75], a[152:155], a[20:23]\n"
"s_waitcnt vmcnt(8)\n"
"buffer_store_dwordx2 v[28:29], off, s[56:59], %24 sc1\n"
"s_add_u32 s34, s34, 8192\n"
"v_mfma_f32_16x16x32_bf16 a[20:23], a[76:79], a[156:159], a[20:23]\n"
"ds_write_b64 v2, a[252:253] offset:31744\n"
"v_mfma_f32_16x16x32_bf16 a[16:19], a[68:71], a[156:159], a[16:19]\n"
"ds_write_b64 v2, a[254:255] offset:31752\n"
"v_mfma_f32_16x16x32_bf16 a[16:19], a[64:67], a[152:155], a[16:19]\n"
"v_mfma_f32_16x16x32_bf16 v[200:203], a[80:83], a[96:99], v[200:203]\n"
"v_lshrrev_b32 v16, 4, v0\n"
"v_mfma_f32_16x16x32_bf16 v[200:203], a[84:87], a[100:103], v[200:203]\n"
"global_load_dwordx4 a[64:67], v0, s[34:35] offset:-4096 sc1\n"
"v_mfma_f32_16x16x32_bf16 v[228:231], a[92:95], a[100:103], v[228:231]\n"
"ds_read_b128 a[100:103], v2 offset:17408\n"
"v_mfma_f32_16x16x32_bf16 v[228:231], a[88:91], a[96:99], v[228:231]\n"
"ds_read_b128 a[96:99], v2 offset:16384\n"
"v_mfma_f32_16x16x32_bf16 v[232:235], a[88:91], a[104:107], v[232:235]\n"
"v_and_b32 v16, 0x30, v16\n"
"v_mfma_f32_16x16x32_bf16 v[232:235], a[92:95], a[108:111], v[232:235]\n"
"buffer_load_dwordx2 v[18:19], v16, s[44:47], s61 offen offset:128\n"
"v_mfma_f32_16x16x32_bf16 v[204:207], a[84:87], a[108:111], v[204:207]\n"
"ds_read_b128 a[108:111], v2 offset:19456\n"
"v_mfma_f32_16x16x32_bf16 v[204:207], a[80:83], a[104:107], v[204:207]\n"
"ds_read_b128 a[104:107], v2 offset:18432\n"
"v_mfma_f32_16x16x32_bf16 v[208:211], a[80:83], a[112:115], v[208:211]\n"
"v_lshrrev_b32 v20, 4, v0\n"
"v_mfma_f32_16x16x32_bf16 v[208:211], a[84:87], a[116:119], v[208:211]\n"
"global_load_dwordx4 a[68:71], v0, s[34:35] offset:-3072 sc1\n"
"v_mfma_f32_16x16x32_bf16 v[236:239], a[92:95], a[116:119], v[236:239]\n"
"ds_read_b128 a[116:119], v2 offset:21504\n"
"v_mfma_f32_16x16x32_bf16 v[236:239], a[88:91], a[112:115], v[236:239]\n"
"ds_read_b128 a[112:115], v2 offset:20480\n"
"v_mfma_f32_16x16x32_bf16 v[240:243], a[88:91], a[120:123], v[240:243]\n"
"v_and_b32 v20, 0x30, v20\n"
"v_mfma_f32_16x16x32_bf16 v[240:243], a[92:95], a[124:127], v[240:243]\n"
"buffer_load_dwordx2 v[22:23], v20, s[44:47], s61 offen offset:136\n"
"v_mfma_f32_16x16x32_bf16 v[212:215], a[84:87], a[124:127], v[212:215]\n"
"ds_read_b128 a[124:127], v2 offset:23552\n"
"v_mfma_f32_16x16x32_bf16 v[212:215], a[80:83], a[120:123], v[212:215]\n"
"ds_read_b128 a[120:123], v2 offset:22528\n"
"v_mfma_f32_16x16x32_bf16 v[216:219], a[80:83], a[128:131], v[216:219]\n"
"v_lshrrev_b32 v24, 4, v0\n"
"v_mfma_f32_16x16x32_bf16 v[216:219], a[84:87], a[132:135], v[216:219]\n"
"global_load_dwordx4 a[72:75], v0, s[34:35] offset:-2048 sc1\n"
"v_mfma_f32_16x16x32_bf16 v[244:247], a[92:95], a[132:135], v[244:247]\n"
"ds_read_b128 a[132:135], v2 offset:25600\n"
"v_and_b32 v24, 0x30, v24\n"
"v_mfma_f32_16x16x32_bf16 v[244:247], a[88:91], a[128:131], v[244:247]\n"
"ds_read_b128 a[128:131], v2 offset:24576\n"
"v_mfma_f32_16x16x32_bf16 v[248:251], a[88:91], a[136:139], v[248:251]\n"
"buffer_load_dwordx2 v[26:27], v24, s[44:47], s61 offen offset:192\n"
"v_mfma_f32_16x16x32_bf16 v[248:251], a[92:95], a[140:143], v[248:251]\n"
"v_lshrrev_b32 v28, 4, v0\n"
"v_mfma_f32_16x16x32_bf16 v[220:223], a[84:87], a[140:143], v[220:223]\n"
"ds_read_b128 a[140:143], v2 offset:27648\n"
"v_mfma_f32_16x16x32_bf16 v[220:223], a[80:83], a[136:139], v[220:223]\n"
"ds_read_b128 a[136:139], v2 offset:26624\n"
"v_mfma_f32_16x16x32_bf16 v[224:227], a[80:83], a[144:147], v[224:227]\n"
"v_and_b32 v28, 0x30, v28\n"
"v_mfma_f32_16x16x32_bf16 v[224:227], a[84:87], a[148:151], v[224:227]\n"
"global_load_dwordx4 a[76:79], v0, s[34:35] offset:-1024 sc1\n"
"v_mfma_f32_16x16x32_bf16 v[252:255], a[92:95], a[148:151], v[252:255]\n"
"ds_read_b128 a[148:151], v2 offset:29696\n"
"v_mfma_f32_16x16x32_bf16 v[252:255], a[88:91], a[144:147], v[252:255]\n"
"ds_read_b128 a[144:147], v2 offset:28672\n"
"v_mfma_f32_16x16x32_bf16 a[28:31], a[88:91], a[152:155], a[28:31]\n"
"buffer_load_dwordx2 v[30:31], v28, s[44:47], s61 offen offset:200\n"
"v_mfma_f32_16x16x32_bf16 a[28:31], a[92:95], a[156:159], a[28:31]\n"
"v_mfma_f32_16x16x32_bf16 a[24:27], a[84:87], a[156:159], a[24:27]\n"
"ds_read_b128 a[156:159], v2 offset:31744\n"
"v_mfma_f32_16x16x32_bf16 a[24:27], a[80:83], a[152:155], a[24:27]\n"
"s_waitcnt vmcnt(13) lgkmcnt(11)\n"
"ds_read_b128 a[152:155], v2 offset:30720\n"
"v_mfma_f32_16x16x32_bf16 v[32:35], a[32:35], a[96:99], v[32:35]\n"
"v_mfma_f32_16x16x32_bf16 v[32:35], a[36:39], a[100:103], v[32:35]\n"
"global_load_dwordx4 a[80:83], v0, s[34:35] offset:0 sc1\n"
"v_mfma_f32_16x16x32_bf16 v[60:63], a[44:47], a[100:103], v[60:63]\n"
"s_barrier\n"
"v_mfma_f32_16x16x32_bf16 v[60:63], a[40:43], a[96:99], v[60:63]\n"
"v_mfma_f32_16x16x32_bf16 v[64:67], a[40:43], a[104:107], v[64:67]\n"
"v_mfma_f32_16x16x32_bf16 v[64:67], a[44:47], a[108:111], v[64:67]\n"
"v_mfma_f32_16x16x32_bf16 v[36:39], a[36:39], a[108:111], v[36:39]\n"
"v_mfma_f32_16x16x32_bf16 v[36:39], a[32:35], a[104:107], v[36:39]\n"
"s_waitcnt lgkmcnt(8)\n"
"v_mfma_f32_16x16x32_bf16 v[40:43], a[32:35], a[112:115], v[40:43]\n"
"v_mfma_f32_16x16x32_bf16 v[40:43], a[36:39], a[116:119], v[40:43]\n"
"global_load_dwordx4 a[84:87], v0, s[34:35] offset:1024 sc1\n"
"v_mfma_f32_16x16x32_bf16 v[68:71], a[44:47], a[116:119], v[68:71]\n"
"v_mfma_f32_16x16x32_bf16 v[68:71], a[40:43], a[112:115], v[68:71]\n"
"v_mfma_f32_16x16x32_bf16 v[72:75], a[40:43], a[120:123], v[72:75]\n"
"v_mfma_f32_16x16x32_bf16 v[72:75], a[44:47], a[124:127], v[72:75]\n"
"s_add_u32 s61, s61, 2048\n"
"v_mfma_f32_16x16x32_bf16 v[44:47], a[36:39], a[124:127], v[44:47]\n"
"v_mfma_f32_16x16x32_bf16 v[44:47], a[32:35], a[120:123], v[44:47]\n"
"s_waitcnt lgkmcnt(4)\n"
"v_mfma_f32_16x16x32_bf16 v[48:51], a[32:35], a[128:131], v[48:51]\n"
"s_waitcnt vmcnt(8)\n"
"v_lshlrev_b32 v16, 16, v18\n"
"v_mfma_f32_16x16x32_bf16 v[48:51], a[36:39], a[132:135], v[48:51]\n"
"global_load_dwordx4 a[88:91], v0, s[34:35] offset:2048 sc1\n"
"v_and_b32 v17, s63, v18\n"
"v_mfma_f32_16x16x32_bf16 v[76:79], a[44:47], a[132:135], v[76:79]\n"
"v_lshlrev_b32 v18, 16, v19\n"
"v_and_b32 v19, s63, v19\n"
"v_mfma_f32_16x16x32_bf16 v[76:79], a[40:43], a[128:131], v[76:79]\n"
"v_add_f32 v144, v144, v16\n"
"v_mfma_f32_16x16x32_bf16 v[80:83], a[40:43], a[136:139], v[80:83]\n"
"v_add_f32 v145, v145, v17\n"
"v_add_f32 v146, v146, v18\n"
"v_mfma_f32_16x16x32_bf16 v[80:83], a[44:47], a[140:143], v[80:83]\n"
"v_add_f32 v147, v147, v19\n"
"v_mfma_f32_16x16x32_bf16 v[52:55], a[36:39], a[140:143], v[52:55]\n"
"v_add_f32 v148, v148, v16\n"
"v_mfma_f32_16x16x32_bf16 v[52:55], a[32:35], a[136:139], v[52:55]\n"
"s_mov_b32 s99, 0\n"
"v_add_f32 v149, v149, v17\n"
"s_waitcnt lgkmcnt(0)\n"
"v_mfma_f32_16x16x32_bf16 v[56:59], a[32:35], a[144:147], v[56:59]\n"
"s_mov_b32 s98, 0\n"
"v_add_f32 v150, v150, v18\n"
"v_add_f32 v151, v151, v19\n"
"v_mfma_f32_16x16x32_bf16 v[56:59], a[36:39], a[148:151], v[56:59]\n"
"global_load_dwordx4 a[92:95], v0, s[34:35] offset:3072 sc1\n"
"s_memtime s[76:77]\n"
"s_load_dword s98, s[96:97], 8448 glc\n"
"v_mfma_f32_16x16x32_bf16 v[84:87], a[44:47], a[148:151], v[84:87]\n"
"v_add_f32 v152, v152, v16\n"
"v_add_f32 v153, v153, v17\n"
"v_mfma_f32_16x16x32_bf16 v[84:87], a[40:43], a[144:147], v[84:87]\n"
"v_add_f32 v154, v154, v18\n"
"v_add_f32 v155, v155, v19\n"
"v_mfma_f32_16x16x32_bf16 a[4:7], a[40:43], a[152:155], a[4:7]\n"
"s_add_u32 s34, s34, 8192\n"
"v_add_f32 v156, v156, v16\n"
"v_mfma_f32_16x16x32_bf16 a[4:7], a[44:47], a[156:159], a[4:7]\n"
"v_add_f32 v157, v157, v17\n"
"v_mfma_f32_16x16x32_bf16 a[0:3], a[36:39], a[156:159], a[0:3]\n"
"v_add_f32 v158, v158, v18\n"
"v_add_f32 v159, v159, v19\n"
"v_mfma_f32_16x16x32_bf16 a[0:3], a[32:35], a[152:155], a[0:3]\n"
"v_add_f32 v160, v160, v16\n"
"v_mfma_f32_16x16x32_bf16 v[88:91], a[48:51], a[96:99], v[88:91]\n"
"v_add_f32 v161, v161, v17\n"
"v_add_f32 v162, v162, v18\n"
"v_mfma_f32_16x16x32_bf16 v[88:91], a[52:55], a[100:103], v[88:91]\n"
"global_load_dwordx4 a[32:35], v0, s[34:35] offset:-4096 sc1\n"
"v_add_f32 v163, v163, v19\n"
"v_mfma_f32_16x16x32_bf16 v[116:119], a[60:63], a[100:103], v[116:119]\n"
"s_mov_b32 s29, s76\n"
"ds_read_b128 a[100:103], v3 offset:17408\n"
"v_add_f32 v164, v164, v16\n"
"v_mfma_f32_16x16x32_bf16 v[116:119], a[56:59], a[96:99], v[116:119]\n"
"ds_read_b128 a[96:99], v3 offset:16384\n"
"v_add_f32 v165, v165, v17\n"
"v_mfma_f32_16x16x32_bf16 v[120:123], a[56:59], a[104:107], v[120:123]\n"
"s_sub_u32 s99, s99, s98\n"
"v_add_f32 v166, v166, v18\n"
"v_add_f32 v167, v167, v19\n"
"v_mfma_f32_16x16x32_bf16 v[120:123], a[60:63], a[108:111], v[120:123]\n"
"s_sub_u32 s99, s99, s98\n"
"v_add_f32 v168, v168, v16\n"
"v_add_f32 v169, v169, v17\n"
"v_mfma_f32_16x16x32_bf16 v[92:95], a[52:55], a[108:111], v[92:95]\n"
"s_sub_u32 s99, s99, s98\n"
"ds_read_b128 a[108:111], v3 offset:19456\n"
"v_add_f32 v170, v170, v18\n"
"v_mfma_f32_16x16x32_bf16 v[92:95], a[48:51], a[104:107], v[92:95]\n"
"s_sub_u32 s99, s99, s98\n"
"ds_read_b128 a[104:107], v3 offset:18432\n"
"v_add_f32 v171, v171, v19\n"
"v_mfma_f32_16x16x32_bf16 v[96:99], a[48:51], a[112:115], v[96:99]\n"
"s_sub_u32 s99, s99, s98\n"
"v_accvgpr_read_b32 v12, a16\n"
"v_accvgpr_read_b32 v13, a17\n"
"v_mfma_f32_16x16x32_bf16 v[96:99], a[52:55], a[116:119], v[96:99]\n"
"global_load_dwordx4 a[36:39], v0, s[34:35] offset:-3072 sc1\n"
"s_sub_u32 s99, s99, s98\n"
"v_mfma_f32_16x16x32_bf16 v[124:127], a[60:63], a[116:119], v[124:127]\n"
"s_sub_u32 s99, s99, s98\n"
"ds_read_b128 a[116:119], v3 offset:21504\n"
"v_accvgpr_read_b32 v14, a18\n"
"v_mfma_f32_16x16x32_bf16 v[124:127], a[56:59], a[112:115], v[124:127]\n"
"s_sub_u32 s99, s99, s98\n"
"ds_read_b128 a[112:115], v3 offset:20480\n"
"v_accvgpr_read_b32 v15, a19\n"
"v_mfma_f32_16x16x32_bf16 v[128:131], a[56:59], a[120:123], v[128:131]\n"
"s_sub_u32 s99, s99, s98\n"
"v_add_f32 v12, v12, v16\n"
"v_add_f32 v13, v13, v17\n"
"v_mfma_f32_16x16x32_bf16 v[128:131], a[60:63], a[124:127], v[128:131]\n"
"s_sub_u32 s99, s99, s98\n"
"v_add_f32 v14, v14, v18\n"
"v_add_f32 v15, v15, v19\n"
"v_mfma_f32_16x16x32_bf16 v[100:103], a[52:55], a[124:127], v[100:103]\n"
"s_sub_u32 s99, s99, s98\n"
"ds_read_b128 a[124:127], v3 offset:23552\n"
"v_accvgpr_write_b32 a16, v12\n"
"v_mfma_f32_16x16x32_bf16 v[100:103], a[48:51], a[120:123], v[100:103]\n"
"s_sub_u32 s99, s99, s98\n"
"ds_read_b128 a[120:123], v3 offset:22528\n"
"v_accvgpr_write_b32 a17, v13\n"
"v_mfma_f32_16x16x32_bf16 v[104:107], a[48:51], a[128:131], v[104:107]\n"
"s_sub_u32 s99, s99, s98\n"
"v_accvgpr_write_b32 a18, v14\n"
"v_accvgpr_write_b32 a19, v15\n"
"v_mfma_f32_16x16x32_bf16 v[104:107], a[52:55], a[132:135], v[104:107]\n"
"s_sub_u32 s99, s99, s98\n"
"s_waitcnt vmcnt(10)\n"
"global_load_dwordx4 a[40:43], v0, s[34:35] offset:-2048 sc1\n"
"v_mfma_f32_16x16x32_bf16 v[132:135], a[60:63], a[132:135], v[132:135]\n"
"s_sub_u32 s99, s99, s98\n"
"ds_read_b128 a[132:135], v3 offset:25600\n"
"v_lshlrev_b32 v20, 16, v22\n"
"v_mfma_f32_16x16x32_bf16 v[132:135], a[56:59], a[128:131], v[132:135]\n"
"s_sub_u32 s99, s99, s98\n"
"ds_read_b128 a[128:131], v3 offset:24576\n"
"v_and_b32 v21, s63, v22\n"
"v_mfma_f32_16x16x32_bf16 v[136:139], a[56:59], a[136:139], v[136:139]\n"
"s_sub_u32 s99, s99, s98\n"
"v_lshlrev_b32 v22, 16, v23\n"
"v_and_b32 v23, s63, v23\n"
"v_mfma_f32_16x16x32_bf16 v[136:139], a[60:63], a[140:143], v[136:139]\n"
"s_sub_u32 s99, s99, s98\n"
"v_add_f32 v172, v172, v20\n"
"v_add_f32 v173, v173, v21\n"
"v_mfma_f32_16x16x32_bf16 v[108:111], a[52:55], a[140:143], v[108:111]\n"
"s_sub_u32 s99, s99, s98\n"
"ds_read_b128 a[140:143], v3 offset:27648\n"
"v_add_f32 v174, v174, v22\n"
"v_mfma_f32_16x16x32_bf16 v[108:111], a[48:51], a[136:139], v[108:111]\n"
"s_sub_u32 s99, s99, s98\n"
"ds_read_b128 a[136:139], v3 offset:26624\n"
"v_add_f32 v175, v175, v23\n"
"v_mfma_f32_16x16x32_bf16 v[112:115], a[48:51], a[144:147], v[112:115]\n"
"s_sub_u32 s99, s99, s98\n"
"v_add_f32 v176, v176, v20\n"
"v_add_f32 v177, v177, v21\n"
"v_mfma_f32_16x16x32_bf16 v[112:115], a[52:55], a[148:151], v[112:115]\n"
"global_load_dwordx4 a[44:47], v0, s[34:35] offset:-1024 sc1\n"
"s_sub_u32 s99, s99, s98\n"
"v_mfma_f32_16x16x32_bf16 v[140:143], a[60:63], a[148:151], v[140:143]\n"
"s_sub_u32 s99, s99, s98\n"
"ds_read_b128 a[148:151], v3 offset:29696\n"
"v_add_f32 v178, v178, v22\n"
"v_mfma_f32_16x16x32_bf16 v[140:143], a[56:59], a[144:147], v[140:143]\n"
"s_sub_u32 s99, s99, s98\n"
"ds_read_b128 a[144:147], v3 offset:28672\n"
"v_add_f32 v179, v179, v23\n"
"v_mfma_f32_16x16x32_bf16 a[12:15], a[56:59], a[152:155], a[12:15]\n"
"s_memtime s[76:77]\n"
"v_add_f32 v180, v180, v20\n"
"v_add_f32 v181, v181, v21\n"
"v_mfma_f32_16x16x32_bf16 a[12:15], a[60:63], a[156:159], a[12:15]\n"
"v_add_f32 v182, v182, v22\n"
"v_add_f32 v183, v183, v23\n"
"v_mfma_f32_16x16x32_bf16 a[8:11], a[52:55], a[156:159], a[8:11]\n"
"ds_read_b128 a[156:159], v3 offset:31744\n"
"v_add_f32 v184, v184, v20\n"
"v_mfma_f32_16x16x32_bf16 a[8:11], a[48:51], a[152:155], a[8:11]\n"
"s_waitcnt vmcnt(9) lgkmcnt(11)\n"
"ds_read_b128 a[152:155], v3 offset:30720\n"
"v_add_f32 v185, v185, v21\n"
"v_mfma_f32_16x16x32_bf16 v[32:35], a[64:67], a[96:99], v[32:35]\n"
"v_add_f32 v186, v186, v22\n"
"v_add_f32 v187, v187, v23\n"
"v_mfma_f32_16x16x32_bf16 v[32:35], a[68:71], a[100:103], v[32:35]\n"
"global_load_dwordx4 a[48:51], v0, s[34:35] offset:0 sc1\n"
"v_add_f32 v188, v188, v20\n"
"v_mfma_f32_16x16x32_bf16 v[60:63], a[76:79], a[100:103], v[60:63]\n"
"v_add_f32 v189, v189, v21\n"
"v_mfma_f32_16x16x32_bf16 v[60:63], a[72:75], a[96:99], v[60:63]\n"
"v_add_f32 v190, v190, v22\n"
"v_add_f32 v191, v191, v23\n"
"v_mfma_f32_16x16x32_bf16 v[64:67], a[72:75], a[104:107], v[64:67]\n"
"v_add_f32 v192, v192, v20\n"
"v_mfma_f32_16x16x32_bf16 v[64:67], a[76:79], a[108:111], v[64:67]\n"
"v_add_f32 v193, v193, v21\n"
"v_mfma_f32_16x16x32_bf16 v[36:39], a[68:71], a[108:111], v[36:39]\n"
"v_add_f32 v194, v194, v22\n"
"v_add_f32 v195, v195, v23\n"
"v_mfma_f32_16x16x32_bf16 v[36:39], a[64:67], a[104:107], v[36:39]\n"
"s_waitcnt lgkmcnt(8)\n"
"v_add_f32 v196, v196, v20\n"
"v_mfma_f32_16x16x32_bf16 v[40:43], a[64:67], a[112:115], v[40:43]\n"
"v_add_f32 v197, v197, v21\n"
"v_mfma_f32_16x16x32_bf16 v[40:43], a[68:71], a[116:119], v[40:43]\n"
"global_load_dwordx4 a[52:55], v0, s[34:35] offset:1024 sc1\n"
"v_add_f32 v198, v198, v22\n"
"v_mfma_f32_16x16x32_bf16 v[68:71], a[76:79], a[116:119], v[68:71]\n"
"v_add_f32 v199, v199, v23\n"
"v_accvgpr_read_b32 v12, a20\n"
"v_mfma_f32_16x16x32_bf16 v[68:71], a[72:75], a[112:115], v[68:71]\n"
"v_accvgpr_read_b32 v13, a21\n"
"v_mfma_f32_16x16x32_bf16 v[72:75], a[72:75], a[120:123], v[72:75]\n"
"v_accvgpr_read_b32 v14, a22\n"
"v_accvgpr_read_b32 v15, a23\n"
"v_mfma_f32_16x16x32_bf16 v[72:75], a[76:79], a[124:127], v[72:75]\n"
"v_add_f32 v12, v12, v20\n"
"v_mfma_f32_16x16x32_bf16 v[44:47], a[68:71], a[124:127], v[44:47]\n"
"v_add_f32 v13, v13, v21\n"
"v_mfma_f32_16x16x32_bf16 v[44:47], a[64:67], a[120:123], v[44:47]\n"
"s_waitcnt lgkmcnt(4)\n"
"v_add_f32 v14, v14, v22\n"
"v_mfma_f32_16x16x32_bf16 v[48:51], a[64:67], a[128:131], v[48:51]\n"
"v_add_f32 v15, v15, v23\n"
"v_accvgpr_write_b32 a20, v12\n"
"v_mfma_f32_16x16x32_bf16 v[48:51], a[68:71], a[132:135], v[48:51]\n"
"global_load_dwordx4 a[56:59], v0, s[34:35] offset:2048 sc1\n"
"v_accvgpr_write_b32 a21, v13\n"
"v_mfma_f32_16x16x32_bf16 v[76:79], a[76:79], a[132:135], v[76:79]\n"
"s_waitcnt lgkmcnt(0)\n"
"v_accvgpr_write_b32 a22, v14\n"
"v_mfma_f32_16x16x32_bf16 v[76:79], a[72:75], a[128:131], v[76:79]\n"
"s_sub_u32 s29, s76, s29\n"
"v_accvgpr_write_b32 a23, v15\n"
"v_lshlrev_b32 v24, 16, v26\n"
"v_mfma_f32_16x16x32_bf16 v[80:83], a[72:75], a[136:139], v[80:83]\n"
"s_cmp_lt_u32 s29, 620\n"
"s_cselect_b32 s99, s99, 0\n"
"v_and_b32 v25, s63, v26\n"
"v_mfma_f32_16x16x32_bf16 v[80:83], a[76:79], a[140:143], v[80:83]\n"
"s_max_u32 s31, s31, s99\n"
"v_lshlrev_b32 v26, 16, v27\n"
"v_mfma_f32_16x16x32_bf16 v[52:55], a[68:71], a[140:143], v[52:55]\n"
"v_and_b32 v27, s63, v27\n"
"v_add_f32 v200, v200, v24\n"
"v_mfma_f32_16x16x32_bf16 v[52:55], a[64:67], a[136:139], v[52:55]\n"
"s_mov_b32 s99, 0\n"
"v_add_f32 v201, v201, v25\n"
"v_mfma_f32_16x16x32_bf16 v[56:59], a[64:67], a[144:147], v[56:59]\n"
"s_mov_b32 s98, 0\n"
"v_add_f32 v202, v202, v26\n"
"v_add_f32 v203, v203, v27\n"
"v_mfma_f32_16x16x32_bf16 v[56:59], a[68:71], a[148:151], v[56:59]\n"
"global_load_dwordx4 a[60:63], v0, s[34:35] offset:3072 sc1\n"
"s_memtime s[76:77]\n"
"s_load_dword s98, s[96:97], 256 glc\n"
"v_mfma_f32_16x16x32_bf16 v[84:87], a[76:79], a[148:151], v[84:87]\n"
"v_add_f32 v204, v204, v24\n"
"v_add_f32 v205, v205, v25\n"
"v_mfma_f32_16x16x32_bf16 v[84:87], a[72:75], a[144:147], v[84:87]\n"
"v_add_f32 v206, v206, v26\n"
"v_mfma_f32_16x16x32_bf16 a[4:7], a[72:75], a[152:155], a[4:7]\n"
"s_add_u32 s34, s34, 8192\n"
"v_add_f32 v207, v207, v27\n"
"v_mfma_f32_16x16x32_bf16 a[4:7], a[76:79], a[156:159], a[4:7]\n"
"v_add_f32 v208, v208, v24\n"
"v_add_f32 v209, v209, v25\n"
"v_mfma_f32_16x16x32_bf16 a[0:3], a[68:71], a[156:159], a[0:3]\n"
"v_add_f32 v210, v210, v26\n"
"v_mfma_f32_16x16x32_bf16 a[0:3], a[64:67], a[152:155], a[0:3]\n"
"s_waitcnt vmcnt(8)\n"
"v_add_f32 v211, v211, v27\n"
"v_mfma_f32_16x16x32_bf16 v[88:91], a[80:83], a[96:99], v[88:91]\n"
"v_add_f32 v212, v212, v24\n"
"v_add_f32 v213, v213, v25\n"
"v_mfma_f32_16x16x32_bf16 v[88:91], a[84:87], a[100:103], v[88:91]\n"
"global_load_dwordx4 a[64:67], v0, s[34:35] offset:-4096 sc1\n"
"v_add_f32 v214, v214, v26\n"
"v_mfma_f32_16x16x32_bf16 v[116:119], a[92:95], a[100:103], v[116:119]\n"
"s_mov_b32 s29, s76\n"
"ds_read_b128 a[100:103], v4 offset:17408\n"
"v_add_f32 v215, v215, v27\n"
"v_mfma_f32_16x16x32_bf16 v[116:119], a[88:91], a[96:99], v[116:119]\n"
"ds_read_b128 a[96:99], v4 offset:16384\n"
"v_add_f32 v216, v216, v24\n"
"v_mfma_f32_16x16x32_bf16 v[120:123], a[88:91], a[104:107], v[120:123]\n"
"s_sub_u32 s99, s99, s98\n"
"v_add_f32 v217, v217, v25\n"
"v_add_f32 v218, v218, v26\n"
"v_mfma_f32_16x16x32_bf16 v[120:123], a[92:95], a[108:111], v[120:123]\n"
"s_sub_u32 s99, s99, s98\n"
"v_add_f32 v219, v219, v27\n"
"v_add_f32 v220, v220, v24\n"
"v_mfma_f32_16x16x32_bf16 v[92:95], a[84:87], a[108:111], v[92:95]\n"
"s_sub_u32 s99, s99, s98\n"
"ds_read_b128 a[108:111], v4 offset:19456\n"
"v_add_f32 v221, v221, v25\n"
"v_mfma_f32_16x16x32_bf16 v[92:95], a[80:83], a[104:107], v[92:95]\n"
"s_sub_u32 s99, s99, s98\n"
"ds_read_b128 a[104:107], v4 offset:18432\n"
"v_add_f32 v222, v222, v26\n"
"v_mfma_f32_16x16x32_bf16 v[96:99], a[80:83], a[112:115], v[96:99]\n"
"s_sub_u32 s99, s99, s98\n"
"v_add_f32 v223, v223, v27\n"
"v_add_f32 v224, v224, v24\n"
"v_mfma_f32_16x16x32_bf16 v[96:99], a[84:87], a[116:119], v[96:99]\n"
"global_load_dwordx4 a[68:71], v0, s[34:35] offset:-3072 sc1\n"
"s_sub_u32 s99, s99, s98\n"
"v_mfma_f32_16x16x32_bf16 v[124:127], a[92:95], a[116:119], v[124:127]\n"
"s_sub_u32 s99, s99, s98\n"
"ds_read_b128 a[116:119], v4 offset:21504\n"
"v_add_f32 v225, v225, v25\n"
"v_mfma_f32_16x16x32_bf16 v[124:127], a[88:91], a[112:115], v[124:127]\n"
"s_sub_u32 s99, s99, s98\n"
"ds_read_b128 a[112:115], v4 offset:20480\n"
"v_add_f32 v226, v226, v26\n"
"v_mfma_f32_16x16x32_bf16 v[128:131], a[88:91], a[120:123], v[128:131]\n"
"s_sub_u32 s99, s99, s98\n"
"v_add_f32 v227, v227, v27\n"
"v_accvgpr_read_b32 v12, a24\n"
"v_mfma_f32_16x16x32_bf16 v[128:131], a[92:95], a[124:127], v[128:131]\n"
"s_sub_u32 s99, s99, s98\n"
"v_accvgpr_read_b32 v13, a25\n"
"v_accvgpr_read_b32 v14, a26\n"
"v_mfma_f32_16x16x32_bf16 v[100:103], a[84:87], a[124:127], v[100:103]\n"
"s_sub_u32 s99, s99, s98\n"
"ds_read_b128 a[124:127], v4 offset:23552\n"
"v_accvgpr_read_b32 v15, a27\n"
"v_mfma_f32_16x16x32_bf16 v[100:103], a[80:83], a[120:123], v[100:103]\n"
"s_sub_u32 s99, s99, s98\n"
"ds_read_b128 a[120:123], v4 offset:22528\n"
"v_add_f32 v12, v12, v24\n"
"v_mfma_f32_16x16x32_bf16 v[104:107], a[80:83], a[128:131], v[104:107]\n"
"s_sub_u32 s99, s99, s98\n"
"v_add_f32 v13, v13, v25\n"
"v_add_f32 v14, v14, v26\n"
"v_mfma_f32_16x16x32_bf16 v[104:107], a[84:87], a[132:135], v[104:107]\n"
"global_load_dwordx4 a[72:75], v0, s[34:35] offset:-2048 sc1\n"
"s_sub_u32 s99, s99, s98\n"
"v_mfma_f32_16x16x32_bf16 v[132:135], a[92:95], a[132:135], v[132:135]\n"
"s_sub_u32 s99, s99, s98\n"
"ds_read_b128 a[132:135], v4 offset:25600\n"
"v_add_f32 v15, v15, v27\n"
"v_mfma_f32_16x16x32_bf16 v[132:135], a[88:91], a[128:131], v[132:135]\n"
"s_sub_u32 s99, s99, s98\n"
"ds_read_b128 a[128:131], v4 offset:24576\n"
"v_accvgpr_write_b32 a24, v12\n"
"v_mfma_f32_16x16x32_bf16 v[136:139], a[88:91], a[136:139], v[136:139]\n"
"s_sub_u32 s99, s99, s98\n"
"v_accvgpr_write_b32 a25, v13\n"
"v_accvgpr_write_b32 a26, v14\n"
"v_mfma_f32_16x16x32_bf16 v[136:139], a[92:95], a[140:143], v[136:139]\n"
"s_sub_u32 s99, s99, s98\n"
"v_accvgpr_write_b32 a27, v15\n"
"v_lshlrev_b32 v28, 16, v30\n"
"v_mfma_f32_16x16x32_bf16 v[108:111], a[84:87], a[140:143], v[108:111]\n"
"s_sub_u32 s99, s99, s98\n"
"ds_read_b128 a[140:143], v4 offset:27648\n"
"v_and_b32 v29, s63, v30\n"
"v_mfma_f32_16x16x32_bf16 v[108:111], a[80:83], a[136:139], v[108:111]\n"
"s_sub_u32 s99, s99, s98\n"
"ds_read_b128 a[136:139], v4 offset:26624\n"
"v_lshlrev_b32 v30, 16, v31\n"
"v_mfma_f32_16x16x32_bf16 v[112:115], a[80:83], a[144:147], v[112:115]\n"
"s_sub_u32 s99, s99, s98\n"
"v_and_b32 v31, s63, v31\n"
"v_add_f32 v228, v228, v28\n"
"v_mfma_f32_16x16x32_bf16 v[112:115], a[84:87], a[148:151], v[112:115]\n"
"global_load_dwordx4 a[76:79], v0, s[34:35] offset:-1024 sc1\n"
"s_sub_u32 s99, s99, s98\n"
"v_mfma_f32_16x16x32_bf16 v[140:143], a[92:95], a[148:151], v[140:143]\n"
"s_sub_u32 s99, s99, s98\n"
"ds_read_b128 a[148:151], v4 offset:29696\n"
"v_add_f32 v229, v229, v29\n"
"v_mfma_f32_16x16x32_bf16 v[140:143], a[88:91], a[144:147], v[140:143]\n"
"s_sub_u32 s99, s99, s98\n"
"ds_read_b128 a[144:147], v4 offset:28672\n"
"v_add_f32 v230, v230, v30\n"
"v_mfma_f32_16x16x32_bf16 a[12:15], a[88:91], a[152:155], a[12:15]\n"
"s_memtime s[76:77]\n"
"v_add_f32 v231, v231, v31\n"
"v_add_f32 v232, v232, v28\n"
"v_mfma_f32_16x16x32_bf16 a[12:15], a[92:95], a[156:159], a[12:15]\n"
"v_add_f32 v233, v233, v29\n"
"v_add_f32 v234, v234, v30\n"
"v_mfma_f32_16x16x32_bf16 a[8:11], a[84:87], a[156:159], a[8:11]\n"
"ds_read_b128 a[156:159], v4 offset:31744\n"
"v_add_f32 v235, v235, v31\n"
"v_mfma_f32_16x16x32_bf16 a[8:11], a[80:83], a[152:155], a[8:11]\n"
"s_waitcnt vmcnt(8) lgkmcnt(11)\n"
"ds_read_b128 a[152:155], v4 offset:30720\n"
"v_add_f32 v236, v236, v28\n"
"v_mfma_f32_16x16x32_bf16 v[32:35], a[32:35], a[96:99], v[32:35]\n"
"v_add_f32 v237, v237, v29\n"
"v_add_f32 v238, v238, v30\n"
"v_mfma_f32_16x16x32_bf16 v[32:35], a[36:39], a[100:103], v[32:35]\n"
"global_load_dwordx4 a[80:83], v0, s[34:35] offset:0 sc1\n"
"v_add_f32 v239, v239, v31\n"
"v_mfma_f32_16x16x32_bf16 v[60:63], a[44:47], a[100:103], v[60:63]\n"
"v_add_f32 v240, v240, v28\n"
"v_add_f32 v241, v241, v29\n"
"v_mfma_f32_16x16x32_bf16 v[60:63], a[40:43], a[96:99], v[60:63]\n"
"v_add_f32 v242, v242, v30\n"
"v_mfma_f32_16x16x32_bf16 v[64:67], a[40:43], a[104:107], v[64:67]\n"
"v_add_f32 v243, v243, v31\n"
"v_add_f32 v244, v244, v28\n"
"v_mfma_f32_16x16x32_bf16 v[64:67], a[44:47], a[108:111], v[64:67]\n"
"v_add_f32 v245, v245, v29\n"
"v_mfma_f32_16x16x32_bf16 v[36:39], a[36:39], a[108:111], v[36:39]\n"
"v_add_f32 v246, v246, v30\n"
"v_mfma_f32_16x16x32_bf16 v[36:39], a[32:35], a[104:107], v[36:39]\n"
"s_waitcnt lgkmcnt(8)\n"
"v_add_f32 v247, v247, v31\n"
"v_mfma_f32_16x16x32_bf16 v[40:43], a[32:35], a[112:115], v[40:43]\n"
"v_add_f32 v248, v248, v28\n"
"v_add_f32 v249, v249, v29\n"
"v_mfma_f32_16x16x32_bf16 v[40:43], a[36:39], a[116:119], v[40:43]\n"
"global_load_dwordx4 a[84:87], v0, s[34:35] offset:1024 sc1\n"
"s_add_u32 s48, s56, %12\n"
"s_addc_u32 s49, s57, 0\n"
"v_mfma_f32_16x16x32_bf16 v[68:71], a[44:47], a[116:119], v[68:71]\n"
"v_add_f32 v250, v250, v30\n"
"v_add_f32 v251, v251, v31\n"
"v_mfma_f32_16x16x32_bf16 v[68:71], a[40:43], a[112:115], v[68:71]\n"
"s_mov_b32 s50, 32768\n"
"v_add_f32 v252, v252, v28\n"
"v_add_f32 v253, v253, v29\n"
"v_mfma_f32_16x16x32_bf16 v[72:75], a[40:43], a[120:123], v[72:75]\n"
"v_add_f32 v254, v254, v30\n"
"v_mfma_f32_16x16x32_bf16 v[72:75], a[44:47], a[124:127], v[72:75]\n"
"s_mov_b32 s65, s78\n"
"v_add_f32 v255, v255, v31\n"
"v_mfma_f32_16x16x32_bf16 v[44:47], a[36:39], a[124:127], v[44:47]\n"
"v_accvgpr_read_b32 v12, a28\n"
"v_accvgpr_read_b32 v13, a29\n"
"v_mfma_f32_16x16x32_bf16 v[44:47], a[32:35], a[120:123], v[44:47]\n"
"s_waitcnt lgkmcnt(4)\n"
"v_accvgpr_read_b32 v14, a30\n"
"v_mfma_f32_16x16x32_bf16 v[48:51], a[32:35], a[128:131], v[48:51]\n"
"v_accvgpr_read_b32 v15, a31\n"
"v_mfma_f32_16x16x32_bf16 v[48:51], a[36:39], a[132:135], v[48:51]\n"
"global_load_dwordx4 a[88:91], v0, s[34:35] offset:2048 sc1\n"
"v_add_f32 v12, v12, v28\n"
"v_mfma_f32_16x16x32_bf16 v[76:79], a[44:47], a[132:135], v[76:79]\n"
"s_waitcnt lgkmcnt(0)\n"
"v_add_f32 v13, v13, v29\n"
"v_add_f32 v14, v14, v30\n"
"v_mfma_f32_16x16x32_bf16 v[76:79], a[40:43], a[128:131], v[76:79]\n"
"s_sub_u32 s29, s76, s29\n"
"v_add_f32 v15, v15, v31\n"
"v_mfma_f32_16x16x32_bf16 v[80:83], a[40:43], a[136:139], v[80:83]\n"
"s_cmp_lt_u32 s29, 620\n"
"s_cselect_b32 s99, s99, 0\n"
"v_accvgpr_write_b32 a28, v12\n"
"v_mfma_f32_16x16x32_bf16 v[80:83], a[44:47], a[140:143], v[80:83]\n"
"s_max_u32 s30, s30, s99\n"
"v_accvgpr_write_b32 a29, v13\n"
"v_accvgpr_write_b32 a30, v14\n"
"v_mfma_f32_16x16x32_bf16 v[52:55], a[36:39], a[140:143], v[52:55]\n"
"s_cmp_eq_u32 s30, 0\n"
"s_cselect_b32 s30, 10, s30\n"
"v_accvgpr_write_b32 a31, v15\n"
"v_mfma_f32_16x16x32_bf16 v[52:55], a[32:35], a[136:139], v[52:55]\n"
"s_cmp_eq_u32 s31, 0\n"
"s_cselect_b32 s31, 10, s31\n"
"v_mfma_f32_16x16x32_bf16 v[56:59], a[32:35], a[144:147], v[56:59]\n"
"s_sub_u32 s69, s31, s30\n"
"v_mfma_f32_16x16x32_bf16 v[56:59], a[36:39], a[148:151], v[56:59]\n"
"global_load_dwordx4 a[92:95], v0, s[34:35] offset:3072 sc1\n"
"v_mov_b32 v20, s69\n"
"v_mfma_f32_16x16x32_bf16 v[84:87], a[44:47], a[148:151], v[84:87]\n"
"ds_write_b32 v2, v20 offset:8192\n"
"v_mfma_f32_16x16x32_bf16 v[84:87], a[40:43], a[144:147], v[84:87]\n"
"v_mfma_f32_16x16x32_bf16 a[4:7], a[40:43], a[152:155], a[4:7]\n"
"s_add_u32 s34, s34, 8192\n"
"v_mfma_f32_16x16x32_bf16 a[4:7], a[44:47], a[156:159], a[4:7]\n"
"v_mfma_f32_16x16x32_bf16 a[0:3], a[36:39], a[156:159], a[0:3]\n"
"v_mfma_f32_16x16x32_bf16 a[0:3], a[32:35], a[152:155], a[0:3]\n"
"s_waitcnt lgkmcnt(0)\n"
"s_barrier\n"
"s_waitcnt vmcnt(8)\n"
"v_mfma_f32_16x16x32_bf16 v[88:91], a[48:51], a[96:99], v[88:91]\n"
"ds_read_b32 v21, v3 offset:8192\n"
"v_mfma_f32_16x16x32_bf16 v[88:91], a[52:55], a[100:103], v[88:91]\n"
"global_load_dwordx4 a[32:35], v0, s[34:35] offset:-4096 sc1\n"
"v_mfma_f32_16x16x32_bf16 v[116:119], a[60:63], a[100:103], v[116:119]\n"
"ds_read_b128 a[100:103], v5 offset:17408\n"
"v_mfma_f32_16x16x32_bf16 v[116:119], a[56:59], a[96:99], v[116:119]\n"
"ds_read_b128 a[96:99], v5 offset:16384\n"
"v_mfma_f32_16x16x32_bf16 v[120:123], a[56:59], a[104:107], v[120:123]\n"
"ds_read_b32 v22, v4 offset:8192\n"
"v_mfma_f32_16x16x32_bf16 v[120:123], a[60:63], a[108:111], v[120:123]\n"
"ds_read_b32 v23, v5 offset:8192\n"
"v_mfma_f32_16x16x32_bf16 v[92:95], a[52:55], a[108:111], v[92:95]\n"
"ds_read_b128 a[108:111], v5 offset:19456\n"
"v_mfma_f32_16x16x32_bf16 v[92:95], a[48:51], a[104:107], v[92:95]\n"
"ds_read_b128 a[104:107], v5 offset:18432\n"
"v_mfma_f32_16x16x32_bf16 v[96:99], a[48:51], a[112:115], v[96:99]\n"
"v_mfma_f32_16x16x32_bf16 v[96:99], a[52:55], a[116:119], v[96:99]\n"
"global_load_dwordx4 a[36:39], v0, s[34:35] offset:-3072 sc1\n"
"v_mfma_f32_16x16x32_bf16 v[124:127], a[60:63], a[116:119], v[124:127]\n"
"s_waitcnt lgkmcnt(2)\n"
"ds_read_b128 a[116:119], v5 offset:21504\n"
"v_add_u32 v20, v20, v21\n"
"v_mfma_f32_16x16x32_bf16 v[124:127], a[56:59], a[112:115], v[124:127]\n"
"ds_read_b128 a[112:115], v5 offset:20480\n"
"v_mfma_f32_16x16x32_bf16 v[128:131], a[56:59], a[120:123], v[128:131]\n"
"v_add3_u32 v20, v20, v22, v23\n"
"v_mfma_f32_16x16x32_bf16 v[128:131], a[60:63], a[124:127], v[128:131]\n"
"v_mfma_f32_16x16x32_bf16 v[100:103], a[52:55], a[124:127], v[100:103]\n"
"ds_read_b128 a[124:127], v5 offset:23552\n"
"v_readfirstlane_b32 s69, v20\n"
"v_mfma_f32_16x16x32_bf16 v[100:103], a[48:51], a[120:123], v[100:103]\n"
"ds_read_b128 a[120:123], v5 offset:22528\n"
"v_mfma_f32_16x16x32_bf16 v[104:107], a[48:51], a[128:131], v[104:107]\n"
"v_mfma_f32_16x16x32_bf16 v[104:107], a[52:55], a[132:135], v[104:107]\n"
"s_cmp_le_i32 s69, -2\n"
"global_load_dwordx4 a[40:43], v0, s[34:35] offset:-2048 sc1\n"
"s_cselect_b32 s66, 2, 1\n"
"v_mfma_f32_16x16x32_bf16 v[132:135], a[60:63], a[132:135], v[132:135]\n"
"ds_read_b128 a[132:135], v5 offset:25600\n"
"v_mfma_f32_16x16x32_bf16 v[132:135], a[56:59], a[128:131], v[132:135]\n"
"s_cmp_ge_i32 s69, 2\n"
"ds_read_b128 a[128:131], v5 offset:24576\n"
"s_cselect_b32 s66, 3, s66\n"
"v_mfma_f32_16x16x32_bf16 v[136:139], a[56:59], a[136:139], v[136:139]\n"
"ds_read_b128 v[16:19], v7 offset:32768\n"
"v_mfma_f32_16x16x32_bf16 v[136:139], a[60:63], a[140:143], v[136:139]\n"
"s_lshl_b32 s73, s66, 13\n"
"s_xor_b32 s60, s79, s66\n"
"v_mfma_f32_16x16x32_bf16 v[108:111], a[52:55], a[140:143], v[108:111]\n"
"ds_read_b128 a[140:143], v5 offset:27648\n"
"v_mfma_f32_16x16x32_bf16 v[108:111], a[48:51], a[136:139], v[108:111]\n"
"s_cmp_eq_u32 s66, 3\n"
"ds_read_b128 a[136:139], v5 offset:26624\n"
"s_cselect_b32 s72, 8192, 0\n"
"v_mfma_f32_16x16x32_bf16 v[112:115], a[48:51], a[144:147], v[112:115]\n"
"s_abs_i32 s30, s69\n"
"v_mfma_f32_16x16x32_bf16 v[112:115], a[52:55], a[148:151], v[112:115]\n"
"global_load_dwordx4 a[44:47], v0, s[34:35] offset:-1024 sc1\n"
"s_add_u32 s52, s56, %11\n"
"s_addc_u32 s53, s57, 0\n"
"v_mfma_f32_16x16x32_bf16 v[140:143], a[60:63], a[148:151], v[140:143]\n"
"s_cmp_lt_u32 s30, 26\n"
"s_cselect_b32 s30, 2, 0\n"
"ds_read_b128 a[148:151], v5 offset:29696\n"
"v_mfma_f32_16x16x32_bf16 v[140:143], a[56:59], a[144:147], v[140:143]\n"
"s_add_u32 s52, s52, 4096\n"
"ds_read_b128 a[144:147], v5 offset:28672\n"
"s_addc_u32 s53, s53, 0\n"
"v_mfma_f32_16x16x32_bf16 a[12:15], a[56:59], a[152:155], a[12:15]\n"
"v_mfma_f32_16x16x32_bf16 a[12:15], a[60:63], a[156:159], a[12:15]\n"
"s_add_u32 s54, s52, s73\n"
"s_addc_u32 s55, s53, 0\n"
"v_mfma_f32_16x16x32_bf16 a[8:11], a[52:55], a[156:159], a[8:11]\n"
"ds_read_b128 a[156:159], v5 offset:31744\n"
"v_mfma_f32_16x16x32_bf16 a[8:11], a[48:51], a[152:155], a[8:11]\n"
"s_add_u32 s52, s52, s72\n"
"ds_read_b128 a[152:155], v5 offset:30720\n"
"s_addc_u32 s53, s53, 0\n"
"L_P6:\n"
"s_waitcnt vmcnt(8) lgkmcnt(13)\n"
"v_mfma_f32_16x16x32_bf16 v[32:35], a[64:67], a[96:99], v[32:35]\n"
"v_mfma_f32_16x16x32_bf16 v[32:35], a[68:71], a[100:103], v[32:35]\n"
"global_load_dwordx4 a[48:51], v0, s[34:35] offset:0 sc1\n"
"v_mfma_f32_16x16x32_bf16 v[60:63], a[76:79], a[100:103], v[60:63]\n"
"v_mfma_f32_16x16x32_bf16 v[60:63], a[72:75], a[96:99], v[60:63]\n"
"v_mfma_f32_16x16x32_bf16 v[64:67], a[72:75], a[104:107], v[64:67]\n"
"v_mfma_f32_16x16x32_bf16 v[64:67], a[76:79], a[108:111], v[64:67]\n"
"v_mfma_f32_16x16x32_bf16 v[36:39], a[68:71], a[108:111], v[36:39]\n"
"s_waitcnt lgkmcnt(6)\n"
"v_lshlrev_b32 v12, 16, v16\n"
"v_mfma_f32_16x16x32_bf16 v[36:39], a[64:67], a[104:107], v[36:39]\n"
"v_and_b32 v13, s63, v16\n"
"v_lshlrev_b32 v14, 16, v17\n"
"v_mfma_f32_16x16x32_bf16 v[40:43], a[64:67], a[112:115], v[40:43]\n"
"v_and_b32 v15, s63, v17\n"
"v_mfma_f32_16x16x32_bf16 v[40:43], a[68:71], a[116:119], v[40:43]\n"
"global_load_dwordx4 a[52:55], v0, s[34:35] offset:1024 sc1\n"
"ds_read_b128 v[20:23], v7 offset:34816\n"
"v_mfma_f32_16x16x32_bf16 v[68:71], a[76:79], a[116:119], v[68:71]\n"
"v_mul_f32 v12, v12, v32\n"
"v_mul_f32 v13, v13, v33\n"
"v_mfma_f32_16x16x32_bf16 v[68:71], a[72:75], a[112:115], v[68:71]\n"
"v_mul_f32 v14, v14, v34\n"
"v_mfma_f32_16x16x32_bf16 v[72:75], a[72:75], a[120:123], v[72:75]\n"
"v_mul_f32 v15, v15, v35\n"
"v_mfma_f32_16x16x32_bf16 v[72:75], a[76:79], a[124:127], v[72:75]\n"
"v_cvt_pk_bf16_f32 v24, v12, v13\n"
"v_cvt_pk_bf16_f32 v25, v14, v15\n"
"v_mfma_f32_16x16x32_bf16 v[44:47], a[68:71], a[124:127], v[44:47]\n"
"ds_write_b64 v2, v[24:25] offset:0\n"
"v_mfma_f32_16x16x32_bf16 v[44:47], a[64:67], a[120:123], v[44:47]\n"
"s_waitcnt lgkmcnt(6)\n"
"v_lshlrev_b32 v12, 16, v18\n"
"v_mfma_f32_16x16x32_bf16 v[48:51], a[64:67], a[128:131], v[48:51]\n"
"v_and_b32 v13, s63, v18\n"
"v_lshlrev_b32 v14, 16, v19\n"
"v_mfma_f32_16x16x32_bf16 v[48:51], a[68:71], a[132:135], v[48:51]\n"
"global_load_dwordx4 a[56:59], v0, s[34:35] offset:2048 sc1\n"
"v_and_b32 v15, s63, v19\n"
"v_mfma_f32_16x16x32_bf16 v[76:79], a[76:79], a[132:135], v[76:79]\n"
"v_mul_f32 v12, v12, v60\n"
"v_mfma_f32_16x16x32_bf16 v[76:79], a[72:75], a[128:131], v[76:79]\n"
"v_mul_f32 v13, v13, v61\n"
"v_mul_f32 v14, v14, v62\n"
"v_mfma_f32_16x16x32_bf16 v[80:83], a[72:75], a[136:139], v[80:83]\n"
"v_mul_f32 v15, v15, v63\n"
"v_mfma_f32_16x16x32_bf16 v[80:83], a[76:79], a[140:143], v[80:83]\n"
"v_cvt_pk_bf16_f32 v26, v12, v13\n"
"v_cvt_pk_bf16_f32 v27, v14, v15\n"
"v_mfma_f32_16x16x32_bf16 v[52:55], a[68:71], a[140:143], v[52:55]\n"
"ds_write_b64 v2, v[26:27] offset:8\n"
"v_mfma_f32_16x16x32_bf16 v[52:55], a[64:67], a[136:139], v[52:55]\n"
"s_waitcnt lgkmcnt(2)\n"
"v_lshlrev_b32 v12, 16, v20\n"
"v_mfma_f32_16x16x32_bf16 v[56:59], a[64:67], a[144:147], v[56:59]\n"
"v_and_b32 v13, s63, v20\n"
"v_mfma_f32_16x16x32_bf16 v[56:59], a[68:71], a[148:151], v[56:59]\n"
"global_load_dwordx4 a[60:63], v0, s[34:35] offset:3072 sc1\n"
"v_lshlrev_b32 v14, 16, v21\n"
"v_mfma_f32_16x16x32_bf16 v[84:87], a[76:79], a[148:151], v[84:87]\n"
"v_and_b32 v15, s63, v21\n"
"v_xor_b32 v16, 64, v7\n"
"v_mfma_f32_16x16x32_bf16 v[84:87], a[72:75], a[144:147], v[84:87]\n"
"ds_read_b128 v[16:19], v16 offset:32768\n"
"v_mfma_f32_16x16x32_bf16 a[4:7], a[72:75], a[152:155], a[4:7]\n"
"s_add_u32 s34, s34, 8192\n"
"v_mul_f32 v12, v12, v36\n"
"v_mul_f32 v13, v13, v37\n"
"v_mfma_f32_16x16x32_bf16 a[4:7], a[76:79], a[156:159], a[4:7]\n"
"v_mul_f32 v14, v14, v38\n"
"v_mfma_f32_16x16x32_bf16 a[0:3], a[68:71], a[156:159], a[0:3]\n"
"v_mul_f32 v15, v15, v39\n"
"v_mfma_f32_16x16x32_bf16 a[0:3], a[64:67], a[152:155], a[0:3]\n"
"s_waitcnt vmcnt(8)\n"
"v_cvt_pk_bf16_f32 v28, v12, v13\n"
"v_cvt_pk_bf16_f32 v29, v14, v15\n"
"v_mfma_f32_16x16x32_bf16 v[88:91], a[80:83], a[96:99], v[88:91]\n"
"v_lshlrev_b32 v12, 16, v22\n"
"v_mfma_f32_16x16x32_bf16 v[88:91], a[84:87], a[100:103], v[88:91]\n"
"global_load_dwordx4 a[64:67], v0, s[34:35] offset:-4096 sc1\n"
"v_and_b32 v13, s63, v22\n"
"v_mfma_f32_16x16x32_bf16 v[116:119], a[92:95], a[100:103], v[116:119]\n"
"ds_read_b128 a[100:103], v2 offset:17408\n"
"v_lshlrev_b32 v14, 16, v23\n"
"v_mfma_f32_16x16x32_bf16 v[116:119], a[88:91], a[96:99], v[116:119]\n"
"ds_read_b128 a[96:99], v2 offset:16384\n"
"v_and_b32 v15, s63, v23\n"
"v_mfma_f32_16x16x32_bf16 v[120:123], a[88:91], a[104:107], v[120:123]\n"
"v_mul_f32 v12, v12, v64\n"
"v_mul_f32 v13, v13, v65\n"
"v_mfma_f32_16x16x32_bf16 v[120:123], a[92:95], a[108:111], v[120:123]\n"
"v_mul_f32 v14, v14, v66\n"
"v_mul_f32 v15, v15, v67\n"
"v_mfma_f32_16x16x32_bf16 v[92:95], a[84:87], a[108:111], v[92:95]\n"
"ds_read_b128 a[108:111], v2 offset:19456\n"
"v_cvt_pk_bf16_f32 v30, v12, v13\n"
"v_mfma_f32_16x16x32_bf16 v[92:95], a[80:83], a[104:107], v[92:95]\n"
"ds_read_b128 a[104:107], v2 offset:18432\n"
"v_cvt_pk_bf16_f32 v31, v14, v15\n"
"v_mfma_f32_16x16x32_bf16 v[96:99], a[80:83], a[112:115], v[96:99]\n"
"global_store_dwordx4 v0, v[28:31], s[52:53] offset:-2048 sc1\n"
"ds_write_b128 v2, v[28:31] offset:2048\n"
"v_mfma_f32_16x16x32_bf16 v[96:99], a[84:87], a[116:119], v[96:99]\n"
"s_waitcnt lgkmcnt(5)\n"
"global_load_dwordx4 a[68:71], v0, s[34:35] offset:-3072 sc1\n"
"v_lshlrev_b32 v12, 16, v16\n"
"v_mfma_f32_16x16x32_bf16 v[124:127], a[92:95], a[116:119], v[124:127]\n"
"ds_read_b128 a[116:119], v2 offset:21504\n"
"v_and_b32 v13, s63, v16\n"
"v_mfma_f32_16x16x32_bf16 v[124:127], a[88:91], a[112:115], v[124:127]\n"
"ds_read_b128 a[112:115], v2 offset:20480\n"
"v_lshlrev_b32 v14, 16, v17\n"
"v_mfma_f32_16x16x32_bf16 v[128:131], a[88:91], a[120:123], v[128:131]\n"
"v_and_b32 v15, s63, v17\n"
"v_xor_b32 v20, 64, v7\n"
"v_mfma_f32_16x16x32_bf16 v[128:131], a[92:95], a[124:127], v[128:131]\n"
"ds_read_b128 v[20:23], v20 offset:34816\n"
"v_mul_f32 v12, v12, v88\n"
"v_mfma_f32_16x16x32_bf16 v[100:103], a[84:87], a[124:127], v[100:103]\n"
"ds_read_b128 a[124:127], v2 offset:23552\n"
"v_mul_f32 v13, v13, v89\n"
"v_mfma_f32_16x16x32_bf16 v[100:103], a[80:83], a[120:123], v[100:103]\n"
"ds_read_b128 a[120:123], v2 offset:22528\n"
"v_mul_f32 v14, v14, v90\n"
"v_mfma_f32_16x16x32_bf16 v[104:107], a[80:83], a[128:131], v[104:107]\n"
"v_mul_f32 v15, v15, v91\n"
"v_cvt_pk_bf16_f32 v24, v12, v13\n"
"v_mfma_f32_16x16x32_bf16 v[104:107], a[84:87], a[132:135], v[104:107]\n"
"global_load_dwordx4 a[72:75], v0, s[34:35] offset:-2048 sc1\n"
"v_cvt_pk_bf16_f32 v25, v14, v15\n"
"v_mfma_f32_16x16x32_bf16 v[132:135], a[92:95], a[132:135], v[132:135]\n"
"ds_read_b128 a[132:135], v2 offset:25600\n"
"v_lshlrev_b32 v12, 16, v18\n"
"v_mfma_f32_16x16x32_bf16 v[132:135], a[88:91], a[128:131], v[132:135]\n"
"ds_read_b128 a[128:131], v2 offset:24576\n"
"v_and_b32 v13, s63, v18\n"
"v_mfma_f32_16x16x32_bf16 v[136:139], a[88:91], a[136:139], v[136:139]\n"
"v_lshlrev_b32 v14, 16, v19\n"
"v_and_b32 v15, s63, v19\n"
"v_mfma_f32_16x16x32_bf16 v[136:139], a[92:95], a[140:143], v[136:139]\n"
"v_mul_f32 v12, v12, v116\n"
"v_mfma_f32_16x16x32_bf16 v[108:111], a[84:87], a[140:143], v[108:111]\n"
"ds_read_b128 a[140:143], v2 offset:27648\n"
"v_mul_f32 v13, v13, v117\n"
"v_mfma_f32_16x16x32_bf16 v[108:111], a[80:83], a[136:139], v[108:111]\n"
"ds_read_b128 a[136:139], v2 offset:26624\n"
"v_mul_f32 v14, v14, v118\n"
"v_mfma_f32_16x16x32_bf16 v[112:115], a[80:83], a[144:147], v[112:115]\n"
"v_mul_f32 v15, v15, v119\n"
"v_cvt_pk_bf16_f32 v26, v12, v13\n"
"v_mfma_f32_16x16x32_bf16 v[112:115], a[84:87], a[148:151], v[112:115]\n"
"global_load_dwordx4 a[76:79], v0, s[34:35] offset:-1024 sc1\n"
"v_cvt_pk_bf16_f32 v27, v14, v15\n"
"v_mfma_f32_16x16x32_bf16 v[140:143], a[92:95], a[148:151], v[140:143]\n"
"ds_read_b128 a[148:151], v2 offset:29696\n"
"v_mfma_f32_16x16x32_bf16 v[140:143], a[88:91], a[144:147], v[140:143]\n"
"ds_read_b128 a[144:147], v2 offset:28672\n"
"v_mfma_f32_16x16x32_bf16 a[12:15], a[88:91], a[152:155], a[12:15]\n"
"global_store_dwordx4 v0, v[24:27], s[52:53] offset:-3072 sc1\n"
"ds_write_b128 v2, v[24:27] offset:1024\n"
"v_mfma_f32_16x16x32_bf16 a[12:15], a[92:95], a[156:159], a[12:15]\n"
"s_waitcnt lgkmcnt(9)\n"
"v_lshlrev_b32 v12, 16, v20\n"
"v_and_b32 v13, s63, v20\n"
"v_mfma_f32_16x16x32_bf16 a[8:11], a[84:87], a[156:159], a[8:11]\n"
"ds_read_b128 a[156:159], v2 offset:31744\n"
"v_lshlrev_b32 v14, 16, v21\n"
"v_mfma_f32_16x16x32_bf16 a[8:11], a[80:83], a[152:155], a[8:11]\n"
"s_waitcnt vmcnt(10)\n"
"ds_read_b128 a[152:155], v2 offset:30720\n"
"v_and_b32 v15, s63, v21\n"
"v_mfma_f32_16x16x32_bf16 v[144:147], a[32:35], a[96:99], v[144:147]\n"
"ds_read_b128 v[16:19], v7 offset:36864\n"
"v_mul_f32 v12, v12, v92\n"
"v_mfma_f32_16x16x32_bf16 v[144:147], a[36:39], a[100:103], v[144:147]\n"
"global_load_dwordx4 a[80:83], v0, s[34:35] offset:0 sc1\n"
"v_mul_f32 v13, v13, v93\n"
"v_mfma_f32_16x16x32_bf16 v[172:175], a[44:47], a[100:103], v[172:175]\n"
"v_mul_f32 v14, v14, v94\n"
"v_mul_f32 v15, v15, v95\n"
"v_mfma_f32_16x16x32_bf16 v[172:175], a[40:43], a[96:99], v[172:175]\n"
"v_cvt_pk_bf16_f32 v28, v12, v13\n"
"v_cvt_pk_bf16_f32 v29, v14, v15\n"
"v_mfma_f32_16x16x32_bf16 v[176:179], a[40:43], a[104:107], v[176:179]\n"
"ds_write_b64 v2, v[28:29] offset:3072\n"
"v_mfma_f32_16x16x32_bf16 v[176:179], a[44:47], a[108:111], v[176:179]\n"
"v_lshlrev_b32 v12, 16, v22\n"
"v_and_b32 v13, s63, v22\n"
"v_mfma_f32_16x16x32_bf16 v[148:151], a[36:39], a[108:111], v[148:151]\n"
"v_lshlrev_b32 v14, 16, v23\n"
"v_mfma_f32_16x16x32_bf16 v[148:151], a[32:35], a[104:107], v[148:151]\n"
"s_waitcnt lgkmcnt(11)\n"
"v_and_b32 v15, s63, v23\n"
"v_mul_f32 v12, v12, v120\n"
"v_mfma_f32_16x16x32_bf16 v[152:155], a[32:35], a[112:115], v[152:155]\n"
"v_mul_f32 v13, v13, v121\n"
"v_mfma_f32_16x16x32_bf16 v[152:155], a[36:39], a[116:119], v[152:155]\n"
"global_load_dwordx4 a[84:87], v0, s[34:35] offset:1024 sc1\n"
"v_mul_f32 v14, v14, v122\n"
"v_mfma_f32_16x16x32_bf16 v[180:183], a[44:47], a[116:119], v[180:183]\n"
"v_mul_f32 v15, v15, v123\n"
"v_cvt_pk_bf16_f32 v30, v12, v13\n"
"v_mfma_f32_16x16x32_bf16 v[180:183], a[40:43], a[112:115], v[180:183]\n"
"v_cvt_pk_bf16_f32 v31, v14, v15\n"
"v_mfma_f32_16x16x32_bf16 v[184:187], a[40:43], a[120:123], v[184:187]\n"
"ds_write_b64 v2, v[30:31] offset:3080\n"
"v_mfma_f32_16x16x32_bf16 v[184:187], a[44:47], a[124:127], v[184:187]\n"
"s_waitcnt lgkmcnt(2)\n"
"v_lshlrev_b32 v12, 16, v16\n"
"v_and_b32 v13, s63, v16\n"
"v_mfma_f32_16x16x32_bf16 v[156:159], a[36:39], a[124:127], v[156:159]\n"
"v_lshlrev_b32 v14, 16, v17\n"
"v_mfma_f32_16x16x32_bf16 v[156:159], a[32:35], a[120:123], v[156:159]\n"
"v_and_b32 v15, s63, v17\n"
"v_xor_b32 v20, 64, v7\n"
"v_mfma_f32_16x16x32_bf16 v[160:163], a[32:35], a[128:131], v[160:163]\n"
"ds_read_b128 v[20:23], v20 offset:36864\n"
"v_mfma_f32_16x16x32_bf16 v[160:163], a[36:39], a[132:135], v[160:163]\n"
"global_load_dwordx4 a[88:91], v0, s[34:35] offset:2048 sc1\n"
"v_mul_f32 v12, v12, v40\n"
"v_mfma_f32_16x16x32_bf16 v[188:191], a[44:47], a[132:135], v[188:191]\n"
"v_mul_f32 v13, v13, v41\n"
"v_mfma_f32_16x16x32_bf16 v[188:191], a[40:43], a[128:131], v[188:191]\n"
"v_mul_f32 v14, v14, v42\n"
"v_mul_f32 v15, v15, v43\n"
"v_mfma_f32_16x16x32_bf16 v[192:195], a[40:43], a[136:139], v[192:195]\n"
"v_cvt_pk_bf16_f32 v24, v12, v13\n"
"v_mfma_f32_16x16x32_bf16 v[192:195], a[44:47], a[140:143], v[192:195]\n"
"v_cvt_pk_bf16_f32 v25, v14, v15\n"
"v_mfma_f32_16x16x32_bf16 v[164:167], a[36:39], a[140:143], v[164:167]\n"
"v_lshlrev_b32 v12, 16, v18\n"
"v_and_b32 v13, s63, v18\n"
"v_mfma_f32_16x16x32_bf16 v[164:167], a[32:35], a[136:139], v[164:167]\n"
"v_lshlrev_b32 v14, 16, v19\n"
"v_mfma_f32_16x16x32_bf16 v[168:171], a[32:35], a[144:147], v[168:171]\n"
"v_and_b32 v15, s63, v19\n"
"v_mul_f32 v12, v12, v68\n"
"v_mfma_f32_16x16x32_bf16 v[168:171], a[36:39], a[148:151], v[168:171]\n"
"global_load_dwordx4 a[92:95], v0, s[34:35] offset:3072 sc1\n"
"v_mul_f32 v13, v13, v69\n"
"v_mfma_f32_16x16x32_bf16 v[196:199], a[44:47], a[148:151], v[196:199]\n"
"v_mul_f32 v14, v14, v70\n"
"v_mfma_f32_16x16x32_bf16 v[196:199], a[40:43], a[144:147], v[196:199]\n"
"v_mul_f32 v15, v15, v71\n"
"v_mfma_f32_16x16x32_bf16 a[20:23], a[40:43], a[152:155], a[20:23]\n"
"s_add_u32 s34, s34, 8192\n"
"v_cvt_pk_bf16_f32 v26, v12, v13\n"
"v_cvt_pk_bf16_f32 v27, v14, v15\n"
"v_mfma_f32_16x16x32_bf16 a[20:23], a[44:47], a[156:159], a[20:23]\n"
"global_store_dwordx4 v0, v[24:27], s[52:53] offset:0 sc1\n"
"ds_write_b128 v2, v[24:27] offset:4096\n"
"v_mfma_f32_16x16x32_bf16 a[16:19], a[36:39], a[156:159], a[16:19]\n"
"s_waitcnt lgkmcnt(1)\n"
"v_lshlrev_b32 v12, 16, v20\n"
"v_mfma_f32_16x16x32_bf16 a[16:19], a[32:35], a[152:155], a[16:19]\n"
"s_waitcnt vmcnt(11)\n"
"v_and_b32 v13, s63, v20\n"
"v_mfma_f32_16x16x32_bf16 v[200:203], a[48:51], a[96:99], v[200:203]\n"
"v_lshlrev_b32 v14, 16, v21\n"
"v_and_b32 v15, s63, v21\n"
"v_mfma_f32_16x16x32_bf16 v[200:203], a[52:55], a[100:103], v[200:203]\n"
"global_load_dwordx4 a[32:35], v0, s[34:35] offset:-4096 sc1\n"
"ds_read_b128 v[16:19], v7 offset:38912\n"
"v_mfma_f32_16x16x32_bf16 v[228:231], a[60:63], a[100:103], v[228:231]\n"
"ds_read_b128 a[100:103], v3 offset:17408\n"
"v_mul_f32 v12, v12, v96\n"
"v_mfma_f32_16x16x32_bf16 v[228:231], a[56:59], a[96:99], v[228:231]\n"
"ds_read_b128 a[96:99], v3 offset:16384\n"
"v_mul_f32 v13, v13, v97\n"
"v_mfma_f32_16x16x32_bf16 v[232:235], a[56:59], a[104:107], v[232:235]\n"
"v_mul_f32 v14, v14, v98\n"
"v_mfma_f32_16x16x32_bf16 v[232:235], a[60:63], a[108:111], v[232:235]\n"
"v_mul_f32 v15, v15, v99\n"
"v_cvt_pk_bf16_f32 v28, v12, v13\n"
"v_mfma_f32_16x16x32_bf16 v[204:207], a[52:55], a[108:111], v[204:207]\n"
"ds_read_b128 a[108:111], v3 offset:19456\n"
"v_cvt_pk_bf16_f32 v29, v14, v15\n"
"v_mfma_f32_16x16x32_bf16 v[204:207], a[48:51], a[104:107], v[204:207]\n"
"ds_read_b128 a[104:107], v3 offset:18432\n"
"v_lshlrev_b32 v12, 16, v22\n"
"v_mfma_f32_16x16x32_bf16 v[208:211], a[48:51], a[112:115], v[208:211]\n"
"v_and_b32 v13, s63, v22\n"
"v_lshlrev_b32 v14, 16, v23\n"
"v_mfma_f32_16x16x32_bf16 v[208:211], a[52:55], a[116:119], v[208:211]\n"
"global_load_dwordx4 a[36:39], v0, s[34:35] offset:-3072 sc1\n"
"v_and_b32 v15, s63, v23\n"
"v_mfma_f32_16x16x32_bf16 v[236:239], a[60:63], a[116:119], v[236:239]\n"
"ds_read_b128 a[116:119], v3 offset:21504\n"
"v_mul_f32 v12, v12, v124\n"
"v_mfma_f32_16x16x32_bf16 v[236:239], a[56:59], a[112:115], v[236:239]\n"
"ds_read_b128 a[112:115], v3 offset:20480\n"
"v_mul_f32 v13, v13, v125\n"
"v_mfma_f32_16x16x32_bf16 v[240:243], a[56:59], a[120:123], v[240:243]\n"
"v_mul_f32 v14, v14, v126\n"
"v_mul_f32 v15, v15, v127\n"
"v_mfma_f32_16x16x32_bf16 v[240:243], a[60:63], a[124:127], v[240:243]\n"
"v_cvt_pk_bf16_f32 v30, v12, v13\n"
"v_mfma_f32_16x16x32_bf16 v[212:215], a[52:55], a[124:127], v[212:215]\n"
"ds_read_b128 a[124:127], v3 offset:23552\n"
"v_cvt_pk_bf16_f32 v31, v14, v15\n"
"v_mfma_f32_16x16x32_bf16 v[212:215], a[48:51], a[120:123], v[212:215]\n"
"ds_read_b128 a[120:123], v3 offset:22528\n"
"v_mfma_f32_16x16x32_bf16 v[216:219], a[48:51], a[128:131], v[216:219]\n"
"global_store_dwordx4 v0, v[28:31], s[52:53] offset:1024 sc1\n"
"ds_write_b128 v2, v[28:31] offset:5120\n"
"v_mfma_f32_16x16x32_bf16 v[216:219], a[52:55], a[132:135], v[216:219]\n"
"s_waitcnt lgkmcnt(9)\n"
"global_load_dwordx4 a[40:43], v0, s[34:35] offset:-2048 sc1\n"
"v_lshlrev_b32 v12, 16, v16\n"
"v_mfma_f32_16x16x32_bf16 v[244:247], a[60:63], a[132:135], v[244:247]\n"
"ds_read_b128 a[132:135], v3 offset:25600\n"
"v_and_b32 v13, s63, v16\n"
"v_mfma_f32_16x16x32_bf16 v[244:247], a[56:59], a[128:131], v[244:247]\n"
"ds_read_b128 a[128:131], v3 offset:24576\n"
"v_lshlrev_b32 v14, 16, v17\n"
"v_mfma_f32_16x16x32_bf16 v[248:251], a[56:59], a[136:139], v[248:251]\n"
"v_and_b32 v15, s63, v17\n"
"v_xor_b32 v20, 64, v7\n"
"v_mfma_f32_16x16x32_bf16 v[248:251], a[60:63], a[140:143], v[248:251]\n"
"ds_read_b128 v[20:23], v20 offset:38912\n"
"v_mul_f32 v12, v12, v44\n"
"v_mfma_f32_16x16x32_bf16 v[220:223], a[52:55], a[140:143], v[220:223]\n"
"ds_read_b128 a[140:143], v3 offset:27648\n"
"v_mul_f32 v13, v13, v45\n"
"v_mfma_f32_16x16x32_bf16 v[220:223], a[48:51], a[136:139], v[220:223]\n"
"ds_read_b128 a[136:139], v3 offset:26624\n"
"v_mul_f32 v14, v14, v46\n"
"v_mfma_f32_16x16x32_bf16 v[224:227], a[48:51], a[144:147], v[224:227]\n"
"v_mul_f32 v15, v15, v47\n"
"v_cvt_pk_bf16_f32 v24, v12, v13\n"
"v_mfma_f32_16x16x32_bf16 v[224:227], a[52:55], a[148:151], v[224:227]\n"
"global_load_dwordx4 a[44:47], v0, s[34:35] offset:-1024 sc1\n"
"v_cvt_pk_bf16_f32 v25, v14, v15\n"
"v_mfma_f32_16x16x32_bf16 v[252:255], a[60:63], a[148:151], v[252:255]\n"
"ds_read_b128 a[148:151], v3 offset:29696\n"
"v_mfma_f32_16x16x32_bf16 v[252:255], a[56:59], a[144:147], v[252:255]\n"
"ds_read_b128 a[144:147], v3 offset:28672\n"
"v_mfma_f32_16x16x32_bf16 a[28:31], a[56:59], a[152:155], a[28:31]\n"
"ds_write_b64 v2, v[24:25] offset:6144\n"
"v_mfma_f32_16x16x32_bf16 a[28:31], a[60:63], a[156:159], a[28:31]\n"
"v_lshlrev_b32 v12, 16, v18\n"
"v_and_b32 v13, s63, v18\n"
"v_mfma_f32_16x16x32_bf16 a[24:27], a[52:55], a[156:159], a[24:27]\n"
"ds_read_b128 a[156:159], v3 offset:31744\n"
"v_lshlrev_b32 v14, 16, v19\n"
"v_mfma_f32_16x16x32_bf16 a[24:27], a[48:51], a[152:155], a[24:27]\n"
"s_waitcnt vmcnt(11) lgkmcnt(14)\n"
"ds_read_b128 a[152:155], v3 offset:30720\n"
"v_and_b32 v15, s63, v19\n"
"v_mfma_f32_16x16x32_bf16 v[144:147], a[64:67], a[96:99], v[144:147]\n"
"v_mul_f32 v12, v12, v72\n"
"v_mul_f32 v13, v13, v73\n"
"v_mfma_f32_16x16x32_bf16 v[144:147], a[68:71], a[100:103], v[144:147]\n"
"global_load_dwordx4 a[48:51], v0, s[34:35] offset:0 sc1\n"
"v_mul_f32 v14, v14, v74\n"
"v_mfma_f32_16x16x32_bf16 v[172:175], a[76:79], a[100:103], v[172:175]\n"
"v_mul_f32 v15, v15, v75\n"
"v_cvt_pk_bf16_f32 v26, v12, v13\n"
"v_mfma_f32_16x16x32_bf16 v[172:175], a[72:75], a[96:99], v[172:175]\n"
"v_cvt_pk_bf16_f32 v27, v14, v15\n"
"v_mfma_f32_16x16x32_bf16 v[176:179], a[72:75], a[104:107], v[176:179]\n"
"ds_write_b64 v2, v[26:27] offset:6152\n"
"v_mfma_f32_16x16x32_bf16 v[176:179], a[76:79], a[108:111], v[176:179]\n"
"s_waitcnt lgkmcnt(8)\n"
"v_lshlrev_b32 v12, 16, v20\n"
"v_and_b32 v13, s63, v20\n"
"v_mfma_f32_16x16x32_bf16 v[148:151], a[68:71], a[108:111], v[148:151]\n"
"v_lshlrev_b32 v14, 16, v21\n"
"v_and_b32 v15, s63, v21\n"
"v_mfma_f32_16x16x32_bf16 v[148:151], a[64:67], a[104:107], v[148:151]\n"
"v_mul_f32 v12, v12, v100\n"
"v_mul_f32 v13, v13, v101\n"
"v_mfma_f32_16x16x32_bf16 v[152:155], a[64:67], a[112:115], v[152:155]\n"
"v_mul_f32 v14, v14, v102\n"
"v_mul_f32 v15, v15, v103\n"
"v_mfma_f32_16x16x32_bf16 v[152:155], a[68:71], a[116:119], v[152:155]\n"
"global_load_dwordx4 a[52:55], v0, s[34:35] offset:1024 sc1\n"
"v_cvt_pk_bf16_f32 v28, v12, v13\n"
"v_mfma_f32_16x16x32_bf16 v[180:183], a[76:79], a[116:119], v[180:183]\n"
"v_cvt_pk_bf16_f32 v29, v14, v15\n"
"v_lshlrev_b32 v12, 16, v22\n"
"v_mfma_f32_16x16x32_bf16 v[180:183], a[72:75], a[112:115], v[180:183]\n"
"v_and_b32 v13, s63, v22\n"
"v_lshlrev_b32 v14, 16, v23\n"
"v_mfma_f32_16x16x32_bf16 v[184:187], a[72:75], a[120:123], v[184:187]\n"
"v_and_b32 v15, s63, v23\n"
"v_mul_f32 v12, v12, v128\n"
"v_mfma_f32_16x16x32_bf16 v[184:187], a[76:79], a[124:127], v[184:187]\n"
"v_mul_f32 v13, v13, v129\n"
"v_mfma_f32_16x16x32_bf16 v[156:159], a[68:71], a[124:127], v[156:159]\n"
"v_mul_f32 v14, v14, v130\n"
"v_mfma_f32_16x16x32_bf16 v[156:159], a[64:67], a[120:123], v[156:159]\n"
"s_waitcnt lgkmcnt(6)\n"
"v_mul_f32 v15, v15, v131\n"
"v_cvt_pk_bf16_f32 v30, v12, v13\n"
"v_mfma_f32_16x16x32_bf16 v[160:163], a[64:67], a[128:131], v[160:163]\n"
"v_cvt_pk_bf16_f32 v31, v14, v15\n"
"v_mfma_f32_16x16x32_bf16 v[160:163], a[68:71], a[132:135], v[160:163]\n"
"global_load_dwordx4 a[56:59], v0, s[34:35] offset:2048 sc1\n"
"v_mfma_f32_16x16x32_bf16 v[188:191], a[76:79], a[132:135], v[188:191]\n"
"global_store_dwordx4 v0, v[28:31], s[52:53] offset:3072 sc1\n"
"ds_write_b128 v2, v[28:31] offset:7168\n"
"v_mfma_f32_16x16x32_bf16 v[188:191], a[72:75], a[128:131], v[188:191]\n"
"v_accvgpr_read_b32 v16, a160\n"
"v_accvgpr_read_b32 v17, a161\n"
"v_mfma_f32_16x16x32_bf16 v[192:195], a[72:75], a[136:139], v[192:195]\n"
"v_accvgpr_read_b32 v18, a162\n"
"v_mfma_f32_16x16x32_bf16 v[192:195], a[76:79], a[140:143], v[192:195]\n"
"v_accvgpr_read_b32 v19, a163\n"
"v_lshlrev_b32 v12, 16, v16\n"
"v_mfma_f32_16x16x32_bf16 v[164:167], a[68:71], a[140:143], v[164:167]\n"
"v_and_b32 v13, s63, v16\n"
"v_mfma_f32_16x16x32_bf16 v[164:167], a[64:67], a[136:139], v[164:167]\n"
"s_waitcnt lgkmcnt(2)\n"
"v_lshlrev_b32 v14, 16, v17\n"
"v_mfma_f32_16x16x32_bf16 v[168:171], a[64:67], a[144:147], v[168:171]\n"
"v_and_b32 v15, s63, v17\n"
"v_mul_f32 v12, v12, v48\n"
"v_mfma_f32_16x16x32_bf16 v[168:171], a[68:71], a[148:151], v[168:171]\n"
"global_load_dwordx4 a[60:63], v0, s[34:35] offset:3072 sc1\n"
"v_mul_f32 v13, v13, v49\n"
"v_mfma_f32_16x16x32_bf16 v[196:199], a[76:79], a[148:151], v[196:199]\n"
"v_mul_f32 v14, v14, v50\n"
"v_mfma_f32_16x16x32_bf16 v[196:199], a[72:75], a[144:147], v[196:199]\n"
"v_mul_f32 v15, v15, v51\n"
"v_cvt_pk_bf16_f32 v24, v12, v13\n"
"v_mfma_f32_16x16x32_bf16 a[20:23], a[72:75], a[152:155], a[20:23]\n"
"s_add_u32 s34, s34, 8192\n"
"v_cvt_pk_bf16_f32 v25, v14, v15\n"
"v_mfma_f32_16x16x32_bf16 a[20:23], a[76:79], a[156:159], a[20:23]\n"
"v_lshlrev_b32 v12, 16, v18\n"
"v_mfma_f32_16x16x32_bf16 a[16:19], a[68:71], a[156:159], a[16:19]\n"
"v_and_b32 v13, s63, v18\n"
"v_lshlrev_b32 v14, 16, v19\n"
"v_mfma_f32_16x16x32_bf16 a[16:19], a[64:67], a[152:155], a[16:19]\n"
"s_waitcnt vmcnt(11)\n"
"v_and_b32 v15, s63, v19\n"
"v_mul_f32 v12, v12, v76\n"
"v_mfma_f32_16x16x32_bf16 v[200:203], a[80:83], a[96:99], v[200:203]\n"
"v_mul_f32 v13, v13, v77\n"
"v_mul_f32 v14, v14, v78\n"
"v_mfma_f32_16x16x32_bf16 v[200:203], a[84:87], a[100:103], v[200:203]\n"
"global_load_dwordx4 a[64:67], v0, s[34:35] offset:-4096 sc1\n"
"v_mul_f32 v15, v15, v79\n"
"v_mfma_f32_16x16x32_bf16 v[228:231], a[92:95], a[100:103], v[228:231]\n"
"ds_read_b128 a[100:103], v4 offset:17408\n"
"v_cvt_pk_bf16_f32 v26, v12, v13\n"
"v_mfma_f32_16x16x32_bf16 v[228:231], a[88:91], a[96:99], v[228:231]\n"
"ds_read_b128 a[96:99], v4 offset:16384\n"
"v_cvt_pk_bf16_f32 v27, v14, v15\n"
"v_mfma_f32_16x16x32_bf16 v[232:235], a[88:91], a[104:107], v[232:235]\n"
"global_store_dwordx4 v0, v[24:27], s[54:55] offset:-4096 sc1\n"
"ds_write_b128 v2, v[24:27] offset:8192\n"
"v_mfma_f32_16x16x32_bf16 v[232:235], a[92:95], a[108:111], v[232:235]\n"
"v_accvgpr_read_b32 v16, a164\n"
"v_mfma_f32_16x16x32_bf16 v[204:207], a[84:87], a[108:111], v[204:207]\n"
"ds_read_b128 a[108:111], v4 offset:19456\n"
"v_accvgpr_read_b32 v17, a165\n"
"v_mfma_f32_16x16x32_bf16 v[204:207], a[80:83], a[104:107], v[204:207]\n"
"ds_read_b128 a[104:107], v4 offset:18432\n"
"v_accvgpr_read_b32 v18, a166\n"
"v_mfma_f32_16x16x32_bf16 v[208:211], a[80:83], a[112:115], v[208:211]\n"
"v_accvgpr_read_b32 v19, a167\n"
"v_lshlrev_b32 v12, 16, v16\n"
"v_mfma_f32_16x16x32_bf16 v[208:211], a[84:87], a[116:119], v[208:211]\n"
"global_load_dwordx4 a[68:71], v0, s[34:35] offset:-3072 sc1\n"
"v_and_b32 v13, s63, v16\n"
"v_mfma_f32_16x16x32_bf16 v[236:239], a[92:95], a[116:119], v[236:239]\n"
"ds_read_b128 a[116:119], v4 offset:21504\n"
"v_lshlrev_b32 v14, 16, v17\n"
"v_mfma_f32_16x16x32_bf16 v[236:239], a[88:91], a[112:115], v[236:239]\n"
"ds_read_b128 a[112:115], v4 offset:20480\n"
"v_and_b32 v15, s63, v17\n"
"v_mfma_f32_16x16x32_bf16 v[240:243], a[88:91], a[120:123], v[240:243]\n"
"v_mul_f32 v12, v12, v104\n"
"v_mul_f32 v13, v13, v105\n"
"v_mfma_f32_16x16x32_bf16 v[240:243], a[92:95], a[124:127], v[240:243]\n"
"v_mul_f32 v14, v14, v106\n"
"v_mul_f32 v15, v15, v107\n"
"v_mfma_f32_16x16x32_bf16 v[212:215], a[84:87], a[124:127], v[212:215]\n"
"ds_read_b128 a[124:127], v4 offset:23552\n"
"v_cvt_pk_bf16_f32 v28, v12, v13\n"
"v_mfma_f32_16x16x32_bf16 v[212:215], a[80:83], a[120:123], v[212:215]\n"
"ds_read_b128 a[120:123], v4 offset:22528\n"
"v_cvt_pk_bf16_f32 v29, v14, v15\n"
"v_mfma_f32_16x16x32_bf16 v[216:219], a[80:83], a[128:131], v[216:219]\n"
"ds_write_b64 v2, v[28:29] offset:9216\n"
"v_mfma_f32_16x16x32_bf16 v[216:219], a[84:87], a[132:135], v[216:219]\n"
"global_load_dwordx4 a[72:75], v0, s[34:35] offset:-2048 sc1\n"
"v_lshlrev_b32 v12, 16, v18\n"
"v_mfma_f32_16x16x32_bf16 v[244:247], a[92:95], a[132:135], v[244:247]\n"
"ds_read_b128 a[132:135], v4 offset:25600\n"
"v_and_b32 v13, s63, v18\n"
"v_mfma_f32_16x16x32_bf16 v[244:247], a[88:91], a[128:131], v[244:247]\n"
"ds_read_b128 a[128:131], v4 offset:24576\n"
"v_lshlrev_b32 v14, 16, v19\n"
"v_mfma_f32_16x16x32_bf16 v[248:251], a[88:91], a[136:139], v[248:251]\n"
"v_and_b32 v15, s63, v19\n"
"v_mul_f32 v12, v12, v132\n"
"v_mfma_f32_16x16x32_bf16 v[248:251], a[92:95], a[140:143], v[248:251]\n"
"v_mul_f32 v13, v13, v133\n"
"v_mfma_f32_16x16x32_bf16 v[220:223], a[84:87], a[140:143], v[220:223]\n"
"ds_read_b128 a[140:143], v4 offset:27648\n"
"v_mul_f32 v14, v14, v134\n"
"v_mfma_f32_16x16x32_bf16 v[220:223], a[80:83], a[136:139], v[220:223]\n"
"ds_read_b128 a[136:139], v4 offset:26624\n"
"v_mul_f32 v15, v15, v135\n"
"v_mfma_f32_16x16x32_bf16 v[224:227], a[80:83], a[144:147], v[224:227]\n"
"v_cvt_pk_bf16_f32 v30, v12, v13\n"
"v_cvt_pk_bf16_f32 v31, v14, v15\n"
"v_mfma_f32_16x16x32_bf16 v[224:227], a[84:87], a[148:151], v[224:227]\n"
"global_load_dwordx4 a[76:79], v0, s[34:35] offset:-1024 sc1\n"
"v_mfma_f32_16x16x32_bf16 v[252:255], a[92:95], a[148:151], v[252:255]\n"
"ds_read_b128 a[148:151], v4 offset:29696\n"
"v_mfma_f32_16x16x32_bf16 v[252:255], a[88:91], a[144:147], v[252:255]\n"
"ds_read_b128 a[144:147], v4 offset:28672\n"
"v_mfma_f32_16x16x32_bf16 a[28:31], a[88:91], a[152:155], a[28:31]\n"
"ds_write_b64 v2, v[30:31] offset:9224\n"
"v_mfma_f32_16x16x32_bf16 a[28:31], a[92:95], a[156:159], a[28:31]\n"
"v_accvgpr_read_b32 v16, a168\n"
"v_accvgpr_read_b32 v17, a169\n"
"v_mfma_f32_16x16x32_bf16 a[24:27], a[84:87], a[156:159], a[24:27]\n"
"ds_read_b128 a[156:159], v4 offset:31744\n"
"v_accvgpr_read_b32 v18, a170\n"
"v_mfma_f32_16x16x32_bf16 a[24:27], a[80:83], a[152:155], a[24:27]\n"
"s_waitcnt vmcnt(10) lgkmcnt(13)\n"
"ds_read_b128 a[152:155], v4 offset:30720\n"
"v_accvgpr_read_b32 v19, a171\n"
"v_mfma_f32_16x16x32_bf16 v[144:147], a[32:35], a[96:99], v[144:147]\n"
"v_lshlrev_b32 v12, 16, v16\n"
"v_and_b32 v13, s63, v16\n"
"v_mfma_f32_16x16x32_bf16 v[144:147], a[36:39], a[100:103], v[144:147]\n"
"global_load_dwordx4 a[80:83], v0, s[34:35] offset:0 sc1\n"
"v_lshlrev_b32 v14, 16, v17\n"
"v_mfma_f32_16x16x32_bf16 v[172:175], a[44:47], a[100:103], v[172:175]\n"
"v_and_b32 v15, s63, v17\n"
"v_mul_f32 v12, v12, v52\n"
"v_mfma_f32_16x16x32_bf16 v[172:175], a[40:43], a[96:99], v[172:175]\n"
"v_mul_f32 v13, v13, v53\n"
"v_mul_f32 v14, v14, v54\n"
"v_mfma_f32_16x16x32_bf16 v[176:179], a[40:43], a[104:107], v[176:179]\n"
"v_mul_f32 v15, v15, v55\n"
"v_cvt_pk_bf16_f32 v24, v12, v13\n"
"v_mfma_f32_16x16x32_bf16 v[176:179], a[44:47], a[108:111], v[176:179]\n"
"v_cvt_pk_bf16_f32 v25, v14, v15\n"
"v_lshlrev_b32 v12, 16, v18\n"
"v_mfma_f32_16x16x32_bf16 v[148:151], a[36:39], a[108:111], v[148:151]\n"
"v_and_b32 v13, s63, v18\n"
"v_lshlrev_b32 v14, 16, v19\n"
"v_mfma_f32_16x16x32_bf16 v[148:151], a[32:35], a[104:107], v[148:151]\n"
"s_waitcnt lgkmcnt(10)\n"
"v_and_b32 v15, s63, v19\n"
"v_mul_f32 v12, v12, v80\n"
"v_mfma_f32_16x16x32_bf16 v[152:155], a[32:35], a[112:115], v[152:155]\n"
"v_mul_f32 v13, v13, v81\n"
"v_mul_f32 v14, v14, v82\n"
"v_mfma_f32_16x16x32_bf16 v[152:155], a[36:39], a[116:119], v[152:155]\n"
"global_load_dwordx4 a[84:87], v0, s[34:35] offset:1024 sc1\n"
"v_mul_f32 v15, v15, v83\n"
"v_mfma_f32_16x16x32_bf16 v[180:183], a[44:47], a[116:119], v[180:183]\n"
"v_cvt_pk_bf16_f32 v26, v12, v13\n"
"v_mfma_f32_16x16x32_bf16 v[180:183], a[40:43], a[112:115], v[180:183]\n"
"v_cvt_pk_bf16_f32 v27, v14, v15\n"
"v_mfma_f32_16x16x32_bf16 v[184:187], a[40:43], a[120:123], v[184:187]\n"
"global_store_dwordx4 v0, v[24:27], s[54:55] offset:-2048 sc1\n"
"ds_write_b128 v2, v[24:27] offset:10240\n"
"v_mfma_f32_16x16x32_bf16 v[184:187], a[44:47], a[124:127], v[184:187]\n"
"v_accvgpr_read_b32 v16, a172\n"
"v_mfma_f32_16x16x32_bf16 v[156:159], a[36:39], a[124:127], v[156:159]\n"
"v_accvgpr_read_b32 v17, a173\n"
"v_mfma_f32_16x16x32_bf16 v[156:159], a[32:35], a[120:123], v[156:159]\n"
"s_waitcnt lgkmcnt(6)\n"
"v_accvgpr_read_b32 v18, a174\n"
"v_accvgpr_read_b32 v19, a175\n"
"v_mfma_f32_16x16x32_bf16 v[160:163], a[32:35], a[128:131], v[160:163]\n"
"v_lshlrev_b32 v12, 16, v16\n"
"v_mfma_f32_16x16x32_bf16 v[160:163], a[36:39], a[132:135], v[160:163]\n"
"global_load_dwordx4 a[88:91], v0, s[34:35] offset:2048 sc1\n"
"v_and_b32 v13, s63, v16\n"
"v_mfma_f32_16x16x32_bf16 v[188:191], a[44:47], a[132:135], v[188:191]\n"
"v_lshlrev_b32 v14, 16, v17\n"
"v_and_b32 v15, s63, v17\n"
"v_mfma_f32_16x16x32_bf16 v[188:191], a[40:43], a[128:131], v[188:191]\n"
"v_mul_f32 v12, v12, v108\n"
"v_mfma_f32_16x16x32_bf16 v[192:195], a[40:43], a[136:139], v[192:195]\n"
"v_mul_f32 v13, v13, v109\n"
"v_mfma_f32_16x16x32_bf16 v[192:195], a[44:47], a[140:143], v[192:195]\n"
"v_mul_f32 v14, v14, v110\n"
"v_mul_f32 v15, v15, v111\n"
"v_mfma_f32_16x16x32_bf16 v[164:167], a[36:39], a[140:143], v[164:167]\n"
"v_cvt_pk_bf16_f32 v28, v12, v13\n"
"v_mfma_f32_16x16x32_bf16 v[164:167], a[32:35], a[136:139], v[164:167]\n"
"s_waitcnt lgkmcnt(1)\n"
"v_cvt_pk_bf16_f32 v29, v14, v15\n"
"v_mfma_f32_16x16x32_bf16 v[168:171], a[32:35], a[144:147], v[168:171]\n"
"v_lshlrev_b32 v12, 16, v18\n"
"v_and_b32 v13, s63, v18\n"
"v_mfma_f32_16x16x32_bf16 v[168:171], a[36:39], a[148:151], v[168:171]\n"
"global_load_dwordx4 a[92:95], v0, s[34:35] offset:3072 sc1\n"
"v_lshlrev_b32 v14, 16, v19\n"
"v_mfma_f32_16x16x32_bf16 v[196:199], a[44:47], a[148:151], v[196:199]\n"
"v_and_b32 v15, s63, v19\n"
"v_mfma_f32_16x16x32_bf16 v[196:199], a[40:43], a[144:147], v[196:199]\n"
"v_mul_f32 v12, v12, v136\n"
"v_mul_f32 v13, v13, v137\n"
"v_mfma_f32_16x16x32_bf16 a[20:23], a[40:43], a[152:155], a[20:23]\n"
"s_add_u32 s34, s34, 8192\n"
"v_mul_f32 v14, v14, v138\n"
"v_mfma_f32_16x16x32_bf16 a[20:23], a[44:47], a[156:159], a[20:23]\n"
"v_mul_f32 v15, v15, v139\n"
"v_mfma_f32_16x16x32_bf16 a[16:19], a[36:39], a[156:159], a[16:19]\n"
"v_cvt_pk_bf16_f32 v30, v12, v13\n"
"v_cvt_pk_bf16_f32 v31, v14, v15\n"
"v_mfma_f32_16x16x32_bf16 a[16:19], a[32:35], a[152:155], a[16:19]\n"
"s_waitcnt vmcnt(10)\n"
"global_store_dwordx4 v0, v[28:31], s[54:55] offset:-1024 sc1\n"
"ds_write_b128 v2, v[28:31] offset:11264\n"
"v_mfma_f32_16x16x32_bf16 v[200:203], a[48:51], a[96:99], v[200:203]\n"
"v_accvgpr_read_b32 v16, a176\n"
"v_mfma_f32_16x16x32_bf16 v[200:203], a[52:55], a[100:103], v[200:203]\n"
"global_load_dwordx4 a[32:35], v0, s[34:35] offset:-4096 sc1\n"
"v_accvgpr_read_b32 v17, a177\n"
"v_mfma_f32_16x16x32_bf16 v[228:231], a[60:63], a[100:103], v[228:231]\n"
"ds_read_b128 a[100:103], v5 offset:17408\n"
"v_accvgpr_read_b32 v18, a178\n"
"v_mfma_f32_16x16x32_bf16 v[228:231], a[56:59], a[96:99], v[228:231]\n"
"ds_read_b128 a[96:99], v5 offset:16384\n"
"v_accvgpr_read_b32 v19, a179\n"
"v_mfma_f32_16x16x32_bf16 v[232:235], a[56:59], a[104:107], v[232:235]\n"
"v_lshlrev_b32 v12, 16, v16\n"
"v_and_b32 v13, s63, v16\n"
"v_mfma_f32_16x16x32_bf16 v[232:235], a[60:63], a[108:111], v[232:235]\n"
"v_lshlrev_b32 v14, 16, v17\n"
"v_mfma_f32_16x16x32_bf16 v[204:207], a[52:55], a[108:111], v[204:207]\n"
"ds_read_b128 a[108:111], v5 offset:19456\n"
"v_and_b32 v15, s63, v17\n"
"v_mfma_f32_16x16x32_bf16 v[204:207], a[48:51], a[104:107], v[204:207]\n"
"ds_read_b128 a[104:107], v5 offset:18432\n"
"v_mul_f32 v12, v12, v56\n"
"v_mfma_f32_16x16x32_bf16 v[208:211], a[48:51], a[112:115], v[208:211]\n"
"v_mul_f32 v13, v13, v57\n"
"v_mul_f32 v14, v14, v58\n"
"v_mfma_f32_16x16x32_bf16 v[208:211], a[52:55], a[116:119], v[208:211]\n"
"global_load_dwordx4 a[36:39], v0, s[34:35] offset:-3072 sc1\n"
"v_mul_f32 v15, v15, v59\n"
"v_mfma_f32_16x16x32_bf16 v[236:239], a[60:63], a[116:119], v[236:239]\n"
"ds_read_b128 a[116:119], v5 offset:21504\n"
"v_cvt_pk_bf16_f32 v24, v12, v13\n"
"v_mfma_f32_16x16x32_bf16 v[236:239], a[56:59], a[112:115], v[236:239]\n"
"ds_read_b128 a[112:115], v5 offset:20480\n"
"v_cvt_pk_bf16_f32 v25, v14, v15\n"
"v_mfma_f32_16x16x32_bf16 v[240:243], a[56:59], a[120:123], v[240:243]\n"
"ds_write_b64 v2, v[24:25] offset:12288\n"
"v_mfma_f32_16x16x32_bf16 v[240:243], a[60:63], a[124:127], v[240:243]\n"
"v_lshlrev_b32 v12, 16, v18\n"
"v_and_b32 v13, s63, v18\n"
"v_mfma_f32_16x16x32_bf16 v[212:215], a[52:55], a[124:127], v[212:215]\n"
"ds_read_b128 a[124:127], v5 offset:23552\n"
"v_lshlrev_b32 v14, 16, v19\n"
"v_mfma_f32_16x16x32_bf16 v[212:215], a[48:51], a[120:123], v[212:215]\n"
"ds_read_b128 a[120:123], v5 offset:22528\n"
"v_and_b32 v15, s63, v19\n"
"v_mfma_f32_16x16x32_bf16 v[216:219], a[48:51], a[128:131], v[216:219]\n"
"v_mul_f32 v12, v12, v84\n"
"v_mul_f32 v13, v13, v85\n"
"v_mfma_f32_16x16x32_bf16 v[216:219], a[52:55], a[132:135], v[216:219]\n"
"global_load_dwordx4 a[40:43], v0, s[34:35] offset:-2048 sc1\n"
"v_mul_f32 v14, v14, v86\n"
"v_mfma_f32_16x16x32_bf16 v[244:247], a[60:63], a[132:135], v[244:247]\n"
"ds_read_b128 a[132:135], v5 offset:25600\n"
"v_mul_f32 v15, v15, v87\n"
"v_mfma_f32_16x16x32_bf16 v[244:247], a[56:59], a[128:131], v[244:247]\n"
"ds_read_b128 a[128:131], v5 offset:24576\n"
"v_cvt_pk_bf16_f32 v26, v12, v13\n"
"v_mfma_f32_16x16x32_bf16 v[248:251], a[56:59], a[136:139], v[248:251]\n"
"v_cvt_pk_bf16_f32 v27, v14, v15\n"
"v_mfma_f32_16x16x32_bf16 v[248:251], a[60:63], a[140:143], v[248:251]\n"
"ds_write_b64 v2, v[26:27] offset:12296\n"
"v_mfma_f32_16x16x32_bf16 v[220:223], a[52:55], a[140:143], v[220:223]\n"
"ds_read_b128 a[140:143], v5 offset:27648\n"
"v_accvgpr_read_b32 v16, a180\n"
"v_mfma_f32_16x16x32_bf16 v[220:223], a[48:51], a[136:139], v[220:223]\n"
"ds_read_b128 a[136:139], v5 offset:26624\n"
"v_accvgpr_read_b32 v17, a181\n"
"v_mfma_f32_16x16x32_bf16 v[224:227], a[48:51], a[144:147], v[224:227]\n"
"v_accvgpr_read_b32 v18, a182\n"
"v_accvgpr_read_b32 v19, a183\n"
"v_mfma_f32_16x16x32_bf16 v[224:227], a[52:55], a[148:151], v[224:227]\n"
"global_load_dwordx4 a[44:47], v0, s[34:35] offset:-1024 sc1\n"
"v_lshlrev_b32 v12, 16, v16\n"
"v_mfma_f32_16x16x32_bf16 v[252:255], a[60:63], a[148:151], v[252:255]\n"
"ds_read_b128 a[148:151], v5 offset:29696\n"
"v_and_b32 v13, s63, v16\n"
"v_mfma_f32_16x16x32_bf16 v[252:255], a[56:59], a[144:147], v[252:255]\n"
"ds_read_b128 a[144:147], v5 offset:28672\n"
"v_lshlrev_b32 v14, 16, v17\n"
"v_mfma_f32_16x16x32_bf16 a[28:31], a[56:59], a[152:155], a[28:31]\n"
"v_and_b32 v15, s63, v17\n"
"v_mul_f32 v12, v12, v112\n"
"v_mfma_f32_16x16x32_bf16 a[28:31], a[60:63], a[156:159], a[28:31]\n"
"v_mul_f32 v13, v13, v113\n"
"v_mul_f32 v14, v14, v114\n"
"v_mfma_f32_16x16x32_bf16 a[24:27], a[52:55], a[156:159], a[24:27]\n"
"ds_read_b128 a[156:159], v5 offset:31744\n"
"v_mul_f32 v15, v15, v115\n"
"v_mfma_f32_16x16x32_bf16 a[24:27], a[48:51], a[152:155], a[24:27]\n"
"s_waitcnt vmcnt(10) lgkmcnt(13)\n"
"ds_read_b128 a[152:155], v5 offset:30720\n"
"v_cvt_pk_bf16_f32 v28, v12, v13\n"
"v_mfma_f32_16x16x32_bf16 v[144:147], a[64:67], a[96:99], v[144:147]\n"
"v_cvt_pk_bf16_f32 v29, v14, v15\n"
"v_lshlrev_b32 v12, 16, v18\n"
"v_mfma_f32_16x16x32_bf16 v[144:147], a[68:71], a[100:103], v[144:147]\n"
"global_load_dwordx4 a[48:51], v0, s[34:35] offset:0 sc1\n"
"v_and_b32 v13, s63, v18\n"
"v_mfma_f32_16x16x32_bf16 v[172:175], a[76:79], a[100:103], v[172:175]\n"
"v_lshlrev_b32 v14, 16, v19\n"
"v_and_b32 v15, s63, v19\n"
"v_mfma_f32_16x16x32_bf16 v[172:175], a[72:75], a[96:99], v[172:175]\n"
"v_mul_f32 v12, v12, v140\n"
"v_mul_f32 v13, v13, v141\n"
"v_mfma_f32_16x16x32_bf16 v[176:179], a[72:75], a[104:107], v[176:179]\n"
"v_mul_f32 v14, v14, v142\n"
"v_mul_f32 v15, v15, v143\n"
"v_mfma_f32_16x16x32_bf16 v[176:179], a[76:79], a[108:111], v[176:179]\n"
"v_cvt_pk_bf16_f32 v30, v12, v13\n"
"v_mfma_f32_16x16x32_bf16 v[148:151], a[68:71], a[108:111], v[148:151]\n"
"v_cvt_pk_bf16_f32 v31, v14, v15\n"
"v_mfma_f32_16x16x32_bf16 v[148:151], a[64:67], a[104:107], v[148:151]\n"
"s_waitcnt lgkmcnt(9)\n"
"global_store_dwordx4 v0, v[28:31], s[54:55] offset:1024 sc1\n"
"ds_write_b128 v2, v[28:31] offset:13312\n"
"v_mfma_f32_16x16x32_bf16 v[152:155], a[64:67], a[112:115], v[152:155]\n"
"v_accvgpr_read_b32 v16, a184\n"
"v_accvgpr_read_b32 v17, a185\n"
"v_mfma_f32_16x16x32_bf16 v[152:155], a[68:71], a[116:119], v[152:155]\n"
"global_load_dwordx4 a[52:55], v0, s[34:35] offset:1024 sc1\n"
"v_accvgpr_read_b32 v18, a186\n"
"v_mfma_f32_16x16x32_bf16 v[180:183], a[76:79], a[116:119], v[180:183]\n"
"v_accvgpr_read_b32 v19, a187\n"
"v_mfma_f32_16x16x32_bf16 v[180:183], a[72:75], a[112:115], v[180:183]\n"
"v_lshlrev_b32 v12, 16, v16\n"
"v_mfma_f32_16x16x32_bf16 v[184:187], a[72:75], a[120:123], v[184:187]\n"
"v_and_b32 v13, s63, v16\n"
"v_lshlrev_b32 v14, 16, v17\n"
"v_mfma_f32_16x16x32_bf16 v[184:187], a[76:79], a[124:127], v[184:187]\n"
"v_and_b32 v15, s63, v17\n"
"v_mfma_f32_16x16x32_bf16 v[156:159], a[68:71], a[124:127], v[156:159]\n"
"v_accvgpr_read_b32 v10, a0\n"
"v_mfma_f32_16x16x32_bf16 v[156:159], a[64:67], a[120:123], v[156:159]\n"
"s_waitcnt lgkmcnt(5)\n"
"v_accvgpr_read_b32 v11, a1\n"
"v_mul_f32 v12, v12, v10\n"
"v_mfma_f32_16x16x32_bf16 v[160:163], a[64:67], a[128:131], v[160:163]\n"
"v_mul_f32 v13, v13, v11\n"
"v_mfma_f32_16x16x32_bf16 v[160:163], a[68:71], a[132:135], v[160:163]\n"
"global_load_dwordx4 a[56:59], v0, s[34:35] offset:2048 sc1\n"
"v_accvgpr_read_b32 v10, a2\n"
"v_mfma_f32_16x16x32_bf16 v[188:191], a[76:79], a[132:135], v[188:191]\n"
"v_accvgpr_read_b32 v11, a3\n"
"v_mul_f32 v14, v14, v10\n"
"v_mfma_f32_16x16x32_bf16 v[188:191], a[72:75], a[128:131], v[188:191]\n"
"v_mul_f32 v15, v15, v11\n"
"v_mfma_f32_16x16x32_bf16 v[192:195], a[72:75], a[136:139], v[192:195]\n"
"v_cvt_pk_bf16_f32 v24, v12, v13\n"
"v_mfma_f32_16x16x32_bf16 v[192:195], a[76:79], a[140:143], v[192:195]\n"
"v_cvt_pk_bf16_f32 v25, v14, v15\n"
"v_lshlrev_b32 v12, 16, v18\n"
"v_mfma_f32_16x16x32_bf16 v[164:167], a[68:71], a[140:143], v[164:167]\n"
"v_and_b32 v13, s63, v18\n"
"v_mfma_f32_16x16x32_bf16 v[164:167], a[64:67], a[136:139], v[164:167]\n"
"s_waitcnt lgkmcnt(1)\n"
"v_lshlrev_b32 v14, 16, v19\n"
"v_mfma_f32_16x16x32_bf16 v[168:171], a[64:67], a[144:147], v[168:171]\n"
"v_and_b32 v15, s63, v19\n"
"v_accvgpr_read_b32 v10, a4\n"
"v_mfma_f32_16x16x32_bf16 v[168:171], a[68:71], a[148:151], v[168:171]\n"
"global_load_dwordx4 a[60:63], v0, s[34:35] offset:3072 sc1\n"
"v_accvgpr_read_b32 v11, a5\n"
"v_mfma_f32_16x16x32_bf16 v[196:199], a[76:79], a[148:151], v[196:199]\n"
"v_mul_f32 v12, v12, v10\n"
"v_mfma_f32_16x16x32_bf16 v[196:199], a[72:75], a[144:147], v[196:199]\n"
"v_mul_f32 v13, v13, v11\n"
"v_accvgpr_read_b32 v10, a6\n"
"v_mfma_f32_16x16x32_bf16 a[20:23], a[72:75], a[152:155], a[20:23]\n"
"s_add_u32 s34, s34, 8192\n"
"v_accvgpr_read_b32 v11, a7\n"
"v_mfma_f32_16x16x32_bf16 a[20:23], a[76:79], a[156:159], a[20:23]\n"
"v_mul_f32 v14, v14, v10\n"
"v_mul_f32 v15, v15, v11\n"
"v_mfma_f32_16x16x32_bf16 a[16:19], a[68:71], a[156:159], a[16:19]\n"
"v_cvt_pk_bf16_f32 v26, v12, v13\n"
"v_cvt_pk_bf16_f32 v27, v14, v15\n"
"v_mfma_f32_16x16x32_bf16 a[16:19], a[64:67], a[152:155], a[16:19]\n"
"s_waitcnt vmcnt(10)\n"
"global_store_dwordx4 v0, v[24:27], s[54:55] offset:2048 sc1\n"
"ds_write_b128 v2, v[24:27] offset:14336\n"
"v_mfma_f32_16x16x32_bf16 v[200:203], a[80:83], a[96:99], v[200:203]\n"
"v_accvgpr_read_b32 v16, a188\n"
"v_mfma_f32_16x16x32_bf16 v[200:203], a[84:87], a[100:103], v[200:203]\n"
"global_load_dwordx4 a[64:67], v0, s[34:35] offset:-4096 sc1\n"
"v_accvgpr_read_b32 v17, a189\n"
"v_mfma_f32_16x16x32_bf16 v[228:231], a[92:95], a[100:103], v[228:231]\n"
"ds_read_b128 a[100:103], v2 offset:1024\n"
"v_accvgpr_read_b32 v18, a190\n"
"v_mfma_f32_16x16x32_bf16 v[228:231], a[88:91], a[96:99], v[228:231]\n"
"ds_read_b128 a[96:99], v2 offset:0\n"
"v_accvgpr_read_b32 v19, a191\n"
"v_mfma_f32_16x16x32_bf16 v[232:235], a[88:91], a[104:107], v[232:235]\n"
"v_lshlrev_b32 v12, 16, v16\n"
"v_and_b32 v13, s63, v16\n"
"v_mfma_f32_16x16x32_bf16 v[232:235], a[92:95], a[108:111], v[232:235]\n"
"v_lshlrev_b32 v14, 16, v17\n"
"v_and_b32 v15, s63, v17\n"
"v_mfma_f32_16x16x32_bf16 v[204:207], a[84:87], a[108:111], v[204:207]\n"
"ds_read_b128 a[108:111], v2 offset:3072\n"
"v_accvgpr_read_b32 v10, a8\n"
"v_mfma_f32_16x16x32_bf16 v[204:207], a[80:83], a[104:107], v[204:207]\n"
"ds_read_b128 a[104:107], v2 offset:2048\n"
"v_accvgpr_read_b32 v11, a9\n"
"v_mfma_f32_16x16x32_bf16 v[208:211], a[80:83], a[112:115], v[208:211]\n"
"v_mul_f32 v12, v12, v10\n"
"v_mul_f32 v13, v13, v11\n"
"v_mfma_f32_16x16x32_bf16 v[208:211], a[84:87], a[116:119], v[208:211]\n"
"global_load_dwordx4 a[68:71], v0, s[34:35] offset:-3072 sc1\n"
"v_accvgpr_read_b32 v10, a10\n"
"v_mfma_f32_16x16x32_bf16 v[236:239], a[92:95], a[116:119], v[236:239]\n"
"ds_read_b128 a[116:119], v2 offset:5120\n"
"v_accvgpr_read_b32 v11, a11\n"
"v_mfma_f32_16x16x32_bf16 v[236:239], a[88:91], a[112:115], v[236:239]\n"
"ds_read_b128 a[112:115], v2 offset:4096\n"
"v_mul_f32 v14, v14, v10\n"
"v_mfma_f32_16x16x32_bf16 v[240:243], a[88:91], a[120:123], v[240:243]\n"
"v_mul_f32 v15, v15, v11\n"
"v_cvt_pk_bf16_f32 v28, v12, v13\n"
"v_mfma_f32_16x16x32_bf16 v[240:243], a[92:95], a[124:127], v[240:243]\n"
"v_cvt_pk_bf16_f32 v29, v14, v15\n"
"v_lshlrev_b32 v12, 16, v18\n"
"v_mfma_f32_16x16x32_bf16 v[212:215], a[84:87], a[124:127], v[212:215]\n"
"ds_read_b128 a[124:127], v2 offset:7168\n"
"v_and_b32 v13, s63, v18\n"
"v_mfma_f32_16x16x32_bf16 v[212:215], a[80:83], a[120:123], v[212:215]\n"
"ds_read_b128 a[120:123], v2 offset:6144\n"
"v_lshlrev_b32 v14, 16, v19\n"
"v_mfma_f32_16x16x32_bf16 v[216:219], a[80:83], a[128:131], v[216:219]\n"
"v_and_b32 v15, s63, v19\n"
"v_accvgpr_read_b32 v10, a12\n"
"v_mfma_f32_16x16x32_bf16 v[216:219], a[84:87], a[132:135], v[216:219]\n"
"global_load_dwordx4 a[72:75], v0, s[34:35] offset:-2048 sc1\n"
"v_accvgpr_read_b32 v11, a13\n"
"v_mfma_f32_16x16x32_bf16 v[244:247], a[92:95], a[132:135], v[244:247]\n"
"ds_read_b128 a[132:135], v2 offset:9216\n"
"v_mul_f32 v12, v12, v10\n"
"v_mfma_f32_16x16x32_bf16 v[244:247], a[88:91], a[128:131], v[244:247]\n"
"ds_read_b128 a[128:131], v2 offset:8192\n"
"v_mul_f32 v13, v13, v11\n"
"v_mfma_f32_16x16x32_bf16 v[248:251], a[88:91], a[136:139], v[248:251]\n"
"v_accvgpr_read_b32 v10, a14\n"
"v_accvgpr_read_b32 v11, a15\n"
"v_mfma_f32_16x16x32_bf16 v[248:251], a[92:95], a[140:143], v[248:251]\n"
"v_mul_f32 v14, v14, v10\n"
"v_mul_f32 v15, v15, v11\n"
"v_mfma_f32_16x16x32_bf16 v[220:223], a[84:87], a[140:143], v[220:223]\n"
"ds_read_b128 a[140:143], v2 offset:11264\n"
"v_cvt_pk_bf16_f32 v30, v12, v13\n"
"v_mfma_f32_16x16x32_bf16 v[220:223], a[80:83], a[136:139], v[220:223]\n"
"ds_read_b128 a[136:139], v2 offset:10240\n"
"v_cvt_pk_bf16_f32 v31, v14, v15\n"
"v_mfma_f32_16x16x32_bf16 v[224:227], a[80:83], a[144:147], v[224:227]\n"
"global_store_dwordx4 v0, v[28:31], s[54:55] offset:3072 sc1\n"
"ds_write_b128 v2, v[28:31] offset:15360\n"
"v_mfma_f32_16x16x32_bf16 v[224:227], a[84:87], a[148:151], v[224:227]\n"
"global_load_dwordx4 a[76:79], v0, s[34:35] offset:-1024 sc1\n"
"v_mfma_f32_16x16x32_bf16 v[252:255], a[92:95], a[148:151], v[252:255]\n"
"ds_read_b128 a[148:151], v2 offset:13312\n"
"v_mfma_f32_16x16x32_bf16 v[252:255], a[88:91], a[144:147], v[252:255]\n"
"ds_read_b128 a[144:147], v2 offset:12288\n"
"v_mfma_f32_16x16x32_bf16 a[28:31], a[88:91], a[152:155], a[28:31]\n"
"s_add_u32 s62, s62, 1\n"
"v_mfma_f32_16x16x32_bf16 a[28:31], a[92:95], a[156:159], a[28:31]\n"
"v_mfma_f32_16x16x32_bf16 a[24:27], a[84:87], a[156:159], a[24:27]\n"
"ds_read_b128 a[156:159], v2 offset:15360\n"
"v_mfma_f32_16x16x32_bf16 a[24:27], a[80:83], a[152:155], a[24:27]\n"
"ds_read_b128 a[152:155], v2 offset:14336\n"
"L_layer:\n"
"s_waitcnt vmcnt(11) lgkmcnt(13)\n"
"v_mfma_f32_16x16x32_bf16 v[32:35], a[32:35], a[96:99], v[32:35]\n"
"s_mov_b64 s[96:97], s[52:53]\n"
"v_accvgpr_read_b32 v16, a192\n"
"v_accvgpr_read_b32 v17, a193\n"
"v_mfma_f32_16x16x32_bf16 v[32:35], a[36:39], a[100:103], v[32:35]\n"
"global_load_dwordx4 a[80:83], v0, s[34:35] offset:0 sc1\n"
"v_accvgpr_read_b32 v18, a194\n"
"v_mfma_f32_16x16x32_bf16 v[60:63], a[44:47], a[100:103], v[60:63]\n"
"s_mov_b64 s[98:99], s[54:55]\n"
"v_accvgpr_read_b32 v19, a195\n"
"v_mfma_f32_16x16x32_bf16 v[60:63], a[40:43], a[96:99], v[60:63]\n"
"v_lshlrev_b32 v12, 16, v16\n"
"v_and_b32 v13, s63, v16\n"
"v_mfma_f32_16x16x32_bf16 v[64:67], a[40:43], a[104:107], v[64:67]\n"
"s_add_u32 s52, s52, 131072\n"
"s_addc_u32 s53, s53, 0\n"
"v_lshlrev_b32 v14, 16, v17\n"
"v_mfma_f32_16x16x32_bf16 v[64:67], a[44:47], a[108:111], v[64:67]\n"
"v_and_b32 v15, s63, v17\n"
"v_mul_f32 v12, v12, v144\n"
"v_mfma_f32_16x16x32_bf16 v[36:39], a[36:39], a[108:111], v[36:39]\n"
"s_add_u32 s54, s54, 131072\n"
"s_addc_u32 s55, s55, 0\n"
"v_mul_f32 v13, v13, v145\n"
"v_mfma_f32_16x16x32_bf16 v[36:39], a[32:35], a[104:107], v[36:39]\n"
"s_waitcnt lgkmcnt(9)\n"
"v_mul_f32 v14, v14, v146\n"
"v_mul_f32 v15, v15, v147\n"
"v_mfma_f32_16x16x32_bf16 v[40:43], a[32:35], a[112:115], v[40:43]\n"
"ds_read_b128 v[20:23], v2 offset:0\n"
"v_cvt_pk_bf16_f32 v24, v12, v13\n"
"v_mfma_f32_16x16x32_bf16 v[40:43], a[36:39], a[116:119], v[40:43]\n"
"global_load_dwordx4 a[84:87], v0, s[34:35] offset:1024 sc1\n"
"v_cvt_pk_bf16_f32 v25, v14, v15\n"
"v_mfma_f32_16x16x32_bf16 v[68:71], a[44:47], a[116:119], v[68:71]\n"
"ds_write_b64 v2, v[24:25] offset:16384\n"
"v_mfma_f32_16x16x32_bf16 v[68:71], a[40:43], a[112:115], v[68:71]\n"
"v_lshlrev_b32 v12, 16, v18\n"
"v_and_b32 v13, s63, v18\n"
"v_mfma_f32_16x16x32_bf16 v[72:75], a[40:43], a[120:123], v[72:75]\n"
"s_waitcnt lgkmcnt(6)\n"
"s_barrier\n"
"v_mfma_f32_16x16x32_bf16 v[72:75], a[44:47], a[124:127], v[72:75]\n"
"v_lshlrev_b32 v14, 16, v19\n"
"v_and_b32 v15, s63, v19\n"
"v_mfma_f32_16x16x32_bf16 v[44:47], a[36:39], a[124:127], v[44:47]\n"
"v_mul_f32 v12, v12, v172\n"
"v_mul_f32 v13, v13, v173\n"
"v_mfma_f32_16x16x32_bf16 v[44:47], a[32:35], a[120:123], v[44:47]\n"
"v_mul_f32 v14, v14, v174\n"
"v_mfma_f32_16x16x32_bf16 v[48:51], a[32:35], a[128:131], v[48:51]\n"
"v_mul_f32 v15, v15, v175\n"
"v_mfma_f32_16x16x32_bf16 v[48:51], a[36:39], a[132:135], v[48:51]\n"
"global_load_dwordx4 a[88:91], v0, s[34:35] offset:2048 sc1\n"
"v_mfma_f32_16x16x32_bf16 v[76:79], a[44:47], a[132:135], v[76:79]\n"
"v_cvt_pk_bf16_f32 v26, v12, v13\n"
"v_mfma_f32_16x16x32_bf16 v[76:79], a[40:43], a[128:131], v[76:79]\n"
"v_cvt_pk_bf16_f32 v27, v14, v15\n"
"v_mfma_f32_16x16x32_bf16 v[80:83], a[40:43], a[136:139], v[80:83]\n"
"s_waitcnt lgkmcnt(1)\n"
"global_store_dwordx4 v0, v[20:23], s[96:97] offset:-4096 sc1\n"
"ds_write_b64 v2, v[26:27] offset:16392\n"
"v_accvgpr_read_b32 v16, a196\n"
"v_mfma_f32_16x16x32_bf16 v[80:83], a[44:47], a[140:143], v[80:83]\n"
"v_accvgpr_read_b32 v17, a197\n"
"v_accvgpr_read_b32 v18, a198\n"
"v_mfma_f32_16x16x32_bf16 v[52:55], a[36:39], a[140:143], v[52:55]\n"
"v_accvgpr_read_b32 v19, a199\n"
"v_lshlrev_b32 v12, 16, v16\n"
"v_mfma_f32_16x16x32_bf16 v[52:55], a[32:35], a[136:139], v[52:55]\n"
"v_and_b32 v13, s63, v16\n"
"v_lshlrev_b32 v14, 16, v17\n"
"v_mfma_f32_16x16x32_bf16 v[56:59], a[32:35], a[144:147], v[56:59]\n"
"v_and_b32 v15, s63, v17\n"
"v_mul_f32 v12, v12, v200\n"
"v_mfma_f32_16x16x32_bf16 v[56:59], a[36:39], a[148:151], v[56:59]\n"
"global_load_dwordx4 a[92:95], v0, s[34:35] offset:3072 sc1\n"
"v_mul_f32 v13, v13, v201\n"
"v_mfma_f32_16x16x32_bf16 v[84:87], a[44:47], a[148:151], v[84:87]\n"
"v_mul_f32 v14, v14, v202\n"
"v_mul_f32 v15, v15, v203\n"
"v_mfma_f32_16x16x32_bf16 v[84:87], a[40:43], a[144:147], v[84:87]\n"
"v_cvt_pk_bf16_f32 v28, v12, v13\n"
"v_cvt_pk_bf16_f32 v29, v14, v15\n"
"v_mfma_f32_16x16x32_bf16 a[4:7], a[40:43], a[152:155], a[4:7]\n"
"s_add_u32 s34, s34, 8192\n"
"ds_write_b64 v2, v[28:29] offset:17408\n"
"v_mfma_f32_16x16x32_bf16 a[4:7], a[44:47], a[156:159], a[4:7]\n"
"v_lshlrev_b32 v12, 16, v18\n"
"v_mfma_f32_16x16x32_bf16 a[0:3], a[36:39], a[156:159], a[0:3]\n"
"v_and_b32 v13, s63, v18\n"
"v_lshlrev_b32 v14, 16, v19\n"
"v_mfma_f32_16x16x32_bf16 a[0:3], a[32:35], a[152:155], a[0:3]\n"
"s_waitcnt vmcnt(11)\n"
"v_and_b32 v15, s63, v19\n"
"v_mul_f32 v12, v12, v228\n"
"v_mfma_f32_16x16x32_bf16 v[88:91], a[48:51], a[96:99], v[88:91]\n"
"v_mul_f32 v13, v13, v229\n"
"v_mul_f32 v14, v14, v230\n"
"v_mfma_f32_16x16x32_bf16 v[88:91], a[52:55], a[100:103], v[88:91]\n"
"global_load_dwordx4 a[32:35], v0, s[34:35] offset:-4096 sc1\n"
"v_mul_f32 v15, v15, v231\n"
"v_mfma_f32_16x16x32_bf16 v[116:119], a[60:63], a[100:103], v[116:119]\n"
"ds_read_b128 a[100:103], v3 offset:1024\n"
"v_cvt_pk_bf16_f32 v30, v12, v13\n"
"v_mfma_f32_16x16x32_bf16 v[116:119], a[56:59], a[96:99], v[116:119]\n"
"ds_read_b128 a[96:99], v3 offset:0\n"
"v_cvt_pk_bf16_f32 v31, v14, v15\n"
"v_mfma_f32_16x16x32_bf16 v[120:123], a[56:59], a[104:107], v[120:123]\n"
"ds_write_b64 v2, v[30:31] offset:17416\n"
"v_mfma_f32_16x16x32_bf16 v[120:123], a[60:63], a[108:111], v[120:123]\n"
"ds_read_b128 v[20:23], v2 offset:3072\n"
"v_accvgpr_read_b32 v16, a200\n"
"v_mfma_f32_16x16x32_bf16 v[92:95], a[52:55], a[108:111], v[92:95]\n"
"ds_read_b128 a[108:111], v3 offset:3072\n"
"v_accvgpr_read_b32 v17, a201\n"
"v_mfma_f32_16x16x32_bf16 v[92:95], a[48:51], a[104:107], v[92:95]\n"
"ds_read_b128 a[104:107], v3 offset:2048\n"
"v_accvgpr_read_b32 v18, a202\n"
"v_mfma_f32_16x16x32_bf16 v[96:99], a[48:51], a[112:115], v[96:99]\n"
"v_accvgpr_read_b32 v19, a203\n"
"v_lshlrev_b32 v12, 16, v16\n"
"v_mfma_f32_16x16x32_bf16 v[96:99], a[52:55], a[116:119], v[96:99]\n"
"global_load_dwordx4 a[36:39], v0, s[34:35] offset:-3072 sc1\n"
"v_and_b32 v13, s63, v16\n"
"v_mfma_f32_16x16x32_bf16 v[124:127], a[60:63], a[116:119], v[124:127]\n"
"ds_read_b128 a[116:119], v3 offset:5120\n"
"v_lshlrev_b32 v14, 16, v17\n"
"v_mfma_f32_16x16x32_bf16 v[124:127], a[56:59], a[112:115], v[124:127]\n"
"ds_read_b128 a[112:115], v3 offset:4096\n"
"v_and_b32 v15, s63, v17\n"
"v_mfma_f32_16x16x32_bf16 v[128:131], a[56:59], a[120:123], v[128:131]\n"
"v_mul_f32 v12, v12, v148\n"
"v_mul_f32 v13, v13, v149\n"
"v_mfma_f32_16x16x32_bf16 v[128:131], a[60:63], a[124:127], v[128:131]\n"
"v_mul_f32 v14, v14, v150\n"
"v_mul_f32 v15, v15, v151\n"
"v_mfma_f32_16x16x32_bf16 v[100:103], a[52:55], a[124:127], v[100:103]\n"
"ds_read_b128 a[124:127], v3 offset:7168\n"
"v_cvt_pk_bf16_f32 v24, v12, v13\n"
"v_mfma_f32_16x16x32_bf16 v[100:103], a[48:51], a[120:123], v[100:103]\n"
"ds_read_b128 a[120:123], v3 offset:6144\n"
"v_cvt_pk_bf16_f32 v25, v14, v15\n"
"v_mfma_f32_16x16x32_bf16 v[104:107], a[48:51], a[128:131], v[104:107]\n"
"s_waitcnt lgkmcnt(6)\n"
"global_store_dwordx4 v0, v[20:23], s[96:97] offset:-1024 sc1\n"
"ds_write_b64 v2, v[24:25] offset:18432\n"
"v_lshlrev_b32 v12, 16, v18\n"
"v_mfma_f32_16x16x32_bf16 v[104:107], a[52:55], a[132:135], v[104:107]\n"
"global_load_dwordx4 a[40:43], v0, s[34:35] offset:-2048 sc1\n"
"v_and_b32 v13, s63, v18\n"
"v_mfma_f32_16x16x32_bf16 v[132:135], a[60:63], a[132:135], v[132:135]\n"
"ds_read_b128 a[132:135], v3 offset:9216\n"
"v_lshlrev_b32 v14, 16, v19\n"
"v_mfma_f32_16x16x32_bf16 v[132:135], a[56:59], a[128:131], v[132:135]\n"
"ds_read_b128 a[128:131], v3 offset:8192\n"
"v_and_b32 v15, s63, v19\n"
"v_mfma_f32_16x16x32_bf16 v[136:139], a[56:59], a[136:139], v[136:139]\n"
"v_mul_f32 v12, v12, v176\n"
"v_mul_f32 v13, v13, v177\n"
"v_mfma_f32_16x16x32_bf16 v[136:139], a[60:63], a[140:143], v[136:139]\n"
"v_mul_f32 v14, v14, v178\n"
"v_mul_f32 v15, v15, v179\n"
"v_mfma_f32_16x16x32_bf16 v[108:111], a[52:55], a[140:143], v[108:111]\n"
"ds_read_b128 a[140:143], v3 offset:11264\n"
"v_cvt_pk_bf16_f32 v26, v12, v13\n"
"v_mfma_f32_16x16x32_bf16 v[108:111], a[48:51], a[136:139], v[108:111]\n"
"ds_read_b128 a[136:139], v3 offset:10240\n"
"v_cvt_pk_bf16_f32 v27, v14, v15\n"
"v_mfma_f32_16x16x32_bf16 v[112:115], a[48:51], a[144:147], v[112:115]\n"
"ds_write_b64 v2, v[26:27] offset:18440\n"
"v_mfma_f32_16x16x32_bf16 v[112:115], a[52:55], a[148:151], v[112:115]\n"
"global_load_dwordx4 a[44:47], v0, s[34:35] offset:-1024 sc1\n"
"v_accvgpr_read_b32 v16, a204\n"
"v_mfma_f32_16x16x32_bf16 v[140:143], a[60:63], a[148:151], v[140:143]\n"
"ds_read_b128 a[148:151], v3 offset:13312\n"
"v_accvgpr_read_b32 v17, a205\n"
"v_mfma_f32_16x16x32_bf16 v[140:143], a[56:59], a[144:147], v[140:143]\n"
"ds_read_b128 a[144:147], v3 offset:12288\n"
"v_accvgpr_read_b32 v18, a206\n"
"v_mfma_f32_16x16x32_bf16 a[12:15], a[56:59], a[152:155], a[12:15]\n"
"v_accvgpr_read_b32 v19, a207\n"
"v_lshlrev_b32 v12, 16, v16\n"
"v_mfma_f32_16x16x32_bf16 a[12:15], a[60:63], a[156:159], a[12:15]\n"
"v_and_b32 v13, s63, v16\n"
"v_lshlrev_b32 v14, 16, v17\n"
"v_mfma_f32_16x16x32_bf16 a[8:11], a[52:55], a[156:159], a[8:11]\n"
"ds_read_b128 a[156:159], v3 offset:15360\n"
"v_and_b32 v15, s63, v17\n"
"v_mfma_f32_16x16x32_bf16 a[8:11], a[48:51], a[152:155], a[8:11]\n"
"s_waitcnt vmcnt(10) lgkmcnt(13)\n"
"ds_read_b128 a[152:155], v3 offset:14336\n"
"v_mul_f32 v12, v12, v204\n"
"v_mfma_f32_16x16x32_bf16 v[32:35], a[64:67], a[96:99], v[32:35]\n"
"ds_read_b128 v[20:23], v2 offset:6144\n"
"v_mul_f32 v13, v13, v205\n"
"v_mfma_f32_16x16x32_bf16 v[32:35], a[68:71], a[100:103], v[32:35]\n"
"global_load_dwordx4 a[48:51], v0, s[34:35] offset:0 sc1\n"
"v_mul_f32 v14, v14, v206\n"
"v_mfma_f32_16x16x32_bf16 v[60:63], a[76:79], a[100:103], v[60:63]\n"
"v_mul_f32 v15, v15, v207\n"
"v_cvt_pk_bf16_f32 v28, v12, v13\n"
"v_mfma_f32_16x16x32_bf16 v[60:63], a[72:75], a[96:99], v[60:63]\n"
"v_cvt_pk_bf16_f32 v29, v14, v15\n"
"v_mfma_f32_16x16x32_bf16 v[64:67], a[72:75], a[104:107], v[64:67]\n"
"ds_write_b64 v2, v[28:29] offset:19456\n"
"v_mfma_f32_16x16x32_bf16 v[64:67], a[76:79], a[108:111], v[64:67]\n"
"v_lshlrev_b32 v12, 16, v18\n"
"v_and_b32 v13, s63, v18\n"
"v_mfma_f32_16x16x32_bf16 v[36:39], a[68:71], a[108:111], v[36:39]\n"
"v_lshlrev_b32 v14, 16, v19\n"
"v_and_b32 v15, s63, v19\n"
"v_mfma_f32_16x16x32_bf16 v[36:39], a[64:67], a[104:107], v[36:39]\n"
"s_waitcnt lgkmcnt(12)\n"
"v_mul_f32 v12, v12, v232\n"
"v_mul_f32 v13, v13, v233\n"
"v_mfma_f32_16x16x32_bf16 v[40:43], a[64:67], a[112:115], v[40:43]\n"
"v_mul_f32 v14, v14, v234\n"
"v_mfma_f32_16x16x32_bf16 v[40:43], a[68:71], a[116:119], v[40:43]\n"
"global_load_dwordx4 a[52:55], v0, s[34:35] offset:1024 sc1\n"
"v_mul_f32 v15, v15, v235\n"
"v_mfma_f32_16x16x32_bf16 v[68:71], a[76:79], a[116:119], v[68:71]\n"
"v_cvt_pk_bf16_f32 v30, v12, v13\n"
"v_mfma_f32_16x16x32_bf16 v[68:71], a[72:75], a[112:115], v[68:71]\n"
"v_cvt_pk_bf16_f32 v31, v14, v15\n"
"v_mfma_f32_16x16x32_bf16 v[72:75], a[72:75], a[120:123], v[72:75]\n"
"s_waitcnt lgkmcnt(1)\n"
"global_store_dwordx4 v0, v[20:23], s[96:97] offset:2048 sc1\n"
"ds_write_b64 v2, v[30:31] offset:19464\n"
"v_accvgpr_read_b32 v16, a208\n"
"v_mfma_f32_16x16x32_bf16 v[72:75], a[76:79], a[124:127], v[72:75]\n"
"v_accvgpr_read_b32 v17, a209\n"
"v_accvgpr_read_b32 v18, a210\n"
"v_mfma_f32_16x16x32_bf16 v[44:47], a[68:71], a[124:127], v[44:47]\n"
"v_accvgpr_read_b32 v19, a211\n"
"v_mfma_f32_16x16x32_bf16 v[44:47], a[64:67], a[120:123], v[44:47]\n"
"v_lshlrev_b32 v12, 16, v16\n"
"v_and_b32 v13, s63, v16\n"
"v_mfma_f32_16x16x32_bf16 v[48:51], a[64:67], a[128:131], v[48:51]\n"
"v_lshlrev_b32 v14, 16, v17\n"
"v_mfma_f32_16x16x32_bf16 v[48:51], a[68:71], a[132:135], v[48:51]\n"
"global_load_dwordx4 a[56:59], v0, s[34:35] offset:2048 sc1\n"
"v_and_b32 v15, s63, v17\n"
"v_mfma_f32_16x16x32_bf16 v[76:79], a[76:79], a[132:135], v[76:79]\n"
"v_mul_f32 v12, v12, v152\n"
"v_mul_f32 v13, v13, v153\n"
"v_mfma_f32_16x16x32_bf16 v[76:79], a[72:75], a[128:131], v[76:79]\n"
"v_mul_f32 v14, v14, v154\n"
"v_mul_f32 v15, v15, v155\n"
"v_mfma_f32_16x16x32_bf16 v[80:83], a[72:75], a[136:139], v[80:83]\n"
"v_cvt_pk_bf16_f32 v24, v12, v13\n"
"v_cvt_pk_bf16_f32 v25, v14, v15\n"
"v_mfma_f32_16x16x32_bf16 v[80:83], a[76:79], a[140:143], v[80:83]\n"
"ds_write_b64 v2, v[24:25] offset:20480\n"
"v_mfma_f32_16x16x32_bf16 v[52:55], a[68:71], a[140:143], v[52:55]\n"
"v_lshlrev_b32 v12, 16, v18\n"
"v_mfma_f32_16x16x32_bf16 v[52:55], a[64:67], a[136:139], v[52:55]\n"
"v_and_b32 v13, s63, v18\n"
"v_lshlrev_b32 v14, 16, v19\n"
"v_mfma_f32_16x16x32_bf16 v[56:59], a[64:67], a[144:147], v[56:59]\n"
"v_and_b32 v15, s63, v19\n"
"v_mul_f32 v12, v12, v180\n"
"v_mfma_f32_16x16x32_bf16 v[56:59], a[68:71], a[148:151], v[56:59]\n"
"global_load_dwordx4 a[60:63], v0, s[34:35] offset:3072 sc1\n"
"v_mul_f32 v13, v13, v181\n"
"v_mfma_f32_16x16x32_bf16 v[84:87], a[76:79], a[148:151], v[84:87]\n"
"v_mul_f32 v14, v14, v182\n"
"v_mul_f32 v15, v15, v183\n"
"v_mfma_f32_16x16x32_bf16 v[84:87], a[72:75], a[144:147], v[84:87]\n"
"v_cvt_pk_bf16_f32 v26, v12, v13\n"
"v_cvt_pk_bf16_f32 v27, v14, v15\n"
"v_mfma_f32_16x16x32_bf16 a[4:7], a[72:75], a[152:155], a[4:7]\n"
"s_add_u32 s34, s34, 8192\n"
"ds_write_b64 v2, v[26:27] offset:20488\n"
"v_mfma_f32_16x16x32_bf16 a[4:7], a[76:79], a[156:159], a[4:7]\n"
"ds_read_b128 v[20:23], v2 offset:9216\n"
"v_mfma_f32_16x16x32_bf16 a[0:3], a[68:71], a[156:159], a[0:3]\n"
"v_accvgpr_read_b32 v16, a212\n"
"v_mfma_f32_16x16x32_bf16 a[0:3], a[64:67], a[152:155], a[0:3]\n"
"s_waitcnt vmcnt(10)\n"
"v_accvgpr_read_b32 v17, a213\n"
"v_accvgpr_read_b32 v18, a214\n"
"v_mfma_f32_16x16x32_bf16 v[88:91], a[80:83], a[96:99], v[88:91]\n"
"v_accvgpr_read_b32 v19, a215\n"
"v_lshlrev_b32 v12, 16, v16\n"
"v_mfma_f32_16x16x32_bf16 v[88:91], a[84:87], a[100:103], v[88:91]\n"
"global_load_dwordx4 a[64:67], v0, s[34:35] offset:-4096 sc1\n"
"v_and_b32 v13, s63, v16\n"
"v_mfma_f32_16x16x32_bf16 v[116:119], a[92:95], a[100:103], v[116:119]\n"
"ds_read_b128 a[100:103], v4 offset:1024\n"
"v_lshlrev_b32 v14, 16, v17\n"
"v_mfma_f32_16x16x32_bf16 v[116:119], a[88:91], a[96:99], v[116:119]\n"
"ds_read_b128 a[96:99], v4 offset:0\n"
"v_and_b32 v15, s63, v17\n"
"v_mfma_f32_16x16x32_bf16 v[120:123], a[88:91], a[104:107], v[120:123]\n"
"v_mul_f32 v12, v12, v208\n"
"v_mul_f32 v13, v13, v209\n"
"v_mfma_f32_16x16x32_bf16 v[120:123], a[92:95], a[108:111], v[120:123]\n"
"v_mul_f32 v14, v14, v210\n"
"v_mul_f32 v15, v15, v211\n"
"v_mfma_f32_16x16x32_bf16 v[92:95], a[84:87], a[108:111], v[92:95]\n"
"ds_read_b128 a[108:111], v4 offset:3072\n"
"v_cvt_pk_bf16_f32 v28, v12, v13\n"
"v_mfma_f32_16x16x32_bf16 v[92:95], a[80:83], a[104:107], v[92:95]\n"
"ds_read_b128 a[104:107], v4 offset:2048\n"
"v_cvt_pk_bf16_f32 v29, v14, v15\n"
"v_mfma_f32_16x16x32_bf16 v[96:99], a[80:83], a[112:115], v[96:99]\n"
"s_waitcnt lgkmcnt(4)\n"
"global_store_dwordx4 v0, v[20:23], s[98:99] offset:-3072 sc1\n"
"ds_write_b64 v2, v[28:29] offset:21504\n"
"v_lshlrev_b32 v12, 16, v18\n"
"v_mfma_f32_16x16x32_bf16 v[96:99], a[84:87], a[116:119], v[96:99]\n"
"global_load_dwordx4 a[68:71], v0, s[34:35] offset:-3072 sc1\n"
"v_and_b32 v13, s63, v18\n"
"v_mfma_f32_16x16x32_bf16 v[124:127], a[92:95], a[116:119], v[124:127]\n"
"ds_read_b128 a[116:119], v4 offset:5120\n"
"v_lshlrev_b32 v14, 16, v19\n"
"v_mfma_f32_16x16x32_bf16 v[124:127], a[88:91], a[112:115], v[124:127]\n"
"ds_read_b128 a[112:115], v4 offset:4096\n"
"v_and_b32 v15, s63, v19\n"
"v_mfma_f32_16x16x32_bf16 v[128:131], a[88:91], a[120:123], v[128:131]\n"
"v_mul_f32 v12, v12, v236\n"
"v_mul_f32 v13, v13, v237\n"
"v_mfma_f32_16x16x32_bf16 v[128:131], a[92:95], a[124:127], v[128:131]\n"
"v_mul_f32 v14, v14, v238\n"
"v_mul_f32 v15, v15, v239\n"
"v_mfma_f32_16x16x32_bf16 v[100:103], a[84:87], a[124:127], v[100:103]\n"
"ds_read_b128 a[124:127], v4 offset:7168\n"
"v_cvt_pk_bf16_f32 v30, v12, v13\n"
"v_mfma_f32_16x16x32_bf16 v[100:103], a[80:83], a[120:123], v[100:103]\n"
"ds_read_b128 a[120:123], v4 offset:6144\n"
"v_cvt_pk_bf16_f32 v31, v14, v15\n"
"v_mfma_f32_16x16x32_bf16 v[104:107], a[80:83], a[128:131], v[104:107]\n"
"ds_write_b64 v2, v[30:31] offset:21512\n"
"v_mfma_f32_16x16x32_bf16 v[104:107], a[84:87], a[132:135], v[104:107]\n"
"global_load_dwordx4 a[72:75], v0, s[34:35] offset:-2048 sc1\n"
"v_accvgpr_read_b32 v16, a216\n"
"v_mfma_f32_16x16x32_bf16 v[132:135], a[92:95], a[132:135], v[132:135]\n"
"ds_read_b128 a[132:135], v4 offset:9216\n"
"v_accvgpr_read_b32 v17, a217\n"
"v_mfma_f32_16x16x32_bf16 v[132:135], a[88:91], a[128:131], v[132:135]\n"
"ds_read_b128 a[128:131], v4 offset:8192\n"
"v_accvgpr_read_b32 v18, a218\n"
"v_mfma_f32_16x16x32_bf16 v[136:139], a[88:91], a[136:139], v[136:139]\n"
"v_accvgpr_read_b32 v19, a219\n"
"v_mfma_f32_16x16x32_bf16 v[136:139], a[92:95], a[140:143], v[136:139]\n"
"v_lshlrev_b32 v12, 16, v16\n"
"v_and_b32 v13, s63, v16\n"
"v_mfma_f32_16x16x32_bf16 v[108:111], a[84:87], a[140:143], v[108:111]\n"
"ds_read_b128 a[140:143], v4 offset:11264\n"
"v_lshlrev_b32 v14, 16, v17\n"
"v_mfma_f32_16x16x32_bf16 v[108:111], a[80:83], a[136:139], v[108:111]\n"
"ds_read_b128 a[136:139], v4 offset:10240\n"
"v_and_b32 v15, s63, v17\n"
"v_mfma_f32_16x16x32_bf16 v[112:115], a[80:83], a[144:147], v[112:115]\n"
"ds_read_b128 v[20:23], v2 offset:12288\n"
"v_mul_f32 v12, v12, v156\n"
"v_mfma_f32_16x16x32_bf16 v[112:115], a[84:87], a[148:151], v[112:115]\n"
"global_load_dwordx4 a[76:79], v0, s[34:35] offset:-1024 sc1\n"
"v_mfma_f32_16x16x32_bf16 v[140:143], a[92:95], a[148:151], v[140:143]\n"
"ds_read_b128 a[148:151], v4 offset:13312\n"
"v_mfma_f32_16x16x32_bf16 v[140:143], a[88:91], a[144:147], v[140:143]\n"
"ds_read_b128 a[144:147], v4 offset:12288\n"
"v_mfma_f32_16x16x32_bf16 a[12:15], a[88:91], a[152:155], a[12:15]\n"
"v_mul_f32 v13, v13, v157\n"
"v_mfma_f32_16x16x32_bf16 a[12:15], a[92:95], a[156:159], a[12:15]\n"
"v_mul_f32 v14, v14, v158\n"
"v_mfma_f32_16x16x32_bf16 a[8:11], a[84:87], a[156:159], a[8:11]\n"
"ds_read_b128 a[156:159], v4 offset:15360\n"
"v_mfma_f32_16x16x32_bf16 a[8:11], a[80:83], a[152:155], a[8:11]\n"
"s_waitcnt vmcnt(10) lgkmcnt(14)\n"
"ds_read_b128 a[152:155], v4 offset:14336\n"
"v_mfma_f32_16x16x32_bf16 v[32:35], a[32:35], a[96:99], v[32:35]\n"
"v_mul_f32 v15, v15, v159\n"
"v_mfma_f32_16x16x32_bf16 v[32:35], a[36:39], a[100:103], v[32:35]\n"
"global_load_dwordx4 a[80:83], v0, s[34:35] offset:0 sc1\n"
"v_mfma_f32_16x16x32_bf16 v[60:63], a[44:47], a[100:103], v[60:63]\n"
"s_add_u32 s65, s65, 1\n"
"v_cvt_pk_bf16_f32 v24, v12, v13\n"
"v_mfma_f32_16x16x32_bf16 v[60:63], a[40:43], a[96:99], v[60:63]\n"
"v_cvt_pk_bf16_f32 v25, v14, v15\n"
"v_mfma_f32_16x16x32_bf16 v[64:67], a[40:43], a[104:107], v[64:67]\n"
"s_waitcnt lgkmcnt(4)\n"
"global_store_dwordx4 v0, v[20:23], s[98:99] offset:0 sc1\n"
"ds_write_b64 v2, v[24:25] offset:22528\n"
"v_lshlrev_b32 v12, 16, v18\n"
"v_mfma_f32_16x16x32_bf16 v[64:67], a[44:47], a[108:111], v[64:67]\n"
"v_and_b32 v13, s63, v18\n"
"v_lshlrev_b32 v14, 16, v19\n"
"v_mfma_f32_16x16x32_bf16 v[36:39], a[36:39], a[108:111], v[36:39]\n"
"v_and_b32 v15, s63, v19\n"
"v_mul_f32 v12, v12, v184\n"
"v_mfma_f32_16x16x32_bf16 v[36:39], a[32:35], a[104:107], v[36:39]\n"
"v_mul_f32 v13, v13, v185\n"
"v_mul_f32 v14, v14, v186\n"
"v_mfma_f32_16x16x32_bf16 v[40:43], a[32:35], a[112:115], v[40:43]\n"
"v_mul_f32 v15, v15, v187\n"
"v_cvt_pk_bf16_f32 v26, v12, v13\n"
"v_mfma_f32_16x16x32_bf16 v[40:43], a[36:39], a[116:119], v[40:43]\n"
"global_load_dwordx4 a[84:87], v0, s[34:35] offset:1024 sc1\n"
"v_cvt_pk_bf16_f32 v27, v14, v15\n"
"v_mfma_f32_16x16x32_bf16 v[68:71], a[44:47], a[116:119], v[68:71]\n"
"ds_write_b64 v2, v[26:27] offset:22536\n"
"v_mfma_f32_16x16x32_bf16 v[68:71], a[40:43], a[112:115], v[68:71]\n"
"v_accvgpr_read_b32 v16, a220\n"
"v_accvgpr_read_b32 v17, a221\n"
"v_mfma_f32_16x16x32_bf16 v[72:75], a[40:43], a[120:123], v[72:75]\n"
"v_accvgpr_read_b32 v18, a222\n"
"v_accvgpr_read_b32 v19, a223\n"
"v_mfma_f32_16x16x32_bf16 v[72:75], a[44:47], a[124:127], v[72:75]\n"
"v_lshlrev_b32 v12, 16, v16\n"
"v_and_b32 v13, s63, v16\n"
"v_mfma_f32_16x16x32_bf16 v[44:47], a[36:39], a[124:127], v[44:47]\n"
"v_lshlrev_b32 v14, 16, v17\n"
"v_and_b32 v15, s63, v17\n"
"v_mfma_f32_16x16x32_bf16 v[44:47], a[32:35], a[120:123], v[44:47]\n"
"v_mul_f32 v12, v12, v212\n"
"v_mul_f32 v13, v13, v213\n"
"v_mfma_f32_16x16x32_bf16 v[48:51], a[32:35], a[128:131], v[48:51]\n"
"v_mul_f32 v14, v14, v214\n"
"v_mul_f32 v15, v15, v215\n"
"v_mfma_f32_16x16x32_bf16 v[48:51], a[36:39], a[132:135], v[48:51]\n"
"global_load_dwordx4 a[88:91], v0, s[34:35] offset:2048 sc1\n"
"v_cvt_pk_bf16_f32 v28, v12, v13\n"
"v_mfma_f32_16x16x32_bf16 v[76:79], a[44:47], a[132:135], v[76:79]\n"
"v_cvt_pk_bf16_f32 v29, v14, v15\n"
"v_mfma_f32_16x16x32_bf16 v[76:79], a[40:43], a[128:131], v[76:79]\n"
"ds_write_b64 v2, v[28:29] offset:23552\n"
"v_mfma_f32_16x16x32_bf16 v[80:83], a[40:43], a[136:139], v[80:83]\n"
"v_lshlrev_b32 v12, 16, v18\n"
"v_and_b32 v13, s63, v18\n"
"v_mfma_f32_16x16x32_bf16 v[80:83], a[44:47], a[140:143], v[80:83]\n"
"v_lshlrev_b32 v14, 16, v19\n"
"v_and_b32 v15, s63, v19\n"
"v_mfma_f32_16x16x32_bf16 v[52:55], a[36:39], a[140:143], v[52:55]\n"
"v_mul_f32 v12, v12, v240\n"
"v_mul_f32 v13, v13, v241\n"
"v_mfma_f32_16x16x32_bf16 v[52:55], a[32:35], a[136:139], v[52:55]\n"
"s_waitcnt lgkmcnt(3)\n"
"v_mul_f32 v14, v14, v242\n"
"v_mul_f32 v15, v15, v243\n"
"v_mfma_f32_16x16x32_bf16 v[56:59], a[32:35], a[144:147], v[56:59]\n"
"v_cvt_pk_bf16_f32 v30, v12, v13\n"
"v_cvt_pk_bf16_f32 v31, v14, v15\n"
"v_mfma_f32_16x16x32_bf16 v[56:59], a[36:39], a[148:151], v[56:59]\n"
"global_load_dwordx4 a[92:95], v0, s[34:35] offset:3072 sc1\n"
"v_mfma_f32_16x16x32_bf16 v[84:87], a[44:47], a[148:151], v[84:87]\n"
"ds_write_b64 v2, v[30:31] offset:23560\n"
"v_mfma_f32_16x16x32_bf16 v[84:87], a[40:43], a[144:147], v[84:87]\n"
"v_accvgpr_read_b32 v16, a224\n"
"v_accvgpr_read_b32 v17, a225\n"
"v_mfma_f32_16x16x32_bf16 a[4:7], a[40:43], a[152:155], a[4:7]\n"
"s_add_u32 s34, s34, 8192\n"
"v_accvgpr_read_b32 v18, a226\n"
"v_accvgpr_read_b32 v19, a227\n"
"v_mfma_f32_16x16x32_bf16 a[4:7], a[44:47], a[156:159], a[4:7]\n"
"v_lshlrev_b32 v12, 16, v16\n"
"v_and_b32 v13, s63, v16\n"
"v_mfma_f32_16x16x32_bf16 a[0:3], a[36:39], a[156:159], a[0:3]\n"
"v_lshlrev_b32 v14, 16, v17\n"
"v_and_b32 v15, s63, v17\n"
"v_mfma_f32_16x16x32_bf16 a[0:3], a[32:35], a[152:155], a[0:3]\n"
"s_waitcnt vmcnt(10)\n"
"v_mul_f32 v12, v12, v160\n"
"v_mul_f32 v13, v13, v161\n"
"v_mfma_f32_16x16x32_bf16 v[88:91], a[48:51], a[96:99], v[88:91]\n"
"v_mul_f32 v14, v14, v162\n"
"v_mul_f32 v15, v15, v163\n"
"v_mfma_f32_16x16x32_bf16 v[88:91], a[52:55], a[100:103], v[88:91]\n"
"global_load_dwordx4 a[32:35], v0, s[34:35] offset:-4096 sc1\n"
"v_cvt_pk_bf16_f32 v24, v12, v13\n"
"v_mfma_f32_16x16x32_bf16 v[116:119], a[60:63], a[100:103], v[116:119]\n"
"ds_read_b128 a[100:103], v5 offset:1024\n"
"v_cvt_pk_bf16_f32 v25, v14, v15\n"
"v_mfma_f32_16x16x32_bf16 v[116:119], a[56:59], a[96:99], v[116:119]\n"
"ds_read_b128 a[96:99], v5 offset:0\n"
"v_mfma_f32_16x16x32_bf16 v[120:123], a[56:59], a[104:107], v[120:123]\n"
"ds_write_b64 v2, v[24:25] offset:24576\n"
"v_mfma_f32_16x16x32_bf16 v[120:123], a[60:63], a[108:111], v[120:123]\n"
"v_lshlrev_b32 v12, 16, v18\n"
"v_and_b32 v13, s63, v18\n"
"v_mfma_f32_16x16x32_bf16 v[92:95], a[52:55], a[108:111], v[92:95]\n"
"ds_read_b128 a[108:111], v5 offset:3072\n"
"v_lshlrev_b32 v14, 16, v19\n"
"v_mfma_f32_16x16x32_bf16 v[92:95], a[48:51], a[104:107], v[92:95]\n"
"ds_read_b128 a[104:107], v5 offset:2048\n"
"v_and_b32 v15, s63, v19\n"
"v_mfma_f32_16x16x32_bf16 v[96:99], a[48:51], a[112:115], v[96:99]\n"
"v_mul_f32 v12, v12, v188\n"
"v_mul_f32 v13, v13, v189\n"
"v_mfma_f32_16x16x32_bf16 v[96:99], a[52:55], a[116:119], v[96:99]\n"
"global_load_dwordx4 a[36:39], v0, s[34:35] offset:-3072 sc1\n"
"v_mul_f32 v14, v14, v190\n"
"v_mfma_f32_16x16x32_bf16 v[124:127], a[60:63], a[116:119], v[124:127]\n"
"ds_read_b128 a[116:119], v5 offset:5120\n"
"v_mul_f32 v15, v15, v191\n"
"v_mfma_f32_16x16x32_bf16 v[124:127], a[56:59], a[112:115], v[124:127]\n"
"ds_read_b128 a[112:115], v5 offset:4096\n"
"v_cvt_pk_bf16_f32 v26, v12, v13\n"
"v_mfma_f32_16x16x32_bf16 v[128:131], a[56:59], a[120:123], v[128:131]\n"
"v_cvt_pk_bf16_f32 v27, v14, v15\n"
"v_mfma_f32_16x16x32_bf16 v[128:131], a[60:63], a[124:127], v[128:131]\n"
"ds_write_b64 v2, v[26:27] offset:24584\n"
"v_mfma_f32_16x16x32_bf16 v[100:103], a[52:55], a[124:127], v[100:103]\n"
"ds_read_b128 a[124:127], v5 offset:7168\n"
"v_accvgpr_read_b32 v16, a228\n"
"v_mfma_f32_16x16x32_bf16 v[100:103], a[48:51], a[120:123], v[100:103]\n"
"ds_read_b128 a[120:123], v5 offset:6144\n"
"v_accvgpr_read_b32 v17, a229\n"
"v_mfma_f32_16x16x32_bf16 v[104:107], a[48:51], a[128:131], v[104:107]\n"
"v_accvgpr_read_b32 v18, a230\n"
"v_accvgpr_read_b32 v19, a231\n"
"v_mfma_f32_16x16x32_bf16 v[104:107], a[52:55], a[132:135], v[104:107]\n"
"global_load_dwordx4 a[40:43], v0, s[34:35] offset:-2048 sc1\n"
"v_lshlrev_b32 v12, 16, v16\n"
"v_mfma_f32_16x16x32_bf16 v[132:135], a[60:63], a[132:135], v[132:135]\n"
"ds_read_b128 a[132:135], v5 offset:9216\n"
"v_and_b32 v13, s63, v16\n"
"v_mfma_f32_16x16x32_bf16 v[132:135], a[56:59], a[128:131], v[132:135]\n"
"ds_read_b128 a[128:131], v5 offset:8192\n"
"v_lshlrev_b32 v14, 16, v17\n"
"v_mfma_f32_16x16x32_bf16 v[136:139], a[56:59], a[136:139], v[136:139]\n"
"v_and_b32 v15, s63, v17\n"
"v_mul_f32 v12, v12, v216\n"
"v_mfma_f32_16x16x32_bf16 v[136:139], a[60:63], a[140:143], v[136:139]\n"
"v_mul_f32 v13, v13, v217\n"
"v_mul_f32 v14, v14, v218\n"
"v_mfma_f32_16x16x32_bf16 v[108:111], a[52:55], a[140:143], v[108:111]\n"
"ds_read_b128 a[140:143], v5 offset:11264\n"
"v_mul_f32 v15, v15, v219\n"
"v_mfma_f32_16x16x32_bf16 v[108:111], a[48:51], a[136:139], v[108:111]\n"
"ds_read_b128 a[136:139], v5 offset:10240\n"
"v_cvt_pk_bf16_f32 v28, v12, v13\n"
"v_mfma_f32_16x16x32_bf16 v[112:115], a[48:51], a[144:147], v[112:115]\n"
"v_cvt_pk_bf16_f32 v29, v14, v15\n"
"v_lshlrev_b32 v12, 16, v18\n"
"v_mfma_f32_16x16x32_bf16 v[112:115], a[52:55], a[148:151], v[112:115]\n"
"global_load_dwordx4 a[44:47], v0, s[34:35] offset:-1024 sc1\n"
"v_and_b32 v13, s63, v18\n"
"v_mfma_f32_16x16x32_bf16 v[140:143], a[60:63], a[148:151], v[140:143]\n"
"ds_read_b128 a[148:151], v5 offset:13312\n"
"v_lshlrev_b32 v14, 16, v19\n"
"v_mfma_f32_16x16x32_bf16 v[140:143], a[56:59], a[144:147], v[140:143]\n"
"ds_read_b128 a[144:147], v5 offset:12288\n"
"v_and_b32 v15, s63, v19\n"
"v_mfma_f32_16x16x32_bf16 a[12:15], a[56:59], a[152:155], a[12:15]\n"
"v_mul_f32 v12, v12, v244\n"
"v_mul_f32 v13, v13, v245\n"
"v_mfma_f32_16x16x32_bf16 a[12:15], a[60:63], a[156:159], a[12:15]\n"
"v_mul_f32 v14, v14, v246\n"
"v_mul_f32 v15, v15, v247\n"
"v_mfma_f32_16x16x32_bf16 a[8:11], a[52:55], a[156:159], a[8:11]\n"
"ds_read_b128 a[156:159], v5 offset:15360\n"
"v_cvt_pk_bf16_f32 v30, v12, v13\n"
"v_mfma_f32_16x16x32_bf16 a[8:11], a[48:51], a[152:155], a[8:11]\n"
"s_waitcnt vmcnt(9) lgkmcnt(12)\n"
"ds_read_b128 a[152:155], v5 offset:14336\n"
"v_cvt_pk_bf16_f32 v31, v14, v15\n"
"v_mfma_f32_16x16x32_bf16 v[32:35], a[64:67], a[96:99], v[32:35]\n"
"global_store_dwordx4 v0, v[28:31], s[54:55] offset:-3072 sc1\n"
"ds_write_b128 v2, v[28:31] offset:25600\n"
"v_mfma_f32_16x16x32_bf16 v[32:35], a[68:71], a[100:103], v[32:35]\n"
"global_load_dwordx4 a[48:51], v0, s[34:35] offset:0 sc1\n"
"v_accvgpr_read_b32 v16, a232\n"
"v_mfma_f32_16x16x32_bf16 v[60:63], a[76:79], a[100:103], v[60:63]\n"
"v_accvgpr_read_b32 v17, a233\n"
"v_accvgpr_read_b32 v18, a234\n"
"v_mfma_f32_16x16x32_bf16 v[60:63], a[72:75], a[96:99], v[60:63]\n"
"v_accvgpr_read_b32 v19, a235\n"
"v_lshlrev_b32 v12, 16, v16\n"
"v_mfma_f32_16x16x32_bf16 v[64:67], a[72:75], a[104:107], v[64:67]\n"
"v_and_b32 v13, s63, v16\n"
"v_lshlrev_b32 v14, 16, v17\n"
"v_mfma_f32_16x16x32_bf16 v[64:67], a[76:79], a[108:111], v[64:67]\n"
"v_and_b32 v15, s63, v17\n"
"v_mul_f32 v12, v12, v164\n"
"v_mfma_f32_16x16x32_bf16 v[36:39], a[68:71], a[108:111], v[36:39]\n"
"v_mul_f32 v13, v13, v165\n"
"v_mul_f32 v14, v14, v166\n"
"v_mfma_f32_16x16x32_bf16 v[36:39], a[64:67], a[104:107], v[36:39]\n"
"s_waitcnt lgkmcnt(9)\n"
"v_mul_f32 v15, v15, v167\n"
"v_cvt_pk_bf16_f32 v24, v12, v13\n"
"v_mfma_f32_16x16x32_bf16 v[40:43], a[64:67], a[112:115], v[40:43]\n"
"v_cvt_pk_bf16_f32 v25, v14, v15\n"
"v_mfma_f32_16x16x32_bf16 v[40:43], a[68:71], a[116:119], v[40:43]\n"
"global_load_dwordx4 a[52:55], v0, s[34:35] offset:1024 sc1\n"
"v_mfma_f32_16x16x32_bf16 v[68:71], a[76:79], a[116:119], v[68:71]\n"
"ds_write_b64 v2, v[24:25] offset:26624\n"
"v_mfma_f32_16x16x32_bf16 v[68:71], a[72:75], a[112:115], v[68:71]\n"
"v_lshlrev_b32 v12, 16, v18\n"
"v_and_b32 v13, s63, v18\n"
"v_mfma_f32_16x16x32_bf16 v[72:75], a[72:75], a[120:123], v[72:75]\n"
"v_lshlrev_b32 v14, 16, v19\n"
"v_and_b32 v15, s63, v19\n"
"v_mfma_f32_16x16x32_bf16 v[72:75], a[76:79], a[124:127], v[72:75]\n"
"v_mul_f32 v12, v12, v192\n"
"v_mul_f32 v13, v13, v193\n"
"v_mfma_f32_16x16x32_bf16 v[44:47], a[68:71], a[124:127], v[44:47]\n"
"v_mul_f32 v14, v14, v194\n"
"v_mul_f32 v15, v15, v195\n"
"v_mfma_f32_16x16x32_bf16 v[44:47], a[64:67], a[120:123], v[44:47]\n"
"s_waitcnt lgkmcnt(6)\n"
"v_cvt_pk_bf16_f32 v26, v12, v13\n"
"v_cvt_pk_bf16_f32 v27, v14, v15\n"
"v_mfma_f32_16x16x32_bf16 v[48:51], a[64:67], a[128:131], v[48:51]\n"
"ds_write_b64 v2, v[26:27] offset:26632\n"
"v_mfma_f32_16x16x32_bf16 v[48:51], a[68:71], a[132:135], v[48:51]\n"
"global_load_dwordx4 a[56:59], v0, s[34:35] offset:2048 sc1\n"
"v_accvgpr_read_b32 v16, a236\n"
"v_mfma_f32_16x16x32_bf16 v[76:79], a[76:79], a[132:135], v[76:79]\n"
"v_accvgpr_read_b32 v17, a237\n"
"v_accvgpr_read_b32 v18, a238\n"
"v_mfma_f32_16x16x32_bf16 v[76:79], a[72:75], a[128:131], v[76:79]\n"
"v_accvgpr_read_b32 v19, a239\n"
"v_lshlrev_b32 v12, 16, v16\n"
"v_mfma_f32_16x16x32_bf16 v[80:83], a[72:75], a[136:139], v[80:83]\n"
"v_and_b32 v13, s63, v16\n"
"v_mfma_f32_16x16x32_bf16 v[80:83], a[76:79], a[140:143], v[80:83]\n"
"v_lshlrev_b32 v14, 16, v17\n"
"v_mfma_f32_16x16x32_bf16 v[52:55], a[68:71], a[140:143], v[52:55]\n"
"v_and_b32 v15, s63, v17\n"
"v_mul_f32 v12, v12, v220\n"
"v_mfma_f32_16x16x32_bf16 v[52:55], a[64:67], a[136:139], v[52:55]\n"
"s_waitcnt lgkmcnt(3)\n"
"v_mul_f32 v13, v13, v221\n"
"v_mfma_f32_16x16x32_bf16 v[56:59], a[64:67], a[144:147], v[56:59]\n"
"v_mul_f32 v14, v14, v222\n"
"v_mul_f32 v15, v15, v223\n"
"v_mfma_f32_16x16x32_bf16 v[56:59], a[68:71], a[148:151], v[56:59]\n"
"global_load_dwordx4 a[60:63], v0, s[34:35] offset:3072 sc1\n"
"v_cvt_pk_bf16_f32 v28, v12, v13\n"
"v_mfma_f32_16x16x32_bf16 v[84:87], a[76:79], a[148:151], v[84:87]\n"
"v_cvt_pk_bf16_f32 v29, v14, v15\n"
"v_mfma_f32_16x16x32_bf16 v[84:87], a[72:75], a[144:147], v[84:87]\n"
"v_lshlrev_b32 v12, 16, v18\n"
"v_and_b32 v13, s63, v18\n"
"v_mfma_f32_16x16x32_bf16 a[4:7], a[72:75], a[152:155], a[4:7]\n"
"s_add_u32 s34, s34, 8192\n"
"v_lshlrev_b32 v14, 16, v19\n"
"v_and_b32 v15, s63, v19\n"
"v_mfma_f32_16x16x32_bf16 a[4:7], a[76:79], a[156:159], a[4:7]\n"
"v_mul_f32 v12, v12, v248\n"
"v_mul_f32 v13, v13, v249\n"
"v_mfma_f32_16x16x32_bf16 a[0:3], a[68:71], a[156:159], a[0:3]\n"
"v_mul_f32 v14, v14, v250\n"
"v_mul_f32 v15, v15, v251\n"
"v_mfma_f32_16x16x32_bf16 a[0:3], a[64:67], a[152:155], a[0:3]\n"
"s_waitcnt vmcnt(9)\n"
"v_cvt_pk_bf16_f32 v30, v12, v13\n"
"v_cvt_pk_bf16_f32 v31, v14, v15\n"
"v_mfma_f32_16x16x32_bf16 v[88:91], a[80:83], a[96:99], v[88:91]\n"
"global_store_dwordx4 v0, v[28:31], s[54:55] offset:-1024 sc1\n"
"ds_write_b128 v2, v[28:31] offset:27648\n"
"v_mfma_f32_16x16x32_bf16 v[88:91], a[84:87], a[100:103], v[88:91]\n"
"global_load_dwordx4 a[64:67], v0, s[34:35] offset:-4096 sc1\n"
"v_mfma_f32_16x16x32_bf16 v[116:119], a[92:95], a[100:103], v[116:119]\n"
"ds_read_b128 a[100:103], v2 offset:1024\n"
"v_accvgpr_read_b32 v16, a240\n"
"v_mfma_f32_16x16x32_bf16 v[116:119], a[88:91], a[96:99], v[116:119]\n"
"ds_read_b128 a[96:99], v2 offset:0\n"
"v_accvgpr_read_b32 v17, a241\n"
"v_mfma_f32_16x16x32_bf16 v[120:123], a[88:91], a[104:107], v[120:123]\n"
"v_accvgpr_read_b32 v18, a242\n"
"v_accvgpr_read_b32 v19, a243\n"
"v_mfma_f32_16x16x32_bf16 v[120:123], a[92:95], a[108:111], v[120:123]\n"
"v_lshlrev_b32 v12, 16, v16\n"
"v_and_b32 v13, s63, v16\n"
"v_mfma_f32_16x16x32_bf16 v[92:95], a[84:87], a[108:111], v[92:95]\n"
"ds_read_b128 a[108:111], v2 offset:3072\n"
"v_lshlrev_b32 v14, 16, v17\n"
"v_mfma_f32_16x16x32_bf16 v[92:95], a[80:83], a[104:107], v[92:95]\n"
"ds_read_b128 a[104:107], v2 offset:2048\n"
"v_and_b32 v15, s63, v17\n"
"v_mfma_f32_16x16x32_bf16 v[96:99], a[80:83], a[112:115], v[96:99]\n"
"v_mul_f32 v12, v12, v168\n"
"v_mul_f32 v13, v13, v169\n"
"v_mfma_f32_16x16x32_bf16 v[96:99], a[84:87], a[116:119], v[96:99]\n"
"global_load_dwordx4 a[68:71], v0, s[34:35] offset:-3072 sc1\n"
"v_mul_f32 v14, v14, v170\n"
"v_mfma_f32_16x16x32_bf16 v[124:127], a[92:95], a[116:119], v[124:127]\n"
"ds_read_b128 a[116:119], v2 offset:5120\n"
"v_mul_f32 v15, v15, v171\n"
"v_mfma_f32_16x16x32_bf16 v[124:127], a[88:91], a[112:115], v[124:127]\n"
"ds_read_b128 a[112:115], v2 offset:4096\n"
"v_cvt_pk_bf16_f32 v24, v12, v13\n"
"v_mfma_f32_16x16x32_bf16 v[128:131], a[88:91], a[120:123], v[128:131]\n"
"v_cvt_pk_bf16_f32 v25, v14, v15\n"
"v_lshlrev_b32 v12, 16, v18\n"
"v_mfma_f32_16x16x32_bf16 v[128:131], a[92:95], a[124:127], v[128:131]\n"
"v_and_b32 v13, s63, v18\n"
"v_lshlrev_b32 v14, 16, v19\n"
"v_mfma_f32_16x16x32_bf16 v[100:103], a[84:87], a[124:127], v[100:103]\n"
"ds_read_b128 a[124:127], v2 offset:7168\n"
"v_and_b32 v15, s63, v19\n"
"v_mfma_f32_16x16x32_bf16 v[100:103], a[80:83], a[120:123], v[100:103]\n"
"ds_read_b128 a[120:123], v2 offset:6144\n"
"v_mul_f32 v12, v12, v196\n"
"v_mfma_f32_16x16x32_bf16 v[104:107], a[80:83], a[128:131], v[104:107]\n"
"v_mul_f32 v13, v13, v197\n"
"v_mul_f32 v14, v14, v198\n"
"v_mfma_f32_16x16x32_bf16 v[104:107], a[84:87], a[132:135], v[104:107]\n"
"global_load_dwordx4 a[72:75], v0, s[34:35] offset:-2048 sc1\n"
"v_mul_f32 v15, v15, v199\n"
"v_mfma_f32_16x16x32_bf16 v[132:135], a[92:95], a[132:135], v[132:135]\n"
"ds_read_b128 a[132:135], v2 offset:9216\n"
"v_cvt_pk_bf16_f32 v26, v12, v13\n"
"v_mfma_f32_16x16x32_bf16 v[132:135], a[88:91], a[128:131], v[132:135]\n"
"ds_read_b128 a[128:131], v2 offset:8192\n"
"v_cvt_pk_bf16_f32 v27, v14, v15\n"
"v_mfma_f32_16x16x32_bf16 v[136:139], a[88:91], a[136:139], v[136:139]\n"
"global_store_dwordx4 v0, v[24:27], s[54:55] offset:0 sc1\n"
"ds_write_b128 v2, v[24:27] offset:28672\n"
"v_mfma_f32_16x16x32_bf16 v[136:139], a[92:95], a[140:143], v[136:139]\n"
"v_accvgpr_read_b32 v16, a248\n"
"v_mfma_f32_16x16x32_bf16 v[108:111], a[84:87], a[140:143], v[108:111]\n"
"ds_read_b128 a[140:143], v2 offset:11264\n"
"v_mfma_f32_16x16x32_bf16 v[108:111], a[80:83], a[136:139], v[108:111]\n"
"ds_read_b128 a[136:139], v2 offset:10240\n"
"v_accvgpr_read_b32 v17, a249\n"
"v_mfma_f32_16x16x32_bf16 v[112:115], a[80:83], a[144:147], v[112:115]\n"
"v_accvgpr_read_b32 v18, a250\n"
"v_accvgpr_read_b32 v19, a251\n"
"v_mfma_f32_16x16x32_bf16 v[112:115], a[84:87], a[148:151], v[112:115]\n"
"global_load_dwordx4 a[76:79], v0, s[34:35] offset:-1024 sc1\n"
"v_lshlrev_b32 v12, 16, v16\n"
"v_mfma_f32_16x16x32_bf16 v[140:143], a[92:95], a[148:151], v[140:143]\n"
"ds_read_b128 a[148:151], v2 offset:13312\n"
"v_and_b32 v13, s63, v16\n"
"v_mfma_f32_16x16x32_bf16 v[140:143], a[88:91], a[144:147], v[140:143]\n"
"ds_read_b128 a[144:147], v2 offset:12288\n"
"v_lshlrev_b32 v14, 16, v17\n"
"v_mfma_f32_16x16x32_bf16 a[12:15], a[88:91], a[152:155], a[12:15]\n"
"v_and_b32 v15, s63, v17\n"
"v_accvgpr_read_b32 v10, a16\n"
"v_mfma_f32_16x16x32_bf16 a[12:15], a[92:95], a[156:159], a[12:15]\n"
"v_accvgpr_read_b32 v11, a17\n"
"v_mul_f32 v12, v12, v10\n"
"v_mfma_f32_16x16x32_bf16 a[8:11], a[84:87], a[156:159], a[8:11]\n"
"ds_read_b128 a[156:159], v2 offset:15360\n"
"v_mul_f32 v13, v13, v11\n"
"v_mfma_f32_16x16x32_bf16 a[8:11], a[80:83], a[152:155], a[8:11]\n"
"s_waitcnt vmcnt(11) lgkmcnt(12)\n"
"ds_read_b128 a[152:155], v2 offset:14336\n"
"v_accvgpr_read_b32 v10, a18\n"
"v_mfma_f32_16x16x32_bf16 v[144:147], a[32:35], a[96:99], v[144:147]\n"
"v_accvgpr_read_b32 v11, a19\n"
"v_mul_f32 v14, v14, v10\n"
"v_mfma_f32_16x16x32_bf16 v[144:147], a[36:39], a[100:103], v[144:147]\n"
"global_load_dwordx4 a[80:83], v0, s[34:35] offset:0 sc1\n"
"v_mul_f32 v15, v15, v11\n"
"v_mfma_f32_16x16x32_bf16 v[172:175], a[44:47], a[100:103], v[172:175]\n"
"v_cvt_pk_bf16_f32 v28, v12, v13\n"
"v_mfma_f32_16x16x32_bf16 v[172:175], a[40:43], a[96:99], v[172:175]\n"
"v_cvt_pk_bf16_f32 v29, v14, v15\n"
"v_lshlrev_b32 v12, 16, v18\n"
"v_mfma_f32_16x16x32_bf16 v[176:179], a[40:43], a[104:107], v[176:179]\n"
"v_and_b32 v13, s63, v18\n"
"v_mfma_f32_16x16x32_bf16 v[176:179], a[44:47], a[108:111], v[176:179]\n"
"v_lshlrev_b32 v14, 16, v19\n"
"v_mfma_f32_16x16x32_bf16 v[148:151], a[36:39], a[108:111], v[148:151]\n"
"v_and_b32 v15, s63, v19\n"
"v_accvgpr_read_b32 v10, a20\n"
"v_mfma_f32_16x16x32_bf16 v[148:151], a[32:35], a[104:107], v[148:151]\n"
"s_waitcnt lgkmcnt(9)\n"
"v_accvgpr_read_b32 v11, a21\n"
"v_mfma_f32_16x16x32_bf16 v[152:155], a[32:35], a[112:115], v[152:155]\n"
"v_mul_f32 v12, v12, v10\n"
"v_mul_f32 v13, v13, v11\n"
"v_mfma_f32_16x16x32_bf16 v[152:155], a[36:39], a[116:119], v[152:155]\n"
"global_load_dwordx4 a[84:87], v0, s[34:35] offset:1024 sc1\n"
"v_accvgpr_read_b32 v10, a22\n"
"v_mfma_f32_16x16x32_bf16 v[180:183], a[44:47], a[116:119], v[180:183]\n"
"v_accvgpr_read_b32 v11, a23\n"
"v_mfma_f32_16x16x32_bf16 v[180:183], a[40:43], a[112:115], v[180:183]\n"
"v_mul_f32 v14, v14, v10\n"
"v_mul_f32 v15, v15, v11\n"
"v_mfma_f32_16x16x32_bf16 v[184:187], a[40:43], a[120:123], v[184:187]\n"
"v_cvt_pk_bf16_f32 v30, v12, v13\n"
"v_cvt_pk_bf16_f32 v31, v14, v15\n"
"v_mfma_f32_16x16x32_bf16 v[184:187], a[44:47], a[124:127], v[184:187]\n"
"global_store_dwordx4 v0, v[28:31], s[54:55] offset:2048 sc1\n"
"ds_write_b128 v2, v[28:31] offset:30720\n"
"v_mfma_f32_16x16x32_bf16 v[156:159], a[36:39], a[124:127], v[156:159]\n"
"v_accvgpr_read_b32 v16, a244\n"
"v_mfma_f32_16x16x32_bf16 v[156:159], a[32:35], a[120:123], v[156:159]\n"
"s_waitcnt lgkmcnt(5)\n"
"v_accvgpr_read_b32 v17, a245\n"
"v_accvgpr_read_b32 v18, a246\n"
"v_mfma_f32_16x16x32_bf16 v[160:163], a[32:35], a[128:131], v[160:163]\n"
"v_accvgpr_read_b32 v19, a247\n"
"v_lshlrev_b32 v12, 16, v16\n"
"v_mfma_f32_16x16x32_bf16 v[160:163], a[36:39], a[132:135], v[160:163]\n"
"global_load_dwordx4 a[88:91], v0, s[34:35] offset:2048 sc1\n"
"v_and_b32 v13, s63, v16\n"
"v_mfma_f32_16x16x32_bf16 v[188:191], a[44:47], a[132:135], v[188:191]\n"
"v_lshlrev_b32 v14, 16, v17\n"
"v_and_b32 v15, s63, v17\n"
"v_mfma_f32_16x16x32_bf16 v[188:191], a[40:43], a[128:131], v[188:191]\n"
"v_mul_f32 v12, v12, v224\n"
"v_mul_f32 v13, v13, v225\n"
"v_mfma_f32_16x16x32_bf16 v[192:195], a[40:43], a[136:139], v[192:195]\n"
"v_mul_f32 v14, v14, v226\n"
"v_mul_f32 v15, v15, v227\n"
"v_mfma_f32_16x16x32_bf16 v[192:195], a[44:47], a[140:143], v[192:195]\n"
"v_cvt_pk_bf16_f32 v24, v12, v13\n"
"v_cvt_pk_bf16_f32 v25, v14, v15\n"
"v_mfma_f32_16x16x32_bf16 v[164:167], a[36:39], a[140:143], v[164:167]\n"
"ds_write_b64 v2, v[24:25] offset:29696\n"
"v_mfma_f32_16x16x32_bf16 v[164:167], a[32:35], a[136:139], v[164:167]\n"
"s_waitcnt lgkmcnt(2)\n"
"v_mov_b32 v8, s65\n"
"v_mfma_f32_16x16x32_bf16 v[168:171], a[32:35], a[144:147], v[168:171]\n"
"v_mov_b32 v9, s60\n"
"v_mfma_f32_16x16x32_bf16 v[168:171], a[36:39], a[148:151], v[168:171]\n"
"global_load_dwordx4 a[92:95], v0, s[34:35] offset:3072 sc1\n"
"v_lshlrev_b32 v12, 16, v18\n"
"v_mfma_f32_16x16x32_bf16 v[196:199], a[44:47], a[148:151], v[196:199]\n"
"buffer_store_dwordx2 v[8:9], off, s[56:59], %13 sc1\n"
"v_and_b32 v13, s63, v18\n"
"v_mfma_f32_16x16x32_bf16 v[196:199], a[40:43], a[144:147], v[196:199]\n"
"v_lshlrev_b32 v14, 16, v19\n"
"v_and_b32 v15, s63, v19\n"
"v_mfma_f32_16x16x32_bf16 a[20:23], a[40:43], a[152:155], a[20:23]\n"
"s_add_u32 s34, s34, 8192\n"
"v_mul_f32 v12, v12, v252\n"
"v_mul_f32 v13, v13, v253\n"
"v_mfma_f32_16x16x32_bf16 a[20:23], a[44:47], a[156:159], a[20:23]\n"
"v_mul_f32 v14, v14, v254\n"
"v_mul_f32 v15, v15, v255\n"
"v_mfma_f32_16x16x32_bf16 a[16:19], a[36:39], a[156:159], a[16:19]\n"
"v_cvt_pk_bf16_f32 v26, v12, v13\n"
"v_cvt_pk_bf16_f32 v27, v14, v15\n"
"v_mfma_f32_16x16x32_bf16 a[16:19], a[32:35], a[152:155], a[16:19]\n"
"s_waitcnt vmcnt(12)\n"
"ds_write_b64 v2, v[26:27] offset:29704\n"
"v_mfma_f32_16x16x32_bf16 v[200:203], a[48:51], a[96:99], v[200:203]\n"
"v_accvgpr_read_b32 v16, a252\n"
"v_mfma_f32_16x16x32_bf16 v[200:203], a[52:55], a[100:103], v[200:203]\n"
"global_load_dwordx4 a[32:35], v0, s[34:35] offset:-4096 sc1\n"
"v_accvgpr_read_b32 v17, a253\n"
"v_mfma_f32_16x16x32_bf16 v[228:231], a[60:63], a[100:103], v[228:231]\n"
"ds_read_b128 a[100:103], v3 offset:1024\n"
"v_accvgpr_read_b32 v18, a254\n"
"v_mfma_f32_16x16x32_bf16 v[228:231], a[56:59], a[96:99], v[228:231]\n"
"ds_read_b128 a[96:99], v3 offset:0\n"
"v_accvgpr_read_b32 v19, a255\n"
"v_mfma_f32_16x16x32_bf16 v[232:235], a[56:59], a[104:107], v[232:235]\n"
"v_lshlrev_b32 v12, 16, v16\n"
"v_and_b32 v13, s63, v16\n"
"v_mfma_f32_16x16x32_bf16 v[232:235], a[60:63], a[108:111], v[232:235]\n"
"v_lshlrev_b32 v14, 16, v17\n"
"v_and_b32 v15, s63, v17\n"
"v_mfma_f32_16x16x32_bf16 v[204:207], a[52:55], a[108:111], v[204:207]\n"
"ds_read_b128 a[108:111], v3 offset:3072\n"
"v_accvgpr_read_b32 v10, a24\n"
"v_mfma_f32_16x16x32_bf16 v[204:207], a[48:51], a[104:107], v[204:207]\n"
"ds_read_b128 a[104:107], v3 offset:2048\n"
"v_accvgpr_read_b32 v11, a25\n"
"v_mfma_f32_16x16x32_bf16 v[208:211], a[48:51], a[112:115], v[208:211]\n"
"v_mul_f32 v12, v12, v10\n"
"v_mul_f32 v13, v13, v11\n"
"v_mfma_f32_16x16x32_bf16 v[208:211], a[52:55], a[116:119], v[208:211]\n"
"global_load_dwordx4 a[36:39], v0, s[34:35] offset:-3072 sc1\n"
"v_accvgpr_read_b32 v10, a26\n"
"v_mfma_f32_16x16x32_bf16 v[236:239], a[60:63], a[116:119], v[236:239]\n"
"ds_read_b128 a[116:119], v3 offset:5120\n"
"v_accvgpr_read_b32 v11, a27\n"
"v_mfma_f32_16x16x32_bf16 v[236:239], a[56:59], a[112:115], v[236:239]\n"
"ds_read_b128 a[112:115], v3 offset:4096\n"
"v_mul_f32 v14, v14, v10\n"
"v_mfma_f32_16x16x32_bf16 v[240:243], a[56:59], a[120:123], v[240:243]\n"
"v_mul_f32 v15, v15, v11\n"
"v_cvt_pk_bf16_f32 v28, v12, v13\n"
"v_mfma_f32_16x16x32_bf16 v[240:243], a[60:63], a[124:127], v[240:243]\n"
"v_cvt_pk_bf16_f32 v29, v14, v15\n"
"v_lshlrev_b32 v12, 16, v18\n"
"v_mfma_f32_16x16x32_bf16 v[212:215], a[52:55], a[124:127], v[212:215]\n"
"ds_read_b128 a[124:127], v3 offset:7168\n"
"v_and_b32 v13, s63, v18\n"
"v_mfma_f32_16x16x32_bf16 v[212:215], a[48:51], a[120:123], v[212:215]\n"
"ds_read_b128 a[120:123], v3 offset:6144\n"
"v_lshlrev_b32 v14, 16, v19\n"
"v_mfma_f32_16x16x32_bf16 v[216:219], a[48:51], a[128:131], v[216:219]\n"
"v_and_b32 v15, s63, v19\n"
"v_accvgpr_read_b32 v10, a28\n"
"v_mfma_f32_16x16x32_bf16 v[216:219], a[52:55], a[132:135], v[216:219]\n"
"global_load_dwordx4 a[40:43], v0, s[34:35] offset:-2048 sc1\n"
"v_accvgpr_read_b32 v11, a29\n"
"v_mfma_f32_16x16x32_bf16 v[244:247], a[60:63], a[132:135], v[244:247]\n"
"ds_read_b128 a[132:135], v3 offset:9216\n"
"v_mul_f32 v12, v12, v10\n"
"v_mfma_f32_16x16x32_bf16 v[244:247], a[56:59], a[128:131], v[244:247]\n"
"ds_read_b128 a[128:131], v3 offset:8192\n"
"v_mul_f32 v13, v13, v11\n"
"v_mfma_f32_16x16x32_bf16 v[248:251], a[56:59], a[136:139], v[248:251]\n"
"v_accvgpr_read_b32 v10, a30\n"
"v_accvgpr_read_b32 v11, a31\n"
"v_mfma_f32_16x16x32_bf16 v[248:251], a[60:63], a[140:143], v[248:251]\n"
"v_mul_f32 v14, v14, v10\n"
"v_mul_f32 v15, v15, v11\n"
"v_mfma_f32_16x16x32_bf16 v[220:223], a[52:55], a[140:143], v[220:223]\n"
"ds_read_b128 a[140:143], v3 offset:11264\n"
"v_cvt_pk_bf16_f32 v30, v12, v13\n"
"v_mfma_f32_16x16x32_bf16 v[220:223], a[48:51], a[136:139], v[220:223]\n"
"ds_read_b128 a[136:139], v3 offset:10240\n"
"v_cvt_pk_bf16_f32 v31, v14, v15\n"
"v_mfma_f32_16x16x32_bf16 v[224:227], a[48:51], a[144:147], v[224:227]\n"
"global_store_dwordx4 v0, v[28:31], s[54:55] offset:3072 sc1\n"
"ds_write_b128 v2, v[28:31] offset:31744\n"
"v_mfma_f32_16x16x32_bf16 v[224:227], a[52:55], a[148:151], v[224:227]\n"
"global_load_dwordx4 a[44:47], v0, s[34:35] offset:-1024 sc1\n"
"v_mfma_f32_16x16x32_bf16 v[252:255], a[60:63], a[148:151], v[252:255]\n"
"ds_read_b128 a[148:151], v3 offset:13312\n"
"v_mfma_f32_16x16x32_bf16 v[252:255], a[56:59], a[144:147], v[252:255]\n"
"ds_read_b128 a[144:147], v3 offset:12288\n"
"v_mfma_f32_16x16x32_bf16 a[28:31], a[56:59], a[152:155], a[28:31]\n"
"v_mfma_f32_16x16x32_bf16 a[28:31], a[60:63], a[156:159], a[28:31]\n"
"v_mfma_f32_16x16x32_bf16 a[24:27], a[52:55], a[156:159], a[24:27]\n"
"ds_read_b128 a[156:159], v3 offset:15360\n"
"v_mfma_f32_16x16x32_bf16 a[24:27], a[48:51], a[152:155], a[24:27]\n"
"s_waitcnt vmcnt(11) lgkmcnt(12)\n"
"ds_read_b128 a[152:155], v3 offset:14336\n"
"v_mfma_f32_16x16x32_bf16 v[144:147], a[64:67], a[96:99], v[144:147]\n"
"ds_read_b128 v[24:27], v2 offset:16384\n"
"v_mfma_f32_16x16x32_bf16 v[144:147], a[68:71], a[100:103], v[144:147]\n"
"global_load_dwordx4 a[48:51], v0, s[34:35] offset:0 sc1\n"
"v_mfma_f32_16x16x32_bf16 v[172:175], a[76:79], a[100:103], v[172:175]\n"
"v_mfma_f32_16x16x32_bf16 v[172:175], a[72:75], a[96:99], v[172:175]\n"
"v_mfma_f32_16x16x32_bf16 v[176:179], a[72:75], a[104:107], v[176:179]\n"
"v_mfma_f32_16x16x32_bf16 v[176:179], a[76:79], a[108:111], v[176:179]\n"
"v_mfma_f32_16x16x32_bf16 v[148:151], a[68:71], a[108:111], v[148:151]\n"
"v_mfma_f32_16x16x32_bf16 v[148:151], a[64:67], a[104:107], v[148:151]\n"
"s_waitcnt lgkmcnt(10)\n"
"v_mfma_f32_16x16x32_bf16 v[152:155], a[64:67], a[112:115], v[152:155]\n"
"v_mfma_f32_16x16x32_bf16 v[152:155], a[68:71], a[116:119], v[152:155]\n"
"global_load_dwordx4 a[52:55], v0, s[34:35] offset:1024 sc1\n"
"v_mfma_f32_16x16x32_bf16 v[180:183], a[76:79], a[116:119], v[180:183]\n"
"v_mfma_f32_16x16x32_bf16 v[180:183], a[72:75], a[112:115], v[180:183]\n"
"v_mfma_f32_16x16x32_bf16 v[184:187], a[72:75], a[120:123], v[184:187]\n"
"s_waitcnt lgkmcnt(0)\n"
"global_store_dwordx4 v0, v[24:27], s[52:53] offset:-4096 sc1\n"
"v_mfma_f32_16x16x32_bf16 v[184:187], a[76:79], a[124:127], v[184:187]\n"
"v_mfma_f32_16x16x32_bf16 v[156:159], a[68:71], a[124:127], v[156:159]\n"
"v_mfma_f32_16x16x32_bf16 v[156:159], a[64:67], a[120:123], v[156:159]\n"
"v_mfma_f32_16x16x32_bf16 v[160:163], a[64:67], a[128:131], v[160:163]\n"
"v_mfma_f32_16x16x32_bf16 v[160:163], a[68:71], a[132:135], v[160:163]\n"
"global_load_dwordx4 a[56:59], v0, s[34:35] offset:2048 sc1\n"
"v_mfma_f32_16x16x32_bf16 v[188:191], a[76:79], a[132:135], v[188:191]\n"
"v_mfma_f32_16x16x32_bf16 v[188:191], a[72:75], a[128:131], v[188:191]\n"
"v_mfma_f32_16x16x32_bf16 v[192:195], a[72:75], a[136:139], v[192:195]\n"
"v_mfma_f32_16x16x32_bf16 v[192:195], a[76:79], a[140:143], v[192:195]\n"
"v_mfma_f32_16x16x32_bf16 v[164:167], a[68:71], a[140:143], v[164:167]\n"
"v_mfma_f32_16x16x32_bf16 v[164:167], a[64:67], a[136:139], v[164:167]\n"
"v_mfma_f32_16x16x32_bf16 v[168:171], a[64:67], a[144:147], v[168:171]\n"
"v_mfma_f32_16x16x32_bf16 v[168:171], a[68:71], a[148:151], v[168:171]\n"
"global_load_dwordx4 a[60:63], v0, s[34:35] offset:3072 sc1\n"
"v_mfma_f32_16x16x32_bf16 v[196:199], a[76:79], a[148:151], v[196:199]\n"
"v_mfma_f32_16x16x32_bf16 v[196:199], a[72:75], a[144:147], v[196:199]\n"
"v_mfma_f32_16x16x32_bf16 a[20:23], a[72:75], a[152:155], a[20:23]\n"
"s_add_u32 s34, s34, 8192\n"
"v_mfma_f32_16x16x32_bf16 a[20:23], a[76:79], a[156:159], a[20:23]\n"
"v_mfma_f32_16x16x32_bf16 a[16:19], a[68:71], a[156:159], a[16:19]\n"
"v_mfma_f32_16x16x32_bf16 a[16:19], a[64:67], a[152:155], a[16:19]\n"
"s_waitcnt vmcnt(11)\n"
"ds_read_b128 v[28:31], v2 offset:17408\n"
"v_mfma_f32_16x16x32_bf16 v[200:203], a[80:83], a[96:99], v[200:203]\n"
"v_mfma_f32_16x16x32_bf16 v[200:203], a[84:87], a[100:103], v[200:203]\n"
"global_load_dwordx4 a[64:67], v0, s[34:35] offset:-4096 sc1\n"
"v_mfma_f32_16x16x32_bf16 v[228:231], a[92:95], a[100:103], v[228:231]\n"
"ds_read_b128 a[100:103], v4 offset:1024\n"
"v_mfma_f32_16x16x32_bf16 v[228:231], a[88:91], a[96:99], v[228:231]\n"
"ds_read_b128 a[96:99], v4 offset:0\n"
"v_mfma_f32_16x16x32_bf16 v[232:235], a[88:91], a[104:107], v[232:235]\n"
"v_mfma_f32_16x16x32_bf16 v[232:235], a[92:95], a[108:111], v[232:235]\n"
"v_mfma_f32_16x16x32_bf16 v[204:207], a[84:87], a[108:111], v[204:207]\n"
"ds_read_b128 a[108:111], v4 offset:3072\n"
"v_mfma_f32_16x16x32_bf16 v[204:207], a[80:83], a[104:107], v[204:207]\n"
"ds_read_b128 a[104:107], v4 offset:2048\n"
"v_mfma_f32_16x16x32_bf16 v[208:211], a[80:83], a[112:115], v[208:211]\n"
"v_mfma_f32_16x16x32_bf16 v[208:211], a[84:87], a[116:119], v[208:211]\n"
"global_load_dwordx4 a[68:71], v0, s[34:35] offset:-3072 sc1\n"
"v_mfma_f32_16x16x32_bf16 v[236:239], a[92:95], a[116:119], v[236:239]\n"
"s_waitcnt lgkmcnt(4)\n"
"global_store_dwordx4 v0, v[28:31], s[52:53] offset:-3072 sc1\n"
"ds_read_b128 a[116:119], v4 offset:5120\n"
"v_mfma_f32_16x16x32_bf16 v[236:239], a[88:91], a[112:115], v[236:239]\n"
"ds_read_b128 a[112:115], v4 offset:4096\n"
"v_mfma_f32_16x16x32_bf16 v[240:243], a[88:91], a[120:123], v[240:243]\n"
"v_mfma_f32_16x16x32_bf16 v[240:243], a[92:95], a[124:127], v[240:243]\n"
"v_mfma_f32_16x16x32_bf16 v[212:215], a[84:87], a[124:127], v[212:215]\n"
"ds_read_b128 a[124:127], v4 offset:7168\n"
"v_mfma_f32_16x16x32_bf16 v[212:215], a[80:83], a[120:123], v[212:215]\n"
"ds_read_b128 a[120:123], v4 offset:6144\n"
"v_mfma_f32_16x16x32_bf16 v[216:219], a[80:83], a[128:131], v[216:219]\n"
"v_mfma_f32_16x16x32_bf16 v[216:219], a[84:87], a[132:135], v[216:219]\n"
"global_load_dwordx4 a[72:75], v0, s[34:35] offset:-2048 sc1\n"
"v_mfma_f32_16x16x32_bf16 v[244:247], a[92:95], a[132:135], v[244:247]\n"
"ds_read_b128 a[132:135], v4 offset:9216\n"
"v_mfma_f32_16x16x32_bf16 v[244:247], a[88:91], a[128:131], v[244:247]\n"
"ds_read_b128 a[128:131], v4 offset:8192\n"
"v_mfma_f32_16x16x32_bf16 v[248:251], a[88:91], a[136:139], v[248:251]\n"
"v_mfma_f32_16x16x32_bf16 v[248:251], a[92:95], a[140:143], v[248:251]\n"
"v_mfma_f32_16x16x32_bf16 v[220:223], a[84:87], a[140:143], v[220:223]\n"
"ds_read_b128 a[140:143], v4 offset:11264\n"
"v_mfma_f32_16x16x32_bf16 v[220:223], a[80:83], a[136:139], v[220:223]\n"
"ds_read_b128 a[136:139], v4 offset:10240\n"
"v_mfma_f32_16x16x32_bf16 v[224:227], a[80:83], a[144:147], v[224:227]\n"
"v_mfma_f32_16x16x32_bf16 v[224:227], a[84:87], a[148:151], v[224:227]\n"
"global_load_dwordx4 a[76:79], v0, s[34:35] offset:-1024 sc1\n"
"v_mfma_f32_16x16x32_bf16 v[252:255], a[92:95], a[148:151], v[252:255]\n"
"ds_read_b128 a[148:151], v4 offset:13312\n"
"v_mfma_f32_16x16x32_bf16 v[252:255], a[88:91], a[144:147], v[252:255]\n"
"ds_read_b128 a[144:147], v4 offset:12288\n"
"v_mfma_f32_16x16x32_bf16 a[28:31], a[88:91], a[152:155], a[28:31]\n"
"v_mfma_f32_16x16x32_bf16 a[28:31], a[92:95], a[156:159], a[28:31]\n"
"ds_read_b128 v[24:27], v2 offset:18432\n"
"v_mfma_f32_16x16x32_bf16 a[24:27], a[84:87], a[156:159], a[24:27]\n"
"ds_read_b128 a[156:159], v4 offset:15360\n"
"v_mfma_f32_16x16x32_bf16 a[24:27], a[80:83], a[152:155], a[24:27]\n"
"s_waitcnt vmcnt(10) lgkmcnt(12)\n"
"ds_read_b128 a[152:155], v4 offset:14336\n"
"v_mfma_f32_16x16x32_bf16 v[144:147], a[32:35], a[96:99], v[144:147]\n"
"v_mfma_f32_16x16x32_bf16 v[144:147], a[36:39], a[100:103], v[144:147]\n"
"global_load_dwordx4 a[80:83], v0, s[34:35] offset:0 sc1\n"
"v_mfma_f32_16x16x32_bf16 v[172:175], a[44:47], a[100:103], v[172:175]\n"
"v_mfma_f32_16x16x32_bf16 v[172:175], a[40:43], a[96:99], v[172:175]\n"
"v_mfma_f32_16x16x32_bf16 v[176:179], a[40:43], a[104:107], v[176:179]\n"
"v_mfma_f32_16x16x32_bf16 v[176:179], a[44:47], a[108:111], v[176:179]\n"
"v_mfma_f32_16x16x32_bf16 v[148:151], a[36:39], a[108:111], v[148:151]\n"
"v_mfma_f32_16x16x32_bf16 v[148:151], a[32:35], a[104:107], v[148:151]\n"
"s_waitcnt lgkmcnt(9)\n"
"v_mfma_f32_16x16x32_bf16 v[152:155], a[32:35], a[112:115], v[152:155]\n"
"s_waitcnt lgkmcnt(2)\n"
"global_store_dwordx4 v0, v[24:27], s[52:53] offset:-2048 sc1\n"
"v_mfma_f32_16x16x32_bf16 v[152:155], a[36:39], a[116:119], v[152:155]\n"
"global_load_dwordx4 a[84:87], v0, s[34:35] offset:1024 sc1\n"
"v_mfma_f32_16x16x32_bf16 v[180:183], a[44:47], a[116:119], v[180:183]\n"
"v_mfma_f32_16x16x32_bf16 v[180:183], a[40:43], a[112:115], v[180:183]\n"
"v_mfma_f32_16x16x32_bf16 v[184:187], a[40:43], a[120:123], v[184:187]\n"
"v_mfma_f32_16x16x32_bf16 v[184:187], a[44:47], a[124:127], v[184:187]\n"
"v_mfma_f32_16x16x32_bf16 v[156:159], a[36:39], a[124:127], v[156:159]\n"
"v_mfma_f32_16x16x32_bf16 v[156:159], a[32:35], a[120:123], v[156:159]\n"
"v_mfma_f32_16x16x32_bf16 v[160:163], a[32:35], a[128:131], v[160:163]\n"
"v_mfma_f32_16x16x32_bf16 v[160:163], a[36:39], a[132:135], v[160:163]\n"
"global_load_dwordx4 a[88:91], v0, s[34:35] offset:2048 sc1\n"
"v_mfma_f32_16x16x32_bf16 v[188:191], a[44:47], a[132:135], v[188:191]\n"
"v_mfma_f32_16x16x32_bf16 v[188:191], a[40:43], a[128:131], v[188:191]\n"
"v_mfma_f32_16x16x32_bf16 v[192:195], a[40:43], a[136:139], v[192:195]\n"
"v_mfma_f32_16x16x32_bf16 v[192:195], a[44:47], a[140:143], v[192:195]\n"
"v_mfma_f32_16x16x32_bf16 v[164:167], a[36:39], a[140:143], v[164:167]\n"
"v_mfma_f32_16x16x32_bf16 v[164:167], a[32:35], a[136:139], v[164:167]\n"
"s_waitcnt lgkmcnt(0)\n"
"v_mfma_f32_16x16x32_bf16 v[168:171], a[32:35], a[144:147], v[168:171]\n"
"v_mfma_f32_16x16x32_bf16 v[168:171], a[36:39], a[148:151], v[168:171]\n"
"global_load_dwordx4 a[92:95], v0, s[34:35] offset:3072 sc1\n"
"v_mfma_f32_16x16x32_bf16 v[196:199], a[44:47], a[148:151], v[196:199]\n"
"v_mfma_f32_16x16x32_bf16 v[196:199], a[40:43], a[144:147], v[196:199]\n"
"ds_read_b128 v[28:31], v2 offset:19456\n"
"v_mfma_f32_16x16x32_bf16 a[20:23], a[40:43], a[152:155], a[20:23]\n"
"s_add_u32 s34, s34, 8192\n"
"v_mfma_f32_16x16x32_bf16 a[20:23], a[44:47], a[156:159], a[20:23]\n"
"v_mfma_f32_16x16x32_bf16 a[16:19], a[36:39], a[156:159], a[16:19]\n"
"v_mfma_f32_16x16x32_bf16 a[16:19], a[32:35], a[152:155], a[16:19]\n"
"s_waitcnt vmcnt(10)\n"
"v_mfma_f32_16x16x32_bf16 v[200:203], a[48:51], a[96:99], v[200:203]\n"
"v_mfma_f32_16x16x32_bf16 v[200:203], a[52:55], a[100:103], v[200:203]\n"
"global_load_dwordx4 a[32:35], v0, s[34:35] offset:-4096 sc1\n"
"v_mfma_f32_16x16x32_bf16 v[228:231], a[60:63], a[100:103], v[228:231]\n"
"ds_read_b128 a[100:103], v5 offset:1024\n"
"v_mfma_f32_16x16x32_bf16 v[228:231], a[56:59], a[96:99], v[228:231]\n"
"ds_read_b128 a[96:99], v5 offset:0\n"
"v_mfma_f32_16x16x32_bf16 v[232:235], a[56:59], a[104:107], v[232:235]\n"
"v_mfma_f32_16x16x32_bf16 v[232:235], a[60:63], a[108:111], v[232:235]\n"
"v_mfma_f32_16x16x32_bf16 v[204:207], a[52:55], a[108:111], v[204:207]\n"
"s_waitcnt lgkmcnt(2)\n"
"global_store_dwordx4 v0, v[28:31], s[52:53] offset:-1024 sc1\n"
"ds_read_b128 a[108:111], v5 offset:3072\n"
"v_mfma_f32_16x16x32_bf16 v[204:207], a[48:51], a[104:107], v[204:207]\n"
"ds_read_b128 a[104:107], v5 offset:2048\n"
"v_mfma_f32_16x16x32_bf16 v[208:211], a[48:51], a[112:115], v[208:211]\n"
"v_mfma_f32_16x16x32_bf16 v[208:211], a[52:55], a[116:119], v[208:211]\n"
"global_load_dwordx4 a[36:39], v0, s[34:35] offset:-3072 sc1\n"
"v_mfma_f32_16x16x32_bf16 v[236:239], a[60:63], a[116:119], v[236:239]\n"
"ds_read_b128 a[116:119], v5 offset:5120\n"
"v_mfma_f32_16x16x32_bf16 v[236:239], a[56:59], a[112:115], v[236:239]\n"
"ds_read_b128 a[112:115], v5 offset:4096\n"
"v_mfma_f32_16x16x32_bf16 v[240:243], a[56:59], a[120:123], v[240:243]\n"
"v_mfma_f32_16x16x32_bf16 v[240:243], a[60:63], a[124:127], v[240:243]\n"
"v_mfma_f32_16x16x32_bf16 v[212:215], a[52:55], a[124:127], v[212:215]\n"
"ds_read_b128 a[124:127], v5 offset:7168\n"
"v_mfma_f32_16x16x32_bf16 v[212:215], a[48:51], a[120:123], v[212:215]\n"
"ds_read_b128 a[120:123], v5 offset:6144\n"
"v_mfma_f32_16x16x32_bf16 v[216:219], a[48:51], a[128:131], v[216:219]\n"
"v_mfma_f32_16x16x32_bf16 v[216:219], a[52:55], a[132:135], v[216:219]\n"
"global_load_dwordx4 a[40:43], v0, s[34:35] offset:-2048 sc1\n"
"v_mfma_f32_16x16x32_bf16 v[244:247], a[60:63], a[132:135], v[244:247]\n"
"ds_read_b128 a[132:135], v5 offset:9216\n"
"v_mfma_f32_16x16x32_bf16 v[244:247], a[56:59], a[128:131], v[244:247]\n"
"ds_read_b128 a[128:131], v5 offset:8192\n"
"v_mfma_f32_16x16x32_bf16 v[248:251], a[56:59], a[136:139], v[248:251]\n"
"v_mfma_f32_16x16x32_bf16 v[248:251], a[60:63], a[140:143], v[248:251]\n"
"v_mfma_f32_16x16x32_bf16 v[220:223], a[52:55], a[140:143], v[220:223]\n"
"ds_read_b128 a[140:143], v5 offset:11264\n"
"v_mfma_f32_16x16x32_bf16 v[220:223], a[48:51], a[136:139], v[220:223]\n"
"ds_read_b128 a[136:139], v5 offset:10240\n"
"v_mfma_f32_16x16x32_bf16 v[224:227], a[48:51], a[144:147], v[224:227]\n"
"ds_read_b128 v[24:27], v2 offset:20480\n"
"v_mfma_f32_16x16x32_bf16 v[224:227], a[52:55], a[148:151], v[224:227]\n"
"global_load_dwordx4 a[44:47], v0, s[34:35] offset:-1024 sc1\n"
"v_mfma_f32_16x16x32_bf16 v[252:255], a[60:63], a[148:151], v[252:255]\n"
"ds_read_b128 a[148:151], v5 offset:13312\n"
"v_mfma_f32_16x16x32_bf16 v[252:255], a[56:59], a[144:147], v[252:255]\n"
"ds_read_b128 a[144:147], v5 offset:12288\n"
"v_mfma_f32_16x16x32_bf16 a[28:31], a[56:59], a[152:155], a[28:31]\n"
"v_mfma_f32_16x16x32_bf16 a[28:31], a[60:63], a[156:159], a[28:31]\n"
"v_mfma_f32_16x16x32_bf16 a[24:27], a[52:55], a[156:159], a[24:27]\n"
"ds_read_b128 a[156:159], v5 offset:15360\n"
"v_mfma_f32_16x16x32_bf16 a[24:27], a[48:51], a[152:155], a[24:27]\n"
"s_waitcnt vmcnt(10) lgkmcnt(12)\n"
"ds_read_b128 a[152:155], v5 offset:14336\n"
"v_mfma_f32_16x16x32_bf16 v[144:147], a[64:67], a[96:99], v[144:147]\n"
"v_mfma_f32_16x16x32_bf16 v[144:147], a[68:71], a[100:103], v[144:147]\n"
"global_load_dwordx4 a[48:51], v0, s[34:35] offset:0 sc1\n"
"v_mfma_f32_16x16x32_bf16 v[172:175], a[76:79], a[100:103], v[172:175]\n"
"v_mfma_f32_16x16x32_bf16 v[172:175], a[72:75], a[96:99], v[172:175]\n"
"v_mfma_f32_16x16x32_bf16 v[176:179], a[72:75], a[104:107], v[176:179]\n"
"s_waitcnt lgkmcnt(4)\n"
"global_store_dwordx4 v0, v[24:27], s[52:53] offset:0 sc1\n"
"v_mfma_f32_16x16x32_bf16 v[176:179], a[76:79], a[108:111], v[176:179]\n"
"v_mfma_f32_16x16x32_bf16 v[148:151], a[68:71], a[108:111], v[148:151]\n"
"v_mfma_f32_16x16x32_bf16 v[148:151], a[64:67], a[104:107], v[148:151]\n"
"v_mfma_f32_16x16x32_bf16 v[152:155], a[64:67], a[112:115], v[152:155]\n"
"v_mfma_f32_16x16x32_bf16 v[152:155], a[68:71], a[116:119], v[152:155]\n"
"global_load_dwordx4 a[52:55], v0, s[34:35] offset:1024 sc1\n"
"v_mfma_f32_16x16x32_bf16 v[180:183], a[76:79], a[116:119], v[180:183]\n"
"v_mfma_f32_16x16x32_bf16 v[180:183], a[72:75], a[112:115], v[180:183]\n"
"v_mfma_f32_16x16x32_bf16 v[184:187], a[72:75], a[120:123], v[184:187]\n"
"v_mfma_f32_16x16x32_bf16 v[184:187], a[76:79], a[124:127], v[184:187]\n"
"v_mfma_f32_16x16x32_bf16 v[156:159], a[68:71], a[124:127], v[156:159]\n"
"v_mfma_f32_16x16x32_bf16 v[156:159], a[64:67], a[120:123], v[156:159]\n"
"v_mfma_f32_16x16x32_bf16 v[160:163], a[64:67], a[128:131], v[160:163]\n"
"v_mfma_f32_16x16x32_bf16 v[160:163], a[68:71], a[132:135], v[160:163]\n"
"global_load_dwordx4 a[56:59], v0, s[34:35] offset:2048 sc1\n"
"v_mfma_f32_16x16x32_bf16 v[188:191], a[76:79], a[132:135], v[188:191]\n"
"v_mfma_f32_16x16x32_bf16 v[188:191], a[72:75], a[128:131], v[188:191]\n"
"v_mfma_f32_16x16x32_bf16 v[192:195], a[72:75], a[136:139], v[192:195]\n"
"v_mfma_f32_16x16x32_bf16 v[192:195], a[76:79], a[140:143], v[192:195]\n"
"v_mfma_f32_16x16x32_bf16 v[164:167], a[68:71], a[140:143], v[164:167]\n"
"v_mfma_f32_16x16x32_bf16 v[164:167], a[64:67], a[136:139], v[164:167]\n"
"s_waitcnt lgkmcnt(0)\n"
"ds_read_b128 v[28:31], v2 offset:21504\n"
"v_mfma_f32_16x16x32_bf16 v[168:171], a[64:67], a[144:147], v[168:171]\n"
"v_mfma_f32_16x16x32_bf16 v[168:171], a[68:71], a[148:151], v[168:171]\n"
"global_load_dwordx4 a[60:63], v0, s[34:35] offset:3072 sc1\n"
"v_mfma_f32_16x16x32_bf16 v[196:199], a[76:79], a[148:151], v[196:199]\n"
"v_mfma_f32_16x16x32_bf16 v[196:199], a[72:75], a[144:147], v[196:199]\n"
"v_mfma_f32_16x16x32_bf16 a[20:23], a[72:75], a[152:155], a[20:23]\n"
"s_add_u32 s34, s34, 8192\n"
"v_mfma_f32_16x16x32_bf16 a[20:23], a[76:79], a[156:159], a[20:23]\n"
"v_mfma_f32_16x16x32_bf16 a[16:19], a[68:71], a[156:159], a[16:19]\n"
"v_mfma_f32_16x16x32_bf16 a[16:19], a[64:67], a[152:155], a[16:19]\n"
"s_waitcnt vmcnt(10)\n"
"v_mfma_f32_16x16x32_bf16 v[200:203], a[80:83], a[96:99], v[200:203]\n"
"v_mfma_f32_16x16x32_bf16 v[200:203], a[84:87], a[100:103], v[200:203]\n"
"global_load_dwordx4 a[64:67], v0, s[34:35] offset:-4096 sc1\n"
"v_mfma_f32_16x16x32_bf16 v[228:231], a[92:95], a[100:103], v[228:231]\n"
"s_waitcnt lgkmcnt(0)\n"
"global_store_dwordx4 v0, v[28:31], s[52:53] offset:1024 sc1\n"
"ds_read_b128 a[100:103], v2 offset:17408\n"
"buffer_load_dwordx2 v[8:9], off, s[56:59], %14 sc1\n"
"v_mfma_f32_16x16x32_bf16 v[228:231], a[88:91], a[96:99], v[228:231]\n"
"ds_read_b128 a[96:99], v2 offset:16384\n"
"v_mfma_f32_16x16x32_bf16 v[232:235], a[88:91], a[104:107], v[232:235]\n"
"v_mfma_f32_16x16x32_bf16 v[232:235], a[92:95], a[108:111], v[232:235]\n"
"v_mfma_f32_16x16x32_bf16 v[204:207], a[84:87], a[108:111], v[204:207]\n"
"ds_read_b128 a[108:111], v2 offset:19456\n"
"v_mfma_f32_16x16x32_bf16 v[204:207], a[80:83], a[104:107], v[204:207]\n"
"ds_read_b128 a[104:107], v2 offset:18432\n"
"v_mfma_f32_16x16x32_bf16 v[208:211], a[80:83], a[112:115], v[208:211]\n"
"v_mfma_f32_16x16x32_bf16 v[208:211], a[84:87], a[116:119], v[208:211]\n"
"global_load_dwordx4 a[68:71], v0, s[34:35] offset:-3072 sc1\n"
"v_mfma_f32_16x16x32_bf16 v[236:239], a[92:95], a[116:119], v[236:239]\n"
"ds_read_b128 a[116:119], v2 offset:21504\n"
"v_mfma_f32_16x16x32_bf16 v[236:239], a[88:91], a[112:115], v[236:239]\n"
"ds_read_b128 a[112:115], v2 offset:20480\n"
"v_mfma_f32_16x16x32_bf16 v[240:243], a[88:91], a[120:123], v[240:243]\n"
"v_mfma_f32_16x16x32_bf16 v[240:243], a[92:95], a[124:127], v[240:243]\n"
"v_mfma_f32_16x16x32_bf16 v[212:215], a[84:87], a[124:127], v[212:215]\n"
"ds_read_b128 a[124:127], v2 offset:23552\n"
"v_mfma_f32_16x16x32_bf16 v[212:215], a[80:83], a[120:123], v[212:215]\n"
"ds_read_b128 a[120:123], v2 offset:22528\n"
"v_mfma_f32_16x16x32_bf16 v[216:219], a[80:83], a[128:131], v[216:219]\n"
"v_mfma_f32_16x16x32_bf16 v[216:219], a[84:87], a[132:135], v[216:219]\n"
"global_load_dwordx4 a[72:75], v0, s[34:35] offset:-2048 sc1\n"
"v_mfma_f32_16x16x32_bf16 v[244:247], a[92:95], a[132:135], v[244:247]\n"
"ds_read_b128 a[132:135], v2 offset:25600\n"
"v_mfma_f32_16x16x32_bf16 v[244:247], a[88:91], a[128:131], v[244:247]\n"
"ds_read_b128 a[128:131], v2 offset:24576\n"
"v_mfma_f32_16x16x32_bf16 v[248:251], a[88:91], a[136:139], v[248:251]\n"
"v_mfma_f32_16x16x32_bf16 v[248:251], a[92:95], a[140:143], v[248:251]\n"
"ds_read_b128 v[24:27], v2 offset:22528\n"
"v_mfma_f32_16x16x32_bf16 v[220:223], a[84:87], a[140:143], v[220:223]\n"
"ds_read_b128 a[140:143], v2 offset:27648\n"
"v_mfma_f32_16x16x32_bf16 v[220:223], a[80:83], a[136:139], v[220:223]\n"
"ds_read_b128 a[136:139], v2 offset:26624\n"
"v_mfma_f32_16x16x32_bf16 v[224:227], a[80:83], a[144:147], v[224:227]\n"
"v_mfma_f32_16x16x32_bf16 v[224:227], a[84:87], a[148:151], v[224:227]\n"
"global_load_dwordx4 a[76:79], v0, s[34:35] offset:-1024 sc1\n"
"v_mfma_f32_16x16x32_bf16 v[252:255], a[92:95], a[148:151], v[252:255]\n"
"ds_read_b128 a[148:151], v2 offset:29696\n"
"v_mfma_f32_16x16x32_bf16 v[252:255], a[88:91], a[144:147], v[252:255]\n"
"ds_read_b128 a[144:147], v2 offset:28672\n"
"v_mfma_f32_16x16x32_bf16 a[28:31], a[88:91], a[152:155], a[28:31]\n"
"v_mfma_f32_16x16x32_bf16 a[28:31], a[92:95], a[156:159], a[28:31]\n"
"v_mfma_f32_16x16x32_bf16 a[24:27], a[84:87], a[156:159], a[24:27]\n"
"ds_read_b128 a[156:159], v2 offset:31744\n"
"v_mfma_f32_16x16x32_bf16 a[24:27], a[80:83], a[152:155], a[24:27]\n"
"s_waitcnt vmcnt(11) lgkmcnt(12)\n"
"ds_read_b128 a[152:155], v2 offset:30720\n"
"v_mfma_f32_16x16x32_bf16 v[32:35], a[32:35], a[96:99], v[32:35]\n"
"s_waitcnt lgkmcnt(6)\n"
"global_store_dwordx4 v0, v[24:27], s[52:53] offset:2048 sc1\n"
"v_mfma_f32_16x16x32_bf16 v[32:35], a[36:39], a[100:103], v[32:35]\n"
"global_load_dwordx4 a[80:83], v0, s[34:35] offset:0 sc1\n"
"L_P3:\n"
"v_mfma_f32_16x16x32_bf16 v[60:63], a[44:47], a[100:103], v[60:63]\n"
"s_barrier\n"
"v_mfma_f32_16x16x32_bf16 v[60:63], a[40:43], a[96:99], v[60:63]\n"
"v_mfma_f32_16x16x32_bf16 v[64:67], a[40:43], a[104:107], v[64:67]\n"
"v_mfma_f32_16x16x32_bf16 v[64:67], a[44:47], a[108:111], v[64:67]\n"
"v_mfma_f32_16x16x32_bf16 v[36:39], a[36:39], a[108:111], v[36:39]\n"
"v_mfma_f32_16x16x32_bf16 v[36:39], a[32:35], a[104:107], v[36:39]\n"
"v_mfma_f32_16x16x32_bf16 v[40:43], a[32:35], a[112:115], v[40:43]\n"
"v_mfma_f32_16x16x32_bf16 v[40:43], a[36:39], a[116:119], v[40:43]\n"
"global_load_dwordx4 a[84:87], v0, s[34:35] offset:1024 sc1\n"
"v_mfma_f32_16x16x32_bf16 v[68:71], a[44:47], a[116:119], v[68:71]\n"
"v_mfma_f32_16x16x32_bf16 v[68:71], a[40:43], a[112:115], v[68:71]\n"
"v_mfma_f32_16x16x32_bf16 v[72:75], a[40:43], a[120:123], v[72:75]\n"
"v_mfma_f32_16x16x32_bf16 v[72:75], a[44:47], a[124:127], v[72:75]\n"
"v_mfma_f32_16x16x32_bf16 v[44:47], a[36:39], a[124:127], v[44:47]\n"
"v_mfma_f32_16x16x32_bf16 v[44:47], a[32:35], a[120:123], v[44:47]\n"
"s_waitcnt lgkmcnt(4)\n"
"v_mfma_f32_16x16x32_bf16 v[48:51], a[32:35], a[128:131], v[48:51]\n"
"v_mfma_f32_16x16x32_bf16 v[48:51], a[36:39], a[132:135], v[48:51]\n"
"global_load_dwordx4 a[88:91], v0, s[34:35] offset:2048 sc1\n"
"v_lshrrev_b32 v16, 4, v0\n"
"v_mfma_f32_16x16x32_bf16 v[76:79], a[44:47], a[132:135], v[76:79]\n"
"v_and_b32 v16, 0x30, v16\n"
"v_mfma_f32_16x16x32_bf16 v[76:79], a[40:43], a[128:131], v[76:79]\n"
"ds_read_b128 v[28:31], v2 offset:23552\n"
"buffer_load_dwordx2 v[18:19], v16, s[44:47], s61 offen offset:0\n"
"v_mfma_f32_16x16x32_bf16 v[80:83], a[40:43], a[136:139], v[80:83]\n"
"v_mfma_f32_16x16x32_bf16 v[80:83], a[44:47], a[140:143], v[80:83]\n"
"v_mfma_f32_16x16x32_bf16 v[52:55], a[36:39], a[140:143], v[52:55]\n"
"v_mfma_f32_16x16x32_bf16 v[52:55], a[32:35], a[136:139], v[52:55]\n"
"s_waitcnt lgkmcnt(1)\n"
"v_mfma_f32_16x16x32_bf16 v[56:59], a[32:35], a[144:147], v[56:59]\n"
"v_mfma_f32_16x16x32_bf16 v[56:59], a[36:39], a[148:151], v[56:59]\n"
"global_load_dwordx4 a[92:95], v0, s[34:35] offset:3072 sc1\n"
"v_lshrrev_b32 v20, 4, v0\n"
"v_mfma_f32_16x16x32_bf16 v[84:87], a[44:47], a[148:151], v[84:87]\n"
"v_and_b32 v20, 0x30, v20\n"
"v_mfma_f32_16x16x32_bf16 v[84:87], a[40:43], a[144:147], v[84:87]\n"
"buffer_load_dwordx2 v[22:23], v20, s[44:47], s61 offen offset:8\n"
"v_mfma_f32_16x16x32_bf16 a[4:7], a[40:43], a[152:155], a[4:7]\n"
"s_add_u32 s34, s34, 8192\n"
"v_mfma_f32_16x16x32_bf16 a[4:7], a[44:47], a[156:159], a[4:7]\n"
"v_mfma_f32_16x16x32_bf16 a[0:3], a[36:39], a[156:159], a[0:3]\n"
"s_waitcnt lgkmcnt(0)\n"
"global_store_dwordx4 v0, v[28:31], s[52:53] offset:3072 sc1\n"
"v_mfma_f32_16x16x32_bf16 a[0:3], a[32:35], a[152:155], a[0:3]\n"
"s_waitcnt vmcnt(14)\n"
"v_mfma_f32_16x16x32_bf16 v[88:91], a[48:51], a[96:99], v[88:91]\n"
"v_mfma_f32_16x16x32_bf16 v[88:91], a[52:55], a[100:103], v[88:91]\n"
"global_load_dwordx4 a[32:35], v0, s[34:35] offset:-4096 sc1\n"
"v_mfma_f32_16x16x32_bf16 v[116:119], a[60:63], a[100:103], v[116:119]\n"
"v_mfma_f32_16x16x32_bf16 v[116:119], a[56:59], a[96:99], v[116:119]\n"
"v_mfma_f32_16x16x32_bf16 v[120:123], a[56:59], a[104:107], v[120:123]\n"
"v_mfma_f32_16x16x32_bf16 v[120:123], a[60:63], a[108:111], v[120:123]\n"
"v_mfma_f32_16x16x32_bf16 v[92:95], a[52:55], a[108:111], v[92:95]\n"
"v_mfma_f32_16x16x32_bf16 v[92:95], a[48:51], a[104:107], v[92:95]\n"
"v_mfma_f32_16x16x32_bf16 v[96:99], a[48:51], a[112:115], v[96:99]\n"
"v_mfma_f32_16x16x32_bf16 v[96:99], a[52:55], a[116:119], v[96:99]\n"
"global_load_dwordx4 a[36:39], v0, s[34:35] offset:-3072 sc1\n"
"v_mfma_f32_16x16x32_bf16 v[124:127], a[60:63], a[116:119], v[124:127]\n"
"v_mfma_f32_16x16x32_bf16 v[124:127], a[56:59], a[112:115], v[124:127]\n"
"v_mfma_f32_16x16x32_bf16 v[128:131], a[56:59], a[120:123], v[128:131]\n"
"v_mfma_f32_16x16x32_bf16 v[128:131], a[60:63], a[124:127], v[128:131]\n"
"v_mfma_f32_16x16x32_bf16 v[100:103], a[52:55], a[124:127], v[100:103]\n"
"v_mfma_f32_16x16x32_bf16 v[100:103], a[48:51], a[120:123], v[100:103]\n"
"v_mfma_f32_16x16x32_bf16 v[104:107], a[48:51], a[128:131], v[104:107]\n"
"ds_read_b128 v[24:27], v2 offset:24576\n"
"v_mfma_f32_16x16x32_bf16 v[104:107], a[52:55], a[132:135], v[104:107]\n"
"global_load_dwordx4 a[40:43], v0, s[34:35] offset:-2048 sc1\n"
"v_mfma_f32_16x16x32_bf16 v[132:135], a[60:63], a[132:135], v[132:135]\n"
"v_mfma_f32_16x16x32_bf16 v[132:135], a[56:59], a[128:131], v[132:135]\n"
"v_mfma_f32_16x16x32_bf16 v[136:139], a[56:59], a[136:139], v[136:139]\n"
"v_mfma_f32_16x16x32_bf16 v[136:139], a[60:63], a[140:143], v[136:139]\n"
"v_mfma_f32_16x16x32_bf16 v[108:111], a[52:55], a[140:143], v[108:111]\n"
"v_mfma_f32_16x16x32_bf16 v[108:111], a[48:51], a[136:139], v[108:111]\n"
"v_mfma_f32_16x16x32_bf16 v[112:115], a[48:51], a[144:147], v[112:115]\n"
"v_mfma_f32_16x16x32_bf16 v[112:115], a[52:55], a[148:151], v[112:115]\n"
"global_load_dwordx4 a[44:47], v0, s[34:35] offset:-1024 sc1\n"
"v_mfma_f32_16x16x32_bf16 v[140:143], a[60:63], a[148:151], v[140:143]\n"
"v_mfma_f32_16x16x32_bf16 v[140:143], a[56:59], a[144:147], v[140:143]\n"
"v_mfma_f32_16x16x32_bf16 a[12:15], a[56:59], a[152:155], a[12:15]\n"
"s_waitcnt lgkmcnt(0)\n"
"global_store_dwordx4 v0, v[24:27], s[54:55] offset:-4096 sc1\n"
"v_mfma_f32_16x16x32_bf16 a[12:15], a[60:63], a[156:159], a[12:15]\n"
"v_mfma_f32_16x16x32_bf16 a[8:11], a[52:55], a[156:159], a[8:11]\n"
"v_mfma_f32_16x16x32_bf16 a[8:11], a[48:51], a[152:155], a[8:11]\n"
"s_waitcnt vmcnt(13)\n"
"v_mfma_f32_16x16x32_bf16 v[144:147], a[64:67], a[96:99], v[144:147]\n"
"v_mfma_f32_16x16x32_bf16 v[144:147], a[68:71], a[100:103], v[144:147]\n"
"global_load_dwordx4 a[48:51], v0, s[34:35] offset:0 sc1\n"
"v_mfma_f32_16x16x32_bf16 v[172:175], a[76:79], a[100:103], v[172:175]\n"
"v_mfma_f32_16x16x32_bf16 v[172:175], a[72:75], a[96:99], v[172:175]\n"
"v_mfma_f32_16x16x32_bf16 v[176:179], a[72:75], a[104:107], v[176:179]\n"
"v_mfma_f32_16x16x32_bf16 v[176:179], a[76:79], a[108:111], v[176:179]\n"
"v_mfma_f32_16x16x32_bf16 v[148:151], a[68:71], a[108:111], v[148:151]\n"
"v_mfma_f32_16x16x32_bf16 v[148:151], a[64:67], a[104:107], v[148:151]\n"
"v_mfma_f32_16x16x32_bf16 v[152:155], a[64:67], a[112:115], v[152:155]\n"
"v_mfma_f32_16x16x32_bf16 v[152:155], a[68:71], a[116:119], v[152:155]\n"
"global_load_dwordx4 a[52:55], v0, s[34:35] offset:1024 sc1\n"
"v_mfma_f32_16x16x32_bf16 v[180:183], a[76:79], a[116:119], v[180:183]\n"
"v_mfma_f32_16x16x32_bf16 v[180:183], a[72:75], a[112:115], v[180:183]\n"
"v_mfma_f32_16x16x32_bf16 v[184:187], a[72:75], a[120:123], v[184:187]\n"
"v_mfma_f32_16x16x32_bf16 v[184:187], a[76:79], a[124:127], v[184:187]\n"
"v_mfma_f32_16x16x32_bf16 v[156:159], a[68:71], a[124:127], v[156:159]\n"
"v_mfma_f32_16x16x32_bf16 v[156:159], a[64:67], a[120:123], v[156:159]\n"
"ds_read_b128 v[28:31], v2 offset:26624\n"
"v_mfma_f32_16x16x32_bf16 v[160:163], a[64:67], a[128:131], v[160:163]\n"
"v_mfma_f32_16x16x32_bf16 v[160:163], a[68:71], a[132:135], v[160:163]\n"
"global_load_dwordx4 a[56:59], v0, s[34:35] offset:2048 sc1\n"
"v_mfma_f32_16x16x32_bf16 v[188:191], a[76:79], a[132:135], v[188:191]\n"
"v_mfma_f32_16x16x32_bf16 v[188:191], a[72:75], a[128:131], v[188:191]\n"
"v_mfma_f32_16x16x32_bf16 v[192:195], a[72:75], a[136:139], v[192:195]\n"
"v_mfma_f32_16x16x32_bf16 v[192:195], a[76:79], a[140:143], v[192:195]\n"
"v_mfma_f32_16x16x32_bf16 v[164:167], a[68:71], a[140:143], v[164:167]\n"
"v_mfma_f32_16x16x32_bf16 v[164:167], a[64:67], a[136:139], v[164:167]\n"
"v_mfma_f32_16x16x32_bf16 v[168:171], a[64:67], a[144:147], v[168:171]\n"
"v_mfma_f32_16x16x32_bf16 v[168:171], a[68:71], a[148:151], v[168:171]\n"
"global_load_dwordx4 a[60:63], v0, s[34:35] offset:3072 sc1\n"
"v_mfma_f32_16x16x32_bf16 v[196:199], a[76:79], a[148:151], v[196:199]\n"
"s_waitcnt lgkmcnt(0)\n"
"global_store_dwordx4 v0, v[28:31], s[54:55] offset:-2048 sc1\n"
"v_mfma_f32_16x16x32_bf16 v[196:199], a[72:75], a[144:147], v[196:199]\n"
"v_mfma_f32_16x16x32_bf16 a[20:23], a[72:75], a[152:155], a[20:23]\n"
"s_add_u32 s34, s34, 8192\n"
"v_mfma_f32_16x16x32_bf16 a[20:23], a[76:79], a[156:159], a[20:23]\n"
"v_mfma_f32_16x16x32_bf16 a[16:19], a[68:71], a[156:159], a[16:19]\n"
"v_mfma_f32_16x16x32_bf16 a[16:19], a[64:67], a[152:155], a[16:19]\n"
"s_waitcnt vmcnt(12)\n"
"v_mfma_f32_16x16x32_bf16 v[200:203], a[80:83], a[96:99], v[200:203]\n"
"v_mfma_f32_16x16x32_bf16 v[200:203], a[84:87], a[100:103], v[200:203]\n"
"global_load_dwordx4 a[64:67], v0, s[34:35] offset:-4096 sc1\n"
"v_mfma_f32_16x16x32_bf16 v[228:231], a[92:95], a[100:103], v[228:231]\n"
"ds_read_b128 a[100:103], v3 offset:17408\n"
"v_mfma_f32_16x16x32_bf16 v[228:231], a[88:91], a[96:99], v[228:231]\n"
"ds_read_b128 a[96:99], v3 offset:16384\n"
"v_mfma_f32_16x16x32_bf16 v[232:235], a[88:91], a[104:107], v[232:235]\n"
"v_mfma_f32_16x16x32_bf16 v[232:235], a[92:95], a[108:111], v[232:235]\n"
"v_mfma_f32_16x16x32_bf16 v[204:207], a[84:87], a[108:111], v[204:207]\n"
"ds_read_b128 a[108:111], v3 offset:19456\n"
"v_mfma_f32_16x16x32_bf16 v[204:207], a[80:83], a[104:107], v[204:207]\n"
"ds_read_b128 a[104:107], v3 offset:18432\n"
"v_mfma_f32_16x16x32_bf16 v[208:211], a[80:83], a[112:115], v[208:211]\n"
"v_mfma_f32_16x16x32_bf16 v[208:211], a[84:87], a[116:119], v[208:211]\n"
"global_load_dwordx4 a[68:71], v0, s[34:35] offset:-3072 sc1\n"
"v_mfma_f32_16x16x32_bf16 v[236:239], a[92:95], a[116:119], v[236:239]\n"
"ds_read_b128 a[116:119], v3 offset:21504\n"
"v_mfma_f32_16x16x32_bf16 v[236:239], a[88:91], a[112:115], v[236:239]\n"
"ds_read_b128 a[112:115], v3 offset:20480\n"
"v_mfma_f32_16x16x32_bf16 v[240:243], a[88:91], a[120:123], v[240:243]\n"
"v_mfma_f32_16x16x32_bf16 v[240:243], a[92:95], a[124:127], v[240:243]\n"
"ds_read_b128 v[24:27], v2 offset:29696\n"
"v_mfma_f32_16x16x32_bf16 v[212:215], a[84:87], a[124:127], v[212:215]\n"
"ds_read_b128 a[124:127], v3 offset:23552\n"
"v_mfma_f32_16x16x32_bf16 v[212:215], a[80:83], a[120:123], v[212:215]\n"
"ds_read_b128 a[120:123], v3 offset:22528\n"
"v_mfma_f32_16x16x32_bf16 v[216:219], a[80:83], a[128:131], v[216:219]\n"
"v_mfma_f32_16x16x32_bf16 v[216:219], a[84:87], a[132:135], v[216:219]\n"
"global_load_dwordx4 a[72:75], v0, s[34:35] offset:-2048 sc1\n"
"v_mfma_f32_16x16x32_bf16 v[244:247], a[92:95], a[132:135], v[244:247]\n"
"ds_read_b128 a[132:135], v3 offset:25600\n"
"v_mfma_f32_16x16x32_bf16 v[244:247], a[88:91], a[128:131], v[244:247]\n"
"ds_read_b128 a[128:131], v3 offset:24576\n"
"v_mfma_f32_16x16x32_bf16 v[248:251], a[88:91], a[136:139], v[248:251]\n"
"v_subrev_u32 v8, s65, v8\n"
"v_mfma_f32_16x16x32_bf16 v[248:251], a[92:95], a[140:143], v[248:251]\n"
"v_xor_b32 v9, s79, v9\n"
"v_mfma_f32_16x16x32_bf16 v[220:223], a[84:87], a[140:143], v[220:223]\n"
"ds_read_b128 a[140:143], v3 offset:27648\n"
"v_or_b32 v8, v8, v9\n"
"v_mfma_f32_16x16x32_bf16 v[220:223], a[80:83], a[136:139], v[220:223]\n"
"ds_read_b128 a[136:139], v3 offset:26624\n"
"v_cmp_lt_u32 vcc, 15, v8\n"
"v_mfma_f32_16x16x32_bf16 v[224:227], a[80:83], a[144:147], v[224:227]\n"
"s_waitcnt lgkmcnt(6)\n"
"global_store_dwordx4 v0, v[24:27], s[54:55] offset:1024 sc1\n"
"s_cbranch_vccnz L_slow0\n"
"L_back0:\n"
"v_mfma_f32_16x16x32_bf16 v[224:227], a[84:87], a[148:151], v[224:227]\n"
"global_load_dwordx4 a[76:79], v0, s[34:35] offset:-1024 sc1\n"
"v_readfirstlane_b32 s66, v9\n"
"v_mfma_f32_16x16x32_bf16 v[252:255], a[92:95], a[148:151], v[252:255]\n"
"ds_read_b128 a[148:151], v3 offset:29696\n"
"v_mfma_f32_16x16x32_bf16 v[252:255], a[88:91], a[144:147], v[252:255]\n"
"s_and_b32 s66, s66, 3\n"
"ds_read_b128 a[144:147], v3 offset:28672\n"
"v_mfma_f32_16x16x32_bf16 a[28:31], a[88:91], a[152:155], a[28:31]\n"
"s_lshl_b32 s69, s66, 13\n"
"v_mfma_f32_16x16x32_bf16 a[28:31], a[92:95], a[156:159], a[28:31]\n"
"s_cmp_eq_u32 s66, 3\n"
"s_cselect_b32 s75, 8192, 0\n"
"v_mfma_f32_16x16x32_bf16 a[24:27], a[84:87], a[156:159], a[24:27]\n"
"s_and_b32 s29, s66, s30\n"
"ds_read_b128 a[156:159], v3 offset:31744\n"
"v_mfma_f32_16x16x32_bf16 a[24:27], a[80:83], a[152:155], a[24:27]\n"
"s_waitcnt vmcnt(11)\n"
"ds_read_b128 a[152:155], v3 offset:30720\n"
"v_mfma_f32_16x16x32_bf16 v[32:35], a[32:35], a[96:99], v[32:35]\n"
"s_xor_b32 s31, s79, s66\n"
"v_mfma_f32_16x16x32_bf16 v[32:35], a[36:39], a[100:103], v[32:35]\n"
"global_load_dwordx4 a[80:83], v0, s[34:35] offset:0 sc1\n"
"v_mfma_f32_16x16x32_bf16 v[60:63], a[44:47], a[100:103], v[60:63]\n"
"v_mfma_f32_16x16x32_bf16 v[60:63], a[40:43], a[96:99], v[60:63]\n"
"v_mfma_f32_16x16x32_bf16 v[64:67], a[40:43], a[104:107], v[64:67]\n"
"v_mfma_f32_16x16x32_bf16 v[64:67], a[44:47], a[108:111], v[64:67]\n"
"v_mfma_f32_16x16x32_bf16 v[36:39], a[36:39], a[108:111], v[36:39]\n"
"s_add_u32 m0, s64, 0\n"
"v_mfma_f32_16x16x32_bf16 v[36:39], a[32:35], a[104:107], v[36:39]\n"
"s_waitcnt lgkmcnt(8)\n"
"v_mfma_f32_16x16x32_bf16 v[40:43], a[32:35], a[112:115], v[40:43]\n"
"v_mfma_f32_16x16x32_bf16 v[40:43], a[36:39], a[116:119], v[40:43]\n"
"global_load_dwordx4 a[84:87], v0, s[34:35] offset:1024 sc1\n"
"v_mfma_f32_16x16x32_bf16 v[68:71], a[44:47], a[116:119], v[68:71]\n"
"v_mfma_f32_16x16x32_bf16 v[68:71], a[40:43], a[112:115], v[68:71]\n"
"v_mfma_f32_16x16x32_bf16 v[72:75], a[40:43], a[120:123], v[72:75]\n"
"v_mfma_f32_16x16x32_bf16 v[72:75], a[44:47], a[124:127], v[72:75]\n"
"v_mfma_f32_16x16x32_bf16 v[44:47], a[36:39], a[124:127], v[44:47]\n"
"v_mfma_f32_16x16x32_bf16 v[44:47], a[32:35], a[120:123], v[44:47]\n"
"s_waitcnt lgkmcnt(4)\n"
"v_mfma_f32_16x16x32_bf16 v[48:51], a[32:35], a[128:131], v[48:51]\n"
"v_mfma_f32_16x16x32_bf16 v[48:51], a[36:39], a[132:135], v[48:51]\n"
"global_load_dwordx4 a[88:91], v0, s[34:35] offset:2048 sc1\n"
"v_mfma_f32_16x16x32_bf16 v[76:79], a[44:47], a[132:135], v[76:79]\n"
"v_mfma_f32_16x16x32_bf16 v[76:79], a[40:43], a[128:131], v[76:79]\n"
"v_mfma_f32_16x16x32_bf16 v[80:83], a[40:43], a[136:139], v[80:83]\n"
"v_mfma_f32_16x16x32_bf16 v[80:83], a[44:47], a[140:143], v[80:83]\n"
"buffer_load_dwordx4 v0, s[48:51], s75 offen offset:0 sc1 lds\n"
"v_mfma_f32_16x16x32_bf16 v[52:55], a[36:39], a[140:143], v[52:55]\n"
"v_mfma_f32_16x16x32_bf16 v[52:55], a[32:35], a[136:139], v[52:55]\n"
"s_waitcnt lgkmcnt(0)\n"
"v_mfma_f32_16x16x32_bf16 v[56:59], a[32:35], a[144:147], v[56:59]\n"
"v_mfma_f32_16x16x32_bf16 v[56:59], a[36:39], a[148:151], v[56:59]\n"
"global_load_dwordx4 a[92:95], v0, s[34:35] offset:3072 sc1\n"
"v_mfma_f32_16x16x32_bf16 v[84:87], a[44:47], a[148:151], v[84:87]\n"
"v_mfma_f32_16x16x32_bf16 v[84:87], a[40:43], a[144:147], v[84:87]\n"
"v_mfma_f32_16x16x32_bf16 a[4:7], a[40:43], a[152:155], a[4:7]\n"
"s_add_u32 s34, s34, 8192\n"
"v_mfma_f32_16x16x32_bf16 a[4:7], a[44:47], a[156:159], a[4:7]\n"
"v_mfma_f32_16x16x32_bf16 a[0:3], a[36:39], a[156:159], a[0:3]\n"
"v_mfma_f32_16x16x32_bf16 a[0:3], a[32:35], a[152:155], a[0:3]\n"
"s_waitcnt vmcnt(11)\n"
"v_mfma_f32_16x16x32_bf16 v[88:91], a[48:51], a[96:99], v[88:91]\n"
"v_mfma_f32_16x16x32_bf16 v[88:91], a[52:55], a[100:103], v[88:91]\n"
"global_load_dwordx4 a[32:35], v0, s[34:35] offset:-4096 sc1\n"
"v_mfma_f32_16x16x32_bf16 v[116:119], a[60:63], a[100:103], v[116:119]\n"
"v_mfma_f32_16x16x32_bf16 v[116:119], a[56:59], a[96:99], v[116:119]\n"
"buffer_load_dwordx4 v0, s[48:51], s75 offen offset:1024 sc1 lds\n"
"v_mfma_f32_16x16x32_bf16 v[120:123], a[56:59], a[104:107], v[120:123]\n"
"v_mfma_f32_16x16x32_bf16 v[120:123], a[60:63], a[108:111], v[120:123]\n"
"v_mfma_f32_16x16x32_bf16 v[92:95], a[52:55], a[108:111], v[92:95]\n"
"v_mfma_f32_16x16x32_bf16 v[92:95], a[48:51], a[104:107], v[92:95]\n"
"v_mfma_f32_16x16x32_bf16 v[96:99], a[48:51], a[112:115], v[96:99]\n"
"v_mfma_f32_16x16x32_bf16 v[96:99], a[52:55], a[116:119], v[96:99]\n"
"global_load_dwordx4 a[36:39], v0, s[34:35] offset:-3072 sc1\n"
"v_mfma_f32_16x16x32_bf16 v[124:127], a[60:63], a[116:119], v[124:127]\n"
"v_mfma_f32_16x16x32_bf16 v[124:127], a[56:59], a[112:115], v[124:127]\n"
"v_mfma_f32_16x16x32_bf16 v[128:131], a[56:59], a[120:123], v[128:131]\n"
"v_mfma_f32_16x16x32_bf16 v[128:131], a[60:63], a[124:127], v[128:131]\n"
"v_mfma_f32_16x16x32_bf16 v[100:103], a[52:55], a[124:127], v[100:103]\n"
"v_mfma_f32_16x16x32_bf16 v[100:103], a[48:51], a[120:123], v[100:103]\n"
"v_mfma_f32_16x16x32_bf16 v[104:107], a[48:51], a[128:131], v[104:107]\n"
"v_mfma_f32_16x16x32_bf16 v[104:107], a[52:55], a[132:135], v[104:107]\n"
"global_load_dwordx4 a[40:43], v0, s[34:35] offset:-2048 sc1\n"
"v_mfma_f32_16x16x32_bf16 v[132:135], a[60:63], a[132:135], v[132:135]\n"
"buffer_load_dwordx4 v0, s[48:51], s75 offen offset:2048 sc1 lds\n"
"v_mfma_f32_16x16x32_bf16 v[132:135], a[56:59], a[128:131], v[132:135]\n"
"v_mfma_f32_16x16x32_bf16 v[136:139], a[56:59], a[136:139], v[136:139]\n"
"v_mfma_f32_16x16x32_bf16 v[136:139], a[60:63], a[140:143], v[136:139]\n"
"v_mfma_f32_16x16x32_bf16 v[108:111], a[52:55], a[140:143], v[108:111]\n"
"v_mfma_f32_16x16x32_bf16 v[108:111], a[48:51], a[136:139], v[108:111]\n"
"v_mfma_f32_16x16x32_bf16 v[112:115], a[48:51], a[144:147], v[112:115]\n"
"v_mfma_f32_16x16x32_bf16 v[112:115], a[52:55], a[148:151], v[112:115]\n"
"global_load_dwordx4 a[44:47], v0, s[34:35] offset:-1024 sc1\n"
"v_mfma_f32_16x16x32_bf16 v[140:143], a[60:63], a[148:151], v[140:143]\n"
"v_mfma_f32_16x16x32_bf16 v[140:143], a[56:59], a[144:147], v[140:143]\n"
"v_mfma_f32_16x16x32_bf16 a[12:15], a[56:59], a[152:155], a[12:15]\n"
"v_mfma_f32_16x16x32_bf16 a[12:15], a[60:63], a[156:159], a[12:15]\n"
"v_mfma_f32_16x16x32_bf16 a[8:11], a[52:55], a[156:159], a[8:11]\n"
"v_mfma_f32_16x16x32_bf16 a[8:11], a[48:51], a[152:155], a[8:11]\n"
"s_waitcnt vmcnt(11)\n"
"v_mfma_f32_16x16x32_bf16 v[144:147], a[64:67], a[96:99], v[144:147]\n"
"buffer_load_dwordx4 v0, s[48:51], s75 offen offset:3072 sc1 lds\n"
"v_mfma_f32_16x16x32_bf16 v[144:147], a[68:71], a[100:103], v[144:147]\n"
"global_load_dwordx4 a[48:51], v0, s[34:35] offset:0 sc1\n"
"v_mfma_f32_16x16x32_bf16 v[172:175], a[76:79], a[100:103], v[172:175]\n"
"v_mfma_f32_16x16x32_bf16 v[172:175], a[72:75], a[96:99], v[172:175]\n"
"v_mfma_f32_16x16x32_bf16 v[176:179], a[72:75], a[104:107], v[176:179]\n"
"v_mfma_f32_16x16x32_bf16 v[176:179], a[76:79], a[108:111], v[176:179]\n"
"v_mfma_f32_16x16x32_bf16 v[148:151], a[68:71], a[108:111], v[148:151]\n"
"v_mfma_f32_16x16x32_bf16 v[148:151], a[64:67], a[104:107], v[148:151]\n"
"v_mfma_f32_16x16x32_bf16 v[152:155], a[64:67], a[112:115], v[152:155]\n"
"v_mfma_f32_16x16x32_bf16 v[152:155], a[68:71], a[116:119], v[152:155]\n"
"global_load_dwordx4 a[52:55], v0, s[34:35] offset:1024 sc1\n"
"v_mfma_f32_16x16x32_bf16 v[180:183], a[76:79], a[116:119], v[180:183]\n"
"v_mfma_f32_16x16x32_bf16 v[180:183], a[72:75], a[112:115], v[180:183]\n"
"v_mfma_f32_16x16x32_bf16 v[184:187], a[72:75], a[120:123], v[184:187]\n"
"v_mfma_f32_16x16x32_bf16 v[184:187], a[76:79], a[124:127], v[184:187]\n"
"v_mfma_f32_16x16x32_bf16 v[156:159], a[68:71], a[124:127], v[156:159]\n"
"v_mfma_f32_16x16x32_bf16 v[156:159], a[64:67], a[120:123], v[156:159]\n"
"s_add_u32 m0, s64, 4096\n"
"v_mfma_f32_16x16x32_bf16 v[160:163], a[64:67], a[128:131], v[160:163]\n"
"v_mfma_f32_16x16x32_bf16 v[160:163], a[68:71], a[132:135], v[160:163]\n"
"global_load_dwordx4 a[56:59], v0, s[34:35] offset:2048 sc1\n"
"v_mfma_f32_16x16x32_bf16 v[188:191], a[76:79], a[132:135], v[188:191]\n"
"v_mfma_f32_16x16x32_bf16 v[188:191], a[72:75], a[128:131], v[188:191]\n"
"v_mfma_f32_16x16x32_bf16 v[192:195], a[72:75], a[136:139], v[192:195]\n"
"v_mfma_f32_16x16x32_bf16 v[192:195], a[76:79], a[140:143], v[192:195]\n"
"v_mfma_f32_16x16x32_bf16 v[164:167], a[68:71], a[140:143], v[164:167]\n"
"v_mfma_f32_16x16x32_bf16 v[164:167], a[64:67], a[136:139], v[164:167]\n"
"v_mfma_f32_16x16x32_bf16 v[168:171], a[64:67], a[144:147], v[168:171]\n"
"v_mfma_f32_16x16x32_bf16 v[168:171], a[68:71], a[148:151], v[168:171]\n"
"global_load_dwordx4 a[60:63], v0, s[34:35] offset:3072 sc1\n"
"v_mfma_f32_16x16x32_bf16 v[196:199], a[76:79], a[148:151], v[196:199]\n"
"v_mfma_f32_16x16x32_bf16 v[196:199], a[72:75], a[144:147], v[196:199]\n"
"v_mfma_f32_16x16x32_bf16 a[20:23], a[72:75], a[152:155], a[20:23]\n"
"s_add_u32 s34, s34, 8192\n"
"v_mfma_f32_16x16x32_bf16 a[20:23], a[76:79], a[156:159], a[20:23]\n"
"v_mfma_f32_16x16x32_bf16 a[16:19], a[68:71], a[156:159], a[16:19]\n"
"buffer_load_dwordx4 v1, s[48:51], s75 offen offset:0 sc1 lds\n"
"v_mfma_f32_16x16x32_bf16 a[16:19], a[64:67], a[152:155], a[16:19]\n"
"s_waitcnt vmcnt(12)\n"
"v_mfma_f32_16x16x32_bf16 v[200:203], a[80:83], a[96:99], v[200:203]\n"
"v_mfma_f32_16x16x32_bf16 v[200:203], a[84:87], a[100:103], v[200:203]\n"
"global_load_dwordx4 a[64:67], v0, s[34:35] offset:-4096 sc1\n"
"v_mfma_f32_16x16x32_bf16 v[228:231], a[92:95], a[100:103], v[228:231]\n"
"ds_read_b128 a[100:103], v4 offset:17408\n"
"v_mfma_f32_16x16x32_bf16 v[228:231], a[88:91], a[96:99], v[228:231]\n"
"ds_read_b128 a[96:99], v4 offset:16384\n"
"v_mfma_f32_16x16x32_bf16 v[232:235], a[88:91], a[104:107], v[232:235]\n"
"v_mfma_f32_16x16x32_bf16 v[232:235], a[92:95], a[108:111], v[232:235]\n"
"v_mfma_f32_16x16x32_bf16 v[204:207], a[84:87], a[108:111], v[204:207]\n"
"ds_read_b128 a[108:111], v4 offset:19456\n"
"v_mfma_f32_16x16x32_bf16 v[204:207], a[80:83], a[104:107], v[204:207]\n"
"ds_read_b128 a[104:107], v4 offset:18432\n"
"v_mfma_f32_16x16x32_bf16 v[208:211], a[80:83], a[112:115], v[208:211]\n"
"v_mfma_f32_16x16x32_bf16 v[208:211], a[84:87], a[116:119], v[208:211]\n"
"global_load_dwordx4 a[68:71], v0, s[34:35] offset:-3072 sc1\n"
"v_mfma_f32_16x16x32_bf16 v[236:239], a[92:95], a[116:119], v[236:239]\n"
"ds_read_b128 a[116:119], v4 offset:21504\n"
"v_mfma_f32_16x16x32_bf16 v[236:239], a[88:91], a[112:115], v[236:239]\n"
"ds_read_b128 a[112:115], v4 offset:20480\n"
"v_mfma_f32_16x16x32_bf16 v[240:243], a[88:91], a[120:123], v[240:243]\n"
"v_mfma_f32_16x16x32_bf16 v[240:243], a[92:95], a[124:127], v[240:243]\n"
"buffer_load_dwordx4 v1, s[48:51], s75 offen offset:1024 sc1 lds\n"
"v_mfma_f32_16x16x32_bf16 v[212:215], a[84:87], a[124:127], v[212:215]\n"
"ds_read_b128 a[124:127], v4 offset:23552\n"
"v_mfma_f32_16x16x32_bf16 v[212:215], a[80:83], a[120:123], v[212:215]\n"
"ds_read_b128 a[120:123], v4 offset:22528\n"
"v_mfma_f32_16x16x32_bf16 v[216:219], a[80:83], a[128:131], v[216:219]\n"
"v_mfma_f32_16x16x32_bf16 v[216:219], a[84:87], a[132:135], v[216:219]\n"
"global_load_dwordx4 a[72:75], v0, s[34:35] offset:-2048 sc1\n"
"v_mfma_f32_16x16x32_bf16 v[244:247], a[92:95], a[132:135], v[244:247]\n"
"ds_read_b128 a[132:135], v4 offset:25600\n"
"v_mfma_f32_16x16x32_bf16 v[244:247], a[88:91], a[128:131], v[244:247]\n"
"ds_read_b128 a[128:131], v4 offset:24576\n"
"v_mfma_f32_16x16x32_bf16 v[248:251], a[88:91], a[136:139], v[248:251]\n"
"v_mfma_f32_16x16x32_bf16 v[248:251], a[92:95], a[140:143], v[248:251]\n"
"v_mfma_f32_16x16x32_bf16 v[220:223], a[84:87], a[140:143], v[220:223]\n"
"ds_read_b128 a[140:143], v4 offset:27648\n"
"v_mfma_f32_16x16x32_bf16 v[220:223], a[80:83], a[136:139], v[220:223]\n"
"ds_read_b128 a[136:139], v4 offset:26624\n"
"v_mfma_f32_16x16x32_bf16 v[224:227], a[80:83], a[144:147], v[224:227]\n"
"v_mfma_f32_16x16x32_bf16 v[224:227], a[84:87], a[148:151], v[224:227]\n"
"global_load_dwordx4 a[76:79], v0, s[34:35] offset:-1024 sc1\n"
"v_mfma_f32_16x16x32_bf16 v[252:255], a[92:95], a[148:151], v[252:255]\n"
"ds_read_b128 a[148:151], v4 offset:29696\n"
"v_mfma_f32_16x16x32_bf16 v[252:255], a[88:91], a[144:147], v[252:255]\n"
"ds_read_b128 a[144:147], v4 offset:28672\n"
"v_mfma_f32_16x16x32_bf16 a[28:31], a[88:91], a[152:155], a[28:31]\n"
"buffer_load_dwordx4 v1, s[48:51], s75 offen offset:2048 sc1 lds\n"
"v_mfma_f32_16x16x32_bf16 a[28:31], a[92:95], a[156:159], a[28:31]\n"
"v_mfma_f32_16x16x32_bf16 a[24:27], a[84:87], a[156:159], a[24:27]\n"
"ds_read_b128 a[156:159], v4 offset:31744\n"
"v_mfma_f32_16x16x32_bf16 a[24:27], a[80:83], a[152:155], a[24:27]\n"
"s_waitcnt vmcnt(12) lgkmcnt(11)\n"
"ds_read_b128 a[152:155], v4 offset:30720\n"
"v_mfma_f32_16x16x32_bf16 v[32:35], a[32:35], a[96:99], v[32:35]\n"
"v_mfma_f32_16x16x32_bf16 v[32:35], a[36:39], a[100:103], v[32:35]\n"
"global_load_dwordx4 a[80:83], v0, s[34:35] offset:0 sc1\n"
"v_mfma_f32_16x16x32_bf16 v[60:63], a[44:47], a[100:103], v[60:63]\n"
"v_mfma_f32_16x16x32_bf16 v[60:63], a[40:43], a[96:99], v[60:63]\n"
"v_mfma_f32_16x16x32_bf16 v[64:67], a[40:43], a[104:107], v[64:67]\n"
"v_mfma_f32_16x16x32_bf16 v[64:67], a[44:47], a[108:111], v[64:67]\n"
"v_mfma_f32_16x16x32_bf16 v[36:39], a[36:39], a[108:111], v[36:39]\n"
"v_mfma_f32_16x16x32_bf16 v[36:39], a[32:35], a[104:107], v[36:39]\n"
"s_waitcnt lgkmcnt(8)\n"
"v_mfma_f32_16x16x32_bf16 v[40:43], a[32:35], a[112:115], v[40:43]\n"
"v_mfma_f32_16x16x32_bf16 v[40:43], a[36:39], a[116:119], v[40:43]\n"
"global_load_dwordx4 a[84:87], v0, s[34:35] offset:1024 sc1\n"
"s_add_u32 s65, s65, 1\n"
"v_mfma_f32_16x16x32_bf16 v[68:71], a[44:47], a[116:119], v[68:71]\n"
"buffer_load_dwordx4 v1, s[48:51], s75 offen offset:3072 sc1 lds\n"
"v_mfma_f32_16x16x32_bf16 v[68:71], a[40:43], a[112:115], v[68:71]\n"
"v_mov_b32 v8, s65\n"
"v_mfma_f32_16x16x32_bf16 v[72:75], a[40:43], a[120:123], v[72:75]\n"
"v_mov_b32 v9, s60\n"
"v_mfma_f32_16x16x32_bf16 v[72:75], a[44:47], a[124:127], v[72:75]\n"
"v_mfma_f32_16x16x32_bf16 v[44:47], a[36:39], a[124:127], v[44:47]\n"
"buffer_store_dwordx2 v[8:9], off, s[56:59], %13 sc1\n"
"v_mfma_f32_16x16x32_bf16 v[44:47], a[32:35], a[120:123], v[44:47]\n"
"s_waitcnt lgkmcnt(4)\n"
"v_mfma_f32_16x16x32_bf16 v[48:51], a[32:35], a[128:131], v[48:51]\n"
"v_mfma_f32_16x16x32_bf16 v[48:51], a[36:39], a[132:135], v[48:51]\n"
"global_load_dwordx4 a[88:91], v0, s[34:35] offset:2048 sc1\n"
"v_mfma_f32_16x16x32_bf16 v[76:79], a[44:47], a[132:135], v[76:79]\n"
"v_mfma_f32_16x16x32_bf16 v[76:79], a[40:43], a[128:131], v[76:79]\n"
"v_mfma_f32_16x16x32_bf16 v[80:83], a[40:43], a[136:139], v[80:83]\n"
"v_mfma_f32_16x16x32_bf16 v[80:83], a[44:47], a[140:143], v[80:83]\n"
"v_mfma_f32_16x16x32_bf16 v[52:55], a[36:39], a[140:143], v[52:55]\n"
"v_mfma_f32_16x16x32_bf16 v[52:55], a[32:35], a[136:139], v[52:55]\n"
"s_waitcnt lgkmcnt(0)\n"
"v_mfma_f32_16x16x32_bf16 v[56:59], a[32:35], a[144:147], v[56:59]\n"
"v_mfma_f32_16x16x32_bf16 v[56:59], a[36:39], a[148:151], v[56:59]\n"
"s_add_u32 m0, s64, 8192\n"
"global_load_dwordx4 a[92:95], v0, s[34:35] offset:3072 sc1\n"
"v_mfma_f32_16x16x32_bf16 v[84:87], a[44:47], a[148:151], v[84:87]\n"
"v_mfma_f32_16x16x32_bf16 v[84:87], a[40:43], a[144:147], v[84:87]\n"
"v_mfma_f32_16x16x32_bf16 a[4:7], a[40:43], a[152:155], a[4:7]\n"
"s_add_u32 s34, s34, 8192\n"
"v_mfma_f32_16x16x32_bf16 a[4:7], a[44:47], a[156:159], a[4:7]\n"
"v_mfma_f32_16x16x32_bf16 a[0:3], a[36:39], a[156:159], a[0:3]\n"
"v_mfma_f32_16x16x32_bf16 a[0:3], a[32:35], a[152:155], a[0:3]\n"
"s_waitcnt vmcnt(13)\n"
"v_mfma_f32_16x16x32_bf16 v[88:91], a[48:51], a[96:99], v[88:91]\n"
"v_mfma_f32_16x16x32_bf16 v[88:91], a[52:55], a[100:103], v[88:91]\n"
"global_load_dwordx4 a[32:35], v0, s[34:35] offset:-4096 sc1\n"
"v_mfma_f32_16x16x32_bf16 v[116:119], a[60:63], a[100:103], v[116:119]\n"
"v_mfma_f32_16x16x32_bf16 v[116:119], a[56:59], a[96:99], v[116:119]\n"
"v_mfma_f32_16x16x32_bf16 v[120:123], a[56:59], a[104:107], v[120:123]\n"
"v_mfma_f32_16x16x32_bf16 v[120:123], a[60:63], a[108:111], v[120:123]\n"
"v_mfma_f32_16x16x32_bf16 v[92:95], a[52:55], a[108:111], v[92:95]\n"
"v_mfma_f32_16x16x32_bf16 v[92:95], a[48:51], a[104:107], v[92:95]\n"
"v_mfma_f32_16x16x32_bf16 v[96:99], a[48:51], a[112:115], v[96:99]\n"
"buffer_load_dwordx4 v0, s[48:51], s69 offen offset:0 sc1 lds\n"
"v_mfma_f32_16x16x32_bf16 v[96:99], a[52:55], a[116:119], v[96:99]\n"
"global_load_dwordx4 a[36:39], v0, s[34:35] offset:-3072 sc1\n"
"v_mfma_f32_16x16x32_bf16 v[124:127], a[60:63], a[116:119], v[124:127]\n"
"v_mfma_f32_16x16x32_bf16 v[124:127], a[56:59], a[112:115], v[124:127]\n"
"v_mfma_f32_16x16x32_bf16 v[128:131], a[56:59], a[120:123], v[128:131]\n"
"v_mfma_f32_16x16x32_bf16 v[128:131], a[60:63], a[124:127], v[128:131]\n"
"v_mfma_f32_16x16x32_bf16 v[100:103], a[52:55], a[124:127], v[100:103]\n"
"v_mfma_f32_16x16x32_bf16 v[100:103], a[48:51], a[120:123], v[100:103]\n"
"v_mfma_f32_16x16x32_bf16 v[104:107], a[48:51], a[128:131], v[104:107]\n"
"v_mfma_f32_16x16x32_bf16 v[104:107], a[52:55], a[132:135], v[104:107]\n"
"global_load_dwordx4 a[40:43], v0, s[34:35] offset:-2048 sc1\n"
"v_mfma_f32_16x16x32_bf16 v[132:135], a[60:63], a[132:135], v[132:135]\n"
"v_mfma_f32_16x16x32_bf16 v[132:135], a[56:59], a[128:131], v[132:135]\n"
"v_mfma_f32_16x16x32_bf16 v[136:139], a[56:59], a[136:139], v[136:139]\n"
"v_mfma_f32_16x16x32_bf16 v[136:139], a[60:63], a[140:143], v[136:139]\n"
"v_mfma_f32_16x16x32_bf16 v[108:111], a[52:55], a[140:143], v[108:111]\n"
"buffer_load_dwordx4 v0, s[48:51], s69 offen offset:1024 sc1 lds\n"
"v_mfma_f32_16x16x32_bf16 v[108:111], a[48:51], a[136:139], v[108:111]\n"
"v_mfma_f32_16x16x32_bf16 v[112:115], a[48:51], a[144:147], v[112:115]\n"
"v_mfma_f32_16x16x32_bf16 v[112:115], a[52:55], a[148:151], v[112:115]\n"
"global_load_dwordx4 a[44:47], v0, s[34:35] offset:-1024 sc1\n"
"v_mfma_f32_16x16x32_bf16 v[140:143], a[60:63], a[148:151], v[140:143]\n"
"v_mfma_f32_16x16x32_bf16 v[140:143], a[56:59], a[144:147], v[140:143]\n"
"v_mfma_f32_16x16x32_bf16 a[12:15], a[56:59], a[152:155], a[12:15]\n"
"v_mfma_f32_16x16x32_bf16 a[12:15], a[60:63], a[156:159], a[12:15]\n"
"v_mfma_f32_16x16x32_bf16 a[8:11], a[52:55], a[156:159], a[8:11]\n"
"v_mfma_f32_16x16x32_bf16 a[8:11], a[48:51], a[152:155], a[8:11]\n"
"s_waitcnt vmcnt(13)\n"
"v_mfma_f32_16x16x32_bf16 v[144:147], a[64:67], a[96:99], v[144:147]\n"
"v_mfma_f32_16x16x32_bf16 v[144:147], a[68:71], a[100:103], v[144:147]\n"
"global_load_dwordx4 a[48:51], v0, s[34:35] offset:0 sc1\n"
"v_mfma_f32_16x16x32_bf16 v[172:175], a[76:79], a[100:103], v[172:175]\n"
"v_mfma_f32_16x16x32_bf16 v[172:175], a[72:75], a[96:99], v[172:175]\n"
"v_mfma_f32_16x16x32_bf16 v[176:179], a[72:75], a[104:107], v[176:179]\n"
"v_mfma_f32_16x16x32_bf16 v[176:179], a[76:79], a[108:111], v[176:179]\n"
"buffer_load_dwordx4 v0, s[48:51], s69 offen offset:2048 sc1 lds\n"
"v_mfma_f32_16x16x32_bf16 v[148:151], a[68:71], a[108:111], v[148:151]\n"
"v_mfma_f32_16x16x32_bf16 v[148:151], a[64:67], a[104:107], v[148:151]\n"
"v_mfma_f32_16x16x32_bf16 v[152:155], a[64:67], a[112:115], v[152:155]\n"
"v_lshlrev_b32 v16, 16, v18\n"
"v_mfma_f32_16x16x32_bf16 v[152:155], a[68:71], a[116:119], v[152:155]\n"
"global_load_dwordx4 a[52:55], v0, s[34:35] offset:1024 sc1\n"
"v_and_b32 v17, s63, v18\n"
"v_mfma_f32_16x16x32_bf16 v[180:183], a[76:79], a[116:119], v[180:183]\n"
"v_lshlrev_b32 v18, 16, v19\n"
"v_mfma_f32_16x16x32_bf16 v[180:183], a[72:75], a[112:115], v[180:183]\n"
"v_and_b32 v19, s63, v19\n"
"v_mfma_f32_16x16x32_bf16 v[184:187], a[72:75], a[120:123], v[184:187]\n"
"v_add_f32 v32, v32, v16\n"
"v_mfma_f32_16x16x32_bf16 v[184:187], a[76:79], a[124:127], v[184:187]\n"
"v_add_f32 v33, v33, v17\n"
"v_add_f32 v34, v34, v18\n"
"v_mfma_f32_16x16x32_bf16 v[156:159], a[68:71], a[124:127], v[156:159]\n"
"v_add_f32 v35, v35, v19\n"
"v_mfma_f32_16x16x32_bf16 v[156:159], a[64:67], a[120:123], v[156:159]\n"
"v_add_f32 v36, v36, v16\n"
"v_mfma_f32_16x16x32_bf16 v[160:163], a[64:67], a[128:131], v[160:163]\n"
"v_add_f32 v37, v37, v17\n"
"v_mfma_f32_16x16x32_bf16 v[160:163], a[68:71], a[132:135], v[160:163]\n"
"global_load_dwordx4 a[56:59], v0, s[34:35] offset:2048 sc1\n"
"v_add_f32 v38, v38, v18\n"
"v_mfma_f32_16x16x32_bf16 v[188:191], a[76:79], a[132:135], v[188:191]\n"
"v_add_f32 v39, v39, v19\n"
"v_mfma_f32_16x16x32_bf16 v[188:191], a[72:75], a[128:131], v[188:191]\n"
"v_add_f32 v40, v40, v16\n"
"v_add_f32 v41, v41, v17\n"
"v_mfma_f32_16x16x32_bf16 v[192:195], a[72:75], a[136:139], v[192:195]\n"
"buffer_load_dwordx4 v0, s[48:51], s69 offen offset:3072 sc1 lds\n"
"v_mfma_f32_16x16x32_bf16 v[192:195], a[76:79], a[140:143], v[192:195]\n"
"v_add_f32 v42, v42, v18\n"
"v_mfma_f32_16x16x32_bf16 v[164:167], a[68:71], a[140:143], v[164:167]\n"
"v_add_f32 v43, v43, v19\n"
"v_mfma_f32_16x16x32_bf16 v[164:167], a[64:67], a[136:139], v[164:167]\n"
"v_add_f32 v44, v44, v16\n"
"v_add_f32 v45, v45, v17\n"
"v_mfma_f32_16x16x32_bf16 v[168:171], a[64:67], a[144:147], v[168:171]\n"
"v_add_f32 v46, v46, v18\n"
"v_mfma_f32_16x16x32_bf16 v[168:171], a[68:71], a[148:151], v[168:171]\n"
"global_load_dwordx4 a[60:63], v0, s[34:35] offset:3072 sc1\n"
"v_add_f32 v47, v47, v19\n"
"v_mfma_f32_16x16x32_bf16 v[196:199], a[76:79], a[148:151], v[196:199]\n"
"v_add_f32 v48, v48, v16\n"
"v_mfma_f32_16x16x32_bf16 v[196:199], a[72:75], a[144:147], v[196:199]\n"
"v_add_f32 v49, v49, v17\n"
"v_mfma_f32_16x16x32_bf16 a[20:23], a[72:75], a[152:155], a[20:23]\n"
"s_add_u32 s34, s34, 8192\n"
"v_add_f32 v50, v50, v18\n"
"v_mfma_f32_16x16x32_bf16 a[20:23], a[76:79], a[156:159], a[20:23]\n"
"v_add_f32 v51, v51, v19\n"
"v_mfma_f32_16x16x32_bf16 a[16:19], a[68:71], a[156:159], a[16:19]\n"
"v_add_f32 v52, v52, v16\n"
"v_mfma_f32_16x16x32_bf16 a[16:19], a[64:67], a[152:155], a[16:19]\n"
"s_waitcnt vmcnt(12)\n"
"v_add_f32 v53, v53, v17\n"
"v_mfma_f32_16x16x32_bf16 v[200:203], a[80:83], a[96:99], v[200:203]\n"
"v_add_f32 v54, v54, v18\n"
"v_add_f32 v55, v55, v19\n"
"v_mfma_f32_16x16x32_bf16 v[200:203], a[84:87], a[100:103], v[200:203]\n"
"global_load_dwordx4 a[64:67], v0, s[34:35] offset:-4096 sc1\n"
"v_add_f32 v56, v56, v16\n"
"v_mfma_f32_16x16x32_bf16 v[228:231], a[92:95], a[100:103], v[228:231]\n"
"s_add_u32 m0, s64, 12288\n"
"ds_read_b128 a[100:103], v5 offset:17408\n"
"v_add_f32 v57, v57, v17\n"
"v_mfma_f32_16x16x32_bf16 v[228:231], a[88:91], a[96:99], v[228:231]\n"
"ds_read_b128 a[96:99], v5 offset:16384\n"
"v_add_f32 v58, v58, v18\n"
"v_mfma_f32_16x16x32_bf16 v[232:235], a[88:91], a[104:107], v[232:235]\n"
"v_add_f32 v59, v59, v19\n"
"v_mfma_f32_16x16x32_bf16 v[232:235], a[92:95], a[108:111], v[232:235]\n"
"v_accvgpr_read_b32 v12, a0\n"
"v_mfma_f32_16x16x32_bf16 v[204:207], a[84:87], a[108:111], v[204:207]\n"
"ds_read_b128 a[108:111], v5 offset:19456\n"
"v_accvgpr_read_b32 v13, a1\n"
"v_mfma_f32_16x16x32_bf16 v[204:207], a[80:83], a[104:107], v[204:207]\n"
"ds_read_b128 a[104:107], v5 offset:18432\n"
"v_accvgpr_read_b32 v14, a2\n"
"v_mfma_f32_16x16x32_bf16 v[208:211], a[80:83], a[112:115], v[208:211]\n"
"v_accvgpr_read_b32 v15, a3\n"
"v_mfma_f32_16x16x32_bf16 v[208:211], a[84:87], a[116:119], v[208:211]\n"
"global_load_dwordx4 a[68:71], v0, s[34:35] offset:-3072 sc1\n"
"v_add_f32 v12, v12, v16\n"
"v_mfma_f32_16x16x32_bf16 v[236:239], a[92:95], a[116:119], v[236:239]\n"
"ds_read_b128 a[116:119], v5 offset:21504\n"
"v_add_f32 v13, v13, v17\n"
"v_mfma_f32_16x16x32_bf16 v[236:239], a[88:91], a[112:115], v[236:239]\n"
"ds_read_b128 a[112:115], v5 offset:20480\n"
"v_add_f32 v14, v14, v18\n"
"v_mfma_f32_16x16x32_bf16 v[240:243], a[88:91], a[120:123], v[240:243]\n"
"v_add_f32 v15, v15, v19\n"
"v_accvgpr_write_b32 a0, v12\n"
"v_mfma_f32_16x16x32_bf16 v[240:243], a[92:95], a[124:127], v[240:243]\n"
"v_accvgpr_write_b32 a1, v13\n"
"v_mfma_f32_16x16x32_bf16 v[212:215], a[84:87], a[124:127], v[212:215]\n"
"ds_read_b128 a[124:127], v5 offset:23552\n"
"v_accvgpr_write_b32 a2, v14\n"
"v_mfma_f32_16x16x32_bf16 v[212:215], a[80:83], a[120:123], v[212:215]\n"
"ds_read_b128 a[120:123], v5 offset:22528\n"
"v_accvgpr_write_b32 a3, v15\n"
"v_mfma_f32_16x16x32_bf16 v[216:219], a[80:83], a[128:131], v[216:219]\n"
"buffer_load_dwordx4 v1, s[48:51], s69 offen offset:0 sc1 lds\n"
"v_mfma_f32_16x16x32_bf16 v[216:219], a[84:87], a[132:135], v[216:219]\n"
"global_load_dwordx4 a[72:75], v0, s[34:35] offset:-2048 sc1\n"
"v_lshrrev_b32 v16, 4, v0\n"
"v_mfma_f32_16x16x32_bf16 v[244:247], a[92:95], a[132:135], v[244:247]\n"
"ds_read_b128 a[132:135], v5 offset:25600\n"
"v_and_b32 v16, 0x30, v16\n"
"v_mfma_f32_16x16x32_bf16 v[244:247], a[88:91], a[128:131], v[244:247]\n"
"ds_read_b128 a[128:131], v5 offset:24576\n"
"buffer_load_dwordx2 v[18:19], v16, s[44:47], s61 offen offset:64\n"
"v_mfma_f32_16x16x32_bf16 v[248:251], a[88:91], a[136:139], v[248:251]\n"
"s_getpc_b64 s[96:97]\n"
"L_c3_:\n"
"s_add_u32 s96, s96, L_last-L_c3_\n"
"s_addc_u32 s97, s97, 0\n"
"v_mfma_f32_16x16x32_bf16 v[248:251], a[92:95], a[140:143], v[248:251]\n"
"s_lshr_b32 s66, %26, 3\n"
"v_mfma_f32_16x16x32_bf16 v[220:223], a[84:87], a[140:143], v[220:223]\n"
"s_lshl2_add_u32 s66, s66, %16\n"
"ds_read_b128 a[140:143], v5 offset:27648\n"
"v_mfma_f32_16x16x32_bf16 v[220:223], a[80:83], a[136:139], v[220:223]\n"
"ds_read_b128 a[136:139], v5 offset:26624\n"
"v_mfma_f32_16x16x32_bf16 v[224:227], a[80:83], a[144:147], v[224:227]\n"
"s_mul_i32 s66, s66, 256\n"
"v_mfma_f32_16x16x32_bf16 v[224:227], a[84:87], a[148:151], v[224:227]\n"
"global_load_dwordx4 a[76:79], v0, s[34:35] offset:-1024 sc1\n"
"v_mfma_f32_16x16x32_bf16 v[252:255], a[92:95], a[148:151], v[252:255]\n"
"ds_read_b128 a[148:151], v5 offset:29696\n"
"v_mfma_f32_16x16x32_bf16 v[252:255], a[88:91], a[144:147], v[252:255]\n"
"s_min_u32 s66, s66, L_headF-L_last-256\n"
"ds_read_b128 a[144:147], v5 offset:28672\n"
"v_mfma_f32_16x16x32_bf16 a[28:31], a[88:91], a[152:155], a[28:31]\n"
"v_mfma_f32_16x16x32_bf16 a[28:31], a[92:95], a[156:159], a[28:31]\n"
"v_min_u32 v11, 16, v0\n"
"v_mfma_f32_16x16x32_bf16 a[24:27], a[84:87], a[156:159], a[24:27]\n"
"ds_read_b128 a[156:159], v5 offset:31744\n"
"buffer_load_dwordx2 v[8:9], off, s[56:59], %14 sc1\n"
"v_mfma_f32_16x16x32_bf16 a[24:27], a[80:83], a[152:155], a[24:27]\n"
"s_waitcnt vmcnt(13) lgkmcnt(11)\n"
"ds_read_b128 a[152:155], v5 offset:30720\n"
"v_mfma_f32_16x16x32_bf16 v[32:35], a[32:35], a[96:99], v[32:35]\n"
"buffer_load_dwordx4 v1, s[48:51], s69 offen offset:1024 sc1 lds\n"
"v_mfma_f32_16x16x32_bf16 v[32:35], a[36:39], a[100:103], v[32:35]\n"
"global_load_dwordx4 a[80:83], v0, s[34:35] offset:0 sc1\n"
"v_mfma_f32_16x16x32_bf16 v[60:63], a[44:47], a[100:103], v[60:63]\n"
"v_lshl_add_u32 v11, v11, 3, s66\n"
"v_mfma_f32_16x16x32_bf16 v[60:63], a[40:43], a[96:99], v[60:63]\n"
"global_load_dword v10, v11, s[96:97]\n"
"v_mfma_f32_16x16x32_bf16 v[64:67], a[40:43], a[104:107], v[64:67]\n"
"v_mfma_f32_16x16x32_bf16 v[64:67], a[44:47], a[108:111], v[64:67]\n"
"v_mfma_f32_16x16x32_bf16 v[36:39], a[36:39], a[108:111], v[36:39]\n"
"v_mfma_f32_16x16x32_bf16 v[36:39], a[32:35], a[104:107], v[36:39]\n"
"s_waitcnt lgkmcnt(8)\n"
"v_mfma_f32_16x16x32_bf16 v[40:43], a[32:35], a[112:115], v[40:43]\n"
"v_mfma_f32_16x16x32_bf16 v[40:43], a[36:39], a[116:119], v[40:43]\n"
"global_load_dwordx4 a[84:87], v0, s[34:35] offset:1024 sc1\n"
"v_mfma_f32_16x16x32_bf16 v[68:71], a[44:47], a[116:119], v[68:71]\n"
"v_mfma_f32_16x16x32_bf16 v[68:71], a[40:43], a[112:115], v[68:71]\n"
"v_mfma_f32_16x16x32_bf16 v[72:75], a[40:43], a[120:123], v[72:75]\n"
"v_mfma_f32_16x16x32_bf16 v[72:75], a[44:47], a[124:127], v[72:75]\n"
"v_mfma_f32_16x16x32_bf16 v[44:47], a[36:39], a[124:127], v[44:47]\n"
"buffer_load_dwordx4 v1, s[48:51], s69 offen offset:2048 sc1 lds\n"
"v_mfma_f32_16x16x32_bf16 v[44:47], a[32:35], a[120:123], v[44:47]\n"
"s_waitcnt lgkmcnt(4)\n"
"v_mfma_f32_16x16x32_bf16 v[48:51], a[32:35], a[128:131], v[48:51]\n"
"v_mfma_f32_16x16x32_bf16 v[48:51], a[36:39], a[132:135], v[48:51]\n"
"global_load_dwordx4 a[88:91], v0, s[34:35] offset:2048 sc1\n"
"v_mfma_f32_16x16x32_bf16 v[76:79], a[44:47], a[132:135], v[76:79]\n"
"v_mfma_f32_16x16x32_bf16 v[76:79], a[40:43], a[128:131], v[76:79]\n"
"v_mfma_f32_16x16x32_bf16 v[80:83], a[40:43], a[136:139], v[80:83]\n"
"v_mfma_f32_16x16x32_bf16 v[80:83], a[44:47], a[140:143], v[80:83]\n"
"v_mfma_f32_16x16x32_bf16 v[52:55], a[36:39], a[140:143], v[52:55]\n"
"v_mfma_f32_16x16x32_bf16 v[52:55], a[32:35], a[136:139], v[52:55]\n"
"s_waitcnt lgkmcnt(0)\n"
"v_mfma_f32_16x16x32_bf16 v[56:59], a[32:35], a[144:147], v[56:59]\n"
"v_mfma_f32_16x16x32_bf16 v[56:59], a[36:39], a[148:151], v[56:59]\n"
"global_load_dwordx4 a[92:95], v0, s[34:35] offset:3072 sc1\n"
"v_mfma_f32_16x16x32_bf16 v[84:87], a[44:47], a[148:151], v[84:87]\n"
"v_mfma_f32_16x16x32_bf16 v[84:87], a[40:43], a[144:147], v[84:87]\n"
"v_mfma_f32_16x16x32_bf16 a[4:7], a[40:43], a[152:155], a[4:7]\n"
"s_add_u32 s34, s34, 8192\n"
"v_mfma_f32_16x16x32_bf16 a[4:7], a[44:47], a[156:159], a[4:7]\n"
"buffer_load_dwordx4 v1, s[48:51], s69 offen offset:3072 sc1 lds\n"
"v_mfma_f32_16x16x32_bf16 a[0:3], a[36:39], a[156:159], a[0:3]\n"
"v_mfma_f32_16x16x32_bf16 a[0:3], a[32:35], a[152:155], a[0:3]\n"
"s_waitcnt vmcnt(15)\n"
"v_mfma_f32_16x16x32_bf16 v[88:91], a[48:51], a[96:99], v[88:91]\n"
"v_mfma_f32_16x16x32_bf16 v[88:91], a[52:55], a[100:103], v[88:91]\n"
"global_load_dwordx4 a[32:35], v0, s[34:35] offset:-4096 sc1\n"
"v_mfma_f32_16x16x32_bf16 v[116:119], a[60:63], a[100:103], v[116:119]\n"
"v_mfma_f32_16x16x32_bf16 v[116:119], a[56:59], a[96:99], v[116:119]\n"
"v_mfma_f32_16x16x32_bf16 v[120:123], a[56:59], a[104:107], v[120:123]\n"
"v_mfma_f32_16x16x32_bf16 v[120:123], a[60:63], a[108:111], v[120:123]\n"
"v_mfma_f32_16x16x32_bf16 v[92:95], a[52:55], a[108:111], v[92:95]\n"
"v_mfma_f32_16x16x32_bf16 v[92:95], a[48:51], a[104:107], v[92:95]\n"
"v_mfma_f32_16x16x32_bf16 v[96:99], a[48:51], a[112:115], v[96:99]\n"
"v_mfma_f32_16x16x32_bf16 v[96:99], a[52:55], a[116:119], v[96:99]\n"
"global_load_dwordx4 a[36:39], v0, s[34:35] offset:-3072 sc1\n"
"v_mfma_f32_16x16x32_bf16 v[124:127], a[60:63], a[116:119], v[124:127]\n"
"v_mfma_f32_16x16x32_bf16 v[124:127], a[56:59], a[112:115], v[124:127]\n"
"v_mfma_f32_16x16x32_bf16 v[128:131], a[56:59], a[120:123], v[128:131]\n"
"v_mfma_f32_16x16x32_bf16 v[128:131], a[60:63], a[124:127], v[128:131]\n"
"v_mfma_f32_16x16x32_bf16 v[100:103], a[52:55], a[124:127], v[100:103]\n"
"v_mfma_f32_16x16x32_bf16 v[100:103], a[48:51], a[120:123], v[100:103]\n"
"v_mfma_f32_16x16x32_bf16 v[104:107], a[48:51], a[128:131], v[104:107]\n"
"v_mfma_f32_16x16x32_bf16 v[104:107], a[52:55], a[132:135], v[104:107]\n"
"global_load_dwordx4 a[40:43], v0, s[34:35] offset:-2048 sc1\n"
"v_mfma_f32_16x16x32_bf16 v[132:135], a[60:63], a[132:135], v[132:135]\n"
"v_mfma_f32_16x16x32_bf16 v[132:135], a[56:59], a[128:131], v[132:135]\n"
"v_mfma_f32_16x16x32_bf16 v[136:139], a[56:59], a[136:139], v[136:139]\n"
"v_mfma_f32_16x16x32_bf16 v[136:139], a[60:63], a[140:143], v[136:139]\n"
"v_mfma_f32_16x16x32_bf16 v[108:111], a[52:55], a[140:143], v[108:111]\n"
"v_mfma_f32_16x16x32_bf16 v[108:111], a[48:51], a[136:139], v[108:111]\n"
"v_mfma_f32_16x16x32_bf16 v[112:115], a[48:51], a[144:147], v[112:115]\n"
"v_mfma_f32_16x16x32_bf16 v[112:115], a[52:55], a[148:151], v[112:115]\n"
"global_load_dwordx4 a[44:47], v0, s[34:35] offset:-1024 sc1\n"
"v_mfma_f32_16x16x32_bf16 v[140:143], a[60:63], a[148:151], v[140:143]\n"
"v_mfma_f32_16x16x32_bf16 v[140:143], a[56:59], a[144:147], v[140:143]\n"
"v_mfma_f32_16x16x32_bf16 a[12:15], a[56:59], a[152:155], a[12:15]\n"
"v_mfma_f32_16x16x32_bf16 a[12:15], a[60:63], a[156:159], a[12:15]\n"
"v_mfma_f32_16x16x32_bf16 a[8:11], a[52:55], a[156:159], a[8:11]\n"
"v_mfma_f32_16x16x32_bf16 a[8:11], a[48:51], a[152:155], a[8:11]\n"
"s_waitcnt vmcnt(13)\n"
"v_mfma_f32_16x16x32_bf16 v[144:147], a[64:67], a[96:99], v[144:147]\n"
"v_mfma_f32_16x16x32_bf16 v[144:147], a[68:71], a[100:103], v[144:147]\n"
"global_load_dwordx4 a[48:51], v0, s[34:35] offset:0 sc1\n"
"v_mfma_f32_16x16x32_bf16 v[172:175], a[76:79], a[100:103], v[172:175]\n"
"v_mfma_f32_16x16x32_bf16 v[172:175], a[72:75], a[96:99], v[172:175]\n"
"v_mfma_f32_16x16x32_bf16 v[176:179], a[72:75], a[104:107], v[176:179]\n"
"v_mfma_f32_16x16x32_bf16 v[176:179], a[76:79], a[108:111], v[176:179]\n"
"v_mfma_f32_16x16x32_bf16 v[148:151], a[68:71], a[108:111], v[148:151]\n"
"v_mfma_f32_16x16x32_bf16 v[148:151], a[64:67], a[104:107], v[148:151]\n"
"v_mfma_f32_16x16x32_bf16 v[152:155], a[64:67], a[112:115], v[152:155]\n"
"v_lshlrev_b32 v20, 16, v22\n"
"v_mfma_f32_16x16x32_bf16 v[152:155], a[68:71], a[116:119], v[152:155]\n"
"global_load_dwordx4 a[52:55], v0, s[34:35] offset:1024 sc1\n"
"v_and_b32 v21, s63, v22\n"
"v_mfma_f32_16x16x32_bf16 v[180:183], a[76:79], a[116:119], v[180:183]\n"
"v_lshlrev_b32 v22, 16, v23\n"
"v_mfma_f32_16x16x32_bf16 v[180:183], a[72:75], a[112:115], v[180:183]\n"
"v_and_b32 v23, s63, v23\n"
"v_mfma_f32_16x16x32_bf16 v[184:187], a[72:75], a[120:123], v[184:187]\n"
"v_add_f32 v60, v60, v20\n"
"v_mfma_f32_16x16x32_bf16 v[184:187], a[76:79], a[124:127], v[184:187]\n"
"v_add_f32 v61, v61, v21\n"
"v_add_f32 v62, v62, v22\n"
"v_mfma_f32_16x16x32_bf16 v[156:159], a[68:71], a[124:127], v[156:159]\n"
"v_add_f32 v63, v63, v23\n"
"v_mfma_f32_16x16x32_bf16 v[156:159], a[64:67], a[120:123], v[156:159]\n"
"v_add_f32 v64, v64, v20\n"
"v_mfma_f32_16x16x32_bf16 v[160:163], a[64:67], a[128:131], v[160:163]\n"
"v_add_f32 v65, v65, v21\n"
"v_mfma_f32_16x16x32_bf16 v[160:163], a[68:71], a[132:135], v[160:163]\n"
"global_load_dwordx4 a[56:59], v0, s[34:35] offset:2048 sc1\n"
"v_add_f32 v66, v66, v22\n"
"v_mfma_f32_16x16x32_bf16 v[188:191], a[76:79], a[132:135], v[188:191]\n"
"v_add_f32 v67, v67, v23\n"
"v_mfma_f32_16x16x32_bf16 v[188:191], a[72:75], a[128:131], v[188:191]\n"
"v_add_f32 v68, v68, v20\n"
"v_mfma_f32_16x16x32_bf16 v[192:195], a[72:75], a[136:139], v[192:195]\n"
"v_add_f32 v69, v69, v21\n"
"v_mfma_f32_16x16x32_bf16 v[192:195], a[76:79], a[140:143], v[192:195]\n"
"v_add_f32 v70, v70, v22\n"
"v_mfma_f32_16x16x32_bf16 v[164:167], a[68:71], a[140:143], v[164:167]\n"
"v_add_f32 v71, v71, v23\n"
"v_mfma_f32_16x16x32_bf16 v[164:167], a[64:67], a[136:139], v[164:167]\n"
"v_add_f32 v72, v72, v20\n"
"v_add_f32 v73, v73, v21\n"
"v_mfma_f32_16x16x32_bf16 v[168:171], a[64:67], a[144:147], v[168:171]\n"
"v_add_f32 v74, v74, v22\n"
"v_mfma_f32_16x16x32_bf16 v[168:171], a[68:71], a[148:151], v[168:171]\n"
"global_load_dwordx4 a[60:63], v0, s[34:35] offset:3072 sc1\n"
"v_add_f32 v75, v75, v23\n"
"v_mfma_f32_16x16x32_bf16 v[196:199], a[76:79], a[148:151], v[196:199]\n"
"v_add_f32 v76, v76, v20\n"
"v_mfma_f32_16x16x32_bf16 v[196:199], a[72:75], a[144:147], v[196:199]\n"
"v_add_f32 v77, v77, v21\n"
"v_mfma_f32_16x16x32_bf16 a[20:23], a[72:75], a[152:155], a[20:23]\n"
"s_add_u32 s34, s34, 8192\n"
"v_add_f32 v78, v78, v22\n"
"v_mfma_f32_16x16x32_bf16 a[20:23], a[76:79], a[156:159], a[20:23]\n"
"v_add_f32 v79, v79, v23\n"
"v_mfma_f32_16x16x32_bf16 a[16:19], a[68:71], a[156:159], a[16:19]\n"
"v_add_f32 v80, v80, v20\n"
"v_mfma_f32_16x16x32_bf16 a[16:19], a[64:67], a[152:155], a[16:19]\n"
"s_waitcnt vmcnt(9)\n"
"v_add_f32 v81, v81, v21\n"
"v_mfma_f32_16x16x32_bf16 v[200:203], a[80:83], a[96:99], v[200:203]\n"
"v_add_f32 v82, v82, v22\n"
"v_add_f32 v83, v83, v23\n"
"v_mfma_f32_16x16x32_bf16 v[200:203], a[84:87], a[100:103], v[200:203]\n"
"global_load_dwordx4 a[64:67], v0, s[34:35] offset:-4096 sc1\n"
"v_add_f32 v84, v84, v20\n"
"v_mfma_f32_16x16x32_bf16 v[228:231], a[92:95], a[100:103], v[228:231]\n"
"s_waitcnt vmcnt(9)\n"
"ds_read_b128 a[100:103], v2 offset:1024\n"
"v_add_f32 v85, v85, v21\n"
"v_mfma_f32_16x16x32_bf16 v[228:231], a[88:91], a[96:99], v[228:231]\n"
"ds_read_b128 a[96:99], v2 offset:0\n"
"v_add_f32 v86, v86, v22\n"
"v_mfma_f32_16x16x32_bf16 v[232:235], a[88:91], a[104:107], v[232:235]\n"
"v_add_f32 v87, v87, v23\n"
"v_mfma_f32_16x16x32_bf16 v[232:235], a[92:95], a[108:111], v[232:235]\n"
"v_accvgpr_read_b32 v12, a4\n"
"v_mfma_f32_16x16x32_bf16 v[204:207], a[84:87], a[108:111], v[204:207]\n"
"ds_read_b128 a[108:111], v2 offset:3072\n"
"v_accvgpr_read_b32 v13, a5\n"
"v_mfma_f32_16x16x32_bf16 v[204:207], a[80:83], a[104:107], v[204:207]\n"
"ds_read_b128 a[104:107], v2 offset:2048\n"
"v_accvgpr_read_b32 v14, a6\n"
"v_mfma_f32_16x16x32_bf16 v[208:211], a[80:83], a[112:115], v[208:211]\n"
"v_accvgpr_read_b32 v15, a7\n"
"v_mfma_f32_16x16x32_bf16 v[208:211], a[84:87], a[116:119], v[208:211]\n"
"global_load_dwordx4 a[68:71], v0, s[34:35] offset:-3072 sc1\n"
"v_add_f32 v12, v12, v20\n"
"v_mfma_f32_16x16x32_bf16 v[236:239], a[92:95], a[116:119], v[236:239]\n"
"ds_read_b128 a[116:119], v2 offset:5120\n"
"v_add_f32 v13, v13, v21\n"
"v_mfma_f32_16x16x32_bf16 v[236:239], a[88:91], a[112:115], v[236:239]\n"
"ds_read_b128 a[112:115], v2 offset:4096\n"
"v_add_f32 v14, v14, v22\n"
"v_mfma_f32_16x16x32_bf16 v[240:243], a[88:91], a[120:123], v[240:243]\n"
"v_add_f32 v15, v15, v23\n"
"v_accvgpr_write_b32 a4, v12\n"
"v_mfma_f32_16x16x32_bf16 v[240:243], a[92:95], a[124:127], v[240:243]\n"
"v_accvgpr_write_b32 a5, v13\n"
"v_mfma_f32_16x16x32_bf16 v[212:215], a[84:87], a[124:127], v[212:215]\n"
"ds_read_b128 a[124:127], v2 offset:7168\n"
"v_accvgpr_write_b32 a6, v14\n"
"v_mfma_f32_16x16x32_bf16 v[212:215], a[80:83], a[120:123], v[212:215]\n"
"ds_read_b128 a[120:123], v2 offset:6144\n"
"v_accvgpr_write_b32 a7, v15\n"
"v_mfma_f32_16x16x32_bf16 v[216:219], a[80:83], a[128:131], v[216:219]\n"
"v_mfma_f32_16x16x32_bf16 v[216:219], a[84:87], a[132:135], v[216:219]\n"
"global_load_dwordx4 a[72:75], v0, s[34:35] offset:-2048 sc1\n"
"v_lshrrev_b32 v20, 4, v0\n"
"v_mfma_f32_16x16x32_bf16 v[244:247], a[92:95], a[132:135], v[244:247]\n"
"ds_read_b128 a[132:135], v2 offset:9216\n"
"v_and_b32 v20, 0x30, v20\n"
"v_mfma_f32_16x16x32_bf16 v[244:247], a[88:91], a[128:131], v[244:247]\n"
"ds_read_b128 a[128:131], v2 offset:8192\n"
"buffer_load_dwordx2 v[22:23], v20, s[44:47], s61 offen offset:72\n"
"v_mfma_f32_16x16x32_bf16 v[248:251], a[88:91], a[136:139], v[248:251]\n"
"v_mfma_f32_16x16x32_bf16 v[248:251], a[92:95], a[140:143], v[248:251]\n"
"v_mfma_f32_16x16x32_bf16 v[220:223], a[84:87], a[140:143], v[220:223]\n"
"ds_read_b128 a[140:143], v2 offset:11264\n"
"v_mfma_f32_16x16x32_bf16 v[220:223], a[80:83], a[136:139], v[220:223]\n"
"ds_read_b128 a[136:139], v2 offset:10240\n"
"v_mfma_f32_16x16x32_bf16 v[224:227], a[80:83], a[144:147], v[224:227]\n"
"v_mfma_f32_16x16x32_bf16 v[224:227], a[84:87], a[148:151], v[224:227]\n"
"global_load_dwordx4 a[76:79], v0, s[34:35] offset:-1024 sc1\n"
"v_mfma_f32_16x16x32_bf16 v[252:255], a[92:95], a[148:151], v[252:255]\n"
"ds_read_b128 a[148:151], v2 offset:13312\n"
"v_mfma_f32_16x16x32_bf16 v[252:255], a[88:91], a[144:147], v[252:255]\n"
"ds_read_b128 a[144:147], v2 offset:12288\n"
"v_mfma_f32_16x16x32_bf16 a[28:31], a[88:91], a[152:155], a[28:31]\n"
"v_mfma_f32_16x16x32_bf16 a[28:31], a[92:95], a[156:159], a[28:31]\n"
"v_mfma_f32_16x16x32_bf16 a[24:27], a[84:87], a[156:159], a[24:27]\n"
"s_cmp_eq_u32 s62, 5\n"
"ds_read_b128 a[156:159], v2 offset:15360\n"
"v_mfma_f32_16x16x32_bf16 a[24:27], a[80:83], a[152:155], a[24:27]\n"
"ds_read_b128 a[152:155], v2 offset:14336\n"
"s_cbranch_scc1 L_last\n"
"s_waitcnt vmcnt(9) lgkmcnt(12)\n"
"v_mfma_f32_16x16x32_bf16 v[32:35], a[32:35], a[96:99], v[32:35]\n"
"s_add_u32 s48, s48, 131072\n"
"s_addc_u32 s49, s49, 0\n"
"v_mfma_f32_16x16x32_bf16 v[32:35], a[36:39], a[100:103], v[32:35]\n"
"global_load_dwordx4 a[80:83], v0, s[34:35] offset:0 sc1\n"
"L_P4:\n"
"v_mfma_f32_16x16x32_bf16 v[60:63], a[44:47], a[100:103], v[60:63]\n"
"s_barrier\n"
"v_mfma_f32_16x16x32_bf16 v[60:63], a[40:43], a[96:99], v[60:63]\n"
"v_mfma_f32_16x16x32_bf16 v[64:67], a[40:43], a[104:107], v[64:67]\n"
"v_mfma_f32_16x16x32_bf16 v[64:67], a[44:47], a[108:111], v[64:67]\n"
"v_mfma_f32_16x16x32_bf16 v[36:39], a[36:39], a[108:111], v[36:39]\n"
"v_subrev_u32 v8, s65, v8\n"
"v_mfma_f32_16x16x32_bf16 v[36:39], a[32:35], a[104:107], v[36:39]\n"
"s_waitcnt lgkmcnt(8)\n"
"v_xor_b32 v9, s79, v9\n"
"v_mfma_f32_16x16x32_bf16 v[40:43], a[32:35], a[112:115], v[40:43]\n"
"v_or_b32 v8, v8, v9\n"
"v_mfma_f32_16x16x32_bf16 v[40:43], a[36:39], a[116:119], v[40:43]\n"
"global_load_dwordx4 a[84:87], v0, s[34:35] offset:1024 sc1\n"
"v_cmp_lt_u32 vcc, 15, v8\n"
"v_mfma_f32_16x16x32_bf16 v[68:71], a[44:47], a[116:119], v[68:71]\n"
"s_cbranch_vccnz L_slow1\n"
"L_back1:\n"
"v_mfma_f32_16x16x32_bf16 v[68:71], a[40:43], a[112:115], v[68:71]\n"
"v_mfma_f32_16x16x32_bf16 v[72:75], a[40:43], a[120:123], v[72:75]\n"
"v_mfma_f32_16x16x32_bf16 v[72:75], a[44:47], a[124:127], v[72:75]\n"
"v_mfma_f32_16x16x32_bf16 v[44:47], a[36:39], a[124:127], v[44:47]\n"
"v_mfma_f32_16x16x32_bf16 v[44:47], a[32:35], a[120:123], v[44:47]\n"
"s_waitcnt lgkmcnt(4)\n"
"v_mfma_f32_16x16x32_bf16 v[48:51], a[32:35], a[128:131], v[48:51]\n"
"v_mfma_f32_16x16x32_bf16 v[48:51], a[36:39], a[132:135], v[48:51]\n"
"global_load_dwordx4 a[88:91], v0, s[34:35] offset:2048 sc1\n"
"v_mfma_f32_16x16x32_bf16 v[76:79], a[44:47], a[132:135], v[76:79]\n"
"v_mfma_f32_16x16x32_bf16 v[76:79], a[40:43], a[128:131], v[76:79]\n"
"s_add_u32 m0, s64, 16384\n"
"v_mfma_f32_16x16x32_bf16 v[80:83], a[40:43], a[136:139], v[80:83]\n"
"v_mfma_f32_16x16x32_bf16 v[80:83], a[44:47], a[140:143], v[80:83]\n"
"v_mfma_f32_16x16x32_bf16 v[52:55], a[36:39], a[140:143], v[52:55]\n"
"v_mfma_f32_16x16x32_bf16 v[52:55], a[32:35], a[136:139], v[52:55]\n"
"s_waitcnt lgkmcnt(0)\n"
"v_mfma_f32_16x16x32_bf16 v[56:59], a[32:35], a[144:147], v[56:59]\n"
"v_mfma_f32_16x16x32_bf16 v[56:59], a[36:39], a[148:151], v[56:59]\n"
"global_load_dwordx4 a[92:95], v0, s[34:35] offset:3072 sc1\n"
"v_mfma_f32_16x16x32_bf16 v[84:87], a[44:47], a[148:151], v[84:87]\n"
"v_mfma_f32_16x16x32_bf16 v[84:87], a[40:43], a[144:147], v[84:87]\n"
"v_mfma_f32_16x16x32_bf16 a[4:7], a[40:43], a[152:155], a[4:7]\n"
"s_add_u32 s34, s34, 8192\n"
"v_mfma_f32_16x16x32_bf16 a[4:7], a[44:47], a[156:159], a[4:7]\n"
"v_mfma_f32_16x16x32_bf16 a[0:3], a[36:39], a[156:159], a[0:3]\n"
"v_mfma_f32_16x16x32_bf16 a[0:3], a[32:35], a[152:155], a[0:3]\n"
"s_waitcnt vmcnt(9)\n"
"v_mfma_f32_16x16x32_bf16 v[88:91], a[48:51], a[96:99], v[88:91]\n"
"v_mfma_f32_16x16x32_bf16 v[88:91], a[52:55], a[100:103], v[88:91]\n"
"global_load_dwordx4 a[32:35], v0, s[34:35] offset:-4096 sc1\n"
"v_mfma_f32_16x16x32_bf16 v[116:119], a[60:63], a[100:103], v[116:119]\n"
"buffer_load_dwordx4 v0, s[48:51], s75 offen offset:0 sc1 lds\n"
"v_mfma_f32_16x16x32_bf16 v[116:119], a[56:59], a[96:99], v[116:119]\n"
"v_mfma_f32_16x16x32_bf16 v[120:123], a[56:59], a[104:107], v[120:123]\n"
"v_mfma_f32_16x16x32_bf16 v[120:123], a[60:63], a[108:111], v[120:123]\n"
"v_mfma_f32_16x16x32_bf16 v[92:95], a[52:55], a[108:111], v[92:95]\n"
"v_mfma_f32_16x16x32_bf16 v[92:95], a[48:51], a[104:107], v[92:95]\n"
"v_mfma_f32_16x16x32_bf16 v[96:99], a[48:51], a[112:115], v[96:99]\n"
"v_mfma_f32_16x16x32_bf16 v[96:99], a[52:55], a[116:119], v[96:99]\n"
"global_load_dwordx4 a[36:39], v0, s[34:35] offset:-3072 sc1\n"
"v_mfma_f32_16x16x32_bf16 v[124:127], a[60:63], a[116:119], v[124:127]\n"
"v_mfma_f32_16x16x32_bf16 v[124:127], a[56:59], a[112:115], v[124:127]\n"
"v_mfma_f32_16x16x32_bf16 v[128:131], a[56:59], a[120:123], v[128:131]\n"
"v_mfma_f32_16x16x32_bf16 v[128:131], a[60:63], a[124:127], v[128:131]\n"
"v_mfma_f32_16x16x32_bf16 v[100:103], a[52:55], a[124:127], v[100:103]\n"
"v_mfma_f32_16x16x32_bf16 v[100:103], a[48:51], a[120:123], v[100:103]\n"
"v_mfma_f32_16x16x32_bf16 v[104:107], a[48:51], a[128:131], v[104:107]\n"
"v_mfma_f32_16x16x32_bf16 v[104:107], a[52:55], a[132:135], v[104:107]\n"
"global_load_dwordx4 a[40:43], v0, s[34:35] offset:-2048 sc1\n"
"v_mfma_f32_16x16x32_bf16 v[132:135], a[60:63], a[132:135], v[132:135]\n"
"buffer_load_dwordx4 v0, s[48:51], s75 offen offset:1024 sc1 lds\n"
"v_mfma_f32_16x16x32_bf16 v[132:135], a[56:59], a[128:131], v[132:135]\n"
"v_mfma_f32_16x16x32_bf16 v[136:139], a[56:59], a[136:139], v[136:139]\n"
"v_mfma_f32_16x16x32_bf16 v[136:139], a[60:63], a[140:143], v[136:139]\n"
"v_mfma_f32_16x16x32_bf16 v[108:111], a[52:55], a[140:143], v[108:111]\n"
"v_mfma_f32_16x16x32_bf16 v[108:111], a[48:51], a[136:139], v[108:111]\n"
"v_mfma_f32_16x16x32_bf16 v[112:115], a[48:51], a[144:147], v[112:115]\n"
"v_mfma_f32_16x16x32_bf16 v[112:115], a[52:55], a[148:151], v[112:115]\n"
"global_load_dwordx4 a[44:47], v0, s[34:35] offset:-1024 sc1\n"
"v_mfma_f32_16x16x32_bf16 v[140:143], a[60:63], a[148:151], v[140:143]\n"
"v_mfma_f32_16x16x32_bf16 v[140:143], a[56:59], a[144:147], v[140:143]\n"
"v_mfma_f32_16x16x32_bf16 a[12:15], a[56:59], a[152:155], a[12:15]\n"
"v_mfma_f32_16x16x32_bf16 a[12:15], a[60:63], a[156:159], a[12:15]\n"
"v_mfma_f32_16x16x32_bf16 a[8:11], a[52:55], a[156:159], a[8:11]\n"
"v_mfma_f32_16x16x32_bf16 a[8:11], a[48:51], a[152:155], a[8:11]\n"
"s_waitcnt vmcnt(10)\n"
"v_mfma_f32_16x16x32_bf16 v[144:147], a[64:67], a[96:99], v[144:147]\n"
"v_mfma_f32_16x16x32_bf16 v[144:147], a[68:71], a[100:103], v[144:147]\n"
"global_load_dwordx4 a[48:51], v0, s[34:35] offset:0 sc1\n"
"v_mfma_f32_16x16x32_bf16 v[172:175], a[76:79], a[100:103], v[172:175]\n"
"v_mfma_f32_16x16x32_bf16 v[172:175], a[72:75], a[96:99], v[172:175]\n"
"buffer_load_dwordx4 v0, s[48:51], s75 offen offset:2048 sc1 lds\n"
"v_mfma_f32_16x16x32_bf16 v[176:179], a[72:75], a[104:107], v[176:179]\n"
"v_mfma_f32_16x16x32_bf16 v[176:179], a[76:79], a[108:111], v[176:179]\n"
"v_mfma_f32_16x16x32_bf16 v[148:151], a[68:71], a[108:111], v[148:151]\n"
"v_mfma_f32_16x16x32_bf16 v[148:151], a[64:67], a[104:107], v[148:151]\n"
"v_mfma_f32_16x16x32_bf16 v[152:155], a[64:67], a[112:115], v[152:155]\n"
"v_lshlrev_b32 v16, 16, v18\n"
"v_mfma_f32_16x16x32_bf16 v[152:155], a[68:71], a[116:119], v[152:155]\n"
"global_load_dwordx4 a[52:55], v0, s[34:35] offset:1024 sc1\n"
"v_and_b32 v17, s63, v18\n"
"v_mfma_f32_16x16x32_bf16 v[180:183], a[76:79], a[116:119], v[180:183]\n"
"v_lshlrev_b32 v18, 16, v19\n"
"v_mfma_f32_16x16x32_bf16 v[180:183], a[72:75], a[112:115], v[180:183]\n"
"v_and_b32 v19, s63, v19\n"
"v_mfma_f32_16x16x32_bf16 v[184:187], a[72:75], a[120:123], v[184:187]\n"
"v_add_f32 v88, v88, v16\n"
"v_mfma_f32_16x16x32_bf16 v[184:187], a[76:79], a[124:127], v[184:187]\n"
"v_add_f32 v89, v89, v17\n"
"v_add_f32 v90, v90, v18\n"
"v_mfma_f32_16x16x32_bf16 v[156:159], a[68:71], a[124:127], v[156:159]\n"
"v_add_f32 v91, v91, v19\n"
"v_mfma_f32_16x16x32_bf16 v[156:159], a[64:67], a[120:123], v[156:159]\n"
"v_add_f32 v92, v92, v16\n"
"v_mfma_f32_16x16x32_bf16 v[160:163], a[64:67], a[128:131], v[160:163]\n"
"v_add_f32 v93, v93, v17\n"
"v_mfma_f32_16x16x32_bf16 v[160:163], a[68:71], a[132:135], v[160:163]\n"
"global_load_dwordx4 a[56:59], v0, s[34:35] offset:2048 sc1\n"
"v_add_f32 v94, v94, v18\n"
"v_mfma_f32_16x16x32_bf16 v[188:191], a[76:79], a[132:135], v[188:191]\n"
"buffer_load_dwordx4 v0, s[48:51], s75 offen offset:3072 sc1 lds\n"
"v_mfma_f32_16x16x32_bf16 v[188:191], a[72:75], a[128:131], v[188:191]\n"
"v_add_f32 v95, v95, v19\n"
"v_add_f32 v96, v96, v16\n"
"v_mfma_f32_16x16x32_bf16 v[192:195], a[72:75], a[136:139], v[192:195]\n"
"v_add_f32 v97, v97, v17\n"
"v_mfma_f32_16x16x32_bf16 v[192:195], a[76:79], a[140:143], v[192:195]\n"
"v_add_f32 v98, v98, v18\n"
"v_mfma_f32_16x16x32_bf16 v[164:167], a[68:71], a[140:143], v[164:167]\n"
"v_add_f32 v99, v99, v19\n"
"v_mfma_f32_16x16x32_bf16 v[164:167], a[64:67], a[136:139], v[164:167]\n"
"v_add_f32 v100, v100, v16\n"
"v_add_f32 v101, v101, v17\n"
"v_mfma_f32_16x16x32_bf16 v[168:171], a[64:67], a[144:147], v[168:171]\n"
"v_add_f32 v102, v102, v18\n"
"v_mfma_f32_16x16x32_bf16 v[168:171], a[68:71], a[148:151], v[168:171]\n"
"global_load_dwordx4 a[60:63], v0, s[34:35] offset:3072 sc1\n"
"v_add_f32 v103, v103, v19\n"
"v_mfma_f32_16x16x32_bf16 v[196:199], a[76:79], a[148:151], v[196:199]\n"
"v_add_f32 v104, v104, v16\n"
"v_mfma_f32_16x16x32_bf16 v[196:199], a[72:75], a[144:147], v[196:199]\n"
"v_add_f32 v105, v105, v17\n"
"v_mfma_f32_16x16x32_bf16 a[20:23], a[72:75], a[152:155], a[20:23]\n"
"s_add_u32 s34, s34, 8192\n"
"v_add_f32 v106, v106, v18\n"
"v_mfma_f32_16x16x32_bf16 a[20:23], a[76:79], a[156:159], a[20:23]\n"
"v_add_f32 v107, v107, v19\n"
"v_mfma_f32_16x16x32_bf16 a[16:19], a[68:71], a[156:159], a[16:19]\n"
"v_add_f32 v108, v108, v16\n"
"v_mfma_f32_16x16x32_bf16 a[16:19], a[64:67], a[152:155], a[16:19]\n"
"s_waitcnt vmcnt(12)\n"
"v_add_f32 v109, v109, v17\n"
"v_mfma_f32_16x16x32_bf16 v[200:203], a[80:83], a[96:99], v[200:203]\n"
"v_add_f32 v110, v110, v18\n"
"v_add_f32 v111, v111, v19\n"
"v_mfma_f32_16x16x32_bf16 v[200:203], a[84:87], a[100:103], v[200:203]\n"
"global_load_dwordx4 a[64:67], v0, s[34:35] offset:-4096 sc1\n"
"v_add_f32 v112, v112, v16\n"
"v_mfma_f32_16x16x32_bf16 v[228:231], a[92:95], a[100:103], v[228:231]\n"
"ds_read_b128 a[100:103], v3 offset:1024\n"
"v_add_f32 v113, v113, v17\n"
"v_mfma_f32_16x16x32_bf16 v[228:231], a[88:91], a[96:99], v[228:231]\n"
"s_add_u32 m0, s64, 20480\n"
"ds_read_b128 a[96:99], v3 offset:0\n"
"v_add_f32 v114, v114, v18\n"
"v_mfma_f32_16x16x32_bf16 v[232:235], a[88:91], a[104:107], v[232:235]\n"
"v_add_f32 v115, v115, v19\n"
"v_mfma_f32_16x16x32_bf16 v[232:235], a[92:95], a[108:111], v[232:235]\n"
"v_accvgpr_read_b32 v12, a8\n"
"v_mfma_f32_16x16x32_bf16 v[204:207], a[84:87], a[108:111], v[204:207]\n"
"ds_read_b128 a[108:111], v3 offset:3072\n"
"v_accvgpr_read_b32 v13, a9\n"
"v_mfma_f32_16x16x32_bf16 v[204:207], a[80:83], a[104:107], v[204:207]\n"
"ds_read_b128 a[104:107], v3 offset:2048\n"
"v_accvgpr_read_b32 v14, a10\n"
"v_mfma_f32_16x16x32_bf16 v[208:211], a[80:83], a[112:115], v[208:211]\n"
"v_accvgpr_read_b32 v15, a11\n"
"v_mfma_f32_16x16x32_bf16 v[208:211], a[84:87], a[116:119], v[208:211]\n"
"global_load_dwordx4 a[68:71], v0, s[34:35] offset:-3072 sc1\n"
"v_add_f32 v12, v12, v16\n"
"v_mfma_f32_16x16x32_bf16 v[236:239], a[92:95], a[116:119], v[236:239]\n"
"ds_read_b128 a[116:119], v3 offset:5120\n"
"v_add_f32 v13, v13, v17\n"
"v_mfma_f32_16x16x32_bf16 v[236:239], a[88:91], a[112:115], v[236:239]\n"
"ds_read_b128 a[112:115], v3 offset:4096\n"
"v_add_f32 v14, v14, v18\n"
"v_mfma_f32_16x16x32_bf16 v[240:243], a[88:91], a[120:123], v[240:243]\n"
"v_add_f32 v15, v15, v19\n"
"v_accvgpr_write_b32 a8, v12\n"
"v_mfma_f32_16x16x32_bf16 v[240:243], a[92:95], a[124:127], v[240:243]\n"
"v_accvgpr_write_b32 a9, v13\n"
"v_mfma_f32_16x16x32_bf16 v[212:215], a[84:87], a[124:127], v[212:215]\n"
"ds_read_b128 a[124:127], v3 offset:7168\n"
"v_accvgpr_write_b32 a10, v14\n"
"v_mfma_f32_16x16x32_bf16 v[212:215], a[80:83], a[120:123], v[212:215]\n"
"ds_read_b128 a[120:123], v3 offset:6144\n"
"v_accvgpr_write_b32 a11, v15\n"
"v_mfma_f32_16x16x32_bf16 v[216:219], a[80:83], a[128:131], v[216:219]\n"
"v_mfma_f32_16x16x32_bf16 v[216:219], a[84:87], a[132:135], v[216:219]\n"
"global_load_dwordx4 a[72:75], v0, s[34:35] offset:-2048 sc1\n"
"v_mfma_f32_16x16x32_bf16 v[244:247], a[92:95], a[132:135], v[244:247]\n"
"ds_read_b128 a[132:135], v3 offset:9216\n"
"v_mfma_f32_16x16x32_bf16 v[244:247], a[88:91], a[128:131], v[244:247]\n"
"ds_read_b128 a[128:131], v3 offset:8192\n"
"v_mfma_f32_16x16x32_bf16 v[248:251], a[88:91], a[136:139], v[248:251]\n"
"buffer_load_dwordx4 v1, s[48:51], s75 offen offset:0 sc1 lds\n"
"v_mfma_f32_16x16x32_bf16 v[248:251], a[92:95], a[140:143], v[248:251]\n"
"v_mfma_f32_16x16x32_bf16 v[220:223], a[84:87], a[140:143], v[220:223]\n"
"ds_read_b128 a[140:143], v3 offset:11264\n"
"v_mfma_f32_16x16x32_bf16 v[220:223], a[80:83], a[136:139], v[220:223]\n"
"ds_read_b128 a[136:139], v3 offset:10240\n"
"v_mfma_f32_16x16x32_bf16 v[224:227], a[80:83], a[144:147], v[224:227]\n"
"v_mfma_f32_16x16x32_bf16 v[224:227], a[84:87], a[148:151], v[224:227]\n"
"global_load_dwordx4 a[76:79], v0, s[34:35] offset:-1024 sc1\n"
"v_mfma_f32_16x16x32_bf16 v[252:255], a[92:95], a[148:151], v[252:255]\n"
"ds_read_b128 a[148:151], v3 offset:13312\n"
"v_mfma_f32_16x16x32_bf16 v[252:255], a[88:91], a[144:147], v[252:255]\n"
"ds_read_b128 a[144:147], v3 offset:12288\n"
"v_mfma_f32_16x16x32_bf16 a[28:31], a[88:91], a[152:155], a[28:31]\n"
"v_mfma_f32_16x16x32_bf16 a[28:31], a[92:95], a[156:159], a[28:31]\n"
"v_mfma_f32_16x16x32_bf16 a[24:27], a[84:87], a[156:159], a[24:27]\n"
"ds_read_b128 a[156:159], v3 offset:15360\n"
"v_mfma_f32_16x16x32_bf16 a[24:27], a[80:83], a[152:155], a[24:27]\n"
"s_waitcnt vmcnt(11) lgkmcnt(11)\n"
"ds_read_b128 a[152:155], v3 offset:14336\n"
"v_mfma_f32_16x16x32_bf16 v[32:35], a[32:35], a[96:99], v[32:35]\n"
"v_mfma_f32_16x16x32_bf16 v[32:35], a[36:39], a[100:103], v[32:35]\n"
"global_load_dwordx4 a[80:83], v0, s[34:35] offset:0 sc1\n"
"v_mfma_f32_16x16x32_bf16 v[60:63], a[44:47], a[100:103], v[60:63]\n"
"v_mfma_f32_16x16x32_bf16 v[60:63], a[40:43], a[96:99], v[60:63]\n"
"buffer_load_dwordx4 v1, s[48:51], s75 offen offset:1024 sc1 lds\n"
"v_mfma_f32_16x16x32_bf16 v[64:67], a[40:43], a[104:107], v[64:67]\n"
"v_mfma_f32_16x16x32_bf16 v[64:67], a[44:47], a[108:111], v[64:67]\n"
"v_mfma_f32_16x16x32_bf16 v[36:39], a[36:39], a[108:111], v[36:39]\n"
"v_mfma_f32_16x16x32_bf16 v[36:39], a[32:35], a[104:107], v[36:39]\n"
"s_waitcnt lgkmcnt(8)\n"
"v_mfma_f32_16x16x32_bf16 v[40:43], a[32:35], a[112:115], v[40:43]\n"
"v_mfma_f32_16x16x32_bf16 v[40:43], a[36:39], a[116:119], v[40:43]\n"
"global_load_dwordx4 a[84:87], v0, s[34:35] offset:1024 sc1\n"
"v_mfma_f32_16x16x32_bf16 v[68:71], a[44:47], a[116:119], v[68:71]\n"
"v_mfma_f32_16x16x32_bf16 v[68:71], a[40:43], a[112:115], v[68:71]\n"
"v_mfma_f32_16x16x32_bf16 v[72:75], a[40:43], a[120:123], v[72:75]\n"
"v_mfma_f32_16x16x32_bf16 v[72:75], a[44:47], a[124:127], v[72:75]\n"
"v_mfma_f32_16x16x32_bf16 v[44:47], a[36:39], a[124:127], v[44:47]\n"
"v_mfma_f32_16x16x32_bf16 v[44:47], a[32:35], a[120:123], v[44:47]\n"
"s_waitcnt lgkmcnt(4)\n"
"v_mfma_f32_16x16x32_bf16 v[48:51], a[32:35], a[128:131], v[48:51]\n"
"v_mfma_f32_16x16x32_bf16 v[48:51], a[36:39], a[132:135], v[48:51]\n"
"global_load_dwordx4 a[88:91], v0, s[34:35] offset:2048 sc1\n"
"v_mfma_f32_16x16x32_bf16 v[76:79], a[44:47], a[132:135], v[76:79]\n"
"v_mfma_f32_16x16x32_bf16 v[76:79], a[40:43], a[128:131], v[76:79]\n"
"buffer_load_dwordx4 v1, s[48:51], s75 offen offset:2048 sc1 lds\n"
"v_mfma_f32_16x16x32_bf16 v[80:83], a[40:43], a[136:139], v[80:83]\n"
"v_mfma_f32_16x16x32_bf16 v[80:83], a[44:47], a[140:143], v[80:83]\n"
"v_mfma_f32_16x16x32_bf16 v[52:55], a[36:39], a[140:143], v[52:55]\n"
"v_mfma_f32_16x16x32_bf16 v[52:55], a[32:35], a[136:139], v[52:55]\n"
"s_waitcnt lgkmcnt(0)\n"
"v_mfma_f32_16x16x32_bf16 v[56:59], a[32:35], a[144:147], v[56:59]\n"
"v_mfma_f32_16x16x32_bf16 v[56:59], a[36:39], a[148:151], v[56:59]\n"
"global_load_dwordx4 a[92:95], v0, s[34:35] offset:3072 sc1\n"
"v_mfma_f32_16x16x32_bf16 v[84:87], a[44:47], a[148:151], v[84:87]\n"
"v_mfma_f32_16x16x32_bf16 v[84:87], a[40:43], a[144:147], v[84:87]\n"
"v_mfma_f32_16x16x32_bf16 a[4:7], a[40:43], a[152:155], a[4:7]\n"
"s_add_u32 s34, s34, 8192\n"
"v_mfma_f32_16x16x32_bf16 a[4:7], a[44:47], a[156:159], a[4:7]\n"
"v_mfma_f32_16x16x32_bf16 a[0:3], a[36:39], a[156:159], a[0:3]\n"
"v_mfma_f32_16x16x32_bf16 a[0:3], a[32:35], a[152:155], a[0:3]\n"
"s_waitcnt vmcnt(11)\n"
"v_mfma_f32_16x16x32_bf16 v[88:91], a[48:51], a[96:99], v[88:91]\n"
"v_mfma_f32_16x16x32_bf16 v[88:91], a[52:55], a[100:103], v[88:91]\n"
"global_load_dwordx4 a[32:35], v0, s[34:35] offset:-4096 sc1\n"
"v_mfma_f32_16x16x32_bf16 v[116:119], a[60:63], a[100:103], v[116:119]\n"
"v_mfma_f32_16x16x32_bf16 v[116:119], a[56:59], a[96:99], v[116:119]\n"
"buffer_load_dwordx4 v1, s[48:51], s75 offen offset:3072 sc1 lds\n"
"v_mfma_f32_16x16x32_bf16 v[120:123], a[56:59], a[104:107], v[120:123]\n"
"v_mfma_f32_16x16x32_bf16 v[120:123], a[60:63], a[108:111], v[120:123]\n"
"v_mfma_f32_16x16x32_bf16 v[92:95], a[52:55], a[108:111], v[92:95]\n"
"v_mfma_f32_16x16x32_bf16 v[92:95], a[48:51], a[104:107], v[92:95]\n"
"v_mfma_f32_16x16x32_bf16 v[96:99], a[48:51], a[112:115], v[96:99]\n"
"v_mfma_f32_16x16x32_bf16 v[96:99], a[52:55], a[116:119], v[96:99]\n"
"global_load_dwordx4 a[36:39], v0, s[34:35] offset:-3072 sc1\n"
"v_mfma_f32_16x16x32_bf16 v[124:127], a[60:63], a[116:119], v[124:127]\n"
"v_mfma_f32_16x16x32_bf16 v[124:127], a[56:59], a[112:115], v[124:127]\n"
"v_mfma_f32_16x16x32_bf16 v[128:131], a[56:59], a[120:123], v[128:131]\n"
"v_mfma_f32_16x16x32_bf16 v[128:131], a[60:63], a[124:127], v[128:131]\n"
"v_mfma_f32_16x16x32_bf16 v[100:103], a[52:55], a[124:127], v[100:103]\n"
"v_mfma_f32_16x16x32_bf16 v[100:103], a[48:51], a[120:123], v[100:103]\n"
"v_mfma_f32_16x16x32_bf16 v[104:107], a[48:51], a[128:131], v[104:107]\n"
"v_mfma_f32_16x16x32_bf16 v[104:107], a[52:55], a[132:135], v[104:107]\n"
"global_load_dwordx4 a[40:43], v0, s[34:35] offset:-2048 sc1\n"
"v_mfma_f32_16x16x32_bf16 v[132:135], a[60:63], a[132:135], v[132:135]\n"
"v_mfma_f32_16x16x32_bf16 v[132:135], a[56:59], a[128:131], v[132:135]\n"
"s_add_u32 m0, s64, 24576\n"
"v_mfma_f32_16x16x32_bf16 v[136:139], a[56:59], a[136:139], v[136:139]\n"
"v_mfma_f32_16x16x32_bf16 v[136:139], a[60:63], a[140:143], v[136:139]\n"
"v_mfma_f32_16x16x32_bf16 v[108:111], a[52:55], a[140:143], v[108:111]\n"
"v_mfma_f32_16x16x32_bf16 v[108:111], a[48:51], a[136:139], v[108:111]\n"
"v_mfma_f32_16x16x32_bf16 v[112:115], a[48:51], a[144:147], v[112:115]\n"
"v_mfma_f32_16x16x32_bf16 v[112:115], a[52:55], a[148:151], v[112:115]\n"
"global_load_dwordx4 a[44:47], v0, s[34:35] offset:-1024 sc1\n"
"v_mfma_f32_16x16x32_bf16 v[140:143], a[60:63], a[148:151], v[140:143]\n"
"v_mfma_f32_16x16x32_bf16 v[140:143], a[56:59], a[144:147], v[140:143]\n"
"v_mfma_f32_16x16x32_bf16 a[12:15], a[56:59], a[152:155], a[12:15]\n"
"v_mfma_f32_16x16x32_bf16 a[12:15], a[60:63], a[156:159], a[12:15]\n"
"v_mfma_f32_16x16x32_bf16 a[8:11], a[52:55], a[156:159], a[8:11]\n"
"v_mfma_f32_16x16x32_bf16 a[8:11], a[48:51], a[152:155], a[8:11]\n"
"s_waitcnt vmcnt(11)\n"
"v_mfma_f32_16x16x32_bf16 v[144:147], a[64:67], a[96:99], v[144:147]\n"
"v_mfma_f32_16x16x32_bf16 v[144:147], a[68:71], a[100:103], v[144:147]\n"
"global_load_dwordx4 a[48:51], v0, s[34:35] offset:0 sc1\n"
"v_mfma_f32_16x16x32_bf16 v[172:175], a[76:79], a[100:103], v[172:175]\n"
"v_mfma_f32_16x16x32_bf16 v[172:175], a[72:75], a[96:99], v[172:175]\n"
"buffer_load_dwordx4 v0, s[48:51], s69 offen offset:0 sc1 lds\n"
"v_mfma_f32_16x16x32_bf16 v[176:179], a[72:75], a[104:107], v[176:179]\n"
"v_mfma_f32_16x16x32_bf16 v[176:179], a[76:79], a[108:111], v[176:179]\n"
"v_mfma_f32_16x16x32_bf16 v[148:151], a[68:71], a[108:111], v[148:151]\n"
"v_mfma_f32_16x16x32_bf16 v[148:151], a[64:67], a[104:107], v[148:151]\n"
"v_mfma_f32_16x16x32_bf16 v[152:155], a[64:67], a[112:115], v[152:155]\n"
"v_lshlrev_b32 v20, 16, v22\n"
"v_mfma_f32_16x16x32_bf16 v[152:155], a[68:71], a[116:119], v[152:155]\n"
"global_load_dwordx4 a[52:55], v0, s[34:35] offset:1024 sc1\n"
"v_and_b32 v21, s63, v22\n"
"v_mfma_f32_16x16x32_bf16 v[180:183], a[76:79], a[116:119], v[180:183]\n"
"v_lshlrev_b32 v22, 16, v23\n"
"v_mfma_f32_16x16x32_bf16 v[180:183], a[72:75], a[112:115], v[180:183]\n"
"v_and_b32 v23, s63, v23\n"
"v_mfma_f32_16x16x32_bf16 v[184:187], a[72:75], a[120:123], v[184:187]\n"
"v_add_f32 v116, v116, v20\n"
"v_mfma_f32_16x16x32_bf16 v[184:187], a[76:79], a[124:127], v[184:187]\n"
"v_add_f32 v117, v117, v21\n"
"v_add_f32 v118, v118, v22\n"
"v_mfma_f32_16x16x32_bf16 v[156:159], a[68:71], a[124:127], v[156:159]\n"
"v_add_f32 v119, v119, v23\n"
"v_mfma_f32_16x16x32_bf16 v[156:159], a[64:67], a[120:123], v[156:159]\n"
"v_add_f32 v120, v120, v20\n"
"v_mfma_f32_16x16x32_bf16 v[160:163], a[64:67], a[128:131], v[160:163]\n"
"v_add_f32 v121, v121, v21\n"
"v_mfma_f32_16x16x32_bf16 v[160:163], a[68:71], a[132:135], v[160:163]\n"
"global_load_dwordx4 a[56:59], v0, s[34:35] offset:2048 sc1\n"
"v_add_f32 v122, v122, v22\n"
"v_mfma_f32_16x16x32_bf16 v[188:191], a[76:79], a[132:135], v[188:191]\n"
"v_add_f32 v123, v123, v23\n"
"v_add_f32 v124, v124, v20\n"
"v_mfma_f32_16x16x32_bf16 v[188:191], a[72:75], a[128:131], v[188:191]\n"
"buffer_load_dwordx4 v0, s[48:51], s69 offen offset:1024 sc1 lds\n"
"v_mfma_f32_16x16x32_bf16 v[192:195], a[72:75], a[136:139], v[192:195]\n"
"v_add_f32 v125, v125, v21\n"
"v_mfma_f32_16x16x32_bf16 v[192:195], a[76:79], a[140:143], v[192:195]\n"
"v_add_f32 v126, v126, v22\n"
"v_mfma_f32_16x16x32_bf16 v[164:167], a[68:71], a[140:143], v[164:167]\n"
"v_add_f32 v127, v127, v23\n"
"v_mfma_f32_16x16x32_bf16 v[164:167], a[64:67], a[136:139], v[164:167]\n"
"v_add_f32 v128, v128, v20\n"
"v_add_f32 v129, v129, v21\n"
"v_mfma_f32_16x16x32_bf16 v[168:171], a[64:67], a[144:147], v[168:171]\n"
"v_add_f32 v130, v130, v22\n"
"v_mfma_f32_16x16x32_bf16 v[168:171], a[68:71], a[148:151], v[168:171]\n"
"global_load_dwordx4 a[60:63], v0, s[34:35] offset:3072 sc1\n"
"v_add_f32 v131, v131, v23\n"
"v_mfma_f32_16x16x32_bf16 v[196:199], a[76:79], a[148:151], v[196:199]\n"
"v_add_f32 v132, v132, v20\n"
"v_mfma_f32_16x16x32_bf16 v[196:199], a[72:75], a[144:147], v[196:199]\n"
"v_add_f32 v133, v133, v21\n"
"v_mfma_f32_16x16x32_bf16 a[20:23], a[72:75], a[152:155], a[20:23]\n"
"s_add_u32 s34, s34, 8192\n"
"v_add_f32 v134, v134, v22\n"
"v_mfma_f32_16x16x32_bf16 a[20:23], a[76:79], a[156:159], a[20:23]\n"
"v_add_f32 v135, v135, v23\n"
"v_mfma_f32_16x16x32_bf16 a[16:19], a[68:71], a[156:159], a[16:19]\n"
"v_add_f32 v136, v136, v20\n"
"v_mfma_f32_16x16x32_bf16 a[16:19], a[64:67], a[152:155], a[16:19]\n"
"s_waitcnt vmcnt(11)\n"
"v_add_f32 v137, v137, v21\n"
"v_mfma_f32_16x16x32_bf16 v[200:203], a[80:83], a[96:99], v[200:203]\n"
"v_add_f32 v138, v138, v22\n"
"v_add_f32 v139, v139, v23\n"
"v_mfma_f32_16x16x32_bf16 v[200:203], a[84:87], a[100:103], v[200:203]\n"
"global_load_dwordx4 a[64:67], v0, s[34:35] offset:-4096 sc1\n"
"v_add_f32 v140, v140, v20\n"
"v_mfma_f32_16x16x32_bf16 v[228:231], a[92:95], a[100:103], v[228:231]\n"
"ds_read_b128 a[100:103], v4 offset:1024\n"
"v_add_f32 v141, v141, v21\n"
"v_mfma_f32_16x16x32_bf16 v[228:231], a[88:91], a[96:99], v[228:231]\n"
"ds_read_b128 a[96:99], v4 offset:0\n"
"v_add_f32 v142, v142, v22\n"
"v_mfma_f32_16x16x32_bf16 v[232:235], a[88:91], a[104:107], v[232:235]\n"
"buffer_load_dwordx4 v0, s[48:51], s69 offen offset:2048 sc1 lds\n"
"v_mfma_f32_16x16x32_bf16 v[232:235], a[92:95], a[108:111], v[232:235]\n"
"v_add_f32 v143, v143, v23\n"
"v_accvgpr_read_b32 v12, a12\n"
"v_mfma_f32_16x16x32_bf16 v[204:207], a[84:87], a[108:111], v[204:207]\n"
"ds_read_b128 a[108:111], v4 offset:3072\n"
"v_accvgpr_read_b32 v13, a13\n"
"v_mfma_f32_16x16x32_bf16 v[204:207], a[80:83], a[104:107], v[204:207]\n"
"ds_read_b128 a[104:107], v4 offset:2048\n"
"v_accvgpr_read_b32 v14, a14\n"
"v_mfma_f32_16x16x32_bf16 v[208:211], a[80:83], a[112:115], v[208:211]\n"
"v_accvgpr_read_b32 v15, a15\n"
"v_mfma_f32_16x16x32_bf16 v[208:211], a[84:87], a[116:119], v[208:211]\n"
"global_load_dwordx4 a[68:71], v0, s[34:35] offset:-3072 sc1\n"
"v_add_f32 v12, v12, v20\n"
"v_mfma_f32_16x16x32_bf16 v[236:239], a[92:95], a[116:119], v[236:239]\n"
"ds_read_b128 a[116:119], v4 offset:5120\n"
"v_add_f32 v13, v13, v21\n"
"v_mfma_f32_16x16x32_bf16 v[236:239], a[88:91], a[112:115], v[236:239]\n"
"ds_read_b128 a[112:115], v4 offset:4096\n"
"v_add_f32 v14, v14, v22\n"
"v_mfma_f32_16x16x32_bf16 v[240:243], a[88:91], a[120:123], v[240:243]\n"
"v_add_f32 v15, v15, v23\n"
"v_accvgpr_write_b32 a12, v12\n"
"v_mfma_f32_16x16x32_bf16 v[240:243], a[92:95], a[124:127], v[240:243]\n"
"v_accvgpr_write_b32 a13, v13\n"
"v_mfma_f32_16x16x32_bf16 v[212:215], a[84:87], a[124:127], v[212:215]\n"
"ds_read_b128 a[124:127], v4 offset:7168\n"
"v_accvgpr_write_b32 a14, v14\n"
"v_mfma_f32_16x16x32_bf16 v[212:215], a[80:83], a[120:123], v[212:215]\n"
"ds_read_b128 a[120:123], v4 offset:6144\n"
"v_accvgpr_write_b32 a15, v15\n"
"v_mfma_f32_16x16x32_bf16 v[216:219], a[80:83], a[128:131], v[216:219]\n"
"v_mfma_f32_16x16x32_bf16 v[216:219], a[84:87], a[132:135], v[216:219]\n"
"global_load_dwordx4 a[72:75], v0, s[34:35] offset:-2048 sc1\n"
"v_mfma_f32_16x16x32_bf16 v[244:247], a[92:95], a[132:135], v[244:247]\n"
"ds_read_b128 a[132:135], v4 offset:9216\n"
"v_mfma_f32_16x16x32_bf16 v[244:247], a[88:91], a[128:131], v[244:247]\n"
"ds_read_b128 a[128:131], v4 offset:8192\n"
"v_mfma_f32_16x16x32_bf16 v[248:251], a[88:91], a[136:139], v[248:251]\n"
"buffer_load_dwordx4 v0, s[48:51], s69 offen offset:3072 sc1 lds\n"
"v_mfma_f32_16x16x32_bf16 v[248:251], a[92:95], a[140:143], v[248:251]\n"
"v_mfma_f32_16x16x32_bf16 v[220:223], a[84:87], a[140:143], v[220:223]\n"
"ds_read_b128 a[140:143], v4 offset:11264\n"
"v_mfma_f32_16x16x32_bf16 v[220:223], a[80:83], a[136:139], v[220:223]\n"
"ds_read_b128 a[136:139], v4 offset:10240\n"
"v_mfma_f32_16x16x32_bf16 v[224:227], a[80:83], a[144:147], v[224:227]\n"
"v_mfma_f32_16x16x32_bf16 v[224:227], a[84:87], a[148:151], v[224:227]\n"
"global_load_dwordx4 a[76:79], v0, s[34:35] offset:-1024 sc1\n"
"v_mfma_f32_16x16x32_bf16 v[252:255], a[92:95], a[148:151], v[252:255]\n"
"ds_read_b128 a[148:151], v4 offset:13312\n"
"v_mfma_f32_16x16x32_bf16 v[252:255], a[88:91], a[144:147], v[252:255]\n"
"ds_read_b128 a[144:147], v4 offset:12288\n"
"v_mfma_f32_16x16x32_bf16 a[28:31], a[88:91], a[152:155], a[28:31]\n"
"v_mfma_f32_16x16x32_bf16 a[28:31], a[92:95], a[156:159], a[28:31]\n"
"v_mfma_f32_16x16x32_bf16 a[24:27], a[84:87], a[156:159], a[24:27]\n"
"ds_read_b128 a[156:159], v4 offset:15360\n"
"v_mfma_f32_16x16x32_bf16 a[24:27], a[80:83], a[152:155], a[24:27]\n"
"s_waitcnt vmcnt(12) lgkmcnt(11)\n"
"ds_read_b128 a[152:155], v4 offset:14336\n"
"v_mfma_f32_16x16x32_bf16 v[32:35], a[32:35], a[96:99], v[32:35]\n"
"v_mfma_f32_16x16x32_bf16 v[32:35], a[36:39], a[100:103], v[32:35]\n"
"global_load_dwordx4 a[80:83], v0, s[34:35] offset:0 sc1\n"
"v_lshrrev_b32 v16, 4, v0\n"
"v_mfma_f32_16x16x32_bf16 v[60:63], a[44:47], a[100:103], v[60:63]\n"
"v_and_b32 v16, 0x30, v16\n"
"v_mfma_f32_16x16x32_bf16 v[60:63], a[40:43], a[96:99], v[60:63]\n"
"s_add_u32 m0, s64, 28672\n"
"buffer_load_dwordx2 v[18:19], v16, s[44:47], s61 offen offset:128\n"
"v_mfma_f32_16x16x32_bf16 v[64:67], a[40:43], a[104:107], v[64:67]\n"
"v_mfma_f32_16x16x32_bf16 v[64:67], a[44:47], a[108:111], v[64:67]\n"
"v_mfma_f32_16x16x32_bf16 v[36:39], a[36:39], a[108:111], v[36:39]\n"
"buffer_load_dwordx4 v[12:15], v0, s[56:59], %25 offen sc1\n"
"v_mfma_f32_16x16x32_bf16 v[36:39], a[32:35], a[104:107], v[36:39]\n"
"s_waitcnt lgkmcnt(8)\n"
"v_mfma_f32_16x16x32_bf16 v[40:43], a[32:35], a[112:115], v[40:43]\n"
"v_mfma_f32_16x16x32_bf16 v[40:43], a[36:39], a[116:119], v[40:43]\n"
"global_load_dwordx4 a[84:87], v0, s[34:35] offset:1024 sc1\n"
"v_lshrrev_b32 v20, 4, v0\n"
"v_mfma_f32_16x16x32_bf16 v[68:71], a[44:47], a[116:119], v[68:71]\n"
"v_and_b32 v20, 0x30, v20\n"
"v_mfma_f32_16x16x32_bf16 v[68:71], a[40:43], a[112:115], v[68:71]\n"
"buffer_load_dwordx2 v[22:23], v20, s[44:47], s61 offen offset:136\n"
"v_mfma_f32_16x16x32_bf16 v[72:75], a[40:43], a[120:123], v[72:75]\n"
"v_mfma_f32_16x16x32_bf16 v[72:75], a[44:47], a[124:127], v[72:75]\n"
"v_mfma_f32_16x16x32_bf16 v[44:47], a[36:39], a[124:127], v[44:47]\n"
"v_mfma_f32_16x16x32_bf16 v[44:47], a[32:35], a[120:123], v[44:47]\n"
"s_waitcnt lgkmcnt(4)\n"
"v_mfma_f32_16x16x32_bf16 v[48:51], a[32:35], a[128:131], v[48:51]\n"
"v_mfma_f32_16x16x32_bf16 v[48:51], a[36:39], a[132:135], v[48:51]\n"
"global_load_dwordx4 a[88:91], v0, s[34:35] offset:2048 sc1\n"
"v_lshrrev_b32 v24, 4, v0\n"
"v_mfma_f32_16x16x32_bf16 v[76:79], a[44:47], a[132:135], v[76:79]\n"
"v_and_b32 v24, 0x30, v24\n"
"v_mfma_f32_16x16x32_bf16 v[76:79], a[40:43], a[128:131], v[76:79]\n"
"buffer_load_dwordx4 v1, s[48:51], s69 offen offset:0 sc1 lds\n"
"buffer_load_dwordx2 v[26:27], v24, s[44:47], s61 offen offset:192\n"
"v_mfma_f32_16x16x32_bf16 v[80:83], a[40:43], a[136:139], v[80:83]\n"
"v_mfma_f32_16x16x32_bf16 v[80:83], a[44:47], a[140:143], v[80:83]\n"
"v_mfma_f32_16x16x32_bf16 v[52:55], a[36:39], a[140:143], v[52:55]\n"
"v_mfma_f32_16x16x32_bf16 v[52:55], a[32:35], a[136:139], v[52:55]\n"
"s_waitcnt lgkmcnt(0)\n"
"v_mfma_f32_16x16x32_bf16 v[56:59], a[32:35], a[144:147], v[56:59]\n"
"v_mfma_f32_16x16x32_bf16 v[56:59], a[36:39], a[148:151], v[56:59]\n"
"global_load_dwordx4 a[92:95], v0, s[34:35] offset:3072 sc1\n"
"v_lshrrev_b32 v28, 4, v0\n"
"v_mfma_f32_16x16x32_bf16 v[84:87], a[44:47], a[148:151], v[84:87]\n"
"v_and_b32 v28, 0x30, v28\n"
"v_mfma_f32_16x16x32_bf16 v[84:87], a[40:43], a[144:147], v[84:87]\n"
"buffer_load_dwordx2 v[30:31], v28, s[44:47], s61 offen offset:200\n"
"v_mfma_f32_16x16x32_bf16 a[4:7], a[40:43], a[152:155], a[4:7]\n"
"s_add_u32 s34, s34, 8192\n"
"v_mfma_f32_16x16x32_bf16 a[4:7], a[44:47], a[156:159], a[4:7]\n"
"v_mfma_f32_16x16x32_bf16 a[0:3], a[36:39], a[156:159], a[0:3]\n"
"v_mfma_f32_16x16x32_bf16 a[0:3], a[32:35], a[152:155], a[0:3]\n"
"s_waitcnt vmcnt(16)\n"
"v_mfma_f32_16x16x32_bf16 v[88:91], a[48:51], a[96:99], v[88:91]\n"
"v_mfma_f32_16x16x32_bf16 v[88:91], a[52:55], a[100:103], v[88:91]\n"
"global_load_dwordx4 a[32:35], v0, s[34:35] offset:-4096 sc1\n"
"v_mfma_f32_16x16x32_bf16 v[116:119], a[60:63], a[100:103], v[116:119]\n"
"v_mfma_f32_16x16x32_bf16 v[116:119], a[56:59], a[96:99], v[116:119]\n"
"buffer_load_dwordx4 v1, s[48:51], s69 offen offset:1024 sc1 lds\n"
"v_mfma_f32_16x16x32_bf16 v[120:123], a[56:59], a[104:107], v[120:123]\n"
"v_mfma_f32_16x16x32_bf16 v[120:123], a[60:63], a[108:111], v[120:123]\n"
"v_mfma_f32_16x16x32_bf16 v[92:95], a[52:55], a[108:111], v[92:95]\n"
"v_mfma_f32_16x16x32_bf16 v[92:95], a[48:51], a[104:107], v[92:95]\n"
"v_mfma_f32_16x16x32_bf16 v[96:99], a[48:51], a[112:115], v[96:99]\n"
"v_mfma_f32_16x16x32_bf16 v[96:99], a[52:55], a[116:119], v[96:99]\n"
"global_load_dwordx4 a[36:39], v0, s[34:35] offset:-3072 sc1\n"
"v_mfma_f32_16x16x32_bf16 v[124:127], a[60:63], a[116:119], v[124:127]\n"
"v_mfma_f32_16x16x32_bf16 v[124:127], a[56:59], a[112:115], v[124:127]\n"
"v_mfma_f32_16x16x32_bf16 v[128:131], a[56:59], a[120:123], v[128:131]\n"
"v_mfma_f32_16x16x32_bf16 v[128:131], a[60:63], a[124:127], v[128:131]\n"
"v_mfma_f32_16x16x32_bf16 v[100:103], a[52:55], a[124:127], v[100:103]\n"
"v_mfma_f32_16x16x32_bf16 v[100:103], a[48:51], a[120:123], v[100:103]\n"
"v_mfma_f32_16x16x32_bf16 v[104:107], a[48:51], a[128:131], v[104:107]\n"
"v_mfma_f32_16x16x32_bf16 v[104:107], a[52:55], a[132:135], v[104:107]\n"
"global_load_dwordx4 a[40:43], v0, s[34:35] offset:-2048 sc1\n"
"v_mfma_f32_16x16x32_bf16 v[132:135], a[60:63], a[132:135], v[132:135]\n"
"v_mfma_f32_16x16x32_bf16 v[132:135], a[56:59], a[128:131], v[132:135]\n"
"buffer_load_dwordx4 v1, s[48:51], s69 offen offset:2048 sc1 lds\n"
"v_mfma_f32_16x16x32_bf16 v[136:139], a[56:59], a[136:139], v[136:139]\n"
"v_mfma_f32_16x16x32_bf16 v[136:139], a[60:63], a[140:143], v[136:139]\n"
"v_mfma_f32_16x16x32_bf16 v[108:111], a[52:55], a[140:143], v[108:111]\n"
"v_mfma_f32_16x16x32_bf16 v[108:111], a[48:51], a[136:139], v[108:111]\n"
"v_mfma_f32_16x16x32_bf16 v[112:115], a[48:51], a[144:147], v[112:115]\n"
"v_mfma_f32_16x16x32_bf16 v[112:115], a[52:55], a[148:151], v[112:115]\n"
"global_load_dwordx4 a[44:47], v0, s[34:35] offset:-1024 sc1\n"
"v_mfma_f32_16x16x32_bf16 v[140:143], a[60:63], a[148:151], v[140:143]\n"
"v_mfma_f32_16x16x32_bf16 v[140:143], a[56:59], a[144:147], v[140:143]\n"
"v_mfma_f32_16x16x32_bf16 a[12:15], a[56:59], a[152:155], a[12:15]\n"
"v_mfma_f32_16x16x32_bf16 a[12:15], a[60:63], a[156:159], a[12:15]\n"
"v_mfma_f32_16x16x32_bf16 a[8:11], a[52:55], a[156:159], a[8:11]\n"
"v_mfma_f32_16x16x32_bf16 a[8:11], a[48:51], a[152:155], a[8:11]\n"
"s_waitcnt vmcnt(16)\n"
"v_mfma_f32_16x16x32_bf16 v[144:147], a[64:67], a[96:99], v[144:147]\n"
"v_mfma_f32_16x16x32_bf16 v[144:147], a[68:71], a[100:103], v[144:147]\n"
"global_load_dwordx4 a[48:51], v0, s[34:35] offset:0 sc1\n"
"v_mfma_f32_16x16x32_bf16 v[172:175], a[76:79], a[100:103], v[172:175]\n"
"v_mfma_f32_16x16x32_bf16 v[172:175], a[72:75], a[96:99], v[172:175]\n"
"buffer_load_dwordx4 v1, s[48:51], s69 offen offset:3072 sc1 lds\n"
"v_mfma_f32_16x16x32_bf16 v[176:179], a[72:75], a[104:107], v[176:179]\n"
"v_mfma_f32_16x16x32_bf16 v[176:179], a[76:79], a[108:111], v[176:179]\n"
"v_mfma_f32_16x16x32_bf16 v[148:151], a[68:71], a[108:111], v[148:151]\n"
"v_mfma_f32_16x16x32_bf16 v[148:151], a[64:67], a[104:107], v[148:151]\n"
"v_mfma_f32_16x16x32_bf16 v[152:155], a[64:67], a[112:115], v[152:155]\n"
"v_mfma_f32_16x16x32_bf16 v[152:155], a[68:71], a[116:119], v[152:155]\n"
"global_load_dwordx4 a[52:55], v0, s[34:35] offset:1024 sc1\n"
"v_mfma_f32_16x16x32_bf16 v[180:183], a[76:79], a[116:119], v[180:183]\n"
"v_mfma_f32_16x16x32_bf16 v[180:183], a[72:75], a[112:115], v[180:183]\n"
"v_mfma_f32_16x16x32_bf16 v[184:187], a[72:75], a[120:123], v[184:187]\n"
"v_mfma_f32_16x16x32_bf16 v[184:187], a[76:79], a[124:127], v[184:187]\n"
"v_mfma_f32_16x16x32_bf16 v[156:159], a[68:71], a[124:127], v[156:159]\n"
"v_mfma_f32_16x16x32_bf16 v[156:159], a[64:67], a[120:123], v[156:159]\n"
"v_mfma_f32_16x16x32_bf16 v[160:163], a[64:67], a[128:131], v[160:163]\n"
"v_mfma_f32_16x16x32_bf16 v[160:163], a[68:71], a[132:135], v[160:163]\n"
"global_load_dwordx4 a[56:59], v0, s[34:35] offset:2048 sc1\n"
"v_mfma_f32_16x16x32_bf16 v[188:191], a[76:79], a[132:135], v[188:191]\n"
"v_mfma_f32_16x16x32_bf16 v[188:191], a[72:75], a[128:131], v[188:191]\n"
"v_mfma_f32_16x16x32_bf16 v[192:195], a[72:75], a[136:139], v[192:195]\n"
"s_cmp_lg_u32 s29, 0\n"
"s_cselect_b32 s60, s31, s60\n"
"s_cselect_b32 s30, 0, s30\n"
"v_mfma_f32_16x16x32_bf16 v[192:195], a[76:79], a[140:143], v[192:195]\n"
"v_mfma_f32_16x16x32_bf16 v[164:167], a[68:71], a[140:143], v[164:167]\n"
"s_cmp_lg_u32 s29, 0\n"
"s_cselect_b32 s31, s75, s72\n"
"v_mfma_f32_16x16x32_bf16 v[164:167], a[64:67], a[136:139], v[164:167]\n"
"s_sub_u32 s52, s52, s72\n"
"s_subb_u32 s53, s53, 0\n"
"v_mfma_f32_16x16x32_bf16 v[168:171], a[64:67], a[144:147], v[168:171]\n"
"s_add_u32 s52, s52, s31\n"
"s_addc_u32 s53, s53, 0\n"
"v_mfma_f32_16x16x32_bf16 v[168:171], a[68:71], a[148:151], v[168:171]\n"
"global_load_dwordx4 a[60:63], v0, s[34:35] offset:3072 sc1\n"
"v_mfma_f32_16x16x32_bf16 v[196:199], a[76:79], a[148:151], v[196:199]\n"
"s_mov_b32 s72, s31\n"
"v_mfma_f32_16x16x32_bf16 v[196:199], a[72:75], a[144:147], v[196:199]\n"
"v_mfma_f32_16x16x32_bf16 a[20:23], a[72:75], a[152:155], a[20:23]\n"
"s_add_u32 s34, s34, 8192\n"
"v_mfma_f32_16x16x32_bf16 a[20:23], a[76:79], a[156:159], a[20:23]\n"
"v_mfma_f32_16x16x32_bf16 a[16:19], a[68:71], a[156:159], a[16:19]\n"
"s_cmp_lg_u32 s29, 0\n"
"s_cselect_b32 s31, s69, s73\n"
"v_mfma_f32_16x16x32_bf16 a[16:19], a[64:67], a[152:155], a[16:19]\n"
"s_sub_u32 s54, s54, s73\n"
"s_subb_u32 s55, s55, 0\n"
"s_waitcnt vmcnt(12)\n"
"v_mfma_f32_16x16x32_bf16 v[200:203], a[80:83], a[96:99], v[200:203]\n"
"s_add_u32 s54, s54, s31\n"
"s_addc_u32 s55, s55, 0\n"
"v_mfma_f32_16x16x32_bf16 v[200:203], a[84:87], a[100:103], v[200:203]\n"
"global_load_dwordx4 a[64:67], v0, s[34:35] offset:-4096 sc1\n"
"v_mfma_f32_16x16x32_bf16 v[228:231], a[92:95], a[100:103], v[228:231]\n"
"s_mov_b32 s73, s31\n"
"ds_read_b128 a[100:103], v5 offset:1024\n"
"v_mfma_f32_16x16x32_bf16 v[228:231], a[88:91], a[96:99], v[228:231]\n"
"ds_read_b128 a[96:99], v5 offset:0\n"
"v_mfma_f32_16x16x32_bf16 v[232:235], a[88:91], a[104:107], v[232:235]\n"
"v_mfma_f32_16x16x32_bf16 v[232:235], a[92:95], a[108:111], v[232:235]\n"
"v_mfma_f32_16x16x32_bf16 v[204:207], a[84:87], a[108:111], v[204:207]\n"
"ds_read_b128 a[108:111], v5 offset:3072\n"
"v_mfma_f32_16x16x32_bf16 v[204:207], a[80:83], a[104:107], v[204:207]\n"
"ds_read_b128 a[104:107], v5 offset:2048\n"
"v_mfma_f32_16x16x32_bf16 v[208:211], a[80:83], a[112:115], v[208:211]\n"
"v_mfma_f32_16x16x32_bf16 v[208:211], a[84:87], a[116:119], v[208:211]\n"
"global_load_dwordx4 a[68:71], v0, s[34:35] offset:-3072 sc1\n"
"v_mfma_f32_16x16x32_bf16 v[236:239], a[92:95], a[116:119], v[236:239]\n"
"ds_read_b128 a[116:119], v5 offset:5120\n"
"v_mfma_f32_16x16x32_bf16 v[236:239], a[88:91], a[112:115], v[236:239]\n"
"ds_read_b128 a[112:115], v5 offset:4096\n"
"v_mfma_f32_16x16x32_bf16 v[240:243], a[88:91], a[120:123], v[240:243]\n"
"v_mfma_f32_16x16x32_bf16 v[240:243], a[92:95], a[124:127], v[240:243]\n"
"v_mfma_f32_16x16x32_bf16 v[212:215], a[84:87], a[124:127], v[212:215]\n"
"ds_read_b128 a[124:127], v5 offset:7168\n"
"v_mfma_f32_16x16x32_bf16 v[212:215], a[80:83], a[120:123], v[212:215]\n"
"ds_read_b128 a[120:123], v5 offset:6144\n"
"v_mfma_f32_16x16x32_bf16 v[216:219], a[80:83], a[128:131], v[216:219]\n"
"v_mfma_f32_16x16x32_bf16 v[216:219], a[84:87], a[132:135], v[216:219]\n"
"global_load_dwordx4 a[72:75], v0, s[34:35] offset:-2048 sc1\n"
"v_mfma_f32_16x16x32_bf16 v[244:247], a[92:95], a[132:135], v[244:247]\n"
"ds_read_b128 a[132:135], v5 offset:9216\n"
"v_mfma_f32_16x16x32_bf16 v[244:247], a[88:91], a[128:131], v[244:247]\n"
"ds_read_b128 a[128:131], v5 offset:8192\n"
"v_mfma_f32_16x16x32_bf16 v[248:251], a[88:91], a[136:139], v[248:251]\n"
"v_mfma_f32_16x16x32_bf16 v[248:251], a[92:95], a[140:143], v[248:251]\n"
"v_mfma_f32_16x16x32_bf16 v[220:223], a[84:87], a[140:143], v[220:223]\n"
"ds_read_b128 a[140:143], v5 offset:11264\n"
"v_mfma_f32_16x16x32_bf16 v[220:223], a[80:83], a[136:139], v[220:223]\n"
"ds_read_b128 a[136:139], v5 offset:10240\n"
"v_mfma_f32_16x16x32_bf16 v[224:227], a[80:83], a[144:147], v[224:227]\n"
"v_mfma_f32_16x16x32_bf16 v[224:227], a[84:87], a[148:151], v[224:227]\n"
"global_load_dwordx4 a[76:79], v0, s[34:35] offset:-1024 sc1\n"
"v_mfma_f32_16x16x32_bf16 v[252:255], a[92:95], a[148:151], v[252:255]\n"
"ds_read_b128 a[148:151], v5 offset:13312\n"
"v_mfma_f32_16x16x32_bf16 v[252:255], a[88:91], a[144:147], v[252:255]\n"
"ds_read_b128 a[144:147], v5 offset:12288\n"
"v_mfma_f32_16x16x32_bf16 a[28:31], a[88:91], a[152:155], a[28:31]\n"
"v_mfma_f32_16x16x32_bf16 a[28:31], a[92:95], a[156:159], a[28:31]\n"
"v_mfma_f32_16x16x32_bf16 a[24:27], a[84:87], a[156:159], a[24:27]\n"
"ds_read_b128 a[156:159], v5 offset:15360\n"
"v_mfma_f32_16x16x32_bf16 a[24:27], a[80:83], a[152:155], a[24:27]\n"
"s_waitcnt vmcnt(9) lgkmcnt(11)\n"
"ds_read_b128 a[152:155], v5 offset:14336\n"
"v_mfma_f32_16x16x32_bf16 v[32:35], a[32:35], a[96:99], v[32:35]\n"
"v_mfma_f32_16x16x32_bf16 v[32:35], a[36:39], a[100:103], v[32:35]\n"
"global_load_dwordx4 a[80:83], v0, s[34:35] offset:0 sc1\n"
"v_mfma_f32_16x16x32_bf16 v[60:63], a[44:47], a[100:103], v[60:63]\n"
"v_mfma_f32_16x16x32_bf16 v[60:63], a[40:43], a[96:99], v[60:63]\n"
"v_mfma_f32_16x16x32_bf16 v[64:67], a[40:43], a[104:107], v[64:67]\n"
"v_mfma_f32_16x16x32_bf16 v[64:67], a[44:47], a[108:111], v[64:67]\n"
"v_mfma_f32_16x16x32_bf16 v[36:39], a[36:39], a[108:111], v[36:39]\n"
"s_add_u32 s67, s78, 2\n"
"v_mfma_f32_16x16x32_bf16 v[36:39], a[32:35], a[104:107], v[36:39]\n"
"s_waitcnt lgkmcnt(8)\n"
"v_mfma_f32_16x16x32_bf16 v[40:43], a[32:35], a[112:115], v[40:43]\n"
"v_mfma_f32_16x16x32_bf16 v[40:43], a[36:39], a[116:119], v[40:43]\n"
"global_load_dwordx4 a[84:87], v0, s[34:35] offset:1024 sc1\n"
"v_mfma_f32_16x16x32_bf16 v[68:71], a[44:47], a[116:119], v[68:71]\n"
"v_mfma_f32_16x16x32_bf16 v[68:71], a[40:43], a[112:115], v[68:71]\n"
"v_mfma_f32_16x16x32_bf16 v[72:75], a[40:43], a[120:123], v[72:75]\n"
"v_mfma_f32_16x16x32_bf16 v[72:75], a[44:47], a[124:127], v[72:75]\n"
"v_mfma_f32_16x16x32_bf16 v[44:47], a[36:39], a[124:127], v[44:47]\n"
"v_mfma_f32_16x16x32_bf16 v[44:47], a[32:35], a[120:123], v[44:47]\n"
"s_waitcnt lgkmcnt(4)\n"
"v_mfma_f32_16x16x32_bf16 v[48:51], a[32:35], a[128:131], v[48:51]\n"
"s_lshr_b32 s66, 20, s62\n"
"v_mfma_f32_16x16x32_bf16 v[48:51], a[36:39], a[132:135], v[48:51]\n"
"global_load_dwordx4 a[88:91], v0, s[34:35] offset:2048 sc1\n"
"v_mfma_f32_16x16x32_bf16 v[76:79], a[44:47], a[132:135], v[76:79]\n"
"s_and_b32 s66, s66, 1\n"
"s_cselect_b32 s70, 4294574080, 0\n"
"s_cselect_b32 s71, -1, 0\n"
"s_cselect_b32 s66, 0, 131072\n"
"v_mfma_f32_16x16x32_bf16 v[76:79], a[40:43], a[128:131], v[76:79]\n"
"v_mfma_f32_16x16x32_bf16 v[80:83], a[40:43], a[136:139], v[80:83]\n"
"s_add_u32 s70, s70, s66\n"
"v_subrev_u32 v12, s67, v12\n"
"v_mfma_f32_16x16x32_bf16 v[80:83], a[44:47], a[140:143], v[80:83]\n"
"v_xor_b32 v13, s79, v13\n"
"v_mfma_f32_16x16x32_bf16 v[52:55], a[36:39], a[140:143], v[52:55]\n"
"v_subrev_u32 v14, s67, v14\n"
"v_mfma_f32_16x16x32_bf16 v[52:55], a[32:35], a[136:139], v[52:55]\n"
"s_waitcnt lgkmcnt(0)\n"
"v_xor_b32 v15, s79, v15\n"
"v_mfma_f32_16x16x32_bf16 v[56:59], a[32:35], a[144:147], v[56:59]\n"
"v_or_b32 v12, v12, v14\n"
"v_mfma_f32_16x16x32_bf16 v[56:59], a[36:39], a[148:151], v[56:59]\n"
"global_load_dwordx4 a[92:95], v0, s[34:35] offset:3072 sc1\n"
"v_or_b32 v13, v13, v15\n"
"v_mfma_f32_16x16x32_bf16 v[84:87], a[44:47], a[148:151], v[84:87]\n"
"v_or_b32 v12, v12, v13\n"
"v_mfma_f32_16x16x32_bf16 v[84:87], a[40:43], a[144:147], v[84:87]\n"
"v_cmp_lt_u32 vcc, 15, v12\n"
"v_mfma_f32_16x16x32_bf16 a[4:7], a[40:43], a[152:155], a[4:7]\n"
"s_add_u32 s34, s34, 8192\n"
"s_cbranch_vccnz L_slow2\n"
"L_back2:\n"
"v_mfma_f32_16x16x32_bf16 a[4:7], a[44:47], a[156:159], a[4:7]\n"
"v_mfma_f32_16x16x32_bf16 a[0:3], a[36:39], a[156:159], a[0:3]\n"
"v_mfma_f32_16x16x32_bf16 a[0:3], a[32:35], a[152:155], a[0:3]\n"
"s_waitcnt vmcnt(8)\n"
"v_mfma_f32_16x16x32_bf16 v[88:91], a[48:51], a[96:99], v[88:91]\n"
"v_mfma_f32_16x16x32_bf16 v[88:91], a[52:55], a[100:103], v[88:91]\n"
"global_load_dwordx4 a[32:35], v0, s[34:35] offset:-4096 sc1\n"
"v_mfma_f32_16x16x32_bf16 v[116:119], a[60:63], a[100:103], v[116:119]\n"
"v_mfma_f32_16x16x32_bf16 v[116:119], a[56:59], a[96:99], v[116:119]\n"
"v_mfma_f32_16x16x32_bf16 v[120:123], a[56:59], a[104:107], v[120:123]\n"
"v_mfma_f32_16x16x32_bf16 v[120:123], a[60:63], a[108:111], v[120:123]\n"
"v_mfma_f32_16x16x32_bf16 v[92:95], a[52:55], a[108:111], v[92:95]\n"
"v_mfma_f32_16x16x32_bf16 v[92:95], a[48:51], a[104:107], v[92:95]\n"
"v_mfma_f32_16x16x32_bf16 v[96:99], a[48:51], a[112:115], v[96:99]\n"
"v_mfma_f32_16x16x32_bf16 v[96:99], a[52:55], a[116:119], v[96:99]\n"
"global_load_dwordx4 a[36:39], v0, s[34:35] offset:-3072 sc1\n"
"v_mfma_f32_16x16x32_bf16 v[124:127], a[60:63], a[116:119], v[124:127]\n"
"v_mfma_f32_16x16x32_bf16 v[124:127], a[56:59], a[112:115], v[124:127]\n"
"v_mfma_f32_16x16x32_bf16 v[128:131], a[56:59], a[120:123], v[128:131]\n"
"v_mfma_f32_16x16x32_bf16 v[128:131], a[60:63], a[124:127], v[128:131]\n"
"v_mfma_f32_16x16x32_bf16 v[100:103], a[52:55], a[124:127], v[100:103]\n"
"v_mfma_f32_16x16x32_bf16 v[100:103], a[48:51], a[120:123], v[100:103]\n"
"v_mfma_f32_16x16x32_bf16 v[104:107], a[48:51], a[128:131], v[104:107]\n"
"v_mfma_f32_16x16x32_bf16 v[104:107], a[52:55], a[132:135], v[104:107]\n"
"global_load_dwordx4 a[40:43], v0, s[34:35] offset:-2048 sc1\n"
"v_mfma_f32_16x16x32_bf16 v[132:135], a[60:63], a[132:135], v[132:135]\n"
"v_mfma_f32_16x16x32_bf16 v[132:135], a[56:59], a[128:131], v[132:135]\n"
"v_mfma_f32_16x16x32_bf16 v[136:139], a[56:59], a[136:139], v[136:139]\n"
"v_mfma_f32_16x16x32_bf16 v[136:139], a[60:63], a[140:143], v[136:139]\n"
"v_mfma_f32_16x16x32_bf16 v[108:111], a[52:55], a[140:143], v[108:111]\n"
"v_mfma_f32_16x16x32_bf16 v[108:111], a[48:51], a[136:139], v[108:111]\n"
"v_mfma_f32_16x16x32_bf16 v[112:115], a[48:51], a[144:147], v[112:115]\n"
"v_mfma_f32_16x16x32_bf16 v[112:115], a[52:55], a[148:151], v[112:115]\n"
"global_load_dwordx4 a[44:47], v0, s[34:35] offset:-1024 sc1\n"
"v_mfma_f32_16x16x32_bf16 v[140:143], a[60:63], a[148:151], v[140:143]\n"
"v_mfma_f32_16x16x32_bf16 v[140:143], a[56:59], a[144:147], v[140:143]\n"
"v_mfma_f32_16x16x32_bf16 a[12:15], a[56:59], a[152:155], a[12:15]\n"
"v_mfma_f32_16x16x32_bf16 a[12:15], a[60:63], a[156:159], a[12:15]\n"
"v_mfma_f32_16x16x32_bf16 a[8:11], a[52:55], a[156:159], a[8:11]\n"
"v_mfma_f32_16x16x32_bf16 a[8:11], a[48:51], a[152:155], a[8:11]\n"
"s_waitcnt vmcnt(8)\n"
"v_mfma_f32_16x16x32_bf16 v[144:147], a[64:67], a[96:99], v[144:147]\n"
"v_mfma_f32_16x16x32_bf16 v[144:147], a[68:71], a[100:103], v[144:147]\n"
"global_load_dwordx4 a[48:51], v0, s[34:35] offset:0 sc1\n"
"v_mfma_f32_16x16x32_bf16 v[172:175], a[76:79], a[100:103], v[172:175]\n"
"v_mfma_f32_16x16x32_bf16 v[172:175], a[72:75], a[96:99], v[172:175]\n"
"v_mfma_f32_16x16x32_bf16 v[176:179], a[72:75], a[104:107], v[176:179]\n"
"v_mfma_f32_16x16x32_bf16 v[176:179], a[76:79], a[108:111], v[176:179]\n"
"v_mfma_f32_16x16x32_bf16 v[148:151], a[68:71], a[108:111], v[148:151]\n"
"v_mfma_f32_16x16x32_bf16 v[148:151], a[64:67], a[104:107], v[148:151]\n"
"v_mfma_f32_16x16x32_bf16 v[152:155], a[64:67], a[112:115], v[152:155]\n"
"v_mfma_f32_16x16x32_bf16 v[152:155], a[68:71], a[116:119], v[152:155]\n"
"global_load_dwordx4 a[52:55], v0, s[34:35] offset:1024 sc1\n"
"v_mfma_f32_16x16x32_bf16 v[180:183], a[76:79], a[116:119], v[180:183]\n"
"v_mfma_f32_16x16x32_bf16 v[180:183], a[72:75], a[112:115], v[180:183]\n"
"v_mfma_f32_16x16x32_bf16 v[184:187], a[72:75], a[120:123], v[184:187]\n"
"v_mfma_f32_16x16x32_bf16 v[184:187], a[76:79], a[124:127], v[184:187]\n"
"v_mfma_f32_16x16x32_bf16 v[156:159], a[68:71], a[124:127], v[156:159]\n"
"v_mfma_f32_16x16x32_bf16 v[156:159], a[64:67], a[120:123], v[156:159]\n"
"v_mfma_f32_16x16x32_bf16 v[160:163], a[64:67], a[128:131], v[160:163]\n"
"v_mfma_f32_16x16x32_bf16 v[160:163], a[68:71], a[132:135], v[160:163]\n"
"global_load_dwordx4 a[56:59], v0, s[34:35] offset:2048 sc1\n"
"v_mfma_f32_16x16x32_bf16 v[188:191], a[76:79], a[132:135], v[188:191]\n"
"v_mfma_f32_16x16x32_bf16 v[188:191], a[72:75], a[128:131], v[188:191]\n"
"v_mfma_f32_16x16x32_bf16 v[192:195], a[72:75], a[136:139], v[192:195]\n"
"v_mfma_f32_16x16x32_bf16 v[192:195], a[76:79], a[140:143], v[192:195]\n"
"v_mfma_f32_16x16x32_bf16 v[164:167], a[68:71], a[140:143], v[164:167]\n"
"v_mfma_f32_16x16x32_bf16 v[164:167], a[64:67], a[136:139], v[164:167]\n"
"v_mfma_f32_16x16x32_bf16 v[168:171], a[64:67], a[144:147], v[168:171]\n"
"v_mfma_f32_16x16x32_bf16 v[168:171], a[68:71], a[148:151], v[168:171]\n"
"global_load_dwordx4 a[60:63], v0, s[34:35] offset:3072 sc1\n"
"v_mfma_f32_16x16x32_bf16 v[196:199], a[76:79], a[148:151], v[196:199]\n"
"v_mfma_f32_16x16x32_bf16 v[196:199], a[72:75], a[144:147], v[196:199]\n"
"v_mfma_f32_16x16x32_bf16 a[20:23], a[72:75], a[152:155], a[20:23]\n"
"s_add_u32 s34, s34, 8192\n"
"v_mfma_f32_16x16x32_bf16 a[20:23], a[76:79], a[156:159], a[20:23]\n"
"v_mfma_f32_16x16x32_bf16 a[16:19], a[68:71], a[156:159], a[16:19]\n"
"v_mfma_f32_16x16x32_bf16 a[16:19], a[64:67], a[152:155], a[16:19]\n"
"s_waitcnt vmcnt(8)\n"
"v_mfma_f32_16x16x32_bf16 v[200:203], a[80:83], a[96:99], v[200:203]\n"
"v_mfma_f32_16x16x32_bf16 v[200:203], a[84:87], a[100:103], v[200:203]\n"
"global_load_dwordx4 a[64:67], v0, s[34:35] offset:-4096 sc1\n"
"v_mfma_f32_16x16x32_bf16 v[228:231], a[92:95], a[100:103], v[228:231]\n"
"ds_read_b128 a[100:103], v2 offset:17408\n"
"v_mfma_f32_16x16x32_bf16 v[228:231], a[88:91], a[96:99], v[228:231]\n"
"ds_read_b128 a[96:99], v2 offset:16384\n"
"v_mfma_f32_16x16x32_bf16 v[232:235], a[88:91], a[104:107], v[232:235]\n"
"v_mfma_f32_16x16x32_bf16 v[232:235], a[92:95], a[108:111], v[232:235]\n"
"v_mfma_f32_16x16x32_bf16 v[204:207], a[84:87], a[108:111], v[204:207]\n"
"ds_read_b128 a[108:111], v2 offset:19456\n"
"v_mfma_f32_16x16x32_bf16 v[204:207], a[80:83], a[104:107], v[204:207]\n"
"ds_read_b128 a[104:107], v2 offset:18432\n"
"v_mfma_f32_16x16x32_bf16 v[208:211], a[80:83], a[112:115], v[208:211]\n"
"v_mfma_f32_16x16x32_bf16 v[208:211], a[84:87], a[116:119], v[208:211]\n"
"global_load_dwordx4 a[68:71], v0, s[34:35] offset:-3072 sc1\n"
"v_mfma_f32_16x16x32_bf16 v[236:239], a[92:95], a[116:119], v[236:239]\n"
"ds_read_b128 a[116:119], v2 offset:21504\n"
"v_mfma_f32_16x16x32_bf16 v[236:239], a[88:91], a[112:115], v[236:239]\n"
"ds_read_b128 a[112:115], v2 offset:20480\n"
"v_mfma_f32_16x16x32_bf16 v[240:243], a[88:91], a[120:123], v[240:243]\n"
"v_mfma_f32_16x16x32_bf16 v[240:243], a[92:95], a[124:127], v[240:243]\n"
"v_mfma_f32_16x16x32_bf16 v[212:215], a[84:87], a[124:127], v[212:215]\n"
"ds_read_b128 a[124:127], v2 offset:23552\n"
"v_mfma_f32_16x16x32_bf16 v[212:215], a[80:83], a[120:123], v[212:215]\n"
"ds_read_b128 a[120:123], v2 offset:22528\n"
"v_mfma_f32_16x16x32_bf16 v[216:219], a[80:83], a[128:131], v[216:219]\n"
"v_mfma_f32_16x16x32_bf16 v[216:219], a[84:87], a[132:135], v[216:219]\n"
"global_load_dwordx4 a[72:75], v0, s[34:35] offset:-2048 sc1\n"
"v_mfma_f32_16x16x32_bf16 v[244:247], a[92:95], a[132:135], v[244:247]\n"
"ds_read_b128 a[132:135], v2 offset:25600\n"
"v_mfma_f32_16x16x32_bf16 v[244:247], a[88:91], a[128:131], v[244:247]\n"
"ds_read_b128 a[128:131], v2 offset:24576\n"
"v_mfma_f32_16x16x32_bf16 v[248:251], a[88:91], a[136:139], v[248:251]\n"
"v_mfma_f32_16x16x32_bf16 v[248:251], a[92:95], a[140:143], v[248:251]\n"
"v_mfma_f32_16x16x32_bf16 v[220:223], a[84:87], a[140:143], v[220:223]\n"
"ds_read_b128 a[140:143], v2 offset:27648\n"
"v_mfma_f32_16x16x32_bf16 v[220:223], a[80:83], a[136:139], v[220:223]\n"
"ds_read_b128 a[136:139], v2 offset:26624\n"
"v_mfma_f32_16x16x32_bf16 v[224:227], a[80:83], a[144:147], v[224:227]\n"
"v_mfma_f32_16x16x32_bf16 v[224:227], a[84:87], a[148:151], v[224:227]\n"
"global_load_dwordx4 a[76:79], v0, s[34:35] offset:-1024 sc1\n"
"v_mfma_f32_16x16x32_bf16 v[252:255], a[92:95], a[148:151], v[252:255]\n"
"ds_read_b128 a[148:151], v2 offset:29696\n"
"v_mfma_f32_16x16x32_bf16 v[252:255], a[88:91], a[144:147], v[252:255]\n"
"ds_read_b128 a[144:147], v2 offset:28672\n"
"v_mfma_f32_16x16x32_bf16 a[28:31], a[88:91], a[152:155], a[28:31]\n"
"v_mfma_f32_16x16x32_bf16 a[28:31], a[92:95], a[156:159], a[28:31]\n"
"v_mfma_f32_16x16x32_bf16 a[24:27], a[84:87], a[156:159], a[24:27]\n"
"ds_read_b128 a[156:159], v2 offset:31744\n"
"v_mfma_f32_16x16x32_bf16 a[24:27], a[80:83], a[152:155], a[24:27]\n"
"ds_read_b128 a[152:155], v2 offset:30720\n"
"L_P5:\n"
"s_waitcnt vmcnt(8) lgkmcnt(12)\n"
"v_mfma_f32_16x16x32_bf16 v[32:35], a[32:35], a[96:99], v[32:35]\n"
"v_mfma_f32_16x16x32_bf16 v[32:35], a[36:39], a[100:103], v[32:35]\n"
"global_load_dwordx4 a[80:83], v0, s[34:35] offset:0 sc1\n"
"v_mfma_f32_16x16x32_bf16 v[60:63], a[44:47], a[100:103], v[60:63]\n"
"s_barrier\n"
"v_mfma_f32_16x16x32_bf16 v[60:63], a[40:43], a[96:99], v[60:63]\n"
"s_add_u32 s48, s48, s70\n"
"s_addc_u32 s49, s49, s71\n"
"v_mfma_f32_16x16x32_bf16 v[64:67], a[40:43], a[104:107], v[64:67]\n"
"v_mfma_f32_16x16x32_bf16 v[64:67], a[44:47], a[108:111], v[64:67]\n"
"v_mfma_f32_16x16x32_bf16 v[36:39], a[36:39], a[108:111], v[36:39]\n"
"s_add_u32 s52, s52, s70\n"
"s_addc_u32 s53, s53, s71\n"
"v_mfma_f32_16x16x32_bf16 v[36:39], a[32:35], a[104:107], v[36:39]\n"
"s_waitcnt lgkmcnt(8)\n"
"v_mfma_f32_16x16x32_bf16 v[40:43], a[32:35], a[112:115], v[40:43]\n"
"s_add_u32 s54, s54, s70\n"
"s_addc_u32 s55, s55, s71\n"
"v_mfma_f32_16x16x32_bf16 v[40:43], a[36:39], a[116:119], v[40:43]\n"
"global_load_dwordx4 a[84:87], v0, s[34:35] offset:1024 sc1\n"
"v_mfma_f32_16x16x32_bf16 v[68:71], a[44:47], a[116:119], v[68:71]\n"
"v_mfma_f32_16x16x32_bf16 v[68:71], a[40:43], a[112:115], v[68:71]\n"
"v_mfma_f32_16x16x32_bf16 v[72:75], a[40:43], a[120:123], v[72:75]\n"
"v_mfma_f32_16x16x32_bf16 v[72:75], a[44:47], a[124:127], v[72:75]\n"
"s_add_u32 s61, s61, 2048\n"
"v_mfma_f32_16x16x32_bf16 v[44:47], a[36:39], a[124:127], v[44:47]\n"
"v_mfma_f32_16x16x32_bf16 v[44:47], a[32:35], a[120:123], v[44:47]\n"
"s_waitcnt lgkmcnt(4)\n"
"v_mfma_f32_16x16x32_bf16 v[48:51], a[32:35], a[128:131], v[48:51]\n"
"v_lshlrev_b32 v16, 16, v18\n"
"v_mfma_f32_16x16x32_bf16 v[48:51], a[36:39], a[132:135], v[48:51]\n"
"global_load_dwordx4 a[88:91], v0, s[34:35] offset:2048 sc1\n"
"v_and_b32 v17, s63, v18\n"
"v_mfma_f32_16x16x32_bf16 v[76:79], a[44:47], a[132:135], v[76:79]\n"
"v_lshlrev_b32 v18, 16, v19\n"
"v_mfma_f32_16x16x32_bf16 v[76:79], a[40:43], a[128:131], v[76:79]\n"
"v_and_b32 v19, s63, v19\n"
"v_mfma_f32_16x16x32_bf16 v[80:83], a[40:43], a[136:139], v[80:83]\n"
"v_add_f32 v144, v144, v16\n"
"v_add_f32 v145, v145, v17\n"
"v_mfma_f32_16x16x32_bf16 v[80:83], a[44:47], a[140:143], v[80:83]\n"
"v_add_f32 v146, v146, v18\n"
"v_mfma_f32_16x16x32_bf16 v[52:55], a[36:39], a[140:143], v[52:55]\n"
"v_add_f32 v147, v147, v19\n"
"v_mfma_f32_16x16x32_bf16 v[52:55], a[32:35], a[136:139], v[52:55]\n"
"s_waitcnt lgkmcnt(0)\n"
"v_add_f32 v148, v148, v16\n"
"v_mfma_f32_16x16x32_bf16 v[56:59], a[32:35], a[144:147], v[56:59]\n"
"v_add_f32 v149, v149, v17\n"
"v_mfma_f32_16x16x32_bf16 v[56:59], a[36:39], a[148:151], v[56:59]\n"
"global_load_dwordx4 a[92:95], v0, s[34:35] offset:3072 sc1\n"
"v_add_f32 v150, v150, v18\n"
"v_mfma_f32_16x16x32_bf16 v[84:87], a[44:47], a[148:151], v[84:87]\n"
"v_add_f32 v151, v151, v19\n"
"v_mfma_f32_16x16x32_bf16 v[84:87], a[40:43], a[144:147], v[84:87]\n"
"v_add_f32 v152, v152, v16\n"
"v_mfma_f32_16x16x32_bf16 a[4:7], a[40:43], a[152:155], a[4:7]\n"
"s_add_u32 s34, s34, 8192\n"
"v_add_f32 v153, v153, v17\n"
"v_mfma_f32_16x16x32_bf16 a[4:7], a[44:47], a[156:159], a[4:7]\n"
"v_add_f32 v154, v154, v18\n"
"v_mfma_f32_16x16x32_bf16 a[0:3], a[36:39], a[156:159], a[0:3]\n"
"v_add_f32 v155, v155, v19\n"
"v_add_f32 v156, v156, v16\n"
"v_mfma_f32_16x16x32_bf16 a[0:3], a[32:35], a[152:155], a[0:3]\n"
"s_waitcnt vmcnt(8)\n"
"v_add_f32 v157, v157, v17\n"
"v_mfma_f32_16x16x32_bf16 v[88:91], a[48:51], a[96:99], v[88:91]\n"
"v_add_f32 v158, v158, v18\n"
"v_mfma_f32_16x16x32_bf16 v[88:91], a[52:55], a[100:103], v[88:91]\n"
"global_load_dwordx4 a[32:35], v0, s[34:35] offset:-4096 sc1\n"
"v_add_f32 v159, v159, v19\n"
"v_mfma_f32_16x16x32_bf16 v[116:119], a[60:63], a[100:103], v[116:119]\n"
"ds_read_b128 a[100:103], v3 offset:17408\n"
"v_add_f32 v160, v160, v16\n"
"v_mfma_f32_16x16x32_bf16 v[116:119], a[56:59], a[96:99], v[116:119]\n"
"ds_read_b128 a[96:99], v3 offset:16384\n"
"v_add_f32 v161, v161, v17\n"
"v_mfma_f32_16x16x32_bf16 v[120:123], a[56:59], a[104:107], v[120:123]\n"
"v_add_f32 v162, v162, v18\n"
"v_mfma_f32_16x16x32_bf16 v[120:123], a[60:63], a[108:111], v[120:123]\n"
"v_add_f32 v163, v163, v19\n"
"v_mfma_f32_16x16x32_bf16 v[92:95], a[52:55], a[108:111], v[92:95]\n"
"ds_read_b128 a[108:111], v3 offset:19456\n"
"v_add_f32 v164, v164, v16\n"
"v_mfma_f32_16x16x32_bf16 v[92:95], a[48:51], a[104:107], v[92:95]\n"
"ds_read_b128 a[104:107], v3 offset:18432\n"
"v_add_f32 v165, v165, v17\n"
"v_mfma_f32_16x16x32_bf16 v[96:99], a[48:51], a[112:115], v[96:99]\n"
"v_add_f32 v166, v166, v18\n"
"v_add_f32 v167, v167, v19\n"
"v_mfma_f32_16x16x32_bf16 v[96:99], a[52:55], a[116:119], v[96:99]\n"
"global_load_dwordx4 a[36:39], v0, s[34:35] offset:-3072 sc1\n"
"v_add_f32 v168, v168, v16\n"
"v_mfma_f32_16x16x32_bf16 v[124:127], a[60:63], a[116:119], v[124:127]\n"
"ds_read_b128 a[116:119], v3 offset:21504\n"
"v_add_f32 v169, v169, v17\n"
"v_mfma_f32_16x16x32_bf16 v[124:127], a[56:59], a[112:115], v[124:127]\n"
"ds_read_b128 a[112:115], v3 offset:20480\n"
"v_add_f32 v170, v170, v18\n"
"v_mfma_f32_16x16x32_bf16 v[128:131], a[56:59], a[120:123], v[128:131]\n"
"v_add_f32 v171, v171, v19\n"
"v_mfma_f32_16x16x32_bf16 v[128:131], a[60:63], a[124:127], v[128:131]\n"
"v_accvgpr_read_b32 v12, a16\n"
"v_mfma_f32_16x16x32_bf16 v[100:103], a[52:55], a[124:127], v[100:103]\n"
"ds_read_b128 a[124:127], v3 offset:23552\n"
"v_accvgpr_read_b32 v13, a17\n"
"v_mfma_f32_16x16x32_bf16 v[100:103], a[48:51], a[120:123], v[100:103]\n"
"ds_read_b128 a[120:123], v3 offset:22528\n"
"v_accvgpr_read_b32 v14, a18\n"
"v_mfma_f32_16x16x32_bf16 v[104:107], a[48:51], a[128:131], v[104:107]\n"
"v_accvgpr_read_b32 v15, a19\n"
"v_add_f32 v12, v12, v16\n"
"v_mfma_f32_16x16x32_bf16 v[104:107], a[52:55], a[132:135], v[104:107]\n"
"global_load_dwordx4 a[40:43], v0, s[34:35] offset:-2048 sc1\n"
"v_add_f32 v13, v13, v17\n"
"v_mfma_f32_16x16x32_bf16 v[132:135], a[60:63], a[132:135], v[132:135]\n"
"ds_read_b128 a[132:135], v3 offset:25600\n"
"v_add_f32 v14, v14, v18\n"
"v_mfma_f32_16x16x32_bf16 v[132:135], a[56:59], a[128:131], v[132:135]\n"
"ds_read_b128 a[128:131], v3 offset:24576\n"
"v_add_f32 v15, v15, v19\n"
"v_mfma_f32_16x16x32_bf16 v[136:139], a[56:59], a[136:139], v[136:139]\n"
"v_accvgpr_write_b32 a16, v12\n"
"v_mfma_f32_16x16x32_bf16 v[136:139], a[60:63], a[140:143], v[136:139]\n"
"v_accvgpr_write_b32 a17, v13\n"
"v_mfma_f32_16x16x32_bf16 v[108:111], a[52:55], a[140:143], v[108:111]\n"
"ds_read_b128 a[140:143], v3 offset:27648\n"
"v_accvgpr_write_b32 a18, v14\n"
"v_mfma_f32_16x16x32_bf16 v[108:111], a[48:51], a[136:139], v[108:111]\n"
"ds_read_b128 a[136:139], v3 offset:26624\n"
"v_accvgpr_write_b32 a19, v15\n"
"v_mfma_f32_16x16x32_bf16 v[112:115], a[48:51], a[144:147], v[112:115]\n"
"v_lshlrev_b32 v20, 16, v22\n"
"v_and_b32 v21, s63, v22\n"
"v_mfma_f32_16x16x32_bf16 v[112:115], a[52:55], a[148:151], v[112:115]\n"
"global_load_dwordx4 a[44:47], v0, s[34:35] offset:-1024 sc1\n"
"v_lshlrev_b32 v22, 16, v23\n"
"v_mfma_f32_16x16x32_bf16 v[140:143], a[60:63], a[148:151], v[140:143]\n"
"ds_read_b128 a[148:151], v3 offset:29696\n"
"v_and_b32 v23, s63, v23\n"
"v_mfma_f32_16x16x32_bf16 v[140:143], a[56:59], a[144:147], v[140:143]\n"
"ds_read_b128 a[144:147], v3 offset:28672\n"
"v_add_f32 v172, v172, v20\n"
"v_mfma_f32_16x16x32_bf16 a[12:15], a[56:59], a[152:155], a[12:15]\n"
"v_add_f32 v173, v173, v21\n"
"v_mfma_f32_16x16x32_bf16 a[12:15], a[60:63], a[156:159], a[12:15]\n"
"v_add_f32 v174, v174, v22\n"
"v_mfma_f32_16x16x32_bf16 a[8:11], a[52:55], a[156:159], a[8:11]\n"
"ds_read_b128 a[156:159], v3 offset:31744\n"
"v_add_f32 v175, v175, v23\n"
"v_mfma_f32_16x16x32_bf16 a[8:11], a[48:51], a[152:155], a[8:11]\n"
"s_waitcnt vmcnt(8) lgkmcnt(11)\n"
"ds_read_b128 a[152:155], v3 offset:30720\n"
"v_add_f32 v176, v176, v20\n"
"v_mfma_f32_16x16x32_bf16 v[32:35], a[64:67], a[96:99], v[32:35]\n"
"v_add_f32 v177, v177, v21\n"
"v_mfma_f32_16x16x32_bf16 v[32:35], a[68:71], a[100:103], v[32:35]\n"
"global_load_dwordx4 a[48:51], v0, s[34:35] offset:0 sc1\n"
"v_add_f32 v178, v178, v22\n"
"v_mfma_f32_16x16x32_bf16 v[60:63], a[76:79], a[100:103], v[60:63]\n"
"v_add_f32 v179, v179, v23\n"
"v_mfma_f32_16x16x32_bf16 v[60:63], a[72:75], a[96:99], v[60:63]\n"
"v_add_f32 v180, v180, v20\n"
"v_add_f32 v181, v181, v21\n"
"v_mfma_f32_16x16x32_bf16 v[64:67], a[72:75], a[104:107], v[64:67]\n"
"v_add_f32 v182, v182, v22\n"
"v_mfma_f32_16x16x32_bf16 v[64:67], a[76:79], a[108:111], v[64:67]\n"
"v_add_f32 v183, v183, v23\n"
"v_mfma_f32_16x16x32_bf16 v[36:39], a[68:71], a[108:111], v[36:39]\n"
"v_add_f32 v184, v184, v20\n"
"v_mfma_f32_16x16x32_bf16 v[36:39], a[64:67], a[104:107], v[36:39]\n"
"s_waitcnt lgkmcnt(8)\n"
"v_add_f32 v185, v185, v21\n"
"v_mfma_f32_16x16x32_bf16 v[40:43], a[64:67], a[112:115], v[40:43]\n"
"v_add_f32 v186, v186, v22\n"
"v_mfma_f32_16x16x32_bf16 v[40:43], a[68:71], a[116:119], v[40:43]\n"
"global_load_dwordx4 a[52:55], v0, s[34:35] offset:1024 sc1\n"
"v_add_f32 v187, v187, v23\n"
"v_mfma_f32_16x16x32_bf16 v[68:71], a[76:79], a[116:119], v[68:71]\n"
"v_add_f32 v188, v188, v20\n"
"v_mfma_f32_16x16x32_bf16 v[68:71], a[72:75], a[112:115], v[68:71]\n"
"v_add_f32 v189, v189, v21\n"
"v_mfma_f32_16x16x32_bf16 v[72:75], a[72:75], a[120:123], v[72:75]\n"
"v_add_f32 v190, v190, v22\n"
"v_add_f32 v191, v191, v23\n"
"v_mfma_f32_16x16x32_bf16 v[72:75], a[76:79], a[124:127], v[72:75]\n"
"v_add_f32 v192, v192, v20\n"
"v_mfma_f32_16x16x32_bf16 v[44:47], a[68:71], a[124:127], v[44:47]\n"
"v_add_f32 v193, v193, v21\n"
"v_mfma_f32_16x16x32_bf16 v[44:47], a[64:67], a[120:123], v[44:47]\n"
"s_waitcnt lgkmcnt(4)\n"
"v_add_f32 v194, v194, v22\n"
"v_mfma_f32_16x16x32_bf16 v[48:51], a[64:67], a[128:131], v[48:51]\n"
"v_add_f32 v195, v195, v23\n"
"v_mfma_f32_16x16x32_bf16 v[48:51], a[68:71], a[132:135], v[48:51]\n"
"global_load_dwordx4 a[56:59], v0, s[34:35] offset:2048 sc1\n"
"v_add_f32 v196, v196, v20\n"
"v_mfma_f32_16x16x32_bf16 v[76:79], a[76:79], a[132:135], v[76:79]\n"
"v_add_f32 v197, v197, v21\n"
"v_mfma_f32_16x16x32_bf16 v[76:79], a[72:75], a[128:131], v[76:79]\n"
"v_add_f32 v198, v198, v22\n"
"v_mfma_f32_16x16x32_bf16 v[80:83], a[72:75], a[136:139], v[80:83]\n"
"v_add_f32 v199, v199, v23\n"
"v_mfma_f32_16x16x32_bf16 v[80:83], a[76:79], a[140:143], v[80:83]\n"
"v_accvgpr_read_b32 v12, a20\n"
"v_mfma_f32_16x16x32_bf16 v[52:55], a[68:71], a[140:143], v[52:55]\n"
"v_accvgpr_read_b32 v13, a21\n"
"v_accvgpr_read_b32 v14, a22\n"
"v_mfma_f32_16x16x32_bf16 v[52:55], a[64:67], a[136:139], v[52:55]\n"
"s_waitcnt lgkmcnt(0)\n"
"v_accvgpr_read_b32 v15, a23\n"
"v_mfma_f32_16x16x32_bf16 v[56:59], a[64:67], a[144:147], v[56:59]\n"
"v_add_f32 v12, v12, v20\n"
"v_mfma_f32_16x16x32_bf16 v[56:59], a[68:71], a[148:151], v[56:59]\n"
"global_load_dwordx4 a[60:63], v0, s[34:35] offset:3072 sc1\n"
"v_add_f32 v13, v13, v21\n"
"v_mfma_f32_16x16x32_bf16 v[84:87], a[76:79], a[148:151], v[84:87]\n"
"v_add_f32 v14, v14, v22\n"
"v_mfma_f32_16x16x32_bf16 v[84:87], a[72:75], a[144:147], v[84:87]\n"
"v_add_f32 v15, v15, v23\n"
"v_mfma_f32_16x16x32_bf16 a[4:7], a[72:75], a[152:155], a[4:7]\n"
"s_add_u32 s34, s34, 8192\n"
"v_accvgpr_write_b32 a20, v12\n"
"v_mfma_f32_16x16x32_bf16 a[4:7], a[76:79], a[156:159], a[4:7]\n"
"v_accvgpr_write_b32 a21, v13\n"
"v_mfma_f32_16x16x32_bf16 a[0:3], a[68:71], a[156:159], a[0:3]\n"
"v_accvgpr_write_b32 a22, v14\n"
"v_mfma_f32_16x16x32_bf16 a[0:3], a[64:67], a[152:155], a[0:3]\n"
"s_waitcnt vmcnt(8)\n"
"v_accvgpr_write_b32 a23, v15\n"
"v_lshlrev_b32 v24, 16, v26\n"
"v_mfma_f32_16x16x32_bf16 v[88:91], a[80:83], a[96:99], v[88:91]\n"
"v_and_b32 v25, s63, v26\n"
"v_mfma_f32_16x16x32_bf16 v[88:91], a[84:87], a[100:103], v[88:91]\n"
"global_load_dwordx4 a[64:67], v0, s[34:35] offset:-4096 sc1\n"
"v_lshlrev_b32 v26, 16, v27\n"
"v_mfma_f32_16x16x32_bf16 v[116:119], a[92:95], a[100:103], v[116:119]\n"
"ds_read_b128 a[100:103], v4 offset:17408\n"
"v_and_b32 v27, s63, v27\n"
"v_mfma_f32_16x16x32_bf16 v[116:119], a[88:91], a[96:99], v[116:119]\n"
"ds_read_b128 a[96:99], v4 offset:16384\n"
"v_add_f32 v200, v200, v24\n"
"v_mfma_f32_16x16x32_bf16 v[120:123], a[88:91], a[104:107], v[120:123]\n"
"v_add_f32 v201, v201, v25\n"
"v_mfma_f32_16x16x32_bf16 v[120:123], a[92:95], a[108:111], v[120:123]\n"
"v_add_f32 v202, v202, v26\n"
"v_mfma_f32_16x16x32_bf16 v[92:95], a[84:87], a[108:111], v[92:95]\n"
"ds_read_b128 a[108:111], v4 offset:19456\n"
"v_add_f32 v203, v203, v27\n"
"v_mfma_f32_16x16x32_bf16 v[92:95], a[80:83], a[104:107], v[92:95]\n"
"ds_read_b128 a[104:107], v4 offset:18432\n"
"v_add_f32 v204, v204, v24\n"
"v_mfma_f32_16x16x32_bf16 v[96:99], a[80:83], a[112:115], v[96:99]\n"
"v_add_f32 v205, v205, v25\n"
"v_add_f32 v206, v206, v26\n"
"v_mfma_f32_16x16x32_bf16 v[96:99], a[84:87], a[116:119], v[96:99]\n"
"global_load_dwordx4 a[68:71], v0, s[34:35] offset:-3072 sc1\n"
"v_add_f32 v207, v207, v27\n"
"v_mfma_f32_16x16x32_bf16 v[124:127], a[92:95], a[116:119], v[124:127]\n"
"ds_read_b128 a[116:119], v4 offset:21504\n"
"v_add_f32 v208, v208, v24\n"
"v_mfma_f32_16x16x32_bf16 v[124:127], a[88:91], a[112:115], v[124:127]\n"
"ds_read_b128 a[112:115], v4 offset:20480\n"
"v_add_f32 v209, v209, v25\n"
"v_mfma_f32_16x16x32_bf16 v[128:131], a[88:91], a[120:123], v[128:131]\n"
"v_add_f32 v210, v210, v26\n"
"v_mfma_f32_16x16x32_bf16 v[128:131], a[92:95], a[124:127], v[128:131]\n"
"v_add_f32 v211, v211, v27\n"
"v_mfma_f32_16x16x32_bf16 v[100:103], a[84:87], a[124:127], v[100:103]\n"
"ds_read_b128 a[124:127], v4 offset:23552\n"
"v_add_f32 v212, v212, v24\n"
"v_mfma_f32_16x16x32_bf16 v[100:103], a[80:83], a[120:123], v[100:103]\n"
"ds_read_b128 a[120:123], v4 offset:22528\n"
"v_add_f32 v213, v213, v25\n"
"v_mfma_f32_16x16x32_bf16 v[104:107], a[80:83], a[128:131], v[104:107]\n"
"v_add_f32 v214, v214, v26\n"
"v_add_f32 v215, v215, v27\n"
"v_mfma_f32_16x16x32_bf16 v[104:107], a[84:87], a[132:135], v[104:107]\n"
"global_load_dwordx4 a[72:75], v0, s[34:35] offset:-2048 sc1\n"
"v_add_f32 v216, v216, v24\n"
"v_mfma_f32_16x16x32_bf16 v[132:135], a[92:95], a[132:135], v[132:135]\n"
"ds_read_b128 a[132:135], v4 offset:25600\n"
"v_add_f32 v217, v217, v25\n"
"v_mfma_f32_16x16x32_bf16 v[132:135], a[88:91], a[128:131], v[132:135]\n"
"ds_read_b128 a[128:131], v4 offset:24576\n"
"v_add_f32 v218, v218, v26\n"
"v_mfma_f32_16x16x32_bf16 v[136:139], a[88:91], a[136:139], v[136:139]\n"
"v_add_f32 v219, v219, v27\n"
"v_mfma_f32_16x16x32_bf16 v[136:139], a[92:95], a[140:143], v[136:139]\n"
"v_add_f32 v220, v220, v24\n"
"v_mfma_f32_16x16x32_bf16 v[108:111], a[84:87], a[140:143], v[108:111]\n"
"ds_read_b128 a[140:143], v4 offset:27648\n"
"v_add_f32 v221, v221, v25\n"
"v_mfma_f32_16x16x32_bf16 v[108:111], a[80:83], a[136:139], v[108:111]\n"
"ds_read_b128 a[136:139], v4 offset:26624\n"
"v_add_f32 v222, v222, v26\n"
"v_mfma_f32_16x16x32_bf16 v[112:115], a[80:83], a[144:147], v[112:115]\n"
"v_add_f32 v223, v223, v27\n"
"v_mfma_f32_16x16x32_bf16 v[112:115], a[84:87], a[148:151], v[112:115]\n"
"global_load_dwordx4 a[76:79], v0, s[34:35] offset:-1024 sc1\n"
"v_add_f32 v224, v224, v24\n"
"v_mfma_f32_16x16x32_bf16 v[140:143], a[92:95], a[148:151], v[140:143]\n"
"ds_read_b128 a[148:151], v4 offset:29696\n"
"v_add_f32 v225, v225, v25\n"
"v_mfma_f32_16x16x32_bf16 v[140:143], a[88:91], a[144:147], v[140:143]\n"
"ds_read_b128 a[144:147], v4 offset:28672\n"
"v_add_f32 v226, v226, v26\n"
"v_mfma_f32_16x16x32_bf16 a[12:15], a[88:91], a[152:155], a[12:15]\n"
"v_add_f32 v227, v227, v27\n"
"v_accvgpr_read_b32 v12, a24\n"
"v_mfma_f32_16x16x32_bf16 a[12:15], a[92:95], a[156:159], a[12:15]\n"
"v_accvgpr_read_b32 v13, a25\n"
"v_mfma_f32_16x16x32_bf16 a[8:11], a[84:87], a[156:159], a[8:11]\n"
"ds_read_b128 a[156:159], v4 offset:31744\n"
"v_accvgpr_read_b32 v14, a26\n"
"v_mfma_f32_16x16x32_bf16 a[8:11], a[80:83], a[152:155], a[8:11]\n"
"s_waitcnt vmcnt(8) lgkmcnt(11)\n"
"ds_read_b128 a[152:155], v4 offset:30720\n"
"v_accvgpr_read_b32 v15, a27\n"
"v_mfma_f32_16x16x32_bf16 v[32:35], a[32:35], a[96:99], v[32:35]\n"
"v_add_f32 v12, v12, v24\n"
"v_mfma_f32_16x16x32_bf16 v[32:35], a[36:39], a[100:103], v[32:35]\n"
"global_load_dwordx4 a[80:83], v0, s[34:35] offset:0 sc1\n"
"v_add_f32 v13, v13, v25\n"
"v_mfma_f32_16x16x32_bf16 v[60:63], a[44:47], a[100:103], v[60:63]\n"
"v_add_f32 v14, v14, v26\n"
"v_mfma_f32_16x16x32_bf16 v[60:63], a[40:43], a[96:99], v[60:63]\n"
"v_add_f32 v15, v15, v27\n"
"v_mfma_f32_16x16x32_bf16 v[64:67], a[40:43], a[104:107], v[64:67]\n"
"v_accvgpr_write_b32 a24, v12\n"
"v_accvgpr_write_b32 a25, v13\n"
"v_mfma_f32_16x16x32_bf16 v[64:67], a[44:47], a[108:111], v[64:67]\n"
"v_accvgpr_write_b32 a26, v14\n"
"v_mfma_f32_16x16x32_bf16 v[36:39], a[36:39], a[108:111], v[36:39]\n"
"v_accvgpr_write_b32 a27, v15\n"
"v_mfma_f32_16x16x32_bf16 v[36:39], a[32:35], a[104:107], v[36:39]\n"
"s_waitcnt lgkmcnt(8)\n"
"v_lshlrev_b32 v28, 16, v30\n"
"v_mfma_f32_16x16x32_bf16 v[40:43], a[32:35], a[112:115], v[40:43]\n"
"v_and_b32 v29, s63, v30\n"
"v_mfma_f32_16x16x32_bf16 v[40:43], a[36:39], a[116:119], v[40:43]\n"
"global_load_dwordx4 a[84:87], v0, s[34:35] offset:1024 sc1\n"
"v_lshlrev_b32 v30, 16, v31\n"
"v_mfma_f32_16x16x32_bf16 v[68:71], a[44:47], a[116:119], v[68:71]\n"
"v_and_b32 v31, s63, v31\n"
"v_mfma_f32_16x16x32_bf16 v[68:71], a[40:43], a[112:115], v[68:71]\n"
"v_add_f32 v228, v228, v28\n"
"v_mfma_f32_16x16x32_bf16 v[72:75], a[40:43], a[120:123], v[72:75]\n"
"v_add_f32 v229, v229, v29\n"
"v_mfma_f32_16x16x32_bf16 v[72:75], a[44:47], a[124:127], v[72:75]\n"
"v_add_f32 v230, v230, v30\n"
"v_mfma_f32_16x16x32_bf16 v[44:47], a[36:39], a[124:127], v[44:47]\n"
"v_add_f32 v231, v231, v31\n"
"v_add_f32 v232, v232, v28\n"
"v_mfma_f32_16x16x32_bf16 v[44:47], a[32:35], a[120:123], v[44:47]\n"
"s_waitcnt lgkmcnt(4)\n"
"v_add_f32 v233, v233, v29\n"
"v_mfma_f32_16x16x32_bf16 v[48:51], a[32:35], a[128:131], v[48:51]\n"
"v_add_f32 v234, v234, v30\n"
"v_mfma_f32_16x16x32_bf16 v[48:51], a[36:39], a[132:135], v[48:51]\n"
"global_load_dwordx4 a[88:91], v0, s[34:35] offset:2048 sc1\n"
"v_add_f32 v235, v235, v31\n"
"v_mfma_f32_16x16x32_bf16 v[76:79], a[44:47], a[132:135], v[76:79]\n"
"v_add_f32 v236, v236, v28\n"
"v_mfma_f32_16x16x32_bf16 v[76:79], a[40:43], a[128:131], v[76:79]\n"
"v_add_f32 v237, v237, v29\n"
"v_mfma_f32_16x16x32_bf16 v[80:83], a[40:43], a[136:139], v[80:83]\n"
"v_add_f32 v238, v238, v30\n"
"v_mfma_f32_16x16x32_bf16 v[80:83], a[44:47], a[140:143], v[80:83]\n"
"v_add_f32 v239, v239, v31\n"
"v_mfma_f32_16x16x32_bf16 v[52:55], a[36:39], a[140:143], v[52:55]\n"
"v_add_f32 v240, v240, v28\n"
"v_mfma_f32_16x16x32_bf16 v[52:55], a[32:35], a[136:139], v[52:55]\n"
"s_waitcnt lgkmcnt(0)\n"
"v_add_f32 v241, v241, v29\n"
"v_add_f32 v242, v242, v30\n"
"v_mfma_f32_16x16x32_bf16 v[56:59], a[32:35], a[144:147], v[56:59]\n"
"v_add_f32 v243, v243, v31\n"
"v_mfma_f32_16x16x32_bf16 v[56:59], a[36:39], a[148:151], v[56:59]\n"
"global_load_dwordx4 a[92:95], v0, s[34:35] offset:3072 sc1\n"
"v_add_f32 v244, v244, v28\n"
"v_mfma_f32_16x16x32_bf16 v[84:87], a[44:47], a[148:151], v[84:87]\n"
"v_add_f32 v245, v245, v29\n"
"v_mfma_f32_16x16x32_bf16 v[84:87], a[40:43], a[144:147], v[84:87]\n"
"v_add_f32 v246, v246, v30\n"
"v_mfma_f32_16x16x32_bf16 a[4:7], a[40:43], a[152:155], a[4:7]\n"
"s_add_u32 s34, s34, 8192\n"
"v_add_f32 v247, v247, v31\n"
"v_mfma_f32_16x16x32_bf16 a[4:7], a[44:47], a[156:159], a[4:7]\n"
"v_add_f32 v248, v248, v28\n"
"v_mfma_f32_16x16x32_bf16 a[0:3], a[36:39], a[156:159], a[0:3]\n"
"v_add_f32 v249, v249, v29\n"
"v_mfma_f32_16x16x32_bf16 a[0:3], a[32:35], a[152:155], a[0:3]\n"
"s_waitcnt vmcnt(8)\n"
"v_add_f32 v250, v250, v30\n"
"v_mfma_f32_16x16x32_bf16 v[88:91], a[48:51], a[96:99], v[88:91]\n"
"v_add_f32 v251, v251, v31\n"
"v_add_f32 v252, v252, v28\n"
"v_mfma_f32_16x16x32_bf16 v[88:91], a[52:55], a[100:103], v[88:91]\n"
"global_load_dwordx4 a[32:35], v0, s[34:35] offset:-4096 sc1\n"
"v_add_f32 v253, v253, v29\n"
"v_mfma_f32_16x16x32_bf16 v[116:119], a[60:63], a[100:103], v[116:119]\n"
"ds_read_b128 a[100:103], v5 offset:17408\n"
"v_add_f32 v254, v254, v30\n"
"v_mfma_f32_16x16x32_bf16 v[116:119], a[56:59], a[96:99], v[116:119]\n"
"ds_read_b128 a[96:99], v5 offset:16384\n"
"v_add_f32 v255, v255, v31\n"
"v_mfma_f32_16x16x32_bf16 v[120:123], a[56:59], a[104:107], v[120:123]\n"
"v_accvgpr_read_b32 v12, a28\n"
"v_mfma_f32_16x16x32_bf16 v[120:123], a[60:63], a[108:111], v[120:123]\n"
"v_accvgpr_read_b32 v13, a29\n"
"v_mfma_f32_16x16x32_bf16 v[92:95], a[52:55], a[108:111], v[92:95]\n"
"ds_read_b128 a[108:111], v5 offset:19456\n"
"v_accvgpr_read_b32 v14, a30\n"
"v_mfma_f32_16x16x32_bf16 v[92:95], a[48:51], a[104:107], v[92:95]\n"
"ds_read_b128 a[104:107], v5 offset:18432\n"
"v_accvgpr_read_b32 v15, a31\n"
"v_mfma_f32_16x16x32_bf16 v[96:99], a[48:51], a[112:115], v[96:99]\n"
"v_add_f32 v12, v12, v28\n"
"v_add_f32 v13, v13, v29\n"
"v_mfma_f32_16x16x32_bf16 v[96:99], a[52:55], a[116:119], v[96:99]\n"
"global_load_dwordx4 a[36:39], v0, s[34:35] offset:-3072 sc1\n"
"v_add_f32 v14, v14, v30\n"
"v_mfma_f32_16x16x32_bf16 v[124:127], a[60:63], a[116:119], v[124:127]\n"
"ds_read_b128 a[116:119], v5 offset:21504\n"
"v_add_f32 v15, v15, v31\n"
"v_mfma_f32_16x16x32_bf16 v[124:127], a[56:59], a[112:115], v[124:127]\n"
"ds_read_b128 a[112:115], v5 offset:20480\n"
"v_accvgpr_write_b32 a28, v12\n"
"v_mfma_f32_16x16x32_bf16 v[128:131], a[56:59], a[120:123], v[128:131]\n"
"v_accvgpr_write_b32 a29, v13\n"
"v_mfma_f32_16x16x32_bf16 v[128:131], a[60:63], a[124:127], v[128:131]\n"
"v_accvgpr_write_b32 a30, v14\n"
"v_mfma_f32_16x16x32_bf16 v[100:103], a[52:55], a[124:127], v[100:103]\n"
"ds_read_b128 a[124:127], v5 offset:23552\n"
"v_accvgpr_write_b32 a31, v15\n"
"v_mfma_f32_16x16x32_bf16 v[100:103], a[48:51], a[120:123], v[100:103]\n"
"ds_read_b128 a[120:123], v5 offset:22528\n"
"v_mfma_f32_16x16x32_bf16 v[104:107], a[48:51], a[128:131], v[104:107]\n"
"v_mfma_f32_16x16x32_bf16 v[104:107], a[52:55], a[132:135], v[104:107]\n"
"global_load_dwordx4 a[40:43], v0, s[34:35] offset:-2048 sc1\n"
"v_mfma_f32_16x16x32_bf16 v[132:135], a[60:63], a[132:135], v[132:135]\n"
"ds_read_b128 a[132:135], v5 offset:25600\n"
"v_mfma_f32_16x16x32_bf16 v[132:135], a[56:59], a[128:131], v[132:135]\n"
"ds_read_b128 a[128:131], v5 offset:24576\n"
"v_mfma_f32_16x16x32_bf16 v[136:139], a[56:59], a[136:139], v[136:139]\n"
"ds_read_b128 v[16:19], v7 offset:32768\n"
"v_mfma_f32_16x16x32_bf16 v[136:139], a[60:63], a[140:143], v[136:139]\n"
"v_mfma_f32_16x16x32_bf16 v[108:111], a[52:55], a[140:143], v[108:111]\n"
"ds_read_b128 a[140:143], v5 offset:27648\n"
"v_mfma_f32_16x16x32_bf16 v[108:111], a[48:51], a[136:139], v[108:111]\n"
"ds_read_b128 a[136:139], v5 offset:26624\n"
"v_mfma_f32_16x16x32_bf16 v[112:115], a[48:51], a[144:147], v[112:115]\n"
"v_mfma_f32_16x16x32_bf16 v[112:115], a[52:55], a[148:151], v[112:115]\n"
"global_load_dwordx4 a[44:47], v0, s[34:35] offset:-1024 sc1\n"
"v_mfma_f32_16x16x32_bf16 v[140:143], a[60:63], a[148:151], v[140:143]\n"
"ds_read_b128 a[148:151], v5 offset:29696\n"
"v_mfma_f32_16x16x32_bf16 v[140:143], a[56:59], a[144:147], v[140:143]\n"
"ds_read_b128 a[144:147], v5 offset:28672\n"
"v_mfma_f32_16x16x32_bf16 a[12:15], a[56:59], a[152:155], a[12:15]\n"
"v_mfma_f32_16x16x32_bf16 a[12:15], a[60:63], a[156:159], a[12:15]\n"
"v_mfma_f32_16x16x32_bf16 a[8:11], a[52:55], a[156:159], a[8:11]\n"
"ds_read_b128 a[156:159], v5 offset:31744\n"
"v_mfma_f32_16x16x32_bf16 a[8:11], a[48:51], a[152:155], a[8:11]\n"
"ds_read_b128 a[152:155], v5 offset:30720\n"
"s_branch L_P6\n"
"L_last:\n"
"s_waitcnt vmcnt(9) lgkmcnt(12)\n"
"v_mfma_f32_16x16x32_bf16 v[32:35], a[32:35], a[96:99], v[32:35]\n"
"s_add_u32 s34, s36, 1454080\n"
"s_add_u32 s48, s48, 131072\n"
"s_addc_u32 s49, s49, 0\n"
"v_mfma_f32_16x16x32_bf16 v[32:35], a[36:39], a[100:103], v[32:35]\n"
"global_load_dwordx4 a[80:83], v0, s[34:35] offset:0 sc1\n"
"v_mfma_f32_16x16x32_bf16 v[60:63], a[44:47], a[100:103], v[60:63]\n"
"s_barrier\n"
"v_mfma_f32_16x16x32_bf16 v[60:63], a[40:43], a[96:99], v[60:63]\n"
"v_mfma_f32_16x16x32_bf16 v[64:67], a[40:43], a[104:107], v[64:67]\n"
"v_mfma_f32_16x16x32_bf16 v[64:67], a[44:47], a[108:111], v[64:67]\n"
"v_mfma_f32_16x16x32_bf16 v[36:39], a[36:39], a[108:111], v[36:39]\n"
"v_subrev_u32 v8, s65, v8\n"
"v_mfma_f32_16x16x32_bf16 v[36:39], a[32:35], a[104:107], v[36:39]\n"
"s_waitcnt lgkmcnt(8)\n"
"v_xor_b32 v9, s79, v9\n"
"v_mfma_f32_16x16x32_bf16 v[40:43], a[32:35], a[112:115], v[40:43]\n"
"v_or_b32 v8, v8, v9\n"
"v_mfma_f32_16x16x32_bf16 v[40:43], a[36:39], a[116:119], v[40:43]\n"
"global_load_dwordx4 a[84:87], v0, s[34:35] offset:1024 sc1\n"
"v_cmp_lt_u32 vcc, 15, v8\n"
"v_mfma_f32_16x16x32_bf16 v[68:71], a[44:47], a[116:119], v[68:71]\n"
"s_cbranch_vccnz L_slow4\n"
"L_back4:\n"
"v_mfma_f32_16x16x32_bf16 v[68:71], a[40:43], a[112:115], v[68:71]\n"
"v_mfma_f32_16x16x32_bf16 v[72:75], a[40:43], a[120:123], v[72:75]\n"
"v_mfma_f32_16x16x32_bf16 v[72:75], a[44:47], a[124:127], v[72:75]\n"
"v_mfma_f32_16x16x32_bf16 v[44:47], a[36:39], a[124:127], v[44:47]\n"
"v_mfma_f32_16x16x32_bf16 v[44:47], a[32:35], a[120:123], v[44:47]\n"
"s_add_u32 m0, s64, 16384\n"
"s_waitcnt lgkmcnt(4)\n"
"v_mfma_f32_16x16x32_bf16 v[48:51], a[32:35], a[128:131], v[48:51]\n"
"v_mfma_f32_16x16x32_bf16 v[48:51], a[36:39], a[132:135], v[48:51]\n"
"global_load_dwordx4 a[88:91], v0, s[34:35] offset:2048 sc1\n"
"v_mfma_f32_16x16x32_bf16 v[76:79], a[44:47], a[132:135], v[76:79]\n"
"v_mfma_f32_16x16x32_bf16 v[76:79], a[40:43], a[128:131], v[76:79]\n"
"v_mfma_f32_16x16x32_bf16 v[80:83], a[40:43], a[136:139], v[80:83]\n"
"s_mov_b32 s97, s37\n"
"v_mfma_f32_16x16x32_bf16 v[80:83], a[44:47], a[140:143], v[80:83]\n"
"v_mfma_f32_16x16x32_bf16 v[52:55], a[36:39], a[140:143], v[52:55]\n"
"v_mfma_f32_16x16x32_bf16 v[52:55], a[32:35], a[136:139], v[52:55]\n"
"s_waitcnt lgkmcnt(0)\n"
"v_mfma_f32_16x16x32_bf16 v[56:59], a[32:35], a[144:147], v[56:59]\n"
"buffer_load_dwordx4 v0, s[48:51], s75 offen offset:0 sc1 lds\n"
"v_mfma_f32_16x16x32_bf16 v[56:59], a[36:39], a[148:151], v[56:59]\n"
"global_load_dwordx4 a[92:95], v0, s[34:35] offset:3072 sc1\n"
"v_mfma_f32_16x16x32_bf16 v[84:87], a[44:47], a[148:151], v[84:87]\n"
"v_mfma_f32_16x16x32_bf16 v[84:87], a[40:43], a[144:147], v[84:87]\n"
"v_mfma_f32_16x16x32_bf16 a[4:7], a[40:43], a[152:155], a[4:7]\n"
"v_mfma_f32_16x16x32_bf16 a[4:7], a[44:47], a[156:159], a[4:7]\n"
"v_mfma_f32_16x16x32_bf16 a[0:3], a[36:39], a[156:159], a[0:3]\n"
"v_mfma_f32_16x16x32_bf16 a[0:3], a[32:35], a[152:155], a[0:3]\n"
"s_waitcnt vmcnt(10)\n"
"v_mfma_f32_16x16x32_bf16 v[88:91], a[48:51], a[96:99], v[88:91]\n"
"s_add_u32 s34, s36, 1458176\n"
"v_mfma_f32_16x16x32_bf16 v[88:91], a[52:55], a[100:103], v[88:91]\n"
"global_load_dwordx4 a[32:35], v0, s[34:35] offset:0 sc1\n"
"v_mfma_f32_16x16x32_bf16 v[116:119], a[60:63], a[100:103], v[116:119]\n"
"buffer_load_dwordx4 v0, s[48:51], s75 offen offset:1024 sc1 lds\n"
"v_mfma_f32_16x16x32_bf16 v[116:119], a[56:59], a[96:99], v[116:119]\n"
"v_mfma_f32_16x16x32_bf16 v[120:123], a[56:59], a[104:107], v[120:123]\n"
"v_mfma_f32_16x16x32_bf16 v[120:123], a[60:63], a[108:111], v[120:123]\n"
"v_mfma_f32_16x16x32_bf16 v[92:95], a[52:55], a[108:111], v[92:95]\n"
"v_mfma_f32_16x16x32_bf16 v[92:95], a[48:51], a[104:107], v[92:95]\n"
"v_mfma_f32_16x16x32_bf16 v[96:99], a[48:51], a[112:115], v[96:99]\n"
"v_mfma_f32_16x16x32_bf16 v[96:99], a[52:55], a[116:119], v[96:99]\n"
"global_load_dwordx4 a[36:39], v0, s[34:35] offset:1024 sc1\n"
"v_mfma_f32_16x16x32_bf16 v[124:127], a[60:63], a[116:119], v[124:127]\n"
"v_mfma_f32_16x16x32_bf16 v[124:127], a[56:59], a[112:115], v[124:127]\n"
"v_mfma_f32_16x16x32_bf16 v[128:131], a[56:59], a[120:123], v[128:131]\n"
"buffer_load_dwordx4 v0, s[48:51], s75 offen offset:2048 sc1 lds\n"
"v_mfma_f32_16x16x32_bf16 v[128:131], a[60:63], a[124:127], v[128:131]\n"
"v_mfma_f32_16x16x32_bf16 v[100:103], a[52:55], a[124:127], v[100:103]\n"
"v_mfma_f32_16x16x32_bf16 v[100:103], a[48:51], a[120:123], v[100:103]\n"
"v_mfma_f32_16x16x32_bf16 v[104:107], a[48:51], a[128:131], v[104:107]\n"
"v_mfma_f32_16x16x32_bf16 v[104:107], a[52:55], a[132:135], v[104:107]\n"
"global_load_dwordx4 a[40:43], v0, s[34:35] offset:2048 sc1\n"
"v_mfma_f32_16x16x32_bf16 v[132:135], a[60:63], a[132:135], v[132:135]\n"
"v_mfma_f32_16x16x32_bf16 v[132:135], a[56:59], a[128:131], v[132:135]\n"
"v_mfma_f32_16x16x32_bf16 v[136:139], a[56:59], a[136:139], v[136:139]\n"
"v_mfma_f32_16x16x32_bf16 v[136:139], a[60:63], a[140:143], v[136:139]\n"
"buffer_load_dwordx4 v0, s[48:51], s75 offen offset:3072 sc1 lds\n"
"v_mfma_f32_16x16x32_bf16 v[108:111], a[52:55], a[140:143], v[108:111]\n"
"v_mfma_f32_16x16x32_bf16 v[108:111], a[48:51], a[136:139], v[108:111]\n"
"v_mfma_f32_16x16x32_bf16 v[112:115], a[48:51], a[144:147], v[112:115]\n"
"v_mfma_f32_16x16x32_bf16 v[112:115], a[52:55], a[148:151], v[112:115]\n"
"global_load_dwordx4 a[44:47], v0, s[34:35] offset:3072 sc1\n"
"v_mfma_f32_16x16x32_bf16 v[140:143], a[60:63], a[148:151], v[140:143]\n"
"v_mfma_f32_16x16x32_bf16 v[140:143], a[56:59], a[144:147], v[140:143]\n"
"v_mfma_f32_16x16x32_bf16 a[12:15], a[56:59], a[152:155], a[12:15]\n"
"v_mfma_f32_16x16x32_bf16 a[12:15], a[60:63], a[156:159], a[12:15]\n"
"v_mfma_f32_16x16x32_bf16 a[8:11], a[52:55], a[156:159], a[8:11]\n"
"v_mfma_f32_16x16x32_bf16 a[8:11], a[48:51], a[152:155], a[8:11]\n"
"s_add_u32 m0, s64, 20480\n"
"s_waitcnt vmcnt(12)\n"
"v_mfma_f32_16x16x32_bf16 v[144:147], a[64:67], a[96:99], v[144:147]\n"
"s_add_u32 s34, s36, 1462272\n"
"v_mfma_f32_16x16x32_bf16 v[144:147], a[68:71], a[100:103], v[144:147]\n"
"global_load_dwordx4 a[48:51], v0, s[34:35] offset:0 sc1\n"
"v_mfma_f32_16x16x32_bf16 v[172:175], a[76:79], a[100:103], v[172:175]\n"
"v_mfma_f32_16x16x32_bf16 v[172:175], a[72:75], a[96:99], v[172:175]\n"
"v_mfma_f32_16x16x32_bf16 v[176:179], a[72:75], a[104:107], v[176:179]\n"
"v_mfma_f32_16x16x32_bf16 v[176:179], a[76:79], a[108:111], v[176:179]\n"
"v_mfma_f32_16x16x32_bf16 v[148:151], a[68:71], a[108:111], v[148:151]\n"
"v_lshlrev_b32 v16, 16, v18\n"
"v_and_b32 v17, s63, v18\n"
"v_mfma_f32_16x16x32_bf16 v[148:151], a[64:67], a[104:107], v[148:151]\n"
"v_lshlrev_b32 v18, 16, v19\n"
"v_and_b32 v19, s63, v19\n"
"v_mfma_f32_16x16x32_bf16 v[152:155], a[64:67], a[112:115], v[152:155]\n"
"buffer_load_dwordx4 v1, s[48:51], s75 offen offset:0 sc1 lds\n"
"v_mfma_f32_16x16x32_bf16 v[152:155], a[68:71], a[116:119], v[152:155]\n"
"global_load_dwordx4 a[52:55], v0, s[34:35] offset:1024 sc1\n"
"v_add_f32 v88, v88, v16\n"
"v_mfma_f32_16x16x32_bf16 v[180:183], a[76:79], a[116:119], v[180:183]\n"
"v_add_f32 v89, v89, v17\n"
"v_add_f32 v90, v90, v18\n"
"v_mfma_f32_16x16x32_bf16 v[180:183], a[72:75], a[112:115], v[180:183]\n"
"v_add_f32 v91, v91, v19\n"
"v_add_f32 v92, v92, v16\n"
"v_mfma_f32_16x16x32_bf16 v[184:187], a[72:75], a[120:123], v[184:187]\n"
"v_add_f32 v93, v93, v17\n"
"v_add_f32 v94, v94, v18\n"
"v_mfma_f32_16x16x32_bf16 v[184:187], a[76:79], a[124:127], v[184:187]\n"
"v_add_f32 v95, v95, v19\n"
"v_add_f32 v96, v96, v16\n"
"v_mfma_f32_16x16x32_bf16 v[156:159], a[68:71], a[124:127], v[156:159]\n"
"v_add_f32 v97, v97, v17\n"
"v_add_f32 v98, v98, v18\n"
"v_mfma_f32_16x16x32_bf16 v[156:159], a[64:67], a[120:123], v[156:159]\n"
"v_add_f32 v99, v99, v19\n"
"v_add_f32 v100, v100, v16\n"
"v_mfma_f32_16x16x32_bf16 v[160:163], a[64:67], a[128:131], v[160:163]\n"
"v_add_f32 v101, v101, v17\n"
"v_add_f32 v102, v102, v18\n"
"v_mfma_f32_16x16x32_bf16 v[160:163], a[68:71], a[132:135], v[160:163]\n"
"global_load_dwordx4 a[56:59], v0, s[34:35] offset:2048 sc1\n"
"v_add_f32 v103, v103, v19\n"
"v_mfma_f32_16x16x32_bf16 v[188:191], a[76:79], a[132:135], v[188:191]\n"
"buffer_load_dwordx4 v1, s[48:51], s75 offen offset:1024 sc1 lds\n"
"v_mfma_f32_16x16x32_bf16 v[188:191], a[72:75], a[128:131], v[188:191]\n"
"v_add_f32 v104, v104, v16\n"
"v_add_f32 v105, v105, v17\n"
"v_mfma_f32_16x16x32_bf16 v[192:195], a[72:75], a[136:139], v[192:195]\n"
"v_add_f32 v106, v106, v18\n"
"v_add_f32 v107, v107, v19\n"
"v_mfma_f32_16x16x32_bf16 v[192:195], a[76:79], a[140:143], v[192:195]\n"
"v_add_f32 v108, v108, v16\n"
"v_add_f32 v109, v109, v17\n"
"v_mfma_f32_16x16x32_bf16 v[164:167], a[68:71], a[140:143], v[164:167]\n"
"v_add_f32 v110, v110, v18\n"
"v_add_f32 v111, v111, v19\n"
"v_mfma_f32_16x16x32_bf16 v[164:167], a[64:67], a[136:139], v[164:167]\n"
"v_add_f32 v112, v112, v16\n"
"v_add_f32 v113, v113, v17\n"
"v_mfma_f32_16x16x32_bf16 v[168:171], a[64:67], a[144:147], v[168:171]\n"
"v_add_f32 v114, v114, v18\n"
"v_add_f32 v115, v115, v19\n"
"v_mfma_f32_16x16x32_bf16 v[168:171], a[68:71], a[148:151], v[168:171]\n"
"global_load_dwordx4 a[60:63], v0, s[34:35] offset:3072 sc1\n"
"v_accvgpr_read_b32 v12, a8\n"
"v_mfma_f32_16x16x32_bf16 v[196:199], a[76:79], a[148:151], v[196:199]\n"
"v_accvgpr_read_b32 v13, a9\n"
"v_accvgpr_read_b32 v14, a10\n"
"v_mfma_f32_16x16x32_bf16 v[196:199], a[72:75], a[144:147], v[196:199]\n"
"buffer_load_dwordx4 v1, s[48:51], s75 offen offset:2048 sc1 lds\n"
"v_mfma_f32_16x16x32_bf16 a[20:23], a[72:75], a[152:155], a[20:23]\n"
"v_accvgpr_read_b32 v15, a11\n"
"v_add_f32 v12, v12, v16\n"
"v_mfma_f32_16x16x32_bf16 a[20:23], a[76:79], a[156:159], a[20:23]\n"
"v_add_f32 v13, v13, v17\n"
"v_add_f32 v14, v14, v18\n"
"v_mfma_f32_16x16x32_bf16 a[16:19], a[68:71], a[156:159], a[16:19]\n"
"v_add_f32 v15, v15, v19\n"
"v_accvgpr_write_b32 a8, v12\n"
"v_mfma_f32_16x16x32_bf16 a[16:19], a[64:67], a[152:155], a[16:19]\n"
"s_waitcnt vmcnt(14)\n"
"v_accvgpr_write_b32 a9, v13\n"
"v_accvgpr_write_b32 a10, v14\n"
"v_accvgpr_write_b32 a11, v15\n"
"v_mfma_f32_16x16x32_bf16 v[200:203], a[80:83], a[96:99], v[200:203]\n"
"s_add_u32 s34, s36, 1474560\n"
"v_lshlrev_b32 v20, 16, v22\n"
"v_and_b32 v21, s63, v22\n"
"v_mfma_f32_16x16x32_bf16 v[200:203], a[84:87], a[100:103], v[200:203]\n"
"global_load_dwordx4 a[64:67], v0, s[34:35] offset:0 sc1\n"
"v_lshlrev_b32 v22, 16, v23\n"
"v_mfma_f32_16x16x32_bf16 v[228:231], a[92:95], a[100:103], v[228:231]\n"
"ds_read_b128 a[100:103], v3 offset:1024\n"
"v_and_b32 v23, s63, v23\n"
"v_mfma_f32_16x16x32_bf16 v[228:231], a[88:91], a[96:99], v[228:231]\n"
"ds_read_b128 a[96:99], v3 offset:0\n"
"v_add_f32 v116, v116, v20\n"
"v_mfma_f32_16x16x32_bf16 v[232:235], a[88:91], a[104:107], v[232:235]\n"
"v_add_f32 v117, v117, v21\n"
"v_add_f32 v118, v118, v22\n"
"v_mfma_f32_16x16x32_bf16 v[232:235], a[92:95], a[108:111], v[232:235]\n"
"buffer_load_dwordx4 v1, s[48:51], s75 offen offset:3072 sc1 lds\n"
"v_mfma_f32_16x16x32_bf16 v[204:207], a[84:87], a[108:111], v[204:207]\n"
"ds_read_b128 a[108:111], v3 offset:3072\n"
"v_add_f32 v119, v119, v23\n"
"v_add_f32 v120, v120, v20\n"
"v_mfma_f32_16x16x32_bf16 v[204:207], a[80:83], a[104:107], v[204:207]\n"
"ds_read_b128 a[104:107], v3 offset:2048\n"
"v_add_f32 v121, v121, v21\n"
"v_mfma_f32_16x16x32_bf16 v[208:211], a[80:83], a[112:115], v[208:211]\n"
"v_add_f32 v122, v122, v22\n"
"v_add_f32 v123, v123, v23\n"
"v_mfma_f32_16x16x32_bf16 v[208:211], a[84:87], a[116:119], v[208:211]\n"
"global_load_dwordx4 a[68:71], v0, s[34:35] offset:1024 sc1\n"
"v_add_f32 v124, v124, v20\n"
"v_add_f32 v125, v125, v21\n"
"v_mfma_f32_16x16x32_bf16 v[236:239], a[92:95], a[116:119], v[236:239]\n"
"ds_read_b128 a[116:119], v3 offset:5120\n"
"v_add_f32 v126, v126, v22\n"
"v_mfma_f32_16x16x32_bf16 v[236:239], a[88:91], a[112:115], v[236:239]\n"
"ds_read_b128 a[112:115], v3 offset:4096\n"
"v_add_f32 v127, v127, v23\n"
"v_mfma_f32_16x16x32_bf16 v[240:243], a[88:91], a[120:123], v[240:243]\n"
"v_add_f32 v128, v128, v20\n"
"v_add_f32 v129, v129, v21\n"
"v_mfma_f32_16x16x32_bf16 v[240:243], a[92:95], a[124:127], v[240:243]\n"
"v_add_f32 v130, v130, v22\n"
"v_add_f32 v131, v131, v23\n"
"v_mfma_f32_16x16x32_bf16 v[212:215], a[84:87], a[124:127], v[212:215]\n"
"s_add_u32 m0, s64, 24576\n"
"ds_read_b128 a[124:127], v3 offset:7168\n"
"v_add_f32 v132, v132, v20\n"
"v_add_f32 v133, v133, v21\n"
"v_mfma_f32_16x16x32_bf16 v[212:215], a[80:83], a[120:123], v[212:215]\n"
"ds_read_b128 a[120:123], v3 offset:6144\n"
"v_add_f32 v134, v134, v22\n"
"v_mfma_f32_16x16x32_bf16 v[216:219], a[80:83], a[128:131], v[216:219]\n"
"v_add_f32 v135, v135, v23\n"
"v_add_f32 v136, v136, v20\n"
"v_mfma_f32_16x16x32_bf16 v[216:219], a[84:87], a[132:135], v[216:219]\n"
"global_load_dwordx4 a[72:75], v0, s[34:35] offset:2048 sc1\n"
"v_add_f32 v137, v137, v21\n"
"v_mfma_f32_16x16x32_bf16 v[244:247], a[92:95], a[132:135], v[244:247]\n"
"ds_read_b128 a[132:135], v3 offset:9216\n"
"v_add_f32 v138, v138, v22\n"
"v_add_f32 v139, v139, v23\n"
"v_mfma_f32_16x16x32_bf16 v[244:247], a[88:91], a[128:131], v[244:247]\n"
"ds_read_b128 a[128:131], v3 offset:8192\n"
"v_add_f32 v140, v140, v20\n"
"v_mfma_f32_16x16x32_bf16 v[248:251], a[88:91], a[136:139], v[248:251]\n"
"v_add_f32 v141, v141, v21\n"
"v_add_f32 v142, v142, v22\n"
"v_mfma_f32_16x16x32_bf16 v[248:251], a[92:95], a[140:143], v[248:251]\n"
"v_add_f32 v143, v143, v23\n"
"v_accvgpr_read_b32 v12, a12\n"
"v_mfma_f32_16x16x32_bf16 v[220:223], a[84:87], a[140:143], v[220:223]\n"
"ds_read_b128 a[140:143], v3 offset:11264\n"
"v_accvgpr_read_b32 v13, a13\n"
"v_mfma_f32_16x16x32_bf16 v[220:223], a[80:83], a[136:139], v[220:223]\n"
"ds_read_b128 a[136:139], v3 offset:10240\n"
"v_accvgpr_read_b32 v14, a14\n"
"v_accvgpr_read_b32 v15, a15\n"
"v_mfma_f32_16x16x32_bf16 v[224:227], a[80:83], a[144:147], v[224:227]\n"
"buffer_load_dwordx4 v0, s[48:51], s69 offen offset:0 sc1 lds\n"
"v_add_f32 v12, v12, v20\n"
"v_mfma_f32_16x16x32_bf16 v[224:227], a[84:87], a[148:151], v[224:227]\n"
"global_load_dwordx4 a[76:79], v0, s[34:35] offset:3072 sc1\n"
"v_add_f32 v13, v13, v21\n"
"v_add_f32 v14, v14, v22\n"
"v_mfma_f32_16x16x32_bf16 v[252:255], a[92:95], a[148:151], v[252:255]\n"
"ds_read_b128 a[148:151], v3 offset:13312\n"
"v_add_f32 v15, v15, v23\n"
"v_accvgpr_write_b32 a12, v12\n"
"v_mfma_f32_16x16x32_bf16 v[252:255], a[88:91], a[144:147], v[252:255]\n"
"ds_read_b128 a[144:147], v3 offset:12288\n"
"v_accvgpr_write_b32 a13, v13\n"
"v_mfma_f32_16x16x32_bf16 a[28:31], a[88:91], a[152:155], a[28:31]\n"
"v_accvgpr_write_b32 a14, v14\n"
"v_accvgpr_write_b32 a15, v15\n"
"v_mfma_f32_16x16x32_bf16 a[28:31], a[92:95], a[156:159], a[28:31]\n"
"v_mfma_f32_16x16x32_bf16 a[24:27], a[84:87], a[156:159], a[24:27]\n"
"ds_read_b128 a[156:159], v3 offset:15360\n"
"v_mfma_f32_16x16x32_bf16 a[24:27], a[80:83], a[152:155], a[24:27]\n"
"s_waitcnt vmcnt(13) lgkmcnt(11)\n"
"ds_read_b128 a[152:155], v3 offset:14336\n"
"v_mfma_f32_16x16x32_bf16 v[32:35], a[32:35], a[96:99], v[32:35]\n"
"s_add_u32 s34, s36, 1478656\n"
"v_mfma_f32_16x16x32_bf16 v[32:35], a[36:39], a[100:103], v[32:35]\n"
"global_load_dwordx4 a[80:83], v0, s[34:35] offset:0 sc1\n"
"v_mfma_f32_16x16x32_bf16 v[60:63], a[44:47], a[100:103], v[60:63]\n"
"buffer_load_dwordx4 v0, s[48:51], s69 offen offset:1024 sc1 lds\n"
"v_mfma_f32_16x16x32_bf16 v[60:63], a[40:43], a[96:99], v[60:63]\n"
"v_lshrrev_b32 v16, 4, v0\n"
"v_mfma_f32_16x16x32_bf16 v[64:67], a[40:43], a[104:107], v[64:67]\n"
"v_mfma_f32_16x16x32_bf16 v[64:67], a[44:47], a[108:111], v[64:67]\n"
"v_mfma_f32_16x16x32_bf16 v[36:39], a[36:39], a[108:111], v[36:39]\n"
"v_mfma_f32_16x16x32_bf16 v[36:39], a[32:35], a[104:107], v[36:39]\n"
"s_waitcnt lgkmcnt(8)\n"
"v_and_b32 v16, 0x30, v16\n"
"v_mfma_f32_16x16x32_bf16 v[40:43], a[32:35], a[112:115], v[40:43]\n"
"v_mfma_f32_16x16x32_bf16 v[40:43], a[36:39], a[116:119], v[40:43]\n"
"global_load_dwordx4 a[84:87], v0, s[34:35] offset:1024 sc1\n"
"v_mfma_f32_16x16x32_bf16 v[68:71], a[44:47], a[116:119], v[68:71]\n"
"buffer_load_dwordx2 v[18:19], v16, s[44:47], s61 offen offset:128\n"
"v_mfma_f32_16x16x32_bf16 v[68:71], a[40:43], a[112:115], v[68:71]\n"
"buffer_load_dwordx4 v0, s[48:51], s69 offen offset:2048 sc1 lds\n"
"v_mfma_f32_16x16x32_bf16 v[72:75], a[40:43], a[120:123], v[72:75]\n"
"v_mfma_f32_16x16x32_bf16 v[72:75], a[44:47], a[124:127], v[72:75]\n"
"v_lshrrev_b32 v20, 4, v0\n"
"v_mfma_f32_16x16x32_bf16 v[44:47], a[36:39], a[124:127], v[44:47]\n"
"v_mfma_f32_16x16x32_bf16 v[44:47], a[32:35], a[120:123], v[44:47]\n"
"s_waitcnt lgkmcnt(4)\n"
"v_mfma_f32_16x16x32_bf16 v[48:51], a[32:35], a[128:131], v[48:51]\n"
"v_and_b32 v20, 0x30, v20\n"
"v_mfma_f32_16x16x32_bf16 v[48:51], a[36:39], a[132:135], v[48:51]\n"
"global_load_dwordx4 a[88:91], v0, s[34:35] offset:2048 sc1\n"
"v_mfma_f32_16x16x32_bf16 v[76:79], a[44:47], a[132:135], v[76:79]\n"
"v_mfma_f32_16x16x32_bf16 v[76:79], a[40:43], a[128:131], v[76:79]\n"
"v_mfma_f32_16x16x32_bf16 v[80:83], a[40:43], a[136:139], v[80:83]\n"
"buffer_load_dwordx4 v0, s[48:51], s69 offen offset:3072 sc1 lds\n"
"v_mfma_f32_16x16x32_bf16 v[80:83], a[44:47], a[140:143], v[80:83]\n"
"buffer_load_dwordx2 v[22:23], v20, s[44:47], s61 offen offset:136\n"
"v_mfma_f32_16x16x32_bf16 v[52:55], a[36:39], a[140:143], v[52:55]\n"
"v_mfma_f32_16x16x32_bf16 v[52:55], a[32:35], a[136:139], v[52:55]\n"
"s_waitcnt lgkmcnt(0)\n"
"v_mfma_f32_16x16x32_bf16 v[56:59], a[32:35], a[144:147], v[56:59]\n"
"v_lshrrev_b32 v24, 4, v0\n"
"v_mfma_f32_16x16x32_bf16 v[56:59], a[36:39], a[148:151], v[56:59]\n"
"global_load_dwordx4 a[92:95], v0, s[34:35] offset:3072 sc1\n"
"v_mfma_f32_16x16x32_bf16 v[84:87], a[44:47], a[148:151], v[84:87]\n"
"v_mfma_f32_16x16x32_bf16 v[84:87], a[40:43], a[144:147], v[84:87]\n"
"v_and_b32 v24, 0x30, v24\n"
"v_mfma_f32_16x16x32_bf16 a[4:7], a[40:43], a[152:155], a[4:7]\n"
"v_mfma_f32_16x16x32_bf16 a[4:7], a[44:47], a[156:159], a[4:7]\n"
"v_mfma_f32_16x16x32_bf16 a[0:3], a[36:39], a[156:159], a[0:3]\n"
"s_add_u32 m0, s64, 28672\n"
"v_mfma_f32_16x16x32_bf16 a[0:3], a[32:35], a[152:155], a[0:3]\n"
"s_waitcnt vmcnt(16)\n"
"buffer_load_dwordx2 v[26:27], v24, s[44:47], s61 offen offset:192\n"
"v_mfma_f32_16x16x32_bf16 v[88:91], a[48:51], a[96:99], v[88:91]\n"
"s_add_u32 s34, s36, 1490944\n"
"v_lshrrev_b32 v28, 4, v0\n"
"v_mfma_f32_16x16x32_bf16 v[88:91], a[52:55], a[100:103], v[88:91]\n"
"global_load_dwordx4 a[32:35], v0, s[34:35] offset:0 sc1\n"
"v_mfma_f32_16x16x32_bf16 v[116:119], a[60:63], a[100:103], v[116:119]\n"
"ds_read_b128 a[100:103], v4 offset:1024\n"
"v_mfma_f32_16x16x32_bf16 v[116:119], a[56:59], a[96:99], v[116:119]\n"
"ds_read_b128 a[96:99], v4 offset:0\n"
"v_mfma_f32_16x16x32_bf16 v[120:123], a[56:59], a[104:107], v[120:123]\n"
"v_and_b32 v28, 0x30, v28\n"
"v_mfma_f32_16x16x32_bf16 v[120:123], a[60:63], a[108:111], v[120:123]\n"
"buffer_load_dwordx2 v[30:31], v28, s[44:47], s61 offen offset:200\n"
"v_mfma_f32_16x16x32_bf16 v[92:95], a[52:55], a[108:111], v[92:95]\n"
"ds_read_b128 a[108:111], v4 offset:3072\n"
"v_mfma_f32_16x16x32_bf16 v[92:95], a[48:51], a[104:107], v[92:95]\n"
"ds_read_b128 a[104:107], v4 offset:2048\n"
"v_mfma_f32_16x16x32_bf16 v[96:99], a[48:51], a[112:115], v[96:99]\n"
"buffer_load_dwordx4 v1, s[48:51], s69 offen offset:0 sc1 lds\n"
"v_mfma_f32_16x16x32_bf16 v[96:99], a[52:55], a[116:119], v[96:99]\n"
"global_load_dwordx4 a[36:39], v0, s[34:35] offset:1024 sc1\n"
"v_mfma_f32_16x16x32_bf16 v[124:127], a[60:63], a[116:119], v[124:127]\n"
"ds_read_b128 a[116:119], v4 offset:5120\n"
"v_mfma_f32_16x16x32_bf16 v[124:127], a[56:59], a[112:115], v[124:127]\n"
"ds_read_b128 a[112:115], v4 offset:4096\n"
"v_mfma_f32_16x16x32_bf16 v[128:131], a[56:59], a[120:123], v[128:131]\n"
"v_mfma_f32_16x16x32_bf16 v[128:131], a[60:63], a[124:127], v[128:131]\n"
"v_mfma_f32_16x16x32_bf16 v[100:103], a[52:55], a[124:127], v[100:103]\n"
"ds_read_b128 a[124:127], v4 offset:7168\n"
"v_mfma_f32_16x16x32_bf16 v[100:103], a[48:51], a[120:123], v[100:103]\n"
"ds_read_b128 a[120:123], v4 offset:6144\n"
"v_mfma_f32_16x16x32_bf16 v[104:107], a[48:51], a[128:131], v[104:107]\n"
"buffer_load_dwordx4 v1, s[48:51], s69 offen offset:1024 sc1 lds\n"
"v_mfma_f32_16x16x32_bf16 v[104:107], a[52:55], a[132:135], v[104:107]\n"
"global_load_dwordx4 a[40:43], v0, s[34:35] offset:2048 sc1\n"
"v_mfma_f32_16x16x32_bf16 v[132:135], a[60:63], a[132:135], v[132:135]\n"
"ds_read_b128 a[132:135], v4 offset:9216\n"
"v_mfma_f32_16x16x32_bf16 v[132:135], a[56:59], a[128:131], v[132:135]\n"
"ds_read_b128 a[128:131], v4 offset:8192\n"
"v_mfma_f32_16x16x32_bf16 v[136:139], a[56:59], a[136:139], v[136:139]\n"
"v_mfma_f32_16x16x32_bf16 v[136:139], a[60:63], a[140:143], v[136:139]\n"
"v_mfma_f32_16x16x32_bf16 v[108:111], a[52:55], a[140:143], v[108:111]\n"
"ds_read_b128 a[140:143], v4 offset:11264\n"
"v_mfma_f32_16x16x32_bf16 v[108:111], a[48:51], a[136:139], v[108:111]\n"
"ds_read_b128 a[136:139], v4 offset:10240\n"
"v_mfma_f32_16x16x32_bf16 v[112:115], a[48:51], a[144:147], v[112:115]\n"
"v_mfma_f32_16x16x32_bf16 v[112:115], a[52:55], a[148:151], v[112:115]\n"
"global_load_dwordx4 a[44:47], v0, s[34:35] offset:3072 sc1\n"
"v_mfma_f32_16x16x32_bf16 v[140:143], a[60:63], a[148:151], v[140:143]\n"
"ds_read_b128 a[148:151], v4 offset:13312\n"
"v_mfma_f32_16x16x32_bf16 v[140:143], a[56:59], a[144:147], v[140:143]\n"
"ds_read_b128 a[144:147], v4 offset:12288\n"
"v_mfma_f32_16x16x32_bf16 a[12:15], a[56:59], a[152:155], a[12:15]\n"
"buffer_load_dwordx4 v1, s[48:51], s69 offen offset:2048 sc1 lds\n"
"v_mfma_f32_16x16x32_bf16 a[12:15], a[60:63], a[156:159], a[12:15]\n"
"v_mfma_f32_16x16x32_bf16 a[8:11], a[52:55], a[156:159], a[8:11]\n"
"ds_read_b128 a[156:159], v4 offset:15360\n"
"v_mfma_f32_16x16x32_bf16 a[8:11], a[48:51], a[152:155], a[8:11]\n"
"s_waitcnt vmcnt(18) lgkmcnt(11)\n"
"ds_read_b128 a[152:155], v4 offset:14336\n"
"v_mfma_f32_16x16x32_bf16 v[32:35], a[64:67], a[96:99], v[32:35]\n"
"s_add_u32 s34, s36, 1495040\n"
"v_mfma_f32_16x16x32_bf16 v[32:35], a[68:71], a[100:103], v[32:35]\n"
"global_load_dwordx4 a[48:51], v0, s[34:35] offset:0 sc1\n"
"v_mfma_f32_16x16x32_bf16 v[60:63], a[76:79], a[100:103], v[60:63]\n"
"v_mfma_f32_16x16x32_bf16 v[60:63], a[72:75], a[96:99], v[60:63]\n"
"v_mfma_f32_16x16x32_bf16 v[64:67], a[72:75], a[104:107], v[64:67]\n"
"buffer_load_dwordx4 v1, s[48:51], s69 offen offset:3072 sc1 lds\n"
"v_mfma_f32_16x16x32_bf16 v[64:67], a[76:79], a[108:111], v[64:67]\n"
"v_mfma_f32_16x16x32_bf16 v[36:39], a[68:71], a[108:111], v[36:39]\n"
"v_mfma_f32_16x16x32_bf16 v[36:39], a[64:67], a[104:107], v[36:39]\n"
"s_waitcnt lgkmcnt(8)\n"
"v_mfma_f32_16x16x32_bf16 v[40:43], a[64:67], a[112:115], v[40:43]\n"
"v_mfma_f32_16x16x32_bf16 v[40:43], a[68:71], a[116:119], v[40:43]\n"
"global_load_dwordx4 a[52:55], v0, s[34:35] offset:1024 sc1\n"
"v_mfma_f32_16x16x32_bf16 v[68:71], a[76:79], a[116:119], v[68:71]\n"
"v_mfma_f32_16x16x32_bf16 v[68:71], a[72:75], a[112:115], v[68:71]\n"
"v_mfma_f32_16x16x32_bf16 v[72:75], a[72:75], a[120:123], v[72:75]\n"
"v_mfma_f32_16x16x32_bf16 v[72:75], a[76:79], a[124:127], v[72:75]\n"
"v_mfma_f32_16x16x32_bf16 v[44:47], a[68:71], a[124:127], v[44:47]\n"
"v_mfma_f32_16x16x32_bf16 v[44:47], a[64:67], a[120:123], v[44:47]\n"
"s_waitcnt lgkmcnt(4)\n"
"v_mfma_f32_16x16x32_bf16 v[48:51], a[64:67], a[128:131], v[48:51]\n"
"v_mfma_f32_16x16x32_bf16 v[48:51], a[68:71], a[132:135], v[48:51]\n"
"global_load_dwordx4 a[56:59], v0, s[34:35] offset:2048 sc1\n"
"v_mfma_f32_16x16x32_bf16 v[76:79], a[76:79], a[132:135], v[76:79]\n"
"v_mfma_f32_16x16x32_bf16 v[76:79], a[72:75], a[128:131], v[76:79]\n"
"v_mfma_f32_16x16x32_bf16 v[80:83], a[72:75], a[136:139], v[80:83]\n"
"v_mfma_f32_16x16x32_bf16 v[80:83], a[76:79], a[140:143], v[80:83]\n"
"v_mfma_f32_16x16x32_bf16 v[52:55], a[68:71], a[140:143], v[52:55]\n"
"v_mfma_f32_16x16x32_bf16 v[52:55], a[64:67], a[136:139], v[52:55]\n"
"s_waitcnt lgkmcnt(0)\n"
"v_mfma_f32_16x16x32_bf16 v[56:59], a[64:67], a[144:147], v[56:59]\n"
"v_mfma_f32_16x16x32_bf16 v[56:59], a[68:71], a[148:151], v[56:59]\n"
"global_load_dwordx4 a[60:63], v0, s[34:35] offset:3072 sc1\n"
"v_mfma_f32_16x16x32_bf16 v[84:87], a[76:79], a[148:151], v[84:87]\n"
"v_mfma_f32_16x16x32_bf16 v[84:87], a[72:75], a[144:147], v[84:87]\n"
"v_mfma_f32_16x16x32_bf16 a[4:7], a[72:75], a[152:155], a[4:7]\n"
"v_mfma_f32_16x16x32_bf16 a[4:7], a[76:79], a[156:159], a[4:7]\n"
"v_mfma_f32_16x16x32_bf16 a[0:3], a[68:71], a[156:159], a[0:3]\n"
"v_mfma_f32_16x16x32_bf16 a[0:3], a[64:67], a[152:155], a[0:3]\n"
"s_waitcnt vmcnt(14)\n"
"v_mfma_f32_16x16x32_bf16 v[88:91], a[80:83], a[96:99], v[88:91]\n"
"s_add_u32 s34, s36, 1507328\n"
"v_mfma_f32_16x16x32_bf16 v[88:91], a[84:87], a[100:103], v[88:91]\n"
"global_load_dwordx4 a[64:67], v0, s[34:35] offset:0 sc1\n"
"v_mfma_f32_16x16x32_bf16 v[116:119], a[92:95], a[100:103], v[116:119]\n"
"ds_read_b128 a[100:103], v5 offset:1024\n"
"v_mfma_f32_16x16x32_bf16 v[116:119], a[88:91], a[96:99], v[116:119]\n"
"ds_read_b128 a[96:99], v5 offset:0\n"
"v_mfma_f32_16x16x32_bf16 v[120:123], a[88:91], a[104:107], v[120:123]\n"
"v_mfma_f32_16x16x32_bf16 v[120:123], a[92:95], a[108:111], v[120:123]\n"
"v_mfma_f32_16x16x32_bf16 v[92:95], a[84:87], a[108:111], v[92:95]\n"
"ds_read_b128 a[108:111], v5 offset:3072\n"
"v_mfma_f32_16x16x32_bf16 v[92:95], a[80:83], a[104:107], v[92:95]\n"
"ds_read_b128 a[104:107], v5 offset:2048\n"
"v_mfma_f32_16x16x32_bf16 v[96:99], a[80:83], a[112:115], v[96:99]\n"
"v_mfma_f32_16x16x32_bf16 v[96:99], a[84:87], a[116:119], v[96:99]\n"
"global_load_dwordx4 a[68:71], v0, s[34:35] offset:1024 sc1\n"
"v_mfma_f32_16x16x32_bf16 v[124:127], a[92:95], a[116:119], v[124:127]\n"
"ds_read_b128 a[116:119], v5 offset:5120\n"
"v_mfma_f32_16x16x32_bf16 v[124:127], a[88:91], a[112:115], v[124:127]\n"
"ds_read_b128 a[112:115], v5 offset:4096\n"
"v_mfma_f32_16x16x32_bf16 v[128:131], a[88:91], a[120:123], v[128:131]\n"
"v_mfma_f32_16x16x32_bf16 v[128:131], a[92:95], a[124:127], v[128:131]\n"
"v_mfma_f32_16x16x32_bf16 v[100:103], a[84:87], a[124:127], v[100:103]\n"
"ds_read_b128 a[124:127], v5 offset:7168\n"
"v_mfma_f32_16x16x32_bf16 v[100:103], a[80:83], a[120:123], v[100:103]\n"
"ds_read_b128 a[120:123], v5 offset:6144\n"
"v_mfma_f32_16x16x32_bf16 v[104:107], a[80:83], a[128:131], v[104:107]\n"
"v_mfma_f32_16x16x32_bf16 v[104:107], a[84:87], a[132:135], v[104:107]\n"
"global_load_dwordx4 a[72:75], v0, s[34:35] offset:2048 sc1\n"
"v_mfma_f32_16x16x32_bf16 v[132:135], a[92:95], a[132:135], v[132:135]\n"
"ds_read_b128 a[132:135], v5 offset:9216\n"
"v_mfma_f32_16x16x32_bf16 v[132:135], a[88:91], a[128:131], v[132:135]\n"
"ds_read_b128 a[128:131], v5 offset:8192\n"
"v_mfma_f32_16x16x32_bf16 v[136:139], a[88:91], a[136:139], v[136:139]\n"
"v_mfma_f32_16x16x32_bf16 v[136:139], a[92:95], a[140:143], v[136:139]\n"
"v_mfma_f32_16x16x32_bf16 v[108:111], a[84:87], a[140:143], v[108:111]\n"
"ds_read_b128 a[140:143], v5 offset:11264\n"
"v_mfma_f32_16x16x32_bf16 v[108:111], a[80:83], a[136:139], v[108:111]\n"
"ds_read_b128 a[136:139], v5 offset:10240\n"
"v_mfma_f32_16x16x32_bf16 v[112:115], a[80:83], a[144:147], v[112:115]\n"
"v_mfma_f32_16x16x32_bf16 v[112:115], a[84:87], a[148:151], v[112:115]\n"
"global_load_dwordx4 a[76:79], v0, s[34:35] offset:3072 sc1\n"
"v_mfma_f32_16x16x32_bf16 v[140:143], a[92:95], a[148:151], v[140:143]\n"
"ds_read_b128 a[148:151], v5 offset:13312\n"
"v_mfma_f32_16x16x32_bf16 v[140:143], a[88:91], a[144:147], v[140:143]\n"
"ds_read_b128 a[144:147], v5 offset:12288\n"
"v_mfma_f32_16x16x32_bf16 a[12:15], a[88:91], a[152:155], a[12:15]\n"
"v_mfma_f32_16x16x32_bf16 a[12:15], a[92:95], a[156:159], a[12:15]\n"
"v_mfma_f32_16x16x32_bf16 a[8:11], a[84:87], a[156:159], a[8:11]\n"
"ds_read_b128 a[156:159], v5 offset:15360\n"
"v_mfma_f32_16x16x32_bf16 a[8:11], a[80:83], a[152:155], a[8:11]\n"
"s_waitcnt vmcnt(10) lgkmcnt(11)\n"
"ds_read_b128 a[152:155], v5 offset:14336\n"
"v_mfma_f32_16x16x32_bf16 v[32:35], a[32:35], a[96:99], v[32:35]\n"
"s_add_u32 s34, s36, 1511424\n"
"v_mfma_f32_16x16x32_bf16 v[32:35], a[36:39], a[100:103], v[32:35]\n"
"global_load_dwordx4 a[80:83], v0, s[34:35] offset:0 sc1\n"
"v_mfma_f32_16x16x32_bf16 v[60:63], a[44:47], a[100:103], v[60:63]\n"
"v_mfma_f32_16x16x32_bf16 v[60:63], a[40:43], a[96:99], v[60:63]\n"
"v_mfma_f32_16x16x32_bf16 v[64:67], a[40:43], a[104:107], v[64:67]\n"
"v_mfma_f32_16x16x32_bf16 v[64:67], a[44:47], a[108:111], v[64:67]\n"
"v_mfma_f32_16x16x32_bf16 v[36:39], a[36:39], a[108:111], v[36:39]\n"
"v_mfma_f32_16x16x32_bf16 v[36:39], a[32:35], a[104:107], v[36:39]\n"
"s_waitcnt lgkmcnt(8)\n"
"v_mfma_f32_16x16x32_bf16 v[40:43], a[32:35], a[112:115], v[40:43]\n"
"v_mfma_f32_16x16x32_bf16 v[40:43], a[36:39], a[116:119], v[40:43]\n"
"global_load_dwordx4 a[84:87], v0, s[34:35] offset:1024 sc1\n"
"v_mfma_f32_16x16x32_bf16 v[68:71], a[44:47], a[116:119], v[68:71]\n"
"v_mfma_f32_16x16x32_bf16 v[68:71], a[40:43], a[112:115], v[68:71]\n"
"v_mfma_f32_16x16x32_bf16 v[72:75], a[40:43], a[120:123], v[72:75]\n"
"v_mfma_f32_16x16x32_bf16 v[72:75], a[44:47], a[124:127], v[72:75]\n"
"v_mfma_f32_16x16x32_bf16 v[44:47], a[36:39], a[124:127], v[44:47]\n"
"v_mfma_f32_16x16x32_bf16 v[44:47], a[32:35], a[120:123], v[44:47]\n"
"s_waitcnt lgkmcnt(4)\n"
"v_mfma_f32_16x16x32_bf16 v[48:51], a[32:35], a[128:131], v[48:51]\n"
"v_mfma_f32_16x16x32_bf16 v[48:51], a[36:39], a[132:135], v[48:51]\n"
"global_load_dwordx4 a[88:91], v0, s[34:35] offset:2048 sc1\n"
"v_mfma_f32_16x16x32_bf16 v[76:79], a[44:47], a[132:135], v[76:79]\n"
"v_mfma_f32_16x16x32_bf16 v[76:79], a[40:43], a[128:131], v[76:79]\n"
"v_mfma_f32_16x16x32_bf16 v[80:83], a[40:43], a[136:139], v[80:83]\n"
"v_mfma_f32_16x16x32_bf16 v[80:83], a[44:47], a[140:143], v[80:83]\n"
"v_mfma_f32_16x16x32_bf16 v[52:55], a[36:39], a[140:143], v[52:55]\n"
"v_mfma_f32_16x16x32_bf16 v[52:55], a[32:35], a[136:139], v[52:55]\n"
"s_waitcnt lgkmcnt(0)\n"
"v_mfma_f32_16x16x32_bf16 v[56:59], a[32:35], a[144:147], v[56:59]\n"
"v_mfma_f32_16x16x32_bf16 v[56:59], a[36:39], a[148:151], v[56:59]\n"
"global_load_dwordx4 a[92:95], v0, s[34:35] offset:3072 sc1\n"
"v_mfma_f32_16x16x32_bf16 v[84:87], a[44:47], a[148:151], v[84:87]\n"
"v_mfma_f32_16x16x32_bf16 v[84:87], a[40:43], a[144:147], v[84:87]\n"
"v_mfma_f32_16x16x32_bf16 a[4:7], a[40:43], a[152:155], a[4:7]\n"
"v_mfma_f32_16x16x32_bf16 a[4:7], a[44:47], a[156:159], a[4:7]\n"
"v_mfma_f32_16x16x32_bf16 a[0:3], a[36:39], a[156:159], a[0:3]\n"
"v_mfma_f32_16x16x32_bf16 a[0:3], a[32:35], a[152:155], a[0:3]\n"
"s_waitcnt vmcnt(8)\n"
"v_mfma_f32_16x16x32_bf16 v[88:91], a[48:51], a[96:99], v[88:91]\n"
"s_add_u32 s34, s36, 1515520\n"
"v_mfma_f32_16x16x32_bf16 v[88:91], a[52:55], a[100:103], v[88:91]\n"
"global_load_dwordx4 a[32:35], v0, s[34:35] offset:0 sc1\n"
"v_mfma_f32_16x16x32_bf16 v[116:119], a[60:63], a[100:103], v[116:119]\n"
"ds_read_b128 a[100:103], v2 offset:17408\n"
"v_mfma_f32_16x16x32_bf16 v[116:119], a[56:59], a[96:99], v[116:119]\n"
"ds_read_b128 a[96:99], v2 offset:16384\n"
"v_mfma_f32_16x16x32_bf16 v[120:123], a[56:59], a[104:107], v[120:123]\n"
"v_mfma_f32_16x16x32_bf16 v[120:123], a[60:63], a[108:111], v[120:123]\n"
"v_mfma_f32_16x16x32_bf16 v[92:95], a[52:55], a[108:111], v[92:95]\n"
"ds_read_b128 a[108:111], v2 offset:19456\n"
"v_mfma_f32_16x16x32_bf16 v[92:95], a[48:51], a[104:107], v[92:95]\n"
"ds_read_b128 a[104:107], v2 offset:18432\n"
"v_mfma_f32_16x16x32_bf16 v[96:99], a[48:51], a[112:115], v[96:99]\n"
"v_mfma_f32_16x16x32_bf16 v[96:99], a[52:55], a[116:119], v[96:99]\n"
"global_load_dwordx4 a[36:39], v0, s[34:35] offset:1024 sc1\n"
"v_mfma_f32_16x16x32_bf16 v[124:127], a[60:63], a[116:119], v[124:127]\n"
"ds_read_b128 a[116:119], v2 offset:21504\n"
"v_mfma_f32_16x16x32_bf16 v[124:127], a[56:59], a[112:115], v[124:127]\n"
"ds_read_b128 a[112:115], v2 offset:20480\n"
"v_mfma_f32_16x16x32_bf16 v[128:131], a[56:59], a[120:123], v[128:131]\n"
"v_mfma_f32_16x16x32_bf16 v[128:131], a[60:63], a[124:127], v[128:131]\n"
"v_mfma_f32_16x16x32_bf16 v[100:103], a[52:55], a[124:127], v[100:103]\n"
"ds_read_b128 a[124:127], v2 offset:23552\n"
"v_mfma_f32_16x16x32_bf16 v[100:103], a[48:51], a[120:123], v[100:103]\n"
"ds_read_b128 a[120:123], v2 offset:22528\n"
"v_mfma_f32_16x16x32_bf16 v[104:107], a[48:51], a[128:131], v[104:107]\n"
"v_mfma_f32_16x16x32_bf16 v[104:107], a[52:55], a[132:135], v[104:107]\n"
"global_load_dwordx4 a[40:43], v0, s[34:35] offset:2048 sc1\n"
"v_mfma_f32_16x16x32_bf16 v[132:135], a[60:63], a[132:135], v[132:135]\n"
"ds_read_b128 a[132:135], v2 offset:25600\n"
"v_mfma_f32_16x16x32_bf16 v[132:135], a[56:59], a[128:131], v[132:135]\n"
"ds_read_b128 a[128:131], v2 offset:24576\n"
"v_mfma_f32_16x16x32_bf16 v[136:139], a[56:59], a[136:139], v[136:139]\n"
"v_mfma_f32_16x16x32_bf16 v[136:139], a[60:63], a[140:143], v[136:139]\n"
"v_mfma_f32_16x16x32_bf16 v[108:111], a[52:55], a[140:143], v[108:111]\n"
"ds_read_b128 a[140:143], v2 offset:27648\n"
"v_mfma_f32_16x16x32_bf16 v[108:111], a[48:51], a[136:139], v[108:111]\n"
"ds_read_b128 a[136:139], v2 offset:26624\n"
"v_mfma_f32_16x16x32_bf16 v[112:115], a[48:51], a[144:147], v[112:115]\n"
"v_mfma_f32_16x16x32_bf16 v[112:115], a[52:55], a[148:151], v[112:115]\n"
"global_load_dwordx4 a[44:47], v0, s[34:35] offset:3072 sc1\n"
"v_mfma_f32_16x16x32_bf16 v[140:143], a[60:63], a[148:151], v[140:143]\n"
"ds_read_b128 a[148:151], v2 offset:29696\n"
"v_mfma_f32_16x16x32_bf16 v[140:143], a[56:59], a[144:147], v[140:143]\n"
"ds_read_b128 a[144:147], v2 offset:28672\n"
"v_mfma_f32_16x16x32_bf16 a[12:15], a[56:59], a[152:155], a[12:15]\n"
"v_mfma_f32_16x16x32_bf16 a[12:15], a[60:63], a[156:159], a[12:15]\n"
"v_mfma_f32_16x16x32_bf16 a[8:11], a[52:55], a[156:159], a[8:11]\n"
"ds_read_b128 a[156:159], v2 offset:31744\n"
"v_mfma_f32_16x16x32_bf16 a[8:11], a[48:51], a[152:155], a[8:11]\n"
"s_waitcnt vmcnt(8) lgkmcnt(11)\n"
"ds_read_b128 a[152:155], v2 offset:30720\n"
"v_mfma_f32_16x16x32_bf16 v[32:35], a[64:67], a[96:99], v[32:35]\n"
"s_add_u32 s34, s36, 1519616\n"
"v_mfma_f32_16x16x32_bf16 v[32:35], a[68:71], a[100:103], v[32:35]\n"
"global_load_dwordx4 a[48:51], v0, s[34:35] offset:0 sc1\n"
"v_mfma_f32_16x16x32_bf16 v[60:63], a[76:79], a[100:103], v[60:63]\n"
"s_barrier\n"
"v_mfma_f32_16x16x32_bf16 v[60:63], a[72:75], a[96:99], v[60:63]\n"
"v_mfma_f32_16x16x32_bf16 v[64:67], a[72:75], a[104:107], v[64:67]\n"
"v_mfma_f32_16x16x32_bf16 v[64:67], a[76:79], a[108:111], v[64:67]\n"
"v_mfma_f32_16x16x32_bf16 v[36:39], a[68:71], a[108:111], v[36:39]\n"
"v_mfma_f32_16x16x32_bf16 v[36:39], a[64:67], a[104:107], v[36:39]\n"
"s_waitcnt lgkmcnt(8)\n"
"v_mfma_f32_16x16x32_bf16 v[40:43], a[64:67], a[112:115], v[40:43]\n"
"v_mfma_f32_16x16x32_bf16 v[40:43], a[68:71], a[116:119], v[40:43]\n"
"global_load_dwordx4 a[52:55], v0, s[34:35] offset:1024 sc1\n"
"v_mfma_f32_16x16x32_bf16 v[68:71], a[76:79], a[116:119], v[68:71]\n"
"v_mfma_f32_16x16x32_bf16 v[68:71], a[72:75], a[112:115], v[68:71]\n"
"v_mfma_f32_16x16x32_bf16 v[72:75], a[72:75], a[120:123], v[72:75]\n"
"v_mfma_f32_16x16x32_bf16 v[72:75], a[76:79], a[124:127], v[72:75]\n"
"v_mfma_f32_16x16x32_bf16 v[44:47], a[68:71], a[124:127], v[44:47]\n"
"v_mfma_f32_16x16x32_bf16 v[44:47], a[64:67], a[120:123], v[44:47]\n"
"s_waitcnt lgkmcnt(4)\n"
"v_mfma_f32_16x16x32_bf16 v[48:51], a[64:67], a[128:131], v[48:51]\n"
"v_mfma_f32_16x16x32_bf16 v[48:51], a[68:71], a[132:135], v[48:51]\n"
"global_load_dwordx4 a[56:59], v0, s[34:35] offset:2048 sc1\n"
"v_mfma_f32_16x16x32_bf16 v[76:79], a[76:79], a[132:135], v[76:79]\n"
"v_mfma_f32_16x16x32_bf16 v[76:79], a[72:75], a[128:131], v[76:79]\n"
"v_mfma_f32_16x16x32_bf16 v[80:83], a[72:75], a[136:139], v[80:83]\n"
"v_lshlrev_b32 v16, 16, v18\n"
"v_mfma_f32_16x16x32_bf16 v[80:83], a[76:79], a[140:143], v[80:83]\n"
"v_and_b32 v17, s63, v18\n"
"v_mfma_f32_16x16x32_bf16 v[52:55], a[68:71], a[140:143], v[52:55]\n"
"v_lshlrev_b32 v18, 16, v19\n"
"v_mfma_f32_16x16x32_bf16 v[52:55], a[64:67], a[136:139], v[52:55]\n"
"s_waitcnt lgkmcnt(0)\n"
"v_and_b32 v19, s63, v19\n"
"v_add_f32 v144, v144, v16\n"
"v_mfma_f32_16x16x32_bf16 v[56:59], a[64:67], a[144:147], v[56:59]\n"
"v_add_f32 v145, v145, v17\n"
"v_mfma_f32_16x16x32_bf16 v[56:59], a[68:71], a[148:151], v[56:59]\n"
"global_load_dwordx4 a[60:63], v0, s[34:35] offset:3072 sc1\n"
"v_add_f32 v146, v146, v18\n"
"v_mfma_f32_16x16x32_bf16 v[84:87], a[76:79], a[148:151], v[84:87]\n"
"v_add_f32 v147, v147, v19\n"
"v_mfma_f32_16x16x32_bf16 v[84:87], a[72:75], a[144:147], v[84:87]\n"
"v_add_f32 v148, v148, v16\n"
"v_mfma_f32_16x16x32_bf16 a[4:7], a[72:75], a[152:155], a[4:7]\n"
"v_add_f32 v149, v149, v17\n"
"v_mfma_f32_16x16x32_bf16 a[4:7], a[76:79], a[156:159], a[4:7]\n"
"v_add_f32 v150, v150, v18\n"
"v_mfma_f32_16x16x32_bf16 a[0:3], a[68:71], a[156:159], a[0:3]\n"
"v_add_f32 v151, v151, v19\n"
"v_mfma_f32_16x16x32_bf16 a[0:3], a[64:67], a[152:155], a[0:3]\n"
"s_waitcnt vmcnt(8)\n"
"v_add_f32 v152, v152, v16\n"
"v_add_f32 v153, v153, v17\n"
"v_mfma_f32_16x16x32_bf16 v[88:91], a[80:83], a[96:99], v[88:91]\n"
"s_add_u32 s34, s36, 1523712\n"
"v_add_f32 v154, v154, v18\n"
"v_mfma_f32_16x16x32_bf16 v[88:91], a[84:87], a[100:103], v[88:91]\n"
"global_load_dwordx4 a[64:67], v0, s[34:35] offset:0 sc1\n"
"v_add_f32 v155, v155, v19\n"
"v_mfma_f32_16x16x32_bf16 v[116:119], a[92:95], a[100:103], v[116:119]\n"
"ds_read_b128 a[100:103], v3 offset:17408\n"
"v_add_f32 v156, v156, v16\n"
"v_mfma_f32_16x16x32_bf16 v[116:119], a[88:91], a[96:99], v[116:119]\n"
"ds_read_b128 a[96:99], v3 offset:16384\n"
"v_add_f32 v157, v157, v17\n"
"v_mfma_f32_16x16x32_bf16 v[120:123], a[88:91], a[104:107], v[120:123]\n"
"v_add_f32 v158, v158, v18\n"
"v_mfma_f32_16x16x32_bf16 v[120:123], a[92:95], a[108:111], v[120:123]\n"
"v_add_f32 v159, v159, v19\n"
"v_add_f32 v160, v160, v16\n"
"v_mfma_f32_16x16x32_bf16 v[92:95], a[84:87], a[108:111], v[92:95]\n"
"ds_read_b128 a[108:111], v3 offset:19456\n"
"v_add_f32 v161, v161, v17\n"
"v_mfma_f32_16x16x32_bf16 v[92:95], a[80:83], a[104:107], v[92:95]\n"
"ds_read_b128 a[104:107], v3 offset:18432\n"
"v_add_f32 v162, v162, v18\n"
"v_mfma_f32_16x16x32_bf16 v[96:99], a[80:83], a[112:115], v[96:99]\n"
"v_add_f32 v163, v163, v19\n"
"v_mfma_f32_16x16x32_bf16 v[96:99], a[84:87], a[116:119], v[96:99]\n"
"global_load_dwordx4 a[68:71], v0, s[34:35] offset:1024 sc1\n"
"v_add_f32 v164, v164, v16\n"
"v_mfma_f32_16x16x32_bf16 v[124:127], a[92:95], a[116:119], v[124:127]\n"
"ds_read_b128 a[116:119], v3 offset:21504\n"
"v_add_f32 v165, v165, v17\n"
"v_mfma_f32_16x16x32_bf16 v[124:127], a[88:91], a[112:115], v[124:127]\n"
"ds_read_b128 a[112:115], v3 offset:20480\n"
"v_add_f32 v166, v166, v18\n"
"v_mfma_f32_16x16x32_bf16 v[128:131], a[88:91], a[120:123], v[128:131]\n"
"v_add_f32 v167, v167, v19\n"
"v_mfma_f32_16x16x32_bf16 v[128:131], a[92:95], a[124:127], v[128:131]\n"
"v_add_f32 v168, v168, v16\n"
"v_add_f32 v169, v169, v17\n"
"v_mfma_f32_16x16x32_bf16 v[100:103], a[84:87], a[124:127], v[100:103]\n"
"ds_read_b128 a[124:127], v3 offset:23552\n"
"v_add_f32 v170, v170, v18\n"
"v_mfma_f32_16x16x32_bf16 v[100:103], a[80:83], a[120:123], v[100:103]\n"
"ds_read_b128 a[120:123], v3 offset:22528\n"
"v_add_f32 v171, v171, v19\n"
"v_mfma_f32_16x16x32_bf16 v[104:107], a[80:83], a[128:131], v[104:107]\n"
"v_accvgpr_read_b32 v12, a16\n"
"v_mfma_f32_16x16x32_bf16 v[104:107], a[84:87], a[132:135], v[104:107]\n"
"global_load_dwordx4 a[72:75], v0, s[34:35] offset:2048 sc1\n"
"v_accvgpr_read_b32 v13, a17\n"
"v_mfma_f32_16x16x32_bf16 v[132:135], a[92:95], a[132:135], v[132:135]\n"
"ds_read_b128 a[132:135], v3 offset:25600\n"
"v_accvgpr_read_b32 v14, a18\n"
"v_mfma_f32_16x16x32_bf16 v[132:135], a[88:91], a[128:131], v[132:135]\n"
"ds_read_b128 a[128:131], v3 offset:24576\n"
"v_accvgpr_read_b32 v15, a19\n"
"v_mfma_f32_16x16x32_bf16 v[136:139], a[88:91], a[136:139], v[136:139]\n"
"v_add_f32 v12, v12, v16\n"
"v_mfma_f32_16x16x32_bf16 v[136:139], a[92:95], a[140:143], v[136:139]\n"
"v_add_f32 v13, v13, v17\n"
"v_add_f32 v14, v14, v18\n"
"v_mfma_f32_16x16x32_bf16 v[108:111], a[84:87], a[140:143], v[108:111]\n"
"ds_read_b128 a[140:143], v3 offset:27648\n"
"v_add_f32 v15, v15, v19\n"
"v_mfma_f32_16x16x32_bf16 v[108:111], a[80:83], a[136:139], v[108:111]\n"
"ds_read_b128 a[136:139], v3 offset:26624\n"
"v_accvgpr_write_b32 a16, v12\n"
"v_mfma_f32_16x16x32_bf16 v[112:115], a[80:83], a[144:147], v[112:115]\n"
"v_accvgpr_write_b32 a17, v13\n"
"v_mfma_f32_16x16x32_bf16 v[112:115], a[84:87], a[148:151], v[112:115]\n"
"global_load_dwordx4 a[76:79], v0, s[34:35] offset:3072 sc1\n"
"v_accvgpr_write_b32 a18, v14\n"
"v_mfma_f32_16x16x32_bf16 v[140:143], a[92:95], a[148:151], v[140:143]\n"
"ds_read_b128 a[148:151], v3 offset:29696\n"
"v_accvgpr_write_b32 a19, v15\n"
"v_mfma_f32_16x16x32_bf16 v[140:143], a[88:91], a[144:147], v[140:143]\n"
"ds_read_b128 a[144:147], v3 offset:28672\n"
"v_lshlrev_b32 v20, 16, v22\n"
"v_mfma_f32_16x16x32_bf16 a[12:15], a[88:91], a[152:155], a[12:15]\n"
"v_and_b32 v21, s63, v22\n"
"v_lshlrev_b32 v22, 16, v23\n"
"v_mfma_f32_16x16x32_bf16 a[12:15], a[92:95], a[156:159], a[12:15]\n"
"v_and_b32 v23, s63, v23\n"
"v_mfma_f32_16x16x32_bf16 a[8:11], a[84:87], a[156:159], a[8:11]\n"
"ds_read_b128 a[156:159], v3 offset:31744\n"
"v_add_f32 v172, v172, v20\n"
"v_mfma_f32_16x16x32_bf16 a[8:11], a[80:83], a[152:155], a[8:11]\n"
"s_waitcnt vmcnt(8) lgkmcnt(11)\n"
"ds_read_b128 a[152:155], v3 offset:30720\n"
"v_add_f32 v173, v173, v21\n"
"v_mfma_f32_16x16x32_bf16 v[32:35], a[32:35], a[96:99], v[32:35]\n"
"s_add_u32 s34, s36, 1527808\n"
"v_add_f32 v174, v174, v22\n"
"v_mfma_f32_16x16x32_bf16 v[32:35], a[36:39], a[100:103], v[32:35]\n"
"global_load_dwordx4 a[80:83], v0, s[34:35] offset:0 sc1\n"
"v_add_f32 v175, v175, v23\n"
"v_mfma_f32_16x16x32_bf16 v[60:63], a[44:47], a[100:103], v[60:63]\n"
"v_add_f32 v176, v176, v20\n"
"v_mfma_f32_16x16x32_bf16 v[60:63], a[40:43], a[96:99], v[60:63]\n"
"v_add_f32 v177, v177, v21\n"
"v_add_f32 v178, v178, v22\n"
"v_mfma_f32_16x16x32_bf16 v[64:67], a[40:43], a[104:107], v[64:67]\n"
"v_add_f32 v179, v179, v23\n"
"v_mfma_f32_16x16x32_bf16 v[64:67], a[44:47], a[108:111], v[64:67]\n"
"v_add_f32 v180, v180, v20\n"
"v_mfma_f32_16x16x32_bf16 v[36:39], a[36:39], a[108:111], v[36:39]\n"
"v_add_f32 v181, v181, v21\n"
"v_mfma_f32_16x16x32_bf16 v[36:39], a[32:35], a[104:107], v[36:39]\n"
"s_waitcnt lgkmcnt(8)\n"
"v_add_f32 v182, v182, v22\n"
"v_mfma_f32_16x16x32_bf16 v[40:43], a[32:35], a[112:115], v[40:43]\n"
"v_add_f32 v183, v183, v23\n"
"v_mfma_f32_16x16x32_bf16 v[40:43], a[36:39], a[116:119], v[40:43]\n"
"global_load_dwordx4 a[84:87], v0, s[34:35] offset:1024 sc1\n"
"v_add_f32 v184, v184, v20\n"
"v_mfma_f32_16x16x32_bf16 v[68:71], a[44:47], a[116:119], v[68:71]\n"
"v_add_f32 v185, v185, v21\n"
"v_mfma_f32_16x16x32_bf16 v[68:71], a[40:43], a[112:115], v[68:71]\n"
"v_add_f32 v186, v186, v22\n"
"v_add_f32 v187, v187, v23\n"
"v_mfma_f32_16x16x32_bf16 v[72:75], a[40:43], a[120:123], v[72:75]\n"
"v_add_f32 v188, v188, v20\n"
"v_mfma_f32_16x16x32_bf16 v[72:75], a[44:47], a[124:127], v[72:75]\n"
"v_add_f32 v189, v189, v21\n"
"v_mfma_f32_16x16x32_bf16 v[44:47], a[36:39], a[124:127], v[44:47]\n"
"v_add_f32 v190, v190, v22\n"
"v_mfma_f32_16x16x32_bf16 v[44:47], a[32:35], a[120:123], v[44:47]\n"
"s_waitcnt lgkmcnt(4)\n"
"v_add_f32 v191, v191, v23\n"
"v_mfma_f32_16x16x32_bf16 v[48:51], a[32:35], a[128:131], v[48:51]\n"
"v_add_f32 v192, v192, v20\n"
"v_mfma_f32_16x16x32_bf16 v[48:51], a[36:39], a[132:135], v[48:51]\n"
"global_load_dwordx4 a[88:91], v0, s[34:35] offset:2048 sc1\n"
"v_add_f32 v193, v193, v21\n"
"v_mfma_f32_16x16x32_bf16 v[76:79], a[44:47], a[132:135], v[76:79]\n"
"v_add_f32 v194, v194, v22\n"
"v_add_f32 v195, v195, v23\n"
"v_mfma_f32_16x16x32_bf16 v[76:79], a[40:43], a[128:131], v[76:79]\n"
"v_add_f32 v196, v196, v20\n"
"v_mfma_f32_16x16x32_bf16 v[80:83], a[40:43], a[136:139], v[80:83]\n"
"v_add_f32 v197, v197, v21\n"
"v_mfma_f32_16x16x32_bf16 v[80:83], a[44:47], a[140:143], v[80:83]\n"
"v_add_f32 v198, v198, v22\n"
"v_mfma_f32_16x16x32_bf16 v[52:55], a[36:39], a[140:143], v[52:55]\n"
"v_add_f32 v199, v199, v23\n"
"v_mfma_f32_16x16x32_bf16 v[52:55], a[32:35], a[136:139], v[52:55]\n"
"s_waitcnt lgkmcnt(0)\n"
"v_accvgpr_read_b32 v12, a20\n"
"v_mfma_f32_16x16x32_bf16 v[56:59], a[32:35], a[144:147], v[56:59]\n"
"v_accvgpr_read_b32 v13, a21\n"
"v_mfma_f32_16x16x32_bf16 v[56:59], a[36:39], a[148:151], v[56:59]\n"
"global_load_dwordx4 a[92:95], v0, s[34:35] offset:3072 sc1\n"
"v_accvgpr_read_b32 v14, a22\n"
"v_mfma_f32_16x16x32_bf16 v[84:87], a[44:47], a[148:151], v[84:87]\n"
"v_accvgpr_read_b32 v15, a23\n"
"v_add_f32 v12, v12, v20\n"
"v_mfma_f32_16x16x32_bf16 v[84:87], a[40:43], a[144:147], v[84:87]\n"
"v_add_f32 v13, v13, v21\n"
"v_mfma_f32_16x16x32_bf16 a[4:7], a[40:43], a[152:155], a[4:7]\n"
"v_add_f32 v14, v14, v22\n"
"v_mfma_f32_16x16x32_bf16 a[4:7], a[44:47], a[156:159], a[4:7]\n"
"v_add_f32 v15, v15, v23\n"
"v_mfma_f32_16x16x32_bf16 a[0:3], a[36:39], a[156:159], a[0:3]\n"
"v_accvgpr_write_b32 a20, v12\n"
"v_mfma_f32_16x16x32_bf16 a[0:3], a[32:35], a[152:155], a[0:3]\n"
"s_waitcnt vmcnt(8)\n"
"v_accvgpr_write_b32 a21, v13\n"
"v_mfma_f32_16x16x32_bf16 v[88:91], a[48:51], a[96:99], v[88:91]\n"
"s_add_u32 s34, s36, 1531904\n"
"v_accvgpr_write_b32 a22, v14\n"
"v_accvgpr_write_b32 a23, v15\n"
"v_mfma_f32_16x16x32_bf16 v[88:91], a[52:55], a[100:103], v[88:91]\n"
"global_load_dwordx4 a[32:35], v0, s[34:35] offset:0 sc1\n"
"v_lshlrev_b32 v24, 16, v26\n"
"v_mfma_f32_16x16x32_bf16 v[116:119], a[60:63], a[100:103], v[116:119]\n"
"ds_read_b128 a[100:103], v4 offset:17408\n"
"v_and_b32 v25, s63, v26\n"
"v_mfma_f32_16x16x32_bf16 v[116:119], a[56:59], a[96:99], v[116:119]\n"
"ds_read_b128 a[96:99], v4 offset:16384\n"
"v_lshlrev_b32 v26, 16, v27\n"
"v_mfma_f32_16x16x32_bf16 v[120:123], a[56:59], a[104:107], v[120:123]\n"
"v_and_b32 v27, s63, v27\n"
"v_mfma_f32_16x16x32_bf16 v[120:123], a[60:63], a[108:111], v[120:123]\n"
"v_add_f32 v200, v200, v24\n"
"v_mfma_f32_16x16x32_bf16 v[92:95], a[52:55], a[108:111], v[92:95]\n"
"ds_read_b128 a[108:111], v4 offset:19456\n"
"v_add_f32 v201, v201, v25\n"
"v_mfma_f32_16x16x32_bf16 v[92:95], a[48:51], a[104:107], v[92:95]\n"
"ds_read_b128 a[104:107], v4 offset:18432\n"
"v_add_f32 v202, v202, v26\n"
"v_mfma_f32_16x16x32_bf16 v[96:99], a[48:51], a[112:115], v[96:99]\n"
"v_add_f32 v203, v203, v27\n"
"v_add_f32 v204, v204, v24\n"
"v_mfma_f32_16x16x32_bf16 v[96:99], a[52:55], a[116:119], v[96:99]\n"
"global_load_dwordx4 a[36:39], v0, s[34:35] offset:1024 sc1\n"
"v_add_f32 v205, v205, v25\n"
"v_mfma_f32_16x16x32_bf16 v[124:127], a[60:63], a[116:119], v[124:127]\n"
"ds_read_b128 a[116:119], v4 offset:21504\n"
"v_add_f32 v206, v206, v26\n"
"v_mfma_f32_16x16x32_bf16 v[124:127], a[56:59], a[112:115], v[124:127]\n"
"ds_read_b128 a[112:115], v4 offset:20480\n"
"v_add_f32 v207, v207, v27\n"
"v_mfma_f32_16x16x32_bf16 v[128:131], a[56:59], a[120:123], v[128:131]\n"
"v_add_f32 v208, v208, v24\n"
"v_mfma_f32_16x16x32_bf16 v[128:131], a[60:63], a[124:127], v[128:131]\n"
"v_add_f32 v209, v209, v25\n"
"v_mfma_f32_16x16x32_bf16 v[100:103], a[52:55], a[124:127], v[100:103]\n"
"ds_read_b128 a[124:127], v4 offset:23552\n"
"v_add_f32 v210, v210, v26\n"
"v_mfma_f32_16x16x32_bf16 v[100:103], a[48:51], a[120:123], v[100:103]\n"
"ds_read_b128 a[120:123], v4 offset:22528\n"
"v_add_f32 v211, v211, v27\n"
"v_mfma_f32_16x16x32_bf16 v[104:107], a[48:51], a[128:131], v[104:107]\n"
"v_add_f32 v212, v212, v24\n"
"v_add_f32 v213, v213, v25\n"
"v_mfma_f32_16x16x32_bf16 v[104:107], a[52:55], a[132:135], v[104:107]\n"
"global_load_dwordx4 a[40:43], v0, s[34:35] offset:2048 sc1\n"
"v_add_f32 v214, v214, v26\n"
"v_mfma_f32_16x16x32_bf16 v[132:135], a[60:63], a[132:135], v[132:135]\n"
"ds_read_b128 a[132:135], v4 offset:25600\n"
"v_add_f32 v215, v215, v27\n"
"v_mfma_f32_16x16x32_bf16 v[132:135], a[56:59], a[128:131], v[132:135]\n"
"ds_read_b128 a[128:131], v4 offset:24576\n"
"v_add_f32 v216, v216, v24\n"
"v_mfma_f32_16x16x32_bf16 v[136:139], a[56:59], a[136:139], v[136:139]\n"
"v_add_f32 v217, v217, v25\n"
"v_mfma_f32_16x16x32_bf16 v[136:139], a[60:63], a[140:143], v[136:139]\n"
"v_add_f32 v218, v218, v26\n"
"v_mfma_f32_16x16x32_bf16 v[108:111], a[52:55], a[140:143], v[108:111]\n"
"ds_read_b128 a[140:143], v4 offset:27648\n"
"v_add_f32 v219, v219, v27\n"
"v_mfma_f32_16x16x32_bf16 v[108:111], a[48:51], a[136:139], v[108:111]\n"
"ds_read_b128 a[136:139], v4 offset:26624\n"
"v_add_f32 v220, v220, v24\n"
"v_mfma_f32_16x16x32_bf16 v[112:115], a[48:51], a[144:147], v[112:115]\n"
"v_add_f32 v221, v221, v25\n"
"v_add_f32 v222, v222, v26\n"
"v_mfma_f32_16x16x32_bf16 v[112:115], a[52:55], a[148:151], v[112:115]\n"
"global_load_dwordx4 a[44:47], v0, s[34:35] offset:3072 sc1\n"
"v_add_f32 v223, v223, v27\n"
"v_mfma_f32_16x16x32_bf16 v[140:143], a[60:63], a[148:151], v[140:143]\n"
"ds_read_b128 a[148:151], v4 offset:29696\n"
"v_add_f32 v224, v224, v24\n"
"v_mfma_f32_16x16x32_bf16 v[140:143], a[56:59], a[144:147], v[140:143]\n"
"ds_read_b128 a[144:147], v4 offset:28672\n"
"v_add_f32 v225, v225, v25\n"
"v_mfma_f32_16x16x32_bf16 a[12:15], a[56:59], a[152:155], a[12:15]\n"
"v_add_f32 v226, v226, v26\n"
"v_mfma_f32_16x16x32_bf16 a[12:15], a[60:63], a[156:159], a[12:15]\n"
"v_add_f32 v227, v227, v27\n"
"v_mfma_f32_16x16x32_bf16 a[8:11], a[52:55], a[156:159], a[8:11]\n"
"ds_read_b128 a[156:159], v4 offset:31744\n"
"v_accvgpr_read_b32 v12, a24\n"
"v_mfma_f32_16x16x32_bf16 a[8:11], a[48:51], a[152:155], a[8:11]\n"
"s_waitcnt vmcnt(8) lgkmcnt(11)\n"
"ds_read_b128 a[152:155], v4 offset:30720\n"
"v_accvgpr_read_b32 v13, a25\n"
"v_mfma_f32_16x16x32_bf16 v[32:35], a[64:67], a[96:99], v[32:35]\n"
"s_add_u32 s34, s36, 1536000\n"
"v_accvgpr_read_b32 v14, a26\n"
"v_accvgpr_read_b32 v15, a27\n"
"v_mfma_f32_16x16x32_bf16 v[32:35], a[68:71], a[100:103], v[32:35]\n"
"global_load_dwordx4 a[48:51], v0, s[34:35] offset:0 sc1\n"
"v_add_f32 v12, v12, v24\n"
"v_mfma_f32_16x16x32_bf16 v[60:63], a[76:79], a[100:103], v[60:63]\n"
"v_add_f32 v13, v13, v25\n"
"v_mfma_f32_16x16x32_bf16 v[60:63], a[72:75], a[96:99], v[60:63]\n"
"v_add_f32 v14, v14, v26\n"
"v_mfma_f32_16x16x32_bf16 v[64:67], a[72:75], a[104:107], v[64:67]\n"
"v_add_f32 v15, v15, v27\n"
"v_mfma_f32_16x16x32_bf16 v[64:67], a[76:79], a[108:111], v[64:67]\n"
"v_accvgpr_write_b32 a24, v12\n"
"v_mfma_f32_16x16x32_bf16 v[36:39], a[68:71], a[108:111], v[36:39]\n"
"v_accvgpr_write_b32 a25, v13\n"
"v_accvgpr_write_b32 a26, v14\n"
"v_mfma_f32_16x16x32_bf16 v[36:39], a[64:67], a[104:107], v[36:39]\n"
"s_waitcnt lgkmcnt(8)\n"
"v_accvgpr_write_b32 a27, v15\n"
"v_mfma_f32_16x16x32_bf16 v[40:43], a[64:67], a[112:115], v[40:43]\n"
"v_lshlrev_b32 v28, 16, v30\n"
"v_mfma_f32_16x16x32_bf16 v[40:43], a[68:71], a[116:119], v[40:43]\n"
"global_load_dwordx4 a[52:55], v0, s[34:35] offset:1024 sc1\n"
"v_and_b32 v29, s63, v30\n"
"v_mfma_f32_16x16x32_bf16 v[68:71], a[76:79], a[116:119], v[68:71]\n"
"v_lshlrev_b32 v30, 16, v31\n"
"v_mfma_f32_16x16x32_bf16 v[68:71], a[72:75], a[112:115], v[68:71]\n"
"v_and_b32 v31, s63, v31\n"
"v_mfma_f32_16x16x32_bf16 v[72:75], a[72:75], a[120:123], v[72:75]\n"
"v_add_f32 v228, v228, v28\n"
"v_mfma_f32_16x16x32_bf16 v[72:75], a[76:79], a[124:127], v[72:75]\n"
"v_add_f32 v229, v229, v29\n"
"v_add_f32 v230, v230, v30\n"
"v_mfma_f32_16x16x32_bf16 v[44:47], a[68:71], a[124:127], v[44:47]\n"
"v_add_f32 v231, v231, v31\n"
"v_mfma_f32_16x16x32_bf16 v[44:47], a[64:67], a[120:123], v[44:47]\n"
"s_waitcnt lgkmcnt(4)\n"
"v_add_f32 v232, v232, v28\n"
"v_mfma_f32_16x16x32_bf16 v[48:51], a[64:67], a[128:131], v[48:51]\n"
"v_add_f32 v233, v233, v29\n"
"v_mfma_f32_16x16x32_bf16 v[48:51], a[68:71], a[132:135], v[48:51]\n"
"global_load_dwordx4 a[56:59], v0, s[34:35] offset:2048 sc1\n"
"v_add_f32 v234, v234, v30\n"
"v_mfma_f32_16x16x32_bf16 v[76:79], a[76:79], a[132:135], v[76:79]\n"
"v_add_f32 v235, v235, v31\n"
"v_mfma_f32_16x16x32_bf16 v[76:79], a[72:75], a[128:131], v[76:79]\n"
"v_add_f32 v236, v236, v28\n"
"v_mfma_f32_16x16x32_bf16 v[80:83], a[72:75], a[136:139], v[80:83]\n"
"v_add_f32 v237, v237, v29\n"
"v_mfma_f32_16x16x32_bf16 v[80:83], a[76:79], a[140:143], v[80:83]\n"
"v_add_f32 v238, v238, v30\n"
"v_add_f32 v239, v239, v31\n"
"v_mfma_f32_16x16x32_bf16 v[52:55], a[68:71], a[140:143], v[52:55]\n"
"v_add_f32 v240, v240, v28\n"
"v_mfma_f32_16x16x32_bf16 v[52:55], a[64:67], a[136:139], v[52:55]\n"
"s_waitcnt lgkmcnt(0)\n"
"v_add_f32 v241, v241, v29\n"
"v_mfma_f32_16x16x32_bf16 v[56:59], a[64:67], a[144:147], v[56:59]\n"
"v_add_f32 v242, v242, v30\n"
"v_mfma_f32_16x16x32_bf16 v[56:59], a[68:71], a[148:151], v[56:59]\n"
"global_load_dwordx4 a[60:63], v0, s[34:35] offset:3072 sc1\n"
"v_add_f32 v243, v243, v31\n"
"v_mfma_f32_16x16x32_bf16 v[84:87], a[76:79], a[148:151], v[84:87]\n"
"v_add_f32 v244, v244, v28\n"
"v_mfma_f32_16x16x32_bf16 v[84:87], a[72:75], a[144:147], v[84:87]\n"
"v_add_f32 v245, v245, v29\n"
"v_mfma_f32_16x16x32_bf16 a[4:7], a[72:75], a[152:155], a[4:7]\n"
"v_add_f32 v246, v246, v30\n"
"v_add_f32 v247, v247, v31\n"
"v_mfma_f32_16x16x32_bf16 a[4:7], a[76:79], a[156:159], a[4:7]\n"
"v_add_f32 v248, v248, v28\n"
"v_mfma_f32_16x16x32_bf16 a[0:3], a[68:71], a[156:159], a[0:3]\n"
"v_add_f32 v249, v249, v29\n"
"v_mfma_f32_16x16x32_bf16 a[0:3], a[64:67], a[152:155], a[0:3]\n"
"s_waitcnt vmcnt(8)\n"
"v_add_f32 v250, v250, v30\n"
"v_mfma_f32_16x16x32_bf16 v[88:91], a[80:83], a[96:99], v[88:91]\n"
"s_add_u32 s34, s36, 1466368\n"
"v_add_f32 v251, v251, v31\n"
"v_mfma_f32_16x16x32_bf16 v[88:91], a[84:87], a[100:103], v[88:91]\n"
"global_load_dwordx4 a[64:67], v0, s[34:35] offset:0 sc1\n"
"v_add_f32 v252, v252, v28\n"
"v_mfma_f32_16x16x32_bf16 v[116:119], a[92:95], a[100:103], v[116:119]\n"
"ds_read_b128 a[100:103], v5 offset:17408\n"
"v_add_f32 v253, v253, v29\n"
"v_mfma_f32_16x16x32_bf16 v[116:119], a[88:91], a[96:99], v[116:119]\n"
"ds_read_b128 a[96:99], v5 offset:16384\n"
"v_add_f32 v254, v254, v30\n"
"v_mfma_f32_16x16x32_bf16 v[120:123], a[88:91], a[104:107], v[120:123]\n"
"v_add_f32 v255, v255, v31\n"
"v_accvgpr_read_b32 v12, a28\n"
"v_mfma_f32_16x16x32_bf16 v[120:123], a[92:95], a[108:111], v[120:123]\n"
"v_accvgpr_read_b32 v13, a29\n"
"v_mfma_f32_16x16x32_bf16 v[92:95], a[84:87], a[108:111], v[92:95]\n"
"ds_read_b128 a[108:111], v5 offset:19456\n"
"v_accvgpr_read_b32 v14, a30\n"
"v_mfma_f32_16x16x32_bf16 v[92:95], a[80:83], a[104:107], v[92:95]\n"
"ds_read_b128 a[104:107], v5 offset:18432\n"
"v_accvgpr_read_b32 v15, a31\n"
"v_mfma_f32_16x16x32_bf16 v[96:99], a[80:83], a[112:115], v[96:99]\n"
"v_add_f32 v12, v12, v28\n"
"v_mfma_f32_16x16x32_bf16 v[96:99], a[84:87], a[116:119], v[96:99]\n"
"global_load_dwordx4 a[68:71], v0, s[34:35] offset:1024 sc1\n"
"v_add_f32 v13, v13, v29\n"
"v_mfma_f32_16x16x32_bf16 v[124:127], a[92:95], a[116:119], v[124:127]\n"
"ds_read_b128 a[116:119], v5 offset:21504\n"
"v_add_f32 v14, v14, v30\n"
"v_mfma_f32_16x16x32_bf16 v[124:127], a[88:91], a[112:115], v[124:127]\n"
"ds_read_b128 a[112:115], v5 offset:20480\n"
"v_add_f32 v15, v15, v31\n"
"v_mfma_f32_16x16x32_bf16 v[128:131], a[88:91], a[120:123], v[128:131]\n"
"v_accvgpr_write_b32 a28, v12\n"
"v_accvgpr_write_b32 a29, v13\n"
"v_mfma_f32_16x16x32_bf16 v[128:131], a[92:95], a[124:127], v[128:131]\n"
"v_accvgpr_write_b32 a30, v14\n"
"v_mfma_f32_16x16x32_bf16 v[100:103], a[84:87], a[124:127], v[100:103]\n"
"ds_read_b128 a[124:127], v5 offset:23552\n"
"v_accvgpr_write_b32 a31, v15\n"
"v_mfma_f32_16x16x32_bf16 v[100:103], a[80:83], a[120:123], v[100:103]\n"
"ds_read_b128 a[120:123], v5 offset:22528\n"
"v_mfma_f32_16x16x32_bf16 v[104:107], a[80:83], a[128:131], v[104:107]\n"
"v_mfma_f32_16x16x32_bf16 v[104:107], a[84:87], a[132:135], v[104:107]\n"
"global_load_dwordx4 a[72:75], v0, s[34:35] offset:2048 sc1\n"
"v_mfma_f32_16x16x32_bf16 v[132:135], a[92:95], a[132:135], v[132:135]\n"
"ds_read_b128 a[132:135], v5 offset:25600\n"
"v_mfma_f32_16x16x32_bf16 v[132:135], a[88:91], a[128:131], v[132:135]\n"
"ds_read_b128 a[128:131], v5 offset:24576\n"
"v_mfma_f32_16x16x32_bf16 v[136:139], a[88:91], a[136:139], v[136:139]\n"
"ds_read_b128 v[16:19], v7 offset:32768\n"
"v_mfma_f32_16x16x32_bf16 v[136:139], a[92:95], a[140:143], v[136:139]\n"
"v_mfma_f32_16x16x32_bf16 v[108:111], a[84:87], a[140:143], v[108:111]\n"
"ds_read_b128 a[140:143], v5 offset:27648\n"
"v_mfma_f32_16x16x32_bf16 v[108:111], a[80:83], a[136:139], v[108:111]\n"
"ds_read_b128 a[136:139], v5 offset:26624\n"
"v_mfma_f32_16x16x32_bf16 v[112:115], a[80:83], a[144:147], v[112:115]\n"
"v_mfma_f32_16x16x32_bf16 v[112:115], a[84:87], a[148:151], v[112:115]\n"
"global_load_dwordx4 a[76:79], v0, s[34:35] offset:3072 sc1\n"
"v_mfma_f32_16x16x32_bf16 v[140:143], a[92:95], a[148:151], v[140:143]\n"
"ds_read_b128 a[148:151], v5 offset:29696\n"
"v_mfma_f32_16x16x32_bf16 v[140:143], a[88:91], a[144:147], v[140:143]\n"
"ds_read_b128 a[144:147], v5 offset:28672\n"
"v_mfma_f32_16x16x32_bf16 a[12:15], a[88:91], a[152:155], a[12:15]\n"
"v_mfma_f32_16x16x32_bf16 a[12:15], a[92:95], a[156:159], a[12:15]\n"
"v_mfma_f32_16x16x32_bf16 a[8:11], a[84:87], a[156:159], a[8:11]\n"
"ds_read_b128 a[156:159], v5 offset:31744\n"
"v_mfma_f32_16x16x32_bf16 a[8:11], a[80:83], a[152:155], a[8:11]\n"
"s_waitcnt vmcnt(8) lgkmcnt(12)\n"
"ds_read_b128 a[152:155], v5 offset:30720\n"
"v_mfma_f32_16x16x32_bf16 v[32:35], a[32:35], a[96:99], v[32:35]\n"
"s_add_u32 s34, s36, 1470464\n"
"v_mfma_f32_16x16x32_bf16 v[32:35], a[36:39], a[100:103], v[32:35]\n"
"global_load_dwordx4 a[80:83], v0, s[34:35] offset:0 sc1\n"
"v_mfma_f32_16x16x32_bf16 v[60:63], a[44:47], a[100:103], v[60:63]\n"
"v_mfma_f32_16x16x32_bf16 v[60:63], a[40:43], a[96:99], v[60:63]\n"
"v_mfma_f32_16x16x32_bf16 v[64:67], a[40:43], a[104:107], v[64:67]\n"
"v_mfma_f32_16x16x32_bf16 v[64:67], a[44:47], a[108:111], v[64:67]\n"
"v_mfma_f32_16x16x32_bf16 v[36:39], a[36:39], a[108:111], v[36:39]\n"
"v_mfma_f32_16x16x32_bf16 v[36:39], a[32:35], a[104:107], v[36:39]\n"
"s_waitcnt lgkmcnt(9)\n"
"v_mfma_f32_16x16x32_bf16 v[40:43], a[32:35], a[112:115], v[40:43]\n"
"s_mov_b32 s52, %4\n"
"s_and_b32 s53, %5, 0xffff\n"
"v_mfma_f32_16x16x32_bf16 v[40:43], a[36:39], a[116:119], v[40:43]\n"
"global_load_dwordx4 a[84:87], v0, s[34:35] offset:1024 sc1\n"
"v_mfma_f32_16x16x32_bf16 v[68:71], a[44:47], a[116:119], v[68:71]\n"
"s_mov_b32 s54, 260352\n"
"s_mov_b32 s55, 0x20000\n"
"v_mfma_f32_16x16x32_bf16 v[68:71], a[40:43], a[112:115], v[68:71]\n"
"v_xor_b32 v8, 64, v7\n"
"v_mfma_f32_16x16x32_bf16 v[72:75], a[40:43], a[120:123], v[72:75]\n"
"s_waitcnt lgkmcnt(6)\n"
"v_lshlrev_b32 v12, 16, v16\n"
"v_mfma_f32_16x16x32_bf16 v[72:75], a[44:47], a[124:127], v[72:75]\n"
"v_and_b32 v13, s63, v16\n"
"v_lshlrev_b32 v14, 16, v17\n"
"v_mfma_f32_16x16x32_bf16 v[44:47], a[36:39], a[124:127], v[44:47]\n"
"v_and_b32 v15, s63, v17\n"
"v_mfma_f32_16x16x32_bf16 v[44:47], a[32:35], a[120:123], v[44:47]\n"
"s_waitcnt lgkmcnt(4)\n"
"v_xor_b32 v20, 64, v7\n"
"v_mfma_f32_16x16x32_bf16 v[48:51], a[32:35], a[128:131], v[48:51]\n"
"ds_read_b128 v[20:23], v20 offset:32768\n"
"v_mul_f32 v12, v12, v32\n"
"v_mfma_f32_16x16x32_bf16 v[48:51], a[36:39], a[132:135], v[48:51]\n"
"global_load_dwordx4 a[88:91], v0, s[34:35] offset:2048 sc1\n"
"v_mul_f32 v13, v13, v33\n"
"v_mfma_f32_16x16x32_bf16 v[76:79], a[44:47], a[132:135], v[76:79]\n"
"v_mul_f32 v14, v14, v34\n"
"v_mfma_f32_16x16x32_bf16 v[76:79], a[40:43], a[128:131], v[76:79]\n"
"v_mul_f32 v15, v15, v35\n"
"v_cvt_pk_bf16_f32 v24, v12, v13\n"
"v_mfma_f32_16x16x32_bf16 v[80:83], a[40:43], a[136:139], v[80:83]\n"
"v_cvt_pk_bf16_f32 v25, v14, v15\n"
"v_mfma_f32_16x16x32_bf16 v[80:83], a[44:47], a[140:143], v[80:83]\n"
"ds_write_b64 v7, v[24:25] offset:32768\n"
"v_mfma_f32_16x16x32_bf16 v[52:55], a[36:39], a[140:143], v[52:55]\n"
"v_lshlrev_b32 v12, 16, v18\n"
"v_mfma_f32_16x16x32_bf16 v[52:55], a[32:35], a[136:139], v[52:55]\n"
"s_waitcnt lgkmcnt(2)\n"
"v_and_b32 v13, s63, v18\n"
"v_lshlrev_b32 v14, 16, v19\n"
"v_mfma_f32_16x16x32_bf16 v[56:59], a[32:35], a[144:147], v[56:59]\n"
"v_and_b32 v15, s63, v19\n"
"v_mfma_f32_16x16x32_bf16 v[56:59], a[36:39], a[148:151], v[56:59]\n"
"global_load_dwordx4 a[92:95], v0, s[34:35] offset:3072 sc1\n"
"v_mul_f32 v12, v12, v60\n"
"v_mfma_f32_16x16x32_bf16 v[84:87], a[44:47], a[148:151], v[84:87]\n"
"v_mul_f32 v13, v13, v61\n"
"v_mul_f32 v14, v14, v62\n"
"v_mfma_f32_16x16x32_bf16 v[84:87], a[40:43], a[144:147], v[84:87]\n"
"v_mul_f32 v15, v15, v63\n"
"v_mfma_f32_16x16x32_bf16 a[4:7], a[40:43], a[152:155], a[4:7]\n"
"v_cvt_pk_bf16_f32 v26, v12, v13\n"
"v_mfma_f32_16x16x32_bf16 a[4:7], a[44:47], a[156:159], a[4:7]\n"
"v_cvt_pk_bf16_f32 v27, v14, v15\n"
"v_mfma_f32_16x16x32_bf16 a[0:3], a[36:39], a[156:159], a[0:3]\n"
"ds_write_b64 v7, v[26:27] offset:32776\n"
"v_mfma_f32_16x16x32_bf16 a[0:3], a[32:35], a[152:155], a[0:3]\n"
"s_waitcnt vmcnt(8) lgkmcnt(2)\n"
"v_lshlrev_b32 v12, 16, v20\n"
"v_mfma_f32_16x16x32_bf16 v[88:91], a[48:51], a[96:99], v[88:91]\n"
"s_add_u32 s34, s36, 1482752\n"
"v_and_b32 v13, s63, v20\n"
"v_mfma_f32_16x16x32_bf16 v[88:91], a[52:55], a[100:103], v[88:91]\n"
"global_load_dwordx4 a[32:35], v0, s[34:35] offset:0 sc1\n"
"v_mfma_f32_16x16x32_bf16 v[116:119], a[60:63], a[100:103], v[116:119]\n"
"ds_read_b128 a[100:103], v3 offset:1024\n"
"v_mfma_f32_16x16x32_bf16 v[116:119], a[56:59], a[96:99], v[116:119]\n"
"ds_read_b128 a[96:99], v3 offset:0\n"
"v_mfma_f32_16x16x32_bf16 v[120:123], a[56:59], a[104:107], v[120:123]\n"
"v_lshlrev_b32 v14, 16, v21\n"
"v_mfma_f32_16x16x32_bf16 v[120:123], a[60:63], a[108:111], v[120:123]\n"
"v_and_b32 v15, s63, v21\n"
"v_mfma_f32_16x16x32_bf16 v[92:95], a[52:55], a[108:111], v[92:95]\n"
"ds_read_b128 a[108:111], v3 offset:3072\n"
"v_mfma_f32_16x16x32_bf16 v[92:95], a[48:51], a[104:107], v[92:95]\n"
"ds_read_b128 a[104:107], v3 offset:2048\n"
"v_mfma_f32_16x16x32_bf16 v[96:99], a[48:51], a[112:115], v[96:99]\n"
"ds_read_b128 v[16:19], v7 offset:34816\n"
"v_mfma_f32_16x16x32_bf16 v[96:99], a[52:55], a[116:119], v[96:99]\n"
"global_load_dwordx4 a[36:39], v0, s[34:35] offset:1024 sc1\n"
"v_mul_f32 v12, v12, v88\n"
"v_mul_f32 v13, v13, v89\n"
"v_mfma_f32_16x16x32_bf16 v[124:127], a[60:63], a[116:119], v[124:127]\n"
"ds_read_b128 a[116:119], v3 offset:5120\n"
"v_mul_f32 v14, v14, v90\n"
"v_mul_f32 v15, v15, v91\n"
"v_mfma_f32_16x16x32_bf16 v[124:127], a[56:59], a[112:115], v[124:127]\n"
"ds_read_b128 a[112:115], v3 offset:4096\n"
"v_cvt_pk_bf16_f32 v28, v12, v13\n"
"v_cvt_pk_bf16_f32 v29, v14, v15\n"
"v_mfma_f32_16x16x32_bf16 v[128:131], a[56:59], a[120:123], v[128:131]\n"
"ds_write_b64 v8, v[28:29] offset:32768\n"
"v_mfma_f32_16x16x32_bf16 v[128:131], a[60:63], a[124:127], v[128:131]\n"
"v_lshlrev_b32 v12, 16, v22\n"
"v_and_b32 v13, s63, v22\n"
"v_lshlrev_b32 v14, 16, v23\n"
"v_mfma_f32_16x16x32_bf16 v[100:103], a[52:55], a[124:127], v[100:103]\n"
"ds_read_b128 a[124:127], v3 offset:7168\n"
"v_and_b32 v15, s63, v23\n"
"v_mul_f32 v12, v12, v116\n"
"v_mfma_f32_16x16x32_bf16 v[100:103], a[48:51], a[120:123], v[100:103]\n"
"ds_read_b128 a[120:123], v3 offset:6144\n"
"v_mul_f32 v13, v13, v117\n"
"v_mfma_f32_16x16x32_bf16 v[104:107], a[48:51], a[128:131], v[104:107]\n"
"v_mul_f32 v14, v14, v118\n"
"v_mul_f32 v15, v15, v119\n"
"v_mfma_f32_16x16x32_bf16 v[104:107], a[52:55], a[132:135], v[104:107]\n"
"global_load_dwordx4 a[40:43], v0, s[34:35] offset:2048 sc1\n"
"v_cvt_pk_bf16_f32 v30, v12, v13\n"
"v_mfma_f32_16x16x32_bf16 v[132:135], a[60:63], a[132:135], v[132:135]\n"
"ds_read_b128 a[132:135], v3 offset:9216\n"
"v_cvt_pk_bf16_f32 v31, v14, v15\n"
"v_mfma_f32_16x16x32_bf16 v[132:135], a[56:59], a[128:131], v[132:135]\n"
"ds_read_b128 a[128:131], v3 offset:8192\n"
"ds_write_b64 v8, v[30:31] offset:32776\n"
"v_mfma_f32_16x16x32_bf16 v[136:139], a[56:59], a[136:139], v[136:139]\n"
"ds_read_b128 v[32:35], v2 offset:32768\n"
"v_mfma_f32_16x16x32_bf16 v[136:139], a[60:63], a[140:143], v[136:139]\n"
"s_add_u32 s96, s36, 1552384\n"
"ds_read_b128 v[60:63], v2 offset:33792\n"
"v_mfma_f32_16x16x32_bf16 v[108:111], a[52:55], a[140:143], v[108:111]\n"
"s_waitcnt lgkmcnt(10)\n"
"ds_read_b128 a[140:143], v3 offset:11264\n"
"v_lshlrev_b32 v12, 16, v16\n"
"v_mfma_f32_16x16x32_bf16 v[108:111], a[48:51], a[136:139], v[108:111]\n"
"ds_read_b128 a[136:139], v3 offset:10240\n"
"v_and_b32 v13, s63, v16\n"
"v_mfma_f32_16x16x32_bf16 v[112:115], a[48:51], a[144:147], v[112:115]\n"
"global_load_dwordx4 v[88:91], v0, s[96:97] offset:-4096 sc1\n"
"v_lshlrev_b32 v14, 16, v17\n"
"v_mfma_f32_16x16x32_bf16 v[112:115], a[52:55], a[148:151], v[112:115]\n"
"global_load_dwordx4 a[44:47], v0, s[34:35] offset:3072 sc1\n"
"v_and_b32 v15, s63, v17\n"
"v_mfma_f32_16x16x32_bf16 v[140:143], a[60:63], a[148:151], v[140:143]\n"
"ds_read_b128 a[148:151], v3 offset:13312\n"
"v_xor_b32 v20, 64, v7\n"
"v_mfma_f32_16x16x32_bf16 v[140:143], a[56:59], a[144:147], v[140:143]\n"
"ds_read_b128 a[144:147], v3 offset:12288\n"
"v_mfma_f32_16x16x32_bf16 a[12:15], a[56:59], a[152:155], a[12:15]\n"
"ds_read_b128 v[20:23], v20 offset:34816\n"
"v_mul_f32 v12, v12, v36\n"
"v_mfma_f32_16x16x32_bf16 a[12:15], a[60:63], a[156:159], a[12:15]\n"
"s_waitcnt lgkmcnt(6)\n"
"buffer_store_dwordx4 v[32:35], v6, s[52:55], s80 offen offset:0 sc1\n"
"v_mul_f32 v13, v13, v37\n"
"v_mul_f32 v14, v14, v38\n"
"v_mul_f32 v15, v15, v39\n"
"v_mfma_f32_16x16x32_bf16 a[8:11], a[52:55], a[156:159], a[8:11]\n"
"ds_read_b128 a[156:159], v3 offset:15360\n"
"v_cvt_pk_bf16_f32 v24, v12, v13\n"
"v_mfma_f32_16x16x32_bf16 a[8:11], a[48:51], a[152:155], a[8:11]\n"
"s_waitcnt vmcnt(10)\n"
"ds_read_b128 a[152:155], v3 offset:14336\n"
"v_cvt_pk_bf16_f32 v25, v14, v15\n"
"v_mfma_f32_16x16x32_bf16 v[144:147], a[64:67], a[96:99], v[144:147]\n"
"s_add_u32 s34, s36, 1486848\n"
"ds_write_b64 v7, v[24:25] offset:34816\n"
"v_mfma_f32_16x16x32_bf16 v[144:147], a[68:71], a[100:103], v[144:147]\n"
"global_load_dwordx4 a[48:51], v0, s[34:35] offset:0 sc1\n"
"v_lshlrev_b32 v12, 16, v18\n"
"v_mfma_f32_16x16x32_bf16 v[172:175], a[76:79], a[100:103], v[172:175]\n"
"global_load_dwordx4 v[116:119], v0, s[96:97] offset:-3072 sc1\n"
"v_and_b32 v13, s63, v18\n"
"v_mfma_f32_16x16x32_bf16 v[172:175], a[72:75], a[96:99], v[172:175]\n"
"v_lshlrev_b32 v14, 16, v19\n"
"v_and_b32 v15, s63, v19\n"
"v_mfma_f32_16x16x32_bf16 v[176:179], a[72:75], a[104:107], v[176:179]\n"
"v_mul_f32 v12, v12, v64\n"
"v_mul_f32 v13, v13, v65\n"
"v_mfma_f32_16x16x32_bf16 v[176:179], a[76:79], a[108:111], v[176:179]\n"
"v_mul_f32 v14, v14, v66\n"
"v_mul_f32 v15, v15, v67\n"
"v_mfma_f32_16x16x32_bf16 v[148:151], a[68:71], a[108:111], v[148:151]\n"
"v_cvt_pk_bf16_f32 v26, v12, v13\n"
"v_cvt_pk_bf16_f32 v27, v14, v15\n"
"v_mfma_f32_16x16x32_bf16 v[148:151], a[64:67], a[104:107], v[148:151]\n"
"ds_write_b64 v7, v[26:27] offset:34824\n"
"v_mfma_f32_16x16x32_bf16 v[152:155], a[64:67], a[112:115], v[152:155]\n"
"s_waitcnt lgkmcnt(4)\n"
"v_lshlrev_b32 v12, 16, v20\n"
"v_and_b32 v13, s63, v20\n"
"v_mfma_f32_16x16x32_bf16 v[152:155], a[68:71], a[116:119], v[152:155]\n"
"global_load_dwordx4 a[52:55], v0, s[34:35] offset:1024 sc1\n"
"v_lshlrev_b32 v14, 16, v21\n"
"v_mfma_f32_16x16x32_bf16 v[180:183], a[76:79], a[116:119], v[180:183]\n"
"ds_read_b128 v[16:19], v7 offset:36864\n"
"v_and_b32 v15, s63, v21\n"
"v_mfma_f32_16x16x32_bf16 v[180:183], a[72:75], a[112:115], v[180:183]\n"
"v_mul_f32 v12, v12, v92\n"
"v_mul_f32 v13, v13, v93\n"
"v_mfma_f32_16x16x32_bf16 v[184:187], a[72:75], a[120:123], v[184:187]\n"
"v_mul_f32 v14, v14, v94\n"
"v_mul_f32 v15, v15, v95\n"
"v_mfma_f32_16x16x32_bf16 v[184:187], a[76:79], a[124:127], v[184:187]\n"
"v_cvt_pk_bf16_f32 v28, v12, v13\n"
"v_cvt_pk_bf16_f32 v29, v14, v15\n"
"v_mfma_f32_16x16x32_bf16 v[156:159], a[68:71], a[124:127], v[156:159]\n"
"ds_write_b64 v8, v[28:29] offset:34816\n"
"v_mfma_f32_16x16x32_bf16 v[156:159], a[64:67], a[120:123], v[156:159]\n"
"v_lshlrev_b32 v12, 16, v22\n"
"v_and_b32 v13, s63, v22\n"
"v_mfma_f32_16x16x32_bf16 v[160:163], a[64:67], a[128:131], v[160:163]\n"
"v_lshlrev_b32 v14, 16, v23\n"
"v_and_b32 v15, s63, v23\n"
"v_mfma_f32_16x16x32_bf16 v[160:163], a[68:71], a[132:135], v[160:163]\n"
"global_load_dwordx4 a[56:59], v0, s[34:35] offset:2048 sc1\n"
"v_mul_f32 v12, v12, v120\n"
"v_mfma_f32_16x16x32_bf16 v[188:191], a[76:79], a[132:135], v[188:191]\n"
"v_mul_f32 v13, v13, v121\n"
"v_mul_f32 v14, v14, v122\n"
"v_mfma_f32_16x16x32_bf16 v[188:191], a[72:75], a[128:131], v[188:191]\n"
"v_mul_f32 v15, v15, v123\n"
"v_cvt_pk_bf16_f32 v30, v12, v13\n"
"v_mfma_f32_16x16x32_bf16 v[192:195], a[72:75], a[136:139], v[192:195]\n"
"v_cvt_pk_bf16_f32 v31, v14, v15\n"
"v_mfma_f32_16x16x32_bf16 v[192:195], a[76:79], a[140:143], v[192:195]\n"
"ds_write_b64 v8, v[30:31] offset:34824\n"
"v_mfma_f32_16x16x32_bf16 v[164:167], a[68:71], a[140:143], v[164:167]\n"
"ds_read_b128 v[36:39], v2 offset:34816\n"
"v_mfma_f32_16x16x32_bf16 v[164:167], a[64:67], a[136:139], v[164:167]\n"
"s_waitcnt lgkmcnt(3)\n"
"ds_read_b128 v[64:67], v2 offset:35840\n"
"v_lshlrev_b32 v12, 16, v16\n"
"v_mfma_f32_16x16x32_bf16 v[168:171], a[64:67], a[144:147], v[168:171]\n"
"buffer_store_dwordx4 v[60:63], v6, s[52:55], s81 offen offset:1024 sc1\n"
"v_and_b32 v13, s63, v16\n"
"v_lshlrev_b32 v14, 16, v17\n"
"v_and_b32 v15, s63, v17\n"
"v_xor_b32 v20, 64, v7\n"
"v_mfma_f32_16x16x32_bf16 v[168:171], a[68:71], a[148:151], v[168:171]\n"
"global_load_dwordx4 a[60:63], v0, s[34:35] offset:3072 sc1\n"
"ds_read_b128 v[20:23], v20 offset:36864\n"
"v_mfma_f32_16x16x32_bf16 v[196:199], a[76:79], a[148:151], v[196:199]\n"
"v_mul_f32 v12, v12, v40\n"
"v_mul_f32 v13, v13, v41\n"
"v_mfma_f32_16x16x32_bf16 v[196:199], a[72:75], a[144:147], v[196:199]\n"
"v_mul_f32 v14, v14, v42\n"
"v_mul_f32 v15, v15, v43\n"
"v_mfma_f32_16x16x32_bf16 a[20:23], a[72:75], a[152:155], a[20:23]\n"
"v_cvt_pk_bf16_f32 v24, v12, v13\n"
"v_cvt_pk_bf16_f32 v25, v14, v15\n"
"v_mfma_f32_16x16x32_bf16 a[20:23], a[76:79], a[156:159], a[20:23]\n"
"ds_write_b64 v7, v[24:25] offset:36864\n"
"v_mfma_f32_16x16x32_bf16 a[16:19], a[68:71], a[156:159], a[16:19]\n"
"global_load_dwordx4 v[92:95], v0, s[96:97] offset:-2048 sc1\n"
"v_lshlrev_b32 v12, 16, v18\n"
"v_mfma_f32_16x16x32_bf16 a[16:19], a[64:67], a[152:155], a[16:19]\n"
"s_waitcnt vmcnt(13)\n"
"v_and_b32 v13, s63, v18\n"
"v_lshlrev_b32 v14, 16, v19\n"
"v_mfma_f32_16x16x32_bf16 v[200:203], a[80:83], a[96:99], v[200:203]\n"
"s_add_u32 s34, s36, 1499136\n"
"v_and_b32 v15, s63, v19\n"
"v_mul_f32 v12, v12, v68\n"
"v_mfma_f32_16x16x32_bf16 v[200:203], a[84:87], a[100:103], v[200:203]\n"
"global_load_dwordx4 a[64:67], v0, s[34:35] offset:0 sc1\n"
"v_mul_f32 v13, v13, v69\n"
"v_mfma_f32_16x16x32_bf16 v[228:231], a[92:95], a[100:103], v[228:231]\n"
"ds_read_b128 a[100:103], v4 offset:1024\n"
"v_mul_f32 v14, v14, v70\n"
"v_mfma_f32_16x16x32_bf16 v[228:231], a[88:91], a[96:99], v[228:231]\n"
"ds_read_b128 a[96:99], v4 offset:0\n"
"v_mul_f32 v15, v15, v71\n"
"v_mfma_f32_16x16x32_bf16 v[232:235], a[88:91], a[104:107], v[232:235]\n"
"v_cvt_pk_bf16_f32 v26, v12, v13\n"
"v_cvt_pk_bf16_f32 v27, v14, v15\n"
"v_mfma_f32_16x16x32_bf16 v[232:235], a[92:95], a[108:111], v[232:235]\n"
"ds_write_b64 v7, v[26:27] offset:36872\n"
"v_mfma_f32_16x16x32_bf16 v[204:207], a[84:87], a[108:111], v[204:207]\n"
"ds_read_b128 a[108:111], v4 offset:3072\n"
"v_mfma_f32_16x16x32_bf16 v[204:207], a[80:83], a[104:107], v[204:207]\n"
"ds_read_b128 a[104:107], v4 offset:2048\n"
"v_mfma_f32_16x16x32_bf16 v[208:211], a[80:83], a[112:115], v[208:211]\n"
"global_load_dwordx4 v[120:123], v0, s[96:97] offset:-1024 sc1\n"
"v_mfma_f32_16x16x32_bf16 v[208:211], a[84:87], a[116:119], v[208:211]\n"
"global_load_dwordx4 a[68:71], v0, s[34:35] offset:1024 sc1\n"
"v_mfma_f32_16x16x32_bf16 v[236:239], a[92:95], a[116:119], v[236:239]\n"
"s_waitcnt lgkmcnt(6)\n"
"ds_read_b128 a[116:119], v4 offset:5120\n"
"v_lshlrev_b32 v12, 16, v20\n"
"v_mfma_f32_16x16x32_bf16 v[236:239], a[88:91], a[112:115], v[236:239]\n"
"ds_read_b128 a[112:115], v4 offset:4096\n"
"v_and_b32 v13, s63, v20\n"
"v_mfma_f32_16x16x32_bf16 v[240:243], a[88:91], a[120:123], v[240:243]\n"
"v_lshlrev_b32 v14, 16, v21\n"
"v_and_b32 v15, s63, v21\n"
"v_mfma_f32_16x16x32_bf16 v[240:243], a[92:95], a[124:127], v[240:243]\n"
"ds_read_b128 v[16:19], v7 offset:38912\n"
"v_mul_f32 v12, v12, v96\n"
"v_mfma_f32_16x16x32_bf16 v[212:215], a[84:87], a[124:127], v[212:215]\n"
"ds_read_b128 a[124:127], v4 offset:7168\n"
"v_mul_f32 v13, v13, v97\n"
"v_mfma_f32_16x16x32_bf16 v[212:215], a[80:83], a[120:123], v[212:215]\n"
"ds_read_b128 a[120:123], v4 offset:6144\n"
"v_mul_f32 v14, v14, v98\n"
"v_mfma_f32_16x16x32_bf16 v[216:219], a[80:83], a[128:131], v[216:219]\n"
"v_mul_f32 v15, v15, v99\n"
"v_mfma_f32_16x16x32_bf16 v[216:219], a[84:87], a[132:135], v[216:219]\n"
"global_load_dwordx4 a[72:75], v0, s[34:35] offset:2048 sc1\n"
"v_cvt_pk_bf16_f32 v28, v12, v13\n"
"v_mfma_f32_16x16x32_bf16 v[244:247], a[92:95], a[132:135], v[244:247]\n"
"ds_read_b128 a[132:135], v4 offset:9216\n"
"global_load_dwordx4 v[32:35], v0, s[96:97] offset:0 sc1\n"
"v_mfma_f32_16x16x32_bf16 v[244:247], a[88:91], a[128:131], v[244:247]\n"
"ds_read_b128 a[128:131], v4 offset:8192\n"
"v_mfma_f32_16x16x32_bf16 v[248:251], a[88:91], a[136:139], v[248:251]\n"
"v_cvt_pk_bf16_f32 v29, v14, v15\n"
"v_mfma_f32_16x16x32_bf16 v[248:251], a[92:95], a[140:143], v[248:251]\n"
"buffer_store_dwordx4 v[36:39], v6, s[52:55], s82 offen offset:2048 sc1\n"
"ds_write_b64 v8, v[28:29] offset:36864\n"
"v_mfma_f32_16x16x32_bf16 v[220:223], a[84:87], a[140:143], v[220:223]\n"
"ds_read_b128 a[140:143], v4 offset:11264\n"
"v_lshlrev_b32 v12, 16, v22\n"
"v_mfma_f32_16x16x32_bf16 v[220:223], a[80:83], a[136:139], v[220:223]\n"
"ds_read_b128 a[136:139], v4 offset:10240\n"
"v_and_b32 v13, s63, v22\n"
"v_mfma_f32_16x16x32_bf16 v[224:227], a[80:83], a[144:147], v[224:227]\n"
"v_lshlrev_b32 v14, 16, v23\n"
"v_and_b32 v15, s63, v23\n"
"v_mfma_f32_16x16x32_bf16 v[224:227], a[84:87], a[148:151], v[224:227]\n"
"global_load_dwordx4 a[76:79], v0, s[34:35] offset:3072 sc1\n"
"v_mul_f32 v12, v12, v124\n"
"v_mfma_f32_16x16x32_bf16 v[252:255], a[92:95], a[148:151], v[252:255]\n"
"ds_read_b128 a[148:151], v4 offset:13312\n"
"v_mul_f32 v13, v13, v125\n"
"v_mfma_f32_16x16x32_bf16 v[252:255], a[88:91], a[144:147], v[252:255]\n"
"ds_read_b128 a[144:147], v4 offset:12288\n"
"v_mul_f32 v14, v14, v126\n"
"v_mfma_f32_16x16x32_bf16 a[28:31], a[88:91], a[152:155], a[28:31]\n"
"global_load_dwordx4 v[60:63], v0, s[96:97] offset:1024 sc1\n"
"v_mul_f32 v15, v15, v127\n"
"v_mfma_f32_16x16x32_bf16 a[28:31], a[92:95], a[156:159], a[28:31]\n"
"v_cvt_pk_bf16_f32 v30, v12, v13\n"
"v_cvt_pk_bf16_f32 v31, v14, v15\n"
"v_mfma_f32_16x16x32_bf16 a[24:27], a[84:87], a[156:159], a[24:27]\n"
"ds_read_b128 a[156:159], v4 offset:15360\n"
"v_mfma_f32_16x16x32_bf16 a[24:27], a[80:83], a[152:155], a[24:27]\n"
"s_waitcnt vmcnt(16) lgkmcnt(13)\n"
"ds_read_b128 a[152:155], v4 offset:14336\n"
"v_mfma_f32_16x16x32_bf16 v[144:147], a[32:35], a[96:99], v[144:147]\n"
"s_add_u32 s34, s36, 1503232\n"
"ds_write_b64 v8, v[30:31] offset:36872\n"
"v_mfma_f32_16x16x32_bf16 v[144:147], a[36:39], a[100:103], v[144:147]\n"
"global_load_dwordx4 a[80:83], v0, s[34:35] offset:0 sc1\n"
"ds_read_b128 v[40:43], v2 offset:36864\n"
"v_mfma_f32_16x16x32_bf16 v[172:175], a[44:47], a[100:103], v[172:175]\n"
"s_waitcnt lgkmcnt(13)\n"
"ds_read_b128 v[68:71], v2 offset:37888\n"
"v_lshlrev_b32 v12, 16, v16\n"
"v_mfma_f32_16x16x32_bf16 v[172:175], a[40:43], a[96:99], v[172:175]\n"
"v_and_b32 v13, s63, v16\n"
"v_lshlrev_b32 v14, 16, v17\n"
"v_mfma_f32_16x16x32_bf16 v[176:179], a[40:43], a[104:107], v[176:179]\n"
"v_and_b32 v15, s63, v17\n"
"v_xor_b32 v20, 64, v7\n"
"v_mfma_f32_16x16x32_bf16 v[176:179], a[44:47], a[108:111], v[176:179]\n"
"ds_read_b128 v[20:23], v20 offset:38912\n"
"v_mul_f32 v12, v12, v44\n"
"v_mfma_f32_16x16x32_bf16 v[148:151], a[36:39], a[108:111], v[148:151]\n"
"v_mul_f32 v13, v13, v45\n"
"v_mul_f32 v14, v14, v46\n"
"v_mfma_f32_16x16x32_bf16 v[148:151], a[32:35], a[104:107], v[148:151]\n"
"s_waitcnt lgkmcnt(13)\n"
"global_load_dwordx4 v[96:99], v0, s[96:97] offset:2048 sc1\n"
"v_mul_f32 v15, v15, v47\n"
"v_mfma_f32_16x16x32_bf16 v[152:155], a[32:35], a[112:115], v[152:155]\n"
"v_cvt_pk_bf16_f32 v24, v12, v13\n"
"v_cvt_pk_bf16_f32 v25, v14, v15\n"
"v_mfma_f32_16x16x32_bf16 v[152:155], a[36:39], a[116:119], v[152:155]\n"
"global_load_dwordx4 a[84:87], v0, s[34:35] offset:1024 sc1\n"
"v_mfma_f32_16x16x32_bf16 v[180:183], a[44:47], a[116:119], v[180:183]\n"
"ds_write_b64 v7, v[24:25] offset:38912\n"
"v_mfma_f32_16x16x32_bf16 v[180:183], a[40:43], a[112:115], v[180:183]\n"
"v_lshlrev_b32 v12, 16, v18\n"
"v_and_b32 v13, s63, v18\n"
"v_mfma_f32_16x16x32_bf16 v[184:187], a[40:43], a[120:123], v[184:187]\n"
"v_lshlrev_b32 v14, 16, v19\n"
"v_and_b32 v15, s63, v19\n"
"v_mfma_f32_16x16x32_bf16 v[184:187], a[44:47], a[124:127], v[184:187]\n"
"v_mul_f32 v12, v12, v72\n"
"v_mul_f32 v13, v13, v73\n"
"v_mfma_f32_16x16x32_bf16 v[156:159], a[36:39], a[124:127], v[156:159]\n"
"v_mul_f32 v14, v14, v74\n"
"v_mul_f32 v15, v15, v75\n"
"v_mfma_f32_16x16x32_bf16 v[156:159], a[32:35], a[120:123], v[156:159]\n"
"s_waitcnt lgkmcnt(9)\n"
"v_cvt_pk_bf16_f32 v26, v12, v13\n"
"v_cvt_pk_bf16_f32 v27, v14, v15\n"
"v_mfma_f32_16x16x32_bf16 v[160:163], a[32:35], a[128:131], v[160:163]\n"
"buffer_store_dwordx4 v[64:67], v6, s[52:55], s83 offen offset:3072 sc1\n"
"ds_write_b64 v7, v[26:27] offset:38920\n"
"v_mfma_f32_16x16x32_bf16 v[160:163], a[36:39], a[132:135], v[160:163]\n"
"s_waitcnt lgkmcnt(2)\n"
"global_load_dwordx4 a[88:91], v0, s[34:35] offset:2048 sc1\n"
"v_lshlrev_b32 v12, 16, v20\n"
"v_mfma_f32_16x16x32_bf16 v[188:191], a[44:47], a[132:135], v[188:191]\n"
"global_load_dwordx4 v[124:127], v0, s[96:97] offset:3072 sc1\n"
"v_and_b32 v13, s63, v20\n"
"v_mfma_f32_16x16x32_bf16 v[188:191], a[40:43], a[128:131], v[188:191]\n"
"v_lshlrev_b32 v14, 16, v21\n"
"v_and_b32 v15, s63, v21\n"
"v_mfma_f32_16x16x32_bf16 v[192:195], a[40:43], a[136:139], v[192:195]\n"
"v_mul_f32 v12, v12, v100\n"
"v_mul_f32 v13, v13, v101\n"
"v_mfma_f32_16x16x32_bf16 v[192:195], a[44:47], a[140:143], v[192:195]\n"
"v_mul_f32 v14, v14, v102\n"
"v_mul_f32 v15, v15, v103\n"
"v_mfma_f32_16x16x32_bf16 v[164:167], a[36:39], a[140:143], v[164:167]\n"
"v_cvt_pk_bf16_f32 v28, v12, v13\n"
"v_cvt_pk_bf16_f32 v29, v14, v15\n"
"v_mfma_f32_16x16x32_bf16 v[164:167], a[32:35], a[136:139], v[164:167]\n"
"ds_write_b64 v8, v[28:29] offset:38912\n"
"v_mfma_f32_16x16x32_bf16 v[168:171], a[32:35], a[144:147], v[168:171]\n"
"v_lshlrev_b32 v12, 16, v22\n"
"v_mfma_f32_16x16x32_bf16 v[168:171], a[36:39], a[148:151], v[168:171]\n"
"global_load_dwordx4 a[92:95], v0, s[34:35] offset:3072 sc1\n"
"v_and_b32 v13, s63, v22\n"
"v_mfma_f32_16x16x32_bf16 v[196:199], a[44:47], a[148:151], v[196:199]\n"
"v_lshlrev_b32 v14, 16, v23\n"
"v_and_b32 v15, s63, v23\n"
"v_mfma_f32_16x16x32_bf16 v[196:199], a[40:43], a[144:147], v[196:199]\n"
"v_mul_f32 v12, v12, v128\n"
"v_mfma_f32_16x16x32_bf16 a[20:23], a[40:43], a[152:155], a[20:23]\n"
"v_mul_f32 v13, v13, v129\n"
"v_mfma_f32_16x16x32_bf16 a[20:23], a[44:47], a[156:159], a[20:23]\n"
"v_mul_f32 v14, v14, v130\n"
"v_mul_f32 v15, v15, v131\n"
"v_mfma_f32_16x16x32_bf16 a[16:19], a[36:39], a[156:159], a[16:19]\n"
"v_cvt_pk_bf16_f32 v30, v12, v13\n"
"v_cvt_pk_bf16_f32 v31, v14, v15\n"
"v_mfma_f32_16x16x32_bf16 a[16:19], a[32:35], a[152:155], a[16:19]\n"
"s_waitcnt vmcnt(16)\n"
"ds_write_b64 v8, v[30:31] offset:38920\n"
"v_mfma_f32_16x16x32_bf16 v[200:203], a[48:51], a[96:99], v[200:203]\n"
"s_add_u32 s34, s36, 1540096\n"
"ds_read_b128 v[44:47], v2 offset:38912\n"
"v_mfma_f32_16x16x32_bf16 v[200:203], a[52:55], a[100:103], v[200:203]\n"
"global_load_dwordx4 a[32:35], v0, s[34:35] offset:0 sc1\n"
"ds_read_b128 v[72:75], v2 offset:39936\n"
"v_mfma_f32_16x16x32_bf16 v[228:231], a[60:63], a[100:103], v[228:231]\n"
"ds_read_b128 a[100:103], v5 offset:1024\n"
"v_accvgpr_read_b32 v16, a160\n"
"v_mfma_f32_16x16x32_bf16 v[228:231], a[56:59], a[96:99], v[228:231]\n"
"ds_read_b128 a[96:99], v5 offset:0\n"
"v_accvgpr_read_b32 v17, a161\n"
"v_mfma_f32_16x16x32_bf16 v[232:235], a[56:59], a[104:107], v[232:235]\n"
"v_accvgpr_read_b32 v18, a162\n"
"v_accvgpr_read_b32 v19, a163\n"
"v_mfma_f32_16x16x32_bf16 v[232:235], a[60:63], a[108:111], v[232:235]\n"
"v_lshlrev_b32 v12, 16, v16\n"
"v_mfma_f32_16x16x32_bf16 v[204:207], a[52:55], a[108:111], v[204:207]\n"
"ds_read_b128 a[108:111], v5 offset:3072\n"
"v_and_b32 v13, s63, v16\n"
"v_mfma_f32_16x16x32_bf16 v[204:207], a[48:51], a[104:107], v[204:207]\n"
"ds_read_b128 a[104:107], v5 offset:2048\n"
"v_lshlrev_b32 v14, 16, v17\n"
"v_mfma_f32_16x16x32_bf16 v[208:211], a[48:51], a[112:115], v[208:211]\n"
"v_and_b32 v15, s63, v17\n"
"v_mul_f32 v12, v12, v48\n"
"v_mfma_f32_16x16x32_bf16 v[208:211], a[52:55], a[116:119], v[208:211]\n"
"global_load_dwordx4 a[36:39], v0, s[34:35] offset:1024 sc1\n"
"v_mul_f32 v13, v13, v49\n"
"v_mfma_f32_16x16x32_bf16 v[236:239], a[60:63], a[116:119], v[236:239]\n"
"ds_read_b128 a[116:119], v5 offset:5120\n"
"v_mul_f32 v14, v14, v50\n"
"v_mfma_f32_16x16x32_bf16 v[236:239], a[56:59], a[112:115], v[236:239]\n"
"ds_read_b128 a[112:115], v5 offset:4096\n"
"v_mul_f32 v15, v15, v51\n"
"v_mfma_f32_16x16x32_bf16 v[240:243], a[56:59], a[120:123], v[240:243]\n"
"v_cvt_pk_bf16_f32 v24, v12, v13\n"
"v_cvt_pk_bf16_f32 v25, v14, v15\n"
"v_mfma_f32_16x16x32_bf16 v[240:243], a[60:63], a[124:127], v[240:243]\n"
"buffer_store_dwordx4 v[40:43], v6, s[52:55], s84 offen offset:0 sc1\n"
"ds_write_b64 v7, v[24:25] offset:32768\n"
"v_mfma_f32_16x16x32_bf16 v[212:215], a[52:55], a[124:127], v[212:215]\n"
"ds_read_b128 a[124:127], v5 offset:7168\n"
"v_lshlrev_b32 v12, 16, v18\n"
"v_mfma_f32_16x16x32_bf16 v[212:215], a[48:51], a[120:123], v[212:215]\n"
"ds_read_b128 a[120:123], v5 offset:6144\n"
"v_and_b32 v13, s63, v18\n"
"v_mfma_f32_16x16x32_bf16 v[216:219], a[48:51], a[128:131], v[216:219]\n"
"v_lshlrev_b32 v14, 16, v19\n"
"v_and_b32 v15, s63, v19\n"
"v_mfma_f32_16x16x32_bf16 v[216:219], a[52:55], a[132:135], v[216:219]\n"
"global_load_dwordx4 a[40:43], v0, s[34:35] offset:2048 sc1\n"
"v_mul_f32 v12, v12, v76\n"
"v_mfma_f32_16x16x32_bf16 v[244:247], a[60:63], a[132:135], v[244:247]\n"
"s_add_u32 s96, s36, 1560576\n"
"ds_read_b128 a[132:135], v5 offset:9216\n"
"v_mul_f32 v13, v13, v77\n"
"v_mfma_f32_16x16x32_bf16 v[244:247], a[56:59], a[128:131], v[244:247]\n"
"ds_read_b128 a[128:131], v5 offset:8192\n"
"v_mul_f32 v14, v14, v78\n"
"v_mfma_f32_16x16x32_bf16 v[248:251], a[56:59], a[136:139], v[248:251]\n"
"global_load_dwordx4 v[100:103], v0, s[96:97] offset:-4096 sc1\n"
"v_mul_f32 v15, v15, v79\n"
"v_mfma_f32_16x16x32_bf16 v[248:251], a[60:63], a[140:143], v[248:251]\n"
"v_cvt_pk_bf16_f32 v26, v12, v13\n"
"v_cvt_pk_bf16_f32 v27, v14, v15\n"
"v_mfma_f32_16x16x32_bf16 v[220:223], a[52:55], a[140:143], v[220:223]\n"
"ds_read_b128 a[140:143], v5 offset:11264\n"
"v_mfma_f32_16x16x32_bf16 v[220:223], a[48:51], a[136:139], v[220:223]\n"
"ds_read_b128 a[136:139], v5 offset:10240\n"
"v_mfma_f32_16x16x32_bf16 v[224:227], a[48:51], a[144:147], v[224:227]\n"
"ds_write_b64 v7, v[26:27] offset:32776\n"
"v_mfma_f32_16x16x32_bf16 v[224:227], a[52:55], a[148:151], v[224:227]\n"
"global_load_dwordx4 a[44:47], v0, s[34:35] offset:3072 sc1\n"
"v_accvgpr_read_b32 v16, a164\n"
"v_mfma_f32_16x16x32_bf16 v[252:255], a[60:63], a[148:151], v[252:255]\n"
"ds_read_b128 a[148:151], v5 offset:13312\n"
"v_accvgpr_read_b32 v17, a165\n"
"v_mfma_f32_16x16x32_bf16 v[252:255], a[56:59], a[144:147], v[252:255]\n"
"ds_read_b128 a[144:147], v5 offset:12288\n"
"v_accvgpr_read_b32 v18, a166\n"
"v_mfma_f32_16x16x32_bf16 a[28:31], a[56:59], a[152:155], a[28:31]\n"
"v_accvgpr_read_b32 v19, a167\n"
"v_lshlrev_b32 v12, 16, v16\n"
"v_mfma_f32_16x16x32_bf16 a[28:31], a[60:63], a[156:159], a[28:31]\n"
"global_load_dwordx4 v[128:131], v0, s[96:97] offset:-3072 sc1\n"
"v_and_b32 v13, s63, v16\n"
"v_mfma_f32_16x16x32_bf16 a[24:27], a[52:55], a[156:159], a[24:27]\n"
"ds_read_b128 a[156:159], v5 offset:15360\n"
"v_lshlrev_b32 v14, 16, v17\n"
"v_mfma_f32_16x16x32_bf16 a[24:27], a[48:51], a[152:155], a[24:27]\n"
"s_waitcnt vmcnt(15) lgkmcnt(13)\n"
"ds_read_b128 a[152:155], v5 offset:14336\n"
"v_and_b32 v15, s63, v17\n"
"v_mfma_f32_16x16x32_bf16 v[144:147], a[64:67], a[96:99], v[144:147]\n"
"s_add_u32 s34, s36, 1544192\n"
"v_mul_f32 v12, v12, v104\n"
"v_mul_f32 v13, v13, v105\n"
"v_mfma_f32_16x16x32_bf16 v[144:147], a[68:71], a[100:103], v[144:147]\n"
"global_load_dwordx4 a[48:51], v0, s[34:35] offset:0 sc1\n"
"v_mul_f32 v14, v14, v106\n"
"v_mfma_f32_16x16x32_bf16 v[172:175], a[76:79], a[100:103], v[172:175]\n"
"v_mul_f32 v15, v15, v107\n"
"v_cvt_pk_bf16_f32 v28, v12, v13\n"
"v_mfma_f32_16x16x32_bf16 v[172:175], a[72:75], a[96:99], v[172:175]\n"
"v_cvt_pk_bf16_f32 v29, v14, v15\n"
"v_mfma_f32_16x16x32_bf16 v[176:179], a[72:75], a[104:107], v[176:179]\n"
"ds_write_b64 v8, v[28:29] offset:32768\n"
"v_mfma_f32_16x16x32_bf16 v[176:179], a[76:79], a[108:111], v[176:179]\n"
"v_lshlrev_b32 v12, 16, v18\n"
"v_and_b32 v13, s63, v18\n"
"v_mfma_f32_16x16x32_bf16 v[148:151], a[68:71], a[108:111], v[148:151]\n"
"v_lshlrev_b32 v14, 16, v19\n"
"v_and_b32 v15, s63, v19\n"
"v_mfma_f32_16x16x32_bf16 v[148:151], a[64:67], a[104:107], v[148:151]\n"
"s_waitcnt lgkmcnt(10)\n"
"v_mul_f32 v12, v12, v132\n"
"v_mul_f32 v13, v13, v133\n"
"v_mfma_f32_16x16x32_bf16 v[152:155], a[64:67], a[112:115], v[152:155]\n"
"buffer_store_dwordx4 v[68:71], v6, s[52:55], s85 offen offset:1024 sc1\n"
"global_load_dwordx4 v[36:39], v0, s[96:97] offset:-2048 sc1\n"
"v_mul_f32 v14, v14, v134\n"
"v_mul_f32 v15, v15, v135\n"
"v_mfma_f32_16x16x32_bf16 v[152:155], a[68:71], a[116:119], v[152:155]\n"
"global_load_dwordx4 a[52:55], v0, s[34:35] offset:1024 sc1\n"
"v_cvt_pk_bf16_f32 v30, v12, v13\n"
"v_mfma_f32_16x16x32_bf16 v[180:183], a[76:79], a[116:119], v[180:183]\n"
"v_cvt_pk_bf16_f32 v31, v14, v15\n"
"v_mfma_f32_16x16x32_bf16 v[180:183], a[72:75], a[112:115], v[180:183]\n"
"ds_write_b64 v8, v[30:31] offset:32776\n"
"v_mfma_f32_16x16x32_bf16 v[184:187], a[72:75], a[120:123], v[184:187]\n"
"ds_read_b128 v[48:51], v2 offset:32768\n"
"v_mfma_f32_16x16x32_bf16 v[184:187], a[76:79], a[124:127], v[184:187]\n"
"ds_read_b128 v[76:79], v2 offset:33792\n"
"v_accvgpr_read_b32 v16, a168\n"
"v_mfma_f32_16x16x32_bf16 v[156:159], a[68:71], a[124:127], v[156:159]\n"
"v_accvgpr_read_b32 v17, a169\n"
"v_accvgpr_read_b32 v18, a170\n"
"v_mfma_f32_16x16x32_bf16 v[156:159], a[64:67], a[120:123], v[156:159]\n"
"s_waitcnt lgkmcnt(9)\n"
"v_accvgpr_read_b32 v19, a171\n"
"v_lshlrev_b32 v12, 16, v16\n"
"v_mfma_f32_16x16x32_bf16 v[160:163], a[64:67], a[128:131], v[160:163]\n"
"v_and_b32 v13, s63, v16\n"
"v_lshlrev_b32 v14, 16, v17\n"
"v_mfma_f32_16x16x32_bf16 v[160:163], a[68:71], a[132:135], v[160:163]\n"
"global_load_dwordx4 a[56:59], v0, s[34:35] offset:2048 sc1\n"
"v_and_b32 v15, s63, v17\n"
"v_mfma_f32_16x16x32_bf16 v[188:191], a[76:79], a[132:135], v[188:191]\n"
"global_load_dwordx4 v[64:67], v0, s[96:97] offset:-1024 sc1\n"
"v_mul_f32 v12, v12, v52\n"
"v_mfma_f32_16x16x32_bf16 v[188:191], a[72:75], a[128:131], v[188:191]\n"
"v_mul_f32 v13, v13, v53\n"
"v_mul_f32 v14, v14, v54\n"
"v_mfma_f32_16x16x32_bf16 v[192:195], a[72:75], a[136:139], v[192:195]\n"
"v_mul_f32 v15, v15, v55\n"
"v_mfma_f32_16x16x32_bf16 v[192:195], a[76:79], a[140:143], v[192:195]\n"
"v_cvt_pk_bf16_f32 v24, v12, v13\n"
"v_cvt_pk_bf16_f32 v25, v14, v15\n"
"v_mfma_f32_16x16x32_bf16 v[164:167], a[68:71], a[140:143], v[164:167]\n"
"ds_write_b64 v7, v[24:25] offset:34816\n"
"v_mfma_f32_16x16x32_bf16 v[164:167], a[64:67], a[136:139], v[164:167]\n"
"s_waitcnt lgkmcnt(5)\n"
"v_lshlrev_b32 v12, 16, v18\n"
"v_and_b32 v13, s63, v18\n"
"v_mfma_f32_16x16x32_bf16 v[168:171], a[64:67], a[144:147], v[168:171]\n"
"v_lshlrev_b32 v14, 16, v19\n"
"v_and_b32 v15, s63, v19\n"
"v_mfma_f32_16x16x32_bf16 v[168:171], a[68:71], a[148:151], v[168:171]\n"
"global_load_dwordx4 a[60:63], v0, s[34:35] offset:3072 sc1\n"
"v_mul_f32 v12, v12, v80\n"
"v_mfma_f32_16x16x32_bf16 v[196:199], a[76:79], a[148:151], v[196:199]\n"
"v_mul_f32 v13, v13, v81\n"
"v_mul_f32 v14, v14, v82\n"
"v_mfma_f32_16x16x32_bf16 v[196:199], a[72:75], a[144:147], v[196:199]\n"
"v_mul_f32 v15, v15, v83\n"
"v_cvt_pk_bf16_f32 v26, v12, v13\n"
"v_mfma_f32_16x16x32_bf16 a[20:23], a[72:75], a[152:155], a[20:23]\n"
"global_load_dwordx4 v[104:107], v0, s[96:97] offset:0 sc1\n"
"v_cvt_pk_bf16_f32 v27, v14, v15\n"
"v_mfma_f32_16x16x32_bf16 a[20:23], a[76:79], a[156:159], a[20:23]\n"
"ds_write_b64 v7, v[26:27] offset:34824\n"
"v_mfma_f32_16x16x32_bf16 a[16:19], a[68:71], a[156:159], a[16:19]\n"
"v_accvgpr_read_b32 v16, a172\n"
"v_mfma_f32_16x16x32_bf16 a[16:19], a[64:67], a[152:155], a[16:19]\n"
"s_waitcnt vmcnt(15)\n"
"v_accvgpr_read_b32 v17, a173\n"
"v_accvgpr_read_b32 v18, a174\n"
"v_mfma_f32_16x16x32_bf16 v[200:203], a[80:83], a[96:99], v[200:203]\n"
"s_add_u32 s34, s36, 1564672\n"
"v_accvgpr_read_b32 v19, a175\n"
"v_lshlrev_b32 v12, 16, v16\n"
"v_mfma_f32_16x16x32_bf16 v[200:203], a[84:87], a[100:103], v[200:203]\n"
"global_load_dwordx4 a[64:67], v0, s[34:35] offset:0 sc1\n"
"v_and_b32 v13, s63, v16\n"
"v_mfma_f32_16x16x32_bf16 v[228:231], a[92:95], a[100:103], v[228:231]\n"
"ds_read_b128 a[100:103], v2 offset:17408\n"
"v_lshlrev_b32 v14, 16, v17\n"
"v_mfma_f32_16x16x32_bf16 v[228:231], a[88:91], a[96:99], v[228:231]\n"
"ds_read_b128 a[96:99], v2 offset:16384\n"
"v_and_b32 v15, s63, v17\n"
"v_mfma_f32_16x16x32_bf16 v[232:235], a[88:91], a[104:107], v[232:235]\n"
"v_mul_f32 v12, v12, v108\n"
"v_mul_f32 v13, v13, v109\n"
"v_mfma_f32_16x16x32_bf16 v[232:235], a[92:95], a[108:111], v[232:235]\n"
"buffer_store_dwordx4 v[44:47], v6, s[52:55], s86 offen offset:2048 sc1\n"
"global_load_dwordx4 v[132:135], v0, s[96:97] offset:1024 sc1\n"
"v_mul_f32 v14, v14, v110\n"
"v_mul_f32 v15, v15, v111\n"
"v_mfma_f32_16x16x32_bf16 v[204:207], a[84:87], a[108:111], v[204:207]\n"
"ds_read_b128 a[108:111], v2 offset:19456\n"
"v_cvt_pk_bf16_f32 v28, v12, v13\n"
"v_mfma_f32_16x16x32_bf16 v[204:207], a[80:83], a[104:107], v[204:207]\n"
"ds_read_b128 a[104:107], v2 offset:18432\n"
"v_cvt_pk_bf16_f32 v29, v14, v15\n"
"v_mfma_f32_16x16x32_bf16 v[208:211], a[80:83], a[112:115], v[208:211]\n"
"ds_write_b64 v8, v[28:29] offset:34816\n"
"v_mfma_f32_16x16x32_bf16 v[208:211], a[84:87], a[116:119], v[208:211]\n"
"global_load_dwordx4 a[68:71], v0, s[34:35] offset:1024 sc1\n"
"v_mfma_f32_16x16x32_bf16 v[236:239], a[92:95], a[116:119], v[236:239]\n"
"ds_read_b128 a[116:119], v2 offset:21504\n"
"v_mfma_f32_16x16x32_bf16 v[236:239], a[88:91], a[112:115], v[236:239]\n"
"ds_read_b128 a[112:115], v2 offset:20480\n"
"v_lshlrev_b32 v12, 16, v18\n"
"v_mfma_f32_16x16x32_bf16 v[240:243], a[88:91], a[120:123], v[240:243]\n"
"v_and_b32 v13, s63, v18\n"
"v_mfma_f32_16x16x32_bf16 v[240:243], a[92:95], a[124:127], v[240:243]\n"
"v_lshlrev_b32 v14, 16, v19\n"
"v_and_b32 v15, s63, v19\n"
"v_mfma_f32_16x16x32_bf16 v[212:215], a[84:87], a[124:127], v[212:215]\n"
"ds_read_b128 a[124:127], v2 offset:23552\n"
"v_mul_f32 v12, v12, v136\n"
"v_mfma_f32_16x16x32_bf16 v[212:215], a[80:83], a[120:123], v[212:215]\n"
"ds_read_b128 a[120:123], v2 offset:22528\n"
"v_mul_f32 v13, v13, v137\n"
"v_mfma_f32_16x16x32_bf16 v[216:219], a[80:83], a[128:131], v[216:219]\n"
"v_mul_f32 v14, v14, v138\n"
"v_mul_f32 v15, v15, v139\n"
"v_mfma_f32_16x16x32_bf16 v[216:219], a[84:87], a[132:135], v[216:219]\n"
"global_load_dwordx4 a[72:75], v0, s[34:35] offset:2048 sc1\n"
"v_cvt_pk_bf16_f32 v30, v12, v13\n"
"v_mfma_f32_16x16x32_bf16 v[244:247], a[92:95], a[132:135], v[244:247]\n"
"ds_read_b128 a[132:135], v2 offset:25600\n"
"v_cvt_pk_bf16_f32 v31, v14, v15\n"
"v_mfma_f32_16x16x32_bf16 v[244:247], a[88:91], a[128:131], v[244:247]\n"
"ds_read_b128 a[128:131], v2 offset:24576\n"
"v_mfma_f32_16x16x32_bf16 v[248:251], a[88:91], a[136:139], v[248:251]\n"
"ds_write_b64 v8, v[30:31] offset:34824\n"
"v_mfma_f32_16x16x32_bf16 v[248:251], a[92:95], a[140:143], v[248:251]\n"
"ds_read_b128 v[52:55], v2 offset:34816\n"
"v_mfma_f32_16x16x32_bf16 v[220:223], a[84:87], a[140:143], v[220:223]\n"
"ds_read_b128 a[140:143], v2 offset:27648\n"
"v_mfma_f32_16x16x32_bf16 v[220:223], a[80:83], a[136:139], v[220:223]\n"
"ds_read_b128 a[136:139], v2 offset:26624\n"
"v_mfma_f32_16x16x32_bf16 v[224:227], a[80:83], a[144:147], v[224:227]\n"
"ds_read_b128 v[80:83], v2 offset:35840\n"
"v_accvgpr_read_b32 v16, a176\n"
"v_mfma_f32_16x16x32_bf16 v[224:227], a[84:87], a[148:151], v[224:227]\n"
"global_load_dwordx4 a[76:79], v0, s[34:35] offset:3072 sc1\n"
"v_accvgpr_read_b32 v17, a177\n"
"v_mfma_f32_16x16x32_bf16 v[252:255], a[92:95], a[148:151], v[252:255]\n"
"ds_read_b128 a[148:151], v2 offset:29696\n"
"v_accvgpr_read_b32 v18, a178\n"
"v_mfma_f32_16x16x32_bf16 v[252:255], a[88:91], a[144:147], v[252:255]\n"
"ds_read_b128 a[144:147], v2 offset:28672\n"
"v_accvgpr_read_b32 v19, a179\n"
"v_mfma_f32_16x16x32_bf16 a[28:31], a[88:91], a[152:155], a[28:31]\n"
"v_lshlrev_b32 v12, 16, v16\n"
"v_and_b32 v13, s63, v16\n"
"v_mfma_f32_16x16x32_bf16 a[28:31], a[92:95], a[156:159], a[28:31]\n"
"v_lshlrev_b32 v14, 16, v17\n"
"v_and_b32 v15, s63, v17\n"
"v_mfma_f32_16x16x32_bf16 a[24:27], a[84:87], a[156:159], a[24:27]\n"
"ds_read_b128 a[156:159], v2 offset:31744\n"
"v_mul_f32 v12, v12, v56\n"
"v_mfma_f32_16x16x32_bf16 a[24:27], a[80:83], a[152:155], a[24:27]\n"
"s_waitcnt vmcnt(15) lgkmcnt(15)\n"
"ds_read_b128 a[152:155], v2 offset:30720\n"
"v_mul_f32 v13, v13, v57\n"
"v_mfma_f32_16x16x32_bf16 v[144:147], a[32:35], a[96:99], v[144:147]\n"
"buffer_store_dwordx4 v[72:75], v6, s[52:55], s87 offen offset:3072 sc1\n"
"s_add_u32 s34, s36, 1568768\n"
"v_mul_f32 v14, v14, v58\n"
"v_mul_f32 v15, v15, v59\n"
"v_mfma_f32_16x16x32_bf16 v[144:147], a[36:39], a[100:103], v[144:147]\n"
"global_load_dwordx4 a[80:83], v0, s[34:35] offset:0 sc1\n"
"v_cvt_pk_bf16_f32 v24, v12, v13\n"
"v_mfma_f32_16x16x32_bf16 v[172:175], a[44:47], a[100:103], v[172:175]\n"
"v_cvt_pk_bf16_f32 v25, v14, v15\n"
"v_mfma_f32_16x16x32_bf16 v[172:175], a[40:43], a[96:99], v[172:175]\n"
"ds_write_b64 v7, v[24:25] offset:36864\n"
"v_mfma_f32_16x16x32_bf16 v[176:179], a[40:43], a[104:107], v[176:179]\n"
"global_load_dwordx4 a[84:87], v0, s[34:35] offset:1024 sc1\n"
"v_lshlrev_b32 v12, 16, v18\n"
"v_mfma_f32_16x16x32_bf16 v[176:179], a[44:47], a[108:111], v[176:179]\n"
"global_load_dwordx4 v[108:111], v0, s[96:97] offset:2048 sc1\n"
"v_and_b32 v13, s63, v18\n"
"v_mfma_f32_16x16x32_bf16 v[148:151], a[36:39], a[108:111], v[148:151]\n"
"v_lshlrev_b32 v14, 16, v19\n"
"v_and_b32 v15, s63, v19\n"
"v_mfma_f32_16x16x32_bf16 v[148:151], a[32:35], a[104:107], v[148:151]\n"
"s_waitcnt lgkmcnt(12)\n"
"global_load_dwordx4 a[88:91], v0, s[34:35] offset:2048 sc1\n"
"v_mul_f32 v12, v12, v84\n"
"v_mfma_f32_16x16x32_bf16 v[152:155], a[32:35], a[112:115], v[152:155]\n"
"v_mul_f32 v13, v13, v85\n"
"v_mul_f32 v14, v14, v86\n"
"v_mfma_f32_16x16x32_bf16 v[152:155], a[36:39], a[116:119], v[152:155]\n"
"v_mul_f32 v15, v15, v87\n"
"v_cvt_pk_bf16_f32 v26, v12, v13\n"
"v_mfma_f32_16x16x32_bf16 v[180:183], a[44:47], a[116:119], v[180:183]\n"
"global_load_dwordx4 a[92:95], v0, s[34:35] offset:3072 sc1\n"
"v_cvt_pk_bf16_f32 v27, v14, v15\n"
"v_mfma_f32_16x16x32_bf16 v[180:183], a[40:43], a[112:115], v[180:183]\n"
"ds_write_b64 v7, v[26:27] offset:36872\n"
"v_mfma_f32_16x16x32_bf16 v[184:187], a[40:43], a[120:123], v[184:187]\n"
"v_accvgpr_read_b32 v16, a180\n"
"v_accvgpr_read_b32 v17, a181\n"
"v_mfma_f32_16x16x32_bf16 v[184:187], a[44:47], a[124:127], v[184:187]\n"
"v_accvgpr_read_b32 v18, a182\n"
"v_accvgpr_read_b32 v19, a183\n"
"v_mfma_f32_16x16x32_bf16 v[156:159], a[36:39], a[124:127], v[156:159]\n"
"global_load_dwordx4 v[136:139], v0, s[96:97] offset:3072 sc1\n"
"v_lshlrev_b32 v12, 16, v16\n"
"v_mfma_f32_16x16x32_bf16 v[156:159], a[32:35], a[120:123], v[156:159]\n"
"s_waitcnt lgkmcnt(7)\n"
"v_and_b32 v13, s63, v16\n"
"v_lshlrev_b32 v14, 16, v17\n"
"v_mfma_f32_16x16x32_bf16 v[160:163], a[32:35], a[128:131], v[160:163]\n"
"v_and_b32 v15, s63, v17\n"
"v_mul_f32 v12, v12, v112\n"
"v_mfma_f32_16x16x32_bf16 v[160:163], a[36:39], a[132:135], v[160:163]\n"
"v_mul_f32 v13, v13, v113\n"
"v_mul_f32 v14, v14, v114\n"
"v_mfma_f32_16x16x32_bf16 v[188:191], a[44:47], a[132:135], v[188:191]\n"
"v_mul_f32 v15, v15, v115\n"
"v_mfma_f32_16x16x32_bf16 v[188:191], a[40:43], a[128:131], v[188:191]\n"
"v_cvt_pk_bf16_f32 v28, v12, v13\n"
"v_cvt_pk_bf16_f32 v29, v14, v15\n"
"v_mfma_f32_16x16x32_bf16 v[192:195], a[40:43], a[136:139], v[192:195]\n"
"ds_write_b64 v8, v[28:29] offset:36864\n"
"v_mfma_f32_16x16x32_bf16 v[192:195], a[44:47], a[140:143], v[192:195]\n"
"v_lshlrev_b32 v12, 16, v18\n"
"v_mfma_f32_16x16x32_bf16 v[164:167], a[36:39], a[140:143], v[164:167]\n"
"v_and_b32 v13, s63, v18\n"
"v_lshlrev_b32 v14, 16, v19\n"
"v_mfma_f32_16x16x32_bf16 v[164:167], a[32:35], a[136:139], v[164:167]\n"
"s_waitcnt lgkmcnt(3)\n"
"v_and_b32 v15, s63, v19\n"
"v_mfma_f32_16x16x32_bf16 v[168:171], a[32:35], a[144:147], v[168:171]\n"
"v_mul_f32 v12, v12, v140\n"
"v_mfma_f32_16x16x32_bf16 v[168:171], a[36:39], a[148:151], v[168:171]\n"
"v_mul_f32 v13, v13, v141\n"
"v_mfma_f32_16x16x32_bf16 v[196:199], a[44:47], a[148:151], v[196:199]\n"
"v_mul_f32 v14, v14, v142\n"
"v_mul_f32 v15, v15, v143\n"
"v_mfma_f32_16x16x32_bf16 v[196:199], a[40:43], a[144:147], v[196:199]\n"
"v_cvt_pk_bf16_f32 v30, v12, v13\n"
"v_mfma_f32_16x16x32_bf16 a[20:23], a[40:43], a[152:155], a[20:23]\n"
"v_cvt_pk_bf16_f32 v31, v14, v15\n"
"v_mfma_f32_16x16x32_bf16 a[20:23], a[44:47], a[156:159], a[20:23]\n"
"buffer_store_dwordx4 v[48:51], v6, s[52:55], s88 offen offset:0 sc1\n"
"ds_write_b64 v8, v[30:31] offset:36872\n"
"v_mfma_f32_16x16x32_bf16 a[16:19], a[36:39], a[156:159], a[16:19]\n"
"ds_read_b128 v[56:59], v2 offset:36864\n"
"v_mfma_f32_16x16x32_bf16 a[16:19], a[32:35], a[152:155], a[16:19]\n"
"s_waitcnt vmcnt(15)\n"
"ds_read_b128 v[84:87], v2 offset:37888\n"
"v_accvgpr_read_b32 v16, a184\n"
"v_mfma_f32_16x16x32_bf16 v[200:203], a[48:51], a[96:99], v[200:203]\n"
"v_accvgpr_read_b32 v17, a185\n"
"v_accvgpr_read_b32 v18, a186\n"
"v_mfma_f32_16x16x32_bf16 v[200:203], a[52:55], a[100:103], v[200:203]\n"
"v_accvgpr_read_b32 v19, a187\n"
"v_mfma_f32_16x16x32_bf16 v[228:231], a[60:63], a[100:103], v[228:231]\n"
"ds_read_b128 a[100:103], v3 offset:17408\n"
"v_lshlrev_b32 v12, 16, v16\n"
"v_mfma_f32_16x16x32_bf16 v[228:231], a[56:59], a[96:99], v[228:231]\n"
"ds_read_b128 a[96:99], v3 offset:16384\n"
"v_and_b32 v13, s63, v16\n"
"v_mfma_f32_16x16x32_bf16 v[232:235], a[56:59], a[104:107], v[232:235]\n"
"v_lshlrev_b32 v14, 16, v17\n"
"v_and_b32 v15, s63, v17\n"
"v_mfma_f32_16x16x32_bf16 v[232:235], a[60:63], a[108:111], v[232:235]\n"
"v_accvgpr_read_b32 v10, a0\n"
"v_mfma_f32_16x16x32_bf16 v[204:207], a[52:55], a[108:111], v[204:207]\n"
"ds_read_b128 a[108:111], v4 offset:17408\n"
"v_accvgpr_read_b32 v11, a1\n"
"v_mfma_f32_16x16x32_bf16 v[204:207], a[48:51], a[104:107], v[204:207]\n"
"ds_read_b128 a[104:107], v4 offset:16384\n"
"v_mul_f32 v12, v12, v10\n"
"v_mfma_f32_16x16x32_bf16 v[208:211], a[48:51], a[112:115], v[208:211]\n"
"v_mul_f32 v13, v13, v11\n"
"v_accvgpr_read_b32 v10, a2\n"
"v_mfma_f32_16x16x32_bf16 v[208:211], a[52:55], a[116:119], v[208:211]\n"
"v_accvgpr_read_b32 v11, a3\n"
"v_mul_f32 v14, v14, v10\n"
"v_mfma_f32_16x16x32_bf16 v[236:239], a[60:63], a[116:119], v[236:239]\n"
"ds_read_b128 a[116:119], v5 offset:17408\n"
"v_mul_f32 v15, v15, v11\n"
"v_mfma_f32_16x16x32_bf16 v[236:239], a[56:59], a[112:115], v[236:239]\n"
"ds_read_b128 a[112:115], v5 offset:16384\n"
"v_cvt_pk_bf16_f32 v24, v12, v13\n"
"v_mfma_f32_16x16x32_bf16 v[240:243], a[56:59], a[120:123], v[240:243]\n"
"v_cvt_pk_bf16_f32 v25, v14, v15\n"
"v_mfma_f32_16x16x32_bf16 v[240:243], a[60:63], a[124:127], v[240:243]\n"
"ds_write_b64 v7, v[24:25] offset:38912\n"
"v_mfma_f32_16x16x32_bf16 v[212:215], a[52:55], a[124:127], v[212:215]\n"
"ds_read_b128 a[124:127], v3 offset:19456\n"
"v_lshlrev_b32 v12, 16, v18\n"
"v_mfma_f32_16x16x32_bf16 v[212:215], a[48:51], a[120:123], v[212:215]\n"
"ds_read_b128 a[120:123], v3 offset:18432\n"
"v_and_b32 v13, s63, v18\n"
"v_mfma_f32_16x16x32_bf16 v[216:219], a[48:51], a[128:131], v[216:219]\n"
"v_lshlrev_b32 v14, 16, v19\n"
"v_and_b32 v15, s63, v19\n"
"v_mfma_f32_16x16x32_bf16 v[216:219], a[52:55], a[132:135], v[216:219]\n"
"v_accvgpr_read_b32 v10, a4\n"
"v_accvgpr_read_b32 v11, a5\n"
"v_mfma_f32_16x16x32_bf16 v[244:247], a[60:63], a[132:135], v[244:247]\n"
"ds_read_b128 a[132:135], v4 offset:19456\n"
"v_mul_f32 v12, v12, v10\n"
"v_mfma_f32_16x16x32_bf16 v[244:247], a[56:59], a[128:131], v[244:247]\n"
"ds_read_b128 a[128:131], v4 offset:18432\n"
"v_mul_f32 v13, v13, v11\n"
"v_mfma_f32_16x16x32_bf16 v[248:251], a[56:59], a[136:139], v[248:251]\n"
"v_accvgpr_read_b32 v10, a6\n"
"v_mfma_f32_16x16x32_bf16 v[248:251], a[60:63], a[140:143], v[248:251]\n"
"v_accvgpr_read_b32 v11, a7\n"
"v_mul_f32 v14, v14, v10\n"
"v_mfma_f32_16x16x32_bf16 v[220:223], a[52:55], a[140:143], v[220:223]\n"
"ds_read_b128 a[140:143], v5 offset:19456\n"
"v_mul_f32 v15, v15, v11\n"
"v_mfma_f32_16x16x32_bf16 v[220:223], a[48:51], a[136:139], v[220:223]\n"
"ds_read_b128 a[136:139], v5 offset:18432\n"
"v_cvt_pk_bf16_f32 v26, v12, v13\n"
"v_mfma_f32_16x16x32_bf16 v[224:227], a[48:51], a[144:147], v[224:227]\n"
"v_cvt_pk_bf16_f32 v27, v14, v15\n"
"v_mfma_f32_16x16x32_bf16 v[224:227], a[52:55], a[148:151], v[224:227]\n"
"buffer_store_dwordx4 v[76:79], v6, s[52:55], s89 offen offset:1024 sc1\n"
"ds_write_b64 v7, v[26:27] offset:38920\n"
"v_mfma_f32_16x16x32_bf16 v[252:255], a[60:63], a[148:151], v[252:255]\n"
"ds_read_b128 a[148:151], v3 offset:21504\n"
"v_accvgpr_read_b32 v16, a188\n"
"v_mfma_f32_16x16x32_bf16 v[252:255], a[56:59], a[144:147], v[252:255]\n"
"ds_read_b128 a[144:147], v3 offset:20480\n"
"v_accvgpr_read_b32 v17, a189\n"
"v_mfma_f32_16x16x32_bf16 a[28:31], a[56:59], a[152:155], a[28:31]\n"
"v_accvgpr_read_b32 v18, a190\n"
"v_accvgpr_read_b32 v19, a191\n"
"v_mfma_f32_16x16x32_bf16 a[28:31], a[60:63], a[156:159], a[28:31]\n"
"v_lshlrev_b32 v12, 16, v16\n"
"v_and_b32 v13, s63, v16\n"
"v_mfma_f32_16x16x32_bf16 a[24:27], a[52:55], a[156:159], a[24:27]\n"
"ds_read_b128 a[156:159], v4 offset:21504\n"
"v_lshlrev_b32 v14, 16, v17\n"
"v_mfma_f32_16x16x32_bf16 a[24:27], a[48:51], a[152:155], a[24:27]\n"
"s_waitcnt lgkmcnt(15)\n"
"ds_read_b128 a[152:155], v4 offset:20480\n"
"v_and_b32 v15, s63, v17\n"
"v_mfma_f32_16x16x32_bf16 v[144:147], v[88:91], a[96:99], v[144:147]\n"
"v_accvgpr_read_b32 v10, a8\n"
"v_accvgpr_read_b32 v11, a9\n"
"v_mfma_f32_16x16x32_bf16 v[144:147], v[116:119], a[100:103], v[144:147]\n"
"v_mul_f32 v12, v12, v10\n"
"v_mul_f32 v13, v13, v11\n"
"v_mfma_f32_16x16x32_bf16 v[172:175], v[120:123], a[100:103], v[172:175]\n"
"v_accvgpr_read_b32 v10, a10\n"
"v_accvgpr_read_b32 v11, a11\n"
"v_mfma_f32_16x16x32_bf16 v[172:175], v[92:95], a[96:99], v[172:175]\n"
"buffer_store_dwordx4 v[52:55], v6, s[52:55], s90 offen offset:2048 sc1\n"
"v_mul_f32 v14, v14, v10\n"
"v_mul_f32 v15, v15, v11\n"
"v_cvt_pk_bf16_f32 v28, v12, v13\n"
"v_cvt_pk_bf16_f32 v29, v14, v15\n"
"v_mfma_f32_16x16x32_bf16 v[200:203], v[32:35], a[96:99], v[200:203]\n"
"ds_write_b64 v8, v[28:29] offset:38912\n"
"v_mfma_f32_16x16x32_bf16 v[200:203], v[60:63], a[100:103], v[200:203]\n"
"v_lshlrev_b32 v12, 16, v18\n"
"v_and_b32 v13, s63, v18\n"
"v_mfma_f32_16x16x32_bf16 v[228:231], v[124:127], a[100:103], v[228:231]\n"
"ds_read_b128 a[100:103], v5 offset:21504\n"
"v_lshlrev_b32 v14, 16, v19\n"
"v_mfma_f32_16x16x32_bf16 v[228:231], v[96:99], a[96:99], v[228:231]\n"
"s_waitcnt vmcnt(3) lgkmcnt(15)\n"
"ds_read_b128 a[96:99], v5 offset:20480\n"
"v_and_b32 v15, s63, v19\n"
"v_mfma_f32_16x16x32_bf16 v[228:231], v[108:111], a[104:107], v[228:231]\n"
"v_accvgpr_read_b32 v10, a12\n"
"v_accvgpr_read_b32 v11, a13\n"
"v_mfma_f32_16x16x32_bf16 v[228:231], v[136:139], a[108:111], v[228:231]\n"
"v_mul_f32 v12, v12, v10\n"
"v_mul_f32 v13, v13, v11\n"
"v_mfma_f32_16x16x32_bf16 v[200:203], v[132:135], a[108:111], v[200:203]\n"
"v_accvgpr_read_b32 v10, a14\n"
"v_accvgpr_read_b32 v11, a15\n"
"v_mfma_f32_16x16x32_bf16 v[200:203], v[104:107], a[104:107], v[200:203]\n"
"v_mul_f32 v14, v14, v10\n"
"v_mul_f32 v15, v15, v11\n"
"v_mfma_f32_16x16x32_bf16 v[172:175], v[36:39], a[104:107], v[172:175]\n"
"v_cvt_pk_bf16_f32 v30, v12, v13\n"
"v_cvt_pk_bf16_f32 v31, v14, v15\n"
"v_mfma_f32_16x16x32_bf16 v[172:175], v[64:67], a[108:111], v[172:175]\n"
"buffer_store_dwordx4 v[80:83], v6, s[52:55], s91 offen offset:3072 sc1\n"
"ds_write_b64 v8, v[30:31] offset:38920\n"
"v_mfma_f32_16x16x32_bf16 v[144:147], v[128:131], a[108:111], v[144:147]\n"
"ds_read_b128 a[108:111], v3 offset:23552\n"
"v_mfma_f32_16x16x32_bf16 v[144:147], v[100:103], a[104:107], v[144:147]\n"
"s_waitcnt lgkmcnt(15)\n"
"ds_read_b128 a[104:107], v3 offset:22528\n"
"v_mfma_f32_16x16x32_bf16 v[144:147], a[64:67], a[112:115], v[144:147]\n"
"v_mfma_f32_16x16x32_bf16 v[144:147], a[68:71], a[116:119], v[144:147]\n"
"v_mfma_f32_16x16x32_bf16 v[172:175], a[76:79], a[116:119], v[172:175]\n"
"v_mfma_f32_16x16x32_bf16 v[172:175], a[72:75], a[112:115], v[172:175]\n"
"ds_read_b128 v[112:115], v2 offset:38912\n"
"v_mfma_f32_16x16x32_bf16 v[200:203], a[80:83], a[112:115], v[200:203]\n"
"ds_read_b128 v[140:143], v2 offset:39936\n"
"v_mfma_f32_16x16x32_bf16 v[200:203], a[84:87], a[116:119], v[200:203]\n"
"v_accvgpr_read_b32 v16, a192\n"
"v_accvgpr_read_b32 v17, a193\n"
"v_mfma_f32_16x16x32_bf16 v[228:231], a[92:95], a[116:119], v[228:231]\n"
"ds_read_b128 a[116:119], v4 offset:23552\n"
"v_accvgpr_read_b32 v18, a194\n"
"v_mfma_f32_16x16x32_bf16 v[228:231], a[88:91], a[112:115], v[228:231]\n"
"s_waitcnt lgkmcnt(15)\n"
"buffer_store_dwordx4 v[56:59], v6, s[52:55], s92 offen offset:0 sc1\n"
"ds_read_b128 a[112:115], v4 offset:22528\n"
"v_accvgpr_read_b32 v19, a195\n"
"v_lshlrev_b32 v12, 16, v16\n"
"v_mfma_f32_16x16x32_bf16 v[232:235], v[96:99], a[120:123], v[232:235]\n"
"v_and_b32 v13, s63, v16\n"
"v_mfma_f32_16x16x32_bf16 v[232:235], v[124:127], a[124:127], v[232:235]\n"
"v_lshlrev_b32 v14, 16, v17\n"
"v_and_b32 v15, s63, v17\n"
"v_mfma_f32_16x16x32_bf16 v[204:207], v[60:63], a[124:127], v[204:207]\n"
"v_mul_f32 v12, v12, v144\n"
"v_mul_f32 v13, v13, v145\n"
"v_mfma_f32_16x16x32_bf16 v[204:207], v[32:35], a[120:123], v[204:207]\n"
"v_mul_f32 v14, v14, v146\n"
"v_mul_f32 v15, v15, v147\n"
"v_mfma_f32_16x16x32_bf16 v[176:179], v[92:95], a[120:123], v[176:179]\n"
"v_cvt_pk_bf16_f32 v24, v12, v13\n"
"v_cvt_pk_bf16_f32 v25, v14, v15\n"
"v_mfma_f32_16x16x32_bf16 v[176:179], v[120:123], a[124:127], v[176:179]\n"
"ds_write_b64 v7, v[24:25] offset:32768\n"
"v_mfma_f32_16x16x32_bf16 v[148:151], v[116:119], a[124:127], v[148:151]\n"
"ds_read_b128 a[124:127], v5 offset:23552\n"
"v_lshlrev_b32 v12, 16, v18\n"
"v_mfma_f32_16x16x32_bf16 v[148:151], v[88:91], a[120:123], v[148:151]\n"
"ds_read_b128 a[120:123], v5 offset:22528\n"
"v_and_b32 v13, s63, v18\n"
"v_mfma_f32_16x16x32_bf16 v[148:151], v[100:103], a[128:131], v[148:151]\n"
"v_lshlrev_b32 v14, 16, v19\n"
"v_mfma_f32_16x16x32_bf16 v[148:151], v[128:131], a[132:135], v[148:151]\n"
"buffer_store_dwordx4 v[84:87], v6, s[52:55], s93 offen offset:1024 sc1\n"
"v_and_b32 v15, s63, v19\n"
"v_mul_f32 v12, v12, v172\n"
"v_mul_f32 v13, v13, v173\n"
"v_mfma_f32_16x16x32_bf16 v[176:179], v[64:67], a[132:135], v[176:179]\n"
"v_mul_f32 v14, v14, v174\n"
"v_mul_f32 v15, v15, v175\n"
"v_mfma_f32_16x16x32_bf16 v[176:179], v[36:39], a[128:131], v[176:179]\n"
"v_cvt_pk_bf16_f32 v26, v12, v13\n"
"v_cvt_pk_bf16_f32 v27, v14, v15\n"
"v_mfma_f32_16x16x32_bf16 v[204:207], v[104:107], a[128:131], v[204:207]\n"
"ds_write_b64 v7, v[26:27] offset:32776\n"
"v_mfma_f32_16x16x32_bf16 v[204:207], v[132:135], a[132:135], v[204:207]\n"
"v_accvgpr_read_b32 v16, a196\n"
"v_mfma_f32_16x16x32_bf16 v[232:235], v[136:139], a[132:135], v[232:235]\n"
"ds_read_b128 a[132:135], v3 offset:25600\n"
"v_accvgpr_read_b32 v17, a197\n"
"v_mfma_f32_16x16x32_bf16 v[232:235], v[108:111], a[128:131], v[232:235]\n"
"s_waitcnt lgkmcnt(15)\n"
"ds_read_b128 a[128:131], v3 offset:24576\n"
"v_accvgpr_read_b32 v18, a198\n"
"v_mfma_f32_16x16x32_bf16 v[232:235], a[88:91], a[136:139], v[232:235]\n"
"v_accvgpr_read_b32 v19, a199\n"
"v_lshlrev_b32 v12, 16, v16\n"
"v_mfma_f32_16x16x32_bf16 v[232:235], a[92:95], a[140:143], v[232:235]\n"
"v_and_b32 v13, s63, v16\n"
"v_lshlrev_b32 v14, 16, v17\n"
"v_mfma_f32_16x16x32_bf16 v[204:207], a[84:87], a[140:143], v[204:207]\n"
"v_and_b32 v15, s63, v17\n"
"v_mul_f32 v12, v12, v200\n"
"v_mfma_f32_16x16x32_bf16 v[204:207], a[80:83], a[136:139], v[204:207]\n"
"s_waitcnt lgkmcnt(9)\n"
"buffer_store_dwordx4 v[112:115], v6, s[52:55], s94 offen offset:2048 sc1\n"
"v_mul_f32 v13, v13, v201\n"
"v_mul_f32 v14, v14, v202\n"
"v_mul_f32 v15, v15, v203\n"
"v_mfma_f32_16x16x32_bf16 v[176:179], a[72:75], a[136:139], v[176:179]\n"
"v_cvt_pk_bf16_f32 v28, v12, v13\n"
"v_cvt_pk_bf16_f32 v29, v14, v15\n"
"v_mfma_f32_16x16x32_bf16 v[176:179], a[76:79], a[140:143], v[176:179]\n"
"ds_write_b64 v8, v[28:29] offset:32768\n"
"v_mfma_f32_16x16x32_bf16 v[148:151], a[68:71], a[140:143], v[148:151]\n"
"ds_read_b128 a[140:143], v4 offset:25600\n"
"v_mfma_f32_16x16x32_bf16 v[148:151], a[64:67], a[136:139], v[148:151]\n"
"ds_read_b128 a[136:139], v4 offset:24576\n"
"v_lshlrev_b32 v12, 16, v18\n"
"v_mfma_f32_16x16x32_bf16 v[152:155], v[88:91], a[144:147], v[152:155]\n"
"v_and_b32 v13, s63, v18\n"
"v_lshlrev_b32 v14, 16, v19\n"
"v_mfma_f32_16x16x32_bf16 v[152:155], v[116:119], a[148:151], v[152:155]\n"
"v_and_b32 v15, s63, v19\n"
"v_mul_f32 v12, v12, v228\n"
"v_mfma_f32_16x16x32_bf16 v[180:183], v[120:123], a[148:151], v[180:183]\n"
"v_mul_f32 v13, v13, v229\n"
"v_mfma_f32_16x16x32_bf16 v[180:183], v[92:95], a[144:147], v[180:183]\n"
"v_mul_f32 v14, v14, v230\n"
"v_mul_f32 v15, v15, v231\n"
"v_mfma_f32_16x16x32_bf16 v[208:211], v[32:35], a[144:147], v[208:211]\n"
"v_cvt_pk_bf16_f32 v30, v12, v13\n"
"v_cvt_pk_bf16_f32 v31, v14, v15\n"
"v_mfma_f32_16x16x32_bf16 v[208:211], v[60:63], a[148:151], v[208:211]\n"
"s_waitcnt lgkmcnt(11)\n"
"buffer_store_dwordx4 v[140:143], v6, s[52:55], s95 offen offset:3072 sc1\n"
"ds_write_b64 v8, v[30:31] offset:32776\n"
"v_mfma_f32_16x16x32_bf16 v[236:239], v[124:127], a[148:151], v[236:239]\n"
"ds_read_b128 a[148:151], v5 offset:25600\n"
"v_mfma_f32_16x16x32_bf16 v[236:239], v[96:99], a[144:147], v[236:239]\n"
"ds_read_b128 a[144:147], v5 offset:24576\n"
"v_mfma_f32_16x16x32_bf16 v[236:239], v[108:111], a[152:155], v[236:239]\n"
"ds_read_b128 v[144:147], v2 offset:32768\n"
"v_mfma_f32_16x16x32_bf16 v[236:239], v[136:139], a[156:159], v[236:239]\n"
"ds_read_b128 v[172:175], v2 offset:33792\n"
"v_accvgpr_read_b32 v16, a200\n"
"v_mfma_f32_16x16x32_bf16 v[208:211], v[132:135], a[156:159], v[208:211]\n"
"v_accvgpr_read_b32 v17, a201\n"
"v_accvgpr_read_b32 v18, a202\n"
"v_mfma_f32_16x16x32_bf16 v[208:211], v[104:107], a[152:155], v[208:211]\n"
"v_accvgpr_read_b32 v19, a203\n"
"v_lshlrev_b32 v12, 16, v16\n"
"v_mfma_f32_16x16x32_bf16 v[180:183], v[36:39], a[152:155], v[180:183]\n"
"v_and_b32 v13, s63, v16\n"
"v_lshlrev_b32 v14, 16, v17\n"
"v_mfma_f32_16x16x32_bf16 v[180:183], v[64:67], a[156:159], v[180:183]\n"
"v_and_b32 v15, s63, v17\n"
"v_mul_f32 v12, v12, v148\n"
"v_mfma_f32_16x16x32_bf16 v[152:155], v[128:131], a[156:159], v[152:155]\n"
"ds_read_b128 a[156:159], v3 offset:27648\n"
"v_mul_f32 v13, v13, v149\n"
"v_mfma_f32_16x16x32_bf16 v[152:155], v[100:103], a[152:155], v[152:155]\n"
"ds_read_b128 a[152:155], v3 offset:26624\n"
"v_mul_f32 v14, v14, v150\n"
"v_mfma_f32_16x16x32_bf16 v[152:155], a[64:67], a[96:99], v[152:155]\n"
"v_mul_f32 v15, v15, v151\n"
"v_cvt_pk_bf16_f32 v24, v12, v13\n"
"v_mfma_f32_16x16x32_bf16 v[152:155], a[68:71], a[100:103], v[152:155]\n"
"v_cvt_pk_bf16_f32 v25, v14, v15\n"
"v_mfma_f32_16x16x32_bf16 v[180:183], a[76:79], a[100:103], v[180:183]\n"
"s_waitcnt lgkmcnt(3)\n"
"buffer_store_dwordx4 v[144:147], v6, s[52:55], s80 offen offset:128 sc1\n"
"ds_write_b64 v7, v[24:25] offset:34816\n"
"v_mfma_f32_16x16x32_bf16 v[180:183], a[72:75], a[96:99], v[180:183]\n"
"v_lshlrev_b32 v12, 16, v18\n"
"v_and_b32 v13, s63, v18\n"
"v_mfma_f32_16x16x32_bf16 v[208:211], a[80:83], a[96:99], v[208:211]\n"
"v_lshlrev_b32 v14, 16, v19\n"
"v_and_b32 v15, s63, v19\n"
"v_mfma_f32_16x16x32_bf16 v[208:211], a[84:87], a[100:103], v[208:211]\n"
"v_mul_f32 v12, v12, v176\n"
"v_mul_f32 v13, v13, v177\n"
"v_mfma_f32_16x16x32_bf16 v[236:239], a[92:95], a[100:103], v[236:239]\n"
"ds_read_b128 a[100:103], v4 offset:27648\n"
"v_mul_f32 v14, v14, v178\n"
"v_mfma_f32_16x16x32_bf16 v[236:239], a[88:91], a[96:99], v[236:239]\n"
"ds_read_b128 a[96:99], v4 offset:26624\n"
"v_mul_f32 v15, v15, v179\n"
"v_mfma_f32_16x16x32_bf16 v[240:243], v[96:99], a[104:107], v[240:243]\n"
"v_cvt_pk_bf16_f32 v26, v12, v13\n"
"v_cvt_pk_bf16_f32 v27, v14, v15\n"
"v_mfma_f32_16x16x32_bf16 v[240:243], v[124:127], a[108:111], v[240:243]\n"
"ds_write_b64 v7, v[26:27] offset:34824\n"
"v_mfma_f32_16x16x32_bf16 v[212:215], v[60:63], a[108:111], v[212:215]\n"
"v_accvgpr_read_b32 v16, a204\n"
"v_accvgpr_read_b32 v17, a205\n"
"v_mfma_f32_16x16x32_bf16 v[212:215], v[32:35], a[104:107], v[212:215]\n"
"v_accvgpr_read_b32 v18, a206\n"
"v_accvgpr_read_b32 v19, a207\n"
"v_mfma_f32_16x16x32_bf16 v[184:187], v[92:95], a[104:107], v[184:187]\n"
"s_waitcnt lgkmcnt(6)\n"
"buffer_store_dwordx4 v[172:175], v6, s[52:55], s81 offen offset:1152 sc1\n"
"v_lshlrev_b32 v12, 16, v16\n"
"v_and_b32 v13, s63, v16\n"
"v_lshlrev_b32 v14, 16, v17\n"
"v_mfma_f32_16x16x32_bf16 v[184:187], v[120:123], a[108:111], v[184:187]\n"
"v_and_b32 v15, s63, v17\n"
"v_mul_f32 v12, v12, v204\n"
"v_mfma_f32_16x16x32_bf16 v[156:159], v[116:119], a[108:111], v[156:159]\n"
"ds_read_b128 a[108:111], v5 offset:27648\n"
"v_mul_f32 v13, v13, v205\n"
"v_mfma_f32_16x16x32_bf16 v[156:159], v[88:91], a[104:107], v[156:159]\n"
"ds_read_b128 a[104:107], v5 offset:26624\n"
"v_mul_f32 v14, v14, v206\n"
"v_mfma_f32_16x16x32_bf16 v[156:159], v[100:103], a[112:115], v[156:159]\n"
"v_mul_f32 v15, v15, v207\n"
"v_cvt_pk_bf16_f32 v28, v12, v13\n"
"v_mfma_f32_16x16x32_bf16 v[156:159], v[128:131], a[116:119], v[156:159]\n"
"v_cvt_pk_bf16_f32 v29, v14, v15\n"
"v_mfma_f32_16x16x32_bf16 v[184:187], v[64:67], a[116:119], v[184:187]\n"
"ds_write_b64 v8, v[28:29] offset:34816\n"
"v_mfma_f32_16x16x32_bf16 v[184:187], v[36:39], a[112:115], v[184:187]\n"
"v_lshlrev_b32 v12, 16, v18\n"
"v_and_b32 v13, s63, v18\n"
"v_mfma_f32_16x16x32_bf16 v[212:215], v[104:107], a[112:115], v[212:215]\n"
"v_lshlrev_b32 v14, 16, v19\n"
"v_and_b32 v15, s63, v19\n"
"v_mfma_f32_16x16x32_bf16 v[212:215], v[132:135], a[116:119], v[212:215]\n"
"v_mul_f32 v12, v12, v232\n"
"v_mul_f32 v13, v13, v233\n"
"v_mfma_f32_16x16x32_bf16 v[240:243], v[136:139], a[116:119], v[240:243]\n"
"ds_read_b128 a[116:119], v3 offset:29696\n"
"v_mul_f32 v14, v14, v234\n"
"v_mfma_f32_16x16x32_bf16 v[240:243], v[108:111], a[112:115], v[240:243]\n"
"ds_read_b128 a[112:115], v3 offset:28672\n"
"v_mul_f32 v15, v15, v235\n"
"v_mfma_f32_16x16x32_bf16 v[240:243], a[88:91], a[120:123], v[240:243]\n"
"v_cvt_pk_bf16_f32 v30, v12, v13\n"
"v_cvt_pk_bf16_f32 v31, v14, v15\n"
"v_mfma_f32_16x16x32_bf16 v[240:243], a[92:95], a[124:127], v[240:243]\n"
"ds_write_b64 v8, v[30:31] offset:34824\n"
"v_mfma_f32_16x16x32_bf16 v[212:215], a[84:87], a[124:127], v[212:215]\n"
"ds_read_b128 v[148:151], v2 offset:34816\n"
"v_mfma_f32_16x16x32_bf16 v[212:215], a[80:83], a[120:123], v[212:215]\n"
"ds_read_b128 v[176:179], v2 offset:35840\n"
"v_accvgpr_read_b32 v16, a208\n"
"v_mfma_f32_16x16x32_bf16 v[184:187], a[72:75], a[120:123], v[184:187]\n"
"v_accvgpr_read_b32 v17, a209\n"
"v_accvgpr_read_b32 v18, a210\n"
"v_mfma_f32_16x16x32_bf16 v[184:187], a[76:79], a[124:127], v[184:187]\n"
"v_accvgpr_read_b32 v19, a211\n"
"v_lshlrev_b32 v12, 16, v16\n"
"v_mfma_f32_16x16x32_bf16 v[156:159], a[68:71], a[124:127], v[156:159]\n"
"ds_read_b128 a[124:127], v4 offset:29696\n"
"v_and_b32 v13, s63, v16\n"
"v_mfma_f32_16x16x32_bf16 v[156:159], a[64:67], a[120:123], v[156:159]\n"
"ds_read_b128 a[120:123], v4 offset:28672\n"
"v_lshlrev_b32 v14, 16, v17\n"
"v_mfma_f32_16x16x32_bf16 v[160:163], v[88:91], a[128:131], v[160:163]\n"
"v_and_b32 v15, s63, v17\n"
"v_mul_f32 v12, v12, v152\n"
"v_mfma_f32_16x16x32_bf16 v[160:163], v[116:119], a[132:135], v[160:163]\n"
"v_mul_f32 v13, v13, v153\n"
"v_mul_f32 v14, v14, v154\n"
"v_mfma_f32_16x16x32_bf16 v[188:191], v[120:123], a[132:135], v[188:191]\n"
"v_mul_f32 v15, v15, v155\n"
"v_cvt_pk_bf16_f32 v24, v12, v13\n"
"v_mfma_f32_16x16x32_bf16 v[188:191], v[92:95], a[128:131], v[188:191]\n"
"v_cvt_pk_bf16_f32 v25, v14, v15\n"
"v_mfma_f32_16x16x32_bf16 v[216:219], v[32:35], a[128:131], v[216:219]\n"
"s_waitcnt lgkmcnt(3)\n"
"buffer_store_dwordx4 v[148:151], v6, s[52:55], s82 offen offset:2176 sc1\n"
"ds_write_b64 v7, v[24:25] offset:36864\n"
"v_mfma_f32_16x16x32_bf16 v[216:219], v[60:63], a[132:135], v[216:219]\n"
"v_lshlrev_b32 v12, 16, v18\n"
"v_and_b32 v13, s63, v18\n"
"v_mfma_f32_16x16x32_bf16 v[244:247], v[124:127], a[132:135], v[244:247]\n"
"ds_read_b128 a[132:135], v5 offset:29696\n"
"v_lshlrev_b32 v14, 16, v19\n"
"v_mfma_f32_16x16x32_bf16 v[244:247], v[96:99], a[128:131], v[244:247]\n"
"ds_read_b128 a[128:131], v5 offset:28672\n"
"v_and_b32 v15, s63, v19\n"
"v_mfma_f32_16x16x32_bf16 v[244:247], v[108:111], a[136:139], v[244:247]\n"
"v_mul_f32 v12, v12, v180\n"
"v_mul_f32 v13, v13, v181\n"
"v_mfma_f32_16x16x32_bf16 v[244:247], v[136:139], a[140:143], v[244:247]\n"
"v_mul_f32 v14, v14, v182\n"
"v_mul_f32 v15, v15, v183\n"
"v_mfma_f32_16x16x32_bf16 v[216:219], v[132:135], a[140:143], v[216:219]\n"
"v_cvt_pk_bf16_f32 v26, v12, v13\n"
"v_cvt_pk_bf16_f32 v27, v14, v15\n"
"v_mfma_f32_16x16x32_bf16 v[216:219], v[104:107], a[136:139], v[216:219]\n"
"ds_write_b64 v7, v[26:27] offset:36872\n"
"v_mfma_f32_16x16x32_bf16 v[188:191], v[36:39], a[136:139], v[188:191]\n"
"v_accvgpr_read_b32 v16, a212\n"
"v_accvgpr_read_b32 v17, a213\n"
"v_mfma_f32_16x16x32_bf16 v[188:191], v[64:67], a[140:143], v[188:191]\n"
"v_accvgpr_read_b32 v18, a214\n"
"v_accvgpr_read_b32 v19, a215\n"
"v_mfma_f32_16x16x32_bf16 v[160:163], v[128:131], a[140:143], v[160:163]\n"
"s_waitcnt lgkmcnt(6)\n"
"buffer_store_dwordx4 v[176:179], v6, s[52:55], s83 offen offset:3200 sc1\n"
"ds_read_b128 a[140:143], v3 offset:31744\n"
"v_lshlrev_b32 v12, 16, v16\n"
"v_and_b32 v13, s63, v16\n"
"v_mfma_f32_16x16x32_bf16 v[160:163], v[100:103], a[136:139], v[160:163]\n"
"ds_read_b128 a[136:139], v3 offset:30720\n"
"v_lshlrev_b32 v14, 16, v17\n"
"v_mfma_f32_16x16x32_bf16 v[160:163], a[64:67], a[144:147], v[160:163]\n"
"v_and_b32 v15, s63, v17\n"
"v_mul_f32 v12, v12, v208\n"
"v_mfma_f32_16x16x32_bf16 v[160:163], a[68:71], a[148:151], v[160:163]\n"
"v_mul_f32 v13, v13, v209\n"
"v_mul_f32 v14, v14, v210\n"
"v_mfma_f32_16x16x32_bf16 v[188:191], a[76:79], a[148:151], v[188:191]\n"
"v_mul_f32 v15, v15, v211\n"
"v_cvt_pk_bf16_f32 v28, v12, v13\n"
"v_mfma_f32_16x16x32_bf16 v[188:191], a[72:75], a[144:147], v[188:191]\n"
"v_cvt_pk_bf16_f32 v29, v14, v15\n"
"v_mfma_f32_16x16x32_bf16 v[216:219], a[80:83], a[144:147], v[216:219]\n"
"ds_write_b64 v8, v[28:29] offset:36864\n"
"v_mfma_f32_16x16x32_bf16 v[216:219], a[84:87], a[148:151], v[216:219]\n"
"v_lshlrev_b32 v12, 16, v18\n"
"v_and_b32 v13, s63, v18\n"
"v_mfma_f32_16x16x32_bf16 v[244:247], a[92:95], a[148:151], v[244:247]\n"
"ds_read_b128 a[148:151], v4 offset:31744\n"
"v_lshlrev_b32 v14, 16, v19\n"
"v_mfma_f32_16x16x32_bf16 v[244:247], a[88:91], a[144:147], v[244:247]\n"
"ds_read_b128 a[144:147], v4 offset:30720\n"
"v_and_b32 v15, s63, v19\n"
"v_mfma_f32_16x16x32_bf16 v[248:251], v[96:99], a[152:155], v[248:251]\n"
"v_mul_f32 v12, v12, v236\n"
"v_mul_f32 v13, v13, v237\n"
"v_mfma_f32_16x16x32_bf16 v[248:251], v[124:127], a[156:159], v[248:251]\n"
"v_mul_f32 v14, v14, v238\n"
"v_mul_f32 v15, v15, v239\n"
"v_mfma_f32_16x16x32_bf16 v[220:223], v[60:63], a[156:159], v[220:223]\n"
"v_cvt_pk_bf16_f32 v30, v12, v13\n"
"v_cvt_pk_bf16_f32 v31, v14, v15\n"
"v_mfma_f32_16x16x32_bf16 v[220:223], v[32:35], a[152:155], v[220:223]\n"
"ds_write_b64 v8, v[30:31] offset:36872\n"
"v_mfma_f32_16x16x32_bf16 v[192:195], v[92:95], a[152:155], v[192:195]\n"
"ds_read_b128 v[152:155], v2 offset:36864\n"
"v_mfma_f32_16x16x32_bf16 v[192:195], v[120:123], a[156:159], v[192:195]\n"
"ds_read_b128 v[180:183], v2 offset:37888\n"
"v_accvgpr_read_b32 v16, a216\n"
"v_mfma_f32_16x16x32_bf16 v[164:167], v[116:119], a[156:159], v[164:167]\n"
"ds_read_b128 a[156:159], v5 offset:31744\n"
"v_accvgpr_read_b32 v17, a217\n"
"v_mfma_f32_16x16x32_bf16 v[164:167], v[88:91], a[152:155], v[164:167]\n"
"ds_read_b128 a[152:155], v5 offset:30720\n"
"v_accvgpr_read_b32 v18, a218\n"
"v_mfma_f32_16x16x32_bf16 v[164:167], v[100:103], a[96:99], v[164:167]\n"
"v_accvgpr_read_b32 v19, a219\n"
"v_lshlrev_b32 v12, 16, v16\n"
"v_mfma_f32_16x16x32_bf16 v[164:167], v[128:131], a[100:103], v[164:167]\n"
"v_and_b32 v13, s63, v16\n"
"v_lshlrev_b32 v14, 16, v17\n"
"v_mfma_f32_16x16x32_bf16 v[192:195], v[64:67], a[100:103], v[192:195]\n"
"v_and_b32 v15, s63, v17\n"
"v_mul_f32 v12, v12, v156\n"
"v_mfma_f32_16x16x32_bf16 v[192:195], v[36:39], a[96:99], v[192:195]\n"
"v_mul_f32 v13, v13, v157\n"
"v_mul_f32 v14, v14, v158\n"
"v_mfma_f32_16x16x32_bf16 v[220:223], v[104:107], a[96:99], v[220:223]\n"
"v_mul_f32 v15, v15, v159\n"
"v_mfma_f32_16x16x32_bf16 v[220:223], v[132:135], a[100:103], v[220:223]\n"
"v_cvt_pk_bf16_f32 v24, v12, v13\n"
"v_mfma_f32_16x16x32_bf16 v[248:251], v[136:139], a[100:103], v[248:251]\n"
"v_cvt_pk_bf16_f32 v25, v14, v15\n"
"v_mfma_f32_16x16x32_bf16 v[248:251], v[108:111], a[96:99], v[248:251]\n"
"s_waitcnt lgkmcnt(3)\n"
"buffer_store_dwordx4 v[152:155], v6, s[52:55], s84 offen offset:128 sc1\n"
"ds_write_b64 v7, v[24:25] offset:38912\n"
"v_mfma_f32_16x16x32_bf16 v[248:251], a[88:91], a[104:107], v[248:251]\n"
"v_lshlrev_b32 v12, 16, v18\n"
"v_and_b32 v13, s63, v18\n"
"v_mfma_f32_16x16x32_bf16 v[248:251], a[92:95], a[108:111], v[248:251]\n"
"v_lshlrev_b32 v14, 16, v19\n"
"v_mfma_f32_16x16x32_bf16 v[220:223], a[84:87], a[108:111], v[220:223]\n"
"v_and_b32 v15, s63, v19\n"
"v_mfma_f32_16x16x32_bf16 v[220:223], a[80:83], a[104:107], v[220:223]\n"
"v_mul_f32 v12, v12, v184\n"
"v_mfma_f32_16x16x32_bf16 v[192:195], a[72:75], a[104:107], v[192:195]\n"
"v_mul_f32 v13, v13, v185\n"
"v_mfma_f32_16x16x32_bf16 v[192:195], a[76:79], a[108:111], v[192:195]\n"
"v_mul_f32 v14, v14, v186\n"
"v_mfma_f32_16x16x32_bf16 v[164:167], a[68:71], a[108:111], v[164:167]\n"
"v_mul_f32 v15, v15, v187\n"
"v_mfma_f32_16x16x32_bf16 v[164:167], a[64:67], a[104:107], v[164:167]\n"
"v_cvt_pk_bf16_f32 v26, v12, v13\n"
"v_mfma_f32_16x16x32_bf16 v[168:171], v[88:91], a[112:115], v[168:171]\n"
"v_cvt_pk_bf16_f32 v27, v14, v15\n"
"v_mfma_f32_16x16x32_bf16 v[168:171], v[116:119], a[116:119], v[168:171]\n"
"s_waitcnt lgkmcnt(3)\n"
"buffer_store_dwordx4 v[180:183], v6, s[52:55], s85 offen offset:1152 sc1\n"
"ds_write_b64 v7, v[26:27] offset:38920\n"
"v_mfma_f32_16x16x32_bf16 v[196:199], v[120:123], a[116:119], v[196:199]\n"
"v_accvgpr_read_b32 v16, a220\n"
"v_accvgpr_read_b32 v17, a221\n"
"v_mfma_f32_16x16x32_bf16 v[196:199], v[92:95], a[112:115], v[196:199]\n"
"v_accvgpr_read_b32 v18, a222\n"
"v_accvgpr_read_b32 v19, a223\n"
"v_mfma_f32_16x16x32_bf16 v[224:227], v[32:35], a[112:115], v[224:227]\n"
"v_lshlrev_b32 v12, 16, v16\n"
"v_and_b32 v13, s63, v16\n"
"v_mfma_f32_16x16x32_bf16 v[224:227], v[60:63], a[116:119], v[224:227]\n"
"v_lshlrev_b32 v14, 16, v17\n"
"v_and_b32 v15, s63, v17\n"
"v_mfma_f32_16x16x32_bf16 v[252:255], v[124:127], a[116:119], v[252:255]\n"
"v_mul_f32 v12, v12, v212\n"
"v_mul_f32 v13, v13, v213\n"
"v_mfma_f32_16x16x32_bf16 v[252:255], v[96:99], a[112:115], v[252:255]\n"
"v_mul_f32 v14, v14, v214\n"
"v_mul_f32 v15, v15, v215\n"
"v_mfma_f32_16x16x32_bf16 v[252:255], v[108:111], a[120:123], v[252:255]\n"
"v_cvt_pk_bf16_f32 v28, v12, v13\n"
"v_cvt_pk_bf16_f32 v29, v14, v15\n"
"v_mfma_f32_16x16x32_bf16 v[252:255], v[136:139], a[124:127], v[252:255]\n"
"ds_write_b64 v8, v[28:29] offset:38912\n"
"v_mfma_f32_16x16x32_bf16 v[224:227], v[132:135], a[124:127], v[224:227]\n"
"v_lshlrev_b32 v12, 16, v18\n"
"v_and_b32 v13, s63, v18\n"
"v_mfma_f32_16x16x32_bf16 v[224:227], v[104:107], a[120:123], v[224:227]\n"
"v_lshlrev_b32 v14, 16, v19\n"
"v_and_b32 v15, s63, v19\n"
"v_mfma_f32_16x16x32_bf16 v[196:199], v[36:39], a[120:123], v[196:199]\n"
"v_mul_f32 v12, v12, v240\n"
"v_mul_f32 v13, v13, v241\n"
"v_mfma_f32_16x16x32_bf16 v[196:199], v[64:67], a[124:127], v[196:199]\n"
"v_mul_f32 v14, v14, v242\n"
"v_mul_f32 v15, v15, v243\n"
"v_mfma_f32_16x16x32_bf16 v[168:171], v[128:131], a[124:127], v[168:171]\n"
"v_cvt_pk_bf16_f32 v30, v12, v13\n"
"v_cvt_pk_bf16_f32 v31, v14, v15\n"
"v_mfma_f32_16x16x32_bf16 v[168:171], v[100:103], a[120:123], v[168:171]\n"
"ds_write_b64 v8, v[30:31] offset:38920\n"
"v_mfma_f32_16x16x32_bf16 v[168:171], a[64:67], a[128:131], v[168:171]\n"
"ds_read_b128 v[156:159], v2 offset:38912\n"
"v_mfma_f32_16x16x32_bf16 v[168:171], a[68:71], a[132:135], v[168:171]\n"
"ds_read_b128 v[184:187], v2 offset:39936\n"
"v_accvgpr_read_b32 v16, a224\n"
"v_mfma_f32_16x16x32_bf16 v[196:199], a[76:79], a[132:135], v[196:199]\n"
"v_accvgpr_read_b32 v17, a225\n"
"v_accvgpr_read_b32 v18, a226\n"
"v_mfma_f32_16x16x32_bf16 v[196:199], a[72:75], a[128:131], v[196:199]\n"
"v_accvgpr_read_b32 v19, a227\n"
"v_lshlrev_b32 v12, 16, v16\n"
"v_mfma_f32_16x16x32_bf16 v[224:227], a[80:83], a[128:131], v[224:227]\n"
"v_and_b32 v13, s63, v16\n"
"v_lshlrev_b32 v14, 16, v17\n"
"v_mfma_f32_16x16x32_bf16 v[224:227], a[84:87], a[132:135], v[224:227]\n"
"v_and_b32 v15, s63, v17\n"
"v_mul_f32 v12, v12, v160\n"
"v_mfma_f32_16x16x32_bf16 v[252:255], a[92:95], a[132:135], v[252:255]\n"
"v_mul_f32 v13, v13, v161\n"
"v_mul_f32 v14, v14, v162\n"
"v_mfma_f32_16x16x32_bf16 v[252:255], a[88:91], a[128:131], v[252:255]\n"
"v_mul_f32 v15, v15, v163\n"
"v_cvt_pk_bf16_f32 v24, v12, v13\n"
"v_mfma_f32_16x16x32_bf16 a[28:31], v[96:99], a[136:139], a[28:31]\n"
"v_cvt_pk_bf16_f32 v25, v14, v15\n"
"v_mfma_f32_16x16x32_bf16 a[28:31], v[124:127], a[140:143], a[28:31]\n"
"s_waitcnt lgkmcnt(1)\n"
"buffer_store_dwordx4 v[156:159], v6, s[52:55], s86 offen offset:2176 sc1\n"
"ds_write_b64 v7, v[24:25] offset:32768\n"
"v_mfma_f32_16x16x32_bf16 a[24:27], v[60:63], a[140:143], a[24:27]\n"
"v_lshlrev_b32 v12, 16, v18\n"
"v_and_b32 v13, s63, v18\n"
"v_mfma_f32_16x16x32_bf16 a[24:27], v[32:35], a[136:139], a[24:27]\n"
"v_lshlrev_b32 v14, 16, v19\n"
"v_and_b32 v15, s63, v19\n"
"v_mfma_f32_16x16x32_bf16 a[20:23], v[92:95], a[136:139], a[20:23]\n"
"v_mul_f32 v12, v12, v188\n"
"v_mul_f32 v13, v13, v189\n"
"v_mfma_f32_16x16x32_bf16 a[20:23], v[120:123], a[140:143], a[20:23]\n"
"v_mul_f32 v14, v14, v190\n"
"v_mul_f32 v15, v15, v191\n"
"v_mfma_f32_16x16x32_bf16 a[16:19], v[116:119], a[140:143], a[16:19]\n"
"v_cvt_pk_bf16_f32 v26, v12, v13\n"
"v_cvt_pk_bf16_f32 v27, v14, v15\n"
"v_mfma_f32_16x16x32_bf16 a[16:19], v[88:91], a[136:139], a[16:19]\n"
"ds_write_b64 v7, v[26:27] offset:32776\n"
"v_mfma_f32_16x16x32_bf16 a[16:19], v[100:103], a[144:147], a[16:19]\n"
"v_accvgpr_read_b32 v16, a228\n"
"v_accvgpr_read_b32 v17, a229\n"
"v_mfma_f32_16x16x32_bf16 a[16:19], v[128:131], a[148:151], a[16:19]\n"
"v_accvgpr_read_b32 v18, a230\n"
"v_accvgpr_read_b32 v19, a231\n"
"v_mfma_f32_16x16x32_bf16 a[20:23], v[64:67], a[148:151], a[20:23]\n"
"v_lshlrev_b32 v12, 16, v16\n"
"v_and_b32 v13, s63, v16\n"
"v_mfma_f32_16x16x32_bf16 a[20:23], v[36:39], a[144:147], a[20:23]\n"
"s_waitcnt lgkmcnt(2)\n"
"buffer_store_dwordx4 v[184:187], v6, s[52:55], s87 offen offset:3200 sc1\n"
"v_lshlrev_b32 v14, 16, v17\n"
"v_and_b32 v15, s63, v17\n"
"v_mul_f32 v12, v12, v216\n"
"v_mfma_f32_16x16x32_bf16 a[24:27], v[104:107], a[144:147], a[24:27]\n"
"v_mul_f32 v13, v13, v217\n"
"v_mul_f32 v14, v14, v218\n"
"v_mfma_f32_16x16x32_bf16 a[24:27], v[132:135], a[148:151], a[24:27]\n"
"v_mul_f32 v15, v15, v219\n"
"v_mfma_f32_16x16x32_bf16 a[28:31], v[136:139], a[148:151], a[28:31]\n"
"v_cvt_pk_bf16_f32 v28, v12, v13\n"
"v_cvt_pk_bf16_f32 v29, v14, v15\n"
"v_mfma_f32_16x16x32_bf16 a[28:31], v[108:111], a[144:147], a[28:31]\n"
"ds_write_b64 v8, v[28:29] offset:32768\n"
"v_mfma_f32_16x16x32_bf16 a[28:31], a[88:91], a[152:155], a[28:31]\n"
"v_lshlrev_b32 v12, 16, v18\n"
"v_and_b32 v13, s63, v18\n"
"v_mfma_f32_16x16x32_bf16 a[28:31], a[92:95], a[156:159], a[28:31]\n"
"v_lshlrev_b32 v14, 16, v19\n"
"v_and_b32 v15, s63, v19\n"
"v_mfma_f32_16x16x32_bf16 a[24:27], a[84:87], a[156:159], a[24:27]\n"
"v_mul_f32 v12, v12, v244\n"
"v_mul_f32 v13, v13, v245\n"
"v_mfma_f32_16x16x32_bf16 a[24:27], a[80:83], a[152:155], a[24:27]\n"
"v_mul_f32 v14, v14, v246\n"
"v_mul_f32 v15, v15, v247\n"
"v_mfma_f32_16x16x32_bf16 a[20:23], a[72:75], a[152:155], a[20:23]\n"
"s_add_u32 s62, s62, 1\n"
"v_cvt_pk_bf16_f32 v30, v12, v13\n"
"v_cvt_pk_bf16_f32 v31, v14, v15\n"
"v_mfma_f32_16x16x32_bf16 a[20:23], a[76:79], a[156:159], a[20:23]\n"
"ds_write_b64 v8, v[30:31] offset:32776\n"
"v_mfma_f32_16x16x32_bf16 a[16:19], a[68:71], a[156:159], a[16:19]\n"
"ds_read_b128 v[160:163], v2 offset:32768\n"
"v_mfma_f32_16x16x32_bf16 a[16:19], a[64:67], a[152:155], a[16:19]\n"
"ds_read_b128 v[188:191], v2 offset:33792\n"
"s_nop 7\n"
"v_accvgpr_read_b32 v16, a232\n"
"v_accvgpr_read_b32 v17, a233\n"
"v_accvgpr_read_b32 v18, a234\n"
"v_accvgpr_read_b32 v19, a235\n"
"v_lshlrev_b32 v12, 16, v16\n"
"v_and_b32 v13, s63, v16\n"
"v_lshlrev_b32 v14, 16, v17\n"
"v_and_b32 v15, s63, v17\n"
"v_pk_mul_f32 v[12:13], v[12:13], v[164:165]\n"
"v_pk_mul_f32 v[14:15], v[14:15], v[166:167]\n"
"v_cvt_pk_bf16_f32 v24, v12, v13\n"
"v_cvt_pk_bf16_f32 v25, v14, v15\n"
"v_lshlrev_b32 v12, 16, v18\n"
"v_and_b32 v13, s63, v18\n"
"v_lshlrev_b32 v14, 16, v19\n"
"v_and_b32 v15, s63, v19\n"
"v_pk_mul_f32 v[12:13], v[12:13], v[192:193]\n"
"v_pk_mul_f32 v[14:15], v[14:15], v[194:195]\n"
"v_cvt_pk_bf16_f32 v26, v12, v13\n"
"v_cvt_pk_bf16_f32 v27, v14, v15\n"
"ds_write_b128 v7, v[24:27] offset:34816\n"
"s_waitcnt lgkmcnt(2)\n"
"buffer_store_dwordx4 v[160:163], v6, s[52:55], s88 offen offset:128 sc1\n"
"s_waitcnt lgkmcnt(1)\n"
"buffer_store_dwordx4 v[188:191], v6, s[52:55], s89 offen offset:1152 sc1\n"
"v_accvgpr_read_b32 v16, a236\n"
"v_accvgpr_read_b32 v17, a237\n"
"v_accvgpr_read_b32 v18, a238\n"
"v_accvgpr_read_b32 v19, a239\n"
"v_lshlrev_b32 v12, 16, v16\n"
"v_and_b32 v13, s63, v16\n"
"v_lshlrev_b32 v14, 16, v17\n"
"v_and_b32 v15, s63, v17\n"
"v_pk_mul_f32 v[12:13], v[12:13], v[220:221]\n"
"v_pk_mul_f32 v[14:15], v[14:15], v[222:223]\n"
"v_cvt_pk_bf16_f32 v28, v12, v13\n"
"v_cvt_pk_bf16_f32 v29, v14, v15\n"
"v_lshlrev_b32 v12, 16, v18\n"
"v_and_b32 v13, s63, v18\n"
"v_lshlrev_b32 v14, 16, v19\n"
"v_and_b32 v15, s63, v19\n"
"v_pk_mul_f32 v[12:13], v[12:13], v[248:249]\n"
"v_pk_mul_f32 v[14:15], v[14:15], v[250:251]\n"
"v_cvt_pk_bf16_f32 v30, v12, v13\n"
"v_cvt_pk_bf16_f32 v31, v14, v15\n"
"ds_write_b128 v8, v[28:31] offset:34816\n"
"ds_read_b128 v[164:167], v2 offset:34816\n"
"ds_read_b128 v[192:195], v2 offset:35840\n"
"v_accvgpr_read_b32 v16, a240\n"
"v_accvgpr_read_b32 v17, a241\n"
"v_accvgpr_read_b32 v18, a242\n"
"v_accvgpr_read_b32 v19, a243\n"
"v_lshlrev_b32 v12, 16, v16\n"
"v_and_b32 v13, s63, v16\n"
"v_lshlrev_b32 v14, 16, v17\n"
"v_and_b32 v15, s63, v17\n"
"v_pk_mul_f32 v[12:13], v[12:13], v[168:169]\n"
"v_pk_mul_f32 v[14:15], v[14:15], v[170:171]\n"
"v_cvt_pk_bf16_f32 v24, v12, v13\n"
"v_cvt_pk_bf16_f32 v25, v14, v15\n"
"v_lshlrev_b32 v12, 16, v18\n"
"v_and_b32 v13, s63, v18\n"
"v_lshlrev_b32 v14, 16, v19\n"
"v_and_b32 v15, s63, v19\n"
"v_pk_mul_f32 v[12:13], v[12:13], v[196:197]\n"
"v_pk_mul_f32 v[14:15], v[14:15], v[198:199]\n"
"v_cvt_pk_bf16_f32 v26, v12, v13\n"
"v_cvt_pk_bf16_f32 v27, v14, v15\n"
"ds_write_b128 v7, v[24:27] offset:36864\n"
"s_waitcnt lgkmcnt(2)\n"
"buffer_store_dwordx4 v[164:167], v6, s[52:55], s90 offen offset:2176 sc1\n"
"s_waitcnt lgkmcnt(1)\n"
"buffer_store_dwordx4 v[192:195], v6, s[52:55], s91 offen offset:3200 sc1\n"
"v_accvgpr_read_b32 v16, a244\n"
"v_accvgpr_read_b32 v17, a245\n"
"v_accvgpr_read_b32 v18, a246\n"
"v_accvgpr_read_b32 v19, a247\n"
"v_lshlrev_b32 v12, 16, v16\n"
"v_and_b32 v13, s63, v16\n"
"v_lshlrev_b32 v14, 16, v17\n"
"v_and_b32 v15, s63, v17\n"
"v_pk_mul_f32 v[12:13], v[12:13], v[224:225]\n"
"v_pk_mul_f32 v[14:15], v[14:15], v[226:227]\n"
"v_cvt_pk_bf16_f32 v28, v12, v13\n"
"v_cvt_pk_bf16_f32 v29, v14, v15\n"
"v_lshlrev_b32 v12, 16, v18\n"
"v_and_b32 v13, s63, v18\n"
"v_lshlrev_b32 v14, 16, v19\n"
"v_and_b32 v15, s63, v19\n"
"v_pk_mul_f32 v[12:13], v[12:13], v[252:253]\n"
"v_pk_mul_f32 v[14:15], v[14:15], v[254:255]\n"
"v_cvt_pk_bf16_f32 v30, v12, v13\n"
"v_cvt_pk_bf16_f32 v31, v14, v15\n"
"ds_write_b128 v8, v[28:31] offset:36864\n"
"ds_read_b128 v[168:171], v2 offset:36864\n"
"ds_read_b128 v[196:199], v2 offset:37888\n"
"v_accvgpr_read_b32 v16, a248\n"
"v_accvgpr_read_b32 v17, a249\n"
"v_accvgpr_read_b32 v18, a250\n"
"v_accvgpr_read_b32 v19, a251\n"
"v_lshlrev_b32 v12, 16, v16\n"
"v_and_b32 v13, s63, v16\n"
"v_lshlrev_b32 v14, 16, v17\n"
"v_and_b32 v15, s63, v17\n"
"v_accvgpr_read_b32 v10, a16\n"
"v_accvgpr_read_b32 v11, a17\n"
"v_mul_f32 v12, v12, v10\n"
"v_mul_f32 v13, v13, v11\n"
"v_accvgpr_read_b32 v10, a18\n"
"v_accvgpr_read_b32 v11, a19\n"
"v_mul_f32 v14, v14, v10\n"
"v_mul_f32 v15, v15, v11\n"
"v_cvt_pk_bf16_f32 v24, v12, v13\n"
"v_cvt_pk_bf16_f32 v25, v14, v15\n"
"v_lshlrev_b32 v12, 16, v18\n"
"v_and_b32 v13, s63, v18\n"
"v_lshlrev_b32 v14, 16, v19\n"
"v_and_b32 v15, s63, v19\n"
"v_accvgpr_read_b32 v10, a20\n"
"v_accvgpr_read_b32 v11, a21\n"
"v_mul_f32 v12, v12, v10\n"
"v_mul_f32 v13, v13, v11\n"
"v_accvgpr_read_b32 v10, a22\n"
"v_accvgpr_read_b32 v11, a23\n"
"v_mul_f32 v14, v14, v10\n"
"v_mul_f32 v15, v15, v11\n"
"v_cvt_pk_bf16_f32 v26, v12, v13\n"
"v_cvt_pk_bf16_f32 v27, v14, v15\n"
"ds_write_b128 v7, v[24:27] offset:38912\n"
"s_waitcnt lgkmcnt(2)\n"
"buffer_store_dwordx4 v[168:171], v6, s[52:55], s92 offen offset:128 sc1\n"
"s_waitcnt lgkmcnt(1)\n"
"buffer_store_dwordx4 v[196:199], v6, s[52:55], s93 offen offset:1152 sc1\n"
"v_accvgpr_read_b32 v16, a252\n"
"v_accvgpr_read_b32 v17, a253\n"
"v_accvgpr_read_b32 v18, a254\n"
"v_accvgpr_read_b32 v19, a255\n"
"v_lshlrev_b32 v12, 16, v16\n"
"v_and_b32 v13, s63, v16\n"
"v_lshlrev_b32 v14, 16, v17\n"
"v_and_b32 v15, s63, v17\n"
"v_accvgpr_read_b32 v10, a24\n"
"v_accvgpr_read_b32 v11, a25\n"
"v_mul_f32 v12, v12, v10\n"
"v_mul_f32 v13, v13, v11\n"
"v_accvgpr_read_b32 v10, a26\n"
"v_accvgpr_read_b32 v11, a27\n"
"v_mul_f32 v14, v14, v10\n"
"v_mul_f32 v15, v15, v11\n"
"v_cvt_pk_bf16_f32 v28, v12, v13\n"
"v_cvt_pk_bf16_f32 v29, v14, v15\n"
"v_lshlrev_b32 v12, 16, v18\n"
"v_and_b32 v13, s63, v18\n"
"v_lshlrev_b32 v14, 16, v19\n"
"v_and_b32 v15, s63, v19\n"
"v_accvgpr_read_b32 v10, a28\n"
"v_accvgpr_read_b32 v11, a29\n"
"v_mul_f32 v12, v12, v10\n"
"v_mul_f32 v13, v13, v11\n"
"v_accvgpr_read_b32 v10, a30\n"
"v_accvgpr_read_b32 v11, a31\n"
"v_mul_f32 v14, v14, v10\n"
"v_mul_f32 v15, v15, v11\n"
"v_cvt_pk_bf16_f32 v30, v12, v13\n"
"v_cvt_pk_bf16_f32 v31, v14, v15\n"
"ds_write_b128 v8, v[28:31] offset:38912\n"
"ds_read_b128 v[224:227], v2 offset:38912\n"
"ds_read_b128 v[252:255], v2 offset:39936\n"
"s_waitcnt lgkmcnt(1)\n"
"buffer_store_dwordx4 v[224:227], v6, s[52:55], s94 offen offset:2176 sc1\n"
"s_waitcnt lgkmcnt(0)\n"
"buffer_store_dwordx4 v[252:255], v6, s[52:55], s95 offen offset:3200 sc1\n"
"s_or_b32 s66, %26, %16\n"
"s_cbranch_scc1 L_end\n"
"v_mov_b32 v10, s78\n"
"v_mov_b32 v11, s79\n"
"s_mov_b32 s66, 139264\n"
"buffer_store_dwordx2 v[10:11], off, s[56:59], s66 sc1\n"
"s_branch L_end\n"
"L_slow0:\n"
"s_cmp_eq_u32 s68, 0\n"
"s_cbranch_scc1 L_back0\n"
"s_sub_u32 s68, s68, 1\n"
"s_sleep 2\n"
"buffer_load_dwordx2 v[8:9], off, s[56:59], %14 sc1\n"
"s_waitcnt vmcnt(0)\n"
"v_subrev_u32 v8, s65, v8\n"
"v_xor_b32 v9, s79, v9\n"
"v_or_b32 v8, v8, v9\n"
"v_cmp_lt_u32 vcc, 15, v8\n"
"s_nop 4\n"
"s_cbranch_vccnz L_slow0\n"
"s_branch L_back0\n"
"L_slow1:\n"
"s_cmp_eq_u32 s68, 0\n"
"s_cbranch_scc1 L_back1\n"
"s_sub_u32 s68, s68, 1\n"
"s_sleep 2\n"
"buffer_load_dwordx2 v[8:9], off, s[56:59], %14 sc1\n"
"s_waitcnt vmcnt(0)\n"
"v_subrev_u32 v8, s65, v8\n"
"v_xor_b32 v9, s79, v9\n"
"v_or_b32 v8, v8, v9\n"
"v_cmp_lt_u32 vcc, 15, v8\n"
"s_nop 4\n"
"s_cbranch_vccnz L_slow1\n"
"s_branch L_back1\n"
"L_slow2:\n"
"s_cmp_eq_u32 s68, 0\n"
"s_cbranch_scc1 L_back2\n"
"s_sub_u32 s68, s68, 1\n"
"s_sleep 2\n"
"buffer_load_dwordx4 v[12:15], v0, s[56:59], %25 offen sc1\n"
"s_waitcnt vmcnt(0)\n"
"v_subrev_u32 v12, s67, v12\n"
"v_xor_b32 v13, s79, v13\n"
"v_subrev_u32 v14, s67, v14\n"
"v_xor_b32 v15, s79, v15\n"
"v_or_b32 v12, v12, v14\n"
"v_or_b32 v13, v13, v15\n"
"v_or_b32 v12, v12, v13\n"
"v_cmp_lt_u32 vcc, 15, v12\n"
"s_nop 4\n"
"s_cbranch_vccnz L_slow2\n"
"s_branch L_back2\n"
"L_slow3:\n"
"s_cmp_eq_u32 s68, 0\n"
"s_cbranch_scc1 L_back3\n"
"s_sub_u32 s68, s68, 1\n"
"s_sleep 2\n"
"buffer_load_dwordx4 v[24:27], v0, s[56:59], s65 offen sc1\n"
"s_waitcnt vmcnt(0)\n"
"v_subrev_u32 v24, s70, v24\n"
"v_xor_b32 v25, s79, v25\n"
"v_subrev_u32 v26, s70, v26\n"
"v_xor_b32 v27, s79, v27\n"
"v_or_b32 v24, v24, v26\n"
"v_or_b32 v25, v25, v27\n"
"v_or_b32 v24, v24, v25\n"
"v_cmp_lt_u32 vcc, 15, v24\n"
"s_nop 4\n"
"s_cbranch_vccnz L_slow3\n"
"s_branch L_back3\n"
"L_slow4:\n"
"s_cmp_eq_u32 s68, 0\n"
"s_cbranch_scc1 L_back4\n"
"s_sub_u32 s68, s68, 1\n"
"s_sleep 2\n"
"buffer_load_dwordx2 v[8:9], off, s[56:59], %14 sc1\n"
"s_waitcnt vmcnt(0)\n"
"v_subrev_u32 v8, s65, v8\n"
"v_xor_b32 v9, s79, v9\n"
"v_or_b32 v8, v8, v9\n"
"v_cmp_lt_u32 vcc, 15, v8\n"
"s_nop 4\n"
"s_cbranch_vccnz L_slow4\n"
"s_branch L_back4\n"
"L_headF:\n"
"L_end:\n"
"s_waitcnt vmcnt(0) lgkmcnt(0)\n"
    :: LO(wsb), HI(wsb), LO(xo), HI(xo), LO(ob), HI(ob), LO(wr), HI(wr), LO(bb), HI(bb), "s"(wo), "s"(eo), "s"(ep), "s"(fo), "s"(fp),
       "s"(ldsa), "s"(w), "s"(col), "v"(threadIdx.x), "s"(db), "s"(rs1), "s"(rs2), "s"(rd1), "s"(rn1), "s"(wfo), "s"(wfa), "s"(b)
    : "memory", "scc", "vcc",
"v1","v2","v3","v4","v5","v6","v7","v8","v9","v10","v11","v12","v13","v14","v15","v16","v17","v18","v19","v20","v21","v22","v23","v24","v25","v26","v27","v28","v29","v30","v31","v32","v33","v34","v35","v36","v37","v38","v39","v40","v41","v42","v43","v44","v45","v46","v47","v48","v49","v50","v51","v52","v53","v54","v55","v56","v57","v58","v59","v60","v61","v62","v63","v64","v65","v66","v67","v68","v69","v70","v71","v72","v73","v74","v75","v76","v77","v78","v79","v80","v81","v82","v83","v84","v85","v86","v87","v88","v89","v90","v91","v92","v93","v94","v95","v96","v97","v98","v99","v100","v101","v102","v103","v104","v105","v106","v107","v108","v109","v110","v111","v112","v113","v114","v115","v116","v117","v118","v119","v120","v121","v122","v123","v124","v125","v126","v127","v128","v129","v130","v131","v132","v133","v134","v135","v136","v137","v138","v139","v140","v141","v142","v143","v144","v145","v146","v147","v148","v149","v150","v151","v152","v153","v154","v155","v156","v157","v158","v159","v160","v161","v162","v163","v164","v165","v166","v167","v168","v169","v170","v171","v172","v173","v174","v175","v176","v177","v178","v179","v180","v181","v182","v183","v184","v185","v186","v187","v188","v189","v190","v191","v192","v193","v194","v195","v196","v197","v198","v199","v200","v201","v202","v203","v204","v205","v206","v207","v208","v209","v210","v211","v212","v213","v214","v215","v216","v217","v218","v219","v220","v221","v222","v223","v224","v225","v226","v227","v228","v229","v230","v231","v232","v233","v234","v235","v236","v237","v238","v239","v240","v241","v242","v243","v244","v245","v246","v247","v248","v249","v250","v251","v252","v253","v254","v255",
"a0","a1","a2","a3","a4","a5","a6","a7","a8","a9","a10","a11","a12","a13","a14","a15","a16","a17","a18","a19","a20","a21","a22","a23","a24","a25","a26","a27","a28","a29","a30","a31","a32","a33","a34","a35","a36","a37","a38","a39","a40","a41","a42","a43","a44","a45","a46","a47","a48","a49","a50","a51","a52","a53","a54","a55","a56","a57","a58","a59","a60","a61","a62","a63","a64","a65","a66","a67","a68","a69","a70","a71","a72","a73","a74","a75","a76","a77","a78","a79","a80","a81","a82","a83","a84","a85","a86","a87","a88","a89","a90","a91","a92","a93","a94","a95","a96","a97","a98","a99","a100","a101","a102","a103","a104","a105","a106","a107","a108","a109","a110","a111","a112","a113","a114","a115","a116","a117","a118","a119","a120","a121","a122","a123","a124","a125","a126","a127","a128","a129","a130","a131","a132","a133","a134","a135","a136","a137","a138","a139","a140","a141","a142","a143","a144","a145","a146","a147","a148","a149","a150","a151","a152","a153","a154","a155","a156","a157","a158","a159","a160","a161","a162","a163","a164","a165","a166","a167","a168","a169","a170","a171","a172","a173","a174","a175","a176","a177","a178","a179","a180","a181","a182","a183","a184","a185","a186","a187","a188","a189","a190","a191","a192","a193","a194","a195","a196","a197","a198","a199","a200","a201","a202","a203","a204","a205","a206","a207","a208","a209","a210","a211","a212","a213","a214","a215","a216","a217","a218","a219","a220","a221","a222","a223","a224","a225","a226","a227","a228","a229","a230","a231","a232","a233","a234","a235","a236","a237","a238","a239","a240","a241","a242","a243","a244","a245","a246","a247","a248","a249","a250","a251","a252","a253","a254","a255",
"s29","s30","s31","s33","s34","s35","s36","s37","s38","s39","s40","s41","s42","s43","s44","s45","s46","s47","s48","s49","s50","s51","s52","s53","s54","s55","s56","s57","s58","s59","s60","s61","s62","s63","s64","s65","s66","s67","s68","s69","s70","s71","s72","s73","s74","s75","s76","s77","s78","s79","s80","s81","s82","s83","s84","s85","s86","s87","s88","s89","s90","s91","s92","s93","s94","s95","s96","s97","s98","s99");
}

extern "C" void kernel_launch(void* const* d_in, const int* in_sizes, int n_in,
                              void* d_out, int out_size, void* d_ws, size_t ws_size,
                              hipStream_t stream) {
  const unsigned short* x0 = (const unsigned short*)d_in[0];
  const unsigned short* ws = (const unsigned short*)d_in[1];
  const unsigned short* bs = (const unsigned short*)d_in[2];
  dcn_cross_kernel<<<256, 256, 0, stream>>>(x0, ws, bs, (unsigned char*)d_ws, (unsigned short*)d_out);
}
```

```cpp
#include <hip/hip_runtime.h>


constexpr int kD = 1024;
constexpr unsigned kWFlagOff = 131072, kWOff = 1u << 20, kWStream = 200 * 8192, kXchOff = 32u << 20, kDbgOff = 224u << 20;

__device__ __forceinline__ unsigned unit_src(unsigned v, unsigned h, unsigned w) {
  const unsigned l = v >> 5, u = v & 31;
  const unsigned split = ((0xff0000ffu >> u) & 1) - 1;
  const unsigned part = l ? u >> 4 : 0u, g = (0xff00ff00u >> u) & 1;
  const unsigned c = u >> (split & 1), jh = (u >> (~split & 2)) & 1;
  const unsigned kc = 8 * (h ^ part) + 2 * ((w + c) & 3) + g;
  return (l * 1024 + 64 * kc) * 2048 + jh * 128;
}

__global__ __launch_bounds__(256, 1) __attribute__((aligned(4096)))
void dcn_cross_kernel(const unsigned short* __restrict__ x0, const unsigned short* __restrict__ ws, const unsigned short* __restrict__ bs,
                      unsigned char* __restrict__ wsp, unsigned short* __restrict__ out) {
  __shared__ __attribute__((aligned(16))) unsigned char lds[163840];
  const unsigned b = blockIdx.x;
  constexpr unsigned kPx = 1 ? 1u : 8u;
  const unsigned h = 1 ? (b & 1) : ((b >> 3) & 1), rb = 1 ? (b >> 1) : ((b >> 4) * 8 + (b & 7));
  const unsigned w = __builtin_amdgcn_readfirstlane(threadIdx.x >> 6);
  unsigned col = (512 * h + 128 * w) * 2, rows = rb * 128 * kD * 2, oofs = rows + col;
  unsigned wo = (h * 4 + w) * kWStream, eo = kXchOff + b * 524288 + w * 32768, ep = kXchOff + (b ^ kPx) * 524288 + w * 32768;
  unsigned fo = b * 512 + w * 128, fp = (b ^ kPx) * 512 + w * 128;
  unsigned db = kDbgOff + (b * 4 + w) * 512;
  const unsigned v1 = 24 + (rb >> 1), v2 = 64 + (rb >> 1), hk = rb & 1, on = v1 < 64 ? ~0u : 0u;
  unsigned rs1 = ((unit_src(v1, h, w) + hk * 65536) & on) | (((1 - h) << 20) & ~on), rd1 = ((v1 * 8192 + hk * 1024) & on) | ((192 * 8192) & ~on);
  unsigned rs2 = unit_src(v2, h, w) + hk * 65536, rn1 = v2 * 8192 + hk * 1024;
  unsigned wfa = kWFlagOff + (4 * h + w) * 1024, wfo = wfa + rb * 8;
  asm volatile("" : "+s"(col), "+s"(rows), "+s"(oofs), "+s"(wo), "+s"(eo), "+s"(ep), "+s"(fo), "+s"(fp), "+s"(db), "+s"(rs1), "+s"(rd1), "+s"(rs2), "+s"(rn1), "+s"(wfa), "+s"(wfo));
  __builtin_amdgcn_sched_barrier(0);
  const unsigned long long wsb = (unsigned long long)wsp, bb = (unsigned long long)bs;
  const unsigned long long xo = (unsigned long long)x0 + rows, ob = (unsigned long long)out + oofs, wr = (unsigned long long)ws + col;
  wo += (unsigned)wsb > ~(15u << 20) ? 16u << 20 : kWOff;
  const unsigned xa = (0u - (unsigned)wsb) & 0x1fffffu;
  eo += xa; ep += xa;
  const unsigned ldsa = (unsigned)(size_t)(__attribute__((address_space(3))) unsigned char*)lds;
#define LO(x) "s"((unsigned)(x))
#define HI(x) "s"((unsigned)((x) >> 32))
  asm volatile(
"L_main_entry:\n"
"v_and_b32 v12, 63, %18\n"
"v_lshrrev_b32 v13, 3, v12\n"
"v_and_b32 v15, 7, v12\n"
"v_lshlrev_b32 v10, 11, v13\n"
"s_andn2_b32 s60, 1024, %17\n"
"s_lshl_b32 s75, s60, 10\n"
"s_mov_b32 s52, %6\n"
"s_and_b32 s53, %7, 0xffff\n"
"s_mov_b32 s54, 12581120\n"
"s_mov_b32 s55, 0x20000\n"
"s_getpc_b64 s[96:97]\n"
"L_pc:\n"
"s_lshr_b32 s66, %26, 3\n"
"s_lshl_b32 s66, s66, 2\n"
"s_add_u32 s66, s66, %16\n"
"s_mul_i32 s66, s66, 640\n"
"s_and_b32 s97, s97, 0xffff\n"
"s_mov_b32 s98, L_end-L_pc\n"
"s_mov_b32 s99, 0x20000\n"
"s_min_u32 s66, s66, L_end-L_pc-640\n"
"v_min_u32 v9, 9, v12\n"
"v_lshlrev_b32 v9, 6, v9\n"
"buffer_load_dword a252, v9, s[96:99], s66 offen\n"
"v_xor_b32 v6, v15, v13\n"
"v_lshl_add_u32 v6, v6, 4, v10\n"
"s_mov_b32 s48, %2\n"
"s_and_b32 s49, %3, 0xffff\n"
"s_mov_b32 s50, 65536\n"
"s_mov_b32 s51, 0x20000\n"
"s_lshl_b32 s66, %16, 16\n"
"s_add_u32 s48, s48, s66\n"
"s_addc_u32 s49, s49, 0\n"
"s_lshl_b32 s72, %16, 12\n"
"s_add_u32 s72, s72, %15\n"
"s_mul_i32 s73, %16, 40960\n"
"s_add_u32 s64, s73, %15\n"
"v_lshrrev_b32 v11, 2, v12\n"
"v_and_b32 v11, 4, v11\n"
"v_xor_b32 v11, v11, v15\n"
"v_lshl_add_u32 v11, v11, 4, v10\n"
"s_bitcmp1_b32 %26, 0\n"
"s_cbranch_scc1 L_y\n"
"s_mov_b64 s[96:97], s[52:53]\n"
"s_cmp_lt_u32 %22, 1572864\n"
"s_cselect_b32 s98, s54, 0\n"
"s_mov_b32 s99, 0x20000\n"
"s_xor_b32 s71, %20, 0x100000\n"
"s_add_u32 m0, s72, 139264\n"
"s_add_u32 s67, %20, 0\n"
"buffer_load_dwordx4 v11, s[96:99], s67 offen offset:0  lds\n"
"s_add_u32 s67, %20, 15360\n"
"buffer_load_dwordx4 v11, s[96:99], s67 offen offset:1024  lds\n"
"s_add_u32 s67, %20, 30720\n"
"buffer_load_dwordx4 v11, s[96:99], s67 offen offset:2048  lds\n"
"s_add_u32 s67, %20, 46080\n"
"buffer_load_dwordx4 v11, s[96:99], s67 offen offset:3072  lds\n"
"s_add_u32 m0, s72, 98304\n"
"s_add_u32 s67, s71, 1024\n"
"buffer_load_dwordx4 v11, s[96:99], s67 offen offset:0  lds\n"
"s_add_u32 s67, s71, 16384\n"
"buffer_load_dwordx4 v11, s[96:99], s67 offen offset:1024  lds\n"
"s_add_u32 s67, s71, 31744\n"
"buffer_load_dwordx4 v11, s[96:99], s67 offen offset:2048  lds\n"
"s_add_u32 s67, s71, 47104\n"
"buffer_load_dwordx4 v11, s[96:99], s67 offen offset:3072  lds\n"
"L_y:\n"
"s_add_u32 m0, s72, 16384\n"
"s_add_u32 s67, s60, 0\n"
"buffer_load_dwordx4 v6, s[48:51], s67 offen offset:0  lds\n"
"s_add_u32 s67, s60, 15360\n"
"buffer_load_dwordx4 v6, s[48:51], s67 offen offset:1024  lds\n"
"s_add_u32 s67, s60, 30720\n"
"buffer_load_dwordx4 v6, s[48:51], s67 offen offset:2048  lds\n"
"s_add_u32 s67, s60, 46080\n"
"buffer_load_dwordx4 v6, s[48:51], s67 offen offset:3072  lds\n"
"s_add_u32 m0, s64, 0\n"
"s_add_u32 s67, s75, 0\n"
"buffer_load_dwordx4 v11, s[52:55], s67 offen offset:0  lds\n"
"s_add_u32 s67, s75, 15360\n"
"buffer_load_dwordx4 v11, s[52:55], s67 offen offset:1024  lds\n"
"s_add_u32 s67, s75, 30720\n"
"buffer_load_dwordx4 v11, s[52:55], s67 offen offset:2048  lds\n"
"s_add_u32 s67, s75, 46080\n"
"buffer_load_dwordx4 v11, s[52:55], s67 offen offset:3072  lds\n"
"s_add_u32 m0, s64, 4096\n"
"s_add_u32 s67, s75, 65536\n"
"buffer_load_dwordx4 v11, s[52:55], s67 offen offset:0  lds\n"
"s_add_u32 s67, s75, 80896\n"
"buffer_load_dwordx4 v11, s[52:55], s67 offen offset:1024  lds\n"
"s_add_u32 s67, s75, 96256\n"
"buffer_load_dwordx4 v11, s[52:55], s67 offen offset:2048  lds\n"
"s_add_u32 s67, s75, 111616\n"
"buffer_load_dwordx4 v11, s[52:55], s67 offen offset:3072  lds\n"
"s_lshr_b32 s65, %26, 4\n"
"s_lshl_b32 s65, s65, 2\n"
"s_add_u32 s65, s65, %16\n"
"s_lshl_b32 s65, s65, 11\n"
"s_lshl_b32 s67, %16, 8\n"
"s_sub_u32 s65, s65, s67\n"
"v_and_b32 v14, 1, v13\n"
"v_lshlrev_b32 v14, 17, v14\n"
"v_lshl_add_u32 v14, v15, 7, v14\n"
"v_and_b32 v14, 0xfffffdff, v14\n"
"s_lshl_b32 s67, %26, 6\n"
"s_and_b32 s67, s67, 512\n"
"s_add_u32 s65, s65, s67\n"
"v_lshlrev_b32 v0, 4, v12\n"
"s_mov_b32 s56, %0\n"
"s_and_b32 s57, %1, 0xffff\n"
"s_mov_b32 s58, 171966464\n"
"s_mov_b32 s59, 0x20000\n"
"v_add_u32 v1, 4096, v0\n"
"s_add_u32 s36, s56, %10\n"
"s_addc_u32 s37, s57, 0\n"
"s_mov_b32 s38, 1572864\n"
"s_mov_b32 s39, 0x20000\n"
"s_add_u32 s34, s36, 200704\n"
"s_mov_b32 s35, s37\n"
"v_and_b32 v13, 15, v12\n"
"v_lshrrev_b32 v10, 4, v12\n"
"v_xor_b32 v15, v15, v10\n"
"v_lshlrev_b32 v7, 7, v13\n"
"v_lshl_add_u32 v7, v15, 4, v7\n"
"v_add_u32 v7, s64, v7\n"
"v_xor_b32 v8, 64, v7\n"
"v_bfe_u32 v13, v12, 2, 2\n"
"v_and_b32 v15, 3, v12\n"
"v_bfe_u32 v9, v12, 3, 1\n"
"v_lshl_add_u32 v15, v9, 2, v15\n"
"v_lshl_add_u32 v13, v10, 3, v13\n"
"v_lshlrev_b32 v13, 7, v13\n"
"v_lshl_add_u32 v9, v15, 4, v13\n"
"v_add_u32 v9, s64, v9\n"
"v_xor_b32 v15, 64, v9\n"
"s_mov_b32 s40, %2\n"
"s_and_b32 s41, %3, 0xffff\n"
"s_mov_b32 s42, 260352\n"
"s_mov_b32 s43, 0x20000\n"
"s_add_u32 s40, s40, %17\n"
"s_addc_u32 s41, s41, 0\n"
"s_mov_b32 s80, 0\n"
"s_mov_b32 s81, 15360\n"
"s_mov_b32 s82, 30720\n"
"s_mov_b32 s83, 46080\n"
"s_mov_b32 s84, 65536\n"
"s_mov_b32 s85, 80896\n"
"s_mov_b32 s86, 96256\n"
"s_mov_b32 s87, 111616\n"
"s_mov_b32 s88, 131072\n"
"s_mov_b32 s89, 146432\n"
"s_mov_b32 s90, 161792\n"
"s_mov_b32 s91, 177152\n"
"s_mov_b32 s92, 196608\n"
"s_mov_b32 s93, 211968\n"
"s_mov_b32 s94, 227328\n"
"s_mov_b32 s95, 242688\n"
"s_mov_b32 s62, 0\n"
"s_mov_b32 s63, 0xffff0000\n"
"s_mov_b32 s68, 0x8000\n"
"s_add_u32 s66, %16, 0\n"
"s_and_b32 s66, s66, 3\n"
"s_mul_i32 s66, s66, 40960\n"
"s_add_u32 s66, s66, %15\n"
"v_add_u32 v2, s66, v0\n"
"s_add_u32 s66, %16, 1\n"
"s_and_b32 s66, s66, 3\n"
"s_mul_i32 s66, s66, 40960\n"
"s_add_u32 s66, s66, %15\n"
"v_add_u32 v3, s66, v0\n"
"s_add_u32 s66, %16, 2\n"
"s_and_b32 s66, s66, 3\n"
"s_mul_i32 s66, s66, 40960\n"
"s_add_u32 s66, s66, %15\n"
"v_add_u32 v4, s66, v0\n"
"s_add_u32 s66, %16, 3\n"
"s_and_b32 s66, s66, 3\n"
"s_mul_i32 s66, s66, 40960\n"
"s_add_u32 s66, s66, %15\n"
"v_add_u32 v5, s66, v0\n"
"s_sub_u32 s66, 0, s73\n"
"v_add_u32 v12, s66, v7\n"
"v_add_u32 v13, s66, v8\n"
"s_mov_b64 exec, 0\n"
"s_cmp_eq_u32 %16, 0\n"
"s_cbranch_scc1 L_st535\n"
"s_cmp_eq_u32 %16, 1\n"
"s_cbranch_scc1 L_st1064\n"
"s_cmp_eq_u32 %16, 2\n"
"s_cbranch_scc1 L_st1593\n"
"s_branch L_st2137\n"
"L_retA:\n"
"s_mov_b64 exec, -1\n"
"s_nop 3\n"
"s_mov_b32 s44, %8\n"
"s_and_b32 s45, %9, 0xffff\n"
"s_mov_b32 s46, 12288\n"
"s_mov_b32 s47, 0x20000\n"
"s_mov_b32 s61, %17\n"
"v_lshrrev_b32 v16, 4, v0\n"
"v_and_b32 v16, 0x30, v16\n"
"buffer_load_dwordx2 v[18:19], v16, s[44:47], s61 offen offset:0\n"
"v_lshrrev_b32 v20, 4, v0\n"
"v_and_b32 v20, 0x30, v20\n"
"buffer_load_dwordx2 v[22:23], v20, s[44:47], s61 offen offset:8\n"
"v_lshrrev_b32 v24, 4, v0\n"
"v_and_b32 v24, 0x30, v24\n"
"buffer_load_dwordx2 v[26:27], v24, s[44:47], s61 offen offset:64\n"
"v_lshrrev_b32 v28, 4, v0\n"
"v_and_b32 v28, 0x30, v28\n"
"buffer_load_dwordx2 v[30:31], v28, s[44:47], s61 offen offset:72\n"
"s_add_u32 s70, s75, 131072\n"
"s_add_u32 s70, s70, s65\n"
"buffer_load_dword a252, v14, s[52:55], s70 offen\n"
"s_mov_b32 s67, 139264\n"
"buffer_load_dwordx2 v[252:253], off, s[56:59], s67 sc1\n"
"s_waitcnt vmcnt(14)\n"
"s_barrier\n"
"ds_read_b128 a[100:103], v13 offset:16384\n"
"ds_read_b128 a[96:99], v12 offset:16384\n"
"ds_read_b128 a[108:111], v13 offset:18432\n"
"ds_read_b128 a[104:107], v12 offset:18432\n"
"ds_read_b128 a[116:119], v13 offset:20480\n"
"ds_read_b128 a[112:115], v12 offset:20480\n"
"ds_read_b128 a[124:127], v13 offset:22528\n"
"ds_read_b128 a[120:123], v12 offset:22528\n"
"ds_read_b128 a[132:135], v13 offset:24576\n"
"ds_read_b128 a[128:131], v12 offset:24576\n"
"ds_read_b128 a[140:143], v13 offset:26624\n"
"ds_read_b128 a[136:139], v12 offset:26624\n"
"ds_read_b128 a[148:151], v13 offset:28672\n"
"ds_read_b128 a[144:147], v12 offset:28672\n"
"ds_read_b128 a[156:159], v13 offset:30720\n"
"ds_read_b128 a[152:155], v12 offset:30720\n"
"s_add_u32 m0, s64, 8192\n"
"s_add_u32 s67, s75, 128\n"
"buffer_load_dwordx4 v11, s[52:55], s67 offen offset:0  lds\n"
"s_add_u32 s67, s75, 15488\n"
"buffer_load_dwordx4 v11, s[52:55], s67 offen offset:1024  lds\n"
"s_add_u32 s67, s75, 30848\n"
"buffer_load_dwordx4 v11, s[52:55], s67 offen offset:2048  lds\n"
"s_add_u32 s67, s75, 46208\n"
"buffer_load_dwordx4 v11, s[52:55], s67 offen offset:3072  lds\n"
"s_add_u32 m0, s64, 12288\n"
"s_add_u32 s67, s75, 65664\n"
"buffer_load_dwordx4 v11, s[52:55], s67 offen offset:0  lds\n"
"s_add_u32 s67, s75, 81024\n"
"buffer_load_dwordx4 v11, s[52:55], s67 offen offset:1024  lds\n"
"s_add_u32 s67, s75, 96384\n"
"buffer_load_dwordx4 v11, s[52:55], s67 offen offset:2048  lds\n"
"s_add_u32 s67, s75, 111744\n"
"buffer_load_dwordx4 v11, s[52:55], s67 offen offset:3072  lds\n"
"s_waitcnt vmcnt(14)\n"
"ds_read_b64_tr_b16 a[32:33], v9 offset:0\n"
"ds_read_b64_tr_b16 a[34:35], v9 offset:512\n"
"ds_read_b64_tr_b16 a[36:37], v9 offset:4096\n"
"ds_read_b64_tr_b16 a[38:39], v9 offset:4608\n"
"ds_read_b64_tr_b16 a[40:41], v9 offset:8\n"
"ds_read_b64_tr_b16 a[42:43], v9 offset:520\n"
"ds_read_b64_tr_b16 a[44:45], v9 offset:4104\n"
"ds_read_b64_tr_b16 a[46:47], v9 offset:4616\n"
"ds_read_b64_tr_b16 a[48:49], v15 offset:0\n"
"ds_read_b64_tr_b16 a[50:51], v15 offset:512\n"
"ds_read_b64_tr_b16 a[52:53], v15 offset:4096\n"
"ds_read_b64_tr_b16 a[54:55], v15 offset:4608\n"
"ds_read_b64_tr_b16 a[56:57], v15 offset:8\n"
"ds_read_b64_tr_b16 a[58:59], v15 offset:520\n"
"ds_read_b64_tr_b16 a[60:61], v15 offset:4104\n"
"ds_read_b64_tr_b16 a[62:63], v15 offset:4616\n"
"s_waitcnt vmcnt(10)\n"
"v_lshlrev_b32 v16, 16, v18\n"
"v_and_b32 v17, s63, v18\n"
"v_lshlrev_b32 v18, 16, v19\n"
"v_and_b32 v19, s63, v19\n"
"v_add_f32 v16, 1.0, v16\n"
"v_add_f32 v17, 1.0, v17\n"
"v_add_f32 v18, 1.0, v18\n"
"v_add_f32 v19, 1.0, v19\n"
"v_lshlrev_b32 v20, 16, v22\n"
"v_and_b32 v21, s63, v22\n"
"v_lshlrev_b32 v22, 16, v23\n"
"v_and_b32 v23, s63, v23\n"
"v_add_f32 v20, 1.0, v20\n"
"v_add_f32 v21, 1.0, v21\n"
"v_add_f32 v22, 1.0, v22\n"
"v_add_f32 v23, 1.0, v23\n"
"v_lshlrev_b32 v24, 16, v26\n"
"v_and_b32 v25, s63, v26\n"
"v_lshlrev_b32 v26, 16, v27\n"
"v_and_b32 v27, s63, v27\n"
"v_add_f32 v24, 1.0, v24\n"
"v_add_f32 v25, 1.0, v25\n"
"v_add_f32 v26, 1.0, v26\n"
"v_add_f32 v27, 1.0, v27\n"
"v_lshlrev_b32 v28, 16, v30\n"
"v_and_b32 v29, s63, v30\n"
"v_lshlrev_b32 v30, 16, v31\n"
"v_and_b32 v31, s63, v31\n"
"v_add_f32 v28, 1.0, v28\n"
"v_add_f32 v29, 1.0, v29\n"
"v_add_f32 v30, 1.0, v30\n"
"v_add_f32 v31, 1.0, v31\n"
"v_accvgpr_write_b32 a0, v16\n"
"v_accvgpr_write_b32 a1, v17\n"
"v_accvgpr_write_b32 a2, v18\n"
"v_accvgpr_write_b32 a3, v19\n"
"v_accvgpr_write_b32 a4, v20\n"
"v_accvgpr_write_b32 a5, v21\n"
"v_accvgpr_write_b32 a6, v22\n"
"v_accvgpr_write_b32 a7, v23\n"
"v_accvgpr_write_b32 a8, v24\n"
"v_accvgpr_write_b32 a9, v25\n"
"v_accvgpr_write_b32 a10, v26\n"
"v_accvgpr_write_b32 a11, v27\n"
"v_accvgpr_write_b32 a12, v28\n"
"v_accvgpr_write_b32 a13, v29\n"
"v_accvgpr_write_b32 a14, v30\n"
"v_accvgpr_write_b32 a15, v31\n"
"s_add_u32 m0, s72, 57344\n"
"s_add_u32 s67, s60, 128\n"
"buffer_load_dwordx4 v6, s[48:51], s67 offen offset:0  lds\n"
"s_add_u32 s67, s60, 15488\n"
"buffer_load_dwordx4 v6, s[48:51], s67 offen offset:1024  lds\n"
"s_add_u32 s67, s60, 30848\n"
"buffer_load_dwordx4 v6, s[48:51], s67 offen offset:2048  lds\n"
"s_add_u32 s67, s60, 46208\n"
"buffer_load_dwordx4 v6, s[48:51], s67 offen offset:3072  lds\n"
"s_bitcmp1_b32 %26, 0\n"
"s_cbranch_scc1 L_z\n"
"s_cmp_lt_u32 %22, 1572864\n"
"s_cselect_b32 s38, 8192000, 0\n"
"s_add_u32 s70, %22, 6553600\n"
"s_sub_u32 s67, s72, s64\n"
"s_add_u32 s67, s67, 98304\n"
"v_add_u32 v40, s67, v9\n"
"v_add_u32 v41, s67, v15\n"
"s_sleep 1\n"
"ds_read_b64_tr_b16 a[160:161], v40 offset:40960\n"
"ds_read_b64_tr_b16 a[162:163], v40 offset:41472\n"
"ds_read_b64_tr_b16 a[164:165], v40 offset:40968\n"
"ds_read_b64_tr_b16 a[166:167], v40 offset:41480\n"
"ds_read_b64_tr_b16 a[168:169], v41 offset:40960\n"
"ds_read_b64_tr_b16 a[170:171], v41 offset:41472\n"
"ds_read_b64_tr_b16 a[172:173], v41 offset:40968\n"
"ds_read_b64_tr_b16 a[174:175], v41 offset:41480\n"
"ds_read_b64_tr_b16 a[176:177], v40 offset:0\n"
"ds_read_b64_tr_b16 a[178:179], v40 offset:512\n"
"ds_read_b64_tr_b16 a[180:181], v40 offset:8\n"
"ds_read_b64_tr_b16 a[182:183], v40 offset:520\n"
"ds_read_b64_tr_b16 a[184:185], v41 offset:0\n"
"ds_read_b64_tr_b16 a[186:187], v41 offset:512\n"
"ds_read_b64_tr_b16 a[188:189], v41 offset:8\n"
"ds_read_b64_tr_b16 a[190:191], v41 offset:520\n"
"s_waitcnt lgkmcnt(14)\n"
"buffer_store_dwordx4 a[160:163], v0, s[36:39], %22 offen offset:0 sc1\n"
"s_waitcnt lgkmcnt(12)\n"
"buffer_store_dwordx4 a[164:167], v0, s[36:39], %22 offen offset:2048 sc1\n"
"s_waitcnt lgkmcnt(10)\n"
"buffer_store_dwordx4 a[168:171], v1, s[36:39], %22 offen offset:0 sc1\n"
"s_waitcnt lgkmcnt(8)\n"
"buffer_store_dwordx4 a[172:175], v1, s[36:39], %22 offen offset:2048 sc1\n"
"s_waitcnt lgkmcnt(6)\n"
"buffer_store_dwordx4 a[176:179], v0, s[36:39], s70 offen offset:0 sc1\n"
"s_waitcnt lgkmcnt(4)\n"
"buffer_store_dwordx4 a[180:183], v0, s[36:39], s70 offen offset:2048 sc1\n"
"s_waitcnt lgkmcnt(2)\n"
"buffer_store_dwordx4 a[184:187], v1, s[36:39], s70 offen offset:0 sc1\n"
"s_waitcnt lgkmcnt(0)\n"
"buffer_store_dwordx4 a[188:191], v1, s[36:39], s70 offen offset:2048 sc1\n"
"s_sleep 12\n"
"L_z:\n"
"s_nop 1\n"
"s_waitcnt lgkmcnt(8)\n"
"v_mfma_f32_16x16x32_bf16 v[32:35], a[32:35], a[96:99], v[16:19]\n"
"s_add_u32 s70, s75, 393216\n"
"v_mfma_f32_16x16x32_bf16 v[32:35], a[36:39], a[100:103], v[32:35]\n"
"s_add_u32 s70, s70, s65\n"
"v_mfma_f32_16x16x32_bf16 v[60:63], a[44:47], a[100:103], v[20:23]\n"
"buffer_load_dword a252, v14, s[52:55], s70 offen\n"
"v_mfma_f32_16x16x32_bf16 v[60:63], a[40:43], a[96:99], v[60:63]\n"
"s_add_u32 m0, s64, 32768\n"
"v_mfma_f32_16x16x32_bf16 v[64:67], a[40:43], a[104:107], v[20:23]\n"
"s_add_u32 s67, s75, 131072\n"
"v_mfma_f32_16x16x32_bf16 v[64:67], a[44:47], a[108:111], v[64:67]\n"
"buffer_load_dwordx4 v11, s[52:55], s67 offen offset:0  lds\n"
"v_mfma_f32_16x16x32_bf16 v[36:39], a[36:39], a[108:111], v[16:19]\n"
"s_add_u32 s67, s75, 146432\n"
"v_mfma_f32_16x16x32_bf16 v[36:39], a[32:35], a[104:107], v[36:39]\n"
"v_mfma_f32_16x16x32_bf16 v[40:43], a[32:35], a[112:115], v[16:19]\n"
"s_waitcnt lgkmcnt(0)\n"
"buffer_load_dwordx4 v11, s[52:55], s67 offen offset:1024  lds\n"
"v_mfma_f32_16x16x32_bf16 v[40:43], a[36:39], a[116:119], v[40:43]\n"
"v_mfma_f32_16x16x32_bf16 v[68:71], a[44:47], a[116:119], v[20:23]\n"
"s_add_u32 s67, s75, 161792\n"
"v_mfma_f32_16x16x32_bf16 v[68:71], a[40:43], a[112:115], v[68:71]\n"
"v_mfma_f32_16x16x32_bf16 v[72:75], a[40:43], a[120:123], v[20:23]\n"
"buffer_load_dwordx4 v11, s[52:55], s67 offen offset:2048  lds\n"
"v_mfma_f32_16x16x32_bf16 v[72:75], a[44:47], a[124:127], v[72:75]\n"
"v_mfma_f32_16x16x32_bf16 v[44:47], a[36:39], a[124:127], v[16:19]\n"
"s_add_u32 s67, s75, 177152\n"
"v_mfma_f32_16x16x32_bf16 v[44:47], a[32:35], a[120:123], v[44:47]\n"
"v_mfma_f32_16x16x32_bf16 v[48:51], a[32:35], a[128:131], v[16:19]\n"
"buffer_load_dwordx4 v11, s[52:55], s67 offen offset:3072  lds\n"
"v_mfma_f32_16x16x32_bf16 v[48:51], a[36:39], a[132:135], v[48:51]\n"
"v_mfma_f32_16x16x32_bf16 v[76:79], a[44:47], a[132:135], v[20:23]\n"
"s_add_u32 m0, s64, 36864\n"
"v_mfma_f32_16x16x32_bf16 v[76:79], a[40:43], a[128:131], v[76:79]\n"
"v_mfma_f32_16x16x32_bf16 v[80:83], a[40:43], a[136:139], v[20:23]\n"
"s_add_u32 s67, s75, 196608\n"
"v_mfma_f32_16x16x32_bf16 v[80:83], a[44:47], a[140:143], v[80:83]\n"
"v_mfma_f32_16x16x32_bf16 v[52:55], a[36:39], a[140:143], v[16:19]\n"
"buffer_load_dwordx4 v11, s[52:55], s67 offen offset:0  lds\n"
"v_mfma_f32_16x16x32_bf16 v[52:55], a[32:35], a[136:139], v[52:55]\n"
"v_mfma_f32_16x16x32_bf16 v[56:59], a[32:35], a[144:147], v[16:19]\n"
"s_mov_b64 exec, 0\n"
"s_cmp_eq_u32 %16, 0\n"
"s_cbranch_scc1 L_st5093\n"
"s_cmp_eq_u32 %16, 1\n"
"s_cbranch_scc1 L_st5700\n"
"s_cmp_eq_u32 %16, 2\n"
"s_cbranch_scc1 L_st6249\n"
"s_branch L_st6802\n"
"L_retB:\n"
"s_mov_b64 exec, -1\n"
"s_nop 3\n"
"s_add_u32 s67, s75, 211968\n"
"v_mfma_f32_16x16x32_bf16 v[56:59], a[36:39], a[148:151], v[56:59]\n"
"v_mfma_f32_16x16x32_bf16 v[84:87], a[44:47], a[148:151], v[20:23]\n"
"buffer_load_dwordx4 v11, s[52:55], s67 offen offset:1024  lds\n"
"v_mfma_f32_16x16x32_bf16 v[84:87], a[40:43], a[144:147], v[84:87]\n"
"v_mfma_f32_16x16x32_bf16 a[4:7], a[40:43], a[152:155], a[4:7]\n"
"s_add_u32 s67, s75, 227328\n"
"v_mfma_f32_16x16x32_bf16 a[4:7], a[44:47], a[156:159], a[4:7]\n"
"v_mfma_f32_16x16x32_bf16 a[0:3], a[36:39], a[156:159], a[0:3]\n"
"buffer_load_dwordx4 v11, s[52:55], s67 offen offset:2048  lds\n"
"v_mfma_f32_16x16x32_bf16 a[0:3], a[32:35], a[152:155], a[0:3]\n"
"v_mfma_f32_16x16x32_bf16 v[88:91], a[48:51], a[96:99], v[24:27]\n"
"s_add_u32 s67, s75, 242688\n"
"v_mfma_f32_16x16x32_bf16 v[88:91], a[52:55], a[100:103], v[88:91]\n"
"v_mfma_f32_16x16x32_bf16 v[116:119], a[60:63], a[100:103], v[28:31]\n"
"v_mfma_f32_16x16x32_bf16 v[116:119], a[56:59], a[96:99], v[116:119]\n"
"buffer_load_dwordx4 v11, s[52:55], s67 offen offset:3072  lds\n"
"v_mfma_f32_16x16x32_bf16 v[120:123], a[56:59], a[104:107], v[28:31]\n"
"s_add_u32 m0, s64, 0\n"
"v_mfma_f32_16x16x32_bf16 v[120:123], a[60:63], a[108:111], v[120:123]\n"
"v_mfma_f32_16x16x32_bf16 v[92:95], a[52:55], a[108:111], v[24:27]\n"
"s_add_u32 s67, s75, 131200\n"
"v_mfma_f32_16x16x32_bf16 v[92:95], a[48:51], a[104:107], v[92:95]\n"
"v_mfma_f32_16x16x32_bf16 v[96:99], a[48:51], a[112:115], v[24:27]\n"
"buffer_load_dwordx4 v11, s[52:55], s67 offen offset:0  lds\n"
"v_mfma_f32_16x16x32_bf16 v[96:99], a[52:55], a[116:119], v[96:99]\n"
"v_mfma_f32_16x16x32_bf16 v[124:127], a[60:63], a[116:119], v[28:31]\n"
"s_add_u32 s67, s75, 146560\n"
"v_mfma_f32_16x16x32_bf16 v[124:127], a[56:59], a[112:115], v[124:127]\n"
"buffer_load_dwordx4 v11, s[52:55], s67 offen offset:1024  lds\n"
"v_mfma_f32_16x16x32_bf16 v[128:131], a[56:59], a[120:123], v[28:31]\n"
"s_waitcnt vmcnt(15)\n"
"ds_read_b64_tr_b16 a[64:65], v9 offset:8192\n"
"v_mfma_f32_16x16x32_bf16 v[128:131], a[60:63], a[124:127], v[128:131]\n"
"s_add_u32 s67, s75, 161920\n"
"ds_read_b64_tr_b16 a[66:67], v9 offset:8704\n"
"v_mfma_f32_16x16x32_bf16 v[100:103], a[52:55], a[124:127], v[24:27]\n"
"ds_read_b64_tr_b16 a[68:69], v9 offset:12288\n"
"v_mfma_f32_16x16x32_bf16 v[100:103], a[48:51], a[120:123], v[100:103]\n"
"ds_read_b64_tr_b16 a[70:71], v9 offset:12800\n"
"v_mfma_f32_16x16x32_bf16 v[104:107], a[48:51], a[128:131], v[24:27]\n"
"ds_read_b64_tr_b16 a[72:73], v9 offset:8200\n"
"v_mfma_f32_16x16x32_bf16 v[104:107], a[52:55], a[132:135], v[104:107]\n"
"ds_read_b64_tr_b16 a[74:75], v9 offset:8712\n"
"buffer_load_dwordx4 v11, s[52:55], s67 offen offset:2048  lds\n"
"v_mfma_f32_16x16x32_bf16 v[132:135], a[60:63], a[132:135], v[28:31]\n"
"ds_read_b64_tr_b16 a[76:77], v9 offset:12296\n"
"v_mfma_f32_16x16x32_bf16 v[132:135], a[56:59], a[128:131], v[132:135]\n"
"s_add_u32 s67, s75, 177280\n"
"ds_read_b64_tr_b16 a[78:79], v9 offset:12808\n"
"v_mfma_f32_16x16x32_bf16 v[136:139], a[56:59], a[136:139], v[28:31]\n"
"buffer_load_dwordx4 v11, s[52:55], s67 offen offset:3072  lds\n"
"v_mfma_f32_16x16x32_bf16 v[136:139], a[60:63], a[140:143], v[136:139]\n"
"ds_read_b64_tr_b16 a[80:81], v15 offset:8192\n"
"v_mfma_f32_16x16x32_bf16 v[108:111], a[52:55], a[140:143], v[24:27]\n"
"s_add_u32 m0, s64, 4096\n"
"ds_read_b64_tr_b16 a[82:83], v15 offset:8704\n"
"v_mfma_f32_16x16x32_bf16 v[108:111], a[48:51], a[136:139], v[108:111]\n"
"ds_read_b64_tr_b16 a[84:85], v15 offset:12288\n"
"v_mfma_f32_16x16x32_bf16 v[112:115], a[48:51], a[144:147], v[24:27]\n"
"s_add_u32 s67, s75, 196736\n"
"ds_read_b64_tr_b16 a[86:87], v15 offset:12800\n"
"v_mfma_f32_16x16x32_bf16 v[112:115], a[52:55], a[148:151], v[112:115]\n"
"ds_read_b64_tr_b16 a[88:89], v15 offset:8200\n"
"v_mfma_f32_16x16x32_bf16 v[140:143], a[60:63], a[148:151], v[28:31]\n"
"ds_read_b64_tr_b16 a[90:91], v15 offset:8712\n"
"buffer_load_dwordx4 v11, s[52:55], s67 offen offset:0  lds\n"
"v_mfma_f32_16x16x32_bf16 v[140:143], a[56:59], a[144:147], v[140:143]\n"
"ds_read_b64_tr_b16 a[92:93], v15 offset:12296\n"
"v_mfma_f32_16x16x32_bf16 a[12:15], a[56:59], a[152:155], a[12:15]\n"
"s_add_u32 s67, s75, 212096\n"
"ds_read_b64_tr_b16 a[94:95], v15 offset:12808\n"
"v_mfma_f32_16x16x32_bf16 a[12:15], a[60:63], a[156:159], a[12:15]\n"
"v_mfma_f32_16x16x32_bf16 a[8:11], a[52:55], a[156:159], a[8:11]\n"
"buffer_load_dwordx4 v11, s[52:55], s67 offen offset:1024  lds\n"
"v_mfma_f32_16x16x32_bf16 a[8:11], a[48:51], a[152:155], a[8:11]\n"
"s_waitcnt lgkmcnt(8)\n"
"v_mfma_f32_16x16x32_bf16 v[144:147], a[64:67], a[96:99], 1.0\n"
"s_add_u32 s67, s75, 227456\n"
"v_mfma_f32_16x16x32_bf16 v[144:147], a[68:71], a[100:103], v[144:147]\n"
"v_mfma_f32_16x16x32_bf16 v[172:175], a[76:79], a[100:103], 1.0\n"
"s_waitcnt lgkmcnt(0)\n"
"v_readfirstlane_b32 s78, v252\n"
"v_readfirstlane_b32 s79, v253\n"
"v_mfma_f32_16x16x32_bf16 v[172:175], a[72:75], a[96:99], v[172:175]\n"
"buffer_load_dwordx4 v11, s[52:55], s67 offen offset:2048  lds\n"
"v_mfma_f32_16x16x32_bf16 v[176:179], a[72:75], a[104:107], 1.0\n"
"v_mfma_f32_16x16x32_bf16 v[176:179], a[76:79], a[108:111], v[176:179]\n"
"s_add_u32 s67, s75, 242816\n"
"v_mfma_f32_16x16x32_bf16 v[148:151], a[68:71], a[108:111], 1.0\n"
"s_add_u32 s78, s78, 16\n"
"v_mfma_f32_16x16x32_bf16 v[148:151], a[64:67], a[104:107], v[148:151]\n"
"s_add_u32 s79, s79, 16\n"
"v_mfma_f32_16x16x32_bf16 v[152:155], a[64:67], a[112:115], 1.0\n"
"s_sub_u32 s66, 40960, s73\n"
"v_mfma_f32_16x16x32_bf16 v[152:155], a[68:71], a[116:119], v[152:155]\n"
"buffer_load_dwordx4 v11, s[52:55], s67 offen offset:3072  lds\n"
"v_mfma_f32_16x16x32_bf16 v[180:183], a[76:79], a[116:119], 1.0\n"
"s_add_u32 m0, s72, 98304\n"
"v_add_u32 v12, s66, v7\n"
"v_mfma_f32_16x16x32_bf16 v[180:183], a[72:75], a[112:115], v[180:183]\n"
"v_mfma_f32_16x16x32_bf16 v[184:187], a[72:75], a[120:123], 1.0\n"
"s_add_u32 s67, s60, 256\n"
"v_add_u32 v13, s66, v8\n"
"v_mfma_f32_16x16x32_bf16 v[184:187], a[76:79], a[124:127], v[184:187]\n"
"buffer_load_dwordx4 v6, s[48:51], s67 offen offset:0  lds\n"
"v_mfma_f32_16x16x32_bf16 v[156:159], a[68:71], a[124:127], 1.0\n"
"v_mfma_f32_16x16x32_bf16 v[156:159], a[64:67], a[120:123], v[156:159]\n"
"s_add_u32 s67, s60, 15616\n"
"v_mfma_f32_16x16x32_bf16 v[160:163], a[64:67], a[128:131], 1.0\n"
"v_mfma_f32_16x16x32_bf16 v[160:163], a[68:71], a[132:135], v[160:163]\n"
"L_st535:\n"
"s_cbranch_execz L_st2683\n"
"buffer_load_dwordx4 v6, s[48:51], s67 offen offset:1024  lds\n"
"v_mfma_f32_16x16x32_bf16 v[188:191], a[76:79], a[132:135], 1.0\n"
"v_mfma_f32_16x16x32_bf16 v[188:191], a[72:75], a[128:131], v[188:191]\n"
"s_add_u32 s67, s60, 30976\n"
"v_mfma_f32_16x16x32_bf16 v[192:195], a[72:75], a[136:139], 1.0\n"
"v_mfma_f32_16x16x32_bf16 v[192:195], a[76:79], a[140:143], v[192:195]\n"
"buffer_load_dwordx4 v6, s[48:51], s67 offen offset:2048  lds\n"
"v_mfma_f32_16x16x32_bf16 v[164:167], a[68:71], a[140:143], 1.0\n"
"v_mfma_f32_16x16x32_bf16 v[164:167], a[64:67], a[136:139], v[164:167]\n"
"s_add_u32 s67, s60, 46336\n"
"v_mfma_f32_16x16x32_bf16 v[168:171], a[64:67], a[144:147], 1.0\n"
"v_mfma_f32_16x16x32_bf16 v[168:171], a[68:71], a[148:151], v[168:171]\n"
"v_mfma_f32_16x16x32_bf16 v[196:199], a[76:79], a[148:151], 1.0\n"
"buffer_load_dwordx4 v6, s[48:51], s67 offen offset:3072  lds\n"
"v_mfma_f32_16x16x32_bf16 v[196:199], a[72:75], a[144:147], v[196:199]\n"
"v_mfma_f32_16x16x32_bf16 a[20:23], a[72:75], a[152:155], 1.0\n"
"v_mfma_f32_16x16x32_bf16 a[20:23], a[76:79], a[156:159], a[20:23]\n"
"v_mfma_f32_16x16x32_bf16 a[16:19], a[68:71], a[156:159], 1.0\n"
"v_mfma_f32_16x16x32_bf16 a[16:19], a[64:67], a[152:155], a[16:19]\n"
"s_waitcnt vmcnt(21)\n"
"s_barrier\n"
"v_mfma_f32_16x16x32_bf16 v[200:203], a[80:83], a[96:99], 1.0\n"
"s_waitcnt vmcnt(12)\n"
"ds_read_b64_tr_b16 a[32:33], v9 offset:32768\n"
"v_mfma_f32_16x16x32_bf16 v[200:203], a[84:87], a[100:103], v[200:203]\n"
"s_add_u32 m0, s64, 8192\n"
"ds_read_b64_tr_b16 a[34:35], v9 offset:33280\n"
"v_mfma_f32_16x16x32_bf16 v[228:231], a[92:95], a[100:103], 1.0\n"
"ds_read_b128 a[100:103], v13 offset:16384\n"
"v_mfma_f32_16x16x32_bf16 v[228:231], a[88:91], a[96:99], v[228:231]\n"
"ds_read_b128 a[96:99], v12 offset:16384\n"
"v_mfma_f32_16x16x32_bf16 v[232:235], a[88:91], a[104:107], 1.0\n"
"ds_read_b64_tr_b16 a[36:37], v9 offset:36864\n"
"v_mfma_f32_16x16x32_bf16 v[232:235], a[92:95], a[108:111], v[232:235]\n"
"ds_read_b64_tr_b16 a[38:39], v9 offset:37376\n"
"v_mfma_f32_16x16x32_bf16 v[204:207], a[84:87], a[108:111], 1.0\n"
"ds_read_b128 a[108:111], v13 offset:18432\n"
"v_mfma_f32_16x16x32_bf16 v[204:207], a[80:83], a[104:107], v[204:207]\n"
"ds_read_b128 a[104:107], v12 offset:18432\n"
"ds_read_b64_tr_b16 a[40:41], v9 offset:32776\n"
"v_mfma_f32_16x16x32_bf16 v[208:211], a[80:83], a[112:115], 1.0\n"
"ds_read_b64_tr_b16 a[42:43], v9 offset:33288\n"
"v_mfma_f32_16x16x32_bf16 v[208:211], a[84:87], a[116:119], v[208:211]\n"
"ds_read_b64_tr_b16 a[44:45], v9 offset:36872\n"
"v_mfma_f32_16x16x32_bf16 v[236:239], a[92:95], a[116:119], 1.0\n"
"ds_read_b128 a[116:119], v13 offset:20480\n"
"v_mfma_f32_16x16x32_bf16 v[236:239], a[88:91], a[112:115], v[236:239]\n"
"ds_read_b128 a[112:115], v12 offset:20480\n"
"v_mfma_f32_16x16x32_bf16 v[240:243], a[88:91], a[120:123], 1.0\n"
"ds_read_b64_tr_b16 a[46:47], v9 offset:37384\n"
"v_mfma_f32_16x16x32_bf16 v[240:243], a[92:95], a[124:127], v[240:243]\n"
"ds_read_b64_tr_b16 a[48:49], v15 offset:32768\n"
"v_mfma_f32_16x16x32_bf16 v[212:215], a[84:87], a[124:127], 1.0\n"
"s_add_u32 s67, s75, 262144\n"
"ds_read_b128 a[124:127], v13 offset:22528\n"
"v_mfma_f32_16x16x32_bf16 v[212:215], a[80:83], a[120:123], v[212:215]\n"
"ds_read_b128 a[120:123], v12 offset:22528\n"
"v_mfma_f32_16x16x32_bf16 v[216:219], a[80:83], a[128:131], 1.0\n"
"ds_read_b64_tr_b16 a[50:51], v15 offset:33280\n"
"v_mfma_f32_16x16x32_bf16 v[216:219], a[84:87], a[132:135], v[216:219]\n"
"ds_read_b64_tr_b16 a[52:53], v15 offset:36864\n"
"v_mfma_f32_16x16x32_bf16 v[244:247], a[92:95], a[132:135], 1.0\n"
"ds_read_b128 a[132:135], v13 offset:24576\n"
"v_mfma_f32_16x16x32_bf16 v[244:247], a[88:91], a[128:131], v[244:247]\n"
"ds_read_b128 a[128:131], v12 offset:24576\n"
"v_mfma_f32_16x16x32_bf16 v[248:251], a[88:91], a[136:139], 1.0\n"
"ds_read_b64_tr_b16 a[54:55], v15 offset:37376\n"
"v_mfma_f32_16x16x32_bf16 v[248:251], a[92:95], a[140:143], v[248:251]\n"
"ds_read_b64_tr_b16 a[56:57], v15 offset:32776\n"
"v_mfma_f32_16x16x32_bf16 v[220:223], a[84:87], a[140:143], 1.0\n"
"ds_read_b128 a[140:143], v13 offset:26624\n"
"v_mfma_f32_16x16x32_bf16 v[220:223], a[80:83], a[136:139], v[220:223]\n"
"ds_read_b128 a[136:139], v12 offset:26624\n"
"v_mfma_f32_16x16x32_bf16 v[224:227], a[80:83], a[144:147], 1.0\n"
"ds_read_b64_tr_b16 a[58:59], v15 offset:33288\n"
"v_mfma_f32_16x16x32_bf16 v[224:227], a[84:87], a[148:151], v[224:227]\n"
"ds_read_b64_tr_b16 a[60:61], v15 offset:36872\n"
"v_mfma_f32_16x16x32_bf16 v[252:255], a[92:95], a[148:151], 1.0\n"
"ds_read_b128 a[148:151], v13 offset:28672\n"
"v_mfma_f32_16x16x32_bf16 v[252:255], a[88:91], a[144:147], v[252:255]\n"
"ds_read_b128 a[144:147], v12 offset:28672\n"
"ds_read_b64_tr_b16 a[62:63], v15 offset:37384\n"
"v_mfma_f32_16x16x32_bf16 a[28:31], a[88:91], a[152:155], 1.0\n"
"v_mfma_f32_16x16x32_bf16 a[28:31], a[92:95], a[156:159], a[28:31]\n"
"v_mfma_f32_16x16x32_bf16 a[24:27], a[84:87], a[156:159], 1.0\n"
"ds_read_b128 a[156:159], v13 offset:30720\n"
"v_mfma_f32_16x16x32_bf16 a[24:27], a[80:83], a[152:155], a[24:27]\n"
"s_waitcnt lgkmcnt(15)\n"
"ds_read_b128 a[152:155], v12 offset:30720\n"
"v_mfma_f32_16x16x32_bf16 v[32:35], a[32:35], a[96:99], v[32:35]\n"
"buffer_load_dwordx4 v11, s[52:55], s67 offen offset:0  lds\n"
"v_mfma_f32_16x16x32_bf16 v[32:35], a[36:39], a[100:103], v[32:35]\n"
"s_waitcnt lgkmcnt(2)\n"
"v_mfma_f32_16x16x32_bf16 v[60:63], a[44:47], a[100:103], v[60:63]\n"
"s_add_u32 s67, s75, 277504\n"
"v_mfma_f32_16x16x32_bf16 v[60:63], a[40:43], a[96:99], v[60:63]\n"
"v_mfma_f32_16x16x32_bf16 v[64:67], a[40:43], a[104:107], v[64:67]\n"
"v_mfma_f32_16x16x32_bf16 v[64:67], a[44:47], a[108:111], v[64:67]\n"
"buffer_load_dwordx4 v11, s[52:55], s67 offen offset:1024  lds\n"
"v_mfma_f32_16x16x32_bf16 v[36:39], a[36:39], a[108:111], v[36:39]\n"
"v_mfma_f32_16x16x32_bf16 v[36:39], a[32:35], a[104:107], v[36:39]\n"
"s_add_u32 s67, s75, 292864\n"
"v_mfma_f32_16x16x32_bf16 v[40:43], a[32:35], a[112:115], v[40:43]\n"
"v_mfma_f32_16x16x32_bf16 v[40:43], a[36:39], a[116:119], v[40:43]\n"
"v_mfma_f32_16x16x32_bf16 v[68:71], a[44:47], a[116:119], v[68:71]\n"
"buffer_load_dwordx4 v11, s[52:55], s67 offen offset:2048  lds\n"
"v_mfma_f32_16x16x32_bf16 v[68:71], a[40:43], a[112:115], v[68:71]\n"
"v_mfma_f32_16x16x32_bf16 v[72:75], a[40:43], a[120:123], v[72:75]\n"
"s_add_u32 s67, s75, 308224\n"
"v_mfma_f32_16x16x32_bf16 v[72:75], a[44:47], a[124:127], v[72:75]\n"
"v_mfma_f32_16x16x32_bf16 v[44:47], a[36:39], a[124:127], v[44:47]\n"
"v_mov_b32 v16, -1\n"
"v_mfma_f32_16x16x32_bf16 v[44:47], a[32:35], a[120:123], v[44:47]\n"
"buffer_load_dwordx4 v11, s[52:55], s67 offen offset:3072  lds\n"
"v_mfma_f32_16x16x32_bf16 v[48:51], a[32:35], a[128:131], v[48:51]\n"
"s_add_u32 m0, s64, 12288\n"
"v_mfma_f32_16x16x32_bf16 v[48:51], a[36:39], a[132:135], v[48:51]\n"
"v_mov_b32 v17, -1\n"
"v_mfma_f32_16x16x32_bf16 v[76:79], a[44:47], a[132:135], v[76:79]\n"
"s_waitcnt vmcnt(8)\n"
"ds_read_b64_tr_b16 a[64:65], v9 offset:0\n"
"v_mfma_f32_16x16x32_bf16 v[76:79], a[40:43], a[128:131], v[76:79]\n"
"s_add_u32 s67, s75, 327680\n"
"ds_read_b64_tr_b16 a[66:67], v9 offset:512\n"
"v_mfma_f32_16x16x32_bf16 v[80:83], a[40:43], a[136:139], v[80:83]\n"
"ds_read_b64_tr_b16 a[68:69], v9 offset:4096\n"
"buffer_load_dwordx4 v11, s[52:55], s67 offen offset:0  lds\n"
"v_mfma_f32_16x16x32_bf16 v[80:83], a[44:47], a[140:143], v[80:83]\n"
"ds_read_b64_tr_b16 a[70:71], v9 offset:4608\n"
"v_mfma_f32_16x16x32_bf16 v[52:55], a[36:39], a[140:143], v[52:55]\n"
"s_add_u32 s67, s75, 343040\n"
"ds_read_b64_tr_b16 a[72:73], v9 offset:8\n"
"v_mfma_f32_16x16x32_bf16 v[52:55], a[32:35], a[136:139], v[52:55]\n"
"s_waitcnt lgkmcnt(5)\n"
"ds_read_b64_tr_b16 a[74:75], v9 offset:520\n"
"v_mfma_f32_16x16x32_bf16 v[56:59], a[32:35], a[144:147], v[56:59]\n"
"ds_read_b64_tr_b16 a[76:77], v9 offset:4104\n"
"buffer_load_dwordx4 v11, s[52:55], s67 offen offset:1024  lds\n"
"v_mfma_f32_16x16x32_bf16 v[56:59], a[36:39], a[148:151], v[56:59]\n"
"s_add_u32 s67, s75, 358400\n"
"ds_read_b64_tr_b16 a[78:79], v9 offset:4616\n"
"v_mfma_f32_16x16x32_bf16 v[84:87], a[44:47], a[148:151], v[84:87]\n"
"s_add_u32 s69, %11, 8192\n"
"v_lshrrev_b32 v18, 1, v0\n"
"v_mfma_f32_16x16x32_bf16 v[84:87], a[40:43], a[144:147], v[84:87]\n"
"ds_read_b64_tr_b16 a[80:81], v15 offset:0\n"
"v_mfma_f32_16x16x32_bf16 a[4:7], a[40:43], a[152:155], a[4:7]\n"
"ds_read_b64_tr_b16 a[82:83], v15 offset:512\n"
"buffer_load_dwordx4 v11, s[52:55], s67 offen offset:2048  lds\n"
"v_mfma_f32_16x16x32_bf16 a[4:7], a[44:47], a[156:159], a[4:7]\n"
"ds_read_b64_tr_b16 a[84:85], v15 offset:4096\n"
"v_mfma_f32_16x16x32_bf16 a[0:3], a[36:39], a[156:159], a[0:3]\n"
"s_add_u32 s67, s75, 373760\n"
"ds_read_b64_tr_b16 a[86:87], v15 offset:4608\n"
"v_mfma_f32_16x16x32_bf16 a[0:3], a[32:35], a[152:155], a[0:3]\n"
"ds_read_b64_tr_b16 a[88:89], v15 offset:8\n"
"v_mfma_f32_16x16x32_bf16 v[88:91], a[48:51], a[96:99], v[88:91]\n"
"ds_read_b64_tr_b16 a[90:91], v15 offset:520\n"
"v_mfma_f32_16x16x32_bf16 v[88:91], a[52:55], a[100:103], v[88:91]\n"
"ds_read_b64_tr_b16 a[92:93], v15 offset:4104\n"
"buffer_load_dwordx4 v11, s[52:55], s67 offen offset:3072  lds\n"
"v_mfma_f32_16x16x32_bf16 v[116:119], a[60:63], a[100:103], v[116:119]\n"
"ds_read_b64_tr_b16 a[94:95], v15 offset:4616\n"
"v_mfma_f32_16x16x32_bf16 v[116:119], a[56:59], a[96:99], v[116:119]\n"
"s_add_u32 m0, s64, 0\n"
"v_mfma_f32_16x16x32_bf16 v[120:123], a[56:59], a[104:107], v[120:123]\n"
"v_mfma_f32_16x16x32_bf16 v[120:123], a[60:63], a[108:111], v[120:123]\n"
"v_mfma_f32_16x16x32_bf16 v[92:95], a[52:55], a[108:111], v[92:95]\n"
"s_add_u32 s67, s75, 262272\n"
"v_mfma_f32_16x16x32_bf16 v[92:95], a[48:51], a[104:107], v[92:95]\n"
"v_mfma_f32_16x16x32_bf16 v[96:99], a[48:51], a[112:115], v[96:99]\n"
"s_waitcnt lgkmcnt(0)\n"
"v_mfma_f32_16x16x32_bf16 v[96:99], a[52:55], a[116:119], v[96:99]\n"
"buffer_load_dwordx4 v11, s[52:55], s67 offen offset:0  lds\n"
"v_mfma_f32_16x16x32_bf16 v[124:127], a[60:63], a[116:119], v[124:127]\n"
"v_mfma_f32_16x16x32_bf16 v[124:127], a[56:59], a[112:115], v[124:127]\n"
"s_add_u32 s67, s75, 277632\n"
"v_mfma_f32_16x16x32_bf16 v[128:131], a[56:59], a[120:123], v[128:131]\n"
"v_mfma_f32_16x16x32_bf16 v[128:131], a[60:63], a[124:127], v[128:131]\n"
"v_mfma_f32_16x16x32_bf16 v[100:103], a[52:55], a[124:127], v[100:103]\n"
"buffer_load_dwordx4 v11, s[52:55], s67 offen offset:1024  lds\n"
"v_mfma_f32_16x16x32_bf16 v[100:103], a[48:51], a[120:123], v[100:103]\n"
"v_mfma_f32_16x16x32_bf16 v[104:107], a[48:51], a[128:131], v[104:107]\n"
"s_add_u32 s67, s75, 292992\n"
"v_mfma_f32_16x16x32_bf16 v[104:107], a[52:55], a[132:135], v[104:107]\n"
"v_mfma_f32_16x16x32_bf16 v[132:135], a[60:63], a[132:135], v[132:135]\n"
"buffer_load_dwordx4 v11, s[52:55], s67 offen offset:2048  lds\n"
"v_mfma_f32_16x16x32_bf16 v[132:135], a[56:59], a[128:131], v[132:135]\n"
"v_mfma_f32_16x16x32_bf16 v[136:139], a[56:59], a[136:139], v[136:139]\n"
"s_add_u32 s67, s75, 308352\n"
"v_mfma_f32_16x16x32_bf16 v[136:139], a[60:63], a[140:143], v[136:139]\n"
"v_mfma_f32_16x16x32_bf16 v[108:111], a[52:55], a[140:143], v[108:111]\n"
"v_mfma_f32_16x16x32_bf16 v[108:111], a[48:51], a[136:139], v[108:111]\n"
"buffer_load_dwordx4 v11, s[52:55], s67 offen offset:3072  lds\n"
"v_mfma_f32_16x16x32_bf16 v[112:115], a[48:51], a[144:147], v[112:115]\n"
"v_mfma_f32_16x16x32_bf16 v[112:115], a[52:55], a[148:151], v[112:115]\n"
"s_add_u32 m0, s64, 4096\n"
"v_mfma_f32_16x16x32_bf16 v[140:143], a[60:63], a[148:151], v[140:143]\n"
"v_mfma_f32_16x16x32_bf16 v[140:143], a[56:59], a[144:147], v[140:143]\n"
"s_add_u32 s67, s75, 327808\n"
"v_mfma_f32_16x16x32_bf16 a[12:15], a[56:59], a[152:155], a[12:15]\n"
"v_mfma_f32_16x16x32_bf16 a[12:15], a[60:63], a[156:159], a[12:15]\n"
"buffer_load_dwordx4 v11, s[52:55], s67 offen offset:0  lds\n"
"v_mfma_f32_16x16x32_bf16 a[8:11], a[52:55], a[156:159], a[8:11]\n"
"v_mfma_f32_16x16x32_bf16 a[8:11], a[48:51], a[152:155], a[8:11]\n"
"s_add_u32 s67, s75, 343168\n"
"v_mfma_f32_16x16x32_bf16 v[144:147], a[64:67], a[96:99], v[144:147]\n"
"v_mfma_f32_16x16x32_bf16 v[144:147], a[68:71], a[100:103], v[144:147]\n"
"buffer_load_dwordx4 v11, s[52:55], s67 offen offset:1024  lds\n"
"v_mfma_f32_16x16x32_bf16 v[172:175], a[76:79], a[100:103], v[172:175]\n"
"v_mfma_f32_16x16x32_bf16 v[172:175], a[72:75], a[96:99], v[172:175]\n"
"s_add_u32 s67, s75, 358528\n"
"v_mfma_f32_16x16x32_bf16 v[176:179], a[72:75], a[104:107], v[176:179]\n"
"v_mfma_f32_16x16x32_bf16 v[176:179], a[76:79], a[108:111], v[176:179]\n"
"buffer_load_dwordx4 v11, s[52:55], s67 offen offset:2048  lds\n"
"v_mfma_f32_16x16x32_bf16 v[148:151], a[68:71], a[108:111], v[148:151]\n"
"v_mfma_f32_16x16x32_bf16 v[148:151], a[64:67], a[104:107], v[148:151]\n"
"s_add_u32 s67, s75, 373888\n"
"v_mfma_f32_16x16x32_bf16 v[152:155], a[64:67], a[112:115], v[152:155]\n"
"s_sub_u32 s66, 81920, s73\n"
"v_mfma_f32_16x16x32_bf16 v[152:155], a[68:71], a[116:119], v[152:155]\n"
"buffer_load_dwordx4 v11, s[52:55], s67 offen offset:3072  lds\n"
"v_mfma_f32_16x16x32_bf16 v[180:183], a[76:79], a[116:119], v[180:183]\n"
"v_add_u32 v12, s66, v7\n"
"v_mfma_f32_16x16x32_bf16 v[180:183], a[72:75], a[112:115], v[180:183]\n"
"s_add_u32 m0, s72, 139264\n"
"v_mfma_f32_16x16x32_bf16 v[184:187], a[72:75], a[120:123], v[184:187]\n"
"v_add_u32 v13, s66, v8\n"
"v_mfma_f32_16x16x32_bf16 v[184:187], a[76:79], a[124:127], v[184:187]\n"
"s_add_u32 s67, s60, 384\n"
"v_mfma_f32_16x16x32_bf16 v[156:159], a[68:71], a[124:127], v[156:159]\n"
"v_mfma_f32_16x16x32_bf16 v[156:159], a[64:67], a[120:123], v[156:159]\n"
"buffer_load_dwordx4 v6, s[48:51], s67 offen offset:0  lds\n"
"v_mfma_f32_16x16x32_bf16 v[160:163], a[64:67], a[128:131], v[160:163]\n"
"v_mfma_f32_16x16x32_bf16 v[160:163], a[68:71], a[132:135], v[160:163]\n"
"s_add_u32 s67, s60, 15744\n"
"v_mfma_f32_16x16x32_bf16 v[188:191], a[76:79], a[132:135], v[188:191]\n"
"v_mfma_f32_16x16x32_bf16 v[188:191], a[72:75], a[128:131], v[188:191]\n"
"buffer_load_dwordx4 v6, s[48:51], s67 offen offset:1024  lds\n"
"v_mfma_f32_16x16x32_bf16 v[192:195], a[72:75], a[136:139], v[192:195]\n"
"v_mfma_f32_16x16x32_bf16 v[192:195], a[76:79], a[140:143], v[192:195]\n"
"s_add_u32 s67, s60, 31104\n"
"v_mfma_f32_16x16x32_bf16 v[164:167], a[68:71], a[140:143], v[164:167]\n"
"v_mfma_f32_16x16x32_bf16 v[164:167], a[64:67], a[136:139], v[164:167]\n"
"buffer_load_dwordx4 v6, s[48:51], s67 offen offset:2048  lds\n"
"v_mfma_f32_16x16x32_bf16 v[168:171], a[64:67], a[144:147], v[168:171]\n"
"v_mfma_f32_16x16x32_bf16 v[168:171], a[68:71], a[148:151], v[168:171]\n"
"s_add_u32 s67, s60, 46464\n"
"v_mfma_f32_16x16x32_bf16 v[196:199], a[76:79], a[148:151], v[196:199]\n"
"v_mfma_f32_16x16x32_bf16 v[196:199], a[72:75], a[144:147], v[196:199]\n"
"v_mfma_f32_16x16x32_bf16 a[20:23], a[72:75], a[152:155], a[20:23]\n"
"buffer_load_dwordx4 v6, s[48:51], s67 offen offset:3072  lds\n"
"v_mfma_f32_16x16x32_bf16 a[20:23], a[76:79], a[156:159], a[20:23]\n"
"s_add_u32 s70, s75, 655360\n"
"v_mfma_f32_16x16x32_bf16 a[16:19], a[68:71], a[156:159], a[16:19]\n"
"s_add_u32 s70, s70, s65\n"
"v_mfma_f32_16x16x32_bf16 a[16:19], a[64:67], a[152:155], a[16:19]\n"
"s_waitcnt vmcnt(20)\n"
"s_barrier\n"
"v_mfma_f32_16x16x32_bf16 v[200:203], a[80:83], a[96:99], v[200:203]\n"
"s_waitcnt vmcnt(12)\n"
"ds_read_b64_tr_b16 a[32:33], v9 offset:8192\n"
"v_mfma_f32_16x16x32_bf16 v[200:203], a[84:87], a[100:103], v[200:203]\n"
"ds_read_b64_tr_b16 a[34:35], v9 offset:8704\n"
"buffer_load_dword a252, v14, s[52:55], s70 offen\n"
"v_mfma_f32_16x16x32_bf16 v[228:231], a[92:95], a[100:103], v[228:231]\n"
"ds_read_b128 a[100:103], v13 offset:16384\n"
"v_mfma_f32_16x16x32_bf16 v[228:231], a[88:91], a[96:99], v[228:231]\n"
"ds_read_b128 a[96:99], v12 offset:16384\n"
"v_mfma_f32_16x16x32_bf16 v[232:235], a[88:91], a[104:107], v[232:235]\n"
"ds_read_b64_tr_b16 a[36:37], v9 offset:12288\n"
"v_mfma_f32_16x16x32_bf16 v[232:235], a[92:95], a[108:111], v[232:235]\n"
"ds_read_b64_tr_b16 a[38:39], v9 offset:12800\n"
"v_mfma_f32_16x16x32_bf16 v[204:207], a[84:87], a[108:111], v[204:207]\n"
"ds_read_b128 a[108:111], v13 offset:18432\n"
"v_mfma_f32_16x16x32_bf16 v[204:207], a[80:83], a[104:107], v[204:207]\n"
"ds_read_b128 a[104:107], v12 offset:18432\n"
"s_add_u32 m0, s64, 8192\n"
"ds_read_b64_tr_b16 a[40:41], v9 offset:8200\n"
"v_mfma_f32_16x16x32_bf16 v[208:211], a[80:83], a[112:115], v[208:211]\n"
"ds_read_b64_tr_b16 a[42:43], v9 offset:8712\n"
"v_mfma_f32_16x16x32_bf16 v[208:211], a[84:87], a[116:119], v[208:211]\n"
"ds_read_b64_tr_b16 a[44:45], v9 offset:12296\n"
"v_mfma_f32_16x16x32_bf16 v[236:239], a[92:95], a[116:119], v[236:239]\n"
"ds_read_b128 a[116:119], v13 offset:20480\n"
"v_mfma_f32_16x16x32_bf16 v[236:239], a[88:91], a[112:115], v[236:239]\n"
"ds_read_b128 a[112:115], v12 offset:20480\n"
"v_mfma_f32_16x16x32_bf16 v[240:243], a[88:91], a[120:123], v[240:243]\n"
"ds_read_b64_tr_b16 a[46:47], v9 offset:12808\n"
"v_mfma_f32_16x16x32_bf16 v[240:243], a[92:95], a[124:127], v[240:243]\n"
"ds_read_b64_tr_b16 a[48:49], v15 offset:8192\n"
"v_mfma_f32_16x16x32_bf16 v[212:215], a[84:87], a[124:127], v[212:215]\n"
"ds_read_b128 a[124:127], v13 offset:22528\n"
"v_mfma_f32_16x16x32_bf16 v[212:215], a[80:83], a[120:123], v[212:215]\n"
"ds_read_b128 a[120:123], v12 offset:22528\n"
"v_mfma_f32_16x16x32_bf16 v[216:219], a[80:83], a[128:131], v[216:219]\n"
"ds_read_b64_tr_b16 a[50:51], v15 offset:8704\n"
"v_mfma_f32_16x16x32_bf16 v[216:219], a[84:87], a[132:135], v[216:219]\n"
"ds_read_b64_tr_b16 a[52:53], v15 offset:12288\n"
"v_mfma_f32_16x16x32_bf16 v[244:247], a[92:95], a[132:135], v[244:247]\n"
"ds_read_b128 a[132:135], v13 offset:24576\n"
"v_mfma_f32_16x16x32_bf16 v[244:247], a[88:91], a[128:131], v[244:247]\n"
"s_add_u32 s67, s75, 393216\n"
"ds_read_b128 a[128:131], v12 offset:24576\n"
"v_mfma_f32_16x16x32_bf16 v[248:251], a[88:91], a[136:139], v[248:251]\n"
"ds_read_b64_tr_b16 a[54:55], v15 offset:12800\n"
"v_mfma_f32_16x16x32_bf16 v[248:251], a[92:95], a[140:143], v[248:251]\n"
"s_add_u32 s66, s78, 1\n"
"ds_read_b64_tr_b16 a[56:57], v15 offset:8200\n"
"v_mfma_f32_16x16x32_bf16 v[220:223], a[84:87], a[140:143], v[220:223]\n"
"ds_read_b128 a[140:143], v13 offset:26624\n"
"v_mfma_f32_16x16x32_bf16 v[220:223], a[80:83], a[136:139], v[220:223]\n"
"ds_read_b128 a[136:139], v12 offset:26624\n"
"v_mov_b32 v28, s66\n"
"v_mfma_f32_16x16x32_bf16 v[224:227], a[80:83], a[144:147], v[224:227]\n"
"ds_read_b64_tr_b16 a[58:59], v15 offset:8712\n"
"v_mov_b32 v29, s79\n"
"v_mfma_f32_16x16x32_bf16 v[224:227], a[84:87], a[148:151], v[224:227]\n"
"ds_read_b64_tr_b16 a[60:61], v15 offset:12296\n"
"v_mfma_f32_16x16x32_bf16 v[252:255], a[92:95], a[148:151], v[252:255]\n"
"buffer_store_dwordx2 v[28:29], off, s[56:59], %24 sc1\n"
"ds_read_b128 a[148:151], v13 offset:28672\n"
"v_mfma_f32_16x16x32_bf16 v[252:255], a[88:91], a[144:147], v[252:255]\n"
"ds_read_b128 a[144:147], v12 offset:28672\n"
"ds_read_b64_tr_b16 a[62:63], v15 offset:12808\n"
"v_mfma_f32_16x16x32_bf16 a[28:31], a[88:91], a[152:155], a[28:31]\n"
"v_mfma_f32_16x16x32_bf16 a[28:31], a[92:95], a[156:159], a[28:31]\n"
"v_mfma_f32_16x16x32_bf16 a[24:27], a[84:87], a[156:159], a[24:27]\n"
"ds_read_b128 a[156:159], v13 offset:30720\n"
"v_mfma_f32_16x16x32_bf16 a[24:27], a[80:83], a[152:155], a[24:27]\n"
"s_waitcnt lgkmcnt(15)\n"
"ds_read_b128 a[152:155], v12 offset:30720\n"
"v_mfma_f32_16x16x32_bf16 v[32:35], a[32:35], a[96:99], v[32:35]\n"
"v_mfma_f32_16x16x32_bf16 v[32:35], a[36:39], a[100:103], v[32:35]\n"
"s_waitcnt lgkmcnt(2)\n"
"v_mfma_f32_16x16x32_bf16 v[60:63], a[44:47], a[100:103], v[60:63]\n"
"buffer_load_dwordx4 v11, s[52:55], s67 offen offset:0  lds\n"
"v_mfma_f32_16x16x32_bf16 v[60:63], a[40:43], a[96:99], v[60:63]\n"
"v_mfma_f32_16x16x32_bf16 v[64:67], a[40:43], a[104:107], v[64:67]\n"
"s_add_u32 s67, s75, 408576\n"
"v_mfma_f32_16x16x32_bf16 v[64:67], a[44:47], a[108:111], v[64:67]\n"
"v_mfma_f32_16x16x32_bf16 v[36:39], a[36:39], a[108:111], v[36:39]\n"
"v_mfma_f32_16x16x32_bf16 v[36:39], a[32:35], a[104:107], v[36:39]\n"
"buffer_load_dwordx4 v11, s[52:55], s67 offen offset:1024  lds\n"
"v_mfma_f32_16x16x32_bf16 v[40:43], a[32:35], a[112:115], v[40:43]\n"
"v_mfma_f32_16x16x32_bf16 v[40:43], a[36:39], a[116:119], v[40:43]\n"
"s_add_u32 s67, s75, 423936\n"
"v_mfma_f32_16x16x32_bf16 v[68:71], a[44:47], a[116:119], v[68:71]\n"
"v_mfma_f32_16x16x32_bf16 v[68:71], a[40:43], a[112:115], v[68:71]\n"
"v_mfma_f32_16x16x32_bf16 v[72:75], a[40:43], a[120:123], v[72:75]\n"
"buffer_load_dwordx4 v11, s[52:55], s67 offen offset:2048  lds\n"
"v_mfma_f32_16x16x32_bf16 v[72:75], a[44:47], a[124:127], v[72:75]\n"
"v_mfma_f32_16x16x32_bf16 v[44:47], a[36:39], a[124:127], v[44:47]\n"
"s_add_u32 s67, s75, 439296\n"
"v_mfma_f32_16x16x32_bf16 v[44:47], a[32:35], a[120:123], v[44:47]\n"
"v_mfma_f32_16x16x32_bf16 v[48:51], a[32:35], a[128:131], v[48:51]\n"
"buffer_load_dwordx4 v11, s[52:55], s67 offen offset:3072  lds\n"
"v_mfma_f32_16x16x32_bf16 v[48:51], a[36:39], a[132:135], v[48:51]\n"
"s_add_u32 m0, s64, 12288\n"
"v_mfma_f32_16x16x32_bf16 v[76:79], a[44:47], a[132:135], v[76:79]\n"
"s_waitcnt vmcnt(10)\n"
"ds_read_b64_tr_b16 a[64:65], v9 offset:0\n"
"v_mfma_f32_16x16x32_bf16 v[76:79], a[40:43], a[128:131], v[76:79]\n"
"ds_read_b64_tr_b16 a[66:67], v9 offset:512\n"
"v_mfma_f32_16x16x32_bf16 v[80:83], a[40:43], a[136:139], v[80:83]\n"
"s_add_u32 s67, s75, 458752\n"
"ds_read_b64_tr_b16 a[68:69], v9 offset:4096\n"
"v_mfma_f32_16x16x32_bf16 v[80:83], a[44:47], a[140:143], v[80:83]\n"
"ds_read_b64_tr_b16 a[70:71], v9 offset:4608\n"
"v_mfma_f32_16x16x32_bf16 v[52:55], a[36:39], a[140:143], v[52:55]\n"
"ds_read_b64_tr_b16 a[72:73], v9 offset:8\n"
"buffer_load_dwordx4 v11, s[52:55], s67 offen offset:0  lds\n"
"v_mfma_f32_16x16x32_bf16 v[52:55], a[32:35], a[136:139], v[52:55]\n"
"s_waitcnt lgkmcnt(5)\n"
"ds_read_b64_tr_b16 a[74:75], v9 offset:520\n"
"v_mfma_f32_16x16x32_bf16 v[56:59], a[32:35], a[144:147], v[56:59]\n"
"s_add_u32 s67, s75, 474112\n"
"ds_read_b64_tr_b16 a[76:77], v9 offset:4104\n"
"v_mfma_f32_16x16x32_bf16 v[56:59], a[36:39], a[148:151], v[56:59]\n"
"ds_read_b64_tr_b16 a[78:79], v9 offset:4616\n"
"v_mfma_f32_16x16x32_bf16 v[84:87], a[44:47], a[148:151], v[84:87]\n"
"buffer_load_dwordx4 v11, s[52:55], s67 offen offset:1024  lds\n"
"v_mfma_f32_16x16x32_bf16 v[84:87], a[40:43], a[144:147], v[84:87]\n"
"ds_read_b64_tr_b16 a[80:81], v15 offset:0\n"
"v_mfma_f32_16x16x32_bf16 a[4:7], a[40:43], a[152:155], a[4:7]\n"
"s_add_u32 s67, s75, 489472\n"
"ds_read_b64_tr_b16 a[82:83], v15 offset:512\n"
"v_mfma_f32_16x16x32_bf16 a[4:7], a[44:47], a[156:159], a[4:7]\n"
"ds_read_b64_tr_b16 a[84:85], v15 offset:4096\n"
"v_mfma_f32_16x16x32_bf16 a[0:3], a[36:39], a[156:159], a[0:3]\n"
"ds_read_b64_tr_b16 a[86:87], v15 offset:4608\n"
"buffer_load_dwordx4 v11, s[52:55], s67 offen offset:2048  lds\n"
"v_mfma_f32_16x16x32_bf16 a[0:3], a[32:35], a[152:155], a[0:3]\n"
"ds_read_b64_tr_b16 a[88:89], v15 offset:8\n"
"v_mfma_f32_16x16x32_bf16 v[88:91], a[48:51], a[96:99], v[88:91]\n"
"s_add_u32 s67, s75, 504832\n"
"ds_read_b64_tr_b16 a[90:91], v15 offset:520\n"
"v_mfma_f32_16x16x32_bf16 v[88:91], a[52:55], a[100:103], v[88:91]\n"
"ds_read_b64_tr_b16 a[92:93], v15 offset:4104\n"
"v_mfma_f32_16x16x32_bf16 v[116:119], a[60:63], a[100:103], v[116:119]\n"
"ds_read_b64_tr_b16 a[94:95], v15 offset:4616\n"
"v_mfma_f32_16x16x32_bf16 v[116:119], a[56:59], a[96:99], v[116:119]\n"
"buffer_load_dwordx4 v11, s[52:55], s67 offen offset:3072  lds\n"
"v_mfma_f32_16x16x32_bf16 v[120:123], a[56:59], a[104:107], v[120:123]\n"
"v_mfma_f32_16x16x32_bf16 v[120:123], a[60:63], a[108:111], v[120:123]\n"
"s_add_u32 m0, s64, 0\n"
"v_mfma_f32_16x16x32_bf16 v[92:95], a[52:55], a[108:111], v[92:95]\n"
"v_mfma_f32_16x16x32_bf16 v[92:95], a[48:51], a[104:107], v[92:95]\n"
"s_add_u32 s67, s75, 393344\n"
"v_mfma_f32_16x16x32_bf16 v[96:99], a[48:51], a[112:115], v[96:99]\n"
"s_waitcnt lgkmcnt(0)\n"
"v_mfma_f32_16x16x32_bf16 v[96:99], a[52:55], a[116:119], v[96:99]\n"
"buffer_load_dwordx4 v11, s[52:55], s67 offen offset:0  lds\n"
"v_mfma_f32_16x16x32_bf16 v[124:127], a[60:63], a[116:119], v[124:127]\n"
"v_mfma_f32_16x16x32_bf16 v[124:127], a[56:59], a[112:115], v[124:127]\n"
"s_add_u32 s67, s75, 408704\n"
"v_mfma_f32_16x16x32_bf16 v[128:131], a[56:59], a[120:123], v[128:131]\n"
"v_mfma_f32_16x16x32_bf16 v[128:131], a[60:63], a[124:127], v[128:131]\n"
"v_mfma_f32_16x16x32_bf16 v[100:103], a[52:55], a[124:127], v[100:103]\n"
"buffer_load_dwordx4 v11, s[52:55], s67 offen offset:1024  lds\n"
"v_mfma_f32_16x16x32_bf16 v[100:103], a[48:51], a[120:123], v[100:103]\n"
"v_mfma_f32_16x16x32_bf16 v[104:107], a[48:51], a[128:131], v[104:107]\n"
"s_add_u32 s67, s75, 424064\n"
"v_mfma_f32_16x16x32_bf16 v[104:107], a[52:55], a[132:135], v[104:107]\n"
"v_mfma_f32_16x16x32_bf16 v[132:135], a[60:63], a[132:135], v[132:135]\n"
"v_mfma_f32_16x16x32_bf16 v[132:135], a[56:59], a[128:131], v[132:135]\n"
"buffer_load_dwordx4 v11, s[52:55], s67 offen offset:2048  lds\n"
"v_mfma_f32_16x16x32_bf16 v[136:139], a[56:59], a[136:139], v[136:139]\n"
"v_mfma_f32_16x16x32_bf16 v[136:139], a[60:63], a[140:143], v[136:139]\n"
"s_add_u32 s67, s75, 439424\n"
"v_mfma_f32_16x16x32_bf16 v[108:111], a[52:55], a[140:143], v[108:111]\n"
"v_mfma_f32_16x16x32_bf16 v[108:111], a[48:51], a[136:139], v[108:111]\n"
"buffer_load_dwordx4 v11, s[52:55], s67 offen offset:3072  lds\n"
"v_mfma_f32_16x16x32_bf16 v[112:115], a[48:51], a[144:147], v[112:115]\n"
"v_mfma_f32_16x16x32_bf16 v[112:115], a[52:55], a[148:151], v[112:115]\n"
"s_add_u32 m0, s64, 4096\n"
"v_mfma_f32_16x16x32_bf16 v[140:143], a[60:63], a[148:151], v[140:143]\n"
"v_mfma_f32_16x16x32_bf16 v[140:143], a[56:59], a[144:147], v[140:143]\n"
"s_add_u32 s67, s75, 458880\n"
"v_mfma_f32_16x16x32_bf16 a[12:15], a[56:59], a[152:155], a[12:15]\n"
"v_mfma_f32_16x16x32_bf16 a[12:15], a[60:63], a[156:159], a[12:15]\n"
"buffer_load_dwordx4 v11, s[52:55], s67 offen offset:0  lds\n"
"v_mfma_f32_16x16x32_bf16 a[8:11], a[52:55], a[156:159], a[8:11]\n"
"v_mfma_f32_16x16x32_bf16 a[8:11], a[48:51], a[152:155], a[8:11]\n"
"s_add_u32 s67, s75, 474240\n"
"v_mfma_f32_16x16x32_bf16 v[144:147], a[64:67], a[96:99], v[144:147]\n"
"v_mfma_f32_16x16x32_bf16 v[144:147], a[68:71], a[100:103], v[144:147]\n"
"buffer_load_dwordx4 v11, s[52:55], s67 offen offset:1024  lds\n"
"v_mfma_f32_16x16x32_bf16 v[172:175], a[76:79], a[100:103], v[172:175]\n"
"v_mfma_f32_16x16x32_bf16 v[172:175], a[72:75], a[96:99], v[172:175]\n"
"s_add_u32 s67, s75, 489600\n"
"v_mfma_f32_16x16x32_bf16 v[176:179], a[72:75], a[104:107], v[176:179]\n"
"v_mfma_f32_16x16x32_bf16 v[176:179], a[76:79], a[108:111], v[176:179]\n"
"buffer_load_dwordx4 v11, s[52:55], s67 offen offset:2048  lds\n"
"v_mfma_f32_16x16x32_bf16 v[148:151], a[68:71], a[108:111], v[148:151]\n"
"v_mfma_f32_16x16x32_bf16 v[148:151], a[64:67], a[104:107], v[148:151]\n"
"s_add_u32 s67, s75, 504960\n"
"v_mfma_f32_16x16x32_bf16 v[152:155], a[64:67], a[112:115], v[152:155]\n"
"s_sub_u32 s66, 122880, s73\n"
"v_mfma_f32_16x16x32_bf16 v[152:155], a[68:71], a[116:119], v[152:155]\n"
"v_mfma_f32_16x16x32_bf16 v[180:183], a[76:79], a[116:119], v[180:183]\n"
"v_add_u32 v12, s66, v7\n"
"v_mfma_f32_16x16x32_bf16 v[180:183], a[72:75], a[112:115], v[180:183]\n"
"buffer_load_dwordx4 v11, s[52:55], s67 offen offset:3072  lds\n"
"v_mfma_f32_16x16x32_bf16 v[184:187], a[72:75], a[120:123], v[184:187]\n"
"s_add_u32 m0, s72, 16384\n"
"v_add_u32 v13, s66, v8\n"
"v_mfma_f32_16x16x32_bf16 v[184:187], a[76:79], a[124:127], v[184:187]\n"
"v_mfma_f32_16x16x32_bf16 v[156:159], a[68:71], a[124:127], v[156:159]\n"
"s_add_u32 s67, s60, 512\n"
"v_mfma_f32_16x16x32_bf16 v[156:159], a[64:67], a[120:123], v[156:159]\n"
"buffer_load_dwordx4 v6, s[48:51], s67 offen offset:0  lds\n"
"v_mfma_f32_16x16x32_bf16 v[160:163], a[64:67], a[128:131], v[160:163]\n"
"v_mfma_f32_16x16x32_bf16 v[160:163], a[68:71], a[132:135], v[160:163]\n"
"s_add_u32 s67, s60, 15872\n"
"v_mfma_f32_16x16x32_bf16 v[188:191], a[76:79], a[132:135], v[188:191]\n"
"v_mfma_f32_16x16x32_bf16 v[188:191], a[72:75], a[128:131], v[188:191]\n"
"buffer_load_dwordx4 v6, s[48:51], s67 offen offset:1024  lds\n"
"v_mfma_f32_16x16x32_bf16 v[192:195], a[72:75], a[136:139], v[192:195]\n"
"v_mfma_f32_16x16x32_bf16 v[192:195], a[76:79], a[140:143], v[192:195]\n"
"s_add_u32 s67, s60, 31232\n"
"v_mfma_f32_16x16x32_bf16 v[164:167], a[68:71], a[140:143], v[164:167]\n"
"v_mfma_f32_16x16x32_bf16 v[164:167], a[64:67], a[136:139], v[164:167]\n"
"buffer_load_dwordx4 v6, s[48:51], s67 offen offset:2048  lds\n"
"v_mfma_f32_16x16x32_bf16 v[168:171], a[64:67], a[144:147], v[168:171]\n"
"v_mfma_f32_16x16x32_bf16 v[168:171], a[68:71], a[148:151], v[168:171]\n"
"s_add_u32 s67, s60, 46592\n"
"v_mfma_f32_16x16x32_bf16 v[196:199], a[76:79], a[148:151], v[196:199]\n"
"v_mfma_f32_16x16x32_bf16 v[196:199], a[72:75], a[144:147], v[196:199]\n"
"v_mfma_f32_16x16x32_bf16 a[20:23], a[72:75], a[152:155], a[20:23]\n"
"buffer_load_dwordx4 v6, s[48:51], s67 offen offset:3072  lds\n"
"v_mfma_f32_16x16x32_bf16 a[20:23], a[76:79], a[156:159], a[20:23]\n"
"v_mfma_f32_16x16x32_bf16 a[16:19], a[68:71], a[156:159], a[16:19]\n"
"s_add_u32 s70, s75, 786432\n"
"s_add_u32 s70, s70, s65\n"
"v_mfma_f32_16x16x32_bf16 a[16:19], a[64:67], a[152:155], a[16:19]\n"
"s_waitcnt vmcnt(22)\n"
"s_barrier\n"
"v_mfma_f32_16x16x32_bf16 v[200:203], a[80:83], a[96:99], v[200:203]\n"
"s_waitcnt vmcnt(12)\n"
"ds_read_b64_tr_b16 a[32:33], v9 offset:8192\n"
"v_mfma_f32_16x16x32_bf16 v[200:203], a[84:87], a[100:103], v[200:203]\n"
"ds_read_b64_tr_b16 a[34:35], v9 offset:8704\n"
"v_mfma_f32_16x16x32_bf16 v[228:231], a[92:95], a[100:103], v[228:231]\n"
"ds_read_b128 a[100:103], v13 offset:16384\n"
"buffer_load_dword a252, v14, s[52:55], s70 offen\n"
"v_mfma_f32_16x16x32_bf16 v[228:231], a[88:91], a[96:99], v[228:231]\n"
"ds_read_b128 a[96:99], v12 offset:16384\n"
"v_mfma_f32_16x16x32_bf16 v[232:235], a[88:91], a[104:107], v[232:235]\n"
"ds_read_b64_tr_b16 a[36:37], v9 offset:12288\n"
"v_mfma_f32_16x16x32_bf16 v[232:235], a[92:95], a[108:111], v[232:235]\n"
"ds_read_b64_tr_b16 a[38:39], v9 offset:12800\n"
"v_mfma_f32_16x16x32_bf16 v[204:207], a[84:87], a[108:111], v[204:207]\n"
"ds_read_b128 a[108:111], v13 offset:18432\n"
"v_mfma_f32_16x16x32_bf16 v[204:207], a[80:83], a[104:107], v[204:207]\n"
"ds_read_b128 a[104:107], v12 offset:18432\n"
"ds_read_b64_tr_b16 a[40:41], v9 offset:8200\n"
"v_mfma_f32_16x16x32_bf16 v[208:211], a[80:83], a[112:115], v[208:211]\n"
"s_add_u32 m0, s64, 8192\n"
"ds_read_b64_tr_b16 a[42:43], v9 offset:8712\n"
"v_mfma_f32_16x16x32_bf16 v[208:211], a[84:87], a[116:119], v[208:211]\n"
"ds_read_b64_tr_b16 a[44:45], v9 offset:12296\n"
"v_mfma_f32_16x16x32_bf16 v[236:239], a[92:95], a[116:119], v[236:239]\n"
"ds_read_b128 a[116:119], v13 offset:20480\n"
"v_mfma_f32_16x16x32_bf16 v[236:239], a[88:91], a[112:115], v[236:239]\n"
"ds_read_b128 a[112:115], v12 offset:20480\n"
"v_mfma_f32_16x16x32_bf16 v[240:243], a[88:91], a[120:123], v[240:243]\n"
"ds_read_b64_tr_b16 a[46:47], v9 offset:12808\n"
"v_mfma_f32_16x16x32_bf16 v[240:243], a[92:95], a[124:127], v[240:243]\n"
"L_st1064:\n"
"s_cbranch_execz L_st3245\n"
"ds_read_b64_tr_b16 a[48:49], v15 offset:8192\n"
"v_mfma_f32_16x16x32_bf16 v[212:215], a[84:87], a[124:127], v[212:215]\n"
"ds_read_b128 a[124:127], v13 offset:22528\n"
"v_mfma_f32_16x16x32_bf16 v[212:215], a[80:83], a[120:123], v[212:215]\n"
"ds_read_b128 a[120:123], v12 offset:22528\n"
"v_mfma_f32_16x16x32_bf16 v[216:219], a[80:83], a[128:131], v[216:219]\n"
"ds_read_b64_tr_b16 a[50:51], v15 offset:8704\n"
"v_mfma_f32_16x16x32_bf16 v[216:219], a[84:87], a[132:135], v[216:219]\n"
"ds_read_b64_tr_b16 a[52:53], v15 offset:12288\n"
"v_mfma_f32_16x16x32_bf16 v[244:247], a[92:95], a[132:135], v[244:247]\n"
"ds_read_b128 a[132:135], v13 offset:24576\n"
"v_mfma_f32_16x16x32_bf16 v[244:247], a[88:91], a[128:131], v[244:247]\n"
"s_add_u32 s67, s75, 524288\n"
"ds_read_b128 a[128:131], v12 offset:24576\n"
"v_mfma_f32_16x16x32_bf16 v[248:251], a[88:91], a[136:139], v[248:251]\n"
"ds_read_b64_tr_b16 a[54:55], v15 offset:12800\n"
"v_mfma_f32_16x16x32_bf16 v[248:251], a[92:95], a[140:143], v[248:251]\n"
"ds_read_b64_tr_b16 a[56:57], v15 offset:8200\n"
"v_mfma_f32_16x16x32_bf16 v[220:223], a[84:87], a[140:143], v[220:223]\n"
"ds_read_b128 a[140:143], v13 offset:26624\n"
"v_mfma_f32_16x16x32_bf16 v[220:223], a[80:83], a[136:139], v[220:223]\n"
"ds_read_b128 a[136:139], v12 offset:26624\n"
"v_mfma_f32_16x16x32_bf16 v[224:227], a[80:83], a[144:147], v[224:227]\n"
"ds_read_b64_tr_b16 a[58:59], v15 offset:8712\n"
"v_mfma_f32_16x16x32_bf16 v[224:227], a[84:87], a[148:151], v[224:227]\n"
"ds_read_b64_tr_b16 a[60:61], v15 offset:12296\n"
"v_mfma_f32_16x16x32_bf16 v[252:255], a[92:95], a[148:151], v[252:255]\n"
"ds_read_b128 a[148:151], v13 offset:28672\n"
"v_mfma_f32_16x16x32_bf16 v[252:255], a[88:91], a[144:147], v[252:255]\n"
"ds_read_b128 a[144:147], v12 offset:28672\n"
"ds_read_b64_tr_b16 a[62:63], v15 offset:12808\n"
"v_mfma_f32_16x16x32_bf16 a[28:31], a[88:91], a[152:155], a[28:31]\n"
"v_mfma_f32_16x16x32_bf16 a[28:31], a[92:95], a[156:159], a[28:31]\n"
"v_mfma_f32_16x16x32_bf16 a[24:27], a[84:87], a[156:159], a[24:27]\n"
"ds_read_b128 a[156:159], v13 offset:30720\n"
"v_mfma_f32_16x16x32_bf16 a[24:27], a[80:83], a[152:155], a[24:27]\n"
"s_waitcnt lgkmcnt(15)\n"
"ds_read_b128 a[152:155], v12 offset:30720\n"
"v_mfma_f32_16x16x32_bf16 v[32:35], a[32:35], a[96:99], v[32:35]\n"
"v_mfma_f32_16x16x32_bf16 v[32:35], a[36:39], a[100:103], v[32:35]\n"
"s_waitcnt lgkmcnt(2)\n"
"v_mfma_f32_16x16x32_bf16 v[60:63], a[44:47], a[100:103], v[60:63]\n"
"buffer_load_dwordx4 v11, s[52:55], s67 offen offset:0  lds\n"
"v_mfma_f32_16x16x32_bf16 v[60:63], a[40:43], a[96:99], v[60:63]\n"
"v_mfma_f32_16x16x32_bf16 v[64:67], a[40:43], a[104:107], v[64:67]\n"
"s_add_u32 s67, s75, 539648\n"
"v_mfma_f32_16x16x32_bf16 v[64:67], a[44:47], a[108:111], v[64:67]\n"
"v_mfma_f32_16x16x32_bf16 v[36:39], a[36:39], a[108:111], v[36:39]\n"
"buffer_load_dwordx4 v11, s[52:55], s67 offen offset:1024  lds\n"
"v_mfma_f32_16x16x32_bf16 v[36:39], a[32:35], a[104:107], v[36:39]\n"
"v_mfma_f32_16x16x32_bf16 v[40:43], a[32:35], a[112:115], v[40:43]\n"
"s_add_u32 s67, s75, 555008\n"
"v_mfma_f32_16x16x32_bf16 v[40:43], a[36:39], a[116:119], v[40:43]\n"
"v_mfma_f32_16x16x32_bf16 v[68:71], a[44:47], a[116:119], v[68:71]\n"
"v_mfma_f32_16x16x32_bf16 v[68:71], a[40:43], a[112:115], v[68:71]\n"
"buffer_load_dwordx4 v11, s[52:55], s67 offen offset:2048  lds\n"
"v_mfma_f32_16x16x32_bf16 v[72:75], a[40:43], a[120:123], v[72:75]\n"
"v_mfma_f32_16x16x32_bf16 v[72:75], a[44:47], a[124:127], v[72:75]\n"
"s_add_u32 s67, s75, 570368\n"
"v_mfma_f32_16x16x32_bf16 v[44:47], a[36:39], a[124:127], v[44:47]\n"
"v_mfma_f32_16x16x32_bf16 v[44:47], a[32:35], a[120:123], v[44:47]\n"
"buffer_load_dwordx4 v11, s[52:55], s67 offen offset:3072  lds\n"
"v_mfma_f32_16x16x32_bf16 v[48:51], a[32:35], a[128:131], v[48:51]\n"
"v_mfma_f32_16x16x32_bf16 v[48:51], a[36:39], a[132:135], v[48:51]\n"
"s_add_u32 m0, s64, 12288\n"
"v_mfma_f32_16x16x32_bf16 v[76:79], a[44:47], a[132:135], v[76:79]\n"
"s_waitcnt vmcnt(9)\n"
"ds_read_b64_tr_b16 a[64:65], v9 offset:0\n"
"v_mfma_f32_16x16x32_bf16 v[76:79], a[40:43], a[128:131], v[76:79]\n"
"s_add_u32 s67, s75, 589824\n"
"ds_read_b64_tr_b16 a[66:67], v9 offset:512\n"
"v_mfma_f32_16x16x32_bf16 v[80:83], a[40:43], a[136:139], v[80:83]\n"
"ds_read_b64_tr_b16 a[68:69], v9 offset:4096\n"
"v_mfma_f32_16x16x32_bf16 v[80:83], a[44:47], a[140:143], v[80:83]\n"
"ds_read_b64_tr_b16 a[70:71], v9 offset:4608\n"
"buffer_load_dwordx4 v11, s[52:55], s67 offen offset:0  lds\n"
"v_mfma_f32_16x16x32_bf16 v[52:55], a[36:39], a[140:143], v[52:55]\n"
"s_add_u32 s67, s75, 605184\n"
"ds_read_b64_tr_b16 a[72:73], v9 offset:8\n"
"v_mfma_f32_16x16x32_bf16 v[52:55], a[32:35], a[136:139], v[52:55]\n"
"s_waitcnt lgkmcnt(5)\n"
"ds_read_b64_tr_b16 a[74:75], v9 offset:520\n"
"v_mfma_f32_16x16x32_bf16 v[56:59], a[32:35], a[144:147], v[56:59]\n"
"ds_read_b64_tr_b16 a[76:77], v9 offset:4104\n"
"v_mfma_f32_16x16x32_bf16 v[56:59], a[36:39], a[148:151], v[56:59]\n"
"ds_read_b64_tr_b16 a[78:79], v9 offset:4616\n"
"v_mfma_f32_16x16x32_bf16 v[84:87], a[44:47], a[148:151], v[84:87]\n"
"buffer_load_dwordx4 v11, s[52:55], s67 offen offset:1024  lds\n"
"v_mfma_f32_16x16x32_bf16 v[84:87], a[40:43], a[144:147], v[84:87]\n"
"ds_read_b64_tr_b16 a[80:81], v15 offset:0\n"
"v_mfma_f32_16x16x32_bf16 a[4:7], a[40:43], a[152:155], a[4:7]\n"
"s_add_u32 s67, s75, 620544\n"
"ds_read_b64_tr_b16 a[82:83], v15 offset:512\n"
"v_mfma_f32_16x16x32_bf16 a[4:7], a[44:47], a[156:159], a[4:7]\n"
"ds_read_b64_tr_b16 a[84:85], v15 offset:4096\n"
"v_mfma_f32_16x16x32_bf16 a[0:3], a[36:39], a[156:159], a[0:3]\n"
"ds_read_b64_tr_b16 a[86:87], v15 offset:4608\n"
"buffer_load_dwordx4 v11, s[52:55], s67 offen offset:2048  lds\n"
"v_mfma_f32_16x16x32_bf16 a[0:3], a[32:35], a[152:155], a[0:3]\n"
"ds_read_b64_tr_b16 a[88:89], v15 offset:8\n"
"v_mfma_f32_16x16x32_bf16 v[88:91], a[48:51], a[96:99], v[88:91]\n"
"s_add_u32 s67, s75, 635904\n"
"ds_read_b64_tr_b16 a[90:91], v15 offset:520\n"
"v_mfma_f32_16x16x32_bf16 v[88:91], a[52:55], a[100:103], v[88:91]\n"
"ds_read_b64_tr_b16 a[92:93], v15 offset:4104\n"
"v_mfma_f32_16x16x32_bf16 v[116:119], a[60:63], a[100:103], v[116:119]\n"
"ds_read_b64_tr_b16 a[94:95], v15 offset:4616\n"
"v_mfma_f32_16x16x32_bf16 v[116:119], a[56:59], a[96:99], v[116:119]\n"
"buffer_load_dwordx4 v11, s[52:55], s67 offen offset:3072  lds\n"
"v_mfma_f32_16x16x32_bf16 v[120:123], a[56:59], a[104:107], v[120:123]\n"
"v_mfma_f32_16x16x32_bf16 v[120:123], a[60:63], a[108:111], v[120:123]\n"
"s_add_u32 m0, s64, 0\n"
"v_mfma_f32_16x16x32_bf16 v[92:95], a[52:55], a[108:111], v[92:95]\n"
"v_mfma_f32_16x16x32_bf16 v[92:95], a[48:51], a[104:107], v[92:95]\n"
"s_add_u32 s67, s75, 524416\n"
"v_mfma_f32_16x16x32_bf16 v[96:99], a[48:51], a[112:115], v[96:99]\n"
"s_waitcnt lgkmcnt(0)\n"
"v_mfma_f32_16x16x32_bf16 v[96:99], a[52:55], a[116:119], v[96:99]\n"
"buffer_load_dwordx4 v11, s[52:55], s67 offen offset:0  lds\n"
"v_mfma_f32_16x16x32_bf16 v[124:127], a[60:63], a[116:119], v[124:127]\n"
"v_mfma_f32_16x16x32_bf16 v[124:127], a[56:59], a[112:115], v[124:127]\n"
"s_add_u32 s67, s75, 539776\n"
"v_mfma_f32_16x16x32_bf16 v[128:131], a[56:59], a[120:123], v[128:131]\n"
"v_mfma_f32_16x16x32_bf16 v[128:131], a[60:63], a[124:127], v[128:131]\n"
"v_mfma_f32_16x16x32_bf16 v[100:103], a[52:55], a[124:127], v[100:103]\n"
"buffer_load_dwordx4 v11, s[52:55], s67 offen offset:1024  lds\n"
"v_mfma_f32_16x16x32_bf16 v[100:103], a[48:51], a[120:123], v[100:103]\n"
"v_mfma_f32_16x16x32_bf16 v[104:107], a[48:51], a[128:131], v[104:107]\n"
"s_add_u32 s67, s75, 555136\n"
"v_mfma_f32_16x16x32_bf16 v[104:107], a[52:55], a[132:135], v[104:107]\n"
"v_mfma_f32_16x16x32_bf16 v[132:135], a[60:63], a[132:135], v[132:135]\n"
"v_mfma_f32_16x16x32_bf16 v[132:135], a[56:59], a[128:131], v[132:135]\n"
"buffer_load_dwordx4 v11, s[52:55], s67 offen offset:2048  lds\n"
"v_mfma_f32_16x16x32_bf16 v[136:139], a[56:59], a[136:139], v[136:139]\n"
"v_mfma_f32_16x16x32_bf16 v[136:139], a[60:63], a[140:143], v[136:139]\n"
"s_add_u32 s67, s75, 570496\n"
"v_mfma_f32_16x16x32_bf16 v[108:111], a[52:55], a[140:143], v[108:111]\n"
"v_mfma_f32_16x16x32_bf16 v[108:111], a[48:51], a[136:139], v[108:111]\n"
"buffer_load_dwordx4 v11, s[52:55], s67 offen offset:3072  lds\n"
"v_mfma_f32_16x16x32_bf16 v[112:115], a[48:51], a[144:147], v[112:115]\n"
"v_mfma_f32_16x16x32_bf16 v[112:115], a[52:55], a[148:151], v[112:115]\n"
"s_add_u32 m0, s64, 4096\n"
"v_mfma_f32_16x16x32_bf16 v[140:143], a[60:63], a[148:151], v[140:143]\n"
"v_mfma_f32_16x16x32_bf16 v[140:143], a[56:59], a[144:147], v[140:143]\n"
"s_add_u32 s67, s75, 589952\n"
"v_mfma_f32_16x16x32_bf16 a[12:15], a[56:59], a[152:155], a[12:15]\n"
"v_mfma_f32_16x16x32_bf16 a[12:15], a[60:63], a[156:159], a[12:15]\n"
"buffer_load_dwordx4 v11, s[52:55], s67 offen offset:0  lds\n"
"v_mfma_f32_16x16x32_bf16 a[8:11], a[52:55], a[156:159], a[8:11]\n"
"v_mfma_f32_16x16x32_bf16 a[8:11], a[48:51], a[152:155], a[8:11]\n"
"s_add_u32 s67, s75, 605312\n"
"v_mfma_f32_16x16x32_bf16 v[144:147], a[64:67], a[96:99], v[144:147]\n"
"v_mfma_f32_16x16x32_bf16 v[144:147], a[68:71], a[100:103], v[144:147]\n"
"buffer_load_dwordx4 v11, s[52:55], s67 offen offset:1024  lds\n"
"v_mfma_f32_16x16x32_bf16 v[172:175], a[76:79], a[100:103], v[172:175]\n"
"v_mfma_f32_16x16x32_bf16 v[172:175], a[72:75], a[96:99], v[172:175]\n"
"s_add_u32 s67, s75, 620672\n"
"v_mfma_f32_16x16x32_bf16 v[176:179], a[72:75], a[104:107], v[176:179]\n"
"v_mfma_f32_16x16x32_bf16 v[176:179], a[76:79], a[108:111], v[176:179]\n"
"buffer_load_dwordx4 v11, s[52:55], s67 offen offset:2048  lds\n"
"v_mfma_f32_16x16x32_bf16 v[148:151], a[68:71], a[108:111], v[148:151]\n"
"v_mfma_f32_16x16x32_bf16 v[148:151], a[64:67], a[104:107], v[148:151]\n"
"s_add_u32 s67, s75, 636032\n"
"v_mfma_f32_16x16x32_bf16 v[152:155], a[64:67], a[112:115], v[152:155]\n"
"s_sub_u32 s66, 0, s73\n"
"v_mfma_f32_16x16x32_bf16 v[152:155], a[68:71], a[116:119], v[152:155]\n"
"v_mfma_f32_16x16x32_bf16 v[180:183], a[76:79], a[116:119], v[180:183]\n"
"v_add_u32 v12, s66, v7\n"
"v_mfma_f32_16x16x32_bf16 v[180:183], a[72:75], a[112:115], v[180:183]\n"
"buffer_load_dwordx4 v11, s[52:55], s67 offen offset:3072  lds\n"
"v_mfma_f32_16x16x32_bf16 v[184:187], a[72:75], a[120:123], v[184:187]\n"
"s_add_u32 m0, s72, 57344\n"
"v_add_u32 v13, s66, v8\n"
"v_mfma_f32_16x16x32_bf16 v[184:187], a[76:79], a[124:127], v[184:187]\n"
"v_mfma_f32_16x16x32_bf16 v[156:159], a[68:71], a[124:127], v[156:159]\n"
"s_add_u32 s67, s60, 640\n"
"v_mfma_f32_16x16x32_bf16 v[156:159], a[64:67], a[120:123], v[156:159]\n"
"buffer_load_dwordx4 v6, s[48:51], s67 offen offset:0  lds\n"
"v_mfma_f32_16x16x32_bf16 v[160:163], a[64:67], a[128:131], v[160:163]\n"
"v_mfma_f32_16x16x32_bf16 v[160:163], a[68:71], a[132:135], v[160:163]\n"
"s_add_u32 s67, s60, 16000\n"
"v_mfma_f32_16x16x32_bf16 v[188:191], a[76:79], a[132:135], v[188:191]\n"
"v_mfma_f32_16x16x32_bf16 v[188:191], a[72:75], a[128:131], v[188:191]\n"
"buffer_load_dwordx4 v6, s[48:51], s67 offen offset:1024  lds\n"
"v_mfma_f32_16x16x32_bf16 v[192:195], a[72:75], a[136:139], v[192:195]\n"
"v_mfma_f32_16x16x32_bf16 v[192:195], a[76:79], a[140:143], v[192:195]\n"
"s_add_u32 s67, s60, 31360\n"
"v_mfma_f32_16x16x32_bf16 v[164:167], a[68:71], a[140:143], v[164:167]\n"
"v_mfma_f32_16x16x32_bf16 v[164:167], a[64:67], a[136:139], v[164:167]\n"
"buffer_load_dwordx4 v6, s[48:51], s67 offen offset:2048  lds\n"
"v_mfma_f32_16x16x32_bf16 v[168:171], a[64:67], a[144:147], v[168:171]\n"
"v_mfma_f32_16x16x32_bf16 v[168:171], a[68:71], a[148:151], v[168:171]\n"
"s_add_u32 s67, s60, 46720\n"
"v_mfma_f32_16x16x32_bf16 v[196:199], a[76:79], a[148:151], v[196:199]\n"
"v_mfma_f32_16x16x32_bf16 v[196:199], a[72:75], a[144:147], v[196:199]\n"
"v_mfma_f32_16x16x32_bf16 a[20:23], a[72:75], a[152:155], a[20:23]\n"
"buffer_load_dwordx4 v6, s[48:51], s67 offen offset:3072  lds\n"
"v_mfma_f32_16x16x32_bf16 a[20:23], a[76:79], a[156:159], a[20:23]\n"
"s_sub_u32 s70, 1048576, s75\n"
"v_and_b32 v19, 0x20000, v14\n"
"v_mfma_f32_16x16x32_bf16 a[16:19], a[68:71], a[156:159], a[16:19]\n"
"v_add_u32 v14, v19, v14\n"
"v_mfma_f32_16x16x32_bf16 a[16:19], a[64:67], a[152:155], a[16:19]\n"
"s_waitcnt vmcnt(21)\n"
"s_barrier\n"
"v_mfma_f32_16x16x32_bf16 v[200:203], a[80:83], a[96:99], v[200:203]\n"
"s_waitcnt vmcnt(12)\n"
"ds_read_b64_tr_b16 a[32:33], v9 offset:8192\n"
"v_mfma_f32_16x16x32_bf16 v[200:203], a[84:87], a[100:103], v[200:203]\n"
"s_add_u32 s70, s70, s65\n"
"ds_read_b64_tr_b16 a[34:35], v9 offset:8704\n"
"s_getpc_b64 s[96:97]\n"
"L_c10_:\n"
"s_add_u32 s96, s96, L_H6S-L_c10_\n"
"s_addc_u32 s97, s97, 0\n"
"v_mfma_f32_16x16x32_bf16 v[228:231], a[92:95], a[100:103], v[228:231]\n"
"s_lshr_b32 s66, %26, 3\n"
"ds_read_b128 a[100:103], v13 offset:16384\n"
"buffer_load_dword a252, v14, s[52:55], s70 offen\n"
"v_mfma_f32_16x16x32_bf16 v[228:231], a[88:91], a[96:99], v[228:231]\n"
"s_lshl2_add_u32 s66, s66, %16\n"
"ds_read_b128 a[96:99], v12 offset:16384\n"
"v_mfma_f32_16x16x32_bf16 v[232:235], a[88:91], a[104:107], v[232:235]\n"
"s_mul_i32 s66, s66, 128\n"
"ds_read_b64_tr_b16 a[36:37], v9 offset:12288\n"
"v_mfma_f32_16x16x32_bf16 v[232:235], a[92:95], a[108:111], v[232:235]\n"
"s_min_u32 s66, s66, L_headF-L_H6S-128\n"
"ds_read_b64_tr_b16 a[38:39], v9 offset:12800\n"
"v_min_u32 v22, 0, v0\n"
"v_mfma_f32_16x16x32_bf16 v[204:207], a[84:87], a[108:111], v[204:207]\n"
"ds_read_b128 a[108:111], v13 offset:18432\n"
"v_lshl_add_u32 v22, v22, 3, s66\n"
"v_mfma_f32_16x16x32_bf16 v[204:207], a[80:83], a[104:107], v[204:207]\n"
"ds_read_b128 a[104:107], v12 offset:18432\n"
"global_load_dword v23, v22, s[96:97]\n"
"ds_read_b64_tr_b16 a[40:41], v9 offset:8200\n"
"v_mfma_f32_16x16x32_bf16 v[208:211], a[80:83], a[112:115], v[208:211]\n"
"s_add_u32 m0, s64, 8192\n"
"ds_read_b64_tr_b16 a[42:43], v9 offset:8712\n"
"v_mfma_f32_16x16x32_bf16 v[208:211], a[84:87], a[116:119], v[208:211]\n"
"ds_read_b64_tr_b16 a[44:45], v9 offset:12296\n"
"v_mfma_f32_16x16x32_bf16 v[236:239], a[92:95], a[116:119], v[236:239]\n"
"ds_read_b128 a[116:119], v13 offset:20480\n"
"v_mfma_f32_16x16x32_bf16 v[236:239], a[88:91], a[112:115], v[236:239]\n"
"ds_read_b128 a[112:115], v12 offset:20480\n"
"v_mfma_f32_16x16x32_bf16 v[240:243], a[88:91], a[120:123], v[240:243]\n"
"ds_read_b64_tr_b16 a[46:47], v9 offset:12808\n"
"v_mfma_f32_16x16x32_bf16 v[240:243], a[92:95], a[124:127], v[240:243]\n"
"ds_read_b64_tr_b16 a[48:49], v15 offset:8192\n"
"v_mfma_f32_16x16x32_bf16 v[212:215], a[84:87], a[124:127], v[212:215]\n"
"ds_read_b128 a[124:127], v13 offset:22528\n"
"v_mfma_f32_16x16x32_bf16 v[212:215], a[80:83], a[120:123], v[212:215]\n"
"ds_read_b128 a[120:123], v12 offset:22528\n"
"v_mfma_f32_16x16x32_bf16 v[216:219], a[80:83], a[128:131], v[216:219]\n"
"ds_read_b64_tr_b16 a[50:51], v15 offset:8704\n"
"v_mfma_f32_16x16x32_bf16 v[216:219], a[84:87], a[132:135], v[216:219]\n"
"ds_read_b64_tr_b16 a[52:53], v15 offset:12288\n"
"v_mfma_f32_16x16x32_bf16 v[244:247], a[92:95], a[132:135], v[244:247]\n"
"ds_read_b128 a[132:135], v13 offset:24576\n"
"v_mfma_f32_16x16x32_bf16 v[244:247], a[88:91], a[128:131], v[244:247]\n"
"s_add_u32 s67, s75, 655360\n"
"ds_read_b128 a[128:131], v12 offset:24576\n"
"v_mfma_f32_16x16x32_bf16 v[248:251], a[88:91], a[136:139], v[248:251]\n"
"ds_read_b64_tr_b16 a[54:55], v15 offset:12800\n"
"v_mfma_f32_16x16x32_bf16 v[248:251], a[92:95], a[140:143], v[248:251]\n"
"ds_read_b64_tr_b16 a[56:57], v15 offset:8200\n"
"v_mfma_f32_16x16x32_bf16 v[220:223], a[84:87], a[140:143], v[220:223]\n"
"ds_read_b128 a[140:143], v13 offset:26624\n"
"v_mfma_f32_16x16x32_bf16 v[220:223], a[80:83], a[136:139], v[220:223]\n"
"ds_read_b128 a[136:139], v12 offset:26624\n"
"v_mfma_f32_16x16x32_bf16 v[224:227], a[80:83], a[144:147], v[224:227]\n"
"ds_read_b64_tr_b16 a[58:59], v15 offset:8712\n"
"v_mfma_f32_16x16x32_bf16 v[224:227], a[84:87], a[148:151], v[224:227]\n"
"ds_read_b64_tr_b16 a[60:61], v15 offset:12296\n"
"v_mfma_f32_16x16x32_bf16 v[252:255], a[92:95], a[148:151], v[252:255]\n"
"ds_read_b128 a[148:151], v13 offset:28672\n"
"v_mfma_f32_16x16x32_bf16 v[252:255], a[88:91], a[144:147], v[252:255]\n"
"ds_read_b128 a[144:147], v12 offset:28672\n"
"ds_read_b64_tr_b16 a[62:63], v15 offset:12808\n"
"v_mfma_f32_16x16x32_bf16 a[28:31], a[88:91], a[152:155], a[28:31]\n"
"v_mfma_f32_16x16x32_bf16 a[28:31], a[92:95], a[156:159], a[28:31]\n"
"v_mfma_f32_16x16x32_bf16 a[24:27], a[84:87], a[156:159], a[24:27]\n"
"ds_read_b128 a[156:159], v13 offset:30720\n"
"v_mfma_f32_16x16x32_bf16 a[24:27], a[80:83], a[152:155], a[24:27]\n"
"s_waitcnt lgkmcnt(15)\n"
"ds_read_b128 a[152:155], v12 offset:30720\n"
"v_mfma_f32_16x16x32_bf16 v[32:35], a[32:35], a[96:99], v[32:35]\n"
"v_mfma_f32_16x16x32_bf16 v[32:35], a[36:39], a[100:103], v[32:35]\n"
"s_waitcnt lgkmcnt(2)\n"
"v_mfma_f32_16x16x32_bf16 v[60:63], a[44:47], a[100:103], v[60:63]\n"
"buffer_load_dwordx4 v11, s[52:55], s67 offen offset:0  lds\n"
"v_mfma_f32_16x16x32_bf16 v[60:63], a[40:43], a[96:99], v[60:63]\n"
"v_mfma_f32_16x16x32_bf16 v[64:67], a[40:43], a[104:107], v[64:67]\n"
"s_add_u32 s67, s75, 670720\n"
"v_mfma_f32_16x16x32_bf16 v[64:67], a[44:47], a[108:111], v[64:67]\n"
"v_mfma_f32_16x16x32_bf16 v[36:39], a[36:39], a[108:111], v[36:39]\n"
"buffer_load_dwordx4 v11, s[52:55], s67 offen offset:1024  lds\n"
"v_mfma_f32_16x16x32_bf16 v[36:39], a[32:35], a[104:107], v[36:39]\n"
"v_mfma_f32_16x16x32_bf16 v[40:43], a[32:35], a[112:115], v[40:43]\n"
"s_add_u32 s67, s75, 686080\n"
"v_mfma_f32_16x16x32_bf16 v[40:43], a[36:39], a[116:119], v[40:43]\n"
"v_mfma_f32_16x16x32_bf16 v[68:71], a[44:47], a[116:119], v[68:71]\n"
"v_mfma_f32_16x16x32_bf16 v[68:71], a[40:43], a[112:115], v[68:71]\n"
"buffer_load_dwordx4 v11, s[52:55], s67 offen offset:2048  lds\n"
"v_mfma_f32_16x16x32_bf16 v[72:75], a[40:43], a[120:123], v[72:75]\n"
"v_mfma_f32_16x16x32_bf16 v[72:75], a[44:47], a[124:127], v[72:75]\n"
"s_add_u32 s67, s75, 701440\n"
"v_mfma_f32_16x16x32_bf16 v[44:47], a[36:39], a[124:127], v[44:47]\n"
"v_mfma_f32_16x16x32_bf16 v[44:47], a[32:35], a[120:123], v[44:47]\n"
"buffer_load_dwordx4 v11, s[52:55], s67 offen offset:3072  lds\n"
"v_mfma_f32_16x16x32_bf16 v[48:51], a[32:35], a[128:131], v[48:51]\n"
"v_mfma_f32_16x16x32_bf16 v[48:51], a[36:39], a[132:135], v[48:51]\n"
"s_add_u32 m0, s64, 12288\n"
"v_mfma_f32_16x16x32_bf16 v[76:79], a[44:47], a[132:135], v[76:79]\n"
"s_waitcnt vmcnt(10)\n"
"ds_read_b64_tr_b16 a[64:65], v9 offset:0\n"
"v_mfma_f32_16x16x32_bf16 v[76:79], a[40:43], a[128:131], v[76:79]\n"
"s_add_u32 s67, s75, 720896\n"
"ds_read_b64_tr_b16 a[66:67], v9 offset:512\n"
"v_mfma_f32_16x16x32_bf16 v[80:83], a[40:43], a[136:139], v[80:83]\n"
"ds_read_b64_tr_b16 a[68:69], v9 offset:4096\n"
"v_mfma_f32_16x16x32_bf16 v[80:83], a[44:47], a[140:143], v[80:83]\n"
"ds_read_b64_tr_b16 a[70:71], v9 offset:4608\n"
"buffer_load_dwordx4 v11, s[52:55], s67 offen offset:0  lds\n"
"v_mfma_f32_16x16x32_bf16 v[52:55], a[36:39], a[140:143], v[52:55]\n"
"s_add_u32 s67, s75, 736256\n"
"ds_read_b64_tr_b16 a[72:73], v9 offset:8\n"
"v_mfma_f32_16x16x32_bf16 v[52:55], a[32:35], a[136:139], v[52:55]\n"
"s_waitcnt lgkmcnt(5)\n"
"ds_read_b64_tr_b16 a[74:75], v9 offset:520\n"
"v_mfma_f32_16x16x32_bf16 v[56:59], a[32:35], a[144:147], v[56:59]\n"
"ds_read_b64_tr_b16 a[76:77], v9 offset:4104\n"
"v_mfma_f32_16x16x32_bf16 v[56:59], a[36:39], a[148:151], v[56:59]\n"
"ds_read_b64_tr_b16 a[78:79], v9 offset:4616\n"
"v_mfma_f32_16x16x32_bf16 v[84:87], a[44:47], a[148:151], v[84:87]\n"
"buffer_load_dwordx4 v11, s[52:55], s67 offen offset:1024  lds\n"
"v_mfma_f32_16x16x32_bf16 v[84:87], a[40:43], a[144:147], v[84:87]\n"
"ds_read_b64_tr_b16 a[80:81], v15 offset:0\n"
"v_mfma_f32_16x16x32_bf16 a[4:7], a[40:43], a[152:155], a[4:7]\n"
"s_add_u32 s67, s75, 751616\n"
"ds_read_b64_tr_b16 a[82:83], v15 offset:512\n"
"v_mfma_f32_16x16x32_bf16 a[4:7], a[44:47], a[156:159], a[4:7]\n"
"ds_read_b64_tr_b16 a[84:85], v15 offset:4096\n"
"v_mfma_f32_16x16x32_bf16 a[0:3], a[36:39], a[156:159], a[0:3]\n"
"ds_read_b64_tr_b16 a[86:87], v15 offset:4608\n"
"buffer_load_dwordx4 v11, s[52:55], s67 offen offset:2048  lds\n"
"v_mfma_f32_16x16x32_bf16 a[0:3], a[32:35], a[152:155], a[0:3]\n"
"ds_read_b64_tr_b16 a[88:89], v15 offset:8\n"
"v_mfma_f32_16x16x32_bf16 v[88:91], a[48:51], a[96:99], v[88:91]\n"
"s_add_u32 s67, s75, 766976\n"
"ds_read_b64_tr_b16 a[90:91], v15 offset:520\n"
"v_mfma_f32_16x16x32_bf16 v[88:91], a[52:55], a[100:103], v[88:91]\n"
"ds_read_b64_tr_b16 a[92:93], v15 offset:4104\n"
"v_mfma_f32_16x16x32_bf16 v[116:119], a[60:63], a[100:103], v[116:119]\n"
"ds_read_b64_tr_b16 a[94:95], v15 offset:4616\n"
"v_mfma_f32_16x16x32_bf16 v[116:119], a[56:59], a[96:99], v[116:119]\n"
"buffer_load_dwordx4 v11, s[52:55], s67 offen offset:3072  lds\n"
"v_mfma_f32_16x16x32_bf16 v[120:123], a[56:59], a[104:107], v[120:123]\n"
"v_mfma_f32_16x16x32_bf16 v[120:123], a[60:63], a[108:111], v[120:123]\n"
"s_add_u32 m0, s64, 0\n"
"v_mfma_f32_16x16x32_bf16 v[92:95], a[52:55], a[108:111], v[92:95]\n"
"v_mfma_f32_16x16x32_bf16 v[92:95], a[48:51], a[104:107], v[92:95]\n"
"s_add_u32 s67, s75, 655488\n"
"v_mfma_f32_16x16x32_bf16 v[96:99], a[48:51], a[112:115], v[96:99]\n"
"s_waitcnt lgkmcnt(0)\n"
"v_mfma_f32_16x16x32_bf16 v[96:99], a[52:55], a[116:119], v[96:99]\n"
"buffer_load_dwordx4 v11, s[52:55], s67 offen offset:0  lds\n"
"v_mfma_f32_16x16x32_bf16 v[124:127], a[60:63], a[116:119], v[124:127]\n"
"v_mfma_f32_16x16x32_bf16 v[124:127], a[56:59], a[112:115], v[124:127]\n"
"s_add_u32 s67, s75, 670848\n"
"v_mfma_f32_16x16x32_bf16 v[128:131], a[56:59], a[120:123], v[128:131]\n"
"v_mfma_f32_16x16x32_bf16 v[128:131], a[60:63], a[124:127], v[128:131]\n"
"v_mfma_f32_16x16x32_bf16 v[100:103], a[52:55], a[124:127], v[100:103]\n"
"buffer_load_dwordx4 v11, s[52:55], s67 offen offset:1024  lds\n"
"v_mfma_f32_16x16x32_bf16 v[100:103], a[48:51], a[120:123], v[100:103]\n"
"v_mfma_f32_16x16x32_bf16 v[104:107], a[48:51], a[128:131], v[104:107]\n"
"s_add_u32 s67, s75, 686208\n"
"v_mfma_f32_16x16x32_bf16 v[104:107], a[52:55], a[132:135], v[104:107]\n"
"v_mfma_f32_16x16x32_bf16 v[132:135], a[60:63], a[132:135], v[132:135]\n"
"v_mfma_f32_16x16x32_bf16 v[132:135], a[56:59], a[128:131], v[132:135]\n"
"buffer_load_dwordx4 v11, s[52:55], s67 offen offset:2048  lds\n"
"v_mfma_f32_16x16x32_bf16 v[136:139], a[56:59], a[136:139], v[136:139]\n"
"v_mfma_f32_16x16x32_bf16 v[136:139], a[60:63], a[140:143], v[136:139]\n"
"s_add_u32 s67, s75, 701568\n"
"v_mfma_f32_16x16x32_bf16 v[108:111], a[52:55], a[140:143], v[108:111]\n"
"v_mfma_f32_16x16x32_bf16 v[108:111], a[48:51], a[136:139], v[108:111]\n"
"buffer_load_dwordx4 v11, s[52:55], s67 offen offset:3072  lds\n"
"v_mfma_f32_16x16x32_bf16 v[112:115], a[48:51], a[144:147], v[112:115]\n"
"v_mfma_f32_16x16x32_bf16 v[112:115], a[52:55], a[148:151], v[112:115]\n"
"s_add_u32 m0, s64, 4096\n"
"v_mfma_f32_16x16x32_bf16 v[140:143], a[60:63], a[148:151], v[140:143]\n"
"v_mfma_f32_16x16x32_bf16 v[140:143], a[56:59], a[144:147], v[140:143]\n"
"s_add_u32 s67, s75, 721024\n"
"v_mfma_f32_16x16x32_bf16 a[12:15], a[56:59], a[152:155], a[12:15]\n"
"v_mfma_f32_16x16x32_bf16 a[12:15], a[60:63], a[156:159], a[12:15]\n"
"buffer_load_dwordx4 v11, s[52:55], s67 offen offset:0  lds\n"
"v_mfma_f32_16x16x32_bf16 a[8:11], a[52:55], a[156:159], a[8:11]\n"
"v_mfma_f32_16x16x32_bf16 a[8:11], a[48:51], a[152:155], a[8:11]\n"
"s_add_u32 s67, s75, 736384\n"
"v_mfma_f32_16x16x32_bf16 v[144:147], a[64:67], a[96:99], v[144:147]\n"
"v_mfma_f32_16x16x32_bf16 v[144:147], a[68:71], a[100:103], v[144:147]\n"
"buffer_load_dwordx4 v11, s[52:55], s67 offen offset:1024  lds\n"
"v_mfma_f32_16x16x32_bf16 v[172:175], a[76:79], a[100:103], v[172:175]\n"
"v_mfma_f32_16x16x32_bf16 v[172:175], a[72:75], a[96:99], v[172:175]\n"
"s_add_u32 s67, s75, 751744\n"
"v_mfma_f32_16x16x32_bf16 v[176:179], a[72:75], a[104:107], v[176:179]\n"
"v_mfma_f32_16x16x32_bf16 v[176:179], a[76:79], a[108:111], v[176:179]\n"
"buffer_load_dwordx4 v11, s[52:55], s67 offen offset:2048  lds\n"
"v_mfma_f32_16x16x32_bf16 v[148:151], a[68:71], a[108:111], v[148:151]\n"
"v_mfma_f32_16x16x32_bf16 v[148:151], a[64:67], a[104:107], v[148:151]\n"
"s_add_u32 s67, s75, 767104\n"
"v_mfma_f32_16x16x32_bf16 v[152:155], a[64:67], a[112:115], v[152:155]\n"
"s_sub_u32 s66, 40960, s73\n"
"v_mfma_f32_16x16x32_bf16 v[152:155], a[68:71], a[116:119], v[152:155]\n"
"v_mfma_f32_16x16x32_bf16 v[180:183], a[76:79], a[116:119], v[180:183]\n"
"v_add_u32 v12, s66, v7\n"
"v_mfma_f32_16x16x32_bf16 v[180:183], a[72:75], a[112:115], v[180:183]\n"
"buffer_load_dwordx4 v11, s[52:55], s67 offen offset:3072  lds\n"
"v_mfma_f32_16x16x32_bf16 v[184:187], a[72:75], a[120:123], v[184:187]\n"
"s_add_u32 m0, s72, 98304\n"
"v_add_u32 v13, s66, v8\n"
"v_mfma_f32_16x16x32_bf16 v[184:187], a[76:79], a[124:127], v[184:187]\n"
"v_mfma_f32_16x16x32_bf16 v[156:159], a[68:71], a[124:127], v[156:159]\n"
"s_add_u32 s67, s60, 768\n"
"v_mfma_f32_16x16x32_bf16 v[156:159], a[64:67], a[120:123], v[156:159]\n"
"buffer_load_dwordx4 v6, s[48:51], s67 offen offset:0  lds\n"
"v_mfma_f32_16x16x32_bf16 v[160:163], a[64:67], a[128:131], v[160:163]\n"
"v_mfma_f32_16x16x32_bf16 v[160:163], a[68:71], a[132:135], v[160:163]\n"
"s_add_u32 s67, s60, 16128\n"
"v_mfma_f32_16x16x32_bf16 v[188:191], a[76:79], a[132:135], v[188:191]\n"
"v_mfma_f32_16x16x32_bf16 v[188:191], a[72:75], a[128:131], v[188:191]\n"
"buffer_load_dwordx4 v6, s[48:51], s67 offen offset:1024  lds\n"
"v_mfma_f32_16x16x32_bf16 v[192:195], a[72:75], a[136:139], v[192:195]\n"
"v_mfma_f32_16x16x32_bf16 v[192:195], a[76:79], a[140:143], v[192:195]\n"
"s_add_u32 s67, s60, 31488\n"
"v_mfma_f32_16x16x32_bf16 v[164:167], a[68:71], a[140:143], v[164:167]\n"
"v_mfma_f32_16x16x32_bf16 v[164:167], a[64:67], a[136:139], v[164:167]\n"
"buffer_load_dwordx4 v6, s[48:51], s67 offen offset:2048  lds\n"
"v_mfma_f32_16x16x32_bf16 v[168:171], a[64:67], a[144:147], v[168:171]\n"
"v_mfma_f32_16x16x32_bf16 v[168:171], a[68:71], a[148:151], v[168:171]\n"
"s_add_u32 s67, s60, 46848\n"
"v_mfma_f32_16x16x32_bf16 v[196:199], a[76:79], a[148:151], v[196:199]\n"
"v_mfma_f32_16x16x32_bf16 v[196:199], a[72:75], a[144:147], v[196:199]\n"
"v_mfma_f32_16x16x32_bf16 a[20:23], a[72:75], a[152:155], a[20:23]\n"
"buffer_load_dwordx4 v6, s[48:51], s67 offen offset:3072  lds\n"
"v_mfma_f32_16x16x32_bf16 a[20:23], a[76:79], a[156:159], a[20:23]\n"
"v_mfma_f32_16x16x32_bf16 a[16:19], a[68:71], a[156:159], a[16:19]\n"
"s_sub_u32 s70, 1572864, s75\n"
"s_add_u32 s70, s70, s65\n"
"v_mfma_f32_16x16x32_bf16 a[16:19], a[64:67], a[152:155], a[16:19]\n"
"s_waitcnt vmcnt(22)\n"
"s_barrier\n"
"v_mfma_f32_16x16x32_bf16 v[200:203], a[80:83], a[96:99], v[200:203]\n"
"s_waitcnt vmcnt(12)\n"
"ds_read_b64_tr_b16 a[32:33], v9 offset:8192\n"
"v_mfma_f32_16x16x32_bf16 v[200:203], a[84:87], a[100:103], v[200:203]\n"
"ds_read_b64_tr_b16 a[34:35], v9 offset:8704\n"
"v_mfma_f32_16x16x32_bf16 v[228:231], a[92:95], a[100:103], v[228:231]\n"
"ds_read_b128 a[100:103], v13 offset:16384\n"
"buffer_load_dword a252, v14, s[52:55], s70 offen\n"
"v_mfma_f32_16x16x32_bf16 v[228:231], a[88:91], a[96:99], v[228:231]\n"
"ds_read_b128 a[96:99], v12 offset:16384\n"
"v_mfma_f32_16x16x32_bf16 v[232:235], a[88:91], a[104:107], v[232:235]\n"
"buffer_store_dwordx2 v[16:17], v18, s[56:59], %11 offen sc1\n"
"ds_read_b64_tr_b16 a[36:37], v9 offset:12288\n"
"v_mfma_f32_16x16x32_bf16 v[232:235], a[92:95], a[108:111], v[232:235]\n"
"ds_read_b64_tr_b16 a[38:39], v9 offset:12800\n"
"v_mfma_f32_16x16x32_bf16 v[204:207], a[84:87], a[108:111], v[204:207]\n"
"ds_read_b128 a[108:111], v13 offset:18432\n"
"v_mfma_f32_16x16x32_bf16 v[204:207], a[80:83], a[104:107], v[204:207]\n"
"ds_read_b128 a[104:107], v12 offset:18432\n"
"buffer_store_dwordx2 v[16:17], v18, s[56:59], s69 offen sc1\n"
"ds_read_b64_tr_b16 a[40:41], v9 offset:8200\n"
"v_mfma_f32_16x16x32_bf16 v[208:211], a[80:83], a[112:115], v[208:211]\n"
"ds_read_b64_tr_b16 a[42:43], v9 offset:8712\n"
"v_mfma_f32_16x16x32_bf16 v[208:211], a[84:87], a[116:119], v[208:211]\n"
"ds_read_b64_tr_b16 a[44:45], v9 offset:12296\n"
"v_mfma_f32_16x16x32_bf16 v[236:239], a[92:95], a[116:119], v[236:239]\n"
"ds_read_b128 a[116:119], v13 offset:20480\n"
"v_mfma_f32_16x16x32_bf16 v[236:239], a[88:91], a[112:115], v[236:239]\n"
"ds_read_b128 a[112:115], v12 offset:20480\n"
"v_mfma_f32_16x16x32_bf16 v[240:243], a[88:91], a[120:123], v[240:243]\n"
"s_add_u32 m0, s64, 8192\n"
"ds_read_b64_tr_b16 a[46:47], v9 offset:12808\n"
"v_mfma_f32_16x16x32_bf16 v[240:243], a[92:95], a[124:127], v[240:243]\n"
"ds_read_b64_tr_b16 a[48:49], v15 offset:8192\n"
"v_mfma_f32_16x16x32_bf16 v[212:215], a[84:87], a[124:127], v[212:215]\n"
"ds_read_b128 a[124:127], v13 offset:22528\n"
"v_mfma_f32_16x16x32_bf16 v[212:215], a[80:83], a[120:123], v[212:215]\n"
"ds_read_b128 a[120:123], v12 offset:22528\n"
"v_mfma_f32_16x16x32_bf16 v[216:219], a[80:83], a[128:131], v[216:219]\n"
"ds_read_b64_tr_b16 a[50:51], v15 offset:8704\n"
"v_mfma_f32_16x16x32_bf16 v[216:219], a[84:87], a[132:135], v[216:219]\n"
"ds_read_b64_tr_b16 a[52:53], v15 offset:12288\n"
"v_mfma_f32_16x16x32_bf16 v[244:247], a[92:95], a[132:135], v[244:247]\n"
"ds_read_b128 a[132:135], v13 offset:24576\n"
"v_mfma_f32_16x16x32_bf16 v[244:247], a[88:91], a[128:131], v[244:247]\n"
"ds_read_b128 a[128:131], v12 offset:24576\n"
"v_mfma_f32_16x16x32_bf16 v[248:251], a[88:91], a[136:139], v[248:251]\n"
"ds_read_b64_tr_b16 a[54:55], v15 offset:12800\n"
"v_mfma_f32_16x16x32_bf16 v[248:251], a[92:95], a[140:143], v[248:251]\n"
"ds_read_b64_tr_b16 a[56:57], v15 offset:8200\n"
"v_mfma_f32_16x16x32_bf16 v[220:223], a[84:87], a[140:143], v[220:223]\n"
"s_add_u32 s67, s75, 786432\n"
"ds_read_b128 a[140:143], v13 offset:26624\n"
"v_mfma_f32_16x16x32_bf16 v[220:223], a[80:83], a[136:139], v[220:223]\n"
"ds_read_b128 a[136:139], v12 offset:26624\n"
"v_mfma_f32_16x16x32_bf16 v[224:227], a[80:83], a[144:147], v[224:227]\n"
"ds_read_b64_tr_b16 a[58:59], v15 offset:8712\n"
"v_mfma_f32_16x16x32_bf16 v[224:227], a[84:87], a[148:151], v[224:227]\n"
"ds_read_b64_tr_b16 a[60:61], v15 offset:12296\n"
"v_mfma_f32_16x16x32_bf16 v[252:255], a[92:95], a[148:151], v[252:255]\n"
"ds_read_b128 a[148:151], v13 offset:28672\n"
"v_mfma_f32_16x16x32_bf16 v[252:255], a[88:91], a[144:147], v[252:255]\n"
"ds_read_b128 a[144:147], v12 offset:28672\n"
"ds_read_b64_tr_b16 a[62:63], v15 offset:12808\n"
"v_mfma_f32_16x16x32_bf16 a[28:31], a[88:91], a[152:155], a[28:31]\n"
"v_mfma_f32_16x16x32_bf16 a[28:31], a[92:95], a[156:159], a[28:31]\n"
"v_mfma_f32_16x16x32_bf16 a[24:27], a[84:87], a[156:159], a[24:27]\n"
"ds_read_b128 a[156:159], v13 offset:30720\n"
"v_mfma_f32_16x16x32_bf16 a[24:27], a[80:83], a[152:155], a[24:27]\n"
"s_waitcnt lgkmcnt(15)\n"
"ds_read_b128 a[152:155], v12 offset:30720\n"
"v_mfma_f32_16x16x32_bf16 v[32:35], a[32:35], a[96:99], v[32:35]\n"
"v_mfma_f32_16x16x32_bf16 v[32:35], a[36:39], a[100:103], v[32:35]\n"
"s_waitcnt lgkmcnt(2)\n"
"v_mfma_f32_16x16x32_bf16 v[60:63], a[44:47], a[100:103], v[60:63]\n"
"buffer_load_dwordx4 v11, s[52:55], s67 offen offset:0  lds\n"
"v_mfma_f32_16x16x32_bf16 v[60:63], a[40:43], a[96:99], v[60:63]\n"
"L_st1593:\n"
"s_cbranch_execz L_st3859\n"
"v_mfma_f32_16x16x32_bf16 v[64:67], a[40:43], a[104:107], v[64:67]\n"
"s_add_u32 s67, s75, 801792\n"
"v_mfma_f32_16x16x32_bf16 v[64:67], a[44:47], a[108:111], v[64:67]\n"
"v_mfma_f32_16x16x32_bf16 v[36:39], a[36:39], a[108:111], v[36:39]\n"
"buffer_load_dwordx4 v11, s[52:55], s67 offen offset:1024  lds\n"
"v_mfma_f32_16x16x32_bf16 v[36:39], a[32:35], a[104:107], v[36:39]\n"
"v_mfma_f32_16x16x32_bf16 v[40:43], a[32:35], a[112:115], v[40:43]\n"
"s_add_u32 s67, s75, 817152\n"
"v_mfma_f32_16x16x32_bf16 v[40:43], a[36:39], a[116:119], v[40:43]\n"
"v_mfma_f32_16x16x32_bf16 v[68:71], a[44:47], a[116:119], v[68:71]\n"
"s_mov_b32 s30, 0\n"
"v_mfma_f32_16x16x32_bf16 v[68:71], a[40:43], a[112:115], v[68:71]\n"
"buffer_load_dwordx4 v11, s[52:55], s67 offen offset:2048  lds\n"
"v_mfma_f32_16x16x32_bf16 v[72:75], a[40:43], a[120:123], v[72:75]\n"
"s_mov_b32 s31, 0\n"
"v_mfma_f32_16x16x32_bf16 v[72:75], a[44:47], a[124:127], v[72:75]\n"
"s_add_u32 s67, s75, 832512\n"
"v_mfma_f32_16x16x32_bf16 v[44:47], a[36:39], a[124:127], v[44:47]\n"
"v_mfma_f32_16x16x32_bf16 v[44:47], a[32:35], a[120:123], v[44:47]\n"
"v_mfma_f32_16x16x32_bf16 v[48:51], a[32:35], a[128:131], v[48:51]\n"
"buffer_load_dwordx4 v11, s[52:55], s67 offen offset:3072  lds\n"
"v_mfma_f32_16x16x32_bf16 v[48:51], a[36:39], a[132:135], v[48:51]\n"
"s_add_u32 m0, s64, 12288\n"
"v_mfma_f32_16x16x32_bf16 v[76:79], a[44:47], a[132:135], v[76:79]\n"
"s_waitcnt vmcnt(11)\n"
"ds_read_b64_tr_b16 a[64:65], v9 offset:0\n"
"v_mfma_f32_16x16x32_bf16 v[76:79], a[40:43], a[128:131], v[76:79]\n"
"s_add_u32 s67, s75, 851968\n"
"ds_read_b64_tr_b16 a[66:67], v9 offset:512\n"
"v_mfma_f32_16x16x32_bf16 v[80:83], a[40:43], a[136:139], v[80:83]\n"
"ds_read_b64_tr_b16 a[68:69], v9 offset:4096\n"
"v_mfma_f32_16x16x32_bf16 v[80:83], a[44:47], a[140:143], v[80:83]\n"
"ds_read_b64_tr_b16 a[70:71], v9 offset:4608\n"
"buffer_load_dwordx4 v11, s[52:55], s67 offen offset:0  lds\n"
"v_mfma_f32_16x16x32_bf16 v[52:55], a[36:39], a[140:143], v[52:55]\n"
"s_add_u32 s67, s75, 867328\n"
"ds_read_b64_tr_b16 a[72:73], v9 offset:8\n"
"v_mfma_f32_16x16x32_bf16 v[52:55], a[32:35], a[136:139], v[52:55]\n"
"s_waitcnt lgkmcnt(5)\n"
"ds_read_b64_tr_b16 a[74:75], v9 offset:520\n"
"v_mfma_f32_16x16x32_bf16 v[56:59], a[32:35], a[144:147], v[56:59]\n"
"ds_read_b64_tr_b16 a[76:77], v9 offset:4104\n"
"v_mfma_f32_16x16x32_bf16 v[56:59], a[36:39], a[148:151], v[56:59]\n"
"ds_read_b64_tr_b16 a[78:79], v9 offset:4616\n"
"v_mfma_f32_16x16x32_bf16 v[84:87], a[44:47], a[148:151], v[84:87]\n"
"buffer_load_dwordx4 v11, s[52:55], s67 offen offset:1024  lds\n"
"v_mfma_f32_16x16x32_bf16 v[84:87], a[40:43], a[144:147], v[84:87]\n"
"ds_read_b64_tr_b16 a[80:81], v15 offset:0\n"
"v_mfma_f32_16x16x32_bf16 a[4:7], a[40:43], a[152:155], a[4:7]\n"
"s_add_u32 s67, s75, 882688\n"
"ds_read_b64_tr_b16 a[82:83], v15 offset:512\n"
"v_mfma_f32_16x16x32_bf16 a[4:7], a[44:47], a[156:159], a[4:7]\n"
"ds_read_b64_tr_b16 a[84:85], v15 offset:4096\n"
"buffer_load_dwordx4 v11, s[52:55], s67 offen offset:2048  lds\n"
"v_mfma_f32_16x16x32_bf16 a[0:3], a[36:39], a[156:159], a[0:3]\n"
"s_add_u32 s96, s56, %11\n"
"ds_read_b64_tr_b16 a[86:87], v15 offset:4608\n"
"s_addc_u32 s97, s57, 0\n"
"v_mfma_f32_16x16x32_bf16 a[0:3], a[32:35], a[152:155], a[0:3]\n"
"ds_read_b64_tr_b16 a[88:89], v15 offset:8\n"
"v_mfma_f32_16x16x32_bf16 v[88:91], a[48:51], a[96:99], v[88:91]\n"
"s_add_u32 s67, s75, 898048\n"
"ds_read_b64_tr_b16 a[90:91], v15 offset:520\n"
"v_mfma_f32_16x16x32_bf16 v[88:91], a[52:55], a[100:103], v[88:91]\n"
"ds_read_b64_tr_b16 a[92:93], v15 offset:4104\n"
"v_mfma_f32_16x16x32_bf16 v[116:119], a[60:63], a[100:103], v[116:119]\n"
"ds_read_b64_tr_b16 a[94:95], v15 offset:4616\n"
"v_mfma_f32_16x16x32_bf16 v[116:119], a[56:59], a[96:99], v[116:119]\n"
"buffer_load_dwordx4 v11, s[52:55], s67 offen offset:3072  lds\n"
"v_mfma_f32_16x16x32_bf16 v[120:123], a[56:59], a[104:107], v[120:123]\n"
"v_mfma_f32_16x16x32_bf16 v[120:123], a[60:63], a[108:111], v[120:123]\n"
"s_add_u32 m0, s64, 0\n"
"v_mfma_f32_16x16x32_bf16 v[92:95], a[52:55], a[108:111], v[92:95]\n"
"v_mfma_f32_16x16x32_bf16 v[92:95], a[48:51], a[104:107], v[92:95]\n"
"s_add_u32 s67, s75, 786560\n"
"v_mfma_f32_16x16x32_bf16 v[96:99], a[48:51], a[112:115], v[96:99]\n"
"s_waitcnt lgkmcnt(0)\n"
"v_mfma_f32_16x16x32_bf16 v[96:99], a[52:55], a[116:119], v[96:99]\n"
"buffer_load_dwordx4 v11, s[52:55], s67 offen offset:0  lds\n"
"v_mfma_f32_16x16x32_bf16 v[124:127], a[60:63], a[116:119], v[124:127]\n"
"s_load_dword s98, s[96:97], 512 glc\n"
"v_mfma_f32_16x16x32_bf16 v[124:127], a[56:59], a[112:115], v[124:127]\n"
"s_load_dword s99, s[96:97], 8704 glc\n"
"s_add_u32 s67, s75, 801920\n"
"v_mfma_f32_16x16x32_bf16 v[128:131], a[56:59], a[120:123], v[128:131]\n"
"v_mfma_f32_16x16x32_bf16 v[128:131], a[60:63], a[124:127], v[128:131]\n"
"buffer_load_dwordx4 v11, s[52:55], s67 offen offset:1024  lds\n"
"v_mfma_f32_16x16x32_bf16 v[100:103], a[52:55], a[124:127], v[100:103]\n"
"v_mfma_f32_16x16x32_bf16 v[100:103], a[48:51], a[120:123], v[100:103]\n"
"s_add_u32 s67, s75, 817280\n"
"v_mfma_f32_16x16x32_bf16 v[104:107], a[48:51], a[128:131], v[104:107]\n"
"v_mfma_f32_16x16x32_bf16 v[104:107], a[52:55], a[132:135], v[104:107]\n"
"v_mfma_f32_16x16x32_bf16 v[132:135], a[60:63], a[132:135], v[132:135]\n"
"buffer_load_dwordx4 v11, s[52:55], s67 offen offset:2048  lds\n"
"v_mfma_f32_16x16x32_bf16 v[132:135], a[56:59], a[128:131], v[132:135]\n"
"v_mfma_f32_16x16x32_bf16 v[136:139], a[56:59], a[136:139], v[136:139]\n"
"s_add_u32 s67, s75, 832640\n"
"v_mfma_f32_16x16x32_bf16 v[136:139], a[60:63], a[140:143], v[136:139]\n"
"v_mfma_f32_16x16x32_bf16 v[108:111], a[52:55], a[140:143], v[108:111]\n"
"buffer_load_dwordx4 v11, s[52:55], s67 offen offset:3072  lds\n"
"v_mfma_f32_16x16x32_bf16 v[108:111], a[48:51], a[136:139], v[108:111]\n"
"v_mfma_f32_16x16x32_bf16 v[112:115], a[48:51], a[144:147], v[112:115]\n"
"s_add_u32 m0, s64, 4096\n"
"v_mfma_f32_16x16x32_bf16 v[112:115], a[52:55], a[148:151], v[112:115]\n"
"v_mfma_f32_16x16x32_bf16 v[140:143], a[60:63], a[148:151], v[140:143]\n"
"s_add_u32 s67, s75, 852096\n"
"v_mfma_f32_16x16x32_bf16 v[140:143], a[56:59], a[144:147], v[140:143]\n"
"v_mfma_f32_16x16x32_bf16 a[12:15], a[56:59], a[152:155], a[12:15]\n"
"buffer_load_dwordx4 v11, s[52:55], s67 offen offset:0  lds\n"
"v_mfma_f32_16x16x32_bf16 a[12:15], a[60:63], a[156:159], a[12:15]\n"
"v_mfma_f32_16x16x32_bf16 a[8:11], a[52:55], a[156:159], a[8:11]\n"
"s_add_u32 s67, s75, 867456\n"
"v_mfma_f32_16x16x32_bf16 a[8:11], a[48:51], a[152:155], a[8:11]\n"
"v_mfma_f32_16x16x32_bf16 v[144:147], a[64:67], a[96:99], v[144:147]\n"
"buffer_load_dwordx4 v11, s[52:55], s67 offen offset:1024  lds\n"
"v_mfma_f32_16x16x32_bf16 v[144:147], a[68:71], a[100:103], v[144:147]\n"
"v_mfma_f32_16x16x32_bf16 v[172:175], a[76:79], a[100:103], v[172:175]\n"
"s_add_u32 s67, s75, 882816\n"
"v_mfma_f32_16x16x32_bf16 v[172:175], a[72:75], a[96:99], v[172:175]\n"
"v_mfma_f32_16x16x32_bf16 v[176:179], a[72:75], a[104:107], v[176:179]\n"
"buffer_load_dwordx4 v11, s[52:55], s67 offen offset:2048  lds\n"
"v_mfma_f32_16x16x32_bf16 v[176:179], a[76:79], a[108:111], v[176:179]\n"
"v_mfma_f32_16x16x32_bf16 v[148:151], a[68:71], a[108:111], v[148:151]\n"
"s_add_u32 s67, s75, 898176\n"
"v_mfma_f32_16x16x32_bf16 v[148:151], a[64:67], a[104:107], v[148:151]\n"
"v_mfma_f32_16x16x32_bf16 v[152:155], a[64:67], a[112:115], v[152:155]\n"
"s_sub_u32 s66, 81920, s73\n"
"v_mfma_f32_16x16x32_bf16 v[152:155], a[68:71], a[116:119], v[152:155]\n"
"buffer_load_dwordx4 v11, s[52:55], s67 offen offset:3072  lds\n"
"v_mfma_f32_16x16x32_bf16 v[180:183], a[76:79], a[116:119], v[180:183]\n"
"s_add_u32 m0, s72, 139264\n"
"v_add_u32 v12, s66, v7\n"
"v_mfma_f32_16x16x32_bf16 v[180:183], a[72:75], a[112:115], v[180:183]\n"
"v_mfma_f32_16x16x32_bf16 v[184:187], a[72:75], a[120:123], v[184:187]\n"
"s_add_u32 s67, s60, 896\n"
"v_add_u32 v13, s66, v8\n"
"v_mfma_f32_16x16x32_bf16 v[184:187], a[76:79], a[124:127], v[184:187]\n"
"v_mfma_f32_16x16x32_bf16 v[156:159], a[68:71], a[124:127], v[156:159]\n"
"buffer_load_dwordx4 v6, s[48:51], s67 offen offset:0  lds\n"
"v_mfma_f32_16x16x32_bf16 v[156:159], a[64:67], a[120:123], v[156:159]\n"
"v_mfma_f32_16x16x32_bf16 v[160:163], a[64:67], a[128:131], v[160:163]\n"
"s_add_u32 s67, s60, 16256\n"
"v_mfma_f32_16x16x32_bf16 v[160:163], a[68:71], a[132:135], v[160:163]\n"
"v_mfma_f32_16x16x32_bf16 v[188:191], a[76:79], a[132:135], v[188:191]\n"
"buffer_load_dwordx4 v6, s[48:51], s67 offen offset:1024  lds\n"
"v_mfma_f32_16x16x32_bf16 v[188:191], a[72:75], a[128:131], v[188:191]\n"
"v_mfma_f32_16x16x32_bf16 v[192:195], a[72:75], a[136:139], v[192:195]\n"
"s_add_u32 s67, s60, 31616\n"
"v_mfma_f32_16x16x32_bf16 v[192:195], a[76:79], a[140:143], v[192:195]\n"
"v_mfma_f32_16x16x32_bf16 v[164:167], a[68:71], a[140:143], v[164:167]\n"
"v_mfma_f32_16x16x32_bf16 v[164:167], a[64:67], a[136:139], v[164:167]\n"
"buffer_load_dwordx4 v6, s[48:51], s67 offen offset:2048  lds\n"
"v_mfma_f32_16x16x32_bf16 v[168:171], a[64:67], a[144:147], v[168:171]\n"
"v_mfma_f32_16x16x32_bf16 v[168:171], a[68:71], a[148:151], v[168:171]\n"
"s_add_u32 s67, s60, 46976\n"
"v_mfma_f32_16x16x32_bf16 v[196:199], a[76:79], a[148:151], v[196:199]\n"
"v_mfma_f32_16x16x32_bf16 v[196:199], a[72:75], a[144:147], v[196:199]\n"
"buffer_load_dwordx4 v6, s[48:51], s67 offen offset:3072  lds\n"
"v_mfma_f32_16x16x32_bf16 a[20:23], a[72:75], a[152:155], a[20:23]\n"
"v_mfma_f32_16x16x32_bf16 a[20:23], a[76:79], a[156:159], a[20:23]\n"
"s_waitcnt lgkmcnt(0)\n"
"s_add_u32 m0, s64, 32640\n"
"v_mfma_f32_16x16x32_bf16 a[16:19], a[68:71], a[156:159], a[16:19]\n"
"v_mfma_f32_16x16x32_bf16 a[16:19], a[64:67], a[152:155], a[16:19]\n"
"s_waitcnt vmcnt(23)\n"
"s_barrier\n"
"v_mfma_f32_16x16x32_bf16 v[200:203], a[80:83], a[96:99], v[200:203]\n"
"s_waitcnt vmcnt(12)\n"
"ds_read_b64_tr_b16 a[32:33], v9 offset:8192\n"
"buffer_load_dwordx4 v6, s[40:43], s80 offen offset:128  lds\n"
"v_mfma_f32_16x16x32_bf16 v[200:203], a[84:87], a[100:103], v[200:203]\n"
"ds_read_b64_tr_b16 a[34:35], v9 offset:8704\n"
"v_mfma_f32_16x16x32_bf16 v[228:231], a[92:95], a[100:103], v[228:231]\n"
"ds_read_b128 a[100:103], v13 offset:16384\n"
"v_mfma_f32_16x16x32_bf16 v[228:231], a[88:91], a[96:99], v[228:231]\n"
"ds_read_b128 a[96:99], v12 offset:16384\n"
"buffer_load_dwordx4 v6, s[40:43], s81 offen offset:1152  lds\n"
"v_mfma_f32_16x16x32_bf16 v[232:235], a[88:91], a[104:107], v[232:235]\n"
"ds_read_b64_tr_b16 a[36:37], v9 offset:12288\n"
"v_mfma_f32_16x16x32_bf16 v[232:235], a[92:95], a[108:111], v[232:235]\n"
"ds_read_b64_tr_b16 a[38:39], v9 offset:12800\n"
"v_mfma_f32_16x16x32_bf16 v[204:207], a[84:87], a[108:111], v[204:207]\n"
"ds_read_b128 a[108:111], v13 offset:18432\n"
"v_mfma_f32_16x16x32_bf16 v[204:207], a[80:83], a[104:107], v[204:207]\n"
"ds_read_b128 a[104:107], v12 offset:18432\n"
"ds_read_b64_tr_b16 a[40:41], v9 offset:8200\n"
"v_mfma_f32_16x16x32_bf16 v[208:211], a[80:83], a[112:115], v[208:211]\n"
"ds_read_b64_tr_b16 a[42:43], v9 offset:8712\n"
"buffer_load_dwordx4 v6, s[40:43], s82 offen offset:2176  lds\n"
"v_mfma_f32_16x16x32_bf16 v[208:211], a[84:87], a[116:119], v[208:211]\n"
"ds_read_b64_tr_b16 a[44:45], v9 offset:12296\n"
"v_mfma_f32_16x16x32_bf16 v[236:239], a[92:95], a[116:119], v[236:239]\n"
"ds_read_b128 a[116:119], v13 offset:20480\n"
"v_mfma_f32_16x16x32_bf16 v[236:239], a[88:91], a[112:115], v[236:239]\n"
"ds_read_b128 a[112:115], v12 offset:20480\n"
"v_mfma_f32_16x16x32_bf16 v[240:243], a[88:91], a[120:123], v[240:243]\n"
"ds_read_b64_tr_b16 a[46:47], v9 offset:12808\n"
"v_mfma_f32_16x16x32_bf16 v[240:243], a[92:95], a[124:127], v[240:243]\n"
"ds_read_b64_tr_b16 a[48:49], v15 offset:8192\n"
"buffer_load_dwordx4 v6, s[40:43], s83 offen offset:3200  lds\n"
"v_mfma_f32_16x16x32_bf16 v[212:215], a[84:87], a[124:127], v[212:215]\n"
"ds_read_b128 a[124:127], v13 offset:22528\n"
"v_mfma_f32_16x16x32_bf16 v[212:215], a[80:83], a[120:123], v[212:215]\n"
"ds_read_b128 a[120:123], v12 offset:22528\n"
"v_mfma_f32_16x16x32_bf16 v[216:219], a[80:83], a[128:131], v[216:219]\n"
"ds_read_b64_tr_b16 a[50:51], v15 offset:8704\n"
"v_mfma_f32_16x16x32_bf16 v[216:219], a[84:87], a[132:135], v[216:219]\n"
"s_add_u32 m0, s64, 8192\n"
"ds_read_b64_tr_b16 a[52:53], v15 offset:12288\n"
"v_mfma_f32_16x16x32_bf16 v[244:247], a[92:95], a[132:135], v[244:247]\n"
"ds_read_b128 a[132:135], v13 offset:24576\n"
"v_mfma_f32_16x16x32_bf16 v[244:247], a[88:91], a[128:131], v[244:247]\n"
"ds_read_b128 a[128:131], v12 offset:24576\n"
"v_mfma_f32_16x16x32_bf16 v[248:251], a[88:91], a[136:139], v[248:251]\n"
"ds_read_b64_tr_b16 a[54:55], v15 offset:12800\n"
"v_mfma_f32_16x16x32_bf16 v[248:251], a[92:95], a[140:143], v[248:251]\n"
"ds_read_b64_tr_b16 a[56:57], v15 offset:8200\n"
"v_mfma_f32_16x16x32_bf16 v[220:223], a[84:87], a[140:143], v[220:223]\n"
"ds_read_b128 a[140:143], v13 offset:26624\n"
"v_mfma_f32_16x16x32_bf16 v[220:223], a[80:83], a[136:139], v[220:223]\n"
"ds_read_b128 a[136:139], v12 offset:26624\n"
"v_mfma_f32_16x16x32_bf16 v[224:227], a[80:83], a[144:147], v[224:227]\n"
"ds_read_b64_tr_b16 a[58:59], v15 offset:8712\n"
"v_mfma_f32_16x16x32_bf16 v[224:227], a[84:87], a[148:151], v[224:227]\n"
"s_add_u32 s67, s75, 917504\n"
"ds_read_b64_tr_b16 a[60:61], v15 offset:12296\n"
"v_mfma_f32_16x16x32_bf16 v[252:255], a[92:95], a[148:151], v[252:255]\n"
"ds_read_b128 a[148:151], v13 offset:28672\n"
"v_mfma_f32_16x16x32_bf16 v[252:255], a[88:91], a[144:147], v[252:255]\n"
"ds_read_b128 a[144:147], v12 offset:28672\n"
"ds_read_b64_tr_b16 a[62:63], v15 offset:12808\n"
"v_mfma_f32_16x16x32_bf16 a[28:31], a[88:91], a[152:155], a[28:31]\n"
"v_mfma_f32_16x16x32_bf16 a[28:31], a[92:95], a[156:159], a[28:31]\n"
"v_mfma_f32_16x16x32_bf16 a[24:27], a[84:87], a[156:159], a[24:27]\n"
"ds_read_b128 a[156:159], v13 offset:30720\n"
"v_mfma_f32_16x16x32_bf16 a[24:27], a[80:83], a[152:155], a[24:27]\n"
"s_waitcnt lgkmcnt(15)\n"
"ds_read_b128 a[152:155], v12 offset:30720\n"
"v_mfma_f32_16x16x32_bf16 v[32:35], a[32:35], a[96:99], v[32:35]\n"
"v_mfma_f32_16x16x32_bf16 v[32:35], a[36:39], a[100:103], v[32:35]\n"
"s_waitcnt lgkmcnt(2)\n"
"L_H6S:\n"
"v_mfma_f32_16x16x32_bf16 v[60:63], a[44:47], a[100:103], v[60:63]\n"
"buffer_load_dwordx4 v11, s[52:55], s67 offen offset:0  lds\n"
"v_mfma_f32_16x16x32_bf16 v[60:63], a[40:43], a[96:99], v[60:63]\n"
"v_mfma_f32_16x16x32_bf16 v[64:67], a[40:43], a[104:107], v[64:67]\n"
"s_add_u32 s67, s75, 932864\n"
"v_mfma_f32_16x16x32_bf16 v[64:67], a[44:47], a[108:111], v[64:67]\n"
"v_mfma_f32_16x16x32_bf16 v[36:39], a[36:39], a[108:111], v[36:39]\n"
"v_mfma_f32_16x16x32_bf16 v[36:39], a[32:35], a[104:107], v[36:39]\n"
"buffer_load_dwordx4 v11, s[52:55], s67 offen offset:1024  lds\n"
"v_mfma_f32_16x16x32_bf16 v[40:43], a[32:35], a[112:115], v[40:43]\n"
"v_mfma_f32_16x16x32_bf16 v[40:43], a[36:39], a[116:119], v[40:43]\n"
"s_add_u32 s67, s75, 948224\n"
"v_mfma_f32_16x16x32_bf16 v[68:71], a[44:47], a[116:119], v[68:71]\n"
"v_mfma_f32_16x16x32_bf16 v[68:71], a[40:43], a[112:115], v[68:71]\n"
"buffer_load_dwordx4 v11, s[52:55], s67 offen offset:2048  lds\n"
"v_mfma_f32_16x16x32_bf16 v[72:75], a[40:43], a[120:123], v[72:75]\n"
"v_mfma_f32_16x16x32_bf16 v[72:75], a[44:47], a[124:127], v[72:75]\n"
"s_add_u32 s67, s75, 963584\n"
"v_mfma_f32_16x16x32_bf16 v[44:47], a[36:39], a[124:127], v[44:47]\n"
"v_mfma_f32_16x16x32_bf16 v[44:47], a[32:35], a[120:123], v[44:47]\n"
"v_mfma_f32_16x16x32_bf16 v[48:51], a[32:35], a[128:131], v[48:51]\n"
"buffer_load_dwordx4 v11, s[52:55], s67 offen offset:3072  lds\n"
"v_mfma_f32_16x16x32_bf16 v[48:51], a[36:39], a[132:135], v[48:51]\n"
"s_add_u32 m0, s64, 12288\n"
"v_mfma_f32_16x16x32_bf16 v[76:79], a[44:47], a[132:135], v[76:79]\n"
"s_waitcnt vmcnt(12)\n"
"ds_read_b64_tr_b16 a[64:65], v9 offset:0\n"
"v_mfma_f32_16x16x32_bf16 v[76:79], a[40:43], a[128:131], v[76:79]\n"
"ds_read_b64_tr_b16 a[66:67], v9 offset:512\n"
"v_mfma_f32_16x16x32_bf16 v[80:83], a[40:43], a[136:139], v[80:83]\n"
"s_add_u32 s67, s75, 983040\n"
"ds_read_b64_tr_b16 a[68:69], v9 offset:4096\n"
"v_mfma_f32_16x16x32_bf16 v[80:83], a[44:47], a[140:143], v[80:83]\n"
"ds_read_b64_tr_b16 a[70:71], v9 offset:4608\n"
"v_mfma_f32_16x16x32_bf16 v[52:55], a[36:39], a[140:143], v[52:55]\n"
"ds_read_b64_tr_b16 a[72:73], v9 offset:8\n"
"buffer_load_dwordx4 v11, s[52:55], s67 offen offset:0  lds\n"
"v_mfma_f32_16x16x32_bf16 v[52:55], a[32:35], a[136:139], v[52:55]\n"
"s_waitcnt lgkmcnt(5)\n"
"ds_read_b64_tr_b16 a[74:75], v9 offset:520\n"
"v_mfma_f32_16x16x32_bf16 v[56:59], a[32:35], a[144:147], v[56:59]\n"
"s_add_u32 s67, s75, 998400\n"
"ds_read_b64_tr_b16 a[76:77], v9 offset:4104\n"
"v_mfma_f32_16x16x32_bf16 v[56:59], a[36:39], a[148:151], v[56:59]\n"
"ds_read_b64_tr_b16 a[78:79], v9 offset:4616\n"
"v_mfma_f32_16x16x32_bf16 v[84:87], a[44:47], a[148:151], v[84:87]\n"
"buffer_load_dwordx4 v11, s[52:55], s67 offen offset:1024  lds\n"
"v_mfma_f32_16x16x32_bf16 v[84:87], a[40:43], a[144:147], v[84:87]\n"
"ds_read_b64_tr_b16 a[80:81], v15 offset:0\n"
"v_mfma_f32_16x16x32_bf16 a[4:7], a[40:43], a[152:155], a[4:7]\n"
"s_add_u32 s67, s75, 1013760\n"
"ds_read_b64_tr_b16 a[82:83], v15 offset:512\n"
"v_mfma_f32_16x16x32_bf16 a[4:7], a[44:47], a[156:159], a[4:7]\n"
"ds_read_b64_tr_b16 a[84:85], v15 offset:4096\n"
"v_mfma_f32_16x16x32_bf16 a[0:3], a[36:39], a[156:159], a[0:3]\n"
"ds_read_b64_tr_b16 a[86:87], v15 offset:4608\n"
"buffer_load_dwordx4 v11, s[52:55], s67 offen offset:2048  lds\n"
"v_mfma_f32_16x16x32_bf16 a[0:3], a[32:35], a[152:155], a[0:3]\n"
"ds_read_b64_tr_b16 a[88:89], v15 offset:8\n"
"v_mfma_f32_16x16x32_bf16 v[88:91], a[48:51], a[96:99], v[88:91]\n"
"s_add_u32 s67, s75, 1029120\n"
"ds_read_b64_tr_b16 a[90:91], v15 offset:520\n"
"v_mfma_f32_16x16x32_bf16 v[88:91], a[52:55], a[100:103], v[88:91]\n"
"ds_read_b64_tr_b16 a[92:93], v15 offset:4104\n"
"v_mfma_f32_16x16x32_bf16 v[116:119], a[60:63], a[100:103], v[116:119]\n"
"ds_read_b64_tr_b16 a[94:95], v15 offset:4616\n"
"v_mfma_f32_16x16x32_bf16 v[116:119], a[56:59], a[96:99], v[116:119]\n"
"buffer_load_dwordx4 v11, s[52:55], s67 offen offset:3072  lds\n"
"v_mfma_f32_16x16x32_bf16 v[120:123], a[56:59], a[104:107], v[120:123]\n"
"v_mfma_f32_16x16x32_bf16 v[120:123], a[60:63], a[108:111], v[120:123]\n"
"s_add_u32 m0, s64, 0\n"
"v_mfma_f32_16x16x32_bf16 v[92:95], a[52:55], a[108:111], v[92:95]\n"
"v_mfma_f32_16x16x32_bf16 v[92:95], a[48:51], a[104:107], v[92:95]\n"
"s_add_u32 s67, s75, 917632\n"
"v_mfma_f32_16x16x32_bf16 v[96:99], a[48:51], a[112:115], v[96:99]\n"
"s_waitcnt lgkmcnt(0)\n"
"s_mov_b32 s99, 0\n"
"v_mfma_f32_16x16x32_bf16 v[96:99], a[52:55], a[116:119], v[96:99]\n"
"s_mov_b32 s98, 0\n"
"buffer_load_dwordx4 v11, s[52:55], s67 offen offset:0  lds\n"
"v_mfma_f32_16x16x32_bf16 v[124:127], a[60:63], a[116:119], v[124:127]\n"
"s_memtime s[76:77]\n"
"s_load_dword s98, s[96:97], 8192 glc\n"
"v_mfma_f32_16x16x32_bf16 v[124:127], a[56:59], a[112:115], v[124:127]\n"
"s_add_u32 s67, s75, 932992\n"
"v_mfma_f32_16x16x32_bf16 v[128:131], a[56:59], a[120:123], v[128:131]\n"
"v_mfma_f32_16x16x32_bf16 v[128:131], a[60:63], a[124:127], v[128:131]\n"
"v_mfma_f32_16x16x32_bf16 v[100:103], a[52:55], a[124:127], v[100:103]\n"
"buffer_load_dwordx4 v11, s[52:55], s67 offen offset:1024  lds\n"
"v_mfma_f32_16x16x32_bf16 v[100:103], a[48:51], a[120:123], v[100:103]\n"
"v_mfma_f32_16x16x32_bf16 v[104:107], a[48:51], a[128:131], v[104:107]\n"
"s_add_u32 s67, s75, 948352\n"
"v_mfma_f32_16x16x32_bf16 v[104:107], a[52:55], a[132:135], v[104:107]\n"
"v_mfma_f32_16x16x32_bf16 v[132:135], a[60:63], a[132:135], v[132:135]\n"
"buffer_load_dwordx4 v11, s[52:55], s67 offen offset:2048  lds\n"
"v_mfma_f32_16x16x32_bf16 v[132:135], a[56:59], a[128:131], v[132:135]\n"
"s_mov_b32 s29, s76\n"
"v_mfma_f32_16x16x32_bf16 v[136:139], a[56:59], a[136:139], v[136:139]\n"
"s_add_u32 s67, s75, 963712\n"
"v_mfma_f32_16x16x32_bf16 v[136:139], a[60:63], a[140:143], v[136:139]\n"
"v_mfma_f32_16x16x32_bf16 v[108:111], a[52:55], a[140:143], v[108:111]\n"
"s_sub_u32 s99, s99, s98\n"
"s_sub_u32 s99, s99, s98\n"
"v_mfma_f32_16x16x32_bf16 v[108:111], a[48:51], a[136:139], v[108:111]\n"
"s_sub_u32 s99, s99, s98\n"
"buffer_load_dwordx4 v11, s[52:55], s67 offen offset:3072  lds\n"
"v_mfma_f32_16x16x32_bf16 v[112:115], a[48:51], a[144:147], v[112:115]\n"
"s_sub_u32 s99, s99, s98\n"
"s_add_u32 m0, s64, 4096\n"
"v_mfma_f32_16x16x32_bf16 v[112:115], a[52:55], a[148:151], v[112:115]\n"
"v_mfma_f32_16x16x32_bf16 v[140:143], a[60:63], a[148:151], v[140:143]\n"
"s_sub_u32 s99, s99, s98\n"
"s_sub_u32 s99, s99, s98\n"
"v_mfma_f32_16x16x32_bf16 v[140:143], a[56:59], a[144:147], v[140:143]\n"
"s_sub_u32 s99, s99, s98\n"
"s_add_u32 s67, s75, 983168\n"
"v_mfma_f32_16x16x32_bf16 a[12:15], a[56:59], a[152:155], a[12:15]\n"
"s_sub_u32 s99, s99, s98\n"
"buffer_load_dwordx4 v11, s[52:55], s67 offen offset:0  lds\n"
"v_mfma_f32_16x16x32_bf16 a[12:15], a[60:63], a[156:159], a[12:15]\n"
"s_add_u32 s67, s75, 998528\n"
"v_mfma_f32_16x16x32_bf16 a[8:11], a[52:55], a[156:159], a[8:11]\n"
"s_sub_u32 s99, s99, s98\n"
"s_sub_u32 s99, s99, s98\n"
"v_mfma_f32_16x16x32_bf16 a[8:11], a[48:51], a[152:155], a[8:11]\n"
"s_sub_u32 s99, s99, s98\n"
"v_mfma_f32_16x16x32_bf16 v[144:147], a[64:67], a[96:99], v[144:147]\n"
"s_sub_u32 s99, s99, s98\n"
"v_mfma_f32_16x16x32_bf16 v[144:147], a[68:71], a[100:103], v[144:147]\n"
"buffer_load_dwordx4 v11, s[52:55], s67 offen offset:1024  lds\n"
"v_mfma_f32_16x16x32_bf16 v[172:175], a[76:79], a[100:103], v[172:175]\n"
"s_sub_u32 s99, s99, s98\n"
"s_sub_u32 s99, s99, s98\n"
"v_mfma_f32_16x16x32_bf16 v[172:175], a[72:75], a[96:99], v[172:175]\n"
"s_sub_u32 s99, s99, s98\n"
"s_add_u32 s67, s75, 1013888\n"
"v_mfma_f32_16x16x32_bf16 v[176:179], a[72:75], a[104:107], v[176:179]\n"
"s_sub_u32 s99, s99, s98\n"
"v_mfma_f32_16x16x32_bf16 v[176:179], a[76:79], a[108:111], v[176:179]\n"
"s_sub_u32 s71, s64, s64\n"
"buffer_load_dwordx4 v11, s[52:55], s67 offen offset:2048  lds\n"
"v_mfma_f32_16x16x32_bf16 v[148:151], a[68:71], a[108:111], v[148:151]\n"
"s_sub_u32 s99, s99, s98\n"
"s_sub_u32 s99, s99, s98\n"
"v_mfma_f32_16x16x32_bf16 v[148:151], a[64:67], a[104:107], v[148:151]\n"
"s_sub_u32 s99, s99, s98\n"
"s_add_u32 s71, s71, 32768\n"
"s_add_u32 s67, s75, 1029248\n"
"v_mfma_f32_16x16x32_bf16 v[152:155], a[64:67], a[112:115], v[152:155]\n"
"s_sub_u32 s66, 122880, s73\n"
"s_sub_u32 s99, s99, s98\n"
"v_mfma_f32_16x16x32_bf16 v[152:155], a[68:71], a[116:119], v[152:155]\n"
"buffer_load_dwordx4 v11, s[52:55], s67 offen offset:3072  lds\n"
"v_mfma_f32_16x16x32_bf16 v[180:183], a[76:79], a[116:119], v[180:183]\n"
"s_sub_u32 s99, s99, s98\n"
"v_add_u32 v12, s66, v7\n"
"s_sub_u32 s99, s99, s98\n"
"v_mfma_f32_16x16x32_bf16 v[180:183], a[72:75], a[112:115], v[180:183]\n"
"s_sub_u32 s99, s99, s98\n"
"v_add_u32 v20, s71, v7\n"
"v_mfma_f32_16x16x32_bf16 v[184:187], a[72:75], a[120:123], v[184:187]\n"
"s_sub_u32 s99, s99, s98\n"
"v_add_u32 v13, s66, v8\n"
"v_mfma_f32_16x16x32_bf16 v[184:187], a[76:79], a[124:127], v[184:187]\n"
"s_memtime s[76:77]\n"
"v_add_u32 v21, s71, v8\n"
"v_mfma_f32_16x16x32_bf16 v[156:159], a[68:71], a[124:127], v[156:159]\n"
"s_add_u32 m0, s72, 57344\n"
"s_add_u32 s67, %21, 0\n"
"v_mfma_f32_16x16x32_bf16 v[156:159], a[64:67], a[120:123], v[156:159]\n"
"s_waitcnt lgkmcnt(0)\n"
"buffer_load_dwordx4 v11, s[52:55], s67 offen offset:0  lds\n"
"v_mfma_f32_16x16x32_bf16 v[160:163], a[64:67], a[128:131], v[160:163]\n"
"s_sub_u32 s29, s76, s29\n"
"s_waitcnt vmcnt(17)\n"
"v_mfma_f32_16x16x32_bf16 v[160:163], a[68:71], a[132:135], v[160:163]\n"
"s_cmp_lt_u32 s29, 620\n"
"s_cselect_b32 s99, s99, 0\n"
"v_mfma_f32_16x16x32_bf16 v[188:191], a[76:79], a[132:135], v[188:191]\n"
"s_max_u32 s31, s31, s99\n"
"ds_read_b128 a[192:195], v20 offset:0\n"
"v_mfma_f32_16x16x32_bf16 v[188:191], a[72:75], a[128:131], v[188:191]\n"
"s_add_u32 s67, %21, 15360\n"
"ds_read_b128 a[196:199], v21 offset:0\n"
"buffer_load_dwordx4 v11, s[52:55], s67 offen offset:1024  lds\n"
"v_mfma_f32_16x16x32_bf16 v[192:195], a[72:75], a[136:139], v[192:195]\n"
"ds_read_b128 a[200:203], v20 offset:2048\n"
"v_mfma_f32_16x16x32_bf16 v[192:195], a[76:79], a[140:143], v[192:195]\n"
"s_add_u32 s67, %21, 30720\n"
"ds_read_b128 a[204:207], v21 offset:2048\n"
"v_mfma_f32_16x16x32_bf16 v[164:167], a[68:71], a[140:143], v[164:167]\n"
"v_mfma_f32_16x16x32_bf16 v[164:167], a[64:67], a[136:139], v[164:167]\n"
"buffer_load_dwordx4 v11, s[52:55], s67 offen offset:2048  lds\n"
"v_mfma_f32_16x16x32_bf16 v[168:171], a[64:67], a[144:147], v[168:171]\n"
"v_mfma_f32_16x16x32_bf16 v[168:171], a[68:71], a[148:151], v[168:171]\n"
"s_add_u32 s67, %21, 46080\n"
"v_mfma_f32_16x16x32_bf16 v[196:199], a[76:79], a[148:151], v[196:199]\n"
"v_mfma_f32_16x16x32_bf16 v[196:199], a[72:75], a[144:147], v[196:199]\n"
"s_waitcnt lgkmcnt(0)\n"
"v_mfma_f32_16x16x32_bf16 a[20:23], a[72:75], a[152:155], a[20:23]\n"
"buffer_load_dwordx4 v11, s[52:55], s67 offen offset:3072  lds\n"
"v_mfma_f32_16x16x32_bf16 a[20:23], a[76:79], a[156:159], a[20:23]\n"
"s_cmp_lt_u32 %16, 2\n"
"s_cselect_b32 s67, 16384, 0\n"
"v_mfma_f32_16x16x32_bf16 a[16:19], a[68:71], a[156:159], a[16:19]\n"
"s_add_u32 s67, s67, s72\n"
"v_mfma_f32_16x16x32_bf16 a[16:19], a[64:67], a[152:155], a[16:19]\n"
"s_barrier\n"
"s_waitcnt vmcnt(12)\n"
"ds_read_b64_tr_b16 a[32:33], v9 offset:8192\n"
"v_mfma_f32_16x16x32_bf16 v[200:203], a[80:83], a[96:99], v[200:203]\n"
"s_add_u32 m0, s67, 16384\n"
"ds_read_b64_tr_b16 a[34:35], v9 offset:8704\n"
"v_mfma_f32_16x16x32_bf16 v[200:203], a[84:87], a[100:103], v[200:203]\n"
"s_sub_u32 s67, 1024, s60\n"
"ds_read_b64_tr_b16 a[36:37], v9 offset:12288\n"
"v_mfma_f32_16x16x32_bf16 v[228:231], a[92:95], a[100:103], v[228:231]\n"
"ds_read_b128 a[100:103], v13 offset:16384\n"
"buffer_load_dwordx4 v6, s[48:51], s67 offen offset:0  lds\n"
"v_mfma_f32_16x16x32_bf16 v[228:231], a[88:91], a[96:99], v[228:231]\n"
"ds_read_b128 a[96:99], v12 offset:16384\n"
"v_mfma_f32_16x16x32_bf16 v[232:235], a[88:91], a[104:107], v[232:235]\n"
"s_sub_u32 s67, 16384, s60\n"
"ds_read_b64_tr_b16 a[38:39], v9 offset:12800\n"
"v_mfma_f32_16x16x32_bf16 v[232:235], a[92:95], a[108:111], v[232:235]\n"
"ds_read_b64_tr_b16 a[40:41], v9 offset:8200\n"
"v_mfma_f32_16x16x32_bf16 v[204:207], a[84:87], a[108:111], v[204:207]\n"
"ds_read_b128 a[108:111], v13 offset:18432\n"
"buffer_load_dwordx4 v6, s[48:51], s67 offen offset:1024  lds\n"
"v_mfma_f32_16x16x32_bf16 v[204:207], a[80:83], a[104:107], v[204:207]\n"
"ds_read_b128 a[104:107], v12 offset:18432\n"
"v_mfma_f32_16x16x32_bf16 v[208:211], a[80:83], a[112:115], v[208:211]\n"
"s_sub_u32 s67, 31744, s60\n"
"ds_read_b64_tr_b16 a[42:43], v9 offset:8712\n"
"v_mfma_f32_16x16x32_bf16 v[208:211], a[84:87], a[116:119], v[208:211]\n"
"ds_read_b64_tr_b16 a[44:45], v9 offset:12296\n"
"v_mfma_f32_16x16x32_bf16 v[236:239], a[92:95], a[116:119], v[236:239]\n"
"ds_read_b128 a[116:119], v13 offset:20480\n"
"buffer_load_dwordx4 v6, s[48:51], s67 offen offset:2048  lds\n"
"v_mfma_f32_16x16x32_bf16 v[236:239], a[88:91], a[112:115], v[236:239]\n"
"ds_read_b128 a[112:115], v12 offset:20480\n"
"v_mfma_f32_16x16x32_bf16 v[240:243], a[88:91], a[120:123], v[240:243]\n"
"s_sub_u32 s67, 47104, s60\n"
"ds_read_b64_tr_b16 a[46:47], v9 offset:12808\n"
"v_mfma_f32_16x16x32_bf16 v[240:243], a[92:95], a[124:127], v[240:243]\n"
"ds_read_b64_tr_b16 a[48:49], v15 offset:8192\n"
"v_mfma_f32_16x16x32_bf16 v[212:215], a[84:87], a[124:127], v[212:215]\n"
"ds_read_b128 a[124:127], v13 offset:22528\n"
"v_mfma_f32_16x16x32_bf16 v[212:215], a[80:83], a[120:123], v[212:215]\n"
"ds_read_b128 a[120:123], v12 offset:22528\n"
"buffer_load_dwordx4 v6, s[48:51], s67 offen offset:3072  lds\n"
"v_mfma_f32_16x16x32_bf16 v[216:219], a[80:83], a[128:131], v[216:219]\n"
"ds_read_b64_tr_b16 a[50:51], v15 offset:8704\n"
"v_mfma_f32_16x16x32_bf16 v[216:219], a[84:87], a[132:135], v[216:219]\n"
"ds_read_b64_tr_b16 a[52:53], v15 offset:12288\n"
"v_mfma_f32_16x16x32_bf16 v[244:247], a[92:95], a[132:135], v[244:247]\n"
"ds_read_b128 a[132:135], v13 offset:24576\n"
"v_mfma_f32_16x16x32_bf16 v[244:247], a[88:91], a[128:131], v[244:247]\n"
"s_add_u32 m0, s64, 8192\n"
"ds_read_b128 a[128:131], v12 offset:24576\n"
"v_mfma_f32_16x16x32_bf16 v[248:251], a[88:91], a[136:139], v[248:251]\n"
"ds_read_b64_tr_b16 a[54:55], v15 offset:12800\n"
"v_mfma_f32_16x16x32_bf16 v[248:251], a[92:95], a[140:143], v[248:251]\n"
"ds_read_b64_tr_b16 a[56:57], v15 offset:8200\n"
"v_mfma_f32_16x16x32_bf16 v[220:223], a[84:87], a[140:143], v[220:223]\n"
"ds_read_b128 a[140:143], v13 offset:26624\n"
"v_mfma_f32_16x16x32_bf16 v[220:223], a[80:83], a[136:139], v[220:223]\n"
"ds_read_b128 a[136:139], v12 offset:26624\n"
"v_mfma_f32_16x16x32_bf16 v[224:227], a[80:83], a[144:147], v[224:227]\n"
"ds_read_b64_tr_b16 a[58:59], v15 offset:8712\n"
"v_mfma_f32_16x16x32_bf16 v[224:227], a[84:87], a[148:151], v[224:227]\n"
"ds_read_b64_tr_b16 a[60:61], v15 offset:12296\n"
"v_mfma_f32_16x16x32_bf16 v[252:255], a[92:95], a[148:151], v[252:255]\n"
"s_sub_u32 s67, 1048576, s75\n"
"ds_read_b128 a[148:151], v13 offset:28672\n"
"v_mfma_f32_16x16x32_bf16 v[252:255], a[88:91], a[144:147], v[252:255]\n"
"ds_read_b128 a[144:147], v12 offset:28672\n"
"ds_read_b64_tr_b16 a[62:63], v15 offset:12808\n"
"v_mfma_f32_16x16x32_bf16 a[28:31], a[88:91], a[152:155], a[28:31]\n"
"v_mfma_f32_16x16x32_bf16 a[28:31], a[92:95], a[156:159], a[28:31]\n"
"v_mfma_f32_16x16x32_bf16 a[24:27], a[84:87], a[156:159], a[24:27]\n"
"ds_read_b128 a[156:159], v13 offset:30720\n"
"v_mfma_f32_16x16x32_bf16 a[24:27], a[80:83], a[152:155], a[24:27]\n"
"s_waitcnt lgkmcnt(15)\n"
"ds_read_b128 a[152:155], v12 offset:30720\n"
"v_mfma_f32_16x16x32_bf16 v[32:35], a[32:35], a[96:99], v[32:35]\n"
"v_mfma_f32_16x16x32_bf16 v[32:35], a[36:39], a[100:103], v[32:35]\n"
"s_waitcnt lgkmcnt(2)\n"
"v_mfma_f32_16x16x32_bf16 v[60:63], a[44:47], a[100:103], v[60:63]\n"
"buffer_load_dwordx4 v11, s[52:55], s67 offen offset:0  lds\n"
"v_mfma_f32_16x16x32_bf16 v[60:63], a[40:43], a[96:99], v[60:63]\n"
"v_mfma_f32_16x16x32_bf16 v[64:67], a[40:43], a[104:107], v[64:67]\n"
"s_sub_u32 s67, 1063936, s75\n"
"v_mfma_f32_16x16x32_bf16 v[64:67], a[44:47], a[108:111], v[64:67]\n"
"v_mfma_f32_16x16x32_bf16 v[36:39], a[36:39], a[108:111], v[36:39]\n"
"buffer_load_dwordx4 v11, s[52:55], s67 offen offset:1024  lds\n"
"v_mfma_f32_16x16x32_bf16 v[36:39], a[32:35], a[104:107], v[36:39]\n"
"v_mfma_f32_16x16x32_bf16 v[40:43], a[32:35], a[112:115], v[40:43]\n"
"s_sub_u32 s67, 1079296, s75\n"
"v_mfma_f32_16x16x32_bf16 v[40:43], a[36:39], a[116:119], v[40:43]\n"
"v_mfma_f32_16x16x32_bf16 v[68:71], a[44:47], a[116:119], v[68:71]\n"
"v_mfma_f32_16x16x32_bf16 v[68:71], a[40:43], a[112:115], v[68:71]\n"
"L_st2137:\n"
"s_cbranch_execz L_st4488\n"
"buffer_load_dwordx4 v11, s[52:55], s67 offen offset:2048  lds\n"
"v_mfma_f32_16x16x32_bf16 v[72:75], a[40:43], a[120:123], v[72:75]\n"
"v_mfma_f32_16x16x32_bf16 v[72:75], a[44:47], a[124:127], v[72:75]\n"
"s_sub_u32 s67, 1094656, s75\n"
"v_mfma_f32_16x16x32_bf16 v[44:47], a[36:39], a[124:127], v[44:47]\n"
"v_mfma_f32_16x16x32_bf16 v[44:47], a[32:35], a[120:123], v[44:47]\n"
"buffer_load_dwordx4 v11, s[52:55], s67 offen offset:3072  lds\n"
"v_mfma_f32_16x16x32_bf16 v[48:51], a[32:35], a[128:131], v[48:51]\n"
"v_mfma_f32_16x16x32_bf16 v[48:51], a[36:39], a[132:135], v[48:51]\n"
"s_add_u32 m0, s64, 12288\n"
"v_mfma_f32_16x16x32_bf16 v[76:79], a[44:47], a[132:135], v[76:79]\n"
"s_waitcnt vmcnt(12)\n"
"ds_read_b64_tr_b16 a[64:65], v9 offset:0\n"
"v_mfma_f32_16x16x32_bf16 v[76:79], a[40:43], a[128:131], v[76:79]\n"
"s_sub_u32 s67, 1114112, s75\n"
"ds_read_b64_tr_b16 a[66:67], v9 offset:512\n"
"v_mfma_f32_16x16x32_bf16 v[80:83], a[40:43], a[136:139], v[80:83]\n"
"ds_read_b64_tr_b16 a[68:69], v9 offset:4096\n"
"v_mfma_f32_16x16x32_bf16 v[80:83], a[44:47], a[140:143], v[80:83]\n"
"ds_read_b64_tr_b16 a[70:71], v9 offset:4608\n"
"buffer_load_dwordx4 v11, s[52:55], s67 offen offset:0  lds\n"
"v_mfma_f32_16x16x32_bf16 v[52:55], a[36:39], a[140:143], v[52:55]\n"
"s_sub_u32 s67, 1129472, s75\n"
"ds_read_b64_tr_b16 a[72:73], v9 offset:8\n"
"v_mfma_f32_16x16x32_bf16 v[52:55], a[32:35], a[136:139], v[52:55]\n"
"s_waitcnt lgkmcnt(5)\n"
"ds_read_b64_tr_b16 a[74:75], v9 offset:520\n"
"v_mfma_f32_16x16x32_bf16 v[56:59], a[32:35], a[144:147], v[56:59]\n"
"ds_read_b64_tr_b16 a[76:77], v9 offset:4104\n"
"v_mfma_f32_16x16x32_bf16 v[56:59], a[36:39], a[148:151], v[56:59]\n"
"ds_read_b64_tr_b16 a[78:79], v9 offset:4616\n"
"v_mfma_f32_16x16x32_bf16 v[84:87], a[44:47], a[148:151], v[84:87]\n"
"buffer_load_dwordx4 v11, s[52:55], s67 offen offset:1024  lds\n"
"v_mfma_f32_16x16x32_bf16 v[84:87], a[40:43], a[144:147], v[84:87]\n"
"ds_read_b64_tr_b16 a[80:81], v15 offset:0\n"
"v_mfma_f32_16x16x32_bf16 a[4:7], a[40:43], a[152:155], a[4:7]\n"
"s_sub_u32 s67, 1144832, s75\n"
"ds_read_b64_tr_b16 a[82:83], v15 offset:512\n"
"v_mfma_f32_16x16x32_bf16 a[4:7], a[44:47], a[156:159], a[4:7]\n"
"ds_read_b64_tr_b16 a[84:85], v15 offset:4096\n"
"v_mfma_f32_16x16x32_bf16 a[0:3], a[36:39], a[156:159], a[0:3]\n"
"ds_read_b64_tr_b16 a[86:87], v15 offset:4608\n"
"buffer_load_dwordx4 v11, s[52:55], s67 offen offset:2048  lds\n"
"v_mfma_f32_16x16x32_bf16 a[0:3], a[32:35], a[152:155], a[0:3]\n"
"ds_read_b64_tr_b16 a[88:89], v15 offset:8\n"
"v_mfma_f32_16x16x32_bf16 v[88:91], a[48:51], a[96:99], v[88:91]\n"
"s_sub_u32 s67, 1160192, s75\n"
"ds_read_b64_tr_b16 a[90:91], v15 offset:520\n"
"v_mfma_f32_16x16x32_bf16 v[88:91], a[52:55], a[100:103], v[88:91]\n"
"ds_read_b64_tr_b16 a[92:93], v15 offset:4104\n"
"v_mfma_f32_16x16x32_bf16 v[116:119], a[60:63], a[100:103], v[116:119]\n"
"ds_read_b64_tr_b16 a[94:95], v15 offset:4616\n"
"v_mfma_f32_16x16x32_bf16 v[116:119], a[56:59], a[96:99], v[116:119]\n"
"buffer_load_dwordx4 v11, s[52:55], s67 offen offset:3072  lds\n"
"v_mfma_f32_16x16x32_bf16 v[120:123], a[56:59], a[104:107], v[120:123]\n"
"v_mfma_f32_16x16x32_bf16 v[120:123], a[60:63], a[108:111], v[120:123]\n"
"s_add_u32 m0, s64, 0\n"
"v_mfma_f32_16x16x32_bf16 v[92:95], a[52:55], a[108:111], v[92:95]\n"
"v_mfma_f32_16x16x32_bf16 v[92:95], a[48:51], a[104:107], v[92:95]\n"
"s_sub_u32 s67, 1048704, s75\n"
"v_mfma_f32_16x16x32_bf16 v[96:99], a[48:51], a[112:115], v[96:99]\n"
"s_waitcnt lgkmcnt(0)\n"
"v_mfma_f32_16x16x32_bf16 v[96:99], a[52:55], a[116:119], v[96:99]\n"
"buffer_load_dwordx4 v11, s[52:55], s67 offen offset:0  lds\n"
"v_mfma_f32_16x16x32_bf16 v[124:127], a[60:63], a[116:119], v[124:127]\n"
"v_mfma_f32_16x16x32_bf16 v[124:127], a[56:59], a[112:115], v[124:127]\n"
"s_sub_u32 s67, 1064064, s75\n"
"v_mfma_f32_16x16x32_bf16 v[128:131], a[56:59], a[120:123], v[128:131]\n"
"v_mfma_f32_16x16x32_bf16 v[128:131], a[60:63], a[124:127], v[128:131]\n"
"v_mfma_f32_16x16x32_bf16 v[100:103], a[52:55], a[124:127], v[100:103]\n"
"buffer_load_dwordx4 v11, s[52:55], s67 offen offset:1024  lds\n"
"v_mfma_f32_16x16x32_bf16 v[100:103], a[48:51], a[120:123], v[100:103]\n"
"v_mfma_f32_16x16x32_bf16 v[104:107], a[48:51], a[128:131], v[104:107]\n"
"s_sub_u32 s67, 1079424, s75\n"
"v_mfma_f32_16x16x32_bf16 v[104:107], a[52:55], a[132:135], v[104:107]\n"
"v_mfma_f32_16x16x32_bf16 v[132:135], a[60:63], a[132:135], v[132:135]\n"
"v_mfma_f32_16x16x32_bf16 v[132:135], a[56:59], a[128:131], v[132:135]\n"
"buffer_load_dwordx4 v11, s[52:55], s67 offen offset:2048  lds\n"
"v_mfma_f32_16x16x32_bf16 v[136:139], a[56:59], a[136:139], v[136:139]\n"
"v_mfma_f32_16x16x32_bf16 v[136:139], a[60:63], a[140:143], v[136:139]\n"
"s_sub_u32 s67, 1094784, s75\n"
"v_mfma_f32_16x16x32_bf16 v[108:111], a[52:55], a[140:143], v[108:111]\n"
"v_mfma_f32_16x16x32_bf16 v[108:111], a[48:51], a[136:139], v[108:111]\n"
"buffer_load_dwordx4 v11, s[52:55], s67 offen offset:3072  lds\n"
"v_mfma_f32_16x16x32_bf16 v[112:115], a[48:51], a[144:147], v[112:115]\n"
"v_mfma_f32_16x16x32_bf16 v[112:115], a[52:55], a[148:151], v[112:115]\n"
"s_add_u32 m0, s64, 4096\n"
"v_mfma_f32_16x16x32_bf16 v[140:143], a[60:63], a[148:151], v[140:143]\n"
"v_mfma_f32_16x16x32_bf16 v[140:143], a[56:59], a[144:147], v[140:143]\n"
"s_sub_u32 s67, 1114240, s75\n"
"v_mfma_f32_16x16x32_bf16 a[12:15], a[56:59], a[152:155], a[12:15]\n"
"v_mfma_f32_16x16x32_bf16 a[12:15], a[60:63], a[156:159], a[12:15]\n"
"buffer_load_dwordx4 v11, s[52:55], s67 offen offset:0  lds\n"
"v_mfma_f32_16x16x32_bf16 a[8:11], a[52:55], a[156:159], a[8:11]\n"
"v_mfma_f32_16x16x32_bf16 a[8:11], a[48:51], a[152:155], a[8:11]\n"
"s_sub_u32 s67, 1129600, s75\n"
"v_mfma_f32_16x16x32_bf16 v[144:147], a[64:67], a[96:99], v[144:147]\n"
"buffer_load_dwordx4 v11, s[52:55], s67 offen offset:1024  lds\n"
"v_mfma_f32_16x16x32_bf16 v[144:147], a[68:71], a[100:103], v[144:147]\n"
"s_sub_u32 s71, s72, s64\n"
"v_mfma_f32_16x16x32_bf16 v[172:175], a[76:79], a[100:103], v[172:175]\n"
"s_sub_u32 s67, 1144960, s75\n"
"v_mfma_f32_16x16x32_bf16 v[172:175], a[72:75], a[96:99], v[172:175]\n"
"v_mfma_f32_16x16x32_bf16 v[176:179], a[72:75], a[104:107], v[176:179]\n"
"s_add_u32 s71, s71, 57344\n"
"v_mfma_f32_16x16x32_bf16 v[176:179], a[76:79], a[108:111], v[176:179]\n"
"buffer_load_dwordx4 v11, s[52:55], s67 offen offset:2048  lds\n"
"v_mfma_f32_16x16x32_bf16 v[148:151], a[68:71], a[108:111], v[148:151]\n"
"v_add_u32 v20, s71, v9\n"
"v_mfma_f32_16x16x32_bf16 v[148:151], a[64:67], a[104:107], v[148:151]\n"
"s_sub_u32 s67, 1160320, s75\n"
"v_mfma_f32_16x16x32_bf16 v[152:155], a[64:67], a[112:115], v[152:155]\n"
"s_sub_u32 s66, 0, s73\n"
"v_mfma_f32_16x16x32_bf16 v[152:155], a[68:71], a[116:119], v[152:155]\n"
"v_add_u32 v21, s71, v15\n"
"v_mfma_f32_16x16x32_bf16 v[180:183], a[76:79], a[116:119], v[180:183]\n"
"v_add_u32 v12, s66, v7\n"
"v_mfma_f32_16x16x32_bf16 v[180:183], a[72:75], a[112:115], v[180:183]\n"
"buffer_load_dwordx4 v11, s[52:55], s67 offen offset:3072  lds\n"
"v_mfma_f32_16x16x32_bf16 v[184:187], a[72:75], a[120:123], v[184:187]\n"
"v_add_u32 v13, s66, v8\n"
"v_mfma_f32_16x16x32_bf16 v[184:187], a[76:79], a[124:127], v[184:187]\n"
"s_waitcnt vmcnt(20)\n"
"ds_read_b64_tr_b16 a[160:161], v20 offset:0\n"
"v_mfma_f32_16x16x32_bf16 v[156:159], a[68:71], a[124:127], v[156:159]\n"
"ds_read_b64_tr_b16 a[162:163], v20 offset:512\n"
"v_mfma_f32_16x16x32_bf16 v[156:159], a[64:67], a[120:123], v[156:159]\n"
"ds_read_b64_tr_b16 a[164:165], v20 offset:8\n"
"v_mfma_f32_16x16x32_bf16 v[160:163], a[64:67], a[128:131], v[160:163]\n"
"ds_read_b64_tr_b16 a[166:167], v20 offset:520\n"
"v_mfma_f32_16x16x32_bf16 v[160:163], a[68:71], a[132:135], v[160:163]\n"
"s_cmp_lt_u32 %16, 2\n"
"s_cselect_b32 s67, 16384, 0\n"
"v_mfma_f32_16x16x32_bf16 v[188:191], a[76:79], a[132:135], v[188:191]\n"
"s_add_u32 s67, s67, s72\n"
"ds_read_b64_tr_b16 a[168:169], v21 offset:0\n"
"v_mfma_f32_16x16x32_bf16 v[188:191], a[72:75], a[128:131], v[188:191]\n"
"ds_read_b64_tr_b16 a[170:171], v21 offset:512\n"
"v_mfma_f32_16x16x32_bf16 v[192:195], a[72:75], a[136:139], v[192:195]\n"
"ds_read_b64_tr_b16 a[172:173], v21 offset:8\n"
"v_mfma_f32_16x16x32_bf16 v[192:195], a[76:79], a[140:143], v[192:195]\n"
"s_add_u32 m0, s67, 57344\n"
"v_mfma_f32_16x16x32_bf16 v[164:167], a[68:71], a[140:143], v[164:167]\n"
"ds_read_b64_tr_b16 a[174:175], v21 offset:520\n"
"v_mfma_f32_16x16x32_bf16 v[164:167], a[64:67], a[136:139], v[164:167]\n"
"v_mfma_f32_16x16x32_bf16 v[168:171], a[64:67], a[144:147], v[168:171]\n"
"v_mfma_f32_16x16x32_bf16 v[168:171], a[68:71], a[148:151], v[168:171]\n"
"s_sub_u32 s67, 1280, s60\n"
"v_mfma_f32_16x16x32_bf16 v[196:199], a[76:79], a[148:151], v[196:199]\n"
"v_mfma_f32_16x16x32_bf16 v[196:199], a[72:75], a[144:147], v[196:199]\n"
"v_mfma_f32_16x16x32_bf16 a[20:23], a[72:75], a[152:155], a[20:23]\n"
"s_waitcnt lgkmcnt(0)\n"
"v_mfma_f32_16x16x32_bf16 a[20:23], a[76:79], a[156:159], a[20:23]\n"
"buffer_load_dwordx4 v6, s[48:51], s67 offen offset:0  lds\n"
"v_mfma_f32_16x16x32_bf16 a[16:19], a[68:71], a[156:159], a[16:19]\n"
"s_sub_u32 s67, 16640, s60\n"
"v_mfma_f32_16x16x32_bf16 a[16:19], a[64:67], a[152:155], a[16:19]\n"
"s_waitcnt vmcnt(17)\n"
"s_barrier\n"
"v_mfma_f32_16x16x32_bf16 v[200:203], a[80:83], a[96:99], v[200:203]\n"
"s_waitcnt vmcnt(9)\n"
"ds_read_b64_tr_b16 a[32:33], v9 offset:8192\n"
"v_mfma_f32_16x16x32_bf16 v[200:203], a[84:87], a[100:103], v[200:203]\n"
"ds_read_b64_tr_b16 a[34:35], v9 offset:8704\n"
"v_mfma_f32_16x16x32_bf16 v[228:231], a[92:95], a[100:103], v[228:231]\n"
"ds_read_b128 a[100:103], v13 offset:32768\n"
"buffer_load_dwordx4 v6, s[48:51], s67 offen offset:1024  lds\n"
"v_mfma_f32_16x16x32_bf16 v[228:231], a[88:91], a[96:99], v[228:231]\n"
"ds_read_b128 a[96:99], v12 offset:32768\n"
"v_mfma_f32_16x16x32_bf16 v[232:235], a[88:91], a[104:107], v[232:235]\n"
"s_sub_u32 s67, 32000, s60\n"
"ds_read_b64_tr_b16 a[36:37], v9 offset:12288\n"
"v_mfma_f32_16x16x32_bf16 v[232:235], a[92:95], a[108:111], v[232:235]\n"
"ds_read_b64_tr_b16 a[38:39], v9 offset:12800\n"
"v_mfma_f32_16x16x32_bf16 v[204:207], a[84:87], a[108:111], v[204:207]\n"
"ds_read_b128 a[108:111], v13 offset:34816\n"
"buffer_load_dwordx4 v6, s[48:51], s67 offen offset:2048  lds\n"
"v_mfma_f32_16x16x32_bf16 v[204:207], a[80:83], a[104:107], v[204:207]\n"
"ds_read_b128 a[104:107], v12 offset:34816\n"
"ds_read_b64_tr_b16 a[40:41], v9 offset:8200\n"
"v_mfma_f32_16x16x32_bf16 v[208:211], a[80:83], a[112:115], v[208:211]\n"
"s_sub_u32 s67, 47360, s60\n"
"ds_read_b64_tr_b16 a[42:43], v9 offset:8712\n"
"v_mfma_f32_16x16x32_bf16 v[208:211], a[84:87], a[116:119], v[208:211]\n"
"ds_read_b64_tr_b16 a[44:45], v9 offset:12296\n"
"v_mfma_f32_16x16x32_bf16 v[236:239], a[92:95], a[116:119], v[236:239]\n"
"ds_read_b128 a[116:119], v13 offset:36864\n"
"v_mfma_f32_16x16x32_bf16 v[236:239], a[88:91], a[112:115], v[236:239]\n"
"ds_read_b128 a[112:115], v12 offset:36864\n"
"buffer_load_dwordx4 v6, s[48:51], s67 offen offset:3072  lds\n"
"v_mfma_f32_16x16x32_bf16 v[240:243], a[88:91], a[120:123], v[240:243]\n"
"ds_read_b64_tr_b16 a[46:47], v9 offset:12808\n"
"v_mfma_f32_16x16x32_bf16 v[240:243], a[92:95], a[124:127], v[240:243]\n"
"s_add_u32 m0, s64, 20352\n"
"ds_read_b64_tr_b16 a[48:49], v15 offset:8192\n"
"v_mfma_f32_16x16x32_bf16 v[212:215], a[84:87], a[124:127], v[212:215]\n"
"ds_read_b128 a[124:127], v13 offset:38912\n"
"v_mfma_f32_16x16x32_bf16 v[212:215], a[80:83], a[120:123], v[212:215]\n"
"ds_read_b128 a[120:123], v12 offset:38912\n"
"v_mfma_f32_16x16x32_bf16 v[216:219], a[80:83], a[128:131], v[216:219]\n"
"ds_read_b64_tr_b16 a[50:51], v15 offset:8704\n"
"buffer_load_dwordx4 v6, s[40:43], s84 offen offset:128  lds\n"
"v_mfma_f32_16x16x32_bf16 v[216:219], a[84:87], a[132:135], v[216:219]\n"
"ds_read_b64_tr_b16 a[52:53], v15 offset:12288\n"
"v_mfma_f32_16x16x32_bf16 v[244:247], a[92:95], a[132:135], v[244:247]\n"
"ds_read_b128 a[132:135], v13 offset:24576\n"
"v_mfma_f32_16x16x32_bf16 v[244:247], a[88:91], a[128:131], v[244:247]\n"
"ds_read_b128 a[128:131], v12 offset:24576\n"
"buffer_load_dwordx4 v6, s[40:43], s85 offen offset:1152  lds\n"
"v_mfma_f32_16x16x32_bf16 v[248:251], a[88:91], a[136:139], v[248:251]\n"
"ds_read_b64_tr_b16 a[54:55], v15 offset:12800\n"
"v_mfma_f32_16x16x32_bf16 v[248:251], a[92:95], a[140:143], v[248:251]\n"
"ds_read_b64_tr_b16 a[56:57], v15 offset:8200\n"
"v_mfma_f32_16x16x32_bf16 v[220:223], a[84:87], a[140:143], v[220:223]\n"
"ds_read_b128 a[140:143], v13 offset:26624\n"
"v_mfma_f32_16x16x32_bf16 v[220:223], a[80:83], a[136:139], v[220:223]\n"
"ds_read_b128 a[136:139], v12 offset:26624\n"
"v_mfma_f32_16x16x32_bf16 v[224:227], a[80:83], a[144:147], v[224:227]\n"
"ds_read_b64_tr_b16 a[58:59], v15 offset:8712\n"
"buffer_load_dwordx4 v6, s[40:43], s86 offen offset:2176  lds\n"
"v_mfma_f32_16x16x32_bf16 v[224:227], a[84:87], a[148:151], v[224:227]\n"
"ds_read_b64_tr_b16 a[60:61], v15 offset:12296\n"
"v_mfma_f32_16x16x32_bf16 v[252:255], a[92:95], a[148:151], v[252:255]\n"
"ds_read_b128 a[148:151], v13 offset:28672\n"
"v_mfma_f32_16x16x32_bf16 v[252:255], a[88:91], a[144:147], v[252:255]\n"
"ds_read_b128 a[144:147], v12 offset:28672\n"
"ds_read_b64_tr_b16 a[62:63], v15 offset:12808\n"
"v_mfma_f32_16x16x32_bf16 a[28:31], a[88:91], a[152:155], a[28:31]\n"
"buffer_load_dwordx4 v6, s[40:43], s87 offen offset:3200  lds\n"
"v_mfma_f32_16x16x32_bf16 a[28:31], a[92:95], a[156:159], a[28:31]\n"
"v_mfma_f32_16x16x32_bf16 a[24:27], a[84:87], a[156:159], a[24:27]\n"
"s_mov_b32 s38, 1572864\n"
"ds_read_b128 a[156:159], v13 offset:30720\n"
"v_mfma_f32_16x16x32_bf16 a[24:27], a[80:83], a[152:155], a[24:27]\n"
"s_waitcnt lgkmcnt(15)\n"
"ds_read_b128 a[152:155], v12 offset:30720\n"
"v_mfma_f32_16x16x32_bf16 v[32:35], a[32:35], a[96:99], v[32:35]\n"
"v_mfma_f32_16x16x32_bf16 v[32:35], a[36:39], a[100:103], v[32:35]\n"
"s_waitcnt lgkmcnt(2)\n"
"v_mfma_f32_16x16x32_bf16 v[60:63], a[44:47], a[100:103], v[60:63]\n"
"buffer_store_dwordx4 a[160:163], v0, s[36:39], %23 offen offset:0 sc1\n"
"v_mfma_f32_16x16x32_bf16 v[60:63], a[40:43], a[96:99], v[60:63]\n"
"v_mfma_f32_16x16x32_bf16 v[64:67], a[40:43], a[104:107], v[64:67]\n"
"s_add_u32 m0, s64, 8192\n"
"v_mfma_f32_16x16x32_bf16 v[64:67], a[44:47], a[108:111], v[64:67]\n"
"v_mfma_f32_16x16x32_bf16 v[36:39], a[36:39], a[108:111], v[36:39]\n"
"s_sub_u32 s67, 1310720, s75\n"
"v_mfma_f32_16x16x32_bf16 v[36:39], a[32:35], a[104:107], v[36:39]\n"
"v_mfma_f32_16x16x32_bf16 v[40:43], a[32:35], a[112:115], v[40:43]\n"
"buffer_load_dwordx4 v11, s[52:55], s67 offen offset:0  lds\n"
"v_mfma_f32_16x16x32_bf16 v[40:43], a[36:39], a[116:119], v[40:43]\n"
"v_mfma_f32_16x16x32_bf16 v[68:71], a[44:47], a[116:119], v[68:71]\n"
"s_sub_u32 s67, 1326080, s75\n"
"v_mfma_f32_16x16x32_bf16 v[68:71], a[40:43], a[112:115], v[68:71]\n"
"v_mfma_f32_16x16x32_bf16 v[72:75], a[40:43], a[120:123], v[72:75]\n"
"buffer_load_dwordx4 v11, s[52:55], s67 offen offset:1024  lds\n"
"v_mfma_f32_16x16x32_bf16 v[72:75], a[44:47], a[124:127], v[72:75]\n"
"v_mfma_f32_16x16x32_bf16 v[44:47], a[36:39], a[124:127], v[44:47]\n"
"s_sub_u32 s67, 1341440, s75\n"
"v_mfma_f32_16x16x32_bf16 v[44:47], a[32:35], a[120:123], v[44:47]\n"
"v_mfma_f32_16x16x32_bf16 v[48:51], a[32:35], a[128:131], v[48:51]\n"
"buffer_load_dwordx4 v11, s[52:55], s67 offen offset:2048  lds\n"
"v_mfma_f32_16x16x32_bf16 v[48:51], a[36:39], a[132:135], v[48:51]\n"
"s_sub_u32 s67, 1356800, s75\n"
"v_mfma_f32_16x16x32_bf16 v[76:79], a[44:47], a[132:135], v[76:79]\n"
"s_waitcnt vmcnt(12)\n"
"ds_read_b64_tr_b16 a[64:65], v9 offset:0\n"
"v_mfma_f32_16x16x32_bf16 v[76:79], a[40:43], a[128:131], v[76:79]\n"
"ds_read_b64_tr_b16 a[66:67], v9 offset:512\n"
"v_mfma_f32_16x16x32_bf16 v[80:83], a[40:43], a[136:139], v[80:83]\n"
"ds_read_b64_tr_b16 a[68:69], v9 offset:4096\n"
"buffer_load_dwordx4 v11, s[52:55], s67 offen offset:3072  lds\n"
"v_mfma_f32_16x16x32_bf16 v[80:83], a[44:47], a[140:143], v[80:83]\n"
"ds_read_b64_tr_b16 a[70:71], v9 offset:4608\n"
"v_mfma_f32_16x16x32_bf16 v[52:55], a[36:39], a[140:143], v[52:55]\n"
"s_add_u32 m0, s64, 12288\n"
"ds_read_b64_tr_b16 a[72:73], v9 offset:8\n"
"v_mfma_f32_16x16x32_bf16 v[52:55], a[32:35], a[136:139], v[52:55]\n"
"s_waitcnt lgkmcnt(5)\n"
"ds_read_b64_tr_b16 a[74:75], v9 offset:520\n"
"v_mfma_f32_16x16x32_bf16 v[56:59], a[32:35], a[144:147], v[56:59]\n"
"s_sub_u32 s67, 1376256, s75\n"
"ds_read_b64_tr_b16 a[76:77], v9 offset:4104\n"
"v_mfma_f32_16x16x32_bf16 v[56:59], a[36:39], a[148:151], v[56:59]\n"
"ds_read_b64_tr_b16 a[78:79], v9 offset:4616\n"
"v_mfma_f32_16x16x32_bf16 v[84:87], a[44:47], a[148:151], v[84:87]\n"
"buffer_load_dwordx4 v11, s[52:55], s67 offen offset:0  lds\n"
"v_mfma_f32_16x16x32_bf16 v[84:87], a[40:43], a[144:147], v[84:87]\n"
"ds_read_b64_tr_b16 a[80:81], v15 offset:0\n"
"v_mfma_f32_16x16x32_bf16 a[4:7], a[40:43], a[152:155], a[4:7]\n"
"s_sub_u32 s67, 1391616, s75\n"
"ds_read_b64_tr_b16 a[82:83], v15 offset:512\n"
"v_mfma_f32_16x16x32_bf16 a[4:7], a[44:47], a[156:159], a[4:7]\n"
"ds_read_b64_tr_b16 a[84:85], v15 offset:4096\n"
"v_mfma_f32_16x16x32_bf16 a[0:3], a[36:39], a[156:159], a[0:3]\n"
"ds_read_b64_tr_b16 a[86:87], v15 offset:4608\n"
"buffer_load_dwordx4 v11, s[52:55], s67 offen offset:1024  lds\n"
"v_mfma_f32_16x16x32_bf16 a[0:3], a[32:35], a[152:155], a[0:3]\n"
"ds_read_b64_tr_b16 a[88:89], v15 offset:8\n"
"v_mfma_f32_16x16x32_bf16 v[88:91], a[48:51], a[96:99], v[88:91]\n"
"s_sub_u32 s67, 1406976, s75\n"
"ds_read_b64_tr_b16 a[90:91], v15 offset:520\n"
"v_mfma_f32_16x16x32_bf16 v[88:91], a[52:55], a[100:103], v[88:91]\n"
"ds_read_b64_tr_b16 a[92:93], v15 offset:4104\n"
"v_mfma_f32_16x16x32_bf16 v[116:119], a[60:63], a[100:103], v[116:119]\n"
"ds_read_b64_tr_b16 a[94:95], v15 offset:4616\n"
"v_mfma_f32_16x16x32_bf16 v[116:119], a[56:59], a[96:99], v[116:119]\n"
"buffer_load_dwordx4 v11, s[52:55], s67 offen offset:2048  lds\n"
"v_mfma_f32_16x16x32_bf16 v[120:123], a[56:59], a[104:107], v[120:123]\n"
"v_mfma_f32_16x16x32_bf16 v[120:123], a[60:63], a[108:111], v[120:123]\n"
"s_sub_u32 s67, 1422336, s75\n"
"v_mfma_f32_16x16x32_bf16 v[92:95], a[52:55], a[108:111], v[92:95]\n"
"v_mfma_f32_16x16x32_bf16 v[92:95], a[48:51], a[104:107], v[92:95]\n"
"buffer_load_dwordx4 v11, s[52:55], s67 offen offset:3072  lds\n"
"v_mfma_f32_16x16x32_bf16 v[96:99], a[48:51], a[112:115], v[96:99]\n"
"s_waitcnt lgkmcnt(0)\n"
"s_mov_b32 s99, 0\n"
"v_mfma_f32_16x16x32_bf16 v[96:99], a[52:55], a[116:119], v[96:99]\n"
"s_mov_b32 s98, 0\n"
"s_add_u32 m0, s64, 0\n"
"v_mfma_f32_16x16x32_bf16 v[124:127], a[60:63], a[116:119], v[124:127]\n"
"s_memtime s[76:77]\n"
"s_load_dword s98, s[96:97], 0 glc\n"
"v_mfma_f32_16x16x32_bf16 v[124:127], a[56:59], a[112:115], v[124:127]\n"
"s_sub_u32 s67, 1310848, s75\n"
"v_mfma_f32_16x16x32_bf16 v[128:131], a[56:59], a[120:123], v[128:131]\n"
"buffer_load_dwordx4 v11, s[52:55], s67 offen offset:0  lds\n"
"v_mfma_f32_16x16x32_bf16 v[128:131], a[60:63], a[124:127], v[128:131]\n"
"v_mfma_f32_16x16x32_bf16 v[100:103], a[52:55], a[124:127], v[100:103]\n"
"s_sub_u32 s67, 1326208, s75\n"
"v_mfma_f32_16x16x32_bf16 v[100:103], a[48:51], a[120:123], v[100:103]\n"
"v_mfma_f32_16x16x32_bf16 v[104:107], a[48:51], a[128:131], v[104:107]\n"
"buffer_load_dwordx4 v11, s[52:55], s67 offen offset:1024  lds\n"
"v_mfma_f32_16x16x32_bf16 v[104:107], a[52:55], a[132:135], v[104:107]\n"
"v_mfma_f32_16x16x32_bf16 v[132:135], a[60:63], a[132:135], v[132:135]\n"
"s_sub_u32 s67, 1341568, s75\n"
"v_mfma_f32_16x16x32_bf16 v[132:135], a[56:59], a[128:131], v[132:135]\n"
"s_mov_b32 s29, s76\n"
"v_mfma_f32_16x16x32_bf16 v[136:139], a[56:59], a[136:139], v[136:139]\n"
"v_mfma_f32_16x16x32_bf16 v[136:139], a[60:63], a[140:143], v[136:139]\n"
"buffer_load_dwordx4 v11, s[52:55], s67 offen offset:2048  lds\n"
"v_mfma_f32_16x16x32_bf16 v[108:111], a[52:55], a[140:143], v[108:111]\n"
"s_sub_u32 s99, s99, s98\n"
"s_sub_u32 s99, s99, s98\n"
"v_mfma_f32_16x16x32_bf16 v[108:111], a[48:51], a[136:139], v[108:111]\n"
"s_sub_u32 s99, s99, s98\n"
"s_sub_u32 s67, 1356928, s75\n"
"v_mfma_f32_16x16x32_bf16 v[112:115], a[48:51], a[144:147], v[112:115]\n"
"s_sub_u32 s99, s99, s98\n"
"v_mfma_f32_16x16x32_bf16 v[112:115], a[52:55], a[148:151], v[112:115]\n"
"buffer_load_dwordx4 v11, s[52:55], s67 offen offset:3072  lds\n"
"v_mfma_f32_16x16x32_bf16 v[140:143], a[60:63], a[148:151], v[140:143]\n"
"s_sub_u32 s99, s99, s98\n"
"s_sub_u32 s99, s99, s98\n"
"v_mfma_f32_16x16x32_bf16 v[140:143], a[56:59], a[144:147], v[140:143]\n"
"s_sub_u32 s99, s99, s98\n"
"s_add_u32 m0, s64, 4096\n"
"v_mfma_f32_16x16x32_bf16 a[12:15], a[56:59], a[152:155], a[12:15]\n"
"s_sub_u32 s99, s99, s98\n"
"s_sub_u32 s67, 1376384, s75\n"
"v_mfma_f32_16x16x32_bf16 a[12:15], a[60:63], a[156:159], a[12:15]\n"
"buffer_load_dwordx4 v11, s[52:55], s67 offen offset:0  lds\n"
"v_mfma_f32_16x16x32_bf16 a[8:11], a[52:55], a[156:159], a[8:11]\n"
"s_sub_u32 s99, s99, s98\n"
"s_sub_u32 s99, s99, s98\n"
"v_mfma_f32_16x16x32_bf16 a[8:11], a[48:51], a[152:155], a[8:11]\n"
"s_sub_u32 s99, s99, s98\n"
"s_sub_u32 s67, 1391744, s75\n"
"v_mfma_f32_16x16x32_bf16 v[144:147], a[64:67], a[96:99], v[144:147]\n"
"s_sub_u32 s99, s99, s98\n"
"v_mfma_f32_16x16x32_bf16 v[144:147], a[68:71], a[100:103], v[144:147]\n"
"buffer_load_dwordx4 v11, s[52:55], s67 offen offset:1024  lds\n"
"v_mfma_f32_16x16x32_bf16 v[172:175], a[76:79], a[100:103], v[172:175]\n"
"s_sub_u32 s99, s99, s98\n"
"s_sub_u32 s99, s99, s98\n"
"v_mfma_f32_16x16x32_bf16 v[172:175], a[72:75], a[96:99], v[172:175]\n"
"s_sub_u32 s99, s99, s98\n"
"s_sub_u32 s67, 1407104, s75\n"
"v_mfma_f32_16x16x32_bf16 v[176:179], a[72:75], a[104:107], v[176:179]\n"
"s_sub_u32 s99, s99, s98\n"
"v_mfma_f32_16x16x32_bf16 v[176:179], a[76:79], a[108:111], v[176:179]\n"
"v_mfma_f32_16x16x32_bf16 v[148:151], a[68:71], a[108:111], v[148:151]\n"
"s_sub_u32 s99, s99, s98\n"
"s_sub_u32 s99, s99, s98\n"
"v_mfma_f32_16x16x32_bf16 v[148:151], a[64:67], a[104:107], v[148:151]\n"
"s_sub_u32 s99, s99, s98\n"
"s_sub_u32 s71, s64, s64\n"
"buffer_load_dwordx4 v11, s[52:55], s67 offen offset:2048  lds\n"
"v_mfma_f32_16x16x32_bf16 v[152:155], a[64:67], a[112:115], v[152:155]\n"
"s_sub_u32 s66, 40960, s73\n"
"s_sub_u32 s99, s99, s98\n"
"v_mfma_f32_16x16x32_bf16 v[152:155], a[68:71], a[116:119], v[152:155]\n"
"s_add_u32 s71, s71, 20480\n"
"s_sub_u32 s67, 1422464, s75\n"
"v_mfma_f32_16x16x32_bf16 v[180:183], a[76:79], a[116:119], v[180:183]\n"
"s_sub_u32 s99, s99, s98\n"
"v_add_u32 v12, s66, v7\n"
"s_sub_u32 s99, s99, s98\n"
"v_mfma_f32_16x16x32_bf16 v[180:183], a[72:75], a[112:115], v[180:183]\n"
"s_sub_u32 s99, s99, s98\n"
"v_mfma_f32_16x16x32_bf16 v[184:187], a[72:75], a[120:123], v[184:187]\n"
"s_sub_u32 s99, s99, s98\n"
"v_add_u32 v13, s66, v8\n"
"v_mfma_f32_16x16x32_bf16 v[184:187], a[76:79], a[124:127], v[184:187]\n"
"s_memtime s[76:77]\n"
"buffer_load_dwordx4 v11, s[52:55], s67 offen offset:3072  lds\n"
"v_add_u32 v20, s71, v7\n"
"v_mfma_f32_16x16x32_bf16 v[156:159], a[68:71], a[124:127], v[156:159]\n"
"s_cmp_lt_u32 %16, 2\n"
"s_cselect_b32 s67, 16384, 0\n"
"s_add_u32 s67, s67, s72\n"
"v_mfma_f32_16x16x32_bf16 v[156:159], a[64:67], a[120:123], v[156:159]\n"
"s_waitcnt lgkmcnt(0)\n"
"v_add_u32 v21, s71, v8\n"
"s_add_u32 m0, s67, 98304\n"
"s_sub_u32 s67, 1536, s60\n"
"v_mfma_f32_16x16x32_bf16 v[160:163], a[64:67], a[128:131], v[160:163]\n"
"s_sub_u32 s29, s76, s29\n"
"buffer_load_dwordx4 v6, s[48:51], s67 offen offset:0  lds\n"
"v_mfma_f32_16x16x32_bf16 v[160:163], a[68:71], a[132:135], v[160:163]\n"
"s_cmp_lt_u32 s29, 620\n"
"s_cselect_b32 s99, s99, 0\n"
"v_mfma_f32_16x16x32_bf16 v[188:191], a[76:79], a[132:135], v[188:191]\n"
"s_max_u32 s30, s30, s99\n"
"s_sub_u32 s67, 16896, s60\n"
"v_mfma_f32_16x16x32_bf16 v[188:191], a[72:75], a[128:131], v[188:191]\n"
"s_waitcnt vmcnt(18)\n"
"ds_read_b128 a[208:211], v20 offset:0\n"
"v_mfma_f32_16x16x32_bf16 v[192:195], a[72:75], a[136:139], v[192:195]\n"
"ds_read_b128 a[212:215], v21 offset:0\n"
"v_mfma_f32_16x16x32_bf16 v[192:195], a[76:79], a[140:143], v[192:195]\n"
"ds_read_b128 a[216:219], v20 offset:2048\n"
"v_mfma_f32_16x16x32_bf16 v[164:167], a[68:71], a[140:143], v[164:167]\n"
"buffer_load_dwordx4 v6, s[48:51], s67 offen offset:1024  lds\n"
"v_mfma_f32_16x16x32_bf16 v[164:167], a[64:67], a[136:139], v[164:167]\n"
"ds_read_b128 a[220:223], v21 offset:2048\n"
"v_mfma_f32_16x16x32_bf16 v[168:171], a[64:67], a[144:147], v[168:171]\n"
"s_sub_u32 s67, 32256, s60\n"
"v_mfma_f32_16x16x32_bf16 v[168:171], a[68:71], a[148:151], v[168:171]\n"
"v_mfma_f32_16x16x32_bf16 v[196:199], a[76:79], a[148:151], v[196:199]\n"
"buffer_load_dwordx4 v6, s[48:51], s67 offen offset:2048  lds\n"
"v_mfma_f32_16x16x32_bf16 v[196:199], a[72:75], a[144:147], v[196:199]\n"
"v_mfma_f32_16x16x32_bf16 a[20:23], a[72:75], a[152:155], a[20:23]\n"
"s_sub_u32 s67, 47616, s60\n"
"v_mfma_f32_16x16x32_bf16 a[20:23], a[76:79], a[156:159], a[20:23]\n"
"s_waitcnt lgkmcnt(0)\n"
"v_mfma_f32_16x16x32_bf16 a[16:19], a[68:71], a[156:159], a[16:19]\n"
"buffer_load_dwordx4 v6, s[48:51], s67 offen offset:3072  lds\n"
"v_mfma_f32_16x16x32_bf16 a[16:19], a[64:67], a[152:155], a[16:19]\n"
"s_barrier\n"
"s_waitcnt vmcnt(12)\n"
"ds_read_b64_tr_b16 a[32:33], v9 offset:8192\n"
"v_mfma_f32_16x16x32_bf16 v[200:203], a[80:83], a[96:99], v[200:203]\n"
"s_add_u32 m0, s64, 16384\n"
"ds_read_b64_tr_b16 a[34:35], v9 offset:8704\n"
"v_mfma_f32_16x16x32_bf16 v[200:203], a[84:87], a[100:103], v[200:203]\n"
"ds_read_b64_tr_b16 a[36:37], v9 offset:12288\n"
"v_mfma_f32_16x16x32_bf16 v[228:231], a[92:95], a[100:103], v[228:231]\n"
"s_add_u32 s67, %21, 4194304\n"
"ds_read_b128 a[100:103], v13 offset:32768\n"
"v_mfma_f32_16x16x32_bf16 v[228:231], a[88:91], a[96:99], v[228:231]\n"
"ds_read_b128 a[96:99], v12 offset:32768\n"
"v_mfma_f32_16x16x32_bf16 v[232:235], a[88:91], a[104:107], v[232:235]\n"
"ds_read_b64_tr_b16 a[38:39], v9 offset:12800\n"
"buffer_load_dwordx4 v11, s[52:55], s67 offen offset:0  lds\n"
"v_mfma_f32_16x16x32_bf16 v[232:235], a[92:95], a[108:111], v[232:235]\n"
"ds_read_b64_tr_b16 a[40:41], v9 offset:8200\n"
"v_mfma_f32_16x16x32_bf16 v[204:207], a[84:87], a[108:111], v[204:207]\n"
"s_add_u32 s67, %21, 4209664\n"
"ds_read_b128 a[108:111], v13 offset:34816\n"
"v_mfma_f32_16x16x32_bf16 v[204:207], a[80:83], a[104:107], v[204:207]\n"
"ds_read_b128 a[104:107], v12 offset:34816\n"
"v_mfma_f32_16x16x32_bf16 v[208:211], a[80:83], a[112:115], v[208:211]\n"
"ds_read_b64_tr_b16 a[42:43], v9 offset:8712\n"
"buffer_load_dwordx4 v11, s[52:55], s67 offen offset:1024  lds\n"
"v_mfma_f32_16x16x32_bf16 v[208:211], a[84:87], a[116:119], v[208:211]\n"
"ds_read_b64_tr_b16 a[44:45], v9 offset:12296\n"
"v_mfma_f32_16x16x32_bf16 v[236:239], a[92:95], a[116:119], v[236:239]\n"
"s_add_u32 s67, %21, 4225024\n"
"ds_read_b128 a[116:119], v13 offset:36864\n"
"v_mfma_f32_16x16x32_bf16 v[236:239], a[88:91], a[112:115], v[236:239]\n"
"ds_read_b128 a[112:115], v12 offset:36864\n"
"v_mfma_f32_16x16x32_bf16 v[240:243], a[88:91], a[120:123], v[240:243]\n"
"ds_read_b64_tr_b16 a[46:47], v9 offset:12808\n"
"buffer_load_dwordx4 v11, s[52:55], s67 offen offset:2048  lds\n"
"v_mfma_f32_16x16x32_bf16 v[240:243], a[92:95], a[124:127], v[240:243]\n"
"ds_read_b64_tr_b16 a[48:49], v15 offset:8192\n"
"v_mfma_f32_16x16x32_bf16 v[212:215], a[84:87], a[124:127], v[212:215]\n"
"s_add_u32 s67, %21, 4240384\n"
"ds_read_b128 a[124:127], v13 offset:38912\n"
"v_mfma_f32_16x16x32_bf16 v[212:215], a[80:83], a[120:123], v[212:215]\n"
"ds_read_b128 a[120:123], v12 offset:38912\n"
"v_mfma_f32_16x16x32_bf16 v[216:219], a[80:83], a[128:131], v[216:219]\n"
"ds_read_b64_tr_b16 a[50:51], v15 offset:8704\n"
"buffer_load_dwordx4 v11, s[52:55], s67 offen offset:3072  lds\n"
"v_mfma_f32_16x16x32_bf16 v[216:219], a[84:87], a[132:135], v[216:219]\n"
"ds_read_b64_tr_b16 a[52:53], v15 offset:12288\n"
"v_mfma_f32_16x16x32_bf16 v[244:247], a[92:95], a[132:135], v[244:247]\n"
"ds_read_b128 a[132:135], v13 offset:24576\n"
"v_mfma_f32_16x16x32_bf16 v[244:247], a[88:91], a[128:131], v[244:247]\n"
"ds_read_b128 a[128:131], v12 offset:24576\n"
"v_mfma_f32_16x16x32_bf16 v[248:251], a[88:91], a[136:139], v[248:251]\n"
"s_add_u32 m0, s64, 8192\n"
"ds_read_b64_tr_b16 a[54:55], v15 offset:12800\n"
"v_mfma_f32_16x16x32_bf16 v[248:251], a[92:95], a[140:143], v[248:251]\n"
"ds_read_b64_tr_b16 a[56:57], v15 offset:8200\n"
"v_mfma_f32_16x16x32_bf16 v[220:223], a[84:87], a[140:143], v[220:223]\n"
"ds_read_b128 a[140:143], v13 offset:26624\n"
"v_mfma_f32_16x16x32_bf16 v[220:223], a[80:83], a[136:139], v[220:223]\n"
"ds_read_b128 a[136:139], v12 offset:26624\n"
"v_mfma_f32_16x16x32_bf16 v[224:227], a[80:83], a[144:147], v[224:227]\n"
"ds_read_b64_tr_b16 a[58:59], v15 offset:8712\n"
"v_mfma_f32_16x16x32_bf16 v[224:227], a[84:87], a[148:151], v[224:227]\n"
"ds_read_b64_tr_b16 a[60:61], v15 offset:12296\n"
"v_mfma_f32_16x16x32_bf16 v[252:255], a[92:95], a[148:151], v[252:255]\n"
"s_sub_u32 s67, 1572864, s75\n"
"ds_read_b128 a[148:151], v13 offset:28672\n"
"v_mfma_f32_16x16x32_bf16 v[252:255], a[88:91], a[144:147], v[252:255]\n"
"ds_read_b128 a[144:147], v12 offset:28672\n"
"ds_read_b64_tr_b16 a[62:63], v15 offset:12808\n"
"v_mfma_f32_16x16x32_bf16 a[28:31], a[88:91], a[152:155], a[28:31]\n"
"v_mfma_f32_16x16x32_bf16 a[28:31], a[92:95], a[156:159], a[28:31]\n"
"v_mfma_f32_16x16x32_bf16 a[24:27], a[84:87], a[156:159], a[24:27]\n"
"ds_read_b128 a[156:159], v13 offset:30720\n"
"v_mfma_f32_16x16x32_bf16 a[24:27], a[80:83], a[152:155], a[24:27]\n"
"s_waitcnt lgkmcnt(15)\n"
"ds_read_b128 a[152:155], v12 offset:30720\n"
"v_mfma_f32_16x16x32_bf16 v[32:35], a[32:35], a[96:99], v[32:35]\n"
"v_mfma_f32_16x16x32_bf16 v[32:35], a[36:39], a[100:103], v[32:35]\n"
"s_waitcnt lgkmcnt(2)\n"
"v_mfma_f32_16x16x32_bf16 v[60:63], a[44:47], a[100:103], v[60:63]\n"
"buffer_load_dwordx4 v11, s[52:55], s67 offen offset:0  lds\n"
"v_mfma_f32_16x16x32_bf16 v[60:63], a[40:43], a[96:99], v[60:63]\n"
"v_mfma_f32_16x16x32_bf16 v[64:67], a[40:43], a[104:107], v[64:67]\n"
"s_sub_u32 s67, 1588224, s75\n"
"v_mfma_f32_16x16x32_bf16 v[64:67], a[44:47], a[108:111], v[64:67]\n"
"v_mfma_f32_16x16x32_bf16 v[36:39], a[36:39], a[108:111], v[36:39]\n"
"v_mfma_f32_16x16x32_bf16 v[36:39], a[32:35], a[104:107], v[36:39]\n"
"buffer_load_dwordx4 v11, s[52:55], s67 offen offset:1024  lds\n"
"v_mfma_f32_16x16x32_bf16 v[40:43], a[32:35], a[112:115], v[40:43]\n"
"v_mfma_f32_16x16x32_bf16 v[40:43], a[36:39], a[116:119], v[40:43]\n"
"s_sub_u32 s67, 1603584, s75\n"
"v_mfma_f32_16x16x32_bf16 v[68:71], a[44:47], a[116:119], v[68:71]\n"
"v_mfma_f32_16x16x32_bf16 v[68:71], a[40:43], a[112:115], v[68:71]\n"
"buffer_load_dwordx4 v11, s[52:55], s67 offen offset:2048  lds\n"
"v_mfma_f32_16x16x32_bf16 v[72:75], a[40:43], a[120:123], v[72:75]\n"
"L_st2683:\n"
"s_cbranch_execz L_retA\n"
"v_mfma_f32_16x16x32_bf16 v[72:75], a[44:47], a[124:127], v[72:75]\n"
"s_sub_u32 s67, 1618944, s75\n"
"v_mfma_f32_16x16x32_bf16 v[44:47], a[36:39], a[124:127], v[44:47]\n"
"v_mfma_f32_16x16x32_bf16 v[44:47], a[32:35], a[120:123], v[44:47]\n"
"v_mfma_f32_16x16x32_bf16 v[48:51], a[32:35], a[128:131], v[48:51]\n"
"buffer_load_dwordx4 v11, s[52:55], s67 offen offset:3072  lds\n"
"v_mfma_f32_16x16x32_bf16 v[48:51], a[36:39], a[132:135], v[48:51]\n"
"s_add_u32 m0, s64, 12288\n"
"v_mfma_f32_16x16x32_bf16 v[76:79], a[44:47], a[132:135], v[76:79]\n"
"s_waitcnt vmcnt(12)\n"
"ds_read_b64_tr_b16 a[64:65], v9 offset:0\n"
"v_mfma_f32_16x16x32_bf16 v[76:79], a[40:43], a[128:131], v[76:79]\n"
"s_sub_u32 s67, 1638400, s75\n"
"ds_read_b64_tr_b16 a[66:67], v9 offset:512\n"
"v_mfma_f32_16x16x32_bf16 v[80:83], a[40:43], a[136:139], v[80:83]\n"
"ds_read_b64_tr_b16 a[68:69], v9 offset:4096\n"
"v_mfma_f32_16x16x32_bf16 v[80:83], a[44:47], a[140:143], v[80:83]\n"
"ds_read_b64_tr_b16 a[70:71], v9 offset:4608\n"
"buffer_load_dwordx4 v11, s[52:55], s67 offen offset:0  lds\n"
"v_mfma_f32_16x16x32_bf16 v[52:55], a[36:39], a[140:143], v[52:55]\n"
"s_sub_u32 s67, 1653760, s75\n"
"ds_read_b64_tr_b16 a[72:73], v9 offset:8\n"
"v_mfma_f32_16x16x32_bf16 v[52:55], a[32:35], a[136:139], v[52:55]\n"
"s_waitcnt lgkmcnt(5)\n"
"ds_read_b64_tr_b16 a[74:75], v9 offset:520\n"
"v_mfma_f32_16x16x32_bf16 v[56:59], a[32:35], a[144:147], v[56:59]\n"
"ds_read_b64_tr_b16 a[76:77], v9 offset:4104\n"
"v_mfma_f32_16x16x32_bf16 v[56:59], a[36:39], a[148:151], v[56:59]\n"
"ds_read_b64_tr_b16 a[78:79], v9 offset:4616\n"
"v_mfma_f32_16x16x32_bf16 v[84:87], a[44:47], a[148:151], v[84:87]\n"
"buffer_load_dwordx4 v11, s[52:55], s67 offen offset:1024  lds\n"
"v_mfma_f32_16x16x32_bf16 v[84:87], a[40:43], a[144:147], v[84:87]\n"
"ds_read_b64_tr_b16 a[80:81], v15 offset:0\n"
"v_mfma_f32_16x16x32_bf16 a[4:7], a[40:43], a[152:155], a[4:7]\n"
"s_sub_u32 s67, 1669120, s75\n"
"ds_read_b64_tr_b16 a[82:83], v15 offset:512\n"
"v_mfma_f32_16x16x32_bf16 a[4:7], a[44:47], a[156:159], a[4:7]\n"
"ds_read_b64_tr_b16 a[84:85], v15 offset:4096\n"
"v_mfma_f32_16x16x32_bf16 a[0:3], a[36:39], a[156:159], a[0:3]\n"
"ds_read_b64_tr_b16 a[86:87], v15 offset:4608\n"
"buffer_load_dwordx4 v11, s[52:55], s67 offen offset:2048  lds\n"
"v_mfma_f32_16x16x32_bf16 a[0:3], a[32:35], a[152:155], a[0:3]\n"
"ds_read_b64_tr_b16 a[88:89], v15 offset:8\n"
"v_mfma_f32_16x16x32_bf16 v[88:91], a[48:51], a[96:99], v[88:91]\n"
"s_sub_u32 s67, 1684480, s75\n"
"ds_read_b64_tr_b16 a[90:91], v15 offset:520\n"
"v_mfma_f32_16x16x32_bf16 v[88:91], a[52:55], a[100:103], v[88:91]\n"
"ds_read_b64_tr_b16 a[92:93], v15 offset:4104\n"
"v_mfma_f32_16x16x32_bf16 v[116:119], a[60:63], a[100:103], v[116:119]\n"
"ds_read_b64_tr_b16 a[94:95], v15 offset:4616\n"
"v_mfma_f32_16x16x32_bf16 v[116:119], a[56:59], a[96:99], v[116:119]\n"
"buffer_load_dwordx4 v11, s[52:55], s67 offen offset:3072  lds\n"
"v_mfma_f32_16x16x32_bf16 v[120:123], a[56:59], a[104:107], v[120:123]\n"
"v_mfma_f32_16x16x32_bf16 v[120:123], a[60:63], a[108:111], v[120:123]\n"
"s_add_u32 m0, s64, 0\n"
"v_mfma_f32_16x16x32_bf16 v[92:95], a[52:55], a[108:111], v[92:95]\n"
"v_mfma_f32_16x16x32_bf16 v[92:95], a[48:51], a[104:107], v[92:95]\n"
"s_sub_u32 s67, 1572992, s75\n"
"v_mfma_f32_16x16x32_bf16 v[96:99], a[48:51], a[112:115], v[96:99]\n"
"s_waitcnt lgkmcnt(0)\n"
"s_mov_b32 s99, 0\n"
"v_mfma_f32_16x16x32_bf16 v[96:99], a[52:55], a[116:119], v[96:99]\n"
"s_mov_b32 s98, 0\n"
"buffer_load_dwordx4 v11, s[52:55], s67 offen offset:0  lds\n"
"v_mfma_f32_16x16x32_bf16 v[124:127], a[60:63], a[116:119], v[124:127]\n"
"s_memtime s[76:77]\n"
"s_load_dword s98, s[96:97], 8320 glc\n"
"v_mfma_f32_16x16x32_bf16 v[124:127], a[56:59], a[112:115], v[124:127]\n"
"s_sub_u32 s67, 1588352, s75\n"
"v_mfma_f32_16x16x32_bf16 v[128:131], a[56:59], a[120:123], v[128:131]\n"
"v_mfma_f32_16x16x32_bf16 v[128:131], a[60:63], a[124:127], v[128:131]\n"
"buffer_load_dwordx4 v11, s[52:55], s67 offen offset:1024  lds\n"
"v_mfma_f32_16x16x32_bf16 v[100:103], a[52:55], a[124:127], v[100:103]\n"
"v_mfma_f32_16x16x32_bf16 v[100:103], a[48:51], a[120:123], v[100:103]\n"
"s_sub_u32 s67, 1603712, s75\n"
"v_mfma_f32_16x16x32_bf16 v[104:107], a[48:51], a[128:131], v[104:107]\n"
"v_mfma_f32_16x16x32_bf16 v[104:107], a[52:55], a[132:135], v[104:107]\n"
"v_mfma_f32_16x16x32_bf16 v[132:135], a[60:63], a[132:135], v[132:135]\n"
"buffer_load_dwordx4 v11, s[52:55], s67 offen offset:2048  lds\n"
"v_mfma_f32_16x16x32_bf16 v[132:135], a[56:59], a[128:131], v[132:135]\n"
"s_mov_b32 s29, s76\n"
"v_mfma_f32_16x16x32_bf16 v[136:139], a[56:59], a[136:139], v[136:139]\n"
"s_sub_u32 s67, 1619072, s75\n"
"v_mfma_f32_16x16x32_bf16 v[136:139], a[60:63], a[140:143], v[136:139]\n"
"buffer_load_dwordx4 v11, s[52:55], s67 offen offset:3072  lds\n"
"v_mfma_f32_16x16x32_bf16 v[108:111], a[52:55], a[140:143], v[108:111]\n"
"s_sub_u32 s99, s99, s98\n"
"s_sub_u32 s99, s99, s98\n"
"v_mfma_f32_16x16x32_bf16 v[108:111], a[48:51], a[136:139], v[108:111]\n"
"s_sub_u32 s99, s99, s98\n"
"s_add_u32 m0, s64, 4096\n"
"v_mfma_f32_16x16x32_bf16 v[112:115], a[48:51], a[144:147], v[112:115]\n"
"s_sub_u32 s99, s99, s98\n"
"v_mfma_f32_16x16x32_bf16 v[112:115], a[52:55], a[148:151], v[112:115]\n"
"s_sub_u32 s67, 1638528, s75\n"
"v_mfma_f32_16x16x32_bf16 v[140:143], a[60:63], a[148:151], v[140:143]\n"
"s_sub_u32 s99, s99, s98\n"
"s_sub_u32 s99, s99, s98\n"
"v_mfma_f32_16x16x32_bf16 v[140:143], a[56:59], a[144:147], v[140:143]\n"
"s_sub_u32 s99, s99, s98\n"
"buffer_load_dwordx4 v11, s[52:55], s67 offen offset:0  lds\n"
"v_mfma_f32_16x16x32_bf16 a[12:15], a[56:59], a[152:155], a[12:15]\n"
"s_sub_u32 s99, s99, s98\n"
"v_mfma_f32_16x16x32_bf16 a[12:15], a[60:63], a[156:159], a[12:15]\n"
"s_sub_u32 s67, 1653888, s75\n"
"v_mfma_f32_16x16x32_bf16 a[8:11], a[52:55], a[156:159], a[8:11]\n"
"s_sub_u32 s99, s99, s98\n"
"s_sub_u32 s99, s99, s98\n"
"v_mfma_f32_16x16x32_bf16 a[8:11], a[48:51], a[152:155], a[8:11]\n"
"s_sub_u32 s99, s99, s98\n"
"v_mfma_f32_16x16x32_bf16 v[144:147], a[64:67], a[96:99], v[144:147]\n"
"s_sub_u32 s99, s99, s98\n"
"v_mfma_f32_16x16x32_bf16 v[144:147], a[68:71], a[100:103], v[144:147]\n"
"buffer_load_dwordx4 v11, s[52:55], s67 offen offset:1024  lds\n"
"v_mfma_f32_16x16x32_bf16 v[172:175], a[76:79], a[100:103], v[172:175]\n"
"s_sub_u32 s99, s99, s98\n"
"s_sub_u32 s99, s99, s98\n"
"v_mfma_f32_16x16x32_bf16 v[172:175], a[72:75], a[96:99], v[172:175]\n"
"s_sub_u32 s99, s99, s98\n"
"s_sub_u32 s67, 1669248, s75\n"
"v_mfma_f32_16x16x32_bf16 v[176:179], a[72:75], a[104:107], v[176:179]\n"
"s_sub_u32 s99, s99, s98\n"
"v_mfma_f32_16x16x32_bf16 v[176:179], a[76:79], a[108:111], v[176:179]\n"
"buffer_load_dwordx4 v11, s[52:55], s67 offen offset:2048  lds\n"
"v_mfma_f32_16x16x32_bf16 v[148:151], a[68:71], a[108:111], v[148:151]\n"
"s_sub_u32 s99, s99, s98\n"
"s_sub_u32 s99, s99, s98\n"
"v_mfma_f32_16x16x32_bf16 v[148:151], a[64:67], a[104:107], v[148:151]\n"
"s_sub_u32 s99, s99, s98\n"
"s_sub_u32 s71, s64, s64\n"
"s_sub_u32 s67, 1684608, s75\n"
"v_mfma_f32_16x16x32_bf16 v[152:155], a[64:67], a[112:115], v[152:155]\n"
"s_sub_u32 s66, 81920, s73\n"
"s_sub_u32 s99, s99, s98\n"
"v_mfma_f32_16x16x32_bf16 v[152:155], a[68:71], a[116:119], v[152:155]\n"
"s_add_u32 s71, s71, 16384\n"
"buffer_load_dwordx4 v11, s[52:55], s67 offen offset:3072  lds\n"
"v_mfma_f32_16x16x32_bf16 v[180:183], a[76:79], a[116:119], v[180:183]\n"
"s_sub_u32 s99, s99, s98\n"
"v_add_u32 v12, s66, v7\n"
"s_sub_u32 s99, s99, s98\n"
"v_mfma_f32_16x16x32_bf16 v[180:183], a[72:75], a[112:115], v[180:183]\n"
"s_sub_u32 s99, s99, s98\n"
"s_cmp_lt_u32 %16, 2\n"
"s_cselect_b32 s67, 16384, 0\n"
"s_add_u32 s67, s67, s72\n"
"v_mfma_f32_16x16x32_bf16 v[184:187], a[72:75], a[120:123], v[184:187]\n"
"s_sub_u32 s99, s99, s98\n"
"v_add_u32 v13, s66, v8\n"
"s_add_u32 m0, s67, 139264\n"
"v_mfma_f32_16x16x32_bf16 v[184:187], a[76:79], a[124:127], v[184:187]\n"
"s_memtime s[76:77]\n"
"v_add_u32 v20, s71, v9\n"
"s_sub_u32 s67, 1792, s60\n"
"v_mfma_f32_16x16x32_bf16 v[156:159], a[68:71], a[124:127], v[156:159]\n"
"buffer_load_dwordx4 v6, s[48:51], s67 offen offset:0  lds\n"
"v_mfma_f32_16x16x32_bf16 v[156:159], a[64:67], a[120:123], v[156:159]\n"
"s_waitcnt lgkmcnt(0)\n"
"v_add_u32 v21, s71, v15\n"
"v_mfma_f32_16x16x32_bf16 v[160:163], a[64:67], a[128:131], v[160:163]\n"
"s_sub_u32 s29, s76, s29\n"
"s_sub_u32 s67, 17152, s60\n"
"v_mfma_f32_16x16x32_bf16 v[160:163], a[68:71], a[132:135], v[160:163]\n"
"s_cmp_lt_u32 s29, 620\n"
"s_cselect_b32 s99, s99, 0\n"
"v_mfma_f32_16x16x32_bf16 v[188:191], a[76:79], a[132:135], v[188:191]\n"
"s_max_u32 s31, s31, s99\n"
"buffer_load_dwordx4 v6, s[48:51], s67 offen offset:1024  lds\n"
"v_mfma_f32_16x16x32_bf16 v[188:191], a[72:75], a[128:131], v[188:191]\n"
"s_waitcnt vmcnt(18)\n"
"ds_read_b64_tr_b16 a[176:177], v20 offset:0\n"
"v_mfma_f32_16x16x32_bf16 v[192:195], a[72:75], a[136:139], v[192:195]\n"
"s_sub_u32 s67, 32512, s60\n"
"ds_read_b64_tr_b16 a[178:179], v20 offset:512\n"
"v_mfma_f32_16x16x32_bf16 v[192:195], a[76:79], a[140:143], v[192:195]\n"
"ds_read_b64_tr_b16 a[180:181], v20 offset:8\n"
"v_mfma_f32_16x16x32_bf16 v[164:167], a[68:71], a[140:143], v[164:167]\n"
"ds_read_b64_tr_b16 a[182:183], v20 offset:520\n"
"v_mfma_f32_16x16x32_bf16 v[164:167], a[64:67], a[136:139], v[164:167]\n"
"buffer_load_dwordx4 v6, s[48:51], s67 offen offset:2048  lds\n"
"v_mfma_f32_16x16x32_bf16 v[168:171], a[64:67], a[144:147], v[168:171]\n"
"ds_read_b64_tr_b16 a[184:185], v21 offset:0\n"
"v_mfma_f32_16x16x32_bf16 v[168:171], a[68:71], a[148:151], v[168:171]\n"
"s_sub_u32 s67, 47872, s60\n"
"ds_read_b64_tr_b16 a[186:187], v21 offset:512\n"
"v_mfma_f32_16x16x32_bf16 v[196:199], a[76:79], a[148:151], v[196:199]\n"
"ds_read_b64_tr_b16 a[188:189], v21 offset:8\n"
"v_mfma_f32_16x16x32_bf16 v[196:199], a[72:75], a[144:147], v[196:199]\n"
"buffer_load_dwordx4 v6, s[48:51], s67 offen offset:3072  lds\n"
"v_mfma_f32_16x16x32_bf16 a[20:23], a[72:75], a[152:155], a[20:23]\n"
"ds_read_b64_tr_b16 a[190:191], v21 offset:520\n"
"v_mfma_f32_16x16x32_bf16 a[20:23], a[76:79], a[156:159], a[20:23]\n"
"s_add_u32 m0, s64, 20352\n"
"v_mfma_f32_16x16x32_bf16 a[16:19], a[68:71], a[156:159], a[16:19]\n"
"v_mfma_f32_16x16x32_bf16 a[16:19], a[64:67], a[152:155], a[16:19]\n"
"s_barrier\n"
"s_waitcnt vmcnt(12)\n"
"ds_read_b64_tr_b16 a[32:33], v9 offset:8192\n"
"v_mfma_f32_16x16x32_bf16 v[200:203], a[80:83], a[96:99], v[200:203]\n"
"ds_read_b64_tr_b16 a[34:35], v9 offset:8704\n"
"buffer_load_dwordx4 v6, s[40:43], s88 offen offset:128  lds\n"
"v_mfma_f32_16x16x32_bf16 v[200:203], a[84:87], a[100:103], v[200:203]\n"
"ds_read_b64_tr_b16 a[36:37], v9 offset:12288\n"
"v_mfma_f32_16x16x32_bf16 v[228:231], a[92:95], a[100:103], v[228:231]\n"
"s_waitcnt lgkmcnt(3)\n"
"ds_read_b128 a[100:103], v13 offset:32768\n"
"v_mfma_f32_16x16x32_bf16 v[228:231], a[88:91], a[96:99], v[228:231]\n"
"ds_read_b128 a[96:99], v12 offset:32768\n"
"buffer_load_dwordx4 v6, s[40:43], s89 offen offset:1152  lds\n"
"v_mfma_f32_16x16x32_bf16 v[232:235], a[88:91], a[104:107], v[232:235]\n"
"ds_read_b64_tr_b16 a[38:39], v9 offset:12800\n"
"v_mfma_f32_16x16x32_bf16 v[232:235], a[92:95], a[108:111], v[232:235]\n"
"ds_read_b64_tr_b16 a[40:41], v9 offset:8200\n"
"v_mfma_f32_16x16x32_bf16 v[204:207], a[84:87], a[108:111], v[204:207]\n"
"ds_read_b128 a[108:111], v13 offset:34816\n"
"v_mfma_f32_16x16x32_bf16 v[204:207], a[80:83], a[104:107], v[204:207]\n"
"ds_read_b128 a[104:107], v12 offset:34816\n"
"v_mfma_f32_16x16x32_bf16 v[208:211], a[80:83], a[112:115], v[208:211]\n"
"ds_read_b64_tr_b16 a[42:43], v9 offset:8712\n"
"buffer_load_dwordx4 v6, s[40:43], s90 offen offset:2176  lds\n"
"v_mfma_f32_16x16x32_bf16 v[208:211], a[84:87], a[116:119], v[208:211]\n"
"ds_read_b64_tr_b16 a[44:45], v9 offset:12296\n"
"v_mfma_f32_16x16x32_bf16 v[236:239], a[92:95], a[116:119], v[236:239]\n"
"ds_read_b128 a[116:119], v13 offset:36864\n"
"v_mfma_f32_16x16x32_bf16 v[236:239], a[88:91], a[112:115], v[236:239]\n"
"ds_read_b128 a[112:115], v12 offset:36864\n"
"v_mfma_f32_16x16x32_bf16 v[240:243], a[88:91], a[120:123], v[240:243]\n"
"ds_read_b64_tr_b16 a[46:47], v9 offset:12808\n"
"v_mfma_f32_16x16x32_bf16 v[240:243], a[92:95], a[124:127], v[240:243]\n"
"ds_read_b64_tr_b16 a[48:49], v15 offset:8192\n"
"buffer_load_dwordx4 v6, s[40:43], s91 offen offset:3200  lds\n"
"v_mfma_f32_16x16x32_bf16 v[212:215], a[84:87], a[124:127], v[212:215]\n"
"ds_read_b128 a[124:127], v13 offset:38912\n"
"v_mfma_f32_16x16x32_bf16 v[212:215], a[80:83], a[120:123], v[212:215]\n"
"ds_read_b128 a[120:123], v12 offset:38912\n"
"v_mfma_f32_16x16x32_bf16 v[216:219], a[80:83], a[128:131], v[216:219]\n"
"ds_read_b64_tr_b16 a[50:51], v15 offset:8704\n"
"v_mfma_f32_16x16x32_bf16 v[216:219], a[84:87], a[132:135], v[216:219]\n"
"buffer_store_dwordx4 a[164:167], v0, s[36:39], %23 offen offset:2048 sc1\n"
"ds_read_b64_tr_b16 a[52:53], v15 offset:12288\n"
"v_mfma_f32_16x16x32_bf16 v[244:247], a[92:95], a[132:135], v[244:247]\n"
"ds_read_b128 a[132:135], v13 offset:24576\n"
"v_mfma_f32_16x16x32_bf16 v[244:247], a[88:91], a[128:131], v[244:247]\n"
"ds_read_b128 a[128:131], v12 offset:24576\n"
"v_mfma_f32_16x16x32_bf16 v[248:251], a[88:91], a[136:139], v[248:251]\n"
"ds_read_b64_tr_b16 a[54:55], v15 offset:12800\n"
"v_mfma_f32_16x16x32_bf16 v[248:251], a[92:95], a[140:143], v[248:251]\n"
"s_add_u32 m0, s64, 8192\n"
"ds_read_b64_tr_b16 a[56:57], v15 offset:8200\n"
"v_mfma_f32_16x16x32_bf16 v[220:223], a[84:87], a[140:143], v[220:223]\n"
"ds_read_b128 a[140:143], v13 offset:26624\n"
"v_mfma_f32_16x16x32_bf16 v[220:223], a[80:83], a[136:139], v[220:223]\n"
"ds_read_b128 a[136:139], v12 offset:26624\n"
"v_mfma_f32_16x16x32_bf16 v[224:227], a[80:83], a[144:147], v[224:227]\n"
"ds_read_b64_tr_b16 a[58:59], v15 offset:8712\n"
"v_mfma_f32_16x16x32_bf16 v[224:227], a[84:87], a[148:151], v[224:227]\n"
"ds_read_b64_tr_b16 a[60:61], v15 offset:12296\n"
"v_mfma_f32_16x16x32_bf16 v[252:255], a[92:95], a[148:151], v[252:255]\n"
"ds_read_b128 a[148:151], v13 offset:28672\n"
"v_mfma_f32_16x16x32_bf16 v[252:255], a[88:91], a[144:147], v[252:255]\n"
"ds_read_b128 a[144:147], v12 offset:28672\n"
"s_sub_u32 s67, 1835008, s75\n"
"ds_read_b64_tr_b16 a[62:63], v15 offset:12808\n"
"v_mfma_f32_16x16x32_bf16 a[28:31], a[88:91], a[152:155], a[28:31]\n"
"v_mfma_f32_16x16x32_bf16 a[28:31], a[92:95], a[156:159], a[28:31]\n"
"v_mfma_f32_16x16x32_bf16 a[24:27], a[84:87], a[156:159], a[24:27]\n"
"ds_read_b128 a[156:159], v13 offset:30720\n"
"v_mfma_f32_16x16x32_bf16 a[24:27], a[80:83], a[152:155], a[24:27]\n"
"s_waitcnt lgkmcnt(15)\n"
"ds_read_b128 a[152:155], v12 offset:30720\n"
"v_mfma_f32_16x16x32_bf16 v[32:35], a[32:35], a[96:99], v[32:35]\n"
"v_mfma_f32_16x16x32_bf16 v[32:35], a[36:39], a[100:103], v[32:35]\n"
"s_waitcnt lgkmcnt(2)\n"
"v_mfma_f32_16x16x32_bf16 v[60:63], a[44:47], a[100:103], v[60:63]\n"
"buffer_load_dwordx4 v11, s[52:55], s67 offen offset:0  lds\n"
"v_mfma_f32_16x16x32_bf16 v[60:63], a[40:43], a[96:99], v[60:63]\n"
"v_mfma_f32_16x16x32_bf16 v[64:67], a[40:43], a[104:107], v[64:67]\n"
"s_sub_u32 s67, 1850368, s75\n"
"v_mfma_f32_16x16x32_bf16 v[64:67], a[44:47], a[108:111], v[64:67]\n"
"v_mfma_f32_16x16x32_bf16 v[36:39], a[36:39], a[108:111], v[36:39]\n"
"buffer_load_dwordx4 v11, s[52:55], s67 offen offset:1024  lds\n"
"v_mfma_f32_16x16x32_bf16 v[36:39], a[32:35], a[104:107], v[36:39]\n"
"v_mfma_f32_16x16x32_bf16 v[40:43], a[32:35], a[112:115], v[40:43]\n"
"s_sub_u32 s67, 1865728, s75\n"
"v_mfma_f32_16x16x32_bf16 v[40:43], a[36:39], a[116:119], v[40:43]\n"
"v_mfma_f32_16x16x32_bf16 v[68:71], a[44:47], a[116:119], v[68:71]\n"
"v_mfma_f32_16x16x32_bf16 v[68:71], a[40:43], a[112:115], v[68:71]\n"
"buffer_load_dwordx4 v11, s[52:55], s67 offen offset:2048  lds\n"
"v_mfma_f32_16x16x32_bf16 v[72:75], a[40:43], a[120:123], v[72:75]\n"
"v_mfma_f32_16x16x32_bf16 v[72:75], a[44:47], a[124:127], v[72:75]\n"
"s_sub_u32 s67, 1881088, s75\n"
"v_mfma_f32_16x16x32_bf16 v[44:47], a[36:39], a[124:127], v[44:47]\n"
"v_mfma_f32_16x16x32_bf16 v[44:47], a[32:35], a[120:123], v[44:47]\n"
"v_mfma_f32_16x16x32_bf16 v[48:51], a[32:35], a[128:131], v[48:51]\n"
"buffer_load_dwordx4 v11, s[52:55], s67 offen offset:3072  lds\n"
"v_mfma_f32_16x16x32_bf16 v[48:51], a[36:39], a[132:135], v[48:51]\n"
"s_add_u32 m0, s64, 12288\n"
"v_mfma_f32_16x16x32_bf16 v[76:79], a[44:47], a[132:135], v[76:79]\n"
"s_waitcnt vmcnt(13)\n"
"ds_read_b64_tr_b16 a[64:65], v9 offset:0\n"
"v_mfma_f32_16x16x32_bf16 v[76:79], a[40:43], a[128:131], v[76:79]\n"
"s_sub_u32 s67, 1900544, s75\n"
"ds_read_b64_tr_b16 a[66:67], v9 offset:512\n"
"v_mfma_f32_16x16x32_bf16 v[80:83], a[40:43], a[136:139], v[80:83]\n"
"ds_read_b64_tr_b16 a[68:69], v9 offset:4096\n"
"v_mfma_f32_16x16x32_bf16 v[80:83], a[44:47], a[140:143], v[80:83]\n"
"ds_read_b64_tr_b16 a[70:71], v9 offset:4608\n"
"buffer_load_dwordx4 v11, s[52:55], s67 offen offset:0  lds\n"
"v_mfma_f32_16x16x32_bf16 v[52:55], a[36:39], a[140:143], v[52:55]\n"
"s_sub_u32 s67, 1915904, s75\n"
"ds_read_b64_tr_b16 a[72:73], v9 offset:8\n"
"v_mfma_f32_16x16x32_bf16 v[52:55], a[32:35], a[136:139], v[52:55]\n"
"s_waitcnt lgkmcnt(5)\n"
"ds_read_b64_tr_b16 a[74:75], v9 offset:520\n"
"v_mfma_f32_16x16x32_bf16 v[56:59], a[32:35], a[144:147], v[56:59]\n"
"ds_read_b64_tr_b16 a[76:77], v9 offset:4104\n"
"v_mfma_f32_16x16x32_bf16 v[56:59], a[36:39], a[148:151], v[56:59]\n"
"ds_read_b64_tr_b16 a[78:79], v9 offset:4616\n"
"v_mfma_f32_16x16x32_bf16 v[84:87], a[44:47], a[148:151], v[84:87]\n"
"buffer_load_dwordx4 v11, s[52:55], s67 offen offset:1024  lds\n"
"v_mfma_f32_16x16x32_bf16 v[84:87], a[40:43], a[144:147], v[84:87]\n"
"ds_read_b64_tr_b16 a[80:81], v15 offset:0\n"
"v_mfma_f32_16x16x32_bf16 a[4:7], a[40:43], a[152:155], a[4:7]\n"
"s_sub_u32 s67, 1931264, s75\n"
"ds_read_b64_tr_b16 a[82:83], v15 offset:512\n"
"v_mfma_f32_16x16x32_bf16 a[4:7], a[44:47], a[156:159], a[4:7]\n"
"ds_read_b64_tr_b16 a[84:85], v15 offset:4096\n"
"v_mfma_f32_16x16x32_bf16 a[0:3], a[36:39], a[156:159], a[0:3]\n"
"ds_read_b64_tr_b16 a[86:87], v15 offset:4608\n"
"buffer_load_dwordx4 v11, s[52:55], s67 offen offset:2048  lds\n"
"v_mfma_f32_16x16x32_bf16 a[0:3], a[32:35], a[152:155], a[0:3]\n"
"ds_read_b64_tr_b16 a[88:89], v15 offset:8\n"
"v_mfma_f32_16x16x32_bf16 v[88:91], a[48:51], a[96:99], v[88:91]\n"
"s_sub_u32 s67, 1946624, s75\n"
"ds_read_b64_tr_b16 a[90:91], v15 offset:520\n"
"v_mfma_f32_16x16x32_bf16 v[88:91], a[52:55], a[100:103], v[88:91]\n"
"ds_read_b64_tr_b16 a[92:93], v15 offset:4104\n"
"v_mfma_f32_16x16x32_bf16 v[116:119], a[60:63], a[100:103], v[116:119]\n"
"ds_read_b64_tr_b16 a[94:95], v15 offset:4616\n"
"v_mfma_f32_16x16x32_bf16 v[116:119], a[56:59], a[96:99], v[116:119]\n"
"buffer_load_dwordx4 v11, s[52:55], s67 offen offset:3072  lds\n"
"v_mfma_f32_16x16x32_bf16 v[120:123], a[56:59], a[104:107], v[120:123]\n"
"v_mfma_f32_16x16x32_bf16 v[120:123], a[60:63], a[108:111], v[120:123]\n"
"s_add_u32 m0, s64, 16256\n"
"v_mfma_f32_16x16x32_bf16 v[92:95], a[52:55], a[108:111], v[92:95]\n"
"v_mfma_f32_16x16x32_bf16 v[92:95], a[48:51], a[104:107], v[92:95]\n"
"buffer_load_dwordx4 v6, s[40:43], s92 offen offset:128  lds\n"
"v_mfma_f32_16x16x32_bf16 v[96:99], a[48:51], a[112:115], v[96:99]\n"
"s_waitcnt lgkmcnt(0)\n"
"s_mov_b32 s99, 0\n"
"v_mfma_f32_16x16x32_bf16 v[96:99], a[52:55], a[116:119], v[96:99]\n"
"s_mov_b32 s98, 0\n"
"v_mfma_f32_16x16x32_bf16 v[124:127], a[60:63], a[116:119], v[124:127]\n"
"s_memtime s[76:77]\n"
"s_load_dword s98, s[96:97], 128 glc\n"
"v_mfma_f32_16x16x32_bf16 v[124:127], a[56:59], a[112:115], v[124:127]\n"
"buffer_load_dwordx4 v6, s[40:43], s93 offen offset:1152  lds\n"
"v_mfma_f32_16x16x32_bf16 v[128:131], a[56:59], a[120:123], v[128:131]\n"
"v_mfma_f32_16x16x32_bf16 v[128:131], a[60:63], a[124:127], v[128:131]\n"
"v_mfma_f32_16x16x32_bf16 v[100:103], a[52:55], a[124:127], v[100:103]\n"
"v_mfma_f32_16x16x32_bf16 v[100:103], a[48:51], a[120:123], v[100:103]\n"
"buffer_load_dwordx4 v6, s[40:43], s94 offen offset:2176  lds\n"
"v_mfma_f32_16x16x32_bf16 v[104:107], a[48:51], a[128:131], v[104:107]\n"
"v_mfma_f32_16x16x32_bf16 v[104:107], a[52:55], a[132:135], v[104:107]\n"
"v_mfma_f32_16x16x32_bf16 v[132:135], a[60:63], a[132:135], v[132:135]\n"
"v_mfma_f32_16x16x32_bf16 v[132:135], a[56:59], a[128:131], v[132:135]\n"
"s_mov_b32 s29, s76\n"
"v_mfma_f32_16x16x32_bf16 v[136:139], a[56:59], a[136:139], v[136:139]\n"
"buffer_load_dwordx4 v6, s[40:43], s95 offen offset:3200  lds\n"
"v_mfma_f32_16x16x32_bf16 v[136:139], a[60:63], a[140:143], v[136:139]\n"
"s_add_u32 m0, s64, 0\n"
"v_mfma_f32_16x16x32_bf16 v[108:111], a[52:55], a[140:143], v[108:111]\n"
"s_sub_u32 s99, s99, s98\n"
"s_sub_u32 s99, s99, s98\n"
"s_andn2_b32 s65, %25, 4096\n"
"v_mfma_f32_16x16x32_bf16 v[108:111], a[48:51], a[136:139], v[108:111]\n"
"s_sub_u32 s99, s99, s98\n"
"v_mfma_f32_16x16x32_bf16 v[112:115], a[48:51], a[144:147], v[112:115]\n"
"s_sub_u32 s99, s99, s98\n"
"s_sub_u32 s67, 1835136, s75\n"
"v_mfma_f32_16x16x32_bf16 v[112:115], a[52:55], a[148:151], v[112:115]\n"
"buffer_load_dwordx4 v11, s[52:55], s67 offen offset:0  lds\n"
"v_mfma_f32_16x16x32_bf16 v[140:143], a[60:63], a[148:151], v[140:143]\n"
"s_sub_u32 s99, s99, s98\n"
"s_sub_u32 s99, s99, s98\n"
"buffer_load_dwordx4 v[24:27], v0, s[56:59], s65 offen sc1\n"
"v_mfma_f32_16x16x32_bf16 v[140:143], a[56:59], a[144:147], v[140:143]\n"
"s_sub_u32 s99, s99, s98\n"
"s_sub_u32 s67, 1850496, s75\n"
"v_mfma_f32_16x16x32_bf16 a[12:15], a[56:59], a[152:155], a[12:15]\n"
"s_sub_u32 s99, s99, s98\n"
"v_mfma_f32_16x16x32_bf16 a[12:15], a[60:63], a[156:159], a[12:15]\n"
"buffer_load_dwordx4 v11, s[52:55], s67 offen offset:1024  lds\n"
"v_mfma_f32_16x16x32_bf16 a[8:11], a[52:55], a[156:159], a[8:11]\n"
"s_sub_u32 s99, s99, s98\n"
"s_sub_u32 s99, s99, s98\n"
"v_mfma_f32_16x16x32_bf16 a[8:11], a[48:51], a[152:155], a[8:11]\n"
"s_sub_u32 s99, s99, s98\n"
"s_sub_u32 s67, 1865856, s75\n"
"v_mfma_f32_16x16x32_bf16 v[144:147], a[64:67], a[96:99], v[144:147]\n"
"s_sub_u32 s99, s99, s98\n"
"v_mfma_f32_16x16x32_bf16 v[144:147], a[68:71], a[100:103], v[144:147]\n"
"buffer_load_dwordx4 v11, s[52:55], s67 offen offset:2048  lds\n"
"v_mfma_f32_16x16x32_bf16 v[172:175], a[76:79], a[100:103], v[172:175]\n"
"s_sub_u32 s99, s99, s98\n"
"s_sub_u32 s99, s99, s98\n"
"v_mfma_f32_16x16x32_bf16 v[172:175], a[72:75], a[96:99], v[172:175]\n"
"s_sub_u32 s99, s99, s98\n"
"s_sub_u32 s67, 1881216, s75\n"
"v_mfma_f32_16x16x32_bf16 v[176:179], a[72:75], a[104:107], v[176:179]\n"
"s_sub_u32 s99, s99, s98\n"
"v_mfma_f32_16x16x32_bf16 v[176:179], a[76:79], a[108:111], v[176:179]\n"
"v_mfma_f32_16x16x32_bf16 v[148:151], a[68:71], a[108:111], v[148:151]\n"
"s_sub_u32 s99, s99, s98\n"
"s_sub_u32 s99, s99, s98\n"
"v_mfma_f32_16x16x32_bf16 v[148:151], a[64:67], a[104:107], v[148:151]\n"
"s_sub_u32 s99, s99, s98\n"
"s_sub_u32 s71, s64, s64\n"
"buffer_load_dwordx4 v11, s[52:55], s67 offen offset:3072  lds\n"
"v_mfma_f32_16x16x32_bf16 v[152:155], a[64:67], a[112:115], v[152:155]\n"
"s_sub_u32 s66, 122880, s73\n"
"s_sub_u32 s99, s99, s98\n"
"s_add_u32 m0, s64, 4096\n"
"v_mfma_f32_16x16x32_bf16 v[152:155], a[68:71], a[116:119], v[152:155]\n"
"s_add_u32 s71, s71, 20480\n"
"s_sub_u32 s67, 1900672, s75\n"
"v_mfma_f32_16x16x32_bf16 v[180:183], a[76:79], a[116:119], v[180:183]\n"
"s_sub_u32 s99, s99, s98\n"
"v_add_u32 v12, s66, v7\n"
"s_sub_u32 s99, s99, s98\n"
"v_mfma_f32_16x16x32_bf16 v[180:183], a[72:75], a[112:115], v[180:183]\n"
"s_sub_u32 s99, s99, s98\n"
"buffer_load_dwordx4 v11, s[52:55], s67 offen offset:0  lds\n"
"v_mfma_f32_16x16x32_bf16 v[184:187], a[72:75], a[120:123], v[184:187]\n"
"s_sub_u32 s99, s99, s98\n"
"v_add_u32 v13, s66, v8\n"
"v_mfma_f32_16x16x32_bf16 v[184:187], a[76:79], a[124:127], v[184:187]\n"
"s_memtime s[76:77]\n"
"v_add_u32 v20, s71, v7\n"
"v_mfma_f32_16x16x32_bf16 v[156:159], a[68:71], a[124:127], v[156:159]\n"
"s_sub_u32 s67, 1916032, s75\n"
"v_mfma_f32_16x16x32_bf16 v[156:159], a[64:67], a[120:123], v[156:159]\n"
"s_waitcnt lgkmcnt(0)\n"
"v_add_u32 v21, s71, v8\n"
"v_mfma_f32_16x16x32_bf16 v[160:163], a[64:67], a[128:131], v[160:163]\n"
"s_sub_u32 s29, s76, s29\n"
"buffer_load_dwordx4 v11, s[52:55], s67 offen offset:1024  lds\n"
"v_mfma_f32_16x16x32_bf16 v[160:163], a[68:71], a[132:135], v[160:163]\n"
"s_cmp_lt_u32 s29, 620\n"
"s_cselect_b32 s99, s99, 0\n"
"v_mfma_f32_16x16x32_bf16 v[188:191], a[76:79], a[132:135], v[188:191]\n"
"s_max_u32 s30, s30, s99\n"
"s_sub_u32 s67, 1931392, s75\n"
"v_mfma_f32_16x16x32_bf16 v[188:191], a[72:75], a[128:131], v[188:191]\n"
"s_waitcnt vmcnt(20)\n"
"ds_read_b128 a[224:227], v20 offset:0\n"
"v_mfma_f32_16x16x32_bf16 v[192:195], a[72:75], a[136:139], v[192:195]\n"
"ds_read_b128 a[228:231], v21 offset:0\n"
"v_mfma_f32_16x16x32_bf16 v[192:195], a[76:79], a[140:143], v[192:195]\n"
"ds_read_b128 a[232:235], v20 offset:2048\n"
"v_mfma_f32_16x16x32_bf16 v[164:167], a[68:71], a[140:143], v[164:167]\n"
"buffer_load_dwordx4 v11, s[52:55], s67 offen offset:2048  lds\n"
"v_mfma_f32_16x16x32_bf16 v[164:167], a[64:67], a[136:139], v[164:167]\n"
"ds_read_b128 a[236:239], v21 offset:2048\n"
"v_mfma_f32_16x16x32_bf16 v[168:171], a[64:67], a[144:147], v[168:171]\n"
"s_sub_u32 s67, 1946752, s75\n"
"v_mfma_f32_16x16x32_bf16 v[168:171], a[68:71], a[148:151], v[168:171]\n"
"v_mfma_f32_16x16x32_bf16 v[196:199], a[76:79], a[148:151], v[196:199]\n"
"buffer_load_dwordx4 v11, s[52:55], s67 offen offset:3072  lds\n"
"v_mfma_f32_16x16x32_bf16 v[196:199], a[72:75], a[144:147], v[196:199]\n"
"v_mfma_f32_16x16x32_bf16 a[20:23], a[72:75], a[152:155], a[20:23]\n"
"v_mfma_f32_16x16x32_bf16 a[20:23], a[76:79], a[156:159], a[20:23]\n"
"s_waitcnt lgkmcnt(0)\n"
"v_mfma_f32_16x16x32_bf16 a[16:19], a[68:71], a[156:159], a[16:19]\n"
"v_mfma_f32_16x16x32_bf16 a[16:19], a[64:67], a[152:155], a[16:19]\n"
"s_barrier\n"
"s_waitcnt vmcnt(13)\n"
"ds_read_b64_tr_b16 a[32:33], v9 offset:8192\n"
"v_mfma_f32_16x16x32_bf16 v[200:203], a[80:83], a[96:99], v[200:203]\n"
"buffer_store_dwordx4 a[168:171], v1, s[36:39], %23 offen offset:0 sc1\n"
"ds_read_b64_tr_b16 a[34:35], v9 offset:8704\n"
"v_mfma_f32_16x16x32_bf16 v[200:203], a[84:87], a[100:103], v[200:203]\n"
"ds_read_b64_tr_b16 a[36:37], v9 offset:12288\n"
"v_mfma_f32_16x16x32_bf16 v[228:231], a[92:95], a[100:103], v[228:231]\n"
"ds_read_b128 a[100:103], v13 offset:32768\n"
"v_mfma_f32_16x16x32_bf16 v[228:231], a[88:91], a[96:99], v[228:231]\n"
"ds_read_b128 a[96:99], v12 offset:32768\n"
"v_mfma_f32_16x16x32_bf16 v[232:235], a[88:91], a[104:107], v[232:235]\n"
"ds_read_b64_tr_b16 a[38:39], v9 offset:12800\n"
"v_mfma_f32_16x16x32_bf16 v[232:235], a[92:95], a[108:111], v[232:235]\n"
"ds_read_b64_tr_b16 a[40:41], v9 offset:8200\n"
"v_mfma_f32_16x16x32_bf16 v[204:207], a[84:87], a[108:111], v[204:207]\n"
"ds_read_b128 a[108:111], v13 offset:34816\n"
"v_mfma_f32_16x16x32_bf16 v[204:207], a[80:83], a[104:107], v[204:207]\n"
"ds_read_b128 a[104:107], v12 offset:34816\n"
"v_mfma_f32_16x16x32_bf16 v[208:211], a[80:83], a[112:115], v[208:211]\n"
"ds_read_b64_tr_b16 a[42:43], v9 offset:8712\n"
"v_mfma_f32_16x16x32_bf16 v[208:211], a[84:87], a[116:119], v[208:211]\n"
"ds_read_b64_tr_b16 a[44:45], v9 offset:12296\n"
"v_mfma_f32_16x16x32_bf16 v[236:239], a[92:95], a[116:119], v[236:239]\n"
"ds_read_b128 a[116:119], v13 offset:36864\n"
"v_mfma_f32_16x16x32_bf16 v[236:239], a[88:91], a[112:115], v[236:239]\n"
"buffer_store_dwordx4 a[172:175], v1, s[36:39], %23 offen offset:2048 sc1\n"
"ds_read_b128 a[112:115], v12 offset:36864\n"
"s_getpc_b64 s[70:71]\n"
"L_c11_:\n"
"s_add_u32 s70, s70, L_P6-L_c11_\n"
"s_addc_u32 s71, s71, 0\n"
"v_mfma_f32_16x16x32_bf16 v[240:243], a[88:91], a[120:123], v[240:243]\n"
"s_lshr_b32 s66, %26, 3\n"
"ds_read_b64_tr_b16 a[46:47], v9 offset:12808\n"
"v_mfma_f32_16x16x32_bf16 v[240:243], a[92:95], a[124:127], v[240:243]\n"
"s_lshl2_add_u32 s66, s66, %16\n"
"ds_read_b64_tr_b16 a[48:49], v15 offset:8192\n"
"v_mfma_f32_16x16x32_bf16 v[212:215], a[84:87], a[124:127], v[212:215]\n"
"s_mul_i32 s66, s66, 256\n"
"ds_read_b128 a[124:127], v13 offset:38912\n"
"v_mfma_f32_16x16x32_bf16 v[212:215], a[80:83], a[120:123], v[212:215]\n"
"s_add_u32 s74, %23, 524288\n"
"ds_read_b128 a[120:123], v12 offset:38912\n"
"v_mfma_f32_16x16x32_bf16 v[216:219], a[80:83], a[128:131], v[216:219]\n"
"s_min_u32 s66, s66, L_headF-L_P6-256\n"
"ds_read_b64_tr_b16 a[50:51], v15 offset:8704\n"
"v_mfma_f32_16x16x32_bf16 v[216:219], a[84:87], a[132:135], v[216:219]\n"
"ds_read_b64_tr_b16 a[52:53], v15 offset:12288\n"
"v_mfma_f32_16x16x32_bf16 v[244:247], a[92:95], a[132:135], v[244:247]\n"
"ds_read_b128 a[132:135], v13 offset:24576\n"
"v_min_u32 v22, 16, v0\n"
"v_mfma_f32_16x16x32_bf16 v[244:247], a[88:91], a[128:131], v[244:247]\n"
"ds_read_b128 a[128:131], v12 offset:24576\n"
"v_mfma_f32_16x16x32_bf16 v[248:251], a[88:91], a[136:139], v[248:251]\n"
"ds_read_b64_tr_b16 a[54:55], v15 offset:12800\n"
"v_mfma_f32_16x16x32_bf16 v[248:251], a[92:95], a[140:143], v[248:251]\n"
"ds_read_b64_tr_b16 a[56:57], v15 offset:8200\n"
"v_lshl_add_u32 v22, v22, 3, s66\n"
"v_mfma_f32_16x16x32_bf16 v[220:223], a[84:87], a[140:143], v[220:223]\n"
"ds_read_b128 a[140:143], v13 offset:26624\n"
"v_mfma_f32_16x16x32_bf16 v[220:223], a[80:83], a[136:139], v[220:223]\n"
"ds_read_b128 a[136:139], v12 offset:26624\n"
"v_mfma_f32_16x16x32_bf16 v[224:227], a[80:83], a[144:147], v[224:227]\n"
"buffer_store_dwordx4 a[176:179], v0, s[36:39], s74 offen offset:0 sc1\n"
"ds_read_b64_tr_b16 a[58:59], v15 offset:8712\n"
"global_load_dword v23, v22, s[70:71]\n"
"v_mfma_f32_16x16x32_bf16 v[224:227], a[84:87], a[148:151], v[224:227]\n"
"ds_read_b64_tr_b16 a[60:61], v15 offset:12296\n"
"v_mfma_f32_16x16x32_bf16 v[252:255], a[92:95], a[148:151], v[252:255]\n"
"ds_read_b128 a[148:151], v13 offset:28672\n"
"v_mfma_f32_16x16x32_bf16 v[252:255], a[88:91], a[144:147], v[252:255]\n"
"ds_read_b128 a[144:147], v12 offset:28672\n"
"s_sub_u32 s71, s64, s64\n"
"ds_read_b64_tr_b16 a[62:63], v15 offset:12808\n"
"v_mfma_f32_16x16x32_bf16 a[28:31], a[88:91], a[152:155], a[28:31]\n"
"v_mfma_f32_16x16x32_bf16 a[28:31], a[92:95], a[156:159], a[28:31]\n"
"v_mfma_f32_16x16x32_bf16 a[24:27], a[84:87], a[156:159], a[24:27]\n"
"s_add_u32 s71, s71, 16384\n"
"ds_read_b128 a[156:159], v13 offset:30720\n"
"v_mfma_f32_16x16x32_bf16 a[24:27], a[80:83], a[152:155], a[24:27]\n"
"s_waitcnt lgkmcnt(15)\n"
"ds_read_b128 a[152:155], v12 offset:30720\n"
"v_mfma_f32_16x16x32_bf16 v[32:35], a[32:35], a[96:99], v[32:35]\n"
"v_add_u32 v20, s71, v7\n"
"v_mfma_f32_16x16x32_bf16 v[32:35], a[36:39], a[100:103], v[32:35]\n"
"L_st3245:\n"
"s_cbranch_execz L_retA\n"
"s_waitcnt lgkmcnt(2)\n"
"v_mfma_f32_16x16x32_bf16 v[60:63], a[44:47], a[100:103], v[60:63]\n"
"v_mfma_f32_16x16x32_bf16 v[60:63], a[40:43], a[96:99], v[60:63]\n"
"v_add_u32 v21, s71, v8\n"
"v_mfma_f32_16x16x32_bf16 v[64:67], a[40:43], a[104:107], v[64:67]\n"
"v_mfma_f32_16x16x32_bf16 v[64:67], a[44:47], a[108:111], v[64:67]\n"
"buffer_store_dwordx4 a[180:183], v0, s[36:39], s74 offen offset:2048 sc1\n"
"v_mfma_f32_16x16x32_bf16 v[36:39], a[36:39], a[108:111], v[36:39]\n"
"s_waitcnt vmcnt(14)\n"
"ds_read_b128 a[240:243], v20 offset:0\n"
"v_mfma_f32_16x16x32_bf16 v[36:39], a[32:35], a[104:107], v[36:39]\n"
"v_mfma_f32_16x16x32_bf16 v[40:43], a[32:35], a[112:115], v[40:43]\n"
"ds_read_b128 a[244:247], v21 offset:0\n"
"v_mfma_f32_16x16x32_bf16 v[40:43], a[36:39], a[116:119], v[40:43]\n"
"ds_read_b128 a[248:251], v20 offset:2048\n"
"v_mfma_f32_16x16x32_bf16 v[68:71], a[44:47], a[116:119], v[68:71]\n"
"s_add_u32 s70, s78, 1\n"
"v_mfma_f32_16x16x32_bf16 v[68:71], a[40:43], a[112:115], v[68:71]\n"
"ds_read_b128 a[252:255], v21 offset:2048\n"
"v_mfma_f32_16x16x32_bf16 v[72:75], a[40:43], a[120:123], v[72:75]\n"
"v_mfma_f32_16x16x32_bf16 v[72:75], a[44:47], a[124:127], v[72:75]\n"
"v_mfma_f32_16x16x32_bf16 v[44:47], a[36:39], a[124:127], v[44:47]\n"
"v_mfma_f32_16x16x32_bf16 v[44:47], a[32:35], a[120:123], v[44:47]\n"
"v_mfma_f32_16x16x32_bf16 v[48:51], a[32:35], a[128:131], v[48:51]\n"
"v_mfma_f32_16x16x32_bf16 v[48:51], a[36:39], a[132:135], v[48:51]\n"
"s_waitcnt lgkmcnt(0)\n"
"buffer_store_dwordx4 a[184:187], v1, s[36:39], s74 offen offset:0 sc1\n"
"v_mfma_f32_16x16x32_bf16 v[76:79], a[44:47], a[132:135], v[76:79]\n"
"s_waitcnt vmcnt(6)\n"
"ds_read_b64_tr_b16 a[64:65], v9 offset:0\n"
"v_mfma_f32_16x16x32_bf16 v[76:79], a[40:43], a[128:131], v[76:79]\n"
"ds_read_b64_tr_b16 a[66:67], v9 offset:512\n"
"v_mfma_f32_16x16x32_bf16 v[80:83], a[40:43], a[136:139], v[80:83]\n"
"ds_read_b64_tr_b16 a[68:69], v9 offset:4096\n"
"v_mfma_f32_16x16x32_bf16 v[80:83], a[44:47], a[140:143], v[80:83]\n"
"ds_read_b64_tr_b16 a[70:71], v9 offset:4608\n"
"v_mfma_f32_16x16x32_bf16 v[52:55], a[36:39], a[140:143], v[52:55]\n"
"ds_read_b64_tr_b16 a[72:73], v9 offset:8\n"
"v_subrev_u32 v24, s70, v24\n"
"v_mfma_f32_16x16x32_bf16 v[52:55], a[32:35], a[136:139], v[52:55]\n"
"ds_read_b64_tr_b16 a[74:75], v9 offset:520\n"
"v_xor_b32 v25, s79, v25\n"
"v_mfma_f32_16x16x32_bf16 v[56:59], a[32:35], a[144:147], v[56:59]\n"
"ds_read_b64_tr_b16 a[76:77], v9 offset:4104\n"
"v_subrev_u32 v26, s70, v26\n"
"v_mfma_f32_16x16x32_bf16 v[56:59], a[36:39], a[148:151], v[56:59]\n"
"ds_read_b64_tr_b16 a[78:79], v9 offset:4616\n"
"v_xor_b32 v27, s79, v27\n"
"v_mfma_f32_16x16x32_bf16 v[84:87], a[44:47], a[148:151], v[84:87]\n"
"ds_read_b64_tr_b16 a[80:81], v15 offset:0\n"
"v_or_b32 v24, v24, v26\n"
"v_mfma_f32_16x16x32_bf16 v[84:87], a[40:43], a[144:147], v[84:87]\n"
"ds_read_b64_tr_b16 a[82:83], v15 offset:512\n"
"v_or_b32 v25, v25, v27\n"
"v_mfma_f32_16x16x32_bf16 a[4:7], a[40:43], a[152:155], a[4:7]\n"
"ds_read_b64_tr_b16 a[84:85], v15 offset:4096\n"
"v_or_b32 v24, v24, v25\n"
"v_mfma_f32_16x16x32_bf16 a[4:7], a[44:47], a[156:159], a[4:7]\n"
"ds_read_b64_tr_b16 a[86:87], v15 offset:4608\n"
"v_cmp_lt_u32 vcc, 15, v24\n"
"v_mfma_f32_16x16x32_bf16 a[0:3], a[36:39], a[156:159], a[0:3]\n"
"buffer_store_dwordx4 a[188:191], v1, s[36:39], s74 offen offset:2048 sc1\n"
"ds_read_b64_tr_b16 a[88:89], v15 offset:8\n"
"s_cbranch_vccnz L_slow3\n"
"L_back3:\n"
"v_mfma_f32_16x16x32_bf16 a[0:3], a[32:35], a[152:155], a[0:3]\n"
"ds_read_b64_tr_b16 a[90:91], v15 offset:520\n"
"v_mfma_f32_16x16x32_bf16 v[88:91], a[48:51], a[96:99], v[88:91]\n"
"ds_read_b64_tr_b16 a[92:93], v15 offset:4104\n"
"v_mfma_f32_16x16x32_bf16 v[88:91], a[52:55], a[100:103], v[88:91]\n"
"global_load_dwordx4 a[32:35], v0, s[34:35] offset:-4096 sc1\n"
"s_barrier\n"
"v_mfma_f32_16x16x32_bf16 v[116:119], a[60:63], a[100:103], v[116:119]\n"
"ds_read_b64_tr_b16 a[94:95], v15 offset:4616\n"
"v_mfma_f32_16x16x32_bf16 v[116:119], a[56:59], a[96:99], v[116:119]\n"
"ds_write_b64 v2, a[192:193] offset:16384\n"
"v_mfma_f32_16x16x32_bf16 v[120:123], a[56:59], a[104:107], v[120:123]\n"
"ds_write_b64 v2, a[194:195] offset:16392\n"
"v_mfma_f32_16x16x32_bf16 v[120:123], a[60:63], a[108:111], v[120:123]\n"
"ds_write_b64 v2, a[196:197] offset:17408\n"
"v_mfma_f32_16x16x32_bf16 v[92:95], a[52:55], a[108:111], v[92:95]\n"
"ds_read_b128 a[160:163], v7 offset:24576\n"
"v_mfma_f32_16x16x32_bf16 v[92:95], a[48:51], a[104:107], v[92:95]\n"
"ds_write_b64 v2, a[198:199] offset:17416\n"
"v_mfma_f32_16x16x32_bf16 v[96:99], a[48:51], a[112:115], v[96:99]\n"
"s_waitcnt lgkmcnt(5)\n"
"ds_read_b128 a[164:167], v8 offset:24576\n"
"v_mfma_f32_16x16x32_bf16 v[96:99], a[52:55], a[116:119], v[96:99]\n"
"global_load_dwordx4 a[36:39], v0, s[34:35] offset:-3072 sc1\n"
"v_mfma_f32_16x16x32_bf16 v[124:127], a[60:63], a[116:119], v[124:127]\n"
"ds_read_b128 a[168:171], v7 offset:26624\n"
"v_mfma_f32_16x16x32_bf16 v[124:127], a[56:59], a[112:115], v[124:127]\n"
"ds_read_b128 a[172:175], v8 offset:26624\n"
"v_mfma_f32_16x16x32_bf16 v[128:131], a[56:59], a[120:123], v[128:131]\n"
"ds_write_b64 v2, a[200:201] offset:18432\n"
"v_mfma_f32_16x16x32_bf16 v[128:131], a[60:63], a[124:127], v[128:131]\n"
"ds_read_b128 a[176:179], v7 offset:28672\n"
"v_mfma_f32_16x16x32_bf16 v[100:103], a[52:55], a[124:127], v[100:103]\n"
"ds_read_b128 a[180:183], v8 offset:28672\n"
"v_mfma_f32_16x16x32_bf16 v[100:103], a[48:51], a[120:123], v[100:103]\n"
"ds_write_b64 v2, a[202:203] offset:18440\n"
"v_mfma_f32_16x16x32_bf16 v[104:107], a[48:51], a[128:131], v[104:107]\n"
"ds_read_b128 a[184:187], v7 offset:30720\n"
"v_mfma_f32_16x16x32_bf16 v[104:107], a[52:55], a[132:135], v[104:107]\n"
"global_load_dwordx4 a[40:43], v0, s[34:35] offset:-2048 sc1\n"
"v_mfma_f32_16x16x32_bf16 v[132:135], a[60:63], a[132:135], v[132:135]\n"
"ds_read_b128 a[188:191], v8 offset:30720\n"
"v_mfma_f32_16x16x32_bf16 v[132:135], a[56:59], a[128:131], v[132:135]\n"
"ds_write_b64 v2, a[204:205] offset:19456\n"
"v_mfma_f32_16x16x32_bf16 v[136:139], a[56:59], a[136:139], v[136:139]\n"
"ds_write_b64 v2, a[206:207] offset:19464\n"
"v_mfma_f32_16x16x32_bf16 v[136:139], a[60:63], a[140:143], v[136:139]\n"
"ds_write_b64 v2, a[208:209] offset:20480\n"
"v_mfma_f32_16x16x32_bf16 v[108:111], a[52:55], a[140:143], v[108:111]\n"
"ds_write_b64 v2, a[210:211] offset:20488\n"
"v_mfma_f32_16x16x32_bf16 v[108:111], a[48:51], a[136:139], v[108:111]\n"
"ds_write_b64 v2, a[212:213] offset:21504\n"
"v_mfma_f32_16x16x32_bf16 v[112:115], a[48:51], a[144:147], v[112:115]\n"
"ds_write_b64 v2, a[214:215] offset:21512\n"
"v_mfma_f32_16x16x32_bf16 v[112:115], a[52:55], a[148:151], v[112:115]\n"
"global_load_dwordx4 a[44:47], v0, s[34:35] offset:-1024 sc1\n"
"v_mfma_f32_16x16x32_bf16 v[140:143], a[60:63], a[148:151], v[140:143]\n"
"ds_write_b64 v2, a[216:217] offset:22528\n"
"v_mfma_f32_16x16x32_bf16 v[140:143], a[56:59], a[144:147], v[140:143]\n"
"v_mfma_f32_16x16x32_bf16 a[12:15], a[56:59], a[152:155], a[12:15]\n"
"ds_write_b64 v2, a[218:219] offset:22536\n"
"v_mfma_f32_16x16x32_bf16 a[12:15], a[60:63], a[156:159], a[12:15]\n"
"v_mfma_f32_16x16x32_bf16 a[8:11], a[52:55], a[156:159], a[8:11]\n"
"ds_write_b64 v2, a[220:221] offset:23552\n"
"v_mfma_f32_16x16x32_bf16 a[8:11], a[48:51], a[152:155], a[8:11]\n"
"v_mfma_f32_16x16x32_bf16 v[144:147], a[64:67], a[96:99], v[144:147]\n"
"ds_write_b64 v2, a[222:223] offset:23560\n"
"v_mfma_f32_16x16x32_bf16 v[144:147], a[68:71], a[100:103], v[144:147]\n"
"global_load_dwordx4 a[48:51], v0, s[34:35] offset:0 sc1\n"
"v_mfma_f32_16x16x32_bf16 v[172:175], a[76:79], a[100:103], v[172:175]\n"
"ds_write_b64 v2, a[224:225] offset:24576\n"
"v_mfma_f32_16x16x32_bf16 v[172:175], a[72:75], a[96:99], v[172:175]\n"
"ds_write_b64 v2, a[226:227] offset:24584\n"
"v_mfma_f32_16x16x32_bf16 v[176:179], a[72:75], a[104:107], v[176:179]\n"
"v_mfma_f32_16x16x32_bf16 v[176:179], a[76:79], a[108:111], v[176:179]\n"
"ds_write_b64 v2, a[228:229] offset:25600\n"
"v_mfma_f32_16x16x32_bf16 v[148:151], a[68:71], a[108:111], v[148:151]\n"
"v_mfma_f32_16x16x32_bf16 v[148:151], a[64:67], a[104:107], v[148:151]\n"
"ds_write_b64 v2, a[230:231] offset:25608\n"
"v_mfma_f32_16x16x32_bf16 v[152:155], a[64:67], a[112:115], v[152:155]\n"
"ds_write_b64 v2, a[232:233] offset:26624\n"
"v_mfma_f32_16x16x32_bf16 v[152:155], a[68:71], a[116:119], v[152:155]\n"
"global_load_dwordx4 a[52:55], v0, s[34:35] offset:1024 sc1\n"
"v_mfma_f32_16x16x32_bf16 v[180:183], a[76:79], a[116:119], v[180:183]\n"
"v_mfma_f32_16x16x32_bf16 v[180:183], a[72:75], a[112:115], v[180:183]\n"
"ds_write_b64 v2, a[234:235] offset:26632\n"
"v_mfma_f32_16x16x32_bf16 v[184:187], a[72:75], a[120:123], v[184:187]\n"
"v_mfma_f32_16x16x32_bf16 v[184:187], a[76:79], a[124:127], v[184:187]\n"
"ds_write_b64 v2, a[236:237] offset:27648\n"
"v_mfma_f32_16x16x32_bf16 v[156:159], a[68:71], a[124:127], v[156:159]\n"
"v_mfma_f32_16x16x32_bf16 v[156:159], a[64:67], a[120:123], v[156:159]\n"
"ds_write_b64 v2, a[238:239] offset:27656\n"
"v_mfma_f32_16x16x32_bf16 v[160:163], a[64:67], a[128:131], v[160:163]\n"
"ds_write_b64 v2, a[240:241] offset:28672\n"
"v_mfma_f32_16x16x32_bf16 v[160:163], a[68:71], a[132:135], v[160:163]\n"
"global_load_dwordx4 a[56:59], v0, s[34:35] offset:2048 sc1\n"
"v_mfma_f32_16x16x32_bf16 v[188:191], a[76:79], a[132:135], v[188:191]\n"
"ds_write_b64 v2, a[242:243] offset:28680\n"
"v_mfma_f32_16x16x32_bf16 v[188:191], a[72:75], a[128:131], v[188:191]\n"
"v_mfma_f32_16x16x32_bf16 v[192:195], a[72:75], a[136:139], v[192:195]\n"
"ds_write_b64 v2, a[244:245] offset:29696\n"
"v_mfma_f32_16x16x32_bf16 v[192:195], a[76:79], a[140:143], v[192:195]\n"
"v_mfma_f32_16x16x32_bf16 v[164:167], a[68:71], a[140:143], v[164:167]\n"
"ds_write_b64 v2, a[246:247] offset:29704\n"
"v_mfma_f32_16x16x32_bf16 v[164:167], a[64:67], a[136:139], v[164:167]\n"
"s_add_u32 s74, s78, 2\n"
"v_mfma_f32_16x16x32_bf16 v[168:171], a[64:67], a[144:147], v[168:171]\n"
"ds_write_b64 v2, a[248:249] offset:30720\n"
"v_mfma_f32_16x16x32_bf16 v[168:171], a[68:71], a[148:151], v[168:171]\n"
"global_load_dwordx4 a[60:63], v0, s[34:35] offset:3072 sc1\n"
"v_mov_b32 v28, s74\n"
"v_mfma_f32_16x16x32_bf16 v[196:199], a[76:79], a[148:151], v[196:199]\n"
"v_mov_b32 v29, s79\n"
"v_mfma_f32_16x16x32_bf16 v[196:199], a[72:75], a[144:147], v[196:199]\n"
"ds_write_b64 v2, a[250:251] offset:30728\n"
"v_mfma_f32_16x16x32_bf16 a[20:23], a[72:75], a[152:155], a[20:23]\n"
"s_waitcnt vmcnt(8)\n"
"buffer_store_dwordx2 v[28:29], off, s[56:59], %24 sc1\n"
"s_add_u32 s34, s34, 8192\n"
"v_mfma_f32_16x16x32_bf16 a[20:23], a[76:79], a[156:159], a[20:23]\n"
"ds_write_b64 v2, a[252:253] offset:31744\n"
"v_mfma_f32_16x16x32_bf16 a[16:19], a[68:71], a[156:159], a[16:19]\n"
"ds_write_b64 v2, a[254:255] offset:31752\n"
"v_mfma_f32_16x16x32_bf16 a[16:19], a[64:67], a[152:155], a[16:19]\n"
"v_mfma_f32_16x16x32_bf16 v[200:203], a[80:83], a[96:99], v[200:203]\n"
"v_lshrrev_b32 v16, 4, v0\n"
"v_mfma_f32_16x16x32_bf16 v[200:203], a[84:87], a[100:103], v[200:203]\n"
"global_load_dwordx4 a[64:67], v0, s[34:35] offset:-4096 sc1\n"
"v_mfma_f32_16x16x32_bf16 v[228:231], a[92:95], a[100:103], v[228:231]\n"
"ds_read_b128 a[100:103], v2 offset:17408\n"
"v_mfma_f32_16x16x32_bf16 v[228:231], a[88:91], a[96:99], v[228:231]\n"
"ds_read_b128 a[96:99], v2 offset:16384\n"
"v_mfma_f32_16x16x32_bf16 v[232:235], a[88:91], a[104:107], v[232:235]\n"
"v_and_b32 v16, 0x30, v16\n"
"v_mfma_f32_16x16x32_bf16 v[232:235], a[92:95], a[108:111], v[232:235]\n"
"buffer_load_dwordx2 v[18:19], v16, s[44:47], s61 offen offset:128\n"
"v_mfma_f32_16x16x32_bf16 v[204:207], a[84:87], a[108:111], v[204:207]\n"
"ds_read_b128 a[108:111], v2 offset:19456\n"
"v_mfma_f32_16x16x32_bf16 v[204:207], a[80:83], a[104:107], v[204:207]\n"
"ds_read_b128 a[104:107], v2 offset:18432\n"
"v_mfma_f32_16x16x32_bf16 v[208:211], a[80:83], a[112:115], v[208:211]\n"
"v_lshrrev_b32 v20, 4, v0\n"
"v_mfma_f32_16x16x32_bf16 v[208:211], a[84:87], a[116:119], v[208:211]\n"
"global_load_dwordx4 a[68:71], v0, s[34:35] offset:-3072 sc1\n"
"v_mfma_f32_16x16x32_bf16 v[236:239], a[92:95], a[116:119], v[236:239]\n"
"ds_read_b128 a[116:119], v2 offset:21504\n"
"v_mfma_f32_16x16x32_bf16 v[236:239], a[88:91], a[112:115], v[236:239]\n"
"ds_read_b128 a[112:115], v2 offset:20480\n"
"v_mfma_f32_16x16x32_bf16 v[240:243], a[88:91], a[120:123], v[240:243]\n"
"v_and_b32 v20, 0x30, v20\n"
"v_mfma_f32_16x16x32_bf16 v[240:243], a[92:95], a[124:127], v[240:243]\n"
"buffer_load_dwordx2 v[22:23], v20, s[44:47], s61 offen offset:136\n"
"v_mfma_f32_16x16x32_bf16 v[212:215], a[84:87], a[124:127], v[212:215]\n"
"ds_read_b128 a[124:127], v2 offset:23552\n"
"v_mfma_f32_16x16x32_bf16 v[212:215], a[80:83], a[120:123], v[212:215]\n"
"ds_read_b128 a[120:123], v2 offset:22528\n"
"v_mfma_f32_16x16x32_bf16 v[216:219], a[80:83], a[128:131], v[216:219]\n"
"v_lshrrev_b32 v24, 4, v0\n"
"v_mfma_f32_16x16x32_bf16 v[216:219], a[84:87], a[132:135], v[216:219]\n"
"global_load_dwordx4 a[72:75], v0, s[34:35] offset:-2048 sc1\n"
"v_mfma_f32_16x16x32_bf16 v[244:247], a[92:95], a[132:135], v[244:247]\n"
"ds_read_b128 a[132:135], v2 offset:25600\n"
"v_and_b32 v24, 0x30, v24\n"
"v_mfma_f32_16x16x32_bf16 v[244:247], a[88:91], a[128:131], v[244:247]\n"
"ds_read_b128 a[128:131], v2 offset:24576\n"
"v_mfma_f32_16x16x32_bf16 v[248:251], a[88:91], a[136:139], v[248:251]\n"
"buffer_load_dwordx2 v[26:27], v24, s[44:47], s61 offen offset:192\n"
"v_mfma_f32_16x16x32_bf16 v[248:251], a[92:95], a[140:143], v[248:251]\n"
"v_lshrrev_b32 v28, 4, v0\n"
"v_mfma_f32_16x16x32_bf16 v[220:223], a[84:87], a[140:143], v[220:223]\n"
"ds_read_b128 a[140:143], v2 offset:27648\n"
"v_mfma_f32_16x16x32_bf16 v[220:223], a[80:83], a[136:139], v[220:223]\n"
"ds_read_b128 a[136:139], v2 offset:26624\n"
"v_mfma_f32_16x16x32_bf16 v[224:227], a[80:83], a[144:147], v[224:227]\n"
"v_and_b32 v28, 0x30, v28\n"
"v_mfma_f32_16x16x32_bf16 v[224:227], a[84:87], a[148:151], v[224:227]\n"
"global_load_dwordx4 a[76:79], v0, s[34:35] offset:-1024 sc1\n"
"v_mfma_f32_16x16x32_bf16 v[252:255], a[92:95], a[148:151], v[252:255]\n"
"ds_read_b128 a[148:151], v2 offset:29696\n"
"v_mfma_f32_16x16x32_bf16 v[252:255], a[88:91], a[144:147], v[252:255]\n"
"ds_read_b128 a[144:147], v2 offset:28672\n"
"v_mfma_f32_16x16x32_bf16 a[28:31], a[88:91], a[152:155], a[28:31]\n"
"buffer_load_dwordx2 v[30:31], v28, s[44:47], s61 offen offset:200\n"
"v_mfma_f32_16x16x32_bf16 a[28:31], a[92:95], a[156:159], a[28:31]\n"
"v_mfma_f32_16x16x32_bf16 a[24:27], a[84:87], a[156:159], a[24:27]\n"
"ds_read_b128 a[156:159], v2 offset:31744\n"
"v_mfma_f32_16x16x32_bf16 a[24:27], a[80:83], a[152:155], a[24:27]\n"
"s_waitcnt vmcnt(13) lgkmcnt(11)\n"
"ds_read_b128 a[152:155], v2 offset:30720\n"
"v_mfma_f32_16x16x32_bf16 v[32:35], a[32:35], a[96:99], v[32:35]\n"
"v_mfma_f32_16x16x32_bf16 v[32:35], a[36:39], a[100:103], v[32:35]\n"
"global_load_dwordx4 a[80:83], v0, s[34:35] offset:0 sc1\n"
"v_mfma_f32_16x16x32_bf16 v[60:63], a[44:47], a[100:103], v[60:63]\n"
"s_barrier\n"
"v_mfma_f32_16x16x32_bf16 v[60:63], a[40:43], a[96:99], v[60:63]\n"
"v_mfma_f32_16x16x32_bf16 v[64:67], a[40:43], a[104:107], v[64:67]\n"
"v_mfma_f32_16x16x32_bf16 v[64:67], a[44:47], a[108:111], v[64:67]\n"
"v_mfma_f32_16x16x32_bf16 v[36:39], a[36:39], a[108:111], v[36:39]\n"
"v_mfma_f32_16x16x32_bf16 v[36:39], a[32:35], a[104:107], v[36:39]\n"
"s_waitcnt lgkmcnt(8)\n"
"v_mfma_f32_16x16x32_bf16 v[40:43], a[32:35], a[112:115], v[40:43]\n"
"v_mfma_f32_16x16x32_bf16 v[40:43], a[36:39], a[116:119], v[40:43]\n"
"global_load_dwordx4 a[84:87], v0, s[34:35] offset:1024 sc1\n"
"v_mfma_f32_16x16x32_bf16 v[68:71], a[44:47], a[116:119], v[68:71]\n"
"v_mfma_f32_16x16x32_bf16 v[68:71], a[40:43], a[112:115], v[68:71]\n"
"v_mfma_f32_16x16x32_bf16 v[72:75], a[40:43], a[120:123], v[72:75]\n"
"v_mfma_f32_16x16x32_bf16 v[72:75], a[44:47], a[124:127], v[72:75]\n"
"s_add_u32 s61, s61, 2048\n"
"v_mfma_f32_16x16x32_bf16 v[44:47], a[36:39], a[124:127], v[44:47]\n"
"v_mfma_f32_16x16x32_bf16 v[44:47], a[32:35], a[120:123], v[44:47]\n"
"s_waitcnt lgkmcnt(4)\n"
"v_mfma_f32_16x16x32_bf16 v[48:51], a[32:35], a[128:131], v[48:51]\n"
"s_waitcnt vmcnt(8)\n"
"v_lshlrev_b32 v16, 16, v18\n"
"v_mfma_f32_16x16x32_bf16 v[48:51], a[36:39], a[132:135], v[48:51]\n"
"global_load_dwordx4 a[88:91], v0, s[34:35] offset:2048 sc1\n"
"v_and_b32 v17, s63, v18\n"
"v_mfma_f32_16x16x32_bf16 v[76:79], a[44:47], a[132:135], v[76:79]\n"
"v_lshlrev_b32 v18, 16, v19\n"
"v_and_b32 v19, s63, v19\n"
"v_mfma_f32_16x16x32_bf16 v[76:79], a[40:43], a[128:131], v[76:79]\n"
"v_add_f32 v144, v144, v16\n"
"v_mfma_f32_16x16x32_bf16 v[80:83], a[40:43], a[136:139], v[80:83]\n"
"v_add_f32 v145, v145, v17\n"
"v_add_f32 v146, v146, v18\n"
"v_mfma_f32_16x16x32_bf16 v[80:83], a[44:47], a[140:143], v[80:83]\n"
"v_add_f32 v147, v147, v19\n"
"v_mfma_f32_16x16x32_bf16 v[52:55], a[36:39], a[140:143], v[52:55]\n"
"v_add_f32 v148, v148, v16\n"
"v_mfma_f32_16x16x32_bf16 v[52:55], a[32:35], a[136:139], v[52:55]\n"
"s_mov_b32 s99, 0\n"
"v_add_f32 v149, v149, v17\n"
"s_waitcnt lgkmcnt(0)\n"
"v_mfma_f32_16x16x32_bf16 v[56:59], a[32:35], a[144:147], v[56:59]\n"
"s_mov_b32 s98, 0\n"
"v_add_f32 v150, v150, v18\n"
"v_add_f32 v151, v151, v19\n"
"v_mfma_f32_16x16x32_bf16 v[56:59], a[36:39], a[148:151], v[56:59]\n"
"global_load_dwordx4 a[92:95], v0, s[34:35] offset:3072 sc1\n"
"s_memtime s[76:77]\n"
"s_load_dword s98, s[96:97], 8448 glc\n"
"v_mfma_f32_16x16x32_bf16 v[84:87], a[44:47], a[148:151], v[84:87]\n"
"v_add_f32 v152, v152, v16\n"
"v_add_f32 v153, v153, v17\n"
"v_mfma_f32_16x16x32_bf16 v[84:87], a[40:43], a[144:147], v[84:87]\n"
"v_add_f32 v154, v154, v18\n"
"v_add_f32 v155, v155, v19\n"
"v_mfma_f32_16x16x32_bf16 a[4:7], a[40:43], a[152:155], a[4:7]\n"
"s_add_u32 s34, s34, 8192\n"
"v_add_f32 v156, v156, v16\n"
"v_mfma_f32_16x16x32_bf16 a[4:7], a[44:47], a[156:159], a[4:7]\n"
"v_add_f32 v157, v157, v17\n"
"v_mfma_f32_16x16x32_bf16 a[0:3], a[36:39], a[156:159], a[0:3]\n"
"v_add_f32 v158, v158, v18\n"
"v_add_f32 v159, v159, v19\n"
"v_mfma_f32_16x16x32_bf16 a[0:3], a[32:35], a[152:155], a[0:3]\n"
"v_add_f32 v160, v160, v16\n"
"v_mfma_f32_16x16x32_bf16 v[88:91], a[48:51], a[96:99], v[88:91]\n"
"v_add_f32 v161, v161, v17\n"
"v_add_f32 v162, v162, v18\n"
"v_mfma_f32_16x16x32_bf16 v[88:91], a[52:55], a[100:103], v[88:91]\n"
"global_load_dwordx4 a[32:35], v0, s[34:35] offset:-4096 sc1\n"
"v_add_f32 v163, v163, v19\n"
"v_mfma_f32_16x16x32_bf16 v[116:119], a[60:63], a[100:103], v[116:119]\n"
"s_mov_b32 s29, s76\n"
"ds_read_b128 a[100:103], v3 offset:17408\n"
"v_add_f32 v164, v164, v16\n"
"v_mfma_f32_16x16x32_bf16 v[116:119], a[56:59], a[96:99], v[116:119]\n"
"ds_read_b128 a[96:99], v3 offset:16384\n"
"v_add_f32 v165, v165, v17\n"
"v_mfma_f32_16x16x32_bf16 v[120:123], a[56:59], a[104:107], v[120:123]\n"
"s_sub_u32 s99, s99, s98\n"
"v_add_f32 v166, v166, v18\n"
"v_add_f32 v167, v167, v19\n"
"v_mfma_f32_16x16x32_bf16 v[120:123], a[60:63], a[108:111], v[120:123]\n"
"s_sub_u32 s99, s99, s98\n"
"v_add_f32 v168, v168, v16\n"
"v_add_f32 v169, v169, v17\n"
"v_mfma_f32_16x16x32_bf16 v[92:95], a[52:55], a[108:111], v[92:95]\n"
"s_sub_u32 s99, s99, s98\n"
"ds_read_b128 a[108:111], v3 offset:19456\n"
"v_add_f32 v170, v170, v18\n"
"v_mfma_f32_16x16x32_bf16 v[92:95], a[48:51], a[104:107], v[92:95]\n"
"s_sub_u32 s99, s99, s98\n"
"ds_read_b128 a[104:107], v3 offset:18432\n"
"v_add_f32 v171, v171, v19\n"
"v_mfma_f32_16x16x32_bf16 v[96:99], a[48:51], a[112:115], v[96:99]\n"
"s_sub_u32 s99, s99, s98\n"
"v_accvgpr_read_b32 v12, a16\n"
"v_accvgpr_read_b32 v13, a17\n"
"v_mfma_f32_16x16x32_bf16 v[96:99], a[52:55], a[116:119], v[96:99]\n"
"global_load_dwordx4 a[36:39], v0, s[34:35] offset:-3072 sc1\n"
"s_sub_u32 s99, s99, s98\n"
"v_mfma_f32_16x16x32_bf16 v[124:127], a[60:63], a[116:119], v[124:127]\n"
"s_sub_u32 s99, s99, s98\n"
"ds_read_b128 a[116:119], v3 offset:21504\n"
"v_accvgpr_read_b32 v14, a18\n"
"v_mfma_f32_16x16x32_bf16 v[124:127], a[56:59], a[112:115], v[124:127]\n"
"s_sub_u32 s99, s99, s98\n"
"ds_read_b128 a[112:115], v3 offset:20480\n"
"v_accvgpr_read_b32 v15, a19\n"
"v_mfma_f32_16x16x32_bf16 v[128:131], a[56:59], a[120:123], v[128:131]\n"
"s_sub_u32 s99, s99, s98\n"
"v_add_f32 v12, v12, v16\n"
"v_add_f32 v13, v13, v17\n"
"v_mfma_f32_16x16x32_bf16 v[128:131], a[60:63], a[124:127], v[128:131]\n"
"s_sub_u32 s99, s99, s98\n"
"v_add_f32 v14, v14, v18\n"
"v_add_f32 v15, v15, v19\n"
"v_mfma_f32_16x16x32_bf16 v[100:103], a[52:55], a[124:127], v[100:103]\n"
"s_sub_u32 s99, s99, s98\n"
"ds_read_b128 a[124:127], v3 offset:23552\n"
"v_accvgpr_write_b32 a16, v12\n"
"v_mfma_f32_16x16x32_bf16 v[100:103], a[48:51], a[120:123], v[100:103]\n"
"s_sub_u32 s99, s99, s98\n"
"ds_read_b128 a[120:123], v3 offset:22528\n"
"v_accvgpr_write_b32 a17, v13\n"
"v_mfma_f32_16x16x32_bf16 v[104:107], a[48:51], a[128:131], v[104:107]\n"
"s_sub_u32 s99, s99, s98\n"
"v_accvgpr_write_b32 a18, v14\n"
"v_accvgpr_write_b32 a19, v15\n"
"v_mfma_f32_16x16x32_bf16 v[104:107], a[52:55], a[132:135], v[104:107]\n"
"s_sub_u32 s99, s99, s98\n"
"s_waitcnt vmcnt(10)\n"
"global_load_dwordx4 a[40:43], v0, s[34:35] offset:-2048 sc1\n"
"v_mfma_f32_16x16x32_bf16 v[132:135], a[60:63], a[132:135], v[132:135]\n"
"s_sub_u32 s99, s99, s98\n"
"ds_read_b128 a[132:135], v3 offset:25600\n"
"v_lshlrev_b32 v20, 16, v22\n"
"v_mfma_f32_16x16x32_bf16 v[132:135], a[56:59], a[128:131], v[132:135]\n"
"s_sub_u32 s99, s99, s98\n"
"ds_read_b128 a[128:131], v3 offset:24576\n"
"v_and_b32 v21, s63, v22\n"
"v_mfma_f32_16x16x32_bf16 v[136:139], a[56:59], a[136:139], v[136:139]\n"
"s_sub_u32 s99, s99, s98\n"
"v_lshlrev_b32 v22, 16, v23\n"
"v_and_b32 v23, s63, v23\n"
"v_mfma_f32_16x16x32_bf16 v[136:139], a[60:63], a[140:143], v[136:139]\n"
"s_sub_u32 s99, s99, s98\n"
"v_add_f32 v172, v172, v20\n"
"v_add_f32 v173, v173, v21\n"
"v_mfma_f32_16x16x32_bf16 v[108:111], a[52:55], a[140:143], v[108:111]\n"
"s_sub_u32 s99, s99, s98\n"
"ds_read_b128 a[140:143], v3 offset:27648\n"
"v_add_f32 v174, v174, v22\n"
"v_mfma_f32_16x16x32_bf16 v[108:111], a[48:51], a[136:139], v[108:111]\n"
"s_sub_u32 s99, s99, s98\n"
"ds_read_b128 a[136:139], v3 offset:26624\n"
"v_add_f32 v175, v175, v23\n"
"v_mfma_f32_16x16x32_bf16 v[112:115], a[48:51], a[144:147], v[112:115]\n"
"s_sub_u32 s99, s99, s98\n"
"v_add_f32 v176, v176, v20\n"
"v_add_f32 v177, v177, v21\n"
"v_mfma_f32_16x16x32_bf16 v[112:115], a[52:55], a[148:151], v[112:115]\n"
"global_load_dwordx4 a[44:47], v0, s[34:35] offset:-1024 sc1\n"
"s_sub_u32 s99, s99, s98\n"
"v_mfma_f32_16x16x32_bf16 v[140:143], a[60:63], a[148:151], v[140:143]\n"
"s_sub_u32 s99, s99, s98\n"
"ds_read_b128 a[148:151], v3 offset:29696\n"
"v_add_f32 v178, v178, v22\n"
"v_mfma_f32_16x16x32_bf16 v[140:143], a[56:59], a[144:147], v[140:143]\n"
"s_sub_u32 s99, s99, s98\n"
"ds_read_b128 a[144:147], v3 offset:28672\n"
"v_add_f32 v179, v179, v23\n"
"v_mfma_f32_16x16x32_bf16 a[12:15], a[56:59], a[152:155], a[12:15]\n"
"s_memtime s[76:77]\n"
"v_add_f32 v180, v180, v20\n"
"v_add_f32 v181, v181, v21\n"
"v_mfma_f32_16x16x32_bf16 a[12:15], a[60:63], a[156:159], a[12:15]\n"
"v_add_f32 v182, v182, v22\n"
"v_add_f32 v183, v183, v23\n"
"v_mfma_f32_16x16x32_bf16 a[8:11], a[52:55], a[156:159], a[8:11]\n"
"ds_read_b128 a[156:159], v3 offset:31744\n"
"v_add_f32 v184, v184, v20\n"
"v_mfma_f32_16x16x32_bf16 a[8:11], a[48:51], a[152:155], a[8:11]\n"
"s_waitcnt vmcnt(9) lgkmcnt(11)\n"
"ds_read_b128 a[152:155], v3 offset:30720\n"
"v_add_f32 v185, v185, v21\n"
"v_mfma_f32_16x16x32_bf16 v[32:35], a[64:67], a[96:99], v[32:35]\n"
"v_add_f32 v186, v186, v22\n"
"v_add_f32 v187, v187, v23\n"
"v_mfma_f32_16x16x32_bf16 v[32:35], a[68:71], a[100:103], v[32:35]\n"
"global_load_dwordx4 a[48:51], v0, s[34:35] offset:0 sc1\n"
"v_add_f32 v188, v188, v20\n"
"v_mfma_f32_16x16x32_bf16 v[60:63], a[76:79], a[100:103], v[60:63]\n"
"v_add_f32 v189, v189, v21\n"
"v_mfma_f32_16x16x32_bf16 v[60:63], a[72:75], a[96:99], v[60:63]\n"
"v_add_f32 v190, v190, v22\n"
"v_add_f32 v191, v191, v23\n"
"v_mfma_f32_16x16x32_bf16 v[64:67], a[72:75], a[104:107], v[64:67]\n"
"v_add_f32 v192, v192, v20\n"
"v_mfma_f32_16x16x32_bf16 v[64:67], a[76:79], a[108:111], v[64:67]\n"
"v_add_f32 v193, v193, v21\n"
"v_mfma_f32_16x16x32_bf16 v[36:39], a[68:71], a[108:111], v[36:39]\n"
"v_add_f32 v194, v194, v22\n"
"v_add_f32 v195, v195, v23\n"
"v_mfma_f32_16x16x32_bf16 v[36:39], a[64:67], a[104:107], v[36:39]\n"
"s_waitcnt lgkmcnt(8)\n"
"v_add_f32 v196, v196, v20\n"
"v_mfma_f32_16x16x32_bf16 v[40:43], a[64:67], a[112:115], v[40:43]\n"
"v_add_f32 v197, v197, v21\n"
"v_mfma_f32_16x16x32_bf16 v[40:43], a[68:71], a[116:119], v[40:43]\n"
"global_load_dwordx4 a[52:55], v0, s[34:35] offset:1024 sc1\n"
"v_add_f32 v198, v198, v22\n"
"v_mfma_f32_16x16x32_bf16 v[68:71], a[76:79], a[116:119], v[68:71]\n"
"v_add_f32 v199, v199, v23\n"
"v_accvgpr_read_b32 v12, a20\n"
"v_mfma_f32_16x16x32_bf16 v[68:71], a[72:75], a[112:115], v[68:71]\n"
"v_accvgpr_read_b32 v13, a21\n"
"v_mfma_f32_16x16x32_bf16 v[72:75], a[72:75], a[120:123], v[72:75]\n"
"v_accvgpr_read_b32 v14, a22\n"
"v_accvgpr_read_b32 v15, a23\n"
"v_mfma_f32_16x16x32_bf16 v[72:75], a[76:79], a[124:127], v[72:75]\n"
"v_add_f32 v12, v12, v20\n"
"v_mfma_f32_16x16x32_bf16 v[44:47], a[68:71], a[124:127], v[44:47]\n"
"v_add_f32 v13, v13, v21\n"
"v_mfma_f32_16x16x32_bf16 v[44:47], a[64:67], a[120:123], v[44:47]\n"
"s_waitcnt lgkmcnt(4)\n"
"v_add_f32 v14, v14, v22\n"
"v_mfma_f32_16x16x32_bf16 v[48:51], a[64:67], a[128:131], v[48:51]\n"
"v_add_f32 v15, v15, v23\n"
"v_accvgpr_write_b32 a20, v12\n"
"v_mfma_f32_16x16x32_bf16 v[48:51], a[68:71], a[132:135], v[48:51]\n"
"global_load_dwordx4 a[56:59], v0, s[34:35] offset:2048 sc1\n"
"v_accvgpr_write_b32 a21, v13\n"
"v_mfma_f32_16x16x32_bf16 v[76:79], a[76:79], a[132:135], v[76:79]\n"
"s_waitcnt lgkmcnt(0)\n"
"v_accvgpr_write_b32 a22, v14\n"
"v_mfma_f32_16x16x32_bf16 v[76:79], a[72:75], a[128:131], v[76:79]\n"
"s_sub_u32 s29, s76, s29\n"
"v_accvgpr_write_b32 a23, v15\n"
"v_lshlrev_b32 v24, 16, v26\n"
"v_mfma_f32_16x16x32_bf16 v[80:83], a[72:75], a[136:139], v[80:83]\n"
"s_cmp_lt_u32 s29, 620\n"
"s_cselect_b32 s99, s99, 0\n"
"v_and_b32 v25, s63, v26\n"
"v_mfma_f32_16x16x32_bf16 v[80:83], a[76:79], a[140:143], v[80:83]\n"
"s_max_u32 s31, s31, s99\n"
"v_lshlrev_b32 v26, 16, v27\n"
"v_mfma_f32_16x16x32_bf16 v[52:55], a[68:71], a[140:143], v[52:55]\n"
"v_and_b32 v27, s63, v27\n"
"v_add_f32 v200, v200, v24\n"
"v_mfma_f32_16x16x32_bf16 v[52:55], a[64:67], a[136:139], v[52:55]\n"
"s_mov_b32 s99, 0\n"
"v_add_f32 v201, v201, v25\n"
"v_mfma_f32_16x16x32_bf16 v[56:59], a[64:67], a[144:147], v[56:59]\n"
"s_mov_b32 s98, 0\n"
"v_add_f32 v202, v202, v26\n"
"v_add_f32 v203, v203, v27\n"
"v_mfma_f32_16x16x32_bf16 v[56:59], a[68:71], a[148:151], v[56:59]\n"
"global_load_dwordx4 a[60:63], v0, s[34:35] offset:3072 sc1\n"
"s_memtime s[76:77]\n"
"s_load_dword s98, s[96:97], 256 glc\n"
"v_mfma_f32_16x16x32_bf16 v[84:87], a[76:79], a[148:151], v[84:87]\n"
"v_add_f32 v204, v204, v24\n"
"v_add_f32 v205, v205, v25\n"
"v_mfma_f32_16x16x32_bf16 v[84:87], a[72:75], a[144:147], v[84:87]\n"
"v_add_f32 v206, v206, v26\n"
"v_mfma_f32_16x16x32_bf16 a[4:7], a[72:75], a[152:155], a[4:7]\n"
"s_add_u32 s34, s34, 8192\n"
"v_add_f32 v207, v207, v27\n"
"v_mfma_f32_16x16x32_bf16 a[4:7], a[76:79], a[156:159], a[4:7]\n"
"v_add_f32 v208, v208, v24\n"
"v_add_f32 v209, v209, v25\n"
"v_mfma_f32_16x16x32_bf16 a[0:3], a[68:71], a[156:159], a[0:3]\n"
"v_add_f32 v210, v210, v26\n"
"v_mfma_f32_16x16x32_bf16 a[0:3], a[64:67], a[152:155], a[0:3]\n"
"s_waitcnt vmcnt(8)\n"
"v_add_f32 v211, v211, v27\n"
"v_mfma_f32_16x16x32_bf16 v[88:91], a[80:83], a[96:99], v[88:91]\n"
"v_add_f32 v212, v212, v24\n"
"v_add_f32 v213, v213, v25\n"
"v_mfma_f32_16x16x32_bf16 v[88:91], a[84:87], a[100:103], v[88:91]\n"
"global_load_dwordx4 a[64:67], v0, s[34:35] offset:-4096 sc1\n"
"v_add_f32 v214, v214, v26\n"
"v_mfma_f32_16x16x32_bf16 v[116:119], a[92:95], a[100:103], v[116:119]\n"
"s_mov_b32 s29, s76\n"
"ds_read_b128 a[100:103], v4 offset:17408\n"
"v_add_f32 v215, v215, v27\n"
"v_mfma_f32_16x16x32_bf16 v[116:119], a[88:91], a[96:99], v[116:119]\n"
"ds_read_b128 a[96:99], v4 offset:16384\n"
"v_add_f32 v216, v216, v24\n"
"v_mfma_f32_16x16x32_bf16 v[120:123], a[88:91], a[104:107], v[120:123]\n"
"s_sub_u32 s99, s99, s98\n"
"v_add_f32 v217, v217, v25\n"
"v_add_f32 v218, v218, v26\n"
"v_mfma_f32_16x16x32_bf16 v[120:123], a[92:95], a[108:111], v[120:123]\n"
"s_sub_u32 s99, s99, s98\n"
"v_add_f32 v219, v219, v27\n"
"v_add_f32 v220, v220, v24\n"
"v_mfma_f32_16x16x32_bf16 v[92:95], a[84:87], a[108:111], v[92:95]\n"
"s_sub_u32 s99, s99, s98\n"
"ds_read_b128 a[108:111], v4 offset:19456\n"
"v_add_f32 v221, v221, v25\n"
"v_mfma_f32_16x16x32_bf16 v[92:95], a[80:83], a[104:107], v[92:95]\n"
"s_sub_u32 s99, s99, s98\n"
"ds_read_b128 a[104:107], v4 offset:18432\n"
"v_add_f32 v222, v222, v26\n"
"v_mfma_f32_16x16x32_bf16 v[96:99], a[80:83], a[112:115], v[96:99]\n"
"s_sub_u32 s99, s99, s98\n"
"v_add_f32 v223, v223, v27\n"
"v_add_f32 v224, v224, v24\n"
"v_mfma_f32_16x16x32_bf16 v[96:99], a[84:87], a[116:119], v[96:99]\n"
"global_load_dwordx4 a[68:71], v0, s[34:35] offset:-3072 sc1\n"
"s_sub_u32 s99, s99, s98\n"
"v_mfma_f32_16x16x32_bf16 v[124:127], a[92:95], a[116:119], v[124:127]\n"
"s_sub_u32 s99, s99, s98\n"
"ds_read_b128 a[116:119], v4 offset:21504\n"
"v_add_f32 v225, v225, v25\n"
"v_mfma_f32_16x16x32_bf16 v[124:127], a[88:91], a[112:115], v[124:127]\n"
"s_sub_u32 s99, s99, s98\n"
"ds_read_b128 a[112:115], v4 offset:20480\n"
"v_add_f32 v226, v226, v26\n"
"v_mfma_f32_16x16x32_bf16 v[128:131], a[88:91], a[120:123], v[128:131]\n"
"s_sub_u32 s99, s99, s98\n"
"v_add_f32 v227, v227, v27\n"
"v_accvgpr_read_b32 v12, a24\n"
"v_mfma_f32_16x16x32_bf16 v[128:131], a[92:95], a[124:127], v[128:131]\n"
"s_sub_u32 s99, s99, s98\n"
"v_accvgpr_read_b32 v13, a25\n"
"v_accvgpr_read_b32 v14, a26\n"
"v_mfma_f32_16x16x32_bf16 v[100:103], a[84:87], a[124:127], v[100:103]\n"
"s_sub_u32 s99, s99, s98\n"
"ds_read_b128 a[124:127], v4 offset:23552\n"
"v_accvgpr_read_b32 v15, a27\n"
"v_mfma_f32_16x16x32_bf16 v[100:103], a[80:83], a[120:123], v[100:103]\n"
"s_sub_u32 s99, s99, s98\n"
"ds_read_b128 a[120:123], v4 offset:22528\n"
"v_add_f32 v12, v12, v24\n"
"v_mfma_f32_16x16x32_bf16 v[104:107], a[80:83], a[128:131], v[104:107]\n"
"s_sub_u32 s99, s99, s98\n"
"v_add_f32 v13, v13, v25\n"
"v_add_f32 v14, v14, v26\n"
"v_mfma_f32_16x16x32_bf16 v[104:107], a[84:87], a[132:135], v[104:107]\n"
"global_load_dwordx4 a[72:75], v0, s[34:35] offset:-2048 sc1\n"
"s_sub_u32 s99, s99, s98\n"
"v_mfma_f32_16x16x32_bf16 v[132:135], a[92:95], a[132:135], v[132:135]\n"
"s_sub_u32 s99, s99, s98\n"
"ds_read_b128 a[132:135], v4 offset:25600\n"
"v_add_f32 v15, v15, v27\n"
"v_mfma_f32_16x16x32_bf16 v[132:135], a[88:91], a[128:131], v[132:135]\n"
"s_sub_u32 s99, s99, s98\n"
"ds_read_b128 a[128:131], v4 offset:24576\n"
"v_accvgpr_write_b32 a24, v12\n"
"v_mfma_f32_16x16x32_bf16 v[136:139], a[88:91], a[136:139], v[136:139]\n"
"s_sub_u32 s99, s99, s98\n"
"v_accvgpr_write_b32 a25, v13\n"
"v_accvgpr_write_b32 a26, v14\n"
"v_mfma_f32_16x16x32_bf16 v[136:139], a[92:95], a[140:143], v[136:139]\n"
"L_st3859:\n"
"s_cbranch_execz L_retA\n"
"s_sub_u32 s99, s99, s98\n"
"v_accvgpr_write_b32 a27, v15\n"
"v_lshlrev_b32 v28, 16, v30\n"
"v_mfma_f32_16x16x32_bf16 v[108:111], a[84:87], a[140:143], v[108:111]\n"
"s_sub_u32 s99, s99, s98\n"
"ds_read_b128 a[140:143], v4 offset:27648\n"
"v_and_b32 v29, s63, v30\n"
"v_mfma_f32_16x16x32_bf16 v[108:111], a[80:83], a[136:139], v[108:111]\n"
"s_sub_u32 s99, s99, s98\n"
"ds_read_b128 a[136:139], v4 offset:26624\n"
"v_lshlrev_b32 v30, 16, v31\n"
"v_mfma_f32_16x16x32_bf16 v[112:115], a[80:83], a[144:147], v[112:115]\n"
"s_sub_u32 s99, s99, s98\n"
"v_and_b32 v31, s63, v31\n"
"v_add_f32 v228, v228, v28\n"
"v_mfma_f32_16x16x32_bf16 v[112:115], a[84:87], a[148:151], v[112:115]\n"
"global_load_dwordx4 a[76:79], v0, s[34:35] offset:-1024 sc1\n"
"s_sub_u32 s99, s99, s98\n"
"v_mfma_f32_16x16x32_bf16 v[140:143], a[92:95], a[148:151], v[140:143]\n"
"s_sub_u32 s99, s99, s98\n"
"ds_read_b128 a[148:151], v4 offset:29696\n"
"v_add_f32 v229, v229, v29\n"
"v_mfma_f32_16x16x32_bf16 v[140:143], a[88:91], a[144:147], v[140:143]\n"
"s_sub_u32 s99, s99, s98\n"
"ds_read_b128 a[144:147], v4 offset:28672\n"
"v_add_f32 v230, v230, v30\n"
"v_mfma_f32_16x16x32_bf16 a[12:15], a[88:91], a[152:155], a[12:15]\n"
"s_memtime s[76:77]\n"
"v_add_f32 v231, v231, v31\n"
"v_add_f32 v232, v232, v28\n"
"v_mfma_f32_16x16x32_bf16 a[12:15], a[92:95], a[156:159], a[12:15]\n"
"v_add_f32 v233, v233, v29\n"
"v_add_f32 v234, v234, v30\n"
"v_mfma_f32_16x16x32_bf16 a[8:11], a[84:87], a[156:159], a[8:11]\n"
"ds_read_b128 a[156:159], v4 offset:31744\n"
"v_add_f32 v235, v235, v31\n"
"v_mfma_f32_16x16x32_bf16 a[8:11], a[80:83], a[152:155], a[8:11]\n"
"s_waitcnt vmcnt(8) lgkmcnt(11)\n"
"ds_read_b128 a[152:155], v4 offset:30720\n"
"v_add_f32 v236, v236, v28\n"
"v_mfma_f32_16x16x32_bf16 v[32:35], a[32:35], a[96:99], v[32:35]\n"
"v_add_f32 v237, v237, v29\n"
"v_add_f32 v238, v238, v30\n"
"v_mfma_f32_16x16x32_bf16 v[32:35], a[36:39], a[100:103], v[32:35]\n"
"global_load_dwordx4 a[80:83], v0, s[34:35] offset:0 sc1\n"
"v_add_f32 v239, v239, v31\n"
"v_mfma_f32_16x16x32_bf16 v[60:63], a[44:47], a[100:103], v[60:63]\n"
"v_add_f32 v240, v240, v28\n"
"v_add_f32 v241, v241, v29\n"
"v_mfma_f32_16x16x32_bf16 v[60:63], a[40:43], a[96:99], v[60:63]\n"
"v_add_f32 v242, v242, v30\n"
"v_mfma_f32_16x16x32_bf16 v[64:67], a[40:43], a[104:107], v[64:67]\n"
"v_add_f32 v243, v243, v31\n"
"v_add_f32 v244, v244, v28\n"
"v_mfma_f32_16x16x32_bf16 v[64:67], a[44:47], a[108:111], v[64:67]\n"
"v_add_f32 v245, v245, v29\n"
"v_mfma_f32_16x16x32_bf16 v[36:39], a[36:39], a[108:111], v[36:39]\n"
"v_add_f32 v246, v246, v30\n"
"v_mfma_f32_16x16x32_bf16 v[36:39], a[32:35], a[104:107], v[36:39]\n"
"s_waitcnt lgkmcnt(8)\n"
"v_add_f32 v247, v247, v31\n"
"v_mfma_f32_16x16x32_bf16 v[40:43], a[32:35], a[112:115], v[40:43]\n"
"v_add_f32 v248, v248, v28\n"
"v_add_f32 v249, v249, v29\n"
"v_mfma_f32_16x16x32_bf16 v[40:43], a[36:39], a[116:119], v[40:43]\n"
"global_load_dwordx4 a[84:87], v0, s[34:35] offset:1024 sc1\n"
"s_add_u32 s48, s56, %12\n"
"s_addc_u32 s49, s57, 0\n"
"v_mfma_f32_16x16x32_bf16 v[68:71], a[44:47], a[116:119], v[68:71]\n"
"v_add_f32 v250, v250, v30\n"
"v_add_f32 v251, v251, v31\n"
"v_mfma_f32_16x16x32_bf16 v[68:71], a[40:43], a[112:115], v[68:71]\n"
"s_mov_b32 s50, 32768\n"
"v_add_f32 v252, v252, v28\n"
"v_add_f32 v253, v253, v29\n"
"v_mfma_f32_16x16x32_bf16 v[72:75], a[40:43], a[120:123], v[72:75]\n"
"v_add_f32 v254, v254, v30\n"
"v_mfma_f32_16x16x32_bf16 v[72:75], a[44:47], a[124:127], v[72:75]\n"
"s_mov_b32 s65, s78\n"
"v_add_f32 v255, v255, v31\n"
"v_mfma_f32_16x16x32_bf16 v[44:47], a[36:39], a[124:127], v[44:47]\n"
"v_accvgpr_read_b32 v12, a28\n"
"v_accvgpr_read_b32 v13, a29\n"
"v_mfma_f32_16x16x32_bf16 v[44:47], a[32:35], a[120:123], v[44:47]\n"
"s_waitcnt lgkmcnt(4)\n"
"v_accvgpr_read_b32 v14, a30\n"
"v_mfma_f32_16x16x32_bf16 v[48:51], a[32:35], a[128:131], v[48:51]\n"
"v_accvgpr_read_b32 v15, a31\n"
"v_mfma_f32_16x16x32_bf16 v[48:51], a[36:39], a[132:135], v[48:51]\n"
"global_load_dwordx4 a[88:91], v0, s[34:35] offset:2048 sc1\n"
"v_add_f32 v12, v12, v28\n"
"v_mfma_f32_16x16x32_bf16 v[76:79], a[44:47], a[132:135], v[76:79]\n"
"s_waitcnt lgkmcnt(0)\n"
"v_add_f32 v13, v13, v29\n"
"v_add_f32 v14, v14, v30\n"
"v_mfma_f32_16x16x32_bf16 v[76:79], a[40:43], a[128:131], v[76:79]\n"
"s_sub_u32 s29, s76, s29\n"
"v_add_f32 v15, v15, v31\n"
"v_mfma_f32_16x16x32_bf16 v[80:83], a[40:43], a[136:139], v[80:83]\n"
"s_cmp_lt_u32 s29, 620\n"
"s_cselect_b32 s99, s99, 0\n"
"v_accvgpr_write_b32 a28, v12\n"
"v_mfma_f32_16x16x32_bf16 v[80:83], a[44:47], a[140:143], v[80:83]\n"
"s_max_u32 s30, s30, s99\n"
"v_accvgpr_write_b32 a29, v13\n"
"v_accvgpr_write_b32 a30, v14\n"
"v_mfma_f32_16x16x32_bf16 v[52:55], a[36:39], a[140:143], v[52:55]\n"
"s_cmp_eq_u32 s30, 0\n"
"s_cselect_b32 s30, 10, s30\n"
"v_accvgpr_write_b32 a31, v15\n"
"v_mfma_f32_16x16x32_bf16 v[52:55], a[32:35], a[136:139], v[52:55]\n"
"s_cmp_eq_u32 s31, 0\n"
"s_cselect_b32 s31, 10, s31\n"
"v_mfma_f32_16x16x32_bf16 v[56:59], a[32:35], a[144:147], v[56:59]\n"
"s_sub_u32 s69, s31, s30\n"
"v_mfma_f32_16x16x32_bf16 v[56:59], a[36:39], a[148:151], v[56:59]\n"
"global_load_dwordx4 a[92:95], v0, s[34:35] offset:3072 sc1\n"
"v_mov_b32 v20, s69\n"
"v_mfma_f32_16x16x32_bf16 v[84:87], a[44:47], a[148:151], v[84:87]\n"
"ds_write_b32 v2, v20 offset:8192\n"
"v_mfma_f32_16x16x32_bf16 v[84:87], a[40:43], a[144:147], v[84:87]\n"
"v_mfma_f32_16x16x32_bf16 a[4:7], a[40:43], a[152:155], a[4:7]\n"
"s_add_u32 s34, s34, 8192\n"
"v_mfma_f32_16x16x32_bf16 a[4:7], a[44:47], a[156:159], a[4:7]\n"
"v_mfma_f32_16x16x32_bf16 a[0:3], a[36:39], a[156:159], a[0:3]\n"
"v_mfma_f32_16x16x32_bf16 a[0:3], a[32:35], a[152:155], a[0:3]\n"
"s_waitcnt lgkmcnt(0)\n"
"s_barrier\n"
"s_waitcnt vmcnt(8)\n"
"v_mfma_f32_16x16x32_bf16 v[88:91], a[48:51], a[96:99], v[88:91]\n"
"ds_read_b32 v21, v3 offset:8192\n"
"v_mfma_f32_16x16x32_bf16 v[88:91], a[52:55], a[100:103], v[88:91]\n"
"global_load_dwordx4 a[32:35], v0, s[34:35] offset:-4096 sc1\n"
"v_mfma_f32_16x16x32_bf16 v[116:119], a[60:63], a[100:103], v[116:119]\n"
"ds_read_b128 a[100:103], v5 offset:17408\n"
"v_mfma_f32_16x16x32_bf16 v[116:119], a[56:59], a[96:99], v[116:119]\n"
"ds_read_b128 a[96:99], v5 offset:16384\n"
"v_mfma_f32_16x16x32_bf16 v[120:123], a[56:59], a[104:107], v[120:123]\n"
"ds_read_b32 v22, v4 offset:8192\n"
"v_mfma_f32_16x16x32_bf16 v[120:123], a[60:63], a[108:111], v[120:123]\n"
"ds_read_b32 v23, v5 offset:8192\n"
"v_mfma_f32_16x16x32_bf16 v[92:95], a[52:55], a[108:111], v[92:95]\n"
"ds_read_b128 a[108:111], v5 offset:19456\n"
"v_mfma_f32_16x16x32_bf16 v[92:95], a[48:51], a[104:107], v[92:95]\n"
"ds_read_b128 a[104:107], v5 offset:18432\n"
"v_mfma_f32_16x16x32_bf16 v[96:99], a[48:51], a[112:115], v[96:99]\n"
"v_mfma_f32_16x16x32_bf16 v[96:99], a[52:55], a[116:119], v[96:99]\n"
"global_load_dwordx4 a[36:39], v0, s[34:35] offset:-3072 sc1\n"
"v_mfma_f32_16x16x32_bf16 v[124:127], a[60:63], a[116:119], v[124:127]\n"
"s_waitcnt lgkmcnt(2)\n"
"ds_read_b128 a[116:119], v5 offset:21504\n"
"v_add_u32 v20, v20, v21\n"
"v_mfma_f32_16x16x32_bf16 v[124:127], a[56:59], a[112:115], v[124:127]\n"
"ds_read_b128 a[112:115], v5 offset:20480\n"
"v_mfma_f32_16x16x32_bf16 v[128:131], a[56:59], a[120:123], v[128:131]\n"
"v_add3_u32 v20, v20, v22, v23\n"
"v_mfma_f32_16x16x32_bf16 v[128:131], a[60:63], a[124:127], v[128:131]\n"
"v_mfma_f32_16x16x32_bf16 v[100:103], a[52:55], a[124:127], v[100:103]\n"
"ds_read_b128 a[124:127], v5 offset:23552\n"
"v_readfirstlane_b32 s69, v20\n"
"v_mfma_f32_16x16x32_bf16 v[100:103], a[48:51], a[120:123], v[100:103]\n"
"ds_read_b128 a[120:123], v5 offset:22528\n"
"v_mfma_f32_16x16x32_bf16 v[104:107], a[48:51], a[128:131], v[104:107]\n"
"v_mfma_f32_16x16x32_bf16 v[104:107], a[52:55], a[132:135], v[104:107]\n"
"s_cmp_le_i32 s69, -2\n"
"global_load_dwordx4 a[40:43], v0, s[34:35] offset:-2048 sc1\n"
"s_cselect_b32 s66, 2, 1\n"
"v_mfma_f32_16x16x32_bf16 v[132:135], a[60:63], a[132:135], v[132:135]\n"
"ds_read_b128 a[132:135], v5 offset:25600\n"
"v_mfma_f32_16x16x32_bf16 v[132:135], a[56:59], a[128:131], v[132:135]\n"
"s_cmp_ge_i32 s69, 2\n"
"ds_read_b128 a[128:131], v5 offset:24576\n"
"s_cselect_b32 s66, 3, s66\n"
"v_mfma_f32_16x16x32_bf16 v[136:139], a[56:59], a[136:139], v[136:139]\n"
"ds_read_b128 v[16:19], v7 offset:32768\n"
"v_mfma_f32_16x16x32_bf16 v[136:139], a[60:63], a[140:143], v[136:139]\n"
"s_lshl_b32 s73, s66, 13\n"
"s_xor_b32 s60, s79, s66\n"
"v_mfma_f32_16x16x32_bf16 v[108:111], a[52:55], a[140:143], v[108:111]\n"
"ds_read_b128 a[140:143], v5 offset:27648\n"
"v_mfma_f32_16x16x32_bf16 v[108:111], a[48:51], a[136:139], v[108:111]\n"
"s_cmp_eq_u32 s66, 3\n"
"ds_read_b128 a[136:139], v5 offset:26624\n"
"s_cselect_b32 s72, 8192, 0\n"
"v_mfma_f32_16x16x32_bf16 v[112:115], a[48:51], a[144:147], v[112:115]\n"
"s_abs_i32 s30, s69\n"
"v_mfma_f32_16x16x32_bf16 v[112:115], a[52:55], a[148:151], v[112:115]\n"
"global_load_dwordx4 a[44:47], v0, s[34:35] offset:-1024 sc1\n"
"s_add_u32 s52, s56, %11\n"
"s_addc_u32 s53, s57, 0\n"
"v_mfma_f32_16x16x32_bf16 v[140:143], a[60:63], a[148:151], v[140:143]\n"
"s_cmp_lt_u32 s30, 26\n"
"s_cselect_b32 s30, 2, 0\n"
"ds_read_b128 a[148:151], v5 offset:29696\n"
"v_mfma_f32_16x16x32_bf16 v[140:143], a[56:59], a[144:147], v[140:143]\n"
"s_add_u32 s52, s52, 4096\n"
"ds_read_b128 a[144:147], v5 offset:28672\n"
"s_addc_u32 s53, s53, 0\n"
"v_mfma_f32_16x16x32_bf16 a[12:15], a[56:59], a[152:155], a[12:15]\n"
"v_mfma_f32_16x16x32_bf16 a[12:15], a[60:63], a[156:159], a[12:15]\n"
"s_add_u32 s54, s52, s73\n"
"s_addc_u32 s55, s53, 0\n"
"v_mfma_f32_16x16x32_bf16 a[8:11], a[52:55], a[156:159], a[8:11]\n"
"ds_read_b128 a[156:159], v5 offset:31744\n"
"v_mfma_f32_16x16x32_bf16 a[8:11], a[48:51], a[152:155], a[8:11]\n"
"s_add_u32 s52, s52, s72\n"
"ds_read_b128 a[152:155], v5 offset:30720\n"
"s_addc_u32 s53, s53, 0\n"
"L_P6:\n"
"s_waitcnt vmcnt(8) lgkmcnt(13)\n"
"v_mfma_f32_16x16x32_bf16 v[32:35], a[64:67], a[96:99], v[32:35]\n"
"v_mfma_f32_16x16x32_bf16 v[32:35], a[68:71], a[100:103], v[32:35]\n"
"global_load_dwordx4 a[48:51], v0, s[34:35] offset:0 sc1\n"
"v_mfma_f32_16x16x32_bf16 v[60:63], a[76:79], a[100:103], v[60:63]\n"
"v_mfma_f32_16x16x32_bf16 v[60:63], a[72:75], a[96:99], v[60:63]\n"
"v_mfma_f32_16x16x32_bf16 v[64:67], a[72:75], a[104:107], v[64:67]\n"
"v_mfma_f32_16x16x32_bf16 v[64:67], a[76:79], a[108:111], v[64:67]\n"
"v_mfma_f32_16x16x32_bf16 v[36:39], a[68:71], a[108:111], v[36:39]\n"
"s_waitcnt lgkmcnt(6)\n"
"v_lshlrev_b32 v12, 16, v16\n"
"v_mfma_f32_16x16x32_bf16 v[36:39], a[64:67], a[104:107], v[36:39]\n"
"v_and_b32 v13, s63, v16\n"
"v_lshlrev_b32 v14, 16, v17\n"
"v_mfma_f32_16x16x32_bf16 v[40:43], a[64:67], a[112:115], v[40:43]\n"
"v_and_b32 v15, s63, v17\n"
"v_mfma_f32_16x16x32_bf16 v[40:43], a[68:71], a[116:119], v[40:43]\n"
"global_load_dwordx4 a[52:55], v0, s[34:35] offset:1024 sc1\n"
"ds_read_b128 v[20:23], v7 offset:34816\n"
"v_mfma_f32_16x16x32_bf16 v[68:71], a[76:79], a[116:119], v[68:71]\n"
"v_mul_f32 v12, v12, v32\n"
"v_mul_f32 v13, v13, v33\n"
"v_mfma_f32_16x16x32_bf16 v[68:71], a[72:75], a[112:115], v[68:71]\n"
"v_mul_f32 v14, v14, v34\n"
"v_mfma_f32_16x16x32_bf16 v[72:75], a[72:75], a[120:123], v[72:75]\n"
"v_mul_f32 v15, v15, v35\n"
"v_mfma_f32_16x16x32_bf16 v[72:75], a[76:79], a[124:127], v[72:75]\n"
"v_cvt_pk_bf16_f32 v24, v12, v13\n"
"v_cvt_pk_bf16_f32 v25, v14, v15\n"
"v_mfma_f32_16x16x32_bf16 v[44:47], a[68:71], a[124:127], v[44:47]\n"
"ds_write_b64 v2, v[24:25] offset:0\n"
"v_mfma_f32_16x16x32_bf16 v[44:47], a[64:67], a[120:123], v[44:47]\n"
"s_waitcnt lgkmcnt(6)\n"
"v_lshlrev_b32 v12, 16, v18\n"
"v_mfma_f32_16x16x32_bf16 v[48:51], a[64:67], a[128:131], v[48:51]\n"
"v_and_b32 v13, s63, v18\n"
"v_lshlrev_b32 v14, 16, v19\n"
"v_mfma_f32_16x16x32_bf16 v[48:51], a[68:71], a[132:135], v[48:51]\n"
"global_load_dwordx4 a[56:59], v0, s[34:35] offset:2048 sc1\n"
"v_and_b32 v15, s63, v19\n"
"v_mfma_f32_16x16x32_bf16 v[76:79], a[76:79], a[132:135], v[76:79]\n"
"v_mul_f32 v12, v12, v60\n"
"v_mfma_f32_16x16x32_bf16 v[76:79], a[72:75], a[128:131], v[76:79]\n"
"v_mul_f32 v13, v13, v61\n"
"v_mul_f32 v14, v14, v62\n"
"v_mfma_f32_16x16x32_bf16 v[80:83], a[72:75], a[136:139], v[80:83]\n"
"v_mul_f32 v15, v15, v63\n"
"v_mfma_f32_16x16x32_bf16 v[80:83], a[76:79], a[140:143], v[80:83]\n"
"v_cvt_pk_bf16_f32 v26, v12, v13\n"
"v_cvt_pk_bf16_f32 v27, v14, v15\n"
"v_mfma_f32_16x16x32_bf16 v[52:55], a[68:71], a[140:143], v[52:55]\n"
"ds_write_b64 v2, v[26:27] offset:8\n"
"v_mfma_f32_16x16x32_bf16 v[52:55], a[64:67], a[136:139], v[52:55]\n"
"s_waitcnt lgkmcnt(2)\n"
"v_lshlrev_b32 v12, 16, v20\n"
"v_mfma_f32_16x16x32_bf16 v[56:59], a[64:67], a[144:147], v[56:59]\n"
"v_and_b32 v13, s63, v20\n"
"v_mfma_f32_16x16x32_bf16 v[56:59], a[68:71], a[148:151], v[56:59]\n"
"global_load_dwordx4 a[60:63], v0, s[34:35] offset:3072 sc1\n"
"v_lshlrev_b32 v14, 16, v21\n"
"v_mfma_f32_16x16x32_bf16 v[84:87], a[76:79], a[148:151], v[84:87]\n"
"v_and_b32 v15, s63, v21\n"
"v_xor_b32 v16, 64, v7\n"
"v_mfma_f32_16x16x32_bf16 v[84:87], a[72:75], a[144:147], v[84:87]\n"
"ds_read_b128 v[16:19], v16 offset:32768\n"
"v_mfma_f32_16x16x32_bf16 a[4:7], a[72:75], a[152:155], a[4:7]\n"
"s_add_u32 s34, s34, 8192\n"
"v_mul_f32 v12, v12, v36\n"
"v_mul_f32 v13, v13, v37\n"
"v_mfma_f32_16x16x32_bf16 a[4:7], a[76:79], a[156:159], a[4:7]\n"
"v_mul_f32 v14, v14, v38\n"
"v_mfma_f32_16x16x32_bf16 a[0:3], a[68:71], a[156:159], a[0:3]\n"
"v_mul_f32 v15, v15, v39\n"
"v_mfma_f32_16x16x32_bf16 a[0:3], a[64:67], a[152:155], a[0:3]\n"
"s_waitcnt vmcnt(8)\n"
"v_cvt_pk_bf16_f32 v28, v12, v13\n"
"v_cvt_pk_bf16_f32 v29, v14, v15\n"
"v_mfma_f32_16x16x32_bf16 v[88:91], a[80:83], a[96:99], v[88:91]\n"
"v_lshlrev_b32 v12, 16, v22\n"
"v_mfma_f32_16x16x32_bf16 v[88:91], a[84:87], a[100:103], v[88:91]\n"
"global_load_dwordx4 a[64:67], v0, s[34:35] offset:-4096 sc1\n"
"v_and_b32 v13, s63, v22\n"
"v_mfma_f32_16x16x32_bf16 v[116:119], a[92:95], a[100:103], v[116:119]\n"
"ds_read_b128 a[100:103], v2 offset:17408\n"
"v_lshlrev_b32 v14, 16, v23\n"
"v_mfma_f32_16x16x32_bf16 v[116:119], a[88:91], a[96:99], v[116:119]\n"
"ds_read_b128 a[96:99], v2 offset:16384\n"
"v_and_b32 v15, s63, v23\n"
"v_mfma_f32_16x16x32_bf16 v[120:123], a[88:91], a[104:107], v[120:123]\n"
"v_mul_f32 v12, v12, v64\n"
"v_mul_f32 v13, v13, v65\n"
"v_mfma_f32_16x16x32_bf16 v[120:123], a[92:95], a[108:111], v[120:123]\n"
"v_mul_f32 v14, v14, v66\n"
"v_mul_f32 v15, v15, v67\n"
"v_mfma_f32_16x16x32_bf16 v[92:95], a[84:87], a[108:111], v[92:95]\n"
"ds_read_b128 a[108:111], v2 offset:19456\n"
"v_cvt_pk_bf16_f32 v30, v12, v13\n"
"v_mfma_f32_16x16x32_bf16 v[92:95], a[80:83], a[104:107], v[92:95]\n"
"ds_read_b128 a[104:107], v2 offset:18432\n"
"v_cvt_pk_bf16_f32 v31, v14, v15\n"
"v_mfma_f32_16x16x32_bf16 v[96:99], a[80:83], a[112:115], v[96:99]\n"
"global_store_dwordx4 v0, v[28:31], s[52:53] offset:-2048 sc1\n"
"ds_write_b128 v2, v[28:31] offset:2048\n"
"v_mfma_f32_16x16x32_bf16 v[96:99], a[84:87], a[116:119], v[96:99]\n"
"s_waitcnt lgkmcnt(5)\n"
"global_load_dwordx4 a[68:71], v0, s[34:35] offset:-3072 sc1\n"
"v_lshlrev_b32 v12, 16, v16\n"
"v_mfma_f32_16x16x32_bf16 v[124:127], a[92:95], a[116:119], v[124:127]\n"
"ds_read_b128 a[116:119], v2 offset:21504\n"
"v_and_b32 v13, s63, v16\n"
"v_mfma_f32_16x16x32_bf16 v[124:127], a[88:91], a[112:115], v[124:127]\n"
"ds_read_b128 a[112:115], v2 offset:20480\n"
"v_lshlrev_b32 v14, 16, v17\n"
"v_mfma_f32_16x16x32_bf16 v[128:131], a[88:91], a[120:123], v[128:131]\n"
"v_and_b32 v15, s63, v17\n"
"v_xor_b32 v20, 64, v7\n"
"v_mfma_f32_16x16x32_bf16 v[128:131], a[92:95], a[124:127], v[128:131]\n"
"ds_read_b128 v[20:23], v20 offset:34816\n"
"v_mul_f32 v12, v12, v88\n"
"v_mfma_f32_16x16x32_bf16 v[100:103], a[84:87], a[124:127], v[100:103]\n"
"ds_read_b128 a[124:127], v2 offset:23552\n"
"v_mul_f32 v13, v13, v89\n"
"v_mfma_f32_16x16x32_bf16 v[100:103], a[80:83], a[120:123], v[100:103]\n"
"ds_read_b128 a[120:123], v2 offset:22528\n"
"v_mul_f32 v14, v14, v90\n"
"v_mfma_f32_16x16x32_bf16 v[104:107], a[80:83], a[128:131], v[104:107]\n"
"v_mul_f32 v15, v15, v91\n"
"v_cvt_pk_bf16_f32 v24, v12, v13\n"
"v_mfma_f32_16x16x32_bf16 v[104:107], a[84:87], a[132:135], v[104:107]\n"
"global_load_dwordx4 a[72:75], v0, s[34:35] offset:-2048 sc1\n"
"v_cvt_pk_bf16_f32 v25, v14, v15\n"
"v_mfma_f32_16x16x32_bf16 v[132:135], a[92:95], a[132:135], v[132:135]\n"
"ds_read_b128 a[132:135], v2 offset:25600\n"
"v_lshlrev_b32 v12, 16, v18\n"
"v_mfma_f32_16x16x32_bf16 v[132:135], a[88:91], a[128:131], v[132:135]\n"
"ds_read_b128 a[128:131], v2 offset:24576\n"
"v_and_b32 v13, s63, v18\n"
"v_mfma_f32_16x16x32_bf16 v[136:139], a[88:91], a[136:139], v[136:139]\n"
"v_lshlrev_b32 v14, 16, v19\n"
"v_and_b32 v15, s63, v19\n"
"v_mfma_f32_16x16x32_bf16 v[136:139], a[92:95], a[140:143], v[136:139]\n"
"v_mul_f32 v12, v12, v116\n"
"v_mfma_f32_16x16x32_bf16 v[108:111], a[84:87], a[140:143], v[108:111]\n"
"ds_read_b128 a[140:143], v2 offset:27648\n"
"v_mul_f32 v13, v13, v117\n"
"v_mfma_f32_16x16x32_bf16 v[108:111], a[80:83], a[136:139], v[108:111]\n"
"ds_read_b128 a[136:139], v2 offset:26624\n"
"v_mul_f32 v14, v14, v118\n"
"v_mfma_f32_16x16x32_bf16 v[112:115], a[80:83], a[144:147], v[112:115]\n"
"v_mul_f32 v15, v15, v119\n"
"v_cvt_pk_bf16_f32 v26, v12, v13\n"
"v_mfma_f32_16x16x32_bf16 v[112:115], a[84:87], a[148:151], v[112:115]\n"
"global_load_dwordx4 a[76:79], v0, s[34:35] offset:-1024 sc1\n"
"v_cvt_pk_bf16_f32 v27, v14, v15\n"
"v_mfma_f32_16x16x32_bf16 v[140:143], a[92:95], a[148:151], v[140:143]\n"
"ds_read_b128 a[148:151], v2 offset:29696\n"
"v_mfma_f32_16x16x32_bf16 v[140:143], a[88:91], a[144:147], v[140:143]\n"
"ds_read_b128 a[144:147], v2 offset:28672\n"
"v_mfma_f32_16x16x32_bf16 a[12:15], a[88:91], a[152:155], a[12:15]\n"
"global_store_dwordx4 v0, v[24:27], s[52:53] offset:-3072 sc1\n"
"ds_write_b128 v2, v[24:27] offset:1024\n"
"v_mfma_f32_16x16x32_bf16 a[12:15], a[92:95], a[156:159], a[12:15]\n"
"s_waitcnt lgkmcnt(9)\n"
"v_lshlrev_b32 v12, 16, v20\n"
"v_and_b32 v13, s63, v20\n"
"v_mfma_f32_16x16x32_bf16 a[8:11], a[84:87], a[156:159], a[8:11]\n"
"ds_read_b128 a[156:159], v2 offset:31744\n"
"v_lshlrev_b32 v14, 16, v21\n"
"v_mfma_f32_16x16x32_bf16 a[8:11], a[80:83], a[152:155], a[8:11]\n"
"s_waitcnt vmcnt(10)\n"
"ds_read_b128 a[152:155], v2 offset:30720\n"
"v_and_b32 v15, s63, v21\n"
"v_mfma_f32_16x16x32_bf16 v[144:147], a[32:35], a[96:99], v[144:147]\n"
"ds_read_b128 v[16:19], v7 offset:36864\n"
"v_mul_f32 v12, v12, v92\n"
"v_mfma_f32_16x16x32_bf16 v[144:147], a[36:39], a[100:103], v[144:147]\n"
"global_load_dwordx4 a[80:83], v0, s[34:35] offset:0 sc1\n"
"v_mul_f32 v13, v13, v93\n"
"v_mfma_f32_16x16x32_bf16 v[172:175], a[44:47], a[100:103], v[172:175]\n"
"v_mul_f32 v14, v14, v94\n"
"v_mul_f32 v15, v15, v95\n"
"v_mfma_f32_16x16x32_bf16 v[172:175], a[40:43], a[96:99], v[172:175]\n"
"v_cvt_pk_bf16_f32 v28, v12, v13\n"
"v_cvt_pk_bf16_f32 v29, v14, v15\n"
"v_mfma_f32_16x16x32_bf16 v[176:179], a[40:43], a[104:107], v[176:179]\n"
"ds_write_b64 v2, v[28:29] offset:3072\n"
"v_mfma_f32_16x16x32_bf16 v[176:179], a[44:47], a[108:111], v[176:179]\n"
"v_lshlrev_b32 v12, 16, v22\n"
"v_and_b32 v13, s63, v22\n"
"v_mfma_f32_16x16x32_bf16 v[148:151], a[36:39], a[108:111], v[148:151]\n"
"v_lshlrev_b32 v14, 16, v23\n"
"v_mfma_f32_16x16x32_bf16 v[148:151], a[32:35], a[104:107], v[148:151]\n"
"s_waitcnt lgkmcnt(11)\n"
"v_and_b32 v15, s63, v23\n"
"v_mul_f32 v12, v12, v120\n"
"v_mfma_f32_16x16x32_bf16 v[152:155], a[32:35], a[112:115], v[152:155]\n"
"v_mul_f32 v13, v13, v121\n"
"v_mfma_f32_16x16x32_bf16 v[152:155], a[36:39], a[116:119], v[152:155]\n"
"global_load_dwordx4 a[84:87], v0, s[34:35] offset:1024 sc1\n"
"v_mul_f32 v14, v14, v122\n"
"v_mfma_f32_16x16x32_bf16 v[180:183], a[44:47], a[116:119], v[180:183]\n"
"v_mul_f32 v15, v15, v123\n"
"v_cvt_pk_bf16_f32 v30, v12, v13\n"
"v_mfma_f32_16x16x32_bf16 v[180:183], a[40:43], a[112:115], v[180:183]\n"
"v_cvt_pk_bf16_f32 v31, v14, v15\n"
"v_mfma_f32_16x16x32_bf16 v[184:187], a[40:43], a[120:123], v[184:187]\n"
"ds_write_b64 v2, v[30:31] offset:3080\n"
"v_mfma_f32_16x16x32_bf16 v[184:187], a[44:47], a[124:127], v[184:187]\n"
"s_waitcnt lgkmcnt(2)\n"
"v_lshlrev_b32 v12, 16, v16\n"
"v_and_b32 v13, s63, v16\n"
"v_mfma_f32_16x16x32_bf16 v[156:159], a[36:39], a[124:127], v[156:159]\n"
"v_lshlrev_b32 v14, 16, v17\n"
"v_mfma_f32_16x16x32_bf16 v[156:159], a[32:35], a[120:123], v[156:159]\n"
"v_and_b32 v15, s63, v17\n"
"v_xor_b32 v20, 64, v7\n"
"v_mfma_f32_16x16x32_bf16 v[160:163], a[32:35], a[128:131], v[160:163]\n"
"ds_read_b128 v[20:23], v20 offset:36864\n"
"v_mfma_f32_16x16x32_bf16 v[160:163], a[36:39], a[132:135], v[160:163]\n"
"global_load_dwordx4 a[88:91], v0, s[34:35] offset:2048 sc1\n"
"v_mul_f32 v12, v12, v40\n"
"v_mfma_f32_16x16x32_bf16 v[188:191], a[44:47], a[132:135], v[188:191]\n"
"v_mul_f32 v13, v13, v41\n"
"v_mfma_f32_16x16x32_bf16 v[188:191], a[40:43], a[128:131], v[188:191]\n"
"v_mul_f32 v14, v14, v42\n"
"v_mul_f32 v15, v15, v43\n"
"v_mfma_f32_16x16x32_bf16 v[192:195], a[40:43], a[136:139], v[192:195]\n"
"v_cvt_pk_bf16_f32 v24, v12, v13\n"
"v_mfma_f32_16x16x32_bf16 v[192:195], a[44:47], a[140:143], v[192:195]\n"
"v_cvt_pk_bf16_f32 v25, v14, v15\n"
"v_mfma_f32_16x16x32_bf16 v[164:167], a[36:39], a[140:143], v[164:167]\n"
"v_lshlrev_b32 v12, 16, v18\n"
"v_and_b32 v13, s63, v18\n"
"v_mfma_f32_16x16x32_bf16 v[164:167], a[32:35], a[136:139], v[164:167]\n"
"v_lshlrev_b32 v14, 16, v19\n"
"v_mfma_f32_16x16x32_bf16 v[168:171], a[32:35], a[144:147], v[168:171]\n"
"v_and_b32 v15, s63, v19\n"
"v_mul_f32 v12, v12, v68\n"
"v_mfma_f32_16x16x32_bf16 v[168:171], a[36:39], a[148:151], v[168:171]\n"
"global_load_dwordx4 a[92:95], v0, s[34:35] offset:3072 sc1\n"
"v_mul_f32 v13, v13, v69\n"
"v_mfma_f32_16x16x32_bf16 v[196:199], a[44:47], a[148:151], v[196:199]\n"
"v_mul_f32 v14, v14, v70\n"
"v_mfma_f32_16x16x32_bf16 v[196:199], a[40:43], a[144:147], v[196:199]\n"
"v_mul_f32 v15, v15, v71\n"
"v_mfma_f32_16x16x32_bf16 a[20:23], a[40:43], a[152:155], a[20:23]\n"
"s_add_u32 s34, s34, 8192\n"
"v_cvt_pk_bf16_f32 v26, v12, v13\n"
"v_cvt_pk_bf16_f32 v27, v14, v15\n"
"v_mfma_f32_16x16x32_bf16 a[20:23], a[44:47], a[156:159], a[20:23]\n"
"global_store_dwordx4 v0, v[24:27], s[52:53] offset:0 sc1\n"
"ds_write_b128 v2, v[24:27] offset:4096\n"
"v_mfma_f32_16x16x32_bf16 a[16:19], a[36:39], a[156:159], a[16:19]\n"
"s_waitcnt lgkmcnt(1)\n"
"v_lshlrev_b32 v12, 16, v20\n"
"v_mfma_f32_16x16x32_bf16 a[16:19], a[32:35], a[152:155], a[16:19]\n"
"s_waitcnt vmcnt(11)\n"
"v_and_b32 v13, s63, v20\n"
"v_mfma_f32_16x16x32_bf16 v[200:203], a[48:51], a[96:99], v[200:203]\n"
"v_lshlrev_b32 v14, 16, v21\n"
"v_and_b32 v15, s63, v21\n"
"v_mfma_f32_16x16x32_bf16 v[200:203], a[52:55], a[100:103], v[200:203]\n"
"global_load_dwordx4 a[32:35], v0, s[34:35] offset:-4096 sc1\n"
"ds_read_b128 v[16:19], v7 offset:38912\n"
"v_mfma_f32_16x16x32_bf16 v[228:231], a[60:63], a[100:103], v[228:231]\n"
"ds_read_b128 a[100:103], v3 offset:17408\n"
"v_mul_f32 v12, v12, v96\n"
"v_mfma_f32_16x16x32_bf16 v[228:231], a[56:59], a[96:99], v[228:231]\n"
"ds_read_b128 a[96:99], v3 offset:16384\n"
"v_mul_f32 v13, v13, v97\n"
"v_mfma_f32_16x16x32_bf16 v[232:235], a[56:59], a[104:107], v[232:235]\n"
"v_mul_f32 v14, v14, v98\n"
"v_mfma_f32_16x16x32_bf16 v[232:235], a[60:63], a[108:111], v[232:235]\n"
"v_mul_f32 v15, v15, v99\n"
"v_cvt_pk_bf16_f32 v28, v12, v13\n"
"v_mfma_f32_16x16x32_bf16 v[204:207], a[52:55], a[108:111], v[204:207]\n"
"ds_read_b128 a[108:111], v3 offset:19456\n"
"v_cvt_pk_bf16_f32 v29, v14, v15\n"
"v_mfma_f32_16x16x32_bf16 v[204:207], a[48:51], a[104:107], v[204:207]\n"
"ds_read_b128 a[104:107], v3 offset:18432\n"
"v_lshlrev_b32 v12, 16, v22\n"
"v_mfma_f32_16x16x32_bf16 v[208:211], a[48:51], a[112:115], v[208:211]\n"
"v_and_b32 v13, s63, v22\n"
"v_lshlrev_b32 v14, 16, v23\n"
"v_mfma_f32_16x16x32_bf16 v[208:211], a[52:55], a[116:119], v[208:211]\n"
"global_load_dwordx4 a[36:39], v0, s[34:35] offset:-3072 sc1\n"
"v_and_b32 v15, s63, v23\n"
"v_mfma_f32_16x16x32_bf16 v[236:239], a[60:63], a[116:119], v[236:239]\n"
"ds_read_b128 a[116:119], v3 offset:21504\n"
"v_mul_f32 v12, v12, v124\n"
"v_mfma_f32_16x16x32_bf16 v[236:239], a[56:59], a[112:115], v[236:239]\n"
"ds_read_b128 a[112:115], v3 offset:20480\n"
"v_mul_f32 v13, v13, v125\n"
"v_mfma_f32_16x16x32_bf16 v[240:243], a[56:59], a[120:123], v[240:243]\n"
"v_mul_f32 v14, v14, v126\n"
"v_mul_f32 v15, v15, v127\n"
"v_mfma_f32_16x16x32_bf16 v[240:243], a[60:63], a[124:127], v[240:243]\n"
"v_cvt_pk_bf16_f32 v30, v12, v13\n"
"v_mfma_f32_16x16x32_bf16 v[212:215], a[52:55], a[124:127], v[212:215]\n"
"ds_read_b128 a[124:127], v3 offset:23552\n"
"v_cvt_pk_bf16_f32 v31, v14, v15\n"
"v_mfma_f32_16x16x32_bf16 v[212:215], a[48:51], a[120:123], v[212:215]\n"
"ds_read_b128 a[120:123], v3 offset:22528\n"
"v_mfma_f32_16x16x32_bf16 v[216:219], a[48:51], a[128:131], v[216:219]\n"
"global_store_dwordx4 v0, v[28:31], s[52:53] offset:1024 sc1\n"
"ds_write_b128 v2, v[28:31] offset:5120\n"
"v_mfma_f32_16x16x32_bf16 v[216:219], a[52:55], a[132:135], v[216:219]\n"
"s_waitcnt lgkmcnt(9)\n"
"global_load_dwordx4 a[40:43], v0, s[34:35] offset:-2048 sc1\n"
"v_lshlrev_b32 v12, 16, v16\n"
"v_mfma_f32_16x16x32_bf16 v[244:247], a[60:63], a[132:135], v[244:247]\n"
"ds_read_b128 a[132:135], v3 offset:25600\n"
"v_and_b32 v13, s63, v16\n"
"v_mfma_f32_16x16x32_bf16 v[244:247], a[56:59], a[128:131], v[244:247]\n"
"ds_read_b128 a[128:131], v3 offset:24576\n"
"v_lshlrev_b32 v14, 16, v17\n"
"v_mfma_f32_16x16x32_bf16 v[248:251], a[56:59], a[136:139], v[248:251]\n"
"v_and_b32 v15, s63, v17\n"
"v_xor_b32 v20, 64, v7\n"
"v_mfma_f32_16x16x32_bf16 v[248:251], a[60:63], a[140:143], v[248:251]\n"
"ds_read_b128 v[20:23], v20 offset:38912\n"
"v_mul_f32 v12, v12, v44\n"
"v_mfma_f32_16x16x32_bf16 v[220:223], a[52:55], a[140:143], v[220:223]\n"
"ds_read_b128 a[140:143], v3 offset:27648\n"
"v_mul_f32 v13, v13, v45\n"
"v_mfma_f32_16x16x32_bf16 v[220:223], a[48:51], a[136:139], v[220:223]\n"
"ds_read_b128 a[136:139], v3 offset:26624\n"
"v_mul_f32 v14, v14, v46\n"
"v_mfma_f32_16x16x32_bf16 v[224:227], a[48:51], a[144:147], v[224:227]\n"
"v_mul_f32 v15, v15, v47\n"
"v_cvt_pk_bf16_f32 v24, v12, v13\n"
"v_mfma_f32_16x16x32_bf16 v[224:227], a[52:55], a[148:151], v[224:227]\n"
"global_load_dwordx4 a[44:47], v0, s[34:35] offset:-1024 sc1\n"
"v_cvt_pk_bf16_f32 v25, v14, v15\n"
"v_mfma_f32_16x16x32_bf16 v[252:255], a[60:63], a[148:151], v[252:255]\n"
"ds_read_b128 a[148:151], v3 offset:29696\n"
"v_mfma_f32_16x16x32_bf16 v[252:255], a[56:59], a[144:147], v[252:255]\n"
"ds_read_b128 a[144:147], v3 offset:28672\n"
"v_mfma_f32_16x16x32_bf16 a[28:31], a[56:59], a[152:155], a[28:31]\n"
"ds_write_b64 v2, v[24:25] offset:6144\n"
"v_mfma_f32_16x16x32_bf16 a[28:31], a[60:63], a[156:159], a[28:31]\n"
"v_lshlrev_b32 v12, 16, v18\n"
"v_and_b32 v13, s63, v18\n"
"v_mfma_f32_16x16x32_bf16 a[24:27], a[52:55], a[156:159], a[24:27]\n"
"ds_read_b128 a[156:159], v3 offset:31744\n"
"v_lshlrev_b32 v14, 16, v19\n"
"v_mfma_f32_16x16x32_bf16 a[24:27], a[48:51], a[152:155], a[24:27]\n"
"s_waitcnt vmcnt(11) lgkmcnt(14)\n"
"ds_read_b128 a[152:155], v3 offset:30720\n"
"v_and_b32 v15, s63, v19\n"
"v_mfma_f32_16x16x32_bf16 v[144:147], a[64:67], a[96:99], v[144:147]\n"
"v_mul_f32 v12, v12, v72\n"
"v_mul_f32 v13, v13, v73\n"
"v_mfma_f32_16x16x32_bf16 v[144:147], a[68:71], a[100:103], v[144:147]\n"
"global_load_dwordx4 a[48:51], v0, s[34:35] offset:0 sc1\n"
"v_mul_f32 v14, v14, v74\n"
"v_mfma_f32_16x16x32_bf16 v[172:175], a[76:79], a[100:103], v[172:175]\n"
"v_mul_f32 v15, v15, v75\n"
"v_cvt_pk_bf16_f32 v26, v12, v13\n"
"v_mfma_f32_16x16x32_bf16 v[172:175], a[72:75], a[96:99], v[172:175]\n"
"v_cvt_pk_bf16_f32 v27, v14, v15\n"
"v_mfma_f32_16x16x32_bf16 v[176:179], a[72:75], a[104:107], v[176:179]\n"
"ds_write_b64 v2, v[26:27] offset:6152\n"
"v_mfma_f32_16x16x32_bf16 v[176:179], a[76:79], a[108:111], v[176:179]\n"
"s_waitcnt lgkmcnt(8)\n"
"v_lshlrev_b32 v12, 16, v20\n"
"v_and_b32 v13, s63, v20\n"
"v_mfma_f32_16x16x32_bf16 v[148:151], a[68:71], a[108:111], v[148:151]\n"
"v_lshlrev_b32 v14, 16, v21\n"
"v_and_b32 v15, s63, v21\n"
"v_mfma_f32_16x16x32_bf16 v[148:151], a[64:67], a[104:107], v[148:151]\n"
"v_mul_f32 v12, v12, v100\n"
"v_mul_f32 v13, v13, v101\n"
"v_mfma_f32_16x16x32_bf16 v[152:155], a[64:67], a[112:115], v[152:155]\n"
"v_mul_f32 v14, v14, v102\n"
"v_mul_f32 v15, v15, v103\n"
"v_mfma_f32_16x16x32_bf16 v[152:155], a[68:71], a[116:119], v[152:155]\n"
"global_load_dwordx4 a[52:55], v0, s[34:35] offset:1024 sc1\n"
"v_cvt_pk_bf16_f32 v28, v12, v13\n"
"v_mfma_f32_16x16x32_bf16 v[180:183], a[76:79], a[116:119], v[180:183]\n"
"v_cvt_pk_bf16_f32 v29, v14, v15\n"
"v_lshlrev_b32 v12, 16, v22\n"
"v_mfma_f32_16x16x32_bf16 v[180:183], a[72:75], a[112:115], v[180:183]\n"
"v_and_b32 v13, s63, v22\n"
"v_lshlrev_b32 v14, 16, v23\n"
"v_mfma_f32_16x16x32_bf16 v[184:187], a[72:75], a[120:123], v[184:187]\n"
"v_and_b32 v15, s63, v23\n"
"v_mul_f32 v12, v12, v128\n"
"v_mfma_f32_16x16x32_bf16 v[184:187], a[76:79], a[124:127], v[184:187]\n"
"v_mul_f32 v13, v13, v129\n"
"v_mfma_f32_16x16x32_bf16 v[156:159], a[68:71], a[124:127], v[156:159]\n"
"v_mul_f32 v14, v14, v130\n"
"v_mfma_f32_16x16x32_bf16 v[156:159], a[64:67], a[120:123], v[156:159]\n"
"s_waitcnt lgkmcnt(6)\n"
"v_mul_f32 v15, v15, v131\n"
"v_cvt_pk_bf16_f32 v30, v12, v13\n"
"v_mfma_f32_16x16x32_bf16 v[160:163], a[64:67], a[128:131], v[160:163]\n"
"v_cvt_pk_bf16_f32 v31, v14, v15\n"
"v_mfma_f32_16x16x32_bf16 v[160:163], a[68:71], a[132:135], v[160:163]\n"
"global_load_dwordx4 a[56:59], v0, s[34:35] offset:2048 sc1\n"
"v_mfma_f32_16x16x32_bf16 v[188:191], a[76:79], a[132:135], v[188:191]\n"
"global_store_dwordx4 v0, v[28:31], s[52:53] offset:3072 sc1\n"
"ds_write_b128 v2, v[28:31] offset:7168\n"
"v_mfma_f32_16x16x32_bf16 v[188:191], a[72:75], a[128:131], v[188:191]\n"
"v_accvgpr_read_b32 v16, a160\n"
"v_accvgpr_read_b32 v17, a161\n"
"v_mfma_f32_16x16x32_bf16 v[192:195], a[72:75], a[136:139], v[192:195]\n"
"v_accvgpr_read_b32 v18, a162\n"
"v_mfma_f32_16x16x32_bf16 v[192:195], a[76:79], a[140:143], v[192:195]\n"
"v_accvgpr_read_b32 v19, a163\n"
"v_lshlrev_b32 v12, 16, v16\n"
"v_mfma_f32_16x16x32_bf16 v[164:167], a[68:71], a[140:143], v[164:167]\n"
"v_and_b32 v13, s63, v16\n"
"v_mfma_f32_16x16x32_bf16 v[164:167], a[64:67], a[136:139], v[164:167]\n"
"s_waitcnt lgkmcnt(2)\n"
"v_lshlrev_b32 v14, 16, v17\n"
"v_mfma_f32_16x16x32_bf16 v[168:171], a[64:67], a[144:147], v[168:171]\n"
"v_and_b32 v15, s63, v17\n"
"v_mul_f32 v12, v12, v48\n"
"v_mfma_f32_16x16x32_bf16 v[168:171], a[68:71], a[148:151], v[168:171]\n"
"L_st4488:\n"
"s_cbranch_execz L_retA\n"
"global_load_dwordx4 a[60:63], v0, s[34:35] offset:3072 sc1\n"
"v_mul_f32 v13, v13, v49\n"
"v_mfma_f32_16x16x32_bf16 v[196:199], a[76:79], a[148:151], v[196:199]\n"
"v_mul_f32 v14, v14, v50\n"
"v_mfma_f32_16x16x32_bf16 v[196:199], a[72:75], a[144:147], v[196:199]\n"
"v_mul_f32 v15, v15, v51\n"
"v_cvt_pk_bf16_f32 v24, v12, v13\n"
"v_mfma_f32_16x16x32_bf16 a[20:23], a[72:75], a[152:155], a[20:23]\n"
"s_add_u32 s34, s34, 8192\n"
"v_cvt_pk_bf16_f32 v25, v14, v15\n"
"v_mfma_f32_16x16x32_bf16 a[20:23], a[76:79], a[156:159], a[20:23]\n"
"v_lshlrev_b32 v12, 16, v18\n"
"v_mfma_f32_16x16x32_bf16 a[16:19], a[68:71], a[156:159], a[16:19]\n"
"v_and_b32 v13, s63, v18\n"
"v_lshlrev_b32 v14, 16, v19\n"
"v_mfma_f32_16x16x32_bf16 a[16:19], a[64:67], a[152:155], a[16:19]\n"
"s_waitcnt vmcnt(11)\n"
"v_and_b32 v15, s63, v19\n"
"v_mul_f32 v12, v12, v76\n"
"v_mfma_f32_16x16x32_bf16 v[200:203], a[80:83], a[96:99], v[200:203]\n"
"v_mul_f32 v13, v13, v77\n"
"v_mul_f32 v14, v14, v78\n"
"v_mfma_f32_16x16x32_bf16 v[200:203], a[84:87], a[100:103], v[200:203]\n"
"global_load_dwordx4 a[64:67], v0, s[34:35] offset:-4096 sc1\n"
"v_mul_f32 v15, v15, v79\n"
"v_mfma_f32_16x16x32_bf16 v[228:231], a[92:95], a[100:103], v[228:231]\n"
"ds_read_b128 a[100:103], v4 offset:17408\n"
"v_cvt_pk_bf16_f32 v26, v12, v13\n"
"v_mfma_f32_16x16x32_bf16 v[228:231], a[88:91], a[96:99], v[228:231]\n"
"ds_read_b128 a[96:99], v4 offset:16384\n"
"v_cvt_pk_bf16_f32 v27, v14, v15\n"
"v_mfma_f32_16x16x32_bf16 v[232:235], a[88:91], a[104:107], v[232:235]\n"
"global_store_dwordx4 v0, v[24:27], s[54:55] offset:-4096 sc1\n"
"ds_write_b128 v2, v[24:27] offset:8192\n"
"v_mfma_f32_16x16x32_bf16 v[232:235], a[92:95], a[108:111], v[232:235]\n"
"v_accvgpr_read_b32 v16, a164\n"
"v_mfma_f32_16x16x32_bf16 v[204:207], a[84:87], a[108:111], v[204:207]\n"
"ds_read_b128 a[108:111], v4 offset:19456\n"
"v_accvgpr_read_b32 v17, a165\n"
"v_mfma_f32_16x16x32_bf16 v[204:207], a[80:83], a[104:107], v[204:207]\n"
"ds_read_b128 a[104:107], v4 offset:18432\n"
"v_accvgpr_read_b32 v18, a166\n"
"v_mfma_f32_16x16x32_bf16 v[208:211], a[80:83], a[112:115], v[208:211]\n"
"v_accvgpr_read_b32 v19, a167\n"
"v_lshlrev_b32 v12, 16, v16\n"
"v_mfma_f32_16x16x32_bf16 v[208:211], a[84:87], a[116:119], v[208:211]\n"
"global_load_dwordx4 a[68:71], v0, s[34:35] offset:-3072 sc1\n"
"v_and_b32 v13, s63, v16\n"
"v_mfma_f32_16x16x32_bf16 v[236:239], a[92:95], a[116:119], v[236:239]\n"
"ds_read_b128 a[116:119], v4 offset:21504\n"
"v_lshlrev_b32 v14, 16, v17\n"
"v_mfma_f32_16x16x32_bf16 v[236:239], a[88:91], a[112:115], v[236:239]\n"
"ds_read_b128 a[112:115], v4 offset:20480\n"
"v_and_b32 v15, s63, v17\n"
"v_mfma_f32_16x16x32_bf16 v[240:243], a[88:91], a[120:123], v[240:243]\n"
"v_mul_f32 v12, v12, v104\n"
"v_mul_f32 v13, v13, v105\n"
"v_mfma_f32_16x16x32_bf16 v[240:243], a[92:95], a[124:127], v[240:243]\n"
"v_mul_f32 v14, v14, v106\n"
"v_mul_f32 v15, v15, v107\n"
"v_mfma_f32_16x16x32_bf16 v[212:215], a[84:87], a[124:127], v[212:215]\n"
"ds_read_b128 a[124:127], v4 offset:23552\n"
"v_cvt_pk_bf16_f32 v28, v12, v13\n"
"v_mfma_f32_16x16x32_bf16 v[212:215], a[80:83], a[120:123], v[212:215]\n"
"ds_read_b128 a[120:123], v4 offset:22528\n"
"v_cvt_pk_bf16_f32 v29, v14, v15\n"
"v_mfma_f32_16x16x32_bf16 v[216:219], a[80:83], a[128:131], v[216:219]\n"
"ds_write_b64 v2, v[28:29] offset:9216\n"
"v_mfma_f32_16x16x32_bf16 v[216:219], a[84:87], a[132:135], v[216:219]\n"
"global_load_dwordx4 a[72:75], v0, s[34:35] offset:-2048 sc1\n"
"v_lshlrev_b32 v12, 16, v18\n"
"v_mfma_f32_16x16x32_bf16 v[244:247], a[92:95], a[132:135], v[244:247]\n"
"ds_read_b128 a[132:135], v4 offset:25600\n"
"v_and_b32 v13, s63, v18\n"
"v_mfma_f32_16x16x32_bf16 v[244:247], a[88:91], a[128:131], v[244:247]\n"
"ds_read_b128 a[128:131], v4 offset:24576\n"
"v_lshlrev_b32 v14, 16, v19\n"
"v_mfma_f32_16x16x32_bf16 v[248:251], a[88:91], a[136:139], v[248:251]\n"
"v_and_b32 v15, s63, v19\n"
"v_mul_f32 v12, v12, v132\n"
"v_mfma_f32_16x16x32_bf16 v[248:251], a[92:95], a[140:143], v[248:251]\n"
"v_mul_f32 v13, v13, v133\n"
"v_mfma_f32_16x16x32_bf16 v[220:223], a[84:87], a[140:143], v[220:223]\n"
"ds_read_b128 a[140:143], v4 offset:27648\n"
"v_mul_f32 v14, v14, v134\n"
"v_mfma_f32_16x16x32_bf16 v[220:223], a[80:83], a[136:139], v[220:223]\n"
"ds_read_b128 a[136:139], v4 offset:26624\n"
"v_mul_f32 v15, v15, v135\n"
"v_mfma_f32_16x16x32_bf16 v[224:227], a[80:83], a[144:147], v[224:227]\n"
"v_cvt_pk_bf16_f32 v30, v12, v13\n"
"v_cvt_pk_bf16_f32 v31, v14, v15\n"
"v_mfma_f32_16x16x32_bf16 v[224:227], a[84:87], a[148:151], v[224:227]\n"
"global_load_dwordx4 a[76:79], v0, s[34:35] offset:-1024 sc1\n"
"v_mfma_f32_16x16x32_bf16 v[252:255], a[92:95], a[148:151], v[252:255]\n"
"ds_read_b128 a[148:151], v4 offset:29696\n"
"v_mfma_f32_16x16x32_bf16 v[252:255], a[88:91], a[144:147], v[252:255]\n"
"ds_read_b128 a[144:147], v4 offset:28672\n"
"v_mfma_f32_16x16x32_bf16 a[28:31], a[88:91], a[152:155], a[28:31]\n"
"ds_write_b64 v2, v[30:31] offset:9224\n"
"v_mfma_f32_16x16x32_bf16 a[28:31], a[92:95], a[156:159], a[28:31]\n"
"v_accvgpr_read_b32 v16, a168\n"
"v_accvgpr_read_b32 v17, a169\n"
"v_mfma_f32_16x16x32_bf16 a[24:27], a[84:87], a[156:159], a[24:27]\n"
"ds_read_b128 a[156:159], v4 offset:31744\n"
"v_accvgpr_read_b32 v18, a170\n"
"v_mfma_f32_16x16x32_bf16 a[24:27], a[80:83], a[152:155], a[24:27]\n"
"s_waitcnt vmcnt(10) lgkmcnt(13)\n"
"ds_read_b128 a[152:155], v4 offset:30720\n"
"v_accvgpr_read_b32 v19, a171\n"
"v_mfma_f32_16x16x32_bf16 v[144:147], a[32:35], a[96:99], v[144:147]\n"
"v_lshlrev_b32 v12, 16, v16\n"
"v_and_b32 v13, s63, v16\n"
"v_mfma_f32_16x16x32_bf16 v[144:147], a[36:39], a[100:103], v[144:147]\n"
"global_load_dwordx4 a[80:83], v0, s[34:35] offset:0 sc1\n"
"v_lshlrev_b32 v14, 16, v17\n"
"v_mfma_f32_16x16x32_bf16 v[172:175], a[44:47], a[100:103], v[172:175]\n"
"v_and_b32 v15, s63, v17\n"
"v_mul_f32 v12, v12, v52\n"
"v_mfma_f32_16x16x32_bf16 v[172:175], a[40:43], a[96:99], v[172:175]\n"
"v_mul_f32 v13, v13, v53\n"
"v_mul_f32 v14, v14, v54\n"
"v_mfma_f32_16x16x32_bf16 v[176:179], a[40:43], a[104:107], v[176:179]\n"
"v_mul_f32 v15, v15, v55\n"
"v_cvt_pk_bf16_f32 v24, v12, v13\n"
"v_mfma_f32_16x16x32_bf16 v[176:179], a[44:47], a[108:111], v[176:179]\n"
"v_cvt_pk_bf16_f32 v25, v14, v15\n"
"v_lshlrev_b32 v12, 16, v18\n"
"v_mfma_f32_16x16x32_bf16 v[148:151], a[36:39], a[108:111], v[148:151]\n"
"v_and_b32 v13, s63, v18\n"
"v_lshlrev_b32 v14, 16, v19\n"
"v_mfma_f32_16x16x32_bf16 v[148:151], a[32:35], a[104:107], v[148:151]\n"
"s_waitcnt lgkmcnt(10)\n"
"v_and_b32 v15, s63, v19\n"
"v_mul_f32 v12, v12, v80\n"
"v_mfma_f32_16x16x32_bf16 v[152:155], a[32:35], a[112:115], v[152:155]\n"
"v_mul_f32 v13, v13, v81\n"
"v_mul_f32 v14, v14, v82\n"
"v_mfma_f32_16x16x32_bf16 v[152:155], a[36:39], a[116:119], v[152:155]\n"
"global_load_dwordx4 a[84:87], v0, s[34:35] offset:1024 sc1\n"
"v_mul_f32 v15, v15, v83\n"
"v_mfma_f32_16x16x32_bf16 v[180:183], a[44:47], a[116:119], v[180:183]\n"
"v_cvt_pk_bf16_f32 v26, v12, v13\n"
"v_mfma_f32_16x16x32_bf16 v[180:183], a[40:43], a[112:115], v[180:183]\n"
"v_cvt_pk_bf16_f32 v27, v14, v15\n"
"v_mfma_f32_16x16x32_bf16 v[184:187], a[40:43], a[120:123], v[184:187]\n"
"global_store_dwordx4 v0, v[24:27], s[54:55] offset:-2048 sc1\n"
"ds_write_b128 v2, v[24:27] offset:10240\n"
"v_mfma_f32_16x16x32_bf16 v[184:187], a[44:47], a[124:127], v[184:187]\n"
"v_accvgpr_read_b32 v16, a172\n"
"v_mfma_f32_16x16x32_bf16 v[156:159], a[36:39], a[124:127], v[156:159]\n"
"v_accvgpr_read_b32 v17, a173\n"
"v_mfma_f32_16x16x32_bf16 v[156:159], a[32:35], a[120:123], v[156:159]\n"
"s_waitcnt lgkmcnt(6)\n"
"v_accvgpr_read_b32 v18, a174\n"
"v_accvgpr_read_b32 v19, a175\n"
"v_mfma_f32_16x16x32_bf16 v[160:163], a[32:35], a[128:131], v[160:163]\n"
"v_lshlrev_b32 v12, 16, v16\n"
"v_mfma_f32_16x16x32_bf16 v[160:163], a[36:39], a[132:135], v[160:163]\n"
"global_load_dwordx4 a[88:91], v0, s[34:35] offset:2048 sc1\n"
"v_and_b32 v13, s63, v16\n"
"v_mfma_f32_16x16x32_bf16 v[188:191], a[44:47], a[132:135], v[188:191]\n"
"v_lshlrev_b32 v14, 16, v17\n"
"v_and_b32 v15, s63, v17\n"
"v_mfma_f32_16x16x32_bf16 v[188:191], a[40:43], a[128:131], v[188:191]\n"
"v_mul_f32 v12, v12, v108\n"
"v_mfma_f32_16x16x32_bf16 v[192:195], a[40:43], a[136:139], v[192:195]\n"
"v_mul_f32 v13, v13, v109\n"
"v_mfma_f32_16x16x32_bf16 v[192:195], a[44:47], a[140:143], v[192:195]\n"
"v_mul_f32 v14, v14, v110\n"
"v_mul_f32 v15, v15, v111\n"
"v_mfma_f32_16x16x32_bf16 v[164:167], a[36:39], a[140:143], v[164:167]\n"
"v_cvt_pk_bf16_f32 v28, v12, v13\n"
"v_mfma_f32_16x16x32_bf16 v[164:167], a[32:35], a[136:139], v[164:167]\n"
"s_waitcnt lgkmcnt(1)\n"
"v_cvt_pk_bf16_f32 v29, v14, v15\n"
"v_mfma_f32_16x16x32_bf16 v[168:171], a[32:35], a[144:147], v[168:171]\n"
"v_lshlrev_b32 v12, 16, v18\n"
"v_and_b32 v13, s63, v18\n"
"v_mfma_f32_16x16x32_bf16 v[168:171], a[36:39], a[148:151], v[168:171]\n"
"global_load_dwordx4 a[92:95], v0, s[34:35] offset:3072 sc1\n"
"v_lshlrev_b32 v14, 16, v19\n"
"v_mfma_f32_16x16x32_bf16 v[196:199], a[44:47], a[148:151], v[196:199]\n"
"v_and_b32 v15, s63, v19\n"
"v_mfma_f32_16x16x32_bf16 v[196:199], a[40:43], a[144:147], v[196:199]\n"
"v_mul_f32 v12, v12, v136\n"
"v_mul_f32 v13, v13, v137\n"
"v_mfma_f32_16x16x32_bf16 a[20:23], a[40:43], a[152:155], a[20:23]\n"
"s_add_u32 s34, s34, 8192\n"
"v_mul_f32 v14, v14, v138\n"
"v_mfma_f32_16x16x32_bf16 a[20:23], a[44:47], a[156:159], a[20:23]\n"
"v_mul_f32 v15, v15, v139\n"
"v_mfma_f32_16x16x32_bf16 a[16:19], a[36:39], a[156:159], a[16:19]\n"
"v_cvt_pk_bf16_f32 v30, v12, v13\n"
"v_cvt_pk_bf16_f32 v31, v14, v15\n"
"v_mfma_f32_16x16x32_bf16 a[16:19], a[32:35], a[152:155], a[16:19]\n"
"s_waitcnt vmcnt(10)\n"
"global_store_dwordx4 v0, v[28:31], s[54:55] offset:-1024 sc1\n"
"ds_write_b128 v2, v[28:31] offset:11264\n"
"v_mfma_f32_16x16x32_bf16 v[200:203], a[48:51], a[96:99], v[200:203]\n"
"v_accvgpr_read_b32 v16, a176\n"
"v_mfma_f32_16x16x32_bf16 v[200:203], a[52:55], a[100:103], v[200:203]\n"
"global_load_dwordx4 a[32:35], v0, s[34:35] offset:-4096 sc1\n"
"v_accvgpr_read_b32 v17, a177\n"
"v_mfma_f32_16x16x32_bf16 v[228:231], a[60:63], a[100:103], v[228:231]\n"
"ds_read_b128 a[100:103], v5 offset:17408\n"
"v_accvgpr_read_b32 v18, a178\n"
"v_mfma_f32_16x16x32_bf16 v[228:231], a[56:59], a[96:99], v[228:231]\n"
"ds_read_b128 a[96:99], v5 offset:16384\n"
"v_accvgpr_read_b32 v19, a179\n"
"v_mfma_f32_16x16x32_bf16 v[232:235], a[56:59], a[104:107], v[232:235]\n"
"v_lshlrev_b32 v12, 16, v16\n"
"v_and_b32 v13, s63, v16\n"
"v_mfma_f32_16x16x32_bf16 v[232:235], a[60:63], a[108:111], v[232:235]\n"
"v_lshlrev_b32 v14, 16, v17\n"
"v_mfma_f32_16x16x32_bf16 v[204:207], a[52:55], a[108:111], v[204:207]\n"
"ds_read_b128 a[108:111], v5 offset:19456\n"
"v_and_b32 v15, s63, v17\n"
"v_mfma_f32_16x16x32_bf16 v[204:207], a[48:51], a[104:107], v[204:207]\n"
"ds_read_b128 a[104:107], v5 offset:18432\n"
"v_mul_f32 v12, v12, v56\n"
"v_mfma_f32_16x16x32_bf16 v[208:211], a[48:51], a[112:115], v[208:211]\n"
"v_mul_f32 v13, v13, v57\n"
"v_mul_f32 v14, v14, v58\n"
"v_mfma_f32_16x16x32_bf16 v[208:211], a[52:55], a[116:119], v[208:211]\n"
"global_load_dwordx4 a[36:39], v0, s[34:35] offset:-3072 sc1\n"
"v_mul_f32 v15, v15, v59\n"
"v_mfma_f32_16x16x32_bf16 v[236:239], a[60:63], a[116:119], v[236:239]\n"
"ds_read_b128 a[116:119], v5 offset:21504\n"
"v_cvt_pk_bf16_f32 v24, v12, v13\n"
"v_mfma_f32_16x16x32_bf16 v[236:239], a[56:59], a[112:115], v[236:239]\n"
"ds_read_b128 a[112:115], v5 offset:20480\n"
"v_cvt_pk_bf16_f32 v25, v14, v15\n"
"v_mfma_f32_16x16x32_bf16 v[240:243], a[56:59], a[120:123], v[240:243]\n"
"ds_write_b64 v2, v[24:25] offset:12288\n"
"v_mfma_f32_16x16x32_bf16 v[240:243], a[60:63], a[124:127], v[240:243]\n"
"v_lshlrev_b32 v12, 16, v18\n"
"v_and_b32 v13, s63, v18\n"
"v_mfma_f32_16x16x32_bf16 v[212:215], a[52:55], a[124:127], v[212:215]\n"
"ds_read_b128 a[124:127], v5 offset:23552\n"
"v_lshlrev_b32 v14, 16, v19\n"
"v_mfma_f32_16x16x32_bf16 v[212:215], a[48:51], a[120:123], v[212:215]\n"
"ds_read_b128 a[120:123], v5 offset:22528\n"
"v_and_b32 v15, s63, v19\n"
"v_mfma_f32_16x16x32_bf16 v[216:219], a[48:51], a[128:131], v[216:219]\n"
"v_mul_f32 v12, v12, v84\n"
"v_mul_f32 v13, v13, v85\n"
"v_mfma_f32_16x16x32_bf16 v[216:219], a[52:55], a[132:135], v[216:219]\n"
"global_load_dwordx4 a[40:43], v0, s[34:35] offset:-2048 sc1\n"
"v_mul_f32 v14, v14, v86\n"
"v_mfma_f32_16x16x32_bf16 v[244:247], a[60:63], a[132:135], v[244:247]\n"
"ds_read_b128 a[132:135], v5 offset:25600\n"
"v_mul_f32 v15, v15, v87\n"
"v_mfma_f32_16x16x32_bf16 v[244:247], a[56:59], a[128:131], v[244:247]\n"
"ds_read_b128 a[128:131], v5 offset:24576\n"
"v_cvt_pk_bf16_f32 v26, v12, v13\n"
"v_mfma_f32_16x16x32_bf16 v[248:251], a[56:59], a[136:139], v[248:251]\n"
"v_cvt_pk_bf16_f32 v27, v14, v15\n"
"v_mfma_f32_16x16x32_bf16 v[248:251], a[60:63], a[140:143], v[248:251]\n"
"ds_write_b64 v2, v[26:27] offset:12296\n"
"v_mfma_f32_16x16x32_bf16 v[220:223], a[52:55], a[140:143], v[220:223]\n"
"ds_read_b128 a[140:143], v5 offset:27648\n"
"v_accvgpr_read_b32 v16, a180\n"
"v_mfma_f32_16x16x32_bf16 v[220:223], a[48:51], a[136:139], v[220:223]\n"
"ds_read_b128 a[136:139], v5 offset:26624\n"
"v_accvgpr_read_b32 v17, a181\n"
"v_mfma_f32_16x16x32_bf16 v[224:227], a[48:51], a[144:147], v[224:227]\n"
"v_accvgpr_read_b32 v18, a182\n"
"v_accvgpr_read_b32 v19, a183\n"
"v_mfma_f32_16x16x32_bf16 v[224:227], a[52:55], a[148:151], v[224:227]\n"
"global_load_dwordx4 a[44:47], v0, s[34:35] offset:-1024 sc1\n"
"v_lshlrev_b32 v12, 16, v16\n"
"v_mfma_f32_16x16x32_bf16 v[252:255], a[60:63], a[148:151], v[252:255]\n"
"ds_read_b128 a[148:151], v5 offset:29696\n"
"v_and_b32 v13, s63, v16\n"
"v_mfma_f32_16x16x32_bf16 v[252:255], a[56:59], a[144:147], v[252:255]\n"
"ds_read_b128 a[144:147], v5 offset:28672\n"
"v_lshlrev_b32 v14, 16, v17\n"
"v_mfma_f32_16x16x32_bf16 a[28:31], a[56:59], a[152:155], a[28:31]\n"
"v_and_b32 v15, s63, v17\n"
"v_mul_f32 v12, v12, v112\n"
"v_mfma_f32_16x16x32_bf16 a[28:31], a[60:63], a[156:159], a[28:31]\n"
"v_mul_f32 v13, v13, v113\n"
"v_mul_f32 v14, v14, v114\n"
"v_mfma_f32_16x16x32_bf16 a[24:27], a[52:55], a[156:159], a[24:27]\n"
"ds_read_b128 a[156:159], v5 offset:31744\n"
"v_mul_f32 v15, v15, v115\n"
"v_mfma_f32_16x16x32_bf16 a[24:27], a[48:51], a[152:155], a[24:27]\n"
"s_waitcnt vmcnt(10) lgkmcnt(13)\n"
"ds_read_b128 a[152:155], v5 offset:30720\n"
"v_cvt_pk_bf16_f32 v28, v12, v13\n"
"v_mfma_f32_16x16x32_bf16 v[144:147], a[64:67], a[96:99], v[144:147]\n"
"v_cvt_pk_bf16_f32 v29, v14, v15\n"
"v_lshlrev_b32 v12, 16, v18\n"
"v_mfma_f32_16x16x32_bf16 v[144:147], a[68:71], a[100:103], v[144:147]\n"
"global_load_dwordx4 a[48:51], v0, s[34:35] offset:0 sc1\n"
"v_and_b32 v13, s63, v18\n"
"v_mfma_f32_16x16x32_bf16 v[172:175], a[76:79], a[100:103], v[172:175]\n"
"v_lshlrev_b32 v14, 16, v19\n"
"v_and_b32 v15, s63, v19\n"
"v_mfma_f32_16x16x32_bf16 v[172:175], a[72:75], a[96:99], v[172:175]\n"
"v_mul_f32 v12, v12, v140\n"
"v_mul_f32 v13, v13, v141\n"
"v_mfma_f32_16x16x32_bf16 v[176:179], a[72:75], a[104:107], v[176:179]\n"
"v_mul_f32 v14, v14, v142\n"
"v_mul_f32 v15, v15, v143\n"
"v_mfma_f32_16x16x32_bf16 v[176:179], a[76:79], a[108:111], v[176:179]\n"
"v_cvt_pk_bf16_f32 v30, v12, v13\n"
"v_mfma_f32_16x16x32_bf16 v[148:151], a[68:71], a[108:111], v[148:151]\n"
"v_cvt_pk_bf16_f32 v31, v14, v15\n"
"v_mfma_f32_16x16x32_bf16 v[148:151], a[64:67], a[104:107], v[148:151]\n"
"s_waitcnt lgkmcnt(9)\n"
"global_store_dwordx4 v0, v[28:31], s[54:55] offset:1024 sc1\n"
"ds_write_b128 v2, v[28:31] offset:13312\n"
"v_mfma_f32_16x16x32_bf16 v[152:155], a[64:67], a[112:115], v[152:155]\n"
"v_accvgpr_read_b32 v16, a184\n"
"v_accvgpr_read_b32 v17, a185\n"
"v_mfma_f32_16x16x32_bf16 v[152:155], a[68:71], a[116:119], v[152:155]\n"
"global_load_dwordx4 a[52:55], v0, s[34:35] offset:1024 sc1\n"
"v_accvgpr_read_b32 v18, a186\n"
"v_mfma_f32_16x16x32_bf16 v[180:183], a[76:79], a[116:119], v[180:183]\n"
"v_accvgpr_read_b32 v19, a187\n"
"v_mfma_f32_16x16x32_bf16 v[180:183], a[72:75], a[112:115], v[180:183]\n"
"v_lshlrev_b32 v12, 16, v16\n"
"v_mfma_f32_16x16x32_bf16 v[184:187], a[72:75], a[120:123], v[184:187]\n"
"v_and_b32 v13, s63, v16\n"
"v_lshlrev_b32 v14, 16, v17\n"
"v_mfma_f32_16x16x32_bf16 v[184:187], a[76:79], a[124:127], v[184:187]\n"
"v_and_b32 v15, s63, v17\n"
"v_mfma_f32_16x16x32_bf16 v[156:159], a[68:71], a[124:127], v[156:159]\n"
"v_accvgpr_read_b32 v10, a0\n"
"v_mfma_f32_16x16x32_bf16 v[156:159], a[64:67], a[120:123], v[156:159]\n"
"s_waitcnt lgkmcnt(5)\n"
"v_accvgpr_read_b32 v11, a1\n"
"v_mul_f32 v12, v12, v10\n"
"v_mfma_f32_16x16x32_bf16 v[160:163], a[64:67], a[128:131], v[160:163]\n"
"v_mul_f32 v13, v13, v11\n"
"v_mfma_f32_16x16x32_bf16 v[160:163], a[68:71], a[132:135], v[160:163]\n"
"global_load_dwordx4 a[56:59], v0, s[34:35] offset:2048 sc1\n"
"v_accvgpr_read_b32 v10, a2\n"
"v_mfma_f32_16x16x32_bf16 v[188:191], a[76:79], a[132:135], v[188:191]\n"
"v_accvgpr_read_b32 v11, a3\n"
"v_mul_f32 v14, v14, v10\n"
"v_mfma_f32_16x16x32_bf16 v[188:191], a[72:75], a[128:131], v[188:191]\n"
"v_mul_f32 v15, v15, v11\n"
"v_mfma_f32_16x16x32_bf16 v[192:195], a[72:75], a[136:139], v[192:195]\n"
"v_cvt_pk_bf16_f32 v24, v12, v13\n"
"v_mfma_f32_16x16x32_bf16 v[192:195], a[76:79], a[140:143], v[192:195]\n"
"v_cvt_pk_bf16_f32 v25, v14, v15\n"
"v_lshlrev_b32 v12, 16, v18\n"
"v_mfma_f32_16x16x32_bf16 v[164:167], a[68:71], a[140:143], v[164:167]\n"
"v_and_b32 v13, s63, v18\n"
"v_mfma_f32_16x16x32_bf16 v[164:167], a[64:67], a[136:139], v[164:167]\n"
"s_waitcnt lgkmcnt(1)\n"
"v_lshlrev_b32 v14, 16, v19\n"
"v_mfma_f32_16x16x32_bf16 v[168:171], a[64:67], a[144:147], v[168:171]\n"
"v_and_b32 v15, s63, v19\n"
"v_accvgpr_read_b32 v10, a4\n"
"v_mfma_f32_16x16x32_bf16 v[168:171], a[68:71], a[148:151], v[168:171]\n"
"global_load_dwordx4 a[60:63], v0, s[34:35] offset:3072 sc1\n"
"v_accvgpr_read_b32 v11, a5\n"
"v_mfma_f32_16x16x32_bf16 v[196:199], a[76:79], a[148:151], v[196:199]\n"
"v_mul_f32 v12, v12, v10\n"
"v_mfma_f32_16x16x32_bf16 v[196:199], a[72:75], a[144:147], v[196:199]\n"
"v_mul_f32 v13, v13, v11\n"
"v_accvgpr_read_b32 v10, a6\n"
"v_mfma_f32_16x16x32_bf16 a[20:23], a[72:75], a[152:155], a[20:23]\n"
"s_add_u32 s34, s34, 8192\n"
"v_accvgpr_read_b32 v11, a7\n"
"v_mfma_f32_16x16x32_bf16 a[20:23], a[76:79], a[156:159], a[20:23]\n"
"v_mul_f32 v14, v14, v10\n"
"v_mul_f32 v15, v15, v11\n"
"v_mfma_f32_16x16x32_bf16 a[16:19], a[68:71], a[156:159], a[16:19]\n"
"v_cvt_pk_bf16_f32 v26, v12, v13\n"
"v_cvt_pk_bf16_f32 v27, v14, v15\n"
"v_mfma_f32_16x16x32_bf16 a[16:19], a[64:67], a[152:155], a[16:19]\n"
"s_waitcnt vmcnt(10)\n"
"global_store_dwordx4 v0, v[24:27], s[54:55] offset:2048 sc1\n"
"ds_write_b128 v2, v[24:27] offset:14336\n"
"v_mfma_f32_16x16x32_bf16 v[200:203], a[80:83], a[96:99], v[200:203]\n"
"v_accvgpr_read_b32 v16, a188\n"
"v_mfma_f32_16x16x32_bf16 v[200:203], a[84:87], a[100:103], v[200:203]\n"
"global_load_dwordx4 a[64:67], v0, s[34:35] offset:-4096 sc1\n"
"v_accvgpr_read_b32 v17, a189\n"
"v_mfma_f32_16x16x32_bf16 v[228:231], a[92:95], a[100:103], v[228:231]\n"
"ds_read_b128 a[100:103], v2 offset:1024\n"
"v_accvgpr_read_b32 v18, a190\n"
"v_mfma_f32_16x16x32_bf16 v[228:231], a[88:91], a[96:99], v[228:231]\n"
"ds_read_b128 a[96:99], v2 offset:0\n"
"v_accvgpr_read_b32 v19, a191\n"
"v_mfma_f32_16x16x32_bf16 v[232:235], a[88:91], a[104:107], v[232:235]\n"
"v_lshlrev_b32 v12, 16, v16\n"
"v_and_b32 v13, s63, v16\n"
"v_mfma_f32_16x16x32_bf16 v[232:235], a[92:95], a[108:111], v[232:235]\n"
"v_lshlrev_b32 v14, 16, v17\n"
"v_and_b32 v15, s63, v17\n"
"v_mfma_f32_16x16x32_bf16 v[204:207], a[84:87], a[108:111], v[204:207]\n"
"ds_read_b128 a[108:111], v2 offset:3072\n"
"v_accvgpr_read_b32 v10, a8\n"
"v_mfma_f32_16x16x32_bf16 v[204:207], a[80:83], a[104:107], v[204:207]\n"
"ds_read_b128 a[104:107], v2 offset:2048\n"
"v_accvgpr_read_b32 v11, a9\n"
"v_mfma_f32_16x16x32_bf16 v[208:211], a[80:83], a[112:115], v[208:211]\n"
"v_mul_f32 v12, v12, v10\n"
"v_mul_f32 v13, v13, v11\n"
"v_mfma_f32_16x16x32_bf16 v[208:211], a[84:87], a[116:119], v[208:211]\n"
"global_load_dwordx4 a[68:71], v0, s[34:35] offset:-3072 sc1\n"
"v_accvgpr_read_b32 v10, a10\n"
"v_mfma_f32_16x16x32_bf16 v[236:239], a[92:95], a[116:119], v[236:239]\n"
"ds_read_b128 a[116:119], v2 offset:5120\n"
"v_accvgpr_read_b32 v11, a11\n"
"v_mfma_f32_16x16x32_bf16 v[236:239], a[88:91], a[112:115], v[236:239]\n"
"ds_read_b128 a[112:115], v2 offset:4096\n"
"v_mul_f32 v14, v14, v10\n"
"v_mfma_f32_16x16x32_bf16 v[240:243], a[88:91], a[120:123], v[240:243]\n"
"v_mul_f32 v15, v15, v11\n"
"v_cvt_pk_bf16_f32 v28, v12, v13\n"
"v_mfma_f32_16x16x32_bf16 v[240:243], a[92:95], a[124:127], v[240:243]\n"
"v_cvt_pk_bf16_f32 v29, v14, v15\n"
"v_lshlrev_b32 v12, 16, v18\n"
"v_mfma_f32_16x16x32_bf16 v[212:215], a[84:87], a[124:127], v[212:215]\n"
"ds_read_b128 a[124:127], v2 offset:7168\n"
"v_and_b32 v13, s63, v18\n"
"v_mfma_f32_16x16x32_bf16 v[212:215], a[80:83], a[120:123], v[212:215]\n"
"ds_read_b128 a[120:123], v2 offset:6144\n"
"v_lshlrev_b32 v14, 16, v19\n"
"v_mfma_f32_16x16x32_bf16 v[216:219], a[80:83], a[128:131], v[216:219]\n"
"v_and_b32 v15, s63, v19\n"
"v_accvgpr_read_b32 v10, a12\n"
"v_mfma_f32_16x16x32_bf16 v[216:219], a[84:87], a[132:135], v[216:219]\n"
"global_load_dwordx4 a[72:75], v0, s[34:35] offset:-2048 sc1\n"
"v_accvgpr_read_b32 v11, a13\n"
"v_mfma_f32_16x16x32_bf16 v[244:247], a[92:95], a[132:135], v[244:247]\n"
"ds_read_b128 a[132:135], v2 offset:9216\n"
"v_mul_f32 v12, v12, v10\n"
"v_mfma_f32_16x16x32_bf16 v[244:247], a[88:91], a[128:131], v[244:247]\n"
"ds_read_b128 a[128:131], v2 offset:8192\n"
"v_mul_f32 v13, v13, v11\n"
"v_mfma_f32_16x16x32_bf16 v[248:251], a[88:91], a[136:139], v[248:251]\n"
"v_accvgpr_read_b32 v10, a14\n"
"v_accvgpr_read_b32 v11, a15\n"
"v_mfma_f32_16x16x32_bf16 v[248:251], a[92:95], a[140:143], v[248:251]\n"
"v_mul_f32 v14, v14, v10\n"
"v_mul_f32 v15, v15, v11\n"
"v_mfma_f32_16x16x32_bf16 v[220:223], a[84:87], a[140:143], v[220:223]\n"
"ds_read_b128 a[140:143], v2 offset:11264\n"
"v_cvt_pk_bf16_f32 v30, v12, v13\n"
"v_mfma_f32_16x16x32_bf16 v[220:223], a[80:83], a[136:139], v[220:223]\n"
"ds_read_b128 a[136:139], v2 offset:10240\n"
"v_cvt_pk_bf16_f32 v31, v14, v15\n"
"v_mfma_f32_16x16x32_bf16 v[224:227], a[80:83], a[144:147], v[224:227]\n"
"global_store_dwordx4 v0, v[28:31], s[54:55] offset:3072 sc1\n"
"ds_write_b128 v2, v[28:31] offset:15360\n"
"v_mfma_f32_16x16x32_bf16 v[224:227], a[84:87], a[148:151], v[224:227]\n"
"global_load_dwordx4 a[76:79], v0, s[34:35] offset:-1024 sc1\n"
"v_mfma_f32_16x16x32_bf16 v[252:255], a[92:95], a[148:151], v[252:255]\n"
"ds_read_b128 a[148:151], v2 offset:13312\n"
"v_mfma_f32_16x16x32_bf16 v[252:255], a[88:91], a[144:147], v[252:255]\n"
"ds_read_b128 a[144:147], v2 offset:12288\n"
"v_mfma_f32_16x16x32_bf16 a[28:31], a[88:91], a[152:155], a[28:31]\n"
"s_add_u32 s62, s62, 1\n"
"v_mfma_f32_16x16x32_bf16 a[28:31], a[92:95], a[156:159], a[28:31]\n"
"v_mfma_f32_16x16x32_bf16 a[24:27], a[84:87], a[156:159], a[24:27]\n"
"ds_read_b128 a[156:159], v2 offset:15360\n"
"v_mfma_f32_16x16x32_bf16 a[24:27], a[80:83], a[152:155], a[24:27]\n"
"ds_read_b128 a[152:155], v2 offset:14336\n"
"L_layer:\n"
"s_waitcnt vmcnt(11) lgkmcnt(13)\n"
"v_mfma_f32_16x16x32_bf16 v[32:35], a[32:35], a[96:99], v[32:35]\n"
"s_mov_b64 s[96:97], s[52:53]\n"
"v_accvgpr_read_b32 v16, a192\n"
"v_accvgpr_read_b32 v17, a193\n"
"v_mfma_f32_16x16x32_bf16 v[32:35], a[36:39], a[100:103], v[32:35]\n"
"global_load_dwordx4 a[80:83], v0, s[34:35] offset:0 sc1\n"
"v_accvgpr_read_b32 v18, a194\n"
"v_mfma_f32_16x16x32_bf16 v[60:63], a[44:47], a[100:103], v[60:63]\n"
"s_mov_b64 s[98:99], s[54:55]\n"
"v_accvgpr_read_b32 v19, a195\n"
"v_mfma_f32_16x16x32_bf16 v[60:63], a[40:43], a[96:99], v[60:63]\n"
"v_lshlrev_b32 v12, 16, v16\n"
"v_and_b32 v13, s63, v16\n"
"v_mfma_f32_16x16x32_bf16 v[64:67], a[40:43], a[104:107], v[64:67]\n"
"s_add_u32 s52, s52, 131072\n"
"s_addc_u32 s53, s53, 0\n"
"v_lshlrev_b32 v14, 16, v17\n"
"v_mfma_f32_16x16x32_bf16 v[64:67], a[44:47], a[108:111], v[64:67]\n"
"v_and_b32 v15, s63, v17\n"
"v_mul_f32 v12, v12, v144\n"
"v_mfma_f32_16x16x32_bf16 v[36:39], a[36:39], a[108:111], v[36:39]\n"
"s_add_u32 s54, s54, 131072\n"
"s_addc_u32 s55, s55, 0\n"
"v_mul_f32 v13, v13, v145\n"
"v_mfma_f32_16x16x32_bf16 v[36:39], a[32:35], a[104:107], v[36:39]\n"
"s_waitcnt lgkmcnt(9)\n"
"v_mul_f32 v14, v14, v146\n"
"v_mul_f32 v15, v15, v147\n"
"v_mfma_f32_16x16x32_bf16 v[40:43], a[32:35], a[112:115], v[40:43]\n"
"ds_read_b128 v[20:23], v2 offset:0\n"
"v_cvt_pk_bf16_f32 v24, v12, v13\n"
"v_mfma_f32_16x16x32_bf16 v[40:43], a[36:39], a[116:119], v[40:43]\n"
"global_load_dwordx4 a[84:87], v0, s[34:35] offset:1024 sc1\n"
"v_cvt_pk_bf16_f32 v25, v14, v15\n"
"v_mfma_f32_16x16x32_bf16 v[68:71], a[44:47], a[116:119], v[68:71]\n"
"ds_write_b64 v2, v[24:25] offset:16384\n"
"v_mfma_f32_16x16x32_bf16 v[68:71], a[40:43], a[112:115], v[68:71]\n"
"v_lshlrev_b32 v12, 16, v18\n"
"v_and_b32 v13, s63, v18\n"
"v_mfma_f32_16x16x32_bf16 v[72:75], a[40:43], a[120:123], v[72:75]\n"
"s_waitcnt lgkmcnt(6)\n"
"s_barrier\n"
"v_mfma_f32_16x16x32_bf16 v[72:75], a[44:47], a[124:127], v[72:75]\n"
"v_lshlrev_b32 v14, 16, v19\n"
"v_and_b32 v15, s63, v19\n"
"v_mfma_f32_16x16x32_bf16 v[44:47], a[36:39], a[124:127], v[44:47]\n"
"v_mul_f32 v12, v12, v172\n"
"v_mul_f32 v13, v13, v173\n"
"v_mfma_f32_16x16x32_bf16 v[44:47], a[32:35], a[120:123], v[44:47]\n"
"v_mul_f32 v14, v14, v174\n"
"v_mfma_f32_16x16x32_bf16 v[48:51], a[32:35], a[128:131], v[48:51]\n"
"v_mul_f32 v15, v15, v175\n"
"v_mfma_f32_16x16x32_bf16 v[48:51], a[36:39], a[132:135], v[48:51]\n"
"global_load_dwordx4 a[88:91], v0, s[34:35] offset:2048 sc1\n"
"v_mfma_f32_16x16x32_bf16 v[76:79], a[44:47], a[132:135], v[76:79]\n"
"v_cvt_pk_bf16_f32 v26, v12, v13\n"
"v_mfma_f32_16x16x32_bf16 v[76:79], a[40:43], a[128:131], v[76:79]\n"
"v_cvt_pk_bf16_f32 v27, v14, v15\n"
"v_mfma_f32_16x16x32_bf16 v[80:83], a[40:43], a[136:139], v[80:83]\n"
"s_waitcnt lgkmcnt(1)\n"
"global_store_dwordx4 v0, v[20:23], s[96:97] offset:-4096 sc1\n"
"ds_write_b64 v2, v[26:27] offset:16392\n"
"v_accvgpr_read_b32 v16, a196\n"
"v_mfma_f32_16x16x32_bf16 v[80:83], a[44:47], a[140:143], v[80:83]\n"
"v_accvgpr_read_b32 v17, a197\n"
"v_accvgpr_read_b32 v18, a198\n"
"v_mfma_f32_16x16x32_bf16 v[52:55], a[36:39], a[140:143], v[52:55]\n"
"v_accvgpr_read_b32 v19, a199\n"
"v_lshlrev_b32 v12, 16, v16\n"
"v_mfma_f32_16x16x32_bf16 v[52:55], a[32:35], a[136:139], v[52:55]\n"
"v_and_b32 v13, s63, v16\n"
"v_lshlrev_b32 v14, 16, v17\n"
"v_mfma_f32_16x16x32_bf16 v[56:59], a[32:35], a[144:147], v[56:59]\n"
"v_and_b32 v15, s63, v17\n"
"v_mul_f32 v12, v12, v200\n"
"v_mfma_f32_16x16x32_bf16 v[56:59], a[36:39], a[148:151], v[56:59]\n"
"global_load_dwordx4 a[92:95], v0, s[34:35] offset:3072 sc1\n"
"v_mul_f32 v13, v13, v201\n"
"v_mfma_f32_16x16x32_bf16 v[84:87], a[44:47], a[148:151], v[84:87]\n"
"v_mul_f32 v14, v14, v202\n"
"v_mul_f32 v15, v15, v203\n"
"v_mfma_f32_16x16x32_bf16 v[84:87], a[40:43], a[144:147], v[84:87]\n"
"v_cvt_pk_bf16_f32 v28, v12, v13\n"
"v_cvt_pk_bf16_f32 v29, v14, v15\n"
"v_mfma_f32_16x16x32_bf16 a[4:7], a[40:43], a[152:155], a[4:7]\n"
"s_add_u32 s34, s34, 8192\n"
"ds_write_b64 v2, v[28:29] offset:17408\n"
"v_mfma_f32_16x16x32_bf16 a[4:7], a[44:47], a[156:159], a[4:7]\n"
"v_lshlrev_b32 v12, 16, v18\n"
"v_mfma_f32_16x16x32_bf16 a[0:3], a[36:39], a[156:159], a[0:3]\n"
"v_and_b32 v13, s63, v18\n"
"v_lshlrev_b32 v14, 16, v19\n"
"v_mfma_f32_16x16x32_bf16 a[0:3], a[32:35], a[152:155], a[0:3]\n"
"s_waitcnt vmcnt(11)\n"
"v_and_b32 v15, s63, v19\n"
"v_mul_f32 v12, v12, v228\n"
"v_mfma_f32_16x16x32_bf16 v[88:91], a[48:51], a[96:99], v[88:91]\n"
"v_mul_f32 v13, v13, v229\n"
"v_mul_f32 v14, v14, v230\n"
"v_mfma_f32_16x16x32_bf16 v[88:91], a[52:55], a[100:103], v[88:91]\n"
"global_load_dwordx4 a[32:35], v0, s[34:35] offset:-4096 sc1\n"
"v_mul_f32 v15, v15, v231\n"
"v_mfma_f32_16x16x32_bf16 v[116:119], a[60:63], a[100:103], v[116:119]\n"
"ds_read_b128 a[100:103], v3 offset:1024\n"
"v_cvt_pk_bf16_f32 v30, v12, v13\n"
"v_mfma_f32_16x16x32_bf16 v[116:119], a[56:59], a[96:99], v[116:119]\n"
"ds_read_b128 a[96:99], v3 offset:0\n"
"v_cvt_pk_bf16_f32 v31, v14, v15\n"
"v_mfma_f32_16x16x32_bf16 v[120:123], a[56:59], a[104:107], v[120:123]\n"
"ds_write_b64 v2, v[30:31] offset:17416\n"
"v_mfma_f32_16x16x32_bf16 v[120:123], a[60:63], a[108:111], v[120:123]\n"
"ds_read_b128 v[20:23], v2 offset:3072\n"
"v_accvgpr_read_b32 v16, a200\n"
"v_mfma_f32_16x16x32_bf16 v[92:95], a[52:55], a[108:111], v[92:95]\n"
"ds_read_b128 a[108:111], v3 offset:3072\n"
"v_accvgpr_read_b32 v17, a201\n"
"v_mfma_f32_16x16x32_bf16 v[92:95], a[48:51], a[104:107], v[92:95]\n"
"ds_read_b128 a[104:107], v3 offset:2048\n"
"v_accvgpr_read_b32 v18, a202\n"
"v_mfma_f32_16x16x32_bf16 v[96:99], a[48:51], a[112:115], v[96:99]\n"
"v_accvgpr_read_b32 v19, a203\n"
"v_lshlrev_b32 v12, 16, v16\n"
"v_mfma_f32_16x16x32_bf16 v[96:99], a[52:55], a[116:119], v[96:99]\n"
"global_load_dwordx4 a[36:39], v0, s[34:35] offset:-3072 sc1\n"
"v_and_b32 v13, s63, v16\n"
"v_mfma_f32_16x16x32_bf16 v[124:127], a[60:63], a[116:119], v[124:127]\n"
"ds_read_b128 a[116:119], v3 offset:5120\n"
"v_lshlrev_b32 v14, 16, v17\n"
"v_mfma_f32_16x16x32_bf16 v[124:127], a[56:59], a[112:115], v[124:127]\n"
"ds_read_b128 a[112:115], v3 offset:4096\n"
"v_and_b32 v15, s63, v17\n"
"v_mfma_f32_16x16x32_bf16 v[128:131], a[56:59], a[120:123], v[128:131]\n"
"v_mul_f32 v12, v12, v148\n"
"v_mul_f32 v13, v13, v149\n"
"v_mfma_f32_16x16x32_bf16 v[128:131], a[60:63], a[124:127], v[128:131]\n"
"v_mul_f32 v14, v14, v150\n"
"v_mul_f32 v15, v15, v151\n"
"v_mfma_f32_16x16x32_bf16 v[100:103], a[52:55], a[124:127], v[100:103]\n"
"L_st5093:\n"
"s_cbranch_execz L_st7375\n"
"ds_read_b128 a[124:127], v3 offset:7168\n"
"v_cvt_pk_bf16_f32 v24, v12, v13\n"
"v_mfma_f32_16x16x32_bf16 v[100:103], a[48:51], a[120:123], v[100:103]\n"
"ds_read_b128 a[120:123], v3 offset:6144\n"
"v_cvt_pk_bf16_f32 v25, v14, v15\n"
"v_mfma_f32_16x16x32_bf16 v[104:107], a[48:51], a[128:131], v[104:107]\n"
"s_waitcnt lgkmcnt(6)\n"
"global_store_dwordx4 v0, v[20:23], s[96:97] offset:-1024 sc1\n"
"ds_write_b64 v2, v[24:25] offset:18432\n"
"v_lshlrev_b32 v12, 16, v18\n"
"v_mfma_f32_16x16x32_bf16 v[104:107], a[52:55], a[132:135], v[104:107]\n"
"global_load_dwordx4 a[40:43], v0, s[34:35] offset:-2048 sc1\n"
"v_and_b32 v13, s63, v18\n"
"v_mfma_f32_16x16x32_bf16 v[132:135], a[60:63], a[132:135], v[132:135]\n"
"ds_read_b128 a[132:135], v3 offset:9216\n"
"v_lshlrev_b32 v14, 16, v19\n"
"v_mfma_f32_16x16x32_bf16 v[132:135], a[56:59], a[128:131], v[132:135]\n"
"ds_read_b128 a[128:131], v3 offset:8192\n"
"v_and_b32 v15, s63, v19\n"
"v_mfma_f32_16x16x32_bf16 v[136:139], a[56:59], a[136:139], v[136:139]\n"
"v_mul_f32 v12, v12, v176\n"
"v_mul_f32 v13, v13, v177\n"
"v_mfma_f32_16x16x32_bf16 v[136:139], a[60:63], a[140:143], v[136:139]\n"
"v_mul_f32 v14, v14, v178\n"
"v_mul_f32 v15, v15, v179\n"
"v_mfma_f32_16x16x32_bf16 v[108:111], a[52:55], a[140:143], v[108:111]\n"
"ds_read_b128 a[140:143], v3 offset:11264\n"
"v_cvt_pk_bf16_f32 v26, v12, v13\n"
"v_mfma_f32_16x16x32_bf16 v[108:111], a[48:51], a[136:139], v[108:111]\n"
"ds_read_b128 a[136:139], v3 offset:10240\n"
"v_cvt_pk_bf16_f32 v27, v14, v15\n"
"v_mfma_f32_16x16x32_bf16 v[112:115], a[48:51], a[144:147], v[112:115]\n"
"ds_write_b64 v2, v[26:27] offset:18440\n"
"v_mfma_f32_16x16x32_bf16 v[112:115], a[52:55], a[148:151], v[112:115]\n"
"global_load_dwordx4 a[44:47], v0, s[34:35] offset:-1024 sc1\n"
"v_accvgpr_read_b32 v16, a204\n"
"v_mfma_f32_16x16x32_bf16 v[140:143], a[60:63], a[148:151], v[140:143]\n"
"ds_read_b128 a[148:151], v3 offset:13312\n"
"v_accvgpr_read_b32 v17, a205\n"
"v_mfma_f32_16x16x32_bf16 v[140:143], a[56:59], a[144:147], v[140:143]\n"
"ds_read_b128 a[144:147], v3 offset:12288\n"
"v_accvgpr_read_b32 v18, a206\n"
"v_mfma_f32_16x16x32_bf16 a[12:15], a[56:59], a[152:155], a[12:15]\n"
"v_accvgpr_read_b32 v19, a207\n"
"v_lshlrev_b32 v12, 16, v16\n"
"v_mfma_f32_16x16x32_bf16 a[12:15], a[60:63], a[156:159], a[12:15]\n"
"v_and_b32 v13, s63, v16\n"
"v_lshlrev_b32 v14, 16, v17\n"
"v_mfma_f32_16x16x32_bf16 a[8:11], a[52:55], a[156:159], a[8:11]\n"
"ds_read_b128 a[156:159], v3 offset:15360\n"
"v_and_b32 v15, s63, v17\n"
"v_mfma_f32_16x16x32_bf16 a[8:11], a[48:51], a[152:155], a[8:11]\n"
"s_waitcnt vmcnt(10) lgkmcnt(13)\n"
"ds_read_b128 a[152:155], v3 offset:14336\n"
"v_mul_f32 v12, v12, v204\n"
"v_mfma_f32_16x16x32_bf16 v[32:35], a[64:67], a[96:99], v[32:35]\n"
"ds_read_b128 v[20:23], v2 offset:6144\n"
"v_mul_f32 v13, v13, v205\n"
"v_mfma_f32_16x16x32_bf16 v[32:35], a[68:71], a[100:103], v[32:35]\n"
"global_load_dwordx4 a[48:51], v0, s[34:35] offset:0 sc1\n"
"v_mul_f32 v14, v14, v206\n"
"v_mfma_f32_16x16x32_bf16 v[60:63], a[76:79], a[100:103], v[60:63]\n"
"v_mul_f32 v15, v15, v207\n"
"v_cvt_pk_bf16_f32 v28, v12, v13\n"
"v_mfma_f32_16x16x32_bf16 v[60:63], a[72:75], a[96:99], v[60:63]\n"
"v_cvt_pk_bf16_f32 v29, v14, v15\n"
"v_mfma_f32_16x16x32_bf16 v[64:67], a[72:75], a[104:107], v[64:67]\n"
"ds_write_b64 v2, v[28:29] offset:19456\n"
"v_mfma_f32_16x16x32_bf16 v[64:67], a[76:79], a[108:111], v[64:67]\n"
"v_lshlrev_b32 v12, 16, v18\n"
"v_and_b32 v13, s63, v18\n"
"v_mfma_f32_16x16x32_bf16 v[36:39], a[68:71], a[108:111], v[36:39]\n"
"v_lshlrev_b32 v14, 16, v19\n"
"v_and_b32 v15, s63, v19\n"
"v_mfma_f32_16x16x32_bf16 v[36:39], a[64:67], a[104:107], v[36:39]\n"
"s_waitcnt lgkmcnt(12)\n"
"v_mul_f32 v12, v12, v232\n"
"v_mul_f32 v13, v13, v233\n"
"v_mfma_f32_16x16x32_bf16 v[40:43], a[64:67], a[112:115], v[40:43]\n"
"v_mul_f32 v14, v14, v234\n"
"v_mfma_f32_16x16x32_bf16 v[40:43], a[68:71], a[116:119], v[40:43]\n"
"global_load_dwordx4 a[52:55], v0, s[34:35] offset:1024 sc1\n"
"v_mul_f32 v15, v15, v235\n"
"v_mfma_f32_16x16x32_bf16 v[68:71], a[76:79], a[116:119], v[68:71]\n"
"v_cvt_pk_bf16_f32 v30, v12, v13\n"
"v_mfma_f32_16x16x32_bf16 v[68:71], a[72:75], a[112:115], v[68:71]\n"
"v_cvt_pk_bf16_f32 v31, v14, v15\n"
"v_mfma_f32_16x16x32_bf16 v[72:75], a[72:75], a[120:123], v[72:75]\n"
"s_waitcnt lgkmcnt(1)\n"
"global_store_dwordx4 v0, v[20:23], s[96:97] offset:2048 sc1\n"
"ds_write_b64 v2, v[30:31] offset:19464\n"
"v_accvgpr_read_b32 v16, a208\n"
"v_mfma_f32_16x16x32_bf16 v[72:75], a[76:79], a[124:127], v[72:75]\n"
"v_accvgpr_read_b32 v17, a209\n"
"v_accvgpr_read_b32 v18, a210\n"
"v_mfma_f32_16x16x32_bf16 v[44:47], a[68:71], a[124:127], v[44:47]\n"
"v_accvgpr_read_b32 v19, a211\n"
"v_mfma_f32_16x16x32_bf16 v[44:47], a[64:67], a[120:123], v[44:47]\n"
"v_lshlrev_b32 v12, 16, v16\n"
"v_and_b32 v13, s63, v16\n"
"v_mfma_f32_16x16x32_bf16 v[48:51], a[64:67], a[128:131], v[48:51]\n"
"v_lshlrev_b32 v14, 16, v17\n"
"v_mfma_f32_16x16x32_bf16 v[48:51], a[68:71], a[132:135], v[48:51]\n"
"global_load_dwordx4 a[56:59], v0, s[34:35] offset:2048 sc1\n"
"v_and_b32 v15, s63, v17\n"
"v_mfma_f32_16x16x32_bf16 v[76:79], a[76:79], a[132:135], v[76:79]\n"
"v_mul_f32 v12, v12, v152\n"
"v_mul_f32 v13, v13, v153\n"
"v_mfma_f32_16x16x32_bf16 v[76:79], a[72:75], a[128:131], v[76:79]\n"
"v_mul_f32 v14, v14, v154\n"
"v_mul_f32 v15, v15, v155\n"
"v_mfma_f32_16x16x32_bf16 v[80:83], a[72:75], a[136:139], v[80:83]\n"
"v_cvt_pk_bf16_f32 v24, v12, v13\n"
"v_cvt_pk_bf16_f32 v25, v14, v15\n"
"v_mfma_f32_16x16x32_bf16 v[80:83], a[76:79], a[140:143], v[80:83]\n"
"ds_write_b64 v2, v[24:25] offset:20480\n"
"v_mfma_f32_16x16x32_bf16 v[52:55], a[68:71], a[140:143], v[52:55]\n"
"v_lshlrev_b32 v12, 16, v18\n"
"v_mfma_f32_16x16x32_bf16 v[52:55], a[64:67], a[136:139], v[52:55]\n"
"v_and_b32 v13, s63, v18\n"
"v_lshlrev_b32 v14, 16, v19\n"
"v_mfma_f32_16x16x32_bf16 v[56:59], a[64:67], a[144:147], v[56:59]\n"
"v_and_b32 v15, s63, v19\n"
"v_mul_f32 v12, v12, v180\n"
"v_mfma_f32_16x16x32_bf16 v[56:59], a[68:71], a[148:151], v[56:59]\n"
"global_load_dwordx4 a[60:63], v0, s[34:35] offset:3072 sc1\n"
"v_mul_f32 v13, v13, v181\n"
"v_mfma_f32_16x16x32_bf16 v[84:87], a[76:79], a[148:151], v[84:87]\n"
"v_mul_f32 v14, v14, v182\n"
"v_mul_f32 v15, v15, v183\n"
"v_mfma_f32_16x16x32_bf16 v[84:87], a[72:75], a[144:147], v[84:87]\n"
"v_cvt_pk_bf16_f32 v26, v12, v13\n"
"v_cvt_pk_bf16_f32 v27, v14, v15\n"
"v_mfma_f32_16x16x32_bf16 a[4:7], a[72:75], a[152:155], a[4:7]\n"
"s_add_u32 s34, s34, 8192\n"
"ds_write_b64 v2, v[26:27] offset:20488\n"
"v_mfma_f32_16x16x32_bf16 a[4:7], a[76:79], a[156:159], a[4:7]\n"
"ds_read_b128 v[20:23], v2 offset:9216\n"
"v_mfma_f32_16x16x32_bf16 a[0:3], a[68:71], a[156:159], a[0:3]\n"
"v_accvgpr_read_b32 v16, a212\n"
"v_mfma_f32_16x16x32_bf16 a[0:3], a[64:67], a[152:155], a[0:3]\n"
"s_waitcnt vmcnt(10)\n"
"v_accvgpr_read_b32 v17, a213\n"
"v_accvgpr_read_b32 v18, a214\n"
"v_mfma_f32_16x16x32_bf16 v[88:91], a[80:83], a[96:99], v[88:91]\n"
"v_accvgpr_read_b32 v19, a215\n"
"v_lshlrev_b32 v12, 16, v16\n"
"v_mfma_f32_16x16x32_bf16 v[88:91], a[84:87], a[100:103], v[88:91]\n"
"global_load_dwordx4 a[64:67], v0, s[34:35] offset:-4096 sc1\n"
"v_and_b32 v13, s63, v16\n"
"v_mfma_f32_16x16x32_bf16 v[116:119], a[92:95], a[100:103], v[116:119]\n"
"ds_read_b128 a[100:103], v4 offset:1024\n"
"v_lshlrev_b32 v14, 16, v17\n"
"v_mfma_f32_16x16x32_bf16 v[116:119], a[88:91], a[96:99], v[116:119]\n"
"ds_read_b128 a[96:99], v4 offset:0\n"
"v_and_b32 v15, s63, v17\n"
"v_mfma_f32_16x16x32_bf16 v[120:123], a[88:91], a[104:107], v[120:123]\n"
"v_mul_f32 v12, v12, v208\n"
"v_mul_f32 v13, v13, v209\n"
"v_mfma_f32_16x16x32_bf16 v[120:123], a[92:95], a[108:111], v[120:123]\n"
"v_mul_f32 v14, v14, v210\n"
"v_mul_f32 v15, v15, v211\n"
"v_mfma_f32_16x16x32_bf16 v[92:95], a[84:87], a[108:111], v[92:95]\n"
"ds_read_b128 a[108:111], v4 offset:3072\n"
"v_cvt_pk_bf16_f32 v28, v12, v13\n"
"v_mfma_f32_16x16x32_bf16 v[92:95], a[80:83], a[104:107], v[92:95]\n"
"ds_read_b128 a[104:107], v4 offset:2048\n"
"v_cvt_pk_bf16_f32 v29, v14, v15\n"
"v_mfma_f32_16x16x32_bf16 v[96:99], a[80:83], a[112:115], v[96:99]\n"
"s_waitcnt lgkmcnt(4)\n"
"global_store_dwordx4 v0, v[20:23], s[98:99] offset:-3072 sc1\n"
"ds_write_b64 v2, v[28:29] offset:21504\n"
"v_lshlrev_b32 v12, 16, v18\n"
"v_mfma_f32_16x16x32_bf16 v[96:99], a[84:87], a[116:119], v[96:99]\n"
"global_load_dwordx4 a[68:71], v0, s[34:35] offset:-3072 sc1\n"
"v_and_b32 v13, s63, v18\n"
"v_mfma_f32_16x16x32_bf16 v[124:127], a[92:95], a[116:119], v[124:127]\n"
"ds_read_b128 a[116:119], v4 offset:5120\n"
"v_lshlrev_b32 v14, 16, v19\n"
"v_mfma_f32_16x16x32_bf16 v[124:127], a[88:91], a[112:115], v[124:127]\n"
"ds_read_b128 a[112:115], v4 offset:4096\n"
"v_and_b32 v15, s63, v19\n"
"v_mfma_f32_16x16x32_bf16 v[128:131], a[88:91], a[120:123], v[128:131]\n"
"v_mul_f32 v12, v12, v236\n"
"v_mul_f32 v13, v13, v237\n"
"v_mfma_f32_16x16x32_bf16 v[128:131], a[92:95], a[124:127], v[128:131]\n"
"v_mul_f32 v14, v14, v238\n"
"v_mul_f32 v15, v15, v239\n"
"v_mfma_f32_16x16x32_bf16 v[100:103], a[84:87], a[124:127], v[100:103]\n"
"ds_read_b128 a[124:127], v4 offset:7168\n"
"v_cvt_pk_bf16_f32 v30, v12, v13\n"
"v_mfma_f32_16x16x32_bf16 v[100:103], a[80:83], a[120:123], v[100:103]\n"
"ds_read_b128 a[120:123], v4 offset:6144\n"
"v_cvt_pk_bf16_f32 v31, v14, v15\n"
"v_mfma_f32_16x16x32_bf16 v[104:107], a[80:83], a[128:131], v[104:107]\n"
"ds_write_b64 v2, v[30:31] offset:21512\n"
"v_mfma_f32_16x16x32_bf16 v[104:107], a[84:87], a[132:135], v[104:107]\n"
"global_load_dwordx4 a[72:75], v0, s[34:35] offset:-2048 sc1\n"
"v_accvgpr_read_b32 v16, a216\n"
"v_mfma_f32_16x16x32_bf16 v[132:135], a[92:95], a[132:135], v[132:135]\n"
"ds_read_b128 a[132:135], v4 offset:9216\n"
"v_accvgpr_read_b32 v17, a217\n"
"v_mfma_f32_16x16x32_bf16 v[132:135], a[88:91], a[128:131], v[132:135]\n"
"ds_read_b128 a[128:131], v4 offset:8192\n"
"v_accvgpr_read_b32 v18, a218\n"
"v_mfma_f32_16x16x32_bf16 v[136:139], a[88:91], a[136:139], v[136:139]\n"
"v_accvgpr_read_b32 v19, a219\n"
"v_mfma_f32_16x16x32_bf16 v[136:139], a[92:95], a[140:143], v[136:139]\n"
"v_lshlrev_b32 v12, 16, v16\n"
"v_and_b32 v13, s63, v16\n"
"v_mfma_f32_16x16x32_bf16 v[108:111], a[84:87], a[140:143], v[108:111]\n"
"ds_read_b128 a[140:143], v4 offset:11264\n"
"v_lshlrev_b32 v14, 16, v17\n"
"v_mfma_f32_16x16x32_bf16 v[108:111], a[80:83], a[136:139], v[108:111]\n"
"ds_read_b128 a[136:139], v4 offset:10240\n"
"v_and_b32 v15, s63, v17\n"
"v_mfma_f32_16x16x32_bf16 v[112:115], a[80:83], a[144:147], v[112:115]\n"
"ds_read_b128 v[20:23], v2 offset:12288\n"
"v_mul_f32 v12, v12, v156\n"
"v_mfma_f32_16x16x32_bf16 v[112:115], a[84:87], a[148:151], v[112:115]\n"
"global_load_dwordx4 a[76:79], v0, s[34:35] offset:-1024 sc1\n"
"v_mfma_f32_16x16x32_bf16 v[140:143], a[92:95], a[148:151], v[140:143]\n"
"ds_read_b128 a[148:151], v4 offset:13312\n"
"v_mfma_f32_16x16x32_bf16 v[140:143], a[88:91], a[144:147], v[140:143]\n"
"ds_read_b128 a[144:147], v4 offset:12288\n"
"v_mfma_f32_16x16x32_bf16 a[12:15], a[88:91], a[152:155], a[12:15]\n"
"v_mul_f32 v13, v13, v157\n"
"v_mfma_f32_16x16x32_bf16 a[12:15], a[92:95], a[156:159], a[12:15]\n"
"v_mul_f32 v14, v14, v158\n"
"v_mfma_f32_16x16x32_bf16 a[8:11], a[84:87], a[156:159], a[8:11]\n"
"ds_read_b128 a[156:159], v4 offset:15360\n"
"v_mfma_f32_16x16x32_bf16 a[8:11], a[80:83], a[152:155], a[8:11]\n"
"s_waitcnt vmcnt(10) lgkmcnt(14)\n"
"ds_read_b128 a[152:155], v4 offset:14336\n"
"v_mfma_f32_16x16x32_bf16 v[32:35], a[32:35], a[96:99], v[32:35]\n"
"v_mul_f32 v15, v15, v159\n"
"v_mfma_f32_16x16x32_bf16 v[32:35], a[36:39], a[100:103], v[32:35]\n"
"global_load_dwordx4 a[80:83], v0, s[34:35] offset:0 sc1\n"
"v_mfma_f32_16x16x32_bf16 v[60:63], a[44:47], a[100:103], v[60:63]\n"
"s_add_u32 s65, s65, 1\n"
"v_cvt_pk_bf16_f32 v24, v12, v13\n"
"v_mfma_f32_16x16x32_bf16 v[60:63], a[40:43], a[96:99], v[60:63]\n"
"v_cvt_pk_bf16_f32 v25, v14, v15\n"
"v_mfma_f32_16x16x32_bf16 v[64:67], a[40:43], a[104:107], v[64:67]\n"
"s_waitcnt lgkmcnt(4)\n"
"global_store_dwordx4 v0, v[20:23], s[98:99] offset:0 sc1\n"
"ds_write_b64 v2, v[24:25] offset:22528\n"
"v_lshlrev_b32 v12, 16, v18\n"
"v_mfma_f32_16x16x32_bf16 v[64:67], a[44:47], a[108:111], v[64:67]\n"
"v_and_b32 v13, s63, v18\n"
"v_lshlrev_b32 v14, 16, v19\n"
"v_mfma_f32_16x16x32_bf16 v[36:39], a[36:39], a[108:111], v[36:39]\n"
"v_and_b32 v15, s63, v19\n"
"v_mul_f32 v12, v12, v184\n"
"v_mfma_f32_16x16x32_bf16 v[36:39], a[32:35], a[104:107], v[36:39]\n"
"v_mul_f32 v13, v13, v185\n"
"v_mul_f32 v14, v14, v186\n"
"v_mfma_f32_16x16x32_bf16 v[40:43], a[32:35], a[112:115], v[40:43]\n"
"v_mul_f32 v15, v15, v187\n"
"v_cvt_pk_bf16_f32 v26, v12, v13\n"
"v_mfma_f32_16x16x32_bf16 v[40:43], a[36:39], a[116:119], v[40:43]\n"
"global_load_dwordx4 a[84:87], v0, s[34:35] offset:1024 sc1\n"
"v_cvt_pk_bf16_f32 v27, v14, v15\n"
"v_mfma_f32_16x16x32_bf16 v[68:71], a[44:47], a[116:119], v[68:71]\n"
"ds_write_b64 v2, v[26:27] offset:22536\n"
"v_mfma_f32_16x16x32_bf16 v[68:71], a[40:43], a[112:115], v[68:71]\n"
"v_accvgpr_read_b32 v16, a220\n"
"v_accvgpr_read_b32 v17, a221\n"
"v_mfma_f32_16x16x32_bf16 v[72:75], a[40:43], a[120:123], v[72:75]\n"
"v_accvgpr_read_b32 v18, a222\n"
"v_accvgpr_read_b32 v19, a223\n"
"v_mfma_f32_16x16x32_bf16 v[72:75], a[44:47], a[124:127], v[72:75]\n"
"v_lshlrev_b32 v12, 16, v16\n"
"v_and_b32 v13, s63, v16\n"
"v_mfma_f32_16x16x32_bf16 v[44:47], a[36:39], a[124:127], v[44:47]\n"
"v_lshlrev_b32 v14, 16, v17\n"
"v_and_b32 v15, s63, v17\n"
"v_mfma_f32_16x16x32_bf16 v[44:47], a[32:35], a[120:123], v[44:47]\n"
"v_mul_f32 v12, v12, v212\n"
"v_mul_f32 v13, v13, v213\n"
"v_mfma_f32_16x16x32_bf16 v[48:51], a[32:35], a[128:131], v[48:51]\n"
"v_mul_f32 v14, v14, v214\n"
"v_mul_f32 v15, v15, v215\n"
"v_mfma_f32_16x16x32_bf16 v[48:51], a[36:39], a[132:135], v[48:51]\n"
"global_load_dwordx4 a[88:91], v0, s[34:35] offset:2048 sc1\n"
"v_cvt_pk_bf16_f32 v28, v12, v13\n"
"v_mfma_f32_16x16x32_bf16 v[76:79], a[44:47], a[132:135], v[76:79]\n"
"v_cvt_pk_bf16_f32 v29, v14, v15\n"
"v_mfma_f32_16x16x32_bf16 v[76:79], a[40:43], a[128:131], v[76:79]\n"
"ds_write_b64 v2, v[28:29] offset:23552\n"
"v_mfma_f32_16x16x32_bf16 v[80:83], a[40:43], a[136:139], v[80:83]\n"
"v_lshlrev_b32 v12, 16, v18\n"
"v_and_b32 v13, s63, v18\n"
"v_mfma_f32_16x16x32_bf16 v[80:83], a[44:47], a[140:143], v[80:83]\n"
"v_lshlrev_b32 v14, 16, v19\n"
"v_and_b32 v15, s63, v19\n"
"v_mfma_f32_16x16x32_bf16 v[52:55], a[36:39], a[140:143], v[52:55]\n"
"v_mul_f32 v12, v12, v240\n"
"v_mul_f32 v13, v13, v241\n"
"v_mfma_f32_16x16x32_bf16 v[52:55], a[32:35], a[136:139], v[52:55]\n"
"s_waitcnt lgkmcnt(3)\n"
"v_mul_f32 v14, v14, v242\n"
"v_mul_f32 v15, v15, v243\n"
"v_mfma_f32_16x16x32_bf16 v[56:59], a[32:35], a[144:147], v[56:59]\n"
"v_cvt_pk_bf16_f32 v30, v12, v13\n"
"v_cvt_pk_bf16_f32 v31, v14, v15\n"
"v_mfma_f32_16x16x32_bf16 v[56:59], a[36:39], a[148:151], v[56:59]\n"
"global_load_dwordx4 a[92:95], v0, s[34:35] offset:3072 sc1\n"
"v_mfma_f32_16x16x32_bf16 v[84:87], a[44:47], a[148:151], v[84:87]\n"
"ds_write_b64 v2, v[30:31] offset:23560\n"
"v_mfma_f32_16x16x32_bf16 v[84:87], a[40:43], a[144:147], v[84:87]\n"
"v_accvgpr_read_b32 v16, a224\n"
"v_accvgpr_read_b32 v17, a225\n"
"v_mfma_f32_16x16x32_bf16 a[4:7], a[40:43], a[152:155], a[4:7]\n"
"s_add_u32 s34, s34, 8192\n"
"v_accvgpr_read_b32 v18, a226\n"
"v_accvgpr_read_b32 v19, a227\n"
"v_mfma_f32_16x16x32_bf16 a[4:7], a[44:47], a[156:159], a[4:7]\n"
"v_lshlrev_b32 v12, 16, v16\n"
"v_and_b32 v13, s63, v16\n"
"v_mfma_f32_16x16x32_bf16 a[0:3], a[36:39], a[156:159], a[0:3]\n"
"v_lshlrev_b32 v14, 16, v17\n"
"v_and_b32 v15, s63, v17\n"
"v_mfma_f32_16x16x32_bf16 a[0:3], a[32:35], a[152:155], a[0:3]\n"
"s_waitcnt vmcnt(10)\n"
"v_mul_f32 v12, v12, v160\n"
"v_mul_f32 v13, v13, v161\n"
"v_mfma_f32_16x16x32_bf16 v[88:91], a[48:51], a[96:99], v[88:91]\n"
"v_mul_f32 v14, v14, v162\n"
"v_mul_f32 v15, v15, v163\n"
"v_mfma_f32_16x16x32_bf16 v[88:91], a[52:55], a[100:103], v[88:91]\n"
"global_load_dwordx4 a[32:35], v0, s[34:35] offset:-4096 sc1\n"
"v_cvt_pk_bf16_f32 v24, v12, v13\n"
"v_mfma_f32_16x16x32_bf16 v[116:119], a[60:63], a[100:103], v[116:119]\n"
"ds_read_b128 a[100:103], v5 offset:1024\n"
"v_cvt_pk_bf16_f32 v25, v14, v15\n"
"v_mfma_f32_16x16x32_bf16 v[116:119], a[56:59], a[96:99], v[116:119]\n"
"ds_read_b128 a[96:99], v5 offset:0\n"
"v_mfma_f32_16x16x32_bf16 v[120:123], a[56:59], a[104:107], v[120:123]\n"
"ds_write_b64 v2, v[24:25] offset:24576\n"
"v_mfma_f32_16x16x32_bf16 v[120:123], a[60:63], a[108:111], v[120:123]\n"
"v_lshlrev_b32 v12, 16, v18\n"
"v_and_b32 v13, s63, v18\n"
"v_mfma_f32_16x16x32_bf16 v[92:95], a[52:55], a[108:111], v[92:95]\n"
"ds_read_b128 a[108:111], v5 offset:3072\n"
"v_lshlrev_b32 v14, 16, v19\n"
"v_mfma_f32_16x16x32_bf16 v[92:95], a[48:51], a[104:107], v[92:95]\n"
"ds_read_b128 a[104:107], v5 offset:2048\n"
"v_and_b32 v15, s63, v19\n"
"v_mfma_f32_16x16x32_bf16 v[96:99], a[48:51], a[112:115], v[96:99]\n"
"v_mul_f32 v12, v12, v188\n"
"v_mul_f32 v13, v13, v189\n"
"v_mfma_f32_16x16x32_bf16 v[96:99], a[52:55], a[116:119], v[96:99]\n"
"global_load_dwordx4 a[36:39], v0, s[34:35] offset:-3072 sc1\n"
"v_mul_f32 v14, v14, v190\n"
"v_mfma_f32_16x16x32_bf16 v[124:127], a[60:63], a[116:119], v[124:127]\n"
"ds_read_b128 a[116:119], v5 offset:5120\n"
"v_mul_f32 v15, v15, v191\n"
"v_mfma_f32_16x16x32_bf16 v[124:127], a[56:59], a[112:115], v[124:127]\n"
"ds_read_b128 a[112:115], v5 offset:4096\n"
"v_cvt_pk_bf16_f32 v26, v12, v13\n"
"v_mfma_f32_16x16x32_bf16 v[128:131], a[56:59], a[120:123], v[128:131]\n"
"v_cvt_pk_bf16_f32 v27, v14, v15\n"
"v_mfma_f32_16x16x32_bf16 v[128:131], a[60:63], a[124:127], v[128:131]\n"
"ds_write_b64 v2, v[26:27] offset:24584\n"
"v_mfma_f32_16x16x32_bf16 v[100:103], a[52:55], a[124:127], v[100:103]\n"
"ds_read_b128 a[124:127], v5 offset:7168\n"
"v_accvgpr_read_b32 v16, a228\n"
"v_mfma_f32_16x16x32_bf16 v[100:103], a[48:51], a[120:123], v[100:103]\n"
"ds_read_b128 a[120:123], v5 offset:6144\n"
"v_accvgpr_read_b32 v17, a229\n"
"v_mfma_f32_16x16x32_bf16 v[104:107], a[48:51], a[128:131], v[104:107]\n"
"v_accvgpr_read_b32 v18, a230\n"
"v_accvgpr_read_b32 v19, a231\n"
"v_mfma_f32_16x16x32_bf16 v[104:107], a[52:55], a[132:135], v[104:107]\n"
"global_load_dwordx4 a[40:43], v0, s[34:35] offset:-2048 sc1\n"
"v_lshlrev_b32 v12, 16, v16\n"
"v_mfma_f32_16x16x32_bf16 v[132:135], a[60:63], a[132:135], v[132:135]\n"
"ds_read_b128 a[132:135], v5 offset:9216\n"
"v_and_b32 v13, s63, v16\n"
"v_mfma_f32_16x16x32_bf16 v[132:135], a[56:59], a[128:131], v[132:135]\n"
"ds_read_b128 a[128:131], v5 offset:8192\n"
"v_lshlrev_b32 v14, 16, v17\n"
"v_mfma_f32_16x16x32_bf16 v[136:139], a[56:59], a[136:139], v[136:139]\n"
"v_and_b32 v15, s63, v17\n"
"v_mul_f32 v12, v12, v216\n"
"v_mfma_f32_16x16x32_bf16 v[136:139], a[60:63], a[140:143], v[136:139]\n"
"v_mul_f32 v13, v13, v217\n"
"v_mul_f32 v14, v14, v218\n"
"v_mfma_f32_16x16x32_bf16 v[108:111], a[52:55], a[140:143], v[108:111]\n"
"ds_read_b128 a[140:143], v5 offset:11264\n"
"v_mul_f32 v15, v15, v219\n"
"v_mfma_f32_16x16x32_bf16 v[108:111], a[48:51], a[136:139], v[108:111]\n"
"ds_read_b128 a[136:139], v5 offset:10240\n"
"v_cvt_pk_bf16_f32 v28, v12, v13\n"
"v_mfma_f32_16x16x32_bf16 v[112:115], a[48:51], a[144:147], v[112:115]\n"
"v_cvt_pk_bf16_f32 v29, v14, v15\n"
"v_lshlrev_b32 v12, 16, v18\n"
"v_mfma_f32_16x16x32_bf16 v[112:115], a[52:55], a[148:151], v[112:115]\n"
"global_load_dwordx4 a[44:47], v0, s[34:35] offset:-1024 sc1\n"
"v_and_b32 v13, s63, v18\n"
"v_mfma_f32_16x16x32_bf16 v[140:143], a[60:63], a[148:151], v[140:143]\n"
"ds_read_b128 a[148:151], v5 offset:13312\n"
"v_lshlrev_b32 v14, 16, v19\n"
"v_mfma_f32_16x16x32_bf16 v[140:143], a[56:59], a[144:147], v[140:143]\n"
"ds_read_b128 a[144:147], v5 offset:12288\n"
"v_and_b32 v15, s63, v19\n"
"v_mfma_f32_16x16x32_bf16 a[12:15], a[56:59], a[152:155], a[12:15]\n"
"v_mul_f32 v12, v12, v244\n"
"v_mul_f32 v13, v13, v245\n"
"v_mfma_f32_16x16x32_bf16 a[12:15], a[60:63], a[156:159], a[12:15]\n"
"v_mul_f32 v14, v14, v246\n"
"v_mul_f32 v15, v15, v247\n"
"v_mfma_f32_16x16x32_bf16 a[8:11], a[52:55], a[156:159], a[8:11]\n"
"ds_read_b128 a[156:159], v5 offset:15360\n"
"v_cvt_pk_bf16_f32 v30, v12, v13\n"
"v_mfma_f32_16x16x32_bf16 a[8:11], a[48:51], a[152:155], a[8:11]\n"
"s_waitcnt vmcnt(9) lgkmcnt(12)\n"
"ds_read_b128 a[152:155], v5 offset:14336\n"
"v_cvt_pk_bf16_f32 v31, v14, v15\n"
"v_mfma_f32_16x16x32_bf16 v[32:35], a[64:67], a[96:99], v[32:35]\n"
"global_store_dwordx4 v0, v[28:31], s[54:55] offset:-3072 sc1\n"
"ds_write_b128 v2, v[28:31] offset:25600\n"
"v_mfma_f32_16x16x32_bf16 v[32:35], a[68:71], a[100:103], v[32:35]\n"
"global_load_dwordx4 a[48:51], v0, s[34:35] offset:0 sc1\n"
"v_accvgpr_read_b32 v16, a232\n"
"v_mfma_f32_16x16x32_bf16 v[60:63], a[76:79], a[100:103], v[60:63]\n"
"v_accvgpr_read_b32 v17, a233\n"
"v_accvgpr_read_b32 v18, a234\n"
"v_mfma_f32_16x16x32_bf16 v[60:63], a[72:75], a[96:99], v[60:63]\n"
"v_accvgpr_read_b32 v19, a235\n"
"v_lshlrev_b32 v12, 16, v16\n"
"v_mfma_f32_16x16x32_bf16 v[64:67], a[72:75], a[104:107], v[64:67]\n"
"v_and_b32 v13, s63, v16\n"
"v_lshlrev_b32 v14, 16, v17\n"
"v_mfma_f32_16x16x32_bf16 v[64:67], a[76:79], a[108:111], v[64:67]\n"
"v_and_b32 v15, s63, v17\n"
"v_mul_f32 v12, v12, v164\n"
"v_mfma_f32_16x16x32_bf16 v[36:39], a[68:71], a[108:111], v[36:39]\n"
"v_mul_f32 v13, v13, v165\n"
"v_mul_f32 v14, v14, v166\n"
"v_mfma_f32_16x16x32_bf16 v[36:39], a[64:67], a[104:107], v[36:39]\n"
"s_waitcnt lgkmcnt(9)\n"
"v_mul_f32 v15, v15, v167\n"
"v_cvt_pk_bf16_f32 v24, v12, v13\n"
"v_mfma_f32_16x16x32_bf16 v[40:43], a[64:67], a[112:115], v[40:43]\n"
"v_cvt_pk_bf16_f32 v25, v14, v15\n"
"v_mfma_f32_16x16x32_bf16 v[40:43], a[68:71], a[116:119], v[40:43]\n"
"global_load_dwordx4 a[52:55], v0, s[34:35] offset:1024 sc1\n"
"v_mfma_f32_16x16x32_bf16 v[68:71], a[76:79], a[116:119], v[68:71]\n"
"ds_write_b64 v2, v[24:25] offset:26624\n"
"v_mfma_f32_16x16x32_bf16 v[68:71], a[72:75], a[112:115], v[68:71]\n"
"v_lshlrev_b32 v12, 16, v18\n"
"v_and_b32 v13, s63, v18\n"
"v_mfma_f32_16x16x32_bf16 v[72:75], a[72:75], a[120:123], v[72:75]\n"
"v_lshlrev_b32 v14, 16, v19\n"
"v_and_b32 v15, s63, v19\n"
"v_mfma_f32_16x16x32_bf16 v[72:75], a[76:79], a[124:127], v[72:75]\n"
"v_mul_f32 v12, v12, v192\n"
"v_mul_f32 v13, v13, v193\n"
"v_mfma_f32_16x16x32_bf16 v[44:47], a[68:71], a[124:127], v[44:47]\n"
"v_mul_f32 v14, v14, v194\n"
"v_mul_f32 v15, v15, v195\n"
"v_mfma_f32_16x16x32_bf16 v[44:47], a[64:67], a[120:123], v[44:47]\n"
"s_waitcnt lgkmcnt(6)\n"
"v_cvt_pk_bf16_f32 v26, v12, v13\n"
"v_cvt_pk_bf16_f32 v27, v14, v15\n"
"v_mfma_f32_16x16x32_bf16 v[48:51], a[64:67], a[128:131], v[48:51]\n"
"ds_write_b64 v2, v[26:27] offset:26632\n"
"v_mfma_f32_16x16x32_bf16 v[48:51], a[68:71], a[132:135], v[48:51]\n"
"global_load_dwordx4 a[56:59], v0, s[34:35] offset:2048 sc1\n"
"v_accvgpr_read_b32 v16, a236\n"
"v_mfma_f32_16x16x32_bf16 v[76:79], a[76:79], a[132:135], v[76:79]\n"
"v_accvgpr_read_b32 v17, a237\n"
"v_accvgpr_read_b32 v18, a238\n"
"v_mfma_f32_16x16x32_bf16 v[76:79], a[72:75], a[128:131], v[76:79]\n"
"v_accvgpr_read_b32 v19, a239\n"
"v_lshlrev_b32 v12, 16, v16\n"
"v_mfma_f32_16x16x32_bf16 v[80:83], a[72:75], a[136:139], v[80:83]\n"
"v_and_b32 v13, s63, v16\n"
"v_mfma_f32_16x16x32_bf16 v[80:83], a[76:79], a[140:143], v[80:83]\n"
"v_lshlrev_b32 v14, 16, v17\n"
"v_mfma_f32_16x16x32_bf16 v[52:55], a[68:71], a[140:143], v[52:55]\n"
"v_and_b32 v15, s63, v17\n"
"v_mul_f32 v12, v12, v220\n"
"v_mfma_f32_16x16x32_bf16 v[52:55], a[64:67], a[136:139], v[52:55]\n"
"s_waitcnt lgkmcnt(3)\n"
"v_mul_f32 v13, v13, v221\n"
"v_mfma_f32_16x16x32_bf16 v[56:59], a[64:67], a[144:147], v[56:59]\n"
"v_mul_f32 v14, v14, v222\n"
"v_mul_f32 v15, v15, v223\n"
"v_mfma_f32_16x16x32_bf16 v[56:59], a[68:71], a[148:151], v[56:59]\n"
"global_load_dwordx4 a[60:63], v0, s[34:35] offset:3072 sc1\n"
"v_cvt_pk_bf16_f32 v28, v12, v13\n"
"v_mfma_f32_16x16x32_bf16 v[84:87], a[76:79], a[148:151], v[84:87]\n"
"v_cvt_pk_bf16_f32 v29, v14, v15\n"
"v_mfma_f32_16x16x32_bf16 v[84:87], a[72:75], a[144:147], v[84:87]\n"
"v_lshlrev_b32 v12, 16, v18\n"
"v_and_b32 v13, s63, v18\n"
"v_mfma_f32_16x16x32_bf16 a[4:7], a[72:75], a[152:155], a[4:7]\n"
"s_add_u32 s34, s34, 8192\n"
"v_lshlrev_b32 v14, 16, v19\n"
"v_and_b32 v15, s63, v19\n"
"v_mfma_f32_16x16x32_bf16 a[4:7], a[76:79], a[156:159], a[4:7]\n"
"v_mul_f32 v12, v12, v248\n"
"v_mul_f32 v13, v13, v249\n"
"v_mfma_f32_16x16x32_bf16 a[0:3], a[68:71], a[156:159], a[0:3]\n"
"v_mul_f32 v14, v14, v250\n"
"v_mul_f32 v15, v15, v251\n"
"v_mfma_f32_16x16x32_bf16 a[0:3], a[64:67], a[152:155], a[0:3]\n"
"s_waitcnt vmcnt(9)\n"
"v_cvt_pk_bf16_f32 v30, v12, v13\n"
"v_cvt_pk_bf16_f32 v31, v14, v15\n"
"v_mfma_f32_16x16x32_bf16 v[88:91], a[80:83], a[96:99], v[88:91]\n"
"global_store_dwordx4 v0, v[28:31], s[54:55] offset:-1024 sc1\n"
"ds_write_b128 v2, v[28:31] offset:27648\n"
"v_mfma_f32_16x16x32_bf16 v[88:91], a[84:87], a[100:103], v[88:91]\n"
"global_load_dwordx4 a[64:67], v0, s[34:35] offset:-4096 sc1\n"
"v_mfma_f32_16x16x32_bf16 v[116:119], a[92:95], a[100:103], v[116:119]\n"
"ds_read_b128 a[100:103], v2 offset:1024\n"
"v_mfma_f32_16x16x32_bf16 v[116:119], a[88:91], a[96:99], v[116:119]\n"
"ds_read_b128 a[96:99], v2 offset:0\n"
"v_accvgpr_read_b32 v16, a240\n"
"v_mfma_f32_16x16x32_bf16 v[120:123], a[88:91], a[104:107], v[120:123]\n"
"v_accvgpr_read_b32 v17, a241\n"
"v_accvgpr_read_b32 v18, a242\n"
"v_mfma_f32_16x16x32_bf16 v[120:123], a[92:95], a[108:111], v[120:123]\n"
"v_accvgpr_read_b32 v19, a243\n"
"v_lshlrev_b32 v12, 16, v16\n"
"v_mfma_f32_16x16x32_bf16 v[92:95], a[84:87], a[108:111], v[92:95]\n"
"ds_read_b128 a[108:111], v2 offset:3072\n"
"v_and_b32 v13, s63, v16\n"
"v_mfma_f32_16x16x32_bf16 v[92:95], a[80:83], a[104:107], v[92:95]\n"
"ds_read_b128 a[104:107], v2 offset:2048\n"
"v_lshlrev_b32 v14, 16, v17\n"
"v_mfma_f32_16x16x32_bf16 v[96:99], a[80:83], a[112:115], v[96:99]\n"
"v_and_b32 v15, s63, v17\n"
"v_mul_f32 v12, v12, v168\n"
"v_mfma_f32_16x16x32_bf16 v[96:99], a[84:87], a[116:119], v[96:99]\n"
"global_load_dwordx4 a[68:71], v0, s[34:35] offset:-3072 sc1\n"
"v_mul_f32 v13, v13, v169\n"
"v_mfma_f32_16x16x32_bf16 v[124:127], a[92:95], a[116:119], v[124:127]\n"
"ds_read_b128 a[116:119], v2 offset:5120\n"
"v_mul_f32 v14, v14, v170\n"
"v_mfma_f32_16x16x32_bf16 v[124:127], a[88:91], a[112:115], v[124:127]\n"
"ds_read_b128 a[112:115], v2 offset:4096\n"
"v_mul_f32 v15, v15, v171\n"
"v_mfma_f32_16x16x32_bf16 v[128:131], a[88:91], a[120:123], v[128:131]\n"
"v_cvt_pk_bf16_f32 v24, v12, v13\n"
"v_cvt_pk_bf16_f32 v25, v14, v15\n"
"v_mfma_f32_16x16x32_bf16 v[128:131], a[92:95], a[124:127], v[128:131]\n"
"v_lshlrev_b32 v12, 16, v18\n"
"v_and_b32 v13, s63, v18\n"
"v_mfma_f32_16x16x32_bf16 v[100:103], a[84:87], a[124:127], v[100:103]\n"
"ds_read_b128 a[124:127], v2 offset:7168\n"
"v_lshlrev_b32 v14, 16, v19\n"
"v_mfma_f32_16x16x32_bf16 v[100:103], a[80:83], a[120:123], v[100:103]\n"
"ds_read_b128 a[120:123], v2 offset:6144\n"
"v_mov_b32 v8, s65\n"
"v_mfma_f32_16x16x32_bf16 v[104:107], a[80:83], a[128:131], v[104:107]\n"
"v_mov_b32 v9, s60\n"
"v_and_b32 v15, s63, v19\n"
"v_mfma_f32_16x16x32_bf16 v[104:107], a[84:87], a[132:135], v[104:107]\n"
"global_load_dwordx4 a[72:75], v0, s[34:35] offset:-2048 sc1\n"
"v_mul_f32 v12, v12, v196\n"
"v_mfma_f32_16x16x32_bf16 v[132:135], a[92:95], a[132:135], v[132:135]\n"
"buffer_store_dwordx2 v[8:9], off, s[56:59], %13 sc1\n"
"ds_read_b128 a[132:135], v2 offset:9216\n"
"v_mfma_f32_16x16x32_bf16 v[132:135], a[88:91], a[128:131], v[132:135]\n"
"ds_read_b128 a[128:131], v2 offset:8192\n"
"v_mul_f32 v13, v13, v197\n"
"v_mfma_f32_16x16x32_bf16 v[136:139], a[88:91], a[136:139], v[136:139]\n"
"v_mul_f32 v14, v14, v198\n"
"v_mul_f32 v15, v15, v199\n"
"v_mfma_f32_16x16x32_bf16 v[136:139], a[92:95], a[140:143], v[136:139]\n"
"v_cvt_pk_bf16_f32 v26, v12, v13\n"
"v_cvt_pk_bf16_f32 v27, v14, v15\n"
"v_mfma_f32_16x16x32_bf16 v[108:111], a[84:87], a[140:143], v[108:111]\n"
"ds_read_b128 a[140:143], v2 offset:11264\n"
"v_mfma_f32_16x16x32_bf16 v[108:111], a[80:83], a[136:139], v[108:111]\n"
"ds_read_b128 a[136:139], v2 offset:10240\n"
"v_mfma_f32_16x16x32_bf16 v[112:115], a[80:83], a[144:147], v[112:115]\n"
"global_store_dwordx4 v0, v[24:27], s[54:55] offset:0 sc1\n"
"ds_write_b128 v2, v[24:27] offset:28672\n"
"v_mfma_f32_16x16x32_bf16 v[112:115], a[84:87], a[148:151], v[112:115]\n"
"global_load_dwordx4 a[76:79], v0, s[34:35] offset:-1024 sc1\n"
"v_accvgpr_read_b32 v16, a248\n"
"v_mfma_f32_16x16x32_bf16 v[140:143], a[92:95], a[148:151], v[140:143]\n"
"ds_read_b128 a[148:151], v2 offset:13312\n"
"v_accvgpr_read_b32 v17, a249\n"
"v_mfma_f32_16x16x32_bf16 v[140:143], a[88:91], a[144:147], v[140:143]\n"
"ds_read_b128 a[144:147], v2 offset:12288\n"
"v_accvgpr_read_b32 v18, a250\n"
"v_mfma_f32_16x16x32_bf16 a[12:15], a[88:91], a[152:155], a[12:15]\n"
"v_accvgpr_read_b32 v19, a251\n"
"v_lshlrev_b32 v12, 16, v16\n"
"v_mfma_f32_16x16x32_bf16 a[12:15], a[92:95], a[156:159], a[12:15]\n"
"v_and_b32 v13, s63, v16\n"
"v_lshlrev_b32 v14, 16, v17\n"
"v_mfma_f32_16x16x32_bf16 a[8:11], a[84:87], a[156:159], a[8:11]\n"
"ds_read_b128 a[156:159], v2 offset:15360\n"
"v_and_b32 v15, s63, v17\n"
"v_mfma_f32_16x16x32_bf16 a[8:11], a[80:83], a[152:155], a[8:11]\n"
"s_waitcnt vmcnt(12) lgkmcnt(12)\n"
"ds_read_b128 a[152:155], v2 offset:14336\n"
"v_accvgpr_read_b32 v10, a16\n"
"v_mfma_f32_16x16x32_bf16 v[144:147], a[32:35], a[96:99], v[144:147]\n"
"L_st5700:\n"
"s_cbranch_execz L_st7943\n"
"v_accvgpr_read_b32 v11, a17\n"
"v_mul_f32 v12, v12, v10\n"
"v_mfma_f32_16x16x32_bf16 v[144:147], a[36:39], a[100:103], v[144:147]\n"
"global_load_dwordx4 a[80:83], v0, s[34:35] offset:0 sc1\n"
"v_mul_f32 v13, v13, v11\n"
"v_mfma_f32_16x16x32_bf16 v[172:175], a[44:47], a[100:103], v[172:175]\n"
"v_accvgpr_read_b32 v10, a18\n"
"v_accvgpr_read_b32 v11, a19\n"
"v_mfma_f32_16x16x32_bf16 v[172:175], a[40:43], a[96:99], v[172:175]\n"
"v_mul_f32 v14, v14, v10\n"
"v_mul_f32 v15, v15, v11\n"
"v_mfma_f32_16x16x32_bf16 v[176:179], a[40:43], a[104:107], v[176:179]\n"
"v_cvt_pk_bf16_f32 v28, v12, v13\n"
"v_cvt_pk_bf16_f32 v29, v14, v15\n"
"v_mfma_f32_16x16x32_bf16 v[176:179], a[44:47], a[108:111], v[176:179]\n"
"v_lshlrev_b32 v12, 16, v18\n"
"v_and_b32 v13, s63, v18\n"
"v_mfma_f32_16x16x32_bf16 v[148:151], a[36:39], a[108:111], v[148:151]\n"
"v_lshlrev_b32 v14, 16, v19\n"
"v_and_b32 v15, s63, v19\n"
"v_mfma_f32_16x16x32_bf16 v[148:151], a[32:35], a[104:107], v[148:151]\n"
"s_waitcnt lgkmcnt(9)\n"
"v_accvgpr_read_b32 v10, a20\n"
"v_accvgpr_read_b32 v11, a21\n"
"v_mfma_f32_16x16x32_bf16 v[152:155], a[32:35], a[112:115], v[152:155]\n"
"v_mul_f32 v12, v12, v10\n"
"v_mul_f32 v13, v13, v11\n"
"v_mfma_f32_16x16x32_bf16 v[152:155], a[36:39], a[116:119], v[152:155]\n"
"global_load_dwordx4 a[84:87], v0, s[34:35] offset:1024 sc1\n"
"v_accvgpr_read_b32 v10, a22\n"
"v_mfma_f32_16x16x32_bf16 v[180:183], a[44:47], a[116:119], v[180:183]\n"
"v_accvgpr_read_b32 v11, a23\n"
"v_mfma_f32_16x16x32_bf16 v[180:183], a[40:43], a[112:115], v[180:183]\n"
"v_mul_f32 v14, v14, v10\n"
"v_mul_f32 v15, v15, v11\n"
"v_mfma_f32_16x16x32_bf16 v[184:187], a[40:43], a[120:123], v[184:187]\n"
"v_cvt_pk_bf16_f32 v30, v12, v13\n"
"v_mfma_f32_16x16x32_bf16 v[184:187], a[44:47], a[124:127], v[184:187]\n"
"v_cvt_pk_bf16_f32 v31, v14, v15\n"
"v_mfma_f32_16x16x32_bf16 v[156:159], a[36:39], a[124:127], v[156:159]\n"
"global_store_dwordx4 v0, v[28:31], s[54:55] offset:2048 sc1\n"
"ds_write_b128 v2, v[28:31] offset:30720\n"
"v_mfma_f32_16x16x32_bf16 v[156:159], a[32:35], a[120:123], v[156:159]\n"
"s_waitcnt lgkmcnt(6)\n"
"v_accvgpr_read_b32 v16, a244\n"
"v_mfma_f32_16x16x32_bf16 v[160:163], a[32:35], a[128:131], v[160:163]\n"
"v_accvgpr_read_b32 v17, a245\n"
"v_accvgpr_read_b32 v18, a246\n"
"v_mfma_f32_16x16x32_bf16 v[160:163], a[36:39], a[132:135], v[160:163]\n"
"global_load_dwordx4 a[88:91], v0, s[34:35] offset:2048 sc1\n"
"v_accvgpr_read_b32 v19, a247\n"
"v_mfma_f32_16x16x32_bf16 v[188:191], a[44:47], a[132:135], v[188:191]\n"
"v_lshlrev_b32 v12, 16, v16\n"
"v_mfma_f32_16x16x32_bf16 v[188:191], a[40:43], a[128:131], v[188:191]\n"
"v_and_b32 v13, s63, v16\n"
"v_lshlrev_b32 v14, 16, v17\n"
"v_mfma_f32_16x16x32_bf16 v[192:195], a[40:43], a[136:139], v[192:195]\n"
"v_and_b32 v15, s63, v17\n"
"v_mfma_f32_16x16x32_bf16 v[192:195], a[44:47], a[140:143], v[192:195]\n"
"v_mul_f32 v12, v12, v224\n"
"v_mul_f32 v13, v13, v225\n"
"v_mfma_f32_16x16x32_bf16 v[164:167], a[36:39], a[140:143], v[164:167]\n"
"v_mul_f32 v14, v14, v226\n"
"v_mul_f32 v15, v15, v227\n"
"v_mfma_f32_16x16x32_bf16 v[164:167], a[32:35], a[136:139], v[164:167]\n"
"s_waitcnt lgkmcnt(1)\n"
"v_cvt_pk_bf16_f32 v24, v12, v13\n"
"v_cvt_pk_bf16_f32 v25, v14, v15\n"
"v_mfma_f32_16x16x32_bf16 v[168:171], a[32:35], a[144:147], v[168:171]\n"
"ds_write_b64 v2, v[24:25] offset:29696\n"
"v_mfma_f32_16x16x32_bf16 v[168:171], a[36:39], a[148:151], v[168:171]\n"
"global_load_dwordx4 a[92:95], v0, s[34:35] offset:3072 sc1\n"
"v_mfma_f32_16x16x32_bf16 v[196:199], a[44:47], a[148:151], v[196:199]\n"
"v_lshlrev_b32 v12, 16, v18\n"
"v_and_b32 v13, s63, v18\n"
"v_mfma_f32_16x16x32_bf16 v[196:199], a[40:43], a[144:147], v[196:199]\n"
"v_lshlrev_b32 v14, 16, v19\n"
"v_and_b32 v15, s63, v19\n"
"v_mfma_f32_16x16x32_bf16 a[20:23], a[40:43], a[152:155], a[20:23]\n"
"s_add_u32 s34, s34, 8192\n"
"v_mul_f32 v12, v12, v252\n"
"v_mul_f32 v13, v13, v253\n"
"v_mfma_f32_16x16x32_bf16 a[20:23], a[44:47], a[156:159], a[20:23]\n"
"v_mul_f32 v14, v14, v254\n"
"v_mul_f32 v15, v15, v255\n"
"v_mfma_f32_16x16x32_bf16 a[16:19], a[36:39], a[156:159], a[16:19]\n"
"v_cvt_pk_bf16_f32 v26, v12, v13\n"
"v_cvt_pk_bf16_f32 v27, v14, v15\n"
"v_mfma_f32_16x16x32_bf16 a[16:19], a[32:35], a[152:155], a[16:19]\n"
"s_waitcnt vmcnt(12)\n"
"ds_write_b64 v2, v[26:27] offset:29704\n"
"v_mfma_f32_16x16x32_bf16 v[200:203], a[48:51], a[96:99], v[200:203]\n"
"v_accvgpr_read_b32 v16, a252\n"
"v_mfma_f32_16x16x32_bf16 v[200:203], a[52:55], a[100:103], v[200:203]\n"
"global_load_dwordx4 a[32:35], v0, s[34:35] offset:-4096 sc1\n"
"v_accvgpr_read_b32 v17, a253\n"
"v_mfma_f32_16x16x32_bf16 v[228:231], a[60:63], a[100:103], v[228:231]\n"
"ds_read_b128 a[100:103], v3 offset:1024\n"
"v_accvgpr_read_b32 v18, a254\n"
"v_mfma_f32_16x16x32_bf16 v[228:231], a[56:59], a[96:99], v[228:231]\n"
"ds_read_b128 a[96:99], v3 offset:0\n"
"v_accvgpr_read_b32 v19, a255\n"
"v_mfma_f32_16x16x32_bf16 v[232:235], a[56:59], a[104:107], v[232:235]\n"
"v_lshlrev_b32 v12, 16, v16\n"
"v_and_b32 v13, s63, v16\n"
"v_mfma_f32_16x16x32_bf16 v[232:235], a[60:63], a[108:111], v[232:235]\n"
"v_lshlrev_b32 v14, 16, v17\n"
"v_and_b32 v15, s63, v17\n"
"v_mfma_f32_16x16x32_bf16 v[204:207], a[52:55], a[108:111], v[204:207]\n"
"ds_read_b128 a[108:111], v3 offset:3072\n"
"v_accvgpr_read_b32 v10, a24\n"
"v_mfma_f32_16x16x32_bf16 v[204:207], a[48:51], a[104:107], v[204:207]\n"
"ds_read_b128 a[104:107], v3 offset:2048\n"
"v_accvgpr_read_b32 v11, a25\n"
"v_mfma_f32_16x16x32_bf16 v[208:211], a[48:51], a[112:115], v[208:211]\n"
"v_mul_f32 v12, v12, v10\n"
"v_mul_f32 v13, v13, v11\n"
"v_mfma_f32_16x16x32_bf16 v[208:211], a[52:55], a[116:119], v[208:211]\n"
"global_load_dwordx4 a[36:39], v0, s[34:35] offset:-3072 sc1\n"
"v_accvgpr_read_b32 v10, a26\n"
"v_mfma_f32_16x16x32_bf16 v[236:239], a[60:63], a[116:119], v[236:239]\n"
"ds_read_b128 a[116:119], v3 offset:5120\n"
"v_accvgpr_read_b32 v11, a27\n"
"v_mfma_f32_16x16x32_bf16 v[236:239], a[56:59], a[112:115], v[236:239]\n"
"ds_read_b128 a[112:115], v3 offset:4096\n"
"v_mul_f32 v14, v14, v10\n"
"v_mfma_f32_16x16x32_bf16 v[240:243], a[56:59], a[120:123], v[240:243]\n"
"v_mul_f32 v15, v15, v11\n"
"v_cvt_pk_bf16_f32 v28, v12, v13\n"
"v_mfma_f32_16x16x32_bf16 v[240:243], a[60:63], a[124:127], v[240:243]\n"
"v_cvt_pk_bf16_f32 v29, v14, v15\n"
"v_lshlrev_b32 v12, 16, v18\n"
"v_mfma_f32_16x16x32_bf16 v[212:215], a[52:55], a[124:127], v[212:215]\n"
"ds_read_b128 a[124:127], v3 offset:7168\n"
"v_and_b32 v13, s63, v18\n"
"v_mfma_f32_16x16x32_bf16 v[212:215], a[48:51], a[120:123], v[212:215]\n"
"ds_read_b128 a[120:123], v3 offset:6144\n"
"v_lshlrev_b32 v14, 16, v19\n"
"v_mfma_f32_16x16x32_bf16 v[216:219], a[48:51], a[128:131], v[216:219]\n"
"v_and_b32 v15, s63, v19\n"
"v_accvgpr_read_b32 v10, a28\n"
"v_mfma_f32_16x16x32_bf16 v[216:219], a[52:55], a[132:135], v[216:219]\n"
"global_load_dwordx4 a[40:43], v0, s[34:35] offset:-2048 sc1\n"
"v_accvgpr_read_b32 v11, a29\n"
"v_mfma_f32_16x16x32_bf16 v[244:247], a[60:63], a[132:135], v[244:247]\n"
"ds_read_b128 a[132:135], v3 offset:9216\n"
"v_mul_f32 v12, v12, v10\n"
"v_mfma_f32_16x16x32_bf16 v[244:247], a[56:59], a[128:131], v[244:247]\n"
"ds_read_b128 a[128:131], v3 offset:8192\n"
"v_mul_f32 v13, v13, v11\n"
"v_mfma_f32_16x16x32_bf16 v[248:251], a[56:59], a[136:139], v[248:251]\n"
"v_accvgpr_read_b32 v10, a30\n"
"v_accvgpr_read_b32 v11, a31\n"
"v_mfma_f32_16x16x32_bf16 v[248:251], a[60:63], a[140:143], v[248:251]\n"
"v_mul_f32 v14, v14, v10\n"
"v_mul_f32 v15, v15, v11\n"
"v_mfma_f32_16x16x32_bf16 v[220:223], a[52:55], a[140:143], v[220:223]\n"
"ds_read_b128 a[140:143], v3 offset:11264\n"
"v_cvt_pk_bf16_f32 v30, v12, v13\n"
"v_mfma_f32_16x16x32_bf16 v[220:223], a[48:51], a[136:139], v[220:223]\n"
"ds_read_b128 a[136:139], v3 offset:10240\n"
"v_cvt_pk_bf16_f32 v31, v14, v15\n"
"v_mfma_f32_16x16x32_bf16 v[224:227], a[48:51], a[144:147], v[224:227]\n"
"global_store_dwordx4 v0, v[28:31], s[54:55] offset:3072 sc1\n"
"ds_write_b128 v2, v[28:31] offset:31744\n"
"v_mfma_f32_16x16x32_bf16 v[224:227], a[52:55], a[148:151], v[224:227]\n"
"global_load_dwordx4 a[44:47], v0, s[34:35] offset:-1024 sc1\n"
"v_mfma_f32_16x16x32_bf16 v[252:255], a[60:63], a[148:151], v[252:255]\n"
"ds_read_b128 a[148:151], v3 offset:13312\n"
"v_mfma_f32_16x16x32_bf16 v[252:255], a[56:59], a[144:147], v[252:255]\n"
"ds_read_b128 a[144:147], v3 offset:12288\n"
"v_mfma_f32_16x16x32_bf16 a[28:31], a[56:59], a[152:155], a[28:31]\n"
"v_mfma_f32_16x16x32_bf16 a[28:31], a[60:63], a[156:159], a[28:31]\n"
"v_mfma_f32_16x16x32_bf16 a[24:27], a[52:55], a[156:159], a[24:27]\n"
"ds_read_b128 a[156:159], v3 offset:15360\n"
"v_mfma_f32_16x16x32_bf16 a[24:27], a[48:51], a[152:155], a[24:27]\n"
"s_waitcnt vmcnt(10) lgkmcnt(12)\n"
"ds_read_b128 a[152:155], v3 offset:14336\n"
"v_mfma_f32_16x16x32_bf16 v[144:147], a[64:67], a[96:99], v[144:147]\n"
"ds_read_b128 v[24:27], v2 offset:16384\n"
"v_mfma_f32_16x16x32_bf16 v[144:147], a[68:71], a[100:103], v[144:147]\n"
"global_load_dwordx4 a[48:51], v0, s[34:35] offset:0 sc1\n"
"v_mfma_f32_16x16x32_bf16 v[172:175], a[76:79], a[100:103], v[172:175]\n"
"v_mfma_f32_16x16x32_bf16 v[172:175], a[72:75], a[96:99], v[172:175]\n"
"v_mfma_f32_16x16x32_bf16 v[176:179], a[72:75], a[104:107], v[176:179]\n"
"v_mfma_f32_16x16x32_bf16 v[176:179], a[76:79], a[108:111], v[176:179]\n"
"v_mfma_f32_16x16x32_bf16 v[148:151], a[68:71], a[108:111], v[148:151]\n"
"v_mfma_f32_16x16x32_bf16 v[148:151], a[64:67], a[104:107], v[148:151]\n"
"s_waitcnt lgkmcnt(10)\n"
"v_mfma_f32_16x16x32_bf16 v[152:155], a[64:67], a[112:115], v[152:155]\n"
"v_mfma_f32_16x16x32_bf16 v[152:155], a[68:71], a[116:119], v[152:155]\n"
"global_load_dwordx4 a[52:55], v0, s[34:35] offset:1024 sc1\n"
"v_mfma_f32_16x16x32_bf16 v[180:183], a[76:79], a[116:119], v[180:183]\n"
"v_mfma_f32_16x16x32_bf16 v[180:183], a[72:75], a[112:115], v[180:183]\n"
"v_mfma_f32_16x16x32_bf16 v[184:187], a[72:75], a[120:123], v[184:187]\n"
"s_waitcnt lgkmcnt(0)\n"
"global_store_dwordx4 v0, v[24:27], s[52:53] offset:-4096 sc1\n"
"v_mfma_f32_16x16x32_bf16 v[184:187], a[76:79], a[124:127], v[184:187]\n"
"v_mfma_f32_16x16x32_bf16 v[156:159], a[68:71], a[124:127], v[156:159]\n"
"v_mfma_f32_16x16x32_bf16 v[156:159], a[64:67], a[120:123], v[156:159]\n"
"v_mfma_f32_16x16x32_bf16 v[160:163], a[64:67], a[128:131], v[160:163]\n"
"v_mfma_f32_16x16x32_bf16 v[160:163], a[68:71], a[132:135], v[160:163]\n"
"global_load_dwordx4 a[56:59], v0, s[34:35] offset:2048 sc1\n"
"v_mfma_f32_16x16x32_bf16 v[188:191], a[76:79], a[132:135], v[188:191]\n"
"v_mfma_f32_16x16x32_bf16 v[188:191], a[72:75], a[128:131], v[188:191]\n"
"v_mfma_f32_16x16x32_bf16 v[192:195], a[72:75], a[136:139], v[192:195]\n"
"v_mfma_f32_16x16x32_bf16 v[192:195], a[76:79], a[140:143], v[192:195]\n"
"v_mfma_f32_16x16x32_bf16 v[164:167], a[68:71], a[140:143], v[164:167]\n"
"v_mfma_f32_16x16x32_bf16 v[164:167], a[64:67], a[136:139], v[164:167]\n"
"v_mfma_f32_16x16x32_bf16 v[168:171], a[64:67], a[144:147], v[168:171]\n"
"v_mfma_f32_16x16x32_bf16 v[168:171], a[68:71], a[148:151], v[168:171]\n"
"global_load_dwordx4 a[60:63], v0, s[34:35] offset:3072 sc1\n"
"v_mfma_f32_16x16x32_bf16 v[196:199], a[76:79], a[148:151], v[196:199]\n"
"v_mfma_f32_16x16x32_bf16 v[196:199], a[72:75], a[144:147], v[196:199]\n"
"v_mfma_f32_16x16x32_bf16 a[20:23], a[72:75], a[152:155], a[20:23]\n"
"s_add_u32 s34, s34, 8192\n"
"v_mfma_f32_16x16x32_bf16 a[20:23], a[76:79], a[156:159], a[20:23]\n"
"v_mfma_f32_16x16x32_bf16 a[16:19], a[68:71], a[156:159], a[16:19]\n"
"v_mfma_f32_16x16x32_bf16 a[16:19], a[64:67], a[152:155], a[16:19]\n"
"s_waitcnt vmcnt(10)\n"
"ds_read_b128 v[28:31], v2 offset:17408\n"
"v_mfma_f32_16x16x32_bf16 v[200:203], a[80:83], a[96:99], v[200:203]\n"
"v_mfma_f32_16x16x32_bf16 v[200:203], a[84:87], a[100:103], v[200:203]\n"
"global_load_dwordx4 a[64:67], v0, s[34:35] offset:-4096 sc1\n"
"v_mfma_f32_16x16x32_bf16 v[228:231], a[92:95], a[100:103], v[228:231]\n"
"ds_read_b128 a[100:103], v4 offset:1024\n"
"v_mfma_f32_16x16x32_bf16 v[228:231], a[88:91], a[96:99], v[228:231]\n"
"ds_read_b128 a[96:99], v4 offset:0\n"
"v_mfma_f32_16x16x32_bf16 v[232:235], a[88:91], a[104:107], v[232:235]\n"
"v_mfma_f32_16x16x32_bf16 v[232:235], a[92:95], a[108:111], v[232:235]\n"
"v_mfma_f32_16x16x32_bf16 v[204:207], a[84:87], a[108:111], v[204:207]\n"
"ds_read_b128 a[108:111], v4 offset:3072\n"
"v_mfma_f32_16x16x32_bf16 v[204:207], a[80:83], a[104:107], v[204:207]\n"
"ds_read_b128 a[104:107], v4 offset:2048\n"
"v_mfma_f32_16x16x32_bf16 v[208:211], a[80:83], a[112:115], v[208:211]\n"
"v_mfma_f32_16x16x32_bf16 v[208:211], a[84:87], a[116:119], v[208:211]\n"
"global_load_dwordx4 a[68:71], v0, s[34:35] offset:-3072 sc1\n"
"v_mfma_f32_16x16x32_bf16 v[236:239], a[92:95], a[116:119], v[236:239]\n"
"s_waitcnt lgkmcnt(4)\n"
"global_store_dwordx4 v0, v[28:31], s[52:53] offset:-3072 sc1\n"
"ds_read_b128 a[116:119], v4 offset:5120\n"
"v_mfma_f32_16x16x32_bf16 v[236:239], a[88:91], a[112:115], v[236:239]\n"
"ds_read_b128 a[112:115], v4 offset:4096\n"
"v_mfma_f32_16x16x32_bf16 v[240:243], a[88:91], a[120:123], v[240:243]\n"
"v_mfma_f32_16x16x32_bf16 v[240:243], a[92:95], a[124:127], v[240:243]\n"
"v_mfma_f32_16x16x32_bf16 v[212:215], a[84:87], a[124:127], v[212:215]\n"
"ds_read_b128 a[124:127], v4 offset:7168\n"
"v_mfma_f32_16x16x32_bf16 v[212:215], a[80:83], a[120:123], v[212:215]\n"
"ds_read_b128 a[120:123], v4 offset:6144\n"
"v_mfma_f32_16x16x32_bf16 v[216:219], a[80:83], a[128:131], v[216:219]\n"
"v_mfma_f32_16x16x32_bf16 v[216:219], a[84:87], a[132:135], v[216:219]\n"
"global_load_dwordx4 a[72:75], v0, s[34:35] offset:-2048 sc1\n"
"v_mfma_f32_16x16x32_bf16 v[244:247], a[92:95], a[132:135], v[244:247]\n"
"ds_read_b128 a[132:135], v4 offset:9216\n"
"v_mfma_f32_16x16x32_bf16 v[244:247], a[88:91], a[128:131], v[244:247]\n"
"ds_read_b128 a[128:131], v4 offset:8192\n"
"v_mfma_f32_16x16x32_bf16 v[248:251], a[88:91], a[136:139], v[248:251]\n"
"v_mfma_f32_16x16x32_bf16 v[248:251], a[92:95], a[140:143], v[248:251]\n"
"v_mfma_f32_16x16x32_bf16 v[220:223], a[84:87], a[140:143], v[220:223]\n"
"ds_read_b128 a[140:143], v4 offset:11264\n"
"v_mfma_f32_16x16x32_bf16 v[220:223], a[80:83], a[136:139], v[220:223]\n"
"ds_read_b128 a[136:139], v4 offset:10240\n"
"v_mfma_f32_16x16x32_bf16 v[224:227], a[80:83], a[144:147], v[224:227]\n"
"v_mfma_f32_16x16x32_bf16 v[224:227], a[84:87], a[148:151], v[224:227]\n"
"global_load_dwordx4 a[76:79], v0, s[34:35] offset:-1024 sc1\n"
"v_mfma_f32_16x16x32_bf16 v[252:255], a[92:95], a[148:151], v[252:255]\n"
"ds_read_b128 a[148:151], v4 offset:13312\n"
"v_mfma_f32_16x16x32_bf16 v[252:255], a[88:91], a[144:147], v[252:255]\n"
"ds_read_b128 a[144:147], v4 offset:12288\n"
"v_mfma_f32_16x16x32_bf16 a[28:31], a[88:91], a[152:155], a[28:31]\n"
"v_mfma_f32_16x16x32_bf16 a[28:31], a[92:95], a[156:159], a[28:31]\n"
"ds_read_b128 v[24:27], v2 offset:18432\n"
"v_mfma_f32_16x16x32_bf16 a[24:27], a[84:87], a[156:159], a[24:27]\n"
"ds_read_b128 a[156:159], v4 offset:15360\n"
"v_mfma_f32_16x16x32_bf16 a[24:27], a[80:83], a[152:155], a[24:27]\n"
"s_waitcnt vmcnt(10) lgkmcnt(12)\n"
"ds_read_b128 a[152:155], v4 offset:14336\n"
"v_mfma_f32_16x16x32_bf16 v[144:147], a[32:35], a[96:99], v[144:147]\n"
"v_mfma_f32_16x16x32_bf16 v[144:147], a[36:39], a[100:103], v[144:147]\n"
"global_load_dwordx4 a[80:83], v0, s[34:35] offset:0 sc1\n"
"v_mfma_f32_16x16x32_bf16 v[172:175], a[44:47], a[100:103], v[172:175]\n"
"v_mfma_f32_16x16x32_bf16 v[172:175], a[40:43], a[96:99], v[172:175]\n"
"v_mfma_f32_16x16x32_bf16 v[176:179], a[40:43], a[104:107], v[176:179]\n"
"v_mfma_f32_16x16x32_bf16 v[176:179], a[44:47], a[108:111], v[176:179]\n"
"v_mfma_f32_16x16x32_bf16 v[148:151], a[36:39], a[108:111], v[148:151]\n"
"v_mfma_f32_16x16x32_bf16 v[148:151], a[32:35], a[104:107], v[148:151]\n"
"s_waitcnt lgkmcnt(9)\n"
"v_mfma_f32_16x16x32_bf16 v[152:155], a[32:35], a[112:115], v[152:155]\n"
"s_waitcnt lgkmcnt(2)\n"
"global_store_dwordx4 v0, v[24:27], s[52:53] offset:-2048 sc1\n"
"v_mfma_f32_16x16x32_bf16 v[152:155], a[36:39], a[116:119], v[152:155]\n"
"global_load_dwordx4 a[84:87], v0, s[34:35] offset:1024 sc1\n"
"v_mfma_f32_16x16x32_bf16 v[180:183], a[44:47], a[116:119], v[180:183]\n"
"v_mfma_f32_16x16x32_bf16 v[180:183], a[40:43], a[112:115], v[180:183]\n"
"v_mfma_f32_16x16x32_bf16 v[184:187], a[40:43], a[120:123], v[184:187]\n"
"v_mfma_f32_16x16x32_bf16 v[184:187], a[44:47], a[124:127], v[184:187]\n"
"v_mfma_f32_16x16x32_bf16 v[156:159], a[36:39], a[124:127], v[156:159]\n"
"v_mfma_f32_16x16x32_bf16 v[156:159], a[32:35], a[120:123], v[156:159]\n"
"v_mfma_f32_16x16x32_bf16 v[160:163], a[32:35], a[128:131], v[160:163]\n"
"v_mfma_f32_16x16x32_bf16 v[160:163], a[36:39], a[132:135], v[160:163]\n"
"global_load_dwordx4 a[88:91], v0, s[34:35] offset:2048 sc1\n"
"v_mfma_f32_16x16x32_bf16 v[188:191], a[44:47], a[132:135], v[188:191]\n"
"v_mfma_f32_16x16x32_bf16 v[188:191], a[40:43], a[128:131], v[188:191]\n"
"v_mfma_f32_16x16x32_bf16 v[192:195], a[40:43], a[136:139], v[192:195]\n"
"v_mfma_f32_16x16x32_bf16 v[192:195], a[44:47], a[140:143], v[192:195]\n"
"v_mfma_f32_16x16x32_bf16 v[164:167], a[36:39], a[140:143], v[164:167]\n"
"v_mfma_f32_16x16x32_bf16 v[164:167], a[32:35], a[136:139], v[164:167]\n"
"s_waitcnt lgkmcnt(0)\n"
"v_mfma_f32_16x16x32_bf16 v[168:171], a[32:35], a[144:147], v[168:171]\n"
"v_mfma_f32_16x16x32_bf16 v[168:171], a[36:39], a[148:151], v[168:171]\n"
"global_load_dwordx4 a[92:95], v0, s[34:35] offset:3072 sc1\n"
"v_mfma_f32_16x16x32_bf16 v[196:199], a[44:47], a[148:151], v[196:199]\n"
"v_mfma_f32_16x16x32_bf16 v[196:199], a[40:43], a[144:147], v[196:199]\n"
"ds_read_b128 v[28:31], v2 offset:19456\n"
"v_mfma_f32_16x16x32_bf16 a[20:23], a[40:43], a[152:155], a[20:23]\n"
"s_add_u32 s34, s34, 8192\n"
"v_mfma_f32_16x16x32_bf16 a[20:23], a[44:47], a[156:159], a[20:23]\n"
"v_mfma_f32_16x16x32_bf16 a[16:19], a[36:39], a[156:159], a[16:19]\n"
"v_mfma_f32_16x16x32_bf16 a[16:19], a[32:35], a[152:155], a[16:19]\n"
"s_waitcnt vmcnt(10)\n"
"v_mfma_f32_16x16x32_bf16 v[200:203], a[48:51], a[96:99], v[200:203]\n"
"v_mfma_f32_16x16x32_bf16 v[200:203], a[52:55], a[100:103], v[200:203]\n"
"global_load_dwordx4 a[32:35], v0, s[34:35] offset:-4096 sc1\n"
"v_mfma_f32_16x16x32_bf16 v[228:231], a[60:63], a[100:103], v[228:231]\n"
"ds_read_b128 a[100:103], v5 offset:1024\n"
"v_mfma_f32_16x16x32_bf16 v[228:231], a[56:59], a[96:99], v[228:231]\n"
"ds_read_b128 a[96:99], v5 offset:0\n"
"v_mfma_f32_16x16x32_bf16 v[232:235], a[56:59], a[104:107], v[232:235]\n"
"v_mfma_f32_16x16x32_bf16 v[232:235], a[60:63], a[108:111], v[232:235]\n"
"v_mfma_f32_16x16x32_bf16 v[204:207], a[52:55], a[108:111], v[204:207]\n"
"s_waitcnt lgkmcnt(2)\n"
"global_store_dwordx4 v0, v[28:31], s[52:53] offset:-1024 sc1\n"
"ds_read_b128 a[108:111], v5 offset:3072\n"
"v_mfma_f32_16x16x32_bf16 v[204:207], a[48:51], a[104:107], v[204:207]\n"
"ds_read_b128 a[104:107], v5 offset:2048\n"
"v_mfma_f32_16x16x32_bf16 v[208:211], a[48:51], a[112:115], v[208:211]\n"
"v_mfma_f32_16x16x32_bf16 v[208:211], a[52:55], a[116:119], v[208:211]\n"
"global_load_dwordx4 a[36:39], v0, s[34:35] offset:-3072 sc1\n"
"v_mfma_f32_16x16x32_bf16 v[236:239], a[60:63], a[116:119], v[236:239]\n"
"ds_read_b128 a[116:119], v5 offset:5120\n"
"v_mfma_f32_16x16x32_bf16 v[236:239], a[56:59], a[112:115], v[236:239]\n"
"ds_read_b128 a[112:115], v5 offset:4096\n"
"v_mfma_f32_16x16x32_bf16 v[240:243], a[56:59], a[120:123], v[240:243]\n"
"v_mfma_f32_16x16x32_bf16 v[240:243], a[60:63], a[124:127], v[240:243]\n"
"v_mfma_f32_16x16x32_bf16 v[212:215], a[52:55], a[124:127], v[212:215]\n"
"ds_read_b128 a[124:127], v5 offset:7168\n"
"v_mfma_f32_16x16x32_bf16 v[212:215], a[48:51], a[120:123], v[212:215]\n"
"ds_read_b128 a[120:123], v5 offset:6144\n"
"v_mfma_f32_16x16x32_bf16 v[216:219], a[48:51], a[128:131], v[216:219]\n"
"v_mfma_f32_16x16x32_bf16 v[216:219], a[52:55], a[132:135], v[216:219]\n"
"global_load_dwordx4 a[40:43], v0, s[34:35] offset:-2048 sc1\n"
"v_mfma_f32_16x16x32_bf16 v[244:247], a[60:63], a[132:135], v[244:247]\n"
"ds_read_b128 a[132:135], v5 offset:9216\n"
"v_mfma_f32_16x16x32_bf16 v[244:247], a[56:59], a[128:131], v[244:247]\n"
"ds_read_b128 a[128:131], v5 offset:8192\n"
"v_mfma_f32_16x16x32_bf16 v[248:251], a[56:59], a[136:139], v[248:251]\n"
"v_mfma_f32_16x16x32_bf16 v[248:251], a[60:63], a[140:143], v[248:251]\n"
"v_mfma_f32_16x16x32_bf16 v[220:223], a[52:55], a[140:143], v[220:223]\n"
"ds_read_b128 a[140:143], v5 offset:11264\n"
"v_mfma_f32_16x16x32_bf16 v[220:223], a[48:51], a[136:139], v[220:223]\n"
"ds_read_b128 a[136:139], v5 offset:10240\n"
"v_mfma_f32_16x16x32_bf16 v[224:227], a[48:51], a[144:147], v[224:227]\n"
"ds_read_b128 v[24:27], v2 offset:20480\n"
"v_mfma_f32_16x16x32_bf16 v[224:227], a[52:55], a[148:151], v[224:227]\n"
"global_load_dwordx4 a[44:47], v0, s[34:35] offset:-1024 sc1\n"
"v_mfma_f32_16x16x32_bf16 v[252:255], a[60:63], a[148:151], v[252:255]\n"
"ds_read_b128 a[148:151], v5 offset:13312\n"
"v_mfma_f32_16x16x32_bf16 v[252:255], a[56:59], a[144:147], v[252:255]\n"
"ds_read_b128 a[144:147], v5 offset:12288\n"
"v_mfma_f32_16x16x32_bf16 a[28:31], a[56:59], a[152:155], a[28:31]\n"
"v_mfma_f32_16x16x32_bf16 a[28:31], a[60:63], a[156:159], a[28:31]\n"
"v_mfma_f32_16x16x32_bf16 a[24:27], a[52:55], a[156:159], a[24:27]\n"
"ds_read_b128 a[156:159], v5 offset:15360\n"
"v_mfma_f32_16x16x32_bf16 a[24:27], a[48:51], a[152:155], a[24:27]\n"
"s_waitcnt vmcnt(10) lgkmcnt(12)\n"
"ds_read_b128 a[152:155], v5 offset:14336\n"
"v_mfma_f32_16x16x32_bf16 v[144:147], a[64:67], a[96:99], v[144:147]\n"
"v_mfma_f32_16x16x32_bf16 v[144:147], a[68:71], a[100:103], v[144:147]\n"
"global_load_dwordx4 a[48:51], v0, s[34:35] offset:0 sc1\n"
"v_mfma_f32_16x16x32_bf16 v[172:175], a[76:79], a[100:103], v[172:175]\n"
"v_mfma_f32_16x16x32_bf16 v[172:175], a[72:75], a[96:99], v[172:175]\n"
"v_mfma_f32_16x16x32_bf16 v[176:179], a[72:75], a[104:107], v[176:179]\n"
"s_waitcnt lgkmcnt(4)\n"
"global_store_dwordx4 v0, v[24:27], s[52:53] offset:0 sc1\n"
"v_mfma_f32_16x16x32_bf16 v[176:179], a[76:79], a[108:111], v[176:179]\n"
"v_mfma_f32_16x16x32_bf16 v[148:151], a[68:71], a[108:111], v[148:151]\n"
"v_mfma_f32_16x16x32_bf16 v[148:151], a[64:67], a[104:107], v[148:151]\n"
"v_mfma_f32_16x16x32_bf16 v[152:155], a[64:67], a[112:115], v[152:155]\n"
"v_mfma_f32_16x16x32_bf16 v[152:155], a[68:71], a[116:119], v[152:155]\n"
"global_load_dwordx4 a[52:55], v0, s[34:35] offset:1024 sc1\n"
"v_mfma_f32_16x16x32_bf16 v[180:183], a[76:79], a[116:119], v[180:183]\n"
"v_mfma_f32_16x16x32_bf16 v[180:183], a[72:75], a[112:115], v[180:183]\n"
"v_mfma_f32_16x16x32_bf16 v[184:187], a[72:75], a[120:123], v[184:187]\n"
"v_mfma_f32_16x16x32_bf16 v[184:187], a[76:79], a[124:127], v[184:187]\n"
"v_mfma_f32_16x16x32_bf16 v[156:159], a[68:71], a[124:127], v[156:159]\n"
"v_mfma_f32_16x16x32_bf16 v[156:159], a[64:67], a[120:123], v[156:159]\n"
"v_mfma_f32_16x16x32_bf16 v[160:163], a[64:67], a[128:131], v[160:163]\n"
"v_mfma_f32_16x16x32_bf16 v[160:163], a[68:71], a[132:135], v[160:163]\n"
"global_load_dwordx4 a[56:59], v0, s[34:35] offset:2048 sc1\n"
"v_mfma_f32_16x16x32_bf16 v[188:191], a[76:79], a[132:135], v[188:191]\n"
"v_mfma_f32_16x16x32_bf16 v[188:191], a[72:75], a[128:131], v[188:191]\n"
"v_mfma_f32_16x16x32_bf16 v[192:195], a[72:75], a[136:139], v[192:195]\n"
"v_mfma_f32_16x16x32_bf16 v[192:195], a[76:79], a[140:143], v[192:195]\n"
"v_mfma_f32_16x16x32_bf16 v[164:167], a[68:71], a[140:143], v[164:167]\n"
"v_mfma_f32_16x16x32_bf16 v[164:167], a[64:67], a[136:139], v[164:167]\n"
"s_waitcnt lgkmcnt(0)\n"
"ds_read_b128 v[28:31], v2 offset:21504\n"
"v_mfma_f32_16x16x32_bf16 v[168:171], a[64:67], a[144:147], v[168:171]\n"
"v_mfma_f32_16x16x32_bf16 v[168:171], a[68:71], a[148:151], v[168:171]\n"
"global_load_dwordx4 a[60:63], v0, s[34:35] offset:3072 sc1\n"
"v_mfma_f32_16x16x32_bf16 v[196:199], a[76:79], a[148:151], v[196:199]\n"
"v_mfma_f32_16x16x32_bf16 v[196:199], a[72:75], a[144:147], v[196:199]\n"
"v_mfma_f32_16x16x32_bf16 a[20:23], a[72:75], a[152:155], a[20:23]\n"
"s_add_u32 s34, s34, 8192\n"
"v_mfma_f32_16x16x32_bf16 a[20:23], a[76:79], a[156:159], a[20:23]\n"
"v_mfma_f32_16x16x32_bf16 a[16:19], a[68:71], a[156:159], a[16:19]\n"
"v_mfma_f32_16x16x32_bf16 a[16:19], a[64:67], a[152:155], a[16:19]\n"
"s_waitcnt vmcnt(10)\n"
"v_mfma_f32_16x16x32_bf16 v[200:203], a[80:83], a[96:99], v[200:203]\n"
"v_mfma_f32_16x16x32_bf16 v[200:203], a[84:87], a[100:103], v[200:203]\n"
"global_load_dwordx4 a[64:67], v0, s[34:35] offset:-4096 sc1\n"
"v_mfma_f32_16x16x32_bf16 v[228:231], a[92:95], a[100:103], v[228:231]\n"
"s_waitcnt lgkmcnt(0)\n"
"global_store_dwordx4 v0, v[28:31], s[52:53] offset:1024 sc1\n"
"ds_read_b128 a[100:103], v2 offset:17408\n"
"v_mfma_f32_16x16x32_bf16 v[228:231], a[88:91], a[96:99], v[228:231]\n"
"ds_read_b128 a[96:99], v2 offset:16384\n"
"v_mfma_f32_16x16x32_bf16 v[232:235], a[88:91], a[104:107], v[232:235]\n"
"v_mfma_f32_16x16x32_bf16 v[232:235], a[92:95], a[108:111], v[232:235]\n"
"v_mfma_f32_16x16x32_bf16 v[204:207], a[84:87], a[108:111], v[204:207]\n"
"ds_read_b128 a[108:111], v2 offset:19456\n"
"v_mfma_f32_16x16x32_bf16 v[204:207], a[80:83], a[104:107], v[204:207]\n"
"ds_read_b128 a[104:107], v2 offset:18432\n"
"v_mfma_f32_16x16x32_bf16 v[208:211], a[80:83], a[112:115], v[208:211]\n"
"v_mfma_f32_16x16x32_bf16 v[208:211], a[84:87], a[116:119], v[208:211]\n"
"global_load_dwordx4 a[68:71], v0, s[34:35] offset:-3072 sc1\n"
"v_mfma_f32_16x16x32_bf16 v[236:239], a[92:95], a[116:119], v[236:239]\n"
"ds_read_b128 a[116:119], v2 offset:21504\n"
"v_mfma_f32_16x16x32_bf16 v[236:239], a[88:91], a[112:115], v[236:239]\n"
"ds_read_b128 a[112:115], v2 offset:20480\n"
"v_mfma_f32_16x16x32_bf16 v[240:243], a[88:91], a[120:123], v[240:243]\n"
"v_mfma_f32_16x16x32_bf16 v[240:243], a[92:95], a[124:127], v[240:243]\n"
"v_mfma_f32_16x16x32_bf16 v[212:215], a[84:87], a[124:127], v[212:215]\n"
"ds_read_b128 a[124:127], v2 offset:23552\n"
"v_mfma_f32_16x16x32_bf16 v[212:215], a[80:83], a[120:123], v[212:215]\n"
"ds_read_b128 a[120:123], v2 offset:22528\n"
"v_mfma_f32_16x16x32_bf16 v[216:219], a[80:83], a[128:131], v[216:219]\n"
"v_mfma_f32_16x16x32_bf16 v[216:219], a[84:87], a[132:135], v[216:219]\n"
"global_load_dwordx4 a[72:75], v0, s[34:35] offset:-2048 sc1\n"
"v_mfma_f32_16x16x32_bf16 v[244:247], a[92:95], a[132:135], v[244:247]\n"
"ds_read_b128 a[132:135], v2 offset:25600\n"
"v_mfma_f32_16x16x32_bf16 v[244:247], a[88:91], a[128:131], v[244:247]\n"
"ds_read_b128 a[128:131], v2 offset:24576\n"
"v_mfma_f32_16x16x32_bf16 v[248:251], a[88:91], a[136:139], v[248:251]\n"
"v_mfma_f32_16x16x32_bf16 v[248:251], a[92:95], a[140:143], v[248:251]\n"
"ds_read_b128 v[24:27], v2 offset:22528\n"
"v_mfma_f32_16x16x32_bf16 v[220:223], a[84:87], a[140:143], v[220:223]\n"
"ds_read_b128 a[140:143], v2 offset:27648\n"
"v_mfma_f32_16x16x32_bf16 v[220:223], a[80:83], a[136:139], v[220:223]\n"
"ds_read_b128 a[136:139], v2 offset:26624\n"
"v_mfma_f32_16x16x32_bf16 v[224:227], a[80:83], a[144:147], v[224:227]\n"
"v_mfma_f32_16x16x32_bf16 v[224:227], a[84:87], a[148:151], v[224:227]\n"
"global_load_dwordx4 a[76:79], v0, s[34:35] offset:-1024 sc1\n"
"v_mfma_f32_16x16x32_bf16 v[252:255], a[92:95], a[148:151], v[252:255]\n"
"ds_read_b128 a[148:151], v2 offset:29696\n"
"v_mfma_f32_16x16x32_bf16 v[252:255], a[88:91], a[144:147], v[252:255]\n"
"ds_read_b128 a[144:147], v2 offset:28672\n"
"v_mfma_f32_16x16x32_bf16 a[28:31], a[88:91], a[152:155], a[28:31]\n"
"v_mfma_f32_16x16x32_bf16 a[28:31], a[92:95], a[156:159], a[28:31]\n"
"v_mfma_f32_16x16x32_bf16 a[24:27], a[84:87], a[156:159], a[24:27]\n"
"ds_read_b128 a[156:159], v2 offset:31744\n"
"v_mfma_f32_16x16x32_bf16 a[24:27], a[80:83], a[152:155], a[24:27]\n"
"s_waitcnt vmcnt(10) lgkmcnt(12)\n"
"ds_read_b128 a[152:155], v2 offset:30720\n"
"v_mfma_f32_16x16x32_bf16 v[32:35], a[32:35], a[96:99], v[32:35]\n"
"s_waitcnt lgkmcnt(6)\n"
"global_store_dwordx4 v0, v[24:27], s[52:53] offset:2048 sc1\n"
"v_mfma_f32_16x16x32_bf16 v[32:35], a[36:39], a[100:103], v[32:35]\n"
"global_load_dwordx4 a[80:83], v0, s[34:35] offset:0 sc1\n"
"L_P3:\n"
"v_mfma_f32_16x16x32_bf16 v[60:63], a[44:47], a[100:103], v[60:63]\n"
"s_barrier\n"
"v_mfma_f32_16x16x32_bf16 v[60:63], a[40:43], a[96:99], v[60:63]\n"
"v_mfma_f32_16x16x32_bf16 v[64:67], a[40:43], a[104:107], v[64:67]\n"
"v_mfma_f32_16x16x32_bf16 v[64:67], a[44:47], a[108:111], v[64:67]\n"
"v_mfma_f32_16x16x32_bf16 v[36:39], a[36:39], a[108:111], v[36:39]\n"
"v_mfma_f32_16x16x32_bf16 v[36:39], a[32:35], a[104:107], v[36:39]\n"
"v_mfma_f32_16x16x32_bf16 v[40:43], a[32:35], a[112:115], v[40:43]\n"
"v_mfma_f32_16x16x32_bf16 v[40:43], a[36:39], a[116:119], v[40:43]\n"
"global_load_dwordx4 a[84:87], v0, s[34:35] offset:1024 sc1\n"
"v_mfma_f32_16x16x32_bf16 v[68:71], a[44:47], a[116:119], v[68:71]\n"
"v_mfma_f32_16x16x32_bf16 v[68:71], a[40:43], a[112:115], v[68:71]\n"
"v_mfma_f32_16x16x32_bf16 v[72:75], a[40:43], a[120:123], v[72:75]\n"
"v_mfma_f32_16x16x32_bf16 v[72:75], a[44:47], a[124:127], v[72:75]\n"
"v_mfma_f32_16x16x32_bf16 v[44:47], a[36:39], a[124:127], v[44:47]\n"
"v_mfma_f32_16x16x32_bf16 v[44:47], a[32:35], a[120:123], v[44:47]\n"
"s_waitcnt lgkmcnt(4)\n"
"v_mfma_f32_16x16x32_bf16 v[48:51], a[32:35], a[128:131], v[48:51]\n"
"v_mfma_f32_16x16x32_bf16 v[48:51], a[36:39], a[132:135], v[48:51]\n"
"global_load_dwordx4 a[88:91], v0, s[34:35] offset:2048 sc1\n"
"v_lshrrev_b32 v16, 4, v0\n"
"v_mfma_f32_16x16x32_bf16 v[76:79], a[44:47], a[132:135], v[76:79]\n"
"v_and_b32 v16, 0x30, v16\n"
"v_mfma_f32_16x16x32_bf16 v[76:79], a[40:43], a[128:131], v[76:79]\n"
"ds_read_b128 v[28:31], v2 offset:23552\n"
"buffer_load_dwordx2 v[18:19], v16, s[44:47], s61 offen offset:0\n"
"v_mfma_f32_16x16x32_bf16 v[80:83], a[40:43], a[136:139], v[80:83]\n"
"buffer_load_dwordx2 v[8:9], off, s[56:59], %14 sc1\n"
"v_mfma_f32_16x16x32_bf16 v[80:83], a[44:47], a[140:143], v[80:83]\n"
"v_mfma_f32_16x16x32_bf16 v[52:55], a[36:39], a[140:143], v[52:55]\n"
"v_mfma_f32_16x16x32_bf16 v[52:55], a[32:35], a[136:139], v[52:55]\n"
"s_waitcnt lgkmcnt(1)\n"
"v_mfma_f32_16x16x32_bf16 v[56:59], a[32:35], a[144:147], v[56:59]\n"
"v_mfma_f32_16x16x32_bf16 v[56:59], a[36:39], a[148:151], v[56:59]\n"
"global_load_dwordx4 a[92:95], v0, s[34:35] offset:3072 sc1\n"
"v_lshrrev_b32 v20, 4, v0\n"
"v_mfma_f32_16x16x32_bf16 v[84:87], a[44:47], a[148:151], v[84:87]\n"
"v_and_b32 v20, 0x30, v20\n"
"v_mfma_f32_16x16x32_bf16 v[84:87], a[40:43], a[144:147], v[84:87]\n"
"buffer_load_dwordx2 v[22:23], v20, s[44:47], s61 offen offset:8\n"
"v_mfma_f32_16x16x32_bf16 a[4:7], a[40:43], a[152:155], a[4:7]\n"
"s_add_u32 s34, s34, 8192\n"
"v_mfma_f32_16x16x32_bf16 a[4:7], a[44:47], a[156:159], a[4:7]\n"
"v_mfma_f32_16x16x32_bf16 a[0:3], a[36:39], a[156:159], a[0:3]\n"
"s_waitcnt lgkmcnt(0)\n"
"global_store_dwordx4 v0, v[28:31], s[52:53] offset:3072 sc1\n"
"v_mfma_f32_16x16x32_bf16 a[0:3], a[32:35], a[152:155], a[0:3]\n"
"s_waitcnt vmcnt(14)\n"
"v_mfma_f32_16x16x32_bf16 v[88:91], a[48:51], a[96:99], v[88:91]\n"
"v_mfma_f32_16x16x32_bf16 v[88:91], a[52:55], a[100:103], v[88:91]\n"
"global_load_dwordx4 a[32:35], v0, s[34:35] offset:-4096 sc1\n"
"v_mfma_f32_16x16x32_bf16 v[116:119], a[60:63], a[100:103], v[116:119]\n"
"v_mfma_f32_16x16x32_bf16 v[116:119], a[56:59], a[96:99], v[116:119]\n"
"v_mfma_f32_16x16x32_bf16 v[120:123], a[56:59], a[104:107], v[120:123]\n"
"v_mfma_f32_16x16x32_bf16 v[120:123], a[60:63], a[108:111], v[120:123]\n"
"v_mfma_f32_16x16x32_bf16 v[92:95], a[52:55], a[108:111], v[92:95]\n"
"v_mfma_f32_16x16x32_bf16 v[92:95], a[48:51], a[104:107], v[92:95]\n"
"v_mfma_f32_16x16x32_bf16 v[96:99], a[48:51], a[112:115], v[96:99]\n"
"v_mfma_f32_16x16x32_bf16 v[96:99], a[52:55], a[116:119], v[96:99]\n"
"global_load_dwordx4 a[36:39], v0, s[34:35] offset:-3072 sc1\n"
"v_mfma_f32_16x16x32_bf16 v[124:127], a[60:63], a[116:119], v[124:127]\n"
"v_mfma_f32_16x16x32_bf16 v[124:127], a[56:59], a[112:115], v[124:127]\n"
"v_mfma_f32_16x16x32_bf16 v[128:131], a[56:59], a[120:123], v[128:131]\n"
"v_mfma_f32_16x16x32_bf16 v[128:131], a[60:63], a[124:127], v[128:131]\n"
"v_mfma_f32_16x16x32_bf16 v[100:103], a[52:55], a[124:127], v[100:103]\n"
"v_mfma_f32_16x16x32_bf16 v[100:103], a[48:51], a[120:123], v[100:103]\n"
"v_mfma_f32_16x16x32_bf16 v[104:107], a[48:51], a[128:131], v[104:107]\n"
"ds_read_b128 v[24:27], v2 offset:24576\n"
"v_mfma_f32_16x16x32_bf16 v[104:107], a[52:55], a[132:135], v[104:107]\n"
"L_st6249:\n"
"s_cbranch_execz L_st8328\n"
"global_load_dwordx4 a[40:43], v0, s[34:35] offset:-2048 sc1\n"
"v_mfma_f32_16x16x32_bf16 v[132:135], a[60:63], a[132:135], v[132:135]\n"
"v_mfma_f32_16x16x32_bf16 v[132:135], a[56:59], a[128:131], v[132:135]\n"
"v_mfma_f32_16x16x32_bf16 v[136:139], a[56:59], a[136:139], v[136:139]\n"
"v_mfma_f32_16x16x32_bf16 v[136:139], a[60:63], a[140:143], v[136:139]\n"
"v_mfma_f32_16x16x32_bf16 v[108:111], a[52:55], a[140:143], v[108:111]\n"
"v_mfma_f32_16x16x32_bf16 v[108:111], a[48:51], a[136:139], v[108:111]\n"
"v_mfma_f32_16x16x32_bf16 v[112:115], a[48:51], a[144:147], v[112:115]\n"
"v_mfma_f32_16x16x32_bf16 v[112:115], a[52:55], a[148:151], v[112:115]\n"
"global_load_dwordx4 a[44:47], v0, s[34:35] offset:-1024 sc1\n"
"v_mfma_f32_16x16x32_bf16 v[140:143], a[60:63], a[148:151], v[140:143]\n"
"v_mfma_f32_16x16x32_bf16 v[140:143], a[56:59], a[144:147], v[140:143]\n"
"v_mfma_f32_16x16x32_bf16 a[12:15], a[56:59], a[152:155], a[12:15]\n"
"s_waitcnt lgkmcnt(0)\n"
"global_store_dwordx4 v0, v[24:27], s[54:55] offset:-4096 sc1\n"
"v_mfma_f32_16x16x32_bf16 a[12:15], a[60:63], a[156:159], a[12:15]\n"
"v_mfma_f32_16x16x32_bf16 a[8:11], a[52:55], a[156:159], a[8:11]\n"
"v_mfma_f32_16x16x32_bf16 a[8:11], a[48:51], a[152:155], a[8:11]\n"
"s_waitcnt vmcnt(14)\n"
"v_mfma_f32_16x16x32_bf16 v[144:147], a[64:67], a[96:99], v[144:147]\n"
"v_mfma_f32_16x16x32_bf16 v[144:147], a[68:71], a[100:103], v[144:147]\n"
"global_load_dwordx4 a[48:51], v0, s[34:35] offset:0 sc1\n"
"v_mfma_f32_16x16x32_bf16 v[172:175], a[76:79], a[100:103], v[172:175]\n"
"v_mfma_f32_16x16x32_bf16 v[172:175], a[72:75], a[96:99], v[172:175]\n"
"v_mfma_f32_16x16x32_bf16 v[176:179], a[72:75], a[104:107], v[176:179]\n"
"v_mfma_f32_16x16x32_bf16 v[176:179], a[76:79], a[108:111], v[176:179]\n"
"v_mfma_f32_16x16x32_bf16 v[148:151], a[68:71], a[108:111], v[148:151]\n"
"v_mfma_f32_16x16x32_bf16 v[148:151], a[64:67], a[104:107], v[148:151]\n"
"v_mfma_f32_16x16x32_bf16 v[152:155], a[64:67], a[112:115], v[152:155]\n"
"v_mfma_f32_16x16x32_bf16 v[152:155], a[68:71], a[116:119], v[152:155]\n"
"global_load_dwordx4 a[52:55], v0, s[34:35] offset:1024 sc1\n"
"v_mfma_f32_16x16x32_bf16 v[180:183], a[76:79], a[116:119], v[180:183]\n"
"v_mfma_f32_16x16x32_bf16 v[180:183], a[72:75], a[112:115], v[180:183]\n"
"v_mfma_f32_16x16x32_bf16 v[184:187], a[72:75], a[120:123], v[184:187]\n"
"v_mfma_f32_16x16x32_bf16 v[184:187], a[76:79], a[124:127], v[184:187]\n"
"v_mfma_f32_16x16x32_bf16 v[156:159], a[68:71], a[124:127], v[156:159]\n"
"v_mfma_f32_16x16x32_bf16 v[156:159], a[64:67], a[120:123], v[156:159]\n"
"ds_read_b128 v[28:31], v2 offset:26624\n"
"v_mfma_f32_16x16x32_bf16 v[160:163], a[64:67], a[128:131], v[160:163]\n"
"v_mfma_f32_16x16x32_bf16 v[160:163], a[68:71], a[132:135], v[160:163]\n"
"global_load_dwordx4 a[56:59], v0, s[34:35] offset:2048 sc1\n"
"v_mfma_f32_16x16x32_bf16 v[188:191], a[76:79], a[132:135], v[188:191]\n"
"v_mfma_f32_16x16x32_bf16 v[188:191], a[72:75], a[128:131], v[188:191]\n"
"v_mfma_f32_16x16x32_bf16 v[192:195], a[72:75], a[136:139], v[192:195]\n"
"v_mfma_f32_16x16x32_bf16 v[192:195], a[76:79], a[140:143], v[192:195]\n"
"v_mfma_f32_16x16x32_bf16 v[164:167], a[68:71], a[140:143], v[164:167]\n"
"v_mfma_f32_16x16x32_bf16 v[164:167], a[64:67], a[136:139], v[164:167]\n"
"v_mfma_f32_16x16x32_bf16 v[168:171], a[64:67], a[144:147], v[168:171]\n"
"v_mfma_f32_16x16x32_bf16 v[168:171], a[68:71], a[148:151], v[168:171]\n"
"global_load_dwordx4 a[60:63], v0, s[34:35] offset:3072 sc1\n"
"v_mfma_f32_16x16x32_bf16 v[196:199], a[76:79], a[148:151], v[196:199]\n"
"s_waitcnt lgkmcnt(0)\n"
"global_store_dwordx4 v0, v[28:31], s[54:55] offset:-2048 sc1\n"
"v_mfma_f32_16x16x32_bf16 v[196:199], a[72:75], a[144:147], v[196:199]\n"
"v_mfma_f32_16x16x32_bf16 a[20:23], a[72:75], a[152:155], a[20:23]\n"
"s_add_u32 s34, s34, 8192\n"
"v_mfma_f32_16x16x32_bf16 a[20:23], a[76:79], a[156:159], a[20:23]\n"
"v_mfma_f32_16x16x32_bf16 a[16:19], a[68:71], a[156:159], a[16:19]\n"
"v_mfma_f32_16x16x32_bf16 a[16:19], a[64:67], a[152:155], a[16:19]\n"
"s_waitcnt vmcnt(12)\n"
"v_mfma_f32_16x16x32_bf16 v[200:203], a[80:83], a[96:99], v[200:203]\n"
"v_mfma_f32_16x16x32_bf16 v[200:203], a[84:87], a[100:103], v[200:203]\n"
"global_load_dwordx4 a[64:67], v0, s[34:35] offset:-4096 sc1\n"
"v_mfma_f32_16x16x32_bf16 v[228:231], a[92:95], a[100:103], v[228:231]\n"
"ds_read_b128 a[100:103], v3 offset:17408\n"
"v_mfma_f32_16x16x32_bf16 v[228:231], a[88:91], a[96:99], v[228:231]\n"
"ds_read_b128 a[96:99], v3 offset:16384\n"
"v_mfma_f32_16x16x32_bf16 v[232:235], a[88:91], a[104:107], v[232:235]\n"
"v_mfma_f32_16x16x32_bf16 v[232:235], a[92:95], a[108:111], v[232:235]\n"
"v_mfma_f32_16x16x32_bf16 v[204:207], a[84:87], a[108:111], v[204:207]\n"
"ds_read_b128 a[108:111], v3 offset:19456\n"
"v_mfma_f32_16x16x32_bf16 v[204:207], a[80:83], a[104:107], v[204:207]\n"
"ds_read_b128 a[104:107], v3 offset:18432\n"
"v_mfma_f32_16x16x32_bf16 v[208:211], a[80:83], a[112:115], v[208:211]\n"
"v_mfma_f32_16x16x32_bf16 v[208:211], a[84:87], a[116:119], v[208:211]\n"
"global_load_dwordx4 a[68:71], v0, s[34:35] offset:-3072 sc1\n"
"v_mfma_f32_16x16x32_bf16 v[236:239], a[92:95], a[116:119], v[236:239]\n"
"ds_read_b128 a[116:119], v3 offset:21504\n"
"v_mfma_f32_16x16x32_bf16 v[236:239], a[88:91], a[112:115], v[236:239]\n"
"ds_read_b128 a[112:115], v3 offset:20480\n"
"v_mfma_f32_16x16x32_bf16 v[240:243], a[88:91], a[120:123], v[240:243]\n"
"v_mfma_f32_16x16x32_bf16 v[240:243], a[92:95], a[124:127], v[240:243]\n"
"ds_read_b128 v[24:27], v2 offset:29696\n"
"v_mfma_f32_16x16x32_bf16 v[212:215], a[84:87], a[124:127], v[212:215]\n"
"ds_read_b128 a[124:127], v3 offset:23552\n"
"v_mfma_f32_16x16x32_bf16 v[212:215], a[80:83], a[120:123], v[212:215]\n"
"ds_read_b128 a[120:123], v3 offset:22528\n"
"v_mfma_f32_16x16x32_bf16 v[216:219], a[80:83], a[128:131], v[216:219]\n"
"v_mfma_f32_16x16x32_bf16 v[216:219], a[84:87], a[132:135], v[216:219]\n"
"global_load_dwordx4 a[72:75], v0, s[34:35] offset:-2048 sc1\n"
"v_mfma_f32_16x16x32_bf16 v[244:247], a[92:95], a[132:135], v[244:247]\n"
"ds_read_b128 a[132:135], v3 offset:25600\n"
"v_mfma_f32_16x16x32_bf16 v[244:247], a[88:91], a[128:131], v[244:247]\n"
"ds_read_b128 a[128:131], v3 offset:24576\n"
"v_mfma_f32_16x16x32_bf16 v[248:251], a[88:91], a[136:139], v[248:251]\n"
"v_subrev_u32 v8, s65, v8\n"
"v_mfma_f32_16x16x32_bf16 v[248:251], a[92:95], a[140:143], v[248:251]\n"
"v_xor_b32 v9, s79, v9\n"
"v_mfma_f32_16x16x32_bf16 v[220:223], a[84:87], a[140:143], v[220:223]\n"
"ds_read_b128 a[140:143], v3 offset:27648\n"
"v_or_b32 v8, v8, v9\n"
"v_mfma_f32_16x16x32_bf16 v[220:223], a[80:83], a[136:139], v[220:223]\n"
"ds_read_b128 a[136:139], v3 offset:26624\n"
"v_cmp_lt_u32 vcc, 15, v8\n"
"v_mfma_f32_16x16x32_bf16 v[224:227], a[80:83], a[144:147], v[224:227]\n"
"s_waitcnt lgkmcnt(6)\n"
"global_store_dwordx4 v0, v[24:27], s[54:55] offset:1024 sc1\n"
"s_cbranch_vccnz L_slow0\n"
"L_back0:\n"
"v_mfma_f32_16x16x32_bf16 v[224:227], a[84:87], a[148:151], v[224:227]\n"
"global_load_dwordx4 a[76:79], v0, s[34:35] offset:-1024 sc1\n"
"v_readfirstlane_b32 s66, v9\n"
"v_mfma_f32_16x16x32_bf16 v[252:255], a[92:95], a[148:151], v[252:255]\n"
"ds_read_b128 a[148:151], v3 offset:29696\n"
"v_mfma_f32_16x16x32_bf16 v[252:255], a[88:91], a[144:147], v[252:255]\n"
"s_and_b32 s66, s66, 3\n"
"ds_read_b128 a[144:147], v3 offset:28672\n"
"v_mfma_f32_16x16x32_bf16 a[28:31], a[88:91], a[152:155], a[28:31]\n"
"s_lshl_b32 s69, s66, 13\n"
"v_mfma_f32_16x16x32_bf16 a[28:31], a[92:95], a[156:159], a[28:31]\n"
"s_cmp_eq_u32 s66, 3\n"
"s_cselect_b32 s75, 8192, 0\n"
"v_mfma_f32_16x16x32_bf16 a[24:27], a[84:87], a[156:159], a[24:27]\n"
"s_and_b32 s29, s66, s30\n"
"ds_read_b128 a[156:159], v3 offset:31744\n"
"v_mfma_f32_16x16x32_bf16 a[24:27], a[80:83], a[152:155], a[24:27]\n"
"s_waitcnt vmcnt(11)\n"
"ds_read_b128 a[152:155], v3 offset:30720\n"
"v_mfma_f32_16x16x32_bf16 v[32:35], a[32:35], a[96:99], v[32:35]\n"
"s_xor_b32 s31, s79, s66\n"
"v_mfma_f32_16x16x32_bf16 v[32:35], a[36:39], a[100:103], v[32:35]\n"
"global_load_dwordx4 a[80:83], v0, s[34:35] offset:0 sc1\n"
"v_mfma_f32_16x16x32_bf16 v[60:63], a[44:47], a[100:103], v[60:63]\n"
"v_mfma_f32_16x16x32_bf16 v[60:63], a[40:43], a[96:99], v[60:63]\n"
"v_mfma_f32_16x16x32_bf16 v[64:67], a[40:43], a[104:107], v[64:67]\n"
"v_mfma_f32_16x16x32_bf16 v[64:67], a[44:47], a[108:111], v[64:67]\n"
"v_mfma_f32_16x16x32_bf16 v[36:39], a[36:39], a[108:111], v[36:39]\n"
"s_add_u32 m0, s64, 0\n"
"v_mfma_f32_16x16x32_bf16 v[36:39], a[32:35], a[104:107], v[36:39]\n"
"s_waitcnt lgkmcnt(8)\n"
"v_mfma_f32_16x16x32_bf16 v[40:43], a[32:35], a[112:115], v[40:43]\n"
"v_mfma_f32_16x16x32_bf16 v[40:43], a[36:39], a[116:119], v[40:43]\n"
"global_load_dwordx4 a[84:87], v0, s[34:35] offset:1024 sc1\n"
"v_mfma_f32_16x16x32_bf16 v[68:71], a[44:47], a[116:119], v[68:71]\n"
"v_mfma_f32_16x16x32_bf16 v[68:71], a[40:43], a[112:115], v[68:71]\n"
"v_mfma_f32_16x16x32_bf16 v[72:75], a[40:43], a[120:123], v[72:75]\n"
"v_mfma_f32_16x16x32_bf16 v[72:75], a[44:47], a[124:127], v[72:75]\n"
"v_mfma_f32_16x16x32_bf16 v[44:47], a[36:39], a[124:127], v[44:47]\n"
"v_mfma_f32_16x16x32_bf16 v[44:47], a[32:35], a[120:123], v[44:47]\n"
"s_waitcnt lgkmcnt(4)\n"
"v_mfma_f32_16x16x32_bf16 v[48:51], a[32:35], a[128:131], v[48:51]\n"
"v_mfma_f32_16x16x32_bf16 v[48:51], a[36:39], a[132:135], v[48:51]\n"
"global_load_dwordx4 a[88:91], v0, s[34:35] offset:2048 sc1\n"
"v_mfma_f32_16x16x32_bf16 v[76:79], a[44:47], a[132:135], v[76:79]\n"
"v_mfma_f32_16x16x32_bf16 v[76:79], a[40:43], a[128:131], v[76:79]\n"
"v_mfma_f32_16x16x32_bf16 v[80:83], a[40:43], a[136:139], v[80:83]\n"
"v_mfma_f32_16x16x32_bf16 v[80:83], a[44:47], a[140:143], v[80:83]\n"
"buffer_load_dwordx4 v0, s[48:51], s75 offen offset:0 sc1 lds\n"
"v_mfma_f32_16x16x32_bf16 v[52:55], a[36:39], a[140:143], v[52:55]\n"
"v_mfma_f32_16x16x32_bf16 v[52:55], a[32:35], a[136:139], v[52:55]\n"
"s_waitcnt lgkmcnt(0)\n"
"v_mfma_f32_16x16x32_bf16 v[56:59], a[32:35], a[144:147], v[56:59]\n"
"v_mfma_f32_16x16x32_bf16 v[56:59], a[36:39], a[148:151], v[56:59]\n"
"global_load_dwordx4 a[92:95], v0, s[34:35] offset:3072 sc1\n"
"v_mfma_f32_16x16x32_bf16 v[84:87], a[44:47], a[148:151], v[84:87]\n"
"v_mfma_f32_16x16x32_bf16 v[84:87], a[40:43], a[144:147], v[84:87]\n"
"v_mfma_f32_16x16x32_bf16 a[4:7], a[40:43], a[152:155], a[4:7]\n"
"s_add_u32 s34, s34, 8192\n"
"v_mfma_f32_16x16x32_bf16 a[4:7], a[44:47], a[156:159], a[4:7]\n"
"v_mfma_f32_16x16x32_bf16 a[0:3], a[36:39], a[156:159], a[0:3]\n"
"v_mfma_f32_16x16x32_bf16 a[0:3], a[32:35], a[152:155], a[0:3]\n"
"s_waitcnt vmcnt(11)\n"
"v_mfma_f32_16x16x32_bf16 v[88:91], a[48:51], a[96:99], v[88:91]\n"
"v_mfma_f32_16x16x32_bf16 v[88:91], a[52:55], a[100:103], v[88:91]\n"
"global_load_dwordx4 a[32:35], v0, s[34:35] offset:-4096 sc1\n"
"v_mfma_f32_16x16x32_bf16 v[116:119], a[60:63], a[100:103], v[116:119]\n"
"v_mfma_f32_16x16x32_bf16 v[116:119], a[56:59], a[96:99], v[116:119]\n"
"buffer_load_dwordx4 v0, s[48:51], s75 offen offset:1024 sc1 lds\n"
"v_mfma_f32_16x16x32_bf16 v[120:123], a[56:59], a[104:107], v[120:123]\n"
"v_mfma_f32_16x16x32_bf16 v[120:123], a[60:63], a[108:111], v[120:123]\n"
"v_mfma_f32_16x16x32_bf16 v[92:95], a[52:55], a[108:111], v[92:95]\n"
"v_mfma_f32_16x16x32_bf16 v[92:95], a[48:51], a[104:107], v[92:95]\n"
"v_mfma_f32_16x16x32_bf16 v[96:99], a[48:51], a[112:115], v[96:99]\n"
"v_mfma_f32_16x16x32_bf16 v[96:99], a[52:55], a[116:119], v[96:99]\n"
"global_load_dwordx4 a[36:39], v0, s[34:35] offset:-3072 sc1\n"
"v_mfma_f32_16x16x32_bf16 v[124:127], a[60:63], a[116:119], v[124:127]\n"
"v_mfma_f32_16x16x32_bf16 v[124:127], a[56:59], a[112:115], v[124:127]\n"
"v_mfma_f32_16x16x32_bf16 v[128:131], a[56:59], a[120:123], v[128:131]\n"
"v_mfma_f32_16x16x32_bf16 v[128:131], a[60:63], a[124:127], v[128:131]\n"
"v_mfma_f32_16x16x32_bf16 v[100:103], a[52:55], a[124:127], v[100:103]\n"
"v_mfma_f32_16x16x32_bf16 v[100:103], a[48:51], a[120:123], v[100:103]\n"
"v_mfma_f32_16x16x32_bf16 v[104:107], a[48:51], a[128:131], v[104:107]\n"
"v_mfma_f32_16x16x32_bf16 v[104:107], a[52:55], a[132:135], v[104:107]\n"
"global_load_dwordx4 a[40:43], v0, s[34:35] offset:-2048 sc1\n"
"v_mfma_f32_16x16x32_bf16 v[132:135], a[60:63], a[132:135], v[132:135]\n"
"buffer_load_dwordx4 v0, s[48:51], s75 offen offset:2048 sc1 lds\n"
"v_mfma_f32_16x16x32_bf16 v[132:135], a[56:59], a[128:131], v[132:135]\n"
"v_mfma_f32_16x16x32_bf16 v[136:139], a[56:59], a[136:139], v[136:139]\n"
"v_mfma_f32_16x16x32_bf16 v[136:139], a[60:63], a[140:143], v[136:139]\n"
"v_mfma_f32_16x16x32_bf16 v[108:111], a[52:55], a[140:143], v[108:111]\n"
"v_mfma_f32_16x16x32_bf16 v[108:111], a[48:51], a[136:139], v[108:111]\n"
"v_mfma_f32_16x16x32_bf16 v[112:115], a[48:51], a[144:147], v[112:115]\n"
"v_mfma_f32_16x16x32_bf16 v[112:115], a[52:55], a[148:151], v[112:115]\n"
"global_load_dwordx4 a[44:47], v0, s[34:35] offset:-1024 sc1\n"
"v_mfma_f32_16x16x32_bf16 v[140:143], a[60:63], a[148:151], v[140:143]\n"
"v_mfma_f32_16x16x32_bf16 v[140:143], a[56:59], a[144:147], v[140:143]\n"
"v_mfma_f32_16x16x32_bf16 a[12:15], a[56:59], a[152:155], a[12:15]\n"
"v_mfma_f32_16x16x32_bf16 a[12:15], a[60:63], a[156:159], a[12:15]\n"
"v_mfma_f32_16x16x32_bf16 a[8:11], a[52:55], a[156:159], a[8:11]\n"
"v_mfma_f32_16x16x32_bf16 a[8:11], a[48:51], a[152:155], a[8:11]\n"
"s_waitcnt vmcnt(11)\n"
"v_mfma_f32_16x16x32_bf16 v[144:147], a[64:67], a[96:99], v[144:147]\n"
"buffer_load_dwordx4 v0, s[48:51], s75 offen offset:3072 sc1 lds\n"
"v_mfma_f32_16x16x32_bf16 v[144:147], a[68:71], a[100:103], v[144:147]\n"
"global_load_dwordx4 a[48:51], v0, s[34:35] offset:0 sc1\n"
"v_mfma_f32_16x16x32_bf16 v[172:175], a[76:79], a[100:103], v[172:175]\n"
"v_mfma_f32_16x16x32_bf16 v[172:175], a[72:75], a[96:99], v[172:175]\n"
"v_mfma_f32_16x16x32_bf16 v[176:179], a[72:75], a[104:107], v[176:179]\n"
"v_mfma_f32_16x16x32_bf16 v[176:179], a[76:79], a[108:111], v[176:179]\n"
"v_mfma_f32_16x16x32_bf16 v[148:151], a[68:71], a[108:111], v[148:151]\n"
"v_mfma_f32_16x16x32_bf16 v[148:151], a[64:67], a[104:107], v[148:151]\n"
"v_mfma_f32_16x16x32_bf16 v[152:155], a[64:67], a[112:115], v[152:155]\n"
"v_mfma_f32_16x16x32_bf16 v[152:155], a[68:71], a[116:119], v[152:155]\n"
"global_load_dwordx4 a[52:55], v0, s[34:35] offset:1024 sc1\n"
"v_mfma_f32_16x16x32_bf16 v[180:183], a[76:79], a[116:119], v[180:183]\n"
"v_mfma_f32_16x16x32_bf16 v[180:183], a[72:75], a[112:115], v[180:183]\n"
"v_mfma_f32_16x16x32_bf16 v[184:187], a[72:75], a[120:123], v[184:187]\n"
"v_mfma_f32_16x16x32_bf16 v[184:187], a[76:79], a[124:127], v[184:187]\n"
"v_mfma_f32_16x16x32_bf16 v[156:159], a[68:71], a[124:127], v[156:159]\n"
"v_mfma_f32_16x16x32_bf16 v[156:159], a[64:67], a[120:123], v[156:159]\n"
"s_add_u32 m0, s64, 4096\n"
"v_mfma_f32_16x16x32_bf16 v[160:163], a[64:67], a[128:131], v[160:163]\n"
"v_mfma_f32_16x16x32_bf16 v[160:163], a[68:71], a[132:135], v[160:163]\n"
"global_load_dwordx4 a[56:59], v0, s[34:35] offset:2048 sc1\n"
"v_mfma_f32_16x16x32_bf16 v[188:191], a[76:79], a[132:135], v[188:191]\n"
"v_mfma_f32_16x16x32_bf16 v[188:191], a[72:75], a[128:131], v[188:191]\n"
"v_mfma_f32_16x16x32_bf16 v[192:195], a[72:75], a[136:139], v[192:195]\n"
"v_mfma_f32_16x16x32_bf16 v[192:195], a[76:79], a[140:143], v[192:195]\n"
"v_mfma_f32_16x16x32_bf16 v[164:167], a[68:71], a[140:143], v[164:167]\n"
"v_mfma_f32_16x16x32_bf16 v[164:167], a[64:67], a[136:139], v[164:167]\n"
"v_mfma_f32_16x16x32_bf16 v[168:171], a[64:67], a[144:147], v[168:171]\n"
"v_mfma_f32_16x16x32_bf16 v[168:171], a[68:71], a[148:151], v[168:171]\n"
"global_load_dwordx4 a[60:63], v0, s[34:35] offset:3072 sc1\n"
"v_mfma_f32_16x16x32_bf16 v[196:199], a[76:79], a[148:151], v[196:199]\n"
"v_mfma_f32_16x16x32_bf16 v[196:199], a[72:75], a[144:147], v[196:199]\n"
"v_mfma_f32_16x16x32_bf16 a[20:23], a[72:75], a[152:155], a[20:23]\n"
"s_add_u32 s34, s34, 8192\n"
"v_mfma_f32_16x16x32_bf16 a[20:23], a[76:79], a[156:159], a[20:23]\n"
"v_mfma_f32_16x16x32_bf16 a[16:19], a[68:71], a[156:159], a[16:19]\n"
"buffer_load_dwordx4 v1, s[48:51], s75 offen offset:0 sc1 lds\n"
"v_mfma_f32_16x16x32_bf16 a[16:19], a[64:67], a[152:155], a[16:19]\n"
"s_waitcnt vmcnt(12)\n"
"v_mfma_f32_16x16x32_bf16 v[200:203], a[80:83], a[96:99], v[200:203]\n"
"v_mfma_f32_16x16x32_bf16 v[200:203], a[84:87], a[100:103], v[200:203]\n"
"global_load_dwordx4 a[64:67], v0, s[34:35] offset:-4096 sc1\n"
"s_add_u32 s65, s65, 1\n"
"v_mfma_f32_16x16x32_bf16 v[228:231], a[92:95], a[100:103], v[228:231]\n"
"ds_read_b128 a[100:103], v4 offset:17408\n"
"v_mfma_f32_16x16x32_bf16 v[228:231], a[88:91], a[96:99], v[228:231]\n"
"ds_read_b128 a[96:99], v4 offset:16384\n"
"v_mov_b32 v8, s65\n"
"v_mfma_f32_16x16x32_bf16 v[232:235], a[88:91], a[104:107], v[232:235]\n"
"v_mov_b32 v9, s60\n"
"v_mfma_f32_16x16x32_bf16 v[232:235], a[92:95], a[108:111], v[232:235]\n"
"v_mfma_f32_16x16x32_bf16 v[204:207], a[84:87], a[108:111], v[204:207]\n"
"buffer_store_dwordx2 v[8:9], off, s[56:59], %13 sc1\n"
"ds_read_b128 a[108:111], v4 offset:19456\n"
"v_mfma_f32_16x16x32_bf16 v[204:207], a[80:83], a[104:107], v[204:207]\n"
"ds_read_b128 a[104:107], v4 offset:18432\n"
"v_mfma_f32_16x16x32_bf16 v[208:211], a[80:83], a[112:115], v[208:211]\n"
"v_mfma_f32_16x16x32_bf16 v[208:211], a[84:87], a[116:119], v[208:211]\n"
"global_load_dwordx4 a[68:71], v0, s[34:35] offset:-3072 sc1\n"
"v_mfma_f32_16x16x32_bf16 v[236:239], a[92:95], a[116:119], v[236:239]\n"
"ds_read_b128 a[116:119], v4 offset:21504\n"
"v_mfma_f32_16x16x32_bf16 v[236:239], a[88:91], a[112:115], v[236:239]\n"
"ds_read_b128 a[112:115], v4 offset:20480\n"
"v_mfma_f32_16x16x32_bf16 v[240:243], a[88:91], a[120:123], v[240:243]\n"
"v_mfma_f32_16x16x32_bf16 v[240:243], a[92:95], a[124:127], v[240:243]\n"
"buffer_load_dwordx4 v1, s[48:51], s75 offen offset:1024 sc1 lds\n"
"v_mfma_f32_16x16x32_bf16 v[212:215], a[84:87], a[124:127], v[212:215]\n"
"ds_read_b128 a[124:127], v4 offset:23552\n"
"v_mfma_f32_16x16x32_bf16 v[212:215], a[80:83], a[120:123], v[212:215]\n"
"ds_read_b128 a[120:123], v4 offset:22528\n"
"v_mfma_f32_16x16x32_bf16 v[216:219], a[80:83], a[128:131], v[216:219]\n"
"v_mfma_f32_16x16x32_bf16 v[216:219], a[84:87], a[132:135], v[216:219]\n"
"global_load_dwordx4 a[72:75], v0, s[34:35] offset:-2048 sc1\n"
"v_mfma_f32_16x16x32_bf16 v[244:247], a[92:95], a[132:135], v[244:247]\n"
"ds_read_b128 a[132:135], v4 offset:25600\n"
"v_mfma_f32_16x16x32_bf16 v[244:247], a[88:91], a[128:131], v[244:247]\n"
"ds_read_b128 a[128:131], v4 offset:24576\n"
"v_mfma_f32_16x16x32_bf16 v[248:251], a[88:91], a[136:139], v[248:251]\n"
"v_mfma_f32_16x16x32_bf16 v[248:251], a[92:95], a[140:143], v[248:251]\n"
"v_mfma_f32_16x16x32_bf16 v[220:223], a[84:87], a[140:143], v[220:223]\n"
"ds_read_b128 a[140:143], v4 offset:27648\n"
"v_mfma_f32_16x16x32_bf16 v[220:223], a[80:83], a[136:139], v[220:223]\n"
"ds_read_b128 a[136:139], v4 offset:26624\n"
"v_mfma_f32_16x16x32_bf16 v[224:227], a[80:83], a[144:147], v[224:227]\n"
"v_mfma_f32_16x16x32_bf16 v[224:227], a[84:87], a[148:151], v[224:227]\n"
"global_load_dwordx4 a[76:79], v0, s[34:35] offset:-1024 sc1\n"
"v_mfma_f32_16x16x32_bf16 v[252:255], a[92:95], a[148:151], v[252:255]\n"
"ds_read_b128 a[148:151], v4 offset:29696\n"
"v_mfma_f32_16x16x32_bf16 v[252:255], a[88:91], a[144:147], v[252:255]\n"
"ds_read_b128 a[144:147], v4 offset:28672\n"
"v_mfma_f32_16x16x32_bf16 a[28:31], a[88:91], a[152:155], a[28:31]\n"
"buffer_load_dwordx4 v1, s[48:51], s75 offen offset:2048 sc1 lds\n"
"v_mfma_f32_16x16x32_bf16 a[28:31], a[92:95], a[156:159], a[28:31]\n"
"v_mfma_f32_16x16x32_bf16 a[24:27], a[84:87], a[156:159], a[24:27]\n"
"ds_read_b128 a[156:159], v4 offset:31744\n"
"v_mfma_f32_16x16x32_bf16 a[24:27], a[80:83], a[152:155], a[24:27]\n"
"s_waitcnt vmcnt(13) lgkmcnt(11)\n"
"ds_read_b128 a[152:155], v4 offset:30720\n"
"v_mfma_f32_16x16x32_bf16 v[32:35], a[32:35], a[96:99], v[32:35]\n"
"v_mfma_f32_16x16x32_bf16 v[32:35], a[36:39], a[100:103], v[32:35]\n"
"global_load_dwordx4 a[80:83], v0, s[34:35] offset:0 sc1\n"
"v_mfma_f32_16x16x32_bf16 v[60:63], a[44:47], a[100:103], v[60:63]\n"
"v_mfma_f32_16x16x32_bf16 v[60:63], a[40:43], a[96:99], v[60:63]\n"
"v_mfma_f32_16x16x32_bf16 v[64:67], a[40:43], a[104:107], v[64:67]\n"
"v_mfma_f32_16x16x32_bf16 v[64:67], a[44:47], a[108:111], v[64:67]\n"
"v_mfma_f32_16x16x32_bf16 v[36:39], a[36:39], a[108:111], v[36:39]\n"
"v_mfma_f32_16x16x32_bf16 v[36:39], a[32:35], a[104:107], v[36:39]\n"
"s_waitcnt lgkmcnt(8)\n"
"v_mfma_f32_16x16x32_bf16 v[40:43], a[32:35], a[112:115], v[40:43]\n"
"v_mfma_f32_16x16x32_bf16 v[40:43], a[36:39], a[116:119], v[40:43]\n"
"global_load_dwordx4 a[84:87], v0, s[34:35] offset:1024 sc1\n"
"v_mfma_f32_16x16x32_bf16 v[68:71], a[44:47], a[116:119], v[68:71]\n"
"buffer_load_dwordx4 v1, s[48:51], s75 offen offset:3072 sc1 lds\n"
"v_mfma_f32_16x16x32_bf16 v[68:71], a[40:43], a[112:115], v[68:71]\n"
"v_mfma_f32_16x16x32_bf16 v[72:75], a[40:43], a[120:123], v[72:75]\n"
"v_mfma_f32_16x16x32_bf16 v[72:75], a[44:47], a[124:127], v[72:75]\n"
"v_mfma_f32_16x16x32_bf16 v[44:47], a[36:39], a[124:127], v[44:47]\n"
"v_mfma_f32_16x16x32_bf16 v[44:47], a[32:35], a[120:123], v[44:47]\n"
"s_waitcnt lgkmcnt(4)\n"
"v_mfma_f32_16x16x32_bf16 v[48:51], a[32:35], a[128:131], v[48:51]\n"
"v_mfma_f32_16x16x32_bf16 v[48:51], a[36:39], a[132:135], v[48:51]\n"
"global_load_dwordx4 a[88:91], v0, s[34:35] offset:2048 sc1\n"
"v_mfma_f32_16x16x32_bf16 v[76:79], a[44:47], a[132:135], v[76:79]\n"
"v_mfma_f32_16x16x32_bf16 v[76:79], a[40:43], a[128:131], v[76:79]\n"
"v_mfma_f32_16x16x32_bf16 v[80:83], a[40:43], a[136:139], v[80:83]\n"
"v_mfma_f32_16x16x32_bf16 v[80:83], a[44:47], a[140:143], v[80:83]\n"
"v_mfma_f32_16x16x32_bf16 v[52:55], a[36:39], a[140:143], v[52:55]\n"
"v_mfma_f32_16x16x32_bf16 v[52:55], a[32:35], a[136:139], v[52:55]\n"
"s_waitcnt lgkmcnt(0)\n"
"v_mfma_f32_16x16x32_bf16 v[56:59], a[32:35], a[144:147], v[56:59]\n"
"v_mfma_f32_16x16x32_bf16 v[56:59], a[36:39], a[148:151], v[56:59]\n"
"s_add_u32 m0, s64, 8192\n"
"global_load_dwordx4 a[92:95], v0, s[34:35] offset:3072 sc1\n"
"v_mfma_f32_16x16x32_bf16 v[84:87], a[44:47], a[148:151], v[84:87]\n"
"v_mfma_f32_16x16x32_bf16 v[84:87], a[40:43], a[144:147], v[84:87]\n"
"v_mfma_f32_16x16x32_bf16 a[4:7], a[40:43], a[152:155], a[4:7]\n"
"s_add_u32 s34, s34, 8192\n"
"v_mfma_f32_16x16x32_bf16 a[4:7], a[44:47], a[156:159], a[4:7]\n"
"v_mfma_f32_16x16x32_bf16 a[0:3], a[36:39], a[156:159], a[0:3]\n"
"v_mfma_f32_16x16x32_bf16 a[0:3], a[32:35], a[152:155], a[0:3]\n"
"s_waitcnt vmcnt(13)\n"
"v_mfma_f32_16x16x32_bf16 v[88:91], a[48:51], a[96:99], v[88:91]\n"
"v_mfma_f32_16x16x32_bf16 v[88:91], a[52:55], a[100:103], v[88:91]\n"
"global_load_dwordx4 a[32:35], v0, s[34:35] offset:-4096 sc1\n"
"v_mfma_f32_16x16x32_bf16 v[116:119], a[60:63], a[100:103], v[116:119]\n"
"v_mfma_f32_16x16x32_bf16 v[116:119], a[56:59], a[96:99], v[116:119]\n"
"v_mfma_f32_16x16x32_bf16 v[120:123], a[56:59], a[104:107], v[120:123]\n"
"v_mfma_f32_16x16x32_bf16 v[120:123], a[60:63], a[108:111], v[120:123]\n"
"v_mfma_f32_16x16x32_bf16 v[92:95], a[52:55], a[108:111], v[92:95]\n"
"v_mfma_f32_16x16x32_bf16 v[92:95], a[48:51], a[104:107], v[92:95]\n"
"v_mfma_f32_16x16x32_bf16 v[96:99], a[48:51], a[112:115], v[96:99]\n"
"buffer_load_dwordx4 v0, s[48:51], s69 offen offset:0 sc1 lds\n"
"v_mfma_f32_16x16x32_bf16 v[96:99], a[52:55], a[116:119], v[96:99]\n"
"global_load_dwordx4 a[36:39], v0, s[34:35] offset:-3072 sc1\n"
"v_mfma_f32_16x16x32_bf16 v[124:127], a[60:63], a[116:119], v[124:127]\n"
"v_mfma_f32_16x16x32_bf16 v[124:127], a[56:59], a[112:115], v[124:127]\n"
"v_mfma_f32_16x16x32_bf16 v[128:131], a[56:59], a[120:123], v[128:131]\n"
"v_mfma_f32_16x16x32_bf16 v[128:131], a[60:63], a[124:127], v[128:131]\n"
"v_mfma_f32_16x16x32_bf16 v[100:103], a[52:55], a[124:127], v[100:103]\n"
"v_mfma_f32_16x16x32_bf16 v[100:103], a[48:51], a[120:123], v[100:103]\n"
"v_mfma_f32_16x16x32_bf16 v[104:107], a[48:51], a[128:131], v[104:107]\n"
"v_mfma_f32_16x16x32_bf16 v[104:107], a[52:55], a[132:135], v[104:107]\n"
"global_load_dwordx4 a[40:43], v0, s[34:35] offset:-2048 sc1\n"
"v_mfma_f32_16x16x32_bf16 v[132:135], a[60:63], a[132:135], v[132:135]\n"
"v_mfma_f32_16x16x32_bf16 v[132:135], a[56:59], a[128:131], v[132:135]\n"
"v_mfma_f32_16x16x32_bf16 v[136:139], a[56:59], a[136:139], v[136:139]\n"
"v_mfma_f32_16x16x32_bf16 v[136:139], a[60:63], a[140:143], v[136:139]\n"
"v_mfma_f32_16x16x32_bf16 v[108:111], a[52:55], a[140:143], v[108:111]\n"
"buffer_load_dwordx4 v0, s[48:51], s69 offen offset:1024 sc1 lds\n"
"v_mfma_f32_16x16x32_bf16 v[108:111], a[48:51], a[136:139], v[108:111]\n"
"v_mfma_f32_16x16x32_bf16 v[112:115], a[48:51], a[144:147], v[112:115]\n"
"v_mfma_f32_16x16x32_bf16 v[112:115], a[52:55], a[148:151], v[112:115]\n"
"global_load_dwordx4 a[44:47], v0, s[34:35] offset:-1024 sc1\n"
"v_mfma_f32_16x16x32_bf16 v[140:143], a[60:63], a[148:151], v[140:143]\n"
"v_mfma_f32_16x16x32_bf16 v[140:143], a[56:59], a[144:147], v[140:143]\n"
"v_mfma_f32_16x16x32_bf16 a[12:15], a[56:59], a[152:155], a[12:15]\n"
"v_mfma_f32_16x16x32_bf16 a[12:15], a[60:63], a[156:159], a[12:15]\n"
"v_mfma_f32_16x16x32_bf16 a[8:11], a[52:55], a[156:159], a[8:11]\n"
"v_mfma_f32_16x16x32_bf16 a[8:11], a[48:51], a[152:155], a[8:11]\n"
"s_waitcnt vmcnt(12)\n"
"v_mfma_f32_16x16x32_bf16 v[144:147], a[64:67], a[96:99], v[144:147]\n"
"v_mfma_f32_16x16x32_bf16 v[144:147], a[68:71], a[100:103], v[144:147]\n"
"global_load_dwordx4 a[48:51], v0, s[34:35] offset:0 sc1\n"
"v_mfma_f32_16x16x32_bf16 v[172:175], a[76:79], a[100:103], v[172:175]\n"
"v_mfma_f32_16x16x32_bf16 v[172:175], a[72:75], a[96:99], v[172:175]\n"
"v_mfma_f32_16x16x32_bf16 v[176:179], a[72:75], a[104:107], v[176:179]\n"
"v_mfma_f32_16x16x32_bf16 v[176:179], a[76:79], a[108:111], v[176:179]\n"
"buffer_load_dwordx4 v0, s[48:51], s69 offen offset:2048 sc1 lds\n"
"v_mfma_f32_16x16x32_bf16 v[148:151], a[68:71], a[108:111], v[148:151]\n"
"v_mfma_f32_16x16x32_bf16 v[148:151], a[64:67], a[104:107], v[148:151]\n"
"v_mfma_f32_16x16x32_bf16 v[152:155], a[64:67], a[112:115], v[152:155]\n"
"v_lshlrev_b32 v16, 16, v18\n"
"v_mfma_f32_16x16x32_bf16 v[152:155], a[68:71], a[116:119], v[152:155]\n"
"global_load_dwordx4 a[52:55], v0, s[34:35] offset:1024 sc1\n"
"v_and_b32 v17, s63, v18\n"
"v_mfma_f32_16x16x32_bf16 v[180:183], a[76:79], a[116:119], v[180:183]\n"
"v_lshlrev_b32 v18, 16, v19\n"
"v_mfma_f32_16x16x32_bf16 v[180:183], a[72:75], a[112:115], v[180:183]\n"
"v_and_b32 v19, s63, v19\n"
"v_mfma_f32_16x16x32_bf16 v[184:187], a[72:75], a[120:123], v[184:187]\n"
"v_add_f32 v32, v32, v16\n"
"v_mfma_f32_16x16x32_bf16 v[184:187], a[76:79], a[124:127], v[184:187]\n"
"v_add_f32 v33, v33, v17\n"
"v_add_f32 v34, v34, v18\n"
"v_mfma_f32_16x16x32_bf16 v[156:159], a[68:71], a[124:127], v[156:159]\n"
"v_add_f32 v35, v35, v19\n"
"v_mfma_f32_16x16x32_bf16 v[156:159], a[64:67], a[120:123], v[156:159]\n"
"v_add_f32 v36, v36, v16\n"
"v_mfma_f32_16x16x32_bf16 v[160:163], a[64:67], a[128:131], v[160:163]\n"
"v_add_f32 v37, v37, v17\n"
"v_mfma_f32_16x16x32_bf16 v[160:163], a[68:71], a[132:135], v[160:163]\n"
"global_load_dwordx4 a[56:59], v0, s[34:35] offset:2048 sc1\n"
"v_add_f32 v38, v38, v18\n"
"v_mfma_f32_16x16x32_bf16 v[188:191], a[76:79], a[132:135], v[188:191]\n"
"v_add_f32 v39, v39, v19\n"
"v_mfma_f32_16x16x32_bf16 v[188:191], a[72:75], a[128:131], v[188:191]\n"
"v_add_f32 v40, v40, v16\n"
"v_add_f32 v41, v41, v17\n"
"v_mfma_f32_16x16x32_bf16 v[192:195], a[72:75], a[136:139], v[192:195]\n"
"buffer_load_dwordx4 v0, s[48:51], s69 offen offset:3072 sc1 lds\n"
"v_mfma_f32_16x16x32_bf16 v[192:195], a[76:79], a[140:143], v[192:195]\n"
"v_add_f32 v42, v42, v18\n"
"v_mfma_f32_16x16x32_bf16 v[164:167], a[68:71], a[140:143], v[164:167]\n"
"v_add_f32 v43, v43, v19\n"
"v_mfma_f32_16x16x32_bf16 v[164:167], a[64:67], a[136:139], v[164:167]\n"
"v_add_f32 v44, v44, v16\n"
"v_add_f32 v45, v45, v17\n"
"v_mfma_f32_16x16x32_bf16 v[168:171], a[64:67], a[144:147], v[168:171]\n"
"v_add_f32 v46, v46, v18\n"
"v_mfma_f32_16x16x32_bf16 v[168:171], a[68:71], a[148:151], v[168:171]\n"
"global_load_dwordx4 a[60:63], v0, s[34:35] offset:3072 sc1\n"
"v_add_f32 v47, v47, v19\n"
"v_mfma_f32_16x16x32_bf16 v[196:199], a[76:79], a[148:151], v[196:199]\n"
"v_add_f32 v48, v48, v16\n"
"v_mfma_f32_16x16x32_bf16 v[196:199], a[72:75], a[144:147], v[196:199]\n"
"v_add_f32 v49, v49, v17\n"
"v_mfma_f32_16x16x32_bf16 a[20:23], a[72:75], a[152:155], a[20:23]\n"
"s_add_u32 s34, s34, 8192\n"
"v_add_f32 v50, v50, v18\n"
"v_mfma_f32_16x16x32_bf16 a[20:23], a[76:79], a[156:159], a[20:23]\n"
"v_add_f32 v51, v51, v19\n"
"v_mfma_f32_16x16x32_bf16 a[16:19], a[68:71], a[156:159], a[16:19]\n"
"v_add_f32 v52, v52, v16\n"
"v_mfma_f32_16x16x32_bf16 a[16:19], a[64:67], a[152:155], a[16:19]\n"
"s_waitcnt vmcnt(12)\n"
"v_add_f32 v53, v53, v17\n"
"v_mfma_f32_16x16x32_bf16 v[200:203], a[80:83], a[96:99], v[200:203]\n"
"v_add_f32 v54, v54, v18\n"
"v_add_f32 v55, v55, v19\n"
"v_mfma_f32_16x16x32_bf16 v[200:203], a[84:87], a[100:103], v[200:203]\n"
"global_load_dwordx4 a[64:67], v0, s[34:35] offset:-4096 sc1\n"
"v_add_f32 v56, v56, v16\n"
"v_mfma_f32_16x16x32_bf16 v[228:231], a[92:95], a[100:103], v[228:231]\n"
"s_add_u32 m0, s64, 12288\n"
"ds_read_b128 a[100:103], v5 offset:17408\n"
"v_add_f32 v57, v57, v17\n"
"v_mfma_f32_16x16x32_bf16 v[228:231], a[88:91], a[96:99], v[228:231]\n"
"ds_read_b128 a[96:99], v5 offset:16384\n"
"v_add_f32 v58, v58, v18\n"
"v_mfma_f32_16x16x32_bf16 v[232:235], a[88:91], a[104:107], v[232:235]\n"
"v_add_f32 v59, v59, v19\n"
"v_mfma_f32_16x16x32_bf16 v[232:235], a[92:95], a[108:111], v[232:235]\n"
"v_accvgpr_read_b32 v12, a0\n"
"v_mfma_f32_16x16x32_bf16 v[204:207], a[84:87], a[108:111], v[204:207]\n"
"ds_read_b128 a[108:111], v5 offset:19456\n"
"v_accvgpr_read_b32 v13, a1\n"
"v_mfma_f32_16x16x32_bf16 v[204:207], a[80:83], a[104:107], v[204:207]\n"
"ds_read_b128 a[104:107], v5 offset:18432\n"
"v_accvgpr_read_b32 v14, a2\n"
"v_mfma_f32_16x16x32_bf16 v[208:211], a[80:83], a[112:115], v[208:211]\n"
"v_accvgpr_read_b32 v15, a3\n"
"v_mfma_f32_16x16x32_bf16 v[208:211], a[84:87], a[116:119], v[208:211]\n"
"global_load_dwordx4 a[68:71], v0, s[34:35] offset:-3072 sc1\n"
"v_add_f32 v12, v12, v16\n"
"v_mfma_f32_16x16x32_bf16 v[236:239], a[92:95], a[116:119], v[236:239]\n"
"ds_read_b128 a[116:119], v5 offset:21504\n"
"v_add_f32 v13, v13, v17\n"
"v_mfma_f32_16x16x32_bf16 v[236:239], a[88:91], a[112:115], v[236:239]\n"
"ds_read_b128 a[112:115], v5 offset:20480\n"
"v_add_f32 v14, v14, v18\n"
"v_mfma_f32_16x16x32_bf16 v[240:243], a[88:91], a[120:123], v[240:243]\n"
"v_add_f32 v15, v15, v19\n"
"v_accvgpr_write_b32 a0, v12\n"
"v_mfma_f32_16x16x32_bf16 v[240:243], a[92:95], a[124:127], v[240:243]\n"
"v_accvgpr_write_b32 a1, v13\n"
"v_mfma_f32_16x16x32_bf16 v[212:215], a[84:87], a[124:127], v[212:215]\n"
"ds_read_b128 a[124:127], v5 offset:23552\n"
"v_accvgpr_write_b32 a2, v14\n"
"v_mfma_f32_16x16x32_bf16 v[212:215], a[80:83], a[120:123], v[212:215]\n"
"ds_read_b128 a[120:123], v5 offset:22528\n"
"v_accvgpr_write_b32 a3, v15\n"
"v_mfma_f32_16x16x32_bf16 v[216:219], a[80:83], a[128:131], v[216:219]\n"
"buffer_load_dwordx4 v1, s[48:51], s69 offen offset:0 sc1 lds\n"
"v_mfma_f32_16x16x32_bf16 v[216:219], a[84:87], a[132:135], v[216:219]\n"
"global_load_dwordx4 a[72:75], v0, s[34:35] offset:-2048 sc1\n"
"v_lshrrev_b32 v16, 4, v0\n"
"v_mfma_f32_16x16x32_bf16 v[244:247], a[92:95], a[132:135], v[244:247]\n"
"ds_read_b128 a[132:135], v5 offset:25600\n"
"v_and_b32 v16, 0x30, v16\n"
"v_mfma_f32_16x16x32_bf16 v[244:247], a[88:91], a[128:131], v[244:247]\n"
"ds_read_b128 a[128:131], v5 offset:24576\n"
"buffer_load_dwordx2 v[18:19], v16, s[44:47], s61 offen offset:64\n"
"v_mfma_f32_16x16x32_bf16 v[248:251], a[88:91], a[136:139], v[248:251]\n"
"s_getpc_b64 s[96:97]\n"
"L_c3_:\n"
"s_add_u32 s96, s96, L_last-L_c3_\n"
"s_addc_u32 s97, s97, 0\n"
"v_mfma_f32_16x16x32_bf16 v[248:251], a[92:95], a[140:143], v[248:251]\n"
"s_lshr_b32 s66, %26, 3\n"
"v_mfma_f32_16x16x32_bf16 v[220:223], a[84:87], a[140:143], v[220:223]\n"
"s_lshl2_add_u32 s66, s66, %16\n"
"ds_read_b128 a[140:143], v5 offset:27648\n"
"v_mfma_f32_16x16x32_bf16 v[220:223], a[80:83], a[136:139], v[220:223]\n"
"ds_read_b128 a[136:139], v5 offset:26624\n"
"v_mfma_f32_16x16x32_bf16 v[224:227], a[80:83], a[144:147], v[224:227]\n"
"s_mul_i32 s66, s66, 256\n"
"v_mfma_f32_16x16x32_bf16 v[224:227], a[84:87], a[148:151], v[224:227]\n"
"global_load_dwordx4 a[76:79], v0, s[34:35] offset:-1024 sc1\n"
"v_mfma_f32_16x16x32_bf16 v[252:255], a[92:95], a[148:151], v[252:255]\n"
"ds_read_b128 a[148:151], v5 offset:29696\n"
"v_mfma_f32_16x16x32_bf16 v[252:255], a[88:91], a[144:147], v[252:255]\n"
"s_min_u32 s66, s66, L_headF-L_last-256\n"
"ds_read_b128 a[144:147], v5 offset:28672\n"
"v_mfma_f32_16x16x32_bf16 a[28:31], a[88:91], a[152:155], a[28:31]\n"
"v_mfma_f32_16x16x32_bf16 a[28:31], a[92:95], a[156:159], a[28:31]\n"
"v_min_u32 v11, 16, v0\n"
"v_mfma_f32_16x16x32_bf16 a[24:27], a[84:87], a[156:159], a[24:27]\n"
"ds_read_b128 a[156:159], v5 offset:31744\n"
"v_mfma_f32_16x16x32_bf16 a[24:27], a[80:83], a[152:155], a[24:27]\n"
"s_waitcnt vmcnt(12) lgkmcnt(11)\n"
"ds_read_b128 a[152:155], v5 offset:30720\n"
"v_mfma_f32_16x16x32_bf16 v[32:35], a[32:35], a[96:99], v[32:35]\n"
"buffer_load_dwordx4 v1, s[48:51], s69 offen offset:1024 sc1 lds\n"
"v_mfma_f32_16x16x32_bf16 v[32:35], a[36:39], a[100:103], v[32:35]\n"
"global_load_dwordx4 a[80:83], v0, s[34:35] offset:0 sc1\n"
"v_mfma_f32_16x16x32_bf16 v[60:63], a[44:47], a[100:103], v[60:63]\n"
"v_lshl_add_u32 v11, v11, 3, s66\n"
"v_mfma_f32_16x16x32_bf16 v[60:63], a[40:43], a[96:99], v[60:63]\n"
"global_load_dword v10, v11, s[96:97]\n"
"v_mfma_f32_16x16x32_bf16 v[64:67], a[40:43], a[104:107], v[64:67]\n"
"L_st6802:\n"
"s_cbranch_execz L_retB\n"
"v_mfma_f32_16x16x32_bf16 v[64:67], a[44:47], a[108:111], v[64:67]\n"
"v_mfma_f32_16x16x32_bf16 v[36:39], a[36:39], a[108:111], v[36:39]\n"
"v_mfma_f32_16x16x32_bf16 v[36:39], a[32:35], a[104:107], v[36:39]\n"
"s_waitcnt lgkmcnt(8)\n"
"v_mfma_f32_16x16x32_bf16 v[40:43], a[32:35], a[112:115], v[40:43]\n"
"v_mfma_f32_16x16x32_bf16 v[40:43], a[36:39], a[116:119], v[40:43]\n"
"global_load_dwordx4 a[84:87], v0, s[34:35] offset:1024 sc1\n"
"v_mfma_f32_16x16x32_bf16 v[68:71], a[44:47], a[116:119], v[68:71]\n"
"v_mfma_f32_16x16x32_bf16 v[68:71], a[40:43], a[112:115], v[68:71]\n"
"v_mfma_f32_16x16x32_bf16 v[72:75], a[40:43], a[120:123], v[72:75]\n"
"v_mfma_f32_16x16x32_bf16 v[72:75], a[44:47], a[124:127], v[72:75]\n"
"v_mfma_f32_16x16x32_bf16 v[44:47], a[36:39], a[124:127], v[44:47]\n"
"buffer_load_dwordx4 v1, s[48:51], s69 offen offset:2048 sc1 lds\n"
"v_mfma_f32_16x16x32_bf16 v[44:47], a[32:35], a[120:123], v[44:47]\n"
"s_waitcnt lgkmcnt(4)\n"
"v_mfma_f32_16x16x32_bf16 v[48:51], a[32:35], a[128:131], v[48:51]\n"
"v_mfma_f32_16x16x32_bf16 v[48:51], a[36:39], a[132:135], v[48:51]\n"
"global_load_dwordx4 a[88:91], v0, s[34:35] offset:2048 sc1\n"
"v_mfma_f32_16x16x32_bf16 v[76:79], a[44:47], a[132:135], v[76:79]\n"
"v_mfma_f32_16x16x32_bf16 v[76:79], a[40:43], a[128:131], v[76:79]\n"
"v_mfma_f32_16x16x32_bf16 v[80:83], a[40:43], a[136:139], v[80:83]\n"
"v_mfma_f32_16x16x32_bf16 v[80:83], a[44:47], a[140:143], v[80:83]\n"
"v_mfma_f32_16x16x32_bf16 v[52:55], a[36:39], a[140:143], v[52:55]\n"
"v_mfma_f32_16x16x32_bf16 v[52:55], a[32:35], a[136:139], v[52:55]\n"
"s_waitcnt lgkmcnt(0)\n"
"v_mfma_f32_16x16x32_bf16 v[56:59], a[32:35], a[144:147], v[56:59]\n"
"v_mfma_f32_16x16x32_bf16 v[56:59], a[36:39], a[148:151], v[56:59]\n"
"global_load_dwordx4 a[92:95], v0, s[34:35] offset:3072 sc1\n"
"v_mfma_f32_16x16x32_bf16 v[84:87], a[44:47], a[148:151], v[84:87]\n"
"v_mfma_f32_16x16x32_bf16 v[84:87], a[40:43], a[144:147], v[84:87]\n"
"v_mfma_f32_16x16x32_bf16 a[4:7], a[40:43], a[152:155], a[4:7]\n"
"s_add_u32 s34, s34, 8192\n"
"v_mfma_f32_16x16x32_bf16 a[4:7], a[44:47], a[156:159], a[4:7]\n"
"buffer_load_dwordx4 v1, s[48:51], s69 offen offset:3072 sc1 lds\n"
"v_mfma_f32_16x16x32_bf16 a[0:3], a[36:39], a[156:159], a[0:3]\n"
"v_mfma_f32_16x16x32_bf16 a[0:3], a[32:35], a[152:155], a[0:3]\n"
"s_waitcnt vmcnt(14)\n"
"v_mfma_f32_16x16x32_bf16 v[88:91], a[48:51], a[96:99], v[88:91]\n"
"v_mfma_f32_16x16x32_bf16 v[88:91], a[52:55], a[100:103], v[88:91]\n"
"global_load_dwordx4 a[32:35], v0, s[34:35] offset:-4096 sc1\n"
"v_mfma_f32_16x16x32_bf16 v[116:119], a[60:63], a[100:103], v[116:119]\n"
"v_mfma_f32_16x16x32_bf16 v[116:119], a[56:59], a[96:99], v[116:119]\n"
"v_mfma_f32_16x16x32_bf16 v[120:123], a[56:59], a[104:107], v[120:123]\n"
"v_mfma_f32_16x16x32_bf16 v[120:123], a[60:63], a[108:111], v[120:123]\n"
"v_mfma_f32_16x16x32_bf16 v[92:95], a[52:55], a[108:111], v[92:95]\n"
"v_mfma_f32_16x16x32_bf16 v[92:95], a[48:51], a[104:107], v[92:95]\n"
"v_mfma_f32_16x16x32_bf16 v[96:99], a[48:51], a[112:115], v[96:99]\n"
"v_mfma_f32_16x16x32_bf16 v[96:99], a[52:55], a[116:119], v[96:99]\n"
"global_load_dwordx4 a[36:39], v0, s[34:35] offset:-3072 sc1\n"
"v_mfma_f32_16x16x32_bf16 v[124:127], a[60:63], a[116:119], v[124:127]\n"
"v_mfma_f32_16x16x32_bf16 v[124:127], a[56:59], a[112:115], v[124:127]\n"
"v_mfma_f32_16x16x32_bf16 v[128:131], a[56:59], a[120:123], v[128:131]\n"
"v_mfma_f32_16x16x32_bf16 v[128:131], a[60:63], a[124:127], v[128:131]\n"
"v_mfma_f32_16x16x32_bf16 v[100:103], a[52:55], a[124:127], v[100:103]\n"
"v_mfma_f32_16x16x32_bf16 v[100:103], a[48:51], a[120:123], v[100:103]\n"
"v_mfma_f32_16x16x32_bf16 v[104:107], a[48:51], a[128:131], v[104:107]\n"
"buffer_load_dwordx2 v[8:9], off, s[56:59], %14 sc1\n"
"v_mfma_f32_16x16x32_bf16 v[104:107], a[52:55], a[132:135], v[104:107]\n"
"global_load_dwordx4 a[40:43], v0, s[34:35] offset:-2048 sc1\n"
"v_mfma_f32_16x16x32_bf16 v[132:135], a[60:63], a[132:135], v[132:135]\n"
"v_mfma_f32_16x16x32_bf16 v[132:135], a[56:59], a[128:131], v[132:135]\n"
"v_mfma_f32_16x16x32_bf16 v[136:139], a[56:59], a[136:139], v[136:139]\n"
"v_mfma_f32_16x16x32_bf16 v[136:139], a[60:63], a[140:143], v[136:139]\n"
"v_mfma_f32_16x16x32_bf16 v[108:111], a[52:55], a[140:143], v[108:111]\n"
"v_mfma_f32_16x16x32_bf16 v[108:111], a[48:51], a[136:139], v[108:111]\n"
"v_mfma_f32_16x16x32_bf16 v[112:115], a[48:51], a[144:147], v[112:115]\n"
"v_mfma_f32_16x16x32_bf16 v[112:115], a[52:55], a[148:151], v[112:115]\n"
"global_load_dwordx4 a[44:47], v0, s[34:35] offset:-1024 sc1\n"
"v_mfma_f32_16x16x32_bf16 v[140:143], a[60:63], a[148:151], v[140:143]\n"
"v_mfma_f32_16x16x32_bf16 v[140:143], a[56:59], a[144:147], v[140:143]\n"
"v_mfma_f32_16x16x32_bf16 a[12:15], a[56:59], a[152:155], a[12:15]\n"
"v_mfma_f32_16x16x32_bf16 a[12:15], a[60:63], a[156:159], a[12:15]\n"
"v_mfma_f32_16x16x32_bf16 a[8:11], a[52:55], a[156:159], a[8:11]\n"
"v_mfma_f32_16x16x32_bf16 a[8:11], a[48:51], a[152:155], a[8:11]\n"
"s_waitcnt vmcnt(13)\n"
"v_mfma_f32_16x16x32_bf16 v[144:147], a[64:67], a[96:99], v[144:147]\n"
"v_mfma_f32_16x16x32_bf16 v[144:147], a[68:71], a[100:103], v[144:147]\n"
"global_load_dwordx4 a[48:51], v0, s[34:35] offset:0 sc1\n"
"v_mfma_f32_16x16x32_bf16 v[172:175], a[76:79], a[100:103], v[172:175]\n"
"v_mfma_f32_16x16x32_bf16 v[172:175], a[72:75], a[96:99], v[172:175]\n"
"v_mfma_f32_16x16x32_bf16 v[176:179], a[72:75], a[104:107], v[176:179]\n"
"v_mfma_f32_16x16x32_bf16 v[176:179], a[76:79], a[108:111], v[176:179]\n"
"v_mfma_f32_16x16x32_bf16 v[148:151], a[68:71], a[108:111], v[148:151]\n"
"v_mfma_f32_16x16x32_bf16 v[148:151], a[64:67], a[104:107], v[148:151]\n"
"v_mfma_f32_16x16x32_bf16 v[152:155], a[64:67], a[112:115], v[152:155]\n"
"v_lshlrev_b32 v20, 16, v22\n"
"v_mfma_f32_16x16x32_bf16 v[152:155], a[68:71], a[116:119], v[152:155]\n"
"global_load_dwordx4 a[52:55], v0, s[34:35] offset:1024 sc1\n"
"v_and_b32 v21, s63, v22\n"
"v_mfma_f32_16x16x32_bf16 v[180:183], a[76:79], a[116:119], v[180:183]\n"
"v_lshlrev_b32 v22, 16, v23\n"
"v_mfma_f32_16x16x32_bf16 v[180:183], a[72:75], a[112:115], v[180:183]\n"
"v_and_b32 v23, s63, v23\n"
"v_mfma_f32_16x16x32_bf16 v[184:187], a[72:75], a[120:123], v[184:187]\n"
"v_add_f32 v60, v60, v20\n"
"v_mfma_f32_16x16x32_bf16 v[184:187], a[76:79], a[124:127], v[184:187]\n"
"v_add_f32 v61, v61, v21\n"
"v_add_f32 v62, v62, v22\n"
"v_mfma_f32_16x16x32_bf16 v[156:159], a[68:71], a[124:127], v[156:159]\n"
"v_add_f32 v63, v63, v23\n"
"v_mfma_f32_16x16x32_bf16 v[156:159], a[64:67], a[120:123], v[156:159]\n"
"v_add_f32 v64, v64, v20\n"
"v_mfma_f32_16x16x32_bf16 v[160:163], a[64:67], a[128:131], v[160:163]\n"
"v_add_f32 v65, v65, v21\n"
"v_mfma_f32_16x16x32_bf16 v[160:163], a[68:71], a[132:135], v[160:163]\n"
"global_load_dwordx4 a[56:59], v0, s[34:35] offset:2048 sc1\n"
"v_add_f32 v66, v66, v22\n"
"v_mfma_f32_16x16x32_bf16 v[188:191], a[76:79], a[132:135], v[188:191]\n"
"v_add_f32 v67, v67, v23\n"
"v_mfma_f32_16x16x32_bf16 v[188:191], a[72:75], a[128:131], v[188:191]\n"
"v_add_f32 v68, v68, v20\n"
"v_mfma_f32_16x16x32_bf16 v[192:195], a[72:75], a[136:139], v[192:195]\n"
"v_add_f32 v69, v69, v21\n"
"v_mfma_f32_16x16x32_bf16 v[192:195], a[76:79], a[140:143], v[192:195]\n"
"v_add_f32 v70, v70, v22\n"
"v_mfma_f32_16x16x32_bf16 v[164:167], a[68:71], a[140:143], v[164:167]\n"
"v_add_f32 v71, v71, v23\n"
"v_mfma_f32_16x16x32_bf16 v[164:167], a[64:67], a[136:139], v[164:167]\n"
"v_add_f32 v72, v72, v20\n"
"v_add_f32 v73, v73, v21\n"
"v_mfma_f32_16x16x32_bf16 v[168:171], a[64:67], a[144:147], v[168:171]\n"
"v_add_f32 v74, v74, v22\n"
"v_mfma_f32_16x16x32_bf16 v[168:171], a[68:71], a[148:151], v[168:171]\n"
"global_load_dwordx4 a[60:63], v0, s[34:35] offset:3072 sc1\n"
"v_add_f32 v75, v75, v23\n"
"v_mfma_f32_16x16x32_bf16 v[196:199], a[76:79], a[148:151], v[196:199]\n"
"v_add_f32 v76, v76, v20\n"
"v_mfma_f32_16x16x32_bf16 v[196:199], a[72:75], a[144:147], v[196:199]\n"
"v_add_f32 v77, v77, v21\n"
"v_mfma_f32_16x16x32_bf16 a[20:23], a[72:75], a[152:155], a[20:23]\n"
"s_add_u32 s34, s34, 8192\n"
"v_add_f32 v78, v78, v22\n"
"v_mfma_f32_16x16x32_bf16 a[20:23], a[76:79], a[156:159], a[20:23]\n"
"v_add_f32 v79, v79, v23\n"
"v_mfma_f32_16x16x32_bf16 a[16:19], a[68:71], a[156:159], a[16:19]\n"
"v_add_f32 v80, v80, v20\n"
"v_mfma_f32_16x16x32_bf16 a[16:19], a[64:67], a[152:155], a[16:19]\n"
"s_waitcnt vmcnt(10)\n"
"v_add_f32 v81, v81, v21\n"
"v_mfma_f32_16x16x32_bf16 v[200:203], a[80:83], a[96:99], v[200:203]\n"
"v_add_f32 v82, v82, v22\n"
"v_add_f32 v83, v83, v23\n"
"v_mfma_f32_16x16x32_bf16 v[200:203], a[84:87], a[100:103], v[200:203]\n"
"global_load_dwordx4 a[64:67], v0, s[34:35] offset:-4096 sc1\n"
"v_add_f32 v84, v84, v20\n"
"v_mfma_f32_16x16x32_bf16 v[228:231], a[92:95], a[100:103], v[228:231]\n"
"s_waitcnt vmcnt(10)\n"
"ds_read_b128 a[100:103], v2 offset:1024\n"
"v_add_f32 v85, v85, v21\n"
"v_mfma_f32_16x16x32_bf16 v[228:231], a[88:91], a[96:99], v[228:231]\n"
"ds_read_b128 a[96:99], v2 offset:0\n"
"v_add_f32 v86, v86, v22\n"
"v_mfma_f32_16x16x32_bf16 v[232:235], a[88:91], a[104:107], v[232:235]\n"
"v_add_f32 v87, v87, v23\n"
"v_mfma_f32_16x16x32_bf16 v[232:235], a[92:95], a[108:111], v[232:235]\n"
"v_accvgpr_read_b32 v12, a4\n"
"v_mfma_f32_16x16x32_bf16 v[204:207], a[84:87], a[108:111], v[204:207]\n"
"ds_read_b128 a[108:111], v2 offset:3072\n"
"v_accvgpr_read_b32 v13, a5\n"
"v_mfma_f32_16x16x32_bf16 v[204:207], a[80:83], a[104:107], v[204:207]\n"
"ds_read_b128 a[104:107], v2 offset:2048\n"
"v_accvgpr_read_b32 v14, a6\n"
"v_mfma_f32_16x16x32_bf16 v[208:211], a[80:83], a[112:115], v[208:211]\n"
"v_accvgpr_read_b32 v15, a7\n"
"v_mfma_f32_16x16x32_bf16 v[208:211], a[84:87], a[116:119], v[208:211]\n"
"global_load_dwordx4 a[68:71], v0, s[34:35] offset:-3072 sc1\n"
"v_add_f32 v12, v12, v20\n"
"v_mfma_f32_16x16x32_bf16 v[236:239], a[92:95], a[116:119], v[236:239]\n"
"ds_read_b128 a[116:119], v2 offset:5120\n"
"v_add_f32 v13, v13, v21\n"
"v_mfma_f32_16x16x32_bf16 v[236:239], a[88:91], a[112:115], v[236:239]\n"
"ds_read_b128 a[112:115], v2 offset:4096\n"
"v_add_f32 v14, v14, v22\n"
"v_mfma_f32_16x16x32_bf16 v[240:243], a[88:91], a[120:123], v[240:243]\n"
"v_add_f32 v15, v15, v23\n"
"v_accvgpr_write_b32 a4, v12\n"
"v_mfma_f32_16x16x32_bf16 v[240:243], a[92:95], a[124:127], v[240:243]\n"
"v_accvgpr_write_b32 a5, v13\n"
"v_mfma_f32_16x16x32_bf16 v[212:215], a[84:87], a[124:127], v[212:215]\n"
"ds_read_b128 a[124:127], v2 offset:7168\n"
"v_accvgpr_write_b32 a6, v14\n"
"v_mfma_f32_16x16x32_bf16 v[212:215], a[80:83], a[120:123], v[212:215]\n"
"ds_read_b128 a[120:123], v2 offset:6144\n"
"v_accvgpr_write_b32 a7, v15\n"
"v_mfma_f32_16x16x32_bf16 v[216:219], a[80:83], a[128:131], v[216:219]\n"
"v_mfma_f32_16x16x32_bf16 v[216:219], a[84:87], a[132:135], v[216:219]\n"
"global_load_dwordx4 a[72:75], v0, s[34:35] offset:-2048 sc1\n"
"v_lshrrev_b32 v20, 4, v0\n"
"v_mfma_f32_16x16x32_bf16 v[244:247], a[92:95], a[132:135], v[244:247]\n"
"ds_read_b128 a[132:135], v2 offset:9216\n"
"v_and_b32 v20, 0x30, v20\n"
"v_mfma_f32_16x16x32_bf16 v[244:247], a[88:91], a[128:131], v[244:247]\n"
"ds_read_b128 a[128:131], v2 offset:8192\n"
"buffer_load_dwordx2 v[22:23], v20, s[44:47], s61 offen offset:72\n"
"v_mfma_f32_16x16x32_bf16 v[248:251], a[88:91], a[136:139], v[248:251]\n"
"v_mfma_f32_16x16x32_bf16 v[248:251], a[92:95], a[140:143], v[248:251]\n"
"v_mfma_f32_16x16x32_bf16 v[220:223], a[84:87], a[140:143], v[220:223]\n"
"ds_read_b128 a[140:143], v2 offset:11264\n"
"v_mfma_f32_16x16x32_bf16 v[220:223], a[80:83], a[136:139], v[220:223]\n"
"ds_read_b128 a[136:139], v2 offset:10240\n"
"v_mfma_f32_16x16x32_bf16 v[224:227], a[80:83], a[144:147], v[224:227]\n"
"v_mfma_f32_16x16x32_bf16 v[224:227], a[84:87], a[148:151], v[224:227]\n"
"global_load_dwordx4 a[76:79], v0, s[34:35] offset:-1024 sc1\n"
"v_mfma_f32_16x16x32_bf16 v[252:255], a[92:95], a[148:151], v[252:255]\n"
"ds_read_b128 a[148:151], v2 offset:13312\n"
"v_mfma_f32_16x16x32_bf16 v[252:255], a[88:91], a[144:147], v[252:255]\n"
"ds_read_b128 a[144:147], v2 offset:12288\n"
"v_mfma_f32_16x16x32_bf16 a[28:31], a[88:91], a[152:155], a[28:31]\n"
"v_mfma_f32_16x16x32_bf16 a[28:31], a[92:95], a[156:159], a[28:31]\n"
"v_mfma_f32_16x16x32_bf16 a[24:27], a[84:87], a[156:159], a[24:27]\n"
"s_cmp_eq_u32 s62, 5\n"
"ds_read_b128 a[156:159], v2 offset:15360\n"
"v_mfma_f32_16x16x32_bf16 a[24:27], a[80:83], a[152:155], a[24:27]\n"
"ds_read_b128 a[152:155], v2 offset:14336\n"
"s_cbranch_scc1 L_last\n"
"s_waitcnt vmcnt(9) lgkmcnt(12)\n"
"v_mfma_f32_16x16x32_bf16 v[32:35], a[32:35], a[96:99], v[32:35]\n"
"s_add_u32 s48, s48, 131072\n"
"s_addc_u32 s49, s49, 0\n"
"v_mfma_f32_16x16x32_bf16 v[32:35], a[36:39], a[100:103], v[32:35]\n"
"global_load_dwordx4 a[80:83], v0, s[34:35] offset:0 sc1\n"
"L_P4:\n"
"v_mfma_f32_16x16x32_bf16 v[60:63], a[44:47], a[100:103], v[60:63]\n"
"s_barrier\n"
"v_mfma_f32_16x16x32_bf16 v[60:63], a[40:43], a[96:99], v[60:63]\n"
"v_mfma_f32_16x16x32_bf16 v[64:67], a[40:43], a[104:107], v[64:67]\n"
"v_mfma_f32_16x16x32_bf16 v[64:67], a[44:47], a[108:111], v[64:67]\n"
"v_mfma_f32_16x16x32_bf16 v[36:39], a[36:39], a[108:111], v[36:39]\n"
"v_subrev_u32 v8, s65, v8\n"
"v_mfma_f32_16x16x32_bf16 v[36:39], a[32:35], a[104:107], v[36:39]\n"
"s_waitcnt lgkmcnt(8)\n"
"v_xor_b32 v9, s79, v9\n"
"v_mfma_f32_16x16x32_bf16 v[40:43], a[32:35], a[112:115], v[40:43]\n"
"v_or_b32 v8, v8, v9\n"
"v_mfma_f32_16x16x32_bf16 v[40:43], a[36:39], a[116:119], v[40:43]\n"
"global_load_dwordx4 a[84:87], v0, s[34:35] offset:1024 sc1\n"
"v_cmp_lt_u32 vcc, 15, v8\n"
"v_mfma_f32_16x16x32_bf16 v[68:71], a[44:47], a[116:119], v[68:71]\n"
"s_cbranch_vccnz L_slow1\n"
"L_back1:\n"
"v_mfma_f32_16x16x32_bf16 v[68:71], a[40:43], a[112:115], v[68:71]\n"
"v_mfma_f32_16x16x32_bf16 v[72:75], a[40:43], a[120:123], v[72:75]\n"
"v_mfma_f32_16x16x32_bf16 v[72:75], a[44:47], a[124:127], v[72:75]\n"
"v_mfma_f32_16x16x32_bf16 v[44:47], a[36:39], a[124:127], v[44:47]\n"
"v_mfma_f32_16x16x32_bf16 v[44:47], a[32:35], a[120:123], v[44:47]\n"
"s_waitcnt lgkmcnt(4)\n"
"v_mfma_f32_16x16x32_bf16 v[48:51], a[32:35], a[128:131], v[48:51]\n"
"v_mfma_f32_16x16x32_bf16 v[48:51], a[36:39], a[132:135], v[48:51]\n"
"global_load_dwordx4 a[88:91], v0, s[34:35] offset:2048 sc1\n"
"v_mfma_f32_16x16x32_bf16 v[76:79], a[44:47], a[132:135], v[76:79]\n"
"v_mfma_f32_16x16x32_bf16 v[76:79], a[40:43], a[128:131], v[76:79]\n"
"s_add_u32 m0, s64, 16384\n"
"v_mfma_f32_16x16x32_bf16 v[80:83], a[40:43], a[136:139], v[80:83]\n"
"v_mfma_f32_16x16x32_bf16 v[80:83], a[44:47], a[140:143], v[80:83]\n"
"v_mfma_f32_16x16x32_bf16 v[52:55], a[36:39], a[140:143], v[52:55]\n"
"v_mfma_f32_16x16x32_bf16 v[52:55], a[32:35], a[136:139], v[52:55]\n"
"s_waitcnt lgkmcnt(0)\n"
"v_mfma_f32_16x16x32_bf16 v[56:59], a[32:35], a[144:147], v[56:59]\n"
"v_mfma_f32_16x16x32_bf16 v[56:59], a[36:39], a[148:151], v[56:59]\n"
"global_load_dwordx4 a[92:95], v0, s[34:35] offset:3072 sc1\n"
"v_mfma_f32_16x16x32_bf16 v[84:87], a[44:47], a[148:151], v[84:87]\n"
"v_mfma_f32_16x16x32_bf16 v[84:87], a[40:43], a[144:147], v[84:87]\n"
"v_mfma_f32_16x16x32_bf16 a[4:7], a[40:43], a[152:155], a[4:7]\n"
"s_add_u32 s34, s34, 8192\n"
"v_mfma_f32_16x16x32_bf16 a[4:7], a[44:47], a[156:159], a[4:7]\n"
"v_mfma_f32_16x16x32_bf16 a[0:3], a[36:39], a[156:159], a[0:3]\n"
"v_mfma_f32_16x16x32_bf16 a[0:3], a[32:35], a[152:155], a[0:3]\n"
"s_waitcnt vmcnt(9)\n"
"v_mfma_f32_16x16x32_bf16 v[88:91], a[48:51], a[96:99], v[88:91]\n"
"v_mfma_f32_16x16x32_bf16 v[88:91], a[52:55], a[100:103], v[88:91]\n"
"global_load_dwordx4 a[32:35], v0, s[34:35] offset:-4096 sc1\n"
"v_mfma_f32_16x16x32_bf16 v[116:119], a[60:63], a[100:103], v[116:119]\n"
"buffer_load_dwordx4 v0, s[48:51], s75 offen offset:0 sc1 lds\n"
"v_mfma_f32_16x16x32_bf16 v[116:119], a[56:59], a[96:99], v[116:119]\n"
"v_mfma_f32_16x16x32_bf16 v[120:123], a[56:59], a[104:107], v[120:123]\n"
"v_mfma_f32_16x16x32_bf16 v[120:123], a[60:63], a[108:111], v[120:123]\n"
"v_mfma_f32_16x16x32_bf16 v[92:95], a[52:55], a[108:111], v[92:95]\n"
"v_mfma_f32_16x16x32_bf16 v[92:95], a[48:51], a[104:107], v[92:95]\n"
"v_mfma_f32_16x16x32_bf16 v[96:99], a[48:51], a[112:115], v[96:99]\n"
"v_mfma_f32_16x16x32_bf16 v[96:99], a[52:55], a[116:119], v[96:99]\n"
"global_load_dwordx4 a[36:39], v0, s[34:35] offset:-3072 sc1\n"
"v_mfma_f32_16x16x32_bf16 v[124:127], a[60:63], a[116:119], v[124:127]\n"
"v_mfma_f32_16x16x32_bf16 v[124:127], a[56:59], a[112:115], v[124:127]\n"
"v_mfma_f32_16x16x32_bf16 v[128:131], a[56:59], a[120:123], v[128:131]\n"
"v_mfma_f32_16x16x32_bf16 v[128:131], a[60:63], a[124:127], v[128:131]\n"
"v_mfma_f32_16x16x32_bf16 v[100:103], a[52:55], a[124:127], v[100:103]\n"
"v_mfma_f32_16x16x32_bf16 v[100:103], a[48:51], a[120:123], v[100:103]\n"
"v_mfma_f32_16x16x32_bf16 v[104:107], a[48:51], a[128:131], v[104:107]\n"
"v_mfma_f32_16x16x32_bf16 v[104:107], a[52:55], a[132:135], v[104:107]\n"
"global_load_dwordx4 a[40:43], v0, s[34:35] offset:-2048 sc1\n"
"v_mfma_f32_16x16x32_bf16 v[132:135], a[60:63], a[132:135], v[132:135]\n"
"buffer_load_dwordx4 v0, s[48:51], s75 offen offset:1024 sc1 lds\n"
"v_mfma_f32_16x16x32_bf16 v[132:135], a[56:59], a[128:131], v[132:135]\n"
"v_mfma_f32_16x16x32_bf16 v[136:139], a[56:59], a[136:139], v[136:139]\n"
"v_mfma_f32_16x16x32_bf16 v[136:139], a[60:63], a[140:143], v[136:139]\n"
"v_mfma_f32_16x16x32_bf16 v[108:111], a[52:55], a[140:143], v[108:111]\n"
"v_mfma_f32_16x16x32_bf16 v[108:111], a[48:51], a[136:139], v[108:111]\n"
"v_mfma_f32_16x16x32_bf16 v[112:115], a[48:51], a[144:147], v[112:115]\n"
"v_mfma_f32_16x16x32_bf16 v[112:115], a[52:55], a[148:151], v[112:115]\n"
"global_load_dwordx4 a[44:47], v0, s[34:35] offset:-1024 sc1\n"
"v_mfma_f32_16x16x32_bf16 v[140:143], a[60:63], a[148:151], v[140:143]\n"
"v_mfma_f32_16x16x32_bf16 v[140:143], a[56:59], a[144:147], v[140:143]\n"
"v_mfma_f32_16x16x32_bf16 a[12:15], a[56:59], a[152:155], a[12:15]\n"
"v_mfma_f32_16x16x32_bf16 a[12:15], a[60:63], a[156:159], a[12:15]\n"
"v_mfma_f32_16x16x32_bf16 a[8:11], a[52:55], a[156:159], a[8:11]\n"
"v_mfma_f32_16x16x32_bf16 a[8:11], a[48:51], a[152:155], a[8:11]\n"
"s_waitcnt vmcnt(10)\n"
"v_mfma_f32_16x16x32_bf16 v[144:147], a[64:67], a[96:99], v[144:147]\n"
"v_mfma_f32_16x16x32_bf16 v[144:147], a[68:71], a[100:103], v[144:147]\n"
"global_load_dwordx4 a[48:51], v0, s[34:35] offset:0 sc1\n"
"v_mfma_f32_16x16x32_bf16 v[172:175], a[76:79], a[100:103], v[172:175]\n"
"v_mfma_f32_16x16x32_bf16 v[172:175], a[72:75], a[96:99], v[172:175]\n"
"buffer_load_dwordx4 v0, s[48:51], s75 offen offset:2048 sc1 lds\n"
"v_mfma_f32_16x16x32_bf16 v[176:179], a[72:75], a[104:107], v[176:179]\n"
"v_mfma_f32_16x16x32_bf16 v[176:179], a[76:79], a[108:111], v[176:179]\n"
"v_mfma_f32_16x16x32_bf16 v[148:151], a[68:71], a[108:111], v[148:151]\n"
"v_mfma_f32_16x16x32_bf16 v[148:151], a[64:67], a[104:107], v[148:151]\n"
"v_mfma_f32_16x16x32_bf16 v[152:155], a[64:67], a[112:115], v[152:155]\n"
"v_lshlrev_b32 v16, 16, v18\n"
"v_mfma_f32_16x16x32_bf16 v[152:155], a[68:71], a[116:119], v[152:155]\n"
"global_load_dwordx4 a[52:55], v0, s[34:35] offset:1024 sc1\n"
"v_and_b32 v17, s63, v18\n"
"v_mfma_f32_16x16x32_bf16 v[180:183], a[76:79], a[116:119], v[180:183]\n"
"v_lshlrev_b32 v18, 16, v19\n"
"v_mfma_f32_16x16x32_bf16 v[180:183], a[72:75], a[112:115], v[180:183]\n"
"v_and_b32 v19, s63, v19\n"
"v_mfma_f32_16x16x32_bf16 v[184:187], a[72:75], a[120:123], v[184:187]\n"
"v_add_f32 v88, v88, v16\n"
"v_mfma_f32_16x16x32_bf16 v[184:187], a[76:79], a[124:127], v[184:187]\n"
"v_add_f32 v89, v89, v17\n"
"v_add_f32 v90, v90, v18\n"
"v_mfma_f32_16x16x32_bf16 v[156:159], a[68:71], a[124:127], v[156:159]\n"
"v_add_f32 v91, v91, v19\n"
"v_mfma_f32_16x16x32_bf16 v[156:159], a[64:67], a[120:123], v[156:159]\n"
"v_add_f32 v92, v92, v16\n"
"v_mfma_f32_16x16x32_bf16 v[160:163], a[64:67], a[128:131], v[160:163]\n"
"v_add_f32 v93, v93, v17\n"
"v_mfma_f32_16x16x32_bf16 v[160:163], a[68:71], a[132:135], v[160:163]\n"
"global_load_dwordx4 a[56:59], v0, s[34:35] offset:2048 sc1\n"
"v_add_f32 v94, v94, v18\n"
"v_mfma_f32_16x16x32_bf16 v[188:191], a[76:79], a[132:135], v[188:191]\n"
"buffer_load_dwordx4 v0, s[48:51], s75 offen offset:3072 sc1 lds\n"
"v_mfma_f32_16x16x32_bf16 v[188:191], a[72:75], a[128:131], v[188:191]\n"
"v_add_f32 v95, v95, v19\n"
"v_add_f32 v96, v96, v16\n"
"v_mfma_f32_16x16x32_bf16 v[192:195], a[72:75], a[136:139], v[192:195]\n"
"v_add_f32 v97, v97, v17\n"
"v_mfma_f32_16x16x32_bf16 v[192:195], a[76:79], a[140:143], v[192:195]\n"
"v_add_f32 v98, v98, v18\n"
"v_mfma_f32_16x16x32_bf16 v[164:167], a[68:71], a[140:143], v[164:167]\n"
"v_add_f32 v99, v99, v19\n"
"v_mfma_f32_16x16x32_bf16 v[164:167], a[64:67], a[136:139], v[164:167]\n"
"v_add_f32 v100, v100, v16\n"
"v_add_f32 v101, v101, v17\n"
"v_mfma_f32_16x16x32_bf16 v[168:171], a[64:67], a[144:147], v[168:171]\n"
"v_add_f32 v102, v102, v18\n"
"v_mfma_f32_16x16x32_bf16 v[168:171], a[68:71], a[148:151], v[168:171]\n"
"global_load_dwordx4 a[60:63], v0, s[34:35] offset:3072 sc1\n"
"v_add_f32 v103, v103, v19\n"
"v_mfma_f32_16x16x32_bf16 v[196:199], a[76:79], a[148:151], v[196:199]\n"
"v_add_f32 v104, v104, v16\n"
"v_mfma_f32_16x16x32_bf16 v[196:199], a[72:75], a[144:147], v[196:199]\n"
"v_add_f32 v105, v105, v17\n"
"v_mfma_f32_16x16x32_bf16 a[20:23], a[72:75], a[152:155], a[20:23]\n"
"s_add_u32 s34, s34, 8192\n"
"v_add_f32 v106, v106, v18\n"
"v_mfma_f32_16x16x32_bf16 a[20:23], a[76:79], a[156:159], a[20:23]\n"
"v_add_f32 v107, v107, v19\n"
"v_mfma_f32_16x16x32_bf16 a[16:19], a[68:71], a[156:159], a[16:19]\n"
"v_add_f32 v108, v108, v16\n"
"v_mfma_f32_16x16x32_bf16 a[16:19], a[64:67], a[152:155], a[16:19]\n"
"s_waitcnt vmcnt(12)\n"
"v_add_f32 v109, v109, v17\n"
"v_mfma_f32_16x16x32_bf16 v[200:203], a[80:83], a[96:99], v[200:203]\n"
"v_add_f32 v110, v110, v18\n"
"v_add_f32 v111, v111, v19\n"
"v_mfma_f32_16x16x32_bf16 v[200:203], a[84:87], a[100:103], v[200:203]\n"
"global_load_dwordx4 a[64:67], v0, s[34:35] offset:-4096 sc1\n"
"v_add_f32 v112, v112, v16\n"
"v_mfma_f32_16x16x32_bf16 v[228:231], a[92:95], a[100:103], v[228:231]\n"
"ds_read_b128 a[100:103], v3 offset:1024\n"
"v_add_f32 v113, v113, v17\n"
"v_mfma_f32_16x16x32_bf16 v[228:231], a[88:91], a[96:99], v[228:231]\n"
"s_add_u32 m0, s64, 20480\n"
"ds_read_b128 a[96:99], v3 offset:0\n"
"v_add_f32 v114, v114, v18\n"
"v_mfma_f32_16x16x32_bf16 v[232:235], a[88:91], a[104:107], v[232:235]\n"
"v_add_f32 v115, v115, v19\n"
"v_mfma_f32_16x16x32_bf16 v[232:235], a[92:95], a[108:111], v[232:235]\n"
"v_accvgpr_read_b32 v12, a8\n"
"v_mfma_f32_16x16x32_bf16 v[204:207], a[84:87], a[108:111], v[204:207]\n"
"ds_read_b128 a[108:111], v3 offset:3072\n"
"v_accvgpr_read_b32 v13, a9\n"
"v_mfma_f32_16x16x32_bf16 v[204:207], a[80:83], a[104:107], v[204:207]\n"
"ds_read_b128 a[104:107], v3 offset:2048\n"
"v_accvgpr_read_b32 v14, a10\n"
"v_mfma_f32_16x16x32_bf16 v[208:211], a[80:83], a[112:115], v[208:211]\n"
"v_accvgpr_read_b32 v15, a11\n"
"v_mfma_f32_16x16x32_bf16 v[208:211], a[84:87], a[116:119], v[208:211]\n"
"global_load_dwordx4 a[68:71], v0, s[34:35] offset:-3072 sc1\n"
"v_add_f32 v12, v12, v16\n"
"v_mfma_f32_16x16x32_bf16 v[236:239], a[92:95], a[116:119], v[236:239]\n"
"ds_read_b128 a[116:119], v3 offset:5120\n"
"v_add_f32 v13, v13, v17\n"
"v_mfma_f32_16x16x32_bf16 v[236:239], a[88:91], a[112:115], v[236:239]\n"
"ds_read_b128 a[112:115], v3 offset:4096\n"
"v_add_f32 v14, v14, v18\n"
"v_mfma_f32_16x16x32_bf16 v[240:243], a[88:91], a[120:123], v[240:243]\n"
"v_add_f32 v15, v15, v19\n"
"v_accvgpr_write_b32 a8, v12\n"
"v_mfma_f32_16x16x32_bf16 v[240:243], a[92:95], a[124:127], v[240:243]\n"
"v_accvgpr_write_b32 a9, v13\n"
"v_mfma_f32_16x16x32_bf16 v[212:215], a[84:87], a[124:127], v[212:215]\n"
"ds_read_b128 a[124:127], v3 offset:7168\n"
"v_accvgpr_write_b32 a10, v14\n"
"v_mfma_f32_16x16x32_bf16 v[212:215], a[80:83], a[120:123], v[212:215]\n"
"ds_read_b128 a[120:123], v3 offset:6144\n"
"v_accvgpr_write_b32 a11, v15\n"
"v_mfma_f32_16x16x32_bf16 v[216:219], a[80:83], a[128:131], v[216:219]\n"
"v_mfma_f32_16x16x32_bf16 v[216:219], a[84:87], a[132:135], v[216:219]\n"
"global_load_dwordx4 a[72:75], v0, s[34:35] offset:-2048 sc1\n"
"v_mfma_f32_16x16x32_bf16 v[244:247], a[92:95], a[132:135], v[244:247]\n"
"ds_read_b128 a[132:135], v3 offset:9216\n"
"v_mfma_f32_16x16x32_bf16 v[244:247], a[88:91], a[128:131], v[244:247]\n"
"ds_read_b128 a[128:131], v3 offset:8192\n"
"v_mfma_f32_16x16x32_bf16 v[248:251], a[88:91], a[136:139], v[248:251]\n"
"buffer_load_dwordx4 v1, s[48:51], s75 offen offset:0 sc1 lds\n"
"v_mfma_f32_16x16x32_bf16 v[248:251], a[92:95], a[140:143], v[248:251]\n"
"v_mfma_f32_16x16x32_bf16 v[220:223], a[84:87], a[140:143], v[220:223]\n"
"ds_read_b128 a[140:143], v3 offset:11264\n"
"v_mfma_f32_16x16x32_bf16 v[220:223], a[80:83], a[136:139], v[220:223]\n"
"ds_read_b128 a[136:139], v3 offset:10240\n"
"v_mfma_f32_16x16x32_bf16 v[224:227], a[80:83], a[144:147], v[224:227]\n"
"v_mfma_f32_16x16x32_bf16 v[224:227], a[84:87], a[148:151], v[224:227]\n"
"global_load_dwordx4 a[76:79], v0, s[34:35] offset:-1024 sc1\n"
"v_mfma_f32_16x16x32_bf16 v[252:255], a[92:95], a[148:151], v[252:255]\n"
"ds_read_b128 a[148:151], v3 offset:13312\n"
"v_mfma_f32_16x16x32_bf16 v[252:255], a[88:91], a[144:147], v[252:255]\n"
"ds_read_b128 a[144:147], v3 offset:12288\n"
"v_mfma_f32_16x16x32_bf16 a[28:31], a[88:91], a[152:155], a[28:31]\n"
"v_mfma_f32_16x16x32_bf16 a[28:31], a[92:95], a[156:159], a[28:31]\n"
"v_mfma_f32_16x16x32_bf16 a[24:27], a[84:87], a[156:159], a[24:27]\n"
"ds_read_b128 a[156:159], v3 offset:15360\n"
"v_mfma_f32_16x16x32_bf16 a[24:27], a[80:83], a[152:155], a[24:27]\n"
"s_waitcnt vmcnt(11) lgkmcnt(11)\n"
"ds_read_b128 a[152:155], v3 offset:14336\n"
"v_mfma_f32_16x16x32_bf16 v[32:35], a[32:35], a[96:99], v[32:35]\n"
"v_mfma_f32_16x16x32_bf16 v[32:35], a[36:39], a[100:103], v[32:35]\n"
"global_load_dwordx4 a[80:83], v0, s[34:35] offset:0 sc1\n"
"v_mfma_f32_16x16x32_bf16 v[60:63], a[44:47], a[100:103], v[60:63]\n"
"v_mfma_f32_16x16x32_bf16 v[60:63], a[40:43], a[96:99], v[60:63]\n"
"buffer_load_dwordx4 v1, s[48:51], s75 offen offset:1024 sc1 lds\n"
"v_mfma_f32_16x16x32_bf16 v[64:67], a[40:43], a[104:107], v[64:67]\n"
"v_mfma_f32_16x16x32_bf16 v[64:67], a[44:47], a[108:111], v[64:67]\n"
"v_mfma_f32_16x16x32_bf16 v[36:39], a[36:39], a[108:111], v[36:39]\n"
"v_mfma_f32_16x16x32_bf16 v[36:39], a[32:35], a[104:107], v[36:39]\n"
"s_waitcnt lgkmcnt(8)\n"
"v_mfma_f32_16x16x32_bf16 v[40:43], a[32:35], a[112:115], v[40:43]\n"
"v_mfma_f32_16x16x32_bf16 v[40:43], a[36:39], a[116:119], v[40:43]\n"
"global_load_dwordx4 a[84:87], v0, s[34:35] offset:1024 sc1\n"
"v_mfma_f32_16x16x32_bf16 v[68:71], a[44:47], a[116:119], v[68:71]\n"
"v_mfma_f32_16x16x32_bf16 v[68:71], a[40:43], a[112:115], v[68:71]\n"
"v_mfma_f32_16x16x32_bf16 v[72:75], a[40:43], a[120:123], v[72:75]\n"
"v_mfma_f32_16x16x32_bf16 v[72:75], a[44:47], a[124:127], v[72:75]\n"
"v_mfma_f32_16x16x32_bf16 v[44:47], a[36:39], a[124:127], v[44:47]\n"
"v_mfma_f32_16x16x32_bf16 v[44:47], a[32:35], a[120:123], v[44:47]\n"
"s_waitcnt lgkmcnt(4)\n"
"v_mfma_f32_16x16x32_bf16 v[48:51], a[32:35], a[128:131], v[48:51]\n"
"v_mfma_f32_16x16x32_bf16 v[48:51], a[36:39], a[132:135], v[48:51]\n"
"global_load_dwordx4 a[88:91], v0, s[34:35] offset:2048 sc1\n"
"v_mfma_f32_16x16x32_bf16 v[76:79], a[44:47], a[132:135], v[76:79]\n"
"v_mfma_f32_16x16x32_bf16 v[76:79], a[40:43], a[128:131], v[76:79]\n"
"buffer_load_dwordx4 v1, s[48:51], s75 offen offset:2048 sc1 lds\n"
"v_mfma_f32_16x16x32_bf16 v[80:83], a[40:43], a[136:139], v[80:83]\n"
"v_mfma_f32_16x16x32_bf16 v[80:83], a[44:47], a[140:143], v[80:83]\n"
"v_mfma_f32_16x16x32_bf16 v[52:55], a[36:39], a[140:143], v[52:55]\n"
"v_mfma_f32_16x16x32_bf16 v[52:55], a[32:35], a[136:139], v[52:55]\n"
"s_waitcnt lgkmcnt(0)\n"
"v_mfma_f32_16x16x32_bf16 v[56:59], a[32:35], a[144:147], v[56:59]\n"
"v_mfma_f32_16x16x32_bf16 v[56:59], a[36:39], a[148:151], v[56:59]\n"
"global_load_dwordx4 a[92:95], v0, s[34:35] offset:3072 sc1\n"
"v_mfma_f32_16x16x32_bf16 v[84:87], a[44:47], a[148:151], v[84:87]\n"
"v_mfma_f32_16x16x32_bf16 v[84:87], a[40:43], a[144:147], v[84:87]\n"
"v_mfma_f32_16x16x32_bf16 a[4:7], a[40:43], a[152:155], a[4:7]\n"
"s_add_u32 s34, s34, 8192\n"
"v_mfma_f32_16x16x32_bf16 a[4:7], a[44:47], a[156:159], a[4:7]\n"
"v_mfma_f32_16x16x32_bf16 a[0:3], a[36:39], a[156:159], a[0:3]\n"
"v_mfma_f32_16x16x32_bf16 a[0:3], a[32:35], a[152:155], a[0:3]\n"
"s_waitcnt vmcnt(11)\n"
"v_mfma_f32_16x16x32_bf16 v[88:91], a[48:51], a[96:99], v[88:91]\n"
"v_mfma_f32_16x16x32_bf16 v[88:91], a[52:55], a[100:103], v[88:91]\n"
"global_load_dwordx4 a[32:35], v0, s[34:35] offset:-4096 sc1\n"
"v_mfma_f32_16x16x32_bf16 v[116:119], a[60:63], a[100:103], v[116:119]\n"
"v_mfma_f32_16x16x32_bf16 v[116:119], a[56:59], a[96:99], v[116:119]\n"
"buffer_load_dwordx4 v1, s[48:51], s75 offen offset:3072 sc1 lds\n"
"v_mfma_f32_16x16x32_bf16 v[120:123], a[56:59], a[104:107], v[120:123]\n"
"v_mfma_f32_16x16x32_bf16 v[120:123], a[60:63], a[108:111], v[120:123]\n"
"v_mfma_f32_16x16x32_bf16 v[92:95], a[52:55], a[108:111], v[92:95]\n"
"v_mfma_f32_16x16x32_bf16 v[92:95], a[48:51], a[104:107], v[92:95]\n"
"v_mfma_f32_16x16x32_bf16 v[96:99], a[48:51], a[112:115], v[96:99]\n"
"v_mfma_f32_16x16x32_bf16 v[96:99], a[52:55], a[116:119], v[96:99]\n"
"global_load_dwordx4 a[36:39], v0, s[34:35] offset:-3072 sc1\n"
"v_mfma_f32_16x16x32_bf16 v[124:127], a[60:63], a[116:119], v[124:127]\n"
"v_mfma_f32_16x16x32_bf16 v[124:127], a[56:59], a[112:115], v[124:127]\n"
"v_mfma_f32_16x16x32_bf16 v[128:131], a[56:59], a[120:123], v[128:131]\n"
"v_mfma_f32_16x16x32_bf16 v[128:131], a[60:63], a[124:127], v[128:131]\n"
"v_mfma_f32_16x16x32_bf16 v[100:103], a[52:55], a[124:127], v[100:103]\n"
"v_mfma_f32_16x16x32_bf16 v[100:103], a[48:51], a[120:123], v[100:103]\n"
"v_mfma_f32_16x16x32_bf16 v[104:107], a[48:51], a[128:131], v[104:107]\n"
"v_mfma_f32_16x16x32_bf16 v[104:107], a[52:55], a[132:135], v[104:107]\n"
"global_load_dwordx4 a[40:43], v0, s[34:35] offset:-2048 sc1\n"
"v_mfma_f32_16x16x32_bf16 v[132:135], a[60:63], a[132:135], v[132:135]\n"
"v_mfma_f32_16x16x32_bf16 v[132:135], a[56:59], a[128:131], v[132:135]\n"
"s_add_u32 m0, s64, 24576\n"
"v_mfma_f32_16x16x32_bf16 v[136:139], a[56:59], a[136:139], v[136:139]\n"
"v_mfma_f32_16x16x32_bf16 v[136:139], a[60:63], a[140:143], v[136:139]\n"
"v_mfma_f32_16x16x32_bf16 v[108:111], a[52:55], a[140:143], v[108:111]\n"
"v_mfma_f32_16x16x32_bf16 v[108:111], a[48:51], a[136:139], v[108:111]\n"
"v_mfma_f32_16x16x32_bf16 v[112:115], a[48:51], a[144:147], v[112:115]\n"
"v_mfma_f32_16x16x32_bf16 v[112:115], a[52:55], a[148:151], v[112:115]\n"
"global_load_dwordx4 a[44:47], v0, s[34:35] offset:-1024 sc1\n"
"v_mfma_f32_16x16x32_bf16 v[140:143], a[60:63], a[148:151], v[140:143]\n"
"v_mfma_f32_16x16x32_bf16 v[140:143], a[56:59], a[144:147], v[140:143]\n"
"v_mfma_f32_16x16x32_bf16 a[12:15], a[56:59], a[152:155], a[12:15]\n"
"v_mfma_f32_16x16x32_bf16 a[12:15], a[60:63], a[156:159], a[12:15]\n"
"v_mfma_f32_16x16x32_bf16 a[8:11], a[52:55], a[156:159], a[8:11]\n"
"v_mfma_f32_16x16x32_bf16 a[8:11], a[48:51], a[152:155], a[8:11]\n"
"s_waitcnt vmcnt(11)\n"
"v_mfma_f32_16x16x32_bf16 v[144:147], a[64:67], a[96:99], v[144:147]\n"
"v_mfma_f32_16x16x32_bf16 v[144:147], a[68:71], a[100:103], v[144:147]\n"
"global_load_dwordx4 a[48:51], v0, s[34:35] offset:0 sc1\n"
"v_mfma_f32_16x16x32_bf16 v[172:175], a[76:79], a[100:103], v[172:175]\n"
"v_mfma_f32_16x16x32_bf16 v[172:175], a[72:75], a[96:99], v[172:175]\n"
"buffer_load_dwordx4 v0, s[48:51], s69 offen offset:0 sc1 lds\n"
"v_mfma_f32_16x16x32_bf16 v[176:179], a[72:75], a[104:107], v[176:179]\n"
"v_mfma_f32_16x16x32_bf16 v[176:179], a[76:79], a[108:111], v[176:179]\n"
"v_mfma_f32_16x16x32_bf16 v[148:151], a[68:71], a[108:111], v[148:151]\n"
"v_mfma_f32_16x16x32_bf16 v[148:151], a[64:67], a[104:107], v[148:151]\n"
"v_mfma_f32_16x16x32_bf16 v[152:155], a[64:67], a[112:115], v[152:155]\n"
"v_lshlrev_b32 v20, 16, v22\n"
"v_mfma_f32_16x16x32_bf16 v[152:155], a[68:71], a[116:119], v[152:155]\n"
"global_load_dwordx4 a[52:55], v0, s[34:35] offset:1024 sc1\n"
"v_and_b32 v21, s63, v22\n"
"v_mfma_f32_16x16x32_bf16 v[180:183], a[76:79], a[116:119], v[180:183]\n"
"v_lshlrev_b32 v22, 16, v23\n"
"v_mfma_f32_16x16x32_bf16 v[180:183], a[72:75], a[112:115], v[180:183]\n"
"v_and_b32 v23, s63, v23\n"
"v_mfma_f32_16x16x32_bf16 v[184:187], a[72:75], a[120:123], v[184:187]\n"
"v_add_f32 v116, v116, v20\n"
"v_mfma_f32_16x16x32_bf16 v[184:187], a[76:79], a[124:127], v[184:187]\n"
"v_add_f32 v117, v117, v21\n"
"v_add_f32 v118, v118, v22\n"
"v_mfma_f32_16x16x32_bf16 v[156:159], a[68:71], a[124:127], v[156:159]\n"
"v_add_f32 v119, v119, v23\n"
"v_mfma_f32_16x16x32_bf16 v[156:159], a[64:67], a[120:123], v[156:159]\n"
"v_add_f32 v120, v120, v20\n"
"v_mfma_f32_16x16x32_bf16 v[160:163], a[64:67], a[128:131], v[160:163]\n"
"v_add_f32 v121, v121, v21\n"
"v_mfma_f32_16x16x32_bf16 v[160:163], a[68:71], a[132:135], v[160:163]\n"
"global_load_dwordx4 a[56:59], v0, s[34:35] offset:2048 sc1\n"
"v_add_f32 v122, v122, v22\n"
"v_mfma_f32_16x16x32_bf16 v[188:191], a[76:79], a[132:135], v[188:191]\n"
"v_add_f32 v123, v123, v23\n"
"v_add_f32 v124, v124, v20\n"
"v_mfma_f32_16x16x32_bf16 v[188:191], a[72:75], a[128:131], v[188:191]\n"
"buffer_load_dwordx4 v0, s[48:51], s69 offen offset:1024 sc1 lds\n"
"v_mfma_f32_16x16x32_bf16 v[192:195], a[72:75], a[136:139], v[192:195]\n"
"v_add_f32 v125, v125, v21\n"
"v_mfma_f32_16x16x32_bf16 v[192:195], a[76:79], a[140:143], v[192:195]\n"
"v_add_f32 v126, v126, v22\n"
"v_mfma_f32_16x16x32_bf16 v[164:167], a[68:71], a[140:143], v[164:167]\n"
"v_add_f32 v127, v127, v23\n"
"v_mfma_f32_16x16x32_bf16 v[164:167], a[64:67], a[136:139], v[164:167]\n"
"L_st7375:\n"
"s_cbranch_execz L_retB\n"
"v_add_f32 v128, v128, v20\n"
"v_add_f32 v129, v129, v21\n"
"v_mfma_f32_16x16x32_bf16 v[168:171], a[64:67], a[144:147], v[168:171]\n"
"v_add_f32 v130, v130, v22\n"
"v_mfma_f32_16x16x32_bf16 v[168:171], a[68:71], a[148:151], v[168:171]\n"
"global_load_dwordx4 a[60:63], v0, s[34:35] offset:3072 sc1\n"
"v_add_f32 v131, v131, v23\n"
"v_mfma_f32_16x16x32_bf16 v[196:199], a[76:79], a[148:151], v[196:199]\n"
"v_add_f32 v132, v132, v20\n"
"v_mfma_f32_16x16x32_bf16 v[196:199], a[72:75], a[144:147], v[196:199]\n"
"v_add_f32 v133, v133, v21\n"
"v_mfma_f32_16x16x32_bf16 a[20:23], a[72:75], a[152:155], a[20:23]\n"
"s_add_u32 s34, s34, 8192\n"
"v_add_f32 v134, v134, v22\n"
"v_mfma_f32_16x16x32_bf16 a[20:23], a[76:79], a[156:159], a[20:23]\n"
"v_add_f32 v135, v135, v23\n"
"v_mfma_f32_16x16x32_bf16 a[16:19], a[68:71], a[156:159], a[16:19]\n"
"v_add_f32 v136, v136, v20\n"
"v_mfma_f32_16x16x32_bf16 a[16:19], a[64:67], a[152:155], a[16:19]\n"
"s_waitcnt vmcnt(11)\n"
"v_add_f32 v137, v137, v21\n"
"v_mfma_f32_16x16x32_bf16 v[200:203], a[80:83], a[96:99], v[200:203]\n"
"v_add_f32 v138, v138, v22\n"
"v_add_f32 v139, v139, v23\n"
"v_mfma_f32_16x16x32_bf16 v[200:203], a[84:87], a[100:103], v[200:203]\n"
"global_load_dwordx4 a[64:67], v0, s[34:35] offset:-4096 sc1\n"
"v_add_f32 v140, v140, v20\n"
"v_mfma_f32_16x16x32_bf16 v[228:231], a[92:95], a[100:103], v[228:231]\n"
"ds_read_b128 a[100:103], v4 offset:1024\n"
"v_add_f32 v141, v141, v21\n"
"v_mfma_f32_16x16x32_bf16 v[228:231], a[88:91], a[96:99], v[228:231]\n"
"ds_read_b128 a[96:99], v4 offset:0\n"
"v_add_f32 v142, v142, v22\n"
"v_mfma_f32_16x16x32_bf16 v[232:235], a[88:91], a[104:107], v[232:235]\n"
"buffer_load_dwordx4 v0, s[48:51], s69 offen offset:2048 sc1 lds\n"
"v_mfma_f32_16x16x32_bf16 v[232:235], a[92:95], a[108:111], v[232:235]\n"
"v_add_f32 v143, v143, v23\n"
"v_accvgpr_read_b32 v12, a12\n"
"v_mfma_f32_16x16x32_bf16 v[204:207], a[84:87], a[108:111], v[204:207]\n"
"ds_read_b128 a[108:111], v4 offset:3072\n"
"v_accvgpr_read_b32 v13, a13\n"
"v_mfma_f32_16x16x32_bf16 v[204:207], a[80:83], a[104:107], v[204:207]\n"
"ds_read_b128 a[104:107], v4 offset:2048\n"
"v_accvgpr_read_b32 v14, a14\n"
"v_mfma_f32_16x16x32_bf16 v[208:211], a[80:83], a[112:115], v[208:211]\n"
"v_accvgpr_read_b32 v15, a15\n"
"v_mfma_f32_16x16x32_bf16 v[208:211], a[84:87], a[116:119], v[208:211]\n"
"global_load_dwordx4 a[68:71], v0, s[34:35] offset:-3072 sc1\n"
"v_add_f32 v12, v12, v20\n"
"v_mfma_f32_16x16x32_bf16 v[236:239], a[92:95], a[116:119], v[236:239]\n"
"ds_read_b128 a[116:119], v4 offset:5120\n"
"v_add_f32 v13, v13, v21\n"
"v_mfma_f32_16x16x32_bf16 v[236:239], a[88:91], a[112:115], v[236:239]\n"
"ds_read_b128 a[112:115], v4 offset:4096\n"
"v_add_f32 v14, v14, v22\n"
"v_mfma_f32_16x16x32_bf16 v[240:243], a[88:91], a[120:123], v[240:243]\n"
"v_add_f32 v15, v15, v23\n"
"v_accvgpr_write_b32 a12, v12\n"
"v_mfma_f32_16x16x32_bf16 v[240:243], a[92:95], a[124:127], v[240:243]\n"
"v_accvgpr_write_b32 a13, v13\n"
"v_mfma_f32_16x16x32_bf16 v[212:215], a[84:87], a[124:127], v[212:215]\n"
"ds_read_b128 a[124:127], v4 offset:7168\n"
"v_accvgpr_write_b32 a14, v14\n"
"v_mfma_f32_16x16x32_bf16 v[212:215], a[80:83], a[120:123], v[212:215]\n"
"ds_read_b128 a[120:123], v4 offset:6144\n"
"v_accvgpr_write_b32 a15, v15\n"
"v_mfma_f32_16x16x32_bf16 v[216:219], a[80:83], a[128:131], v[216:219]\n"
"v_mfma_f32_16x16x32_bf16 v[216:219], a[84:87], a[132:135], v[216:219]\n"
"global_load_dwordx4 a[72:75], v0, s[34:35] offset:-2048 sc1\n"
"v_mfma_f32_16x16x32_bf16 v[244:247], a[92:95], a[132:135], v[244:247]\n"
"ds_read_b128 a[132:135], v4 offset:9216\n"
"v_mfma_f32_16x16x32_bf16 v[244:247], a[88:91], a[128:131], v[244:247]\n"
"ds_read_b128 a[128:131], v4 offset:8192\n"
"v_mfma_f32_16x16x32_bf16 v[248:251], a[88:91], a[136:139], v[248:251]\n"
"buffer_load_dwordx4 v0, s[48:51], s69 offen offset:3072 sc1 lds\n"
"v_mfma_f32_16x16x32_bf16 v[248:251], a[92:95], a[140:143], v[248:251]\n"
"v_mfma_f32_16x16x32_bf16 v[220:223], a[84:87], a[140:143], v[220:223]\n"
"ds_read_b128 a[140:143], v4 offset:11264\n"
"v_mfma_f32_16x16x32_bf16 v[220:223], a[80:83], a[136:139], v[220:223]\n"
"ds_read_b128 a[136:139], v4 offset:10240\n"
"v_mfma_f32_16x16x32_bf16 v[224:227], a[80:83], a[144:147], v[224:227]\n"
"v_mfma_f32_16x16x32_bf16 v[224:227], a[84:87], a[148:151], v[224:227]\n"
"global_load_dwordx4 a[76:79], v0, s[34:35] offset:-1024 sc1\n"
"v_mfma_f32_16x16x32_bf16 v[252:255], a[92:95], a[148:151], v[252:255]\n"
"ds_read_b128 a[148:151], v4 offset:13312\n"
"v_mfma_f32_16x16x32_bf16 v[252:255], a[88:91], a[144:147], v[252:255]\n"
"ds_read_b128 a[144:147], v4 offset:12288\n"
"v_mfma_f32_16x16x32_bf16 a[28:31], a[88:91], a[152:155], a[28:31]\n"
"v_mfma_f32_16x16x32_bf16 a[28:31], a[92:95], a[156:159], a[28:31]\n"
"v_mfma_f32_16x16x32_bf16 a[24:27], a[84:87], a[156:159], a[24:27]\n"
"ds_read_b128 a[156:159], v4 offset:15360\n"
"v_mfma_f32_16x16x32_bf16 a[24:27], a[80:83], a[152:155], a[24:27]\n"
"s_waitcnt vmcnt(12) lgkmcnt(11)\n"
"ds_read_b128 a[152:155], v4 offset:14336\n"
"v_mfma_f32_16x16x32_bf16 v[32:35], a[32:35], a[96:99], v[32:35]\n"
"v_mfma_f32_16x16x32_bf16 v[32:35], a[36:39], a[100:103], v[32:35]\n"
"global_load_dwordx4 a[80:83], v0, s[34:35] offset:0 sc1\n"
"v_lshrrev_b32 v16, 4, v0\n"
"v_mfma_f32_16x16x32_bf16 v[60:63], a[44:47], a[100:103], v[60:63]\n"
"v_and_b32 v16, 0x30, v16\n"
"v_mfma_f32_16x16x32_bf16 v[60:63], a[40:43], a[96:99], v[60:63]\n"
"s_add_u32 m0, s64, 28672\n"
"buffer_load_dwordx2 v[18:19], v16, s[44:47], s61 offen offset:128\n"
"v_mfma_f32_16x16x32_bf16 v[64:67], a[40:43], a[104:107], v[64:67]\n"
"v_mfma_f32_16x16x32_bf16 v[64:67], a[44:47], a[108:111], v[64:67]\n"
"v_mfma_f32_16x16x32_bf16 v[36:39], a[36:39], a[108:111], v[36:39]\n"
"buffer_load_dwordx4 v[12:15], v0, s[56:59], %25 offen sc1\n"
"v_mfma_f32_16x16x32_bf16 v[36:39], a[32:35], a[104:107], v[36:39]\n"
"s_waitcnt lgkmcnt(8)\n"
"v_mfma_f32_16x16x32_bf16 v[40:43], a[32:35], a[112:115], v[40:43]\n"
"v_mfma_f32_16x16x32_bf16 v[40:43], a[36:39], a[116:119], v[40:43]\n"
"global_load_dwordx4 a[84:87], v0, s[34:35] offset:1024 sc1\n"
"v_lshrrev_b32 v20, 4, v0\n"
"v_mfma_f32_16x16x32_bf16 v[68:71], a[44:47], a[116:119], v[68:71]\n"
"v_and_b32 v20, 0x30, v20\n"
"v_mfma_f32_16x16x32_bf16 v[68:71], a[40:43], a[112:115], v[68:71]\n"
"buffer_load_dwordx2 v[22:23], v20, s[44:47], s61 offen offset:136\n"
"v_mfma_f32_16x16x32_bf16 v[72:75], a[40:43], a[120:123], v[72:75]\n"
"v_mfma_f32_16x16x32_bf16 v[72:75], a[44:47], a[124:127], v[72:75]\n"
"v_mfma_f32_16x16x32_bf16 v[44:47], a[36:39], a[124:127], v[44:47]\n"
"v_mfma_f32_16x16x32_bf16 v[44:47], a[32:35], a[120:123], v[44:47]\n"
"s_waitcnt lgkmcnt(4)\n"
"v_mfma_f32_16x16x32_bf16 v[48:51], a[32:35], a[128:131], v[48:51]\n"
"v_mfma_f32_16x16x32_bf16 v[48:51], a[36:39], a[132:135], v[48:51]\n"
"global_load_dwordx4 a[88:91], v0, s[34:35] offset:2048 sc1\n"
"v_lshrrev_b32 v24, 4, v0\n"
"v_mfma_f32_16x16x32_bf16 v[76:79], a[44:47], a[132:135], v[76:79]\n"
"v_and_b32 v24, 0x30, v24\n"
"v_mfma_f32_16x16x32_bf16 v[76:79], a[40:43], a[128:131], v[76:79]\n"
"buffer_load_dwordx4 v1, s[48:51], s69 offen offset:0 sc1 lds\n"
"buffer_load_dwordx2 v[26:27], v24, s[44:47], s61 offen offset:192\n"
"v_mfma_f32_16x16x32_bf16 v[80:83], a[40:43], a[136:139], v[80:83]\n"
"v_mfma_f32_16x16x32_bf16 v[80:83], a[44:47], a[140:143], v[80:83]\n"
"v_mfma_f32_16x16x32_bf16 v[52:55], a[36:39], a[140:143], v[52:55]\n"
"v_mfma_f32_16x16x32_bf16 v[52:55], a[32:35], a[136:139], v[52:55]\n"
"s_waitcnt lgkmcnt(0)\n"
"v_mfma_f32_16x16x32_bf16 v[56:59], a[32:35], a[144:147], v[56:59]\n"
"v_mfma_f32_16x16x32_bf16 v[56:59], a[36:39], a[148:151], v[56:59]\n"
"global_load_dwordx4 a[92:95], v0, s[34:35] offset:3072 sc1\n"
"v_lshrrev_b32 v28, 4, v0\n"
"v_mfma_f32_16x16x32_bf16 v[84:87], a[44:47], a[148:151], v[84:87]\n"
"v_and_b32 v28, 0x30, v28\n"
"v_mfma_f32_16x16x32_bf16 v[84:87], a[40:43], a[144:147], v[84:87]\n"
"buffer_load_dwordx2 v[30:31], v28, s[44:47], s61 offen offset:200\n"
"v_mfma_f32_16x16x32_bf16 a[4:7], a[40:43], a[152:155], a[4:7]\n"
"s_add_u32 s34, s34, 8192\n"
"v_mfma_f32_16x16x32_bf16 a[4:7], a[44:47], a[156:159], a[4:7]\n"
"v_mfma_f32_16x16x32_bf16 a[0:3], a[36:39], a[156:159], a[0:3]\n"
"v_mfma_f32_16x16x32_bf16 a[0:3], a[32:35], a[152:155], a[0:3]\n"
"s_waitcnt vmcnt(16)\n"
"v_mfma_f32_16x16x32_bf16 v[88:91], a[48:51], a[96:99], v[88:91]\n"
"v_mfma_f32_16x16x32_bf16 v[88:91], a[52:55], a[100:103], v[88:91]\n"
"global_load_dwordx4 a[32:35], v0, s[34:35] offset:-4096 sc1\n"
"v_mfma_f32_16x16x32_bf16 v[116:119], a[60:63], a[100:103], v[116:119]\n"
"v_mfma_f32_16x16x32_bf16 v[116:119], a[56:59], a[96:99], v[116:119]\n"
"buffer_load_dwordx4 v1, s[48:51], s69 offen offset:1024 sc1 lds\n"
"v_mfma_f32_16x16x32_bf16 v[120:123], a[56:59], a[104:107], v[120:123]\n"
"v_mfma_f32_16x16x32_bf16 v[120:123], a[60:63], a[108:111], v[120:123]\n"
"v_mfma_f32_16x16x32_bf16 v[92:95], a[52:55], a[108:111], v[92:95]\n"
"v_mfma_f32_16x16x32_bf16 v[92:95], a[48:51], a[104:107], v[92:95]\n"
"v_mfma_f32_16x16x32_bf16 v[96:99], a[48:51], a[112:115], v[96:99]\n"
"v_mfma_f32_16x16x32_bf16 v[96:99], a[52:55], a[116:119], v[96:99]\n"
"global_load_dwordx4 a[36:39], v0, s[34:35] offset:-3072 sc1\n"
"v_mfma_f32_16x16x32_bf16 v[124:127], a[60:63], a[116:119], v[124:127]\n"
"v_mfma_f32_16x16x32_bf16 v[124:127], a[56:59], a[112:115], v[124:127]\n"
"v_mfma_f32_16x16x32_bf16 v[128:131], a[56:59], a[120:123], v[128:131]\n"
"v_mfma_f32_16x16x32_bf16 v[128:131], a[60:63], a[124:127], v[128:131]\n"
"v_mfma_f32_16x16x32_bf16 v[100:103], a[52:55], a[124:127], v[100:103]\n"
"v_mfma_f32_16x16x32_bf16 v[100:103], a[48:51], a[120:123], v[100:103]\n"
"v_mfma_f32_16x16x32_bf16 v[104:107], a[48:51], a[128:131], v[104:107]\n"
"v_mfma_f32_16x16x32_bf16 v[104:107], a[52:55], a[132:135], v[104:107]\n"
"global_load_dwordx4 a[40:43], v0, s[34:35] offset:-2048 sc1\n"
"v_mfma_f32_16x16x32_bf16 v[132:135], a[60:63], a[132:135], v[132:135]\n"
"v_mfma_f32_16x16x32_bf16 v[132:135], a[56:59], a[128:131], v[132:135]\n"
"buffer_load_dwordx4 v1, s[48:51], s69 offen offset:2048 sc1 lds\n"
"v_mfma_f32_16x16x32_bf16 v[136:139], a[56:59], a[136:139], v[136:139]\n"
"v_mfma_f32_16x16x32_bf16 v[136:139], a[60:63], a[140:143], v[136:139]\n"
"v_mfma_f32_16x16x32_bf16 v[108:111], a[52:55], a[140:143], v[108:111]\n"
"v_mfma_f32_16x16x32_bf16 v[108:111], a[48:51], a[136:139], v[108:111]\n"
"v_mfma_f32_16x16x32_bf16 v[112:115], a[48:51], a[144:147], v[112:115]\n"
"v_mfma_f32_16x16x32_bf16 v[112:115], a[52:55], a[148:151], v[112:115]\n"
"global_load_dwordx4 a[44:47], v0, s[34:35] offset:-1024 sc1\n"
"v_mfma_f32_16x16x32_bf16 v[140:143], a[60:63], a[148:151], v[140:143]\n"
"v_mfma_f32_16x16x32_bf16 v[140:143], a[56:59], a[144:147], v[140:143]\n"
"v_mfma_f32_16x16x32_bf16 a[12:15], a[56:59], a[152:155], a[12:15]\n"
"v_mfma_f32_16x16x32_bf16 a[12:15], a[60:63], a[156:159], a[12:15]\n"
"v_mfma_f32_16x16x32_bf16 a[8:11], a[52:55], a[156:159], a[8:11]\n"
"v_mfma_f32_16x16x32_bf16 a[8:11], a[48:51], a[152:155], a[8:11]\n"
"s_waitcnt vmcnt(16)\n"
"v_mfma_f32_16x16x32_bf16 v[144:147], a[64:67], a[96:99], v[144:147]\n"
"v_mfma_f32_16x16x32_bf16 v[144:147], a[68:71], a[100:103], v[144:147]\n"
"global_load_dwordx4 a[48:51], v0, s[34:35] offset:0 sc1\n"
"v_mfma_f32_16x16x32_bf16 v[172:175], a[76:79], a[100:103], v[172:175]\n"
"v_mfma_f32_16x16x32_bf16 v[172:175], a[72:75], a[96:99], v[172:175]\n"
"buffer_load_dwordx4 v1, s[48:51], s69 offen offset:3072 sc1 lds\n"
"v_mfma_f32_16x16x32_bf16 v[176:179], a[72:75], a[104:107], v[176:179]\n"
"v_mfma_f32_16x16x32_bf16 v[176:179], a[76:79], a[108:111], v[176:179]\n"
"v_mfma_f32_16x16x32_bf16 v[148:151], a[68:71], a[108:111], v[148:151]\n"
"v_mfma_f32_16x16x32_bf16 v[148:151], a[64:67], a[104:107], v[148:151]\n"
"v_mfma_f32_16x16x32_bf16 v[152:155], a[64:67], a[112:115], v[152:155]\n"
"v_mfma_f32_16x16x32_bf16 v[152:155], a[68:71], a[116:119], v[152:155]\n"
"global_load_dwordx4 a[52:55], v0, s[34:35] offset:1024 sc1\n"
"v_mfma_f32_16x16x32_bf16 v[180:183], a[76:79], a[116:119], v[180:183]\n"
"v_mfma_f32_16x16x32_bf16 v[180:183], a[72:75], a[112:115], v[180:183]\n"
"v_mfma_f32_16x16x32_bf16 v[184:187], a[72:75], a[120:123], v[184:187]\n"
"v_mfma_f32_16x16x32_bf16 v[184:187], a[76:79], a[124:127], v[184:187]\n"
"v_mfma_f32_16x16x32_bf16 v[156:159], a[68:71], a[124:127], v[156:159]\n"
"v_mfma_f32_16x16x32_bf16 v[156:159], a[64:67], a[120:123], v[156:159]\n"
"v_mfma_f32_16x16x32_bf16 v[160:163], a[64:67], a[128:131], v[160:163]\n"
"v_mfma_f32_16x16x32_bf16 v[160:163], a[68:71], a[132:135], v[160:163]\n"
"global_load_dwordx4 a[56:59], v0, s[34:35] offset:2048 sc1\n"
"v_mfma_f32_16x16x32_bf16 v[188:191], a[76:79], a[132:135], v[188:191]\n"
"v_mfma_f32_16x16x32_bf16 v[188:191], a[72:75], a[128:131], v[188:191]\n"
"v_mfma_f32_16x16x32_bf16 v[192:195], a[72:75], a[136:139], v[192:195]\n"
"s_cmp_lg_u32 s29, 0\n"
"s_cselect_b32 s60, s31, s60\n"
"s_cselect_b32 s30, 0, s30\n"
"v_mfma_f32_16x16x32_bf16 v[192:195], a[76:79], a[140:143], v[192:195]\n"
"v_mfma_f32_16x16x32_bf16 v[164:167], a[68:71], a[140:143], v[164:167]\n"
"s_cmp_lg_u32 s29, 0\n"
"s_cselect_b32 s31, s75, s72\n"
"v_mfma_f32_16x16x32_bf16 v[164:167], a[64:67], a[136:139], v[164:167]\n"
"s_sub_u32 s52, s52, s72\n"
"s_subb_u32 s53, s53, 0\n"
"v_mfma_f32_16x16x32_bf16 v[168:171], a[64:67], a[144:147], v[168:171]\n"
"s_add_u32 s52, s52, s31\n"
"s_addc_u32 s53, s53, 0\n"
"v_mfma_f32_16x16x32_bf16 v[168:171], a[68:71], a[148:151], v[168:171]\n"
"global_load_dwordx4 a[60:63], v0, s[34:35] offset:3072 sc1\n"
"v_mfma_f32_16x16x32_bf16 v[196:199], a[76:79], a[148:151], v[196:199]\n"
"s_mov_b32 s72, s31\n"
"v_mfma_f32_16x16x32_bf16 v[196:199], a[72:75], a[144:147], v[196:199]\n"
"v_mfma_f32_16x16x32_bf16 a[20:23], a[72:75], a[152:155], a[20:23]\n"
"s_add_u32 s34, s34, 8192\n"
"v_mfma_f32_16x16x32_bf16 a[20:23], a[76:79], a[156:159], a[20:23]\n"
"v_mfma_f32_16x16x32_bf16 a[16:19], a[68:71], a[156:159], a[16:19]\n"
"s_cmp_lg_u32 s29, 0\n"
"s_cselect_b32 s31, s69, s73\n"
"v_mfma_f32_16x16x32_bf16 a[16:19], a[64:67], a[152:155], a[16:19]\n"
"s_sub_u32 s54, s54, s73\n"
"s_subb_u32 s55, s55, 0\n"
"s_waitcnt vmcnt(12)\n"
"v_mfma_f32_16x16x32_bf16 v[200:203], a[80:83], a[96:99], v[200:203]\n"
"s_add_u32 s54, s54, s31\n"
"s_addc_u32 s55, s55, 0\n"
"v_mfma_f32_16x16x32_bf16 v[200:203], a[84:87], a[100:103], v[200:203]\n"
"global_load_dwordx4 a[64:67], v0, s[34:35] offset:-4096 sc1\n"
"v_mfma_f32_16x16x32_bf16 v[228:231], a[92:95], a[100:103], v[228:231]\n"
"s_mov_b32 s73, s31\n"
"ds_read_b128 a[100:103], v5 offset:1024\n"
"v_mfma_f32_16x16x32_bf16 v[228:231], a[88:91], a[96:99], v[228:231]\n"
"ds_read_b128 a[96:99], v5 offset:0\n"
"v_mfma_f32_16x16x32_bf16 v[232:235], a[88:91], a[104:107], v[232:235]\n"
"v_mfma_f32_16x16x32_bf16 v[232:235], a[92:95], a[108:111], v[232:235]\n"
"v_mfma_f32_16x16x32_bf16 v[204:207], a[84:87], a[108:111], v[204:207]\n"
"ds_read_b128 a[108:111], v5 offset:3072\n"
"v_mfma_f32_16x16x32_bf16 v[204:207], a[80:83], a[104:107], v[204:207]\n"
"ds_read_b128 a[104:107], v5 offset:2048\n"
"v_mfma_f32_16x16x32_bf16 v[208:211], a[80:83], a[112:115], v[208:211]\n"
"v_mfma_f32_16x16x32_bf16 v[208:211], a[84:87], a[116:119], v[208:211]\n"
"global_load_dwordx4 a[68:71], v0, s[34:35] offset:-3072 sc1\n"
"v_mfma_f32_16x16x32_bf16 v[236:239], a[92:95], a[116:119], v[236:239]\n"
"ds_read_b128 a[116:119], v5 offset:5120\n"
"v_mfma_f32_16x16x32_bf16 v[236:239], a[88:91], a[112:115], v[236:239]\n"
"ds_read_b128 a[112:115], v5 offset:4096\n"
"v_mfma_f32_16x16x32_bf16 v[240:243], a[88:91], a[120:123], v[240:243]\n"
"v_mfma_f32_16x16x32_bf16 v[240:243], a[92:95], a[124:127], v[240:243]\n"
"v_mfma_f32_16x16x32_bf16 v[212:215], a[84:87], a[124:127], v[212:215]\n"
"ds_read_b128 a[124:127], v5 offset:7168\n"
"v_mfma_f32_16x16x32_bf16 v[212:215], a[80:83], a[120:123], v[212:215]\n"
"ds_read_b128 a[120:123], v5 offset:6144\n"
"v_mfma_f32_16x16x32_bf16 v[216:219], a[80:83], a[128:131], v[216:219]\n"
"v_mfma_f32_16x16x32_bf16 v[216:219], a[84:87], a[132:135], v[216:219]\n"
"global_load_dwordx4 a[72:75], v0, s[34:35] offset:-2048 sc1\n"
"v_mfma_f32_16x16x32_bf16 v[244:247], a[92:95], a[132:135], v[244:247]\n"
"ds_read_b128 a[132:135], v5 offset:9216\n"
"v_mfma_f32_16x16x32_bf16 v[244:247], a[88:91], a[128:131], v[244:247]\n"
"ds_read_b128 a[128:131], v5 offset:8192\n"
"v_mfma_f32_16x16x32_bf16 v[248:251], a[88:91], a[136:139], v[248:251]\n"
"v_mfma_f32_16x16x32_bf16 v[248:251], a[92:95], a[140:143], v[248:251]\n"
"v_mfma_f32_16x16x32_bf16 v[220:223], a[84:87], a[140:143], v[220:223]\n"
"ds_read_b128 a[140:143], v5 offset:11264\n"
"v_mfma_f32_16x16x32_bf16 v[220:223], a[80:83], a[136:139], v[220:223]\n"
"ds_read_b128 a[136:139], v5 offset:10240\n"
"v_mfma_f32_16x16x32_bf16 v[224:227], a[80:83], a[144:147], v[224:227]\n"
"v_mfma_f32_16x16x32_bf16 v[224:227], a[84:87], a[148:151], v[224:227]\n"
"global_load_dwordx4 a[76:79], v0, s[34:35] offset:-1024 sc1\n"
"v_mfma_f32_16x16x32_bf16 v[252:255], a[92:95], a[148:151], v[252:255]\n"
"ds_read_b128 a[148:151], v5 offset:13312\n"
"v_mfma_f32_16x16x32_bf16 v[252:255], a[88:91], a[144:147], v[252:255]\n"
"ds_read_b128 a[144:147], v5 offset:12288\n"
"v_mfma_f32_16x16x32_bf16 a[28:31], a[88:91], a[152:155], a[28:31]\n"
"v_mfma_f32_16x16x32_bf16 a[28:31], a[92:95], a[156:159], a[28:31]\n"
"v_mfma_f32_16x16x32_bf16 a[24:27], a[84:87], a[156:159], a[24:27]\n"
"ds_read_b128 a[156:159], v5 offset:15360\n"
"v_mfma_f32_16x16x32_bf16 a[24:27], a[80:83], a[152:155], a[24:27]\n"
"s_waitcnt vmcnt(9) lgkmcnt(11)\n"
"ds_read_b128 a[152:155], v5 offset:14336\n"
"v_mfma_f32_16x16x32_bf16 v[32:35], a[32:35], a[96:99], v[32:35]\n"
"v_mfma_f32_16x16x32_bf16 v[32:35], a[36:39], a[100:103], v[32:35]\n"
"global_load_dwordx4 a[80:83], v0, s[34:35] offset:0 sc1\n"
"v_mfma_f32_16x16x32_bf16 v[60:63], a[44:47], a[100:103], v[60:63]\n"
"v_mfma_f32_16x16x32_bf16 v[60:63], a[40:43], a[96:99], v[60:63]\n"
"v_mfma_f32_16x16x32_bf16 v[64:67], a[40:43], a[104:107], v[64:67]\n"
"v_mfma_f32_16x16x32_bf16 v[64:67], a[44:47], a[108:111], v[64:67]\n"
"v_mfma_f32_16x16x32_bf16 v[36:39], a[36:39], a[108:111], v[36:39]\n"
"s_add_u32 s67, s78, 2\n"
"v_mfma_f32_16x16x32_bf16 v[36:39], a[32:35], a[104:107], v[36:39]\n"
"s_waitcnt lgkmcnt(8)\n"
"v_mfma_f32_16x16x32_bf16 v[40:43], a[32:35], a[112:115], v[40:43]\n"
"v_mfma_f32_16x16x32_bf16 v[40:43], a[36:39], a[116:119], v[40:43]\n"
"global_load_dwordx4 a[84:87], v0, s[34:35] offset:1024 sc1\n"
"v_mfma_f32_16x16x32_bf16 v[68:71], a[44:47], a[116:119], v[68:71]\n"
"v_mfma_f32_16x16x32_bf16 v[68:71], a[40:43], a[112:115], v[68:71]\n"
"v_mfma_f32_16x16x32_bf16 v[72:75], a[40:43], a[120:123], v[72:75]\n"
"v_mfma_f32_16x16x32_bf16 v[72:75], a[44:47], a[124:127], v[72:75]\n"
"v_mfma_f32_16x16x32_bf16 v[44:47], a[36:39], a[124:127], v[44:47]\n"
"v_mfma_f32_16x16x32_bf16 v[44:47], a[32:35], a[120:123], v[44:47]\n"
"s_waitcnt lgkmcnt(4)\n"
"v_mfma_f32_16x16x32_bf16 v[48:51], a[32:35], a[128:131], v[48:51]\n"
"s_lshr_b32 s66, 20, s62\n"
"v_mfma_f32_16x16x32_bf16 v[48:51], a[36:39], a[132:135], v[48:51]\n"
"global_load_dwordx4 a[88:91], v0, s[34:35] offset:2048 sc1\n"
"v_mfma_f32_16x16x32_bf16 v[76:79], a[44:47], a[132:135], v[76:79]\n"
"s_and_b32 s66, s66, 1\n"
"s_cselect_b32 s70, 4294574080, 0\n"
"s_cselect_b32 s71, -1, 0\n"
"s_cselect_b32 s66, 0, 131072\n"
"v_mfma_f32_16x16x32_bf16 v[76:79], a[40:43], a[128:131], v[76:79]\n"
"v_mfma_f32_16x16x32_bf16 v[80:83], a[40:43], a[136:139], v[80:83]\n"
"s_add_u32 s70, s70, s66\n"
"v_subrev_u32 v12, s67, v12\n"
"v_mfma_f32_16x16x32_bf16 v[80:83], a[44:47], a[140:143], v[80:83]\n"
"v_xor_b32 v13, s79, v13\n"
"v_mfma_f32_16x16x32_bf16 v[52:55], a[36:39], a[140:143], v[52:55]\n"
"v_subrev_u32 v14, s67, v14\n"
"v_mfma_f32_16x16x32_bf16 v[52:55], a[32:35], a[136:139], v[52:55]\n"
"s_waitcnt lgkmcnt(0)\n"
"v_xor_b32 v15, s79, v15\n"
"v_mfma_f32_16x16x32_bf16 v[56:59], a[32:35], a[144:147], v[56:59]\n"
"v_or_b32 v12, v12, v14\n"
"v_mfma_f32_16x16x32_bf16 v[56:59], a[36:39], a[148:151], v[56:59]\n"
"global_load_dwordx4 a[92:95], v0, s[34:35] offset:3072 sc1\n"
"v_or_b32 v13, v13, v15\n"
"v_mfma_f32_16x16x32_bf16 v[84:87], a[44:47], a[148:151], v[84:87]\n"
"v_or_b32 v12, v12, v13\n"
"v_mfma_f32_16x16x32_bf16 v[84:87], a[40:43], a[144:147], v[84:87]\n"
"v_cmp_lt_u32 vcc, 15, v12\n"
"v_mfma_f32_16x16x32_bf16 a[4:7], a[40:43], a[152:155], a[4:7]\n"
"s_add_u32 s34, s34, 8192\n"
"s_cbranch_vccnz L_slow2\n"
"L_back2:\n"
"v_mfma_f32_16x16x32_bf16 a[4:7], a[44:47], a[156:159], a[4:7]\n"
"v_mfma_f32_16x16x32_bf16 a[0:3], a[36:39], a[156:159], a[0:3]\n"
"v_mfma_f32_16x16x32_bf16 a[0:3], a[32:35], a[152:155], a[0:3]\n"
"s_waitcnt vmcnt(8)\n"
"v_mfma_f32_16x16x32_bf16 v[88:91], a[48:51], a[96:99], v[88:91]\n"
"v_mfma_f32_16x16x32_bf16 v[88:91], a[52:55], a[100:103], v[88:91]\n"
"global_load_dwordx4 a[32:35], v0, s[34:35] offset:-4096 sc1\n"
"v_mfma_f32_16x16x32_bf16 v[116:119], a[60:63], a[100:103], v[116:119]\n"
"v_mfma_f32_16x16x32_bf16 v[116:119], a[56:59], a[96:99], v[116:119]\n"
"v_mfma_f32_16x16x32_bf16 v[120:123], a[56:59], a[104:107], v[120:123]\n"
"v_mfma_f32_16x16x32_bf16 v[120:123], a[60:63], a[108:111], v[120:123]\n"
"v_mfma_f32_16x16x32_bf16 v[92:95], a[52:55], a[108:111], v[92:95]\n"
"v_mfma_f32_16x16x32_bf16 v[92:95], a[48:51], a[104:107], v[92:95]\n"
"v_mfma_f32_16x16x32_bf16 v[96:99], a[48:51], a[112:115], v[96:99]\n"
"v_mfma_f32_16x16x32_bf16 v[96:99], a[52:55], a[116:119], v[96:99]\n"
"global_load_dwordx4 a[36:39], v0, s[34:35] offset:-3072 sc1\n"
"v_mfma_f32_16x16x32_bf16 v[124:127], a[60:63], a[116:119], v[124:127]\n"
"v_mfma_f32_16x16x32_bf16 v[124:127], a[56:59], a[112:115], v[124:127]\n"
"v_mfma_f32_16x16x32_bf16 v[128:131], a[56:59], a[120:123], v[128:131]\n"
"v_mfma_f32_16x16x32_bf16 v[128:131], a[60:63], a[124:127], v[128:131]\n"
"v_mfma_f32_16x16x32_bf16 v[100:103], a[52:55], a[124:127], v[100:103]\n"
"v_mfma_f32_16x16x32_bf16 v[100:103], a[48:51], a[120:123], v[100:103]\n"
"v_mfma_f32_16x16x32_bf16 v[104:107], a[48:51], a[128:131], v[104:107]\n"
"v_mfma_f32_16x16x32_bf16 v[104:107], a[52:55], a[132:135], v[104:107]\n"
"global_load_dwordx4 a[40:43], v0, s[34:35] offset:-2048 sc1\n"
"v_mfma_f32_16x16x32_bf16 v[132:135], a[60:63], a[132:135], v[132:135]\n"
"v_mfma_f32_16x16x32_bf16 v[132:135], a[56:59], a[128:131], v[132:135]\n"
"v_mfma_f32_16x16x32_bf16 v[136:139], a[56:59], a[136:139], v[136:139]\n"
"v_mfma_f32_16x16x32_bf16 v[136:139], a[60:63], a[140:143], v[136:139]\n"
"v_mfma_f32_16x16x32_bf16 v[108:111], a[52:55], a[140:143], v[108:111]\n"
"v_mfma_f32_16x16x32_bf16 v[108:111], a[48:51], a[136:139], v[108:111]\n"
"v_mfma_f32_16x16x32_bf16 v[112:115], a[48:51], a[144:147], v[112:115]\n"
"v_mfma_f32_16x16x32_bf16 v[112:115], a[52:55], a[148:151], v[112:115]\n"
"global_load_dwordx4 a[44:47], v0, s[34:35] offset:-1024 sc1\n"
"v_mfma_f32_16x16x32_bf16 v[140:143], a[60:63], a[148:151], v[140:143]\n"
"v_mfma_f32_16x16x32_bf16 v[140:143], a[56:59], a[144:147], v[140:143]\n"
"v_mfma_f32_16x16x32_bf16 a[12:15], a[56:59], a[152:155], a[12:15]\n"
"v_mfma_f32_16x16x32_bf16 a[12:15], a[60:63], a[156:159], a[12:15]\n"
"v_mfma_f32_16x16x32_bf16 a[8:11], a[52:55], a[156:159], a[8:11]\n"
"v_mfma_f32_16x16x32_bf16 a[8:11], a[48:51], a[152:155], a[8:11]\n"
"s_waitcnt vmcnt(8)\n"
"v_mfma_f32_16x16x32_bf16 v[144:147], a[64:67], a[96:99], v[144:147]\n"
"v_mfma_f32_16x16x32_bf16 v[144:147], a[68:71], a[100:103], v[144:147]\n"
"global_load_dwordx4 a[48:51], v0, s[34:35] offset:0 sc1\n"
"v_mfma_f32_16x16x32_bf16 v[172:175], a[76:79], a[100:103], v[172:175]\n"
"v_mfma_f32_16x16x32_bf16 v[172:175], a[72:75], a[96:99], v[172:175]\n"
"v_mfma_f32_16x16x32_bf16 v[176:179], a[72:75], a[104:107], v[176:179]\n"
"v_mfma_f32_16x16x32_bf16 v[176:179], a[76:79], a[108:111], v[176:179]\n"
"v_mfma_f32_16x16x32_bf16 v[148:151], a[68:71], a[108:111], v[148:151]\n"
"v_mfma_f32_16x16x32_bf16 v[148:151], a[64:67], a[104:107], v[148:151]\n"
"v_mfma_f32_16x16x32_bf16 v[152:155], a[64:67], a[112:115], v[152:155]\n"
"v_mfma_f32_16x16x32_bf16 v[152:155], a[68:71], a[116:119], v[152:155]\n"
"global_load_dwordx4 a[52:55], v0, s[34:35] offset:1024 sc1\n"
"v_mfma_f32_16x16x32_bf16 v[180:183], a[76:79], a[116:119], v[180:183]\n"
"v_mfma_f32_16x16x32_bf16 v[180:183], a[72:75], a[112:115], v[180:183]\n"
"v_mfma_f32_16x16x32_bf16 v[184:187], a[72:75], a[120:123], v[184:187]\n"
"v_mfma_f32_16x16x32_bf16 v[184:187], a[76:79], a[124:127], v[184:187]\n"
"v_mfma_f32_16x16x32_bf16 v[156:159], a[68:71], a[124:127], v[156:159]\n"
"v_mfma_f32_16x16x32_bf16 v[156:159], a[64:67], a[120:123], v[156:159]\n"
"v_mfma_f32_16x16x32_bf16 v[160:163], a[64:67], a[128:131], v[160:163]\n"
"v_mfma_f32_16x16x32_bf16 v[160:163], a[68:71], a[132:135], v[160:163]\n"
"global_load_dwordx4 a[56:59], v0, s[34:35] offset:2048 sc1\n"
"v_mfma_f32_16x16x32_bf16 v[188:191], a[76:79], a[132:135], v[188:191]\n"
"v_mfma_f32_16x16x32_bf16 v[188:191], a[72:75], a[128:131], v[188:191]\n"
"v_mfma_f32_16x16x32_bf16 v[192:195], a[72:75], a[136:139], v[192:195]\n"
"v_mfma_f32_16x16x32_bf16 v[192:195], a[76:79], a[140:143], v[192:195]\n"
"v_mfma_f32_16x16x32_bf16 v[164:167], a[68:71], a[140:143], v[164:167]\n"
"v_mfma_f32_16x16x32_bf16 v[164:167], a[64:67], a[136:139], v[164:167]\n"
"v_mfma_f32_16x16x32_bf16 v[168:171], a[64:67], a[144:147], v[168:171]\n"
"v_mfma_f32_16x16x32_bf16 v[168:171], a[68:71], a[148:151], v[168:171]\n"
"global_load_dwordx4 a[60:63], v0, s[34:35] offset:3072 sc1\n"
"v_mfma_f32_16x16x32_bf16 v[196:199], a[76:79], a[148:151], v[196:199]\n"
"v_mfma_f32_16x16x32_bf16 v[196:199], a[72:75], a[144:147], v[196:199]\n"
"v_mfma_f32_16x16x32_bf16 a[20:23], a[72:75], a[152:155], a[20:23]\n"
"s_add_u32 s34, s34, 8192\n"
"v_mfma_f32_16x16x32_bf16 a[20:23], a[76:79], a[156:159], a[20:23]\n"
"v_mfma_f32_16x16x32_bf16 a[16:19], a[68:71], a[156:159], a[16:19]\n"
"v_mfma_f32_16x16x32_bf16 a[16:19], a[64:67], a[152:155], a[16:19]\n"
"s_waitcnt vmcnt(8)\n"
"v_mfma_f32_16x16x32_bf16 v[200:203], a[80:83], a[96:99], v[200:203]\n"
"v_mfma_f32_16x16x32_bf16 v[200:203], a[84:87], a[100:103], v[200:203]\n"
"global_load_dwordx4 a[64:67], v0, s[34:35] offset:-4096 sc1\n"
"v_mfma_f32_16x16x32_bf16 v[228:231], a[92:95], a[100:103], v[228:231]\n"
"ds_read_b128 a[100:103], v2 offset:17408\n"
"v_mfma_f32_16x16x32_bf16 v[228:231], a[88:91], a[96:99], v[228:231]\n"
"ds_read_b128 a[96:99], v2 offset:16384\n"
"v_mfma_f32_16x16x32_bf16 v[232:235], a[88:91], a[104:107], v[232:235]\n"
"v_mfma_f32_16x16x32_bf16 v[232:235], a[92:95], a[108:111], v[232:235]\n"
"v_mfma_f32_16x16x32_bf16 v[204:207], a[84:87], a[108:111], v[204:207]\n"
"ds_read_b128 a[108:111], v2 offset:19456\n"
"v_mfma_f32_16x16x32_bf16 v[204:207], a[80:83], a[104:107], v[204:207]\n"
"ds_read_b128 a[104:107], v2 offset:18432\n"
"v_mfma_f32_16x16x32_bf16 v[208:211], a[80:83], a[112:115], v[208:211]\n"
"v_mfma_f32_16x16x32_bf16 v[208:211], a[84:87], a[116:119], v[208:211]\n"
"global_load_dwordx4 a[68:71], v0, s[34:35] offset:-3072 sc1\n"
"v_mfma_f32_16x16x32_bf16 v[236:239], a[92:95], a[116:119], v[236:239]\n"
"ds_read_b128 a[116:119], v2 offset:21504\n"
"v_mfma_f32_16x16x32_bf16 v[236:239], a[88:91], a[112:115], v[236:239]\n"
"ds_read_b128 a[112:115], v2 offset:20480\n"
"v_mfma_f32_16x16x32_bf16 v[240:243], a[88:91], a[120:123], v[240:243]\n"
"v_mfma_f32_16x16x32_bf16 v[240:243], a[92:95], a[124:127], v[240:243]\n"
"v_mfma_f32_16x16x32_bf16 v[212:215], a[84:87], a[124:127], v[212:215]\n"
"ds_read_b128 a[124:127], v2 offset:23552\n"
"v_mfma_f32_16x16x32_bf16 v[212:215], a[80:83], a[120:123], v[212:215]\n"
"ds_read_b128 a[120:123], v2 offset:22528\n"
"v_mfma_f32_16x16x32_bf16 v[216:219], a[80:83], a[128:131], v[216:219]\n"
"v_mfma_f32_16x16x32_bf16 v[216:219], a[84:87], a[132:135], v[216:219]\n"
"global_load_dwordx4 a[72:75], v0, s[34:35] offset:-2048 sc1\n"
"v_mfma_f32_16x16x32_bf16 v[244:247], a[92:95], a[132:135], v[244:247]\n"
"ds_read_b128 a[132:135], v2 offset:25600\n"
"v_mfma_f32_16x16x32_bf16 v[244:247], a[88:91], a[128:131], v[244:247]\n"
"ds_read_b128 a[128:131], v2 offset:24576\n"
"v_mfma_f32_16x16x32_bf16 v[248:251], a[88:91], a[136:139], v[248:251]\n"
"v_mfma_f32_16x16x32_bf16 v[248:251], a[92:95], a[140:143], v[248:251]\n"
"v_mfma_f32_16x16x32_bf16 v[220:223], a[84:87], a[140:143], v[220:223]\n"
"ds_read_b128 a[140:143], v2 offset:27648\n"
"v_mfma_f32_16x16x32_bf16 v[220:223], a[80:83], a[136:139], v[220:223]\n"
"ds_read_b128 a[136:139], v2 offset:26624\n"
"v_mfma_f32_16x16x32_bf16 v[224:227], a[80:83], a[144:147], v[224:227]\n"
"v_mfma_f32_16x16x32_bf16 v[224:227], a[84:87], a[148:151], v[224:227]\n"
"global_load_dwordx4 a[76:79], v0, s[34:35] offset:-1024 sc1\n"
"v_mfma_f32_16x16x32_bf16 v[252:255], a[92:95], a[148:151], v[252:255]\n"
"ds_read_b128 a[148:151], v2 offset:29696\n"
"v_mfma_f32_16x16x32_bf16 v[252:255], a[88:91], a[144:147], v[252:255]\n"
"ds_read_b128 a[144:147], v2 offset:28672\n"
"v_mfma_f32_16x16x32_bf16 a[28:31], a[88:91], a[152:155], a[28:31]\n"
"v_mfma_f32_16x16x32_bf16 a[28:31], a[92:95], a[156:159], a[28:31]\n"
"v_mfma_f32_16x16x32_bf16 a[24:27], a[84:87], a[156:159], a[24:27]\n"
"ds_read_b128 a[156:159], v2 offset:31744\n"
"v_mfma_f32_16x16x32_bf16 a[24:27], a[80:83], a[152:155], a[24:27]\n"
"ds_read_b128 a[152:155], v2 offset:30720\n"
"L_P5:\n"
"s_waitcnt vmcnt(8) lgkmcnt(12)\n"
"v_mfma_f32_16x16x32_bf16 v[32:35], a[32:35], a[96:99], v[32:35]\n"
"v_mfma_f32_16x16x32_bf16 v[32:35], a[36:39], a[100:103], v[32:35]\n"
"global_load_dwordx4 a[80:83], v0, s[34:35] offset:0 sc1\n"
"v_mfma_f32_16x16x32_bf16 v[60:63], a[44:47], a[100:103], v[60:63]\n"
"s_barrier\n"
"v_mfma_f32_16x16x32_bf16 v[60:63], a[40:43], a[96:99], v[60:63]\n"
"s_add_u32 s48, s48, s70\n"
"s_addc_u32 s49, s49, s71\n"
"v_mfma_f32_16x16x32_bf16 v[64:67], a[40:43], a[104:107], v[64:67]\n"
"v_mfma_f32_16x16x32_bf16 v[64:67], a[44:47], a[108:111], v[64:67]\n"
"v_mfma_f32_16x16x32_bf16 v[36:39], a[36:39], a[108:111], v[36:39]\n"
"s_add_u32 s52, s52, s70\n"
"s_addc_u32 s53, s53, s71\n"
"v_mfma_f32_16x16x32_bf16 v[36:39], a[32:35], a[104:107], v[36:39]\n"
"s_waitcnt lgkmcnt(8)\n"
"v_mfma_f32_16x16x32_bf16 v[40:43], a[32:35], a[112:115], v[40:43]\n"
"s_add_u32 s54, s54, s70\n"
"s_addc_u32 s55, s55, s71\n"
"v_mfma_f32_16x16x32_bf16 v[40:43], a[36:39], a[116:119], v[40:43]\n"
"global_load_dwordx4 a[84:87], v0, s[34:35] offset:1024 sc1\n"
"v_mfma_f32_16x16x32_bf16 v[68:71], a[44:47], a[116:119], v[68:71]\n"
"v_mfma_f32_16x16x32_bf16 v[68:71], a[40:43], a[112:115], v[68:71]\n"
"v_mfma_f32_16x16x32_bf16 v[72:75], a[40:43], a[120:123], v[72:75]\n"
"v_mfma_f32_16x16x32_bf16 v[72:75], a[44:47], a[124:127], v[72:75]\n"
"s_add_u32 s61, s61, 2048\n"
"v_mfma_f32_16x16x32_bf16 v[44:47], a[36:39], a[124:127], v[44:47]\n"
"v_mfma_f32_16x16x32_bf16 v[44:47], a[32:35], a[120:123], v[44:47]\n"
"s_waitcnt lgkmcnt(4)\n"
"v_mfma_f32_16x16x32_bf16 v[48:51], a[32:35], a[128:131], v[48:51]\n"
"v_lshlrev_b32 v16, 16, v18\n"
"v_mfma_f32_16x16x32_bf16 v[48:51], a[36:39], a[132:135], v[48:51]\n"
"global_load_dwordx4 a[88:91], v0, s[34:35] offset:2048 sc1\n"
"v_and_b32 v17, s63, v18\n"
"v_mfma_f32_16x16x32_bf16 v[76:79], a[44:47], a[132:135], v[76:79]\n"
"v_lshlrev_b32 v18, 16, v19\n"
"v_mfma_f32_16x16x32_bf16 v[76:79], a[40:43], a[128:131], v[76:79]\n"
"v_and_b32 v19, s63, v19\n"
"v_mfma_f32_16x16x32_bf16 v[80:83], a[40:43], a[136:139], v[80:83]\n"
"v_add_f32 v144, v144, v16\n"
"v_add_f32 v145, v145, v17\n"
"v_mfma_f32_16x16x32_bf16 v[80:83], a[44:47], a[140:143], v[80:83]\n"
"v_add_f32 v146, v146, v18\n"
"v_mfma_f32_16x16x32_bf16 v[52:55], a[36:39], a[140:143], v[52:55]\n"
"v_add_f32 v147, v147, v19\n"
"v_mfma_f32_16x16x32_bf16 v[52:55], a[32:35], a[136:139], v[52:55]\n"
"s_waitcnt lgkmcnt(0)\n"
"v_add_f32 v148, v148, v16\n"
"v_mfma_f32_16x16x32_bf16 v[56:59], a[32:35], a[144:147], v[56:59]\n"
"v_add_f32 v149, v149, v17\n"
"v_mfma_f32_16x16x32_bf16 v[56:59], a[36:39], a[148:151], v[56:59]\n"
"global_load_dwordx4 a[92:95], v0, s[34:35] offset:3072 sc1\n"
"v_add_f32 v150, v150, v18\n"
"v_mfma_f32_16x16x32_bf16 v[84:87], a[44:47], a[148:151], v[84:87]\n"
"v_add_f32 v151, v151, v19\n"
"v_mfma_f32_16x16x32_bf16 v[84:87], a[40:43], a[144:147], v[84:87]\n"
"v_add_f32 v152, v152, v16\n"
"v_mfma_f32_16x16x32_bf16 a[4:7], a[40:43], a[152:155], a[4:7]\n"
"s_add_u32 s34, s34, 8192\n"
"v_add_f32 v153, v153, v17\n"
"v_mfma_f32_16x16x32_bf16 a[4:7], a[44:47], a[156:159], a[4:7]\n"
"v_add_f32 v154, v154, v18\n"
"v_mfma_f32_16x16x32_bf16 a[0:3], a[36:39], a[156:159], a[0:3]\n"
"v_add_f32 v155, v155, v19\n"
"v_add_f32 v156, v156, v16\n"
"v_mfma_f32_16x16x32_bf16 a[0:3], a[32:35], a[152:155], a[0:3]\n"
"s_waitcnt vmcnt(8)\n"
"v_add_f32 v157, v157, v17\n"
"v_mfma_f32_16x16x32_bf16 v[88:91], a[48:51], a[96:99], v[88:91]\n"
"v_add_f32 v158, v158, v18\n"
"v_mfma_f32_16x16x32_bf16 v[88:91], a[52:55], a[100:103], v[88:91]\n"
"global_load_dwordx4 a[32:35], v0, s[34:35] offset:-4096 sc1\n"
"v_add_f32 v159, v159, v19\n"
"v_mfma_f32_16x16x32_bf16 v[116:119], a[60:63], a[100:103], v[116:119]\n"
"ds_read_b128 a[100:103], v3 offset:17408\n"
"v_add_f32 v160, v160, v16\n"
"v_mfma_f32_16x16x32_bf16 v[116:119], a[56:59], a[96:99], v[116:119]\n"
"ds_read_b128 a[96:99], v3 offset:16384\n"
"v_add_f32 v161, v161, v17\n"
"v_mfma_f32_16x16x32_bf16 v[120:123], a[56:59], a[104:107], v[120:123]\n"
"v_add_f32 v162, v162, v18\n"
"v_mfma_f32_16x16x32_bf16 v[120:123], a[60:63], a[108:111], v[120:123]\n"
"v_add_f32 v163, v163, v19\n"
"v_mfma_f32_16x16x32_bf16 v[92:95], a[52:55], a[108:111], v[92:95]\n"
"L_st7943:\n"
"s_cbranch_execz L_retB\n"
"ds_read_b128 a[108:111], v3 offset:19456\n"
"v_add_f32 v164, v164, v16\n"
"v_mfma_f32_16x16x32_bf16 v[92:95], a[48:51], a[104:107], v[92:95]\n"
"ds_read_b128 a[104:107], v3 offset:18432\n"
"v_add_f32 v165, v165, v17\n"
"v_mfma_f32_16x16x32_bf16 v[96:99], a[48:51], a[112:115], v[96:99]\n"
"v_add_f32 v166, v166, v18\n"
"v_add_f32 v167, v167, v19\n"
"v_mfma_f32_16x16x32_bf16 v[96:99], a[52:55], a[116:119], v[96:99]\n"
"global_load_dwordx4 a[36:39], v0, s[34:35] offset:-3072 sc1\n"
"v_add_f32 v168, v168, v16\n"
"v_mfma_f32_16x16x32_bf16 v[124:127], a[60:63], a[116:119], v[124:127]\n"
"ds_read_b128 a[116:119], v3 offset:21504\n"
"v_add_f32 v169, v169, v17\n"
"v_mfma_f32_16x16x32_bf16 v[124:127], a[56:59], a[112:115], v[124:127]\n"
"ds_read_b128 a[112:115], v3 offset:20480\n"
"v_add_f32 v170, v170, v18\n"
"v_mfma_f32_16x16x32_bf16 v[128:131], a[56:59], a[120:123], v[128:131]\n"
"v_add_f32 v171, v171, v19\n"
"v_mfma_f32_16x16x32_bf16 v[128:131], a[60:63], a[124:127], v[128:131]\n"
"v_accvgpr_read_b32 v12, a16\n"
"v_mfma_f32_16x16x32_bf16 v[100:103], a[52:55], a[124:127], v[100:103]\n"
"ds_read_b128 a[124:127], v3 offset:23552\n"
"v_accvgpr_read_b32 v13, a17\n"
"v_mfma_f32_16x16x32_bf16 v[100:103], a[48:51], a[120:123], v[100:103]\n"
"ds_read_b128 a[120:123], v3 offset:22528\n"
"v_accvgpr_read_b32 v14, a18\n"
"v_mfma_f32_16x16x32_bf16 v[104:107], a[48:51], a[128:131], v[104:107]\n"
"v_accvgpr_read_b32 v15, a19\n"
"v_add_f32 v12, v12, v16\n"
"v_mfma_f32_16x16x32_bf16 v[104:107], a[52:55], a[132:135], v[104:107]\n"
"global_load_dwordx4 a[40:43], v0, s[34:35] offset:-2048 sc1\n"
"v_add_f32 v13, v13, v17\n"
"v_mfma_f32_16x16x32_bf16 v[132:135], a[60:63], a[132:135], v[132:135]\n"
"ds_read_b128 a[132:135], v3 offset:25600\n"
"v_add_f32 v14, v14, v18\n"
"v_mfma_f32_16x16x32_bf16 v[132:135], a[56:59], a[128:131], v[132:135]\n"
"ds_read_b128 a[128:131], v3 offset:24576\n"
"v_add_f32 v15, v15, v19\n"
"v_mfma_f32_16x16x32_bf16 v[136:139], a[56:59], a[136:139], v[136:139]\n"
"v_accvgpr_write_b32 a16, v12\n"
"v_mfma_f32_16x16x32_bf16 v[136:139], a[60:63], a[140:143], v[136:139]\n"
"v_accvgpr_write_b32 a17, v13\n"
"v_mfma_f32_16x16x32_bf16 v[108:111], a[52:55], a[140:143], v[108:111]\n"
"ds_read_b128 a[140:143], v3 offset:27648\n"
"v_accvgpr_write_b32 a18, v14\n"
"v_mfma_f32_16x16x32_bf16 v[108:111], a[48:51], a[136:139], v[108:111]\n"
"ds_read_b128 a[136:139], v3 offset:26624\n"
"v_accvgpr_write_b32 a19, v15\n"
"v_mfma_f32_16x16x32_bf16 v[112:115], a[48:51], a[144:147], v[112:115]\n"
"v_lshlrev_b32 v20, 16, v22\n"
"v_and_b32 v21, s63, v22\n"
"v_mfma_f32_16x16x32_bf16 v[112:115], a[52:55], a[148:151], v[112:115]\n"
"global_load_dwordx4 a[44:47], v0, s[34:35] offset:-1024 sc1\n"
"v_lshlrev_b32 v22, 16, v23\n"
"v_mfma_f32_16x16x32_bf16 v[140:143], a[60:63], a[148:151], v[140:143]\n"
"ds_read_b128 a[148:151], v3 offset:29696\n"
"v_and_b32 v23, s63, v23\n"
"v_mfma_f32_16x16x32_bf16 v[140:143], a[56:59], a[144:147], v[140:143]\n"
"ds_read_b128 a[144:147], v3 offset:28672\n"
"v_add_f32 v172, v172, v20\n"
"v_mfma_f32_16x16x32_bf16 a[12:15], a[56:59], a[152:155], a[12:15]\n"
"v_add_f32 v173, v173, v21\n"
"v_mfma_f32_16x16x32_bf16 a[12:15], a[60:63], a[156:159], a[12:15]\n"
"v_add_f32 v174, v174, v22\n"
"v_mfma_f32_16x16x32_bf16 a[8:11], a[52:55], a[156:159], a[8:11]\n"
"ds_read_b128 a[156:159], v3 offset:31744\n"
"v_add_f32 v175, v175, v23\n"
"v_mfma_f32_16x16x32_bf16 a[8:11], a[48:51], a[152:155], a[8:11]\n"
"s_waitcnt vmcnt(8) lgkmcnt(11)\n"
"ds_read_b128 a[152:155], v3 offset:30720\n"
"v_add_f32 v176, v176, v20\n"
"v_mfma_f32_16x16x32_bf16 v[32:35], a[64:67], a[96:99], v[32:35]\n"
"v_add_f32 v177, v177, v21\n"
"v_mfma_f32_16x16x32_bf16 v[32:35], a[68:71], a[100:103], v[32:35]\n"
"global_load_dwordx4 a[48:51], v0, s[34:35] offset:0 sc1\n"
"v_add_f32 v178, v178, v22\n"
"v_mfma_f32_16x16x32_bf16 v[60:63], a[76:79], a[100:103], v[60:63]\n"
"v_add_f32 v179, v179, v23\n"
"v_mfma_f32_16x16x32_bf16 v[60:63], a[72:75], a[96:99], v[60:63]\n"
"v_add_f32 v180, v180, v20\n"
"v_add_f32 v181, v181, v21\n"
"v_mfma_f32_16x16x32_bf16 v[64:67], a[72:75], a[104:107], v[64:67]\n"
"v_add_f32 v182, v182, v22\n"
"v_mfma_f32_16x16x32_bf16 v[64:67], a[76:79], a[108:111], v[64:67]\n"
"v_add_f32 v183, v183, v23\n"
"v_mfma_f32_16x16x32_bf16 v[36:39], a[68:71], a[108:111], v[36:39]\n"
"v_add_f32 v184, v184, v20\n"
"v_mfma_f32_16x16x32_bf16 v[36:39], a[64:67], a[104:107], v[36:39]\n"
"s_waitcnt lgkmcnt(8)\n"
"v_add_f32 v185, v185, v21\n"
"v_mfma_f32_16x16x32_bf16 v[40:43], a[64:67], a[112:115], v[40:43]\n"
"v_add_f32 v186, v186, v22\n"
"v_mfma_f32_16x16x32_bf16 v[40:43], a[68:71], a[116:119], v[40:43]\n"
"global_load_dwordx4 a[52:55], v0, s[34:35] offset:1024 sc1\n"
"v_add_f32 v187, v187, v23\n"
"v_mfma_f32_16x16x32_bf16 v[68:71], a[76:79], a[116:119], v[68:71]\n"
"v_add_f32 v188, v188, v20\n"
"v_mfma_f32_16x16x32_bf16 v[68:71], a[72:75], a[112:115], v[68:71]\n"
"v_add_f32 v189, v189, v21\n"
"v_mfma_f32_16x16x32_bf16 v[72:75], a[72:75], a[120:123], v[72:75]\n"
"v_add_f32 v190, v190, v22\n"
"v_add_f32 v191, v191, v23\n"
"v_mfma_f32_16x16x32_bf16 v[72:75], a[76:79], a[124:127], v[72:75]\n"
"v_add_f32 v192, v192, v20\n"
"v_mfma_f32_16x16x32_bf16 v[44:47], a[68:71], a[124:127], v[44:47]\n"
"v_add_f32 v193, v193, v21\n"
"v_mfma_f32_16x16x32_bf16 v[44:47], a[64:67], a[120:123], v[44:47]\n"
"s_waitcnt lgkmcnt(4)\n"
"v_add_f32 v194, v194, v22\n"
"v_mfma_f32_16x16x32_bf16 v[48:51], a[64:67], a[128:131], v[48:51]\n"
"v_add_f32 v195, v195, v23\n"
"v_mfma_f32_16x16x32_bf16 v[48:51], a[68:71], a[132:135], v[48:51]\n"
"global_load_dwordx4 a[56:59], v0, s[34:35] offset:2048 sc1\n"
"v_add_f32 v196, v196, v20\n"
"v_mfma_f32_16x16x32_bf16 v[76:79], a[76:79], a[132:135], v[76:79]\n"
"v_add_f32 v197, v197, v21\n"
"v_mfma_f32_16x16x32_bf16 v[76:79], a[72:75], a[128:131], v[76:79]\n"
"v_add_f32 v198, v198, v22\n"
"v_mfma_f32_16x16x32_bf16 v[80:83], a[72:75], a[136:139], v[80:83]\n"
"v_add_f32 v199, v199, v23\n"
"v_mfma_f32_16x16x32_bf16 v[80:83], a[76:79], a[140:143], v[80:83]\n"
"v_accvgpr_read_b32 v12, a20\n"
"v_mfma_f32_16x16x32_bf16 v[52:55], a[68:71], a[140:143], v[52:55]\n"
"v_accvgpr_read_b32 v13, a21\n"
"v_accvgpr_read_b32 v14, a22\n"
"v_mfma_f32_16x16x32_bf16 v[52:55], a[64:67], a[136:139], v[52:55]\n"
"s_waitcnt lgkmcnt(0)\n"
"v_accvgpr_read_b32 v15, a23\n"
"v_mfma_f32_16x16x32_bf16 v[56:59], a[64:67], a[144:147], v[56:59]\n"
"v_add_f32 v12, v12, v20\n"
"v_mfma_f32_16x16x32_bf16 v[56:59], a[68:71], a[148:151], v[56:59]\n"
"global_load_dwordx4 a[60:63], v0, s[34:35] offset:3072 sc1\n"
"v_add_f32 v13, v13, v21\n"
"v_mfma_f32_16x16x32_bf16 v[84:87], a[76:79], a[148:151], v[84:87]\n"
"v_add_f32 v14, v14, v22\n"
"v_mfma_f32_16x16x32_bf16 v[84:87], a[72:75], a[144:147], v[84:87]\n"
"v_add_f32 v15, v15, v23\n"
"v_mfma_f32_16x16x32_bf16 a[4:7], a[72:75], a[152:155], a[4:7]\n"
"s_add_u32 s34, s34, 8192\n"
"v_accvgpr_write_b32 a20, v12\n"
"v_mfma_f32_16x16x32_bf16 a[4:7], a[76:79], a[156:159], a[4:7]\n"
"v_accvgpr_write_b32 a21, v13\n"
"v_mfma_f32_16x16x32_bf16 a[0:3], a[68:71], a[156:159], a[0:3]\n"
"v_accvgpr_write_b32 a22, v14\n"
"v_mfma_f32_16x16x32_bf16 a[0:3], a[64:67], a[152:155], a[0:3]\n"
"s_waitcnt vmcnt(8)\n"
"v_accvgpr_write_b32 a23, v15\n"
"v_lshlrev_b32 v24, 16, v26\n"
"v_mfma_f32_16x16x32_bf16 v[88:91], a[80:83], a[96:99], v[88:91]\n"
"v_and_b32 v25, s63, v26\n"
"v_mfma_f32_16x16x32_bf16 v[88:91], a[84:87], a[100:103], v[88:91]\n"
"global_load_dwordx4 a[64:67], v0, s[34:35] offset:-4096 sc1\n"
"v_lshlrev_b32 v26, 16, v27\n"
"v_mfma_f32_16x16x32_bf16 v[116:119], a[92:95], a[100:103], v[116:119]\n"
"ds_read_b128 a[100:103], v4 offset:17408\n"
"v_and_b32 v27, s63, v27\n"
"v_mfma_f32_16x16x32_bf16 v[116:119], a[88:91], a[96:99], v[116:119]\n"
"ds_read_b128 a[96:99], v4 offset:16384\n"
"v_add_f32 v200, v200, v24\n"
"v_mfma_f32_16x16x32_bf16 v[120:123], a[88:91], a[104:107], v[120:123]\n"
"v_add_f32 v201, v201, v25\n"
"v_mfma_f32_16x16x32_bf16 v[120:123], a[92:95], a[108:111], v[120:123]\n"
"v_add_f32 v202, v202, v26\n"
"v_mfma_f32_16x16x32_bf16 v[92:95], a[84:87], a[108:111], v[92:95]\n"
"ds_read_b128 a[108:111], v4 offset:19456\n"
"v_add_f32 v203, v203, v27\n"
"v_mfma_f32_16x16x32_bf16 v[92:95], a[80:83], a[104:107], v[92:95]\n"
"ds_read_b128 a[104:107], v4 offset:18432\n"
"v_add_f32 v204, v204, v24\n"
"v_mfma_f32_16x16x32_bf16 v[96:99], a[80:83], a[112:115], v[96:99]\n"
"v_add_f32 v205, v205, v25\n"
"v_add_f32 v206, v206, v26\n"
"v_mfma_f32_16x16x32_bf16 v[96:99], a[84:87], a[116:119], v[96:99]\n"
"global_load_dwordx4 a[68:71], v0, s[34:35] offset:-3072 sc1\n"
"v_add_f32 v207, v207, v27\n"
"v_mfma_f32_16x16x32_bf16 v[124:127], a[92:95], a[116:119], v[124:127]\n"
"ds_read_b128 a[116:119], v4 offset:21504\n"
"v_add_f32 v208, v208, v24\n"
"v_mfma_f32_16x16x32_bf16 v[124:127], a[88:91], a[112:115], v[124:127]\n"
"ds_read_b128 a[112:115], v4 offset:20480\n"
"v_add_f32 v209, v209, v25\n"
"v_mfma_f32_16x16x32_bf16 v[128:131], a[88:91], a[120:123], v[128:131]\n"
"v_add_f32 v210, v210, v26\n"
"v_mfma_f32_16x16x32_bf16 v[128:131], a[92:95], a[124:127], v[128:131]\n"
"v_add_f32 v211, v211, v27\n"
"v_mfma_f32_16x16x32_bf16 v[100:103], a[84:87], a[124:127], v[100:103]\n"
"ds_read_b128 a[124:127], v4 offset:23552\n"
"v_add_f32 v212, v212, v24\n"
"v_mfma_f32_16x16x32_bf16 v[100:103], a[80:83], a[120:123], v[100:103]\n"
"ds_read_b128 a[120:123], v4 offset:22528\n"
"v_add_f32 v213, v213, v25\n"
"v_mfma_f32_16x16x32_bf16 v[104:107], a[80:83], a[128:131], v[104:107]\n"
"v_add_f32 v214, v214, v26\n"
"v_add_f32 v215, v215, v27\n"
"v_mfma_f32_16x16x32_bf16 v[104:107], a[84:87], a[132:135], v[104:107]\n"
"global_load_dwordx4 a[72:75], v0, s[34:35] offset:-2048 sc1\n"
"v_add_f32 v216, v216, v24\n"
"v_mfma_f32_16x16x32_bf16 v[132:135], a[92:95], a[132:135], v[132:135]\n"
"ds_read_b128 a[132:135], v4 offset:25600\n"
"v_add_f32 v217, v217, v25\n"
"v_mfma_f32_16x16x32_bf16 v[132:135], a[88:91], a[128:131], v[132:135]\n"
"ds_read_b128 a[128:131], v4 offset:24576\n"
"v_add_f32 v218, v218, v26\n"
"v_mfma_f32_16x16x32_bf16 v[136:139], a[88:91], a[136:139], v[136:139]\n"
"v_add_f32 v219, v219, v27\n"
"v_mfma_f32_16x16x32_bf16 v[136:139], a[92:95], a[140:143], v[136:139]\n"
"v_add_f32 v220, v220, v24\n"
"v_mfma_f32_16x16x32_bf16 v[108:111], a[84:87], a[140:143], v[108:111]\n"
"ds_read_b128 a[140:143], v4 offset:27648\n"
"v_add_f32 v221, v221, v25\n"
"v_mfma_f32_16x16x32_bf16 v[108:111], a[80:83], a[136:139], v[108:111]\n"
"ds_read_b128 a[136:139], v4 offset:26624\n"
"v_add_f32 v222, v222, v26\n"
"v_mfma_f32_16x16x32_bf16 v[112:115], a[80:83], a[144:147], v[112:115]\n"
"v_add_f32 v223, v223, v27\n"
"v_mfma_f32_16x16x32_bf16 v[112:115], a[84:87], a[148:151], v[112:115]\n"
"global_load_dwordx4 a[76:79], v0, s[34:35] offset:-1024 sc1\n"
"v_add_f32 v224, v224, v24\n"
"v_mfma_f32_16x16x32_bf16 v[140:143], a[92:95], a[148:151], v[140:143]\n"
"ds_read_b128 a[148:151], v4 offset:29696\n"
"v_add_f32 v225, v225, v25\n"
"v_mfma_f32_16x16x32_bf16 v[140:143], a[88:91], a[144:147], v[140:143]\n"
"ds_read_b128 a[144:147], v4 offset:28672\n"
"v_add_f32 v226, v226, v26\n"
"v_mfma_f32_16x16x32_bf16 a[12:15], a[88:91], a[152:155], a[12:15]\n"
"v_add_f32 v227, v227, v27\n"
"v_accvgpr_read_b32 v12, a24\n"
"v_mfma_f32_16x16x32_bf16 a[12:15], a[92:95], a[156:159], a[12:15]\n"
"v_accvgpr_read_b32 v13, a25\n"
"v_mfma_f32_16x16x32_bf16 a[8:11], a[84:87], a[156:159], a[8:11]\n"
"ds_read_b128 a[156:159], v4 offset:31744\n"
"v_accvgpr_read_b32 v14, a26\n"
"v_mfma_f32_16x16x32_bf16 a[8:11], a[80:83], a[152:155], a[8:11]\n"
"s_waitcnt vmcnt(8) lgkmcnt(11)\n"
"ds_read_b128 a[152:155], v4 offset:30720\n"
"v_accvgpr_read_b32 v15, a27\n"
"v_mfma_f32_16x16x32_bf16 v[32:35], a[32:35], a[96:99], v[32:35]\n"
"v_add_f32 v12, v12, v24\n"
"v_mfma_f32_16x16x32_bf16 v[32:35], a[36:39], a[100:103], v[32:35]\n"
"global_load_dwordx4 a[80:83], v0, s[34:35] offset:0 sc1\n"
"v_add_f32 v13, v13, v25\n"
"v_mfma_f32_16x16x32_bf16 v[60:63], a[44:47], a[100:103], v[60:63]\n"
"v_add_f32 v14, v14, v26\n"
"v_mfma_f32_16x16x32_bf16 v[60:63], a[40:43], a[96:99], v[60:63]\n"
"v_add_f32 v15, v15, v27\n"
"v_mfma_f32_16x16x32_bf16 v[64:67], a[40:43], a[104:107], v[64:67]\n"
"v_accvgpr_write_b32 a24, v12\n"
"v_accvgpr_write_b32 a25, v13\n"
"v_mfma_f32_16x16x32_bf16 v[64:67], a[44:47], a[108:111], v[64:67]\n"
"v_accvgpr_write_b32 a26, v14\n"
"v_mfma_f32_16x16x32_bf16 v[36:39], a[36:39], a[108:111], v[36:39]\n"
"v_accvgpr_write_b32 a27, v15\n"
"v_mfma_f32_16x16x32_bf16 v[36:39], a[32:35], a[104:107], v[36:39]\n"
"s_waitcnt lgkmcnt(8)\n"
"v_lshlrev_b32 v28, 16, v30\n"
"v_mfma_f32_16x16x32_bf16 v[40:43], a[32:35], a[112:115], v[40:43]\n"
"v_and_b32 v29, s63, v30\n"
"v_mfma_f32_16x16x32_bf16 v[40:43], a[36:39], a[116:119], v[40:43]\n"
"global_load_dwordx4 a[84:87], v0, s[34:35] offset:1024 sc1\n"
"v_lshlrev_b32 v30, 16, v31\n"
"v_mfma_f32_16x16x32_bf16 v[68:71], a[44:47], a[116:119], v[68:71]\n"
"v_and_b32 v31, s63, v31\n"
"v_mfma_f32_16x16x32_bf16 v[68:71], a[40:43], a[112:115], v[68:71]\n"
"v_add_f32 v228, v228, v28\n"
"v_mfma_f32_16x16x32_bf16 v[72:75], a[40:43], a[120:123], v[72:75]\n"
"v_add_f32 v229, v229, v29\n"
"v_mfma_f32_16x16x32_bf16 v[72:75], a[44:47], a[124:127], v[72:75]\n"
"v_add_f32 v230, v230, v30\n"
"v_mfma_f32_16x16x32_bf16 v[44:47], a[36:39], a[124:127], v[44:47]\n"
"v_add_f32 v231, v231, v31\n"
"v_add_f32 v232, v232, v28\n"
"v_mfma_f32_16x16x32_bf16 v[44:47], a[32:35], a[120:123], v[44:47]\n"
"s_waitcnt lgkmcnt(4)\n"
"v_add_f32 v233, v233, v29\n"
"v_mfma_f32_16x16x32_bf16 v[48:51], a[32:35], a[128:131], v[48:51]\n"
"v_add_f32 v234, v234, v30\n"
"v_mfma_f32_16x16x32_bf16 v[48:51], a[36:39], a[132:135], v[48:51]\n"
"global_load_dwordx4 a[88:91], v0, s[34:35] offset:2048 sc1\n"
"v_add_f32 v235, v235, v31\n"
"v_mfma_f32_16x16x32_bf16 v[76:79], a[44:47], a[132:135], v[76:79]\n"
"v_add_f32 v236, v236, v28\n"
"v_mfma_f32_16x16x32_bf16 v[76:79], a[40:43], a[128:131], v[76:79]\n"
"v_add_f32 v237, v237, v29\n"
"v_mfma_f32_16x16x32_bf16 v[80:83], a[40:43], a[136:139], v[80:83]\n"
"v_add_f32 v238, v238, v30\n"
"v_mfma_f32_16x16x32_bf16 v[80:83], a[44:47], a[140:143], v[80:83]\n"
"v_add_f32 v239, v239, v31\n"
"v_mfma_f32_16x16x32_bf16 v[52:55], a[36:39], a[140:143], v[52:55]\n"
"v_add_f32 v240, v240, v28\n"
"v_mfma_f32_16x16x32_bf16 v[52:55], a[32:35], a[136:139], v[52:55]\n"
"s_waitcnt lgkmcnt(0)\n"
"v_add_f32 v241, v241, v29\n"
"v_add_f32 v242, v242, v30\n"
"v_mfma_f32_16x16x32_bf16 v[56:59], a[32:35], a[144:147], v[56:59]\n"
"v_add_f32 v243, v243, v31\n"
"v_mfma_f32_16x16x32_bf16 v[56:59], a[36:39], a[148:151], v[56:59]\n"
"global_load_dwordx4 a[92:95], v0, s[34:35] offset:3072 sc1\n"
"v_add_f32 v244, v244, v28\n"
"v_mfma_f32_16x16x32_bf16 v[84:87], a[44:47], a[148:151], v[84:87]\n"
"v_add_f32 v245, v245, v29\n"
"v_mfma_f32_16x16x32_bf16 v[84:87], a[40:43], a[144:147], v[84:87]\n"
"v_add_f32 v246, v246, v30\n"
"v_mfma_f32_16x16x32_bf16 a[4:7], a[40:43], a[152:155], a[4:7]\n"
"s_add_u32 s34, s34, 8192\n"
"v_add_f32 v247, v247, v31\n"
"v_mfma_f32_16x16x32_bf16 a[4:7], a[44:47], a[156:159], a[4:7]\n"
"v_add_f32 v248, v248, v28\n"
"v_mfma_f32_16x16x32_bf16 a[0:3], a[36:39], a[156:159], a[0:3]\n"
"v_add_f32 v249, v249, v29\n"
"v_mfma_f32_16x16x32_bf16 a[0:3], a[32:35], a[152:155], a[0:3]\n"
"s_waitcnt vmcnt(8)\n"
"v_add_f32 v250, v250, v30\n"
"v_mfma_f32_16x16x32_bf16 v[88:91], a[48:51], a[96:99], v[88:91]\n"
"v_add_f32 v251, v251, v31\n"
"v_add_f32 v252, v252, v28\n"
"v_mfma_f32_16x16x32_bf16 v[88:91], a[52:55], a[100:103], v[88:91]\n"
"global_load_dwordx4 a[32:35], v0, s[34:35] offset:-4096 sc1\n"
"v_add_f32 v253, v253, v29\n"
"v_mfma_f32_16x16x32_bf16 v[116:119], a[60:63], a[100:103], v[116:119]\n"
"ds_read_b128 a[100:103], v5 offset:17408\n"
"v_add_f32 v254, v254, v30\n"
"v_mfma_f32_16x16x32_bf16 v[116:119], a[56:59], a[96:99], v[116:119]\n"
"ds_read_b128 a[96:99], v5 offset:16384\n"
"v_add_f32 v255, v255, v31\n"
"v_mfma_f32_16x16x32_bf16 v[120:123], a[56:59], a[104:107], v[120:123]\n"
"v_accvgpr_read_b32 v12, a28\n"
"v_mfma_f32_16x16x32_bf16 v[120:123], a[60:63], a[108:111], v[120:123]\n"
"v_accvgpr_read_b32 v13, a29\n"
"v_mfma_f32_16x16x32_bf16 v[92:95], a[52:55], a[108:111], v[92:95]\n"
"ds_read_b128 a[108:111], v5 offset:19456\n"
"v_accvgpr_read_b32 v14, a30\n"
"v_mfma_f32_16x16x32_bf16 v[92:95], a[48:51], a[104:107], v[92:95]\n"
"ds_read_b128 a[104:107], v5 offset:18432\n"
"v_accvgpr_read_b32 v15, a31\n"
"v_mfma_f32_16x16x32_bf16 v[96:99], a[48:51], a[112:115], v[96:99]\n"
"v_add_f32 v12, v12, v28\n"
"v_add_f32 v13, v13, v29\n"
"v_mfma_f32_16x16x32_bf16 v[96:99], a[52:55], a[116:119], v[96:99]\n"
"global_load_dwordx4 a[36:39], v0, s[34:35] offset:-3072 sc1\n"
"v_add_f32 v14, v14, v30\n"
"v_mfma_f32_16x16x32_bf16 v[124:127], a[60:63], a[116:119], v[124:127]\n"
"ds_read_b128 a[116:119], v5 offset:21504\n"
"v_add_f32 v15, v15, v31\n"
"v_mfma_f32_16x16x32_bf16 v[124:127], a[56:59], a[112:115], v[124:127]\n"
"ds_read_b128 a[112:115], v5 offset:20480\n"
"v_accvgpr_write_b32 a28, v12\n"
"v_mfma_f32_16x16x32_bf16 v[128:131], a[56:59], a[120:123], v[128:131]\n"
"v_accvgpr_write_b32 a29, v13\n"
"v_mfma_f32_16x16x32_bf16 v[128:131], a[60:63], a[124:127], v[128:131]\n"
"v_accvgpr_write_b32 a30, v14\n"
"v_mfma_f32_16x16x32_bf16 v[100:103], a[52:55], a[124:127], v[100:103]\n"
"ds_read_b128 a[124:127], v5 offset:23552\n"
"v_accvgpr_write_b32 a31, v15\n"
"v_mfma_f32_16x16x32_bf16 v[100:103], a[48:51], a[120:123], v[100:103]\n"
"ds_read_b128 a[120:123], v5 offset:22528\n"
"v_mfma_f32_16x16x32_bf16 v[104:107], a[48:51], a[128:131], v[104:107]\n"
"v_mfma_f32_16x16x32_bf16 v[104:107], a[52:55], a[132:135], v[104:107]\n"
"global_load_dwordx4 a[40:43], v0, s[34:35] offset:-2048 sc1\n"
"v_mfma_f32_16x16x32_bf16 v[132:135], a[60:63], a[132:135], v[132:135]\n"
"ds_read_b128 a[132:135], v5 offset:25600\n"
"v_mfma_f32_16x16x32_bf16 v[132:135], a[56:59], a[128:131], v[132:135]\n"
"ds_read_b128 a[128:131], v5 offset:24576\n"
"v_mfma_f32_16x16x32_bf16 v[136:139], a[56:59], a[136:139], v[136:139]\n"
"ds_read_b128 v[16:19], v7 offset:32768\n"
"v_mfma_f32_16x16x32_bf16 v[136:139], a[60:63], a[140:143], v[136:139]\n"
"v_mfma_f32_16x16x32_bf16 v[108:111], a[52:55], a[140:143], v[108:111]\n"
"ds_read_b128 a[140:143], v5 offset:27648\n"
"v_mfma_f32_16x16x32_bf16 v[108:111], a[48:51], a[136:139], v[108:111]\n"
"ds_read_b128 a[136:139], v5 offset:26624\n"
"v_mfma_f32_16x16x32_bf16 v[112:115], a[48:51], a[144:147], v[112:115]\n"
"v_mfma_f32_16x16x32_bf16 v[112:115], a[52:55], a[148:151], v[112:115]\n"
"global_load_dwordx4 a[44:47], v0, s[34:35] offset:-1024 sc1\n"
"v_mfma_f32_16x16x32_bf16 v[140:143], a[60:63], a[148:151], v[140:143]\n"
"ds_read_b128 a[148:151], v5 offset:29696\n"
"v_mfma_f32_16x16x32_bf16 v[140:143], a[56:59], a[144:147], v[140:143]\n"
"ds_read_b128 a[144:147], v5 offset:28672\n"
"v_mfma_f32_16x16x32_bf16 a[12:15], a[56:59], a[152:155], a[12:15]\n"
"v_mfma_f32_16x16x32_bf16 a[12:15], a[60:63], a[156:159], a[12:15]\n"
"v_mfma_f32_16x16x32_bf16 a[8:11], a[52:55], a[156:159], a[8:11]\n"
"ds_read_b128 a[156:159], v5 offset:31744\n"
"v_mfma_f32_16x16x32_bf16 a[8:11], a[48:51], a[152:155], a[8:11]\n"
"ds_read_b128 a[152:155], v5 offset:30720\n"
"s_branch L_P6\n"
"L_last:\n"
"L_st8328:\n"
"s_cbranch_execz L_retB\n"
"s_waitcnt vmcnt(9) lgkmcnt(12)\n"
"v_mfma_f32_16x16x32_bf16 v[32:35], a[32:35], a[96:99], v[32:35]\n"
"s_add_u32 s34, s36, 1454080\n"
"s_add_u32 s48, s48, 131072\n"
"s_addc_u32 s49, s49, 0\n"
"v_mfma_f32_16x16x32_bf16 v[32:35], a[36:39], a[100:103], v[32:35]\n"
"s_mov_b64 exec, 0\n"
"s_cmp_eq_u32 %16, 0\n"
"s_cbranch_scc1 L_st8562\n"
"s_cmp_eq_u32 %16, 1\n"
"s_cbranch_scc1 L_st9133\n"
"s_cmp_eq_u32 %16, 2\n"
"s_cbranch_scc1 L_st9745\n"
"s_branch L_st10346\n"
"L_retC:\n"
"s_mov_b64 exec, -1\n"
"s_nop 3\n"
"global_load_dwordx4 a[80:83], v0, s[34:35] offset:0 sc1\n"
"v_mfma_f32_16x16x32_bf16 v[60:63], a[44:47], a[100:103], v[60:63]\n"
"s_barrier\n"
"v_mfma_f32_16x16x32_bf16 v[60:63], a[40:43], a[96:99], v[60:63]\n"
"v_mfma_f32_16x16x32_bf16 v[64:67], a[40:43], a[104:107], v[64:67]\n"
"v_mfma_f32_16x16x32_bf16 v[64:67], a[44:47], a[108:111], v[64:67]\n"
"v_mfma_f32_16x16x32_bf16 v[36:39], a[36:39], a[108:111], v[36:39]\n"
"v_subrev_u32 v8, s65, v8\n"
"v_mfma_f32_16x16x32_bf16 v[36:39], a[32:35], a[104:107], v[36:39]\n"
"s_waitcnt lgkmcnt(8)\n"
"v_xor_b32 v9, s79, v9\n"
"v_mfma_f32_16x16x32_bf16 v[40:43], a[32:35], a[112:115], v[40:43]\n"
"v_or_b32 v8, v8, v9\n"
"v_mfma_f32_16x16x32_bf16 v[40:43], a[36:39], a[116:119], v[40:43]\n"
"global_load_dwordx4 a[84:87], v0, s[34:35] offset:1024 sc1\n"
"v_cmp_lt_u32 vcc, 15, v8\n"
"v_mfma_f32_16x16x32_bf16 v[68:71], a[44:47], a[116:119], v[68:71]\n"
"s_cbranch_vccnz L_slow4\n"
"L_back4:\n"
"v_mfma_f32_16x16x32_bf16 v[68:71], a[40:43], a[112:115], v[68:71]\n"
"v_mfma_f32_16x16x32_bf16 v[72:75], a[40:43], a[120:123], v[72:75]\n"
"v_mfma_f32_16x16x32_bf16 v[72:75], a[44:47], a[124:127], v[72:75]\n"
"v_mfma_f32_16x16x32_bf16 v[44:47], a[36:39], a[124:127], v[44:47]\n"
"v_mfma_f32_16x16x32_bf16 v[44:47], a[32:35], a[120:123], v[44:47]\n"
"s_add_u32 m0, s64, 16384\n"
"s_waitcnt lgkmcnt(4)\n"
"v_mfma_f32_16x16x32_bf16 v[48:51], a[32:35], a[128:131], v[48:51]\n"
"v_mfma_f32_16x16x32_bf16 v[48:51], a[36:39], a[132:135], v[48:51]\n"
"global_load_dwordx4 a[88:91], v0, s[34:35] offset:2048 sc1\n"
"v_mfma_f32_16x16x32_bf16 v[76:79], a[44:47], a[132:135], v[76:79]\n"
"v_mfma_f32_16x16x32_bf16 v[76:79], a[40:43], a[128:131], v[76:79]\n"
"v_mfma_f32_16x16x32_bf16 v[80:83], a[40:43], a[136:139], v[80:83]\n"
"s_mov_b32 s97, s37\n"
"v_mfma_f32_16x16x32_bf16 v[80:83], a[44:47], a[140:143], v[80:83]\n"
"v_mfma_f32_16x16x32_bf16 v[52:55], a[36:39], a[140:143], v[52:55]\n"
"v_mfma_f32_16x16x32_bf16 v[52:55], a[32:35], a[136:139], v[52:55]\n"
"s_waitcnt lgkmcnt(0)\n"
"v_mfma_f32_16x16x32_bf16 v[56:59], a[32:35], a[144:147], v[56:59]\n"
"buffer_load_dwordx4 v0, s[48:51], s75 offen offset:0 sc1 lds\n"
"v_mfma_f32_16x16x32_bf16 v[56:59], a[36:39], a[148:151], v[56:59]\n"
"global_load_dwordx4 a[92:95], v0, s[34:35] offset:3072 sc1\n"
"v_mfma_f32_16x16x32_bf16 v[84:87], a[44:47], a[148:151], v[84:87]\n"
"v_mfma_f32_16x16x32_bf16 v[84:87], a[40:43], a[144:147], v[84:87]\n"
"v_mfma_f32_16x16x32_bf16 a[4:7], a[40:43], a[152:155], a[4:7]\n"
"v_mfma_f32_16x16x32_bf16 a[4:7], a[44:47], a[156:159], a[4:7]\n"
"v_mfma_f32_16x16x32_bf16 a[0:3], a[36:39], a[156:159], a[0:3]\n"
"v_mfma_f32_16x16x32_bf16 a[0:3], a[32:35], a[152:155], a[0:3]\n"
"s_waitcnt vmcnt(10)\n"
"v_mfma_f32_16x16x32_bf16 v[88:91], a[48:51], a[96:99], v[88:91]\n"
"s_add_u32 s34, s36, 1458176\n"
"v_mfma_f32_16x16x32_bf16 v[88:91], a[52:55], a[100:103], v[88:91]\n"
"global_load_dwordx4 a[32:35], v0, s[34:35] offset:0 sc1\n"
"v_mfma_f32_16x16x32_bf16 v[116:119], a[60:63], a[100:103], v[116:119]\n"
"buffer_load_dwordx4 v0, s[48:51], s75 offen offset:1024 sc1 lds\n"
"v_mfma_f32_16x16x32_bf16 v[116:119], a[56:59], a[96:99], v[116:119]\n"
"v_mfma_f32_16x16x32_bf16 v[120:123], a[56:59], a[104:107], v[120:123]\n"
"v_mfma_f32_16x16x32_bf16 v[120:123], a[60:63], a[108:111], v[120:123]\n"
"v_mfma_f32_16x16x32_bf16 v[92:95], a[52:55], a[108:111], v[92:95]\n"
"v_mfma_f32_16x16x32_bf16 v[92:95], a[48:51], a[104:107], v[92:95]\n"
"v_mfma_f32_16x16x32_bf16 v[96:99], a[48:51], a[112:115], v[96:99]\n"
"v_mfma_f32_16x16x32_bf16 v[96:99], a[52:55], a[116:119], v[96:99]\n"
"global_load_dwordx4 a[36:39], v0, s[34:35] offset:1024 sc1\n"
"v_mfma_f32_16x16x32_bf16 v[124:127], a[60:63], a[116:119], v[124:127]\n"
"v_mfma_f32_16x16x32_bf16 v[124:127], a[56:59], a[112:115], v[124:127]\n"
"v_mfma_f32_16x16x32_bf16 v[128:131], a[56:59], a[120:123], v[128:131]\n"
"buffer_load_dwordx4 v0, s[48:51], s75 offen offset:2048 sc1 lds\n"
"v_mfma_f32_16x16x32_bf16 v[128:131], a[60:63], a[124:127], v[128:131]\n"
"v_mfma_f32_16x16x32_bf16 v[100:103], a[52:55], a[124:127], v[100:103]\n"
"v_mfma_f32_16x16x32_bf16 v[100:103], a[48:51], a[120:123], v[100:103]\n"
"v_mfma_f32_16x16x32_bf16 v[104:107], a[48:51], a[128:131], v[104:107]\n"
"v_mfma_f32_16x16x32_bf16 v[104:107], a[52:55], a[132:135], v[104:107]\n"
"global_load_dwordx4 a[40:43], v0, s[34:35] offset:2048 sc1\n"
"v_mfma_f32_16x16x32_bf16 v[132:135], a[60:63], a[132:135], v[132:135]\n"
"v_mfma_f32_16x16x32_bf16 v[132:135], a[56:59], a[128:131], v[132:135]\n"
"v_mfma_f32_16x16x32_bf16 v[136:139], a[56:59], a[136:139], v[136:139]\n"
"v_mfma_f32_16x16x32_bf16 v[136:139], a[60:63], a[140:143], v[136:139]\n"
"buffer_load_dwordx4 v0, s[48:51], s75 offen offset:3072 sc1 lds\n"
"v_mfma_f32_16x16x32_bf16 v[108:111], a[52:55], a[140:143], v[108:111]\n"
"v_mfma_f32_16x16x32_bf16 v[108:111], a[48:51], a[136:139], v[108:111]\n"
"v_mfma_f32_16x16x32_bf16 v[112:115], a[48:51], a[144:147], v[112:115]\n"
"v_mfma_f32_16x16x32_bf16 v[112:115], a[52:55], a[148:151], v[112:115]\n"
"global_load_dwordx4 a[44:47], v0, s[34:35] offset:3072 sc1\n"
"v_mfma_f32_16x16x32_bf16 v[140:143], a[60:63], a[148:151], v[140:143]\n"
"v_mfma_f32_16x16x32_bf16 v[140:143], a[56:59], a[144:147], v[140:143]\n"
"v_mfma_f32_16x16x32_bf16 a[12:15], a[56:59], a[152:155], a[12:15]\n"
"v_mfma_f32_16x16x32_bf16 a[12:15], a[60:63], a[156:159], a[12:15]\n"
"v_mfma_f32_16x16x32_bf16 a[8:11], a[52:55], a[156:159], a[8:11]\n"
"v_mfma_f32_16x16x32_bf16 a[8:11], a[48:51], a[152:155], a[8:11]\n"
"s_add_u32 m0, s64, 20480\n"
"s_waitcnt vmcnt(12)\n"
"v_mfma_f32_16x16x32_bf16 v[144:147], a[64:67], a[96:99], v[144:147]\n"
"s_add_u32 s34, s36, 1462272\n"
"v_mfma_f32_16x16x32_bf16 v[144:147], a[68:71], a[100:103], v[144:147]\n"
"global_load_dwordx4 a[48:51], v0, s[34:35] offset:0 sc1\n"
"v_mfma_f32_16x16x32_bf16 v[172:175], a[76:79], a[100:103], v[172:175]\n"
"v_mfma_f32_16x16x32_bf16 v[172:175], a[72:75], a[96:99], v[172:175]\n"
"v_mfma_f32_16x16x32_bf16 v[176:179], a[72:75], a[104:107], v[176:179]\n"
"v_mfma_f32_16x16x32_bf16 v[176:179], a[76:79], a[108:111], v[176:179]\n"
"v_mfma_f32_16x16x32_bf16 v[148:151], a[68:71], a[108:111], v[148:151]\n"
"v_lshlrev_b32 v16, 16, v18\n"
"v_and_b32 v17, s63, v18\n"
"v_mfma_f32_16x16x32_bf16 v[148:151], a[64:67], a[104:107], v[148:151]\n"
"v_lshlrev_b32 v18, 16, v19\n"
"v_and_b32 v19, s63, v19\n"
"v_mfma_f32_16x16x32_bf16 v[152:155], a[64:67], a[112:115], v[152:155]\n"
"buffer_load_dwordx4 v1, s[48:51], s75 offen offset:0 sc1 lds\n"
"v_mfma_f32_16x16x32_bf16 v[152:155], a[68:71], a[116:119], v[152:155]\n"
"global_load_dwordx4 a[52:55], v0, s[34:35] offset:1024 sc1\n"
"v_add_f32 v88, v88, v16\n"
"v_mfma_f32_16x16x32_bf16 v[180:183], a[76:79], a[116:119], v[180:183]\n"
"v_add_f32 v89, v89, v17\n"
"v_add_f32 v90, v90, v18\n"
"v_mfma_f32_16x16x32_bf16 v[180:183], a[72:75], a[112:115], v[180:183]\n"
"v_add_f32 v91, v91, v19\n"
"v_add_f32 v92, v92, v16\n"
"v_mfma_f32_16x16x32_bf16 v[184:187], a[72:75], a[120:123], v[184:187]\n"
"v_add_f32 v93, v93, v17\n"
"v_add_f32 v94, v94, v18\n"
"v_mfma_f32_16x16x32_bf16 v[184:187], a[76:79], a[124:127], v[184:187]\n"
"v_add_f32 v95, v95, v19\n"
"v_add_f32 v96, v96, v16\n"
"v_mfma_f32_16x16x32_bf16 v[156:159], a[68:71], a[124:127], v[156:159]\n"
"v_add_f32 v97, v97, v17\n"
"v_add_f32 v98, v98, v18\n"
"v_mfma_f32_16x16x32_bf16 v[156:159], a[64:67], a[120:123], v[156:159]\n"
"v_add_f32 v99, v99, v19\n"
"v_add_f32 v100, v100, v16\n"
"v_mfma_f32_16x16x32_bf16 v[160:163], a[64:67], a[128:131], v[160:163]\n"
"v_add_f32 v101, v101, v17\n"
"v_add_f32 v102, v102, v18\n"
"v_mfma_f32_16x16x32_bf16 v[160:163], a[68:71], a[132:135], v[160:163]\n"
"global_load_dwordx4 a[56:59], v0, s[34:35] offset:2048 sc1\n"
"v_add_f32 v103, v103, v19\n"
"v_mfma_f32_16x16x32_bf16 v[188:191], a[76:79], a[132:135], v[188:191]\n"
"buffer_load_dwordx4 v1, s[48:51], s75 offen offset:1024 sc1 lds\n"
"v_mfma_f32_16x16x32_bf16 v[188:191], a[72:75], a[128:131], v[188:191]\n"
"v_add_f32 v104, v104, v16\n"
"v_add_f32 v105, v105, v17\n"
"v_mfma_f32_16x16x32_bf16 v[192:195], a[72:75], a[136:139], v[192:195]\n"
"v_add_f32 v106, v106, v18\n"
"v_add_f32 v107, v107, v19\n"
"v_mfma_f32_16x16x32_bf16 v[192:195], a[76:79], a[140:143], v[192:195]\n"
"v_add_f32 v108, v108, v16\n"
"v_add_f32 v109, v109, v17\n"
"v_mfma_f32_16x16x32_bf16 v[164:167], a[68:71], a[140:143], v[164:167]\n"
"v_add_f32 v110, v110, v18\n"
"v_add_f32 v111, v111, v19\n"
"v_mfma_f32_16x16x32_bf16 v[164:167], a[64:67], a[136:139], v[164:167]\n"
"v_add_f32 v112, v112, v16\n"
"v_add_f32 v113, v113, v17\n"
"v_mfma_f32_16x16x32_bf16 v[168:171], a[64:67], a[144:147], v[168:171]\n"
"v_add_f32 v114, v114, v18\n"
"v_add_f32 v115, v115, v19\n"
"v_mfma_f32_16x16x32_bf16 v[168:171], a[68:71], a[148:151], v[168:171]\n"
"global_load_dwordx4 a[60:63], v0, s[34:35] offset:3072 sc1\n"
"v_accvgpr_read_b32 v12, a8\n"
"v_mfma_f32_16x16x32_bf16 v[196:199], a[76:79], a[148:151], v[196:199]\n"
"v_accvgpr_read_b32 v13, a9\n"
"v_accvgpr_read_b32 v14, a10\n"
"v_mfma_f32_16x16x32_bf16 v[196:199], a[72:75], a[144:147], v[196:199]\n"
"buffer_load_dwordx4 v1, s[48:51], s75 offen offset:2048 sc1 lds\n"
"v_mfma_f32_16x16x32_bf16 a[20:23], a[72:75], a[152:155], a[20:23]\n"
"v_accvgpr_read_b32 v15, a11\n"
"v_add_f32 v12, v12, v16\n"
"v_mfma_f32_16x16x32_bf16 a[20:23], a[76:79], a[156:159], a[20:23]\n"
"v_add_f32 v13, v13, v17\n"
"v_add_f32 v14, v14, v18\n"
"v_mfma_f32_16x16x32_bf16 a[16:19], a[68:71], a[156:159], a[16:19]\n"
"v_add_f32 v15, v15, v19\n"
"v_accvgpr_write_b32 a8, v12\n"
"v_mfma_f32_16x16x32_bf16 a[16:19], a[64:67], a[152:155], a[16:19]\n"
"s_waitcnt vmcnt(14)\n"
"v_accvgpr_write_b32 a9, v13\n"
"v_accvgpr_write_b32 a10, v14\n"
"v_accvgpr_write_b32 a11, v15\n"
"v_mfma_f32_16x16x32_bf16 v[200:203], a[80:83], a[96:99], v[200:203]\n"
"s_add_u32 s34, s36, 1474560\n"
"v_lshlrev_b32 v20, 16, v22\n"
"v_and_b32 v21, s63, v22\n"
"v_mfma_f32_16x16x32_bf16 v[200:203], a[84:87], a[100:103], v[200:203]\n"
"global_load_dwordx4 a[64:67], v0, s[34:35] offset:0 sc1\n"
"v_lshlrev_b32 v22, 16, v23\n"
"v_mfma_f32_16x16x32_bf16 v[228:231], a[92:95], a[100:103], v[228:231]\n"
"ds_read_b128 a[100:103], v3 offset:1024\n"
"v_and_b32 v23, s63, v23\n"
"v_mfma_f32_16x16x32_bf16 v[228:231], a[88:91], a[96:99], v[228:231]\n"
"ds_read_b128 a[96:99], v3 offset:0\n"
"v_add_f32 v116, v116, v20\n"
"v_mfma_f32_16x16x32_bf16 v[232:235], a[88:91], a[104:107], v[232:235]\n"
"v_add_f32 v117, v117, v21\n"
"v_add_f32 v118, v118, v22\n"
"v_mfma_f32_16x16x32_bf16 v[232:235], a[92:95], a[108:111], v[232:235]\n"
"buffer_load_dwordx4 v1, s[48:51], s75 offen offset:3072 sc1 lds\n"
"v_mfma_f32_16x16x32_bf16 v[204:207], a[84:87], a[108:111], v[204:207]\n"
"ds_read_b128 a[108:111], v3 offset:3072\n"
"v_add_f32 v119, v119, v23\n"
"v_add_f32 v120, v120, v20\n"
"v_mfma_f32_16x16x32_bf16 v[204:207], a[80:83], a[104:107], v[204:207]\n"
"ds_read_b128 a[104:107], v3 offset:2048\n"
"v_add_f32 v121, v121, v21\n"
"v_mfma_f32_16x16x32_bf16 v[208:211], a[80:83], a[112:115], v[208:211]\n"
"v_add_f32 v122, v122, v22\n"
"v_add_f32 v123, v123, v23\n"
"v_mfma_f32_16x16x32_bf16 v[208:211], a[84:87], a[116:119], v[208:211]\n"
"global_load_dwordx4 a[68:71], v0, s[34:35] offset:1024 sc1\n"
"v_add_f32 v124, v124, v20\n"
"v_add_f32 v125, v125, v21\n"
"v_mfma_f32_16x16x32_bf16 v[236:239], a[92:95], a[116:119], v[236:239]\n"
"ds_read_b128 a[116:119], v3 offset:5120\n"
"v_add_f32 v126, v126, v22\n"
"v_mfma_f32_16x16x32_bf16 v[236:239], a[88:91], a[112:115], v[236:239]\n"
"ds_read_b128 a[112:115], v3 offset:4096\n"
"v_add_f32 v127, v127, v23\n"
"v_mfma_f32_16x16x32_bf16 v[240:243], a[88:91], a[120:123], v[240:243]\n"
"v_add_f32 v128, v128, v20\n"
"v_add_f32 v129, v129, v21\n"
"v_mfma_f32_16x16x32_bf16 v[240:243], a[92:95], a[124:127], v[240:243]\n"
"L_st8562:\n"
"s_cbranch_execz L_st10967\n"
"v_add_f32 v130, v130, v22\n"
"v_add_f32 v131, v131, v23\n"
"v_mfma_f32_16x16x32_bf16 v[212:215], a[84:87], a[124:127], v[212:215]\n"
"s_add_u32 m0, s64, 24576\n"
"ds_read_b128 a[124:127], v3 offset:7168\n"
"v_add_f32 v132, v132, v20\n"
"v_add_f32 v133, v133, v21\n"
"v_mfma_f32_16x16x32_bf16 v[212:215], a[80:83], a[120:123], v[212:215]\n"
"ds_read_b128 a[120:123], v3 offset:6144\n"
"v_add_f32 v134, v134, v22\n"
"v_mfma_f32_16x16x32_bf16 v[216:219], a[80:83], a[128:131], v[216:219]\n"
"v_add_f32 v135, v135, v23\n"
"v_add_f32 v136, v136, v20\n"
"v_mfma_f32_16x16x32_bf16 v[216:219], a[84:87], a[132:135], v[216:219]\n"
"global_load_dwordx4 a[72:75], v0, s[34:35] offset:2048 sc1\n"
"v_add_f32 v137, v137, v21\n"
"v_mfma_f32_16x16x32_bf16 v[244:247], a[92:95], a[132:135], v[244:247]\n"
"ds_read_b128 a[132:135], v3 offset:9216\n"
"v_add_f32 v138, v138, v22\n"
"v_add_f32 v139, v139, v23\n"
"v_mfma_f32_16x16x32_bf16 v[244:247], a[88:91], a[128:131], v[244:247]\n"
"ds_read_b128 a[128:131], v3 offset:8192\n"
"v_add_f32 v140, v140, v20\n"
"v_mfma_f32_16x16x32_bf16 v[248:251], a[88:91], a[136:139], v[248:251]\n"
"v_add_f32 v141, v141, v21\n"
"v_add_f32 v142, v142, v22\n"
"v_mfma_f32_16x16x32_bf16 v[248:251], a[92:95], a[140:143], v[248:251]\n"
"v_add_f32 v143, v143, v23\n"
"v_accvgpr_read_b32 v12, a12\n"
"v_mfma_f32_16x16x32_bf16 v[220:223], a[84:87], a[140:143], v[220:223]\n"
"ds_read_b128 a[140:143], v3 offset:11264\n"
"v_accvgpr_read_b32 v13, a13\n"
"v_mfma_f32_16x16x32_bf16 v[220:223], a[80:83], a[136:139], v[220:223]\n"
"ds_read_b128 a[136:139], v3 offset:10240\n"
"v_accvgpr_read_b32 v14, a14\n"
"v_accvgpr_read_b32 v15, a15\n"
"v_mfma_f32_16x16x32_bf16 v[224:227], a[80:83], a[144:147], v[224:227]\n"
"buffer_load_dwordx4 v0, s[48:51], s69 offen offset:0 sc1 lds\n"
"v_add_f32 v12, v12, v20\n"
"v_mfma_f32_16x16x32_bf16 v[224:227], a[84:87], a[148:151], v[224:227]\n"
"global_load_dwordx4 a[76:79], v0, s[34:35] offset:3072 sc1\n"
"v_add_f32 v13, v13, v21\n"
"v_add_f32 v14, v14, v22\n"
"v_mfma_f32_16x16x32_bf16 v[252:255], a[92:95], a[148:151], v[252:255]\n"
"ds_read_b128 a[148:151], v3 offset:13312\n"
"v_add_f32 v15, v15, v23\n"
"v_accvgpr_write_b32 a12, v12\n"
"v_mfma_f32_16x16x32_bf16 v[252:255], a[88:91], a[144:147], v[252:255]\n"
"ds_read_b128 a[144:147], v3 offset:12288\n"
"v_accvgpr_write_b32 a13, v13\n"
"v_mfma_f32_16x16x32_bf16 a[28:31], a[88:91], a[152:155], a[28:31]\n"
"v_accvgpr_write_b32 a14, v14\n"
"v_accvgpr_write_b32 a15, v15\n"
"v_mfma_f32_16x16x32_bf16 a[28:31], a[92:95], a[156:159], a[28:31]\n"
"v_mfma_f32_16x16x32_bf16 a[24:27], a[84:87], a[156:159], a[24:27]\n"
"ds_read_b128 a[156:159], v3 offset:15360\n"
"v_mfma_f32_16x16x32_bf16 a[24:27], a[80:83], a[152:155], a[24:27]\n"
"s_waitcnt vmcnt(13) lgkmcnt(11)\n"
"ds_read_b128 a[152:155], v3 offset:14336\n"
"v_mfma_f32_16x16x32_bf16 v[32:35], a[32:35], a[96:99], v[32:35]\n"
"s_add_u32 s34, s36, 1478656\n"
"v_mfma_f32_16x16x32_bf16 v[32:35], a[36:39], a[100:103], v[32:35]\n"
"global_load_dwordx4 a[80:83], v0, s[34:35] offset:0 sc1\n"
"v_mfma_f32_16x16x32_bf16 v[60:63], a[44:47], a[100:103], v[60:63]\n"
"buffer_load_dwordx4 v0, s[48:51], s69 offen offset:1024 sc1 lds\n"
"v_mfma_f32_16x16x32_bf16 v[60:63], a[40:43], a[96:99], v[60:63]\n"
"v_lshrrev_b32 v16, 4, v0\n"
"v_mfma_f32_16x16x32_bf16 v[64:67], a[40:43], a[104:107], v[64:67]\n"
"v_mfma_f32_16x16x32_bf16 v[64:67], a[44:47], a[108:111], v[64:67]\n"
"v_mfma_f32_16x16x32_bf16 v[36:39], a[36:39], a[108:111], v[36:39]\n"
"v_mfma_f32_16x16x32_bf16 v[36:39], a[32:35], a[104:107], v[36:39]\n"
"s_waitcnt lgkmcnt(8)\n"
"v_and_b32 v16, 0x30, v16\n"
"v_mfma_f32_16x16x32_bf16 v[40:43], a[32:35], a[112:115], v[40:43]\n"
"v_mfma_f32_16x16x32_bf16 v[40:43], a[36:39], a[116:119], v[40:43]\n"
"global_load_dwordx4 a[84:87], v0, s[34:35] offset:1024 sc1\n"
"v_mfma_f32_16x16x32_bf16 v[68:71], a[44:47], a[116:119], v[68:71]\n"
"buffer_load_dwordx2 v[18:19], v16, s[44:47], s61 offen offset:128\n"
"v_mfma_f32_16x16x32_bf16 v[68:71], a[40:43], a[112:115], v[68:71]\n"
"buffer_load_dwordx4 v0, s[48:51], s69 offen offset:2048 sc1 lds\n"
"v_mfma_f32_16x16x32_bf16 v[72:75], a[40:43], a[120:123], v[72:75]\n"
"v_mfma_f32_16x16x32_bf16 v[72:75], a[44:47], a[124:127], v[72:75]\n"
"v_lshrrev_b32 v20, 4, v0\n"
"v_mfma_f32_16x16x32_bf16 v[44:47], a[36:39], a[124:127], v[44:47]\n"
"v_mfma_f32_16x16x32_bf16 v[44:47], a[32:35], a[120:123], v[44:47]\n"
"s_waitcnt lgkmcnt(4)\n"
"v_mfma_f32_16x16x32_bf16 v[48:51], a[32:35], a[128:131], v[48:51]\n"
"v_and_b32 v20, 0x30, v20\n"
"v_mfma_f32_16x16x32_bf16 v[48:51], a[36:39], a[132:135], v[48:51]\n"
"global_load_dwordx4 a[88:91], v0, s[34:35] offset:2048 sc1\n"
"v_mfma_f32_16x16x32_bf16 v[76:79], a[44:47], a[132:135], v[76:79]\n"
"v_mfma_f32_16x16x32_bf16 v[76:79], a[40:43], a[128:131], v[76:79]\n"
"v_mfma_f32_16x16x32_bf16 v[80:83], a[40:43], a[136:139], v[80:83]\n"
"buffer_load_dwordx4 v0, s[48:51], s69 offen offset:3072 sc1 lds\n"
"v_mfma_f32_16x16x32_bf16 v[80:83], a[44:47], a[140:143], v[80:83]\n"
"buffer_load_dwordx2 v[22:23], v20, s[44:47], s61 offen offset:136\n"
"v_mfma_f32_16x16x32_bf16 v[52:55], a[36:39], a[140:143], v[52:55]\n"
"v_mfma_f32_16x16x32_bf16 v[52:55], a[32:35], a[136:139], v[52:55]\n"
"s_waitcnt lgkmcnt(0)\n"
"v_mfma_f32_16x16x32_bf16 v[56:59], a[32:35], a[144:147], v[56:59]\n"
"v_lshrrev_b32 v24, 4, v0\n"
"v_mfma_f32_16x16x32_bf16 v[56:59], a[36:39], a[148:151], v[56:59]\n"
"global_load_dwordx4 a[92:95], v0, s[34:35] offset:3072 sc1\n"
"v_mfma_f32_16x16x32_bf16 v[84:87], a[44:47], a[148:151], v[84:87]\n"
"v_mfma_f32_16x16x32_bf16 v[84:87], a[40:43], a[144:147], v[84:87]\n"
"v_and_b32 v24, 0x30, v24\n"
"v_mfma_f32_16x16x32_bf16 a[4:7], a[40:43], a[152:155], a[4:7]\n"
"v_mfma_f32_16x16x32_bf16 a[4:7], a[44:47], a[156:159], a[4:7]\n"
"v_mfma_f32_16x16x32_bf16 a[0:3], a[36:39], a[156:159], a[0:3]\n"
"s_add_u32 m0, s64, 28672\n"
"v_mfma_f32_16x16x32_bf16 a[0:3], a[32:35], a[152:155], a[0:3]\n"
"s_waitcnt vmcnt(16)\n"
"buffer_load_dwordx2 v[26:27], v24, s[44:47], s61 offen offset:192\n"
"v_mfma_f32_16x16x32_bf16 v[88:91], a[48:51], a[96:99], v[88:91]\n"
"s_add_u32 s34, s36, 1490944\n"
"v_lshrrev_b32 v28, 4, v0\n"
"v_mfma_f32_16x16x32_bf16 v[88:91], a[52:55], a[100:103], v[88:91]\n"
"global_load_dwordx4 a[32:35], v0, s[34:35] offset:0 sc1\n"
"v_mfma_f32_16x16x32_bf16 v[116:119], a[60:63], a[100:103], v[116:119]\n"
"ds_read_b128 a[100:103], v4 offset:1024\n"
"v_mfma_f32_16x16x32_bf16 v[116:119], a[56:59], a[96:99], v[116:119]\n"
"ds_read_b128 a[96:99], v4 offset:0\n"
"v_mfma_f32_16x16x32_bf16 v[120:123], a[56:59], a[104:107], v[120:123]\n"
"v_and_b32 v28, 0x30, v28\n"
"v_mfma_f32_16x16x32_bf16 v[120:123], a[60:63], a[108:111], v[120:123]\n"
"buffer_load_dwordx2 v[30:31], v28, s[44:47], s61 offen offset:200\n"
"v_mfma_f32_16x16x32_bf16 v[92:95], a[52:55], a[108:111], v[92:95]\n"
"ds_read_b128 a[108:111], v4 offset:3072\n"
"v_mfma_f32_16x16x32_bf16 v[92:95], a[48:51], a[104:107], v[92:95]\n"
"ds_read_b128 a[104:107], v4 offset:2048\n"
"v_mfma_f32_16x16x32_bf16 v[96:99], a[48:51], a[112:115], v[96:99]\n"
"buffer_load_dwordx4 v1, s[48:51], s69 offen offset:0 sc1 lds\n"
"v_mfma_f32_16x16x32_bf16 v[96:99], a[52:55], a[116:119], v[96:99]\n"
"global_load_dwordx4 a[36:39], v0, s[34:35] offset:1024 sc1\n"
"v_mfma_f32_16x16x32_bf16 v[124:127], a[60:63], a[116:119], v[124:127]\n"
"ds_read_b128 a[116:119], v4 offset:5120\n"
"v_mfma_f32_16x16x32_bf16 v[124:127], a[56:59], a[112:115], v[124:127]\n"
"ds_read_b128 a[112:115], v4 offset:4096\n"
"v_mfma_f32_16x16x32_bf16 v[128:131], a[56:59], a[120:123], v[128:131]\n"
"v_mfma_f32_16x16x32_bf16 v[128:131], a[60:63], a[124:127], v[128:131]\n"
"v_mfma_f32_16x16x32_bf16 v[100:103], a[52:55], a[124:127], v[100:103]\n"
"ds_read_b128 a[124:127], v4 offset:7168\n"
"v_mfma_f32_16x16x32_bf16 v[100:103], a[48:51], a[120:123], v[100:103]\n"
"ds_read_b128 a[120:123], v4 offset:6144\n"
"v_mfma_f32_16x16x32_bf16 v[104:107], a[48:51], a[128:131], v[104:107]\n"
"buffer_load_dwordx4 v1, s[48:51], s69 offen offset:1024 sc1 lds\n"
"v_mfma_f32_16x16x32_bf16 v[104:107], a[52:55], a[132:135], v[104:107]\n"
"global_load_dwordx4 a[40:43], v0, s[34:35] offset:2048 sc1\n"
"v_mfma_f32_16x16x32_bf16 v[132:135], a[60:63], a[132:135], v[132:135]\n"
"ds_read_b128 a[132:135], v4 offset:9216\n"
"v_mfma_f32_16x16x32_bf16 v[132:135], a[56:59], a[128:131], v[132:135]\n"
"ds_read_b128 a[128:131], v4 offset:8192\n"
"v_mfma_f32_16x16x32_bf16 v[136:139], a[56:59], a[136:139], v[136:139]\n"
"v_mfma_f32_16x16x32_bf16 v[136:139], a[60:63], a[140:143], v[136:139]\n"
"v_mfma_f32_16x16x32_bf16 v[108:111], a[52:55], a[140:143], v[108:111]\n"
"ds_read_b128 a[140:143], v4 offset:11264\n"
"v_mfma_f32_16x16x32_bf16 v[108:111], a[48:51], a[136:139], v[108:111]\n"
"ds_read_b128 a[136:139], v4 offset:10240\n"
"v_mfma_f32_16x16x32_bf16 v[112:115], a[48:51], a[144:147], v[112:115]\n"
"v_mfma_f32_16x16x32_bf16 v[112:115], a[52:55], a[148:151], v[112:115]\n"
"global_load_dwordx4 a[44:47], v0, s[34:35] offset:3072 sc1\n"
"v_mfma_f32_16x16x32_bf16 v[140:143], a[60:63], a[148:151], v[140:143]\n"
"ds_read_b128 a[148:151], v4 offset:13312\n"
"v_mfma_f32_16x16x32_bf16 v[140:143], a[56:59], a[144:147], v[140:143]\n"
"ds_read_b128 a[144:147], v4 offset:12288\n"
"v_mfma_f32_16x16x32_bf16 a[12:15], a[56:59], a[152:155], a[12:15]\n"
"buffer_load_dwordx4 v1, s[48:51], s69 offen offset:2048 sc1 lds\n"
"v_mfma_f32_16x16x32_bf16 a[12:15], a[60:63], a[156:159], a[12:15]\n"
"v_mfma_f32_16x16x32_bf16 a[8:11], a[52:55], a[156:159], a[8:11]\n"
"ds_read_b128 a[156:159], v4 offset:15360\n"
"v_mfma_f32_16x16x32_bf16 a[8:11], a[48:51], a[152:155], a[8:11]\n"
"s_waitcnt vmcnt(18) lgkmcnt(11)\n"
"ds_read_b128 a[152:155], v4 offset:14336\n"
"v_mfma_f32_16x16x32_bf16 v[32:35], a[64:67], a[96:99], v[32:35]\n"
"s_add_u32 s34, s36, 1495040\n"
"v_mfma_f32_16x16x32_bf16 v[32:35], a[68:71], a[100:103], v[32:35]\n"
"global_load_dwordx4 a[48:51], v0, s[34:35] offset:0 sc1\n"
"v_mfma_f32_16x16x32_bf16 v[60:63], a[76:79], a[100:103], v[60:63]\n"
"v_mfma_f32_16x16x32_bf16 v[60:63], a[72:75], a[96:99], v[60:63]\n"
"v_mfma_f32_16x16x32_bf16 v[64:67], a[72:75], a[104:107], v[64:67]\n"
"buffer_load_dwordx4 v1, s[48:51], s69 offen offset:3072 sc1 lds\n"
"v_mfma_f32_16x16x32_bf16 v[64:67], a[76:79], a[108:111], v[64:67]\n"
"v_mfma_f32_16x16x32_bf16 v[36:39], a[68:71], a[108:111], v[36:39]\n"
"v_mfma_f32_16x16x32_bf16 v[36:39], a[64:67], a[104:107], v[36:39]\n"
"s_waitcnt lgkmcnt(8)\n"
"v_mfma_f32_16x16x32_bf16 v[40:43], a[64:67], a[112:115], v[40:43]\n"
"v_mfma_f32_16x16x32_bf16 v[40:43], a[68:71], a[116:119], v[40:43]\n"
"global_load_dwordx4 a[52:55], v0, s[34:35] offset:1024 sc1\n"
"v_mfma_f32_16x16x32_bf16 v[68:71], a[76:79], a[116:119], v[68:71]\n"
"v_mfma_f32_16x16x32_bf16 v[68:71], a[72:75], a[112:115], v[68:71]\n"
"v_mfma_f32_16x16x32_bf16 v[72:75], a[72:75], a[120:123], v[72:75]\n"
"v_mfma_f32_16x16x32_bf16 v[72:75], a[76:79], a[124:127], v[72:75]\n"
"v_mfma_f32_16x16x32_bf16 v[44:47], a[68:71], a[124:127], v[44:47]\n"
"v_mfma_f32_16x16x32_bf16 v[44:47], a[64:67], a[120:123], v[44:47]\n"
"s_waitcnt lgkmcnt(4)\n"
"v_mfma_f32_16x16x32_bf16 v[48:51], a[64:67], a[128:131], v[48:51]\n"
"v_mfma_f32_16x16x32_bf16 v[48:51], a[68:71], a[132:135], v[48:51]\n"
"global_load_dwordx4 a[56:59], v0, s[34:35] offset:2048 sc1\n"
"v_mfma_f32_16x16x32_bf16 v[76:79], a[76:79], a[132:135], v[76:79]\n"
"v_mfma_f32_16x16x32_bf16 v[76:79], a[72:75], a[128:131], v[76:79]\n"
"v_mfma_f32_16x16x32_bf16 v[80:83], a[72:75], a[136:139], v[80:83]\n"
"v_mfma_f32_16x16x32_bf16 v[80:83], a[76:79], a[140:143], v[80:83]\n"
"v_mfma_f32_16x16x32_bf16 v[52:55], a[68:71], a[140:143], v[52:55]\n"
"v_mfma_f32_16x16x32_bf16 v[52:55], a[64:67], a[136:139], v[52:55]\n"
"s_waitcnt lgkmcnt(0)\n"
"v_mfma_f32_16x16x32_bf16 v[56:59], a[64:67], a[144:147], v[56:59]\n"
"v_mfma_f32_16x16x32_bf16 v[56:59], a[68:71], a[148:151], v[56:59]\n"
"global_load_dwordx4 a[60:63], v0, s[34:35] offset:3072 sc1\n"
"v_mfma_f32_16x16x32_bf16 v[84:87], a[76:79], a[148:151], v[84:87]\n"
"v_mfma_f32_16x16x32_bf16 v[84:87], a[72:75], a[144:147], v[84:87]\n"
"v_mfma_f32_16x16x32_bf16 a[4:7], a[72:75], a[152:155], a[4:7]\n"
"v_mfma_f32_16x16x32_bf16 a[4:7], a[76:79], a[156:159], a[4:7]\n"
"v_mfma_f32_16x16x32_bf16 a[0:3], a[68:71], a[156:159], a[0:3]\n"
"v_mfma_f32_16x16x32_bf16 a[0:3], a[64:67], a[152:155], a[0:3]\n"
"s_waitcnt vmcnt(14)\n"
"v_mfma_f32_16x16x32_bf16 v[88:91], a[80:83], a[96:99], v[88:91]\n"
"s_add_u32 s34, s36, 1507328\n"
"v_mfma_f32_16x16x32_bf16 v[88:91], a[84:87], a[100:103], v[88:91]\n"
"global_load_dwordx4 a[64:67], v0, s[34:35] offset:0 sc1\n"
"v_mfma_f32_16x16x32_bf16 v[116:119], a[92:95], a[100:103], v[116:119]\n"
"ds_read_b128 a[100:103], v5 offset:1024\n"
"v_mfma_f32_16x16x32_bf16 v[116:119], a[88:91], a[96:99], v[116:119]\n"
"ds_read_b128 a[96:99], v5 offset:0\n"
"v_mfma_f32_16x16x32_bf16 v[120:123], a[88:91], a[104:107], v[120:123]\n"
"v_mfma_f32_16x16x32_bf16 v[120:123], a[92:95], a[108:111], v[120:123]\n"
"v_mfma_f32_16x16x32_bf16 v[92:95], a[84:87], a[108:111], v[92:95]\n"
"ds_read_b128 a[108:111], v5 offset:3072\n"
"v_mfma_f32_16x16x32_bf16 v[92:95], a[80:83], a[104:107], v[92:95]\n"
"ds_read_b128 a[104:107], v5 offset:2048\n"
"v_mfma_f32_16x16x32_bf16 v[96:99], a[80:83], a[112:115], v[96:99]\n"
"v_mfma_f32_16x16x32_bf16 v[96:99], a[84:87], a[116:119], v[96:99]\n"
"global_load_dwordx4 a[68:71], v0, s[34:35] offset:1024 sc1\n"
"v_mfma_f32_16x16x32_bf16 v[124:127], a[92:95], a[116:119], v[124:127]\n"
"ds_read_b128 a[116:119], v5 offset:5120\n"
"v_mfma_f32_16x16x32_bf16 v[124:127], a[88:91], a[112:115], v[124:127]\n"
"ds_read_b128 a[112:115], v5 offset:4096\n"
"v_mfma_f32_16x16x32_bf16 v[128:131], a[88:91], a[120:123], v[128:131]\n"
"v_mfma_f32_16x16x32_bf16 v[128:131], a[92:95], a[124:127], v[128:131]\n"
"v_mfma_f32_16x16x32_bf16 v[100:103], a[84:87], a[124:127], v[100:103]\n"
"ds_read_b128 a[124:127], v5 offset:7168\n"
"v_mfma_f32_16x16x32_bf16 v[100:103], a[80:83], a[120:123], v[100:103]\n"
"ds_read_b128 a[120:123], v5 offset:6144\n"
"v_mfma_f32_16x16x32_bf16 v[104:107], a[80:83], a[128:131], v[104:107]\n"
"v_mfma_f32_16x16x32_bf16 v[104:107], a[84:87], a[132:135], v[104:107]\n"
"global_load_dwordx4 a[72:75], v0, s[34:35] offset:2048 sc1\n"
"v_mfma_f32_16x16x32_bf16 v[132:135], a[92:95], a[132:135], v[132:135]\n"
"ds_read_b128 a[132:135], v5 offset:9216\n"
"v_mfma_f32_16x16x32_bf16 v[132:135], a[88:91], a[128:131], v[132:135]\n"
"ds_read_b128 a[128:131], v5 offset:8192\n"
"v_mfma_f32_16x16x32_bf16 v[136:139], a[88:91], a[136:139], v[136:139]\n"
"v_mfma_f32_16x16x32_bf16 v[136:139], a[92:95], a[140:143], v[136:139]\n"
"v_mfma_f32_16x16x32_bf16 v[108:111], a[84:87], a[140:143], v[108:111]\n"
"ds_read_b128 a[140:143], v5 offset:11264\n"
"v_mfma_f32_16x16x32_bf16 v[108:111], a[80:83], a[136:139], v[108:111]\n"
"ds_read_b128 a[136:139], v5 offset:10240\n"
"v_mfma_f32_16x16x32_bf16 v[112:115], a[80:83], a[144:147], v[112:115]\n"
"v_mfma_f32_16x16x32_bf16 v[112:115], a[84:87], a[148:151], v[112:115]\n"
"global_load_dwordx4 a[76:79], v0, s[34:35] offset:3072 sc1\n"
"v_mfma_f32_16x16x32_bf16 v[140:143], a[92:95], a[148:151], v[140:143]\n"
"ds_read_b128 a[148:151], v5 offset:13312\n"
"v_mfma_f32_16x16x32_bf16 v[140:143], a[88:91], a[144:147], v[140:143]\n"
"ds_read_b128 a[144:147], v5 offset:12288\n"
"v_mfma_f32_16x16x32_bf16 a[12:15], a[88:91], a[152:155], a[12:15]\n"
"v_mfma_f32_16x16x32_bf16 a[12:15], a[92:95], a[156:159], a[12:15]\n"
"v_mfma_f32_16x16x32_bf16 a[8:11], a[84:87], a[156:159], a[8:11]\n"
"ds_read_b128 a[156:159], v5 offset:15360\n"
"v_mfma_f32_16x16x32_bf16 a[8:11], a[80:83], a[152:155], a[8:11]\n"
"s_waitcnt vmcnt(10) lgkmcnt(11)\n"
"ds_read_b128 a[152:155], v5 offset:14336\n"
"v_mfma_f32_16x16x32_bf16 v[32:35], a[32:35], a[96:99], v[32:35]\n"
"s_add_u32 s34, s36, 1511424\n"
"v_mfma_f32_16x16x32_bf16 v[32:35], a[36:39], a[100:103], v[32:35]\n"
"global_load_dwordx4 a[80:83], v0, s[34:35] offset:0 sc1\n"
"v_mfma_f32_16x16x32_bf16 v[60:63], a[44:47], a[100:103], v[60:63]\n"
"v_mfma_f32_16x16x32_bf16 v[60:63], a[40:43], a[96:99], v[60:63]\n"
"v_mfma_f32_16x16x32_bf16 v[64:67], a[40:43], a[104:107], v[64:67]\n"
"v_mfma_f32_16x16x32_bf16 v[64:67], a[44:47], a[108:111], v[64:67]\n"
"v_mfma_f32_16x16x32_bf16 v[36:39], a[36:39], a[108:111], v[36:39]\n"
"v_mfma_f32_16x16x32_bf16 v[36:39], a[32:35], a[104:107], v[36:39]\n"
"s_waitcnt lgkmcnt(8)\n"
"v_mfma_f32_16x16x32_bf16 v[40:43], a[32:35], a[112:115], v[40:43]\n"
"v_mfma_f32_16x16x32_bf16 v[40:43], a[36:39], a[116:119], v[40:43]\n"
"global_load_dwordx4 a[84:87], v0, s[34:35] offset:1024 sc1\n"
"v_mfma_f32_16x16x32_bf16 v[68:71], a[44:47], a[116:119], v[68:71]\n"
"v_mfma_f32_16x16x32_bf16 v[68:71], a[40:43], a[112:115], v[68:71]\n"
"v_mfma_f32_16x16x32_bf16 v[72:75], a[40:43], a[120:123], v[72:75]\n"
"v_mfma_f32_16x16x32_bf16 v[72:75], a[44:47], a[124:127], v[72:75]\n"
"v_mfma_f32_16x16x32_bf16 v[44:47], a[36:39], a[124:127], v[44:47]\n"
"v_mfma_f32_16x16x32_bf16 v[44:47], a[32:35], a[120:123], v[44:47]\n"
"s_waitcnt lgkmcnt(4)\n"
"v_mfma_f32_16x16x32_bf16 v[48:51], a[32:35], a[128:131], v[48:51]\n"
"v_mfma_f32_16x16x32_bf16 v[48:51], a[36:39], a[132:135], v[48:51]\n"
"global_load_dwordx4 a[88:91], v0, s[34:35] offset:2048 sc1\n"
"v_mfma_f32_16x16x32_bf16 v[76:79], a[44:47], a[132:135], v[76:79]\n"
"v_mfma_f32_16x16x32_bf16 v[76:79], a[40:43], a[128:131], v[76:79]\n"
"v_mfma_f32_16x16x32_bf16 v[80:83], a[40:43], a[136:139], v[80:83]\n"
"v_mfma_f32_16x16x32_bf16 v[80:83], a[44:47], a[140:143], v[80:83]\n"
"v_mfma_f32_16x16x32_bf16 v[52:55], a[36:39], a[140:143], v[52:55]\n"
"v_mfma_f32_16x16x32_bf16 v[52:55], a[32:35], a[136:139], v[52:55]\n"
"s_waitcnt lgkmcnt(0)\n"
"v_mfma_f32_16x16x32_bf16 v[56:59], a[32:35], a[144:147], v[56:59]\n"
"v_mfma_f32_16x16x32_bf16 v[56:59], a[36:39], a[148:151], v[56:59]\n"
"global_load_dwordx4 a[92:95], v0, s[34:35] offset:3072 sc1\n"
"v_mfma_f32_16x16x32_bf16 v[84:87], a[44:47], a[148:151], v[84:87]\n"
"v_mfma_f32_16x16x32_bf16 v[84:87], a[40:43], a[144:147], v[84:87]\n"
"v_mfma_f32_16x16x32_bf16 a[4:7], a[40:43], a[152:155], a[4:7]\n"
"v_mfma_f32_16x16x32_bf16 a[4:7], a[44:47], a[156:159], a[4:7]\n"
"v_mfma_f32_16x16x32_bf16 a[0:3], a[36:39], a[156:159], a[0:3]\n"
"v_mfma_f32_16x16x32_bf16 a[0:3], a[32:35], a[152:155], a[0:3]\n"
"s_waitcnt vmcnt(8)\n"
"v_mfma_f32_16x16x32_bf16 v[88:91], a[48:51], a[96:99], v[88:91]\n"
"s_add_u32 s34, s36, 1515520\n"
"v_mfma_f32_16x16x32_bf16 v[88:91], a[52:55], a[100:103], v[88:91]\n"
"global_load_dwordx4 a[32:35], v0, s[34:35] offset:0 sc1\n"
"v_mfma_f32_16x16x32_bf16 v[116:119], a[60:63], a[100:103], v[116:119]\n"
"ds_read_b128 a[100:103], v2 offset:17408\n"
"v_mfma_f32_16x16x32_bf16 v[116:119], a[56:59], a[96:99], v[116:119]\n"
"ds_read_b128 a[96:99], v2 offset:16384\n"
"v_mfma_f32_16x16x32_bf16 v[120:123], a[56:59], a[104:107], v[120:123]\n"
"v_mfma_f32_16x16x32_bf16 v[120:123], a[60:63], a[108:111], v[120:123]\n"
"v_mfma_f32_16x16x32_bf16 v[92:95], a[52:55], a[108:111], v[92:95]\n"
"ds_read_b128 a[108:111], v2 offset:19456\n"
"v_mfma_f32_16x16x32_bf16 v[92:95], a[48:51], a[104:107], v[92:95]\n"
"ds_read_b128 a[104:107], v2 offset:18432\n"
"v_mfma_f32_16x16x32_bf16 v[96:99], a[48:51], a[112:115], v[96:99]\n"
"v_mfma_f32_16x16x32_bf16 v[96:99], a[52:55], a[116:119], v[96:99]\n"
"global_load_dwordx4 a[36:39], v0, s[34:35] offset:1024 sc1\n"
"v_mfma_f32_16x16x32_bf16 v[124:127], a[60:63], a[116:119], v[124:127]\n"
"ds_read_b128 a[116:119], v2 offset:21504\n"
"v_mfma_f32_16x16x32_bf16 v[124:127], a[56:59], a[112:115], v[124:127]\n"
"ds_read_b128 a[112:115], v2 offset:20480\n"
"v_mfma_f32_16x16x32_bf16 v[128:131], a[56:59], a[120:123], v[128:131]\n"
"v_mfma_f32_16x16x32_bf16 v[128:131], a[60:63], a[124:127], v[128:131]\n"
"v_mfma_f32_16x16x32_bf16 v[100:103], a[52:55], a[124:127], v[100:103]\n"
"ds_read_b128 a[124:127], v2 offset:23552\n"
"v_mfma_f32_16x16x32_bf16 v[100:103], a[48:51], a[120:123], v[100:103]\n"
"ds_read_b128 a[120:123], v2 offset:22528\n"
"v_mfma_f32_16x16x32_bf16 v[104:107], a[48:51], a[128:131], v[104:107]\n"
"v_mfma_f32_16x16x32_bf16 v[104:107], a[52:55], a[132:135], v[104:107]\n"
"global_load_dwordx4 a[40:43], v0, s[34:35] offset:2048 sc1\n"
"v_mfma_f32_16x16x32_bf16 v[132:135], a[60:63], a[132:135], v[132:135]\n"
"ds_read_b128 a[132:135], v2 offset:25600\n"
"v_mfma_f32_16x16x32_bf16 v[132:135], a[56:59], a[128:131], v[132:135]\n"
"ds_read_b128 a[128:131], v2 offset:24576\n"
"v_mfma_f32_16x16x32_bf16 v[136:139], a[56:59], a[136:139], v[136:139]\n"
"v_mfma_f32_16x16x32_bf16 v[136:139], a[60:63], a[140:143], v[136:139]\n"
"v_mfma_f32_16x16x32_bf16 v[108:111], a[52:55], a[140:143], v[108:111]\n"
"ds_read_b128 a[140:143], v2 offset:27648\n"
"v_mfma_f32_16x16x32_bf16 v[108:111], a[48:51], a[136:139], v[108:111]\n"
"ds_read_b128 a[136:139], v2 offset:26624\n"
"v_mfma_f32_16x16x32_bf16 v[112:115], a[48:51], a[144:147], v[112:115]\n"
"v_mfma_f32_16x16x32_bf16 v[112:115], a[52:55], a[148:151], v[112:115]\n"
"global_load_dwordx4 a[44:47], v0, s[34:35] offset:3072 sc1\n"
"v_mfma_f32_16x16x32_bf16 v[140:143], a[60:63], a[148:151], v[140:143]\n"
"ds_read_b128 a[148:151], v2 offset:29696\n"
"v_mfma_f32_16x16x32_bf16 v[140:143], a[56:59], a[144:147], v[140:143]\n"
"ds_read_b128 a[144:147], v2 offset:28672\n"
"v_mfma_f32_16x16x32_bf16 a[12:15], a[56:59], a[152:155], a[12:15]\n"
"v_mfma_f32_16x16x32_bf16 a[12:15], a[60:63], a[156:159], a[12:15]\n"
"v_mfma_f32_16x16x32_bf16 a[8:11], a[52:55], a[156:159], a[8:11]\n"
"ds_read_b128 a[156:159], v2 offset:31744\n"
"v_mfma_f32_16x16x32_bf16 a[8:11], a[48:51], a[152:155], a[8:11]\n"
"s_waitcnt vmcnt(8) lgkmcnt(11)\n"
"ds_read_b128 a[152:155], v2 offset:30720\n"
"v_mfma_f32_16x16x32_bf16 v[32:35], a[64:67], a[96:99], v[32:35]\n"
"s_add_u32 s34, s36, 1519616\n"
"v_mfma_f32_16x16x32_bf16 v[32:35], a[68:71], a[100:103], v[32:35]\n"
"global_load_dwordx4 a[48:51], v0, s[34:35] offset:0 sc1\n"
"v_mfma_f32_16x16x32_bf16 v[60:63], a[76:79], a[100:103], v[60:63]\n"
"s_barrier\n"
"v_mfma_f32_16x16x32_bf16 v[60:63], a[72:75], a[96:99], v[60:63]\n"
"v_mfma_f32_16x16x32_bf16 v[64:67], a[72:75], a[104:107], v[64:67]\n"
"v_mfma_f32_16x16x32_bf16 v[64:67], a[76:79], a[108:111], v[64:67]\n"
"v_mfma_f32_16x16x32_bf16 v[36:39], a[68:71], a[108:111], v[36:39]\n"
"v_mfma_f32_16x16x32_bf16 v[36:39], a[64:67], a[104:107], v[36:39]\n"
"s_waitcnt lgkmcnt(8)\n"
"v_mfma_f32_16x16x32_bf16 v[40:43], a[64:67], a[112:115], v[40:43]\n"
"v_mfma_f32_16x16x32_bf16 v[40:43], a[68:71], a[116:119], v[40:43]\n"
"global_load_dwordx4 a[52:55], v0, s[34:35] offset:1024 sc1\n"
"v_mfma_f32_16x16x32_bf16 v[68:71], a[76:79], a[116:119], v[68:71]\n"
"v_mfma_f32_16x16x32_bf16 v[68:71], a[72:75], a[112:115], v[68:71]\n"
"v_mfma_f32_16x16x32_bf16 v[72:75], a[72:75], a[120:123], v[72:75]\n"
"v_mfma_f32_16x16x32_bf16 v[72:75], a[76:79], a[124:127], v[72:75]\n"
"v_mfma_f32_16x16x32_bf16 v[44:47], a[68:71], a[124:127], v[44:47]\n"
"v_mfma_f32_16x16x32_bf16 v[44:47], a[64:67], a[120:123], v[44:47]\n"
"s_waitcnt lgkmcnt(4)\n"
"v_mfma_f32_16x16x32_bf16 v[48:51], a[64:67], a[128:131], v[48:51]\n"
"v_mfma_f32_16x16x32_bf16 v[48:51], a[68:71], a[132:135], v[48:51]\n"
"global_load_dwordx4 a[56:59], v0, s[34:35] offset:2048 sc1\n"
"v_mfma_f32_16x16x32_bf16 v[76:79], a[76:79], a[132:135], v[76:79]\n"
"v_mfma_f32_16x16x32_bf16 v[76:79], a[72:75], a[128:131], v[76:79]\n"
"v_mfma_f32_16x16x32_bf16 v[80:83], a[72:75], a[136:139], v[80:83]\n"
"v_lshlrev_b32 v16, 16, v18\n"
"v_mfma_f32_16x16x32_bf16 v[80:83], a[76:79], a[140:143], v[80:83]\n"
"v_and_b32 v17, s63, v18\n"
"v_mfma_f32_16x16x32_bf16 v[52:55], a[68:71], a[140:143], v[52:55]\n"
"v_lshlrev_b32 v18, 16, v19\n"
"v_mfma_f32_16x16x32_bf16 v[52:55], a[64:67], a[136:139], v[52:55]\n"
"s_waitcnt lgkmcnt(0)\n"
"v_and_b32 v19, s63, v19\n"
"v_add_f32 v144, v144, v16\n"
"v_mfma_f32_16x16x32_bf16 v[56:59], a[64:67], a[144:147], v[56:59]\n"
"v_add_f32 v145, v145, v17\n"
"v_mfma_f32_16x16x32_bf16 v[56:59], a[68:71], a[148:151], v[56:59]\n"
"global_load_dwordx4 a[60:63], v0, s[34:35] offset:3072 sc1\n"
"v_add_f32 v146, v146, v18\n"
"v_mfma_f32_16x16x32_bf16 v[84:87], a[76:79], a[148:151], v[84:87]\n"
"v_add_f32 v147, v147, v19\n"
"v_mfma_f32_16x16x32_bf16 v[84:87], a[72:75], a[144:147], v[84:87]\n"
"v_add_f32 v148, v148, v16\n"
"v_mfma_f32_16x16x32_bf16 a[4:7], a[72:75], a[152:155], a[4:7]\n"
"v_add_f32 v149, v149, v17\n"
"v_mfma_f32_16x16x32_bf16 a[4:7], a[76:79], a[156:159], a[4:7]\n"
"v_add_f32 v150, v150, v18\n"
"v_mfma_f32_16x16x32_bf16 a[0:3], a[68:71], a[156:159], a[0:3]\n"
"v_add_f32 v151, v151, v19\n"
"v_mfma_f32_16x16x32_bf16 a[0:3], a[64:67], a[152:155], a[0:3]\n"
"s_waitcnt vmcnt(8)\n"
"v_add_f32 v152, v152, v16\n"
"v_add_f32 v153, v153, v17\n"
"v_mfma_f32_16x16x32_bf16 v[88:91], a[80:83], a[96:99], v[88:91]\n"
"s_add_u32 s34, s36, 1523712\n"
"v_add_f32 v154, v154, v18\n"
"v_mfma_f32_16x16x32_bf16 v[88:91], a[84:87], a[100:103], v[88:91]\n"
"global_load_dwordx4 a[64:67], v0, s[34:35] offset:0 sc1\n"
"v_add_f32 v155, v155, v19\n"
"v_mfma_f32_16x16x32_bf16 v[116:119], a[92:95], a[100:103], v[116:119]\n"
"ds_read_b128 a[100:103], v3 offset:17408\n"
"v_add_f32 v156, v156, v16\n"
"v_mfma_f32_16x16x32_bf16 v[116:119], a[88:91], a[96:99], v[116:119]\n"
"ds_read_b128 a[96:99], v3 offset:16384\n"
"v_add_f32 v157, v157, v17\n"
"v_mfma_f32_16x16x32_bf16 v[120:123], a[88:91], a[104:107], v[120:123]\n"
"v_add_f32 v158, v158, v18\n"
"v_mfma_f32_16x16x32_bf16 v[120:123], a[92:95], a[108:111], v[120:123]\n"
"v_add_f32 v159, v159, v19\n"
"v_add_f32 v160, v160, v16\n"
"v_mfma_f32_16x16x32_bf16 v[92:95], a[84:87], a[108:111], v[92:95]\n"
"ds_read_b128 a[108:111], v3 offset:19456\n"
"v_add_f32 v161, v161, v17\n"
"v_mfma_f32_16x16x32_bf16 v[92:95], a[80:83], a[104:107], v[92:95]\n"
"ds_read_b128 a[104:107], v3 offset:18432\n"
"v_add_f32 v162, v162, v18\n"
"v_mfma_f32_16x16x32_bf16 v[96:99], a[80:83], a[112:115], v[96:99]\n"
"v_add_f32 v163, v163, v19\n"
"v_mfma_f32_16x16x32_bf16 v[96:99], a[84:87], a[116:119], v[96:99]\n"
"global_load_dwordx4 a[68:71], v0, s[34:35] offset:1024 sc1\n"
"v_add_f32 v164, v164, v16\n"
"v_mfma_f32_16x16x32_bf16 v[124:127], a[92:95], a[116:119], v[124:127]\n"
"ds_read_b128 a[116:119], v3 offset:21504\n"
"v_add_f32 v165, v165, v17\n"
"v_mfma_f32_16x16x32_bf16 v[124:127], a[88:91], a[112:115], v[124:127]\n"
"ds_read_b128 a[112:115], v3 offset:20480\n"
"v_add_f32 v166, v166, v18\n"
"v_mfma_f32_16x16x32_bf16 v[128:131], a[88:91], a[120:123], v[128:131]\n"
"v_add_f32 v167, v167, v19\n"
"v_mfma_f32_16x16x32_bf16 v[128:131], a[92:95], a[124:127], v[128:131]\n"
"v_add_f32 v168, v168, v16\n"
"v_add_f32 v169, v169, v17\n"
"v_mfma_f32_16x16x32_bf16 v[100:103], a[84:87], a[124:127], v[100:103]\n"
"ds_read_b128 a[124:127], v3 offset:23552\n"
"v_add_f32 v170, v170, v18\n"
"v_mfma_f32_16x16x32_bf16 v[100:103], a[80:83], a[120:123], v[100:103]\n"
"ds_read_b128 a[120:123], v3 offset:22528\n"
"v_add_f32 v171, v171, v19\n"
"v_mfma_f32_16x16x32_bf16 v[104:107], a[80:83], a[128:131], v[104:107]\n"
"v_accvgpr_read_b32 v12, a16\n"
"v_mfma_f32_16x16x32_bf16 v[104:107], a[84:87], a[132:135], v[104:107]\n"
"global_load_dwordx4 a[72:75], v0, s[34:35] offset:2048 sc1\n"
"v_accvgpr_read_b32 v13, a17\n"
"v_mfma_f32_16x16x32_bf16 v[132:135], a[92:95], a[132:135], v[132:135]\n"
"ds_read_b128 a[132:135], v3 offset:25600\n"
"v_accvgpr_read_b32 v14, a18\n"
"v_mfma_f32_16x16x32_bf16 v[132:135], a[88:91], a[128:131], v[132:135]\n"
"ds_read_b128 a[128:131], v3 offset:24576\n"
"v_accvgpr_read_b32 v15, a19\n"
"v_mfma_f32_16x16x32_bf16 v[136:139], a[88:91], a[136:139], v[136:139]\n"
"v_add_f32 v12, v12, v16\n"
"v_mfma_f32_16x16x32_bf16 v[136:139], a[92:95], a[140:143], v[136:139]\n"
"v_add_f32 v13, v13, v17\n"
"v_add_f32 v14, v14, v18\n"
"v_mfma_f32_16x16x32_bf16 v[108:111], a[84:87], a[140:143], v[108:111]\n"
"ds_read_b128 a[140:143], v3 offset:27648\n"
"v_add_f32 v15, v15, v19\n"
"v_mfma_f32_16x16x32_bf16 v[108:111], a[80:83], a[136:139], v[108:111]\n"
"ds_read_b128 a[136:139], v3 offset:26624\n"
"v_accvgpr_write_b32 a16, v12\n"
"v_mfma_f32_16x16x32_bf16 v[112:115], a[80:83], a[144:147], v[112:115]\n"
"v_accvgpr_write_b32 a17, v13\n"
"v_mfma_f32_16x16x32_bf16 v[112:115], a[84:87], a[148:151], v[112:115]\n"
"global_load_dwordx4 a[76:79], v0, s[34:35] offset:3072 sc1\n"
"v_accvgpr_write_b32 a18, v14\n"
"v_mfma_f32_16x16x32_bf16 v[140:143], a[92:95], a[148:151], v[140:143]\n"
"ds_read_b128 a[148:151], v3 offset:29696\n"
"v_accvgpr_write_b32 a19, v15\n"
"v_mfma_f32_16x16x32_bf16 v[140:143], a[88:91], a[144:147], v[140:143]\n"
"ds_read_b128 a[144:147], v3 offset:28672\n"
"v_lshlrev_b32 v20, 16, v22\n"
"v_mfma_f32_16x16x32_bf16 a[12:15], a[88:91], a[152:155], a[12:15]\n"
"v_and_b32 v21, s63, v22\n"
"v_lshlrev_b32 v22, 16, v23\n"
"v_mfma_f32_16x16x32_bf16 a[12:15], a[92:95], a[156:159], a[12:15]\n"
"v_and_b32 v23, s63, v23\n"
"v_mfma_f32_16x16x32_bf16 a[8:11], a[84:87], a[156:159], a[8:11]\n"
"ds_read_b128 a[156:159], v3 offset:31744\n"
"v_add_f32 v172, v172, v20\n"
"v_mfma_f32_16x16x32_bf16 a[8:11], a[80:83], a[152:155], a[8:11]\n"
"s_waitcnt vmcnt(8) lgkmcnt(11)\n"
"ds_read_b128 a[152:155], v3 offset:30720\n"
"v_add_f32 v173, v173, v21\n"
"v_mfma_f32_16x16x32_bf16 v[32:35], a[32:35], a[96:99], v[32:35]\n"
"s_add_u32 s34, s36, 1527808\n"
"v_add_f32 v174, v174, v22\n"
"v_mfma_f32_16x16x32_bf16 v[32:35], a[36:39], a[100:103], v[32:35]\n"
"global_load_dwordx4 a[80:83], v0, s[34:35] offset:0 sc1\n"
"v_add_f32 v175, v175, v23\n"
"v_mfma_f32_16x16x32_bf16 v[60:63], a[44:47], a[100:103], v[60:63]\n"
"v_add_f32 v176, v176, v20\n"
"v_mfma_f32_16x16x32_bf16 v[60:63], a[40:43], a[96:99], v[60:63]\n"
"v_add_f32 v177, v177, v21\n"
"v_add_f32 v178, v178, v22\n"
"v_mfma_f32_16x16x32_bf16 v[64:67], a[40:43], a[104:107], v[64:67]\n"
"v_add_f32 v179, v179, v23\n"
"v_mfma_f32_16x16x32_bf16 v[64:67], a[44:47], a[108:111], v[64:67]\n"
"v_add_f32 v180, v180, v20\n"
"v_mfma_f32_16x16x32_bf16 v[36:39], a[36:39], a[108:111], v[36:39]\n"
"v_add_f32 v181, v181, v21\n"
"v_mfma_f32_16x16x32_bf16 v[36:39], a[32:35], a[104:107], v[36:39]\n"
"s_waitcnt lgkmcnt(8)\n"
"v_add_f32 v182, v182, v22\n"
"v_mfma_f32_16x16x32_bf16 v[40:43], a[32:35], a[112:115], v[40:43]\n"
"v_add_f32 v183, v183, v23\n"
"v_mfma_f32_16x16x32_bf16 v[40:43], a[36:39], a[116:119], v[40:43]\n"
"global_load_dwordx4 a[84:87], v0, s[34:35] offset:1024 sc1\n"
"v_add_f32 v184, v184, v20\n"
"v_mfma_f32_16x16x32_bf16 v[68:71], a[44:47], a[116:119], v[68:71]\n"
"v_add_f32 v185, v185, v21\n"
"v_mfma_f32_16x16x32_bf16 v[68:71], a[40:43], a[112:115], v[68:71]\n"
"v_add_f32 v186, v186, v22\n"
"v_add_f32 v187, v187, v23\n"
"v_mfma_f32_16x16x32_bf16 v[72:75], a[40:43], a[120:123], v[72:75]\n"
"v_add_f32 v188, v188, v20\n"
"v_mfma_f32_16x16x32_bf16 v[72:75], a[44:47], a[124:127], v[72:75]\n"
"v_add_f32 v189, v189, v21\n"
"v_mfma_f32_16x16x32_bf16 v[44:47], a[36:39], a[124:127], v[44:47]\n"
"v_add_f32 v190, v190, v22\n"
"v_mfma_f32_16x16x32_bf16 v[44:47], a[32:35], a[120:123], v[44:47]\n"
"s_waitcnt lgkmcnt(4)\n"
"v_add_f32 v191, v191, v23\n"
"v_mfma_f32_16x16x32_bf16 v[48:51], a[32:35], a[128:131], v[48:51]\n"
"v_add_f32 v192, v192, v20\n"
"v_mfma_f32_16x16x32_bf16 v[48:51], a[36:39], a[132:135], v[48:51]\n"
"global_load_dwordx4 a[88:91], v0, s[34:35] offset:2048 sc1\n"
"v_add_f32 v193, v193, v21\n"
"v_mfma_f32_16x16x32_bf16 v[76:79], a[44:47], a[132:135], v[76:79]\n"
"v_add_f32 v194, v194, v22\n"
"v_add_f32 v195, v195, v23\n"
"v_mfma_f32_16x16x32_bf16 v[76:79], a[40:43], a[128:131], v[76:79]\n"
"v_add_f32 v196, v196, v20\n"
"v_mfma_f32_16x16x32_bf16 v[80:83], a[40:43], a[136:139], v[80:83]\n"
"v_add_f32 v197, v197, v21\n"
"v_mfma_f32_16x16x32_bf16 v[80:83], a[44:47], a[140:143], v[80:83]\n"
"v_add_f32 v198, v198, v22\n"
"v_mfma_f32_16x16x32_bf16 v[52:55], a[36:39], a[140:143], v[52:55]\n"
"v_add_f32 v199, v199, v23\n"
"v_mfma_f32_16x16x32_bf16 v[52:55], a[32:35], a[136:139], v[52:55]\n"
"s_waitcnt lgkmcnt(0)\n"
"v_accvgpr_read_b32 v12, a20\n"
"v_mfma_f32_16x16x32_bf16 v[56:59], a[32:35], a[144:147], v[56:59]\n"
"v_accvgpr_read_b32 v13, a21\n"
"v_mfma_f32_16x16x32_bf16 v[56:59], a[36:39], a[148:151], v[56:59]\n"
"L_st9133:\n"
"s_cbranch_execz L_retC\n"
"global_load_dwordx4 a[92:95], v0, s[34:35] offset:3072 sc1\n"
"v_accvgpr_read_b32 v14, a22\n"
"v_mfma_f32_16x16x32_bf16 v[84:87], a[44:47], a[148:151], v[84:87]\n"
"v_accvgpr_read_b32 v15, a23\n"
"v_add_f32 v12, v12, v20\n"
"v_mfma_f32_16x16x32_bf16 v[84:87], a[40:43], a[144:147], v[84:87]\n"
"v_add_f32 v13, v13, v21\n"
"v_mfma_f32_16x16x32_bf16 a[4:7], a[40:43], a[152:155], a[4:7]\n"
"v_add_f32 v14, v14, v22\n"
"v_mfma_f32_16x16x32_bf16 a[4:7], a[44:47], a[156:159], a[4:7]\n"
"v_add_f32 v15, v15, v23\n"
"v_mfma_f32_16x16x32_bf16 a[0:3], a[36:39], a[156:159], a[0:3]\n"
"v_accvgpr_write_b32 a20, v12\n"
"v_mfma_f32_16x16x32_bf16 a[0:3], a[32:35], a[152:155], a[0:3]\n"
"s_waitcnt vmcnt(8)\n"
"v_accvgpr_write_b32 a21, v13\n"
"v_mfma_f32_16x16x32_bf16 v[88:91], a[48:51], a[96:99], v[88:91]\n"
"s_add_u32 s34, s36, 1531904\n"
"v_accvgpr_write_b32 a22, v14\n"
"v_accvgpr_write_b32 a23, v15\n"
"v_mfma_f32_16x16x32_bf16 v[88:91], a[52:55], a[100:103], v[88:91]\n"
"global_load_dwordx4 a[32:35], v0, s[34:35] offset:0 sc1\n"
"v_lshlrev_b32 v24, 16, v26\n"
"v_mfma_f32_16x16x32_bf16 v[116:119], a[60:63], a[100:103], v[116:119]\n"
"ds_read_b128 a[100:103], v4 offset:17408\n"
"v_and_b32 v25, s63, v26\n"
"v_mfma_f32_16x16x32_bf16 v[116:119], a[56:59], a[96:99], v[116:119]\n"
"ds_read_b128 a[96:99], v4 offset:16384\n"
"v_lshlrev_b32 v26, 16, v27\n"
"v_mfma_f32_16x16x32_bf16 v[120:123], a[56:59], a[104:107], v[120:123]\n"
"v_and_b32 v27, s63, v27\n"
"v_mfma_f32_16x16x32_bf16 v[120:123], a[60:63], a[108:111], v[120:123]\n"
"v_add_f32 v200, v200, v24\n"
"v_mfma_f32_16x16x32_bf16 v[92:95], a[52:55], a[108:111], v[92:95]\n"
"ds_read_b128 a[108:111], v4 offset:19456\n"
"v_add_f32 v201, v201, v25\n"
"v_mfma_f32_16x16x32_bf16 v[92:95], a[48:51], a[104:107], v[92:95]\n"
"ds_read_b128 a[104:107], v4 offset:18432\n"
"v_add_f32 v202, v202, v26\n"
"v_mfma_f32_16x16x32_bf16 v[96:99], a[48:51], a[112:115], v[96:99]\n"
"v_add_f32 v203, v203, v27\n"
"v_add_f32 v204, v204, v24\n"
"v_mfma_f32_16x16x32_bf16 v[96:99], a[52:55], a[116:119], v[96:99]\n"
"global_load_dwordx4 a[36:39], v0, s[34:35] offset:1024 sc1\n"
"v_add_f32 v205, v205, v25\n"
"v_mfma_f32_16x16x32_bf16 v[124:127], a[60:63], a[116:119], v[124:127]\n"
"ds_read_b128 a[116:119], v4 offset:21504\n"
"v_add_f32 v206, v206, v26\n"
"v_mfma_f32_16x16x32_bf16 v[124:127], a[56:59], a[112:115], v[124:127]\n"
"ds_read_b128 a[112:115], v4 offset:20480\n"
"v_add_f32 v207, v207, v27\n"
"v_mfma_f32_16x16x32_bf16 v[128:131], a[56:59], a[120:123], v[128:131]\n"
"v_add_f32 v208, v208, v24\n"
"v_mfma_f32_16x16x32_bf16 v[128:131], a[60:63], a[124:127], v[128:131]\n"
"v_add_f32 v209, v209, v25\n"
"v_mfma_f32_16x16x32_bf16 v[100:103], a[52:55], a[124:127], v[100:103]\n"
"ds_read_b128 a[124:127], v4 offset:23552\n"
"v_add_f32 v210, v210, v26\n"
"v_mfma_f32_16x16x32_bf16 v[100:103], a[48:51], a[120:123], v[100:103]\n"
"ds_read_b128 a[120:123], v4 offset:22528\n"
"v_add_f32 v211, v211, v27\n"
"v_mfma_f32_16x16x32_bf16 v[104:107], a[48:51], a[128:131], v[104:107]\n"
"v_add_f32 v212, v212, v24\n"
"v_add_f32 v213, v213, v25\n"
"v_mfma_f32_16x16x32_bf16 v[104:107], a[52:55], a[132:135], v[104:107]\n"
"global_load_dwordx4 a[40:43], v0, s[34:35] offset:2048 sc1\n"
"v_add_f32 v214, v214, v26\n"
"v_mfma_f32_16x16x32_bf16 v[132:135], a[60:63], a[132:135], v[132:135]\n"
"ds_read_b128 a[132:135], v4 offset:25600\n"
"v_add_f32 v215, v215, v27\n"
"v_mfma_f32_16x16x32_bf16 v[132:135], a[56:59], a[128:131], v[132:135]\n"
"ds_read_b128 a[128:131], v4 offset:24576\n"
"v_add_f32 v216, v216, v24\n"
"v_mfma_f32_16x16x32_bf16 v[136:139], a[56:59], a[136:139], v[136:139]\n"
"v_add_f32 v217, v217, v25\n"
"v_mfma_f32_16x16x32_bf16 v[136:139], a[60:63], a[140:143], v[136:139]\n"
"v_add_f32 v218, v218, v26\n"
"v_mfma_f32_16x16x32_bf16 v[108:111], a[52:55], a[140:143], v[108:111]\n"
"ds_read_b128 a[140:143], v4 offset:27648\n"
"v_add_f32 v219, v219, v27\n"
"v_mfma_f32_16x16x32_bf16 v[108:111], a[48:51], a[136:139], v[108:111]\n"
"ds_read_b128 a[136:139], v4 offset:26624\n"
"v_add_f32 v220, v220, v24\n"
"v_mfma_f32_16x16x32_bf16 v[112:115], a[48:51], a[144:147], v[112:115]\n"
"v_add_f32 v221, v221, v25\n"
"v_add_f32 v222, v222, v26\n"
"v_mfma_f32_16x16x32_bf16 v[112:115], a[52:55], a[148:151], v[112:115]\n"
"global_load_dwordx4 a[44:47], v0, s[34:35] offset:3072 sc1\n"
"v_add_f32 v223, v223, v27\n"
"v_mfma_f32_16x16x32_bf16 v[140:143], a[60:63], a[148:151], v[140:143]\n"
"ds_read_b128 a[148:151], v4 offset:29696\n"
"v_add_f32 v224, v224, v24\n"
"v_mfma_f32_16x16x32_bf16 v[140:143], a[56:59], a[144:147], v[140:143]\n"
"ds_read_b128 a[144:147], v4 offset:28672\n"
"v_add_f32 v225, v225, v25\n"
"v_mfma_f32_16x16x32_bf16 a[12:15], a[56:59], a[152:155], a[12:15]\n"
"v_add_f32 v226, v226, v26\n"
"v_mfma_f32_16x16x32_bf16 a[12:15], a[60:63], a[156:159], a[12:15]\n"
"v_add_f32 v227, v227, v27\n"
"v_mfma_f32_16x16x32_bf16 a[8:11], a[52:55], a[156:159], a[8:11]\n"
"ds_read_b128 a[156:159], v4 offset:31744\n"
"v_accvgpr_read_b32 v12, a24\n"
"v_mfma_f32_16x16x32_bf16 a[8:11], a[48:51], a[152:155], a[8:11]\n"
"s_waitcnt vmcnt(8) lgkmcnt(11)\n"
"ds_read_b128 a[152:155], v4 offset:30720\n"
"v_accvgpr_read_b32 v13, a25\n"
"v_mfma_f32_16x16x32_bf16 v[32:35], a[64:67], a[96:99], v[32:35]\n"
"s_add_u32 s34, s36, 1536000\n"
"v_accvgpr_read_b32 v14, a26\n"
"v_accvgpr_read_b32 v15, a27\n"
"v_mfma_f32_16x16x32_bf16 v[32:35], a[68:71], a[100:103], v[32:35]\n"
"global_load_dwordx4 a[48:51], v0, s[34:35] offset:0 sc1\n"
"v_add_f32 v12, v12, v24\n"
"v_mfma_f32_16x16x32_bf16 v[60:63], a[76:79], a[100:103], v[60:63]\n"
"v_add_f32 v13, v13, v25\n"
"v_mfma_f32_16x16x32_bf16 v[60:63], a[72:75], a[96:99], v[60:63]\n"
"v_add_f32 v14, v14, v26\n"
"v_mfma_f32_16x16x32_bf16 v[64:67], a[72:75], a[104:107], v[64:67]\n"
"v_add_f32 v15, v15, v27\n"
"v_mfma_f32_16x16x32_bf16 v[64:67], a[76:79], a[108:111], v[64:67]\n"
"v_accvgpr_write_b32 a24, v12\n"
"v_mfma_f32_16x16x32_bf16 v[36:39], a[68:71], a[108:111], v[36:39]\n"
"v_accvgpr_write_b32 a25, v13\n"
"v_accvgpr_write_b32 a26, v14\n"
"v_mfma_f32_16x16x32_bf16 v[36:39], a[64:67], a[104:107], v[36:39]\n"
"s_waitcnt lgkmcnt(8)\n"
"v_accvgpr_write_b32 a27, v15\n"
"v_mfma_f32_16x16x32_bf16 v[40:43], a[64:67], a[112:115], v[40:43]\n"
"v_lshlrev_b32 v28, 16, v30\n"
"v_mfma_f32_16x16x32_bf16 v[40:43], a[68:71], a[116:119], v[40:43]\n"
"global_load_dwordx4 a[52:55], v0, s[34:35] offset:1024 sc1\n"
"v_and_b32 v29, s63, v30\n"
"v_mfma_f32_16x16x32_bf16 v[68:71], a[76:79], a[116:119], v[68:71]\n"
"v_lshlrev_b32 v30, 16, v31\n"
"v_mfma_f32_16x16x32_bf16 v[68:71], a[72:75], a[112:115], v[68:71]\n"
"v_and_b32 v31, s63, v31\n"
"v_mfma_f32_16x16x32_bf16 v[72:75], a[72:75], a[120:123], v[72:75]\n"
"v_add_f32 v228, v228, v28\n"
"v_mfma_f32_16x16x32_bf16 v[72:75], a[76:79], a[124:127], v[72:75]\n"
"v_add_f32 v229, v229, v29\n"
"v_add_f32 v230, v230, v30\n"
"v_mfma_f32_16x16x32_bf16 v[44:47], a[68:71], a[124:127], v[44:47]\n"
"v_add_f32 v231, v231, v31\n"
"v_mfma_f32_16x16x32_bf16 v[44:47], a[64:67], a[120:123], v[44:47]\n"
"s_waitcnt lgkmcnt(4)\n"
"v_add_f32 v232, v232, v28\n"
"v_mfma_f32_16x16x32_bf16 v[48:51], a[64:67], a[128:131], v[48:51]\n"
"v_add_f32 v233, v233, v29\n"
"v_mfma_f32_16x16x32_bf16 v[48:51], a[68:71], a[132:135], v[48:51]\n"
"global_load_dwordx4 a[56:59], v0, s[34:35] offset:2048 sc1\n"
"v_add_f32 v234, v234, v30\n"
"v_mfma_f32_16x16x32_bf16 v[76:79], a[76:79], a[132:135], v[76:79]\n"
"v_add_f32 v235, v235, v31\n"
"v_mfma_f32_16x16x32_bf16 v[76:79], a[72:75], a[128:131], v[76:79]\n"
"v_add_f32 v236, v236, v28\n"
"v_mfma_f32_16x16x32_bf16 v[80:83], a[72:75], a[136:139], v[80:83]\n"
"v_add_f32 v237, v237, v29\n"
"v_mfma_f32_16x16x32_bf16 v[80:83], a[76:79], a[140:143], v[80:83]\n"
"v_add_f32 v238, v238, v30\n"
"v_add_f32 v239, v239, v31\n"
"v_mfma_f32_16x16x32_bf16 v[52:55], a[68:71], a[140:143], v[52:55]\n"
"v_add_f32 v240, v240, v28\n"
"v_mfma_f32_16x16x32_bf16 v[52:55], a[64:67], a[136:139], v[52:55]\n"
"s_waitcnt lgkmcnt(0)\n"
"v_add_f32 v241, v241, v29\n"
"v_mfma_f32_16x16x32_bf16 v[56:59], a[64:67], a[144:147], v[56:59]\n"
"v_add_f32 v242, v242, v30\n"
"v_mfma_f32_16x16x32_bf16 v[56:59], a[68:71], a[148:151], v[56:59]\n"
"global_load_dwordx4 a[60:63], v0, s[34:35] offset:3072 sc1\n"
"v_add_f32 v243, v243, v31\n"
"v_mfma_f32_16x16x32_bf16 v[84:87], a[76:79], a[148:151], v[84:87]\n"
"v_add_f32 v244, v244, v28\n"
"v_mfma_f32_16x16x32_bf16 v[84:87], a[72:75], a[144:147], v[84:87]\n"
"v_add_f32 v245, v245, v29\n"
"v_mfma_f32_16x16x32_bf16 a[4:7], a[72:75], a[152:155], a[4:7]\n"
"v_add_f32 v246, v246, v30\n"
"v_add_f32 v247, v247, v31\n"
"v_mfma_f32_16x16x32_bf16 a[4:7], a[76:79], a[156:159], a[4:7]\n"
"v_add_f32 v248, v248, v28\n"
"v_mfma_f32_16x16x32_bf16 a[0:3], a[68:71], a[156:159], a[0:3]\n"
"v_add_f32 v249, v249, v29\n"
"v_mfma_f32_16x16x32_bf16 a[0:3], a[64:67], a[152:155], a[0:3]\n"
"s_waitcnt vmcnt(8)\n"
"v_add_f32 v250, v250, v30\n"
"v_mfma_f32_16x16x32_bf16 v[88:91], a[80:83], a[96:99], v[88:91]\n"
"s_add_u32 s34, s36, 1466368\n"
"v_add_f32 v251, v251, v31\n"
"v_mfma_f32_16x16x32_bf16 v[88:91], a[84:87], a[100:103], v[88:91]\n"
"global_load_dwordx4 a[64:67], v0, s[34:35] offset:0 sc1\n"
"v_add_f32 v252, v252, v28\n"
"v_mfma_f32_16x16x32_bf16 v[116:119], a[92:95], a[100:103], v[116:119]\n"
"ds_read_b128 a[100:103], v5 offset:17408\n"
"v_add_f32 v253, v253, v29\n"
"v_mfma_f32_16x16x32_bf16 v[116:119], a[88:91], a[96:99], v[116:119]\n"
"ds_read_b128 a[96:99], v5 offset:16384\n"
"v_add_f32 v254, v254, v30\n"
"v_mfma_f32_16x16x32_bf16 v[120:123], a[88:91], a[104:107], v[120:123]\n"
"v_add_f32 v255, v255, v31\n"
"v_accvgpr_read_b32 v12, a28\n"
"v_mfma_f32_16x16x32_bf16 v[120:123], a[92:95], a[108:111], v[120:123]\n"
"v_accvgpr_read_b32 v13, a29\n"
"v_mfma_f32_16x16x32_bf16 v[92:95], a[84:87], a[108:111], v[92:95]\n"
"ds_read_b128 a[108:111], v5 offset:19456\n"
"v_accvgpr_read_b32 v14, a30\n"
"v_mfma_f32_16x16x32_bf16 v[92:95], a[80:83], a[104:107], v[92:95]\n"
"ds_read_b128 a[104:107], v5 offset:18432\n"
"v_accvgpr_read_b32 v15, a31\n"
"v_mfma_f32_16x16x32_bf16 v[96:99], a[80:83], a[112:115], v[96:99]\n"
"v_add_f32 v12, v12, v28\n"
"v_mfma_f32_16x16x32_bf16 v[96:99], a[84:87], a[116:119], v[96:99]\n"
"global_load_dwordx4 a[68:71], v0, s[34:35] offset:1024 sc1\n"
"v_add_f32 v13, v13, v29\n"
"v_mfma_f32_16x16x32_bf16 v[124:127], a[92:95], a[116:119], v[124:127]\n"
"ds_read_b128 a[116:119], v5 offset:21504\n"
"v_add_f32 v14, v14, v30\n"
"v_mfma_f32_16x16x32_bf16 v[124:127], a[88:91], a[112:115], v[124:127]\n"
"ds_read_b128 a[112:115], v5 offset:20480\n"
"v_add_f32 v15, v15, v31\n"
"v_mfma_f32_16x16x32_bf16 v[128:131], a[88:91], a[120:123], v[128:131]\n"
"v_accvgpr_write_b32 a28, v12\n"
"v_accvgpr_write_b32 a29, v13\n"
"v_mfma_f32_16x16x32_bf16 v[128:131], a[92:95], a[124:127], v[128:131]\n"
"v_accvgpr_write_b32 a30, v14\n"
"v_mfma_f32_16x16x32_bf16 v[100:103], a[84:87], a[124:127], v[100:103]\n"
"ds_read_b128 a[124:127], v5 offset:23552\n"
"v_accvgpr_write_b32 a31, v15\n"
"v_mfma_f32_16x16x32_bf16 v[100:103], a[80:83], a[120:123], v[100:103]\n"
"ds_read_b128 a[120:123], v5 offset:22528\n"
"v_mfma_f32_16x16x32_bf16 v[104:107], a[80:83], a[128:131], v[104:107]\n"
"v_mfma_f32_16x16x32_bf16 v[104:107], a[84:87], a[132:135], v[104:107]\n"
"global_load_dwordx4 a[72:75], v0, s[34:35] offset:2048 sc1\n"
"v_mfma_f32_16x16x32_bf16 v[132:135], a[92:95], a[132:135], v[132:135]\n"
"ds_read_b128 a[132:135], v5 offset:25600\n"
"v_mfma_f32_16x16x32_bf16 v[132:135], a[88:91], a[128:131], v[132:135]\n"
"ds_read_b128 a[128:131], v5 offset:24576\n"
"v_mfma_f32_16x16x32_bf16 v[136:139], a[88:91], a[136:139], v[136:139]\n"
"ds_read_b128 v[16:19], v7 offset:32768\n"
"v_mfma_f32_16x16x32_bf16 v[136:139], a[92:95], a[140:143], v[136:139]\n"
"v_mfma_f32_16x16x32_bf16 v[108:111], a[84:87], a[140:143], v[108:111]\n"
"ds_read_b128 a[140:143], v5 offset:27648\n"
"v_mfma_f32_16x16x32_bf16 v[108:111], a[80:83], a[136:139], v[108:111]\n"
"ds_read_b128 a[136:139], v5 offset:26624\n"
"v_mfma_f32_16x16x32_bf16 v[112:115], a[80:83], a[144:147], v[112:115]\n"
"v_mfma_f32_16x16x32_bf16 v[112:115], a[84:87], a[148:151], v[112:115]\n"
"global_load_dwordx4 a[76:79], v0, s[34:35] offset:3072 sc1\n"
"v_mfma_f32_16x16x32_bf16 v[140:143], a[92:95], a[148:151], v[140:143]\n"
"ds_read_b128 a[148:151], v5 offset:29696\n"
"v_mfma_f32_16x16x32_bf16 v[140:143], a[88:91], a[144:147], v[140:143]\n"
"ds_read_b128 a[144:147], v5 offset:28672\n"
"v_mfma_f32_16x16x32_bf16 a[12:15], a[88:91], a[152:155], a[12:15]\n"
"v_mfma_f32_16x16x32_bf16 a[12:15], a[92:95], a[156:159], a[12:15]\n"
"v_mfma_f32_16x16x32_bf16 a[8:11], a[84:87], a[156:159], a[8:11]\n"
"ds_read_b128 a[156:159], v5 offset:31744\n"
"v_mfma_f32_16x16x32_bf16 a[8:11], a[80:83], a[152:155], a[8:11]\n"
"s_waitcnt vmcnt(8) lgkmcnt(12)\n"
"ds_read_b128 a[152:155], v5 offset:30720\n"
"v_mfma_f32_16x16x32_bf16 v[32:35], a[32:35], a[96:99], v[32:35]\n"
"s_add_u32 s34, s36, 1470464\n"
"v_mfma_f32_16x16x32_bf16 v[32:35], a[36:39], a[100:103], v[32:35]\n"
"global_load_dwordx4 a[80:83], v0, s[34:35] offset:0 sc1\n"
"v_mfma_f32_16x16x32_bf16 v[60:63], a[44:47], a[100:103], v[60:63]\n"
"v_mfma_f32_16x16x32_bf16 v[60:63], a[40:43], a[96:99], v[60:63]\n"
"v_mfma_f32_16x16x32_bf16 v[64:67], a[40:43], a[104:107], v[64:67]\n"
"v_mfma_f32_16x16x32_bf16 v[64:67], a[44:47], a[108:111], v[64:67]\n"
"v_mfma_f32_16x16x32_bf16 v[36:39], a[36:39], a[108:111], v[36:39]\n"
"v_mfma_f32_16x16x32_bf16 v[36:39], a[32:35], a[104:107], v[36:39]\n"
"s_waitcnt lgkmcnt(9)\n"
"v_mfma_f32_16x16x32_bf16 v[40:43], a[32:35], a[112:115], v[40:43]\n"
"s_mov_b32 s52, %4\n"
"s_and_b32 s53, %5, 0xffff\n"
"v_mfma_f32_16x16x32_bf16 v[40:43], a[36:39], a[116:119], v[40:43]\n"
"global_load_dwordx4 a[84:87], v0, s[34:35] offset:1024 sc1\n"
"v_mfma_f32_16x16x32_bf16 v[68:71], a[44:47], a[116:119], v[68:71]\n"
"s_mov_b32 s54, 260352\n"
"s_mov_b32 s55, 0x20000\n"
"v_mfma_f32_16x16x32_bf16 v[68:71], a[40:43], a[112:115], v[68:71]\n"
"v_xor_b32 v8, 64, v7\n"
"v_mfma_f32_16x16x32_bf16 v[72:75], a[40:43], a[120:123], v[72:75]\n"
"s_waitcnt lgkmcnt(6)\n"
"v_lshlrev_b32 v12, 16, v16\n"
"v_mfma_f32_16x16x32_bf16 v[72:75], a[44:47], a[124:127], v[72:75]\n"
"v_and_b32 v13, s63, v16\n"
"v_lshlrev_b32 v14, 16, v17\n"
"v_mfma_f32_16x16x32_bf16 v[44:47], a[36:39], a[124:127], v[44:47]\n"
"v_and_b32 v15, s63, v17\n"
"v_mfma_f32_16x16x32_bf16 v[44:47], a[32:35], a[120:123], v[44:47]\n"
"s_waitcnt lgkmcnt(4)\n"
"v_xor_b32 v20, 64, v7\n"
"v_mfma_f32_16x16x32_bf16 v[48:51], a[32:35], a[128:131], v[48:51]\n"
"ds_read_b128 v[20:23], v20 offset:32768\n"
"v_mul_f32 v12, v12, v32\n"
"v_mfma_f32_16x16x32_bf16 v[48:51], a[36:39], a[132:135], v[48:51]\n"
"global_load_dwordx4 a[88:91], v0, s[34:35] offset:2048 sc1\n"
"v_mul_f32 v13, v13, v33\n"
"v_mfma_f32_16x16x32_bf16 v[76:79], a[44:47], a[132:135], v[76:79]\n"
"v_mul_f32 v14, v14, v34\n"
"v_mfma_f32_16x16x32_bf16 v[76:79], a[40:43], a[128:131], v[76:79]\n"
"v_mul_f32 v15, v15, v35\n"
"v_cvt_pk_bf16_f32 v24, v12, v13\n"
"v_mfma_f32_16x16x32_bf16 v[80:83], a[40:43], a[136:139], v[80:83]\n"
"v_cvt_pk_bf16_f32 v25, v14, v15\n"
"v_mfma_f32_16x16x32_bf16 v[80:83], a[44:47], a[140:143], v[80:83]\n"
"ds_write_b64 v7, v[24:25] offset:32768\n"
"v_mfma_f32_16x16x32_bf16 v[52:55], a[36:39], a[140:143], v[52:55]\n"
"v_lshlrev_b32 v12, 16, v18\n"
"v_mfma_f32_16x16x32_bf16 v[52:55], a[32:35], a[136:139], v[52:55]\n"
"s_waitcnt lgkmcnt(2)\n"
"v_and_b32 v13, s63, v18\n"
"v_lshlrev_b32 v14, 16, v19\n"
"v_mfma_f32_16x16x32_bf16 v[56:59], a[32:35], a[144:147], v[56:59]\n"
"v_and_b32 v15, s63, v19\n"
"v_mfma_f32_16x16x32_bf16 v[56:59], a[36:39], a[148:151], v[56:59]\n"
"global_load_dwordx4 a[92:95], v0, s[34:35] offset:3072 sc1\n"
"v_mul_f32 v12, v12, v60\n"
"v_mfma_f32_16x16x32_bf16 v[84:87], a[44:47], a[148:151], v[84:87]\n"
"v_mul_f32 v13, v13, v61\n"
"v_mul_f32 v14, v14, v62\n"
"v_mfma_f32_16x16x32_bf16 v[84:87], a[40:43], a[144:147], v[84:87]\n"
"v_mul_f32 v15, v15, v63\n"
"v_mfma_f32_16x16x32_bf16 a[4:7], a[40:43], a[152:155], a[4:7]\n"
"v_cvt_pk_bf16_f32 v26, v12, v13\n"
"v_mfma_f32_16x16x32_bf16 a[4:7], a[44:47], a[156:159], a[4:7]\n"
"v_cvt_pk_bf16_f32 v27, v14, v15\n"
"v_mfma_f32_16x16x32_bf16 a[0:3], a[36:39], a[156:159], a[0:3]\n"
"ds_write_b64 v7, v[26:27] offset:32776\n"
"v_mfma_f32_16x16x32_bf16 a[0:3], a[32:35], a[152:155], a[0:3]\n"
"s_waitcnt vmcnt(8) lgkmcnt(2)\n"
"v_lshlrev_b32 v12, 16, v20\n"
"v_mfma_f32_16x16x32_bf16 v[88:91], a[48:51], a[96:99], v[88:91]\n"
"s_add_u32 s34, s36, 1482752\n"
"v_and_b32 v13, s63, v20\n"
"v_mfma_f32_16x16x32_bf16 v[88:91], a[52:55], a[100:103], v[88:91]\n"
"global_load_dwordx4 a[32:35], v0, s[34:35] offset:0 sc1\n"
"v_mfma_f32_16x16x32_bf16 v[116:119], a[60:63], a[100:103], v[116:119]\n"
"ds_read_b128 a[100:103], v3 offset:1024\n"
"v_mfma_f32_16x16x32_bf16 v[116:119], a[56:59], a[96:99], v[116:119]\n"
"ds_read_b128 a[96:99], v3 offset:0\n"
"v_mfma_f32_16x16x32_bf16 v[120:123], a[56:59], a[104:107], v[120:123]\n"
"v_lshlrev_b32 v14, 16, v21\n"
"v_mfma_f32_16x16x32_bf16 v[120:123], a[60:63], a[108:111], v[120:123]\n"
"v_and_b32 v15, s63, v21\n"
"v_mfma_f32_16x16x32_bf16 v[92:95], a[52:55], a[108:111], v[92:95]\n"
"ds_read_b128 a[108:111], v3 offset:3072\n"
"v_mfma_f32_16x16x32_bf16 v[92:95], a[48:51], a[104:107], v[92:95]\n"
"ds_read_b128 a[104:107], v3 offset:2048\n"
"v_mfma_f32_16x16x32_bf16 v[96:99], a[48:51], a[112:115], v[96:99]\n"
"ds_read_b128 v[16:19], v7 offset:34816\n"
"v_mfma_f32_16x16x32_bf16 v[96:99], a[52:55], a[116:119], v[96:99]\n"
"global_load_dwordx4 a[36:39], v0, s[34:35] offset:1024 sc1\n"
"v_mul_f32 v12, v12, v88\n"
"v_mul_f32 v13, v13, v89\n"
"v_mfma_f32_16x16x32_bf16 v[124:127], a[60:63], a[116:119], v[124:127]\n"
"ds_read_b128 a[116:119], v3 offset:5120\n"
"v_mul_f32 v14, v14, v90\n"
"v_mul_f32 v15, v15, v91\n"
"v_mfma_f32_16x16x32_bf16 v[124:127], a[56:59], a[112:115], v[124:127]\n"
"ds_read_b128 a[112:115], v3 offset:4096\n"
"v_cvt_pk_bf16_f32 v28, v12, v13\n"
"v_cvt_pk_bf16_f32 v29, v14, v15\n"
"v_mfma_f32_16x16x32_bf16 v[128:131], a[56:59], a[120:123], v[128:131]\n"
"ds_write_b64 v8, v[28:29] offset:32768\n"
"v_mfma_f32_16x16x32_bf16 v[128:131], a[60:63], a[124:127], v[128:131]\n"
"v_lshlrev_b32 v12, 16, v22\n"
"v_and_b32 v13, s63, v22\n"
"v_lshlrev_b32 v14, 16, v23\n"
"v_mfma_f32_16x16x32_bf16 v[100:103], a[52:55], a[124:127], v[100:103]\n"
"ds_read_b128 a[124:127], v3 offset:7168\n"
"v_and_b32 v15, s63, v23\n"
"v_mul_f32 v12, v12, v116\n"
"v_mfma_f32_16x16x32_bf16 v[100:103], a[48:51], a[120:123], v[100:103]\n"
"ds_read_b128 a[120:123], v3 offset:6144\n"
"v_mul_f32 v13, v13, v117\n"
"v_mfma_f32_16x16x32_bf16 v[104:107], a[48:51], a[128:131], v[104:107]\n"
"v_mul_f32 v14, v14, v118\n"
"v_mul_f32 v15, v15, v119\n"
"v_mfma_f32_16x16x32_bf16 v[104:107], a[52:55], a[132:135], v[104:107]\n"
"global_load_dwordx4 a[40:43], v0, s[34:35] offset:2048 sc1\n"
"v_cvt_pk_bf16_f32 v30, v12, v13\n"
"v_mfma_f32_16x16x32_bf16 v[132:135], a[60:63], a[132:135], v[132:135]\n"
"ds_read_b128 a[132:135], v3 offset:9216\n"
"v_cvt_pk_bf16_f32 v31, v14, v15\n"
"v_mfma_f32_16x16x32_bf16 v[132:135], a[56:59], a[128:131], v[132:135]\n"
"ds_read_b128 a[128:131], v3 offset:8192\n"
"ds_write_b64 v8, v[30:31] offset:32776\n"
"v_mfma_f32_16x16x32_bf16 v[136:139], a[56:59], a[136:139], v[136:139]\n"
"ds_read_b128 v[32:35], v2 offset:32768\n"
"v_mfma_f32_16x16x32_bf16 v[136:139], a[60:63], a[140:143], v[136:139]\n"
"s_add_u32 s96, s36, 1552384\n"
"ds_read_b128 v[60:63], v2 offset:33792\n"
"v_mfma_f32_16x16x32_bf16 v[108:111], a[52:55], a[140:143], v[108:111]\n"
"s_waitcnt lgkmcnt(10)\n"
"ds_read_b128 a[140:143], v3 offset:11264\n"
"v_lshlrev_b32 v12, 16, v16\n"
"v_mfma_f32_16x16x32_bf16 v[108:111], a[48:51], a[136:139], v[108:111]\n"
"ds_read_b128 a[136:139], v3 offset:10240\n"
"v_and_b32 v13, s63, v16\n"
"v_mfma_f32_16x16x32_bf16 v[112:115], a[48:51], a[144:147], v[112:115]\n"
"global_load_dwordx4 v[88:91], v0, s[96:97] offset:-4096 sc1\n"
"v_lshlrev_b32 v14, 16, v17\n"
"v_mfma_f32_16x16x32_bf16 v[112:115], a[52:55], a[148:151], v[112:115]\n"
"global_load_dwordx4 a[44:47], v0, s[34:35] offset:3072 sc1\n"
"v_and_b32 v15, s63, v17\n"
"v_mfma_f32_16x16x32_bf16 v[140:143], a[60:63], a[148:151], v[140:143]\n"
"ds_read_b128 a[148:151], v3 offset:13312\n"
"v_xor_b32 v20, 64, v7\n"
"v_mfma_f32_16x16x32_bf16 v[140:143], a[56:59], a[144:147], v[140:143]\n"
"ds_read_b128 a[144:147], v3 offset:12288\n"
"v_mfma_f32_16x16x32_bf16 a[12:15], a[56:59], a[152:155], a[12:15]\n"
"ds_read_b128 v[20:23], v20 offset:34816\n"
"v_mul_f32 v12, v12, v36\n"
"v_mfma_f32_16x16x32_bf16 a[12:15], a[60:63], a[156:159], a[12:15]\n"
"s_waitcnt lgkmcnt(6)\n"
"buffer_store_dwordx4 v[32:35], v6, s[52:55], s80 offen offset:0 sc1\n"
"v_mul_f32 v13, v13, v37\n"
"v_mul_f32 v14, v14, v38\n"
"v_mul_f32 v15, v15, v39\n"
"v_mfma_f32_16x16x32_bf16 a[8:11], a[52:55], a[156:159], a[8:11]\n"
"ds_read_b128 a[156:159], v3 offset:15360\n"
"v_cvt_pk_bf16_f32 v24, v12, v13\n"
"v_mfma_f32_16x16x32_bf16 a[8:11], a[48:51], a[152:155], a[8:11]\n"
"s_waitcnt vmcnt(10)\n"
"ds_read_b128 a[152:155], v3 offset:14336\n"
"v_cvt_pk_bf16_f32 v25, v14, v15\n"
"v_mfma_f32_16x16x32_bf16 v[144:147], a[64:67], a[96:99], v[144:147]\n"
"s_add_u32 s34, s36, 1486848\n"
"ds_write_b64 v7, v[24:25] offset:34816\n"
"v_mfma_f32_16x16x32_bf16 v[144:147], a[68:71], a[100:103], v[144:147]\n"
"global_load_dwordx4 a[48:51], v0, s[34:35] offset:0 sc1\n"
"v_lshlrev_b32 v12, 16, v18\n"
"v_mfma_f32_16x16x32_bf16 v[172:175], a[76:79], a[100:103], v[172:175]\n"
"global_load_dwordx4 v[116:119], v0, s[96:97] offset:-3072 sc1\n"
"v_and_b32 v13, s63, v18\n"
"v_mfma_f32_16x16x32_bf16 v[172:175], a[72:75], a[96:99], v[172:175]\n"
"v_lshlrev_b32 v14, 16, v19\n"
"v_and_b32 v15, s63, v19\n"
"v_mfma_f32_16x16x32_bf16 v[176:179], a[72:75], a[104:107], v[176:179]\n"
"v_mul_f32 v12, v12, v64\n"
"v_mul_f32 v13, v13, v65\n"
"v_mfma_f32_16x16x32_bf16 v[176:179], a[76:79], a[108:111], v[176:179]\n"
"v_mul_f32 v14, v14, v66\n"
"v_mul_f32 v15, v15, v67\n"
"v_mfma_f32_16x16x32_bf16 v[148:151], a[68:71], a[108:111], v[148:151]\n"
"v_cvt_pk_bf16_f32 v26, v12, v13\n"
"v_cvt_pk_bf16_f32 v27, v14, v15\n"
"v_mfma_f32_16x16x32_bf16 v[148:151], a[64:67], a[104:107], v[148:151]\n"
"ds_write_b64 v7, v[26:27] offset:34824\n"
"v_mfma_f32_16x16x32_bf16 v[152:155], a[64:67], a[112:115], v[152:155]\n"
"s_waitcnt lgkmcnt(4)\n"
"v_lshlrev_b32 v12, 16, v20\n"
"v_and_b32 v13, s63, v20\n"
"v_mfma_f32_16x16x32_bf16 v[152:155], a[68:71], a[116:119], v[152:155]\n"
"global_load_dwordx4 a[52:55], v0, s[34:35] offset:1024 sc1\n"
"v_lshlrev_b32 v14, 16, v21\n"
"v_mfma_f32_16x16x32_bf16 v[180:183], a[76:79], a[116:119], v[180:183]\n"
"ds_read_b128 v[16:19], v7 offset:36864\n"
"v_and_b32 v15, s63, v21\n"
"v_mfma_f32_16x16x32_bf16 v[180:183], a[72:75], a[112:115], v[180:183]\n"
"v_mul_f32 v12, v12, v92\n"
"v_mul_f32 v13, v13, v93\n"
"v_mfma_f32_16x16x32_bf16 v[184:187], a[72:75], a[120:123], v[184:187]\n"
"v_mul_f32 v14, v14, v94\n"
"v_mul_f32 v15, v15, v95\n"
"v_mfma_f32_16x16x32_bf16 v[184:187], a[76:79], a[124:127], v[184:187]\n"
"v_cvt_pk_bf16_f32 v28, v12, v13\n"
"v_cvt_pk_bf16_f32 v29, v14, v15\n"
"v_mfma_f32_16x16x32_bf16 v[156:159], a[68:71], a[124:127], v[156:159]\n"
"ds_write_b64 v8, v[28:29] offset:34816\n"
"v_mfma_f32_16x16x32_bf16 v[156:159], a[64:67], a[120:123], v[156:159]\n"
"v_lshlrev_b32 v12, 16, v22\n"
"v_and_b32 v13, s63, v22\n"
"v_mfma_f32_16x16x32_bf16 v[160:163], a[64:67], a[128:131], v[160:163]\n"
"v_lshlrev_b32 v14, 16, v23\n"
"v_and_b32 v15, s63, v23\n"
"v_mfma_f32_16x16x32_bf16 v[160:163], a[68:71], a[132:135], v[160:163]\n"
"global_load_dwordx4 a[56:59], v0, s[34:35] offset:2048 sc1\n"
"v_mul_f32 v12, v12, v120\n"
"v_mfma_f32_16x16x32_bf16 v[188:191], a[76:79], a[132:135], v[188:191]\n"
"v_mul_f32 v13, v13, v121\n"
"v_mul_f32 v14, v14, v122\n"
"v_mfma_f32_16x16x32_bf16 v[188:191], a[72:75], a[128:131], v[188:191]\n"
"v_mul_f32 v15, v15, v123\n"
"v_cvt_pk_bf16_f32 v30, v12, v13\n"
"v_mfma_f32_16x16x32_bf16 v[192:195], a[72:75], a[136:139], v[192:195]\n"
"v_cvt_pk_bf16_f32 v31, v14, v15\n"
"v_mfma_f32_16x16x32_bf16 v[192:195], a[76:79], a[140:143], v[192:195]\n"
"ds_write_b64 v8, v[30:31] offset:34824\n"
"v_mfma_f32_16x16x32_bf16 v[164:167], a[68:71], a[140:143], v[164:167]\n"
"ds_read_b128 v[36:39], v2 offset:34816\n"
"v_mfma_f32_16x16x32_bf16 v[164:167], a[64:67], a[136:139], v[164:167]\n"
"s_waitcnt lgkmcnt(3)\n"
"ds_read_b128 v[64:67], v2 offset:35840\n"
"v_lshlrev_b32 v12, 16, v16\n"
"v_mfma_f32_16x16x32_bf16 v[168:171], a[64:67], a[144:147], v[168:171]\n"
"buffer_store_dwordx4 v[60:63], v6, s[52:55], s81 offen offset:1024 sc1\n"
"v_and_b32 v13, s63, v16\n"
"v_lshlrev_b32 v14, 16, v17\n"
"v_and_b32 v15, s63, v17\n"
"v_xor_b32 v20, 64, v7\n"
"v_mfma_f32_16x16x32_bf16 v[168:171], a[68:71], a[148:151], v[168:171]\n"
"global_load_dwordx4 a[60:63], v0, s[34:35] offset:3072 sc1\n"
"ds_read_b128 v[20:23], v20 offset:36864\n"
"v_mfma_f32_16x16x32_bf16 v[196:199], a[76:79], a[148:151], v[196:199]\n"
"v_mul_f32 v12, v12, v40\n"
"v_mul_f32 v13, v13, v41\n"
"v_mfma_f32_16x16x32_bf16 v[196:199], a[72:75], a[144:147], v[196:199]\n"
"v_mul_f32 v14, v14, v42\n"
"v_mul_f32 v15, v15, v43\n"
"v_mfma_f32_16x16x32_bf16 a[20:23], a[72:75], a[152:155], a[20:23]\n"
"v_cvt_pk_bf16_f32 v24, v12, v13\n"
"v_cvt_pk_bf16_f32 v25, v14, v15\n"
"v_mfma_f32_16x16x32_bf16 a[20:23], a[76:79], a[156:159], a[20:23]\n"
"ds_write_b64 v7, v[24:25] offset:36864\n"
"v_mfma_f32_16x16x32_bf16 a[16:19], a[68:71], a[156:159], a[16:19]\n"
"global_load_dwordx4 v[92:95], v0, s[96:97] offset:-2048 sc1\n"
"v_lshlrev_b32 v12, 16, v18\n"
"v_mfma_f32_16x16x32_bf16 a[16:19], a[64:67], a[152:155], a[16:19]\n"
"s_waitcnt vmcnt(13)\n"
"v_and_b32 v13, s63, v18\n"
"v_lshlrev_b32 v14, 16, v19\n"
"v_mfma_f32_16x16x32_bf16 v[200:203], a[80:83], a[96:99], v[200:203]\n"
"s_add_u32 s34, s36, 1499136\n"
"v_and_b32 v15, s63, v19\n"
"v_mul_f32 v12, v12, v68\n"
"v_mfma_f32_16x16x32_bf16 v[200:203], a[84:87], a[100:103], v[200:203]\n"
"global_load_dwordx4 a[64:67], v0, s[34:35] offset:0 sc1\n"
"v_mul_f32 v13, v13, v69\n"
"v_mfma_f32_16x16x32_bf16 v[228:231], a[92:95], a[100:103], v[228:231]\n"
"ds_read_b128 a[100:103], v4 offset:1024\n"
"v_mul_f32 v14, v14, v70\n"
"v_mfma_f32_16x16x32_bf16 v[228:231], a[88:91], a[96:99], v[228:231]\n"
"ds_read_b128 a[96:99], v4 offset:0\n"
"v_mul_f32 v15, v15, v71\n"
"v_mfma_f32_16x16x32_bf16 v[232:235], a[88:91], a[104:107], v[232:235]\n"
"v_cvt_pk_bf16_f32 v26, v12, v13\n"
"v_cvt_pk_bf16_f32 v27, v14, v15\n"
"v_mfma_f32_16x16x32_bf16 v[232:235], a[92:95], a[108:111], v[232:235]\n"
"ds_write_b64 v7, v[26:27] offset:36872\n"
"v_mfma_f32_16x16x32_bf16 v[204:207], a[84:87], a[108:111], v[204:207]\n"
"ds_read_b128 a[108:111], v4 offset:3072\n"
"v_mfma_f32_16x16x32_bf16 v[204:207], a[80:83], a[104:107], v[204:207]\n"
"ds_read_b128 a[104:107], v4 offset:2048\n"
"v_mfma_f32_16x16x32_bf16 v[208:211], a[80:83], a[112:115], v[208:211]\n"
"global_load_dwordx4 v[120:123], v0, s[96:97] offset:-1024 sc1\n"
"v_mfma_f32_16x16x32_bf16 v[208:211], a[84:87], a[116:119], v[208:211]\n"
"global_load_dwordx4 a[68:71], v0, s[34:35] offset:1024 sc1\n"
"v_mfma_f32_16x16x32_bf16 v[236:239], a[92:95], a[116:119], v[236:239]\n"
"s_waitcnt lgkmcnt(6)\n"
"ds_read_b128 a[116:119], v4 offset:5120\n"
"v_lshlrev_b32 v12, 16, v20\n"
"v_mfma_f32_16x16x32_bf16 v[236:239], a[88:91], a[112:115], v[236:239]\n"
"ds_read_b128 a[112:115], v4 offset:4096\n"
"v_and_b32 v13, s63, v20\n"
"v_mfma_f32_16x16x32_bf16 v[240:243], a[88:91], a[120:123], v[240:243]\n"
"v_lshlrev_b32 v14, 16, v21\n"
"v_and_b32 v15, s63, v21\n"
"v_mfma_f32_16x16x32_bf16 v[240:243], a[92:95], a[124:127], v[240:243]\n"
"ds_read_b128 v[16:19], v7 offset:38912\n"
"v_mul_f32 v12, v12, v96\n"
"v_mfma_f32_16x16x32_bf16 v[212:215], a[84:87], a[124:127], v[212:215]\n"
"ds_read_b128 a[124:127], v4 offset:7168\n"
"v_mul_f32 v13, v13, v97\n"
"v_mfma_f32_16x16x32_bf16 v[212:215], a[80:83], a[120:123], v[212:215]\n"
"ds_read_b128 a[120:123], v4 offset:6144\n"
"v_mul_f32 v14, v14, v98\n"
"v_mfma_f32_16x16x32_bf16 v[216:219], a[80:83], a[128:131], v[216:219]\n"
"v_mul_f32 v15, v15, v99\n"
"v_mfma_f32_16x16x32_bf16 v[216:219], a[84:87], a[132:135], v[216:219]\n"
"global_load_dwordx4 a[72:75], v0, s[34:35] offset:2048 sc1\n"
"v_cvt_pk_bf16_f32 v28, v12, v13\n"
"v_mfma_f32_16x16x32_bf16 v[244:247], a[92:95], a[132:135], v[244:247]\n"
"ds_read_b128 a[132:135], v4 offset:9216\n"
"global_load_dwordx4 v[32:35], v0, s[96:97] offset:0 sc1\n"
"v_mfma_f32_16x16x32_bf16 v[244:247], a[88:91], a[128:131], v[244:247]\n"
"ds_read_b128 a[128:131], v4 offset:8192\n"
"v_mfma_f32_16x16x32_bf16 v[248:251], a[88:91], a[136:139], v[248:251]\n"
"v_cvt_pk_bf16_f32 v29, v14, v15\n"
"v_mfma_f32_16x16x32_bf16 v[248:251], a[92:95], a[140:143], v[248:251]\n"
"buffer_store_dwordx4 v[36:39], v6, s[52:55], s82 offen offset:2048 sc1\n"
"ds_write_b64 v8, v[28:29] offset:36864\n"
"v_mfma_f32_16x16x32_bf16 v[220:223], a[84:87], a[140:143], v[220:223]\n"
"ds_read_b128 a[140:143], v4 offset:11264\n"
"v_lshlrev_b32 v12, 16, v22\n"
"v_mfma_f32_16x16x32_bf16 v[220:223], a[80:83], a[136:139], v[220:223]\n"
"ds_read_b128 a[136:139], v4 offset:10240\n"
"v_and_b32 v13, s63, v22\n"
"v_mfma_f32_16x16x32_bf16 v[224:227], a[80:83], a[144:147], v[224:227]\n"
"v_lshlrev_b32 v14, 16, v23\n"
"v_and_b32 v15, s63, v23\n"
"v_mfma_f32_16x16x32_bf16 v[224:227], a[84:87], a[148:151], v[224:227]\n"
"global_load_dwordx4 a[76:79], v0, s[34:35] offset:3072 sc1\n"
"v_mul_f32 v12, v12, v124\n"
"v_mfma_f32_16x16x32_bf16 v[252:255], a[92:95], a[148:151], v[252:255]\n"
"ds_read_b128 a[148:151], v4 offset:13312\n"
"v_mul_f32 v13, v13, v125\n"
"v_mfma_f32_16x16x32_bf16 v[252:255], a[88:91], a[144:147], v[252:255]\n"
"ds_read_b128 a[144:147], v4 offset:12288\n"
"v_mul_f32 v14, v14, v126\n"
"v_mfma_f32_16x16x32_bf16 a[28:31], a[88:91], a[152:155], a[28:31]\n"
"global_load_dwordx4 v[60:63], v0, s[96:97] offset:1024 sc1\n"
"v_mul_f32 v15, v15, v127\n"
"v_mfma_f32_16x16x32_bf16 a[28:31], a[92:95], a[156:159], a[28:31]\n"
"v_cvt_pk_bf16_f32 v30, v12, v13\n"
"v_cvt_pk_bf16_f32 v31, v14, v15\n"
"v_mfma_f32_16x16x32_bf16 a[24:27], a[84:87], a[156:159], a[24:27]\n"
"ds_read_b128 a[156:159], v4 offset:15360\n"
"v_mfma_f32_16x16x32_bf16 a[24:27], a[80:83], a[152:155], a[24:27]\n"
"s_waitcnt vmcnt(16) lgkmcnt(13)\n"
"ds_read_b128 a[152:155], v4 offset:14336\n"
"v_mfma_f32_16x16x32_bf16 v[144:147], a[32:35], a[96:99], v[144:147]\n"
"s_add_u32 s34, s36, 1503232\n"
"ds_write_b64 v8, v[30:31] offset:36872\n"
"v_mfma_f32_16x16x32_bf16 v[144:147], a[36:39], a[100:103], v[144:147]\n"
"L_st9745:\n"
"s_cbranch_execz L_retC\n"
"global_load_dwordx4 a[80:83], v0, s[34:35] offset:0 sc1\n"
"ds_read_b128 v[40:43], v2 offset:36864\n"
"v_mfma_f32_16x16x32_bf16 v[172:175], a[44:47], a[100:103], v[172:175]\n"
"s_waitcnt lgkmcnt(13)\n"
"ds_read_b128 v[68:71], v2 offset:37888\n"
"v_lshlrev_b32 v12, 16, v16\n"
"v_mfma_f32_16x16x32_bf16 v[172:175], a[40:43], a[96:99], v[172:175]\n"
"v_and_b32 v13, s63, v16\n"
"v_lshlrev_b32 v14, 16, v17\n"
"v_mfma_f32_16x16x32_bf16 v[176:179], a[40:43], a[104:107], v[176:179]\n"
"v_and_b32 v15, s63, v17\n"
"v_xor_b32 v20, 64, v7\n"
"v_mfma_f32_16x16x32_bf16 v[176:179], a[44:47], a[108:111], v[176:179]\n"
"ds_read_b128 v[20:23], v20 offset:38912\n"
"v_mul_f32 v12, v12, v44\n"
"v_mfma_f32_16x16x32_bf16 v[148:151], a[36:39], a[108:111], v[148:151]\n"
"v_mul_f32 v13, v13, v45\n"
"v_mul_f32 v14, v14, v46\n"
"v_mfma_f32_16x16x32_bf16 v[148:151], a[32:35], a[104:107], v[148:151]\n"
"s_waitcnt lgkmcnt(13)\n"
"global_load_dwordx4 v[96:99], v0, s[96:97] offset:2048 sc1\n"
"v_mul_f32 v15, v15, v47\n"
"v_mfma_f32_16x16x32_bf16 v[152:155], a[32:35], a[112:115], v[152:155]\n"
"v_cvt_pk_bf16_f32 v24, v12, v13\n"
"v_cvt_pk_bf16_f32 v25, v14, v15\n"
"v_mfma_f32_16x16x32_bf16 v[152:155], a[36:39], a[116:119], v[152:155]\n"
"global_load_dwordx4 a[84:87], v0, s[34:35] offset:1024 sc1\n"
"v_mfma_f32_16x16x32_bf16 v[180:183], a[44:47], a[116:119], v[180:183]\n"
"ds_write_b64 v7, v[24:25] offset:38912\n"
"v_mfma_f32_16x16x32_bf16 v[180:183], a[40:43], a[112:115], v[180:183]\n"
"v_lshlrev_b32 v12, 16, v18\n"
"v_and_b32 v13, s63, v18\n"
"v_mfma_f32_16x16x32_bf16 v[184:187], a[40:43], a[120:123], v[184:187]\n"
"v_lshlrev_b32 v14, 16, v19\n"
"v_and_b32 v15, s63, v19\n"
"v_mfma_f32_16x16x32_bf16 v[184:187], a[44:47], a[124:127], v[184:187]\n"
"v_mul_f32 v12, v12, v72\n"
"v_mul_f32 v13, v13, v73\n"
"v_mfma_f32_16x16x32_bf16 v[156:159], a[36:39], a[124:127], v[156:159]\n"
"v_mul_f32 v14, v14, v74\n"
"v_mul_f32 v15, v15, v75\n"
"v_mfma_f32_16x16x32_bf16 v[156:159], a[32:35], a[120:123], v[156:159]\n"
"s_waitcnt lgkmcnt(9)\n"
"v_cvt_pk_bf16_f32 v26, v12, v13\n"
"v_cvt_pk_bf16_f32 v27, v14, v15\n"
"v_mfma_f32_16x16x32_bf16 v[160:163], a[32:35], a[128:131], v[160:163]\n"
"buffer_store_dwordx4 v[64:67], v6, s[52:55], s83 offen offset:3072 sc1\n"
"ds_write_b64 v7, v[26:27] offset:38920\n"
"v_mfma_f32_16x16x32_bf16 v[160:163], a[36:39], a[132:135], v[160:163]\n"
"s_waitcnt lgkmcnt(2)\n"
"global_load_dwordx4 a[88:91], v0, s[34:35] offset:2048 sc1\n"
"v_lshlrev_b32 v12, 16, v20\n"
"v_mfma_f32_16x16x32_bf16 v[188:191], a[44:47], a[132:135], v[188:191]\n"
"global_load_dwordx4 v[124:127], v0, s[96:97] offset:3072 sc1\n"
"v_and_b32 v13, s63, v20\n"
"v_mfma_f32_16x16x32_bf16 v[188:191], a[40:43], a[128:131], v[188:191]\n"
"v_lshlrev_b32 v14, 16, v21\n"
"v_and_b32 v15, s63, v21\n"
"v_mfma_f32_16x16x32_bf16 v[192:195], a[40:43], a[136:139], v[192:195]\n"
"v_mul_f32 v12, v12, v100\n"
"v_mul_f32 v13, v13, v101\n"
"v_mfma_f32_16x16x32_bf16 v[192:195], a[44:47], a[140:143], v[192:195]\n"
"v_mul_f32 v14, v14, v102\n"
"v_mul_f32 v15, v15, v103\n"
"v_mfma_f32_16x16x32_bf16 v[164:167], a[36:39], a[140:143], v[164:167]\n"
"v_cvt_pk_bf16_f32 v28, v12, v13\n"
"v_cvt_pk_bf16_f32 v29, v14, v15\n"
"v_mfma_f32_16x16x32_bf16 v[164:167], a[32:35], a[136:139], v[164:167]\n"
"ds_write_b64 v8, v[28:29] offset:38912\n"
"v_mfma_f32_16x16x32_bf16 v[168:171], a[32:35], a[144:147], v[168:171]\n"
"v_lshlrev_b32 v12, 16, v22\n"
"v_mfma_f32_16x16x32_bf16 v[168:171], a[36:39], a[148:151], v[168:171]\n"
"global_load_dwordx4 a[92:95], v0, s[34:35] offset:3072 sc1\n"
"v_and_b32 v13, s63, v22\n"
"v_mfma_f32_16x16x32_bf16 v[196:199], a[44:47], a[148:151], v[196:199]\n"
"v_lshlrev_b32 v14, 16, v23\n"
"v_and_b32 v15, s63, v23\n"
"v_mfma_f32_16x16x32_bf16 v[196:199], a[40:43], a[144:147], v[196:199]\n"
"v_mul_f32 v12, v12, v128\n"
"v_mfma_f32_16x16x32_bf16 a[20:23], a[40:43], a[152:155], a[20:23]\n"
"v_mul_f32 v13, v13, v129\n"
"v_mfma_f32_16x16x32_bf16 a[20:23], a[44:47], a[156:159], a[20:23]\n"
"v_mul_f32 v14, v14, v130\n"
"v_mul_f32 v15, v15, v131\n"
"v_mfma_f32_16x16x32_bf16 a[16:19], a[36:39], a[156:159], a[16:19]\n"
"v_cvt_pk_bf16_f32 v30, v12, v13\n"
"v_cvt_pk_bf16_f32 v31, v14, v15\n"
"v_mfma_f32_16x16x32_bf16 a[16:19], a[32:35], a[152:155], a[16:19]\n"
"s_waitcnt vmcnt(16)\n"
"ds_write_b64 v8, v[30:31] offset:38920\n"
"v_mfma_f32_16x16x32_bf16 v[200:203], a[48:51], a[96:99], v[200:203]\n"
"s_add_u32 s34, s36, 1540096\n"
"ds_read_b128 v[44:47], v2 offset:38912\n"
"v_mfma_f32_16x16x32_bf16 v[200:203], a[52:55], a[100:103], v[200:203]\n"
"global_load_dwordx4 a[32:35], v0, s[34:35] offset:0 sc1\n"
"ds_read_b128 v[72:75], v2 offset:39936\n"
"v_mfma_f32_16x16x32_bf16 v[228:231], a[60:63], a[100:103], v[228:231]\n"
"ds_read_b128 a[100:103], v5 offset:1024\n"
"v_accvgpr_read_b32 v16, a160\n"
"v_mfma_f32_16x16x32_bf16 v[228:231], a[56:59], a[96:99], v[228:231]\n"
"ds_read_b128 a[96:99], v5 offset:0\n"
"v_accvgpr_read_b32 v17, a161\n"
"v_mfma_f32_16x16x32_bf16 v[232:235], a[56:59], a[104:107], v[232:235]\n"
"v_accvgpr_read_b32 v18, a162\n"
"v_accvgpr_read_b32 v19, a163\n"
"v_mfma_f32_16x16x32_bf16 v[232:235], a[60:63], a[108:111], v[232:235]\n"
"v_lshlrev_b32 v12, 16, v16\n"
"v_mfma_f32_16x16x32_bf16 v[204:207], a[52:55], a[108:111], v[204:207]\n"
"ds_read_b128 a[108:111], v5 offset:3072\n"
"v_and_b32 v13, s63, v16\n"
"v_mfma_f32_16x16x32_bf16 v[204:207], a[48:51], a[104:107], v[204:207]\n"
"ds_read_b128 a[104:107], v5 offset:2048\n"
"v_lshlrev_b32 v14, 16, v17\n"
"v_mfma_f32_16x16x32_bf16 v[208:211], a[48:51], a[112:115], v[208:211]\n"
"v_and_b32 v15, s63, v17\n"
"v_mul_f32 v12, v12, v48\n"
"v_mfma_f32_16x16x32_bf16 v[208:211], a[52:55], a[116:119], v[208:211]\n"
"global_load_dwordx4 a[36:39], v0, s[34:35] offset:1024 sc1\n"
"v_mul_f32 v13, v13, v49\n"
"v_mfma_f32_16x16x32_bf16 v[236:239], a[60:63], a[116:119], v[236:239]\n"
"ds_read_b128 a[116:119], v5 offset:5120\n"
"v_mul_f32 v14, v14, v50\n"
"v_mfma_f32_16x16x32_bf16 v[236:239], a[56:59], a[112:115], v[236:239]\n"
"ds_read_b128 a[112:115], v5 offset:4096\n"
"v_mul_f32 v15, v15, v51\n"
"v_mfma_f32_16x16x32_bf16 v[240:243], a[56:59], a[120:123], v[240:243]\n"
"v_cvt_pk_bf16_f32 v24, v12, v13\n"
"v_cvt_pk_bf16_f32 v25, v14, v15\n"
"v_mfma_f32_16x16x32_bf16 v[240:243], a[60:63], a[124:127], v[240:243]\n"
"buffer_store_dwordx4 v[40:43], v6, s[52:55], s84 offen offset:0 sc1\n"
"ds_write_b64 v7, v[24:25] offset:32768\n"
"v_mfma_f32_16x16x32_bf16 v[212:215], a[52:55], a[124:127], v[212:215]\n"
"ds_read_b128 a[124:127], v5 offset:7168\n"
"v_lshlrev_b32 v12, 16, v18\n"
"v_mfma_f32_16x16x32_bf16 v[212:215], a[48:51], a[120:123], v[212:215]\n"
"ds_read_b128 a[120:123], v5 offset:6144\n"
"v_and_b32 v13, s63, v18\n"
"v_mfma_f32_16x16x32_bf16 v[216:219], a[48:51], a[128:131], v[216:219]\n"
"v_lshlrev_b32 v14, 16, v19\n"
"v_and_b32 v15, s63, v19\n"
"v_mfma_f32_16x16x32_bf16 v[216:219], a[52:55], a[132:135], v[216:219]\n"
"global_load_dwordx4 a[40:43], v0, s[34:35] offset:2048 sc1\n"
"v_mul_f32 v12, v12, v76\n"
"v_mfma_f32_16x16x32_bf16 v[244:247], a[60:63], a[132:135], v[244:247]\n"
"s_add_u32 s96, s36, 1560576\n"
"ds_read_b128 a[132:135], v5 offset:9216\n"
"v_mul_f32 v13, v13, v77\n"
"v_mfma_f32_16x16x32_bf16 v[244:247], a[56:59], a[128:131], v[244:247]\n"
"ds_read_b128 a[128:131], v5 offset:8192\n"
"v_mul_f32 v14, v14, v78\n"
"v_mfma_f32_16x16x32_bf16 v[248:251], a[56:59], a[136:139], v[248:251]\n"
"global_load_dwordx4 v[100:103], v0, s[96:97] offset:-4096 sc1\n"
"v_mul_f32 v15, v15, v79\n"
"v_mfma_f32_16x16x32_bf16 v[248:251], a[60:63], a[140:143], v[248:251]\n"
"v_cvt_pk_bf16_f32 v26, v12, v13\n"
"v_cvt_pk_bf16_f32 v27, v14, v15\n"
"v_mfma_f32_16x16x32_bf16 v[220:223], a[52:55], a[140:143], v[220:223]\n"
"ds_read_b128 a[140:143], v5 offset:11264\n"
"v_mfma_f32_16x16x32_bf16 v[220:223], a[48:51], a[136:139], v[220:223]\n"
"ds_read_b128 a[136:139], v5 offset:10240\n"
"v_mfma_f32_16x16x32_bf16 v[224:227], a[48:51], a[144:147], v[224:227]\n"
"ds_write_b64 v7, v[26:27] offset:32776\n"
"v_mfma_f32_16x16x32_bf16 v[224:227], a[52:55], a[148:151], v[224:227]\n"
"global_load_dwordx4 a[44:47], v0, s[34:35] offset:3072 sc1\n"
"v_accvgpr_read_b32 v16, a164\n"
"v_mfma_f32_16x16x32_bf16 v[252:255], a[60:63], a[148:151], v[252:255]\n"
"ds_read_b128 a[148:151], v5 offset:13312\n"
"v_accvgpr_read_b32 v17, a165\n"
"v_mfma_f32_16x16x32_bf16 v[252:255], a[56:59], a[144:147], v[252:255]\n"
"ds_read_b128 a[144:147], v5 offset:12288\n"
"v_accvgpr_read_b32 v18, a166\n"
"v_mfma_f32_16x16x32_bf16 a[28:31], a[56:59], a[152:155], a[28:31]\n"
"v_accvgpr_read_b32 v19, a167\n"
"v_lshlrev_b32 v12, 16, v16\n"
"v_mfma_f32_16x16x32_bf16 a[28:31], a[60:63], a[156:159], a[28:31]\n"
"global_load_dwordx4 v[128:131], v0, s[96:97] offset:-3072 sc1\n"
"v_and_b32 v13, s63, v16\n"
"v_mfma_f32_16x16x32_bf16 a[24:27], a[52:55], a[156:159], a[24:27]\n"
"ds_read_b128 a[156:159], v5 offset:15360\n"
"v_lshlrev_b32 v14, 16, v17\n"
"v_mfma_f32_16x16x32_bf16 a[24:27], a[48:51], a[152:155], a[24:27]\n"
"s_waitcnt vmcnt(15) lgkmcnt(13)\n"
"ds_read_b128 a[152:155], v5 offset:14336\n"
"v_and_b32 v15, s63, v17\n"
"v_mfma_f32_16x16x32_bf16 v[144:147], a[64:67], a[96:99], v[144:147]\n"
"s_add_u32 s34, s36, 1544192\n"
"v_mul_f32 v12, v12, v104\n"
"v_mul_f32 v13, v13, v105\n"
"v_mfma_f32_16x16x32_bf16 v[144:147], a[68:71], a[100:103], v[144:147]\n"
"global_load_dwordx4 a[48:51], v0, s[34:35] offset:0 sc1\n"
"v_mul_f32 v14, v14, v106\n"
"v_mfma_f32_16x16x32_bf16 v[172:175], a[76:79], a[100:103], v[172:175]\n"
"v_mul_f32 v15, v15, v107\n"
"v_cvt_pk_bf16_f32 v28, v12, v13\n"
"v_mfma_f32_16x16x32_bf16 v[172:175], a[72:75], a[96:99], v[172:175]\n"
"v_cvt_pk_bf16_f32 v29, v14, v15\n"
"v_mfma_f32_16x16x32_bf16 v[176:179], a[72:75], a[104:107], v[176:179]\n"
"ds_write_b64 v8, v[28:29] offset:32768\n"
"v_mfma_f32_16x16x32_bf16 v[176:179], a[76:79], a[108:111], v[176:179]\n"
"v_lshlrev_b32 v12, 16, v18\n"
"v_and_b32 v13, s63, v18\n"
"v_mfma_f32_16x16x32_bf16 v[148:151], a[68:71], a[108:111], v[148:151]\n"
"v_lshlrev_b32 v14, 16, v19\n"
"v_and_b32 v15, s63, v19\n"
"v_mfma_f32_16x16x32_bf16 v[148:151], a[64:67], a[104:107], v[148:151]\n"
"s_waitcnt lgkmcnt(10)\n"
"v_mul_f32 v12, v12, v132\n"
"v_mul_f32 v13, v13, v133\n"
"v_mfma_f32_16x16x32_bf16 v[152:155], a[64:67], a[112:115], v[152:155]\n"
"buffer_store_dwordx4 v[68:71], v6, s[52:55], s85 offen offset:1024 sc1\n"
"global_load_dwordx4 v[36:39], v0, s[96:97] offset:-2048 sc1\n"
"v_mul_f32 v14, v14, v134\n"
"v_mul_f32 v15, v15, v135\n"
"v_mfma_f32_16x16x32_bf16 v[152:155], a[68:71], a[116:119], v[152:155]\n"
"global_load_dwordx4 a[52:55], v0, s[34:35] offset:1024 sc1\n"
"v_cvt_pk_bf16_f32 v30, v12, v13\n"
"v_mfma_f32_16x16x32_bf16 v[180:183], a[76:79], a[116:119], v[180:183]\n"
"v_cvt_pk_bf16_f32 v31, v14, v15\n"
"v_mfma_f32_16x16x32_bf16 v[180:183], a[72:75], a[112:115], v[180:183]\n"
"ds_write_b64 v8, v[30:31] offset:32776\n"
"v_mfma_f32_16x16x32_bf16 v[184:187], a[72:75], a[120:123], v[184:187]\n"
"ds_read_b128 v[48:51], v2 offset:32768\n"
"v_mfma_f32_16x16x32_bf16 v[184:187], a[76:79], a[124:127], v[184:187]\n"
"ds_read_b128 v[76:79], v2 offset:33792\n"
"v_accvgpr_read_b32 v16, a168\n"
"v_mfma_f32_16x16x32_bf16 v[156:159], a[68:71], a[124:127], v[156:159]\n"
"v_accvgpr_read_b32 v17, a169\n"
"v_accvgpr_read_b32 v18, a170\n"
"v_mfma_f32_16x16x32_bf16 v[156:159], a[64:67], a[120:123], v[156:159]\n"
"s_waitcnt lgkmcnt(9)\n"
"v_accvgpr_read_b32 v19, a171\n"
"v_lshlrev_b32 v12, 16, v16\n"
"v_mfma_f32_16x16x32_bf16 v[160:163], a[64:67], a[128:131], v[160:163]\n"
"v_and_b32 v13, s63, v16\n"
"v_lshlrev_b32 v14, 16, v17\n"
"v_mfma_f32_16x16x32_bf16 v[160:163], a[68:71], a[132:135], v[160:163]\n"
"global_load_dwordx4 a[56:59], v0, s[34:35] offset:2048 sc1\n"
"v_and_b32 v15, s63, v17\n"
"v_mfma_f32_16x16x32_bf16 v[188:191], a[76:79], a[132:135], v[188:191]\n"
"global_load_dwordx4 v[64:67], v0, s[96:97] offset:-1024 sc1\n"
"v_mul_f32 v12, v12, v52\n"
"v_mfma_f32_16x16x32_bf16 v[188:191], a[72:75], a[128:131], v[188:191]\n"
"v_mul_f32 v13, v13, v53\n"
"v_mul_f32 v14, v14, v54\n"
"v_mfma_f32_16x16x32_bf16 v[192:195], a[72:75], a[136:139], v[192:195]\n"
"v_mul_f32 v15, v15, v55\n"
"v_mfma_f32_16x16x32_bf16 v[192:195], a[76:79], a[140:143], v[192:195]\n"
"v_cvt_pk_bf16_f32 v24, v12, v13\n"
"v_cvt_pk_bf16_f32 v25, v14, v15\n"
"v_mfma_f32_16x16x32_bf16 v[164:167], a[68:71], a[140:143], v[164:167]\n"
"ds_write_b64 v7, v[24:25] offset:34816\n"
"v_mfma_f32_16x16x32_bf16 v[164:167], a[64:67], a[136:139], v[164:167]\n"
"s_waitcnt lgkmcnt(5)\n"
"v_lshlrev_b32 v12, 16, v18\n"
"v_and_b32 v13, s63, v18\n"
"v_mfma_f32_16x16x32_bf16 v[168:171], a[64:67], a[144:147], v[168:171]\n"
"v_lshlrev_b32 v14, 16, v19\n"
"v_and_b32 v15, s63, v19\n"
"v_mfma_f32_16x16x32_bf16 v[168:171], a[68:71], a[148:151], v[168:171]\n"
"global_load_dwordx4 a[60:63], v0, s[34:35] offset:3072 sc1\n"
"v_mul_f32 v12, v12, v80\n"
"v_mfma_f32_16x16x32_bf16 v[196:199], a[76:79], a[148:151], v[196:199]\n"
"v_mul_f32 v13, v13, v81\n"
"v_mul_f32 v14, v14, v82\n"
"v_mfma_f32_16x16x32_bf16 v[196:199], a[72:75], a[144:147], v[196:199]\n"
"v_mul_f32 v15, v15, v83\n"
"v_cvt_pk_bf16_f32 v26, v12, v13\n"
"v_mfma_f32_16x16x32_bf16 a[20:23], a[72:75], a[152:155], a[20:23]\n"
"global_load_dwordx4 v[104:107], v0, s[96:97] offset:0 sc1\n"
"v_cvt_pk_bf16_f32 v27, v14, v15\n"
"v_mfma_f32_16x16x32_bf16 a[20:23], a[76:79], a[156:159], a[20:23]\n"
"ds_write_b64 v7, v[26:27] offset:34824\n"
"v_mfma_f32_16x16x32_bf16 a[16:19], a[68:71], a[156:159], a[16:19]\n"
"v_accvgpr_read_b32 v16, a172\n"
"v_mfma_f32_16x16x32_bf16 a[16:19], a[64:67], a[152:155], a[16:19]\n"
"s_waitcnt vmcnt(15)\n"
"v_accvgpr_read_b32 v17, a173\n"
"v_accvgpr_read_b32 v18, a174\n"
"v_mfma_f32_16x16x32_bf16 v[200:203], a[80:83], a[96:99], v[200:203]\n"
"s_add_u32 s34, s36, 1564672\n"
"v_accvgpr_read_b32 v19, a175\n"
"v_lshlrev_b32 v12, 16, v16\n"
"v_mfma_f32_16x16x32_bf16 v[200:203], a[84:87], a[100:103], v[200:203]\n"
"global_load_dwordx4 a[64:67], v0, s[34:35] offset:0 sc1\n"
"v_and_b32 v13, s63, v16\n"
"v_mfma_f32_16x16x32_bf16 v[228:231], a[92:95], a[100:103], v[228:231]\n"
"ds_read_b128 a[100:103], v2 offset:17408\n"
"v_lshlrev_b32 v14, 16, v17\n"
"v_mfma_f32_16x16x32_bf16 v[228:231], a[88:91], a[96:99], v[228:231]\n"
"ds_read_b128 a[96:99], v2 offset:16384\n"
"v_and_b32 v15, s63, v17\n"
"v_mfma_f32_16x16x32_bf16 v[232:235], a[88:91], a[104:107], v[232:235]\n"
"v_mul_f32 v12, v12, v108\n"
"v_mul_f32 v13, v13, v109\n"
"v_mfma_f32_16x16x32_bf16 v[232:235], a[92:95], a[108:111], v[232:235]\n"
"buffer_store_dwordx4 v[44:47], v6, s[52:55], s86 offen offset:2048 sc1\n"
"global_load_dwordx4 v[132:135], v0, s[96:97] offset:1024 sc1\n"
"v_mul_f32 v14, v14, v110\n"
"v_mul_f32 v15, v15, v111\n"
"v_mfma_f32_16x16x32_bf16 v[204:207], a[84:87], a[108:111], v[204:207]\n"
"ds_read_b128 a[108:111], v2 offset:19456\n"
"v_cvt_pk_bf16_f32 v28, v12, v13\n"
"v_mfma_f32_16x16x32_bf16 v[204:207], a[80:83], a[104:107], v[204:207]\n"
"ds_read_b128 a[104:107], v2 offset:18432\n"
"v_cvt_pk_bf16_f32 v29, v14, v15\n"
"v_mfma_f32_16x16x32_bf16 v[208:211], a[80:83], a[112:115], v[208:211]\n"
"ds_write_b64 v8, v[28:29] offset:34816\n"
"v_mfma_f32_16x16x32_bf16 v[208:211], a[84:87], a[116:119], v[208:211]\n"
"global_load_dwordx4 a[68:71], v0, s[34:35] offset:1024 sc1\n"
"v_mfma_f32_16x16x32_bf16 v[236:239], a[92:95], a[116:119], v[236:239]\n"
"ds_read_b128 a[116:119], v2 offset:21504\n"
"v_mfma_f32_16x16x32_bf16 v[236:239], a[88:91], a[112:115], v[236:239]\n"
"ds_read_b128 a[112:115], v2 offset:20480\n"
"v_lshlrev_b32 v12, 16, v18\n"
"v_mfma_f32_16x16x32_bf16 v[240:243], a[88:91], a[120:123], v[240:243]\n"
"v_and_b32 v13, s63, v18\n"
"v_mfma_f32_16x16x32_bf16 v[240:243], a[92:95], a[124:127], v[240:243]\n"
"v_lshlrev_b32 v14, 16, v19\n"
"v_and_b32 v15, s63, v19\n"
"v_mfma_f32_16x16x32_bf16 v[212:215], a[84:87], a[124:127], v[212:215]\n"
"ds_read_b128 a[124:127], v2 offset:23552\n"
"v_mul_f32 v12, v12, v136\n"
"v_mfma_f32_16x16x32_bf16 v[212:215], a[80:83], a[120:123], v[212:215]\n"
"ds_read_b128 a[120:123], v2 offset:22528\n"
"v_mul_f32 v13, v13, v137\n"
"v_mfma_f32_16x16x32_bf16 v[216:219], a[80:83], a[128:131], v[216:219]\n"
"v_mul_f32 v14, v14, v138\n"
"v_mul_f32 v15, v15, v139\n"
"v_mfma_f32_16x16x32_bf16 v[216:219], a[84:87], a[132:135], v[216:219]\n"
"global_load_dwordx4 a[72:75], v0, s[34:35] offset:2048 sc1\n"
"v_cvt_pk_bf16_f32 v30, v12, v13\n"
"v_mfma_f32_16x16x32_bf16 v[244:247], a[92:95], a[132:135], v[244:247]\n"
"ds_read_b128 a[132:135], v2 offset:25600\n"
"v_cvt_pk_bf16_f32 v31, v14, v15\n"
"v_mfma_f32_16x16x32_bf16 v[244:247], a[88:91], a[128:131], v[244:247]\n"
"ds_read_b128 a[128:131], v2 offset:24576\n"
"v_mfma_f32_16x16x32_bf16 v[248:251], a[88:91], a[136:139], v[248:251]\n"
"ds_write_b64 v8, v[30:31] offset:34824\n"
"v_mfma_f32_16x16x32_bf16 v[248:251], a[92:95], a[140:143], v[248:251]\n"
"ds_read_b128 v[52:55], v2 offset:34816\n"
"v_mfma_f32_16x16x32_bf16 v[220:223], a[84:87], a[140:143], v[220:223]\n"
"ds_read_b128 a[140:143], v2 offset:27648\n"
"v_mfma_f32_16x16x32_bf16 v[220:223], a[80:83], a[136:139], v[220:223]\n"
"ds_read_b128 a[136:139], v2 offset:26624\n"
"v_mfma_f32_16x16x32_bf16 v[224:227], a[80:83], a[144:147], v[224:227]\n"
"ds_read_b128 v[80:83], v2 offset:35840\n"
"v_accvgpr_read_b32 v16, a176\n"
"v_mfma_f32_16x16x32_bf16 v[224:227], a[84:87], a[148:151], v[224:227]\n"
"global_load_dwordx4 a[76:79], v0, s[34:35] offset:3072 sc1\n"
"v_accvgpr_read_b32 v17, a177\n"
"v_mfma_f32_16x16x32_bf16 v[252:255], a[92:95], a[148:151], v[252:255]\n"
"ds_read_b128 a[148:151], v2 offset:29696\n"
"v_accvgpr_read_b32 v18, a178\n"
"v_mfma_f32_16x16x32_bf16 v[252:255], a[88:91], a[144:147], v[252:255]\n"
"ds_read_b128 a[144:147], v2 offset:28672\n"
"v_accvgpr_read_b32 v19, a179\n"
"v_mfma_f32_16x16x32_bf16 a[28:31], a[88:91], a[152:155], a[28:31]\n"
"v_lshlrev_b32 v12, 16, v16\n"
"v_and_b32 v13, s63, v16\n"
"v_mfma_f32_16x16x32_bf16 a[28:31], a[92:95], a[156:159], a[28:31]\n"
"v_lshlrev_b32 v14, 16, v17\n"
"v_and_b32 v15, s63, v17\n"
"v_mfma_f32_16x16x32_bf16 a[24:27], a[84:87], a[156:159], a[24:27]\n"
"ds_read_b128 a[156:159], v2 offset:31744\n"
"v_mul_f32 v12, v12, v56\n"
"v_mfma_f32_16x16x32_bf16 a[24:27], a[80:83], a[152:155], a[24:27]\n"
"s_waitcnt vmcnt(15) lgkmcnt(15)\n"
"ds_read_b128 a[152:155], v2 offset:30720\n"
"v_mul_f32 v13, v13, v57\n"
"v_mfma_f32_16x16x32_bf16 v[144:147], a[32:35], a[96:99], v[144:147]\n"
"buffer_store_dwordx4 v[72:75], v6, s[52:55], s87 offen offset:3072 sc1\n"
"s_add_u32 s34, s36, 1568768\n"
"v_mul_f32 v14, v14, v58\n"
"v_mul_f32 v15, v15, v59\n"
"v_mfma_f32_16x16x32_bf16 v[144:147], a[36:39], a[100:103], v[144:147]\n"
"global_load_dwordx4 a[80:83], v0, s[34:35] offset:0 sc1\n"
"v_cvt_pk_bf16_f32 v24, v12, v13\n"
"v_mfma_f32_16x16x32_bf16 v[172:175], a[44:47], a[100:103], v[172:175]\n"
"v_cvt_pk_bf16_f32 v25, v14, v15\n"
"v_mfma_f32_16x16x32_bf16 v[172:175], a[40:43], a[96:99], v[172:175]\n"
"ds_write_b64 v7, v[24:25] offset:36864\n"
"v_mfma_f32_16x16x32_bf16 v[176:179], a[40:43], a[104:107], v[176:179]\n"
"global_load_dwordx4 a[84:87], v0, s[34:35] offset:1024 sc1\n"
"v_lshlrev_b32 v12, 16, v18\n"
"v_mfma_f32_16x16x32_bf16 v[176:179], a[44:47], a[108:111], v[176:179]\n"
"global_load_dwordx4 v[108:111], v0, s[96:97] offset:2048 sc1\n"
"v_and_b32 v13, s63, v18\n"
"v_mfma_f32_16x16x32_bf16 v[148:151], a[36:39], a[108:111], v[148:151]\n"
"v_lshlrev_b32 v14, 16, v19\n"
"v_and_b32 v15, s63, v19\n"
"v_mfma_f32_16x16x32_bf16 v[148:151], a[32:35], a[104:107], v[148:151]\n"
"s_waitcnt lgkmcnt(12)\n"
"global_load_dwordx4 a[88:91], v0, s[34:35] offset:2048 sc1\n"
"v_mul_f32 v12, v12, v84\n"
"v_mfma_f32_16x16x32_bf16 v[152:155], a[32:35], a[112:115], v[152:155]\n"
"v_mul_f32 v13, v13, v85\n"
"v_mul_f32 v14, v14, v86\n"
"v_mfma_f32_16x16x32_bf16 v[152:155], a[36:39], a[116:119], v[152:155]\n"
"v_mul_f32 v15, v15, v87\n"
"v_cvt_pk_bf16_f32 v26, v12, v13\n"
"v_mfma_f32_16x16x32_bf16 v[180:183], a[44:47], a[116:119], v[180:183]\n"
"global_load_dwordx4 a[92:95], v0, s[34:35] offset:3072 sc1\n"
"v_cvt_pk_bf16_f32 v27, v14, v15\n"
"v_mfma_f32_16x16x32_bf16 v[180:183], a[40:43], a[112:115], v[180:183]\n"
"ds_write_b64 v7, v[26:27] offset:36872\n"
"v_mfma_f32_16x16x32_bf16 v[184:187], a[40:43], a[120:123], v[184:187]\n"
"v_accvgpr_read_b32 v16, a180\n"
"v_accvgpr_read_b32 v17, a181\n"
"v_mfma_f32_16x16x32_bf16 v[184:187], a[44:47], a[124:127], v[184:187]\n"
"v_accvgpr_read_b32 v18, a182\n"
"v_accvgpr_read_b32 v19, a183\n"
"v_mfma_f32_16x16x32_bf16 v[156:159], a[36:39], a[124:127], v[156:159]\n"
"global_load_dwordx4 v[136:139], v0, s[96:97] offset:3072 sc1\n"
"v_lshlrev_b32 v12, 16, v16\n"
"v_mfma_f32_16x16x32_bf16 v[156:159], a[32:35], a[120:123], v[156:159]\n"
"s_waitcnt lgkmcnt(7)\n"
"v_and_b32 v13, s63, v16\n"
"v_lshlrev_b32 v14, 16, v17\n"
"v_mfma_f32_16x16x32_bf16 v[160:163], a[32:35], a[128:131], v[160:163]\n"
"v_and_b32 v15, s63, v17\n"
"v_mul_f32 v12, v12, v112\n"
"v_mfma_f32_16x16x32_bf16 v[160:163], a[36:39], a[132:135], v[160:163]\n"
"v_mul_f32 v13, v13, v113\n"
"v_mul_f32 v14, v14, v114\n"
"v_mfma_f32_16x16x32_bf16 v[188:191], a[44:47], a[132:135], v[188:191]\n"
"v_mul_f32 v15, v15, v115\n"
"v_mfma_f32_16x16x32_bf16 v[188:191], a[40:43], a[128:131], v[188:191]\n"
"v_cvt_pk_bf16_f32 v28, v12, v13\n"
"v_cvt_pk_bf16_f32 v29, v14, v15\n"
"v_mfma_f32_16x16x32_bf16 v[192:195], a[40:43], a[136:139], v[192:195]\n"
"ds_write_b64 v8, v[28:29] offset:36864\n"
"v_mfma_f32_16x16x32_bf16 v[192:195], a[44:47], a[140:143], v[192:195]\n"
"v_lshlrev_b32 v12, 16, v18\n"
"v_mfma_f32_16x16x32_bf16 v[164:167], a[36:39], a[140:143], v[164:167]\n"
"v_and_b32 v13, s63, v18\n"
"v_lshlrev_b32 v14, 16, v19\n"
"v_mfma_f32_16x16x32_bf16 v[164:167], a[32:35], a[136:139], v[164:167]\n"
"s_waitcnt lgkmcnt(3)\n"
"v_and_b32 v15, s63, v19\n"
"v_mfma_f32_16x16x32_bf16 v[168:171], a[32:35], a[144:147], v[168:171]\n"
"v_mul_f32 v12, v12, v140\n"
"v_mfma_f32_16x16x32_bf16 v[168:171], a[36:39], a[148:151], v[168:171]\n"
"v_mul_f32 v13, v13, v141\n"
"v_mfma_f32_16x16x32_bf16 v[196:199], a[44:47], a[148:151], v[196:199]\n"
"v_mul_f32 v14, v14, v142\n"
"v_mul_f32 v15, v15, v143\n"
"v_mfma_f32_16x16x32_bf16 v[196:199], a[40:43], a[144:147], v[196:199]\n"
"v_cvt_pk_bf16_f32 v30, v12, v13\n"
"v_mfma_f32_16x16x32_bf16 a[20:23], a[40:43], a[152:155], a[20:23]\n"
"v_cvt_pk_bf16_f32 v31, v14, v15\n"
"v_mfma_f32_16x16x32_bf16 a[20:23], a[44:47], a[156:159], a[20:23]\n"
"buffer_store_dwordx4 v[48:51], v6, s[52:55], s88 offen offset:0 sc1\n"
"ds_write_b64 v8, v[30:31] offset:36872\n"
"v_mfma_f32_16x16x32_bf16 a[16:19], a[36:39], a[156:159], a[16:19]\n"
"ds_read_b128 v[56:59], v2 offset:36864\n"
"v_mfma_f32_16x16x32_bf16 a[16:19], a[32:35], a[152:155], a[16:19]\n"
"s_waitcnt vmcnt(15)\n"
"ds_read_b128 v[84:87], v2 offset:37888\n"
"v_accvgpr_read_b32 v16, a184\n"
"v_mfma_f32_16x16x32_bf16 v[200:203], a[48:51], a[96:99], v[200:203]\n"
"v_accvgpr_read_b32 v17, a185\n"
"v_accvgpr_read_b32 v18, a186\n"
"v_mfma_f32_16x16x32_bf16 v[200:203], a[52:55], a[100:103], v[200:203]\n"
"v_accvgpr_read_b32 v19, a187\n"
"v_mfma_f32_16x16x32_bf16 v[228:231], a[60:63], a[100:103], v[228:231]\n"
"ds_read_b128 a[100:103], v3 offset:17408\n"
"v_lshlrev_b32 v12, 16, v16\n"
"v_mfma_f32_16x16x32_bf16 v[228:231], a[56:59], a[96:99], v[228:231]\n"
"ds_read_b128 a[96:99], v3 offset:16384\n"
"v_and_b32 v13, s63, v16\n"
"v_mfma_f32_16x16x32_bf16 v[232:235], a[56:59], a[104:107], v[232:235]\n"
"v_lshlrev_b32 v14, 16, v17\n"
"v_and_b32 v15, s63, v17\n"
"v_mfma_f32_16x16x32_bf16 v[232:235], a[60:63], a[108:111], v[232:235]\n"
"v_accvgpr_read_b32 v10, a0\n"
"v_mfma_f32_16x16x32_bf16 v[204:207], a[52:55], a[108:111], v[204:207]\n"
"ds_read_b128 a[108:111], v4 offset:17408\n"
"v_accvgpr_read_b32 v11, a1\n"
"v_mfma_f32_16x16x32_bf16 v[204:207], a[48:51], a[104:107], v[204:207]\n"
"ds_read_b128 a[104:107], v4 offset:16384\n"
"v_mul_f32 v12, v12, v10\n"
"v_mfma_f32_16x16x32_bf16 v[208:211], a[48:51], a[112:115], v[208:211]\n"
"v_mul_f32 v13, v13, v11\n"
"v_accvgpr_read_b32 v10, a2\n"
"v_mfma_f32_16x16x32_bf16 v[208:211], a[52:55], a[116:119], v[208:211]\n"
"v_accvgpr_read_b32 v11, a3\n"
"v_mul_f32 v14, v14, v10\n"
"v_mfma_f32_16x16x32_bf16 v[236:239], a[60:63], a[116:119], v[236:239]\n"
"ds_read_b128 a[116:119], v5 offset:17408\n"
"v_mul_f32 v15, v15, v11\n"
"v_mfma_f32_16x16x32_bf16 v[236:239], a[56:59], a[112:115], v[236:239]\n"
"ds_read_b128 a[112:115], v5 offset:16384\n"
"v_cvt_pk_bf16_f32 v24, v12, v13\n"
"v_mfma_f32_16x16x32_bf16 v[240:243], a[56:59], a[120:123], v[240:243]\n"
"v_cvt_pk_bf16_f32 v25, v14, v15\n"
"v_mfma_f32_16x16x32_bf16 v[240:243], a[60:63], a[124:127], v[240:243]\n"
"ds_write_b64 v7, v[24:25] offset:38912\n"
"v_mfma_f32_16x16x32_bf16 v[212:215], a[52:55], a[124:127], v[212:215]\n"
"ds_read_b128 a[124:127], v3 offset:19456\n"
"v_lshlrev_b32 v12, 16, v18\n"
"v_mfma_f32_16x16x32_bf16 v[212:215], a[48:51], a[120:123], v[212:215]\n"
"ds_read_b128 a[120:123], v3 offset:18432\n"
"v_and_b32 v13, s63, v18\n"
"v_mfma_f32_16x16x32_bf16 v[216:219], a[48:51], a[128:131], v[216:219]\n"
"v_lshlrev_b32 v14, 16, v19\n"
"v_and_b32 v15, s63, v19\n"
"v_mfma_f32_16x16x32_bf16 v[216:219], a[52:55], a[132:135], v[216:219]\n"
"v_accvgpr_read_b32 v10, a4\n"
"v_accvgpr_read_b32 v11, a5\n"
"v_mfma_f32_16x16x32_bf16 v[244:247], a[60:63], a[132:135], v[244:247]\n"
"ds_read_b128 a[132:135], v4 offset:19456\n"
"v_mul_f32 v12, v12, v10\n"
"v_mfma_f32_16x16x32_bf16 v[244:247], a[56:59], a[128:131], v[244:247]\n"
"ds_read_b128 a[128:131], v4 offset:18432\n"
"v_mul_f32 v13, v13, v11\n"
"v_mfma_f32_16x16x32_bf16 v[248:251], a[56:59], a[136:139], v[248:251]\n"
"v_accvgpr_read_b32 v10, a6\n"
"v_mfma_f32_16x16x32_bf16 v[248:251], a[60:63], a[140:143], v[248:251]\n"
"v_accvgpr_read_b32 v11, a7\n"
"v_mul_f32 v14, v14, v10\n"
"v_mfma_f32_16x16x32_bf16 v[220:223], a[52:55], a[140:143], v[220:223]\n"
"ds_read_b128 a[140:143], v5 offset:19456\n"
"v_mul_f32 v15, v15, v11\n"
"v_mfma_f32_16x16x32_bf16 v[220:223], a[48:51], a[136:139], v[220:223]\n"
"ds_read_b128 a[136:139], v5 offset:18432\n"
"v_cvt_pk_bf16_f32 v26, v12, v13\n"
"v_mfma_f32_16x16x32_bf16 v[224:227], a[48:51], a[144:147], v[224:227]\n"
"v_cvt_pk_bf16_f32 v27, v14, v15\n"
"v_mfma_f32_16x16x32_bf16 v[224:227], a[52:55], a[148:151], v[224:227]\n"
"buffer_store_dwordx4 v[76:79], v6, s[52:55], s89 offen offset:1024 sc1\n"
"ds_write_b64 v7, v[26:27] offset:38920\n"
"v_mfma_f32_16x16x32_bf16 v[252:255], a[60:63], a[148:151], v[252:255]\n"
"ds_read_b128 a[148:151], v3 offset:21504\n"
"v_accvgpr_read_b32 v16, a188\n"
"v_mfma_f32_16x16x32_bf16 v[252:255], a[56:59], a[144:147], v[252:255]\n"
"ds_read_b128 a[144:147], v3 offset:20480\n"
"v_accvgpr_read_b32 v17, a189\n"
"v_mfma_f32_16x16x32_bf16 a[28:31], a[56:59], a[152:155], a[28:31]\n"
"v_accvgpr_read_b32 v18, a190\n"
"v_accvgpr_read_b32 v19, a191\n"
"v_mfma_f32_16x16x32_bf16 a[28:31], a[60:63], a[156:159], a[28:31]\n"
"v_lshlrev_b32 v12, 16, v16\n"
"v_and_b32 v13, s63, v16\n"
"v_mfma_f32_16x16x32_bf16 a[24:27], a[52:55], a[156:159], a[24:27]\n"
"ds_read_b128 a[156:159], v4 offset:21504\n"
"v_lshlrev_b32 v14, 16, v17\n"
"v_mfma_f32_16x16x32_bf16 a[24:27], a[48:51], a[152:155], a[24:27]\n"
"s_waitcnt lgkmcnt(15)\n"
"ds_read_b128 a[152:155], v4 offset:20480\n"
"v_and_b32 v15, s63, v17\n"
"v_mfma_f32_16x16x32_bf16 v[144:147], v[88:91], a[96:99], v[144:147]\n"
"v_accvgpr_read_b32 v10, a8\n"
"v_accvgpr_read_b32 v11, a9\n"
"v_mfma_f32_16x16x32_bf16 v[144:147], v[116:119], a[100:103], v[144:147]\n"
"v_mul_f32 v12, v12, v10\n"
"v_mul_f32 v13, v13, v11\n"
"v_mfma_f32_16x16x32_bf16 v[172:175], v[120:123], a[100:103], v[172:175]\n"
"v_accvgpr_read_b32 v10, a10\n"
"v_accvgpr_read_b32 v11, a11\n"
"v_mfma_f32_16x16x32_bf16 v[172:175], v[92:95], a[96:99], v[172:175]\n"
"buffer_store_dwordx4 v[52:55], v6, s[52:55], s90 offen offset:2048 sc1\n"
"v_mul_f32 v14, v14, v10\n"
"v_mul_f32 v15, v15, v11\n"
"v_cvt_pk_bf16_f32 v28, v12, v13\n"
"v_cvt_pk_bf16_f32 v29, v14, v15\n"
"v_mfma_f32_16x16x32_bf16 v[200:203], v[32:35], a[96:99], v[200:203]\n"
"ds_write_b64 v8, v[28:29] offset:38912\n"
"v_mfma_f32_16x16x32_bf16 v[200:203], v[60:63], a[100:103], v[200:203]\n"
"v_lshlrev_b32 v12, 16, v18\n"
"v_and_b32 v13, s63, v18\n"
"v_mfma_f32_16x16x32_bf16 v[228:231], v[124:127], a[100:103], v[228:231]\n"
"ds_read_b128 a[100:103], v5 offset:21504\n"
"v_lshlrev_b32 v14, 16, v19\n"
"v_mfma_f32_16x16x32_bf16 v[228:231], v[96:99], a[96:99], v[228:231]\n"
"s_waitcnt vmcnt(3) lgkmcnt(15)\n"
"ds_read_b128 a[96:99], v5 offset:20480\n"
"v_and_b32 v15, s63, v19\n"
"v_mfma_f32_16x16x32_bf16 v[228:231], v[108:111], a[104:107], v[228:231]\n"
"v_accvgpr_read_b32 v10, a12\n"
"v_accvgpr_read_b32 v11, a13\n"
"v_mfma_f32_16x16x32_bf16 v[228:231], v[136:139], a[108:111], v[228:231]\n"
"v_mul_f32 v12, v12, v10\n"
"v_mul_f32 v13, v13, v11\n"
"v_mfma_f32_16x16x32_bf16 v[200:203], v[132:135], a[108:111], v[200:203]\n"
"v_accvgpr_read_b32 v10, a14\n"
"v_accvgpr_read_b32 v11, a15\n"
"v_mfma_f32_16x16x32_bf16 v[200:203], v[104:107], a[104:107], v[200:203]\n"
"v_mul_f32 v14, v14, v10\n"
"v_mul_f32 v15, v15, v11\n"
"v_mfma_f32_16x16x32_bf16 v[172:175], v[36:39], a[104:107], v[172:175]\n"
"v_cvt_pk_bf16_f32 v30, v12, v13\n"
"v_cvt_pk_bf16_f32 v31, v14, v15\n"
"v_mfma_f32_16x16x32_bf16 v[172:175], v[64:67], a[108:111], v[172:175]\n"
"buffer_store_dwordx4 v[80:83], v6, s[52:55], s91 offen offset:3072 sc1\n"
"ds_write_b64 v8, v[30:31] offset:38920\n"
"v_mfma_f32_16x16x32_bf16 v[144:147], v[128:131], a[108:111], v[144:147]\n"
"ds_read_b128 a[108:111], v3 offset:23552\n"
"v_mfma_f32_16x16x32_bf16 v[144:147], v[100:103], a[104:107], v[144:147]\n"
"s_waitcnt lgkmcnt(15)\n"
"ds_read_b128 a[104:107], v3 offset:22528\n"
"v_mfma_f32_16x16x32_bf16 v[144:147], a[64:67], a[112:115], v[144:147]\n"
"L_st10346:\n"
"s_cbranch_execz L_retC\n"
"v_mfma_f32_16x16x32_bf16 v[144:147], a[68:71], a[116:119], v[144:147]\n"
"v_mfma_f32_16x16x32_bf16 v[172:175], a[76:79], a[116:119], v[172:175]\n"
"v_mfma_f32_16x16x32_bf16 v[172:175], a[72:75], a[112:115], v[172:175]\n"
"ds_read_b128 v[112:115], v2 offset:38912\n"
"v_mfma_f32_16x16x32_bf16 v[200:203], a[80:83], a[112:115], v[200:203]\n"
"ds_read_b128 v[140:143], v2 offset:39936\n"
"v_mfma_f32_16x16x32_bf16 v[200:203], a[84:87], a[116:119], v[200:203]\n"
"v_accvgpr_read_b32 v16, a192\n"
"v_accvgpr_read_b32 v17, a193\n"
"v_mfma_f32_16x16x32_bf16 v[228:231], a[92:95], a[116:119], v[228:231]\n"
"ds_read_b128 a[116:119], v4 offset:23552\n"
"v_accvgpr_read_b32 v18, a194\n"
"v_mfma_f32_16x16x32_bf16 v[228:231], a[88:91], a[112:115], v[228:231]\n"
"s_waitcnt lgkmcnt(15)\n"
"buffer_store_dwordx4 v[56:59], v6, s[52:55], s92 offen offset:0 sc1\n"
"ds_read_b128 a[112:115], v4 offset:22528\n"
"v_accvgpr_read_b32 v19, a195\n"
"v_lshlrev_b32 v12, 16, v16\n"
"v_mfma_f32_16x16x32_bf16 v[232:235], v[96:99], a[120:123], v[232:235]\n"
"v_and_b32 v13, s63, v16\n"
"v_mfma_f32_16x16x32_bf16 v[232:235], v[124:127], a[124:127], v[232:235]\n"
"v_lshlrev_b32 v14, 16, v17\n"
"v_and_b32 v15, s63, v17\n"
"v_mfma_f32_16x16x32_bf16 v[204:207], v[60:63], a[124:127], v[204:207]\n"
"v_mul_f32 v12, v12, v144\n"
"v_mul_f32 v13, v13, v145\n"
"v_mfma_f32_16x16x32_bf16 v[204:207], v[32:35], a[120:123], v[204:207]\n"
"v_mul_f32 v14, v14, v146\n"
"v_mul_f32 v15, v15, v147\n"
"v_mfma_f32_16x16x32_bf16 v[176:179], v[92:95], a[120:123], v[176:179]\n"
"v_cvt_pk_bf16_f32 v24, v12, v13\n"
"v_cvt_pk_bf16_f32 v25, v14, v15\n"
"v_mfma_f32_16x16x32_bf16 v[176:179], v[120:123], a[124:127], v[176:179]\n"
"ds_write_b64 v7, v[24:25] offset:32768\n"
"v_mfma_f32_16x16x32_bf16 v[148:151], v[116:119], a[124:127], v[148:151]\n"
"ds_read_b128 a[124:127], v5 offset:23552\n"
"v_lshlrev_b32 v12, 16, v18\n"
"v_mfma_f32_16x16x32_bf16 v[148:151], v[88:91], a[120:123], v[148:151]\n"
"ds_read_b128 a[120:123], v5 offset:22528\n"
"v_and_b32 v13, s63, v18\n"
"v_mfma_f32_16x16x32_bf16 v[148:151], v[100:103], a[128:131], v[148:151]\n"
"v_lshlrev_b32 v14, 16, v19\n"
"v_mfma_f32_16x16x32_bf16 v[148:151], v[128:131], a[132:135], v[148:151]\n"
"buffer_store_dwordx4 v[84:87], v6, s[52:55], s93 offen offset:1024 sc1\n"
"v_and_b32 v15, s63, v19\n"
"v_mul_f32 v12, v12, v172\n"
"v_mul_f32 v13, v13, v173\n"
"v_mfma_f32_16x16x32_bf16 v[176:179], v[64:67], a[132:135], v[176:179]\n"
"v_mul_f32 v14, v14, v174\n"
"v_mul_f32 v15, v15, v175\n"
"v_mfma_f32_16x16x32_bf16 v[176:179], v[36:39], a[128:131], v[176:179]\n"
"v_cvt_pk_bf16_f32 v26, v12, v13\n"
"v_cvt_pk_bf16_f32 v27, v14, v15\n"
"v_mfma_f32_16x16x32_bf16 v[204:207], v[104:107], a[128:131], v[204:207]\n"
"ds_write_b64 v7, v[26:27] offset:32776\n"
"v_mfma_f32_16x16x32_bf16 v[204:207], v[132:135], a[132:135], v[204:207]\n"
"v_accvgpr_read_b32 v16, a196\n"
"v_mfma_f32_16x16x32_bf16 v[232:235], v[136:139], a[132:135], v[232:235]\n"
"ds_read_b128 a[132:135], v3 offset:25600\n"
"v_accvgpr_read_b32 v17, a197\n"
"v_mfma_f32_16x16x32_bf16 v[232:235], v[108:111], a[128:131], v[232:235]\n"
"s_waitcnt lgkmcnt(15)\n"
"ds_read_b128 a[128:131], v3 offset:24576\n"
"v_accvgpr_read_b32 v18, a198\n"
"v_mfma_f32_16x16x32_bf16 v[232:235], a[88:91], a[136:139], v[232:235]\n"
"v_accvgpr_read_b32 v19, a199\n"
"v_lshlrev_b32 v12, 16, v16\n"
"v_mfma_f32_16x16x32_bf16 v[232:235], a[92:95], a[140:143], v[232:235]\n"
"v_and_b32 v13, s63, v16\n"
"v_lshlrev_b32 v14, 16, v17\n"
"v_mfma_f32_16x16x32_bf16 v[204:207], a[84:87], a[140:143], v[204:207]\n"
"v_and_b32 v15, s63, v17\n"
"v_mul_f32 v12, v12, v200\n"
"v_mfma_f32_16x16x32_bf16 v[204:207], a[80:83], a[136:139], v[204:207]\n"
"s_waitcnt lgkmcnt(9)\n"
"buffer_store_dwordx4 v[112:115], v6, s[52:55], s94 offen offset:2048 sc1\n"
"v_mul_f32 v13, v13, v201\n"
"v_mul_f32 v14, v14, v202\n"
"v_mul_f32 v15, v15, v203\n"
"v_mfma_f32_16x16x32_bf16 v[176:179], a[72:75], a[136:139], v[176:179]\n"
"v_cvt_pk_bf16_f32 v28, v12, v13\n"
"v_cvt_pk_bf16_f32 v29, v14, v15\n"
"v_mfma_f32_16x16x32_bf16 v[176:179], a[76:79], a[140:143], v[176:179]\n"
"ds_write_b64 v8, v[28:29] offset:32768\n"
"v_mfma_f32_16x16x32_bf16 v[148:151], a[68:71], a[140:143], v[148:151]\n"
"ds_read_b128 a[140:143], v4 offset:25600\n"
"v_mfma_f32_16x16x32_bf16 v[148:151], a[64:67], a[136:139], v[148:151]\n"
"ds_read_b128 a[136:139], v4 offset:24576\n"
"v_lshlrev_b32 v12, 16, v18\n"
"v_mfma_f32_16x16x32_bf16 v[152:155], v[88:91], a[144:147], v[152:155]\n"
"v_and_b32 v13, s63, v18\n"
"v_lshlrev_b32 v14, 16, v19\n"
"v_mfma_f32_16x16x32_bf16 v[152:155], v[116:119], a[148:151], v[152:155]\n"
"v_and_b32 v15, s63, v19\n"
"v_mul_f32 v12, v12, v228\n"
"v_mfma_f32_16x16x32_bf16 v[180:183], v[120:123], a[148:151], v[180:183]\n"
"v_mul_f32 v13, v13, v229\n"
"v_mfma_f32_16x16x32_bf16 v[180:183], v[92:95], a[144:147], v[180:183]\n"
"v_mul_f32 v14, v14, v230\n"
"v_mul_f32 v15, v15, v231\n"
"v_mfma_f32_16x16x32_bf16 v[208:211], v[32:35], a[144:147], v[208:211]\n"
"v_cvt_pk_bf16_f32 v30, v12, v13\n"
"v_cvt_pk_bf16_f32 v31, v14, v15\n"
"v_mfma_f32_16x16x32_bf16 v[208:211], v[60:63], a[148:151], v[208:211]\n"
"s_waitcnt lgkmcnt(11)\n"
"buffer_store_dwordx4 v[140:143], v6, s[52:55], s95 offen offset:3072 sc1\n"
"ds_write_b64 v8, v[30:31] offset:32776\n"
"v_mfma_f32_16x16x32_bf16 v[236:239], v[124:127], a[148:151], v[236:239]\n"
"ds_read_b128 a[148:151], v5 offset:25600\n"
"v_mfma_f32_16x16x32_bf16 v[236:239], v[96:99], a[144:147], v[236:239]\n"
"ds_read_b128 a[144:147], v5 offset:24576\n"
"v_mfma_f32_16x16x32_bf16 v[236:239], v[108:111], a[152:155], v[236:239]\n"
"ds_read_b128 v[144:147], v2 offset:32768\n"
"v_mfma_f32_16x16x32_bf16 v[236:239], v[136:139], a[156:159], v[236:239]\n"
"ds_read_b128 v[172:175], v2 offset:33792\n"
"v_accvgpr_read_b32 v16, a200\n"
"v_mfma_f32_16x16x32_bf16 v[208:211], v[132:135], a[156:159], v[208:211]\n"
"v_accvgpr_read_b32 v17, a201\n"
"v_accvgpr_read_b32 v18, a202\n"
"v_mfma_f32_16x16x32_bf16 v[208:211], v[104:107], a[152:155], v[208:211]\n"
"v_accvgpr_read_b32 v19, a203\n"
"v_lshlrev_b32 v12, 16, v16\n"
"v_mfma_f32_16x16x32_bf16 v[180:183], v[36:39], a[152:155], v[180:183]\n"
"v_and_b32 v13, s63, v16\n"
"v_lshlrev_b32 v14, 16, v17\n"
"v_mfma_f32_16x16x32_bf16 v[180:183], v[64:67], a[156:159], v[180:183]\n"
"v_and_b32 v15, s63, v17\n"
"v_mul_f32 v12, v12, v148\n"
"v_mfma_f32_16x16x32_bf16 v[152:155], v[128:131], a[156:159], v[152:155]\n"
"ds_read_b128 a[156:159], v3 offset:27648\n"
"v_mul_f32 v13, v13, v149\n"
"v_mfma_f32_16x16x32_bf16 v[152:155], v[100:103], a[152:155], v[152:155]\n"
"ds_read_b128 a[152:155], v3 offset:26624\n"
"v_mul_f32 v14, v14, v150\n"
"v_mfma_f32_16x16x32_bf16 v[152:155], a[64:67], a[96:99], v[152:155]\n"
"v_mul_f32 v15, v15, v151\n"
"v_cvt_pk_bf16_f32 v24, v12, v13\n"
"v_mfma_f32_16x16x32_bf16 v[152:155], a[68:71], a[100:103], v[152:155]\n"
"v_cvt_pk_bf16_f32 v25, v14, v15\n"
"v_mfma_f32_16x16x32_bf16 v[180:183], a[76:79], a[100:103], v[180:183]\n"
"s_waitcnt lgkmcnt(3)\n"
"buffer_store_dwordx4 v[144:147], v6, s[52:55], s80 offen offset:128 sc1\n"
"ds_write_b64 v7, v[24:25] offset:34816\n"
"v_mfma_f32_16x16x32_bf16 v[180:183], a[72:75], a[96:99], v[180:183]\n"
"v_lshlrev_b32 v12, 16, v18\n"
"v_and_b32 v13, s63, v18\n"
"v_mfma_f32_16x16x32_bf16 v[208:211], a[80:83], a[96:99], v[208:211]\n"
"v_lshlrev_b32 v14, 16, v19\n"
"v_and_b32 v15, s63, v19\n"
"v_mfma_f32_16x16x32_bf16 v[208:211], a[84:87], a[100:103], v[208:211]\n"
"v_mul_f32 v12, v12, v176\n"
"v_mul_f32 v13, v13, v177\n"
"v_mfma_f32_16x16x32_bf16 v[236:239], a[92:95], a[100:103], v[236:239]\n"
"ds_read_b128 a[100:103], v4 offset:27648\n"
"v_mul_f32 v14, v14, v178\n"
"v_mfma_f32_16x16x32_bf16 v[236:239], a[88:91], a[96:99], v[236:239]\n"
"ds_read_b128 a[96:99], v4 offset:26624\n"
"v_mul_f32 v15, v15, v179\n"
"v_mfma_f32_16x16x32_bf16 v[240:243], v[96:99], a[104:107], v[240:243]\n"
"v_cvt_pk_bf16_f32 v26, v12, v13\n"
"v_cvt_pk_bf16_f32 v27, v14, v15\n"
"v_mfma_f32_16x16x32_bf16 v[240:243], v[124:127], a[108:111], v[240:243]\n"
"ds_write_b64 v7, v[26:27] offset:34824\n"
"v_mfma_f32_16x16x32_bf16 v[212:215], v[60:63], a[108:111], v[212:215]\n"
"v_accvgpr_read_b32 v16, a204\n"
"v_accvgpr_read_b32 v17, a205\n"
"v_mfma_f32_16x16x32_bf16 v[212:215], v[32:35], a[104:107], v[212:215]\n"
"v_accvgpr_read_b32 v18, a206\n"
"v_accvgpr_read_b32 v19, a207\n"
"v_mfma_f32_16x16x32_bf16 v[184:187], v[92:95], a[104:107], v[184:187]\n"
"s_waitcnt lgkmcnt(6)\n"
"buffer_store_dwordx4 v[172:175], v6, s[52:55], s81 offen offset:1152 sc1\n"
"v_lshlrev_b32 v12, 16, v16\n"
"v_and_b32 v13, s63, v16\n"
"v_lshlrev_b32 v14, 16, v17\n"
"v_mfma_f32_16x16x32_bf16 v[184:187], v[120:123], a[108:111], v[184:187]\n"
"v_and_b32 v15, s63, v17\n"
"v_mul_f32 v12, v12, v204\n"
"v_mfma_f32_16x16x32_bf16 v[156:159], v[116:119], a[108:111], v[156:159]\n"
"ds_read_b128 a[108:111], v5 offset:27648\n"
"v_mul_f32 v13, v13, v205\n"
"v_mfma_f32_16x16x32_bf16 v[156:159], v[88:91], a[104:107], v[156:159]\n"
"ds_read_b128 a[104:107], v5 offset:26624\n"
"v_mul_f32 v14, v14, v206\n"
"v_mfma_f32_16x16x32_bf16 v[156:159], v[100:103], a[112:115], v[156:159]\n"
"v_mul_f32 v15, v15, v207\n"
"v_cvt_pk_bf16_f32 v28, v12, v13\n"
"v_mfma_f32_16x16x32_bf16 v[156:159], v[128:131], a[116:119], v[156:159]\n"
"v_cvt_pk_bf16_f32 v29, v14, v15\n"
"v_mfma_f32_16x16x32_bf16 v[184:187], v[64:67], a[116:119], v[184:187]\n"
"ds_write_b64 v8, v[28:29] offset:34816\n"
"v_mfma_f32_16x16x32_bf16 v[184:187], v[36:39], a[112:115], v[184:187]\n"
"v_lshlrev_b32 v12, 16, v18\n"
"v_and_b32 v13, s63, v18\n"
"v_mfma_f32_16x16x32_bf16 v[212:215], v[104:107], a[112:115], v[212:215]\n"
"v_lshlrev_b32 v14, 16, v19\n"
"v_and_b32 v15, s63, v19\n"
"v_mfma_f32_16x16x32_bf16 v[212:215], v[132:135], a[116:119], v[212:215]\n"
"v_mul_f32 v12, v12, v232\n"
"v_mul_f32 v13, v13, v233\n"
"v_mfma_f32_16x16x32_bf16 v[240:243], v[136:139], a[116:119], v[240:243]\n"
"ds_read_b128 a[116:119], v3 offset:29696\n"
"v_mul_f32 v14, v14, v234\n"
"v_mfma_f32_16x16x32_bf16 v[240:243], v[108:111], a[112:115], v[240:243]\n"
"ds_read_b128 a[112:115], v3 offset:28672\n"
"v_mul_f32 v15, v15, v235\n"
"v_mfma_f32_16x16x32_bf16 v[240:243], a[88:91], a[120:123], v[240:243]\n"
"v_cvt_pk_bf16_f32 v30, v12, v13\n"
"v_cvt_pk_bf16_f32 v31, v14, v15\n"
"v_mfma_f32_16x16x32_bf16 v[240:243], a[92:95], a[124:127], v[240:243]\n"
"ds_write_b64 v8, v[30:31] offset:34824\n"
"v_mfma_f32_16x16x32_bf16 v[212:215], a[84:87], a[124:127], v[212:215]\n"
"ds_read_b128 v[148:151], v2 offset:34816\n"
"v_mfma_f32_16x16x32_bf16 v[212:215], a[80:83], a[120:123], v[212:215]\n"
"ds_read_b128 v[176:179], v2 offset:35840\n"
"v_accvgpr_read_b32 v16, a208\n"
"v_mfma_f32_16x16x32_bf16 v[184:187], a[72:75], a[120:123], v[184:187]\n"
"v_accvgpr_read_b32 v17, a209\n"
"v_accvgpr_read_b32 v18, a210\n"
"v_mfma_f32_16x16x32_bf16 v[184:187], a[76:79], a[124:127], v[184:187]\n"
"v_accvgpr_read_b32 v19, a211\n"
"v_lshlrev_b32 v12, 16, v16\n"
"v_mfma_f32_16x16x32_bf16 v[156:159], a[68:71], a[124:127], v[156:159]\n"
"ds_read_b128 a[124:127], v4 offset:29696\n"
"v_and_b32 v13, s63, v16\n"
"v_mfma_f32_16x16x32_bf16 v[156:159], a[64:67], a[120:123], v[156:159]\n"
"ds_read_b128 a[120:123], v4 offset:28672\n"
"v_lshlrev_b32 v14, 16, v17\n"
"v_mfma_f32_16x16x32_bf16 v[160:163], v[88:91], a[128:131], v[160:163]\n"
"v_and_b32 v15, s63, v17\n"
"v_mul_f32 v12, v12, v152\n"
"v_mfma_f32_16x16x32_bf16 v[160:163], v[116:119], a[132:135], v[160:163]\n"
"v_mul_f32 v13, v13, v153\n"
"v_mul_f32 v14, v14, v154\n"
"v_mfma_f32_16x16x32_bf16 v[188:191], v[120:123], a[132:135], v[188:191]\n"
"v_mul_f32 v15, v15, v155\n"
"v_cvt_pk_bf16_f32 v24, v12, v13\n"
"v_mfma_f32_16x16x32_bf16 v[188:191], v[92:95], a[128:131], v[188:191]\n"
"v_cvt_pk_bf16_f32 v25, v14, v15\n"
"v_mfma_f32_16x16x32_bf16 v[216:219], v[32:35], a[128:131], v[216:219]\n"
"s_waitcnt lgkmcnt(3)\n"
"buffer_store_dwordx4 v[148:151], v6, s[52:55], s82 offen offset:2176 sc1\n"
"ds_write_b64 v7, v[24:25] offset:36864\n"
"v_mfma_f32_16x16x32_bf16 v[216:219], v[60:63], a[132:135], v[216:219]\n"
"v_lshlrev_b32 v12, 16, v18\n"
"v_and_b32 v13, s63, v18\n"
"v_mfma_f32_16x16x32_bf16 v[244:247], v[124:127], a[132:135], v[244:247]\n"
"ds_read_b128 a[132:135], v5 offset:29696\n"
"v_lshlrev_b32 v14, 16, v19\n"
"v_mfma_f32_16x16x32_bf16 v[244:247], v[96:99], a[128:131], v[244:247]\n"
"ds_read_b128 a[128:131], v5 offset:28672\n"
"v_and_b32 v15, s63, v19\n"
"v_mfma_f32_16x16x32_bf16 v[244:247], v[108:111], a[136:139], v[244:247]\n"
"v_mul_f32 v12, v12, v180\n"
"v_mul_f32 v13, v13, v181\n"
"v_mfma_f32_16x16x32_bf16 v[244:247], v[136:139], a[140:143], v[244:247]\n"
"v_mul_f32 v14, v14, v182\n"
"v_mul_f32 v15, v15, v183\n"
"v_mfma_f32_16x16x32_bf16 v[216:219], v[132:135], a[140:143], v[216:219]\n"
"v_cvt_pk_bf16_f32 v26, v12, v13\n"
"v_cvt_pk_bf16_f32 v27, v14, v15\n"
"v_mfma_f32_16x16x32_bf16 v[216:219], v[104:107], a[136:139], v[216:219]\n"
"ds_write_b64 v7, v[26:27] offset:36872\n"
"v_mfma_f32_16x16x32_bf16 v[188:191], v[36:39], a[136:139], v[188:191]\n"
"v_accvgpr_read_b32 v16, a212\n"
"v_accvgpr_read_b32 v17, a213\n"
"v_mfma_f32_16x16x32_bf16 v[188:191], v[64:67], a[140:143], v[188:191]\n"
"v_accvgpr_read_b32 v18, a214\n"
"v_accvgpr_read_b32 v19, a215\n"
"v_mfma_f32_16x16x32_bf16 v[160:163], v[128:131], a[140:143], v[160:163]\n"
"s_waitcnt lgkmcnt(6)\n"
"buffer_store_dwordx4 v[176:179], v6, s[52:55], s83 offen offset:3200 sc1\n"
"ds_read_b128 a[140:143], v3 offset:31744\n"
"v_lshlrev_b32 v12, 16, v16\n"
"v_and_b32 v13, s63, v16\n"
"v_mfma_f32_16x16x32_bf16 v[160:163], v[100:103], a[136:139], v[160:163]\n"
"ds_read_b128 a[136:139], v3 offset:30720\n"
"v_lshlrev_b32 v14, 16, v17\n"
"v_mfma_f32_16x16x32_bf16 v[160:163], a[64:67], a[144:147], v[160:163]\n"
"v_and_b32 v15, s63, v17\n"
"v_mul_f32 v12, v12, v208\n"
"v_mfma_f32_16x16x32_bf16 v[160:163], a[68:71], a[148:151], v[160:163]\n"
"v_mul_f32 v13, v13, v209\n"
"v_mul_f32 v14, v14, v210\n"
"v_mfma_f32_16x16x32_bf16 v[188:191], a[76:79], a[148:151], v[188:191]\n"
"v_mul_f32 v15, v15, v211\n"
"v_cvt_pk_bf16_f32 v28, v12, v13\n"
"v_mfma_f32_16x16x32_bf16 v[188:191], a[72:75], a[144:147], v[188:191]\n"
"v_cvt_pk_bf16_f32 v29, v14, v15\n"
"v_mfma_f32_16x16x32_bf16 v[216:219], a[80:83], a[144:147], v[216:219]\n"
"ds_write_b64 v8, v[28:29] offset:36864\n"
"v_mfma_f32_16x16x32_bf16 v[216:219], a[84:87], a[148:151], v[216:219]\n"
"v_lshlrev_b32 v12, 16, v18\n"
"v_and_b32 v13, s63, v18\n"
"v_mfma_f32_16x16x32_bf16 v[244:247], a[92:95], a[148:151], v[244:247]\n"
"ds_read_b128 a[148:151], v4 offset:31744\n"
"v_lshlrev_b32 v14, 16, v19\n"
"v_mfma_f32_16x16x32_bf16 v[244:247], a[88:91], a[144:147], v[244:247]\n"
"ds_read_b128 a[144:147], v4 offset:30720\n"
"v_and_b32 v15, s63, v19\n"
"v_mfma_f32_16x16x32_bf16 v[248:251], v[96:99], a[152:155], v[248:251]\n"
"v_mul_f32 v12, v12, v236\n"
"v_mul_f32 v13, v13, v237\n"
"v_mfma_f32_16x16x32_bf16 v[248:251], v[124:127], a[156:159], v[248:251]\n"
"v_mul_f32 v14, v14, v238\n"
"v_mul_f32 v15, v15, v239\n"
"v_mfma_f32_16x16x32_bf16 v[220:223], v[60:63], a[156:159], v[220:223]\n"
"v_cvt_pk_bf16_f32 v30, v12, v13\n"
"v_cvt_pk_bf16_f32 v31, v14, v15\n"
"v_mfma_f32_16x16x32_bf16 v[220:223], v[32:35], a[152:155], v[220:223]\n"
"ds_write_b64 v8, v[30:31] offset:36872\n"
"v_mfma_f32_16x16x32_bf16 v[192:195], v[92:95], a[152:155], v[192:195]\n"
"ds_read_b128 v[152:155], v2 offset:36864\n"
"v_mfma_f32_16x16x32_bf16 v[192:195], v[120:123], a[156:159], v[192:195]\n"
"ds_read_b128 v[180:183], v2 offset:37888\n"
"v_accvgpr_read_b32 v16, a216\n"
"v_mfma_f32_16x16x32_bf16 v[164:167], v[116:119], a[156:159], v[164:167]\n"
"ds_read_b128 a[156:159], v5 offset:31744\n"
"v_accvgpr_read_b32 v17, a217\n"
"v_mfma_f32_16x16x32_bf16 v[164:167], v[88:91], a[152:155], v[164:167]\n"
"ds_read_b128 a[152:155], v5 offset:30720\n"
"v_accvgpr_read_b32 v18, a218\n"
"v_mfma_f32_16x16x32_bf16 v[164:167], v[100:103], a[96:99], v[164:167]\n"
"v_accvgpr_read_b32 v19, a219\n"
"v_lshlrev_b32 v12, 16, v16\n"
"v_mfma_f32_16x16x32_bf16 v[164:167], v[128:131], a[100:103], v[164:167]\n"
"v_and_b32 v13, s63, v16\n"
"v_lshlrev_b32 v14, 16, v17\n"
"v_mfma_f32_16x16x32_bf16 v[192:195], v[64:67], a[100:103], v[192:195]\n"
"v_and_b32 v15, s63, v17\n"
"v_mul_f32 v12, v12, v156\n"
"v_mfma_f32_16x16x32_bf16 v[192:195], v[36:39], a[96:99], v[192:195]\n"
"v_mul_f32 v13, v13, v157\n"
"v_mul_f32 v14, v14, v158\n"
"v_mfma_f32_16x16x32_bf16 v[220:223], v[104:107], a[96:99], v[220:223]\n"
"v_mul_f32 v15, v15, v159\n"
"v_mfma_f32_16x16x32_bf16 v[220:223], v[132:135], a[100:103], v[220:223]\n"
"v_cvt_pk_bf16_f32 v24, v12, v13\n"
"v_mfma_f32_16x16x32_bf16 v[248:251], v[136:139], a[100:103], v[248:251]\n"
"v_cvt_pk_bf16_f32 v25, v14, v15\n"
"v_mfma_f32_16x16x32_bf16 v[248:251], v[108:111], a[96:99], v[248:251]\n"
"s_waitcnt lgkmcnt(3)\n"
"buffer_store_dwordx4 v[152:155], v6, s[52:55], s84 offen offset:128 sc1\n"
"ds_write_b64 v7, v[24:25] offset:38912\n"
"v_mfma_f32_16x16x32_bf16 v[248:251], a[88:91], a[104:107], v[248:251]\n"
"v_lshlrev_b32 v12, 16, v18\n"
"v_and_b32 v13, s63, v18\n"
"v_mfma_f32_16x16x32_bf16 v[248:251], a[92:95], a[108:111], v[248:251]\n"
"v_lshlrev_b32 v14, 16, v19\n"
"v_mfma_f32_16x16x32_bf16 v[220:223], a[84:87], a[108:111], v[220:223]\n"
"v_and_b32 v15, s63, v19\n"
"v_mfma_f32_16x16x32_bf16 v[220:223], a[80:83], a[104:107], v[220:223]\n"
"v_mul_f32 v12, v12, v184\n"
"v_mfma_f32_16x16x32_bf16 v[192:195], a[72:75], a[104:107], v[192:195]\n"
"v_mul_f32 v13, v13, v185\n"
"v_mfma_f32_16x16x32_bf16 v[192:195], a[76:79], a[108:111], v[192:195]\n"
"v_mul_f32 v14, v14, v186\n"
"v_mfma_f32_16x16x32_bf16 v[164:167], a[68:71], a[108:111], v[164:167]\n"
"v_mul_f32 v15, v15, v187\n"
"v_mfma_f32_16x16x32_bf16 v[164:167], a[64:67], a[104:107], v[164:167]\n"
"v_cvt_pk_bf16_f32 v26, v12, v13\n"
"v_mfma_f32_16x16x32_bf16 v[168:171], v[88:91], a[112:115], v[168:171]\n"
"v_cvt_pk_bf16_f32 v27, v14, v15\n"
"v_mfma_f32_16x16x32_bf16 v[168:171], v[116:119], a[116:119], v[168:171]\n"
"s_waitcnt lgkmcnt(3)\n"
"buffer_store_dwordx4 v[180:183], v6, s[52:55], s85 offen offset:1152 sc1\n"
"ds_write_b64 v7, v[26:27] offset:38920\n"
"v_mfma_f32_16x16x32_bf16 v[196:199], v[120:123], a[116:119], v[196:199]\n"
"v_accvgpr_read_b32 v16, a220\n"
"v_accvgpr_read_b32 v17, a221\n"
"v_mfma_f32_16x16x32_bf16 v[196:199], v[92:95], a[112:115], v[196:199]\n"
"v_accvgpr_read_b32 v18, a222\n"
"v_accvgpr_read_b32 v19, a223\n"
"v_mfma_f32_16x16x32_bf16 v[224:227], v[32:35], a[112:115], v[224:227]\n"
"v_lshlrev_b32 v12, 16, v16\n"
"v_and_b32 v13, s63, v16\n"
"v_mfma_f32_16x16x32_bf16 v[224:227], v[60:63], a[116:119], v[224:227]\n"
"v_lshlrev_b32 v14, 16, v17\n"
"v_and_b32 v15, s63, v17\n"
"v_mfma_f32_16x16x32_bf16 v[252:255], v[124:127], a[116:119], v[252:255]\n"
"v_mul_f32 v12, v12, v212\n"
"v_mul_f32 v13, v13, v213\n"
"v_mfma_f32_16x16x32_bf16 v[252:255], v[96:99], a[112:115], v[252:255]\n"
"v_mul_f32 v14, v14, v214\n"
"v_mul_f32 v15, v15, v215\n"
"v_mfma_f32_16x16x32_bf16 v[252:255], v[108:111], a[120:123], v[252:255]\n"
"v_cvt_pk_bf16_f32 v28, v12, v13\n"
"v_cvt_pk_bf16_f32 v29, v14, v15\n"
"v_mfma_f32_16x16x32_bf16 v[252:255], v[136:139], a[124:127], v[252:255]\n"
"ds_write_b64 v8, v[28:29] offset:38912\n"
"v_mfma_f32_16x16x32_bf16 v[224:227], v[132:135], a[124:127], v[224:227]\n"
"v_lshlrev_b32 v12, 16, v18\n"
"v_and_b32 v13, s63, v18\n"
"v_mfma_f32_16x16x32_bf16 v[224:227], v[104:107], a[120:123], v[224:227]\n"
"v_lshlrev_b32 v14, 16, v19\n"
"v_and_b32 v15, s63, v19\n"
"v_mfma_f32_16x16x32_bf16 v[196:199], v[36:39], a[120:123], v[196:199]\n"
"v_mul_f32 v12, v12, v240\n"
"v_mul_f32 v13, v13, v241\n"
"v_mfma_f32_16x16x32_bf16 v[196:199], v[64:67], a[124:127], v[196:199]\n"
"v_mul_f32 v14, v14, v242\n"
"v_mul_f32 v15, v15, v243\n"
"v_mfma_f32_16x16x32_bf16 v[168:171], v[128:131], a[124:127], v[168:171]\n"
"v_cvt_pk_bf16_f32 v30, v12, v13\n"
"v_cvt_pk_bf16_f32 v31, v14, v15\n"
"v_mfma_f32_16x16x32_bf16 v[168:171], v[100:103], a[120:123], v[168:171]\n"
"ds_write_b64 v8, v[30:31] offset:38920\n"
"v_mfma_f32_16x16x32_bf16 v[168:171], a[64:67], a[128:131], v[168:171]\n"
"ds_read_b128 v[156:159], v2 offset:38912\n"
"v_mfma_f32_16x16x32_bf16 v[168:171], a[68:71], a[132:135], v[168:171]\n"
"ds_read_b128 v[184:187], v2 offset:39936\n"
"v_accvgpr_read_b32 v16, a224\n"
"v_mfma_f32_16x16x32_bf16 v[196:199], a[76:79], a[132:135], v[196:199]\n"
"v_accvgpr_read_b32 v17, a225\n"
"v_accvgpr_read_b32 v18, a226\n"
"v_mfma_f32_16x16x32_bf16 v[196:199], a[72:75], a[128:131], v[196:199]\n"
"v_accvgpr_read_b32 v19, a227\n"
"v_lshlrev_b32 v12, 16, v16\n"
"v_mfma_f32_16x16x32_bf16 v[224:227], a[80:83], a[128:131], v[224:227]\n"
"v_and_b32 v13, s63, v16\n"
"v_lshlrev_b32 v14, 16, v17\n"
"v_mfma_f32_16x16x32_bf16 v[224:227], a[84:87], a[132:135], v[224:227]\n"
"v_and_b32 v15, s63, v17\n"
"v_mul_f32 v12, v12, v160\n"
"v_mfma_f32_16x16x32_bf16 v[252:255], a[92:95], a[132:135], v[252:255]\n"
"v_mul_f32 v13, v13, v161\n"
"v_mul_f32 v14, v14, v162\n"
"v_mfma_f32_16x16x32_bf16 v[252:255], a[88:91], a[128:131], v[252:255]\n"
"v_mul_f32 v15, v15, v163\n"
"v_cvt_pk_bf16_f32 v24, v12, v13\n"
"v_mfma_f32_16x16x32_bf16 a[28:31], v[96:99], a[136:139], a[28:31]\n"
"v_cvt_pk_bf16_f32 v25, v14, v15\n"
"v_mfma_f32_16x16x32_bf16 a[28:31], v[124:127], a[140:143], a[28:31]\n"
"s_waitcnt lgkmcnt(1)\n"
"buffer_store_dwordx4 v[156:159], v6, s[52:55], s86 offen offset:2176 sc1\n"
"ds_write_b64 v7, v[24:25] offset:32768\n"
"v_mfma_f32_16x16x32_bf16 a[24:27], v[60:63], a[140:143], a[24:27]\n"
"v_lshlrev_b32 v12, 16, v18\n"
"v_and_b32 v13, s63, v18\n"
"v_mfma_f32_16x16x32_bf16 a[24:27], v[32:35], a[136:139], a[24:27]\n"
"v_lshlrev_b32 v14, 16, v19\n"
"v_and_b32 v15, s63, v19\n"
"v_mfma_f32_16x16x32_bf16 a[20:23], v[92:95], a[136:139], a[20:23]\n"
"v_mul_f32 v12, v12, v188\n"
"v_mul_f32 v13, v13, v189\n"
"v_mfma_f32_16x16x32_bf16 a[20:23], v[120:123], a[140:143], a[20:23]\n"
"v_mul_f32 v14, v14, v190\n"
"v_mul_f32 v15, v15, v191\n"
"v_mfma_f32_16x16x32_bf16 a[16:19], v[116:119], a[140:143], a[16:19]\n"
"v_cvt_pk_bf16_f32 v26, v12, v13\n"
"v_cvt_pk_bf16_f32 v27, v14, v15\n"
"v_mfma_f32_16x16x32_bf16 a[16:19], v[88:91], a[136:139], a[16:19]\n"
"ds_write_b64 v7, v[26:27] offset:32776\n"
"v_mfma_f32_16x16x32_bf16 a[16:19], v[100:103], a[144:147], a[16:19]\n"
"v_accvgpr_read_b32 v16, a228\n"
"v_accvgpr_read_b32 v17, a229\n"
"v_mfma_f32_16x16x32_bf16 a[16:19], v[128:131], a[148:151], a[16:19]\n"
"v_accvgpr_read_b32 v18, a230\n"
"v_accvgpr_read_b32 v19, a231\n"
"v_mfma_f32_16x16x32_bf16 a[20:23], v[64:67], a[148:151], a[20:23]\n"
"v_lshlrev_b32 v12, 16, v16\n"
"v_and_b32 v13, s63, v16\n"
"v_mfma_f32_16x16x32_bf16 a[20:23], v[36:39], a[144:147], a[20:23]\n"
"s_waitcnt lgkmcnt(2)\n"
"buffer_store_dwordx4 v[184:187], v6, s[52:55], s87 offen offset:3200 sc1\n"
"v_lshlrev_b32 v14, 16, v17\n"
"v_and_b32 v15, s63, v17\n"
"v_mul_f32 v12, v12, v216\n"
"v_mfma_f32_16x16x32_bf16 a[24:27], v[104:107], a[144:147], a[24:27]\n"
"v_mul_f32 v13, v13, v217\n"
"v_mul_f32 v14, v14, v218\n"
"v_mfma_f32_16x16x32_bf16 a[24:27], v[132:135], a[148:151], a[24:27]\n"
"v_mul_f32 v15, v15, v219\n"
"v_mfma_f32_16x16x32_bf16 a[28:31], v[136:139], a[148:151], a[28:31]\n"
"v_cvt_pk_bf16_f32 v28, v12, v13\n"
"v_cvt_pk_bf16_f32 v29, v14, v15\n"
"v_mfma_f32_16x16x32_bf16 a[28:31], v[108:111], a[144:147], a[28:31]\n"
"ds_write_b64 v8, v[28:29] offset:32768\n"
"v_mfma_f32_16x16x32_bf16 a[28:31], a[88:91], a[152:155], a[28:31]\n"
"v_lshlrev_b32 v12, 16, v18\n"
"v_and_b32 v13, s63, v18\n"
"v_mfma_f32_16x16x32_bf16 a[28:31], a[92:95], a[156:159], a[28:31]\n"
"v_lshlrev_b32 v14, 16, v19\n"
"v_and_b32 v15, s63, v19\n"
"v_mfma_f32_16x16x32_bf16 a[24:27], a[84:87], a[156:159], a[24:27]\n"
"v_mul_f32 v12, v12, v244\n"
"v_mul_f32 v13, v13, v245\n"
"v_mfma_f32_16x16x32_bf16 a[24:27], a[80:83], a[152:155], a[24:27]\n"
"v_mul_f32 v14, v14, v246\n"
"v_mul_f32 v15, v15, v247\n"
"v_mfma_f32_16x16x32_bf16 a[20:23], a[72:75], a[152:155], a[20:23]\n"
"s_add_u32 s62, s62, 1\n"
"v_cvt_pk_bf16_f32 v30, v12, v13\n"
"v_cvt_pk_bf16_f32 v31, v14, v15\n"
"v_mfma_f32_16x16x32_bf16 a[20:23], a[76:79], a[156:159], a[20:23]\n"
"ds_write_b64 v8, v[30:31] offset:32776\n"
"v_mfma_f32_16x16x32_bf16 a[16:19], a[68:71], a[156:159], a[16:19]\n"
"ds_read_b128 v[160:163], v2 offset:32768\n"
"v_mfma_f32_16x16x32_bf16 a[16:19], a[64:67], a[152:155], a[16:19]\n"
"ds_read_b128 v[188:191], v2 offset:33792\n"
"s_nop 7\n"
"v_accvgpr_read_b32 v16, a232\n"
"v_accvgpr_read_b32 v17, a233\n"
"v_accvgpr_read_b32 v18, a234\n"
"v_accvgpr_read_b32 v19, a235\n"
"v_lshlrev_b32 v12, 16, v16\n"
"v_and_b32 v13, s63, v16\n"
"v_lshlrev_b32 v14, 16, v17\n"
"v_and_b32 v15, s63, v17\n"
"v_pk_mul_f32 v[12:13], v[12:13], v[164:165]\n"
"v_pk_mul_f32 v[14:15], v[14:15], v[166:167]\n"
"v_cvt_pk_bf16_f32 v24, v12, v13\n"
"v_cvt_pk_bf16_f32 v25, v14, v15\n"
"v_lshlrev_b32 v12, 16, v18\n"
"v_and_b32 v13, s63, v18\n"
"v_lshlrev_b32 v14, 16, v19\n"
"v_and_b32 v15, s63, v19\n"
"v_pk_mul_f32 v[12:13], v[12:13], v[192:193]\n"
"v_pk_mul_f32 v[14:15], v[14:15], v[194:195]\n"
"v_cvt_pk_bf16_f32 v26, v12, v13\n"
"v_cvt_pk_bf16_f32 v27, v14, v15\n"
"ds_write_b128 v7, v[24:27] offset:34816\n"
"s_waitcnt lgkmcnt(2)\n"
"buffer_store_dwordx4 v[160:163], v6, s[52:55], s88 offen offset:128 sc1\n"
"s_waitcnt lgkmcnt(1)\n"
"buffer_store_dwordx4 v[188:191], v6, s[52:55], s89 offen offset:1152 sc1\n"
"v_accvgpr_read_b32 v16, a236\n"
"v_accvgpr_read_b32 v17, a237\n"
"v_accvgpr_read_b32 v18, a238\n"
"v_accvgpr_read_b32 v19, a239\n"
"v_lshlrev_b32 v12, 16, v16\n"
"v_and_b32 v13, s63, v16\n"
"v_lshlrev_b32 v14, 16, v17\n"
"v_and_b32 v15, s63, v17\n"
"v_pk_mul_f32 v[12:13], v[12:13], v[220:221]\n"
"v_pk_mul_f32 v[14:15], v[14:15], v[222:223]\n"
"v_cvt_pk_bf16_f32 v28, v12, v13\n"
"v_cvt_pk_bf16_f32 v29, v14, v15\n"
"v_lshlrev_b32 v12, 16, v18\n"
"v_and_b32 v13, s63, v18\n"
"v_lshlrev_b32 v14, 16, v19\n"
"v_and_b32 v15, s63, v19\n"
"v_pk_mul_f32 v[12:13], v[12:13], v[248:249]\n"
"v_pk_mul_f32 v[14:15], v[14:15], v[250:251]\n"
"v_cvt_pk_bf16_f32 v30, v12, v13\n"
"v_cvt_pk_bf16_f32 v31, v14, v15\n"
"ds_write_b128 v8, v[28:31] offset:34816\n"
"ds_read_b128 v[164:167], v2 offset:34816\n"
"ds_read_b128 v[192:195], v2 offset:35840\n"
"v_accvgpr_read_b32 v16, a240\n"
"v_accvgpr_read_b32 v17, a241\n"
"v_accvgpr_read_b32 v18, a242\n"
"v_accvgpr_read_b32 v19, a243\n"
"v_lshlrev_b32 v12, 16, v16\n"
"v_and_b32 v13, s63, v16\n"
"v_lshlrev_b32 v14, 16, v17\n"
"v_and_b32 v15, s63, v17\n"
"v_pk_mul_f32 v[12:13], v[12:13], v[168:169]\n"
"v_pk_mul_f32 v[14:15], v[14:15], v[170:171]\n"
"v_cvt_pk_bf16_f32 v24, v12, v13\n"
"v_cvt_pk_bf16_f32 v25, v14, v15\n"
"v_lshlrev_b32 v12, 16, v18\n"
"v_and_b32 v13, s63, v18\n"
"v_lshlrev_b32 v14, 16, v19\n"
"v_and_b32 v15, s63, v19\n"
"v_pk_mul_f32 v[12:13], v[12:13], v[196:197]\n"
"v_pk_mul_f32 v[14:15], v[14:15], v[198:199]\n"
"v_cvt_pk_bf16_f32 v26, v12, v13\n"
"v_cvt_pk_bf16_f32 v27, v14, v15\n"
"ds_write_b128 v7, v[24:27] offset:36864\n"
"s_waitcnt lgkmcnt(2)\n"
"buffer_store_dwordx4 v[164:167], v6, s[52:55], s90 offen offset:2176 sc1\n"
"s_waitcnt lgkmcnt(1)\n"
"buffer_store_dwordx4 v[192:195], v6, s[52:55], s91 offen offset:3200 sc1\n"
"v_accvgpr_read_b32 v16, a244\n"
"v_accvgpr_read_b32 v17, a245\n"
"v_accvgpr_read_b32 v18, a246\n"
"v_accvgpr_read_b32 v19, a247\n"
"v_lshlrev_b32 v12, 16, v16\n"
"v_and_b32 v13, s63, v16\n"
"v_lshlrev_b32 v14, 16, v17\n"
"v_and_b32 v15, s63, v17\n"
"v_pk_mul_f32 v[12:13], v[12:13], v[224:225]\n"
"v_pk_mul_f32 v[14:15], v[14:15], v[226:227]\n"
"v_cvt_pk_bf16_f32 v28, v12, v13\n"
"v_cvt_pk_bf16_f32 v29, v14, v15\n"
"v_lshlrev_b32 v12, 16, v18\n"
"v_and_b32 v13, s63, v18\n"
"v_lshlrev_b32 v14, 16, v19\n"
"v_and_b32 v15, s63, v19\n"
"v_pk_mul_f32 v[12:13], v[12:13], v[252:253]\n"
"v_pk_mul_f32 v[14:15], v[14:15], v[254:255]\n"
"v_cvt_pk_bf16_f32 v30, v12, v13\n"
"v_cvt_pk_bf16_f32 v31, v14, v15\n"
"ds_write_b128 v8, v[28:31] offset:36864\n"
"ds_read_b128 v[168:171], v2 offset:36864\n"
"ds_read_b128 v[196:199], v2 offset:37888\n"
"v_accvgpr_read_b32 v16, a248\n"
"v_accvgpr_read_b32 v17, a249\n"
"v_accvgpr_read_b32 v18, a250\n"
"v_accvgpr_read_b32 v19, a251\n"
"v_lshlrev_b32 v12, 16, v16\n"
"v_and_b32 v13, s63, v16\n"
"v_lshlrev_b32 v14, 16, v17\n"
"v_and_b32 v15, s63, v17\n"
"v_accvgpr_read_b32 v10, a16\n"
"v_accvgpr_read_b32 v11, a17\n"
"v_mul_f32 v12, v12, v10\n"
"v_mul_f32 v13, v13, v11\n"
"v_accvgpr_read_b32 v10, a18\n"
"v_accvgpr_read_b32 v11, a19\n"
"v_mul_f32 v14, v14, v10\n"
"v_mul_f32 v15, v15, v11\n"
"v_cvt_pk_bf16_f32 v24, v12, v13\n"
"v_cvt_pk_bf16_f32 v25, v14, v15\n"
"v_lshlrev_b32 v12, 16, v18\n"
"v_and_b32 v13, s63, v18\n"
"v_lshlrev_b32 v14, 16, v19\n"
"v_and_b32 v15, s63, v19\n"
"v_accvgpr_read_b32 v10, a20\n"
"v_accvgpr_read_b32 v11, a21\n"
"L_st10967:\n"
"s_cbranch_execz L_retC\n"
"v_mul_f32 v12, v12, v10\n"
"v_mul_f32 v13, v13, v11\n"
"v_accvgpr_read_b32 v10, a22\n"
"v_accvgpr_read_b32 v11, a23\n"
"v_mul_f32 v14, v14, v10\n"
"v_mul_f32 v15, v15, v11\n"
"v_cvt_pk_bf16_f32 v26, v12, v13\n"
"v_cvt_pk_bf16_f32 v27, v14, v15\n"
"ds_write_b128 v7, v[24:27] offset:38912\n"
"s_waitcnt lgkmcnt(2)\n"
"buffer_store_dwordx4 v[168:171], v6, s[52:55], s92 offen offset:128 sc1\n"
"s_waitcnt lgkmcnt(1)\n"
"buffer_store_dwordx4 v[196:199], v6, s[52:55], s93 offen offset:1152 sc1\n"
"v_accvgpr_read_b32 v16, a252\n"
"v_accvgpr_read_b32 v17, a253\n"
"v_accvgpr_read_b32 v18, a254\n"
"v_accvgpr_read_b32 v19, a255\n"
"v_lshlrev_b32 v12, 16, v16\n"
"v_and_b32 v13, s63, v16\n"
"v_lshlrev_b32 v14, 16, v17\n"
"v_and_b32 v15, s63, v17\n"
"v_accvgpr_read_b32 v10, a24\n"
"v_accvgpr_read_b32 v11, a25\n"
"v_mul_f32 v12, v12, v10\n"
"v_mul_f32 v13, v13, v11\n"
"v_accvgpr_read_b32 v10, a26\n"
"v_accvgpr_read_b32 v11, a27\n"
"v_mul_f32 v14, v14, v10\n"
"v_mul_f32 v15, v15, v11\n"
"v_cvt_pk_bf16_f32 v28, v12, v13\n"
"v_cvt_pk_bf16_f32 v29, v14, v15\n"
"v_lshlrev_b32 v12, 16, v18\n"
"v_and_b32 v13, s63, v18\n"
"v_lshlrev_b32 v14, 16, v19\n"
"v_and_b32 v15, s63, v19\n"
"v_accvgpr_read_b32 v10, a28\n"
"v_accvgpr_read_b32 v11, a29\n"
"v_mul_f32 v12, v12, v10\n"
"v_mul_f32 v13, v13, v11\n"
"v_accvgpr_read_b32 v10, a30\n"
"v_accvgpr_read_b32 v11, a31\n"
"v_mul_f32 v14, v14, v10\n"
"v_mul_f32 v15, v15, v11\n"
"v_cvt_pk_bf16_f32 v30, v12, v13\n"
"v_cvt_pk_bf16_f32 v31, v14, v15\n"
"ds_write_b128 v8, v[28:31] offset:38912\n"
"ds_read_b128 v[224:227], v2 offset:38912\n"
"ds_read_b128 v[252:255], v2 offset:39936\n"
"s_waitcnt lgkmcnt(1)\n"
"buffer_store_dwordx4 v[224:227], v6, s[52:55], s94 offen offset:2176 sc1\n"
"s_waitcnt lgkmcnt(0)\n"
"buffer_store_dwordx4 v[252:255], v6, s[52:55], s95 offen offset:3200 sc1\n"
"s_or_b32 s66, %26, %16\n"
"s_cbranch_scc1 L_end\n"
"v_mov_b32 v10, s78\n"
"v_mov_b32 v11, s79\n"
"s_mov_b32 s66, 139264\n"
"buffer_store_dwordx2 v[10:11], off, s[56:59], s66 sc1\n"
"s_branch L_end\n"
"L_slow0:\n"
"s_cmp_eq_u32 s68, 0\n"
"s_cbranch_scc1 L_back0\n"
"s_sub_u32 s68, s68, 1\n"
"s_sleep 2\n"
"buffer_load_dwordx2 v[8:9], off, s[56:59], %14 sc1\n"
"s_waitcnt vmcnt(0)\n"
"v_subrev_u32 v8, s65, v8\n"
"v_xor_b32 v9, s79, v9\n"
"v_or_b32 v8, v8, v9\n"
"v_cmp_lt_u32 vcc, 15, v8\n"
"s_nop 4\n"
"s_cbranch_vccnz L_slow0\n"
"s_branch L_back0\n"
"L_slow1:\n"
"s_cmp_eq_u32 s68, 0\n"
"s_cbranch_scc1 L_back1\n"
"s_sub_u32 s68, s68, 1\n"
"s_sleep 2\n"
"buffer_load_dwordx2 v[8:9], off, s[56:59], %14 sc1\n"
"s_waitcnt vmcnt(0)\n"
"v_subrev_u32 v8, s65, v8\n"
"v_xor_b32 v9, s79, v9\n"
"v_or_b32 v8, v8, v9\n"
"v_cmp_lt_u32 vcc, 15, v8\n"
"s_nop 4\n"
"s_cbranch_vccnz L_slow1\n"
"s_branch L_back1\n"
"L_slow2:\n"
"s_cmp_eq_u32 s68, 0\n"
"s_cbranch_scc1 L_back2\n"
"s_sub_u32 s68, s68, 1\n"
"s_sleep 2\n"
"buffer_load_dwordx4 v[12:15], v0, s[56:59], %25 offen sc1\n"
"s_waitcnt vmcnt(0)\n"
"v_subrev_u32 v12, s67, v12\n"
"v_xor_b32 v13, s79, v13\n"
"v_subrev_u32 v14, s67, v14\n"
"v_xor_b32 v15, s79, v15\n"
"v_or_b32 v12, v12, v14\n"
"v_or_b32 v13, v13, v15\n"
"v_or_b32 v12, v12, v13\n"
"v_cmp_lt_u32 vcc, 15, v12\n"
"s_nop 4\n"
"s_cbranch_vccnz L_slow2\n"
"s_branch L_back2\n"
"L_slow3:\n"
"s_cmp_eq_u32 s68, 0\n"
"s_cbranch_scc1 L_back3\n"
"s_sub_u32 s68, s68, 1\n"
"s_sleep 2\n"
"buffer_load_dwordx4 v[24:27], v0, s[56:59], s65 offen sc1\n"
"s_waitcnt vmcnt(0)\n"
"v_subrev_u32 v24, s70, v24\n"
"v_xor_b32 v25, s79, v25\n"
"v_subrev_u32 v26, s70, v26\n"
"v_xor_b32 v27, s79, v27\n"
"v_or_b32 v24, v24, v26\n"
"v_or_b32 v25, v25, v27\n"
"v_or_b32 v24, v24, v25\n"
"v_cmp_lt_u32 vcc, 15, v24\n"
"s_nop 4\n"
"s_cbranch_vccnz L_slow3\n"
"s_branch L_back3\n"
"L_slow4:\n"
"s_cmp_eq_u32 s68, 0\n"
"s_cbranch_scc1 L_back4\n"
"s_sub_u32 s68, s68, 1\n"
"s_sleep 2\n"
"buffer_load_dwordx2 v[8:9], off, s[56:59], %14 sc1\n"
"s_waitcnt vmcnt(0)\n"
"v_subrev_u32 v8, s65, v8\n"
"v_xor_b32 v9, s79, v9\n"
"v_or_b32 v8, v8, v9\n"
"v_cmp_lt_u32 vcc, 15, v8\n"
"s_nop 4\n"
"s_cbranch_vccnz L_slow4\n"
"s_branch L_back4\n"
"L_headF:\n"
"L_end:\n"
"s_waitcnt vmcnt(0) lgkmcnt(0)\n"
    :: LO(wsb), HI(wsb), LO(xo), HI(xo), LO(ob), HI(ob), LO(wr), HI(wr), LO(bb), HI(bb), "s"(wo), "s"(eo), "s"(ep), "s"(fo), "s"(fp),
       "s"(ldsa), "s"(w), "s"(col), "v"(threadIdx.x), "s"(db), "s"(rs1), "s"(rs2), "s"(rd1), "s"(rn1), "s"(wfo), "s"(wfa), "s"(b)
    : "memory", "scc", "vcc",
"v1","v2","v3","v4","v5","v6","v7","v8","v9","v10","v11","v12","v13","v14","v15","v16","v17","v18","v19","v20","v21","v22","v23","v24","v25","v26","v27","v28","v29","v30","v31","v32","v33","v34","v35","v36","v37","v38","v39","v40","v41","v42","v43","v44","v45","v46","v47","v48","v49","v50","v51","v52","v53","v54","v55","v56","v57","v58","v59","v60","v61","v62","v63","v64","v65","v66","v67","v68","v69","v70","v71","v72","v73","v74","v75","v76","v77","v78","v79","v80","v81","v82","v83","v84","v85","v86","v87","v88","v89","v90","v91","v92","v93","v94","v95","v96","v97","v98","v99","v100","v101","v102","v103","v104","v105","v106","v107","v108","v109","v110","v111","v112","v113","v114","v115","v116","v117","v118","v119","v120","v121","v122","v123","v124","v125","v126","v127","v128","v129","v130","v131","v132","v133","v134","v135","v136","v137","v138","v139","v140","v141","v142","v143","v144","v145","v146","v147","v148","v149","v150","v151","v152","v153","v154","v155","v156","v157","v158","v159","v160","v161","v162","v163","v164","v165","v166","v167","v168","v169","v170","v171","v172","v173","v174","v175","v176","v177","v178","v179","v180","v181","v182","v183","v184","v185","v186","v187","v188","v189","v190","v191","v192","v193","v194","v195","v196","v197","v198","v199","v200","v201","v202","v203","v204","v205","v206","v207","v208","v209","v210","v211","v212","v213","v214","v215","v216","v217","v218","v219","v220","v221","v222","v223","v224","v225","v226","v227","v228","v229","v230","v231","v232","v233","v234","v235","v236","v237","v238","v239","v240","v241","v242","v243","v244","v245","v246","v247","v248","v249","v250","v251","v252","v253","v254","v255",
"a0","a1","a2","a3","a4","a5","a6","a7","a8","a9","a10","a11","a12","a13","a14","a15","a16","a17","a18","a19","a20","a21","a22","a23","a24","a25","a26","a27","a28","a29","a30","a31","a32","a33","a34","a35","a36","a37","a38","a39","a40","a41","a42","a43","a44","a45","a46","a47","a48","a49","a50","a51","a52","a53","a54","a55","a56","a57","a58","a59","a60","a61","a62","a63","a64","a65","a66","a67","a68","a69","a70","a71","a72","a73","a74","a75","a76","a77","a78","a79","a80","a81","a82","a83","a84","a85","a86","a87","a88","a89","a90","a91","a92","a93","a94","a95","a96","a97","a98","a99","a100","a101","a102","a103","a104","a105","a106","a107","a108","a109","a110","a111","a112","a113","a114","a115","a116","a117","a118","a119","a120","a121","a122","a123","a124","a125","a126","a127","a128","a129","a130","a131","a132","a133","a134","a135","a136","a137","a138","a139","a140","a141","a142","a143","a144","a145","a146","a147","a148","a149","a150","a151","a152","a153","a154","a155","a156","a157","a158","a159","a160","a161","a162","a163","a164","a165","a166","a167","a168","a169","a170","a171","a172","a173","a174","a175","a176","a177","a178","a179","a180","a181","a182","a183","a184","a185","a186","a187","a188","a189","a190","a191","a192","a193","a194","a195","a196","a197","a198","a199","a200","a201","a202","a203","a204","a205","a206","a207","a208","a209","a210","a211","a212","a213","a214","a215","a216","a217","a218","a219","a220","a221","a222","a223","a224","a225","a226","a227","a228","a229","a230","a231","a232","a233","a234","a235","a236","a237","a238","a239","a240","a241","a242","a243","a244","a245","a246","a247","a248","a249","a250","a251","a252","a253","a254","a255",
"s29","s30","s31","s33","s34","s35","s36","s37","s38","s39","s40","s41","s42","s43","s44","s45","s46","s47","s48","s49","s50","s51","s52","s53","s54","s55","s56","s57","s58","s59","s60","s61","s62","s63","s64","s65","s66","s67","s68","s69","s70","s71","s72","s73","s74","s75","s76","s77","s78","s79","s80","s81","s82","s83","s84","s85","s86","s87","s88","s89","s90","s91","s92","s93","s94","s95","s96","s97","s98","s99");
}

extern "C" void kernel_launch(void* const* d_in, const int* in_sizes, int n_in,
                              void* d_out, int out_size, void* d_ws, size_t ws_size,
                              hipStream_t stream) {
  const unsigned short* x0 = (const unsigned short*)d_in[0];
  const unsigned short* ws = (const unsigned short*)d_in[1];
  const unsigned short* bs = (const unsigned short*)d_in[2];
  dcn_cross_kernel<<<256, 256, 0, stream>>>(x0, ws, bs, (unsigned char*)d_ws, (unsigned short*)d_out);
}
```

```cpp
#include <hip/hip_runtime.h>


constexpr int kD = 1024;
constexpr unsigned kWFlagOff = 131072, kWOff = 1u << 20, kWStream = 200 * 8192, kXchOff = 32u << 20, kDbgOff = 224u << 20;

__device__ __forceinline__ unsigned unit_src(unsigned v, unsigned h, unsigned w) {
  const unsigned l = v >> 5, u = v & 31;
  const unsigned split = ((0xff0000ffu >> u) & 1) - 1;
  const unsigned part = l ? u >> 4 : 0u, g = (0xff00ff00u >> u) & 1;
  const unsigned c = u >> (split & 1), jh = (u >> (~split & 2)) & 1;
  const unsigned kc = 8 * (h ^ part) + 2 * ((w + c) & 3) + g;
  return (l * 1024 + 64 * kc) * 2048 + jh * 128;
}

__global__ __launch_bounds__(256, 1) __attribute__((aligned(4096)))
void dcn_cross_kernel(const unsigned short* __restrict__ x0, const unsigned short* __restrict__ ws, const unsigned short* __restrict__ bs,
                      unsigned char* __restrict__ wsp, unsigned short* __restrict__ out) {
  __shared__ __attribute__((aligned(16))) unsigned char lds[163840];
  const unsigned b = blockIdx.x;
  constexpr unsigned kPx = 1 ? 1u : 8u;
  const unsigned h = 1 ? (b & 1) : ((b >> 3) & 1), rb = 1 ? (b >> 1) : ((b >> 4) * 8 + (b & 7));
  const unsigned w = __builtin_amdgcn_readfirstlane(threadIdx.x >> 6);
  unsigned col = (512 * h + 128 * w) * 2, rows = rb * 128 * kD * 2, oofs = rows + col;
  unsigned wo = (h * 4 + w) * kWStream, eo = kXchOff + b * 524288 + w * 32768, ep = kXchOff + (b ^ kPx) * 524288 + w * 32768;
  unsigned fo = b * 512 + w * 128, fp = (b ^ kPx) * 512 + w * 128;
  unsigned db = kDbgOff + (b * 4 + w) * 512;
  const unsigned v1 = 24 + (rb >> 1), v2 = 64 + ((rb >> 1) & 31), hk = rb & 1, h2 = rb >> 6;
  unsigned rs1 = unit_src(v1, 0, w) + hk * 65536, rd1 = v1 * 8192 + hk * 1024;
  unsigned rs2 = unit_src(v2, h2, w) + hk * 65536 + h2 * 1024 + h * ((4u << 20) - 1024), rn1 = v2 * 8192 + hk * 1024 + h2 * 4 * kWStream;
  unsigned wfa = kWFlagOff + w * 1024, wfo = wfa + h * 4096 + rb * 8;
  asm volatile("" : "+s"(col), "+s"(rows), "+s"(oofs), "+s"(wo), "+s"(eo), "+s"(ep), "+s"(fo), "+s"(fp), "+s"(db), "+s"(rs1), "+s"(rd1), "+s"(rs2), "+s"(rn1), "+s"(wfa), "+s"(wfo));
  __builtin_amdgcn_sched_barrier(0);
  const unsigned long long wsb = (unsigned long long)wsp, bb = (unsigned long long)bs;
  const unsigned long long xo = (unsigned long long)x0 + rows, ob = (unsigned long long)out + oofs, wr = (unsigned long long)ws + col;
  wo += (unsigned)wsb > ~(15u << 20) ? 16u << 20 : kWOff;
  const unsigned xa = (0u - (unsigned)wsb) & 0x1fffffu;
  eo += xa; ep += xa;
  const unsigned ldsa = (unsigned)(size_t)(__attribute__((address_space(3))) unsigned char*)lds;
#define LO(x) "s"((unsigned)(x))
#define HI(x) "s"((unsigned)((x) >> 32))
  asm volatile(
"L_main_entry:\n"
"v_and_b32 v12, 63, %18\n"
"v_lshrrev_b32 v13, 3, v12\n"
"v_and_b32 v15, 7, v12\n"
"v_lshlrev_b32 v10, 11, v13\n"
"s_andn2_b32 s60, 1024, %17\n"
"s_lshl_b32 s75, s60, 10\n"
"s_mov_b32 s52, %6\n"
"s_and_b32 s53, %7, 0xffff\n"
"s_mov_b32 s54, 12581120\n"
"s_mov_b32 s55, 0x20000\n"
"s_getpc_b64 s[96:97]\n"
"L_pc:\n"
"s_lshr_b32 s66, %26, 3\n"
"s_lshl_b32 s66, s66, 2\n"
"s_add_u32 s66, s66, %16\n"
"s_mul_i32 s66, s66, 704\n"
"s_and_b32 s97, s97, 0xffff\n"
"s_mov_b32 s98, L_end-L_pc\n"
"s_mov_b32 s99, 0x20000\n"
"s_min_u32 s66, s66, L_end-L_pc-704\n"
"v_min_u32 v9, 10, v12\n"
"v_lshlrev_b32 v9, 6, v9\n"
"buffer_load_dword a252, v9, s[96:99], s66 offen\n"
"v_xor_b32 v6, v15, v13\n"
"v_lshl_add_u32 v6, v6, 4, v10\n"
"s_mov_b32 s48, %2\n"
"s_and_b32 s49, %3, 0xffff\n"
"s_mov_b32 s50, 65536\n"
"s_mov_b32 s51, 0x20000\n"
"s_lshl_b32 s66, %16, 16\n"
"s_add_u32 s48, s48, s66\n"
"s_addc_u32 s49, s49, 0\n"
"s_lshl_b32 s72, %16, 12\n"
"s_add_u32 s72, s72, %15\n"
"s_mul_i32 s73, %16, 40960\n"
"s_add_u32 s64, s73, %15\n"
"v_lshrrev_b32 v11, 2, v12\n"
"v_and_b32 v11, 4, v11\n"
"v_xor_b32 v11, v11, v15\n"
"v_lshl_add_u32 v11, v11, 4, v10\n"
"s_bitcmp1_b32 %26, 0\n"
"s_cbranch_scc1 L_y\n"
"s_mov_b64 s[96:97], s[52:53]\n"
"s_cmp_lt_u32 %22, 524288\n"
"s_cselect_b32 s98, s54, 0\n"
"s_mov_b32 s99, 0x20000\n"
"s_xor_b32 s71, %20, 0x100000\n"
"s_add_u32 m0, s72, 139264\n"
"s_add_u32 s67, %20, 0\n"
"buffer_load_dwordx4 v11, s[96:99], s67 offen offset:0  lds\n"
"s_add_u32 s67, %20, 15360\n"
"buffer_load_dwordx4 v11, s[96:99], s67 offen offset:1024  lds\n"
"s_add_u32 s67, %20, 30720\n"
"buffer_load_dwordx4 v11, s[96:99], s67 offen offset:2048  lds\n"
"s_add_u32 s67, %20, 46080\n"
"buffer_load_dwordx4 v11, s[96:99], s67 offen offset:3072  lds\n"
"s_add_u32 m0, s72, 98304\n"
"s_add_u32 s67, s71, 1024\n"
"buffer_load_dwordx4 v11, s[96:99], s67 offen offset:0  lds\n"
"s_add_u32 s67, s71, 16384\n"
"buffer_load_dwordx4 v11, s[96:99], s67 offen offset:1024  lds\n"
"s_add_u32 s67, s71, 31744\n"
"buffer_load_dwordx4 v11, s[96:99], s67 offen offset:2048  lds\n"
"s_add_u32 s67, s71, 47104\n"
"buffer_load_dwordx4 v11, s[96:99], s67 offen offset:3072  lds\n"
"L_y:\n"
"s_add_u32 m0, s72, 16384\n"
"s_add_u32 s67, s60, 0\n"
"buffer_load_dwordx4 v6, s[48:51], s67 offen offset:0  lds\n"
"s_add_u32 s67, s60, 15360\n"
"buffer_load_dwordx4 v6, s[48:51], s67 offen offset:1024  lds\n"
"s_add_u32 s67, s60, 30720\n"
"buffer_load_dwordx4 v6, s[48:51], s67 offen offset:2048  lds\n"
"s_add_u32 s67, s60, 46080\n"
"buffer_load_dwordx4 v6, s[48:51], s67 offen offset:3072  lds\n"
"s_add_u32 m0, s64, 0\n"
"s_add_u32 s67, s75, 0\n"
"buffer_load_dwordx4 v11, s[52:55], s67 offen offset:0  lds\n"
"s_add_u32 s67, s75, 15360\n"
"buffer_load_dwordx4 v11, s[52:55], s67 offen offset:1024  lds\n"
"s_add_u32 s67, s75, 30720\n"
"buffer_load_dwordx4 v11, s[52:55], s67 offen offset:2048  lds\n"
"s_add_u32 s67, s75, 46080\n"
"buffer_load_dwordx4 v11, s[52:55], s67 offen offset:3072  lds\n"
"s_add_u32 m0, s64, 4096\n"
"s_add_u32 s67, s75, 65536\n"
"buffer_load_dwordx4 v11, s[52:55], s67 offen offset:0  lds\n"
"s_add_u32 s67, s75, 80896\n"
"buffer_load_dwordx4 v11, s[52:55], s67 offen offset:1024  lds\n"
"s_add_u32 s67, s75, 96256\n"
"buffer_load_dwordx4 v11, s[52:55], s67 offen offset:2048  lds\n"
"s_add_u32 s67, s75, 111616\n"
"buffer_load_dwordx4 v11, s[52:55], s67 offen offset:3072  lds\n"
"s_lshr_b32 s65, %26, 4\n"
"s_lshl_b32 s65, s65, 2\n"
"s_add_u32 s65, s65, %16\n"
"s_lshl_b32 s65, s65, 11\n"
"s_lshl_b32 s67, %16, 8\n"
"s_sub_u32 s65, s65, s67\n"
"v_and_b32 v14, 1, v13\n"
"v_lshlrev_b32 v14, 17, v14\n"
"v_lshl_add_u32 v14, v15, 7, v14\n"
"v_and_b32 v14, 0xfffffdff, v14\n"
"s_lshl_b32 s67, %26, 6\n"
"s_and_b32 s67, s67, 512\n"
"s_add_u32 s65, s65, s67\n"
"v_lshlrev_b32 v0, 4, v12\n"
"s_mov_b32 s56, %0\n"
"s_and_b32 s57, %1, 0xffff\n"
"s_mov_b32 s58, 171966464\n"
"s_mov_b32 s59, 0x20000\n"
"v_add_u32 v1, 4096, v0\n"
"s_add_u32 s36, s56, %10\n"
"s_addc_u32 s37, s57, 0\n"
"s_mov_b32 s38, 1572864\n"
"s_mov_b32 s39, 0x20000\n"
"s_add_u32 s34, s36, 200704\n"
"s_mov_b32 s35, s37\n"
"v_and_b32 v13, 15, v12\n"
"v_lshrrev_b32 v10, 4, v12\n"
"v_xor_b32 v15, v15, v10\n"
"v_lshlrev_b32 v7, 7, v13\n"
"v_lshl_add_u32 v7, v15, 4, v7\n"
"v_add_u32 v7, s64, v7\n"
"v_xor_b32 v8, 64, v7\n"
"v_bfe_u32 v13, v12, 2, 2\n"
"v_and_b32 v15, 3, v12\n"
"v_bfe_u32 v9, v12, 3, 1\n"
"v_lshl_add_u32 v15, v9, 2, v15\n"
"v_lshl_add_u32 v13, v10, 3, v13\n"
"v_lshlrev_b32 v13, 7, v13\n"
"v_lshl_add_u32 v9, v15, 4, v13\n"
"v_add_u32 v9, s64, v9\n"
"v_xor_b32 v15, 64, v9\n"
"s_mov_b32 s40, %2\n"
"s_and_b32 s41, %3, 0xffff\n"
"s_mov_b32 s42, 260352\n"
"s_mov_b32 s43, 0x20000\n"
"s_add_u32 s40, s40, %17\n"
"s_addc_u32 s41, s41, 0\n"
"s_mov_b32 s80, 0\n"
"s_mov_b32 s81, 15360\n"
"s_mov_b32 s82, 30720\n"
"s_mov_b32 s83, 46080\n"
"s_mov_b32 s84, 65536\n"
"s_mov_b32 s85, 80896\n"
"s_mov_b32 s86, 96256\n"
"s_mov_b32 s87, 111616\n"
"s_mov_b32 s88, 131072\n"
"s_mov_b32 s89, 146432\n"
"s_mov_b32 s90, 161792\n"
"s_mov_b32 s91, 177152\n"
"s_mov_b32 s92, 196608\n"
"s_mov_b32 s93, 211968\n"
"s_mov_b32 s94, 227328\n"
"s_mov_b32 s95, 242688\n"
"s_mov_b32 s62, 0\n"
"s_mov_b32 s63, 0xffff0000\n"
"s_mov_b32 s68, 0x8000\n"
"s_add_u32 s66, %16, 0\n"
"s_and_b32 s66, s66, 3\n"
"s_mul_i32 s66, s66, 40960\n"
"s_add_u32 s66, s66, %15\n"
"v_add_u32 v2, s66, v0\n"
"s_add_u32 s66, %16, 1\n"
"s_and_b32 s66, s66, 3\n"
"s_mul_i32 s66, s66, 40960\n"
"s_add_u32 s66, s66, %15\n"
"v_add_u32 v3, s66, v0\n"
"s_add_u32 s66, %16, 2\n"
"s_and_b32 s66, s66, 3\n"
"s_mul_i32 s66, s66, 40960\n"
"s_add_u32 s66, s66, %15\n"
"v_add_u32 v4, s66, v0\n"
"s_add_u32 s66, %16, 3\n"
"s_and_b32 s66, s66, 3\n"
"s_mul_i32 s66, s66, 40960\n"
"s_add_u32 s66, s66, %15\n"
"v_add_u32 v5, s66, v0\n"
"s_sub_u32 s66, 0, s73\n"
"v_add_u32 v12, s66, v7\n"
"v_add_u32 v13, s66, v8\n"
"s_mov_b64 exec, 0\n"
"s_cmp_eq_u32 %16, 0\n"
"s_cbranch_scc1 L_st534\n"
"s_cmp_eq_u32 %16, 1\n"
"s_cbranch_scc1 L_st1062\n"
"s_cmp_eq_u32 %16, 2\n"
"s_cbranch_scc1 L_st1591\n"
"s_branch L_st2135\n"
"L_retA:\n"
"s_mov_b64 exec, -1\n"
"s_nop 3\n"
"s_mov_b32 s44, %8\n"
"s_and_b32 s45, %9, 0xffff\n"
"s_mov_b32 s46, 12288\n"
"s_mov_b32 s47, 0x20000\n"
"s_mov_b32 s61, %17\n"
"v_lshrrev_b32 v16, 4, v0\n"
"v_and_b32 v16, 0x30, v16\n"
"buffer_load_dwordx2 v[18:19], v16, s[44:47], s61 offen offset:0\n"
"v_lshrrev_b32 v20, 4, v0\n"
"v_and_b32 v20, 0x30, v20\n"
"buffer_load_dwordx2 v[22:23], v20, s[44:47], s61 offen offset:8\n"
"v_lshrrev_b32 v24, 4, v0\n"
"v_and_b32 v24, 0x30, v24\n"
"buffer_load_dwordx2 v[26:27], v24, s[44:47], s61 offen offset:64\n"
"v_lshrrev_b32 v28, 4, v0\n"
"v_and_b32 v28, 0x30, v28\n"
"buffer_load_dwordx2 v[30:31], v28, s[44:47], s61 offen offset:72\n"
"s_add_u32 s70, s75, 131072\n"
"s_add_u32 s70, s70, s65\n"
"buffer_load_dword a252, v14, s[52:55], s70 offen\n"
"s_mov_b32 s67, 139264\n"
"buffer_load_dwordx2 v[252:253], off, s[56:59], s67 sc1\n"
"s_waitcnt vmcnt(14)\n"
"s_barrier\n"
"ds_read_b128 a[100:103], v13 offset:16384\n"
"ds_read_b128 a[96:99], v12 offset:16384\n"
"ds_read_b128 a[108:111], v13 offset:18432\n"
"ds_read_b128 a[104:107], v12 offset:18432\n"
"ds_read_b128 a[116:119], v13 offset:20480\n"
"ds_read_b128 a[112:115], v12 offset:20480\n"
"ds_read_b128 a[124:127], v13 offset:22528\n"
"ds_read_b128 a[120:123], v12 offset:22528\n"
"ds_read_b128 a[132:135], v13 offset:24576\n"
"ds_read_b128 a[128:131], v12 offset:24576\n"
"ds_read_b128 a[140:143], v13 offset:26624\n"
"ds_read_b128 a[136:139], v12 offset:26624\n"
"ds_read_b128 a[148:151], v13 offset:28672\n"
"ds_read_b128 a[144:147], v12 offset:28672\n"
"ds_read_b128 a[156:159], v13 offset:30720\n"
"ds_read_b128 a[152:155], v12 offset:30720\n"
"s_add_u32 m0, s64, 8192\n"
"s_add_u32 s67, s75, 128\n"
"buffer_load_dwordx4 v11, s[52:55], s67 offen offset:0  lds\n"
"s_add_u32 s67, s75, 15488\n"
"buffer_load_dwordx4 v11, s[52:55], s67 offen offset:1024  lds\n"
"s_add_u32 s67, s75, 30848\n"
"buffer_load_dwordx4 v11, s[52:55], s67 offen offset:2048  lds\n"
"s_add_u32 s67, s75, 46208\n"
"buffer_load_dwordx4 v11, s[52:55], s67 offen offset:3072  lds\n"
"s_add_u32 m0, s64, 12288\n"
"s_add_u32 s67, s75, 65664\n"
"buffer_load_dwordx4 v11, s[52:55], s67 offen offset:0  lds\n"
"s_add_u32 s67, s75, 81024\n"
"buffer_load_dwordx4 v11, s[52:55], s67 offen offset:1024  lds\n"
"s_add_u32 s67, s75, 96384\n"
"buffer_load_dwordx4 v11, s[52:55], s67 offen offset:2048  lds\n"
"s_add_u32 s67, s75, 111744\n"
"buffer_load_dwordx4 v11, s[52:55], s67 offen offset:3072  lds\n"
"s_waitcnt vmcnt(14)\n"
"ds_read_b64_tr_b16 a[32:33], v9 offset:0\n"
"ds_read_b64_tr_b16 a[34:35], v9 offset:512\n"
"ds_read_b64_tr_b16 a[36:37], v9 offset:4096\n"
"ds_read_b64_tr_b16 a[38:39], v9 offset:4608\n"
"ds_read_b64_tr_b16 a[40:41], v9 offset:8\n"
"ds_read_b64_tr_b16 a[42:43], v9 offset:520\n"
"ds_read_b64_tr_b16 a[44:45], v9 offset:4104\n"
"ds_read_b64_tr_b16 a[46:47], v9 offset:4616\n"
"ds_read_b64_tr_b16 a[48:49], v15 offset:0\n"
"ds_read_b64_tr_b16 a[50:51], v15 offset:512\n"
"ds_read_b64_tr_b16 a[52:53], v15 offset:4096\n"
"ds_read_b64_tr_b16 a[54:55], v15 offset:4608\n"
"ds_read_b64_tr_b16 a[56:57], v15 offset:8\n"
"ds_read_b64_tr_b16 a[58:59], v15 offset:520\n"
"ds_read_b64_tr_b16 a[60:61], v15 offset:4104\n"
"ds_read_b64_tr_b16 a[62:63], v15 offset:4616\n"
"s_waitcnt vmcnt(10)\n"
"v_lshlrev_b32 v16, 16, v18\n"
"v_and_b32 v17, s63, v18\n"
"v_lshlrev_b32 v18, 16, v19\n"
"v_and_b32 v19, s63, v19\n"
"v_add_f32 v16, 1.0, v16\n"
"v_add_f32 v17, 1.0, v17\n"
"v_add_f32 v18, 1.0, v18\n"
"v_add_f32 v19, 1.0, v19\n"
"v_lshlrev_b32 v20, 16, v22\n"
"v_and_b32 v21, s63, v22\n"
"v_lshlrev_b32 v22, 16, v23\n"
"v_and_b32 v23, s63, v23\n"
"v_add_f32 v20, 1.0, v20\n"
"v_add_f32 v21, 1.0, v21\n"
"v_add_f32 v22, 1.0, v22\n"
"v_add_f32 v23, 1.0, v23\n"
"v_lshlrev_b32 v24, 16, v26\n"
"v_and_b32 v25, s63, v26\n"
"v_lshlrev_b32 v26, 16, v27\n"
"v_and_b32 v27, s63, v27\n"
"v_add_f32 v24, 1.0, v24\n"
"v_add_f32 v25, 1.0, v25\n"
"v_add_f32 v26, 1.0, v26\n"
"v_add_f32 v27, 1.0, v27\n"
"v_lshlrev_b32 v28, 16, v30\n"
"v_and_b32 v29, s63, v30\n"
"v_lshlrev_b32 v30, 16, v31\n"
"v_and_b32 v31, s63, v31\n"
"v_add_f32 v28, 1.0, v28\n"
"v_add_f32 v29, 1.0, v29\n"
"v_add_f32 v30, 1.0, v30\n"
"v_add_f32 v31, 1.0, v31\n"
"v_accvgpr_write_b32 a0, v16\n"
"v_accvgpr_write_b32 a1, v17\n"
"v_accvgpr_write_b32 a2, v18\n"
"v_accvgpr_write_b32 a3, v19\n"
"v_accvgpr_write_b32 a4, v20\n"
"v_accvgpr_write_b32 a5, v21\n"
"v_accvgpr_write_b32 a6, v22\n"
"v_accvgpr_write_b32 a7, v23\n"
"v_accvgpr_write_b32 a8, v24\n"
"v_accvgpr_write_b32 a9, v25\n"
"v_accvgpr_write_b32 a10, v26\n"
"v_accvgpr_write_b32 a11, v27\n"
"v_accvgpr_write_b32 a12, v28\n"
"v_accvgpr_write_b32 a13, v29\n"
"v_accvgpr_write_b32 a14, v30\n"
"v_accvgpr_write_b32 a15, v31\n"
"s_add_u32 m0, s72, 57344\n"
"s_add_u32 s67, s60, 128\n"
"buffer_load_dwordx4 v6, s[48:51], s67 offen offset:0  lds\n"
"s_add_u32 s67, s60, 15488\n"
"buffer_load_dwordx4 v6, s[48:51], s67 offen offset:1024  lds\n"
"s_add_u32 s67, s60, 30848\n"
"buffer_load_dwordx4 v6, s[48:51], s67 offen offset:2048  lds\n"
"s_add_u32 s67, s60, 46208\n"
"buffer_load_dwordx4 v6, s[48:51], s67 offen offset:3072  lds\n"
"s_bitcmp1_b32 %26, 0\n"
"s_cbranch_scc1 L_z\n"
"s_cmp_lt_u32 %22, 524288\n"
"s_cselect_b32 s38, 8192000, 0\n"
"s_add_u32 s70, %22, 6553600\n"
"s_sub_u32 s67, s72, s64\n"
"s_add_u32 s67, s67, 98304\n"
"v_add_u32 v40, s67, v9\n"
"v_add_u32 v41, s67, v15\n"
"s_sleep 1\n"
"ds_read_b64_tr_b16 a[160:161], v40 offset:40960\n"
"ds_read_b64_tr_b16 a[162:163], v40 offset:41472\n"
"ds_read_b64_tr_b16 a[164:165], v40 offset:40968\n"
"ds_read_b64_tr_b16 a[166:167], v40 offset:41480\n"
"ds_read_b64_tr_b16 a[168:169], v41 offset:40960\n"
"ds_read_b64_tr_b16 a[170:171], v41 offset:41472\n"
"ds_read_b64_tr_b16 a[172:173], v41 offset:40968\n"
"ds_read_b64_tr_b16 a[174:175], v41 offset:41480\n"
"ds_read_b64_tr_b16 a[176:177], v40 offset:0\n"
"ds_read_b64_tr_b16 a[178:179], v40 offset:512\n"
"ds_read_b64_tr_b16 a[180:181], v40 offset:8\n"
"ds_read_b64_tr_b16 a[182:183], v40 offset:520\n"
"ds_read_b64_tr_b16 a[184:185], v41 offset:0\n"
"ds_read_b64_tr_b16 a[186:187], v41 offset:512\n"
"ds_read_b64_tr_b16 a[188:189], v41 offset:8\n"
"ds_read_b64_tr_b16 a[190:191], v41 offset:520\n"
"s_waitcnt lgkmcnt(14)\n"
"buffer_store_dwordx4 a[160:163], v0, s[36:39], %22 offen offset:0 sc1\n"
"s_waitcnt lgkmcnt(12)\n"
"buffer_store_dwordx4 a[164:167], v0, s[36:39], %22 offen offset:2048 sc1\n"
"s_waitcnt lgkmcnt(10)\n"
"buffer_store_dwordx4 a[168:171], v1, s[36:39], %22 offen offset:0 sc1\n"
"s_waitcnt lgkmcnt(8)\n"
"buffer_store_dwordx4 a[172:175], v1, s[36:39], %22 offen offset:2048 sc1\n"
"s_waitcnt lgkmcnt(6)\n"
"buffer_store_dwordx4 a[176:179], v0, s[36:39], s70 offen offset:0 sc1\n"
"s_waitcnt lgkmcnt(4)\n"
"buffer_store_dwordx4 a[180:183], v0, s[36:39], s70 offen offset:2048 sc1\n"
"s_waitcnt lgkmcnt(2)\n"
"buffer_store_dwordx4 a[184:187], v1, s[36:39], s70 offen offset:0 sc1\n"
"s_waitcnt lgkmcnt(0)\n"
"buffer_store_dwordx4 a[188:191], v1, s[36:39], s70 offen offset:2048 sc1\n"
"s_sleep 10\n"
"L_z:\n"
"s_nop 1\n"
"s_waitcnt lgkmcnt(8)\n"
"v_mfma_f32_16x16x32_bf16 v[32:35], a[32:35], a[96:99], v[16:19]\n"
"s_add_u32 s70, s75, 393216\n"
"v_mfma_f32_16x16x32_bf16 v[32:35], a[36:39], a[100:103], v[32:35]\n"
"s_add_u32 s70, s70, s65\n"
"v_mfma_f32_16x16x32_bf16 v[60:63], a[44:47], a[100:103], v[20:23]\n"
"buffer_load_dword a252, v14, s[52:55], s70 offen\n"
"v_mfma_f32_16x16x32_bf16 v[60:63], a[40:43], a[96:99], v[60:63]\n"
"s_add_u32 m0, s64, 32768\n"
"v_mfma_f32_16x16x32_bf16 v[64:67], a[40:43], a[104:107], v[20:23]\n"
"s_add_u32 s67, s75, 131072\n"
"v_mfma_f32_16x16x32_bf16 v[64:67], a[44:47], a[108:111], v[64:67]\n"
"buffer_load_dwordx4 v11, s[52:55], s67 offen offset:0  lds\n"
"v_mfma_f32_16x16x32_bf16 v[36:39], a[36:39], a[108:111], v[16:19]\n"
"s_add_u32 s67, s75, 146432\n"
"v_mfma_f32_16x16x32_bf16 v[36:39], a[32:35], a[104:107], v[36:39]\n"
"v_mfma_f32_16x16x32_bf16 v[40:43], a[32:35], a[112:115], v[16:19]\n"
"s_waitcnt lgkmcnt(0)\n"
"buffer_load_dwordx4 v11, s[52:55], s67 offen offset:1024  lds\n"
"v_mfma_f32_16x16x32_bf16 v[40:43], a[36:39], a[116:119], v[40:43]\n"
"v_mfma_f32_16x16x32_bf16 v[68:71], a[44:47], a[116:119], v[20:23]\n"
"s_add_u32 s67, s75, 161792\n"
"v_mfma_f32_16x16x32_bf16 v[68:71], a[40:43], a[112:115], v[68:71]\n"
"v_mfma_f32_16x16x32_bf16 v[72:75], a[40:43], a[120:123], v[20:23]\n"
"buffer_load_dwordx4 v11, s[52:55], s67 offen offset:2048  lds\n"
"v_mfma_f32_16x16x32_bf16 v[72:75], a[44:47], a[124:127], v[72:75]\n"
"v_mfma_f32_16x16x32_bf16 v[44:47], a[36:39], a[124:127], v[16:19]\n"
"s_add_u32 s67, s75, 177152\n"
"v_mfma_f32_16x16x32_bf16 v[44:47], a[32:35], a[120:123], v[44:47]\n"
"v_mfma_f32_16x16x32_bf16 v[48:51], a[32:35], a[128:131], v[16:19]\n"
"buffer_load_dwordx4 v11, s[52:55], s67 offen offset:3072  lds\n"
"v_mfma_f32_16x16x32_bf16 v[48:51], a[36:39], a[132:135], v[48:51]\n"
"v_mfma_f32_16x16x32_bf16 v[76:79], a[44:47], a[132:135], v[20:23]\n"
"s_add_u32 m0, s64, 36864\n"
"v_mfma_f32_16x16x32_bf16 v[76:79], a[40:43], a[128:131], v[76:79]\n"
"v_mfma_f32_16x16x32_bf16 v[80:83], a[40:43], a[136:139], v[20:23]\n"
"s_add_u32 s67, s75, 196608\n"
"v_mfma_f32_16x16x32_bf16 v[80:83], a[44:47], a[140:143], v[80:83]\n"
"v_mfma_f32_16x16x32_bf16 v[52:55], a[36:39], a[140:143], v[16:19]\n"
"buffer_load_dwordx4 v11, s[52:55], s67 offen offset:0  lds\n"
"v_mfma_f32_16x16x32_bf16 v[52:55], a[32:35], a[136:139], v[52:55]\n"
"v_mfma_f32_16x16x32_bf16 v[56:59], a[32:35], a[144:147], v[16:19]\n"
"s_mov_b64 exec, 0\n"
"s_cmp_eq_u32 %16, 0\n"
"s_cbranch_scc1 L_st5092\n"
"s_cmp_eq_u32 %16, 1\n"
"s_cbranch_scc1 L_st5702\n"
"s_cmp_eq_u32 %16, 2\n"
"s_cbranch_scc1 L_st6244\n"
"s_branch L_st6798\n"
"L_retB:\n"
"s_mov_b64 exec, -1\n"
"s_nop 3\n"
"s_add_u32 s67, s75, 211968\n"
"v_mfma_f32_16x16x32_bf16 v[56:59], a[36:39], a[148:151], v[56:59]\n"
"v_mfma_f32_16x16x32_bf16 v[84:87], a[44:47], a[148:151], v[20:23]\n"
"buffer_load_dwordx4 v11, s[52:55], s67 offen offset:1024  lds\n"
"v_mfma_f32_16x16x32_bf16 v[84:87], a[40:43], a[144:147], v[84:87]\n"
"v_mfma_f32_16x16x32_bf16 a[4:7], a[40:43], a[152:155], a[4:7]\n"
"s_add_u32 s67, s75, 227328\n"
"v_mfma_f32_16x16x32_bf16 a[4:7], a[44:47], a[156:159], a[4:7]\n"
"v_mfma_f32_16x16x32_bf16 a[0:3], a[36:39], a[156:159], a[0:3]\n"
"buffer_load_dwordx4 v11, s[52:55], s67 offen offset:2048  lds\n"
"v_mfma_f32_16x16x32_bf16 a[0:3], a[32:35], a[152:155], a[0:3]\n"
"v_mfma_f32_16x16x32_bf16 v[88:91], a[48:51], a[96:99], v[24:27]\n"
"s_add_u32 s67, s75, 242688\n"
"v_mfma_f32_16x16x32_bf16 v[88:91], a[52:55], a[100:103], v[88:91]\n"
"v_mfma_f32_16x16x32_bf16 v[116:119], a[60:63], a[100:103], v[28:31]\n"
"v_mfma_f32_16x16x32_bf16 v[116:119], a[56:59], a[96:99], v[116:119]\n"
"buffer_load_dwordx4 v11, s[52:55], s67 offen offset:3072  lds\n"
"v_mfma_f32_16x16x32_bf16 v[120:123], a[56:59], a[104:107], v[28:31]\n"
"s_add_u32 m0, s64, 0\n"
"v_mfma_f32_16x16x32_bf16 v[120:123], a[60:63], a[108:111], v[120:123]\n"
"v_mfma_f32_16x16x32_bf16 v[92:95], a[52:55], a[108:111], v[24:27]\n"
"s_add_u32 s67, s75, 131200\n"
"v_mfma_f32_16x16x32_bf16 v[92:95], a[48:51], a[104:107], v[92:95]\n"
"v_mfma_f32_16x16x32_bf16 v[96:99], a[48:51], a[112:115], v[24:27]\n"
"buffer_load_dwordx4 v11, s[52:55], s67 offen offset:0  lds\n"
"v_mfma_f32_16x16x32_bf16 v[96:99], a[52:55], a[116:119], v[96:99]\n"
"v_mfma_f32_16x16x32_bf16 v[124:127], a[60:63], a[116:119], v[28:31]\n"
"s_add_u32 s67, s75, 146560\n"
"v_mfma_f32_16x16x32_bf16 v[124:127], a[56:59], a[112:115], v[124:127]\n"
"buffer_load_dwordx4 v11, s[52:55], s67 offen offset:1024  lds\n"
"v_mfma_f32_16x16x32_bf16 v[128:131], a[56:59], a[120:123], v[28:31]\n"
"s_waitcnt vmcnt(15)\n"
"ds_read_b64_tr_b16 a[64:65], v9 offset:8192\n"
"v_mfma_f32_16x16x32_bf16 v[128:131], a[60:63], a[124:127], v[128:131]\n"
"s_add_u32 s67, s75, 161920\n"
"ds_read_b64_tr_b16 a[66:67], v9 offset:8704\n"
"v_mfma_f32_16x16x32_bf16 v[100:103], a[52:55], a[124:127], v[24:27]\n"
"ds_read_b64_tr_b16 a[68:69], v9 offset:12288\n"
"v_mfma_f32_16x16x32_bf16 v[100:103], a[48:51], a[120:123], v[100:103]\n"
"ds_read_b64_tr_b16 a[70:71], v9 offset:12800\n"
"v_mfma_f32_16x16x32_bf16 v[104:107], a[48:51], a[128:131], v[24:27]\n"
"ds_read_b64_tr_b16 a[72:73], v9 offset:8200\n"
"v_mfma_f32_16x16x32_bf16 v[104:107], a[52:55], a[132:135], v[104:107]\n"
"ds_read_b64_tr_b16 a[74:75], v9 offset:8712\n"
"buffer_load_dwordx4 v11, s[52:55], s67 offen offset:2048  lds\n"
"v_mfma_f32_16x16x32_bf16 v[132:135], a[60:63], a[132:135], v[28:31]\n"
"ds_read_b64_tr_b16 a[76:77], v9 offset:12296\n"
"v_mfma_f32_16x16x32_bf16 v[132:135], a[56:59], a[128:131], v[132:135]\n"
"s_add_u32 s67, s75, 177280\n"
"ds_read_b64_tr_b16 a[78:79], v9 offset:12808\n"
"v_mfma_f32_16x16x32_bf16 v[136:139], a[56:59], a[136:139], v[28:31]\n"
"buffer_load_dwordx4 v11, s[52:55], s67 offen offset:3072  lds\n"
"v_mfma_f32_16x16x32_bf16 v[136:139], a[60:63], a[140:143], v[136:139]\n"
"ds_read_b64_tr_b16 a[80:81], v15 offset:8192\n"
"v_mfma_f32_16x16x32_bf16 v[108:111], a[52:55], a[140:143], v[24:27]\n"
"s_add_u32 m0, s64, 4096\n"
"ds_read_b64_tr_b16 a[82:83], v15 offset:8704\n"
"v_mfma_f32_16x16x32_bf16 v[108:111], a[48:51], a[136:139], v[108:111]\n"
"ds_read_b64_tr_b16 a[84:85], v15 offset:12288\n"
"v_mfma_f32_16x16x32_bf16 v[112:115], a[48:51], a[144:147], v[24:27]\n"
"s_add_u32 s67, s75, 196736\n"
"ds_read_b64_tr_b16 a[86:87], v15 offset:12800\n"
"v_mfma_f32_16x16x32_bf16 v[112:115], a[52:55], a[148:151], v[112:115]\n"
"ds_read_b64_tr_b16 a[88:89], v15 offset:8200\n"
"v_mfma_f32_16x16x32_bf16 v[140:143], a[60:63], a[148:151], v[28:31]\n"
"ds_read_b64_tr_b16 a[90:91], v15 offset:8712\n"
"buffer_load_dwordx4 v11, s[52:55], s67 offen offset:0  lds\n"
"v_mfma_f32_16x16x32_bf16 v[140:143], a[56:59], a[144:147], v[140:143]\n"
"ds_read_b64_tr_b16 a[92:93], v15 offset:12296\n"
"v_mfma_f32_16x16x32_bf16 a[12:15], a[56:59], a[152:155], a[12:15]\n"
"s_add_u32 s67, s75, 212096\n"
"ds_read_b64_tr_b16 a[94:95], v15 offset:12808\n"
"v_mfma_f32_16x16x32_bf16 a[12:15], a[60:63], a[156:159], a[12:15]\n"
"v_mfma_f32_16x16x32_bf16 a[8:11], a[52:55], a[156:159], a[8:11]\n"
"buffer_load_dwordx4 v11, s[52:55], s67 offen offset:1024  lds\n"
"v_mfma_f32_16x16x32_bf16 a[8:11], a[48:51], a[152:155], a[8:11]\n"
"s_waitcnt lgkmcnt(8)\n"
"v_mfma_f32_16x16x32_bf16 v[144:147], a[64:67], a[96:99], 1.0\n"
"s_add_u32 s67, s75, 227456\n"
"v_mfma_f32_16x16x32_bf16 v[144:147], a[68:71], a[100:103], v[144:147]\n"
"v_mfma_f32_16x16x32_bf16 v[172:175], a[76:79], a[100:103], 1.0\n"
"s_waitcnt lgkmcnt(0)\n"
"v_readfirstlane_b32 s78, v252\n"
"v_readfirstlane_b32 s79, v253\n"
"v_mfma_f32_16x16x32_bf16 v[172:175], a[72:75], a[96:99], v[172:175]\n"
"buffer_load_dwordx4 v11, s[52:55], s67 offen offset:2048  lds\n"
"v_mfma_f32_16x16x32_bf16 v[176:179], a[72:75], a[104:107], 1.0\n"
"v_mfma_f32_16x16x32_bf16 v[176:179], a[76:79], a[108:111], v[176:179]\n"
"s_add_u32 s67, s75, 242816\n"
"v_mfma_f32_16x16x32_bf16 v[148:151], a[68:71], a[108:111], 1.0\n"
"s_add_u32 s78, s78, 16\n"
"v_mfma_f32_16x16x32_bf16 v[148:151], a[64:67], a[104:107], v[148:151]\n"
"s_add_u32 s79, s79, 16\n"
"v_mfma_f32_16x16x32_bf16 v[152:155], a[64:67], a[112:115], 1.0\n"
"s_sub_u32 s66, 40960, s73\n"
"v_mfma_f32_16x16x32_bf16 v[152:155], a[68:71], a[116:119], v[152:155]\n"
"buffer_load_dwordx4 v11, s[52:55], s67 offen offset:3072  lds\n"
"v_mfma_f32_16x16x32_bf16 v[180:183], a[76:79], a[116:119], 1.0\n"
"s_add_u32 m0, s72, 98304\n"
"v_add_u32 v12, s66, v7\n"
"v_mfma_f32_16x16x32_bf16 v[180:183], a[72:75], a[112:115], v[180:183]\n"
"v_mfma_f32_16x16x32_bf16 v[184:187], a[72:75], a[120:123], 1.0\n"
"s_add_u32 s67, s60, 256\n"
"v_add_u32 v13, s66, v8\n"
"v_mfma_f32_16x16x32_bf16 v[184:187], a[76:79], a[124:127], v[184:187]\n"
"buffer_load_dwordx4 v6, s[48:51], s67 offen offset:0  lds\n"
"v_mfma_f32_16x16x32_bf16 v[156:159], a[68:71], a[124:127], 1.0\n"
"v_mfma_f32_16x16x32_bf16 v[156:159], a[64:67], a[120:123], v[156:159]\n"
"s_add_u32 s67, s60, 15616\n"
"v_mfma_f32_16x16x32_bf16 v[160:163], a[64:67], a[128:131], 1.0\n"
"L_st534:\n"
"s_cbranch_execz L_st2680\n"
"v_mfma_f32_16x16x32_bf16 v[160:163], a[68:71], a[132:135], v[160:163]\n"
"buffer_load_dwordx4 v6, s[48:51], s67 offen offset:1024  lds\n"
"v_mfma_f32_16x16x32_bf16 v[188:191], a[76:79], a[132:135], 1.0\n"
"v_mfma_f32_16x16x32_bf16 v[188:191], a[72:75], a[128:131], v[188:191]\n"
"s_add_u32 s67, s60, 30976\n"
"v_mfma_f32_16x16x32_bf16 v[192:195], a[72:75], a[136:139], 1.0\n"
"v_mfma_f32_16x16x32_bf16 v[192:195], a[76:79], a[140:143], v[192:195]\n"
"buffer_load_dwordx4 v6, s[48:51], s67 offen offset:2048  lds\n"
"v_mfma_f32_16x16x32_bf16 v[164:167], a[68:71], a[140:143], 1.0\n"
"v_mfma_f32_16x16x32_bf16 v[164:167], a[64:67], a[136:139], v[164:167]\n"
"s_add_u32 s67, s60, 46336\n"
"v_mfma_f32_16x16x32_bf16 v[168:171], a[64:67], a[144:147], 1.0\n"
"v_mfma_f32_16x16x32_bf16 v[168:171], a[68:71], a[148:151], v[168:171]\n"
"v_mfma_f32_16x16x32_bf16 v[196:199], a[76:79], a[148:151], 1.0\n"
"buffer_load_dwordx4 v6, s[48:51], s67 offen offset:3072  lds\n"
"v_mfma_f32_16x16x32_bf16 v[196:199], a[72:75], a[144:147], v[196:199]\n"
"v_mfma_f32_16x16x32_bf16 a[20:23], a[72:75], a[152:155], 1.0\n"
"v_mfma_f32_16x16x32_bf16 a[20:23], a[76:79], a[156:159], a[20:23]\n"
"v_mfma_f32_16x16x32_bf16 a[16:19], a[68:71], a[156:159], 1.0\n"
"v_mfma_f32_16x16x32_bf16 a[16:19], a[64:67], a[152:155], a[16:19]\n"
"s_waitcnt vmcnt(21)\n"
"s_barrier\n"
"v_mfma_f32_16x16x32_bf16 v[200:203], a[80:83], a[96:99], 1.0\n"
"s_waitcnt vmcnt(12)\n"
"ds_read_b64_tr_b16 a[32:33], v9 offset:32768\n"
"v_mfma_f32_16x16x32_bf16 v[200:203], a[84:87], a[100:103], v[200:203]\n"
"s_add_u32 m0, s64, 8192\n"
"ds_read_b64_tr_b16 a[34:35], v9 offset:33280\n"
"v_mfma_f32_16x16x32_bf16 v[228:231], a[92:95], a[100:103], 1.0\n"
"ds_read_b128 a[100:103], v13 offset:16384\n"
"v_mfma_f32_16x16x32_bf16 v[228:231], a[88:91], a[96:99], v[228:231]\n"
"ds_read_b128 a[96:99], v12 offset:16384\n"
"v_mfma_f32_16x16x32_bf16 v[232:235], a[88:91], a[104:107], 1.0\n"
"ds_read_b64_tr_b16 a[36:37], v9 offset:36864\n"
"v_mfma_f32_16x16x32_bf16 v[232:235], a[92:95], a[108:111], v[232:235]\n"
"ds_read_b64_tr_b16 a[38:39], v9 offset:37376\n"
"v_mfma_f32_16x16x32_bf16 v[204:207], a[84:87], a[108:111], 1.0\n"
"ds_read_b128 a[108:111], v13 offset:18432\n"
"v_mfma_f32_16x16x32_bf16 v[204:207], a[80:83], a[104:107], v[204:207]\n"
"ds_read_b128 a[104:107], v12 offset:18432\n"
"ds_read_b64_tr_b16 a[40:41], v9 offset:32776\n"
"v_mfma_f32_16x16x32_bf16 v[208:211], a[80:83], a[112:115], 1.0\n"
"ds_read_b64_tr_b16 a[42:43], v9 offset:33288\n"
"v_mfma_f32_16x16x32_bf16 v[208:211], a[84:87], a[116:119], v[208:211]\n"
"ds_read_b64_tr_b16 a[44:45], v9 offset:36872\n"
"v_mfma_f32_16x16x32_bf16 v[236:239], a[92:95], a[116:119], 1.0\n"
"ds_read_b128 a[116:119], v13 offset:20480\n"
"v_mfma_f32_16x16x32_bf16 v[236:239], a[88:91], a[112:115], v[236:239]\n"
"ds_read_b128 a[112:115], v12 offset:20480\n"
"v_mfma_f32_16x16x32_bf16 v[240:243], a[88:91], a[120:123], 1.0\n"
"ds_read_b64_tr_b16 a[46:47], v9 offset:37384\n"
"v_mfma_f32_16x16x32_bf16 v[240:243], a[92:95], a[124:127], v[240:243]\n"
"ds_read_b64_tr_b16 a[48:49], v15 offset:32768\n"
"v_mfma_f32_16x16x32_bf16 v[212:215], a[84:87], a[124:127], 1.0\n"
"s_add_u32 s67, s75, 262144\n"
"ds_read_b128 a[124:127], v13 offset:22528\n"
"v_mfma_f32_16x16x32_bf16 v[212:215], a[80:83], a[120:123], v[212:215]\n"
"ds_read_b128 a[120:123], v12 offset:22528\n"
"v_mfma_f32_16x16x32_bf16 v[216:219], a[80:83], a[128:131], 1.0\n"
"ds_read_b64_tr_b16 a[50:51], v15 offset:33280\n"
"v_mfma_f32_16x16x32_bf16 v[216:219], a[84:87], a[132:135], v[216:219]\n"
"ds_read_b64_tr_b16 a[52:53], v15 offset:36864\n"
"v_mfma_f32_16x16x32_bf16 v[244:247], a[92:95], a[132:135], 1.0\n"
"ds_read_b128 a[132:135], v13 offset:24576\n"
"v_mfma_f32_16x16x32_bf16 v[244:247], a[88:91], a[128:131], v[244:247]\n"
"ds_read_b128 a[128:131], v12 offset:24576\n"
"v_mfma_f32_16x16x32_bf16 v[248:251], a[88:91], a[136:139], 1.0\n"
"ds_read_b64_tr_b16 a[54:55], v15 offset:37376\n"
"v_mfma_f32_16x16x32_bf16 v[248:251], a[92:95], a[140:143], v[248:251]\n"
"ds_read_b64_tr_b16 a[56:57], v15 offset:32776\n"
"v_mfma_f32_16x16x32_bf16 v[220:223], a[84:87], a[140:143], 1.0\n"
"ds_read_b128 a[140:143], v13 offset:26624\n"
"v_mfma_f32_16x16x32_bf16 v[220:223], a[80:83], a[136:139], v[220:223]\n"
"ds_read_b128 a[136:139], v12 offset:26624\n"
"v_mfma_f32_16x16x32_bf16 v[224:227], a[80:83], a[144:147], 1.0\n"
"ds_read_b64_tr_b16 a[58:59], v15 offset:33288\n"
"v_mfma_f32_16x16x32_bf16 v[224:227], a[84:87], a[148:151], v[224:227]\n"
"ds_read_b64_tr_b16 a[60:61], v15 offset:36872\n"
"v_mfma_f32_16x16x32_bf16 v[252:255], a[92:95], a[148:151], 1.0\n"
"ds_read_b128 a[148:151], v13 offset:28672\n"
"v_mfma_f32_16x16x32_bf16 v[252:255], a[88:91], a[144:147], v[252:255]\n"
"ds_read_b128 a[144:147], v12 offset:28672\n"
"ds_read_b64_tr_b16 a[62:63], v15 offset:37384\n"
"v_mfma_f32_16x16x32_bf16 a[28:31], a[88:91], a[152:155], 1.0\n"
"v_mfma_f32_16x16x32_bf16 a[28:31], a[92:95], a[156:159], a[28:31]\n"
"v_mfma_f32_16x16x32_bf16 a[24:27], a[84:87], a[156:159], 1.0\n"
"ds_read_b128 a[156:159], v13 offset:30720\n"
"v_mfma_f32_16x16x32_bf16 a[24:27], a[80:83], a[152:155], a[24:27]\n"
"s_waitcnt lgkmcnt(15)\n"
"ds_read_b128 a[152:155], v12 offset:30720\n"
"v_mfma_f32_16x16x32_bf16 v[32:35], a[32:35], a[96:99], v[32:35]\n"
"buffer_load_dwordx4 v11, s[52:55], s67 offen offset:0  lds\n"
"v_mfma_f32_16x16x32_bf16 v[32:35], a[36:39], a[100:103], v[32:35]\n"
"s_waitcnt lgkmcnt(2)\n"
"v_mfma_f32_16x16x32_bf16 v[60:63], a[44:47], a[100:103], v[60:63]\n"
"s_add_u32 s67, s75, 277504\n"
"v_mfma_f32_16x16x32_bf16 v[60:63], a[40:43], a[96:99], v[60:63]\n"
"v_mfma_f32_16x16x32_bf16 v[64:67], a[40:43], a[104:107], v[64:67]\n"
"v_mfma_f32_16x16x32_bf16 v[64:67], a[44:47], a[108:111], v[64:67]\n"
"buffer_load_dwordx4 v11, s[52:55], s67 offen offset:1024  lds\n"
"v_mfma_f32_16x16x32_bf16 v[36:39], a[36:39], a[108:111], v[36:39]\n"
"v_mfma_f32_16x16x32_bf16 v[36:39], a[32:35], a[104:107], v[36:39]\n"
"s_add_u32 s67, s75, 292864\n"
"v_mfma_f32_16x16x32_bf16 v[40:43], a[32:35], a[112:115], v[40:43]\n"
"v_mfma_f32_16x16x32_bf16 v[40:43], a[36:39], a[116:119], v[40:43]\n"
"v_mfma_f32_16x16x32_bf16 v[68:71], a[44:47], a[116:119], v[68:71]\n"
"buffer_load_dwordx4 v11, s[52:55], s67 offen offset:2048  lds\n"
"v_mfma_f32_16x16x32_bf16 v[68:71], a[40:43], a[112:115], v[68:71]\n"
"v_mfma_f32_16x16x32_bf16 v[72:75], a[40:43], a[120:123], v[72:75]\n"
"s_add_u32 s67, s75, 308224\n"
"v_mfma_f32_16x16x32_bf16 v[72:75], a[44:47], a[124:127], v[72:75]\n"
"v_mfma_f32_16x16x32_bf16 v[44:47], a[36:39], a[124:127], v[44:47]\n"
"v_mov_b32 v16, -1\n"
"v_mfma_f32_16x16x32_bf16 v[44:47], a[32:35], a[120:123], v[44:47]\n"
"buffer_load_dwordx4 v11, s[52:55], s67 offen offset:3072  lds\n"
"v_mfma_f32_16x16x32_bf16 v[48:51], a[32:35], a[128:131], v[48:51]\n"
"s_add_u32 m0, s64, 12288\n"
"v_mfma_f32_16x16x32_bf16 v[48:51], a[36:39], a[132:135], v[48:51]\n"
"v_mov_b32 v17, -1\n"
"v_mfma_f32_16x16x32_bf16 v[76:79], a[44:47], a[132:135], v[76:79]\n"
"s_waitcnt vmcnt(8)\n"
"ds_read_b64_tr_b16 a[64:65], v9 offset:0\n"
"v_mfma_f32_16x16x32_bf16 v[76:79], a[40:43], a[128:131], v[76:79]\n"
"s_add_u32 s67, s75, 327680\n"
"ds_read_b64_tr_b16 a[66:67], v9 offset:512\n"
"v_mfma_f32_16x16x32_bf16 v[80:83], a[40:43], a[136:139], v[80:83]\n"
"ds_read_b64_tr_b16 a[68:69], v9 offset:4096\n"
"buffer_load_dwordx4 v11, s[52:55], s67 offen offset:0  lds\n"
"v_mfma_f32_16x16x32_bf16 v[80:83], a[44:47], a[140:143], v[80:83]\n"
"ds_read_b64_tr_b16 a[70:71], v9 offset:4608\n"
"v_mfma_f32_16x16x32_bf16 v[52:55], a[36:39], a[140:143], v[52:55]\n"
"s_add_u32 s67, s75, 343040\n"
"ds_read_b64_tr_b16 a[72:73], v9 offset:8\n"
"v_mfma_f32_16x16x32_bf16 v[52:55], a[32:35], a[136:139], v[52:55]\n"
"s_waitcnt lgkmcnt(5)\n"
"ds_read_b64_tr_b16 a[74:75], v9 offset:520\n"
"v_mfma_f32_16x16x32_bf16 v[56:59], a[32:35], a[144:147], v[56:59]\n"
"ds_read_b64_tr_b16 a[76:77], v9 offset:4104\n"
"buffer_load_dwordx4 v11, s[52:55], s67 offen offset:1024  lds\n"
"v_mfma_f32_16x16x32_bf16 v[56:59], a[36:39], a[148:151], v[56:59]\n"
"s_add_u32 s67, s75, 358400\n"
"ds_read_b64_tr_b16 a[78:79], v9 offset:4616\n"
"v_mfma_f32_16x16x32_bf16 v[84:87], a[44:47], a[148:151], v[84:87]\n"
"s_add_u32 s69, %11, 8192\n"
"v_lshrrev_b32 v18, 1, v0\n"
"v_mfma_f32_16x16x32_bf16 v[84:87], a[40:43], a[144:147], v[84:87]\n"
"ds_read_b64_tr_b16 a[80:81], v15 offset:0\n"
"v_mfma_f32_16x16x32_bf16 a[4:7], a[40:43], a[152:155], a[4:7]\n"
"ds_read_b64_tr_b16 a[82:83], v15 offset:512\n"
"buffer_load_dwordx4 v11, s[52:55], s67 offen offset:2048  lds\n"
"v_mfma_f32_16x16x32_bf16 a[4:7], a[44:47], a[156:159], a[4:7]\n"
"ds_read_b64_tr_b16 a[84:85], v15 offset:4096\n"
"v_mfma_f32_16x16x32_bf16 a[0:3], a[36:39], a[156:159], a[0:3]\n"
"s_add_u32 s67, s75, 373760\n"
"ds_read_b64_tr_b16 a[86:87], v15 offset:4608\n"
"v_mfma_f32_16x16x32_bf16 a[0:3], a[32:35], a[152:155], a[0:3]\n"
"ds_read_b64_tr_b16 a[88:89], v15 offset:8\n"
"v_mfma_f32_16x16x32_bf16 v[88:91], a[48:51], a[96:99], v[88:91]\n"
"ds_read_b64_tr_b16 a[90:91], v15 offset:520\n"
"v_mfma_f32_16x16x32_bf16 v[88:91], a[52:55], a[100:103], v[88:91]\n"
"ds_read_b64_tr_b16 a[92:93], v15 offset:4104\n"
"buffer_load_dwordx4 v11, s[52:55], s67 offen offset:3072  lds\n"
"v_mfma_f32_16x16x32_bf16 v[116:119], a[60:63], a[100:103], v[116:119]\n"
"ds_read_b64_tr_b16 a[94:95], v15 offset:4616\n"
"v_mfma_f32_16x16x32_bf16 v[116:119], a[56:59], a[96:99], v[116:119]\n"
"s_add_u32 m0, s64, 0\n"
"v_mfma_f32_16x16x32_bf16 v[120:123], a[56:59], a[104:107], v[120:123]\n"
"v_mfma_f32_16x16x32_bf16 v[120:123], a[60:63], a[108:111], v[120:123]\n"
"v_mfma_f32_16x16x32_bf16 v[92:95], a[52:55], a[108:111], v[92:95]\n"
"s_add_u32 s67, s75, 262272\n"
"v_mfma_f32_16x16x32_bf16 v[92:95], a[48:51], a[104:107], v[92:95]\n"
"v_mfma_f32_16x16x32_bf16 v[96:99], a[48:51], a[112:115], v[96:99]\n"
"s_waitcnt lgkmcnt(0)\n"
"v_mfma_f32_16x16x32_bf16 v[96:99], a[52:55], a[116:119], v[96:99]\n"
"buffer_load_dwordx4 v11, s[52:55], s67 offen offset:0  lds\n"
"v_mfma_f32_16x16x32_bf16 v[124:127], a[60:63], a[116:119], v[124:127]\n"
"v_mfma_f32_16x16x32_bf16 v[124:127], a[56:59], a[112:115], v[124:127]\n"
"s_add_u32 s67, s75, 277632\n"
"v_mfma_f32_16x16x32_bf16 v[128:131], a[56:59], a[120:123], v[128:131]\n"
"v_mfma_f32_16x16x32_bf16 v[128:131], a[60:63], a[124:127], v[128:131]\n"
"v_mfma_f32_16x16x32_bf16 v[100:103], a[52:55], a[124:127], v[100:103]\n"
"buffer_load_dwordx4 v11, s[52:55], s67 offen offset:1024  lds\n"
"v_mfma_f32_16x16x32_bf16 v[100:103], a[48:51], a[120:123], v[100:103]\n"
"v_mfma_f32_16x16x32_bf16 v[104:107], a[48:51], a[128:131], v[104:107]\n"
"s_add_u32 s67, s75, 292992\n"
"v_mfma_f32_16x16x32_bf16 v[104:107], a[52:55], a[132:135], v[104:107]\n"
"v_mfma_f32_16x16x32_bf16 v[132:135], a[60:63], a[132:135], v[132:135]\n"
"buffer_load_dwordx4 v11, s[52:55], s67 offen offset:2048  lds\n"
"v_mfma_f32_16x16x32_bf16 v[132:135], a[56:59], a[128:131], v[132:135]\n"
"v_mfma_f32_16x16x32_bf16 v[136:139], a[56:59], a[136:139], v[136:139]\n"
"s_add_u32 s67, s75, 308352\n"
"v_mfma_f32_16x16x32_bf16 v[136:139], a[60:63], a[140:143], v[136:139]\n"
"v_mfma_f32_16x16x32_bf16 v[108:111], a[52:55], a[140:143], v[108:111]\n"
"v_mfma_f32_16x16x32_bf16 v[108:111], a[48:51], a[136:139], v[108:111]\n"
"buffer_load_dwordx4 v11, s[52:55], s67 offen offset:3072  lds\n"
"v_mfma_f32_16x16x32_bf16 v[112:115], a[48:51], a[144:147], v[112:115]\n"
"v_mfma_f32_16x16x32_bf16 v[112:115], a[52:55], a[148:151], v[112:115]\n"
"s_add_u32 m0, s64, 4096\n"
"v_mfma_f32_16x16x32_bf16 v[140:143], a[60:63], a[148:151], v[140:143]\n"
"v_mfma_f32_16x16x32_bf16 v[140:143], a[56:59], a[144:147], v[140:143]\n"
"s_add_u32 s67, s75, 327808\n"
"v_mfma_f32_16x16x32_bf16 a[12:15], a[56:59], a[152:155], a[12:15]\n"
"v_mfma_f32_16x16x32_bf16 a[12:15], a[60:63], a[156:159], a[12:15]\n"
"buffer_load_dwordx4 v11, s[52:55], s67 offen offset:0  lds\n"
"v_mfma_f32_16x16x32_bf16 a[8:11], a[52:55], a[156:159], a[8:11]\n"
"v_mfma_f32_16x16x32_bf16 a[8:11], a[48:51], a[152:155], a[8:11]\n"
"s_add_u32 s67, s75, 343168\n"
"v_mfma_f32_16x16x32_bf16 v[144:147], a[64:67], a[96:99], v[144:147]\n"
"v_mfma_f32_16x16x32_bf16 v[144:147], a[68:71], a[100:103], v[144:147]\n"
"buffer_load_dwordx4 v11, s[52:55], s67 offen offset:1024  lds\n"
"v_mfma_f32_16x16x32_bf16 v[172:175], a[76:79], a[100:103], v[172:175]\n"
"v_mfma_f32_16x16x32_bf16 v[172:175], a[72:75], a[96:99], v[172:175]\n"
"s_add_u32 s67, s75, 358528\n"
"v_mfma_f32_16x16x32_bf16 v[176:179], a[72:75], a[104:107], v[176:179]\n"
"v_mfma_f32_16x16x32_bf16 v[176:179], a[76:79], a[108:111], v[176:179]\n"
"buffer_load_dwordx4 v11, s[52:55], s67 offen offset:2048  lds\n"
"v_mfma_f32_16x16x32_bf16 v[148:151], a[68:71], a[108:111], v[148:151]\n"
"v_mfma_f32_16x16x32_bf16 v[148:151], a[64:67], a[104:107], v[148:151]\n"
"s_add_u32 s67, s75, 373888\n"
"v_mfma_f32_16x16x32_bf16 v[152:155], a[64:67], a[112:115], v[152:155]\n"
"s_sub_u32 s66, 81920, s73\n"
"v_mfma_f32_16x16x32_bf16 v[152:155], a[68:71], a[116:119], v[152:155]\n"
"buffer_load_dwordx4 v11, s[52:55], s67 offen offset:3072  lds\n"
"v_mfma_f32_16x16x32_bf16 v[180:183], a[76:79], a[116:119], v[180:183]\n"
"v_add_u32 v12, s66, v7\n"
"v_mfma_f32_16x16x32_bf16 v[180:183], a[72:75], a[112:115], v[180:183]\n"
"s_add_u32 m0, s72, 139264\n"
"v_mfma_f32_16x16x32_bf16 v[184:187], a[72:75], a[120:123], v[184:187]\n"
"v_add_u32 v13, s66, v8\n"
"v_mfma_f32_16x16x32_bf16 v[184:187], a[76:79], a[124:127], v[184:187]\n"
"s_add_u32 s67, s60, 384\n"
"v_mfma_f32_16x16x32_bf16 v[156:159], a[68:71], a[124:127], v[156:159]\n"
"v_mfma_f32_16x16x32_bf16 v[156:159], a[64:67], a[120:123], v[156:159]\n"
"buffer_load_dwordx4 v6, s[48:51], s67 offen offset:0  lds\n"
"v_mfma_f32_16x16x32_bf16 v[160:163], a[64:67], a[128:131], v[160:163]\n"
"v_mfma_f32_16x16x32_bf16 v[160:163], a[68:71], a[132:135], v[160:163]\n"
"s_add_u32 s67, s60, 15744\n"
"v_mfma_f32_16x16x32_bf16 v[188:191], a[76:79], a[132:135], v[188:191]\n"
"v_mfma_f32_16x16x32_bf16 v[188:191], a[72:75], a[128:131], v[188:191]\n"
"buffer_load_dwordx4 v6, s[48:51], s67 offen offset:1024  lds\n"
"v_mfma_f32_16x16x32_bf16 v[192:195], a[72:75], a[136:139], v[192:195]\n"
"v_mfma_f32_16x16x32_bf16 v[192:195], a[76:79], a[140:143], v[192:195]\n"
"s_add_u32 s67, s60, 31104\n"
"v_mfma_f32_16x16x32_bf16 v[164:167], a[68:71], a[140:143], v[164:167]\n"
"v_mfma_f32_16x16x32_bf16 v[164:167], a[64:67], a[136:139], v[164:167]\n"
"buffer_load_dwordx4 v6, s[48:51], s67 offen offset:2048  lds\n"
"v_mfma_f32_16x16x32_bf16 v[168:171], a[64:67], a[144:147], v[168:171]\n"
"v_mfma_f32_16x16x32_bf16 v[168:171], a[68:71], a[148:151], v[168:171]\n"
"s_add_u32 s67, s60, 46464\n"
"v_mfma_f32_16x16x32_bf16 v[196:199], a[76:79], a[148:151], v[196:199]\n"
"v_mfma_f32_16x16x32_bf16 v[196:199], a[72:75], a[144:147], v[196:199]\n"
"v_mfma_f32_16x16x32_bf16 a[20:23], a[72:75], a[152:155], a[20:23]\n"
"buffer_load_dwordx4 v6, s[48:51], s67 offen offset:3072  lds\n"
"v_mfma_f32_16x16x32_bf16 a[20:23], a[76:79], a[156:159], a[20:23]\n"
"s_add_u32 s70, s75, 655360\n"
"v_mfma_f32_16x16x32_bf16 a[16:19], a[68:71], a[156:159], a[16:19]\n"
"s_add_u32 s70, s70, s65\n"
"v_mfma_f32_16x16x32_bf16 a[16:19], a[64:67], a[152:155], a[16:19]\n"
"s_waitcnt vmcnt(20)\n"
"s_barrier\n"
"v_mfma_f32_16x16x32_bf16 v[200:203], a[80:83], a[96:99], v[200:203]\n"
"s_waitcnt vmcnt(12)\n"
"ds_read_b64_tr_b16 a[32:33], v9 offset:8192\n"
"v_mfma_f32_16x16x32_bf16 v[200:203], a[84:87], a[100:103], v[200:203]\n"
"ds_read_b64_tr_b16 a[34:35], v9 offset:8704\n"
"buffer_load_dword a252, v14, s[52:55], s70 offen\n"
"v_mfma_f32_16x16x32_bf16 v[228:231], a[92:95], a[100:103], v[228:231]\n"
"ds_read_b128 a[100:103], v13 offset:16384\n"
"v_mfma_f32_16x16x32_bf16 v[228:231], a[88:91], a[96:99], v[228:231]\n"
"ds_read_b128 a[96:99], v12 offset:16384\n"
"v_mfma_f32_16x16x32_bf16 v[232:235], a[88:91], a[104:107], v[232:235]\n"
"ds_read_b64_tr_b16 a[36:37], v9 offset:12288\n"
"v_mfma_f32_16x16x32_bf16 v[232:235], a[92:95], a[108:111], v[232:235]\n"
"ds_read_b64_tr_b16 a[38:39], v9 offset:12800\n"
"v_mfma_f32_16x16x32_bf16 v[204:207], a[84:87], a[108:111], v[204:207]\n"
"ds_read_b128 a[108:111], v13 offset:18432\n"
"v_mfma_f32_16x16x32_bf16 v[204:207], a[80:83], a[104:107], v[204:207]\n"
"ds_read_b128 a[104:107], v12 offset:18432\n"
"s_add_u32 m0, s64, 8192\n"
"ds_read_b64_tr_b16 a[40:41], v9 offset:8200\n"
"v_mfma_f32_16x16x32_bf16 v[208:211], a[80:83], a[112:115], v[208:211]\n"
"ds_read_b64_tr_b16 a[42:43], v9 offset:8712\n"
"v_mfma_f32_16x16x32_bf16 v[208:211], a[84:87], a[116:119], v[208:211]\n"
"ds_read_b64_tr_b16 a[44:45], v9 offset:12296\n"
"v_mfma_f32_16x16x32_bf16 v[236:239], a[92:95], a[116:119], v[236:239]\n"
"ds_read_b128 a[116:119], v13 offset:20480\n"
"v_mfma_f32_16x16x32_bf16 v[236:239], a[88:91], a[112:115], v[236:239]\n"
"ds_read_b128 a[112:115], v12 offset:20480\n"
"v_mfma_f32_16x16x32_bf16 v[240:243], a[88:91], a[120:123], v[240:243]\n"
"ds_read_b64_tr_b16 a[46:47], v9 offset:12808\n"
"v_mfma_f32_16x16x32_bf16 v[240:243], a[92:95], a[124:127], v[240:243]\n"
"ds_read_b64_tr_b16 a[48:49], v15 offset:8192\n"
"v_mfma_f32_16x16x32_bf16 v[212:215], a[84:87], a[124:127], v[212:215]\n"
"ds_read_b128 a[124:127], v13 offset:22528\n"
"v_mfma_f32_16x16x32_bf16 v[212:215], a[80:83], a[120:123], v[212:215]\n"
"ds_read_b128 a[120:123], v12 offset:22528\n"
"v_mfma_f32_16x16x32_bf16 v[216:219], a[80:83], a[128:131], v[216:219]\n"
"ds_read_b64_tr_b16 a[50:51], v15 offset:8704\n"
"v_mfma_f32_16x16x32_bf16 v[216:219], a[84:87], a[132:135], v[216:219]\n"
"ds_read_b64_tr_b16 a[52:53], v15 offset:12288\n"
"v_mfma_f32_16x16x32_bf16 v[244:247], a[92:95], a[132:135], v[244:247]\n"
"ds_read_b128 a[132:135], v13 offset:24576\n"
"v_mfma_f32_16x16x32_bf16 v[244:247], a[88:91], a[128:131], v[244:247]\n"
"s_add_u32 s67, s75, 393216\n"
"ds_read_b128 a[128:131], v12 offset:24576\n"
"v_mfma_f32_16x16x32_bf16 v[248:251], a[88:91], a[136:139], v[248:251]\n"
"ds_read_b64_tr_b16 a[54:55], v15 offset:12800\n"
"v_mfma_f32_16x16x32_bf16 v[248:251], a[92:95], a[140:143], v[248:251]\n"
"s_add_u32 s66, s78, 1\n"
"ds_read_b64_tr_b16 a[56:57], v15 offset:8200\n"
"v_mfma_f32_16x16x32_bf16 v[220:223], a[84:87], a[140:143], v[220:223]\n"
"ds_read_b128 a[140:143], v13 offset:26624\n"
"v_mfma_f32_16x16x32_bf16 v[220:223], a[80:83], a[136:139], v[220:223]\n"
"ds_read_b128 a[136:139], v12 offset:26624\n"
"v_mov_b32 v28, s66\n"
"v_mfma_f32_16x16x32_bf16 v[224:227], a[80:83], a[144:147], v[224:227]\n"
"ds_read_b64_tr_b16 a[58:59], v15 offset:8712\n"
"v_mov_b32 v29, s79\n"
"v_mfma_f32_16x16x32_bf16 v[224:227], a[84:87], a[148:151], v[224:227]\n"
"ds_read_b64_tr_b16 a[60:61], v15 offset:12296\n"
"v_mfma_f32_16x16x32_bf16 v[252:255], a[92:95], a[148:151], v[252:255]\n"
"buffer_store_dwordx2 v[28:29], off, s[56:59], %24 sc1\n"
"ds_read_b128 a[148:151], v13 offset:28672\n"
"v_mfma_f32_16x16x32_bf16 v[252:255], a[88:91], a[144:147], v[252:255]\n"
"ds_read_b128 a[144:147], v12 offset:28672\n"
"ds_read_b64_tr_b16 a[62:63], v15 offset:12808\n"
"v_mfma_f32_16x16x32_bf16 a[28:31], a[88:91], a[152:155], a[28:31]\n"
"v_mfma_f32_16x16x32_bf16 a[28:31], a[92:95], a[156:159], a[28:31]\n"
"v_mfma_f32_16x16x32_bf16 a[24:27], a[84:87], a[156:159], a[24:27]\n"
"ds_read_b128 a[156:159], v13 offset:30720\n"
"v_mfma_f32_16x16x32_bf16 a[24:27], a[80:83], a[152:155], a[24:27]\n"
"s_waitcnt lgkmcnt(15)\n"
"ds_read_b128 a[152:155], v12 offset:30720\n"
"v_mfma_f32_16x16x32_bf16 v[32:35], a[32:35], a[96:99], v[32:35]\n"
"v_mfma_f32_16x16x32_bf16 v[32:35], a[36:39], a[100:103], v[32:35]\n"
"s_waitcnt lgkmcnt(2)\n"
"v_mfma_f32_16x16x32_bf16 v[60:63], a[44:47], a[100:103], v[60:63]\n"
"buffer_load_dwordx4 v11, s[52:55], s67 offen offset:0  lds\n"
"v_mfma_f32_16x16x32_bf16 v[60:63], a[40:43], a[96:99], v[60:63]\n"
"v_mfma_f32_16x16x32_bf16 v[64:67], a[40:43], a[104:107], v[64:67]\n"
"s_add_u32 s67, s75, 408576\n"
"v_mfma_f32_16x16x32_bf16 v[64:67], a[44:47], a[108:111], v[64:67]\n"
"v_mfma_f32_16x16x32_bf16 v[36:39], a[36:39], a[108:111], v[36:39]\n"
"v_mfma_f32_16x16x32_bf16 v[36:39], a[32:35], a[104:107], v[36:39]\n"
"buffer_load_dwordx4 v11, s[52:55], s67 offen offset:1024  lds\n"
"v_mfma_f32_16x16x32_bf16 v[40:43], a[32:35], a[112:115], v[40:43]\n"
"v_mfma_f32_16x16x32_bf16 v[40:43], a[36:39], a[116:119], v[40:43]\n"
"s_add_u32 s67, s75, 423936\n"
"v_mfma_f32_16x16x32_bf16 v[68:71], a[44:47], a[116:119], v[68:71]\n"
"v_mfma_f32_16x16x32_bf16 v[68:71], a[40:43], a[112:115], v[68:71]\n"
"v_mfma_f32_16x16x32_bf16 v[72:75], a[40:43], a[120:123], v[72:75]\n"
"buffer_load_dwordx4 v11, s[52:55], s67 offen offset:2048  lds\n"
"v_mfma_f32_16x16x32_bf16 v[72:75], a[44:47], a[124:127], v[72:75]\n"
"v_mfma_f32_16x16x32_bf16 v[44:47], a[36:39], a[124:127], v[44:47]\n"
"s_add_u32 s67, s75, 439296\n"
"v_mfma_f32_16x16x32_bf16 v[44:47], a[32:35], a[120:123], v[44:47]\n"
"v_mfma_f32_16x16x32_bf16 v[48:51], a[32:35], a[128:131], v[48:51]\n"
"buffer_load_dwordx4 v11, s[52:55], s67 offen offset:3072  lds\n"
"v_mfma_f32_16x16x32_bf16 v[48:51], a[36:39], a[132:135], v[48:51]\n"
"s_add_u32 m0, s64, 12288\n"
"v_mfma_f32_16x16x32_bf16 v[76:79], a[44:47], a[132:135], v[76:79]\n"
"s_waitcnt vmcnt(10)\n"
"ds_read_b64_tr_b16 a[64:65], v9 offset:0\n"
"v_mfma_f32_16x16x32_bf16 v[76:79], a[40:43], a[128:131], v[76:79]\n"
"ds_read_b64_tr_b16 a[66:67], v9 offset:512\n"
"v_mfma_f32_16x16x32_bf16 v[80:83], a[40:43], a[136:139], v[80:83]\n"
"s_add_u32 s67, s75, 458752\n"
"ds_read_b64_tr_b16 a[68:69], v9 offset:4096\n"
"v_mfma_f32_16x16x32_bf16 v[80:83], a[44:47], a[140:143], v[80:83]\n"
"ds_read_b64_tr_b16 a[70:71], v9 offset:4608\n"
"v_mfma_f32_16x16x32_bf16 v[52:55], a[36:39], a[140:143], v[52:55]\n"
"ds_read_b64_tr_b16 a[72:73], v9 offset:8\n"
"buffer_load_dwordx4 v11, s[52:55], s67 offen offset:0  lds\n"
"v_mfma_f32_16x16x32_bf16 v[52:55], a[32:35], a[136:139], v[52:55]\n"
"s_waitcnt lgkmcnt(5)\n"
"ds_read_b64_tr_b16 a[74:75], v9 offset:520\n"
"v_mfma_f32_16x16x32_bf16 v[56:59], a[32:35], a[144:147], v[56:59]\n"
"s_add_u32 s67, s75, 474112\n"
"ds_read_b64_tr_b16 a[76:77], v9 offset:4104\n"
"v_mfma_f32_16x16x32_bf16 v[56:59], a[36:39], a[148:151], v[56:59]\n"
"ds_read_b64_tr_b16 a[78:79], v9 offset:4616\n"
"v_mfma_f32_16x16x32_bf16 v[84:87], a[44:47], a[148:151], v[84:87]\n"
"buffer_load_dwordx4 v11, s[52:55], s67 offen offset:1024  lds\n"
"v_mfma_f32_16x16x32_bf16 v[84:87], a[40:43], a[144:147], v[84:87]\n"
"ds_read_b64_tr_b16 a[80:81], v15 offset:0\n"
"v_mfma_f32_16x16x32_bf16 a[4:7], a[40:43], a[152:155], a[4:7]\n"
"s_add_u32 s67, s75, 489472\n"
"ds_read_b64_tr_b16 a[82:83], v15 offset:512\n"
"v_mfma_f32_16x16x32_bf16 a[4:7], a[44:47], a[156:159], a[4:7]\n"
"ds_read_b64_tr_b16 a[84:85], v15 offset:4096\n"
"v_mfma_f32_16x16x32_bf16 a[0:3], a[36:39], a[156:159], a[0:3]\n"
"ds_read_b64_tr_b16 a[86:87], v15 offset:4608\n"
"buffer_load_dwordx4 v11, s[52:55], s67 offen offset:2048  lds\n"
"v_mfma_f32_16x16x32_bf16 a[0:3], a[32:35], a[152:155], a[0:3]\n"
"ds_read_b64_tr_b16 a[88:89], v15 offset:8\n"
"v_mfma_f32_16x16x32_bf16 v[88:91], a[48:51], a[96:99], v[88:91]\n"
"s_add_u32 s67, s75, 504832\n"
"ds_read_b64_tr_b16 a[90:91], v15 offset:520\n"
"v_mfma_f32_16x16x32_bf16 v[88:91], a[52:55], a[100:103], v[88:91]\n"
"ds_read_b64_tr_b16 a[92:93], v15 offset:4104\n"
"v_mfma_f32_16x16x32_bf16 v[116:119], a[60:63], a[100:103], v[116:119]\n"
"ds_read_b64_tr_b16 a[94:95], v15 offset:4616\n"
"v_mfma_f32_16x16x32_bf16 v[116:119], a[56:59], a[96:99], v[116:119]\n"
"buffer_load_dwordx4 v11, s[52:55], s67 offen offset:3072  lds\n"
"v_mfma_f32_16x16x32_bf16 v[120:123], a[56:59], a[104:107], v[120:123]\n"
"v_mfma_f32_16x16x32_bf16 v[120:123], a[60:63], a[108:111], v[120:123]\n"
"s_add_u32 m0, s64, 0\n"
"v_mfma_f32_16x16x32_bf16 v[92:95], a[52:55], a[108:111], v[92:95]\n"
"v_mfma_f32_16x16x32_bf16 v[92:95], a[48:51], a[104:107], v[92:95]\n"
"s_add_u32 s67, s75, 393344\n"
"v_mfma_f32_16x16x32_bf16 v[96:99], a[48:51], a[112:115], v[96:99]\n"
"s_waitcnt lgkmcnt(0)\n"
"v_mfma_f32_16x16x32_bf16 v[96:99], a[52:55], a[116:119], v[96:99]\n"
"buffer_load_dwordx4 v11, s[52:55], s67 offen offset:0  lds\n"
"v_mfma_f32_16x16x32_bf16 v[124:127], a[60:63], a[116:119], v[124:127]\n"
"v_mfma_f32_16x16x32_bf16 v[124:127], a[56:59], a[112:115], v[124:127]\n"
"s_add_u32 s67, s75, 408704\n"
"v_mfma_f32_16x16x32_bf16 v[128:131], a[56:59], a[120:123], v[128:131]\n"
"v_mfma_f32_16x16x32_bf16 v[128:131], a[60:63], a[124:127], v[128:131]\n"
"v_mfma_f32_16x16x32_bf16 v[100:103], a[52:55], a[124:127], v[100:103]\n"
"buffer_load_dwordx4 v11, s[52:55], s67 offen offset:1024  lds\n"
"v_mfma_f32_16x16x32_bf16 v[100:103], a[48:51], a[120:123], v[100:103]\n"
"v_mfma_f32_16x16x32_bf16 v[104:107], a[48:51], a[128:131], v[104:107]\n"
"s_add_u32 s67, s75, 424064\n"
"v_mfma_f32_16x16x32_bf16 v[104:107], a[52:55], a[132:135], v[104:107]\n"
"v_mfma_f32_16x16x32_bf16 v[132:135], a[60:63], a[132:135], v[132:135]\n"
"v_mfma_f32_16x16x32_bf16 v[132:135], a[56:59], a[128:131], v[132:135]\n"
"buffer_load_dwordx4 v11, s[52:55], s67 offen offset:2048  lds\n"
"v_mfma_f32_16x16x32_bf16 v[136:139], a[56:59], a[136:139], v[136:139]\n"
"v_mfma_f32_16x16x32_bf16 v[136:139], a[60:63], a[140:143], v[136:139]\n"
"s_add_u32 s67, s75, 439424\n"
"v_mfma_f32_16x16x32_bf16 v[108:111], a[52:55], a[140:143], v[108:111]\n"
"v_mfma_f32_16x16x32_bf16 v[108:111], a[48:51], a[136:139], v[108:111]\n"
"buffer_load_dwordx4 v11, s[52:55], s67 offen offset:3072  lds\n"
"v_mfma_f32_16x16x32_bf16 v[112:115], a[48:51], a[144:147], v[112:115]\n"
"v_mfma_f32_16x16x32_bf16 v[112:115], a[52:55], a[148:151], v[112:115]\n"
"s_add_u32 m0, s64, 4096\n"
"v_mfma_f32_16x16x32_bf16 v[140:143], a[60:63], a[148:151], v[140:143]\n"
"v_mfma_f32_16x16x32_bf16 v[140:143], a[56:59], a[144:147], v[140:143]\n"
"s_add_u32 s67, s75, 458880\n"
"v_mfma_f32_16x16x32_bf16 a[12:15], a[56:59], a[152:155], a[12:15]\n"
"v_mfma_f32_16x16x32_bf16 a[12:15], a[60:63], a[156:159], a[12:15]\n"
"buffer_load_dwordx4 v11, s[52:55], s67 offen offset:0  lds\n"
"v_mfma_f32_16x16x32_bf16 a[8:11], a[52:55], a[156:159], a[8:11]\n"
"v_mfma_f32_16x16x32_bf16 a[8:11], a[48:51], a[152:155], a[8:11]\n"
"s_add_u32 s67, s75, 474240\n"
"v_mfma_f32_16x16x32_bf16 v[144:147], a[64:67], a[96:99], v[144:147]\n"
"v_mfma_f32_16x16x32_bf16 v[144:147], a[68:71], a[100:103], v[144:147]\n"
"buffer_load_dwordx4 v11, s[52:55], s67 offen offset:1024  lds\n"
"v_mfma_f32_16x16x32_bf16 v[172:175], a[76:79], a[100:103], v[172:175]\n"
"v_mfma_f32_16x16x32_bf16 v[172:175], a[72:75], a[96:99], v[172:175]\n"
"s_add_u32 s67, s75, 489600\n"
"v_mfma_f32_16x16x32_bf16 v[176:179], a[72:75], a[104:107], v[176:179]\n"
"v_mfma_f32_16x16x32_bf16 v[176:179], a[76:79], a[108:111], v[176:179]\n"
"buffer_load_dwordx4 v11, s[52:55], s67 offen offset:2048  lds\n"
"v_mfma_f32_16x16x32_bf16 v[148:151], a[68:71], a[108:111], v[148:151]\n"
"v_mfma_f32_16x16x32_bf16 v[148:151], a[64:67], a[104:107], v[148:151]\n"
"s_add_u32 s67, s75, 504960\n"
"v_mfma_f32_16x16x32_bf16 v[152:155], a[64:67], a[112:115], v[152:155]\n"
"s_sub_u32 s66, 122880, s73\n"
"v_mfma_f32_16x16x32_bf16 v[152:155], a[68:71], a[116:119], v[152:155]\n"
"v_mfma_f32_16x16x32_bf16 v[180:183], a[76:79], a[116:119], v[180:183]\n"
"v_add_u32 v12, s66, v7\n"
"v_mfma_f32_16x16x32_bf16 v[180:183], a[72:75], a[112:115], v[180:183]\n"
"buffer_load_dwordx4 v11, s[52:55], s67 offen offset:3072  lds\n"
"v_mfma_f32_16x16x32_bf16 v[184:187], a[72:75], a[120:123], v[184:187]\n"
"s_add_u32 m0, s72, 16384\n"
"v_add_u32 v13, s66, v8\n"
"v_mfma_f32_16x16x32_bf16 v[184:187], a[76:79], a[124:127], v[184:187]\n"
"v_mfma_f32_16x16x32_bf16 v[156:159], a[68:71], a[124:127], v[156:159]\n"
"s_add_u32 s67, s60, 512\n"
"v_mfma_f32_16x16x32_bf16 v[156:159], a[64:67], a[120:123], v[156:159]\n"
"buffer_load_dwordx4 v6, s[48:51], s67 offen offset:0  lds\n"
"v_mfma_f32_16x16x32_bf16 v[160:163], a[64:67], a[128:131], v[160:163]\n"
"v_mfma_f32_16x16x32_bf16 v[160:163], a[68:71], a[132:135], v[160:163]\n"
"s_add_u32 s67, s60, 15872\n"
"v_mfma_f32_16x16x32_bf16 v[188:191], a[76:79], a[132:135], v[188:191]\n"
"v_mfma_f32_16x16x32_bf16 v[188:191], a[72:75], a[128:131], v[188:191]\n"
"buffer_load_dwordx4 v6, s[48:51], s67 offen offset:1024  lds\n"
"v_mfma_f32_16x16x32_bf16 v[192:195], a[72:75], a[136:139], v[192:195]\n"
"v_mfma_f32_16x16x32_bf16 v[192:195], a[76:79], a[140:143], v[192:195]\n"
"s_add_u32 s67, s60, 31232\n"
"v_mfma_f32_16x16x32_bf16 v[164:167], a[68:71], a[140:143], v[164:167]\n"
"v_mfma_f32_16x16x32_bf16 v[164:167], a[64:67], a[136:139], v[164:167]\n"
"buffer_load_dwordx4 v6, s[48:51], s67 offen offset:2048  lds\n"
"v_mfma_f32_16x16x32_bf16 v[168:171], a[64:67], a[144:147], v[168:171]\n"
"v_mfma_f32_16x16x32_bf16 v[168:171], a[68:71], a[148:151], v[168:171]\n"
"s_add_u32 s67, s60, 46592\n"
"v_mfma_f32_16x16x32_bf16 v[196:199], a[76:79], a[148:151], v[196:199]\n"
"v_mfma_f32_16x16x32_bf16 v[196:199], a[72:75], a[144:147], v[196:199]\n"
"v_mfma_f32_16x16x32_bf16 a[20:23], a[72:75], a[152:155], a[20:23]\n"
"buffer_load_dwordx4 v6, s[48:51], s67 offen offset:3072  lds\n"
"v_mfma_f32_16x16x32_bf16 a[20:23], a[76:79], a[156:159], a[20:23]\n"
"v_mfma_f32_16x16x32_bf16 a[16:19], a[68:71], a[156:159], a[16:19]\n"
"s_add_u32 s70, s75, 786432\n"
"s_add_u32 s70, s70, s65\n"
"v_mfma_f32_16x16x32_bf16 a[16:19], a[64:67], a[152:155], a[16:19]\n"
"s_waitcnt vmcnt(22)\n"
"s_barrier\n"
"v_mfma_f32_16x16x32_bf16 v[200:203], a[80:83], a[96:99], v[200:203]\n"
"s_waitcnt vmcnt(12)\n"
"ds_read_b64_tr_b16 a[32:33], v9 offset:8192\n"
"v_mfma_f32_16x16x32_bf16 v[200:203], a[84:87], a[100:103], v[200:203]\n"
"ds_read_b64_tr_b16 a[34:35], v9 offset:8704\n"
"v_mfma_f32_16x16x32_bf16 v[228:231], a[92:95], a[100:103], v[228:231]\n"
"ds_read_b128 a[100:103], v13 offset:16384\n"
"buffer_load_dword a252, v14, s[52:55], s70 offen\n"
"v_mfma_f32_16x16x32_bf16 v[228:231], a[88:91], a[96:99], v[228:231]\n"
"ds_read_b128 a[96:99], v12 offset:16384\n"
"v_mfma_f32_16x16x32_bf16 v[232:235], a[88:91], a[104:107], v[232:235]\n"
"ds_read_b64_tr_b16 a[36:37], v9 offset:12288\n"
"v_mfma_f32_16x16x32_bf16 v[232:235], a[92:95], a[108:111], v[232:235]\n"
"ds_read_b64_tr_b16 a[38:39], v9 offset:12800\n"
"v_mfma_f32_16x16x32_bf16 v[204:207], a[84:87], a[108:111], v[204:207]\n"
"ds_read_b128 a[108:111], v13 offset:18432\n"
"v_mfma_f32_16x16x32_bf16 v[204:207], a[80:83], a[104:107], v[204:207]\n"
"ds_read_b128 a[104:107], v12 offset:18432\n"
"ds_read_b64_tr_b16 a[40:41], v9 offset:8200\n"
"v_mfma_f32_16x16x32_bf16 v[208:211], a[80:83], a[112:115], v[208:211]\n"
"s_add_u32 m0, s64, 8192\n"
"ds_read_b64_tr_b16 a[42:43], v9 offset:8712\n"
"v_mfma_f32_16x16x32_bf16 v[208:211], a[84:87], a[116:119], v[208:211]\n"
"ds_read_b64_tr_b16 a[44:45], v9 offset:12296\n"
"v_mfma_f32_16x16x32_bf16 v[236:239], a[92:95], a[116:119], v[236:239]\n"
"ds_read_b128 a[116:119], v13 offset:20480\n"
"v_mfma_f32_16x16x32_bf16 v[236:239], a[88:91], a[112:115], v[236:239]\n"
"ds_read_b128 a[112:115], v12 offset:20480\n"
"v_mfma_f32_16x16x32_bf16 v[240:243], a[88:91], a[120:123], v[240:243]\n"
"L_st1062:\n"
"s_cbranch_execz L_st3242\n"
"ds_read_b64_tr_b16 a[46:47], v9 offset:12808\n"
"v_mfma_f32_16x16x32_bf16 v[240:243], a[92:95], a[124:127], v[240:243]\n"
"ds_read_b64_tr_b16 a[48:49], v15 offset:8192\n"
"v_mfma_f32_16x16x32_bf16 v[212:215], a[84:87], a[124:127], v[212:215]\n"
"ds_read_b128 a[124:127], v13 offset:22528\n"
"v_mfma_f32_16x16x32_bf16 v[212:215], a[80:83], a[120:123], v[212:215]\n"
"ds_read_b128 a[120:123], v12 offset:22528\n"
"v_mfma_f32_16x16x32_bf16 v[216:219], a[80:83], a[128:131], v[216:219]\n"
"ds_read_b64_tr_b16 a[50:51], v15 offset:8704\n"
"v_mfma_f32_16x16x32_bf16 v[216:219], a[84:87], a[132:135], v[216:219]\n"
"ds_read_b64_tr_b16 a[52:53], v15 offset:12288\n"
"v_mfma_f32_16x16x32_bf16 v[244:247], a[92:95], a[132:135], v[244:247]\n"
"ds_read_b128 a[132:135], v13 offset:24576\n"
"v_mfma_f32_16x16x32_bf16 v[244:247], a[88:91], a[128:131], v[244:247]\n"
"s_add_u32 s67, s75, 524288\n"
"ds_read_b128 a[128:131], v12 offset:24576\n"
"v_mfma_f32_16x16x32_bf16 v[248:251], a[88:91], a[136:139], v[248:251]\n"
"ds_read_b64_tr_b16 a[54:55], v15 offset:12800\n"
"v_mfma_f32_16x16x32_bf16 v[248:251], a[92:95], a[140:143], v[248:251]\n"
"ds_read_b64_tr_b16 a[56:57], v15 offset:8200\n"
"v_mfma_f32_16x16x32_bf16 v[220:223], a[84:87], a[140:143], v[220:223]\n"
"ds_read_b128 a[140:143], v13 offset:26624\n"
"v_mfma_f32_16x16x32_bf16 v[220:223], a[80:83], a[136:139], v[220:223]\n"
"ds_read_b128 a[136:139], v12 offset:26624\n"
"v_mfma_f32_16x16x32_bf16 v[224:227], a[80:83], a[144:147], v[224:227]\n"
"ds_read_b64_tr_b16 a[58:59], v15 offset:8712\n"
"v_mfma_f32_16x16x32_bf16 v[224:227], a[84:87], a[148:151], v[224:227]\n"
"ds_read_b64_tr_b16 a[60:61], v15 offset:12296\n"
"v_mfma_f32_16x16x32_bf16 v[252:255], a[92:95], a[148:151], v[252:255]\n"
"ds_read_b128 a[148:151], v13 offset:28672\n"
"v_mfma_f32_16x16x32_bf16 v[252:255], a[88:91], a[144:147], v[252:255]\n"
"ds_read_b128 a[144:147], v12 offset:28672\n"
"ds_read_b64_tr_b16 a[62:63], v15 offset:12808\n"
"v_mfma_f32_16x16x32_bf16 a[28:31], a[88:91], a[152:155], a[28:31]\n"
"v_mfma_f32_16x16x32_bf16 a[28:31], a[92:95], a[156:159], a[28:31]\n"
"v_mfma_f32_16x16x32_bf16 a[24:27], a[84:87], a[156:159], a[24:27]\n"
"ds_read_b128 a[156:159], v13 offset:30720\n"
"v_mfma_f32_16x16x32_bf16 a[24:27], a[80:83], a[152:155], a[24:27]\n"
"s_waitcnt lgkmcnt(15)\n"
"ds_read_b128 a[152:155], v12 offset:30720\n"
"v_mfma_f32_16x16x32_bf16 v[32:35], a[32:35], a[96:99], v[32:35]\n"
"v_mfma_f32_16x16x32_bf16 v[32:35], a[36:39], a[100:103], v[32:35]\n"
"s_waitcnt lgkmcnt(2)\n"
"v_mfma_f32_16x16x32_bf16 v[60:63], a[44:47], a[100:103], v[60:63]\n"
"buffer_load_dwordx4 v11, s[52:55], s67 offen offset:0  lds\n"
"v_mfma_f32_16x16x32_bf16 v[60:63], a[40:43], a[96:99], v[60:63]\n"
"v_mfma_f32_16x16x32_bf16 v[64:67], a[40:43], a[104:107], v[64:67]\n"
"s_add_u32 s67, s75, 539648\n"
"v_mfma_f32_16x16x32_bf16 v[64:67], a[44:47], a[108:111], v[64:67]\n"
"v_mfma_f32_16x16x32_bf16 v[36:39], a[36:39], a[108:111], v[36:39]\n"
"buffer_load_dwordx4 v11, s[52:55], s67 offen offset:1024  lds\n"
"v_mfma_f32_16x16x32_bf16 v[36:39], a[32:35], a[104:107], v[36:39]\n"
"v_mfma_f32_16x16x32_bf16 v[40:43], a[32:35], a[112:115], v[40:43]\n"
"s_add_u32 s67, s75, 555008\n"
"v_mfma_f32_16x16x32_bf16 v[40:43], a[36:39], a[116:119], v[40:43]\n"
"v_mfma_f32_16x16x32_bf16 v[68:71], a[44:47], a[116:119], v[68:71]\n"
"v_mfma_f32_16x16x32_bf16 v[68:71], a[40:43], a[112:115], v[68:71]\n"
"buffer_load_dwordx4 v11, s[52:55], s67 offen offset:2048  lds\n"
"v_mfma_f32_16x16x32_bf16 v[72:75], a[40:43], a[120:123], v[72:75]\n"
"v_mfma_f32_16x16x32_bf16 v[72:75], a[44:47], a[124:127], v[72:75]\n"
"s_add_u32 s67, s75, 570368\n"
"v_mfma_f32_16x16x32_bf16 v[44:47], a[36:39], a[124:127], v[44:47]\n"
"v_mfma_f32_16x16x32_bf16 v[44:47], a[32:35], a[120:123], v[44:47]\n"
"buffer_load_dwordx4 v11, s[52:55], s67 offen offset:3072  lds\n"
"v_mfma_f32_16x16x32_bf16 v[48:51], a[32:35], a[128:131], v[48:51]\n"
"v_mfma_f32_16x16x32_bf16 v[48:51], a[36:39], a[132:135], v[48:51]\n"
"s_add_u32 m0, s64, 12288\n"
"v_mfma_f32_16x16x32_bf16 v[76:79], a[44:47], a[132:135], v[76:79]\n"
"s_waitcnt vmcnt(9)\n"
"ds_read_b64_tr_b16 a[64:65], v9 offset:0\n"
"v_mfma_f32_16x16x32_bf16 v[76:79], a[40:43], a[128:131], v[76:79]\n"
"s_add_u32 s67, s75, 589824\n"
"ds_read_b64_tr_b16 a[66:67], v9 offset:512\n"
"v_mfma_f32_16x16x32_bf16 v[80:83], a[40:43], a[136:139], v[80:83]\n"
"ds_read_b64_tr_b16 a[68:69], v9 offset:4096\n"
"v_mfma_f32_16x16x32_bf16 v[80:83], a[44:47], a[140:143], v[80:83]\n"
"ds_read_b64_tr_b16 a[70:71], v9 offset:4608\n"
"buffer_load_dwordx4 v11, s[52:55], s67 offen offset:0  lds\n"
"v_mfma_f32_16x16x32_bf16 v[52:55], a[36:39], a[140:143], v[52:55]\n"
"s_add_u32 s67, s75, 605184\n"
"ds_read_b64_tr_b16 a[72:73], v9 offset:8\n"
"v_mfma_f32_16x16x32_bf16 v[52:55], a[32:35], a[136:139], v[52:55]\n"
"s_waitcnt lgkmcnt(5)\n"
"ds_read_b64_tr_b16 a[74:75], v9 offset:520\n"
"v_mfma_f32_16x16x32_bf16 v[56:59], a[32:35], a[144:147], v[56:59]\n"
"ds_read_b64_tr_b16 a[76:77], v9 offset:4104\n"
"v_mfma_f32_16x16x32_bf16 v[56:59], a[36:39], a[148:151], v[56:59]\n"
"ds_read_b64_tr_b16 a[78:79], v9 offset:4616\n"
"v_mfma_f32_16x16x32_bf16 v[84:87], a[44:47], a[148:151], v[84:87]\n"
"buffer_load_dwordx4 v11, s[52:55], s67 offen offset:1024  lds\n"
"v_mfma_f32_16x16x32_bf16 v[84:87], a[40:43], a[144:147], v[84:87]\n"
"ds_read_b64_tr_b16 a[80:81], v15 offset:0\n"
"v_mfma_f32_16x16x32_bf16 a[4:7], a[40:43], a[152:155], a[4:7]\n"
"s_add_u32 s67, s75, 620544\n"
"ds_read_b64_tr_b16 a[82:83], v15 offset:512\n"
"v_mfma_f32_16x16x32_bf16 a[4:7], a[44:47], a[156:159], a[4:7]\n"
"ds_read_b64_tr_b16 a[84:85], v15 offset:4096\n"
"v_mfma_f32_16x16x32_bf16 a[0:3], a[36:39], a[156:159], a[0:3]\n"
"ds_read_b64_tr_b16 a[86:87], v15 offset:4608\n"
"buffer_load_dwordx4 v11, s[52:55], s67 offen offset:2048  lds\n"
"v_mfma_f32_16x16x32_bf16 a[0:3], a[32:35], a[152:155], a[0:3]\n"
"ds_read_b64_tr_b16 a[88:89], v15 offset:8\n"
"v_mfma_f32_16x16x32_bf16 v[88:91], a[48:51], a[96:99], v[88:91]\n"
"s_add_u32 s67, s75, 635904\n"
"ds_read_b64_tr_b16 a[90:91], v15 offset:520\n"
"v_mfma_f32_16x16x32_bf16 v[88:91], a[52:55], a[100:103], v[88:91]\n"
"ds_read_b64_tr_b16 a[92:93], v15 offset:4104\n"
"v_mfma_f32_16x16x32_bf16 v[116:119], a[60:63], a[100:103], v[116:119]\n"
"ds_read_b64_tr_b16 a[94:95], v15 offset:4616\n"
"v_mfma_f32_16x16x32_bf16 v[116:119], a[56:59], a[96:99], v[116:119]\n"
"buffer_load_dwordx4 v11, s[52:55], s67 offen offset:3072  lds\n"
"v_mfma_f32_16x16x32_bf16 v[120:123], a[56:59], a[104:107], v[120:123]\n"
"v_mfma_f32_16x16x32_bf16 v[120:123], a[60:63], a[108:111], v[120:123]\n"
"s_add_u32 m0, s64, 0\n"
"v_mfma_f32_16x16x32_bf16 v[92:95], a[52:55], a[108:111], v[92:95]\n"
"v_mfma_f32_16x16x32_bf16 v[92:95], a[48:51], a[104:107], v[92:95]\n"
"s_add_u32 s67, s75, 524416\n"
"v_mfma_f32_16x16x32_bf16 v[96:99], a[48:51], a[112:115], v[96:99]\n"
"s_waitcnt lgkmcnt(0)\n"
"v_mfma_f32_16x16x32_bf16 v[96:99], a[52:55], a[116:119], v[96:99]\n"
"buffer_load_dwordx4 v11, s[52:55], s67 offen offset:0  lds\n"
"v_mfma_f32_16x16x32_bf16 v[124:127], a[60:63], a[116:119], v[124:127]\n"
"v_mfma_f32_16x16x32_bf16 v[124:127], a[56:59], a[112:115], v[124:127]\n"
"s_add_u32 s67, s75, 539776\n"
"v_mfma_f32_16x16x32_bf16 v[128:131], a[56:59], a[120:123], v[128:131]\n"
"v_mfma_f32_16x16x32_bf16 v[128:131], a[60:63], a[124:127], v[128:131]\n"
"v_mfma_f32_16x16x32_bf16 v[100:103], a[52:55], a[124:127], v[100:103]\n"
"buffer_load_dwordx4 v11, s[52:55], s67 offen offset:1024  lds\n"
"v_mfma_f32_16x16x32_bf16 v[100:103], a[48:51], a[120:123], v[100:103]\n"
"v_mfma_f32_16x16x32_bf16 v[104:107], a[48:51], a[128:131], v[104:107]\n"
"s_add_u32 s67, s75, 555136\n"
"v_mfma_f32_16x16x32_bf16 v[104:107], a[52:55], a[132:135], v[104:107]\n"
"v_mfma_f32_16x16x32_bf16 v[132:135], a[60:63], a[132:135], v[132:135]\n"
"v_mfma_f32_16x16x32_bf16 v[132:135], a[56:59], a[128:131], v[132:135]\n"
"buffer_load_dwordx4 v11, s[52:55], s67 offen offset:2048  lds\n"
"v_mfma_f32_16x16x32_bf16 v[136:139], a[56:59], a[136:139], v[136:139]\n"
"v_mfma_f32_16x16x32_bf16 v[136:139], a[60:63], a[140:143], v[136:139]\n"
"s_add_u32 s67, s75, 570496\n"
"v_mfma_f32_16x16x32_bf16 v[108:111], a[52:55], a[140:143], v[108:111]\n"
"v_mfma_f32_16x16x32_bf16 v[108:111], a[48:51], a[136:139], v[108:111]\n"
"buffer_load_dwordx4 v11, s[52:55], s67 offen offset:3072  lds\n"
"v_mfma_f32_16x16x32_bf16 v[112:115], a[48:51], a[144:147], v[112:115]\n"
"v_mfma_f32_16x16x32_bf16 v[112:115], a[52:55], a[148:151], v[112:115]\n"
"s_add_u32 m0, s64, 4096\n"
"v_mfma_f32_16x16x32_bf16 v[140:143], a[60:63], a[148:151], v[140:143]\n"
"v_mfma_f32_16x16x32_bf16 v[140:143], a[56:59], a[144:147], v[140:143]\n"
"s_add_u32 s67, s75, 589952\n"
"v_mfma_f32_16x16x32_bf16 a[12:15], a[56:59], a[152:155], a[12:15]\n"
"v_mfma_f32_16x16x32_bf16 a[12:15], a[60:63], a[156:159], a[12:15]\n"
"buffer_load_dwordx4 v11, s[52:55], s67 offen offset:0  lds\n"
"v_mfma_f32_16x16x32_bf16 a[8:11], a[52:55], a[156:159], a[8:11]\n"
"v_mfma_f32_16x16x32_bf16 a[8:11], a[48:51], a[152:155], a[8:11]\n"
"s_add_u32 s67, s75, 605312\n"
"v_mfma_f32_16x16x32_bf16 v[144:147], a[64:67], a[96:99], v[144:147]\n"
"v_mfma_f32_16x16x32_bf16 v[144:147], a[68:71], a[100:103], v[144:147]\n"
"buffer_load_dwordx4 v11, s[52:55], s67 offen offset:1024  lds\n"
"v_mfma_f32_16x16x32_bf16 v[172:175], a[76:79], a[100:103], v[172:175]\n"
"v_mfma_f32_16x16x32_bf16 v[172:175], a[72:75], a[96:99], v[172:175]\n"
"s_add_u32 s67, s75, 620672\n"
"v_mfma_f32_16x16x32_bf16 v[176:179], a[72:75], a[104:107], v[176:179]\n"
"v_mfma_f32_16x16x32_bf16 v[176:179], a[76:79], a[108:111], v[176:179]\n"
"buffer_load_dwordx4 v11, s[52:55], s67 offen offset:2048  lds\n"
"v_mfma_f32_16x16x32_bf16 v[148:151], a[68:71], a[108:111], v[148:151]\n"
"v_mfma_f32_16x16x32_bf16 v[148:151], a[64:67], a[104:107], v[148:151]\n"
"s_add_u32 s67, s75, 636032\n"
"v_mfma_f32_16x16x32_bf16 v[152:155], a[64:67], a[112:115], v[152:155]\n"
"s_sub_u32 s66, 0, s73\n"
"v_mfma_f32_16x16x32_bf16 v[152:155], a[68:71], a[116:119], v[152:155]\n"
"v_mfma_f32_16x16x32_bf16 v[180:183], a[76:79], a[116:119], v[180:183]\n"
"v_add_u32 v12, s66, v7\n"
"v_mfma_f32_16x16x32_bf16 v[180:183], a[72:75], a[112:115], v[180:183]\n"
"buffer_load_dwordx4 v11, s[52:55], s67 offen offset:3072  lds\n"
"v_mfma_f32_16x16x32_bf16 v[184:187], a[72:75], a[120:123], v[184:187]\n"
"s_add_u32 m0, s72, 57344\n"
"v_add_u32 v13, s66, v8\n"
"v_mfma_f32_16x16x32_bf16 v[184:187], a[76:79], a[124:127], v[184:187]\n"
"v_mfma_f32_16x16x32_bf16 v[156:159], a[68:71], a[124:127], v[156:159]\n"
"s_add_u32 s67, s60, 640\n"
"v_mfma_f32_16x16x32_bf16 v[156:159], a[64:67], a[120:123], v[156:159]\n"
"buffer_load_dwordx4 v6, s[48:51], s67 offen offset:0  lds\n"
"v_mfma_f32_16x16x32_bf16 v[160:163], a[64:67], a[128:131], v[160:163]\n"
"v_mfma_f32_16x16x32_bf16 v[160:163], a[68:71], a[132:135], v[160:163]\n"
"s_add_u32 s67, s60, 16000\n"
"v_mfma_f32_16x16x32_bf16 v[188:191], a[76:79], a[132:135], v[188:191]\n"
"v_mfma_f32_16x16x32_bf16 v[188:191], a[72:75], a[128:131], v[188:191]\n"
"buffer_load_dwordx4 v6, s[48:51], s67 offen offset:1024  lds\n"
"v_mfma_f32_16x16x32_bf16 v[192:195], a[72:75], a[136:139], v[192:195]\n"
"v_mfma_f32_16x16x32_bf16 v[192:195], a[76:79], a[140:143], v[192:195]\n"
"s_add_u32 s67, s60, 31360\n"
"v_mfma_f32_16x16x32_bf16 v[164:167], a[68:71], a[140:143], v[164:167]\n"
"v_mfma_f32_16x16x32_bf16 v[164:167], a[64:67], a[136:139], v[164:167]\n"
"buffer_load_dwordx4 v6, s[48:51], s67 offen offset:2048  lds\n"
"v_mfma_f32_16x16x32_bf16 v[168:171], a[64:67], a[144:147], v[168:171]\n"
"v_mfma_f32_16x16x32_bf16 v[168:171], a[68:71], a[148:151], v[168:171]\n"
"s_add_u32 s67, s60, 46720\n"
"v_mfma_f32_16x16x32_bf16 v[196:199], a[76:79], a[148:151], v[196:199]\n"
"v_mfma_f32_16x16x32_bf16 v[196:199], a[72:75], a[144:147], v[196:199]\n"
"v_mfma_f32_16x16x32_bf16 a[20:23], a[72:75], a[152:155], a[20:23]\n"
"buffer_load_dwordx4 v6, s[48:51], s67 offen offset:3072  lds\n"
"v_mfma_f32_16x16x32_bf16 a[20:23], a[76:79], a[156:159], a[20:23]\n"
"s_sub_u32 s70, 1048576, s75\n"
"v_and_b32 v19, 0x20000, v14\n"
"v_mfma_f32_16x16x32_bf16 a[16:19], a[68:71], a[156:159], a[16:19]\n"
"v_add_u32 v14, v19, v14\n"
"v_mfma_f32_16x16x32_bf16 a[16:19], a[64:67], a[152:155], a[16:19]\n"
"s_waitcnt vmcnt(21)\n"
"s_barrier\n"
"v_mfma_f32_16x16x32_bf16 v[200:203], a[80:83], a[96:99], v[200:203]\n"
"s_waitcnt vmcnt(12)\n"
"ds_read_b64_tr_b16 a[32:33], v9 offset:8192\n"
"v_mfma_f32_16x16x32_bf16 v[200:203], a[84:87], a[100:103], v[200:203]\n"
"s_add_u32 s70, s70, s65\n"
"ds_read_b64_tr_b16 a[34:35], v9 offset:8704\n"
"s_getpc_b64 s[96:97]\n"
"L_c10_:\n"
"s_add_u32 s96, s96, L_H6S-L_c10_\n"
"s_addc_u32 s97, s97, 0\n"
"v_mfma_f32_16x16x32_bf16 v[228:231], a[92:95], a[100:103], v[228:231]\n"
"s_lshr_b32 s66, %26, 3\n"
"ds_read_b128 a[100:103], v13 offset:16384\n"
"buffer_load_dword a252, v14, s[52:55], s70 offen\n"
"v_mfma_f32_16x16x32_bf16 v[228:231], a[88:91], a[96:99], v[228:231]\n"
"s_lshl2_add_u32 s66, s66, %16\n"
"ds_read_b128 a[96:99], v12 offset:16384\n"
"v_mfma_f32_16x16x32_bf16 v[232:235], a[88:91], a[104:107], v[232:235]\n"
"s_mul_i32 s66, s66, 128\n"
"ds_read_b64_tr_b16 a[36:37], v9 offset:12288\n"
"v_mfma_f32_16x16x32_bf16 v[232:235], a[92:95], a[108:111], v[232:235]\n"
"s_min_u32 s66, s66, L_headF-L_H6S-128\n"
"ds_read_b64_tr_b16 a[38:39], v9 offset:12800\n"
"v_min_u32 v22, 0, v0\n"
"v_mfma_f32_16x16x32_bf16 v[204:207], a[84:87], a[108:111], v[204:207]\n"
"ds_read_b128 a[108:111], v13 offset:18432\n"
"v_lshl_add_u32 v22, v22, 3, s66\n"
"v_mfma_f32_16x16x32_bf16 v[204:207], a[80:83], a[104:107], v[204:207]\n"
"ds_read_b128 a[104:107], v12 offset:18432\n"
"global_load_dword v23, v22, s[96:97]\n"
"ds_read_b64_tr_b16 a[40:41], v9 offset:8200\n"
"v_mfma_f32_16x16x32_bf16 v[208:211], a[80:83], a[112:115], v[208:211]\n"
"s_add_u32 m0, s64, 8192\n"
"ds_read_b64_tr_b16 a[42:43], v9 offset:8712\n"
"v_mfma_f32_16x16x32_bf16 v[208:211], a[84:87], a[116:119], v[208:211]\n"
"ds_read_b64_tr_b16 a[44:45], v9 offset:12296\n"
"v_mfma_f32_16x16x32_bf16 v[236:239], a[92:95], a[116:119], v[236:239]\n"
"ds_read_b128 a[116:119], v13 offset:20480\n"
"v_mfma_f32_16x16x32_bf16 v[236:239], a[88:91], a[112:115], v[236:239]\n"
"ds_read_b128 a[112:115], v12 offset:20480\n"
"v_mfma_f32_16x16x32_bf16 v[240:243], a[88:91], a[120:123], v[240:243]\n"
"ds_read_b64_tr_b16 a[46:47], v9 offset:12808\n"
"v_mfma_f32_16x16x32_bf16 v[240:243], a[92:95], a[124:127], v[240:243]\n"
"ds_read_b64_tr_b16 a[48:49], v15 offset:8192\n"
"v_mfma_f32_16x16x32_bf16 v[212:215], a[84:87], a[124:127], v[212:215]\n"
"ds_read_b128 a[124:127], v13 offset:22528\n"
"v_mfma_f32_16x16x32_bf16 v[212:215], a[80:83], a[120:123], v[212:215]\n"
"ds_read_b128 a[120:123], v12 offset:22528\n"
"v_mfma_f32_16x16x32_bf16 v[216:219], a[80:83], a[128:131], v[216:219]\n"
"ds_read_b64_tr_b16 a[50:51], v15 offset:8704\n"
"v_mfma_f32_16x16x32_bf16 v[216:219], a[84:87], a[132:135], v[216:219]\n"
"ds_read_b64_tr_b16 a[52:53], v15 offset:12288\n"
"v_mfma_f32_16x16x32_bf16 v[244:247], a[92:95], a[132:135], v[244:247]\n"
"ds_read_b128 a[132:135], v13 offset:24576\n"
"v_mfma_f32_16x16x32_bf16 v[244:247], a[88:91], a[128:131], v[244:247]\n"
"s_add_u32 s67, s75, 655360\n"
"ds_read_b128 a[128:131], v12 offset:24576\n"
"v_mfma_f32_16x16x32_bf16 v[248:251], a[88:91], a[136:139], v[248:251]\n"
"ds_read_b64_tr_b16 a[54:55], v15 offset:12800\n"
"v_mfma_f32_16x16x32_bf16 v[248:251], a[92:95], a[140:143], v[248:251]\n"
"ds_read_b64_tr_b16 a[56:57], v15 offset:8200\n"
"v_mfma_f32_16x16x32_bf16 v[220:223], a[84:87], a[140:143], v[220:223]\n"
"ds_read_b128 a[140:143], v13 offset:26624\n"
"v_mfma_f32_16x16x32_bf16 v[220:223], a[80:83], a[136:139], v[220:223]\n"
"ds_read_b128 a[136:139], v12 offset:26624\n"
"v_mfma_f32_16x16x32_bf16 v[224:227], a[80:83], a[144:147], v[224:227]\n"
"ds_read_b64_tr_b16 a[58:59], v15 offset:8712\n"
"v_mfma_f32_16x16x32_bf16 v[224:227], a[84:87], a[148:151], v[224:227]\n"
"ds_read_b64_tr_b16 a[60:61], v15 offset:12296\n"
"v_mfma_f32_16x16x32_bf16 v[252:255], a[92:95], a[148:151], v[252:255]\n"
"ds_read_b128 a[148:151], v13 offset:28672\n"
"v_mfma_f32_16x16x32_bf16 v[252:255], a[88:91], a[144:147], v[252:255]\n"
"ds_read_b128 a[144:147], v12 offset:28672\n"
"ds_read_b64_tr_b16 a[62:63], v15 offset:12808\n"
"v_mfma_f32_16x16x32_bf16 a[28:31], a[88:91], a[152:155], a[28:31]\n"
"v_mfma_f32_16x16x32_bf16 a[28:31], a[92:95], a[156:159], a[28:31]\n"
"v_mfma_f32_16x16x32_bf16 a[24:27], a[84:87], a[156:159], a[24:27]\n"
"ds_read_b128 a[156:159], v13 offset:30720\n"
"v_mfma_f32_16x16x32_bf16 a[24:27], a[80:83], a[152:155], a[24:27]\n"
"s_waitcnt lgkmcnt(15)\n"
"ds_read_b128 a[152:155], v12 offset:30720\n"
"v_mfma_f32_16x16x32_bf16 v[32:35], a[32:35], a[96:99], v[32:35]\n"
"v_mfma_f32_16x16x32_bf16 v[32:35], a[36:39], a[100:103], v[32:35]\n"
"s_waitcnt lgkmcnt(2)\n"
"v_mfma_f32_16x16x32_bf16 v[60:63], a[44:47], a[100:103], v[60:63]\n"
"buffer_load_dwordx4 v11, s[52:55], s67 offen offset:0  lds\n"
"v_mfma_f32_16x16x32_bf16 v[60:63], a[40:43], a[96:99], v[60:63]\n"
"v_mfma_f32_16x16x32_bf16 v[64:67], a[40:43], a[104:107], v[64:67]\n"
"s_add_u32 s67, s75, 670720\n"
"v_mfma_f32_16x16x32_bf16 v[64:67], a[44:47], a[108:111], v[64:67]\n"
"v_mfma_f32_16x16x32_bf16 v[36:39], a[36:39], a[108:111], v[36:39]\n"
"buffer_load_dwordx4 v11, s[52:55], s67 offen offset:1024  lds\n"
"v_mfma_f32_16x16x32_bf16 v[36:39], a[32:35], a[104:107], v[36:39]\n"
"v_mfma_f32_16x16x32_bf16 v[40:43], a[32:35], a[112:115], v[40:43]\n"
"s_add_u32 s67, s75, 686080\n"
"v_mfma_f32_16x16x32_bf16 v[40:43], a[36:39], a[116:119], v[40:43]\n"
"v_mfma_f32_16x16x32_bf16 v[68:71], a[44:47], a[116:119], v[68:71]\n"
"v_mfma_f32_16x16x32_bf16 v[68:71], a[40:43], a[112:115], v[68:71]\n"
"buffer_load_dwordx4 v11, s[52:55], s67 offen offset:2048  lds\n"
"v_mfma_f32_16x16x32_bf16 v[72:75], a[40:43], a[120:123], v[72:75]\n"
"v_mfma_f32_16x16x32_bf16 v[72:75], a[44:47], a[124:127], v[72:75]\n"
"s_add_u32 s67, s75, 701440\n"
"v_mfma_f32_16x16x32_bf16 v[44:47], a[36:39], a[124:127], v[44:47]\n"
"v_mfma_f32_16x16x32_bf16 v[44:47], a[32:35], a[120:123], v[44:47]\n"
"buffer_load_dwordx4 v11, s[52:55], s67 offen offset:3072  lds\n"
"v_mfma_f32_16x16x32_bf16 v[48:51], a[32:35], a[128:131], v[48:51]\n"
"v_mfma_f32_16x16x32_bf16 v[48:51], a[36:39], a[132:135], v[48:51]\n"
"s_add_u32 m0, s64, 12288\n"
"v_mfma_f32_16x16x32_bf16 v[76:79], a[44:47], a[132:135], v[76:79]\n"
"s_waitcnt vmcnt(10)\n"
"ds_read_b64_tr_b16 a[64:65], v9 offset:0\n"
"v_mfma_f32_16x16x32_bf16 v[76:79], a[40:43], a[128:131], v[76:79]\n"
"s_add_u32 s67, s75, 720896\n"
"ds_read_b64_tr_b16 a[66:67], v9 offset:512\n"
"v_mfma_f32_16x16x32_bf16 v[80:83], a[40:43], a[136:139], v[80:83]\n"
"ds_read_b64_tr_b16 a[68:69], v9 offset:4096\n"
"v_mfma_f32_16x16x32_bf16 v[80:83], a[44:47], a[140:143], v[80:83]\n"
"ds_read_b64_tr_b16 a[70:71], v9 offset:4608\n"
"buffer_load_dwordx4 v11, s[52:55], s67 offen offset:0  lds\n"
"v_mfma_f32_16x16x32_bf16 v[52:55], a[36:39], a[140:143], v[52:55]\n"
"s_add_u32 s67, s75, 736256\n"
"ds_read_b64_tr_b16 a[72:73], v9 offset:8\n"
"v_mfma_f32_16x16x32_bf16 v[52:55], a[32:35], a[136:139], v[52:55]\n"
"s_waitcnt lgkmcnt(5)\n"
"ds_read_b64_tr_b16 a[74:75], v9 offset:520\n"
"v_mfma_f32_16x16x32_bf16 v[56:59], a[32:35], a[144:147], v[56:59]\n"
"ds_read_b64_tr_b16 a[76:77], v9 offset:4104\n"
"v_mfma_f32_16x16x32_bf16 v[56:59], a[36:39], a[148:151], v[56:59]\n"
"ds_read_b64_tr_b16 a[78:79], v9 offset:4616\n"
"v_mfma_f32_16x16x32_bf16 v[84:87], a[44:47], a[148:151], v[84:87]\n"
"buffer_load_dwordx4 v11, s[52:55], s67 offen offset:1024  lds\n"
"v_mfma_f32_16x16x32_bf16 v[84:87], a[40:43], a[144:147], v[84:87]\n"
"ds_read_b64_tr_b16 a[80:81], v15 offset:0\n"
"v_mfma_f32_16x16x32_bf16 a[4:7], a[40:43], a[152:155], a[4:7]\n"
"s_add_u32 s67, s75, 751616\n"
"ds_read_b64_tr_b16 a[82:83], v15 offset:512\n"
"v_mfma_f32_16x16x32_bf16 a[4:7], a[44:47], a[156:159], a[4:7]\n"
"ds_read_b64_tr_b16 a[84:85], v15 offset:4096\n"
"v_mfma_f32_16x16x32_bf16 a[0:3], a[36:39], a[156:159], a[0:3]\n"
"ds_read_b64_tr_b16 a[86:87], v15 offset:4608\n"
"buffer_load_dwordx4 v11, s[52:55], s67 offen offset:2048  lds\n"
"v_mfma_f32_16x16x32_bf16 a[0:3], a[32:35], a[152:155], a[0:3]\n"
"ds_read_b64_tr_b16 a[88:89], v15 offset:8\n"
"v_mfma_f32_16x16x32_bf16 v[88:91], a[48:51], a[96:99], v[88:91]\n"
"s_add_u32 s67, s75, 766976\n"
"ds_read_b64_tr_b16 a[90:91], v15 offset:520\n"
"v_mfma_f32_16x16x32_bf16 v[88:91], a[52:55], a[100:103], v[88:91]\n"
"ds_read_b64_tr_b16 a[92:93], v15 offset:4104\n"
"v_mfma_f32_16x16x32_bf16 v[116:119], a[60:63], a[100:103], v[116:119]\n"
"ds_read_b64_tr_b16 a[94:95], v15 offset:4616\n"
"v_mfma_f32_16x16x32_bf16 v[116:119], a[56:59], a[96:99], v[116:119]\n"
"buffer_load_dwordx4 v11, s[52:55], s67 offen offset:3072  lds\n"
"v_mfma_f32_16x16x32_bf16 v[120:123], a[56:59], a[104:107], v[120:123]\n"
"v_mfma_f32_16x16x32_bf16 v[120:123], a[60:63], a[108:111], v[120:123]\n"
"s_add_u32 m0, s64, 0\n"
"v_mfma_f32_16x16x32_bf16 v[92:95], a[52:55], a[108:111], v[92:95]\n"
"v_mfma_f32_16x16x32_bf16 v[92:95], a[48:51], a[104:107], v[92:95]\n"
"s_add_u32 s67, s75, 655488\n"
"v_mfma_f32_16x16x32_bf16 v[96:99], a[48:51], a[112:115], v[96:99]\n"
"s_waitcnt lgkmcnt(0)\n"
"v_mfma_f32_16x16x32_bf16 v[96:99], a[52:55], a[116:119], v[96:99]\n"
"buffer_load_dwordx4 v11, s[52:55], s67 offen offset:0  lds\n"
"v_mfma_f32_16x16x32_bf16 v[124:127], a[60:63], a[116:119], v[124:127]\n"
"v_mfma_f32_16x16x32_bf16 v[124:127], a[56:59], a[112:115], v[124:127]\n"
"s_add_u32 s67, s75, 670848\n"
"v_mfma_f32_16x16x32_bf16 v[128:131], a[56:59], a[120:123], v[128:131]\n"
"v_mfma_f32_16x16x32_bf16 v[128:131], a[60:63], a[124:127], v[128:131]\n"
"v_mfma_f32_16x16x32_bf16 v[100:103], a[52:55], a[124:127], v[100:103]\n"
"buffer_load_dwordx4 v11, s[52:55], s67 offen offset:1024  lds\n"
"v_mfma_f32_16x16x32_bf16 v[100:103], a[48:51], a[120:123], v[100:103]\n"
"v_mfma_f32_16x16x32_bf16 v[104:107], a[48:51], a[128:131], v[104:107]\n"
"s_add_u32 s67, s75, 686208\n"
"v_mfma_f32_16x16x32_bf16 v[104:107], a[52:55], a[132:135], v[104:107]\n"
"v_mfma_f32_16x16x32_bf16 v[132:135], a[60:63], a[132:135], v[132:135]\n"
"v_mfma_f32_16x16x32_bf16 v[132:135], a[56:59], a[128:131], v[132:135]\n"
"buffer_load_dwordx4 v11, s[52:55], s67 offen offset:2048  lds\n"
"v_mfma_f32_16x16x32_bf16 v[136:139], a[56:59], a[136:139], v[136:139]\n"
"v_mfma_f32_16x16x32_bf16 v[136:139], a[60:63], a[140:143], v[136:139]\n"
"s_add_u32 s67, s75, 701568\n"
"v_mfma_f32_16x16x32_bf16 v[108:111], a[52:55], a[140:143], v[108:111]\n"
"v_mfma_f32_16x16x32_bf16 v[108:111], a[48:51], a[136:139], v[108:111]\n"
"buffer_load_dwordx4 v11, s[52:55], s67 offen offset:3072  lds\n"
"v_mfma_f32_16x16x32_bf16 v[112:115], a[48:51], a[144:147], v[112:115]\n"
"v_mfma_f32_16x16x32_bf16 v[112:115], a[52:55], a[148:151], v[112:115]\n"
"s_add_u32 m0, s64, 4096\n"
"v_mfma_f32_16x16x32_bf16 v[140:143], a[60:63], a[148:151], v[140:143]\n"
"v_mfma_f32_16x16x32_bf16 v[140:143], a[56:59], a[144:147], v[140:143]\n"
"s_add_u32 s67, s75, 721024\n"
"v_mfma_f32_16x16x32_bf16 a[12:15], a[56:59], a[152:155], a[12:15]\n"
"v_mfma_f32_16x16x32_bf16 a[12:15], a[60:63], a[156:159], a[12:15]\n"
"buffer_load_dwordx4 v11, s[52:55], s67 offen offset:0  lds\n"
"v_mfma_f32_16x16x32_bf16 a[8:11], a[52:55], a[156:159], a[8:11]\n"
"v_mfma_f32_16x16x32_bf16 a[8:11], a[48:51], a[152:155], a[8:11]\n"
"s_add_u32 s67, s75, 736384\n"
"v_mfma_f32_16x16x32_bf16 v[144:147], a[64:67], a[96:99], v[144:147]\n"
"v_mfma_f32_16x16x32_bf16 v[144:147], a[68:71], a[100:103], v[144:147]\n"
"buffer_load_dwordx4 v11, s[52:55], s67 offen offset:1024  lds\n"
"v_mfma_f32_16x16x32_bf16 v[172:175], a[76:79], a[100:103], v[172:175]\n"
"v_mfma_f32_16x16x32_bf16 v[172:175], a[72:75], a[96:99], v[172:175]\n"
"s_add_u32 s67, s75, 751744\n"
"v_mfma_f32_16x16x32_bf16 v[176:179], a[72:75], a[104:107], v[176:179]\n"
"v_mfma_f32_16x16x32_bf16 v[176:179], a[76:79], a[108:111], v[176:179]\n"
"buffer_load_dwordx4 v11, s[52:55], s67 offen offset:2048  lds\n"
"v_mfma_f32_16x16x32_bf16 v[148:151], a[68:71], a[108:111], v[148:151]\n"
"v_mfma_f32_16x16x32_bf16 v[148:151], a[64:67], a[104:107], v[148:151]\n"
"s_add_u32 s67, s75, 767104\n"
"v_mfma_f32_16x16x32_bf16 v[152:155], a[64:67], a[112:115], v[152:155]\n"
"s_sub_u32 s66, 40960, s73\n"
"v_mfma_f32_16x16x32_bf16 v[152:155], a[68:71], a[116:119], v[152:155]\n"
"v_mfma_f32_16x16x32_bf16 v[180:183], a[76:79], a[116:119], v[180:183]\n"
"v_add_u32 v12, s66, v7\n"
"v_mfma_f32_16x16x32_bf16 v[180:183], a[72:75], a[112:115], v[180:183]\n"
"buffer_load_dwordx4 v11, s[52:55], s67 offen offset:3072  lds\n"
"v_mfma_f32_16x16x32_bf16 v[184:187], a[72:75], a[120:123], v[184:187]\n"
"s_add_u32 m0, s72, 98304\n"
"v_add_u32 v13, s66, v8\n"
"v_mfma_f32_16x16x32_bf16 v[184:187], a[76:79], a[124:127], v[184:187]\n"
"v_mfma_f32_16x16x32_bf16 v[156:159], a[68:71], a[124:127], v[156:159]\n"
"s_add_u32 s67, s60, 768\n"
"v_mfma_f32_16x16x32_bf16 v[156:159], a[64:67], a[120:123], v[156:159]\n"
"buffer_load_dwordx4 v6, s[48:51], s67 offen offset:0  lds\n"
"v_mfma_f32_16x16x32_bf16 v[160:163], a[64:67], a[128:131], v[160:163]\n"
"v_mfma_f32_16x16x32_bf16 v[160:163], a[68:71], a[132:135], v[160:163]\n"
"s_add_u32 s67, s60, 16128\n"
"v_mfma_f32_16x16x32_bf16 v[188:191], a[76:79], a[132:135], v[188:191]\n"
"v_mfma_f32_16x16x32_bf16 v[188:191], a[72:75], a[128:131], v[188:191]\n"
"buffer_load_dwordx4 v6, s[48:51], s67 offen offset:1024  lds\n"
"v_mfma_f32_16x16x32_bf16 v[192:195], a[72:75], a[136:139], v[192:195]\n"
"v_mfma_f32_16x16x32_bf16 v[192:195], a[76:79], a[140:143], v[192:195]\n"
"s_add_u32 s67, s60, 31488\n"
"v_mfma_f32_16x16x32_bf16 v[164:167], a[68:71], a[140:143], v[164:167]\n"
"v_mfma_f32_16x16x32_bf16 v[164:167], a[64:67], a[136:139], v[164:167]\n"
"buffer_load_dwordx4 v6, s[48:51], s67 offen offset:2048  lds\n"
"v_mfma_f32_16x16x32_bf16 v[168:171], a[64:67], a[144:147], v[168:171]\n"
"v_mfma_f32_16x16x32_bf16 v[168:171], a[68:71], a[148:151], v[168:171]\n"
"s_add_u32 s67, s60, 46848\n"
"v_mfma_f32_16x16x32_bf16 v[196:199], a[76:79], a[148:151], v[196:199]\n"
"v_mfma_f32_16x16x32_bf16 v[196:199], a[72:75], a[144:147], v[196:199]\n"
"v_mfma_f32_16x16x32_bf16 a[20:23], a[72:75], a[152:155], a[20:23]\n"
"buffer_load_dwordx4 v6, s[48:51], s67 offen offset:3072  lds\n"
"v_mfma_f32_16x16x32_bf16 a[20:23], a[76:79], a[156:159], a[20:23]\n"
"v_mfma_f32_16x16x32_bf16 a[16:19], a[68:71], a[156:159], a[16:19]\n"
"s_sub_u32 s70, 1572864, s75\n"
"s_add_u32 s70, s70, s65\n"
"v_mfma_f32_16x16x32_bf16 a[16:19], a[64:67], a[152:155], a[16:19]\n"
"s_waitcnt vmcnt(22)\n"
"s_barrier\n"
"v_mfma_f32_16x16x32_bf16 v[200:203], a[80:83], a[96:99], v[200:203]\n"
"s_waitcnt vmcnt(12)\n"
"ds_read_b64_tr_b16 a[32:33], v9 offset:8192\n"
"v_mfma_f32_16x16x32_bf16 v[200:203], a[84:87], a[100:103], v[200:203]\n"
"ds_read_b64_tr_b16 a[34:35], v9 offset:8704\n"
"v_mfma_f32_16x16x32_bf16 v[228:231], a[92:95], a[100:103], v[228:231]\n"
"ds_read_b128 a[100:103], v13 offset:16384\n"
"buffer_load_dword a252, v14, s[52:55], s70 offen\n"
"v_mfma_f32_16x16x32_bf16 v[228:231], a[88:91], a[96:99], v[228:231]\n"
"ds_read_b128 a[96:99], v12 offset:16384\n"
"v_mfma_f32_16x16x32_bf16 v[232:235], a[88:91], a[104:107], v[232:235]\n"
"buffer_store_dwordx2 v[16:17], v18, s[56:59], %11 offen sc1\n"
"ds_read_b64_tr_b16 a[36:37], v9 offset:12288\n"
"v_mfma_f32_16x16x32_bf16 v[232:235], a[92:95], a[108:111], v[232:235]\n"
"ds_read_b64_tr_b16 a[38:39], v9 offset:12800\n"
"v_mfma_f32_16x16x32_bf16 v[204:207], a[84:87], a[108:111], v[204:207]\n"
"ds_read_b128 a[108:111], v13 offset:18432\n"
"v_mfma_f32_16x16x32_bf16 v[204:207], a[80:83], a[104:107], v[204:207]\n"
"ds_read_b128 a[104:107], v12 offset:18432\n"
"buffer_store_dwordx2 v[16:17], v18, s[56:59], s69 offen sc1\n"
"ds_read_b64_tr_b16 a[40:41], v9 offset:8200\n"
"v_mfma_f32_16x16x32_bf16 v[208:211], a[80:83], a[112:115], v[208:211]\n"
"ds_read_b64_tr_b16 a[42:43], v9 offset:8712\n"
"v_mfma_f32_16x16x32_bf16 v[208:211], a[84:87], a[116:119], v[208:211]\n"
"ds_read_b64_tr_b16 a[44:45], v9 offset:12296\n"
"v_mfma_f32_16x16x32_bf16 v[236:239], a[92:95], a[116:119], v[236:239]\n"
"ds_read_b128 a[116:119], v13 offset:20480\n"
"v_mfma_f32_16x16x32_bf16 v[236:239], a[88:91], a[112:115], v[236:239]\n"
"ds_read_b128 a[112:115], v12 offset:20480\n"
"v_mfma_f32_16x16x32_bf16 v[240:243], a[88:91], a[120:123], v[240:243]\n"
"s_add_u32 m0, s64, 8192\n"
"ds_read_b64_tr_b16 a[46:47], v9 offset:12808\n"
"v_mfma_f32_16x16x32_bf16 v[240:243], a[92:95], a[124:127], v[240:243]\n"
"ds_read_b64_tr_b16 a[48:49], v15 offset:8192\n"
"v_mfma_f32_16x16x32_bf16 v[212:215], a[84:87], a[124:127], v[212:215]\n"
"ds_read_b128 a[124:127], v13 offset:22528\n"
"v_mfma_f32_16x16x32_bf16 v[212:215], a[80:83], a[120:123], v[212:215]\n"
"ds_read_b128 a[120:123], v12 offset:22528\n"
"v_mfma_f32_16x16x32_bf16 v[216:219], a[80:83], a[128:131], v[216:219]\n"
"ds_read_b64_tr_b16 a[50:51], v15 offset:8704\n"
"v_mfma_f32_16x16x32_bf16 v[216:219], a[84:87], a[132:135], v[216:219]\n"
"ds_read_b64_tr_b16 a[52:53], v15 offset:12288\n"
"v_mfma_f32_16x16x32_bf16 v[244:247], a[92:95], a[132:135], v[244:247]\n"
"ds_read_b128 a[132:135], v13 offset:24576\n"
"v_mfma_f32_16x16x32_bf16 v[244:247], a[88:91], a[128:131], v[244:247]\n"
"ds_read_b128 a[128:131], v12 offset:24576\n"
"v_mfma_f32_16x16x32_bf16 v[248:251], a[88:91], a[136:139], v[248:251]\n"
"ds_read_b64_tr_b16 a[54:55], v15 offset:12800\n"
"v_mfma_f32_16x16x32_bf16 v[248:251], a[92:95], a[140:143], v[248:251]\n"
"ds_read_b64_tr_b16 a[56:57], v15 offset:8200\n"
"v_mfma_f32_16x16x32_bf16 v[220:223], a[84:87], a[140:143], v[220:223]\n"
"s_add_u32 s67, s75, 786432\n"
"ds_read_b128 a[140:143], v13 offset:26624\n"
"v_mfma_f32_16x16x32_bf16 v[220:223], a[80:83], a[136:139], v[220:223]\n"
"ds_read_b128 a[136:139], v12 offset:26624\n"
"v_mfma_f32_16x16x32_bf16 v[224:227], a[80:83], a[144:147], v[224:227]\n"
"ds_read_b64_tr_b16 a[58:59], v15 offset:8712\n"
"v_mfma_f32_16x16x32_bf16 v[224:227], a[84:87], a[148:151], v[224:227]\n"
"ds_read_b64_tr_b16 a[60:61], v15 offset:12296\n"
"v_mfma_f32_16x16x32_bf16 v[252:255], a[92:95], a[148:151], v[252:255]\n"
"ds_read_b128 a[148:151], v13 offset:28672\n"
"v_mfma_f32_16x16x32_bf16 v[252:255], a[88:91], a[144:147], v[252:255]\n"
"ds_read_b128 a[144:147], v12 offset:28672\n"
"ds_read_b64_tr_b16 a[62:63], v15 offset:12808\n"
"v_mfma_f32_16x16x32_bf16 a[28:31], a[88:91], a[152:155], a[28:31]\n"
"v_mfma_f32_16x16x32_bf16 a[28:31], a[92:95], a[156:159], a[28:31]\n"
"v_mfma_f32_16x16x32_bf16 a[24:27], a[84:87], a[156:159], a[24:27]\n"
"ds_read_b128 a[156:159], v13 offset:30720\n"
"v_mfma_f32_16x16x32_bf16 a[24:27], a[80:83], a[152:155], a[24:27]\n"
"s_waitcnt lgkmcnt(15)\n"
"ds_read_b128 a[152:155], v12 offset:30720\n"
"v_mfma_f32_16x16x32_bf16 v[32:35], a[32:35], a[96:99], v[32:35]\n"
"v_mfma_f32_16x16x32_bf16 v[32:35], a[36:39], a[100:103], v[32:35]\n"
"s_waitcnt lgkmcnt(2)\n"
"v_mfma_f32_16x16x32_bf16 v[60:63], a[44:47], a[100:103], v[60:63]\n"
"L_st1591:\n"
"s_cbranch_execz L_st3865\n"
"buffer_load_dwordx4 v11, s[52:55], s67 offen offset:0  lds\n"
"v_mfma_f32_16x16x32_bf16 v[60:63], a[40:43], a[96:99], v[60:63]\n"
"v_mfma_f32_16x16x32_bf16 v[64:67], a[40:43], a[104:107], v[64:67]\n"
"s_add_u32 s67, s75, 801792\n"
"v_mfma_f32_16x16x32_bf16 v[64:67], a[44:47], a[108:111], v[64:67]\n"
"v_mfma_f32_16x16x32_bf16 v[36:39], a[36:39], a[108:111], v[36:39]\n"
"buffer_load_dwordx4 v11, s[52:55], s67 offen offset:1024  lds\n"
"v_mfma_f32_16x16x32_bf16 v[36:39], a[32:35], a[104:107], v[36:39]\n"
"v_mfma_f32_16x16x32_bf16 v[40:43], a[32:35], a[112:115], v[40:43]\n"
"s_add_u32 s67, s75, 817152\n"
"v_mfma_f32_16x16x32_bf16 v[40:43], a[36:39], a[116:119], v[40:43]\n"
"v_mfma_f32_16x16x32_bf16 v[68:71], a[44:47], a[116:119], v[68:71]\n"
"s_mov_b32 s30, 0\n"
"v_mfma_f32_16x16x32_bf16 v[68:71], a[40:43], a[112:115], v[68:71]\n"
"buffer_load_dwordx4 v11, s[52:55], s67 offen offset:2048  lds\n"
"v_mfma_f32_16x16x32_bf16 v[72:75], a[40:43], a[120:123], v[72:75]\n"
"s_mov_b32 s31, 0\n"
"v_mfma_f32_16x16x32_bf16 v[72:75], a[44:47], a[124:127], v[72:75]\n"
"s_add_u32 s67, s75, 832512\n"
"v_mfma_f32_16x16x32_bf16 v[44:47], a[36:39], a[124:127], v[44:47]\n"
"v_mfma_f32_16x16x32_bf16 v[44:47], a[32:35], a[120:123], v[44:47]\n"
"v_mfma_f32_16x16x32_bf16 v[48:51], a[32:35], a[128:131], v[48:51]\n"
"buffer_load_dwordx4 v11, s[52:55], s67 offen offset:3072  lds\n"
"v_mfma_f32_16x16x32_bf16 v[48:51], a[36:39], a[132:135], v[48:51]\n"
"s_add_u32 m0, s64, 12288\n"
"v_mfma_f32_16x16x32_bf16 v[76:79], a[44:47], a[132:135], v[76:79]\n"
"s_waitcnt vmcnt(11)\n"
"ds_read_b64_tr_b16 a[64:65], v9 offset:0\n"
"v_mfma_f32_16x16x32_bf16 v[76:79], a[40:43], a[128:131], v[76:79]\n"
"s_add_u32 s67, s75, 851968\n"
"ds_read_b64_tr_b16 a[66:67], v9 offset:512\n"
"v_mfma_f32_16x16x32_bf16 v[80:83], a[40:43], a[136:139], v[80:83]\n"
"ds_read_b64_tr_b16 a[68:69], v9 offset:4096\n"
"v_mfma_f32_16x16x32_bf16 v[80:83], a[44:47], a[140:143], v[80:83]\n"
"ds_read_b64_tr_b16 a[70:71], v9 offset:4608\n"
"buffer_load_dwordx4 v11, s[52:55], s67 offen offset:0  lds\n"
"v_mfma_f32_16x16x32_bf16 v[52:55], a[36:39], a[140:143], v[52:55]\n"
"s_add_u32 s67, s75, 867328\n"
"ds_read_b64_tr_b16 a[72:73], v9 offset:8\n"
"v_mfma_f32_16x16x32_bf16 v[52:55], a[32:35], a[136:139], v[52:55]\n"
"s_waitcnt lgkmcnt(5)\n"
"ds_read_b64_tr_b16 a[74:75], v9 offset:520\n"
"v_mfma_f32_16x16x32_bf16 v[56:59], a[32:35], a[144:147], v[56:59]\n"
"ds_read_b64_tr_b16 a[76:77], v9 offset:4104\n"
"v_mfma_f32_16x16x32_bf16 v[56:59], a[36:39], a[148:151], v[56:59]\n"
"ds_read_b64_tr_b16 a[78:79], v9 offset:4616\n"
"v_mfma_f32_16x16x32_bf16 v[84:87], a[44:47], a[148:151], v[84:87]\n"
"buffer_load_dwordx4 v11, s[52:55], s67 offen offset:1024  lds\n"
"v_mfma_f32_16x16x32_bf16 v[84:87], a[40:43], a[144:147], v[84:87]\n"
"ds_read_b64_tr_b16 a[80:81], v15 offset:0\n"
"v_mfma_f32_16x16x32_bf16 a[4:7], a[40:43], a[152:155], a[4:7]\n"
"s_add_u32 s67, s75, 882688\n"
"ds_read_b64_tr_b16 a[82:83], v15 offset:512\n"
"v_mfma_f32_16x16x32_bf16 a[4:7], a[44:47], a[156:159], a[4:7]\n"
"ds_read_b64_tr_b16 a[84:85], v15 offset:4096\n"
"buffer_load_dwordx4 v11, s[52:55], s67 offen offset:2048  lds\n"
"v_mfma_f32_16x16x32_bf16 a[0:3], a[36:39], a[156:159], a[0:3]\n"
"s_add_u32 s96, s56, %11\n"
"ds_read_b64_tr_b16 a[86:87], v15 offset:4608\n"
"s_addc_u32 s97, s57, 0\n"
"v_mfma_f32_16x16x32_bf16 a[0:3], a[32:35], a[152:155], a[0:3]\n"
"ds_read_b64_tr_b16 a[88:89], v15 offset:8\n"
"v_mfma_f32_16x16x32_bf16 v[88:91], a[48:51], a[96:99], v[88:91]\n"
"s_add_u32 s67, s75, 898048\n"
"ds_read_b64_tr_b16 a[90:91], v15 offset:520\n"
"v_mfma_f32_16x16x32_bf16 v[88:91], a[52:55], a[100:103], v[88:91]\n"
"ds_read_b64_tr_b16 a[92:93], v15 offset:4104\n"
"v_mfma_f32_16x16x32_bf16 v[116:119], a[60:63], a[100:103], v[116:119]\n"
"ds_read_b64_tr_b16 a[94:95], v15 offset:4616\n"
"v_mfma_f32_16x16x32_bf16 v[116:119], a[56:59], a[96:99], v[116:119]\n"
"buffer_load_dwordx4 v11, s[52:55], s67 offen offset:3072  lds\n"
"v_mfma_f32_16x16x32_bf16 v[120:123], a[56:59], a[104:107], v[120:123]\n"
"v_mfma_f32_16x16x32_bf16 v[120:123], a[60:63], a[108:111], v[120:123]\n"
"s_add_u32 m0, s64, 0\n"
"v_mfma_f32_16x16x32_bf16 v[92:95], a[52:55], a[108:111], v[92:95]\n"
"v_mfma_f32_16x16x32_bf16 v[92:95], a[48:51], a[104:107], v[92:95]\n"
"s_add_u32 s67, s75, 786560\n"
"v_mfma_f32_16x16x32_bf16 v[96:99], a[48:51], a[112:115], v[96:99]\n"
"s_waitcnt lgkmcnt(0)\n"
"v_mfma_f32_16x16x32_bf16 v[96:99], a[52:55], a[116:119], v[96:99]\n"
"buffer_load_dwordx4 v11, s[52:55], s67 offen offset:0  lds\n"
"v_mfma_f32_16x16x32_bf16 v[124:127], a[60:63], a[116:119], v[124:127]\n"
"s_load_dword s98, s[96:97], 512 glc\n"
"v_mfma_f32_16x16x32_bf16 v[124:127], a[56:59], a[112:115], v[124:127]\n"
"s_load_dword s99, s[96:97], 8704 glc\n"
"s_add_u32 s67, s75, 801920\n"
"v_mfma_f32_16x16x32_bf16 v[128:131], a[56:59], a[120:123], v[128:131]\n"
"v_mfma_f32_16x16x32_bf16 v[128:131], a[60:63], a[124:127], v[128:131]\n"
"buffer_load_dwordx4 v11, s[52:55], s67 offen offset:1024  lds\n"
"v_mfma_f32_16x16x32_bf16 v[100:103], a[52:55], a[124:127], v[100:103]\n"
"v_mfma_f32_16x16x32_bf16 v[100:103], a[48:51], a[120:123], v[100:103]\n"
"s_add_u32 s67, s75, 817280\n"
"v_mfma_f32_16x16x32_bf16 v[104:107], a[48:51], a[128:131], v[104:107]\n"
"v_mfma_f32_16x16x32_bf16 v[104:107], a[52:55], a[132:135], v[104:107]\n"
"v_mfma_f32_16x16x32_bf16 v[132:135], a[60:63], a[132:135], v[132:135]\n"
"buffer_load_dwordx4 v11, s[52:55], s67 offen offset:2048  lds\n"
"v_mfma_f32_16x16x32_bf16 v[132:135], a[56:59], a[128:131], v[132:135]\n"
"v_mfma_f32_16x16x32_bf16 v[136:139], a[56:59], a[136:139], v[136:139]\n"
"s_add_u32 s67, s75, 832640\n"
"v_mfma_f32_16x16x32_bf16 v[136:139], a[60:63], a[140:143], v[136:139]\n"
"v_mfma_f32_16x16x32_bf16 v[108:111], a[52:55], a[140:143], v[108:111]\n"
"buffer_load_dwordx4 v11, s[52:55], s67 offen offset:3072  lds\n"
"v_mfma_f32_16x16x32_bf16 v[108:111], a[48:51], a[136:139], v[108:111]\n"
"v_mfma_f32_16x16x32_bf16 v[112:115], a[48:51], a[144:147], v[112:115]\n"
"s_add_u32 m0, s64, 4096\n"
"v_mfma_f32_16x16x32_bf16 v[112:115], a[52:55], a[148:151], v[112:115]\n"
"v_mfma_f32_16x16x32_bf16 v[140:143], a[60:63], a[148:151], v[140:143]\n"
"s_add_u32 s67, s75, 852096\n"
"v_mfma_f32_16x16x32_bf16 v[140:143], a[56:59], a[144:147], v[140:143]\n"
"v_mfma_f32_16x16x32_bf16 a[12:15], a[56:59], a[152:155], a[12:15]\n"
"buffer_load_dwordx4 v11, s[52:55], s67 offen offset:0  lds\n"
"v_mfma_f32_16x16x32_bf16 a[12:15], a[60:63], a[156:159], a[12:15]\n"
"v_mfma_f32_16x16x32_bf16 a[8:11], a[52:55], a[156:159], a[8:11]\n"
"s_add_u32 s67, s75, 867456\n"
"v_mfma_f32_16x16x32_bf16 a[8:11], a[48:51], a[152:155], a[8:11]\n"
"v_mfma_f32_16x16x32_bf16 v[144:147], a[64:67], a[96:99], v[144:147]\n"
"buffer_load_dwordx4 v11, s[52:55], s67 offen offset:1024  lds\n"
"v_mfma_f32_16x16x32_bf16 v[144:147], a[68:71], a[100:103], v[144:147]\n"
"v_mfma_f32_16x16x32_bf16 v[172:175], a[76:79], a[100:103], v[172:175]\n"
"s_add_u32 s67, s75, 882816\n"
"v_mfma_f32_16x16x32_bf16 v[172:175], a[72:75], a[96:99], v[172:175]\n"
"v_mfma_f32_16x16x32_bf16 v[176:179], a[72:75], a[104:107], v[176:179]\n"
"buffer_load_dwordx4 v11, s[52:55], s67 offen offset:2048  lds\n"
"v_mfma_f32_16x16x32_bf16 v[176:179], a[76:79], a[108:111], v[176:179]\n"
"v_mfma_f32_16x16x32_bf16 v[148:151], a[68:71], a[108:111], v[148:151]\n"
"s_add_u32 s67, s75, 898176\n"
"v_mfma_f32_16x16x32_bf16 v[148:151], a[64:67], a[104:107], v[148:151]\n"
"v_mfma_f32_16x16x32_bf16 v[152:155], a[64:67], a[112:115], v[152:155]\n"
"s_sub_u32 s66, 81920, s73\n"
"v_mfma_f32_16x16x32_bf16 v[152:155], a[68:71], a[116:119], v[152:155]\n"
"buffer_load_dwordx4 v11, s[52:55], s67 offen offset:3072  lds\n"
"v_mfma_f32_16x16x32_bf16 v[180:183], a[76:79], a[116:119], v[180:183]\n"
"s_add_u32 m0, s72, 139264\n"
"v_add_u32 v12, s66, v7\n"
"v_mfma_f32_16x16x32_bf16 v[180:183], a[72:75], a[112:115], v[180:183]\n"
"v_mfma_f32_16x16x32_bf16 v[184:187], a[72:75], a[120:123], v[184:187]\n"
"s_add_u32 s67, s60, 896\n"
"v_add_u32 v13, s66, v8\n"
"v_mfma_f32_16x16x32_bf16 v[184:187], a[76:79], a[124:127], v[184:187]\n"
"v_mfma_f32_16x16x32_bf16 v[156:159], a[68:71], a[124:127], v[156:159]\n"
"buffer_load_dwordx4 v6, s[48:51], s67 offen offset:0  lds\n"
"v_mfma_f32_16x16x32_bf16 v[156:159], a[64:67], a[120:123], v[156:159]\n"
"v_mfma_f32_16x16x32_bf16 v[160:163], a[64:67], a[128:131], v[160:163]\n"
"s_add_u32 s67, s60, 16256\n"
"v_mfma_f32_16x16x32_bf16 v[160:163], a[68:71], a[132:135], v[160:163]\n"
"v_mfma_f32_16x16x32_bf16 v[188:191], a[76:79], a[132:135], v[188:191]\n"
"buffer_load_dwordx4 v6, s[48:51], s67 offen offset:1024  lds\n"
"v_mfma_f32_16x16x32_bf16 v[188:191], a[72:75], a[128:131], v[188:191]\n"
"v_mfma_f32_16x16x32_bf16 v[192:195], a[72:75], a[136:139], v[192:195]\n"
"s_add_u32 s67, s60, 31616\n"
"v_mfma_f32_16x16x32_bf16 v[192:195], a[76:79], a[140:143], v[192:195]\n"
"v_mfma_f32_16x16x32_bf16 v[164:167], a[68:71], a[140:143], v[164:167]\n"
"v_mfma_f32_16x16x32_bf16 v[164:167], a[64:67], a[136:139], v[164:167]\n"
"buffer_load_dwordx4 v6, s[48:51], s67 offen offset:2048  lds\n"
"v_mfma_f32_16x16x32_bf16 v[168:171], a[64:67], a[144:147], v[168:171]\n"
"v_mfma_f32_16x16x32_bf16 v[168:171], a[68:71], a[148:151], v[168:171]\n"
"s_add_u32 s67, s60, 46976\n"
"v_mfma_f32_16x16x32_bf16 v[196:199], a[76:79], a[148:151], v[196:199]\n"
"v_mfma_f32_16x16x32_bf16 v[196:199], a[72:75], a[144:147], v[196:199]\n"
"buffer_load_dwordx4 v6, s[48:51], s67 offen offset:3072  lds\n"
"v_mfma_f32_16x16x32_bf16 a[20:23], a[72:75], a[152:155], a[20:23]\n"
"v_mfma_f32_16x16x32_bf16 a[20:23], a[76:79], a[156:159], a[20:23]\n"
"s_waitcnt lgkmcnt(0)\n"
"v_mfma_f32_16x16x32_bf16 a[16:19], a[68:71], a[156:159], a[16:19]\n"
"v_mfma_f32_16x16x32_bf16 a[16:19], a[64:67], a[152:155], a[16:19]\n"
"s_waitcnt vmcnt(23)\n"
"s_barrier\n"
"v_mfma_f32_16x16x32_bf16 v[200:203], a[80:83], a[96:99], v[200:203]\n"
"s_waitcnt vmcnt(12)\n"
"ds_read_b64_tr_b16 a[32:33], v9 offset:8192\n"
"v_mfma_f32_16x16x32_bf16 v[200:203], a[84:87], a[100:103], v[200:203]\n"
"ds_read_b64_tr_b16 a[34:35], v9 offset:8704\n"
"v_mfma_f32_16x16x32_bf16 v[228:231], a[92:95], a[100:103], v[228:231]\n"
"s_add_u32 m0, s64, 8192\n"
"ds_read_b128 a[100:103], v13 offset:16384\n"
"v_mfma_f32_16x16x32_bf16 v[228:231], a[88:91], a[96:99], v[228:231]\n"
"ds_read_b128 a[96:99], v12 offset:16384\n"
"v_mfma_f32_16x16x32_bf16 v[232:235], a[88:91], a[104:107], v[232:235]\n"
"ds_read_b64_tr_b16 a[36:37], v9 offset:12288\n"
"v_mfma_f32_16x16x32_bf16 v[232:235], a[92:95], a[108:111], v[232:235]\n"
"ds_read_b64_tr_b16 a[38:39], v9 offset:12800\n"
"v_mfma_f32_16x16x32_bf16 v[204:207], a[84:87], a[108:111], v[204:207]\n"
"ds_read_b128 a[108:111], v13 offset:18432\n"
"v_mfma_f32_16x16x32_bf16 v[204:207], a[80:83], a[104:107], v[204:207]\n"
"ds_read_b128 a[104:107], v12 offset:18432\n"
"ds_read_b64_tr_b16 a[40:41], v9 offset:8200\n"
"v_mfma_f32_16x16x32_bf16 v[208:211], a[80:83], a[112:115], v[208:211]\n"
"ds_read_b64_tr_b16 a[42:43], v9 offset:8712\n"
"v_mfma_f32_16x16x32_bf16 v[208:211], a[84:87], a[116:119], v[208:211]\n"
"ds_read_b64_tr_b16 a[44:45], v9 offset:12296\n"
"v_mfma_f32_16x16x32_bf16 v[236:239], a[92:95], a[116:119], v[236:239]\n"
"ds_read_b128 a[116:119], v13 offset:20480\n"
"v_mfma_f32_16x16x32_bf16 v[236:239], a[88:91], a[112:115], v[236:239]\n"
"ds_read_b128 a[112:115], v12 offset:20480\n"
"v_mfma_f32_16x16x32_bf16 v[240:243], a[88:91], a[120:123], v[240:243]\n"
"ds_read_b64_tr_b16 a[46:47], v9 offset:12808\n"
"v_mfma_f32_16x16x32_bf16 v[240:243], a[92:95], a[124:127], v[240:243]\n"
"ds_read_b64_tr_b16 a[48:49], v15 offset:8192\n"
"v_mfma_f32_16x16x32_bf16 v[212:215], a[84:87], a[124:127], v[212:215]\n"
"ds_read_b128 a[124:127], v13 offset:22528\n"
"v_mfma_f32_16x16x32_bf16 v[212:215], a[80:83], a[120:123], v[212:215]\n"
"ds_read_b128 a[120:123], v12 offset:22528\n"
"v_mfma_f32_16x16x32_bf16 v[216:219], a[80:83], a[128:131], v[216:219]\n"
"s_add_u32 s67, s75, 917504\n"
"ds_read_b64_tr_b16 a[50:51], v15 offset:8704\n"
"v_mfma_f32_16x16x32_bf16 v[216:219], a[84:87], a[132:135], v[216:219]\n"
"ds_read_b64_tr_b16 a[52:53], v15 offset:12288\n"
"v_mfma_f32_16x16x32_bf16 v[244:247], a[92:95], a[132:135], v[244:247]\n"
"ds_read_b128 a[132:135], v13 offset:24576\n"
"v_mfma_f32_16x16x32_bf16 v[244:247], a[88:91], a[128:131], v[244:247]\n"
"ds_read_b128 a[128:131], v12 offset:24576\n"
"v_mfma_f32_16x16x32_bf16 v[248:251], a[88:91], a[136:139], v[248:251]\n"
"ds_read_b64_tr_b16 a[54:55], v15 offset:12800\n"
"v_mfma_f32_16x16x32_bf16 v[248:251], a[92:95], a[140:143], v[248:251]\n"
"ds_read_b64_tr_b16 a[56:57], v15 offset:8200\n"
"v_mfma_f32_16x16x32_bf16 v[220:223], a[84:87], a[140:143], v[220:223]\n"
"ds_read_b128 a[140:143], v13 offset:26624\n"
"v_mfma_f32_16x16x32_bf16 v[220:223], a[80:83], a[136:139], v[220:223]\n"
"ds_read_b128 a[136:139], v12 offset:26624\n"
"v_mfma_f32_16x16x32_bf16 v[224:227], a[80:83], a[144:147], v[224:227]\n"
"ds_read_b64_tr_b16 a[58:59], v15 offset:8712\n"
"v_mfma_f32_16x16x32_bf16 v[224:227], a[84:87], a[148:151], v[224:227]\n"
"ds_read_b64_tr_b16 a[60:61], v15 offset:12296\n"
"v_mfma_f32_16x16x32_bf16 v[252:255], a[92:95], a[148:151], v[252:255]\n"
"ds_read_b128 a[148:151], v13 offset:28672\n"
"v_mfma_f32_16x16x32_bf16 v[252:255], a[88:91], a[144:147], v[252:255]\n"
"ds_read_b128 a[144:147], v12 offset:28672\n"
"ds_read_b64_tr_b16 a[62:63], v15 offset:12808\n"
"v_mfma_f32_16x16x32_bf16 a[28:31], a[88:91], a[152:155], a[28:31]\n"
"v_mfma_f32_16x16x32_bf16 a[28:31], a[92:95], a[156:159], a[28:31]\n"
"v_mfma_f32_16x16x32_bf16 a[24:27], a[84:87], a[156:159], a[24:27]\n"
"ds_read_b128 a[156:159], v13 offset:30720\n"
"v_mfma_f32_16x16x32_bf16 a[24:27], a[80:83], a[152:155], a[24:27]\n"
"s_waitcnt lgkmcnt(15)\n"
"ds_read_b128 a[152:155], v12 offset:30720\n"
"v_mfma_f32_16x16x32_bf16 v[32:35], a[32:35], a[96:99], v[32:35]\n"
"v_mfma_f32_16x16x32_bf16 v[32:35], a[36:39], a[100:103], v[32:35]\n"
"s_waitcnt lgkmcnt(2)\n"
"L_H6S:\n"
"v_mfma_f32_16x16x32_bf16 v[60:63], a[44:47], a[100:103], v[60:63]\n"
"buffer_load_dwordx4 v11, s[52:55], s67 offen offset:0  lds\n"
"v_mfma_f32_16x16x32_bf16 v[60:63], a[40:43], a[96:99], v[60:63]\n"
"v_mfma_f32_16x16x32_bf16 v[64:67], a[40:43], a[104:107], v[64:67]\n"
"s_add_u32 s67, s75, 932864\n"
"v_mfma_f32_16x16x32_bf16 v[64:67], a[44:47], a[108:111], v[64:67]\n"
"v_mfma_f32_16x16x32_bf16 v[36:39], a[36:39], a[108:111], v[36:39]\n"
"buffer_load_dwordx4 v11, s[52:55], s67 offen offset:1024  lds\n"
"v_mfma_f32_16x16x32_bf16 v[36:39], a[32:35], a[104:107], v[36:39]\n"
"v_mfma_f32_16x16x32_bf16 v[40:43], a[32:35], a[112:115], v[40:43]\n"
"s_add_u32 s67, s75, 948224\n"
"v_mfma_f32_16x16x32_bf16 v[40:43], a[36:39], a[116:119], v[40:43]\n"
"v_mfma_f32_16x16x32_bf16 v[68:71], a[44:47], a[116:119], v[68:71]\n"
"v_mfma_f32_16x16x32_bf16 v[68:71], a[40:43], a[112:115], v[68:71]\n"
"buffer_load_dwordx4 v11, s[52:55], s67 offen offset:2048  lds\n"
"v_mfma_f32_16x16x32_bf16 v[72:75], a[40:43], a[120:123], v[72:75]\n"
"v_mfma_f32_16x16x32_bf16 v[72:75], a[44:47], a[124:127], v[72:75]\n"
"s_add_u32 s67, s75, 963584\n"
"v_mfma_f32_16x16x32_bf16 v[44:47], a[36:39], a[124:127], v[44:47]\n"
"v_mfma_f32_16x16x32_bf16 v[44:47], a[32:35], a[120:123], v[44:47]\n"
"v_mfma_f32_16x16x32_bf16 v[48:51], a[32:35], a[128:131], v[48:51]\n"
"buffer_load_dwordx4 v11, s[52:55], s67 offen offset:3072  lds\n"
"v_mfma_f32_16x16x32_bf16 v[48:51], a[36:39], a[132:135], v[48:51]\n"
"s_add_u32 m0, s64, 12288\n"
"v_mfma_f32_16x16x32_bf16 v[76:79], a[44:47], a[132:135], v[76:79]\n"
"s_waitcnt vmcnt(8)\n"
"ds_read_b64_tr_b16 a[64:65], v9 offset:0\n"
"v_mfma_f32_16x16x32_bf16 v[76:79], a[40:43], a[128:131], v[76:79]\n"
"s_add_u32 s67, s75, 983040\n"
"ds_read_b64_tr_b16 a[66:67], v9 offset:512\n"
"v_mfma_f32_16x16x32_bf16 v[80:83], a[40:43], a[136:139], v[80:83]\n"
"ds_read_b64_tr_b16 a[68:69], v9 offset:4096\n"
"v_mfma_f32_16x16x32_bf16 v[80:83], a[44:47], a[140:143], v[80:83]\n"
"ds_read_b64_tr_b16 a[70:71], v9 offset:4608\n"
"buffer_load_dwordx4 v11, s[52:55], s67 offen offset:0  lds\n"
"v_mfma_f32_16x16x32_bf16 v[52:55], a[36:39], a[140:143], v[52:55]\n"
"s_add_u32 s67, s75, 998400\n"
"ds_read_b64_tr_b16 a[72:73], v9 offset:8\n"
"v_mfma_f32_16x16x32_bf16 v[52:55], a[32:35], a[136:139], v[52:55]\n"
"s_waitcnt lgkmcnt(5)\n"
"ds_read_b64_tr_b16 a[74:75], v9 offset:520\n"
"v_mfma_f32_16x16x32_bf16 v[56:59], a[32:35], a[144:147], v[56:59]\n"
"ds_read_b64_tr_b16 a[76:77], v9 offset:4104\n"
"v_mfma_f32_16x16x32_bf16 v[56:59], a[36:39], a[148:151], v[56:59]\n"
"ds_read_b64_tr_b16 a[78:79], v9 offset:4616\n"
"v_mfma_f32_16x16x32_bf16 v[84:87], a[44:47], a[148:151], v[84:87]\n"
"buffer_load_dwordx4 v11, s[52:55], s67 offen offset:1024  lds\n"
"v_mfma_f32_16x16x32_bf16 v[84:87], a[40:43], a[144:147], v[84:87]\n"
"ds_read_b64_tr_b16 a[80:81], v15 offset:0\n"
"v_mfma_f32_16x16x32_bf16 a[4:7], a[40:43], a[152:155], a[4:7]\n"
"s_add_u32 s67, s75, 1013760\n"
"ds_read_b64_tr_b16 a[82:83], v15 offset:512\n"
"v_mfma_f32_16x16x32_bf16 a[4:7], a[44:47], a[156:159], a[4:7]\n"
"ds_read_b64_tr_b16 a[84:85], v15 offset:4096\n"
"v_mfma_f32_16x16x32_bf16 a[0:3], a[36:39], a[156:159], a[0:3]\n"
"ds_read_b64_tr_b16 a[86:87], v15 offset:4608\n"
"buffer_load_dwordx4 v11, s[52:55], s67 offen offset:2048  lds\n"
"v_mfma_f32_16x16x32_bf16 a[0:3], a[32:35], a[152:155], a[0:3]\n"
"ds_read_b64_tr_b16 a[88:89], v15 offset:8\n"
"v_mfma_f32_16x16x32_bf16 v[88:91], a[48:51], a[96:99], v[88:91]\n"
"s_add_u32 s67, s75, 1029120\n"
"ds_read_b64_tr_b16 a[90:91], v15 offset:520\n"
"v_mfma_f32_16x16x32_bf16 v[88:91], a[52:55], a[100:103], v[88:91]\n"
"ds_read_b64_tr_b16 a[92:93], v15 offset:4104\n"
"v_mfma_f32_16x16x32_bf16 v[116:119], a[60:63], a[100:103], v[116:119]\n"
"ds_read_b64_tr_b16 a[94:95], v15 offset:4616\n"
"v_mfma_f32_16x16x32_bf16 v[116:119], a[56:59], a[96:99], v[116:119]\n"
"buffer_load_dwordx4 v11, s[52:55], s67 offen offset:3072  lds\n"
"v_mfma_f32_16x16x32_bf16 v[120:123], a[56:59], a[104:107], v[120:123]\n"
"v_mfma_f32_16x16x32_bf16 v[120:123], a[60:63], a[108:111], v[120:123]\n"
"s_add_u32 m0, s64, 0\n"
"v_mfma_f32_16x16x32_bf16 v[92:95], a[52:55], a[108:111], v[92:95]\n"
"v_mfma_f32_16x16x32_bf16 v[92:95], a[48:51], a[104:107], v[92:95]\n"
"s_add_u32 s67, s75, 917632\n"
"v_mfma_f32_16x16x32_bf16 v[96:99], a[48:51], a[112:115], v[96:99]\n"
"s_waitcnt lgkmcnt(0)\n"
"s_mov_b32 s99, 0\n"
"v_mfma_f32_16x16x32_bf16 v[96:99], a[52:55], a[116:119], v[96:99]\n"
"s_mov_b32 s98, 0\n"
"buffer_load_dwordx4 v11, s[52:55], s67 offen offset:0  lds\n"
"v_mfma_f32_16x16x32_bf16 v[124:127], a[60:63], a[116:119], v[124:127]\n"
"s_memtime s[76:77]\n"
"s_load_dword s98, s[96:97], 8192 glc\n"
"v_mfma_f32_16x16x32_bf16 v[124:127], a[56:59], a[112:115], v[124:127]\n"
"s_add_u32 s67, s75, 932992\n"
"v_mfma_f32_16x16x32_bf16 v[128:131], a[56:59], a[120:123], v[128:131]\n"
"v_mfma_f32_16x16x32_bf16 v[128:131], a[60:63], a[124:127], v[128:131]\n"
"buffer_load_dwordx4 v11, s[52:55], s67 offen offset:1024  lds\n"
"v_mfma_f32_16x16x32_bf16 v[100:103], a[52:55], a[124:127], v[100:103]\n"
"v_mfma_f32_16x16x32_bf16 v[100:103], a[48:51], a[120:123], v[100:103]\n"
"s_add_u32 s67, s75, 948352\n"
"v_mfma_f32_16x16x32_bf16 v[104:107], a[48:51], a[128:131], v[104:107]\n"
"v_mfma_f32_16x16x32_bf16 v[104:107], a[52:55], a[132:135], v[104:107]\n"
"v_mfma_f32_16x16x32_bf16 v[132:135], a[60:63], a[132:135], v[132:135]\n"
"buffer_load_dwordx4 v11, s[52:55], s67 offen offset:2048  lds\n"
"v_mfma_f32_16x16x32_bf16 v[132:135], a[56:59], a[128:131], v[132:135]\n"
"s_mov_b32 s29, s76\n"
"v_mfma_f32_16x16x32_bf16 v[136:139], a[56:59], a[136:139], v[136:139]\n"
"s_add_u32 s67, s75, 963712\n"
"v_mfma_f32_16x16x32_bf16 v[136:139], a[60:63], a[140:143], v[136:139]\n"
"buffer_load_dwordx4 v11, s[52:55], s67 offen offset:3072  lds\n"
"v_mfma_f32_16x16x32_bf16 v[108:111], a[52:55], a[140:143], v[108:111]\n"
"s_sub_u32 s99, s99, s98\n"
"s_sub_u32 s99, s99, s98\n"
"v_mfma_f32_16x16x32_bf16 v[108:111], a[48:51], a[136:139], v[108:111]\n"
"s_sub_u32 s99, s99, s98\n"
"s_add_u32 m0, s64, 4096\n"
"v_mfma_f32_16x16x32_bf16 v[112:115], a[48:51], a[144:147], v[112:115]\n"
"s_sub_u32 s99, s99, s98\n"
"v_mfma_f32_16x16x32_bf16 v[112:115], a[52:55], a[148:151], v[112:115]\n"
"s_add_u32 s67, s75, 983168\n"
"v_mfma_f32_16x16x32_bf16 v[140:143], a[60:63], a[148:151], v[140:143]\n"
"s_sub_u32 s99, s99, s98\n"
"s_sub_u32 s99, s99, s98\n"
"v_mfma_f32_16x16x32_bf16 v[140:143], a[56:59], a[144:147], v[140:143]\n"
"s_sub_u32 s99, s99, s98\n"
"buffer_load_dwordx4 v11, s[52:55], s67 offen offset:0  lds\n"
"v_mfma_f32_16x16x32_bf16 a[12:15], a[56:59], a[152:155], a[12:15]\n"
"s_sub_u32 s99, s99, s98\n"
"v_mfma_f32_16x16x32_bf16 a[12:15], a[60:63], a[156:159], a[12:15]\n"
"s_add_u32 s67, s75, 998528\n"
"v_mfma_f32_16x16x32_bf16 a[8:11], a[52:55], a[156:159], a[8:11]\n"
"s_sub_u32 s99, s99, s98\n"
"s_sub_u32 s99, s99, s98\n"
"v_mfma_f32_16x16x32_bf16 a[8:11], a[48:51], a[152:155], a[8:11]\n"
"s_sub_u32 s99, s99, s98\n"
"v_mfma_f32_16x16x32_bf16 v[144:147], a[64:67], a[96:99], v[144:147]\n"
"s_sub_u32 s99, s99, s98\n"
"v_mfma_f32_16x16x32_bf16 v[144:147], a[68:71], a[100:103], v[144:147]\n"
"buffer_load_dwordx4 v11, s[52:55], s67 offen offset:1024  lds\n"
"v_mfma_f32_16x16x32_bf16 v[172:175], a[76:79], a[100:103], v[172:175]\n"
"s_sub_u32 s99, s99, s98\n"
"s_sub_u32 s99, s99, s98\n"
"v_mfma_f32_16x16x32_bf16 v[172:175], a[72:75], a[96:99], v[172:175]\n"
"s_sub_u32 s99, s99, s98\n"
"s_add_u32 s67, s75, 1013888\n"
"v_mfma_f32_16x16x32_bf16 v[176:179], a[72:75], a[104:107], v[176:179]\n"
"s_sub_u32 s99, s99, s98\n"
"v_mfma_f32_16x16x32_bf16 v[176:179], a[76:79], a[108:111], v[176:179]\n"
"buffer_load_dwordx4 v11, s[52:55], s67 offen offset:2048  lds\n"
"v_mfma_f32_16x16x32_bf16 v[148:151], a[68:71], a[108:111], v[148:151]\n"
"s_sub_u32 s99, s99, s98\n"
"s_sub_u32 s99, s99, s98\n"
"v_mfma_f32_16x16x32_bf16 v[148:151], a[64:67], a[104:107], v[148:151]\n"
"s_sub_u32 s99, s99, s98\n"
"s_add_u32 s67, s75, 1029248\n"
"v_mfma_f32_16x16x32_bf16 v[152:155], a[64:67], a[112:115], v[152:155]\n"
"s_sub_u32 s66, 122880, s73\n"
"s_sub_u32 s99, s99, s98\n"
"v_mfma_f32_16x16x32_bf16 v[152:155], a[68:71], a[116:119], v[152:155]\n"
"buffer_load_dwordx4 v11, s[52:55], s67 offen offset:3072  lds\n"
"v_mfma_f32_16x16x32_bf16 v[180:183], a[76:79], a[116:119], v[180:183]\n"
"s_sub_u32 s99, s99, s98\n"
"v_add_u32 v12, s66, v7\n"
"s_sub_u32 s99, s99, s98\n"
"v_mfma_f32_16x16x32_bf16 v[180:183], a[72:75], a[112:115], v[180:183]\n"
"s_sub_u32 s99, s99, s98\n"
"s_cmp_lt_u32 %16, 2\n"
"s_cselect_b32 s67, 16384, 0\n"
"v_mfma_f32_16x16x32_bf16 v[184:187], a[72:75], a[120:123], v[184:187]\n"
"s_sub_u32 s99, s99, s98\n"
"v_add_u32 v13, s66, v8\n"
"s_add_u32 s67, s67, s72\n"
"v_mfma_f32_16x16x32_bf16 v[184:187], a[76:79], a[124:127], v[184:187]\n"
"s_memtime s[76:77]\n"
"s_add_u32 m0, s67, 16384\n"
"s_sub_u32 s67, 1024, s60\n"
"v_mfma_f32_16x16x32_bf16 v[156:159], a[68:71], a[124:127], v[156:159]\n"
"buffer_load_dwordx4 v6, s[48:51], s67 offen offset:0  lds\n"
"v_mfma_f32_16x16x32_bf16 v[156:159], a[64:67], a[120:123], v[156:159]\n"
"s_waitcnt lgkmcnt(0)\n"
"v_mfma_f32_16x16x32_bf16 v[160:163], a[64:67], a[128:131], v[160:163]\n"
"s_sub_u32 s29, s76, s29\n"
"s_sub_u32 s67, 16384, s60\n"
"v_mfma_f32_16x16x32_bf16 v[160:163], a[68:71], a[132:135], v[160:163]\n"
"s_cmp_lt_u32 s29, 620\n"
"s_cselect_b32 s99, s99, 0\n"
"v_mfma_f32_16x16x32_bf16 v[188:191], a[76:79], a[132:135], v[188:191]\n"
"s_max_u32 s31, s31, s99\n"
"v_mfma_f32_16x16x32_bf16 v[188:191], a[72:75], a[128:131], v[188:191]\n"
"buffer_load_dwordx4 v6, s[48:51], s67 offen offset:1024  lds\n"
"v_mfma_f32_16x16x32_bf16 v[192:195], a[72:75], a[136:139], v[192:195]\n"
"v_mfma_f32_16x16x32_bf16 v[192:195], a[76:79], a[140:143], v[192:195]\n"
"s_sub_u32 s67, 31744, s60\n"
"v_mfma_f32_16x16x32_bf16 v[164:167], a[68:71], a[140:143], v[164:167]\n"
"v_mfma_f32_16x16x32_bf16 v[164:167], a[64:67], a[136:139], v[164:167]\n"
"buffer_load_dwordx4 v6, s[48:51], s67 offen offset:2048  lds\n"
"v_mfma_f32_16x16x32_bf16 v[168:171], a[64:67], a[144:147], v[168:171]\n"
"v_mfma_f32_16x16x32_bf16 v[168:171], a[68:71], a[148:151], v[168:171]\n"
"s_sub_u32 s67, 47104, s60\n"
"v_mfma_f32_16x16x32_bf16 v[196:199], a[76:79], a[148:151], v[196:199]\n"
"v_mfma_f32_16x16x32_bf16 v[196:199], a[72:75], a[144:147], v[196:199]\n"
"buffer_load_dwordx4 v6, s[48:51], s67 offen offset:3072  lds\n"
"v_mfma_f32_16x16x32_bf16 a[20:23], a[72:75], a[152:155], a[20:23]\n"
"v_mfma_f32_16x16x32_bf16 a[20:23], a[76:79], a[156:159], a[20:23]\n"
"s_add_u32 m0, s72, 57216\n"
"v_mfma_f32_16x16x32_bf16 a[16:19], a[68:71], a[156:159], a[16:19]\n"
"v_mfma_f32_16x16x32_bf16 a[16:19], a[64:67], a[152:155], a[16:19]\n"
"s_waitcnt vmcnt(20)\n"
"s_barrier\n"
"v_mfma_f32_16x16x32_bf16 v[200:203], a[80:83], a[96:99], v[200:203]\n"
"s_waitcnt vmcnt(12)\n"
"ds_read_b64_tr_b16 a[32:33], v9 offset:8192\n"
"buffer_load_dwordx4 v6, s[40:43], s80 offen offset:128  lds\n"
"v_mfma_f32_16x16x32_bf16 v[200:203], a[84:87], a[100:103], v[200:203]\n"
"ds_read_b64_tr_b16 a[34:35], v9 offset:8704\n"
"v_mfma_f32_16x16x32_bf16 v[228:231], a[92:95], a[100:103], v[228:231]\n"
"ds_read_b128 a[100:103], v13 offset:16384\n"
"v_mfma_f32_16x16x32_bf16 v[228:231], a[88:91], a[96:99], v[228:231]\n"
"ds_read_b128 a[96:99], v12 offset:16384\n"
"buffer_load_dwordx4 v6, s[40:43], s81 offen offset:1152  lds\n"
"v_mfma_f32_16x16x32_bf16 v[232:235], a[88:91], a[104:107], v[232:235]\n"
"ds_read_b64_tr_b16 a[36:37], v9 offset:12288\n"
"v_mfma_f32_16x16x32_bf16 v[232:235], a[92:95], a[108:111], v[232:235]\n"
"ds_read_b64_tr_b16 a[38:39], v9 offset:12800\n"
"v_mfma_f32_16x16x32_bf16 v[204:207], a[84:87], a[108:111], v[204:207]\n"
"ds_read_b128 a[108:111], v13 offset:18432\n"
"v_mfma_f32_16x16x32_bf16 v[204:207], a[80:83], a[104:107], v[204:207]\n"
"ds_read_b128 a[104:107], v12 offset:18432\n"
"ds_read_b64_tr_b16 a[40:41], v9 offset:8200\n"
"v_mfma_f32_16x16x32_bf16 v[208:211], a[80:83], a[112:115], v[208:211]\n"
"ds_read_b64_tr_b16 a[42:43], v9 offset:8712\n"
"buffer_load_dwordx4 v6, s[40:43], s82 offen offset:2176  lds\n"
"v_mfma_f32_16x16x32_bf16 v[208:211], a[84:87], a[116:119], v[208:211]\n"
"ds_read_b64_tr_b16 a[44:45], v9 offset:12296\n"
"v_mfma_f32_16x16x32_bf16 v[236:239], a[92:95], a[116:119], v[236:239]\n"
"ds_read_b128 a[116:119], v13 offset:20480\n"
"v_mfma_f32_16x16x32_bf16 v[236:239], a[88:91], a[112:115], v[236:239]\n"
"ds_read_b128 a[112:115], v12 offset:20480\n"
"v_mfma_f32_16x16x32_bf16 v[240:243], a[88:91], a[120:123], v[240:243]\n"
"ds_read_b64_tr_b16 a[46:47], v9 offset:12808\n"
"v_mfma_f32_16x16x32_bf16 v[240:243], a[92:95], a[124:127], v[240:243]\n"
"ds_read_b64_tr_b16 a[48:49], v15 offset:8192\n"
"buffer_load_dwordx4 v6, s[40:43], s83 offen offset:3200  lds\n"
"v_mfma_f32_16x16x32_bf16 v[212:215], a[84:87], a[124:127], v[212:215]\n"
"ds_read_b128 a[124:127], v13 offset:22528\n"
"v_mfma_f32_16x16x32_bf16 v[212:215], a[80:83], a[120:123], v[212:215]\n"
"ds_read_b128 a[120:123], v12 offset:22528\n"
"v_mfma_f32_16x16x32_bf16 v[216:219], a[80:83], a[128:131], v[216:219]\n"
"ds_read_b64_tr_b16 a[50:51], v15 offset:8704\n"
"v_mfma_f32_16x16x32_bf16 v[216:219], a[84:87], a[132:135], v[216:219]\n"
"s_add_u32 m0, s64, 8192\n"
"ds_read_b64_tr_b16 a[52:53], v15 offset:12288\n"
"v_mfma_f32_16x16x32_bf16 v[244:247], a[92:95], a[132:135], v[244:247]\n"
"ds_read_b128 a[132:135], v13 offset:24576\n"
"v_mfma_f32_16x16x32_bf16 v[244:247], a[88:91], a[128:131], v[244:247]\n"
"ds_read_b128 a[128:131], v12 offset:24576\n"
"v_mfma_f32_16x16x32_bf16 v[248:251], a[88:91], a[136:139], v[248:251]\n"
"ds_read_b64_tr_b16 a[54:55], v15 offset:12800\n"
"v_mfma_f32_16x16x32_bf16 v[248:251], a[92:95], a[140:143], v[248:251]\n"
"ds_read_b64_tr_b16 a[56:57], v15 offset:8200\n"
"v_mfma_f32_16x16x32_bf16 v[220:223], a[84:87], a[140:143], v[220:223]\n"
"ds_read_b128 a[140:143], v13 offset:26624\n"
"v_mfma_f32_16x16x32_bf16 v[220:223], a[80:83], a[136:139], v[220:223]\n"
"ds_read_b128 a[136:139], v12 offset:26624\n"
"v_mfma_f32_16x16x32_bf16 v[224:227], a[80:83], a[144:147], v[224:227]\n"
"ds_read_b64_tr_b16 a[58:59], v15 offset:8712\n"
"v_mfma_f32_16x16x32_bf16 v[224:227], a[84:87], a[148:151], v[224:227]\n"
"s_sub_u32 s67, 1048576, s75\n"
"ds_read_b64_tr_b16 a[60:61], v15 offset:12296\n"
"v_mfma_f32_16x16x32_bf16 v[252:255], a[92:95], a[148:151], v[252:255]\n"
"ds_read_b128 a[148:151], v13 offset:28672\n"
"v_mfma_f32_16x16x32_bf16 v[252:255], a[88:91], a[144:147], v[252:255]\n"
"ds_read_b128 a[144:147], v12 offset:28672\n"
"ds_read_b64_tr_b16 a[62:63], v15 offset:12808\n"
"v_mfma_f32_16x16x32_bf16 a[28:31], a[88:91], a[152:155], a[28:31]\n"
"v_mfma_f32_16x16x32_bf16 a[28:31], a[92:95], a[156:159], a[28:31]\n"
"v_mfma_f32_16x16x32_bf16 a[24:27], a[84:87], a[156:159], a[24:27]\n"
"ds_read_b128 a[156:159], v13 offset:30720\n"
"v_mfma_f32_16x16x32_bf16 a[24:27], a[80:83], a[152:155], a[24:27]\n"
"s_waitcnt lgkmcnt(15)\n"
"ds_read_b128 a[152:155], v12 offset:30720\n"
"v_mfma_f32_16x16x32_bf16 v[32:35], a[32:35], a[96:99], v[32:35]\n"
"v_mfma_f32_16x16x32_bf16 v[32:35], a[36:39], a[100:103], v[32:35]\n"
"s_waitcnt lgkmcnt(2)\n"
"v_mfma_f32_16x16x32_bf16 v[60:63], a[44:47], a[100:103], v[60:63]\n"
"buffer_load_dwordx4 v11, s[52:55], s67 offen offset:0  lds\n"
"v_mfma_f32_16x16x32_bf16 v[60:63], a[40:43], a[96:99], v[60:63]\n"
"v_mfma_f32_16x16x32_bf16 v[64:67], a[40:43], a[104:107], v[64:67]\n"
"s_sub_u32 s67, 1063936, s75\n"
"v_mfma_f32_16x16x32_bf16 v[64:67], a[44:47], a[108:111], v[64:67]\n"
"v_mfma_f32_16x16x32_bf16 v[36:39], a[36:39], a[108:111], v[36:39]\n"
"v_mfma_f32_16x16x32_bf16 v[36:39], a[32:35], a[104:107], v[36:39]\n"
"buffer_load_dwordx4 v11, s[52:55], s67 offen offset:1024  lds\n"
"v_mfma_f32_16x16x32_bf16 v[40:43], a[32:35], a[112:115], v[40:43]\n"
"v_mfma_f32_16x16x32_bf16 v[40:43], a[36:39], a[116:119], v[40:43]\n"
"s_sub_u32 s67, 1079296, s75\n"
"v_mfma_f32_16x16x32_bf16 v[68:71], a[44:47], a[116:119], v[68:71]\n"
"v_mfma_f32_16x16x32_bf16 v[68:71], a[40:43], a[112:115], v[68:71]\n"
"buffer_load_dwordx4 v11, s[52:55], s67 offen offset:2048  lds\n"
"v_mfma_f32_16x16x32_bf16 v[72:75], a[40:43], a[120:123], v[72:75]\n"
"v_mfma_f32_16x16x32_bf16 v[72:75], a[44:47], a[124:127], v[72:75]\n"
"s_sub_u32 s67, 1094656, s75\n"
"v_mfma_f32_16x16x32_bf16 v[44:47], a[36:39], a[124:127], v[44:47]\n"
"v_mfma_f32_16x16x32_bf16 v[44:47], a[32:35], a[120:123], v[44:47]\n"
"v_mfma_f32_16x16x32_bf16 v[48:51], a[32:35], a[128:131], v[48:51]\n"
"buffer_load_dwordx4 v11, s[52:55], s67 offen offset:3072  lds\n"
"v_mfma_f32_16x16x32_bf16 v[48:51], a[36:39], a[132:135], v[48:51]\n"
"s_add_u32 m0, s64, 12288\n"
"v_mfma_f32_16x16x32_bf16 v[76:79], a[44:47], a[132:135], v[76:79]\n"
"s_waitcnt vmcnt(12)\n"
"ds_read_b64_tr_b16 a[64:65], v9 offset:0\n"
"v_mfma_f32_16x16x32_bf16 v[76:79], a[40:43], a[128:131], v[76:79]\n"
"ds_read_b64_tr_b16 a[66:67], v9 offset:512\n"
"v_mfma_f32_16x16x32_bf16 v[80:83], a[40:43], a[136:139], v[80:83]\n"
"L_st2135:\n"
"s_cbranch_execz L_st4490\n"
"s_sub_u32 s67, 1114112, s75\n"
"ds_read_b64_tr_b16 a[68:69], v9 offset:4096\n"
"v_mfma_f32_16x16x32_bf16 v[80:83], a[44:47], a[140:143], v[80:83]\n"
"ds_read_b64_tr_b16 a[70:71], v9 offset:4608\n"
"v_mfma_f32_16x16x32_bf16 v[52:55], a[36:39], a[140:143], v[52:55]\n"
"ds_read_b64_tr_b16 a[72:73], v9 offset:8\n"
"buffer_load_dwordx4 v11, s[52:55], s67 offen offset:0  lds\n"
"v_mfma_f32_16x16x32_bf16 v[52:55], a[32:35], a[136:139], v[52:55]\n"
"s_waitcnt lgkmcnt(5)\n"
"ds_read_b64_tr_b16 a[74:75], v9 offset:520\n"
"v_mfma_f32_16x16x32_bf16 v[56:59], a[32:35], a[144:147], v[56:59]\n"
"s_sub_u32 s67, 1129472, s75\n"
"ds_read_b64_tr_b16 a[76:77], v9 offset:4104\n"
"v_mfma_f32_16x16x32_bf16 v[56:59], a[36:39], a[148:151], v[56:59]\n"
"ds_read_b64_tr_b16 a[78:79], v9 offset:4616\n"
"v_mfma_f32_16x16x32_bf16 v[84:87], a[44:47], a[148:151], v[84:87]\n"
"buffer_load_dwordx4 v11, s[52:55], s67 offen offset:1024  lds\n"
"v_mfma_f32_16x16x32_bf16 v[84:87], a[40:43], a[144:147], v[84:87]\n"
"ds_read_b64_tr_b16 a[80:81], v15 offset:0\n"
"v_mfma_f32_16x16x32_bf16 a[4:7], a[40:43], a[152:155], a[4:7]\n"
"s_sub_u32 s67, 1144832, s75\n"
"ds_read_b64_tr_b16 a[82:83], v15 offset:512\n"
"v_mfma_f32_16x16x32_bf16 a[4:7], a[44:47], a[156:159], a[4:7]\n"
"ds_read_b64_tr_b16 a[84:85], v15 offset:4096\n"
"v_mfma_f32_16x16x32_bf16 a[0:3], a[36:39], a[156:159], a[0:3]\n"
"ds_read_b64_tr_b16 a[86:87], v15 offset:4608\n"
"buffer_load_dwordx4 v11, s[52:55], s67 offen offset:2048  lds\n"
"v_mfma_f32_16x16x32_bf16 a[0:3], a[32:35], a[152:155], a[0:3]\n"
"ds_read_b64_tr_b16 a[88:89], v15 offset:8\n"
"v_mfma_f32_16x16x32_bf16 v[88:91], a[48:51], a[96:99], v[88:91]\n"
"s_sub_u32 s67, 1160192, s75\n"
"ds_read_b64_tr_b16 a[90:91], v15 offset:520\n"
"v_mfma_f32_16x16x32_bf16 v[88:91], a[52:55], a[100:103], v[88:91]\n"
"ds_read_b64_tr_b16 a[92:93], v15 offset:4104\n"
"v_mfma_f32_16x16x32_bf16 v[116:119], a[60:63], a[100:103], v[116:119]\n"
"ds_read_b64_tr_b16 a[94:95], v15 offset:4616\n"
"v_mfma_f32_16x16x32_bf16 v[116:119], a[56:59], a[96:99], v[116:119]\n"
"buffer_load_dwordx4 v11, s[52:55], s67 offen offset:3072  lds\n"
"v_mfma_f32_16x16x32_bf16 v[120:123], a[56:59], a[104:107], v[120:123]\n"
"v_mfma_f32_16x16x32_bf16 v[120:123], a[60:63], a[108:111], v[120:123]\n"
"s_add_u32 m0, s64, 0\n"
"v_mfma_f32_16x16x32_bf16 v[92:95], a[52:55], a[108:111], v[92:95]\n"
"v_mfma_f32_16x16x32_bf16 v[92:95], a[48:51], a[104:107], v[92:95]\n"
"s_sub_u32 s67, 1048704, s75\n"
"v_mfma_f32_16x16x32_bf16 v[96:99], a[48:51], a[112:115], v[96:99]\n"
"s_waitcnt lgkmcnt(0)\n"
"v_mfma_f32_16x16x32_bf16 v[96:99], a[52:55], a[116:119], v[96:99]\n"
"buffer_load_dwordx4 v11, s[52:55], s67 offen offset:0  lds\n"
"v_mfma_f32_16x16x32_bf16 v[124:127], a[60:63], a[116:119], v[124:127]\n"
"v_mfma_f32_16x16x32_bf16 v[124:127], a[56:59], a[112:115], v[124:127]\n"
"s_sub_u32 s67, 1064064, s75\n"
"v_mfma_f32_16x16x32_bf16 v[128:131], a[56:59], a[120:123], v[128:131]\n"
"v_mfma_f32_16x16x32_bf16 v[128:131], a[60:63], a[124:127], v[128:131]\n"
"v_mfma_f32_16x16x32_bf16 v[100:103], a[52:55], a[124:127], v[100:103]\n"
"buffer_load_dwordx4 v11, s[52:55], s67 offen offset:1024  lds\n"
"v_mfma_f32_16x16x32_bf16 v[100:103], a[48:51], a[120:123], v[100:103]\n"
"v_mfma_f32_16x16x32_bf16 v[104:107], a[48:51], a[128:131], v[104:107]\n"
"s_sub_u32 s67, 1079424, s75\n"
"v_mfma_f32_16x16x32_bf16 v[104:107], a[52:55], a[132:135], v[104:107]\n"
"v_mfma_f32_16x16x32_bf16 v[132:135], a[60:63], a[132:135], v[132:135]\n"
"buffer_load_dwordx4 v11, s[52:55], s67 offen offset:2048  lds\n"
"v_mfma_f32_16x16x32_bf16 v[132:135], a[56:59], a[128:131], v[132:135]\n"
"v_mfma_f32_16x16x32_bf16 v[136:139], a[56:59], a[136:139], v[136:139]\n"
"s_sub_u32 s67, 1094784, s75\n"
"v_mfma_f32_16x16x32_bf16 v[136:139], a[60:63], a[140:143], v[136:139]\n"
"v_mfma_f32_16x16x32_bf16 v[108:111], a[52:55], a[140:143], v[108:111]\n"
"v_mfma_f32_16x16x32_bf16 v[108:111], a[48:51], a[136:139], v[108:111]\n"
"buffer_load_dwordx4 v11, s[52:55], s67 offen offset:3072  lds\n"
"v_mfma_f32_16x16x32_bf16 v[112:115], a[48:51], a[144:147], v[112:115]\n"
"s_add_u32 m0, s64, 4096\n"
"v_mfma_f32_16x16x32_bf16 v[112:115], a[52:55], a[148:151], v[112:115]\n"
"v_mfma_f32_16x16x32_bf16 v[140:143], a[60:63], a[148:151], v[140:143]\n"
"s_sub_u32 s67, 1114240, s75\n"
"v_mfma_f32_16x16x32_bf16 v[140:143], a[56:59], a[144:147], v[140:143]\n"
"v_mfma_f32_16x16x32_bf16 a[12:15], a[56:59], a[152:155], a[12:15]\n"
"buffer_load_dwordx4 v11, s[52:55], s67 offen offset:0  lds\n"
"v_mfma_f32_16x16x32_bf16 a[12:15], a[60:63], a[156:159], a[12:15]\n"
"v_mfma_f32_16x16x32_bf16 a[8:11], a[52:55], a[156:159], a[8:11]\n"
"s_sub_u32 s67, 1129600, s75\n"
"v_mfma_f32_16x16x32_bf16 a[8:11], a[48:51], a[152:155], a[8:11]\n"
"v_mfma_f32_16x16x32_bf16 v[144:147], a[64:67], a[96:99], v[144:147]\n"
"buffer_load_dwordx4 v11, s[52:55], s67 offen offset:1024  lds\n"
"v_mfma_f32_16x16x32_bf16 v[144:147], a[68:71], a[100:103], v[144:147]\n"
"v_mfma_f32_16x16x32_bf16 v[172:175], a[76:79], a[100:103], v[172:175]\n"
"s_sub_u32 s67, 1144960, s75\n"
"v_mfma_f32_16x16x32_bf16 v[172:175], a[72:75], a[96:99], v[172:175]\n"
"v_mfma_f32_16x16x32_bf16 v[176:179], a[72:75], a[104:107], v[176:179]\n"
"v_mfma_f32_16x16x32_bf16 v[176:179], a[76:79], a[108:111], v[176:179]\n"
"s_sub_u32 s71, s72, s64\n"
"v_mfma_f32_16x16x32_bf16 v[148:151], a[68:71], a[108:111], v[148:151]\n"
"buffer_load_dwordx4 v11, s[52:55], s67 offen offset:2048  lds\n"
"v_mfma_f32_16x16x32_bf16 v[148:151], a[64:67], a[104:107], v[148:151]\n"
"s_sub_u32 s67, 1160320, s75\n"
"v_mfma_f32_16x16x32_bf16 v[152:155], a[64:67], a[112:115], v[152:155]\n"
"s_sub_u32 s66, 0, s73\n"
"s_add_u32 s71, s71, 57344\n"
"v_mfma_f32_16x16x32_bf16 v[152:155], a[68:71], a[116:119], v[152:155]\n"
"buffer_load_dwordx4 v11, s[52:55], s67 offen offset:3072  lds\n"
"v_mfma_f32_16x16x32_bf16 v[180:183], a[76:79], a[116:119], v[180:183]\n"
"v_add_u32 v12, s66, v7\n"
"v_mfma_f32_16x16x32_bf16 v[180:183], a[72:75], a[112:115], v[180:183]\n"
"v_add_u32 v20, s71, v7\n"
"v_mfma_f32_16x16x32_bf16 v[184:187], a[72:75], a[120:123], v[184:187]\n"
"v_add_u32 v13, s66, v8\n"
"v_mfma_f32_16x16x32_bf16 v[184:187], a[76:79], a[124:127], v[184:187]\n"
"v_add_u32 v21, s71, v8\n"
"v_mfma_f32_16x16x32_bf16 v[156:159], a[68:71], a[124:127], v[156:159]\n"
"s_cmp_lt_u32 %16, 2\n"
"s_cselect_b32 s67, 16384, 0\n"
"v_mfma_f32_16x16x32_bf16 v[156:159], a[64:67], a[120:123], v[156:159]\n"
"s_add_u32 s67, s67, s72\n"
"v_mfma_f32_16x16x32_bf16 v[160:163], a[64:67], a[128:131], v[160:163]\n"
"s_waitcnt vmcnt(16)\n"
"ds_read_b128 a[192:195], v20 offset:0\n"
"v_mfma_f32_16x16x32_bf16 v[160:163], a[68:71], a[132:135], v[160:163]\n"
"v_mfma_f32_16x16x32_bf16 v[188:191], a[76:79], a[132:135], v[188:191]\n"
"ds_read_b128 a[196:199], v21 offset:0\n"
"v_mfma_f32_16x16x32_bf16 v[188:191], a[72:75], a[128:131], v[188:191]\n"
"ds_read_b128 a[200:203], v20 offset:2048\n"
"v_mfma_f32_16x16x32_bf16 v[192:195], a[72:75], a[136:139], v[192:195]\n"
"s_add_u32 m0, s67, 57344\n"
"v_mfma_f32_16x16x32_bf16 v[192:195], a[76:79], a[140:143], v[192:195]\n"
"ds_read_b128 a[204:207], v21 offset:2048\n"
"v_mfma_f32_16x16x32_bf16 v[164:167], a[68:71], a[140:143], v[164:167]\n"
"v_mfma_f32_16x16x32_bf16 v[164:167], a[64:67], a[136:139], v[164:167]\n"
"v_mfma_f32_16x16x32_bf16 v[168:171], a[64:67], a[144:147], v[168:171]\n"
"s_sub_u32 s67, 1280, s60\n"
"v_mfma_f32_16x16x32_bf16 v[168:171], a[68:71], a[148:151], v[168:171]\n"
"v_mfma_f32_16x16x32_bf16 v[196:199], a[76:79], a[148:151], v[196:199]\n"
"v_mfma_f32_16x16x32_bf16 v[196:199], a[72:75], a[144:147], v[196:199]\n"
"s_waitcnt lgkmcnt(0)\n"
"v_mfma_f32_16x16x32_bf16 a[20:23], a[72:75], a[152:155], a[20:23]\n"
"buffer_load_dwordx4 v6, s[48:51], s67 offen offset:0  lds\n"
"v_mfma_f32_16x16x32_bf16 a[20:23], a[76:79], a[156:159], a[20:23]\n"
"v_mfma_f32_16x16x32_bf16 a[16:19], a[68:71], a[156:159], a[16:19]\n"
"s_sub_u32 s67, 16640, s60\n"
"v_mfma_f32_16x16x32_bf16 a[16:19], a[64:67], a[152:155], a[16:19]\n"
"s_barrier\n"
"s_waitcnt vmcnt(9)\n"
"ds_read_b64_tr_b16 a[32:33], v9 offset:8192\n"
"v_mfma_f32_16x16x32_bf16 v[200:203], a[80:83], a[96:99], v[200:203]\n"
"ds_read_b64_tr_b16 a[34:35], v9 offset:8704\n"
"buffer_load_dwordx4 v6, s[48:51], s67 offen offset:1024  lds\n"
"v_mfma_f32_16x16x32_bf16 v[200:203], a[84:87], a[100:103], v[200:203]\n"
"ds_read_b64_tr_b16 a[36:37], v9 offset:12288\n"
"v_mfma_f32_16x16x32_bf16 v[228:231], a[92:95], a[100:103], v[228:231]\n"
"s_sub_u32 s67, 32000, s60\n"
"ds_read_b128 a[100:103], v13 offset:32768\n"
"v_mfma_f32_16x16x32_bf16 v[228:231], a[88:91], a[96:99], v[228:231]\n"
"ds_read_b128 a[96:99], v12 offset:32768\n"
"v_mfma_f32_16x16x32_bf16 v[232:235], a[88:91], a[104:107], v[232:235]\n"
"ds_read_b64_tr_b16 a[38:39], v9 offset:12800\n"
"v_mfma_f32_16x16x32_bf16 v[232:235], a[92:95], a[108:111], v[232:235]\n"
"ds_read_b64_tr_b16 a[40:41], v9 offset:8200\n"
"buffer_load_dwordx4 v6, s[48:51], s67 offen offset:2048  lds\n"
"v_mfma_f32_16x16x32_bf16 v[204:207], a[84:87], a[108:111], v[204:207]\n"
"ds_read_b128 a[108:111], v13 offset:34816\n"
"v_mfma_f32_16x16x32_bf16 v[204:207], a[80:83], a[104:107], v[204:207]\n"
"s_sub_u32 s67, 47360, s60\n"
"ds_read_b128 a[104:107], v12 offset:34816\n"
"v_mfma_f32_16x16x32_bf16 v[208:211], a[80:83], a[112:115], v[208:211]\n"
"ds_read_b64_tr_b16 a[42:43], v9 offset:8712\n"
"v_mfma_f32_16x16x32_bf16 v[208:211], a[84:87], a[116:119], v[208:211]\n"
"ds_read_b64_tr_b16 a[44:45], v9 offset:12296\n"
"v_mfma_f32_16x16x32_bf16 v[236:239], a[92:95], a[116:119], v[236:239]\n"
"ds_read_b128 a[116:119], v13 offset:36864\n"
"buffer_load_dwordx4 v6, s[48:51], s67 offen offset:3072  lds\n"
"v_mfma_f32_16x16x32_bf16 v[236:239], a[88:91], a[112:115], v[236:239]\n"
"ds_read_b128 a[112:115], v12 offset:36864\n"
"v_mfma_f32_16x16x32_bf16 v[240:243], a[88:91], a[120:123], v[240:243]\n"
"s_add_u32 m0, s64, 16256\n"
"ds_read_b64_tr_b16 a[46:47], v9 offset:12808\n"
"v_mfma_f32_16x16x32_bf16 v[240:243], a[92:95], a[124:127], v[240:243]\n"
"ds_read_b64_tr_b16 a[48:49], v15 offset:8192\n"
"v_mfma_f32_16x16x32_bf16 v[212:215], a[84:87], a[124:127], v[212:215]\n"
"ds_read_b128 a[124:127], v13 offset:38912\n"
"buffer_load_dwordx4 v6, s[40:43], s84 offen offset:128  lds\n"
"v_mfma_f32_16x16x32_bf16 v[212:215], a[80:83], a[120:123], v[212:215]\n"
"ds_read_b128 a[120:123], v12 offset:38912\n"
"v_mfma_f32_16x16x32_bf16 v[216:219], a[80:83], a[128:131], v[216:219]\n"
"ds_read_b64_tr_b16 a[50:51], v15 offset:8704\n"
"v_mfma_f32_16x16x32_bf16 v[216:219], a[84:87], a[132:135], v[216:219]\n"
"ds_read_b64_tr_b16 a[52:53], v15 offset:12288\n"
"v_mfma_f32_16x16x32_bf16 v[244:247], a[92:95], a[132:135], v[244:247]\n"
"ds_read_b128 a[132:135], v13 offset:24576\n"
"buffer_load_dwordx4 v6, s[40:43], s85 offen offset:1152  lds\n"
"v_mfma_f32_16x16x32_bf16 v[244:247], a[88:91], a[128:131], v[244:247]\n"
"ds_read_b128 a[128:131], v12 offset:24576\n"
"v_mfma_f32_16x16x32_bf16 v[248:251], a[88:91], a[136:139], v[248:251]\n"
"ds_read_b64_tr_b16 a[54:55], v15 offset:12800\n"
"v_mfma_f32_16x16x32_bf16 v[248:251], a[92:95], a[140:143], v[248:251]\n"
"ds_read_b64_tr_b16 a[56:57], v15 offset:8200\n"
"v_mfma_f32_16x16x32_bf16 v[220:223], a[84:87], a[140:143], v[220:223]\n"
"ds_read_b128 a[140:143], v13 offset:26624\n"
"buffer_load_dwordx4 v6, s[40:43], s86 offen offset:2176  lds\n"
"v_mfma_f32_16x16x32_bf16 v[220:223], a[80:83], a[136:139], v[220:223]\n"
"ds_read_b128 a[136:139], v12 offset:26624\n"
"v_mfma_f32_16x16x32_bf16 v[224:227], a[80:83], a[144:147], v[224:227]\n"
"ds_read_b64_tr_b16 a[58:59], v15 offset:8712\n"
"v_mfma_f32_16x16x32_bf16 v[224:227], a[84:87], a[148:151], v[224:227]\n"
"ds_read_b64_tr_b16 a[60:61], v15 offset:12296\n"
"v_mfma_f32_16x16x32_bf16 v[252:255], a[92:95], a[148:151], v[252:255]\n"
"ds_read_b128 a[148:151], v13 offset:28672\n"
"v_mfma_f32_16x16x32_bf16 v[252:255], a[88:91], a[144:147], v[252:255]\n"
"ds_read_b128 a[144:147], v12 offset:28672\n"
"ds_read_b64_tr_b16 a[62:63], v15 offset:12808\n"
"v_mfma_f32_16x16x32_bf16 a[28:31], a[88:91], a[152:155], a[28:31]\n"
"buffer_load_dwordx4 v6, s[40:43], s87 offen offset:3200  lds\n"
"v_mfma_f32_16x16x32_bf16 a[28:31], a[92:95], a[156:159], a[28:31]\n"
"v_mfma_f32_16x16x32_bf16 a[24:27], a[84:87], a[156:159], a[24:27]\n"
"s_add_u32 m0, s64, 8192\n"
"ds_read_b128 a[156:159], v13 offset:30720\n"
"v_mfma_f32_16x16x32_bf16 a[24:27], a[80:83], a[152:155], a[24:27]\n"
"s_waitcnt lgkmcnt(15)\n"
"ds_read_b128 a[152:155], v12 offset:30720\n"
"v_mfma_f32_16x16x32_bf16 v[32:35], a[32:35], a[96:99], v[32:35]\n"
"s_sub_u32 s67, 1310720, s75\n"
"v_mfma_f32_16x16x32_bf16 v[32:35], a[36:39], a[100:103], v[32:35]\n"
"s_waitcnt lgkmcnt(2)\n"
"v_mfma_f32_16x16x32_bf16 v[60:63], a[44:47], a[100:103], v[60:63]\n"
"buffer_load_dwordx4 v11, s[52:55], s67 offen offset:0  lds\n"
"v_mfma_f32_16x16x32_bf16 v[60:63], a[40:43], a[96:99], v[60:63]\n"
"v_mfma_f32_16x16x32_bf16 v[64:67], a[40:43], a[104:107], v[64:67]\n"
"s_sub_u32 s67, 1326080, s75\n"
"v_mfma_f32_16x16x32_bf16 v[64:67], a[44:47], a[108:111], v[64:67]\n"
"v_mfma_f32_16x16x32_bf16 v[36:39], a[36:39], a[108:111], v[36:39]\n"
"buffer_load_dwordx4 v11, s[52:55], s67 offen offset:1024  lds\n"
"v_mfma_f32_16x16x32_bf16 v[36:39], a[32:35], a[104:107], v[36:39]\n"
"v_mfma_f32_16x16x32_bf16 v[40:43], a[32:35], a[112:115], v[40:43]\n"
"s_sub_u32 s67, 1341440, s75\n"
"v_mfma_f32_16x16x32_bf16 v[40:43], a[36:39], a[116:119], v[40:43]\n"
"v_mfma_f32_16x16x32_bf16 v[68:71], a[44:47], a[116:119], v[68:71]\n"
"v_mfma_f32_16x16x32_bf16 v[68:71], a[40:43], a[112:115], v[68:71]\n"
"buffer_load_dwordx4 v11, s[52:55], s67 offen offset:2048  lds\n"
"v_mfma_f32_16x16x32_bf16 v[72:75], a[40:43], a[120:123], v[72:75]\n"
"v_mfma_f32_16x16x32_bf16 v[72:75], a[44:47], a[124:127], v[72:75]\n"
"s_sub_u32 s67, 1356800, s75\n"
"v_mfma_f32_16x16x32_bf16 v[44:47], a[36:39], a[124:127], v[44:47]\n"
"v_mfma_f32_16x16x32_bf16 v[44:47], a[32:35], a[120:123], v[44:47]\n"
"buffer_load_dwordx4 v11, s[52:55], s67 offen offset:3072  lds\n"
"v_mfma_f32_16x16x32_bf16 v[48:51], a[32:35], a[128:131], v[48:51]\n"
"v_mfma_f32_16x16x32_bf16 v[48:51], a[36:39], a[132:135], v[48:51]\n"
"s_add_u32 m0, s64, 12288\n"
"v_mfma_f32_16x16x32_bf16 v[76:79], a[44:47], a[132:135], v[76:79]\n"
"s_waitcnt vmcnt(12)\n"
"ds_read_b64_tr_b16 a[64:65], v9 offset:0\n"
"v_mfma_f32_16x16x32_bf16 v[76:79], a[40:43], a[128:131], v[76:79]\n"
"s_sub_u32 s67, 1376256, s75\n"
"ds_read_b64_tr_b16 a[66:67], v9 offset:512\n"
"v_mfma_f32_16x16x32_bf16 v[80:83], a[40:43], a[136:139], v[80:83]\n"
"ds_read_b64_tr_b16 a[68:69], v9 offset:4096\n"
"v_mfma_f32_16x16x32_bf16 v[80:83], a[44:47], a[140:143], v[80:83]\n"
"ds_read_b64_tr_b16 a[70:71], v9 offset:4608\n"
"buffer_load_dwordx4 v11, s[52:55], s67 offen offset:0  lds\n"
"v_mfma_f32_16x16x32_bf16 v[52:55], a[36:39], a[140:143], v[52:55]\n"
"s_sub_u32 s67, 1391616, s75\n"
"ds_read_b64_tr_b16 a[72:73], v9 offset:8\n"
"v_mfma_f32_16x16x32_bf16 v[52:55], a[32:35], a[136:139], v[52:55]\n"
"s_waitcnt lgkmcnt(5)\n"
"ds_read_b64_tr_b16 a[74:75], v9 offset:520\n"
"v_mfma_f32_16x16x32_bf16 v[56:59], a[32:35], a[144:147], v[56:59]\n"
"ds_read_b64_tr_b16 a[76:77], v9 offset:4104\n"
"v_mfma_f32_16x16x32_bf16 v[56:59], a[36:39], a[148:151], v[56:59]\n"
"ds_read_b64_tr_b16 a[78:79], v9 offset:4616\n"
"v_mfma_f32_16x16x32_bf16 v[84:87], a[44:47], a[148:151], v[84:87]\n"
"buffer_load_dwordx4 v11, s[52:55], s67 offen offset:1024  lds\n"
"v_mfma_f32_16x16x32_bf16 v[84:87], a[40:43], a[144:147], v[84:87]\n"
"ds_read_b64_tr_b16 a[80:81], v15 offset:0\n"
"v_mfma_f32_16x16x32_bf16 a[4:7], a[40:43], a[152:155], a[4:7]\n"
"s_sub_u32 s67, 1406976, s75\n"
"ds_read_b64_tr_b16 a[82:83], v15 offset:512\n"
"v_mfma_f32_16x16x32_bf16 a[4:7], a[44:47], a[156:159], a[4:7]\n"
"ds_read_b64_tr_b16 a[84:85], v15 offset:4096\n"
"v_mfma_f32_16x16x32_bf16 a[0:3], a[36:39], a[156:159], a[0:3]\n"
"ds_read_b64_tr_b16 a[86:87], v15 offset:4608\n"
"buffer_load_dwordx4 v11, s[52:55], s67 offen offset:2048  lds\n"
"v_mfma_f32_16x16x32_bf16 a[0:3], a[32:35], a[152:155], a[0:3]\n"
"ds_read_b64_tr_b16 a[88:89], v15 offset:8\n"
"v_mfma_f32_16x16x32_bf16 v[88:91], a[48:51], a[96:99], v[88:91]\n"
"s_sub_u32 s67, 1422336, s75\n"
"ds_read_b64_tr_b16 a[90:91], v15 offset:520\n"
"v_mfma_f32_16x16x32_bf16 v[88:91], a[52:55], a[100:103], v[88:91]\n"
"ds_read_b64_tr_b16 a[92:93], v15 offset:4104\n"
"v_mfma_f32_16x16x32_bf16 v[116:119], a[60:63], a[100:103], v[116:119]\n"
"ds_read_b64_tr_b16 a[94:95], v15 offset:4616\n"
"v_mfma_f32_16x16x32_bf16 v[116:119], a[56:59], a[96:99], v[116:119]\n"
"buffer_load_dwordx4 v11, s[52:55], s67 offen offset:3072  lds\n"
"v_mfma_f32_16x16x32_bf16 v[120:123], a[56:59], a[104:107], v[120:123]\n"
"v_mfma_f32_16x16x32_bf16 v[120:123], a[60:63], a[108:111], v[120:123]\n"
"s_add_u32 m0, s64, 0\n"
"v_mfma_f32_16x16x32_bf16 v[92:95], a[52:55], a[108:111], v[92:95]\n"
"v_mfma_f32_16x16x32_bf16 v[92:95], a[48:51], a[104:107], v[92:95]\n"
"s_sub_u32 s67, 1310848, s75\n"
"v_mfma_f32_16x16x32_bf16 v[96:99], a[48:51], a[112:115], v[96:99]\n"
"s_waitcnt lgkmcnt(0)\n"
"s_mov_b32 s99, 0\n"
"v_mfma_f32_16x16x32_bf16 v[96:99], a[52:55], a[116:119], v[96:99]\n"
"s_mov_b32 s98, 0\n"
"buffer_load_dwordx4 v11, s[52:55], s67 offen offset:0  lds\n"
"v_mfma_f32_16x16x32_bf16 v[124:127], a[60:63], a[116:119], v[124:127]\n"
"s_memtime s[76:77]\n"
"s_load_dword s98, s[96:97], 0 glc\n"
"v_mfma_f32_16x16x32_bf16 v[124:127], a[56:59], a[112:115], v[124:127]\n"
"s_sub_u32 s67, 1326208, s75\n"
"v_mfma_f32_16x16x32_bf16 v[128:131], a[56:59], a[120:123], v[128:131]\n"
"v_mfma_f32_16x16x32_bf16 v[128:131], a[60:63], a[124:127], v[128:131]\n"
"v_mfma_f32_16x16x32_bf16 v[100:103], a[52:55], a[124:127], v[100:103]\n"
"buffer_load_dwordx4 v11, s[52:55], s67 offen offset:1024  lds\n"
"v_mfma_f32_16x16x32_bf16 v[100:103], a[48:51], a[120:123], v[100:103]\n"
"v_mfma_f32_16x16x32_bf16 v[104:107], a[48:51], a[128:131], v[104:107]\n"
"s_sub_u32 s67, 1341568, s75\n"
"v_mfma_f32_16x16x32_bf16 v[104:107], a[52:55], a[132:135], v[104:107]\n"
"v_mfma_f32_16x16x32_bf16 v[132:135], a[60:63], a[132:135], v[132:135]\n"
"v_mfma_f32_16x16x32_bf16 v[132:135], a[56:59], a[128:131], v[132:135]\n"
"s_mov_b32 s29, s76\n"
"v_mfma_f32_16x16x32_bf16 v[136:139], a[56:59], a[136:139], v[136:139]\n"
"buffer_load_dwordx4 v11, s[52:55], s67 offen offset:2048  lds\n"
"v_mfma_f32_16x16x32_bf16 v[136:139], a[60:63], a[140:143], v[136:139]\n"
"s_sub_u32 s67, 1356928, s75\n"
"v_mfma_f32_16x16x32_bf16 v[108:111], a[52:55], a[140:143], v[108:111]\n"
"s_sub_u32 s99, s99, s98\n"
"s_sub_u32 s99, s99, s98\n"
"v_mfma_f32_16x16x32_bf16 v[108:111], a[48:51], a[136:139], v[108:111]\n"
"s_sub_u32 s99, s99, s98\n"
"v_mfma_f32_16x16x32_bf16 v[112:115], a[48:51], a[144:147], v[112:115]\n"
"s_sub_u32 s99, s99, s98\n"
"buffer_load_dwordx4 v11, s[52:55], s67 offen offset:3072  lds\n"
"v_mfma_f32_16x16x32_bf16 v[112:115], a[52:55], a[148:151], v[112:115]\n"
"s_add_u32 m0, s64, 4096\n"
"v_mfma_f32_16x16x32_bf16 v[140:143], a[60:63], a[148:151], v[140:143]\n"
"s_sub_u32 s99, s99, s98\n"
"s_sub_u32 s99, s99, s98\n"
"v_mfma_f32_16x16x32_bf16 v[140:143], a[56:59], a[144:147], v[140:143]\n"
"s_sub_u32 s99, s99, s98\n"
"s_sub_u32 s67, 1376384, s75\n"
"v_mfma_f32_16x16x32_bf16 a[12:15], a[56:59], a[152:155], a[12:15]\n"
"s_sub_u32 s99, s99, s98\n"
"v_mfma_f32_16x16x32_bf16 a[12:15], a[60:63], a[156:159], a[12:15]\n"
"buffer_load_dwordx4 v11, s[52:55], s67 offen offset:0  lds\n"
"v_mfma_f32_16x16x32_bf16 a[8:11], a[52:55], a[156:159], a[8:11]\n"
"s_sub_u32 s99, s99, s98\n"
"s_sub_u32 s99, s99, s98\n"
"v_mfma_f32_16x16x32_bf16 a[8:11], a[48:51], a[152:155], a[8:11]\n"
"s_sub_u32 s99, s99, s98\n"
"s_sub_u32 s67, 1391744, s75\n"
"v_mfma_f32_16x16x32_bf16 v[144:147], a[64:67], a[96:99], v[144:147]\n"
"s_sub_u32 s99, s99, s98\n"
"v_mfma_f32_16x16x32_bf16 v[144:147], a[68:71], a[100:103], v[144:147]\n"
"buffer_load_dwordx4 v11, s[52:55], s67 offen offset:1024  lds\n"
"v_mfma_f32_16x16x32_bf16 v[172:175], a[76:79], a[100:103], v[172:175]\n"
"s_sub_u32 s99, s99, s98\n"
"s_sub_u32 s99, s99, s98\n"
"v_mfma_f32_16x16x32_bf16 v[172:175], a[72:75], a[96:99], v[172:175]\n"
"s_sub_u32 s99, s99, s98\n"
"s_sub_u32 s67, 1407104, s75\n"
"v_mfma_f32_16x16x32_bf16 v[176:179], a[72:75], a[104:107], v[176:179]\n"
"s_sub_u32 s99, s99, s98\n"
"v_mfma_f32_16x16x32_bf16 v[176:179], a[76:79], a[108:111], v[176:179]\n"
"buffer_load_dwordx4 v11, s[52:55], s67 offen offset:2048  lds\n"
"v_mfma_f32_16x16x32_bf16 v[148:151], a[68:71], a[108:111], v[148:151]\n"
"s_sub_u32 s99, s99, s98\n"
"s_sub_u32 s99, s99, s98\n"
"v_mfma_f32_16x16x32_bf16 v[148:151], a[64:67], a[104:107], v[148:151]\n"
"s_sub_u32 s99, s99, s98\n"
"s_sub_u32 s71, s64, s64\n"
"v_mfma_f32_16x16x32_bf16 v[152:155], a[64:67], a[112:115], v[152:155]\n"
"s_sub_u32 s66, 40960, s73\n"
"s_sub_u32 s99, s99, s98\n"
"v_mfma_f32_16x16x32_bf16 v[152:155], a[68:71], a[116:119], v[152:155]\n"
"s_add_u32 s71, s71, 16384\n"
"s_sub_u32 s67, 1422464, s75\n"
"v_mfma_f32_16x16x32_bf16 v[180:183], a[76:79], a[116:119], v[180:183]\n"
"s_sub_u32 s99, s99, s98\n"
"v_add_u32 v12, s66, v7\n"
"s_sub_u32 s99, s99, s98\n"
"v_mfma_f32_16x16x32_bf16 v[180:183], a[72:75], a[112:115], v[180:183]\n"
"s_sub_u32 s99, s99, s98\n"
"buffer_load_dwordx4 v11, s[52:55], s67 offen offset:3072  lds\n"
"v_mfma_f32_16x16x32_bf16 v[184:187], a[72:75], a[120:123], v[184:187]\n"
"s_sub_u32 s99, s99, s98\n"
"v_add_u32 v13, s66, v8\n"
"s_cmp_lt_u32 %16, 2\n"
"s_cselect_b32 s67, 16384, 0\n"
"v_mfma_f32_16x16x32_bf16 v[184:187], a[76:79], a[124:127], v[184:187]\n"
"s_memtime s[76:77]\n"
"v_add_u32 v20, s71, v7\n"
"s_add_u32 s67, s67, s72\n"
"v_mfma_f32_16x16x32_bf16 v[156:159], a[68:71], a[124:127], v[156:159]\n"
"s_add_u32 m0, s67, 98304\n"
"s_sub_u32 s67, 1536, s60\n"
"v_mfma_f32_16x16x32_bf16 v[156:159], a[64:67], a[120:123], v[156:159]\n"
"s_waitcnt lgkmcnt(0)\n"
"buffer_load_dwordx4 v6, s[48:51], s67 offen offset:0  lds\n"
"v_add_u32 v21, s71, v8\n"
"v_mfma_f32_16x16x32_bf16 v[160:163], a[64:67], a[128:131], v[160:163]\n"
"s_sub_u32 s29, s76, s29\n"
"s_sub_u32 s67, 16896, s60\n"
"v_mfma_f32_16x16x32_bf16 v[160:163], a[68:71], a[132:135], v[160:163]\n"
"s_cmp_lt_u32 s29, 620\n"
"s_cselect_b32 s99, s99, 0\n"
"v_mfma_f32_16x16x32_bf16 v[188:191], a[76:79], a[132:135], v[188:191]\n"
"s_max_u32 s30, s30, s99\n"
"v_mfma_f32_16x16x32_bf16 v[188:191], a[72:75], a[128:131], v[188:191]\n"
"s_waitcnt vmcnt(17)\n"
"ds_read_b128 a[208:211], v20 offset:0\n"
"buffer_load_dwordx4 v6, s[48:51], s67 offen offset:1024  lds\n"
"v_mfma_f32_16x16x32_bf16 v[192:195], a[72:75], a[136:139], v[192:195]\n"
"ds_read_b128 a[212:215], v21 offset:0\n"
"v_mfma_f32_16x16x32_bf16 v[192:195], a[76:79], a[140:143], v[192:195]\n"
"s_sub_u32 s67, 32256, s60\n"
"ds_read_b128 a[216:219], v20 offset:2048\n"
"v_mfma_f32_16x16x32_bf16 v[164:167], a[68:71], a[140:143], v[164:167]\n"
"v_mfma_f32_16x16x32_bf16 v[164:167], a[64:67], a[136:139], v[164:167]\n"
"ds_read_b128 a[220:223], v21 offset:2048\n"
"v_mfma_f32_16x16x32_bf16 v[168:171], a[64:67], a[144:147], v[168:171]\n"
"buffer_load_dwordx4 v6, s[48:51], s67 offen offset:2048  lds\n"
"v_mfma_f32_16x16x32_bf16 v[168:171], a[68:71], a[148:151], v[168:171]\n"
"v_mfma_f32_16x16x32_bf16 v[196:199], a[76:79], a[148:151], v[196:199]\n"
"s_sub_u32 s67, 47616, s60\n"
"v_mfma_f32_16x16x32_bf16 v[196:199], a[72:75], a[144:147], v[196:199]\n"
"v_mfma_f32_16x16x32_bf16 a[20:23], a[72:75], a[152:155], a[20:23]\n"
"buffer_load_dwordx4 v6, s[48:51], s67 offen offset:3072  lds\n"
"v_mfma_f32_16x16x32_bf16 a[20:23], a[76:79], a[156:159], a[20:23]\n"
"s_waitcnt lgkmcnt(0)\n"
"v_mfma_f32_16x16x32_bf16 a[16:19], a[68:71], a[156:159], a[16:19]\n"
"s_add_u32 m0, s64, 20352\n"
"v_mfma_f32_16x16x32_bf16 a[16:19], a[64:67], a[152:155], a[16:19]\n"
"s_barrier\n"
"s_waitcnt vmcnt(12)\n"
"ds_read_b64_tr_b16 a[32:33], v9 offset:8192\n"
"v_mfma_f32_16x16x32_bf16 v[200:203], a[80:83], a[96:99], v[200:203]\n"
"ds_read_b64_tr_b16 a[34:35], v9 offset:8704\n"
"v_mfma_f32_16x16x32_bf16 v[200:203], a[84:87], a[100:103], v[200:203]\n"
"ds_read_b64_tr_b16 a[36:37], v9 offset:12288\n"
"buffer_load_dwordx4 v6, s[40:43], s88 offen offset:128  lds\n"
"v_mfma_f32_16x16x32_bf16 v[228:231], a[92:95], a[100:103], v[228:231]\n"
"ds_read_b128 a[100:103], v13 offset:32768\n"
"v_mfma_f32_16x16x32_bf16 v[228:231], a[88:91], a[96:99], v[228:231]\n"
"ds_read_b128 a[96:99], v12 offset:32768\n"
"v_mfma_f32_16x16x32_bf16 v[232:235], a[88:91], a[104:107], v[232:235]\n"
"ds_read_b64_tr_b16 a[38:39], v9 offset:12800\n"
"buffer_load_dwordx4 v6, s[40:43], s89 offen offset:1152  lds\n"
"v_mfma_f32_16x16x32_bf16 v[232:235], a[92:95], a[108:111], v[232:235]\n"
"ds_read_b64_tr_b16 a[40:41], v9 offset:8200\n"
"v_mfma_f32_16x16x32_bf16 v[204:207], a[84:87], a[108:111], v[204:207]\n"
"ds_read_b128 a[108:111], v13 offset:34816\n"
"v_mfma_f32_16x16x32_bf16 v[204:207], a[80:83], a[104:107], v[204:207]\n"
"ds_read_b128 a[104:107], v12 offset:34816\n"
"v_mfma_f32_16x16x32_bf16 v[208:211], a[80:83], a[112:115], v[208:211]\n"
"ds_read_b64_tr_b16 a[42:43], v9 offset:8712\n"
"v_mfma_f32_16x16x32_bf16 v[208:211], a[84:87], a[116:119], v[208:211]\n"
"ds_read_b64_tr_b16 a[44:45], v9 offset:12296\n"
"buffer_load_dwordx4 v6, s[40:43], s90 offen offset:2176  lds\n"
"v_mfma_f32_16x16x32_bf16 v[236:239], a[92:95], a[116:119], v[236:239]\n"
"ds_read_b128 a[116:119], v13 offset:36864\n"
"v_mfma_f32_16x16x32_bf16 v[236:239], a[88:91], a[112:115], v[236:239]\n"
"ds_read_b128 a[112:115], v12 offset:36864\n"
"v_mfma_f32_16x16x32_bf16 v[240:243], a[88:91], a[120:123], v[240:243]\n"
"ds_read_b64_tr_b16 a[46:47], v9 offset:12808\n"
"v_mfma_f32_16x16x32_bf16 v[240:243], a[92:95], a[124:127], v[240:243]\n"
"ds_read_b64_tr_b16 a[48:49], v15 offset:8192\n"
"buffer_load_dwordx4 v6, s[40:43], s91 offen offset:3200  lds\n"
"v_mfma_f32_16x16x32_bf16 v[212:215], a[84:87], a[124:127], v[212:215]\n"
"ds_read_b128 a[124:127], v13 offset:38912\n"
"v_mfma_f32_16x16x32_bf16 v[212:215], a[80:83], a[120:123], v[212:215]\n"
"ds_read_b128 a[120:123], v12 offset:38912\n"
"v_mfma_f32_16x16x32_bf16 v[216:219], a[80:83], a[128:131], v[216:219]\n"
"ds_read_b64_tr_b16 a[50:51], v15 offset:8704\n"
"v_mfma_f32_16x16x32_bf16 v[216:219], a[84:87], a[132:135], v[216:219]\n"
"s_add_u32 m0, s64, 8192\n"
"ds_read_b64_tr_b16 a[52:53], v15 offset:12288\n"
"v_mfma_f32_16x16x32_bf16 v[244:247], a[92:95], a[132:135], v[244:247]\n"
"ds_read_b128 a[132:135], v13 offset:24576\n"
"v_mfma_f32_16x16x32_bf16 v[244:247], a[88:91], a[128:131], v[244:247]\n"
"ds_read_b128 a[128:131], v12 offset:24576\n"
"v_mfma_f32_16x16x32_bf16 v[248:251], a[88:91], a[136:139], v[248:251]\n"
"ds_read_b64_tr_b16 a[54:55], v15 offset:12800\n"
"v_mfma_f32_16x16x32_bf16 v[248:251], a[92:95], a[140:143], v[248:251]\n"
"ds_read_b64_tr_b16 a[56:57], v15 offset:8200\n"
"v_mfma_f32_16x16x32_bf16 v[220:223], a[84:87], a[140:143], v[220:223]\n"
"ds_read_b128 a[140:143], v13 offset:26624\n"
"v_mfma_f32_16x16x32_bf16 v[220:223], a[80:83], a[136:139], v[220:223]\n"
"ds_read_b128 a[136:139], v12 offset:26624\n"
"v_mfma_f32_16x16x32_bf16 v[224:227], a[80:83], a[144:147], v[224:227]\n"
"ds_read_b64_tr_b16 a[58:59], v15 offset:8712\n"
"v_mfma_f32_16x16x32_bf16 v[224:227], a[84:87], a[148:151], v[224:227]\n"
"s_sub_u32 s67, 1572864, s75\n"
"ds_read_b64_tr_b16 a[60:61], v15 offset:12296\n"
"v_mfma_f32_16x16x32_bf16 v[252:255], a[92:95], a[148:151], v[252:255]\n"
"ds_read_b128 a[148:151], v13 offset:28672\n"
"v_mfma_f32_16x16x32_bf16 v[252:255], a[88:91], a[144:147], v[252:255]\n"
"ds_read_b128 a[144:147], v12 offset:28672\n"
"ds_read_b64_tr_b16 a[62:63], v15 offset:12808\n"
"v_mfma_f32_16x16x32_bf16 a[28:31], a[88:91], a[152:155], a[28:31]\n"
"v_mfma_f32_16x16x32_bf16 a[28:31], a[92:95], a[156:159], a[28:31]\n"
"v_mfma_f32_16x16x32_bf16 a[24:27], a[84:87], a[156:159], a[24:27]\n"
"ds_read_b128 a[156:159], v13 offset:30720\n"
"v_mfma_f32_16x16x32_bf16 a[24:27], a[80:83], a[152:155], a[24:27]\n"
"s_waitcnt lgkmcnt(15)\n"
"ds_read_b128 a[152:155], v12 offset:30720\n"
"v_mfma_f32_16x16x32_bf16 v[32:35], a[32:35], a[96:99], v[32:35]\n"
"v_mfma_f32_16x16x32_bf16 v[32:35], a[36:39], a[100:103], v[32:35]\n"
"s_waitcnt lgkmcnt(2)\n"
"v_mfma_f32_16x16x32_bf16 v[60:63], a[44:47], a[100:103], v[60:63]\n"
"buffer_load_dwordx4 v11, s[52:55], s67 offen offset:0  lds\n"
"v_mfma_f32_16x16x32_bf16 v[60:63], a[40:43], a[96:99], v[60:63]\n"
"v_mfma_f32_16x16x32_bf16 v[64:67], a[40:43], a[104:107], v[64:67]\n"
"s_sub_u32 s67, 1588224, s75\n"
"v_mfma_f32_16x16x32_bf16 v[64:67], a[44:47], a[108:111], v[64:67]\n"
"v_mfma_f32_16x16x32_bf16 v[36:39], a[36:39], a[108:111], v[36:39]\n"
"v_mfma_f32_16x16x32_bf16 v[36:39], a[32:35], a[104:107], v[36:39]\n"
"buffer_load_dwordx4 v11, s[52:55], s67 offen offset:1024  lds\n"
"v_mfma_f32_16x16x32_bf16 v[40:43], a[32:35], a[112:115], v[40:43]\n"
"v_mfma_f32_16x16x32_bf16 v[40:43], a[36:39], a[116:119], v[40:43]\n"
"s_sub_u32 s67, 1603584, s75\n"
"v_mfma_f32_16x16x32_bf16 v[68:71], a[44:47], a[116:119], v[68:71]\n"
"v_mfma_f32_16x16x32_bf16 v[68:71], a[40:43], a[112:115], v[68:71]\n"
"buffer_load_dwordx4 v11, s[52:55], s67 offen offset:2048  lds\n"
"v_mfma_f32_16x16x32_bf16 v[72:75], a[40:43], a[120:123], v[72:75]\n"
"v_mfma_f32_16x16x32_bf16 v[72:75], a[44:47], a[124:127], v[72:75]\n"
"s_sub_u32 s67, 1618944, s75\n"
"v_mfma_f32_16x16x32_bf16 v[44:47], a[36:39], a[124:127], v[44:47]\n"
"v_mfma_f32_16x16x32_bf16 v[44:47], a[32:35], a[120:123], v[44:47]\n"
"v_mfma_f32_16x16x32_bf16 v[48:51], a[32:35], a[128:131], v[48:51]\n"
"buffer_load_dwordx4 v11, s[52:55], s67 offen offset:3072  lds\n"
"v_mfma_f32_16x16x32_bf16 v[48:51], a[36:39], a[132:135], v[48:51]\n"
"s_add_u32 m0, s64, 12288\n"
"v_mfma_f32_16x16x32_bf16 v[76:79], a[44:47], a[132:135], v[76:79]\n"
"s_waitcnt vmcnt(12)\n"
"ds_read_b64_tr_b16 a[64:65], v9 offset:0\n"
"v_mfma_f32_16x16x32_bf16 v[76:79], a[40:43], a[128:131], v[76:79]\n"
"s_sub_u32 s67, 1638400, s75\n"
"ds_read_b64_tr_b16 a[66:67], v9 offset:512\n"
"v_mfma_f32_16x16x32_bf16 v[80:83], a[40:43], a[136:139], v[80:83]\n"
"ds_read_b64_tr_b16 a[68:69], v9 offset:4096\n"
"v_mfma_f32_16x16x32_bf16 v[80:83], a[44:47], a[140:143], v[80:83]\n"
"ds_read_b64_tr_b16 a[70:71], v9 offset:4608\n"
"buffer_load_dwordx4 v11, s[52:55], s67 offen offset:0  lds\n"
"v_mfma_f32_16x16x32_bf16 v[52:55], a[36:39], a[140:143], v[52:55]\n"
"s_sub_u32 s67, 1653760, s75\n"
"ds_read_b64_tr_b16 a[72:73], v9 offset:8\n"
"v_mfma_f32_16x16x32_bf16 v[52:55], a[32:35], a[136:139], v[52:55]\n"
"s_waitcnt lgkmcnt(5)\n"
"ds_read_b64_tr_b16 a[74:75], v9 offset:520\n"
"v_mfma_f32_16x16x32_bf16 v[56:59], a[32:35], a[144:147], v[56:59]\n"
"L_st2680:\n"
"s_cbranch_execz L_retA\n"
"ds_read_b64_tr_b16 a[76:77], v9 offset:4104\n"
"v_mfma_f32_16x16x32_bf16 v[56:59], a[36:39], a[148:151], v[56:59]\n"
"ds_read_b64_tr_b16 a[78:79], v9 offset:4616\n"
"v_mfma_f32_16x16x32_bf16 v[84:87], a[44:47], a[148:151], v[84:87]\n"
"buffer_load_dwordx4 v11, s[52:55], s67 offen offset:1024  lds\n"
"v_mfma_f32_16x16x32_bf16 v[84:87], a[40:43], a[144:147], v[84:87]\n"
"ds_read_b64_tr_b16 a[80:81], v15 offset:0\n"
"v_mfma_f32_16x16x32_bf16 a[4:7], a[40:43], a[152:155], a[4:7]\n"
"s_sub_u32 s67, 1669120, s75\n"
"ds_read_b64_tr_b16 a[82:83], v15 offset:512\n"
"v_mfma_f32_16x16x32_bf16 a[4:7], a[44:47], a[156:159], a[4:7]\n"
"ds_read_b64_tr_b16 a[84:85], v15 offset:4096\n"
"v_mfma_f32_16x16x32_bf16 a[0:3], a[36:39], a[156:159], a[0:3]\n"
"ds_read_b64_tr_b16 a[86:87], v15 offset:4608\n"
"buffer_load_dwordx4 v11, s[52:55], s67 offen offset:2048  lds\n"
"v_mfma_f32_16x16x32_bf16 a[0:3], a[32:35], a[152:155], a[0:3]\n"
"ds_read_b64_tr_b16 a[88:89], v15 offset:8\n"
"v_mfma_f32_16x16x32_bf16 v[88:91], a[48:51], a[96:99], v[88:91]\n"
"s_sub_u32 s67, 1684480, s75\n"
"ds_read_b64_tr_b16 a[90:91], v15 offset:520\n"
"v_mfma_f32_16x16x32_bf16 v[88:91], a[52:55], a[100:103], v[88:91]\n"
"ds_read_b64_tr_b16 a[92:93], v15 offset:4104\n"
"v_mfma_f32_16x16x32_bf16 v[116:119], a[60:63], a[100:103], v[116:119]\n"
"ds_read_b64_tr_b16 a[94:95], v15 offset:4616\n"
"v_mfma_f32_16x16x32_bf16 v[116:119], a[56:59], a[96:99], v[116:119]\n"
"buffer_load_dwordx4 v11, s[52:55], s67 offen offset:3072  lds\n"
"v_mfma_f32_16x16x32_bf16 v[120:123], a[56:59], a[104:107], v[120:123]\n"
"v_mfma_f32_16x16x32_bf16 v[120:123], a[60:63], a[108:111], v[120:123]\n"
"s_add_u32 m0, s64, 0\n"
"v_mfma_f32_16x16x32_bf16 v[92:95], a[52:55], a[108:111], v[92:95]\n"
"v_mfma_f32_16x16x32_bf16 v[92:95], a[48:51], a[104:107], v[92:95]\n"
"s_sub_u32 s67, 1572992, s75\n"
"v_mfma_f32_16x16x32_bf16 v[96:99], a[48:51], a[112:115], v[96:99]\n"
"s_waitcnt lgkmcnt(0)\n"
"s_mov_b32 s99, 0\n"
"v_mfma_f32_16x16x32_bf16 v[96:99], a[52:55], a[116:119], v[96:99]\n"
"s_mov_b32 s98, 0\n"
"buffer_load_dwordx4 v11, s[52:55], s67 offen offset:0  lds\n"
"v_mfma_f32_16x16x32_bf16 v[124:127], a[60:63], a[116:119], v[124:127]\n"
"s_memtime s[76:77]\n"
"s_load_dword s98, s[96:97], 8320 glc\n"
"v_mfma_f32_16x16x32_bf16 v[124:127], a[56:59], a[112:115], v[124:127]\n"
"s_sub_u32 s67, 1588352, s75\n"
"v_mfma_f32_16x16x32_bf16 v[128:131], a[56:59], a[120:123], v[128:131]\n"
"v_mfma_f32_16x16x32_bf16 v[128:131], a[60:63], a[124:127], v[128:131]\n"
"buffer_load_dwordx4 v11, s[52:55], s67 offen offset:1024  lds\n"
"v_mfma_f32_16x16x32_bf16 v[100:103], a[52:55], a[124:127], v[100:103]\n"
"v_mfma_f32_16x16x32_bf16 v[100:103], a[48:51], a[120:123], v[100:103]\n"
"s_sub_u32 s67, 1603712, s75\n"
"v_mfma_f32_16x16x32_bf16 v[104:107], a[48:51], a[128:131], v[104:107]\n"
"v_mfma_f32_16x16x32_bf16 v[104:107], a[52:55], a[132:135], v[104:107]\n"
"v_mfma_f32_16x16x32_bf16 v[132:135], a[60:63], a[132:135], v[132:135]\n"
"buffer_load_dwordx4 v11, s[52:55], s67 offen offset:2048  lds\n"
"v_mfma_f32_16x16x32_bf16 v[132:135], a[56:59], a[128:131], v[132:135]\n"
"s_mov_b32 s29, s76\n"
"v_mfma_f32_16x16x32_bf16 v[136:139], a[56:59], a[136:139], v[136:139]\n"
"s_sub_u32 s67, 1619072, s75\n"
"v_mfma_f32_16x16x32_bf16 v[136:139], a[60:63], a[140:143], v[136:139]\n"
"buffer_load_dwordx4 v11, s[52:55], s67 offen offset:3072  lds\n"
"v_mfma_f32_16x16x32_bf16 v[108:111], a[52:55], a[140:143], v[108:111]\n"
"s_sub_u32 s99, s99, s98\n"
"s_sub_u32 s99, s99, s98\n"
"v_mfma_f32_16x16x32_bf16 v[108:111], a[48:51], a[136:139], v[108:111]\n"
"s_sub_u32 s99, s99, s98\n"
"s_add_u32 m0, s64, 4096\n"
"v_mfma_f32_16x16x32_bf16 v[112:115], a[48:51], a[144:147], v[112:115]\n"
"s_sub_u32 s99, s99, s98\n"
"v_mfma_f32_16x16x32_bf16 v[112:115], a[52:55], a[148:151], v[112:115]\n"
"s_sub_u32 s67, 1638528, s75\n"
"v_mfma_f32_16x16x32_bf16 v[140:143], a[60:63], a[148:151], v[140:143]\n"
"s_sub_u32 s99, s99, s98\n"
"s_sub_u32 s99, s99, s98\n"
"v_mfma_f32_16x16x32_bf16 v[140:143], a[56:59], a[144:147], v[140:143]\n"
"s_sub_u32 s99, s99, s98\n"
"buffer_load_dwordx4 v11, s[52:55], s67 offen offset:0  lds\n"
"v_mfma_f32_16x16x32_bf16 a[12:15], a[56:59], a[152:155], a[12:15]\n"
"s_sub_u32 s99, s99, s98\n"
"v_mfma_f32_16x16x32_bf16 a[12:15], a[60:63], a[156:159], a[12:15]\n"
"s_sub_u32 s67, 1653888, s75\n"
"v_mfma_f32_16x16x32_bf16 a[8:11], a[52:55], a[156:159], a[8:11]\n"
"s_sub_u32 s99, s99, s98\n"
"s_sub_u32 s99, s99, s98\n"
"v_mfma_f32_16x16x32_bf16 a[8:11], a[48:51], a[152:155], a[8:11]\n"
"s_sub_u32 s99, s99, s98\n"
"v_mfma_f32_16x16x32_bf16 v[144:147], a[64:67], a[96:99], v[144:147]\n"
"s_sub_u32 s99, s99, s98\n"
"v_mfma_f32_16x16x32_bf16 v[144:147], a[68:71], a[100:103], v[144:147]\n"
"buffer_load_dwordx4 v11, s[52:55], s67 offen offset:1024  lds\n"
"v_mfma_f32_16x16x32_bf16 v[172:175], a[76:79], a[100:103], v[172:175]\n"
"s_sub_u32 s99, s99, s98\n"
"s_sub_u32 s99, s99, s98\n"
"v_mfma_f32_16x16x32_bf16 v[172:175], a[72:75], a[96:99], v[172:175]\n"
"s_sub_u32 s99, s99, s98\n"
"s_sub_u32 s67, 1669248, s75\n"
"v_mfma_f32_16x16x32_bf16 v[176:179], a[72:75], a[104:107], v[176:179]\n"
"s_sub_u32 s99, s99, s98\n"
"v_mfma_f32_16x16x32_bf16 v[176:179], a[76:79], a[108:111], v[176:179]\n"
"buffer_load_dwordx4 v11, s[52:55], s67 offen offset:2048  lds\n"
"v_mfma_f32_16x16x32_bf16 v[148:151], a[68:71], a[108:111], v[148:151]\n"
"s_sub_u32 s99, s99, s98\n"
"s_sub_u32 s99, s99, s98\n"
"v_mfma_f32_16x16x32_bf16 v[148:151], a[64:67], a[104:107], v[148:151]\n"
"s_sub_u32 s99, s99, s98\n"
"s_sub_u32 s71, s64, s64\n"
"s_sub_u32 s67, 1684608, s75\n"
"v_mfma_f32_16x16x32_bf16 v[152:155], a[64:67], a[112:115], v[152:155]\n"
"s_sub_u32 s66, 81920, s73\n"
"s_sub_u32 s99, s99, s98\n"
"v_mfma_f32_16x16x32_bf16 v[152:155], a[68:71], a[116:119], v[152:155]\n"
"s_add_u32 s71, s71, 20480\n"
"buffer_load_dwordx4 v11, s[52:55], s67 offen offset:3072  lds\n"
"v_mfma_f32_16x16x32_bf16 v[180:183], a[76:79], a[116:119], v[180:183]\n"
"s_sub_u32 s99, s99, s98\n"
"v_add_u32 v12, s66, v7\n"
"s_sub_u32 s99, s99, s98\n"
"v_mfma_f32_16x16x32_bf16 v[180:183], a[72:75], a[112:115], v[180:183]\n"
"s_sub_u32 s99, s99, s98\n"
"s_cmp_lt_u32 %16, 2\n"
"s_cselect_b32 s67, 16384, 0\n"
"s_add_u32 s67, s67, s72\n"
"v_mfma_f32_16x16x32_bf16 v[184:187], a[72:75], a[120:123], v[184:187]\n"
"s_sub_u32 s99, s99, s98\n"
"v_add_u32 v13, s66, v8\n"
"s_add_u32 m0, s67, 139264\n"
"v_mfma_f32_16x16x32_bf16 v[184:187], a[76:79], a[124:127], v[184:187]\n"
"s_memtime s[76:77]\n"
"v_add_u32 v20, s71, v7\n"
"s_sub_u32 s67, 1792, s60\n"
"v_mfma_f32_16x16x32_bf16 v[156:159], a[68:71], a[124:127], v[156:159]\n"
"buffer_load_dwordx4 v6, s[48:51], s67 offen offset:0  lds\n"
"v_mfma_f32_16x16x32_bf16 v[156:159], a[64:67], a[120:123], v[156:159]\n"
"s_waitcnt lgkmcnt(0)\n"
"v_add_u32 v21, s71, v8\n"
"v_mfma_f32_16x16x32_bf16 v[160:163], a[64:67], a[128:131], v[160:163]\n"
"s_sub_u32 s29, s76, s29\n"
"s_sub_u32 s67, 17152, s60\n"
"v_mfma_f32_16x16x32_bf16 v[160:163], a[68:71], a[132:135], v[160:163]\n"
"s_cmp_lt_u32 s29, 620\n"
"s_cselect_b32 s99, s99, 0\n"
"v_mfma_f32_16x16x32_bf16 v[188:191], a[76:79], a[132:135], v[188:191]\n"
"s_max_u32 s31, s31, s99\n"
"buffer_load_dwordx4 v6, s[48:51], s67 offen offset:1024  lds\n"
"v_mfma_f32_16x16x32_bf16 v[188:191], a[72:75], a[128:131], v[188:191]\n"
"s_waitcnt vmcnt(18)\n"
"ds_read_b128 a[224:227], v20 offset:0\n"
"v_mfma_f32_16x16x32_bf16 v[192:195], a[72:75], a[136:139], v[192:195]\n"
"s_sub_u32 s67, 32512, s60\n"
"ds_read_b128 a[228:231], v21 offset:0\n"
"v_mfma_f32_16x16x32_bf16 v[192:195], a[76:79], a[140:143], v[192:195]\n"
"ds_read_b128 a[232:235], v20 offset:2048\n"
"v_mfma_f32_16x16x32_bf16 v[164:167], a[68:71], a[140:143], v[164:167]\n"
"v_mfma_f32_16x16x32_bf16 v[164:167], a[64:67], a[136:139], v[164:167]\n"
"ds_read_b128 a[236:239], v21 offset:2048\n"
"v_mfma_f32_16x16x32_bf16 v[168:171], a[64:67], a[144:147], v[168:171]\n"
"buffer_load_dwordx4 v6, s[48:51], s67 offen offset:2048  lds\n"
"v_mfma_f32_16x16x32_bf16 v[168:171], a[68:71], a[148:151], v[168:171]\n"
"v_mfma_f32_16x16x32_bf16 v[196:199], a[76:79], a[148:151], v[196:199]\n"
"s_sub_u32 s67, 47872, s60\n"
"v_mfma_f32_16x16x32_bf16 v[196:199], a[72:75], a[144:147], v[196:199]\n"
"v_mfma_f32_16x16x32_bf16 a[20:23], a[72:75], a[152:155], a[20:23]\n"
"buffer_load_dwordx4 v6, s[48:51], s67 offen offset:3072  lds\n"
"v_mfma_f32_16x16x32_bf16 a[20:23], a[76:79], a[156:159], a[20:23]\n"
"s_waitcnt lgkmcnt(0)\n"
"v_mfma_f32_16x16x32_bf16 a[16:19], a[68:71], a[156:159], a[16:19]\n"
"s_add_u32 m0, s64, 16256\n"
"v_mfma_f32_16x16x32_bf16 a[16:19], a[64:67], a[152:155], a[16:19]\n"
"s_barrier\n"
"s_waitcnt vmcnt(12)\n"
"ds_read_b64_tr_b16 a[32:33], v9 offset:8192\n"
"v_mfma_f32_16x16x32_bf16 v[200:203], a[80:83], a[96:99], v[200:203]\n"
"ds_read_b64_tr_b16 a[34:35], v9 offset:8704\n"
"buffer_load_dwordx4 v6, s[40:43], s92 offen offset:128  lds\n"
"v_mfma_f32_16x16x32_bf16 v[200:203], a[84:87], a[100:103], v[200:203]\n"
"ds_read_b64_tr_b16 a[36:37], v9 offset:12288\n"
"v_mfma_f32_16x16x32_bf16 v[228:231], a[92:95], a[100:103], v[228:231]\n"
"ds_read_b128 a[100:103], v13 offset:32768\n"
"v_mfma_f32_16x16x32_bf16 v[228:231], a[88:91], a[96:99], v[228:231]\n"
"ds_read_b128 a[96:99], v12 offset:32768\n"
"buffer_load_dwordx4 v6, s[40:43], s93 offen offset:1152  lds\n"
"v_mfma_f32_16x16x32_bf16 v[232:235], a[88:91], a[104:107], v[232:235]\n"
"ds_read_b64_tr_b16 a[38:39], v9 offset:12800\n"
"v_mfma_f32_16x16x32_bf16 v[232:235], a[92:95], a[108:111], v[232:235]\n"
"ds_read_b64_tr_b16 a[40:41], v9 offset:8200\n"
"v_mfma_f32_16x16x32_bf16 v[204:207], a[84:87], a[108:111], v[204:207]\n"
"ds_read_b128 a[108:111], v13 offset:34816\n"
"v_mfma_f32_16x16x32_bf16 v[204:207], a[80:83], a[104:107], v[204:207]\n"
"ds_read_b128 a[104:107], v12 offset:34816\n"
"v_mfma_f32_16x16x32_bf16 v[208:211], a[80:83], a[112:115], v[208:211]\n"
"ds_read_b64_tr_b16 a[42:43], v9 offset:8712\n"
"buffer_load_dwordx4 v6, s[40:43], s94 offen offset:2176  lds\n"
"v_mfma_f32_16x16x32_bf16 v[208:211], a[84:87], a[116:119], v[208:211]\n"
"ds_read_b64_tr_b16 a[44:45], v9 offset:12296\n"
"v_mfma_f32_16x16x32_bf16 v[236:239], a[92:95], a[116:119], v[236:239]\n"
"ds_read_b128 a[116:119], v13 offset:36864\n"
"v_mfma_f32_16x16x32_bf16 v[236:239], a[88:91], a[112:115], v[236:239]\n"
"ds_read_b128 a[112:115], v12 offset:36864\n"
"v_mfma_f32_16x16x32_bf16 v[240:243], a[88:91], a[120:123], v[240:243]\n"
"ds_read_b64_tr_b16 a[46:47], v9 offset:12808\n"
"v_mfma_f32_16x16x32_bf16 v[240:243], a[92:95], a[124:127], v[240:243]\n"
"ds_read_b64_tr_b16 a[48:49], v15 offset:8192\n"
"buffer_load_dwordx4 v6, s[40:43], s95 offen offset:3200  lds\n"
"v_mfma_f32_16x16x32_bf16 v[212:215], a[84:87], a[124:127], v[212:215]\n"
"ds_read_b128 a[124:127], v13 offset:38912\n"
"v_mfma_f32_16x16x32_bf16 v[212:215], a[80:83], a[120:123], v[212:215]\n"
"ds_read_b128 a[120:123], v12 offset:38912\n"
"v_mfma_f32_16x16x32_bf16 v[216:219], a[80:83], a[128:131], v[216:219]\n"
"ds_read_b64_tr_b16 a[50:51], v15 offset:8704\n"
"v_mfma_f32_16x16x32_bf16 v[216:219], a[84:87], a[132:135], v[216:219]\n"
"s_add_u32 m0, s64, 8192\n"
"ds_read_b64_tr_b16 a[52:53], v15 offset:12288\n"
"v_mfma_f32_16x16x32_bf16 v[244:247], a[92:95], a[132:135], v[244:247]\n"
"ds_read_b128 a[132:135], v13 offset:24576\n"
"v_mfma_f32_16x16x32_bf16 v[244:247], a[88:91], a[128:131], v[244:247]\n"
"ds_read_b128 a[128:131], v12 offset:24576\n"
"v_mfma_f32_16x16x32_bf16 v[248:251], a[88:91], a[136:139], v[248:251]\n"
"ds_read_b64_tr_b16 a[54:55], v15 offset:12800\n"
"v_mfma_f32_16x16x32_bf16 v[248:251], a[92:95], a[140:143], v[248:251]\n"
"ds_read_b64_tr_b16 a[56:57], v15 offset:8200\n"
"v_mfma_f32_16x16x32_bf16 v[220:223], a[84:87], a[140:143], v[220:223]\n"
"ds_read_b128 a[140:143], v13 offset:26624\n"
"v_mfma_f32_16x16x32_bf16 v[220:223], a[80:83], a[136:139], v[220:223]\n"
"ds_read_b128 a[136:139], v12 offset:26624\n"
"v_mfma_f32_16x16x32_bf16 v[224:227], a[80:83], a[144:147], v[224:227]\n"
"ds_read_b64_tr_b16 a[58:59], v15 offset:8712\n"
"v_mfma_f32_16x16x32_bf16 v[224:227], a[84:87], a[148:151], v[224:227]\n"
"s_sub_u32 s67, 1835008, s75\n"
"ds_read_b64_tr_b16 a[60:61], v15 offset:12296\n"
"v_mfma_f32_16x16x32_bf16 v[252:255], a[92:95], a[148:151], v[252:255]\n"
"ds_read_b128 a[148:151], v13 offset:28672\n"
"v_mfma_f32_16x16x32_bf16 v[252:255], a[88:91], a[144:147], v[252:255]\n"
"ds_read_b128 a[144:147], v12 offset:28672\n"
"ds_read_b64_tr_b16 a[62:63], v15 offset:12808\n"
"v_mfma_f32_16x16x32_bf16 a[28:31], a[88:91], a[152:155], a[28:31]\n"
"v_mfma_f32_16x16x32_bf16 a[28:31], a[92:95], a[156:159], a[28:31]\n"
"v_mfma_f32_16x16x32_bf16 a[24:27], a[84:87], a[156:159], a[24:27]\n"
"ds_read_b128 a[156:159], v13 offset:30720\n"
"v_mfma_f32_16x16x32_bf16 a[24:27], a[80:83], a[152:155], a[24:27]\n"
"s_waitcnt lgkmcnt(15)\n"
"ds_read_b128 a[152:155], v12 offset:30720\n"
"v_mfma_f32_16x16x32_bf16 v[32:35], a[32:35], a[96:99], v[32:35]\n"
"v_mfma_f32_16x16x32_bf16 v[32:35], a[36:39], a[100:103], v[32:35]\n"
"s_waitcnt lgkmcnt(2)\n"
"v_mfma_f32_16x16x32_bf16 v[60:63], a[44:47], a[100:103], v[60:63]\n"
"buffer_load_dwordx4 v11, s[52:55], s67 offen offset:0  lds\n"
"v_mfma_f32_16x16x32_bf16 v[60:63], a[40:43], a[96:99], v[60:63]\n"
"v_mfma_f32_16x16x32_bf16 v[64:67], a[40:43], a[104:107], v[64:67]\n"
"s_sub_u32 s67, 1850368, s75\n"
"v_mfma_f32_16x16x32_bf16 v[64:67], a[44:47], a[108:111], v[64:67]\n"
"v_mfma_f32_16x16x32_bf16 v[36:39], a[36:39], a[108:111], v[36:39]\n"
"v_mfma_f32_16x16x32_bf16 v[36:39], a[32:35], a[104:107], v[36:39]\n"
"buffer_load_dwordx4 v11, s[52:55], s67 offen offset:1024  lds\n"
"v_mfma_f32_16x16x32_bf16 v[40:43], a[32:35], a[112:115], v[40:43]\n"
"v_mfma_f32_16x16x32_bf16 v[40:43], a[36:39], a[116:119], v[40:43]\n"
"s_sub_u32 s67, 1865728, s75\n"
"v_mfma_f32_16x16x32_bf16 v[68:71], a[44:47], a[116:119], v[68:71]\n"
"v_mfma_f32_16x16x32_bf16 v[68:71], a[40:43], a[112:115], v[68:71]\n"
"buffer_load_dwordx4 v11, s[52:55], s67 offen offset:2048  lds\n"
"v_mfma_f32_16x16x32_bf16 v[72:75], a[40:43], a[120:123], v[72:75]\n"
"v_mfma_f32_16x16x32_bf16 v[72:75], a[44:47], a[124:127], v[72:75]\n"
"s_sub_u32 s67, 1881088, s75\n"
"v_mfma_f32_16x16x32_bf16 v[44:47], a[36:39], a[124:127], v[44:47]\n"
"v_mfma_f32_16x16x32_bf16 v[44:47], a[32:35], a[120:123], v[44:47]\n"
"v_mfma_f32_16x16x32_bf16 v[48:51], a[32:35], a[128:131], v[48:51]\n"
"buffer_load_dwordx4 v11, s[52:55], s67 offen offset:3072  lds\n"
"v_mfma_f32_16x16x32_bf16 v[48:51], a[36:39], a[132:135], v[48:51]\n"
"s_add_u32 m0, s64, 12288\n"
"v_mfma_f32_16x16x32_bf16 v[76:79], a[44:47], a[132:135], v[76:79]\n"
"s_waitcnt vmcnt(12)\n"
"ds_read_b64_tr_b16 a[64:65], v9 offset:0\n"
"v_mfma_f32_16x16x32_bf16 v[76:79], a[40:43], a[128:131], v[76:79]\n"
"ds_read_b64_tr_b16 a[66:67], v9 offset:512\n"
"v_mfma_f32_16x16x32_bf16 v[80:83], a[40:43], a[136:139], v[80:83]\n"
"s_sub_u32 s67, 1900544, s75\n"
"ds_read_b64_tr_b16 a[68:69], v9 offset:4096\n"
"v_mfma_f32_16x16x32_bf16 v[80:83], a[44:47], a[140:143], v[80:83]\n"
"ds_read_b64_tr_b16 a[70:71], v9 offset:4608\n"
"v_mfma_f32_16x16x32_bf16 v[52:55], a[36:39], a[140:143], v[52:55]\n"
"ds_read_b64_tr_b16 a[72:73], v9 offset:8\n"
"buffer_load_dwordx4 v11, s[52:55], s67 offen offset:0  lds\n"
"v_mfma_f32_16x16x32_bf16 v[52:55], a[32:35], a[136:139], v[52:55]\n"
"s_waitcnt lgkmcnt(5)\n"
"ds_read_b64_tr_b16 a[74:75], v9 offset:520\n"
"v_mfma_f32_16x16x32_bf16 v[56:59], a[32:35], a[144:147], v[56:59]\n"
"s_sub_u32 s67, 1915904, s75\n"
"ds_read_b64_tr_b16 a[76:77], v9 offset:4104\n"
"v_mfma_f32_16x16x32_bf16 v[56:59], a[36:39], a[148:151], v[56:59]\n"
"ds_read_b64_tr_b16 a[78:79], v9 offset:4616\n"
"v_mfma_f32_16x16x32_bf16 v[84:87], a[44:47], a[148:151], v[84:87]\n"
"buffer_load_dwordx4 v11, s[52:55], s67 offen offset:1024  lds\n"
"v_mfma_f32_16x16x32_bf16 v[84:87], a[40:43], a[144:147], v[84:87]\n"
"ds_read_b64_tr_b16 a[80:81], v15 offset:0\n"
"v_mfma_f32_16x16x32_bf16 a[4:7], a[40:43], a[152:155], a[4:7]\n"
"s_sub_u32 s67, 1931264, s75\n"
"ds_read_b64_tr_b16 a[82:83], v15 offset:512\n"
"v_mfma_f32_16x16x32_bf16 a[4:7], a[44:47], a[156:159], a[4:7]\n"
"ds_read_b64_tr_b16 a[84:85], v15 offset:4096\n"
"v_mfma_f32_16x16x32_bf16 a[0:3], a[36:39], a[156:159], a[0:3]\n"
"ds_read_b64_tr_b16 a[86:87], v15 offset:4608\n"
"buffer_load_dwordx4 v11, s[52:55], s67 offen offset:2048  lds\n"
"v_mfma_f32_16x16x32_bf16 a[0:3], a[32:35], a[152:155], a[0:3]\n"
"ds_read_b64_tr_b16 a[88:89], v15 offset:8\n"
"v_mfma_f32_16x16x32_bf16 v[88:91], a[48:51], a[96:99], v[88:91]\n"
"s_sub_u32 s67, 1946624, s75\n"
"ds_read_b64_tr_b16 a[90:91], v15 offset:520\n"
"v_mfma_f32_16x16x32_bf16 v[88:91], a[52:55], a[100:103], v[88:91]\n"
"ds_read_b64_tr_b16 a[92:93], v15 offset:4104\n"
"v_mfma_f32_16x16x32_bf16 v[116:119], a[60:63], a[100:103], v[116:119]\n"
"ds_read_b64_tr_b16 a[94:95], v15 offset:4616\n"
"v_mfma_f32_16x16x32_bf16 v[116:119], a[56:59], a[96:99], v[116:119]\n"
"buffer_load_dwordx4 v11, s[52:55], s67 offen offset:3072  lds\n"
"v_mfma_f32_16x16x32_bf16 v[120:123], a[56:59], a[104:107], v[120:123]\n"
"v_mfma_f32_16x16x32_bf16 v[120:123], a[60:63], a[108:111], v[120:123]\n"
"s_add_u32 m0, s64, 0\n"
"v_mfma_f32_16x16x32_bf16 v[92:95], a[52:55], a[108:111], v[92:95]\n"
"v_mfma_f32_16x16x32_bf16 v[92:95], a[48:51], a[104:107], v[92:95]\n"
"s_sub_u32 s67, 1835136, s75\n"
"v_mfma_f32_16x16x32_bf16 v[96:99], a[48:51], a[112:115], v[96:99]\n"
"s_waitcnt lgkmcnt(0)\n"
"s_mov_b32 s99, 0\n"
"v_mfma_f32_16x16x32_bf16 v[96:99], a[52:55], a[116:119], v[96:99]\n"
"s_mov_b32 s98, 0\n"
"buffer_load_dwordx4 v11, s[52:55], s67 offen offset:0  lds\n"
"v_mfma_f32_16x16x32_bf16 v[124:127], a[60:63], a[116:119], v[124:127]\n"
"s_memtime s[76:77]\n"
"s_load_dword s98, s[96:97], 128 glc\n"
"v_mfma_f32_16x16x32_bf16 v[124:127], a[56:59], a[112:115], v[124:127]\n"
"s_sub_u32 s67, 1850496, s75\n"
"v_mfma_f32_16x16x32_bf16 v[128:131], a[56:59], a[120:123], v[128:131]\n"
"v_mfma_f32_16x16x32_bf16 v[128:131], a[60:63], a[124:127], v[128:131]\n"
"v_mfma_f32_16x16x32_bf16 v[100:103], a[52:55], a[124:127], v[100:103]\n"
"buffer_load_dwordx4 v11, s[52:55], s67 offen offset:1024  lds\n"
"v_mfma_f32_16x16x32_bf16 v[100:103], a[48:51], a[120:123], v[100:103]\n"
"v_mfma_f32_16x16x32_bf16 v[104:107], a[48:51], a[128:131], v[104:107]\n"
"s_sub_u32 s67, 1865856, s75\n"
"v_mfma_f32_16x16x32_bf16 v[104:107], a[52:55], a[132:135], v[104:107]\n"
"v_mfma_f32_16x16x32_bf16 v[132:135], a[60:63], a[132:135], v[132:135]\n"
"buffer_load_dwordx4 v11, s[52:55], s67 offen offset:2048  lds\n"
"v_mfma_f32_16x16x32_bf16 v[132:135], a[56:59], a[128:131], v[132:135]\n"
"s_mov_b32 s29, s76\n"
"v_mfma_f32_16x16x32_bf16 v[136:139], a[56:59], a[136:139], v[136:139]\n"
"s_sub_u32 s67, 1881216, s75\n"
"v_mfma_f32_16x16x32_bf16 v[136:139], a[60:63], a[140:143], v[136:139]\n"
"v_mfma_f32_16x16x32_bf16 v[108:111], a[52:55], a[140:143], v[108:111]\n"
"s_sub_u32 s99, s99, s98\n"
"s_sub_u32 s99, s99, s98\n"
"v_mfma_f32_16x16x32_bf16 v[108:111], a[48:51], a[136:139], v[108:111]\n"
"s_sub_u32 s99, s99, s98\n"
"buffer_load_dwordx4 v11, s[52:55], s67 offen offset:3072  lds\n"
"v_mfma_f32_16x16x32_bf16 v[112:115], a[48:51], a[144:147], v[112:115]\n"
"s_sub_u32 s99, s99, s98\n"
"s_add_u32 m0, s64, 4096\n"
"v_mfma_f32_16x16x32_bf16 v[112:115], a[52:55], a[148:151], v[112:115]\n"
"v_mfma_f32_16x16x32_bf16 v[140:143], a[60:63], a[148:151], v[140:143]\n"
"s_sub_u32 s99, s99, s98\n"
"s_sub_u32 s99, s99, s98\n"
"buffer_load_dwordx4 v[24:27], v0, s[56:59], %25 offen sc1\n"
"v_mfma_f32_16x16x32_bf16 v[140:143], a[56:59], a[144:147], v[140:143]\n"
"s_sub_u32 s99, s99, s98\n"
"s_sub_u32 s67, 1900672, s75\n"
"v_mfma_f32_16x16x32_bf16 a[12:15], a[56:59], a[152:155], a[12:15]\n"
"s_sub_u32 s99, s99, s98\n"
"buffer_load_dwordx4 v11, s[52:55], s67 offen offset:0  lds\n"
"v_mfma_f32_16x16x32_bf16 a[12:15], a[60:63], a[156:159], a[12:15]\n"
"s_sub_u32 s67, 1916032, s75\n"
"v_mfma_f32_16x16x32_bf16 a[8:11], a[52:55], a[156:159], a[8:11]\n"
"s_sub_u32 s99, s99, s98\n"
"s_sub_u32 s99, s99, s98\n"
"v_mfma_f32_16x16x32_bf16 a[8:11], a[48:51], a[152:155], a[8:11]\n"
"s_sub_u32 s99, s99, s98\n"
"v_mfma_f32_16x16x32_bf16 v[144:147], a[64:67], a[96:99], v[144:147]\n"
"s_sub_u32 s99, s99, s98\n"
"v_mfma_f32_16x16x32_bf16 v[144:147], a[68:71], a[100:103], v[144:147]\n"
"buffer_load_dwordx4 v11, s[52:55], s67 offen offset:1024  lds\n"
"v_mfma_f32_16x16x32_bf16 v[172:175], a[76:79], a[100:103], v[172:175]\n"
"s_sub_u32 s99, s99, s98\n"
"s_sub_u32 s99, s99, s98\n"
"v_mfma_f32_16x16x32_bf16 v[172:175], a[72:75], a[96:99], v[172:175]\n"
"s_sub_u32 s99, s99, s98\n"
"s_sub_u32 s67, 1931392, s75\n"
"v_mfma_f32_16x16x32_bf16 v[176:179], a[72:75], a[104:107], v[176:179]\n"
"s_sub_u32 s99, s99, s98\n"
"v_mfma_f32_16x16x32_bf16 v[176:179], a[76:79], a[108:111], v[176:179]\n"
"buffer_load_dwordx4 v11, s[52:55], s67 offen offset:2048  lds\n"
"v_mfma_f32_16x16x32_bf16 v[148:151], a[68:71], a[108:111], v[148:151]\n"
"s_sub_u32 s99, s99, s98\n"
"s_sub_u32 s99, s99, s98\n"
"v_mfma_f32_16x16x32_bf16 v[148:151], a[64:67], a[104:107], v[148:151]\n"
"s_sub_u32 s99, s99, s98\n"
"s_sub_u32 s71, s64, s64\n"
"s_sub_u32 s67, 1946752, s75\n"
"v_mfma_f32_16x16x32_bf16 v[152:155], a[64:67], a[112:115], v[152:155]\n"
"s_sub_u32 s66, 122880, s73\n"
"s_sub_u32 s99, s99, s98\n"
"v_mfma_f32_16x16x32_bf16 v[152:155], a[68:71], a[116:119], v[152:155]\n"
"s_add_u32 s71, s71, 16384\n"
"buffer_load_dwordx4 v11, s[52:55], s67 offen offset:3072  lds\n"
"v_mfma_f32_16x16x32_bf16 v[180:183], a[76:79], a[116:119], v[180:183]\n"
"s_sub_u32 s99, s99, s98\n"
"v_add_u32 v12, s66, v7\n"
"s_sub_u32 s99, s99, s98\n"
"v_mfma_f32_16x16x32_bf16 v[180:183], a[72:75], a[112:115], v[180:183]\n"
"s_sub_u32 s99, s99, s98\n"
"v_min_u32 v19, 496, v0\n"
"v_mfma_f32_16x16x32_bf16 v[184:187], a[72:75], a[120:123], v[184:187]\n"
"s_sub_u32 s99, s99, s98\n"
"v_add_u32 v13, s66, v8\n"
"s_add_u32 s70, %21, 0\n"
"v_mfma_f32_16x16x32_bf16 v[184:187], a[76:79], a[124:127], v[184:187]\n"
"s_memtime s[76:77]\n"
"v_add_u32 v20, s71, v7\n"
"v_lshl_add_u32 v19, v19, 7, s70\n"
"v_mfma_f32_16x16x32_bf16 v[156:159], a[68:71], a[124:127], v[156:159]\n"
"buffer_load_dword v23, v19, s[52:55], 0 offen\n"
"v_mfma_f32_16x16x32_bf16 v[156:159], a[64:67], a[120:123], v[156:159]\n"
"s_waitcnt lgkmcnt(0)\n"
"v_add_u32 v21, s71, v8\n"
"v_min_u32 v19, 496, v0\n"
"v_mfma_f32_16x16x32_bf16 v[160:163], a[64:67], a[128:131], v[160:163]\n"
"s_sub_u32 s29, s76, s29\n"
"s_add_u32 s70, %21, 2097152\n"
"v_lshl_add_u32 v19, v19, 7, s70\n"
"v_mfma_f32_16x16x32_bf16 v[160:163], a[68:71], a[132:135], v[160:163]\n"
"s_cmp_lt_u32 s29, 620\n"
"s_cselect_b32 s99, s99, 0\n"
"buffer_load_dword v23, v19, s[52:55], 0 offen\n"
"v_mfma_f32_16x16x32_bf16 v[188:191], a[76:79], a[132:135], v[188:191]\n"
"s_max_u32 s30, s30, s99\n"
"v_mfma_f32_16x16x32_bf16 v[188:191], a[72:75], a[128:131], v[188:191]\n"
"s_waitcnt vmcnt(19)\n"
"ds_read_b128 a[240:243], v20 offset:0\n"
"v_mfma_f32_16x16x32_bf16 v[192:195], a[72:75], a[136:139], v[192:195]\n"
"ds_read_b128 a[244:247], v21 offset:0\n"
"v_mfma_f32_16x16x32_bf16 v[192:195], a[76:79], a[140:143], v[192:195]\n"
"ds_read_b128 a[248:251], v20 offset:2048\n"
"v_mfma_f32_16x16x32_bf16 v[164:167], a[68:71], a[140:143], v[164:167]\n"
"v_mfma_f32_16x16x32_bf16 v[164:167], a[64:67], a[136:139], v[164:167]\n"
"ds_read_b128 a[252:255], v21 offset:2048\n"
"v_mfma_f32_16x16x32_bf16 v[168:171], a[64:67], a[144:147], v[168:171]\n"
"v_mfma_f32_16x16x32_bf16 v[168:171], a[68:71], a[148:151], v[168:171]\n"
"v_mfma_f32_16x16x32_bf16 v[196:199], a[76:79], a[148:151], v[196:199]\n"
"v_mfma_f32_16x16x32_bf16 v[196:199], a[72:75], a[144:147], v[196:199]\n"
"v_mfma_f32_16x16x32_bf16 a[20:23], a[72:75], a[152:155], a[20:23]\n"
"v_mfma_f32_16x16x32_bf16 a[20:23], a[76:79], a[156:159], a[20:23]\n"
"s_waitcnt lgkmcnt(0)\n"
"v_mfma_f32_16x16x32_bf16 a[16:19], a[68:71], a[156:159], a[16:19]\n"
"v_mfma_f32_16x16x32_bf16 a[16:19], a[64:67], a[152:155], a[16:19]\n"
"s_barrier\n"
"s_waitcnt vmcnt(11)\n"
"ds_read_b64_tr_b16 a[32:33], v9 offset:8192\n"
"v_mfma_f32_16x16x32_bf16 v[200:203], a[80:83], a[96:99], v[200:203]\n"
"ds_read_b64_tr_b16 a[34:35], v9 offset:8704\n"
"v_mfma_f32_16x16x32_bf16 v[200:203], a[84:87], a[100:103], v[200:203]\n"
"ds_read_b64_tr_b16 a[36:37], v9 offset:12288\n"
"v_mfma_f32_16x16x32_bf16 v[228:231], a[92:95], a[100:103], v[228:231]\n"
"ds_read_b128 a[100:103], v13 offset:32768\n"
"v_mfma_f32_16x16x32_bf16 v[228:231], a[88:91], a[96:99], v[228:231]\n"
"ds_read_b128 a[96:99], v12 offset:32768\n"
"v_mfma_f32_16x16x32_bf16 v[232:235], a[88:91], a[104:107], v[232:235]\n"
"ds_read_b64_tr_b16 a[38:39], v9 offset:12800\n"
"v_mfma_f32_16x16x32_bf16 v[232:235], a[92:95], a[108:111], v[232:235]\n"
"ds_read_b64_tr_b16 a[40:41], v9 offset:8200\n"
"s_getpc_b64 s[70:71]\n"
"L_c11_:\n"
"s_add_u32 s70, s70, L_P6-L_c11_\n"
"s_addc_u32 s71, s71, 0\n"
"v_mfma_f32_16x16x32_bf16 v[204:207], a[84:87], a[108:111], v[204:207]\n"
"ds_read_b128 a[108:111], v13 offset:34816\n"
"v_mfma_f32_16x16x32_bf16 v[204:207], a[80:83], a[104:107], v[204:207]\n"
"s_lshr_b32 s66, %26, 3\n"
"ds_read_b128 a[104:107], v12 offset:34816\n"
"v_mfma_f32_16x16x32_bf16 v[208:211], a[80:83], a[112:115], v[208:211]\n"
"ds_read_b64_tr_b16 a[42:43], v9 offset:8712\n"
"v_mfma_f32_16x16x32_bf16 v[208:211], a[84:87], a[116:119], v[208:211]\n"
"ds_read_b64_tr_b16 a[44:45], v9 offset:12296\n"
"v_mfma_f32_16x16x32_bf16 v[236:239], a[92:95], a[116:119], v[236:239]\n"
"s_lshl2_add_u32 s66, s66, %16\n"
"ds_read_b128 a[116:119], v13 offset:36864\n"
"v_mfma_f32_16x16x32_bf16 v[236:239], a[88:91], a[112:115], v[236:239]\n"
"ds_read_b128 a[112:115], v12 offset:36864\n"
"v_mfma_f32_16x16x32_bf16 v[240:243], a[88:91], a[120:123], v[240:243]\n"
"ds_read_b64_tr_b16 a[46:47], v9 offset:12808\n"
"v_mfma_f32_16x16x32_bf16 v[240:243], a[92:95], a[124:127], v[240:243]\n"
"s_mul_i32 s66, s66, 384\n"
"ds_read_b64_tr_b16 a[48:49], v15 offset:8192\n"
"v_mfma_f32_16x16x32_bf16 v[212:215], a[84:87], a[124:127], v[212:215]\n"
"ds_read_b128 a[124:127], v13 offset:38912\n"
"v_mfma_f32_16x16x32_bf16 v[212:215], a[80:83], a[120:123], v[212:215]\n"
"s_min_u32 s66, s66, L_headF-L_P6-384\n"
"ds_read_b128 a[120:123], v12 offset:38912\n"
"v_mfma_f32_16x16x32_bf16 v[216:219], a[80:83], a[128:131], v[216:219]\n"
"ds_read_b64_tr_b16 a[50:51], v15 offset:8704\n"
"v_mfma_f32_16x16x32_bf16 v[216:219], a[84:87], a[132:135], v[216:219]\n"
"ds_read_b64_tr_b16 a[52:53], v15 offset:12288\n"
"v_mfma_f32_16x16x32_bf16 v[244:247], a[92:95], a[132:135], v[244:247]\n"
"ds_read_b128 a[132:135], v13 offset:24576\n"
"v_min_u32 v22, 32, v0\n"
"v_mfma_f32_16x16x32_bf16 v[244:247], a[88:91], a[128:131], v[244:247]\n"
"ds_read_b128 a[128:131], v12 offset:24576\n"
"v_mfma_f32_16x16x32_bf16 v[248:251], a[88:91], a[136:139], v[248:251]\n"
"ds_read_b64_tr_b16 a[54:55], v15 offset:12800\n"
"v_mfma_f32_16x16x32_bf16 v[248:251], a[92:95], a[140:143], v[248:251]\n"
"ds_read_b64_tr_b16 a[56:57], v15 offset:8200\n"
"v_lshl_add_u32 v22, v22, 3, s66\n"
"v_mfma_f32_16x16x32_bf16 v[220:223], a[84:87], a[140:143], v[220:223]\n"
"ds_read_b128 a[140:143], v13 offset:26624\n"
"v_mfma_f32_16x16x32_bf16 v[220:223], a[80:83], a[136:139], v[220:223]\n"
"ds_read_b128 a[136:139], v12 offset:26624\n"
"global_load_dword v23, v22, s[70:71]\n"
"v_mfma_f32_16x16x32_bf16 v[224:227], a[80:83], a[144:147], v[224:227]\n"
"ds_read_b64_tr_b16 a[58:59], v15 offset:8712\n"
"v_mfma_f32_16x16x32_bf16 v[224:227], a[84:87], a[148:151], v[224:227]\n"
"ds_read_b64_tr_b16 a[60:61], v15 offset:12296\n"
"v_mfma_f32_16x16x32_bf16 v[252:255], a[92:95], a[148:151], v[252:255]\n"
"ds_read_b128 a[148:151], v13 offset:28672\n"
"v_mfma_f32_16x16x32_bf16 v[252:255], a[88:91], a[144:147], v[252:255]\n"
"ds_read_b128 a[144:147], v12 offset:28672\n"
"ds_read_b64_tr_b16 a[62:63], v15 offset:12808\n"
"v_mfma_f32_16x16x32_bf16 a[28:31], a[88:91], a[152:155], a[28:31]\n"
"v_mfma_f32_16x16x32_bf16 a[28:31], a[92:95], a[156:159], a[28:31]\n"
"v_mfma_f32_16x16x32_bf16 a[24:27], a[84:87], a[156:159], a[24:27]\n"
"ds_read_b128 a[156:159], v13 offset:30720\n"
"v_mfma_f32_16x16x32_bf16 a[24:27], a[80:83], a[152:155], a[24:27]\n"
"s_waitcnt lgkmcnt(15)\n"
"ds_read_b128 a[152:155], v12 offset:30720\n"
"v_mfma_f32_16x16x32_bf16 v[32:35], a[32:35], a[96:99], v[32:35]\n"
"v_mfma_f32_16x16x32_bf16 v[32:35], a[36:39], a[100:103], v[32:35]\n"
"s_waitcnt lgkmcnt(2)\n"
"v_mfma_f32_16x16x32_bf16 v[60:63], a[44:47], a[100:103], v[60:63]\n"
"v_mfma_f32_16x16x32_bf16 v[60:63], a[40:43], a[96:99], v[60:63]\n"
"v_mfma_f32_16x16x32_bf16 v[64:67], a[40:43], a[104:107], v[64:67]\n"
"ds_read_b128 a[160:163], v7 offset:24576\n"
"v_mfma_f32_16x16x32_bf16 v[64:67], a[44:47], a[108:111], v[64:67]\n"
"v_mfma_f32_16x16x32_bf16 v[36:39], a[36:39], a[108:111], v[36:39]\n"
"ds_read_b128 a[164:167], v8 offset:24576\n"
"v_mfma_f32_16x16x32_bf16 v[36:39], a[32:35], a[104:107], v[36:39]\n"
"v_mfma_f32_16x16x32_bf16 v[40:43], a[32:35], a[112:115], v[40:43]\n"
"ds_read_b128 a[168:171], v7 offset:26624\n"
"v_mfma_f32_16x16x32_bf16 v[40:43], a[36:39], a[116:119], v[40:43]\n"
"ds_read_b128 a[172:175], v8 offset:26624\n"
"v_mfma_f32_16x16x32_bf16 v[68:71], a[44:47], a[116:119], v[68:71]\n"
"s_add_u32 s70, s78, 1\n"
"v_mfma_f32_16x16x32_bf16 v[68:71], a[40:43], a[112:115], v[68:71]\n"
"ds_read_b128 a[176:179], v7 offset:28672\n"
"v_mfma_f32_16x16x32_bf16 v[72:75], a[40:43], a[120:123], v[72:75]\n"
"ds_read_b128 a[180:183], v8 offset:28672\n"
"v_mfma_f32_16x16x32_bf16 v[72:75], a[44:47], a[124:127], v[72:75]\n"
"v_mfma_f32_16x16x32_bf16 v[44:47], a[36:39], a[124:127], v[44:47]\n"
"ds_read_b128 a[184:187], v7 offset:30720\n"
"v_mfma_f32_16x16x32_bf16 v[44:47], a[32:35], a[120:123], v[44:47]\n"
"v_mfma_f32_16x16x32_bf16 v[48:51], a[32:35], a[128:131], v[48:51]\n"
"ds_read_b128 a[188:191], v8 offset:30720\n"
"v_mfma_f32_16x16x32_bf16 v[48:51], a[36:39], a[132:135], v[48:51]\n"
"v_mfma_f32_16x16x32_bf16 v[76:79], a[44:47], a[132:135], v[76:79]\n"
"s_waitcnt vmcnt(3)\n"
"ds_read_b64_tr_b16 a[64:65], v9 offset:0\n"
"v_mfma_f32_16x16x32_bf16 v[76:79], a[40:43], a[128:131], v[76:79]\n"
"ds_read_b64_tr_b16 a[66:67], v9 offset:512\n"
"v_mfma_f32_16x16x32_bf16 v[80:83], a[40:43], a[136:139], v[80:83]\n"
"ds_read_b64_tr_b16 a[68:69], v9 offset:4096\n"
"v_mfma_f32_16x16x32_bf16 v[80:83], a[44:47], a[140:143], v[80:83]\n"
"ds_read_b64_tr_b16 a[70:71], v9 offset:4608\n"
"v_mfma_f32_16x16x32_bf16 v[52:55], a[36:39], a[140:143], v[52:55]\n"
"L_st3242:\n"
"s_cbranch_execz L_retA\n"
"ds_read_b64_tr_b16 a[72:73], v9 offset:8\n"
"v_subrev_u32 v24, s70, v24\n"
"v_mfma_f32_16x16x32_bf16 v[52:55], a[32:35], a[136:139], v[52:55]\n"
"s_waitcnt lgkmcnt(13)\n"
"ds_read_b64_tr_b16 a[74:75], v9 offset:520\n"
"v_xor_b32 v25, s79, v25\n"
"v_mfma_f32_16x16x32_bf16 v[56:59], a[32:35], a[144:147], v[56:59]\n"
"ds_read_b64_tr_b16 a[76:77], v9 offset:4104\n"
"v_subrev_u32 v26, s70, v26\n"
"v_mfma_f32_16x16x32_bf16 v[56:59], a[36:39], a[148:151], v[56:59]\n"
"ds_read_b64_tr_b16 a[78:79], v9 offset:4616\n"
"v_xor_b32 v27, s79, v27\n"
"v_mfma_f32_16x16x32_bf16 v[84:87], a[44:47], a[148:151], v[84:87]\n"
"ds_read_b64_tr_b16 a[80:81], v15 offset:0\n"
"v_or_b32 v24, v24, v26\n"
"v_mfma_f32_16x16x32_bf16 v[84:87], a[40:43], a[144:147], v[84:87]\n"
"ds_read_b64_tr_b16 a[82:83], v15 offset:512\n"
"v_or_b32 v25, v25, v27\n"
"v_mfma_f32_16x16x32_bf16 a[4:7], a[40:43], a[152:155], a[4:7]\n"
"ds_read_b64_tr_b16 a[84:85], v15 offset:4096\n"
"v_or_b32 v24, v24, v25\n"
"v_mfma_f32_16x16x32_bf16 a[4:7], a[44:47], a[156:159], a[4:7]\n"
"ds_read_b64_tr_b16 a[86:87], v15 offset:4608\n"
"v_cmp_lt_u32 vcc, 15, v24\n"
"v_mfma_f32_16x16x32_bf16 a[0:3], a[36:39], a[156:159], a[0:3]\n"
"ds_read_b64_tr_b16 a[88:89], v15 offset:8\n"
"s_cbranch_vccnz L_slow3\n"
"L_back3:\n"
"v_mfma_f32_16x16x32_bf16 a[0:3], a[32:35], a[152:155], a[0:3]\n"
"ds_read_b64_tr_b16 a[90:91], v15 offset:520\n"
"v_mfma_f32_16x16x32_bf16 v[88:91], a[48:51], a[96:99], v[88:91]\n"
"ds_read_b64_tr_b16 a[92:93], v15 offset:4104\n"
"v_mfma_f32_16x16x32_bf16 v[88:91], a[52:55], a[100:103], v[88:91]\n"
"global_load_dwordx4 a[32:35], v0, s[34:35] offset:-4096 sc1\n"
"s_barrier\n"
"v_mfma_f32_16x16x32_bf16 v[116:119], a[60:63], a[100:103], v[116:119]\n"
"ds_read_b64_tr_b16 a[94:95], v15 offset:4616\n"
"v_mfma_f32_16x16x32_bf16 v[116:119], a[56:59], a[96:99], v[116:119]\n"
"ds_write_b64 v2, a[192:193] offset:16384\n"
"v_mfma_f32_16x16x32_bf16 v[120:123], a[56:59], a[104:107], v[120:123]\n"
"v_mfma_f32_16x16x32_bf16 v[120:123], a[60:63], a[108:111], v[120:123]\n"
"ds_write_b64 v2, a[194:195] offset:16392\n"
"v_mfma_f32_16x16x32_bf16 v[92:95], a[52:55], a[108:111], v[92:95]\n"
"v_mfma_f32_16x16x32_bf16 v[92:95], a[48:51], a[104:107], v[92:95]\n"
"ds_write_b64 v2, a[196:197] offset:17408\n"
"v_mfma_f32_16x16x32_bf16 v[96:99], a[48:51], a[112:115], v[96:99]\n"
"s_waitcnt lgkmcnt(3)\n"
"v_mfma_f32_16x16x32_bf16 v[96:99], a[52:55], a[116:119], v[96:99]\n"
"global_load_dwordx4 a[36:39], v0, s[34:35] offset:-3072 sc1\n"
"v_mfma_f32_16x16x32_bf16 v[124:127], a[60:63], a[116:119], v[124:127]\n"
"ds_write_b64 v2, a[198:199] offset:17416\n"
"v_mfma_f32_16x16x32_bf16 v[124:127], a[56:59], a[112:115], v[124:127]\n"
"ds_write_b64 v2, a[200:201] offset:18432\n"
"v_mfma_f32_16x16x32_bf16 v[128:131], a[56:59], a[120:123], v[128:131]\n"
"v_mfma_f32_16x16x32_bf16 v[128:131], a[60:63], a[124:127], v[128:131]\n"
"ds_write_b64 v2, a[202:203] offset:18440\n"
"v_mfma_f32_16x16x32_bf16 v[100:103], a[52:55], a[124:127], v[100:103]\n"
"v_mfma_f32_16x16x32_bf16 v[100:103], a[48:51], a[120:123], v[100:103]\n"
"ds_write_b64 v2, a[204:205] offset:19456\n"
"v_mfma_f32_16x16x32_bf16 v[104:107], a[48:51], a[128:131], v[104:107]\n"
"ds_write_b64 v2, a[206:207] offset:19464\n"
"v_mfma_f32_16x16x32_bf16 v[104:107], a[52:55], a[132:135], v[104:107]\n"
"global_load_dwordx4 a[40:43], v0, s[34:35] offset:-2048 sc1\n"
"v_mfma_f32_16x16x32_bf16 v[132:135], a[60:63], a[132:135], v[132:135]\n"
"ds_write_b64 v2, a[208:209] offset:20480\n"
"v_mfma_f32_16x16x32_bf16 v[132:135], a[56:59], a[128:131], v[132:135]\n"
"v_mfma_f32_16x16x32_bf16 v[136:139], a[56:59], a[136:139], v[136:139]\n"
"ds_write_b64 v2, a[210:211] offset:20488\n"
"v_mfma_f32_16x16x32_bf16 v[136:139], a[60:63], a[140:143], v[136:139]\n"
"v_mfma_f32_16x16x32_bf16 v[108:111], a[52:55], a[140:143], v[108:111]\n"
"ds_write_b64 v2, a[212:213] offset:21504\n"
"v_mfma_f32_16x16x32_bf16 v[108:111], a[48:51], a[136:139], v[108:111]\n"
"v_mfma_f32_16x16x32_bf16 v[112:115], a[48:51], a[144:147], v[112:115]\n"
"ds_write_b64 v2, a[214:215] offset:21512\n"
"v_mfma_f32_16x16x32_bf16 v[112:115], a[52:55], a[148:151], v[112:115]\n"
"global_load_dwordx4 a[44:47], v0, s[34:35] offset:-1024 sc1\n"
"v_mfma_f32_16x16x32_bf16 v[140:143], a[60:63], a[148:151], v[140:143]\n"
"ds_write_b64 v2, a[216:217] offset:22528\n"
"v_mfma_f32_16x16x32_bf16 v[140:143], a[56:59], a[144:147], v[140:143]\n"
"v_mfma_f32_16x16x32_bf16 a[12:15], a[56:59], a[152:155], a[12:15]\n"
"ds_write_b64 v2, a[218:219] offset:22536\n"
"v_mfma_f32_16x16x32_bf16 a[12:15], a[60:63], a[156:159], a[12:15]\n"
"v_mfma_f32_16x16x32_bf16 a[8:11], a[52:55], a[156:159], a[8:11]\n"
"ds_write_b64 v2, a[220:221] offset:23552\n"
"v_mfma_f32_16x16x32_bf16 a[8:11], a[48:51], a[152:155], a[8:11]\n"
"v_mfma_f32_16x16x32_bf16 v[144:147], a[64:67], a[96:99], v[144:147]\n"
"ds_write_b64 v2, a[222:223] offset:23560\n"
"v_mfma_f32_16x16x32_bf16 v[144:147], a[68:71], a[100:103], v[144:147]\n"
"global_load_dwordx4 a[48:51], v0, s[34:35] offset:0 sc1\n"
"v_mfma_f32_16x16x32_bf16 v[172:175], a[76:79], a[100:103], v[172:175]\n"
"ds_write_b64 v2, a[224:225] offset:24576\n"
"v_mfma_f32_16x16x32_bf16 v[172:175], a[72:75], a[96:99], v[172:175]\n"
"ds_write_b64 v2, a[226:227] offset:24584\n"
"v_mfma_f32_16x16x32_bf16 v[176:179], a[72:75], a[104:107], v[176:179]\n"
"v_mfma_f32_16x16x32_bf16 v[176:179], a[76:79], a[108:111], v[176:179]\n"
"ds_write_b64 v2, a[228:229] offset:25600\n"
"v_mfma_f32_16x16x32_bf16 v[148:151], a[68:71], a[108:111], v[148:151]\n"
"v_mfma_f32_16x16x32_bf16 v[148:151], a[64:67], a[104:107], v[148:151]\n"
"ds_write_b64 v2, a[230:231] offset:25608\n"
"v_mfma_f32_16x16x32_bf16 v[152:155], a[64:67], a[112:115], v[152:155]\n"
"ds_write_b64 v2, a[232:233] offset:26624\n"
"v_mfma_f32_16x16x32_bf16 v[152:155], a[68:71], a[116:119], v[152:155]\n"
"global_load_dwordx4 a[52:55], v0, s[34:35] offset:1024 sc1\n"
"v_mfma_f32_16x16x32_bf16 v[180:183], a[76:79], a[116:119], v[180:183]\n"
"v_mfma_f32_16x16x32_bf16 v[180:183], a[72:75], a[112:115], v[180:183]\n"
"ds_write_b64 v2, a[234:235] offset:26632\n"
"v_mfma_f32_16x16x32_bf16 v[184:187], a[72:75], a[120:123], v[184:187]\n"
"v_mfma_f32_16x16x32_bf16 v[184:187], a[76:79], a[124:127], v[184:187]\n"
"ds_write_b64 v2, a[236:237] offset:27648\n"
"v_mfma_f32_16x16x32_bf16 v[156:159], a[68:71], a[124:127], v[156:159]\n"
"v_mfma_f32_16x16x32_bf16 v[156:159], a[64:67], a[120:123], v[156:159]\n"
"ds_write_b64 v2, a[238:239] offset:27656\n"
"v_mfma_f32_16x16x32_bf16 v[160:163], a[64:67], a[128:131], v[160:163]\n"
"ds_write_b64 v2, a[240:241] offset:28672\n"
"v_mfma_f32_16x16x32_bf16 v[160:163], a[68:71], a[132:135], v[160:163]\n"
"global_load_dwordx4 a[56:59], v0, s[34:35] offset:2048 sc1\n"
"v_mfma_f32_16x16x32_bf16 v[188:191], a[76:79], a[132:135], v[188:191]\n"
"ds_write_b64 v2, a[242:243] offset:28680\n"
"v_mfma_f32_16x16x32_bf16 v[188:191], a[72:75], a[128:131], v[188:191]\n"
"v_mfma_f32_16x16x32_bf16 v[192:195], a[72:75], a[136:139], v[192:195]\n"
"ds_write_b64 v2, a[244:245] offset:29696\n"
"v_mfma_f32_16x16x32_bf16 v[192:195], a[76:79], a[140:143], v[192:195]\n"
"v_mfma_f32_16x16x32_bf16 v[164:167], a[68:71], a[140:143], v[164:167]\n"
"ds_write_b64 v2, a[246:247] offset:29704\n"
"v_mfma_f32_16x16x32_bf16 v[164:167], a[64:67], a[136:139], v[164:167]\n"
"v_mfma_f32_16x16x32_bf16 v[168:171], a[64:67], a[144:147], v[168:171]\n"
"ds_write_b64 v2, a[248:249] offset:30720\n"
"v_mfma_f32_16x16x32_bf16 v[168:171], a[68:71], a[148:151], v[168:171]\n"
"global_load_dwordx4 a[60:63], v0, s[34:35] offset:3072 sc1\n"
"v_mfma_f32_16x16x32_bf16 v[196:199], a[76:79], a[148:151], v[196:199]\n"
"ds_write_b64 v2, a[250:251] offset:30728\n"
"v_mfma_f32_16x16x32_bf16 v[196:199], a[72:75], a[144:147], v[196:199]\n"
"v_mfma_f32_16x16x32_bf16 a[20:23], a[72:75], a[152:155], a[20:23]\n"
"s_add_u32 s34, s34, 8192\n"
"ds_write_b64 v2, a[252:253] offset:31744\n"
"v_mfma_f32_16x16x32_bf16 a[20:23], a[76:79], a[156:159], a[20:23]\n"
"v_mfma_f32_16x16x32_bf16 a[16:19], a[68:71], a[156:159], a[16:19]\n"
"ds_write_b64 v2, a[254:255] offset:31752\n"
"v_mfma_f32_16x16x32_bf16 a[16:19], a[64:67], a[152:155], a[16:19]\n"
"v_mfma_f32_16x16x32_bf16 v[200:203], a[80:83], a[96:99], v[200:203]\n"
"v_lshrrev_b32 v16, 4, v0\n"
"v_mfma_f32_16x16x32_bf16 v[200:203], a[84:87], a[100:103], v[200:203]\n"
"global_load_dwordx4 a[64:67], v0, s[34:35] offset:-4096 sc1\n"
"v_mfma_f32_16x16x32_bf16 v[228:231], a[92:95], a[100:103], v[228:231]\n"
"ds_read_b128 a[100:103], v2 offset:17408\n"
"v_mfma_f32_16x16x32_bf16 v[228:231], a[88:91], a[96:99], v[228:231]\n"
"ds_read_b128 a[96:99], v2 offset:16384\n"
"v_mfma_f32_16x16x32_bf16 v[232:235], a[88:91], a[104:107], v[232:235]\n"
"v_and_b32 v16, 0x30, v16\n"
"v_mfma_f32_16x16x32_bf16 v[232:235], a[92:95], a[108:111], v[232:235]\n"
"buffer_load_dwordx2 v[18:19], v16, s[44:47], s61 offen offset:128\n"
"v_mfma_f32_16x16x32_bf16 v[204:207], a[84:87], a[108:111], v[204:207]\n"
"ds_read_b128 a[108:111], v2 offset:19456\n"
"v_mfma_f32_16x16x32_bf16 v[204:207], a[80:83], a[104:107], v[204:207]\n"
"ds_read_b128 a[104:107], v2 offset:18432\n"
"v_mfma_f32_16x16x32_bf16 v[208:211], a[80:83], a[112:115], v[208:211]\n"
"v_lshrrev_b32 v20, 4, v0\n"
"v_mfma_f32_16x16x32_bf16 v[208:211], a[84:87], a[116:119], v[208:211]\n"
"global_load_dwordx4 a[68:71], v0, s[34:35] offset:-3072 sc1\n"
"v_mfma_f32_16x16x32_bf16 v[236:239], a[92:95], a[116:119], v[236:239]\n"
"ds_read_b128 a[116:119], v2 offset:21504\n"
"v_mfma_f32_16x16x32_bf16 v[236:239], a[88:91], a[112:115], v[236:239]\n"
"ds_read_b128 a[112:115], v2 offset:20480\n"
"v_mfma_f32_16x16x32_bf16 v[240:243], a[88:91], a[120:123], v[240:243]\n"
"v_and_b32 v20, 0x30, v20\n"
"v_mfma_f32_16x16x32_bf16 v[240:243], a[92:95], a[124:127], v[240:243]\n"
"buffer_load_dwordx2 v[22:23], v20, s[44:47], s61 offen offset:136\n"
"v_mfma_f32_16x16x32_bf16 v[212:215], a[84:87], a[124:127], v[212:215]\n"
"ds_read_b128 a[124:127], v2 offset:23552\n"
"v_mfma_f32_16x16x32_bf16 v[212:215], a[80:83], a[120:123], v[212:215]\n"
"ds_read_b128 a[120:123], v2 offset:22528\n"
"v_mfma_f32_16x16x32_bf16 v[216:219], a[80:83], a[128:131], v[216:219]\n"
"v_lshrrev_b32 v24, 4, v0\n"
"v_mfma_f32_16x16x32_bf16 v[216:219], a[84:87], a[132:135], v[216:219]\n"
"global_load_dwordx4 a[72:75], v0, s[34:35] offset:-2048 sc1\n"
"v_mfma_f32_16x16x32_bf16 v[244:247], a[92:95], a[132:135], v[244:247]\n"
"ds_read_b128 a[132:135], v2 offset:25600\n"
"v_and_b32 v24, 0x30, v24\n"
"v_mfma_f32_16x16x32_bf16 v[244:247], a[88:91], a[128:131], v[244:247]\n"
"ds_read_b128 a[128:131], v2 offset:24576\n"
"v_mfma_f32_16x16x32_bf16 v[248:251], a[88:91], a[136:139], v[248:251]\n"
"buffer_load_dwordx2 v[26:27], v24, s[44:47], s61 offen offset:192\n"
"v_mfma_f32_16x16x32_bf16 v[248:251], a[92:95], a[140:143], v[248:251]\n"
"v_lshrrev_b32 v28, 4, v0\n"
"v_mfma_f32_16x16x32_bf16 v[220:223], a[84:87], a[140:143], v[220:223]\n"
"ds_read_b128 a[140:143], v2 offset:27648\n"
"v_mfma_f32_16x16x32_bf16 v[220:223], a[80:83], a[136:139], v[220:223]\n"
"ds_read_b128 a[136:139], v2 offset:26624\n"
"v_mfma_f32_16x16x32_bf16 v[224:227], a[80:83], a[144:147], v[224:227]\n"
"v_and_b32 v28, 0x30, v28\n"
"v_mfma_f32_16x16x32_bf16 v[224:227], a[84:87], a[148:151], v[224:227]\n"
"global_load_dwordx4 a[76:79], v0, s[34:35] offset:-1024 sc1\n"
"v_mfma_f32_16x16x32_bf16 v[252:255], a[92:95], a[148:151], v[252:255]\n"
"ds_read_b128 a[148:151], v2 offset:29696\n"
"v_mfma_f32_16x16x32_bf16 v[252:255], a[88:91], a[144:147], v[252:255]\n"
"ds_read_b128 a[144:147], v2 offset:28672\n"
"v_mfma_f32_16x16x32_bf16 a[28:31], a[88:91], a[152:155], a[28:31]\n"
"buffer_load_dwordx2 v[30:31], v28, s[44:47], s61 offen offset:200\n"
"v_mfma_f32_16x16x32_bf16 a[28:31], a[92:95], a[156:159], a[28:31]\n"
"v_mfma_f32_16x16x32_bf16 a[24:27], a[84:87], a[156:159], a[24:27]\n"
"ds_read_b128 a[156:159], v2 offset:31744\n"
"v_mfma_f32_16x16x32_bf16 a[24:27], a[80:83], a[152:155], a[24:27]\n"
"s_waitcnt vmcnt(12) lgkmcnt(11)\n"
"ds_read_b128 a[152:155], v2 offset:30720\n"
"v_mfma_f32_16x16x32_bf16 v[32:35], a[32:35], a[96:99], v[32:35]\n"
"v_mfma_f32_16x16x32_bf16 v[32:35], a[36:39], a[100:103], v[32:35]\n"
"global_load_dwordx4 a[80:83], v0, s[34:35] offset:0 sc1\n"
"v_mfma_f32_16x16x32_bf16 v[60:63], a[44:47], a[100:103], v[60:63]\n"
"s_barrier\n"
"v_mfma_f32_16x16x32_bf16 v[60:63], a[40:43], a[96:99], v[60:63]\n"
"v_mfma_f32_16x16x32_bf16 v[64:67], a[40:43], a[104:107], v[64:67]\n"
"v_mfma_f32_16x16x32_bf16 v[64:67], a[44:47], a[108:111], v[64:67]\n"
"v_mfma_f32_16x16x32_bf16 v[36:39], a[36:39], a[108:111], v[36:39]\n"
"v_mfma_f32_16x16x32_bf16 v[36:39], a[32:35], a[104:107], v[36:39]\n"
"s_waitcnt lgkmcnt(8)\n"
"v_mfma_f32_16x16x32_bf16 v[40:43], a[32:35], a[112:115], v[40:43]\n"
"v_mfma_f32_16x16x32_bf16 v[40:43], a[36:39], a[116:119], v[40:43]\n"
"global_load_dwordx4 a[84:87], v0, s[34:35] offset:1024 sc1\n"
"v_mfma_f32_16x16x32_bf16 v[68:71], a[44:47], a[116:119], v[68:71]\n"
"v_mfma_f32_16x16x32_bf16 v[68:71], a[40:43], a[112:115], v[68:71]\n"
"v_mfma_f32_16x16x32_bf16 v[72:75], a[40:43], a[120:123], v[72:75]\n"
"v_mfma_f32_16x16x32_bf16 v[72:75], a[44:47], a[124:127], v[72:75]\n"
"s_add_u32 s61, s61, 2048\n"
"v_mfma_f32_16x16x32_bf16 v[44:47], a[36:39], a[124:127], v[44:47]\n"
"v_mfma_f32_16x16x32_bf16 v[44:47], a[32:35], a[120:123], v[44:47]\n"
"s_waitcnt lgkmcnt(4)\n"
"v_mfma_f32_16x16x32_bf16 v[48:51], a[32:35], a[128:131], v[48:51]\n"
"s_waitcnt vmcnt(8)\n"
"v_lshlrev_b32 v16, 16, v18\n"
"v_mfma_f32_16x16x32_bf16 v[48:51], a[36:39], a[132:135], v[48:51]\n"
"global_load_dwordx4 a[88:91], v0, s[34:35] offset:2048 sc1\n"
"v_and_b32 v17, s63, v18\n"
"v_mfma_f32_16x16x32_bf16 v[76:79], a[44:47], a[132:135], v[76:79]\n"
"v_lshlrev_b32 v18, 16, v19\n"
"v_and_b32 v19, s63, v19\n"
"v_mfma_f32_16x16x32_bf16 v[76:79], a[40:43], a[128:131], v[76:79]\n"
"v_add_f32 v144, v144, v16\n"
"v_mfma_f32_16x16x32_bf16 v[80:83], a[40:43], a[136:139], v[80:83]\n"
"v_add_f32 v145, v145, v17\n"
"v_add_f32 v146, v146, v18\n"
"v_mfma_f32_16x16x32_bf16 v[80:83], a[44:47], a[140:143], v[80:83]\n"
"v_add_f32 v147, v147, v19\n"
"v_mfma_f32_16x16x32_bf16 v[52:55], a[36:39], a[140:143], v[52:55]\n"
"v_add_f32 v148, v148, v16\n"
"v_mfma_f32_16x16x32_bf16 v[52:55], a[32:35], a[136:139], v[52:55]\n"
"s_mov_b32 s99, 0\n"
"v_add_f32 v149, v149, v17\n"
"s_waitcnt lgkmcnt(0)\n"
"v_mfma_f32_16x16x32_bf16 v[56:59], a[32:35], a[144:147], v[56:59]\n"
"s_mov_b32 s98, 0\n"
"v_add_f32 v150, v150, v18\n"
"v_add_f32 v151, v151, v19\n"
"v_mfma_f32_16x16x32_bf16 v[56:59], a[36:39], a[148:151], v[56:59]\n"
"global_load_dwordx4 a[92:95], v0, s[34:35] offset:3072 sc1\n"
"s_memtime s[76:77]\n"
"s_load_dword s98, s[96:97], 8448 glc\n"
"v_mfma_f32_16x16x32_bf16 v[84:87], a[44:47], a[148:151], v[84:87]\n"
"v_add_f32 v152, v152, v16\n"
"v_add_f32 v153, v153, v17\n"
"v_mfma_f32_16x16x32_bf16 v[84:87], a[40:43], a[144:147], v[84:87]\n"
"v_add_f32 v154, v154, v18\n"
"v_add_f32 v155, v155, v19\n"
"v_mfma_f32_16x16x32_bf16 a[4:7], a[40:43], a[152:155], a[4:7]\n"
"s_add_u32 s34, s34, 8192\n"
"v_add_f32 v156, v156, v16\n"
"v_mfma_f32_16x16x32_bf16 a[4:7], a[44:47], a[156:159], a[4:7]\n"
"v_add_f32 v157, v157, v17\n"
"v_mfma_f32_16x16x32_bf16 a[0:3], a[36:39], a[156:159], a[0:3]\n"
"v_add_f32 v158, v158, v18\n"
"v_add_f32 v159, v159, v19\n"
"v_mfma_f32_16x16x32_bf16 a[0:3], a[32:35], a[152:155], a[0:3]\n"
"v_add_f32 v160, v160, v16\n"
"v_mfma_f32_16x16x32_bf16 v[88:91], a[48:51], a[96:99], v[88:91]\n"
"v_add_f32 v161, v161, v17\n"
"v_add_f32 v162, v162, v18\n"
"v_mfma_f32_16x16x32_bf16 v[88:91], a[52:55], a[100:103], v[88:91]\n"
"global_load_dwordx4 a[32:35], v0, s[34:35] offset:-4096 sc1\n"
"v_add_f32 v163, v163, v19\n"
"v_mfma_f32_16x16x32_bf16 v[116:119], a[60:63], a[100:103], v[116:119]\n"
"s_mov_b32 s29, s76\n"
"ds_read_b128 a[100:103], v3 offset:17408\n"
"v_add_f32 v164, v164, v16\n"
"v_mfma_f32_16x16x32_bf16 v[116:119], a[56:59], a[96:99], v[116:119]\n"
"ds_read_b128 a[96:99], v3 offset:16384\n"
"v_add_f32 v165, v165, v17\n"
"v_mfma_f32_16x16x32_bf16 v[120:123], a[56:59], a[104:107], v[120:123]\n"
"s_sub_u32 s99, s99, s98\n"
"v_add_f32 v166, v166, v18\n"
"v_add_f32 v167, v167, v19\n"
"v_mfma_f32_16x16x32_bf16 v[120:123], a[60:63], a[108:111], v[120:123]\n"
"s_sub_u32 s99, s99, s98\n"
"v_add_f32 v168, v168, v16\n"
"v_add_f32 v169, v169, v17\n"
"v_mfma_f32_16x16x32_bf16 v[92:95], a[52:55], a[108:111], v[92:95]\n"
"s_sub_u32 s99, s99, s98\n"
"ds_read_b128 a[108:111], v3 offset:19456\n"
"v_add_f32 v170, v170, v18\n"
"v_mfma_f32_16x16x32_bf16 v[92:95], a[48:51], a[104:107], v[92:95]\n"
"s_sub_u32 s99, s99, s98\n"
"ds_read_b128 a[104:107], v3 offset:18432\n"
"v_add_f32 v171, v171, v19\n"
"v_mfma_f32_16x16x32_bf16 v[96:99], a[48:51], a[112:115], v[96:99]\n"
"s_sub_u32 s99, s99, s98\n"
"v_accvgpr_read_b32 v12, a16\n"
"v_accvgpr_read_b32 v13, a17\n"
"v_mfma_f32_16x16x32_bf16 v[96:99], a[52:55], a[116:119], v[96:99]\n"
"global_load_dwordx4 a[36:39], v0, s[34:35] offset:-3072 sc1\n"
"s_sub_u32 s99, s99, s98\n"
"v_mfma_f32_16x16x32_bf16 v[124:127], a[60:63], a[116:119], v[124:127]\n"
"s_sub_u32 s99, s99, s98\n"
"ds_read_b128 a[116:119], v3 offset:21504\n"
"v_accvgpr_read_b32 v14, a18\n"
"v_mfma_f32_16x16x32_bf16 v[124:127], a[56:59], a[112:115], v[124:127]\n"
"s_sub_u32 s99, s99, s98\n"
"ds_read_b128 a[112:115], v3 offset:20480\n"
"v_accvgpr_read_b32 v15, a19\n"
"v_mfma_f32_16x16x32_bf16 v[128:131], a[56:59], a[120:123], v[128:131]\n"
"s_sub_u32 s99, s99, s98\n"
"v_add_f32 v12, v12, v16\n"
"v_add_f32 v13, v13, v17\n"
"v_mfma_f32_16x16x32_bf16 v[128:131], a[60:63], a[124:127], v[128:131]\n"
"s_sub_u32 s99, s99, s98\n"
"v_add_f32 v14, v14, v18\n"
"v_add_f32 v15, v15, v19\n"
"v_mfma_f32_16x16x32_bf16 v[100:103], a[52:55], a[124:127], v[100:103]\n"
"s_sub_u32 s99, s99, s98\n"
"ds_read_b128 a[124:127], v3 offset:23552\n"
"v_accvgpr_write_b32 a16, v12\n"
"v_mfma_f32_16x16x32_bf16 v[100:103], a[48:51], a[120:123], v[100:103]\n"
"s_sub_u32 s99, s99, s98\n"
"ds_read_b128 a[120:123], v3 offset:22528\n"
"v_accvgpr_write_b32 a17, v13\n"
"v_mfma_f32_16x16x32_bf16 v[104:107], a[48:51], a[128:131], v[104:107]\n"
"s_sub_u32 s99, s99, s98\n"
"v_accvgpr_write_b32 a18, v14\n"
"v_accvgpr_write_b32 a19, v15\n"
"v_mfma_f32_16x16x32_bf16 v[104:107], a[52:55], a[132:135], v[104:107]\n"
"s_sub_u32 s99, s99, s98\n"
"s_waitcnt vmcnt(10)\n"
"global_load_dwordx4 a[40:43], v0, s[34:35] offset:-2048 sc1\n"
"v_mfma_f32_16x16x32_bf16 v[132:135], a[60:63], a[132:135], v[132:135]\n"
"s_sub_u32 s99, s99, s98\n"
"ds_read_b128 a[132:135], v3 offset:25600\n"
"v_lshlrev_b32 v20, 16, v22\n"
"v_mfma_f32_16x16x32_bf16 v[132:135], a[56:59], a[128:131], v[132:135]\n"
"s_sub_u32 s99, s99, s98\n"
"ds_read_b128 a[128:131], v3 offset:24576\n"
"v_and_b32 v21, s63, v22\n"
"v_mfma_f32_16x16x32_bf16 v[136:139], a[56:59], a[136:139], v[136:139]\n"
"s_sub_u32 s99, s99, s98\n"
"v_lshlrev_b32 v22, 16, v23\n"
"v_and_b32 v23, s63, v23\n"
"v_mfma_f32_16x16x32_bf16 v[136:139], a[60:63], a[140:143], v[136:139]\n"
"s_sub_u32 s99, s99, s98\n"
"v_add_f32 v172, v172, v20\n"
"v_add_f32 v173, v173, v21\n"
"v_mfma_f32_16x16x32_bf16 v[108:111], a[52:55], a[140:143], v[108:111]\n"
"s_sub_u32 s99, s99, s98\n"
"ds_read_b128 a[140:143], v3 offset:27648\n"
"v_add_f32 v174, v174, v22\n"
"v_mfma_f32_16x16x32_bf16 v[108:111], a[48:51], a[136:139], v[108:111]\n"
"s_sub_u32 s99, s99, s98\n"
"ds_read_b128 a[136:139], v3 offset:26624\n"
"v_add_f32 v175, v175, v23\n"
"v_mfma_f32_16x16x32_bf16 v[112:115], a[48:51], a[144:147], v[112:115]\n"
"s_sub_u32 s99, s99, s98\n"
"v_add_f32 v176, v176, v20\n"
"v_add_f32 v177, v177, v21\n"
"v_mfma_f32_16x16x32_bf16 v[112:115], a[52:55], a[148:151], v[112:115]\n"
"global_load_dwordx4 a[44:47], v0, s[34:35] offset:-1024 sc1\n"
"s_sub_u32 s99, s99, s98\n"
"v_mfma_f32_16x16x32_bf16 v[140:143], a[60:63], a[148:151], v[140:143]\n"
"s_sub_u32 s99, s99, s98\n"
"ds_read_b128 a[148:151], v3 offset:29696\n"
"v_add_f32 v178, v178, v22\n"
"v_mfma_f32_16x16x32_bf16 v[140:143], a[56:59], a[144:147], v[140:143]\n"
"s_sub_u32 s99, s99, s98\n"
"ds_read_b128 a[144:147], v3 offset:28672\n"
"v_add_f32 v179, v179, v23\n"
"v_mfma_f32_16x16x32_bf16 a[12:15], a[56:59], a[152:155], a[12:15]\n"
"s_memtime s[76:77]\n"
"v_add_f32 v180, v180, v20\n"
"v_add_f32 v181, v181, v21\n"
"v_mfma_f32_16x16x32_bf16 a[12:15], a[60:63], a[156:159], a[12:15]\n"
"v_add_f32 v182, v182, v22\n"
"v_add_f32 v183, v183, v23\n"
"v_mfma_f32_16x16x32_bf16 a[8:11], a[52:55], a[156:159], a[8:11]\n"
"ds_read_b128 a[156:159], v3 offset:31744\n"
"v_add_f32 v184, v184, v20\n"
"v_mfma_f32_16x16x32_bf16 a[8:11], a[48:51], a[152:155], a[8:11]\n"
"s_waitcnt vmcnt(9) lgkmcnt(11)\n"
"ds_read_b128 a[152:155], v3 offset:30720\n"
"v_add_f32 v185, v185, v21\n"
"v_mfma_f32_16x16x32_bf16 v[32:35], a[64:67], a[96:99], v[32:35]\n"
"v_add_f32 v186, v186, v22\n"
"v_add_f32 v187, v187, v23\n"
"v_mfma_f32_16x16x32_bf16 v[32:35], a[68:71], a[100:103], v[32:35]\n"
"global_load_dwordx4 a[48:51], v0, s[34:35] offset:0 sc1\n"
"v_add_f32 v188, v188, v20\n"
"v_mfma_f32_16x16x32_bf16 v[60:63], a[76:79], a[100:103], v[60:63]\n"
"v_add_f32 v189, v189, v21\n"
"v_mfma_f32_16x16x32_bf16 v[60:63], a[72:75], a[96:99], v[60:63]\n"
"v_add_f32 v190, v190, v22\n"
"v_add_f32 v191, v191, v23\n"
"v_mfma_f32_16x16x32_bf16 v[64:67], a[72:75], a[104:107], v[64:67]\n"
"v_add_f32 v192, v192, v20\n"
"v_mfma_f32_16x16x32_bf16 v[64:67], a[76:79], a[108:111], v[64:67]\n"
"v_add_f32 v193, v193, v21\n"
"v_mfma_f32_16x16x32_bf16 v[36:39], a[68:71], a[108:111], v[36:39]\n"
"v_add_f32 v194, v194, v22\n"
"v_add_f32 v195, v195, v23\n"
"v_mfma_f32_16x16x32_bf16 v[36:39], a[64:67], a[104:107], v[36:39]\n"
"s_waitcnt lgkmcnt(8)\n"
"v_add_f32 v196, v196, v20\n"
"v_mfma_f32_16x16x32_bf16 v[40:43], a[64:67], a[112:115], v[40:43]\n"
"v_add_f32 v197, v197, v21\n"
"v_mfma_f32_16x16x32_bf16 v[40:43], a[68:71], a[116:119], v[40:43]\n"
"global_load_dwordx4 a[52:55], v0, s[34:35] offset:1024 sc1\n"
"v_add_f32 v198, v198, v22\n"
"v_mfma_f32_16x16x32_bf16 v[68:71], a[76:79], a[116:119], v[68:71]\n"
"v_add_f32 v199, v199, v23\n"
"v_accvgpr_read_b32 v12, a20\n"
"v_mfma_f32_16x16x32_bf16 v[68:71], a[72:75], a[112:115], v[68:71]\n"
"v_accvgpr_read_b32 v13, a21\n"
"v_mfma_f32_16x16x32_bf16 v[72:75], a[72:75], a[120:123], v[72:75]\n"
"v_accvgpr_read_b32 v14, a22\n"
"v_accvgpr_read_b32 v15, a23\n"
"v_mfma_f32_16x16x32_bf16 v[72:75], a[76:79], a[124:127], v[72:75]\n"
"v_add_f32 v12, v12, v20\n"
"v_mfma_f32_16x16x32_bf16 v[44:47], a[68:71], a[124:127], v[44:47]\n"
"v_add_f32 v13, v13, v21\n"
"v_mfma_f32_16x16x32_bf16 v[44:47], a[64:67], a[120:123], v[44:47]\n"
"s_waitcnt lgkmcnt(4)\n"
"v_add_f32 v14, v14, v22\n"
"v_mfma_f32_16x16x32_bf16 v[48:51], a[64:67], a[128:131], v[48:51]\n"
"v_add_f32 v15, v15, v23\n"
"v_accvgpr_write_b32 a20, v12\n"
"v_mfma_f32_16x16x32_bf16 v[48:51], a[68:71], a[132:135], v[48:51]\n"
"global_load_dwordx4 a[56:59], v0, s[34:35] offset:2048 sc1\n"
"v_accvgpr_write_b32 a21, v13\n"
"v_mfma_f32_16x16x32_bf16 v[76:79], a[76:79], a[132:135], v[76:79]\n"
"s_waitcnt lgkmcnt(0)\n"
"v_accvgpr_write_b32 a22, v14\n"
"v_mfma_f32_16x16x32_bf16 v[76:79], a[72:75], a[128:131], v[76:79]\n"
"s_sub_u32 s29, s76, s29\n"
"v_accvgpr_write_b32 a23, v15\n"
"v_lshlrev_b32 v24, 16, v26\n"
"v_mfma_f32_16x16x32_bf16 v[80:83], a[72:75], a[136:139], v[80:83]\n"
"s_cmp_lt_u32 s29, 620\n"
"s_cselect_b32 s99, s99, 0\n"
"v_and_b32 v25, s63, v26\n"
"v_mfma_f32_16x16x32_bf16 v[80:83], a[76:79], a[140:143], v[80:83]\n"
"s_max_u32 s31, s31, s99\n"
"v_lshlrev_b32 v26, 16, v27\n"
"v_mfma_f32_16x16x32_bf16 v[52:55], a[68:71], a[140:143], v[52:55]\n"
"v_and_b32 v27, s63, v27\n"
"v_add_f32 v200, v200, v24\n"
"v_mfma_f32_16x16x32_bf16 v[52:55], a[64:67], a[136:139], v[52:55]\n"
"s_mov_b32 s99, 0\n"
"v_add_f32 v201, v201, v25\n"
"v_mfma_f32_16x16x32_bf16 v[56:59], a[64:67], a[144:147], v[56:59]\n"
"s_mov_b32 s98, 0\n"
"v_add_f32 v202, v202, v26\n"
"v_add_f32 v203, v203, v27\n"
"v_mfma_f32_16x16x32_bf16 v[56:59], a[68:71], a[148:151], v[56:59]\n"
"global_load_dwordx4 a[60:63], v0, s[34:35] offset:3072 sc1\n"
"s_memtime s[76:77]\n"
"s_load_dword s98, s[96:97], 256 glc\n"
"v_mfma_f32_16x16x32_bf16 v[84:87], a[76:79], a[148:151], v[84:87]\n"
"v_add_f32 v204, v204, v24\n"
"v_add_f32 v205, v205, v25\n"
"v_mfma_f32_16x16x32_bf16 v[84:87], a[72:75], a[144:147], v[84:87]\n"
"v_add_f32 v206, v206, v26\n"
"v_mfma_f32_16x16x32_bf16 a[4:7], a[72:75], a[152:155], a[4:7]\n"
"s_add_u32 s34, s34, 8192\n"
"v_add_f32 v207, v207, v27\n"
"v_mfma_f32_16x16x32_bf16 a[4:7], a[76:79], a[156:159], a[4:7]\n"
"v_add_f32 v208, v208, v24\n"
"v_add_f32 v209, v209, v25\n"
"v_mfma_f32_16x16x32_bf16 a[0:3], a[68:71], a[156:159], a[0:3]\n"
"v_add_f32 v210, v210, v26\n"
"v_mfma_f32_16x16x32_bf16 a[0:3], a[64:67], a[152:155], a[0:3]\n"
"s_waitcnt vmcnt(8)\n"
"v_add_f32 v211, v211, v27\n"
"v_mfma_f32_16x16x32_bf16 v[88:91], a[80:83], a[96:99], v[88:91]\n"
"v_add_f32 v212, v212, v24\n"
"v_add_f32 v213, v213, v25\n"
"v_mfma_f32_16x16x32_bf16 v[88:91], a[84:87], a[100:103], v[88:91]\n"
"global_load_dwordx4 a[64:67], v0, s[34:35] offset:-4096 sc1\n"
"v_add_f32 v214, v214, v26\n"
"v_mfma_f32_16x16x32_bf16 v[116:119], a[92:95], a[100:103], v[116:119]\n"
"s_mov_b32 s29, s76\n"
"ds_read_b128 a[100:103], v4 offset:17408\n"
"v_add_f32 v215, v215, v27\n"
"v_mfma_f32_16x16x32_bf16 v[116:119], a[88:91], a[96:99], v[116:119]\n"
"ds_read_b128 a[96:99], v4 offset:16384\n"
"v_add_f32 v216, v216, v24\n"
"v_mfma_f32_16x16x32_bf16 v[120:123], a[88:91], a[104:107], v[120:123]\n"
"s_sub_u32 s99, s99, s98\n"
"v_add_f32 v217, v217, v25\n"
"v_add_f32 v218, v218, v26\n"
"v_mfma_f32_16x16x32_bf16 v[120:123], a[92:95], a[108:111], v[120:123]\n"
"s_sub_u32 s99, s99, s98\n"
"v_add_f32 v219, v219, v27\n"
"v_add_f32 v220, v220, v24\n"
"v_mfma_f32_16x16x32_bf16 v[92:95], a[84:87], a[108:111], v[92:95]\n"
"s_sub_u32 s99, s99, s98\n"
"ds_read_b128 a[108:111], v4 offset:19456\n"
"v_add_f32 v221, v221, v25\n"
"v_mfma_f32_16x16x32_bf16 v[92:95], a[80:83], a[104:107], v[92:95]\n"
"s_sub_u32 s99, s99, s98\n"
"ds_read_b128 a[104:107], v4 offset:18432\n"
"v_add_f32 v222, v222, v26\n"
"v_mfma_f32_16x16x32_bf16 v[96:99], a[80:83], a[112:115], v[96:99]\n"
"s_sub_u32 s99, s99, s98\n"
"v_add_f32 v223, v223, v27\n"
"v_add_f32 v224, v224, v24\n"
"v_mfma_f32_16x16x32_bf16 v[96:99], a[84:87], a[116:119], v[96:99]\n"
"global_load_dwordx4 a[68:71], v0, s[34:35] offset:-3072 sc1\n"
"s_sub_u32 s99, s99, s98\n"
"v_mfma_f32_16x16x32_bf16 v[124:127], a[92:95], a[116:119], v[124:127]\n"
"s_sub_u32 s99, s99, s98\n"
"ds_read_b128 a[116:119], v4 offset:21504\n"
"v_add_f32 v225, v225, v25\n"
"v_mfma_f32_16x16x32_bf16 v[124:127], a[88:91], a[112:115], v[124:127]\n"
"s_sub_u32 s99, s99, s98\n"
"ds_read_b128 a[112:115], v4 offset:20480\n"
"v_add_f32 v226, v226, v26\n"
"v_mfma_f32_16x16x32_bf16 v[128:131], a[88:91], a[120:123], v[128:131]\n"
"s_sub_u32 s99, s99, s98\n"
"v_add_f32 v227, v227, v27\n"
"v_accvgpr_read_b32 v12, a24\n"
"v_mfma_f32_16x16x32_bf16 v[128:131], a[92:95], a[124:127], v[128:131]\n"
"s_sub_u32 s99, s99, s98\n"
"v_accvgpr_read_b32 v13, a25\n"
"v_accvgpr_read_b32 v14, a26\n"
"v_mfma_f32_16x16x32_bf16 v[100:103], a[84:87], a[124:127], v[100:103]\n"
"s_sub_u32 s99, s99, s98\n"
"ds_read_b128 a[124:127], v4 offset:23552\n"
"v_accvgpr_read_b32 v15, a27\n"
"v_mfma_f32_16x16x32_bf16 v[100:103], a[80:83], a[120:123], v[100:103]\n"
"s_sub_u32 s99, s99, s98\n"
"ds_read_b128 a[120:123], v4 offset:22528\n"
"v_add_f32 v12, v12, v24\n"
"v_mfma_f32_16x16x32_bf16 v[104:107], a[80:83], a[128:131], v[104:107]\n"
"s_sub_u32 s99, s99, s98\n"
"v_add_f32 v13, v13, v25\n"
"v_add_f32 v14, v14, v26\n"
"v_mfma_f32_16x16x32_bf16 v[104:107], a[84:87], a[132:135], v[104:107]\n"
"global_load_dwordx4 a[72:75], v0, s[34:35] offset:-2048 sc1\n"
"s_sub_u32 s99, s99, s98\n"
"v_mfma_f32_16x16x32_bf16 v[132:135], a[92:95], a[132:135], v[132:135]\n"
"s_sub_u32 s99, s99, s98\n"
"ds_read_b128 a[132:135], v4 offset:25600\n"
"v_add_f32 v15, v15, v27\n"
"v_mfma_f32_16x16x32_bf16 v[132:135], a[88:91], a[128:131], v[132:135]\n"
"s_sub_u32 s99, s99, s98\n"
"ds_read_b128 a[128:131], v4 offset:24576\n"
"v_accvgpr_write_b32 a24, v12\n"
"v_mfma_f32_16x16x32_bf16 v[136:139], a[88:91], a[136:139], v[136:139]\n"
"s_sub_u32 s99, s99, s98\n"
"v_accvgpr_write_b32 a25, v13\n"
"v_accvgpr_write_b32 a26, v14\n"
"v_mfma_f32_16x16x32_bf16 v[136:139], a[92:95], a[140:143], v[136:139]\n"
"s_sub_u32 s99, s99, s98\n"
"v_accvgpr_write_b32 a27, v15\n"
"v_lshlrev_b32 v28, 16, v30\n"
"v_mfma_f32_16x16x32_bf16 v[108:111], a[84:87], a[140:143], v[108:111]\n"
"s_sub_u32 s99, s99, s98\n"
"ds_read_b128 a[140:143], v4 offset:27648\n"
"v_and_b32 v29, s63, v30\n"
"v_mfma_f32_16x16x32_bf16 v[108:111], a[80:83], a[136:139], v[108:111]\n"
"s_sub_u32 s99, s99, s98\n"
"ds_read_b128 a[136:139], v4 offset:26624\n"
"v_lshlrev_b32 v30, 16, v31\n"
"v_mfma_f32_16x16x32_bf16 v[112:115], a[80:83], a[144:147], v[112:115]\n"
"s_sub_u32 s99, s99, s98\n"
"v_and_b32 v31, s63, v31\n"
"v_add_f32 v228, v228, v28\n"
"v_mfma_f32_16x16x32_bf16 v[112:115], a[84:87], a[148:151], v[112:115]\n"
"global_load_dwordx4 a[76:79], v0, s[34:35] offset:-1024 sc1\n"
"s_sub_u32 s99, s99, s98\n"
"v_mfma_f32_16x16x32_bf16 v[140:143], a[92:95], a[148:151], v[140:143]\n"
"s_sub_u32 s99, s99, s98\n"
"ds_read_b128 a[148:151], v4 offset:29696\n"
"v_add_f32 v229, v229, v29\n"
"v_mfma_f32_16x16x32_bf16 v[140:143], a[88:91], a[144:147], v[140:143]\n"
"s_sub_u32 s99, s99, s98\n"
"ds_read_b128 a[144:147], v4 offset:28672\n"
"v_add_f32 v230, v230, v30\n"
"v_mfma_f32_16x16x32_bf16 a[12:15], a[88:91], a[152:155], a[12:15]\n"
"s_memtime s[76:77]\n"
"v_add_f32 v231, v231, v31\n"
"v_add_f32 v232, v232, v28\n"
"v_mfma_f32_16x16x32_bf16 a[12:15], a[92:95], a[156:159], a[12:15]\n"
"v_add_f32 v233, v233, v29\n"
"v_add_f32 v234, v234, v30\n"
"v_mfma_f32_16x16x32_bf16 a[8:11], a[84:87], a[156:159], a[8:11]\n"
"ds_read_b128 a[156:159], v4 offset:31744\n"
"v_add_f32 v235, v235, v31\n"
"v_mfma_f32_16x16x32_bf16 a[8:11], a[80:83], a[152:155], a[8:11]\n"
"s_waitcnt vmcnt(8) lgkmcnt(11)\n"
"ds_read_b128 a[152:155], v4 offset:30720\n"
"v_add_f32 v236, v236, v28\n"
"v_mfma_f32_16x16x32_bf16 v[32:35], a[32:35], a[96:99], v[32:35]\n"
"v_add_f32 v237, v237, v29\n"
"v_add_f32 v238, v238, v30\n"
"v_mfma_f32_16x16x32_bf16 v[32:35], a[36:39], a[100:103], v[32:35]\n"
"global_load_dwordx4 a[80:83], v0, s[34:35] offset:0 sc1\n"
"v_add_f32 v239, v239, v31\n"
"v_mfma_f32_16x16x32_bf16 v[60:63], a[44:47], a[100:103], v[60:63]\n"
"v_add_f32 v240, v240, v28\n"
"v_add_f32 v241, v241, v29\n"
"v_mfma_f32_16x16x32_bf16 v[60:63], a[40:43], a[96:99], v[60:63]\n"
"v_add_f32 v242, v242, v30\n"
"v_mfma_f32_16x16x32_bf16 v[64:67], a[40:43], a[104:107], v[64:67]\n"
"v_add_f32 v243, v243, v31\n"
"v_add_f32 v244, v244, v28\n"
"v_mfma_f32_16x16x32_bf16 v[64:67], a[44:47], a[108:111], v[64:67]\n"
"v_add_f32 v245, v245, v29\n"
"v_mfma_f32_16x16x32_bf16 v[36:39], a[36:39], a[108:111], v[36:39]\n"
"v_add_f32 v246, v246, v30\n"
"v_mfma_f32_16x16x32_bf16 v[36:39], a[32:35], a[104:107], v[36:39]\n"
"L_st3865:\n"
"s_cbranch_execz L_retA\n"
"s_waitcnt lgkmcnt(8)\n"
"v_add_f32 v247, v247, v31\n"
"v_mfma_f32_16x16x32_bf16 v[40:43], a[32:35], a[112:115], v[40:43]\n"
"v_add_f32 v248, v248, v28\n"
"v_add_f32 v249, v249, v29\n"
"v_mfma_f32_16x16x32_bf16 v[40:43], a[36:39], a[116:119], v[40:43]\n"
"global_load_dwordx4 a[84:87], v0, s[34:35] offset:1024 sc1\n"
"s_add_u32 s48, s56, %12\n"
"s_addc_u32 s49, s57, 0\n"
"v_mfma_f32_16x16x32_bf16 v[68:71], a[44:47], a[116:119], v[68:71]\n"
"v_add_f32 v250, v250, v30\n"
"v_add_f32 v251, v251, v31\n"
"v_mfma_f32_16x16x32_bf16 v[68:71], a[40:43], a[112:115], v[68:71]\n"
"s_mov_b32 s50, 32768\n"
"v_add_f32 v252, v252, v28\n"
"v_add_f32 v253, v253, v29\n"
"v_mfma_f32_16x16x32_bf16 v[72:75], a[40:43], a[120:123], v[72:75]\n"
"v_add_f32 v254, v254, v30\n"
"v_mfma_f32_16x16x32_bf16 v[72:75], a[44:47], a[124:127], v[72:75]\n"
"s_mov_b32 s65, s78\n"
"v_add_f32 v255, v255, v31\n"
"v_mfma_f32_16x16x32_bf16 v[44:47], a[36:39], a[124:127], v[44:47]\n"
"v_accvgpr_read_b32 v12, a28\n"
"v_accvgpr_read_b32 v13, a29\n"
"v_mfma_f32_16x16x32_bf16 v[44:47], a[32:35], a[120:123], v[44:47]\n"
"s_mov_b64 s[40:41], s[52:53]\n"
"v_accvgpr_read_b32 v14, a30\n"
"s_waitcnt lgkmcnt(4)\n"
"v_mfma_f32_16x16x32_bf16 v[48:51], a[32:35], a[128:131], v[48:51]\n"
"s_mov_b32 s27, %21\n"
"v_accvgpr_read_b32 v15, a31\n"
"v_add_f32 v12, v12, v28\n"
"v_mfma_f32_16x16x32_bf16 v[48:51], a[36:39], a[132:135], v[48:51]\n"
"global_load_dwordx4 a[88:91], v0, s[34:35] offset:2048 sc1\n"
"s_mov_b32 s28, %23\n"
"v_mfma_f32_16x16x32_bf16 v[76:79], a[44:47], a[132:135], v[76:79]\n"
"s_waitcnt lgkmcnt(0)\n"
"v_add_f32 v13, v13, v29\n"
"v_add_f32 v14, v14, v30\n"
"v_mfma_f32_16x16x32_bf16 v[76:79], a[40:43], a[128:131], v[76:79]\n"
"s_sub_u32 s29, s76, s29\n"
"v_add_f32 v15, v15, v31\n"
"v_mfma_f32_16x16x32_bf16 v[80:83], a[40:43], a[136:139], v[80:83]\n"
"s_cmp_lt_u32 s29, 620\n"
"s_cselect_b32 s99, s99, 0\n"
"v_accvgpr_write_b32 a28, v12\n"
"v_mfma_f32_16x16x32_bf16 v[80:83], a[44:47], a[140:143], v[80:83]\n"
"s_max_u32 s30, s30, s99\n"
"v_accvgpr_write_b32 a29, v13\n"
"v_accvgpr_write_b32 a30, v14\n"
"v_mfma_f32_16x16x32_bf16 v[52:55], a[36:39], a[140:143], v[52:55]\n"
"s_cmp_eq_u32 s30, 0\n"
"s_cselect_b32 s30, 10, s30\n"
"v_accvgpr_write_b32 a31, v15\n"
"v_mfma_f32_16x16x32_bf16 v[52:55], a[32:35], a[136:139], v[52:55]\n"
"s_cmp_eq_u32 s31, 0\n"
"s_cselect_b32 s31, 10, s31\n"
"v_mfma_f32_16x16x32_bf16 v[56:59], a[32:35], a[144:147], v[56:59]\n"
"s_sub_u32 s69, s31, s30\n"
"v_mfma_f32_16x16x32_bf16 v[56:59], a[36:39], a[148:151], v[56:59]\n"
"global_load_dwordx4 a[92:95], v0, s[34:35] offset:3072 sc1\n"
"v_mov_b32 v20, s69\n"
"v_mfma_f32_16x16x32_bf16 v[84:87], a[44:47], a[148:151], v[84:87]\n"
"ds_write_b32 v2, v20 offset:8192\n"
"v_mfma_f32_16x16x32_bf16 v[84:87], a[40:43], a[144:147], v[84:87]\n"
"v_mfma_f32_16x16x32_bf16 a[4:7], a[40:43], a[152:155], a[4:7]\n"
"s_add_u32 s34, s34, 8192\n"
"v_mfma_f32_16x16x32_bf16 a[4:7], a[44:47], a[156:159], a[4:7]\n"
"v_mfma_f32_16x16x32_bf16 a[0:3], a[36:39], a[156:159], a[0:3]\n"
"v_mfma_f32_16x16x32_bf16 a[0:3], a[32:35], a[152:155], a[0:3]\n"
"s_waitcnt lgkmcnt(0)\n"
"s_barrier\n"
"s_waitcnt vmcnt(8)\n"
"v_mfma_f32_16x16x32_bf16 v[88:91], a[48:51], a[96:99], v[88:91]\n"
"ds_read_b32 v21, v3 offset:8192\n"
"v_mfma_f32_16x16x32_bf16 v[88:91], a[52:55], a[100:103], v[88:91]\n"
"global_load_dwordx4 a[32:35], v0, s[34:35] offset:-4096 sc1\n"
"v_mfma_f32_16x16x32_bf16 v[116:119], a[60:63], a[100:103], v[116:119]\n"
"ds_read_b128 a[100:103], v5 offset:17408\n"
"v_mfma_f32_16x16x32_bf16 v[116:119], a[56:59], a[96:99], v[116:119]\n"
"ds_read_b128 a[96:99], v5 offset:16384\n"
"v_mfma_f32_16x16x32_bf16 v[120:123], a[56:59], a[104:107], v[120:123]\n"
"ds_read_b32 v22, v4 offset:8192\n"
"v_mfma_f32_16x16x32_bf16 v[120:123], a[60:63], a[108:111], v[120:123]\n"
"ds_read_b32 v23, v5 offset:8192\n"
"v_mfma_f32_16x16x32_bf16 v[92:95], a[52:55], a[108:111], v[92:95]\n"
"ds_read_b128 a[108:111], v5 offset:19456\n"
"v_mfma_f32_16x16x32_bf16 v[92:95], a[48:51], a[104:107], v[92:95]\n"
"ds_read_b128 a[104:107], v5 offset:18432\n"
"v_mfma_f32_16x16x32_bf16 v[96:99], a[48:51], a[112:115], v[96:99]\n"
"v_mfma_f32_16x16x32_bf16 v[96:99], a[52:55], a[116:119], v[96:99]\n"
"global_load_dwordx4 a[36:39], v0, s[34:35] offset:-3072 sc1\n"
"v_mfma_f32_16x16x32_bf16 v[124:127], a[60:63], a[116:119], v[124:127]\n"
"s_waitcnt lgkmcnt(2)\n"
"ds_read_b128 a[116:119], v5 offset:21504\n"
"v_add_u32 v20, v20, v21\n"
"v_mfma_f32_16x16x32_bf16 v[124:127], a[56:59], a[112:115], v[124:127]\n"
"ds_read_b128 a[112:115], v5 offset:20480\n"
"v_mfma_f32_16x16x32_bf16 v[128:131], a[56:59], a[120:123], v[128:131]\n"
"v_add3_u32 v20, v20, v22, v23\n"
"v_mfma_f32_16x16x32_bf16 v[128:131], a[60:63], a[124:127], v[128:131]\n"
"v_mfma_f32_16x16x32_bf16 v[100:103], a[52:55], a[124:127], v[100:103]\n"
"ds_read_b128 a[124:127], v5 offset:23552\n"
"v_readfirstlane_b32 s69, v20\n"
"v_mfma_f32_16x16x32_bf16 v[100:103], a[48:51], a[120:123], v[100:103]\n"
"ds_read_b128 a[120:123], v5 offset:22528\n"
"v_mfma_f32_16x16x32_bf16 v[104:107], a[48:51], a[128:131], v[104:107]\n"
"v_mfma_f32_16x16x32_bf16 v[104:107], a[52:55], a[132:135], v[104:107]\n"
"s_cmp_le_i32 s69, -2\n"
"global_load_dwordx4 a[40:43], v0, s[34:35] offset:-2048 sc1\n"
"s_cselect_b32 s66, 2, 1\n"
"v_mfma_f32_16x16x32_bf16 v[132:135], a[60:63], a[132:135], v[132:135]\n"
"ds_read_b128 a[132:135], v5 offset:25600\n"
"v_mfma_f32_16x16x32_bf16 v[132:135], a[56:59], a[128:131], v[132:135]\n"
"s_cmp_ge_i32 s69, 2\n"
"ds_read_b128 a[128:131], v5 offset:24576\n"
"s_cselect_b32 s66, 3, s66\n"
"v_mfma_f32_16x16x32_bf16 v[136:139], a[56:59], a[136:139], v[136:139]\n"
"ds_read_b128 v[16:19], v7 offset:32768\n"
"v_mfma_f32_16x16x32_bf16 v[136:139], a[60:63], a[140:143], v[136:139]\n"
"s_lshl_b32 s73, s66, 13\n"
"s_xor_b32 s60, s79, s66\n"
"v_mfma_f32_16x16x32_bf16 v[108:111], a[52:55], a[140:143], v[108:111]\n"
"ds_read_b128 a[140:143], v5 offset:27648\n"
"v_mfma_f32_16x16x32_bf16 v[108:111], a[48:51], a[136:139], v[108:111]\n"
"s_cmp_eq_u32 s66, 3\n"
"ds_read_b128 a[136:139], v5 offset:26624\n"
"s_cselect_b32 s72, 8192, 0\n"
"v_mfma_f32_16x16x32_bf16 v[112:115], a[48:51], a[144:147], v[112:115]\n"
"s_abs_i32 s30, s69\n"
"v_mfma_f32_16x16x32_bf16 v[112:115], a[52:55], a[148:151], v[112:115]\n"
"global_load_dwordx4 a[44:47], v0, s[34:35] offset:-1024 sc1\n"
"s_add_u32 s52, s56, %11\n"
"s_addc_u32 s53, s57, 0\n"
"v_mfma_f32_16x16x32_bf16 v[140:143], a[60:63], a[148:151], v[140:143]\n"
"s_cmp_lt_u32 s30, 26\n"
"s_cselect_b32 s30, 2, 0\n"
"ds_read_b128 a[148:151], v5 offset:29696\n"
"v_mfma_f32_16x16x32_bf16 v[140:143], a[56:59], a[144:147], v[140:143]\n"
"s_add_u32 s52, s52, 4096\n"
"ds_read_b128 a[144:147], v5 offset:28672\n"
"s_addc_u32 s53, s53, 0\n"
"v_mfma_f32_16x16x32_bf16 a[12:15], a[56:59], a[152:155], a[12:15]\n"
"v_mfma_f32_16x16x32_bf16 a[12:15], a[60:63], a[156:159], a[12:15]\n"
"s_add_u32 s54, s52, s73\n"
"s_addc_u32 s55, s53, 0\n"
"v_mfma_f32_16x16x32_bf16 a[8:11], a[52:55], a[156:159], a[8:11]\n"
"ds_read_b128 a[156:159], v5 offset:31744\n"
"v_mfma_f32_16x16x32_bf16 a[8:11], a[48:51], a[152:155], a[8:11]\n"
"s_add_u32 s52, s52, s72\n"
"ds_read_b128 a[152:155], v5 offset:30720\n"
"s_addc_u32 s53, s53, 0\n"
"L_P6:\n"
"s_waitcnt vmcnt(8) lgkmcnt(13)\n"
"v_mfma_f32_16x16x32_bf16 v[32:35], a[64:67], a[96:99], v[32:35]\n"
"v_mfma_f32_16x16x32_bf16 v[32:35], a[68:71], a[100:103], v[32:35]\n"
"global_load_dwordx4 a[48:51], v0, s[34:35] offset:0 sc1\n"
"v_mfma_f32_16x16x32_bf16 v[60:63], a[76:79], a[100:103], v[60:63]\n"
"v_mfma_f32_16x16x32_bf16 v[60:63], a[72:75], a[96:99], v[60:63]\n"
"v_mfma_f32_16x16x32_bf16 v[64:67], a[72:75], a[104:107], v[64:67]\n"
"v_mfma_f32_16x16x32_bf16 v[64:67], a[76:79], a[108:111], v[64:67]\n"
"v_mfma_f32_16x16x32_bf16 v[36:39], a[68:71], a[108:111], v[36:39]\n"
"s_waitcnt lgkmcnt(6)\n"
"v_lshlrev_b32 v12, 16, v16\n"
"v_mfma_f32_16x16x32_bf16 v[36:39], a[64:67], a[104:107], v[36:39]\n"
"v_and_b32 v13, s63, v16\n"
"v_lshlrev_b32 v14, 16, v17\n"
"v_mfma_f32_16x16x32_bf16 v[40:43], a[64:67], a[112:115], v[40:43]\n"
"v_and_b32 v15, s63, v17\n"
"v_mfma_f32_16x16x32_bf16 v[40:43], a[68:71], a[116:119], v[40:43]\n"
"global_load_dwordx4 a[52:55], v0, s[34:35] offset:1024 sc1\n"
"ds_read_b128 v[20:23], v7 offset:34816\n"
"v_mfma_f32_16x16x32_bf16 v[68:71], a[76:79], a[116:119], v[68:71]\n"
"v_mul_f32 v12, v12, v32\n"
"v_mul_f32 v13, v13, v33\n"
"v_mfma_f32_16x16x32_bf16 v[68:71], a[72:75], a[112:115], v[68:71]\n"
"v_mul_f32 v14, v14, v34\n"
"v_mfma_f32_16x16x32_bf16 v[72:75], a[72:75], a[120:123], v[72:75]\n"
"v_mul_f32 v15, v15, v35\n"
"v_mfma_f32_16x16x32_bf16 v[72:75], a[76:79], a[124:127], v[72:75]\n"
"v_cvt_pk_bf16_f32 v24, v12, v13\n"
"v_cvt_pk_bf16_f32 v25, v14, v15\n"
"v_mfma_f32_16x16x32_bf16 v[44:47], a[68:71], a[124:127], v[44:47]\n"
"ds_write_b64 v2, v[24:25] offset:0\n"
"v_mfma_f32_16x16x32_bf16 v[44:47], a[64:67], a[120:123], v[44:47]\n"
"s_waitcnt lgkmcnt(6)\n"
"v_lshlrev_b32 v12, 16, v18\n"
"v_mfma_f32_16x16x32_bf16 v[48:51], a[64:67], a[128:131], v[48:51]\n"
"v_and_b32 v13, s63, v18\n"
"v_lshlrev_b32 v14, 16, v19\n"
"v_mfma_f32_16x16x32_bf16 v[48:51], a[68:71], a[132:135], v[48:51]\n"
"global_load_dwordx4 a[56:59], v0, s[34:35] offset:2048 sc1\n"
"v_and_b32 v15, s63, v19\n"
"v_mfma_f32_16x16x32_bf16 v[76:79], a[76:79], a[132:135], v[76:79]\n"
"v_mul_f32 v12, v12, v60\n"
"v_mfma_f32_16x16x32_bf16 v[76:79], a[72:75], a[128:131], v[76:79]\n"
"v_mul_f32 v13, v13, v61\n"
"v_mul_f32 v14, v14, v62\n"
"v_mfma_f32_16x16x32_bf16 v[80:83], a[72:75], a[136:139], v[80:83]\n"
"v_mul_f32 v15, v15, v63\n"
"v_mfma_f32_16x16x32_bf16 v[80:83], a[76:79], a[140:143], v[80:83]\n"
"v_cvt_pk_bf16_f32 v26, v12, v13\n"
"v_cvt_pk_bf16_f32 v27, v14, v15\n"
"v_mfma_f32_16x16x32_bf16 v[52:55], a[68:71], a[140:143], v[52:55]\n"
"ds_write_b64 v2, v[26:27] offset:8\n"
"v_mfma_f32_16x16x32_bf16 v[52:55], a[64:67], a[136:139], v[52:55]\n"
"s_waitcnt lgkmcnt(2)\n"
"v_lshlrev_b32 v12, 16, v20\n"
"v_mfma_f32_16x16x32_bf16 v[56:59], a[64:67], a[144:147], v[56:59]\n"
"v_and_b32 v13, s63, v20\n"
"v_mfma_f32_16x16x32_bf16 v[56:59], a[68:71], a[148:151], v[56:59]\n"
"global_load_dwordx4 a[60:63], v0, s[34:35] offset:3072 sc1\n"
"v_lshlrev_b32 v14, 16, v21\n"
"v_mfma_f32_16x16x32_bf16 v[84:87], a[76:79], a[148:151], v[84:87]\n"
"v_and_b32 v15, s63, v21\n"
"v_xor_b32 v16, 64, v7\n"
"v_mfma_f32_16x16x32_bf16 v[84:87], a[72:75], a[144:147], v[84:87]\n"
"ds_read_b128 v[16:19], v16 offset:32768\n"
"v_mfma_f32_16x16x32_bf16 a[4:7], a[72:75], a[152:155], a[4:7]\n"
"s_add_u32 s34, s34, 8192\n"
"v_mul_f32 v12, v12, v36\n"
"v_mul_f32 v13, v13, v37\n"
"v_mfma_f32_16x16x32_bf16 a[4:7], a[76:79], a[156:159], a[4:7]\n"
"v_mul_f32 v14, v14, v38\n"
"v_mfma_f32_16x16x32_bf16 a[0:3], a[68:71], a[156:159], a[0:3]\n"
"v_mul_f32 v15, v15, v39\n"
"v_mfma_f32_16x16x32_bf16 a[0:3], a[64:67], a[152:155], a[0:3]\n"
"s_waitcnt vmcnt(8)\n"
"v_cvt_pk_bf16_f32 v28, v12, v13\n"
"v_cvt_pk_bf16_f32 v29, v14, v15\n"
"v_mfma_f32_16x16x32_bf16 v[88:91], a[80:83], a[96:99], v[88:91]\n"
"v_lshlrev_b32 v12, 16, v22\n"
"v_mfma_f32_16x16x32_bf16 v[88:91], a[84:87], a[100:103], v[88:91]\n"
"global_load_dwordx4 a[64:67], v0, s[34:35] offset:-4096 sc1\n"
"v_and_b32 v13, s63, v22\n"
"v_mfma_f32_16x16x32_bf16 v[116:119], a[92:95], a[100:103], v[116:119]\n"
"ds_read_b128 a[100:103], v2 offset:17408\n"
"v_lshlrev_b32 v14, 16, v23\n"
"v_mfma_f32_16x16x32_bf16 v[116:119], a[88:91], a[96:99], v[116:119]\n"
"ds_read_b128 a[96:99], v2 offset:16384\n"
"v_and_b32 v15, s63, v23\n"
"v_mfma_f32_16x16x32_bf16 v[120:123], a[88:91], a[104:107], v[120:123]\n"
"v_mul_f32 v12, v12, v64\n"
"v_mul_f32 v13, v13, v65\n"
"v_mfma_f32_16x16x32_bf16 v[120:123], a[92:95], a[108:111], v[120:123]\n"
"v_mul_f32 v14, v14, v66\n"
"v_mul_f32 v15, v15, v67\n"
"v_mfma_f32_16x16x32_bf16 v[92:95], a[84:87], a[108:111], v[92:95]\n"
"ds_read_b128 a[108:111], v2 offset:19456\n"
"v_cvt_pk_bf16_f32 v30, v12, v13\n"
"v_mfma_f32_16x16x32_bf16 v[92:95], a[80:83], a[104:107], v[92:95]\n"
"ds_read_b128 a[104:107], v2 offset:18432\n"
"v_cvt_pk_bf16_f32 v31, v14, v15\n"
"v_mfma_f32_16x16x32_bf16 v[96:99], a[80:83], a[112:115], v[96:99]\n"
"global_store_dwordx4 v0, v[28:31], s[52:53] offset:-2048 sc1\n"
"ds_write_b128 v2, v[28:31] offset:2048\n"
"v_mfma_f32_16x16x32_bf16 v[96:99], a[84:87], a[116:119], v[96:99]\n"
"s_waitcnt lgkmcnt(5)\n"
"global_load_dwordx4 a[68:71], v0, s[34:35] offset:-3072 sc1\n"
"v_lshlrev_b32 v12, 16, v16\n"
"v_mfma_f32_16x16x32_bf16 v[124:127], a[92:95], a[116:119], v[124:127]\n"
"ds_read_b128 a[116:119], v2 offset:21504\n"
"v_and_b32 v13, s63, v16\n"
"v_mfma_f32_16x16x32_bf16 v[124:127], a[88:91], a[112:115], v[124:127]\n"
"ds_read_b128 a[112:115], v2 offset:20480\n"
"v_lshlrev_b32 v14, 16, v17\n"
"v_mfma_f32_16x16x32_bf16 v[128:131], a[88:91], a[120:123], v[128:131]\n"
"v_and_b32 v15, s63, v17\n"
"v_xor_b32 v20, 64, v7\n"
"v_mfma_f32_16x16x32_bf16 v[128:131], a[92:95], a[124:127], v[128:131]\n"
"ds_read_b128 v[20:23], v20 offset:34816\n"
"v_mul_f32 v12, v12, v88\n"
"v_mfma_f32_16x16x32_bf16 v[100:103], a[84:87], a[124:127], v[100:103]\n"
"ds_read_b128 a[124:127], v2 offset:23552\n"
"v_mul_f32 v13, v13, v89\n"
"v_mfma_f32_16x16x32_bf16 v[100:103], a[80:83], a[120:123], v[100:103]\n"
"ds_read_b128 a[120:123], v2 offset:22528\n"
"v_mul_f32 v14, v14, v90\n"
"v_mfma_f32_16x16x32_bf16 v[104:107], a[80:83], a[128:131], v[104:107]\n"
"v_mul_f32 v15, v15, v91\n"
"v_cvt_pk_bf16_f32 v24, v12, v13\n"
"v_mfma_f32_16x16x32_bf16 v[104:107], a[84:87], a[132:135], v[104:107]\n"
"global_load_dwordx4 a[72:75], v0, s[34:35] offset:-2048 sc1\n"
"v_cvt_pk_bf16_f32 v25, v14, v15\n"
"v_mfma_f32_16x16x32_bf16 v[132:135], a[92:95], a[132:135], v[132:135]\n"
"ds_read_b128 a[132:135], v2 offset:25600\n"
"v_lshlrev_b32 v12, 16, v18\n"
"v_mfma_f32_16x16x32_bf16 v[132:135], a[88:91], a[128:131], v[132:135]\n"
"ds_read_b128 a[128:131], v2 offset:24576\n"
"v_and_b32 v13, s63, v18\n"
"v_mfma_f32_16x16x32_bf16 v[136:139], a[88:91], a[136:139], v[136:139]\n"
"v_lshlrev_b32 v14, 16, v19\n"
"v_and_b32 v15, s63, v19\n"
"v_mfma_f32_16x16x32_bf16 v[136:139], a[92:95], a[140:143], v[136:139]\n"
"v_mul_f32 v12, v12, v116\n"
"v_mfma_f32_16x16x32_bf16 v[108:111], a[84:87], a[140:143], v[108:111]\n"
"ds_read_b128 a[140:143], v2 offset:27648\n"
"v_mul_f32 v13, v13, v117\n"
"v_mfma_f32_16x16x32_bf16 v[108:111], a[80:83], a[136:139], v[108:111]\n"
"ds_read_b128 a[136:139], v2 offset:26624\n"
"v_mul_f32 v14, v14, v118\n"
"v_mfma_f32_16x16x32_bf16 v[112:115], a[80:83], a[144:147], v[112:115]\n"
"v_mul_f32 v15, v15, v119\n"
"v_cvt_pk_bf16_f32 v26, v12, v13\n"
"v_mfma_f32_16x16x32_bf16 v[112:115], a[84:87], a[148:151], v[112:115]\n"
"global_load_dwordx4 a[76:79], v0, s[34:35] offset:-1024 sc1\n"
"v_cvt_pk_bf16_f32 v27, v14, v15\n"
"v_mfma_f32_16x16x32_bf16 v[140:143], a[92:95], a[148:151], v[140:143]\n"
"ds_read_b128 a[148:151], v2 offset:29696\n"
"v_mfma_f32_16x16x32_bf16 v[140:143], a[88:91], a[144:147], v[140:143]\n"
"ds_read_b128 a[144:147], v2 offset:28672\n"
"v_mfma_f32_16x16x32_bf16 a[12:15], a[88:91], a[152:155], a[12:15]\n"
"global_store_dwordx4 v0, v[24:27], s[52:53] offset:-3072 sc1\n"
"ds_write_b128 v2, v[24:27] offset:1024\n"
"v_mfma_f32_16x16x32_bf16 a[12:15], a[92:95], a[156:159], a[12:15]\n"
"s_waitcnt lgkmcnt(9)\n"
"v_lshlrev_b32 v12, 16, v20\n"
"v_and_b32 v13, s63, v20\n"
"v_mfma_f32_16x16x32_bf16 a[8:11], a[84:87], a[156:159], a[8:11]\n"
"ds_read_b128 a[156:159], v2 offset:31744\n"
"v_lshlrev_b32 v14, 16, v21\n"
"v_mfma_f32_16x16x32_bf16 a[8:11], a[80:83], a[152:155], a[8:11]\n"
"s_waitcnt vmcnt(10)\n"
"ds_read_b128 a[152:155], v2 offset:30720\n"
"v_and_b32 v15, s63, v21\n"
"v_mfma_f32_16x16x32_bf16 v[144:147], a[32:35], a[96:99], v[144:147]\n"
"ds_read_b128 v[16:19], v7 offset:36864\n"
"v_mul_f32 v12, v12, v92\n"
"v_mfma_f32_16x16x32_bf16 v[144:147], a[36:39], a[100:103], v[144:147]\n"
"global_load_dwordx4 a[80:83], v0, s[34:35] offset:0 sc1\n"
"v_mul_f32 v13, v13, v93\n"
"v_mfma_f32_16x16x32_bf16 v[172:175], a[44:47], a[100:103], v[172:175]\n"
"v_mul_f32 v14, v14, v94\n"
"v_mul_f32 v15, v15, v95\n"
"v_mfma_f32_16x16x32_bf16 v[172:175], a[40:43], a[96:99], v[172:175]\n"
"v_cvt_pk_bf16_f32 v28, v12, v13\n"
"v_cvt_pk_bf16_f32 v29, v14, v15\n"
"v_mfma_f32_16x16x32_bf16 v[176:179], a[40:43], a[104:107], v[176:179]\n"
"ds_write_b64 v2, v[28:29] offset:3072\n"
"v_mfma_f32_16x16x32_bf16 v[176:179], a[44:47], a[108:111], v[176:179]\n"
"v_lshlrev_b32 v12, 16, v22\n"
"v_and_b32 v13, s63, v22\n"
"v_mfma_f32_16x16x32_bf16 v[148:151], a[36:39], a[108:111], v[148:151]\n"
"v_lshlrev_b32 v14, 16, v23\n"
"v_mfma_f32_16x16x32_bf16 v[148:151], a[32:35], a[104:107], v[148:151]\n"
"s_waitcnt lgkmcnt(11)\n"
"v_and_b32 v15, s63, v23\n"
"v_mul_f32 v12, v12, v120\n"
"v_mfma_f32_16x16x32_bf16 v[152:155], a[32:35], a[112:115], v[152:155]\n"
"v_mul_f32 v13, v13, v121\n"
"v_mfma_f32_16x16x32_bf16 v[152:155], a[36:39], a[116:119], v[152:155]\n"
"global_load_dwordx4 a[84:87], v0, s[34:35] offset:1024 sc1\n"
"v_mul_f32 v14, v14, v122\n"
"v_mfma_f32_16x16x32_bf16 v[180:183], a[44:47], a[116:119], v[180:183]\n"
"v_mul_f32 v15, v15, v123\n"
"v_cvt_pk_bf16_f32 v30, v12, v13\n"
"v_mfma_f32_16x16x32_bf16 v[180:183], a[40:43], a[112:115], v[180:183]\n"
"v_cvt_pk_bf16_f32 v31, v14, v15\n"
"v_mfma_f32_16x16x32_bf16 v[184:187], a[40:43], a[120:123], v[184:187]\n"
"ds_write_b64 v2, v[30:31] offset:3080\n"
"v_mfma_f32_16x16x32_bf16 v[184:187], a[44:47], a[124:127], v[184:187]\n"
"s_waitcnt lgkmcnt(2)\n"
"v_lshlrev_b32 v12, 16, v16\n"
"v_and_b32 v13, s63, v16\n"
"v_mfma_f32_16x16x32_bf16 v[156:159], a[36:39], a[124:127], v[156:159]\n"
"v_lshlrev_b32 v14, 16, v17\n"
"v_mfma_f32_16x16x32_bf16 v[156:159], a[32:35], a[120:123], v[156:159]\n"
"v_and_b32 v15, s63, v17\n"
"v_xor_b32 v20, 64, v7\n"
"v_mfma_f32_16x16x32_bf16 v[160:163], a[32:35], a[128:131], v[160:163]\n"
"ds_read_b128 v[20:23], v20 offset:36864\n"
"v_mfma_f32_16x16x32_bf16 v[160:163], a[36:39], a[132:135], v[160:163]\n"
"global_load_dwordx4 a[88:91], v0, s[34:35] offset:2048 sc1\n"
"v_mul_f32 v12, v12, v40\n"
"v_mfma_f32_16x16x32_bf16 v[188:191], a[44:47], a[132:135], v[188:191]\n"
"v_mul_f32 v13, v13, v41\n"
"v_mfma_f32_16x16x32_bf16 v[188:191], a[40:43], a[128:131], v[188:191]\n"
"v_mul_f32 v14, v14, v42\n"
"v_mul_f32 v15, v15, v43\n"
"v_mfma_f32_16x16x32_bf16 v[192:195], a[40:43], a[136:139], v[192:195]\n"
"v_cvt_pk_bf16_f32 v24, v12, v13\n"
"v_mfma_f32_16x16x32_bf16 v[192:195], a[44:47], a[140:143], v[192:195]\n"
"v_cvt_pk_bf16_f32 v25, v14, v15\n"
"v_mfma_f32_16x16x32_bf16 v[164:167], a[36:39], a[140:143], v[164:167]\n"
"v_lshlrev_b32 v12, 16, v18\n"
"v_and_b32 v13, s63, v18\n"
"v_mfma_f32_16x16x32_bf16 v[164:167], a[32:35], a[136:139], v[164:167]\n"
"v_lshlrev_b32 v14, 16, v19\n"
"v_mfma_f32_16x16x32_bf16 v[168:171], a[32:35], a[144:147], v[168:171]\n"
"v_and_b32 v15, s63, v19\n"
"v_mul_f32 v12, v12, v68\n"
"v_mfma_f32_16x16x32_bf16 v[168:171], a[36:39], a[148:151], v[168:171]\n"
"global_load_dwordx4 a[92:95], v0, s[34:35] offset:3072 sc1\n"
"v_mul_f32 v13, v13, v69\n"
"v_mfma_f32_16x16x32_bf16 v[196:199], a[44:47], a[148:151], v[196:199]\n"
"v_mul_f32 v14, v14, v70\n"
"v_mfma_f32_16x16x32_bf16 v[196:199], a[40:43], a[144:147], v[196:199]\n"
"v_mul_f32 v15, v15, v71\n"
"v_mfma_f32_16x16x32_bf16 a[20:23], a[40:43], a[152:155], a[20:23]\n"
"s_add_u32 s34, s34, 8192\n"
"v_cvt_pk_bf16_f32 v26, v12, v13\n"
"v_cvt_pk_bf16_f32 v27, v14, v15\n"
"v_mfma_f32_16x16x32_bf16 a[20:23], a[44:47], a[156:159], a[20:23]\n"
"global_store_dwordx4 v0, v[24:27], s[52:53] offset:0 sc1\n"
"ds_write_b128 v2, v[24:27] offset:4096\n"
"v_mfma_f32_16x16x32_bf16 a[16:19], a[36:39], a[156:159], a[16:19]\n"
"s_waitcnt lgkmcnt(1)\n"
"v_lshlrev_b32 v12, 16, v20\n"
"v_mfma_f32_16x16x32_bf16 a[16:19], a[32:35], a[152:155], a[16:19]\n"
"s_waitcnt vmcnt(11)\n"
"v_and_b32 v13, s63, v20\n"
"v_mfma_f32_16x16x32_bf16 v[200:203], a[48:51], a[96:99], v[200:203]\n"
"v_lshlrev_b32 v14, 16, v21\n"
"v_and_b32 v15, s63, v21\n"
"v_mfma_f32_16x16x32_bf16 v[200:203], a[52:55], a[100:103], v[200:203]\n"
"global_load_dwordx4 a[32:35], v0, s[34:35] offset:-4096 sc1\n"
"ds_read_b128 v[16:19], v7 offset:38912\n"
"v_mfma_f32_16x16x32_bf16 v[228:231], a[60:63], a[100:103], v[228:231]\n"
"ds_read_b128 a[100:103], v3 offset:17408\n"
"v_mul_f32 v12, v12, v96\n"
"v_mfma_f32_16x16x32_bf16 v[228:231], a[56:59], a[96:99], v[228:231]\n"
"ds_read_b128 a[96:99], v3 offset:16384\n"
"v_mul_f32 v13, v13, v97\n"
"v_mfma_f32_16x16x32_bf16 v[232:235], a[56:59], a[104:107], v[232:235]\n"
"v_mul_f32 v14, v14, v98\n"
"v_mfma_f32_16x16x32_bf16 v[232:235], a[60:63], a[108:111], v[232:235]\n"
"v_mul_f32 v15, v15, v99\n"
"v_cvt_pk_bf16_f32 v28, v12, v13\n"
"v_mfma_f32_16x16x32_bf16 v[204:207], a[52:55], a[108:111], v[204:207]\n"
"ds_read_b128 a[108:111], v3 offset:19456\n"
"v_cvt_pk_bf16_f32 v29, v14, v15\n"
"v_mfma_f32_16x16x32_bf16 v[204:207], a[48:51], a[104:107], v[204:207]\n"
"ds_read_b128 a[104:107], v3 offset:18432\n"
"v_lshlrev_b32 v12, 16, v22\n"
"v_mfma_f32_16x16x32_bf16 v[208:211], a[48:51], a[112:115], v[208:211]\n"
"v_and_b32 v13, s63, v22\n"
"v_lshlrev_b32 v14, 16, v23\n"
"v_mfma_f32_16x16x32_bf16 v[208:211], a[52:55], a[116:119], v[208:211]\n"
"global_load_dwordx4 a[36:39], v0, s[34:35] offset:-3072 sc1\n"
"v_and_b32 v15, s63, v23\n"
"v_mfma_f32_16x16x32_bf16 v[236:239], a[60:63], a[116:119], v[236:239]\n"
"ds_read_b128 a[116:119], v3 offset:21504\n"
"v_mul_f32 v12, v12, v124\n"
"v_mfma_f32_16x16x32_bf16 v[236:239], a[56:59], a[112:115], v[236:239]\n"
"ds_read_b128 a[112:115], v3 offset:20480\n"
"v_mul_f32 v13, v13, v125\n"
"v_mfma_f32_16x16x32_bf16 v[240:243], a[56:59], a[120:123], v[240:243]\n"
"v_mul_f32 v14, v14, v126\n"
"v_mul_f32 v15, v15, v127\n"
"v_mfma_f32_16x16x32_bf16 v[240:243], a[60:63], a[124:127], v[240:243]\n"
"v_cvt_pk_bf16_f32 v30, v12, v13\n"
"v_mfma_f32_16x16x32_bf16 v[212:215], a[52:55], a[124:127], v[212:215]\n"
"ds_read_b128 a[124:127], v3 offset:23552\n"
"v_cvt_pk_bf16_f32 v31, v14, v15\n"
"v_mfma_f32_16x16x32_bf16 v[212:215], a[48:51], a[120:123], v[212:215]\n"
"ds_read_b128 a[120:123], v3 offset:22528\n"
"v_mfma_f32_16x16x32_bf16 v[216:219], a[48:51], a[128:131], v[216:219]\n"
"global_store_dwordx4 v0, v[28:31], s[52:53] offset:1024 sc1\n"
"ds_write_b128 v2, v[28:31] offset:5120\n"
"v_mfma_f32_16x16x32_bf16 v[216:219], a[52:55], a[132:135], v[216:219]\n"
"s_waitcnt lgkmcnt(9)\n"
"global_load_dwordx4 a[40:43], v0, s[34:35] offset:-2048 sc1\n"
"v_lshlrev_b32 v12, 16, v16\n"
"v_mfma_f32_16x16x32_bf16 v[244:247], a[60:63], a[132:135], v[244:247]\n"
"ds_read_b128 a[132:135], v3 offset:25600\n"
"v_and_b32 v13, s63, v16\n"
"v_mfma_f32_16x16x32_bf16 v[244:247], a[56:59], a[128:131], v[244:247]\n"
"ds_read_b128 a[128:131], v3 offset:24576\n"
"v_lshlrev_b32 v14, 16, v17\n"
"v_mfma_f32_16x16x32_bf16 v[248:251], a[56:59], a[136:139], v[248:251]\n"
"v_and_b32 v15, s63, v17\n"
"v_xor_b32 v20, 64, v7\n"
"v_mfma_f32_16x16x32_bf16 v[248:251], a[60:63], a[140:143], v[248:251]\n"
"ds_read_b128 v[20:23], v20 offset:38912\n"
"v_mul_f32 v12, v12, v44\n"
"v_mfma_f32_16x16x32_bf16 v[220:223], a[52:55], a[140:143], v[220:223]\n"
"ds_read_b128 a[140:143], v3 offset:27648\n"
"v_mul_f32 v13, v13, v45\n"
"v_mfma_f32_16x16x32_bf16 v[220:223], a[48:51], a[136:139], v[220:223]\n"
"ds_read_b128 a[136:139], v3 offset:26624\n"
"v_mul_f32 v14, v14, v46\n"
"v_mfma_f32_16x16x32_bf16 v[224:227], a[48:51], a[144:147], v[224:227]\n"
"v_mul_f32 v15, v15, v47\n"
"v_cvt_pk_bf16_f32 v24, v12, v13\n"
"v_mfma_f32_16x16x32_bf16 v[224:227], a[52:55], a[148:151], v[224:227]\n"
"global_load_dwordx4 a[44:47], v0, s[34:35] offset:-1024 sc1\n"
"v_cvt_pk_bf16_f32 v25, v14, v15\n"
"v_mfma_f32_16x16x32_bf16 v[252:255], a[60:63], a[148:151], v[252:255]\n"
"ds_read_b128 a[148:151], v3 offset:29696\n"
"v_mfma_f32_16x16x32_bf16 v[252:255], a[56:59], a[144:147], v[252:255]\n"
"ds_read_b128 a[144:147], v3 offset:28672\n"
"v_mfma_f32_16x16x32_bf16 a[28:31], a[56:59], a[152:155], a[28:31]\n"
"ds_write_b64 v2, v[24:25] offset:6144\n"
"v_mfma_f32_16x16x32_bf16 a[28:31], a[60:63], a[156:159], a[28:31]\n"
"v_lshlrev_b32 v12, 16, v18\n"
"v_and_b32 v13, s63, v18\n"
"v_mfma_f32_16x16x32_bf16 a[24:27], a[52:55], a[156:159], a[24:27]\n"
"ds_read_b128 a[156:159], v3 offset:31744\n"
"v_lshlrev_b32 v14, 16, v19\n"
"v_mfma_f32_16x16x32_bf16 a[24:27], a[48:51], a[152:155], a[24:27]\n"
"s_waitcnt vmcnt(11) lgkmcnt(14)\n"
"ds_read_b128 a[152:155], v3 offset:30720\n"
"v_and_b32 v15, s63, v19\n"
"v_mfma_f32_16x16x32_bf16 v[144:147], a[64:67], a[96:99], v[144:147]\n"
"v_mul_f32 v12, v12, v72\n"
"v_mul_f32 v13, v13, v73\n"
"v_mfma_f32_16x16x32_bf16 v[144:147], a[68:71], a[100:103], v[144:147]\n"
"global_load_dwordx4 a[48:51], v0, s[34:35] offset:0 sc1\n"
"v_mul_f32 v14, v14, v74\n"
"v_mfma_f32_16x16x32_bf16 v[172:175], a[76:79], a[100:103], v[172:175]\n"
"v_mul_f32 v15, v15, v75\n"
"v_cvt_pk_bf16_f32 v26, v12, v13\n"
"v_mfma_f32_16x16x32_bf16 v[172:175], a[72:75], a[96:99], v[172:175]\n"
"v_cvt_pk_bf16_f32 v27, v14, v15\n"
"v_mfma_f32_16x16x32_bf16 v[176:179], a[72:75], a[104:107], v[176:179]\n"
"ds_write_b64 v2, v[26:27] offset:6152\n"
"v_mfma_f32_16x16x32_bf16 v[176:179], a[76:79], a[108:111], v[176:179]\n"
"s_waitcnt lgkmcnt(8)\n"
"v_lshlrev_b32 v12, 16, v20\n"
"v_and_b32 v13, s63, v20\n"
"v_mfma_f32_16x16x32_bf16 v[148:151], a[68:71], a[108:111], v[148:151]\n"
"v_lshlrev_b32 v14, 16, v21\n"
"v_and_b32 v15, s63, v21\n"
"v_mfma_f32_16x16x32_bf16 v[148:151], a[64:67], a[104:107], v[148:151]\n"
"v_mul_f32 v12, v12, v100\n"
"v_mul_f32 v13, v13, v101\n"
"v_mfma_f32_16x16x32_bf16 v[152:155], a[64:67], a[112:115], v[152:155]\n"
"v_mul_f32 v14, v14, v102\n"
"v_mul_f32 v15, v15, v103\n"
"v_mfma_f32_16x16x32_bf16 v[152:155], a[68:71], a[116:119], v[152:155]\n"
"global_load_dwordx4 a[52:55], v0, s[34:35] offset:1024 sc1\n"
"v_cvt_pk_bf16_f32 v28, v12, v13\n"
"v_mfma_f32_16x16x32_bf16 v[180:183], a[76:79], a[116:119], v[180:183]\n"
"v_cvt_pk_bf16_f32 v29, v14, v15\n"
"v_lshlrev_b32 v12, 16, v22\n"
"v_mfma_f32_16x16x32_bf16 v[180:183], a[72:75], a[112:115], v[180:183]\n"
"v_and_b32 v13, s63, v22\n"
"v_lshlrev_b32 v14, 16, v23\n"
"v_mfma_f32_16x16x32_bf16 v[184:187], a[72:75], a[120:123], v[184:187]\n"
"v_and_b32 v15, s63, v23\n"
"v_mul_f32 v12, v12, v128\n"
"v_mfma_f32_16x16x32_bf16 v[184:187], a[76:79], a[124:127], v[184:187]\n"
"v_mul_f32 v13, v13, v129\n"
"v_mfma_f32_16x16x32_bf16 v[156:159], a[68:71], a[124:127], v[156:159]\n"
"v_mul_f32 v14, v14, v130\n"
"v_mfma_f32_16x16x32_bf16 v[156:159], a[64:67], a[120:123], v[156:159]\n"
"s_waitcnt lgkmcnt(6)\n"
"v_mul_f32 v15, v15, v131\n"
"v_cvt_pk_bf16_f32 v30, v12, v13\n"
"v_mfma_f32_16x16x32_bf16 v[160:163], a[64:67], a[128:131], v[160:163]\n"
"v_cvt_pk_bf16_f32 v31, v14, v15\n"
"v_mfma_f32_16x16x32_bf16 v[160:163], a[68:71], a[132:135], v[160:163]\n"
"global_load_dwordx4 a[56:59], v0, s[34:35] offset:2048 sc1\n"
"v_mfma_f32_16x16x32_bf16 v[188:191], a[76:79], a[132:135], v[188:191]\n"
"global_store_dwordx4 v0, v[28:31], s[52:53] offset:3072 sc1\n"
"ds_write_b128 v2, v[28:31] offset:7168\n"
"v_mfma_f32_16x16x32_bf16 v[188:191], a[72:75], a[128:131], v[188:191]\n"
"v_accvgpr_read_b32 v16, a160\n"
"v_accvgpr_read_b32 v17, a161\n"
"v_mfma_f32_16x16x32_bf16 v[192:195], a[72:75], a[136:139], v[192:195]\n"
"v_accvgpr_read_b32 v18, a162\n"
"v_mfma_f32_16x16x32_bf16 v[192:195], a[76:79], a[140:143], v[192:195]\n"
"v_accvgpr_read_b32 v19, a163\n"
"v_lshlrev_b32 v12, 16, v16\n"
"v_mfma_f32_16x16x32_bf16 v[164:167], a[68:71], a[140:143], v[164:167]\n"
"v_and_b32 v13, s63, v16\n"
"v_mfma_f32_16x16x32_bf16 v[164:167], a[64:67], a[136:139], v[164:167]\n"
"s_waitcnt lgkmcnt(2)\n"
"v_lshlrev_b32 v14, 16, v17\n"
"v_mfma_f32_16x16x32_bf16 v[168:171], a[64:67], a[144:147], v[168:171]\n"
"v_and_b32 v15, s63, v17\n"
"v_mul_f32 v12, v12, v48\n"
"v_mfma_f32_16x16x32_bf16 v[168:171], a[68:71], a[148:151], v[168:171]\n"
"global_load_dwordx4 a[60:63], v0, s[34:35] offset:3072 sc1\n"
"v_mul_f32 v13, v13, v49\n"
"v_mfma_f32_16x16x32_bf16 v[196:199], a[76:79], a[148:151], v[196:199]\n"
"v_mul_f32 v14, v14, v50\n"
"v_mfma_f32_16x16x32_bf16 v[196:199], a[72:75], a[144:147], v[196:199]\n"
"v_mul_f32 v15, v15, v51\n"
"v_cvt_pk_bf16_f32 v24, v12, v13\n"
"v_mfma_f32_16x16x32_bf16 a[20:23], a[72:75], a[152:155], a[20:23]\n"
"s_add_u32 s34, s34, 8192\n"
"v_cvt_pk_bf16_f32 v25, v14, v15\n"
"v_mfma_f32_16x16x32_bf16 a[20:23], a[76:79], a[156:159], a[20:23]\n"
"v_lshlrev_b32 v12, 16, v18\n"
"v_mfma_f32_16x16x32_bf16 a[16:19], a[68:71], a[156:159], a[16:19]\n"
"v_and_b32 v13, s63, v18\n"
"v_lshlrev_b32 v14, 16, v19\n"
"v_mfma_f32_16x16x32_bf16 a[16:19], a[64:67], a[152:155], a[16:19]\n"
"s_waitcnt vmcnt(11)\n"
"v_and_b32 v15, s63, v19\n"
"v_mul_f32 v12, v12, v76\n"
"v_mfma_f32_16x16x32_bf16 v[200:203], a[80:83], a[96:99], v[200:203]\n"
"v_mul_f32 v13, v13, v77\n"
"v_mul_f32 v14, v14, v78\n"
"v_mfma_f32_16x16x32_bf16 v[200:203], a[84:87], a[100:103], v[200:203]\n"
"global_load_dwordx4 a[64:67], v0, s[34:35] offset:-4096 sc1\n"
"v_mul_f32 v15, v15, v79\n"
"v_mfma_f32_16x16x32_bf16 v[228:231], a[92:95], a[100:103], v[228:231]\n"
"ds_read_b128 a[100:103], v4 offset:17408\n"
"v_cvt_pk_bf16_f32 v26, v12, v13\n"
"v_mfma_f32_16x16x32_bf16 v[228:231], a[88:91], a[96:99], v[228:231]\n"
"ds_read_b128 a[96:99], v4 offset:16384\n"
"v_cvt_pk_bf16_f32 v27, v14, v15\n"
"v_mfma_f32_16x16x32_bf16 v[232:235], a[88:91], a[104:107], v[232:235]\n"
"global_store_dwordx4 v0, v[24:27], s[54:55] offset:-4096 sc1\n"
"ds_write_b128 v2, v[24:27] offset:8192\n"
"v_mfma_f32_16x16x32_bf16 v[232:235], a[92:95], a[108:111], v[232:235]\n"
"v_accvgpr_read_b32 v16, a164\n"
"v_mfma_f32_16x16x32_bf16 v[204:207], a[84:87], a[108:111], v[204:207]\n"
"ds_read_b128 a[108:111], v4 offset:19456\n"
"v_accvgpr_read_b32 v17, a165\n"
"v_mfma_f32_16x16x32_bf16 v[204:207], a[80:83], a[104:107], v[204:207]\n"
"ds_read_b128 a[104:107], v4 offset:18432\n"
"v_accvgpr_read_b32 v18, a166\n"
"v_mfma_f32_16x16x32_bf16 v[208:211], a[80:83], a[112:115], v[208:211]\n"
"v_accvgpr_read_b32 v19, a167\n"
"v_lshlrev_b32 v12, 16, v16\n"
"v_mfma_f32_16x16x32_bf16 v[208:211], a[84:87], a[116:119], v[208:211]\n"
"global_load_dwordx4 a[68:71], v0, s[34:35] offset:-3072 sc1\n"
"v_and_b32 v13, s63, v16\n"
"v_mfma_f32_16x16x32_bf16 v[236:239], a[92:95], a[116:119], v[236:239]\n"
"ds_read_b128 a[116:119], v4 offset:21504\n"
"v_lshlrev_b32 v14, 16, v17\n"
"v_mfma_f32_16x16x32_bf16 v[236:239], a[88:91], a[112:115], v[236:239]\n"
"L_st4490:\n"
"s_cbranch_execz L_retA\n"
"ds_read_b128 a[112:115], v4 offset:20480\n"
"v_and_b32 v15, s63, v17\n"
"v_mfma_f32_16x16x32_bf16 v[240:243], a[88:91], a[120:123], v[240:243]\n"
"v_mul_f32 v12, v12, v104\n"
"v_mul_f32 v13, v13, v105\n"
"v_mfma_f32_16x16x32_bf16 v[240:243], a[92:95], a[124:127], v[240:243]\n"
"v_mul_f32 v14, v14, v106\n"
"v_mul_f32 v15, v15, v107\n"
"v_mfma_f32_16x16x32_bf16 v[212:215], a[84:87], a[124:127], v[212:215]\n"
"ds_read_b128 a[124:127], v4 offset:23552\n"
"v_cvt_pk_bf16_f32 v28, v12, v13\n"
"v_mfma_f32_16x16x32_bf16 v[212:215], a[80:83], a[120:123], v[212:215]\n"
"ds_read_b128 a[120:123], v4 offset:22528\n"
"v_cvt_pk_bf16_f32 v29, v14, v15\n"
"v_mfma_f32_16x16x32_bf16 v[216:219], a[80:83], a[128:131], v[216:219]\n"
"ds_write_b64 v2, v[28:29] offset:9216\n"
"v_mfma_f32_16x16x32_bf16 v[216:219], a[84:87], a[132:135], v[216:219]\n"
"global_load_dwordx4 a[72:75], v0, s[34:35] offset:-2048 sc1\n"
"v_lshlrev_b32 v12, 16, v18\n"
"v_mfma_f32_16x16x32_bf16 v[244:247], a[92:95], a[132:135], v[244:247]\n"
"ds_read_b128 a[132:135], v4 offset:25600\n"
"v_and_b32 v13, s63, v18\n"
"v_mfma_f32_16x16x32_bf16 v[244:247], a[88:91], a[128:131], v[244:247]\n"
"ds_read_b128 a[128:131], v4 offset:24576\n"
"v_lshlrev_b32 v14, 16, v19\n"
"v_mfma_f32_16x16x32_bf16 v[248:251], a[88:91], a[136:139], v[248:251]\n"
"v_and_b32 v15, s63, v19\n"
"v_mul_f32 v12, v12, v132\n"
"v_mfma_f32_16x16x32_bf16 v[248:251], a[92:95], a[140:143], v[248:251]\n"
"v_mul_f32 v13, v13, v133\n"
"v_mfma_f32_16x16x32_bf16 v[220:223], a[84:87], a[140:143], v[220:223]\n"
"ds_read_b128 a[140:143], v4 offset:27648\n"
"v_mul_f32 v14, v14, v134\n"
"v_mfma_f32_16x16x32_bf16 v[220:223], a[80:83], a[136:139], v[220:223]\n"
"ds_read_b128 a[136:139], v4 offset:26624\n"
"v_mul_f32 v15, v15, v135\n"
"v_mfma_f32_16x16x32_bf16 v[224:227], a[80:83], a[144:147], v[224:227]\n"
"v_cvt_pk_bf16_f32 v30, v12, v13\n"
"v_cvt_pk_bf16_f32 v31, v14, v15\n"
"v_mfma_f32_16x16x32_bf16 v[224:227], a[84:87], a[148:151], v[224:227]\n"
"global_load_dwordx4 a[76:79], v0, s[34:35] offset:-1024 sc1\n"
"v_mfma_f32_16x16x32_bf16 v[252:255], a[92:95], a[148:151], v[252:255]\n"
"ds_read_b128 a[148:151], v4 offset:29696\n"
"v_mfma_f32_16x16x32_bf16 v[252:255], a[88:91], a[144:147], v[252:255]\n"
"ds_read_b128 a[144:147], v4 offset:28672\n"
"v_mfma_f32_16x16x32_bf16 a[28:31], a[88:91], a[152:155], a[28:31]\n"
"ds_write_b64 v2, v[30:31] offset:9224\n"
"v_mfma_f32_16x16x32_bf16 a[28:31], a[92:95], a[156:159], a[28:31]\n"
"v_accvgpr_read_b32 v16, a168\n"
"v_accvgpr_read_b32 v17, a169\n"
"v_mfma_f32_16x16x32_bf16 a[24:27], a[84:87], a[156:159], a[24:27]\n"
"ds_read_b128 a[156:159], v4 offset:31744\n"
"v_accvgpr_read_b32 v18, a170\n"
"v_mfma_f32_16x16x32_bf16 a[24:27], a[80:83], a[152:155], a[24:27]\n"
"s_waitcnt vmcnt(10) lgkmcnt(13)\n"
"ds_read_b128 a[152:155], v4 offset:30720\n"
"v_accvgpr_read_b32 v19, a171\n"
"v_mfma_f32_16x16x32_bf16 v[144:147], a[32:35], a[96:99], v[144:147]\n"
"v_lshlrev_b32 v12, 16, v16\n"
"v_and_b32 v13, s63, v16\n"
"v_mfma_f32_16x16x32_bf16 v[144:147], a[36:39], a[100:103], v[144:147]\n"
"global_load_dwordx4 a[80:83], v0, s[34:35] offset:0 sc1\n"
"v_lshlrev_b32 v14, 16, v17\n"
"v_mfma_f32_16x16x32_bf16 v[172:175], a[44:47], a[100:103], v[172:175]\n"
"v_and_b32 v15, s63, v17\n"
"v_mul_f32 v12, v12, v52\n"
"v_mfma_f32_16x16x32_bf16 v[172:175], a[40:43], a[96:99], v[172:175]\n"
"v_mul_f32 v13, v13, v53\n"
"v_mul_f32 v14, v14, v54\n"
"v_mfma_f32_16x16x32_bf16 v[176:179], a[40:43], a[104:107], v[176:179]\n"
"v_mul_f32 v15, v15, v55\n"
"v_cvt_pk_bf16_f32 v24, v12, v13\n"
"v_mfma_f32_16x16x32_bf16 v[176:179], a[44:47], a[108:111], v[176:179]\n"
"v_cvt_pk_bf16_f32 v25, v14, v15\n"
"v_lshlrev_b32 v12, 16, v18\n"
"v_mfma_f32_16x16x32_bf16 v[148:151], a[36:39], a[108:111], v[148:151]\n"
"v_and_b32 v13, s63, v18\n"
"v_lshlrev_b32 v14, 16, v19\n"
"v_mfma_f32_16x16x32_bf16 v[148:151], a[32:35], a[104:107], v[148:151]\n"
"s_waitcnt lgkmcnt(10)\n"
"v_and_b32 v15, s63, v19\n"
"v_mul_f32 v12, v12, v80\n"
"v_mfma_f32_16x16x32_bf16 v[152:155], a[32:35], a[112:115], v[152:155]\n"
"v_mul_f32 v13, v13, v81\n"
"v_mul_f32 v14, v14, v82\n"
"v_mfma_f32_16x16x32_bf16 v[152:155], a[36:39], a[116:119], v[152:155]\n"
"global_load_dwordx4 a[84:87], v0, s[34:35] offset:1024 sc1\n"
"v_mul_f32 v15, v15, v83\n"
"v_mfma_f32_16x16x32_bf16 v[180:183], a[44:47], a[116:119], v[180:183]\n"
"v_cvt_pk_bf16_f32 v26, v12, v13\n"
"v_mfma_f32_16x16x32_bf16 v[180:183], a[40:43], a[112:115], v[180:183]\n"
"v_cvt_pk_bf16_f32 v27, v14, v15\n"
"v_mfma_f32_16x16x32_bf16 v[184:187], a[40:43], a[120:123], v[184:187]\n"
"global_store_dwordx4 v0, v[24:27], s[54:55] offset:-2048 sc1\n"
"ds_write_b128 v2, v[24:27] offset:10240\n"
"v_mfma_f32_16x16x32_bf16 v[184:187], a[44:47], a[124:127], v[184:187]\n"
"v_accvgpr_read_b32 v16, a172\n"
"v_mfma_f32_16x16x32_bf16 v[156:159], a[36:39], a[124:127], v[156:159]\n"
"v_accvgpr_read_b32 v17, a173\n"
"v_mfma_f32_16x16x32_bf16 v[156:159], a[32:35], a[120:123], v[156:159]\n"
"s_waitcnt lgkmcnt(6)\n"
"v_accvgpr_read_b32 v18, a174\n"
"v_accvgpr_read_b32 v19, a175\n"
"v_mfma_f32_16x16x32_bf16 v[160:163], a[32:35], a[128:131], v[160:163]\n"
"v_lshlrev_b32 v12, 16, v16\n"
"v_mfma_f32_16x16x32_bf16 v[160:163], a[36:39], a[132:135], v[160:163]\n"
"global_load_dwordx4 a[88:91], v0, s[34:35] offset:2048 sc1\n"
"v_and_b32 v13, s63, v16\n"
"v_mfma_f32_16x16x32_bf16 v[188:191], a[44:47], a[132:135], v[188:191]\n"
"v_lshlrev_b32 v14, 16, v17\n"
"v_and_b32 v15, s63, v17\n"
"v_mfma_f32_16x16x32_bf16 v[188:191], a[40:43], a[128:131], v[188:191]\n"
"v_mul_f32 v12, v12, v108\n"
"v_mfma_f32_16x16x32_bf16 v[192:195], a[40:43], a[136:139], v[192:195]\n"
"v_mul_f32 v13, v13, v109\n"
"v_mfma_f32_16x16x32_bf16 v[192:195], a[44:47], a[140:143], v[192:195]\n"
"v_mul_f32 v14, v14, v110\n"
"v_mul_f32 v15, v15, v111\n"
"v_mfma_f32_16x16x32_bf16 v[164:167], a[36:39], a[140:143], v[164:167]\n"
"v_cvt_pk_bf16_f32 v28, v12, v13\n"
"v_mfma_f32_16x16x32_bf16 v[164:167], a[32:35], a[136:139], v[164:167]\n"
"s_waitcnt lgkmcnt(1)\n"
"v_cvt_pk_bf16_f32 v29, v14, v15\n"
"v_mfma_f32_16x16x32_bf16 v[168:171], a[32:35], a[144:147], v[168:171]\n"
"v_lshlrev_b32 v12, 16, v18\n"
"v_and_b32 v13, s63, v18\n"
"v_mfma_f32_16x16x32_bf16 v[168:171], a[36:39], a[148:151], v[168:171]\n"
"global_load_dwordx4 a[92:95], v0, s[34:35] offset:3072 sc1\n"
"v_lshlrev_b32 v14, 16, v19\n"
"v_mfma_f32_16x16x32_bf16 v[196:199], a[44:47], a[148:151], v[196:199]\n"
"v_and_b32 v15, s63, v19\n"
"v_mfma_f32_16x16x32_bf16 v[196:199], a[40:43], a[144:147], v[196:199]\n"
"v_mul_f32 v12, v12, v136\n"
"v_mul_f32 v13, v13, v137\n"
"v_mfma_f32_16x16x32_bf16 a[20:23], a[40:43], a[152:155], a[20:23]\n"
"s_add_u32 s34, s34, 8192\n"
"v_mul_f32 v14, v14, v138\n"
"v_mfma_f32_16x16x32_bf16 a[20:23], a[44:47], a[156:159], a[20:23]\n"
"v_mul_f32 v15, v15, v139\n"
"v_mfma_f32_16x16x32_bf16 a[16:19], a[36:39], a[156:159], a[16:19]\n"
"v_cvt_pk_bf16_f32 v30, v12, v13\n"
"v_cvt_pk_bf16_f32 v31, v14, v15\n"
"v_mfma_f32_16x16x32_bf16 a[16:19], a[32:35], a[152:155], a[16:19]\n"
"s_waitcnt vmcnt(10)\n"
"global_store_dwordx4 v0, v[28:31], s[54:55] offset:-1024 sc1\n"
"ds_write_b128 v2, v[28:31] offset:11264\n"
"v_mfma_f32_16x16x32_bf16 v[200:203], a[48:51], a[96:99], v[200:203]\n"
"v_accvgpr_read_b32 v16, a176\n"
"v_mfma_f32_16x16x32_bf16 v[200:203], a[52:55], a[100:103], v[200:203]\n"
"global_load_dwordx4 a[32:35], v0, s[34:35] offset:-4096 sc1\n"
"v_accvgpr_read_b32 v17, a177\n"
"v_mfma_f32_16x16x32_bf16 v[228:231], a[60:63], a[100:103], v[228:231]\n"
"ds_read_b128 a[100:103], v5 offset:17408\n"
"v_accvgpr_read_b32 v18, a178\n"
"v_mfma_f32_16x16x32_bf16 v[228:231], a[56:59], a[96:99], v[228:231]\n"
"ds_read_b128 a[96:99], v5 offset:16384\n"
"v_accvgpr_read_b32 v19, a179\n"
"v_mfma_f32_16x16x32_bf16 v[232:235], a[56:59], a[104:107], v[232:235]\n"
"v_lshlrev_b32 v12, 16, v16\n"
"v_and_b32 v13, s63, v16\n"
"v_mfma_f32_16x16x32_bf16 v[232:235], a[60:63], a[108:111], v[232:235]\n"
"v_lshlrev_b32 v14, 16, v17\n"
"v_mfma_f32_16x16x32_bf16 v[204:207], a[52:55], a[108:111], v[204:207]\n"
"ds_read_b128 a[108:111], v5 offset:19456\n"
"v_and_b32 v15, s63, v17\n"
"v_mfma_f32_16x16x32_bf16 v[204:207], a[48:51], a[104:107], v[204:207]\n"
"ds_read_b128 a[104:107], v5 offset:18432\n"
"v_mul_f32 v12, v12, v56\n"
"v_mfma_f32_16x16x32_bf16 v[208:211], a[48:51], a[112:115], v[208:211]\n"
"v_mul_f32 v13, v13, v57\n"
"v_mul_f32 v14, v14, v58\n"
"v_mfma_f32_16x16x32_bf16 v[208:211], a[52:55], a[116:119], v[208:211]\n"
"global_load_dwordx4 a[36:39], v0, s[34:35] offset:-3072 sc1\n"
"v_mul_f32 v15, v15, v59\n"
"v_mfma_f32_16x16x32_bf16 v[236:239], a[60:63], a[116:119], v[236:239]\n"
"ds_read_b128 a[116:119], v5 offset:21504\n"
"v_cvt_pk_bf16_f32 v24, v12, v13\n"
"v_mfma_f32_16x16x32_bf16 v[236:239], a[56:59], a[112:115], v[236:239]\n"
"ds_read_b128 a[112:115], v5 offset:20480\n"
"v_cvt_pk_bf16_f32 v25, v14, v15\n"
"v_mfma_f32_16x16x32_bf16 v[240:243], a[56:59], a[120:123], v[240:243]\n"
"ds_write_b64 v2, v[24:25] offset:12288\n"
"v_mfma_f32_16x16x32_bf16 v[240:243], a[60:63], a[124:127], v[240:243]\n"
"v_lshlrev_b32 v12, 16, v18\n"
"v_and_b32 v13, s63, v18\n"
"v_mfma_f32_16x16x32_bf16 v[212:215], a[52:55], a[124:127], v[212:215]\n"
"ds_read_b128 a[124:127], v5 offset:23552\n"
"v_lshlrev_b32 v14, 16, v19\n"
"v_mfma_f32_16x16x32_bf16 v[212:215], a[48:51], a[120:123], v[212:215]\n"
"ds_read_b128 a[120:123], v5 offset:22528\n"
"v_and_b32 v15, s63, v19\n"
"v_mfma_f32_16x16x32_bf16 v[216:219], a[48:51], a[128:131], v[216:219]\n"
"v_mul_f32 v12, v12, v84\n"
"v_mul_f32 v13, v13, v85\n"
"v_mfma_f32_16x16x32_bf16 v[216:219], a[52:55], a[132:135], v[216:219]\n"
"global_load_dwordx4 a[40:43], v0, s[34:35] offset:-2048 sc1\n"
"v_mul_f32 v14, v14, v86\n"
"v_mfma_f32_16x16x32_bf16 v[244:247], a[60:63], a[132:135], v[244:247]\n"
"ds_read_b128 a[132:135], v5 offset:25600\n"
"v_mul_f32 v15, v15, v87\n"
"v_mfma_f32_16x16x32_bf16 v[244:247], a[56:59], a[128:131], v[244:247]\n"
"ds_read_b128 a[128:131], v5 offset:24576\n"
"v_cvt_pk_bf16_f32 v26, v12, v13\n"
"v_mfma_f32_16x16x32_bf16 v[248:251], a[56:59], a[136:139], v[248:251]\n"
"v_cvt_pk_bf16_f32 v27, v14, v15\n"
"v_mfma_f32_16x16x32_bf16 v[248:251], a[60:63], a[140:143], v[248:251]\n"
"ds_write_b64 v2, v[26:27] offset:12296\n"
"v_mfma_f32_16x16x32_bf16 v[220:223], a[52:55], a[140:143], v[220:223]\n"
"ds_read_b128 a[140:143], v5 offset:27648\n"
"v_accvgpr_read_b32 v16, a180\n"
"v_mfma_f32_16x16x32_bf16 v[220:223], a[48:51], a[136:139], v[220:223]\n"
"ds_read_b128 a[136:139], v5 offset:26624\n"
"v_accvgpr_read_b32 v17, a181\n"
"v_mfma_f32_16x16x32_bf16 v[224:227], a[48:51], a[144:147], v[224:227]\n"
"v_accvgpr_read_b32 v18, a182\n"
"v_accvgpr_read_b32 v19, a183\n"
"v_mfma_f32_16x16x32_bf16 v[224:227], a[52:55], a[148:151], v[224:227]\n"
"global_load_dwordx4 a[44:47], v0, s[34:35] offset:-1024 sc1\n"
"v_lshlrev_b32 v12, 16, v16\n"
"v_mfma_f32_16x16x32_bf16 v[252:255], a[60:63], a[148:151], v[252:255]\n"
"ds_read_b128 a[148:151], v5 offset:29696\n"
"v_and_b32 v13, s63, v16\n"
"v_mfma_f32_16x16x32_bf16 v[252:255], a[56:59], a[144:147], v[252:255]\n"
"ds_read_b128 a[144:147], v5 offset:28672\n"
"v_lshlrev_b32 v14, 16, v17\n"
"v_mfma_f32_16x16x32_bf16 a[28:31], a[56:59], a[152:155], a[28:31]\n"
"v_and_b32 v15, s63, v17\n"
"v_mul_f32 v12, v12, v112\n"
"v_mfma_f32_16x16x32_bf16 a[28:31], a[60:63], a[156:159], a[28:31]\n"
"v_mul_f32 v13, v13, v113\n"
"v_mul_f32 v14, v14, v114\n"
"v_mfma_f32_16x16x32_bf16 a[24:27], a[52:55], a[156:159], a[24:27]\n"
"ds_read_b128 a[156:159], v5 offset:31744\n"
"v_mul_f32 v15, v15, v115\n"
"v_mfma_f32_16x16x32_bf16 a[24:27], a[48:51], a[152:155], a[24:27]\n"
"s_waitcnt vmcnt(10) lgkmcnt(13)\n"
"ds_read_b128 a[152:155], v5 offset:30720\n"
"v_cvt_pk_bf16_f32 v28, v12, v13\n"
"v_mfma_f32_16x16x32_bf16 v[144:147], a[64:67], a[96:99], v[144:147]\n"
"v_cvt_pk_bf16_f32 v29, v14, v15\n"
"v_lshlrev_b32 v12, 16, v18\n"
"v_mfma_f32_16x16x32_bf16 v[144:147], a[68:71], a[100:103], v[144:147]\n"
"global_load_dwordx4 a[48:51], v0, s[34:35] offset:0 sc1\n"
"v_and_b32 v13, s63, v18\n"
"v_mfma_f32_16x16x32_bf16 v[172:175], a[76:79], a[100:103], v[172:175]\n"
"v_lshlrev_b32 v14, 16, v19\n"
"v_and_b32 v15, s63, v19\n"
"v_mfma_f32_16x16x32_bf16 v[172:175], a[72:75], a[96:99], v[172:175]\n"
"v_mul_f32 v12, v12, v140\n"
"v_mul_f32 v13, v13, v141\n"
"v_mfma_f32_16x16x32_bf16 v[176:179], a[72:75], a[104:107], v[176:179]\n"
"v_mul_f32 v14, v14, v142\n"
"v_mul_f32 v15, v15, v143\n"
"v_mfma_f32_16x16x32_bf16 v[176:179], a[76:79], a[108:111], v[176:179]\n"
"v_cvt_pk_bf16_f32 v30, v12, v13\n"
"v_mfma_f32_16x16x32_bf16 v[148:151], a[68:71], a[108:111], v[148:151]\n"
"v_cvt_pk_bf16_f32 v31, v14, v15\n"
"v_mfma_f32_16x16x32_bf16 v[148:151], a[64:67], a[104:107], v[148:151]\n"
"s_waitcnt lgkmcnt(9)\n"
"global_store_dwordx4 v0, v[28:31], s[54:55] offset:1024 sc1\n"
"ds_write_b128 v2, v[28:31] offset:13312\n"
"v_mfma_f32_16x16x32_bf16 v[152:155], a[64:67], a[112:115], v[152:155]\n"
"v_accvgpr_read_b32 v16, a184\n"
"v_accvgpr_read_b32 v17, a185\n"
"v_mfma_f32_16x16x32_bf16 v[152:155], a[68:71], a[116:119], v[152:155]\n"
"global_load_dwordx4 a[52:55], v0, s[34:35] offset:1024 sc1\n"
"v_accvgpr_read_b32 v18, a186\n"
"v_mfma_f32_16x16x32_bf16 v[180:183], a[76:79], a[116:119], v[180:183]\n"
"v_accvgpr_read_b32 v19, a187\n"
"v_mfma_f32_16x16x32_bf16 v[180:183], a[72:75], a[112:115], v[180:183]\n"
"v_lshlrev_b32 v12, 16, v16\n"
"v_mfma_f32_16x16x32_bf16 v[184:187], a[72:75], a[120:123], v[184:187]\n"
"v_and_b32 v13, s63, v16\n"
"v_lshlrev_b32 v14, 16, v17\n"
"v_mfma_f32_16x16x32_bf16 v[184:187], a[76:79], a[124:127], v[184:187]\n"
"v_and_b32 v15, s63, v17\n"
"v_mfma_f32_16x16x32_bf16 v[156:159], a[68:71], a[124:127], v[156:159]\n"
"v_accvgpr_read_b32 v10, a0\n"
"v_mfma_f32_16x16x32_bf16 v[156:159], a[64:67], a[120:123], v[156:159]\n"
"s_waitcnt lgkmcnt(5)\n"
"v_accvgpr_read_b32 v11, a1\n"
"v_mul_f32 v12, v12, v10\n"
"v_mfma_f32_16x16x32_bf16 v[160:163], a[64:67], a[128:131], v[160:163]\n"
"v_mul_f32 v13, v13, v11\n"
"v_mfma_f32_16x16x32_bf16 v[160:163], a[68:71], a[132:135], v[160:163]\n"
"global_load_dwordx4 a[56:59], v0, s[34:35] offset:2048 sc1\n"
"v_accvgpr_read_b32 v10, a2\n"
"v_mfma_f32_16x16x32_bf16 v[188:191], a[76:79], a[132:135], v[188:191]\n"
"v_accvgpr_read_b32 v11, a3\n"
"v_mul_f32 v14, v14, v10\n"
"v_mfma_f32_16x16x32_bf16 v[188:191], a[72:75], a[128:131], v[188:191]\n"
"v_mul_f32 v15, v15, v11\n"
"v_mfma_f32_16x16x32_bf16 v[192:195], a[72:75], a[136:139], v[192:195]\n"
"v_cvt_pk_bf16_f32 v24, v12, v13\n"
"v_mfma_f32_16x16x32_bf16 v[192:195], a[76:79], a[140:143], v[192:195]\n"
"v_cvt_pk_bf16_f32 v25, v14, v15\n"
"v_lshlrev_b32 v12, 16, v18\n"
"v_mfma_f32_16x16x32_bf16 v[164:167], a[68:71], a[140:143], v[164:167]\n"
"v_and_b32 v13, s63, v18\n"
"v_mfma_f32_16x16x32_bf16 v[164:167], a[64:67], a[136:139], v[164:167]\n"
"s_waitcnt lgkmcnt(1)\n"
"v_lshlrev_b32 v14, 16, v19\n"
"v_mfma_f32_16x16x32_bf16 v[168:171], a[64:67], a[144:147], v[168:171]\n"
"v_and_b32 v15, s63, v19\n"
"v_accvgpr_read_b32 v10, a4\n"
"v_mfma_f32_16x16x32_bf16 v[168:171], a[68:71], a[148:151], v[168:171]\n"
"global_load_dwordx4 a[60:63], v0, s[34:35] offset:3072 sc1\n"
"v_accvgpr_read_b32 v11, a5\n"
"v_mfma_f32_16x16x32_bf16 v[196:199], a[76:79], a[148:151], v[196:199]\n"
"v_mul_f32 v12, v12, v10\n"
"v_mfma_f32_16x16x32_bf16 v[196:199], a[72:75], a[144:147], v[196:199]\n"
"v_mul_f32 v13, v13, v11\n"
"v_accvgpr_read_b32 v10, a6\n"
"v_mfma_f32_16x16x32_bf16 a[20:23], a[72:75], a[152:155], a[20:23]\n"
"s_add_u32 s34, s34, 8192\n"
"v_accvgpr_read_b32 v11, a7\n"
"v_mfma_f32_16x16x32_bf16 a[20:23], a[76:79], a[156:159], a[20:23]\n"
"v_mul_f32 v14, v14, v10\n"
"v_mul_f32 v15, v15, v11\n"
"v_mfma_f32_16x16x32_bf16 a[16:19], a[68:71], a[156:159], a[16:19]\n"
"v_cvt_pk_bf16_f32 v26, v12, v13\n"
"v_cvt_pk_bf16_f32 v27, v14, v15\n"
"v_mfma_f32_16x16x32_bf16 a[16:19], a[64:67], a[152:155], a[16:19]\n"
"s_waitcnt vmcnt(10)\n"
"global_store_dwordx4 v0, v[24:27], s[54:55] offset:2048 sc1\n"
"ds_write_b128 v2, v[24:27] offset:14336\n"
"v_mfma_f32_16x16x32_bf16 v[200:203], a[80:83], a[96:99], v[200:203]\n"
"v_accvgpr_read_b32 v16, a188\n"
"v_mfma_f32_16x16x32_bf16 v[200:203], a[84:87], a[100:103], v[200:203]\n"
"global_load_dwordx4 a[64:67], v0, s[34:35] offset:-4096 sc1\n"
"v_accvgpr_read_b32 v17, a189\n"
"v_mfma_f32_16x16x32_bf16 v[228:231], a[92:95], a[100:103], v[228:231]\n"
"ds_read_b128 a[100:103], v2 offset:1024\n"
"v_accvgpr_read_b32 v18, a190\n"
"v_mfma_f32_16x16x32_bf16 v[228:231], a[88:91], a[96:99], v[228:231]\n"
"ds_read_b128 a[96:99], v2 offset:0\n"
"v_accvgpr_read_b32 v19, a191\n"
"v_mfma_f32_16x16x32_bf16 v[232:235], a[88:91], a[104:107], v[232:235]\n"
"v_lshlrev_b32 v12, 16, v16\n"
"v_and_b32 v13, s63, v16\n"
"v_mfma_f32_16x16x32_bf16 v[232:235], a[92:95], a[108:111], v[232:235]\n"
"v_lshlrev_b32 v14, 16, v17\n"
"v_and_b32 v15, s63, v17\n"
"v_mfma_f32_16x16x32_bf16 v[204:207], a[84:87], a[108:111], v[204:207]\n"
"ds_read_b128 a[108:111], v2 offset:3072\n"
"v_accvgpr_read_b32 v10, a8\n"
"v_mfma_f32_16x16x32_bf16 v[204:207], a[80:83], a[104:107], v[204:207]\n"
"ds_read_b128 a[104:107], v2 offset:2048\n"
"v_accvgpr_read_b32 v11, a9\n"
"v_mfma_f32_16x16x32_bf16 v[208:211], a[80:83], a[112:115], v[208:211]\n"
"v_mul_f32 v12, v12, v10\n"
"v_mul_f32 v13, v13, v11\n"
"v_mfma_f32_16x16x32_bf16 v[208:211], a[84:87], a[116:119], v[208:211]\n"
"global_load_dwordx4 a[68:71], v0, s[34:35] offset:-3072 sc1\n"
"v_accvgpr_read_b32 v10, a10\n"
"v_mfma_f32_16x16x32_bf16 v[236:239], a[92:95], a[116:119], v[236:239]\n"
"ds_read_b128 a[116:119], v2 offset:5120\n"
"v_accvgpr_read_b32 v11, a11\n"
"v_mfma_f32_16x16x32_bf16 v[236:239], a[88:91], a[112:115], v[236:239]\n"
"ds_read_b128 a[112:115], v2 offset:4096\n"
"v_mul_f32 v14, v14, v10\n"
"v_mfma_f32_16x16x32_bf16 v[240:243], a[88:91], a[120:123], v[240:243]\n"
"v_mul_f32 v15, v15, v11\n"
"v_cvt_pk_bf16_f32 v28, v12, v13\n"
"v_mfma_f32_16x16x32_bf16 v[240:243], a[92:95], a[124:127], v[240:243]\n"
"v_cvt_pk_bf16_f32 v29, v14, v15\n"
"v_lshlrev_b32 v12, 16, v18\n"
"v_mfma_f32_16x16x32_bf16 v[212:215], a[84:87], a[124:127], v[212:215]\n"
"ds_read_b128 a[124:127], v2 offset:7168\n"
"v_and_b32 v13, s63, v18\n"
"v_mfma_f32_16x16x32_bf16 v[212:215], a[80:83], a[120:123], v[212:215]\n"
"ds_read_b128 a[120:123], v2 offset:6144\n"
"v_lshlrev_b32 v14, 16, v19\n"
"v_mfma_f32_16x16x32_bf16 v[216:219], a[80:83], a[128:131], v[216:219]\n"
"v_and_b32 v15, s63, v19\n"
"v_accvgpr_read_b32 v10, a12\n"
"v_mfma_f32_16x16x32_bf16 v[216:219], a[84:87], a[132:135], v[216:219]\n"
"global_load_dwordx4 a[72:75], v0, s[34:35] offset:-2048 sc1\n"
"v_accvgpr_read_b32 v11, a13\n"
"v_mfma_f32_16x16x32_bf16 v[244:247], a[92:95], a[132:135], v[244:247]\n"
"ds_read_b128 a[132:135], v2 offset:9216\n"
"v_mul_f32 v12, v12, v10\n"
"v_mfma_f32_16x16x32_bf16 v[244:247], a[88:91], a[128:131], v[244:247]\n"
"ds_read_b128 a[128:131], v2 offset:8192\n"
"v_mul_f32 v13, v13, v11\n"
"v_mfma_f32_16x16x32_bf16 v[248:251], a[88:91], a[136:139], v[248:251]\n"
"v_accvgpr_read_b32 v10, a14\n"
"v_accvgpr_read_b32 v11, a15\n"
"v_mfma_f32_16x16x32_bf16 v[248:251], a[92:95], a[140:143], v[248:251]\n"
"v_mul_f32 v14, v14, v10\n"
"v_mul_f32 v15, v15, v11\n"
"v_mfma_f32_16x16x32_bf16 v[220:223], a[84:87], a[140:143], v[220:223]\n"
"ds_read_b128 a[140:143], v2 offset:11264\n"
"v_cvt_pk_bf16_f32 v30, v12, v13\n"
"v_mfma_f32_16x16x32_bf16 v[220:223], a[80:83], a[136:139], v[220:223]\n"
"ds_read_b128 a[136:139], v2 offset:10240\n"
"v_cvt_pk_bf16_f32 v31, v14, v15\n"
"v_mfma_f32_16x16x32_bf16 v[224:227], a[80:83], a[144:147], v[224:227]\n"
"global_store_dwordx4 v0, v[28:31], s[54:55] offset:3072 sc1\n"
"ds_write_b128 v2, v[28:31] offset:15360\n"
"v_mfma_f32_16x16x32_bf16 v[224:227], a[84:87], a[148:151], v[224:227]\n"
"global_load_dwordx4 a[76:79], v0, s[34:35] offset:-1024 sc1\n"
"v_mfma_f32_16x16x32_bf16 v[252:255], a[92:95], a[148:151], v[252:255]\n"
"ds_read_b128 a[148:151], v2 offset:13312\n"
"v_mfma_f32_16x16x32_bf16 v[252:255], a[88:91], a[144:147], v[252:255]\n"
"ds_read_b128 a[144:147], v2 offset:12288\n"
"v_mfma_f32_16x16x32_bf16 a[28:31], a[88:91], a[152:155], a[28:31]\n"
"s_add_u32 s62, s62, 1\n"
"v_mfma_f32_16x16x32_bf16 a[28:31], a[92:95], a[156:159], a[28:31]\n"
"v_mfma_f32_16x16x32_bf16 a[24:27], a[84:87], a[156:159], a[24:27]\n"
"ds_read_b128 a[156:159], v2 offset:15360\n"
"v_mfma_f32_16x16x32_bf16 a[24:27], a[80:83], a[152:155], a[24:27]\n"
"ds_read_b128 a[152:155], v2 offset:14336\n"
"L_layer:\n"
"s_waitcnt vmcnt(11) lgkmcnt(13)\n"
"v_mfma_f32_16x16x32_bf16 v[32:35], a[32:35], a[96:99], v[32:35]\n"
"s_mov_b64 s[96:97], s[52:53]\n"
"v_accvgpr_read_b32 v16, a192\n"
"v_accvgpr_read_b32 v17, a193\n"
"v_mfma_f32_16x16x32_bf16 v[32:35], a[36:39], a[100:103], v[32:35]\n"
"global_load_dwordx4 a[80:83], v0, s[34:35] offset:0 sc1\n"
"v_accvgpr_read_b32 v18, a194\n"
"v_mfma_f32_16x16x32_bf16 v[60:63], a[44:47], a[100:103], v[60:63]\n"
"s_mov_b64 s[98:99], s[54:55]\n"
"v_accvgpr_read_b32 v19, a195\n"
"v_mfma_f32_16x16x32_bf16 v[60:63], a[40:43], a[96:99], v[60:63]\n"
"v_lshlrev_b32 v12, 16, v16\n"
"v_and_b32 v13, s63, v16\n"
"v_mfma_f32_16x16x32_bf16 v[64:67], a[40:43], a[104:107], v[64:67]\n"
"s_add_u32 s52, s52, 131072\n"
"s_addc_u32 s53, s53, 0\n"
"v_lshlrev_b32 v14, 16, v17\n"
"v_mfma_f32_16x16x32_bf16 v[64:67], a[44:47], a[108:111], v[64:67]\n"
"v_and_b32 v15, s63, v17\n"
"v_mul_f32 v12, v12, v144\n"
"v_mfma_f32_16x16x32_bf16 v[36:39], a[36:39], a[108:111], v[36:39]\n"
"s_add_u32 s54, s54, 131072\n"
"s_addc_u32 s55, s55, 0\n"
"v_mul_f32 v13, v13, v145\n"
"v_mfma_f32_16x16x32_bf16 v[36:39], a[32:35], a[104:107], v[36:39]\n"
"s_waitcnt lgkmcnt(9)\n"
"v_mul_f32 v14, v14, v146\n"
"v_mul_f32 v15, v15, v147\n"
"v_mfma_f32_16x16x32_bf16 v[40:43], a[32:35], a[112:115], v[40:43]\n"
"ds_read_b128 v[20:23], v2 offset:0\n"
"v_cvt_pk_bf16_f32 v24, v12, v13\n"
"v_mfma_f32_16x16x32_bf16 v[40:43], a[36:39], a[116:119], v[40:43]\n"
"global_load_dwordx4 a[84:87], v0, s[34:35] offset:1024 sc1\n"
"v_cvt_pk_bf16_f32 v25, v14, v15\n"
"v_mfma_f32_16x16x32_bf16 v[68:71], a[44:47], a[116:119], v[68:71]\n"
"ds_write_b64 v2, v[24:25] offset:16384\n"
"v_mfma_f32_16x16x32_bf16 v[68:71], a[40:43], a[112:115], v[68:71]\n"
"v_lshlrev_b32 v12, 16, v18\n"
"v_and_b32 v13, s63, v18\n"
"v_mfma_f32_16x16x32_bf16 v[72:75], a[40:43], a[120:123], v[72:75]\n"
"s_waitcnt lgkmcnt(6)\n"
"s_barrier\n"
"v_mfma_f32_16x16x32_bf16 v[72:75], a[44:47], a[124:127], v[72:75]\n"
"v_lshlrev_b32 v14, 16, v19\n"
"v_and_b32 v15, s63, v19\n"
"v_mfma_f32_16x16x32_bf16 v[44:47], a[36:39], a[124:127], v[44:47]\n"
"v_mul_f32 v12, v12, v172\n"
"v_mul_f32 v13, v13, v173\n"
"v_mfma_f32_16x16x32_bf16 v[44:47], a[32:35], a[120:123], v[44:47]\n"
"v_mul_f32 v14, v14, v174\n"
"v_mfma_f32_16x16x32_bf16 v[48:51], a[32:35], a[128:131], v[48:51]\n"
"v_mul_f32 v15, v15, v175\n"
"v_mfma_f32_16x16x32_bf16 v[48:51], a[36:39], a[132:135], v[48:51]\n"
"global_load_dwordx4 a[88:91], v0, s[34:35] offset:2048 sc1\n"
"v_mfma_f32_16x16x32_bf16 v[76:79], a[44:47], a[132:135], v[76:79]\n"
"v_cvt_pk_bf16_f32 v26, v12, v13\n"
"v_mfma_f32_16x16x32_bf16 v[76:79], a[40:43], a[128:131], v[76:79]\n"
"v_cvt_pk_bf16_f32 v27, v14, v15\n"
"v_mfma_f32_16x16x32_bf16 v[80:83], a[40:43], a[136:139], v[80:83]\n"
"s_waitcnt lgkmcnt(1)\n"
"global_store_dwordx4 v0, v[20:23], s[96:97] offset:-4096 sc1\n"
"ds_write_b64 v2, v[26:27] offset:16392\n"
"v_accvgpr_read_b32 v16, a196\n"
"v_mfma_f32_16x16x32_bf16 v[80:83], a[44:47], a[140:143], v[80:83]\n"
"v_accvgpr_read_b32 v17, a197\n"
"v_accvgpr_read_b32 v18, a198\n"
"v_mfma_f32_16x16x32_bf16 v[52:55], a[36:39], a[140:143], v[52:55]\n"
"v_accvgpr_read_b32 v19, a199\n"
"v_lshlrev_b32 v12, 16, v16\n"
"v_mfma_f32_16x16x32_bf16 v[52:55], a[32:35], a[136:139], v[52:55]\n"
"v_and_b32 v13, s63, v16\n"
"v_lshlrev_b32 v14, 16, v17\n"
"v_mfma_f32_16x16x32_bf16 v[56:59], a[32:35], a[144:147], v[56:59]\n"
"v_and_b32 v15, s63, v17\n"
"v_mul_f32 v12, v12, v200\n"
"v_mfma_f32_16x16x32_bf16 v[56:59], a[36:39], a[148:151], v[56:59]\n"
"global_load_dwordx4 a[92:95], v0, s[34:35] offset:3072 sc1\n"
"v_mul_f32 v13, v13, v201\n"
"v_mfma_f32_16x16x32_bf16 v[84:87], a[44:47], a[148:151], v[84:87]\n"
"v_mul_f32 v14, v14, v202\n"
"v_mul_f32 v15, v15, v203\n"
"v_mfma_f32_16x16x32_bf16 v[84:87], a[40:43], a[144:147], v[84:87]\n"
"v_cvt_pk_bf16_f32 v28, v12, v13\n"
"v_cvt_pk_bf16_f32 v29, v14, v15\n"
"v_mfma_f32_16x16x32_bf16 a[4:7], a[40:43], a[152:155], a[4:7]\n"
"s_add_u32 s34, s34, 8192\n"
"ds_write_b64 v2, v[28:29] offset:17408\n"
"v_mfma_f32_16x16x32_bf16 a[4:7], a[44:47], a[156:159], a[4:7]\n"
"v_lshlrev_b32 v12, 16, v18\n"
"v_mfma_f32_16x16x32_bf16 a[0:3], a[36:39], a[156:159], a[0:3]\n"
"v_and_b32 v13, s63, v18\n"
"v_lshlrev_b32 v14, 16, v19\n"
"v_mfma_f32_16x16x32_bf16 a[0:3], a[32:35], a[152:155], a[0:3]\n"
"s_waitcnt vmcnt(11)\n"
"v_and_b32 v15, s63, v19\n"
"v_mul_f32 v12, v12, v228\n"
"v_mfma_f32_16x16x32_bf16 v[88:91], a[48:51], a[96:99], v[88:91]\n"
"v_mul_f32 v13, v13, v229\n"
"v_mul_f32 v14, v14, v230\n"
"v_mfma_f32_16x16x32_bf16 v[88:91], a[52:55], a[100:103], v[88:91]\n"
"global_load_dwordx4 a[32:35], v0, s[34:35] offset:-4096 sc1\n"
"v_mul_f32 v15, v15, v231\n"
"v_mfma_f32_16x16x32_bf16 v[116:119], a[60:63], a[100:103], v[116:119]\n"
"ds_read_b128 a[100:103], v3 offset:1024\n"
"v_cvt_pk_bf16_f32 v30, v12, v13\n"
"v_mfma_f32_16x16x32_bf16 v[116:119], a[56:59], a[96:99], v[116:119]\n"
"ds_read_b128 a[96:99], v3 offset:0\n"
"v_cvt_pk_bf16_f32 v31, v14, v15\n"
"v_mfma_f32_16x16x32_bf16 v[120:123], a[56:59], a[104:107], v[120:123]\n"
"ds_write_b64 v2, v[30:31] offset:17416\n"
"v_mfma_f32_16x16x32_bf16 v[120:123], a[60:63], a[108:111], v[120:123]\n"
"ds_read_b128 v[20:23], v2 offset:3072\n"
"v_accvgpr_read_b32 v16, a200\n"
"v_mfma_f32_16x16x32_bf16 v[92:95], a[52:55], a[108:111], v[92:95]\n"
"ds_read_b128 a[108:111], v3 offset:3072\n"
"v_accvgpr_read_b32 v17, a201\n"
"v_mfma_f32_16x16x32_bf16 v[92:95], a[48:51], a[104:107], v[92:95]\n"
"ds_read_b128 a[104:107], v3 offset:2048\n"
"v_accvgpr_read_b32 v18, a202\n"
"v_mfma_f32_16x16x32_bf16 v[96:99], a[48:51], a[112:115], v[96:99]\n"
"v_accvgpr_read_b32 v19, a203\n"
"v_lshlrev_b32 v12, 16, v16\n"
"v_mfma_f32_16x16x32_bf16 v[96:99], a[52:55], a[116:119], v[96:99]\n"
"global_load_dwordx4 a[36:39], v0, s[34:35] offset:-3072 sc1\n"
"v_and_b32 v13, s63, v16\n"
"v_mfma_f32_16x16x32_bf16 v[124:127], a[60:63], a[116:119], v[124:127]\n"
"ds_read_b128 a[116:119], v3 offset:5120\n"
"v_lshlrev_b32 v14, 16, v17\n"
"v_mfma_f32_16x16x32_bf16 v[124:127], a[56:59], a[112:115], v[124:127]\n"
"ds_read_b128 a[112:115], v3 offset:4096\n"
"v_and_b32 v15, s63, v17\n"
"v_mfma_f32_16x16x32_bf16 v[128:131], a[56:59], a[120:123], v[128:131]\n"
"v_mul_f32 v12, v12, v148\n"
"v_mul_f32 v13, v13, v149\n"
"v_mfma_f32_16x16x32_bf16 v[128:131], a[60:63], a[124:127], v[128:131]\n"
"v_mul_f32 v14, v14, v150\n"
"v_mul_f32 v15, v15, v151\n"
"v_mfma_f32_16x16x32_bf16 v[100:103], a[52:55], a[124:127], v[100:103]\n"
"ds_read_b128 a[124:127], v3 offset:7168\n"
"v_cvt_pk_bf16_f32 v24, v12, v13\n"
"v_mfma_f32_16x16x32_bf16 v[100:103], a[48:51], a[120:123], v[100:103]\n"
"ds_read_b128 a[120:123], v3 offset:6144\n"
"v_cvt_pk_bf16_f32 v25, v14, v15\n"
"v_mfma_f32_16x16x32_bf16 v[104:107], a[48:51], a[128:131], v[104:107]\n"
"s_waitcnt lgkmcnt(6)\n"
"global_store_dwordx4 v0, v[20:23], s[96:97] offset:-1024 sc1\n"
"ds_write_b64 v2, v[24:25] offset:18432\n"
"v_lshlrev_b32 v12, 16, v18\n"
"v_mfma_f32_16x16x32_bf16 v[104:107], a[52:55], a[132:135], v[104:107]\n"
"global_load_dwordx4 a[40:43], v0, s[34:35] offset:-2048 sc1\n"
"v_and_b32 v13, s63, v18\n"
"v_mfma_f32_16x16x32_bf16 v[132:135], a[60:63], a[132:135], v[132:135]\n"
"ds_read_b128 a[132:135], v3 offset:9216\n"
"v_lshlrev_b32 v14, 16, v19\n"
"v_mfma_f32_16x16x32_bf16 v[132:135], a[56:59], a[128:131], v[132:135]\n"
"ds_read_b128 a[128:131], v3 offset:8192\n"
"v_and_b32 v15, s63, v19\n"
"v_mfma_f32_16x16x32_bf16 v[136:139], a[56:59], a[136:139], v[136:139]\n"
"v_mul_f32 v12, v12, v176\n"
"v_mul_f32 v13, v13, v177\n"
"v_mfma_f32_16x16x32_bf16 v[136:139], a[60:63], a[140:143], v[136:139]\n"
"v_mul_f32 v14, v14, v178\n"
"v_mul_f32 v15, v15, v179\n"
"v_mfma_f32_16x16x32_bf16 v[108:111], a[52:55], a[140:143], v[108:111]\n"
"ds_read_b128 a[140:143], v3 offset:11264\n"
"v_cvt_pk_bf16_f32 v26, v12, v13\n"
"v_mfma_f32_16x16x32_bf16 v[108:111], a[48:51], a[136:139], v[108:111]\n"
"ds_read_b128 a[136:139], v3 offset:10240\n"
"v_cvt_pk_bf16_f32 v27, v14, v15\n"
"v_mfma_f32_16x16x32_bf16 v[112:115], a[48:51], a[144:147], v[112:115]\n"
"ds_write_b64 v2, v[26:27] offset:18440\n"
"v_mfma_f32_16x16x32_bf16 v[112:115], a[52:55], a[148:151], v[112:115]\n"
"global_load_dwordx4 a[44:47], v0, s[34:35] offset:-1024 sc1\n"
"v_accvgpr_read_b32 v16, a204\n"
"v_mfma_f32_16x16x32_bf16 v[140:143], a[60:63], a[148:151], v[140:143]\n"
"ds_read_b128 a[148:151], v3 offset:13312\n"
"v_accvgpr_read_b32 v17, a205\n"
"v_mfma_f32_16x16x32_bf16 v[140:143], a[56:59], a[144:147], v[140:143]\n"
"ds_read_b128 a[144:147], v3 offset:12288\n"
"v_accvgpr_read_b32 v18, a206\n"
"v_mfma_f32_16x16x32_bf16 a[12:15], a[56:59], a[152:155], a[12:15]\n"
"v_accvgpr_read_b32 v19, a207\n"
"v_lshlrev_b32 v12, 16, v16\n"
"v_mfma_f32_16x16x32_bf16 a[12:15], a[60:63], a[156:159], a[12:15]\n"
"v_and_b32 v13, s63, v16\n"
"v_lshlrev_b32 v14, 16, v17\n"
"v_mfma_f32_16x16x32_bf16 a[8:11], a[52:55], a[156:159], a[8:11]\n"
"L_st5092:\n"
"s_cbranch_execz L_st7379\n"
"ds_read_b128 a[156:159], v3 offset:15360\n"
"v_and_b32 v15, s63, v17\n"
"v_mfma_f32_16x16x32_bf16 a[8:11], a[48:51], a[152:155], a[8:11]\n"
"s_waitcnt vmcnt(10) lgkmcnt(13)\n"
"ds_read_b128 a[152:155], v3 offset:14336\n"
"v_mul_f32 v12, v12, v204\n"
"v_mfma_f32_16x16x32_bf16 v[32:35], a[64:67], a[96:99], v[32:35]\n"
"ds_read_b128 v[20:23], v2 offset:6144\n"
"v_mul_f32 v13, v13, v205\n"
"v_mfma_f32_16x16x32_bf16 v[32:35], a[68:71], a[100:103], v[32:35]\n"
"global_load_dwordx4 a[48:51], v0, s[34:35] offset:0 sc1\n"
"v_mul_f32 v14, v14, v206\n"
"v_mfma_f32_16x16x32_bf16 v[60:63], a[76:79], a[100:103], v[60:63]\n"
"v_mul_f32 v15, v15, v207\n"
"v_cvt_pk_bf16_f32 v28, v12, v13\n"
"v_mfma_f32_16x16x32_bf16 v[60:63], a[72:75], a[96:99], v[60:63]\n"
"v_cvt_pk_bf16_f32 v29, v14, v15\n"
"v_mfma_f32_16x16x32_bf16 v[64:67], a[72:75], a[104:107], v[64:67]\n"
"ds_write_b64 v2, v[28:29] offset:19456\n"
"v_mfma_f32_16x16x32_bf16 v[64:67], a[76:79], a[108:111], v[64:67]\n"
"v_lshlrev_b32 v12, 16, v18\n"
"v_and_b32 v13, s63, v18\n"
"v_mfma_f32_16x16x32_bf16 v[36:39], a[68:71], a[108:111], v[36:39]\n"
"v_lshlrev_b32 v14, 16, v19\n"
"v_and_b32 v15, s63, v19\n"
"v_mfma_f32_16x16x32_bf16 v[36:39], a[64:67], a[104:107], v[36:39]\n"
"s_waitcnt lgkmcnt(12)\n"
"v_mul_f32 v12, v12, v232\n"
"v_mul_f32 v13, v13, v233\n"
"v_mfma_f32_16x16x32_bf16 v[40:43], a[64:67], a[112:115], v[40:43]\n"
"v_mul_f32 v14, v14, v234\n"
"v_mfma_f32_16x16x32_bf16 v[40:43], a[68:71], a[116:119], v[40:43]\n"
"global_load_dwordx4 a[52:55], v0, s[34:35] offset:1024 sc1\n"
"v_mul_f32 v15, v15, v235\n"
"v_mfma_f32_16x16x32_bf16 v[68:71], a[76:79], a[116:119], v[68:71]\n"
"v_cvt_pk_bf16_f32 v30, v12, v13\n"
"v_mfma_f32_16x16x32_bf16 v[68:71], a[72:75], a[112:115], v[68:71]\n"
"v_cvt_pk_bf16_f32 v31, v14, v15\n"
"v_mfma_f32_16x16x32_bf16 v[72:75], a[72:75], a[120:123], v[72:75]\n"
"s_waitcnt lgkmcnt(1)\n"
"global_store_dwordx4 v0, v[20:23], s[96:97] offset:2048 sc1\n"
"ds_write_b64 v2, v[30:31] offset:19464\n"
"v_accvgpr_read_b32 v16, a208\n"
"v_mfma_f32_16x16x32_bf16 v[72:75], a[76:79], a[124:127], v[72:75]\n"
"v_accvgpr_read_b32 v17, a209\n"
"v_accvgpr_read_b32 v18, a210\n"
"v_mfma_f32_16x16x32_bf16 v[44:47], a[68:71], a[124:127], v[44:47]\n"
"v_accvgpr_read_b32 v19, a211\n"
"v_mfma_f32_16x16x32_bf16 v[44:47], a[64:67], a[120:123], v[44:47]\n"
"v_lshlrev_b32 v12, 16, v16\n"
"v_and_b32 v13, s63, v16\n"
"v_mfma_f32_16x16x32_bf16 v[48:51], a[64:67], a[128:131], v[48:51]\n"
"v_lshlrev_b32 v14, 16, v17\n"
"v_mfma_f32_16x16x32_bf16 v[48:51], a[68:71], a[132:135], v[48:51]\n"
"global_load_dwordx4 a[56:59], v0, s[34:35] offset:2048 sc1\n"
"v_and_b32 v15, s63, v17\n"
"v_mfma_f32_16x16x32_bf16 v[76:79], a[76:79], a[132:135], v[76:79]\n"
"v_mul_f32 v12, v12, v152\n"
"v_mul_f32 v13, v13, v153\n"
"v_mfma_f32_16x16x32_bf16 v[76:79], a[72:75], a[128:131], v[76:79]\n"
"v_mul_f32 v14, v14, v154\n"
"v_mul_f32 v15, v15, v155\n"
"v_mfma_f32_16x16x32_bf16 v[80:83], a[72:75], a[136:139], v[80:83]\n"
"v_cvt_pk_bf16_f32 v24, v12, v13\n"
"v_cvt_pk_bf16_f32 v25, v14, v15\n"
"v_mfma_f32_16x16x32_bf16 v[80:83], a[76:79], a[140:143], v[80:83]\n"
"ds_write_b64 v2, v[24:25] offset:20480\n"
"v_mfma_f32_16x16x32_bf16 v[52:55], a[68:71], a[140:143], v[52:55]\n"
"v_lshlrev_b32 v12, 16, v18\n"
"v_mfma_f32_16x16x32_bf16 v[52:55], a[64:67], a[136:139], v[52:55]\n"
"v_and_b32 v13, s63, v18\n"
"v_lshlrev_b32 v14, 16, v19\n"
"v_mfma_f32_16x16x32_bf16 v[56:59], a[64:67], a[144:147], v[56:59]\n"
"v_and_b32 v15, s63, v19\n"
"v_mul_f32 v12, v12, v180\n"
"v_mfma_f32_16x16x32_bf16 v[56:59], a[68:71], a[148:151], v[56:59]\n"
"global_load_dwordx4 a[60:63], v0, s[34:35] offset:3072 sc1\n"
"v_mul_f32 v13, v13, v181\n"
"v_mfma_f32_16x16x32_bf16 v[84:87], a[76:79], a[148:151], v[84:87]\n"
"v_mul_f32 v14, v14, v182\n"
"v_mul_f32 v15, v15, v183\n"
"v_mfma_f32_16x16x32_bf16 v[84:87], a[72:75], a[144:147], v[84:87]\n"
"v_cvt_pk_bf16_f32 v26, v12, v13\n"
"v_cvt_pk_bf16_f32 v27, v14, v15\n"
"v_mfma_f32_16x16x32_bf16 a[4:7], a[72:75], a[152:155], a[4:7]\n"
"s_add_u32 s34, s34, 8192\n"
"ds_write_b64 v2, v[26:27] offset:20488\n"
"v_mfma_f32_16x16x32_bf16 a[4:7], a[76:79], a[156:159], a[4:7]\n"
"ds_read_b128 v[20:23], v2 offset:9216\n"
"v_mfma_f32_16x16x32_bf16 a[0:3], a[68:71], a[156:159], a[0:3]\n"
"v_accvgpr_read_b32 v16, a212\n"
"v_mfma_f32_16x16x32_bf16 a[0:3], a[64:67], a[152:155], a[0:3]\n"
"s_waitcnt vmcnt(10)\n"
"v_accvgpr_read_b32 v17, a213\n"
"v_accvgpr_read_b32 v18, a214\n"
"v_mfma_f32_16x16x32_bf16 v[88:91], a[80:83], a[96:99], v[88:91]\n"
"v_accvgpr_read_b32 v19, a215\n"
"v_lshlrev_b32 v12, 16, v16\n"
"v_mfma_f32_16x16x32_bf16 v[88:91], a[84:87], a[100:103], v[88:91]\n"
"global_load_dwordx4 a[64:67], v0, s[34:35] offset:-4096 sc1\n"
"v_and_b32 v13, s63, v16\n"
"v_mfma_f32_16x16x32_bf16 v[116:119], a[92:95], a[100:103], v[116:119]\n"
"ds_read_b128 a[100:103], v4 offset:1024\n"
"v_lshlrev_b32 v14, 16, v17\n"
"v_mfma_f32_16x16x32_bf16 v[116:119], a[88:91], a[96:99], v[116:119]\n"
"ds_read_b128 a[96:99], v4 offset:0\n"
"v_and_b32 v15, s63, v17\n"
"v_mfma_f32_16x16x32_bf16 v[120:123], a[88:91], a[104:107], v[120:123]\n"
"v_mul_f32 v12, v12, v208\n"
"v_mul_f32 v13, v13, v209\n"
"v_mfma_f32_16x16x32_bf16 v[120:123], a[92:95], a[108:111], v[120:123]\n"
"v_mul_f32 v14, v14, v210\n"
"v_mul_f32 v15, v15, v211\n"
"v_mfma_f32_16x16x32_bf16 v[92:95], a[84:87], a[108:111], v[92:95]\n"
"ds_read_b128 a[108:111], v4 offset:3072\n"
"v_cvt_pk_bf16_f32 v28, v12, v13\n"
"v_mfma_f32_16x16x32_bf16 v[92:95], a[80:83], a[104:107], v[92:95]\n"
"ds_read_b128 a[104:107], v4 offset:2048\n"
"v_cvt_pk_bf16_f32 v29, v14, v15\n"
"v_mfma_f32_16x16x32_bf16 v[96:99], a[80:83], a[112:115], v[96:99]\n"
"s_waitcnt lgkmcnt(4)\n"
"global_store_dwordx4 v0, v[20:23], s[98:99] offset:-3072 sc1\n"
"ds_write_b64 v2, v[28:29] offset:21504\n"
"v_lshlrev_b32 v12, 16, v18\n"
"v_mfma_f32_16x16x32_bf16 v[96:99], a[84:87], a[116:119], v[96:99]\n"
"global_load_dwordx4 a[68:71], v0, s[34:35] offset:-3072 sc1\n"
"v_and_b32 v13, s63, v18\n"
"v_mfma_f32_16x16x32_bf16 v[124:127], a[92:95], a[116:119], v[124:127]\n"
"ds_read_b128 a[116:119], v4 offset:5120\n"
"v_lshlrev_b32 v14, 16, v19\n"
"v_mfma_f32_16x16x32_bf16 v[124:127], a[88:91], a[112:115], v[124:127]\n"
"ds_read_b128 a[112:115], v4 offset:4096\n"
"v_and_b32 v15, s63, v19\n"
"v_mfma_f32_16x16x32_bf16 v[128:131], a[88:91], a[120:123], v[128:131]\n"
"v_mul_f32 v12, v12, v236\n"
"v_mul_f32 v13, v13, v237\n"
"v_mfma_f32_16x16x32_bf16 v[128:131], a[92:95], a[124:127], v[128:131]\n"
"v_mul_f32 v14, v14, v238\n"
"v_mul_f32 v15, v15, v239\n"
"v_mfma_f32_16x16x32_bf16 v[100:103], a[84:87], a[124:127], v[100:103]\n"
"ds_read_b128 a[124:127], v4 offset:7168\n"
"v_cvt_pk_bf16_f32 v30, v12, v13\n"
"v_mfma_f32_16x16x32_bf16 v[100:103], a[80:83], a[120:123], v[100:103]\n"
"ds_read_b128 a[120:123], v4 offset:6144\n"
"v_cvt_pk_bf16_f32 v31, v14, v15\n"
"v_mfma_f32_16x16x32_bf16 v[104:107], a[80:83], a[128:131], v[104:107]\n"
"ds_write_b64 v2, v[30:31] offset:21512\n"
"v_mfma_f32_16x16x32_bf16 v[104:107], a[84:87], a[132:135], v[104:107]\n"
"global_load_dwordx4 a[72:75], v0, s[34:35] offset:-2048 sc1\n"
"v_accvgpr_read_b32 v16, a216\n"
"v_mfma_f32_16x16x32_bf16 v[132:135], a[92:95], a[132:135], v[132:135]\n"
"ds_read_b128 a[132:135], v4 offset:9216\n"
"v_accvgpr_read_b32 v17, a217\n"
"v_mfma_f32_16x16x32_bf16 v[132:135], a[88:91], a[128:131], v[132:135]\n"
"ds_read_b128 a[128:131], v4 offset:8192\n"
"v_accvgpr_read_b32 v18, a218\n"
"v_mfma_f32_16x16x32_bf16 v[136:139], a[88:91], a[136:139], v[136:139]\n"
"v_accvgpr_read_b32 v19, a219\n"
"v_mfma_f32_16x16x32_bf16 v[136:139], a[92:95], a[140:143], v[136:139]\n"
"v_lshlrev_b32 v12, 16, v16\n"
"v_and_b32 v13, s63, v16\n"
"v_mfma_f32_16x16x32_bf16 v[108:111], a[84:87], a[140:143], v[108:111]\n"
"ds_read_b128 a[140:143], v4 offset:11264\n"
"v_lshlrev_b32 v14, 16, v17\n"
"v_mfma_f32_16x16x32_bf16 v[108:111], a[80:83], a[136:139], v[108:111]\n"
"ds_read_b128 a[136:139], v4 offset:10240\n"
"v_and_b32 v15, s63, v17\n"
"v_mfma_f32_16x16x32_bf16 v[112:115], a[80:83], a[144:147], v[112:115]\n"
"ds_read_b128 v[20:23], v2 offset:12288\n"
"v_mul_f32 v12, v12, v156\n"
"v_mfma_f32_16x16x32_bf16 v[112:115], a[84:87], a[148:151], v[112:115]\n"
"global_load_dwordx4 a[76:79], v0, s[34:35] offset:-1024 sc1\n"
"v_mfma_f32_16x16x32_bf16 v[140:143], a[92:95], a[148:151], v[140:143]\n"
"ds_read_b128 a[148:151], v4 offset:13312\n"
"v_mfma_f32_16x16x32_bf16 v[140:143], a[88:91], a[144:147], v[140:143]\n"
"ds_read_b128 a[144:147], v4 offset:12288\n"
"v_mfma_f32_16x16x32_bf16 a[12:15], a[88:91], a[152:155], a[12:15]\n"
"v_mul_f32 v13, v13, v157\n"
"v_mfma_f32_16x16x32_bf16 a[12:15], a[92:95], a[156:159], a[12:15]\n"
"v_mul_f32 v14, v14, v158\n"
"v_mfma_f32_16x16x32_bf16 a[8:11], a[84:87], a[156:159], a[8:11]\n"
"ds_read_b128 a[156:159], v4 offset:15360\n"
"v_mfma_f32_16x16x32_bf16 a[8:11], a[80:83], a[152:155], a[8:11]\n"
"s_waitcnt vmcnt(10) lgkmcnt(14)\n"
"ds_read_b128 a[152:155], v4 offset:14336\n"
"v_mfma_f32_16x16x32_bf16 v[32:35], a[32:35], a[96:99], v[32:35]\n"
"v_mul_f32 v15, v15, v159\n"
"v_mfma_f32_16x16x32_bf16 v[32:35], a[36:39], a[100:103], v[32:35]\n"
"global_load_dwordx4 a[80:83], v0, s[34:35] offset:0 sc1\n"
"v_mfma_f32_16x16x32_bf16 v[60:63], a[44:47], a[100:103], v[60:63]\n"
"s_add_u32 s65, s65, 1\n"
"v_cvt_pk_bf16_f32 v24, v12, v13\n"
"v_mfma_f32_16x16x32_bf16 v[60:63], a[40:43], a[96:99], v[60:63]\n"
"v_cvt_pk_bf16_f32 v25, v14, v15\n"
"v_mfma_f32_16x16x32_bf16 v[64:67], a[40:43], a[104:107], v[64:67]\n"
"s_waitcnt lgkmcnt(4)\n"
"global_store_dwordx4 v0, v[20:23], s[98:99] offset:0 sc1\n"
"ds_write_b64 v2, v[24:25] offset:22528\n"
"v_lshlrev_b32 v12, 16, v18\n"
"v_mfma_f32_16x16x32_bf16 v[64:67], a[44:47], a[108:111], v[64:67]\n"
"v_and_b32 v13, s63, v18\n"
"v_lshlrev_b32 v14, 16, v19\n"
"v_mfma_f32_16x16x32_bf16 v[36:39], a[36:39], a[108:111], v[36:39]\n"
"v_and_b32 v15, s63, v19\n"
"v_mul_f32 v12, v12, v184\n"
"v_mfma_f32_16x16x32_bf16 v[36:39], a[32:35], a[104:107], v[36:39]\n"
"v_mul_f32 v13, v13, v185\n"
"v_mul_f32 v14, v14, v186\n"
"v_mfma_f32_16x16x32_bf16 v[40:43], a[32:35], a[112:115], v[40:43]\n"
"v_mul_f32 v15, v15, v187\n"
"v_cvt_pk_bf16_f32 v26, v12, v13\n"
"v_mfma_f32_16x16x32_bf16 v[40:43], a[36:39], a[116:119], v[40:43]\n"
"global_load_dwordx4 a[84:87], v0, s[34:35] offset:1024 sc1\n"
"v_cvt_pk_bf16_f32 v27, v14, v15\n"
"v_mfma_f32_16x16x32_bf16 v[68:71], a[44:47], a[116:119], v[68:71]\n"
"ds_write_b64 v2, v[26:27] offset:22536\n"
"v_mfma_f32_16x16x32_bf16 v[68:71], a[40:43], a[112:115], v[68:71]\n"
"v_accvgpr_read_b32 v16, a220\n"
"v_accvgpr_read_b32 v17, a221\n"
"v_mfma_f32_16x16x32_bf16 v[72:75], a[40:43], a[120:123], v[72:75]\n"
"v_accvgpr_read_b32 v18, a222\n"
"v_accvgpr_read_b32 v19, a223\n"
"v_mfma_f32_16x16x32_bf16 v[72:75], a[44:47], a[124:127], v[72:75]\n"
"v_lshlrev_b32 v12, 16, v16\n"
"v_and_b32 v13, s63, v16\n"
"v_mfma_f32_16x16x32_bf16 v[44:47], a[36:39], a[124:127], v[44:47]\n"
"v_lshlrev_b32 v14, 16, v17\n"
"v_and_b32 v15, s63, v17\n"
"v_mfma_f32_16x16x32_bf16 v[44:47], a[32:35], a[120:123], v[44:47]\n"
"v_mul_f32 v12, v12, v212\n"
"v_mul_f32 v13, v13, v213\n"
"v_mfma_f32_16x16x32_bf16 v[48:51], a[32:35], a[128:131], v[48:51]\n"
"v_mul_f32 v14, v14, v214\n"
"v_mul_f32 v15, v15, v215\n"
"v_mfma_f32_16x16x32_bf16 v[48:51], a[36:39], a[132:135], v[48:51]\n"
"global_load_dwordx4 a[88:91], v0, s[34:35] offset:2048 sc1\n"
"v_cvt_pk_bf16_f32 v28, v12, v13\n"
"v_mfma_f32_16x16x32_bf16 v[76:79], a[44:47], a[132:135], v[76:79]\n"
"v_cvt_pk_bf16_f32 v29, v14, v15\n"
"v_mfma_f32_16x16x32_bf16 v[76:79], a[40:43], a[128:131], v[76:79]\n"
"ds_write_b64 v2, v[28:29] offset:23552\n"
"v_mfma_f32_16x16x32_bf16 v[80:83], a[40:43], a[136:139], v[80:83]\n"
"v_lshlrev_b32 v12, 16, v18\n"
"v_and_b32 v13, s63, v18\n"
"v_mfma_f32_16x16x32_bf16 v[80:83], a[44:47], a[140:143], v[80:83]\n"
"v_lshlrev_b32 v14, 16, v19\n"
"v_and_b32 v15, s63, v19\n"
"v_mfma_f32_16x16x32_bf16 v[52:55], a[36:39], a[140:143], v[52:55]\n"
"v_mul_f32 v12, v12, v240\n"
"v_mul_f32 v13, v13, v241\n"
"v_mfma_f32_16x16x32_bf16 v[52:55], a[32:35], a[136:139], v[52:55]\n"
"s_waitcnt lgkmcnt(3)\n"
"v_mul_f32 v14, v14, v242\n"
"v_mul_f32 v15, v15, v243\n"
"v_mfma_f32_16x16x32_bf16 v[56:59], a[32:35], a[144:147], v[56:59]\n"
"v_cvt_pk_bf16_f32 v30, v12, v13\n"
"v_cvt_pk_bf16_f32 v31, v14, v15\n"
"v_mfma_f32_16x16x32_bf16 v[56:59], a[36:39], a[148:151], v[56:59]\n"
"global_load_dwordx4 a[92:95], v0, s[34:35] offset:3072 sc1\n"
"v_mfma_f32_16x16x32_bf16 v[84:87], a[44:47], a[148:151], v[84:87]\n"
"ds_write_b64 v2, v[30:31] offset:23560\n"
"v_mfma_f32_16x16x32_bf16 v[84:87], a[40:43], a[144:147], v[84:87]\n"
"v_accvgpr_read_b32 v16, a224\n"
"v_accvgpr_read_b32 v17, a225\n"
"v_mfma_f32_16x16x32_bf16 a[4:7], a[40:43], a[152:155], a[4:7]\n"
"s_add_u32 s34, s34, 8192\n"
"v_accvgpr_read_b32 v18, a226\n"
"v_accvgpr_read_b32 v19, a227\n"
"v_mfma_f32_16x16x32_bf16 a[4:7], a[44:47], a[156:159], a[4:7]\n"
"v_lshlrev_b32 v12, 16, v16\n"
"v_and_b32 v13, s63, v16\n"
"v_mfma_f32_16x16x32_bf16 a[0:3], a[36:39], a[156:159], a[0:3]\n"
"v_lshlrev_b32 v14, 16, v17\n"
"v_and_b32 v15, s63, v17\n"
"v_mfma_f32_16x16x32_bf16 a[0:3], a[32:35], a[152:155], a[0:3]\n"
"s_waitcnt vmcnt(10)\n"
"v_mul_f32 v12, v12, v160\n"
"v_mul_f32 v13, v13, v161\n"
"v_mfma_f32_16x16x32_bf16 v[88:91], a[48:51], a[96:99], v[88:91]\n"
"v_mul_f32 v14, v14, v162\n"
"v_mul_f32 v15, v15, v163\n"
"v_mfma_f32_16x16x32_bf16 v[88:91], a[52:55], a[100:103], v[88:91]\n"
"global_load_dwordx4 a[32:35], v0, s[34:35] offset:-4096 sc1\n"
"v_cvt_pk_bf16_f32 v24, v12, v13\n"
"v_mfma_f32_16x16x32_bf16 v[116:119], a[60:63], a[100:103], v[116:119]\n"
"ds_read_b128 a[100:103], v5 offset:1024\n"
"v_cvt_pk_bf16_f32 v25, v14, v15\n"
"v_mfma_f32_16x16x32_bf16 v[116:119], a[56:59], a[96:99], v[116:119]\n"
"ds_read_b128 a[96:99], v5 offset:0\n"
"v_mfma_f32_16x16x32_bf16 v[120:123], a[56:59], a[104:107], v[120:123]\n"
"ds_write_b64 v2, v[24:25] offset:24576\n"
"v_mfma_f32_16x16x32_bf16 v[120:123], a[60:63], a[108:111], v[120:123]\n"
"v_lshlrev_b32 v12, 16, v18\n"
"v_and_b32 v13, s63, v18\n"
"v_mfma_f32_16x16x32_bf16 v[92:95], a[52:55], a[108:111], v[92:95]\n"
"ds_read_b128 a[108:111], v5 offset:3072\n"
"v_lshlrev_b32 v14, 16, v19\n"
"v_mfma_f32_16x16x32_bf16 v[92:95], a[48:51], a[104:107], v[92:95]\n"
"ds_read_b128 a[104:107], v5 offset:2048\n"
"v_and_b32 v15, s63, v19\n"
"v_mfma_f32_16x16x32_bf16 v[96:99], a[48:51], a[112:115], v[96:99]\n"
"v_mul_f32 v12, v12, v188\n"
"v_mul_f32 v13, v13, v189\n"
"v_mfma_f32_16x16x32_bf16 v[96:99], a[52:55], a[116:119], v[96:99]\n"
"global_load_dwordx4 a[36:39], v0, s[34:35] offset:-3072 sc1\n"
"v_mul_f32 v14, v14, v190\n"
"v_mfma_f32_16x16x32_bf16 v[124:127], a[60:63], a[116:119], v[124:127]\n"
"ds_read_b128 a[116:119], v5 offset:5120\n"
"v_mul_f32 v15, v15, v191\n"
"v_mfma_f32_16x16x32_bf16 v[124:127], a[56:59], a[112:115], v[124:127]\n"
"ds_read_b128 a[112:115], v5 offset:4096\n"
"v_cvt_pk_bf16_f32 v26, v12, v13\n"
"v_mfma_f32_16x16x32_bf16 v[128:131], a[56:59], a[120:123], v[128:131]\n"
"v_cvt_pk_bf16_f32 v27, v14, v15\n"
"v_mfma_f32_16x16x32_bf16 v[128:131], a[60:63], a[124:127], v[128:131]\n"
"ds_write_b64 v2, v[26:27] offset:24584\n"
"v_mfma_f32_16x16x32_bf16 v[100:103], a[52:55], a[124:127], v[100:103]\n"
"ds_read_b128 a[124:127], v5 offset:7168\n"
"v_accvgpr_read_b32 v16, a228\n"
"v_mfma_f32_16x16x32_bf16 v[100:103], a[48:51], a[120:123], v[100:103]\n"
"ds_read_b128 a[120:123], v5 offset:6144\n"
"v_accvgpr_read_b32 v17, a229\n"
"v_mfma_f32_16x16x32_bf16 v[104:107], a[48:51], a[128:131], v[104:107]\n"
"v_accvgpr_read_b32 v18, a230\n"
"v_accvgpr_read_b32 v19, a231\n"
"v_mfma_f32_16x16x32_bf16 v[104:107], a[52:55], a[132:135], v[104:107]\n"
"global_load_dwordx4 a[40:43], v0, s[34:35] offset:-2048 sc1\n"
"v_lshlrev_b32 v12, 16, v16\n"
"v_mfma_f32_16x16x32_bf16 v[132:135], a[60:63], a[132:135], v[132:135]\n"
"ds_read_b128 a[132:135], v5 offset:9216\n"
"v_and_b32 v13, s63, v16\n"
"v_mfma_f32_16x16x32_bf16 v[132:135], a[56:59], a[128:131], v[132:135]\n"
"ds_read_b128 a[128:131], v5 offset:8192\n"
"v_lshlrev_b32 v14, 16, v17\n"
"v_mfma_f32_16x16x32_bf16 v[136:139], a[56:59], a[136:139], v[136:139]\n"
"v_and_b32 v15, s63, v17\n"
"v_mul_f32 v12, v12, v216\n"
"v_mfma_f32_16x16x32_bf16 v[136:139], a[60:63], a[140:143], v[136:139]\n"
"v_mul_f32 v13, v13, v217\n"
"v_mul_f32 v14, v14, v218\n"
"v_mfma_f32_16x16x32_bf16 v[108:111], a[52:55], a[140:143], v[108:111]\n"
"ds_read_b128 a[140:143], v5 offset:11264\n"
"v_mul_f32 v15, v15, v219\n"
"v_mfma_f32_16x16x32_bf16 v[108:111], a[48:51], a[136:139], v[108:111]\n"
"ds_read_b128 a[136:139], v5 offset:10240\n"
"v_cvt_pk_bf16_f32 v28, v12, v13\n"
"v_mfma_f32_16x16x32_bf16 v[112:115], a[48:51], a[144:147], v[112:115]\n"
"v_cvt_pk_bf16_f32 v29, v14, v15\n"
"v_lshlrev_b32 v12, 16, v18\n"
"v_mfma_f32_16x16x32_bf16 v[112:115], a[52:55], a[148:151], v[112:115]\n"
"global_load_dwordx4 a[44:47], v0, s[34:35] offset:-1024 sc1\n"
"v_and_b32 v13, s63, v18\n"
"v_mfma_f32_16x16x32_bf16 v[140:143], a[60:63], a[148:151], v[140:143]\n"
"ds_read_b128 a[148:151], v5 offset:13312\n"
"v_lshlrev_b32 v14, 16, v19\n"
"v_mfma_f32_16x16x32_bf16 v[140:143], a[56:59], a[144:147], v[140:143]\n"
"ds_read_b128 a[144:147], v5 offset:12288\n"
"v_and_b32 v15, s63, v19\n"
"v_mfma_f32_16x16x32_bf16 a[12:15], a[56:59], a[152:155], a[12:15]\n"
"v_mul_f32 v12, v12, v244\n"
"v_mul_f32 v13, v13, v245\n"
"v_mfma_f32_16x16x32_bf16 a[12:15], a[60:63], a[156:159], a[12:15]\n"
"v_mul_f32 v14, v14, v246\n"
"v_mul_f32 v15, v15, v247\n"
"v_mfma_f32_16x16x32_bf16 a[8:11], a[52:55], a[156:159], a[8:11]\n"
"ds_read_b128 a[156:159], v5 offset:15360\n"
"v_cvt_pk_bf16_f32 v30, v12, v13\n"
"v_mfma_f32_16x16x32_bf16 a[8:11], a[48:51], a[152:155], a[8:11]\n"
"s_waitcnt vmcnt(9) lgkmcnt(12)\n"
"ds_read_b128 a[152:155], v5 offset:14336\n"
"v_cvt_pk_bf16_f32 v31, v14, v15\n"
"v_mfma_f32_16x16x32_bf16 v[32:35], a[64:67], a[96:99], v[32:35]\n"
"global_store_dwordx4 v0, v[28:31], s[54:55] offset:-3072 sc1\n"
"ds_write_b128 v2, v[28:31] offset:25600\n"
"v_mfma_f32_16x16x32_bf16 v[32:35], a[68:71], a[100:103], v[32:35]\n"
"global_load_dwordx4 a[48:51], v0, s[34:35] offset:0 sc1\n"
"v_accvgpr_read_b32 v16, a232\n"
"v_mfma_f32_16x16x32_bf16 v[60:63], a[76:79], a[100:103], v[60:63]\n"
"v_accvgpr_read_b32 v17, a233\n"
"v_accvgpr_read_b32 v18, a234\n"
"v_mfma_f32_16x16x32_bf16 v[60:63], a[72:75], a[96:99], v[60:63]\n"
"v_accvgpr_read_b32 v19, a235\n"
"v_lshlrev_b32 v12, 16, v16\n"
"v_mfma_f32_16x16x32_bf16 v[64:67], a[72:75], a[104:107], v[64:67]\n"
"v_and_b32 v13, s63, v16\n"
"v_lshlrev_b32 v14, 16, v17\n"
"v_mfma_f32_16x16x32_bf16 v[64:67], a[76:79], a[108:111], v[64:67]\n"
"v_and_b32 v15, s63, v17\n"
"v_mul_f32 v12, v12, v164\n"
"v_mfma_f32_16x16x32_bf16 v[36:39], a[68:71], a[108:111], v[36:39]\n"
"v_mul_f32 v13, v13, v165\n"
"v_mul_f32 v14, v14, v166\n"
"v_mfma_f32_16x16x32_bf16 v[36:39], a[64:67], a[104:107], v[36:39]\n"
"s_waitcnt lgkmcnt(9)\n"
"v_mul_f32 v15, v15, v167\n"
"v_cvt_pk_bf16_f32 v24, v12, v13\n"
"v_mfma_f32_16x16x32_bf16 v[40:43], a[64:67], a[112:115], v[40:43]\n"
"v_cvt_pk_bf16_f32 v25, v14, v15\n"
"v_mfma_f32_16x16x32_bf16 v[40:43], a[68:71], a[116:119], v[40:43]\n"
"global_load_dwordx4 a[52:55], v0, s[34:35] offset:1024 sc1\n"
"v_mfma_f32_16x16x32_bf16 v[68:71], a[76:79], a[116:119], v[68:71]\n"
"ds_write_b64 v2, v[24:25] offset:26624\n"
"v_mfma_f32_16x16x32_bf16 v[68:71], a[72:75], a[112:115], v[68:71]\n"
"v_lshlrev_b32 v12, 16, v18\n"
"v_and_b32 v13, s63, v18\n"
"v_mfma_f32_16x16x32_bf16 v[72:75], a[72:75], a[120:123], v[72:75]\n"
"v_lshlrev_b32 v14, 16, v19\n"
"v_and_b32 v15, s63, v19\n"
"v_mfma_f32_16x16x32_bf16 v[72:75], a[76:79], a[124:127], v[72:75]\n"
"v_mul_f32 v12, v12, v192\n"
"v_mul_f32 v13, v13, v193\n"
"v_mfma_f32_16x16x32_bf16 v[44:47], a[68:71], a[124:127], v[44:47]\n"
"v_mul_f32 v14, v14, v194\n"
"v_mul_f32 v15, v15, v195\n"
"v_mfma_f32_16x16x32_bf16 v[44:47], a[64:67], a[120:123], v[44:47]\n"
"s_waitcnt lgkmcnt(6)\n"
"v_cvt_pk_bf16_f32 v26, v12, v13\n"
"v_cvt_pk_bf16_f32 v27, v14, v15\n"
"v_mfma_f32_16x16x32_bf16 v[48:51], a[64:67], a[128:131], v[48:51]\n"
"ds_write_b64 v2, v[26:27] offset:26632\n"
"v_mfma_f32_16x16x32_bf16 v[48:51], a[68:71], a[132:135], v[48:51]\n"
"global_load_dwordx4 a[56:59], v0, s[34:35] offset:2048 sc1\n"
"v_accvgpr_read_b32 v16, a236\n"
"v_mfma_f32_16x16x32_bf16 v[76:79], a[76:79], a[132:135], v[76:79]\n"
"v_accvgpr_read_b32 v17, a237\n"
"v_accvgpr_read_b32 v18, a238\n"
"v_mfma_f32_16x16x32_bf16 v[76:79], a[72:75], a[128:131], v[76:79]\n"
"v_accvgpr_read_b32 v19, a239\n"
"v_lshlrev_b32 v12, 16, v16\n"
"v_mfma_f32_16x16x32_bf16 v[80:83], a[72:75], a[136:139], v[80:83]\n"
"v_and_b32 v13, s63, v16\n"
"v_mfma_f32_16x16x32_bf16 v[80:83], a[76:79], a[140:143], v[80:83]\n"
"v_lshlrev_b32 v14, 16, v17\n"
"v_mfma_f32_16x16x32_bf16 v[52:55], a[68:71], a[140:143], v[52:55]\n"
"v_and_b32 v15, s63, v17\n"
"v_mul_f32 v12, v12, v220\n"
"v_mfma_f32_16x16x32_bf16 v[52:55], a[64:67], a[136:139], v[52:55]\n"
"s_waitcnt lgkmcnt(3)\n"
"v_mul_f32 v13, v13, v221\n"
"v_mfma_f32_16x16x32_bf16 v[56:59], a[64:67], a[144:147], v[56:59]\n"
"v_mul_f32 v14, v14, v222\n"
"v_mul_f32 v15, v15, v223\n"
"v_mfma_f32_16x16x32_bf16 v[56:59], a[68:71], a[148:151], v[56:59]\n"
"global_load_dwordx4 a[60:63], v0, s[34:35] offset:3072 sc1\n"
"v_cvt_pk_bf16_f32 v28, v12, v13\n"
"v_mfma_f32_16x16x32_bf16 v[84:87], a[76:79], a[148:151], v[84:87]\n"
"v_cvt_pk_bf16_f32 v29, v14, v15\n"
"v_mfma_f32_16x16x32_bf16 v[84:87], a[72:75], a[144:147], v[84:87]\n"
"v_lshlrev_b32 v12, 16, v18\n"
"v_and_b32 v13, s63, v18\n"
"v_mfma_f32_16x16x32_bf16 a[4:7], a[72:75], a[152:155], a[4:7]\n"
"s_add_u32 s34, s34, 8192\n"
"v_lshlrev_b32 v14, 16, v19\n"
"v_and_b32 v15, s63, v19\n"
"v_mfma_f32_16x16x32_bf16 a[4:7], a[76:79], a[156:159], a[4:7]\n"
"v_mul_f32 v12, v12, v248\n"
"v_mul_f32 v13, v13, v249\n"
"v_mfma_f32_16x16x32_bf16 a[0:3], a[68:71], a[156:159], a[0:3]\n"
"v_mul_f32 v14, v14, v250\n"
"v_mul_f32 v15, v15, v251\n"
"v_mfma_f32_16x16x32_bf16 a[0:3], a[64:67], a[152:155], a[0:3]\n"
"s_waitcnt vmcnt(9)\n"
"v_cvt_pk_bf16_f32 v30, v12, v13\n"
"v_cvt_pk_bf16_f32 v31, v14, v15\n"
"v_mfma_f32_16x16x32_bf16 v[88:91], a[80:83], a[96:99], v[88:91]\n"
"global_store_dwordx4 v0, v[28:31], s[54:55] offset:-1024 sc1\n"
"ds_write_b128 v2, v[28:31] offset:27648\n"
"v_mfma_f32_16x16x32_bf16 v[88:91], a[84:87], a[100:103], v[88:91]\n"
"global_load_dwordx4 a[64:67], v0, s[34:35] offset:-4096 sc1\n"
"v_mfma_f32_16x16x32_bf16 v[116:119], a[92:95], a[100:103], v[116:119]\n"
"ds_read_b128 a[100:103], v2 offset:1024\n"
"v_mfma_f32_16x16x32_bf16 v[116:119], a[88:91], a[96:99], v[116:119]\n"
"ds_read_b128 a[96:99], v2 offset:0\n"
"v_accvgpr_read_b32 v16, a240\n"
"v_mfma_f32_16x16x32_bf16 v[120:123], a[88:91], a[104:107], v[120:123]\n"
"v_accvgpr_read_b32 v17, a241\n"
"v_accvgpr_read_b32 v18, a242\n"
"v_mfma_f32_16x16x32_bf16 v[120:123], a[92:95], a[108:111], v[120:123]\n"
"v_accvgpr_read_b32 v19, a243\n"
"v_lshlrev_b32 v12, 16, v16\n"
"v_mfma_f32_16x16x32_bf16 v[92:95], a[84:87], a[108:111], v[92:95]\n"
"ds_read_b128 a[108:111], v2 offset:3072\n"
"v_and_b32 v13, s63, v16\n"
"v_mfma_f32_16x16x32_bf16 v[92:95], a[80:83], a[104:107], v[92:95]\n"
"ds_read_b128 a[104:107], v2 offset:2048\n"
"v_lshlrev_b32 v14, 16, v17\n"
"v_mfma_f32_16x16x32_bf16 v[96:99], a[80:83], a[112:115], v[96:99]\n"
"v_and_b32 v15, s63, v17\n"
"v_mul_f32 v12, v12, v168\n"
"v_mfma_f32_16x16x32_bf16 v[96:99], a[84:87], a[116:119], v[96:99]\n"
"global_load_dwordx4 a[68:71], v0, s[34:35] offset:-3072 sc1\n"
"v_mul_f32 v13, v13, v169\n"
"v_mfma_f32_16x16x32_bf16 v[124:127], a[92:95], a[116:119], v[124:127]\n"
"ds_read_b128 a[116:119], v2 offset:5120\n"
"v_mul_f32 v14, v14, v170\n"
"v_mfma_f32_16x16x32_bf16 v[124:127], a[88:91], a[112:115], v[124:127]\n"
"ds_read_b128 a[112:115], v2 offset:4096\n"
"v_mul_f32 v15, v15, v171\n"
"v_mfma_f32_16x16x32_bf16 v[128:131], a[88:91], a[120:123], v[128:131]\n"
"v_cvt_pk_bf16_f32 v24, v12, v13\n"
"v_cvt_pk_bf16_f32 v25, v14, v15\n"
"v_mfma_f32_16x16x32_bf16 v[128:131], a[92:95], a[124:127], v[128:131]\n"
"v_lshlrev_b32 v12, 16, v18\n"
"v_and_b32 v13, s63, v18\n"
"v_mfma_f32_16x16x32_bf16 v[100:103], a[84:87], a[124:127], v[100:103]\n"
"ds_read_b128 a[124:127], v2 offset:7168\n"
"v_lshlrev_b32 v14, 16, v19\n"
"v_mfma_f32_16x16x32_bf16 v[100:103], a[80:83], a[120:123], v[100:103]\n"
"ds_read_b128 a[120:123], v2 offset:6144\n"
"v_mov_b32 v8, s65\n"
"v_mfma_f32_16x16x32_bf16 v[104:107], a[80:83], a[128:131], v[104:107]\n"
"v_mov_b32 v9, s60\n"
"v_and_b32 v15, s63, v19\n"
"v_mfma_f32_16x16x32_bf16 v[104:107], a[84:87], a[132:135], v[104:107]\n"
"global_load_dwordx4 a[72:75], v0, s[34:35] offset:-2048 sc1\n"
"v_mul_f32 v12, v12, v196\n"
"v_mfma_f32_16x16x32_bf16 v[132:135], a[92:95], a[132:135], v[132:135]\n"
"buffer_store_dwordx2 v[8:9], off, s[56:59], %13 sc1\n"
"ds_read_b128 a[132:135], v2 offset:9216\n"
"v_mfma_f32_16x16x32_bf16 v[132:135], a[88:91], a[128:131], v[132:135]\n"
"ds_read_b128 a[128:131], v2 offset:8192\n"
"v_mul_f32 v13, v13, v197\n"
"v_mfma_f32_16x16x32_bf16 v[136:139], a[88:91], a[136:139], v[136:139]\n"
"v_mul_f32 v14, v14, v198\n"
"v_mul_f32 v15, v15, v199\n"
"v_mfma_f32_16x16x32_bf16 v[136:139], a[92:95], a[140:143], v[136:139]\n"
"v_cvt_pk_bf16_f32 v26, v12, v13\n"
"v_cvt_pk_bf16_f32 v27, v14, v15\n"
"v_mfma_f32_16x16x32_bf16 v[108:111], a[84:87], a[140:143], v[108:111]\n"
"ds_read_b128 a[140:143], v2 offset:11264\n"
"v_mfma_f32_16x16x32_bf16 v[108:111], a[80:83], a[136:139], v[108:111]\n"
"ds_read_b128 a[136:139], v2 offset:10240\n"
"v_mfma_f32_16x16x32_bf16 v[112:115], a[80:83], a[144:147], v[112:115]\n"
"global_store_dwordx4 v0, v[24:27], s[54:55] offset:0 sc1\n"
"ds_write_b128 v2, v[24:27] offset:28672\n"
"v_mfma_f32_16x16x32_bf16 v[112:115], a[84:87], a[148:151], v[112:115]\n"
"global_load_dwordx4 a[76:79], v0, s[34:35] offset:-1024 sc1\n"
"v_accvgpr_read_b32 v16, a248\n"
"v_mfma_f32_16x16x32_bf16 v[140:143], a[92:95], a[148:151], v[140:143]\n"
"ds_read_b128 a[148:151], v2 offset:13312\n"
"v_accvgpr_read_b32 v17, a249\n"
"v_mfma_f32_16x16x32_bf16 v[140:143], a[88:91], a[144:147], v[140:143]\n"
"ds_read_b128 a[144:147], v2 offset:12288\n"
"v_accvgpr_read_b32 v18, a250\n"
"v_mfma_f32_16x16x32_bf16 a[12:15], a[88:91], a[152:155], a[12:15]\n"
"v_accvgpr_read_b32 v19, a251\n"
"v_lshlrev_b32 v12, 16, v16\n"
"v_mfma_f32_16x16x32_bf16 a[12:15], a[92:95], a[156:159], a[12:15]\n"
"v_and_b32 v13, s63, v16\n"
"v_lshlrev_b32 v14, 16, v17\n"
"v_mfma_f32_16x16x32_bf16 a[8:11], a[84:87], a[156:159], a[8:11]\n"
"ds_read_b128 a[156:159], v2 offset:15360\n"
"v_and_b32 v15, s63, v17\n"
"v_mfma_f32_16x16x32_bf16 a[8:11], a[80:83], a[152:155], a[8:11]\n"
"s_waitcnt vmcnt(12) lgkmcnt(12)\n"
"ds_read_b128 a[152:155], v2 offset:14336\n"
"v_accvgpr_read_b32 v10, a16\n"
"v_mfma_f32_16x16x32_bf16 v[144:147], a[32:35], a[96:99], v[144:147]\n"
"v_accvgpr_read_b32 v11, a17\n"
"v_mul_f32 v12, v12, v10\n"
"v_mfma_f32_16x16x32_bf16 v[144:147], a[36:39], a[100:103], v[144:147]\n"
"global_load_dwordx4 a[80:83], v0, s[34:35] offset:0 sc1\n"
"v_mul_f32 v13, v13, v11\n"
"v_mfma_f32_16x16x32_bf16 v[172:175], a[44:47], a[100:103], v[172:175]\n"
"v_accvgpr_read_b32 v10, a18\n"
"v_accvgpr_read_b32 v11, a19\n"
"v_mfma_f32_16x16x32_bf16 v[172:175], a[40:43], a[96:99], v[172:175]\n"
"v_mul_f32 v14, v14, v10\n"
"v_mul_f32 v15, v15, v11\n"
"v_mfma_f32_16x16x32_bf16 v[176:179], a[40:43], a[104:107], v[176:179]\n"
"v_cvt_pk_bf16_f32 v28, v12, v13\n"
"v_cvt_pk_bf16_f32 v29, v14, v15\n"
"v_mfma_f32_16x16x32_bf16 v[176:179], a[44:47], a[108:111], v[176:179]\n"
"v_lshlrev_b32 v12, 16, v18\n"
"v_and_b32 v13, s63, v18\n"
"v_mfma_f32_16x16x32_bf16 v[148:151], a[36:39], a[108:111], v[148:151]\n"
"v_lshlrev_b32 v14, 16, v19\n"
"v_and_b32 v15, s63, v19\n"
"v_mfma_f32_16x16x32_bf16 v[148:151], a[32:35], a[104:107], v[148:151]\n"
"s_waitcnt lgkmcnt(9)\n"
"v_accvgpr_read_b32 v10, a20\n"
"v_accvgpr_read_b32 v11, a21\n"
"v_mfma_f32_16x16x32_bf16 v[152:155], a[32:35], a[112:115], v[152:155]\n"
"v_mul_f32 v12, v12, v10\n"
"v_mul_f32 v13, v13, v11\n"
"v_mfma_f32_16x16x32_bf16 v[152:155], a[36:39], a[116:119], v[152:155]\n"
"global_load_dwordx4 a[84:87], v0, s[34:35] offset:1024 sc1\n"
"v_accvgpr_read_b32 v10, a22\n"
"v_mfma_f32_16x16x32_bf16 v[180:183], a[44:47], a[116:119], v[180:183]\n"
"v_accvgpr_read_b32 v11, a23\n"
"v_mfma_f32_16x16x32_bf16 v[180:183], a[40:43], a[112:115], v[180:183]\n"
"v_mul_f32 v14, v14, v10\n"
"v_mul_f32 v15, v15, v11\n"
"v_mfma_f32_16x16x32_bf16 v[184:187], a[40:43], a[120:123], v[184:187]\n"
"v_cvt_pk_bf16_f32 v30, v12, v13\n"
"v_mfma_f32_16x16x32_bf16 v[184:187], a[44:47], a[124:127], v[184:187]\n"
"v_cvt_pk_bf16_f32 v31, v14, v15\n"
"v_mfma_f32_16x16x32_bf16 v[156:159], a[36:39], a[124:127], v[156:159]\n"
"global_store_dwordx4 v0, v[28:31], s[54:55] offset:2048 sc1\n"
"ds_write_b128 v2, v[28:31] offset:30720\n"
"v_mfma_f32_16x16x32_bf16 v[156:159], a[32:35], a[120:123], v[156:159]\n"
"s_waitcnt lgkmcnt(6)\n"
"v_accvgpr_read_b32 v16, a244\n"
"v_mfma_f32_16x16x32_bf16 v[160:163], a[32:35], a[128:131], v[160:163]\n"
"v_accvgpr_read_b32 v17, a245\n"
"v_accvgpr_read_b32 v18, a246\n"
"v_mfma_f32_16x16x32_bf16 v[160:163], a[36:39], a[132:135], v[160:163]\n"
"global_load_dwordx4 a[88:91], v0, s[34:35] offset:2048 sc1\n"
"v_accvgpr_read_b32 v19, a247\n"
"v_mfma_f32_16x16x32_bf16 v[188:191], a[44:47], a[132:135], v[188:191]\n"
"L_st5702:\n"
"s_cbranch_execz L_st7944\n"
"v_lshlrev_b32 v12, 16, v16\n"
"v_mfma_f32_16x16x32_bf16 v[188:191], a[40:43], a[128:131], v[188:191]\n"
"v_and_b32 v13, s63, v16\n"
"v_lshlrev_b32 v14, 16, v17\n"
"v_mfma_f32_16x16x32_bf16 v[192:195], a[40:43], a[136:139], v[192:195]\n"
"v_and_b32 v15, s63, v17\n"
"v_mfma_f32_16x16x32_bf16 v[192:195], a[44:47], a[140:143], v[192:195]\n"
"v_mul_f32 v12, v12, v224\n"
"v_mul_f32 v13, v13, v225\n"
"v_mfma_f32_16x16x32_bf16 v[164:167], a[36:39], a[140:143], v[164:167]\n"
"v_mul_f32 v14, v14, v226\n"
"v_mul_f32 v15, v15, v227\n"
"v_mfma_f32_16x16x32_bf16 v[164:167], a[32:35], a[136:139], v[164:167]\n"
"s_waitcnt lgkmcnt(1)\n"
"v_cvt_pk_bf16_f32 v24, v12, v13\n"
"v_cvt_pk_bf16_f32 v25, v14, v15\n"
"v_mfma_f32_16x16x32_bf16 v[168:171], a[32:35], a[144:147], v[168:171]\n"
"ds_write_b64 v2, v[24:25] offset:29696\n"
"v_mfma_f32_16x16x32_bf16 v[168:171], a[36:39], a[148:151], v[168:171]\n"
"global_load_dwordx4 a[92:95], v0, s[34:35] offset:3072 sc1\n"
"v_mfma_f32_16x16x32_bf16 v[196:199], a[44:47], a[148:151], v[196:199]\n"
"v_lshlrev_b32 v12, 16, v18\n"
"v_and_b32 v13, s63, v18\n"
"v_mfma_f32_16x16x32_bf16 v[196:199], a[40:43], a[144:147], v[196:199]\n"
"v_lshlrev_b32 v14, 16, v19\n"
"v_and_b32 v15, s63, v19\n"
"v_mfma_f32_16x16x32_bf16 a[20:23], a[40:43], a[152:155], a[20:23]\n"
"s_add_u32 s34, s34, 8192\n"
"v_mul_f32 v12, v12, v252\n"
"v_mul_f32 v13, v13, v253\n"
"v_mfma_f32_16x16x32_bf16 a[20:23], a[44:47], a[156:159], a[20:23]\n"
"v_mul_f32 v14, v14, v254\n"
"v_mul_f32 v15, v15, v255\n"
"v_mfma_f32_16x16x32_bf16 a[16:19], a[36:39], a[156:159], a[16:19]\n"
"v_cvt_pk_bf16_f32 v26, v12, v13\n"
"v_cvt_pk_bf16_f32 v27, v14, v15\n"
"v_mfma_f32_16x16x32_bf16 a[16:19], a[32:35], a[152:155], a[16:19]\n"
"s_waitcnt vmcnt(12)\n"
"ds_write_b64 v2, v[26:27] offset:29704\n"
"v_mfma_f32_16x16x32_bf16 v[200:203], a[48:51], a[96:99], v[200:203]\n"
"v_accvgpr_read_b32 v16, a252\n"
"v_mfma_f32_16x16x32_bf16 v[200:203], a[52:55], a[100:103], v[200:203]\n"
"global_load_dwordx4 a[32:35], v0, s[34:35] offset:-4096 sc1\n"
"v_accvgpr_read_b32 v17, a253\n"
"v_mfma_f32_16x16x32_bf16 v[228:231], a[60:63], a[100:103], v[228:231]\n"
"ds_read_b128 a[100:103], v3 offset:1024\n"
"v_accvgpr_read_b32 v18, a254\n"
"v_mfma_f32_16x16x32_bf16 v[228:231], a[56:59], a[96:99], v[228:231]\n"
"ds_read_b128 a[96:99], v3 offset:0\n"
"v_accvgpr_read_b32 v19, a255\n"
"v_mfma_f32_16x16x32_bf16 v[232:235], a[56:59], a[104:107], v[232:235]\n"
"v_lshlrev_b32 v12, 16, v16\n"
"v_and_b32 v13, s63, v16\n"
"v_mfma_f32_16x16x32_bf16 v[232:235], a[60:63], a[108:111], v[232:235]\n"
"v_lshlrev_b32 v14, 16, v17\n"
"v_and_b32 v15, s63, v17\n"
"v_mfma_f32_16x16x32_bf16 v[204:207], a[52:55], a[108:111], v[204:207]\n"
"ds_read_b128 a[108:111], v3 offset:3072\n"
"v_accvgpr_read_b32 v10, a24\n"
"v_mfma_f32_16x16x32_bf16 v[204:207], a[48:51], a[104:107], v[204:207]\n"
"ds_read_b128 a[104:107], v3 offset:2048\n"
"v_accvgpr_read_b32 v11, a25\n"
"v_mfma_f32_16x16x32_bf16 v[208:211], a[48:51], a[112:115], v[208:211]\n"
"v_mul_f32 v12, v12, v10\n"
"v_mul_f32 v13, v13, v11\n"
"v_mfma_f32_16x16x32_bf16 v[208:211], a[52:55], a[116:119], v[208:211]\n"
"global_load_dwordx4 a[36:39], v0, s[34:35] offset:-3072 sc1\n"
"v_accvgpr_read_b32 v10, a26\n"
"v_mfma_f32_16x16x32_bf16 v[236:239], a[60:63], a[116:119], v[236:239]\n"
"ds_read_b128 a[116:119], v3 offset:5120\n"
"v_accvgpr_read_b32 v11, a27\n"
"v_mfma_f32_16x16x32_bf16 v[236:239], a[56:59], a[112:115], v[236:239]\n"
"ds_read_b128 a[112:115], v3 offset:4096\n"
"v_mul_f32 v14, v14, v10\n"
"v_mfma_f32_16x16x32_bf16 v[240:243], a[56:59], a[120:123], v[240:243]\n"
"v_mul_f32 v15, v15, v11\n"
"v_cvt_pk_bf16_f32 v28, v12, v13\n"
"v_mfma_f32_16x16x32_bf16 v[240:243], a[60:63], a[124:127], v[240:243]\n"
"v_cvt_pk_bf16_f32 v29, v14, v15\n"
"v_lshlrev_b32 v12, 16, v18\n"
"v_mfma_f32_16x16x32_bf16 v[212:215], a[52:55], a[124:127], v[212:215]\n"
"ds_read_b128 a[124:127], v3 offset:7168\n"
"v_and_b32 v13, s63, v18\n"
"v_mfma_f32_16x16x32_bf16 v[212:215], a[48:51], a[120:123], v[212:215]\n"
"ds_read_b128 a[120:123], v3 offset:6144\n"
"v_lshlrev_b32 v14, 16, v19\n"
"v_mfma_f32_16x16x32_bf16 v[216:219], a[48:51], a[128:131], v[216:219]\n"
"v_and_b32 v15, s63, v19\n"
"v_accvgpr_read_b32 v10, a28\n"
"v_mfma_f32_16x16x32_bf16 v[216:219], a[52:55], a[132:135], v[216:219]\n"
"global_load_dwordx4 a[40:43], v0, s[34:35] offset:-2048 sc1\n"
"v_accvgpr_read_b32 v11, a29\n"
"v_mfma_f32_16x16x32_bf16 v[244:247], a[60:63], a[132:135], v[244:247]\n"
"ds_read_b128 a[132:135], v3 offset:9216\n"
"v_mul_f32 v12, v12, v10\n"
"v_mfma_f32_16x16x32_bf16 v[244:247], a[56:59], a[128:131], v[244:247]\n"
"ds_read_b128 a[128:131], v3 offset:8192\n"
"v_mul_f32 v13, v13, v11\n"
"v_mfma_f32_16x16x32_bf16 v[248:251], a[56:59], a[136:139], v[248:251]\n"
"v_accvgpr_read_b32 v10, a30\n"
"v_accvgpr_read_b32 v11, a31\n"
"v_mfma_f32_16x16x32_bf16 v[248:251], a[60:63], a[140:143], v[248:251]\n"
"v_mul_f32 v14, v14, v10\n"
"v_mul_f32 v15, v15, v11\n"
"v_mfma_f32_16x16x32_bf16 v[220:223], a[52:55], a[140:143], v[220:223]\n"
"ds_read_b128 a[140:143], v3 offset:11264\n"
"v_cvt_pk_bf16_f32 v30, v12, v13\n"
"v_mfma_f32_16x16x32_bf16 v[220:223], a[48:51], a[136:139], v[220:223]\n"
"ds_read_b128 a[136:139], v3 offset:10240\n"
"v_cvt_pk_bf16_f32 v31, v14, v15\n"
"v_mfma_f32_16x16x32_bf16 v[224:227], a[48:51], a[144:147], v[224:227]\n"
"global_store_dwordx4 v0, v[28:31], s[54:55] offset:3072 sc1\n"
"ds_write_b128 v2, v[28:31] offset:31744\n"
"v_mfma_f32_16x16x32_bf16 v[224:227], a[52:55], a[148:151], v[224:227]\n"
"global_load_dwordx4 a[44:47], v0, s[34:35] offset:-1024 sc1\n"
"v_mfma_f32_16x16x32_bf16 v[252:255], a[60:63], a[148:151], v[252:255]\n"
"ds_read_b128 a[148:151], v3 offset:13312\n"
"v_mfma_f32_16x16x32_bf16 v[252:255], a[56:59], a[144:147], v[252:255]\n"
"ds_read_b128 a[144:147], v3 offset:12288\n"
"v_mfma_f32_16x16x32_bf16 a[28:31], a[56:59], a[152:155], a[28:31]\n"
"v_mfma_f32_16x16x32_bf16 a[28:31], a[60:63], a[156:159], a[28:31]\n"
"v_mfma_f32_16x16x32_bf16 a[24:27], a[52:55], a[156:159], a[24:27]\n"
"ds_read_b128 a[156:159], v3 offset:15360\n"
"v_mfma_f32_16x16x32_bf16 a[24:27], a[48:51], a[152:155], a[24:27]\n"
"s_waitcnt vmcnt(10) lgkmcnt(12)\n"
"ds_read_b128 a[152:155], v3 offset:14336\n"
"v_mfma_f32_16x16x32_bf16 v[144:147], a[64:67], a[96:99], v[144:147]\n"
"ds_read_b128 v[24:27], v2 offset:16384\n"
"v_mfma_f32_16x16x32_bf16 v[144:147], a[68:71], a[100:103], v[144:147]\n"
"global_load_dwordx4 a[48:51], v0, s[34:35] offset:0 sc1\n"
"v_mfma_f32_16x16x32_bf16 v[172:175], a[76:79], a[100:103], v[172:175]\n"
"v_mfma_f32_16x16x32_bf16 v[172:175], a[72:75], a[96:99], v[172:175]\n"
"v_mfma_f32_16x16x32_bf16 v[176:179], a[72:75], a[104:107], v[176:179]\n"
"v_mfma_f32_16x16x32_bf16 v[176:179], a[76:79], a[108:111], v[176:179]\n"
"v_mfma_f32_16x16x32_bf16 v[148:151], a[68:71], a[108:111], v[148:151]\n"
"v_mfma_f32_16x16x32_bf16 v[148:151], a[64:67], a[104:107], v[148:151]\n"
"s_waitcnt lgkmcnt(10)\n"
"v_mfma_f32_16x16x32_bf16 v[152:155], a[64:67], a[112:115], v[152:155]\n"
"v_mfma_f32_16x16x32_bf16 v[152:155], a[68:71], a[116:119], v[152:155]\n"
"global_load_dwordx4 a[52:55], v0, s[34:35] offset:1024 sc1\n"
"v_mfma_f32_16x16x32_bf16 v[180:183], a[76:79], a[116:119], v[180:183]\n"
"v_mfma_f32_16x16x32_bf16 v[180:183], a[72:75], a[112:115], v[180:183]\n"
"v_mfma_f32_16x16x32_bf16 v[184:187], a[72:75], a[120:123], v[184:187]\n"
"s_waitcnt lgkmcnt(0)\n"
"global_store_dwordx4 v0, v[24:27], s[52:53] offset:-4096 sc1\n"
"v_mfma_f32_16x16x32_bf16 v[184:187], a[76:79], a[124:127], v[184:187]\n"
"v_mfma_f32_16x16x32_bf16 v[156:159], a[68:71], a[124:127], v[156:159]\n"
"v_mfma_f32_16x16x32_bf16 v[156:159], a[64:67], a[120:123], v[156:159]\n"
"v_mfma_f32_16x16x32_bf16 v[160:163], a[64:67], a[128:131], v[160:163]\n"
"v_mfma_f32_16x16x32_bf16 v[160:163], a[68:71], a[132:135], v[160:163]\n"
"global_load_dwordx4 a[56:59], v0, s[34:35] offset:2048 sc1\n"
"v_mfma_f32_16x16x32_bf16 v[188:191], a[76:79], a[132:135], v[188:191]\n"
"v_mfma_f32_16x16x32_bf16 v[188:191], a[72:75], a[128:131], v[188:191]\n"
"v_mfma_f32_16x16x32_bf16 v[192:195], a[72:75], a[136:139], v[192:195]\n"
"v_mfma_f32_16x16x32_bf16 v[192:195], a[76:79], a[140:143], v[192:195]\n"
"v_mfma_f32_16x16x32_bf16 v[164:167], a[68:71], a[140:143], v[164:167]\n"
"v_mfma_f32_16x16x32_bf16 v[164:167], a[64:67], a[136:139], v[164:167]\n"
"v_mfma_f32_16x16x32_bf16 v[168:171], a[64:67], a[144:147], v[168:171]\n"
"v_mfma_f32_16x16x32_bf16 v[168:171], a[68:71], a[148:151], v[168:171]\n"
"global_load_dwordx4 a[60:63], v0, s[34:35] offset:3072 sc1\n"
"v_mfma_f32_16x16x32_bf16 v[196:199], a[76:79], a[148:151], v[196:199]\n"
"v_mfma_f32_16x16x32_bf16 v[196:199], a[72:75], a[144:147], v[196:199]\n"
"v_mfma_f32_16x16x32_bf16 a[20:23], a[72:75], a[152:155], a[20:23]\n"
"s_add_u32 s34, s34, 8192\n"
"v_mfma_f32_16x16x32_bf16 a[20:23], a[76:79], a[156:159], a[20:23]\n"
"v_mfma_f32_16x16x32_bf16 a[16:19], a[68:71], a[156:159], a[16:19]\n"
"v_mfma_f32_16x16x32_bf16 a[16:19], a[64:67], a[152:155], a[16:19]\n"
"s_waitcnt vmcnt(10)\n"
"ds_read_b128 v[28:31], v2 offset:17408\n"
"v_mfma_f32_16x16x32_bf16 v[200:203], a[80:83], a[96:99], v[200:203]\n"
"v_mfma_f32_16x16x32_bf16 v[200:203], a[84:87], a[100:103], v[200:203]\n"
"global_load_dwordx4 a[64:67], v0, s[34:35] offset:-4096 sc1\n"
"v_mfma_f32_16x16x32_bf16 v[228:231], a[92:95], a[100:103], v[228:231]\n"
"ds_read_b128 a[100:103], v4 offset:1024\n"
"v_mfma_f32_16x16x32_bf16 v[228:231], a[88:91], a[96:99], v[228:231]\n"
"ds_read_b128 a[96:99], v4 offset:0\n"
"v_mfma_f32_16x16x32_bf16 v[232:235], a[88:91], a[104:107], v[232:235]\n"
"v_mfma_f32_16x16x32_bf16 v[232:235], a[92:95], a[108:111], v[232:235]\n"
"v_mfma_f32_16x16x32_bf16 v[204:207], a[84:87], a[108:111], v[204:207]\n"
"ds_read_b128 a[108:111], v4 offset:3072\n"
"v_mfma_f32_16x16x32_bf16 v[204:207], a[80:83], a[104:107], v[204:207]\n"
"ds_read_b128 a[104:107], v4 offset:2048\n"
"v_mfma_f32_16x16x32_bf16 v[208:211], a[80:83], a[112:115], v[208:211]\n"
"v_mfma_f32_16x16x32_bf16 v[208:211], a[84:87], a[116:119], v[208:211]\n"
"global_load_dwordx4 a[68:71], v0, s[34:35] offset:-3072 sc1\n"
"v_mfma_f32_16x16x32_bf16 v[236:239], a[92:95], a[116:119], v[236:239]\n"
"s_waitcnt lgkmcnt(4)\n"
"global_store_dwordx4 v0, v[28:31], s[52:53] offset:-3072 sc1\n"
"ds_read_b128 a[116:119], v4 offset:5120\n"
"v_mfma_f32_16x16x32_bf16 v[236:239], a[88:91], a[112:115], v[236:239]\n"
"ds_read_b128 a[112:115], v4 offset:4096\n"
"v_mfma_f32_16x16x32_bf16 v[240:243], a[88:91], a[120:123], v[240:243]\n"
"v_mfma_f32_16x16x32_bf16 v[240:243], a[92:95], a[124:127], v[240:243]\n"
"v_mfma_f32_16x16x32_bf16 v[212:215], a[84:87], a[124:127], v[212:215]\n"
"ds_read_b128 a[124:127], v4 offset:7168\n"
"v_mfma_f32_16x16x32_bf16 v[212:215], a[80:83], a[120:123], v[212:215]\n"
"ds_read_b128 a[120:123], v4 offset:6144\n"
"v_mfma_f32_16x16x32_bf16 v[216:219], a[80:83], a[128:131], v[216:219]\n"
"v_mfma_f32_16x16x32_bf16 v[216:219], a[84:87], a[132:135], v[216:219]\n"
"global_load_dwordx4 a[72:75], v0, s[34:35] offset:-2048 sc1\n"
"v_mfma_f32_16x16x32_bf16 v[244:247], a[92:95], a[132:135], v[244:247]\n"
"ds_read_b128 a[132:135], v4 offset:9216\n"
"v_mfma_f32_16x16x32_bf16 v[244:247], a[88:91], a[128:131], v[244:247]\n"
"ds_read_b128 a[128:131], v4 offset:8192\n"
"v_mfma_f32_16x16x32_bf16 v[248:251], a[88:91], a[136:139], v[248:251]\n"
"v_mfma_f32_16x16x32_bf16 v[248:251], a[92:95], a[140:143], v[248:251]\n"
"v_mfma_f32_16x16x32_bf16 v[220:223], a[84:87], a[140:143], v[220:223]\n"
"ds_read_b128 a[140:143], v4 offset:11264\n"
"v_mfma_f32_16x16x32_bf16 v[220:223], a[80:83], a[136:139], v[220:223]\n"
"ds_read_b128 a[136:139], v4 offset:10240\n"
"v_mfma_f32_16x16x32_bf16 v[224:227], a[80:83], a[144:147], v[224:227]\n"
"v_mfma_f32_16x16x32_bf16 v[224:227], a[84:87], a[148:151], v[224:227]\n"
"global_load_dwordx4 a[76:79], v0, s[34:35] offset:-1024 sc1\n"
"v_mfma_f32_16x16x32_bf16 v[252:255], a[92:95], a[148:151], v[252:255]\n"
"ds_read_b128 a[148:151], v4 offset:13312\n"
"v_mfma_f32_16x16x32_bf16 v[252:255], a[88:91], a[144:147], v[252:255]\n"
"ds_read_b128 a[144:147], v4 offset:12288\n"
"v_mfma_f32_16x16x32_bf16 a[28:31], a[88:91], a[152:155], a[28:31]\n"
"v_mfma_f32_16x16x32_bf16 a[28:31], a[92:95], a[156:159], a[28:31]\n"
"ds_read_b128 v[24:27], v2 offset:18432\n"
"v_mfma_f32_16x16x32_bf16 a[24:27], a[84:87], a[156:159], a[24:27]\n"
"ds_read_b128 a[156:159], v4 offset:15360\n"
"v_mfma_f32_16x16x32_bf16 a[24:27], a[80:83], a[152:155], a[24:27]\n"
"s_waitcnt vmcnt(10) lgkmcnt(12)\n"
"ds_read_b128 a[152:155], v4 offset:14336\n"
"v_mfma_f32_16x16x32_bf16 v[144:147], a[32:35], a[96:99], v[144:147]\n"
"v_mfma_f32_16x16x32_bf16 v[144:147], a[36:39], a[100:103], v[144:147]\n"
"global_load_dwordx4 a[80:83], v0, s[34:35] offset:0 sc1\n"
"v_mfma_f32_16x16x32_bf16 v[172:175], a[44:47], a[100:103], v[172:175]\n"
"v_mfma_f32_16x16x32_bf16 v[172:175], a[40:43], a[96:99], v[172:175]\n"
"v_mfma_f32_16x16x32_bf16 v[176:179], a[40:43], a[104:107], v[176:179]\n"
"v_mfma_f32_16x16x32_bf16 v[176:179], a[44:47], a[108:111], v[176:179]\n"
"v_mfma_f32_16x16x32_bf16 v[148:151], a[36:39], a[108:111], v[148:151]\n"
"v_mfma_f32_16x16x32_bf16 v[148:151], a[32:35], a[104:107], v[148:151]\n"
"s_waitcnt lgkmcnt(9)\n"
"v_mfma_f32_16x16x32_bf16 v[152:155], a[32:35], a[112:115], v[152:155]\n"
"s_waitcnt lgkmcnt(2)\n"
"global_store_dwordx4 v0, v[24:27], s[52:53] offset:-2048 sc1\n"
"v_mfma_f32_16x16x32_bf16 v[152:155], a[36:39], a[116:119], v[152:155]\n"
"global_load_dwordx4 a[84:87], v0, s[34:35] offset:1024 sc1\n"
"v_mfma_f32_16x16x32_bf16 v[180:183], a[44:47], a[116:119], v[180:183]\n"
"v_mfma_f32_16x16x32_bf16 v[180:183], a[40:43], a[112:115], v[180:183]\n"
"v_mfma_f32_16x16x32_bf16 v[184:187], a[40:43], a[120:123], v[184:187]\n"
"v_mfma_f32_16x16x32_bf16 v[184:187], a[44:47], a[124:127], v[184:187]\n"
"v_mfma_f32_16x16x32_bf16 v[156:159], a[36:39], a[124:127], v[156:159]\n"
"v_mfma_f32_16x16x32_bf16 v[156:159], a[32:35], a[120:123], v[156:159]\n"
"v_mfma_f32_16x16x32_bf16 v[160:163], a[32:35], a[128:131], v[160:163]\n"
"v_mfma_f32_16x16x32_bf16 v[160:163], a[36:39], a[132:135], v[160:163]\n"
"global_load_dwordx4 a[88:91], v0, s[34:35] offset:2048 sc1\n"
"v_mfma_f32_16x16x32_bf16 v[188:191], a[44:47], a[132:135], v[188:191]\n"
"v_mfma_f32_16x16x32_bf16 v[188:191], a[40:43], a[128:131], v[188:191]\n"
"v_mfma_f32_16x16x32_bf16 v[192:195], a[40:43], a[136:139], v[192:195]\n"
"v_mfma_f32_16x16x32_bf16 v[192:195], a[44:47], a[140:143], v[192:195]\n"
"v_mfma_f32_16x16x32_bf16 v[164:167], a[36:39], a[140:143], v[164:167]\n"
"v_mfma_f32_16x16x32_bf16 v[164:167], a[32:35], a[136:139], v[164:167]\n"
"s_waitcnt lgkmcnt(0)\n"
"v_mfma_f32_16x16x32_bf16 v[168:171], a[32:35], a[144:147], v[168:171]\n"
"v_mfma_f32_16x16x32_bf16 v[168:171], a[36:39], a[148:151], v[168:171]\n"
"global_load_dwordx4 a[92:95], v0, s[34:35] offset:3072 sc1\n"
"v_mfma_f32_16x16x32_bf16 v[196:199], a[44:47], a[148:151], v[196:199]\n"
"v_mfma_f32_16x16x32_bf16 v[196:199], a[40:43], a[144:147], v[196:199]\n"
"ds_read_b128 v[28:31], v2 offset:19456\n"
"v_mfma_f32_16x16x32_bf16 a[20:23], a[40:43], a[152:155], a[20:23]\n"
"s_add_u32 s34, s34, 8192\n"
"v_mfma_f32_16x16x32_bf16 a[20:23], a[44:47], a[156:159], a[20:23]\n"
"v_mfma_f32_16x16x32_bf16 a[16:19], a[36:39], a[156:159], a[16:19]\n"
"v_mfma_f32_16x16x32_bf16 a[16:19], a[32:35], a[152:155], a[16:19]\n"
"s_waitcnt vmcnt(10)\n"
"v_mfma_f32_16x16x32_bf16 v[200:203], a[48:51], a[96:99], v[200:203]\n"
"v_mfma_f32_16x16x32_bf16 v[200:203], a[52:55], a[100:103], v[200:203]\n"
"global_load_dwordx4 a[32:35], v0, s[34:35] offset:-4096 sc1\n"
"v_mfma_f32_16x16x32_bf16 v[228:231], a[60:63], a[100:103], v[228:231]\n"
"ds_read_b128 a[100:103], v5 offset:1024\n"
"v_mfma_f32_16x16x32_bf16 v[228:231], a[56:59], a[96:99], v[228:231]\n"
"ds_read_b128 a[96:99], v5 offset:0\n"
"v_mfma_f32_16x16x32_bf16 v[232:235], a[56:59], a[104:107], v[232:235]\n"
"v_mfma_f32_16x16x32_bf16 v[232:235], a[60:63], a[108:111], v[232:235]\n"
"v_mfma_f32_16x16x32_bf16 v[204:207], a[52:55], a[108:111], v[204:207]\n"
"s_waitcnt lgkmcnt(2)\n"
"global_store_dwordx4 v0, v[28:31], s[52:53] offset:-1024 sc1\n"
"ds_read_b128 a[108:111], v5 offset:3072\n"
"v_mfma_f32_16x16x32_bf16 v[204:207], a[48:51], a[104:107], v[204:207]\n"
"ds_read_b128 a[104:107], v5 offset:2048\n"
"v_mfma_f32_16x16x32_bf16 v[208:211], a[48:51], a[112:115], v[208:211]\n"
"v_mfma_f32_16x16x32_bf16 v[208:211], a[52:55], a[116:119], v[208:211]\n"
"global_load_dwordx4 a[36:39], v0, s[34:35] offset:-3072 sc1\n"
"v_mfma_f32_16x16x32_bf16 v[236:239], a[60:63], a[116:119], v[236:239]\n"
"ds_read_b128 a[116:119], v5 offset:5120\n"
"v_mfma_f32_16x16x32_bf16 v[236:239], a[56:59], a[112:115], v[236:239]\n"
"ds_read_b128 a[112:115], v5 offset:4096\n"
"v_mfma_f32_16x16x32_bf16 v[240:243], a[56:59], a[120:123], v[240:243]\n"
"v_mfma_f32_16x16x32_bf16 v[240:243], a[60:63], a[124:127], v[240:243]\n"
"v_mfma_f32_16x16x32_bf16 v[212:215], a[52:55], a[124:127], v[212:215]\n"
"ds_read_b128 a[124:127], v5 offset:7168\n"
"v_mfma_f32_16x16x32_bf16 v[212:215], a[48:51], a[120:123], v[212:215]\n"
"ds_read_b128 a[120:123], v5 offset:6144\n"
"v_mfma_f32_16x16x32_bf16 v[216:219], a[48:51], a[128:131], v[216:219]\n"
"v_mfma_f32_16x16x32_bf16 v[216:219], a[52:55], a[132:135], v[216:219]\n"
"global_load_dwordx4 a[40:43], v0, s[34:35] offset:-2048 sc1\n"
"v_mfma_f32_16x16x32_bf16 v[244:247], a[60:63], a[132:135], v[244:247]\n"
"ds_read_b128 a[132:135], v5 offset:9216\n"
"v_mfma_f32_16x16x32_bf16 v[244:247], a[56:59], a[128:131], v[244:247]\n"
"ds_read_b128 a[128:131], v5 offset:8192\n"
"v_mfma_f32_16x16x32_bf16 v[248:251], a[56:59], a[136:139], v[248:251]\n"
"v_mfma_f32_16x16x32_bf16 v[248:251], a[60:63], a[140:143], v[248:251]\n"
"v_mfma_f32_16x16x32_bf16 v[220:223], a[52:55], a[140:143], v[220:223]\n"
"ds_read_b128 a[140:143], v5 offset:11264\n"
"v_mfma_f32_16x16x32_bf16 v[220:223], a[48:51], a[136:139], v[220:223]\n"
"ds_read_b128 a[136:139], v5 offset:10240\n"
"v_mfma_f32_16x16x32_bf16 v[224:227], a[48:51], a[144:147], v[224:227]\n"
"ds_read_b128 v[24:27], v2 offset:20480\n"
"v_mfma_f32_16x16x32_bf16 v[224:227], a[52:55], a[148:151], v[224:227]\n"
"global_load_dwordx4 a[44:47], v0, s[34:35] offset:-1024 sc1\n"
"v_mfma_f32_16x16x32_bf16 v[252:255], a[60:63], a[148:151], v[252:255]\n"
"ds_read_b128 a[148:151], v5 offset:13312\n"
"v_mfma_f32_16x16x32_bf16 v[252:255], a[56:59], a[144:147], v[252:255]\n"
"ds_read_b128 a[144:147], v5 offset:12288\n"
"v_mfma_f32_16x16x32_bf16 a[28:31], a[56:59], a[152:155], a[28:31]\n"
"v_mfma_f32_16x16x32_bf16 a[28:31], a[60:63], a[156:159], a[28:31]\n"
"v_mfma_f32_16x16x32_bf16 a[24:27], a[52:55], a[156:159], a[24:27]\n"
"ds_read_b128 a[156:159], v5 offset:15360\n"
"v_mfma_f32_16x16x32_bf16 a[24:27], a[48:51], a[152:155], a[24:27]\n"
"s_waitcnt vmcnt(10) lgkmcnt(12)\n"
"ds_read_b128 a[152:155], v5 offset:14336\n"
"v_mfma_f32_16x16x32_bf16 v[144:147], a[64:67], a[96:99], v[144:147]\n"
"v_mfma_f32_16x16x32_bf16 v[144:147], a[68:71], a[100:103], v[144:147]\n"
"global_load_dwordx4 a[48:51], v0, s[34:35] offset:0 sc1\n"
"v_mfma_f32_16x16x32_bf16 v[172:175], a[76:79], a[100:103], v[172:175]\n"
"v_mfma_f32_16x16x32_bf16 v[172:175], a[72:75], a[96:99], v[172:175]\n"
"v_mfma_f32_16x16x32_bf16 v[176:179], a[72:75], a[104:107], v[176:179]\n"
"s_waitcnt lgkmcnt(4)\n"
"global_store_dwordx4 v0, v[24:27], s[52:53] offset:0 sc1\n"
"v_mfma_f32_16x16x32_bf16 v[176:179], a[76:79], a[108:111], v[176:179]\n"
"v_mfma_f32_16x16x32_bf16 v[148:151], a[68:71], a[108:111], v[148:151]\n"
"v_mfma_f32_16x16x32_bf16 v[148:151], a[64:67], a[104:107], v[148:151]\n"
"v_mfma_f32_16x16x32_bf16 v[152:155], a[64:67], a[112:115], v[152:155]\n"
"v_mfma_f32_16x16x32_bf16 v[152:155], a[68:71], a[116:119], v[152:155]\n"
"global_load_dwordx4 a[52:55], v0, s[34:35] offset:1024 sc1\n"
"v_mfma_f32_16x16x32_bf16 v[180:183], a[76:79], a[116:119], v[180:183]\n"
"v_mfma_f32_16x16x32_bf16 v[180:183], a[72:75], a[112:115], v[180:183]\n"
"v_mfma_f32_16x16x32_bf16 v[184:187], a[72:75], a[120:123], v[184:187]\n"
"v_mfma_f32_16x16x32_bf16 v[184:187], a[76:79], a[124:127], v[184:187]\n"
"v_mfma_f32_16x16x32_bf16 v[156:159], a[68:71], a[124:127], v[156:159]\n"
"v_mfma_f32_16x16x32_bf16 v[156:159], a[64:67], a[120:123], v[156:159]\n"
"v_mfma_f32_16x16x32_bf16 v[160:163], a[64:67], a[128:131], v[160:163]\n"
"v_mfma_f32_16x16x32_bf16 v[160:163], a[68:71], a[132:135], v[160:163]\n"
"global_load_dwordx4 a[56:59], v0, s[34:35] offset:2048 sc1\n"
"v_mfma_f32_16x16x32_bf16 v[188:191], a[76:79], a[132:135], v[188:191]\n"
"v_mfma_f32_16x16x32_bf16 v[188:191], a[72:75], a[128:131], v[188:191]\n"
"v_mfma_f32_16x16x32_bf16 v[192:195], a[72:75], a[136:139], v[192:195]\n"
"v_mfma_f32_16x16x32_bf16 v[192:195], a[76:79], a[140:143], v[192:195]\n"
"v_mfma_f32_16x16x32_bf16 v[164:167], a[68:71], a[140:143], v[164:167]\n"
"v_mfma_f32_16x16x32_bf16 v[164:167], a[64:67], a[136:139], v[164:167]\n"
"s_waitcnt lgkmcnt(0)\n"
"ds_read_b128 v[28:31], v2 offset:21504\n"
"v_mfma_f32_16x16x32_bf16 v[168:171], a[64:67], a[144:147], v[168:171]\n"
"v_mfma_f32_16x16x32_bf16 v[168:171], a[68:71], a[148:151], v[168:171]\n"
"global_load_dwordx4 a[60:63], v0, s[34:35] offset:3072 sc1\n"
"v_mfma_f32_16x16x32_bf16 v[196:199], a[76:79], a[148:151], v[196:199]\n"
"v_mfma_f32_16x16x32_bf16 v[196:199], a[72:75], a[144:147], v[196:199]\n"
"v_mfma_f32_16x16x32_bf16 a[20:23], a[72:75], a[152:155], a[20:23]\n"
"s_add_u32 s34, s34, 8192\n"
"v_mfma_f32_16x16x32_bf16 a[20:23], a[76:79], a[156:159], a[20:23]\n"
"v_mfma_f32_16x16x32_bf16 a[16:19], a[68:71], a[156:159], a[16:19]\n"
"v_mfma_f32_16x16x32_bf16 a[16:19], a[64:67], a[152:155], a[16:19]\n"
"s_waitcnt vmcnt(10)\n"
"v_mfma_f32_16x16x32_bf16 v[200:203], a[80:83], a[96:99], v[200:203]\n"
"v_mfma_f32_16x16x32_bf16 v[200:203], a[84:87], a[100:103], v[200:203]\n"
"global_load_dwordx4 a[64:67], v0, s[34:35] offset:-4096 sc1\n"
"v_mfma_f32_16x16x32_bf16 v[228:231], a[92:95], a[100:103], v[228:231]\n"
"s_waitcnt lgkmcnt(0)\n"
"global_store_dwordx4 v0, v[28:31], s[52:53] offset:1024 sc1\n"
"ds_read_b128 a[100:103], v2 offset:17408\n"
"v_mfma_f32_16x16x32_bf16 v[228:231], a[88:91], a[96:99], v[228:231]\n"
"ds_read_b128 a[96:99], v2 offset:16384\n"
"v_mfma_f32_16x16x32_bf16 v[232:235], a[88:91], a[104:107], v[232:235]\n"
"v_mfma_f32_16x16x32_bf16 v[232:235], a[92:95], a[108:111], v[232:235]\n"
"v_mfma_f32_16x16x32_bf16 v[204:207], a[84:87], a[108:111], v[204:207]\n"
"ds_read_b128 a[108:111], v2 offset:19456\n"
"v_mfma_f32_16x16x32_bf16 v[204:207], a[80:83], a[104:107], v[204:207]\n"
"ds_read_b128 a[104:107], v2 offset:18432\n"
"v_mfma_f32_16x16x32_bf16 v[208:211], a[80:83], a[112:115], v[208:211]\n"
"v_mfma_f32_16x16x32_bf16 v[208:211], a[84:87], a[116:119], v[208:211]\n"
"global_load_dwordx4 a[68:71], v0, s[34:35] offset:-3072 sc1\n"
"v_mfma_f32_16x16x32_bf16 v[236:239], a[92:95], a[116:119], v[236:239]\n"
"ds_read_b128 a[116:119], v2 offset:21504\n"
"v_mfma_f32_16x16x32_bf16 v[236:239], a[88:91], a[112:115], v[236:239]\n"
"ds_read_b128 a[112:115], v2 offset:20480\n"
"v_mfma_f32_16x16x32_bf16 v[240:243], a[88:91], a[120:123], v[240:243]\n"
"v_mfma_f32_16x16x32_bf16 v[240:243], a[92:95], a[124:127], v[240:243]\n"
"v_mfma_f32_16x16x32_bf16 v[212:215], a[84:87], a[124:127], v[212:215]\n"
"ds_read_b128 a[124:127], v2 offset:23552\n"
"v_mfma_f32_16x16x32_bf16 v[212:215], a[80:83], a[120:123], v[212:215]\n"
"ds_read_b128 a[120:123], v2 offset:22528\n"
"v_mfma_f32_16x16x32_bf16 v[216:219], a[80:83], a[128:131], v[216:219]\n"
"v_mfma_f32_16x16x32_bf16 v[216:219], a[84:87], a[132:135], v[216:219]\n"
"global_load_dwordx4 a[72:75], v0, s[34:35] offset:-2048 sc1\n"
"v_mfma_f32_16x16x32_bf16 v[244:247], a[92:95], a[132:135], v[244:247]\n"
"ds_read_b128 a[132:135], v2 offset:25600\n"
"v_mfma_f32_16x16x32_bf16 v[244:247], a[88:91], a[128:131], v[244:247]\n"
"ds_read_b128 a[128:131], v2 offset:24576\n"
"v_mfma_f32_16x16x32_bf16 v[248:251], a[88:91], a[136:139], v[248:251]\n"
"v_mfma_f32_16x16x32_bf16 v[248:251], a[92:95], a[140:143], v[248:251]\n"
"ds_read_b128 v[24:27], v2 offset:22528\n"
"v_mfma_f32_16x16x32_bf16 v[220:223], a[84:87], a[140:143], v[220:223]\n"
"ds_read_b128 a[140:143], v2 offset:27648\n"
"v_mfma_f32_16x16x32_bf16 v[220:223], a[80:83], a[136:139], v[220:223]\n"
"ds_read_b128 a[136:139], v2 offset:26624\n"
"v_mfma_f32_16x16x32_bf16 v[224:227], a[80:83], a[144:147], v[224:227]\n"
"v_mfma_f32_16x16x32_bf16 v[224:227], a[84:87], a[148:151], v[224:227]\n"
"global_load_dwordx4 a[76:79], v0, s[34:35] offset:-1024 sc1\n"
"v_mfma_f32_16x16x32_bf16 v[252:255], a[92:95], a[148:151], v[252:255]\n"
"ds_read_b128 a[148:151], v2 offset:29696\n"
"v_mfma_f32_16x16x32_bf16 v[252:255], a[88:91], a[144:147], v[252:255]\n"
"ds_read_b128 a[144:147], v2 offset:28672\n"
"v_mfma_f32_16x16x32_bf16 a[28:31], a[88:91], a[152:155], a[28:31]\n"
"v_mfma_f32_16x16x32_bf16 a[28:31], a[92:95], a[156:159], a[28:31]\n"
"v_mfma_f32_16x16x32_bf16 a[24:27], a[84:87], a[156:159], a[24:27]\n"
"s_bitcmp0_b32 %26, 0\n"
"ds_read_b128 a[156:159], v2 offset:31744\n"
"v_mfma_f32_16x16x32_bf16 a[24:27], a[80:83], a[152:155], a[24:27]\n"
"ds_read_b128 a[152:155], v2 offset:30720\n"
"s_cbranch_scc1 L_P3F\n"
"s_waitcnt vmcnt(10) lgkmcnt(13)\n"
"v_mfma_f32_16x16x32_bf16 v[32:35], a[32:35], a[96:99], v[32:35]\n"
"s_waitcnt lgkmcnt(6)\n"
"global_store_dwordx4 v0, v[24:27], s[52:53] offset:2048 sc1\n"
"v_mfma_f32_16x16x32_bf16 v[32:35], a[36:39], a[100:103], v[32:35]\n"
"global_load_dwordx4 a[80:83], v0, s[34:35] offset:0 sc1\n"
"v_mfma_f32_16x16x32_bf16 v[60:63], a[44:47], a[100:103], v[60:63]\n"
"s_barrier\n"
"v_mfma_f32_16x16x32_bf16 v[60:63], a[40:43], a[96:99], v[60:63]\n"
"v_mfma_f32_16x16x32_bf16 v[64:67], a[40:43], a[104:107], v[64:67]\n"
"v_mfma_f32_16x16x32_bf16 v[64:67], a[44:47], a[108:111], v[64:67]\n"
"v_mfma_f32_16x16x32_bf16 v[36:39], a[36:39], a[108:111], v[36:39]\n"
"v_mfma_f32_16x16x32_bf16 v[36:39], a[32:35], a[104:107], v[36:39]\n"
"v_mfma_f32_16x16x32_bf16 v[40:43], a[32:35], a[112:115], v[40:43]\n"
"v_mfma_f32_16x16x32_bf16 v[40:43], a[36:39], a[116:119], v[40:43]\n"
"global_load_dwordx4 a[84:87], v0, s[34:35] offset:1024 sc1\n"
"v_mfma_f32_16x16x32_bf16 v[68:71], a[44:47], a[116:119], v[68:71]\n"
"v_mfma_f32_16x16x32_bf16 v[68:71], a[40:43], a[112:115], v[68:71]\n"
"v_mfma_f32_16x16x32_bf16 v[72:75], a[40:43], a[120:123], v[72:75]\n"
"v_mfma_f32_16x16x32_bf16 v[72:75], a[44:47], a[124:127], v[72:75]\n"
"v_mfma_f32_16x16x32_bf16 v[44:47], a[36:39], a[124:127], v[44:47]\n"
"v_mfma_f32_16x16x32_bf16 v[44:47], a[32:35], a[120:123], v[44:47]\n"
"s_waitcnt lgkmcnt(4)\n"
"v_mfma_f32_16x16x32_bf16 v[48:51], a[32:35], a[128:131], v[48:51]\n"
"v_mfma_f32_16x16x32_bf16 v[48:51], a[36:39], a[132:135], v[48:51]\n"
"global_load_dwordx4 a[88:91], v0, s[34:35] offset:2048 sc1\n"
"v_lshrrev_b32 v16, 4, v0\n"
"v_mfma_f32_16x16x32_bf16 v[76:79], a[44:47], a[132:135], v[76:79]\n"
"v_and_b32 v16, 0x30, v16\n"
"v_mfma_f32_16x16x32_bf16 v[76:79], a[40:43], a[128:131], v[76:79]\n"
"ds_read_b128 v[28:31], v2 offset:23552\n"
"buffer_load_dwordx2 v[18:19], v16, s[44:47], s61 offen offset:0\n"
"v_mfma_f32_16x16x32_bf16 v[80:83], a[40:43], a[136:139], v[80:83]\n"
"buffer_load_dwordx2 v[8:9], off, s[56:59], %14 sc1\n"
"v_mfma_f32_16x16x32_bf16 v[80:83], a[44:47], a[140:143], v[80:83]\n"
"v_mfma_f32_16x16x32_bf16 v[52:55], a[36:39], a[140:143], v[52:55]\n"
"v_mfma_f32_16x16x32_bf16 v[52:55], a[32:35], a[136:139], v[52:55]\n"
"s_waitcnt lgkmcnt(1)\n"
"v_mfma_f32_16x16x32_bf16 v[56:59], a[32:35], a[144:147], v[56:59]\n"
"v_mfma_f32_16x16x32_bf16 v[56:59], a[36:39], a[148:151], v[56:59]\n"
"global_load_dwordx4 a[92:95], v0, s[34:35] offset:3072 sc1\n"
"v_lshrrev_b32 v20, 4, v0\n"
"v_mfma_f32_16x16x32_bf16 v[84:87], a[44:47], a[148:151], v[84:87]\n"
"v_and_b32 v20, 0x30, v20\n"
"v_mfma_f32_16x16x32_bf16 v[84:87], a[40:43], a[144:147], v[84:87]\n"
"buffer_load_dwordx2 v[22:23], v20, s[44:47], s61 offen offset:8\n"
"v_mfma_f32_16x16x32_bf16 a[4:7], a[40:43], a[152:155], a[4:7]\n"
"s_add_u32 s34, s34, 8192\n"
"v_mfma_f32_16x16x32_bf16 a[4:7], a[44:47], a[156:159], a[4:7]\n"
"v_mfma_f32_16x16x32_bf16 a[0:3], a[36:39], a[156:159], a[0:3]\n"
"s_waitcnt lgkmcnt(0)\n"
"global_store_dwordx4 v0, v[28:31], s[52:53] offset:3072 sc1\n"
"v_mfma_f32_16x16x32_bf16 a[0:3], a[32:35], a[152:155], a[0:3]\n"
"s_waitcnt vmcnt(14)\n"
"v_mfma_f32_16x16x32_bf16 v[88:91], a[48:51], a[96:99], v[88:91]\n"
"v_mfma_f32_16x16x32_bf16 v[88:91], a[52:55], a[100:103], v[88:91]\n"
"global_load_dwordx4 a[32:35], v0, s[34:35] offset:-4096 sc1\n"
"v_mfma_f32_16x16x32_bf16 v[116:119], a[60:63], a[100:103], v[116:119]\n"
"v_mfma_f32_16x16x32_bf16 v[116:119], a[56:59], a[96:99], v[116:119]\n"
"v_mfma_f32_16x16x32_bf16 v[120:123], a[56:59], a[104:107], v[120:123]\n"
"v_mfma_f32_16x16x32_bf16 v[120:123], a[60:63], a[108:111], v[120:123]\n"
"v_mfma_f32_16x16x32_bf16 v[92:95], a[52:55], a[108:111], v[92:95]\n"
"v_mfma_f32_16x16x32_bf16 v[92:95], a[48:51], a[104:107], v[92:95]\n"
"v_mfma_f32_16x16x32_bf16 v[96:99], a[48:51], a[112:115], v[96:99]\n"
"v_mfma_f32_16x16x32_bf16 v[96:99], a[52:55], a[116:119], v[96:99]\n"
"global_load_dwordx4 a[36:39], v0, s[34:35] offset:-3072 sc1\n"
"v_mfma_f32_16x16x32_bf16 v[124:127], a[60:63], a[116:119], v[124:127]\n"
"v_mfma_f32_16x16x32_bf16 v[124:127], a[56:59], a[112:115], v[124:127]\n"
"v_mfma_f32_16x16x32_bf16 v[128:131], a[56:59], a[120:123], v[128:131]\n"
"v_mfma_f32_16x16x32_bf16 v[128:131], a[60:63], a[124:127], v[128:131]\n"
"v_mfma_f32_16x16x32_bf16 v[100:103], a[52:55], a[124:127], v[100:103]\n"
"v_mfma_f32_16x16x32_bf16 v[100:103], a[48:51], a[120:123], v[100:103]\n"
"v_mfma_f32_16x16x32_bf16 v[104:107], a[48:51], a[128:131], v[104:107]\n"
"ds_read_b128 v[24:27], v2 offset:24576\n"
"v_mfma_f32_16x16x32_bf16 v[104:107], a[52:55], a[132:135], v[104:107]\n"
"global_load_dwordx4 a[40:43], v0, s[34:35] offset:-2048 sc1\n"
"v_mfma_f32_16x16x32_bf16 v[132:135], a[60:63], a[132:135], v[132:135]\n"
"v_mfma_f32_16x16x32_bf16 v[132:135], a[56:59], a[128:131], v[132:135]\n"
"v_mfma_f32_16x16x32_bf16 v[136:139], a[56:59], a[136:139], v[136:139]\n"
"v_mfma_f32_16x16x32_bf16 v[136:139], a[60:63], a[140:143], v[136:139]\n"
"v_mfma_f32_16x16x32_bf16 v[108:111], a[52:55], a[140:143], v[108:111]\n"
"v_mfma_f32_16x16x32_bf16 v[108:111], a[48:51], a[136:139], v[108:111]\n"
"v_mfma_f32_16x16x32_bf16 v[112:115], a[48:51], a[144:147], v[112:115]\n"
"v_mfma_f32_16x16x32_bf16 v[112:115], a[52:55], a[148:151], v[112:115]\n"
"global_load_dwordx4 a[44:47], v0, s[34:35] offset:-1024 sc1\n"
"v_mfma_f32_16x16x32_bf16 v[140:143], a[60:63], a[148:151], v[140:143]\n"
"v_mfma_f32_16x16x32_bf16 v[140:143], a[56:59], a[144:147], v[140:143]\n"
"v_mfma_f32_16x16x32_bf16 a[12:15], a[56:59], a[152:155], a[12:15]\n"
"s_waitcnt lgkmcnt(0)\n"
"global_store_dwordx4 v0, v[24:27], s[54:55] offset:-4096 sc1\n"
"v_mfma_f32_16x16x32_bf16 a[12:15], a[60:63], a[156:159], a[12:15]\n"
"v_mfma_f32_16x16x32_bf16 a[8:11], a[52:55], a[156:159], a[8:11]\n"
"v_mfma_f32_16x16x32_bf16 a[8:11], a[48:51], a[152:155], a[8:11]\n"
"s_waitcnt vmcnt(14)\n"
"v_mfma_f32_16x16x32_bf16 v[144:147], a[64:67], a[96:99], v[144:147]\n"
"v_mfma_f32_16x16x32_bf16 v[144:147], a[68:71], a[100:103], v[144:147]\n"
"global_load_dwordx4 a[48:51], v0, s[34:35] offset:0 sc1\n"
"v_mfma_f32_16x16x32_bf16 v[172:175], a[76:79], a[100:103], v[172:175]\n"
"v_mfma_f32_16x16x32_bf16 v[172:175], a[72:75], a[96:99], v[172:175]\n"
"v_mfma_f32_16x16x32_bf16 v[176:179], a[72:75], a[104:107], v[176:179]\n"
"v_mfma_f32_16x16x32_bf16 v[176:179], a[76:79], a[108:111], v[176:179]\n"
"v_mfma_f32_16x16x32_bf16 v[148:151], a[68:71], a[108:111], v[148:151]\n"
"v_mfma_f32_16x16x32_bf16 v[148:151], a[64:67], a[104:107], v[148:151]\n"
"v_mfma_f32_16x16x32_bf16 v[152:155], a[64:67], a[112:115], v[152:155]\n"
"v_mfma_f32_16x16x32_bf16 v[152:155], a[68:71], a[116:119], v[152:155]\n"
"global_load_dwordx4 a[52:55], v0, s[34:35] offset:1024 sc1\n"
"v_mfma_f32_16x16x32_bf16 v[180:183], a[76:79], a[116:119], v[180:183]\n"
"v_mfma_f32_16x16x32_bf16 v[180:183], a[72:75], a[112:115], v[180:183]\n"
"v_mfma_f32_16x16x32_bf16 v[184:187], a[72:75], a[120:123], v[184:187]\n"
"v_mfma_f32_16x16x32_bf16 v[184:187], a[76:79], a[124:127], v[184:187]\n"
"v_mfma_f32_16x16x32_bf16 v[156:159], a[68:71], a[124:127], v[156:159]\n"
"v_mfma_f32_16x16x32_bf16 v[156:159], a[64:67], a[120:123], v[156:159]\n"
"ds_read_b128 v[28:31], v2 offset:26624\n"
"v_mfma_f32_16x16x32_bf16 v[160:163], a[64:67], a[128:131], v[160:163]\n"
"v_mfma_f32_16x16x32_bf16 v[160:163], a[68:71], a[132:135], v[160:163]\n"
"global_load_dwordx4 a[56:59], v0, s[34:35] offset:2048 sc1\n"
"v_mfma_f32_16x16x32_bf16 v[188:191], a[76:79], a[132:135], v[188:191]\n"
"v_mfma_f32_16x16x32_bf16 v[188:191], a[72:75], a[128:131], v[188:191]\n"
"v_mfma_f32_16x16x32_bf16 v[192:195], a[72:75], a[136:139], v[192:195]\n"
"L_st6244:\n"
"s_cbranch_execz L_st8533\n"
"v_mfma_f32_16x16x32_bf16 v[192:195], a[76:79], a[140:143], v[192:195]\n"
"v_mfma_f32_16x16x32_bf16 v[164:167], a[68:71], a[140:143], v[164:167]\n"
"v_mfma_f32_16x16x32_bf16 v[164:167], a[64:67], a[136:139], v[164:167]\n"
"v_mfma_f32_16x16x32_bf16 v[168:171], a[64:67], a[144:147], v[168:171]\n"
"v_mfma_f32_16x16x32_bf16 v[168:171], a[68:71], a[148:151], v[168:171]\n"
"global_load_dwordx4 a[60:63], v0, s[34:35] offset:3072 sc1\n"
"v_mfma_f32_16x16x32_bf16 v[196:199], a[76:79], a[148:151], v[196:199]\n"
"s_waitcnt lgkmcnt(0)\n"
"global_store_dwordx4 v0, v[28:31], s[54:55] offset:-2048 sc1\n"
"v_mfma_f32_16x16x32_bf16 v[196:199], a[72:75], a[144:147], v[196:199]\n"
"v_mfma_f32_16x16x32_bf16 a[20:23], a[72:75], a[152:155], a[20:23]\n"
"s_add_u32 s34, s34, 8192\n"
"v_mfma_f32_16x16x32_bf16 a[20:23], a[76:79], a[156:159], a[20:23]\n"
"v_mfma_f32_16x16x32_bf16 a[16:19], a[68:71], a[156:159], a[16:19]\n"
"v_mfma_f32_16x16x32_bf16 a[16:19], a[64:67], a[152:155], a[16:19]\n"
"s_waitcnt vmcnt(12)\n"
"v_mfma_f32_16x16x32_bf16 v[200:203], a[80:83], a[96:99], v[200:203]\n"
"v_mfma_f32_16x16x32_bf16 v[200:203], a[84:87], a[100:103], v[200:203]\n"
"global_load_dwordx4 a[64:67], v0, s[34:35] offset:-4096 sc1\n"
"v_mfma_f32_16x16x32_bf16 v[228:231], a[92:95], a[100:103], v[228:231]\n"
"ds_read_b128 a[100:103], v3 offset:17408\n"
"v_mfma_f32_16x16x32_bf16 v[228:231], a[88:91], a[96:99], v[228:231]\n"
"ds_read_b128 a[96:99], v3 offset:16384\n"
"v_mfma_f32_16x16x32_bf16 v[232:235], a[88:91], a[104:107], v[232:235]\n"
"v_mfma_f32_16x16x32_bf16 v[232:235], a[92:95], a[108:111], v[232:235]\n"
"v_mfma_f32_16x16x32_bf16 v[204:207], a[84:87], a[108:111], v[204:207]\n"
"ds_read_b128 a[108:111], v3 offset:19456\n"
"v_mfma_f32_16x16x32_bf16 v[204:207], a[80:83], a[104:107], v[204:207]\n"
"ds_read_b128 a[104:107], v3 offset:18432\n"
"v_mfma_f32_16x16x32_bf16 v[208:211], a[80:83], a[112:115], v[208:211]\n"
"v_mfma_f32_16x16x32_bf16 v[208:211], a[84:87], a[116:119], v[208:211]\n"
"global_load_dwordx4 a[68:71], v0, s[34:35] offset:-3072 sc1\n"
"v_mfma_f32_16x16x32_bf16 v[236:239], a[92:95], a[116:119], v[236:239]\n"
"ds_read_b128 a[116:119], v3 offset:21504\n"
"v_mfma_f32_16x16x32_bf16 v[236:239], a[88:91], a[112:115], v[236:239]\n"
"ds_read_b128 a[112:115], v3 offset:20480\n"
"v_mfma_f32_16x16x32_bf16 v[240:243], a[88:91], a[120:123], v[240:243]\n"
"v_mfma_f32_16x16x32_bf16 v[240:243], a[92:95], a[124:127], v[240:243]\n"
"ds_read_b128 v[24:27], v2 offset:29696\n"
"v_mfma_f32_16x16x32_bf16 v[212:215], a[84:87], a[124:127], v[212:215]\n"
"ds_read_b128 a[124:127], v3 offset:23552\n"
"v_mfma_f32_16x16x32_bf16 v[212:215], a[80:83], a[120:123], v[212:215]\n"
"ds_read_b128 a[120:123], v3 offset:22528\n"
"v_mfma_f32_16x16x32_bf16 v[216:219], a[80:83], a[128:131], v[216:219]\n"
"v_mfma_f32_16x16x32_bf16 v[216:219], a[84:87], a[132:135], v[216:219]\n"
"global_load_dwordx4 a[72:75], v0, s[34:35] offset:-2048 sc1\n"
"v_mfma_f32_16x16x32_bf16 v[244:247], a[92:95], a[132:135], v[244:247]\n"
"ds_read_b128 a[132:135], v3 offset:25600\n"
"v_mfma_f32_16x16x32_bf16 v[244:247], a[88:91], a[128:131], v[244:247]\n"
"ds_read_b128 a[128:131], v3 offset:24576\n"
"v_mfma_f32_16x16x32_bf16 v[248:251], a[88:91], a[136:139], v[248:251]\n"
"v_subrev_u32 v8, s65, v8\n"
"v_mfma_f32_16x16x32_bf16 v[248:251], a[92:95], a[140:143], v[248:251]\n"
"v_xor_b32 v9, s79, v9\n"
"v_mfma_f32_16x16x32_bf16 v[220:223], a[84:87], a[140:143], v[220:223]\n"
"ds_read_b128 a[140:143], v3 offset:27648\n"
"v_or_b32 v8, v8, v9\n"
"v_mfma_f32_16x16x32_bf16 v[220:223], a[80:83], a[136:139], v[220:223]\n"
"ds_read_b128 a[136:139], v3 offset:26624\n"
"v_cmp_lt_u32 vcc, 15, v8\n"
"v_mfma_f32_16x16x32_bf16 v[224:227], a[80:83], a[144:147], v[224:227]\n"
"s_waitcnt lgkmcnt(6)\n"
"global_store_dwordx4 v0, v[24:27], s[54:55] offset:1024 sc1\n"
"s_cbranch_vccnz L_slow0\n"
"L_back0:\n"
"v_mfma_f32_16x16x32_bf16 v[224:227], a[84:87], a[148:151], v[224:227]\n"
"global_load_dwordx4 a[76:79], v0, s[34:35] offset:-1024 sc1\n"
"v_readfirstlane_b32 s66, v9\n"
"v_mfma_f32_16x16x32_bf16 v[252:255], a[92:95], a[148:151], v[252:255]\n"
"ds_read_b128 a[148:151], v3 offset:29696\n"
"v_mfma_f32_16x16x32_bf16 v[252:255], a[88:91], a[144:147], v[252:255]\n"
"s_and_b32 s66, s66, 3\n"
"ds_read_b128 a[144:147], v3 offset:28672\n"
"v_mfma_f32_16x16x32_bf16 a[28:31], a[88:91], a[152:155], a[28:31]\n"
"s_lshl_b32 s69, s66, 13\n"
"v_mfma_f32_16x16x32_bf16 a[28:31], a[92:95], a[156:159], a[28:31]\n"
"s_cmp_eq_u32 s66, 3\n"
"s_cselect_b32 s75, 8192, 0\n"
"v_mfma_f32_16x16x32_bf16 a[24:27], a[84:87], a[156:159], a[24:27]\n"
"s_and_b32 s29, s66, s30\n"
"ds_read_b128 a[156:159], v3 offset:31744\n"
"v_mfma_f32_16x16x32_bf16 a[24:27], a[80:83], a[152:155], a[24:27]\n"
"s_waitcnt vmcnt(11)\n"
"ds_read_b128 a[152:155], v3 offset:30720\n"
"v_mfma_f32_16x16x32_bf16 v[32:35], a[32:35], a[96:99], v[32:35]\n"
"s_xor_b32 s31, s79, s66\n"
"v_mfma_f32_16x16x32_bf16 v[32:35], a[36:39], a[100:103], v[32:35]\n"
"global_load_dwordx4 a[80:83], v0, s[34:35] offset:0 sc1\n"
"v_mfma_f32_16x16x32_bf16 v[60:63], a[44:47], a[100:103], v[60:63]\n"
"v_mfma_f32_16x16x32_bf16 v[60:63], a[40:43], a[96:99], v[60:63]\n"
"v_mfma_f32_16x16x32_bf16 v[64:67], a[40:43], a[104:107], v[64:67]\n"
"v_mfma_f32_16x16x32_bf16 v[64:67], a[44:47], a[108:111], v[64:67]\n"
"v_mfma_f32_16x16x32_bf16 v[36:39], a[36:39], a[108:111], v[36:39]\n"
"s_add_u32 m0, s64, 0\n"
"v_mfma_f32_16x16x32_bf16 v[36:39], a[32:35], a[104:107], v[36:39]\n"
"s_waitcnt lgkmcnt(8)\n"
"v_mfma_f32_16x16x32_bf16 v[40:43], a[32:35], a[112:115], v[40:43]\n"
"v_mfma_f32_16x16x32_bf16 v[40:43], a[36:39], a[116:119], v[40:43]\n"
"global_load_dwordx4 a[84:87], v0, s[34:35] offset:1024 sc1\n"
"v_mfma_f32_16x16x32_bf16 v[68:71], a[44:47], a[116:119], v[68:71]\n"
"v_mfma_f32_16x16x32_bf16 v[68:71], a[40:43], a[112:115], v[68:71]\n"
"v_mfma_f32_16x16x32_bf16 v[72:75], a[40:43], a[120:123], v[72:75]\n"
"v_mfma_f32_16x16x32_bf16 v[72:75], a[44:47], a[124:127], v[72:75]\n"
"v_mfma_f32_16x16x32_bf16 v[44:47], a[36:39], a[124:127], v[44:47]\n"
"v_mfma_f32_16x16x32_bf16 v[44:47], a[32:35], a[120:123], v[44:47]\n"
"s_waitcnt lgkmcnt(4)\n"
"v_mfma_f32_16x16x32_bf16 v[48:51], a[32:35], a[128:131], v[48:51]\n"
"v_mfma_f32_16x16x32_bf16 v[48:51], a[36:39], a[132:135], v[48:51]\n"
"global_load_dwordx4 a[88:91], v0, s[34:35] offset:2048 sc1\n"
"v_mfma_f32_16x16x32_bf16 v[76:79], a[44:47], a[132:135], v[76:79]\n"
"v_mfma_f32_16x16x32_bf16 v[76:79], a[40:43], a[128:131], v[76:79]\n"
"v_mfma_f32_16x16x32_bf16 v[80:83], a[40:43], a[136:139], v[80:83]\n"
"v_mfma_f32_16x16x32_bf16 v[80:83], a[44:47], a[140:143], v[80:83]\n"
"buffer_load_dwordx4 v0, s[48:51], s75 offen offset:0 sc1 lds\n"
"v_mfma_f32_16x16x32_bf16 v[52:55], a[36:39], a[140:143], v[52:55]\n"
"v_mfma_f32_16x16x32_bf16 v[52:55], a[32:35], a[136:139], v[52:55]\n"
"s_waitcnt lgkmcnt(0)\n"
"v_mfma_f32_16x16x32_bf16 v[56:59], a[32:35], a[144:147], v[56:59]\n"
"v_mfma_f32_16x16x32_bf16 v[56:59], a[36:39], a[148:151], v[56:59]\n"
"global_load_dwordx4 a[92:95], v0, s[34:35] offset:3072 sc1\n"
"v_mfma_f32_16x16x32_bf16 v[84:87], a[44:47], a[148:151], v[84:87]\n"
"v_mfma_f32_16x16x32_bf16 v[84:87], a[40:43], a[144:147], v[84:87]\n"
"v_mfma_f32_16x16x32_bf16 a[4:7], a[40:43], a[152:155], a[4:7]\n"
"s_add_u32 s34, s34, 8192\n"
"v_mfma_f32_16x16x32_bf16 a[4:7], a[44:47], a[156:159], a[4:7]\n"
"v_mfma_f32_16x16x32_bf16 a[0:3], a[36:39], a[156:159], a[0:3]\n"
"v_mfma_f32_16x16x32_bf16 a[0:3], a[32:35], a[152:155], a[0:3]\n"
"s_waitcnt vmcnt(11)\n"
"v_mfma_f32_16x16x32_bf16 v[88:91], a[48:51], a[96:99], v[88:91]\n"
"v_mfma_f32_16x16x32_bf16 v[88:91], a[52:55], a[100:103], v[88:91]\n"
"global_load_dwordx4 a[32:35], v0, s[34:35] offset:-4096 sc1\n"
"v_mfma_f32_16x16x32_bf16 v[116:119], a[60:63], a[100:103], v[116:119]\n"
"v_mfma_f32_16x16x32_bf16 v[116:119], a[56:59], a[96:99], v[116:119]\n"
"buffer_load_dwordx4 v0, s[48:51], s75 offen offset:1024 sc1 lds\n"
"v_mfma_f32_16x16x32_bf16 v[120:123], a[56:59], a[104:107], v[120:123]\n"
"v_mfma_f32_16x16x32_bf16 v[120:123], a[60:63], a[108:111], v[120:123]\n"
"v_mfma_f32_16x16x32_bf16 v[92:95], a[52:55], a[108:111], v[92:95]\n"
"v_mfma_f32_16x16x32_bf16 v[92:95], a[48:51], a[104:107], v[92:95]\n"
"v_mfma_f32_16x16x32_bf16 v[96:99], a[48:51], a[112:115], v[96:99]\n"
"v_mfma_f32_16x16x32_bf16 v[96:99], a[52:55], a[116:119], v[96:99]\n"
"global_load_dwordx4 a[36:39], v0, s[34:35] offset:-3072 sc1\n"
"v_mfma_f32_16x16x32_bf16 v[124:127], a[60:63], a[116:119], v[124:127]\n"
"v_mfma_f32_16x16x32_bf16 v[124:127], a[56:59], a[112:115], v[124:127]\n"
"v_mfma_f32_16x16x32_bf16 v[128:131], a[56:59], a[120:123], v[128:131]\n"
"v_mfma_f32_16x16x32_bf16 v[128:131], a[60:63], a[124:127], v[128:131]\n"
"v_mfma_f32_16x16x32_bf16 v[100:103], a[52:55], a[124:127], v[100:103]\n"
"v_mfma_f32_16x16x32_bf16 v[100:103], a[48:51], a[120:123], v[100:103]\n"
"v_mfma_f32_16x16x32_bf16 v[104:107], a[48:51], a[128:131], v[104:107]\n"
"v_mfma_f32_16x16x32_bf16 v[104:107], a[52:55], a[132:135], v[104:107]\n"
"global_load_dwordx4 a[40:43], v0, s[34:35] offset:-2048 sc1\n"
"v_mfma_f32_16x16x32_bf16 v[132:135], a[60:63], a[132:135], v[132:135]\n"
"buffer_load_dwordx4 v0, s[48:51], s75 offen offset:2048 sc1 lds\n"
"v_mfma_f32_16x16x32_bf16 v[132:135], a[56:59], a[128:131], v[132:135]\n"
"v_mfma_f32_16x16x32_bf16 v[136:139], a[56:59], a[136:139], v[136:139]\n"
"v_mfma_f32_16x16x32_bf16 v[136:139], a[60:63], a[140:143], v[136:139]\n"
"v_mfma_f32_16x16x32_bf16 v[108:111], a[52:55], a[140:143], v[108:111]\n"
"v_mfma_f32_16x16x32_bf16 v[108:111], a[48:51], a[136:139], v[108:111]\n"
"v_mfma_f32_16x16x32_bf16 v[112:115], a[48:51], a[144:147], v[112:115]\n"
"v_mfma_f32_16x16x32_bf16 v[112:115], a[52:55], a[148:151], v[112:115]\n"
"global_load_dwordx4 a[44:47], v0, s[34:35] offset:-1024 sc1\n"
"v_mfma_f32_16x16x32_bf16 v[140:143], a[60:63], a[148:151], v[140:143]\n"
"v_mfma_f32_16x16x32_bf16 v[140:143], a[56:59], a[144:147], v[140:143]\n"
"v_mfma_f32_16x16x32_bf16 a[12:15], a[56:59], a[152:155], a[12:15]\n"
"v_mfma_f32_16x16x32_bf16 a[12:15], a[60:63], a[156:159], a[12:15]\n"
"v_mfma_f32_16x16x32_bf16 a[8:11], a[52:55], a[156:159], a[8:11]\n"
"v_mfma_f32_16x16x32_bf16 a[8:11], a[48:51], a[152:155], a[8:11]\n"
"s_waitcnt vmcnt(11)\n"
"v_mfma_f32_16x16x32_bf16 v[144:147], a[64:67], a[96:99], v[144:147]\n"
"buffer_load_dwordx4 v0, s[48:51], s75 offen offset:3072 sc1 lds\n"
"v_mfma_f32_16x16x32_bf16 v[144:147], a[68:71], a[100:103], v[144:147]\n"
"global_load_dwordx4 a[48:51], v0, s[34:35] offset:0 sc1\n"
"v_mfma_f32_16x16x32_bf16 v[172:175], a[76:79], a[100:103], v[172:175]\n"
"v_mfma_f32_16x16x32_bf16 v[172:175], a[72:75], a[96:99], v[172:175]\n"
"v_mfma_f32_16x16x32_bf16 v[176:179], a[72:75], a[104:107], v[176:179]\n"
"v_mfma_f32_16x16x32_bf16 v[176:179], a[76:79], a[108:111], v[176:179]\n"
"v_mfma_f32_16x16x32_bf16 v[148:151], a[68:71], a[108:111], v[148:151]\n"
"v_mfma_f32_16x16x32_bf16 v[148:151], a[64:67], a[104:107], v[148:151]\n"
"v_mfma_f32_16x16x32_bf16 v[152:155], a[64:67], a[112:115], v[152:155]\n"
"v_mfma_f32_16x16x32_bf16 v[152:155], a[68:71], a[116:119], v[152:155]\n"
"global_load_dwordx4 a[52:55], v0, s[34:35] offset:1024 sc1\n"
"v_mfma_f32_16x16x32_bf16 v[180:183], a[76:79], a[116:119], v[180:183]\n"
"v_mfma_f32_16x16x32_bf16 v[180:183], a[72:75], a[112:115], v[180:183]\n"
"v_mfma_f32_16x16x32_bf16 v[184:187], a[72:75], a[120:123], v[184:187]\n"
"v_mfma_f32_16x16x32_bf16 v[184:187], a[76:79], a[124:127], v[184:187]\n"
"v_mfma_f32_16x16x32_bf16 v[156:159], a[68:71], a[124:127], v[156:159]\n"
"v_mfma_f32_16x16x32_bf16 v[156:159], a[64:67], a[120:123], v[156:159]\n"
"s_add_u32 m0, s64, 4096\n"
"v_mfma_f32_16x16x32_bf16 v[160:163], a[64:67], a[128:131], v[160:163]\n"
"v_mfma_f32_16x16x32_bf16 v[160:163], a[68:71], a[132:135], v[160:163]\n"
"global_load_dwordx4 a[56:59], v0, s[34:35] offset:2048 sc1\n"
"v_mfma_f32_16x16x32_bf16 v[188:191], a[76:79], a[132:135], v[188:191]\n"
"v_mfma_f32_16x16x32_bf16 v[188:191], a[72:75], a[128:131], v[188:191]\n"
"v_mfma_f32_16x16x32_bf16 v[192:195], a[72:75], a[136:139], v[192:195]\n"
"v_mfma_f32_16x16x32_bf16 v[192:195], a[76:79], a[140:143], v[192:195]\n"
"v_mfma_f32_16x16x32_bf16 v[164:167], a[68:71], a[140:143], v[164:167]\n"
"v_mfma_f32_16x16x32_bf16 v[164:167], a[64:67], a[136:139], v[164:167]\n"
"v_mfma_f32_16x16x32_bf16 v[168:171], a[64:67], a[144:147], v[168:171]\n"
"v_mfma_f32_16x16x32_bf16 v[168:171], a[68:71], a[148:151], v[168:171]\n"
"global_load_dwordx4 a[60:63], v0, s[34:35] offset:3072 sc1\n"
"v_mfma_f32_16x16x32_bf16 v[196:199], a[76:79], a[148:151], v[196:199]\n"
"v_mfma_f32_16x16x32_bf16 v[196:199], a[72:75], a[144:147], v[196:199]\n"
"v_mfma_f32_16x16x32_bf16 a[20:23], a[72:75], a[152:155], a[20:23]\n"
"s_add_u32 s34, s34, 8192\n"
"v_mfma_f32_16x16x32_bf16 a[20:23], a[76:79], a[156:159], a[20:23]\n"
"v_mfma_f32_16x16x32_bf16 a[16:19], a[68:71], a[156:159], a[16:19]\n"
"buffer_load_dwordx4 v1, s[48:51], s75 offen offset:0 sc1 lds\n"
"v_mfma_f32_16x16x32_bf16 a[16:19], a[64:67], a[152:155], a[16:19]\n"
"s_waitcnt vmcnt(12)\n"
"v_mfma_f32_16x16x32_bf16 v[200:203], a[80:83], a[96:99], v[200:203]\n"
"v_mfma_f32_16x16x32_bf16 v[200:203], a[84:87], a[100:103], v[200:203]\n"
"global_load_dwordx4 a[64:67], v0, s[34:35] offset:-4096 sc1\n"
"s_add_u32 s65, s65, 1\n"
"v_mfma_f32_16x16x32_bf16 v[228:231], a[92:95], a[100:103], v[228:231]\n"
"ds_read_b128 a[100:103], v4 offset:17408\n"
"v_mfma_f32_16x16x32_bf16 v[228:231], a[88:91], a[96:99], v[228:231]\n"
"ds_read_b128 a[96:99], v4 offset:16384\n"
"v_mov_b32 v8, s65\n"
"v_mfma_f32_16x16x32_bf16 v[232:235], a[88:91], a[104:107], v[232:235]\n"
"v_mov_b32 v9, s60\n"
"v_mfma_f32_16x16x32_bf16 v[232:235], a[92:95], a[108:111], v[232:235]\n"
"v_mfma_f32_16x16x32_bf16 v[204:207], a[84:87], a[108:111], v[204:207]\n"
"buffer_store_dwordx2 v[8:9], off, s[56:59], %13 sc1\n"
"ds_read_b128 a[108:111], v4 offset:19456\n"
"v_mfma_f32_16x16x32_bf16 v[204:207], a[80:83], a[104:107], v[204:207]\n"
"ds_read_b128 a[104:107], v4 offset:18432\n"
"v_mfma_f32_16x16x32_bf16 v[208:211], a[80:83], a[112:115], v[208:211]\n"
"v_mfma_f32_16x16x32_bf16 v[208:211], a[84:87], a[116:119], v[208:211]\n"
"global_load_dwordx4 a[68:71], v0, s[34:35] offset:-3072 sc1\n"
"v_mfma_f32_16x16x32_bf16 v[236:239], a[92:95], a[116:119], v[236:239]\n"
"ds_read_b128 a[116:119], v4 offset:21504\n"
"v_mfma_f32_16x16x32_bf16 v[236:239], a[88:91], a[112:115], v[236:239]\n"
"ds_read_b128 a[112:115], v4 offset:20480\n"
"v_mfma_f32_16x16x32_bf16 v[240:243], a[88:91], a[120:123], v[240:243]\n"
"v_mfma_f32_16x16x32_bf16 v[240:243], a[92:95], a[124:127], v[240:243]\n"
"buffer_load_dwordx4 v1, s[48:51], s75 offen offset:1024 sc1 lds\n"
"v_mfma_f32_16x16x32_bf16 v[212:215], a[84:87], a[124:127], v[212:215]\n"
"ds_read_b128 a[124:127], v4 offset:23552\n"
"v_mfma_f32_16x16x32_bf16 v[212:215], a[80:83], a[120:123], v[212:215]\n"
"ds_read_b128 a[120:123], v4 offset:22528\n"
"v_mfma_f32_16x16x32_bf16 v[216:219], a[80:83], a[128:131], v[216:219]\n"
"v_mfma_f32_16x16x32_bf16 v[216:219], a[84:87], a[132:135], v[216:219]\n"
"global_load_dwordx4 a[72:75], v0, s[34:35] offset:-2048 sc1\n"
"v_mfma_f32_16x16x32_bf16 v[244:247], a[92:95], a[132:135], v[244:247]\n"
"ds_read_b128 a[132:135], v4 offset:25600\n"
"v_mfma_f32_16x16x32_bf16 v[244:247], a[88:91], a[128:131], v[244:247]\n"
"ds_read_b128 a[128:131], v4 offset:24576\n"
"v_mfma_f32_16x16x32_bf16 v[248:251], a[88:91], a[136:139], v[248:251]\n"
"v_mfma_f32_16x16x32_bf16 v[248:251], a[92:95], a[140:143], v[248:251]\n"
"v_mfma_f32_16x16x32_bf16 v[220:223], a[84:87], a[140:143], v[220:223]\n"
"ds_read_b128 a[140:143], v4 offset:27648\n"
"v_mfma_f32_16x16x32_bf16 v[220:223], a[80:83], a[136:139], v[220:223]\n"
"ds_read_b128 a[136:139], v4 offset:26624\n"
"v_mfma_f32_16x16x32_bf16 v[224:227], a[80:83], a[144:147], v[224:227]\n"
"v_mfma_f32_16x16x32_bf16 v[224:227], a[84:87], a[148:151], v[224:227]\n"
"global_load_dwordx4 a[76:79], v0, s[34:35] offset:-1024 sc1\n"
"v_mfma_f32_16x16x32_bf16 v[252:255], a[92:95], a[148:151], v[252:255]\n"
"ds_read_b128 a[148:151], v4 offset:29696\n"
"v_mfma_f32_16x16x32_bf16 v[252:255], a[88:91], a[144:147], v[252:255]\n"
"ds_read_b128 a[144:147], v4 offset:28672\n"
"v_mfma_f32_16x16x32_bf16 a[28:31], a[88:91], a[152:155], a[28:31]\n"
"buffer_load_dwordx4 v1, s[48:51], s75 offen offset:2048 sc1 lds\n"
"v_mfma_f32_16x16x32_bf16 a[28:31], a[92:95], a[156:159], a[28:31]\n"
"v_mfma_f32_16x16x32_bf16 a[24:27], a[84:87], a[156:159], a[24:27]\n"
"ds_read_b128 a[156:159], v4 offset:31744\n"
"v_mfma_f32_16x16x32_bf16 a[24:27], a[80:83], a[152:155], a[24:27]\n"
"s_waitcnt vmcnt(13) lgkmcnt(11)\n"
"ds_read_b128 a[152:155], v4 offset:30720\n"
"v_mfma_f32_16x16x32_bf16 v[32:35], a[32:35], a[96:99], v[32:35]\n"
"v_mfma_f32_16x16x32_bf16 v[32:35], a[36:39], a[100:103], v[32:35]\n"
"global_load_dwordx4 a[80:83], v0, s[34:35] offset:0 sc1\n"
"v_mfma_f32_16x16x32_bf16 v[60:63], a[44:47], a[100:103], v[60:63]\n"
"v_mfma_f32_16x16x32_bf16 v[60:63], a[40:43], a[96:99], v[60:63]\n"
"v_mfma_f32_16x16x32_bf16 v[64:67], a[40:43], a[104:107], v[64:67]\n"
"v_mfma_f32_16x16x32_bf16 v[64:67], a[44:47], a[108:111], v[64:67]\n"
"v_mfma_f32_16x16x32_bf16 v[36:39], a[36:39], a[108:111], v[36:39]\n"
"v_mfma_f32_16x16x32_bf16 v[36:39], a[32:35], a[104:107], v[36:39]\n"
"s_waitcnt lgkmcnt(8)\n"
"v_mfma_f32_16x16x32_bf16 v[40:43], a[32:35], a[112:115], v[40:43]\n"
"v_mfma_f32_16x16x32_bf16 v[40:43], a[36:39], a[116:119], v[40:43]\n"
"global_load_dwordx4 a[84:87], v0, s[34:35] offset:1024 sc1\n"
"v_mfma_f32_16x16x32_bf16 v[68:71], a[44:47], a[116:119], v[68:71]\n"
"buffer_load_dwordx4 v1, s[48:51], s75 offen offset:3072 sc1 lds\n"
"v_mfma_f32_16x16x32_bf16 v[68:71], a[40:43], a[112:115], v[68:71]\n"
"v_mfma_f32_16x16x32_bf16 v[72:75], a[40:43], a[120:123], v[72:75]\n"
"v_mfma_f32_16x16x32_bf16 v[72:75], a[44:47], a[124:127], v[72:75]\n"
"v_mfma_f32_16x16x32_bf16 v[44:47], a[36:39], a[124:127], v[44:47]\n"
"v_mfma_f32_16x16x32_bf16 v[44:47], a[32:35], a[120:123], v[44:47]\n"
"s_waitcnt lgkmcnt(4)\n"
"v_mfma_f32_16x16x32_bf16 v[48:51], a[32:35], a[128:131], v[48:51]\n"
"v_mfma_f32_16x16x32_bf16 v[48:51], a[36:39], a[132:135], v[48:51]\n"
"global_load_dwordx4 a[88:91], v0, s[34:35] offset:2048 sc1\n"
"v_mfma_f32_16x16x32_bf16 v[76:79], a[44:47], a[132:135], v[76:79]\n"
"v_mfma_f32_16x16x32_bf16 v[76:79], a[40:43], a[128:131], v[76:79]\n"
"v_mfma_f32_16x16x32_bf16 v[80:83], a[40:43], a[136:139], v[80:83]\n"
"v_mfma_f32_16x16x32_bf16 v[80:83], a[44:47], a[140:143], v[80:83]\n"
"v_mfma_f32_16x16x32_bf16 v[52:55], a[36:39], a[140:143], v[52:55]\n"
"v_mfma_f32_16x16x32_bf16 v[52:55], a[32:35], a[136:139], v[52:55]\n"
"s_waitcnt lgkmcnt(0)\n"
"v_mfma_f32_16x16x32_bf16 v[56:59], a[32:35], a[144:147], v[56:59]\n"
"v_mfma_f32_16x16x32_bf16 v[56:59], a[36:39], a[148:151], v[56:59]\n"
"s_add_u32 m0, s64, 8192\n"
"global_load_dwordx4 a[92:95], v0, s[34:35] offset:3072 sc1\n"
"v_mfma_f32_16x16x32_bf16 v[84:87], a[44:47], a[148:151], v[84:87]\n"
"v_mfma_f32_16x16x32_bf16 v[84:87], a[40:43], a[144:147], v[84:87]\n"
"v_mfma_f32_16x16x32_bf16 a[4:7], a[40:43], a[152:155], a[4:7]\n"
"s_add_u32 s34, s34, 8192\n"
"v_mfma_f32_16x16x32_bf16 a[4:7], a[44:47], a[156:159], a[4:7]\n"
"v_mfma_f32_16x16x32_bf16 a[0:3], a[36:39], a[156:159], a[0:3]\n"
"v_mfma_f32_16x16x32_bf16 a[0:3], a[32:35], a[152:155], a[0:3]\n"
"s_waitcnt vmcnt(13)\n"
"v_mfma_f32_16x16x32_bf16 v[88:91], a[48:51], a[96:99], v[88:91]\n"
"v_mfma_f32_16x16x32_bf16 v[88:91], a[52:55], a[100:103], v[88:91]\n"
"global_load_dwordx4 a[32:35], v0, s[34:35] offset:-4096 sc1\n"
"v_mfma_f32_16x16x32_bf16 v[116:119], a[60:63], a[100:103], v[116:119]\n"
"v_mfma_f32_16x16x32_bf16 v[116:119], a[56:59], a[96:99], v[116:119]\n"
"v_mfma_f32_16x16x32_bf16 v[120:123], a[56:59], a[104:107], v[120:123]\n"
"v_mfma_f32_16x16x32_bf16 v[120:123], a[60:63], a[108:111], v[120:123]\n"
"v_mfma_f32_16x16x32_bf16 v[92:95], a[52:55], a[108:111], v[92:95]\n"
"v_mfma_f32_16x16x32_bf16 v[92:95], a[48:51], a[104:107], v[92:95]\n"
"v_mfma_f32_16x16x32_bf16 v[96:99], a[48:51], a[112:115], v[96:99]\n"
"buffer_load_dwordx4 v0, s[48:51], s69 offen offset:0 sc1 lds\n"
"v_mfma_f32_16x16x32_bf16 v[96:99], a[52:55], a[116:119], v[96:99]\n"
"global_load_dwordx4 a[36:39], v0, s[34:35] offset:-3072 sc1\n"
"v_mfma_f32_16x16x32_bf16 v[124:127], a[60:63], a[116:119], v[124:127]\n"
"v_mfma_f32_16x16x32_bf16 v[124:127], a[56:59], a[112:115], v[124:127]\n"
"v_mfma_f32_16x16x32_bf16 v[128:131], a[56:59], a[120:123], v[128:131]\n"
"v_mfma_f32_16x16x32_bf16 v[128:131], a[60:63], a[124:127], v[128:131]\n"
"v_mfma_f32_16x16x32_bf16 v[100:103], a[52:55], a[124:127], v[100:103]\n"
"v_mfma_f32_16x16x32_bf16 v[100:103], a[48:51], a[120:123], v[100:103]\n"
"v_mfma_f32_16x16x32_bf16 v[104:107], a[48:51], a[128:131], v[104:107]\n"
"v_mfma_f32_16x16x32_bf16 v[104:107], a[52:55], a[132:135], v[104:107]\n"
"global_load_dwordx4 a[40:43], v0, s[34:35] offset:-2048 sc1\n"
"v_mfma_f32_16x16x32_bf16 v[132:135], a[60:63], a[132:135], v[132:135]\n"
"v_mfma_f32_16x16x32_bf16 v[132:135], a[56:59], a[128:131], v[132:135]\n"
"v_mfma_f32_16x16x32_bf16 v[136:139], a[56:59], a[136:139], v[136:139]\n"
"v_mfma_f32_16x16x32_bf16 v[136:139], a[60:63], a[140:143], v[136:139]\n"
"v_mfma_f32_16x16x32_bf16 v[108:111], a[52:55], a[140:143], v[108:111]\n"
"buffer_load_dwordx4 v0, s[48:51], s69 offen offset:1024 sc1 lds\n"
"v_mfma_f32_16x16x32_bf16 v[108:111], a[48:51], a[136:139], v[108:111]\n"
"v_mfma_f32_16x16x32_bf16 v[112:115], a[48:51], a[144:147], v[112:115]\n"
"v_mfma_f32_16x16x32_bf16 v[112:115], a[52:55], a[148:151], v[112:115]\n"
"global_load_dwordx4 a[44:47], v0, s[34:35] offset:-1024 sc1\n"
"v_mfma_f32_16x16x32_bf16 v[140:143], a[60:63], a[148:151], v[140:143]\n"
"v_mfma_f32_16x16x32_bf16 v[140:143], a[56:59], a[144:147], v[140:143]\n"
"v_mfma_f32_16x16x32_bf16 a[12:15], a[56:59], a[152:155], a[12:15]\n"
"v_mfma_f32_16x16x32_bf16 a[12:15], a[60:63], a[156:159], a[12:15]\n"
"v_mfma_f32_16x16x32_bf16 a[8:11], a[52:55], a[156:159], a[8:11]\n"
"v_mfma_f32_16x16x32_bf16 a[8:11], a[48:51], a[152:155], a[8:11]\n"
"s_waitcnt vmcnt(12)\n"
"v_mfma_f32_16x16x32_bf16 v[144:147], a[64:67], a[96:99], v[144:147]\n"
"v_mfma_f32_16x16x32_bf16 v[144:147], a[68:71], a[100:103], v[144:147]\n"
"global_load_dwordx4 a[48:51], v0, s[34:35] offset:0 sc1\n"
"v_mfma_f32_16x16x32_bf16 v[172:175], a[76:79], a[100:103], v[172:175]\n"
"v_mfma_f32_16x16x32_bf16 v[172:175], a[72:75], a[96:99], v[172:175]\n"
"v_mfma_f32_16x16x32_bf16 v[176:179], a[72:75], a[104:107], v[176:179]\n"
"v_mfma_f32_16x16x32_bf16 v[176:179], a[76:79], a[108:111], v[176:179]\n"
"buffer_load_dwordx4 v0, s[48:51], s69 offen offset:2048 sc1 lds\n"
"v_mfma_f32_16x16x32_bf16 v[148:151], a[68:71], a[108:111], v[148:151]\n"
"v_mfma_f32_16x16x32_bf16 v[148:151], a[64:67], a[104:107], v[148:151]\n"
"v_mfma_f32_16x16x32_bf16 v[152:155], a[64:67], a[112:115], v[152:155]\n"
"v_lshlrev_b32 v16, 16, v18\n"
"v_mfma_f32_16x16x32_bf16 v[152:155], a[68:71], a[116:119], v[152:155]\n"
"global_load_dwordx4 a[52:55], v0, s[34:35] offset:1024 sc1\n"
"v_and_b32 v17, s63, v18\n"
"v_mfma_f32_16x16x32_bf16 v[180:183], a[76:79], a[116:119], v[180:183]\n"
"v_lshlrev_b32 v18, 16, v19\n"
"v_mfma_f32_16x16x32_bf16 v[180:183], a[72:75], a[112:115], v[180:183]\n"
"v_and_b32 v19, s63, v19\n"
"v_mfma_f32_16x16x32_bf16 v[184:187], a[72:75], a[120:123], v[184:187]\n"
"v_add_f32 v32, v32, v16\n"
"v_mfma_f32_16x16x32_bf16 v[184:187], a[76:79], a[124:127], v[184:187]\n"
"v_add_f32 v33, v33, v17\n"
"v_add_f32 v34, v34, v18\n"
"v_mfma_f32_16x16x32_bf16 v[156:159], a[68:71], a[124:127], v[156:159]\n"
"v_add_f32 v35, v35, v19\n"
"v_mfma_f32_16x16x32_bf16 v[156:159], a[64:67], a[120:123], v[156:159]\n"
"v_add_f32 v36, v36, v16\n"
"v_mfma_f32_16x16x32_bf16 v[160:163], a[64:67], a[128:131], v[160:163]\n"
"v_add_f32 v37, v37, v17\n"
"v_mfma_f32_16x16x32_bf16 v[160:163], a[68:71], a[132:135], v[160:163]\n"
"global_load_dwordx4 a[56:59], v0, s[34:35] offset:2048 sc1\n"
"v_add_f32 v38, v38, v18\n"
"v_mfma_f32_16x16x32_bf16 v[188:191], a[76:79], a[132:135], v[188:191]\n"
"v_add_f32 v39, v39, v19\n"
"v_mfma_f32_16x16x32_bf16 v[188:191], a[72:75], a[128:131], v[188:191]\n"
"v_add_f32 v40, v40, v16\n"
"v_add_f32 v41, v41, v17\n"
"v_mfma_f32_16x16x32_bf16 v[192:195], a[72:75], a[136:139], v[192:195]\n"
"buffer_load_dwordx4 v0, s[48:51], s69 offen offset:3072 sc1 lds\n"
"v_mfma_f32_16x16x32_bf16 v[192:195], a[76:79], a[140:143], v[192:195]\n"
"v_add_f32 v42, v42, v18\n"
"v_mfma_f32_16x16x32_bf16 v[164:167], a[68:71], a[140:143], v[164:167]\n"
"v_add_f32 v43, v43, v19\n"
"v_mfma_f32_16x16x32_bf16 v[164:167], a[64:67], a[136:139], v[164:167]\n"
"v_add_f32 v44, v44, v16\n"
"v_add_f32 v45, v45, v17\n"
"v_mfma_f32_16x16x32_bf16 v[168:171], a[64:67], a[144:147], v[168:171]\n"
"v_add_f32 v46, v46, v18\n"
"v_mfma_f32_16x16x32_bf16 v[168:171], a[68:71], a[148:151], v[168:171]\n"
"global_load_dwordx4 a[60:63], v0, s[34:35] offset:3072 sc1\n"
"v_add_f32 v47, v47, v19\n"
"v_mfma_f32_16x16x32_bf16 v[196:199], a[76:79], a[148:151], v[196:199]\n"
"v_add_f32 v48, v48, v16\n"
"v_mfma_f32_16x16x32_bf16 v[196:199], a[72:75], a[144:147], v[196:199]\n"
"v_add_f32 v49, v49, v17\n"
"v_mfma_f32_16x16x32_bf16 a[20:23], a[72:75], a[152:155], a[20:23]\n"
"s_add_u32 s34, s34, 8192\n"
"v_add_f32 v50, v50, v18\n"
"v_mfma_f32_16x16x32_bf16 a[20:23], a[76:79], a[156:159], a[20:23]\n"
"v_add_f32 v51, v51, v19\n"
"v_mfma_f32_16x16x32_bf16 a[16:19], a[68:71], a[156:159], a[16:19]\n"
"v_add_f32 v52, v52, v16\n"
"v_mfma_f32_16x16x32_bf16 a[16:19], a[64:67], a[152:155], a[16:19]\n"
"s_waitcnt vmcnt(12)\n"
"v_add_f32 v53, v53, v17\n"
"v_mfma_f32_16x16x32_bf16 v[200:203], a[80:83], a[96:99], v[200:203]\n"
"v_add_f32 v54, v54, v18\n"
"v_add_f32 v55, v55, v19\n"
"v_mfma_f32_16x16x32_bf16 v[200:203], a[84:87], a[100:103], v[200:203]\n"
"global_load_dwordx4 a[64:67], v0, s[34:35] offset:-4096 sc1\n"
"v_add_f32 v56, v56, v16\n"
"v_mfma_f32_16x16x32_bf16 v[228:231], a[92:95], a[100:103], v[228:231]\n"
"s_add_u32 m0, s64, 12288\n"
"ds_read_b128 a[100:103], v5 offset:17408\n"
"v_add_f32 v57, v57, v17\n"
"v_mfma_f32_16x16x32_bf16 v[228:231], a[88:91], a[96:99], v[228:231]\n"
"ds_read_b128 a[96:99], v5 offset:16384\n"
"v_add_f32 v58, v58, v18\n"
"v_mfma_f32_16x16x32_bf16 v[232:235], a[88:91], a[104:107], v[232:235]\n"
"v_add_f32 v59, v59, v19\n"
"v_mfma_f32_16x16x32_bf16 v[232:235], a[92:95], a[108:111], v[232:235]\n"
"v_accvgpr_read_b32 v12, a0\n"
"v_mfma_f32_16x16x32_bf16 v[204:207], a[84:87], a[108:111], v[204:207]\n"
"ds_read_b128 a[108:111], v5 offset:19456\n"
"v_accvgpr_read_b32 v13, a1\n"
"v_mfma_f32_16x16x32_bf16 v[204:207], a[80:83], a[104:107], v[204:207]\n"
"ds_read_b128 a[104:107], v5 offset:18432\n"
"v_accvgpr_read_b32 v14, a2\n"
"v_mfma_f32_16x16x32_bf16 v[208:211], a[80:83], a[112:115], v[208:211]\n"
"v_accvgpr_read_b32 v15, a3\n"
"v_mfma_f32_16x16x32_bf16 v[208:211], a[84:87], a[116:119], v[208:211]\n"
"global_load_dwordx4 a[68:71], v0, s[34:35] offset:-3072 sc1\n"
"v_add_f32 v12, v12, v16\n"
"v_mfma_f32_16x16x32_bf16 v[236:239], a[92:95], a[116:119], v[236:239]\n"
"ds_read_b128 a[116:119], v5 offset:21504\n"
"v_add_f32 v13, v13, v17\n"
"v_mfma_f32_16x16x32_bf16 v[236:239], a[88:91], a[112:115], v[236:239]\n"
"ds_read_b128 a[112:115], v5 offset:20480\n"
"v_add_f32 v14, v14, v18\n"
"v_mfma_f32_16x16x32_bf16 v[240:243], a[88:91], a[120:123], v[240:243]\n"
"v_add_f32 v15, v15, v19\n"
"v_accvgpr_write_b32 a0, v12\n"
"v_mfma_f32_16x16x32_bf16 v[240:243], a[92:95], a[124:127], v[240:243]\n"
"v_accvgpr_write_b32 a1, v13\n"
"v_mfma_f32_16x16x32_bf16 v[212:215], a[84:87], a[124:127], v[212:215]\n"
"ds_read_b128 a[124:127], v5 offset:23552\n"
"v_accvgpr_write_b32 a2, v14\n"
"v_mfma_f32_16x16x32_bf16 v[212:215], a[80:83], a[120:123], v[212:215]\n"
"ds_read_b128 a[120:123], v5 offset:22528\n"
"v_accvgpr_write_b32 a3, v15\n"
"v_mfma_f32_16x16x32_bf16 v[216:219], a[80:83], a[128:131], v[216:219]\n"
"buffer_load_dwordx4 v1, s[48:51], s69 offen offset:0 sc1 lds\n"
"v_mfma_f32_16x16x32_bf16 v[216:219], a[84:87], a[132:135], v[216:219]\n"
"global_load_dwordx4 a[72:75], v0, s[34:35] offset:-2048 sc1\n"
"v_lshrrev_b32 v16, 4, v0\n"
"v_mfma_f32_16x16x32_bf16 v[244:247], a[92:95], a[132:135], v[244:247]\n"
"ds_read_b128 a[132:135], v5 offset:25600\n"
"v_and_b32 v16, 0x30, v16\n"
"v_mfma_f32_16x16x32_bf16 v[244:247], a[88:91], a[128:131], v[244:247]\n"
"ds_read_b128 a[128:131], v5 offset:24576\n"
"buffer_load_dwordx2 v[18:19], v16, s[44:47], s61 offen offset:64\n"
"v_mfma_f32_16x16x32_bf16 v[248:251], a[88:91], a[136:139], v[248:251]\n"
"s_getpc_b64 s[96:97]\n"
"L_c3_:\n"
"s_add_u32 s96, s96, L_last-L_c3_\n"
"s_addc_u32 s97, s97, 0\n"
"v_mfma_f32_16x16x32_bf16 v[248:251], a[92:95], a[140:143], v[248:251]\n"
"s_lshr_b32 s66, %26, 3\n"
"v_mfma_f32_16x16x32_bf16 v[220:223], a[84:87], a[140:143], v[220:223]\n"
"s_lshl2_add_u32 s66, s66, %16\n"
"ds_read_b128 a[140:143], v5 offset:27648\n"
"v_mfma_f32_16x16x32_bf16 v[220:223], a[80:83], a[136:139], v[220:223]\n"
"ds_read_b128 a[136:139], v5 offset:26624\n"
"v_mfma_f32_16x16x32_bf16 v[224:227], a[80:83], a[144:147], v[224:227]\n"
"s_mul_i32 s66, s66, 256\n"
"v_mfma_f32_16x16x32_bf16 v[224:227], a[84:87], a[148:151], v[224:227]\n"
"global_load_dwordx4 a[76:79], v0, s[34:35] offset:-1024 sc1\n"
"v_mfma_f32_16x16x32_bf16 v[252:255], a[92:95], a[148:151], v[252:255]\n"
"ds_read_b128 a[148:151], v5 offset:29696\n"
"v_mfma_f32_16x16x32_bf16 v[252:255], a[88:91], a[144:147], v[252:255]\n"
"s_min_u32 s66, s66, L_headF-L_last-256\n"
"ds_read_b128 a[144:147], v5 offset:28672\n"
"v_mfma_f32_16x16x32_bf16 a[28:31], a[88:91], a[152:155], a[28:31]\n"
"v_mfma_f32_16x16x32_bf16 a[28:31], a[92:95], a[156:159], a[28:31]\n"
"v_min_u32 v11, 16, v0\n"
"v_mfma_f32_16x16x32_bf16 a[24:27], a[84:87], a[156:159], a[24:27]\n"
"ds_read_b128 a[156:159], v5 offset:31744\n"
"v_mfma_f32_16x16x32_bf16 a[24:27], a[80:83], a[152:155], a[24:27]\n"
"s_waitcnt vmcnt(12) lgkmcnt(11)\n"
"ds_read_b128 a[152:155], v5 offset:30720\n"
"v_mfma_f32_16x16x32_bf16 v[32:35], a[32:35], a[96:99], v[32:35]\n"
"buffer_load_dwordx4 v1, s[48:51], s69 offen offset:1024 sc1 lds\n"
"v_mfma_f32_16x16x32_bf16 v[32:35], a[36:39], a[100:103], v[32:35]\n"
"global_load_dwordx4 a[80:83], v0, s[34:35] offset:0 sc1\n"
"v_mfma_f32_16x16x32_bf16 v[60:63], a[44:47], a[100:103], v[60:63]\n"
"v_lshl_add_u32 v11, v11, 3, s66\n"
"v_mfma_f32_16x16x32_bf16 v[60:63], a[40:43], a[96:99], v[60:63]\n"
"global_load_dword v10, v11, s[96:97]\n"
"v_mfma_f32_16x16x32_bf16 v[64:67], a[40:43], a[104:107], v[64:67]\n"
"v_mfma_f32_16x16x32_bf16 v[64:67], a[44:47], a[108:111], v[64:67]\n"
"v_mfma_f32_16x16x32_bf16 v[36:39], a[36:39], a[108:111], v[36:39]\n"
"v_mfma_f32_16x16x32_bf16 v[36:39], a[32:35], a[104:107], v[36:39]\n"
"s_waitcnt lgkmcnt(8)\n"
"v_mfma_f32_16x16x32_bf16 v[40:43], a[32:35], a[112:115], v[40:43]\n"
"v_mfma_f32_16x16x32_bf16 v[40:43], a[36:39], a[116:119], v[40:43]\n"
"global_load_dwordx4 a[84:87], v0, s[34:35] offset:1024 sc1\n"
"v_mfma_f32_16x16x32_bf16 v[68:71], a[44:47], a[116:119], v[68:71]\n"
"v_mfma_f32_16x16x32_bf16 v[68:71], a[40:43], a[112:115], v[68:71]\n"
"v_mfma_f32_16x16x32_bf16 v[72:75], a[40:43], a[120:123], v[72:75]\n"
"v_mfma_f32_16x16x32_bf16 v[72:75], a[44:47], a[124:127], v[72:75]\n"
"v_mfma_f32_16x16x32_bf16 v[44:47], a[36:39], a[124:127], v[44:47]\n"
"buffer_load_dwordx4 v1, s[48:51], s69 offen offset:2048 sc1 lds\n"
"v_mfma_f32_16x16x32_bf16 v[44:47], a[32:35], a[120:123], v[44:47]\n"
"s_waitcnt lgkmcnt(4)\n"
"v_mfma_f32_16x16x32_bf16 v[48:51], a[32:35], a[128:131], v[48:51]\n"
"v_mfma_f32_16x16x32_bf16 v[48:51], a[36:39], a[132:135], v[48:51]\n"
"global_load_dwordx4 a[88:91], v0, s[34:35] offset:2048 sc1\n"
"v_mfma_f32_16x16x32_bf16 v[76:79], a[44:47], a[132:135], v[76:79]\n"
"v_mfma_f32_16x16x32_bf16 v[76:79], a[40:43], a[128:131], v[76:79]\n"
"v_mfma_f32_16x16x32_bf16 v[80:83], a[40:43], a[136:139], v[80:83]\n"
"v_mfma_f32_16x16x32_bf16 v[80:83], a[44:47], a[140:143], v[80:83]\n"
"v_mfma_f32_16x16x32_bf16 v[52:55], a[36:39], a[140:143], v[52:55]\n"
"v_mfma_f32_16x16x32_bf16 v[52:55], a[32:35], a[136:139], v[52:55]\n"
"s_waitcnt lgkmcnt(0)\n"
"v_mfma_f32_16x16x32_bf16 v[56:59], a[32:35], a[144:147], v[56:59]\n"
"v_mfma_f32_16x16x32_bf16 v[56:59], a[36:39], a[148:151], v[56:59]\n"
"global_load_dwordx4 a[92:95], v0, s[34:35] offset:3072 sc1\n"
"v_mfma_f32_16x16x32_bf16 v[84:87], a[44:47], a[148:151], v[84:87]\n"
"v_mfma_f32_16x16x32_bf16 v[84:87], a[40:43], a[144:147], v[84:87]\n"
"v_mfma_f32_16x16x32_bf16 a[4:7], a[40:43], a[152:155], a[4:7]\n"
"s_add_u32 s34, s34, 8192\n"
"v_mfma_f32_16x16x32_bf16 a[4:7], a[44:47], a[156:159], a[4:7]\n"
"buffer_load_dwordx4 v1, s[48:51], s69 offen offset:3072 sc1 lds\n"
"v_mfma_f32_16x16x32_bf16 a[0:3], a[36:39], a[156:159], a[0:3]\n"
"v_mfma_f32_16x16x32_bf16 a[0:3], a[32:35], a[152:155], a[0:3]\n"
"s_waitcnt vmcnt(14)\n"
"v_mfma_f32_16x16x32_bf16 v[88:91], a[48:51], a[96:99], v[88:91]\n"
"v_mfma_f32_16x16x32_bf16 v[88:91], a[52:55], a[100:103], v[88:91]\n"
"global_load_dwordx4 a[32:35], v0, s[34:35] offset:-4096 sc1\n"
"v_mfma_f32_16x16x32_bf16 v[116:119], a[60:63], a[100:103], v[116:119]\n"
"v_mfma_f32_16x16x32_bf16 v[116:119], a[56:59], a[96:99], v[116:119]\n"
"v_mfma_f32_16x16x32_bf16 v[120:123], a[56:59], a[104:107], v[120:123]\n"
"v_mfma_f32_16x16x32_bf16 v[120:123], a[60:63], a[108:111], v[120:123]\n"
"v_mfma_f32_16x16x32_bf16 v[92:95], a[52:55], a[108:111], v[92:95]\n"
"L_st6798:\n"
"s_cbranch_execz L_st9095\n"
"v_mfma_f32_16x16x32_bf16 v[92:95], a[48:51], a[104:107], v[92:95]\n"
"v_mfma_f32_16x16x32_bf16 v[96:99], a[48:51], a[112:115], v[96:99]\n"
"v_mfma_f32_16x16x32_bf16 v[96:99], a[52:55], a[116:119], v[96:99]\n"
"global_load_dwordx4 a[36:39], v0, s[34:35] offset:-3072 sc1\n"
"v_mfma_f32_16x16x32_bf16 v[124:127], a[60:63], a[116:119], v[124:127]\n"
"v_mfma_f32_16x16x32_bf16 v[124:127], a[56:59], a[112:115], v[124:127]\n"
"v_mfma_f32_16x16x32_bf16 v[128:131], a[56:59], a[120:123], v[128:131]\n"
"v_mfma_f32_16x16x32_bf16 v[128:131], a[60:63], a[124:127], v[128:131]\n"
"v_mfma_f32_16x16x32_bf16 v[100:103], a[52:55], a[124:127], v[100:103]\n"
"v_mfma_f32_16x16x32_bf16 v[100:103], a[48:51], a[120:123], v[100:103]\n"
"v_mfma_f32_16x16x32_bf16 v[104:107], a[48:51], a[128:131], v[104:107]\n"
"buffer_load_dwordx2 v[8:9], off, s[56:59], %14 sc1\n"
"v_mfma_f32_16x16x32_bf16 v[104:107], a[52:55], a[132:135], v[104:107]\n"
"global_load_dwordx4 a[40:43], v0, s[34:35] offset:-2048 sc1\n"
"v_mfma_f32_16x16x32_bf16 v[132:135], a[60:63], a[132:135], v[132:135]\n"
"v_mfma_f32_16x16x32_bf16 v[132:135], a[56:59], a[128:131], v[132:135]\n"
"v_mfma_f32_16x16x32_bf16 v[136:139], a[56:59], a[136:139], v[136:139]\n"
"v_mfma_f32_16x16x32_bf16 v[136:139], a[60:63], a[140:143], v[136:139]\n"
"v_mfma_f32_16x16x32_bf16 v[108:111], a[52:55], a[140:143], v[108:111]\n"
"v_mfma_f32_16x16x32_bf16 v[108:111], a[48:51], a[136:139], v[108:111]\n"
"v_mfma_f32_16x16x32_bf16 v[112:115], a[48:51], a[144:147], v[112:115]\n"
"v_mfma_f32_16x16x32_bf16 v[112:115], a[52:55], a[148:151], v[112:115]\n"
"global_load_dwordx4 a[44:47], v0, s[34:35] offset:-1024 sc1\n"
"v_mfma_f32_16x16x32_bf16 v[140:143], a[60:63], a[148:151], v[140:143]\n"
"v_mfma_f32_16x16x32_bf16 v[140:143], a[56:59], a[144:147], v[140:143]\n"
"v_mfma_f32_16x16x32_bf16 a[12:15], a[56:59], a[152:155], a[12:15]\n"
"v_mfma_f32_16x16x32_bf16 a[12:15], a[60:63], a[156:159], a[12:15]\n"
"v_mfma_f32_16x16x32_bf16 a[8:11], a[52:55], a[156:159], a[8:11]\n"
"v_mfma_f32_16x16x32_bf16 a[8:11], a[48:51], a[152:155], a[8:11]\n"
"s_waitcnt vmcnt(13)\n"
"v_mfma_f32_16x16x32_bf16 v[144:147], a[64:67], a[96:99], v[144:147]\n"
"v_mfma_f32_16x16x32_bf16 v[144:147], a[68:71], a[100:103], v[144:147]\n"
"global_load_dwordx4 a[48:51], v0, s[34:35] offset:0 sc1\n"
"v_mfma_f32_16x16x32_bf16 v[172:175], a[76:79], a[100:103], v[172:175]\n"
"v_mfma_f32_16x16x32_bf16 v[172:175], a[72:75], a[96:99], v[172:175]\n"
"v_mfma_f32_16x16x32_bf16 v[176:179], a[72:75], a[104:107], v[176:179]\n"
"v_mfma_f32_16x16x32_bf16 v[176:179], a[76:79], a[108:111], v[176:179]\n"
"v_mfma_f32_16x16x32_bf16 v[148:151], a[68:71], a[108:111], v[148:151]\n"
"v_mfma_f32_16x16x32_bf16 v[148:151], a[64:67], a[104:107], v[148:151]\n"
"v_mfma_f32_16x16x32_bf16 v[152:155], a[64:67], a[112:115], v[152:155]\n"
"v_lshlrev_b32 v20, 16, v22\n"
"v_mfma_f32_16x16x32_bf16 v[152:155], a[68:71], a[116:119], v[152:155]\n"
"global_load_dwordx4 a[52:55], v0, s[34:35] offset:1024 sc1\n"
"v_and_b32 v21, s63, v22\n"
"v_mfma_f32_16x16x32_bf16 v[180:183], a[76:79], a[116:119], v[180:183]\n"
"v_lshlrev_b32 v22, 16, v23\n"
"v_mfma_f32_16x16x32_bf16 v[180:183], a[72:75], a[112:115], v[180:183]\n"
"v_and_b32 v23, s63, v23\n"
"v_mfma_f32_16x16x32_bf16 v[184:187], a[72:75], a[120:123], v[184:187]\n"
"v_add_f32 v60, v60, v20\n"
"v_mfma_f32_16x16x32_bf16 v[184:187], a[76:79], a[124:127], v[184:187]\n"
"v_add_f32 v61, v61, v21\n"
"v_add_f32 v62, v62, v22\n"
"v_mfma_f32_16x16x32_bf16 v[156:159], a[68:71], a[124:127], v[156:159]\n"
"v_add_f32 v63, v63, v23\n"
"v_mfma_f32_16x16x32_bf16 v[156:159], a[64:67], a[120:123], v[156:159]\n"
"v_add_f32 v64, v64, v20\n"
"v_mfma_f32_16x16x32_bf16 v[160:163], a[64:67], a[128:131], v[160:163]\n"
"v_add_f32 v65, v65, v21\n"
"v_mfma_f32_16x16x32_bf16 v[160:163], a[68:71], a[132:135], v[160:163]\n"
"global_load_dwordx4 a[56:59], v0, s[34:35] offset:2048 sc1\n"
"v_add_f32 v66, v66, v22\n"
"v_mfma_f32_16x16x32_bf16 v[188:191], a[76:79], a[132:135], v[188:191]\n"
"v_add_f32 v67, v67, v23\n"
"v_mfma_f32_16x16x32_bf16 v[188:191], a[72:75], a[128:131], v[188:191]\n"
"v_add_f32 v68, v68, v20\n"
"v_mfma_f32_16x16x32_bf16 v[192:195], a[72:75], a[136:139], v[192:195]\n"
"v_add_f32 v69, v69, v21\n"
"v_mfma_f32_16x16x32_bf16 v[192:195], a[76:79], a[140:143], v[192:195]\n"
"v_add_f32 v70, v70, v22\n"
"v_mfma_f32_16x16x32_bf16 v[164:167], a[68:71], a[140:143], v[164:167]\n"
"v_add_f32 v71, v71, v23\n"
"v_mfma_f32_16x16x32_bf16 v[164:167], a[64:67], a[136:139], v[164:167]\n"
"v_add_f32 v72, v72, v20\n"
"v_add_f32 v73, v73, v21\n"
"v_mfma_f32_16x16x32_bf16 v[168:171], a[64:67], a[144:147], v[168:171]\n"
"v_add_f32 v74, v74, v22\n"
"v_mfma_f32_16x16x32_bf16 v[168:171], a[68:71], a[148:151], v[168:171]\n"
"global_load_dwordx4 a[60:63], v0, s[34:35] offset:3072 sc1\n"
"v_add_f32 v75, v75, v23\n"
"v_mfma_f32_16x16x32_bf16 v[196:199], a[76:79], a[148:151], v[196:199]\n"
"v_add_f32 v76, v76, v20\n"
"v_mfma_f32_16x16x32_bf16 v[196:199], a[72:75], a[144:147], v[196:199]\n"
"v_add_f32 v77, v77, v21\n"
"v_mfma_f32_16x16x32_bf16 a[20:23], a[72:75], a[152:155], a[20:23]\n"
"s_add_u32 s34, s34, 8192\n"
"v_add_f32 v78, v78, v22\n"
"v_mfma_f32_16x16x32_bf16 a[20:23], a[76:79], a[156:159], a[20:23]\n"
"v_add_f32 v79, v79, v23\n"
"v_mfma_f32_16x16x32_bf16 a[16:19], a[68:71], a[156:159], a[16:19]\n"
"v_add_f32 v80, v80, v20\n"
"v_mfma_f32_16x16x32_bf16 a[16:19], a[64:67], a[152:155], a[16:19]\n"
"s_waitcnt vmcnt(10)\n"
"v_add_f32 v81, v81, v21\n"
"v_mfma_f32_16x16x32_bf16 v[200:203], a[80:83], a[96:99], v[200:203]\n"
"v_add_f32 v82, v82, v22\n"
"v_add_f32 v83, v83, v23\n"
"v_mfma_f32_16x16x32_bf16 v[200:203], a[84:87], a[100:103], v[200:203]\n"
"global_load_dwordx4 a[64:67], v0, s[34:35] offset:-4096 sc1\n"
"v_add_f32 v84, v84, v20\n"
"v_mfma_f32_16x16x32_bf16 v[228:231], a[92:95], a[100:103], v[228:231]\n"
"s_waitcnt vmcnt(10)\n"
"ds_read_b128 a[100:103], v2 offset:1024\n"
"v_add_f32 v85, v85, v21\n"
"v_mfma_f32_16x16x32_bf16 v[228:231], a[88:91], a[96:99], v[228:231]\n"
"ds_read_b128 a[96:99], v2 offset:0\n"
"v_add_f32 v86, v86, v22\n"
"v_mfma_f32_16x16x32_bf16 v[232:235], a[88:91], a[104:107], v[232:235]\n"
"v_add_f32 v87, v87, v23\n"
"v_mfma_f32_16x16x32_bf16 v[232:235], a[92:95], a[108:111], v[232:235]\n"
"v_accvgpr_read_b32 v12, a4\n"
"v_mfma_f32_16x16x32_bf16 v[204:207], a[84:87], a[108:111], v[204:207]\n"
"ds_read_b128 a[108:111], v2 offset:3072\n"
"v_accvgpr_read_b32 v13, a5\n"
"v_mfma_f32_16x16x32_bf16 v[204:207], a[80:83], a[104:107], v[204:207]\n"
"ds_read_b128 a[104:107], v2 offset:2048\n"
"v_accvgpr_read_b32 v14, a6\n"
"v_mfma_f32_16x16x32_bf16 v[208:211], a[80:83], a[112:115], v[208:211]\n"
"v_accvgpr_read_b32 v15, a7\n"
"v_mfma_f32_16x16x32_bf16 v[208:211], a[84:87], a[116:119], v[208:211]\n"
"global_load_dwordx4 a[68:71], v0, s[34:35] offset:-3072 sc1\n"
"v_add_f32 v12, v12, v20\n"
"v_mfma_f32_16x16x32_bf16 v[236:239], a[92:95], a[116:119], v[236:239]\n"
"ds_read_b128 a[116:119], v2 offset:5120\n"
"v_add_f32 v13, v13, v21\n"
"v_mfma_f32_16x16x32_bf16 v[236:239], a[88:91], a[112:115], v[236:239]\n"
"ds_read_b128 a[112:115], v2 offset:4096\n"
"v_add_f32 v14, v14, v22\n"
"v_mfma_f32_16x16x32_bf16 v[240:243], a[88:91], a[120:123], v[240:243]\n"
"v_add_f32 v15, v15, v23\n"
"v_accvgpr_write_b32 a4, v12\n"
"v_mfma_f32_16x16x32_bf16 v[240:243], a[92:95], a[124:127], v[240:243]\n"
"v_accvgpr_write_b32 a5, v13\n"
"v_mfma_f32_16x16x32_bf16 v[212:215], a[84:87], a[124:127], v[212:215]\n"
"ds_read_b128 a[124:127], v2 offset:7168\n"
"v_accvgpr_write_b32 a6, v14\n"
"v_mfma_f32_16x16x32_bf16 v[212:215], a[80:83], a[120:123], v[212:215]\n"
"ds_read_b128 a[120:123], v2 offset:6144\n"
"v_accvgpr_write_b32 a7, v15\n"
"v_mfma_f32_16x16x32_bf16 v[216:219], a[80:83], a[128:131], v[216:219]\n"
"v_mfma_f32_16x16x32_bf16 v[216:219], a[84:87], a[132:135], v[216:219]\n"
"global_load_dwordx4 a[72:75], v0, s[34:35] offset:-2048 sc1\n"
"v_lshrrev_b32 v20, 4, v0\n"
"v_mfma_f32_16x16x32_bf16 v[244:247], a[92:95], a[132:135], v[244:247]\n"
"ds_read_b128 a[132:135], v2 offset:9216\n"
"v_and_b32 v20, 0x30, v20\n"
"v_mfma_f32_16x16x32_bf16 v[244:247], a[88:91], a[128:131], v[244:247]\n"
"ds_read_b128 a[128:131], v2 offset:8192\n"
"buffer_load_dwordx2 v[22:23], v20, s[44:47], s61 offen offset:72\n"
"v_mfma_f32_16x16x32_bf16 v[248:251], a[88:91], a[136:139], v[248:251]\n"
"v_mfma_f32_16x16x32_bf16 v[248:251], a[92:95], a[140:143], v[248:251]\n"
"v_mfma_f32_16x16x32_bf16 v[220:223], a[84:87], a[140:143], v[220:223]\n"
"ds_read_b128 a[140:143], v2 offset:11264\n"
"v_mfma_f32_16x16x32_bf16 v[220:223], a[80:83], a[136:139], v[220:223]\n"
"ds_read_b128 a[136:139], v2 offset:10240\n"
"v_mfma_f32_16x16x32_bf16 v[224:227], a[80:83], a[144:147], v[224:227]\n"
"v_mfma_f32_16x16x32_bf16 v[224:227], a[84:87], a[148:151], v[224:227]\n"
"global_load_dwordx4 a[76:79], v0, s[34:35] offset:-1024 sc1\n"
"v_mfma_f32_16x16x32_bf16 v[252:255], a[92:95], a[148:151], v[252:255]\n"
"ds_read_b128 a[148:151], v2 offset:13312\n"
"v_mfma_f32_16x16x32_bf16 v[252:255], a[88:91], a[144:147], v[252:255]\n"
"ds_read_b128 a[144:147], v2 offset:12288\n"
"v_mfma_f32_16x16x32_bf16 a[28:31], a[88:91], a[152:155], a[28:31]\n"
"v_mfma_f32_16x16x32_bf16 a[28:31], a[92:95], a[156:159], a[28:31]\n"
"v_mfma_f32_16x16x32_bf16 a[24:27], a[84:87], a[156:159], a[24:27]\n"
"s_cmp_eq_u32 s62, 5\n"
"ds_read_b128 a[156:159], v2 offset:15360\n"
"v_mfma_f32_16x16x32_bf16 a[24:27], a[80:83], a[152:155], a[24:27]\n"
"ds_read_b128 a[152:155], v2 offset:14336\n"
"s_cbranch_scc1 L_last\n"
"L_P4:\n"
"s_waitcnt vmcnt(9) lgkmcnt(12)\n"
"v_mfma_f32_16x16x32_bf16 v[32:35], a[32:35], a[96:99], v[32:35]\n"
"s_add_u32 s48, s48, 131072\n"
"s_addc_u32 s49, s49, 0\n"
"v_mfma_f32_16x16x32_bf16 v[32:35], a[36:39], a[100:103], v[32:35]\n"
"global_load_dwordx4 a[80:83], v0, s[34:35] offset:0 sc1\n"
"v_mfma_f32_16x16x32_bf16 v[60:63], a[44:47], a[100:103], v[60:63]\n"
"s_barrier\n"
"v_mfma_f32_16x16x32_bf16 v[60:63], a[40:43], a[96:99], v[60:63]\n"
"v_mfma_f32_16x16x32_bf16 v[64:67], a[40:43], a[104:107], v[64:67]\n"
"v_mfma_f32_16x16x32_bf16 v[64:67], a[44:47], a[108:111], v[64:67]\n"
"v_mfma_f32_16x16x32_bf16 v[36:39], a[36:39], a[108:111], v[36:39]\n"
"v_subrev_u32 v8, s65, v8\n"
"v_mfma_f32_16x16x32_bf16 v[36:39], a[32:35], a[104:107], v[36:39]\n"
"s_waitcnt lgkmcnt(8)\n"
"v_xor_b32 v9, s79, v9\n"
"v_mfma_f32_16x16x32_bf16 v[40:43], a[32:35], a[112:115], v[40:43]\n"
"v_or_b32 v8, v8, v9\n"
"v_mfma_f32_16x16x32_bf16 v[40:43], a[36:39], a[116:119], v[40:43]\n"
"global_load_dwordx4 a[84:87], v0, s[34:35] offset:1024 sc1\n"
"v_cmp_lt_u32 vcc, 15, v8\n"
"v_mfma_f32_16x16x32_bf16 v[68:71], a[44:47], a[116:119], v[68:71]\n"
"s_cbranch_vccnz L_slow1\n"
"L_back1:\n"
"v_mfma_f32_16x16x32_bf16 v[68:71], a[40:43], a[112:115], v[68:71]\n"
"v_mfma_f32_16x16x32_bf16 v[72:75], a[40:43], a[120:123], v[72:75]\n"
"v_mfma_f32_16x16x32_bf16 v[72:75], a[44:47], a[124:127], v[72:75]\n"
"v_mfma_f32_16x16x32_bf16 v[44:47], a[36:39], a[124:127], v[44:47]\n"
"v_mfma_f32_16x16x32_bf16 v[44:47], a[32:35], a[120:123], v[44:47]\n"
"s_waitcnt lgkmcnt(4)\n"
"v_mfma_f32_16x16x32_bf16 v[48:51], a[32:35], a[128:131], v[48:51]\n"
"v_mfma_f32_16x16x32_bf16 v[48:51], a[36:39], a[132:135], v[48:51]\n"
"global_load_dwordx4 a[88:91], v0, s[34:35] offset:2048 sc1\n"
"v_mfma_f32_16x16x32_bf16 v[76:79], a[44:47], a[132:135], v[76:79]\n"
"v_mfma_f32_16x16x32_bf16 v[76:79], a[40:43], a[128:131], v[76:79]\n"
"s_add_u32 m0, s64, 16384\n"
"v_mfma_f32_16x16x32_bf16 v[80:83], a[40:43], a[136:139], v[80:83]\n"
"v_mfma_f32_16x16x32_bf16 v[80:83], a[44:47], a[140:143], v[80:83]\n"
"v_mfma_f32_16x16x32_bf16 v[52:55], a[36:39], a[140:143], v[52:55]\n"
"v_mfma_f32_16x16x32_bf16 v[52:55], a[32:35], a[136:139], v[52:55]\n"
"s_waitcnt lgkmcnt(0)\n"
"v_mfma_f32_16x16x32_bf16 v[56:59], a[32:35], a[144:147], v[56:59]\n"
"v_mfma_f32_16x16x32_bf16 v[56:59], a[36:39], a[148:151], v[56:59]\n"
"global_load_dwordx4 a[92:95], v0, s[34:35] offset:3072 sc1\n"
"v_mfma_f32_16x16x32_bf16 v[84:87], a[44:47], a[148:151], v[84:87]\n"
"v_mfma_f32_16x16x32_bf16 v[84:87], a[40:43], a[144:147], v[84:87]\n"
"v_mfma_f32_16x16x32_bf16 a[4:7], a[40:43], a[152:155], a[4:7]\n"
"s_add_u32 s34, s34, 8192\n"
"v_mfma_f32_16x16x32_bf16 a[4:7], a[44:47], a[156:159], a[4:7]\n"
"v_mfma_f32_16x16x32_bf16 a[0:3], a[36:39], a[156:159], a[0:3]\n"
"v_mfma_f32_16x16x32_bf16 a[0:3], a[32:35], a[152:155], a[0:3]\n"
"s_waitcnt vmcnt(9)\n"
"v_mfma_f32_16x16x32_bf16 v[88:91], a[48:51], a[96:99], v[88:91]\n"
"v_mfma_f32_16x16x32_bf16 v[88:91], a[52:55], a[100:103], v[88:91]\n"
"global_load_dwordx4 a[32:35], v0, s[34:35] offset:-4096 sc1\n"
"v_mfma_f32_16x16x32_bf16 v[116:119], a[60:63], a[100:103], v[116:119]\n"
"buffer_load_dwordx4 v0, s[48:51], s75 offen offset:0 sc1 lds\n"
"v_mfma_f32_16x16x32_bf16 v[116:119], a[56:59], a[96:99], v[116:119]\n"
"v_mfma_f32_16x16x32_bf16 v[120:123], a[56:59], a[104:107], v[120:123]\n"
"v_mfma_f32_16x16x32_bf16 v[120:123], a[60:63], a[108:111], v[120:123]\n"
"v_mfma_f32_16x16x32_bf16 v[92:95], a[52:55], a[108:111], v[92:95]\n"
"v_mfma_f32_16x16x32_bf16 v[92:95], a[48:51], a[104:107], v[92:95]\n"
"v_mfma_f32_16x16x32_bf16 v[96:99], a[48:51], a[112:115], v[96:99]\n"
"v_mfma_f32_16x16x32_bf16 v[96:99], a[52:55], a[116:119], v[96:99]\n"
"global_load_dwordx4 a[36:39], v0, s[34:35] offset:-3072 sc1\n"
"v_mfma_f32_16x16x32_bf16 v[124:127], a[60:63], a[116:119], v[124:127]\n"
"v_mfma_f32_16x16x32_bf16 v[124:127], a[56:59], a[112:115], v[124:127]\n"
"v_mfma_f32_16x16x32_bf16 v[128:131], a[56:59], a[120:123], v[128:131]\n"
"v_mfma_f32_16x16x32_bf16 v[128:131], a[60:63], a[124:127], v[128:131]\n"
"v_mfma_f32_16x16x32_bf16 v[100:103], a[52:55], a[124:127], v[100:103]\n"
"v_mfma_f32_16x16x32_bf16 v[100:103], a[48:51], a[120:123], v[100:103]\n"
"v_mfma_f32_16x16x32_bf16 v[104:107], a[48:51], a[128:131], v[104:107]\n"
"v_mfma_f32_16x16x32_bf16 v[104:107], a[52:55], a[132:135], v[104:107]\n"
"global_load_dwordx4 a[40:43], v0, s[34:35] offset:-2048 sc1\n"
"v_mfma_f32_16x16x32_bf16 v[132:135], a[60:63], a[132:135], v[132:135]\n"
"buffer_load_dwordx4 v0, s[48:51], s75 offen offset:1024 sc1 lds\n"
"v_mfma_f32_16x16x32_bf16 v[132:135], a[56:59], a[128:131], v[132:135]\n"
"v_mfma_f32_16x16x32_bf16 v[136:139], a[56:59], a[136:139], v[136:139]\n"
"v_mfma_f32_16x16x32_bf16 v[136:139], a[60:63], a[140:143], v[136:139]\n"
"v_mfma_f32_16x16x32_bf16 v[108:111], a[52:55], a[140:143], v[108:111]\n"
"v_mfma_f32_16x16x32_bf16 v[108:111], a[48:51], a[136:139], v[108:111]\n"
"v_mfma_f32_16x16x32_bf16 v[112:115], a[48:51], a[144:147], v[112:115]\n"
"v_mfma_f32_16x16x32_bf16 v[112:115], a[52:55], a[148:151], v[112:115]\n"
"global_load_dwordx4 a[44:47], v0, s[34:35] offset:-1024 sc1\n"
"v_mfma_f32_16x16x32_bf16 v[140:143], a[60:63], a[148:151], v[140:143]\n"
"v_mfma_f32_16x16x32_bf16 v[140:143], a[56:59], a[144:147], v[140:143]\n"
"v_mfma_f32_16x16x32_bf16 a[12:15], a[56:59], a[152:155], a[12:15]\n"
"v_mfma_f32_16x16x32_bf16 a[12:15], a[60:63], a[156:159], a[12:15]\n"
"v_mfma_f32_16x16x32_bf16 a[8:11], a[52:55], a[156:159], a[8:11]\n"
"v_mfma_f32_16x16x32_bf16 a[8:11], a[48:51], a[152:155], a[8:11]\n"
"s_waitcnt vmcnt(10)\n"
"v_mfma_f32_16x16x32_bf16 v[144:147], a[64:67], a[96:99], v[144:147]\n"
"v_mfma_f32_16x16x32_bf16 v[144:147], a[68:71], a[100:103], v[144:147]\n"
"global_load_dwordx4 a[48:51], v0, s[34:35] offset:0 sc1\n"
"v_mfma_f32_16x16x32_bf16 v[172:175], a[76:79], a[100:103], v[172:175]\n"
"v_mfma_f32_16x16x32_bf16 v[172:175], a[72:75], a[96:99], v[172:175]\n"
"buffer_load_dwordx4 v0, s[48:51], s75 offen offset:2048 sc1 lds\n"
"v_mfma_f32_16x16x32_bf16 v[176:179], a[72:75], a[104:107], v[176:179]\n"
"v_mfma_f32_16x16x32_bf16 v[176:179], a[76:79], a[108:111], v[176:179]\n"
"v_mfma_f32_16x16x32_bf16 v[148:151], a[68:71], a[108:111], v[148:151]\n"
"v_mfma_f32_16x16x32_bf16 v[148:151], a[64:67], a[104:107], v[148:151]\n"
"v_mfma_f32_16x16x32_bf16 v[152:155], a[64:67], a[112:115], v[152:155]\n"
"v_lshlrev_b32 v16, 16, v18\n"
"v_mfma_f32_16x16x32_bf16 v[152:155], a[68:71], a[116:119], v[152:155]\n"
"global_load_dwordx4 a[52:55], v0, s[34:35] offset:1024 sc1\n"
"v_and_b32 v17, s63, v18\n"
"v_mfma_f32_16x16x32_bf16 v[180:183], a[76:79], a[116:119], v[180:183]\n"
"v_lshlrev_b32 v18, 16, v19\n"
"v_mfma_f32_16x16x32_bf16 v[180:183], a[72:75], a[112:115], v[180:183]\n"
"v_and_b32 v19, s63, v19\n"
"v_mfma_f32_16x16x32_bf16 v[184:187], a[72:75], a[120:123], v[184:187]\n"
"v_add_f32 v88, v88, v16\n"
"v_mfma_f32_16x16x32_bf16 v[184:187], a[76:79], a[124:127], v[184:187]\n"
"v_add_f32 v89, v89, v17\n"
"v_add_f32 v90, v90, v18\n"
"v_mfma_f32_16x16x32_bf16 v[156:159], a[68:71], a[124:127], v[156:159]\n"
"v_add_f32 v91, v91, v19\n"
"v_mfma_f32_16x16x32_bf16 v[156:159], a[64:67], a[120:123], v[156:159]\n"
"v_add_f32 v92, v92, v16\n"
"v_mfma_f32_16x16x32_bf16 v[160:163], a[64:67], a[128:131], v[160:163]\n"
"v_add_f32 v93, v93, v17\n"
"v_mfma_f32_16x16x32_bf16 v[160:163], a[68:71], a[132:135], v[160:163]\n"
"global_load_dwordx4 a[56:59], v0, s[34:35] offset:2048 sc1\n"
"v_add_f32 v94, v94, v18\n"
"v_mfma_f32_16x16x32_bf16 v[188:191], a[76:79], a[132:135], v[188:191]\n"
"buffer_load_dwordx4 v0, s[48:51], s75 offen offset:3072 sc1 lds\n"
"v_mfma_f32_16x16x32_bf16 v[188:191], a[72:75], a[128:131], v[188:191]\n"
"v_add_f32 v95, v95, v19\n"
"v_add_f32 v96, v96, v16\n"
"v_mfma_f32_16x16x32_bf16 v[192:195], a[72:75], a[136:139], v[192:195]\n"
"v_add_f32 v97, v97, v17\n"
"v_mfma_f32_16x16x32_bf16 v[192:195], a[76:79], a[140:143], v[192:195]\n"
"v_add_f32 v98, v98, v18\n"
"v_mfma_f32_16x16x32_bf16 v[164:167], a[68:71], a[140:143], v[164:167]\n"
"v_add_f32 v99, v99, v19\n"
"v_mfma_f32_16x16x32_bf16 v[164:167], a[64:67], a[136:139], v[164:167]\n"
"v_add_f32 v100, v100, v16\n"
"v_add_f32 v101, v101, v17\n"
"v_mfma_f32_16x16x32_bf16 v[168:171], a[64:67], a[144:147], v[168:171]\n"
"v_add_f32 v102, v102, v18\n"
"v_mfma_f32_16x16x32_bf16 v[168:171], a[68:71], a[148:151], v[168:171]\n"
"global_load_dwordx4 a[60:63], v0, s[34:35] offset:3072 sc1\n"
"v_add_f32 v103, v103, v19\n"
"v_mfma_f32_16x16x32_bf16 v[196:199], a[76:79], a[148:151], v[196:199]\n"
"v_add_f32 v104, v104, v16\n"
"v_mfma_f32_16x16x32_bf16 v[196:199], a[72:75], a[144:147], v[196:199]\n"
"v_add_f32 v105, v105, v17\n"
"v_mfma_f32_16x16x32_bf16 a[20:23], a[72:75], a[152:155], a[20:23]\n"
"s_add_u32 s34, s34, 8192\n"
"v_add_f32 v106, v106, v18\n"
"v_mfma_f32_16x16x32_bf16 a[20:23], a[76:79], a[156:159], a[20:23]\n"
"v_add_f32 v107, v107, v19\n"
"v_mfma_f32_16x16x32_bf16 a[16:19], a[68:71], a[156:159], a[16:19]\n"
"v_add_f32 v108, v108, v16\n"
"v_mfma_f32_16x16x32_bf16 a[16:19], a[64:67], a[152:155], a[16:19]\n"
"s_waitcnt vmcnt(12)\n"
"v_add_f32 v109, v109, v17\n"
"v_mfma_f32_16x16x32_bf16 v[200:203], a[80:83], a[96:99], v[200:203]\n"
"v_add_f32 v110, v110, v18\n"
"v_add_f32 v111, v111, v19\n"
"v_mfma_f32_16x16x32_bf16 v[200:203], a[84:87], a[100:103], v[200:203]\n"
"global_load_dwordx4 a[64:67], v0, s[34:35] offset:-4096 sc1\n"
"v_add_f32 v112, v112, v16\n"
"v_mfma_f32_16x16x32_bf16 v[228:231], a[92:95], a[100:103], v[228:231]\n"
"ds_read_b128 a[100:103], v3 offset:1024\n"
"v_add_f32 v113, v113, v17\n"
"v_mfma_f32_16x16x32_bf16 v[228:231], a[88:91], a[96:99], v[228:231]\n"
"s_add_u32 m0, s64, 20480\n"
"ds_read_b128 a[96:99], v3 offset:0\n"
"v_add_f32 v114, v114, v18\n"
"v_mfma_f32_16x16x32_bf16 v[232:235], a[88:91], a[104:107], v[232:235]\n"
"v_add_f32 v115, v115, v19\n"
"v_mfma_f32_16x16x32_bf16 v[232:235], a[92:95], a[108:111], v[232:235]\n"
"v_accvgpr_read_b32 v12, a8\n"
"v_mfma_f32_16x16x32_bf16 v[204:207], a[84:87], a[108:111], v[204:207]\n"
"ds_read_b128 a[108:111], v3 offset:3072\n"
"v_accvgpr_read_b32 v13, a9\n"
"v_mfma_f32_16x16x32_bf16 v[204:207], a[80:83], a[104:107], v[204:207]\n"
"ds_read_b128 a[104:107], v3 offset:2048\n"
"v_accvgpr_read_b32 v14, a10\n"
"v_mfma_f32_16x16x32_bf16 v[208:211], a[80:83], a[112:115], v[208:211]\n"
"v_accvgpr_read_b32 v15, a11\n"
"v_mfma_f32_16x16x32_bf16 v[208:211], a[84:87], a[116:119], v[208:211]\n"
"global_load_dwordx4 a[68:71], v0, s[34:35] offset:-3072 sc1\n"
"v_add_f32 v12, v12, v16\n"
"v_mfma_f32_16x16x32_bf16 v[236:239], a[92:95], a[116:119], v[236:239]\n"
"ds_read_b128 a[116:119], v3 offset:5120\n"
"v_add_f32 v13, v13, v17\n"
"v_mfma_f32_16x16x32_bf16 v[236:239], a[88:91], a[112:115], v[236:239]\n"
"ds_read_b128 a[112:115], v3 offset:4096\n"
"v_add_f32 v14, v14, v18\n"
"v_mfma_f32_16x16x32_bf16 v[240:243], a[88:91], a[120:123], v[240:243]\n"
"v_add_f32 v15, v15, v19\n"
"v_accvgpr_write_b32 a8, v12\n"
"v_mfma_f32_16x16x32_bf16 v[240:243], a[92:95], a[124:127], v[240:243]\n"
"v_accvgpr_write_b32 a9, v13\n"
"v_mfma_f32_16x16x32_bf16 v[212:215], a[84:87], a[124:127], v[212:215]\n"
"ds_read_b128 a[124:127], v3 offset:7168\n"
"v_accvgpr_write_b32 a10, v14\n"
"v_mfma_f32_16x16x32_bf16 v[212:215], a[80:83], a[120:123], v[212:215]\n"
"ds_read_b128 a[120:123], v3 offset:6144\n"
"v_accvgpr_write_b32 a11, v15\n"
"v_mfma_f32_16x16x32_bf16 v[216:219], a[80:83], a[128:131], v[216:219]\n"
"v_mfma_f32_16x16x32_bf16 v[216:219], a[84:87], a[132:135], v[216:219]\n"
"global_load_dwordx4 a[72:75], v0, s[34:35] offset:-2048 sc1\n"
"v_mfma_f32_16x16x32_bf16 v[244:247], a[92:95], a[132:135], v[244:247]\n"
"ds_read_b128 a[132:135], v3 offset:9216\n"
"v_mfma_f32_16x16x32_bf16 v[244:247], a[88:91], a[128:131], v[244:247]\n"
"ds_read_b128 a[128:131], v3 offset:8192\n"
"v_mfma_f32_16x16x32_bf16 v[248:251], a[88:91], a[136:139], v[248:251]\n"
"buffer_load_dwordx4 v1, s[48:51], s75 offen offset:0 sc1 lds\n"
"v_mfma_f32_16x16x32_bf16 v[248:251], a[92:95], a[140:143], v[248:251]\n"
"v_mfma_f32_16x16x32_bf16 v[220:223], a[84:87], a[140:143], v[220:223]\n"
"ds_read_b128 a[140:143], v3 offset:11264\n"
"v_mfma_f32_16x16x32_bf16 v[220:223], a[80:83], a[136:139], v[220:223]\n"
"ds_read_b128 a[136:139], v3 offset:10240\n"
"v_mfma_f32_16x16x32_bf16 v[224:227], a[80:83], a[144:147], v[224:227]\n"
"v_mfma_f32_16x16x32_bf16 v[224:227], a[84:87], a[148:151], v[224:227]\n"
"global_load_dwordx4 a[76:79], v0, s[34:35] offset:-1024 sc1\n"
"v_mfma_f32_16x16x32_bf16 v[252:255], a[92:95], a[148:151], v[252:255]\n"
"ds_read_b128 a[148:151], v3 offset:13312\n"
"v_mfma_f32_16x16x32_bf16 v[252:255], a[88:91], a[144:147], v[252:255]\n"
"ds_read_b128 a[144:147], v3 offset:12288\n"
"v_mfma_f32_16x16x32_bf16 a[28:31], a[88:91], a[152:155], a[28:31]\n"
"v_mfma_f32_16x16x32_bf16 a[28:31], a[92:95], a[156:159], a[28:31]\n"
"v_mfma_f32_16x16x32_bf16 a[24:27], a[84:87], a[156:159], a[24:27]\n"
"ds_read_b128 a[156:159], v3 offset:15360\n"
"v_mfma_f32_16x16x32_bf16 a[24:27], a[80:83], a[152:155], a[24:27]\n"
"s_waitcnt vmcnt(11) lgkmcnt(11)\n"
"ds_read_b128 a[152:155], v3 offset:14336\n"
"v_mfma_f32_16x16x32_bf16 v[32:35], a[32:35], a[96:99], v[32:35]\n"
"v_mfma_f32_16x16x32_bf16 v[32:35], a[36:39], a[100:103], v[32:35]\n"
"global_load_dwordx4 a[80:83], v0, s[34:35] offset:0 sc1\n"
"v_mfma_f32_16x16x32_bf16 v[60:63], a[44:47], a[100:103], v[60:63]\n"
"v_mfma_f32_16x16x32_bf16 v[60:63], a[40:43], a[96:99], v[60:63]\n"
"buffer_load_dwordx4 v1, s[48:51], s75 offen offset:1024 sc1 lds\n"
"v_mfma_f32_16x16x32_bf16 v[64:67], a[40:43], a[104:107], v[64:67]\n"
"v_mfma_f32_16x16x32_bf16 v[64:67], a[44:47], a[108:111], v[64:67]\n"
"v_mfma_f32_16x16x32_bf16 v[36:39], a[36:39], a[108:111], v[36:39]\n"
"v_mfma_f32_16x16x32_bf16 v[36:39], a[32:35], a[104:107], v[36:39]\n"
"s_waitcnt lgkmcnt(8)\n"
"v_mfma_f32_16x16x32_bf16 v[40:43], a[32:35], a[112:115], v[40:43]\n"
"v_mfma_f32_16x16x32_bf16 v[40:43], a[36:39], a[116:119], v[40:43]\n"
"global_load_dwordx4 a[84:87], v0, s[34:35] offset:1024 sc1\n"
"v_mfma_f32_16x16x32_bf16 v[68:71], a[44:47], a[116:119], v[68:71]\n"
"v_mfma_f32_16x16x32_bf16 v[68:71], a[40:43], a[112:115], v[68:71]\n"
"v_mfma_f32_16x16x32_bf16 v[72:75], a[40:43], a[120:123], v[72:75]\n"
"v_mfma_f32_16x16x32_bf16 v[72:75], a[44:47], a[124:127], v[72:75]\n"
"v_mfma_f32_16x16x32_bf16 v[44:47], a[36:39], a[124:127], v[44:47]\n"
"v_mfma_f32_16x16x32_bf16 v[44:47], a[32:35], a[120:123], v[44:47]\n"
"s_waitcnt lgkmcnt(4)\n"
"v_mfma_f32_16x16x32_bf16 v[48:51], a[32:35], a[128:131], v[48:51]\n"
"v_mfma_f32_16x16x32_bf16 v[48:51], a[36:39], a[132:135], v[48:51]\n"
"global_load_dwordx4 a[88:91], v0, s[34:35] offset:2048 sc1\n"
"v_mfma_f32_16x16x32_bf16 v[76:79], a[44:47], a[132:135], v[76:79]\n"
"v_mfma_f32_16x16x32_bf16 v[76:79], a[40:43], a[128:131], v[76:79]\n"
"buffer_load_dwordx4 v1, s[48:51], s75 offen offset:2048 sc1 lds\n"
"v_mfma_f32_16x16x32_bf16 v[80:83], a[40:43], a[136:139], v[80:83]\n"
"v_mfma_f32_16x16x32_bf16 v[80:83], a[44:47], a[140:143], v[80:83]\n"
"v_mfma_f32_16x16x32_bf16 v[52:55], a[36:39], a[140:143], v[52:55]\n"
"v_mfma_f32_16x16x32_bf16 v[52:55], a[32:35], a[136:139], v[52:55]\n"
"s_waitcnt lgkmcnt(0)\n"
"v_mfma_f32_16x16x32_bf16 v[56:59], a[32:35], a[144:147], v[56:59]\n"
"v_mfma_f32_16x16x32_bf16 v[56:59], a[36:39], a[148:151], v[56:59]\n"
"global_load_dwordx4 a[92:95], v0, s[34:35] offset:3072 sc1\n"
"v_mfma_f32_16x16x32_bf16 v[84:87], a[44:47], a[148:151], v[84:87]\n"
"v_mfma_f32_16x16x32_bf16 v[84:87], a[40:43], a[144:147], v[84:87]\n"
"v_mfma_f32_16x16x32_bf16 a[4:7], a[40:43], a[152:155], a[4:7]\n"
"s_add_u32 s34, s34, 8192\n"
"v_mfma_f32_16x16x32_bf16 a[4:7], a[44:47], a[156:159], a[4:7]\n"
"v_mfma_f32_16x16x32_bf16 a[0:3], a[36:39], a[156:159], a[0:3]\n"
"v_mfma_f32_16x16x32_bf16 a[0:3], a[32:35], a[152:155], a[0:3]\n"
"s_waitcnt vmcnt(11)\n"
"v_mfma_f32_16x16x32_bf16 v[88:91], a[48:51], a[96:99], v[88:91]\n"
"v_mfma_f32_16x16x32_bf16 v[88:91], a[52:55], a[100:103], v[88:91]\n"
"global_load_dwordx4 a[32:35], v0, s[34:35] offset:-4096 sc1\n"
"v_mfma_f32_16x16x32_bf16 v[116:119], a[60:63], a[100:103], v[116:119]\n"
"v_mfma_f32_16x16x32_bf16 v[116:119], a[56:59], a[96:99], v[116:119]\n"
"buffer_load_dwordx4 v1, s[48:51], s75 offen offset:3072 sc1 lds\n"
"v_mfma_f32_16x16x32_bf16 v[120:123], a[56:59], a[104:107], v[120:123]\n"
"v_mfma_f32_16x16x32_bf16 v[120:123], a[60:63], a[108:111], v[120:123]\n"
"v_mfma_f32_16x16x32_bf16 v[92:95], a[52:55], a[108:111], v[92:95]\n"
"v_mfma_f32_16x16x32_bf16 v[92:95], a[48:51], a[104:107], v[92:95]\n"
"v_mfma_f32_16x16x32_bf16 v[96:99], a[48:51], a[112:115], v[96:99]\n"
"v_mfma_f32_16x16x32_bf16 v[96:99], a[52:55], a[116:119], v[96:99]\n"
"global_load_dwordx4 a[36:39], v0, s[34:35] offset:-3072 sc1\n"
"v_mfma_f32_16x16x32_bf16 v[124:127], a[60:63], a[116:119], v[124:127]\n"
"v_mfma_f32_16x16x32_bf16 v[124:127], a[56:59], a[112:115], v[124:127]\n"
"v_mfma_f32_16x16x32_bf16 v[128:131], a[56:59], a[120:123], v[128:131]\n"
"v_mfma_f32_16x16x32_bf16 v[128:131], a[60:63], a[124:127], v[128:131]\n"
"v_mfma_f32_16x16x32_bf16 v[100:103], a[52:55], a[124:127], v[100:103]\n"
"v_mfma_f32_16x16x32_bf16 v[100:103], a[48:51], a[120:123], v[100:103]\n"
"v_mfma_f32_16x16x32_bf16 v[104:107], a[48:51], a[128:131], v[104:107]\n"
"v_mfma_f32_16x16x32_bf16 v[104:107], a[52:55], a[132:135], v[104:107]\n"
"global_load_dwordx4 a[40:43], v0, s[34:35] offset:-2048 sc1\n"
"v_mfma_f32_16x16x32_bf16 v[132:135], a[60:63], a[132:135], v[132:135]\n"
"v_mfma_f32_16x16x32_bf16 v[132:135], a[56:59], a[128:131], v[132:135]\n"
"s_add_u32 m0, s64, 24576\n"
"v_mfma_f32_16x16x32_bf16 v[136:139], a[56:59], a[136:139], v[136:139]\n"
"v_mfma_f32_16x16x32_bf16 v[136:139], a[60:63], a[140:143], v[136:139]\n"
"v_mfma_f32_16x16x32_bf16 v[108:111], a[52:55], a[140:143], v[108:111]\n"
"v_mfma_f32_16x16x32_bf16 v[108:111], a[48:51], a[136:139], v[108:111]\n"
"v_mfma_f32_16x16x32_bf16 v[112:115], a[48:51], a[144:147], v[112:115]\n"
"v_mfma_f32_16x16x32_bf16 v[112:115], a[52:55], a[148:151], v[112:115]\n"
"global_load_dwordx4 a[44:47], v0, s[34:35] offset:-1024 sc1\n"
"v_mfma_f32_16x16x32_bf16 v[140:143], a[60:63], a[148:151], v[140:143]\n"
"v_mfma_f32_16x16x32_bf16 v[140:143], a[56:59], a[144:147], v[140:143]\n"
"v_mfma_f32_16x16x32_bf16 a[12:15], a[56:59], a[152:155], a[12:15]\n"
"v_mfma_f32_16x16x32_bf16 a[12:15], a[60:63], a[156:159], a[12:15]\n"
"v_mfma_f32_16x16x32_bf16 a[8:11], a[52:55], a[156:159], a[8:11]\n"
"v_mfma_f32_16x16x32_bf16 a[8:11], a[48:51], a[152:155], a[8:11]\n"
"s_waitcnt vmcnt(11)\n"
"v_mfma_f32_16x16x32_bf16 v[144:147], a[64:67], a[96:99], v[144:147]\n"
"v_mfma_f32_16x16x32_bf16 v[144:147], a[68:71], a[100:103], v[144:147]\n"
"global_load_dwordx4 a[48:51], v0, s[34:35] offset:0 sc1\n"
"v_mfma_f32_16x16x32_bf16 v[172:175], a[76:79], a[100:103], v[172:175]\n"
"v_mfma_f32_16x16x32_bf16 v[172:175], a[72:75], a[96:99], v[172:175]\n"
"buffer_load_dwordx4 v0, s[48:51], s69 offen offset:0 sc1 lds\n"
"v_mfma_f32_16x16x32_bf16 v[176:179], a[72:75], a[104:107], v[176:179]\n"
"v_mfma_f32_16x16x32_bf16 v[176:179], a[76:79], a[108:111], v[176:179]\n"
"v_mfma_f32_16x16x32_bf16 v[148:151], a[68:71], a[108:111], v[148:151]\n"
"v_mfma_f32_16x16x32_bf16 v[148:151], a[64:67], a[104:107], v[148:151]\n"
"v_mfma_f32_16x16x32_bf16 v[152:155], a[64:67], a[112:115], v[152:155]\n"
"v_lshlrev_b32 v20, 16, v22\n"
"v_mfma_f32_16x16x32_bf16 v[152:155], a[68:71], a[116:119], v[152:155]\n"
"global_load_dwordx4 a[52:55], v0, s[34:35] offset:1024 sc1\n"
"v_and_b32 v21, s63, v22\n"
"v_mfma_f32_16x16x32_bf16 v[180:183], a[76:79], a[116:119], v[180:183]\n"
"v_lshlrev_b32 v22, 16, v23\n"
"v_mfma_f32_16x16x32_bf16 v[180:183], a[72:75], a[112:115], v[180:183]\n"
"v_and_b32 v23, s63, v23\n"
"v_mfma_f32_16x16x32_bf16 v[184:187], a[72:75], a[120:123], v[184:187]\n"
"v_add_f32 v116, v116, v20\n"
"v_mfma_f32_16x16x32_bf16 v[184:187], a[76:79], a[124:127], v[184:187]\n"
"v_add_f32 v117, v117, v21\n"
"v_add_f32 v118, v118, v22\n"
"v_mfma_f32_16x16x32_bf16 v[156:159], a[68:71], a[124:127], v[156:159]\n"
"v_add_f32 v119, v119, v23\n"
"v_mfma_f32_16x16x32_bf16 v[156:159], a[64:67], a[120:123], v[156:159]\n"
"v_add_f32 v120, v120, v20\n"
"v_mfma_f32_16x16x32_bf16 v[160:163], a[64:67], a[128:131], v[160:163]\n"
"v_add_f32 v121, v121, v21\n"
"v_mfma_f32_16x16x32_bf16 v[160:163], a[68:71], a[132:135], v[160:163]\n"
"global_load_dwordx4 a[56:59], v0, s[34:35] offset:2048 sc1\n"
"v_add_f32 v122, v122, v22\n"
"v_mfma_f32_16x16x32_bf16 v[188:191], a[76:79], a[132:135], v[188:191]\n"
"v_add_f32 v123, v123, v23\n"
"v_add_f32 v124, v124, v20\n"
"v_mfma_f32_16x16x32_bf16 v[188:191], a[72:75], a[128:131], v[188:191]\n"
"buffer_load_dwordx4 v0, s[48:51], s69 offen offset:1024 sc1 lds\n"
"v_mfma_f32_16x16x32_bf16 v[192:195], a[72:75], a[136:139], v[192:195]\n"
"v_add_f32 v125, v125, v21\n"
"v_mfma_f32_16x16x32_bf16 v[192:195], a[76:79], a[140:143], v[192:195]\n"
"v_add_f32 v126, v126, v22\n"
"v_mfma_f32_16x16x32_bf16 v[164:167], a[68:71], a[140:143], v[164:167]\n"
"v_add_f32 v127, v127, v23\n"
"v_mfma_f32_16x16x32_bf16 v[164:167], a[64:67], a[136:139], v[164:167]\n"
"v_add_f32 v128, v128, v20\n"
"v_add_f32 v129, v129, v21\n"
"v_mfma_f32_16x16x32_bf16 v[168:171], a[64:67], a[144:147], v[168:171]\n"
"v_add_f32 v130, v130, v22\n"
"v_mfma_f32_16x16x32_bf16 v[168:171], a[68:71], a[148:151], v[168:171]\n"
"global_load_dwordx4 a[60:63], v0, s[34:35] offset:3072 sc1\n"
"v_add_f32 v131, v131, v23\n"
"v_mfma_f32_16x16x32_bf16 v[196:199], a[76:79], a[148:151], v[196:199]\n"
"v_add_f32 v132, v132, v20\n"
"v_mfma_f32_16x16x32_bf16 v[196:199], a[72:75], a[144:147], v[196:199]\n"
"v_add_f32 v133, v133, v21\n"
"v_mfma_f32_16x16x32_bf16 a[20:23], a[72:75], a[152:155], a[20:23]\n"
"s_add_u32 s34, s34, 8192\n"
"v_add_f32 v134, v134, v22\n"
"v_mfma_f32_16x16x32_bf16 a[20:23], a[76:79], a[156:159], a[20:23]\n"
"v_add_f32 v135, v135, v23\n"
"v_mfma_f32_16x16x32_bf16 a[16:19], a[68:71], a[156:159], a[16:19]\n"
"v_add_f32 v136, v136, v20\n"
"v_mfma_f32_16x16x32_bf16 a[16:19], a[64:67], a[152:155], a[16:19]\n"
"s_waitcnt vmcnt(11)\n"
"v_add_f32 v137, v137, v21\n"
"v_mfma_f32_16x16x32_bf16 v[200:203], a[80:83], a[96:99], v[200:203]\n"
"v_add_f32 v138, v138, v22\n"
"v_add_f32 v139, v139, v23\n"
"v_mfma_f32_16x16x32_bf16 v[200:203], a[84:87], a[100:103], v[200:203]\n"
"global_load_dwordx4 a[64:67], v0, s[34:35] offset:-4096 sc1\n"
"v_add_f32 v140, v140, v20\n"
"v_mfma_f32_16x16x32_bf16 v[228:231], a[92:95], a[100:103], v[228:231]\n"
"ds_read_b128 a[100:103], v4 offset:1024\n"
"v_add_f32 v141, v141, v21\n"
"v_mfma_f32_16x16x32_bf16 v[228:231], a[88:91], a[96:99], v[228:231]\n"
"ds_read_b128 a[96:99], v4 offset:0\n"
"v_add_f32 v142, v142, v22\n"
"v_mfma_f32_16x16x32_bf16 v[232:235], a[88:91], a[104:107], v[232:235]\n"
"buffer_load_dwordx4 v0, s[48:51], s69 offen offset:2048 sc1 lds\n"
"v_mfma_f32_16x16x32_bf16 v[232:235], a[92:95], a[108:111], v[232:235]\n"
"v_add_f32 v143, v143, v23\n"
"v_accvgpr_read_b32 v12, a12\n"
"v_mfma_f32_16x16x32_bf16 v[204:207], a[84:87], a[108:111], v[204:207]\n"
"ds_read_b128 a[108:111], v4 offset:3072\n"
"v_accvgpr_read_b32 v13, a13\n"
"v_mfma_f32_16x16x32_bf16 v[204:207], a[80:83], a[104:107], v[204:207]\n"
"ds_read_b128 a[104:107], v4 offset:2048\n"
"v_accvgpr_read_b32 v14, a14\n"
"v_mfma_f32_16x16x32_bf16 v[208:211], a[80:83], a[112:115], v[208:211]\n"
"v_accvgpr_read_b32 v15, a15\n"
"v_mfma_f32_16x16x32_bf16 v[208:211], a[84:87], a[116:119], v[208:211]\n"
"global_load_dwordx4 a[68:71], v0, s[34:35] offset:-3072 sc1\n"
"v_add_f32 v12, v12, v20\n"
"v_mfma_f32_16x16x32_bf16 v[236:239], a[92:95], a[116:119], v[236:239]\n"
"ds_read_b128 a[116:119], v4 offset:5120\n"
"v_add_f32 v13, v13, v21\n"
"v_mfma_f32_16x16x32_bf16 v[236:239], a[88:91], a[112:115], v[236:239]\n"
"L_st7379:\n"
"s_cbranch_execz L_st8279\n"
"ds_read_b128 a[112:115], v4 offset:4096\n"
"v_add_f32 v14, v14, v22\n"
"v_mfma_f32_16x16x32_bf16 v[240:243], a[88:91], a[120:123], v[240:243]\n"
"v_add_f32 v15, v15, v23\n"
"v_accvgpr_write_b32 a12, v12\n"
"v_mfma_f32_16x16x32_bf16 v[240:243], a[92:95], a[124:127], v[240:243]\n"
"v_accvgpr_write_b32 a13, v13\n"
"v_mfma_f32_16x16x32_bf16 v[212:215], a[84:87], a[124:127], v[212:215]\n"
"ds_read_b128 a[124:127], v4 offset:7168\n"
"v_accvgpr_write_b32 a14, v14\n"
"v_mfma_f32_16x16x32_bf16 v[212:215], a[80:83], a[120:123], v[212:215]\n"
"ds_read_b128 a[120:123], v4 offset:6144\n"
"v_accvgpr_write_b32 a15, v15\n"
"v_mfma_f32_16x16x32_bf16 v[216:219], a[80:83], a[128:131], v[216:219]\n"
"v_mfma_f32_16x16x32_bf16 v[216:219], a[84:87], a[132:135], v[216:219]\n"
"global_load_dwordx4 a[72:75], v0, s[34:35] offset:-2048 sc1\n"
"v_mfma_f32_16x16x32_bf16 v[244:247], a[92:95], a[132:135], v[244:247]\n"
"ds_read_b128 a[132:135], v4 offset:9216\n"
"v_mfma_f32_16x16x32_bf16 v[244:247], a[88:91], a[128:131], v[244:247]\n"
"ds_read_b128 a[128:131], v4 offset:8192\n"
"v_mfma_f32_16x16x32_bf16 v[248:251], a[88:91], a[136:139], v[248:251]\n"
"buffer_load_dwordx4 v0, s[48:51], s69 offen offset:3072 sc1 lds\n"
"v_mfma_f32_16x16x32_bf16 v[248:251], a[92:95], a[140:143], v[248:251]\n"
"v_mfma_f32_16x16x32_bf16 v[220:223], a[84:87], a[140:143], v[220:223]\n"
"ds_read_b128 a[140:143], v4 offset:11264\n"
"v_mfma_f32_16x16x32_bf16 v[220:223], a[80:83], a[136:139], v[220:223]\n"
"ds_read_b128 a[136:139], v4 offset:10240\n"
"v_mfma_f32_16x16x32_bf16 v[224:227], a[80:83], a[144:147], v[224:227]\n"
"v_mfma_f32_16x16x32_bf16 v[224:227], a[84:87], a[148:151], v[224:227]\n"
"global_load_dwordx4 a[76:79], v0, s[34:35] offset:-1024 sc1\n"
"v_mfma_f32_16x16x32_bf16 v[252:255], a[92:95], a[148:151], v[252:255]\n"
"ds_read_b128 a[148:151], v4 offset:13312\n"
"v_mfma_f32_16x16x32_bf16 v[252:255], a[88:91], a[144:147], v[252:255]\n"
"ds_read_b128 a[144:147], v4 offset:12288\n"
"v_mfma_f32_16x16x32_bf16 a[28:31], a[88:91], a[152:155], a[28:31]\n"
"v_mfma_f32_16x16x32_bf16 a[28:31], a[92:95], a[156:159], a[28:31]\n"
"v_mfma_f32_16x16x32_bf16 a[24:27], a[84:87], a[156:159], a[24:27]\n"
"ds_read_b128 a[156:159], v4 offset:15360\n"
"v_mfma_f32_16x16x32_bf16 a[24:27], a[80:83], a[152:155], a[24:27]\n"
"s_waitcnt vmcnt(12) lgkmcnt(11)\n"
"ds_read_b128 a[152:155], v4 offset:14336\n"
"v_mfma_f32_16x16x32_bf16 v[32:35], a[32:35], a[96:99], v[32:35]\n"
"v_mfma_f32_16x16x32_bf16 v[32:35], a[36:39], a[100:103], v[32:35]\n"
"global_load_dwordx4 a[80:83], v0, s[34:35] offset:0 sc1\n"
"v_lshrrev_b32 v16, 4, v0\n"
"v_mfma_f32_16x16x32_bf16 v[60:63], a[44:47], a[100:103], v[60:63]\n"
"v_and_b32 v16, 0x30, v16\n"
"v_mfma_f32_16x16x32_bf16 v[60:63], a[40:43], a[96:99], v[60:63]\n"
"s_add_u32 m0, s64, 28672\n"
"buffer_load_dwordx2 v[18:19], v16, s[44:47], s61 offen offset:128\n"
"v_mfma_f32_16x16x32_bf16 v[64:67], a[40:43], a[104:107], v[64:67]\n"
"v_mfma_f32_16x16x32_bf16 v[64:67], a[44:47], a[108:111], v[64:67]\n"
"v_mfma_f32_16x16x32_bf16 v[36:39], a[36:39], a[108:111], v[36:39]\n"
"v_mfma_f32_16x16x32_bf16 v[36:39], a[32:35], a[104:107], v[36:39]\n"
"s_waitcnt lgkmcnt(8)\n"
"v_mfma_f32_16x16x32_bf16 v[40:43], a[32:35], a[112:115], v[40:43]\n"
"v_mfma_f32_16x16x32_bf16 v[40:43], a[36:39], a[116:119], v[40:43]\n"
"global_load_dwordx4 a[84:87], v0, s[34:35] offset:1024 sc1\n"
"v_lshrrev_b32 v20, 4, v0\n"
"v_mfma_f32_16x16x32_bf16 v[68:71], a[44:47], a[116:119], v[68:71]\n"
"v_and_b32 v20, 0x30, v20\n"
"v_mfma_f32_16x16x32_bf16 v[68:71], a[40:43], a[112:115], v[68:71]\n"
"buffer_load_dwordx2 v[22:23], v20, s[44:47], s61 offen offset:136\n"
"v_mfma_f32_16x16x32_bf16 v[72:75], a[40:43], a[120:123], v[72:75]\n"
"v_mfma_f32_16x16x32_bf16 v[72:75], a[44:47], a[124:127], v[72:75]\n"
"v_mfma_f32_16x16x32_bf16 v[44:47], a[36:39], a[124:127], v[44:47]\n"
"v_mfma_f32_16x16x32_bf16 v[44:47], a[32:35], a[120:123], v[44:47]\n"
"s_waitcnt lgkmcnt(4)\n"
"v_mfma_f32_16x16x32_bf16 v[48:51], a[32:35], a[128:131], v[48:51]\n"
"v_mfma_f32_16x16x32_bf16 v[48:51], a[36:39], a[132:135], v[48:51]\n"
"global_load_dwordx4 a[88:91], v0, s[34:35] offset:2048 sc1\n"
"v_lshrrev_b32 v24, 4, v0\n"
"v_mfma_f32_16x16x32_bf16 v[76:79], a[44:47], a[132:135], v[76:79]\n"
"v_and_b32 v24, 0x30, v24\n"
"v_mfma_f32_16x16x32_bf16 v[76:79], a[40:43], a[128:131], v[76:79]\n"
"buffer_load_dwordx4 v1, s[48:51], s69 offen offset:0 sc1 lds\n"
"buffer_load_dwordx2 v[26:27], v24, s[44:47], s61 offen offset:192\n"
"v_mfma_f32_16x16x32_bf16 v[80:83], a[40:43], a[136:139], v[80:83]\n"
"v_mfma_f32_16x16x32_bf16 v[80:83], a[44:47], a[140:143], v[80:83]\n"
"v_mfma_f32_16x16x32_bf16 v[52:55], a[36:39], a[140:143], v[52:55]\n"
"v_mfma_f32_16x16x32_bf16 v[52:55], a[32:35], a[136:139], v[52:55]\n"
"s_waitcnt lgkmcnt(0)\n"
"v_mfma_f32_16x16x32_bf16 v[56:59], a[32:35], a[144:147], v[56:59]\n"
"v_mfma_f32_16x16x32_bf16 v[56:59], a[36:39], a[148:151], v[56:59]\n"
"global_load_dwordx4 a[92:95], v0, s[34:35] offset:3072 sc1\n"
"v_lshrrev_b32 v28, 4, v0\n"
"v_mfma_f32_16x16x32_bf16 v[84:87], a[44:47], a[148:151], v[84:87]\n"
"v_and_b32 v28, 0x30, v28\n"
"v_mfma_f32_16x16x32_bf16 v[84:87], a[40:43], a[144:147], v[84:87]\n"
"buffer_load_dwordx2 v[30:31], v28, s[44:47], s61 offen offset:200\n"
"v_mfma_f32_16x16x32_bf16 a[4:7], a[40:43], a[152:155], a[4:7]\n"
"s_add_u32 s34, s34, 8192\n"
"v_mfma_f32_16x16x32_bf16 a[4:7], a[44:47], a[156:159], a[4:7]\n"
"v_mfma_f32_16x16x32_bf16 a[0:3], a[36:39], a[156:159], a[0:3]\n"
"v_mfma_f32_16x16x32_bf16 a[0:3], a[32:35], a[152:155], a[0:3]\n"
"s_waitcnt vmcnt(15)\n"
"v_mfma_f32_16x16x32_bf16 v[88:91], a[48:51], a[96:99], v[88:91]\n"
"v_mfma_f32_16x16x32_bf16 v[88:91], a[52:55], a[100:103], v[88:91]\n"
"global_load_dwordx4 a[32:35], v0, s[34:35] offset:-4096 sc1\n"
"v_mfma_f32_16x16x32_bf16 v[116:119], a[60:63], a[100:103], v[116:119]\n"
"v_mfma_f32_16x16x32_bf16 v[116:119], a[56:59], a[96:99], v[116:119]\n"
"buffer_load_dwordx4 v1, s[48:51], s69 offen offset:1024 sc1 lds\n"
"v_mfma_f32_16x16x32_bf16 v[120:123], a[56:59], a[104:107], v[120:123]\n"
"v_mfma_f32_16x16x32_bf16 v[120:123], a[60:63], a[108:111], v[120:123]\n"
"v_mfma_f32_16x16x32_bf16 v[92:95], a[52:55], a[108:111], v[92:95]\n"
"v_mfma_f32_16x16x32_bf16 v[92:95], a[48:51], a[104:107], v[92:95]\n"
"v_mfma_f32_16x16x32_bf16 v[96:99], a[48:51], a[112:115], v[96:99]\n"
"v_mfma_f32_16x16x32_bf16 v[96:99], a[52:55], a[116:119], v[96:99]\n"
"global_load_dwordx4 a[36:39], v0, s[34:35] offset:-3072 sc1\n"
"v_mfma_f32_16x16x32_bf16 v[124:127], a[60:63], a[116:119], v[124:127]\n"
"v_mfma_f32_16x16x32_bf16 v[124:127], a[56:59], a[112:115], v[124:127]\n"
"v_mfma_f32_16x16x32_bf16 v[128:131], a[56:59], a[120:123], v[128:131]\n"
"v_mfma_f32_16x16x32_bf16 v[128:131], a[60:63], a[124:127], v[128:131]\n"
"v_mfma_f32_16x16x32_bf16 v[100:103], a[52:55], a[124:127], v[100:103]\n"
"v_mfma_f32_16x16x32_bf16 v[100:103], a[48:51], a[120:123], v[100:103]\n"
"v_mfma_f32_16x16x32_bf16 v[104:107], a[48:51], a[128:131], v[104:107]\n"
"v_mfma_f32_16x16x32_bf16 v[104:107], a[52:55], a[132:135], v[104:107]\n"
"global_load_dwordx4 a[40:43], v0, s[34:35] offset:-2048 sc1\n"
"v_mfma_f32_16x16x32_bf16 v[132:135], a[60:63], a[132:135], v[132:135]\n"
"v_mfma_f32_16x16x32_bf16 v[132:135], a[56:59], a[128:131], v[132:135]\n"
"buffer_load_dwordx4 v1, s[48:51], s69 offen offset:2048 sc1 lds\n"
"v_mfma_f32_16x16x32_bf16 v[136:139], a[56:59], a[136:139], v[136:139]\n"
"v_mfma_f32_16x16x32_bf16 v[136:139], a[60:63], a[140:143], v[136:139]\n"
"v_mfma_f32_16x16x32_bf16 v[108:111], a[52:55], a[140:143], v[108:111]\n"
"v_mfma_f32_16x16x32_bf16 v[108:111], a[48:51], a[136:139], v[108:111]\n"
"v_mfma_f32_16x16x32_bf16 v[112:115], a[48:51], a[144:147], v[112:115]\n"
"v_mfma_f32_16x16x32_bf16 v[112:115], a[52:55], a[148:151], v[112:115]\n"
"global_load_dwordx4 a[44:47], v0, s[34:35] offset:-1024 sc1\n"
"v_mfma_f32_16x16x32_bf16 v[140:143], a[60:63], a[148:151], v[140:143]\n"
"v_mfma_f32_16x16x32_bf16 v[140:143], a[56:59], a[144:147], v[140:143]\n"
"v_mfma_f32_16x16x32_bf16 a[12:15], a[56:59], a[152:155], a[12:15]\n"
"v_mfma_f32_16x16x32_bf16 a[12:15], a[60:63], a[156:159], a[12:15]\n"
"v_mfma_f32_16x16x32_bf16 a[8:11], a[52:55], a[156:159], a[8:11]\n"
"v_mfma_f32_16x16x32_bf16 a[8:11], a[48:51], a[152:155], a[8:11]\n"
"s_waitcnt vmcnt(15)\n"
"v_mfma_f32_16x16x32_bf16 v[144:147], a[64:67], a[96:99], v[144:147]\n"
"v_mfma_f32_16x16x32_bf16 v[144:147], a[68:71], a[100:103], v[144:147]\n"
"global_load_dwordx4 a[48:51], v0, s[34:35] offset:0 sc1\n"
"v_mfma_f32_16x16x32_bf16 v[172:175], a[76:79], a[100:103], v[172:175]\n"
"v_mfma_f32_16x16x32_bf16 v[172:175], a[72:75], a[96:99], v[172:175]\n"
"buffer_load_dwordx4 v1, s[48:51], s69 offen offset:3072 sc1 lds\n"
"v_mfma_f32_16x16x32_bf16 v[176:179], a[72:75], a[104:107], v[176:179]\n"
"v_mfma_f32_16x16x32_bf16 v[176:179], a[76:79], a[108:111], v[176:179]\n"
"v_mfma_f32_16x16x32_bf16 v[148:151], a[68:71], a[108:111], v[148:151]\n"
"v_mfma_f32_16x16x32_bf16 v[148:151], a[64:67], a[104:107], v[148:151]\n"
"v_mfma_f32_16x16x32_bf16 v[152:155], a[64:67], a[112:115], v[152:155]\n"
"v_mfma_f32_16x16x32_bf16 v[152:155], a[68:71], a[116:119], v[152:155]\n"
"global_load_dwordx4 a[52:55], v0, s[34:35] offset:1024 sc1\n"
"v_mfma_f32_16x16x32_bf16 v[180:183], a[76:79], a[116:119], v[180:183]\n"
"v_mfma_f32_16x16x32_bf16 v[180:183], a[72:75], a[112:115], v[180:183]\n"
"v_mfma_f32_16x16x32_bf16 v[184:187], a[72:75], a[120:123], v[184:187]\n"
"v_mfma_f32_16x16x32_bf16 v[184:187], a[76:79], a[124:127], v[184:187]\n"
"v_mfma_f32_16x16x32_bf16 v[156:159], a[68:71], a[124:127], v[156:159]\n"
"v_mfma_f32_16x16x32_bf16 v[156:159], a[64:67], a[120:123], v[156:159]\n"
"v_mfma_f32_16x16x32_bf16 v[160:163], a[64:67], a[128:131], v[160:163]\n"
"v_mfma_f32_16x16x32_bf16 v[160:163], a[68:71], a[132:135], v[160:163]\n"
"global_load_dwordx4 a[56:59], v0, s[34:35] offset:2048 sc1\n"
"v_mfma_f32_16x16x32_bf16 v[188:191], a[76:79], a[132:135], v[188:191]\n"
"v_mfma_f32_16x16x32_bf16 v[188:191], a[72:75], a[128:131], v[188:191]\n"
"v_mfma_f32_16x16x32_bf16 v[192:195], a[72:75], a[136:139], v[192:195]\n"
"s_cmp_lg_u32 s29, 0\n"
"s_cselect_b32 s60, s31, s60\n"
"s_cselect_b32 s30, 0, s30\n"
"v_mfma_f32_16x16x32_bf16 v[192:195], a[76:79], a[140:143], v[192:195]\n"
"v_mfma_f32_16x16x32_bf16 v[164:167], a[68:71], a[140:143], v[164:167]\n"
"s_cmp_lg_u32 s29, 0\n"
"s_cselect_b32 s31, s75, s72\n"
"v_mfma_f32_16x16x32_bf16 v[164:167], a[64:67], a[136:139], v[164:167]\n"
"s_sub_u32 s52, s52, s72\n"
"s_subb_u32 s53, s53, 0\n"
"v_mfma_f32_16x16x32_bf16 v[168:171], a[64:67], a[144:147], v[168:171]\n"
"s_add_u32 s52, s52, s31\n"
"s_addc_u32 s53, s53, 0\n"
"v_mfma_f32_16x16x32_bf16 v[168:171], a[68:71], a[148:151], v[168:171]\n"
"global_load_dwordx4 a[60:63], v0, s[34:35] offset:3072 sc1\n"
"v_mfma_f32_16x16x32_bf16 v[196:199], a[76:79], a[148:151], v[196:199]\n"
"s_mov_b32 s72, s31\n"
"v_mfma_f32_16x16x32_bf16 v[196:199], a[72:75], a[144:147], v[196:199]\n"
"v_mfma_f32_16x16x32_bf16 a[20:23], a[72:75], a[152:155], a[20:23]\n"
"s_add_u32 s34, s34, 8192\n"
"v_mfma_f32_16x16x32_bf16 a[20:23], a[76:79], a[156:159], a[20:23]\n"
"v_mfma_f32_16x16x32_bf16 a[16:19], a[68:71], a[156:159], a[16:19]\n"
"s_cmp_lg_u32 s29, 0\n"
"s_cselect_b32 s31, s69, s73\n"
"v_mfma_f32_16x16x32_bf16 a[16:19], a[64:67], a[152:155], a[16:19]\n"
"s_sub_u32 s54, s54, s73\n"
"s_subb_u32 s55, s55, 0\n"
"s_waitcnt vmcnt(12)\n"
"v_mfma_f32_16x16x32_bf16 v[200:203], a[80:83], a[96:99], v[200:203]\n"
"s_add_u32 s54, s54, s31\n"
"s_addc_u32 s55, s55, 0\n"
"v_mfma_f32_16x16x32_bf16 v[200:203], a[84:87], a[100:103], v[200:203]\n"
"global_load_dwordx4 a[64:67], v0, s[34:35] offset:-4096 sc1\n"
"v_mfma_f32_16x16x32_bf16 v[228:231], a[92:95], a[100:103], v[228:231]\n"
"s_mov_b32 s73, s31\n"
"ds_read_b128 a[100:103], v5 offset:1024\n"
"v_mfma_f32_16x16x32_bf16 v[228:231], a[88:91], a[96:99], v[228:231]\n"
"ds_read_b128 a[96:99], v5 offset:0\n"
"v_mfma_f32_16x16x32_bf16 v[232:235], a[88:91], a[104:107], v[232:235]\n"
"v_mfma_f32_16x16x32_bf16 v[232:235], a[92:95], a[108:111], v[232:235]\n"
"v_mfma_f32_16x16x32_bf16 v[204:207], a[84:87], a[108:111], v[204:207]\n"
"ds_read_b128 a[108:111], v5 offset:3072\n"
"v_mfma_f32_16x16x32_bf16 v[204:207], a[80:83], a[104:107], v[204:207]\n"
"ds_read_b128 a[104:107], v5 offset:2048\n"
"v_mfma_f32_16x16x32_bf16 v[208:211], a[80:83], a[112:115], v[208:211]\n"
"v_mfma_f32_16x16x32_bf16 v[208:211], a[84:87], a[116:119], v[208:211]\n"
"global_load_dwordx4 a[68:71], v0, s[34:35] offset:-3072 sc1\n"
"v_mfma_f32_16x16x32_bf16 v[236:239], a[92:95], a[116:119], v[236:239]\n"
"ds_read_b128 a[116:119], v5 offset:5120\n"
"v_mfma_f32_16x16x32_bf16 v[236:239], a[88:91], a[112:115], v[236:239]\n"
"ds_read_b128 a[112:115], v5 offset:4096\n"
"v_mfma_f32_16x16x32_bf16 v[240:243], a[88:91], a[120:123], v[240:243]\n"
"v_mfma_f32_16x16x32_bf16 v[240:243], a[92:95], a[124:127], v[240:243]\n"
"v_mfma_f32_16x16x32_bf16 v[212:215], a[84:87], a[124:127], v[212:215]\n"
"ds_read_b128 a[124:127], v5 offset:7168\n"
"v_mfma_f32_16x16x32_bf16 v[212:215], a[80:83], a[120:123], v[212:215]\n"
"ds_read_b128 a[120:123], v5 offset:6144\n"
"v_mfma_f32_16x16x32_bf16 v[216:219], a[80:83], a[128:131], v[216:219]\n"
"v_mfma_f32_16x16x32_bf16 v[216:219], a[84:87], a[132:135], v[216:219]\n"
"global_load_dwordx4 a[72:75], v0, s[34:35] offset:-2048 sc1\n"
"v_mfma_f32_16x16x32_bf16 v[244:247], a[92:95], a[132:135], v[244:247]\n"
"ds_read_b128 a[132:135], v5 offset:9216\n"
"v_mfma_f32_16x16x32_bf16 v[244:247], a[88:91], a[128:131], v[244:247]\n"
"ds_read_b128 a[128:131], v5 offset:8192\n"
"v_mfma_f32_16x16x32_bf16 v[248:251], a[88:91], a[136:139], v[248:251]\n"
"v_mfma_f32_16x16x32_bf16 v[248:251], a[92:95], a[140:143], v[248:251]\n"
"v_mfma_f32_16x16x32_bf16 v[220:223], a[84:87], a[140:143], v[220:223]\n"
"ds_read_b128 a[140:143], v5 offset:11264\n"
"v_mfma_f32_16x16x32_bf16 v[220:223], a[80:83], a[136:139], v[220:223]\n"
"ds_read_b128 a[136:139], v5 offset:10240\n"
"v_mfma_f32_16x16x32_bf16 v[224:227], a[80:83], a[144:147], v[224:227]\n"
"v_mfma_f32_16x16x32_bf16 v[224:227], a[84:87], a[148:151], v[224:227]\n"
"global_load_dwordx4 a[76:79], v0, s[34:35] offset:-1024 sc1\n"
"v_mfma_f32_16x16x32_bf16 v[252:255], a[92:95], a[148:151], v[252:255]\n"
"ds_read_b128 a[148:151], v5 offset:13312\n"
"v_mfma_f32_16x16x32_bf16 v[252:255], a[88:91], a[144:147], v[252:255]\n"
"ds_read_b128 a[144:147], v5 offset:12288\n"
"v_mfma_f32_16x16x32_bf16 a[28:31], a[88:91], a[152:155], a[28:31]\n"
"v_mfma_f32_16x16x32_bf16 a[28:31], a[92:95], a[156:159], a[28:31]\n"
"v_mfma_f32_16x16x32_bf16 a[24:27], a[84:87], a[156:159], a[24:27]\n"
"ds_read_b128 a[156:159], v5 offset:15360\n"
"v_mfma_f32_16x16x32_bf16 a[24:27], a[80:83], a[152:155], a[24:27]\n"
"s_waitcnt vmcnt(9) lgkmcnt(11)\n"
"ds_read_b128 a[152:155], v5 offset:14336\n"
"v_mfma_f32_16x16x32_bf16 v[32:35], a[32:35], a[96:99], v[32:35]\n"
"v_mfma_f32_16x16x32_bf16 v[32:35], a[36:39], a[100:103], v[32:35]\n"
"global_load_dwordx4 a[80:83], v0, s[34:35] offset:0 sc1\n"
"v_mfma_f32_16x16x32_bf16 v[60:63], a[44:47], a[100:103], v[60:63]\n"
"v_mfma_f32_16x16x32_bf16 v[60:63], a[40:43], a[96:99], v[60:63]\n"
"v_mfma_f32_16x16x32_bf16 v[64:67], a[40:43], a[104:107], v[64:67]\n"
"v_mfma_f32_16x16x32_bf16 v[64:67], a[44:47], a[108:111], v[64:67]\n"
"v_mfma_f32_16x16x32_bf16 v[36:39], a[36:39], a[108:111], v[36:39]\n"
"v_mfma_f32_16x16x32_bf16 v[36:39], a[32:35], a[104:107], v[36:39]\n"
"s_waitcnt lgkmcnt(8)\n"
"v_mfma_f32_16x16x32_bf16 v[40:43], a[32:35], a[112:115], v[40:43]\n"
"v_mfma_f32_16x16x32_bf16 v[40:43], a[36:39], a[116:119], v[40:43]\n"
"global_load_dwordx4 a[84:87], v0, s[34:35] offset:1024 sc1\n"
"v_mfma_f32_16x16x32_bf16 v[68:71], a[44:47], a[116:119], v[68:71]\n"
"v_mfma_f32_16x16x32_bf16 v[68:71], a[40:43], a[112:115], v[68:71]\n"
"v_mfma_f32_16x16x32_bf16 v[72:75], a[40:43], a[120:123], v[72:75]\n"
"v_mfma_f32_16x16x32_bf16 v[72:75], a[44:47], a[124:127], v[72:75]\n"
"v_mfma_f32_16x16x32_bf16 v[44:47], a[36:39], a[124:127], v[44:47]\n"
"v_mfma_f32_16x16x32_bf16 v[44:47], a[32:35], a[120:123], v[44:47]\n"
"s_waitcnt lgkmcnt(4)\n"
"v_mfma_f32_16x16x32_bf16 v[48:51], a[32:35], a[128:131], v[48:51]\n"
"s_lshr_b32 s66, 20, s62\n"
"v_mfma_f32_16x16x32_bf16 v[48:51], a[36:39], a[132:135], v[48:51]\n"
"global_load_dwordx4 a[88:91], v0, s[34:35] offset:2048 sc1\n"
"v_mfma_f32_16x16x32_bf16 v[76:79], a[44:47], a[132:135], v[76:79]\n"
"s_and_b32 s66, s66, 1\n"
"s_cselect_b32 s70, 4294574080, 0\n"
"s_cselect_b32 s71, -1, 0\n"
"s_cselect_b32 s66, 0, 131072\n"
"v_mfma_f32_16x16x32_bf16 v[76:79], a[40:43], a[128:131], v[76:79]\n"
"v_mfma_f32_16x16x32_bf16 v[80:83], a[40:43], a[136:139], v[80:83]\n"
"s_add_u32 s70, s70, s66\n"
"v_mfma_f32_16x16x32_bf16 v[80:83], a[44:47], a[140:143], v[80:83]\n"
"v_mfma_f32_16x16x32_bf16 v[52:55], a[36:39], a[140:143], v[52:55]\n"
"v_mfma_f32_16x16x32_bf16 v[52:55], a[32:35], a[136:139], v[52:55]\n"
"s_waitcnt lgkmcnt(0)\n"
"v_mfma_f32_16x16x32_bf16 v[56:59], a[32:35], a[144:147], v[56:59]\n"
"v_mfma_f32_16x16x32_bf16 v[56:59], a[36:39], a[148:151], v[56:59]\n"
"global_load_dwordx4 a[92:95], v0, s[34:35] offset:3072 sc1\n"
"v_mfma_f32_16x16x32_bf16 v[84:87], a[44:47], a[148:151], v[84:87]\n"
"v_mfma_f32_16x16x32_bf16 v[84:87], a[40:43], a[144:147], v[84:87]\n"
"v_mfma_f32_16x16x32_bf16 a[4:7], a[40:43], a[152:155], a[4:7]\n"
"s_add_u32 s34, s34, 8192\n"
"v_mfma_f32_16x16x32_bf16 a[4:7], a[44:47], a[156:159], a[4:7]\n"
"v_mfma_f32_16x16x32_bf16 a[0:3], a[36:39], a[156:159], a[0:3]\n"
"v_mfma_f32_16x16x32_bf16 a[0:3], a[32:35], a[152:155], a[0:3]\n"
"s_waitcnt vmcnt(8)\n"
"v_mfma_f32_16x16x32_bf16 v[88:91], a[48:51], a[96:99], v[88:91]\n"
"v_mfma_f32_16x16x32_bf16 v[88:91], a[52:55], a[100:103], v[88:91]\n"
"global_load_dwordx4 a[32:35], v0, s[34:35] offset:-4096 sc1\n"
"v_mfma_f32_16x16x32_bf16 v[116:119], a[60:63], a[100:103], v[116:119]\n"
"v_mfma_f32_16x16x32_bf16 v[116:119], a[56:59], a[96:99], v[116:119]\n"
"v_mfma_f32_16x16x32_bf16 v[120:123], a[56:59], a[104:107], v[120:123]\n"
"v_mfma_f32_16x16x32_bf16 v[120:123], a[60:63], a[108:111], v[120:123]\n"
"v_mfma_f32_16x16x32_bf16 v[92:95], a[52:55], a[108:111], v[92:95]\n"
"v_mfma_f32_16x16x32_bf16 v[92:95], a[48:51], a[104:107], v[92:95]\n"
"v_mfma_f32_16x16x32_bf16 v[96:99], a[48:51], a[112:115], v[96:99]\n"
"v_mfma_f32_16x16x32_bf16 v[96:99], a[52:55], a[116:119], v[96:99]\n"
"global_load_dwordx4 a[36:39], v0, s[34:35] offset:-3072 sc1\n"
"v_mfma_f32_16x16x32_bf16 v[124:127], a[60:63], a[116:119], v[124:127]\n"
"buffer_load_dwordx4 v[12:15], v0, s[56:59], %25 offen sc1\n"
"v_mfma_f32_16x16x32_bf16 v[124:127], a[56:59], a[112:115], v[124:127]\n"
"v_mfma_f32_16x16x32_bf16 v[128:131], a[56:59], a[120:123], v[128:131]\n"
"v_mfma_f32_16x16x32_bf16 v[128:131], a[60:63], a[124:127], v[128:131]\n"
"v_mfma_f32_16x16x32_bf16 v[100:103], a[52:55], a[124:127], v[100:103]\n"
"v_mfma_f32_16x16x32_bf16 v[100:103], a[48:51], a[120:123], v[100:103]\n"
"v_mfma_f32_16x16x32_bf16 v[104:107], a[48:51], a[128:131], v[104:107]\n"
"v_mfma_f32_16x16x32_bf16 v[104:107], a[52:55], a[132:135], v[104:107]\n"
"global_load_dwordx4 a[40:43], v0, s[34:35] offset:-2048 sc1\n"
"v_mfma_f32_16x16x32_bf16 v[132:135], a[60:63], a[132:135], v[132:135]\n"
"v_mfma_f32_16x16x32_bf16 v[132:135], a[56:59], a[128:131], v[132:135]\n"
"v_mfma_f32_16x16x32_bf16 v[136:139], a[56:59], a[136:139], v[136:139]\n"
"v_mfma_f32_16x16x32_bf16 v[136:139], a[60:63], a[140:143], v[136:139]\n"
"v_mfma_f32_16x16x32_bf16 v[108:111], a[52:55], a[140:143], v[108:111]\n"
"v_mfma_f32_16x16x32_bf16 v[108:111], a[48:51], a[136:139], v[108:111]\n"
"v_mfma_f32_16x16x32_bf16 v[112:115], a[48:51], a[144:147], v[112:115]\n"
"v_mfma_f32_16x16x32_bf16 v[112:115], a[52:55], a[148:151], v[112:115]\n"
"global_load_dwordx4 a[44:47], v0, s[34:35] offset:-1024 sc1\n"
"v_mfma_f32_16x16x32_bf16 v[140:143], a[60:63], a[148:151], v[140:143]\n"
"v_mfma_f32_16x16x32_bf16 v[140:143], a[56:59], a[144:147], v[140:143]\n"
"v_mfma_f32_16x16x32_bf16 a[12:15], a[56:59], a[152:155], a[12:15]\n"
"v_mfma_f32_16x16x32_bf16 a[12:15], a[60:63], a[156:159], a[12:15]\n"
"v_mfma_f32_16x16x32_bf16 a[8:11], a[52:55], a[156:159], a[8:11]\n"
"v_mfma_f32_16x16x32_bf16 a[8:11], a[48:51], a[152:155], a[8:11]\n"
"s_waitcnt vmcnt(9)\n"
"v_mfma_f32_16x16x32_bf16 v[144:147], a[64:67], a[96:99], v[144:147]\n"
"v_mfma_f32_16x16x32_bf16 v[144:147], a[68:71], a[100:103], v[144:147]\n"
"global_load_dwordx4 a[48:51], v0, s[34:35] offset:0 sc1\n"
"v_mfma_f32_16x16x32_bf16 v[172:175], a[76:79], a[100:103], v[172:175]\n"
"v_mfma_f32_16x16x32_bf16 v[172:175], a[72:75], a[96:99], v[172:175]\n"
"v_mfma_f32_16x16x32_bf16 v[176:179], a[72:75], a[104:107], v[176:179]\n"
"v_mfma_f32_16x16x32_bf16 v[176:179], a[76:79], a[108:111], v[176:179]\n"
"v_mfma_f32_16x16x32_bf16 v[148:151], a[68:71], a[108:111], v[148:151]\n"
"v_mfma_f32_16x16x32_bf16 v[148:151], a[64:67], a[104:107], v[148:151]\n"
"v_mfma_f32_16x16x32_bf16 v[152:155], a[64:67], a[112:115], v[152:155]\n"
"v_mfma_f32_16x16x32_bf16 v[152:155], a[68:71], a[116:119], v[152:155]\n"
"global_load_dwordx4 a[52:55], v0, s[34:35] offset:1024 sc1\n"
"v_mfma_f32_16x16x32_bf16 v[180:183], a[76:79], a[116:119], v[180:183]\n"
"v_mfma_f32_16x16x32_bf16 v[180:183], a[72:75], a[112:115], v[180:183]\n"
"v_mfma_f32_16x16x32_bf16 v[184:187], a[72:75], a[120:123], v[184:187]\n"
"v_mfma_f32_16x16x32_bf16 v[184:187], a[76:79], a[124:127], v[184:187]\n"
"v_mfma_f32_16x16x32_bf16 v[156:159], a[68:71], a[124:127], v[156:159]\n"
"v_mfma_f32_16x16x32_bf16 v[156:159], a[64:67], a[120:123], v[156:159]\n"
"v_mfma_f32_16x16x32_bf16 v[160:163], a[64:67], a[128:131], v[160:163]\n"
"v_mfma_f32_16x16x32_bf16 v[160:163], a[68:71], a[132:135], v[160:163]\n"
"global_load_dwordx4 a[56:59], v0, s[34:35] offset:2048 sc1\n"
"v_mfma_f32_16x16x32_bf16 v[188:191], a[76:79], a[132:135], v[188:191]\n"
"v_mfma_f32_16x16x32_bf16 v[188:191], a[72:75], a[128:131], v[188:191]\n"
"v_mfma_f32_16x16x32_bf16 v[192:195], a[72:75], a[136:139], v[192:195]\n"
"v_mfma_f32_16x16x32_bf16 v[192:195], a[76:79], a[140:143], v[192:195]\n"
"v_mfma_f32_16x16x32_bf16 v[164:167], a[68:71], a[140:143], v[164:167]\n"
"s_add_u32 s67, s78, s62\n"
"v_mfma_f32_16x16x32_bf16 v[164:167], a[64:67], a[136:139], v[164:167]\n"
"v_mfma_f32_16x16x32_bf16 v[168:171], a[64:67], a[144:147], v[168:171]\n"
"s_add_u32 s67, s67, 1\n"
"v_mfma_f32_16x16x32_bf16 v[168:171], a[68:71], a[148:151], v[168:171]\n"
"global_load_dwordx4 a[60:63], v0, s[34:35] offset:3072 sc1\n"
"v_mfma_f32_16x16x32_bf16 v[196:199], a[76:79], a[148:151], v[196:199]\n"
"v_mfma_f32_16x16x32_bf16 v[196:199], a[72:75], a[144:147], v[196:199]\n"
"v_mfma_f32_16x16x32_bf16 a[20:23], a[72:75], a[152:155], a[20:23]\n"
"s_add_u32 s34, s34, 8192\n"
"v_mfma_f32_16x16x32_bf16 a[20:23], a[76:79], a[156:159], a[20:23]\n"
"v_mfma_f32_16x16x32_bf16 a[16:19], a[68:71], a[156:159], a[16:19]\n"
"v_mfma_f32_16x16x32_bf16 a[16:19], a[64:67], a[152:155], a[16:19]\n"
"s_waitcnt vmcnt(9)\n"
"v_mfma_f32_16x16x32_bf16 v[200:203], a[80:83], a[96:99], v[200:203]\n"
"v_mfma_f32_16x16x32_bf16 v[200:203], a[84:87], a[100:103], v[200:203]\n"
"global_load_dwordx4 a[64:67], v0, s[34:35] offset:-4096 sc1\n"
"v_mfma_f32_16x16x32_bf16 v[228:231], a[92:95], a[100:103], v[228:231]\n"
"ds_read_b128 a[100:103], v2 offset:17408\n"
"v_mfma_f32_16x16x32_bf16 v[228:231], a[88:91], a[96:99], v[228:231]\n"
"ds_read_b128 a[96:99], v2 offset:16384\n"
"v_mfma_f32_16x16x32_bf16 v[232:235], a[88:91], a[104:107], v[232:235]\n"
"v_mfma_f32_16x16x32_bf16 v[232:235], a[92:95], a[108:111], v[232:235]\n"
"v_mfma_f32_16x16x32_bf16 v[204:207], a[84:87], a[108:111], v[204:207]\n"
"ds_read_b128 a[108:111], v2 offset:19456\n"
"v_mfma_f32_16x16x32_bf16 v[204:207], a[80:83], a[104:107], v[204:207]\n"
"ds_read_b128 a[104:107], v2 offset:18432\n"
"v_mfma_f32_16x16x32_bf16 v[208:211], a[80:83], a[112:115], v[208:211]\n"
"v_mfma_f32_16x16x32_bf16 v[208:211], a[84:87], a[116:119], v[208:211]\n"
"global_load_dwordx4 a[68:71], v0, s[34:35] offset:-3072 sc1\n"
"v_mfma_f32_16x16x32_bf16 v[236:239], a[92:95], a[116:119], v[236:239]\n"
"ds_read_b128 a[116:119], v2 offset:21504\n"
"v_mfma_f32_16x16x32_bf16 v[236:239], a[88:91], a[112:115], v[236:239]\n"
"ds_read_b128 a[112:115], v2 offset:20480\n"
"v_mfma_f32_16x16x32_bf16 v[240:243], a[88:91], a[120:123], v[240:243]\n"
"v_mfma_f32_16x16x32_bf16 v[240:243], a[92:95], a[124:127], v[240:243]\n"
"v_mfma_f32_16x16x32_bf16 v[212:215], a[84:87], a[124:127], v[212:215]\n"
"ds_read_b128 a[124:127], v2 offset:23552\n"
"v_mfma_f32_16x16x32_bf16 v[212:215], a[80:83], a[120:123], v[212:215]\n"
"ds_read_b128 a[120:123], v2 offset:22528\n"
"v_mfma_f32_16x16x32_bf16 v[216:219], a[80:83], a[128:131], v[216:219]\n"
"v_mfma_f32_16x16x32_bf16 v[216:219], a[84:87], a[132:135], v[216:219]\n"
"global_load_dwordx4 a[72:75], v0, s[34:35] offset:-2048 sc1\n"
"v_mfma_f32_16x16x32_bf16 v[244:247], a[92:95], a[132:135], v[244:247]\n"
"ds_read_b128 a[132:135], v2 offset:25600\n"
"v_mfma_f32_16x16x32_bf16 v[244:247], a[88:91], a[128:131], v[244:247]\n"
"ds_read_b128 a[128:131], v2 offset:24576\n"
"v_mfma_f32_16x16x32_bf16 v[248:251], a[88:91], a[136:139], v[248:251]\n"
"v_mfma_f32_16x16x32_bf16 v[248:251], a[92:95], a[140:143], v[248:251]\n"
"v_mfma_f32_16x16x32_bf16 v[220:223], a[84:87], a[140:143], v[220:223]\n"
"ds_read_b128 a[140:143], v2 offset:27648\n"
"v_mfma_f32_16x16x32_bf16 v[220:223], a[80:83], a[136:139], v[220:223]\n"
"ds_read_b128 a[136:139], v2 offset:26624\n"
"v_mfma_f32_16x16x32_bf16 v[224:227], a[80:83], a[144:147], v[224:227]\n"
"v_mfma_f32_16x16x32_bf16 v[224:227], a[84:87], a[148:151], v[224:227]\n"
"global_load_dwordx4 a[76:79], v0, s[34:35] offset:-1024 sc1\n"
"v_mfma_f32_16x16x32_bf16 v[252:255], a[92:95], a[148:151], v[252:255]\n"
"ds_read_b128 a[148:151], v2 offset:29696\n"
"v_mfma_f32_16x16x32_bf16 v[252:255], a[88:91], a[144:147], v[252:255]\n"
"ds_read_b128 a[144:147], v2 offset:28672\n"
"v_mfma_f32_16x16x32_bf16 a[28:31], a[88:91], a[152:155], a[28:31]\n"
"v_mfma_f32_16x16x32_bf16 a[28:31], a[92:95], a[156:159], a[28:31]\n"
"v_mfma_f32_16x16x32_bf16 a[24:27], a[84:87], a[156:159], a[24:27]\n"
"ds_read_b128 a[156:159], v2 offset:31744\n"
"v_mfma_f32_16x16x32_bf16 a[24:27], a[80:83], a[152:155], a[24:27]\n"
"ds_read_b128 a[152:155], v2 offset:30720\n"
"s_waitcnt vmcnt(8) lgkmcnt(12)\n"
"v_mfma_f32_16x16x32_bf16 v[32:35], a[32:35], a[96:99], v[32:35]\n"
"v_mfma_f32_16x16x32_bf16 v[32:35], a[36:39], a[100:103], v[32:35]\n"
"global_load_dwordx4 a[80:83], v0, s[34:35] offset:0 sc1\n"
"v_mfma_f32_16x16x32_bf16 v[60:63], a[44:47], a[100:103], v[60:63]\n"
"s_barrier\n"
"v_mfma_f32_16x16x32_bf16 v[60:63], a[40:43], a[96:99], v[60:63]\n"
"s_add_u32 s48, s48, s70\n"
"s_addc_u32 s49, s49, s71\n"
"v_mfma_f32_16x16x32_bf16 v[64:67], a[40:43], a[104:107], v[64:67]\n"
"v_mfma_f32_16x16x32_bf16 v[64:67], a[44:47], a[108:111], v[64:67]\n"
"v_mfma_f32_16x16x32_bf16 v[36:39], a[36:39], a[108:111], v[36:39]\n"
"s_add_u32 s52, s52, s70\n"
"s_addc_u32 s53, s53, s71\n"
"v_subrev_u32 v12, s67, v12\n"
"v_mfma_f32_16x16x32_bf16 v[36:39], a[32:35], a[104:107], v[36:39]\n"
"s_waitcnt lgkmcnt(8)\n"
"v_xor_b32 v13, s79, v13\n"
"v_mfma_f32_16x16x32_bf16 v[40:43], a[32:35], a[112:115], v[40:43]\n"
"s_add_u32 s54, s54, s70\n"
"s_addc_u32 s55, s55, s71\n"
"v_subrev_u32 v14, s67, v14\n"
"v_mfma_f32_16x16x32_bf16 v[40:43], a[36:39], a[116:119], v[40:43]\n"
"global_load_dwordx4 a[84:87], v0, s[34:35] offset:1024 sc1\n"
"v_xor_b32 v15, s79, v15\n"
"v_mfma_f32_16x16x32_bf16 v[68:71], a[44:47], a[116:119], v[68:71]\n"
"v_or_b32 v12, v12, v14\n"
"v_mfma_f32_16x16x32_bf16 v[68:71], a[40:43], a[112:115], v[68:71]\n"
"v_or_b32 v13, v13, v15\n"
"v_mfma_f32_16x16x32_bf16 v[72:75], a[40:43], a[120:123], v[72:75]\n"
"v_or_b32 v12, v12, v13\n"
"v_mfma_f32_16x16x32_bf16 v[72:75], a[44:47], a[124:127], v[72:75]\n"
"s_add_u32 s61, s61, 2048\n"
"v_cmp_lt_u32 vcc, 15, v12\n"
"v_mfma_f32_16x16x32_bf16 v[44:47], a[36:39], a[124:127], v[44:47]\n"
"s_cbranch_vccnz L_slow2\n"
"L_back2:\n"
"v_mfma_f32_16x16x32_bf16 v[44:47], a[32:35], a[120:123], v[44:47]\n"
"s_waitcnt lgkmcnt(4)\n"
"v_mfma_f32_16x16x32_bf16 v[48:51], a[32:35], a[128:131], v[48:51]\n"
"v_lshlrev_b32 v16, 16, v18\n"
"v_mfma_f32_16x16x32_bf16 v[48:51], a[36:39], a[132:135], v[48:51]\n"
"global_load_dwordx4 a[88:91], v0, s[34:35] offset:2048 sc1\n"
"v_and_b32 v17, s63, v18\n"
"v_mfma_f32_16x16x32_bf16 v[76:79], a[44:47], a[132:135], v[76:79]\n"
"v_lshlrev_b32 v18, 16, v19\n"
"v_mfma_f32_16x16x32_bf16 v[76:79], a[40:43], a[128:131], v[76:79]\n"
"v_and_b32 v19, s63, v19\n"
"v_mfma_f32_16x16x32_bf16 v[80:83], a[40:43], a[136:139], v[80:83]\n"
"v_add_f32 v144, v144, v16\n"
"v_add_f32 v145, v145, v17\n"
"v_mfma_f32_16x16x32_bf16 v[80:83], a[44:47], a[140:143], v[80:83]\n"
"v_add_f32 v146, v146, v18\n"
"v_mfma_f32_16x16x32_bf16 v[52:55], a[36:39], a[140:143], v[52:55]\n"
"v_add_f32 v147, v147, v19\n"
"v_mfma_f32_16x16x32_bf16 v[52:55], a[32:35], a[136:139], v[52:55]\n"
"s_waitcnt lgkmcnt(0)\n"
"v_add_f32 v148, v148, v16\n"
"v_mfma_f32_16x16x32_bf16 v[56:59], a[32:35], a[144:147], v[56:59]\n"
"v_add_f32 v149, v149, v17\n"
"v_mfma_f32_16x16x32_bf16 v[56:59], a[36:39], a[148:151], v[56:59]\n"
"global_load_dwordx4 a[92:95], v0, s[34:35] offset:3072 sc1\n"
"v_add_f32 v150, v150, v18\n"
"v_mfma_f32_16x16x32_bf16 v[84:87], a[44:47], a[148:151], v[84:87]\n"
"v_add_f32 v151, v151, v19\n"
"v_mfma_f32_16x16x32_bf16 v[84:87], a[40:43], a[144:147], v[84:87]\n"
"v_add_f32 v152, v152, v16\n"
"v_mfma_f32_16x16x32_bf16 a[4:7], a[40:43], a[152:155], a[4:7]\n"
"s_add_u32 s34, s34, 8192\n"
"v_add_f32 v153, v153, v17\n"
"v_mfma_f32_16x16x32_bf16 a[4:7], a[44:47], a[156:159], a[4:7]\n"
"v_add_f32 v154, v154, v18\n"
"v_mfma_f32_16x16x32_bf16 a[0:3], a[36:39], a[156:159], a[0:3]\n"
"v_add_f32 v155, v155, v19\n"
"v_add_f32 v156, v156, v16\n"
"v_mfma_f32_16x16x32_bf16 a[0:3], a[32:35], a[152:155], a[0:3]\n"
"s_waitcnt vmcnt(8)\n"
"v_add_f32 v157, v157, v17\n"
"v_mfma_f32_16x16x32_bf16 v[88:91], a[48:51], a[96:99], v[88:91]\n"
"v_add_f32 v158, v158, v18\n"
"v_mfma_f32_16x16x32_bf16 v[88:91], a[52:55], a[100:103], v[88:91]\n"
"global_load_dwordx4 a[32:35], v0, s[34:35] offset:-4096 sc1\n"
"v_add_f32 v159, v159, v19\n"
"v_mfma_f32_16x16x32_bf16 v[116:119], a[60:63], a[100:103], v[116:119]\n"
"ds_read_b128 a[100:103], v3 offset:17408\n"
"v_add_f32 v160, v160, v16\n"
"v_mfma_f32_16x16x32_bf16 v[116:119], a[56:59], a[96:99], v[116:119]\n"
"ds_read_b128 a[96:99], v3 offset:16384\n"
"v_add_f32 v161, v161, v17\n"
"v_mfma_f32_16x16x32_bf16 v[120:123], a[56:59], a[104:107], v[120:123]\n"
"v_add_f32 v162, v162, v18\n"
"v_mfma_f32_16x16x32_bf16 v[120:123], a[60:63], a[108:111], v[120:123]\n"
"v_add_f32 v163, v163, v19\n"
"v_mfma_f32_16x16x32_bf16 v[92:95], a[52:55], a[108:111], v[92:95]\n"
"ds_read_b128 a[108:111], v3 offset:19456\n"
"v_add_f32 v164, v164, v16\n"
"v_mfma_f32_16x16x32_bf16 v[92:95], a[48:51], a[104:107], v[92:95]\n"
"ds_read_b128 a[104:107], v3 offset:18432\n"
"v_add_f32 v165, v165, v17\n"
"v_mfma_f32_16x16x32_bf16 v[96:99], a[48:51], a[112:115], v[96:99]\n"
"v_add_f32 v166, v166, v18\n"
"v_add_f32 v167, v167, v19\n"
"v_mfma_f32_16x16x32_bf16 v[96:99], a[52:55], a[116:119], v[96:99]\n"
"global_load_dwordx4 a[36:39], v0, s[34:35] offset:-3072 sc1\n"
"v_add_f32 v168, v168, v16\n"
"v_mfma_f32_16x16x32_bf16 v[124:127], a[60:63], a[116:119], v[124:127]\n"
"ds_read_b128 a[116:119], v3 offset:21504\n"
"v_add_f32 v169, v169, v17\n"
"v_mfma_f32_16x16x32_bf16 v[124:127], a[56:59], a[112:115], v[124:127]\n"
"ds_read_b128 a[112:115], v3 offset:20480\n"
"v_add_f32 v170, v170, v18\n"
"v_mfma_f32_16x16x32_bf16 v[128:131], a[56:59], a[120:123], v[128:131]\n"
"v_add_f32 v171, v171, v19\n"
"v_mfma_f32_16x16x32_bf16 v[128:131], a[60:63], a[124:127], v[128:131]\n"
"v_accvgpr_read_b32 v12, a16\n"
"v_mfma_f32_16x16x32_bf16 v[100:103], a[52:55], a[124:127], v[100:103]\n"
"ds_read_b128 a[124:127], v3 offset:23552\n"
"v_accvgpr_read_b32 v13, a17\n"
"v_mfma_f32_16x16x32_bf16 v[100:103], a[48:51], a[120:123], v[100:103]\n"
"ds_read_b128 a[120:123], v3 offset:22528\n"
"v_accvgpr_read_b32 v14, a18\n"
"v_mfma_f32_16x16x32_bf16 v[104:107], a[48:51], a[128:131], v[104:107]\n"
"v_accvgpr_read_b32 v15, a19\n"
"v_add_f32 v12, v12, v16\n"
"v_mfma_f32_16x16x32_bf16 v[104:107], a[52:55], a[132:135], v[104:107]\n"
"global_load_dwordx4 a[40:43], v0, s[34:35] offset:-2048 sc1\n"
"v_add_f32 v13, v13, v17\n"
"v_mfma_f32_16x16x32_bf16 v[132:135], a[60:63], a[132:135], v[132:135]\n"
"ds_read_b128 a[132:135], v3 offset:25600\n"
"v_add_f32 v14, v14, v18\n"
"v_mfma_f32_16x16x32_bf16 v[132:135], a[56:59], a[128:131], v[132:135]\n"
"ds_read_b128 a[128:131], v3 offset:24576\n"
"v_add_f32 v15, v15, v19\n"
"v_mfma_f32_16x16x32_bf16 v[136:139], a[56:59], a[136:139], v[136:139]\n"
"v_accvgpr_write_b32 a16, v12\n"
"v_mfma_f32_16x16x32_bf16 v[136:139], a[60:63], a[140:143], v[136:139]\n"
"v_accvgpr_write_b32 a17, v13\n"
"v_mfma_f32_16x16x32_bf16 v[108:111], a[52:55], a[140:143], v[108:111]\n"
"ds_read_b128 a[140:143], v3 offset:27648\n"
"v_accvgpr_write_b32 a18, v14\n"
"v_mfma_f32_16x16x32_bf16 v[108:111], a[48:51], a[136:139], v[108:111]\n"
"ds_read_b128 a[136:139], v3 offset:26624\n"
"v_accvgpr_write_b32 a19, v15\n"
"v_mfma_f32_16x16x32_bf16 v[112:115], a[48:51], a[144:147], v[112:115]\n"
"L_st7944:\n"
"s_cbranch_execz L_st9164\n"
"v_lshlrev_b32 v20, 16, v22\n"
"v_and_b32 v21, s63, v22\n"
"v_mfma_f32_16x16x32_bf16 v[112:115], a[52:55], a[148:151], v[112:115]\n"
"global_load_dwordx4 a[44:47], v0, s[34:35] offset:-1024 sc1\n"
"v_lshlrev_b32 v22, 16, v23\n"
"v_mfma_f32_16x16x32_bf16 v[140:143], a[60:63], a[148:151], v[140:143]\n"
"ds_read_b128 a[148:151], v3 offset:29696\n"
"v_and_b32 v23, s63, v23\n"
"v_mfma_f32_16x16x32_bf16 v[140:143], a[56:59], a[144:147], v[140:143]\n"
"ds_read_b128 a[144:147], v3 offset:28672\n"
"v_add_f32 v172, v172, v20\n"
"v_mfma_f32_16x16x32_bf16 a[12:15], a[56:59], a[152:155], a[12:15]\n"
"v_add_f32 v173, v173, v21\n"
"v_mfma_f32_16x16x32_bf16 a[12:15], a[60:63], a[156:159], a[12:15]\n"
"v_add_f32 v174, v174, v22\n"
"v_mfma_f32_16x16x32_bf16 a[8:11], a[52:55], a[156:159], a[8:11]\n"
"ds_read_b128 a[156:159], v3 offset:31744\n"
"v_add_f32 v175, v175, v23\n"
"v_mfma_f32_16x16x32_bf16 a[8:11], a[48:51], a[152:155], a[8:11]\n"
"s_waitcnt vmcnt(8) lgkmcnt(11)\n"
"ds_read_b128 a[152:155], v3 offset:30720\n"
"v_add_f32 v176, v176, v20\n"
"v_mfma_f32_16x16x32_bf16 v[32:35], a[64:67], a[96:99], v[32:35]\n"
"v_add_f32 v177, v177, v21\n"
"v_mfma_f32_16x16x32_bf16 v[32:35], a[68:71], a[100:103], v[32:35]\n"
"global_load_dwordx4 a[48:51], v0, s[34:35] offset:0 sc1\n"
"v_add_f32 v178, v178, v22\n"
"v_mfma_f32_16x16x32_bf16 v[60:63], a[76:79], a[100:103], v[60:63]\n"
"v_add_f32 v179, v179, v23\n"
"v_mfma_f32_16x16x32_bf16 v[60:63], a[72:75], a[96:99], v[60:63]\n"
"v_add_f32 v180, v180, v20\n"
"v_add_f32 v181, v181, v21\n"
"v_mfma_f32_16x16x32_bf16 v[64:67], a[72:75], a[104:107], v[64:67]\n"
"v_add_f32 v182, v182, v22\n"
"v_mfma_f32_16x16x32_bf16 v[64:67], a[76:79], a[108:111], v[64:67]\n"
"v_add_f32 v183, v183, v23\n"
"v_mfma_f32_16x16x32_bf16 v[36:39], a[68:71], a[108:111], v[36:39]\n"
"v_add_f32 v184, v184, v20\n"
"v_mfma_f32_16x16x32_bf16 v[36:39], a[64:67], a[104:107], v[36:39]\n"
"s_waitcnt lgkmcnt(8)\n"
"v_add_f32 v185, v185, v21\n"
"v_mfma_f32_16x16x32_bf16 v[40:43], a[64:67], a[112:115], v[40:43]\n"
"v_add_f32 v186, v186, v22\n"
"v_mfma_f32_16x16x32_bf16 v[40:43], a[68:71], a[116:119], v[40:43]\n"
"global_load_dwordx4 a[52:55], v0, s[34:35] offset:1024 sc1\n"
"v_add_f32 v187, v187, v23\n"
"v_mfma_f32_16x16x32_bf16 v[68:71], a[76:79], a[116:119], v[68:71]\n"
"v_add_f32 v188, v188, v20\n"
"v_mfma_f32_16x16x32_bf16 v[68:71], a[72:75], a[112:115], v[68:71]\n"
"v_add_f32 v189, v189, v21\n"
"v_mfma_f32_16x16x32_bf16 v[72:75], a[72:75], a[120:123], v[72:75]\n"
"v_add_f32 v190, v190, v22\n"
"v_add_f32 v191, v191, v23\n"
"v_mfma_f32_16x16x32_bf16 v[72:75], a[76:79], a[124:127], v[72:75]\n"
"v_add_f32 v192, v192, v20\n"
"v_mfma_f32_16x16x32_bf16 v[44:47], a[68:71], a[124:127], v[44:47]\n"
"v_add_f32 v193, v193, v21\n"
"v_mfma_f32_16x16x32_bf16 v[44:47], a[64:67], a[120:123], v[44:47]\n"
"s_waitcnt lgkmcnt(4)\n"
"v_add_f32 v194, v194, v22\n"
"v_mfma_f32_16x16x32_bf16 v[48:51], a[64:67], a[128:131], v[48:51]\n"
"v_add_f32 v195, v195, v23\n"
"v_mfma_f32_16x16x32_bf16 v[48:51], a[68:71], a[132:135], v[48:51]\n"
"global_load_dwordx4 a[56:59], v0, s[34:35] offset:2048 sc1\n"
"v_add_f32 v196, v196, v20\n"
"v_mfma_f32_16x16x32_bf16 v[76:79], a[76:79], a[132:135], v[76:79]\n"
"v_add_f32 v197, v197, v21\n"
"v_mfma_f32_16x16x32_bf16 v[76:79], a[72:75], a[128:131], v[76:79]\n"
"v_add_f32 v198, v198, v22\n"
"v_mfma_f32_16x16x32_bf16 v[80:83], a[72:75], a[136:139], v[80:83]\n"
"v_add_f32 v199, v199, v23\n"
"v_mfma_f32_16x16x32_bf16 v[80:83], a[76:79], a[140:143], v[80:83]\n"
"v_accvgpr_read_b32 v12, a20\n"
"v_mfma_f32_16x16x32_bf16 v[52:55], a[68:71], a[140:143], v[52:55]\n"
"v_accvgpr_read_b32 v13, a21\n"
"v_accvgpr_read_b32 v14, a22\n"
"v_mfma_f32_16x16x32_bf16 v[52:55], a[64:67], a[136:139], v[52:55]\n"
"s_waitcnt lgkmcnt(0)\n"
"v_accvgpr_read_b32 v15, a23\n"
"v_mfma_f32_16x16x32_bf16 v[56:59], a[64:67], a[144:147], v[56:59]\n"
"v_add_f32 v12, v12, v20\n"
"v_mfma_f32_16x16x32_bf16 v[56:59], a[68:71], a[148:151], v[56:59]\n"
"global_load_dwordx4 a[60:63], v0, s[34:35] offset:3072 sc1\n"
"v_add_f32 v13, v13, v21\n"
"v_mfma_f32_16x16x32_bf16 v[84:87], a[76:79], a[148:151], v[84:87]\n"
"v_add_f32 v14, v14, v22\n"
"v_mfma_f32_16x16x32_bf16 v[84:87], a[72:75], a[144:147], v[84:87]\n"
"v_add_f32 v15, v15, v23\n"
"v_mfma_f32_16x16x32_bf16 a[4:7], a[72:75], a[152:155], a[4:7]\n"
"s_add_u32 s34, s34, 8192\n"
"v_accvgpr_write_b32 a20, v12\n"
"v_mfma_f32_16x16x32_bf16 a[4:7], a[76:79], a[156:159], a[4:7]\n"
"v_accvgpr_write_b32 a21, v13\n"
"v_mfma_f32_16x16x32_bf16 a[0:3], a[68:71], a[156:159], a[0:3]\n"
"v_accvgpr_write_b32 a22, v14\n"
"v_mfma_f32_16x16x32_bf16 a[0:3], a[64:67], a[152:155], a[0:3]\n"
"s_waitcnt vmcnt(8)\n"
"v_accvgpr_write_b32 a23, v15\n"
"v_lshlrev_b32 v24, 16, v26\n"
"v_mfma_f32_16x16x32_bf16 v[88:91], a[80:83], a[96:99], v[88:91]\n"
"v_and_b32 v25, s63, v26\n"
"v_mfma_f32_16x16x32_bf16 v[88:91], a[84:87], a[100:103], v[88:91]\n"
"global_load_dwordx4 a[64:67], v0, s[34:35] offset:-4096 sc1\n"
"v_lshlrev_b32 v26, 16, v27\n"
"v_mfma_f32_16x16x32_bf16 v[116:119], a[92:95], a[100:103], v[116:119]\n"
"ds_read_b128 a[100:103], v4 offset:17408\n"
"v_and_b32 v27, s63, v27\n"
"v_mfma_f32_16x16x32_bf16 v[116:119], a[88:91], a[96:99], v[116:119]\n"
"ds_read_b128 a[96:99], v4 offset:16384\n"
"v_add_f32 v200, v200, v24\n"
"v_mfma_f32_16x16x32_bf16 v[120:123], a[88:91], a[104:107], v[120:123]\n"
"v_add_f32 v201, v201, v25\n"
"v_mfma_f32_16x16x32_bf16 v[120:123], a[92:95], a[108:111], v[120:123]\n"
"v_add_f32 v202, v202, v26\n"
"v_mfma_f32_16x16x32_bf16 v[92:95], a[84:87], a[108:111], v[92:95]\n"
"ds_read_b128 a[108:111], v4 offset:19456\n"
"v_add_f32 v203, v203, v27\n"
"v_mfma_f32_16x16x32_bf16 v[92:95], a[80:83], a[104:107], v[92:95]\n"
"ds_read_b128 a[104:107], v4 offset:18432\n"
"v_add_f32 v204, v204, v24\n"
"v_mfma_f32_16x16x32_bf16 v[96:99], a[80:83], a[112:115], v[96:99]\n"
"v_add_f32 v205, v205, v25\n"
"v_add_f32 v206, v206, v26\n"
"v_mfma_f32_16x16x32_bf16 v[96:99], a[84:87], a[116:119], v[96:99]\n"
"global_load_dwordx4 a[68:71], v0, s[34:35] offset:-3072 sc1\n"
"v_add_f32 v207, v207, v27\n"
"v_mfma_f32_16x16x32_bf16 v[124:127], a[92:95], a[116:119], v[124:127]\n"
"ds_read_b128 a[116:119], v4 offset:21504\n"
"v_add_f32 v208, v208, v24\n"
"v_mfma_f32_16x16x32_bf16 v[124:127], a[88:91], a[112:115], v[124:127]\n"
"ds_read_b128 a[112:115], v4 offset:20480\n"
"v_add_f32 v209, v209, v25\n"
"v_mfma_f32_16x16x32_bf16 v[128:131], a[88:91], a[120:123], v[128:131]\n"
"v_add_f32 v210, v210, v26\n"
"v_mfma_f32_16x16x32_bf16 v[128:131], a[92:95], a[124:127], v[128:131]\n"
"v_add_f32 v211, v211, v27\n"
"v_mfma_f32_16x16x32_bf16 v[100:103], a[84:87], a[124:127], v[100:103]\n"
"ds_read_b128 a[124:127], v4 offset:23552\n"
"v_add_f32 v212, v212, v24\n"
"v_mfma_f32_16x16x32_bf16 v[100:103], a[80:83], a[120:123], v[100:103]\n"
"ds_read_b128 a[120:123], v4 offset:22528\n"
"v_add_f32 v213, v213, v25\n"
"v_mfma_f32_16x16x32_bf16 v[104:107], a[80:83], a[128:131], v[104:107]\n"
"v_add_f32 v214, v214, v26\n"
"v_add_f32 v215, v215, v27\n"
"v_mfma_f32_16x16x32_bf16 v[104:107], a[84:87], a[132:135], v[104:107]\n"
"global_load_dwordx4 a[72:75], v0, s[34:35] offset:-2048 sc1\n"
"v_add_f32 v216, v216, v24\n"
"v_mfma_f32_16x16x32_bf16 v[132:135], a[92:95], a[132:135], v[132:135]\n"
"ds_read_b128 a[132:135], v4 offset:25600\n"
"v_add_f32 v217, v217, v25\n"
"v_mfma_f32_16x16x32_bf16 v[132:135], a[88:91], a[128:131], v[132:135]\n"
"ds_read_b128 a[128:131], v4 offset:24576\n"
"v_add_f32 v218, v218, v26\n"
"v_mfma_f32_16x16x32_bf16 v[136:139], a[88:91], a[136:139], v[136:139]\n"
"v_add_f32 v219, v219, v27\n"
"v_mfma_f32_16x16x32_bf16 v[136:139], a[92:95], a[140:143], v[136:139]\n"
"v_add_f32 v220, v220, v24\n"
"v_mfma_f32_16x16x32_bf16 v[108:111], a[84:87], a[140:143], v[108:111]\n"
"ds_read_b128 a[140:143], v4 offset:27648\n"
"v_add_f32 v221, v221, v25\n"
"v_mfma_f32_16x16x32_bf16 v[108:111], a[80:83], a[136:139], v[108:111]\n"
"ds_read_b128 a[136:139], v4 offset:26624\n"
"v_add_f32 v222, v222, v26\n"
"v_mfma_f32_16x16x32_bf16 v[112:115], a[80:83], a[144:147], v[112:115]\n"
"v_add_f32 v223, v223, v27\n"
"v_mfma_f32_16x16x32_bf16 v[112:115], a[84:87], a[148:151], v[112:115]\n"
"global_load_dwordx4 a[76:79], v0, s[34:35] offset:-1024 sc1\n"
"v_add_f32 v224, v224, v24\n"
"v_mfma_f32_16x16x32_bf16 v[140:143], a[92:95], a[148:151], v[140:143]\n"
"ds_read_b128 a[148:151], v4 offset:29696\n"
"v_add_f32 v225, v225, v25\n"
"v_mfma_f32_16x16x32_bf16 v[140:143], a[88:91], a[144:147], v[140:143]\n"
"ds_read_b128 a[144:147], v4 offset:28672\n"
"v_add_f32 v226, v226, v26\n"
"v_mfma_f32_16x16x32_bf16 a[12:15], a[88:91], a[152:155], a[12:15]\n"
"v_add_f32 v227, v227, v27\n"
"v_accvgpr_read_b32 v12, a24\n"
"v_mfma_f32_16x16x32_bf16 a[12:15], a[92:95], a[156:159], a[12:15]\n"
"v_accvgpr_read_b32 v13, a25\n"
"v_mfma_f32_16x16x32_bf16 a[8:11], a[84:87], a[156:159], a[8:11]\n"
"ds_read_b128 a[156:159], v4 offset:31744\n"
"v_accvgpr_read_b32 v14, a26\n"
"v_mfma_f32_16x16x32_bf16 a[8:11], a[80:83], a[152:155], a[8:11]\n"
"s_waitcnt vmcnt(8) lgkmcnt(11)\n"
"ds_read_b128 a[152:155], v4 offset:30720\n"
"v_accvgpr_read_b32 v15, a27\n"
"v_mfma_f32_16x16x32_bf16 v[32:35], a[32:35], a[96:99], v[32:35]\n"
"v_add_f32 v12, v12, v24\n"
"v_mfma_f32_16x16x32_bf16 v[32:35], a[36:39], a[100:103], v[32:35]\n"
"global_load_dwordx4 a[80:83], v0, s[34:35] offset:0 sc1\n"
"v_add_f32 v13, v13, v25\n"
"v_mfma_f32_16x16x32_bf16 v[60:63], a[44:47], a[100:103], v[60:63]\n"
"v_add_f32 v14, v14, v26\n"
"v_mfma_f32_16x16x32_bf16 v[60:63], a[40:43], a[96:99], v[60:63]\n"
"v_add_f32 v15, v15, v27\n"
"v_mfma_f32_16x16x32_bf16 v[64:67], a[40:43], a[104:107], v[64:67]\n"
"v_accvgpr_write_b32 a24, v12\n"
"v_accvgpr_write_b32 a25, v13\n"
"v_mfma_f32_16x16x32_bf16 v[64:67], a[44:47], a[108:111], v[64:67]\n"
"v_accvgpr_write_b32 a26, v14\n"
"v_mfma_f32_16x16x32_bf16 v[36:39], a[36:39], a[108:111], v[36:39]\n"
"v_accvgpr_write_b32 a27, v15\n"
"v_mfma_f32_16x16x32_bf16 v[36:39], a[32:35], a[104:107], v[36:39]\n"
"s_waitcnt lgkmcnt(8)\n"
"v_lshlrev_b32 v28, 16, v30\n"
"v_mfma_f32_16x16x32_bf16 v[40:43], a[32:35], a[112:115], v[40:43]\n"
"v_and_b32 v29, s63, v30\n"
"v_mfma_f32_16x16x32_bf16 v[40:43], a[36:39], a[116:119], v[40:43]\n"
"global_load_dwordx4 a[84:87], v0, s[34:35] offset:1024 sc1\n"
"v_lshlrev_b32 v30, 16, v31\n"
"v_mfma_f32_16x16x32_bf16 v[68:71], a[44:47], a[116:119], v[68:71]\n"
"v_and_b32 v31, s63, v31\n"
"v_mfma_f32_16x16x32_bf16 v[68:71], a[40:43], a[112:115], v[68:71]\n"
"v_add_f32 v228, v228, v28\n"
"v_mfma_f32_16x16x32_bf16 v[72:75], a[40:43], a[120:123], v[72:75]\n"
"v_add_f32 v229, v229, v29\n"
"v_mfma_f32_16x16x32_bf16 v[72:75], a[44:47], a[124:127], v[72:75]\n"
"v_add_f32 v230, v230, v30\n"
"v_mfma_f32_16x16x32_bf16 v[44:47], a[36:39], a[124:127], v[44:47]\n"
"v_add_f32 v231, v231, v31\n"
"v_add_f32 v232, v232, v28\n"
"v_mfma_f32_16x16x32_bf16 v[44:47], a[32:35], a[120:123], v[44:47]\n"
"s_waitcnt lgkmcnt(4)\n"
"v_add_f32 v233, v233, v29\n"
"v_mfma_f32_16x16x32_bf16 v[48:51], a[32:35], a[128:131], v[48:51]\n"
"v_add_f32 v234, v234, v30\n"
"v_mfma_f32_16x16x32_bf16 v[48:51], a[36:39], a[132:135], v[48:51]\n"
"global_load_dwordx4 a[88:91], v0, s[34:35] offset:2048 sc1\n"
"v_add_f32 v235, v235, v31\n"
"v_mfma_f32_16x16x32_bf16 v[76:79], a[44:47], a[132:135], v[76:79]\n"
"v_add_f32 v236, v236, v28\n"
"v_mfma_f32_16x16x32_bf16 v[76:79], a[40:43], a[128:131], v[76:79]\n"
"v_add_f32 v237, v237, v29\n"
"v_mfma_f32_16x16x32_bf16 v[80:83], a[40:43], a[136:139], v[80:83]\n"
"v_add_f32 v238, v238, v30\n"
"v_mfma_f32_16x16x32_bf16 v[80:83], a[44:47], a[140:143], v[80:83]\n"
"v_add_f32 v239, v239, v31\n"
"v_mfma_f32_16x16x32_bf16 v[52:55], a[36:39], a[140:143], v[52:55]\n"
"v_add_f32 v240, v240, v28\n"
"v_mfma_f32_16x16x32_bf16 v[52:55], a[32:35], a[136:139], v[52:55]\n"
"s_waitcnt lgkmcnt(0)\n"
"v_add_f32 v241, v241, v29\n"
"v_add_f32 v242, v242, v30\n"
"v_mfma_f32_16x16x32_bf16 v[56:59], a[32:35], a[144:147], v[56:59]\n"
"v_add_f32 v243, v243, v31\n"
"v_mfma_f32_16x16x32_bf16 v[56:59], a[36:39], a[148:151], v[56:59]\n"
"global_load_dwordx4 a[92:95], v0, s[34:35] offset:3072 sc1\n"
"v_add_f32 v244, v244, v28\n"
"v_mfma_f32_16x16x32_bf16 v[84:87], a[44:47], a[148:151], v[84:87]\n"
"v_add_f32 v245, v245, v29\n"
"v_mfma_f32_16x16x32_bf16 v[84:87], a[40:43], a[144:147], v[84:87]\n"
"v_add_f32 v246, v246, v30\n"
"v_mfma_f32_16x16x32_bf16 a[4:7], a[40:43], a[152:155], a[4:7]\n"
"s_add_u32 s34, s34, 8192\n"
"v_add_f32 v247, v247, v31\n"
"v_mfma_f32_16x16x32_bf16 a[4:7], a[44:47], a[156:159], a[4:7]\n"
"v_add_f32 v248, v248, v28\n"
"v_mfma_f32_16x16x32_bf16 a[0:3], a[36:39], a[156:159], a[0:3]\n"
"v_add_f32 v249, v249, v29\n"
"v_mfma_f32_16x16x32_bf16 a[0:3], a[32:35], a[152:155], a[0:3]\n"
"s_waitcnt vmcnt(8)\n"
"v_add_f32 v250, v250, v30\n"
"v_mfma_f32_16x16x32_bf16 v[88:91], a[48:51], a[96:99], v[88:91]\n"
"v_add_f32 v251, v251, v31\n"
"v_add_f32 v252, v252, v28\n"
"v_mfma_f32_16x16x32_bf16 v[88:91], a[52:55], a[100:103], v[88:91]\n"
"global_load_dwordx4 a[32:35], v0, s[34:35] offset:-4096 sc1\n"
"v_add_f32 v253, v253, v29\n"
"v_mfma_f32_16x16x32_bf16 v[116:119], a[60:63], a[100:103], v[116:119]\n"
"ds_read_b128 a[100:103], v5 offset:17408\n"
"v_add_f32 v254, v254, v30\n"
"v_mfma_f32_16x16x32_bf16 v[116:119], a[56:59], a[96:99], v[116:119]\n"
"ds_read_b128 a[96:99], v5 offset:16384\n"
"v_add_f32 v255, v255, v31\n"
"v_mfma_f32_16x16x32_bf16 v[120:123], a[56:59], a[104:107], v[120:123]\n"
"v_accvgpr_read_b32 v12, a28\n"
"v_mfma_f32_16x16x32_bf16 v[120:123], a[60:63], a[108:111], v[120:123]\n"
"v_accvgpr_read_b32 v13, a29\n"
"v_mfma_f32_16x16x32_bf16 v[92:95], a[52:55], a[108:111], v[92:95]\n"
"ds_read_b128 a[108:111], v5 offset:19456\n"
"v_accvgpr_read_b32 v14, a30\n"
"v_mfma_f32_16x16x32_bf16 v[92:95], a[48:51], a[104:107], v[92:95]\n"
"ds_read_b128 a[104:107], v5 offset:18432\n"
"v_accvgpr_read_b32 v15, a31\n"
"v_mfma_f32_16x16x32_bf16 v[96:99], a[48:51], a[112:115], v[96:99]\n"
"v_add_f32 v12, v12, v28\n"
"v_add_f32 v13, v13, v29\n"
"v_mfma_f32_16x16x32_bf16 v[96:99], a[52:55], a[116:119], v[96:99]\n"
"global_load_dwordx4 a[36:39], v0, s[34:35] offset:-3072 sc1\n"
"v_add_f32 v14, v14, v30\n"
"v_mfma_f32_16x16x32_bf16 v[124:127], a[60:63], a[116:119], v[124:127]\n"
"ds_read_b128 a[116:119], v5 offset:21504\n"
"v_add_f32 v15, v15, v31\n"
"v_mfma_f32_16x16x32_bf16 v[124:127], a[56:59], a[112:115], v[124:127]\n"
"ds_read_b128 a[112:115], v5 offset:20480\n"
"v_accvgpr_write_b32 a28, v12\n"
"v_mfma_f32_16x16x32_bf16 v[128:131], a[56:59], a[120:123], v[128:131]\n"
"v_accvgpr_write_b32 a29, v13\n"
"v_mfma_f32_16x16x32_bf16 v[128:131], a[60:63], a[124:127], v[128:131]\n"
"v_accvgpr_write_b32 a30, v14\n"
"v_mfma_f32_16x16x32_bf16 v[100:103], a[52:55], a[124:127], v[100:103]\n"
"ds_read_b128 a[124:127], v5 offset:23552\n"
"v_accvgpr_write_b32 a31, v15\n"
"v_mfma_f32_16x16x32_bf16 v[100:103], a[48:51], a[120:123], v[100:103]\n"
"ds_read_b128 a[120:123], v5 offset:22528\n"
"v_mfma_f32_16x16x32_bf16 v[104:107], a[48:51], a[128:131], v[104:107]\n"
"v_mfma_f32_16x16x32_bf16 v[104:107], a[52:55], a[132:135], v[104:107]\n"
"global_load_dwordx4 a[40:43], v0, s[34:35] offset:-2048 sc1\n"
"v_mfma_f32_16x16x32_bf16 v[132:135], a[60:63], a[132:135], v[132:135]\n"
"ds_read_b128 a[132:135], v5 offset:25600\n"
"v_mfma_f32_16x16x32_bf16 v[132:135], a[56:59], a[128:131], v[132:135]\n"
"ds_read_b128 a[128:131], v5 offset:24576\n"
"v_mfma_f32_16x16x32_bf16 v[136:139], a[56:59], a[136:139], v[136:139]\n"
"ds_read_b128 v[16:19], v7 offset:32768\n"
"v_mfma_f32_16x16x32_bf16 v[136:139], a[60:63], a[140:143], v[136:139]\n"
"v_mfma_f32_16x16x32_bf16 v[108:111], a[52:55], a[140:143], v[108:111]\n"
"ds_read_b128 a[140:143], v5 offset:27648\n"
"v_mfma_f32_16x16x32_bf16 v[108:111], a[48:51], a[136:139], v[108:111]\n"
"ds_read_b128 a[136:139], v5 offset:26624\n"
"v_mfma_f32_16x16x32_bf16 v[112:115], a[48:51], a[144:147], v[112:115]\n"
"v_mfma_f32_16x16x32_bf16 v[112:115], a[52:55], a[148:151], v[112:115]\n"
"global_load_dwordx4 a[44:47], v0, s[34:35] offset:-1024 sc1\n"
"v_mfma_f32_16x16x32_bf16 v[140:143], a[60:63], a[148:151], v[140:143]\n"
"ds_read_b128 a[148:151], v5 offset:29696\n"
"v_mfma_f32_16x16x32_bf16 v[140:143], a[56:59], a[144:147], v[140:143]\n"
"ds_read_b128 a[144:147], v5 offset:28672\n"
"v_mfma_f32_16x16x32_bf16 a[12:15], a[56:59], a[152:155], a[12:15]\n"
"v_mfma_f32_16x16x32_bf16 a[12:15], a[60:63], a[156:159], a[12:15]\n"
"v_mfma_f32_16x16x32_bf16 a[8:11], a[52:55], a[156:159], a[8:11]\n"
"ds_read_b128 a[156:159], v5 offset:31744\n"
"v_mfma_f32_16x16x32_bf16 a[8:11], a[48:51], a[152:155], a[8:11]\n"
"ds_read_b128 a[152:155], v5 offset:30720\n"
"s_branch L_P6\n"
"L_P3F:\n"
"L_st8279:\n"
"s_cbranch_execz L_retB\n"
"s_waitcnt vmcnt(10) lgkmcnt(13)\n"
"v_mfma_f32_16x16x32_bf16 v[32:35], a[32:35], a[96:99], v[32:35]\n"
"s_waitcnt lgkmcnt(6)\n"
"global_store_dwordx4 v0, v[24:27], s[52:53] offset:2048 sc1\n"
"s_cmp_lt_u32 s27, 12582912\n"
"s_cselect_b32 s38, 8192000, 0\n"
"s_cselect_b32 s42, 12582144, 0\n"
"v_mfma_f32_16x16x32_bf16 v[32:35], a[36:39], a[100:103], v[32:35]\n"
"global_load_dwordx4 a[80:83], v0, s[34:35] offset:0 sc1\n"
"v_mfma_f32_16x16x32_bf16 v[60:63], a[44:47], a[100:103], v[60:63]\n"
"s_barrier\n"
"v_mfma_f32_16x16x32_bf16 v[60:63], a[40:43], a[96:99], v[60:63]\n"
"s_add_u32 m0, s64, 12288\n"
"v_mfma_f32_16x16x32_bf16 v[64:67], a[40:43], a[104:107], v[64:67]\n"
"v_mfma_f32_16x16x32_bf16 v[64:67], a[44:47], a[108:111], v[64:67]\n"
"s_add_u32 s67, s27, 0\n"
"v_mfma_f32_16x16x32_bf16 v[36:39], a[36:39], a[108:111], v[36:39]\n"
"v_mfma_f32_16x16x32_bf16 v[36:39], a[32:35], a[104:107], v[36:39]\n"
"v_mfma_f32_16x16x32_bf16 v[40:43], a[32:35], a[112:115], v[40:43]\n"
"buffer_load_dwordx4 v6, s[40:43], s67 offen offset:0 lds\n"
"v_mfma_f32_16x16x32_bf16 v[40:43], a[36:39], a[116:119], v[40:43]\n"
"global_load_dwordx4 a[84:87], v0, s[34:35] offset:1024 sc1\n"
"v_mfma_f32_16x16x32_bf16 v[68:71], a[44:47], a[116:119], v[68:71]\n"
"v_mfma_f32_16x16x32_bf16 v[68:71], a[40:43], a[112:115], v[68:71]\n"
"s_add_u32 s67, s27, 15360\n"
"v_mfma_f32_16x16x32_bf16 v[72:75], a[40:43], a[120:123], v[72:75]\n"
"v_mfma_f32_16x16x32_bf16 v[72:75], a[44:47], a[124:127], v[72:75]\n"
"v_mfma_f32_16x16x32_bf16 v[44:47], a[36:39], a[124:127], v[44:47]\n"
"buffer_load_dwordx4 v6, s[40:43], s67 offen offset:1024 lds\n"
"v_mfma_f32_16x16x32_bf16 v[44:47], a[32:35], a[120:123], v[44:47]\n"
"s_waitcnt lgkmcnt(4)\n"
"v_mfma_f32_16x16x32_bf16 v[48:51], a[32:35], a[128:131], v[48:51]\n"
"v_mfma_f32_16x16x32_bf16 v[48:51], a[36:39], a[132:135], v[48:51]\n"
"global_load_dwordx4 a[88:91], v0, s[34:35] offset:2048 sc1\n"
"v_lshrrev_b32 v16, 4, v0\n"
"v_mfma_f32_16x16x32_bf16 v[76:79], a[44:47], a[132:135], v[76:79]\n"
"s_add_u32 s67, s27, 30720\n"
"v_and_b32 v16, 0x30, v16\n"
"v_mfma_f32_16x16x32_bf16 v[76:79], a[40:43], a[128:131], v[76:79]\n"
"ds_read_b128 v[28:31], v2 offset:23552\n"
"buffer_load_dwordx2 v[18:19], v16, s[44:47], s61 offen offset:0\n"
"v_mfma_f32_16x16x32_bf16 v[80:83], a[40:43], a[136:139], v[80:83]\n"
"buffer_load_dwordx2 v[8:9], off, s[56:59], %14 sc1\n"
"v_mfma_f32_16x16x32_bf16 v[80:83], a[44:47], a[140:143], v[80:83]\n"
"buffer_load_dwordx4 v6, s[40:43], s67 offen offset:2048 lds\n"
"v_mfma_f32_16x16x32_bf16 v[52:55], a[36:39], a[140:143], v[52:55]\n"
"v_mfma_f32_16x16x32_bf16 v[52:55], a[32:35], a[136:139], v[52:55]\n"
"s_waitcnt lgkmcnt(1)\n"
"v_mfma_f32_16x16x32_bf16 v[56:59], a[32:35], a[144:147], v[56:59]\n"
"s_add_u32 s67, s27, 46080\n"
"v_mfma_f32_16x16x32_bf16 v[56:59], a[36:39], a[148:151], v[56:59]\n"
"global_load_dwordx4 a[92:95], v0, s[34:35] offset:3072 sc1\n"
"v_lshrrev_b32 v20, 4, v0\n"
"v_mfma_f32_16x16x32_bf16 v[84:87], a[44:47], a[148:151], v[84:87]\n"
"v_and_b32 v20, 0x30, v20\n"
"v_mfma_f32_16x16x32_bf16 v[84:87], a[40:43], a[144:147], v[84:87]\n"
"buffer_load_dwordx4 v6, s[40:43], s67 offen offset:3072 lds\n"
"buffer_load_dwordx2 v[22:23], v20, s[44:47], s61 offen offset:8\n"
"v_mfma_f32_16x16x32_bf16 a[4:7], a[40:43], a[152:155], a[4:7]\n"
"s_add_u32 s34, s34, 8192\n"
"v_mfma_f32_16x16x32_bf16 a[4:7], a[44:47], a[156:159], a[4:7]\n"
"v_mfma_f32_16x16x32_bf16 a[0:3], a[36:39], a[156:159], a[0:3]\n"
"s_waitcnt lgkmcnt(0)\n"
"global_store_dwordx4 v0, v[28:31], s[52:53] offset:3072 sc1\n"
"v_mfma_f32_16x16x32_bf16 a[0:3], a[32:35], a[152:155], a[0:3]\n"
"s_waitcnt vmcnt(18)\n"
"v_mfma_f32_16x16x32_bf16 v[88:91], a[48:51], a[96:99], v[88:91]\n"
"v_mfma_f32_16x16x32_bf16 v[88:91], a[52:55], a[100:103], v[88:91]\n"
"global_load_dwordx4 a[32:35], v0, s[34:35] offset:-4096 sc1\n"
"v_mfma_f32_16x16x32_bf16 v[116:119], a[60:63], a[100:103], v[116:119]\n"
"v_mfma_f32_16x16x32_bf16 v[116:119], a[56:59], a[96:99], v[116:119]\n"
"v_mfma_f32_16x16x32_bf16 v[120:123], a[56:59], a[104:107], v[120:123]\n"
"v_mfma_f32_16x16x32_bf16 v[120:123], a[60:63], a[108:111], v[120:123]\n"
"v_mfma_f32_16x16x32_bf16 v[92:95], a[52:55], a[108:111], v[92:95]\n"
"v_mfma_f32_16x16x32_bf16 v[92:95], a[48:51], a[104:107], v[92:95]\n"
"v_mfma_f32_16x16x32_bf16 v[96:99], a[48:51], a[112:115], v[96:99]\n"
"v_mfma_f32_16x16x32_bf16 v[96:99], a[52:55], a[116:119], v[96:99]\n"
"global_load_dwordx4 a[36:39], v0, s[34:35] offset:-3072 sc1\n"
"v_mfma_f32_16x16x32_bf16 v[124:127], a[60:63], a[116:119], v[124:127]\n"
"v_mfma_f32_16x16x32_bf16 v[124:127], a[56:59], a[112:115], v[124:127]\n"
"v_mfma_f32_16x16x32_bf16 v[128:131], a[56:59], a[120:123], v[128:131]\n"
"v_mfma_f32_16x16x32_bf16 v[128:131], a[60:63], a[124:127], v[128:131]\n"
"v_mfma_f32_16x16x32_bf16 v[100:103], a[52:55], a[124:127], v[100:103]\n"
"v_mfma_f32_16x16x32_bf16 v[100:103], a[48:51], a[120:123], v[100:103]\n"
"v_mfma_f32_16x16x32_bf16 v[104:107], a[48:51], a[128:131], v[104:107]\n"
"ds_read_b128 v[24:27], v2 offset:24576\n"
"v_mfma_f32_16x16x32_bf16 v[104:107], a[52:55], a[132:135], v[104:107]\n"
"global_load_dwordx4 a[40:43], v0, s[34:35] offset:-2048 sc1\n"
"v_mfma_f32_16x16x32_bf16 v[132:135], a[60:63], a[132:135], v[132:135]\n"
"v_mfma_f32_16x16x32_bf16 v[132:135], a[56:59], a[128:131], v[132:135]\n"
"v_mfma_f32_16x16x32_bf16 v[136:139], a[56:59], a[136:139], v[136:139]\n"
"v_mfma_f32_16x16x32_bf16 v[136:139], a[60:63], a[140:143], v[136:139]\n"
"v_mfma_f32_16x16x32_bf16 v[108:111], a[52:55], a[140:143], v[108:111]\n"
"v_mfma_f32_16x16x32_bf16 v[108:111], a[48:51], a[136:139], v[108:111]\n"
"v_mfma_f32_16x16x32_bf16 v[112:115], a[48:51], a[144:147], v[112:115]\n"
"v_mfma_f32_16x16x32_bf16 v[112:115], a[52:55], a[148:151], v[112:115]\n"
"global_load_dwordx4 a[44:47], v0, s[34:35] offset:-1024 sc1\n"
"v_mfma_f32_16x16x32_bf16 v[140:143], a[60:63], a[148:151], v[140:143]\n"
"v_mfma_f32_16x16x32_bf16 v[140:143], a[56:59], a[144:147], v[140:143]\n"
"v_mfma_f32_16x16x32_bf16 a[12:15], a[56:59], a[152:155], a[12:15]\n"
"s_waitcnt lgkmcnt(0)\n"
"global_store_dwordx4 v0, v[24:27], s[54:55] offset:-4096 sc1\n"
"v_mfma_f32_16x16x32_bf16 a[12:15], a[60:63], a[156:159], a[12:15]\n"
"v_mfma_f32_16x16x32_bf16 a[8:11], a[52:55], a[156:159], a[8:11]\n"
"v_mfma_f32_16x16x32_bf16 a[8:11], a[48:51], a[152:155], a[8:11]\n"
"s_waitcnt vmcnt(18)\n"
"v_mfma_f32_16x16x32_bf16 v[144:147], a[64:67], a[96:99], v[144:147]\n"
"v_mfma_f32_16x16x32_bf16 v[144:147], a[68:71], a[100:103], v[144:147]\n"
"global_load_dwordx4 a[48:51], v0, s[34:35] offset:0 sc1\n"
"v_mfma_f32_16x16x32_bf16 v[172:175], a[76:79], a[100:103], v[172:175]\n"
"v_mfma_f32_16x16x32_bf16 v[172:175], a[72:75], a[96:99], v[172:175]\n"
"v_mfma_f32_16x16x32_bf16 v[176:179], a[72:75], a[104:107], v[176:179]\n"
"v_mfma_f32_16x16x32_bf16 v[176:179], a[76:79], a[108:111], v[176:179]\n"
"v_mfma_f32_16x16x32_bf16 v[148:151], a[68:71], a[108:111], v[148:151]\n"
"v_mfma_f32_16x16x32_bf16 v[148:151], a[64:67], a[104:107], v[148:151]\n"
"v_mfma_f32_16x16x32_bf16 v[152:155], a[64:67], a[112:115], v[152:155]\n"
"v_mfma_f32_16x16x32_bf16 v[152:155], a[68:71], a[116:119], v[152:155]\n"
"global_load_dwordx4 a[52:55], v0, s[34:35] offset:1024 sc1\n"
"v_mfma_f32_16x16x32_bf16 v[180:183], a[76:79], a[116:119], v[180:183]\n"
"v_mfma_f32_16x16x32_bf16 v[180:183], a[72:75], a[112:115], v[180:183]\n"
"v_mfma_f32_16x16x32_bf16 v[184:187], a[72:75], a[120:123], v[184:187]\n"
"v_mfma_f32_16x16x32_bf16 v[184:187], a[76:79], a[124:127], v[184:187]\n"
"v_mfma_f32_16x16x32_bf16 v[156:159], a[68:71], a[124:127], v[156:159]\n"
"v_mfma_f32_16x16x32_bf16 v[156:159], a[64:67], a[120:123], v[156:159]\n"
"ds_read_b128 v[28:31], v2 offset:26624\n"
"v_mfma_f32_16x16x32_bf16 v[160:163], a[64:67], a[128:131], v[160:163]\n"
"v_mfma_f32_16x16x32_bf16 v[160:163], a[68:71], a[132:135], v[160:163]\n"
"global_load_dwordx4 a[56:59], v0, s[34:35] offset:2048 sc1\n"
"v_mfma_f32_16x16x32_bf16 v[188:191], a[76:79], a[132:135], v[188:191]\n"
"v_mfma_f32_16x16x32_bf16 v[188:191], a[72:75], a[128:131], v[188:191]\n"
"v_mfma_f32_16x16x32_bf16 v[192:195], a[72:75], a[136:139], v[192:195]\n"
"v_mfma_f32_16x16x32_bf16 v[192:195], a[76:79], a[140:143], v[192:195]\n"
"v_mfma_f32_16x16x32_bf16 v[164:167], a[68:71], a[140:143], v[164:167]\n"
"v_mfma_f32_16x16x32_bf16 v[164:167], a[64:67], a[136:139], v[164:167]\n"
"v_mfma_f32_16x16x32_bf16 v[168:171], a[64:67], a[144:147], v[168:171]\n"
"v_mfma_f32_16x16x32_bf16 v[168:171], a[68:71], a[148:151], v[168:171]\n"
"global_load_dwordx4 a[60:63], v0, s[34:35] offset:3072 sc1\n"
"v_mfma_f32_16x16x32_bf16 v[196:199], a[76:79], a[148:151], v[196:199]\n"
"s_waitcnt lgkmcnt(0)\n"
"global_store_dwordx4 v0, v[28:31], s[54:55] offset:-2048 sc1\n"
"v_mfma_f32_16x16x32_bf16 v[196:199], a[72:75], a[144:147], v[196:199]\n"
"v_mfma_f32_16x16x32_bf16 a[20:23], a[72:75], a[152:155], a[20:23]\n"
"s_add_u32 s34, s34, 8192\n"
"v_mfma_f32_16x16x32_bf16 a[20:23], a[76:79], a[156:159], a[20:23]\n"
"v_mfma_f32_16x16x32_bf16 a[16:19], a[68:71], a[156:159], a[16:19]\n"
"v_mfma_f32_16x16x32_bf16 a[16:19], a[64:67], a[152:155], a[16:19]\n"
"s_waitcnt vmcnt(13)\n"
"v_mfma_f32_16x16x32_bf16 v[200:203], a[80:83], a[96:99], v[200:203]\n"
"v_mfma_f32_16x16x32_bf16 v[200:203], a[84:87], a[100:103], v[200:203]\n"
"global_load_dwordx4 a[64:67], v0, s[34:35] offset:-4096 sc1\n"
"v_mfma_f32_16x16x32_bf16 v[228:231], a[92:95], a[100:103], v[228:231]\n"
"ds_read_b128 a[100:103], v3 offset:17408\n"
"v_mfma_f32_16x16x32_bf16 v[228:231], a[88:91], a[96:99], v[228:231]\n"
"ds_read_b128 a[96:99], v3 offset:16384\n"
"v_mfma_f32_16x16x32_bf16 v[232:235], a[88:91], a[104:107], v[232:235]\n"
"v_mfma_f32_16x16x32_bf16 v[232:235], a[92:95], a[108:111], v[232:235]\n"
"v_mfma_f32_16x16x32_bf16 v[204:207], a[84:87], a[108:111], v[204:207]\n"
"ds_read_b128 a[108:111], v3 offset:19456\n"
"v_mfma_f32_16x16x32_bf16 v[204:207], a[80:83], a[104:107], v[204:207]\n"
"ds_read_b128 a[104:107], v3 offset:18432\n"
"v_mfma_f32_16x16x32_bf16 v[208:211], a[80:83], a[112:115], v[208:211]\n"
"v_mfma_f32_16x16x32_bf16 v[208:211], a[84:87], a[116:119], v[208:211]\n"
"global_load_dwordx4 a[68:71], v0, s[34:35] offset:-3072 sc1\n"
"v_mfma_f32_16x16x32_bf16 v[236:239], a[92:95], a[116:119], v[236:239]\n"
"ds_read_b128 a[116:119], v3 offset:21504\n"
"v_mfma_f32_16x16x32_bf16 v[236:239], a[88:91], a[112:115], v[236:239]\n"
"ds_read_b128 a[112:115], v3 offset:20480\n"
"v_mfma_f32_16x16x32_bf16 v[240:243], a[88:91], a[120:123], v[240:243]\n"
"v_mfma_f32_16x16x32_bf16 v[240:243], a[92:95], a[124:127], v[240:243]\n"
"ds_read_b128 v[24:27], v2 offset:29696\n"
"v_mfma_f32_16x16x32_bf16 v[212:215], a[84:87], a[124:127], v[212:215]\n"
"ds_read_b128 a[124:127], v3 offset:23552\n"
"v_mfma_f32_16x16x32_bf16 v[212:215], a[80:83], a[120:123], v[212:215]\n"
"ds_read_b128 a[120:123], v3 offset:22528\n"
"v_mfma_f32_16x16x32_bf16 v[216:219], a[80:83], a[128:131], v[216:219]\n"
"v_mfma_f32_16x16x32_bf16 v[216:219], a[84:87], a[132:135], v[216:219]\n"
"global_load_dwordx4 a[72:75], v0, s[34:35] offset:-2048 sc1\n"
"v_mfma_f32_16x16x32_bf16 v[244:247], a[92:95], a[132:135], v[244:247]\n"
"ds_read_b128 a[132:135], v3 offset:25600\n"
"v_mfma_f32_16x16x32_bf16 v[244:247], a[88:91], a[128:131], v[244:247]\n"
"ds_read_b128 a[128:131], v3 offset:24576\n"
"v_mfma_f32_16x16x32_bf16 v[248:251], a[88:91], a[136:139], v[248:251]\n"
"v_subrev_u32 v8, s65, v8\n"
"v_mfma_f32_16x16x32_bf16 v[248:251], a[92:95], a[140:143], v[248:251]\n"
"v_xor_b32 v9, s79, v9\n"
"v_mfma_f32_16x16x32_bf16 v[220:223], a[84:87], a[140:143], v[220:223]\n"
"ds_read_b128 a[140:143], v3 offset:27648\n"
"v_or_b32 v8, v8, v9\n"
"v_mfma_f32_16x16x32_bf16 v[220:223], a[80:83], a[136:139], v[220:223]\n"
"ds_read_b128 a[136:139], v3 offset:26624\n"
"v_cmp_lt_u32 vcc, 15, v8\n"
"v_mfma_f32_16x16x32_bf16 v[224:227], a[80:83], a[144:147], v[224:227]\n"
"s_waitcnt lgkmcnt(6)\n"
"global_store_dwordx4 v0, v[24:27], s[54:55] offset:1024 sc1\n"
"s_cbranch_vccnz L_slow7\n"
"L_back7:\n"
"v_mfma_f32_16x16x32_bf16 v[224:227], a[84:87], a[148:151], v[224:227]\n"
"global_load_dwordx4 a[76:79], v0, s[34:35] offset:-1024 sc1\n"
"v_readfirstlane_b32 s66, v9\n"
"v_mfma_f32_16x16x32_bf16 v[252:255], a[92:95], a[148:151], v[252:255]\n"
"ds_read_b128 a[148:151], v3 offset:29696\n"
"v_mfma_f32_16x16x32_bf16 v[252:255], a[88:91], a[144:147], v[252:255]\n"
"s_and_b32 s66, s66, 3\n"
"ds_read_b128 a[144:147], v3 offset:28672\n"
"v_mfma_f32_16x16x32_bf16 a[28:31], a[88:91], a[152:155], a[28:31]\n"
"s_lshl_b32 s69, s66, 13\n"
"v_mfma_f32_16x16x32_bf16 a[28:31], a[92:95], a[156:159], a[28:31]\n"
"s_cmp_eq_u32 s66, 3\n"
"s_cselect_b32 s75, 8192, 0\n"
"v_mfma_f32_16x16x32_bf16 a[24:27], a[84:87], a[156:159], a[24:27]\n"
"s_and_b32 s29, s66, s30\n"
"ds_read_b128 a[156:159], v3 offset:31744\n"
"v_mfma_f32_16x16x32_bf16 a[24:27], a[80:83], a[152:155], a[24:27]\n"
"s_waitcnt vmcnt(11)\n"
"ds_read_b128 a[152:155], v3 offset:30720\n"
"v_mfma_f32_16x16x32_bf16 v[32:35], a[32:35], a[96:99], v[32:35]\n"
"s_xor_b32 s31, s79, s66\n"
"v_mfma_f32_16x16x32_bf16 v[32:35], a[36:39], a[100:103], v[32:35]\n"
"global_load_dwordx4 a[80:83], v0, s[34:35] offset:0 sc1\n"
"v_mfma_f32_16x16x32_bf16 v[60:63], a[44:47], a[100:103], v[60:63]\n"
"v_mfma_f32_16x16x32_bf16 v[60:63], a[40:43], a[96:99], v[60:63]\n"
"v_mfma_f32_16x16x32_bf16 v[64:67], a[40:43], a[104:107], v[64:67]\n"
"v_mfma_f32_16x16x32_bf16 v[64:67], a[44:47], a[108:111], v[64:67]\n"
"v_mfma_f32_16x16x32_bf16 v[36:39], a[36:39], a[108:111], v[36:39]\n"
"s_add_u32 m0, s64, 0\n"
"v_mfma_f32_16x16x32_bf16 v[36:39], a[32:35], a[104:107], v[36:39]\n"
"s_waitcnt lgkmcnt(8)\n"
"v_mfma_f32_16x16x32_bf16 v[40:43], a[32:35], a[112:115], v[40:43]\n"
"v_mfma_f32_16x16x32_bf16 v[40:43], a[36:39], a[116:119], v[40:43]\n"
"global_load_dwordx4 a[84:87], v0, s[34:35] offset:1024 sc1\n"
"v_mfma_f32_16x16x32_bf16 v[68:71], a[44:47], a[116:119], v[68:71]\n"
"v_mfma_f32_16x16x32_bf16 v[68:71], a[40:43], a[112:115], v[68:71]\n"
"v_mfma_f32_16x16x32_bf16 v[72:75], a[40:43], a[120:123], v[72:75]\n"
"v_mfma_f32_16x16x32_bf16 v[72:75], a[44:47], a[124:127], v[72:75]\n"
"v_and_b32 v10, 0x300, v0\n"
"v_mfma_f32_16x16x32_bf16 v[44:47], a[36:39], a[124:127], v[44:47]\n"
"v_mfma_f32_16x16x32_bf16 v[44:47], a[32:35], a[120:123], v[44:47]\n"
"s_waitcnt lgkmcnt(4)\n"
"v_mfma_f32_16x16x32_bf16 v[48:51], a[32:35], a[128:131], v[48:51]\n"
"v_and_b32 v11, 0x3c0, v0\n"
"v_mfma_f32_16x16x32_bf16 v[48:51], a[36:39], a[132:135], v[48:51]\n"
"global_load_dwordx4 a[88:91], v0, s[34:35] offset:2048 sc1\n"
"v_mfma_f32_16x16x32_bf16 v[76:79], a[44:47], a[132:135], v[76:79]\n"
"v_mfma_f32_16x16x32_bf16 v[76:79], a[40:43], a[128:131], v[76:79]\n"
"v_mfma_f32_16x16x32_bf16 v[80:83], a[40:43], a[136:139], v[80:83]\n"
"v_add_lshl_u32 v11, v10, v11, 1\n"
"v_mfma_f32_16x16x32_bf16 v[80:83], a[44:47], a[140:143], v[80:83]\n"
"buffer_load_dwordx4 v0, s[48:51], s75 offen offset:0 sc1 lds\n"
"v_mfma_f32_16x16x32_bf16 v[52:55], a[36:39], a[140:143], v[52:55]\n"
"v_mfma_f32_16x16x32_bf16 v[52:55], a[32:35], a[136:139], v[52:55]\n"
"s_waitcnt lgkmcnt(0)\n"
"v_mfma_f32_16x16x32_bf16 v[56:59], a[32:35], a[144:147], v[56:59]\n"
"v_lshrrev_b32 v10, 2, v0\n"
"v_mfma_f32_16x16x32_bf16 v[56:59], a[36:39], a[148:151], v[56:59]\n"
"L_st8533:\n"
"s_cbranch_execz L_retB\n"
"global_load_dwordx4 a[92:95], v0, s[34:35] offset:3072 sc1\n"
"v_mfma_f32_16x16x32_bf16 v[84:87], a[44:47], a[148:151], v[84:87]\n"
"v_mfma_f32_16x16x32_bf16 v[84:87], a[40:43], a[144:147], v[84:87]\n"
"v_xor_b32 v10, v10, v0\n"
"v_mfma_f32_16x16x32_bf16 a[4:7], a[40:43], a[152:155], a[4:7]\n"
"s_add_u32 s34, s34, 8192\n"
"v_mfma_f32_16x16x32_bf16 a[4:7], a[44:47], a[156:159], a[4:7]\n"
"v_mfma_f32_16x16x32_bf16 a[0:3], a[36:39], a[156:159], a[0:3]\n"
"v_mfma_f32_16x16x32_bf16 a[0:3], a[32:35], a[152:155], a[0:3]\n"
"s_waitcnt vmcnt(11)\n"
"v_and_b32 v10, 0x30, v10\n"
"v_mfma_f32_16x16x32_bf16 v[88:91], a[48:51], a[96:99], v[88:91]\n"
"v_mfma_f32_16x16x32_bf16 v[88:91], a[52:55], a[100:103], v[88:91]\n"
"global_load_dwordx4 a[32:35], v0, s[34:35] offset:-4096 sc1\n"
"v_mfma_f32_16x16x32_bf16 v[116:119], a[60:63], a[100:103], v[116:119]\n"
"v_add3_u32 v11, v10, v11, s64\n"
"v_mfma_f32_16x16x32_bf16 v[116:119], a[56:59], a[96:99], v[116:119]\n"
"buffer_load_dwordx4 v0, s[48:51], s75 offen offset:1024 sc1 lds\n"
"v_mfma_f32_16x16x32_bf16 v[120:123], a[56:59], a[104:107], v[120:123]\n"
"v_mfma_f32_16x16x32_bf16 v[120:123], a[60:63], a[108:111], v[120:123]\n"
"v_mfma_f32_16x16x32_bf16 v[92:95], a[52:55], a[108:111], v[92:95]\n"
"s_add_u32 s27, s27, 0x200000\n"
"v_mfma_f32_16x16x32_bf16 v[92:95], a[48:51], a[104:107], v[92:95]\n"
"v_mfma_f32_16x16x32_bf16 v[96:99], a[48:51], a[112:115], v[96:99]\n"
"v_mfma_f32_16x16x32_bf16 v[96:99], a[52:55], a[116:119], v[96:99]\n"
"global_load_dwordx4 a[36:39], v0, s[34:35] offset:-3072 sc1\n"
"v_mfma_f32_16x16x32_bf16 v[124:127], a[60:63], a[116:119], v[124:127]\n"
"ds_read_b64_tr_b16 v[24:25], v11 offset:12288\n"
"v_mfma_f32_16x16x32_bf16 v[124:127], a[56:59], a[112:115], v[124:127]\n"
"v_mfma_f32_16x16x32_bf16 v[128:131], a[56:59], a[120:123], v[128:131]\n"
"ds_read_b64_tr_b16 v[26:27], v11 offset:12864\n"
"v_mfma_f32_16x16x32_bf16 v[128:131], a[60:63], a[124:127], v[128:131]\n"
"v_mfma_f32_16x16x32_bf16 v[100:103], a[52:55], a[124:127], v[100:103]\n"
"ds_read_b64_tr_b16 v[28:29], v11 offset:12296\n"
"v_mfma_f32_16x16x32_bf16 v[100:103], a[48:51], a[120:123], v[100:103]\n"
"v_mfma_f32_16x16x32_bf16 v[104:107], a[48:51], a[128:131], v[104:107]\n"
"ds_read_b64_tr_b16 v[30:31], v11 offset:12872\n"
"v_mfma_f32_16x16x32_bf16 v[104:107], a[52:55], a[132:135], v[104:107]\n"
"global_load_dwordx4 a[40:43], v0, s[34:35] offset:-2048 sc1\n"
"v_mfma_f32_16x16x32_bf16 v[132:135], a[60:63], a[132:135], v[132:135]\n"
"buffer_load_dwordx4 v0, s[48:51], s75 offen offset:2048 sc1 lds\n"
"v_mfma_f32_16x16x32_bf16 v[132:135], a[56:59], a[128:131], v[132:135]\n"
"v_mfma_f32_16x16x32_bf16 v[136:139], a[56:59], a[136:139], v[136:139]\n"
"v_mfma_f32_16x16x32_bf16 v[136:139], a[60:63], a[140:143], v[136:139]\n"
"v_mfma_f32_16x16x32_bf16 v[108:111], a[52:55], a[140:143], v[108:111]\n"
"v_mfma_f32_16x16x32_bf16 v[108:111], a[48:51], a[136:139], v[108:111]\n"
"v_mfma_f32_16x16x32_bf16 v[112:115], a[48:51], a[144:147], v[112:115]\n"
"s_waitcnt lgkmcnt(0)\n"
"v_mfma_f32_16x16x32_bf16 v[112:115], a[52:55], a[148:151], v[112:115]\n"
"global_load_dwordx4 a[44:47], v0, s[34:35] offset:-1024 sc1\n"
"v_mfma_f32_16x16x32_bf16 v[140:143], a[60:63], a[148:151], v[140:143]\n"
"v_mfma_f32_16x16x32_bf16 v[140:143], a[56:59], a[144:147], v[140:143]\n"
"v_mfma_f32_16x16x32_bf16 a[12:15], a[56:59], a[152:155], a[12:15]\n"
"v_mfma_f32_16x16x32_bf16 a[12:15], a[60:63], a[156:159], a[12:15]\n"
"v_mfma_f32_16x16x32_bf16 a[8:11], a[52:55], a[156:159], a[8:11]\n"
"v_mfma_f32_16x16x32_bf16 a[8:11], a[48:51], a[152:155], a[8:11]\n"
"s_waitcnt vmcnt(11)\n"
"v_mfma_f32_16x16x32_bf16 v[144:147], a[64:67], a[96:99], v[144:147]\n"
"buffer_store_dwordx4 v[24:27], v0, s[36:39], s28 offen offset:0 sc1\n"
"buffer_load_dwordx4 v0, s[48:51], s75 offen offset:3072 sc1 lds\n"
"v_mfma_f32_16x16x32_bf16 v[144:147], a[68:71], a[100:103], v[144:147]\n"
"global_load_dwordx4 a[48:51], v0, s[34:35] offset:0 sc1\n"
"v_mfma_f32_16x16x32_bf16 v[172:175], a[76:79], a[100:103], v[172:175]\n"
"v_mfma_f32_16x16x32_bf16 v[172:175], a[72:75], a[96:99], v[172:175]\n"
"v_mfma_f32_16x16x32_bf16 v[176:179], a[72:75], a[104:107], v[176:179]\n"
"v_mfma_f32_16x16x32_bf16 v[176:179], a[76:79], a[108:111], v[176:179]\n"
"v_mfma_f32_16x16x32_bf16 v[148:151], a[68:71], a[108:111], v[148:151]\n"
"v_mfma_f32_16x16x32_bf16 v[148:151], a[64:67], a[104:107], v[148:151]\n"
"v_mfma_f32_16x16x32_bf16 v[152:155], a[64:67], a[112:115], v[152:155]\n"
"v_mfma_f32_16x16x32_bf16 v[152:155], a[68:71], a[116:119], v[152:155]\n"
"global_load_dwordx4 a[52:55], v0, s[34:35] offset:1024 sc1\n"
"v_mfma_f32_16x16x32_bf16 v[180:183], a[76:79], a[116:119], v[180:183]\n"
"v_mfma_f32_16x16x32_bf16 v[180:183], a[72:75], a[112:115], v[180:183]\n"
"v_mfma_f32_16x16x32_bf16 v[184:187], a[72:75], a[120:123], v[184:187]\n"
"v_mfma_f32_16x16x32_bf16 v[184:187], a[76:79], a[124:127], v[184:187]\n"
"v_mfma_f32_16x16x32_bf16 v[156:159], a[68:71], a[124:127], v[156:159]\n"
"v_mfma_f32_16x16x32_bf16 v[156:159], a[64:67], a[120:123], v[156:159]\n"
"s_add_u32 m0, s64, 4096\n"
"v_mfma_f32_16x16x32_bf16 v[160:163], a[64:67], a[128:131], v[160:163]\n"
"v_mfma_f32_16x16x32_bf16 v[160:163], a[68:71], a[132:135], v[160:163]\n"
"global_load_dwordx4 a[56:59], v0, s[34:35] offset:2048 sc1\n"
"v_mfma_f32_16x16x32_bf16 v[188:191], a[76:79], a[132:135], v[188:191]\n"
"v_mfma_f32_16x16x32_bf16 v[188:191], a[72:75], a[128:131], v[188:191]\n"
"v_mfma_f32_16x16x32_bf16 v[192:195], a[72:75], a[136:139], v[192:195]\n"
"v_mfma_f32_16x16x32_bf16 v[192:195], a[76:79], a[140:143], v[192:195]\n"
"v_mfma_f32_16x16x32_bf16 v[164:167], a[68:71], a[140:143], v[164:167]\n"
"v_mfma_f32_16x16x32_bf16 v[164:167], a[64:67], a[136:139], v[164:167]\n"
"v_mfma_f32_16x16x32_bf16 v[168:171], a[64:67], a[144:147], v[168:171]\n"
"v_mfma_f32_16x16x32_bf16 v[168:171], a[68:71], a[148:151], v[168:171]\n"
"global_load_dwordx4 a[60:63], v0, s[34:35] offset:3072 sc1\n"
"v_mfma_f32_16x16x32_bf16 v[196:199], a[76:79], a[148:151], v[196:199]\n"
"v_mfma_f32_16x16x32_bf16 v[196:199], a[72:75], a[144:147], v[196:199]\n"
"v_mfma_f32_16x16x32_bf16 a[20:23], a[72:75], a[152:155], a[20:23]\n"
"s_add_u32 s34, s34, 8192\n"
"v_mfma_f32_16x16x32_bf16 a[20:23], a[76:79], a[156:159], a[20:23]\n"
"v_mfma_f32_16x16x32_bf16 a[16:19], a[68:71], a[156:159], a[16:19]\n"
"buffer_load_dwordx4 v1, s[48:51], s75 offen offset:0 sc1 lds\n"
"v_mfma_f32_16x16x32_bf16 a[16:19], a[64:67], a[152:155], a[16:19]\n"
"s_waitcnt vmcnt(13)\n"
"v_mfma_f32_16x16x32_bf16 v[200:203], a[80:83], a[96:99], v[200:203]\n"
"v_mfma_f32_16x16x32_bf16 v[200:203], a[84:87], a[100:103], v[200:203]\n"
"global_load_dwordx4 a[64:67], v0, s[34:35] offset:-4096 sc1\n"
"s_add_u32 s65, s65, 1\n"
"v_mfma_f32_16x16x32_bf16 v[228:231], a[92:95], a[100:103], v[228:231]\n"
"ds_read_b128 a[100:103], v4 offset:17408\n"
"v_mfma_f32_16x16x32_bf16 v[228:231], a[88:91], a[96:99], v[228:231]\n"
"ds_read_b128 a[96:99], v4 offset:16384\n"
"v_mov_b32 v8, s65\n"
"v_mfma_f32_16x16x32_bf16 v[232:235], a[88:91], a[104:107], v[232:235]\n"
"v_mov_b32 v9, s60\n"
"v_mfma_f32_16x16x32_bf16 v[232:235], a[92:95], a[108:111], v[232:235]\n"
"v_mfma_f32_16x16x32_bf16 v[204:207], a[84:87], a[108:111], v[204:207]\n"
"buffer_store_dwordx2 v[8:9], off, s[56:59], %13 sc1\n"
"ds_read_b128 a[108:111], v4 offset:19456\n"
"v_mfma_f32_16x16x32_bf16 v[204:207], a[80:83], a[104:107], v[204:207]\n"
"ds_read_b128 a[104:107], v4 offset:18432\n"
"v_mfma_f32_16x16x32_bf16 v[208:211], a[80:83], a[112:115], v[208:211]\n"
"buffer_store_dwordx4 v[28:31], v0, s[36:39], s28 offen offset:2048 sc1\n"
"buffer_load_dwordx4 v1, s[48:51], s75 offen offset:1024 sc1 lds\n"
"v_mfma_f32_16x16x32_bf16 v[208:211], a[84:87], a[116:119], v[208:211]\n"
"global_load_dwordx4 a[68:71], v0, s[34:35] offset:-3072 sc1\n"
"v_mfma_f32_16x16x32_bf16 v[236:239], a[92:95], a[116:119], v[236:239]\n"
"ds_read_b128 a[116:119], v4 offset:21504\n"
"v_mfma_f32_16x16x32_bf16 v[236:239], a[88:91], a[112:115], v[236:239]\n"
"ds_read_b128 a[112:115], v4 offset:20480\n"
"v_mfma_f32_16x16x32_bf16 v[240:243], a[88:91], a[120:123], v[240:243]\n"
"v_mfma_f32_16x16x32_bf16 v[240:243], a[92:95], a[124:127], v[240:243]\n"
"v_mfma_f32_16x16x32_bf16 v[212:215], a[84:87], a[124:127], v[212:215]\n"
"ds_read_b128 a[124:127], v4 offset:23552\n"
"v_mfma_f32_16x16x32_bf16 v[212:215], a[80:83], a[120:123], v[212:215]\n"
"ds_read_b128 a[120:123], v4 offset:22528\n"
"v_mfma_f32_16x16x32_bf16 v[216:219], a[80:83], a[128:131], v[216:219]\n"
"ds_read_b64_tr_b16 v[24:25], v11 offset:12352\n"
"v_mfma_f32_16x16x32_bf16 v[216:219], a[84:87], a[132:135], v[216:219]\n"
"global_load_dwordx4 a[72:75], v0, s[34:35] offset:-2048 sc1\n"
"ds_read_b64_tr_b16 v[26:27], v11 offset:12800\n"
"v_mfma_f32_16x16x32_bf16 v[244:247], a[92:95], a[132:135], v[244:247]\n"
"ds_read_b128 a[132:135], v4 offset:25600\n"
"v_mfma_f32_16x16x32_bf16 v[244:247], a[88:91], a[128:131], v[244:247]\n"
"ds_read_b128 a[128:131], v4 offset:24576\n"
"v_mfma_f32_16x16x32_bf16 v[248:251], a[88:91], a[136:139], v[248:251]\n"
"ds_read_b64_tr_b16 v[28:29], v11 offset:12360\n"
"v_mfma_f32_16x16x32_bf16 v[248:251], a[92:95], a[140:143], v[248:251]\n"
"ds_read_b64_tr_b16 v[30:31], v11 offset:12808\n"
"v_mfma_f32_16x16x32_bf16 v[220:223], a[84:87], a[140:143], v[220:223]\n"
"ds_read_b128 a[140:143], v4 offset:27648\n"
"v_mfma_f32_16x16x32_bf16 v[220:223], a[80:83], a[136:139], v[220:223]\n"
"ds_read_b128 a[136:139], v4 offset:26624\n"
"v_mfma_f32_16x16x32_bf16 v[224:227], a[80:83], a[144:147], v[224:227]\n"
"v_mfma_f32_16x16x32_bf16 v[224:227], a[84:87], a[148:151], v[224:227]\n"
"global_load_dwordx4 a[76:79], v0, s[34:35] offset:-1024 sc1\n"
"v_mfma_f32_16x16x32_bf16 v[252:255], a[92:95], a[148:151], v[252:255]\n"
"ds_read_b128 a[148:151], v4 offset:29696\n"
"v_mfma_f32_16x16x32_bf16 v[252:255], a[88:91], a[144:147], v[252:255]\n"
"ds_read_b128 a[144:147], v4 offset:28672\n"
"v_mfma_f32_16x16x32_bf16 a[28:31], a[88:91], a[152:155], a[28:31]\n"
"buffer_load_dwordx4 v1, s[48:51], s75 offen offset:2048 sc1 lds\n"
"v_mfma_f32_16x16x32_bf16 a[28:31], a[92:95], a[156:159], a[28:31]\n"
"v_mfma_f32_16x16x32_bf16 a[24:27], a[84:87], a[156:159], a[24:27]\n"
"s_waitcnt lgkmcnt(4)\n"
"ds_read_b128 a[156:159], v4 offset:31744\n"
"v_mfma_f32_16x16x32_bf16 a[24:27], a[80:83], a[152:155], a[24:27]\n"
"s_waitcnt vmcnt(15)\n"
"ds_read_b128 a[152:155], v4 offset:30720\n"
"v_mfma_f32_16x16x32_bf16 v[32:35], a[32:35], a[96:99], v[32:35]\n"
"v_mfma_f32_16x16x32_bf16 v[32:35], a[36:39], a[100:103], v[32:35]\n"
"global_load_dwordx4 a[80:83], v0, s[34:35] offset:0 sc1\n"
"v_mfma_f32_16x16x32_bf16 v[60:63], a[44:47], a[100:103], v[60:63]\n"
"v_mfma_f32_16x16x32_bf16 v[60:63], a[40:43], a[96:99], v[60:63]\n"
"v_mfma_f32_16x16x32_bf16 v[64:67], a[40:43], a[104:107], v[64:67]\n"
"v_mfma_f32_16x16x32_bf16 v[64:67], a[44:47], a[108:111], v[64:67]\n"
"v_mfma_f32_16x16x32_bf16 v[36:39], a[36:39], a[108:111], v[36:39]\n"
"v_mfma_f32_16x16x32_bf16 v[36:39], a[32:35], a[104:107], v[36:39]\n"
"v_mfma_f32_16x16x32_bf16 v[40:43], a[32:35], a[112:115], v[40:43]\n"
"v_mfma_f32_16x16x32_bf16 v[40:43], a[36:39], a[116:119], v[40:43]\n"
"global_load_dwordx4 a[84:87], v0, s[34:35] offset:1024 sc1\n"
"v_mfma_f32_16x16x32_bf16 v[68:71], a[44:47], a[116:119], v[68:71]\n"
"buffer_load_dwordx4 v1, s[48:51], s75 offen offset:3072 sc1 lds\n"
"v_mfma_f32_16x16x32_bf16 v[68:71], a[40:43], a[112:115], v[68:71]\n"
"v_mfma_f32_16x16x32_bf16 v[72:75], a[40:43], a[120:123], v[72:75]\n"
"v_mfma_f32_16x16x32_bf16 v[72:75], a[44:47], a[124:127], v[72:75]\n"
"v_mfma_f32_16x16x32_bf16 v[44:47], a[36:39], a[124:127], v[44:47]\n"
"v_mfma_f32_16x16x32_bf16 v[44:47], a[32:35], a[120:123], v[44:47]\n"
"s_waitcnt lgkmcnt(4)\n"
"v_mfma_f32_16x16x32_bf16 v[48:51], a[32:35], a[128:131], v[48:51]\n"
"v_mfma_f32_16x16x32_bf16 v[48:51], a[36:39], a[132:135], v[48:51]\n"
"global_load_dwordx4 a[88:91], v0, s[34:35] offset:2048 sc1\n"
"v_mfma_f32_16x16x32_bf16 v[76:79], a[44:47], a[132:135], v[76:79]\n"
"v_mfma_f32_16x16x32_bf16 v[76:79], a[40:43], a[128:131], v[76:79]\n"
"v_mfma_f32_16x16x32_bf16 v[80:83], a[40:43], a[136:139], v[80:83]\n"
"v_mfma_f32_16x16x32_bf16 v[80:83], a[44:47], a[140:143], v[80:83]\n"
"v_mfma_f32_16x16x32_bf16 v[52:55], a[36:39], a[140:143], v[52:55]\n"
"v_mfma_f32_16x16x32_bf16 v[52:55], a[32:35], a[136:139], v[52:55]\n"
"s_waitcnt lgkmcnt(0)\n"
"v_mfma_f32_16x16x32_bf16 v[56:59], a[32:35], a[144:147], v[56:59]\n"
"v_mfma_f32_16x16x32_bf16 v[56:59], a[36:39], a[148:151], v[56:59]\n"
"s_add_u32 m0, s64, 8192\n"
"global_load_dwordx4 a[92:95], v0, s[34:35] offset:3072 sc1\n"
"v_mfma_f32_16x16x32_bf16 v[84:87], a[44:47], a[148:151], v[84:87]\n"
"v_mfma_f32_16x16x32_bf16 v[84:87], a[40:43], a[144:147], v[84:87]\n"
"v_mfma_f32_16x16x32_bf16 a[4:7], a[40:43], a[152:155], a[4:7]\n"
"s_add_u32 s34, s34, 8192\n"
"v_mfma_f32_16x16x32_bf16 a[4:7], a[44:47], a[156:159], a[4:7]\n"
"v_mfma_f32_16x16x32_bf16 a[0:3], a[36:39], a[156:159], a[0:3]\n"
"v_mfma_f32_16x16x32_bf16 a[0:3], a[32:35], a[152:155], a[0:3]\n"
"s_waitcnt vmcnt(14)\n"
"v_mfma_f32_16x16x32_bf16 v[88:91], a[48:51], a[96:99], v[88:91]\n"
"v_mfma_f32_16x16x32_bf16 v[88:91], a[52:55], a[100:103], v[88:91]\n"
"global_load_dwordx4 a[32:35], v0, s[34:35] offset:-4096 sc1\n"
"v_mfma_f32_16x16x32_bf16 v[116:119], a[60:63], a[100:103], v[116:119]\n"
"v_mfma_f32_16x16x32_bf16 v[116:119], a[56:59], a[96:99], v[116:119]\n"
"v_mfma_f32_16x16x32_bf16 v[120:123], a[56:59], a[104:107], v[120:123]\n"
"v_mfma_f32_16x16x32_bf16 v[120:123], a[60:63], a[108:111], v[120:123]\n"
"v_mfma_f32_16x16x32_bf16 v[92:95], a[52:55], a[108:111], v[92:95]\n"
"v_mfma_f32_16x16x32_bf16 v[92:95], a[48:51], a[104:107], v[92:95]\n"
"v_mfma_f32_16x16x32_bf16 v[96:99], a[48:51], a[112:115], v[96:99]\n"
"buffer_load_dwordx4 v0, s[48:51], s69 offen offset:0 sc1 lds\n"
"v_mfma_f32_16x16x32_bf16 v[96:99], a[52:55], a[116:119], v[96:99]\n"
"global_load_dwordx4 a[36:39], v0, s[34:35] offset:-3072 sc1\n"
"v_mfma_f32_16x16x32_bf16 v[124:127], a[60:63], a[116:119], v[124:127]\n"
"v_mfma_f32_16x16x32_bf16 v[124:127], a[56:59], a[112:115], v[124:127]\n"
"v_mfma_f32_16x16x32_bf16 v[128:131], a[56:59], a[120:123], v[128:131]\n"
"v_mfma_f32_16x16x32_bf16 v[128:131], a[60:63], a[124:127], v[128:131]\n"
"v_mfma_f32_16x16x32_bf16 v[100:103], a[52:55], a[124:127], v[100:103]\n"
"v_mfma_f32_16x16x32_bf16 v[100:103], a[48:51], a[120:123], v[100:103]\n"
"v_mfma_f32_16x16x32_bf16 v[104:107], a[48:51], a[128:131], v[104:107]\n"
"v_mfma_f32_16x16x32_bf16 v[104:107], a[52:55], a[132:135], v[104:107]\n"
"global_load_dwordx4 a[40:43], v0, s[34:35] offset:-2048 sc1\n"
"v_mfma_f32_16x16x32_bf16 v[132:135], a[60:63], a[132:135], v[132:135]\n"
"v_mfma_f32_16x16x32_bf16 v[132:135], a[56:59], a[128:131], v[132:135]\n"
"v_mfma_f32_16x16x32_bf16 v[136:139], a[56:59], a[136:139], v[136:139]\n"
"v_mfma_f32_16x16x32_bf16 v[136:139], a[60:63], a[140:143], v[136:139]\n"
"v_mfma_f32_16x16x32_bf16 v[108:111], a[52:55], a[140:143], v[108:111]\n"
"buffer_load_dwordx4 v0, s[48:51], s69 offen offset:1024 sc1 lds\n"
"v_mfma_f32_16x16x32_bf16 v[108:111], a[48:51], a[136:139], v[108:111]\n"
"v_mfma_f32_16x16x32_bf16 v[112:115], a[48:51], a[144:147], v[112:115]\n"
"v_mfma_f32_16x16x32_bf16 v[112:115], a[52:55], a[148:151], v[112:115]\n"
"global_load_dwordx4 a[44:47], v0, s[34:35] offset:-1024 sc1\n"
"v_mfma_f32_16x16x32_bf16 v[140:143], a[60:63], a[148:151], v[140:143]\n"
"v_mfma_f32_16x16x32_bf16 v[140:143], a[56:59], a[144:147], v[140:143]\n"
"v_mfma_f32_16x16x32_bf16 a[12:15], a[56:59], a[152:155], a[12:15]\n"
"v_mfma_f32_16x16x32_bf16 a[12:15], a[60:63], a[156:159], a[12:15]\n"
"v_mfma_f32_16x16x32_bf16 a[8:11], a[52:55], a[156:159], a[8:11]\n"
"v_mfma_f32_16x16x32_bf16 a[8:11], a[48:51], a[152:155], a[8:11]\n"
"s_waitcnt vmcnt(12)\n"
"v_mfma_f32_16x16x32_bf16 v[144:147], a[64:67], a[96:99], v[144:147]\n"
"v_mfma_f32_16x16x32_bf16 v[144:147], a[68:71], a[100:103], v[144:147]\n"
"global_load_dwordx4 a[48:51], v0, s[34:35] offset:0 sc1\n"
"v_mfma_f32_16x16x32_bf16 v[172:175], a[76:79], a[100:103], v[172:175]\n"
"v_mfma_f32_16x16x32_bf16 v[172:175], a[72:75], a[96:99], v[172:175]\n"
"v_mfma_f32_16x16x32_bf16 v[176:179], a[72:75], a[104:107], v[176:179]\n"
"v_mfma_f32_16x16x32_bf16 v[176:179], a[76:79], a[108:111], v[176:179]\n"
"buffer_load_dwordx4 v0, s[48:51], s69 offen offset:2048 sc1 lds\n"
"v_mfma_f32_16x16x32_bf16 v[148:151], a[68:71], a[108:111], v[148:151]\n"
"v_mfma_f32_16x16x32_bf16 v[148:151], a[64:67], a[104:107], v[148:151]\n"
"v_mfma_f32_16x16x32_bf16 v[152:155], a[64:67], a[112:115], v[152:155]\n"
"v_lshlrev_b32 v16, 16, v18\n"
"v_mfma_f32_16x16x32_bf16 v[152:155], a[68:71], a[116:119], v[152:155]\n"
"global_load_dwordx4 a[52:55], v0, s[34:35] offset:1024 sc1\n"
"v_and_b32 v17, s63, v18\n"
"v_mfma_f32_16x16x32_bf16 v[180:183], a[76:79], a[116:119], v[180:183]\n"
"v_lshlrev_b32 v18, 16, v19\n"
"v_mfma_f32_16x16x32_bf16 v[180:183], a[72:75], a[112:115], v[180:183]\n"
"v_and_b32 v19, s63, v19\n"
"v_mfma_f32_16x16x32_bf16 v[184:187], a[72:75], a[120:123], v[184:187]\n"
"v_add_f32 v32, v32, v16\n"
"v_mfma_f32_16x16x32_bf16 v[184:187], a[76:79], a[124:127], v[184:187]\n"
"v_add_f32 v33, v33, v17\n"
"v_add_f32 v34, v34, v18\n"
"v_mfma_f32_16x16x32_bf16 v[156:159], a[68:71], a[124:127], v[156:159]\n"
"v_add_f32 v35, v35, v19\n"
"v_mfma_f32_16x16x32_bf16 v[156:159], a[64:67], a[120:123], v[156:159]\n"
"v_add_f32 v36, v36, v16\n"
"v_mfma_f32_16x16x32_bf16 v[160:163], a[64:67], a[128:131], v[160:163]\n"
"v_add_f32 v37, v37, v17\n"
"v_mfma_f32_16x16x32_bf16 v[160:163], a[68:71], a[132:135], v[160:163]\n"
"global_load_dwordx4 a[56:59], v0, s[34:35] offset:2048 sc1\n"
"v_add_f32 v38, v38, v18\n"
"v_mfma_f32_16x16x32_bf16 v[188:191], a[76:79], a[132:135], v[188:191]\n"
"v_add_f32 v39, v39, v19\n"
"v_mfma_f32_16x16x32_bf16 v[188:191], a[72:75], a[128:131], v[188:191]\n"
"v_add_f32 v40, v40, v16\n"
"v_add_f32 v41, v41, v17\n"
"v_mfma_f32_16x16x32_bf16 v[192:195], a[72:75], a[136:139], v[192:195]\n"
"buffer_load_dwordx4 v0, s[48:51], s69 offen offset:3072 sc1 lds\n"
"v_mfma_f32_16x16x32_bf16 v[192:195], a[76:79], a[140:143], v[192:195]\n"
"v_add_f32 v42, v42, v18\n"
"v_mfma_f32_16x16x32_bf16 v[164:167], a[68:71], a[140:143], v[164:167]\n"
"v_add_f32 v43, v43, v19\n"
"v_mfma_f32_16x16x32_bf16 v[164:167], a[64:67], a[136:139], v[164:167]\n"
"v_add_f32 v44, v44, v16\n"
"v_add_f32 v45, v45, v17\n"
"v_mfma_f32_16x16x32_bf16 v[168:171], a[64:67], a[144:147], v[168:171]\n"
"v_add_f32 v46, v46, v18\n"
"v_mfma_f32_16x16x32_bf16 v[168:171], a[68:71], a[148:151], v[168:171]\n"
"global_load_dwordx4 a[60:63], v0, s[34:35] offset:3072 sc1\n"
"v_add_f32 v47, v47, v19\n"
"v_mfma_f32_16x16x32_bf16 v[196:199], a[76:79], a[148:151], v[196:199]\n"
"v_add_f32 v48, v48, v16\n"
"v_mfma_f32_16x16x32_bf16 v[196:199], a[72:75], a[144:147], v[196:199]\n"
"v_add_f32 v49, v49, v17\n"
"v_mfma_f32_16x16x32_bf16 a[20:23], a[72:75], a[152:155], a[20:23]\n"
"s_add_u32 s34, s34, 8192\n"
"v_add_f32 v50, v50, v18\n"
"v_mfma_f32_16x16x32_bf16 a[20:23], a[76:79], a[156:159], a[20:23]\n"
"v_add_f32 v51, v51, v19\n"
"v_mfma_f32_16x16x32_bf16 a[16:19], a[68:71], a[156:159], a[16:19]\n"
"v_add_f32 v52, v52, v16\n"
"v_mfma_f32_16x16x32_bf16 a[16:19], a[64:67], a[152:155], a[16:19]\n"
"s_waitcnt vmcnt(12)\n"
"v_add_f32 v53, v53, v17\n"
"v_mfma_f32_16x16x32_bf16 v[200:203], a[80:83], a[96:99], v[200:203]\n"
"v_add_f32 v54, v54, v18\n"
"v_add_f32 v55, v55, v19\n"
"v_mfma_f32_16x16x32_bf16 v[200:203], a[84:87], a[100:103], v[200:203]\n"
"global_load_dwordx4 a[64:67], v0, s[34:35] offset:-4096 sc1\n"
"v_add_f32 v56, v56, v16\n"
"v_mfma_f32_16x16x32_bf16 v[228:231], a[92:95], a[100:103], v[228:231]\n"
"s_add_u32 m0, s64, 12288\n"
"ds_read_b128 a[100:103], v5 offset:17408\n"
"v_add_f32 v57, v57, v17\n"
"v_mfma_f32_16x16x32_bf16 v[228:231], a[88:91], a[96:99], v[228:231]\n"
"ds_read_b128 a[96:99], v5 offset:16384\n"
"v_add_f32 v58, v58, v18\n"
"v_mfma_f32_16x16x32_bf16 v[232:235], a[88:91], a[104:107], v[232:235]\n"
"v_add_f32 v59, v59, v19\n"
"v_mfma_f32_16x16x32_bf16 v[232:235], a[92:95], a[108:111], v[232:235]\n"
"v_accvgpr_read_b32 v12, a0\n"
"v_mfma_f32_16x16x32_bf16 v[204:207], a[84:87], a[108:111], v[204:207]\n"
"ds_read_b128 a[108:111], v5 offset:19456\n"
"v_accvgpr_read_b32 v13, a1\n"
"v_mfma_f32_16x16x32_bf16 v[204:207], a[80:83], a[104:107], v[204:207]\n"
"ds_read_b128 a[104:107], v5 offset:18432\n"
"v_accvgpr_read_b32 v14, a2\n"
"v_mfma_f32_16x16x32_bf16 v[208:211], a[80:83], a[112:115], v[208:211]\n"
"v_accvgpr_read_b32 v15, a3\n"
"v_mfma_f32_16x16x32_bf16 v[208:211], a[84:87], a[116:119], v[208:211]\n"
"global_load_dwordx4 a[68:71], v0, s[34:35] offset:-3072 sc1\n"
"v_add_f32 v12, v12, v16\n"
"v_mfma_f32_16x16x32_bf16 v[236:239], a[92:95], a[116:119], v[236:239]\n"
"ds_read_b128 a[116:119], v5 offset:21504\n"
"v_add_f32 v13, v13, v17\n"
"v_mfma_f32_16x16x32_bf16 v[236:239], a[88:91], a[112:115], v[236:239]\n"
"ds_read_b128 a[112:115], v5 offset:20480\n"
"v_add_f32 v14, v14, v18\n"
"v_mfma_f32_16x16x32_bf16 v[240:243], a[88:91], a[120:123], v[240:243]\n"
"v_add_f32 v15, v15, v19\n"
"v_accvgpr_write_b32 a0, v12\n"
"v_mfma_f32_16x16x32_bf16 v[240:243], a[92:95], a[124:127], v[240:243]\n"
"v_accvgpr_write_b32 a1, v13\n"
"v_mfma_f32_16x16x32_bf16 v[212:215], a[84:87], a[124:127], v[212:215]\n"
"ds_read_b128 a[124:127], v5 offset:23552\n"
"v_accvgpr_write_b32 a2, v14\n"
"v_mfma_f32_16x16x32_bf16 v[212:215], a[80:83], a[120:123], v[212:215]\n"
"ds_read_b128 a[120:123], v5 offset:22528\n"
"v_accvgpr_write_b32 a3, v15\n"
"v_mfma_f32_16x16x32_bf16 v[216:219], a[80:83], a[128:131], v[216:219]\n"
"buffer_load_dwordx4 v1, s[48:51], s69 offen offset:0 sc1 lds\n"
"v_mfma_f32_16x16x32_bf16 v[216:219], a[84:87], a[132:135], v[216:219]\n"
"global_load_dwordx4 a[72:75], v0, s[34:35] offset:-2048 sc1\n"
"v_lshrrev_b32 v16, 4, v0\n"
"v_mfma_f32_16x16x32_bf16 v[244:247], a[92:95], a[132:135], v[244:247]\n"
"ds_read_b128 a[132:135], v5 offset:25600\n"
"v_and_b32 v16, 0x30, v16\n"
"v_mfma_f32_16x16x32_bf16 v[244:247], a[88:91], a[128:131], v[244:247]\n"
"ds_read_b128 a[128:131], v5 offset:24576\n"
"buffer_load_dwordx2 v[18:19], v16, s[44:47], s61 offen offset:64\n"
"v_mfma_f32_16x16x32_bf16 v[248:251], a[88:91], a[136:139], v[248:251]\n"
"s_getpc_b64 s[96:97]\n"
"L_c6_:\n"
"s_add_u32 s96, s96, L_last-L_c6_\n"
"s_addc_u32 s97, s97, 0\n"
"v_mfma_f32_16x16x32_bf16 v[248:251], a[92:95], a[140:143], v[248:251]\n"
"s_lshr_b32 s66, %26, 3\n"
"v_mfma_f32_16x16x32_bf16 v[220:223], a[84:87], a[140:143], v[220:223]\n"
"s_lshl2_add_u32 s66, s66, %16\n"
"ds_read_b128 a[140:143], v5 offset:27648\n"
"v_mfma_f32_16x16x32_bf16 v[220:223], a[80:83], a[136:139], v[220:223]\n"
"ds_read_b128 a[136:139], v5 offset:26624\n"
"v_mfma_f32_16x16x32_bf16 v[224:227], a[80:83], a[144:147], v[224:227]\n"
"s_mul_i32 s66, s66, 256\n"
"v_mfma_f32_16x16x32_bf16 v[224:227], a[84:87], a[148:151], v[224:227]\n"
"global_load_dwordx4 a[76:79], v0, s[34:35] offset:-1024 sc1\n"
"v_mfma_f32_16x16x32_bf16 v[252:255], a[92:95], a[148:151], v[252:255]\n"
"ds_read_b128 a[148:151], v5 offset:29696\n"
"v_mfma_f32_16x16x32_bf16 v[252:255], a[88:91], a[144:147], v[252:255]\n"
"s_min_u32 s66, s66, L_headF-L_last-256\n"
"ds_read_b128 a[144:147], v5 offset:28672\n"
"v_mfma_f32_16x16x32_bf16 a[28:31], a[88:91], a[152:155], a[28:31]\n"
"v_mfma_f32_16x16x32_bf16 a[28:31], a[92:95], a[156:159], a[28:31]\n"
"v_min_u32 v11, 16, v0\n"
"v_mfma_f32_16x16x32_bf16 a[24:27], a[84:87], a[156:159], a[24:27]\n"
"ds_read_b128 a[156:159], v5 offset:31744\n"
"v_mfma_f32_16x16x32_bf16 a[24:27], a[80:83], a[152:155], a[24:27]\n"
"s_waitcnt vmcnt(12) lgkmcnt(11)\n"
"ds_read_b128 a[152:155], v5 offset:30720\n"
"v_mfma_f32_16x16x32_bf16 v[32:35], a[32:35], a[96:99], v[32:35]\n"
"buffer_load_dwordx4 v1, s[48:51], s69 offen offset:1024 sc1 lds\n"
"v_mfma_f32_16x16x32_bf16 v[32:35], a[36:39], a[100:103], v[32:35]\n"
"global_load_dwordx4 a[80:83], v0, s[34:35] offset:0 sc1\n"
"v_mfma_f32_16x16x32_bf16 v[60:63], a[44:47], a[100:103], v[60:63]\n"
"v_lshl_add_u32 v11, v11, 3, s66\n"
"v_mfma_f32_16x16x32_bf16 v[60:63], a[40:43], a[96:99], v[60:63]\n"
"global_load_dword v10, v11, s[96:97]\n"
"v_mfma_f32_16x16x32_bf16 v[64:67], a[40:43], a[104:107], v[64:67]\n"
"v_mfma_f32_16x16x32_bf16 v[64:67], a[44:47], a[108:111], v[64:67]\n"
"v_mfma_f32_16x16x32_bf16 v[36:39], a[36:39], a[108:111], v[36:39]\n"
"v_mfma_f32_16x16x32_bf16 v[36:39], a[32:35], a[104:107], v[36:39]\n"
"s_waitcnt lgkmcnt(8)\n"
"v_mfma_f32_16x16x32_bf16 v[40:43], a[32:35], a[112:115], v[40:43]\n"
"v_mfma_f32_16x16x32_bf16 v[40:43], a[36:39], a[116:119], v[40:43]\n"
"global_load_dwordx4 a[84:87], v0, s[34:35] offset:1024 sc1\n"
"v_mfma_f32_16x16x32_bf16 v[68:71], a[44:47], a[116:119], v[68:71]\n"
"v_mfma_f32_16x16x32_bf16 v[68:71], a[40:43], a[112:115], v[68:71]\n"
"v_mfma_f32_16x16x32_bf16 v[72:75], a[40:43], a[120:123], v[72:75]\n"
"v_mfma_f32_16x16x32_bf16 v[72:75], a[44:47], a[124:127], v[72:75]\n"
"v_mfma_f32_16x16x32_bf16 v[44:47], a[36:39], a[124:127], v[44:47]\n"
"buffer_load_dwordx4 v1, s[48:51], s69 offen offset:2048 sc1 lds\n"
"v_mfma_f32_16x16x32_bf16 v[44:47], a[32:35], a[120:123], v[44:47]\n"
"s_waitcnt lgkmcnt(4)\n"
"v_mfma_f32_16x16x32_bf16 v[48:51], a[32:35], a[128:131], v[48:51]\n"
"v_mfma_f32_16x16x32_bf16 v[48:51], a[36:39], a[132:135], v[48:51]\n"
"global_load_dwordx4 a[88:91], v0, s[34:35] offset:2048 sc1\n"
"v_mfma_f32_16x16x32_bf16 v[76:79], a[44:47], a[132:135], v[76:79]\n"
"v_mfma_f32_16x16x32_bf16 v[76:79], a[40:43], a[128:131], v[76:79]\n"
"v_mfma_f32_16x16x32_bf16 v[80:83], a[40:43], a[136:139], v[80:83]\n"
"v_mfma_f32_16x16x32_bf16 v[80:83], a[44:47], a[140:143], v[80:83]\n"
"v_mfma_f32_16x16x32_bf16 v[52:55], a[36:39], a[140:143], v[52:55]\n"
"v_mfma_f32_16x16x32_bf16 v[52:55], a[32:35], a[136:139], v[52:55]\n"
"s_waitcnt lgkmcnt(0)\n"
"v_mfma_f32_16x16x32_bf16 v[56:59], a[32:35], a[144:147], v[56:59]\n"
"v_mfma_f32_16x16x32_bf16 v[56:59], a[36:39], a[148:151], v[56:59]\n"
"global_load_dwordx4 a[92:95], v0, s[34:35] offset:3072 sc1\n"
"v_mfma_f32_16x16x32_bf16 v[84:87], a[44:47], a[148:151], v[84:87]\n"
"v_mfma_f32_16x16x32_bf16 v[84:87], a[40:43], a[144:147], v[84:87]\n"
"v_mfma_f32_16x16x32_bf16 a[4:7], a[40:43], a[152:155], a[4:7]\n"
"s_add_u32 s34, s34, 8192\n"
"v_mfma_f32_16x16x32_bf16 a[4:7], a[44:47], a[156:159], a[4:7]\n"
"buffer_load_dwordx4 v1, s[48:51], s69 offen offset:3072 sc1 lds\n"
"v_mfma_f32_16x16x32_bf16 a[0:3], a[36:39], a[156:159], a[0:3]\n"
"v_mfma_f32_16x16x32_bf16 a[0:3], a[32:35], a[152:155], a[0:3]\n"
"s_waitcnt vmcnt(14)\n"
"v_mfma_f32_16x16x32_bf16 v[88:91], a[48:51], a[96:99], v[88:91]\n"
"v_mfma_f32_16x16x32_bf16 v[88:91], a[52:55], a[100:103], v[88:91]\n"
"global_load_dwordx4 a[32:35], v0, s[34:35] offset:-4096 sc1\n"
"v_mfma_f32_16x16x32_bf16 v[116:119], a[60:63], a[100:103], v[116:119]\n"
"v_mfma_f32_16x16x32_bf16 v[116:119], a[56:59], a[96:99], v[116:119]\n"
"v_mfma_f32_16x16x32_bf16 v[120:123], a[56:59], a[104:107], v[120:123]\n"
"v_mfma_f32_16x16x32_bf16 v[120:123], a[60:63], a[108:111], v[120:123]\n"
"v_mfma_f32_16x16x32_bf16 v[92:95], a[52:55], a[108:111], v[92:95]\n"
"v_mfma_f32_16x16x32_bf16 v[92:95], a[48:51], a[104:107], v[92:95]\n"
"v_mfma_f32_16x16x32_bf16 v[96:99], a[48:51], a[112:115], v[96:99]\n"
"v_mfma_f32_16x16x32_bf16 v[96:99], a[52:55], a[116:119], v[96:99]\n"
"global_load_dwordx4 a[36:39], v0, s[34:35] offset:-3072 sc1\n"
"v_mfma_f32_16x16x32_bf16 v[124:127], a[60:63], a[116:119], v[124:127]\n"
"v_mfma_f32_16x16x32_bf16 v[124:127], a[56:59], a[112:115], v[124:127]\n"
"v_mfma_f32_16x16x32_bf16 v[128:131], a[56:59], a[120:123], v[128:131]\n"
"v_mfma_f32_16x16x32_bf16 v[128:131], a[60:63], a[124:127], v[128:131]\n"
"v_mfma_f32_16x16x32_bf16 v[100:103], a[52:55], a[124:127], v[100:103]\n"
"v_mfma_f32_16x16x32_bf16 v[100:103], a[48:51], a[120:123], v[100:103]\n"
"v_mfma_f32_16x16x32_bf16 v[104:107], a[48:51], a[128:131], v[104:107]\n"
"buffer_store_dwordx4 v[24:27], v1, s[36:39], s28 offen offset:0 sc1\n"
"buffer_load_dwordx2 v[8:9], off, s[56:59], %14 sc1\n"
"v_mfma_f32_16x16x32_bf16 v[104:107], a[52:55], a[132:135], v[104:107]\n"
"global_load_dwordx4 a[40:43], v0, s[34:35] offset:-2048 sc1\n"
"v_mfma_f32_16x16x32_bf16 v[132:135], a[60:63], a[132:135], v[132:135]\n"
"v_mfma_f32_16x16x32_bf16 v[132:135], a[56:59], a[128:131], v[132:135]\n"
"v_mfma_f32_16x16x32_bf16 v[136:139], a[56:59], a[136:139], v[136:139]\n"
"v_mfma_f32_16x16x32_bf16 v[136:139], a[60:63], a[140:143], v[136:139]\n"
"v_mfma_f32_16x16x32_bf16 v[108:111], a[52:55], a[140:143], v[108:111]\n"
"v_mfma_f32_16x16x32_bf16 v[108:111], a[48:51], a[136:139], v[108:111]\n"
"v_mfma_f32_16x16x32_bf16 v[112:115], a[48:51], a[144:147], v[112:115]\n"
"v_mfma_f32_16x16x32_bf16 v[112:115], a[52:55], a[148:151], v[112:115]\n"
"global_load_dwordx4 a[44:47], v0, s[34:35] offset:-1024 sc1\n"
"v_mfma_f32_16x16x32_bf16 v[140:143], a[60:63], a[148:151], v[140:143]\n"
"v_mfma_f32_16x16x32_bf16 v[140:143], a[56:59], a[144:147], v[140:143]\n"
"v_mfma_f32_16x16x32_bf16 a[12:15], a[56:59], a[152:155], a[12:15]\n"
"v_mfma_f32_16x16x32_bf16 a[12:15], a[60:63], a[156:159], a[12:15]\n"
"v_mfma_f32_16x16x32_bf16 a[8:11], a[52:55], a[156:159], a[8:11]\n"
"v_mfma_f32_16x16x32_bf16 a[8:11], a[48:51], a[152:155], a[8:11]\n"
"s_waitcnt vmcnt(14)\n"
"v_mfma_f32_16x16x32_bf16 v[144:147], a[64:67], a[96:99], v[144:147]\n"
"v_mfma_f32_16x16x32_bf16 v[144:147], a[68:71], a[100:103], v[144:147]\n"
"global_load_dwordx4 a[48:51], v0, s[34:35] offset:0 sc1\n"
"v_mfma_f32_16x16x32_bf16 v[172:175], a[76:79], a[100:103], v[172:175]\n"
"v_mfma_f32_16x16x32_bf16 v[172:175], a[72:75], a[96:99], v[172:175]\n"
"v_mfma_f32_16x16x32_bf16 v[176:179], a[72:75], a[104:107], v[176:179]\n"
"v_mfma_f32_16x16x32_bf16 v[176:179], a[76:79], a[108:111], v[176:179]\n"
"v_mfma_f32_16x16x32_bf16 v[148:151], a[68:71], a[108:111], v[148:151]\n"
"v_mfma_f32_16x16x32_bf16 v[148:151], a[64:67], a[104:107], v[148:151]\n"
"v_mfma_f32_16x16x32_bf16 v[152:155], a[64:67], a[112:115], v[152:155]\n"
"v_lshlrev_b32 v20, 16, v22\n"
"v_mfma_f32_16x16x32_bf16 v[152:155], a[68:71], a[116:119], v[152:155]\n"
"global_load_dwordx4 a[52:55], v0, s[34:35] offset:1024 sc1\n"
"v_and_b32 v21, s63, v22\n"
"v_mfma_f32_16x16x32_bf16 v[180:183], a[76:79], a[116:119], v[180:183]\n"
"v_lshlrev_b32 v22, 16, v23\n"
"v_mfma_f32_16x16x32_bf16 v[180:183], a[72:75], a[112:115], v[180:183]\n"
"v_and_b32 v23, s63, v23\n"
"v_mfma_f32_16x16x32_bf16 v[184:187], a[72:75], a[120:123], v[184:187]\n"
"v_add_f32 v60, v60, v20\n"
"v_mfma_f32_16x16x32_bf16 v[184:187], a[76:79], a[124:127], v[184:187]\n"
"v_add_f32 v61, v61, v21\n"
"v_mfma_f32_16x16x32_bf16 v[156:159], a[68:71], a[124:127], v[156:159]\n"
"buffer_store_dwordx4 v[28:31], v1, s[36:39], s28 offen offset:2048 sc1\n"
"v_add_f32 v62, v62, v22\n"
"v_add_f32 v63, v63, v23\n"
"v_add_f32 v64, v64, v20\n"
"v_mfma_f32_16x16x32_bf16 v[156:159], a[64:67], a[120:123], v[156:159]\n"
"v_add_f32 v65, v65, v21\n"
"v_mfma_f32_16x16x32_bf16 v[160:163], a[64:67], a[128:131], v[160:163]\n"
"v_add_f32 v66, v66, v22\n"
"v_mfma_f32_16x16x32_bf16 v[160:163], a[68:71], a[132:135], v[160:163]\n"
"global_load_dwordx4 a[56:59], v0, s[34:35] offset:2048 sc1\n"
"v_mfma_f32_16x16x32_bf16 v[188:191], a[76:79], a[132:135], v[188:191]\n"
"v_add_f32 v67, v67, v23\n"
"v_mfma_f32_16x16x32_bf16 v[188:191], a[72:75], a[128:131], v[188:191]\n"
"v_add_f32 v68, v68, v20\n"
"v_mfma_f32_16x16x32_bf16 v[192:195], a[72:75], a[136:139], v[192:195]\n"
"v_add_f32 v69, v69, v21\n"
"v_mfma_f32_16x16x32_bf16 v[192:195], a[76:79], a[140:143], v[192:195]\n"
"v_add_f32 v70, v70, v22\n"
"v_mfma_f32_16x16x32_bf16 v[164:167], a[68:71], a[140:143], v[164:167]\n"
"v_add_f32 v71, v71, v23\n"
"v_mfma_f32_16x16x32_bf16 v[164:167], a[64:67], a[136:139], v[164:167]\n"
"v_add_f32 v72, v72, v20\n"
"v_add_f32 v73, v73, v21\n"
"v_mfma_f32_16x16x32_bf16 v[168:171], a[64:67], a[144:147], v[168:171]\n"
"v_add_f32 v74, v74, v22\n"
"v_mfma_f32_16x16x32_bf16 v[168:171], a[68:71], a[148:151], v[168:171]\n"
"global_load_dwordx4 a[60:63], v0, s[34:35] offset:3072 sc1\n"
"v_add_f32 v75, v75, v23\n"
"v_mfma_f32_16x16x32_bf16 v[196:199], a[76:79], a[148:151], v[196:199]\n"
"v_add_f32 v76, v76, v20\n"
"v_mfma_f32_16x16x32_bf16 v[196:199], a[72:75], a[144:147], v[196:199]\n"
"v_add_f32 v77, v77, v21\n"
"v_mfma_f32_16x16x32_bf16 a[20:23], a[72:75], a[152:155], a[20:23]\n"
"s_add_u32 s34, s34, 8192\n"
"v_add_f32 v78, v78, v22\n"
"v_mfma_f32_16x16x32_bf16 a[20:23], a[76:79], a[156:159], a[20:23]\n"
"v_add_f32 v79, v79, v23\n"
"v_mfma_f32_16x16x32_bf16 a[16:19], a[68:71], a[156:159], a[16:19]\n"
"v_add_f32 v80, v80, v20\n"
"v_mfma_f32_16x16x32_bf16 a[16:19], a[64:67], a[152:155], a[16:19]\n"
"s_waitcnt vmcnt(12)\n"
"v_add_f32 v81, v81, v21\n"
"v_mfma_f32_16x16x32_bf16 v[200:203], a[80:83], a[96:99], v[200:203]\n"
"v_add_f32 v82, v82, v22\n"
"v_add_f32 v83, v83, v23\n"
"v_mfma_f32_16x16x32_bf16 v[200:203], a[84:87], a[100:103], v[200:203]\n"
"global_load_dwordx4 a[64:67], v0, s[34:35] offset:-4096 sc1\n"
"v_add_f32 v84, v84, v20\n"
"v_mfma_f32_16x16x32_bf16 v[228:231], a[92:95], a[100:103], v[228:231]\n"
"s_waitcnt vmcnt(12)\n"
"ds_read_b128 a[100:103], v2 offset:1024\n"
"v_add_f32 v85, v85, v21\n"
"v_mfma_f32_16x16x32_bf16 v[228:231], a[88:91], a[96:99], v[228:231]\n"
"ds_read_b128 a[96:99], v2 offset:0\n"
"v_add_f32 v86, v86, v22\n"
"v_mfma_f32_16x16x32_bf16 v[232:235], a[88:91], a[104:107], v[232:235]\n"
"v_add_f32 v87, v87, v23\n"
"v_mfma_f32_16x16x32_bf16 v[232:235], a[92:95], a[108:111], v[232:235]\n"
"L_st9095:\n"
"s_cbranch_execz L_retB\n"
"v_accvgpr_read_b32 v12, a4\n"
"v_mfma_f32_16x16x32_bf16 v[204:207], a[84:87], a[108:111], v[204:207]\n"
"ds_read_b128 a[108:111], v2 offset:3072\n"
"v_accvgpr_read_b32 v13, a5\n"
"v_mfma_f32_16x16x32_bf16 v[204:207], a[80:83], a[104:107], v[204:207]\n"
"ds_read_b128 a[104:107], v2 offset:2048\n"
"v_accvgpr_read_b32 v14, a6\n"
"v_mfma_f32_16x16x32_bf16 v[208:211], a[80:83], a[112:115], v[208:211]\n"
"v_accvgpr_read_b32 v15, a7\n"
"v_mfma_f32_16x16x32_bf16 v[208:211], a[84:87], a[116:119], v[208:211]\n"
"global_load_dwordx4 a[68:71], v0, s[34:35] offset:-3072 sc1\n"
"v_add_f32 v12, v12, v20\n"
"v_mfma_f32_16x16x32_bf16 v[236:239], a[92:95], a[116:119], v[236:239]\n"
"ds_read_b128 a[116:119], v2 offset:5120\n"
"v_add_f32 v13, v13, v21\n"
"v_mfma_f32_16x16x32_bf16 v[236:239], a[88:91], a[112:115], v[236:239]\n"
"ds_read_b128 a[112:115], v2 offset:4096\n"
"v_add_f32 v14, v14, v22\n"
"v_mfma_f32_16x16x32_bf16 v[240:243], a[88:91], a[120:123], v[240:243]\n"
"v_add_f32 v15, v15, v23\n"
"v_accvgpr_write_b32 a4, v12\n"
"v_mfma_f32_16x16x32_bf16 v[240:243], a[92:95], a[124:127], v[240:243]\n"
"v_accvgpr_write_b32 a5, v13\n"
"v_mfma_f32_16x16x32_bf16 v[212:215], a[84:87], a[124:127], v[212:215]\n"
"ds_read_b128 a[124:127], v2 offset:7168\n"
"v_accvgpr_write_b32 a6, v14\n"
"v_mfma_f32_16x16x32_bf16 v[212:215], a[80:83], a[120:123], v[212:215]\n"
"ds_read_b128 a[120:123], v2 offset:6144\n"
"v_accvgpr_write_b32 a7, v15\n"
"v_mfma_f32_16x16x32_bf16 v[216:219], a[80:83], a[128:131], v[216:219]\n"
"s_add_u32 s28, s28, 0x40000\n"
"v_mfma_f32_16x16x32_bf16 v[216:219], a[84:87], a[132:135], v[216:219]\n"
"global_load_dwordx4 a[72:75], v0, s[34:35] offset:-2048 sc1\n"
"v_lshrrev_b32 v20, 4, v0\n"
"v_mfma_f32_16x16x32_bf16 v[244:247], a[92:95], a[132:135], v[244:247]\n"
"s_add_u32 s67, s78, s62\n"
"ds_read_b128 a[132:135], v2 offset:9216\n"
"v_and_b32 v20, 0x30, v20\n"
"v_mfma_f32_16x16x32_bf16 v[244:247], a[88:91], a[128:131], v[244:247]\n"
"ds_read_b128 a[128:131], v2 offset:8192\n"
"buffer_load_dwordx2 v[22:23], v20, s[44:47], s61 offen offset:72\n"
"v_mfma_f32_16x16x32_bf16 v[248:251], a[88:91], a[136:139], v[248:251]\n"
"s_add_u32 s67, s67, 1\n"
"v_mfma_f32_16x16x32_bf16 v[248:251], a[92:95], a[140:143], v[248:251]\n"
"v_mfma_f32_16x16x32_bf16 v[220:223], a[84:87], a[140:143], v[220:223]\n"
"ds_read_b128 a[140:143], v2 offset:11264\n"
"v_mov_b32 v24, s67\n"
"v_mfma_f32_16x16x32_bf16 v[220:223], a[80:83], a[136:139], v[220:223]\n"
"ds_read_b128 a[136:139], v2 offset:10240\n"
"v_mov_b32 v25, s79\n"
"v_mfma_f32_16x16x32_bf16 v[224:227], a[80:83], a[144:147], v[224:227]\n"
"v_mfma_f32_16x16x32_bf16 v[224:227], a[84:87], a[148:151], v[224:227]\n"
"s_waitcnt vmcnt(6)\n"
"global_load_dwordx4 a[76:79], v0, s[34:35] offset:-1024 sc1\n"
"buffer_store_dwordx2 v[24:25], off, s[56:59], %24 sc1\n"
"v_mfma_f32_16x16x32_bf16 v[252:255], a[92:95], a[148:151], v[252:255]\n"
"ds_read_b128 a[148:151], v2 offset:13312\n"
"v_mfma_f32_16x16x32_bf16 v[252:255], a[88:91], a[144:147], v[252:255]\n"
"ds_read_b128 a[144:147], v2 offset:12288\n"
"v_mfma_f32_16x16x32_bf16 a[28:31], a[88:91], a[152:155], a[28:31]\n"
"v_mfma_f32_16x16x32_bf16 a[28:31], a[92:95], a[156:159], a[28:31]\n"
"v_mfma_f32_16x16x32_bf16 a[24:27], a[84:87], a[156:159], a[24:27]\n"
"s_cmp_eq_u32 s62, 5\n"
"ds_read_b128 a[156:159], v2 offset:15360\n"
"v_mfma_f32_16x16x32_bf16 a[24:27], a[80:83], a[152:155], a[24:27]\n"
"ds_read_b128 a[152:155], v2 offset:14336\n"
"s_cbranch_scc1 L_last\n"
"s_branch L_P4\n"
"L_last:\n"
"L_st9164:\n"
"s_cbranch_execz L_retB\n"
"s_waitcnt vmcnt(9) lgkmcnt(12)\n"
"v_mfma_f32_16x16x32_bf16 v[32:35], a[32:35], a[96:99], v[32:35]\n"
"s_add_u32 s34, s36, 1454080\n"
"s_add_u32 s48, s48, 131072\n"
"s_addc_u32 s49, s49, 0\n"
"v_mfma_f32_16x16x32_bf16 v[32:35], a[36:39], a[100:103], v[32:35]\n"
"s_mov_b64 exec, 0\n"
"s_cmp_eq_u32 %16, 0\n"
"s_cbranch_scc1 L_st9674\n"
"s_cmp_eq_u32 %16, 1\n"
"s_cbranch_scc1 L_st10279\n"
"s_cmp_eq_u32 %16, 2\n"
"s_cbranch_scc1 L_st10887\n"
"s_branch L_st11492\n"
"L_retC:\n"
"s_mov_b64 exec, -1\n"
"s_nop 3\n"
"global_load_dwordx4 a[80:83], v0, s[34:35] offset:0 sc1\n"
"v_mfma_f32_16x16x32_bf16 v[60:63], a[44:47], a[100:103], v[60:63]\n"
"s_barrier\n"
"v_mfma_f32_16x16x32_bf16 v[60:63], a[40:43], a[96:99], v[60:63]\n"
"v_mfma_f32_16x16x32_bf16 v[64:67], a[40:43], a[104:107], v[64:67]\n"
"v_mfma_f32_16x16x32_bf16 v[64:67], a[44:47], a[108:111], v[64:67]\n"
"v_mfma_f32_16x16x32_bf16 v[36:39], a[36:39], a[108:111], v[36:39]\n"
"v_subrev_u32 v8, s65, v8\n"
"v_mfma_f32_16x16x32_bf16 v[36:39], a[32:35], a[104:107], v[36:39]\n"
"s_waitcnt lgkmcnt(8)\n"
"v_xor_b32 v9, s79, v9\n"
"v_mfma_f32_16x16x32_bf16 v[40:43], a[32:35], a[112:115], v[40:43]\n"
"v_or_b32 v8, v8, v9\n"
"v_mfma_f32_16x16x32_bf16 v[40:43], a[36:39], a[116:119], v[40:43]\n"
"global_load_dwordx4 a[84:87], v0, s[34:35] offset:1024 sc1\n"
"v_cmp_lt_u32 vcc, 15, v8\n"
"v_mfma_f32_16x16x32_bf16 v[68:71], a[44:47], a[116:119], v[68:71]\n"
"s_cbranch_vccnz L_slow4\n"
"L_back4:\n"
"v_mfma_f32_16x16x32_bf16 v[68:71], a[40:43], a[112:115], v[68:71]\n"
"v_mfma_f32_16x16x32_bf16 v[72:75], a[40:43], a[120:123], v[72:75]\n"
"v_mfma_f32_16x16x32_bf16 v[72:75], a[44:47], a[124:127], v[72:75]\n"
"v_mfma_f32_16x16x32_bf16 v[44:47], a[36:39], a[124:127], v[44:47]\n"
"v_mfma_f32_16x16x32_bf16 v[44:47], a[32:35], a[120:123], v[44:47]\n"
"s_add_u32 m0, s64, 16384\n"
"s_waitcnt lgkmcnt(4)\n"
"v_mfma_f32_16x16x32_bf16 v[48:51], a[32:35], a[128:131], v[48:51]\n"
"v_mfma_f32_16x16x32_bf16 v[48:51], a[36:39], a[132:135], v[48:51]\n"
"global_load_dwordx4 a[88:91], v0, s[34:35] offset:2048 sc1\n"
"v_mfma_f32_16x16x32_bf16 v[76:79], a[44:47], a[132:135], v[76:79]\n"
"v_mfma_f32_16x16x32_bf16 v[76:79], a[40:43], a[128:131], v[76:79]\n"
"v_mfma_f32_16x16x32_bf16 v[80:83], a[40:43], a[136:139], v[80:83]\n"
"s_mov_b32 s97, s37\n"
"v_mfma_f32_16x16x32_bf16 v[80:83], a[44:47], a[140:143], v[80:83]\n"
"v_mfma_f32_16x16x32_bf16 v[52:55], a[36:39], a[140:143], v[52:55]\n"
"v_mfma_f32_16x16x32_bf16 v[52:55], a[32:35], a[136:139], v[52:55]\n"
"s_waitcnt lgkmcnt(0)\n"
"v_mfma_f32_16x16x32_bf16 v[56:59], a[32:35], a[144:147], v[56:59]\n"
"buffer_load_dwordx4 v0, s[48:51], s75 offen offset:0 sc1 lds\n"
"v_mfma_f32_16x16x32_bf16 v[56:59], a[36:39], a[148:151], v[56:59]\n"
"global_load_dwordx4 a[92:95], v0, s[34:35] offset:3072 sc1\n"
"v_mfma_f32_16x16x32_bf16 v[84:87], a[44:47], a[148:151], v[84:87]\n"
"v_mfma_f32_16x16x32_bf16 v[84:87], a[40:43], a[144:147], v[84:87]\n"
"v_mfma_f32_16x16x32_bf16 a[4:7], a[40:43], a[152:155], a[4:7]\n"
"v_mfma_f32_16x16x32_bf16 a[4:7], a[44:47], a[156:159], a[4:7]\n"
"v_mfma_f32_16x16x32_bf16 a[0:3], a[36:39], a[156:159], a[0:3]\n"
"v_mfma_f32_16x16x32_bf16 a[0:3], a[32:35], a[152:155], a[0:3]\n"
"s_waitcnt vmcnt(10)\n"
"v_mfma_f32_16x16x32_bf16 v[88:91], a[48:51], a[96:99], v[88:91]\n"
"s_add_u32 s34, s36, 1458176\n"
"v_mfma_f32_16x16x32_bf16 v[88:91], a[52:55], a[100:103], v[88:91]\n"
"global_load_dwordx4 a[32:35], v0, s[34:35] offset:0 sc1\n"
"v_mfma_f32_16x16x32_bf16 v[116:119], a[60:63], a[100:103], v[116:119]\n"
"buffer_load_dwordx4 v0, s[48:51], s75 offen offset:1024 sc1 lds\n"
"v_mfma_f32_16x16x32_bf16 v[116:119], a[56:59], a[96:99], v[116:119]\n"
"v_mfma_f32_16x16x32_bf16 v[120:123], a[56:59], a[104:107], v[120:123]\n"
"v_mfma_f32_16x16x32_bf16 v[120:123], a[60:63], a[108:111], v[120:123]\n"
"v_mfma_f32_16x16x32_bf16 v[92:95], a[52:55], a[108:111], v[92:95]\n"
"v_mfma_f32_16x16x32_bf16 v[92:95], a[48:51], a[104:107], v[92:95]\n"
"v_mfma_f32_16x16x32_bf16 v[96:99], a[48:51], a[112:115], v[96:99]\n"
"v_mfma_f32_16x16x32_bf16 v[96:99], a[52:55], a[116:119], v[96:99]\n"
"global_load_dwordx4 a[36:39], v0, s[34:35] offset:1024 sc1\n"
"v_mfma_f32_16x16x32_bf16 v[124:127], a[60:63], a[116:119], v[124:127]\n"
"v_mfma_f32_16x16x32_bf16 v[124:127], a[56:59], a[112:115], v[124:127]\n"
"v_mfma_f32_16x16x32_bf16 v[128:131], a[56:59], a[120:123], v[128:131]\n"
"buffer_load_dwordx4 v0, s[48:51], s75 offen offset:2048 sc1 lds\n"
"v_mfma_f32_16x16x32_bf16 v[128:131], a[60:63], a[124:127], v[128:131]\n"
"v_mfma_f32_16x16x32_bf16 v[100:103], a[52:55], a[124:127], v[100:103]\n"
"v_mfma_f32_16x16x32_bf16 v[100:103], a[48:51], a[120:123], v[100:103]\n"
"v_mfma_f32_16x16x32_bf16 v[104:107], a[48:51], a[128:131], v[104:107]\n"
"v_mfma_f32_16x16x32_bf16 v[104:107], a[52:55], a[132:135], v[104:107]\n"
"global_load_dwordx4 a[40:43], v0, s[34:35] offset:2048 sc1\n"
"v_mfma_f32_16x16x32_bf16 v[132:135], a[60:63], a[132:135], v[132:135]\n"
"v_mfma_f32_16x16x32_bf16 v[132:135], a[56:59], a[128:131], v[132:135]\n"
"v_mfma_f32_16x16x32_bf16 v[136:139], a[56:59], a[136:139], v[136:139]\n"
"v_mfma_f32_16x16x32_bf16 v[136:139], a[60:63], a[140:143], v[136:139]\n"
"buffer_load_dwordx4 v0, s[48:51], s75 offen offset:3072 sc1 lds\n"
"v_mfma_f32_16x16x32_bf16 v[108:111], a[52:55], a[140:143], v[108:111]\n"
"v_mfma_f32_16x16x32_bf16 v[108:111], a[48:51], a[136:139], v[108:111]\n"
"v_mfma_f32_16x16x32_bf16 v[112:115], a[48:51], a[144:147], v[112:115]\n"
"v_mfma_f32_16x16x32_bf16 v[112:115], a[52:55], a[148:151], v[112:115]\n"
"global_load_dwordx4 a[44:47], v0, s[34:35] offset:3072 sc1\n"
"v_mfma_f32_16x16x32_bf16 v[140:143], a[60:63], a[148:151], v[140:143]\n"
"v_mfma_f32_16x16x32_bf16 v[140:143], a[56:59], a[144:147], v[140:143]\n"
"v_mfma_f32_16x16x32_bf16 a[12:15], a[56:59], a[152:155], a[12:15]\n"
"v_mfma_f32_16x16x32_bf16 a[12:15], a[60:63], a[156:159], a[12:15]\n"
"v_mfma_f32_16x16x32_bf16 a[8:11], a[52:55], a[156:159], a[8:11]\n"
"v_mfma_f32_16x16x32_bf16 a[8:11], a[48:51], a[152:155], a[8:11]\n"
"s_add_u32 m0, s64, 20480\n"
"s_waitcnt vmcnt(12)\n"
"v_mfma_f32_16x16x32_bf16 v[144:147], a[64:67], a[96:99], v[144:147]\n"
"s_add_u32 s34, s36, 1462272\n"
"v_mfma_f32_16x16x32_bf16 v[144:147], a[68:71], a[100:103], v[144:147]\n"
"global_load_dwordx4 a[48:51], v0, s[34:35] offset:0 sc1\n"
"v_mfma_f32_16x16x32_bf16 v[172:175], a[76:79], a[100:103], v[172:175]\n"
"v_mfma_f32_16x16x32_bf16 v[172:175], a[72:75], a[96:99], v[172:175]\n"
"v_mfma_f32_16x16x32_bf16 v[176:179], a[72:75], a[104:107], v[176:179]\n"
"v_mfma_f32_16x16x32_bf16 v[176:179], a[76:79], a[108:111], v[176:179]\n"
"v_mfma_f32_16x16x32_bf16 v[148:151], a[68:71], a[108:111], v[148:151]\n"
"v_lshlrev_b32 v16, 16, v18\n"
"v_and_b32 v17, s63, v18\n"
"v_mfma_f32_16x16x32_bf16 v[148:151], a[64:67], a[104:107], v[148:151]\n"
"v_lshlrev_b32 v18, 16, v19\n"
"v_and_b32 v19, s63, v19\n"
"v_mfma_f32_16x16x32_bf16 v[152:155], a[64:67], a[112:115], v[152:155]\n"
"buffer_load_dwordx4 v1, s[48:51], s75 offen offset:0 sc1 lds\n"
"v_mfma_f32_16x16x32_bf16 v[152:155], a[68:71], a[116:119], v[152:155]\n"
"global_load_dwordx4 a[52:55], v0, s[34:35] offset:1024 sc1\n"
"v_add_f32 v88, v88, v16\n"
"v_mfma_f32_16x16x32_bf16 v[180:183], a[76:79], a[116:119], v[180:183]\n"
"v_add_f32 v89, v89, v17\n"
"v_add_f32 v90, v90, v18\n"
"v_mfma_f32_16x16x32_bf16 v[180:183], a[72:75], a[112:115], v[180:183]\n"
"v_add_f32 v91, v91, v19\n"
"v_add_f32 v92, v92, v16\n"
"v_mfma_f32_16x16x32_bf16 v[184:187], a[72:75], a[120:123], v[184:187]\n"
"v_add_f32 v93, v93, v17\n"
"v_add_f32 v94, v94, v18\n"
"v_mfma_f32_16x16x32_bf16 v[184:187], a[76:79], a[124:127], v[184:187]\n"
"v_add_f32 v95, v95, v19\n"
"v_add_f32 v96, v96, v16\n"
"v_mfma_f32_16x16x32_bf16 v[156:159], a[68:71], a[124:127], v[156:159]\n"
"v_add_f32 v97, v97, v17\n"
"v_add_f32 v98, v98, v18\n"
"v_mfma_f32_16x16x32_bf16 v[156:159], a[64:67], a[120:123], v[156:159]\n"
"v_add_f32 v99, v99, v19\n"
"v_add_f32 v100, v100, v16\n"
"v_mfma_f32_16x16x32_bf16 v[160:163], a[64:67], a[128:131], v[160:163]\n"
"v_add_f32 v101, v101, v17\n"
"v_add_f32 v102, v102, v18\n"
"v_mfma_f32_16x16x32_bf16 v[160:163], a[68:71], a[132:135], v[160:163]\n"
"global_load_dwordx4 a[56:59], v0, s[34:35] offset:2048 sc1\n"
"v_add_f32 v103, v103, v19\n"
"v_mfma_f32_16x16x32_bf16 v[188:191], a[76:79], a[132:135], v[188:191]\n"
"buffer_load_dwordx4 v1, s[48:51], s75 offen offset:1024 sc1 lds\n"
"v_mfma_f32_16x16x32_bf16 v[188:191], a[72:75], a[128:131], v[188:191]\n"
"v_add_f32 v104, v104, v16\n"
"v_add_f32 v105, v105, v17\n"
"v_mfma_f32_16x16x32_bf16 v[192:195], a[72:75], a[136:139], v[192:195]\n"
"v_add_f32 v106, v106, v18\n"
"v_add_f32 v107, v107, v19\n"
"v_mfma_f32_16x16x32_bf16 v[192:195], a[76:79], a[140:143], v[192:195]\n"
"v_add_f32 v108, v108, v16\n"
"v_add_f32 v109, v109, v17\n"
"v_mfma_f32_16x16x32_bf16 v[164:167], a[68:71], a[140:143], v[164:167]\n"
"v_add_f32 v110, v110, v18\n"
"v_add_f32 v111, v111, v19\n"
"v_mfma_f32_16x16x32_bf16 v[164:167], a[64:67], a[136:139], v[164:167]\n"
"v_add_f32 v112, v112, v16\n"
"v_add_f32 v113, v113, v17\n"
"v_mfma_f32_16x16x32_bf16 v[168:171], a[64:67], a[144:147], v[168:171]\n"
"v_add_f32 v114, v114, v18\n"
"v_add_f32 v115, v115, v19\n"
"v_mfma_f32_16x16x32_bf16 v[168:171], a[68:71], a[148:151], v[168:171]\n"
"global_load_dwordx4 a[60:63], v0, s[34:35] offset:3072 sc1\n"
"v_accvgpr_read_b32 v12, a8\n"
"v_mfma_f32_16x16x32_bf16 v[196:199], a[76:79], a[148:151], v[196:199]\n"
"v_accvgpr_read_b32 v13, a9\n"
"v_accvgpr_read_b32 v14, a10\n"
"v_mfma_f32_16x16x32_bf16 v[196:199], a[72:75], a[144:147], v[196:199]\n"
"buffer_load_dwordx4 v1, s[48:51], s75 offen offset:2048 sc1 lds\n"
"v_mfma_f32_16x16x32_bf16 a[20:23], a[72:75], a[152:155], a[20:23]\n"
"v_accvgpr_read_b32 v15, a11\n"
"v_add_f32 v12, v12, v16\n"
"v_mfma_f32_16x16x32_bf16 a[20:23], a[76:79], a[156:159], a[20:23]\n"
"v_add_f32 v13, v13, v17\n"
"v_add_f32 v14, v14, v18\n"
"v_mfma_f32_16x16x32_bf16 a[16:19], a[68:71], a[156:159], a[16:19]\n"
"v_add_f32 v15, v15, v19\n"
"v_accvgpr_write_b32 a8, v12\n"
"v_mfma_f32_16x16x32_bf16 a[16:19], a[64:67], a[152:155], a[16:19]\n"
"s_waitcnt vmcnt(14)\n"
"v_accvgpr_write_b32 a9, v13\n"
"v_accvgpr_write_b32 a10, v14\n"
"v_accvgpr_write_b32 a11, v15\n"
"v_mfma_f32_16x16x32_bf16 v[200:203], a[80:83], a[96:99], v[200:203]\n"
"s_add_u32 s34, s36, 1474560\n"
"v_lshlrev_b32 v20, 16, v22\n"
"v_and_b32 v21, s63, v22\n"
"v_mfma_f32_16x16x32_bf16 v[200:203], a[84:87], a[100:103], v[200:203]\n"
"global_load_dwordx4 a[64:67], v0, s[34:35] offset:0 sc1\n"
"v_lshlrev_b32 v22, 16, v23\n"
"v_mfma_f32_16x16x32_bf16 v[228:231], a[92:95], a[100:103], v[228:231]\n"
"ds_read_b128 a[100:103], v3 offset:1024\n"
"v_and_b32 v23, s63, v23\n"
"v_mfma_f32_16x16x32_bf16 v[228:231], a[88:91], a[96:99], v[228:231]\n"
"ds_read_b128 a[96:99], v3 offset:0\n"
"v_add_f32 v116, v116, v20\n"
"v_mfma_f32_16x16x32_bf16 v[232:235], a[88:91], a[104:107], v[232:235]\n"
"v_add_f32 v117, v117, v21\n"
"v_add_f32 v118, v118, v22\n"
"v_mfma_f32_16x16x32_bf16 v[232:235], a[92:95], a[108:111], v[232:235]\n"
"buffer_load_dwordx4 v1, s[48:51], s75 offen offset:3072 sc1 lds\n"
"v_mfma_f32_16x16x32_bf16 v[204:207], a[84:87], a[108:111], v[204:207]\n"
"ds_read_b128 a[108:111], v3 offset:3072\n"
"v_add_f32 v119, v119, v23\n"
"v_add_f32 v120, v120, v20\n"
"v_mfma_f32_16x16x32_bf16 v[204:207], a[80:83], a[104:107], v[204:207]\n"
"ds_read_b128 a[104:107], v3 offset:2048\n"
"v_add_f32 v121, v121, v21\n"
"v_mfma_f32_16x16x32_bf16 v[208:211], a[80:83], a[112:115], v[208:211]\n"
"v_add_f32 v122, v122, v22\n"
"v_add_f32 v123, v123, v23\n"
"v_mfma_f32_16x16x32_bf16 v[208:211], a[84:87], a[116:119], v[208:211]\n"
"global_load_dwordx4 a[68:71], v0, s[34:35] offset:1024 sc1\n"
"v_add_f32 v124, v124, v20\n"
"v_add_f32 v125, v125, v21\n"
"v_mfma_f32_16x16x32_bf16 v[236:239], a[92:95], a[116:119], v[236:239]\n"
"ds_read_b128 a[116:119], v3 offset:5120\n"
"v_add_f32 v126, v126, v22\n"
"v_mfma_f32_16x16x32_bf16 v[236:239], a[88:91], a[112:115], v[236:239]\n"
"ds_read_b128 a[112:115], v3 offset:4096\n"
"v_add_f32 v127, v127, v23\n"
"v_mfma_f32_16x16x32_bf16 v[240:243], a[88:91], a[120:123], v[240:243]\n"
"v_add_f32 v128, v128, v20\n"
"v_add_f32 v129, v129, v21\n"
"v_mfma_f32_16x16x32_bf16 v[240:243], a[92:95], a[124:127], v[240:243]\n"
"v_add_f32 v130, v130, v22\n"
"v_add_f32 v131, v131, v23\n"
"v_mfma_f32_16x16x32_bf16 v[212:215], a[84:87], a[124:127], v[212:215]\n"
"s_add_u32 m0, s64, 24576\n"
"ds_read_b128 a[124:127], v3 offset:7168\n"
"v_add_f32 v132, v132, v20\n"
"v_add_f32 v133, v133, v21\n"
"v_mfma_f32_16x16x32_bf16 v[212:215], a[80:83], a[120:123], v[212:215]\n"
"ds_read_b128 a[120:123], v3 offset:6144\n"
"v_add_f32 v134, v134, v22\n"
"v_mfma_f32_16x16x32_bf16 v[216:219], a[80:83], a[128:131], v[216:219]\n"
"v_add_f32 v135, v135, v23\n"
"v_add_f32 v136, v136, v20\n"
"v_mfma_f32_16x16x32_bf16 v[216:219], a[84:87], a[132:135], v[216:219]\n"
"global_load_dwordx4 a[72:75], v0, s[34:35] offset:2048 sc1\n"
"v_add_f32 v137, v137, v21\n"
"v_mfma_f32_16x16x32_bf16 v[244:247], a[92:95], a[132:135], v[244:247]\n"
"ds_read_b128 a[132:135], v3 offset:9216\n"
"v_add_f32 v138, v138, v22\n"
"v_add_f32 v139, v139, v23\n"
"v_mfma_f32_16x16x32_bf16 v[244:247], a[88:91], a[128:131], v[244:247]\n"
"ds_read_b128 a[128:131], v3 offset:8192\n"
"v_add_f32 v140, v140, v20\n"
"v_mfma_f32_16x16x32_bf16 v[248:251], a[88:91], a[136:139], v[248:251]\n"
"v_add_f32 v141, v141, v21\n"
"v_add_f32 v142, v142, v22\n"
"v_mfma_f32_16x16x32_bf16 v[248:251], a[92:95], a[140:143], v[248:251]\n"
"v_add_f32 v143, v143, v23\n"
"v_accvgpr_read_b32 v12, a12\n"
"v_mfma_f32_16x16x32_bf16 v[220:223], a[84:87], a[140:143], v[220:223]\n"
"ds_read_b128 a[140:143], v3 offset:11264\n"
"v_accvgpr_read_b32 v13, a13\n"
"v_mfma_f32_16x16x32_bf16 v[220:223], a[80:83], a[136:139], v[220:223]\n"
"ds_read_b128 a[136:139], v3 offset:10240\n"
"v_accvgpr_read_b32 v14, a14\n"
"v_accvgpr_read_b32 v15, a15\n"
"v_mfma_f32_16x16x32_bf16 v[224:227], a[80:83], a[144:147], v[224:227]\n"
"buffer_load_dwordx4 v0, s[48:51], s69 offen offset:0 sc1 lds\n"
"v_add_f32 v12, v12, v20\n"
"v_mfma_f32_16x16x32_bf16 v[224:227], a[84:87], a[148:151], v[224:227]\n"
"global_load_dwordx4 a[76:79], v0, s[34:35] offset:3072 sc1\n"
"v_add_f32 v13, v13, v21\n"
"v_add_f32 v14, v14, v22\n"
"v_mfma_f32_16x16x32_bf16 v[252:255], a[92:95], a[148:151], v[252:255]\n"
"ds_read_b128 a[148:151], v3 offset:13312\n"
"v_add_f32 v15, v15, v23\n"
"v_accvgpr_write_b32 a12, v12\n"
"v_mfma_f32_16x16x32_bf16 v[252:255], a[88:91], a[144:147], v[252:255]\n"
"ds_read_b128 a[144:147], v3 offset:12288\n"
"v_accvgpr_write_b32 a13, v13\n"
"v_mfma_f32_16x16x32_bf16 a[28:31], a[88:91], a[152:155], a[28:31]\n"
"v_accvgpr_write_b32 a14, v14\n"
"v_accvgpr_write_b32 a15, v15\n"
"v_mfma_f32_16x16x32_bf16 a[28:31], a[92:95], a[156:159], a[28:31]\n"
"v_mfma_f32_16x16x32_bf16 a[24:27], a[84:87], a[156:159], a[24:27]\n"
"ds_read_b128 a[156:159], v3 offset:15360\n"
"v_mfma_f32_16x16x32_bf16 a[24:27], a[80:83], a[152:155], a[24:27]\n"
"s_waitcnt vmcnt(13) lgkmcnt(11)\n"
"ds_read_b128 a[152:155], v3 offset:14336\n"
"v_mfma_f32_16x16x32_bf16 v[32:35], a[32:35], a[96:99], v[32:35]\n"
"s_add_u32 s34, s36, 1478656\n"
"v_mfma_f32_16x16x32_bf16 v[32:35], a[36:39], a[100:103], v[32:35]\n"
"global_load_dwordx4 a[80:83], v0, s[34:35] offset:0 sc1\n"
"v_mfma_f32_16x16x32_bf16 v[60:63], a[44:47], a[100:103], v[60:63]\n"
"buffer_load_dwordx4 v0, s[48:51], s69 offen offset:1024 sc1 lds\n"
"v_mfma_f32_16x16x32_bf16 v[60:63], a[40:43], a[96:99], v[60:63]\n"
"v_lshrrev_b32 v16, 4, v0\n"
"v_mfma_f32_16x16x32_bf16 v[64:67], a[40:43], a[104:107], v[64:67]\n"
"v_mfma_f32_16x16x32_bf16 v[64:67], a[44:47], a[108:111], v[64:67]\n"
"v_mfma_f32_16x16x32_bf16 v[36:39], a[36:39], a[108:111], v[36:39]\n"
"v_mfma_f32_16x16x32_bf16 v[36:39], a[32:35], a[104:107], v[36:39]\n"
"s_waitcnt lgkmcnt(8)\n"
"v_and_b32 v16, 0x30, v16\n"
"v_mfma_f32_16x16x32_bf16 v[40:43], a[32:35], a[112:115], v[40:43]\n"
"v_mfma_f32_16x16x32_bf16 v[40:43], a[36:39], a[116:119], v[40:43]\n"
"global_load_dwordx4 a[84:87], v0, s[34:35] offset:1024 sc1\n"
"v_mfma_f32_16x16x32_bf16 v[68:71], a[44:47], a[116:119], v[68:71]\n"
"buffer_load_dwordx2 v[18:19], v16, s[44:47], s61 offen offset:128\n"
"v_mfma_f32_16x16x32_bf16 v[68:71], a[40:43], a[112:115], v[68:71]\n"
"buffer_load_dwordx4 v0, s[48:51], s69 offen offset:2048 sc1 lds\n"
"v_mfma_f32_16x16x32_bf16 v[72:75], a[40:43], a[120:123], v[72:75]\n"
"v_mfma_f32_16x16x32_bf16 v[72:75], a[44:47], a[124:127], v[72:75]\n"
"v_lshrrev_b32 v20, 4, v0\n"
"v_mfma_f32_16x16x32_bf16 v[44:47], a[36:39], a[124:127], v[44:47]\n"
"v_mfma_f32_16x16x32_bf16 v[44:47], a[32:35], a[120:123], v[44:47]\n"
"s_waitcnt lgkmcnt(4)\n"
"v_mfma_f32_16x16x32_bf16 v[48:51], a[32:35], a[128:131], v[48:51]\n"
"v_and_b32 v20, 0x30, v20\n"
"v_mfma_f32_16x16x32_bf16 v[48:51], a[36:39], a[132:135], v[48:51]\n"
"global_load_dwordx4 a[88:91], v0, s[34:35] offset:2048 sc1\n"
"v_mfma_f32_16x16x32_bf16 v[76:79], a[44:47], a[132:135], v[76:79]\n"
"v_mfma_f32_16x16x32_bf16 v[76:79], a[40:43], a[128:131], v[76:79]\n"
"v_mfma_f32_16x16x32_bf16 v[80:83], a[40:43], a[136:139], v[80:83]\n"
"buffer_load_dwordx4 v0, s[48:51], s69 offen offset:3072 sc1 lds\n"
"v_mfma_f32_16x16x32_bf16 v[80:83], a[44:47], a[140:143], v[80:83]\n"
"buffer_load_dwordx2 v[22:23], v20, s[44:47], s61 offen offset:136\n"
"v_mfma_f32_16x16x32_bf16 v[52:55], a[36:39], a[140:143], v[52:55]\n"
"v_mfma_f32_16x16x32_bf16 v[52:55], a[32:35], a[136:139], v[52:55]\n"
"s_waitcnt lgkmcnt(0)\n"
"v_mfma_f32_16x16x32_bf16 v[56:59], a[32:35], a[144:147], v[56:59]\n"
"v_lshrrev_b32 v24, 4, v0\n"
"v_mfma_f32_16x16x32_bf16 v[56:59], a[36:39], a[148:151], v[56:59]\n"
"global_load_dwordx4 a[92:95], v0, s[34:35] offset:3072 sc1\n"
"v_mfma_f32_16x16x32_bf16 v[84:87], a[44:47], a[148:151], v[84:87]\n"
"v_mfma_f32_16x16x32_bf16 v[84:87], a[40:43], a[144:147], v[84:87]\n"
"v_and_b32 v24, 0x30, v24\n"
"v_mfma_f32_16x16x32_bf16 a[4:7], a[40:43], a[152:155], a[4:7]\n"
"v_mfma_f32_16x16x32_bf16 a[4:7], a[44:47], a[156:159], a[4:7]\n"
"v_mfma_f32_16x16x32_bf16 a[0:3], a[36:39], a[156:159], a[0:3]\n"
"s_add_u32 m0, s64, 28672\n"
"v_mfma_f32_16x16x32_bf16 a[0:3], a[32:35], a[152:155], a[0:3]\n"
"s_waitcnt vmcnt(16)\n"
"buffer_load_dwordx2 v[26:27], v24, s[44:47], s61 offen offset:192\n"
"v_mfma_f32_16x16x32_bf16 v[88:91], a[48:51], a[96:99], v[88:91]\n"
"s_add_u32 s34, s36, 1490944\n"
"v_lshrrev_b32 v28, 4, v0\n"
"v_mfma_f32_16x16x32_bf16 v[88:91], a[52:55], a[100:103], v[88:91]\n"
"global_load_dwordx4 a[32:35], v0, s[34:35] offset:0 sc1\n"
"v_mfma_f32_16x16x32_bf16 v[116:119], a[60:63], a[100:103], v[116:119]\n"
"ds_read_b128 a[100:103], v4 offset:1024\n"
"v_mfma_f32_16x16x32_bf16 v[116:119], a[56:59], a[96:99], v[116:119]\n"
"ds_read_b128 a[96:99], v4 offset:0\n"
"v_mfma_f32_16x16x32_bf16 v[120:123], a[56:59], a[104:107], v[120:123]\n"
"v_and_b32 v28, 0x30, v28\n"
"v_mfma_f32_16x16x32_bf16 v[120:123], a[60:63], a[108:111], v[120:123]\n"
"buffer_load_dwordx2 v[30:31], v28, s[44:47], s61 offen offset:200\n"
"v_mfma_f32_16x16x32_bf16 v[92:95], a[52:55], a[108:111], v[92:95]\n"
"ds_read_b128 a[108:111], v4 offset:3072\n"
"v_mfma_f32_16x16x32_bf16 v[92:95], a[48:51], a[104:107], v[92:95]\n"
"ds_read_b128 a[104:107], v4 offset:2048\n"
"v_mfma_f32_16x16x32_bf16 v[96:99], a[48:51], a[112:115], v[96:99]\n"
"buffer_load_dwordx4 v1, s[48:51], s69 offen offset:0 sc1 lds\n"
"v_mfma_f32_16x16x32_bf16 v[96:99], a[52:55], a[116:119], v[96:99]\n"
"global_load_dwordx4 a[36:39], v0, s[34:35] offset:1024 sc1\n"
"v_mfma_f32_16x16x32_bf16 v[124:127], a[60:63], a[116:119], v[124:127]\n"
"ds_read_b128 a[116:119], v4 offset:5120\n"
"v_mfma_f32_16x16x32_bf16 v[124:127], a[56:59], a[112:115], v[124:127]\n"
"ds_read_b128 a[112:115], v4 offset:4096\n"
"v_mfma_f32_16x16x32_bf16 v[128:131], a[56:59], a[120:123], v[128:131]\n"
"v_mfma_f32_16x16x32_bf16 v[128:131], a[60:63], a[124:127], v[128:131]\n"
"v_mfma_f32_16x16x32_bf16 v[100:103], a[52:55], a[124:127], v[100:103]\n"
"ds_read_b128 a[124:127], v4 offset:7168\n"
"v_mfma_f32_16x16x32_bf16 v[100:103], a[48:51], a[120:123], v[100:103]\n"
"ds_read_b128 a[120:123], v4 offset:6144\n"
"v_mfma_f32_16x16x32_bf16 v[104:107], a[48:51], a[128:131], v[104:107]\n"
"buffer_load_dwordx4 v1, s[48:51], s69 offen offset:1024 sc1 lds\n"
"v_mfma_f32_16x16x32_bf16 v[104:107], a[52:55], a[132:135], v[104:107]\n"
"global_load_dwordx4 a[40:43], v0, s[34:35] offset:2048 sc1\n"
"v_mfma_f32_16x16x32_bf16 v[132:135], a[60:63], a[132:135], v[132:135]\n"
"ds_read_b128 a[132:135], v4 offset:9216\n"
"v_mfma_f32_16x16x32_bf16 v[132:135], a[56:59], a[128:131], v[132:135]\n"
"ds_read_b128 a[128:131], v4 offset:8192\n"
"v_mfma_f32_16x16x32_bf16 v[136:139], a[56:59], a[136:139], v[136:139]\n"
"v_mfma_f32_16x16x32_bf16 v[136:139], a[60:63], a[140:143], v[136:139]\n"
"v_mfma_f32_16x16x32_bf16 v[108:111], a[52:55], a[140:143], v[108:111]\n"
"ds_read_b128 a[140:143], v4 offset:11264\n"
"v_mfma_f32_16x16x32_bf16 v[108:111], a[48:51], a[136:139], v[108:111]\n"
"ds_read_b128 a[136:139], v4 offset:10240\n"
"v_mfma_f32_16x16x32_bf16 v[112:115], a[48:51], a[144:147], v[112:115]\n"
"v_mfma_f32_16x16x32_bf16 v[112:115], a[52:55], a[148:151], v[112:115]\n"
"global_load_dwordx4 a[44:47], v0, s[34:35] offset:3072 sc1\n"
"v_mfma_f32_16x16x32_bf16 v[140:143], a[60:63], a[148:151], v[140:143]\n"
"ds_read_b128 a[148:151], v4 offset:13312\n"
"v_mfma_f32_16x16x32_bf16 v[140:143], a[56:59], a[144:147], v[140:143]\n"
"ds_read_b128 a[144:147], v4 offset:12288\n"
"v_mfma_f32_16x16x32_bf16 a[12:15], a[56:59], a[152:155], a[12:15]\n"
"buffer_load_dwordx4 v1, s[48:51], s69 offen offset:2048 sc1 lds\n"
"v_mfma_f32_16x16x32_bf16 a[12:15], a[60:63], a[156:159], a[12:15]\n"
"v_mfma_f32_16x16x32_bf16 a[8:11], a[52:55], a[156:159], a[8:11]\n"
"ds_read_b128 a[156:159], v4 offset:15360\n"
"v_mfma_f32_16x16x32_bf16 a[8:11], a[48:51], a[152:155], a[8:11]\n"
"s_waitcnt vmcnt(18) lgkmcnt(11)\n"
"ds_read_b128 a[152:155], v4 offset:14336\n"
"v_mfma_f32_16x16x32_bf16 v[32:35], a[64:67], a[96:99], v[32:35]\n"
"s_add_u32 s34, s36, 1495040\n"
"v_mfma_f32_16x16x32_bf16 v[32:35], a[68:71], a[100:103], v[32:35]\n"
"global_load_dwordx4 a[48:51], v0, s[34:35] offset:0 sc1\n"
"v_mfma_f32_16x16x32_bf16 v[60:63], a[76:79], a[100:103], v[60:63]\n"
"v_mfma_f32_16x16x32_bf16 v[60:63], a[72:75], a[96:99], v[60:63]\n"
"v_mfma_f32_16x16x32_bf16 v[64:67], a[72:75], a[104:107], v[64:67]\n"
"buffer_load_dwordx4 v1, s[48:51], s69 offen offset:3072 sc1 lds\n"
"v_mfma_f32_16x16x32_bf16 v[64:67], a[76:79], a[108:111], v[64:67]\n"
"v_mfma_f32_16x16x32_bf16 v[36:39], a[68:71], a[108:111], v[36:39]\n"
"v_mfma_f32_16x16x32_bf16 v[36:39], a[64:67], a[104:107], v[36:39]\n"
"s_waitcnt lgkmcnt(8)\n"
"v_mfma_f32_16x16x32_bf16 v[40:43], a[64:67], a[112:115], v[40:43]\n"
"v_mfma_f32_16x16x32_bf16 v[40:43], a[68:71], a[116:119], v[40:43]\n"
"global_load_dwordx4 a[52:55], v0, s[34:35] offset:1024 sc1\n"
"v_mfma_f32_16x16x32_bf16 v[68:71], a[76:79], a[116:119], v[68:71]\n"
"v_mfma_f32_16x16x32_bf16 v[68:71], a[72:75], a[112:115], v[68:71]\n"
"v_mfma_f32_16x16x32_bf16 v[72:75], a[72:75], a[120:123], v[72:75]\n"
"v_mfma_f32_16x16x32_bf16 v[72:75], a[76:79], a[124:127], v[72:75]\n"
"v_mfma_f32_16x16x32_bf16 v[44:47], a[68:71], a[124:127], v[44:47]\n"
"v_mfma_f32_16x16x32_bf16 v[44:47], a[64:67], a[120:123], v[44:47]\n"
"s_waitcnt lgkmcnt(4)\n"
"v_mfma_f32_16x16x32_bf16 v[48:51], a[64:67], a[128:131], v[48:51]\n"
"v_mfma_f32_16x16x32_bf16 v[48:51], a[68:71], a[132:135], v[48:51]\n"
"global_load_dwordx4 a[56:59], v0, s[34:35] offset:2048 sc1\n"
"v_mfma_f32_16x16x32_bf16 v[76:79], a[76:79], a[132:135], v[76:79]\n"
"v_mfma_f32_16x16x32_bf16 v[76:79], a[72:75], a[128:131], v[76:79]\n"
"v_mfma_f32_16x16x32_bf16 v[80:83], a[72:75], a[136:139], v[80:83]\n"
"v_mfma_f32_16x16x32_bf16 v[80:83], a[76:79], a[140:143], v[80:83]\n"
"v_mfma_f32_16x16x32_bf16 v[52:55], a[68:71], a[140:143], v[52:55]\n"
"v_mfma_f32_16x16x32_bf16 v[52:55], a[64:67], a[136:139], v[52:55]\n"
"s_waitcnt lgkmcnt(0)\n"
"v_mfma_f32_16x16x32_bf16 v[56:59], a[64:67], a[144:147], v[56:59]\n"
"v_mfma_f32_16x16x32_bf16 v[56:59], a[68:71], a[148:151], v[56:59]\n"
"global_load_dwordx4 a[60:63], v0, s[34:35] offset:3072 sc1\n"
"v_mfma_f32_16x16x32_bf16 v[84:87], a[76:79], a[148:151], v[84:87]\n"
"v_mfma_f32_16x16x32_bf16 v[84:87], a[72:75], a[144:147], v[84:87]\n"
"v_mfma_f32_16x16x32_bf16 a[4:7], a[72:75], a[152:155], a[4:7]\n"
"v_mfma_f32_16x16x32_bf16 a[4:7], a[76:79], a[156:159], a[4:7]\n"
"v_mfma_f32_16x16x32_bf16 a[0:3], a[68:71], a[156:159], a[0:3]\n"
"v_mfma_f32_16x16x32_bf16 a[0:3], a[64:67], a[152:155], a[0:3]\n"
"s_waitcnt vmcnt(14)\n"
"v_mfma_f32_16x16x32_bf16 v[88:91], a[80:83], a[96:99], v[88:91]\n"
"s_add_u32 s34, s36, 1507328\n"
"v_mfma_f32_16x16x32_bf16 v[88:91], a[84:87], a[100:103], v[88:91]\n"
"global_load_dwordx4 a[64:67], v0, s[34:35] offset:0 sc1\n"
"v_mfma_f32_16x16x32_bf16 v[116:119], a[92:95], a[100:103], v[116:119]\n"
"ds_read_b128 a[100:103], v5 offset:1024\n"
"v_mfma_f32_16x16x32_bf16 v[116:119], a[88:91], a[96:99], v[116:119]\n"
"ds_read_b128 a[96:99], v5 offset:0\n"
"v_mfma_f32_16x16x32_bf16 v[120:123], a[88:91], a[104:107], v[120:123]\n"
"v_mfma_f32_16x16x32_bf16 v[120:123], a[92:95], a[108:111], v[120:123]\n"
"v_mfma_f32_16x16x32_bf16 v[92:95], a[84:87], a[108:111], v[92:95]\n"
"ds_read_b128 a[108:111], v5 offset:3072\n"
"v_mfma_f32_16x16x32_bf16 v[92:95], a[80:83], a[104:107], v[92:95]\n"
"ds_read_b128 a[104:107], v5 offset:2048\n"
"v_mfma_f32_16x16x32_bf16 v[96:99], a[80:83], a[112:115], v[96:99]\n"
"v_mfma_f32_16x16x32_bf16 v[96:99], a[84:87], a[116:119], v[96:99]\n"
"global_load_dwordx4 a[68:71], v0, s[34:35] offset:1024 sc1\n"
"v_mfma_f32_16x16x32_bf16 v[124:127], a[92:95], a[116:119], v[124:127]\n"
"ds_read_b128 a[116:119], v5 offset:5120\n"
"v_mfma_f32_16x16x32_bf16 v[124:127], a[88:91], a[112:115], v[124:127]\n"
"ds_read_b128 a[112:115], v5 offset:4096\n"
"v_mfma_f32_16x16x32_bf16 v[128:131], a[88:91], a[120:123], v[128:131]\n"
"v_mfma_f32_16x16x32_bf16 v[128:131], a[92:95], a[124:127], v[128:131]\n"
"v_mfma_f32_16x16x32_bf16 v[100:103], a[84:87], a[124:127], v[100:103]\n"
"ds_read_b128 a[124:127], v5 offset:7168\n"
"v_mfma_f32_16x16x32_bf16 v[100:103], a[80:83], a[120:123], v[100:103]\n"
"ds_read_b128 a[120:123], v5 offset:6144\n"
"v_mfma_f32_16x16x32_bf16 v[104:107], a[80:83], a[128:131], v[104:107]\n"
"v_mfma_f32_16x16x32_bf16 v[104:107], a[84:87], a[132:135], v[104:107]\n"
"global_load_dwordx4 a[72:75], v0, s[34:35] offset:2048 sc1\n"
"v_mfma_f32_16x16x32_bf16 v[132:135], a[92:95], a[132:135], v[132:135]\n"
"ds_read_b128 a[132:135], v5 offset:9216\n"
"v_mfma_f32_16x16x32_bf16 v[132:135], a[88:91], a[128:131], v[132:135]\n"
"ds_read_b128 a[128:131], v5 offset:8192\n"
"v_mfma_f32_16x16x32_bf16 v[136:139], a[88:91], a[136:139], v[136:139]\n"
"v_mfma_f32_16x16x32_bf16 v[136:139], a[92:95], a[140:143], v[136:139]\n"
"v_mfma_f32_16x16x32_bf16 v[108:111], a[84:87], a[140:143], v[108:111]\n"
"ds_read_b128 a[140:143], v5 offset:11264\n"
"v_mfma_f32_16x16x32_bf16 v[108:111], a[80:83], a[136:139], v[108:111]\n"
"ds_read_b128 a[136:139], v5 offset:10240\n"
"v_mfma_f32_16x16x32_bf16 v[112:115], a[80:83], a[144:147], v[112:115]\n"
"v_mfma_f32_16x16x32_bf16 v[112:115], a[84:87], a[148:151], v[112:115]\n"
"global_load_dwordx4 a[76:79], v0, s[34:35] offset:3072 sc1\n"
"v_mfma_f32_16x16x32_bf16 v[140:143], a[92:95], a[148:151], v[140:143]\n"
"ds_read_b128 a[148:151], v5 offset:13312\n"
"v_mfma_f32_16x16x32_bf16 v[140:143], a[88:91], a[144:147], v[140:143]\n"
"ds_read_b128 a[144:147], v5 offset:12288\n"
"v_mfma_f32_16x16x32_bf16 a[12:15], a[88:91], a[152:155], a[12:15]\n"
"v_mfma_f32_16x16x32_bf16 a[12:15], a[92:95], a[156:159], a[12:15]\n"
"v_mfma_f32_16x16x32_bf16 a[8:11], a[84:87], a[156:159], a[8:11]\n"
"ds_read_b128 a[156:159], v5 offset:15360\n"
"v_mfma_f32_16x16x32_bf16 a[8:11], a[80:83], a[152:155], a[8:11]\n"
"s_waitcnt vmcnt(10) lgkmcnt(11)\n"
"ds_read_b128 a[152:155], v5 offset:14336\n"
"v_mfma_f32_16x16x32_bf16 v[32:35], a[32:35], a[96:99], v[32:35]\n"
"s_add_u32 s34, s36, 1511424\n"
"v_mfma_f32_16x16x32_bf16 v[32:35], a[36:39], a[100:103], v[32:35]\n"
"global_load_dwordx4 a[80:83], v0, s[34:35] offset:0 sc1\n"
"v_mfma_f32_16x16x32_bf16 v[60:63], a[44:47], a[100:103], v[60:63]\n"
"v_mfma_f32_16x16x32_bf16 v[60:63], a[40:43], a[96:99], v[60:63]\n"
"v_mfma_f32_16x16x32_bf16 v[64:67], a[40:43], a[104:107], v[64:67]\n"
"L_st9674:\n"
"s_cbranch_execz L_retC\n"
"v_mfma_f32_16x16x32_bf16 v[64:67], a[44:47], a[108:111], v[64:67]\n"
"v_mfma_f32_16x16x32_bf16 v[36:39], a[36:39], a[108:111], v[36:39]\n"
"v_mfma_f32_16x16x32_bf16 v[36:39], a[32:35], a[104:107], v[36:39]\n"
"s_waitcnt lgkmcnt(8)\n"
"v_mfma_f32_16x16x32_bf16 v[40:43], a[32:35], a[112:115], v[40:43]\n"
"v_mfma_f32_16x16x32_bf16 v[40:43], a[36:39], a[116:119], v[40:43]\n"
"global_load_dwordx4 a[84:87], v0, s[34:35] offset:1024 sc1\n"
"v_mfma_f32_16x16x32_bf16 v[68:71], a[44:47], a[116:119], v[68:71]\n"
"v_mfma_f32_16x16x32_bf16 v[68:71], a[40:43], a[112:115], v[68:71]\n"
"v_mfma_f32_16x16x32_bf16 v[72:75], a[40:43], a[120:123], v[72:75]\n"
"v_mfma_f32_16x16x32_bf16 v[72:75], a[44:47], a[124:127], v[72:75]\n"
"v_mfma_f32_16x16x32_bf16 v[44:47], a[36:39], a[124:127], v[44:47]\n"
"v_mfma_f32_16x16x32_bf16 v[44:47], a[32:35], a[120:123], v[44:47]\n"
"s_waitcnt lgkmcnt(4)\n"
"v_mfma_f32_16x16x32_bf16 v[48:51], a[32:35], a[128:131], v[48:51]\n"
"v_mfma_f32_16x16x32_bf16 v[48:51], a[36:39], a[132:135], v[48:51]\n"
"global_load_dwordx4 a[88:91], v0, s[34:35] offset:2048 sc1\n"
"v_mfma_f32_16x16x32_bf16 v[76:79], a[44:47], a[132:135], v[76:79]\n"
"v_mfma_f32_16x16x32_bf16 v[76:79], a[40:43], a[128:131], v[76:79]\n"
"v_mfma_f32_16x16x32_bf16 v[80:83], a[40:43], a[136:139], v[80:83]\n"
"v_mfma_f32_16x16x32_bf16 v[80:83], a[44:47], a[140:143], v[80:83]\n"
"v_mfma_f32_16x16x32_bf16 v[52:55], a[36:39], a[140:143], v[52:55]\n"
"v_mfma_f32_16x16x32_bf16 v[52:55], a[32:35], a[136:139], v[52:55]\n"
"s_waitcnt lgkmcnt(0)\n"
"v_mfma_f32_16x16x32_bf16 v[56:59], a[32:35], a[144:147], v[56:59]\n"
"v_mfma_f32_16x16x32_bf16 v[56:59], a[36:39], a[148:151], v[56:59]\n"
"global_load_dwordx4 a[92:95], v0, s[34:35] offset:3072 sc1\n"
"v_mfma_f32_16x16x32_bf16 v[84:87], a[44:47], a[148:151], v[84:87]\n"
"v_mfma_f32_16x16x32_bf16 v[84:87], a[40:43], a[144:147], v[84:87]\n"
"v_mfma_f32_16x16x32_bf16 a[4:7], a[40:43], a[152:155], a[4:7]\n"
"v_mfma_f32_16x16x32_bf16 a[4:7], a[44:47], a[156:159], a[4:7]\n"
"v_mfma_f32_16x16x32_bf16 a[0:3], a[36:39], a[156:159], a[0:3]\n"
"v_mfma_f32_16x16x32_bf16 a[0:3], a[32:35], a[152:155], a[0:3]\n"
"s_waitcnt vmcnt(8)\n"
"v_mfma_f32_16x16x32_bf16 v[88:91], a[48:51], a[96:99], v[88:91]\n"
"s_add_u32 s34, s36, 1515520\n"
"v_mfma_f32_16x16x32_bf16 v[88:91], a[52:55], a[100:103], v[88:91]\n"
"global_load_dwordx4 a[32:35], v0, s[34:35] offset:0 sc1\n"
"v_mfma_f32_16x16x32_bf16 v[116:119], a[60:63], a[100:103], v[116:119]\n"
"ds_read_b128 a[100:103], v2 offset:17408\n"
"v_mfma_f32_16x16x32_bf16 v[116:119], a[56:59], a[96:99], v[116:119]\n"
"ds_read_b128 a[96:99], v2 offset:16384\n"
"v_mfma_f32_16x16x32_bf16 v[120:123], a[56:59], a[104:107], v[120:123]\n"
"v_mfma_f32_16x16x32_bf16 v[120:123], a[60:63], a[108:111], v[120:123]\n"
"v_mfma_f32_16x16x32_bf16 v[92:95], a[52:55], a[108:111], v[92:95]\n"
"ds_read_b128 a[108:111], v2 offset:19456\n"
"v_mfma_f32_16x16x32_bf16 v[92:95], a[48:51], a[104:107], v[92:95]\n"
"ds_read_b128 a[104:107], v2 offset:18432\n"
"v_mfma_f32_16x16x32_bf16 v[96:99], a[48:51], a[112:115], v[96:99]\n"
"v_mfma_f32_16x16x32_bf16 v[96:99], a[52:55], a[116:119], v[96:99]\n"
"global_load_dwordx4 a[36:39], v0, s[34:35] offset:1024 sc1\n"
"v_mfma_f32_16x16x32_bf16 v[124:127], a[60:63], a[116:119], v[124:127]\n"
"ds_read_b128 a[116:119], v2 offset:21504\n"
"v_mfma_f32_16x16x32_bf16 v[124:127], a[56:59], a[112:115], v[124:127]\n"
"ds_read_b128 a[112:115], v2 offset:20480\n"
"v_mfma_f32_16x16x32_bf16 v[128:131], a[56:59], a[120:123], v[128:131]\n"
"v_mfma_f32_16x16x32_bf16 v[128:131], a[60:63], a[124:127], v[128:131]\n"
"v_mfma_f32_16x16x32_bf16 v[100:103], a[52:55], a[124:127], v[100:103]\n"
"ds_read_b128 a[124:127], v2 offset:23552\n"
"v_mfma_f32_16x16x32_bf16 v[100:103], a[48:51], a[120:123], v[100:103]\n"
"ds_read_b128 a[120:123], v2 offset:22528\n"
"v_mfma_f32_16x16x32_bf16 v[104:107], a[48:51], a[128:131], v[104:107]\n"
"v_mfma_f32_16x16x32_bf16 v[104:107], a[52:55], a[132:135], v[104:107]\n"
"global_load_dwordx4 a[40:43], v0, s[34:35] offset:2048 sc1\n"
"v_mfma_f32_16x16x32_bf16 v[132:135], a[60:63], a[132:135], v[132:135]\n"
"ds_read_b128 a[132:135], v2 offset:25600\n"
"v_mfma_f32_16x16x32_bf16 v[132:135], a[56:59], a[128:131], v[132:135]\n"
"ds_read_b128 a[128:131], v2 offset:24576\n"
"v_mfma_f32_16x16x32_bf16 v[136:139], a[56:59], a[136:139], v[136:139]\n"
"v_mfma_f32_16x16x32_bf16 v[136:139], a[60:63], a[140:143], v[136:139]\n"
"v_mfma_f32_16x16x32_bf16 v[108:111], a[52:55], a[140:143], v[108:111]\n"
"ds_read_b128 a[140:143], v2 offset:27648\n"
"v_mfma_f32_16x16x32_bf16 v[108:111], a[48:51], a[136:139], v[108:111]\n"
"ds_read_b128 a[136:139], v2 offset:26624\n"
"v_mfma_f32_16x16x32_bf16 v[112:115], a[48:51], a[144:147], v[112:115]\n"
"v_mfma_f32_16x16x32_bf16 v[112:115], a[52:55], a[148:151], v[112:115]\n"
"global_load_dwordx4 a[44:47], v0, s[34:35] offset:3072 sc1\n"
"v_mfma_f32_16x16x32_bf16 v[140:143], a[60:63], a[148:151], v[140:143]\n"
"ds_read_b128 a[148:151], v2 offset:29696\n"
"v_mfma_f32_16x16x32_bf16 v[140:143], a[56:59], a[144:147], v[140:143]\n"
"ds_read_b128 a[144:147], v2 offset:28672\n"
"v_mfma_f32_16x16x32_bf16 a[12:15], a[56:59], a[152:155], a[12:15]\n"
"v_mfma_f32_16x16x32_bf16 a[12:15], a[60:63], a[156:159], a[12:15]\n"
"v_mfma_f32_16x16x32_bf16 a[8:11], a[52:55], a[156:159], a[8:11]\n"
"ds_read_b128 a[156:159], v2 offset:31744\n"
"v_mfma_f32_16x16x32_bf16 a[8:11], a[48:51], a[152:155], a[8:11]\n"
"s_waitcnt vmcnt(8) lgkmcnt(11)\n"
"ds_read_b128 a[152:155], v2 offset:30720\n"
"v_mfma_f32_16x16x32_bf16 v[32:35], a[64:67], a[96:99], v[32:35]\n"
"s_add_u32 s34, s36, 1519616\n"
"v_mfma_f32_16x16x32_bf16 v[32:35], a[68:71], a[100:103], v[32:35]\n"
"global_load_dwordx4 a[48:51], v0, s[34:35] offset:0 sc1\n"
"v_mfma_f32_16x16x32_bf16 v[60:63], a[76:79], a[100:103], v[60:63]\n"
"s_barrier\n"
"v_mfma_f32_16x16x32_bf16 v[60:63], a[72:75], a[96:99], v[60:63]\n"
"v_mfma_f32_16x16x32_bf16 v[64:67], a[72:75], a[104:107], v[64:67]\n"
"v_mfma_f32_16x16x32_bf16 v[64:67], a[76:79], a[108:111], v[64:67]\n"
"v_mfma_f32_16x16x32_bf16 v[36:39], a[68:71], a[108:111], v[36:39]\n"
"v_mfma_f32_16x16x32_bf16 v[36:39], a[64:67], a[104:107], v[36:39]\n"
"s_waitcnt lgkmcnt(8)\n"
"v_mfma_f32_16x16x32_bf16 v[40:43], a[64:67], a[112:115], v[40:43]\n"
"v_mfma_f32_16x16x32_bf16 v[40:43], a[68:71], a[116:119], v[40:43]\n"
"global_load_dwordx4 a[52:55], v0, s[34:35] offset:1024 sc1\n"
"v_mfma_f32_16x16x32_bf16 v[68:71], a[76:79], a[116:119], v[68:71]\n"
"v_mfma_f32_16x16x32_bf16 v[68:71], a[72:75], a[112:115], v[68:71]\n"
"v_mfma_f32_16x16x32_bf16 v[72:75], a[72:75], a[120:123], v[72:75]\n"
"v_mfma_f32_16x16x32_bf16 v[72:75], a[76:79], a[124:127], v[72:75]\n"
"v_mfma_f32_16x16x32_bf16 v[44:47], a[68:71], a[124:127], v[44:47]\n"
"v_mfma_f32_16x16x32_bf16 v[44:47], a[64:67], a[120:123], v[44:47]\n"
"s_waitcnt lgkmcnt(4)\n"
"v_mfma_f32_16x16x32_bf16 v[48:51], a[64:67], a[128:131], v[48:51]\n"
"v_mfma_f32_16x16x32_bf16 v[48:51], a[68:71], a[132:135], v[48:51]\n"
"global_load_dwordx4 a[56:59], v0, s[34:35] offset:2048 sc1\n"
"v_mfma_f32_16x16x32_bf16 v[76:79], a[76:79], a[132:135], v[76:79]\n"
"v_mfma_f32_16x16x32_bf16 v[76:79], a[72:75], a[128:131], v[76:79]\n"
"v_mfma_f32_16x16x32_bf16 v[80:83], a[72:75], a[136:139], v[80:83]\n"
"v_lshlrev_b32 v16, 16, v18\n"
"v_mfma_f32_16x16x32_bf16 v[80:83], a[76:79], a[140:143], v[80:83]\n"
"v_and_b32 v17, s63, v18\n"
"v_mfma_f32_16x16x32_bf16 v[52:55], a[68:71], a[140:143], v[52:55]\n"
"v_lshlrev_b32 v18, 16, v19\n"
"v_mfma_f32_16x16x32_bf16 v[52:55], a[64:67], a[136:139], v[52:55]\n"
"s_waitcnt lgkmcnt(0)\n"
"v_and_b32 v19, s63, v19\n"
"v_add_f32 v144, v144, v16\n"
"v_mfma_f32_16x16x32_bf16 v[56:59], a[64:67], a[144:147], v[56:59]\n"
"v_add_f32 v145, v145, v17\n"
"v_mfma_f32_16x16x32_bf16 v[56:59], a[68:71], a[148:151], v[56:59]\n"
"global_load_dwordx4 a[60:63], v0, s[34:35] offset:3072 sc1\n"
"v_add_f32 v146, v146, v18\n"
"v_mfma_f32_16x16x32_bf16 v[84:87], a[76:79], a[148:151], v[84:87]\n"
"v_add_f32 v147, v147, v19\n"
"v_mfma_f32_16x16x32_bf16 v[84:87], a[72:75], a[144:147], v[84:87]\n"
"v_add_f32 v148, v148, v16\n"
"v_mfma_f32_16x16x32_bf16 a[4:7], a[72:75], a[152:155], a[4:7]\n"
"v_add_f32 v149, v149, v17\n"
"v_mfma_f32_16x16x32_bf16 a[4:7], a[76:79], a[156:159], a[4:7]\n"
"v_add_f32 v150, v150, v18\n"
"v_mfma_f32_16x16x32_bf16 a[0:3], a[68:71], a[156:159], a[0:3]\n"
"v_add_f32 v151, v151, v19\n"
"v_mfma_f32_16x16x32_bf16 a[0:3], a[64:67], a[152:155], a[0:3]\n"
"s_waitcnt vmcnt(8)\n"
"v_add_f32 v152, v152, v16\n"
"v_add_f32 v153, v153, v17\n"
"v_mfma_f32_16x16x32_bf16 v[88:91], a[80:83], a[96:99], v[88:91]\n"
"s_add_u32 s34, s36, 1523712\n"
"v_add_f32 v154, v154, v18\n"
"v_mfma_f32_16x16x32_bf16 v[88:91], a[84:87], a[100:103], v[88:91]\n"
"global_load_dwordx4 a[64:67], v0, s[34:35] offset:0 sc1\n"
"v_add_f32 v155, v155, v19\n"
"v_mfma_f32_16x16x32_bf16 v[116:119], a[92:95], a[100:103], v[116:119]\n"
"ds_read_b128 a[100:103], v3 offset:17408\n"
"v_add_f32 v156, v156, v16\n"
"v_mfma_f32_16x16x32_bf16 v[116:119], a[88:91], a[96:99], v[116:119]\n"
"ds_read_b128 a[96:99], v3 offset:16384\n"
"v_add_f32 v157, v157, v17\n"
"v_mfma_f32_16x16x32_bf16 v[120:123], a[88:91], a[104:107], v[120:123]\n"
"v_add_f32 v158, v158, v18\n"
"v_mfma_f32_16x16x32_bf16 v[120:123], a[92:95], a[108:111], v[120:123]\n"
"v_add_f32 v159, v159, v19\n"
"v_add_f32 v160, v160, v16\n"
"v_mfma_f32_16x16x32_bf16 v[92:95], a[84:87], a[108:111], v[92:95]\n"
"ds_read_b128 a[108:111], v3 offset:19456\n"
"v_add_f32 v161, v161, v17\n"
"v_mfma_f32_16x16x32_bf16 v[92:95], a[80:83], a[104:107], v[92:95]\n"
"ds_read_b128 a[104:107], v3 offset:18432\n"
"v_add_f32 v162, v162, v18\n"
"v_mfma_f32_16x16x32_bf16 v[96:99], a[80:83], a[112:115], v[96:99]\n"
"v_add_f32 v163, v163, v19\n"
"v_mfma_f32_16x16x32_bf16 v[96:99], a[84:87], a[116:119], v[96:99]\n"
"global_load_dwordx4 a[68:71], v0, s[34:35] offset:1024 sc1\n"
"v_add_f32 v164, v164, v16\n"
"v_mfma_f32_16x16x32_bf16 v[124:127], a[92:95], a[116:119], v[124:127]\n"
"ds_read_b128 a[116:119], v3 offset:21504\n"
"v_add_f32 v165, v165, v17\n"
"v_mfma_f32_16x16x32_bf16 v[124:127], a[88:91], a[112:115], v[124:127]\n"
"ds_read_b128 a[112:115], v3 offset:20480\n"
"v_add_f32 v166, v166, v18\n"
"v_mfma_f32_16x16x32_bf16 v[128:131], a[88:91], a[120:123], v[128:131]\n"
"v_add_f32 v167, v167, v19\n"
"v_mfma_f32_16x16x32_bf16 v[128:131], a[92:95], a[124:127], v[128:131]\n"
"v_add_f32 v168, v168, v16\n"
"v_add_f32 v169, v169, v17\n"
"v_mfma_f32_16x16x32_bf16 v[100:103], a[84:87], a[124:127], v[100:103]\n"
"ds_read_b128 a[124:127], v3 offset:23552\n"
"v_add_f32 v170, v170, v18\n"
"v_mfma_f32_16x16x32_bf16 v[100:103], a[80:83], a[120:123], v[100:103]\n"
"ds_read_b128 a[120:123], v3 offset:22528\n"
"v_add_f32 v171, v171, v19\n"
"v_mfma_f32_16x16x32_bf16 v[104:107], a[80:83], a[128:131], v[104:107]\n"
"v_accvgpr_read_b32 v12, a16\n"
"v_mfma_f32_16x16x32_bf16 v[104:107], a[84:87], a[132:135], v[104:107]\n"
"global_load_dwordx4 a[72:75], v0, s[34:35] offset:2048 sc1\n"
"v_accvgpr_read_b32 v13, a17\n"
"v_mfma_f32_16x16x32_bf16 v[132:135], a[92:95], a[132:135], v[132:135]\n"
"ds_read_b128 a[132:135], v3 offset:25600\n"
"v_accvgpr_read_b32 v14, a18\n"
"v_mfma_f32_16x16x32_bf16 v[132:135], a[88:91], a[128:131], v[132:135]\n"
"ds_read_b128 a[128:131], v3 offset:24576\n"
"v_accvgpr_read_b32 v15, a19\n"
"v_mfma_f32_16x16x32_bf16 v[136:139], a[88:91], a[136:139], v[136:139]\n"
"v_add_f32 v12, v12, v16\n"
"v_mfma_f32_16x16x32_bf16 v[136:139], a[92:95], a[140:143], v[136:139]\n"
"v_add_f32 v13, v13, v17\n"
"v_add_f32 v14, v14, v18\n"
"v_mfma_f32_16x16x32_bf16 v[108:111], a[84:87], a[140:143], v[108:111]\n"
"ds_read_b128 a[140:143], v3 offset:27648\n"
"v_add_f32 v15, v15, v19\n"
"v_mfma_f32_16x16x32_bf16 v[108:111], a[80:83], a[136:139], v[108:111]\n"
"ds_read_b128 a[136:139], v3 offset:26624\n"
"v_accvgpr_write_b32 a16, v12\n"
"v_mfma_f32_16x16x32_bf16 v[112:115], a[80:83], a[144:147], v[112:115]\n"
"v_accvgpr_write_b32 a17, v13\n"
"v_mfma_f32_16x16x32_bf16 v[112:115], a[84:87], a[148:151], v[112:115]\n"
"global_load_dwordx4 a[76:79], v0, s[34:35] offset:3072 sc1\n"
"v_accvgpr_write_b32 a18, v14\n"
"v_mfma_f32_16x16x32_bf16 v[140:143], a[92:95], a[148:151], v[140:143]\n"
"ds_read_b128 a[148:151], v3 offset:29696\n"
"v_accvgpr_write_b32 a19, v15\n"
"v_mfma_f32_16x16x32_bf16 v[140:143], a[88:91], a[144:147], v[140:143]\n"
"ds_read_b128 a[144:147], v3 offset:28672\n"
"v_lshlrev_b32 v20, 16, v22\n"
"v_mfma_f32_16x16x32_bf16 a[12:15], a[88:91], a[152:155], a[12:15]\n"
"v_and_b32 v21, s63, v22\n"
"v_lshlrev_b32 v22, 16, v23\n"
"v_mfma_f32_16x16x32_bf16 a[12:15], a[92:95], a[156:159], a[12:15]\n"
"v_and_b32 v23, s63, v23\n"
"v_mfma_f32_16x16x32_bf16 a[8:11], a[84:87], a[156:159], a[8:11]\n"
"ds_read_b128 a[156:159], v3 offset:31744\n"
"v_add_f32 v172, v172, v20\n"
"v_mfma_f32_16x16x32_bf16 a[8:11], a[80:83], a[152:155], a[8:11]\n"
"s_waitcnt vmcnt(8) lgkmcnt(11)\n"
"ds_read_b128 a[152:155], v3 offset:30720\n"
"v_add_f32 v173, v173, v21\n"
"v_mfma_f32_16x16x32_bf16 v[32:35], a[32:35], a[96:99], v[32:35]\n"
"s_add_u32 s34, s36, 1527808\n"
"v_add_f32 v174, v174, v22\n"
"v_mfma_f32_16x16x32_bf16 v[32:35], a[36:39], a[100:103], v[32:35]\n"
"global_load_dwordx4 a[80:83], v0, s[34:35] offset:0 sc1\n"
"v_add_f32 v175, v175, v23\n"
"v_mfma_f32_16x16x32_bf16 v[60:63], a[44:47], a[100:103], v[60:63]\n"
"v_add_f32 v176, v176, v20\n"
"v_mfma_f32_16x16x32_bf16 v[60:63], a[40:43], a[96:99], v[60:63]\n"
"v_add_f32 v177, v177, v21\n"
"v_add_f32 v178, v178, v22\n"
"v_mfma_f32_16x16x32_bf16 v[64:67], a[40:43], a[104:107], v[64:67]\n"
"v_add_f32 v179, v179, v23\n"
"v_mfma_f32_16x16x32_bf16 v[64:67], a[44:47], a[108:111], v[64:67]\n"
"v_add_f32 v180, v180, v20\n"
"v_mfma_f32_16x16x32_bf16 v[36:39], a[36:39], a[108:111], v[36:39]\n"
"v_add_f32 v181, v181, v21\n"
"v_mfma_f32_16x16x32_bf16 v[36:39], a[32:35], a[104:107], v[36:39]\n"
"s_waitcnt lgkmcnt(8)\n"
"v_add_f32 v182, v182, v22\n"
"v_mfma_f32_16x16x32_bf16 v[40:43], a[32:35], a[112:115], v[40:43]\n"
"v_add_f32 v183, v183, v23\n"
"v_mfma_f32_16x16x32_bf16 v[40:43], a[36:39], a[116:119], v[40:43]\n"
"global_load_dwordx4 a[84:87], v0, s[34:35] offset:1024 sc1\n"
"v_add_f32 v184, v184, v20\n"
"v_mfma_f32_16x16x32_bf16 v[68:71], a[44:47], a[116:119], v[68:71]\n"
"v_add_f32 v185, v185, v21\n"
"v_mfma_f32_16x16x32_bf16 v[68:71], a[40:43], a[112:115], v[68:71]\n"
"v_add_f32 v186, v186, v22\n"
"v_add_f32 v187, v187, v23\n"
"v_mfma_f32_16x16x32_bf16 v[72:75], a[40:43], a[120:123], v[72:75]\n"
"v_add_f32 v188, v188, v20\n"
"v_mfma_f32_16x16x32_bf16 v[72:75], a[44:47], a[124:127], v[72:75]\n"
"v_add_f32 v189, v189, v21\n"
"v_mfma_f32_16x16x32_bf16 v[44:47], a[36:39], a[124:127], v[44:47]\n"
"v_add_f32 v190, v190, v22\n"
"v_mfma_f32_16x16x32_bf16 v[44:47], a[32:35], a[120:123], v[44:47]\n"
"s_waitcnt lgkmcnt(4)\n"
"v_add_f32 v191, v191, v23\n"
"v_mfma_f32_16x16x32_bf16 v[48:51], a[32:35], a[128:131], v[48:51]\n"
"v_add_f32 v192, v192, v20\n"
"v_mfma_f32_16x16x32_bf16 v[48:51], a[36:39], a[132:135], v[48:51]\n"
"global_load_dwordx4 a[88:91], v0, s[34:35] offset:2048 sc1\n"
"v_add_f32 v193, v193, v21\n"
"v_mfma_f32_16x16x32_bf16 v[76:79], a[44:47], a[132:135], v[76:79]\n"
"v_add_f32 v194, v194, v22\n"
"v_add_f32 v195, v195, v23\n"
"v_mfma_f32_16x16x32_bf16 v[76:79], a[40:43], a[128:131], v[76:79]\n"
"v_add_f32 v196, v196, v20\n"
"v_mfma_f32_16x16x32_bf16 v[80:83], a[40:43], a[136:139], v[80:83]\n"
"v_add_f32 v197, v197, v21\n"
"v_mfma_f32_16x16x32_bf16 v[80:83], a[44:47], a[140:143], v[80:83]\n"
"v_add_f32 v198, v198, v22\n"
"v_mfma_f32_16x16x32_bf16 v[52:55], a[36:39], a[140:143], v[52:55]\n"
"v_add_f32 v199, v199, v23\n"
"v_mfma_f32_16x16x32_bf16 v[52:55], a[32:35], a[136:139], v[52:55]\n"
"s_waitcnt lgkmcnt(0)\n"
"v_accvgpr_read_b32 v12, a20\n"
"v_mfma_f32_16x16x32_bf16 v[56:59], a[32:35], a[144:147], v[56:59]\n"
"v_accvgpr_read_b32 v13, a21\n"
"v_mfma_f32_16x16x32_bf16 v[56:59], a[36:39], a[148:151], v[56:59]\n"
"global_load_dwordx4 a[92:95], v0, s[34:35] offset:3072 sc1\n"
"v_accvgpr_read_b32 v14, a22\n"
"v_mfma_f32_16x16x32_bf16 v[84:87], a[44:47], a[148:151], v[84:87]\n"
"v_accvgpr_read_b32 v15, a23\n"
"v_add_f32 v12, v12, v20\n"
"v_mfma_f32_16x16x32_bf16 v[84:87], a[40:43], a[144:147], v[84:87]\n"
"v_add_f32 v13, v13, v21\n"
"v_mfma_f32_16x16x32_bf16 a[4:7], a[40:43], a[152:155], a[4:7]\n"
"v_add_f32 v14, v14, v22\n"
"v_mfma_f32_16x16x32_bf16 a[4:7], a[44:47], a[156:159], a[4:7]\n"
"v_add_f32 v15, v15, v23\n"
"v_mfma_f32_16x16x32_bf16 a[0:3], a[36:39], a[156:159], a[0:3]\n"
"v_accvgpr_write_b32 a20, v12\n"
"v_mfma_f32_16x16x32_bf16 a[0:3], a[32:35], a[152:155], a[0:3]\n"
"s_waitcnt vmcnt(8)\n"
"v_accvgpr_write_b32 a21, v13\n"
"v_mfma_f32_16x16x32_bf16 v[88:91], a[48:51], a[96:99], v[88:91]\n"
"s_add_u32 s34, s36, 1531904\n"
"v_accvgpr_write_b32 a22, v14\n"
"v_accvgpr_write_b32 a23, v15\n"
"v_mfma_f32_16x16x32_bf16 v[88:91], a[52:55], a[100:103], v[88:91]\n"
"global_load_dwordx4 a[32:35], v0, s[34:35] offset:0 sc1\n"
"v_lshlrev_b32 v24, 16, v26\n"
"v_mfma_f32_16x16x32_bf16 v[116:119], a[60:63], a[100:103], v[116:119]\n"
"ds_read_b128 a[100:103], v4 offset:17408\n"
"v_and_b32 v25, s63, v26\n"
"v_mfma_f32_16x16x32_bf16 v[116:119], a[56:59], a[96:99], v[116:119]\n"
"ds_read_b128 a[96:99], v4 offset:16384\n"
"v_lshlrev_b32 v26, 16, v27\n"
"v_mfma_f32_16x16x32_bf16 v[120:123], a[56:59], a[104:107], v[120:123]\n"
"v_and_b32 v27, s63, v27\n"
"v_mfma_f32_16x16x32_bf16 v[120:123], a[60:63], a[108:111], v[120:123]\n"
"v_add_f32 v200, v200, v24\n"
"v_mfma_f32_16x16x32_bf16 v[92:95], a[52:55], a[108:111], v[92:95]\n"
"ds_read_b128 a[108:111], v4 offset:19456\n"
"v_add_f32 v201, v201, v25\n"
"v_mfma_f32_16x16x32_bf16 v[92:95], a[48:51], a[104:107], v[92:95]\n"
"ds_read_b128 a[104:107], v4 offset:18432\n"
"v_add_f32 v202, v202, v26\n"
"v_mfma_f32_16x16x32_bf16 v[96:99], a[48:51], a[112:115], v[96:99]\n"
"v_add_f32 v203, v203, v27\n"
"v_add_f32 v204, v204, v24\n"
"v_mfma_f32_16x16x32_bf16 v[96:99], a[52:55], a[116:119], v[96:99]\n"
"global_load_dwordx4 a[36:39], v0, s[34:35] offset:1024 sc1\n"
"v_add_f32 v205, v205, v25\n"
"v_mfma_f32_16x16x32_bf16 v[124:127], a[60:63], a[116:119], v[124:127]\n"
"ds_read_b128 a[116:119], v4 offset:21504\n"
"v_add_f32 v206, v206, v26\n"
"v_mfma_f32_16x16x32_bf16 v[124:127], a[56:59], a[112:115], v[124:127]\n"
"ds_read_b128 a[112:115], v4 offset:20480\n"
"v_add_f32 v207, v207, v27\n"
"v_mfma_f32_16x16x32_bf16 v[128:131], a[56:59], a[120:123], v[128:131]\n"
"v_add_f32 v208, v208, v24\n"
"v_mfma_f32_16x16x32_bf16 v[128:131], a[60:63], a[124:127], v[128:131]\n"
"v_add_f32 v209, v209, v25\n"
"v_mfma_f32_16x16x32_bf16 v[100:103], a[52:55], a[124:127], v[100:103]\n"
"ds_read_b128 a[124:127], v4 offset:23552\n"
"v_add_f32 v210, v210, v26\n"
"v_mfma_f32_16x16x32_bf16 v[100:103], a[48:51], a[120:123], v[100:103]\n"
"ds_read_b128 a[120:123], v4 offset:22528\n"
"v_add_f32 v211, v211, v27\n"
"v_mfma_f32_16x16x32_bf16 v[104:107], a[48:51], a[128:131], v[104:107]\n"
"v_add_f32 v212, v212, v24\n"
"v_add_f32 v213, v213, v25\n"
"v_mfma_f32_16x16x32_bf16 v[104:107], a[52:55], a[132:135], v[104:107]\n"
"global_load_dwordx4 a[40:43], v0, s[34:35] offset:2048 sc1\n"
"v_add_f32 v214, v214, v26\n"
"v_mfma_f32_16x16x32_bf16 v[132:135], a[60:63], a[132:135], v[132:135]\n"
"ds_read_b128 a[132:135], v4 offset:25600\n"
"v_add_f32 v215, v215, v27\n"
"v_mfma_f32_16x16x32_bf16 v[132:135], a[56:59], a[128:131], v[132:135]\n"
"ds_read_b128 a[128:131], v4 offset:24576\n"
"v_add_f32 v216, v216, v24\n"
"v_mfma_f32_16x16x32_bf16 v[136:139], a[56:59], a[136:139], v[136:139]\n"
"v_add_f32 v217, v217, v25\n"
"v_mfma_f32_16x16x32_bf16 v[136:139], a[60:63], a[140:143], v[136:139]\n"
"v_add_f32 v218, v218, v26\n"
"v_mfma_f32_16x16x32_bf16 v[108:111], a[52:55], a[140:143], v[108:111]\n"
"ds_read_b128 a[140:143], v4 offset:27648\n"
"v_add_f32 v219, v219, v27\n"
"v_mfma_f32_16x16x32_bf16 v[108:111], a[48:51], a[136:139], v[108:111]\n"
"ds_read_b128 a[136:139], v4 offset:26624\n"
"v_add_f32 v220, v220, v24\n"
"v_mfma_f32_16x16x32_bf16 v[112:115], a[48:51], a[144:147], v[112:115]\n"
"v_add_f32 v221, v221, v25\n"
"v_add_f32 v222, v222, v26\n"
"v_mfma_f32_16x16x32_bf16 v[112:115], a[52:55], a[148:151], v[112:115]\n"
"global_load_dwordx4 a[44:47], v0, s[34:35] offset:3072 sc1\n"
"v_add_f32 v223, v223, v27\n"
"v_mfma_f32_16x16x32_bf16 v[140:143], a[60:63], a[148:151], v[140:143]\n"
"ds_read_b128 a[148:151], v4 offset:29696\n"
"v_add_f32 v224, v224, v24\n"
"v_mfma_f32_16x16x32_bf16 v[140:143], a[56:59], a[144:147], v[140:143]\n"
"ds_read_b128 a[144:147], v4 offset:28672\n"
"v_add_f32 v225, v225, v25\n"
"v_mfma_f32_16x16x32_bf16 a[12:15], a[56:59], a[152:155], a[12:15]\n"
"v_add_f32 v226, v226, v26\n"
"v_mfma_f32_16x16x32_bf16 a[12:15], a[60:63], a[156:159], a[12:15]\n"
"v_add_f32 v227, v227, v27\n"
"v_mfma_f32_16x16x32_bf16 a[8:11], a[52:55], a[156:159], a[8:11]\n"
"ds_read_b128 a[156:159], v4 offset:31744\n"
"v_accvgpr_read_b32 v12, a24\n"
"v_mfma_f32_16x16x32_bf16 a[8:11], a[48:51], a[152:155], a[8:11]\n"
"s_waitcnt vmcnt(8) lgkmcnt(11)\n"
"ds_read_b128 a[152:155], v4 offset:30720\n"
"v_accvgpr_read_b32 v13, a25\n"
"v_mfma_f32_16x16x32_bf16 v[32:35], a[64:67], a[96:99], v[32:35]\n"
"s_add_u32 s34, s36, 1536000\n"
"v_accvgpr_read_b32 v14, a26\n"
"v_accvgpr_read_b32 v15, a27\n"
"v_mfma_f32_16x16x32_bf16 v[32:35], a[68:71], a[100:103], v[32:35]\n"
"global_load_dwordx4 a[48:51], v0, s[34:35] offset:0 sc1\n"
"v_add_f32 v12, v12, v24\n"
"v_mfma_f32_16x16x32_bf16 v[60:63], a[76:79], a[100:103], v[60:63]\n"
"v_add_f32 v13, v13, v25\n"
"v_mfma_f32_16x16x32_bf16 v[60:63], a[72:75], a[96:99], v[60:63]\n"
"v_add_f32 v14, v14, v26\n"
"v_mfma_f32_16x16x32_bf16 v[64:67], a[72:75], a[104:107], v[64:67]\n"
"v_add_f32 v15, v15, v27\n"
"v_mfma_f32_16x16x32_bf16 v[64:67], a[76:79], a[108:111], v[64:67]\n"
"v_accvgpr_write_b32 a24, v12\n"
"v_mfma_f32_16x16x32_bf16 v[36:39], a[68:71], a[108:111], v[36:39]\n"
"v_accvgpr_write_b32 a25, v13\n"
"v_accvgpr_write_b32 a26, v14\n"
"v_mfma_f32_16x16x32_bf16 v[36:39], a[64:67], a[104:107], v[36:39]\n"
"s_waitcnt lgkmcnt(8)\n"
"v_accvgpr_write_b32 a27, v15\n"
"v_mfma_f32_16x16x32_bf16 v[40:43], a[64:67], a[112:115], v[40:43]\n"
"v_lshlrev_b32 v28, 16, v30\n"
"v_mfma_f32_16x16x32_bf16 v[40:43], a[68:71], a[116:119], v[40:43]\n"
"global_load_dwordx4 a[52:55], v0, s[34:35] offset:1024 sc1\n"
"v_and_b32 v29, s63, v30\n"
"v_mfma_f32_16x16x32_bf16 v[68:71], a[76:79], a[116:119], v[68:71]\n"
"v_lshlrev_b32 v30, 16, v31\n"
"v_mfma_f32_16x16x32_bf16 v[68:71], a[72:75], a[112:115], v[68:71]\n"
"v_and_b32 v31, s63, v31\n"
"v_mfma_f32_16x16x32_bf16 v[72:75], a[72:75], a[120:123], v[72:75]\n"
"v_add_f32 v228, v228, v28\n"
"v_mfma_f32_16x16x32_bf16 v[72:75], a[76:79], a[124:127], v[72:75]\n"
"v_add_f32 v229, v229, v29\n"
"v_add_f32 v230, v230, v30\n"
"v_mfma_f32_16x16x32_bf16 v[44:47], a[68:71], a[124:127], v[44:47]\n"
"v_add_f32 v231, v231, v31\n"
"v_mfma_f32_16x16x32_bf16 v[44:47], a[64:67], a[120:123], v[44:47]\n"
"s_waitcnt lgkmcnt(4)\n"
"v_add_f32 v232, v232, v28\n"
"v_mfma_f32_16x16x32_bf16 v[48:51], a[64:67], a[128:131], v[48:51]\n"
"v_add_f32 v233, v233, v29\n"
"v_mfma_f32_16x16x32_bf16 v[48:51], a[68:71], a[132:135], v[48:51]\n"
"global_load_dwordx4 a[56:59], v0, s[34:35] offset:2048 sc1\n"
"v_add_f32 v234, v234, v30\n"
"v_mfma_f32_16x16x32_bf16 v[76:79], a[76:79], a[132:135], v[76:79]\n"
"v_add_f32 v235, v235, v31\n"
"v_mfma_f32_16x16x32_bf16 v[76:79], a[72:75], a[128:131], v[76:79]\n"
"v_add_f32 v236, v236, v28\n"
"v_mfma_f32_16x16x32_bf16 v[80:83], a[72:75], a[136:139], v[80:83]\n"
"v_add_f32 v237, v237, v29\n"
"v_mfma_f32_16x16x32_bf16 v[80:83], a[76:79], a[140:143], v[80:83]\n"
"v_add_f32 v238, v238, v30\n"
"v_add_f32 v239, v239, v31\n"
"v_mfma_f32_16x16x32_bf16 v[52:55], a[68:71], a[140:143], v[52:55]\n"
"v_add_f32 v240, v240, v28\n"
"v_mfma_f32_16x16x32_bf16 v[52:55], a[64:67], a[136:139], v[52:55]\n"
"s_waitcnt lgkmcnt(0)\n"
"v_add_f32 v241, v241, v29\n"
"v_mfma_f32_16x16x32_bf16 v[56:59], a[64:67], a[144:147], v[56:59]\n"
"v_add_f32 v242, v242, v30\n"
"v_mfma_f32_16x16x32_bf16 v[56:59], a[68:71], a[148:151], v[56:59]\n"
"global_load_dwordx4 a[60:63], v0, s[34:35] offset:3072 sc1\n"
"v_add_f32 v243, v243, v31\n"
"v_mfma_f32_16x16x32_bf16 v[84:87], a[76:79], a[148:151], v[84:87]\n"
"v_add_f32 v244, v244, v28\n"
"v_mfma_f32_16x16x32_bf16 v[84:87], a[72:75], a[144:147], v[84:87]\n"
"v_add_f32 v245, v245, v29\n"
"v_mfma_f32_16x16x32_bf16 a[4:7], a[72:75], a[152:155], a[4:7]\n"
"v_add_f32 v246, v246, v30\n"
"v_add_f32 v247, v247, v31\n"
"v_mfma_f32_16x16x32_bf16 a[4:7], a[76:79], a[156:159], a[4:7]\n"
"v_add_f32 v248, v248, v28\n"
"v_mfma_f32_16x16x32_bf16 a[0:3], a[68:71], a[156:159], a[0:3]\n"
"v_add_f32 v249, v249, v29\n"
"v_mfma_f32_16x16x32_bf16 a[0:3], a[64:67], a[152:155], a[0:3]\n"
"s_waitcnt vmcnt(8)\n"
"v_add_f32 v250, v250, v30\n"
"v_mfma_f32_16x16x32_bf16 v[88:91], a[80:83], a[96:99], v[88:91]\n"
"s_add_u32 s34, s36, 1466368\n"
"v_add_f32 v251, v251, v31\n"
"v_mfma_f32_16x16x32_bf16 v[88:91], a[84:87], a[100:103], v[88:91]\n"
"global_load_dwordx4 a[64:67], v0, s[34:35] offset:0 sc1\n"
"v_add_f32 v252, v252, v28\n"
"v_mfma_f32_16x16x32_bf16 v[116:119], a[92:95], a[100:103], v[116:119]\n"
"ds_read_b128 a[100:103], v5 offset:17408\n"
"v_add_f32 v253, v253, v29\n"
"v_mfma_f32_16x16x32_bf16 v[116:119], a[88:91], a[96:99], v[116:119]\n"
"ds_read_b128 a[96:99], v5 offset:16384\n"
"v_add_f32 v254, v254, v30\n"
"v_mfma_f32_16x16x32_bf16 v[120:123], a[88:91], a[104:107], v[120:123]\n"
"v_add_f32 v255, v255, v31\n"
"v_accvgpr_read_b32 v12, a28\n"
"v_mfma_f32_16x16x32_bf16 v[120:123], a[92:95], a[108:111], v[120:123]\n"
"v_accvgpr_read_b32 v13, a29\n"
"v_mfma_f32_16x16x32_bf16 v[92:95], a[84:87], a[108:111], v[92:95]\n"
"ds_read_b128 a[108:111], v5 offset:19456\n"
"v_accvgpr_read_b32 v14, a30\n"
"v_mfma_f32_16x16x32_bf16 v[92:95], a[80:83], a[104:107], v[92:95]\n"
"ds_read_b128 a[104:107], v5 offset:18432\n"
"v_accvgpr_read_b32 v15, a31\n"
"v_mfma_f32_16x16x32_bf16 v[96:99], a[80:83], a[112:115], v[96:99]\n"
"v_add_f32 v12, v12, v28\n"
"v_mfma_f32_16x16x32_bf16 v[96:99], a[84:87], a[116:119], v[96:99]\n"
"global_load_dwordx4 a[68:71], v0, s[34:35] offset:1024 sc1\n"
"v_add_f32 v13, v13, v29\n"
"v_mfma_f32_16x16x32_bf16 v[124:127], a[92:95], a[116:119], v[124:127]\n"
"ds_read_b128 a[116:119], v5 offset:21504\n"
"v_add_f32 v14, v14, v30\n"
"v_mfma_f32_16x16x32_bf16 v[124:127], a[88:91], a[112:115], v[124:127]\n"
"ds_read_b128 a[112:115], v5 offset:20480\n"
"v_add_f32 v15, v15, v31\n"
"v_mfma_f32_16x16x32_bf16 v[128:131], a[88:91], a[120:123], v[128:131]\n"
"v_accvgpr_write_b32 a28, v12\n"
"v_accvgpr_write_b32 a29, v13\n"
"v_mfma_f32_16x16x32_bf16 v[128:131], a[92:95], a[124:127], v[128:131]\n"
"v_accvgpr_write_b32 a30, v14\n"
"v_mfma_f32_16x16x32_bf16 v[100:103], a[84:87], a[124:127], v[100:103]\n"
"ds_read_b128 a[124:127], v5 offset:23552\n"
"v_accvgpr_write_b32 a31, v15\n"
"v_mfma_f32_16x16x32_bf16 v[100:103], a[80:83], a[120:123], v[100:103]\n"
"ds_read_b128 a[120:123], v5 offset:22528\n"
"v_mfma_f32_16x16x32_bf16 v[104:107], a[80:83], a[128:131], v[104:107]\n"
"v_mfma_f32_16x16x32_bf16 v[104:107], a[84:87], a[132:135], v[104:107]\n"
"global_load_dwordx4 a[72:75], v0, s[34:35] offset:2048 sc1\n"
"v_mfma_f32_16x16x32_bf16 v[132:135], a[92:95], a[132:135], v[132:135]\n"
"ds_read_b128 a[132:135], v5 offset:25600\n"
"v_mfma_f32_16x16x32_bf16 v[132:135], a[88:91], a[128:131], v[132:135]\n"
"ds_read_b128 a[128:131], v5 offset:24576\n"
"v_mfma_f32_16x16x32_bf16 v[136:139], a[88:91], a[136:139], v[136:139]\n"
"ds_read_b128 v[16:19], v7 offset:32768\n"
"v_mfma_f32_16x16x32_bf16 v[136:139], a[92:95], a[140:143], v[136:139]\n"
"v_mfma_f32_16x16x32_bf16 v[108:111], a[84:87], a[140:143], v[108:111]\n"
"ds_read_b128 a[140:143], v5 offset:27648\n"
"v_mfma_f32_16x16x32_bf16 v[108:111], a[80:83], a[136:139], v[108:111]\n"
"ds_read_b128 a[136:139], v5 offset:26624\n"
"v_mfma_f32_16x16x32_bf16 v[112:115], a[80:83], a[144:147], v[112:115]\n"
"v_mfma_f32_16x16x32_bf16 v[112:115], a[84:87], a[148:151], v[112:115]\n"
"global_load_dwordx4 a[76:79], v0, s[34:35] offset:3072 sc1\n"
"v_mfma_f32_16x16x32_bf16 v[140:143], a[92:95], a[148:151], v[140:143]\n"
"ds_read_b128 a[148:151], v5 offset:29696\n"
"v_mfma_f32_16x16x32_bf16 v[140:143], a[88:91], a[144:147], v[140:143]\n"
"ds_read_b128 a[144:147], v5 offset:28672\n"
"v_mfma_f32_16x16x32_bf16 a[12:15], a[88:91], a[152:155], a[12:15]\n"
"v_mfma_f32_16x16x32_bf16 a[12:15], a[92:95], a[156:159], a[12:15]\n"
"v_mfma_f32_16x16x32_bf16 a[8:11], a[84:87], a[156:159], a[8:11]\n"
"ds_read_b128 a[156:159], v5 offset:31744\n"
"v_mfma_f32_16x16x32_bf16 a[8:11], a[80:83], a[152:155], a[8:11]\n"
"s_waitcnt vmcnt(8) lgkmcnt(12)\n"
"ds_read_b128 a[152:155], v5 offset:30720\n"
"v_mfma_f32_16x16x32_bf16 v[32:35], a[32:35], a[96:99], v[32:35]\n"
"s_add_u32 s34, s36, 1470464\n"
"v_mfma_f32_16x16x32_bf16 v[32:35], a[36:39], a[100:103], v[32:35]\n"
"global_load_dwordx4 a[80:83], v0, s[34:35] offset:0 sc1\n"
"v_mfma_f32_16x16x32_bf16 v[60:63], a[44:47], a[100:103], v[60:63]\n"
"v_mfma_f32_16x16x32_bf16 v[60:63], a[40:43], a[96:99], v[60:63]\n"
"v_mfma_f32_16x16x32_bf16 v[64:67], a[40:43], a[104:107], v[64:67]\n"
"v_mfma_f32_16x16x32_bf16 v[64:67], a[44:47], a[108:111], v[64:67]\n"
"v_mfma_f32_16x16x32_bf16 v[36:39], a[36:39], a[108:111], v[36:39]\n"
"v_mfma_f32_16x16x32_bf16 v[36:39], a[32:35], a[104:107], v[36:39]\n"
"s_waitcnt lgkmcnt(9)\n"
"v_mfma_f32_16x16x32_bf16 v[40:43], a[32:35], a[112:115], v[40:43]\n"
"s_mov_b32 s52, %4\n"
"s_and_b32 s53, %5, 0xffff\n"
"v_mfma_f32_16x16x32_bf16 v[40:43], a[36:39], a[116:119], v[40:43]\n"
"global_load_dwordx4 a[84:87], v0, s[34:35] offset:1024 sc1\n"
"v_mfma_f32_16x16x32_bf16 v[68:71], a[44:47], a[116:119], v[68:71]\n"
"s_mov_b32 s54, 260352\n"
"s_mov_b32 s55, 0x20000\n"
"v_mfma_f32_16x16x32_bf16 v[68:71], a[40:43], a[112:115], v[68:71]\n"
"v_xor_b32 v8, 64, v7\n"
"v_mfma_f32_16x16x32_bf16 v[72:75], a[40:43], a[120:123], v[72:75]\n"
"s_waitcnt lgkmcnt(6)\n"
"v_lshlrev_b32 v12, 16, v16\n"
"v_mfma_f32_16x16x32_bf16 v[72:75], a[44:47], a[124:127], v[72:75]\n"
"v_and_b32 v13, s63, v16\n"
"v_lshlrev_b32 v14, 16, v17\n"
"v_mfma_f32_16x16x32_bf16 v[44:47], a[36:39], a[124:127], v[44:47]\n"
"v_and_b32 v15, s63, v17\n"
"v_mfma_f32_16x16x32_bf16 v[44:47], a[32:35], a[120:123], v[44:47]\n"
"s_waitcnt lgkmcnt(4)\n"
"v_xor_b32 v20, 64, v7\n"
"v_mfma_f32_16x16x32_bf16 v[48:51], a[32:35], a[128:131], v[48:51]\n"
"ds_read_b128 v[20:23], v20 offset:32768\n"
"v_mul_f32 v12, v12, v32\n"
"v_mfma_f32_16x16x32_bf16 v[48:51], a[36:39], a[132:135], v[48:51]\n"
"global_load_dwordx4 a[88:91], v0, s[34:35] offset:2048 sc1\n"
"v_mul_f32 v13, v13, v33\n"
"v_mfma_f32_16x16x32_bf16 v[76:79], a[44:47], a[132:135], v[76:79]\n"
"v_mul_f32 v14, v14, v34\n"
"v_mfma_f32_16x16x32_bf16 v[76:79], a[40:43], a[128:131], v[76:79]\n"
"v_mul_f32 v15, v15, v35\n"
"v_cvt_pk_bf16_f32 v24, v12, v13\n"
"v_mfma_f32_16x16x32_bf16 v[80:83], a[40:43], a[136:139], v[80:83]\n"
"v_cvt_pk_bf16_f32 v25, v14, v15\n"
"v_mfma_f32_16x16x32_bf16 v[80:83], a[44:47], a[140:143], v[80:83]\n"
"ds_write_b64 v7, v[24:25] offset:32768\n"
"v_mfma_f32_16x16x32_bf16 v[52:55], a[36:39], a[140:143], v[52:55]\n"
"v_lshlrev_b32 v12, 16, v18\n"
"v_mfma_f32_16x16x32_bf16 v[52:55], a[32:35], a[136:139], v[52:55]\n"
"s_waitcnt lgkmcnt(2)\n"
"v_and_b32 v13, s63, v18\n"
"v_lshlrev_b32 v14, 16, v19\n"
"v_mfma_f32_16x16x32_bf16 v[56:59], a[32:35], a[144:147], v[56:59]\n"
"L_st10279:\n"
"s_cbranch_execz L_retC\n"
"v_and_b32 v15, s63, v19\n"
"v_mfma_f32_16x16x32_bf16 v[56:59], a[36:39], a[148:151], v[56:59]\n"
"global_load_dwordx4 a[92:95], v0, s[34:35] offset:3072 sc1\n"
"v_mul_f32 v12, v12, v60\n"
"v_mfma_f32_16x16x32_bf16 v[84:87], a[44:47], a[148:151], v[84:87]\n"
"v_mul_f32 v13, v13, v61\n"
"v_mul_f32 v14, v14, v62\n"
"v_mfma_f32_16x16x32_bf16 v[84:87], a[40:43], a[144:147], v[84:87]\n"
"v_mul_f32 v15, v15, v63\n"
"v_mfma_f32_16x16x32_bf16 a[4:7], a[40:43], a[152:155], a[4:7]\n"
"v_cvt_pk_bf16_f32 v26, v12, v13\n"
"v_mfma_f32_16x16x32_bf16 a[4:7], a[44:47], a[156:159], a[4:7]\n"
"v_cvt_pk_bf16_f32 v27, v14, v15\n"
"v_mfma_f32_16x16x32_bf16 a[0:3], a[36:39], a[156:159], a[0:3]\n"
"ds_write_b64 v7, v[26:27] offset:32776\n"
"v_mfma_f32_16x16x32_bf16 a[0:3], a[32:35], a[152:155], a[0:3]\n"
"s_waitcnt vmcnt(8) lgkmcnt(2)\n"
"v_lshlrev_b32 v12, 16, v20\n"
"v_mfma_f32_16x16x32_bf16 v[88:91], a[48:51], a[96:99], v[88:91]\n"
"s_add_u32 s34, s36, 1482752\n"
"v_and_b32 v13, s63, v20\n"
"v_mfma_f32_16x16x32_bf16 v[88:91], a[52:55], a[100:103], v[88:91]\n"
"global_load_dwordx4 a[32:35], v0, s[34:35] offset:0 sc1\n"
"v_mfma_f32_16x16x32_bf16 v[116:119], a[60:63], a[100:103], v[116:119]\n"
"ds_read_b128 a[100:103], v3 offset:1024\n"
"v_mfma_f32_16x16x32_bf16 v[116:119], a[56:59], a[96:99], v[116:119]\n"
"ds_read_b128 a[96:99], v3 offset:0\n"
"v_mfma_f32_16x16x32_bf16 v[120:123], a[56:59], a[104:107], v[120:123]\n"
"v_lshlrev_b32 v14, 16, v21\n"
"v_mfma_f32_16x16x32_bf16 v[120:123], a[60:63], a[108:111], v[120:123]\n"
"v_and_b32 v15, s63, v21\n"
"v_mfma_f32_16x16x32_bf16 v[92:95], a[52:55], a[108:111], v[92:95]\n"
"ds_read_b128 a[108:111], v3 offset:3072\n"
"v_mfma_f32_16x16x32_bf16 v[92:95], a[48:51], a[104:107], v[92:95]\n"
"ds_read_b128 a[104:107], v3 offset:2048\n"
"v_mfma_f32_16x16x32_bf16 v[96:99], a[48:51], a[112:115], v[96:99]\n"
"ds_read_b128 v[16:19], v7 offset:34816\n"
"v_mfma_f32_16x16x32_bf16 v[96:99], a[52:55], a[116:119], v[96:99]\n"
"global_load_dwordx4 a[36:39], v0, s[34:35] offset:1024 sc1\n"
"v_mul_f32 v12, v12, v88\n"
"v_mul_f32 v13, v13, v89\n"
"v_mfma_f32_16x16x32_bf16 v[124:127], a[60:63], a[116:119], v[124:127]\n"
"ds_read_b128 a[116:119], v3 offset:5120\n"
"v_mul_f32 v14, v14, v90\n"
"v_mul_f32 v15, v15, v91\n"
"v_mfma_f32_16x16x32_bf16 v[124:127], a[56:59], a[112:115], v[124:127]\n"
"ds_read_b128 a[112:115], v3 offset:4096\n"
"v_cvt_pk_bf16_f32 v28, v12, v13\n"
"v_cvt_pk_bf16_f32 v29, v14, v15\n"
"v_mfma_f32_16x16x32_bf16 v[128:131], a[56:59], a[120:123], v[128:131]\n"
"ds_write_b64 v8, v[28:29] offset:32768\n"
"v_mfma_f32_16x16x32_bf16 v[128:131], a[60:63], a[124:127], v[128:131]\n"
"v_lshlrev_b32 v12, 16, v22\n"
"v_and_b32 v13, s63, v22\n"
"v_lshlrev_b32 v14, 16, v23\n"
"v_mfma_f32_16x16x32_bf16 v[100:103], a[52:55], a[124:127], v[100:103]\n"
"ds_read_b128 a[124:127], v3 offset:7168\n"
"v_and_b32 v15, s63, v23\n"
"v_mul_f32 v12, v12, v116\n"
"v_mfma_f32_16x16x32_bf16 v[100:103], a[48:51], a[120:123], v[100:103]\n"
"ds_read_b128 a[120:123], v3 offset:6144\n"
"v_mul_f32 v13, v13, v117\n"
"v_mfma_f32_16x16x32_bf16 v[104:107], a[48:51], a[128:131], v[104:107]\n"
"v_mul_f32 v14, v14, v118\n"
"v_mul_f32 v15, v15, v119\n"
"v_mfma_f32_16x16x32_bf16 v[104:107], a[52:55], a[132:135], v[104:107]\n"
"global_load_dwordx4 a[40:43], v0, s[34:35] offset:2048 sc1\n"
"v_cvt_pk_bf16_f32 v30, v12, v13\n"
"v_mfma_f32_16x16x32_bf16 v[132:135], a[60:63], a[132:135], v[132:135]\n"
"ds_read_b128 a[132:135], v3 offset:9216\n"
"v_cvt_pk_bf16_f32 v31, v14, v15\n"
"v_mfma_f32_16x16x32_bf16 v[132:135], a[56:59], a[128:131], v[132:135]\n"
"ds_read_b128 a[128:131], v3 offset:8192\n"
"ds_write_b64 v8, v[30:31] offset:32776\n"
"v_mfma_f32_16x16x32_bf16 v[136:139], a[56:59], a[136:139], v[136:139]\n"
"ds_read_b128 v[32:35], v2 offset:32768\n"
"v_mfma_f32_16x16x32_bf16 v[136:139], a[60:63], a[140:143], v[136:139]\n"
"s_add_u32 s96, s36, 1552384\n"
"ds_read_b128 v[60:63], v2 offset:33792\n"
"v_mfma_f32_16x16x32_bf16 v[108:111], a[52:55], a[140:143], v[108:111]\n"
"s_waitcnt lgkmcnt(10)\n"
"ds_read_b128 a[140:143], v3 offset:11264\n"
"v_lshlrev_b32 v12, 16, v16\n"
"v_mfma_f32_16x16x32_bf16 v[108:111], a[48:51], a[136:139], v[108:111]\n"
"ds_read_b128 a[136:139], v3 offset:10240\n"
"v_and_b32 v13, s63, v16\n"
"v_mfma_f32_16x16x32_bf16 v[112:115], a[48:51], a[144:147], v[112:115]\n"
"global_load_dwordx4 v[88:91], v0, s[96:97] offset:-4096 sc1\n"
"v_lshlrev_b32 v14, 16, v17\n"
"v_mfma_f32_16x16x32_bf16 v[112:115], a[52:55], a[148:151], v[112:115]\n"
"global_load_dwordx4 a[44:47], v0, s[34:35] offset:3072 sc1\n"
"v_and_b32 v15, s63, v17\n"
"v_mfma_f32_16x16x32_bf16 v[140:143], a[60:63], a[148:151], v[140:143]\n"
"ds_read_b128 a[148:151], v3 offset:13312\n"
"v_xor_b32 v20, 64, v7\n"
"v_mfma_f32_16x16x32_bf16 v[140:143], a[56:59], a[144:147], v[140:143]\n"
"ds_read_b128 a[144:147], v3 offset:12288\n"
"v_mfma_f32_16x16x32_bf16 a[12:15], a[56:59], a[152:155], a[12:15]\n"
"ds_read_b128 v[20:23], v20 offset:34816\n"
"v_mul_f32 v12, v12, v36\n"
"v_mfma_f32_16x16x32_bf16 a[12:15], a[60:63], a[156:159], a[12:15]\n"
"s_waitcnt lgkmcnt(6)\n"
"buffer_store_dwordx4 v[32:35], v6, s[52:55], s80 offen offset:0 sc1\n"
"v_mul_f32 v13, v13, v37\n"
"v_mul_f32 v14, v14, v38\n"
"v_mul_f32 v15, v15, v39\n"
"v_mfma_f32_16x16x32_bf16 a[8:11], a[52:55], a[156:159], a[8:11]\n"
"ds_read_b128 a[156:159], v3 offset:15360\n"
"v_cvt_pk_bf16_f32 v24, v12, v13\n"
"v_mfma_f32_16x16x32_bf16 a[8:11], a[48:51], a[152:155], a[8:11]\n"
"s_waitcnt vmcnt(10)\n"
"ds_read_b128 a[152:155], v3 offset:14336\n"
"v_cvt_pk_bf16_f32 v25, v14, v15\n"
"v_mfma_f32_16x16x32_bf16 v[144:147], a[64:67], a[96:99], v[144:147]\n"
"s_add_u32 s34, s36, 1486848\n"
"ds_write_b64 v7, v[24:25] offset:34816\n"
"v_mfma_f32_16x16x32_bf16 v[144:147], a[68:71], a[100:103], v[144:147]\n"
"global_load_dwordx4 a[48:51], v0, s[34:35] offset:0 sc1\n"
"v_lshlrev_b32 v12, 16, v18\n"
"v_mfma_f32_16x16x32_bf16 v[172:175], a[76:79], a[100:103], v[172:175]\n"
"global_load_dwordx4 v[116:119], v0, s[96:97] offset:-3072 sc1\n"
"v_and_b32 v13, s63, v18\n"
"v_mfma_f32_16x16x32_bf16 v[172:175], a[72:75], a[96:99], v[172:175]\n"
"v_lshlrev_b32 v14, 16, v19\n"
"v_and_b32 v15, s63, v19\n"
"v_mfma_f32_16x16x32_bf16 v[176:179], a[72:75], a[104:107], v[176:179]\n"
"v_mul_f32 v12, v12, v64\n"
"v_mul_f32 v13, v13, v65\n"
"v_mfma_f32_16x16x32_bf16 v[176:179], a[76:79], a[108:111], v[176:179]\n"
"v_mul_f32 v14, v14, v66\n"
"v_mul_f32 v15, v15, v67\n"
"v_mfma_f32_16x16x32_bf16 v[148:151], a[68:71], a[108:111], v[148:151]\n"
"v_cvt_pk_bf16_f32 v26, v12, v13\n"
"v_cvt_pk_bf16_f32 v27, v14, v15\n"
"v_mfma_f32_16x16x32_bf16 v[148:151], a[64:67], a[104:107], v[148:151]\n"
"ds_write_b64 v7, v[26:27] offset:34824\n"
"v_mfma_f32_16x16x32_bf16 v[152:155], a[64:67], a[112:115], v[152:155]\n"
"s_waitcnt lgkmcnt(4)\n"
"v_lshlrev_b32 v12, 16, v20\n"
"v_and_b32 v13, s63, v20\n"
"v_mfma_f32_16x16x32_bf16 v[152:155], a[68:71], a[116:119], v[152:155]\n"
"global_load_dwordx4 a[52:55], v0, s[34:35] offset:1024 sc1\n"
"v_lshlrev_b32 v14, 16, v21\n"
"v_mfma_f32_16x16x32_bf16 v[180:183], a[76:79], a[116:119], v[180:183]\n"
"ds_read_b128 v[16:19], v7 offset:36864\n"
"v_and_b32 v15, s63, v21\n"
"v_mfma_f32_16x16x32_bf16 v[180:183], a[72:75], a[112:115], v[180:183]\n"
"v_mul_f32 v12, v12, v92\n"
"v_mul_f32 v13, v13, v93\n"
"v_mfma_f32_16x16x32_bf16 v[184:187], a[72:75], a[120:123], v[184:187]\n"
"v_mul_f32 v14, v14, v94\n"
"v_mul_f32 v15, v15, v95\n"
"v_mfma_f32_16x16x32_bf16 v[184:187], a[76:79], a[124:127], v[184:187]\n"
"v_cvt_pk_bf16_f32 v28, v12, v13\n"
"v_cvt_pk_bf16_f32 v29, v14, v15\n"
"v_mfma_f32_16x16x32_bf16 v[156:159], a[68:71], a[124:127], v[156:159]\n"
"ds_write_b64 v8, v[28:29] offset:34816\n"
"v_mfma_f32_16x16x32_bf16 v[156:159], a[64:67], a[120:123], v[156:159]\n"
"v_lshlrev_b32 v12, 16, v22\n"
"v_and_b32 v13, s63, v22\n"
"v_mfma_f32_16x16x32_bf16 v[160:163], a[64:67], a[128:131], v[160:163]\n"
"v_lshlrev_b32 v14, 16, v23\n"
"v_and_b32 v15, s63, v23\n"
"v_mfma_f32_16x16x32_bf16 v[160:163], a[68:71], a[132:135], v[160:163]\n"
"global_load_dwordx4 a[56:59], v0, s[34:35] offset:2048 sc1\n"
"v_mul_f32 v12, v12, v120\n"
"v_mfma_f32_16x16x32_bf16 v[188:191], a[76:79], a[132:135], v[188:191]\n"
"v_mul_f32 v13, v13, v121\n"
"v_mul_f32 v14, v14, v122\n"
"v_mfma_f32_16x16x32_bf16 v[188:191], a[72:75], a[128:131], v[188:191]\n"
"v_mul_f32 v15, v15, v123\n"
"v_cvt_pk_bf16_f32 v30, v12, v13\n"
"v_mfma_f32_16x16x32_bf16 v[192:195], a[72:75], a[136:139], v[192:195]\n"
"v_cvt_pk_bf16_f32 v31, v14, v15\n"
"v_mfma_f32_16x16x32_bf16 v[192:195], a[76:79], a[140:143], v[192:195]\n"
"ds_write_b64 v8, v[30:31] offset:34824\n"
"v_mfma_f32_16x16x32_bf16 v[164:167], a[68:71], a[140:143], v[164:167]\n"
"ds_read_b128 v[36:39], v2 offset:34816\n"
"v_mfma_f32_16x16x32_bf16 v[164:167], a[64:67], a[136:139], v[164:167]\n"
"s_waitcnt lgkmcnt(3)\n"
"ds_read_b128 v[64:67], v2 offset:35840\n"
"v_lshlrev_b32 v12, 16, v16\n"
"v_mfma_f32_16x16x32_bf16 v[168:171], a[64:67], a[144:147], v[168:171]\n"
"buffer_store_dwordx4 v[60:63], v6, s[52:55], s81 offen offset:1024 sc1\n"
"v_and_b32 v13, s63, v16\n"
"v_lshlrev_b32 v14, 16, v17\n"
"v_and_b32 v15, s63, v17\n"
"v_xor_b32 v20, 64, v7\n"
"v_mfma_f32_16x16x32_bf16 v[168:171], a[68:71], a[148:151], v[168:171]\n"
"global_load_dwordx4 a[60:63], v0, s[34:35] offset:3072 sc1\n"
"ds_read_b128 v[20:23], v20 offset:36864\n"
"v_mfma_f32_16x16x32_bf16 v[196:199], a[76:79], a[148:151], v[196:199]\n"
"v_mul_f32 v12, v12, v40\n"
"v_mul_f32 v13, v13, v41\n"
"v_mfma_f32_16x16x32_bf16 v[196:199], a[72:75], a[144:147], v[196:199]\n"
"v_mul_f32 v14, v14, v42\n"
"v_mul_f32 v15, v15, v43\n"
"v_mfma_f32_16x16x32_bf16 a[20:23], a[72:75], a[152:155], a[20:23]\n"
"v_cvt_pk_bf16_f32 v24, v12, v13\n"
"v_cvt_pk_bf16_f32 v25, v14, v15\n"
"v_mfma_f32_16x16x32_bf16 a[20:23], a[76:79], a[156:159], a[20:23]\n"
"ds_write_b64 v7, v[24:25] offset:36864\n"
"v_mfma_f32_16x16x32_bf16 a[16:19], a[68:71], a[156:159], a[16:19]\n"
"global_load_dwordx4 v[92:95], v0, s[96:97] offset:-2048 sc1\n"
"v_lshlrev_b32 v12, 16, v18\n"
"v_mfma_f32_16x16x32_bf16 a[16:19], a[64:67], a[152:155], a[16:19]\n"
"s_waitcnt vmcnt(13)\n"
"v_and_b32 v13, s63, v18\n"
"v_lshlrev_b32 v14, 16, v19\n"
"v_mfma_f32_16x16x32_bf16 v[200:203], a[80:83], a[96:99], v[200:203]\n"
"s_add_u32 s34, s36, 1499136\n"
"v_and_b32 v15, s63, v19\n"
"v_mul_f32 v12, v12, v68\n"
"v_mfma_f32_16x16x32_bf16 v[200:203], a[84:87], a[100:103], v[200:203]\n"
"global_load_dwordx4 a[64:67], v0, s[34:35] offset:0 sc1\n"
"v_mul_f32 v13, v13, v69\n"
"v_mfma_f32_16x16x32_bf16 v[228:231], a[92:95], a[100:103], v[228:231]\n"
"ds_read_b128 a[100:103], v4 offset:1024\n"
"v_mul_f32 v14, v14, v70\n"
"v_mfma_f32_16x16x32_bf16 v[228:231], a[88:91], a[96:99], v[228:231]\n"
"ds_read_b128 a[96:99], v4 offset:0\n"
"v_mul_f32 v15, v15, v71\n"
"v_mfma_f32_16x16x32_bf16 v[232:235], a[88:91], a[104:107], v[232:235]\n"
"v_cvt_pk_bf16_f32 v26, v12, v13\n"
"v_cvt_pk_bf16_f32 v27, v14, v15\n"
"v_mfma_f32_16x16x32_bf16 v[232:235], a[92:95], a[108:111], v[232:235]\n"
"ds_write_b64 v7, v[26:27] offset:36872\n"
"v_mfma_f32_16x16x32_bf16 v[204:207], a[84:87], a[108:111], v[204:207]\n"
"ds_read_b128 a[108:111], v4 offset:3072\n"
"v_mfma_f32_16x16x32_bf16 v[204:207], a[80:83], a[104:107], v[204:207]\n"
"ds_read_b128 a[104:107], v4 offset:2048\n"
"v_mfma_f32_16x16x32_bf16 v[208:211], a[80:83], a[112:115], v[208:211]\n"
"global_load_dwordx4 v[120:123], v0, s[96:97] offset:-1024 sc1\n"
"v_mfma_f32_16x16x32_bf16 v[208:211], a[84:87], a[116:119], v[208:211]\n"
"global_load_dwordx4 a[68:71], v0, s[34:35] offset:1024 sc1\n"
"v_mfma_f32_16x16x32_bf16 v[236:239], a[92:95], a[116:119], v[236:239]\n"
"s_waitcnt lgkmcnt(6)\n"
"ds_read_b128 a[116:119], v4 offset:5120\n"
"v_lshlrev_b32 v12, 16, v20\n"
"v_mfma_f32_16x16x32_bf16 v[236:239], a[88:91], a[112:115], v[236:239]\n"
"ds_read_b128 a[112:115], v4 offset:4096\n"
"v_and_b32 v13, s63, v20\n"
"v_mfma_f32_16x16x32_bf16 v[240:243], a[88:91], a[120:123], v[240:243]\n"
"v_lshlrev_b32 v14, 16, v21\n"
"v_and_b32 v15, s63, v21\n"
"v_mfma_f32_16x16x32_bf16 v[240:243], a[92:95], a[124:127], v[240:243]\n"
"ds_read_b128 v[16:19], v7 offset:38912\n"
"v_mul_f32 v12, v12, v96\n"
"v_mfma_f32_16x16x32_bf16 v[212:215], a[84:87], a[124:127], v[212:215]\n"
"ds_read_b128 a[124:127], v4 offset:7168\n"
"v_mul_f32 v13, v13, v97\n"
"v_mfma_f32_16x16x32_bf16 v[212:215], a[80:83], a[120:123], v[212:215]\n"
"ds_read_b128 a[120:123], v4 offset:6144\n"
"v_mul_f32 v14, v14, v98\n"
"v_mfma_f32_16x16x32_bf16 v[216:219], a[80:83], a[128:131], v[216:219]\n"
"v_mul_f32 v15, v15, v99\n"
"v_mfma_f32_16x16x32_bf16 v[216:219], a[84:87], a[132:135], v[216:219]\n"
"global_load_dwordx4 a[72:75], v0, s[34:35] offset:2048 sc1\n"
"v_cvt_pk_bf16_f32 v28, v12, v13\n"
"v_mfma_f32_16x16x32_bf16 v[244:247], a[92:95], a[132:135], v[244:247]\n"
"ds_read_b128 a[132:135], v4 offset:9216\n"
"global_load_dwordx4 v[32:35], v0, s[96:97] offset:0 sc1\n"
"v_mfma_f32_16x16x32_bf16 v[244:247], a[88:91], a[128:131], v[244:247]\n"
"ds_read_b128 a[128:131], v4 offset:8192\n"
"v_mfma_f32_16x16x32_bf16 v[248:251], a[88:91], a[136:139], v[248:251]\n"
"v_cvt_pk_bf16_f32 v29, v14, v15\n"
"v_mfma_f32_16x16x32_bf16 v[248:251], a[92:95], a[140:143], v[248:251]\n"
"buffer_store_dwordx4 v[36:39], v6, s[52:55], s82 offen offset:2048 sc1\n"
"ds_write_b64 v8, v[28:29] offset:36864\n"
"v_mfma_f32_16x16x32_bf16 v[220:223], a[84:87], a[140:143], v[220:223]\n"
"ds_read_b128 a[140:143], v4 offset:11264\n"
"v_lshlrev_b32 v12, 16, v22\n"
"v_mfma_f32_16x16x32_bf16 v[220:223], a[80:83], a[136:139], v[220:223]\n"
"ds_read_b128 a[136:139], v4 offset:10240\n"
"v_and_b32 v13, s63, v22\n"
"v_mfma_f32_16x16x32_bf16 v[224:227], a[80:83], a[144:147], v[224:227]\n"
"v_lshlrev_b32 v14, 16, v23\n"
"v_and_b32 v15, s63, v23\n"
"v_mfma_f32_16x16x32_bf16 v[224:227], a[84:87], a[148:151], v[224:227]\n"
"global_load_dwordx4 a[76:79], v0, s[34:35] offset:3072 sc1\n"
"v_mul_f32 v12, v12, v124\n"
"v_mfma_f32_16x16x32_bf16 v[252:255], a[92:95], a[148:151], v[252:255]\n"
"ds_read_b128 a[148:151], v4 offset:13312\n"
"v_mul_f32 v13, v13, v125\n"
"v_mfma_f32_16x16x32_bf16 v[252:255], a[88:91], a[144:147], v[252:255]\n"
"ds_read_b128 a[144:147], v4 offset:12288\n"
"v_mul_f32 v14, v14, v126\n"
"v_mfma_f32_16x16x32_bf16 a[28:31], a[88:91], a[152:155], a[28:31]\n"
"global_load_dwordx4 v[60:63], v0, s[96:97] offset:1024 sc1\n"
"v_mul_f32 v15, v15, v127\n"
"v_mfma_f32_16x16x32_bf16 a[28:31], a[92:95], a[156:159], a[28:31]\n"
"v_cvt_pk_bf16_f32 v30, v12, v13\n"
"v_cvt_pk_bf16_f32 v31, v14, v15\n"
"v_mfma_f32_16x16x32_bf16 a[24:27], a[84:87], a[156:159], a[24:27]\n"
"ds_read_b128 a[156:159], v4 offset:15360\n"
"v_mfma_f32_16x16x32_bf16 a[24:27], a[80:83], a[152:155], a[24:27]\n"
"s_waitcnt vmcnt(16) lgkmcnt(13)\n"
"ds_read_b128 a[152:155], v4 offset:14336\n"
"v_mfma_f32_16x16x32_bf16 v[144:147], a[32:35], a[96:99], v[144:147]\n"
"s_add_u32 s34, s36, 1503232\n"
"ds_write_b64 v8, v[30:31] offset:36872\n"
"v_mfma_f32_16x16x32_bf16 v[144:147], a[36:39], a[100:103], v[144:147]\n"
"global_load_dwordx4 a[80:83], v0, s[34:35] offset:0 sc1\n"
"ds_read_b128 v[40:43], v2 offset:36864\n"
"v_mfma_f32_16x16x32_bf16 v[172:175], a[44:47], a[100:103], v[172:175]\n"
"s_waitcnt lgkmcnt(13)\n"
"ds_read_b128 v[68:71], v2 offset:37888\n"
"v_lshlrev_b32 v12, 16, v16\n"
"v_mfma_f32_16x16x32_bf16 v[172:175], a[40:43], a[96:99], v[172:175]\n"
"v_and_b32 v13, s63, v16\n"
"v_lshlrev_b32 v14, 16, v17\n"
"v_mfma_f32_16x16x32_bf16 v[176:179], a[40:43], a[104:107], v[176:179]\n"
"v_and_b32 v15, s63, v17\n"
"v_xor_b32 v20, 64, v7\n"
"v_mfma_f32_16x16x32_bf16 v[176:179], a[44:47], a[108:111], v[176:179]\n"
"ds_read_b128 v[20:23], v20 offset:38912\n"
"v_mul_f32 v12, v12, v44\n"
"v_mfma_f32_16x16x32_bf16 v[148:151], a[36:39], a[108:111], v[148:151]\n"
"v_mul_f32 v13, v13, v45\n"
"v_mul_f32 v14, v14, v46\n"
"v_mfma_f32_16x16x32_bf16 v[148:151], a[32:35], a[104:107], v[148:151]\n"
"s_waitcnt lgkmcnt(13)\n"
"global_load_dwordx4 v[96:99], v0, s[96:97] offset:2048 sc1\n"
"v_mul_f32 v15, v15, v47\n"
"v_mfma_f32_16x16x32_bf16 v[152:155], a[32:35], a[112:115], v[152:155]\n"
"v_cvt_pk_bf16_f32 v24, v12, v13\n"
"v_cvt_pk_bf16_f32 v25, v14, v15\n"
"v_mfma_f32_16x16x32_bf16 v[152:155], a[36:39], a[116:119], v[152:155]\n"
"global_load_dwordx4 a[84:87], v0, s[34:35] offset:1024 sc1\n"
"v_mfma_f32_16x16x32_bf16 v[180:183], a[44:47], a[116:119], v[180:183]\n"
"ds_write_b64 v7, v[24:25] offset:38912\n"
"v_mfma_f32_16x16x32_bf16 v[180:183], a[40:43], a[112:115], v[180:183]\n"
"v_lshlrev_b32 v12, 16, v18\n"
"v_and_b32 v13, s63, v18\n"
"v_mfma_f32_16x16x32_bf16 v[184:187], a[40:43], a[120:123], v[184:187]\n"
"v_lshlrev_b32 v14, 16, v19\n"
"v_and_b32 v15, s63, v19\n"
"v_mfma_f32_16x16x32_bf16 v[184:187], a[44:47], a[124:127], v[184:187]\n"
"v_mul_f32 v12, v12, v72\n"
"v_mul_f32 v13, v13, v73\n"
"v_mfma_f32_16x16x32_bf16 v[156:159], a[36:39], a[124:127], v[156:159]\n"
"v_mul_f32 v14, v14, v74\n"
"v_mul_f32 v15, v15, v75\n"
"v_mfma_f32_16x16x32_bf16 v[156:159], a[32:35], a[120:123], v[156:159]\n"
"s_waitcnt lgkmcnt(9)\n"
"v_cvt_pk_bf16_f32 v26, v12, v13\n"
"v_cvt_pk_bf16_f32 v27, v14, v15\n"
"v_mfma_f32_16x16x32_bf16 v[160:163], a[32:35], a[128:131], v[160:163]\n"
"buffer_store_dwordx4 v[64:67], v6, s[52:55], s83 offen offset:3072 sc1\n"
"ds_write_b64 v7, v[26:27] offset:38920\n"
"v_mfma_f32_16x16x32_bf16 v[160:163], a[36:39], a[132:135], v[160:163]\n"
"s_waitcnt lgkmcnt(2)\n"
"global_load_dwordx4 a[88:91], v0, s[34:35] offset:2048 sc1\n"
"v_lshlrev_b32 v12, 16, v20\n"
"v_mfma_f32_16x16x32_bf16 v[188:191], a[44:47], a[132:135], v[188:191]\n"
"global_load_dwordx4 v[124:127], v0, s[96:97] offset:3072 sc1\n"
"v_and_b32 v13, s63, v20\n"
"v_mfma_f32_16x16x32_bf16 v[188:191], a[40:43], a[128:131], v[188:191]\n"
"v_lshlrev_b32 v14, 16, v21\n"
"v_and_b32 v15, s63, v21\n"
"v_mfma_f32_16x16x32_bf16 v[192:195], a[40:43], a[136:139], v[192:195]\n"
"v_mul_f32 v12, v12, v100\n"
"v_mul_f32 v13, v13, v101\n"
"v_mfma_f32_16x16x32_bf16 v[192:195], a[44:47], a[140:143], v[192:195]\n"
"v_mul_f32 v14, v14, v102\n"
"v_mul_f32 v15, v15, v103\n"
"v_mfma_f32_16x16x32_bf16 v[164:167], a[36:39], a[140:143], v[164:167]\n"
"v_cvt_pk_bf16_f32 v28, v12, v13\n"
"v_cvt_pk_bf16_f32 v29, v14, v15\n"
"v_mfma_f32_16x16x32_bf16 v[164:167], a[32:35], a[136:139], v[164:167]\n"
"ds_write_b64 v8, v[28:29] offset:38912\n"
"v_mfma_f32_16x16x32_bf16 v[168:171], a[32:35], a[144:147], v[168:171]\n"
"v_lshlrev_b32 v12, 16, v22\n"
"v_mfma_f32_16x16x32_bf16 v[168:171], a[36:39], a[148:151], v[168:171]\n"
"global_load_dwordx4 a[92:95], v0, s[34:35] offset:3072 sc1\n"
"v_and_b32 v13, s63, v22\n"
"v_mfma_f32_16x16x32_bf16 v[196:199], a[44:47], a[148:151], v[196:199]\n"
"v_lshlrev_b32 v14, 16, v23\n"
"v_and_b32 v15, s63, v23\n"
"v_mfma_f32_16x16x32_bf16 v[196:199], a[40:43], a[144:147], v[196:199]\n"
"v_mul_f32 v12, v12, v128\n"
"v_mfma_f32_16x16x32_bf16 a[20:23], a[40:43], a[152:155], a[20:23]\n"
"v_mul_f32 v13, v13, v129\n"
"v_mfma_f32_16x16x32_bf16 a[20:23], a[44:47], a[156:159], a[20:23]\n"
"v_mul_f32 v14, v14, v130\n"
"v_mul_f32 v15, v15, v131\n"
"v_mfma_f32_16x16x32_bf16 a[16:19], a[36:39], a[156:159], a[16:19]\n"
"v_cvt_pk_bf16_f32 v30, v12, v13\n"
"v_cvt_pk_bf16_f32 v31, v14, v15\n"
"v_mfma_f32_16x16x32_bf16 a[16:19], a[32:35], a[152:155], a[16:19]\n"
"s_waitcnt vmcnt(16)\n"
"ds_write_b64 v8, v[30:31] offset:38920\n"
"v_mfma_f32_16x16x32_bf16 v[200:203], a[48:51], a[96:99], v[200:203]\n"
"s_add_u32 s34, s36, 1540096\n"
"ds_read_b128 v[44:47], v2 offset:38912\n"
"v_mfma_f32_16x16x32_bf16 v[200:203], a[52:55], a[100:103], v[200:203]\n"
"global_load_dwordx4 a[32:35], v0, s[34:35] offset:0 sc1\n"
"ds_read_b128 v[72:75], v2 offset:39936\n"
"v_mfma_f32_16x16x32_bf16 v[228:231], a[60:63], a[100:103], v[228:231]\n"
"ds_read_b128 a[100:103], v5 offset:1024\n"
"v_accvgpr_read_b32 v16, a160\n"
"v_mfma_f32_16x16x32_bf16 v[228:231], a[56:59], a[96:99], v[228:231]\n"
"ds_read_b128 a[96:99], v5 offset:0\n"
"v_accvgpr_read_b32 v17, a161\n"
"v_mfma_f32_16x16x32_bf16 v[232:235], a[56:59], a[104:107], v[232:235]\n"
"v_accvgpr_read_b32 v18, a162\n"
"v_accvgpr_read_b32 v19, a163\n"
"v_mfma_f32_16x16x32_bf16 v[232:235], a[60:63], a[108:111], v[232:235]\n"
"v_lshlrev_b32 v12, 16, v16\n"
"v_mfma_f32_16x16x32_bf16 v[204:207], a[52:55], a[108:111], v[204:207]\n"
"ds_read_b128 a[108:111], v5 offset:3072\n"
"v_and_b32 v13, s63, v16\n"
"v_mfma_f32_16x16x32_bf16 v[204:207], a[48:51], a[104:107], v[204:207]\n"
"ds_read_b128 a[104:107], v5 offset:2048\n"
"v_lshlrev_b32 v14, 16, v17\n"
"v_mfma_f32_16x16x32_bf16 v[208:211], a[48:51], a[112:115], v[208:211]\n"
"v_and_b32 v15, s63, v17\n"
"v_mul_f32 v12, v12, v48\n"
"v_mfma_f32_16x16x32_bf16 v[208:211], a[52:55], a[116:119], v[208:211]\n"
"global_load_dwordx4 a[36:39], v0, s[34:35] offset:1024 sc1\n"
"v_mul_f32 v13, v13, v49\n"
"v_mfma_f32_16x16x32_bf16 v[236:239], a[60:63], a[116:119], v[236:239]\n"
"ds_read_b128 a[116:119], v5 offset:5120\n"
"v_mul_f32 v14, v14, v50\n"
"v_mfma_f32_16x16x32_bf16 v[236:239], a[56:59], a[112:115], v[236:239]\n"
"ds_read_b128 a[112:115], v5 offset:4096\n"
"v_mul_f32 v15, v15, v51\n"
"v_mfma_f32_16x16x32_bf16 v[240:243], a[56:59], a[120:123], v[240:243]\n"
"v_cvt_pk_bf16_f32 v24, v12, v13\n"
"v_cvt_pk_bf16_f32 v25, v14, v15\n"
"v_mfma_f32_16x16x32_bf16 v[240:243], a[60:63], a[124:127], v[240:243]\n"
"buffer_store_dwordx4 v[40:43], v6, s[52:55], s84 offen offset:0 sc1\n"
"ds_write_b64 v7, v[24:25] offset:32768\n"
"v_mfma_f32_16x16x32_bf16 v[212:215], a[52:55], a[124:127], v[212:215]\n"
"ds_read_b128 a[124:127], v5 offset:7168\n"
"v_lshlrev_b32 v12, 16, v18\n"
"v_mfma_f32_16x16x32_bf16 v[212:215], a[48:51], a[120:123], v[212:215]\n"
"ds_read_b128 a[120:123], v5 offset:6144\n"
"v_and_b32 v13, s63, v18\n"
"v_mfma_f32_16x16x32_bf16 v[216:219], a[48:51], a[128:131], v[216:219]\n"
"v_lshlrev_b32 v14, 16, v19\n"
"v_and_b32 v15, s63, v19\n"
"v_mfma_f32_16x16x32_bf16 v[216:219], a[52:55], a[132:135], v[216:219]\n"
"global_load_dwordx4 a[40:43], v0, s[34:35] offset:2048 sc1\n"
"v_mul_f32 v12, v12, v76\n"
"v_mfma_f32_16x16x32_bf16 v[244:247], a[60:63], a[132:135], v[244:247]\n"
"s_add_u32 s96, s36, 1560576\n"
"ds_read_b128 a[132:135], v5 offset:9216\n"
"v_mul_f32 v13, v13, v77\n"
"v_mfma_f32_16x16x32_bf16 v[244:247], a[56:59], a[128:131], v[244:247]\n"
"ds_read_b128 a[128:131], v5 offset:8192\n"
"v_mul_f32 v14, v14, v78\n"
"v_mfma_f32_16x16x32_bf16 v[248:251], a[56:59], a[136:139], v[248:251]\n"
"global_load_dwordx4 v[100:103], v0, s[96:97] offset:-4096 sc1\n"
"v_mul_f32 v15, v15, v79\n"
"v_mfma_f32_16x16x32_bf16 v[248:251], a[60:63], a[140:143], v[248:251]\n"
"v_cvt_pk_bf16_f32 v26, v12, v13\n"
"v_cvt_pk_bf16_f32 v27, v14, v15\n"
"v_mfma_f32_16x16x32_bf16 v[220:223], a[52:55], a[140:143], v[220:223]\n"
"ds_read_b128 a[140:143], v5 offset:11264\n"
"v_mfma_f32_16x16x32_bf16 v[220:223], a[48:51], a[136:139], v[220:223]\n"
"ds_read_b128 a[136:139], v5 offset:10240\n"
"v_mfma_f32_16x16x32_bf16 v[224:227], a[48:51], a[144:147], v[224:227]\n"
"ds_write_b64 v7, v[26:27] offset:32776\n"
"v_mfma_f32_16x16x32_bf16 v[224:227], a[52:55], a[148:151], v[224:227]\n"
"global_load_dwordx4 a[44:47], v0, s[34:35] offset:3072 sc1\n"
"v_accvgpr_read_b32 v16, a164\n"
"v_mfma_f32_16x16x32_bf16 v[252:255], a[60:63], a[148:151], v[252:255]\n"
"ds_read_b128 a[148:151], v5 offset:13312\n"
"v_accvgpr_read_b32 v17, a165\n"
"v_mfma_f32_16x16x32_bf16 v[252:255], a[56:59], a[144:147], v[252:255]\n"
"ds_read_b128 a[144:147], v5 offset:12288\n"
"v_accvgpr_read_b32 v18, a166\n"
"v_mfma_f32_16x16x32_bf16 a[28:31], a[56:59], a[152:155], a[28:31]\n"
"v_accvgpr_read_b32 v19, a167\n"
"v_lshlrev_b32 v12, 16, v16\n"
"v_mfma_f32_16x16x32_bf16 a[28:31], a[60:63], a[156:159], a[28:31]\n"
"global_load_dwordx4 v[128:131], v0, s[96:97] offset:-3072 sc1\n"
"v_and_b32 v13, s63, v16\n"
"v_mfma_f32_16x16x32_bf16 a[24:27], a[52:55], a[156:159], a[24:27]\n"
"ds_read_b128 a[156:159], v5 offset:15360\n"
"v_lshlrev_b32 v14, 16, v17\n"
"v_mfma_f32_16x16x32_bf16 a[24:27], a[48:51], a[152:155], a[24:27]\n"
"s_waitcnt vmcnt(15) lgkmcnt(13)\n"
"ds_read_b128 a[152:155], v5 offset:14336\n"
"v_and_b32 v15, s63, v17\n"
"v_mfma_f32_16x16x32_bf16 v[144:147], a[64:67], a[96:99], v[144:147]\n"
"s_add_u32 s34, s36, 1544192\n"
"v_mul_f32 v12, v12, v104\n"
"v_mul_f32 v13, v13, v105\n"
"v_mfma_f32_16x16x32_bf16 v[144:147], a[68:71], a[100:103], v[144:147]\n"
"global_load_dwordx4 a[48:51], v0, s[34:35] offset:0 sc1\n"
"v_mul_f32 v14, v14, v106\n"
"v_mfma_f32_16x16x32_bf16 v[172:175], a[76:79], a[100:103], v[172:175]\n"
"v_mul_f32 v15, v15, v107\n"
"v_cvt_pk_bf16_f32 v28, v12, v13\n"
"v_mfma_f32_16x16x32_bf16 v[172:175], a[72:75], a[96:99], v[172:175]\n"
"v_cvt_pk_bf16_f32 v29, v14, v15\n"
"v_mfma_f32_16x16x32_bf16 v[176:179], a[72:75], a[104:107], v[176:179]\n"
"ds_write_b64 v8, v[28:29] offset:32768\n"
"v_mfma_f32_16x16x32_bf16 v[176:179], a[76:79], a[108:111], v[176:179]\n"
"v_lshlrev_b32 v12, 16, v18\n"
"v_and_b32 v13, s63, v18\n"
"v_mfma_f32_16x16x32_bf16 v[148:151], a[68:71], a[108:111], v[148:151]\n"
"v_lshlrev_b32 v14, 16, v19\n"
"v_and_b32 v15, s63, v19\n"
"v_mfma_f32_16x16x32_bf16 v[148:151], a[64:67], a[104:107], v[148:151]\n"
"s_waitcnt lgkmcnt(10)\n"
"v_mul_f32 v12, v12, v132\n"
"v_mul_f32 v13, v13, v133\n"
"v_mfma_f32_16x16x32_bf16 v[152:155], a[64:67], a[112:115], v[152:155]\n"
"buffer_store_dwordx4 v[68:71], v6, s[52:55], s85 offen offset:1024 sc1\n"
"global_load_dwordx4 v[36:39], v0, s[96:97] offset:-2048 sc1\n"
"v_mul_f32 v14, v14, v134\n"
"v_mul_f32 v15, v15, v135\n"
"v_mfma_f32_16x16x32_bf16 v[152:155], a[68:71], a[116:119], v[152:155]\n"
"global_load_dwordx4 a[52:55], v0, s[34:35] offset:1024 sc1\n"
"v_cvt_pk_bf16_f32 v30, v12, v13\n"
"v_mfma_f32_16x16x32_bf16 v[180:183], a[76:79], a[116:119], v[180:183]\n"
"v_cvt_pk_bf16_f32 v31, v14, v15\n"
"v_mfma_f32_16x16x32_bf16 v[180:183], a[72:75], a[112:115], v[180:183]\n"
"ds_write_b64 v8, v[30:31] offset:32776\n"
"v_mfma_f32_16x16x32_bf16 v[184:187], a[72:75], a[120:123], v[184:187]\n"
"ds_read_b128 v[48:51], v2 offset:32768\n"
"v_mfma_f32_16x16x32_bf16 v[184:187], a[76:79], a[124:127], v[184:187]\n"
"ds_read_b128 v[76:79], v2 offset:33792\n"
"v_accvgpr_read_b32 v16, a168\n"
"v_mfma_f32_16x16x32_bf16 v[156:159], a[68:71], a[124:127], v[156:159]\n"
"v_accvgpr_read_b32 v17, a169\n"
"v_accvgpr_read_b32 v18, a170\n"
"v_mfma_f32_16x16x32_bf16 v[156:159], a[64:67], a[120:123], v[156:159]\n"
"s_waitcnt lgkmcnt(9)\n"
"v_accvgpr_read_b32 v19, a171\n"
"v_lshlrev_b32 v12, 16, v16\n"
"v_mfma_f32_16x16x32_bf16 v[160:163], a[64:67], a[128:131], v[160:163]\n"
"v_and_b32 v13, s63, v16\n"
"v_lshlrev_b32 v14, 16, v17\n"
"v_mfma_f32_16x16x32_bf16 v[160:163], a[68:71], a[132:135], v[160:163]\n"
"global_load_dwordx4 a[56:59], v0, s[34:35] offset:2048 sc1\n"
"v_and_b32 v15, s63, v17\n"
"v_mfma_f32_16x16x32_bf16 v[188:191], a[76:79], a[132:135], v[188:191]\n"
"global_load_dwordx4 v[64:67], v0, s[96:97] offset:-1024 sc1\n"
"v_mul_f32 v12, v12, v52\n"
"v_mfma_f32_16x16x32_bf16 v[188:191], a[72:75], a[128:131], v[188:191]\n"
"v_mul_f32 v13, v13, v53\n"
"v_mul_f32 v14, v14, v54\n"
"v_mfma_f32_16x16x32_bf16 v[192:195], a[72:75], a[136:139], v[192:195]\n"
"v_mul_f32 v15, v15, v55\n"
"v_mfma_f32_16x16x32_bf16 v[192:195], a[76:79], a[140:143], v[192:195]\n"
"v_cvt_pk_bf16_f32 v24, v12, v13\n"
"v_cvt_pk_bf16_f32 v25, v14, v15\n"
"v_mfma_f32_16x16x32_bf16 v[164:167], a[68:71], a[140:143], v[164:167]\n"
"ds_write_b64 v7, v[24:25] offset:34816\n"
"v_mfma_f32_16x16x32_bf16 v[164:167], a[64:67], a[136:139], v[164:167]\n"
"s_waitcnt lgkmcnt(5)\n"
"v_lshlrev_b32 v12, 16, v18\n"
"v_and_b32 v13, s63, v18\n"
"v_mfma_f32_16x16x32_bf16 v[168:171], a[64:67], a[144:147], v[168:171]\n"
"v_lshlrev_b32 v14, 16, v19\n"
"v_and_b32 v15, s63, v19\n"
"v_mfma_f32_16x16x32_bf16 v[168:171], a[68:71], a[148:151], v[168:171]\n"
"global_load_dwordx4 a[60:63], v0, s[34:35] offset:3072 sc1\n"
"v_mul_f32 v12, v12, v80\n"
"v_mfma_f32_16x16x32_bf16 v[196:199], a[76:79], a[148:151], v[196:199]\n"
"v_mul_f32 v13, v13, v81\n"
"v_mul_f32 v14, v14, v82\n"
"v_mfma_f32_16x16x32_bf16 v[196:199], a[72:75], a[144:147], v[196:199]\n"
"v_mul_f32 v15, v15, v83\n"
"v_cvt_pk_bf16_f32 v26, v12, v13\n"
"v_mfma_f32_16x16x32_bf16 a[20:23], a[72:75], a[152:155], a[20:23]\n"
"global_load_dwordx4 v[104:107], v0, s[96:97] offset:0 sc1\n"
"v_cvt_pk_bf16_f32 v27, v14, v15\n"
"v_mfma_f32_16x16x32_bf16 a[20:23], a[76:79], a[156:159], a[20:23]\n"
"ds_write_b64 v7, v[26:27] offset:34824\n"
"v_mfma_f32_16x16x32_bf16 a[16:19], a[68:71], a[156:159], a[16:19]\n"
"v_accvgpr_read_b32 v16, a172\n"
"v_mfma_f32_16x16x32_bf16 a[16:19], a[64:67], a[152:155], a[16:19]\n"
"s_waitcnt vmcnt(15)\n"
"v_accvgpr_read_b32 v17, a173\n"
"v_accvgpr_read_b32 v18, a174\n"
"v_mfma_f32_16x16x32_bf16 v[200:203], a[80:83], a[96:99], v[200:203]\n"
"s_add_u32 s34, s36, 1564672\n"
"v_accvgpr_read_b32 v19, a175\n"
"v_lshlrev_b32 v12, 16, v16\n"
"v_mfma_f32_16x16x32_bf16 v[200:203], a[84:87], a[100:103], v[200:203]\n"
"global_load_dwordx4 a[64:67], v0, s[34:35] offset:0 sc1\n"
"v_and_b32 v13, s63, v16\n"
"v_mfma_f32_16x16x32_bf16 v[228:231], a[92:95], a[100:103], v[228:231]\n"
"ds_read_b128 a[100:103], v2 offset:17408\n"
"v_lshlrev_b32 v14, 16, v17\n"
"v_mfma_f32_16x16x32_bf16 v[228:231], a[88:91], a[96:99], v[228:231]\n"
"ds_read_b128 a[96:99], v2 offset:16384\n"
"v_and_b32 v15, s63, v17\n"
"v_mfma_f32_16x16x32_bf16 v[232:235], a[88:91], a[104:107], v[232:235]\n"
"v_mul_f32 v12, v12, v108\n"
"v_mul_f32 v13, v13, v109\n"
"v_mfma_f32_16x16x32_bf16 v[232:235], a[92:95], a[108:111], v[232:235]\n"
"buffer_store_dwordx4 v[44:47], v6, s[52:55], s86 offen offset:2048 sc1\n"
"global_load_dwordx4 v[132:135], v0, s[96:97] offset:1024 sc1\n"
"v_mul_f32 v14, v14, v110\n"
"v_mul_f32 v15, v15, v111\n"
"v_mfma_f32_16x16x32_bf16 v[204:207], a[84:87], a[108:111], v[204:207]\n"
"ds_read_b128 a[108:111], v2 offset:19456\n"
"v_cvt_pk_bf16_f32 v28, v12, v13\n"
"v_mfma_f32_16x16x32_bf16 v[204:207], a[80:83], a[104:107], v[204:207]\n"
"ds_read_b128 a[104:107], v2 offset:18432\n"
"v_cvt_pk_bf16_f32 v29, v14, v15\n"
"v_mfma_f32_16x16x32_bf16 v[208:211], a[80:83], a[112:115], v[208:211]\n"
"L_st10887:\n"
"s_cbranch_execz L_retC\n"
"ds_write_b64 v8, v[28:29] offset:34816\n"
"v_mfma_f32_16x16x32_bf16 v[208:211], a[84:87], a[116:119], v[208:211]\n"
"global_load_dwordx4 a[68:71], v0, s[34:35] offset:1024 sc1\n"
"v_mfma_f32_16x16x32_bf16 v[236:239], a[92:95], a[116:119], v[236:239]\n"
"ds_read_b128 a[116:119], v2 offset:21504\n"
"v_mfma_f32_16x16x32_bf16 v[236:239], a[88:91], a[112:115], v[236:239]\n"
"ds_read_b128 a[112:115], v2 offset:20480\n"
"v_lshlrev_b32 v12, 16, v18\n"
"v_mfma_f32_16x16x32_bf16 v[240:243], a[88:91], a[120:123], v[240:243]\n"
"v_and_b32 v13, s63, v18\n"
"v_mfma_f32_16x16x32_bf16 v[240:243], a[92:95], a[124:127], v[240:243]\n"
"v_lshlrev_b32 v14, 16, v19\n"
"v_and_b32 v15, s63, v19\n"
"v_mfma_f32_16x16x32_bf16 v[212:215], a[84:87], a[124:127], v[212:215]\n"
"ds_read_b128 a[124:127], v2 offset:23552\n"
"v_mul_f32 v12, v12, v136\n"
"v_mfma_f32_16x16x32_bf16 v[212:215], a[80:83], a[120:123], v[212:215]\n"
"ds_read_b128 a[120:123], v2 offset:22528\n"
"v_mul_f32 v13, v13, v137\n"
"v_mfma_f32_16x16x32_bf16 v[216:219], a[80:83], a[128:131], v[216:219]\n"
"v_mul_f32 v14, v14, v138\n"
"v_mul_f32 v15, v15, v139\n"
"v_mfma_f32_16x16x32_bf16 v[216:219], a[84:87], a[132:135], v[216:219]\n"
"global_load_dwordx4 a[72:75], v0, s[34:35] offset:2048 sc1\n"
"v_cvt_pk_bf16_f32 v30, v12, v13\n"
"v_mfma_f32_16x16x32_bf16 v[244:247], a[92:95], a[132:135], v[244:247]\n"
"ds_read_b128 a[132:135], v2 offset:25600\n"
"v_cvt_pk_bf16_f32 v31, v14, v15\n"
"v_mfma_f32_16x16x32_bf16 v[244:247], a[88:91], a[128:131], v[244:247]\n"
"ds_read_b128 a[128:131], v2 offset:24576\n"
"v_mfma_f32_16x16x32_bf16 v[248:251], a[88:91], a[136:139], v[248:251]\n"
"ds_write_b64 v8, v[30:31] offset:34824\n"
"v_mfma_f32_16x16x32_bf16 v[248:251], a[92:95], a[140:143], v[248:251]\n"
"ds_read_b128 v[52:55], v2 offset:34816\n"
"v_mfma_f32_16x16x32_bf16 v[220:223], a[84:87], a[140:143], v[220:223]\n"
"ds_read_b128 a[140:143], v2 offset:27648\n"
"v_mfma_f32_16x16x32_bf16 v[220:223], a[80:83], a[136:139], v[220:223]\n"
"ds_read_b128 a[136:139], v2 offset:26624\n"
"v_mfma_f32_16x16x32_bf16 v[224:227], a[80:83], a[144:147], v[224:227]\n"
"ds_read_b128 v[80:83], v2 offset:35840\n"
"v_accvgpr_read_b32 v16, a176\n"
"v_mfma_f32_16x16x32_bf16 v[224:227], a[84:87], a[148:151], v[224:227]\n"
"global_load_dwordx4 a[76:79], v0, s[34:35] offset:3072 sc1\n"
"v_accvgpr_read_b32 v17, a177\n"
"v_mfma_f32_16x16x32_bf16 v[252:255], a[92:95], a[148:151], v[252:255]\n"
"ds_read_b128 a[148:151], v2 offset:29696\n"
"v_accvgpr_read_b32 v18, a178\n"
"v_mfma_f32_16x16x32_bf16 v[252:255], a[88:91], a[144:147], v[252:255]\n"
"ds_read_b128 a[144:147], v2 offset:28672\n"
"v_accvgpr_read_b32 v19, a179\n"
"v_mfma_f32_16x16x32_bf16 a[28:31], a[88:91], a[152:155], a[28:31]\n"
"v_lshlrev_b32 v12, 16, v16\n"
"v_and_b32 v13, s63, v16\n"
"v_mfma_f32_16x16x32_bf16 a[28:31], a[92:95], a[156:159], a[28:31]\n"
"v_lshlrev_b32 v14, 16, v17\n"
"v_and_b32 v15, s63, v17\n"
"v_mfma_f32_16x16x32_bf16 a[24:27], a[84:87], a[156:159], a[24:27]\n"
"ds_read_b128 a[156:159], v2 offset:31744\n"
"v_mul_f32 v12, v12, v56\n"
"v_mfma_f32_16x16x32_bf16 a[24:27], a[80:83], a[152:155], a[24:27]\n"
"s_waitcnt vmcnt(15) lgkmcnt(15)\n"
"ds_read_b128 a[152:155], v2 offset:30720\n"
"v_mul_f32 v13, v13, v57\n"
"v_mfma_f32_16x16x32_bf16 v[144:147], a[32:35], a[96:99], v[144:147]\n"
"buffer_store_dwordx4 v[72:75], v6, s[52:55], s87 offen offset:3072 sc1\n"
"s_add_u32 s34, s36, 1568768\n"
"v_mul_f32 v14, v14, v58\n"
"v_mul_f32 v15, v15, v59\n"
"v_mfma_f32_16x16x32_bf16 v[144:147], a[36:39], a[100:103], v[144:147]\n"
"global_load_dwordx4 a[80:83], v0, s[34:35] offset:0 sc1\n"
"v_cvt_pk_bf16_f32 v24, v12, v13\n"
"v_mfma_f32_16x16x32_bf16 v[172:175], a[44:47], a[100:103], v[172:175]\n"
"v_cvt_pk_bf16_f32 v25, v14, v15\n"
"v_mfma_f32_16x16x32_bf16 v[172:175], a[40:43], a[96:99], v[172:175]\n"
"ds_write_b64 v7, v[24:25] offset:36864\n"
"v_mfma_f32_16x16x32_bf16 v[176:179], a[40:43], a[104:107], v[176:179]\n"
"global_load_dwordx4 a[84:87], v0, s[34:35] offset:1024 sc1\n"
"v_lshlrev_b32 v12, 16, v18\n"
"v_mfma_f32_16x16x32_bf16 v[176:179], a[44:47], a[108:111], v[176:179]\n"
"global_load_dwordx4 v[108:111], v0, s[96:97] offset:2048 sc1\n"
"v_and_b32 v13, s63, v18\n"
"v_mfma_f32_16x16x32_bf16 v[148:151], a[36:39], a[108:111], v[148:151]\n"
"v_lshlrev_b32 v14, 16, v19\n"
"v_and_b32 v15, s63, v19\n"
"v_mfma_f32_16x16x32_bf16 v[148:151], a[32:35], a[104:107], v[148:151]\n"
"s_waitcnt lgkmcnt(12)\n"
"global_load_dwordx4 a[88:91], v0, s[34:35] offset:2048 sc1\n"
"v_mul_f32 v12, v12, v84\n"
"v_mfma_f32_16x16x32_bf16 v[152:155], a[32:35], a[112:115], v[152:155]\n"
"v_mul_f32 v13, v13, v85\n"
"v_mul_f32 v14, v14, v86\n"
"v_mfma_f32_16x16x32_bf16 v[152:155], a[36:39], a[116:119], v[152:155]\n"
"v_mul_f32 v15, v15, v87\n"
"v_cvt_pk_bf16_f32 v26, v12, v13\n"
"v_mfma_f32_16x16x32_bf16 v[180:183], a[44:47], a[116:119], v[180:183]\n"
"global_load_dwordx4 a[92:95], v0, s[34:35] offset:3072 sc1\n"
"v_cvt_pk_bf16_f32 v27, v14, v15\n"
"v_mfma_f32_16x16x32_bf16 v[180:183], a[40:43], a[112:115], v[180:183]\n"
"ds_write_b64 v7, v[26:27] offset:36872\n"
"v_mfma_f32_16x16x32_bf16 v[184:187], a[40:43], a[120:123], v[184:187]\n"
"v_accvgpr_read_b32 v16, a180\n"
"v_accvgpr_read_b32 v17, a181\n"
"v_mfma_f32_16x16x32_bf16 v[184:187], a[44:47], a[124:127], v[184:187]\n"
"v_accvgpr_read_b32 v18, a182\n"
"v_accvgpr_read_b32 v19, a183\n"
"v_mfma_f32_16x16x32_bf16 v[156:159], a[36:39], a[124:127], v[156:159]\n"
"global_load_dwordx4 v[136:139], v0, s[96:97] offset:3072 sc1\n"
"v_lshlrev_b32 v12, 16, v16\n"
"v_mfma_f32_16x16x32_bf16 v[156:159], a[32:35], a[120:123], v[156:159]\n"
"s_waitcnt lgkmcnt(7)\n"
"v_and_b32 v13, s63, v16\n"
"v_lshlrev_b32 v14, 16, v17\n"
"v_mfma_f32_16x16x32_bf16 v[160:163], a[32:35], a[128:131], v[160:163]\n"
"v_and_b32 v15, s63, v17\n"
"v_mul_f32 v12, v12, v112\n"
"v_mfma_f32_16x16x32_bf16 v[160:163], a[36:39], a[132:135], v[160:163]\n"
"v_mul_f32 v13, v13, v113\n"
"v_mul_f32 v14, v14, v114\n"
"v_mfma_f32_16x16x32_bf16 v[188:191], a[44:47], a[132:135], v[188:191]\n"
"v_mul_f32 v15, v15, v115\n"
"v_mfma_f32_16x16x32_bf16 v[188:191], a[40:43], a[128:131], v[188:191]\n"
"v_cvt_pk_bf16_f32 v28, v12, v13\n"
"v_cvt_pk_bf16_f32 v29, v14, v15\n"
"v_mfma_f32_16x16x32_bf16 v[192:195], a[40:43], a[136:139], v[192:195]\n"
"ds_write_b64 v8, v[28:29] offset:36864\n"
"v_mfma_f32_16x16x32_bf16 v[192:195], a[44:47], a[140:143], v[192:195]\n"
"v_lshlrev_b32 v12, 16, v18\n"
"v_mfma_f32_16x16x32_bf16 v[164:167], a[36:39], a[140:143], v[164:167]\n"
"v_and_b32 v13, s63, v18\n"
"v_lshlrev_b32 v14, 16, v19\n"
"v_mfma_f32_16x16x32_bf16 v[164:167], a[32:35], a[136:139], v[164:167]\n"
"s_waitcnt lgkmcnt(3)\n"
"v_and_b32 v15, s63, v19\n"
"v_mfma_f32_16x16x32_bf16 v[168:171], a[32:35], a[144:147], v[168:171]\n"
"v_mul_f32 v12, v12, v140\n"
"v_mfma_f32_16x16x32_bf16 v[168:171], a[36:39], a[148:151], v[168:171]\n"
"v_mul_f32 v13, v13, v141\n"
"v_mfma_f32_16x16x32_bf16 v[196:199], a[44:47], a[148:151], v[196:199]\n"
"v_mul_f32 v14, v14, v142\n"
"v_mul_f32 v15, v15, v143\n"
"v_mfma_f32_16x16x32_bf16 v[196:199], a[40:43], a[144:147], v[196:199]\n"
"v_cvt_pk_bf16_f32 v30, v12, v13\n"
"v_mfma_f32_16x16x32_bf16 a[20:23], a[40:43], a[152:155], a[20:23]\n"
"v_cvt_pk_bf16_f32 v31, v14, v15\n"
"v_mfma_f32_16x16x32_bf16 a[20:23], a[44:47], a[156:159], a[20:23]\n"
"buffer_store_dwordx4 v[48:51], v6, s[52:55], s88 offen offset:0 sc1\n"
"ds_write_b64 v8, v[30:31] offset:36872\n"
"v_mfma_f32_16x16x32_bf16 a[16:19], a[36:39], a[156:159], a[16:19]\n"
"ds_read_b128 v[56:59], v2 offset:36864\n"
"v_mfma_f32_16x16x32_bf16 a[16:19], a[32:35], a[152:155], a[16:19]\n"
"s_waitcnt vmcnt(15)\n"
"ds_read_b128 v[84:87], v2 offset:37888\n"
"v_accvgpr_read_b32 v16, a184\n"
"v_mfma_f32_16x16x32_bf16 v[200:203], a[48:51], a[96:99], v[200:203]\n"
"v_accvgpr_read_b32 v17, a185\n"
"v_accvgpr_read_b32 v18, a186\n"
"v_mfma_f32_16x16x32_bf16 v[200:203], a[52:55], a[100:103], v[200:203]\n"
"v_accvgpr_read_b32 v19, a187\n"
"v_mfma_f32_16x16x32_bf16 v[228:231], a[60:63], a[100:103], v[228:231]\n"
"ds_read_b128 a[100:103], v3 offset:17408\n"
"v_lshlrev_b32 v12, 16, v16\n"
"v_mfma_f32_16x16x32_bf16 v[228:231], a[56:59], a[96:99], v[228:231]\n"
"ds_read_b128 a[96:99], v3 offset:16384\n"
"v_and_b32 v13, s63, v16\n"
"v_mfma_f32_16x16x32_bf16 v[232:235], a[56:59], a[104:107], v[232:235]\n"
"v_lshlrev_b32 v14, 16, v17\n"
"v_and_b32 v15, s63, v17\n"
"v_mfma_f32_16x16x32_bf16 v[232:235], a[60:63], a[108:111], v[232:235]\n"
"v_accvgpr_read_b32 v10, a0\n"
"v_mfma_f32_16x16x32_bf16 v[204:207], a[52:55], a[108:111], v[204:207]\n"
"ds_read_b128 a[108:111], v4 offset:17408\n"
"v_accvgpr_read_b32 v11, a1\n"
"v_mfma_f32_16x16x32_bf16 v[204:207], a[48:51], a[104:107], v[204:207]\n"
"ds_read_b128 a[104:107], v4 offset:16384\n"
"v_mul_f32 v12, v12, v10\n"
"v_mfma_f32_16x16x32_bf16 v[208:211], a[48:51], a[112:115], v[208:211]\n"
"v_mul_f32 v13, v13, v11\n"
"v_accvgpr_read_b32 v10, a2\n"
"v_mfma_f32_16x16x32_bf16 v[208:211], a[52:55], a[116:119], v[208:211]\n"
"v_accvgpr_read_b32 v11, a3\n"
"v_mul_f32 v14, v14, v10\n"
"v_mfma_f32_16x16x32_bf16 v[236:239], a[60:63], a[116:119], v[236:239]\n"
"ds_read_b128 a[116:119], v5 offset:17408\n"
"v_mul_f32 v15, v15, v11\n"
"v_mfma_f32_16x16x32_bf16 v[236:239], a[56:59], a[112:115], v[236:239]\n"
"ds_read_b128 a[112:115], v5 offset:16384\n"
"v_cvt_pk_bf16_f32 v24, v12, v13\n"
"v_mfma_f32_16x16x32_bf16 v[240:243], a[56:59], a[120:123], v[240:243]\n"
"v_cvt_pk_bf16_f32 v25, v14, v15\n"
"v_mfma_f32_16x16x32_bf16 v[240:243], a[60:63], a[124:127], v[240:243]\n"
"ds_write_b64 v7, v[24:25] offset:38912\n"
"v_mfma_f32_16x16x32_bf16 v[212:215], a[52:55], a[124:127], v[212:215]\n"
"ds_read_b128 a[124:127], v3 offset:19456\n"
"v_lshlrev_b32 v12, 16, v18\n"
"v_mfma_f32_16x16x32_bf16 v[212:215], a[48:51], a[120:123], v[212:215]\n"
"ds_read_b128 a[120:123], v3 offset:18432\n"
"v_and_b32 v13, s63, v18\n"
"v_mfma_f32_16x16x32_bf16 v[216:219], a[48:51], a[128:131], v[216:219]\n"
"v_lshlrev_b32 v14, 16, v19\n"
"v_and_b32 v15, s63, v19\n"
"v_mfma_f32_16x16x32_bf16 v[216:219], a[52:55], a[132:135], v[216:219]\n"
"v_accvgpr_read_b32 v10, a4\n"
"v_accvgpr_read_b32 v11, a5\n"
"v_mfma_f32_16x16x32_bf16 v[244:247], a[60:63], a[132:135], v[244:247]\n"
"ds_read_b128 a[132:135], v4 offset:19456\n"
"v_mul_f32 v12, v12, v10\n"
"v_mfma_f32_16x16x32_bf16 v[244:247], a[56:59], a[128:131], v[244:247]\n"
"ds_read_b128 a[128:131], v4 offset:18432\n"
"v_mul_f32 v13, v13, v11\n"
"v_mfma_f32_16x16x32_bf16 v[248:251], a[56:59], a[136:139], v[248:251]\n"
"v_accvgpr_read_b32 v10, a6\n"
"v_mfma_f32_16x16x32_bf16 v[248:251], a[60:63], a[140:143], v[248:251]\n"
"v_accvgpr_read_b32 v11, a7\n"
"v_mul_f32 v14, v14, v10\n"
"v_mfma_f32_16x16x32_bf16 v[220:223], a[52:55], a[140:143], v[220:223]\n"
"ds_read_b128 a[140:143], v5 offset:19456\n"
"v_mul_f32 v15, v15, v11\n"
"v_mfma_f32_16x16x32_bf16 v[220:223], a[48:51], a[136:139], v[220:223]\n"
"ds_read_b128 a[136:139], v5 offset:18432\n"
"v_cvt_pk_bf16_f32 v26, v12, v13\n"
"v_mfma_f32_16x16x32_bf16 v[224:227], a[48:51], a[144:147], v[224:227]\n"
"v_cvt_pk_bf16_f32 v27, v14, v15\n"
"v_mfma_f32_16x16x32_bf16 v[224:227], a[52:55], a[148:151], v[224:227]\n"
"buffer_store_dwordx4 v[76:79], v6, s[52:55], s89 offen offset:1024 sc1\n"
"ds_write_b64 v7, v[26:27] offset:38920\n"
"v_mfma_f32_16x16x32_bf16 v[252:255], a[60:63], a[148:151], v[252:255]\n"
"ds_read_b128 a[148:151], v3 offset:21504\n"
"v_accvgpr_read_b32 v16, a188\n"
"v_mfma_f32_16x16x32_bf16 v[252:255], a[56:59], a[144:147], v[252:255]\n"
"ds_read_b128 a[144:147], v3 offset:20480\n"
"v_accvgpr_read_b32 v17, a189\n"
"v_mfma_f32_16x16x32_bf16 a[28:31], a[56:59], a[152:155], a[28:31]\n"
"v_accvgpr_read_b32 v18, a190\n"
"v_accvgpr_read_b32 v19, a191\n"
"v_mfma_f32_16x16x32_bf16 a[28:31], a[60:63], a[156:159], a[28:31]\n"
"v_lshlrev_b32 v12, 16, v16\n"
"v_and_b32 v13, s63, v16\n"
"v_mfma_f32_16x16x32_bf16 a[24:27], a[52:55], a[156:159], a[24:27]\n"
"ds_read_b128 a[156:159], v4 offset:21504\n"
"v_lshlrev_b32 v14, 16, v17\n"
"v_mfma_f32_16x16x32_bf16 a[24:27], a[48:51], a[152:155], a[24:27]\n"
"s_waitcnt lgkmcnt(15)\n"
"ds_read_b128 a[152:155], v4 offset:20480\n"
"v_and_b32 v15, s63, v17\n"
"v_mfma_f32_16x16x32_bf16 v[144:147], v[88:91], a[96:99], v[144:147]\n"
"v_accvgpr_read_b32 v10, a8\n"
"v_accvgpr_read_b32 v11, a9\n"
"v_mfma_f32_16x16x32_bf16 v[144:147], v[116:119], a[100:103], v[144:147]\n"
"v_mul_f32 v12, v12, v10\n"
"v_mul_f32 v13, v13, v11\n"
"v_mfma_f32_16x16x32_bf16 v[172:175], v[120:123], a[100:103], v[172:175]\n"
"v_accvgpr_read_b32 v10, a10\n"
"v_accvgpr_read_b32 v11, a11\n"
"v_mfma_f32_16x16x32_bf16 v[172:175], v[92:95], a[96:99], v[172:175]\n"
"buffer_store_dwordx4 v[52:55], v6, s[52:55], s90 offen offset:2048 sc1\n"
"v_mul_f32 v14, v14, v10\n"
"v_mul_f32 v15, v15, v11\n"
"v_cvt_pk_bf16_f32 v28, v12, v13\n"
"v_cvt_pk_bf16_f32 v29, v14, v15\n"
"v_mfma_f32_16x16x32_bf16 v[200:203], v[32:35], a[96:99], v[200:203]\n"
"ds_write_b64 v8, v[28:29] offset:38912\n"
"v_mfma_f32_16x16x32_bf16 v[200:203], v[60:63], a[100:103], v[200:203]\n"
"v_lshlrev_b32 v12, 16, v18\n"
"v_and_b32 v13, s63, v18\n"
"v_mfma_f32_16x16x32_bf16 v[228:231], v[124:127], a[100:103], v[228:231]\n"
"ds_read_b128 a[100:103], v5 offset:21504\n"
"v_lshlrev_b32 v14, 16, v19\n"
"v_mfma_f32_16x16x32_bf16 v[228:231], v[96:99], a[96:99], v[228:231]\n"
"s_waitcnt vmcnt(3) lgkmcnt(15)\n"
"ds_read_b128 a[96:99], v5 offset:20480\n"
"v_and_b32 v15, s63, v19\n"
"v_mfma_f32_16x16x32_bf16 v[228:231], v[108:111], a[104:107], v[228:231]\n"
"v_accvgpr_read_b32 v10, a12\n"
"v_accvgpr_read_b32 v11, a13\n"
"v_mfma_f32_16x16x32_bf16 v[228:231], v[136:139], a[108:111], v[228:231]\n"
"v_mul_f32 v12, v12, v10\n"
"v_mul_f32 v13, v13, v11\n"
"v_mfma_f32_16x16x32_bf16 v[200:203], v[132:135], a[108:111], v[200:203]\n"
"v_accvgpr_read_b32 v10, a14\n"
"v_accvgpr_read_b32 v11, a15\n"
"v_mfma_f32_16x16x32_bf16 v[200:203], v[104:107], a[104:107], v[200:203]\n"
"v_mul_f32 v14, v14, v10\n"
"v_mul_f32 v15, v15, v11\n"
"v_mfma_f32_16x16x32_bf16 v[172:175], v[36:39], a[104:107], v[172:175]\n"
"v_cvt_pk_bf16_f32 v30, v12, v13\n"
"v_cvt_pk_bf16_f32 v31, v14, v15\n"
"v_mfma_f32_16x16x32_bf16 v[172:175], v[64:67], a[108:111], v[172:175]\n"
"buffer_store_dwordx4 v[80:83], v6, s[52:55], s91 offen offset:3072 sc1\n"
"ds_write_b64 v8, v[30:31] offset:38920\n"
"v_mfma_f32_16x16x32_bf16 v[144:147], v[128:131], a[108:111], v[144:147]\n"
"ds_read_b128 a[108:111], v3 offset:23552\n"
"v_mfma_f32_16x16x32_bf16 v[144:147], v[100:103], a[104:107], v[144:147]\n"
"s_waitcnt lgkmcnt(15)\n"
"ds_read_b128 a[104:107], v3 offset:22528\n"
"v_mfma_f32_16x16x32_bf16 v[144:147], a[64:67], a[112:115], v[144:147]\n"
"v_mfma_f32_16x16x32_bf16 v[144:147], a[68:71], a[116:119], v[144:147]\n"
"v_mfma_f32_16x16x32_bf16 v[172:175], a[76:79], a[116:119], v[172:175]\n"
"v_mfma_f32_16x16x32_bf16 v[172:175], a[72:75], a[112:115], v[172:175]\n"
"ds_read_b128 v[112:115], v2 offset:38912\n"
"v_mfma_f32_16x16x32_bf16 v[200:203], a[80:83], a[112:115], v[200:203]\n"
"ds_read_b128 v[140:143], v2 offset:39936\n"
"v_mfma_f32_16x16x32_bf16 v[200:203], a[84:87], a[116:119], v[200:203]\n"
"v_accvgpr_read_b32 v16, a192\n"
"v_accvgpr_read_b32 v17, a193\n"
"v_mfma_f32_16x16x32_bf16 v[228:231], a[92:95], a[116:119], v[228:231]\n"
"ds_read_b128 a[116:119], v4 offset:23552\n"
"v_accvgpr_read_b32 v18, a194\n"
"v_mfma_f32_16x16x32_bf16 v[228:231], a[88:91], a[112:115], v[228:231]\n"
"s_waitcnt lgkmcnt(15)\n"
"buffer_store_dwordx4 v[56:59], v6, s[52:55], s92 offen offset:0 sc1\n"
"ds_read_b128 a[112:115], v4 offset:22528\n"
"v_accvgpr_read_b32 v19, a195\n"
"v_lshlrev_b32 v12, 16, v16\n"
"v_mfma_f32_16x16x32_bf16 v[232:235], v[96:99], a[120:123], v[232:235]\n"
"v_and_b32 v13, s63, v16\n"
"v_mfma_f32_16x16x32_bf16 v[232:235], v[124:127], a[124:127], v[232:235]\n"
"v_lshlrev_b32 v14, 16, v17\n"
"v_and_b32 v15, s63, v17\n"
"v_mfma_f32_16x16x32_bf16 v[204:207], v[60:63], a[124:127], v[204:207]\n"
"v_mul_f32 v12, v12, v144\n"
"v_mul_f32 v13, v13, v145\n"
"v_mfma_f32_16x16x32_bf16 v[204:207], v[32:35], a[120:123], v[204:207]\n"
"v_mul_f32 v14, v14, v146\n"
"v_mul_f32 v15, v15, v147\n"
"v_mfma_f32_16x16x32_bf16 v[176:179], v[92:95], a[120:123], v[176:179]\n"
"v_cvt_pk_bf16_f32 v24, v12, v13\n"
"v_cvt_pk_bf16_f32 v25, v14, v15\n"
"v_mfma_f32_16x16x32_bf16 v[176:179], v[120:123], a[124:127], v[176:179]\n"
"ds_write_b64 v7, v[24:25] offset:32768\n"
"v_mfma_f32_16x16x32_bf16 v[148:151], v[116:119], a[124:127], v[148:151]\n"
"ds_read_b128 a[124:127], v5 offset:23552\n"
"v_lshlrev_b32 v12, 16, v18\n"
"v_mfma_f32_16x16x32_bf16 v[148:151], v[88:91], a[120:123], v[148:151]\n"
"ds_read_b128 a[120:123], v5 offset:22528\n"
"v_and_b32 v13, s63, v18\n"
"v_mfma_f32_16x16x32_bf16 v[148:151], v[100:103], a[128:131], v[148:151]\n"
"v_lshlrev_b32 v14, 16, v19\n"
"v_mfma_f32_16x16x32_bf16 v[148:151], v[128:131], a[132:135], v[148:151]\n"
"buffer_store_dwordx4 v[84:87], v6, s[52:55], s93 offen offset:1024 sc1\n"
"v_and_b32 v15, s63, v19\n"
"v_mul_f32 v12, v12, v172\n"
"v_mul_f32 v13, v13, v173\n"
"v_mfma_f32_16x16x32_bf16 v[176:179], v[64:67], a[132:135], v[176:179]\n"
"v_mul_f32 v14, v14, v174\n"
"v_mul_f32 v15, v15, v175\n"
"v_mfma_f32_16x16x32_bf16 v[176:179], v[36:39], a[128:131], v[176:179]\n"
"v_cvt_pk_bf16_f32 v26, v12, v13\n"
"v_cvt_pk_bf16_f32 v27, v14, v15\n"
"v_mfma_f32_16x16x32_bf16 v[204:207], v[104:107], a[128:131], v[204:207]\n"
"ds_write_b64 v7, v[26:27] offset:32776\n"
"v_mfma_f32_16x16x32_bf16 v[204:207], v[132:135], a[132:135], v[204:207]\n"
"v_accvgpr_read_b32 v16, a196\n"
"v_mfma_f32_16x16x32_bf16 v[232:235], v[136:139], a[132:135], v[232:235]\n"
"ds_read_b128 a[132:135], v3 offset:25600\n"
"v_accvgpr_read_b32 v17, a197\n"
"v_mfma_f32_16x16x32_bf16 v[232:235], v[108:111], a[128:131], v[232:235]\n"
"s_waitcnt lgkmcnt(15)\n"
"ds_read_b128 a[128:131], v3 offset:24576\n"
"v_accvgpr_read_b32 v18, a198\n"
"v_mfma_f32_16x16x32_bf16 v[232:235], a[88:91], a[136:139], v[232:235]\n"
"v_accvgpr_read_b32 v19, a199\n"
"v_lshlrev_b32 v12, 16, v16\n"
"v_mfma_f32_16x16x32_bf16 v[232:235], a[92:95], a[140:143], v[232:235]\n"
"v_and_b32 v13, s63, v16\n"
"v_lshlrev_b32 v14, 16, v17\n"
"v_mfma_f32_16x16x32_bf16 v[204:207], a[84:87], a[140:143], v[204:207]\n"
"v_and_b32 v15, s63, v17\n"
"v_mul_f32 v12, v12, v200\n"
"v_mfma_f32_16x16x32_bf16 v[204:207], a[80:83], a[136:139], v[204:207]\n"
"s_waitcnt lgkmcnt(9)\n"
"buffer_store_dwordx4 v[112:115], v6, s[52:55], s94 offen offset:2048 sc1\n"
"v_mul_f32 v13, v13, v201\n"
"v_mul_f32 v14, v14, v202\n"
"v_mul_f32 v15, v15, v203\n"
"v_mfma_f32_16x16x32_bf16 v[176:179], a[72:75], a[136:139], v[176:179]\n"
"v_cvt_pk_bf16_f32 v28, v12, v13\n"
"v_cvt_pk_bf16_f32 v29, v14, v15\n"
"v_mfma_f32_16x16x32_bf16 v[176:179], a[76:79], a[140:143], v[176:179]\n"
"ds_write_b64 v8, v[28:29] offset:32768\n"
"v_mfma_f32_16x16x32_bf16 v[148:151], a[68:71], a[140:143], v[148:151]\n"
"ds_read_b128 a[140:143], v4 offset:25600\n"
"v_mfma_f32_16x16x32_bf16 v[148:151], a[64:67], a[136:139], v[148:151]\n"
"ds_read_b128 a[136:139], v4 offset:24576\n"
"v_lshlrev_b32 v12, 16, v18\n"
"v_mfma_f32_16x16x32_bf16 v[152:155], v[88:91], a[144:147], v[152:155]\n"
"v_and_b32 v13, s63, v18\n"
"v_lshlrev_b32 v14, 16, v19\n"
"v_mfma_f32_16x16x32_bf16 v[152:155], v[116:119], a[148:151], v[152:155]\n"
"v_and_b32 v15, s63, v19\n"
"v_mul_f32 v12, v12, v228\n"
"v_mfma_f32_16x16x32_bf16 v[180:183], v[120:123], a[148:151], v[180:183]\n"
"v_mul_f32 v13, v13, v229\n"
"v_mfma_f32_16x16x32_bf16 v[180:183], v[92:95], a[144:147], v[180:183]\n"
"v_mul_f32 v14, v14, v230\n"
"v_mul_f32 v15, v15, v231\n"
"v_mfma_f32_16x16x32_bf16 v[208:211], v[32:35], a[144:147], v[208:211]\n"
"v_cvt_pk_bf16_f32 v30, v12, v13\n"
"v_cvt_pk_bf16_f32 v31, v14, v15\n"
"v_mfma_f32_16x16x32_bf16 v[208:211], v[60:63], a[148:151], v[208:211]\n"
"s_waitcnt lgkmcnt(11)\n"
"buffer_store_dwordx4 v[140:143], v6, s[52:55], s95 offen offset:3072 sc1\n"
"ds_write_b64 v8, v[30:31] offset:32776\n"
"v_mfma_f32_16x16x32_bf16 v[236:239], v[124:127], a[148:151], v[236:239]\n"
"ds_read_b128 a[148:151], v5 offset:25600\n"
"v_mfma_f32_16x16x32_bf16 v[236:239], v[96:99], a[144:147], v[236:239]\n"
"ds_read_b128 a[144:147], v5 offset:24576\n"
"v_mfma_f32_16x16x32_bf16 v[236:239], v[108:111], a[152:155], v[236:239]\n"
"ds_read_b128 v[144:147], v2 offset:32768\n"
"v_mfma_f32_16x16x32_bf16 v[236:239], v[136:139], a[156:159], v[236:239]\n"
"ds_read_b128 v[172:175], v2 offset:33792\n"
"v_accvgpr_read_b32 v16, a200\n"
"v_mfma_f32_16x16x32_bf16 v[208:211], v[132:135], a[156:159], v[208:211]\n"
"v_accvgpr_read_b32 v17, a201\n"
"v_accvgpr_read_b32 v18, a202\n"
"v_mfma_f32_16x16x32_bf16 v[208:211], v[104:107], a[152:155], v[208:211]\n"
"v_accvgpr_read_b32 v19, a203\n"
"v_lshlrev_b32 v12, 16, v16\n"
"v_mfma_f32_16x16x32_bf16 v[180:183], v[36:39], a[152:155], v[180:183]\n"
"v_and_b32 v13, s63, v16\n"
"v_lshlrev_b32 v14, 16, v17\n"
"v_mfma_f32_16x16x32_bf16 v[180:183], v[64:67], a[156:159], v[180:183]\n"
"v_and_b32 v15, s63, v17\n"
"v_mul_f32 v12, v12, v148\n"
"v_mfma_f32_16x16x32_bf16 v[152:155], v[128:131], a[156:159], v[152:155]\n"
"ds_read_b128 a[156:159], v3 offset:27648\n"
"v_mul_f32 v13, v13, v149\n"
"v_mfma_f32_16x16x32_bf16 v[152:155], v[100:103], a[152:155], v[152:155]\n"
"ds_read_b128 a[152:155], v3 offset:26624\n"
"v_mul_f32 v14, v14, v150\n"
"v_mfma_f32_16x16x32_bf16 v[152:155], a[64:67], a[96:99], v[152:155]\n"
"v_mul_f32 v15, v15, v151\n"
"v_cvt_pk_bf16_f32 v24, v12, v13\n"
"v_mfma_f32_16x16x32_bf16 v[152:155], a[68:71], a[100:103], v[152:155]\n"
"v_cvt_pk_bf16_f32 v25, v14, v15\n"
"v_mfma_f32_16x16x32_bf16 v[180:183], a[76:79], a[100:103], v[180:183]\n"
"s_waitcnt lgkmcnt(3)\n"
"buffer_store_dwordx4 v[144:147], v6, s[52:55], s80 offen offset:128 sc1\n"
"ds_write_b64 v7, v[24:25] offset:34816\n"
"v_mfma_f32_16x16x32_bf16 v[180:183], a[72:75], a[96:99], v[180:183]\n"
"v_lshlrev_b32 v12, 16, v18\n"
"v_and_b32 v13, s63, v18\n"
"v_mfma_f32_16x16x32_bf16 v[208:211], a[80:83], a[96:99], v[208:211]\n"
"v_lshlrev_b32 v14, 16, v19\n"
"v_and_b32 v15, s63, v19\n"
"v_mfma_f32_16x16x32_bf16 v[208:211], a[84:87], a[100:103], v[208:211]\n"
"v_mul_f32 v12, v12, v176\n"
"v_mul_f32 v13, v13, v177\n"
"v_mfma_f32_16x16x32_bf16 v[236:239], a[92:95], a[100:103], v[236:239]\n"
"ds_read_b128 a[100:103], v4 offset:27648\n"
"v_mul_f32 v14, v14, v178\n"
"v_mfma_f32_16x16x32_bf16 v[236:239], a[88:91], a[96:99], v[236:239]\n"
"ds_read_b128 a[96:99], v4 offset:26624\n"
"v_mul_f32 v15, v15, v179\n"
"v_mfma_f32_16x16x32_bf16 v[240:243], v[96:99], a[104:107], v[240:243]\n"
"v_cvt_pk_bf16_f32 v26, v12, v13\n"
"v_cvt_pk_bf16_f32 v27, v14, v15\n"
"v_mfma_f32_16x16x32_bf16 v[240:243], v[124:127], a[108:111], v[240:243]\n"
"ds_write_b64 v7, v[26:27] offset:34824\n"
"v_mfma_f32_16x16x32_bf16 v[212:215], v[60:63], a[108:111], v[212:215]\n"
"v_accvgpr_read_b32 v16, a204\n"
"v_accvgpr_read_b32 v17, a205\n"
"v_mfma_f32_16x16x32_bf16 v[212:215], v[32:35], a[104:107], v[212:215]\n"
"v_accvgpr_read_b32 v18, a206\n"
"v_accvgpr_read_b32 v19, a207\n"
"v_mfma_f32_16x16x32_bf16 v[184:187], v[92:95], a[104:107], v[184:187]\n"
"s_waitcnt lgkmcnt(6)\n"
"buffer_store_dwordx4 v[172:175], v6, s[52:55], s81 offen offset:1152 sc1\n"
"v_lshlrev_b32 v12, 16, v16\n"
"v_and_b32 v13, s63, v16\n"
"v_lshlrev_b32 v14, 16, v17\n"
"v_mfma_f32_16x16x32_bf16 v[184:187], v[120:123], a[108:111], v[184:187]\n"
"v_and_b32 v15, s63, v17\n"
"v_mul_f32 v12, v12, v204\n"
"v_mfma_f32_16x16x32_bf16 v[156:159], v[116:119], a[108:111], v[156:159]\n"
"ds_read_b128 a[108:111], v5 offset:27648\n"
"v_mul_f32 v13, v13, v205\n"
"v_mfma_f32_16x16x32_bf16 v[156:159], v[88:91], a[104:107], v[156:159]\n"
"ds_read_b128 a[104:107], v5 offset:26624\n"
"v_mul_f32 v14, v14, v206\n"
"v_mfma_f32_16x16x32_bf16 v[156:159], v[100:103], a[112:115], v[156:159]\n"
"v_mul_f32 v15, v15, v207\n"
"v_cvt_pk_bf16_f32 v28, v12, v13\n"
"v_mfma_f32_16x16x32_bf16 v[156:159], v[128:131], a[116:119], v[156:159]\n"
"v_cvt_pk_bf16_f32 v29, v14, v15\n"
"v_mfma_f32_16x16x32_bf16 v[184:187], v[64:67], a[116:119], v[184:187]\n"
"ds_write_b64 v8, v[28:29] offset:34816\n"
"v_mfma_f32_16x16x32_bf16 v[184:187], v[36:39], a[112:115], v[184:187]\n"
"v_lshlrev_b32 v12, 16, v18\n"
"v_and_b32 v13, s63, v18\n"
"v_mfma_f32_16x16x32_bf16 v[212:215], v[104:107], a[112:115], v[212:215]\n"
"v_lshlrev_b32 v14, 16, v19\n"
"v_and_b32 v15, s63, v19\n"
"v_mfma_f32_16x16x32_bf16 v[212:215], v[132:135], a[116:119], v[212:215]\n"
"v_mul_f32 v12, v12, v232\n"
"v_mul_f32 v13, v13, v233\n"
"v_mfma_f32_16x16x32_bf16 v[240:243], v[136:139], a[116:119], v[240:243]\n"
"ds_read_b128 a[116:119], v3 offset:29696\n"
"v_mul_f32 v14, v14, v234\n"
"v_mfma_f32_16x16x32_bf16 v[240:243], v[108:111], a[112:115], v[240:243]\n"
"ds_read_b128 a[112:115], v3 offset:28672\n"
"v_mul_f32 v15, v15, v235\n"
"v_mfma_f32_16x16x32_bf16 v[240:243], a[88:91], a[120:123], v[240:243]\n"
"v_cvt_pk_bf16_f32 v30, v12, v13\n"
"v_cvt_pk_bf16_f32 v31, v14, v15\n"
"v_mfma_f32_16x16x32_bf16 v[240:243], a[92:95], a[124:127], v[240:243]\n"
"ds_write_b64 v8, v[30:31] offset:34824\n"
"v_mfma_f32_16x16x32_bf16 v[212:215], a[84:87], a[124:127], v[212:215]\n"
"ds_read_b128 v[148:151], v2 offset:34816\n"
"v_mfma_f32_16x16x32_bf16 v[212:215], a[80:83], a[120:123], v[212:215]\n"
"ds_read_b128 v[176:179], v2 offset:35840\n"
"v_accvgpr_read_b32 v16, a208\n"
"v_mfma_f32_16x16x32_bf16 v[184:187], a[72:75], a[120:123], v[184:187]\n"
"v_accvgpr_read_b32 v17, a209\n"
"v_accvgpr_read_b32 v18, a210\n"
"v_mfma_f32_16x16x32_bf16 v[184:187], a[76:79], a[124:127], v[184:187]\n"
"v_accvgpr_read_b32 v19, a211\n"
"v_lshlrev_b32 v12, 16, v16\n"
"v_mfma_f32_16x16x32_bf16 v[156:159], a[68:71], a[124:127], v[156:159]\n"
"ds_read_b128 a[124:127], v4 offset:29696\n"
"v_and_b32 v13, s63, v16\n"
"v_mfma_f32_16x16x32_bf16 v[156:159], a[64:67], a[120:123], v[156:159]\n"
"ds_read_b128 a[120:123], v4 offset:28672\n"
"v_lshlrev_b32 v14, 16, v17\n"
"v_mfma_f32_16x16x32_bf16 v[160:163], v[88:91], a[128:131], v[160:163]\n"
"v_and_b32 v15, s63, v17\n"
"v_mul_f32 v12, v12, v152\n"
"v_mfma_f32_16x16x32_bf16 v[160:163], v[116:119], a[132:135], v[160:163]\n"
"v_mul_f32 v13, v13, v153\n"
"v_mul_f32 v14, v14, v154\n"
"v_mfma_f32_16x16x32_bf16 v[188:191], v[120:123], a[132:135], v[188:191]\n"
"v_mul_f32 v15, v15, v155\n"
"v_cvt_pk_bf16_f32 v24, v12, v13\n"
"v_mfma_f32_16x16x32_bf16 v[188:191], v[92:95], a[128:131], v[188:191]\n"
"v_cvt_pk_bf16_f32 v25, v14, v15\n"
"v_mfma_f32_16x16x32_bf16 v[216:219], v[32:35], a[128:131], v[216:219]\n"
"s_waitcnt lgkmcnt(3)\n"
"buffer_store_dwordx4 v[148:151], v6, s[52:55], s82 offen offset:2176 sc1\n"
"ds_write_b64 v7, v[24:25] offset:36864\n"
"v_mfma_f32_16x16x32_bf16 v[216:219], v[60:63], a[132:135], v[216:219]\n"
"v_lshlrev_b32 v12, 16, v18\n"
"v_and_b32 v13, s63, v18\n"
"v_mfma_f32_16x16x32_bf16 v[244:247], v[124:127], a[132:135], v[244:247]\n"
"ds_read_b128 a[132:135], v5 offset:29696\n"
"v_lshlrev_b32 v14, 16, v19\n"
"v_mfma_f32_16x16x32_bf16 v[244:247], v[96:99], a[128:131], v[244:247]\n"
"ds_read_b128 a[128:131], v5 offset:28672\n"
"v_and_b32 v15, s63, v19\n"
"v_mfma_f32_16x16x32_bf16 v[244:247], v[108:111], a[136:139], v[244:247]\n"
"v_mul_f32 v12, v12, v180\n"
"v_mul_f32 v13, v13, v181\n"
"v_mfma_f32_16x16x32_bf16 v[244:247], v[136:139], a[140:143], v[244:247]\n"
"v_mul_f32 v14, v14, v182\n"
"v_mul_f32 v15, v15, v183\n"
"v_mfma_f32_16x16x32_bf16 v[216:219], v[132:135], a[140:143], v[216:219]\n"
"v_cvt_pk_bf16_f32 v26, v12, v13\n"
"v_cvt_pk_bf16_f32 v27, v14, v15\n"
"v_mfma_f32_16x16x32_bf16 v[216:219], v[104:107], a[136:139], v[216:219]\n"
"ds_write_b64 v7, v[26:27] offset:36872\n"
"v_mfma_f32_16x16x32_bf16 v[188:191], v[36:39], a[136:139], v[188:191]\n"
"v_accvgpr_read_b32 v16, a212\n"
"v_accvgpr_read_b32 v17, a213\n"
"v_mfma_f32_16x16x32_bf16 v[188:191], v[64:67], a[140:143], v[188:191]\n"
"v_accvgpr_read_b32 v18, a214\n"
"v_accvgpr_read_b32 v19, a215\n"
"v_mfma_f32_16x16x32_bf16 v[160:163], v[128:131], a[140:143], v[160:163]\n"
"s_waitcnt lgkmcnt(6)\n"
"buffer_store_dwordx4 v[176:179], v6, s[52:55], s83 offen offset:3200 sc1\n"
"ds_read_b128 a[140:143], v3 offset:31744\n"
"v_lshlrev_b32 v12, 16, v16\n"
"v_and_b32 v13, s63, v16\n"
"v_mfma_f32_16x16x32_bf16 v[160:163], v[100:103], a[136:139], v[160:163]\n"
"ds_read_b128 a[136:139], v3 offset:30720\n"
"v_lshlrev_b32 v14, 16, v17\n"
"v_mfma_f32_16x16x32_bf16 v[160:163], a[64:67], a[144:147], v[160:163]\n"
"v_and_b32 v15, s63, v17\n"
"v_mul_f32 v12, v12, v208\n"
"v_mfma_f32_16x16x32_bf16 v[160:163], a[68:71], a[148:151], v[160:163]\n"
"v_mul_f32 v13, v13, v209\n"
"v_mul_f32 v14, v14, v210\n"
"v_mfma_f32_16x16x32_bf16 v[188:191], a[76:79], a[148:151], v[188:191]\n"
"v_mul_f32 v15, v15, v211\n"
"v_cvt_pk_bf16_f32 v28, v12, v13\n"
"v_mfma_f32_16x16x32_bf16 v[188:191], a[72:75], a[144:147], v[188:191]\n"
"v_cvt_pk_bf16_f32 v29, v14, v15\n"
"v_mfma_f32_16x16x32_bf16 v[216:219], a[80:83], a[144:147], v[216:219]\n"
"ds_write_b64 v8, v[28:29] offset:36864\n"
"v_mfma_f32_16x16x32_bf16 v[216:219], a[84:87], a[148:151], v[216:219]\n"
"v_lshlrev_b32 v12, 16, v18\n"
"v_and_b32 v13, s63, v18\n"
"v_mfma_f32_16x16x32_bf16 v[244:247], a[92:95], a[148:151], v[244:247]\n"
"ds_read_b128 a[148:151], v4 offset:31744\n"
"v_lshlrev_b32 v14, 16, v19\n"
"v_mfma_f32_16x16x32_bf16 v[244:247], a[88:91], a[144:147], v[244:247]\n"
"ds_read_b128 a[144:147], v4 offset:30720\n"
"v_and_b32 v15, s63, v19\n"
"v_mfma_f32_16x16x32_bf16 v[248:251], v[96:99], a[152:155], v[248:251]\n"
"v_mul_f32 v12, v12, v236\n"
"v_mul_f32 v13, v13, v237\n"
"v_mfma_f32_16x16x32_bf16 v[248:251], v[124:127], a[156:159], v[248:251]\n"
"v_mul_f32 v14, v14, v238\n"
"v_mul_f32 v15, v15, v239\n"
"v_mfma_f32_16x16x32_bf16 v[220:223], v[60:63], a[156:159], v[220:223]\n"
"v_cvt_pk_bf16_f32 v30, v12, v13\n"
"v_cvt_pk_bf16_f32 v31, v14, v15\n"
"v_mfma_f32_16x16x32_bf16 v[220:223], v[32:35], a[152:155], v[220:223]\n"
"L_st11492:\n"
"s_cbranch_execz L_retC\n"
"ds_write_b64 v8, v[30:31] offset:36872\n"
"v_mfma_f32_16x16x32_bf16 v[192:195], v[92:95], a[152:155], v[192:195]\n"
"ds_read_b128 v[152:155], v2 offset:36864\n"
"v_mfma_f32_16x16x32_bf16 v[192:195], v[120:123], a[156:159], v[192:195]\n"
"ds_read_b128 v[180:183], v2 offset:37888\n"
"v_accvgpr_read_b32 v16, a216\n"
"v_mfma_f32_16x16x32_bf16 v[164:167], v[116:119], a[156:159], v[164:167]\n"
"ds_read_b128 a[156:159], v5 offset:31744\n"
"v_accvgpr_read_b32 v17, a217\n"
"v_mfma_f32_16x16x32_bf16 v[164:167], v[88:91], a[152:155], v[164:167]\n"
"ds_read_b128 a[152:155], v5 offset:30720\n"
"v_accvgpr_read_b32 v18, a218\n"
"v_mfma_f32_16x16x32_bf16 v[164:167], v[100:103], a[96:99], v[164:167]\n"
"v_accvgpr_read_b32 v19, a219\n"
"v_lshlrev_b32 v12, 16, v16\n"
"v_mfma_f32_16x16x32_bf16 v[164:167], v[128:131], a[100:103], v[164:167]\n"
"v_and_b32 v13, s63, v16\n"
"v_lshlrev_b32 v14, 16, v17\n"
"v_mfma_f32_16x16x32_bf16 v[192:195], v[64:67], a[100:103], v[192:195]\n"
"v_and_b32 v15, s63, v17\n"
"v_mul_f32 v12, v12, v156\n"
"v_mfma_f32_16x16x32_bf16 v[192:195], v[36:39], a[96:99], v[192:195]\n"
"v_mul_f32 v13, v13, v157\n"
"v_mul_f32 v14, v14, v158\n"
"v_mfma_f32_16x16x32_bf16 v[220:223], v[104:107], a[96:99], v[220:223]\n"
"v_mul_f32 v15, v15, v159\n"
"v_mfma_f32_16x16x32_bf16 v[220:223], v[132:135], a[100:103], v[220:223]\n"
"v_cvt_pk_bf16_f32 v24, v12, v13\n"
"v_mfma_f32_16x16x32_bf16 v[248:251], v[136:139], a[100:103], v[248:251]\n"
"v_cvt_pk_bf16_f32 v25, v14, v15\n"
"v_mfma_f32_16x16x32_bf16 v[248:251], v[108:111], a[96:99], v[248:251]\n"
"s_waitcnt lgkmcnt(3)\n"
"buffer_store_dwordx4 v[152:155], v6, s[52:55], s84 offen offset:128 sc1\n"
"ds_write_b64 v7, v[24:25] offset:38912\n"
"v_mfma_f32_16x16x32_bf16 v[248:251], a[88:91], a[104:107], v[248:251]\n"
"v_lshlrev_b32 v12, 16, v18\n"
"v_and_b32 v13, s63, v18\n"
"v_mfma_f32_16x16x32_bf16 v[248:251], a[92:95], a[108:111], v[248:251]\n"
"v_lshlrev_b32 v14, 16, v19\n"
"v_mfma_f32_16x16x32_bf16 v[220:223], a[84:87], a[108:111], v[220:223]\n"
"v_and_b32 v15, s63, v19\n"
"v_mfma_f32_16x16x32_bf16 v[220:223], a[80:83], a[104:107], v[220:223]\n"
"v_mul_f32 v12, v12, v184\n"
"v_mfma_f32_16x16x32_bf16 v[192:195], a[72:75], a[104:107], v[192:195]\n"
"v_mul_f32 v13, v13, v185\n"
"v_mfma_f32_16x16x32_bf16 v[192:195], a[76:79], a[108:111], v[192:195]\n"
"v_mul_f32 v14, v14, v186\n"
"v_mfma_f32_16x16x32_bf16 v[164:167], a[68:71], a[108:111], v[164:167]\n"
"v_mul_f32 v15, v15, v187\n"
"v_mfma_f32_16x16x32_bf16 v[164:167], a[64:67], a[104:107], v[164:167]\n"
"v_cvt_pk_bf16_f32 v26, v12, v13\n"
"v_mfma_f32_16x16x32_bf16 v[168:171], v[88:91], a[112:115], v[168:171]\n"
"v_cvt_pk_bf16_f32 v27, v14, v15\n"
"v_mfma_f32_16x16x32_bf16 v[168:171], v[116:119], a[116:119], v[168:171]\n"
"s_waitcnt lgkmcnt(3)\n"
"buffer_store_dwordx4 v[180:183], v6, s[52:55], s85 offen offset:1152 sc1\n"
"ds_write_b64 v7, v[26:27] offset:38920\n"
"v_mfma_f32_16x16x32_bf16 v[196:199], v[120:123], a[116:119], v[196:199]\n"
"v_accvgpr_read_b32 v16, a220\n"
"v_accvgpr_read_b32 v17, a221\n"
"v_mfma_f32_16x16x32_bf16 v[196:199], v[92:95], a[112:115], v[196:199]\n"
"v_accvgpr_read_b32 v18, a222\n"
"v_accvgpr_read_b32 v19, a223\n"
"v_mfma_f32_16x16x32_bf16 v[224:227], v[32:35], a[112:115], v[224:227]\n"
"v_lshlrev_b32 v12, 16, v16\n"
"v_and_b32 v13, s63, v16\n"
"v_mfma_f32_16x16x32_bf16 v[224:227], v[60:63], a[116:119], v[224:227]\n"
"v_lshlrev_b32 v14, 16, v17\n"
"v_and_b32 v15, s63, v17\n"
"v_mfma_f32_16x16x32_bf16 v[252:255], v[124:127], a[116:119], v[252:255]\n"
"v_mul_f32 v12, v12, v212\n"
"v_mul_f32 v13, v13, v213\n"
"v_mfma_f32_16x16x32_bf16 v[252:255], v[96:99], a[112:115], v[252:255]\n"
"v_mul_f32 v14, v14, v214\n"
"v_mul_f32 v15, v15, v215\n"
"v_mfma_f32_16x16x32_bf16 v[252:255], v[108:111], a[120:123], v[252:255]\n"
"v_cvt_pk_bf16_f32 v28, v12, v13\n"
"v_cvt_pk_bf16_f32 v29, v14, v15\n"
"v_mfma_f32_16x16x32_bf16 v[252:255], v[136:139], a[124:127], v[252:255]\n"
"ds_write_b64 v8, v[28:29] offset:38912\n"
"v_mfma_f32_16x16x32_bf16 v[224:227], v[132:135], a[124:127], v[224:227]\n"
"v_lshlrev_b32 v12, 16, v18\n"
"v_and_b32 v13, s63, v18\n"
"v_mfma_f32_16x16x32_bf16 v[224:227], v[104:107], a[120:123], v[224:227]\n"
"v_lshlrev_b32 v14, 16, v19\n"
"v_and_b32 v15, s63, v19\n"
"v_mfma_f32_16x16x32_bf16 v[196:199], v[36:39], a[120:123], v[196:199]\n"
"v_mul_f32 v12, v12, v240\n"
"v_mul_f32 v13, v13, v241\n"
"v_mfma_f32_16x16x32_bf16 v[196:199], v[64:67], a[124:127], v[196:199]\n"
"v_mul_f32 v14, v14, v242\n"
"v_mul_f32 v15, v15, v243\n"
"v_mfma_f32_16x16x32_bf16 v[168:171], v[128:131], a[124:127], v[168:171]\n"
"v_cvt_pk_bf16_f32 v30, v12, v13\n"
"v_cvt_pk_bf16_f32 v31, v14, v15\n"
"v_mfma_f32_16x16x32_bf16 v[168:171], v[100:103], a[120:123], v[168:171]\n"
"ds_write_b64 v8, v[30:31] offset:38920\n"
"v_mfma_f32_16x16x32_bf16 v[168:171], a[64:67], a[128:131], v[168:171]\n"
"ds_read_b128 v[156:159], v2 offset:38912\n"
"v_mfma_f32_16x16x32_bf16 v[168:171], a[68:71], a[132:135], v[168:171]\n"
"ds_read_b128 v[184:187], v2 offset:39936\n"
"v_accvgpr_read_b32 v16, a224\n"
"v_mfma_f32_16x16x32_bf16 v[196:199], a[76:79], a[132:135], v[196:199]\n"
"v_accvgpr_read_b32 v17, a225\n"
"v_accvgpr_read_b32 v18, a226\n"
"v_mfma_f32_16x16x32_bf16 v[196:199], a[72:75], a[128:131], v[196:199]\n"
"v_accvgpr_read_b32 v19, a227\n"
"v_lshlrev_b32 v12, 16, v16\n"
"v_mfma_f32_16x16x32_bf16 v[224:227], a[80:83], a[128:131], v[224:227]\n"
"v_and_b32 v13, s63, v16\n"
"v_lshlrev_b32 v14, 16, v17\n"
"v_mfma_f32_16x16x32_bf16 v[224:227], a[84:87], a[132:135], v[224:227]\n"
"v_and_b32 v15, s63, v17\n"
"v_mul_f32 v12, v12, v160\n"
"v_mfma_f32_16x16x32_bf16 v[252:255], a[92:95], a[132:135], v[252:255]\n"
"v_mul_f32 v13, v13, v161\n"
"v_mul_f32 v14, v14, v162\n"
"v_mfma_f32_16x16x32_bf16 v[252:255], a[88:91], a[128:131], v[252:255]\n"
"v_mul_f32 v15, v15, v163\n"
"v_cvt_pk_bf16_f32 v24, v12, v13\n"
"v_mfma_f32_16x16x32_bf16 a[28:31], v[96:99], a[136:139], a[28:31]\n"
"v_cvt_pk_bf16_f32 v25, v14, v15\n"
"v_mfma_f32_16x16x32_bf16 a[28:31], v[124:127], a[140:143], a[28:31]\n"
"s_waitcnt lgkmcnt(1)\n"
"buffer_store_dwordx4 v[156:159], v6, s[52:55], s86 offen offset:2176 sc1\n"
"ds_write_b64 v7, v[24:25] offset:32768\n"
"v_mfma_f32_16x16x32_bf16 a[24:27], v[60:63], a[140:143], a[24:27]\n"
"v_lshlrev_b32 v12, 16, v18\n"
"v_and_b32 v13, s63, v18\n"
"v_mfma_f32_16x16x32_bf16 a[24:27], v[32:35], a[136:139], a[24:27]\n"
"v_lshlrev_b32 v14, 16, v19\n"
"v_and_b32 v15, s63, v19\n"
"v_mfma_f32_16x16x32_bf16 a[20:23], v[92:95], a[136:139], a[20:23]\n"
"v_mul_f32 v12, v12, v188\n"
"v_mul_f32 v13, v13, v189\n"
"v_mfma_f32_16x16x32_bf16 a[20:23], v[120:123], a[140:143], a[20:23]\n"
"v_mul_f32 v14, v14, v190\n"
"v_mul_f32 v15, v15, v191\n"
"v_mfma_f32_16x16x32_bf16 a[16:19], v[116:119], a[140:143], a[16:19]\n"
"v_cvt_pk_bf16_f32 v26, v12, v13\n"
"v_cvt_pk_bf16_f32 v27, v14, v15\n"
"v_mfma_f32_16x16x32_bf16 a[16:19], v[88:91], a[136:139], a[16:19]\n"
"ds_write_b64 v7, v[26:27] offset:32776\n"
"v_mfma_f32_16x16x32_bf16 a[16:19], v[100:103], a[144:147], a[16:19]\n"
"v_accvgpr_read_b32 v16, a228\n"
"v_accvgpr_read_b32 v17, a229\n"
"v_mfma_f32_16x16x32_bf16 a[16:19], v[128:131], a[148:151], a[16:19]\n"
"v_accvgpr_read_b32 v18, a230\n"
"v_accvgpr_read_b32 v19, a231\n"
"v_mfma_f32_16x16x32_bf16 a[20:23], v[64:67], a[148:151], a[20:23]\n"
"v_lshlrev_b32 v12, 16, v16\n"
"v_and_b32 v13, s63, v16\n"
"v_mfma_f32_16x16x32_bf16 a[20:23], v[36:39], a[144:147], a[20:23]\n"
"s_waitcnt lgkmcnt(2)\n"
"buffer_store_dwordx4 v[184:187], v6, s[52:55], s87 offen offset:3200 sc1\n"
"v_lshlrev_b32 v14, 16, v17\n"
"v_and_b32 v15, s63, v17\n"
"v_mul_f32 v12, v12, v216\n"
"v_mfma_f32_16x16x32_bf16 a[24:27], v[104:107], a[144:147], a[24:27]\n"
"v_mul_f32 v13, v13, v217\n"
"v_mul_f32 v14, v14, v218\n"
"v_mfma_f32_16x16x32_bf16 a[24:27], v[132:135], a[148:151], a[24:27]\n"
"v_mul_f32 v15, v15, v219\n"
"v_mfma_f32_16x16x32_bf16 a[28:31], v[136:139], a[148:151], a[28:31]\n"
"v_cvt_pk_bf16_f32 v28, v12, v13\n"
"v_cvt_pk_bf16_f32 v29, v14, v15\n"
"v_mfma_f32_16x16x32_bf16 a[28:31], v[108:111], a[144:147], a[28:31]\n"
"ds_write_b64 v8, v[28:29] offset:32768\n"
"v_mfma_f32_16x16x32_bf16 a[28:31], a[88:91], a[152:155], a[28:31]\n"
"v_lshlrev_b32 v12, 16, v18\n"
"v_and_b32 v13, s63, v18\n"
"v_mfma_f32_16x16x32_bf16 a[28:31], a[92:95], a[156:159], a[28:31]\n"
"v_lshlrev_b32 v14, 16, v19\n"
"v_and_b32 v15, s63, v19\n"
"v_mfma_f32_16x16x32_bf16 a[24:27], a[84:87], a[156:159], a[24:27]\n"
"v_mul_f32 v12, v12, v244\n"
"v_mul_f32 v13, v13, v245\n"
"v_mfma_f32_16x16x32_bf16 a[24:27], a[80:83], a[152:155], a[24:27]\n"
"v_mul_f32 v14, v14, v246\n"
"v_mul_f32 v15, v15, v247\n"
"v_mfma_f32_16x16x32_bf16 a[20:23], a[72:75], a[152:155], a[20:23]\n"
"s_add_u32 s62, s62, 1\n"
"v_cvt_pk_bf16_f32 v30, v12, v13\n"
"v_cvt_pk_bf16_f32 v31, v14, v15\n"
"v_mfma_f32_16x16x32_bf16 a[20:23], a[76:79], a[156:159], a[20:23]\n"
"ds_write_b64 v8, v[30:31] offset:32776\n"
"v_mfma_f32_16x16x32_bf16 a[16:19], a[68:71], a[156:159], a[16:19]\n"
"ds_read_b128 v[160:163], v2 offset:32768\n"
"v_mfma_f32_16x16x32_bf16 a[16:19], a[64:67], a[152:155], a[16:19]\n"
"ds_read_b128 v[188:191], v2 offset:33792\n"
"s_nop 7\n"
"v_accvgpr_read_b32 v16, a232\n"
"v_accvgpr_read_b32 v17, a233\n"
"v_accvgpr_read_b32 v18, a234\n"
"v_accvgpr_read_b32 v19, a235\n"
"v_lshlrev_b32 v12, 16, v16\n"
"v_and_b32 v13, s63, v16\n"
"v_lshlrev_b32 v14, 16, v17\n"
"v_and_b32 v15, s63, v17\n"
"v_pk_mul_f32 v[12:13], v[12:13], v[164:165]\n"
"v_pk_mul_f32 v[14:15], v[14:15], v[166:167]\n"
"v_cvt_pk_bf16_f32 v24, v12, v13\n"
"v_cvt_pk_bf16_f32 v25, v14, v15\n"
"v_lshlrev_b32 v12, 16, v18\n"
"v_and_b32 v13, s63, v18\n"
"v_lshlrev_b32 v14, 16, v19\n"
"v_and_b32 v15, s63, v19\n"
"v_pk_mul_f32 v[12:13], v[12:13], v[192:193]\n"
"v_pk_mul_f32 v[14:15], v[14:15], v[194:195]\n"
"v_cvt_pk_bf16_f32 v26, v12, v13\n"
"v_cvt_pk_bf16_f32 v27, v14, v15\n"
"ds_write_b128 v7, v[24:27] offset:34816\n"
"s_waitcnt lgkmcnt(2)\n"
"buffer_store_dwordx4 v[160:163], v6, s[52:55], s88 offen offset:128 sc1\n"
"s_waitcnt lgkmcnt(1)\n"
"buffer_store_dwordx4 v[188:191], v6, s[52:55], s89 offen offset:1152 sc1\n"
"v_accvgpr_read_b32 v16, a236\n"
"v_accvgpr_read_b32 v17, a237\n"
"v_accvgpr_read_b32 v18, a238\n"
"v_accvgpr_read_b32 v19, a239\n"
"v_lshlrev_b32 v12, 16, v16\n"
"v_and_b32 v13, s63, v16\n"
"v_lshlrev_b32 v14, 16, v17\n"
"v_and_b32 v15, s63, v17\n"
"v_pk_mul_f32 v[12:13], v[12:13], v[220:221]\n"
"v_pk_mul_f32 v[14:15], v[14:15], v[222:223]\n"
"v_cvt_pk_bf16_f32 v28, v12, v13\n"
"v_cvt_pk_bf16_f32 v29, v14, v15\n"
"v_lshlrev_b32 v12, 16, v18\n"
"v_and_b32 v13, s63, v18\n"
"v_lshlrev_b32 v14, 16, v19\n"
"v_and_b32 v15, s63, v19\n"
"v_pk_mul_f32 v[12:13], v[12:13], v[248:249]\n"
"v_pk_mul_f32 v[14:15], v[14:15], v[250:251]\n"
"v_cvt_pk_bf16_f32 v30, v12, v13\n"
"v_cvt_pk_bf16_f32 v31, v14, v15\n"
"ds_write_b128 v8, v[28:31] offset:34816\n"
"ds_read_b128 v[164:167], v2 offset:34816\n"
"ds_read_b128 v[192:195], v2 offset:35840\n"
"v_accvgpr_read_b32 v16, a240\n"
"v_accvgpr_read_b32 v17, a241\n"
"v_accvgpr_read_b32 v18, a242\n"
"v_accvgpr_read_b32 v19, a243\n"
"v_lshlrev_b32 v12, 16, v16\n"
"v_and_b32 v13, s63, v16\n"
"v_lshlrev_b32 v14, 16, v17\n"
"v_and_b32 v15, s63, v17\n"
"v_pk_mul_f32 v[12:13], v[12:13], v[168:169]\n"
"v_pk_mul_f32 v[14:15], v[14:15], v[170:171]\n"
"v_cvt_pk_bf16_f32 v24, v12, v13\n"
"v_cvt_pk_bf16_f32 v25, v14, v15\n"
"v_lshlrev_b32 v12, 16, v18\n"
"v_and_b32 v13, s63, v18\n"
"v_lshlrev_b32 v14, 16, v19\n"
"v_and_b32 v15, s63, v19\n"
"v_pk_mul_f32 v[12:13], v[12:13], v[196:197]\n"
"v_pk_mul_f32 v[14:15], v[14:15], v[198:199]\n"
"v_cvt_pk_bf16_f32 v26, v12, v13\n"
"v_cvt_pk_bf16_f32 v27, v14, v15\n"
"ds_write_b128 v7, v[24:27] offset:36864\n"
"s_waitcnt lgkmcnt(2)\n"
"buffer_store_dwordx4 v[164:167], v6, s[52:55], s90 offen offset:2176 sc1\n"
"s_waitcnt lgkmcnt(1)\n"
"buffer_store_dwordx4 v[192:195], v6, s[52:55], s91 offen offset:3200 sc1\n"
"v_accvgpr_read_b32 v16, a244\n"
"v_accvgpr_read_b32 v17, a245\n"
"v_accvgpr_read_b32 v18, a246\n"
"v_accvgpr_read_b32 v19, a247\n"
"v_lshlrev_b32 v12, 16, v16\n"
"v_and_b32 v13, s63, v16\n"
"v_lshlrev_b32 v14, 16, v17\n"
"v_and_b32 v15, s63, v17\n"
"v_pk_mul_f32 v[12:13], v[12:13], v[224:225]\n"
"v_pk_mul_f32 v[14:15], v[14:15], v[226:227]\n"
"v_cvt_pk_bf16_f32 v28, v12, v13\n"
"v_cvt_pk_bf16_f32 v29, v14, v15\n"
"v_lshlrev_b32 v12, 16, v18\n"
"v_and_b32 v13, s63, v18\n"
"v_lshlrev_b32 v14, 16, v19\n"
"v_and_b32 v15, s63, v19\n"
"v_pk_mul_f32 v[12:13], v[12:13], v[252:253]\n"
"v_pk_mul_f32 v[14:15], v[14:15], v[254:255]\n"
"v_cvt_pk_bf16_f32 v30, v12, v13\n"
"v_cvt_pk_bf16_f32 v31, v14, v15\n"
"ds_write_b128 v8, v[28:31] offset:36864\n"
"ds_read_b128 v[168:171], v2 offset:36864\n"
"ds_read_b128 v[196:199], v2 offset:37888\n"
"v_accvgpr_read_b32 v16, a248\n"
"v_accvgpr_read_b32 v17, a249\n"
"v_accvgpr_read_b32 v18, a250\n"
"v_accvgpr_read_b32 v19, a251\n"
"v_lshlrev_b32 v12, 16, v16\n"
"v_and_b32 v13, s63, v16\n"
"v_lshlrev_b32 v14, 16, v17\n"
"v_and_b32 v15, s63, v17\n"
"v_accvgpr_read_b32 v10, a16\n"
"v_accvgpr_read_b32 v11, a17\n"
"v_mul_f32 v12, v12, v10\n"
"v_mul_f32 v13, v13, v11\n"
"v_accvgpr_read_b32 v10, a18\n"
"v_accvgpr_read_b32 v11, a19\n"
"v_mul_f32 v14, v14, v10\n"
"v_mul_f32 v15, v15, v11\n"
"v_cvt_pk_bf16_f32 v24, v12, v13\n"
"v_cvt_pk_bf16_f32 v25, v14, v15\n"
"v_lshlrev_b32 v12, 16, v18\n"
"v_and_b32 v13, s63, v18\n"
"v_lshlrev_b32 v14, 16, v19\n"
"v_and_b32 v15, s63, v19\n"
"v_accvgpr_read_b32 v10, a20\n"
"v_accvgpr_read_b32 v11, a21\n"
"v_mul_f32 v12, v12, v10\n"
"v_mul_f32 v13, v13, v11\n"
"v_accvgpr_read_b32 v10, a22\n"
"v_accvgpr_read_b32 v11, a23\n"
"v_mul_f32 v14, v14, v10\n"
"v_mul_f32 v15, v15, v11\n"
"v_cvt_pk_bf16_f32 v26, v12, v13\n"
"v_cvt_pk_bf16_f32 v27, v14, v15\n"
"ds_write_b128 v7, v[24:27] offset:38912\n"
"s_waitcnt lgkmcnt(2)\n"
"buffer_store_dwordx4 v[168:171], v6, s[52:55], s92 offen offset:128 sc1\n"
"s_waitcnt lgkmcnt(1)\n"
"buffer_store_dwordx4 v[196:199], v6, s[52:55], s93 offen offset:1152 sc1\n"
"v_accvgpr_read_b32 v16, a252\n"
"v_accvgpr_read_b32 v17, a253\n"
"v_accvgpr_read_b32 v18, a254\n"
"v_accvgpr_read_b32 v19, a255\n"
"v_lshlrev_b32 v12, 16, v16\n"
"v_and_b32 v13, s63, v16\n"
"v_lshlrev_b32 v14, 16, v17\n"
"v_and_b32 v15, s63, v17\n"
"v_accvgpr_read_b32 v10, a24\n"
"v_accvgpr_read_b32 v11, a25\n"
"v_mul_f32 v12, v12, v10\n"
"v_mul_f32 v13, v13, v11\n"
"v_accvgpr_read_b32 v10, a26\n"
"v_accvgpr_read_b32 v11, a27\n"
"v_mul_f32 v14, v14, v10\n"
"v_mul_f32 v15, v15, v11\n"
"v_cvt_pk_bf16_f32 v28, v12, v13\n"
"v_cvt_pk_bf16_f32 v29, v14, v15\n"
"v_lshlrev_b32 v12, 16, v18\n"
"v_and_b32 v13, s63, v18\n"
"v_lshlrev_b32 v14, 16, v19\n"
"v_and_b32 v15, s63, v19\n"
"v_accvgpr_read_b32 v10, a28\n"
"v_accvgpr_read_b32 v11, a29\n"
"v_mul_f32 v12, v12, v10\n"
"v_mul_f32 v13, v13, v11\n"
"v_accvgpr_read_b32 v10, a30\n"
"v_accvgpr_read_b32 v11, a31\n"
"v_mul_f32 v14, v14, v10\n"
"v_mul_f32 v15, v15, v11\n"
"v_cvt_pk_bf16_f32 v30, v12, v13\n"
"v_cvt_pk_bf16_f32 v31, v14, v15\n"
"ds_write_b128 v8, v[28:31] offset:38912\n"
"ds_read_b128 v[224:227], v2 offset:38912\n"
"ds_read_b128 v[252:255], v2 offset:39936\n"
"s_waitcnt lgkmcnt(1)\n"
"buffer_store_dwordx4 v[224:227], v6, s[52:55], s94 offen offset:2176 sc1\n"
"s_waitcnt lgkmcnt(0)\n"
"buffer_store_dwordx4 v[252:255], v6, s[52:55], s95 offen offset:3200 sc1\n"
"s_or_b32 s66, %26, %16\n"
"s_cbranch_scc1 L_end\n"
"v_mov_b32 v10, s78\n"
"v_mov_b32 v11, s79\n"
"s_mov_b32 s66, 139264\n"
"buffer_store_dwordx2 v[10:11], off, s[56:59], s66 sc1\n"
"s_branch L_end\n"
"L_slow0:\n"
"s_cmp_eq_u32 s68, 0\n"
"s_cbranch_scc1 L_back0\n"
"s_sub_u32 s68, s68, 1\n"
"s_sleep 2\n"
"buffer_load_dwordx2 v[8:9], off, s[56:59], %14 sc1\n"
"s_waitcnt vmcnt(0)\n"
"v_subrev_u32 v8, s65, v8\n"
"v_xor_b32 v9, s79, v9\n"
"v_or_b32 v8, v8, v9\n"
"v_cmp_lt_u32 vcc, 15, v8\n"
"s_nop 4\n"
"s_cbranch_vccnz L_slow0\n"
"s_branch L_back0\n"
"L_slow1:\n"
"s_cmp_eq_u32 s68, 0\n"
"s_cbranch_scc1 L_back1\n"
"s_sub_u32 s68, s68, 1\n"
"s_sleep 2\n"
"buffer_load_dwordx2 v[8:9], off, s[56:59], %14 sc1\n"
"s_waitcnt vmcnt(0)\n"
"v_subrev_u32 v8, s65, v8\n"
"v_xor_b32 v9, s79, v9\n"
"v_or_b32 v8, v8, v9\n"
"v_cmp_lt_u32 vcc, 15, v8\n"
"s_nop 4\n"
"s_cbranch_vccnz L_slow1\n"
"s_branch L_back1\n"
"L_slow2:\n"
"s_cmp_eq_u32 s68, 0\n"
"s_cbranch_scc1 L_back2\n"
"s_sub_u32 s68, s68, 1\n"
"s_sleep 2\n"
"buffer_load_dwordx4 v[12:15], v0, s[56:59], %25 offen sc1\n"
"s_waitcnt vmcnt(0)\n"
"v_subrev_u32 v12, s67, v12\n"
"v_xor_b32 v13, s79, v13\n"
"v_subrev_u32 v14, s67, v14\n"
"v_xor_b32 v15, s79, v15\n"
"v_or_b32 v12, v12, v14\n"
"v_or_b32 v13, v13, v15\n"
"v_or_b32 v12, v12, v13\n"
"v_cmp_lt_u32 vcc, 15, v12\n"
"s_nop 4\n"
"s_cbranch_vccnz L_slow2\n"
"s_branch L_back2\n"
"L_slow3:\n"
"s_cmp_eq_u32 s68, 0\n"
"s_cbranch_scc1 L_back3\n"
"s_sub_u32 s68, s68, 1\n"
"s_sleep 2\n"
"buffer_load_dwordx4 v[24:27], v0, s[56:59], %25 offen sc1\n"
"s_waitcnt vmcnt(0)\n"
"v_subrev_u32 v24, s70, v24\n"
"v_xor_b32 v25, s79, v25\n"
"v_subrev_u32 v26, s70, v26\n"
"v_xor_b32 v27, s79, v27\n"
"v_or_b32 v24, v24, v26\n"
"v_or_b32 v25, v25, v27\n"
"v_or_b32 v24, v24, v25\n"
"v_cmp_lt_u32 vcc, 15, v24\n"
"s_nop 4\n"
"s_cbranch_vccnz L_slow3\n"
"s_branch L_back3\n"
"L_slow4:\n"
"s_cmp_eq_u32 s68, 0\n"
"s_cbranch_scc1 L_back4\n"
"s_sub_u32 s68, s68, 1\n"
"s_sleep 2\n"
"buffer_load_dwordx2 v[8:9], off, s[56:59], %14 sc1\n"
"s_waitcnt vmcnt(0)\n"
"v_subrev_u32 v8, s65, v8\n"
"v_xor_b32 v9, s79, v9\n"
"v_or_b32 v8, v8, v9\n"
"v_cmp_lt_u32 vcc, 15, v8\n"
"s_nop 4\n"
"s_cbranch_vccnz L_slow4\n"
"s_branch L_back4\n"
"L_slow7:\n"
"s_cmp_eq_u32 s68, 0\n"
"s_cbranch_scc1 L_back7\n"
"s_sub_u32 s68, s68, 1\n"
"s_sleep 2\n"
"buffer_load_dwordx2 v[8:9], off, s[56:59], %14 sc1\n"
"s_waitcnt vmcnt(0)\n"
"v_subrev_u32 v8, s65, v8\n"
"v_xor_b32 v9, s79, v9\n"
"v_or_b32 v8, v8, v9\n"
"v_cmp_lt_u32 vcc, 15, v8\n"
"s_nop 4\n"
"s_cbranch_vccnz L_slow7\n"
"s_branch L_back7\n"
"L_headF:\n"
"L_end:\n"
"s_waitcnt vmcnt(0) lgkmcnt(0)\n"
    :: LO(wsb), HI(wsb), LO(xo), HI(xo), LO(ob), HI(ob), LO(wr), HI(wr), LO(bb), HI(bb), "s"(wo), "s"(eo), "s"(ep), "s"(fo), "s"(fp),
       "s"(ldsa), "s"(w), "s"(col), "v"(threadIdx.x), "s"(db), "s"(rs1), "s"(rs2), "s"(rd1), "s"(rn1), "s"(wfo), "s"(wfa), "s"(b)
    : "memory", "scc", "vcc",
"v1","v2","v3","v4","v5","v6","v7","v8","v9","v10","v11","v12","v13","v14","v15","v16","v17","v18","v19","v20","v21","v22","v23","v24","v25","v26","v27","v28","v29","v30","v31","v32","v33","v34","v35","v36","v37","v38","v39","v40","v41","v42","v43","v44","v45","v46","v47","v48","v49","v50","v51","v52","v53","v54","v55","v56","v57","v58","v59","v60","v61","v62","v63","v64","v65","v66","v67","v68","v69","v70","v71","v72","v73","v74","v75","v76","v77","v78","v79","v80","v81","v82","v83","v84","v85","v86","v87","v88","v89","v90","v91","v92","v93","v94","v95","v96","v97","v98","v99","v100","v101","v102","v103","v104","v105","v106","v107","v108","v109","v110","v111","v112","v113","v114","v115","v116","v117","v118","v119","v120","v121","v122","v123","v124","v125","v126","v127","v128","v129","v130","v131","v132","v133","v134","v135","v136","v137","v138","v139","v140","v141","v142","v143","v144","v145","v146","v147","v148","v149","v150","v151","v152","v153","v154","v155","v156","v157","v158","v159","v160","v161","v162","v163","v164","v165","v166","v167","v168","v169","v170","v171","v172","v173","v174","v175","v176","v177","v178","v179","v180","v181","v182","v183","v184","v185","v186","v187","v188","v189","v190","v191","v192","v193","v194","v195","v196","v197","v198","v199","v200","v201","v202","v203","v204","v205","v206","v207","v208","v209","v210","v211","v212","v213","v214","v215","v216","v217","v218","v219","v220","v221","v222","v223","v224","v225","v226","v227","v228","v229","v230","v231","v232","v233","v234","v235","v236","v237","v238","v239","v240","v241","v242","v243","v244","v245","v246","v247","v248","v249","v250","v251","v252","v253","v254","v255",
"a0","a1","a2","a3","a4","a5","a6","a7","a8","a9","a10","a11","a12","a13","a14","a15","a16","a17","a18","a19","a20","a21","a22","a23","a24","a25","a26","a27","a28","a29","a30","a31","a32","a33","a34","a35","a36","a37","a38","a39","a40","a41","a42","a43","a44","a45","a46","a47","a48","a49","a50","a51","a52","a53","a54","a55","a56","a57","a58","a59","a60","a61","a62","a63","a64","a65","a66","a67","a68","a69","a70","a71","a72","a73","a74","a75","a76","a77","a78","a79","a80","a81","a82","a83","a84","a85","a86","a87","a88","a89","a90","a91","a92","a93","a94","a95","a96","a97","a98","a99","a100","a101","a102","a103","a104","a105","a106","a107","a108","a109","a110","a111","a112","a113","a114","a115","a116","a117","a118","a119","a120","a121","a122","a123","a124","a125","a126","a127","a128","a129","a130","a131","a132","a133","a134","a135","a136","a137","a138","a139","a140","a141","a142","a143","a144","a145","a146","a147","a148","a149","a150","a151","a152","a153","a154","a155","a156","a157","a158","a159","a160","a161","a162","a163","a164","a165","a166","a167","a168","a169","a170","a171","a172","a173","a174","a175","a176","a177","a178","a179","a180","a181","a182","a183","a184","a185","a186","a187","a188","a189","a190","a191","a192","a193","a194","a195","a196","a197","a198","a199","a200","a201","a202","a203","a204","a205","a206","a207","a208","a209","a210","a211","a212","a213","a214","a215","a216","a217","a218","a219","a220","a221","a222","a223","a224","a225","a226","a227","a228","a229","a230","a231","a232","a233","a234","a235","a236","a237","a238","a239","a240","a241","a242","a243","a244","a245","a246","a247","a248","a249","a250","a251","a252","a253","a254","a255",
"s27","s28","s29","s30","s31","s33","s34","s35","s36","s37","s38","s39","s40","s41","s42","s43","s44","s45","s46","s47","s48","s49","s50","s51","s52","s53","s54","s55","s56","s57","s58","s59","s60","s61","s62","s63","s64","s65","s66","s67","s68","s69","s70","s71","s72","s73","s74","s75","s76","s77","s78","s79","s80","s81","s82","s83","s84","s85","s86","s87","s88","s89","s90","s91","s92","s93","s94","s95","s96","s97","s98","s99");
}

extern "C" void kernel_launch(void* const* d_in, const int* in_sizes, int n_in,
                              void* d_out, int out_size, void* d_ws, size_t ws_size,
                              hipStream_t stream) {
  const unsigned short* x0 = (const unsigned short*)d_in[0];
  const unsigned short* ws = (const unsigned short*)d_in[1];
  const unsigned short* bs = (const unsigned short*)d_in[2];
  dcn_cross_kernel<<<256, 256, 0, stream>>>(x0, ws, bs, (unsigned char*)d_ws, (unsigned short*)d_out);
}
```

```cpp
#include <hip/hip_runtime.h>


constexpr int kD = 1024;
constexpr unsigned kWFlagOff = 131072, kWOff = 1u << 20, kWStream = 200 * 8192, kXchOff = 32u << 20, kDbgOff = 224u << 20;

__device__ __forceinline__ unsigned unit_src(unsigned v, unsigned h, unsigned w) {
  const unsigned l = v >> 5, u = v & 31;
  const unsigned split = ((0xff0000ffu >> u) & 1) - 1;
  const unsigned part = l ? u >> 4 : 0u, g = (0xff00ff00u >> u) & 1;
  const unsigned c = u >> (split & 1), jh = (u >> (~split & 2)) & 1;
  const unsigned kc = 8 * (h ^ part) + 2 * ((w + c) & 3) + g;
  return (l * 1024 + 64 * kc) * 2048 + jh * 128;
}

__global__ __launch_bounds__(256, 1) __attribute__((aligned(4096)))
void dcn_cross_kernel(const unsigned short* __restrict__ x0, const unsigned short* __restrict__ ws, const unsigned short* __restrict__ bs,
                      unsigned char* __restrict__ wsp, unsigned short* __restrict__ out) {
  __shared__ __attribute__((aligned(16))) unsigned char lds[163840];
  const unsigned b = blockIdx.x;
  constexpr unsigned kPx = 1 ? 1u : 8u;
  const unsigned h = 1 ? (b & 1) : ((b >> 3) & 1), rb = 1 ? (b >> 1) : ((b >> 4) * 8 + (b & 7));
  const unsigned w = __builtin_amdgcn_readfirstlane(threadIdx.x >> 6);
  unsigned col = (512 * h + 128 * w) * 2, rows = rb * 128 * kD * 2, oofs = rows + col;
  unsigned wo = (h * 4 + w) * kWStream, eo = kXchOff + b * 524288 + w * 32768, ep = kXchOff + (b ^ kPx) * 524288 + w * 32768;
  unsigned fo = b * 512 + w * 128, fp = (b ^ kPx) * 512 + w * 128;
  unsigned db = kDbgOff + (b * 4 + w) * 512;
  const unsigned v1 = 24 + (rb >> 1), v2 = 64 + ((rb >> 1) & 31), hk = rb & 1, h2 = rb >> 6;
  unsigned rs1 = unit_src(v1, 0, w) + hk * 65536, rd1 = v1 * 8192 + hk * 1024;
  unsigned rs2 = unit_src(v2, h2, w) + hk * 65536 + h2 * 1024 + h * ((4u << 20) - 1024), rn1 = v2 * 8192 + hk * 1024 + h2 * 4 * kWStream;
  unsigned wfa = kWFlagOff + w * 1024, wfo = wfa + h * 4096 + rb * 8;
  asm volatile("" : "+s"(col), "+s"(rows), "+s"(oofs), "+s"(wo), "+s"(eo), "+s"(ep), "+s"(fo), "+s"(fp), "+s"(db), "+s"(rs1), "+s"(rd1), "+s"(rs2), "+s"(rn1), "+s"(wfa), "+s"(wfo));
  __builtin_amdgcn_sched_barrier(0);
  const unsigned long long wsb = (unsigned long long)wsp, bb = (unsigned long long)bs;
  const unsigned long long xo = (unsigned long long)x0 + rows, ob = (unsigned long long)out + oofs, wr = (unsigned long long)ws + col;
  wo += (unsigned)wsb > ~(15u << 20) ? 16u << 20 : kWOff;
  const unsigned xa = (0u - (unsigned)wsb) & 0x1fffffu;
  eo += xa; ep += xa;
  const unsigned ldsa = (unsigned)(size_t)(__attribute__((address_space(3))) unsigned char*)lds;
#define LO(x) "s"((unsigned)(x))
#define HI(x) "s"((unsigned)((x) >> 32))
  asm volatile(
"L_main_entry:\n"
"v_and_b32 v12, 63, %18\n"
"v_lshrrev_b32 v13, 3, v12\n"
"v_and_b32 v15, 7, v12\n"
"v_lshlrev_b32 v10, 11, v13\n"
"s_andn2_b32 s60, 1024, %17\n"
"s_lshl_b32 s75, s60, 10\n"
"s_mov_b32 s52, %6\n"
"s_and_b32 s53, %7, 0xffff\n"
"s_mov_b32 s54, 12581120\n"
"s_mov_b32 s55, 0x20000\n"
"s_getpc_b64 s[96:97]\n"
"L_pc:\n"
"s_lshr_b32 s66, %26, 3\n"
"s_lshl_b32 s66, s66, 2\n"
"s_add_u32 s66, s66, %16\n"
"s_mul_i32 s66, s66, 704\n"
"s_and_b32 s97, s97, 0xffff\n"
"s_mov_b32 s98, L_end-L_pc\n"
"s_mov_b32 s99, 0x20000\n"
"s_min_u32 s66, s66, L_end-L_pc-704\n"
"v_min_u32 v9, 10, v12\n"
"v_lshlrev_b32 v9, 6, v9\n"
"buffer_load_dword a252, v9, s[96:99], s66 offen\n"
"v_xor_b32 v6, v15, v13\n"
"v_lshl_add_u32 v6, v6, 4, v10\n"
"s_mov_b32 s48, %2\n"
"s_and_b32 s49, %3, 0xffff\n"
"s_mov_b32 s50, 65536\n"
"s_mov_b32 s51, 0x20000\n"
"s_lshl_b32 s66, %16, 16\n"
"s_add_u32 s48, s48, s66\n"
"s_addc_u32 s49, s49, 0\n"
"s_lshl_b32 s72, %16, 12\n"
"s_add_u32 s72, s72, %15\n"
"s_mul_i32 s73, %16, 40960\n"
"s_add_u32 s64, s73, %15\n"
"v_lshrrev_b32 v11, 2, v12\n"
"v_and_b32 v11, 4, v11\n"
"v_xor_b32 v11, v11, v15\n"
"v_lshl_add_u32 v11, v11, 4, v10\n"
"s_bitcmp1_b32 %26, 0\n"
"s_cbranch_scc1 L_y\n"
"s_mov_b64 s[96:97], s[52:53]\n"
"s_cmp_lt_u32 %22, 524288\n"
"s_cselect_b32 s98, s54, 0\n"
"s_mov_b32 s99, 0x20000\n"
"s_xor_b32 s71, %20, 0x100000\n"
"s_add_u32 m0, s72, 139264\n"
"s_add_u32 s67, %20, 0\n"
"buffer_load_dwordx4 v11, s[96:99], s67 offen offset:0  lds\n"
"s_add_u32 s67, %20, 15360\n"
"buffer_load_dwordx4 v11, s[96:99], s67 offen offset:1024  lds\n"
"s_add_u32 s67, %20, 30720\n"
"buffer_load_dwordx4 v11, s[96:99], s67 offen offset:2048  lds\n"
"s_add_u32 s67, %20, 46080\n"
"buffer_load_dwordx4 v11, s[96:99], s67 offen offset:3072  lds\n"
"s_add_u32 m0, s72, 98304\n"
"s_add_u32 s67, s71, 1024\n"
"buffer_load_dwordx4 v11, s[96:99], s67 offen offset:0  lds\n"
"s_add_u32 s67, s71, 16384\n"
"buffer_load_dwordx4 v11, s[96:99], s67 offen offset:1024  lds\n"
"s_add_u32 s67, s71, 31744\n"
"buffer_load_dwordx4 v11, s[96:99], s67 offen offset:2048  lds\n"
"s_add_u32 s67, s71, 47104\n"
"buffer_load_dwordx4 v11, s[96:99], s67 offen offset:3072  lds\n"
"L_y:\n"
"s_add_u32 m0, s72, 16384\n"
"s_add_u32 s67, s60, 0\n"
"buffer_load_dwordx4 v6, s[48:51], s67 offen offset:0  lds\n"
"s_add_u32 s67, s60, 15360\n"
"buffer_load_dwordx4 v6, s[48:51], s67 offen offset:1024  lds\n"
"s_add_u32 s67, s60, 30720\n"
"buffer_load_dwordx4 v6, s[48:51], s67 offen offset:2048  lds\n"
"s_add_u32 s67, s60, 46080\n"
"buffer_load_dwordx4 v6, s[48:51], s67 offen offset:3072  lds\n"
"s_add_u32 m0, s64, 0\n"
"s_add_u32 s67, s75, 0\n"
"buffer_load_dwordx4 v11, s[52:55], s67 offen offset:0  lds\n"
"s_add_u32 s67, s75, 15360\n"
"buffer_load_dwordx4 v11, s[52:55], s67 offen offset:1024  lds\n"
"s_add_u32 s67, s75, 30720\n"
"buffer_load_dwordx4 v11, s[52:55], s67 offen offset:2048  lds\n"
"s_add_u32 s67, s75, 46080\n"
"buffer_load_dwordx4 v11, s[52:55], s67 offen offset:3072  lds\n"
"s_add_u32 m0, s64, 4096\n"
"s_add_u32 s67, s75, 65536\n"
"buffer_load_dwordx4 v11, s[52:55], s67 offen offset:0  lds\n"
"s_add_u32 s67, s75, 80896\n"
"buffer_load_dwordx4 v11, s[52:55], s67 offen offset:1024  lds\n"
"s_add_u32 s67, s75, 96256\n"
"buffer_load_dwordx4 v11, s[52:55], s67 offen offset:2048  lds\n"
"s_add_u32 s67, s75, 111616\n"
"buffer_load_dwordx4 v11, s[52:55], s67 offen offset:3072  lds\n"
"s_lshr_b32 s65, %26, 4\n"
"s_lshl_b32 s65, s65, 2\n"
"s_add_u32 s65, s65, %16\n"
"s_lshl_b32 s65, s65, 11\n"
"s_lshl_b32 s67, %16, 8\n"
"s_sub_u32 s65, s65, s67\n"
"v_and_b32 v14, 1, v13\n"
"v_lshlrev_b32 v14, 17, v14\n"
"v_lshl_add_u32 v14, v15, 7, v14\n"
"v_and_b32 v14, 0xfffffdff, v14\n"
"s_lshl_b32 s67, %26, 6\n"
"s_and_b32 s67, s67, 512\n"
"s_add_u32 s65, s65, s67\n"
"v_lshlrev_b32 v0, 4, v12\n"
"s_mov_b32 s56, %0\n"
"s_and_b32 s57, %1, 0xffff\n"
"s_mov_b32 s58, 171966464\n"
"s_mov_b32 s59, 0x20000\n"
"v_add_u32 v1, 4096, v0\n"
"s_add_u32 s36, s56, %10\n"
"s_addc_u32 s37, s57, 0\n"
"s_mov_b32 s38, 1572864\n"
"s_mov_b32 s39, 0x20000\n"
"s_add_u32 s34, s36, 200704\n"
"s_mov_b32 s35, s37\n"
"v_and_b32 v13, 15, v12\n"
"v_lshrrev_b32 v10, 4, v12\n"
"v_xor_b32 v15, v15, v10\n"
"v_lshlrev_b32 v7, 7, v13\n"
"v_lshl_add_u32 v7, v15, 4, v7\n"
"v_add_u32 v7, s64, v7\n"
"v_xor_b32 v8, 64, v7\n"
"v_bfe_u32 v13, v12, 2, 2\n"
"v_and_b32 v15, 3, v12\n"
"v_bfe_u32 v9, v12, 3, 1\n"
"v_lshl_add_u32 v15, v9, 2, v15\n"
"v_lshl_add_u32 v13, v10, 3, v13\n"
"v_lshlrev_b32 v13, 7, v13\n"
"v_lshl_add_u32 v9, v15, 4, v13\n"
"v_add_u32 v9, s64, v9\n"
"v_xor_b32 v15, 64, v9\n"
"s_mov_b32 s40, %2\n"
"s_and_b32 s41, %3, 0xffff\n"
"s_mov_b32 s42, 260352\n"
"s_mov_b32 s43, 0x20000\n"
"s_add_u32 s40, s40, %17\n"
"s_addc_u32 s41, s41, 0\n"
"s_mov_b32 s80, 0\n"
"s_mov_b32 s81, 15360\n"
"s_mov_b32 s82, 30720\n"
"s_mov_b32 s83, 46080\n"
"s_mov_b32 s84, 65536\n"
"s_mov_b32 s85, 80896\n"
"s_mov_b32 s86, 96256\n"
"s_mov_b32 s87, 111616\n"
"s_mov_b32 s88, 131072\n"
"s_mov_b32 s89, 146432\n"
"s_mov_b32 s90, 161792\n"
"s_mov_b32 s91, 177152\n"
"s_mov_b32 s92, 196608\n"
"s_mov_b32 s93, 211968\n"
"s_mov_b32 s94, 227328\n"
"s_mov_b32 s95, 242688\n"
"s_mov_b32 s62, 0\n"
"s_mov_b32 s63, 0xffff0000\n"
"s_mov_b32 s68, 0x8000\n"
"s_add_u32 s66, %16, 0\n"
"s_and_b32 s66, s66, 3\n"
"s_mul_i32 s66, s66, 40960\n"
"s_add_u32 s66, s66, %15\n"
"v_add_u32 v2, s66, v0\n"
"s_add_u32 s66, %16, 1\n"
"s_and_b32 s66, s66, 3\n"
"s_mul_i32 s66, s66, 40960\n"
"s_add_u32 s66, s66, %15\n"
"v_add_u32 v3, s66, v0\n"
"s_add_u32 s66, %16, 2\n"
"s_and_b32 s66, s66, 3\n"
"s_mul_i32 s66, s66, 40960\n"
"s_add_u32 s66, s66, %15\n"
"v_add_u32 v4, s66, v0\n"
"s_add_u32 s66, %16, 3\n"
"s_and_b32 s66, s66, 3\n"
"s_mul_i32 s66, s66, 40960\n"
"s_add_u32 s66, s66, %15\n"
"v_add_u32 v5, s66, v0\n"
"s_sub_u32 s66, 0, s73\n"
"v_add_u32 v12, s66, v7\n"
"v_add_u32 v13, s66, v8\n"
"s_mov_b64 exec, 0\n"
"s_cmp_eq_u32 %16, 0\n"
"s_cbranch_scc1 L_st534\n"
"s_cmp_eq_u32 %16, 1\n"
"s_cbranch_scc1 L_st1062\n"
"s_cmp_eq_u32 %16, 2\n"
"s_cbranch_scc1 L_st1591\n"
"s_branch L_st2135\n"
"L_retA:\n"
"s_mov_b64 exec, -1\n"
"s_nop 3\n"
"s_mov_b32 s44, %8\n"
"s_and_b32 s45, %9, 0xffff\n"
"s_mov_b32 s46, 12288\n"
"s_mov_b32 s47, 0x20000\n"
"s_mov_b32 s61, %17\n"
"v_lshrrev_b32 v16, 4, v0\n"
"v_and_b32 v16, 0x30, v16\n"
"buffer_load_dwordx2 v[18:19], v16, s[44:47], s61 offen offset:0\n"
"v_lshrrev_b32 v20, 4, v0\n"
"v_and_b32 v20, 0x30, v20\n"
"buffer_load_dwordx2 v[22:23], v20, s[44:47], s61 offen offset:8\n"
"v_lshrrev_b32 v24, 4, v0\n"
"v_and_b32 v24, 0x30, v24\n"
"buffer_load_dwordx2 v[26:27], v24, s[44:47], s61 offen offset:64\n"
"v_lshrrev_b32 v28, 4, v0\n"
"v_and_b32 v28, 0x30, v28\n"
"buffer_load_dwordx2 v[30:31], v28, s[44:47], s61 offen offset:72\n"
"s_add_u32 s70, s75, 131072\n"
"s_add_u32 s70, s70, s65\n"
"buffer_load_dword a252, v14, s[52:55], s70 offen\n"
"s_add_u32 m0, s64, 8192\n"
"s_add_u32 s67, s75, 128\n"
"buffer_load_dwordx4 v11, s[52:55], s67 offen offset:0  lds\n"
"s_add_u32 s67, s75, 15488\n"
"buffer_load_dwordx4 v11, s[52:55], s67 offen offset:1024  lds\n"
"s_add_u32 s67, s75, 30848\n"
"buffer_load_dwordx4 v11, s[52:55], s67 offen offset:2048  lds\n"
"s_add_u32 s67, s75, 46208\n"
"buffer_load_dwordx4 v11, s[52:55], s67 offen offset:3072  lds\n"
"s_add_u32 m0, s64, 12288\n"
"s_add_u32 s67, s75, 65664\n"
"buffer_load_dwordx4 v11, s[52:55], s67 offen offset:0  lds\n"
"s_add_u32 s67, s75, 81024\n"
"buffer_load_dwordx4 v11, s[52:55], s67 offen offset:1024  lds\n"
"s_add_u32 s67, s75, 96384\n"
"buffer_load_dwordx4 v11, s[52:55], s67 offen offset:2048  lds\n"
"s_add_u32 s67, s75, 111744\n"
"buffer_load_dwordx4 v11, s[52:55], s67 offen offset:3072  lds\n"
"s_mov_b32 s67, 139264\n"
"buffer_load_dwordx2 v[252:253], off, s[56:59], s67 sc1\n"
"s_waitcnt vmcnt(22)\n"
"s_barrier\n"
"ds_read_b128 a[100:103], v13 offset:16384\n"
"ds_read_b128 a[96:99], v12 offset:16384\n"
"ds_read_b128 a[108:111], v13 offset:18432\n"
"ds_read_b128 a[104:107], v12 offset:18432\n"
"ds_read_b128 a[116:119], v13 offset:20480\n"
"ds_read_b128 a[112:115], v12 offset:20480\n"
"ds_read_b128 a[124:127], v13 offset:22528\n"
"ds_read_b128 a[120:123], v12 offset:22528\n"
"ds_read_b128 a[132:135], v13 offset:24576\n"
"ds_read_b128 a[128:131], v12 offset:24576\n"
"ds_read_b128 a[140:143], v13 offset:26624\n"
"ds_read_b128 a[136:139], v12 offset:26624\n"
"ds_read_b128 a[148:151], v13 offset:28672\n"
"ds_read_b128 a[144:147], v12 offset:28672\n"
"ds_read_b128 a[156:159], v13 offset:30720\n"
"ds_read_b128 a[152:155], v12 offset:30720\n"
"s_add_u32 m0, s72, 57344\n"
"s_add_u32 s67, s60, 128\n"
"buffer_load_dwordx4 v6, s[48:51], s67 offen offset:0  lds\n"
"s_add_u32 s67, s60, 15488\n"
"buffer_load_dwordx4 v6, s[48:51], s67 offen offset:1024  lds\n"
"s_add_u32 s67, s60, 30848\n"
"buffer_load_dwordx4 v6, s[48:51], s67 offen offset:2048  lds\n"
"s_add_u32 s67, s60, 46208\n"
"buffer_load_dwordx4 v6, s[48:51], s67 offen offset:3072  lds\n"
"s_waitcnt vmcnt(18)\n"
"ds_read_b64_tr_b16 a[32:33], v9 offset:0\n"
"ds_read_b64_tr_b16 a[34:35], v9 offset:512\n"
"ds_read_b64_tr_b16 a[36:37], v9 offset:4096\n"
"ds_read_b64_tr_b16 a[38:39], v9 offset:4608\n"
"ds_read_b64_tr_b16 a[40:41], v9 offset:8\n"
"ds_read_b64_tr_b16 a[42:43], v9 offset:520\n"
"ds_read_b64_tr_b16 a[44:45], v9 offset:4104\n"
"ds_read_b64_tr_b16 a[46:47], v9 offset:4616\n"
"ds_read_b64_tr_b16 a[48:49], v15 offset:0\n"
"ds_read_b64_tr_b16 a[50:51], v15 offset:512\n"
"ds_read_b64_tr_b16 a[52:53], v15 offset:4096\n"
"ds_read_b64_tr_b16 a[54:55], v15 offset:4608\n"
"ds_read_b64_tr_b16 a[56:57], v15 offset:8\n"
"ds_read_b64_tr_b16 a[58:59], v15 offset:520\n"
"ds_read_b64_tr_b16 a[60:61], v15 offset:4104\n"
"ds_read_b64_tr_b16 a[62:63], v15 offset:4616\n"
"s_waitcnt vmcnt(14)\n"
"v_lshlrev_b32 v16, 16, v18\n"
"v_and_b32 v17, s63, v18\n"
"v_lshlrev_b32 v18, 16, v19\n"
"v_and_b32 v19, s63, v19\n"
"v_add_f32 v16, 1.0, v16\n"
"v_add_f32 v17, 1.0, v17\n"
"v_add_f32 v18, 1.0, v18\n"
"v_add_f32 v19, 1.0, v19\n"
"v_lshlrev_b32 v20, 16, v22\n"
"v_and_b32 v21, s63, v22\n"
"v_lshlrev_b32 v22, 16, v23\n"
"v_and_b32 v23, s63, v23\n"
"v_add_f32 v20, 1.0, v20\n"
"v_add_f32 v21, 1.0, v21\n"
"v_add_f32 v22, 1.0, v22\n"
"v_add_f32 v23, 1.0, v23\n"
"v_lshlrev_b32 v24, 16, v26\n"
"v_and_b32 v25, s63, v26\n"
"v_lshlrev_b32 v26, 16, v27\n"
"v_and_b32 v27, s63, v27\n"
"v_add_f32 v24, 1.0, v24\n"
"v_add_f32 v25, 1.0, v25\n"
"v_add_f32 v26, 1.0, v26\n"
"v_add_f32 v27, 1.0, v27\n"
"v_lshlrev_b32 v28, 16, v30\n"
"v_and_b32 v29, s63, v30\n"
"v_lshlrev_b32 v30, 16, v31\n"
"v_and_b32 v31, s63, v31\n"
"v_add_f32 v28, 1.0, v28\n"
"v_add_f32 v29, 1.0, v29\n"
"v_add_f32 v30, 1.0, v30\n"
"v_add_f32 v31, 1.0, v31\n"
"v_accvgpr_write_b32 a0, v16\n"
"v_accvgpr_write_b32 a1, v17\n"
"v_accvgpr_write_b32 a2, v18\n"
"v_accvgpr_write_b32 a3, v19\n"
"v_accvgpr_write_b32 a4, v20\n"
"v_accvgpr_write_b32 a5, v21\n"
"v_accvgpr_write_b32 a6, v22\n"
"v_accvgpr_write_b32 a7, v23\n"
"v_accvgpr_write_b32 a8, v24\n"
"v_accvgpr_write_b32 a9, v25\n"
"v_accvgpr_write_b32 a10, v26\n"
"v_accvgpr_write_b32 a11, v27\n"
"v_accvgpr_write_b32 a12, v28\n"
"v_accvgpr_write_b32 a13, v29\n"
"v_accvgpr_write_b32 a14, v30\n"
"v_accvgpr_write_b32 a15, v31\n"
"s_bitcmp1_b32 %26, 0\n"
"s_cbranch_scc1 L_z\n"
"s_cmp_lt_u32 %22, 524288\n"
"s_cselect_b32 s38, 8192000, 0\n"
"s_add_u32 s70, %22, 6553600\n"
"s_sub_u32 s67, s72, s64\n"
"s_add_u32 s67, s67, 98304\n"
"v_add_u32 v40, s67, v9\n"
"v_add_u32 v41, s67, v15\n"
"s_sleep 1\n"
"ds_read_b64_tr_b16 a[160:161], v40 offset:40960\n"
"ds_read_b64_tr_b16 a[162:163], v40 offset:41472\n"
"ds_read_b64_tr_b16 a[164:165], v40 offset:40968\n"
"ds_read_b64_tr_b16 a[166:167], v40 offset:41480\n"
"ds_read_b64_tr_b16 a[168:169], v41 offset:40960\n"
"ds_read_b64_tr_b16 a[170:171], v41 offset:41472\n"
"ds_read_b64_tr_b16 a[172:173], v41 offset:40968\n"
"ds_read_b64_tr_b16 a[174:175], v41 offset:41480\n"
"ds_read_b64_tr_b16 a[176:177], v40 offset:0\n"
"ds_read_b64_tr_b16 a[178:179], v40 offset:512\n"
"ds_read_b64_tr_b16 a[180:181], v40 offset:8\n"
"ds_read_b64_tr_b16 a[182:183], v40 offset:520\n"
"ds_read_b64_tr_b16 a[184:185], v41 offset:0\n"
"ds_read_b64_tr_b16 a[186:187], v41 offset:512\n"
"ds_read_b64_tr_b16 a[188:189], v41 offset:8\n"
"ds_read_b64_tr_b16 a[190:191], v41 offset:520\n"
"s_waitcnt lgkmcnt(14)\n"
"buffer_store_dwordx4 a[160:163], v0, s[36:39], %22 offen offset:0 sc1\n"
"s_waitcnt lgkmcnt(12)\n"
"buffer_store_dwordx4 a[164:167], v0, s[36:39], %22 offen offset:2048 sc1\n"
"s_waitcnt lgkmcnt(10)\n"
"buffer_store_dwordx4 a[168:171], v1, s[36:39], %22 offen offset:0 sc1\n"
"s_waitcnt lgkmcnt(8)\n"
"buffer_store_dwordx4 a[172:175], v1, s[36:39], %22 offen offset:2048 sc1\n"
"s_waitcnt lgkmcnt(6)\n"
"buffer_store_dwordx4 a[176:179], v0, s[36:39], s70 offen offset:0 sc1\n"
"s_waitcnt lgkmcnt(4)\n"
"buffer_store_dwordx4 a[180:183], v0, s[36:39], s70 offen offset:2048 sc1\n"
"s_waitcnt lgkmcnt(2)\n"
"buffer_store_dwordx4 a[184:187], v1, s[36:39], s70 offen offset:0 sc1\n"
"s_waitcnt lgkmcnt(0)\n"
"buffer_store_dwordx4 a[188:191], v1, s[36:39], s70 offen offset:2048 sc1\n"
"s_sleep 10\n"
"L_z:\n"
"s_nop 1\n"
"s_waitcnt lgkmcnt(8)\n"
"v_mfma_f32_16x16x32_bf16 v[32:35], a[32:35], a[96:99], v[16:19]\n"
"s_add_u32 s70, s75, 393216\n"
"v_mfma_f32_16x16x32_bf16 v[32:35], a[36:39], a[100:103], v[32:35]\n"
"s_add_u32 s70, s70, s65\n"
"v_mfma_f32_16x16x32_bf16 v[60:63], a[44:47], a[100:103], v[20:23]\n"
"buffer_load_dword a252, v14, s[52:55], s70 offen\n"
"v_mfma_f32_16x16x32_bf16 v[60:63], a[40:43], a[96:99], v[60:63]\n"
"s_add_u32 m0, s64, 32768\n"
"v_mfma_f32_16x16x32_bf16 v[64:67], a[40:43], a[104:107], v[20:23]\n"
"s_add_u32 s67, s75, 131072\n"
"v_mfma_f32_16x16x32_bf16 v[64:67], a[44:47], a[108:111], v[64:67]\n"
"buffer_load_dwordx4 v11, s[52:55], s67 offen offset:0  lds\n"
"v_mfma_f32_16x16x32_bf16 v[36:39], a[36:39], a[108:111], v[16:19]\n"
"s_add_u32 s67, s75, 146432\n"
"v_mfma_f32_16x16x32_bf16 v[36:39], a[32:35], a[104:107], v[36:39]\n"
"v_mfma_f32_16x16x32_bf16 v[40:43], a[32:35], a[112:115], v[16:19]\n"
"s_waitcnt lgkmcnt(0)\n"
"buffer_load_dwordx4 v11, s[52:55], s67 offen offset:1024  lds\n"
"v_mfma_f32_16x16x32_bf16 v[40:43], a[36:39], a[116:119], v[40:43]\n"
"v_mfma_f32_16x16x32_bf16 v[68:71], a[44:47], a[116:119], v[20:23]\n"
"s_add_u32 s67, s75, 161792\n"
"v_mfma_f32_16x16x32_bf16 v[68:71], a[40:43], a[112:115], v[68:71]\n"
"v_mfma_f32_16x16x32_bf16 v[72:75], a[40:43], a[120:123], v[20:23]\n"
"buffer_load_dwordx4 v11, s[52:55], s67 offen offset:2048  lds\n"
"v_mfma_f32_16x16x32_bf16 v[72:75], a[44:47], a[124:127], v[72:75]\n"
"v_mfma_f32_16x16x32_bf16 v[44:47], a[36:39], a[124:127], v[16:19]\n"
"s_add_u32 s67, s75, 177152\n"
"v_mfma_f32_16x16x32_bf16 v[44:47], a[32:35], a[120:123], v[44:47]\n"
"v_mfma_f32_16x16x32_bf16 v[48:51], a[32:35], a[128:131], v[16:19]\n"
"buffer_load_dwordx4 v11, s[52:55], s67 offen offset:3072  lds\n"
"v_mfma_f32_16x16x32_bf16 v[48:51], a[36:39], a[132:135], v[48:51]\n"
"v_mfma_f32_16x16x32_bf16 v[76:79], a[44:47], a[132:135], v[20:23]\n"
"s_add_u32 m0, s64, 36864\n"
"v_mfma_f32_16x16x32_bf16 v[76:79], a[40:43], a[128:131], v[76:79]\n"
"v_mfma_f32_16x16x32_bf16 v[80:83], a[40:43], a[136:139], v[20:23]\n"
"s_add_u32 s67, s75, 196608\n"
"v_mfma_f32_16x16x32_bf16 v[80:83], a[44:47], a[140:143], v[80:83]\n"
"v_mfma_f32_16x16x32_bf16 v[52:55], a[36:39], a[140:143], v[16:19]\n"
"buffer_load_dwordx4 v11, s[52:55], s67 offen offset:0  lds\n"
"v_mfma_f32_16x16x32_bf16 v[52:55], a[32:35], a[136:139], v[52:55]\n"
"v_mfma_f32_16x16x32_bf16 v[56:59], a[32:35], a[144:147], v[16:19]\n"
"s_mov_b64 exec, 0\n"
"s_cmp_eq_u32 %16, 0\n"
"s_cbranch_scc1 L_st5092\n"
"s_cmp_eq_u32 %16, 1\n"
"s_cbranch_scc1 L_st5702\n"
"s_cmp_eq_u32 %16, 2\n"
"s_cbranch_scc1 L_st6244\n"
"s_branch L_st6798\n"
"L_retB:\n"
"s_mov_b64 exec, -1\n"
"s_nop 3\n"
"s_add_u32 s67, s75, 211968\n"
"v_mfma_f32_16x16x32_bf16 v[56:59], a[36:39], a[148:151], v[56:59]\n"
"v_mfma_f32_16x16x32_bf16 v[84:87], a[44:47], a[148:151], v[20:23]\n"
"buffer_load_dwordx4 v11, s[52:55], s67 offen offset:1024  lds\n"
"v_mfma_f32_16x16x32_bf16 v[84:87], a[40:43], a[144:147], v[84:87]\n"
"v_mfma_f32_16x16x32_bf16 a[4:7], a[40:43], a[152:155], a[4:7]\n"
"s_add_u32 s67, s75, 227328\n"
"v_mfma_f32_16x16x32_bf16 a[4:7], a[44:47], a[156:159], a[4:7]\n"
"v_mfma_f32_16x16x32_bf16 a[0:3], a[36:39], a[156:159], a[0:3]\n"
"buffer_load_dwordx4 v11, s[52:55], s67 offen offset:2048  lds\n"
"v_mfma_f32_16x16x32_bf16 a[0:3], a[32:35], a[152:155], a[0:3]\n"
"v_mfma_f32_16x16x32_bf16 v[88:91], a[48:51], a[96:99], v[24:27]\n"
"s_add_u32 s67, s75, 242688\n"
"v_mfma_f32_16x16x32_bf16 v[88:91], a[52:55], a[100:103], v[88:91]\n"
"v_mfma_f32_16x16x32_bf16 v[116:119], a[60:63], a[100:103], v[28:31]\n"
"v_mfma_f32_16x16x32_bf16 v[116:119], a[56:59], a[96:99], v[116:119]\n"
"buffer_load_dwordx4 v11, s[52:55], s67 offen offset:3072  lds\n"
"v_mfma_f32_16x16x32_bf16 v[120:123], a[56:59], a[104:107], v[28:31]\n"
"s_add_u32 m0, s64, 0\n"
"v_mfma_f32_16x16x32_bf16 v[120:123], a[60:63], a[108:111], v[120:123]\n"
"v_mfma_f32_16x16x32_bf16 v[92:95], a[52:55], a[108:111], v[24:27]\n"
"s_add_u32 s67, s75, 131200\n"
"v_mfma_f32_16x16x32_bf16 v[92:95], a[48:51], a[104:107], v[92:95]\n"
"v_mfma_f32_16x16x32_bf16 v[96:99], a[48:51], a[112:115], v[24:27]\n"
"buffer_load_dwordx4 v11, s[52:55], s67 offen offset:0  lds\n"
"v_mfma_f32_16x16x32_bf16 v[96:99], a[52:55], a[116:119], v[96:99]\n"
"v_mfma_f32_16x16x32_bf16 v[124:127], a[60:63], a[116:119], v[28:31]\n"
"s_add_u32 s67, s75, 146560\n"
"v_mfma_f32_16x16x32_bf16 v[124:127], a[56:59], a[112:115], v[124:127]\n"
"buffer_load_dwordx4 v11, s[52:55], s67 offen offset:1024  lds\n"
"v_mfma_f32_16x16x32_bf16 v[128:131], a[56:59], a[120:123], v[28:31]\n"
"s_waitcnt vmcnt(16)\n"
"ds_read_b64_tr_b16 a[64:65], v9 offset:8192\n"
"v_mfma_f32_16x16x32_bf16 v[128:131], a[60:63], a[124:127], v[128:131]\n"
"s_add_u32 s67, s75, 161920\n"
"ds_read_b64_tr_b16 a[66:67], v9 offset:8704\n"
"v_mfma_f32_16x16x32_bf16 v[100:103], a[52:55], a[124:127], v[24:27]\n"
"ds_read_b64_tr_b16 a[68:69], v9 offset:12288\n"
"v_mfma_f32_16x16x32_bf16 v[100:103], a[48:51], a[120:123], v[100:103]\n"
"ds_read_b64_tr_b16 a[70:71], v9 offset:12800\n"
"v_mfma_f32_16x16x32_bf16 v[104:107], a[48:51], a[128:131], v[24:27]\n"
"ds_read_b64_tr_b16 a[72:73], v9 offset:8200\n"
"v_mfma_f32_16x16x32_bf16 v[104:107], a[52:55], a[132:135], v[104:107]\n"
"ds_read_b64_tr_b16 a[74:75], v9 offset:8712\n"
"buffer_load_dwordx4 v11, s[52:55], s67 offen offset:2048  lds\n"
"v_mfma_f32_16x16x32_bf16 v[132:135], a[60:63], a[132:135], v[28:31]\n"
"ds_read_b64_tr_b16 a[76:77], v9 offset:12296\n"
"v_mfma_f32_16x16x32_bf16 v[132:135], a[56:59], a[128:131], v[132:135]\n"
"s_add_u32 s67, s75, 177280\n"
"ds_read_b64_tr_b16 a[78:79], v9 offset:12808\n"
"v_mfma_f32_16x16x32_bf16 v[136:139], a[56:59], a[136:139], v[28:31]\n"
"buffer_load_dwordx4 v11, s[52:55], s67 offen offset:3072  lds\n"
"v_mfma_f32_16x16x32_bf16 v[136:139], a[60:63], a[140:143], v[136:139]\n"
"ds_read_b64_tr_b16 a[80:81], v15 offset:8192\n"
"v_mfma_f32_16x16x32_bf16 v[108:111], a[52:55], a[140:143], v[24:27]\n"
"s_add_u32 m0, s64, 4096\n"
"ds_read_b64_tr_b16 a[82:83], v15 offset:8704\n"
"v_mfma_f32_16x16x32_bf16 v[108:111], a[48:51], a[136:139], v[108:111]\n"
"ds_read_b64_tr_b16 a[84:85], v15 offset:12288\n"
"v_mfma_f32_16x16x32_bf16 v[112:115], a[48:51], a[144:147], v[24:27]\n"
"s_add_u32 s67, s75, 196736\n"
"ds_read_b64_tr_b16 a[86:87], v15 offset:12800\n"
"v_mfma_f32_16x16x32_bf16 v[112:115], a[52:55], a[148:151], v[112:115]\n"
"ds_read_b64_tr_b16 a[88:89], v15 offset:8200\n"
"v_mfma_f32_16x16x32_bf16 v[140:143], a[60:63], a[148:151], v[28:31]\n"
"ds_read_b64_tr_b16 a[90:91], v15 offset:8712\n"
"buffer_load_dwordx4 v11, s[52:55], s67 offen offset:0  lds\n"
"v_mfma_f32_16x16x32_bf16 v[140:143], a[56:59], a[144:147], v[140:143]\n"
"ds_read_b64_tr_b16 a[92:93], v15 offset:12296\n"
"v_mfma_f32_16x16x32_bf16 a[12:15], a[56:59], a[152:155], a[12:15]\n"
"s_add_u32 s67, s75, 212096\n"
"ds_read_b64_tr_b16 a[94:95], v15 offset:12808\n"
"v_mfma_f32_16x16x32_bf16 a[12:15], a[60:63], a[156:159], a[12:15]\n"
"v_mfma_f32_16x16x32_bf16 a[8:11], a[52:55], a[156:159], a[8:11]\n"
"buffer_load_dwordx4 v11, s[52:55], s67 offen offset:1024  lds\n"
"v_mfma_f32_16x16x32_bf16 a[8:11], a[48:51], a[152:155], a[8:11]\n"
"s_waitcnt lgkmcnt(8)\n"
"v_mfma_f32_16x16x32_bf16 v[144:147], a[64:67], a[96:99], 1.0\n"
"s_add_u32 s67, s75, 227456\n"
"v_mfma_f32_16x16x32_bf16 v[144:147], a[68:71], a[100:103], v[144:147]\n"
"v_mfma_f32_16x16x32_bf16 v[172:175], a[76:79], a[100:103], 1.0\n"
"s_waitcnt vmcnt(19) lgkmcnt(0)\n"
"v_readfirstlane_b32 s78, v252\n"
"v_readfirstlane_b32 s79, v253\n"
"v_mfma_f32_16x16x32_bf16 v[172:175], a[72:75], a[96:99], v[172:175]\n"
"buffer_load_dwordx4 v11, s[52:55], s67 offen offset:2048  lds\n"
"v_mfma_f32_16x16x32_bf16 v[176:179], a[72:75], a[104:107], 1.0\n"
"v_mfma_f32_16x16x32_bf16 v[176:179], a[76:79], a[108:111], v[176:179]\n"
"s_add_u32 s67, s75, 242816\n"
"v_mfma_f32_16x16x32_bf16 v[148:151], a[68:71], a[108:111], 1.0\n"
"s_add_u32 s78, s78, 16\n"
"v_mfma_f32_16x16x32_bf16 v[148:151], a[64:67], a[104:107], v[148:151]\n"
"s_add_u32 s79, s79, 16\n"
"v_mfma_f32_16x16x32_bf16 v[152:155], a[64:67], a[112:115], 1.0\n"
"s_sub_u32 s66, 40960, s73\n"
"v_mfma_f32_16x16x32_bf16 v[152:155], a[68:71], a[116:119], v[152:155]\n"
"buffer_load_dwordx4 v11, s[52:55], s67 offen offset:3072  lds\n"
"v_mfma_f32_16x16x32_bf16 v[180:183], a[76:79], a[116:119], 1.0\n"
"s_add_u32 m0, s72, 98304\n"
"v_add_u32 v12, s66, v7\n"
"v_mfma_f32_16x16x32_bf16 v[180:183], a[72:75], a[112:115], v[180:183]\n"
"v_mfma_f32_16x16x32_bf16 v[184:187], a[72:75], a[120:123], 1.0\n"
"s_add_u32 s67, s60, 256\n"
"v_add_u32 v13, s66, v8\n"
"v_mfma_f32_16x16x32_bf16 v[184:187], a[76:79], a[124:127], v[184:187]\n"
"buffer_load_dwordx4 v6, s[48:51], s67 offen offset:0  lds\n"
"v_mfma_f32_16x16x32_bf16 v[156:159], a[68:71], a[124:127], 1.0\n"
"v_mfma_f32_16x16x32_bf16 v[156:159], a[64:67], a[120:123], v[156:159]\n"
"s_add_u32 s67, s60, 15616\n"
"v_mfma_f32_16x16x32_bf16 v[160:163], a[64:67], a[128:131], 1.0\n"
"L_st534:\n"
"s_cbranch_execz L_st2680\n"
"v_mfma_f32_16x16x32_bf16 v[160:163], a[68:71], a[132:135], v[160:163]\n"
"buffer_load_dwordx4 v6, s[48:51], s67 offen offset:1024  lds\n"
"v_mfma_f32_16x16x32_bf16 v[188:191], a[76:79], a[132:135], 1.0\n"
"v_mfma_f32_16x16x32_bf16 v[188:191], a[72:75], a[128:131], v[188:191]\n"
"s_add_u32 s67, s60, 30976\n"
"v_mfma_f32_16x16x32_bf16 v[192:195], a[72:75], a[136:139], 1.0\n"
"v_mfma_f32_16x16x32_bf16 v[192:195], a[76:79], a[140:143], v[192:195]\n"
"buffer_load_dwordx4 v6, s[48:51], s67 offen offset:2048  lds\n"
"v_mfma_f32_16x16x32_bf16 v[164:167], a[68:71], a[140:143], 1.0\n"
"v_mfma_f32_16x16x32_bf16 v[164:167], a[64:67], a[136:139], v[164:167]\n"
"s_add_u32 s67, s60, 46336\n"
"v_mfma_f32_16x16x32_bf16 v[168:171], a[64:67], a[144:147], 1.0\n"
"v_mfma_f32_16x16x32_bf16 v[168:171], a[68:71], a[148:151], v[168:171]\n"
"v_mfma_f32_16x16x32_bf16 v[196:199], a[76:79], a[148:151], 1.0\n"
"buffer_load_dwordx4 v6, s[48:51], s67 offen offset:3072  lds\n"
"v_mfma_f32_16x16x32_bf16 v[196:199], a[72:75], a[144:147], v[196:199]\n"
"v_mfma_f32_16x16x32_bf16 a[20:23], a[72:75], a[152:155], 1.0\n"
"v_mfma_f32_16x16x32_bf16 a[20:23], a[76:79], a[156:159], a[20:23]\n"
"v_mfma_f32_16x16x32_bf16 a[16:19], a[68:71], a[156:159], 1.0\n"
"v_mfma_f32_16x16x32_bf16 a[16:19], a[64:67], a[152:155], a[16:19]\n"
"s_waitcnt vmcnt(21)\n"
"s_barrier\n"
"v_mfma_f32_16x16x32_bf16 v[200:203], a[80:83], a[96:99], 1.0\n"
"s_waitcnt vmcnt(12)\n"
"ds_read_b64_tr_b16 a[32:33], v9 offset:32768\n"
"v_mfma_f32_16x16x32_bf16 v[200:203], a[84:87], a[100:103], v[200:203]\n"
"s_add_u32 m0, s64, 8192\n"
"ds_read_b64_tr_b16 a[34:35], v9 offset:33280\n"
"v_mfma_f32_16x16x32_bf16 v[228:231], a[92:95], a[100:103], 1.0\n"
"ds_read_b128 a[100:103], v13 offset:16384\n"
"v_mfma_f32_16x16x32_bf16 v[228:231], a[88:91], a[96:99], v[228:231]\n"
"ds_read_b128 a[96:99], v12 offset:16384\n"
"v_mfma_f32_16x16x32_bf16 v[232:235], a[88:91], a[104:107], 1.0\n"
"ds_read_b64_tr_b16 a[36:37], v9 offset:36864\n"
"v_mfma_f32_16x16x32_bf16 v[232:235], a[92:95], a[108:111], v[232:235]\n"
"ds_read_b64_tr_b16 a[38:39], v9 offset:37376\n"
"v_mfma_f32_16x16x32_bf16 v[204:207], a[84:87], a[108:111], 1.0\n"
"ds_read_b128 a[108:111], v13 offset:18432\n"
"v_mfma_f32_16x16x32_bf16 v[204:207], a[80:83], a[104:107], v[204:207]\n"
"ds_read_b128 a[104:107], v12 offset:18432\n"
"ds_read_b64_tr_b16 a[40:41], v9 offset:32776\n"
"v_mfma_f32_16x16x32_bf16 v[208:211], a[80:83], a[112:115], 1.0\n"
"ds_read_b64_tr_b16 a[42:43], v9 offset:33288\n"
"v_mfma_f32_16x16x32_bf16 v[208:211], a[84:87], a[116:119], v[208:211]\n"
"ds_read_b64_tr_b16 a[44:45], v9 offset:36872\n"
"v_mfma_f32_16x16x32_bf16 v[236:239], a[92:95], a[116:119], 1.0\n"
"ds_read_b128 a[116:119], v13 offset:20480\n"
"v_mfma_f32_16x16x32_bf16 v[236:239], a[88:91], a[112:115], v[236:239]\n"
"ds_read_b128 a[112:115], v12 offset:20480\n"
"v_mfma_f32_16x16x32_bf16 v[240:243], a[88:91], a[120:123], 1.0\n"
"ds_read_b64_tr_b16 a[46:47], v9 offset:37384\n"
"v_mfma_f32_16x16x32_bf16 v[240:243], a[92:95], a[124:127], v[240:243]\n"
"ds_read_b64_tr_b16 a[48:49], v15 offset:32768\n"
"v_mfma_f32_16x16x32_bf16 v[212:215], a[84:87], a[124:127], 1.0\n"
"s_add_u32 s67, s75, 262144\n"
"ds_read_b128 a[124:127], v13 offset:22528\n"
"v_mfma_f32_16x16x32_bf16 v[212:215], a[80:83], a[120:123], v[212:215]\n"
"ds_read_b128 a[120:123], v12 offset:22528\n"
"v_mfma_f32_16x16x32_bf16 v[216:219], a[80:83], a[128:131], 1.0\n"
"ds_read_b64_tr_b16 a[50:51], v15 offset:33280\n"
"v_mfma_f32_16x16x32_bf16 v[216:219], a[84:87], a[132:135], v[216:219]\n"
"ds_read_b64_tr_b16 a[52:53], v15 offset:36864\n"
"v_mfma_f32_16x16x32_bf16 v[244:247], a[92:95], a[132:135], 1.0\n"
"ds_read_b128 a[132:135], v13 offset:24576\n"
"v_mfma_f32_16x16x32_bf16 v[244:247], a[88:91], a[128:131], v[244:247]\n"
"ds_read_b128 a[128:131], v12 offset:24576\n"
"v_mfma_f32_16x16x32_bf16 v[248:251], a[88:91], a[136:139], 1.0\n"
"ds_read_b64_tr_b16 a[54:55], v15 offset:37376\n"
"v_mfma_f32_16x16x32_bf16 v[248:251], a[92:95], a[140:143], v[248:251]\n"
"ds_read_b64_tr_b16 a[56:57], v15 offset:32776\n"
"v_mfma_f32_16x16x32_bf16 v[220:223], a[84:87], a[140:143], 1.0\n"
"ds_read_b128 a[140:143], v13 offset:26624\n"
"v_mfma_f32_16x16x32_bf16 v[220:223], a[80:83], a[136:139], v[220:223]\n"
"ds_read_b128 a[136:139], v12 offset:26624\n"
"v_mfma_f32_16x16x32_bf16 v[224:227], a[80:83], a[144:147], 1.0\n"
"ds_read_b64_tr_b16 a[58:59], v15 offset:33288\n"
"v_mfma_f32_16x16x32_bf16 v[224:227], a[84:87], a[148:151], v[224:227]\n"
"ds_read_b64_tr_b16 a[60:61], v15 offset:36872\n"
"v_mfma_f32_16x16x32_bf16 v[252:255], a[92:95], a[148:151], 1.0\n"
"ds_read_b128 a[148:151], v13 offset:28672\n"
"v_mfma_f32_16x16x32_bf16 v[252:255], a[88:91], a[144:147], v[252:255]\n"
"ds_read_b128 a[144:147], v12 offset:28672\n"
"ds_read_b64_tr_b16 a[62:63], v15 offset:37384\n"
"v_mfma_f32_16x16x32_bf16 a[28:31], a[88:91], a[152:155], 1.0\n"
"v_mfma_f32_16x16x32_bf16 a[28:31], a[92:95], a[156:159], a[28:31]\n"
"v_mfma_f32_16x16x32_bf16 a[24:27], a[84:87], a[156:159], 1.0\n"
"ds_read_b128 a[156:159], v13 offset:30720\n"
"v_mfma_f32_16x16x32_bf16 a[24:27], a[80:83], a[152:155], a[24:27]\n"
"s_waitcnt lgkmcnt(15)\n"
"ds_read_b128 a[152:155], v12 offset:30720\n"
"v_mfma_f32_16x16x32_bf16 v[32:35], a[32:35], a[96:99], v[32:35]\n"
"buffer_load_dwordx4 v11, s[52:55], s67 offen offset:0  lds\n"
"v_mfma_f32_16x16x32_bf16 v[32:35], a[36:39], a[100:103], v[32:35]\n"
"s_waitcnt lgkmcnt(2)\n"
"v_mfma_f32_16x16x32_bf16 v[60:63], a[44:47], a[100:103], v[60:63]\n"
"s_add_u32 s67, s75, 277504\n"
"v_mfma_f32_16x16x32_bf16 v[60:63], a[40:43], a[96:99], v[60:63]\n"
"v_mfma_f32_16x16x32_bf16 v[64:67], a[40:43], a[104:107], v[64:67]\n"
"v_mfma_f32_16x16x32_bf16 v[64:67], a[44:47], a[108:111], v[64:67]\n"
"buffer_load_dwordx4 v11, s[52:55], s67 offen offset:1024  lds\n"
"v_mfma_f32_16x16x32_bf16 v[36:39], a[36:39], a[108:111], v[36:39]\n"
"v_mfma_f32_16x16x32_bf16 v[36:39], a[32:35], a[104:107], v[36:39]\n"
"s_add_u32 s67, s75, 292864\n"
"v_mfma_f32_16x16x32_bf16 v[40:43], a[32:35], a[112:115], v[40:43]\n"
"v_mfma_f32_16x16x32_bf16 v[40:43], a[36:39], a[116:119], v[40:43]\n"
"v_mfma_f32_16x16x32_bf16 v[68:71], a[44:47], a[116:119], v[68:71]\n"
"buffer_load_dwordx4 v11, s[52:55], s67 offen offset:2048  lds\n"
"v_mfma_f32_16x16x32_bf16 v[68:71], a[40:43], a[112:115], v[68:71]\n"
"v_mfma_f32_16x16x32_bf16 v[72:75], a[40:43], a[120:123], v[72:75]\n"
"s_add_u32 s67, s75, 308224\n"
"v_mfma_f32_16x16x32_bf16 v[72:75], a[44:47], a[124:127], v[72:75]\n"
"v_mfma_f32_16x16x32_bf16 v[44:47], a[36:39], a[124:127], v[44:47]\n"
"v_mov_b32 v16, -1\n"
"v_mfma_f32_16x16x32_bf16 v[44:47], a[32:35], a[120:123], v[44:47]\n"
"buffer_load_dwordx4 v11, s[52:55], s67 offen offset:3072  lds\n"
"v_mfma_f32_16x16x32_bf16 v[48:51], a[32:35], a[128:131], v[48:51]\n"
"s_add_u32 m0, s64, 12288\n"
"v_mfma_f32_16x16x32_bf16 v[48:51], a[36:39], a[132:135], v[48:51]\n"
"v_mov_b32 v17, -1\n"
"v_mfma_f32_16x16x32_bf16 v[76:79], a[44:47], a[132:135], v[76:79]\n"
"s_waitcnt vmcnt(8)\n"
"ds_read_b64_tr_b16 a[64:65], v9 offset:0\n"
"v_mfma_f32_16x16x32_bf16 v[76:79], a[40:43], a[128:131], v[76:79]\n"
"s_add_u32 s67, s75, 327680\n"
"ds_read_b64_tr_b16 a[66:67], v9 offset:512\n"
"v_mfma_f32_16x16x32_bf16 v[80:83], a[40:43], a[136:139], v[80:83]\n"
"ds_read_b64_tr_b16 a[68:69], v9 offset:4096\n"
"buffer_load_dwordx4 v11, s[52:55], s67 offen offset:0  lds\n"
"v_mfma_f32_16x16x32_bf16 v[80:83], a[44:47], a[140:143], v[80:83]\n"
"ds_read_b64_tr_b16 a[70:71], v9 offset:4608\n"
"v_mfma_f32_16x16x32_bf16 v[52:55], a[36:39], a[140:143], v[52:55]\n"
"s_add_u32 s67, s75, 343040\n"
"ds_read_b64_tr_b16 a[72:73], v9 offset:8\n"
"v_mfma_f32_16x16x32_bf16 v[52:55], a[32:35], a[136:139], v[52:55]\n"
"s_waitcnt lgkmcnt(5)\n"
"ds_read_b64_tr_b16 a[74:75], v9 offset:520\n"
"v_mfma_f32_16x16x32_bf16 v[56:59], a[32:35], a[144:147], v[56:59]\n"
"ds_read_b64_tr_b16 a[76:77], v9 offset:4104\n"
"buffer_load_dwordx4 v11, s[52:55], s67 offen offset:1024  lds\n"
"v_mfma_f32_16x16x32_bf16 v[56:59], a[36:39], a[148:151], v[56:59]\n"
"s_add_u32 s67, s75, 358400\n"
"ds_read_b64_tr_b16 a[78:79], v9 offset:4616\n"
"v_mfma_f32_16x16x32_bf16 v[84:87], a[44:47], a[148:151], v[84:87]\n"
"s_add_u32 s69, %11, 8192\n"
"v_lshrrev_b32 v18, 1, v0\n"
"v_mfma_f32_16x16x32_bf16 v[84:87], a[40:43], a[144:147], v[84:87]\n"
"ds_read_b64_tr_b16 a[80:81], v15 offset:0\n"
"v_mfma_f32_16x16x32_bf16 a[4:7], a[40:43], a[152:155], a[4:7]\n"
"ds_read_b64_tr_b16 a[82:83], v15 offset:512\n"
"buffer_load_dwordx4 v11, s[52:55], s67 offen offset:2048  lds\n"
"v_mfma_f32_16x16x32_bf16 a[4:7], a[44:47], a[156:159], a[4:7]\n"
"ds_read_b64_tr_b16 a[84:85], v15 offset:4096\n"
"v_mfma_f32_16x16x32_bf16 a[0:3], a[36:39], a[156:159], a[0:3]\n"
"s_add_u32 s67, s75, 373760\n"
"ds_read_b64_tr_b16 a[86:87], v15 offset:4608\n"
"v_mfma_f32_16x16x32_bf16 a[0:3], a[32:35], a[152:155], a[0:3]\n"
"ds_read_b64_tr_b16 a[88:89], v15 offset:8\n"
"v_mfma_f32_16x16x32_bf16 v[88:91], a[48:51], a[96:99], v[88:91]\n"
"ds_read_b64_tr_b16 a[90:91], v15 offset:520\n"
"v_mfma_f32_16x16x32_bf16 v[88:91], a[52:55], a[100:103], v[88:91]\n"
"ds_read_b64_tr_b16 a[92:93], v15 offset:4104\n"
"buffer_load_dwordx4 v11, s[52:55], s67 offen offset:3072  lds\n"
"v_mfma_f32_16x16x32_bf16 v[116:119], a[60:63], a[100:103], v[116:119]\n"
"ds_read_b64_tr_b16 a[94:95], v15 offset:4616\n"
"v_mfma_f32_16x16x32_bf16 v[116:119], a[56:59], a[96:99], v[116:119]\n"
"s_add_u32 m0, s64, 0\n"
"v_mfma_f32_16x16x32_bf16 v[120:123], a[56:59], a[104:107], v[120:123]\n"
"v_mfma_f32_16x16x32_bf16 v[120:123], a[60:63], a[108:111], v[120:123]\n"
"v_mfma_f32_16x16x32_bf16 v[92:95], a[52:55], a[108:111], v[92:95]\n"
"s_add_u32 s67, s75, 262272\n"
"v_mfma_f32_16x16x32_bf16 v[92:95], a[48:51], a[104:107], v[92:95]\n"
"v_mfma_f32_16x16x32_bf16 v[96:99], a[48:51], a[112:115], v[96:99]\n"
"s_waitcnt lgkmcnt(0)\n"
"v_mfma_f32_16x16x32_bf16 v[96:99], a[52:55], a[116:119], v[96:99]\n"
"buffer_load_dwordx4 v11, s[52:55], s67 offen offset:0  lds\n"
"v_mfma_f32_16x16x32_bf16 v[124:127], a[60:63], a[116:119], v[124:127]\n"
"v_mfma_f32_16x16x32_bf16 v[124:127], a[56:59], a[112:115], v[124:127]\n"
"s_add_u32 s67, s75, 277632\n"
"v_mfma_f32_16x16x32_bf16 v[128:131], a[56:59], a[120:123], v[128:131]\n"
"v_mfma_f32_16x16x32_bf16 v[128:131], a[60:63], a[124:127], v[128:131]\n"
"v_mfma_f32_16x16x32_bf16 v[100:103], a[52:55], a[124:127], v[100:103]\n"
"buffer_load_dwordx4 v11, s[52:55], s67 offen offset:1024  lds\n"
"v_mfma_f32_16x16x32_bf16 v[100:103], a[48:51], a[120:123], v[100:103]\n"
"v_mfma_f32_16x16x32_bf16 v[104:107], a[48:51], a[128:131], v[104:107]\n"
"s_add_u32 s67, s75, 292992\n"
"v_mfma_f32_16x16x32_bf16 v[104:107], a[52:55], a[132:135], v[104:107]\n"
"v_mfma_f32_16x16x32_bf16 v[132:135], a[60:63], a[132:135], v[132:135]\n"
"buffer_load_dwordx4 v11, s[52:55], s67 offen offset:2048  lds\n"
"v_mfma_f32_16x16x32_bf16 v[132:135], a[56:59], a[128:131], v[132:135]\n"
"v_mfma_f32_16x16x32_bf16 v[136:139], a[56:59], a[136:139], v[136:139]\n"
"s_add_u32 s67, s75, 308352\n"
"v_mfma_f32_16x16x32_bf16 v[136:139], a[60:63], a[140:143], v[136:139]\n"
"v_mfma_f32_16x16x32_bf16 v[108:111], a[52:55], a[140:143], v[108:111]\n"
"v_mfma_f32_16x16x32_bf16 v[108:111], a[48:51], a[136:139], v[108:111]\n"
"buffer_load_dwordx4 v11, s[52:55], s67 offen offset:3072  lds\n"
"v_mfma_f32_16x16x32_bf16 v[112:115], a[48:51], a[144:147], v[112:115]\n"
"v_mfma_f32_16x16x32_bf16 v[112:115], a[52:55], a[148:151], v[112:115]\n"
"s_add_u32 m0, s64, 4096\n"
"v_mfma_f32_16x16x32_bf16 v[140:143], a[60:63], a[148:151], v[140:143]\n"
"v_mfma_f32_16x16x32_bf16 v[140:143], a[56:59], a[144:147], v[140:143]\n"
"s_add_u32 s67, s75, 327808\n"
"v_mfma_f32_16x16x32_bf16 a[12:15], a[56:59], a[152:155], a[12:15]\n"
"v_mfma_f32_16x16x32_bf16 a[12:15], a[60:63], a[156:159], a[12:15]\n"
"buffer_load_dwordx4 v11, s[52:55], s67 offen offset:0  lds\n"
"v_mfma_f32_16x16x32_bf16 a[8:11], a[52:55], a[156:159], a[8:11]\n"
"v_mfma_f32_16x16x32_bf16 a[8:11], a[48:51], a[152:155], a[8:11]\n"
"s_add_u32 s67, s75, 343168\n"
"v_mfma_f32_16x16x32_bf16 v[144:147], a[64:67], a[96:99], v[144:147]\n"
"v_mfma_f32_16x16x32_bf16 v[144:147], a[68:71], a[100:103], v[144:147]\n"
"buffer_load_dwordx4 v11, s[52:55], s67 offen offset:1024  lds\n"
"v_mfma_f32_16x16x32_bf16 v[172:175], a[76:79], a[100:103], v[172:175]\n"
"v_mfma_f32_16x16x32_bf16 v[172:175], a[72:75], a[96:99], v[172:175]\n"
"s_add_u32 s67, s75, 358528\n"
"v_mfma_f32_16x16x32_bf16 v[176:179], a[72:75], a[104:107], v[176:179]\n"
"v_mfma_f32_16x16x32_bf16 v[176:179], a[76:79], a[108:111], v[176:179]\n"
"buffer_load_dwordx4 v11, s[52:55], s67 offen offset:2048  lds\n"
"v_mfma_f32_16x16x32_bf16 v[148:151], a[68:71], a[108:111], v[148:151]\n"
"v_mfma_f32_16x16x32_bf16 v[148:151], a[64:67], a[104:107], v[148:151]\n"
"s_add_u32 s67, s75, 373888\n"
"v_mfma_f32_16x16x32_bf16 v[152:155], a[64:67], a[112:115], v[152:155]\n"
"s_sub_u32 s66, 81920, s73\n"
"v_mfma_f32_16x16x32_bf16 v[152:155], a[68:71], a[116:119], v[152:155]\n"
"buffer_load_dwordx4 v11, s[52:55], s67 offen offset:3072  lds\n"
"v_mfma_f32_16x16x32_bf16 v[180:183], a[76:79], a[116:119], v[180:183]\n"
"v_add_u32 v12, s66, v7\n"
"v_mfma_f32_16x16x32_bf16 v[180:183], a[72:75], a[112:115], v[180:183]\n"
"s_add_u32 m0, s72, 139264\n"
"v_mfma_f32_16x16x32_bf16 v[184:187], a[72:75], a[120:123], v[184:187]\n"
"v_add_u32 v13, s66, v8\n"
"v_mfma_f32_16x16x32_bf16 v[184:187], a[76:79], a[124:127], v[184:187]\n"
"s_add_u32 s67, s60, 384\n"
"v_mfma_f32_16x16x32_bf16 v[156:159], a[68:71], a[124:127], v[156:159]\n"
"v_mfma_f32_16x16x32_bf16 v[156:159], a[64:67], a[120:123], v[156:159]\n"
"buffer_load_dwordx4 v6, s[48:51], s67 offen offset:0  lds\n"
"v_mfma_f32_16x16x32_bf16 v[160:163], a[64:67], a[128:131], v[160:163]\n"
"v_mfma_f32_16x16x32_bf16 v[160:163], a[68:71], a[132:135], v[160:163]\n"
"s_add_u32 s67, s60, 15744\n"
"v_mfma_f32_16x16x32_bf16 v[188:191], a[76:79], a[132:135], v[188:191]\n"
"v_mfma_f32_16x16x32_bf16 v[188:191], a[72:75], a[128:131], v[188:191]\n"
"buffer_load_dwordx4 v6, s[48:51], s67 offen offset:1024  lds\n"
"v_mfma_f32_16x16x32_bf16 v[192:195], a[72:75], a[136:139], v[192:195]\n"
"v_mfma_f32_16x16x32_bf16 v[192:195], a[76:79], a[140:143], v[192:195]\n"
"s_add_u32 s67, s60, 31104\n"
"v_mfma_f32_16x16x32_bf16 v[164:167], a[68:71], a[140:143], v[164:167]\n"
"v_mfma_f32_16x16x32_bf16 v[164:167], a[64:67], a[136:139], v[164:167]\n"
"buffer_load_dwordx4 v6, s[48:51], s67 offen offset:2048  lds\n"
"v_mfma_f32_16x16x32_bf16 v[168:171], a[64:67], a[144:147], v[168:171]\n"
"v_mfma_f32_16x16x32_bf16 v[168:171], a[68:71], a[148:151], v[168:171]\n"
"s_add_u32 s67, s60, 46464\n"
"v_mfma_f32_16x16x32_bf16 v[196:199], a[76:79], a[148:151], v[196:199]\n"
"v_mfma_f32_16x16x32_bf16 v[196:199], a[72:75], a[144:147], v[196:199]\n"
"v_mfma_f32_16x16x32_bf16 a[20:23], a[72:75], a[152:155], a[20:23]\n"
"buffer_load_dwordx4 v6, s[48:51], s67 offen offset:3072  lds\n"
"v_mfma_f32_16x16x32_bf16 a[20:23], a[76:79], a[156:159], a[20:23]\n"
"s_add_u32 s70, s75, 655360\n"
"v_mfma_f32_16x16x32_bf16 a[16:19], a[68:71], a[156:159], a[16:19]\n"
"s_add_u32 s70, s70, s65\n"
"v_mfma_f32_16x16x32_bf16 a[16:19], a[64:67], a[152:155], a[16:19]\n"
"s_waitcnt vmcnt(20)\n"
"s_barrier\n"
"v_mfma_f32_16x16x32_bf16 v[200:203], a[80:83], a[96:99], v[200:203]\n"
"s_waitcnt vmcnt(12)\n"
"ds_read_b64_tr_b16 a[32:33], v9 offset:8192\n"
"v_mfma_f32_16x16x32_bf16 v[200:203], a[84:87], a[100:103], v[200:203]\n"
"ds_read_b64_tr_b16 a[34:35], v9 offset:8704\n"
"buffer_load_dword a252, v14, s[52:55], s70 offen\n"
"v_mfma_f32_16x16x32_bf16 v[228:231], a[92:95], a[100:103], v[228:231]\n"
"ds_read_b128 a[100:103], v13 offset:16384\n"
"v_mfma_f32_16x16x32_bf16 v[228:231], a[88:91], a[96:99], v[228:231]\n"
"ds_read_b128 a[96:99], v12 offset:16384\n"
"v_mfma_f32_16x16x32_bf16 v[232:235], a[88:91], a[104:107], v[232:235]\n"
"ds_read_b64_tr_b16 a[36:37], v9 offset:12288\n"
"v_mfma_f32_16x16x32_bf16 v[232:235], a[92:95], a[108:111], v[232:235]\n"
"ds_read_b64_tr_b16 a[38:39], v9 offset:12800\n"
"v_mfma_f32_16x16x32_bf16 v[204:207], a[84:87], a[108:111], v[204:207]\n"
"ds_read_b128 a[108:111], v13 offset:18432\n"
"v_mfma_f32_16x16x32_bf16 v[204:207], a[80:83], a[104:107], v[204:207]\n"
"ds_read_b128 a[104:107], v12 offset:18432\n"
"s_add_u32 m0, s64, 8192\n"
"ds_read_b64_tr_b16 a[40:41], v9 offset:8200\n"
"v_mfma_f32_16x16x32_bf16 v[208:211], a[80:83], a[112:115], v[208:211]\n"
"ds_read_b64_tr_b16 a[42:43], v9 offset:8712\n"
"v_mfma_f32_16x16x32_bf16 v[208:211], a[84:87], a[116:119], v[208:211]\n"
"ds_read_b64_tr_b16 a[44:45], v9 offset:12296\n"
"v_mfma_f32_16x16x32_bf16 v[236:239], a[92:95], a[116:119], v[236:239]\n"
"ds_read_b128 a[116:119], v13 offset:20480\n"
"v_mfma_f32_16x16x32_bf16 v[236:239], a[88:91], a[112:115], v[236:239]\n"
"ds_read_b128 a[112:115], v12 offset:20480\n"
"v_mfma_f32_16x16x32_bf16 v[240:243], a[88:91], a[120:123], v[240:243]\n"
"ds_read_b64_tr_b16 a[46:47], v9 offset:12808\n"
"v_mfma_f32_16x16x32_bf16 v[240:243], a[92:95], a[124:127], v[240:243]\n"
"ds_read_b64_tr_b16 a[48:49], v15 offset:8192\n"
"v_mfma_f32_16x16x32_bf16 v[212:215], a[84:87], a[124:127], v[212:215]\n"
"ds_read_b128 a[124:127], v13 offset:22528\n"
"v_mfma_f32_16x16x32_bf16 v[212:215], a[80:83], a[120:123], v[212:215]\n"
"ds_read_b128 a[120:123], v12 offset:22528\n"
"v_mfma_f32_16x16x32_bf16 v[216:219], a[80:83], a[128:131], v[216:219]\n"
"ds_read_b64_tr_b16 a[50:51], v15 offset:8704\n"
"v_mfma_f32_16x16x32_bf16 v[216:219], a[84:87], a[132:135], v[216:219]\n"
"ds_read_b64_tr_b16 a[52:53], v15 offset:12288\n"
"v_mfma_f32_16x16x32_bf16 v[244:247], a[92:95], a[132:135], v[244:247]\n"
"ds_read_b128 a[132:135], v13 offset:24576\n"
"v_mfma_f32_16x16x32_bf16 v[244:247], a[88:91], a[128:131], v[244:247]\n"
"s_add_u32 s67, s75, 393216\n"
"ds_read_b128 a[128:131], v12 offset:24576\n"
"v_mfma_f32_16x16x32_bf16 v[248:251], a[88:91], a[136:139], v[248:251]\n"
"ds_read_b64_tr_b16 a[54:55], v15 offset:12800\n"
"v_mfma_f32_16x16x32_bf16 v[248:251], a[92:95], a[140:143], v[248:251]\n"
"s_add_u32 s66, s78, 1\n"
"ds_read_b64_tr_b16 a[56:57], v15 offset:8200\n"
"v_mfma_f32_16x16x32_bf16 v[220:223], a[84:87], a[140:143], v[220:223]\n"
"ds_read_b128 a[140:143], v13 offset:26624\n"
"v_mfma_f32_16x16x32_bf16 v[220:223], a[80:83], a[136:139], v[220:223]\n"
"ds_read_b128 a[136:139], v12 offset:26624\n"
"v_mov_b32 v28, s66\n"
"v_mfma_f32_16x16x32_bf16 v[224:227], a[80:83], a[144:147], v[224:227]\n"
"ds_read_b64_tr_b16 a[58:59], v15 offset:8712\n"
"v_mov_b32 v29, s79\n"
"v_mfma_f32_16x16x32_bf16 v[224:227], a[84:87], a[148:151], v[224:227]\n"
"ds_read_b64_tr_b16 a[60:61], v15 offset:12296\n"
"v_mfma_f32_16x16x32_bf16 v[252:255], a[92:95], a[148:151], v[252:255]\n"
"buffer_store_dwordx2 v[28:29], off, s[56:59], %24 sc1\n"
"ds_read_b128 a[148:151], v13 offset:28672\n"
"v_mfma_f32_16x16x32_bf16 v[252:255], a[88:91], a[144:147], v[252:255]\n"
"ds_read_b128 a[144:147], v12 offset:28672\n"
"ds_read_b64_tr_b16 a[62:63], v15 offset:12808\n"
"v_mfma_f32_16x16x32_bf16 a[28:31], a[88:91], a[152:155], a[28:31]\n"
"v_mfma_f32_16x16x32_bf16 a[28:31], a[92:95], a[156:159], a[28:31]\n"
"v_mfma_f32_16x16x32_bf16 a[24:27], a[84:87], a[156:159], a[24:27]\n"
"ds_read_b128 a[156:159], v13 offset:30720\n"
"v_mfma_f32_16x16x32_bf16 a[24:27], a[80:83], a[152:155], a[24:27]\n"
"s_waitcnt lgkmcnt(15)\n"
"ds_read_b128 a[152:155], v12 offset:30720\n"
"v_mfma_f32_16x16x32_bf16 v[32:35], a[32:35], a[96:99], v[32:35]\n"
"v_mfma_f32_16x16x32_bf16 v[32:35], a[36:39], a[100:103], v[32:35]\n"
"s_waitcnt lgkmcnt(2)\n"
"v_mfma_f32_16x16x32_bf16 v[60:63], a[44:47], a[100:103], v[60:63]\n"
"buffer_load_dwordx4 v11, s[52:55], s67 offen offset:0  lds\n"
"v_mfma_f32_16x16x32_bf16 v[60:63], a[40:43], a[96:99], v[60:63]\n"
"v_mfma_f32_16x16x32_bf16 v[64:67], a[40:43], a[104:107], v[64:67]\n"
"s_add_u32 s67, s75, 408576\n"
"v_mfma_f32_16x16x32_bf16 v[64:67], a[44:47], a[108:111], v[64:67]\n"
"v_mfma_f32_16x16x32_bf16 v[36:39], a[36:39], a[108:111], v[36:39]\n"
"v_mfma_f32_16x16x32_bf16 v[36:39], a[32:35], a[104:107], v[36:39]\n"
"buffer_load_dwordx4 v11, s[52:55], s67 offen offset:1024  lds\n"
"v_mfma_f32_16x16x32_bf16 v[40:43], a[32:35], a[112:115], v[40:43]\n"
"v_mfma_f32_16x16x32_bf16 v[40:43], a[36:39], a[116:119], v[40:43]\n"
"s_add_u32 s67, s75, 423936\n"
"v_mfma_f32_16x16x32_bf16 v[68:71], a[44:47], a[116:119], v[68:71]\n"
"v_mfma_f32_16x16x32_bf16 v[68:71], a[40:43], a[112:115], v[68:71]\n"
"v_mfma_f32_16x16x32_bf16 v[72:75], a[40:43], a[120:123], v[72:75]\n"
"buffer_load_dwordx4 v11, s[52:55], s67 offen offset:2048  lds\n"
"v_mfma_f32_16x16x32_bf16 v[72:75], a[44:47], a[124:127], v[72:75]\n"
"v_mfma_f32_16x16x32_bf16 v[44:47], a[36:39], a[124:127], v[44:47]\n"
"s_add_u32 s67, s75, 439296\n"
"v_mfma_f32_16x16x32_bf16 v[44:47], a[32:35], a[120:123], v[44:47]\n"
"v_mfma_f32_16x16x32_bf16 v[48:51], a[32:35], a[128:131], v[48:51]\n"
"buffer_load_dwordx4 v11, s[52:55], s67 offen offset:3072  lds\n"
"v_mfma_f32_16x16x32_bf16 v[48:51], a[36:39], a[132:135], v[48:51]\n"
"s_add_u32 m0, s64, 12288\n"
"v_mfma_f32_16x16x32_bf16 v[76:79], a[44:47], a[132:135], v[76:79]\n"
"s_waitcnt vmcnt(10)\n"
"ds_read_b64_tr_b16 a[64:65], v9 offset:0\n"
"v_mfma_f32_16x16x32_bf16 v[76:79], a[40:43], a[128:131], v[76:79]\n"
"ds_read_b64_tr_b16 a[66:67], v9 offset:512\n"
"v_mfma_f32_16x16x32_bf16 v[80:83], a[40:43], a[136:139], v[80:83]\n"
"s_add_u32 s67, s75, 458752\n"
"ds_read_b64_tr_b16 a[68:69], v9 offset:4096\n"
"v_mfma_f32_16x16x32_bf16 v[80:83], a[44:47], a[140:143], v[80:83]\n"
"ds_read_b64_tr_b16 a[70:71], v9 offset:4608\n"
"v_mfma_f32_16x16x32_bf16 v[52:55], a[36:39], a[140:143], v[52:55]\n"
"ds_read_b64_tr_b16 a[72:73], v9 offset:8\n"
"buffer_load_dwordx4 v11, s[52:55], s67 offen offset:0  lds\n"
"v_mfma_f32_16x16x32_bf16 v[52:55], a[32:35], a[136:139], v[52:55]\n"
"s_waitcnt lgkmcnt(5)\n"
"ds_read_b64_tr_b16 a[74:75], v9 offset:520\n"
"v_mfma_f32_16x16x32_bf16 v[56:59], a[32:35], a[144:147], v[56:59]\n"
"s_add_u32 s67, s75, 474112\n"
"ds_read_b64_tr_b16 a[76:77], v9 offset:4104\n"
"v_mfma_f32_16x16x32_bf16 v[56:59], a[36:39], a[148:151], v[56:59]\n"
"ds_read_b64_tr_b16 a[78:79], v9 offset:4616\n"
"v_mfma_f32_16x16x32_bf16 v[84:87], a[44:47], a[148:151], v[84:87]\n"
"buffer_load_dwordx4 v11, s[52:55], s67 offen offset:1024  lds\n"
"v_mfma_f32_16x16x32_bf16 v[84:87], a[40:43], a[144:147], v[84:87]\n"
"ds_read_b64_tr_b16 a[80:81], v15 offset:0\n"
"v_mfma_f32_16x16x32_bf16 a[4:7], a[40:43], a[152:155], a[4:7]\n"
"s_add_u32 s67, s75, 489472\n"
"ds_read_b64_tr_b16 a[82:83], v15 offset:512\n"
"v_mfma_f32_16x16x32_bf16 a[4:7], a[44:47], a[156:159], a[4:7]\n"
"ds_read_b64_tr_b16 a[84:85], v15 offset:4096\n"
"v_mfma_f32_16x16x32_bf16 a[0:3], a[36:39], a[156:159], a[0:3]\n"
"ds_read_b64_tr_b16 a[86:87], v15 offset:4608\n"
"buffer_load_dwordx4 v11, s[52:55], s67 offen offset:2048  lds\n"
"v_mfma_f32_16x16x32_bf16 a[0:3], a[32:35], a[152:155], a[0:3]\n"
"ds_read_b64_tr_b16 a[88:89], v15 offset:8\n"
"v_mfma_f32_16x16x32_bf16 v[88:91], a[48:51], a[96:99], v[88:91]\n"
"s_add_u32 s67, s75, 504832\n"
"ds_read_b64_tr_b16 a[90:91], v15 offset:520\n"
"v_mfma_f32_16x16x32_bf16 v[88:91], a[52:55], a[100:103], v[88:91]\n"
"ds_read_b64_tr_b16 a[92:93], v15 offset:4104\n"
"v_mfma_f32_16x16x32_bf16 v[116:119], a[60:63], a[100:103], v[116:119]\n"
"ds_read_b64_tr_b16 a[94:95], v15 offset:4616\n"
"v_mfma_f32_16x16x32_bf16 v[116:119], a[56:59], a[96:99], v[116:119]\n"
"buffer_load_dwordx4 v11, s[52:55], s67 offen offset:3072  lds\n"
"v_mfma_f32_16x16x32_bf16 v[120:123], a[56:59], a[104:107], v[120:123]\n"
"v_mfma_f32_16x16x32_bf16 v[120:123], a[60:63], a[108:111], v[120:123]\n"
"s_add_u32 m0, s64, 0\n"
"v_mfma_f32_16x16x32_bf16 v[92:95], a[52:55], a[108:111], v[92:95]\n"
"v_mfma_f32_16x16x32_bf16 v[92:95], a[48:51], a[104:107], v[92:95]\n"
"s_add_u32 s67, s75, 393344\n"
"v_mfma_f32_16x16x32_bf16 v[96:99], a[48:51], a[112:115], v[96:99]\n"
"s_waitcnt lgkmcnt(0)\n"
"v_mfma_f32_16x16x32_bf16 v[96:99], a[52:55], a[116:119], v[96:99]\n"
"buffer_load_dwordx4 v11, s[52:55], s67 offen offset:0  lds\n"
"v_mfma_f32_16x16x32_bf16 v[124:127], a[60:63], a[116:119], v[124:127]\n"
"v_mfma_f32_16x16x32_bf16 v[124:127], a[56:59], a[112:115], v[124:127]\n"
"s_add_u32 s67, s75, 408704\n"
"v_mfma_f32_16x16x32_bf16 v[128:131], a[56:59], a[120:123], v[128:131]\n"
"v_mfma_f32_16x16x32_bf16 v[128:131], a[60:63], a[124:127], v[128:131]\n"
"v_mfma_f32_16x16x32_bf16 v[100:103], a[52:55], a[124:127], v[100:103]\n"
"buffer_load_dwordx4 v11, s[52:55], s67 offen offset:1024  lds\n"
"v_mfma_f32_16x16x32_bf16 v[100:103], a[48:51], a[120:123], v[100:103]\n"
"v_mfma_f32_16x16x32_bf16 v[104:107], a[48:51], a[128:131], v[104:107]\n"
"s_add_u32 s67, s75, 424064\n"
"v_mfma_f32_16x16x32_bf16 v[104:107], a[52:55], a[132:135], v[104:107]\n"
"v_mfma_f32_16x16x32_bf16 v[132:135], a[60:63], a[132:135], v[132:135]\n"
"v_mfma_f32_16x16x32_bf16 v[132:135], a[56:59], a[128:131], v[132:135]\n"
"buffer_load_dwordx4 v11, s[52:55], s67 offen offset:2048  lds\n"
"v_mfma_f32_16x16x32_bf16 v[136:139], a[56:59], a[136:139], v[136:139]\n"
"v_mfma_f32_16x16x32_bf16 v[136:139], a[60:63], a[140:143], v[136:139]\n"
"s_add_u32 s67, s75, 439424\n"
"v_mfma_f32_16x16x32_bf16 v[108:111], a[52:55], a[140:143], v[108:111]\n"
"v_mfma_f32_16x16x32_bf16 v[108:111], a[48:51], a[136:139], v[108:111]\n"
"buffer_load_dwordx4 v11, s[52:55], s67 offen offset:3072  lds\n"
"v_mfma_f32_16x16x32_bf16 v[112:115], a[48:51], a[144:147], v[112:115]\n"
"v_mfma_f32_16x16x32_bf16 v[112:115], a[52:55], a[148:151], v[112:115]\n"
"s_add_u32 m0, s64, 4096\n"
"v_mfma_f32_16x16x32_bf16 v[140:143], a[60:63], a[148:151], v[140:143]\n"
"v_mfma_f32_16x16x32_bf16 v[140:143], a[56:59], a[144:147], v[140:143]\n"
"s_add_u32 s67, s75, 458880\n"
"v_mfma_f32_16x16x32_bf16 a[12:15], a[56:59], a[152:155], a[12:15]\n"
"v_mfma_f32_16x16x32_bf16 a[12:15], a[60:63], a[156:159], a[12:15]\n"
"buffer_load_dwordx4 v11, s[52:55], s67 offen offset:0  lds\n"
"v_mfma_f32_16x16x32_bf16 a[8:11], a[52:55], a[156:159], a[8:11]\n"
"v_mfma_f32_16x16x32_bf16 a[8:11], a[48:51], a[152:155], a[8:11]\n"
"s_add_u32 s67, s75, 474240\n"
"v_mfma_f32_16x16x32_bf16 v[144:147], a[64:67], a[96:99], v[144:147]\n"
"v_mfma_f32_16x16x32_bf16 v[144:147], a[68:71], a[100:103], v[144:147]\n"
"buffer_load_dwordx4 v11, s[52:55], s67 offen offset:1024  lds\n"
"v_mfma_f32_16x16x32_bf16 v[172:175], a[76:79], a[100:103], v[172:175]\n"
"v_mfma_f32_16x16x32_bf16 v[172:175], a[72:75], a[96:99], v[172:175]\n"
"s_add_u32 s67, s75, 489600\n"
"v_mfma_f32_16x16x32_bf16 v[176:179], a[72:75], a[104:107], v[176:179]\n"
"v_mfma_f32_16x16x32_bf16 v[176:179], a[76:79], a[108:111], v[176:179]\n"
"buffer_load_dwordx4 v11, s[52:55], s67 offen offset:2048  lds\n"
"v_mfma_f32_16x16x32_bf16 v[148:151], a[68:71], a[108:111], v[148:151]\n"
"v_mfma_f32_16x16x32_bf16 v[148:151], a[64:67], a[104:107], v[148:151]\n"
"s_add_u32 s67, s75, 504960\n"
"v_mfma_f32_16x16x32_bf16 v[152:155], a[64:67], a[112:115], v[152:155]\n"
"s_sub_u32 s66, 122880, s73\n"
"v_mfma_f32_16x16x32_bf16 v[152:155], a[68:71], a[116:119], v[152:155]\n"
"v_mfma_f32_16x16x32_bf16 v[180:183], a[76:79], a[116:119], v[180:183]\n"
"v_add_u32 v12, s66, v7\n"
"v_mfma_f32_16x16x32_bf16 v[180:183], a[72:75], a[112:115], v[180:183]\n"
"buffer_load_dwordx4 v11, s[52:55], s67 offen offset:3072  lds\n"
"v_mfma_f32_16x16x32_bf16 v[184:187], a[72:75], a[120:123], v[184:187]\n"
"s_add_u32 m0, s72, 16384\n"
"v_add_u32 v13, s66, v8\n"
"v_mfma_f32_16x16x32_bf16 v[184:187], a[76:79], a[124:127], v[184:187]\n"
"v_mfma_f32_16x16x32_bf16 v[156:159], a[68:71], a[124:127], v[156:159]\n"
"s_add_u32 s67, s60, 512\n"
"v_mfma_f32_16x16x32_bf16 v[156:159], a[64:67], a[120:123], v[156:159]\n"
"buffer_load_dwordx4 v6, s[48:51], s67 offen offset:0  lds\n"
"v_mfma_f32_16x16x32_bf16 v[160:163], a[64:67], a[128:131], v[160:163]\n"
"v_mfma_f32_16x16x32_bf16 v[160:163], a[68:71], a[132:135], v[160:163]\n"
"s_add_u32 s67, s60, 15872\n"
"v_mfma_f32_16x16x32_bf16 v[188:191], a[76:79], a[132:135], v[188:191]\n"
"v_mfma_f32_16x16x32_bf16 v[188:191], a[72:75], a[128:131], v[188:191]\n"
"buffer_load_dwordx4 v6, s[48:51], s67 offen offset:1024  lds\n"
"v_mfma_f32_16x16x32_bf16 v[192:195], a[72:75], a[136:139], v[192:195]\n"
"v_mfma_f32_16x16x32_bf16 v[192:195], a[76:79], a[140:143], v[192:195]\n"
"s_add_u32 s67, s60, 31232\n"
"v_mfma_f32_16x16x32_bf16 v[164:167], a[68:71], a[140:143], v[164:167]\n"
"v_mfma_f32_16x16x32_bf16 v[164:167], a[64:67], a[136:139], v[164:167]\n"
"buffer_load_dwordx4 v6, s[48:51], s67 offen offset:2048  lds\n"
"v_mfma_f32_16x16x32_bf16 v[168:171], a[64:67], a[144:147], v[168:171]\n"
"v_mfma_f32_16x16x32_bf16 v[168:171], a[68:71], a[148:151], v[168:171]\n"
"s_add_u32 s67, s60, 46592\n"
"v_mfma_f32_16x16x32_bf16 v[196:199], a[76:79], a[148:151], v[196:199]\n"
"v_mfma_f32_16x16x32_bf16 v[196:199], a[72:75], a[144:147], v[196:199]\n"
"v_mfma_f32_16x16x32_bf16 a[20:23], a[72:75], a[152:155], a[20:23]\n"
"buffer_load_dwordx4 v6, s[48:51], s67 offen offset:3072  lds\n"
"v_mfma_f32_16x16x32_bf16 a[20:23], a[76:79], a[156:159], a[20:23]\n"
"v_mfma_f32_16x16x32_bf16 a[16:19], a[68:71], a[156:159], a[16:19]\n"
"s_add_u32 s70, s75, 786432\n"
"s_add_u32 s70, s70, s65\n"
"v_mfma_f32_16x16x32_bf16 a[16:19], a[64:67], a[152:155], a[16:19]\n"
"s_waitcnt vmcnt(22)\n"
"s_barrier\n"
"v_mfma_f32_16x16x32_bf16 v[200:203], a[80:83], a[96:99], v[200:203]\n"
"s_waitcnt vmcnt(12)\n"
"ds_read_b64_tr_b16 a[32:33], v9 offset:8192\n"
"v_mfma_f32_16x16x32_bf16 v[200:203], a[84:87], a[100:103], v[200:203]\n"
"ds_read_b64_tr_b16 a[34:35], v9 offset:8704\n"
"v_mfma_f32_16x16x32_bf16 v[228:231], a[92:95], a[100:103], v[228:231]\n"
"ds_read_b128 a[100:103], v13 offset:16384\n"
"buffer_load_dword a252, v14, s[52:55], s70 offen\n"
"v_mfma_f32_16x16x32_bf16 v[228:231], a[88:91], a[96:99], v[228:231]\n"
"ds_read_b128 a[96:99], v12 offset:16384\n"
"v_mfma_f32_16x16x32_bf16 v[232:235], a[88:91], a[104:107], v[232:235]\n"
"ds_read_b64_tr_b16 a[36:37], v9 offset:12288\n"
"v_mfma_f32_16x16x32_bf16 v[232:235], a[92:95], a[108:111], v[232:235]\n"
"ds_read_b64_tr_b16 a[38:39], v9 offset:12800\n"
"v_mfma_f32_16x16x32_bf16 v[204:207], a[84:87], a[108:111], v[204:207]\n"
"ds_read_b128 a[108:111], v13 offset:18432\n"
"v_mfma_f32_16x16x32_bf16 v[204:207], a[80:83], a[104:107], v[204:207]\n"
"ds_read_b128 a[104:107], v12 offset:18432\n"
"ds_read_b64_tr_b16 a[40:41], v9 offset:8200\n"
"v_mfma_f32_16x16x32_bf16 v[208:211], a[80:83], a[112:115], v[208:211]\n"
"s_add_u32 m0, s64, 8192\n"
"ds_read_b64_tr_b16 a[42:43], v9 offset:8712\n"
"v_mfma_f32_16x16x32_bf16 v[208:211], a[84:87], a[116:119], v[208:211]\n"
"ds_read_b64_tr_b16 a[44:45], v9 offset:12296\n"
"v_mfma_f32_16x16x32_bf16 v[236:239], a[92:95], a[116:119], v[236:239]\n"
"ds_read_b128 a[116:119], v13 offset:20480\n"
"v_mfma_f32_16x16x32_bf16 v[236:239], a[88:91], a[112:115], v[236:239]\n"
"ds_read_b128 a[112:115], v12 offset:20480\n"
"v_mfma_f32_16x16x32_bf16 v[240:243], a[88:91], a[120:123], v[240:243]\n"
"L_st1062:\n"
"s_cbranch_execz L_st3242\n"
"ds_read_b64_tr_b16 a[46:47], v9 offset:12808\n"
"v_mfma_f32_16x16x32_bf16 v[240:243], a[92:95], a[124:127], v[240:243]\n"
"ds_read_b64_tr_b16 a[48:49], v15 offset:8192\n"
"v_mfma_f32_16x16x32_bf16 v[212:215], a[84:87], a[124:127], v[212:215]\n"
"ds_read_b128 a[124:127], v13 offset:22528\n"
"v_mfma_f32_16x16x32_bf16 v[212:215], a[80:83], a[120:123], v[212:215]\n"
"ds_read_b128 a[120:123], v12 offset:22528\n"
"v_mfma_f32_16x16x32_bf16 v[216:219], a[80:83], a[128:131], v[216:219]\n"
"ds_read_b64_tr_b16 a[50:51], v15 offset:8704\n"
"v_mfma_f32_16x16x32_bf16 v[216:219], a[84:87], a[132:135], v[216:219]\n"
"ds_read_b64_tr_b16 a[52:53], v15 offset:12288\n"
"v_mfma_f32_16x16x32_bf16 v[244:247], a[92:95], a[132:135], v[244:247]\n"
"ds_read_b128 a[132:135], v13 offset:24576\n"
"v_mfma_f32_16x16x32_bf16 v[244:247], a[88:91], a[128:131], v[244:247]\n"
"s_add_u32 s67, s75, 524288\n"
"ds_read_b128 a[128:131], v12 offset:24576\n"
"v_mfma_f32_16x16x32_bf16 v[248:251], a[88:91], a[136:139], v[248:251]\n"
"ds_read_b64_tr_b16 a[54:55], v15 offset:12800\n"
"v_mfma_f32_16x16x32_bf16 v[248:251], a[92:95], a[140:143], v[248:251]\n"
"ds_read_b64_tr_b16 a[56:57], v15 offset:8200\n"
"v_mfma_f32_16x16x32_bf16 v[220:223], a[84:87], a[140:143], v[220:223]\n"
"ds_read_b128 a[140:143], v13 offset:26624\n"
"v_mfma_f32_16x16x32_bf16 v[220:223], a[80:83], a[136:139], v[220:223]\n"
"ds_read_b128 a[136:139], v12 offset:26624\n"
"v_mfma_f32_16x16x32_bf16 v[224:227], a[80:83], a[144:147], v[224:227]\n"
"ds_read_b64_tr_b16 a[58:59], v15 offset:8712\n"
"v_mfma_f32_16x16x32_bf16 v[224:227], a[84:87], a[148:151], v[224:227]\n"
"ds_read_b64_tr_b16 a[60:61], v15 offset:12296\n"
"v_mfma_f32_16x16x32_bf16 v[252:255], a[92:95], a[148:151], v[252:255]\n"
"ds_read_b128 a[148:151], v13 offset:28672\n"
"v_mfma_f32_16x16x32_bf16 v[252:255], a[88:91], a[144:147], v[252:255]\n"
"ds_read_b128 a[144:147], v12 offset:28672\n"
"ds_read_b64_tr_b16 a[62:63], v15 offset:12808\n"
"v_mfma_f32_16x16x32_bf16 a[28:31], a[88:91], a[152:155], a[28:31]\n"
"v_mfma_f32_16x16x32_bf16 a[28:31], a[92:95], a[156:159], a[28:31]\n"
"v_mfma_f32_16x16x32_bf16 a[24:27], a[84:87], a[156:159], a[24:27]\n"
"ds_read_b128 a[156:159], v13 offset:30720\n"
"v_mfma_f32_16x16x32_bf16 a[24:27], a[80:83], a[152:155], a[24:27]\n"
"s_waitcnt lgkmcnt(15)\n"
"ds_read_b128 a[152:155], v12 offset:30720\n"
"v_mfma_f32_16x16x32_bf16 v[32:35], a[32:35], a[96:99], v[32:35]\n"
"v_mfma_f32_16x16x32_bf16 v[32:35], a[36:39], a[100:103], v[32:35]\n"
"s_waitcnt lgkmcnt(2)\n"
"v_mfma_f32_16x16x32_bf16 v[60:63], a[44:47], a[100:103], v[60:63]\n"
"buffer_load_dwordx4 v11, s[52:55], s67 offen offset:0  lds\n"
"v_mfma_f32_16x16x32_bf16 v[60:63], a[40:43], a[96:99], v[60:63]\n"
"v_mfma_f32_16x16x32_bf16 v[64:67], a[40:43], a[104:107], v[64:67]\n"
"s_add_u32 s67, s75, 539648\n"
"v_mfma_f32_16x16x32_bf16 v[64:67], a[44:47], a[108:111], v[64:67]\n"
"v_mfma_f32_16x16x32_bf16 v[36:39], a[36:39], a[108:111], v[36:39]\n"
"buffer_load_dwordx4 v11, s[52:55], s67 offen offset:1024  lds\n"
"v_mfma_f32_16x16x32_bf16 v[36:39], a[32:35], a[104:107], v[36:39]\n"
"v_mfma_f32_16x16x32_bf16 v[40:43], a[32:35], a[112:115], v[40:43]\n"
"s_add_u32 s67, s75, 555008\n"
"v_mfma_f32_16x16x32_bf16 v[40:43], a[36:39], a[116:119], v[40:43]\n"
"v_mfma_f32_16x16x32_bf16 v[68:71], a[44:47], a[116:119], v[68:71]\n"
"v_mfma_f32_16x16x32_bf16 v[68:71], a[40:43], a[112:115], v[68:71]\n"
"buffer_load_dwordx4 v11, s[52:55], s67 offen offset:2048  lds\n"
"v_mfma_f32_16x16x32_bf16 v[72:75], a[40:43], a[120:123], v[72:75]\n"
"v_mfma_f32_16x16x32_bf16 v[72:75], a[44:47], a[124:127], v[72:75]\n"
"s_add_u32 s67, s75, 570368\n"
"v_mfma_f32_16x16x32_bf16 v[44:47], a[36:39], a[124:127], v[44:47]\n"
"v_mfma_f32_16x16x32_bf16 v[44:47], a[32:35], a[120:123], v[44:47]\n"
"buffer_load_dwordx4 v11, s[52:55], s67 offen offset:3072  lds\n"
"v_mfma_f32_16x16x32_bf16 v[48:51], a[32:35], a[128:131], v[48:51]\n"
"v_mfma_f32_16x16x32_bf16 v[48:51], a[36:39], a[132:135], v[48:51]\n"
"s_add_u32 m0, s64, 12288\n"
"v_mfma_f32_16x16x32_bf16 v[76:79], a[44:47], a[132:135], v[76:79]\n"
"s_waitcnt vmcnt(9)\n"
"ds_read_b64_tr_b16 a[64:65], v9 offset:0\n"
"v_mfma_f32_16x16x32_bf16 v[76:79], a[40:43], a[128:131], v[76:79]\n"
"s_add_u32 s67, s75, 589824\n"
"ds_read_b64_tr_b16 a[66:67], v9 offset:512\n"
"v_mfma_f32_16x16x32_bf16 v[80:83], a[40:43], a[136:139], v[80:83]\n"
"ds_read_b64_tr_b16 a[68:69], v9 offset:4096\n"
"v_mfma_f32_16x16x32_bf16 v[80:83], a[44:47], a[140:143], v[80:83]\n"
"ds_read_b64_tr_b16 a[70:71], v9 offset:4608\n"
"buffer_load_dwordx4 v11, s[52:55], s67 offen offset:0  lds\n"
"v_mfma_f32_16x16x32_bf16 v[52:55], a[36:39], a[140:143], v[52:55]\n"
"s_add_u32 s67, s75, 605184\n"
"ds_read_b64_tr_b16 a[72:73], v9 offset:8\n"
"v_mfma_f32_16x16x32_bf16 v[52:55], a[32:35], a[136:139], v[52:55]\n"
"s_waitcnt lgkmcnt(5)\n"
"ds_read_b64_tr_b16 a[74:75], v9 offset:520\n"
"v_mfma_f32_16x16x32_bf16 v[56:59], a[32:35], a[144:147], v[56:59]\n"
"ds_read_b64_tr_b16 a[76:77], v9 offset:4104\n"
"v_mfma_f32_16x16x32_bf16 v[56:59], a[36:39], a[148:151], v[56:59]\n"
"ds_read_b64_tr_b16 a[78:79], v9 offset:4616\n"
"v_mfma_f32_16x16x32_bf16 v[84:87], a[44:47], a[148:151], v[84:87]\n"
"buffer_load_dwordx4 v11, s[52:55], s67 offen offset:1024  lds\n"
"v_mfma_f32_16x16x32_bf16 v[84:87], a[40:43], a[144:147], v[84:87]\n"
"ds_read_b64_tr_b16 a[80:81], v15 offset:0\n"
"v_mfma_f32_16x16x32_bf16 a[4:7], a[40:43], a[152:155], a[4:7]\n"
"s_add_u32 s67, s75, 620544\n"
"ds_read_b64_tr_b16 a[82:83], v15 offset:512\n"
"v_mfma_f32_16x16x32_bf16 a[4:7], a[44:47], a[156:159], a[4:7]\n"
"ds_read_b64_tr_b16 a[84:85], v15 offset:4096\n"
"v_mfma_f32_16x16x32_bf16 a[0:3], a[36:39], a[156:159], a[0:3]\n"
"ds_read_b64_tr_b16 a[86:87], v15 offset:4608\n"
"buffer_load_dwordx4 v11, s[52:55], s67 offen offset:2048  lds\n"
"v_mfma_f32_16x16x32_bf16 a[0:3], a[32:35], a[152:155], a[0:3]\n"
"ds_read_b64_tr_b16 a[88:89], v15 offset:8\n"
"v_mfma_f32_16x16x32_bf16 v[88:91], a[48:51], a[96:99], v[88:91]\n"
"s_add_u32 s67, s75, 635904\n"
"ds_read_b64_tr_b16 a[90:91], v15 offset:520\n"
"v_mfma_f32_16x16x32_bf16 v[88:91], a[52:55], a[100:103], v[88:91]\n"
"ds_read_b64_tr_b16 a[92:93], v15 offset:4104\n"
"v_mfma_f32_16x16x32_bf16 v[116:119], a[60:63], a[100:103], v[116:119]\n"
"ds_read_b64_tr_b16 a[94:95], v15 offset:4616\n"
"v_mfma_f32_16x16x32_bf16 v[116:119], a[56:59], a[96:99], v[116:119]\n"
"buffer_load_dwordx4 v11, s[52:55], s67 offen offset:3072  lds\n"
"v_mfma_f32_16x16x32_bf16 v[120:123], a[56:59], a[104:107], v[120:123]\n"
"v_mfma_f32_16x16x32_bf16 v[120:123], a[60:63], a[108:111], v[120:123]\n"
"s_add_u32 m0, s64, 0\n"
"v_mfma_f32_16x16x32_bf16 v[92:95], a[52:55], a[108:111], v[92:95]\n"
"v_mfma_f32_16x16x32_bf16 v[92:95], a[48:51], a[104:107], v[92:95]\n"
"s_add_u32 s67, s75, 524416\n"
"v_mfma_f32_16x16x32_bf16 v[96:99], a[48:51], a[112:115], v[96:99]\n"
"s_waitcnt lgkmcnt(0)\n"
"v_mfma_f32_16x16x32_bf16 v[96:99], a[52:55], a[116:119], v[96:99]\n"
"buffer_load_dwordx4 v11, s[52:55], s67 offen offset:0  lds\n"
"v_mfma_f32_16x16x32_bf16 v[124:127], a[60:63], a[116:119], v[124:127]\n"
"v_mfma_f32_16x16x32_bf16 v[124:127], a[56:59], a[112:115], v[124:127]\n"
"s_add_u32 s67, s75, 539776\n"
"v_mfma_f32_16x16x32_bf16 v[128:131], a[56:59], a[120:123], v[128:131]\n"
"v_mfma_f32_16x16x32_bf16 v[128:131], a[60:63], a[124:127], v[128:131]\n"
"v_mfma_f32_16x16x32_bf16 v[100:103], a[52:55], a[124:127], v[100:103]\n"
"buffer_load_dwordx4 v11, s[52:55], s67 offen offset:1024  lds\n"
"v_mfma_f32_16x16x32_bf16 v[100:103], a[48:51], a[120:123], v[100:103]\n"
"v_mfma_f32_16x16x32_bf16 v[104:107], a[48:51], a[128:131], v[104:107]\n"
"s_add_u32 s67, s75, 555136\n"
"v_mfma_f32_16x16x32_bf16 v[104:107], a[52:55], a[132:135], v[104:107]\n"
"v_mfma_f32_16x16x32_bf16 v[132:135], a[60:63], a[132:135], v[132:135]\n"
"v_mfma_f32_16x16x32_bf16 v[132:135], a[56:59], a[128:131], v[132:135]\n"
"buffer_load_dwordx4 v11, s[52:55], s67 offen offset:2048  lds\n"
"v_mfma_f32_16x16x32_bf16 v[136:139], a[56:59], a[136:139], v[136:139]\n"
"v_mfma_f32_16x16x32_bf16 v[136:139], a[60:63], a[140:143], v[136:139]\n"
"s_add_u32 s67, s75, 570496\n"
"v_mfma_f32_16x16x32_bf16 v[108:111], a[52:55], a[140:143], v[108:111]\n"
"v_mfma_f32_16x16x32_bf16 v[108:111], a[48:51], a[136:139], v[108:111]\n"
"buffer_load_dwordx4 v11, s[52:55], s67 offen offset:3072  lds\n"
"v_mfma_f32_16x16x32_bf16 v[112:115], a[48:51], a[144:147], v[112:115]\n"
"v_mfma_f32_16x16x32_bf16 v[112:115], a[52:55], a[148:151], v[112:115]\n"
"s_add_u32 m0, s64, 4096\n"
"v_mfma_f32_16x16x32_bf16 v[140:143], a[60:63], a[148:151], v[140:143]\n"
"v_mfma_f32_16x16x32_bf16 v[140:143], a[56:59], a[144:147], v[140:143]\n"
"s_add_u32 s67, s75, 589952\n"
"v_mfma_f32_16x16x32_bf16 a[12:15], a[56:59], a[152:155], a[12:15]\n"
"v_mfma_f32_16x16x32_bf16 a[12:15], a[60:63], a[156:159], a[12:15]\n"
"buffer_load_dwordx4 v11, s[52:55], s67 offen offset:0  lds\n"
"v_mfma_f32_16x16x32_bf16 a[8:11], a[52:55], a[156:159], a[8:11]\n"
"v_mfma_f32_16x16x32_bf16 a[8:11], a[48:51], a[152:155], a[8:11]\n"
"s_add_u32 s67, s75, 605312\n"
"v_mfma_f32_16x16x32_bf16 v[144:147], a[64:67], a[96:99], v[144:147]\n"
"v_mfma_f32_16x16x32_bf16 v[144:147], a[68:71], a[100:103], v[144:147]\n"
"buffer_load_dwordx4 v11, s[52:55], s67 offen offset:1024  lds\n"
"v_mfma_f32_16x16x32_bf16 v[172:175], a[76:79], a[100:103], v[172:175]\n"
"v_mfma_f32_16x16x32_bf16 v[172:175], a[72:75], a[96:99], v[172:175]\n"
"s_add_u32 s67, s75, 620672\n"
"v_mfma_f32_16x16x32_bf16 v[176:179], a[72:75], a[104:107], v[176:179]\n"
"v_mfma_f32_16x16x32_bf16 v[176:179], a[76:79], a[108:111], v[176:179]\n"
"buffer_load_dwordx4 v11, s[52:55], s67 offen offset:2048  lds\n"
"v_mfma_f32_16x16x32_bf16 v[148:151], a[68:71], a[108:111], v[148:151]\n"
"v_mfma_f32_16x16x32_bf16 v[148:151], a[64:67], a[104:107], v[148:151]\n"
"s_add_u32 s67, s75, 636032\n"
"v_mfma_f32_16x16x32_bf16 v[152:155], a[64:67], a[112:115], v[152:155]\n"
"s_sub_u32 s66, 0, s73\n"
"v_mfma_f32_16x16x32_bf16 v[152:155], a[68:71], a[116:119], v[152:155]\n"
"v_mfma_f32_16x16x32_bf16 v[180:183], a[76:79], a[116:119], v[180:183]\n"
"v_add_u32 v12, s66, v7\n"
"v_mfma_f32_16x16x32_bf16 v[180:183], a[72:75], a[112:115], v[180:183]\n"
"buffer_load_dwordx4 v11, s[52:55], s67 offen offset:3072  lds\n"
"v_mfma_f32_16x16x32_bf16 v[184:187], a[72:75], a[120:123], v[184:187]\n"
"s_add_u32 m0, s72, 57344\n"
"v_add_u32 v13, s66, v8\n"
"v_mfma_f32_16x16x32_bf16 v[184:187], a[76:79], a[124:127], v[184:187]\n"
"v_mfma_f32_16x16x32_bf16 v[156:159], a[68:71], a[124:127], v[156:159]\n"
"s_add_u32 s67, s60, 640\n"
"v_mfma_f32_16x16x32_bf16 v[156:159], a[64:67], a[120:123], v[156:159]\n"
"buffer_load_dwordx4 v6, s[48:51], s67 offen offset:0  lds\n"
"v_mfma_f32_16x16x32_bf16 v[160:163], a[64:67], a[128:131], v[160:163]\n"
"v_mfma_f32_16x16x32_bf16 v[160:163], a[68:71], a[132:135], v[160:163]\n"
"s_add_u32 s67, s60, 16000\n"
"v_mfma_f32_16x16x32_bf16 v[188:191], a[76:79], a[132:135], v[188:191]\n"
"v_mfma_f32_16x16x32_bf16 v[188:191], a[72:75], a[128:131], v[188:191]\n"
"buffer_load_dwordx4 v6, s[48:51], s67 offen offset:1024  lds\n"
"v_mfma_f32_16x16x32_bf16 v[192:195], a[72:75], a[136:139], v[192:195]\n"
"v_mfma_f32_16x16x32_bf16 v[192:195], a[76:79], a[140:143], v[192:195]\n"
"s_add_u32 s67, s60, 31360\n"
"v_mfma_f32_16x16x32_bf16 v[164:167], a[68:71], a[140:143], v[164:167]\n"
"v_mfma_f32_16x16x32_bf16 v[164:167], a[64:67], a[136:139], v[164:167]\n"
"buffer_load_dwordx4 v6, s[48:51], s67 offen offset:2048  lds\n"
"v_mfma_f32_16x16x32_bf16 v[168:171], a[64:67], a[144:147], v[168:171]\n"
"v_mfma_f32_16x16x32_bf16 v[168:171], a[68:71], a[148:151], v[168:171]\n"
"s_add_u32 s67, s60, 46720\n"
"v_mfma_f32_16x16x32_bf16 v[196:199], a[76:79], a[148:151], v[196:199]\n"
"v_mfma_f32_16x16x32_bf16 v[196:199], a[72:75], a[144:147], v[196:199]\n"
"v_mfma_f32_16x16x32_bf16 a[20:23], a[72:75], a[152:155], a[20:23]\n"
"buffer_load_dwordx4 v6, s[48:51], s67 offen offset:3072  lds\n"
"v_mfma_f32_16x16x32_bf16 a[20:23], a[76:79], a[156:159], a[20:23]\n"
"s_sub_u32 s70, 1048576, s75\n"
"v_and_b32 v19, 0x20000, v14\n"
"v_mfma_f32_16x16x32_bf16 a[16:19], a[68:71], a[156:159], a[16:19]\n"
"v_add_u32 v14, v19, v14\n"
"v_mfma_f32_16x16x32_bf16 a[16:19], a[64:67], a[152:155], a[16:19]\n"
"s_waitcnt vmcnt(21)\n"
"s_barrier\n"
"v_mfma_f32_16x16x32_bf16 v[200:203], a[80:83], a[96:99], v[200:203]\n"
"s_waitcnt vmcnt(12)\n"
"ds_read_b64_tr_b16 a[32:33], v9 offset:8192\n"
"v_mfma_f32_16x16x32_bf16 v[200:203], a[84:87], a[100:103], v[200:203]\n"
"s_add_u32 s70, s70, s65\n"
"ds_read_b64_tr_b16 a[34:35], v9 offset:8704\n"
"s_getpc_b64 s[96:97]\n"
"L_c10_:\n"
"s_add_u32 s96, s96, L_H6S-L_c10_\n"
"s_addc_u32 s97, s97, 0\n"
"v_mfma_f32_16x16x32_bf16 v[228:231], a[92:95], a[100:103], v[228:231]\n"
"s_lshr_b32 s66, %26, 3\n"
"ds_read_b128 a[100:103], v13 offset:16384\n"
"buffer_load_dword a252, v14, s[52:55], s70 offen\n"
"v_mfma_f32_16x16x32_bf16 v[228:231], a[88:91], a[96:99], v[228:231]\n"
"s_lshl2_add_u32 s66, s66, %16\n"
"ds_read_b128 a[96:99], v12 offset:16384\n"
"v_mfma_f32_16x16x32_bf16 v[232:235], a[88:91], a[104:107], v[232:235]\n"
"s_mul_i32 s66, s66, 128\n"
"ds_read_b64_tr_b16 a[36:37], v9 offset:12288\n"
"v_mfma_f32_16x16x32_bf16 v[232:235], a[92:95], a[108:111], v[232:235]\n"
"s_min_u32 s66, s66, L_headF-L_H6S-128\n"
"ds_read_b64_tr_b16 a[38:39], v9 offset:12800\n"
"v_min_u32 v22, 0, v0\n"
"v_mfma_f32_16x16x32_bf16 v[204:207], a[84:87], a[108:111], v[204:207]\n"
"ds_read_b128 a[108:111], v13 offset:18432\n"
"v_lshl_add_u32 v22, v22, 3, s66\n"
"v_mfma_f32_16x16x32_bf16 v[204:207], a[80:83], a[104:107], v[204:207]\n"
"ds_read_b128 a[104:107], v12 offset:18432\n"
"global_load_dword v23, v22, s[96:97]\n"
"ds_read_b64_tr_b16 a[40:41], v9 offset:8200\n"
"v_mfma_f32_16x16x32_bf16 v[208:211], a[80:83], a[112:115], v[208:211]\n"
"s_add_u32 m0, s64, 8192\n"
"ds_read_b64_tr_b16 a[42:43], v9 offset:8712\n"
"v_mfma_f32_16x16x32_bf16 v[208:211], a[84:87], a[116:119], v[208:211]\n"
"ds_read_b64_tr_b16 a[44:45], v9 offset:12296\n"
"v_mfma_f32_16x16x32_bf16 v[236:239], a[92:95], a[116:119], v[236:239]\n"
"ds_read_b128 a[116:119], v13 offset:20480\n"
"v_mfma_f32_16x16x32_bf16 v[236:239], a[88:91], a[112:115], v[236:239]\n"
"ds_read_b128 a[112:115], v12 offset:20480\n"
"v_mfma_f32_16x16x32_bf16 v[240:243], a[88:91], a[120:123], v[240:243]\n"
"ds_read_b64_tr_b16 a[46:47], v9 offset:12808\n"
"v_mfma_f32_16x16x32_bf16 v[240:243], a[92:95], a[124:127], v[240:243]\n"
"ds_read_b64_tr_b16 a[48:49], v15 offset:8192\n"
"v_mfma_f32_16x16x32_bf16 v[212:215], a[84:87], a[124:127], v[212:215]\n"
"ds_read_b128 a[124:127], v13 offset:22528\n"
"v_mfma_f32_16x16x32_bf16 v[212:215], a[80:83], a[120:123], v[212:215]\n"
"ds_read_b128 a[120:123], v12 offset:22528\n"
"v_mfma_f32_16x16x32_bf16 v[216:219], a[80:83], a[128:131], v[216:219]\n"
"ds_read_b64_tr_b16 a[50:51], v15 offset:8704\n"
"v_mfma_f32_16x16x32_bf16 v[216:219], a[84:87], a[132:135], v[216:219]\n"
"ds_read_b64_tr_b16 a[52:53], v15 offset:12288\n"
"v_mfma_f32_16x16x32_bf16 v[244:247], a[92:95], a[132:135], v[244:247]\n"
"ds_read_b128 a[132:135], v13 offset:24576\n"
"v_mfma_f32_16x16x32_bf16 v[244:247], a[88:91], a[128:131], v[244:247]\n"
"s_add_u32 s67, s75, 655360\n"
"ds_read_b128 a[128:131], v12 offset:24576\n"
"v_mfma_f32_16x16x32_bf16 v[248:251], a[88:91], a[136:139], v[248:251]\n"
"ds_read_b64_tr_b16 a[54:55], v15 offset:12800\n"
"v_mfma_f32_16x16x32_bf16 v[248:251], a[92:95], a[140:143], v[248:251]\n"
"ds_read_b64_tr_b16 a[56:57], v15 offset:8200\n"
"v_mfma_f32_16x16x32_bf16 v[220:223], a[84:87], a[140:143], v[220:223]\n"
"ds_read_b128 a[140:143], v13 offset:26624\n"
"v_mfma_f32_16x16x32_bf16 v[220:223], a[80:83], a[136:139], v[220:223]\n"
"ds_read_b128 a[136:139], v12 offset:26624\n"
"v_mfma_f32_16x16x32_bf16 v[224:227], a[80:83], a[144:147], v[224:227]\n"
"ds_read_b64_tr_b16 a[58:59], v15 offset:8712\n"
"v_mfma_f32_16x16x32_bf16 v[224:227], a[84:87], a[148:151], v[224:227]\n"
"ds_read_b64_tr_b16 a[60:61], v15 offset:12296\n"
"v_mfma_f32_16x16x32_bf16 v[252:255], a[92:95], a[148:151], v[252:255]\n"
"ds_read_b128 a[148:151], v13 offset:28672\n"
"v_mfma_f32_16x16x32_bf16 v[252:255], a[88:91], a[144:147], v[252:255]\n"
"ds_read_b128 a[144:147], v12 offset:28672\n"
"ds_read_b64_tr_b16 a[62:63], v15 offset:12808\n"
"v_mfma_f32_16x16x32_bf16 a[28:31], a[88:91], a[152:155], a[28:31]\n"
"v_mfma_f32_16x16x32_bf16 a[28:31], a[92:95], a[156:159], a[28:31]\n"
"v_mfma_f32_16x16x32_bf16 a[24:27], a[84:87], a[156:159], a[24:27]\n"
"ds_read_b128 a[156:159], v13 offset:30720\n"
"v_mfma_f32_16x16x32_bf16 a[24:27], a[80:83], a[152:155], a[24:27]\n"
"s_waitcnt lgkmcnt(15)\n"
"ds_read_b128 a[152:155], v12 offset:30720\n"
"v_mfma_f32_16x16x32_bf16 v[32:35], a[32:35], a[96:99], v[32:35]\n"
"v_mfma_f32_16x16x32_bf16 v[32:35], a[36:39], a[100:103], v[32:35]\n"
"s_waitcnt lgkmcnt(2)\n"
"v_mfma_f32_16x16x32_bf16 v[60:63], a[44:47], a[100:103], v[60:63]\n"
"buffer_load_dwordx4 v11, s[52:55], s67 offen offset:0  lds\n"
"v_mfma_f32_16x16x32_bf16 v[60:63], a[40:43], a[96:99], v[60:63]\n"
"v_mfma_f32_16x16x32_bf16 v[64:67], a[40:43], a[104:107], v[64:67]\n"
"s_add_u32 s67, s75, 670720\n"
"v_mfma_f32_16x16x32_bf16 v[64:67], a[44:47], a[108:111], v[64:67]\n"
"v_mfma_f32_16x16x32_bf16 v[36:39], a[36:39], a[108:111], v[36:39]\n"
"buffer_load_dwordx4 v11, s[52:55], s67 offen offset:1024  lds\n"
"v_mfma_f32_16x16x32_bf16 v[36:39], a[32:35], a[104:107], v[36:39]\n"
"v_mfma_f32_16x16x32_bf16 v[40:43], a[32:35], a[112:115], v[40:43]\n"
"s_add_u32 s67, s75, 686080\n"
"v_mfma_f32_16x16x32_bf16 v[40:43], a[36:39], a[116:119], v[40:43]\n"
"v_mfma_f32_16x16x32_bf16 v[68:71], a[44:47], a[116:119], v[68:71]\n"
"v_mfma_f32_16x16x32_bf16 v[68:71], a[40:43], a[112:115], v[68:71]\n"
"buffer_load_dwordx4 v11, s[52:55], s67 offen offset:2048  lds\n"
"v_mfma_f32_16x16x32_bf16 v[72:75], a[40:43], a[120:123], v[72:75]\n"
"v_mfma_f32_16x16x32_bf16 v[72:75], a[44:47], a[124:127], v[72:75]\n"
"s_add_u32 s67, s75, 701440\n"
"v_mfma_f32_16x16x32_bf16 v[44:47], a[36:39], a[124:127], v[44:47]\n"
"v_mfma_f32_16x16x32_bf16 v[44:47], a[32:35], a[120:123], v[44:47]\n"
"buffer_load_dwordx4 v11, s[52:55], s67 offen offset:3072  lds\n"
"v_mfma_f32_16x16x32_bf16 v[48:51], a[32:35], a[128:131], v[48:51]\n"
"v_mfma_f32_16x16x32_bf16 v[48:51], a[36:39], a[132:135], v[48:51]\n"
"s_add_u32 m0, s64, 12288\n"
"v_mfma_f32_16x16x32_bf16 v[76:79], a[44:47], a[132:135], v[76:79]\n"
"s_waitcnt vmcnt(10)\n"
"ds_read_b64_tr_b16 a[64:65], v9 offset:0\n"
"v_mfma_f32_16x16x32_bf16 v[76:79], a[40:43], a[128:131], v[76:79]\n"
"s_add_u32 s67, s75, 720896\n"
"ds_read_b64_tr_b16 a[66:67], v9 offset:512\n"
"v_mfma_f32_16x16x32_bf16 v[80:83], a[40:43], a[136:139], v[80:83]\n"
"ds_read_b64_tr_b16 a[68:69], v9 offset:4096\n"
"v_mfma_f32_16x16x32_bf16 v[80:83], a[44:47], a[140:143], v[80:83]\n"
"ds_read_b64_tr_b16 a[70:71], v9 offset:4608\n"
"buffer_load_dwordx4 v11, s[52:55], s67 offen offset:0  lds\n"
"v_mfma_f32_16x16x32_bf16 v[52:55], a[36:39], a[140:143], v[52:55]\n"
"s_add_u32 s67, s75, 736256\n"
"ds_read_b64_tr_b16 a[72:73], v9 offset:8\n"
"v_mfma_f32_16x16x32_bf16 v[52:55], a[32:35], a[136:139], v[52:55]\n"
"s_waitcnt lgkmcnt(5)\n"
"ds_read_b64_tr_b16 a[74:75], v9 offset:520\n"
"v_mfma_f32_16x16x32_bf16 v[56:59], a[32:35], a[144:147], v[56:59]\n"
"ds_read_b64_tr_b16 a[76:77], v9 offset:4104\n"
"v_mfma_f32_16x16x32_bf16 v[56:59], a[36:39], a[148:151], v[56:59]\n"
"ds_read_b64_tr_b16 a[78:79], v9 offset:4616\n"
"v_mfma_f32_16x16x32_bf16 v[84:87], a[44:47], a[148:151], v[84:87]\n"
"buffer_load_dwordx4 v11, s[52:55], s67 offen offset:1024  lds\n"
"v_mfma_f32_16x16x32_bf16 v[84:87], a[40:43], a[144:147], v[84:87]\n"
"ds_read_b64_tr_b16 a[80:81], v15 offset:0\n"
"v_mfma_f32_16x16x32_bf16 a[4:7], a[40:43], a[152:155], a[4:7]\n"
"s_add_u32 s67, s75, 751616\n"
"ds_read_b64_tr_b16 a[82:83], v15 offset:512\n"
"v_mfma_f32_16x16x32_bf16 a[4:7], a[44:47], a[156:159], a[4:7]\n"
"ds_read_b64_tr_b16 a[84:85], v15 offset:4096\n"
"v_mfma_f32_16x16x32_bf16 a[0:3], a[36:39], a[156:159], a[0:3]\n"
"ds_read_b64_tr_b16 a[86:87], v15 offset:4608\n"
"buffer_load_dwordx4 v11, s[52:55], s67 offen offset:2048  lds\n"
"v_mfma_f32_16x16x32_bf16 a[0:3], a[32:35], a[152:155], a[0:3]\n"
"ds_read_b64_tr_b16 a[88:89], v15 offset:8\n"
"v_mfma_f32_16x16x32_bf16 v[88:91], a[48:51], a[96:99], v[88:91]\n"
"s_add_u32 s67, s75, 766976\n"
"ds_read_b64_tr_b16 a[90:91], v15 offset:520\n"
"v_mfma_f32_16x16x32_bf16 v[88:91], a[52:55], a[100:103], v[88:91]\n"
"ds_read_b64_tr_b16 a[92:93], v15 offset:4104\n"
"v_mfma_f32_16x16x32_bf16 v[116:119], a[60:63], a[100:103], v[116:119]\n"
"ds_read_b64_tr_b16 a[94:95], v15 offset:4616\n"
"v_mfma_f32_16x16x32_bf16 v[116:119], a[56:59], a[96:99], v[116:119]\n"
"buffer_load_dwordx4 v11, s[52:55], s67 offen offset:3072  lds\n"
"v_mfma_f32_16x16x32_bf16 v[120:123], a[56:59], a[104:107], v[120:123]\n"
"v_mfma_f32_16x16x32_bf16 v[120:123], a[60:63], a[108:111], v[120:123]\n"
"s_add_u32 m0, s64, 0\n"
"v_mfma_f32_16x16x32_bf16 v[92:95], a[52:55], a[108:111], v[92:95]\n"
"v_mfma_f32_16x16x32_bf16 v[92:95], a[48:51], a[104:107], v[92:95]\n"
"s_add_u32 s67, s75, 655488\n"
"v_mfma_f32_16x16x32_bf16 v[96:99], a[48:51], a[112:115], v[96:99]\n"
"s_waitcnt lgkmcnt(0)\n"
"v_mfma_f32_16x16x32_bf16 v[96:99], a[52:55], a[116:119], v[96:99]\n"
"buffer_load_dwordx4 v11, s[52:55], s67 offen offset:0  lds\n"
"v_mfma_f32_16x16x32_bf16 v[124:127], a[60:63], a[116:119], v[124:127]\n"
"v_mfma_f32_16x16x32_bf16 v[124:127], a[56:59], a[112:115], v[124:127]\n"
"s_add_u32 s67, s75, 670848\n"
"v_mfma_f32_16x16x32_bf16 v[128:131], a[56:59], a[120:123], v[128:131]\n"
"v_mfma_f32_16x16x32_bf16 v[128:131], a[60:63], a[124:127], v[128:131]\n"
"v_mfma_f32_16x16x32_bf16 v[100:103], a[52:55], a[124:127], v[100:103]\n"
"buffer_load_dwordx4 v11, s[52:55], s67 offen offset:1024  lds\n"
"v_mfma_f32_16x16x32_bf16 v[100:103], a[48:51], a[120:123], v[100:103]\n"
"v_mfma_f32_16x16x32_bf16 v[104:107], a[48:51], a[128:131], v[104:107]\n"
"s_add_u32 s67, s75, 686208\n"
"v_mfma_f32_16x16x32_bf16 v[104:107], a[52:55], a[132:135], v[104:107]\n"
"v_mfma_f32_16x16x32_bf16 v[132:135], a[60:63], a[132:135], v[132:135]\n"
"v_mfma_f32_16x16x32_bf16 v[132:135], a[56:59], a[128:131], v[132:135]\n"
"buffer_load_dwordx4 v11, s[52:55], s67 offen offset:2048  lds\n"
"v_mfma_f32_16x16x32_bf16 v[136:139], a[56:59], a[136:139], v[136:139]\n"
"v_mfma_f32_16x16x32_bf16 v[136:139], a[60:63], a[140:143], v[136:139]\n"
"s_add_u32 s67, s75, 701568\n"
"v_mfma_f32_16x16x32_bf16 v[108:111], a[52:55], a[140:143], v[108:111]\n"
"v_mfma_f32_16x16x32_bf16 v[108:111], a[48:51], a[136:139], v[108:111]\n"
"buffer_load_dwordx4 v11, s[52:55], s67 offen offset:3072  lds\n"
"v_mfma_f32_16x16x32_bf16 v[112:115], a[48:51], a[144:147], v[112:115]\n"
"v_mfma_f32_16x16x32_bf16 v[112:115], a[52:55], a[148:151], v[112:115]\n"
"s_add_u32 m0, s64, 4096\n"
"v_mfma_f32_16x16x32_bf16 v[140:143], a[60:63], a[148:151], v[140:143]\n"
"v_mfma_f32_16x16x32_bf16 v[140:143], a[56:59], a[144:147], v[140:143]\n"
"s_add_u32 s67, s75, 721024\n"
"v_mfma_f32_16x16x32_bf16 a[12:15], a[56:59], a[152:155], a[12:15]\n"
"v_mfma_f32_16x16x32_bf16 a[12:15], a[60:63], a[156:159], a[12:15]\n"
"buffer_load_dwordx4 v11, s[52:55], s67 offen offset:0  lds\n"
"v_mfma_f32_16x16x32_bf16 a[8:11], a[52:55], a[156:159], a[8:11]\n"
"v_mfma_f32_16x16x32_bf16 a[8:11], a[48:51], a[152:155], a[8:11]\n"
"s_add_u32 s67, s75, 736384\n"
"v_mfma_f32_16x16x32_bf16 v[144:147], a[64:67], a[96:99], v[144:147]\n"
"v_mfma_f32_16x16x32_bf16 v[144:147], a[68:71], a[100:103], v[144:147]\n"
"buffer_load_dwordx4 v11, s[52:55], s67 offen offset:1024  lds\n"
"v_mfma_f32_16x16x32_bf16 v[172:175], a[76:79], a[100:103], v[172:175]\n"
"v_mfma_f32_16x16x32_bf16 v[172:175], a[72:75], a[96:99], v[172:175]\n"
"s_add_u32 s67, s75, 751744\n"
"v_mfma_f32_16x16x32_bf16 v[176:179], a[72:75], a[104:107], v[176:179]\n"
"v_mfma_f32_16x16x32_bf16 v[176:179], a[76:79], a[108:111], v[176:179]\n"
"buffer_load_dwordx4 v11, s[52:55], s67 offen offset:2048  lds\n"
"v_mfma_f32_16x16x32_bf16 v[148:151], a[68:71], a[108:111], v[148:151]\n"
"v_mfma_f32_16x16x32_bf16 v[148:151], a[64:67], a[104:107], v[148:151]\n"
"s_add_u32 s67, s75, 767104\n"
"v_mfma_f32_16x16x32_bf16 v[152:155], a[64:67], a[112:115], v[152:155]\n"
"s_sub_u32 s66, 40960, s73\n"
"v_mfma_f32_16x16x32_bf16 v[152:155], a[68:71], a[116:119], v[152:155]\n"
"v_mfma_f32_16x16x32_bf16 v[180:183], a[76:79], a[116:119], v[180:183]\n"
"v_add_u32 v12, s66, v7\n"
"v_mfma_f32_16x16x32_bf16 v[180:183], a[72:75], a[112:115], v[180:183]\n"
"buffer_load_dwordx4 v11, s[52:55], s67 offen offset:3072  lds\n"
"v_mfma_f32_16x16x32_bf16 v[184:187], a[72:75], a[120:123], v[184:187]\n"
"s_add_u32 m0, s72, 98304\n"
"v_add_u32 v13, s66, v8\n"
"v_mfma_f32_16x16x32_bf16 v[184:187], a[76:79], a[124:127], v[184:187]\n"
"v_mfma_f32_16x16x32_bf16 v[156:159], a[68:71], a[124:127], v[156:159]\n"
"s_add_u32 s67, s60, 768\n"
"v_mfma_f32_16x16x32_bf16 v[156:159], a[64:67], a[120:123], v[156:159]\n"
"buffer_load_dwordx4 v6, s[48:51], s67 offen offset:0  lds\n"
"v_mfma_f32_16x16x32_bf16 v[160:163], a[64:67], a[128:131], v[160:163]\n"
"v_mfma_f32_16x16x32_bf16 v[160:163], a[68:71], a[132:135], v[160:163]\n"
"s_add_u32 s67, s60, 16128\n"
"v_mfma_f32_16x16x32_bf16 v[188:191], a[76:79], a[132:135], v[188:191]\n"
"v_mfma_f32_16x16x32_bf16 v[188:191], a[72:75], a[128:131], v[188:191]\n"
"buffer_load_dwordx4 v6, s[48:51], s67 offen offset:1024  lds\n"
"v_mfma_f32_16x16x32_bf16 v[192:195], a[72:75], a[136:139], v[192:195]\n"
"v_mfma_f32_16x16x32_bf16 v[192:195], a[76:79], a[140:143], v[192:195]\n"
"s_add_u32 s67, s60, 31488\n"
"v_mfma_f32_16x16x32_bf16 v[164:167], a[68:71], a[140:143], v[164:167]\n"
"v_mfma_f32_16x16x32_bf16 v[164:167], a[64:67], a[136:139], v[164:167]\n"
"buffer_load_dwordx4 v6, s[48:51], s67 offen offset:2048  lds\n"
"v_mfma_f32_16x16x32_bf16 v[168:171], a[64:67], a[144:147], v[168:171]\n"
"v_mfma_f32_16x16x32_bf16 v[168:171], a[68:71], a[148:151], v[168:171]\n"
"s_add_u32 s67, s60, 46848\n"
"v_mfma_f32_16x16x32_bf16 v[196:199], a[76:79], a[148:151], v[196:199]\n"
"v_mfma_f32_16x16x32_bf16 v[196:199], a[72:75], a[144:147], v[196:199]\n"
"v_mfma_f32_16x16x32_bf16 a[20:23], a[72:75], a[152:155], a[20:23]\n"
"buffer_load_dwordx4 v6, s[48:51], s67 offen offset:3072  lds\n"
"v_mfma_f32_16x16x32_bf16 a[20:23], a[76:79], a[156:159], a[20:23]\n"
"v_mfma_f32_16x16x32_bf16 a[16:19], a[68:71], a[156:159], a[16:19]\n"
"s_sub_u32 s70, 1572864, s75\n"
"s_add_u32 s70, s70, s65\n"
"v_mfma_f32_16x16x32_bf16 a[16:19], a[64:67], a[152:155], a[16:19]\n"
"s_waitcnt vmcnt(22)\n"
"s_barrier\n"
"v_mfma_f32_16x16x32_bf16 v[200:203], a[80:83], a[96:99], v[200:203]\n"
"s_waitcnt vmcnt(12)\n"
"ds_read_b64_tr_b16 a[32:33], v9 offset:8192\n"
"v_mfma_f32_16x16x32_bf16 v[200:203], a[84:87], a[100:103], v[200:203]\n"
"ds_read_b64_tr_b16 a[34:35], v9 offset:8704\n"
"v_mfma_f32_16x16x32_bf16 v[228:231], a[92:95], a[100:103], v[228:231]\n"
"ds_read_b128 a[100:103], v13 offset:16384\n"
"buffer_load_dword a252, v14, s[52:55], s70 offen\n"
"v_mfma_f32_16x16x32_bf16 v[228:231], a[88:91], a[96:99], v[228:231]\n"
"ds_read_b128 a[96:99], v12 offset:16384\n"
"v_mfma_f32_16x16x32_bf16 v[232:235], a[88:91], a[104:107], v[232:235]\n"
"buffer_store_dwordx2 v[16:17], v18, s[56:59], %11 offen sc1\n"
"ds_read_b64_tr_b16 a[36:37], v9 offset:12288\n"
"v_mfma_f32_16x16x32_bf16 v[232:235], a[92:95], a[108:111], v[232:235]\n"
"ds_read_b64_tr_b16 a[38:39], v9 offset:12800\n"
"v_mfma_f32_16x16x32_bf16 v[204:207], a[84:87], a[108:111], v[204:207]\n"
"ds_read_b128 a[108:111], v13 offset:18432\n"
"v_mfma_f32_16x16x32_bf16 v[204:207], a[80:83], a[104:107], v[204:207]\n"
"ds_read_b128 a[104:107], v12 offset:18432\n"
"buffer_store_dwordx2 v[16:17], v18, s[56:59], s69 offen sc1\n"
"ds_read_b64_tr_b16 a[40:41], v9 offset:8200\n"
"v_mfma_f32_16x16x32_bf16 v[208:211], a[80:83], a[112:115], v[208:211]\n"
"ds_read_b64_tr_b16 a[42:43], v9 offset:8712\n"
"v_mfma_f32_16x16x32_bf16 v[208:211], a[84:87], a[116:119], v[208:211]\n"
"ds_read_b64_tr_b16 a[44:45], v9 offset:12296\n"
"v_mfma_f32_16x16x32_bf16 v[236:239], a[92:95], a[116:119], v[236:239]\n"
"ds_read_b128 a[116:119], v13 offset:20480\n"
"v_mfma_f32_16x16x32_bf16 v[236:239], a[88:91], a[112:115], v[236:239]\n"
"ds_read_b128 a[112:115], v12 offset:20480\n"
"v_mfma_f32_16x16x32_bf16 v[240:243], a[88:91], a[120:123], v[240:243]\n"
"s_add_u32 m0, s64, 8192\n"
"ds_read_b64_tr_b16 a[46:47], v9 offset:12808\n"
"v_mfma_f32_16x16x32_bf16 v[240:243], a[92:95], a[124:127], v[240:243]\n"
"ds_read_b64_tr_b16 a[48:49], v15 offset:8192\n"
"v_mfma_f32_16x16x32_bf16 v[212:215], a[84:87], a[124:127], v[212:215]\n"
"ds_read_b128 a[124:127], v13 offset:22528\n"
"v_mfma_f32_16x16x32_bf16 v[212:215], a[80:83], a[120:123], v[212:215]\n"
"ds_read_b128 a[120:123], v12 offset:22528\n"
"v_mfma_f32_16x16x32_bf16 v[216:219], a[80:83], a[128:131], v[216:219]\n"
"ds_read_b64_tr_b16 a[50:51], v15 offset:8704\n"
"v_mfma_f32_16x16x32_bf16 v[216:219], a[84:87], a[132:135], v[216:219]\n"
"ds_read_b64_tr_b16 a[52:53], v15 offset:12288\n"
"v_mfma_f32_16x16x32_bf16 v[244:247], a[92:95], a[132:135], v[244:247]\n"
"ds_read_b128 a[132:135], v13 offset:24576\n"
"v_mfma_f32_16x16x32_bf16 v[244:247], a[88:91], a[128:131], v[244:247]\n"
"ds_read_b128 a[128:131], v12 offset:24576\n"
"v_mfma_f32_16x16x32_bf16 v[248:251], a[88:91], a[136:139], v[248:251]\n"
"ds_read_b64_tr_b16 a[54:55], v15 offset:12800\n"
"v_mfma_f32_16x16x32_bf16 v[248:251], a[92:95], a[140:143], v[248:251]\n"
"ds_read_b64_tr_b16 a[56:57], v15 offset:8200\n"
"v_mfma_f32_16x16x32_bf16 v[220:223], a[84:87], a[140:143], v[220:223]\n"
"s_add_u32 s67, s75, 786432\n"
"ds_read_b128 a[140:143], v13 offset:26624\n"
"v_mfma_f32_16x16x32_bf16 v[220:223], a[80:83], a[136:139], v[220:223]\n"
"ds_read_b128 a[136:139], v12 offset:26624\n"
"v_mfma_f32_16x16x32_bf16 v[224:227], a[80:83], a[144:147], v[224:227]\n"
"ds_read_b64_tr_b16 a[58:59], v15 offset:8712\n"
"v_mfma_f32_16x16x32_bf16 v[224:227], a[84:87], a[148:151], v[224:227]\n"
"ds_read_b64_tr_b16 a[60:61], v15 offset:12296\n"
"v_mfma_f32_16x16x32_bf16 v[252:255], a[92:95], a[148:151], v[252:255]\n"
"ds_read_b128 a[148:151], v13 offset:28672\n"
"v_mfma_f32_16x16x32_bf16 v[252:255], a[88:91], a[144:147], v[252:255]\n"
"ds_read_b128 a[144:147], v12 offset:28672\n"
"ds_read_b64_tr_b16 a[62:63], v15 offset:12808\n"
"v_mfma_f32_16x16x32_bf16 a[28:31], a[88:91], a[152:155], a[28:31]\n"
"v_mfma_f32_16x16x32_bf16 a[28:31], a[92:95], a[156:159], a[28:31]\n"
"v_mfma_f32_16x16x32_bf16 a[24:27], a[84:87], a[156:159], a[24:27]\n"
"ds_read_b128 a[156:159], v13 offset:30720\n"
"v_mfma_f32_16x16x32_bf16 a[24:27], a[80:83], a[152:155], a[24:27]\n"
"s_waitcnt lgkmcnt(15)\n"
"ds_read_b128 a[152:155], v12 offset:30720\n"
"v_mfma_f32_16x16x32_bf16 v[32:35], a[32:35], a[96:99], v[32:35]\n"
"v_mfma_f32_16x16x32_bf16 v[32:35], a[36:39], a[100:103], v[32:35]\n"
"s_waitcnt lgkmcnt(2)\n"
"v_mfma_f32_16x16x32_bf16 v[60:63], a[44:47], a[100:103], v[60:63]\n"
"L_st1591:\n"
"s_cbranch_execz L_st3865\n"
"buffer_load_dwordx4 v11, s[52:55], s67 offen offset:0  lds\n"
"v_mfma_f32_16x16x32_bf16 v[60:63], a[40:43], a[96:99], v[60:63]\n"
"v_mfma_f32_16x16x32_bf16 v[64:67], a[40:43], a[104:107], v[64:67]\n"
"s_add_u32 s67, s75, 801792\n"
"v_mfma_f32_16x16x32_bf16 v[64:67], a[44:47], a[108:111], v[64:67]\n"
"v_mfma_f32_16x16x32_bf16 v[36:39], a[36:39], a[108:111], v[36:39]\n"
"buffer_load_dwordx4 v11, s[52:55], s67 offen offset:1024  lds\n"
"v_mfma_f32_16x16x32_bf16 v[36:39], a[32:35], a[104:107], v[36:39]\n"
"v_mfma_f32_16x16x32_bf16 v[40:43], a[32:35], a[112:115], v[40:43]\n"
"s_add_u32 s67, s75, 817152\n"
"v_mfma_f32_16x16x32_bf16 v[40:43], a[36:39], a[116:119], v[40:43]\n"
"v_mfma_f32_16x16x32_bf16 v[68:71], a[44:47], a[116:119], v[68:71]\n"
"s_mov_b32 s30, 0\n"
"v_mfma_f32_16x16x32_bf16 v[68:71], a[40:43], a[112:115], v[68:71]\n"
"buffer_load_dwordx4 v11, s[52:55], s67 offen offset:2048  lds\n"
"v_mfma_f32_16x16x32_bf16 v[72:75], a[40:43], a[120:123], v[72:75]\n"
"s_mov_b32 s31, 0\n"
"v_mfma_f32_16x16x32_bf16 v[72:75], a[44:47], a[124:127], v[72:75]\n"
"s_add_u32 s67, s75, 832512\n"
"v_mfma_f32_16x16x32_bf16 v[44:47], a[36:39], a[124:127], v[44:47]\n"
"v_mfma_f32_16x16x32_bf16 v[44:47], a[32:35], a[120:123], v[44:47]\n"
"v_mfma_f32_16x16x32_bf16 v[48:51], a[32:35], a[128:131], v[48:51]\n"
"buffer_load_dwordx4 v11, s[52:55], s67 offen offset:3072  lds\n"
"v_mfma_f32_16x16x32_bf16 v[48:51], a[36:39], a[132:135], v[48:51]\n"
"s_add_u32 m0, s64, 12288\n"
"v_mfma_f32_16x16x32_bf16 v[76:79], a[44:47], a[132:135], v[76:79]\n"
"s_waitcnt vmcnt(11)\n"
"ds_read_b64_tr_b16 a[64:65], v9 offset:0\n"
"v_mfma_f32_16x16x32_bf16 v[76:79], a[40:43], a[128:131], v[76:79]\n"
"s_add_u32 s67, s75, 851968\n"
"ds_read_b64_tr_b16 a[66:67], v9 offset:512\n"
"v_mfma_f32_16x16x32_bf16 v[80:83], a[40:43], a[136:139], v[80:83]\n"
"ds_read_b64_tr_b16 a[68:69], v9 offset:4096\n"
"v_mfma_f32_16x16x32_bf16 v[80:83], a[44:47], a[140:143], v[80:83]\n"
"ds_read_b64_tr_b16 a[70:71], v9 offset:4608\n"
"buffer_load_dwordx4 v11, s[52:55], s67 offen offset:0  lds\n"
"v_mfma_f32_16x16x32_bf16 v[52:55], a[36:39], a[140:143], v[52:55]\n"
"s_add_u32 s67, s75, 867328\n"
"ds_read_b64_tr_b16 a[72:73], v9 offset:8\n"
"v_mfma_f32_16x16x32_bf16 v[52:55], a[32:35], a[136:139], v[52:55]\n"
"s_waitcnt lgkmcnt(5)\n"
"ds_read_b64_tr_b16 a[74:75], v9 offset:520\n"
"v_mfma_f32_16x16x32_bf16 v[56:59], a[32:35], a[144:147], v[56:59]\n"
"ds_read_b64_tr_b16 a[76:77], v9 offset:4104\n"
"v_mfma_f32_16x16x32_bf16 v[56:59], a[36:39], a[148:151], v[56:59]\n"
"ds_read_b64_tr_b16 a[78:79], v9 offset:4616\n"
"v_mfma_f32_16x16x32_bf16 v[84:87], a[44:47], a[148:151], v[84:87]\n"
"buffer_load_dwordx4 v11, s[52:55], s67 offen offset:1024  lds\n"
"v_mfma_f32_16x16x32_bf16 v[84:87], a[40:43], a[144:147], v[84:87]\n"
"ds_read_b64_tr_b16 a[80:81], v15 offset:0\n"
"v_mfma_f32_16x16x32_bf16 a[4:7], a[40:43], a[152:155], a[4:7]\n"
"s_add_u32 s67, s75, 882688\n"
"ds_read_b64_tr_b16 a[82:83], v15 offset:512\n"
"v_mfma_f32_16x16x32_bf16 a[4:7], a[44:47], a[156:159], a[4:7]\n"
"ds_read_b64_tr_b16 a[84:85], v15 offset:4096\n"
"buffer_load_dwordx4 v11, s[52:55], s67 offen offset:2048  lds\n"
"v_mfma_f32_16x16x32_bf16 a[0:3], a[36:39], a[156:159], a[0:3]\n"
"s_add_u32 s96, s56, %11\n"
"ds_read_b64_tr_b16 a[86:87], v15 offset:4608\n"
"s_addc_u32 s97, s57, 0\n"
"v_mfma_f32_16x16x32_bf16 a[0:3], a[32:35], a[152:155], a[0:3]\n"
"ds_read_b64_tr_b16 a[88:89], v15 offset:8\n"
"v_mfma_f32_16x16x32_bf16 v[88:91], a[48:51], a[96:99], v[88:91]\n"
"s_add_u32 s67, s75, 898048\n"
"ds_read_b64_tr_b16 a[90:91], v15 offset:520\n"
"v_mfma_f32_16x16x32_bf16 v[88:91], a[52:55], a[100:103], v[88:91]\n"
"ds_read_b64_tr_b16 a[92:93], v15 offset:4104\n"
"v_mfma_f32_16x16x32_bf16 v[116:119], a[60:63], a[100:103], v[116:119]\n"
"ds_read_b64_tr_b16 a[94:95], v15 offset:4616\n"
"v_mfma_f32_16x16x32_bf16 v[116:119], a[56:59], a[96:99], v[116:119]\n"
"buffer_load_dwordx4 v11, s[52:55], s67 offen offset:3072  lds\n"
"v_mfma_f32_16x16x32_bf16 v[120:123], a[56:59], a[104:107], v[120:123]\n"
"v_mfma_f32_16x16x32_bf16 v[120:123], a[60:63], a[108:111], v[120:123]\n"
"s_add_u32 m0, s64, 0\n"
"v_mfma_f32_16x16x32_bf16 v[92:95], a[52:55], a[108:111], v[92:95]\n"
"v_mfma_f32_16x16x32_bf16 v[92:95], a[48:51], a[104:107], v[92:95]\n"
"s_add_u32 s67, s75, 786560\n"
"v_mfma_f32_16x16x32_bf16 v[96:99], a[48:51], a[112:115], v[96:99]\n"
"s_waitcnt lgkmcnt(0)\n"
"v_mfma_f32_16x16x32_bf16 v[96:99], a[52:55], a[116:119], v[96:99]\n"
"buffer_load_dwordx4 v11, s[52:55], s67 offen offset:0  lds\n"
"v_mfma_f32_16x16x32_bf16 v[124:127], a[60:63], a[116:119], v[124:127]\n"
"s_load_dword s98, s[96:97], 512 glc\n"
"v_mfma_f32_16x16x32_bf16 v[124:127], a[56:59], a[112:115], v[124:127]\n"
"s_load_dword s99, s[96:97], 8704 glc\n"
"s_add_u32 s67, s75, 801920\n"
"v_mfma_f32_16x16x32_bf16 v[128:131], a[56:59], a[120:123], v[128:131]\n"
"v_mfma_f32_16x16x32_bf16 v[128:131], a[60:63], a[124:127], v[128:131]\n"
"buffer_load_dwordx4 v11, s[52:55], s67 offen offset:1024  lds\n"
"v_mfma_f32_16x16x32_bf16 v[100:103], a[52:55], a[124:127], v[100:103]\n"
"v_mfma_f32_16x16x32_bf16 v[100:103], a[48:51], a[120:123], v[100:103]\n"
"s_add_u32 s67, s75, 817280\n"
"v_mfma_f32_16x16x32_bf16 v[104:107], a[48:51], a[128:131], v[104:107]\n"
"v_mfma_f32_16x16x32_bf16 v[104:107], a[52:55], a[132:135], v[104:107]\n"
"v_mfma_f32_16x16x32_bf16 v[132:135], a[60:63], a[132:135], v[132:135]\n"
"buffer_load_dwordx4 v11, s[52:55], s67 offen offset:2048  lds\n"
"v_mfma_f32_16x16x32_bf16 v[132:135], a[56:59], a[128:131], v[132:135]\n"
"v_mfma_f32_16x16x32_bf16 v[136:139], a[56:59], a[136:139], v[136:139]\n"
"s_add_u32 s67, s75, 832640\n"
"v_mfma_f32_16x16x32_bf16 v[136:139], a[60:63], a[140:143], v[136:139]\n"
"v_mfma_f32_16x16x32_bf16 v[108:111], a[52:55], a[140:143], v[108:111]\n"
"buffer_load_dwordx4 v11, s[52:55], s67 offen offset:3072  lds\n"
"v_mfma_f32_16x16x32_bf16 v[108:111], a[48:51], a[136:139], v[108:111]\n"
"v_mfma_f32_16x16x32_bf16 v[112:115], a[48:51], a[144:147], v[112:115]\n"
"s_add_u32 m0, s64, 4096\n"
"v_mfma_f32_16x16x32_bf16 v[112:115], a[52:55], a[148:151], v[112:115]\n"
"v_mfma_f32_16x16x32_bf16 v[140:143], a[60:63], a[148:151], v[140:143]\n"
"s_add_u32 s67, s75, 852096\n"
"v_mfma_f32_16x16x32_bf16 v[140:143], a[56:59], a[144:147], v[140:143]\n"
"v_mfma_f32_16x16x32_bf16 a[12:15], a[56:59], a[152:155], a[12:15]\n"
"buffer_load_dwordx4 v11, s[52:55], s67 offen offset:0  lds\n"
"v_mfma_f32_16x16x32_bf16 a[12:15], a[60:63], a[156:159], a[12:15]\n"
"v_mfma_f32_16x16x32_bf16 a[8:11], a[52:55], a[156:159], a[8:11]\n"
"s_add_u32 s67, s75, 867456\n"
"v_mfma_f32_16x16x32_bf16 a[8:11], a[48:51], a[152:155], a[8:11]\n"
"v_mfma_f32_16x16x32_bf16 v[144:147], a[64:67], a[96:99], v[144:147]\n"
"buffer_load_dwordx4 v11, s[52:55], s67 offen offset:1024  lds\n"
"v_mfma_f32_16x16x32_bf16 v[144:147], a[68:71], a[100:103], v[144:147]\n"
"v_mfma_f32_16x16x32_bf16 v[172:175], a[76:79], a[100:103], v[172:175]\n"
"s_add_u32 s67, s75, 882816\n"
"v_mfma_f32_16x16x32_bf16 v[172:175], a[72:75], a[96:99], v[172:175]\n"
"v_mfma_f32_16x16x32_bf16 v[176:179], a[72:75], a[104:107], v[176:179]\n"
"buffer_load_dwordx4 v11, s[52:55], s67 offen offset:2048  lds\n"
"v_mfma_f32_16x16x32_bf16 v[176:179], a[76:79], a[108:111], v[176:179]\n"
"v_mfma_f32_16x16x32_bf16 v[148:151], a[68:71], a[108:111], v[148:151]\n"
"s_add_u32 s67, s75, 898176\n"
"v_mfma_f32_16x16x32_bf16 v[148:151], a[64:67], a[104:107], v[148:151]\n"
"v_mfma_f32_16x16x32_bf16 v[152:155], a[64:67], a[112:115], v[152:155]\n"
"s_sub_u32 s66, 81920, s73\n"
"v_mfma_f32_16x16x32_bf16 v[152:155], a[68:71], a[116:119], v[152:155]\n"
"buffer_load_dwordx4 v11, s[52:55], s67 offen offset:3072  lds\n"
"v_mfma_f32_16x16x32_bf16 v[180:183], a[76:79], a[116:119], v[180:183]\n"
"s_add_u32 m0, s72, 139264\n"
"v_add_u32 v12, s66, v7\n"
"v_mfma_f32_16x16x32_bf16 v[180:183], a[72:75], a[112:115], v[180:183]\n"
"v_mfma_f32_16x16x32_bf16 v[184:187], a[72:75], a[120:123], v[184:187]\n"
"s_add_u32 s67, s60, 896\n"
"v_add_u32 v13, s66, v8\n"
"v_mfma_f32_16x16x32_bf16 v[184:187], a[76:79], a[124:127], v[184:187]\n"
"v_mfma_f32_16x16x32_bf16 v[156:159], a[68:71], a[124:127], v[156:159]\n"
"buffer_load_dwordx4 v6, s[48:51], s67 offen offset:0  lds\n"
"v_mfma_f32_16x16x32_bf16 v[156:159], a[64:67], a[120:123], v[156:159]\n"
"v_mfma_f32_16x16x32_bf16 v[160:163], a[64:67], a[128:131], v[160:163]\n"
"s_add_u32 s67, s60, 16256\n"
"v_mfma_f32_16x16x32_bf16 v[160:163], a[68:71], a[132:135], v[160:163]\n"
"v_mfma_f32_16x16x32_bf16 v[188:191], a[76:79], a[132:135], v[188:191]\n"
"buffer_load_dwordx4 v6, s[48:51], s67 offen offset:1024  lds\n"
"v_mfma_f32_16x16x32_bf16 v[188:191], a[72:75], a[128:131], v[188:191]\n"
"v_mfma_f32_16x16x32_bf16 v[192:195], a[72:75], a[136:139], v[192:195]\n"
"s_add_u32 s67, s60, 31616\n"
"v_mfma_f32_16x16x32_bf16 v[192:195], a[76:79], a[140:143], v[192:195]\n"
"v_mfma_f32_16x16x32_bf16 v[164:167], a[68:71], a[140:143], v[164:167]\n"
"v_mfma_f32_16x16x32_bf16 v[164:167], a[64:67], a[136:139], v[164:167]\n"
"buffer_load_dwordx4 v6, s[48:51], s67 offen offset:2048  lds\n"
"v_mfma_f32_16x16x32_bf16 v[168:171], a[64:67], a[144:147], v[168:171]\n"
"v_mfma_f32_16x16x32_bf16 v[168:171], a[68:71], a[148:151], v[168:171]\n"
"s_add_u32 s67, s60, 46976\n"
"v_mfma_f32_16x16x32_bf16 v[196:199], a[76:79], a[148:151], v[196:199]\n"
"v_mfma_f32_16x16x32_bf16 v[196:199], a[72:75], a[144:147], v[196:199]\n"
"buffer_load_dwordx4 v6, s[48:51], s67 offen offset:3072  lds\n"
"v_mfma_f32_16x16x32_bf16 a[20:23], a[72:75], a[152:155], a[20:23]\n"
"v_mfma_f32_16x16x32_bf16 a[20:23], a[76:79], a[156:159], a[20:23]\n"
"s_waitcnt lgkmcnt(0)\n"
"v_mfma_f32_16x16x32_bf16 a[16:19], a[68:71], a[156:159], a[16:19]\n"
"v_mfma_f32_16x16x32_bf16 a[16:19], a[64:67], a[152:155], a[16:19]\n"
"s_waitcnt vmcnt(23)\n"
"s_barrier\n"
"v_mfma_f32_16x16x32_bf16 v[200:203], a[80:83], a[96:99], v[200:203]\n"
"s_waitcnt vmcnt(12)\n"
"ds_read_b64_tr_b16 a[32:33], v9 offset:8192\n"
"v_mfma_f32_16x16x32_bf16 v[200:203], a[84:87], a[100:103], v[200:203]\n"
"ds_read_b64_tr_b16 a[34:35], v9 offset:8704\n"
"v_mfma_f32_16x16x32_bf16 v[228:231], a[92:95], a[100:103], v[228:231]\n"
"s_add_u32 m0, s64, 8192\n"
"ds_read_b128 a[100:103], v13 offset:16384\n"
"v_mfma_f32_16x16x32_bf16 v[228:231], a[88:91], a[96:99], v[228:231]\n"
"ds_read_b128 a[96:99], v12 offset:16384\n"
"v_mfma_f32_16x16x32_bf16 v[232:235], a[88:91], a[104:107], v[232:235]\n"
"ds_read_b64_tr_b16 a[36:37], v9 offset:12288\n"
"v_mfma_f32_16x16x32_bf16 v[232:235], a[92:95], a[108:111], v[232:235]\n"
"ds_read_b64_tr_b16 a[38:39], v9 offset:12800\n"
"v_mfma_f32_16x16x32_bf16 v[204:207], a[84:87], a[108:111], v[204:207]\n"
"ds_read_b128 a[108:111], v13 offset:18432\n"
"v_mfma_f32_16x16x32_bf16 v[204:207], a[80:83], a[104:107], v[204:207]\n"
"ds_read_b128 a[104:107], v12 offset:18432\n"
"ds_read_b64_tr_b16 a[40:41], v9 offset:8200\n"
"v_mfma_f32_16x16x32_bf16 v[208:211], a[80:83], a[112:115], v[208:211]\n"
"ds_read_b64_tr_b16 a[42:43], v9 offset:8712\n"
"v_mfma_f32_16x16x32_bf16 v[208:211], a[84:87], a[116:119], v[208:211]\n"
"ds_read_b64_tr_b16 a[44:45], v9 offset:12296\n"
"v_mfma_f32_16x16x32_bf16 v[236:239], a[92:95], a[116:119], v[236:239]\n"
"ds_read_b128 a[116:119], v13 offset:20480\n"
"v_mfma_f32_16x16x32_bf16 v[236:239], a[88:91], a[112:115], v[236:239]\n"
"ds_read_b128 a[112:115], v12 offset:20480\n"
"v_mfma_f32_16x16x32_bf16 v[240:243], a[88:91], a[120:123], v[240:243]\n"
"ds_read_b64_tr_b16 a[46:47], v9 offset:12808\n"
"v_mfma_f32_16x16x32_bf16 v[240:243], a[92:95], a[124:127], v[240:243]\n"
"ds_read_b64_tr_b16 a[48:49], v15 offset:8192\n"
"v_mfma_f32_16x16x32_bf16 v[212:215], a[84:87], a[124:127], v[212:215]\n"
"ds_read_b128 a[124:127], v13 offset:22528\n"
"v_mfma_f32_16x16x32_bf16 v[212:215], a[80:83], a[120:123], v[212:215]\n"
"ds_read_b128 a[120:123], v12 offset:22528\n"
"v_mfma_f32_16x16x32_bf16 v[216:219], a[80:83], a[128:131], v[216:219]\n"
"s_add_u32 s67, s75, 917504\n"
"ds_read_b64_tr_b16 a[50:51], v15 offset:8704\n"
"v_mfma_f32_16x16x32_bf16 v[216:219], a[84:87], a[132:135], v[216:219]\n"
"ds_read_b64_tr_b16 a[52:53], v15 offset:12288\n"
"v_mfma_f32_16x16x32_bf16 v[244:247], a[92:95], a[132:135], v[244:247]\n"
"ds_read_b128 a[132:135], v13 offset:24576\n"
"v_mfma_f32_16x16x32_bf16 v[244:247], a[88:91], a[128:131], v[244:247]\n"
"ds_read_b128 a[128:131], v12 offset:24576\n"
"v_mfma_f32_16x16x32_bf16 v[248:251], a[88:91], a[136:139], v[248:251]\n"
"ds_read_b64_tr_b16 a[54:55], v15 offset:12800\n"
"v_mfma_f32_16x16x32_bf16 v[248:251], a[92:95], a[140:143], v[248:251]\n"
"ds_read_b64_tr_b16 a[56:57], v15 offset:8200\n"
"v_mfma_f32_16x16x32_bf16 v[220:223], a[84:87], a[140:143], v[220:223]\n"
"ds_read_b128 a[140:143], v13 offset:26624\n"
"v_mfma_f32_16x16x32_bf16 v[220:223], a[80:83], a[136:139], v[220:223]\n"
"ds_read_b128 a[136:139], v12 offset:26624\n"
"v_mfma_f32_16x16x32_bf16 v[224:227], a[80:83], a[144:147], v[224:227]\n"
"ds_read_b64_tr_b16 a[58:59], v15 offset:8712\n"
"v_mfma_f32_16x16x32_bf16 v[224:227], a[84:87], a[148:151], v[224:227]\n"
"ds_read_b64_tr_b16 a[60:61], v15 offset:12296\n"
"v_mfma_f32_16x16x32_bf16 v[252:255], a[92:95], a[148:151], v[252:255]\n"
"ds_read_b128 a[148:151], v13 offset:28672\n"
"v_mfma_f32_16x16x32_bf16 v[252:255], a[88:91], a[144:147], v[252:255]\n"
"ds_read_b128 a[144:147], v12 offset:28672\n"
"ds_read_b64_tr_b16 a[62:63], v15 offset:12808\n"
"v_mfma_f32_16x16x32_bf16 a[28:31], a[88:91], a[152:155], a[28:31]\n"
"v_mfma_f32_16x16x32_bf16 a[28:31], a[92:95], a[156:159], a[28:31]\n"
"v_mfma_f32_16x16x32_bf16 a[24:27], a[84:87], a[156:159], a[24:27]\n"
"ds_read_b128 a[156:159], v13 offset:30720\n"
"v_mfma_f32_16x16x32_bf16 a[24:27], a[80:83], a[152:155], a[24:27]\n"
"s_waitcnt lgkmcnt(15)\n"
"ds_read_b128 a[152:155], v12 offset:30720\n"
"v_mfma_f32_16x16x32_bf16 v[32:35], a[32:35], a[96:99], v[32:35]\n"
"v_mfma_f32_16x16x32_bf16 v[32:35], a[36:39], a[100:103], v[32:35]\n"
"s_waitcnt lgkmcnt(2)\n"
"L_H6S:\n"
"v_mfma_f32_16x16x32_bf16 v[60:63], a[44:47], a[100:103], v[60:63]\n"
"buffer_load_dwordx4 v11, s[52:55], s67 offen offset:0  lds\n"
"v_mfma_f32_16x16x32_bf16 v[60:63], a[40:43], a[96:99], v[60:63]\n"
"v_mfma_f32_16x16x32_bf16 v[64:67], a[40:43], a[104:107], v[64:67]\n"
"s_add_u32 s67, s75, 932864\n"
"v_mfma_f32_16x16x32_bf16 v[64:67], a[44:47], a[108:111], v[64:67]\n"
"v_mfma_f32_16x16x32_bf16 v[36:39], a[36:39], a[108:111], v[36:39]\n"
"buffer_load_dwordx4 v11, s[52:55], s67 offen offset:1024  lds\n"
"v_mfma_f32_16x16x32_bf16 v[36:39], a[32:35], a[104:107], v[36:39]\n"
"v_mfma_f32_16x16x32_bf16 v[40:43], a[32:35], a[112:115], v[40:43]\n"
"s_add_u32 s67, s75, 948224\n"
"v_mfma_f32_16x16x32_bf16 v[40:43], a[36:39], a[116:119], v[40:43]\n"
"v_mfma_f32_16x16x32_bf16 v[68:71], a[44:47], a[116:119], v[68:71]\n"
"v_mfma_f32_16x16x32_bf16 v[68:71], a[40:43], a[112:115], v[68:71]\n"
"buffer_load_dwordx4 v11, s[52:55], s67 offen offset:2048  lds\n"
"v_mfma_f32_16x16x32_bf16 v[72:75], a[40:43], a[120:123], v[72:75]\n"
"v_mfma_f32_16x16x32_bf16 v[72:75], a[44:47], a[124:127], v[72:75]\n"
"s_add_u32 s67, s75, 963584\n"
"v_mfma_f32_16x16x32_bf16 v[44:47], a[36:39], a[124:127], v[44:47]\n"
"v_mfma_f32_16x16x32_bf16 v[44:47], a[32:35], a[120:123], v[44:47]\n"
"v_mfma_f32_16x16x32_bf16 v[48:51], a[32:35], a[128:131], v[48:51]\n"
"buffer_load_dwordx4 v11, s[52:55], s67 offen offset:3072  lds\n"
"v_mfma_f32_16x16x32_bf16 v[48:51], a[36:39], a[132:135], v[48:51]\n"
"s_add_u32 m0, s64, 12288\n"
"v_mfma_f32_16x16x32_bf16 v[76:79], a[44:47], a[132:135], v[76:79]\n"
"s_waitcnt vmcnt(8)\n"
"ds_read_b64_tr_b16 a[64:65], v9 offset:0\n"
"v_mfma_f32_16x16x32_bf16 v[76:79], a[40:43], a[128:131], v[76:79]\n"
"s_add_u32 s67, s75, 983040\n"
"ds_read_b64_tr_b16 a[66:67], v9 offset:512\n"
"v_mfma_f32_16x16x32_bf16 v[80:83], a[40:43], a[136:139], v[80:83]\n"
"ds_read_b64_tr_b16 a[68:69], v9 offset:4096\n"
"v_mfma_f32_16x16x32_bf16 v[80:83], a[44:47], a[140:143], v[80:83]\n"
"ds_read_b64_tr_b16 a[70:71], v9 offset:4608\n"
"buffer_load_dwordx4 v11, s[52:55], s67 offen offset:0  lds\n"
"v_mfma_f32_16x16x32_bf16 v[52:55], a[36:39], a[140:143], v[52:55]\n"
"s_add_u32 s67, s75, 998400\n"
"ds_read_b64_tr_b16 a[72:73], v9 offset:8\n"
"v_mfma_f32_16x16x32_bf16 v[52:55], a[32:35], a[136:139], v[52:55]\n"
"s_waitcnt lgkmcnt(5)\n"
"ds_read_b64_tr_b16 a[74:75], v9 offset:520\n"
"v_mfma_f32_16x16x32_bf16 v[56:59], a[32:35], a[144:147], v[56:59]\n"
"ds_read_b64_tr_b16 a[76:77], v9 offset:4104\n"
"v_mfma_f32_16x16x32_bf16 v[56:59], a[36:39], a[148:151], v[56:59]\n"
"ds_read_b64_tr_b16 a[78:79], v9 offset:4616\n"
"v_mfma_f32_16x16x32_bf16 v[84:87], a[44:47], a[148:151], v[84:87]\n"
"buffer_load_dwordx4 v11, s[52:55], s67 offen offset:1024  lds\n"
"v_mfma_f32_16x16x32_bf16 v[84:87], a[40:43], a[144:147], v[84:87]\n"
"ds_read_b64_tr_b16 a[80:81], v15 offset:0\n"
"v_mfma_f32_16x16x32_bf16 a[4:7], a[40:43], a[152:155], a[4:7]\n"
"s_add_u32 s67, s75, 1013760\n"
"ds_read_b64_tr_b16 a[82:83], v15 offset:512\n"
"v_mfma_f32_16x16x32_bf16 a[4:7], a[44:47], a[156:159], a[4:7]\n"
"ds_read_b64_tr_b16 a[84:85], v15 offset:4096\n"
"v_mfma_f32_16x16x32_bf16 a[0:3], a[36:39], a[156:159], a[0:3]\n"
"ds_read_b64_tr_b16 a[86:87], v15 offset:4608\n"
"buffer_load_dwordx4 v11, s[52:55], s67 offen offset:2048  lds\n"
"v_mfma_f32_16x16x32_bf16 a[0:3], a[32:35], a[152:155], a[0:3]\n"
"ds_read_b64_tr_b16 a[88:89], v15 offset:8\n"
"v_mfma_f32_16x16x32_bf16 v[88:91], a[48:51], a[96:99], v[88:91]\n"
"s_add_u32 s67, s75, 1029120\n"
"ds_read_b64_tr_b16 a[90:91], v15 offset:520\n"
"v_mfma_f32_16x16x32_bf16 v[88:91], a[52:55], a[100:103], v[88:91]\n"
"ds_read_b64_tr_b16 a[92:93], v15 offset:4104\n"
"v_mfma_f32_16x16x32_bf16 v[116:119], a[60:63], a[100:103], v[116:119]\n"
"ds_read_b64_tr_b16 a[94:95], v15 offset:4616\n"
"v_mfma_f32_16x16x32_bf16 v[116:119], a[56:59], a[96:99], v[116:119]\n"
"buffer_load_dwordx4 v11, s[52:55], s67 offen offset:3072  lds\n"
"v_mfma_f32_16x16x32_bf16 v[120:123], a[56:59], a[104:107], v[120:123]\n"
"v_mfma_f32_16x16x32_bf16 v[120:123], a[60:63], a[108:111], v[120:123]\n"
"s_add_u32 m0, s64, 0\n"
"v_mfma_f32_16x16x32_bf16 v[92:95], a[52:55], a[108:111], v[92:95]\n"
"v_mfma_f32_16x16x32_bf16 v[92:95], a[48:51], a[104:107], v[92:95]\n"
"s_add_u32 s67, s75, 917632\n"
"v_mfma_f32_16x16x32_bf16 v[96:99], a[48:51], a[112:115], v[96:99]\n"
"s_waitcnt lgkmcnt(0)\n"
"s_mov_b32 s99, 0\n"
"v_mfma_f32_16x16x32_bf16 v[96:99], a[52:55], a[116:119], v[96:99]\n"
"s_mov_b32 s98, 0\n"
"buffer_load_dwordx4 v11, s[52:55], s67 offen offset:0  lds\n"
"v_mfma_f32_16x16x32_bf16 v[124:127], a[60:63], a[116:119], v[124:127]\n"
"s_memtime s[76:77]\n"
"s_load_dword s98, s[96:97], 8192 glc\n"
"v_mfma_f32_16x16x32_bf16 v[124:127], a[56:59], a[112:115], v[124:127]\n"
"s_add_u32 s67, s75, 932992\n"
"v_mfma_f32_16x16x32_bf16 v[128:131], a[56:59], a[120:123], v[128:131]\n"
"v_mfma_f32_16x16x32_bf16 v[128:131], a[60:63], a[124:127], v[128:131]\n"
"buffer_load_dwordx4 v11, s[52:55], s67 offen offset:1024  lds\n"
"v_mfma_f32_16x16x32_bf16 v[100:103], a[52:55], a[124:127], v[100:103]\n"
"v_mfma_f32_16x16x32_bf16 v[100:103], a[48:51], a[120:123], v[100:103]\n"
"s_add_u32 s67, s75, 948352\n"
"v_mfma_f32_16x16x32_bf16 v[104:107], a[48:51], a[128:131], v[104:107]\n"
"v_mfma_f32_16x16x32_bf16 v[104:107], a[52:55], a[132:135], v[104:107]\n"
"v_mfma_f32_16x16x32_bf16 v[132:135], a[60:63], a[132:135], v[132:135]\n"
"buffer_load_dwordx4 v11, s[52:55], s67 offen offset:2048  lds\n"
"v_mfma_f32_16x16x32_bf16 v[132:135], a[56:59], a[128:131], v[132:135]\n"
"s_mov_b32 s29, s76\n"
"v_mfma_f32_16x16x32_bf16 v[136:139], a[56:59], a[136:139], v[136:139]\n"
"s_add_u32 s67, s75, 963712\n"
"v_mfma_f32_16x16x32_bf16 v[136:139], a[60:63], a[140:143], v[136:139]\n"
"buffer_load_dwordx4 v11, s[52:55], s67 offen offset:3072  lds\n"
"v_mfma_f32_16x16x32_bf16 v[108:111], a[52:55], a[140:143], v[108:111]\n"
"s_sub_u32 s99, s99, s98\n"
"s_sub_u32 s99, s99, s98\n"
"v_mfma_f32_16x16x32_bf16 v[108:111], a[48:51], a[136:139], v[108:111]\n"
"s_sub_u32 s99, s99, s98\n"
"s_add_u32 m0, s64, 4096\n"
"v_mfma_f32_16x16x32_bf16 v[112:115], a[48:51], a[144:147], v[112:115]\n"
"s_sub_u32 s99, s99, s98\n"
"v_mfma_f32_16x16x32_bf16 v[112:115], a[52:55], a[148:151], v[112:115]\n"
"s_add_u32 s67, s75, 983168\n"
"v_mfma_f32_16x16x32_bf16 v[140:143], a[60:63], a[148:151], v[140:143]\n"
"s_sub_u32 s99, s99, s98\n"
"s_sub_u32 s99, s99, s98\n"
"v_mfma_f32_16x16x32_bf16 v[140:143], a[56:59], a[144:147], v[140:143]\n"
"s_sub_u32 s99, s99, s98\n"
"buffer_load_dwordx4 v11, s[52:55], s67 offen offset:0  lds\n"
"v_mfma_f32_16x16x32_bf16 a[12:15], a[56:59], a[152:155], a[12:15]\n"
"s_sub_u32 s99, s99, s98\n"
"v_mfma_f32_16x16x32_bf16 a[12:15], a[60:63], a[156:159], a[12:15]\n"
"s_add_u32 s67, s75, 998528\n"
"v_mfma_f32_16x16x32_bf16 a[8:11], a[52:55], a[156:159], a[8:11]\n"
"s_sub_u32 s99, s99, s98\n"
"s_sub_u32 s99, s99, s98\n"
"v_mfma_f32_16x16x32_bf16 a[8:11], a[48:51], a[152:155], a[8:11]\n"
"s_sub_u32 s99, s99, s98\n"
"v_mfma_f32_16x16x32_bf16 v[144:147], a[64:67], a[96:99], v[144:147]\n"
"s_sub_u32 s99, s99, s98\n"
"v_mfma_f32_16x16x32_bf16 v[144:147], a[68:71], a[100:103], v[144:147]\n"
"buffer_load_dwordx4 v11, s[52:55], s67 offen offset:1024  lds\n"
"v_mfma_f32_16x16x32_bf16 v[172:175], a[76:79], a[100:103], v[172:175]\n"
"s_sub_u32 s99, s99, s98\n"
"s_sub_u32 s99, s99, s98\n"
"v_mfma_f32_16x16x32_bf16 v[172:175], a[72:75], a[96:99], v[172:175]\n"
"s_sub_u32 s99, s99, s98\n"
"s_add_u32 s67, s75, 1013888\n"
"v_mfma_f32_16x16x32_bf16 v[176:179], a[72:75], a[104:107], v[176:179]\n"
"s_sub_u32 s99, s99, s98\n"
"v_mfma_f32_16x16x32_bf16 v[176:179], a[76:79], a[108:111], v[176:179]\n"
"buffer_load_dwordx4 v11, s[52:55], s67 offen offset:2048  lds\n"
"v_mfma_f32_16x16x32_bf16 v[148:151], a[68:71], a[108:111], v[148:151]\n"
"s_sub_u32 s99, s99, s98\n"
"s_sub_u32 s99, s99, s98\n"
"v_mfma_f32_16x16x32_bf16 v[148:151], a[64:67], a[104:107], v[148:151]\n"
"s_sub_u32 s99, s99, s98\n"
"s_add_u32 s67, s75, 1029248\n"
"v_mfma_f32_16x16x32_bf16 v[152:155], a[64:67], a[112:115], v[152:155]\n"
"s_sub_u32 s66, 122880, s73\n"
"s_sub_u32 s99, s99, s98\n"
"v_mfma_f32_16x16x32_bf16 v[152:155], a[68:71], a[116:119], v[152:155]\n"
"buffer_load_dwordx4 v11, s[52:55], s67 offen offset:3072  lds\n"
"v_mfma_f32_16x16x32_bf16 v[180:183], a[76:79], a[116:119], v[180:183]\n"
"s_sub_u32 s99, s99, s98\n"
"v_add_u32 v12, s66, v7\n"
"s_sub_u32 s99, s99, s98\n"
"v_mfma_f32_16x16x32_bf16 v[180:183], a[72:75], a[112:115], v[180:183]\n"
"s_sub_u32 s99, s99, s98\n"
"s_cmp_lt_u32 %16, 2\n"
"s_cselect_b32 s67, 16384, 0\n"
"v_mfma_f32_16x16x32_bf16 v[184:187], a[72:75], a[120:123], v[184:187]\n"
"s_sub_u32 s99, s99, s98\n"
"v_add_u32 v13, s66, v8\n"
"s_add_u32 s67, s67, s72\n"
"v_mfma_f32_16x16x32_bf16 v[184:187], a[76:79], a[124:127], v[184:187]\n"
"s_memtime s[76:77]\n"
"s_add_u32 m0, s67, 16384\n"
"s_sub_u32 s67, 1024, s60\n"
"v_mfma_f32_16x16x32_bf16 v[156:159], a[68:71], a[124:127], v[156:159]\n"
"buffer_load_dwordx4 v6, s[48:51], s67 offen offset:0  lds\n"
"v_mfma_f32_16x16x32_bf16 v[156:159], a[64:67], a[120:123], v[156:159]\n"
"s_waitcnt lgkmcnt(0)\n"
"v_mfma_f32_16x16x32_bf16 v[160:163], a[64:67], a[128:131], v[160:163]\n"
"s_sub_u32 s29, s76, s29\n"
"s_sub_u32 s67, 16384, s60\n"
"v_mfma_f32_16x16x32_bf16 v[160:163], a[68:71], a[132:135], v[160:163]\n"
"s_cmp_lt_u32 s29, 620\n"
"s_cselect_b32 s99, s99, 0\n"
"v_mfma_f32_16x16x32_bf16 v[188:191], a[76:79], a[132:135], v[188:191]\n"
"s_max_u32 s31, s31, s99\n"
"v_mfma_f32_16x16x32_bf16 v[188:191], a[72:75], a[128:131], v[188:191]\n"
"buffer_load_dwordx4 v6, s[48:51], s67 offen offset:1024  lds\n"
"v_mfma_f32_16x16x32_bf16 v[192:195], a[72:75], a[136:139], v[192:195]\n"
"v_mfma_f32_16x16x32_bf16 v[192:195], a[76:79], a[140:143], v[192:195]\n"
"s_sub_u32 s67, 31744, s60\n"
"v_mfma_f32_16x16x32_bf16 v[164:167], a[68:71], a[140:143], v[164:167]\n"
"v_mfma_f32_16x16x32_bf16 v[164:167], a[64:67], a[136:139], v[164:167]\n"
"buffer_load_dwordx4 v6, s[48:51], s67 offen offset:2048  lds\n"
"v_mfma_f32_16x16x32_bf16 v[168:171], a[64:67], a[144:147], v[168:171]\n"
"v_mfma_f32_16x16x32_bf16 v[168:171], a[68:71], a[148:151], v[168:171]\n"
"s_sub_u32 s67, 47104, s60\n"
"v_mfma_f32_16x16x32_bf16 v[196:199], a[76:79], a[148:151], v[196:199]\n"
"v_mfma_f32_16x16x32_bf16 v[196:199], a[72:75], a[144:147], v[196:199]\n"
"buffer_load_dwordx4 v6, s[48:51], s67 offen offset:3072  lds\n"
"v_mfma_f32_16x16x32_bf16 a[20:23], a[72:75], a[152:155], a[20:23]\n"
"v_mfma_f32_16x16x32_bf16 a[20:23], a[76:79], a[156:159], a[20:23]\n"
"s_add_u32 m0, s72, 57216\n"
"v_mfma_f32_16x16x32_bf16 a[16:19], a[68:71], a[156:159], a[16:19]\n"
"v_mfma_f32_16x16x32_bf16 a[16:19], a[64:67], a[152:155], a[16:19]\n"
"s_waitcnt vmcnt(20)\n"
"s_barrier\n"
"v_mfma_f32_16x16x32_bf16 v[200:203], a[80:83], a[96:99], v[200:203]\n"
"s_waitcnt vmcnt(12)\n"
"ds_read_b64_tr_b16 a[32:33], v9 offset:8192\n"
"buffer_load_dwordx4 v6, s[40:43], s80 offen offset:128  lds\n"
"v_mfma_f32_16x16x32_bf16 v[200:203], a[84:87], a[100:103], v[200:203]\n"
"ds_read_b64_tr_b16 a[34:35], v9 offset:8704\n"
"v_mfma_f32_16x16x32_bf16 v[228:231], a[92:95], a[100:103], v[228:231]\n"
"ds_read_b128 a[100:103], v13 offset:16384\n"
"v_mfma_f32_16x16x32_bf16 v[228:231], a[88:91], a[96:99], v[228:231]\n"
"ds_read_b128 a[96:99], v12 offset:16384\n"
"buffer_load_dwordx4 v6, s[40:43], s81 offen offset:1152  lds\n"
"v_mfma_f32_16x16x32_bf16 v[232:235], a[88:91], a[104:107], v[232:235]\n"
"ds_read_b64_tr_b16 a[36:37], v9 offset:12288\n"
"v_mfma_f32_16x16x32_bf16 v[232:235], a[92:95], a[108:111], v[232:235]\n"
"ds_read_b64_tr_b16 a[38:39], v9 offset:12800\n"
"v_mfma_f32_16x16x32_bf16 v[204:207], a[84:87], a[108:111], v[204:207]\n"
"ds_read_b128 a[108:111], v13 offset:18432\n"
"v_mfma_f32_16x16x32_bf16 v[204:207], a[80:83], a[104:107], v[204:207]\n"
"ds_read_b128 a[104:107], v12 offset:18432\n"
"ds_read_b64_tr_b16 a[40:41], v9 offset:8200\n"
"v_mfma_f32_16x16x32_bf16 v[208:211], a[80:83], a[112:115], v[208:211]\n"
"ds_read_b64_tr_b16 a[42:43], v9 offset:8712\n"
"buffer_load_dwordx4 v6, s[40:43], s82 offen offset:2176  lds\n"
"v_mfma_f32_16x16x32_bf16 v[208:211], a[84:87], a[116:119], v[208:211]\n"
"ds_read_b64_tr_b16 a[44:45], v9 offset:12296\n"
"v_mfma_f32_16x16x32_bf16 v[236:239], a[92:95], a[116:119], v[236:239]\n"
"ds_read_b128 a[116:119], v13 offset:20480\n"
"v_mfma_f32_16x16x32_bf16 v[236:239], a[88:91], a[112:115], v[236:239]\n"
"ds_read_b128 a[112:115], v12 offset:20480\n"
"v_mfma_f32_16x16x32_bf16 v[240:243], a[88:91], a[120:123], v[240:243]\n"
"ds_read_b64_tr_b16 a[46:47], v9 offset:12808\n"
"v_mfma_f32_16x16x32_bf16 v[240:243], a[92:95], a[124:127], v[240:243]\n"
"ds_read_b64_tr_b16 a[48:49], v15 offset:8192\n"
"buffer_load_dwordx4 v6, s[40:43], s83 offen offset:3200  lds\n"
"v_mfma_f32_16x16x32_bf16 v[212:215], a[84:87], a[124:127], v[212:215]\n"
"ds_read_b128 a[124:127], v13 offset:22528\n"
"v_mfma_f32_16x16x32_bf16 v[212:215], a[80:83], a[120:123], v[212:215]\n"
"ds_read_b128 a[120:123], v12 offset:22528\n"
"v_mfma_f32_16x16x32_bf16 v[216:219], a[80:83], a[128:131], v[216:219]\n"
"ds_read_b64_tr_b16 a[50:51], v15 offset:8704\n"
"v_mfma_f32_16x16x32_bf16 v[216:219], a[84:87], a[132:135], v[216:219]\n"
"s_add_u32 m0, s64, 8192\n"
"ds_read_b64_tr_b16 a[52:53], v15 offset:12288\n"
"v_mfma_f32_16x16x32_bf16 v[244:247], a[92:95], a[132:135], v[244:247]\n"
"ds_read_b128 a[132:135], v13 offset:24576\n"
"v_mfma_f32_16x16x32_bf16 v[244:247], a[88:91], a[128:131], v[244:247]\n"
"ds_read_b128 a[128:131], v12 offset:24576\n"
"v_mfma_f32_16x16x32_bf16 v[248:251], a[88:91], a[136:139], v[248:251]\n"
"ds_read_b64_tr_b16 a[54:55], v15 offset:12800\n"
"v_mfma_f32_16x16x32_bf16 v[248:251], a[92:95], a[140:143], v[248:251]\n"
"ds_read_b64_tr_b16 a[56:57], v15 offset:8200\n"
"v_mfma_f32_16x16x32_bf16 v[220:223], a[84:87], a[140:143], v[220:223]\n"
"ds_read_b128 a[140:143], v13 offset:26624\n"
"v_mfma_f32_16x16x32_bf16 v[220:223], a[80:83], a[136:139], v[220:223]\n"
"ds_read_b128 a[136:139], v12 offset:26624\n"
"v_mfma_f32_16x16x32_bf16 v[224:227], a[80:83], a[144:147], v[224:227]\n"
"ds_read_b64_tr_b16 a[58:59], v15 offset:8712\n"
"v_mfma_f32_16x16x32_bf16 v[224:227], a[84:87], a[148:151], v[224:227]\n"
"s_sub_u32 s67, 1048576, s75\n"
"ds_read_b64_tr_b16 a[60:61], v15 offset:12296\n"
"v_mfma_f32_16x16x32_bf16 v[252:255], a[92:95], a[148:151], v[252:255]\n"
"ds_read_b128 a[148:151], v13 offset:28672\n"
"v_mfma_f32_16x16x32_bf16 v[252:255], a[88:91], a[144:147], v[252:255]\n"
"ds_read_b128 a[144:147], v12 offset:28672\n"
"ds_read_b64_tr_b16 a[62:63], v15 offset:12808\n"
"v_mfma_f32_16x16x32_bf16 a[28:31], a[88:91], a[152:155], a[28:31]\n"
"v_mfma_f32_16x16x32_bf16 a[28:31], a[92:95], a[156:159], a[28:31]\n"
"v_mfma_f32_16x16x32_bf16 a[24:27], a[84:87], a[156:159], a[24:27]\n"
"ds_read_b128 a[156:159], v13 offset:30720\n"
"v_mfma_f32_16x16x32_bf16 a[24:27], a[80:83], a[152:155], a[24:27]\n"
"s_waitcnt lgkmcnt(15)\n"
"ds_read_b128 a[152:155], v12 offset:30720\n"
"v_mfma_f32_16x16x32_bf16 v[32:35], a[32:35], a[96:99], v[32:35]\n"
"v_mfma_f32_16x16x32_bf16 v[32:35], a[36:39], a[100:103], v[32:35]\n"
"s_waitcnt lgkmcnt(2)\n"
"v_mfma_f32_16x16x32_bf16 v[60:63], a[44:47], a[100:103], v[60:63]\n"
"buffer_load_dwordx4 v11, s[52:55], s67 offen offset:0  lds\n"
"v_mfma_f32_16x16x32_bf16 v[60:63], a[40:43], a[96:99], v[60:63]\n"
"v_mfma_f32_16x16x32_bf16 v[64:67], a[40:43], a[104:107], v[64:67]\n"
"s_sub_u32 s67, 1063936, s75\n"
"v_mfma_f32_16x16x32_bf16 v[64:67], a[44:47], a[108:111], v[64:67]\n"
"v_mfma_f32_16x16x32_bf16 v[36:39], a[36:39], a[108:111], v[36:39]\n"
"v_mfma_f32_16x16x32_bf16 v[36:39], a[32:35], a[104:107], v[36:39]\n"
"buffer_load_dwordx4 v11, s[52:55], s67 offen offset:1024  lds\n"
"v_mfma_f32_16x16x32_bf16 v[40:43], a[32:35], a[112:115], v[40:43]\n"
"v_mfma_f32_16x16x32_bf16 v[40:43], a[36:39], a[116:119], v[40:43]\n"
"s_sub_u32 s67, 1079296, s75\n"
"v_mfma_f32_16x16x32_bf16 v[68:71], a[44:47], a[116:119], v[68:71]\n"
"v_mfma_f32_16x16x32_bf16 v[68:71], a[40:43], a[112:115], v[68:71]\n"
"buffer_load_dwordx4 v11, s[52:55], s67 offen offset:2048  lds\n"
"v_mfma_f32_16x16x32_bf16 v[72:75], a[40:43], a[120:123], v[72:75]\n"
"v_mfma_f32_16x16x32_bf16 v[72:75], a[44:47], a[124:127], v[72:75]\n"
"s_sub_u32 s67, 1094656, s75\n"
"v_mfma_f32_16x16x32_bf16 v[44:47], a[36:39], a[124:127], v[44:47]\n"
"v_mfma_f32_16x16x32_bf16 v[44:47], a[32:35], a[120:123], v[44:47]\n"
"v_mfma_f32_16x16x32_bf16 v[48:51], a[32:35], a[128:131], v[48:51]\n"
"buffer_load_dwordx4 v11, s[52:55], s67 offen offset:3072  lds\n"
"v_mfma_f32_16x16x32_bf16 v[48:51], a[36:39], a[132:135], v[48:51]\n"
"s_add_u32 m0, s64, 12288\n"
"v_mfma_f32_16x16x32_bf16 v[76:79], a[44:47], a[132:135], v[76:79]\n"
"s_waitcnt vmcnt(12)\n"
"ds_read_b64_tr_b16 a[64:65], v9 offset:0\n"
"v_mfma_f32_16x16x32_bf16 v[76:79], a[40:43], a[128:131], v[76:79]\n"
"ds_read_b64_tr_b16 a[66:67], v9 offset:512\n"
"v_mfma_f32_16x16x32_bf16 v[80:83], a[40:43], a[136:139], v[80:83]\n"
"L_st2135:\n"
"s_cbranch_execz L_st4490\n"
"s_sub_u32 s67, 1114112, s75\n"
"ds_read_b64_tr_b16 a[68:69], v9 offset:4096\n"
"v_mfma_f32_16x16x32_bf16 v[80:83], a[44:47], a[140:143], v[80:83]\n"
"ds_read_b64_tr_b16 a[70:71], v9 offset:4608\n"
"v_mfma_f32_16x16x32_bf16 v[52:55], a[36:39], a[140:143], v[52:55]\n"
"ds_read_b64_tr_b16 a[72:73], v9 offset:8\n"
"buffer_load_dwordx4 v11, s[52:55], s67 offen offset:0  lds\n"
"v_mfma_f32_16x16x32_bf16 v[52:55], a[32:35], a[136:139], v[52:55]\n"
"s_waitcnt lgkmcnt(5)\n"
"ds_read_b64_tr_b16 a[74:75], v9 offset:520\n"
"v_mfma_f32_16x16x32_bf16 v[56:59], a[32:35], a[144:147], v[56:59]\n"
"s_sub_u32 s67, 1129472, s75\n"
"ds_read_b64_tr_b16 a[76:77], v9 offset:4104\n"
"v_mfma_f32_16x16x32_bf16 v[56:59], a[36:39], a[148:151], v[56:59]\n"
"ds_read_b64_tr_b16 a[78:79], v9 offset:4616\n"
"v_mfma_f32_16x16x32_bf16 v[84:87], a[44:47], a[148:151], v[84:87]\n"
"buffer_load_dwordx4 v11, s[52:55], s67 offen offset:1024  lds\n"
"v_mfma_f32_16x16x32_bf16 v[84:87], a[40:43], a[144:147], v[84:87]\n"
"ds_read_b64_tr_b16 a[80:81], v15 offset:0\n"
"v_mfma_f32_16x16x32_bf16 a[4:7], a[40:43], a[152:155], a[4:7]\n"
"s_sub_u32 s67, 1144832, s75\n"
"ds_read_b64_tr_b16 a[82:83], v15 offset:512\n"
"v_mfma_f32_16x16x32_bf16 a[4:7], a[44:47], a[156:159], a[4:7]\n"
"ds_read_b64_tr_b16 a[84:85], v15 offset:4096\n"
"v_mfma_f32_16x16x32_bf16 a[0:3], a[36:39], a[156:159], a[0:3]\n"
"ds_read_b64_tr_b16 a[86:87], v15 offset:4608\n"
"buffer_load_dwordx4 v11, s[52:55], s67 offen offset:2048  lds\n"
"v_mfma_f32_16x16x32_bf16 a[0:3], a[32:35], a[152:155], a[0:3]\n"
"ds_read_b64_tr_b16 a[88:89], v15 offset:8\n"
"v_mfma_f32_16x16x32_bf16 v[88:91], a[48:51], a[96:99], v[88:91]\n"
"s_sub_u32 s67, 1160192, s75\n"
"ds_read_b64_tr_b16 a[90:91], v15 offset:520\n"
"v_mfma_f32_16x16x32_bf16 v[88:91], a[52:55], a[100:103], v[88:91]\n"
"ds_read_b64_tr_b16 a[92:93], v15 offset:4104\n"
"v_mfma_f32_16x16x32_bf16 v[116:119], a[60:63], a[100:103], v[116:119]\n"
"ds_read_b64_tr_b16 a[94:95], v15 offset:4616\n"
"v_mfma_f32_16x16x32_bf16 v[116:119], a[56:59], a[96:99], v[116:119]\n"
"buffer_load_dwordx4 v11, s[52:55], s67 offen offset:3072  lds\n"
"v_mfma_f32_16x16x32_bf16 v[120:123], a[56:59], a[104:107], v[120:123]\n"
"v_mfma_f32_16x16x32_bf16 v[120:123], a[60:63], a[108:111], v[120:123]\n"
"s_add_u32 m0, s64, 0\n"
"v_mfma_f32_16x16x32_bf16 v[92:95], a[52:55], a[108:111], v[92:95]\n"
"v_mfma_f32_16x16x32_bf16 v[92:95], a[48:51], a[104:107], v[92:95]\n"
"s_sub_u32 s67, 1048704, s75\n"
"v_mfma_f32_16x16x32_bf16 v[96:99], a[48:51], a[112:115], v[96:99]\n"
"s_waitcnt lgkmcnt(0)\n"
"v_mfma_f32_16x16x32_bf16 v[96:99], a[52:55], a[116:119], v[96:99]\n"
"buffer_load_dwordx4 v11, s[52:55], s67 offen offset:0  lds\n"
"v_mfma_f32_16x16x32_bf16 v[124:127], a[60:63], a[116:119], v[124:127]\n"
"v_mfma_f32_16x16x32_bf16 v[124:127], a[56:59], a[112:115], v[124:127]\n"
"s_sub_u32 s67, 1064064, s75\n"
"v_mfma_f32_16x16x32_bf16 v[128:131], a[56:59], a[120:123], v[128:131]\n"
"v_mfma_f32_16x16x32_bf16 v[128:131], a[60:63], a[124:127], v[128:131]\n"
"v_mfma_f32_16x16x32_bf16 v[100:103], a[52:55], a[124:127], v[100:103]\n"
"buffer_load_dwordx4 v11, s[52:55], s67 offen offset:1024  lds\n"
"v_mfma_f32_16x16x32_bf16 v[100:103], a[48:51], a[120:123], v[100:103]\n"
"v_mfma_f32_16x16x32_bf16 v[104:107], a[48:51], a[128:131], v[104:107]\n"
"s_sub_u32 s67, 1079424, s75\n"
"v_mfma_f32_16x16x32_bf16 v[104:107], a[52:55], a[132:135], v[104:107]\n"
"v_mfma_f32_16x16x32_bf16 v[132:135], a[60:63], a[132:135], v[132:135]\n"
"buffer_load_dwordx4 v11, s[52:55], s67 offen offset:2048  lds\n"
"v_mfma_f32_16x16x32_bf16 v[132:135], a[56:59], a[128:131], v[132:135]\n"
"v_mfma_f32_16x16x32_bf16 v[136:139], a[56:59], a[136:139], v[136:139]\n"
"s_sub_u32 s67, 1094784, s75\n"
"v_mfma_f32_16x16x32_bf16 v[136:139], a[60:63], a[140:143], v[136:139]\n"
"v_mfma_f32_16x16x32_bf16 v[108:111], a[52:55], a[140:143], v[108:111]\n"
"v_mfma_f32_16x16x32_bf16 v[108:111], a[48:51], a[136:139], v[108:111]\n"
"buffer_load_dwordx4 v11, s[52:55], s67 offen offset:3072  lds\n"
"v_mfma_f32_16x16x32_bf16 v[112:115], a[48:51], a[144:147], v[112:115]\n"
"s_add_u32 m0, s64, 4096\n"
"v_mfma_f32_16x16x32_bf16 v[112:115], a[52:55], a[148:151], v[112:115]\n"
"v_mfma_f32_16x16x32_bf16 v[140:143], a[60:63], a[148:151], v[140:143]\n"
"s_sub_u32 s67, 1114240, s75\n"
"v_mfma_f32_16x16x32_bf16 v[140:143], a[56:59], a[144:147], v[140:143]\n"
"v_mfma_f32_16x16x32_bf16 a[12:15], a[56:59], a[152:155], a[12:15]\n"
"buffer_load_dwordx4 v11, s[52:55], s67 offen offset:0  lds\n"
"v_mfma_f32_16x16x32_bf16 a[12:15], a[60:63], a[156:159], a[12:15]\n"
"v_mfma_f32_16x16x32_bf16 a[8:11], a[52:55], a[156:159], a[8:11]\n"
"s_sub_u32 s67, 1129600, s75\n"
"v_mfma_f32_16x16x32_bf16 a[8:11], a[48:51], a[152:155], a[8:11]\n"
"v_mfma_f32_16x16x32_bf16 v[144:147], a[64:67], a[96:99], v[144:147]\n"
"buffer_load_dwordx4 v11, s[52:55], s67 offen offset:1024  lds\n"
"v_mfma_f32_16x16x32_bf16 v[144:147], a[68:71], a[100:103], v[144:147]\n"
"v_mfma_f32_16x16x32_bf16 v[172:175], a[76:79], a[100:103], v[172:175]\n"
"s_sub_u32 s67, 1144960, s75\n"
"v_mfma_f32_16x16x32_bf16 v[172:175], a[72:75], a[96:99], v[172:175]\n"
"v_mfma_f32_16x16x32_bf16 v[176:179], a[72:75], a[104:107], v[176:179]\n"
"v_mfma_f32_16x16x32_bf16 v[176:179], a[76:79], a[108:111], v[176:179]\n"
"s_sub_u32 s71, s72, s64\n"
"v_mfma_f32_16x16x32_bf16 v[148:151], a[68:71], a[108:111], v[148:151]\n"
"buffer_load_dwordx4 v11, s[52:55], s67 offen offset:2048  lds\n"
"v_mfma_f32_16x16x32_bf16 v[148:151], a[64:67], a[104:107], v[148:151]\n"
"s_sub_u32 s67, 1160320, s75\n"
"v_mfma_f32_16x16x32_bf16 v[152:155], a[64:67], a[112:115], v[152:155]\n"
"s_sub_u32 s66, 0, s73\n"
"s_add_u32 s71, s71, 57344\n"
"v_mfma_f32_16x16x32_bf16 v[152:155], a[68:71], a[116:119], v[152:155]\n"
"buffer_load_dwordx4 v11, s[52:55], s67 offen offset:3072  lds\n"
"v_mfma_f32_16x16x32_bf16 v[180:183], a[76:79], a[116:119], v[180:183]\n"
"v_add_u32 v12, s66, v7\n"
"v_mfma_f32_16x16x32_bf16 v[180:183], a[72:75], a[112:115], v[180:183]\n"
"v_add_u32 v20, s71, v7\n"
"v_mfma_f32_16x16x32_bf16 v[184:187], a[72:75], a[120:123], v[184:187]\n"
"v_add_u32 v13, s66, v8\n"
"v_mfma_f32_16x16x32_bf16 v[184:187], a[76:79], a[124:127], v[184:187]\n"
"v_add_u32 v21, s71, v8\n"
"v_mfma_f32_16x16x32_bf16 v[156:159], a[68:71], a[124:127], v[156:159]\n"
"s_cmp_lt_u32 %16, 2\n"
"s_cselect_b32 s67, 16384, 0\n"
"v_mfma_f32_16x16x32_bf16 v[156:159], a[64:67], a[120:123], v[156:159]\n"
"s_add_u32 s67, s67, s72\n"
"v_mfma_f32_16x16x32_bf16 v[160:163], a[64:67], a[128:131], v[160:163]\n"
"s_waitcnt vmcnt(16)\n"
"ds_read_b128 a[192:195], v20 offset:0\n"
"v_mfma_f32_16x16x32_bf16 v[160:163], a[68:71], a[132:135], v[160:163]\n"
"v_mfma_f32_16x16x32_bf16 v[188:191], a[76:79], a[132:135], v[188:191]\n"
"ds_read_b128 a[196:199], v21 offset:0\n"
"v_mfma_f32_16x16x32_bf16 v[188:191], a[72:75], a[128:131], v[188:191]\n"
"ds_read_b128 a[200:203], v20 offset:2048\n"
"v_mfma_f32_16x16x32_bf16 v[192:195], a[72:75], a[136:139], v[192:195]\n"
"s_add_u32 m0, s67, 57344\n"
"v_mfma_f32_16x16x32_bf16 v[192:195], a[76:79], a[140:143], v[192:195]\n"
"ds_read_b128 a[204:207], v21 offset:2048\n"
"v_mfma_f32_16x16x32_bf16 v[164:167], a[68:71], a[140:143], v[164:167]\n"
"v_mfma_f32_16x16x32_bf16 v[164:167], a[64:67], a[136:139], v[164:167]\n"
"v_mfma_f32_16x16x32_bf16 v[168:171], a[64:67], a[144:147], v[168:171]\n"
"s_sub_u32 s67, 1280, s60\n"
"v_mfma_f32_16x16x32_bf16 v[168:171], a[68:71], a[148:151], v[168:171]\n"
"v_mfma_f32_16x16x32_bf16 v[196:199], a[76:79], a[148:151], v[196:199]\n"
"v_mfma_f32_16x16x32_bf16 v[196:199], a[72:75], a[144:147], v[196:199]\n"
"s_waitcnt lgkmcnt(0)\n"
"v_mfma_f32_16x16x32_bf16 a[20:23], a[72:75], a[152:155], a[20:23]\n"
"buffer_load_dwordx4 v6, s[48:51], s67 offen offset:0  lds\n"
"v_mfma_f32_16x16x32_bf16 a[20:23], a[76:79], a[156:159], a[20:23]\n"
"v_mfma_f32_16x16x32_bf16 a[16:19], a[68:71], a[156:159], a[16:19]\n"
"s_sub_u32 s67, 16640, s60\n"
"v_mfma_f32_16x16x32_bf16 a[16:19], a[64:67], a[152:155], a[16:19]\n"
"s_barrier\n"
"s_waitcnt vmcnt(9)\n"
"ds_read_b64_tr_b16 a[32:33], v9 offset:8192\n"
"v_mfma_f32_16x16x32_bf16 v[200:203], a[80:83], a[96:99], v[200:203]\n"
"ds_read_b64_tr_b16 a[34:35], v9 offset:8704\n"
"buffer_load_dwordx4 v6, s[48:51], s67 offen offset:1024  lds\n"
"v_mfma_f32_16x16x32_bf16 v[200:203], a[84:87], a[100:103], v[200:203]\n"
"ds_read_b64_tr_b16 a[36:37], v9 offset:12288\n"
"v_mfma_f32_16x16x32_bf16 v[228:231], a[92:95], a[100:103], v[228:231]\n"
"s_sub_u32 s67, 32000, s60\n"
"ds_read_b128 a[100:103], v13 offset:32768\n"
"v_mfma_f32_16x16x32_bf16 v[228:231], a[88:91], a[96:99], v[228:231]\n"
"ds_read_b128 a[96:99], v12 offset:32768\n"
"v_mfma_f32_16x16x32_bf16 v[232:235], a[88:91], a[104:107], v[232:235]\n"
"ds_read_b64_tr_b16 a[38:39], v9 offset:12800\n"
"v_mfma_f32_16x16x32_bf16 v[232:235], a[92:95], a[108:111], v[232:235]\n"
"ds_read_b64_tr_b16 a[40:41], v9 offset:8200\n"
"buffer_load_dwordx4 v6, s[48:51], s67 offen offset:2048  lds\n"
"v_mfma_f32_16x16x32_bf16 v[204:207], a[84:87], a[108:111], v[204:207]\n"
"ds_read_b128 a[108:111], v13 offset:34816\n"
"v_mfma_f32_16x16x32_bf16 v[204:207], a[80:83], a[104:107], v[204:207]\n"
"s_sub_u32 s67, 47360, s60\n"
"ds_read_b128 a[104:107], v12 offset:34816\n"
"v_mfma_f32_16x16x32_bf16 v[208:211], a[80:83], a[112:115], v[208:211]\n"
"ds_read_b64_tr_b16 a[42:43], v9 offset:8712\n"
"v_mfma_f32_16x16x32_bf16 v[208:211], a[84:87], a[116:119], v[208:211]\n"
"ds_read_b64_tr_b16 a[44:45], v9 offset:12296\n"
"v_mfma_f32_16x16x32_bf16 v[236:239], a[92:95], a[116:119], v[236:239]\n"
"ds_read_b128 a[116:119], v13 offset:36864\n"
"buffer_load_dwordx4 v6, s[48:51], s67 offen offset:3072  lds\n"
"v_mfma_f32_16x16x32_bf16 v[236:239], a[88:91], a[112:115], v[236:239]\n"
"ds_read_b128 a[112:115], v12 offset:36864\n"
"v_mfma_f32_16x16x32_bf16 v[240:243], a[88:91], a[120:123], v[240:243]\n"
"s_add_u32 m0, s64, 16256\n"
"ds_read_b64_tr_b16 a[46:47], v9 offset:12808\n"
"v_mfma_f32_16x16x32_bf16 v[240:243], a[92:95], a[124:127], v[240:243]\n"
"ds_read_b64_tr_b16 a[48:49], v15 offset:8192\n"
"v_mfma_f32_16x16x32_bf16 v[212:215], a[84:87], a[124:127], v[212:215]\n"
"ds_read_b128 a[124:127], v13 offset:38912\n"
"buffer_load_dwordx4 v6, s[40:43], s84 offen offset:128  lds\n"
"v_mfma_f32_16x16x32_bf16 v[212:215], a[80:83], a[120:123], v[212:215]\n"
"ds_read_b128 a[120:123], v12 offset:38912\n"
"v_mfma_f32_16x16x32_bf16 v[216:219], a[80:83], a[128:131], v[216:219]\n"
"ds_read_b64_tr_b16 a[50:51], v15 offset:8704\n"
"v_mfma_f32_16x16x32_bf16 v[216:219], a[84:87], a[132:135], v[216:219]\n"
"ds_read_b64_tr_b16 a[52:53], v15 offset:12288\n"
"v_mfma_f32_16x16x32_bf16 v[244:247], a[92:95], a[132:135], v[244:247]\n"
"ds_read_b128 a[132:135], v13 offset:24576\n"
"buffer_load_dwordx4 v6, s[40:43], s85 offen offset:1152  lds\n"
"v_mfma_f32_16x16x32_bf16 v[244:247], a[88:91], a[128:131], v[244:247]\n"
"ds_read_b128 a[128:131], v12 offset:24576\n"
"v_mfma_f32_16x16x32_bf16 v[248:251], a[88:91], a[136:139], v[248:251]\n"
"ds_read_b64_tr_b16 a[54:55], v15 offset:12800\n"
"v_mfma_f32_16x16x32_bf16 v[248:251], a[92:95], a[140:143], v[248:251]\n"
"ds_read_b64_tr_b16 a[56:57], v15 offset:8200\n"
"v_mfma_f32_16x16x32_bf16 v[220:223], a[84:87], a[140:143], v[220:223]\n"
"ds_read_b128 a[140:143], v13 offset:26624\n"
"buffer_load_dwordx4 v6, s[40:43], s86 offen offset:2176  lds\n"
"v_mfma_f32_16x16x32_bf16 v[220:223], a[80:83], a[136:139], v[220:223]\n"
"ds_read_b128 a[136:139], v12 offset:26624\n"
"v_mfma_f32_16x16x32_bf16 v[224:227], a[80:83], a[144:147], v[224:227]\n"
"ds_read_b64_tr_b16 a[58:59], v15 offset:8712\n"
"v_mfma_f32_16x16x32_bf16 v[224:227], a[84:87], a[148:151], v[224:227]\n"
"ds_read_b64_tr_b16 a[60:61], v15 offset:12296\n"
"v_mfma_f32_16x16x32_bf16 v[252:255], a[92:95], a[148:151], v[252:255]\n"
"ds_read_b128 a[148:151], v13 offset:28672\n"
"v_mfma_f32_16x16x32_bf16 v[252:255], a[88:91], a[144:147], v[252:255]\n"
"ds_read_b128 a[144:147], v12 offset:28672\n"
"ds_read_b64_tr_b16 a[62:63], v15 offset:12808\n"
"v_mfma_f32_16x16x32_bf16 a[28:31], a[88:91], a[152:155], a[28:31]\n"
"buffer_load_dwordx4 v6, s[40:43], s87 offen offset:3200  lds\n"
"v_mfma_f32_16x16x32_bf16 a[28:31], a[92:95], a[156:159], a[28:31]\n"
"v_mfma_f32_16x16x32_bf16 a[24:27], a[84:87], a[156:159], a[24:27]\n"
"s_add_u32 m0, s64, 8192\n"
"ds_read_b128 a[156:159], v13 offset:30720\n"
"v_mfma_f32_16x16x32_bf16 a[24:27], a[80:83], a[152:155], a[24:27]\n"
"s_waitcnt lgkmcnt(15)\n"
"ds_read_b128 a[152:155], v12 offset:30720\n"
"v_mfma_f32_16x16x32_bf16 v[32:35], a[32:35], a[96:99], v[32:35]\n"
"s_sub_u32 s67, 1310720, s75\n"
"v_mfma_f32_16x16x32_bf16 v[32:35], a[36:39], a[100:103], v[32:35]\n"
"s_waitcnt lgkmcnt(2)\n"
"v_mfma_f32_16x16x32_bf16 v[60:63], a[44:47], a[100:103], v[60:63]\n"
"buffer_load_dwordx4 v11, s[52:55], s67 offen offset:0  lds\n"
"v_mfma_f32_16x16x32_bf16 v[60:63], a[40:43], a[96:99], v[60:63]\n"
"v_mfma_f32_16x16x32_bf16 v[64:67], a[40:43], a[104:107], v[64:67]\n"
"s_sub_u32 s67, 1326080, s75\n"
"v_mfma_f32_16x16x32_bf16 v[64:67], a[44:47], a[108:111], v[64:67]\n"
"v_mfma_f32_16x16x32_bf16 v[36:39], a[36:39], a[108:111], v[36:39]\n"
"buffer_load_dwordx4 v11, s[52:55], s67 offen offset:1024  lds\n"
"v_mfma_f32_16x16x32_bf16 v[36:39], a[32:35], a[104:107], v[36:39]\n"
"v_mfma_f32_16x16x32_bf16 v[40:43], a[32:35], a[112:115], v[40:43]\n"
"s_sub_u32 s67, 1341440, s75\n"
"v_mfma_f32_16x16x32_bf16 v[40:43], a[36:39], a[116:119], v[40:43]\n"
"v_mfma_f32_16x16x32_bf16 v[68:71], a[44:47], a[116:119], v[68:71]\n"
"v_mfma_f32_16x16x32_bf16 v[68:71], a[40:43], a[112:115], v[68:71]\n"
"buffer_load_dwordx4 v11, s[52:55], s67 offen offset:2048  lds\n"
"v_mfma_f32_16x16x32_bf16 v[72:75], a[40:43], a[120:123], v[72:75]\n"
"v_mfma_f32_16x16x32_bf16 v[72:75], a[44:47], a[124:127], v[72:75]\n"
"s_sub_u32 s67, 1356800, s75\n"
"v_mfma_f32_16x16x32_bf16 v[44:47], a[36:39], a[124:127], v[44:47]\n"
"v_mfma_f32_16x16x32_bf16 v[44:47], a[32:35], a[120:123], v[44:47]\n"
"buffer_load_dwordx4 v11, s[52:55], s67 offen offset:3072  lds\n"
"v_mfma_f32_16x16x32_bf16 v[48:51], a[32:35], a[128:131], v[48:51]\n"
"v_mfma_f32_16x16x32_bf16 v[48:51], a[36:39], a[132:135], v[48:51]\n"
"s_add_u32 m0, s64, 12288\n"
"v_mfma_f32_16x16x32_bf16 v[76:79], a[44:47], a[132:135], v[76:79]\n"
"s_waitcnt vmcnt(12)\n"
"ds_read_b64_tr_b16 a[64:65], v9 offset:0\n"
"v_mfma_f32_16x16x32_bf16 v[76:79], a[40:43], a[128:131], v[76:79]\n"
"s_sub_u32 s67, 1376256, s75\n"
"ds_read_b64_tr_b16 a[66:67], v9 offset:512\n"
"v_mfma_f32_16x16x32_bf16 v[80:83], a[40:43], a[136:139], v[80:83]\n"
"ds_read_b64_tr_b16 a[68:69], v9 offset:4096\n"
"v_mfma_f32_16x16x32_bf16 v[80:83], a[44:47], a[140:143], v[80:83]\n"
"ds_read_b64_tr_b16 a[70:71], v9 offset:4608\n"
"buffer_load_dwordx4 v11, s[52:55], s67 offen offset:0  lds\n"
"v_mfma_f32_16x16x32_bf16 v[52:55], a[36:39], a[140:143], v[52:55]\n"
"s_sub_u32 s67, 1391616, s75\n"
"ds_read_b64_tr_b16 a[72:73], v9 offset:8\n"
"v_mfma_f32_16x16x32_bf16 v[52:55], a[32:35], a[136:139], v[52:55]\n"
"s_waitcnt lgkmcnt(5)\n"
"ds_read_b64_tr_b16 a[74:75], v9 offset:520\n"
"v_mfma_f32_16x16x32_bf16 v[56:59], a[32:35], a[144:147], v[56:59]\n"
"ds_read_b64_tr_b16 a[76:77], v9 offset:4104\n"
"v_mfma_f32_16x16x32_bf16 v[56:59], a[36:39], a[148:151], v[56:59]\n"
"ds_read_b64_tr_b16 a[78:79], v9 offset:4616\n"
"v_mfma_f32_16x16x32_bf16 v[84:87], a[44:47], a[148:151], v[84:87]\n"
"buffer_load_dwordx4 v11, s[52:55], s67 offen offset:1024  lds\n"
"v_mfma_f32_16x16x32_bf16 v[84:87], a[40:43], a[144:147], v[84:87]\n"
"ds_read_b64_tr_b16 a[80:81], v15 offset:0\n"
"v_mfma_f32_16x16x32_bf16 a[4:7], a[40:43], a[152:155], a[4:7]\n"
"s_sub_u32 s67, 1406976, s75\n"
"ds_read_b64_tr_b16 a[82:83], v15 offset:512\n"
"v_mfma_f32_16x16x32_bf16 a[4:7], a[44:47], a[156:159], a[4:7]\n"
"ds_read_b64_tr_b16 a[84:85], v15 offset:4096\n"
"v_mfma_f32_16x16x32_bf16 a[0:3], a[36:39], a[156:159], a[0:3]\n"
"ds_read_b64_tr_b16 a[86:87], v15 offset:4608\n"
"buffer_load_dwordx4 v11, s[52:55], s67 offen offset:2048  lds\n"
"v_mfma_f32_16x16x32_bf16 a[0:3], a[32:35], a[152:155], a[0:3]\n"
"ds_read_b64_tr_b16 a[88:89], v15 offset:8\n"
"v_mfma_f32_16x16x32_bf16 v[88:91], a[48:51], a[96:99], v[88:91]\n"
"s_sub_u32 s67, 1422336, s75\n"
"ds_read_b64_tr_b16 a[90:91], v15 offset:520\n"
"v_mfma_f32_16x16x32_bf16 v[88:91], a[52:55], a[100:103], v[88:91]\n"
"ds_read_b64_tr_b16 a[92:93], v15 offset:4104\n"
"v_mfma_f32_16x16x32_bf16 v[116:119], a[60:63], a[100:103], v[116:119]\n"
"ds_read_b64_tr_b16 a[94:95], v15 offset:4616\n"
"v_mfma_f32_16x16x32_bf16 v[116:119], a[56:59], a[96:99], v[116:119]\n"
"buffer_load_dwordx4 v11, s[52:55], s67 offen offset:3072  lds\n"
"v_mfma_f32_16x16x32_bf16 v[120:123], a[56:59], a[104:107], v[120:123]\n"
"v_mfma_f32_16x16x32_bf16 v[120:123], a[60:63], a[108:111], v[120:123]\n"
"s_add_u32 m0, s64, 0\n"
"v_mfma_f32_16x16x32_bf16 v[92:95], a[52:55], a[108:111], v[92:95]\n"
"v_mfma_f32_16x16x32_bf16 v[92:95], a[48:51], a[104:107], v[92:95]\n"
"s_sub_u32 s67, 1310848, s75\n"
"v_mfma_f32_16x16x32_bf16 v[96:99], a[48:51], a[112:115], v[96:99]\n"
"s_waitcnt lgkmcnt(0)\n"
"s_mov_b32 s99, 0\n"
"v_mfma_f32_16x16x32_bf16 v[96:99], a[52:55], a[116:119], v[96:99]\n"
"s_mov_b32 s98, 0\n"
"buffer_load_dwordx4 v11, s[52:55], s67 offen offset:0  lds\n"
"v_mfma_f32_16x16x32_bf16 v[124:127], a[60:63], a[116:119], v[124:127]\n"
"s_memtime s[76:77]\n"
"s_load_dword s98, s[96:97], 0 glc\n"
"v_mfma_f32_16x16x32_bf16 v[124:127], a[56:59], a[112:115], v[124:127]\n"
"s_sub_u32 s67, 1326208, s75\n"
"v_mfma_f32_16x16x32_bf16 v[128:131], a[56:59], a[120:123], v[128:131]\n"
"v_mfma_f32_16x16x32_bf16 v[128:131], a[60:63], a[124:127], v[128:131]\n"
"v_mfma_f32_16x16x32_bf16 v[100:103], a[52:55], a[124:127], v[100:103]\n"
"buffer_load_dwordx4 v11, s[52:55], s67 offen offset:1024  lds\n"
"v_mfma_f32_16x16x32_bf16 v[100:103], a[48:51], a[120:123], v[100:103]\n"
"v_mfma_f32_16x16x32_bf16 v[104:107], a[48:51], a[128:131], v[104:107]\n"
"s_sub_u32 s67, 1341568, s75\n"
"v_mfma_f32_16x16x32_bf16 v[104:107], a[52:55], a[132:135], v[104:107]\n"
"v_mfma_f32_16x16x32_bf16 v[132:135], a[60:63], a[132:135], v[132:135]\n"
"v_mfma_f32_16x16x32_bf16 v[132:135], a[56:59], a[128:131], v[132:135]\n"
"s_mov_b32 s29, s76\n"
"v_mfma_f32_16x16x32_bf16 v[136:139], a[56:59], a[136:139], v[136:139]\n"
"buffer_load_dwordx4 v11, s[52:55], s67 offen offset:2048  lds\n"
"v_mfma_f32_16x16x32_bf16 v[136:139], a[60:63], a[140:143], v[136:139]\n"
"s_sub_u32 s67, 1356928, s75\n"
"v_mfma_f32_16x16x32_bf16 v[108:111], a[52:55], a[140:143], v[108:111]\n"
"s_sub_u32 s99, s99, s98\n"
"s_sub_u32 s99, s99, s98\n"
"v_mfma_f32_16x16x32_bf16 v[108:111], a[48:51], a[136:139], v[108:111]\n"
"s_sub_u32 s99, s99, s98\n"
"v_mfma_f32_16x16x32_bf16 v[112:115], a[48:51], a[144:147], v[112:115]\n"
"s_sub_u32 s99, s99, s98\n"
"buffer_load_dwordx4 v11, s[52:55], s67 offen offset:3072  lds\n"
"v_mfma_f32_16x16x32_bf16 v[112:115], a[52:55], a[148:151], v[112:115]\n"
"s_add_u32 m0, s64, 4096\n"
"v_mfma_f32_16x16x32_bf16 v[140:143], a[60:63], a[148:151], v[140:143]\n"
"s_sub_u32 s99, s99, s98\n"
"s_sub_u32 s99, s99, s98\n"
"v_mfma_f32_16x16x32_bf16 v[140:143], a[56:59], a[144:147], v[140:143]\n"
"s_sub_u32 s99, s99, s98\n"
"s_sub_u32 s67, 1376384, s75\n"
"v_mfma_f32_16x16x32_bf16 a[12:15], a[56:59], a[152:155], a[12:15]\n"
"s_sub_u32 s99, s99, s98\n"
"v_mfma_f32_16x16x32_bf16 a[12:15], a[60:63], a[156:159], a[12:15]\n"
"buffer_load_dwordx4 v11, s[52:55], s67 offen offset:0  lds\n"
"v_mfma_f32_16x16x32_bf16 a[8:11], a[52:55], a[156:159], a[8:11]\n"
"s_sub_u32 s99, s99, s98\n"
"s_sub_u32 s99, s99, s98\n"
"v_mfma_f32_16x16x32_bf16 a[8:11], a[48:51], a[152:155], a[8:11]\n"
"s_sub_u32 s99, s99, s98\n"
"s_sub_u32 s67, 1391744, s75\n"
"v_mfma_f32_16x16x32_bf16 v[144:147], a[64:67], a[96:99], v[144:147]\n"
"s_sub_u32 s99, s99, s98\n"
"v_mfma_f32_16x16x32_bf16 v[144:147], a[68:71], a[100:103], v[144:147]\n"
"buffer_load_dwordx4 v11, s[52:55], s67 offen offset:1024  lds\n"
"v_mfma_f32_16x16x32_bf16 v[172:175], a[76:79], a[100:103], v[172:175]\n"
"s_sub_u32 s99, s99, s98\n"
"s_sub_u32 s99, s99, s98\n"
"v_mfma_f32_16x16x32_bf16 v[172:175], a[72:75], a[96:99], v[172:175]\n"
"s_sub_u32 s99, s99, s98\n"
"s_sub_u32 s67, 1407104, s75\n"
"v_mfma_f32_16x16x32_bf16 v[176:179], a[72:75], a[104:107], v[176:179]\n"
"s_sub_u32 s99, s99, s98\n"
"v_mfma_f32_16x16x32_bf16 v[176:179], a[76:79], a[108:111], v[176:179]\n"
"buffer_load_dwordx4 v11, s[52:55], s67 offen offset:2048  lds\n"
"v_mfma_f32_16x16x32_bf16 v[148:151], a[68:71], a[108:111], v[148:151]\n"
"s_sub_u32 s99, s99, s98\n"
"s_sub_u32 s99, s99, s98\n"
"v_mfma_f32_16x16x32_bf16 v[148:151], a[64:67], a[104:107], v[148:151]\n"
"s_sub_u32 s99, s99, s98\n"
"s_sub_u32 s71, s64, s64\n"
"v_mfma_f32_16x16x32_bf16 v[152:155], a[64:67], a[112:115], v[152:155]\n"
"s_sub_u32 s66, 40960, s73\n"
"s_sub_u32 s99, s99, s98\n"
"v_mfma_f32_16x16x32_bf16 v[152:155], a[68:71], a[116:119], v[152:155]\n"
"s_add_u32 s71, s71, 16384\n"
"s_sub_u32 s67, 1422464, s75\n"
"v_mfma_f32_16x16x32_bf16 v[180:183], a[76:79], a[116:119], v[180:183]\n"
"s_sub_u32 s99, s99, s98\n"
"v_add_u32 v12, s66, v7\n"
"s_sub_u32 s99, s99, s98\n"
"v_mfma_f32_16x16x32_bf16 v[180:183], a[72:75], a[112:115], v[180:183]\n"
"s_sub_u32 s99, s99, s98\n"
"buffer_load_dwordx4 v11, s[52:55], s67 offen offset:3072  lds\n"
"v_mfma_f32_16x16x32_bf16 v[184:187], a[72:75], a[120:123], v[184:187]\n"
"s_sub_u32 s99, s99, s98\n"
"v_add_u32 v13, s66, v8\n"
"s_cmp_lt_u32 %16, 2\n"
"s_cselect_b32 s67, 16384, 0\n"
"v_mfma_f32_16x16x32_bf16 v[184:187], a[76:79], a[124:127], v[184:187]\n"
"s_memtime s[76:77]\n"
"v_add_u32 v20, s71, v7\n"
"s_add_u32 s67, s67, s72\n"
"v_mfma_f32_16x16x32_bf16 v[156:159], a[68:71], a[124:127], v[156:159]\n"
"s_add_u32 m0, s67, 98304\n"
"s_sub_u32 s67, 1536, s60\n"
"v_mfma_f32_16x16x32_bf16 v[156:159], a[64:67], a[120:123], v[156:159]\n"
"s_waitcnt lgkmcnt(0)\n"
"buffer_load_dwordx4 v6, s[48:51], s67 offen offset:0  lds\n"
"v_add_u32 v21, s71, v8\n"
"v_mfma_f32_16x16x32_bf16 v[160:163], a[64:67], a[128:131], v[160:163]\n"
"s_sub_u32 s29, s76, s29\n"
"s_sub_u32 s67, 16896, s60\n"
"v_mfma_f32_16x16x32_bf16 v[160:163], a[68:71], a[132:135], v[160:163]\n"
"s_cmp_lt_u32 s29, 620\n"
"s_cselect_b32 s99, s99, 0\n"
"v_mfma_f32_16x16x32_bf16 v[188:191], a[76:79], a[132:135], v[188:191]\n"
"s_max_u32 s30, s30, s99\n"
"v_mfma_f32_16x16x32_bf16 v[188:191], a[72:75], a[128:131], v[188:191]\n"
"s_waitcnt vmcnt(17)\n"
"ds_read_b128 a[208:211], v20 offset:0\n"
"buffer_load_dwordx4 v6, s[48:51], s67 offen offset:1024  lds\n"
"v_mfma_f32_16x16x32_bf16 v[192:195], a[72:75], a[136:139], v[192:195]\n"
"ds_read_b128 a[212:215], v21 offset:0\n"
"v_mfma_f32_16x16x32_bf16 v[192:195], a[76:79], a[140:143], v[192:195]\n"
"s_sub_u32 s67, 32256, s60\n"
"ds_read_b128 a[216:219], v20 offset:2048\n"
"v_mfma_f32_16x16x32_bf16 v[164:167], a[68:71], a[140:143], v[164:167]\n"
"v_mfma_f32_16x16x32_bf16 v[164:167], a[64:67], a[136:139], v[164:167]\n"
"ds_read_b128 a[220:223], v21 offset:2048\n"
"v_mfma_f32_16x16x32_bf16 v[168:171], a[64:67], a[144:147], v[168:171]\n"
"buffer_load_dwordx4 v6, s[48:51], s67 offen offset:2048  lds\n"
"v_mfma_f32_16x16x32_bf16 v[168:171], a[68:71], a[148:151], v[168:171]\n"
"v_mfma_f32_16x16x32_bf16 v[196:199], a[76:79], a[148:151], v[196:199]\n"
"s_sub_u32 s67, 47616, s60\n"
"v_mfma_f32_16x16x32_bf16 v[196:199], a[72:75], a[144:147], v[196:199]\n"
"v_mfma_f32_16x16x32_bf16 a[20:23], a[72:75], a[152:155], a[20:23]\n"
"buffer_load_dwordx4 v6, s[48:51], s67 offen offset:3072  lds\n"
"v_mfma_f32_16x16x32_bf16 a[20:23], a[76:79], a[156:159], a[20:23]\n"
"s_waitcnt lgkmcnt(0)\n"
"v_mfma_f32_16x16x32_bf16 a[16:19], a[68:71], a[156:159], a[16:19]\n"
"s_add_u32 m0, s64, 20352\n"
"v_mfma_f32_16x16x32_bf16 a[16:19], a[64:67], a[152:155], a[16:19]\n"
"s_barrier\n"
"s_waitcnt vmcnt(12)\n"
"ds_read_b64_tr_b16 a[32:33], v9 offset:8192\n"
"v_mfma_f32_16x16x32_bf16 v[200:203], a[80:83], a[96:99], v[200:203]\n"
"ds_read_b64_tr_b16 a[34:35], v9 offset:8704\n"
"v_mfma_f32_16x16x32_bf16 v[200:203], a[84:87], a[100:103], v[200:203]\n"
"ds_read_b64_tr_b16 a[36:37], v9 offset:12288\n"
"buffer_load_dwordx4 v6, s[40:43], s88 offen offset:128  lds\n"
"v_mfma_f32_16x16x32_bf16 v[228:231], a[92:95], a[100:103], v[228:231]\n"
"ds_read_b128 a[100:103], v13 offset:32768\n"
"v_mfma_f32_16x16x32_bf16 v[228:231], a[88:91], a[96:99], v[228:231]\n"
"ds_read_b128 a[96:99], v12 offset:32768\n"
"v_mfma_f32_16x16x32_bf16 v[232:235], a[88:91], a[104:107], v[232:235]\n"
"ds_read_b64_tr_b16 a[38:39], v9 offset:12800\n"
"buffer_load_dwordx4 v6, s[40:43], s89 offen offset:1152  lds\n"
"v_mfma_f32_16x16x32_bf16 v[232:235], a[92:95], a[108:111], v[232:235]\n"
"ds_read_b64_tr_b16 a[40:41], v9 offset:8200\n"
"v_mfma_f32_16x16x32_bf16 v[204:207], a[84:87], a[108:111], v[204:207]\n"
"ds_read_b128 a[108:111], v13 offset:34816\n"
"v_mfma_f32_16x16x32_bf16 v[204:207], a[80:83], a[104:107], v[204:207]\n"
"ds_read_b128 a[104:107], v12 offset:34816\n"
"v_mfma_f32_16x16x32_bf16 v[208:211], a[80:83], a[112:115], v[208:211]\n"
"ds_read_b64_tr_b16 a[42:43], v9 offset:8712\n"
"v_mfma_f32_16x16x32_bf16 v[208:211], a[84:87], a[116:119], v[208:211]\n"
"ds_read_b64_tr_b16 a[44:45], v9 offset:12296\n"
"buffer_load_dwordx4 v6, s[40:43], s90 offen offset:2176  lds\n"
"v_mfma_f32_16x16x32_bf16 v[236:239], a[92:95], a[116:119], v[236:239]\n"
"ds_read_b128 a[116:119], v13 offset:36864\n"
"v_mfma_f32_16x16x32_bf16 v[236:239], a[88:91], a[112:115], v[236:239]\n"
"ds_read_b128 a[112:115], v12 offset:36864\n"
"v_mfma_f32_16x16x32_bf16 v[240:243], a[88:91], a[120:123], v[240:243]\n"
"ds_read_b64_tr_b16 a[46:47], v9 offset:12808\n"
"v_mfma_f32_16x16x32_bf16 v[240:243], a[92:95], a[124:127], v[240:243]\n"
"ds_read_b64_tr_b16 a[48:49], v15 offset:8192\n"
"buffer_load_dwordx4 v6, s[40:43], s91 offen offset:3200  lds\n"
"v_mfma_f32_16x16x32_bf16 v[212:215], a[84:87], a[124:127], v[212:215]\n"
"ds_read_b128 a[124:127], v13 offset:38912\n"
"v_mfma_f32_16x16x32_bf16 v[212:215], a[80:83], a[120:123], v[212:215]\n"
"ds_read_b128 a[120:123], v12 offset:38912\n"
"v_mfma_f32_16x16x32_bf16 v[216:219], a[80:83], a[128:131], v[216:219]\n"
"ds_read_b64_tr_b16 a[50:51], v15 offset:8704\n"
"v_mfma_f32_16x16x32_bf16 v[216:219], a[84:87], a[132:135], v[216:219]\n"
"s_add_u32 m0, s64, 8192\n"
"ds_read_b64_tr_b16 a[52:53], v15 offset:12288\n"
"v_mfma_f32_16x16x32_bf16 v[244:247], a[92:95], a[132:135], v[244:247]\n"
"ds_read_b128 a[132:135], v13 offset:24576\n"
"v_mfma_f32_16x16x32_bf16 v[244:247], a[88:91], a[128:131], v[244:247]\n"
"ds_read_b128 a[128:131], v12 offset:24576\n"
"v_mfma_f32_16x16x32_bf16 v[248:251], a[88:91], a[136:139], v[248:251]\n"
"ds_read_b64_tr_b16 a[54:55], v15 offset:12800\n"
"v_mfma_f32_16x16x32_bf16 v[248:251], a[92:95], a[140:143], v[248:251]\n"
"ds_read_b64_tr_b16 a[56:57], v15 offset:8200\n"
"v_mfma_f32_16x16x32_bf16 v[220:223], a[84:87], a[140:143], v[220:223]\n"
"ds_read_b128 a[140:143], v13 offset:26624\n"
"v_mfma_f32_16x16x32_bf16 v[220:223], a[80:83], a[136:139], v[220:223]\n"
"ds_read_b128 a[136:139], v12 offset:26624\n"
"v_mfma_f32_16x16x32_bf16 v[224:227], a[80:83], a[144:147], v[224:227]\n"
"ds_read_b64_tr_b16 a[58:59], v15 offset:8712\n"
"v_mfma_f32_16x16x32_bf16 v[224:227], a[84:87], a[148:151], v[224:227]\n"
"s_sub_u32 s67, 1572864, s75\n"
"ds_read_b64_tr_b16 a[60:61], v15 offset:12296\n"
"v_mfma_f32_16x16x32_bf16 v[252:255], a[92:95], a[148:151], v[252:255]\n"
"ds_read_b128 a[148:151], v13 offset:28672\n"
"v_mfma_f32_16x16x32_bf16 v[252:255], a[88:91], a[144:147], v[252:255]\n"
"ds_read_b128 a[144:147], v12 offset:28672\n"
"ds_read_b64_tr_b16 a[62:63], v15 offset:12808\n"
"v_mfma_f32_16x16x32_bf16 a[28:31], a[88:91], a[152:155], a[28:31]\n"
"v_mfma_f32_16x16x32_bf16 a[28:31], a[92:95], a[156:159], a[28:31]\n"
"v_mfma_f32_16x16x32_bf16 a[24:27], a[84:87], a[156:159], a[24:27]\n"
"ds_read_b128 a[156:159], v13 offset:30720\n"
"v_mfma_f32_16x16x32_bf16 a[24:27], a[80:83], a[152:155], a[24:27]\n"
"s_waitcnt lgkmcnt(15)\n"
"ds_read_b128 a[152:155], v12 offset:30720\n"
"v_mfma_f32_16x16x32_bf16 v[32:35], a[32:35], a[96:99], v[32:35]\n"
"v_mfma_f32_16x16x32_bf16 v[32:35], a[36:39], a[100:103], v[32:35]\n"
"s_waitcnt lgkmcnt(2)\n"
"v_mfma_f32_16x16x32_bf16 v[60:63], a[44:47], a[100:103], v[60:63]\n"
"buffer_load_dwordx4 v11, s[52:55], s67 offen offset:0  lds\n"
"v_mfma_f32_16x16x32_bf16 v[60:63], a[40:43], a[96:99], v[60:63]\n"
"v_mfma_f32_16x16x32_bf16 v[64:67], a[40:43], a[104:107], v[64:67]\n"
"s_sub_u32 s67, 1588224, s75\n"
"v_mfma_f32_16x16x32_bf16 v[64:67], a[44:47], a[108:111], v[64:67]\n"
"v_mfma_f32_16x16x32_bf16 v[36:39], a[36:39], a[108:111], v[36:39]\n"
"v_mfma_f32_16x16x32_bf16 v[36:39], a[32:35], a[104:107], v[36:39]\n"
"buffer_load_dwordx4 v11, s[52:55], s67 offen offset:1024  lds\n"
"v_mfma_f32_16x16x32_bf16 v[40:43], a[32:35], a[112:115], v[40:43]\n"
"v_mfma_f32_16x16x32_bf16 v[40:43], a[36:39], a[116:119], v[40:43]\n"
"s_sub_u32 s67, 1603584, s75\n"
"v_mfma_f32_16x16x32_bf16 v[68:71], a[44:47], a[116:119], v[68:71]\n"
"v_mfma_f32_16x16x32_bf16 v[68:71], a[40:43], a[112:115], v[68:71]\n"
"buffer_load_dwordx4 v11, s[52:55], s67 offen offset:2048  lds\n"
"v_mfma_f32_16x16x32_bf16 v[72:75], a[40:43], a[120:123], v[72:75]\n"
"v_mfma_f32_16x16x32_bf16 v[72:75], a[44:47], a[124:127], v[72:75]\n"
"s_sub_u32 s67, 1618944, s75\n"
"v_mfma_f32_16x16x32_bf16 v[44:47], a[36:39], a[124:127], v[44:47]\n"
"v_mfma_f32_16x16x32_bf16 v[44:47], a[32:35], a[120:123], v[44:47]\n"
"v_mfma_f32_16x16x32_bf16 v[48:51], a[32:35], a[128:131], v[48:51]\n"
"buffer_load_dwordx4 v11, s[52:55], s67 offen offset:3072  lds\n"
"v_mfma_f32_16x16x32_bf16 v[48:51], a[36:39], a[132:135], v[48:51]\n"
"s_add_u32 m0, s64, 12288\n"
"v_mfma_f32_16x16x32_bf16 v[76:79], a[44:47], a[132:135], v[76:79]\n"
"s_waitcnt vmcnt(12)\n"
"ds_read_b64_tr_b16 a[64:65], v9 offset:0\n"
"v_mfma_f32_16x16x32_bf16 v[76:79], a[40:43], a[128:131], v[76:79]\n"
"s_sub_u32 s67, 1638400, s75\n"
"ds_read_b64_tr_b16 a[66:67], v9 offset:512\n"
"v_mfma_f32_16x16x32_bf16 v[80:83], a[40:43], a[136:139], v[80:83]\n"
"ds_read_b64_tr_b16 a[68:69], v9 offset:4096\n"
"v_mfma_f32_16x16x32_bf16 v[80:83], a[44:47], a[140:143], v[80:83]\n"
"ds_read_b64_tr_b16 a[70:71], v9 offset:4608\n"
"buffer_load_dwordx4 v11, s[52:55], s67 offen offset:0  lds\n"
"v_mfma_f32_16x16x32_bf16 v[52:55], a[36:39], a[140:143], v[52:55]\n"
"s_sub_u32 s67, 1653760, s75\n"
"ds_read_b64_tr_b16 a[72:73], v9 offset:8\n"
"v_mfma_f32_16x16x32_bf16 v[52:55], a[32:35], a[136:139], v[52:55]\n"
"s_waitcnt lgkmcnt(5)\n"
"ds_read_b64_tr_b16 a[74:75], v9 offset:520\n"
"v_mfma_f32_16x16x32_bf16 v[56:59], a[32:35], a[144:147], v[56:59]\n"
"L_st2680:\n"
"s_cbranch_execz L_retA\n"
"ds_read_b64_tr_b16 a[76:77], v9 offset:4104\n"
"v_mfma_f32_16x16x32_bf16 v[56:59], a[36:39], a[148:151], v[56:59]\n"
"ds_read_b64_tr_b16 a[78:79], v9 offset:4616\n"
"v_mfma_f32_16x16x32_bf16 v[84:87], a[44:47], a[148:151], v[84:87]\n"
"buffer_load_dwordx4 v11, s[52:55], s67 offen offset:1024  lds\n"
"v_mfma_f32_16x16x32_bf16 v[84:87], a[40:43], a[144:147], v[84:87]\n"
"ds_read_b64_tr_b16 a[80:81], v15 offset:0\n"
"v_mfma_f32_16x16x32_bf16 a[4:7], a[40:43], a[152:155], a[4:7]\n"
"s_sub_u32 s67, 1669120, s75\n"
"ds_read_b64_tr_b16 a[82:83], v15 offset:512\n"
"v_mfma_f32_16x16x32_bf16 a[4:7], a[44:47], a[156:159], a[4:7]\n"
"ds_read_b64_tr_b16 a[84:85], v15 offset:4096\n"
"v_mfma_f32_16x16x32_bf16 a[0:3], a[36:39], a[156:159], a[0:3]\n"
"ds_read_b64_tr_b16 a[86:87], v15 offset:4608\n"
"buffer_load_dwordx4 v11, s[52:55], s67 offen offset:2048  lds\n"
"v_mfma_f32_16x16x32_bf16 a[0:3], a[32:35], a[152:155], a[0:3]\n"
"ds_read_b64_tr_b16 a[88:89], v15 offset:8\n"
"v_mfma_f32_16x16x32_bf16 v[88:91], a[48:51], a[96:99], v[88:91]\n"
"s_sub_u32 s67, 1684480, s75\n"
"ds_read_b64_tr_b16 a[90:91], v15 offset:520\n"
"v_mfma_f32_16x16x32_bf16 v[88:91], a[52:55], a[100:103], v[88:91]\n"
"ds_read_b64_tr_b16 a[92:93], v15 offset:4104\n"
"v_mfma_f32_16x16x32_bf16 v[116:119], a[60:63], a[100:103], v[116:119]\n"
"ds_read_b64_tr_b16 a[94:95], v15 offset:4616\n"
"v_mfma_f32_16x16x32_bf16 v[116:119], a[56:59], a[96:99], v[116:119]\n"
"buffer_load_dwordx4 v11, s[52:55], s67 offen offset:3072  lds\n"
"v_mfma_f32_16x16x32_bf16 v[120:123], a[56:59], a[104:107], v[120:123]\n"
"v_mfma_f32_16x16x32_bf16 v[120:123], a[60:63], a[108:111], v[120:123]\n"
"s_add_u32 m0, s64, 0\n"
"v_mfma_f32_16x16x32_bf16 v[92:95], a[52:55], a[108:111], v[92:95]\n"
"v_mfma_f32_16x16x32_bf16 v[92:95], a[48:51], a[104:107], v[92:95]\n"
"s_sub_u32 s67, 1572992, s75\n"
"v_mfma_f32_16x16x32_bf16 v[96:99], a[48:51], a[112:115], v[96:99]\n"
"s_waitcnt lgkmcnt(0)\n"
"s_mov_b32 s99, 0\n"
"v_mfma_f32_16x16x32_bf16 v[96:99], a[52:55], a[116:119], v[96:99]\n"
"s_mov_b32 s98, 0\n"
"buffer_load_dwordx4 v11, s[52:55], s67 offen offset:0  lds\n"
"v_mfma_f32_16x16x32_bf16 v[124:127], a[60:63], a[116:119], v[124:127]\n"
"s_memtime s[76:77]\n"
"s_load_dword s98, s[96:97], 8320 glc\n"
"v_mfma_f32_16x16x32_bf16 v[124:127], a[56:59], a[112:115], v[124:127]\n"
"s_sub_u32 s67, 1588352, s75\n"
"v_mfma_f32_16x16x32_bf16 v[128:131], a[56:59], a[120:123], v[128:131]\n"
"v_mfma_f32_16x16x32_bf16 v[128:131], a[60:63], a[124:127], v[128:131]\n"
"buffer_load_dwordx4 v11, s[52:55], s67 offen offset:1024  lds\n"
"v_mfma_f32_16x16x32_bf16 v[100:103], a[52:55], a[124:127], v[100:103]\n"
"v_mfma_f32_16x16x32_bf16 v[100:103], a[48:51], a[120:123], v[100:103]\n"
"s_sub_u32 s67, 1603712, s75\n"
"v_mfma_f32_16x16x32_bf16 v[104:107], a[48:51], a[128:131], v[104:107]\n"
"v_mfma_f32_16x16x32_bf16 v[104:107], a[52:55], a[132:135], v[104:107]\n"
"v_mfma_f32_16x16x32_bf16 v[132:135], a[60:63], a[132:135], v[132:135]\n"
"buffer_load_dwordx4 v11, s[52:55], s67 offen offset:2048  lds\n"
"v_mfma_f32_16x16x32_bf16 v[132:135], a[56:59], a[128:131], v[132:135]\n"
"s_mov_b32 s29, s76\n"
"v_mfma_f32_16x16x32_bf16 v[136:139], a[56:59], a[136:139], v[136:139]\n"
"s_sub_u32 s67, 1619072, s75\n"
"v_mfma_f32_16x16x32_bf16 v[136:139], a[60:63], a[140:143], v[136:139]\n"
"buffer_load_dwordx4 v11, s[52:55], s67 offen offset:3072  lds\n"
"v_mfma_f32_16x16x32_bf16 v[108:111], a[52:55], a[140:143], v[108:111]\n"
"s_sub_u32 s99, s99, s98\n"
"s_sub_u32 s99, s99, s98\n"
"v_mfma_f32_16x16x32_bf16 v[108:111], a[48:51], a[136:139], v[108:111]\n"
"s_sub_u32 s99, s99, s98\n"
"s_add_u32 m0, s64, 4096\n"
"v_mfma_f32_16x16x32_bf16 v[112:115], a[48:51], a[144:147], v[112:115]\n"
"s_sub_u32 s99, s99, s98\n"
"v_mfma_f32_16x16x32_bf16 v[112:115], a[52:55], a[148:151], v[112:115]\n"
"s_sub_u32 s67, 1638528, s75\n"
"v_mfma_f32_16x16x32_bf16 v[140:143], a[60:63], a[148:151], v[140:143]\n"
"s_sub_u32 s99, s99, s98\n"
"s_sub_u32 s99, s99, s98\n"
"v_mfma_f32_16x16x32_bf16 v[140:143], a[56:59], a[144:147], v[140:143]\n"
"s_sub_u32 s99, s99, s98\n"
"buffer_load_dwordx4 v11, s[52:55], s67 offen offset:0  lds\n"
"v_mfma_f32_16x16x32_bf16 a[12:15], a[56:59], a[152:155], a[12:15]\n"
"s_sub_u32 s99, s99, s98\n"
"v_mfma_f32_16x16x32_bf16 a[12:15], a[60:63], a[156:159], a[12:15]\n"
"s_sub_u32 s67, 1653888, s75\n"
"v_mfma_f32_16x16x32_bf16 a[8:11], a[52:55], a[156:159], a[8:11]\n"
"s_sub_u32 s99, s99, s98\n"
"s_sub_u32 s99, s99, s98\n"
"v_mfma_f32_16x16x32_bf16 a[8:11], a[48:51], a[152:155], a[8:11]\n"
"s_sub_u32 s99, s99, s98\n"
"v_mfma_f32_16x16x32_bf16 v[144:147], a[64:67], a[96:99], v[144:147]\n"
"s_sub_u32 s99, s99, s98\n"
"v_mfma_f32_16x16x32_bf16 v[144:147], a[68:71], a[100:103], v[144:147]\n"
"buffer_load_dwordx4 v11, s[52:55], s67 offen offset:1024  lds\n"
"v_mfma_f32_16x16x32_bf16 v[172:175], a[76:79], a[100:103], v[172:175]\n"
"s_sub_u32 s99, s99, s98\n"
"s_sub_u32 s99, s99, s98\n"
"v_mfma_f32_16x16x32_bf16 v[172:175], a[72:75], a[96:99], v[172:175]\n"
"s_sub_u32 s99, s99, s98\n"
"s_sub_u32 s67, 1669248, s75\n"
"v_mfma_f32_16x16x32_bf16 v[176:179], a[72:75], a[104:107], v[176:179]\n"
"s_sub_u32 s99, s99, s98\n"
"v_mfma_f32_16x16x32_bf16 v[176:179], a[76:79], a[108:111], v[176:179]\n"
"buffer_load_dwordx4 v11, s[52:55], s67 offen offset:2048  lds\n"
"v_mfma_f32_16x16x32_bf16 v[148:151], a[68:71], a[108:111], v[148:151]\n"
"s_sub_u32 s99, s99, s98\n"
"s_sub_u32 s99, s99, s98\n"
"v_mfma_f32_16x16x32_bf16 v[148:151], a[64:67], a[104:107], v[148:151]\n"
"s_sub_u32 s99, s99, s98\n"
"s_sub_u32 s71, s64, s64\n"
"s_sub_u32 s67, 1684608, s75\n"
"v_mfma_f32_16x16x32_bf16 v[152:155], a[64:67], a[112:115], v[152:155]\n"
"s_sub_u32 s66, 81920, s73\n"
"s_sub_u32 s99, s99, s98\n"
"v_mfma_f32_16x16x32_bf16 v[152:155], a[68:71], a[116:119], v[152:155]\n"
"s_add_u32 s71, s71, 20480\n"
"buffer_load_dwordx4 v11, s[52:55], s67 offen offset:3072  lds\n"
"v_mfma_f32_16x16x32_bf16 v[180:183], a[76:79], a[116:119], v[180:183]\n"
"s_sub_u32 s99, s99, s98\n"
"v_add_u32 v12, s66, v7\n"
"s_sub_u32 s99, s99, s98\n"
"v_mfma_f32_16x16x32_bf16 v[180:183], a[72:75], a[112:115], v[180:183]\n"
"s_sub_u32 s99, s99, s98\n"
"s_cmp_lt_u32 %16, 2\n"
"s_cselect_b32 s67, 16384, 0\n"
"s_add_u32 s67, s67, s72\n"
"v_mfma_f32_16x16x32_bf16 v[184:187], a[72:75], a[120:123], v[184:187]\n"
"s_sub_u32 s99, s99, s98\n"
"v_add_u32 v13, s66, v8\n"
"s_add_u32 m0, s67, 139264\n"
"v_mfma_f32_16x16x32_bf16 v[184:187], a[76:79], a[124:127], v[184:187]\n"
"s_memtime s[76:77]\n"
"v_add_u32 v20, s71, v7\n"
"s_sub_u32 s67, 1792, s60\n"
"v_mfma_f32_16x16x32_bf16 v[156:159], a[68:71], a[124:127], v[156:159]\n"
"buffer_load_dwordx4 v6, s[48:51], s67 offen offset:0  lds\n"
"v_mfma_f32_16x16x32_bf16 v[156:159], a[64:67], a[120:123], v[156:159]\n"
"s_waitcnt lgkmcnt(0)\n"
"v_add_u32 v21, s71, v8\n"
"v_mfma_f32_16x16x32_bf16 v[160:163], a[64:67], a[128:131], v[160:163]\n"
"s_sub_u32 s29, s76, s29\n"
"s_sub_u32 s67, 17152, s60\n"
"v_mfma_f32_16x16x32_bf16 v[160:163], a[68:71], a[132:135], v[160:163]\n"
"s_cmp_lt_u32 s29, 620\n"
"s_cselect_b32 s99, s99, 0\n"
"v_mfma_f32_16x16x32_bf16 v[188:191], a[76:79], a[132:135], v[188:191]\n"
"s_max_u32 s31, s31, s99\n"
"buffer_load_dwordx4 v6, s[48:51], s67 offen offset:1024  lds\n"
"v_mfma_f32_16x16x32_bf16 v[188:191], a[72:75], a[128:131], v[188:191]\n"
"s_waitcnt vmcnt(18)\n"
"ds_read_b128 a[224:227], v20 offset:0\n"
"v_mfma_f32_16x16x32_bf16 v[192:195], a[72:75], a[136:139], v[192:195]\n"
"s_sub_u32 s67, 32512, s60\n"
"ds_read_b128 a[228:231], v21 offset:0\n"
"v_mfma_f32_16x16x32_bf16 v[192:195], a[76:79], a[140:143], v[192:195]\n"
"ds_read_b128 a[232:235], v20 offset:2048\n"
"v_mfma_f32_16x16x32_bf16 v[164:167], a[68:71], a[140:143], v[164:167]\n"
"v_mfma_f32_16x16x32_bf16 v[164:167], a[64:67], a[136:139], v[164:167]\n"
"ds_read_b128 a[236:239], v21 offset:2048\n"
"v_mfma_f32_16x16x32_bf16 v[168:171], a[64:67], a[144:147], v[168:171]\n"
"buffer_load_dwordx4 v6, s[48:51], s67 offen offset:2048  lds\n"
"v_mfma_f32_16x16x32_bf16 v[168:171], a[68:71], a[148:151], v[168:171]\n"
"v_mfma_f32_16x16x32_bf16 v[196:199], a[76:79], a[148:151], v[196:199]\n"
"s_sub_u32 s67, 47872, s60\n"
"v_mfma_f32_16x16x32_bf16 v[196:199], a[72:75], a[144:147], v[196:199]\n"
"v_mfma_f32_16x16x32_bf16 a[20:23], a[72:75], a[152:155], a[20:23]\n"
"buffer_load_dwordx4 v6, s[48:51], s67 offen offset:3072  lds\n"
"v_mfma_f32_16x16x32_bf16 a[20:23], a[76:79], a[156:159], a[20:23]\n"
"s_waitcnt lgkmcnt(0)\n"
"v_mfma_f32_16x16x32_bf16 a[16:19], a[68:71], a[156:159], a[16:19]\n"
"s_add_u32 m0, s64, 16256\n"
"v_mfma_f32_16x16x32_bf16 a[16:19], a[64:67], a[152:155], a[16:19]\n"
"s_barrier\n"
"s_waitcnt vmcnt(12)\n"
"ds_read_b64_tr_b16 a[32:33], v9 offset:8192\n"
"v_mfma_f32_16x16x32_bf16 v[200:203], a[80:83], a[96:99], v[200:203]\n"
"ds_read_b64_tr_b16 a[34:35], v9 offset:8704\n"
"buffer_load_dwordx4 v6, s[40:43], s92 offen offset:128  lds\n"
"v_mfma_f32_16x16x32_bf16 v[200:203], a[84:87], a[100:103], v[200:203]\n"
"ds_read_b64_tr_b16 a[36:37], v9 offset:12288\n"
"v_mfma_f32_16x16x32_bf16 v[228:231], a[92:95], a[100:103], v[228:231]\n"
"ds_read_b128 a[100:103], v13 offset:32768\n"
"v_mfma_f32_16x16x32_bf16 v[228:231], a[88:91], a[96:99], v[228:231]\n"
"ds_read_b128 a[96:99], v12 offset:32768\n"
"buffer_load_dwordx4 v6, s[40:43], s93 offen offset:1152  lds\n"
"v_mfma_f32_16x16x32_bf16 v[232:235], a[88:91], a[104:107], v[232:235]\n"
"ds_read_b64_tr_b16 a[38:39], v9 offset:12800\n"
"v_mfma_f32_16x16x32_bf16 v[232:235], a[92:95], a[108:111], v[232:235]\n"
"ds_read_b64_tr_b16 a[40:41], v9 offset:8200\n"
"v_mfma_f32_16x16x32_bf16 v[204:207], a[84:87], a[108:111], v[204:207]\n"
"ds_read_b128 a[108:111], v13 offset:34816\n"
"v_mfma_f32_16x16x32_bf16 v[204:207], a[80:83], a[104:107], v[204:207]\n"
"ds_read_b128 a[104:107], v12 offset:34816\n"
"v_mfma_f32_16x16x32_bf16 v[208:211], a[80:83], a[112:115], v[208:211]\n"
"ds_read_b64_tr_b16 a[42:43], v9 offset:8712\n"
"buffer_load_dwordx4 v6, s[40:43], s94 offen offset:2176  lds\n"
"v_mfma_f32_16x16x32_bf16 v[208:211], a[84:87], a[116:119], v[208:211]\n"
"ds_read_b64_tr_b16 a[44:45], v9 offset:12296\n"
"v_mfma_f32_16x16x32_bf16 v[236:239], a[92:95], a[116:119], v[236:239]\n"
"ds_read_b128 a[116:119], v13 offset:36864\n"
"v_mfma_f32_16x16x32_bf16 v[236:239], a[88:91], a[112:115], v[236:239]\n"
"ds_read_b128 a[112:115], v12 offset:36864\n"
"v_mfma_f32_16x16x32_bf16 v[240:243], a[88:91], a[120:123], v[240:243]\n"
"ds_read_b64_tr_b16 a[46:47], v9 offset:12808\n"
"v_mfma_f32_16x16x32_bf16 v[240:243], a[92:95], a[124:127], v[240:243]\n"
"ds_read_b64_tr_b16 a[48:49], v15 offset:8192\n"
"buffer_load_dwordx4 v6, s[40:43], s95 offen offset:3200  lds\n"
"v_mfma_f32_16x16x32_bf16 v[212:215], a[84:87], a[124:127], v[212:215]\n"
"ds_read_b128 a[124:127], v13 offset:38912\n"
"v_mfma_f32_16x16x32_bf16 v[212:215], a[80:83], a[120:123], v[212:215]\n"
"ds_read_b128 a[120:123], v12 offset:38912\n"
"v_mfma_f32_16x16x32_bf16 v[216:219], a[80:83], a[128:131], v[216:219]\n"
"ds_read_b64_tr_b16 a[50:51], v15 offset:8704\n"
"v_mfma_f32_16x16x32_bf16 v[216:219], a[84:87], a[132:135], v[216:219]\n"
"s_add_u32 m0, s64, 8192\n"
"ds_read_b64_tr_b16 a[52:53], v15 offset:12288\n"
"v_mfma_f32_16x16x32_bf16 v[244:247], a[92:95], a[132:135], v[244:247]\n"
"ds_read_b128 a[132:135], v13 offset:24576\n"
"v_mfma_f32_16x16x32_bf16 v[244:247], a[88:91], a[128:131], v[244:247]\n"
"ds_read_b128 a[128:131], v12 offset:24576\n"
"v_mfma_f32_16x16x32_bf16 v[248:251], a[88:91], a[136:139], v[248:251]\n"
"ds_read_b64_tr_b16 a[54:55], v15 offset:12800\n"
"v_mfma_f32_16x16x32_bf16 v[248:251], a[92:95], a[140:143], v[248:251]\n"
"ds_read_b64_tr_b16 a[56:57], v15 offset:8200\n"
"v_mfma_f32_16x16x32_bf16 v[220:223], a[84:87], a[140:143], v[220:223]\n"
"ds_read_b128 a[140:143], v13 offset:26624\n"
"v_mfma_f32_16x16x32_bf16 v[220:223], a[80:83], a[136:139], v[220:223]\n"
"ds_read_b128 a[136:139], v12 offset:26624\n"
"v_mfma_f32_16x16x32_bf16 v[224:227], a[80:83], a[144:147], v[224:227]\n"
"ds_read_b64_tr_b16 a[58:59], v15 offset:8712\n"
"v_mfma_f32_16x16x32_bf16 v[224:227], a[84:87], a[148:151], v[224:227]\n"
"s_sub_u32 s67, 1835008, s75\n"
"ds_read_b64_tr_b16 a[60:61], v15 offset:12296\n"
"v_mfma_f32_16x16x32_bf16 v[252:255], a[92:95], a[148:151], v[252:255]\n"
"ds_read_b128 a[148:151], v13 offset:28672\n"
"v_mfma_f32_16x16x32_bf16 v[252:255], a[88:91], a[144:147], v[252:255]\n"
"ds_read_b128 a[144:147], v12 offset:28672\n"
"ds_read_b64_tr_b16 a[62:63], v15 offset:12808\n"
"v_mfma_f32_16x16x32_bf16 a[28:31], a[88:91], a[152:155], a[28:31]\n"
"v_mfma_f32_16x16x32_bf16 a[28:31], a[92:95], a[156:159], a[28:31]\n"
"v_mfma_f32_16x16x32_bf16 a[24:27], a[84:87], a[156:159], a[24:27]\n"
"ds_read_b128 a[156:159], v13 offset:30720\n"
"v_mfma_f32_16x16x32_bf16 a[24:27], a[80:83], a[152:155], a[24:27]\n"
"s_waitcnt lgkmcnt(15)\n"
"ds_read_b128 a[152:155], v12 offset:30720\n"
"v_mfma_f32_16x16x32_bf16 v[32:35], a[32:35], a[96:99], v[32:35]\n"
"v_mfma_f32_16x16x32_bf16 v[32:35], a[36:39], a[100:103], v[32:35]\n"
"s_waitcnt lgkmcnt(2)\n"
"v_mfma_f32_16x16x32_bf16 v[60:63], a[44:47], a[100:103], v[60:63]\n"
"buffer_load_dwordx4 v11, s[52:55], s67 offen offset:0  lds\n"
"v_mfma_f32_16x16x32_bf16 v[60:63], a[40:43], a[96:99], v[60:63]\n"
"v_mfma_f32_16x16x32_bf16 v[64:67], a[40:43], a[104:107], v[64:67]\n"
"s_sub_u32 s67, 1850368, s75\n"
"v_mfma_f32_16x16x32_bf16 v[64:67], a[44:47], a[108:111], v[64:67]\n"
"v_mfma_f32_16x16x32_bf16 v[36:39], a[36:39], a[108:111], v[36:39]\n"
"v_mfma_f32_16x16x32_bf16 v[36:39], a[32:35], a[104:107], v[36:39]\n"
"buffer_load_dwordx4 v11, s[52:55], s67 offen offset:1024  lds\n"
"v_mfma_f32_16x16x32_bf16 v[40:43], a[32:35], a[112:115], v[40:43]\n"
"v_mfma_f32_16x16x32_bf16 v[40:43], a[36:39], a[116:119], v[40:43]\n"
"s_sub_u32 s67, 1865728, s75\n"
"v_mfma_f32_16x16x32_bf16 v[68:71], a[44:47], a[116:119], v[68:71]\n"
"v_mfma_f32_16x16x32_bf16 v[68:71], a[40:43], a[112:115], v[68:71]\n"
"buffer_load_dwordx4 v11, s[52:55], s67 offen offset:2048  lds\n"
"v_mfma_f32_16x16x32_bf16 v[72:75], a[40:43], a[120:123], v[72:75]\n"
"v_mfma_f32_16x16x32_bf16 v[72:75], a[44:47], a[124:127], v[72:75]\n"
"s_sub_u32 s67, 1881088, s75\n"
"v_mfma_f32_16x16x32_bf16 v[44:47], a[36:39], a[124:127], v[44:47]\n"
"v_mfma_f32_16x16x32_bf16 v[44:47], a[32:35], a[120:123], v[44:47]\n"
"v_mfma_f32_16x16x32_bf16 v[48:51], a[32:35], a[128:131], v[48:51]\n"
"buffer_load_dwordx4 v11, s[52:55], s67 offen offset:3072  lds\n"
"v_mfma_f32_16x16x32_bf16 v[48:51], a[36:39], a[132:135], v[48:51]\n"
"s_add_u32 m0, s64, 12288\n"
"v_mfma_f32_16x16x32_bf16 v[76:79], a[44:47], a[132:135], v[76:79]\n"
"s_waitcnt vmcnt(12)\n"
"ds_read_b64_tr_b16 a[64:65], v9 offset:0\n"
"v_mfma_f32_16x16x32_bf16 v[76:79], a[40:43], a[128:131], v[76:79]\n"
"ds_read_b64_tr_b16 a[66:67], v9 offset:512\n"
"v_mfma_f32_16x16x32_bf16 v[80:83], a[40:43], a[136:139], v[80:83]\n"
"s_sub_u32 s67, 1900544, s75\n"
"ds_read_b64_tr_b16 a[68:69], v9 offset:4096\n"
"v_mfma_f32_16x16x32_bf16 v[80:83], a[44:47], a[140:143], v[80:83]\n"
"ds_read_b64_tr_b16 a[70:71], v9 offset:4608\n"
"v_mfma_f32_16x16x32_bf16 v[52:55], a[36:39], a[140:143], v[52:55]\n"
"ds_read_b64_tr_b16 a[72:73], v9 offset:8\n"
"buffer_load_dwordx4 v11, s[52:55], s67 offen offset:0  lds\n"
"v_mfma_f32_16x16x32_bf16 v[52:55], a[32:35], a[136:139], v[52:55]\n"
"s_waitcnt lgkmcnt(5)\n"
"ds_read_b64_tr_b16 a[74:75], v9 offset:520\n"
"v_mfma_f32_16x16x32_bf16 v[56:59], a[32:35], a[144:147], v[56:59]\n"
"s_sub_u32 s67, 1915904, s75\n"
"ds_read_b64_tr_b16 a[76:77], v9 offset:4104\n"
"v_mfma_f32_16x16x32_bf16 v[56:59], a[36:39], a[148:151], v[56:59]\n"
"ds_read_b64_tr_b16 a[78:79], v9 offset:4616\n"
"v_mfma_f32_16x16x32_bf16 v[84:87], a[44:47], a[148:151], v[84:87]\n"
"buffer_load_dwordx4 v11, s[52:55], s67 offen offset:1024  lds\n"
"v_mfma_f32_16x16x32_bf16 v[84:87], a[40:43], a[144:147], v[84:87]\n"
"ds_read_b64_tr_b16 a[80:81], v15 offset:0\n"
"v_mfma_f32_16x16x32_bf16 a[4:7], a[40:43], a[152:155], a[4:7]\n"
"s_sub_u32 s67, 1931264, s75\n"
"ds_read_b64_tr_b16 a[82:83], v15 offset:512\n"
"v_mfma_f32_16x16x32_bf16 a[4:7], a[44:47], a[156:159], a[4:7]\n"
"ds_read_b64_tr_b16 a[84:85], v15 offset:4096\n"
"v_mfma_f32_16x16x32_bf16 a[0:3], a[36:39], a[156:159], a[0:3]\n"
"ds_read_b64_tr_b16 a[86:87], v15 offset:4608\n"
"buffer_load_dwordx4 v11, s[52:55], s67 offen offset:2048  lds\n"
"v_mfma_f32_16x16x32_bf16 a[0:3], a[32:35], a[152:155], a[0:3]\n"
"ds_read_b64_tr_b16 a[88:89], v15 offset:8\n"
"v_mfma_f32_16x16x32_bf16 v[88:91], a[48:51], a[96:99], v[88:91]\n"
"s_sub_u32 s67, 1946624, s75\n"
"ds_read_b64_tr_b16 a[90:91], v15 offset:520\n"
"v_mfma_f32_16x16x32_bf16 v[88:91], a[52:55], a[100:103], v[88:91]\n"
"ds_read_b64_tr_b16 a[92:93], v15 offset:4104\n"
"v_mfma_f32_16x16x32_bf16 v[116:119], a[60:63], a[100:103], v[116:119]\n"
"ds_read_b64_tr_b16 a[94:95], v15 offset:4616\n"
"v_mfma_f32_16x16x32_bf16 v[116:119], a[56:59], a[96:99], v[116:119]\n"
"buffer_load_dwordx4 v11, s[52:55], s67 offen offset:3072  lds\n"
"v_mfma_f32_16x16x32_bf16 v[120:123], a[56:59], a[104:107], v[120:123]\n"
"v_mfma_f32_16x16x32_bf16 v[120:123], a[60:63], a[108:111], v[120:123]\n"
"s_add_u32 m0, s64, 0\n"
"v_mfma_f32_16x16x32_bf16 v[92:95], a[52:55], a[108:111], v[92:95]\n"
"v_mfma_f32_16x16x32_bf16 v[92:95], a[48:51], a[104:107], v[92:95]\n"
"s_sub_u32 s67, 1835136, s75\n"
"v_mfma_f32_16x16x32_bf16 v[96:99], a[48:51], a[112:115], v[96:99]\n"
"s_waitcnt lgkmcnt(0)\n"
"s_mov_b32 s99, 0\n"
"v_mfma_f32_16x16x32_bf16 v[96:99], a[52:55], a[116:119], v[96:99]\n"
"s_mov_b32 s98, 0\n"
"buffer_load_dwordx4 v11, s[52:55], s67 offen offset:0  lds\n"
"v_mfma_f32_16x16x32_bf16 v[124:127], a[60:63], a[116:119], v[124:127]\n"
"s_memtime s[76:77]\n"
"s_load_dword s98, s[96:97], 128 glc\n"
"v_mfma_f32_16x16x32_bf16 v[124:127], a[56:59], a[112:115], v[124:127]\n"
"s_sub_u32 s67, 1850496, s75\n"
"v_mfma_f32_16x16x32_bf16 v[128:131], a[56:59], a[120:123], v[128:131]\n"
"v_mfma_f32_16x16x32_bf16 v[128:131], a[60:63], a[124:127], v[128:131]\n"
"v_mfma_f32_16x16x32_bf16 v[100:103], a[52:55], a[124:127], v[100:103]\n"
"buffer_load_dwordx4 v11, s[52:55], s67 offen offset:1024  lds\n"
"v_mfma_f32_16x16x32_bf16 v[100:103], a[48:51], a[120:123], v[100:103]\n"
"v_mfma_f32_16x16x32_bf16 v[104:107], a[48:51], a[128:131], v[104:107]\n"
"s_sub_u32 s67, 1865856, s75\n"
"v_mfma_f32_16x16x32_bf16 v[104:107], a[52:55], a[132:135], v[104:107]\n"
"v_mfma_f32_16x16x32_bf16 v[132:135], a[60:63], a[132:135], v[132:135]\n"
"buffer_load_dwordx4 v11, s[52:55], s67 offen offset:2048  lds\n"
"v_mfma_f32_16x16x32_bf16 v[132:135], a[56:59], a[128:131], v[132:135]\n"
"s_mov_b32 s29, s76\n"
"v_mfma_f32_16x16x32_bf16 v[136:139], a[56:59], a[136:139], v[136:139]\n"
"s_sub_u32 s67, 1881216, s75\n"
"v_mfma_f32_16x16x32_bf16 v[136:139], a[60:63], a[140:143], v[136:139]\n"
"v_mfma_f32_16x16x32_bf16 v[108:111], a[52:55], a[140:143], v[108:111]\n"
"s_sub_u32 s99, s99, s98\n"
"s_sub_u32 s99, s99, s98\n"
"v_mfma_f32_16x16x32_bf16 v[108:111], a[48:51], a[136:139], v[108:111]\n"
"s_sub_u32 s99, s99, s98\n"
"buffer_load_dwordx4 v11, s[52:55], s67 offen offset:3072  lds\n"
"v_mfma_f32_16x16x32_bf16 v[112:115], a[48:51], a[144:147], v[112:115]\n"
"s_sub_u32 s99, s99, s98\n"
"s_add_u32 m0, s64, 4096\n"
"v_mfma_f32_16x16x32_bf16 v[112:115], a[52:55], a[148:151], v[112:115]\n"
"v_mfma_f32_16x16x32_bf16 v[140:143], a[60:63], a[148:151], v[140:143]\n"
"s_sub_u32 s99, s99, s98\n"
"s_sub_u32 s99, s99, s98\n"
"buffer_load_dwordx4 v[24:27], v0, s[56:59], %25 offen sc1\n"
"v_mfma_f32_16x16x32_bf16 v[140:143], a[56:59], a[144:147], v[140:143]\n"
"s_sub_u32 s99, s99, s98\n"
"s_sub_u32 s67, 1900672, s75\n"
"v_mfma_f32_16x16x32_bf16 a[12:15], a[56:59], a[152:155], a[12:15]\n"
"s_sub_u32 s99, s99, s98\n"
"buffer_load_dwordx4 v11, s[52:55], s67 offen offset:0  lds\n"
"v_mfma_f32_16x16x32_bf16 a[12:15], a[60:63], a[156:159], a[12:15]\n"
"s_sub_u32 s67, 1916032, s75\n"
"v_mfma_f32_16x16x32_bf16 a[8:11], a[52:55], a[156:159], a[8:11]\n"
"s_sub_u32 s99, s99, s98\n"
"s_sub_u32 s99, s99, s98\n"
"v_mfma_f32_16x16x32_bf16 a[8:11], a[48:51], a[152:155], a[8:11]\n"
"s_sub_u32 s99, s99, s98\n"
"v_mfma_f32_16x16x32_bf16 v[144:147], a[64:67], a[96:99], v[144:147]\n"
"s_sub_u32 s99, s99, s98\n"
"v_mfma_f32_16x16x32_bf16 v[144:147], a[68:71], a[100:103], v[144:147]\n"
"buffer_load_dwordx4 v11, s[52:55], s67 offen offset:1024  lds\n"
"v_mfma_f32_16x16x32_bf16 v[172:175], a[76:79], a[100:103], v[172:175]\n"
"s_sub_u32 s99, s99, s98\n"
"s_sub_u32 s99, s99, s98\n"
"v_mfma_f32_16x16x32_bf16 v[172:175], a[72:75], a[96:99], v[172:175]\n"
"s_sub_u32 s99, s99, s98\n"
"s_sub_u32 s67, 1931392, s75\n"
"v_mfma_f32_16x16x32_bf16 v[176:179], a[72:75], a[104:107], v[176:179]\n"
"s_sub_u32 s99, s99, s98\n"
"v_mfma_f32_16x16x32_bf16 v[176:179], a[76:79], a[108:111], v[176:179]\n"
"buffer_load_dwordx4 v11, s[52:55], s67 offen offset:2048  lds\n"
"v_mfma_f32_16x16x32_bf16 v[148:151], a[68:71], a[108:111], v[148:151]\n"
"s_sub_u32 s99, s99, s98\n"
"s_sub_u32 s99, s99, s98\n"
"v_mfma_f32_16x16x32_bf16 v[148:151], a[64:67], a[104:107], v[148:151]\n"
"s_sub_u32 s99, s99, s98\n"
"s_sub_u32 s71, s64, s64\n"
"s_sub_u32 s67, 1946752, s75\n"
"v_mfma_f32_16x16x32_bf16 v[152:155], a[64:67], a[112:115], v[152:155]\n"
"s_sub_u32 s66, 122880, s73\n"
"s_sub_u32 s99, s99, s98\n"
"v_mfma_f32_16x16x32_bf16 v[152:155], a[68:71], a[116:119], v[152:155]\n"
"s_add_u32 s71, s71, 16384\n"
"buffer_load_dwordx4 v11, s[52:55], s67 offen offset:3072  lds\n"
"v_mfma_f32_16x16x32_bf16 v[180:183], a[76:79], a[116:119], v[180:183]\n"
"s_sub_u32 s99, s99, s98\n"
"v_add_u32 v12, s66, v7\n"
"s_sub_u32 s99, s99, s98\n"
"v_mfma_f32_16x16x32_bf16 v[180:183], a[72:75], a[112:115], v[180:183]\n"
"s_sub_u32 s99, s99, s98\n"
"v_min_u32 v19, 496, v0\n"
"v_mfma_f32_16x16x32_bf16 v[184:187], a[72:75], a[120:123], v[184:187]\n"
"s_sub_u32 s99, s99, s98\n"
"v_add_u32 v13, s66, v8\n"
"s_add_u32 s70, %21, 0\n"
"v_mfma_f32_16x16x32_bf16 v[184:187], a[76:79], a[124:127], v[184:187]\n"
"s_memtime s[76:77]\n"
"v_add_u32 v20, s71, v7\n"
"v_lshl_add_u32 v19, v19, 7, s70\n"
"v_mfma_f32_16x16x32_bf16 v[156:159], a[68:71], a[124:127], v[156:159]\n"
"buffer_load_dword v23, v19, s[52:55], 0 offen\n"
"v_mfma_f32_16x16x32_bf16 v[156:159], a[64:67], a[120:123], v[156:159]\n"
"s_waitcnt lgkmcnt(0)\n"
"v_add_u32 v21, s71, v8\n"
"v_min_u32 v19, 496, v0\n"
"v_mfma_f32_16x16x32_bf16 v[160:163], a[64:67], a[128:131], v[160:163]\n"
"s_sub_u32 s29, s76, s29\n"
"s_add_u32 s70, %21, 2097152\n"
"v_lshl_add_u32 v19, v19, 7, s70\n"
"v_mfma_f32_16x16x32_bf16 v[160:163], a[68:71], a[132:135], v[160:163]\n"
"s_cmp_lt_u32 s29, 620\n"
"s_cselect_b32 s99, s99, 0\n"
"buffer_load_dword v23, v19, s[52:55], 0 offen\n"
"v_mfma_f32_16x16x32_bf16 v[188:191], a[76:79], a[132:135], v[188:191]\n"
"s_max_u32 s30, s30, s99\n"
"v_mfma_f32_16x16x32_bf16 v[188:191], a[72:75], a[128:131], v[188:191]\n"
"s_waitcnt vmcnt(19)\n"
"ds_read_b128 a[240:243], v20 offset:0\n"
"v_mfma_f32_16x16x32_bf16 v[192:195], a[72:75], a[136:139], v[192:195]\n"
"ds_read_b128 a[244:247], v21 offset:0\n"
"v_mfma_f32_16x16x32_bf16 v[192:195], a[76:79], a[140:143], v[192:195]\n"
"ds_read_b128 a[248:251], v20 offset:2048\n"
"v_mfma_f32_16x16x32_bf16 v[164:167], a[68:71], a[140:143], v[164:167]\n"
"v_mfma_f32_16x16x32_bf16 v[164:167], a[64:67], a[136:139], v[164:167]\n"
"ds_read_b128 a[252:255], v21 offset:2048\n"
"v_mfma_f32_16x16x32_bf16 v[168:171], a[64:67], a[144:147], v[168:171]\n"
"v_mfma_f32_16x16x32_bf16 v[168:171], a[68:71], a[148:151], v[168:171]\n"
"v_mfma_f32_16x16x32_bf16 v[196:199], a[76:79], a[148:151], v[196:199]\n"
"v_mfma_f32_16x16x32_bf16 v[196:199], a[72:75], a[144:147], v[196:199]\n"
"v_mfma_f32_16x16x32_bf16 a[20:23], a[72:75], a[152:155], a[20:23]\n"
"v_mfma_f32_16x16x32_bf16 a[20:23], a[76:79], a[156:159], a[20:23]\n"
"s_waitcnt lgkmcnt(0)\n"
"v_mfma_f32_16x16x32_bf16 a[16:19], a[68:71], a[156:159], a[16:19]\n"
"v_mfma_f32_16x16x32_bf16 a[16:19], a[64:67], a[152:155], a[16:19]\n"
"s_barrier\n"
"s_waitcnt vmcnt(11)\n"
"ds_read_b64_tr_b16 a[32:33], v9 offset:8192\n"
"v_mfma_f32_16x16x32_bf16 v[200:203], a[80:83], a[96:99], v[200:203]\n"
"ds_read_b64_tr_b16 a[34:35], v9 offset:8704\n"
"v_mfma_f32_16x16x32_bf16 v[200:203], a[84:87], a[100:103], v[200:203]\n"
"ds_read_b64_tr_b16 a[36:37], v9 offset:12288\n"
"v_mfma_f32_16x16x32_bf16 v[228:231], a[92:95], a[100:103], v[228:231]\n"
"ds_read_b128 a[100:103], v13 offset:32768\n"
"v_mfma_f32_16x16x32_bf16 v[228:231], a[88:91], a[96:99], v[228:231]\n"
"ds_read_b128 a[96:99], v12 offset:32768\n"
"v_mfma_f32_16x16x32_bf16 v[232:235], a[88:91], a[104:107], v[232:235]\n"
"ds_read_b64_tr_b16 a[38:39], v9 offset:12800\n"
"v_mfma_f32_16x16x32_bf16 v[232:235], a[92:95], a[108:111], v[232:235]\n"
"ds_read_b64_tr_b16 a[40:41], v9 offset:8200\n"
"s_getpc_b64 s[70:71]\n"
"L_c11_:\n"
"s_add_u32 s70, s70, L_P6-L_c11_\n"
"s_addc_u32 s71, s71, 0\n"
"v_mfma_f32_16x16x32_bf16 v[204:207], a[84:87], a[108:111], v[204:207]\n"
"ds_read_b128 a[108:111], v13 offset:34816\n"
"v_mfma_f32_16x16x32_bf16 v[204:207], a[80:83], a[104:107], v[204:207]\n"
"s_lshr_b32 s66, %26, 3\n"
"ds_read_b128 a[104:107], v12 offset:34816\n"
"v_mfma_f32_16x16x32_bf16 v[208:211], a[80:83], a[112:115], v[208:211]\n"
"ds_read_b64_tr_b16 a[42:43], v9 offset:8712\n"
"v_mfma_f32_16x16x32_bf16 v[208:211], a[84:87], a[116:119], v[208:211]\n"
"ds_read_b64_tr_b16 a[44:45], v9 offset:12296\n"
"v_mfma_f32_16x16x32_bf16 v[236:239], a[92:95], a[116:119], v[236:239]\n"
"s_lshl2_add_u32 s66, s66, %16\n"
"ds_read_b128 a[116:119], v13 offset:36864\n"
"v_mfma_f32_16x16x32_bf16 v[236:239], a[88:91], a[112:115], v[236:239]\n"
"ds_read_b128 a[112:115], v12 offset:36864\n"
"v_mfma_f32_16x16x32_bf16 v[240:243], a[88:91], a[120:123], v[240:243]\n"
"ds_read_b64_tr_b16 a[46:47], v9 offset:12808\n"
"v_mfma_f32_16x16x32_bf16 v[240:243], a[92:95], a[124:127], v[240:243]\n"
"s_mul_i32 s66, s66, 384\n"
"ds_read_b64_tr_b16 a[48:49], v15 offset:8192\n"
"v_mfma_f32_16x16x32_bf16 v[212:215], a[84:87], a[124:127], v[212:215]\n"
"ds_read_b128 a[124:127], v13 offset:38912\n"
"v_mfma_f32_16x16x32_bf16 v[212:215], a[80:83], a[120:123], v[212:215]\n"
"s_min_u32 s66, s66, L_headF-L_P6-384\n"
"ds_read_b128 a[120:123], v12 offset:38912\n"
"v_mfma_f32_16x16x32_bf16 v[216:219], a[80:83], a[128:131], v[216:219]\n"
"ds_read_b64_tr_b16 a[50:51], v15 offset:8704\n"
"v_mfma_f32_16x16x32_bf16 v[216:219], a[84:87], a[132:135], v[216:219]\n"
"ds_read_b64_tr_b16 a[52:53], v15 offset:12288\n"
"v_mfma_f32_16x16x32_bf16 v[244:247], a[92:95], a[132:135], v[244:247]\n"
"ds_read_b128 a[132:135], v13 offset:24576\n"
"v_min_u32 v22, 32, v0\n"
"v_mfma_f32_16x16x32_bf16 v[244:247], a[88:91], a[128:131], v[244:247]\n"
"ds_read_b128 a[128:131], v12 offset:24576\n"
"v_mfma_f32_16x16x32_bf16 v[248:251], a[88:91], a[136:139], v[248:251]\n"
"ds_read_b64_tr_b16 a[54:55], v15 offset:12800\n"
"v_mfma_f32_16x16x32_bf16 v[248:251], a[92:95], a[140:143], v[248:251]\n"
"ds_read_b64_tr_b16 a[56:57], v15 offset:8200\n"
"v_lshl_add_u32 v22, v22, 3, s66\n"
"v_mfma_f32_16x16x32_bf16 v[220:223], a[84:87], a[140:143], v[220:223]\n"
"ds_read_b128 a[140:143], v13 offset:26624\n"
"v_mfma_f32_16x16x32_bf16 v[220:223], a[80:83], a[136:139], v[220:223]\n"
"ds_read_b128 a[136:139], v12 offset:26624\n"
"global_load_dword v23, v22, s[70:71]\n"
"v_mfma_f32_16x16x32_bf16 v[224:227], a[80:83], a[144:147], v[224:227]\n"
"ds_read_b64_tr_b16 a[58:59], v15 offset:8712\n"
"v_mfma_f32_16x16x32_bf16 v[224:227], a[84:87], a[148:151], v[224:227]\n"
"ds_read_b64_tr_b16 a[60:61], v15 offset:12296\n"
"v_mfma_f32_16x16x32_bf16 v[252:255], a[92:95], a[148:151], v[252:255]\n"
"ds_read_b128 a[148:151], v13 offset:28672\n"
"v_mfma_f32_16x16x32_bf16 v[252:255], a[88:91], a[144:147], v[252:255]\n"
"ds_read_b128 a[144:147], v12 offset:28672\n"
"ds_read_b64_tr_b16 a[62:63], v15 offset:12808\n"
"v_mfma_f32_16x16x32_bf16 a[28:31], a[88:91], a[152:155], a[28:31]\n"
"v_mfma_f32_16x16x32_bf16 a[28:31], a[92:95], a[156:159], a[28:31]\n"
"v_mfma_f32_16x16x32_bf16 a[24:27], a[84:87], a[156:159], a[24:27]\n"
"ds_read_b128 a[156:159], v13 offset:30720\n"
"v_mfma_f32_16x16x32_bf16 a[24:27], a[80:83], a[152:155], a[24:27]\n"
"s_waitcnt lgkmcnt(15)\n"
"ds_read_b128 a[152:155], v12 offset:30720\n"
"v_mfma_f32_16x16x32_bf16 v[32:35], a[32:35], a[96:99], v[32:35]\n"
"v_mfma_f32_16x16x32_bf16 v[32:35], a[36:39], a[100:103], v[32:35]\n"
"s_waitcnt lgkmcnt(2)\n"
"v_mfma_f32_16x16x32_bf16 v[60:63], a[44:47], a[100:103], v[60:63]\n"
"v_mfma_f32_16x16x32_bf16 v[60:63], a[40:43], a[96:99], v[60:63]\n"
"v_mfma_f32_16x16x32_bf16 v[64:67], a[40:43], a[104:107], v[64:67]\n"
"ds_read_b128 a[160:163], v7 offset:24576\n"
"v_mfma_f32_16x16x32_bf16 v[64:67], a[44:47], a[108:111], v[64:67]\n"
"v_mfma_f32_16x16x32_bf16 v[36:39], a[36:39], a[108:111], v[36:39]\n"
"ds_read_b128 a[164:167], v8 offset:24576\n"
"v_mfma_f32_16x16x32_bf16 v[36:39], a[32:35], a[104:107], v[36:39]\n"
"v_mfma_f32_16x16x32_bf16 v[40:43], a[32:35], a[112:115], v[40:43]\n"
"ds_read_b128 a[168:171], v7 offset:26624\n"
"v_mfma_f32_16x16x32_bf16 v[40:43], a[36:39], a[116:119], v[40:43]\n"
"ds_read_b128 a[172:175], v8 offset:26624\n"
"v_mfma_f32_16x16x32_bf16 v[68:71], a[44:47], a[116:119], v[68:71]\n"
"s_add_u32 s70, s78, 1\n"
"v_mfma_f32_16x16x32_bf16 v[68:71], a[40:43], a[112:115], v[68:71]\n"
"ds_read_b128 a[176:179], v7 offset:28672\n"
"v_mfma_f32_16x16x32_bf16 v[72:75], a[40:43], a[120:123], v[72:75]\n"
"ds_read_b128 a[180:183], v8 offset:28672\n"
"v_mfma_f32_16x16x32_bf16 v[72:75], a[44:47], a[124:127], v[72:75]\n"
"v_mfma_f32_16x16x32_bf16 v[44:47], a[36:39], a[124:127], v[44:47]\n"
"ds_read_b128 a[184:187], v7 offset:30720\n"
"v_mfma_f32_16x16x32_bf16 v[44:47], a[32:35], a[120:123], v[44:47]\n"
"v_mfma_f32_16x16x32_bf16 v[48:51], a[32:35], a[128:131], v[48:51]\n"
"ds_read_b128 a[188:191], v8 offset:30720\n"
"v_mfma_f32_16x16x32_bf16 v[48:51], a[36:39], a[132:135], v[48:51]\n"
"v_mfma_f32_16x16x32_bf16 v[76:79], a[44:47], a[132:135], v[76:79]\n"
"s_waitcnt vmcnt(3)\n"
"ds_read_b64_tr_b16 a[64:65], v9 offset:0\n"
"v_mfma_f32_16x16x32_bf16 v[76:79], a[40:43], a[128:131], v[76:79]\n"
"ds_read_b64_tr_b16 a[66:67], v9 offset:512\n"
"v_mfma_f32_16x16x32_bf16 v[80:83], a[40:43], a[136:139], v[80:83]\n"
"ds_read_b64_tr_b16 a[68:69], v9 offset:4096\n"
"v_mfma_f32_16x16x32_bf16 v[80:83], a[44:47], a[140:143], v[80:83]\n"
"ds_read_b64_tr_b16 a[70:71], v9 offset:4608\n"
"v_mfma_f32_16x16x32_bf16 v[52:55], a[36:39], a[140:143], v[52:55]\n"
"L_st3242:\n"
"s_cbranch_execz L_retA\n"
"ds_read_b64_tr_b16 a[72:73], v9 offset:8\n"
"v_subrev_u32 v24, s70, v24\n"
"v_mfma_f32_16x16x32_bf16 v[52:55], a[32:35], a[136:139], v[52:55]\n"
"s_waitcnt lgkmcnt(13)\n"
"ds_read_b64_tr_b16 a[74:75], v9 offset:520\n"
"v_xor_b32 v25, s79, v25\n"
"v_mfma_f32_16x16x32_bf16 v[56:59], a[32:35], a[144:147], v[56:59]\n"
"ds_read_b64_tr_b16 a[76:77], v9 offset:4104\n"
"v_subrev_u32 v26, s70, v26\n"
"v_mfma_f32_16x16x32_bf16 v[56:59], a[36:39], a[148:151], v[56:59]\n"
"ds_read_b64_tr_b16 a[78:79], v9 offset:4616\n"
"v_xor_b32 v27, s79, v27\n"
"v_mfma_f32_16x16x32_bf16 v[84:87], a[44:47], a[148:151], v[84:87]\n"
"ds_read_b64_tr_b16 a[80:81], v15 offset:0\n"
"v_or_b32 v24, v24, v26\n"
"v_mfma_f32_16x16x32_bf16 v[84:87], a[40:43], a[144:147], v[84:87]\n"
"ds_read_b64_tr_b16 a[82:83], v15 offset:512\n"
"v_or_b32 v25, v25, v27\n"
"v_mfma_f32_16x16x32_bf16 a[4:7], a[40:43], a[152:155], a[4:7]\n"
"ds_read_b64_tr_b16 a[84:85], v15 offset:4096\n"
"v_or_b32 v24, v24, v25\n"
"v_mfma_f32_16x16x32_bf16 a[4:7], a[44:47], a[156:159], a[4:7]\n"
"ds_read_b64_tr_b16 a[86:87], v15 offset:4608\n"
"v_cmp_lt_u32 vcc, 15, v24\n"
"v_mfma_f32_16x16x32_bf16 a[0:3], a[36:39], a[156:159], a[0:3]\n"
"ds_read_b64_tr_b16 a[88:89], v15 offset:8\n"
"s_cbranch_vccnz L_slow3\n"
"L_back3:\n"
"v_mfma_f32_16x16x32_bf16 a[0:3], a[32:35], a[152:155], a[0:3]\n"
"ds_read_b64_tr_b16 a[90:91], v15 offset:520\n"
"v_mfma_f32_16x16x32_bf16 v[88:91], a[48:51], a[96:99], v[88:91]\n"
"ds_read_b64_tr_b16 a[92:93], v15 offset:4104\n"
"v_mfma_f32_16x16x32_bf16 v[88:91], a[52:55], a[100:103], v[88:91]\n"
"global_load_dwordx4 a[32:35], v0, s[34:35] offset:-4096 sc1\n"
"s_barrier\n"
"v_mfma_f32_16x16x32_bf16 v[116:119], a[60:63], a[100:103], v[116:119]\n"
"ds_read_b64_tr_b16 a[94:95], v15 offset:4616\n"
"v_mfma_f32_16x16x32_bf16 v[116:119], a[56:59], a[96:99], v[116:119]\n"
"ds_write_b64 v2, a[192:193] offset:16384\n"
"v_mfma_f32_16x16x32_bf16 v[120:123], a[56:59], a[104:107], v[120:123]\n"
"v_mfma_f32_16x16x32_bf16 v[120:123], a[60:63], a[108:111], v[120:123]\n"
"ds_write_b64 v2, a[194:195] offset:16392\n"
"v_mfma_f32_16x16x32_bf16 v[92:95], a[52:55], a[108:111], v[92:95]\n"
"v_mfma_f32_16x16x32_bf16 v[92:95], a[48:51], a[104:107], v[92:95]\n"
"ds_write_b64 v2, a[196:197] offset:17408\n"
"v_mfma_f32_16x16x32_bf16 v[96:99], a[48:51], a[112:115], v[96:99]\n"
"s_waitcnt lgkmcnt(3)\n"
"v_mfma_f32_16x16x32_bf16 v[96:99], a[52:55], a[116:119], v[96:99]\n"
"global_load_dwordx4 a[36:39], v0, s[34:35] offset:-3072 sc1\n"
"v_mfma_f32_16x16x32_bf16 v[124:127], a[60:63], a[116:119], v[124:127]\n"
"ds_write_b64 v2, a[198:199] offset:17416\n"
"v_mfma_f32_16x16x32_bf16 v[124:127], a[56:59], a[112:115], v[124:127]\n"
"ds_write_b64 v2, a[200:201] offset:18432\n"
"v_mfma_f32_16x16x32_bf16 v[128:131], a[56:59], a[120:123], v[128:131]\n"
"v_mfma_f32_16x16x32_bf16 v[128:131], a[60:63], a[124:127], v[128:131]\n"
"ds_write_b64 v2, a[202:203] offset:18440\n"
"v_mfma_f32_16x16x32_bf16 v[100:103], a[52:55], a[124:127], v[100:103]\n"
"v_mfma_f32_16x16x32_bf16 v[100:103], a[48:51], a[120:123], v[100:103]\n"
"ds_write_b64 v2, a[204:205] offset:19456\n"
"v_mfma_f32_16x16x32_bf16 v[104:107], a[48:51], a[128:131], v[104:107]\n"
"ds_write_b64 v2, a[206:207] offset:19464\n"
"v_mfma_f32_16x16x32_bf16 v[104:107], a[52:55], a[132:135], v[104:107]\n"
"global_load_dwordx4 a[40:43], v0, s[34:35] offset:-2048 sc1\n"
"v_mfma_f32_16x16x32_bf16 v[132:135], a[60:63], a[132:135], v[132:135]\n"
"ds_write_b64 v2, a[208:209] offset:20480\n"
"v_mfma_f32_16x16x32_bf16 v[132:135], a[56:59], a[128:131], v[132:135]\n"
"v_mfma_f32_16x16x32_bf16 v[136:139], a[56:59], a[136:139], v[136:139]\n"
"ds_write_b64 v2, a[210:211] offset:20488\n"
"v_mfma_f32_16x16x32_bf16 v[136:139], a[60:63], a[140:143], v[136:139]\n"
"v_mfma_f32_16x16x32_bf16 v[108:111], a[52:55], a[140:143], v[108:111]\n"
"ds_write_b64 v2, a[212:213] offset:21504\n"
"v_mfma_f32_16x16x32_bf16 v[108:111], a[48:51], a[136:139], v[108:111]\n"
"v_mfma_f32_16x16x32_bf16 v[112:115], a[48:51], a[144:147], v[112:115]\n"
"ds_write_b64 v2, a[214:215] offset:21512\n"
"v_mfma_f32_16x16x32_bf16 v[112:115], a[52:55], a[148:151], v[112:115]\n"
"global_load_dwordx4 a[44:47], v0, s[34:35] offset:-1024 sc1\n"
"v_mfma_f32_16x16x32_bf16 v[140:143], a[60:63], a[148:151], v[140:143]\n"
"ds_write_b64 v2, a[216:217] offset:22528\n"
"v_mfma_f32_16x16x32_bf16 v[140:143], a[56:59], a[144:147], v[140:143]\n"
"v_mfma_f32_16x16x32_bf16 a[12:15], a[56:59], a[152:155], a[12:15]\n"
"ds_write_b64 v2, a[218:219] offset:22536\n"
"v_mfma_f32_16x16x32_bf16 a[12:15], a[60:63], a[156:159], a[12:15]\n"
"v_mfma_f32_16x16x32_bf16 a[8:11], a[52:55], a[156:159], a[8:11]\n"
"ds_write_b64 v2, a[220:221] offset:23552\n"
"v_mfma_f32_16x16x32_bf16 a[8:11], a[48:51], a[152:155], a[8:11]\n"
"v_mfma_f32_16x16x32_bf16 v[144:147], a[64:67], a[96:99], v[144:147]\n"
"ds_write_b64 v2, a[222:223] offset:23560\n"
"v_mfma_f32_16x16x32_bf16 v[144:147], a[68:71], a[100:103], v[144:147]\n"
"global_load_dwordx4 a[48:51], v0, s[34:35] offset:0 sc1\n"
"v_mfma_f32_16x16x32_bf16 v[172:175], a[76:79], a[100:103], v[172:175]\n"
"ds_write_b64 v2, a[224:225] offset:24576\n"
"v_mfma_f32_16x16x32_bf16 v[172:175], a[72:75], a[96:99], v[172:175]\n"
"ds_write_b64 v2, a[226:227] offset:24584\n"
"v_mfma_f32_16x16x32_bf16 v[176:179], a[72:75], a[104:107], v[176:179]\n"
"v_mfma_f32_16x16x32_bf16 v[176:179], a[76:79], a[108:111], v[176:179]\n"
"ds_write_b64 v2, a[228:229] offset:25600\n"
"v_mfma_f32_16x16x32_bf16 v[148:151], a[68:71], a[108:111], v[148:151]\n"
"v_mfma_f32_16x16x32_bf16 v[148:151], a[64:67], a[104:107], v[148:151]\n"
"ds_write_b64 v2, a[230:231] offset:25608\n"
"v_mfma_f32_16x16x32_bf16 v[152:155], a[64:67], a[112:115], v[152:155]\n"
"ds_write_b64 v2, a[232:233] offset:26624\n"
"v_mfma_f32_16x16x32_bf16 v[152:155], a[68:71], a[116:119], v[152:155]\n"
"global_load_dwordx4 a[52:55], v0, s[34:35] offset:1024 sc1\n"
"v_mfma_f32_16x16x32_bf16 v[180:183], a[76:79], a[116:119], v[180:183]\n"
"v_mfma_f32_16x16x32_bf16 v[180:183], a[72:75], a[112:115], v[180:183]\n"
"ds_write_b64 v2, a[234:235] offset:26632\n"
"v_mfma_f32_16x16x32_bf16 v[184:187], a[72:75], a[120:123], v[184:187]\n"
"v_mfma_f32_16x16x32_bf16 v[184:187], a[76:79], a[124:127], v[184:187]\n"
"ds_write_b64 v2, a[236:237] offset:27648\n"
"v_mfma_f32_16x16x32_bf16 v[156:159], a[68:71], a[124:127], v[156:159]\n"
"v_mfma_f32_16x16x32_bf16 v[156:159], a[64:67], a[120:123], v[156:159]\n"
"ds_write_b64 v2, a[238:239] offset:27656\n"
"v_mfma_f32_16x16x32_bf16 v[160:163], a[64:67], a[128:131], v[160:163]\n"
"ds_write_b64 v2, a[240:241] offset:28672\n"
"v_mfma_f32_16x16x32_bf16 v[160:163], a[68:71], a[132:135], v[160:163]\n"
"global_load_dwordx4 a[56:59], v0, s[34:35] offset:2048 sc1\n"
"v_mfma_f32_16x16x32_bf16 v[188:191], a[76:79], a[132:135], v[188:191]\n"
"ds_write_b64 v2, a[242:243] offset:28680\n"
"v_mfma_f32_16x16x32_bf16 v[188:191], a[72:75], a[128:131], v[188:191]\n"
"v_mfma_f32_16x16x32_bf16 v[192:195], a[72:75], a[136:139], v[192:195]\n"
"ds_write_b64 v2, a[244:245] offset:29696\n"
"v_mfma_f32_16x16x32_bf16 v[192:195], a[76:79], a[140:143], v[192:195]\n"
"v_mfma_f32_16x16x32_bf16 v[164:167], a[68:71], a[140:143], v[164:167]\n"
"ds_write_b64 v2, a[246:247] offset:29704\n"
"v_mfma_f32_16x16x32_bf16 v[164:167], a[64:67], a[136:139], v[164:167]\n"
"v_mfma_f32_16x16x32_bf16 v[168:171], a[64:67], a[144:147], v[168:171]\n"
"ds_write_b64 v2, a[248:249] offset:30720\n"
"v_mfma_f32_16x16x32_bf16 v[168:171], a[68:71], a[148:151], v[168:171]\n"
"global_load_dwordx4 a[60:63], v0, s[34:35] offset:3072 sc1\n"
"v_mfma_f32_16x16x32_bf16 v[196:199], a[76:79], a[148:151], v[196:199]\n"
"ds_write_b64 v2, a[250:251] offset:30728\n"
"v_mfma_f32_16x16x32_bf16 v[196:199], a[72:75], a[144:147], v[196:199]\n"
"v_mfma_f32_16x16x32_bf16 a[20:23], a[72:75], a[152:155], a[20:23]\n"
"s_add_u32 s34, s34, 8192\n"
"ds_write_b64 v2, a[252:253] offset:31744\n"
"v_mfma_f32_16x16x32_bf16 a[20:23], a[76:79], a[156:159], a[20:23]\n"
"v_mfma_f32_16x16x32_bf16 a[16:19], a[68:71], a[156:159], a[16:19]\n"
"ds_write_b64 v2, a[254:255] offset:31752\n"
"v_mfma_f32_16x16x32_bf16 a[16:19], a[64:67], a[152:155], a[16:19]\n"
"v_mfma_f32_16x16x32_bf16 v[200:203], a[80:83], a[96:99], v[200:203]\n"
"v_lshrrev_b32 v16, 4, v0\n"
"v_mfma_f32_16x16x32_bf16 v[200:203], a[84:87], a[100:103], v[200:203]\n"
"global_load_dwordx4 a[64:67], v0, s[34:35] offset:-4096 sc1\n"
"v_mfma_f32_16x16x32_bf16 v[228:231], a[92:95], a[100:103], v[228:231]\n"
"ds_read_b128 a[100:103], v2 offset:17408\n"
"v_mfma_f32_16x16x32_bf16 v[228:231], a[88:91], a[96:99], v[228:231]\n"
"ds_read_b128 a[96:99], v2 offset:16384\n"
"v_mfma_f32_16x16x32_bf16 v[232:235], a[88:91], a[104:107], v[232:235]\n"
"v_and_b32 v16, 0x30, v16\n"
"v_mfma_f32_16x16x32_bf16 v[232:235], a[92:95], a[108:111], v[232:235]\n"
"buffer_load_dwordx2 v[18:19], v16, s[44:47], s61 offen offset:128\n"
"v_mfma_f32_16x16x32_bf16 v[204:207], a[84:87], a[108:111], v[204:207]\n"
"ds_read_b128 a[108:111], v2 offset:19456\n"
"v_mfma_f32_16x16x32_bf16 v[204:207], a[80:83], a[104:107], v[204:207]\n"
"ds_read_b128 a[104:107], v2 offset:18432\n"
"v_mfma_f32_16x16x32_bf16 v[208:211], a[80:83], a[112:115], v[208:211]\n"
"v_lshrrev_b32 v20, 4, v0\n"
"v_mfma_f32_16x16x32_bf16 v[208:211], a[84:87], a[116:119], v[208:211]\n"
"global_load_dwordx4 a[68:71], v0, s[34:35] offset:-3072 sc1\n"
"v_mfma_f32_16x16x32_bf16 v[236:239], a[92:95], a[116:119], v[236:239]\n"
"ds_read_b128 a[116:119], v2 offset:21504\n"
"v_mfma_f32_16x16x32_bf16 v[236:239], a[88:91], a[112:115], v[236:239]\n"
"ds_read_b128 a[112:115], v2 offset:20480\n"
"v_mfma_f32_16x16x32_bf16 v[240:243], a[88:91], a[120:123], v[240:243]\n"
"v_and_b32 v20, 0x30, v20\n"
"v_mfma_f32_16x16x32_bf16 v[240:243], a[92:95], a[124:127], v[240:243]\n"
"buffer_load_dwordx2 v[22:23], v20, s[44:47], s61 offen offset:136\n"
"v_mfma_f32_16x16x32_bf16 v[212:215], a[84:87], a[124:127], v[212:215]\n"
"ds_read_b128 a[124:127], v2 offset:23552\n"
"v_mfma_f32_16x16x32_bf16 v[212:215], a[80:83], a[120:123], v[212:215]\n"
"ds_read_b128 a[120:123], v2 offset:22528\n"
"v_mfma_f32_16x16x32_bf16 v[216:219], a[80:83], a[128:131], v[216:219]\n"
"v_lshrrev_b32 v24, 4, v0\n"
"v_mfma_f32_16x16x32_bf16 v[216:219], a[84:87], a[132:135], v[216:219]\n"
"global_load_dwordx4 a[72:75], v0, s[34:35] offset:-2048 sc1\n"
"v_mfma_f32_16x16x32_bf16 v[244:247], a[92:95], a[132:135], v[244:247]\n"
"ds_read_b128 a[132:135], v2 offset:25600\n"
"v_and_b32 v24, 0x30, v24\n"
"v_mfma_f32_16x16x32_bf16 v[244:247], a[88:91], a[128:131], v[244:247]\n"
"ds_read_b128 a[128:131], v2 offset:24576\n"
"v_mfma_f32_16x16x32_bf16 v[248:251], a[88:91], a[136:139], v[248:251]\n"
"buffer_load_dwordx2 v[26:27], v24, s[44:47], s61 offen offset:192\n"
"v_mfma_f32_16x16x32_bf16 v[248:251], a[92:95], a[140:143], v[248:251]\n"
"v_lshrrev_b32 v28, 4, v0\n"
"v_mfma_f32_16x16x32_bf16 v[220:223], a[84:87], a[140:143], v[220:223]\n"
"ds_read_b128 a[140:143], v2 offset:27648\n"
"v_mfma_f32_16x16x32_bf16 v[220:223], a[80:83], a[136:139], v[220:223]\n"
"ds_read_b128 a[136:139], v2 offset:26624\n"
"v_mfma_f32_16x16x32_bf16 v[224:227], a[80:83], a[144:147], v[224:227]\n"
"v_and_b32 v28, 0x30, v28\n"
"v_mfma_f32_16x16x32_bf16 v[224:227], a[84:87], a[148:151], v[224:227]\n"
"global_load_dwordx4 a[76:79], v0, s[34:35] offset:-1024 sc1\n"
"v_mfma_f32_16x16x32_bf16 v[252:255], a[92:95], a[148:151], v[252:255]\n"
"ds_read_b128 a[148:151], v2 offset:29696\n"
"v_mfma_f32_16x16x32_bf16 v[252:255], a[88:91], a[144:147], v[252:255]\n"
"ds_read_b128 a[144:147], v2 offset:28672\n"
"v_mfma_f32_16x16x32_bf16 a[28:31], a[88:91], a[152:155], a[28:31]\n"
"buffer_load_dwordx2 v[30:31], v28, s[44:47], s61 offen offset:200\n"
"v_mfma_f32_16x16x32_bf16 a[28:31], a[92:95], a[156:159], a[28:31]\n"
"v_mfma_f32_16x16x32_bf16 a[24:27], a[84:87], a[156:159], a[24:27]\n"
"ds_read_b128 a[156:159], v2 offset:31744\n"
"v_mfma_f32_16x16x32_bf16 a[24:27], a[80:83], a[152:155], a[24:27]\n"
"s_waitcnt vmcnt(12) lgkmcnt(11)\n"
"ds_read_b128 a[152:155], v2 offset:30720\n"
"v_mfma_f32_16x16x32_bf16 v[32:35], a[32:35], a[96:99], v[32:35]\n"
"v_mfma_f32_16x16x32_bf16 v[32:35], a[36:39], a[100:103], v[32:35]\n"
"global_load_dwordx4 a[80:83], v0, s[34:35] offset:0 sc1\n"
"v_mfma_f32_16x16x32_bf16 v[60:63], a[44:47], a[100:103], v[60:63]\n"
"s_barrier\n"
"v_mfma_f32_16x16x32_bf16 v[60:63], a[40:43], a[96:99], v[60:63]\n"
"v_mfma_f32_16x16x32_bf16 v[64:67], a[40:43], a[104:107], v[64:67]\n"
"v_mfma_f32_16x16x32_bf16 v[64:67], a[44:47], a[108:111], v[64:67]\n"
"v_mfma_f32_16x16x32_bf16 v[36:39], a[36:39], a[108:111], v[36:39]\n"
"v_mfma_f32_16x16x32_bf16 v[36:39], a[32:35], a[104:107], v[36:39]\n"
"s_waitcnt lgkmcnt(8)\n"
"v_mfma_f32_16x16x32_bf16 v[40:43], a[32:35], a[112:115], v[40:43]\n"
"v_mfma_f32_16x16x32_bf16 v[40:43], a[36:39], a[116:119], v[40:43]\n"
"global_load_dwordx4 a[84:87], v0, s[34:35] offset:1024 sc1\n"
"v_mfma_f32_16x16x32_bf16 v[68:71], a[44:47], a[116:119], v[68:71]\n"
"v_mfma_f32_16x16x32_bf16 v[68:71], a[40:43], a[112:115], v[68:71]\n"
"v_mfma_f32_16x16x32_bf16 v[72:75], a[40:43], a[120:123], v[72:75]\n"
"v_mfma_f32_16x16x32_bf16 v[72:75], a[44:47], a[124:127], v[72:75]\n"
"s_add_u32 s61, s61, 2048\n"
"v_mfma_f32_16x16x32_bf16 v[44:47], a[36:39], a[124:127], v[44:47]\n"
"v_mfma_f32_16x16x32_bf16 v[44:47], a[32:35], a[120:123], v[44:47]\n"
"s_waitcnt lgkmcnt(4)\n"
"v_mfma_f32_16x16x32_bf16 v[48:51], a[32:35], a[128:131], v[48:51]\n"
"s_waitcnt vmcnt(8)\n"
"v_lshlrev_b32 v16, 16, v18\n"
"v_mfma_f32_16x16x32_bf16 v[48:51], a[36:39], a[132:135], v[48:51]\n"
"global_load_dwordx4 a[88:91], v0, s[34:35] offset:2048 sc1\n"
"v_and_b32 v17, s63, v18\n"
"v_mfma_f32_16x16x32_bf16 v[76:79], a[44:47], a[132:135], v[76:79]\n"
"v_lshlrev_b32 v18, 16, v19\n"
"v_and_b32 v19, s63, v19\n"
"v_mfma_f32_16x16x32_bf16 v[76:79], a[40:43], a[128:131], v[76:79]\n"
"v_add_f32 v144, v144, v16\n"
"v_mfma_f32_16x16x32_bf16 v[80:83], a[40:43], a[136:139], v[80:83]\n"
"v_add_f32 v145, v145, v17\n"
"v_add_f32 v146, v146, v18\n"
"v_mfma_f32_16x16x32_bf16 v[80:83], a[44:47], a[140:143], v[80:83]\n"
"v_add_f32 v147, v147, v19\n"
"v_mfma_f32_16x16x32_bf16 v[52:55], a[36:39], a[140:143], v[52:55]\n"
"v_add_f32 v148, v148, v16\n"
"v_mfma_f32_16x16x32_bf16 v[52:55], a[32:35], a[136:139], v[52:55]\n"
"s_mov_b32 s99, 0\n"
"v_add_f32 v149, v149, v17\n"
"s_waitcnt lgkmcnt(0)\n"
"v_mfma_f32_16x16x32_bf16 v[56:59], a[32:35], a[144:147], v[56:59]\n"
"s_mov_b32 s98, 0\n"
"v_add_f32 v150, v150, v18\n"
"v_add_f32 v151, v151, v19\n"
"v_mfma_f32_16x16x32_bf16 v[56:59], a[36:39], a[148:151], v[56:59]\n"
"global_load_dwordx4 a[92:95], v0, s[34:35] offset:3072 sc1\n"
"s_memtime s[76:77]\n"
"s_load_dword s98, s[96:97], 8448 glc\n"
"v_mfma_f32_16x16x32_bf16 v[84:87], a[44:47], a[148:151], v[84:87]\n"
"v_add_f32 v152, v152, v16\n"
"v_add_f32 v153, v153, v17\n"
"v_mfma_f32_16x16x32_bf16 v[84:87], a[40:43], a[144:147], v[84:87]\n"
"v_add_f32 v154, v154, v18\n"
"v_add_f32 v155, v155, v19\n"
"v_mfma_f32_16x16x32_bf16 a[4:7], a[40:43], a[152:155], a[4:7]\n"
"s_add_u32 s34, s34, 8192\n"
"v_add_f32 v156, v156, v16\n"
"v_mfma_f32_16x16x32_bf16 a[4:7], a[44:47], a[156:159], a[4:7]\n"
"v_add_f32 v157, v157, v17\n"
"v_mfma_f32_16x16x32_bf16 a[0:3], a[36:39], a[156:159], a[0:3]\n"
"v_add_f32 v158, v158, v18\n"
"v_add_f32 v159, v159, v19\n"
"v_mfma_f32_16x16x32_bf16 a[0:3], a[32:35], a[152:155], a[0:3]\n"
"v_add_f32 v160, v160, v16\n"
"v_mfma_f32_16x16x32_bf16 v[88:91], a[48:51], a[96:99], v[88:91]\n"
"v_add_f32 v161, v161, v17\n"
"v_add_f32 v162, v162, v18\n"
"v_mfma_f32_16x16x32_bf16 v[88:91], a[52:55], a[100:103], v[88:91]\n"
"global_load_dwordx4 a[32:35], v0, s[34:35] offset:-4096 sc1\n"
"v_add_f32 v163, v163, v19\n"
"v_mfma_f32_16x16x32_bf16 v[116:119], a[60:63], a[100:103], v[116:119]\n"
"s_mov_b32 s29, s76\n"
"ds_read_b128 a[100:103], v3 offset:17408\n"
"v_add_f32 v164, v164, v16\n"
"v_mfma_f32_16x16x32_bf16 v[116:119], a[56:59], a[96:99], v[116:119]\n"
"ds_read_b128 a[96:99], v3 offset:16384\n"
"v_add_f32 v165, v165, v17\n"
"v_mfma_f32_16x16x32_bf16 v[120:123], a[56:59], a[104:107], v[120:123]\n"
"s_sub_u32 s99, s99, s98\n"
"v_add_f32 v166, v166, v18\n"
"v_add_f32 v167, v167, v19\n"
"v_mfma_f32_16x16x32_bf16 v[120:123], a[60:63], a[108:111], v[120:123]\n"
"s_sub_u32 s99, s99, s98\n"
"v_add_f32 v168, v168, v16\n"
"v_add_f32 v169, v169, v17\n"
"v_mfma_f32_16x16x32_bf16 v[92:95], a[52:55], a[108:111], v[92:95]\n"
"s_sub_u32 s99, s99, s98\n"
"ds_read_b128 a[108:111], v3 offset:19456\n"
"v_add_f32 v170, v170, v18\n"
"v_mfma_f32_16x16x32_bf16 v[92:95], a[48:51], a[104:107], v[92:95]\n"
"s_sub_u32 s99, s99, s98\n"
"ds_read_b128 a[104:107], v3 offset:18432\n"
"v_add_f32 v171, v171, v19\n"
"v_mfma_f32_16x16x32_bf16 v[96:99], a[48:51], a[112:115], v[96:99]\n"
"s_sub_u32 s99, s99, s98\n"
"v_accvgpr_read_b32 v12, a16\n"
"v_accvgpr_read_b32 v13, a17\n"
"v_mfma_f32_16x16x32_bf16 v[96:99], a[52:55], a[116:119], v[96:99]\n"
"global_load_dwordx4 a[36:39], v0, s[34:35] offset:-3072 sc1\n"
"s_sub_u32 s99, s99, s98\n"
"v_mfma_f32_16x16x32_bf16 v[124:127], a[60:63], a[116:119], v[124:127]\n"
"s_sub_u32 s99, s99, s98\n"
"ds_read_b128 a[116:119], v3 offset:21504\n"
"v_accvgpr_read_b32 v14, a18\n"
"v_mfma_f32_16x16x32_bf16 v[124:127], a[56:59], a[112:115], v[124:127]\n"
"s_sub_u32 s99, s99, s98\n"
"ds_read_b128 a[112:115], v3 offset:20480\n"
"v_accvgpr_read_b32 v15, a19\n"
"v_mfma_f32_16x16x32_bf16 v[128:131], a[56:59], a[120:123], v[128:131]\n"
"s_sub_u32 s99, s99, s98\n"
"v_add_f32 v12, v12, v16\n"
"v_add_f32 v13, v13, v17\n"
"v_mfma_f32_16x16x32_bf16 v[128:131], a[60:63], a[124:127], v[128:131]\n"
"s_sub_u32 s99, s99, s98\n"
"v_add_f32 v14, v14, v18\n"
"v_add_f32 v15, v15, v19\n"
"v_mfma_f32_16x16x32_bf16 v[100:103], a[52:55], a[124:127], v[100:103]\n"
"s_sub_u32 s99, s99, s98\n"
"ds_read_b128 a[124:127], v3 offset:23552\n"
"v_accvgpr_write_b32 a16, v12\n"
"v_mfma_f32_16x16x32_bf16 v[100:103], a[48:51], a[120:123], v[100:103]\n"
"s_sub_u32 s99, s99, s98\n"
"ds_read_b128 a[120:123], v3 offset:22528\n"
"v_accvgpr_write_b32 a17, v13\n"
"v_mfma_f32_16x16x32_bf16 v[104:107], a[48:51], a[128:131], v[104:107]\n"
"s_sub_u32 s99, s99, s98\n"
"v_accvgpr_write_b32 a18, v14\n"
"v_accvgpr_write_b32 a19, v15\n"
"v_mfma_f32_16x16x32_bf16 v[104:107], a[52:55], a[132:135], v[104:107]\n"
"s_sub_u32 s99, s99, s98\n"
"s_waitcnt vmcnt(10)\n"
"global_load_dwordx4 a[40:43], v0, s[34:35] offset:-2048 sc1\n"
"v_mfma_f32_16x16x32_bf16 v[132:135], a[60:63], a[132:135], v[132:135]\n"
"s_sub_u32 s99, s99, s98\n"
"ds_read_b128 a[132:135], v3 offset:25600\n"
"v_lshlrev_b32 v20, 16, v22\n"
"v_mfma_f32_16x16x32_bf16 v[132:135], a[56:59], a[128:131], v[132:135]\n"
"s_sub_u32 s99, s99, s98\n"
"ds_read_b128 a[128:131], v3 offset:24576\n"
"v_and_b32 v21, s63, v22\n"
"v_mfma_f32_16x16x32_bf16 v[136:139], a[56:59], a[136:139], v[136:139]\n"
"s_sub_u32 s99, s99, s98\n"
"v_lshlrev_b32 v22, 16, v23\n"
"v_and_b32 v23, s63, v23\n"
"v_mfma_f32_16x16x32_bf16 v[136:139], a[60:63], a[140:143], v[136:139]\n"
"s_sub_u32 s99, s99, s98\n"
"v_add_f32 v172, v172, v20\n"
"v_add_f32 v173, v173, v21\n"
"v_mfma_f32_16x16x32_bf16 v[108:111], a[52:55], a[140:143], v[108:111]\n"
"s_sub_u32 s99, s99, s98\n"
"ds_read_b128 a[140:143], v3 offset:27648\n"
"v_add_f32 v174, v174, v22\n"
"v_mfma_f32_16x16x32_bf16 v[108:111], a[48:51], a[136:139], v[108:111]\n"
"s_sub_u32 s99, s99, s98\n"
"ds_read_b128 a[136:139], v3 offset:26624\n"
"v_add_f32 v175, v175, v23\n"
"v_mfma_f32_16x16x32_bf16 v[112:115], a[48:51], a[144:147], v[112:115]\n"
"s_sub_u32 s99, s99, s98\n"
"v_add_f32 v176, v176, v20\n"
"v_add_f32 v177, v177, v21\n"
"v_mfma_f32_16x16x32_bf16 v[112:115], a[52:55], a[148:151], v[112:115]\n"
"global_load_dwordx4 a[44:47], v0, s[34:35] offset:-1024 sc1\n"
"s_sub_u32 s99, s99, s98\n"
"v_mfma_f32_16x16x32_bf16 v[140:143], a[60:63], a[148:151], v[140:143]\n"
"s_sub_u32 s99, s99, s98\n"
"ds_read_b128 a[148:151], v3 offset:29696\n"
"v_add_f32 v178, v178, v22\n"
"v_mfma_f32_16x16x32_bf16 v[140:143], a[56:59], a[144:147], v[140:143]\n"
"s_sub_u32 s99, s99, s98\n"
"ds_read_b128 a[144:147], v3 offset:28672\n"
"v_add_f32 v179, v179, v23\n"
"v_mfma_f32_16x16x32_bf16 a[12:15], a[56:59], a[152:155], a[12:15]\n"
"s_memtime s[76:77]\n"
"v_add_f32 v180, v180, v20\n"
"v_add_f32 v181, v181, v21\n"
"v_mfma_f32_16x16x32_bf16 a[12:15], a[60:63], a[156:159], a[12:15]\n"
"v_add_f32 v182, v182, v22\n"
"v_add_f32 v183, v183, v23\n"
"v_mfma_f32_16x16x32_bf16 a[8:11], a[52:55], a[156:159], a[8:11]\n"
"ds_read_b128 a[156:159], v3 offset:31744\n"
"v_add_f32 v184, v184, v20\n"
"v_mfma_f32_16x16x32_bf16 a[8:11], a[48:51], a[152:155], a[8:11]\n"
"s_waitcnt vmcnt(9) lgkmcnt(11)\n"
"ds_read_b128 a[152:155], v3 offset:30720\n"
"v_add_f32 v185, v185, v21\n"
"v_mfma_f32_16x16x32_bf16 v[32:35], a[64:67], a[96:99], v[32:35]\n"
"v_add_f32 v186, v186, v22\n"
"v_add_f32 v187, v187, v23\n"
"v_mfma_f32_16x16x32_bf16 v[32:35], a[68:71], a[100:103], v[32:35]\n"
"global_load_dwordx4 a[48:51], v0, s[34:35] offset:0 sc1\n"
"v_add_f32 v188, v188, v20\n"
"v_mfma_f32_16x16x32_bf16 v[60:63], a[76:79], a[100:103], v[60:63]\n"
"v_add_f32 v189, v189, v21\n"
"v_mfma_f32_16x16x32_bf16 v[60:63], a[72:75], a[96:99], v[60:63]\n"
"v_add_f32 v190, v190, v22\n"
"v_add_f32 v191, v191, v23\n"
"v_mfma_f32_16x16x32_bf16 v[64:67], a[72:75], a[104:107], v[64:67]\n"
"v_add_f32 v192, v192, v20\n"
"v_mfma_f32_16x16x32_bf16 v[64:67], a[76:79], a[108:111], v[64:67]\n"
"v_add_f32 v193, v193, v21\n"
"v_mfma_f32_16x16x32_bf16 v[36:39], a[68:71], a[108:111], v[36:39]\n"
"v_add_f32 v194, v194, v22\n"
"v_add_f32 v195, v195, v23\n"
"v_mfma_f32_16x16x32_bf16 v[36:39], a[64:67], a[104:107], v[36:39]\n"
"s_waitcnt lgkmcnt(8)\n"
"v_add_f32 v196, v196, v20\n"
"v_mfma_f32_16x16x32_bf16 v[40:43], a[64:67], a[112:115], v[40:43]\n"
"v_add_f32 v197, v197, v21\n"
"v_mfma_f32_16x16x32_bf16 v[40:43], a[68:71], a[116:119], v[40:43]\n"
"global_load_dwordx4 a[52:55], v0, s[34:35] offset:1024 sc1\n"
"v_add_f32 v198, v198, v22\n"
"v_mfma_f32_16x16x32_bf16 v[68:71], a[76:79], a[116:119], v[68:71]\n"
"v_add_f32 v199, v199, v23\n"
"v_accvgpr_read_b32 v12, a20\n"
"v_mfma_f32_16x16x32_bf16 v[68:71], a[72:75], a[112:115], v[68:71]\n"
"v_accvgpr_read_b32 v13, a21\n"
"v_mfma_f32_16x16x32_bf16 v[72:75], a[72:75], a[120:123], v[72:75]\n"
"v_accvgpr_read_b32 v14, a22\n"
"v_accvgpr_read_b32 v15, a23\n"
"v_mfma_f32_16x16x32_bf16 v[72:75], a[76:79], a[124:127], v[72:75]\n"
"v_add_f32 v12, v12, v20\n"
"v_mfma_f32_16x16x32_bf16 v[44:47], a[68:71], a[124:127], v[44:47]\n"
"v_add_f32 v13, v13, v21\n"
"v_mfma_f32_16x16x32_bf16 v[44:47], a[64:67], a[120:123], v[44:47]\n"
"s_waitcnt lgkmcnt(4)\n"
"v_add_f32 v14, v14, v22\n"
"v_mfma_f32_16x16x32_bf16 v[48:51], a[64:67], a[128:131], v[48:51]\n"
"v_add_f32 v15, v15, v23\n"
"v_accvgpr_write_b32 a20, v12\n"
"v_mfma_f32_16x16x32_bf16 v[48:51], a[68:71], a[132:135], v[48:51]\n"
"global_load_dwordx4 a[56:59], v0, s[34:35] offset:2048 sc1\n"
"v_accvgpr_write_b32 a21, v13\n"
"v_mfma_f32_16x16x32_bf16 v[76:79], a[76:79], a[132:135], v[76:79]\n"
"s_waitcnt lgkmcnt(0)\n"
"v_accvgpr_write_b32 a22, v14\n"
"v_mfma_f32_16x16x32_bf16 v[76:79], a[72:75], a[128:131], v[76:79]\n"
"s_sub_u32 s29, s76, s29\n"
"v_accvgpr_write_b32 a23, v15\n"
"v_lshlrev_b32 v24, 16, v26\n"
"v_mfma_f32_16x16x32_bf16 v[80:83], a[72:75], a[136:139], v[80:83]\n"
"s_cmp_lt_u32 s29, 620\n"
"s_cselect_b32 s99, s99, 0\n"
"v_and_b32 v25, s63, v26\n"
"v_mfma_f32_16x16x32_bf16 v[80:83], a[76:79], a[140:143], v[80:83]\n"
"s_max_u32 s31, s31, s99\n"
"v_lshlrev_b32 v26, 16, v27\n"
"v_mfma_f32_16x16x32_bf16 v[52:55], a[68:71], a[140:143], v[52:55]\n"
"v_and_b32 v27, s63, v27\n"
"v_add_f32 v200, v200, v24\n"
"v_mfma_f32_16x16x32_bf16 v[52:55], a[64:67], a[136:139], v[52:55]\n"
"s_mov_b32 s99, 0\n"
"v_add_f32 v201, v201, v25\n"
"v_mfma_f32_16x16x32_bf16 v[56:59], a[64:67], a[144:147], v[56:59]\n"
"s_mov_b32 s98, 0\n"
"v_add_f32 v202, v202, v26\n"
"v_add_f32 v203, v203, v27\n"
"v_mfma_f32_16x16x32_bf16 v[56:59], a[68:71], a[148:151], v[56:59]\n"
"global_load_dwordx4 a[60:63], v0, s[34:35] offset:3072 sc1\n"
"s_memtime s[76:77]\n"
"s_load_dword s98, s[96:97], 256 glc\n"
"v_mfma_f32_16x16x32_bf16 v[84:87], a[76:79], a[148:151], v[84:87]\n"
"v_add_f32 v204, v204, v24\n"
"v_add_f32 v205, v205, v25\n"
"v_mfma_f32_16x16x32_bf16 v[84:87], a[72:75], a[144:147], v[84:87]\n"
"v_add_f32 v206, v206, v26\n"
"v_mfma_f32_16x16x32_bf16 a[4:7], a[72:75], a[152:155], a[4:7]\n"
"s_add_u32 s34, s34, 8192\n"
"v_add_f32 v207, v207, v27\n"
"v_mfma_f32_16x16x32_bf16 a[4:7], a[76:79], a[156:159], a[4:7]\n"
"v_add_f32 v208, v208, v24\n"
"v_add_f32 v209, v209, v25\n"
"v_mfma_f32_16x16x32_bf16 a[0:3], a[68:71], a[156:159], a[0:3]\n"
"v_add_f32 v210, v210, v26\n"
"v_mfma_f32_16x16x32_bf16 a[0:3], a[64:67], a[152:155], a[0:3]\n"
"s_waitcnt vmcnt(8)\n"
"v_add_f32 v211, v211, v27\n"
"v_mfma_f32_16x16x32_bf16 v[88:91], a[80:83], a[96:99], v[88:91]\n"
"v_add_f32 v212, v212, v24\n"
"v_add_f32 v213, v213, v25\n"
"v_mfma_f32_16x16x32_bf16 v[88:91], a[84:87], a[100:103], v[88:91]\n"
"global_load_dwordx4 a[64:67], v0, s[34:35] offset:-4096 sc1\n"
"v_add_f32 v214, v214, v26\n"
"v_mfma_f32_16x16x32_bf16 v[116:119], a[92:95], a[100:103], v[116:119]\n"
"s_mov_b32 s29, s76\n"
"ds_read_b128 a[100:103], v4 offset:17408\n"
"v_add_f32 v215, v215, v27\n"
"v_mfma_f32_16x16x32_bf16 v[116:119], a[88:91], a[96:99], v[116:119]\n"
"ds_read_b128 a[96:99], v4 offset:16384\n"
"v_add_f32 v216, v216, v24\n"
"v_mfma_f32_16x16x32_bf16 v[120:123], a[88:91], a[104:107], v[120:123]\n"
"s_sub_u32 s99, s99, s98\n"
"v_add_f32 v217, v217, v25\n"
"v_add_f32 v218, v218, v26\n"
"v_mfma_f32_16x16x32_bf16 v[120:123], a[92:95], a[108:111], v[120:123]\n"
"s_sub_u32 s99, s99, s98\n"
"v_add_f32 v219, v219, v27\n"
"v_add_f32 v220, v220, v24\n"
"v_mfma_f32_16x16x32_bf16 v[92:95], a[84:87], a[108:111], v[92:95]\n"
"s_sub_u32 s99, s99, s98\n"
"ds_read_b128 a[108:111], v4 offset:19456\n"
"v_add_f32 v221, v221, v25\n"
"v_mfma_f32_16x16x32_bf16 v[92:95], a[80:83], a[104:107], v[92:95]\n"
"s_sub_u32 s99, s99, s98\n"
"ds_read_b128 a[104:107], v4 offset:18432\n"
"v_add_f32 v222, v222, v26\n"
"v_mfma_f32_16x16x32_bf16 v[96:99], a[80:83], a[112:115], v[96:99]\n"
"s_sub_u32 s99, s99, s98\n"
"v_add_f32 v223, v223, v27\n"
"v_add_f32 v224, v224, v24\n"
"v_mfma_f32_16x16x32_bf16 v[96:99], a[84:87], a[116:119], v[96:99]\n"
"global_load_dwordx4 a[68:71], v0, s[34:35] offset:-3072 sc1\n"
"s_sub_u32 s99, s99, s98\n"
"v_mfma_f32_16x16x32_bf16 v[124:127], a[92:95], a[116:119], v[124:127]\n"
"s_sub_u32 s99, s99, s98\n"
"ds_read_b128 a[116:119], v4 offset:21504\n"
"v_add_f32 v225, v225, v25\n"
"v_mfma_f32_16x16x32_bf16 v[124:127], a[88:91], a[112:115], v[124:127]\n"
"s_sub_u32 s99, s99, s98\n"
"ds_read_b128 a[112:115], v4 offset:20480\n"
"v_add_f32 v226, v226, v26\n"
"v_mfma_f32_16x16x32_bf16 v[128:131], a[88:91], a[120:123], v[128:131]\n"
"s_sub_u32 s99, s99, s98\n"
"v_add_f32 v227, v227, v27\n"
"v_accvgpr_read_b32 v12, a24\n"
"v_mfma_f32_16x16x32_bf16 v[128:131], a[92:95], a[124:127], v[128:131]\n"
"s_sub_u32 s99, s99, s98\n"
"v_accvgpr_read_b32 v13, a25\n"
"v_accvgpr_read_b32 v14, a26\n"
"v_mfma_f32_16x16x32_bf16 v[100:103], a[84:87], a[124:127], v[100:103]\n"
"s_sub_u32 s99, s99, s98\n"
"ds_read_b128 a[124:127], v4 offset:23552\n"
"v_accvgpr_read_b32 v15, a27\n"
"v_mfma_f32_16x16x32_bf16 v[100:103], a[80:83], a[120:123], v[100:103]\n"
"s_sub_u32 s99, s99, s98\n"
"ds_read_b128 a[120:123], v4 offset:22528\n"
"v_add_f32 v12, v12, v24\n"
"v_mfma_f32_16x16x32_bf16 v[104:107], a[80:83], a[128:131], v[104:107]\n"
"s_sub_u32 s99, s99, s98\n"
"v_add_f32 v13, v13, v25\n"
"v_add_f32 v14, v14, v26\n"
"v_mfma_f32_16x16x32_bf16 v[104:107], a[84:87], a[132:135], v[104:107]\n"
"global_load_dwordx4 a[72:75], v0, s[34:35] offset:-2048 sc1\n"
"s_sub_u32 s99, s99, s98\n"
"v_mfma_f32_16x16x32_bf16 v[132:135], a[92:95], a[132:135], v[132:135]\n"
"s_sub_u32 s99, s99, s98\n"
"ds_read_b128 a[132:135], v4 offset:25600\n"
"v_add_f32 v15, v15, v27\n"
"v_mfma_f32_16x16x32_bf16 v[132:135], a[88:91], a[128:131], v[132:135]\n"
"s_sub_u32 s99, s99, s98\n"
"ds_read_b128 a[128:131], v4 offset:24576\n"
"v_accvgpr_write_b32 a24, v12\n"
"v_mfma_f32_16x16x32_bf16 v[136:139], a[88:91], a[136:139], v[136:139]\n"
"s_sub_u32 s99, s99, s98\n"
"v_accvgpr_write_b32 a25, v13\n"
"v_accvgpr_write_b32 a26, v14\n"
"v_mfma_f32_16x16x32_bf16 v[136:139], a[92:95], a[140:143], v[136:139]\n"
"s_sub_u32 s99, s99, s98\n"
"v_accvgpr_write_b32 a27, v15\n"
"v_lshlrev_b32 v28, 16, v30\n"
"v_mfma_f32_16x16x32_bf16 v[108:111], a[84:87], a[140:143], v[108:111]\n"
"s_sub_u32 s99, s99, s98\n"
"ds_read_b128 a[140:143], v4 offset:27648\n"
"v_and_b32 v29, s63, v30\n"
"v_mfma_f32_16x16x32_bf16 v[108:111], a[80:83], a[136:139], v[108:111]\n"
"s_sub_u32 s99, s99, s98\n"
"ds_read_b128 a[136:139], v4 offset:26624\n"
"v_lshlrev_b32 v30, 16, v31\n"
"v_mfma_f32_16x16x32_bf16 v[112:115], a[80:83], a[144:147], v[112:115]\n"
"s_sub_u32 s99, s99, s98\n"
"v_and_b32 v31, s63, v31\n"
"v_add_f32 v228, v228, v28\n"
"v_mfma_f32_16x16x32_bf16 v[112:115], a[84:87], a[148:151], v[112:115]\n"
"global_load_dwordx4 a[76:79], v0, s[34:35] offset:-1024 sc1\n"
"s_sub_u32 s99, s99, s98\n"
"v_mfma_f32_16x16x32_bf16 v[140:143], a[92:95], a[148:151], v[140:143]\n"
"s_sub_u32 s99, s99, s98\n"
"ds_read_b128 a[148:151], v4 offset:29696\n"
"v_add_f32 v229, v229, v29\n"
"v_mfma_f32_16x16x32_bf16 v[140:143], a[88:91], a[144:147], v[140:143]\n"
"s_sub_u32 s99, s99, s98\n"
"ds_read_b128 a[144:147], v4 offset:28672\n"
"v_add_f32 v230, v230, v30\n"
"v_mfma_f32_16x16x32_bf16 a[12:15], a[88:91], a[152:155], a[12:15]\n"
"s_memtime s[76:77]\n"
"v_add_f32 v231, v231, v31\n"
"v_add_f32 v232, v232, v28\n"
"v_mfma_f32_16x16x32_bf16 a[12:15], a[92:95], a[156:159], a[12:15]\n"
"v_add_f32 v233, v233, v29\n"
"v_add_f32 v234, v234, v30\n"
"v_mfma_f32_16x16x32_bf16 a[8:11], a[84:87], a[156:159], a[8:11]\n"
"ds_read_b128 a[156:159], v4 offset:31744\n"
"v_add_f32 v235, v235, v31\n"
"v_mfma_f32_16x16x32_bf16 a[8:11], a[80:83], a[152:155], a[8:11]\n"
"s_waitcnt vmcnt(8) lgkmcnt(11)\n"
"ds_read_b128 a[152:155], v4 offset:30720\n"
"v_add_f32 v236, v236, v28\n"
"v_mfma_f32_16x16x32_bf16 v[32:35], a[32:35], a[96:99], v[32:35]\n"
"v_add_f32 v237, v237, v29\n"
"v_add_f32 v238, v238, v30\n"
"v_mfma_f32_16x16x32_bf16 v[32:35], a[36:39], a[100:103], v[32:35]\n"
"global_load_dwordx4 a[80:83], v0, s[34:35] offset:0 sc1\n"
"v_add_f32 v239, v239, v31\n"
"v_mfma_f32_16x16x32_bf16 v[60:63], a[44:47], a[100:103], v[60:63]\n"
"v_add_f32 v240, v240, v28\n"
"v_add_f32 v241, v241, v29\n"
"v_mfma_f32_16x16x32_bf16 v[60:63], a[40:43], a[96:99], v[60:63]\n"
"v_add_f32 v242, v242, v30\n"
"v_mfma_f32_16x16x32_bf16 v[64:67], a[40:43], a[104:107], v[64:67]\n"
"v_add_f32 v243, v243, v31\n"
"v_add_f32 v244, v244, v28\n"
"v_mfma_f32_16x16x32_bf16 v[64:67], a[44:47], a[108:111], v[64:67]\n"
"v_add_f32 v245, v245, v29\n"
"v_mfma_f32_16x16x32_bf16 v[36:39], a[36:39], a[108:111], v[36:39]\n"
"v_add_f32 v246, v246, v30\n"
"v_mfma_f32_16x16x32_bf16 v[36:39], a[32:35], a[104:107], v[36:39]\n"
"L_st3865:\n"
"s_cbranch_execz L_retA\n"
"s_waitcnt lgkmcnt(8)\n"
"v_add_f32 v247, v247, v31\n"
"v_mfma_f32_16x16x32_bf16 v[40:43], a[32:35], a[112:115], v[40:43]\n"
"v_add_f32 v248, v248, v28\n"
"v_add_f32 v249, v249, v29\n"
"v_mfma_f32_16x16x32_bf16 v[40:43], a[36:39], a[116:119], v[40:43]\n"
"global_load_dwordx4 a[84:87], v0, s[34:35] offset:1024 sc1\n"
"s_add_u32 s48, s56, %12\n"
"s_addc_u32 s49, s57, 0\n"
"v_mfma_f32_16x16x32_bf16 v[68:71], a[44:47], a[116:119], v[68:71]\n"
"v_add_f32 v250, v250, v30\n"
"v_add_f32 v251, v251, v31\n"
"v_mfma_f32_16x16x32_bf16 v[68:71], a[40:43], a[112:115], v[68:71]\n"
"s_mov_b32 s50, 32768\n"
"v_add_f32 v252, v252, v28\n"
"v_add_f32 v253, v253, v29\n"
"v_mfma_f32_16x16x32_bf16 v[72:75], a[40:43], a[120:123], v[72:75]\n"
"v_add_f32 v254, v254, v30\n"
"v_mfma_f32_16x16x32_bf16 v[72:75], a[44:47], a[124:127], v[72:75]\n"
"s_mov_b32 s65, s78\n"
"v_add_f32 v255, v255, v31\n"
"v_mfma_f32_16x16x32_bf16 v[44:47], a[36:39], a[124:127], v[44:47]\n"
"v_accvgpr_read_b32 v12, a28\n"
"v_accvgpr_read_b32 v13, a29\n"
"v_mfma_f32_16x16x32_bf16 v[44:47], a[32:35], a[120:123], v[44:47]\n"
"s_mov_b64 s[40:41], s[52:53]\n"
"v_accvgpr_read_b32 v14, a30\n"
"s_waitcnt lgkmcnt(4)\n"
"v_mfma_f32_16x16x32_bf16 v[48:51], a[32:35], a[128:131], v[48:51]\n"
"s_mov_b32 s27, %21\n"
"v_accvgpr_read_b32 v15, a31\n"
"v_add_f32 v12, v12, v28\n"
"v_mfma_f32_16x16x32_bf16 v[48:51], a[36:39], a[132:135], v[48:51]\n"
"global_load_dwordx4 a[88:91], v0, s[34:35] offset:2048 sc1\n"
"s_mov_b32 s28, %23\n"
"v_mfma_f32_16x16x32_bf16 v[76:79], a[44:47], a[132:135], v[76:79]\n"
"s_waitcnt lgkmcnt(0)\n"
"v_add_f32 v13, v13, v29\n"
"v_add_f32 v14, v14, v30\n"
"v_mfma_f32_16x16x32_bf16 v[76:79], a[40:43], a[128:131], v[76:79]\n"
"s_sub_u32 s29, s76, s29\n"
"v_add_f32 v15, v15, v31\n"
"v_mfma_f32_16x16x32_bf16 v[80:83], a[40:43], a[136:139], v[80:83]\n"
"s_cmp_lt_u32 s29, 620\n"
"s_cselect_b32 s99, s99, 0\n"
"v_accvgpr_write_b32 a28, v12\n"
"v_mfma_f32_16x16x32_bf16 v[80:83], a[44:47], a[140:143], v[80:83]\n"
"s_max_u32 s30, s30, s99\n"
"v_accvgpr_write_b32 a29, v13\n"
"v_accvgpr_write_b32 a30, v14\n"
"v_mfma_f32_16x16x32_bf16 v[52:55], a[36:39], a[140:143], v[52:55]\n"
"s_cmp_eq_u32 s30, 0\n"
"s_cselect_b32 s30, 10, s30\n"
"v_accvgpr_write_b32 a31, v15\n"
"v_mfma_f32_16x16x32_bf16 v[52:55], a[32:35], a[136:139], v[52:55]\n"
"s_cmp_eq_u32 s31, 0\n"
"s_cselect_b32 s31, 10, s31\n"
"v_mfma_f32_16x16x32_bf16 v[56:59], a[32:35], a[144:147], v[56:59]\n"
"s_sub_u32 s69, s31, s30\n"
"v_mfma_f32_16x16x32_bf16 v[56:59], a[36:39], a[148:151], v[56:59]\n"
"global_load_dwordx4 a[92:95], v0, s[34:35] offset:3072 sc1\n"
"v_mov_b32 v20, s69\n"
"v_mfma_f32_16x16x32_bf16 v[84:87], a[44:47], a[148:151], v[84:87]\n"
"ds_write_b32 v2, v20 offset:8192\n"
"v_mfma_f32_16x16x32_bf16 v[84:87], a[40:43], a[144:147], v[84:87]\n"
"v_mfma_f32_16x16x32_bf16 a[4:7], a[40:43], a[152:155], a[4:7]\n"
"s_add_u32 s34, s34, 8192\n"
"v_mfma_f32_16x16x32_bf16 a[4:7], a[44:47], a[156:159], a[4:7]\n"
"v_mfma_f32_16x16x32_bf16 a[0:3], a[36:39], a[156:159], a[0:3]\n"
"v_mfma_f32_16x16x32_bf16 a[0:3], a[32:35], a[152:155], a[0:3]\n"
"s_waitcnt lgkmcnt(0)\n"
"s_barrier\n"
"s_waitcnt vmcnt(8)\n"
"v_mfma_f32_16x16x32_bf16 v[88:91], a[48:51], a[96:99], v[88:91]\n"
"ds_read_b32 v21, v3 offset:8192\n"
"v_mfma_f32_16x16x32_bf16 v[88:91], a[52:55], a[100:103], v[88:91]\n"
"global_load_dwordx4 a[32:35], v0, s[34:35] offset:-4096 sc1\n"
"v_mfma_f32_16x16x32_bf16 v[116:119], a[60:63], a[100:103], v[116:119]\n"
"ds_read_b128 a[100:103], v5 offset:17408\n"
"v_mfma_f32_16x16x32_bf16 v[116:119], a[56:59], a[96:99], v[116:119]\n"
"ds_read_b128 a[96:99], v5 offset:16384\n"
"v_mfma_f32_16x16x32_bf16 v[120:123], a[56:59], a[104:107], v[120:123]\n"
"ds_read_b32 v22, v4 offset:8192\n"
"v_mfma_f32_16x16x32_bf16 v[120:123], a[60:63], a[108:111], v[120:123]\n"
"ds_read_b32 v23, v5 offset:8192\n"
"v_mfma_f32_16x16x32_bf16 v[92:95], a[52:55], a[108:111], v[92:95]\n"
"ds_read_b128 a[108:111], v5 offset:19456\n"
"v_mfma_f32_16x16x32_bf16 v[92:95], a[48:51], a[104:107], v[92:95]\n"
"ds_read_b128 a[104:107], v5 offset:18432\n"
"v_mfma_f32_16x16x32_bf16 v[96:99], a[48:51], a[112:115], v[96:99]\n"
"v_mfma_f32_16x16x32_bf16 v[96:99], a[52:55], a[116:119], v[96:99]\n"
"global_load_dwordx4 a[36:39], v0, s[34:35] offset:-3072 sc1\n"
"v_mfma_f32_16x16x32_bf16 v[124:127], a[60:63], a[116:119], v[124:127]\n"
"s_waitcnt lgkmcnt(2)\n"
"ds_read_b128 a[116:119], v5 offset:21504\n"
"v_add_u32 v20, v20, v21\n"
"v_mfma_f32_16x16x32_bf16 v[124:127], a[56:59], a[112:115], v[124:127]\n"
"ds_read_b128 a[112:115], v5 offset:20480\n"
"v_mfma_f32_16x16x32_bf16 v[128:131], a[56:59], a[120:123], v[128:131]\n"
"v_add3_u32 v20, v20, v22, v23\n"
"v_mfma_f32_16x16x32_bf16 v[128:131], a[60:63], a[124:127], v[128:131]\n"
"v_mfma_f32_16x16x32_bf16 v[100:103], a[52:55], a[124:127], v[100:103]\n"
"ds_read_b128 a[124:127], v5 offset:23552\n"
"v_readfirstlane_b32 s69, v20\n"
"v_mfma_f32_16x16x32_bf16 v[100:103], a[48:51], a[120:123], v[100:103]\n"
"ds_read_b128 a[120:123], v5 offset:22528\n"
"v_mfma_f32_16x16x32_bf16 v[104:107], a[48:51], a[128:131], v[104:107]\n"
"v_mfma_f32_16x16x32_bf16 v[104:107], a[52:55], a[132:135], v[104:107]\n"
"s_cmp_le_i32 s69, -2\n"
"global_load_dwordx4 a[40:43], v0, s[34:35] offset:-2048 sc1\n"
"s_cselect_b32 s66, 2, 1\n"
"v_mfma_f32_16x16x32_bf16 v[132:135], a[60:63], a[132:135], v[132:135]\n"
"ds_read_b128 a[132:135], v5 offset:25600\n"
"v_mfma_f32_16x16x32_bf16 v[132:135], a[56:59], a[128:131], v[132:135]\n"
"s_cmp_ge_i32 s69, 2\n"
"ds_read_b128 a[128:131], v5 offset:24576\n"
"s_cselect_b32 s66, 3, s66\n"
"v_mfma_f32_16x16x32_bf16 v[136:139], a[56:59], a[136:139], v[136:139]\n"
"ds_read_b128 v[16:19], v7 offset:32768\n"
"v_mfma_f32_16x16x32_bf16 v[136:139], a[60:63], a[140:143], v[136:139]\n"
"s_lshl_b32 s73, s66, 13\n"
"s_xor_b32 s60, s79, s66\n"
"v_mfma_f32_16x16x32_bf16 v[108:111], a[52:55], a[140:143], v[108:111]\n"
"ds_read_b128 a[140:143], v5 offset:27648\n"
"v_mfma_f32_16x16x32_bf16 v[108:111], a[48:51], a[136:139], v[108:111]\n"
"s_cmp_eq_u32 s66, 3\n"
"ds_read_b128 a[136:139], v5 offset:26624\n"
"s_cselect_b32 s72, 8192, 0\n"
"v_mfma_f32_16x16x32_bf16 v[112:115], a[48:51], a[144:147], v[112:115]\n"
"s_abs_i32 s30, s69\n"
"v_mfma_f32_16x16x32_bf16 v[112:115], a[52:55], a[148:151], v[112:115]\n"
"global_load_dwordx4 a[44:47], v0, s[34:35] offset:-1024 sc1\n"
"s_add_u32 s52, s56, %11\n"
"s_addc_u32 s53, s57, 0\n"
"v_mfma_f32_16x16x32_bf16 v[140:143], a[60:63], a[148:151], v[140:143]\n"
"s_cmp_lt_u32 s30, 26\n"
"s_cselect_b32 s30, 2, 0\n"
"ds_read_b128 a[148:151], v5 offset:29696\n"
"v_mfma_f32_16x16x32_bf16 v[140:143], a[56:59], a[144:147], v[140:143]\n"
"s_add_u32 s52, s52, 4096\n"
"ds_read_b128 a[144:147], v5 offset:28672\n"
"s_addc_u32 s53, s53, 0\n"
"v_mfma_f32_16x16x32_bf16 a[12:15], a[56:59], a[152:155], a[12:15]\n"
"v_mfma_f32_16x16x32_bf16 a[12:15], a[60:63], a[156:159], a[12:15]\n"
"s_add_u32 s54, s52, s73\n"
"s_addc_u32 s55, s53, 0\n"
"v_mfma_f32_16x16x32_bf16 a[8:11], a[52:55], a[156:159], a[8:11]\n"
"ds_read_b128 a[156:159], v5 offset:31744\n"
"v_mfma_f32_16x16x32_bf16 a[8:11], a[48:51], a[152:155], a[8:11]\n"
"s_add_u32 s52, s52, s72\n"
"ds_read_b128 a[152:155], v5 offset:30720\n"
"s_addc_u32 s53, s53, 0\n"
"L_P6:\n"
"s_waitcnt vmcnt(8) lgkmcnt(13)\n"
"v_mfma_f32_16x16x32_bf16 v[32:35], a[64:67], a[96:99], v[32:35]\n"
"v_mfma_f32_16x16x32_bf16 v[32:35], a[68:71], a[100:103], v[32:35]\n"
"global_load_dwordx4 a[48:51], v0, s[34:35] offset:0 sc1\n"
"v_mfma_f32_16x16x32_bf16 v[60:63], a[76:79], a[100:103], v[60:63]\n"
"v_mfma_f32_16x16x32_bf16 v[60:63], a[72:75], a[96:99], v[60:63]\n"
"v_mfma_f32_16x16x32_bf16 v[64:67], a[72:75], a[104:107], v[64:67]\n"
"v_mfma_f32_16x16x32_bf16 v[64:67], a[76:79], a[108:111], v[64:67]\n"
"v_mfma_f32_16x16x32_bf16 v[36:39], a[68:71], a[108:111], v[36:39]\n"
"s_waitcnt lgkmcnt(6)\n"
"v_lshlrev_b32 v12, 16, v16\n"
"v_mfma_f32_16x16x32_bf16 v[36:39], a[64:67], a[104:107], v[36:39]\n"
"v_and_b32 v13, s63, v16\n"
"v_lshlrev_b32 v14, 16, v17\n"
"v_mfma_f32_16x16x32_bf16 v[40:43], a[64:67], a[112:115], v[40:43]\n"
"v_and_b32 v15, s63, v17\n"
"v_mfma_f32_16x16x32_bf16 v[40:43], a[68:71], a[116:119], v[40:43]\n"
"global_load_dwordx4 a[52:55], v0, s[34:35] offset:1024 sc1\n"
"ds_read_b128 v[20:23], v7 offset:34816\n"
"v_mfma_f32_16x16x32_bf16 v[68:71], a[76:79], a[116:119], v[68:71]\n"
"v_mul_f32 v12, v12, v32\n"
"v_mul_f32 v13, v13, v33\n"
"v_mfma_f32_16x16x32_bf16 v[68:71], a[72:75], a[112:115], v[68:71]\n"
"v_mul_f32 v14, v14, v34\n"
"v_mfma_f32_16x16x32_bf16 v[72:75], a[72:75], a[120:123], v[72:75]\n"
"v_mul_f32 v15, v15, v35\n"
"v_mfma_f32_16x16x32_bf16 v[72:75], a[76:79], a[124:127], v[72:75]\n"
"v_cvt_pk_bf16_f32 v24, v12, v13\n"
"v_cvt_pk_bf16_f32 v25, v14, v15\n"
"v_mfma_f32_16x16x32_bf16 v[44:47], a[68:71], a[124:127], v[44:47]\n"
"ds_write_b64 v2, v[24:25] offset:0\n"
"v_mfma_f32_16x16x32_bf16 v[44:47], a[64:67], a[120:123], v[44:47]\n"
"s_waitcnt lgkmcnt(6)\n"
"v_lshlrev_b32 v12, 16, v18\n"
"v_mfma_f32_16x16x32_bf16 v[48:51], a[64:67], a[128:131], v[48:51]\n"
"v_and_b32 v13, s63, v18\n"
"v_lshlrev_b32 v14, 16, v19\n"
"v_mfma_f32_16x16x32_bf16 v[48:51], a[68:71], a[132:135], v[48:51]\n"
"global_load_dwordx4 a[56:59], v0, s[34:35] offset:2048 sc1\n"
"v_and_b32 v15, s63, v19\n"
"v_mfma_f32_16x16x32_bf16 v[76:79], a[76:79], a[132:135], v[76:79]\n"
"v_mul_f32 v12, v12, v60\n"
"v_mfma_f32_16x16x32_bf16 v[76:79], a[72:75], a[128:131], v[76:79]\n"
"v_mul_f32 v13, v13, v61\n"
"v_mul_f32 v14, v14, v62\n"
"v_mfma_f32_16x16x32_bf16 v[80:83], a[72:75], a[136:139], v[80:83]\n"
"v_mul_f32 v15, v15, v63\n"
"v_mfma_f32_16x16x32_bf16 v[80:83], a[76:79], a[140:143], v[80:83]\n"
"v_cvt_pk_bf16_f32 v26, v12, v13\n"
"v_cvt_pk_bf16_f32 v27, v14, v15\n"
"v_mfma_f32_16x16x32_bf16 v[52:55], a[68:71], a[140:143], v[52:55]\n"
"ds_write_b64 v2, v[26:27] offset:8\n"
"v_mfma_f32_16x16x32_bf16 v[52:55], a[64:67], a[136:139], v[52:55]\n"
"s_waitcnt lgkmcnt(2)\n"
"v_lshlrev_b32 v12, 16, v20\n"
"v_mfma_f32_16x16x32_bf16 v[56:59], a[64:67], a[144:147], v[56:59]\n"
"v_and_b32 v13, s63, v20\n"
"v_mfma_f32_16x16x32_bf16 v[56:59], a[68:71], a[148:151], v[56:59]\n"
"global_load_dwordx4 a[60:63], v0, s[34:35] offset:3072 sc1\n"
"v_lshlrev_b32 v14, 16, v21\n"
"v_mfma_f32_16x16x32_bf16 v[84:87], a[76:79], a[148:151], v[84:87]\n"
"v_and_b32 v15, s63, v21\n"
"v_xor_b32 v16, 64, v7\n"
"v_mfma_f32_16x16x32_bf16 v[84:87], a[72:75], a[144:147], v[84:87]\n"
"ds_read_b128 v[16:19], v16 offset:32768\n"
"v_mfma_f32_16x16x32_bf16 a[4:7], a[72:75], a[152:155], a[4:7]\n"
"s_add_u32 s34, s34, 8192\n"
"v_mul_f32 v12, v12, v36\n"
"v_mul_f32 v13, v13, v37\n"
"v_mfma_f32_16x16x32_bf16 a[4:7], a[76:79], a[156:159], a[4:7]\n"
"v_mul_f32 v14, v14, v38\n"
"v_mfma_f32_16x16x32_bf16 a[0:3], a[68:71], a[156:159], a[0:3]\n"
"v_mul_f32 v15, v15, v39\n"
"v_mfma_f32_16x16x32_bf16 a[0:3], a[64:67], a[152:155], a[0:3]\n"
"s_waitcnt vmcnt(8)\n"
"v_cvt_pk_bf16_f32 v28, v12, v13\n"
"v_cvt_pk_bf16_f32 v29, v14, v15\n"
"v_mfma_f32_16x16x32_bf16 v[88:91], a[80:83], a[96:99], v[88:91]\n"
"v_lshlrev_b32 v12, 16, v22\n"
"v_mfma_f32_16x16x32_bf16 v[88:91], a[84:87], a[100:103], v[88:91]\n"
"global_load_dwordx4 a[64:67], v0, s[34:35] offset:-4096 sc1\n"
"v_and_b32 v13, s63, v22\n"
"v_mfma_f32_16x16x32_bf16 v[116:119], a[92:95], a[100:103], v[116:119]\n"
"ds_read_b128 a[100:103], v2 offset:17408\n"
"v_lshlrev_b32 v14, 16, v23\n"
"v_mfma_f32_16x16x32_bf16 v[116:119], a[88:91], a[96:99], v[116:119]\n"
"ds_read_b128 a[96:99], v2 offset:16384\n"
"v_and_b32 v15, s63, v23\n"
"v_mfma_f32_16x16x32_bf16 v[120:123], a[88:91], a[104:107], v[120:123]\n"
"v_mul_f32 v12, v12, v64\n"
"v_mul_f32 v13, v13, v65\n"
"v_mfma_f32_16x16x32_bf16 v[120:123], a[92:95], a[108:111], v[120:123]\n"
"v_mul_f32 v14, v14, v66\n"
"v_mul_f32 v15, v15, v67\n"
"v_mfma_f32_16x16x32_bf16 v[92:95], a[84:87], a[108:111], v[92:95]\n"
"ds_read_b128 a[108:111], v2 offset:19456\n"
"v_cvt_pk_bf16_f32 v30, v12, v13\n"
"v_mfma_f32_16x16x32_bf16 v[92:95], a[80:83], a[104:107], v[92:95]\n"
"ds_read_b128 a[104:107], v2 offset:18432\n"
"v_cvt_pk_bf16_f32 v31, v14, v15\n"
"v_mfma_f32_16x16x32_bf16 v[96:99], a[80:83], a[112:115], v[96:99]\n"
"global_store_dwordx4 v0, v[28:31], s[52:53] offset:-2048 sc1\n"
"ds_write_b128 v2, v[28:31] offset:2048\n"
"v_mfma_f32_16x16x32_bf16 v[96:99], a[84:87], a[116:119], v[96:99]\n"
"s_waitcnt lgkmcnt(5)\n"
"global_load_dwordx4 a[68:71], v0, s[34:35] offset:-3072 sc1\n"
"v_lshlrev_b32 v12, 16, v16\n"
"v_mfma_f32_16x16x32_bf16 v[124:127], a[92:95], a[116:119], v[124:127]\n"
"ds_read_b128 a[116:119], v2 offset:21504\n"
"v_and_b32 v13, s63, v16\n"
"v_mfma_f32_16x16x32_bf16 v[124:127], a[88:91], a[112:115], v[124:127]\n"
"ds_read_b128 a[112:115], v2 offset:20480\n"
"v_lshlrev_b32 v14, 16, v17\n"
"v_mfma_f32_16x16x32_bf16 v[128:131], a[88:91], a[120:123], v[128:131]\n"
"v_and_b32 v15, s63, v17\n"
"v_xor_b32 v20, 64, v7\n"
"v_mfma_f32_16x16x32_bf16 v[128:131], a[92:95], a[124:127], v[128:131]\n"
"ds_read_b128 v[20:23], v20 offset:34816\n"
"v_mul_f32 v12, v12, v88\n"
"v_mfma_f32_16x16x32_bf16 v[100:103], a[84:87], a[124:127], v[100:103]\n"
"ds_read_b128 a[124:127], v2 offset:23552\n"
"v_mul_f32 v13, v13, v89\n"
"v_mfma_f32_16x16x32_bf16 v[100:103], a[80:83], a[120:123], v[100:103]\n"
"ds_read_b128 a[120:123], v2 offset:22528\n"
"v_mul_f32 v14, v14, v90\n"
"v_mfma_f32_16x16x32_bf16 v[104:107], a[80:83], a[128:131], v[104:107]\n"
"v_mul_f32 v15, v15, v91\n"
"v_cvt_pk_bf16_f32 v24, v12, v13\n"
"v_mfma_f32_16x16x32_bf16 v[104:107], a[84:87], a[132:135], v[104:107]\n"
"global_load_dwordx4 a[72:75], v0, s[34:35] offset:-2048 sc1\n"
"v_cvt_pk_bf16_f32 v25, v14, v15\n"
"v_mfma_f32_16x16x32_bf16 v[132:135], a[92:95], a[132:135], v[132:135]\n"
"ds_read_b128 a[132:135], v2 offset:25600\n"
"v_lshlrev_b32 v12, 16, v18\n"
"v_mfma_f32_16x16x32_bf16 v[132:135], a[88:91], a[128:131], v[132:135]\n"
"ds_read_b128 a[128:131], v2 offset:24576\n"
"v_and_b32 v13, s63, v18\n"
"v_mfma_f32_16x16x32_bf16 v[136:139], a[88:91], a[136:139], v[136:139]\n"
"v_lshlrev_b32 v14, 16, v19\n"
"v_and_b32 v15, s63, v19\n"
"v_mfma_f32_16x16x32_bf16 v[136:139], a[92:95], a[140:143], v[136:139]\n"
"v_mul_f32 v12, v12, v116\n"
"v_mfma_f32_16x16x32_bf16 v[108:111], a[84:87], a[140:143], v[108:111]\n"
"ds_read_b128 a[140:143], v2 offset:27648\n"
"v_mul_f32 v13, v13, v117\n"
"v_mfma_f32_16x16x32_bf16 v[108:111], a[80:83], a[136:139], v[108:111]\n"
"ds_read_b128 a[136:139], v2 offset:26624\n"
"v_mul_f32 v14, v14, v118\n"
"v_mfma_f32_16x16x32_bf16 v[112:115], a[80:83], a[144:147], v[112:115]\n"
"v_mul_f32 v15, v15, v119\n"
"v_cvt_pk_bf16_f32 v26, v12, v13\n"
"v_mfma_f32_16x16x32_bf16 v[112:115], a[84:87], a[148:151], v[112:115]\n"
"global_load_dwordx4 a[76:79], v0, s[34:35] offset:-1024 sc1\n"
"v_cvt_pk_bf16_f32 v27, v14, v15\n"
"v_mfma_f32_16x16x32_bf16 v[140:143], a[92:95], a[148:151], v[140:143]\n"
"ds_read_b128 a[148:151], v2 offset:29696\n"
"v_mfma_f32_16x16x32_bf16 v[140:143], a[88:91], a[144:147], v[140:143]\n"
"ds_read_b128 a[144:147], v2 offset:28672\n"
"v_mfma_f32_16x16x32_bf16 a[12:15], a[88:91], a[152:155], a[12:15]\n"
"global_store_dwordx4 v0, v[24:27], s[52:53] offset:-3072 sc1\n"
"ds_write_b128 v2, v[24:27] offset:1024\n"
"v_mfma_f32_16x16x32_bf16 a[12:15], a[92:95], a[156:159], a[12:15]\n"
"s_waitcnt lgkmcnt(9)\n"
"v_lshlrev_b32 v12, 16, v20\n"
"v_and_b32 v13, s63, v20\n"
"v_mfma_f32_16x16x32_bf16 a[8:11], a[84:87], a[156:159], a[8:11]\n"
"ds_read_b128 a[156:159], v2 offset:31744\n"
"v_lshlrev_b32 v14, 16, v21\n"
"v_mfma_f32_16x16x32_bf16 a[8:11], a[80:83], a[152:155], a[8:11]\n"
"s_waitcnt vmcnt(10)\n"
"ds_read_b128 a[152:155], v2 offset:30720\n"
"v_and_b32 v15, s63, v21\n"
"v_mfma_f32_16x16x32_bf16 v[144:147], a[32:35], a[96:99], v[144:147]\n"
"ds_read_b128 v[16:19], v7 offset:36864\n"
"v_mul_f32 v12, v12, v92\n"
"v_mfma_f32_16x16x32_bf16 v[144:147], a[36:39], a[100:103], v[144:147]\n"
"global_load_dwordx4 a[80:83], v0, s[34:35] offset:0 sc1\n"
"v_mul_f32 v13, v13, v93\n"
"v_mfma_f32_16x16x32_bf16 v[172:175], a[44:47], a[100:103], v[172:175]\n"
"v_mul_f32 v14, v14, v94\n"
"v_mul_f32 v15, v15, v95\n"
"v_mfma_f32_16x16x32_bf16 v[172:175], a[40:43], a[96:99], v[172:175]\n"
"v_cvt_pk_bf16_f32 v28, v12, v13\n"
"v_cvt_pk_bf16_f32 v29, v14, v15\n"
"v_mfma_f32_16x16x32_bf16 v[176:179], a[40:43], a[104:107], v[176:179]\n"
"ds_write_b64 v2, v[28:29] offset:3072\n"
"v_mfma_f32_16x16x32_bf16 v[176:179], a[44:47], a[108:111], v[176:179]\n"
"v_lshlrev_b32 v12, 16, v22\n"
"v_and_b32 v13, s63, v22\n"
"v_mfma_f32_16x16x32_bf16 v[148:151], a[36:39], a[108:111], v[148:151]\n"
"v_lshlrev_b32 v14, 16, v23\n"
"v_mfma_f32_16x16x32_bf16 v[148:151], a[32:35], a[104:107], v[148:151]\n"
"s_waitcnt lgkmcnt(11)\n"
"v_and_b32 v15, s63, v23\n"
"v_mul_f32 v12, v12, v120\n"
"v_mfma_f32_16x16x32_bf16 v[152:155], a[32:35], a[112:115], v[152:155]\n"
"v_mul_f32 v13, v13, v121\n"
"v_mfma_f32_16x16x32_bf16 v[152:155], a[36:39], a[116:119], v[152:155]\n"
"global_load_dwordx4 a[84:87], v0, s[34:35] offset:1024 sc1\n"
"v_mul_f32 v14, v14, v122\n"
"v_mfma_f32_16x16x32_bf16 v[180:183], a[44:47], a[116:119], v[180:183]\n"
"v_mul_f32 v15, v15, v123\n"
"v_cvt_pk_bf16_f32 v30, v12, v13\n"
"v_mfma_f32_16x16x32_bf16 v[180:183], a[40:43], a[112:115], v[180:183]\n"
"v_cvt_pk_bf16_f32 v31, v14, v15\n"
"v_mfma_f32_16x16x32_bf16 v[184:187], a[40:43], a[120:123], v[184:187]\n"
"ds_write_b64 v2, v[30:31] offset:3080\n"
"v_mfma_f32_16x16x32_bf16 v[184:187], a[44:47], a[124:127], v[184:187]\n"
"s_waitcnt lgkmcnt(2)\n"
"v_lshlrev_b32 v12, 16, v16\n"
"v_and_b32 v13, s63, v16\n"
"v_mfma_f32_16x16x32_bf16 v[156:159], a[36:39], a[124:127], v[156:159]\n"
"v_lshlrev_b32 v14, 16, v17\n"
"v_mfma_f32_16x16x32_bf16 v[156:159], a[32:35], a[120:123], v[156:159]\n"
"v_and_b32 v15, s63, v17\n"
"v_xor_b32 v20, 64, v7\n"
"v_mfma_f32_16x16x32_bf16 v[160:163], a[32:35], a[128:131], v[160:163]\n"
"ds_read_b128 v[20:23], v20 offset:36864\n"
"v_mfma_f32_16x16x32_bf16 v[160:163], a[36:39], a[132:135], v[160:163]\n"
"global_load_dwordx4 a[88:91], v0, s[34:35] offset:2048 sc1\n"
"v_mul_f32 v12, v12, v40\n"
"v_mfma_f32_16x16x32_bf16 v[188:191], a[44:47], a[132:135], v[188:191]\n"
"v_mul_f32 v13, v13, v41\n"
"v_mfma_f32_16x16x32_bf16 v[188:191], a[40:43], a[128:131], v[188:191]\n"
"v_mul_f32 v14, v14, v42\n"
"v_mul_f32 v15, v15, v43\n"
"v_mfma_f32_16x16x32_bf16 v[192:195], a[40:43], a[136:139], v[192:195]\n"
"v_cvt_pk_bf16_f32 v24, v12, v13\n"
"v_mfma_f32_16x16x32_bf16 v[192:195], a[44:47], a[140:143], v[192:195]\n"
"v_cvt_pk_bf16_f32 v25, v14, v15\n"
"v_mfma_f32_16x16x32_bf16 v[164:167], a[36:39], a[140:143], v[164:167]\n"
"v_lshlrev_b32 v12, 16, v18\n"
"v_and_b32 v13, s63, v18\n"
"v_mfma_f32_16x16x32_bf16 v[164:167], a[32:35], a[136:139], v[164:167]\n"
"v_lshlrev_b32 v14, 16, v19\n"
"v_mfma_f32_16x16x32_bf16 v[168:171], a[32:35], a[144:147], v[168:171]\n"
"v_and_b32 v15, s63, v19\n"
"v_mul_f32 v12, v12, v68\n"
"v_mfma_f32_16x16x32_bf16 v[168:171], a[36:39], a[148:151], v[168:171]\n"
"global_load_dwordx4 a[92:95], v0, s[34:35] offset:3072 sc1\n"
"v_mul_f32 v13, v13, v69\n"
"v_mfma_f32_16x16x32_bf16 v[196:199], a[44:47], a[148:151], v[196:199]\n"
"v_mul_f32 v14, v14, v70\n"
"v_mfma_f32_16x16x32_bf16 v[196:199], a[40:43], a[144:147], v[196:199]\n"
"v_mul_f32 v15, v15, v71\n"
"v_mfma_f32_16x16x32_bf16 a[20:23], a[40:43], a[152:155], a[20:23]\n"
"s_add_u32 s34, s34, 8192\n"
"v_cvt_pk_bf16_f32 v26, v12, v13\n"
"v_cvt_pk_bf16_f32 v27, v14, v15\n"
"v_mfma_f32_16x16x32_bf16 a[20:23], a[44:47], a[156:159], a[20:23]\n"
"global_store_dwordx4 v0, v[24:27], s[52:53] offset:0 sc1\n"
"ds_write_b128 v2, v[24:27] offset:4096\n"
"v_mfma_f32_16x16x32_bf16 a[16:19], a[36:39], a[156:159], a[16:19]\n"
"s_waitcnt lgkmcnt(1)\n"
"v_lshlrev_b32 v12, 16, v20\n"
"v_mfma_f32_16x16x32_bf16 a[16:19], a[32:35], a[152:155], a[16:19]\n"
"s_waitcnt vmcnt(11)\n"
"v_and_b32 v13, s63, v20\n"
"v_mfma_f32_16x16x32_bf16 v[200:203], a[48:51], a[96:99], v[200:203]\n"
"v_lshlrev_b32 v14, 16, v21\n"
"v_and_b32 v15, s63, v21\n"
"v_mfma_f32_16x16x32_bf16 v[200:203], a[52:55], a[100:103], v[200:203]\n"
"global_load_dwordx4 a[32:35], v0, s[34:35] offset:-4096 sc1\n"
"ds_read_b128 v[16:19], v7 offset:38912\n"
"v_mfma_f32_16x16x32_bf16 v[228:231], a[60:63], a[100:103], v[228:231]\n"
"ds_read_b128 a[100:103], v3 offset:17408\n"
"v_mul_f32 v12, v12, v96\n"
"v_mfma_f32_16x16x32_bf16 v[228:231], a[56:59], a[96:99], v[228:231]\n"
"ds_read_b128 a[96:99], v3 offset:16384\n"
"v_mul_f32 v13, v13, v97\n"
"v_mfma_f32_16x16x32_bf16 v[232:235], a[56:59], a[104:107], v[232:235]\n"
"v_mul_f32 v14, v14, v98\n"
"v_mfma_f32_16x16x32_bf16 v[232:235], a[60:63], a[108:111], v[232:235]\n"
"v_mul_f32 v15, v15, v99\n"
"v_cvt_pk_bf16_f32 v28, v12, v13\n"
"v_mfma_f32_16x16x32_bf16 v[204:207], a[52:55], a[108:111], v[204:207]\n"
"ds_read_b128 a[108:111], v3 offset:19456\n"
"v_cvt_pk_bf16_f32 v29, v14, v15\n"
"v_mfma_f32_16x16x32_bf16 v[204:207], a[48:51], a[104:107], v[204:207]\n"
"ds_read_b128 a[104:107], v3 offset:18432\n"
"v_lshlrev_b32 v12, 16, v22\n"
"v_mfma_f32_16x16x32_bf16 v[208:211], a[48:51], a[112:115], v[208:211]\n"
"v_and_b32 v13, s63, v22\n"
"v_lshlrev_b32 v14, 16, v23\n"
"v_mfma_f32_16x16x32_bf16 v[208:211], a[52:55], a[116:119], v[208:211]\n"
"global_load_dwordx4 a[36:39], v0, s[34:35] offset:-3072 sc1\n"
"v_and_b32 v15, s63, v23\n"
"v_mfma_f32_16x16x32_bf16 v[236:239], a[60:63], a[116:119], v[236:239]\n"
"ds_read_b128 a[116:119], v3 offset:21504\n"
"v_mul_f32 v12, v12, v124\n"
"v_mfma_f32_16x16x32_bf16 v[236:239], a[56:59], a[112:115], v[236:239]\n"
"ds_read_b128 a[112:115], v3 offset:20480\n"
"v_mul_f32 v13, v13, v125\n"
"v_mfma_f32_16x16x32_bf16 v[240:243], a[56:59], a[120:123], v[240:243]\n"
"v_mul_f32 v14, v14, v126\n"
"v_mul_f32 v15, v15, v127\n"
"v_mfma_f32_16x16x32_bf16 v[240:243], a[60:63], a[124:127], v[240:243]\n"
"v_cvt_pk_bf16_f32 v30, v12, v13\n"
"v_mfma_f32_16x16x32_bf16 v[212:215], a[52:55], a[124:127], v[212:215]\n"
"ds_read_b128 a[124:127], v3 offset:23552\n"
"v_cvt_pk_bf16_f32 v31, v14, v15\n"
"v_mfma_f32_16x16x32_bf16 v[212:215], a[48:51], a[120:123], v[212:215]\n"
"ds_read_b128 a[120:123], v3 offset:22528\n"
"v_mfma_f32_16x16x32_bf16 v[216:219], a[48:51], a[128:131], v[216:219]\n"
"global_store_dwordx4 v0, v[28:31], s[52:53] offset:1024 sc1\n"
"ds_write_b128 v2, v[28:31] offset:5120\n"
"v_mfma_f32_16x16x32_bf16 v[216:219], a[52:55], a[132:135], v[216:219]\n"
"s_waitcnt lgkmcnt(9)\n"
"global_load_dwordx4 a[40:43], v0, s[34:35] offset:-2048 sc1\n"
"v_lshlrev_b32 v12, 16, v16\n"
"v_mfma_f32_16x16x32_bf16 v[244:247], a[60:63], a[132:135], v[244:247]\n"
"ds_read_b128 a[132:135], v3 offset:25600\n"
"v_and_b32 v13, s63, v16\n"
"v_mfma_f32_16x16x32_bf16 v[244:247], a[56:59], a[128:131], v[244:247]\n"
"ds_read_b128 a[128:131], v3 offset:24576\n"
"v_lshlrev_b32 v14, 16, v17\n"
"v_mfma_f32_16x16x32_bf16 v[248:251], a[56:59], a[136:139], v[248:251]\n"
"v_and_b32 v15, s63, v17\n"
"v_xor_b32 v20, 64, v7\n"
"v_mfma_f32_16x16x32_bf16 v[248:251], a[60:63], a[140:143], v[248:251]\n"
"ds_read_b128 v[20:23], v20 offset:38912\n"
"v_mul_f32 v12, v12, v44\n"
"v_mfma_f32_16x16x32_bf16 v[220:223], a[52:55], a[140:143], v[220:223]\n"
"ds_read_b128 a[140:143], v3 offset:27648\n"
"v_mul_f32 v13, v13, v45\n"
"v_mfma_f32_16x16x32_bf16 v[220:223], a[48:51], a[136:139], v[220:223]\n"
"ds_read_b128 a[136:139], v3 offset:26624\n"
"v_mul_f32 v14, v14, v46\n"
"v_mfma_f32_16x16x32_bf16 v[224:227], a[48:51], a[144:147], v[224:227]\n"
"v_mul_f32 v15, v15, v47\n"
"v_cvt_pk_bf16_f32 v24, v12, v13\n"
"v_mfma_f32_16x16x32_bf16 v[224:227], a[52:55], a[148:151], v[224:227]\n"
"global_load_dwordx4 a[44:47], v0, s[34:35] offset:-1024 sc1\n"
"v_cvt_pk_bf16_f32 v25, v14, v15\n"
"v_mfma_f32_16x16x32_bf16 v[252:255], a[60:63], a[148:151], v[252:255]\n"
"ds_read_b128 a[148:151], v3 offset:29696\n"
"v_mfma_f32_16x16x32_bf16 v[252:255], a[56:59], a[144:147], v[252:255]\n"
"ds_read_b128 a[144:147], v3 offset:28672\n"
"v_mfma_f32_16x16x32_bf16 a[28:31], a[56:59], a[152:155], a[28:31]\n"
"ds_write_b64 v2, v[24:25] offset:6144\n"
"v_mfma_f32_16x16x32_bf16 a[28:31], a[60:63], a[156:159], a[28:31]\n"
"v_lshlrev_b32 v12, 16, v18\n"
"v_and_b32 v13, s63, v18\n"
"v_mfma_f32_16x16x32_bf16 a[24:27], a[52:55], a[156:159], a[24:27]\n"
"ds_read_b128 a[156:159], v3 offset:31744\n"
"v_lshlrev_b32 v14, 16, v19\n"
"v_mfma_f32_16x16x32_bf16 a[24:27], a[48:51], a[152:155], a[24:27]\n"
"s_waitcnt vmcnt(11) lgkmcnt(14)\n"
"ds_read_b128 a[152:155], v3 offset:30720\n"
"v_and_b32 v15, s63, v19\n"
"v_mfma_f32_16x16x32_bf16 v[144:147], a[64:67], a[96:99], v[144:147]\n"
"v_mul_f32 v12, v12, v72\n"
"v_mul_f32 v13, v13, v73\n"
"v_mfma_f32_16x16x32_bf16 v[144:147], a[68:71], a[100:103], v[144:147]\n"
"global_load_dwordx4 a[48:51], v0, s[34:35] offset:0 sc1\n"
"v_mul_f32 v14, v14, v74\n"
"v_mfma_f32_16x16x32_bf16 v[172:175], a[76:79], a[100:103], v[172:175]\n"
"v_mul_f32 v15, v15, v75\n"
"v_cvt_pk_bf16_f32 v26, v12, v13\n"
"v_mfma_f32_16x16x32_bf16 v[172:175], a[72:75], a[96:99], v[172:175]\n"
"v_cvt_pk_bf16_f32 v27, v14, v15\n"
"v_mfma_f32_16x16x32_bf16 v[176:179], a[72:75], a[104:107], v[176:179]\n"
"ds_write_b64 v2, v[26:27] offset:6152\n"
"v_mfma_f32_16x16x32_bf16 v[176:179], a[76:79], a[108:111], v[176:179]\n"
"s_waitcnt lgkmcnt(8)\n"
"v_lshlrev_b32 v12, 16, v20\n"
"v_and_b32 v13, s63, v20\n"
"v_mfma_f32_16x16x32_bf16 v[148:151], a[68:71], a[108:111], v[148:151]\n"
"v_lshlrev_b32 v14, 16, v21\n"
"v_and_b32 v15, s63, v21\n"
"v_mfma_f32_16x16x32_bf16 v[148:151], a[64:67], a[104:107], v[148:151]\n"
"v_mul_f32 v12, v12, v100\n"
"v_mul_f32 v13, v13, v101\n"
"v_mfma_f32_16x16x32_bf16 v[152:155], a[64:67], a[112:115], v[152:155]\n"
"v_mul_f32 v14, v14, v102\n"
"v_mul_f32 v15, v15, v103\n"
"v_mfma_f32_16x16x32_bf16 v[152:155], a[68:71], a[116:119], v[152:155]\n"
"global_load_dwordx4 a[52:55], v0, s[34:35] offset:1024 sc1\n"
"v_cvt_pk_bf16_f32 v28, v12, v13\n"
"v_mfma_f32_16x16x32_bf16 v[180:183], a[76:79], a[116:119], v[180:183]\n"
"v_cvt_pk_bf16_f32 v29, v14, v15\n"
"v_lshlrev_b32 v12, 16, v22\n"
"v_mfma_f32_16x16x32_bf16 v[180:183], a[72:75], a[112:115], v[180:183]\n"
"v_and_b32 v13, s63, v22\n"
"v_lshlrev_b32 v14, 16, v23\n"
"v_mfma_f32_16x16x32_bf16 v[184:187], a[72:75], a[120:123], v[184:187]\n"
"v_and_b32 v15, s63, v23\n"
"v_mul_f32 v12, v12, v128\n"
"v_mfma_f32_16x16x32_bf16 v[184:187], a[76:79], a[124:127], v[184:187]\n"
"v_mul_f32 v13, v13, v129\n"
"v_mfma_f32_16x16x32_bf16 v[156:159], a[68:71], a[124:127], v[156:159]\n"
"v_mul_f32 v14, v14, v130\n"
"v_mfma_f32_16x16x32_bf16 v[156:159], a[64:67], a[120:123], v[156:159]\n"
"s_waitcnt lgkmcnt(6)\n"
"v_mul_f32 v15, v15, v131\n"
"v_cvt_pk_bf16_f32 v30, v12, v13\n"
"v_mfma_f32_16x16x32_bf16 v[160:163], a[64:67], a[128:131], v[160:163]\n"
"v_cvt_pk_bf16_f32 v31, v14, v15\n"
"v_mfma_f32_16x16x32_bf16 v[160:163], a[68:71], a[132:135], v[160:163]\n"
"global_load_dwordx4 a[56:59], v0, s[34:35] offset:2048 sc1\n"
"v_mfma_f32_16x16x32_bf16 v[188:191], a[76:79], a[132:135], v[188:191]\n"
"global_store_dwordx4 v0, v[28:31], s[52:53] offset:3072 sc1\n"
"ds_write_b128 v2, v[28:31] offset:7168\n"
"v_mfma_f32_16x16x32_bf16 v[188:191], a[72:75], a[128:131], v[188:191]\n"
"v_accvgpr_read_b32 v16, a160\n"
"v_accvgpr_read_b32 v17, a161\n"
"v_mfma_f32_16x16x32_bf16 v[192:195], a[72:75], a[136:139], v[192:195]\n"
"v_accvgpr_read_b32 v18, a162\n"
"v_mfma_f32_16x16x32_bf16 v[192:195], a[76:79], a[140:143], v[192:195]\n"
"v_accvgpr_read_b32 v19, a163\n"
"v_lshlrev_b32 v12, 16, v16\n"
"v_mfma_f32_16x16x32_bf16 v[164:167], a[68:71], a[140:143], v[164:167]\n"
"v_and_b32 v13, s63, v16\n"
"v_mfma_f32_16x16x32_bf16 v[164:167], a[64:67], a[136:139], v[164:167]\n"
"s_waitcnt lgkmcnt(2)\n"
"v_lshlrev_b32 v14, 16, v17\n"
"v_mfma_f32_16x16x32_bf16 v[168:171], a[64:67], a[144:147], v[168:171]\n"
"v_and_b32 v15, s63, v17\n"
"v_mul_f32 v12, v12, v48\n"
"v_mfma_f32_16x16x32_bf16 v[168:171], a[68:71], a[148:151], v[168:171]\n"
"global_load_dwordx4 a[60:63], v0, s[34:35] offset:3072 sc1\n"
"v_mul_f32 v13, v13, v49\n"
"v_mfma_f32_16x16x32_bf16 v[196:199], a[76:79], a[148:151], v[196:199]\n"
"v_mul_f32 v14, v14, v50\n"
"v_mfma_f32_16x16x32_bf16 v[196:199], a[72:75], a[144:147], v[196:199]\n"
"v_mul_f32 v15, v15, v51\n"
"v_cvt_pk_bf16_f32 v24, v12, v13\n"
"v_mfma_f32_16x16x32_bf16 a[20:23], a[72:75], a[152:155], a[20:23]\n"
"s_add_u32 s34, s34, 8192\n"
"v_cvt_pk_bf16_f32 v25, v14, v15\n"
"v_mfma_f32_16x16x32_bf16 a[20:23], a[76:79], a[156:159], a[20:23]\n"
"v_lshlrev_b32 v12, 16, v18\n"
"v_mfma_f32_16x16x32_bf16 a[16:19], a[68:71], a[156:159], a[16:19]\n"
"v_and_b32 v13, s63, v18\n"
"v_lshlrev_b32 v14, 16, v19\n"
"v_mfma_f32_16x16x32_bf16 a[16:19], a[64:67], a[152:155], a[16:19]\n"
"s_waitcnt vmcnt(11)\n"
"v_and_b32 v15, s63, v19\n"
"v_mul_f32 v12, v12, v76\n"
"v_mfma_f32_16x16x32_bf16 v[200:203], a[80:83], a[96:99], v[200:203]\n"
"v_mul_f32 v13, v13, v77\n"
"v_mul_f32 v14, v14, v78\n"
"v_mfma_f32_16x16x32_bf16 v[200:203], a[84:87], a[100:103], v[200:203]\n"
"global_load_dwordx4 a[64:67], v0, s[34:35] offset:-4096 sc1\n"
"v_mul_f32 v15, v15, v79\n"
"v_mfma_f32_16x16x32_bf16 v[228:231], a[92:95], a[100:103], v[228:231]\n"
"ds_read_b128 a[100:103], v4 offset:17408\n"
"v_cvt_pk_bf16_f32 v26, v12, v13\n"
"v_mfma_f32_16x16x32_bf16 v[228:231], a[88:91], a[96:99], v[228:231]\n"
"ds_read_b128 a[96:99], v4 offset:16384\n"
"v_cvt_pk_bf16_f32 v27, v14, v15\n"
"v_mfma_f32_16x16x32_bf16 v[232:235], a[88:91], a[104:107], v[232:235]\n"
"global_store_dwordx4 v0, v[24:27], s[54:55] offset:-4096 sc1\n"
"ds_write_b128 v2, v[24:27] offset:8192\n"
"v_mfma_f32_16x16x32_bf16 v[232:235], a[92:95], a[108:111], v[232:235]\n"
"v_accvgpr_read_b32 v16, a164\n"
"v_mfma_f32_16x16x32_bf16 v[204:207], a[84:87], a[108:111], v[204:207]\n"
"ds_read_b128 a[108:111], v4 offset:19456\n"
"v_accvgpr_read_b32 v17, a165\n"
"v_mfma_f32_16x16x32_bf16 v[204:207], a[80:83], a[104:107], v[204:207]\n"
"ds_read_b128 a[104:107], v4 offset:18432\n"
"v_accvgpr_read_b32 v18, a166\n"
"v_mfma_f32_16x16x32_bf16 v[208:211], a[80:83], a[112:115], v[208:211]\n"
"v_accvgpr_read_b32 v19, a167\n"
"v_lshlrev_b32 v12, 16, v16\n"
"v_mfma_f32_16x16x32_bf16 v[208:211], a[84:87], a[116:119], v[208:211]\n"
"global_load_dwordx4 a[68:71], v0, s[34:35] offset:-3072 sc1\n"
"v_and_b32 v13, s63, v16\n"
"v_mfma_f32_16x16x32_bf16 v[236:239], a[92:95], a[116:119], v[236:239]\n"
"ds_read_b128 a[116:119], v4 offset:21504\n"
"v_lshlrev_b32 v14, 16, v17\n"
"v_mfma_f32_16x16x32_bf16 v[236:239], a[88:91], a[112:115], v[236:239]\n"
"L_st4490:\n"
"s_cbranch_execz L_retA\n"
"ds_read_b128 a[112:115], v4 offset:20480\n"
"v_and_b32 v15, s63, v17\n"
"v_mfma_f32_16x16x32_bf16 v[240:243], a[88:91], a[120:123], v[240:243]\n"
"v_mul_f32 v12, v12, v104\n"
"v_mul_f32 v13, v13, v105\n"
"v_mfma_f32_16x16x32_bf16 v[240:243], a[92:95], a[124:127], v[240:243]\n"
"v_mul_f32 v14, v14, v106\n"
"v_mul_f32 v15, v15, v107\n"
"v_mfma_f32_16x16x32_bf16 v[212:215], a[84:87], a[124:127], v[212:215]\n"
"ds_read_b128 a[124:127], v4 offset:23552\n"
"v_cvt_pk_bf16_f32 v28, v12, v13\n"
"v_mfma_f32_16x16x32_bf16 v[212:215], a[80:83], a[120:123], v[212:215]\n"
"ds_read_b128 a[120:123], v4 offset:22528\n"
"v_cvt_pk_bf16_f32 v29, v14, v15\n"
"v_mfma_f32_16x16x32_bf16 v[216:219], a[80:83], a[128:131], v[216:219]\n"
"ds_write_b64 v2, v[28:29] offset:9216\n"
"v_mfma_f32_16x16x32_bf16 v[216:219], a[84:87], a[132:135], v[216:219]\n"
"global_load_dwordx4 a[72:75], v0, s[34:35] offset:-2048 sc1\n"
"v_lshlrev_b32 v12, 16, v18\n"
"v_mfma_f32_16x16x32_bf16 v[244:247], a[92:95], a[132:135], v[244:247]\n"
"ds_read_b128 a[132:135], v4 offset:25600\n"
"v_and_b32 v13, s63, v18\n"
"v_mfma_f32_16x16x32_bf16 v[244:247], a[88:91], a[128:131], v[244:247]\n"
"ds_read_b128 a[128:131], v4 offset:24576\n"
"v_lshlrev_b32 v14, 16, v19\n"
"v_mfma_f32_16x16x32_bf16 v[248:251], a[88:91], a[136:139], v[248:251]\n"
"v_and_b32 v15, s63, v19\n"
"v_mul_f32 v12, v12, v132\n"
"v_mfma_f32_16x16x32_bf16 v[248:251], a[92:95], a[140:143], v[248:251]\n"
"v_mul_f32 v13, v13, v133\n"
"v_mfma_f32_16x16x32_bf16 v[220:223], a[84:87], a[140:143], v[220:223]\n"
"ds_read_b128 a[140:143], v4 offset:27648\n"
"v_mul_f32 v14, v14, v134\n"
"v_mfma_f32_16x16x32_bf16 v[220:223], a[80:83], a[136:139], v[220:223]\n"
"ds_read_b128 a[136:139], v4 offset:26624\n"
"v_mul_f32 v15, v15, v135\n"
"v_mfma_f32_16x16x32_bf16 v[224:227], a[80:83], a[144:147], v[224:227]\n"
"v_cvt_pk_bf16_f32 v30, v12, v13\n"
"v_cvt_pk_bf16_f32 v31, v14, v15\n"
"v_mfma_f32_16x16x32_bf16 v[224:227], a[84:87], a[148:151], v[224:227]\n"
"global_load_dwordx4 a[76:79], v0, s[34:35] offset:-1024 sc1\n"
"v_mfma_f32_16x16x32_bf16 v[252:255], a[92:95], a[148:151], v[252:255]\n"
"ds_read_b128 a[148:151], v4 offset:29696\n"
"v_mfma_f32_16x16x32_bf16 v[252:255], a[88:91], a[144:147], v[252:255]\n"
"ds_read_b128 a[144:147], v4 offset:28672\n"
"v_mfma_f32_16x16x32_bf16 a[28:31], a[88:91], a[152:155], a[28:31]\n"
"ds_write_b64 v2, v[30:31] offset:9224\n"
"v_mfma_f32_16x16x32_bf16 a[28:31], a[92:95], a[156:159], a[28:31]\n"
"v_accvgpr_read_b32 v16, a168\n"
"v_accvgpr_read_b32 v17, a169\n"
"v_mfma_f32_16x16x32_bf16 a[24:27], a[84:87], a[156:159], a[24:27]\n"
"ds_read_b128 a[156:159], v4 offset:31744\n"
"v_accvgpr_read_b32 v18, a170\n"
"v_mfma_f32_16x16x32_bf16 a[24:27], a[80:83], a[152:155], a[24:27]\n"
"s_waitcnt vmcnt(10) lgkmcnt(13)\n"
"ds_read_b128 a[152:155], v4 offset:30720\n"
"v_accvgpr_read_b32 v19, a171\n"
"v_mfma_f32_16x16x32_bf16 v[144:147], a[32:35], a[96:99], v[144:147]\n"
"v_lshlrev_b32 v12, 16, v16\n"
"v_and_b32 v13, s63, v16\n"
"v_mfma_f32_16x16x32_bf16 v[144:147], a[36:39], a[100:103], v[144:147]\n"
"global_load_dwordx4 a[80:83], v0, s[34:35] offset:0 sc1\n"
"v_lshlrev_b32 v14, 16, v17\n"
"v_mfma_f32_16x16x32_bf16 v[172:175], a[44:47], a[100:103], v[172:175]\n"
"v_and_b32 v15, s63, v17\n"
"v_mul_f32 v12, v12, v52\n"
"v_mfma_f32_16x16x32_bf16 v[172:175], a[40:43], a[96:99], v[172:175]\n"
"v_mul_f32 v13, v13, v53\n"
"v_mul_f32 v14, v14, v54\n"
"v_mfma_f32_16x16x32_bf16 v[176:179], a[40:43], a[104:107], v[176:179]\n"
"v_mul_f32 v15, v15, v55\n"
"v_cvt_pk_bf16_f32 v24, v12, v13\n"
"v_mfma_f32_16x16x32_bf16 v[176:179], a[44:47], a[108:111], v[176:179]\n"
"v_cvt_pk_bf16_f32 v25, v14, v15\n"
"v_lshlrev_b32 v12, 16, v18\n"
"v_mfma_f32_16x16x32_bf16 v[148:151], a[36:39], a[108:111], v[148:151]\n"
"v_and_b32 v13, s63, v18\n"
"v_lshlrev_b32 v14, 16, v19\n"
"v_mfma_f32_16x16x32_bf16 v[148:151], a[32:35], a[104:107], v[148:151]\n"
"s_waitcnt lgkmcnt(10)\n"
"v_and_b32 v15, s63, v19\n"
"v_mul_f32 v12, v12, v80\n"
"v_mfma_f32_16x16x32_bf16 v[152:155], a[32:35], a[112:115], v[152:155]\n"
"v_mul_f32 v13, v13, v81\n"
"v_mul_f32 v14, v14, v82\n"
"v_mfma_f32_16x16x32_bf16 v[152:155], a[36:39], a[116:119], v[152:155]\n"
"global_load_dwordx4 a[84:87], v0, s[34:35] offset:1024 sc1\n"
"v_mul_f32 v15, v15, v83\n"
"v_mfma_f32_16x16x32_bf16 v[180:183], a[44:47], a[116:119], v[180:183]\n"
"v_cvt_pk_bf16_f32 v26, v12, v13\n"
"v_mfma_f32_16x16x32_bf16 v[180:183], a[40:43], a[112:115], v[180:183]\n"
"v_cvt_pk_bf16_f32 v27, v14, v15\n"
"v_mfma_f32_16x16x32_bf16 v[184:187], a[40:43], a[120:123], v[184:187]\n"
"global_store_dwordx4 v0, v[24:27], s[54:55] offset:-2048 sc1\n"
"ds_write_b128 v2, v[24:27] offset:10240\n"
"v_mfma_f32_16x16x32_bf16 v[184:187], a[44:47], a[124:127], v[184:187]\n"
"v_accvgpr_read_b32 v16, a172\n"
"v_mfma_f32_16x16x32_bf16 v[156:159], a[36:39], a[124:127], v[156:159]\n"
"v_accvgpr_read_b32 v17, a173\n"
"v_mfma_f32_16x16x32_bf16 v[156:159], a[32:35], a[120:123], v[156:159]\n"
"s_waitcnt lgkmcnt(6)\n"
"v_accvgpr_read_b32 v18, a174\n"
"v_accvgpr_read_b32 v19, a175\n"
"v_mfma_f32_16x16x32_bf16 v[160:163], a[32:35], a[128:131], v[160:163]\n"
"v_lshlrev_b32 v12, 16, v16\n"
"v_mfma_f32_16x16x32_bf16 v[160:163], a[36:39], a[132:135], v[160:163]\n"
"global_load_dwordx4 a[88:91], v0, s[34:35] offset:2048 sc1\n"
"v_and_b32 v13, s63, v16\n"
"v_mfma_f32_16x16x32_bf16 v[188:191], a[44:47], a[132:135], v[188:191]\n"
"v_lshlrev_b32 v14, 16, v17\n"
"v_and_b32 v15, s63, v17\n"
"v_mfma_f32_16x16x32_bf16 v[188:191], a[40:43], a[128:131], v[188:191]\n"
"v_mul_f32 v12, v12, v108\n"
"v_mfma_f32_16x16x32_bf16 v[192:195], a[40:43], a[136:139], v[192:195]\n"
"v_mul_f32 v13, v13, v109\n"
"v_mfma_f32_16x16x32_bf16 v[192:195], a[44:47], a[140:143], v[192:195]\n"
"v_mul_f32 v14, v14, v110\n"
"v_mul_f32 v15, v15, v111\n"
"v_mfma_f32_16x16x32_bf16 v[164:167], a[36:39], a[140:143], v[164:167]\n"
"v_cvt_pk_bf16_f32 v28, v12, v13\n"
"v_mfma_f32_16x16x32_bf16 v[164:167], a[32:35], a[136:139], v[164:167]\n"
"s_waitcnt lgkmcnt(1)\n"
"v_cvt_pk_bf16_f32 v29, v14, v15\n"
"v_mfma_f32_16x16x32_bf16 v[168:171], a[32:35], a[144:147], v[168:171]\n"
"v_lshlrev_b32 v12, 16, v18\n"
"v_and_b32 v13, s63, v18\n"
"v_mfma_f32_16x16x32_bf16 v[168:171], a[36:39], a[148:151], v[168:171]\n"
"global_load_dwordx4 a[92:95], v0, s[34:35] offset:3072 sc1\n"
"v_lshlrev_b32 v14, 16, v19\n"
"v_mfma_f32_16x16x32_bf16 v[196:199], a[44:47], a[148:151], v[196:199]\n"
"v_and_b32 v15, s63, v19\n"
"v_mfma_f32_16x16x32_bf16 v[196:199], a[40:43], a[144:147], v[196:199]\n"
"v_mul_f32 v12, v12, v136\n"
"v_mul_f32 v13, v13, v137\n"
"v_mfma_f32_16x16x32_bf16 a[20:23], a[40:43], a[152:155], a[20:23]\n"
"s_add_u32 s34, s34, 8192\n"
"v_mul_f32 v14, v14, v138\n"
"v_mfma_f32_16x16x32_bf16 a[20:23], a[44:47], a[156:159], a[20:23]\n"
"v_mul_f32 v15, v15, v139\n"
"v_mfma_f32_16x16x32_bf16 a[16:19], a[36:39], a[156:159], a[16:19]\n"
"v_cvt_pk_bf16_f32 v30, v12, v13\n"
"v_cvt_pk_bf16_f32 v31, v14, v15\n"
"v_mfma_f32_16x16x32_bf16 a[16:19], a[32:35], a[152:155], a[16:19]\n"
"s_waitcnt vmcnt(10)\n"
"global_store_dwordx4 v0, v[28:31], s[54:55] offset:-1024 sc1\n"
"ds_write_b128 v2, v[28:31] offset:11264\n"
"v_mfma_f32_16x16x32_bf16 v[200:203], a[48:51], a[96:99], v[200:203]\n"
"v_accvgpr_read_b32 v16, a176\n"
"v_mfma_f32_16x16x32_bf16 v[200:203], a[52:55], a[100:103], v[200:203]\n"
"global_load_dwordx4 a[32:35], v0, s[34:35] offset:-4096 sc1\n"
"v_accvgpr_read_b32 v17, a177\n"
"v_mfma_f32_16x16x32_bf16 v[228:231], a[60:63], a[100:103], v[228:231]\n"
"ds_read_b128 a[100:103], v5 offset:17408\n"
"v_accvgpr_read_b32 v18, a178\n"
"v_mfma_f32_16x16x32_bf16 v[228:231], a[56:59], a[96:99], v[228:231]\n"
"ds_read_b128 a[96:99], v5 offset:16384\n"
"v_accvgpr_read_b32 v19, a179\n"
"v_mfma_f32_16x16x32_bf16 v[232:235], a[56:59], a[104:107], v[232:235]\n"
"v_lshlrev_b32 v12, 16, v16\n"
"v_and_b32 v13, s63, v16\n"
"v_mfma_f32_16x16x32_bf16 v[232:235], a[60:63], a[108:111], v[232:235]\n"
"v_lshlrev_b32 v14, 16, v17\n"
"v_mfma_f32_16x16x32_bf16 v[204:207], a[52:55], a[108:111], v[204:207]\n"
"ds_read_b128 a[108:111], v5 offset:19456\n"
"v_and_b32 v15, s63, v17\n"
"v_mfma_f32_16x16x32_bf16 v[204:207], a[48:51], a[104:107], v[204:207]\n"
"ds_read_b128 a[104:107], v5 offset:18432\n"
"v_mul_f32 v12, v12, v56\n"
"v_mfma_f32_16x16x32_bf16 v[208:211], a[48:51], a[112:115], v[208:211]\n"
"v_mul_f32 v13, v13, v57\n"
"v_mul_f32 v14, v14, v58\n"
"v_mfma_f32_16x16x32_bf16 v[208:211], a[52:55], a[116:119], v[208:211]\n"
"global_load_dwordx4 a[36:39], v0, s[34:35] offset:-3072 sc1\n"
"v_mul_f32 v15, v15, v59\n"
"v_mfma_f32_16x16x32_bf16 v[236:239], a[60:63], a[116:119], v[236:239]\n"
"ds_read_b128 a[116:119], v5 offset:21504\n"
"v_cvt_pk_bf16_f32 v24, v12, v13\n"
"v_mfma_f32_16x16x32_bf16 v[236:239], a[56:59], a[112:115], v[236:239]\n"
"ds_read_b128 a[112:115], v5 offset:20480\n"
"v_cvt_pk_bf16_f32 v25, v14, v15\n"
"v_mfma_f32_16x16x32_bf16 v[240:243], a[56:59], a[120:123], v[240:243]\n"
"ds_write_b64 v2, v[24:25] offset:12288\n"
"v_mfma_f32_16x16x32_bf16 v[240:243], a[60:63], a[124:127], v[240:243]\n"
"v_lshlrev_b32 v12, 16, v18\n"
"v_and_b32 v13, s63, v18\n"
"v_mfma_f32_16x16x32_bf16 v[212:215], a[52:55], a[124:127], v[212:215]\n"
"ds_read_b128 a[124:127], v5 offset:23552\n"
"v_lshlrev_b32 v14, 16, v19\n"
"v_mfma_f32_16x16x32_bf16 v[212:215], a[48:51], a[120:123], v[212:215]\n"
"ds_read_b128 a[120:123], v5 offset:22528\n"
"v_and_b32 v15, s63, v19\n"
"v_mfma_f32_16x16x32_bf16 v[216:219], a[48:51], a[128:131], v[216:219]\n"
"v_mul_f32 v12, v12, v84\n"
"v_mul_f32 v13, v13, v85\n"
"v_mfma_f32_16x16x32_bf16 v[216:219], a[52:55], a[132:135], v[216:219]\n"
"global_load_dwordx4 a[40:43], v0, s[34:35] offset:-2048 sc1\n"
"v_mul_f32 v14, v14, v86\n"
"v_mfma_f32_16x16x32_bf16 v[244:247], a[60:63], a[132:135], v[244:247]\n"
"ds_read_b128 a[132:135], v5 offset:25600\n"
"v_mul_f32 v15, v15, v87\n"
"v_mfma_f32_16x16x32_bf16 v[244:247], a[56:59], a[128:131], v[244:247]\n"
"ds_read_b128 a[128:131], v5 offset:24576\n"
"v_cvt_pk_bf16_f32 v26, v12, v13\n"
"v_mfma_f32_16x16x32_bf16 v[248:251], a[56:59], a[136:139], v[248:251]\n"
"v_cvt_pk_bf16_f32 v27, v14, v15\n"
"v_mfma_f32_16x16x32_bf16 v[248:251], a[60:63], a[140:143], v[248:251]\n"
"ds_write_b64 v2, v[26:27] offset:12296\n"
"v_mfma_f32_16x16x32_bf16 v[220:223], a[52:55], a[140:143], v[220:223]\n"
"ds_read_b128 a[140:143], v5 offset:27648\n"
"v_accvgpr_read_b32 v16, a180\n"
"v_mfma_f32_16x16x32_bf16 v[220:223], a[48:51], a[136:139], v[220:223]\n"
"ds_read_b128 a[136:139], v5 offset:26624\n"
"v_accvgpr_read_b32 v17, a181\n"
"v_mfma_f32_16x16x32_bf16 v[224:227], a[48:51], a[144:147], v[224:227]\n"
"v_accvgpr_read_b32 v18, a182\n"
"v_accvgpr_read_b32 v19, a183\n"
"v_mfma_f32_16x16x32_bf16 v[224:227], a[52:55], a[148:151], v[224:227]\n"
"global_load_dwordx4 a[44:47], v0, s[34:35] offset:-1024 sc1\n"
"v_lshlrev_b32 v12, 16, v16\n"
"v_mfma_f32_16x16x32_bf16 v[252:255], a[60:63], a[148:151], v[252:255]\n"
"ds_read_b128 a[148:151], v5 offset:29696\n"
"v_and_b32 v13, s63, v16\n"
"v_mfma_f32_16x16x32_bf16 v[252:255], a[56:59], a[144:147], v[252:255]\n"
"ds_read_b128 a[144:147], v5 offset:28672\n"
"v_lshlrev_b32 v14, 16, v17\n"
"v_mfma_f32_16x16x32_bf16 a[28:31], a[56:59], a[152:155], a[28:31]\n"
"v_and_b32 v15, s63, v17\n"
"v_mul_f32 v12, v12, v112\n"
"v_mfma_f32_16x16x32_bf16 a[28:31], a[60:63], a[156:159], a[28:31]\n"
"v_mul_f32 v13, v13, v113\n"
"v_mul_f32 v14, v14, v114\n"
"v_mfma_f32_16x16x32_bf16 a[24:27], a[52:55], a[156:159], a[24:27]\n"
"ds_read_b128 a[156:159], v5 offset:31744\n"
"v_mul_f32 v15, v15, v115\n"
"v_mfma_f32_16x16x32_bf16 a[24:27], a[48:51], a[152:155], a[24:27]\n"
"s_waitcnt vmcnt(10) lgkmcnt(13)\n"
"ds_read_b128 a[152:155], v5 offset:30720\n"
"v_cvt_pk_bf16_f32 v28, v12, v13\n"
"v_mfma_f32_16x16x32_bf16 v[144:147], a[64:67], a[96:99], v[144:147]\n"
"v_cvt_pk_bf16_f32 v29, v14, v15\n"
"v_lshlrev_b32 v12, 16, v18\n"
"v_mfma_f32_16x16x32_bf16 v[144:147], a[68:71], a[100:103], v[144:147]\n"
"global_load_dwordx4 a[48:51], v0, s[34:35] offset:0 sc1\n"
"v_and_b32 v13, s63, v18\n"
"v_mfma_f32_16x16x32_bf16 v[172:175], a[76:79], a[100:103], v[172:175]\n"
"v_lshlrev_b32 v14, 16, v19\n"
"v_and_b32 v15, s63, v19\n"
"v_mfma_f32_16x16x32_bf16 v[172:175], a[72:75], a[96:99], v[172:175]\n"
"v_mul_f32 v12, v12, v140\n"
"v_mul_f32 v13, v13, v141\n"
"v_mfma_f32_16x16x32_bf16 v[176:179], a[72:75], a[104:107], v[176:179]\n"
"v_mul_f32 v14, v14, v142\n"
"v_mul_f32 v15, v15, v143\n"
"v_mfma_f32_16x16x32_bf16 v[176:179], a[76:79], a[108:111], v[176:179]\n"
"v_cvt_pk_bf16_f32 v30, v12, v13\n"
"v_mfma_f32_16x16x32_bf16 v[148:151], a[68:71], a[108:111], v[148:151]\n"
"v_cvt_pk_bf16_f32 v31, v14, v15\n"
"v_mfma_f32_16x16x32_bf16 v[148:151], a[64:67], a[104:107], v[148:151]\n"
"s_waitcnt lgkmcnt(9)\n"
"global_store_dwordx4 v0, v[28:31], s[54:55] offset:1024 sc1\n"
"ds_write_b128 v2, v[28:31] offset:13312\n"
"v_mfma_f32_16x16x32_bf16 v[152:155], a[64:67], a[112:115], v[152:155]\n"
"v_accvgpr_read_b32 v16, a184\n"
"v_accvgpr_read_b32 v17, a185\n"
"v_mfma_f32_16x16x32_bf16 v[152:155], a[68:71], a[116:119], v[152:155]\n"
"global_load_dwordx4 a[52:55], v0, s[34:35] offset:1024 sc1\n"
"v_accvgpr_read_b32 v18, a186\n"
"v_mfma_f32_16x16x32_bf16 v[180:183], a[76:79], a[116:119], v[180:183]\n"
"v_accvgpr_read_b32 v19, a187\n"
"v_mfma_f32_16x16x32_bf16 v[180:183], a[72:75], a[112:115], v[180:183]\n"
"v_lshlrev_b32 v12, 16, v16\n"
"v_mfma_f32_16x16x32_bf16 v[184:187], a[72:75], a[120:123], v[184:187]\n"
"v_and_b32 v13, s63, v16\n"
"v_lshlrev_b32 v14, 16, v17\n"
"v_mfma_f32_16x16x32_bf16 v[184:187], a[76:79], a[124:127], v[184:187]\n"
"v_and_b32 v15, s63, v17\n"
"v_mfma_f32_16x16x32_bf16 v[156:159], a[68:71], a[124:127], v[156:159]\n"
"v_accvgpr_read_b32 v10, a0\n"
"v_mfma_f32_16x16x32_bf16 v[156:159], a[64:67], a[120:123], v[156:159]\n"
"s_waitcnt lgkmcnt(5)\n"
"v_accvgpr_read_b32 v11, a1\n"
"v_mul_f32 v12, v12, v10\n"
"v_mfma_f32_16x16x32_bf16 v[160:163], a[64:67], a[128:131], v[160:163]\n"
"v_mul_f32 v13, v13, v11\n"
"v_mfma_f32_16x16x32_bf16 v[160:163], a[68:71], a[132:135], v[160:163]\n"
"global_load_dwordx4 a[56:59], v0, s[34:35] offset:2048 sc1\n"
"v_accvgpr_read_b32 v10, a2\n"
"v_mfma_f32_16x16x32_bf16 v[188:191], a[76:79], a[132:135], v[188:191]\n"
"v_accvgpr_read_b32 v11, a3\n"
"v_mul_f32 v14, v14, v10\n"
"v_mfma_f32_16x16x32_bf16 v[188:191], a[72:75], a[128:131], v[188:191]\n"
"v_mul_f32 v15, v15, v11\n"
"v_mfma_f32_16x16x32_bf16 v[192:195], a[72:75], a[136:139], v[192:195]\n"
"v_cvt_pk_bf16_f32 v24, v12, v13\n"
"v_mfma_f32_16x16x32_bf16 v[192:195], a[76:79], a[140:143], v[192:195]\n"
"v_cvt_pk_bf16_f32 v25, v14, v15\n"
"v_lshlrev_b32 v12, 16, v18\n"
"v_mfma_f32_16x16x32_bf16 v[164:167], a[68:71], a[140:143], v[164:167]\n"
"v_and_b32 v13, s63, v18\n"
"v_mfma_f32_16x16x32_bf16 v[164:167], a[64:67], a[136:139], v[164:167]\n"
"s_waitcnt lgkmcnt(1)\n"
"v_lshlrev_b32 v14, 16, v19\n"
"v_mfma_f32_16x16x32_bf16 v[168:171], a[64:67], a[144:147], v[168:171]\n"
"v_and_b32 v15, s63, v19\n"
"v_accvgpr_read_b32 v10, a4\n"
"v_mfma_f32_16x16x32_bf16 v[168:171], a[68:71], a[148:151], v[168:171]\n"
"global_load_dwordx4 a[60:63], v0, s[34:35] offset:3072 sc1\n"
"v_accvgpr_read_b32 v11, a5\n"
"v_mfma_f32_16x16x32_bf16 v[196:199], a[76:79], a[148:151], v[196:199]\n"
"v_mul_f32 v12, v12, v10\n"
"v_mfma_f32_16x16x32_bf16 v[196:199], a[72:75], a[144:147], v[196:199]\n"
"v_mul_f32 v13, v13, v11\n"
"v_accvgpr_read_b32 v10, a6\n"
"v_mfma_f32_16x16x32_bf16 a[20:23], a[72:75], a[152:155], a[20:23]\n"
"s_add_u32 s34, s34, 8192\n"
"v_accvgpr_read_b32 v11, a7\n"
"v_mfma_f32_16x16x32_bf16 a[20:23], a[76:79], a[156:159], a[20:23]\n"
"v_mul_f32 v14, v14, v10\n"
"v_mul_f32 v15, v15, v11\n"
"v_mfma_f32_16x16x32_bf16 a[16:19], a[68:71], a[156:159], a[16:19]\n"
"v_cvt_pk_bf16_f32 v26, v12, v13\n"
"v_cvt_pk_bf16_f32 v27, v14, v15\n"
"v_mfma_f32_16x16x32_bf16 a[16:19], a[64:67], a[152:155], a[16:19]\n"
"s_waitcnt vmcnt(10)\n"
"global_store_dwordx4 v0, v[24:27], s[54:55] offset:2048 sc1\n"
"ds_write_b128 v2, v[24:27] offset:14336\n"
"v_mfma_f32_16x16x32_bf16 v[200:203], a[80:83], a[96:99], v[200:203]\n"
"v_accvgpr_read_b32 v16, a188\n"
"v_mfma_f32_16x16x32_bf16 v[200:203], a[84:87], a[100:103], v[200:203]\n"
"global_load_dwordx4 a[64:67], v0, s[34:35] offset:-4096 sc1\n"
"v_accvgpr_read_b32 v17, a189\n"
"v_mfma_f32_16x16x32_bf16 v[228:231], a[92:95], a[100:103], v[228:231]\n"
"ds_read_b128 a[100:103], v2 offset:1024\n"
"v_accvgpr_read_b32 v18, a190\n"
"v_mfma_f32_16x16x32_bf16 v[228:231], a[88:91], a[96:99], v[228:231]\n"
"ds_read_b128 a[96:99], v2 offset:0\n"
"v_accvgpr_read_b32 v19, a191\n"
"v_mfma_f32_16x16x32_bf16 v[232:235], a[88:91], a[104:107], v[232:235]\n"
"v_lshlrev_b32 v12, 16, v16\n"
"v_and_b32 v13, s63, v16\n"
"v_mfma_f32_16x16x32_bf16 v[232:235], a[92:95], a[108:111], v[232:235]\n"
"v_lshlrev_b32 v14, 16, v17\n"
"v_and_b32 v15, s63, v17\n"
"v_mfma_f32_16x16x32_bf16 v[204:207], a[84:87], a[108:111], v[204:207]\n"
"ds_read_b128 a[108:111], v2 offset:3072\n"
"v_accvgpr_read_b32 v10, a8\n"
"v_mfma_f32_16x16x32_bf16 v[204:207], a[80:83], a[104:107], v[204:207]\n"
"ds_read_b128 a[104:107], v2 offset:2048\n"
"v_accvgpr_read_b32 v11, a9\n"
"v_mfma_f32_16x16x32_bf16 v[208:211], a[80:83], a[112:115], v[208:211]\n"
"v_mul_f32 v12, v12, v10\n"
"v_mul_f32 v13, v13, v11\n"
"v_mfma_f32_16x16x32_bf16 v[208:211], a[84:87], a[116:119], v[208:211]\n"
"global_load_dwordx4 a[68:71], v0, s[34:35] offset:-3072 sc1\n"
"v_accvgpr_read_b32 v10, a10\n"
"v_mfma_f32_16x16x32_bf16 v[236:239], a[92:95], a[116:119], v[236:239]\n"
"ds_read_b128 a[116:119], v2 offset:5120\n"
"v_accvgpr_read_b32 v11, a11\n"
"v_mfma_f32_16x16x32_bf16 v[236:239], a[88:91], a[112:115], v[236:239]\n"
"ds_read_b128 a[112:115], v2 offset:4096\n"
"v_mul_f32 v14, v14, v10\n"
"v_mfma_f32_16x16x32_bf16 v[240:243], a[88:91], a[120:123], v[240:243]\n"
"v_mul_f32 v15, v15, v11\n"
"v_cvt_pk_bf16_f32 v28, v12, v13\n"
"v_mfma_f32_16x16x32_bf16 v[240:243], a[92:95], a[124:127], v[240:243]\n"
"v_cvt_pk_bf16_f32 v29, v14, v15\n"
"v_lshlrev_b32 v12, 16, v18\n"
"v_mfma_f32_16x16x32_bf16 v[212:215], a[84:87], a[124:127], v[212:215]\n"
"ds_read_b128 a[124:127], v2 offset:7168\n"
"v_and_b32 v13, s63, v18\n"
"v_mfma_f32_16x16x32_bf16 v[212:215], a[80:83], a[120:123], v[212:215]\n"
"ds_read_b128 a[120:123], v2 offset:6144\n"
"v_lshlrev_b32 v14, 16, v19\n"
"v_mfma_f32_16x16x32_bf16 v[216:219], a[80:83], a[128:131], v[216:219]\n"
"v_and_b32 v15, s63, v19\n"
"v_accvgpr_read_b32 v10, a12\n"
"v_mfma_f32_16x16x32_bf16 v[216:219], a[84:87], a[132:135], v[216:219]\n"
"global_load_dwordx4 a[72:75], v0, s[34:35] offset:-2048 sc1\n"
"v_accvgpr_read_b32 v11, a13\n"
"v_mfma_f32_16x16x32_bf16 v[244:247], a[92:95], a[132:135], v[244:247]\n"
"ds_read_b128 a[132:135], v2 offset:9216\n"
"v_mul_f32 v12, v12, v10\n"
"v_mfma_f32_16x16x32_bf16 v[244:247], a[88:91], a[128:131], v[244:247]\n"
"ds_read_b128 a[128:131], v2 offset:8192\n"
"v_mul_f32 v13, v13, v11\n"
"v_mfma_f32_16x16x32_bf16 v[248:251], a[88:91], a[136:139], v[248:251]\n"
"v_accvgpr_read_b32 v10, a14\n"
"v_accvgpr_read_b32 v11, a15\n"
"v_mfma_f32_16x16x32_bf16 v[248:251], a[92:95], a[140:143], v[248:251]\n"
"v_mul_f32 v14, v14, v10\n"
"v_mul_f32 v15, v15, v11\n"
"v_mfma_f32_16x16x32_bf16 v[220:223], a[84:87], a[140:143], v[220:223]\n"
"ds_read_b128 a[140:143], v2 offset:11264\n"
"v_cvt_pk_bf16_f32 v30, v12, v13\n"
"v_mfma_f32_16x16x32_bf16 v[220:223], a[80:83], a[136:139], v[220:223]\n"
"ds_read_b128 a[136:139], v2 offset:10240\n"
"v_cvt_pk_bf16_f32 v31, v14, v15\n"
"v_mfma_f32_16x16x32_bf16 v[224:227], a[80:83], a[144:147], v[224:227]\n"
"global_store_dwordx4 v0, v[28:31], s[54:55] offset:3072 sc1\n"
"ds_write_b128 v2, v[28:31] offset:15360\n"
"v_mfma_f32_16x16x32_bf16 v[224:227], a[84:87], a[148:151], v[224:227]\n"
"global_load_dwordx4 a[76:79], v0, s[34:35] offset:-1024 sc1\n"
"v_mfma_f32_16x16x32_bf16 v[252:255], a[92:95], a[148:151], v[252:255]\n"
"ds_read_b128 a[148:151], v2 offset:13312\n"
"v_mfma_f32_16x16x32_bf16 v[252:255], a[88:91], a[144:147], v[252:255]\n"
"ds_read_b128 a[144:147], v2 offset:12288\n"
"v_mfma_f32_16x16x32_bf16 a[28:31], a[88:91], a[152:155], a[28:31]\n"
"s_add_u32 s62, s62, 1\n"
"v_mfma_f32_16x16x32_bf16 a[28:31], a[92:95], a[156:159], a[28:31]\n"
"v_mfma_f32_16x16x32_bf16 a[24:27], a[84:87], a[156:159], a[24:27]\n"
"ds_read_b128 a[156:159], v2 offset:15360\n"
"v_mfma_f32_16x16x32_bf16 a[24:27], a[80:83], a[152:155], a[24:27]\n"
"ds_read_b128 a[152:155], v2 offset:14336\n"
"L_layer:\n"
"s_waitcnt vmcnt(11) lgkmcnt(13)\n"
"v_mfma_f32_16x16x32_bf16 v[32:35], a[32:35], a[96:99], v[32:35]\n"
"s_mov_b64 s[96:97], s[52:53]\n"
"v_accvgpr_read_b32 v16, a192\n"
"v_accvgpr_read_b32 v17, a193\n"
"v_mfma_f32_16x16x32_bf16 v[32:35], a[36:39], a[100:103], v[32:35]\n"
"global_load_dwordx4 a[80:83], v0, s[34:35] offset:0 sc1\n"
"v_accvgpr_read_b32 v18, a194\n"
"v_mfma_f32_16x16x32_bf16 v[60:63], a[44:47], a[100:103], v[60:63]\n"
"s_mov_b64 s[98:99], s[54:55]\n"
"v_accvgpr_read_b32 v19, a195\n"
"v_mfma_f32_16x16x32_bf16 v[60:63], a[40:43], a[96:99], v[60:63]\n"
"v_lshlrev_b32 v12, 16, v16\n"
"v_and_b32 v13, s63, v16\n"
"v_mfma_f32_16x16x32_bf16 v[64:67], a[40:43], a[104:107], v[64:67]\n"
"s_add_u32 s52, s52, 131072\n"
"s_addc_u32 s53, s53, 0\n"
"v_lshlrev_b32 v14, 16, v17\n"
"v_mfma_f32_16x16x32_bf16 v[64:67], a[44:47], a[108:111], v[64:67]\n"
"v_and_b32 v15, s63, v17\n"
"v_mul_f32 v12, v12, v144\n"
"v_mfma_f32_16x16x32_bf16 v[36:39], a[36:39], a[108:111], v[36:39]\n"
"s_add_u32 s54, s54, 131072\n"
"s_addc_u32 s55, s55, 0\n"
"v_mul_f32 v13, v13, v145\n"
"v_mfma_f32_16x16x32_bf16 v[36:39], a[32:35], a[104:107], v[36:39]\n"
"s_waitcnt lgkmcnt(9)\n"
"v_mul_f32 v14, v14, v146\n"
"v_mul_f32 v15, v15, v147\n"
"v_mfma_f32_16x16x32_bf16 v[40:43], a[32:35], a[112:115], v[40:43]\n"
"ds_read_b128 v[20:23], v2 offset:0\n"
"v_cvt_pk_bf16_f32 v24, v12, v13\n"
"v_mfma_f32_16x16x32_bf16 v[40:43], a[36:39], a[116:119], v[40:43]\n"
"global_load_dwordx4 a[84:87], v0, s[34:35] offset:1024 sc1\n"
"v_cvt_pk_bf16_f32 v25, v14, v15\n"
"v_mfma_f32_16x16x32_bf16 v[68:71], a[44:47], a[116:119], v[68:71]\n"
"ds_write_b64 v2, v[24:25] offset:16384\n"
"v_mfma_f32_16x16x32_bf16 v[68:71], a[40:43], a[112:115], v[68:71]\n"
"v_lshlrev_b32 v12, 16, v18\n"
"v_and_b32 v13, s63, v18\n"
"v_mfma_f32_16x16x32_bf16 v[72:75], a[40:43], a[120:123], v[72:75]\n"
"s_waitcnt lgkmcnt(6)\n"
"s_barrier\n"
"v_mfma_f32_16x16x32_bf16 v[72:75], a[44:47], a[124:127], v[72:75]\n"
"v_lshlrev_b32 v14, 16, v19\n"
"v_and_b32 v15, s63, v19\n"
"v_mfma_f32_16x16x32_bf16 v[44:47], a[36:39], a[124:127], v[44:47]\n"
"v_mul_f32 v12, v12, v172\n"
"v_mul_f32 v13, v13, v173\n"
"v_mfma_f32_16x16x32_bf16 v[44:47], a[32:35], a[120:123], v[44:47]\n"
"v_mul_f32 v14, v14, v174\n"
"v_mfma_f32_16x16x32_bf16 v[48:51], a[32:35], a[128:131], v[48:51]\n"
"v_mul_f32 v15, v15, v175\n"
"v_mfma_f32_16x16x32_bf16 v[48:51], a[36:39], a[132:135], v[48:51]\n"
"global_load_dwordx4 a[88:91], v0, s[34:35] offset:2048 sc1\n"
"v_mfma_f32_16x16x32_bf16 v[76:79], a[44:47], a[132:135], v[76:79]\n"
"v_cvt_pk_bf16_f32 v26, v12, v13\n"
"v_mfma_f32_16x16x32_bf16 v[76:79], a[40:43], a[128:131], v[76:79]\n"
"v_cvt_pk_bf16_f32 v27, v14, v15\n"
"v_mfma_f32_16x16x32_bf16 v[80:83], a[40:43], a[136:139], v[80:83]\n"
"s_waitcnt lgkmcnt(1)\n"
"global_store_dwordx4 v0, v[20:23], s[96:97] offset:-4096 sc1\n"
"ds_write_b64 v2, v[26:27] offset:16392\n"
"v_accvgpr_read_b32 v16, a196\n"
"v_mfma_f32_16x16x32_bf16 v[80:83], a[44:47], a[140:143], v[80:83]\n"
"v_accvgpr_read_b32 v17, a197\n"
"v_accvgpr_read_b32 v18, a198\n"
"v_mfma_f32_16x16x32_bf16 v[52:55], a[36:39], a[140:143], v[52:55]\n"
"v_accvgpr_read_b32 v19, a199\n"
"v_lshlrev_b32 v12, 16, v16\n"
"v_mfma_f32_16x16x32_bf16 v[52:55], a[32:35], a[136:139], v[52:55]\n"
"v_and_b32 v13, s63, v16\n"
"v_lshlrev_b32 v14, 16, v17\n"
"v_mfma_f32_16x16x32_bf16 v[56:59], a[32:35], a[144:147], v[56:59]\n"
"v_and_b32 v15, s63, v17\n"
"v_mul_f32 v12, v12, v200\n"
"v_mfma_f32_16x16x32_bf16 v[56:59], a[36:39], a[148:151], v[56:59]\n"
"global_load_dwordx4 a[92:95], v0, s[34:35] offset:3072 sc1\n"
"v_mul_f32 v13, v13, v201\n"
"v_mfma_f32_16x16x32_bf16 v[84:87], a[44:47], a[148:151], v[84:87]\n"
"v_mul_f32 v14, v14, v202\n"
"v_mul_f32 v15, v15, v203\n"
"v_mfma_f32_16x16x32_bf16 v[84:87], a[40:43], a[144:147], v[84:87]\n"
"v_cvt_pk_bf16_f32 v28, v12, v13\n"
"v_cvt_pk_bf16_f32 v29, v14, v15\n"
"v_mfma_f32_16x16x32_bf16 a[4:7], a[40:43], a[152:155], a[4:7]\n"
"s_add_u32 s34, s34, 8192\n"
"ds_write_b64 v2, v[28:29] offset:17408\n"
"v_mfma_f32_16x16x32_bf16 a[4:7], a[44:47], a[156:159], a[4:7]\n"
"v_lshlrev_b32 v12, 16, v18\n"
"v_mfma_f32_16x16x32_bf16 a[0:3], a[36:39], a[156:159], a[0:3]\n"
"v_and_b32 v13, s63, v18\n"
"v_lshlrev_b32 v14, 16, v19\n"
"v_mfma_f32_16x16x32_bf16 a[0:3], a[32:35], a[152:155], a[0:3]\n"
"s_waitcnt vmcnt(11)\n"
"v_and_b32 v15, s63, v19\n"
"v_mul_f32 v12, v12, v228\n"
"v_mfma_f32_16x16x32_bf16 v[88:91], a[48:51], a[96:99], v[88:91]\n"
"v_mul_f32 v13, v13, v229\n"
"v_mul_f32 v14, v14, v230\n"
"v_mfma_f32_16x16x32_bf16 v[88:91], a[52:55], a[100:103], v[88:91]\n"
"global_load_dwordx4 a[32:35], v0, s[34:35] offset:-4096 sc1\n"
"v_mul_f32 v15, v15, v231\n"
"v_mfma_f32_16x16x32_bf16 v[116:119], a[60:63], a[100:103], v[116:119]\n"
"ds_read_b128 a[100:103], v3 offset:1024\n"
"v_cvt_pk_bf16_f32 v30, v12, v13\n"
"v_mfma_f32_16x16x32_bf16 v[116:119], a[56:59], a[96:99], v[116:119]\n"
"ds_read_b128 a[96:99], v3 offset:0\n"
"v_cvt_pk_bf16_f32 v31, v14, v15\n"
"v_mfma_f32_16x16x32_bf16 v[120:123], a[56:59], a[104:107], v[120:123]\n"
"ds_write_b64 v2, v[30:31] offset:17416\n"
"v_mfma_f32_16x16x32_bf16 v[120:123], a[60:63], a[108:111], v[120:123]\n"
"ds_read_b128 v[20:23], v2 offset:3072\n"
"v_accvgpr_read_b32 v16, a200\n"
"v_mfma_f32_16x16x32_bf16 v[92:95], a[52:55], a[108:111], v[92:95]\n"
"ds_read_b128 a[108:111], v3 offset:3072\n"
"v_accvgpr_read_b32 v17, a201\n"
"v_mfma_f32_16x16x32_bf16 v[92:95], a[48:51], a[104:107], v[92:95]\n"
"ds_read_b128 a[104:107], v3 offset:2048\n"
"v_accvgpr_read_b32 v18, a202\n"
"v_mfma_f32_16x16x32_bf16 v[96:99], a[48:51], a[112:115], v[96:99]\n"
"v_accvgpr_read_b32 v19, a203\n"
"v_lshlrev_b32 v12, 16, v16\n"
"v_mfma_f32_16x16x32_bf16 v[96:99], a[52:55], a[116:119], v[96:99]\n"
"global_load_dwordx4 a[36:39], v0, s[34:35] offset:-3072 sc1\n"
"v_and_b32 v13, s63, v16\n"
"v_mfma_f32_16x16x32_bf16 v[124:127], a[60:63], a[116:119], v[124:127]\n"
"ds_read_b128 a[116:119], v3 offset:5120\n"
"v_lshlrev_b32 v14, 16, v17\n"
"v_mfma_f32_16x16x32_bf16 v[124:127], a[56:59], a[112:115], v[124:127]\n"
"ds_read_b128 a[112:115], v3 offset:4096\n"
"v_and_b32 v15, s63, v17\n"
"v_mfma_f32_16x16x32_bf16 v[128:131], a[56:59], a[120:123], v[128:131]\n"
"v_mul_f32 v12, v12, v148\n"
"v_mul_f32 v13, v13, v149\n"
"v_mfma_f32_16x16x32_bf16 v[128:131], a[60:63], a[124:127], v[128:131]\n"
"v_mul_f32 v14, v14, v150\n"
"v_mul_f32 v15, v15, v151\n"
"v_mfma_f32_16x16x32_bf16 v[100:103], a[52:55], a[124:127], v[100:103]\n"
"ds_read_b128 a[124:127], v3 offset:7168\n"
"v_cvt_pk_bf16_f32 v24, v12, v13\n"
"v_mfma_f32_16x16x32_bf16 v[100:103], a[48:51], a[120:123], v[100:103]\n"
"ds_read_b128 a[120:123], v3 offset:6144\n"
"v_cvt_pk_bf16_f32 v25, v14, v15\n"
"v_mfma_f32_16x16x32_bf16 v[104:107], a[48:51], a[128:131], v[104:107]\n"
"s_waitcnt lgkmcnt(6)\n"
"global_store_dwordx4 v0, v[20:23], s[96:97] offset:-1024 sc1\n"
"ds_write_b64 v2, v[24:25] offset:18432\n"
"v_lshlrev_b32 v12, 16, v18\n"
"v_mfma_f32_16x16x32_bf16 v[104:107], a[52:55], a[132:135], v[104:107]\n"
"global_load_dwordx4 a[40:43], v0, s[34:35] offset:-2048 sc1\n"
"v_and_b32 v13, s63, v18\n"
"v_mfma_f32_16x16x32_bf16 v[132:135], a[60:63], a[132:135], v[132:135]\n"
"ds_read_b128 a[132:135], v3 offset:9216\n"
"v_lshlrev_b32 v14, 16, v19\n"
"v_mfma_f32_16x16x32_bf16 v[132:135], a[56:59], a[128:131], v[132:135]\n"
"ds_read_b128 a[128:131], v3 offset:8192\n"
"v_and_b32 v15, s63, v19\n"
"v_mfma_f32_16x16x32_bf16 v[136:139], a[56:59], a[136:139], v[136:139]\n"
"v_mul_f32 v12, v12, v176\n"
"v_mul_f32 v13, v13, v177\n"
"v_mfma_f32_16x16x32_bf16 v[136:139], a[60:63], a[140:143], v[136:139]\n"
"v_mul_f32 v14, v14, v178\n"
"v_mul_f32 v15, v15, v179\n"
"v_mfma_f32_16x16x32_bf16 v[108:111], a[52:55], a[140:143], v[108:111]\n"
"ds_read_b128 a[140:143], v3 offset:11264\n"
"v_cvt_pk_bf16_f32 v26, v12, v13\n"
"v_mfma_f32_16x16x32_bf16 v[108:111], a[48:51], a[136:139], v[108:111]\n"
"ds_read_b128 a[136:139], v3 offset:10240\n"
"v_cvt_pk_bf16_f32 v27, v14, v15\n"
"v_mfma_f32_16x16x32_bf16 v[112:115], a[48:51], a[144:147], v[112:115]\n"
"ds_write_b64 v2, v[26:27] offset:18440\n"
"v_mfma_f32_16x16x32_bf16 v[112:115], a[52:55], a[148:151], v[112:115]\n"
"global_load_dwordx4 a[44:47], v0, s[34:35] offset:-1024 sc1\n"
"v_accvgpr_read_b32 v16, a204\n"
"v_mfma_f32_16x16x32_bf16 v[140:143], a[60:63], a[148:151], v[140:143]\n"
"ds_read_b128 a[148:151], v3 offset:13312\n"
"v_accvgpr_read_b32 v17, a205\n"
"v_mfma_f32_16x16x32_bf16 v[140:143], a[56:59], a[144:147], v[140:143]\n"
"ds_read_b128 a[144:147], v3 offset:12288\n"
"v_accvgpr_read_b32 v18, a206\n"
"v_mfma_f32_16x16x32_bf16 a[12:15], a[56:59], a[152:155], a[12:15]\n"
"v_accvgpr_read_b32 v19, a207\n"
"v_lshlrev_b32 v12, 16, v16\n"
"v_mfma_f32_16x16x32_bf16 a[12:15], a[60:63], a[156:159], a[12:15]\n"
"v_and_b32 v13, s63, v16\n"
"v_lshlrev_b32 v14, 16, v17\n"
"v_mfma_f32_16x16x32_bf16 a[8:11], a[52:55], a[156:159], a[8:11]\n"
"L_st5092:\n"
"s_cbranch_execz L_st7379\n"
"ds_read_b128 a[156:159], v3 offset:15360\n"
"v_and_b32 v15, s63, v17\n"
"v_mfma_f32_16x16x32_bf16 a[8:11], a[48:51], a[152:155], a[8:11]\n"
"s_waitcnt vmcnt(10) lgkmcnt(13)\n"
"ds_read_b128 a[152:155], v3 offset:14336\n"
"v_mul_f32 v12, v12, v204\n"
"v_mfma_f32_16x16x32_bf16 v[32:35], a[64:67], a[96:99], v[32:35]\n"
"ds_read_b128 v[20:23], v2 offset:6144\n"
"v_mul_f32 v13, v13, v205\n"
"v_mfma_f32_16x16x32_bf16 v[32:35], a[68:71], a[100:103], v[32:35]\n"
"global_load_dwordx4 a[48:51], v0, s[34:35] offset:0 sc1\n"
"v_mul_f32 v14, v14, v206\n"
"v_mfma_f32_16x16x32_bf16 v[60:63], a[76:79], a[100:103], v[60:63]\n"
"v_mul_f32 v15, v15, v207\n"
"v_cvt_pk_bf16_f32 v28, v12, v13\n"
"v_mfma_f32_16x16x32_bf16 v[60:63], a[72:75], a[96:99], v[60:63]\n"
"v_cvt_pk_bf16_f32 v29, v14, v15\n"
"v_mfma_f32_16x16x32_bf16 v[64:67], a[72:75], a[104:107], v[64:67]\n"
"ds_write_b64 v2, v[28:29] offset:19456\n"
"v_mfma_f32_16x16x32_bf16 v[64:67], a[76:79], a[108:111], v[64:67]\n"
"v_lshlrev_b32 v12, 16, v18\n"
"v_and_b32 v13, s63, v18\n"
"v_mfma_f32_16x16x32_bf16 v[36:39], a[68:71], a[108:111], v[36:39]\n"
"v_lshlrev_b32 v14, 16, v19\n"
"v_and_b32 v15, s63, v19\n"
"v_mfma_f32_16x16x32_bf16 v[36:39], a[64:67], a[104:107], v[36:39]\n"
"s_waitcnt lgkmcnt(12)\n"
"v_mul_f32 v12, v12, v232\n"
"v_mul_f32 v13, v13, v233\n"
"v_mfma_f32_16x16x32_bf16 v[40:43], a[64:67], a[112:115], v[40:43]\n"
"v_mul_f32 v14, v14, v234\n"
"v_mfma_f32_16x16x32_bf16 v[40:43], a[68:71], a[116:119], v[40:43]\n"
"global_load_dwordx4 a[52:55], v0, s[34:35] offset:1024 sc1\n"
"v_mul_f32 v15, v15, v235\n"
"v_mfma_f32_16x16x32_bf16 v[68:71], a[76:79], a[116:119], v[68:71]\n"
"v_cvt_pk_bf16_f32 v30, v12, v13\n"
"v_mfma_f32_16x16x32_bf16 v[68:71], a[72:75], a[112:115], v[68:71]\n"
"v_cvt_pk_bf16_f32 v31, v14, v15\n"
"v_mfma_f32_16x16x32_bf16 v[72:75], a[72:75], a[120:123], v[72:75]\n"
"s_waitcnt lgkmcnt(1)\n"
"global_store_dwordx4 v0, v[20:23], s[96:97] offset:2048 sc1\n"
"ds_write_b64 v2, v[30:31] offset:19464\n"
"v_accvgpr_read_b32 v16, a208\n"
"v_mfma_f32_16x16x32_bf16 v[72:75], a[76:79], a[124:127], v[72:75]\n"
"v_accvgpr_read_b32 v17, a209\n"
"v_accvgpr_read_b32 v18, a210\n"
"v_mfma_f32_16x16x32_bf16 v[44:47], a[68:71], a[124:127], v[44:47]\n"
"v_accvgpr_read_b32 v19, a211\n"
"v_mfma_f32_16x16x32_bf16 v[44:47], a[64:67], a[120:123], v[44:47]\n"
"v_lshlrev_b32 v12, 16, v16\n"
"v_and_b32 v13, s63, v16\n"
"v_mfma_f32_16x16x32_bf16 v[48:51], a[64:67], a[128:131], v[48:51]\n"
"v_lshlrev_b32 v14, 16, v17\n"
"v_mfma_f32_16x16x32_bf16 v[48:51], a[68:71], a[132:135], v[48:51]\n"
"global_load_dwordx4 a[56:59], v0, s[34:35] offset:2048 sc1\n"
"v_and_b32 v15, s63, v17\n"
"v_mfma_f32_16x16x32_bf16 v[76:79], a[76:79], a[132:135], v[76:79]\n"
"v_mul_f32 v12, v12, v152\n"
"v_mul_f32 v13, v13, v153\n"
"v_mfma_f32_16x16x32_bf16 v[76:79], a[72:75], a[128:131], v[76:79]\n"
"v_mul_f32 v14, v14, v154\n"
"v_mul_f32 v15, v15, v155\n"
"v_mfma_f32_16x16x32_bf16 v[80:83], a[72:75], a[136:139], v[80:83]\n"
"v_cvt_pk_bf16_f32 v24, v12, v13\n"
"v_cvt_pk_bf16_f32 v25, v14, v15\n"
"v_mfma_f32_16x16x32_bf16 v[80:83], a[76:79], a[140:143], v[80:83]\n"
"ds_write_b64 v2, v[24:25] offset:20480\n"
"v_mfma_f32_16x16x32_bf16 v[52:55], a[68:71], a[140:143], v[52:55]\n"
"v_lshlrev_b32 v12, 16, v18\n"
"v_mfma_f32_16x16x32_bf16 v[52:55], a[64:67], a[136:139], v[52:55]\n"
"v_and_b32 v13, s63, v18\n"
"v_lshlrev_b32 v14, 16, v19\n"
"v_mfma_f32_16x16x32_bf16 v[56:59], a[64:67], a[144:147], v[56:59]\n"
"v_and_b32 v15, s63, v19\n"
"v_mul_f32 v12, v12, v180\n"
"v_mfma_f32_16x16x32_bf16 v[56:59], a[68:71], a[148:151], v[56:59]\n"
"global_load_dwordx4 a[60:63], v0, s[34:35] offset:3072 sc1\n"
"v_mul_f32 v13, v13, v181\n"
"v_mfma_f32_16x16x32_bf16 v[84:87], a[76:79], a[148:151], v[84:87]\n"
"v_mul_f32 v14, v14, v182\n"
"v_mul_f32 v15, v15, v183\n"
"v_mfma_f32_16x16x32_bf16 v[84:87], a[72:75], a[144:147], v[84:87]\n"
"v_cvt_pk_bf16_f32 v26, v12, v13\n"
"v_cvt_pk_bf16_f32 v27, v14, v15\n"
"v_mfma_f32_16x16x32_bf16 a[4:7], a[72:75], a[152:155], a[4:7]\n"
"s_add_u32 s34, s34, 8192\n"
"ds_write_b64 v2, v[26:27] offset:20488\n"
"v_mfma_f32_16x16x32_bf16 a[4:7], a[76:79], a[156:159], a[4:7]\n"
"ds_read_b128 v[20:23], v2 offset:9216\n"
"v_mfma_f32_16x16x32_bf16 a[0:3], a[68:71], a[156:159], a[0:3]\n"
"v_accvgpr_read_b32 v16, a212\n"
"v_mfma_f32_16x16x32_bf16 a[0:3], a[64:67], a[152:155], a[0:3]\n"
"s_waitcnt vmcnt(10)\n"
"v_accvgpr_read_b32 v17, a213\n"
"v_accvgpr_read_b32 v18, a214\n"
"v_mfma_f32_16x16x32_bf16 v[88:91], a[80:83], a[96:99], v[88:91]\n"
"v_accvgpr_read_b32 v19, a215\n"
"v_lshlrev_b32 v12, 16, v16\n"
"v_mfma_f32_16x16x32_bf16 v[88:91], a[84:87], a[100:103], v[88:91]\n"
"global_load_dwordx4 a[64:67], v0, s[34:35] offset:-4096 sc1\n"
"v_and_b32 v13, s63, v16\n"
"v_mfma_f32_16x16x32_bf16 v[116:119], a[92:95], a[100:103], v[116:119]\n"
"ds_read_b128 a[100:103], v4 offset:1024\n"
"v_lshlrev_b32 v14, 16, v17\n"
"v_mfma_f32_16x16x32_bf16 v[116:119], a[88:91], a[96:99], v[116:119]\n"
"ds_read_b128 a[96:99], v4 offset:0\n"
"v_and_b32 v15, s63, v17\n"
"v_mfma_f32_16x16x32_bf16 v[120:123], a[88:91], a[104:107], v[120:123]\n"
"v_mul_f32 v12, v12, v208\n"
"v_mul_f32 v13, v13, v209\n"
"v_mfma_f32_16x16x32_bf16 v[120:123], a[92:95], a[108:111], v[120:123]\n"
"v_mul_f32 v14, v14, v210\n"
"v_mul_f32 v15, v15, v211\n"
"v_mfma_f32_16x16x32_bf16 v[92:95], a[84:87], a[108:111], v[92:95]\n"
"ds_read_b128 a[108:111], v4 offset:3072\n"
"v_cvt_pk_bf16_f32 v28, v12, v13\n"
"v_mfma_f32_16x16x32_bf16 v[92:95], a[80:83], a[104:107], v[92:95]\n"
"ds_read_b128 a[104:107], v4 offset:2048\n"
"v_cvt_pk_bf16_f32 v29, v14, v15\n"
"v_mfma_f32_16x16x32_bf16 v[96:99], a[80:83], a[112:115], v[96:99]\n"
"s_waitcnt lgkmcnt(4)\n"
"global_store_dwordx4 v0, v[20:23], s[98:99] offset:-3072 sc1\n"
"ds_write_b64 v2, v[28:29] offset:21504\n"
"v_lshlrev_b32 v12, 16, v18\n"
"v_mfma_f32_16x16x32_bf16 v[96:99], a[84:87], a[116:119], v[96:99]\n"
"global_load_dwordx4 a[68:71], v0, s[34:35] offset:-3072 sc1\n"
"v_and_b32 v13, s63, v18\n"
"v_mfma_f32_16x16x32_bf16 v[124:127], a[92:95], a[116:119], v[124:127]\n"
"ds_read_b128 a[116:119], v4 offset:5120\n"
"v_lshlrev_b32 v14, 16, v19\n"
"v_mfma_f32_16x16x32_bf16 v[124:127], a[88:91], a[112:115], v[124:127]\n"
"ds_read_b128 a[112:115], v4 offset:4096\n"
"v_and_b32 v15, s63, v19\n"
"v_mfma_f32_16x16x32_bf16 v[128:131], a[88:91], a[120:123], v[128:131]\n"
"v_mul_f32 v12, v12, v236\n"
"v_mul_f32 v13, v13, v237\n"
"v_mfma_f32_16x16x32_bf16 v[128:131], a[92:95], a[124:127], v[128:131]\n"
"v_mul_f32 v14, v14, v238\n"
"v_mul_f32 v15, v15, v239\n"
"v_mfma_f32_16x16x32_bf16 v[100:103], a[84:87], a[124:127], v[100:103]\n"
"ds_read_b128 a[124:127], v4 offset:7168\n"
"v_cvt_pk_bf16_f32 v30, v12, v13\n"
"v_mfma_f32_16x16x32_bf16 v[100:103], a[80:83], a[120:123], v[100:103]\n"
"ds_read_b128 a[120:123], v4 offset:6144\n"
"v_cvt_pk_bf16_f32 v31, v14, v15\n"
"v_mfma_f32_16x16x32_bf16 v[104:107], a[80:83], a[128:131], v[104:107]\n"
"ds_write_b64 v2, v[30:31] offset:21512\n"
"v_mfma_f32_16x16x32_bf16 v[104:107], a[84:87], a[132:135], v[104:107]\n"
"global_load_dwordx4 a[72:75], v0, s[34:35] offset:-2048 sc1\n"
"v_accvgpr_read_b32 v16, a216\n"
"v_mfma_f32_16x16x32_bf16 v[132:135], a[92:95], a[132:135], v[132:135]\n"
"ds_read_b128 a[132:135], v4 offset:9216\n"
"v_accvgpr_read_b32 v17, a217\n"
"v_mfma_f32_16x16x32_bf16 v[132:135], a[88:91], a[128:131], v[132:135]\n"
"ds_read_b128 a[128:131], v4 offset:8192\n"
"v_accvgpr_read_b32 v18, a218\n"
"v_mfma_f32_16x16x32_bf16 v[136:139], a[88:91], a[136:139], v[136:139]\n"
"v_accvgpr_read_b32 v19, a219\n"
"v_mfma_f32_16x16x32_bf16 v[136:139], a[92:95], a[140:143], v[136:139]\n"
"v_lshlrev_b32 v12, 16, v16\n"
"v_and_b32 v13, s63, v16\n"
"v_mfma_f32_16x16x32_bf16 v[108:111], a[84:87], a[140:143], v[108:111]\n"
"ds_read_b128 a[140:143], v4 offset:11264\n"
"v_lshlrev_b32 v14, 16, v17\n"
"v_mfma_f32_16x16x32_bf16 v[108:111], a[80:83], a[136:139], v[108:111]\n"
"ds_read_b128 a[136:139], v4 offset:10240\n"
"v_and_b32 v15, s63, v17\n"
"v_mfma_f32_16x16x32_bf16 v[112:115], a[80:83], a[144:147], v[112:115]\n"
"ds_read_b128 v[20:23], v2 offset:12288\n"
"v_mul_f32 v12, v12, v156\n"
"v_mfma_f32_16x16x32_bf16 v[112:115], a[84:87], a[148:151], v[112:115]\n"
"global_load_dwordx4 a[76:79], v0, s[34:35] offset:-1024 sc1\n"
"v_mfma_f32_16x16x32_bf16 v[140:143], a[92:95], a[148:151], v[140:143]\n"
"ds_read_b128 a[148:151], v4 offset:13312\n"
"v_mfma_f32_16x16x32_bf16 v[140:143], a[88:91], a[144:147], v[140:143]\n"
"ds_read_b128 a[144:147], v4 offset:12288\n"
"v_mfma_f32_16x16x32_bf16 a[12:15], a[88:91], a[152:155], a[12:15]\n"
"v_mul_f32 v13, v13, v157\n"
"v_mfma_f32_16x16x32_bf16 a[12:15], a[92:95], a[156:159], a[12:15]\n"
"v_mul_f32 v14, v14, v158\n"
"v_mfma_f32_16x16x32_bf16 a[8:11], a[84:87], a[156:159], a[8:11]\n"
"ds_read_b128 a[156:159], v4 offset:15360\n"
"v_mfma_f32_16x16x32_bf16 a[8:11], a[80:83], a[152:155], a[8:11]\n"
"s_waitcnt vmcnt(10) lgkmcnt(14)\n"
"ds_read_b128 a[152:155], v4 offset:14336\n"
"v_mfma_f32_16x16x32_bf16 v[32:35], a[32:35], a[96:99], v[32:35]\n"
"v_mul_f32 v15, v15, v159\n"
"v_mfma_f32_16x16x32_bf16 v[32:35], a[36:39], a[100:103], v[32:35]\n"
"global_load_dwordx4 a[80:83], v0, s[34:35] offset:0 sc1\n"
"v_mfma_f32_16x16x32_bf16 v[60:63], a[44:47], a[100:103], v[60:63]\n"
"s_add_u32 s65, s65, 1\n"
"v_cvt_pk_bf16_f32 v24, v12, v13\n"
"v_mfma_f32_16x16x32_bf16 v[60:63], a[40:43], a[96:99], v[60:63]\n"
"v_cvt_pk_bf16_f32 v25, v14, v15\n"
"v_mfma_f32_16x16x32_bf16 v[64:67], a[40:43], a[104:107], v[64:67]\n"
"s_waitcnt lgkmcnt(4)\n"
"global_store_dwordx4 v0, v[20:23], s[98:99] offset:0 sc1\n"
"ds_write_b64 v2, v[24:25] offset:22528\n"
"v_lshlrev_b32 v12, 16, v18\n"
"v_mfma_f32_16x16x32_bf16 v[64:67], a[44:47], a[108:111], v[64:67]\n"
"v_and_b32 v13, s63, v18\n"
"v_lshlrev_b32 v14, 16, v19\n"
"v_mfma_f32_16x16x32_bf16 v[36:39], a[36:39], a[108:111], v[36:39]\n"
"v_and_b32 v15, s63, v19\n"
"v_mul_f32 v12, v12, v184\n"
"v_mfma_f32_16x16x32_bf16 v[36:39], a[32:35], a[104:107], v[36:39]\n"
"v_mul_f32 v13, v13, v185\n"
"v_mul_f32 v14, v14, v186\n"
"v_mfma_f32_16x16x32_bf16 v[40:43], a[32:35], a[112:115], v[40:43]\n"
"v_mul_f32 v15, v15, v187\n"
"v_cvt_pk_bf16_f32 v26, v12, v13\n"
"v_mfma_f32_16x16x32_bf16 v[40:43], a[36:39], a[116:119], v[40:43]\n"
"global_load_dwordx4 a[84:87], v0, s[34:35] offset:1024 sc1\n"
"v_cvt_pk_bf16_f32 v27, v14, v15\n"
"v_mfma_f32_16x16x32_bf16 v[68:71], a[44:47], a[116:119], v[68:71]\n"
"ds_write_b64 v2, v[26:27] offset:22536\n"
"v_mfma_f32_16x16x32_bf16 v[68:71], a[40:43], a[112:115], v[68:71]\n"
"v_accvgpr_read_b32 v16, a220\n"
"v_accvgpr_read_b32 v17, a221\n"
"v_mfma_f32_16x16x32_bf16 v[72:75], a[40:43], a[120:123], v[72:75]\n"
"v_accvgpr_read_b32 v18, a222\n"
"v_accvgpr_read_b32 v19, a223\n"
"v_mfma_f32_16x16x32_bf16 v[72:75], a[44:47], a[124:127], v[72:75]\n"
"v_lshlrev_b32 v12, 16, v16\n"
"v_and_b32 v13, s63, v16\n"
"v_mfma_f32_16x16x32_bf16 v[44:47], a[36:39], a[124:127], v[44:47]\n"
"v_lshlrev_b32 v14, 16, v17\n"
"v_and_b32 v15, s63, v17\n"
"v_mfma_f32_16x16x32_bf16 v[44:47], a[32:35], a[120:123], v[44:47]\n"
"v_mul_f32 v12, v12, v212\n"
"v_mul_f32 v13, v13, v213\n"
"v_mfma_f32_16x16x32_bf16 v[48:51], a[32:35], a[128:131], v[48:51]\n"
"v_mul_f32 v14, v14, v214\n"
"v_mul_f32 v15, v15, v215\n"
"v_mfma_f32_16x16x32_bf16 v[48:51], a[36:39], a[132:135], v[48:51]\n"
"global_load_dwordx4 a[88:91], v0, s[34:35] offset:2048 sc1\n"
"v_cvt_pk_bf16_f32 v28, v12, v13\n"
"v_mfma_f32_16x16x32_bf16 v[76:79], a[44:47], a[132:135], v[76:79]\n"
"v_cvt_pk_bf16_f32 v29, v14, v15\n"
"v_mfma_f32_16x16x32_bf16 v[76:79], a[40:43], a[128:131], v[76:79]\n"
"ds_write_b64 v2, v[28:29] offset:23552\n"
"v_mfma_f32_16x16x32_bf16 v[80:83], a[40:43], a[136:139], v[80:83]\n"
"v_lshlrev_b32 v12, 16, v18\n"
"v_and_b32 v13, s63, v18\n"
"v_mfma_f32_16x16x32_bf16 v[80:83], a[44:47], a[140:143], v[80:83]\n"
"v_lshlrev_b32 v14, 16, v19\n"
"v_and_b32 v15, s63, v19\n"
"v_mfma_f32_16x16x32_bf16 v[52:55], a[36:39], a[140:143], v[52:55]\n"
"v_mul_f32 v12, v12, v240\n"
"v_mul_f32 v13, v13, v241\n"
"v_mfma_f32_16x16x32_bf16 v[52:55], a[32:35], a[136:139], v[52:55]\n"
"s_waitcnt lgkmcnt(3)\n"
"v_mul_f32 v14, v14, v242\n"
"v_mul_f32 v15, v15, v243\n"
"v_mfma_f32_16x16x32_bf16 v[56:59], a[32:35], a[144:147], v[56:59]\n"
"v_cvt_pk_bf16_f32 v30, v12, v13\n"
"v_cvt_pk_bf16_f32 v31, v14, v15\n"
"v_mfma_f32_16x16x32_bf16 v[56:59], a[36:39], a[148:151], v[56:59]\n"
"global_load_dwordx4 a[92:95], v0, s[34:35] offset:3072 sc1\n"
"v_mfma_f32_16x16x32_bf16 v[84:87], a[44:47], a[148:151], v[84:87]\n"
"ds_write_b64 v2, v[30:31] offset:23560\n"
"v_mfma_f32_16x16x32_bf16 v[84:87], a[40:43], a[144:147], v[84:87]\n"
"v_accvgpr_read_b32 v16, a224\n"
"v_accvgpr_read_b32 v17, a225\n"
"v_mfma_f32_16x16x32_bf16 a[4:7], a[40:43], a[152:155], a[4:7]\n"
"s_add_u32 s34, s34, 8192\n"
"v_accvgpr_read_b32 v18, a226\n"
"v_accvgpr_read_b32 v19, a227\n"
"v_mfma_f32_16x16x32_bf16 a[4:7], a[44:47], a[156:159], a[4:7]\n"
"v_lshlrev_b32 v12, 16, v16\n"
"v_and_b32 v13, s63, v16\n"
"v_mfma_f32_16x16x32_bf16 a[0:3], a[36:39], a[156:159], a[0:3]\n"
"v_lshlrev_b32 v14, 16, v17\n"
"v_and_b32 v15, s63, v17\n"
"v_mfma_f32_16x16x32_bf16 a[0:3], a[32:35], a[152:155], a[0:3]\n"
"s_waitcnt vmcnt(10)\n"
"v_mul_f32 v12, v12, v160\n"
"v_mul_f32 v13, v13, v161\n"
"v_mfma_f32_16x16x32_bf16 v[88:91], a[48:51], a[96:99], v[88:91]\n"
"v_mul_f32 v14, v14, v162\n"
"v_mul_f32 v15, v15, v163\n"
"v_mfma_f32_16x16x32_bf16 v[88:91], a[52:55], a[100:103], v[88:91]\n"
"global_load_dwordx4 a[32:35], v0, s[34:35] offset:-4096 sc1\n"
"v_cvt_pk_bf16_f32 v24, v12, v13\n"
"v_mfma_f32_16x16x32_bf16 v[116:119], a[60:63], a[100:103], v[116:119]\n"
"ds_read_b128 a[100:103], v5 offset:1024\n"
"v_cvt_pk_bf16_f32 v25, v14, v15\n"
"v_mfma_f32_16x16x32_bf16 v[116:119], a[56:59], a[96:99], v[116:119]\n"
"ds_read_b128 a[96:99], v5 offset:0\n"
"v_mfma_f32_16x16x32_bf16 v[120:123], a[56:59], a[104:107], v[120:123]\n"
"ds_write_b64 v2, v[24:25] offset:24576\n"
"v_mfma_f32_16x16x32_bf16 v[120:123], a[60:63], a[108:111], v[120:123]\n"
"v_lshlrev_b32 v12, 16, v18\n"
"v_and_b32 v13, s63, v18\n"
"v_mfma_f32_16x16x32_bf16 v[92:95], a[52:55], a[108:111], v[92:95]\n"
"ds_read_b128 a[108:111], v5 offset:3072\n"
"v_lshlrev_b32 v14, 16, v19\n"
"v_mfma_f32_16x16x32_bf16 v[92:95], a[48:51], a[104:107], v[92:95]\n"
"ds_read_b128 a[104:107], v5 offset:2048\n"
"v_and_b32 v15, s63, v19\n"
"v_mfma_f32_16x16x32_bf16 v[96:99], a[48:51], a[112:115], v[96:99]\n"
"v_mul_f32 v12, v12, v188\n"
"v_mul_f32 v13, v13, v189\n"
"v_mfma_f32_16x16x32_bf16 v[96:99], a[52:55], a[116:119], v[96:99]\n"
"global_load_dwordx4 a[36:39], v0, s[34:35] offset:-3072 sc1\n"
"v_mul_f32 v14, v14, v190\n"
"v_mfma_f32_16x16x32_bf16 v[124:127], a[60:63], a[116:119], v[124:127]\n"
"ds_read_b128 a[116:119], v5 offset:5120\n"
"v_mul_f32 v15, v15, v191\n"
"v_mfma_f32_16x16x32_bf16 v[124:127], a[56:59], a[112:115], v[124:127]\n"
"ds_read_b128 a[112:115], v5 offset:4096\n"
"v_cvt_pk_bf16_f32 v26, v12, v13\n"
"v_mfma_f32_16x16x32_bf16 v[128:131], a[56:59], a[120:123], v[128:131]\n"
"v_cvt_pk_bf16_f32 v27, v14, v15\n"
"v_mfma_f32_16x16x32_bf16 v[128:131], a[60:63], a[124:127], v[128:131]\n"
"ds_write_b64 v2, v[26:27] offset:24584\n"
"v_mfma_f32_16x16x32_bf16 v[100:103], a[52:55], a[124:127], v[100:103]\n"
"ds_read_b128 a[124:127], v5 offset:7168\n"
"v_accvgpr_read_b32 v16, a228\n"
"v_mfma_f32_16x16x32_bf16 v[100:103], a[48:51], a[120:123], v[100:103]\n"
"ds_read_b128 a[120:123], v5 offset:6144\n"
"v_accvgpr_read_b32 v17, a229\n"
"v_mfma_f32_16x16x32_bf16 v[104:107], a[48:51], a[128:131], v[104:107]\n"
"v_accvgpr_read_b32 v18, a230\n"
"v_accvgpr_read_b32 v19, a231\n"
"v_mfma_f32_16x16x32_bf16 v[104:107], a[52:55], a[132:135], v[104:107]\n"
"global_load_dwordx4 a[40:43], v0, s[34:35] offset:-2048 sc1\n"
"v_lshlrev_b32 v12, 16, v16\n"
"v_mfma_f32_16x16x32_bf16 v[132:135], a[60:63], a[132:135], v[132:135]\n"
"ds_read_b128 a[132:135], v5 offset:9216\n"
"v_and_b32 v13, s63, v16\n"
"v_mfma_f32_16x16x32_bf16 v[132:135], a[56:59], a[128:131], v[132:135]\n"
"ds_read_b128 a[128:131], v5 offset:8192\n"
"v_lshlrev_b32 v14, 16, v17\n"
"v_mfma_f32_16x16x32_bf16 v[136:139], a[56:59], a[136:139], v[136:139]\n"
"v_and_b32 v15, s63, v17\n"
"v_mul_f32 v12, v12, v216\n"
"v_mfma_f32_16x16x32_bf16 v[136:139], a[60:63], a[140:143], v[136:139]\n"
"v_mul_f32 v13, v13, v217\n"
"v_mul_f32 v14, v14, v218\n"
"v_mfma_f32_16x16x32_bf16 v[108:111], a[52:55], a[140:143], v[108:111]\n"
"ds_read_b128 a[140:143], v5 offset:11264\n"
"v_mul_f32 v15, v15, v219\n"
"v_mfma_f32_16x16x32_bf16 v[108:111], a[48:51], a[136:139], v[108:111]\n"
"ds_read_b128 a[136:139], v5 offset:10240\n"
"v_cvt_pk_bf16_f32 v28, v12, v13\n"
"v_mfma_f32_16x16x32_bf16 v[112:115], a[48:51], a[144:147], v[112:115]\n"
"v_cvt_pk_bf16_f32 v29, v14, v15\n"
"v_lshlrev_b32 v12, 16, v18\n"
"v_mfma_f32_16x16x32_bf16 v[112:115], a[52:55], a[148:151], v[112:115]\n"
"global_load_dwordx4 a[44:47], v0, s[34:35] offset:-1024 sc1\n"
"v_and_b32 v13, s63, v18\n"
"v_mfma_f32_16x16x32_bf16 v[140:143], a[60:63], a[148:151], v[140:143]\n"
"ds_read_b128 a[148:151], v5 offset:13312\n"
"v_lshlrev_b32 v14, 16, v19\n"
"v_mfma_f32_16x16x32_bf16 v[140:143], a[56:59], a[144:147], v[140:143]\n"
"ds_read_b128 a[144:147], v5 offset:12288\n"
"v_and_b32 v15, s63, v19\n"
"v_mfma_f32_16x16x32_bf16 a[12:15], a[56:59], a[152:155], a[12:15]\n"
"v_mul_f32 v12, v12, v244\n"
"v_mul_f32 v13, v13, v245\n"
"v_mfma_f32_16x16x32_bf16 a[12:15], a[60:63], a[156:159], a[12:15]\n"
"v_mul_f32 v14, v14, v246\n"
"v_mul_f32 v15, v15, v247\n"
"v_mfma_f32_16x16x32_bf16 a[8:11], a[52:55], a[156:159], a[8:11]\n"
"ds_read_b128 a[156:159], v5 offset:15360\n"
"v_cvt_pk_bf16_f32 v30, v12, v13\n"
"v_mfma_f32_16x16x32_bf16 a[8:11], a[48:51], a[152:155], a[8:11]\n"
"s_waitcnt vmcnt(9) lgkmcnt(12)\n"
"ds_read_b128 a[152:155], v5 offset:14336\n"
"v_cvt_pk_bf16_f32 v31, v14, v15\n"
"v_mfma_f32_16x16x32_bf16 v[32:35], a[64:67], a[96:99], v[32:35]\n"
"global_store_dwordx4 v0, v[28:31], s[54:55] offset:-3072 sc1\n"
"ds_write_b128 v2, v[28:31] offset:25600\n"
"v_mfma_f32_16x16x32_bf16 v[32:35], a[68:71], a[100:103], v[32:35]\n"
"global_load_dwordx4 a[48:51], v0, s[34:35] offset:0 sc1\n"
"v_accvgpr_read_b32 v16, a232\n"
"v_mfma_f32_16x16x32_bf16 v[60:63], a[76:79], a[100:103], v[60:63]\n"
"v_accvgpr_read_b32 v17, a233\n"
"v_accvgpr_read_b32 v18, a234\n"
"v_mfma_f32_16x16x32_bf16 v[60:63], a[72:75], a[96:99], v[60:63]\n"
"v_accvgpr_read_b32 v19, a235\n"
"v_lshlrev_b32 v12, 16, v16\n"
"v_mfma_f32_16x16x32_bf16 v[64:67], a[72:75], a[104:107], v[64:67]\n"
"v_and_b32 v13, s63, v16\n"
"v_lshlrev_b32 v14, 16, v17\n"
"v_mfma_f32_16x16x32_bf16 v[64:67], a[76:79], a[108:111], v[64:67]\n"
"v_and_b32 v15, s63, v17\n"
"v_mul_f32 v12, v12, v164\n"
"v_mfma_f32_16x16x32_bf16 v[36:39], a[68:71], a[108:111], v[36:39]\n"
"v_mul_f32 v13, v13, v165\n"
"v_mul_f32 v14, v14, v166\n"
"v_mfma_f32_16x16x32_bf16 v[36:39], a[64:67], a[104:107], v[36:39]\n"
"s_waitcnt lgkmcnt(9)\n"
"v_mul_f32 v15, v15, v167\n"
"v_cvt_pk_bf16_f32 v24, v12, v13\n"
"v_mfma_f32_16x16x32_bf16 v[40:43], a[64:67], a[112:115], v[40:43]\n"
"v_cvt_pk_bf16_f32 v25, v14, v15\n"
"v_mfma_f32_16x16x32_bf16 v[40:43], a[68:71], a[116:119], v[40:43]\n"
"global_load_dwordx4 a[52:55], v0, s[34:35] offset:1024 sc1\n"
"v_mfma_f32_16x16x32_bf16 v[68:71], a[76:79], a[116:119], v[68:71]\n"
"ds_write_b64 v2, v[24:25] offset:26624\n"
"v_mfma_f32_16x16x32_bf16 v[68:71], a[72:75], a[112:115], v[68:71]\n"
"v_lshlrev_b32 v12, 16, v18\n"
"v_and_b32 v13, s63, v18\n"
"v_mfma_f32_16x16x32_bf16 v[72:75], a[72:75], a[120:123], v[72:75]\n"
"v_lshlrev_b32 v14, 16, v19\n"
"v_and_b32 v15, s63, v19\n"
"v_mfma_f32_16x16x32_bf16 v[72:75], a[76:79], a[124:127], v[72:75]\n"
"v_mul_f32 v12, v12, v192\n"
"v_mul_f32 v13, v13, v193\n"
"v_mfma_f32_16x16x32_bf16 v[44:47], a[68:71], a[124:127], v[44:47]\n"
"v_mul_f32 v14, v14, v194\n"
"v_mul_f32 v15, v15, v195\n"
"v_mfma_f32_16x16x32_bf16 v[44:47], a[64:67], a[120:123], v[44:47]\n"
"s_waitcnt lgkmcnt(6)\n"
"v_cvt_pk_bf16_f32 v26, v12, v13\n"
"v_cvt_pk_bf16_f32 v27, v14, v15\n"
"v_mfma_f32_16x16x32_bf16 v[48:51], a[64:67], a[128:131], v[48:51]\n"
"ds_write_b64 v2, v[26:27] offset:26632\n"
"v_mfma_f32_16x16x32_bf16 v[48:51], a[68:71], a[132:135], v[48:51]\n"
"global_load_dwordx4 a[56:59], v0, s[34:35] offset:2048 sc1\n"
"v_accvgpr_read_b32 v16, a236\n"
"v_mfma_f32_16x16x32_bf16 v[76:79], a[76:79], a[132:135], v[76:79]\n"
"v_accvgpr_read_b32 v17, a237\n"
"v_accvgpr_read_b32 v18, a238\n"
"v_mfma_f32_16x16x32_bf16 v[76:79], a[72:75], a[128:131], v[76:79]\n"
"v_accvgpr_read_b32 v19, a239\n"
"v_lshlrev_b32 v12, 16, v16\n"
"v_mfma_f32_16x16x32_bf16 v[80:83], a[72:75], a[136:139], v[80:83]\n"
"v_and_b32 v13, s63, v16\n"
"v_mfma_f32_16x16x32_bf16 v[80:83], a[76:79], a[140:143], v[80:83]\n"
"v_lshlrev_b32 v14, 16, v17\n"
"v_mfma_f32_16x16x32_bf16 v[52:55], a[68:71], a[140:143], v[52:55]\n"
"v_and_b32 v15, s63, v17\n"
"v_mul_f32 v12, v12, v220\n"
"v_mfma_f32_16x16x32_bf16 v[52:55], a[64:67], a[136:139], v[52:55]\n"
"s_waitcnt lgkmcnt(3)\n"
"v_mul_f32 v13, v13, v221\n"
"v_mfma_f32_16x16x32_bf16 v[56:59], a[64:67], a[144:147], v[56:59]\n"
"v_mul_f32 v14, v14, v222\n"
"v_mul_f32 v15, v15, v223\n"
"v_mfma_f32_16x16x32_bf16 v[56:59], a[68:71], a[148:151], v[56:59]\n"
"global_load_dwordx4 a[60:63], v0, s[34:35] offset:3072 sc1\n"
"v_cvt_pk_bf16_f32 v28, v12, v13\n"
"v_mfma_f32_16x16x32_bf16 v[84:87], a[76:79], a[148:151], v[84:87]\n"
"v_cvt_pk_bf16_f32 v29, v14, v15\n"
"v_mfma_f32_16x16x32_bf16 v[84:87], a[72:75], a[144:147], v[84:87]\n"
"v_lshlrev_b32 v12, 16, v18\n"
"v_and_b32 v13, s63, v18\n"
"v_mfma_f32_16x16x32_bf16 a[4:7], a[72:75], a[152:155], a[4:7]\n"
"s_add_u32 s34, s34, 8192\n"
"v_lshlrev_b32 v14, 16, v19\n"
"v_and_b32 v15, s63, v19\n"
"v_mfma_f32_16x16x32_bf16 a[4:7], a[76:79], a[156:159], a[4:7]\n"
"v_mul_f32 v12, v12, v248\n"
"v_mul_f32 v13, v13, v249\n"
"v_mfma_f32_16x16x32_bf16 a[0:3], a[68:71], a[156:159], a[0:3]\n"
"v_mul_f32 v14, v14, v250\n"
"v_mul_f32 v15, v15, v251\n"
"v_mfma_f32_16x16x32_bf16 a[0:3], a[64:67], a[152:155], a[0:3]\n"
"s_waitcnt vmcnt(9)\n"
"v_cvt_pk_bf16_f32 v30, v12, v13\n"
"v_cvt_pk_bf16_f32 v31, v14, v15\n"
"v_mfma_f32_16x16x32_bf16 v[88:91], a[80:83], a[96:99], v[88:91]\n"
"global_store_dwordx4 v0, v[28:31], s[54:55] offset:-1024 sc1\n"
"ds_write_b128 v2, v[28:31] offset:27648\n"
"v_mfma_f32_16x16x32_bf16 v[88:91], a[84:87], a[100:103], v[88:91]\n"
"global_load_dwordx4 a[64:67], v0, s[34:35] offset:-4096 sc1\n"
"v_mfma_f32_16x16x32_bf16 v[116:119], a[92:95], a[100:103], v[116:119]\n"
"ds_read_b128 a[100:103], v2 offset:1024\n"
"v_mfma_f32_16x16x32_bf16 v[116:119], a[88:91], a[96:99], v[116:119]\n"
"ds_read_b128 a[96:99], v2 offset:0\n"
"v_accvgpr_read_b32 v16, a240\n"
"v_mfma_f32_16x16x32_bf16 v[120:123], a[88:91], a[104:107], v[120:123]\n"
"v_accvgpr_read_b32 v17, a241\n"
"v_accvgpr_read_b32 v18, a242\n"
"v_mfma_f32_16x16x32_bf16 v[120:123], a[92:95], a[108:111], v[120:123]\n"
"v_accvgpr_read_b32 v19, a243\n"
"v_lshlrev_b32 v12, 16, v16\n"
"v_mfma_f32_16x16x32_bf16 v[92:95], a[84:87], a[108:111], v[92:95]\n"
"ds_read_b128 a[108:111], v2 offset:3072\n"
"v_and_b32 v13, s63, v16\n"
"v_mfma_f32_16x16x32_bf16 v[92:95], a[80:83], a[104:107], v[92:95]\n"
"ds_read_b128 a[104:107], v2 offset:2048\n"
"v_lshlrev_b32 v14, 16, v17\n"
"v_mfma_f32_16x16x32_bf16 v[96:99], a[80:83], a[112:115], v[96:99]\n"
"v_and_b32 v15, s63, v17\n"
"v_mul_f32 v12, v12, v168\n"
"v_mfma_f32_16x16x32_bf16 v[96:99], a[84:87], a[116:119], v[96:99]\n"
"global_load_dwordx4 a[68:71], v0, s[34:35] offset:-3072 sc1\n"
"v_mul_f32 v13, v13, v169\n"
"v_mfma_f32_16x16x32_bf16 v[124:127], a[92:95], a[116:119], v[124:127]\n"
"ds_read_b128 a[116:119], v2 offset:5120\n"
"v_mul_f32 v14, v14, v170\n"
"v_mfma_f32_16x16x32_bf16 v[124:127], a[88:91], a[112:115], v[124:127]\n"
"ds_read_b128 a[112:115], v2 offset:4096\n"
"v_mul_f32 v15, v15, v171\n"
"v_mfma_f32_16x16x32_bf16 v[128:131], a[88:91], a[120:123], v[128:131]\n"
"v_cvt_pk_bf16_f32 v24, v12, v13\n"
"v_cvt_pk_bf16_f32 v25, v14, v15\n"
"v_mfma_f32_16x16x32_bf16 v[128:131], a[92:95], a[124:127], v[128:131]\n"
"v_lshlrev_b32 v12, 16, v18\n"
"v_and_b32 v13, s63, v18\n"
"v_mfma_f32_16x16x32_bf16 v[100:103], a[84:87], a[124:127], v[100:103]\n"
"ds_read_b128 a[124:127], v2 offset:7168\n"
"v_lshlrev_b32 v14, 16, v19\n"
"v_mfma_f32_16x16x32_bf16 v[100:103], a[80:83], a[120:123], v[100:103]\n"
"ds_read_b128 a[120:123], v2 offset:6144\n"
"v_mov_b32 v8, s65\n"
"v_mfma_f32_16x16x32_bf16 v[104:107], a[80:83], a[128:131], v[104:107]\n"
"v_mov_b32 v9, s60\n"
"v_and_b32 v15, s63, v19\n"
"v_mfma_f32_16x16x32_bf16 v[104:107], a[84:87], a[132:135], v[104:107]\n"
"global_load_dwordx4 a[72:75], v0, s[34:35] offset:-2048 sc1\n"
"v_mul_f32 v12, v12, v196\n"
"v_mfma_f32_16x16x32_bf16 v[132:135], a[92:95], a[132:135], v[132:135]\n"
"buffer_store_dwordx2 v[8:9], off, s[56:59], %13 sc1\n"
"ds_read_b128 a[132:135], v2 offset:9216\n"
"v_mfma_f32_16x16x32_bf16 v[132:135], a[88:91], a[128:131], v[132:135]\n"
"ds_read_b128 a[128:131], v2 offset:8192\n"
"v_mul_f32 v13, v13, v197\n"
"v_mfma_f32_16x16x32_bf16 v[136:139], a[88:91], a[136:139], v[136:139]\n"
"v_mul_f32 v14, v14, v198\n"
"v_mul_f32 v15, v15, v199\n"
"v_mfma_f32_16x16x32_bf16 v[136:139], a[92:95], a[140:143], v[136:139]\n"
"v_cvt_pk_bf16_f32 v26, v12, v13\n"
"v_cvt_pk_bf16_f32 v27, v14, v15\n"
"v_mfma_f32_16x16x32_bf16 v[108:111], a[84:87], a[140:143], v[108:111]\n"
"ds_read_b128 a[140:143], v2 offset:11264\n"
"v_mfma_f32_16x16x32_bf16 v[108:111], a[80:83], a[136:139], v[108:111]\n"
"ds_read_b128 a[136:139], v2 offset:10240\n"
"v_mfma_f32_16x16x32_bf16 v[112:115], a[80:83], a[144:147], v[112:115]\n"
"global_store_dwordx4 v0, v[24:27], s[54:55] offset:0 sc1\n"
"ds_write_b128 v2, v[24:27] offset:28672\n"
"v_mfma_f32_16x16x32_bf16 v[112:115], a[84:87], a[148:151], v[112:115]\n"
"global_load_dwordx4 a[76:79], v0, s[34:35] offset:-1024 sc1\n"
"v_accvgpr_read_b32 v16, a248\n"
"v_mfma_f32_16x16x32_bf16 v[140:143], a[92:95], a[148:151], v[140:143]\n"
"ds_read_b128 a[148:151], v2 offset:13312\n"
"v_accvgpr_read_b32 v17, a249\n"
"v_mfma_f32_16x16x32_bf16 v[140:143], a[88:91], a[144:147], v[140:143]\n"
"ds_read_b128 a[144:147], v2 offset:12288\n"
"v_accvgpr_read_b32 v18, a250\n"
"v_mfma_f32_16x16x32_bf16 a[12:15], a[88:91], a[152:155], a[12:15]\n"
"v_accvgpr_read_b32 v19, a251\n"
"v_lshlrev_b32 v12, 16, v16\n"
"v_mfma_f32_16x16x32_bf16 a[12:15], a[92:95], a[156:159], a[12:15]\n"
"v_and_b32 v13, s63, v16\n"
"v_lshlrev_b32 v14, 16, v17\n"
"v_mfma_f32_16x16x32_bf16 a[8:11], a[84:87], a[156:159], a[8:11]\n"
"ds_read_b128 a[156:159], v2 offset:15360\n"
"v_and_b32 v15, s63, v17\n"
"v_mfma_f32_16x16x32_bf16 a[8:11], a[80:83], a[152:155], a[8:11]\n"
"s_waitcnt vmcnt(12) lgkmcnt(12)\n"
"ds_read_b128 a[152:155], v2 offset:14336\n"
"v_accvgpr_read_b32 v10, a16\n"
"v_mfma_f32_16x16x32_bf16 v[144:147], a[32:35], a[96:99], v[144:147]\n"
"v_accvgpr_read_b32 v11, a17\n"
"v_mul_f32 v12, v12, v10\n"
"v_mfma_f32_16x16x32_bf16 v[144:147], a[36:39], a[100:103], v[144:147]\n"
"global_load_dwordx4 a[80:83], v0, s[34:35] offset:0 sc1\n"
"v_mul_f32 v13, v13, v11\n"
"v_mfma_f32_16x16x32_bf16 v[172:175], a[44:47], a[100:103], v[172:175]\n"
"v_accvgpr_read_b32 v10, a18\n"
"v_accvgpr_read_b32 v11, a19\n"
"v_mfma_f32_16x16x32_bf16 v[172:175], a[40:43], a[96:99], v[172:175]\n"
"v_mul_f32 v14, v14, v10\n"
"v_mul_f32 v15, v15, v11\n"
"v_mfma_f32_16x16x32_bf16 v[176:179], a[40:43], a[104:107], v[176:179]\n"
"v_cvt_pk_bf16_f32 v28, v12, v13\n"
"v_cvt_pk_bf16_f32 v29, v14, v15\n"
"v_mfma_f32_16x16x32_bf16 v[176:179], a[44:47], a[108:111], v[176:179]\n"
"v_lshlrev_b32 v12, 16, v18\n"
"v_and_b32 v13, s63, v18\n"
"v_mfma_f32_16x16x32_bf16 v[148:151], a[36:39], a[108:111], v[148:151]\n"
"v_lshlrev_b32 v14, 16, v19\n"
"v_and_b32 v15, s63, v19\n"
"v_mfma_f32_16x16x32_bf16 v[148:151], a[32:35], a[104:107], v[148:151]\n"
"s_waitcnt lgkmcnt(9)\n"
"v_accvgpr_read_b32 v10, a20\n"
"v_accvgpr_read_b32 v11, a21\n"
"v_mfma_f32_16x16x32_bf16 v[152:155], a[32:35], a[112:115], v[152:155]\n"
"v_mul_f32 v12, v12, v10\n"
"v_mul_f32 v13, v13, v11\n"
"v_mfma_f32_16x16x32_bf16 v[152:155], a[36:39], a[116:119], v[152:155]\n"
"global_load_dwordx4 a[84:87], v0, s[34:35] offset:1024 sc1\n"
"v_accvgpr_read_b32 v10, a22\n"
"v_mfma_f32_16x16x32_bf16 v[180:183], a[44:47], a[116:119], v[180:183]\n"
"v_accvgpr_read_b32 v11, a23\n"
"v_mfma_f32_16x16x32_bf16 v[180:183], a[40:43], a[112:115], v[180:183]\n"
"v_mul_f32 v14, v14, v10\n"
"v_mul_f32 v15, v15, v11\n"
"v_mfma_f32_16x16x32_bf16 v[184:187], a[40:43], a[120:123], v[184:187]\n"
"v_cvt_pk_bf16_f32 v30, v12, v13\n"
"v_mfma_f32_16x16x32_bf16 v[184:187], a[44:47], a[124:127], v[184:187]\n"
"v_cvt_pk_bf16_f32 v31, v14, v15\n"
"v_mfma_f32_16x16x32_bf16 v[156:159], a[36:39], a[124:127], v[156:159]\n"
"global_store_dwordx4 v0, v[28:31], s[54:55] offset:2048 sc1\n"
"ds_write_b128 v2, v[28:31] offset:30720\n"
"v_mfma_f32_16x16x32_bf16 v[156:159], a[32:35], a[120:123], v[156:159]\n"
"s_waitcnt lgkmcnt(6)\n"
"v_accvgpr_read_b32 v16, a244\n"
"v_mfma_f32_16x16x32_bf16 v[160:163], a[32:35], a[128:131], v[160:163]\n"
"v_accvgpr_read_b32 v17, a245\n"
"v_accvgpr_read_b32 v18, a246\n"
"v_mfma_f32_16x16x32_bf16 v[160:163], a[36:39], a[132:135], v[160:163]\n"
"global_load_dwordx4 a[88:91], v0, s[34:35] offset:2048 sc1\n"
"v_accvgpr_read_b32 v19, a247\n"
"v_mfma_f32_16x16x32_bf16 v[188:191], a[44:47], a[132:135], v[188:191]\n"
"L_st5702:\n"
"s_cbranch_execz L_st7944\n"
"v_lshlrev_b32 v12, 16, v16\n"
"v_mfma_f32_16x16x32_bf16 v[188:191], a[40:43], a[128:131], v[188:191]\n"
"v_and_b32 v13, s63, v16\n"
"v_lshlrev_b32 v14, 16, v17\n"
"v_mfma_f32_16x16x32_bf16 v[192:195], a[40:43], a[136:139], v[192:195]\n"
"v_and_b32 v15, s63, v17\n"
"v_mfma_f32_16x16x32_bf16 v[192:195], a[44:47], a[140:143], v[192:195]\n"
"v_mul_f32 v12, v12, v224\n"
"v_mul_f32 v13, v13, v225\n"
"v_mfma_f32_16x16x32_bf16 v[164:167], a[36:39], a[140:143], v[164:167]\n"
"v_mul_f32 v14, v14, v226\n"
"v_mul_f32 v15, v15, v227\n"
"v_mfma_f32_16x16x32_bf16 v[164:167], a[32:35], a[136:139], v[164:167]\n"
"s_waitcnt lgkmcnt(1)\n"
"v_cvt_pk_bf16_f32 v24, v12, v13\n"
"v_cvt_pk_bf16_f32 v25, v14, v15\n"
"v_mfma_f32_16x16x32_bf16 v[168:171], a[32:35], a[144:147], v[168:171]\n"
"ds_write_b64 v2, v[24:25] offset:29696\n"
"v_mfma_f32_16x16x32_bf16 v[168:171], a[36:39], a[148:151], v[168:171]\n"
"global_load_dwordx4 a[92:95], v0, s[34:35] offset:3072 sc1\n"
"v_mfma_f32_16x16x32_bf16 v[196:199], a[44:47], a[148:151], v[196:199]\n"
"v_lshlrev_b32 v12, 16, v18\n"
"v_and_b32 v13, s63, v18\n"
"v_mfma_f32_16x16x32_bf16 v[196:199], a[40:43], a[144:147], v[196:199]\n"
"v_lshlrev_b32 v14, 16, v19\n"
"v_and_b32 v15, s63, v19\n"
"v_mfma_f32_16x16x32_bf16 a[20:23], a[40:43], a[152:155], a[20:23]\n"
"s_add_u32 s34, s34, 8192\n"
"v_mul_f32 v12, v12, v252\n"
"v_mul_f32 v13, v13, v253\n"
"v_mfma_f32_16x16x32_bf16 a[20:23], a[44:47], a[156:159], a[20:23]\n"
"v_mul_f32 v14, v14, v254\n"
"v_mul_f32 v15, v15, v255\n"
"v_mfma_f32_16x16x32_bf16 a[16:19], a[36:39], a[156:159], a[16:19]\n"
"v_cvt_pk_bf16_f32 v26, v12, v13\n"
"v_cvt_pk_bf16_f32 v27, v14, v15\n"
"v_mfma_f32_16x16x32_bf16 a[16:19], a[32:35], a[152:155], a[16:19]\n"
"s_waitcnt vmcnt(12)\n"
"ds_write_b64 v2, v[26:27] offset:29704\n"
"v_mfma_f32_16x16x32_bf16 v[200:203], a[48:51], a[96:99], v[200:203]\n"
"v_accvgpr_read_b32 v16, a252\n"
"v_mfma_f32_16x16x32_bf16 v[200:203], a[52:55], a[100:103], v[200:203]\n"
"global_load_dwordx4 a[32:35], v0, s[34:35] offset:-4096 sc1\n"
"v_accvgpr_read_b32 v17, a253\n"
"v_mfma_f32_16x16x32_bf16 v[228:231], a[60:63], a[100:103], v[228:231]\n"
"ds_read_b128 a[100:103], v3 offset:1024\n"
"v_accvgpr_read_b32 v18, a254\n"
"v_mfma_f32_16x16x32_bf16 v[228:231], a[56:59], a[96:99], v[228:231]\n"
"ds_read_b128 a[96:99], v3 offset:0\n"
"v_accvgpr_read_b32 v19, a255\n"
"v_mfma_f32_16x16x32_bf16 v[232:235], a[56:59], a[104:107], v[232:235]\n"
"v_lshlrev_b32 v12, 16, v16\n"
"v_and_b32 v13, s63, v16\n"
"v_mfma_f32_16x16x32_bf16 v[232:235], a[60:63], a[108:111], v[232:235]\n"
"v_lshlrev_b32 v14, 16, v17\n"
"v_and_b32 v15, s63, v17\n"
"v_mfma_f32_16x16x32_bf16 v[204:207], a[52:55], a[108:111], v[204:207]\n"
"ds_read_b128 a[108:111], v3 offset:3072\n"
"v_accvgpr_read_b32 v10, a24\n"
"v_mfma_f32_16x16x32_bf16 v[204:207], a[48:51], a[104:107], v[204:207]\n"
"ds_read_b128 a[104:107], v3 offset:2048\n"
"v_accvgpr_read_b32 v11, a25\n"
"v_mfma_f32_16x16x32_bf16 v[208:211], a[48:51], a[112:115], v[208:211]\n"
"v_mul_f32 v12, v12, v10\n"
"v_mul_f32 v13, v13, v11\n"
"v_mfma_f32_16x16x32_bf16 v[208:211], a[52:55], a[116:119], v[208:211]\n"
"global_load_dwordx4 a[36:39], v0, s[34:35] offset:-3072 sc1\n"
"v_accvgpr_read_b32 v10, a26\n"
"v_mfma_f32_16x16x32_bf16 v[236:239], a[60:63], a[116:119], v[236:239]\n"
"ds_read_b128 a[116:119], v3 offset:5120\n"
"v_accvgpr_read_b32 v11, a27\n"
"v_mfma_f32_16x16x32_bf16 v[236:239], a[56:59], a[112:115], v[236:239]\n"
"ds_read_b128 a[112:115], v3 offset:4096\n"
"v_mul_f32 v14, v14, v10\n"
"v_mfma_f32_16x16x32_bf16 v[240:243], a[56:59], a[120:123], v[240:243]\n"
"v_mul_f32 v15, v15, v11\n"
"v_cvt_pk_bf16_f32 v28, v12, v13\n"
"v_mfma_f32_16x16x32_bf16 v[240:243], a[60:63], a[124:127], v[240:243]\n"
"v_cvt_pk_bf16_f32 v29, v14, v15\n"
"v_lshlrev_b32 v12, 16, v18\n"
"v_mfma_f32_16x16x32_bf16 v[212:215], a[52:55], a[124:127], v[212:215]\n"
"ds_read_b128 a[124:127], v3 offset:7168\n"
"v_and_b32 v13, s63, v18\n"
"v_mfma_f32_16x16x32_bf16 v[212:215], a[48:51], a[120:123], v[212:215]\n"
"ds_read_b128 a[120:123], v3 offset:6144\n"
"v_lshlrev_b32 v14, 16, v19\n"
"v_mfma_f32_16x16x32_bf16 v[216:219], a[48:51], a[128:131], v[216:219]\n"
"v_and_b32 v15, s63, v19\n"
"v_accvgpr_read_b32 v10, a28\n"
"v_mfma_f32_16x16x32_bf16 v[216:219], a[52:55], a[132:135], v[216:219]\n"
"global_load_dwordx4 a[40:43], v0, s[34:35] offset:-2048 sc1\n"
"v_accvgpr_read_b32 v11, a29\n"
"v_mfma_f32_16x16x32_bf16 v[244:247], a[60:63], a[132:135], v[244:247]\n"
"ds_read_b128 a[132:135], v3 offset:9216\n"
"v_mul_f32 v12, v12, v10\n"
"v_mfma_f32_16x16x32_bf16 v[244:247], a[56:59], a[128:131], v[244:247]\n"
"ds_read_b128 a[128:131], v3 offset:8192\n"
"v_mul_f32 v13, v13, v11\n"
"v_mfma_f32_16x16x32_bf16 v[248:251], a[56:59], a[136:139], v[248:251]\n"
"v_accvgpr_read_b32 v10, a30\n"
"v_accvgpr_read_b32 v11, a31\n"
"v_mfma_f32_16x16x32_bf16 v[248:251], a[60:63], a[140:143], v[248:251]\n"
"v_mul_f32 v14, v14, v10\n"
"v_mul_f32 v15, v15, v11\n"
"v_mfma_f32_16x16x32_bf16 v[220:223], a[52:55], a[140:143], v[220:223]\n"
"ds_read_b128 a[140:143], v3 offset:11264\n"
"v_cvt_pk_bf16_f32 v30, v12, v13\n"
"v_mfma_f32_16x16x32_bf16 v[220:223], a[48:51], a[136:139], v[220:223]\n"
"ds_read_b128 a[136:139], v3 offset:10240\n"
"v_cvt_pk_bf16_f32 v31, v14, v15\n"
"v_mfma_f32_16x16x32_bf16 v[224:227], a[48:51], a[144:147], v[224:227]\n"
"global_store_dwordx4 v0, v[28:31], s[54:55] offset:3072 sc1\n"
"ds_write_b128 v2, v[28:31] offset:31744\n"
"v_mfma_f32_16x16x32_bf16 v[224:227], a[52:55], a[148:151], v[224:227]\n"
"global_load_dwordx4 a[44:47], v0, s[34:35] offset:-1024 sc1\n"
"v_mfma_f32_16x16x32_bf16 v[252:255], a[60:63], a[148:151], v[252:255]\n"
"ds_read_b128 a[148:151], v3 offset:13312\n"
"v_mfma_f32_16x16x32_bf16 v[252:255], a[56:59], a[144:147], v[252:255]\n"
"ds_read_b128 a[144:147], v3 offset:12288\n"
"v_mfma_f32_16x16x32_bf16 a[28:31], a[56:59], a[152:155], a[28:31]\n"
"v_mfma_f32_16x16x32_bf16 a[28:31], a[60:63], a[156:159], a[28:31]\n"
"v_mfma_f32_16x16x32_bf16 a[24:27], a[52:55], a[156:159], a[24:27]\n"
"ds_read_b128 a[156:159], v3 offset:15360\n"
"v_mfma_f32_16x16x32_bf16 a[24:27], a[48:51], a[152:155], a[24:27]\n"
"s_waitcnt vmcnt(10) lgkmcnt(12)\n"
"ds_read_b128 a[152:155], v3 offset:14336\n"
"v_mfma_f32_16x16x32_bf16 v[144:147], a[64:67], a[96:99], v[144:147]\n"
"ds_read_b128 v[24:27], v2 offset:16384\n"
"v_mfma_f32_16x16x32_bf16 v[144:147], a[68:71], a[100:103], v[144:147]\n"
"global_load_dwordx4 a[48:51], v0, s[34:35] offset:0 sc1\n"
"v_mfma_f32_16x16x32_bf16 v[172:175], a[76:79], a[100:103], v[172:175]\n"
"v_mfma_f32_16x16x32_bf16 v[172:175], a[72:75], a[96:99], v[172:175]\n"
"v_mfma_f32_16x16x32_bf16 v[176:179], a[72:75], a[104:107], v[176:179]\n"
"v_mfma_f32_16x16x32_bf16 v[176:179], a[76:79], a[108:111], v[176:179]\n"
"v_mfma_f32_16x16x32_bf16 v[148:151], a[68:71], a[108:111], v[148:151]\n"
"v_mfma_f32_16x16x32_bf16 v[148:151], a[64:67], a[104:107], v[148:151]\n"
"s_waitcnt lgkmcnt(10)\n"
"v_mfma_f32_16x16x32_bf16 v[152:155], a[64:67], a[112:115], v[152:155]\n"
"v_mfma_f32_16x16x32_bf16 v[152:155], a[68:71], a[116:119], v[152:155]\n"
"global_load_dwordx4 a[52:55], v0, s[34:35] offset:1024 sc1\n"
"v_mfma_f32_16x16x32_bf16 v[180:183], a[76:79], a[116:119], v[180:183]\n"
"v_mfma_f32_16x16x32_bf16 v[180:183], a[72:75], a[112:115], v[180:183]\n"
"v_mfma_f32_16x16x32_bf16 v[184:187], a[72:75], a[120:123], v[184:187]\n"
"s_waitcnt lgkmcnt(0)\n"
"global_store_dwordx4 v0, v[24:27], s[52:53] offset:-4096 sc1\n"
"v_mfma_f32_16x16x32_bf16 v[184:187], a[76:79], a[124:127], v[184:187]\n"
"v_mfma_f32_16x16x32_bf16 v[156:159], a[68:71], a[124:127], v[156:159]\n"
"v_mfma_f32_16x16x32_bf16 v[156:159], a[64:67], a[120:123], v[156:159]\n"
"v_mfma_f32_16x16x32_bf16 v[160:163], a[64:67], a[128:131], v[160:163]\n"
"v_mfma_f32_16x16x32_bf16 v[160:163], a[68:71], a[132:135], v[160:163]\n"
"global_load_dwordx4 a[56:59], v0, s[34:35] offset:2048 sc1\n"
"v_mfma_f32_16x16x32_bf16 v[188:191], a[76:79], a[132:135], v[188:191]\n"
"v_mfma_f32_16x16x32_bf16 v[188:191], a[72:75], a[128:131], v[188:191]\n"
"v_mfma_f32_16x16x32_bf16 v[192:195], a[72:75], a[136:139], v[192:195]\n"
"v_mfma_f32_16x16x32_bf16 v[192:195], a[76:79], a[140:143], v[192:195]\n"
"v_mfma_f32_16x16x32_bf16 v[164:167], a[68:71], a[140:143], v[164:167]\n"
"v_mfma_f32_16x16x32_bf16 v[164:167], a[64:67], a[136:139], v[164:167]\n"
"v_mfma_f32_16x16x32_bf16 v[168:171], a[64:67], a[144:147], v[168:171]\n"
"v_mfma_f32_16x16x32_bf16 v[168:171], a[68:71], a[148:151], v[168:171]\n"
"global_load_dwordx4 a[60:63], v0, s[34:35] offset:3072 sc1\n"
"v_mfma_f32_16x16x32_bf16 v[196:199], a[76:79], a[148:151], v[196:199]\n"
"v_mfma_f32_16x16x32_bf16 v[196:199], a[72:75], a[144:147], v[196:199]\n"
"v_mfma_f32_16x16x32_bf16 a[20:23], a[72:75], a[152:155], a[20:23]\n"
"s_add_u32 s34, s34, 8192\n"
"v_mfma_f32_16x16x32_bf16 a[20:23], a[76:79], a[156:159], a[20:23]\n"
"v_mfma_f32_16x16x32_bf16 a[16:19], a[68:71], a[156:159], a[16:19]\n"
"v_mfma_f32_16x16x32_bf16 a[16:19], a[64:67], a[152:155], a[16:19]\n"
"s_waitcnt vmcnt(10)\n"
"ds_read_b128 v[28:31], v2 offset:17408\n"
"v_mfma_f32_16x16x32_bf16 v[200:203], a[80:83], a[96:99], v[200:203]\n"
"v_mfma_f32_16x16x32_bf16 v[200:203], a[84:87], a[100:103], v[200:203]\n"
"global_load_dwordx4 a[64:67], v0, s[34:35] offset:-4096 sc1\n"
"v_mfma_f32_16x16x32_bf16 v[228:231], a[92:95], a[100:103], v[228:231]\n"
"ds_read_b128 a[100:103], v4 offset:1024\n"
"v_mfma_f32_16x16x32_bf16 v[228:231], a[88:91], a[96:99], v[228:231]\n"
"ds_read_b128 a[96:99], v4 offset:0\n"
"v_mfma_f32_16x16x32_bf16 v[232:235], a[88:91], a[104:107], v[232:235]\n"
"v_mfma_f32_16x16x32_bf16 v[232:235], a[92:95], a[108:111], v[232:235]\n"
"v_mfma_f32_16x16x32_bf16 v[204:207], a[84:87], a[108:111], v[204:207]\n"
"ds_read_b128 a[108:111], v4 offset:3072\n"
"v_mfma_f32_16x16x32_bf16 v[204:207], a[80:83], a[104:107], v[204:207]\n"
"ds_read_b128 a[104:107], v4 offset:2048\n"
"v_mfma_f32_16x16x32_bf16 v[208:211], a[80:83], a[112:115], v[208:211]\n"
"v_mfma_f32_16x16x32_bf16 v[208:211], a[84:87], a[116:119], v[208:211]\n"
"global_load_dwordx4 a[68:71], v0, s[34:35] offset:-3072 sc1\n"
"v_mfma_f32_16x16x32_bf16 v[236:239], a[92:95], a[116:119], v[236:239]\n"
"s_waitcnt lgkmcnt(4)\n"
"global_store_dwordx4 v0, v[28:31], s[52:53] offset:-3072 sc1\n"
"ds_read_b128 a[116:119], v4 offset:5120\n"
"v_mfma_f32_16x16x32_bf16 v[236:239], a[88:91], a[112:115], v[236:239]\n"
"ds_read_b128 a[112:115], v4 offset:4096\n"
"v_mfma_f32_16x16x32_bf16 v[240:243], a[88:91], a[120:123], v[240:243]\n"
"v_mfma_f32_16x16x32_bf16 v[240:243], a[92:95], a[124:127], v[240:243]\n"
"v_mfma_f32_16x16x32_bf16 v[212:215], a[84:87], a[124:127], v[212:215]\n"
"ds_read_b128 a[124:127], v4 offset:7168\n"
"v_mfma_f32_16x16x32_bf16 v[212:215], a[80:83], a[120:123], v[212:215]\n"
"ds_read_b128 a[120:123], v4 offset:6144\n"
"v_mfma_f32_16x16x32_bf16 v[216:219], a[80:83], a[128:131], v[216:219]\n"
"v_mfma_f32_16x16x32_bf16 v[216:219], a[84:87], a[132:135], v[216:219]\n"
"global_load_dwordx4 a[72:75], v0, s[34:35] offset:-2048 sc1\n"
"v_mfma_f32_16x16x32_bf16 v[244:247], a[92:95], a[132:135], v[244:247]\n"
"ds_read_b128 a[132:135], v4 offset:9216\n"
"v_mfma_f32_16x16x32_bf16 v[244:247], a[88:91], a[128:131], v[244:247]\n"
"ds_read_b128 a[128:131], v4 offset:8192\n"
"v_mfma_f32_16x16x32_bf16 v[248:251], a[88:91], a[136:139], v[248:251]\n"
"v_mfma_f32_16x16x32_bf16 v[248:251], a[92:95], a[140:143], v[248:251]\n"
"v_mfma_f32_16x16x32_bf16 v[220:223], a[84:87], a[140:143], v[220:223]\n"
"ds_read_b128 a[140:143], v4 offset:11264\n"
"v_mfma_f32_16x16x32_bf16 v[220:223], a[80:83], a[136:139], v[220:223]\n"
"ds_read_b128 a[136:139], v4 offset:10240\n"
"v_mfma_f32_16x16x32_bf16 v[224:227], a[80:83], a[144:147], v[224:227]\n"
"v_mfma_f32_16x16x32_bf16 v[224:227], a[84:87], a[148:151], v[224:227]\n"
"global_load_dwordx4 a[76:79], v0, s[34:35] offset:-1024 sc1\n"
"v_mfma_f32_16x16x32_bf16 v[252:255], a[92:95], a[148:151], v[252:255]\n"
"ds_read_b128 a[148:151], v4 offset:13312\n"
"v_mfma_f32_16x16x32_bf16 v[252:255], a[88:91], a[144:147], v[252:255]\n"
"ds_read_b128 a[144:147], v4 offset:12288\n"
"v_mfma_f32_16x16x32_bf16 a[28:31], a[88:91], a[152:155], a[28:31]\n"
"v_mfma_f32_16x16x32_bf16 a[28:31], a[92:95], a[156:159], a[28:31]\n"
"ds_read_b128 v[24:27], v2 offset:18432\n"
"v_mfma_f32_16x16x32_bf16 a[24:27], a[84:87], a[156:159], a[24:27]\n"
"ds_read_b128 a[156:159], v4 offset:15360\n"
"v_mfma_f32_16x16x32_bf16 a[24:27], a[80:83], a[152:155], a[24:27]\n"
"s_waitcnt vmcnt(10) lgkmcnt(12)\n"
"ds_read_b128 a[152:155], v4 offset:14336\n"
"v_mfma_f32_16x16x32_bf16 v[144:147], a[32:35], a[96:99], v[144:147]\n"
"v_mfma_f32_16x16x32_bf16 v[144:147], a[36:39], a[100:103], v[144:147]\n"
"global_load_dwordx4 a[80:83], v0, s[34:35] offset:0 sc1\n"
"v_mfma_f32_16x16x32_bf16 v[172:175], a[44:47], a[100:103], v[172:175]\n"
"v_mfma_f32_16x16x32_bf16 v[172:175], a[40:43], a[96:99], v[172:175]\n"
"v_mfma_f32_16x16x32_bf16 v[176:179], a[40:43], a[104:107], v[176:179]\n"
"v_mfma_f32_16x16x32_bf16 v[176:179], a[44:47], a[108:111], v[176:179]\n"
"v_mfma_f32_16x16x32_bf16 v[148:151], a[36:39], a[108:111], v[148:151]\n"
"v_mfma_f32_16x16x32_bf16 v[148:151], a[32:35], a[104:107], v[148:151]\n"
"s_waitcnt lgkmcnt(9)\n"
"v_mfma_f32_16x16x32_bf16 v[152:155], a[32:35], a[112:115], v[152:155]\n"
"s_waitcnt lgkmcnt(2)\n"
"global_store_dwordx4 v0, v[24:27], s[52:53] offset:-2048 sc1\n"
"v_mfma_f32_16x16x32_bf16 v[152:155], a[36:39], a[116:119], v[152:155]\n"
"global_load_dwordx4 a[84:87], v0, s[34:35] offset:1024 sc1\n"
"v_mfma_f32_16x16x32_bf16 v[180:183], a[44:47], a[116:119], v[180:183]\n"
"v_mfma_f32_16x16x32_bf16 v[180:183], a[40:43], a[112:115], v[180:183]\n"
"v_mfma_f32_16x16x32_bf16 v[184:187], a[40:43], a[120:123], v[184:187]\n"
"v_mfma_f32_16x16x32_bf16 v[184:187], a[44:47], a[124:127], v[184:187]\n"
"v_mfma_f32_16x16x32_bf16 v[156:159], a[36:39], a[124:127], v[156:159]\n"
"v_mfma_f32_16x16x32_bf16 v[156:159], a[32:35], a[120:123], v[156:159]\n"
"v_mfma_f32_16x16x32_bf16 v[160:163], a[32:35], a[128:131], v[160:163]\n"
"v_mfma_f32_16x16x32_bf16 v[160:163], a[36:39], a[132:135], v[160:163]\n"
"global_load_dwordx4 a[88:91], v0, s[34:35] offset:2048 sc1\n"
"v_mfma_f32_16x16x32_bf16 v[188:191], a[44:47], a[132:135], v[188:191]\n"
"v_mfma_f32_16x16x32_bf16 v[188:191], a[40:43], a[128:131], v[188:191]\n"
"v_mfma_f32_16x16x32_bf16 v[192:195], a[40:43], a[136:139], v[192:195]\n"
"v_mfma_f32_16x16x32_bf16 v[192:195], a[44:47], a[140:143], v[192:195]\n"
"v_mfma_f32_16x16x32_bf16 v[164:167], a[36:39], a[140:143], v[164:167]\n"
"v_mfma_f32_16x16x32_bf16 v[164:167], a[32:35], a[136:139], v[164:167]\n"
"s_waitcnt lgkmcnt(0)\n"
"v_mfma_f32_16x16x32_bf16 v[168:171], a[32:35], a[144:147], v[168:171]\n"
"v_mfma_f32_16x16x32_bf16 v[168:171], a[36:39], a[148:151], v[168:171]\n"
"global_load_dwordx4 a[92:95], v0, s[34:35] offset:3072 sc1\n"
"v_mfma_f32_16x16x32_bf16 v[196:199], a[44:47], a[148:151], v[196:199]\n"
"v_mfma_f32_16x16x32_bf16 v[196:199], a[40:43], a[144:147], v[196:199]\n"
"ds_read_b128 v[28:31], v2 offset:19456\n"
"v_mfma_f32_16x16x32_bf16 a[20:23], a[40:43], a[152:155], a[20:23]\n"
"s_add_u32 s34, s34, 8192\n"
"v_mfma_f32_16x16x32_bf16 a[20:23], a[44:47], a[156:159], a[20:23]\n"
"v_mfma_f32_16x16x32_bf16 a[16:19], a[36:39], a[156:159], a[16:19]\n"
"v_mfma_f32_16x16x32_bf16 a[16:19], a[32:35], a[152:155], a[16:19]\n"
"s_waitcnt vmcnt(10)\n"
"v_mfma_f32_16x16x32_bf16 v[200:203], a[48:51], a[96:99], v[200:203]\n"
"v_mfma_f32_16x16x32_bf16 v[200:203], a[52:55], a[100:103], v[200:203]\n"
"global_load_dwordx4 a[32:35], v0, s[34:35] offset:-4096 sc1\n"
"v_mfma_f32_16x16x32_bf16 v[228:231], a[60:63], a[100:103], v[228:231]\n"
"ds_read_b128 a[100:103], v5 offset:1024\n"
"v_mfma_f32_16x16x32_bf16 v[228:231], a[56:59], a[96:99], v[228:231]\n"
"ds_read_b128 a[96:99], v5 offset:0\n"
"v_mfma_f32_16x16x32_bf16 v[232:235], a[56:59], a[104:107], v[232:235]\n"
"v_mfma_f32_16x16x32_bf16 v[232:235], a[60:63], a[108:111], v[232:235]\n"
"v_mfma_f32_16x16x32_bf16 v[204:207], a[52:55], a[108:111], v[204:207]\n"
"s_waitcnt lgkmcnt(2)\n"
"global_store_dwordx4 v0, v[28:31], s[52:53] offset:-1024 sc1\n"
"ds_read_b128 a[108:111], v5 offset:3072\n"
"v_mfma_f32_16x16x32_bf16 v[204:207], a[48:51], a[104:107], v[204:207]\n"
"ds_read_b128 a[104:107], v5 offset:2048\n"
"v_mfma_f32_16x16x32_bf16 v[208:211], a[48:51], a[112:115], v[208:211]\n"
"v_mfma_f32_16x16x32_bf16 v[208:211], a[52:55], a[116:119], v[208:211]\n"
"global_load_dwordx4 a[36:39], v0, s[34:35] offset:-3072 sc1\n"
"v_mfma_f32_16x16x32_bf16 v[236:239], a[60:63], a[116:119], v[236:239]\n"
"ds_read_b128 a[116:119], v5 offset:5120\n"
"v_mfma_f32_16x16x32_bf16 v[236:239], a[56:59], a[112:115], v[236:239]\n"
"ds_read_b128 a[112:115], v5 offset:4096\n"
"v_mfma_f32_16x16x32_bf16 v[240:243], a[56:59], a[120:123], v[240:243]\n"
"v_mfma_f32_16x16x32_bf16 v[240:243], a[60:63], a[124:127], v[240:243]\n"
"v_mfma_f32_16x16x32_bf16 v[212:215], a[52:55], a[124:127], v[212:215]\n"
"ds_read_b128 a[124:127], v5 offset:7168\n"
"v_mfma_f32_16x16x32_bf16 v[212:215], a[48:51], a[120:123], v[212:215]\n"
"ds_read_b128 a[120:123], v5 offset:6144\n"
"v_mfma_f32_16x16x32_bf16 v[216:219], a[48:51], a[128:131], v[216:219]\n"
"v_mfma_f32_16x16x32_bf16 v[216:219], a[52:55], a[132:135], v[216:219]\n"
"global_load_dwordx4 a[40:43], v0, s[34:35] offset:-2048 sc1\n"
"v_mfma_f32_16x16x32_bf16 v[244:247], a[60:63], a[132:135], v[244:247]\n"
"ds_read_b128 a[132:135], v5 offset:9216\n"
"v_mfma_f32_16x16x32_bf16 v[244:247], a[56:59], a[128:131], v[244:247]\n"
"ds_read_b128 a[128:131], v5 offset:8192\n"
"v_mfma_f32_16x16x32_bf16 v[248:251], a[56:59], a[136:139], v[248:251]\n"
"v_mfma_f32_16x16x32_bf16 v[248:251], a[60:63], a[140:143], v[248:251]\n"
"v_mfma_f32_16x16x32_bf16 v[220:223], a[52:55], a[140:143], v[220:223]\n"
"ds_read_b128 a[140:143], v5 offset:11264\n"
"v_mfma_f32_16x16x32_bf16 v[220:223], a[48:51], a[136:139], v[220:223]\n"
"ds_read_b128 a[136:139], v5 offset:10240\n"
"v_mfma_f32_16x16x32_bf16 v[224:227], a[48:51], a[144:147], v[224:227]\n"
"ds_read_b128 v[24:27], v2 offset:20480\n"
"v_mfma_f32_16x16x32_bf16 v[224:227], a[52:55], a[148:151], v[224:227]\n"
"global_load_dwordx4 a[44:47], v0, s[34:35] offset:-1024 sc1\n"
"v_mfma_f32_16x16x32_bf16 v[252:255], a[60:63], a[148:151], v[252:255]\n"
"ds_read_b128 a[148:151], v5 offset:13312\n"
"v_mfma_f32_16x16x32_bf16 v[252:255], a[56:59], a[144:147], v[252:255]\n"
"ds_read_b128 a[144:147], v5 offset:12288\n"
"v_mfma_f32_16x16x32_bf16 a[28:31], a[56:59], a[152:155], a[28:31]\n"
"v_mfma_f32_16x16x32_bf16 a[28:31], a[60:63], a[156:159], a[28:31]\n"
"v_mfma_f32_16x16x32_bf16 a[24:27], a[52:55], a[156:159], a[24:27]\n"
"ds_read_b128 a[156:159], v5 offset:15360\n"
"v_mfma_f32_16x16x32_bf16 a[24:27], a[48:51], a[152:155], a[24:27]\n"
"s_waitcnt vmcnt(10) lgkmcnt(12)\n"
"ds_read_b128 a[152:155], v5 offset:14336\n"
"v_mfma_f32_16x16x32_bf16 v[144:147], a[64:67], a[96:99], v[144:147]\n"
"v_mfma_f32_16x16x32_bf16 v[144:147], a[68:71], a[100:103], v[144:147]\n"
"global_load_dwordx4 a[48:51], v0, s[34:35] offset:0 sc1\n"
"v_mfma_f32_16x16x32_bf16 v[172:175], a[76:79], a[100:103], v[172:175]\n"
"v_mfma_f32_16x16x32_bf16 v[172:175], a[72:75], a[96:99], v[172:175]\n"
"v_mfma_f32_16x16x32_bf16 v[176:179], a[72:75], a[104:107], v[176:179]\n"
"s_waitcnt lgkmcnt(4)\n"
"global_store_dwordx4 v0, v[24:27], s[52:53] offset:0 sc1\n"
"v_mfma_f32_16x16x32_bf16 v[176:179], a[76:79], a[108:111], v[176:179]\n"
"v_mfma_f32_16x16x32_bf16 v[148:151], a[68:71], a[108:111], v[148:151]\n"
"v_mfma_f32_16x16x32_bf16 v[148:151], a[64:67], a[104:107], v[148:151]\n"
"v_mfma_f32_16x16x32_bf16 v[152:155], a[64:67], a[112:115], v[152:155]\n"
"v_mfma_f32_16x16x32_bf16 v[152:155], a[68:71], a[116:119], v[152:155]\n"
"global_load_dwordx4 a[52:55], v0, s[34:35] offset:1024 sc1\n"
"v_mfma_f32_16x16x32_bf16 v[180:183], a[76:79], a[116:119], v[180:183]\n"
"v_mfma_f32_16x16x32_bf16 v[180:183], a[72:75], a[112:115], v[180:183]\n"
"v_mfma_f32_16x16x32_bf16 v[184:187], a[72:75], a[120:123], v[184:187]\n"
"v_mfma_f32_16x16x32_bf16 v[184:187], a[76:79], a[124:127], v[184:187]\n"
"v_mfma_f32_16x16x32_bf16 v[156:159], a[68:71], a[124:127], v[156:159]\n"
"v_mfma_f32_16x16x32_bf16 v[156:159], a[64:67], a[120:123], v[156:159]\n"
"v_mfma_f32_16x16x32_bf16 v[160:163], a[64:67], a[128:131], v[160:163]\n"
"v_mfma_f32_16x16x32_bf16 v[160:163], a[68:71], a[132:135], v[160:163]\n"
"global_load_dwordx4 a[56:59], v0, s[34:35] offset:2048 sc1\n"
"v_mfma_f32_16x16x32_bf16 v[188:191], a[76:79], a[132:135], v[188:191]\n"
"v_mfma_f32_16x16x32_bf16 v[188:191], a[72:75], a[128:131], v[188:191]\n"
"v_mfma_f32_16x16x32_bf16 v[192:195], a[72:75], a[136:139], v[192:195]\n"
"v_mfma_f32_16x16x32_bf16 v[192:195], a[76:79], a[140:143], v[192:195]\n"
"v_mfma_f32_16x16x32_bf16 v[164:167], a[68:71], a[140:143], v[164:167]\n"
"v_mfma_f32_16x16x32_bf16 v[164:167], a[64:67], a[136:139], v[164:167]\n"
"s_waitcnt lgkmcnt(0)\n"
"ds_read_b128 v[28:31], v2 offset:21504\n"
"v_mfma_f32_16x16x32_bf16 v[168:171], a[64:67], a[144:147], v[168:171]\n"
"v_mfma_f32_16x16x32_bf16 v[168:171], a[68:71], a[148:151], v[168:171]\n"
"global_load_dwordx4 a[60:63], v0, s[34:35] offset:3072 sc1\n"
"v_mfma_f32_16x16x32_bf16 v[196:199], a[76:79], a[148:151], v[196:199]\n"
"v_mfma_f32_16x16x32_bf16 v[196:199], a[72:75], a[144:147], v[196:199]\n"
"v_mfma_f32_16x16x32_bf16 a[20:23], a[72:75], a[152:155], a[20:23]\n"
"s_add_u32 s34, s34, 8192\n"
"v_mfma_f32_16x16x32_bf16 a[20:23], a[76:79], a[156:159], a[20:23]\n"
"v_mfma_f32_16x16x32_bf16 a[16:19], a[68:71], a[156:159], a[16:19]\n"
"v_mfma_f32_16x16x32_bf16 a[16:19], a[64:67], a[152:155], a[16:19]\n"
"s_waitcnt vmcnt(10)\n"
"v_mfma_f32_16x16x32_bf16 v[200:203], a[80:83], a[96:99], v[200:203]\n"
"v_mfma_f32_16x16x32_bf16 v[200:203], a[84:87], a[100:103], v[200:203]\n"
"global_load_dwordx4 a[64:67], v0, s[34:35] offset:-4096 sc1\n"
"v_mfma_f32_16x16x32_bf16 v[228:231], a[92:95], a[100:103], v[228:231]\n"
"s_waitcnt lgkmcnt(0)\n"
"global_store_dwordx4 v0, v[28:31], s[52:53] offset:1024 sc1\n"
"ds_read_b128 a[100:103], v2 offset:17408\n"
"v_mfma_f32_16x16x32_bf16 v[228:231], a[88:91], a[96:99], v[228:231]\n"
"ds_read_b128 a[96:99], v2 offset:16384\n"
"v_mfma_f32_16x16x32_bf16 v[232:235], a[88:91], a[104:107], v[232:235]\n"
"v_mfma_f32_16x16x32_bf16 v[232:235], a[92:95], a[108:111], v[232:235]\n"
"v_mfma_f32_16x16x32_bf16 v[204:207], a[84:87], a[108:111], v[204:207]\n"
"ds_read_b128 a[108:111], v2 offset:19456\n"
"v_mfma_f32_16x16x32_bf16 v[204:207], a[80:83], a[104:107], v[204:207]\n"
"ds_read_b128 a[104:107], v2 offset:18432\n"
"v_mfma_f32_16x16x32_bf16 v[208:211], a[80:83], a[112:115], v[208:211]\n"
"v_mfma_f32_16x16x32_bf16 v[208:211], a[84:87], a[116:119], v[208:211]\n"
"global_load_dwordx4 a[68:71], v0, s[34:35] offset:-3072 sc1\n"
"v_mfma_f32_16x16x32_bf16 v[236:239], a[92:95], a[116:119], v[236:239]\n"
"ds_read_b128 a[116:119], v2 offset:21504\n"
"v_mfma_f32_16x16x32_bf16 v[236:239], a[88:91], a[112:115], v[236:239]\n"
"ds_read_b128 a[112:115], v2 offset:20480\n"
"v_mfma_f32_16x16x32_bf16 v[240:243], a[88:91], a[120:123], v[240:243]\n"
"v_mfma_f32_16x16x32_bf16 v[240:243], a[92:95], a[124:127], v[240:243]\n"
"v_mfma_f32_16x16x32_bf16 v[212:215], a[84:87], a[124:127], v[212:215]\n"
"ds_read_b128 a[124:127], v2 offset:23552\n"
"v_mfma_f32_16x16x32_bf16 v[212:215], a[80:83], a[120:123], v[212:215]\n"
"ds_read_b128 a[120:123], v2 offset:22528\n"
"v_mfma_f32_16x16x32_bf16 v[216:219], a[80:83], a[128:131], v[216:219]\n"
"v_mfma_f32_16x16x32_bf16 v[216:219], a[84:87], a[132:135], v[216:219]\n"
"global_load_dwordx4 a[72:75], v0, s[34:35] offset:-2048 sc1\n"
"v_mfma_f32_16x16x32_bf16 v[244:247], a[92:95], a[132:135], v[244:247]\n"
"ds_read_b128 a[132:135], v2 offset:25600\n"
"v_mfma_f32_16x16x32_bf16 v[244:247], a[88:91], a[128:131], v[244:247]\n"
"ds_read_b128 a[128:131], v2 offset:24576\n"
"v_mfma_f32_16x16x32_bf16 v[248:251], a[88:91], a[136:139], v[248:251]\n"
"v_mfma_f32_16x16x32_bf16 v[248:251], a[92:95], a[140:143], v[248:251]\n"
"ds_read_b128 v[24:27], v2 offset:22528\n"
"v_mfma_f32_16x16x32_bf16 v[220:223], a[84:87], a[140:143], v[220:223]\n"
"ds_read_b128 a[140:143], v2 offset:27648\n"
"v_mfma_f32_16x16x32_bf16 v[220:223], a[80:83], a[136:139], v[220:223]\n"
"ds_read_b128 a[136:139], v2 offset:26624\n"
"v_mfma_f32_16x16x32_bf16 v[224:227], a[80:83], a[144:147], v[224:227]\n"
"v_mfma_f32_16x16x32_bf16 v[224:227], a[84:87], a[148:151], v[224:227]\n"
"global_load_dwordx4 a[76:79], v0, s[34:35] offset:-1024 sc1\n"
"v_mfma_f32_16x16x32_bf16 v[252:255], a[92:95], a[148:151], v[252:255]\n"
"ds_read_b128 a[148:151], v2 offset:29696\n"
"v_mfma_f32_16x16x32_bf16 v[252:255], a[88:91], a[144:147], v[252:255]\n"
"ds_read_b128 a[144:147], v2 offset:28672\n"
"v_mfma_f32_16x16x32_bf16 a[28:31], a[88:91], a[152:155], a[28:31]\n"
"v_mfma_f32_16x16x32_bf16 a[28:31], a[92:95], a[156:159], a[28:31]\n"
"v_mfma_f32_16x16x32_bf16 a[24:27], a[84:87], a[156:159], a[24:27]\n"
"s_bitcmp0_b32 %26, 0\n"
"ds_read_b128 a[156:159], v2 offset:31744\n"
"v_mfma_f32_16x16x32_bf16 a[24:27], a[80:83], a[152:155], a[24:27]\n"
"ds_read_b128 a[152:155], v2 offset:30720\n"
"s_cbranch_scc1 L_P3F\n"
"s_waitcnt vmcnt(10) lgkmcnt(13)\n"
"v_mfma_f32_16x16x32_bf16 v[32:35], a[32:35], a[96:99], v[32:35]\n"
"s_waitcnt lgkmcnt(6)\n"
"global_store_dwordx4 v0, v[24:27], s[52:53] offset:2048 sc1\n"
"v_mfma_f32_16x16x32_bf16 v[32:35], a[36:39], a[100:103], v[32:35]\n"
"global_load_dwordx4 a[80:83], v0, s[34:35] offset:0 sc1\n"
"v_mfma_f32_16x16x32_bf16 v[60:63], a[44:47], a[100:103], v[60:63]\n"
"s_barrier\n"
"v_mfma_f32_16x16x32_bf16 v[60:63], a[40:43], a[96:99], v[60:63]\n"
"v_mfma_f32_16x16x32_bf16 v[64:67], a[40:43], a[104:107], v[64:67]\n"
"v_mfma_f32_16x16x32_bf16 v[64:67], a[44:47], a[108:111], v[64:67]\n"
"v_mfma_f32_16x16x32_bf16 v[36:39], a[36:39], a[108:111], v[36:39]\n"
"v_mfma_f32_16x16x32_bf16 v[36:39], a[32:35], a[104:107], v[36:39]\n"
"v_mfma_f32_16x16x32_bf16 v[40:43], a[32:35], a[112:115], v[40:43]\n"
"v_mfma_f32_16x16x32_bf16 v[40:43], a[36:39], a[116:119], v[40:43]\n"
"global_load_dwordx4 a[84:87], v0, s[34:35] offset:1024 sc1\n"
"v_mfma_f32_16x16x32_bf16 v[68:71], a[44:47], a[116:119], v[68:71]\n"
"v_mfma_f32_16x16x32_bf16 v[68:71], a[40:43], a[112:115], v[68:71]\n"
"v_mfma_f32_16x16x32_bf16 v[72:75], a[40:43], a[120:123], v[72:75]\n"
"v_mfma_f32_16x16x32_bf16 v[72:75], a[44:47], a[124:127], v[72:75]\n"
"v_mfma_f32_16x16x32_bf16 v[44:47], a[36:39], a[124:127], v[44:47]\n"
"v_mfma_f32_16x16x32_bf16 v[44:47], a[32:35], a[120:123], v[44:47]\n"
"s_waitcnt lgkmcnt(4)\n"
"v_mfma_f32_16x16x32_bf16 v[48:51], a[32:35], a[128:131], v[48:51]\n"
"v_mfma_f32_16x16x32_bf16 v[48:51], a[36:39], a[132:135], v[48:51]\n"
"global_load_dwordx4 a[88:91], v0, s[34:35] offset:2048 sc1\n"
"v_lshrrev_b32 v16, 4, v0\n"
"v_mfma_f32_16x16x32_bf16 v[76:79], a[44:47], a[132:135], v[76:79]\n"
"v_and_b32 v16, 0x30, v16\n"
"v_mfma_f32_16x16x32_bf16 v[76:79], a[40:43], a[128:131], v[76:79]\n"
"ds_read_b128 v[28:31], v2 offset:23552\n"
"buffer_load_dwordx2 v[18:19], v16, s[44:47], s61 offen offset:0\n"
"v_mfma_f32_16x16x32_bf16 v[80:83], a[40:43], a[136:139], v[80:83]\n"
"buffer_load_dwordx2 v[8:9], off, s[56:59], %14 sc1\n"
"v_mfma_f32_16x16x32_bf16 v[80:83], a[44:47], a[140:143], v[80:83]\n"
"v_mfma_f32_16x16x32_bf16 v[52:55], a[36:39], a[140:143], v[52:55]\n"
"v_mfma_f32_16x16x32_bf16 v[52:55], a[32:35], a[136:139], v[52:55]\n"
"s_waitcnt lgkmcnt(1)\n"
"v_mfma_f32_16x16x32_bf16 v[56:59], a[32:35], a[144:147], v[56:59]\n"
"v_mfma_f32_16x16x32_bf16 v[56:59], a[36:39], a[148:151], v[56:59]\n"
"global_load_dwordx4 a[92:95], v0, s[34:35] offset:3072 sc1\n"
"v_lshrrev_b32 v20, 4, v0\n"
"v_mfma_f32_16x16x32_bf16 v[84:87], a[44:47], a[148:151], v[84:87]\n"
"v_and_b32 v20, 0x30, v20\n"
"v_mfma_f32_16x16x32_bf16 v[84:87], a[40:43], a[144:147], v[84:87]\n"
"buffer_load_dwordx2 v[22:23], v20, s[44:47], s61 offen offset:8\n"
"v_mfma_f32_16x16x32_bf16 a[4:7], a[40:43], a[152:155], a[4:7]\n"
"s_add_u32 s34, s34, 8192\n"
"v_mfma_f32_16x16x32_bf16 a[4:7], a[44:47], a[156:159], a[4:7]\n"
"v_mfma_f32_16x16x32_bf16 a[0:3], a[36:39], a[156:159], a[0:3]\n"
"s_waitcnt lgkmcnt(0)\n"
"global_store_dwordx4 v0, v[28:31], s[52:53] offset:3072 sc1\n"
"v_mfma_f32_16x16x32_bf16 a[0:3], a[32:35], a[152:155], a[0:3]\n"
"s_waitcnt vmcnt(14)\n"
"v_mfma_f32_16x16x32_bf16 v[88:91], a[48:51], a[96:99], v[88:91]\n"
"v_mfma_f32_16x16x32_bf16 v[88:91], a[52:55], a[100:103], v[88:91]\n"
"global_load_dwordx4 a[32:35], v0, s[34:35] offset:-4096 sc1\n"
"v_mfma_f32_16x16x32_bf16 v[116:119], a[60:63], a[100:103], v[116:119]\n"
"v_mfma_f32_16x16x32_bf16 v[116:119], a[56:59], a[96:99], v[116:119]\n"
"v_mfma_f32_16x16x32_bf16 v[120:123], a[56:59], a[104:107], v[120:123]\n"
"v_mfma_f32_16x16x32_bf16 v[120:123], a[60:63], a[108:111], v[120:123]\n"
"v_mfma_f32_16x16x32_bf16 v[92:95], a[52:55], a[108:111], v[92:95]\n"
"v_mfma_f32_16x16x32_bf16 v[92:95], a[48:51], a[104:107], v[92:95]\n"
"v_mfma_f32_16x16x32_bf16 v[96:99], a[48:51], a[112:115], v[96:99]\n"
"v_mfma_f32_16x16x32_bf16 v[96:99], a[52:55], a[116:119], v[96:99]\n"
"global_load_dwordx4 a[36:39], v0, s[34:35] offset:-3072 sc1\n"
"v_mfma_f32_16x16x32_bf16 v[124:127], a[60:63], a[116:119], v[124:127]\n"
"v_mfma_f32_16x16x32_bf16 v[124:127], a[56:59], a[112:115], v[124:127]\n"
"v_mfma_f32_16x16x32_bf16 v[128:131], a[56:59], a[120:123], v[128:131]\n"
"v_mfma_f32_16x16x32_bf16 v[128:131], a[60:63], a[124:127], v[128:131]\n"
"v_mfma_f32_16x16x32_bf16 v[100:103], a[52:55], a[124:127], v[100:103]\n"
"v_mfma_f32_16x16x32_bf16 v[100:103], a[48:51], a[120:123], v[100:103]\n"
"v_mfma_f32_16x16x32_bf16 v[104:107], a[48:51], a[128:131], v[104:107]\n"
"ds_read_b128 v[24:27], v2 offset:24576\n"
"v_mfma_f32_16x16x32_bf16 v[104:107], a[52:55], a[132:135], v[104:107]\n"
"global_load_dwordx4 a[40:43], v0, s[34:35] offset:-2048 sc1\n"
"v_mfma_f32_16x16x32_bf16 v[132:135], a[60:63], a[132:135], v[132:135]\n"
"v_mfma_f32_16x16x32_bf16 v[132:135], a[56:59], a[128:131], v[132:135]\n"
"v_mfma_f32_16x16x32_bf16 v[136:139], a[56:59], a[136:139], v[136:139]\n"
"v_mfma_f32_16x16x32_bf16 v[136:139], a[60:63], a[140:143], v[136:139]\n"
"v_mfma_f32_16x16x32_bf16 v[108:111], a[52:55], a[140:143], v[108:111]\n"
"v_mfma_f32_16x16x32_bf16 v[108:111], a[48:51], a[136:139], v[108:111]\n"
"v_mfma_f32_16x16x32_bf16 v[112:115], a[48:51], a[144:147], v[112:115]\n"
"v_mfma_f32_16x16x32_bf16 v[112:115], a[52:55], a[148:151], v[112:115]\n"
"global_load_dwordx4 a[44:47], v0, s[34:35] offset:-1024 sc1\n"
"v_mfma_f32_16x16x32_bf16 v[140:143], a[60:63], a[148:151], v[140:143]\n"
"v_mfma_f32_16x16x32_bf16 v[140:143], a[56:59], a[144:147], v[140:143]\n"
"v_mfma_f32_16x16x32_bf16 a[12:15], a[56:59], a[152:155], a[12:15]\n"
"s_waitcnt lgkmcnt(0)\n"
"global_store_dwordx4 v0, v[24:27], s[54:55] offset:-4096 sc1\n"
"v_mfma_f32_16x16x32_bf16 a[12:15], a[60:63], a[156:159], a[12:15]\n"
"v_mfma_f32_16x16x32_bf16 a[8:11], a[52:55], a[156:159], a[8:11]\n"
"v_mfma_f32_16x16x32_bf16 a[8:11], a[48:51], a[152:155], a[8:11]\n"
"s_waitcnt vmcnt(14)\n"
"v_mfma_f32_16x16x32_bf16 v[144:147], a[64:67], a[96:99], v[144:147]\n"
"v_mfma_f32_16x16x32_bf16 v[144:147], a[68:71], a[100:103], v[144:147]\n"
"global_load_dwordx4 a[48:51], v0, s[34:35] offset:0 sc1\n"
"v_mfma_f32_16x16x32_bf16 v[172:175], a[76:79], a[100:103], v[172:175]\n"
"v_mfma_f32_16x16x32_bf16 v[172:175], a[72:75], a[96:99], v[172:175]\n"
"v_mfma_f32_16x16x32_bf16 v[176:179], a[72:75], a[104:107], v[176:179]\n"
"v_mfma_f32_16x16x32_bf16 v[176:179], a[76:79], a[108:111], v[176:179]\n"
"v_mfma_f32_16x16x32_bf16 v[148:151], a[68:71], a[108:111], v[148:151]\n"
"v_mfma_f32_16x16x32_bf16 v[148:151], a[64:67], a[104:107], v[148:151]\n"
"v_mfma_f32_16x16x32_bf16 v[152:155], a[64:67], a[112:115], v[152:155]\n"
"v_mfma_f32_16x16x32_bf16 v[152:155], a[68:71], a[116:119], v[152:155]\n"
"global_load_dwordx4 a[52:55], v0, s[34:35] offset:1024 sc1\n"
"v_mfma_f32_16x16x32_bf16 v[180:183], a[76:79], a[116:119], v[180:183]\n"
"v_mfma_f32_16x16x32_bf16 v[180:183], a[72:75], a[112:115], v[180:183]\n"
"v_mfma_f32_16x16x32_bf16 v[184:187], a[72:75], a[120:123], v[184:187]\n"
"v_mfma_f32_16x16x32_bf16 v[184:187], a[76:79], a[124:127], v[184:187]\n"
"v_mfma_f32_16x16x32_bf16 v[156:159], a[68:71], a[124:127], v[156:159]\n"
"v_mfma_f32_16x16x32_bf16 v[156:159], a[64:67], a[120:123], v[156:159]\n"
"ds_read_b128 v[28:31], v2 offset:26624\n"
"v_mfma_f32_16x16x32_bf16 v[160:163], a[64:67], a[128:131], v[160:163]\n"
"v_mfma_f32_16x16x32_bf16 v[160:163], a[68:71], a[132:135], v[160:163]\n"
"global_load_dwordx4 a[56:59], v0, s[34:35] offset:2048 sc1\n"
"v_mfma_f32_16x16x32_bf16 v[188:191], a[76:79], a[132:135], v[188:191]\n"
"v_mfma_f32_16x16x32_bf16 v[188:191], a[72:75], a[128:131], v[188:191]\n"
"v_mfma_f32_16x16x32_bf16 v[192:195], a[72:75], a[136:139], v[192:195]\n"
"L_st6244:\n"
"s_cbranch_execz L_st8533\n"
"v_mfma_f32_16x16x32_bf16 v[192:195], a[76:79], a[140:143], v[192:195]\n"
"v_mfma_f32_16x16x32_bf16 v[164:167], a[68:71], a[140:143], v[164:167]\n"
"v_mfma_f32_16x16x32_bf16 v[164:167], a[64:67], a[136:139], v[164:167]\n"
"v_mfma_f32_16x16x32_bf16 v[168:171], a[64:67], a[144:147], v[168:171]\n"
"v_mfma_f32_16x16x32_bf16 v[168:171], a[68:71], a[148:151], v[168:171]\n"
"global_load_dwordx4 a[60:63], v0, s[34:35] offset:3072 sc1\n"
"v_mfma_f32_16x16x32_bf16 v[196:199], a[76:79], a[148:151], v[196:199]\n"
"s_waitcnt lgkmcnt(0)\n"
"global_store_dwordx4 v0, v[28:31], s[54:55] offset:-2048 sc1\n"
"v_mfma_f32_16x16x32_bf16 v[196:199], a[72:75], a[144:147], v[196:199]\n"
"v_mfma_f32_16x16x32_bf16 a[20:23], a[72:75], a[152:155], a[20:23]\n"
"s_add_u32 s34, s34, 8192\n"
"v_mfma_f32_16x16x32_bf16 a[20:23], a[76:79], a[156:159], a[20:23]\n"
"v_mfma_f32_16x16x32_bf16 a[16:19], a[68:71], a[156:159], a[16:19]\n"
"v_mfma_f32_16x16x32_bf16 a[16:19], a[64:67], a[152:155], a[16:19]\n"
"s_waitcnt vmcnt(12)\n"
"v_mfma_f32_16x16x32_bf16 v[200:203], a[80:83], a[96:99], v[200:203]\n"
"v_mfma_f32_16x16x32_bf16 v[200:203], a[84:87], a[100:103], v[200:203]\n"
"global_load_dwordx4 a[64:67], v0, s[34:35] offset:-4096 sc1\n"
"v_mfma_f32_16x16x32_bf16 v[228:231], a[92:95], a[100:103], v[228:231]\n"
"ds_read_b128 a[100:103], v3 offset:17408\n"
"v_mfma_f32_16x16x32_bf16 v[228:231], a[88:91], a[96:99], v[228:231]\n"
"ds_read_b128 a[96:99], v3 offset:16384\n"
"v_mfma_f32_16x16x32_bf16 v[232:235], a[88:91], a[104:107], v[232:235]\n"
"v_mfma_f32_16x16x32_bf16 v[232:235], a[92:95], a[108:111], v[232:235]\n"
"v_mfma_f32_16x16x32_bf16 v[204:207], a[84:87], a[108:111], v[204:207]\n"
"ds_read_b128 a[108:111], v3 offset:19456\n"
"v_mfma_f32_16x16x32_bf16 v[204:207], a[80:83], a[104:107], v[204:207]\n"
"ds_read_b128 a[104:107], v3 offset:18432\n"
"v_mfma_f32_16x16x32_bf16 v[208:211], a[80:83], a[112:115], v[208:211]\n"
"v_mfma_f32_16x16x32_bf16 v[208:211], a[84:87], a[116:119], v[208:211]\n"
"global_load_dwordx4 a[68:71], v0, s[34:35] offset:-3072 sc1\n"
"v_mfma_f32_16x16x32_bf16 v[236:239], a[92:95], a[116:119], v[236:239]\n"
"ds_read_b128 a[116:119], v3 offset:21504\n"
"v_mfma_f32_16x16x32_bf16 v[236:239], a[88:91], a[112:115], v[236:239]\n"
"ds_read_b128 a[112:115], v3 offset:20480\n"
"v_mfma_f32_16x16x32_bf16 v[240:243], a[88:91], a[120:123], v[240:243]\n"
"v_mfma_f32_16x16x32_bf16 v[240:243], a[92:95], a[124:127], v[240:243]\n"
"ds_read_b128 v[24:27], v2 offset:29696\n"
"v_mfma_f32_16x16x32_bf16 v[212:215], a[84:87], a[124:127], v[212:215]\n"
"ds_read_b128 a[124:127], v3 offset:23552\n"
"v_mfma_f32_16x16x32_bf16 v[212:215], a[80:83], a[120:123], v[212:215]\n"
"ds_read_b128 a[120:123], v3 offset:22528\n"
"v_mfma_f32_16x16x32_bf16 v[216:219], a[80:83], a[128:131], v[216:219]\n"
"v_mfma_f32_16x16x32_bf16 v[216:219], a[84:87], a[132:135], v[216:219]\n"
"global_load_dwordx4 a[72:75], v0, s[34:35] offset:-2048 sc1\n"
"v_mfma_f32_16x16x32_bf16 v[244:247], a[92:95], a[132:135], v[244:247]\n"
"ds_read_b128 a[132:135], v3 offset:25600\n"
"v_mfma_f32_16x16x32_bf16 v[244:247], a[88:91], a[128:131], v[244:247]\n"
"ds_read_b128 a[128:131], v3 offset:24576\n"
"v_mfma_f32_16x16x32_bf16 v[248:251], a[88:91], a[136:139], v[248:251]\n"
"v_subrev_u32 v8, s65, v8\n"
"v_mfma_f32_16x16x32_bf16 v[248:251], a[92:95], a[140:143], v[248:251]\n"
"v_xor_b32 v9, s79, v9\n"
"v_mfma_f32_16x16x32_bf16 v[220:223], a[84:87], a[140:143], v[220:223]\n"
"ds_read_b128 a[140:143], v3 offset:27648\n"
"v_or_b32 v8, v8, v9\n"
"v_mfma_f32_16x16x32_bf16 v[220:223], a[80:83], a[136:139], v[220:223]\n"
"ds_read_b128 a[136:139], v3 offset:26624\n"
"v_cmp_lt_u32 vcc, 15, v8\n"
"v_mfma_f32_16x16x32_bf16 v[224:227], a[80:83], a[144:147], v[224:227]\n"
"s_waitcnt lgkmcnt(6)\n"
"global_store_dwordx4 v0, v[24:27], s[54:55] offset:1024 sc1\n"
"s_cbranch_vccnz L_slow0\n"
"L_back0:\n"
"v_mfma_f32_16x16x32_bf16 v[224:227], a[84:87], a[148:151], v[224:227]\n"
"global_load_dwordx4 a[76:79], v0, s[34:35] offset:-1024 sc1\n"
"v_readfirstlane_b32 s66, v9\n"
"v_mfma_f32_16x16x32_bf16 v[252:255], a[92:95], a[148:151], v[252:255]\n"
"ds_read_b128 a[148:151], v3 offset:29696\n"
"v_mfma_f32_16x16x32_bf16 v[252:255], a[88:91], a[144:147], v[252:255]\n"
"s_and_b32 s66, s66, 3\n"
"ds_read_b128 a[144:147], v3 offset:28672\n"
"v_mfma_f32_16x16x32_bf16 a[28:31], a[88:91], a[152:155], a[28:31]\n"
"s_lshl_b32 s69, s66, 13\n"
"v_mfma_f32_16x16x32_bf16 a[28:31], a[92:95], a[156:159], a[28:31]\n"
"s_cmp_eq_u32 s66, 3\n"
"s_cselect_b32 s75, 8192, 0\n"
"v_mfma_f32_16x16x32_bf16 a[24:27], a[84:87], a[156:159], a[24:27]\n"
"s_and_b32 s29, s66, s30\n"
"ds_read_b128 a[156:159], v3 offset:31744\n"
"v_mfma_f32_16x16x32_bf16 a[24:27], a[80:83], a[152:155], a[24:27]\n"
"s_waitcnt vmcnt(11)\n"
"ds_read_b128 a[152:155], v3 offset:30720\n"
"v_mfma_f32_16x16x32_bf16 v[32:35], a[32:35], a[96:99], v[32:35]\n"
"s_xor_b32 s31, s79, s66\n"
"v_mfma_f32_16x16x32_bf16 v[32:35], a[36:39], a[100:103], v[32:35]\n"
"global_load_dwordx4 a[80:83], v0, s[34:35] offset:0 sc1\n"
"v_mfma_f32_16x16x32_bf16 v[60:63], a[44:47], a[100:103], v[60:63]\n"
"v_mfma_f32_16x16x32_bf16 v[60:63], a[40:43], a[96:99], v[60:63]\n"
"v_mfma_f32_16x16x32_bf16 v[64:67], a[40:43], a[104:107], v[64:67]\n"
"v_mfma_f32_16x16x32_bf16 v[64:67], a[44:47], a[108:111], v[64:67]\n"
"v_mfma_f32_16x16x32_bf16 v[36:39], a[36:39], a[108:111], v[36:39]\n"
"s_add_u32 m0, s64, 0\n"
"v_mfma_f32_16x16x32_bf16 v[36:39], a[32:35], a[104:107], v[36:39]\n"
"s_waitcnt lgkmcnt(8)\n"
"v_mfma_f32_16x16x32_bf16 v[40:43], a[32:35], a[112:115], v[40:43]\n"
"v_mfma_f32_16x16x32_bf16 v[40:43], a[36:39], a[116:119], v[40:43]\n"
"global_load_dwordx4 a[84:87], v0, s[34:35] offset:1024 sc1\n"
"v_mfma_f32_16x16x32_bf16 v[68:71], a[44:47], a[116:119], v[68:71]\n"
"v_mfma_f32_16x16x32_bf16 v[68:71], a[40:43], a[112:115], v[68:71]\n"
"v_mfma_f32_16x16x32_bf16 v[72:75], a[40:43], a[120:123], v[72:75]\n"
"v_mfma_f32_16x16x32_bf16 v[72:75], a[44:47], a[124:127], v[72:75]\n"
"v_mfma_f32_16x16x32_bf16 v[44:47], a[36:39], a[124:127], v[44:47]\n"
"v_mfma_f32_16x16x32_bf16 v[44:47], a[32:35], a[120:123], v[44:47]\n"
"s_waitcnt lgkmcnt(4)\n"
"v_mfma_f32_16x16x32_bf16 v[48:51], a[32:35], a[128:131], v[48:51]\n"
"v_mfma_f32_16x16x32_bf16 v[48:51], a[36:39], a[132:135], v[48:51]\n"
"global_load_dwordx4 a[88:91], v0, s[34:35] offset:2048 sc1\n"
"v_mfma_f32_16x16x32_bf16 v[76:79], a[44:47], a[132:135], v[76:79]\n"
"v_mfma_f32_16x16x32_bf16 v[76:79], a[40:43], a[128:131], v[76:79]\n"
"v_mfma_f32_16x16x32_bf16 v[80:83], a[40:43], a[136:139], v[80:83]\n"
"v_mfma_f32_16x16x32_bf16 v[80:83], a[44:47], a[140:143], v[80:83]\n"
"buffer_load_dwordx4 v0, s[48:51], s75 offen offset:0 sc1 lds\n"
"v_mfma_f32_16x16x32_bf16 v[52:55], a[36:39], a[140:143], v[52:55]\n"
"v_mfma_f32_16x16x32_bf16 v[52:55], a[32:35], a[136:139], v[52:55]\n"
"s_waitcnt lgkmcnt(0)\n"
"v_mfma_f32_16x16x32_bf16 v[56:59], a[32:35], a[144:147], v[56:59]\n"
"v_mfma_f32_16x16x32_bf16 v[56:59], a[36:39], a[148:151], v[56:59]\n"
"global_load_dwordx4 a[92:95], v0, s[34:35] offset:3072 sc1\n"
"v_mfma_f32_16x16x32_bf16 v[84:87], a[44:47], a[148:151], v[84:87]\n"
"v_mfma_f32_16x16x32_bf16 v[84:87], a[40:43], a[144:147], v[84:87]\n"
"v_mfma_f32_16x16x32_bf16 a[4:7], a[40:43], a[152:155], a[4:7]\n"
"s_add_u32 s34, s34, 8192\n"
"v_mfma_f32_16x16x32_bf16 a[4:7], a[44:47], a[156:159], a[4:7]\n"
"v_mfma_f32_16x16x32_bf16 a[0:3], a[36:39], a[156:159], a[0:3]\n"
"v_mfma_f32_16x16x32_bf16 a[0:3], a[32:35], a[152:155], a[0:3]\n"
"s_waitcnt vmcnt(11)\n"
"v_mfma_f32_16x16x32_bf16 v[88:91], a[48:51], a[96:99], v[88:91]\n"
"v_mfma_f32_16x16x32_bf16 v[88:91], a[52:55], a[100:103], v[88:91]\n"
"global_load_dwordx4 a[32:35], v0, s[34:35] offset:-4096 sc1\n"
"v_mfma_f32_16x16x32_bf16 v[116:119], a[60:63], a[100:103], v[116:119]\n"
"v_mfma_f32_16x16x32_bf16 v[116:119], a[56:59], a[96:99], v[116:119]\n"
"buffer_load_dwordx4 v0, s[48:51], s75 offen offset:1024 sc1 lds\n"
"v_mfma_f32_16x16x32_bf16 v[120:123], a[56:59], a[104:107], v[120:123]\n"
"v_mfma_f32_16x16x32_bf16 v[120:123], a[60:63], a[108:111], v[120:123]\n"
"v_mfma_f32_16x16x32_bf16 v[92:95], a[52:55], a[108:111], v[92:95]\n"
"v_mfma_f32_16x16x32_bf16 v[92:95], a[48:51], a[104:107], v[92:95]\n"
"v_mfma_f32_16x16x32_bf16 v[96:99], a[48:51], a[112:115], v[96:99]\n"
"v_mfma_f32_16x16x32_bf16 v[96:99], a[52:55], a[116:119], v[96:99]\n"
"global_load_dwordx4 a[36:39], v0, s[34:35] offset:-3072 sc1\n"
"v_mfma_f32_16x16x32_bf16 v[124:127], a[60:63], a[116:119], v[124:127]\n"
"v_mfma_f32_16x16x32_bf16 v[124:127], a[56:59], a[112:115], v[124:127]\n"
"v_mfma_f32_16x16x32_bf16 v[128:131], a[56:59], a[120:123], v[128:131]\n"
"v_mfma_f32_16x16x32_bf16 v[128:131], a[60:63], a[124:127], v[128:131]\n"
"v_mfma_f32_16x16x32_bf16 v[100:103], a[52:55], a[124:127], v[100:103]\n"
"v_mfma_f32_16x16x32_bf16 v[100:103], a[48:51], a[120:123], v[100:103]\n"
"v_mfma_f32_16x16x32_bf16 v[104:107], a[48:51], a[128:131], v[104:107]\n"
"v_mfma_f32_16x16x32_bf16 v[104:107], a[52:55], a[132:135], v[104:107]\n"
"global_load_dwordx4 a[40:43], v0, s[34:35] offset:-2048 sc1\n"
"v_mfma_f32_16x16x32_bf16 v[132:135], a[60:63], a[132:135], v[132:135]\n"
"buffer_load_dwordx4 v0, s[48:51], s75 offen offset:2048 sc1 lds\n"
"v_mfma_f32_16x16x32_bf16 v[132:135], a[56:59], a[128:131], v[132:135]\n"
"v_mfma_f32_16x16x32_bf16 v[136:139], a[56:59], a[136:139], v[136:139]\n"
"v_mfma_f32_16x16x32_bf16 v[136:139], a[60:63], a[140:143], v[136:139]\n"
"v_mfma_f32_16x16x32_bf16 v[108:111], a[52:55], a[140:143], v[108:111]\n"
"v_mfma_f32_16x16x32_bf16 v[108:111], a[48:51], a[136:139], v[108:111]\n"
"v_mfma_f32_16x16x32_bf16 v[112:115], a[48:51], a[144:147], v[112:115]\n"
"v_mfma_f32_16x16x32_bf16 v[112:115], a[52:55], a[148:151], v[112:115]\n"
"global_load_dwordx4 a[44:47], v0, s[34:35] offset:-1024 sc1\n"
"v_mfma_f32_16x16x32_bf16 v[140:143], a[60:63], a[148:151], v[140:143]\n"
"v_mfma_f32_16x16x32_bf16 v[140:143], a[56:59], a[144:147], v[140:143]\n"
"v_mfma_f32_16x16x32_bf16 a[12:15], a[56:59], a[152:155], a[12:15]\n"
"v_mfma_f32_16x16x32_bf16 a[12:15], a[60:63], a[156:159], a[12:15]\n"
"v_mfma_f32_16x16x32_bf16 a[8:11], a[52:55], a[156:159], a[8:11]\n"
"v_mfma_f32_16x16x32_bf16 a[8:11], a[48:51], a[152:155], a[8:11]\n"
"s_waitcnt vmcnt(11)\n"
"v_mfma_f32_16x16x32_bf16 v[144:147], a[64:67], a[96:99], v[144:147]\n"
"buffer_load_dwordx4 v0, s[48:51], s75 offen offset:3072 sc1 lds\n"
"v_mfma_f32_16x16x32_bf16 v[144:147], a[68:71], a[100:103], v[144:147]\n"
"global_load_dwordx4 a[48:51], v0, s[34:35] offset:0 sc1\n"
"v_mfma_f32_16x16x32_bf16 v[172:175], a[76:79], a[100:103], v[172:175]\n"
"v_mfma_f32_16x16x32_bf16 v[172:175], a[72:75], a[96:99], v[172:175]\n"
"v_mfma_f32_16x16x32_bf16 v[176:179], a[72:75], a[104:107], v[176:179]\n"
"v_mfma_f32_16x16x32_bf16 v[176:179], a[76:79], a[108:111], v[176:179]\n"
"v_mfma_f32_16x16x32_bf16 v[148:151], a[68:71], a[108:111], v[148:151]\n"
"v_mfma_f32_16x16x32_bf16 v[148:151], a[64:67], a[104:107], v[148:151]\n"
"v_mfma_f32_16x16x32_bf16 v[152:155], a[64:67], a[112:115], v[152:155]\n"
"v_mfma_f32_16x16x32_bf16 v[152:155], a[68:71], a[116:119], v[152:155]\n"
"global_load_dwordx4 a[52:55], v0, s[34:35] offset:1024 sc1\n"
"v_mfma_f32_16x16x32_bf16 v[180:183], a[76:79], a[116:119], v[180:183]\n"
"v_mfma_f32_16x16x32_bf16 v[180:183], a[72:75], a[112:115], v[180:183]\n"
"v_mfma_f32_16x16x32_bf16 v[184:187], a[72:75], a[120:123], v[184:187]\n"
"v_mfma_f32_16x16x32_bf16 v[184:187], a[76:79], a[124:127], v[184:187]\n"
"v_mfma_f32_16x16x32_bf16 v[156:159], a[68:71], a[124:127], v[156:159]\n"
"v_mfma_f32_16x16x32_bf16 v[156:159], a[64:67], a[120:123], v[156:159]\n"
"s_add_u32 m0, s64, 4096\n"
"v_mfma_f32_16x16x32_bf16 v[160:163], a[64:67], a[128:131], v[160:163]\n"
"v_mfma_f32_16x16x32_bf16 v[160:163], a[68:71], a[132:135], v[160:163]\n"
"global_load_dwordx4 a[56:59], v0, s[34:35] offset:2048 sc1\n"
"v_mfma_f32_16x16x32_bf16 v[188:191], a[76:79], a[132:135], v[188:191]\n"
"v_mfma_f32_16x16x32_bf16 v[188:191], a[72:75], a[128:131], v[188:191]\n"
"v_mfma_f32_16x16x32_bf16 v[192:195], a[72:75], a[136:139], v[192:195]\n"
"v_mfma_f32_16x16x32_bf16 v[192:195], a[76:79], a[140:143], v[192:195]\n"
"v_mfma_f32_16x16x32_bf16 v[164:167], a[68:71], a[140:143], v[164:167]\n"
"v_mfma_f32_16x16x32_bf16 v[164:167], a[64:67], a[136:139], v[164:167]\n"
"v_mfma_f32_16x16x32_bf16 v[168:171], a[64:67], a[144:147], v[168:171]\n"
"v_mfma_f32_16x16x32_bf16 v[168:171], a[68:71], a[148:151], v[168:171]\n"
"global_load_dwordx4 a[60:63], v0, s[34:35] offset:3072 sc1\n"
"v_mfma_f32_16x16x32_bf16 v[196:199], a[76:79], a[148:151], v[196:199]\n"
"v_mfma_f32_16x16x32_bf16 v[196:199], a[72:75], a[144:147], v[196:199]\n"
"v_mfma_f32_16x16x32_bf16 a[20:23], a[72:75], a[152:155], a[20:23]\n"
"s_add_u32 s34, s34, 8192\n"
"v_mfma_f32_16x16x32_bf16 a[20:23], a[76:79], a[156:159], a[20:23]\n"
"v_mfma_f32_16x16x32_bf16 a[16:19], a[68:71], a[156:159], a[16:19]\n"
"buffer_load_dwordx4 v1, s[48:51], s75 offen offset:0 sc1 lds\n"
"v_mfma_f32_16x16x32_bf16 a[16:19], a[64:67], a[152:155], a[16:19]\n"
"s_waitcnt vmcnt(12)\n"
"v_mfma_f32_16x16x32_bf16 v[200:203], a[80:83], a[96:99], v[200:203]\n"
"v_mfma_f32_16x16x32_bf16 v[200:203], a[84:87], a[100:103], v[200:203]\n"
"global_load_dwordx4 a[64:67], v0, s[34:35] offset:-4096 sc1\n"
"s_add_u32 s65, s65, 1\n"
"v_mfma_f32_16x16x32_bf16 v[228:231], a[92:95], a[100:103], v[228:231]\n"
"ds_read_b128 a[100:103], v4 offset:17408\n"
"v_mfma_f32_16x16x32_bf16 v[228:231], a[88:91], a[96:99], v[228:231]\n"
"ds_read_b128 a[96:99], v4 offset:16384\n"
"v_mov_b32 v8, s65\n"
"v_mfma_f32_16x16x32_bf16 v[232:235], a[88:91], a[104:107], v[232:235]\n"
"v_mov_b32 v9, s60\n"
"v_mfma_f32_16x16x32_bf16 v[232:235], a[92:95], a[108:111], v[232:235]\n"
"v_mfma_f32_16x16x32_bf16 v[204:207], a[84:87], a[108:111], v[204:207]\n"
"buffer_store_dwordx2 v[8:9], off, s[56:59], %13 sc1\n"
"ds_read_b128 a[108:111], v4 offset:19456\n"
"v_mfma_f32_16x16x32_bf16 v[204:207], a[80:83], a[104:107], v[204:207]\n"
"ds_read_b128 a[104:107], v4 offset:18432\n"
"v_mfma_f32_16x16x32_bf16 v[208:211], a[80:83], a[112:115], v[208:211]\n"
"v_mfma_f32_16x16x32_bf16 v[208:211], a[84:87], a[116:119], v[208:211]\n"
"global_load_dwordx4 a[68:71], v0, s[34:35] offset:-3072 sc1\n"
"v_mfma_f32_16x16x32_bf16 v[236:239], a[92:95], a[116:119], v[236:239]\n"
"ds_read_b128 a[116:119], v4 offset:21504\n"
"v_mfma_f32_16x16x32_bf16 v[236:239], a[88:91], a[112:115], v[236:239]\n"
"ds_read_b128 a[112:115], v4 offset:20480\n"
"v_mfma_f32_16x16x32_bf16 v[240:243], a[88:91], a[120:123], v[240:243]\n"
"v_mfma_f32_16x16x32_bf16 v[240:243], a[92:95], a[124:127], v[240:243]\n"
"buffer_load_dwordx4 v1, s[48:51], s75 offen offset:1024 sc1 lds\n"
"v_mfma_f32_16x16x32_bf16 v[212:215], a[84:87], a[124:127], v[212:215]\n"
"ds_read_b128 a[124:127], v4 offset:23552\n"
"v_mfma_f32_16x16x32_bf16 v[212:215], a[80:83], a[120:123], v[212:215]\n"
"ds_read_b128 a[120:123], v4 offset:22528\n"
"v_mfma_f32_16x16x32_bf16 v[216:219], a[80:83], a[128:131], v[216:219]\n"
"v_mfma_f32_16x16x32_bf16 v[216:219], a[84:87], a[132:135], v[216:219]\n"
"global_load_dwordx4 a[72:75], v0, s[34:35] offset:-2048 sc1\n"
"v_mfma_f32_16x16x32_bf16 v[244:247], a[92:95], a[132:135], v[244:247]\n"
"ds_read_b128 a[132:135], v4 offset:25600\n"
"v_mfma_f32_16x16x32_bf16 v[244:247], a[88:91], a[128:131], v[244:247]\n"
"ds_read_b128 a[128:131], v4 offset:24576\n"
"v_mfma_f32_16x16x32_bf16 v[248:251], a[88:91], a[136:139], v[248:251]\n"
"v_mfma_f32_16x16x32_bf16 v[248:251], a[92:95], a[140:143], v[248:251]\n"
"v_mfma_f32_16x16x32_bf16 v[220:223], a[84:87], a[140:143], v[220:223]\n"
"ds_read_b128 a[140:143], v4 offset:27648\n"
"v_mfma_f32_16x16x32_bf16 v[220:223], a[80:83], a[136:139], v[220:223]\n"
"ds_read_b128 a[136:139], v4 offset:26624\n"
"v_mfma_f32_16x16x32_bf16 v[224:227], a[80:83], a[144:147], v[224:227]\n"
"v_mfma_f32_16x16x32_bf16 v[224:227], a[84:87], a[148:151], v[224:227]\n"
"global_load_dwordx4 a[76:79], v0, s[34:35] offset:-1024 sc1\n"
"v_mfma_f32_16x16x32_bf16 v[252:255], a[92:95], a[148:151], v[252:255]\n"
"ds_read_b128 a[148:151], v4 offset:29696\n"
"v_mfma_f32_16x16x32_bf16 v[252:255], a[88:91], a[144:147], v[252:255]\n"
"ds_read_b128 a[144:147], v4 offset:28672\n"
"v_mfma_f32_16x16x32_bf16 a[28:31], a[88:91], a[152:155], a[28:31]\n"
"buffer_load_dwordx4 v1, s[48:51], s75 offen offset:2048 sc1 lds\n"
"v_mfma_f32_16x16x32_bf16 a[28:31], a[92:95], a[156:159], a[28:31]\n"
"v_mfma_f32_16x16x32_bf16 a[24:27], a[84:87], a[156:159], a[24:27]\n"
"ds_read_b128 a[156:159], v4 offset:31744\n"
"v_mfma_f32_16x16x32_bf16 a[24:27], a[80:83], a[152:155], a[24:27]\n"
"s_waitcnt vmcnt(13) lgkmcnt(11)\n"
"ds_read_b128 a[152:155], v4 offset:30720\n"
"v_mfma_f32_16x16x32_bf16 v[32:35], a[32:35], a[96:99], v[32:35]\n"
"v_mfma_f32_16x16x32_bf16 v[32:35], a[36:39], a[100:103], v[32:35]\n"
"global_load_dwordx4 a[80:83], v0, s[34:35] offset:0 sc1\n"
"v_mfma_f32_16x16x32_bf16 v[60:63], a[44:47], a[100:103], v[60:63]\n"
"v_mfma_f32_16x16x32_bf16 v[60:63], a[40:43], a[96:99], v[60:63]\n"
"v_mfma_f32_16x16x32_bf16 v[64:67], a[40:43], a[104:107], v[64:67]\n"
"v_mfma_f32_16x16x32_bf16 v[64:67], a[44:47], a[108:111], v[64:67]\n"
"v_mfma_f32_16x16x32_bf16 v[36:39], a[36:39], a[108:111], v[36:39]\n"
"v_mfma_f32_16x16x32_bf16 v[36:39], a[32:35], a[104:107], v[36:39]\n"
"s_waitcnt lgkmcnt(8)\n"
"v_mfma_f32_16x16x32_bf16 v[40:43], a[32:35], a[112:115], v[40:43]\n"
"v_mfma_f32_16x16x32_bf16 v[40:43], a[36:39], a[116:119], v[40:43]\n"
"global_load_dwordx4 a[84:87], v0, s[34:35] offset:1024 sc1\n"
"v_mfma_f32_16x16x32_bf16 v[68:71], a[44:47], a[116:119], v[68:71]\n"
"buffer_load_dwordx4 v1, s[48:51], s75 offen offset:3072 sc1 lds\n"
"v_mfma_f32_16x16x32_bf16 v[68:71], a[40:43], a[112:115], v[68:71]\n"
"v_mfma_f32_16x16x32_bf16 v[72:75], a[40:43], a[120:123], v[72:75]\n"
"v_mfma_f32_16x16x32_bf16 v[72:75], a[44:47], a[124:127], v[72:75]\n"
"v_mfma_f32_16x16x32_bf16 v[44:47], a[36:39], a[124:127], v[44:47]\n"
"v_mfma_f32_16x16x32_bf16 v[44:47], a[32:35], a[120:123], v[44:47]\n"
"s_waitcnt lgkmcnt(4)\n"
"v_mfma_f32_16x16x32_bf16 v[48:51], a[32:35], a[128:131], v[48:51]\n"
"v_mfma_f32_16x16x32_bf16 v[48:51], a[36:39], a[132:135], v[48:51]\n"
"global_load_dwordx4 a[88:91], v0, s[34:35] offset:2048 sc1\n"
"v_mfma_f32_16x16x32_bf16 v[76:79], a[44:47], a[132:135], v[76:79]\n"
"v_mfma_f32_16x16x32_bf16 v[76:79], a[40:43], a[128:131], v[76:79]\n"
"v_mfma_f32_16x16x32_bf16 v[80:83], a[40:43], a[136:139], v[80:83]\n"
"v_mfma_f32_16x16x32_bf16 v[80:83], a[44:47], a[140:143], v[80:83]\n"
"v_mfma_f32_16x16x32_bf16 v[52:55], a[36:39], a[140:143], v[52:55]\n"
"v_mfma_f32_16x16x32_bf16 v[52:55], a[32:35], a[136:139], v[52:55]\n"
"s_waitcnt lgkmcnt(0)\n"
"v_mfma_f32_16x16x32_bf16 v[56:59], a[32:35], a[144:147], v[56:59]\n"
"v_mfma_f32_16x16x32_bf16 v[56:59], a[36:39], a[148:151], v[56:59]\n"
"s_add_u32 m0, s64, 8192\n"
"global_load_dwordx4 a[92:95], v0, s[34:35] offset:3072 sc1\n"
"v_mfma_f32_16x16x32_bf16 v[84:87], a[44:47], a[148:151], v[84:87]\n"
"v_mfma_f32_16x16x32_bf16 v[84:87], a[40:43], a[144:147], v[84:87]\n"
"v_mfma_f32_16x16x32_bf16 a[4:7], a[40:43], a[152:155], a[4:7]\n"
"s_add_u32 s34, s34, 8192\n"
"v_mfma_f32_16x16x32_bf16 a[4:7], a[44:47], a[156:159], a[4:7]\n"
"v_mfma_f32_16x16x32_bf16 a[0:3], a[36:39], a[156:159], a[0:3]\n"
"v_mfma_f32_16x16x32_bf16 a[0:3], a[32:35], a[152:155], a[0:3]\n"
"s_waitcnt vmcnt(13)\n"
"v_mfma_f32_16x16x32_bf16 v[88:91], a[48:51], a[96:99], v[88:91]\n"
"v_mfma_f32_16x16x32_bf16 v[88:91], a[52:55], a[100:103], v[88:91]\n"
"global_load_dwordx4 a[32:35], v0, s[34:35] offset:-4096 sc1\n"
"v_mfma_f32_16x16x32_bf16 v[116:119], a[60:63], a[100:103], v[116:119]\n"
"v_mfma_f32_16x16x32_bf16 v[116:119], a[56:59], a[96:99], v[116:119]\n"
"v_mfma_f32_16x16x32_bf16 v[120:123], a[56:59], a[104:107], v[120:123]\n"
"v_mfma_f32_16x16x32_bf16 v[120:123], a[60:63], a[108:111], v[120:123]\n"
"v_mfma_f32_16x16x32_bf16 v[92:95], a[52:55], a[108:111], v[92:95]\n"
"v_mfma_f32_16x16x32_bf16 v[92:95], a[48:51], a[104:107], v[92:95]\n"
"v_mfma_f32_16x16x32_bf16 v[96:99], a[48:51], a[112:115], v[96:99]\n"
"buffer_load_dwordx4 v0, s[48:51], s69 offen offset:0 sc1 lds\n"
"v_mfma_f32_16x16x32_bf16 v[96:99], a[52:55], a[116:119], v[96:99]\n"
"global_load_dwordx4 a[36:39], v0, s[34:35] offset:-3072 sc1\n"
"v_mfma_f32_16x16x32_bf16 v[124:127], a[60:63], a[116:119], v[124:127]\n"
"v_mfma_f32_16x16x32_bf16 v[124:127], a[56:59], a[112:115], v[124:127]\n"
"v_mfma_f32_16x16x32_bf16 v[128:131], a[56:59], a[120:123], v[128:131]\n"
"v_mfma_f32_16x16x32_bf16 v[128:131], a[60:63], a[124:127], v[128:131]\n"
"v_mfma_f32_16x16x32_bf16 v[100:103], a[52:55], a[124:127], v[100:103]\n"
"v_mfma_f32_16x16x32_bf16 v[100:103], a[48:51], a[120:123], v[100:103]\n"
"v_mfma_f32_16x16x32_bf16 v[104:107], a[48:51], a[128:131], v[104:107]\n"
"v_mfma_f32_16x16x32_bf16 v[104:107], a[52:55], a[132:135], v[104:107]\n"
"global_load_dwordx4 a[40:43], v0, s[34:35] offset:-2048 sc1\n"
"v_mfma_f32_16x16x32_bf16 v[132:135], a[60:63], a[132:135], v[132:135]\n"
"v_mfma_f32_16x16x32_bf16 v[132:135], a[56:59], a[128:131], v[132:135]\n"
"v_mfma_f32_16x16x32_bf16 v[136:139], a[56:59], a[136:139], v[136:139]\n"
"v_mfma_f32_16x16x32_bf16 v[136:139], a[60:63], a[140:143], v[136:139]\n"
"v_mfma_f32_16x16x32_bf16 v[108:111], a[52:55], a[140:143], v[108:111]\n"
"buffer_load_dwordx4 v0, s[48:51], s69 offen offset:1024 sc1 lds\n"
"v_mfma_f32_16x16x32_bf16 v[108:111], a[48:51], a[136:139], v[108:111]\n"
"v_mfma_f32_16x16x32_bf16 v[112:115], a[48:51], a[144:147], v[112:115]\n"
"v_mfma_f32_16x16x32_bf16 v[112:115], a[52:55], a[148:151], v[112:115]\n"
"global_load_dwordx4 a[44:47], v0, s[34:35] offset:-1024 sc1\n"
"v_mfma_f32_16x16x32_bf16 v[140:143], a[60:63], a[148:151], v[140:143]\n"
"v_mfma_f32_16x16x32_bf16 v[140:143], a[56:59], a[144:147], v[140:143]\n"
"v_mfma_f32_16x16x32_bf16 a[12:15], a[56:59], a[152:155], a[12:15]\n"
"v_mfma_f32_16x16x32_bf16 a[12:15], a[60:63], a[156:159], a[12:15]\n"
"v_mfma_f32_16x16x32_bf16 a[8:11], a[52:55], a[156:159], a[8:11]\n"
"v_mfma_f32_16x16x32_bf16 a[8:11], a[48:51], a[152:155], a[8:11]\n"
"s_waitcnt vmcnt(12)\n"
"v_mfma_f32_16x16x32_bf16 v[144:147], a[64:67], a[96:99], v[144:147]\n"
"v_mfma_f32_16x16x32_bf16 v[144:147], a[68:71], a[100:103], v[144:147]\n"
"global_load_dwordx4 a[48:51], v0, s[34:35] offset:0 sc1\n"
"v_mfma_f32_16x16x32_bf16 v[172:175], a[76:79], a[100:103], v[172:175]\n"
"v_mfma_f32_16x16x32_bf16 v[172:175], a[72:75], a[96:99], v[172:175]\n"
"v_mfma_f32_16x16x32_bf16 v[176:179], a[72:75], a[104:107], v[176:179]\n"
"v_mfma_f32_16x16x32_bf16 v[176:179], a[76:79], a[108:111], v[176:179]\n"
"buffer_load_dwordx4 v0, s[48:51], s69 offen offset:2048 sc1 lds\n"
"v_mfma_f32_16x16x32_bf16 v[148:151], a[68:71], a[108:111], v[148:151]\n"
"v_mfma_f32_16x16x32_bf16 v[148:151], a[64:67], a[104:107], v[148:151]\n"
"v_mfma_f32_16x16x32_bf16 v[152:155], a[64:67], a[112:115], v[152:155]\n"
"v_lshlrev_b32 v16, 16, v18\n"
"v_mfma_f32_16x16x32_bf16 v[152:155], a[68:71], a[116:119], v[152:155]\n"
"global_load_dwordx4 a[52:55], v0, s[34:35] offset:1024 sc1\n"
"v_and_b32 v17, s63, v18\n"
"v_mfma_f32_16x16x32_bf16 v[180:183], a[76:79], a[116:119], v[180:183]\n"
"v_lshlrev_b32 v18, 16, v19\n"
"v_mfma_f32_16x16x32_bf16 v[180:183], a[72:75], a[112:115], v[180:183]\n"
"v_and_b32 v19, s63, v19\n"
"v_mfma_f32_16x16x32_bf16 v[184:187], a[72:75], a[120:123], v[184:187]\n"
"v_add_f32 v32, v32, v16\n"
"v_mfma_f32_16x16x32_bf16 v[184:187], a[76:79], a[124:127], v[184:187]\n"
"v_add_f32 v33, v33, v17\n"
"v_add_f32 v34, v34, v18\n"
"v_mfma_f32_16x16x32_bf16 v[156:159], a[68:71], a[124:127], v[156:159]\n"
"v_add_f32 v35, v35, v19\n"
"v_mfma_f32_16x16x32_bf16 v[156:159], a[64:67], a[120:123], v[156:159]\n"
"v_add_f32 v36, v36, v16\n"
"v_mfma_f32_16x16x32_bf16 v[160:163], a[64:67], a[128:131], v[160:163]\n"
"v_add_f32 v37, v37, v17\n"
"v_mfma_f32_16x16x32_bf16 v[160:163], a[68:71], a[132:135], v[160:163]\n"
"global_load_dwordx4 a[56:59], v0, s[34:35] offset:2048 sc1\n"
"v_add_f32 v38, v38, v18\n"
"v_mfma_f32_16x16x32_bf16 v[188:191], a[76:79], a[132:135], v[188:191]\n"
"v_add_f32 v39, v39, v19\n"
"v_mfma_f32_16x16x32_bf16 v[188:191], a[72:75], a[128:131], v[188:191]\n"
"v_add_f32 v40, v40, v16\n"
"v_add_f32 v41, v41, v17\n"
"v_mfma_f32_16x16x32_bf16 v[192:195], a[72:75], a[136:139], v[192:195]\n"
"buffer_load_dwordx4 v0, s[48:51], s69 offen offset:3072 sc1 lds\n"
"v_mfma_f32_16x16x32_bf16 v[192:195], a[76:79], a[140:143], v[192:195]\n"
"v_add_f32 v42, v42, v18\n"
"v_mfma_f32_16x16x32_bf16 v[164:167], a[68:71], a[140:143], v[164:167]\n"
"v_add_f32 v43, v43, v19\n"
"v_mfma_f32_16x16x32_bf16 v[164:167], a[64:67], a[136:139], v[164:167]\n"
"v_add_f32 v44, v44, v16\n"
"v_add_f32 v45, v45, v17\n"
"v_mfma_f32_16x16x32_bf16 v[168:171], a[64:67], a[144:147], v[168:171]\n"
"v_add_f32 v46, v46, v18\n"
"v_mfma_f32_16x16x32_bf16 v[168:171], a[68:71], a[148:151], v[168:171]\n"
"global_load_dwordx4 a[60:63], v0, s[34:35] offset:3072 sc1\n"
"v_add_f32 v47, v47, v19\n"
"v_mfma_f32_16x16x32_bf16 v[196:199], a[76:79], a[148:151], v[196:199]\n"
"v_add_f32 v48, v48, v16\n"
"v_mfma_f32_16x16x32_bf16 v[196:199], a[72:75], a[144:147], v[196:199]\n"
"v_add_f32 v49, v49, v17\n"
"v_mfma_f32_16x16x32_bf16 a[20:23], a[72:75], a[152:155], a[20:23]\n"
"s_add_u32 s34, s34, 8192\n"
"v_add_f32 v50, v50, v18\n"
"v_mfma_f32_16x16x32_bf16 a[20:23], a[76:79], a[156:159], a[20:23]\n"
"v_add_f32 v51, v51, v19\n"
"v_mfma_f32_16x16x32_bf16 a[16:19], a[68:71], a[156:159], a[16:19]\n"
"v_add_f32 v52, v52, v16\n"
"v_mfma_f32_16x16x32_bf16 a[16:19], a[64:67], a[152:155], a[16:19]\n"
"s_waitcnt vmcnt(12)\n"
"v_add_f32 v53, v53, v17\n"
"v_mfma_f32_16x16x32_bf16 v[200:203], a[80:83], a[96:99], v[200:203]\n"
"v_add_f32 v54, v54, v18\n"
"v_add_f32 v55, v55, v19\n"
"v_mfma_f32_16x16x32_bf16 v[200:203], a[84:87], a[100:103], v[200:203]\n"
"global_load_dwordx4 a[64:67], v0, s[34:35] offset:-4096 sc1\n"
"v_add_f32 v56, v56, v16\n"
"v_mfma_f32_16x16x32_bf16 v[228:231], a[92:95], a[100:103], v[228:231]\n"
"s_add_u32 m0, s64, 12288\n"
"ds_read_b128 a[100:103], v5 offset:17408\n"
"v_add_f32 v57, v57, v17\n"
"v_mfma_f32_16x16x32_bf16 v[228:231], a[88:91], a[96:99], v[228:231]\n"
"ds_read_b128 a[96:99], v5 offset:16384\n"
"v_add_f32 v58, v58, v18\n"
"v_mfma_f32_16x16x32_bf16 v[232:235], a[88:91], a[104:107], v[232:235]\n"
"v_add_f32 v59, v59, v19\n"
"v_mfma_f32_16x16x32_bf16 v[232:235], a[92:95], a[108:111], v[232:235]\n"
"v_accvgpr_read_b32 v12, a0\n"
"v_mfma_f32_16x16x32_bf16 v[204:207], a[84:87], a[108:111], v[204:207]\n"
"ds_read_b128 a[108:111], v5 offset:19456\n"
"v_accvgpr_read_b32 v13, a1\n"
"v_mfma_f32_16x16x32_bf16 v[204:207], a[80:83], a[104:107], v[204:207]\n"
"ds_read_b128 a[104:107], v5 offset:18432\n"
"v_accvgpr_read_b32 v14, a2\n"
"v_mfma_f32_16x16x32_bf16 v[208:211], a[80:83], a[112:115], v[208:211]\n"
"v_accvgpr_read_b32 v15, a3\n"
"v_mfma_f32_16x16x32_bf16 v[208:211], a[84:87], a[116:119], v[208:211]\n"
"global_load_dwordx4 a[68:71], v0, s[34:35] offset:-3072 sc1\n"
"v_add_f32 v12, v12, v16\n"
"v_mfma_f32_16x16x32_bf16 v[236:239], a[92:95], a[116:119], v[236:239]\n"
"ds_read_b128 a[116:119], v5 offset:21504\n"
"v_add_f32 v13, v13, v17\n"
"v_mfma_f32_16x16x32_bf16 v[236:239], a[88:91], a[112:115], v[236:239]\n"
"ds_read_b128 a[112:115], v5 offset:20480\n"
"v_add_f32 v14, v14, v18\n"
"v_mfma_f32_16x16x32_bf16 v[240:243], a[88:91], a[120:123], v[240:243]\n"
"v_add_f32 v15, v15, v19\n"
"v_accvgpr_write_b32 a0, v12\n"
"v_mfma_f32_16x16x32_bf16 v[240:243], a[92:95], a[124:127], v[240:243]\n"
"v_accvgpr_write_b32 a1, v13\n"
"v_mfma_f32_16x16x32_bf16 v[212:215], a[84:87], a[124:127], v[212:215]\n"
"ds_read_b128 a[124:127], v5 offset:23552\n"
"v_accvgpr_write_b32 a2, v14\n"
"v_mfma_f32_16x16x32_bf16 v[212:215], a[80:83], a[120:123], v[212:215]\n"
"ds_read_b128 a[120:123], v5 offset:22528\n"
"v_accvgpr_write_b32 a3, v15\n"
"v_mfma_f32_16x16x32_bf16 v[216:219], a[80:83], a[128:131], v[216:219]\n"
"buffer_load_dwordx4 v1, s[48:51], s69 offen offset:0 sc1 lds\n"
"v_mfma_f32_16x16x32_bf16 v[216:219], a[84:87], a[132:135], v[216:219]\n"
"global_load_dwordx4 a[72:75], v0, s[34:35] offset:-2048 sc1\n"
"v_lshrrev_b32 v16, 4, v0\n"
"v_mfma_f32_16x16x32_bf16 v[244:247], a[92:95], a[132:135], v[244:247]\n"
"ds_read_b128 a[132:135], v5 offset:25600\n"
"v_and_b32 v16, 0x30, v16\n"
"v_mfma_f32_16x16x32_bf16 v[244:247], a[88:91], a[128:131], v[244:247]\n"
"ds_read_b128 a[128:131], v5 offset:24576\n"
"buffer_load_dwordx2 v[18:19], v16, s[44:47], s61 offen offset:64\n"
"v_mfma_f32_16x16x32_bf16 v[248:251], a[88:91], a[136:139], v[248:251]\n"
"s_getpc_b64 s[96:97]\n"
"L_c3_:\n"
"s_add_u32 s96, s96, L_last-L_c3_\n"
"s_addc_u32 s97, s97, 0\n"
"v_mfma_f32_16x16x32_bf16 v[248:251], a[92:95], a[140:143], v[248:251]\n"
"s_lshr_b32 s66, %26, 3\n"
"v_mfma_f32_16x16x32_bf16 v[220:223], a[84:87], a[140:143], v[220:223]\n"
"s_lshl2_add_u32 s66, s66, %16\n"
"ds_read_b128 a[140:143], v5 offset:27648\n"
"v_mfma_f32_16x16x32_bf16 v[220:223], a[80:83], a[136:139], v[220:223]\n"
"ds_read_b128 a[136:139], v5 offset:26624\n"
"v_mfma_f32_16x16x32_bf16 v[224:227], a[80:83], a[144:147], v[224:227]\n"
"s_mul_i32 s66, s66, 256\n"
"v_mfma_f32_16x16x32_bf16 v[224:227], a[84:87], a[148:151], v[224:227]\n"
"global_load_dwordx4 a[76:79], v0, s[34:35] offset:-1024 sc1\n"
"v_mfma_f32_16x16x32_bf16 v[252:255], a[92:95], a[148:151], v[252:255]\n"
"ds_read_b128 a[148:151], v5 offset:29696\n"
"v_mfma_f32_16x16x32_bf16 v[252:255], a[88:91], a[144:147], v[252:255]\n"
"s_min_u32 s66, s66, L_headF-L_last-256\n"
"ds_read_b128 a[144:147], v5 offset:28672\n"
"v_mfma_f32_16x16x32_bf16 a[28:31], a[88:91], a[152:155], a[28:31]\n"
"v_mfma_f32_16x16x32_bf16 a[28:31], a[92:95], a[156:159], a[28:31]\n"
"v_min_u32 v11, 16, v0\n"
"v_mfma_f32_16x16x32_bf16 a[24:27], a[84:87], a[156:159], a[24:27]\n"
"ds_read_b128 a[156:159], v5 offset:31744\n"
"v_mfma_f32_16x16x32_bf16 a[24:27], a[80:83], a[152:155], a[24:27]\n"
"s_waitcnt vmcnt(12) lgkmcnt(11)\n"
"ds_read_b128 a[152:155], v5 offset:30720\n"
"v_mfma_f32_16x16x32_bf16 v[32:35], a[32:35], a[96:99], v[32:35]\n"
"buffer_load_dwordx4 v1, s[48:51], s69 offen offset:1024 sc1 lds\n"
"v_mfma_f32_16x16x32_bf16 v[32:35], a[36:39], a[100:103], v[32:35]\n"
"global_load_dwordx4 a[80:83], v0, s[34:35] offset:0 sc1\n"
"v_mfma_f32_16x16x32_bf16 v[60:63], a[44:47], a[100:103], v[60:63]\n"
"v_lshl_add_u32 v11, v11, 3, s66\n"
"v_mfma_f32_16x16x32_bf16 v[60:63], a[40:43], a[96:99], v[60:63]\n"
"global_load_dword v10, v11, s[96:97]\n"
"v_mfma_f32_16x16x32_bf16 v[64:67], a[40:43], a[104:107], v[64:67]\n"
"v_mfma_f32_16x16x32_bf16 v[64:67], a[44:47], a[108:111], v[64:67]\n"
"v_mfma_f32_16x16x32_bf16 v[36:39], a[36:39], a[108:111], v[36:39]\n"
"v_mfma_f32_16x16x32_bf16 v[36:39], a[32:35], a[104:107], v[36:39]\n"
"s_waitcnt lgkmcnt(8)\n"
"v_mfma_f32_16x16x32_bf16 v[40:43], a[32:35], a[112:115], v[40:43]\n"
"v_mfma_f32_16x16x32_bf16 v[40:43], a[36:39], a[116:119], v[40:43]\n"
"global_load_dwordx4 a[84:87], v0, s[34:35] offset:1024 sc1\n"
"v_mfma_f32_16x16x32_bf16 v[68:71], a[44:47], a[116:119], v[68:71]\n"
"v_mfma_f32_16x16x32_bf16 v[68:71], a[40:43], a[112:115], v[68:71]\n"
"v_mfma_f32_16x16x32_bf16 v[72:75], a[40:43], a[120:123], v[72:75]\n"
"v_mfma_f32_16x16x32_bf16 v[72:75], a[44:47], a[124:127], v[72:75]\n"
"v_mfma_f32_16x16x32_bf16 v[44:47], a[36:39], a[124:127], v[44:47]\n"
"buffer_load_dwordx4 v1, s[48:51], s69 offen offset:2048 sc1 lds\n"
"v_mfma_f32_16x16x32_bf16 v[44:47], a[32:35], a[120:123], v[44:47]\n"
"s_waitcnt lgkmcnt(4)\n"
"v_mfma_f32_16x16x32_bf16 v[48:51], a[32:35], a[128:131], v[48:51]\n"
"v_mfma_f32_16x16x32_bf16 v[48:51], a[36:39], a[132:135], v[48:51]\n"
"global_load_dwordx4 a[88:91], v0, s[34:35] offset:2048 sc1\n"
"v_mfma_f32_16x16x32_bf16 v[76:79], a[44:47], a[132:135], v[76:79]\n"
"v_mfma_f32_16x16x32_bf16 v[76:79], a[40:43], a[128:131], v[76:79]\n"
"v_mfma_f32_16x16x32_bf16 v[80:83], a[40:43], a[136:139], v[80:83]\n"
"v_mfma_f32_16x16x32_bf16 v[80:83], a[44:47], a[140:143], v[80:83]\n"
"v_mfma_f32_16x16x32_bf16 v[52:55], a[36:39], a[140:143], v[52:55]\n"
"v_mfma_f32_16x16x32_bf16 v[52:55], a[32:35], a[136:139], v[52:55]\n"
"s_waitcnt lgkmcnt(0)\n"
"v_mfma_f32_16x16x32_bf16 v[56:59], a[32:35], a[144:147], v[56:59]\n"
"v_mfma_f32_16x16x32_bf16 v[56:59], a[36:39], a[148:151], v[56:59]\n"
"global_load_dwordx4 a[92:95], v0, s[34:35] offset:3072 sc1\n"
"v_mfma_f32_16x16x32_bf16 v[84:87], a[44:47], a[148:151], v[84:87]\n"
"v_mfma_f32_16x16x32_bf16 v[84:87], a[40:43], a[144:147], v[84:87]\n"
"v_mfma_f32_16x16x32_bf16 a[4:7], a[40:43], a[152:155], a[4:7]\n"
"s_add_u32 s34, s34, 8192\n"
"v_mfma_f32_16x16x32_bf16 a[4:7], a[44:47], a[156:159], a[4:7]\n"
"buffer_load_dwordx4 v1, s[48:51], s69 offen offset:3072 sc1 lds\n"
"v_mfma_f32_16x16x32_bf16 a[0:3], a[36:39], a[156:159], a[0:3]\n"
"v_mfma_f32_16x16x32_bf16 a[0:3], a[32:35], a[152:155], a[0:3]\n"
"s_waitcnt vmcnt(14)\n"
"v_mfma_f32_16x16x32_bf16 v[88:91], a[48:51], a[96:99], v[88:91]\n"
"v_mfma_f32_16x16x32_bf16 v[88:91], a[52:55], a[100:103], v[88:91]\n"
"global_load_dwordx4 a[32:35], v0, s[34:35] offset:-4096 sc1\n"
"v_mfma_f32_16x16x32_bf16 v[116:119], a[60:63], a[100:103], v[116:119]\n"
"v_mfma_f32_16x16x32_bf16 v[116:119], a[56:59], a[96:99], v[116:119]\n"
"v_mfma_f32_16x16x32_bf16 v[120:123], a[56:59], a[104:107], v[120:123]\n"
"v_mfma_f32_16x16x32_bf16 v[120:123], a[60:63], a[108:111], v[120:123]\n"
"v_mfma_f32_16x16x32_bf16 v[92:95], a[52:55], a[108:111], v[92:95]\n"
"L_st6798:\n"
"s_cbranch_execz L_st9095\n"
"v_mfma_f32_16x16x32_bf16 v[92:95], a[48:51], a[104:107], v[92:95]\n"
"v_mfma_f32_16x16x32_bf16 v[96:99], a[48:51], a[112:115], v[96:99]\n"
"v_mfma_f32_16x16x32_bf16 v[96:99], a[52:55], a[116:119], v[96:99]\n"
"global_load_dwordx4 a[36:39], v0, s[34:35] offset:-3072 sc1\n"
"v_mfma_f32_16x16x32_bf16 v[124:127], a[60:63], a[116:119], v[124:127]\n"
"v_mfma_f32_16x16x32_bf16 v[124:127], a[56:59], a[112:115], v[124:127]\n"
"v_mfma_f32_16x16x32_bf16 v[128:131], a[56:59], a[120:123], v[128:131]\n"
"v_mfma_f32_16x16x32_bf16 v[128:131], a[60:63], a[124:127], v[128:131]\n"
"v_mfma_f32_16x16x32_bf16 v[100:103], a[52:55], a[124:127], v[100:103]\n"
"v_mfma_f32_16x16x32_bf16 v[100:103], a[48:51], a[120:123], v[100:103]\n"
"v_mfma_f32_16x16x32_bf16 v[104:107], a[48:51], a[128:131], v[104:107]\n"
"buffer_load_dwordx2 v[8:9], off, s[56:59], %14 sc1\n"
"v_mfma_f32_16x16x32_bf16 v[104:107], a[52:55], a[132:135], v[104:107]\n"
"global_load_dwordx4 a[40:43], v0, s[34:35] offset:-2048 sc1\n"
"v_mfma_f32_16x16x32_bf16 v[132:135], a[60:63], a[132:135], v[132:135]\n"
"v_mfma_f32_16x16x32_bf16 v[132:135], a[56:59], a[128:131], v[132:135]\n"
"v_mfma_f32_16x16x32_bf16 v[136:139], a[56:59], a[136:139], v[136:139]\n"
"v_mfma_f32_16x16x32_bf16 v[136:139], a[60:63], a[140:143], v[136:139]\n"
"v_mfma_f32_16x16x32_bf16 v[108:111], a[52:55], a[140:143], v[108:111]\n"
"v_mfma_f32_16x16x32_bf16 v[108:111], a[48:51], a[136:139], v[108:111]\n"
"v_mfma_f32_16x16x32_bf16 v[112:115], a[48:51], a[144:147], v[112:115]\n"
"v_mfma_f32_16x16x32_bf16 v[112:115], a[52:55], a[148:151], v[112:115]\n"
"global_load_dwordx4 a[44:47], v0, s[34:35] offset:-1024 sc1\n"
"v_mfma_f32_16x16x32_bf16 v[140:143], a[60:63], a[148:151], v[140:143]\n"
"v_mfma_f32_16x16x32_bf16 v[140:143], a[56:59], a[144:147], v[140:143]\n"
"v_mfma_f32_16x16x32_bf16 a[12:15], a[56:59], a[152:155], a[12:15]\n"
"v_mfma_f32_16x16x32_bf16 a[12:15], a[60:63], a[156:159], a[12:15]\n"
"v_mfma_f32_16x16x32_bf16 a[8:11], a[52:55], a[156:159], a[8:11]\n"
"v_mfma_f32_16x16x32_bf16 a[8:11], a[48:51], a[152:155], a[8:11]\n"
"s_waitcnt vmcnt(13)\n"
"v_mfma_f32_16x16x32_bf16 v[144:147], a[64:67], a[96:99], v[144:147]\n"
"v_mfma_f32_16x16x32_bf16 v[144:147], a[68:71], a[100:103], v[144:147]\n"
"global_load_dwordx4 a[48:51], v0, s[34:35] offset:0 sc1\n"
"v_mfma_f32_16x16x32_bf16 v[172:175], a[76:79], a[100:103], v[172:175]\n"
"v_mfma_f32_16x16x32_bf16 v[172:175], a[72:75], a[96:99], v[172:175]\n"
"v_mfma_f32_16x16x32_bf16 v[176:179], a[72:75], a[104:107], v[176:179]\n"
"v_mfma_f32_16x16x32_bf16 v[176:179], a[76:79], a[108:111], v[176:179]\n"
"v_mfma_f32_16x16x32_bf16 v[148:151], a[68:71], a[108:111], v[148:151]\n"
"v_mfma_f32_16x16x32_bf16 v[148:151], a[64:67], a[104:107], v[148:151]\n"
"v_mfma_f32_16x16x32_bf16 v[152:155], a[64:67], a[112:115], v[152:155]\n"
"v_lshlrev_b32 v20, 16, v22\n"
"v_mfma_f32_16x16x32_bf16 v[152:155], a[68:71], a[116:119], v[152:155]\n"
"global_load_dwordx4 a[52:55], v0, s[34:35] offset:1024 sc1\n"
"v_and_b32 v21, s63, v22\n"
"v_mfma_f32_16x16x32_bf16 v[180:183], a[76:79], a[116:119], v[180:183]\n"
"v_lshlrev_b32 v22, 16, v23\n"
"v_mfma_f32_16x16x32_bf16 v[180:183], a[72:75], a[112:115], v[180:183]\n"
"v_and_b32 v23, s63, v23\n"
"v_mfma_f32_16x16x32_bf16 v[184:187], a[72:75], a[120:123], v[184:187]\n"
"v_add_f32 v60, v60, v20\n"
"v_mfma_f32_16x16x32_bf16 v[184:187], a[76:79], a[124:127], v[184:187]\n"
"v_add_f32 v61, v61, v21\n"
"v_add_f32 v62, v62, v22\n"
"v_mfma_f32_16x16x32_bf16 v[156:159], a[68:71], a[124:127], v[156:159]\n"
"v_add_f32 v63, v63, v23\n"
"v_mfma_f32_16x16x32_bf16 v[156:159], a[64:67], a[120:123], v[156:159]\n"
"v_add_f32 v64, v64, v20\n"
"v_mfma_f32_16x16x32_bf16 v[160:163], a[64:67], a[128:131], v[160:163]\n"
"v_add_f32 v65, v65, v21\n"
"v_mfma_f32_16x16x32_bf16 v[160:163], a[68:71], a[132:135], v[160:163]\n"
"global_load_dwordx4 a[56:59], v0, s[34:35] offset:2048 sc1\n"
"v_add_f32 v66, v66, v22\n"
"v_mfma_f32_16x16x32_bf16 v[188:191], a[76:79], a[132:135], v[188:191]\n"
"v_add_f32 v67, v67, v23\n"
"v_mfma_f32_16x16x32_bf16 v[188:191], a[72:75], a[128:131], v[188:191]\n"
"v_add_f32 v68, v68, v20\n"
"v_mfma_f32_16x16x32_bf16 v[192:195], a[72:75], a[136:139], v[192:195]\n"
"v_add_f32 v69, v69, v21\n"
"v_mfma_f32_16x16x32_bf16 v[192:195], a[76:79], a[140:143], v[192:195]\n"
"v_add_f32 v70, v70, v22\n"
"v_mfma_f32_16x16x32_bf16 v[164:167], a[68:71], a[140:143], v[164:167]\n"
"v_add_f32 v71, v71, v23\n"
"v_mfma_f32_16x16x32_bf16 v[164:167], a[64:67], a[136:139], v[164:167]\n"
"v_add_f32 v72, v72, v20\n"
"v_add_f32 v73, v73, v21\n"
"v_mfma_f32_16x16x32_bf16 v[168:171], a[64:67], a[144:147], v[168:171]\n"
"v_add_f32 v74, v74, v22\n"
"v_mfma_f32_16x16x32_bf16 v[168:171], a[68:71], a[148:151], v[168:171]\n"
"global_load_dwordx4 a[60:63], v0, s[34:35] offset:3072 sc1\n"
"v_add_f32 v75, v75, v23\n"
"v_mfma_f32_16x16x32_bf16 v[196:199], a[76:79], a[148:151], v[196:199]\n"
"v_add_f32 v76, v76, v20\n"
"v_mfma_f32_16x16x32_bf16 v[196:199], a[72:75], a[144:147], v[196:199]\n"
"v_add_f32 v77, v77, v21\n"
"v_mfma_f32_16x16x32_bf16 a[20:23], a[72:75], a[152:155], a[20:23]\n"
"s_add_u32 s34, s34, 8192\n"
"v_add_f32 v78, v78, v22\n"
"v_mfma_f32_16x16x32_bf16 a[20:23], a[76:79], a[156:159], a[20:23]\n"
"v_add_f32 v79, v79, v23\n"
"v_mfma_f32_16x16x32_bf16 a[16:19], a[68:71], a[156:159], a[16:19]\n"
"v_add_f32 v80, v80, v20\n"
"v_mfma_f32_16x16x32_bf16 a[16:19], a[64:67], a[152:155], a[16:19]\n"
"s_waitcnt vmcnt(10)\n"
"v_add_f32 v81, v81, v21\n"
"v_mfma_f32_16x16x32_bf16 v[200:203], a[80:83], a[96:99], v[200:203]\n"
"v_add_f32 v82, v82, v22\n"
"v_add_f32 v83, v83, v23\n"
"v_mfma_f32_16x16x32_bf16 v[200:203], a[84:87], a[100:103], v[200:203]\n"
"global_load_dwordx4 a[64:67], v0, s[34:35] offset:-4096 sc1\n"
"v_add_f32 v84, v84, v20\n"
"v_mfma_f32_16x16x32_bf16 v[228:231], a[92:95], a[100:103], v[228:231]\n"
"s_waitcnt vmcnt(10)\n"
"ds_read_b128 a[100:103], v2 offset:1024\n"
"v_add_f32 v85, v85, v21\n"
"v_mfma_f32_16x16x32_bf16 v[228:231], a[88:91], a[96:99], v[228:231]\n"
"ds_read_b128 a[96:99], v2 offset:0\n"
"v_add_f32 v86, v86, v22\n"
"v_mfma_f32_16x16x32_bf16 v[232:235], a[88:91], a[104:107], v[232:235]\n"
"v_add_f32 v87, v87, v23\n"
"v_mfma_f32_16x16x32_bf16 v[232:235], a[92:95], a[108:111], v[232:235]\n"
"v_accvgpr_read_b32 v12, a4\n"
"v_mfma_f32_16x16x32_bf16 v[204:207], a[84:87], a[108:111], v[204:207]\n"
"ds_read_b128 a[108:111], v2 offset:3072\n"
"v_accvgpr_read_b32 v13, a5\n"
"v_mfma_f32_16x16x32_bf16 v[204:207], a[80:83], a[104:107], v[204:207]\n"
"ds_read_b128 a[104:107], v2 offset:2048\n"
"v_accvgpr_read_b32 v14, a6\n"
"v_mfma_f32_16x16x32_bf16 v[208:211], a[80:83], a[112:115], v[208:211]\n"
"v_accvgpr_read_b32 v15, a7\n"
"v_mfma_f32_16x16x32_bf16 v[208:211], a[84:87], a[116:119], v[208:211]\n"
"global_load_dwordx4 a[68:71], v0, s[34:35] offset:-3072 sc1\n"
"v_add_f32 v12, v12, v20\n"
"v_mfma_f32_16x16x32_bf16 v[236:239], a[92:95], a[116:119], v[236:239]\n"
"ds_read_b128 a[116:119], v2 offset:5120\n"
"v_add_f32 v13, v13, v21\n"
"v_mfma_f32_16x16x32_bf16 v[236:239], a[88:91], a[112:115], v[236:239]\n"
"ds_read_b128 a[112:115], v2 offset:4096\n"
"v_add_f32 v14, v14, v22\n"
"v_mfma_f32_16x16x32_bf16 v[240:243], a[88:91], a[120:123], v[240:243]\n"
"v_add_f32 v15, v15, v23\n"
"v_accvgpr_write_b32 a4, v12\n"
"v_mfma_f32_16x16x32_bf16 v[240:243], a[92:95], a[124:127], v[240:243]\n"
"v_accvgpr_write_b32 a5, v13\n"
"v_mfma_f32_16x16x32_bf16 v[212:215], a[84:87], a[124:127], v[212:215]\n"
"ds_read_b128 a[124:127], v2 offset:7168\n"
"v_accvgpr_write_b32 a6, v14\n"
"v_mfma_f32_16x16x32_bf16 v[212:215], a[80:83], a[120:123], v[212:215]\n"
"ds_read_b128 a[120:123], v2 offset:6144\n"
"v_accvgpr_write_b32 a7, v15\n"
"v_mfma_f32_16x16x32_bf16 v[216:219], a[80:83], a[128:131], v[216:219]\n"
"v_mfma_f32_16x16x32_bf16 v[216:219], a[84:87], a[132:135], v[216:219]\n"
"global_load_dwordx4 a[72:75], v0, s[34:35] offset:-2048 sc1\n"
"v_lshrrev_b32 v20, 4, v0\n"
"v_mfma_f32_16x16x32_bf16 v[244:247], a[92:95], a[132:135], v[244:247]\n"
"ds_read_b128 a[132:135], v2 offset:9216\n"
"v_and_b32 v20, 0x30, v20\n"
"v_mfma_f32_16x16x32_bf16 v[244:247], a[88:91], a[128:131], v[244:247]\n"
"ds_read_b128 a[128:131], v2 offset:8192\n"
"buffer_load_dwordx2 v[22:23], v20, s[44:47], s61 offen offset:72\n"
"v_mfma_f32_16x16x32_bf16 v[248:251], a[88:91], a[136:139], v[248:251]\n"
"v_mfma_f32_16x16x32_bf16 v[248:251], a[92:95], a[140:143], v[248:251]\n"
"v_mfma_f32_16x16x32_bf16 v[220:223], a[84:87], a[140:143], v[220:223]\n"
"ds_read_b128 a[140:143], v2 offset:11264\n"
"v_mfma_f32_16x16x32_bf16 v[220:223], a[80:83], a[136:139], v[220:223]\n"
"ds_read_b128 a[136:139], v2 offset:10240\n"
"v_mfma_f32_16x16x32_bf16 v[224:227], a[80:83], a[144:147], v[224:227]\n"
"v_mfma_f32_16x16x32_bf16 v[224:227], a[84:87], a[148:151], v[224:227]\n"
"global_load_dwordx4 a[76:79], v0, s[34:35] offset:-1024 sc1\n"
"v_mfma_f32_16x16x32_bf16 v[252:255], a[92:95], a[148:151], v[252:255]\n"
"ds_read_b128 a[148:151], v2 offset:13312\n"
"v_mfma_f32_16x16x32_bf16 v[252:255], a[88:91], a[144:147], v[252:255]\n"
"ds_read_b128 a[144:147], v2 offset:12288\n"
"v_mfma_f32_16x16x32_bf16 a[28:31], a[88:91], a[152:155], a[28:31]\n"
"v_mfma_f32_16x16x32_bf16 a[28:31], a[92:95], a[156:159], a[28:31]\n"
"v_mfma_f32_16x16x32_bf16 a[24:27], a[84:87], a[156:159], a[24:27]\n"
"s_cmp_eq_u32 s62, 5\n"
"ds_read_b128 a[156:159], v2 offset:15360\n"
"v_mfma_f32_16x16x32_bf16 a[24:27], a[80:83], a[152:155], a[24:27]\n"
"ds_read_b128 a[152:155], v2 offset:14336\n"
"s_cbranch_scc1 L_last\n"
"L_P4:\n"
"s_waitcnt vmcnt(9) lgkmcnt(12)\n"
"v_mfma_f32_16x16x32_bf16 v[32:35], a[32:35], a[96:99], v[32:35]\n"
"s_add_u32 s48, s48, 131072\n"
"s_addc_u32 s49, s49, 0\n"
"v_mfma_f32_16x16x32_bf16 v[32:35], a[36:39], a[100:103], v[32:35]\n"
"global_load_dwordx4 a[80:83], v0, s[34:35] offset:0 sc1\n"
"v_mfma_f32_16x16x32_bf16 v[60:63], a[44:47], a[100:103], v[60:63]\n"
"s_barrier\n"
"v_mfma_f32_16x16x32_bf16 v[60:63], a[40:43], a[96:99], v[60:63]\n"
"v_mfma_f32_16x16x32_bf16 v[64:67], a[40:43], a[104:107], v[64:67]\n"
"v_mfma_f32_16x16x32_bf16 v[64:67], a[44:47], a[108:111], v[64:67]\n"
"v_mfma_f32_16x16x32_bf16 v[36:39], a[36:39], a[108:111], v[36:39]\n"
"v_subrev_u32 v8, s65, v8\n"
"v_mfma_f32_16x16x32_bf16 v[36:39], a[32:35], a[104:107], v[36:39]\n"
"s_waitcnt lgkmcnt(8)\n"
"v_xor_b32 v9, s79, v9\n"
"v_mfma_f32_16x16x32_bf16 v[40:43], a[32:35], a[112:115], v[40:43]\n"
"v_or_b32 v8, v8, v9\n"
"v_mfma_f32_16x16x32_bf16 v[40:43], a[36:39], a[116:119], v[40:43]\n"
"global_load_dwordx4 a[84:87], v0, s[34:35] offset:1024 sc1\n"
"v_cmp_lt_u32 vcc, 15, v8\n"
"v_mfma_f32_16x16x32_bf16 v[68:71], a[44:47], a[116:119], v[68:71]\n"
"s_cbranch_vccnz L_slow1\n"
"L_back1:\n"
"v_mfma_f32_16x16x32_bf16 v[68:71], a[40:43], a[112:115], v[68:71]\n"
"v_mfma_f32_16x16x32_bf16 v[72:75], a[40:43], a[120:123], v[72:75]\n"
"v_mfma_f32_16x16x32_bf16 v[72:75], a[44:47], a[124:127], v[72:75]\n"
"v_mfma_f32_16x16x32_bf16 v[44:47], a[36:39], a[124:127], v[44:47]\n"
"v_mfma_f32_16x16x32_bf16 v[44:47], a[32:35], a[120:123], v[44:47]\n"
"s_waitcnt lgkmcnt(4)\n"
"v_mfma_f32_16x16x32_bf16 v[48:51], a[32:35], a[128:131], v[48:51]\n"
"v_mfma_f32_16x16x32_bf16 v[48:51], a[36:39], a[132:135], v[48:51]\n"
"global_load_dwordx4 a[88:91], v0, s[34:35] offset:2048 sc1\n"
"v_mfma_f32_16x16x32_bf16 v[76:79], a[44:47], a[132:135], v[76:79]\n"
"v_mfma_f32_16x16x32_bf16 v[76:79], a[40:43], a[128:131], v[76:79]\n"
"s_add_u32 m0, s64, 16384\n"
"v_mfma_f32_16x16x32_bf16 v[80:83], a[40:43], a[136:139], v[80:83]\n"
"v_mfma_f32_16x16x32_bf16 v[80:83], a[44:47], a[140:143], v[80:83]\n"
"v_mfma_f32_16x16x32_bf16 v[52:55], a[36:39], a[140:143], v[52:55]\n"
"v_mfma_f32_16x16x32_bf16 v[52:55], a[32:35], a[136:139], v[52:55]\n"
"s_waitcnt lgkmcnt(0)\n"
"v_mfma_f32_16x16x32_bf16 v[56:59], a[32:35], a[144:147], v[56:59]\n"
"v_mfma_f32_16x16x32_bf16 v[56:59], a[36:39], a[148:151], v[56:59]\n"
"global_load_dwordx4 a[92:95], v0, s[34:35] offset:3072 sc1\n"
"v_mfma_f32_16x16x32_bf16 v[84:87], a[44:47], a[148:151], v[84:87]\n"
"v_mfma_f32_16x16x32_bf16 v[84:87], a[40:43], a[144:147], v[84:87]\n"
"v_mfma_f32_16x16x32_bf16 a[4:7], a[40:43], a[152:155], a[4:7]\n"
"s_add_u32 s34, s34, 8192\n"
"v_mfma_f32_16x16x32_bf16 a[4:7], a[44:47], a[156:159], a[4:7]\n"
"v_mfma_f32_16x16x32_bf16 a[0:3], a[36:39], a[156:159], a[0:3]\n"
"v_mfma_f32_16x16x32_bf16 a[0:3], a[32:35], a[152:155], a[0:3]\n"
"s_waitcnt vmcnt(9)\n"
"v_mfma_f32_16x16x32_bf16 v[88:91], a[48:51], a[96:99], v[88:91]\n"
"v_mfma_f32_16x16x32_bf16 v[88:91], a[52:55], a[100:103], v[88:91]\n"
"global_load_dwordx4 a[32:35], v0, s[34:35] offset:-4096 sc1\n"
"v_mfma_f32_16x16x32_bf16 v[116:119], a[60:63], a[100:103], v[116:119]\n"
"buffer_load_dwordx4 v0, s[48:51], s75 offen offset:0 sc1 lds\n"
"v_mfma_f32_16x16x32_bf16 v[116:119], a[56:59], a[96:99], v[116:119]\n"
"v_mfma_f32_16x16x32_bf16 v[120:123], a[56:59], a[104:107], v[120:123]\n"
"v_mfma_f32_16x16x32_bf16 v[120:123], a[60:63], a[108:111], v[120:123]\n"
"v_mfma_f32_16x16x32_bf16 v[92:95], a[52:55], a[108:111], v[92:95]\n"
"v_mfma_f32_16x16x32_bf16 v[92:95], a[48:51], a[104:107], v[92:95]\n"
"v_mfma_f32_16x16x32_bf16 v[96:99], a[48:51], a[112:115], v[96:99]\n"
"v_mfma_f32_16x16x32_bf16 v[96:99], a[52:55], a[116:119], v[96:99]\n"
"global_load_dwordx4 a[36:39], v0, s[34:35] offset:-3072 sc1\n"
"v_mfma_f32_16x16x32_bf16 v[124:127], a[60:63], a[116:119], v[124:127]\n"
"v_mfma_f32_16x16x32_bf16 v[124:127], a[56:59], a[112:115], v[124:127]\n"
"v_mfma_f32_16x16x32_bf16 v[128:131], a[56:59], a[120:123], v[128:131]\n"
"v_mfma_f32_16x16x32_bf16 v[128:131], a[60:63], a[124:127], v[128:131]\n"
"v_mfma_f32_16x16x32_bf16 v[100:103], a[52:55], a[124:127], v[100:103]\n"
"v_mfma_f32_16x16x32_bf16 v[100:103], a[48:51], a[120:123], v[100:103]\n"
"v_mfma_f32_16x16x32_bf16 v[104:107], a[48:51], a[128:131], v[104:107]\n"
"v_mfma_f32_16x16x32_bf16 v[104:107], a[52:55], a[132:135], v[104:107]\n"
"global_load_dwordx4 a[40:43], v0, s[34:35] offset:-2048 sc1\n"
"v_mfma_f32_16x16x32_bf16 v[132:135], a[60:63], a[132:135], v[132:135]\n"
"buffer_load_dwordx4 v0, s[48:51], s75 offen offset:1024 sc1 lds\n"
"v_mfma_f32_16x16x32_bf16 v[132:135], a[56:59], a[128:131], v[132:135]\n"
"v_mfma_f32_16x16x32_bf16 v[136:139], a[56:59], a[136:139], v[136:139]\n"
"v_mfma_f32_16x16x32_bf16 v[136:139], a[60:63], a[140:143], v[136:139]\n"
"v_mfma_f32_16x16x32_bf16 v[108:111], a[52:55], a[140:143], v[108:111]\n"
"v_mfma_f32_16x16x32_bf16 v[108:111], a[48:51], a[136:139], v[108:111]\n"
"v_mfma_f32_16x16x32_bf16 v[112:115], a[48:51], a[144:147], v[112:115]\n"
"v_mfma_f32_16x16x32_bf16 v[112:115], a[52:55], a[148:151], v[112:115]\n"
"global_load_dwordx4 a[44:47], v0, s[34:35] offset:-1024 sc1\n"
"v_mfma_f32_16x16x32_bf16 v[140:143], a[60:63], a[148:151], v[140:143]\n"
"v_mfma_f32_16x16x32_bf16 v[140:143], a[56:59], a[144:147], v[140:143]\n"
"v_mfma_f32_16x16x32_bf16 a[12:15], a[56:59], a[152:155], a[12:15]\n"
"v_mfma_f32_16x16x32_bf16 a[12:15], a[60:63], a[156:159], a[12:15]\n"
"v_mfma_f32_16x16x32_bf16 a[8:11], a[52:55], a[156:159], a[8:11]\n"
"v_mfma_f32_16x16x32_bf16 a[8:11], a[48:51], a[152:155], a[8:11]\n"
"s_waitcnt vmcnt(10)\n"
"v_mfma_f32_16x16x32_bf16 v[144:147], a[64:67], a[96:99], v[144:147]\n"
"v_mfma_f32_16x16x32_bf16 v[144:147], a[68:71], a[100:103], v[144:147]\n"
"global_load_dwordx4 a[48:51], v0, s[34:35] offset:0 sc1\n"
"v_mfma_f32_16x16x32_bf16 v[172:175], a[76:79], a[100:103], v[172:175]\n"
"v_mfma_f32_16x16x32_bf16 v[172:175], a[72:75], a[96:99], v[172:175]\n"
"buffer_load_dwordx4 v0, s[48:51], s75 offen offset:2048 sc1 lds\n"
"v_mfma_f32_16x16x32_bf16 v[176:179], a[72:75], a[104:107], v[176:179]\n"
"v_mfma_f32_16x16x32_bf16 v[176:179], a[76:79], a[108:111], v[176:179]\n"
"v_mfma_f32_16x16x32_bf16 v[148:151], a[68:71], a[108:111], v[148:151]\n"
"v_mfma_f32_16x16x32_bf16 v[148:151], a[64:67], a[104:107], v[148:151]\n"
"v_mfma_f32_16x16x32_bf16 v[152:155], a[64:67], a[112:115], v[152:155]\n"
"v_lshlrev_b32 v16, 16, v18\n"
"v_mfma_f32_16x16x32_bf16 v[152:155], a[68:71], a[116:119], v[152:155]\n"
"global_load_dwordx4 a[52:55], v0, s[34:35] offset:1024 sc1\n"
"v_and_b32 v17, s63, v18\n"
"v_mfma_f32_16x16x32_bf16 v[180:183], a[76:79], a[116:119], v[180:183]\n"
"v_lshlrev_b32 v18, 16, v19\n"
"v_mfma_f32_16x16x32_bf16 v[180:183], a[72:75], a[112:115], v[180:183]\n"
"v_and_b32 v19, s63, v19\n"
"v_mfma_f32_16x16x32_bf16 v[184:187], a[72:75], a[120:123], v[184:187]\n"
"v_add_f32 v88, v88, v16\n"
"v_mfma_f32_16x16x32_bf16 v[184:187], a[76:79], a[124:127], v[184:187]\n"
"v_add_f32 v89, v89, v17\n"
"v_add_f32 v90, v90, v18\n"
"v_mfma_f32_16x16x32_bf16 v[156:159], a[68:71], a[124:127], v[156:159]\n"
"v_add_f32 v91, v91, v19\n"
"v_mfma_f32_16x16x32_bf16 v[156:159], a[64:67], a[120:123], v[156:159]\n"
"v_add_f32 v92, v92, v16\n"
"v_mfma_f32_16x16x32_bf16 v[160:163], a[64:67], a[128:131], v[160:163]\n"
"v_add_f32 v93, v93, v17\n"
"v_mfma_f32_16x16x32_bf16 v[160:163], a[68:71], a[132:135], v[160:163]\n"
"global_load_dwordx4 a[56:59], v0, s[34:35] offset:2048 sc1\n"
"v_add_f32 v94, v94, v18\n"
"v_mfma_f32_16x16x32_bf16 v[188:191], a[76:79], a[132:135], v[188:191]\n"
"buffer_load_dwordx4 v0, s[48:51], s75 offen offset:3072 sc1 lds\n"
"v_mfma_f32_16x16x32_bf16 v[188:191], a[72:75], a[128:131], v[188:191]\n"
"v_add_f32 v95, v95, v19\n"
"v_add_f32 v96, v96, v16\n"
"v_mfma_f32_16x16x32_bf16 v[192:195], a[72:75], a[136:139], v[192:195]\n"
"v_add_f32 v97, v97, v17\n"
"v_mfma_f32_16x16x32_bf16 v[192:195], a[76:79], a[140:143], v[192:195]\n"
"v_add_f32 v98, v98, v18\n"
"v_mfma_f32_16x16x32_bf16 v[164:167], a[68:71], a[140:143], v[164:167]\n"
"v_add_f32 v99, v99, v19\n"
"v_mfma_f32_16x16x32_bf16 v[164:167], a[64:67], a[136:139], v[164:167]\n"
"v_add_f32 v100, v100, v16\n"
"v_add_f32 v101, v101, v17\n"
"v_mfma_f32_16x16x32_bf16 v[168:171], a[64:67], a[144:147], v[168:171]\n"
"v_add_f32 v102, v102, v18\n"
"v_mfma_f32_16x16x32_bf16 v[168:171], a[68:71], a[148:151], v[168:171]\n"
"global_load_dwordx4 a[60:63], v0, s[34:35] offset:3072 sc1\n"
"v_add_f32 v103, v103, v19\n"
"v_mfma_f32_16x16x32_bf16 v[196:199], a[76:79], a[148:151], v[196:199]\n"
"v_add_f32 v104, v104, v16\n"
"v_mfma_f32_16x16x32_bf16 v[196:199], a[72:75], a[144:147], v[196:199]\n"
"v_add_f32 v105, v105, v17\n"
"v_mfma_f32_16x16x32_bf16 a[20:23], a[72:75], a[152:155], a[20:23]\n"
"s_add_u32 s34, s34, 8192\n"
"v_add_f32 v106, v106, v18\n"
"v_mfma_f32_16x16x32_bf16 a[20:23], a[76:79], a[156:159], a[20:23]\n"
"v_add_f32 v107, v107, v19\n"
"v_mfma_f32_16x16x32_bf16 a[16:19], a[68:71], a[156:159], a[16:19]\n"
"v_add_f32 v108, v108, v16\n"
"v_mfma_f32_16x16x32_bf16 a[16:19], a[64:67], a[152:155], a[16:19]\n"
"s_waitcnt vmcnt(12)\n"
"v_add_f32 v109, v109, v17\n"
"v_mfma_f32_16x16x32_bf16 v[200:203], a[80:83], a[96:99], v[200:203]\n"
"v_add_f32 v110, v110, v18\n"
"v_add_f32 v111, v111, v19\n"
"v_mfma_f32_16x16x32_bf16 v[200:203], a[84:87], a[100:103], v[200:203]\n"
"global_load_dwordx4 a[64:67], v0, s[34:35] offset:-4096 sc1\n"
"v_add_f32 v112, v112, v16\n"
"v_mfma_f32_16x16x32_bf16 v[228:231], a[92:95], a[100:103], v[228:231]\n"
"ds_read_b128 a[100:103], v3 offset:1024\n"
"v_add_f32 v113, v113, v17\n"
"v_mfma_f32_16x16x32_bf16 v[228:231], a[88:91], a[96:99], v[228:231]\n"
"s_add_u32 m0, s64, 20480\n"
"ds_read_b128 a[96:99], v3 offset:0\n"
"v_add_f32 v114, v114, v18\n"
"v_mfma_f32_16x16x32_bf16 v[232:235], a[88:91], a[104:107], v[232:235]\n"
"v_add_f32 v115, v115, v19\n"
"v_mfma_f32_16x16x32_bf16 v[232:235], a[92:95], a[108:111], v[232:235]\n"
"v_accvgpr_read_b32 v12, a8\n"
"v_mfma_f32_16x16x32_bf16 v[204:207], a[84:87], a[108:111], v[204:207]\n"
"ds_read_b128 a[108:111], v3 offset:3072\n"
"v_accvgpr_read_b32 v13, a9\n"
"v_mfma_f32_16x16x32_bf16 v[204:207], a[80:83], a[104:107], v[204:207]\n"
"ds_read_b128 a[104:107], v3 offset:2048\n"
"v_accvgpr_read_b32 v14, a10\n"
"v_mfma_f32_16x16x32_bf16 v[208:211], a[80:83], a[112:115], v[208:211]\n"
"v_accvgpr_read_b32 v15, a11\n"
"v_mfma_f32_16x16x32_bf16 v[208:211], a[84:87], a[116:119], v[208:211]\n"
"global_load_dwordx4 a[68:71], v0, s[34:35] offset:-3072 sc1\n"
"v_add_f32 v12, v12, v16\n"
"v_mfma_f32_16x16x32_bf16 v[236:239], a[92:95], a[116:119], v[236:239]\n"
"ds_read_b128 a[116:119], v3 offset:5120\n"
"v_add_f32 v13, v13, v17\n"
"v_mfma_f32_16x16x32_bf16 v[236:239], a[88:91], a[112:115], v[236:239]\n"
"ds_read_b128 a[112:115], v3 offset:4096\n"
"v_add_f32 v14, v14, v18\n"
"v_mfma_f32_16x16x32_bf16 v[240:243], a[88:91], a[120:123], v[240:243]\n"
"v_add_f32 v15, v15, v19\n"
"v_accvgpr_write_b32 a8, v12\n"
"v_mfma_f32_16x16x32_bf16 v[240:243], a[92:95], a[124:127], v[240:243]\n"
"v_accvgpr_write_b32 a9, v13\n"
"v_mfma_f32_16x16x32_bf16 v[212:215], a[84:87], a[124:127], v[212:215]\n"
"ds_read_b128 a[124:127], v3 offset:7168\n"
"v_accvgpr_write_b32 a10, v14\n"
"v_mfma_f32_16x16x32_bf16 v[212:215], a[80:83], a[120:123], v[212:215]\n"
"ds_read_b128 a[120:123], v3 offset:6144\n"
"v_accvgpr_write_b32 a11, v15\n"
"v_mfma_f32_16x16x32_bf16 v[216:219], a[80:83], a[128:131], v[216:219]\n"
"v_mfma_f32_16x16x32_bf16 v[216:219], a[84:87], a[132:135], v[216:219]\n"
"global_load_dwordx4 a[72:75], v0, s[34:35] offset:-2048 sc1\n"
"v_mfma_f32_16x16x32_bf16 v[244:247], a[92:95], a[132:135], v[244:247]\n"
"ds_read_b128 a[132:135], v3 offset:9216\n"
"v_mfma_f32_16x16x32_bf16 v[244:247], a[88:91], a[128:131], v[244:247]\n"
"ds_read_b128 a[128:131], v3 offset:8192\n"
"v_mfma_f32_16x16x32_bf16 v[248:251], a[88:91], a[136:139], v[248:251]\n"
"buffer_load_dwordx4 v1, s[48:51], s75 offen offset:0 sc1 lds\n"
"v_mfma_f32_16x16x32_bf16 v[248:251], a[92:95], a[140:143], v[248:251]\n"
"v_mfma_f32_16x16x32_bf16 v[220:223], a[84:87], a[140:143], v[220:223]\n"
"ds_read_b128 a[140:143], v3 offset:11264\n"
"v_mfma_f32_16x16x32_bf16 v[220:223], a[80:83], a[136:139], v[220:223]\n"
"ds_read_b128 a[136:139], v3 offset:10240\n"
"v_mfma_f32_16x16x32_bf16 v[224:227], a[80:83], a[144:147], v[224:227]\n"
"v_mfma_f32_16x16x32_bf16 v[224:227], a[84:87], a[148:151], v[224:227]\n"
"global_load_dwordx4 a[76:79], v0, s[34:35] offset:-1024 sc1\n"
"v_mfma_f32_16x16x32_bf16 v[252:255], a[92:95], a[148:151], v[252:255]\n"
"ds_read_b128 a[148:151], v3 offset:13312\n"
"v_mfma_f32_16x16x32_bf16 v[252:255], a[88:91], a[144:147], v[252:255]\n"
"ds_read_b128 a[144:147], v3 offset:12288\n"
"v_mfma_f32_16x16x32_bf16 a[28:31], a[88:91], a[152:155], a[28:31]\n"
"v_mfma_f32_16x16x32_bf16 a[28:31], a[92:95], a[156:159], a[28:31]\n"
"v_mfma_f32_16x16x32_bf16 a[24:27], a[84:87], a[156:159], a[24:27]\n"
"ds_read_b128 a[156:159], v3 offset:15360\n"
"v_mfma_f32_16x16x32_bf16 a[24:27], a[80:83], a[152:155], a[24:27]\n"
"s_waitcnt vmcnt(11) lgkmcnt(11)\n"
"ds_read_b128 a[152:155], v3 offset:14336\n"
"v_mfma_f32_16x16x32_bf16 v[32:35], a[32:35], a[96:99], v[32:35]\n"
"v_mfma_f32_16x16x32_bf16 v[32:35], a[36:39], a[100:103], v[32:35]\n"
"global_load_dwordx4 a[80:83], v0, s[34:35] offset:0 sc1\n"
"v_mfma_f32_16x16x32_bf16 v[60:63], a[44:47], a[100:103], v[60:63]\n"
"v_mfma_f32_16x16x32_bf16 v[60:63], a[40:43], a[96:99], v[60:63]\n"
"buffer_load_dwordx4 v1, s[48:51], s75 offen offset:1024 sc1 lds\n"
"v_mfma_f32_16x16x32_bf16 v[64:67], a[40:43], a[104:107], v[64:67]\n"
"v_mfma_f32_16x16x32_bf16 v[64:67], a[44:47], a[108:111], v[64:67]\n"
"v_mfma_f32_16x16x32_bf16 v[36:39], a[36:39], a[108:111], v[36:39]\n"
"v_mfma_f32_16x16x32_bf16 v[36:39], a[32:35], a[104:107], v[36:39]\n"
"s_waitcnt lgkmcnt(8)\n"
"v_mfma_f32_16x16x32_bf16 v[40:43], a[32:35], a[112:115], v[40:43]\n"
"v_mfma_f32_16x16x32_bf16 v[40:43], a[36:39], a[116:119], v[40:43]\n"
"global_load_dwordx4 a[84:87], v0, s[34:35] offset:1024 sc1\n"
"v_mfma_f32_16x16x32_bf16 v[68:71], a[44:47], a[116:119], v[68:71]\n"
"v_mfma_f32_16x16x32_bf16 v[68:71], a[40:43], a[112:115], v[68:71]\n"
"v_mfma_f32_16x16x32_bf16 v[72:75], a[40:43], a[120:123], v[72:75]\n"
"v_mfma_f32_16x16x32_bf16 v[72:75], a[44:47], a[124:127], v[72:75]\n"
"v_mfma_f32_16x16x32_bf16 v[44:47], a[36:39], a[124:127], v[44:47]\n"
"v_mfma_f32_16x16x32_bf16 v[44:47], a[32:35], a[120:123], v[44:47]\n"
"s_waitcnt lgkmcnt(4)\n"
"v_mfma_f32_16x16x32_bf16 v[48:51], a[32:35], a[128:131], v[48:51]\n"
"v_mfma_f32_16x16x32_bf16 v[48:51], a[36:39], a[132:135], v[48:51]\n"
"global_load_dwordx4 a[88:91], v0, s[34:35] offset:2048 sc1\n"
"v_mfma_f32_16x16x32_bf16 v[76:79], a[44:47], a[132:135], v[76:79]\n"
"v_mfma_f32_16x16x32_bf16 v[76:79], a[40:43], a[128:131], v[76:79]\n"
"buffer_load_dwordx4 v1, s[48:51], s75 offen offset:2048 sc1 lds\n"
"v_mfma_f32_16x16x32_bf16 v[80:83], a[40:43], a[136:139], v[80:83]\n"
"v_mfma_f32_16x16x32_bf16 v[80:83], a[44:47], a[140:143], v[80:83]\n"
"v_mfma_f32_16x16x32_bf16 v[52:55], a[36:39], a[140:143], v[52:55]\n"
"v_mfma_f32_16x16x32_bf16 v[52:55], a[32:35], a[136:139], v[52:55]\n"
"s_waitcnt lgkmcnt(0)\n"
"v_mfma_f32_16x16x32_bf16 v[56:59], a[32:35], a[144:147], v[56:59]\n"
"v_mfma_f32_16x16x32_bf16 v[56:59], a[36:39], a[148:151], v[56:59]\n"
"global_load_dwordx4 a[92:95], v0, s[34:35] offset:3072 sc1\n"
"v_mfma_f32_16x16x32_bf16 v[84:87], a[44:47], a[148:151], v[84:87]\n"
"v_mfma_f32_16x16x32_bf16 v[84:87], a[40:43], a[144:147], v[84:87]\n"
"v_mfma_f32_16x16x32_bf16 a[4:7], a[40:43], a[152:155], a[4:7]\n"
"s_add_u32 s34, s34, 8192\n"
"v_mfma_f32_16x16x32_bf16 a[4:7], a[44:47], a[156:159], a[4:7]\n"
"v_mfma_f32_16x16x32_bf16 a[0:3], a[36:39], a[156:159], a[0:3]\n"
"v_mfma_f32_16x16x32_bf16 a[0:3], a[32:35], a[152:155], a[0:3]\n"
"s_waitcnt vmcnt(11)\n"
"v_mfma_f32_16x16x32_bf16 v[88:91], a[48:51], a[96:99], v[88:91]\n"
"v_mfma_f32_16x16x32_bf16 v[88:91], a[52:55], a[100:103], v[88:91]\n"
"global_load_dwordx4 a[32:35], v0, s[34:35] offset:-4096 sc1\n"
"v_mfma_f32_16x16x32_bf16 v[116:119], a[60:63], a[100:103], v[116:119]\n"
"v_mfma_f32_16x16x32_bf16 v[116:119], a[56:59], a[96:99], v[116:119]\n"
"buffer_load_dwordx4 v1, s[48:51], s75 offen offset:3072 sc1 lds\n"
"v_mfma_f32_16x16x32_bf16 v[120:123], a[56:59], a[104:107], v[120:123]\n"
"v_mfma_f32_16x16x32_bf16 v[120:123], a[60:63], a[108:111], v[120:123]\n"
"v_mfma_f32_16x16x32_bf16 v[92:95], a[52:55], a[108:111], v[92:95]\n"
"v_mfma_f32_16x16x32_bf16 v[92:95], a[48:51], a[104:107], v[92:95]\n"
"v_mfma_f32_16x16x32_bf16 v[96:99], a[48:51], a[112:115], v[96:99]\n"
"v_mfma_f32_16x16x32_bf16 v[96:99], a[52:55], a[116:119], v[96:99]\n"
"global_load_dwordx4 a[36:39], v0, s[34:35] offset:-3072 sc1\n"
"v_mfma_f32_16x16x32_bf16 v[124:127], a[60:63], a[116:119], v[124:127]\n"
"v_mfma_f32_16x16x32_bf16 v[124:127], a[56:59], a[112:115], v[124:127]\n"
"v_mfma_f32_16x16x32_bf16 v[128:131], a[56:59], a[120:123], v[128:131]\n"
"v_mfma_f32_16x16x32_bf16 v[128:131], a[60:63], a[124:127], v[128:131]\n"
"v_mfma_f32_16x16x32_bf16 v[100:103], a[52:55], a[124:127], v[100:103]\n"
"v_mfma_f32_16x16x32_bf16 v[100:103], a[48:51], a[120:123], v[100:103]\n"
"v_mfma_f32_16x16x32_bf16 v[104:107], a[48:51], a[128:131], v[104:107]\n"
"v_mfma_f32_16x16x32_bf16 v[104:107], a[52:55], a[132:135], v[104:107]\n"
"global_load_dwordx4 a[40:43], v0, s[34:35] offset:-2048 sc1\n"
"v_mfma_f32_16x16x32_bf16 v[132:135], a[60:63], a[132:135], v[132:135]\n"
"v_mfma_f32_16x16x32_bf16 v[132:135], a[56:59], a[128:131], v[132:135]\n"
"s_add_u32 m0, s64, 24576\n"
"v_mfma_f32_16x16x32_bf16 v[136:139], a[56:59], a[136:139], v[136:139]\n"
"v_mfma_f32_16x16x32_bf16 v[136:139], a[60:63], a[140:143], v[136:139]\n"
"v_mfma_f32_16x16x32_bf16 v[108:111], a[52:55], a[140:143], v[108:111]\n"
"v_mfma_f32_16x16x32_bf16 v[108:111], a[48:51], a[136:139], v[108:111]\n"
"v_mfma_f32_16x16x32_bf16 v[112:115], a[48:51], a[144:147], v[112:115]\n"
"v_mfma_f32_16x16x32_bf16 v[112:115], a[52:55], a[148:151], v[112:115]\n"
"global_load_dwordx4 a[44:47], v0, s[34:35] offset:-1024 sc1\n"
"v_mfma_f32_16x16x32_bf16 v[140:143], a[60:63], a[148:151], v[140:143]\n"
"v_mfma_f32_16x16x32_bf16 v[140:143], a[56:59], a[144:147], v[140:143]\n"
"v_mfma_f32_16x16x32_bf16 a[12:15], a[56:59], a[152:155], a[12:15]\n"
"v_mfma_f32_16x16x32_bf16 a[12:15], a[60:63], a[156:159], a[12:15]\n"
"v_mfma_f32_16x16x32_bf16 a[8:11], a[52:55], a[156:159], a[8:11]\n"
"v_mfma_f32_16x16x32_bf16 a[8:11], a[48:51], a[152:155], a[8:11]\n"
"s_waitcnt vmcnt(11)\n"
"v_mfma_f32_16x16x32_bf16 v[144:147], a[64:67], a[96:99], v[144:147]\n"
"v_mfma_f32_16x16x32_bf16 v[144:147], a[68:71], a[100:103], v[144:147]\n"
"global_load_dwordx4 a[48:51], v0, s[34:35] offset:0 sc1\n"
"v_mfma_f32_16x16x32_bf16 v[172:175], a[76:79], a[100:103], v[172:175]\n"
"v_mfma_f32_16x16x32_bf16 v[172:175], a[72:75], a[96:99], v[172:175]\n"
"buffer_load_dwordx4 v0, s[48:51], s69 offen offset:0 sc1 lds\n"
"v_mfma_f32_16x16x32_bf16 v[176:179], a[72:75], a[104:107], v[176:179]\n"
"v_mfma_f32_16x16x32_bf16 v[176:179], a[76:79], a[108:111], v[176:179]\n"
"v_mfma_f32_16x16x32_bf16 v[148:151], a[68:71], a[108:111], v[148:151]\n"
"v_mfma_f32_16x16x32_bf16 v[148:151], a[64:67], a[104:107], v[148:151]\n"
"v_mfma_f32_16x16x32_bf16 v[152:155], a[64:67], a[112:115], v[152:155]\n"
"v_lshlrev_b32 v20, 16, v22\n"
"v_mfma_f32_16x16x32_bf16 v[152:155], a[68:71], a[116:119], v[152:155]\n"
"global_load_dwordx4 a[52:55], v0, s[34:35] offset:1024 sc1\n"
"v_and_b32 v21, s63, v22\n"
"v_mfma_f32_16x16x32_bf16 v[180:183], a[76:79], a[116:119], v[180:183]\n"
"v_lshlrev_b32 v22, 16, v23\n"
"v_mfma_f32_16x16x32_bf16 v[180:183], a[72:75], a[112:115], v[180:183]\n"
"v_and_b32 v23, s63, v23\n"
"v_mfma_f32_16x16x32_bf16 v[184:187], a[72:75], a[120:123], v[184:187]\n"
"v_add_f32 v116, v116, v20\n"
"v_mfma_f32_16x16x32_bf16 v[184:187], a[76:79], a[124:127], v[184:187]\n"
"v_add_f32 v117, v117, v21\n"
"v_add_f32 v118, v118, v22\n"
"v_mfma_f32_16x16x32_bf16 v[156:159], a[68:71], a[124:127], v[156:159]\n"
"v_add_f32 v119, v119, v23\n"
"v_mfma_f32_16x16x32_bf16 v[156:159], a[64:67], a[120:123], v[156:159]\n"
"v_add_f32 v120, v120, v20\n"
"v_mfma_f32_16x16x32_bf16 v[160:163], a[64:67], a[128:131], v[160:163]\n"
"v_add_f32 v121, v121, v21\n"
"v_mfma_f32_16x16x32_bf16 v[160:163], a[68:71], a[132:135], v[160:163]\n"
"global_load_dwordx4 a[56:59], v0, s[34:35] offset:2048 sc1\n"
"v_add_f32 v122, v122, v22\n"
"v_mfma_f32_16x16x32_bf16 v[188:191], a[76:79], a[132:135], v[188:191]\n"
"v_add_f32 v123, v123, v23\n"
"v_add_f32 v124, v124, v20\n"
"v_mfma_f32_16x16x32_bf16 v[188:191], a[72:75], a[128:131], v[188:191]\n"
"buffer_load_dwordx4 v0, s[48:51], s69 offen offset:1024 sc1 lds\n"
"v_mfma_f32_16x16x32_bf16 v[192:195], a[72:75], a[136:139], v[192:195]\n"
"v_add_f32 v125, v125, v21\n"
"v_mfma_f32_16x16x32_bf16 v[192:195], a[76:79], a[140:143], v[192:195]\n"
"v_add_f32 v126, v126, v22\n"
"v_mfma_f32_16x16x32_bf16 v[164:167], a[68:71], a[140:143], v[164:167]\n"
"v_add_f32 v127, v127, v23\n"
"v_mfma_f32_16x16x32_bf16 v[164:167], a[64:67], a[136:139], v[164:167]\n"
"v_add_f32 v128, v128, v20\n"
"v_add_f32 v129, v129, v21\n"
"v_mfma_f32_16x16x32_bf16 v[168:171], a[64:67], a[144:147], v[168:171]\n"
"v_add_f32 v130, v130, v22\n"
"v_mfma_f32_16x16x32_bf16 v[168:171], a[68:71], a[148:151], v[168:171]\n"
"global_load_dwordx4 a[60:63], v0, s[34:35] offset:3072 sc1\n"
"v_add_f32 v131, v131, v23\n"
"v_mfma_f32_16x16x32_bf16 v[196:199], a[76:79], a[148:151], v[196:199]\n"
"v_add_f32 v132, v132, v20\n"
"v_mfma_f32_16x16x32_bf16 v[196:199], a[72:75], a[144:147], v[196:199]\n"
"v_add_f32 v133, v133, v21\n"
"v_mfma_f32_16x16x32_bf16 a[20:23], a[72:75], a[152:155], a[20:23]\n"
"s_add_u32 s34, s34, 8192\n"
"v_add_f32 v134, v134, v22\n"
"v_mfma_f32_16x16x32_bf16 a[20:23], a[76:79], a[156:159], a[20:23]\n"
"v_add_f32 v135, v135, v23\n"
"v_mfma_f32_16x16x32_bf16 a[16:19], a[68:71], a[156:159], a[16:19]\n"
"v_add_f32 v136, v136, v20\n"
"v_mfma_f32_16x16x32_bf16 a[16:19], a[64:67], a[152:155], a[16:19]\n"
"s_waitcnt vmcnt(11)\n"
"v_add_f32 v137, v137, v21\n"
"v_mfma_f32_16x16x32_bf16 v[200:203], a[80:83], a[96:99], v[200:203]\n"
"v_add_f32 v138, v138, v22\n"
"v_add_f32 v139, v139, v23\n"
"v_mfma_f32_16x16x32_bf16 v[200:203], a[84:87], a[100:103], v[200:203]\n"
"global_load_dwordx4 a[64:67], v0, s[34:35] offset:-4096 sc1\n"
"v_add_f32 v140, v140, v20\n"
"v_mfma_f32_16x16x32_bf16 v[228:231], a[92:95], a[100:103], v[228:231]\n"
"ds_read_b128 a[100:103], v4 offset:1024\n"
"v_add_f32 v141, v141, v21\n"
"v_mfma_f32_16x16x32_bf16 v[228:231], a[88:91], a[96:99], v[228:231]\n"
"ds_read_b128 a[96:99], v4 offset:0\n"
"v_add_f32 v142, v142, v22\n"
"v_mfma_f32_16x16x32_bf16 v[232:235], a[88:91], a[104:107], v[232:235]\n"
"buffer_load_dwordx4 v0, s[48:51], s69 offen offset:2048 sc1 lds\n"
"v_mfma_f32_16x16x32_bf16 v[232:235], a[92:95], a[108:111], v[232:235]\n"
"v_add_f32 v143, v143, v23\n"
"v_accvgpr_read_b32 v12, a12\n"
"v_mfma_f32_16x16x32_bf16 v[204:207], a[84:87], a[108:111], v[204:207]\n"
"ds_read_b128 a[108:111], v4 offset:3072\n"
"v_accvgpr_read_b32 v13, a13\n"
"v_mfma_f32_16x16x32_bf16 v[204:207], a[80:83], a[104:107], v[204:207]\n"
"ds_read_b128 a[104:107], v4 offset:2048\n"
"v_accvgpr_read_b32 v14, a14\n"
"v_mfma_f32_16x16x32_bf16 v[208:211], a[80:83], a[112:115], v[208:211]\n"
"v_accvgpr_read_b32 v15, a15\n"
"v_mfma_f32_16x16x32_bf16 v[208:211], a[84:87], a[116:119], v[208:211]\n"
"global_load_dwordx4 a[68:71], v0, s[34:35] offset:-3072 sc1\n"
"v_add_f32 v12, v12, v20\n"
"v_mfma_f32_16x16x32_bf16 v[236:239], a[92:95], a[116:119], v[236:239]\n"
"ds_read_b128 a[116:119], v4 offset:5120\n"
"v_add_f32 v13, v13, v21\n"
"v_mfma_f32_16x16x32_bf16 v[236:239], a[88:91], a[112:115], v[236:239]\n"
"L_st7379:\n"
"s_cbranch_execz L_st8279\n"
"ds_read_b128 a[112:115], v4 offset:4096\n"
"v_add_f32 v14, v14, v22\n"
"v_mfma_f32_16x16x32_bf16 v[240:243], a[88:91], a[120:123], v[240:243]\n"
"v_add_f32 v15, v15, v23\n"
"v_accvgpr_write_b32 a12, v12\n"
"v_mfma_f32_16x16x32_bf16 v[240:243], a[92:95], a[124:127], v[240:243]\n"
"v_accvgpr_write_b32 a13, v13\n"
"v_mfma_f32_16x16x32_bf16 v[212:215], a[84:87], a[124:127], v[212:215]\n"
"ds_read_b128 a[124:127], v4 offset:7168\n"
"v_accvgpr_write_b32 a14, v14\n"
"v_mfma_f32_16x16x32_bf16 v[212:215], a[80:83], a[120:123], v[212:215]\n"
"ds_read_b128 a[120:123], v4 offset:6144\n"
"v_accvgpr_write_b32 a15, v15\n"
"v_mfma_f32_16x16x32_bf16 v[216:219], a[80:83], a[128:131], v[216:219]\n"
"v_mfma_f32_16x16x32_bf16 v[216:219], a[84:87], a[132:135], v[216:219]\n"
"global_load_dwordx4 a[72:75], v0, s[34:35] offset:-2048 sc1\n"
"v_mfma_f32_16x16x32_bf16 v[244:247], a[92:95], a[132:135], v[244:247]\n"
"ds_read_b128 a[132:135], v4 offset:9216\n"
"v_mfma_f32_16x16x32_bf16 v[244:247], a[88:91], a[128:131], v[244:247]\n"
"ds_read_b128 a[128:131], v4 offset:8192\n"
"v_mfma_f32_16x16x32_bf16 v[248:251], a[88:91], a[136:139], v[248:251]\n"
"buffer_load_dwordx4 v0, s[48:51], s69 offen offset:3072 sc1 lds\n"
"v_mfma_f32_16x16x32_bf16 v[248:251], a[92:95], a[140:143], v[248:251]\n"
"v_mfma_f32_16x16x32_bf16 v[220:223], a[84:87], a[140:143], v[220:223]\n"
"ds_read_b128 a[140:143], v4 offset:11264\n"
"v_mfma_f32_16x16x32_bf16 v[220:223], a[80:83], a[136:139], v[220:223]\n"
"ds_read_b128 a[136:139], v4 offset:10240\n"
"v_mfma_f32_16x16x32_bf16 v[224:227], a[80:83], a[144:147], v[224:227]\n"
"v_mfma_f32_16x16x32_bf16 v[224:227], a[84:87], a[148:151], v[224:227]\n"
"global_load_dwordx4 a[76:79], v0, s[34:35] offset:-1024 sc1\n"
"v_mfma_f32_16x16x32_bf16 v[252:255], a[92:95], a[148:151], v[252:255]\n"
"ds_read_b128 a[148:151], v4 offset:13312\n"
"v_mfma_f32_16x16x32_bf16 v[252:255], a[88:91], a[144:147], v[252:255]\n"
"ds_read_b128 a[144:147], v4 offset:12288\n"
"v_mfma_f32_16x16x32_bf16 a[28:31], a[88:91], a[152:155], a[28:31]\n"
"v_mfma_f32_16x16x32_bf16 a[28:31], a[92:95], a[156:159], a[28:31]\n"
"v_mfma_f32_16x16x32_bf16 a[24:27], a[84:87], a[156:159], a[24:27]\n"
"ds_read_b128 a[156:159], v4 offset:15360\n"
"v_mfma_f32_16x16x32_bf16 a[24:27], a[80:83], a[152:155], a[24:27]\n"
"s_waitcnt vmcnt(12) lgkmcnt(11)\n"
"ds_read_b128 a[152:155], v4 offset:14336\n"
"v_mfma_f32_16x16x32_bf16 v[32:35], a[32:35], a[96:99], v[32:35]\n"
"v_mfma_f32_16x16x32_bf16 v[32:35], a[36:39], a[100:103], v[32:35]\n"
"global_load_dwordx4 a[80:83], v0, s[34:35] offset:0 sc1\n"
"v_lshrrev_b32 v16, 4, v0\n"
"v_mfma_f32_16x16x32_bf16 v[60:63], a[44:47], a[100:103], v[60:63]\n"
"v_and_b32 v16, 0x30, v16\n"
"v_mfma_f32_16x16x32_bf16 v[60:63], a[40:43], a[96:99], v[60:63]\n"
"s_add_u32 m0, s64, 28672\n"
"buffer_load_dwordx2 v[18:19], v16, s[44:47], s61 offen offset:128\n"
"v_mfma_f32_16x16x32_bf16 v[64:67], a[40:43], a[104:107], v[64:67]\n"
"v_mfma_f32_16x16x32_bf16 v[64:67], a[44:47], a[108:111], v[64:67]\n"
"v_mfma_f32_16x16x32_bf16 v[36:39], a[36:39], a[108:111], v[36:39]\n"
"v_mfma_f32_16x16x32_bf16 v[36:39], a[32:35], a[104:107], v[36:39]\n"
"s_waitcnt lgkmcnt(8)\n"
"v_mfma_f32_16x16x32_bf16 v[40:43], a[32:35], a[112:115], v[40:43]\n"
"v_mfma_f32_16x16x32_bf16 v[40:43], a[36:39], a[116:119], v[40:43]\n"
"global_load_dwordx4 a[84:87], v0, s[34:35] offset:1024 sc1\n"
"v_lshrrev_b32 v20, 4, v0\n"
"v_mfma_f32_16x16x32_bf16 v[68:71], a[44:47], a[116:119], v[68:71]\n"
"v_and_b32 v20, 0x30, v20\n"
"v_mfma_f32_16x16x32_bf16 v[68:71], a[40:43], a[112:115], v[68:71]\n"
"buffer_load_dwordx2 v[22:23], v20, s[44:47], s61 offen offset:136\n"
"v_mfma_f32_16x16x32_bf16 v[72:75], a[40:43], a[120:123], v[72:75]\n"
"v_mfma_f32_16x16x32_bf16 v[72:75], a[44:47], a[124:127], v[72:75]\n"
"v_mfma_f32_16x16x32_bf16 v[44:47], a[36:39], a[124:127], v[44:47]\n"
"v_mfma_f32_16x16x32_bf16 v[44:47], a[32:35], a[120:123], v[44:47]\n"
"s_waitcnt lgkmcnt(4)\n"
"v_mfma_f32_16x16x32_bf16 v[48:51], a[32:35], a[128:131], v[48:51]\n"
"v_mfma_f32_16x16x32_bf16 v[48:51], a[36:39], a[132:135], v[48:51]\n"
"global_load_dwordx4 a[88:91], v0, s[34:35] offset:2048 sc1\n"
"v_lshrrev_b32 v24, 4, v0\n"
"v_mfma_f32_16x16x32_bf16 v[76:79], a[44:47], a[132:135], v[76:79]\n"
"v_and_b32 v24, 0x30, v24\n"
"v_mfma_f32_16x16x32_bf16 v[76:79], a[40:43], a[128:131], v[76:79]\n"
"buffer_load_dwordx4 v1, s[48:51], s69 offen offset:0 sc1 lds\n"
"buffer_load_dwordx2 v[26:27], v24, s[44:47], s61 offen offset:192\n"
"v_mfma_f32_16x16x32_bf16 v[80:83], a[40:43], a[136:139], v[80:83]\n"
"v_mfma_f32_16x16x32_bf16 v[80:83], a[44:47], a[140:143], v[80:83]\n"
"v_mfma_f32_16x16x32_bf16 v[52:55], a[36:39], a[140:143], v[52:55]\n"
"v_mfma_f32_16x16x32_bf16 v[52:55], a[32:35], a[136:139], v[52:55]\n"
"s_waitcnt lgkmcnt(0)\n"
"v_mfma_f32_16x16x32_bf16 v[56:59], a[32:35], a[144:147], v[56:59]\n"
"v_mfma_f32_16x16x32_bf16 v[56:59], a[36:39], a[148:151], v[56:59]\n"
"global_load_dwordx4 a[92:95], v0, s[34:35] offset:3072 sc1\n"
"v_lshrrev_b32 v28, 4, v0\n"
"v_mfma_f32_16x16x32_bf16 v[84:87], a[44:47], a[148:151], v[84:87]\n"
"v_and_b32 v28, 0x30, v28\n"
"v_mfma_f32_16x16x32_bf16 v[84:87], a[40:43], a[144:147], v[84:87]\n"
"buffer_load_dwordx2 v[30:31], v28, s[44:47], s61 offen offset:200\n"
"v_mfma_f32_16x16x32_bf16 a[4:7], a[40:43], a[152:155], a[4:7]\n"
"s_add_u32 s34, s34, 8192\n"
"v_mfma_f32_16x16x32_bf16 a[4:7], a[44:47], a[156:159], a[4:7]\n"
"v_mfma_f32_16x16x32_bf16 a[0:3], a[36:39], a[156:159], a[0:3]\n"
"v_mfma_f32_16x16x32_bf16 a[0:3], a[32:35], a[152:155], a[0:3]\n"
"s_waitcnt vmcnt(15)\n"
"v_mfma_f32_16x16x32_bf16 v[88:91], a[48:51], a[96:99], v[88:91]\n"
"v_mfma_f32_16x16x32_bf16 v[88:91], a[52:55], a[100:103], v[88:91]\n"
"global_load_dwordx4 a[32:35], v0, s[34:35] offset:-4096 sc1\n"
"v_mfma_f32_16x16x32_bf16 v[116:119], a[60:63], a[100:103], v[116:119]\n"
"v_mfma_f32_16x16x32_bf16 v[116:119], a[56:59], a[96:99], v[116:119]\n"
"buffer_load_dwordx4 v1, s[48:51], s69 offen offset:1024 sc1 lds\n"
"v_mfma_f32_16x16x32_bf16 v[120:123], a[56:59], a[104:107], v[120:123]\n"
"v_mfma_f32_16x16x32_bf16 v[120:123], a[60:63], a[108:111], v[120:123]\n"
"v_mfma_f32_16x16x32_bf16 v[92:95], a[52:55], a[108:111], v[92:95]\n"
"v_mfma_f32_16x16x32_bf16 v[92:95], a[48:51], a[104:107], v[92:95]\n"
"v_mfma_f32_16x16x32_bf16 v[96:99], a[48:51], a[112:115], v[96:99]\n"
"v_mfma_f32_16x16x32_bf16 v[96:99], a[52:55], a[116:119], v[96:99]\n"
"global_load_dwordx4 a[36:39], v0, s[34:35] offset:-3072 sc1\n"
"v_mfma_f32_16x16x32_bf16 v[124:127], a[60:63], a[116:119], v[124:127]\n"
"v_mfma_f32_16x16x32_bf16 v[124:127], a[56:59], a[112:115], v[124:127]\n"
"v_mfma_f32_16x16x32_bf16 v[128:131], a[56:59], a[120:123], v[128:131]\n"
"v_mfma_f32_16x16x32_bf16 v[128:131], a[60:63], a[124:127], v[128:131]\n"
"v_mfma_f32_16x16x32_bf16 v[100:103], a[52:55], a[124:127], v[100:103]\n"
"v_mfma_f32_16x16x32_bf16 v[100:103], a[48:51], a[120:123], v[100:103]\n"
"v_mfma_f32_16x16x32_bf16 v[104:107], a[48:51], a[128:131], v[104:107]\n"
"v_mfma_f32_16x16x32_bf16 v[104:107], a[52:55], a[132:135], v[104:107]\n"
"global_load_dwordx4 a[40:43], v0, s[34:35] offset:-2048 sc1\n"
"v_mfma_f32_16x16x32_bf16 v[132:135], a[60:63], a[132:135], v[132:135]\n"
"v_mfma_f32_16x16x32_bf16 v[132:135], a[56:59], a[128:131], v[132:135]\n"
"buffer_load_dwordx4 v1, s[48:51], s69 offen offset:2048 sc1 lds\n"
"v_mfma_f32_16x16x32_bf16 v[136:139], a[56:59], a[136:139], v[136:139]\n"
"v_mfma_f32_16x16x32_bf16 v[136:139], a[60:63], a[140:143], v[136:139]\n"
"v_mfma_f32_16x16x32_bf16 v[108:111], a[52:55], a[140:143], v[108:111]\n"
"v_mfma_f32_16x16x32_bf16 v[108:111], a[48:51], a[136:139], v[108:111]\n"
"v_mfma_f32_16x16x32_bf16 v[112:115], a[48:51], a[144:147], v[112:115]\n"
"v_mfma_f32_16x16x32_bf16 v[112:115], a[52:55], a[148:151], v[112:115]\n"
"global_load_dwordx4 a[44:47], v0, s[34:35] offset:-1024 sc1\n"
"v_mfma_f32_16x16x32_bf16 v[140:143], a[60:63], a[148:151], v[140:143]\n"
"v_mfma_f32_16x16x32_bf16 v[140:143], a[56:59], a[144:147], v[140:143]\n"
"v_mfma_f32_16x16x32_bf16 a[12:15], a[56:59], a[152:155], a[12:15]\n"
"v_mfma_f32_16x16x32_bf16 a[12:15], a[60:63], a[156:159], a[12:15]\n"
"v_mfma_f32_16x16x32_bf16 a[8:11], a[52:55], a[156:159], a[8:11]\n"
"v_mfma_f32_16x16x32_bf16 a[8:11], a[48:51], a[152:155], a[8:11]\n"
"s_waitcnt vmcnt(15)\n"
"v_mfma_f32_16x16x32_bf16 v[144:147], a[64:67], a[96:99], v[144:147]\n"
"v_mfma_f32_16x16x32_bf16 v[144:147], a[68:71], a[100:103], v[144:147]\n"
"global_load_dwordx4 a[48:51], v0, s[34:35] offset:0 sc1\n"
"v_mfma_f32_16x16x32_bf16 v[172:175], a[76:79], a[100:103], v[172:175]\n"
"v_mfma_f32_16x16x32_bf16 v[172:175], a[72:75], a[96:99], v[172:175]\n"
"buffer_load_dwordx4 v1, s[48:51], s69 offen offset:3072 sc1 lds\n"
"v_mfma_f32_16x16x32_bf16 v[176:179], a[72:75], a[104:107], v[176:179]\n"
"v_mfma_f32_16x16x32_bf16 v[176:179], a[76:79], a[108:111], v[176:179]\n"
"v_mfma_f32_16x16x32_bf16 v[148:151], a[68:71], a[108:111], v[148:151]\n"
"v_mfma_f32_16x16x32_bf16 v[148:151], a[64:67], a[104:107], v[148:151]\n"
"v_mfma_f32_16x16x32_bf16 v[152:155], a[64:67], a[112:115], v[152:155]\n"
"v_mfma_f32_16x16x32_bf16 v[152:155], a[68:71], a[116:119], v[152:155]\n"
"global_load_dwordx4 a[52:55], v0, s[34:35] offset:1024 sc1\n"
"v_mfma_f32_16x16x32_bf16 v[180:183], a[76:79], a[116:119], v[180:183]\n"
"v_mfma_f32_16x16x32_bf16 v[180:183], a[72:75], a[112:115], v[180:183]\n"
"v_mfma_f32_16x16x32_bf16 v[184:187], a[72:75], a[120:123], v[184:187]\n"
"v_mfma_f32_16x16x32_bf16 v[184:187], a[76:79], a[124:127], v[184:187]\n"
"v_mfma_f32_16x16x32_bf16 v[156:159], a[68:71], a[124:127], v[156:159]\n"
"v_mfma_f32_16x16x32_bf16 v[156:159], a[64:67], a[120:123], v[156:159]\n"
"v_mfma_f32_16x16x32_bf16 v[160:163], a[64:67], a[128:131], v[160:163]\n"
"v_mfma_f32_16x16x32_bf16 v[160:163], a[68:71], a[132:135], v[160:163]\n"
"global_load_dwordx4 a[56:59], v0, s[34:35] offset:2048 sc1\n"
"v_mfma_f32_16x16x32_bf16 v[188:191], a[76:79], a[132:135], v[188:191]\n"
"v_mfma_f32_16x16x32_bf16 v[188:191], a[72:75], a[128:131], v[188:191]\n"
"v_mfma_f32_16x16x32_bf16 v[192:195], a[72:75], a[136:139], v[192:195]\n"
"s_cmp_lg_u32 s29, 0\n"
"s_cselect_b32 s60, s31, s60\n"
"s_cselect_b32 s30, 0, s30\n"
"v_mfma_f32_16x16x32_bf16 v[192:195], a[76:79], a[140:143], v[192:195]\n"
"v_mfma_f32_16x16x32_bf16 v[164:167], a[68:71], a[140:143], v[164:167]\n"
"s_cmp_lg_u32 s29, 0\n"
"s_cselect_b32 s31, s75, s72\n"
"v_mfma_f32_16x16x32_bf16 v[164:167], a[64:67], a[136:139], v[164:167]\n"
"s_sub_u32 s52, s52, s72\n"
"s_subb_u32 s53, s53, 0\n"
"v_mfma_f32_16x16x32_bf16 v[168:171], a[64:67], a[144:147], v[168:171]\n"
"s_add_u32 s52, s52, s31\n"
"s_addc_u32 s53, s53, 0\n"
"v_mfma_f32_16x16x32_bf16 v[168:171], a[68:71], a[148:151], v[168:171]\n"
"global_load_dwordx4 a[60:63], v0, s[34:35] offset:3072 sc1\n"
"v_mfma_f32_16x16x32_bf16 v[196:199], a[76:79], a[148:151], v[196:199]\n"
"s_mov_b32 s72, s31\n"
"v_mfma_f32_16x16x32_bf16 v[196:199], a[72:75], a[144:147], v[196:199]\n"
"v_mfma_f32_16x16x32_bf16 a[20:23], a[72:75], a[152:155], a[20:23]\n"
"s_add_u32 s34, s34, 8192\n"
"v_mfma_f32_16x16x32_bf16 a[20:23], a[76:79], a[156:159], a[20:23]\n"
"v_mfma_f32_16x16x32_bf16 a[16:19], a[68:71], a[156:159], a[16:19]\n"
"s_cmp_lg_u32 s29, 0\n"
"s_cselect_b32 s31, s69, s73\n"
"v_mfma_f32_16x16x32_bf16 a[16:19], a[64:67], a[152:155], a[16:19]\n"
"s_sub_u32 s54, s54, s73\n"
"s_subb_u32 s55, s55, 0\n"
"s_waitcnt vmcnt(12)\n"
"v_mfma_f32_16x16x32_bf16 v[200:203], a[80:83], a[96:99], v[200:203]\n"
"s_add_u32 s54, s54, s31\n"
"s_addc_u32 s55, s55, 0\n"
"v_mfma_f32_16x16x32_bf16 v[200:203], a[84:87], a[100:103], v[200:203]\n"
"global_load_dwordx4 a[64:67], v0, s[34:35] offset:-4096 sc1\n"
"v_mfma_f32_16x16x32_bf16 v[228:231], a[92:95], a[100:103], v[228:231]\n"
"s_mov_b32 s73, s31\n"
"ds_read_b128 a[100:103], v5 offset:1024\n"
"v_mfma_f32_16x16x32_bf16 v[228:231], a[88:91], a[96:99], v[228:231]\n"
"ds_read_b128 a[96:99], v5 offset:0\n"
"v_mfma_f32_16x16x32_bf16 v[232:235], a[88:91], a[104:107], v[232:235]\n"
"v_mfma_f32_16x16x32_bf16 v[232:235], a[92:95], a[108:111], v[232:235]\n"
"v_mfma_f32_16x16x32_bf16 v[204:207], a[84:87], a[108:111], v[204:207]\n"
"ds_read_b128 a[108:111], v5 offset:3072\n"
"v_mfma_f32_16x16x32_bf16 v[204:207], a[80:83], a[104:107], v[204:207]\n"
"ds_read_b128 a[104:107], v5 offset:2048\n"
"v_mfma_f32_16x16x32_bf16 v[208:211], a[80:83], a[112:115], v[208:211]\n"
"v_mfma_f32_16x16x32_bf16 v[208:211], a[84:87], a[116:119], v[208:211]\n"
"global_load_dwordx4 a[68:71], v0, s[34:35] offset:-3072 sc1\n"
"v_mfma_f32_16x16x32_bf16 v[236:239], a[92:95], a[116:119], v[236:239]\n"
"ds_read_b128 a[116:119], v5 offset:5120\n"
"v_mfma_f32_16x16x32_bf16 v[236:239], a[88:91], a[112:115], v[236:239]\n"
"ds_read_b128 a[112:115], v5 offset:4096\n"
"v_mfma_f32_16x16x32_bf16 v[240:243], a[88:91], a[120:123], v[240:243]\n"
"v_mfma_f32_16x16x32_bf16 v[240:243], a[92:95], a[124:127], v[240:243]\n"
"v_mfma_f32_16x16x32_bf16 v[212:215], a[84:87], a[124:127], v[212:215]\n"
"ds_read_b128 a[124:127], v5 offset:7168\n"
"v_mfma_f32_16x16x32_bf16 v[212:215], a[80:83], a[120:123], v[212:215]\n"
"ds_read_b128 a[120:123], v5 offset:6144\n"
"v_mfma_f32_16x16x32_bf16 v[216:219], a[80:83], a[128:131], v[216:219]\n"
"v_mfma_f32_16x16x32_bf16 v[216:219], a[84:87], a[132:135], v[216:219]\n"
"global_load_dwordx4 a[72:75], v0, s[34:35] offset:-2048 sc1\n"
"v_mfma_f32_16x16x32_bf16 v[244:247], a[92:95], a[132:135], v[244:247]\n"
"ds_read_b128 a[132:135], v5 offset:9216\n"
"v_mfma_f32_16x16x32_bf16 v[244:247], a[88:91], a[128:131], v[244:247]\n"
"ds_read_b128 a[128:131], v5 offset:8192\n"
"v_mfma_f32_16x16x32_bf16 v[248:251], a[88:91], a[136:139], v[248:251]\n"
"v_mfma_f32_16x16x32_bf16 v[248:251], a[92:95], a[140:143], v[248:251]\n"
"v_mfma_f32_16x16x32_bf16 v[220:223], a[84:87], a[140:143], v[220:223]\n"
"ds_read_b128 a[140:143], v5 offset:11264\n"
"v_mfma_f32_16x16x32_bf16 v[220:223], a[80:83], a[136:139], v[220:223]\n"
"ds_read_b128 a[136:139], v5 offset:10240\n"
"v_mfma_f32_16x16x32_bf16 v[224:227], a[80:83], a[144:147], v[224:227]\n"
"v_mfma_f32_16x16x32_bf16 v[224:227], a[84:87], a[148:151], v[224:227]\n"
"global_load_dwordx4 a[76:79], v0, s[34:35] offset:-1024 sc1\n"
"v_mfma_f32_16x16x32_bf16 v[252:255], a[92:95], a[148:151], v[252:255]\n"
"ds_read_b128 a[148:151], v5 offset:13312\n"
"v_mfma_f32_16x16x32_bf16 v[252:255], a[88:91], a[144:147], v[252:255]\n"
"ds_read_b128 a[144:147], v5 offset:12288\n"
"v_mfma_f32_16x16x32_bf16 a[28:31], a[88:91], a[152:155], a[28:31]\n"
"v_mfma_f32_16x16x32_bf16 a[28:31], a[92:95], a[156:159], a[28:31]\n"
"v_mfma_f32_16x16x32_bf16 a[24:27], a[84:87], a[156:159], a[24:27]\n"
"ds_read_b128 a[156:159], v5 offset:15360\n"
"v_mfma_f32_16x16x32_bf16 a[24:27], a[80:83], a[152:155], a[24:27]\n"
"s_waitcnt vmcnt(9) lgkmcnt(11)\n"
"ds_read_b128 a[152:155], v5 offset:14336\n"
"v_mfma_f32_16x16x32_bf16 v[32:35], a[32:35], a[96:99], v[32:35]\n"
"v_mfma_f32_16x16x32_bf16 v[32:35], a[36:39], a[100:103], v[32:35]\n"
"global_load_dwordx4 a[80:83], v0, s[34:35] offset:0 sc1\n"
"v_mfma_f32_16x16x32_bf16 v[60:63], a[44:47], a[100:103], v[60:63]\n"
"v_mfma_f32_16x16x32_bf16 v[60:63], a[40:43], a[96:99], v[60:63]\n"
"v_mfma_f32_16x16x32_bf16 v[64:67], a[40:43], a[104:107], v[64:67]\n"
"v_mfma_f32_16x16x32_bf16 v[64:67], a[44:47], a[108:111], v[64:67]\n"
"v_mfma_f32_16x16x32_bf16 v[36:39], a[36:39], a[108:111], v[36:39]\n"
"v_mfma_f32_16x16x32_bf16 v[36:39], a[32:35], a[104:107], v[36:39]\n"
"s_waitcnt lgkmcnt(8)\n"
"v_mfma_f32_16x16x32_bf16 v[40:43], a[32:35], a[112:115], v[40:43]\n"
"v_mfma_f32_16x16x32_bf16 v[40:43], a[36:39], a[116:119], v[40:43]\n"
"global_load_dwordx4 a[84:87], v0, s[34:35] offset:1024 sc1\n"
"v_mfma_f32_16x16x32_bf16 v[68:71], a[44:47], a[116:119], v[68:71]\n"
"v_mfma_f32_16x16x32_bf16 v[68:71], a[40:43], a[112:115], v[68:71]\n"
"v_mfma_f32_16x16x32_bf16 v[72:75], a[40:43], a[120:123], v[72:75]\n"
"v_mfma_f32_16x16x32_bf16 v[72:75], a[44:47], a[124:127], v[72:75]\n"
"v_mfma_f32_16x16x32_bf16 v[44:47], a[36:39], a[124:127], v[44:47]\n"
"v_mfma_f32_16x16x32_bf16 v[44:47], a[32:35], a[120:123], v[44:47]\n"
"s_waitcnt lgkmcnt(4)\n"
"v_mfma_f32_16x16x32_bf16 v[48:51], a[32:35], a[128:131], v[48:51]\n"
"s_lshr_b32 s66, 20, s62\n"
"v_mfma_f32_16x16x32_bf16 v[48:51], a[36:39], a[132:135], v[48:51]\n"
"global_load_dwordx4 a[88:91], v0, s[34:35] offset:2048 sc1\n"
"v_mfma_f32_16x16x32_bf16 v[76:79], a[44:47], a[132:135], v[76:79]\n"
"s_and_b32 s66, s66, 1\n"
"s_cselect_b32 s70, 4294574080, 0\n"
"s_cselect_b32 s71, -1, 0\n"
"s_cselect_b32 s66, 0, 131072\n"
"v_mfma_f32_16x16x32_bf16 v[76:79], a[40:43], a[128:131], v[76:79]\n"
"v_mfma_f32_16x16x32_bf16 v[80:83], a[40:43], a[136:139], v[80:83]\n"
"s_add_u32 s70, s70, s66\n"
"v_mfma_f32_16x16x32_bf16 v[80:83], a[44:47], a[140:143], v[80:83]\n"
"v_mfma_f32_16x16x32_bf16 v[52:55], a[36:39], a[140:143], v[52:55]\n"
"v_mfma_f32_16x16x32_bf16 v[52:55], a[32:35], a[136:139], v[52:55]\n"
"s_waitcnt lgkmcnt(0)\n"
"v_mfma_f32_16x16x32_bf16 v[56:59], a[32:35], a[144:147], v[56:59]\n"
"v_mfma_f32_16x16x32_bf16 v[56:59], a[36:39], a[148:151], v[56:59]\n"
"global_load_dwordx4 a[92:95], v0, s[34:35] offset:3072 sc1\n"
"v_mfma_f32_16x16x32_bf16 v[84:87], a[44:47], a[148:151], v[84:87]\n"
"v_mfma_f32_16x16x32_bf16 v[84:87], a[40:43], a[144:147], v[84:87]\n"
"v_mfma_f32_16x16x32_bf16 a[4:7], a[40:43], a[152:155], a[4:7]\n"
"s_add_u32 s34, s34, 8192\n"
"v_mfma_f32_16x16x32_bf16 a[4:7], a[44:47], a[156:159], a[4:7]\n"
"v_mfma_f32_16x16x32_bf16 a[0:3], a[36:39], a[156:159], a[0:3]\n"
"v_mfma_f32_16x16x32_bf16 a[0:3], a[32:35], a[152:155], a[0:3]\n"
"s_waitcnt vmcnt(8)\n"
"v_mfma_f32_16x16x32_bf16 v[88:91], a[48:51], a[96:99], v[88:91]\n"
"v_mfma_f32_16x16x32_bf16 v[88:91], a[52:55], a[100:103], v[88:91]\n"
"global_load_dwordx4 a[32:35], v0, s[34:35] offset:-4096 sc1\n"
"v_mfma_f32_16x16x32_bf16 v[116:119], a[60:63], a[100:103], v[116:119]\n"
"v_mfma_f32_16x16x32_bf16 v[116:119], a[56:59], a[96:99], v[116:119]\n"
"v_mfma_f32_16x16x32_bf16 v[120:123], a[56:59], a[104:107], v[120:123]\n"
"v_mfma_f32_16x16x32_bf16 v[120:123], a[60:63], a[108:111], v[120:123]\n"
"v_mfma_f32_16x16x32_bf16 v[92:95], a[52:55], a[108:111], v[92:95]\n"
"v_mfma_f32_16x16x32_bf16 v[92:95], a[48:51], a[104:107], v[92:95]\n"
"v_mfma_f32_16x16x32_bf16 v[96:99], a[48:51], a[112:115], v[96:99]\n"
"v_mfma_f32_16x16x32_bf16 v[96:99], a[52:55], a[116:119], v[96:99]\n"
"global_load_dwordx4 a[36:39], v0, s[34:35] offset:-3072 sc1\n"
"v_mfma_f32_16x16x32_bf16 v[124:127], a[60:63], a[116:119], v[124:127]\n"
"buffer_load_dwordx4 v[12:15], v0, s[56:59], %25 offen sc1\n"
"v_mfma_f32_16x16x32_bf16 v[124:127], a[56:59], a[112:115], v[124:127]\n"
"v_mfma_f32_16x16x32_bf16 v[128:131], a[56:59], a[120:123], v[128:131]\n"
"v_mfma_f32_16x16x32_bf16 v[128:131], a[60:63], a[124:127], v[128:131]\n"
"v_mfma_f32_16x16x32_bf16 v[100:103], a[52:55], a[124:127], v[100:103]\n"
"v_mfma_f32_16x16x32_bf16 v[100:103], a[48:51], a[120:123], v[100:103]\n"
"v_mfma_f32_16x16x32_bf16 v[104:107], a[48:51], a[128:131], v[104:107]\n"
"v_mfma_f32_16x16x32_bf16 v[104:107], a[52:55], a[132:135], v[104:107]\n"
"global_load_dwordx4 a[40:43], v0, s[34:35] offset:-2048 sc1\n"
"v_mfma_f32_16x16x32_bf16 v[132:135], a[60:63], a[132:135], v[132:135]\n"
"v_mfma_f32_16x16x32_bf16 v[132:135], a[56:59], a[128:131], v[132:135]\n"
"v_mfma_f32_16x16x32_bf16 v[136:139], a[56:59], a[136:139], v[136:139]\n"
"v_mfma_f32_16x16x32_bf16 v[136:139], a[60:63], a[140:143], v[136:139]\n"
"v_mfma_f32_16x16x32_bf16 v[108:111], a[52:55], a[140:143], v[108:111]\n"
"v_mfma_f32_16x16x32_bf16 v[108:111], a[48:51], a[136:139], v[108:111]\n"
"v_mfma_f32_16x16x32_bf16 v[112:115], a[48:51], a[144:147], v[112:115]\n"
"v_mfma_f32_16x16x32_bf16 v[112:115], a[52:55], a[148:151], v[112:115]\n"
"global_load_dwordx4 a[44:47], v0, s[34:35] offset:-1024 sc1\n"
"v_mfma_f32_16x16x32_bf16 v[140:143], a[60:63], a[148:151], v[140:143]\n"
"v_mfma_f32_16x16x32_bf16 v[140:143], a[56:59], a[144:147], v[140:143]\n"
"v_mfma_f32_16x16x32_bf16 a[12:15], a[56:59], a[152:155], a[12:15]\n"
"v_mfma_f32_16x16x32_bf16 a[12:15], a[60:63], a[156:159], a[12:15]\n"
"v_mfma_f32_16x16x32_bf16 a[8:11], a[52:55], a[156:159], a[8:11]\n"
"v_mfma_f32_16x16x32_bf16 a[8:11], a[48:51], a[152:155], a[8:11]\n"
"s_waitcnt vmcnt(9)\n"
"v_mfma_f32_16x16x32_bf16 v[144:147], a[64:67], a[96:99], v[144:147]\n"
"v_mfma_f32_16x16x32_bf16 v[144:147], a[68:71], a[100:103], v[144:147]\n"
"global_load_dwordx4 a[48:51], v0, s[34:35] offset:0 sc1\n"
"v_mfma_f32_16x16x32_bf16 v[172:175], a[76:79], a[100:103], v[172:175]\n"
"v_mfma_f32_16x16x32_bf16 v[172:175], a[72:75], a[96:99], v[172:175]\n"
"v_mfma_f32_16x16x32_bf16 v[176:179], a[72:75], a[104:107], v[176:179]\n"
"v_mfma_f32_16x16x32_bf16 v[176:179], a[76:79], a[108:111], v[176:179]\n"
"v_mfma_f32_16x16x32_bf16 v[148:151], a[68:71], a[108:111], v[148:151]\n"
"v_mfma_f32_16x16x32_bf16 v[148:151], a[64:67], a[104:107], v[148:151]\n"
"v_mfma_f32_16x16x32_bf16 v[152:155], a[64:67], a[112:115], v[152:155]\n"
"v_mfma_f32_16x16x32_bf16 v[152:155], a[68:71], a[116:119], v[152:155]\n"
"global_load_dwordx4 a[52:55], v0, s[34:35] offset:1024 sc1\n"
"v_mfma_f32_16x16x32_bf16 v[180:183], a[76:79], a[116:119], v[180:183]\n"
"v_mfma_f32_16x16x32_bf16 v[180:183], a[72:75], a[112:115], v[180:183]\n"
"v_mfma_f32_16x16x32_bf16 v[184:187], a[72:75], a[120:123], v[184:187]\n"
"v_mfma_f32_16x16x32_bf16 v[184:187], a[76:79], a[124:127], v[184:187]\n"
"v_mfma_f32_16x16x32_bf16 v[156:159], a[68:71], a[124:127], v[156:159]\n"
"v_mfma_f32_16x16x32_bf16 v[156:159], a[64:67], a[120:123], v[156:159]\n"
"v_mfma_f32_16x16x32_bf16 v[160:163], a[64:67], a[128:131], v[160:163]\n"
"v_mfma_f32_16x16x32_bf16 v[160:163], a[68:71], a[132:135], v[160:163]\n"
"global_load_dwordx4 a[56:59], v0, s[34:35] offset:2048 sc1\n"
"v_mfma_f32_16x16x32_bf16 v[188:191], a[76:79], a[132:135], v[188:191]\n"
"v_mfma_f32_16x16x32_bf16 v[188:191], a[72:75], a[128:131], v[188:191]\n"
"v_mfma_f32_16x16x32_bf16 v[192:195], a[72:75], a[136:139], v[192:195]\n"
"v_mfma_f32_16x16x32_bf16 v[192:195], a[76:79], a[140:143], v[192:195]\n"
"v_mfma_f32_16x16x32_bf16 v[164:167], a[68:71], a[140:143], v[164:167]\n"
"s_add_u32 s67, s78, s62\n"
"v_mfma_f32_16x16x32_bf16 v[164:167], a[64:67], a[136:139], v[164:167]\n"
"v_mfma_f32_16x16x32_bf16 v[168:171], a[64:67], a[144:147], v[168:171]\n"
"s_add_u32 s67, s67, 1\n"
"v_mfma_f32_16x16x32_bf16 v[168:171], a[68:71], a[148:151], v[168:171]\n"
"global_load_dwordx4 a[60:63], v0, s[34:35] offset:3072 sc1\n"
"v_mfma_f32_16x16x32_bf16 v[196:199], a[76:79], a[148:151], v[196:199]\n"
"v_mfma_f32_16x16x32_bf16 v[196:199], a[72:75], a[144:147], v[196:199]\n"
"v_mfma_f32_16x16x32_bf16 a[20:23], a[72:75], a[152:155], a[20:23]\n"
"s_add_u32 s34, s34, 8192\n"
"v_mfma_f32_16x16x32_bf16 a[20:23], a[76:79], a[156:159], a[20:23]\n"
"v_mfma_f32_16x16x32_bf16 a[16:19], a[68:71], a[156:159], a[16:19]\n"
"v_mfma_f32_16x16x32_bf16 a[16:19], a[64:67], a[152:155], a[16:19]\n"
"s_waitcnt vmcnt(9)\n"
"v_mfma_f32_16x16x32_bf16 v[200:203], a[80:83], a[96:99], v[200:203]\n"
"v_mfma_f32_16x16x32_bf16 v[200:203], a[84:87], a[100:103], v[200:203]\n"
"global_load_dwordx4 a[64:67], v0, s[34:35] offset:-4096 sc1\n"
"v_mfma_f32_16x16x32_bf16 v[228:231], a[92:95], a[100:103], v[228:231]\n"
"ds_read_b128 a[100:103], v2 offset:17408\n"
"v_mfma_f32_16x16x32_bf16 v[228:231], a[88:91], a[96:99], v[228:231]\n"
"ds_read_b128 a[96:99], v2 offset:16384\n"
"v_mfma_f32_16x16x32_bf16 v[232:235], a[88:91], a[104:107], v[232:235]\n"
"v_mfma_f32_16x16x32_bf16 v[232:235], a[92:95], a[108:111], v[232:235]\n"
"v_mfma_f32_16x16x32_bf16 v[204:207], a[84:87], a[108:111], v[204:207]\n"
"ds_read_b128 a[108:111], v2 offset:19456\n"
"v_mfma_f32_16x16x32_bf16 v[204:207], a[80:83], a[104:107], v[204:207]\n"
"ds_read_b128 a[104:107], v2 offset:18432\n"
"v_mfma_f32_16x16x32_bf16 v[208:211], a[80:83], a[112:115], v[208:211]\n"
"v_mfma_f32_16x16x32_bf16 v[208:211], a[84:87], a[116:119], v[208:211]\n"
"global_load_dwordx4 a[68:71], v0, s[34:35] offset:-3072 sc1\n"
"v_mfma_f32_16x16x32_bf16 v[236:239], a[92:95], a[116:119], v[236:239]\n"
"ds_read_b128 a[116:119], v2 offset:21504\n"
"v_mfma_f32_16x16x32_bf16 v[236:239], a[88:91], a[112:115], v[236:239]\n"
"ds_read_b128 a[112:115], v2 offset:20480\n"
"v_mfma_f32_16x16x32_bf16 v[240:243], a[88:91], a[120:123], v[240:243]\n"
"v_mfma_f32_16x16x32_bf16 v[240:243], a[92:95], a[124:127], v[240:243]\n"
"v_mfma_f32_16x16x32_bf16 v[212:215], a[84:87], a[124:127], v[212:215]\n"
"ds_read_b128 a[124:127], v2 offset:23552\n"
"v_mfma_f32_16x16x32_bf16 v[212:215], a[80:83], a[120:123], v[212:215]\n"
"ds_read_b128 a[120:123], v2 offset:22528\n"
"v_mfma_f32_16x16x32_bf16 v[216:219], a[80:83], a[128:131], v[216:219]\n"
"v_mfma_f32_16x16x32_bf16 v[216:219], a[84:87], a[132:135], v[216:219]\n"
"global_load_dwordx4 a[72:75], v0, s[34:35] offset:-2048 sc1\n"
"v_mfma_f32_16x16x32_bf16 v[244:247], a[92:95], a[132:135], v[244:247]\n"
"ds_read_b128 a[132:135], v2 offset:25600\n"
"v_mfma_f32_16x16x32_bf16 v[244:247], a[88:91], a[128:131], v[244:247]\n"
"ds_read_b128 a[128:131], v2 offset:24576\n"
"v_mfma_f32_16x16x32_bf16 v[248:251], a[88:91], a[136:139], v[248:251]\n"
"v_mfma_f32_16x16x32_bf16 v[248:251], a[92:95], a[140:143], v[248:251]\n"
"v_mfma_f32_16x16x32_bf16 v[220:223], a[84:87], a[140:143], v[220:223]\n"
"ds_read_b128 a[140:143], v2 offset:27648\n"
"v_mfma_f32_16x16x32_bf16 v[220:223], a[80:83], a[136:139], v[220:223]\n"
"ds_read_b128 a[136:139], v2 offset:26624\n"
"v_mfma_f32_16x16x32_bf16 v[224:227], a[80:83], a[144:147], v[224:227]\n"
"v_mfma_f32_16x16x32_bf16 v[224:227], a[84:87], a[148:151], v[224:227]\n"
"global_load_dwordx4 a[76:79], v0, s[34:35] offset:-1024 sc1\n"
"v_mfma_f32_16x16x32_bf16 v[252:255], a[92:95], a[148:151], v[252:255]\n"
"ds_read_b128 a[148:151], v2 offset:29696\n"
"v_mfma_f32_16x16x32_bf16 v[252:255], a[88:91], a[144:147], v[252:255]\n"
"ds_read_b128 a[144:147], v2 offset:28672\n"
"v_mfma_f32_16x16x32_bf16 a[28:31], a[88:91], a[152:155], a[28:31]\n"
"v_mfma_f32_16x16x32_bf16 a[28:31], a[92:95], a[156:159], a[28:31]\n"
"v_mfma_f32_16x16x32_bf16 a[24:27], a[84:87], a[156:159], a[24:27]\n"
"ds_read_b128 a[156:159], v2 offset:31744\n"
"v_mfma_f32_16x16x32_bf16 a[24:27], a[80:83], a[152:155], a[24:27]\n"
"ds_read_b128 a[152:155], v2 offset:30720\n"
"s_waitcnt vmcnt(8) lgkmcnt(12)\n"
"v_mfma_f32_16x16x32_bf16 v[32:35], a[32:35], a[96:99], v[32:35]\n"
"v_mfma_f32_16x16x32_bf16 v[32:35], a[36:39], a[100:103], v[32:35]\n"
"global_load_dwordx4 a[80:83], v0, s[34:35] offset:0 sc1\n"
"v_mfma_f32_16x16x32_bf16 v[60:63], a[44:47], a[100:103], v[60:63]\n"
"s_barrier\n"
"v_mfma_f32_16x16x32_bf16 v[60:63], a[40:43], a[96:99], v[60:63]\n"
"s_add_u32 s48, s48, s70\n"
"s_addc_u32 s49, s49, s71\n"
"v_mfma_f32_16x16x32_bf16 v[64:67], a[40:43], a[104:107], v[64:67]\n"
"v_mfma_f32_16x16x32_bf16 v[64:67], a[44:47], a[108:111], v[64:67]\n"
"v_mfma_f32_16x16x32_bf16 v[36:39], a[36:39], a[108:111], v[36:39]\n"
"s_add_u32 s52, s52, s70\n"
"s_addc_u32 s53, s53, s71\n"
"v_subrev_u32 v12, s67, v12\n"
"v_mfma_f32_16x16x32_bf16 v[36:39], a[32:35], a[104:107], v[36:39]\n"
"s_waitcnt lgkmcnt(8)\n"
"v_xor_b32 v13, s79, v13\n"
"v_mfma_f32_16x16x32_bf16 v[40:43], a[32:35], a[112:115], v[40:43]\n"
"s_add_u32 s54, s54, s70\n"
"s_addc_u32 s55, s55, s71\n"
"v_subrev_u32 v14, s67, v14\n"
"v_mfma_f32_16x16x32_bf16 v[40:43], a[36:39], a[116:119], v[40:43]\n"
"global_load_dwordx4 a[84:87], v0, s[34:35] offset:1024 sc1\n"
"v_xor_b32 v15, s79, v15\n"
"v_mfma_f32_16x16x32_bf16 v[68:71], a[44:47], a[116:119], v[68:71]\n"
"v_or_b32 v12, v12, v14\n"
"v_mfma_f32_16x16x32_bf16 v[68:71], a[40:43], a[112:115], v[68:71]\n"
"v_or_b32 v13, v13, v15\n"
"v_mfma_f32_16x16x32_bf16 v[72:75], a[40:43], a[120:123], v[72:75]\n"
"v_or_b32 v12, v12, v13\n"
"v_mfma_f32_16x16x32_bf16 v[72:75], a[44:47], a[124:127], v[72:75]\n"
"s_add_u32 s61, s61, 2048\n"
"v_cmp_lt_u32 vcc, 15, v12\n"
"v_mfma_f32_16x16x32_bf16 v[44:47], a[36:39], a[124:127], v[44:47]\n"
"s_cbranch_vccnz L_slow2\n"
"L_back2:\n"
"v_mfma_f32_16x16x32_bf16 v[44:47], a[32:35], a[120:123], v[44:47]\n"
"s_waitcnt lgkmcnt(4)\n"
"v_mfma_f32_16x16x32_bf16 v[48:51], a[32:35], a[128:131], v[48:51]\n"
"v_lshlrev_b32 v16, 16, v18\n"
"v_mfma_f32_16x16x32_bf16 v[48:51], a[36:39], a[132:135], v[48:51]\n"
"global_load_dwordx4 a[88:91], v0, s[34:35] offset:2048 sc1\n"
"v_and_b32 v17, s63, v18\n"
"v_mfma_f32_16x16x32_bf16 v[76:79], a[44:47], a[132:135], v[76:79]\n"
"v_lshlrev_b32 v18, 16, v19\n"
"v_mfma_f32_16x16x32_bf16 v[76:79], a[40:43], a[128:131], v[76:79]\n"
"v_and_b32 v19, s63, v19\n"
"v_mfma_f32_16x16x32_bf16 v[80:83], a[40:43], a[136:139], v[80:83]\n"
"v_add_f32 v144, v144, v16\n"
"v_add_f32 v145, v145, v17\n"
"v_mfma_f32_16x16x32_bf16 v[80:83], a[44:47], a[140:143], v[80:83]\n"
"v_add_f32 v146, v146, v18\n"
"v_mfma_f32_16x16x32_bf16 v[52:55], a[36:39], a[140:143], v[52:55]\n"
"v_add_f32 v147, v147, v19\n"
"v_mfma_f32_16x16x32_bf16 v[52:55], a[32:35], a[136:139], v[52:55]\n"
"s_waitcnt lgkmcnt(0)\n"
"v_add_f32 v148, v148, v16\n"
"v_mfma_f32_16x16x32_bf16 v[56:59], a[32:35], a[144:147], v[56:59]\n"
"v_add_f32 v149, v149, v17\n"
"v_mfma_f32_16x16x32_bf16 v[56:59], a[36:39], a[148:151], v[56:59]\n"
"global_load_dwordx4 a[92:95], v0, s[34:35] offset:3072 sc1\n"
"v_add_f32 v150, v150, v18\n"
"v_mfma_f32_16x16x32_bf16 v[84:87], a[44:47], a[148:151], v[84:87]\n"
"v_add_f32 v151, v151, v19\n"
"v_mfma_f32_16x16x32_bf16 v[84:87], a[40:43], a[144:147], v[84:87]\n"
"v_add_f32 v152, v152, v16\n"
"v_mfma_f32_16x16x32_bf16 a[4:7], a[40:43], a[152:155], a[4:7]\n"
"s_add_u32 s34, s34, 8192\n"
"v_add_f32 v153, v153, v17\n"
"v_mfma_f32_16x16x32_bf16 a[4:7], a[44:47], a[156:159], a[4:7]\n"
"v_add_f32 v154, v154, v18\n"
"v_mfma_f32_16x16x32_bf16 a[0:3], a[36:39], a[156:159], a[0:3]\n"
"v_add_f32 v155, v155, v19\n"
"v_add_f32 v156, v156, v16\n"
"v_mfma_f32_16x16x32_bf16 a[0:3], a[32:35], a[152:155], a[0:3]\n"
"s_waitcnt vmcnt(8)\n"
"v_add_f32 v157, v157, v17\n"
"v_mfma_f32_16x16x32_bf16 v[88:91], a[48:51], a[96:99], v[88:91]\n"
"v_add_f32 v158, v158, v18\n"
"v_mfma_f32_16x16x32_bf16 v[88:91], a[52:55], a[100:103], v[88:91]\n"
"global_load_dwordx4 a[32:35], v0, s[34:35] offset:-4096 sc1\n"
"v_add_f32 v159, v159, v19\n"
"v_mfma_f32_16x16x32_bf16 v[116:119], a[60:63], a[100:103], v[116:119]\n"
"ds_read_b128 a[100:103], v3 offset:17408\n"
"v_add_f32 v160, v160, v16\n"
"v_mfma_f32_16x16x32_bf16 v[116:119], a[56:59], a[96:99], v[116:119]\n"
"ds_read_b128 a[96:99], v3 offset:16384\n"
"v_add_f32 v161, v161, v17\n"
"v_mfma_f32_16x16x32_bf16 v[120:123], a[56:59], a[104:107], v[120:123]\n"
"v_add_f32 v162, v162, v18\n"
"v_mfma_f32_16x16x32_bf16 v[120:123], a[60:63], a[108:111], v[120:123]\n"
"v_add_f32 v163, v163, v19\n"
"v_mfma_f32_16x16x32_bf16 v[92:95], a[52:55], a[108:111], v[92:95]\n"
"ds_read_b128 a[108:111], v3 offset:19456\n"
"v_add_f32 v164, v164, v16\n"
"v_mfma_f32_16x16x32_bf16 v[92:95], a[48:51], a[104:107], v[92:95]\n"
"ds_read_b128 a[104:107], v3 offset:18432\n"
"v_add_f32 v165, v165, v17\n"
"v_mfma_f32_16x16x32_bf16 v[96:99], a[48:51], a[112:115], v[96:99]\n"
"v_add_f32 v166, v166, v18\n"
"v_add_f32 v167, v167, v19\n"
"v_mfma_f32_16x16x32_bf16 v[96:99], a[52:55], a[116:119], v[96:99]\n"
"global_load_dwordx4 a[36:39], v0, s[34:35] offset:-3072 sc1\n"
"v_add_f32 v168, v168, v16\n"
"v_mfma_f32_16x16x32_bf16 v[124:127], a[60:63], a[116:119], v[124:127]\n"
"ds_read_b128 a[116:119], v3 offset:21504\n"
"v_add_f32 v169, v169, v17\n"
"v_mfma_f32_16x16x32_bf16 v[124:127], a[56:59], a[112:115], v[124:127]\n"
"ds_read_b128 a[112:115], v3 offset:20480\n"
"v_add_f32 v170, v170, v18\n"
"v_mfma_f32_16x16x32_bf16 v[128:131], a[56:59], a[120:123], v[128:131]\n"
"v_add_f32 v171, v171, v19\n"
"v_mfma_f32_16x16x32_bf16 v[128:131], a[60:63], a[124:127], v[128:131]\n"
"v_accvgpr_read_b32 v12, a16\n"
"v_mfma_f32_16x16x32_bf16 v[100:103], a[52:55], a[124:127], v[100:103]\n"
"ds_read_b128 a[124:127], v3 offset:23552\n"
"v_accvgpr_read_b32 v13, a17\n"
"v_mfma_f32_16x16x32_bf16 v[100:103], a[48:51], a[120:123], v[100:103]\n"
"ds_read_b128 a[120:123], v3 offset:22528\n"
"v_accvgpr_read_b32 v14, a18\n"
"v_mfma_f32_16x16x32_bf16 v[104:107], a[48:51], a[128:131], v[104:107]\n"
"v_accvgpr_read_b32 v15, a19\n"
"v_add_f32 v12, v12, v16\n"
"v_mfma_f32_16x16x32_bf16 v[104:107], a[52:55], a[132:135], v[104:107]\n"
"global_load_dwordx4 a[40:43], v0, s[34:35] offset:-2048 sc1\n"
"v_add_f32 v13, v13, v17\n"
"v_mfma_f32_16x16x32_bf16 v[132:135], a[60:63], a[132:135], v[132:135]\n"
"ds_read_b128 a[132:135], v3 offset:25600\n"
"v_add_f32 v14, v14, v18\n"
"v_mfma_f32_16x16x32_bf16 v[132:135], a[56:59], a[128:131], v[132:135]\n"
"ds_read_b128 a[128:131], v3 offset:24576\n"
"v_add_f32 v15, v15, v19\n"
"v_mfma_f32_16x16x32_bf16 v[136:139], a[56:59], a[136:139], v[136:139]\n"
"v_accvgpr_write_b32 a16, v12\n"
"v_mfma_f32_16x16x32_bf16 v[136:139], a[60:63], a[140:143], v[136:139]\n"
"v_accvgpr_write_b32 a17, v13\n"
"v_mfma_f32_16x16x32_bf16 v[108:111], a[52:55], a[140:143], v[108:111]\n"
"ds_read_b128 a[140:143], v3 offset:27648\n"
"v_accvgpr_write_b32 a18, v14\n"
"v_mfma_f32_16x16x32_bf16 v[108:111], a[48:51], a[136:139], v[108:111]\n"
"ds_read_b128 a[136:139], v3 offset:26624\n"
"v_accvgpr_write_b32 a19, v15\n"
"v_mfma_f32_16x16x32_bf16 v[112:115], a[48:51], a[144:147], v[112:115]\n"
"L_st7944:\n"
"s_cbranch_execz L_st9164\n"
"v_lshlrev_b32 v20, 16, v22\n"
"v_and_b32 v21, s63, v22\n"
"v_mfma_f32_16x16x32_bf16 v[112:115], a[52:55], a[148:151], v[112:115]\n"
"global_load_dwordx4 a[44:47], v0, s[34:35] offset:-1024 sc1\n"
"v_lshlrev_b32 v22, 16, v23\n"
"v_mfma_f32_16x16x32_bf16 v[140:143], a[60:63], a[148:151], v[140:143]\n"
"ds_read_b128 a[148:151], v3 offset:29696\n"
"v_and_b32 v23, s63, v23\n"
"v_mfma_f32_16x16x32_bf16 v[140:143], a[56:59], a[144:147], v[140:143]\n"
"ds_read_b128 a[144:147], v3 offset:28672\n"
"v_add_f32 v172, v172, v20\n"
"v_mfma_f32_16x16x32_bf16 a[12:15], a[56:59], a[152:155], a[12:15]\n"
"v_add_f32 v173, v173, v21\n"
"v_mfma_f32_16x16x32_bf16 a[12:15], a[60:63], a[156:159], a[12:15]\n"
"v_add_f32 v174, v174, v22\n"
"v_mfma_f32_16x16x32_bf16 a[8:11], a[52:55], a[156:159], a[8:11]\n"
"ds_read_b128 a[156:159], v3 offset:31744\n"
"v_add_f32 v175, v175, v23\n"
"v_mfma_f32_16x16x32_bf16 a[8:11], a[48:51], a[152:155], a[8:11]\n"
"s_waitcnt vmcnt(8) lgkmcnt(11)\n"
"ds_read_b128 a[152:155], v3 offset:30720\n"
"v_add_f32 v176, v176, v20\n"
"v_mfma_f32_16x16x32_bf16 v[32:35], a[64:67], a[96:99], v[32:35]\n"
"v_add_f32 v177, v177, v21\n"
"v_mfma_f32_16x16x32_bf16 v[32:35], a[68:71], a[100:103], v[32:35]\n"
"global_load_dwordx4 a[48:51], v0, s[34:35] offset:0 sc1\n"
"v_add_f32 v178, v178, v22\n"
"v_mfma_f32_16x16x32_bf16 v[60:63], a[76:79], a[100:103], v[60:63]\n"
"v_add_f32 v179, v179, v23\n"
"v_mfma_f32_16x16x32_bf16 v[60:63], a[72:75], a[96:99], v[60:63]\n"
"v_add_f32 v180, v180, v20\n"
"v_add_f32 v181, v181, v21\n"
"v_mfma_f32_16x16x32_bf16 v[64:67], a[72:75], a[104:107], v[64:67]\n"
"v_add_f32 v182, v182, v22\n"
"v_mfma_f32_16x16x32_bf16 v[64:67], a[76:79], a[108:111], v[64:67]\n"
"v_add_f32 v183, v183, v23\n"
"v_mfma_f32_16x16x32_bf16 v[36:39], a[68:71], a[108:111], v[36:39]\n"
"v_add_f32 v184, v184, v20\n"
"v_mfma_f32_16x16x32_bf16 v[36:39], a[64:67], a[104:107], v[36:39]\n"
"s_waitcnt lgkmcnt(8)\n"
"v_add_f32 v185, v185, v21\n"
"v_mfma_f32_16x16x32_bf16 v[40:43], a[64:67], a[112:115], v[40:43]\n"
"v_add_f32 v186, v186, v22\n"
"v_mfma_f32_16x16x32_bf16 v[40:43], a[68:71], a[116:119], v[40:43]\n"
"global_load_dwordx4 a[52:55], v0, s[34:35] offset:1024 sc1\n"
"v_add_f32 v187, v187, v23\n"
"v_mfma_f32_16x16x32_bf16 v[68:71], a[76:79], a[116:119], v[68:71]\n"
"v_add_f32 v188, v188, v20\n"
"v_mfma_f32_16x16x32_bf16 v[68:71], a[72:75], a[112:115], v[68:71]\n"
"v_add_f32 v189, v189, v21\n"
"v_mfma_f32_16x16x32_bf16 v[72:75], a[72:75], a[120:123], v[72:75]\n"
"v_add_f32 v190, v190, v22\n"
"v_add_f32 v191, v191, v23\n"
"v_mfma_f32_16x16x32_bf16 v[72:75], a[76:79], a[124:127], v[72:75]\n"
"v_add_f32 v192, v192, v20\n"
"v_mfma_f32_16x16x32_bf16 v[44:47], a[68:71], a[124:127], v[44:47]\n"
"v_add_f32 v193, v193, v21\n"
"v_mfma_f32_16x16x32_bf16 v[44:47], a[64:67], a[120:123], v[44:47]\n"
"s_waitcnt lgkmcnt(4)\n"
"v_add_f32 v194, v194, v22\n"
"v_mfma_f32_16x16x32_bf16 v[48:51], a[64:67], a[128:131], v[48:51]\n"
"v_add_f32 v195, v195, v23\n"
"v_mfma_f32_16x16x32_bf16 v[48:51], a[68:71], a[132:135], v[48:51]\n"
"global_load_dwordx4 a[56:59], v0, s[34:35] offset:2048 sc1\n"
"v_add_f32 v196, v196, v20\n"
"v_mfma_f32_16x16x32_bf16 v[76:79], a[76:79], a[132:135], v[76:79]\n"
"v_add_f32 v197, v197, v21\n"
"v_mfma_f32_16x16x32_bf16 v[76:79], a[72:75], a[128:131], v[76:79]\n"
"v_add_f32 v198, v198, v22\n"
"v_mfma_f32_16x16x32_bf16 v[80:83], a[72:75], a[136:139], v[80:83]\n"
"v_add_f32 v199, v199, v23\n"
"v_mfma_f32_16x16x32_bf16 v[80:83], a[76:79], a[140:143], v[80:83]\n"
"v_accvgpr_read_b32 v12, a20\n"
"v_mfma_f32_16x16x32_bf16 v[52:55], a[68:71], a[140:143], v[52:55]\n"
"v_accvgpr_read_b32 v13, a21\n"
"v_accvgpr_read_b32 v14, a22\n"
"v_mfma_f32_16x16x32_bf16 v[52:55], a[64:67], a[136:139], v[52:55]\n"
"s_waitcnt lgkmcnt(0)\n"
"v_accvgpr_read_b32 v15, a23\n"
"v_mfma_f32_16x16x32_bf16 v[56:59], a[64:67], a[144:147], v[56:59]\n"
"v_add_f32 v12, v12, v20\n"
"v_mfma_f32_16x16x32_bf16 v[56:59], a[68:71], a[148:151], v[56:59]\n"
"global_load_dwordx4 a[60:63], v0, s[34:35] offset:3072 sc1\n"
"v_add_f32 v13, v13, v21\n"
"v_mfma_f32_16x16x32_bf16 v[84:87], a[76:79], a[148:151], v[84:87]\n"
"v_add_f32 v14, v14, v22\n"
"v_mfma_f32_16x16x32_bf16 v[84:87], a[72:75], a[144:147], v[84:87]\n"
"v_add_f32 v15, v15, v23\n"
"v_mfma_f32_16x16x32_bf16 a[4:7], a[72:75], a[152:155], a[4:7]\n"
"s_add_u32 s34, s34, 8192\n"
"v_accvgpr_write_b32 a20, v12\n"
"v_mfma_f32_16x16x32_bf16 a[4:7], a[76:79], a[156:159], a[4:7]\n"
"v_accvgpr_write_b32 a21, v13\n"
"v_mfma_f32_16x16x32_bf16 a[0:3], a[68:71], a[156:159], a[0:3]\n"
"v_accvgpr_write_b32 a22, v14\n"
"v_mfma_f32_16x16x32_bf16 a[0:3], a[64:67], a[152:155], a[0:3]\n"
"s_waitcnt vmcnt(8)\n"
"v_accvgpr_write_b32 a23, v15\n"
"v_lshlrev_b32 v24, 16, v26\n"
"v_mfma_f32_16x16x32_bf16 v[88:91], a[80:83], a[96:99], v[88:91]\n"
"v_and_b32 v25, s63, v26\n"
"v_mfma_f32_16x16x32_bf16 v[88:91], a[84:87], a[100:103], v[88:91]\n"
"global_load_dwordx4 a[64:67], v0, s[34:35] offset:-4096 sc1\n"
"v_lshlrev_b32 v26, 16, v27\n"
"v_mfma_f32_16x16x32_bf16 v[116:119], a[92:95], a[100:103], v[116:119]\n"
"ds_read_b128 a[100:103], v4 offset:17408\n"
"v_and_b32 v27, s63, v27\n"
"v_mfma_f32_16x16x32_bf16 v[116:119], a[88:91], a[96:99], v[116:119]\n"
"ds_read_b128 a[96:99], v4 offset:16384\n"
"v_add_f32 v200, v200, v24\n"
"v_mfma_f32_16x16x32_bf16 v[120:123], a[88:91], a[104:107], v[120:123]\n"
"v_add_f32 v201, v201, v25\n"
"v_mfma_f32_16x16x32_bf16 v[120:123], a[92:95], a[108:111], v[120:123]\n"
"v_add_f32 v202, v202, v26\n"
"v_mfma_f32_16x16x32_bf16 v[92:95], a[84:87], a[108:111], v[92:95]\n"
"ds_read_b128 a[108:111], v4 offset:19456\n"
"v_add_f32 v203, v203, v27\n"
"v_mfma_f32_16x16x32_bf16 v[92:95], a[80:83], a[104:107], v[92:95]\n"
"ds_read_b128 a[104:107], v4 offset:18432\n"
"v_add_f32 v204, v204, v24\n"
"v_mfma_f32_16x16x32_bf16 v[96:99], a[80:83], a[112:115], v[96:99]\n"
"v_add_f32 v205, v205, v25\n"
"v_add_f32 v206, v206, v26\n"
"v_mfma_f32_16x16x32_bf16 v[96:99], a[84:87], a[116:119], v[96:99]\n"
"global_load_dwordx4 a[68:71], v0, s[34:35] offset:-3072 sc1\n"
"v_add_f32 v207, v207, v27\n"
"v_mfma_f32_16x16x32_bf16 v[124:127], a[92:95], a[116:119], v[124:127]\n"
"ds_read_b128 a[116:119], v4 offset:21504\n"
"v_add_f32 v208, v208, v24\n"
"v_mfma_f32_16x16x32_bf16 v[124:127], a[88:91], a[112:115], v[124:127]\n"
"ds_read_b128 a[112:115], v4 offset:20480\n"
"v_add_f32 v209, v209, v25\n"
"v_mfma_f32_16x16x32_bf16 v[128:131], a[88:91], a[120:123], v[128:131]\n"
"v_add_f32 v210, v210, v26\n"
"v_mfma_f32_16x16x32_bf16 v[128:131], a[92:95], a[124:127], v[128:131]\n"
"v_add_f32 v211, v211, v27\n"
"v_mfma_f32_16x16x32_bf16 v[100:103], a[84:87], a[124:127], v[100:103]\n"
"ds_read_b128 a[124:127], v4 offset:23552\n"
"v_add_f32 v212, v212, v24\n"
"v_mfma_f32_16x16x32_bf16 v[100:103], a[80:83], a[120:123], v[100:103]\n"
"ds_read_b128 a[120:123], v4 offset:22528\n"
"v_add_f32 v213, v213, v25\n"
"v_mfma_f32_16x16x32_bf16 v[104:107], a[80:83], a[128:131], v[104:107]\n"
"v_add_f32 v214, v214, v26\n"
"v_add_f32 v215, v215, v27\n"
"v_mfma_f32_16x16x32_bf16 v[104:107], a[84:87], a[132:135], v[104:107]\n"
"global_load_dwordx4 a[72:75], v0, s[34:35] offset:-2048 sc1\n"
"v_add_f32 v216, v216, v24\n"
"v_mfma_f32_16x16x32_bf16 v[132:135], a[92:95], a[132:135], v[132:135]\n"
"ds_read_b128 a[132:135], v4 offset:25600\n"
"v_add_f32 v217, v217, v25\n"
"v_mfma_f32_16x16x32_bf16 v[132:135], a[88:91], a[128:131], v[132:135]\n"
"ds_read_b128 a[128:131], v4 offset:24576\n"
"v_add_f32 v218, v218, v26\n"
"v_mfma_f32_16x16x32_bf16 v[136:139], a[88:91], a[136:139], v[136:139]\n"
"v_add_f32 v219, v219, v27\n"
"v_mfma_f32_16x16x32_bf16 v[136:139], a[92:95], a[140:143], v[136:139]\n"
"v_add_f32 v220, v220, v24\n"
"v_mfma_f32_16x16x32_bf16 v[108:111], a[84:87], a[140:143], v[108:111]\n"
"ds_read_b128 a[140:143], v4 offset:27648\n"
"v_add_f32 v221, v221, v25\n"
"v_mfma_f32_16x16x32_bf16 v[108:111], a[80:83], a[136:139], v[108:111]\n"
"ds_read_b128 a[136:139], v4 offset:26624\n"
"v_add_f32 v222, v222, v26\n"
"v_mfma_f32_16x16x32_bf16 v[112:115], a[80:83], a[144:147], v[112:115]\n"
"v_add_f32 v223, v223, v27\n"
"v_mfma_f32_16x16x32_bf16 v[112:115], a[84:87], a[148:151], v[112:115]\n"
"global_load_dwordx4 a[76:79], v0, s[34:35] offset:-1024 sc1\n"
"v_add_f32 v224, v224, v24\n"
"v_mfma_f32_16x16x32_bf16 v[140:143], a[92:95], a[148:151], v[140:143]\n"
"ds_read_b128 a[148:151], v4 offset:29696\n"
"v_add_f32 v225, v225, v25\n"
"v_mfma_f32_16x16x32_bf16 v[140:143], a[88:91], a[144:147], v[140:143]\n"
"ds_read_b128 a[144:147], v4 offset:28672\n"
"v_add_f32 v226, v226, v26\n"
"v_mfma_f32_16x16x32_bf16 a[12:15], a[88:91], a[152:155], a[12:15]\n"
"v_add_f32 v227, v227, v27\n"
"v_accvgpr_read_b32 v12, a24\n"
"v_mfma_f32_16x16x32_bf16 a[12:15], a[92:95], a[156:159], a[12:15]\n"
"v_accvgpr_read_b32 v13, a25\n"
"v_mfma_f32_16x16x32_bf16 a[8:11], a[84:87], a[156:159], a[8:11]\n"
"ds_read_b128 a[156:159], v4 offset:31744\n"
"v_accvgpr_read_b32 v14, a26\n"
"v_mfma_f32_16x16x32_bf16 a[8:11], a[80:83], a[152:155], a[8:11]\n"
"s_waitcnt vmcnt(8) lgkmcnt(11)\n"
"ds_read_b128 a[152:155], v4 offset:30720\n"
"v_accvgpr_read_b32 v15, a27\n"
"v_mfma_f32_16x16x32_bf16 v[32:35], a[32:35], a[96:99], v[32:35]\n"
"v_add_f32 v12, v12, v24\n"
"v_mfma_f32_16x16x32_bf16 v[32:35], a[36:39], a[100:103], v[32:35]\n"
"global_load_dwordx4 a[80:83], v0, s[34:35] offset:0 sc1\n"
"v_add_f32 v13, v13, v25\n"
"v_mfma_f32_16x16x32_bf16 v[60:63], a[44:47], a[100:103], v[60:63]\n"
"v_add_f32 v14, v14, v26\n"
"v_mfma_f32_16x16x32_bf16 v[60:63], a[40:43], a[96:99], v[60:63]\n"
"v_add_f32 v15, v15, v27\n"
"v_mfma_f32_16x16x32_bf16 v[64:67], a[40:43], a[104:107], v[64:67]\n"
"v_accvgpr_write_b32 a24, v12\n"
"v_accvgpr_write_b32 a25, v13\n"
"v_mfma_f32_16x16x32_bf16 v[64:67], a[44:47], a[108:111], v[64:67]\n"
"v_accvgpr_write_b32 a26, v14\n"
"v_mfma_f32_16x16x32_bf16 v[36:39], a[36:39], a[108:111], v[36:39]\n"
"v_accvgpr_write_b32 a27, v15\n"
"v_mfma_f32_16x16x32_bf16 v[36:39], a[32:35], a[104:107], v[36:39]\n"
"s_waitcnt lgkmcnt(8)\n"
"v_lshlrev_b32 v28, 16, v30\n"
"v_mfma_f32_16x16x32_bf16 v[40:43], a[32:35], a[112:115], v[40:43]\n"
"v_and_b32 v29, s63, v30\n"
"v_mfma_f32_16x16x32_bf16 v[40:43], a[36:39], a[116:119], v[40:43]\n"
"global_load_dwordx4 a[84:87], v0, s[34:35] offset:1024 sc1\n"
"v_lshlrev_b32 v30, 16, v31\n"
"v_mfma_f32_16x16x32_bf16 v[68:71], a[44:47], a[116:119], v[68:71]\n"
"v_and_b32 v31, s63, v31\n"
"v_mfma_f32_16x16x32_bf16 v[68:71], a[40:43], a[112:115], v[68:71]\n"
"v_add_f32 v228, v228, v28\n"
"v_mfma_f32_16x16x32_bf16 v[72:75], a[40:43], a[120:123], v[72:75]\n"
"v_add_f32 v229, v229, v29\n"
"v_mfma_f32_16x16x32_bf16 v[72:75], a[44:47], a[124:127], v[72:75]\n"
"v_add_f32 v230, v230, v30\n"
"v_mfma_f32_16x16x32_bf16 v[44:47], a[36:39], a[124:127], v[44:47]\n"
"v_add_f32 v231, v231, v31\n"
"v_add_f32 v232, v232, v28\n"
"v_mfma_f32_16x16x32_bf16 v[44:47], a[32:35], a[120:123], v[44:47]\n"
"s_waitcnt lgkmcnt(4)\n"
"v_add_f32 v233, v233, v29\n"
"v_mfma_f32_16x16x32_bf16 v[48:51], a[32:35], a[128:131], v[48:51]\n"
"v_add_f32 v234, v234, v30\n"
"v_mfma_f32_16x16x32_bf16 v[48:51], a[36:39], a[132:135], v[48:51]\n"
"global_load_dwordx4 a[88:91], v0, s[34:35] offset:2048 sc1\n"
"v_add_f32 v235, v235, v31\n"
"v_mfma_f32_16x16x32_bf16 v[76:79], a[44:47], a[132:135], v[76:79]\n"
"v_add_f32 v236, v236, v28\n"
"v_mfma_f32_16x16x32_bf16 v[76:79], a[40:43], a[128:131], v[76:79]\n"
"v_add_f32 v237, v237, v29\n"
"v_mfma_f32_16x16x32_bf16 v[80:83], a[40:43], a[136:139], v[80:83]\n"
"v_add_f32 v238, v238, v30\n"
"v_mfma_f32_16x16x32_bf16 v[80:83], a[44:47], a[140:143], v[80:83]\n"
"v_add_f32 v239, v239, v31\n"
"v_mfma_f32_16x16x32_bf16 v[52:55], a[36:39], a[140:143], v[52:55]\n"
"v_add_f32 v240, v240, v28\n"
"v_mfma_f32_16x16x32_bf16 v[52:55], a[32:35], a[136:139], v[52:55]\n"
"s_waitcnt lgkmcnt(0)\n"
"v_add_f32 v241, v241, v29\n"
"v_add_f32 v242, v242, v30\n"
"v_mfma_f32_16x16x32_bf16 v[56:59], a[32:35], a[144:147], v[56:59]\n"
"v_add_f32 v243, v243, v31\n"
"v_mfma_f32_16x16x32_bf16 v[56:59], a[36:39], a[148:151], v[56:59]\n"
"global_load_dwordx4 a[92:95], v0, s[34:35] offset:3072 sc1\n"
"v_add_f32 v244, v244, v28\n"
"v_mfma_f32_16x16x32_bf16 v[84:87], a[44:47], a[148:151], v[84:87]\n"
"v_add_f32 v245, v245, v29\n"
"v_mfma_f32_16x16x32_bf16 v[84:87], a[40:43], a[144:147], v[84:87]\n"
"v_add_f32 v246, v246, v30\n"
"v_mfma_f32_16x16x32_bf16 a[4:7], a[40:43], a[152:155], a[4:7]\n"
"s_add_u32 s34, s34, 8192\n"
"v_add_f32 v247, v247, v31\n"
"v_mfma_f32_16x16x32_bf16 a[4:7], a[44:47], a[156:159], a[4:7]\n"
"v_add_f32 v248, v248, v28\n"
"v_mfma_f32_16x16x32_bf16 a[0:3], a[36:39], a[156:159], a[0:3]\n"
"v_add_f32 v249, v249, v29\n"
"v_mfma_f32_16x16x32_bf16 a[0:3], a[32:35], a[152:155], a[0:3]\n"
"s_waitcnt vmcnt(8)\n"
"v_add_f32 v250, v250, v30\n"
"v_mfma_f32_16x16x32_bf16 v[88:91], a[48:51], a[96:99], v[88:91]\n"
"v_add_f32 v251, v251, v31\n"
"v_add_f32 v252, v252, v28\n"
"v_mfma_f32_16x16x32_bf16 v[88:91], a[52:55], a[100:103], v[88:91]\n"
"global_load_dwordx4 a[32:35], v0, s[34:35] offset:-4096 sc1\n"
"v_add_f32 v253, v253, v29\n"
"v_mfma_f32_16x16x32_bf16 v[116:119], a[60:63], a[100:103], v[116:119]\n"
"ds_read_b128 a[100:103], v5 offset:17408\n"
"v_add_f32 v254, v254, v30\n"
"v_mfma_f32_16x16x32_bf16 v[116:119], a[56:59], a[96:99], v[116:119]\n"
"ds_read_b128 a[96:99], v5 offset:16384\n"
"v_add_f32 v255, v255, v31\n"
"v_mfma_f32_16x16x32_bf16 v[120:123], a[56:59], a[104:107], v[120:123]\n"
"v_accvgpr_read_b32 v12, a28\n"
"v_mfma_f32_16x16x32_bf16 v[120:123], a[60:63], a[108:111], v[120:123]\n"
"v_accvgpr_read_b32 v13, a29\n"
"v_mfma_f32_16x16x32_bf16 v[92:95], a[52:55], a[108:111], v[92:95]\n"
"ds_read_b128 a[108:111], v5 offset:19456\n"
"v_accvgpr_read_b32 v14, a30\n"
"v_mfma_f32_16x16x32_bf16 v[92:95], a[48:51], a[104:107], v[92:95]\n"
"ds_read_b128 a[104:107], v5 offset:18432\n"
"v_accvgpr_read_b32 v15, a31\n"
"v_mfma_f32_16x16x32_bf16 v[96:99], a[48:51], a[112:115], v[96:99]\n"
"v_add_f32 v12, v12, v28\n"
"v_add_f32 v13, v13, v29\n"
"v_mfma_f32_16x16x32_bf16 v[96:99], a[52:55], a[116:119], v[96:99]\n"
"global_load_dwordx4 a[36:39], v0, s[34:35] offset:-3072 sc1\n"
"v_add_f32 v14, v14, v30\n"
"v_mfma_f32_16x16x32_bf16 v[124:127], a[60:63], a[116:119], v[124:127]\n"
"ds_read_b128 a[116:119], v5 offset:21504\n"
"v_add_f32 v15, v15, v31\n"
"v_mfma_f32_16x16x32_bf16 v[124:127], a[56:59], a[112:115], v[124:127]\n"
"ds_read_b128 a[112:115], v5 offset:20480\n"
"v_accvgpr_write_b32 a28, v12\n"
"v_mfma_f32_16x16x32_bf16 v[128:131], a[56:59], a[120:123], v[128:131]\n"
"v_accvgpr_write_b32 a29, v13\n"
"v_mfma_f32_16x16x32_bf16 v[128:131], a[60:63], a[124:127], v[128:131]\n"
"v_accvgpr_write_b32 a30, v14\n"
"v_mfma_f32_16x16x32_bf16 v[100:103], a[52:55], a[124:127], v[100:103]\n"
"ds_read_b128 a[124:127], v5 offset:23552\n"
"v_accvgpr_write_b32 a31, v15\n"
"v_mfma_f32_16x16x32_bf16 v[100:103], a[48:51], a[120:123], v[100:103]\n"
"ds_read_b128 a[120:123], v5 offset:22528\n"
"v_mfma_f32_16x16x32_bf16 v[104:107], a[48:51], a[128:131], v[104:107]\n"
"v_mfma_f32_16x16x32_bf16 v[104:107], a[52:55], a[132:135], v[104:107]\n"
"global_load_dwordx4 a[40:43], v0, s[34:35] offset:-2048 sc1\n"
"v_mfma_f32_16x16x32_bf16 v[132:135], a[60:63], a[132:135], v[132:135]\n"
"ds_read_b128 a[132:135], v5 offset:25600\n"
"v_mfma_f32_16x16x32_bf16 v[132:135], a[56:59], a[128:131], v[132:135]\n"
"ds_read_b128 a[128:131], v5 offset:24576\n"
"v_mfma_f32_16x16x32_bf16 v[136:139], a[56:59], a[136:139], v[136:139]\n"
"ds_read_b128 v[16:19], v7 offset:32768\n"
"v_mfma_f32_16x16x32_bf16 v[136:139], a[60:63], a[140:143], v[136:139]\n"
"v_mfma_f32_16x16x32_bf16 v[108:111], a[52:55], a[140:143], v[108:111]\n"
"ds_read_b128 a[140:143], v5 offset:27648\n"
"v_mfma_f32_16x16x32_bf16 v[108:111], a[48:51], a[136:139], v[108:111]\n"
"ds_read_b128 a[136:139], v5 offset:26624\n"
"v_mfma_f32_16x16x32_bf16 v[112:115], a[48:51], a[144:147], v[112:115]\n"
"v_mfma_f32_16x16x32_bf16 v[112:115], a[52:55], a[148:151], v[112:115]\n"
"global_load_dwordx4 a[44:47], v0, s[34:35] offset:-1024 sc1\n"
"v_mfma_f32_16x16x32_bf16 v[140:143], a[60:63], a[148:151], v[140:143]\n"
"ds_read_b128 a[148:151], v5 offset:29696\n"
"v_mfma_f32_16x16x32_bf16 v[140:143], a[56:59], a[144:147], v[140:143]\n"
"ds_read_b128 a[144:147], v5 offset:28672\n"
"v_mfma_f32_16x16x32_bf16 a[12:15], a[56:59], a[152:155], a[12:15]\n"
"v_mfma_f32_16x16x32_bf16 a[12:15], a[60:63], a[156:159], a[12:15]\n"
"v_mfma_f32_16x16x32_bf16 a[8:11], a[52:55], a[156:159], a[8:11]\n"
"ds_read_b128 a[156:159], v5 offset:31744\n"
"v_mfma_f32_16x16x32_bf16 a[8:11], a[48:51], a[152:155], a[8:11]\n"
"ds_read_b128 a[152:155], v5 offset:30720\n"
"s_branch L_P6\n"
"L_P3F:\n"
"L_st8279:\n"
"s_cbranch_execz L_retB\n"
"s_waitcnt vmcnt(10) lgkmcnt(13)\n"
"v_mfma_f32_16x16x32_bf16 v[32:35], a[32:35], a[96:99], v[32:35]\n"
"s_waitcnt lgkmcnt(6)\n"
"global_store_dwordx4 v0, v[24:27], s[52:53] offset:2048 sc1\n"
"s_cmp_lt_u32 s27, 12582912\n"
"s_cselect_b32 s38, 8192000, 0\n"
"s_cselect_b32 s42, 12582144, 0\n"
"v_mfma_f32_16x16x32_bf16 v[32:35], a[36:39], a[100:103], v[32:35]\n"
"global_load_dwordx4 a[80:83], v0, s[34:35] offset:0 sc1\n"
"v_mfma_f32_16x16x32_bf16 v[60:63], a[44:47], a[100:103], v[60:63]\n"
"s_barrier\n"
"v_mfma_f32_16x16x32_bf16 v[60:63], a[40:43], a[96:99], v[60:63]\n"
"s_add_u32 m0, s64, 12288\n"
"v_mfma_f32_16x16x32_bf16 v[64:67], a[40:43], a[104:107], v[64:67]\n"
"v_mfma_f32_16x16x32_bf16 v[64:67], a[44:47], a[108:111], v[64:67]\n"
"s_add_u32 s67, s27, 0\n"
"v_mfma_f32_16x16x32_bf16 v[36:39], a[36:39], a[108:111], v[36:39]\n"
"v_mfma_f32_16x16x32_bf16 v[36:39], a[32:35], a[104:107], v[36:39]\n"
"v_mfma_f32_16x16x32_bf16 v[40:43], a[32:35], a[112:115], v[40:43]\n"
"buffer_load_dwordx4 v6, s[40:43], s67 offen offset:0 lds\n"
"v_mfma_f32_16x16x32_bf16 v[40:43], a[36:39], a[116:119], v[40:43]\n"
"global_load_dwordx4 a[84:87], v0, s[34:35] offset:1024 sc1\n"
"v_mfma_f32_16x16x32_bf16 v[68:71], a[44:47], a[116:119], v[68:71]\n"
"v_mfma_f32_16x16x32_bf16 v[68:71], a[40:43], a[112:115], v[68:71]\n"
"s_add_u32 s67, s27, 15360\n"
"v_mfma_f32_16x16x32_bf16 v[72:75], a[40:43], a[120:123], v[72:75]\n"
"v_mfma_f32_16x16x32_bf16 v[72:75], a[44:47], a[124:127], v[72:75]\n"
"v_mfma_f32_16x16x32_bf16 v[44:47], a[36:39], a[124:127], v[44:47]\n"
"buffer_load_dwordx4 v6, s[40:43], s67 offen offset:1024 lds\n"
"v_mfma_f32_16x16x32_bf16 v[44:47], a[32:35], a[120:123], v[44:47]\n"
"s_waitcnt lgkmcnt(4)\n"
"v_mfma_f32_16x16x32_bf16 v[48:51], a[32:35], a[128:131], v[48:51]\n"
"v_mfma_f32_16x16x32_bf16 v[48:51], a[36:39], a[132:135], v[48:51]\n"
"global_load_dwordx4 a[88:91], v0, s[34:35] offset:2048 sc1\n"
"v_lshrrev_b32 v16, 4, v0\n"
"v_mfma_f32_16x16x32_bf16 v[76:79], a[44:47], a[132:135], v[76:79]\n"
"s_add_u32 s67, s27, 30720\n"
"v_and_b32 v16, 0x30, v16\n"
"v_mfma_f32_16x16x32_bf16 v[76:79], a[40:43], a[128:131], v[76:79]\n"
"ds_read_b128 v[28:31], v2 offset:23552\n"
"buffer_load_dwordx2 v[18:19], v16, s[44:47], s61 offen offset:0\n"
"v_mfma_f32_16x16x32_bf16 v[80:83], a[40:43], a[136:139], v[80:83]\n"
"buffer_load_dwordx2 v[8:9], off, s[56:59], %14 sc1\n"
"v_mfma_f32_16x16x32_bf16 v[80:83], a[44:47], a[140:143], v[80:83]\n"
"buffer_load_dwordx4 v6, s[40:43], s67 offen offset:2048 lds\n"
"v_mfma_f32_16x16x32_bf16 v[52:55], a[36:39], a[140:143], v[52:55]\n"
"v_mfma_f32_16x16x32_bf16 v[52:55], a[32:35], a[136:139], v[52:55]\n"
"s_waitcnt lgkmcnt(1)\n"
"v_mfma_f32_16x16x32_bf16 v[56:59], a[32:35], a[144:147], v[56:59]\n"
"s_add_u32 s67, s27, 46080\n"
"v_mfma_f32_16x16x32_bf16 v[56:59], a[36:39], a[148:151], v[56:59]\n"
"global_load_dwordx4 a[92:95], v0, s[34:35] offset:3072 sc1\n"
"v_lshrrev_b32 v20, 4, v0\n"
"v_mfma_f32_16x16x32_bf16 v[84:87], a[44:47], a[148:151], v[84:87]\n"
"v_and_b32 v20, 0x30, v20\n"
"v_mfma_f32_16x16x32_bf16 v[84:87], a[40:43], a[144:147], v[84:87]\n"
"buffer_load_dwordx4 v6, s[40:43], s67 offen offset:3072 lds\n"
"buffer_load_dwordx2 v[22:23], v20, s[44:47], s61 offen offset:8\n"
"v_mfma_f32_16x16x32_bf16 a[4:7], a[40:43], a[152:155], a[4:7]\n"
"s_add_u32 s34, s34, 8192\n"
"v_mfma_f32_16x16x32_bf16 a[4:7], a[44:47], a[156:159], a[4:7]\n"
"v_mfma_f32_16x16x32_bf16 a[0:3], a[36:39], a[156:159], a[0:3]\n"
"s_waitcnt lgkmcnt(0)\n"
"global_store_dwordx4 v0, v[28:31], s[52:53] offset:3072 sc1\n"
"v_mfma_f32_16x16x32_bf16 a[0:3], a[32:35], a[152:155], a[0:3]\n"
"s_waitcnt vmcnt(18)\n"
"v_mfma_f32_16x16x32_bf16 v[88:91], a[48:51], a[96:99], v[88:91]\n"
"v_mfma_f32_16x16x32_bf16 v[88:91], a[52:55], a[100:103], v[88:91]\n"
"global_load_dwordx4 a[32:35], v0, s[34:35] offset:-4096 sc1\n"
"v_mfma_f32_16x16x32_bf16 v[116:119], a[60:63], a[100:103], v[116:119]\n"
"v_mfma_f32_16x16x32_bf16 v[116:119], a[56:59], a[96:99], v[116:119]\n"
"v_mfma_f32_16x16x32_bf16 v[120:123], a[56:59], a[104:107], v[120:123]\n"
"v_mfma_f32_16x16x32_bf16 v[120:123], a[60:63], a[108:111], v[120:123]\n"
"v_mfma_f32_16x16x32_bf16 v[92:95], a[52:55], a[108:111], v[92:95]\n"
"v_mfma_f32_16x16x32_bf16 v[92:95], a[48:51], a[104:107], v[92:95]\n"
"v_mfma_f32_16x16x32_bf16 v[96:99], a[48:51], a[112:115], v[96:99]\n"
"v_mfma_f32_16x16x32_bf16 v[96:99], a[52:55], a[116:119], v[96:99]\n"
"global_load_dwordx4 a[36:39], v0, s[34:35] offset:-3072 sc1\n"
"v_mfma_f32_16x16x32_bf16 v[124:127], a[60:63], a[116:119], v[124:127]\n"
"v_mfma_f32_16x16x32_bf16 v[124:127], a[56:59], a[112:115], v[124:127]\n"
"v_mfma_f32_16x16x32_bf16 v[128:131], a[56:59], a[120:123], v[128:131]\n"
"v_mfma_f32_16x16x32_bf16 v[128:131], a[60:63], a[124:127], v[128:131]\n"
"v_mfma_f32_16x16x32_bf16 v[100:103], a[52:55], a[124:127], v[100:103]\n"
"v_mfma_f32_16x16x32_bf16 v[100:103], a[48:51], a[120:123], v[100:103]\n"
"v_mfma_f32_16x16x32_bf16 v[104:107], a[48:51], a[128:131], v[104:107]\n"
"ds_read_b128 v[24:27], v2 offset:24576\n"
"v_mfma_f32_16x16x32_bf16 v[104:107], a[52:55], a[132:135], v[104:107]\n"
"global_load_dwordx4 a[40:43], v0, s[34:35] offset:-2048 sc1\n"
"v_mfma_f32_16x16x32_bf16 v[132:135], a[60:63], a[132:135], v[132:135]\n"
"v_mfma_f32_16x16x32_bf16 v[132:135], a[56:59], a[128:131], v[132:135]\n"
"v_mfma_f32_16x16x32_bf16 v[136:139], a[56:59], a[136:139], v[136:139]\n"
"v_mfma_f32_16x16x32_bf16 v[136:139], a[60:63], a[140:143], v[136:139]\n"
"v_mfma_f32_16x16x32_bf16 v[108:111], a[52:55], a[140:143], v[108:111]\n"
"v_mfma_f32_16x16x32_bf16 v[108:111], a[48:51], a[136:139], v[108:111]\n"
"v_mfma_f32_16x16x32_bf16 v[112:115], a[48:51], a[144:147], v[112:115]\n"
"v_mfma_f32_16x16x32_bf16 v[112:115], a[52:55], a[148:151], v[112:115]\n"
"global_load_dwordx4 a[44:47], v0, s[34:35] offset:-1024 sc1\n"
"v_mfma_f32_16x16x32_bf16 v[140:143], a[60:63], a[148:151], v[140:143]\n"
"v_mfma_f32_16x16x32_bf16 v[140:143], a[56:59], a[144:147], v[140:143]\n"
"v_mfma_f32_16x16x32_bf16 a[12:15], a[56:59], a[152:155], a[12:15]\n"
"s_waitcnt lgkmcnt(0)\n"
"global_store_dwordx4 v0, v[24:27], s[54:55] offset:-4096 sc1\n"
"v_mfma_f32_16x16x32_bf16 a[12:15], a[60:63], a[156:159], a[12:15]\n"
"v_mfma_f32_16x16x32_bf16 a[8:11], a[52:55], a[156:159], a[8:11]\n"
"v_mfma_f32_16x16x32_bf16 a[8:11], a[48:51], a[152:155], a[8:11]\n"
"s_waitcnt vmcnt(18)\n"
"v_mfma_f32_16x16x32_bf16 v[144:147], a[64:67], a[96:99], v[144:147]\n"
"v_mfma_f32_16x16x32_bf16 v[144:147], a[68:71], a[100:103], v[144:147]\n"
"global_load_dwordx4 a[48:51], v0, s[34:35] offset:0 sc1\n"
"v_mfma_f32_16x16x32_bf16 v[172:175], a[76:79], a[100:103], v[172:175]\n"
"v_mfma_f32_16x16x32_bf16 v[172:175], a[72:75], a[96:99], v[172:175]\n"
"v_mfma_f32_16x16x32_bf16 v[176:179], a[72:75], a[104:107], v[176:179]\n"
"v_mfma_f32_16x16x32_bf16 v[176:179], a[76:79], a[108:111], v[176:179]\n"
"v_mfma_f32_16x16x32_bf16 v[148:151], a[68:71], a[108:111], v[148:151]\n"
"v_mfma_f32_16x16x32_bf16 v[148:151], a[64:67], a[104:107], v[148:151]\n"
"v_mfma_f32_16x16x32_bf16 v[152:155], a[64:67], a[112:115], v[152:155]\n"
"v_mfma_f32_16x16x32_bf16 v[152:155], a[68:71], a[116:119], v[152:155]\n"
"global_load_dwordx4 a[52:55], v0, s[34:35] offset:1024 sc1\n"
"v_mfma_f32_16x16x32_bf16 v[180:183], a[76:79], a[116:119], v[180:183]\n"
"v_mfma_f32_16x16x32_bf16 v[180:183], a[72:75], a[112:115], v[180:183]\n"
"v_mfma_f32_16x16x32_bf16 v[184:187], a[72:75], a[120:123], v[184:187]\n"
"v_mfma_f32_16x16x32_bf16 v[184:187], a[76:79], a[124:127], v[184:187]\n"
"v_mfma_f32_16x16x32_bf16 v[156:159], a[68:71], a[124:127], v[156:159]\n"
"v_mfma_f32_16x16x32_bf16 v[156:159], a[64:67], a[120:123], v[156:159]\n"
"ds_read_b128 v[28:31], v2 offset:26624\n"
"v_mfma_f32_16x16x32_bf16 v[160:163], a[64:67], a[128:131], v[160:163]\n"
"v_mfma_f32_16x16x32_bf16 v[160:163], a[68:71], a[132:135], v[160:163]\n"
"global_load_dwordx4 a[56:59], v0, s[34:35] offset:2048 sc1\n"
"v_mfma_f32_16x16x32_bf16 v[188:191], a[76:79], a[132:135], v[188:191]\n"
"v_mfma_f32_16x16x32_bf16 v[188:191], a[72:75], a[128:131], v[188:191]\n"
"v_mfma_f32_16x16x32_bf16 v[192:195], a[72:75], a[136:139], v[192:195]\n"
"v_mfma_f32_16x16x32_bf16 v[192:195], a[76:79], a[140:143], v[192:195]\n"
"v_mfma_f32_16x16x32_bf16 v[164:167], a[68:71], a[140:143], v[164:167]\n"
"v_mfma_f32_16x16x32_bf16 v[164:167], a[64:67], a[136:139], v[164:167]\n"
"v_mfma_f32_16x16x32_bf16 v[168:171], a[64:67], a[144:147], v[168:171]\n"
"v_mfma_f32_16x16x32_bf16 v[168:171], a[68:71], a[148:151], v[168:171]\n"
"global_load_dwordx4 a[60:63], v0, s[34:35] offset:3072 sc1\n"
"v_mfma_f32_16x16x32_bf16 v[196:199], a[76:79], a[148:151], v[196:199]\n"
"s_waitcnt lgkmcnt(0)\n"
"global_store_dwordx4 v0, v[28:31], s[54:55] offset:-2048 sc1\n"
"v_mfma_f32_16x16x32_bf16 v[196:199], a[72:75], a[144:147], v[196:199]\n"
"v_mfma_f32_16x16x32_bf16 a[20:23], a[72:75], a[152:155], a[20:23]\n"
"s_add_u32 s34, s34, 8192\n"
"v_mfma_f32_16x16x32_bf16 a[20:23], a[76:79], a[156:159], a[20:23]\n"
"v_mfma_f32_16x16x32_bf16 a[16:19], a[68:71], a[156:159], a[16:19]\n"
"v_mfma_f32_16x16x32_bf16 a[16:19], a[64:67], a[152:155], a[16:19]\n"
"s_waitcnt vmcnt(13)\n"
"v_mfma_f32_16x16x32_bf16 v[200:203], a[80:83], a[96:99], v[200:203]\n"
"v_mfma_f32_16x16x32_bf16 v[200:203], a[84:87], a[100:103], v[200:203]\n"
"global_load_dwordx4 a[64:67], v0, s[34:35] offset:-4096 sc1\n"
"v_mfma_f32_16x16x32_bf16 v[228:231], a[92:95], a[100:103], v[228:231]\n"
"ds_read_b128 a[100:103], v3 offset:17408\n"
"v_mfma_f32_16x16x32_bf16 v[228:231], a[88:91], a[96:99], v[228:231]\n"
"ds_read_b128 a[96:99], v3 offset:16384\n"
"v_mfma_f32_16x16x32_bf16 v[232:235], a[88:91], a[104:107], v[232:235]\n"
"v_mfma_f32_16x16x32_bf16 v[232:235], a[92:95], a[108:111], v[232:235]\n"
"v_mfma_f32_16x16x32_bf16 v[204:207], a[84:87], a[108:111], v[204:207]\n"
"ds_read_b128 a[108:111], v3 offset:19456\n"
"v_mfma_f32_16x16x32_bf16 v[204:207], a[80:83], a[104:107], v[204:207]\n"
"ds_read_b128 a[104:107], v3 offset:18432\n"
"v_mfma_f32_16x16x32_bf16 v[208:211], a[80:83], a[112:115], v[208:211]\n"
"v_mfma_f32_16x16x32_bf16 v[208:211], a[84:87], a[116:119], v[208:211]\n"
"global_load_dwordx4 a[68:71], v0, s[34:35] offset:-3072 sc1\n"
"v_mfma_f32_16x16x32_bf16 v[236:239], a[92:95], a[116:119], v[236:239]\n"
"ds_read_b128 a[116:119], v3 offset:21504\n"
"v_mfma_f32_16x16x32_bf16 v[236:239], a[88:91], a[112:115], v[236:239]\n"
"ds_read_b128 a[112:115], v3 offset:20480\n"
"v_mfma_f32_16x16x32_bf16 v[240:243], a[88:91], a[120:123], v[240:243]\n"
"v_mfma_f32_16x16x32_bf16 v[240:243], a[92:95], a[124:127], v[240:243]\n"
"ds_read_b128 v[24:27], v2 offset:29696\n"
"v_mfma_f32_16x16x32_bf16 v[212:215], a[84:87], a[124:127], v[212:215]\n"
"ds_read_b128 a[124:127], v3 offset:23552\n"
"v_mfma_f32_16x16x32_bf16 v[212:215], a[80:83], a[120:123], v[212:215]\n"
"ds_read_b128 a[120:123], v3 offset:22528\n"
"v_mfma_f32_16x16x32_bf16 v[216:219], a[80:83], a[128:131], v[216:219]\n"
"v_mfma_f32_16x16x32_bf16 v[216:219], a[84:87], a[132:135], v[216:219]\n"
"global_load_dwordx4 a[72:75], v0, s[34:35] offset:-2048 sc1\n"
"v_mfma_f32_16x16x32_bf16 v[244:247], a[92:95], a[132:135], v[244:247]\n"
"ds_read_b128 a[132:135], v3 offset:25600\n"
"v_mfma_f32_16x16x32_bf16 v[244:247], a[88:91], a[128:131], v[244:247]\n"
"ds_read_b128 a[128:131], v3 offset:24576\n"
"v_mfma_f32_16x16x32_bf16 v[248:251], a[88:91], a[136:139], v[248:251]\n"
"v_subrev_u32 v8, s65, v8\n"
"v_mfma_f32_16x16x32_bf16 v[248:251], a[92:95], a[140:143], v[248:251]\n"
"v_xor_b32 v9, s79, v9\n"
"v_mfma_f32_16x16x32_bf16 v[220:223], a[84:87], a[140:143], v[220:223]\n"
"ds_read_b128 a[140:143], v3 offset:27648\n"
"v_or_b32 v8, v8, v9\n"
"v_mfma_f32_16x16x32_bf16 v[220:223], a[80:83], a[136:139], v[220:223]\n"
"ds_read_b128 a[136:139], v3 offset:26624\n"
"v_cmp_lt_u32 vcc, 15, v8\n"
"v_mfma_f32_16x16x32_bf16 v[224:227], a[80:83], a[144:147], v[224:227]\n"
"s_waitcnt lgkmcnt(6)\n"
"global_store_dwordx4 v0, v[24:27], s[54:55] offset:1024 sc1\n"
"s_cbranch_vccnz L_slow7\n"
"L_back7:\n"
"v_mfma_f32_16x16x32_bf16 v[224:227], a[84:87], a[148:151], v[224:227]\n"
"global_load_dwordx4 a[76:79], v0, s[34:35] offset:-1024 sc1\n"
"v_readfirstlane_b32 s66, v9\n"
"v_mfma_f32_16x16x32_bf16 v[252:255], a[92:95], a[148:151], v[252:255]\n"
"ds_read_b128 a[148:151], v3 offset:29696\n"
"v_mfma_f32_16x16x32_bf16 v[252:255], a[88:91], a[144:147], v[252:255]\n"
"s_and_b32 s66, s66, 3\n"
"ds_read_b128 a[144:147], v3 offset:28672\n"
"v_mfma_f32_16x16x32_bf16 a[28:31], a[88:91], a[152:155], a[28:31]\n"
"s_lshl_b32 s69, s66, 13\n"
"v_mfma_f32_16x16x32_bf16 a[28:31], a[92:95], a[156:159], a[28:31]\n"
"s_cmp_eq_u32 s66, 3\n"
"s_cselect_b32 s75, 8192, 0\n"
"v_mfma_f32_16x16x32_bf16 a[24:27], a[84:87], a[156:159], a[24:27]\n"
"s_and_b32 s29, s66, s30\n"
"ds_read_b128 a[156:159], v3 offset:31744\n"
"v_mfma_f32_16x16x32_bf16 a[24:27], a[80:83], a[152:155], a[24:27]\n"
"s_waitcnt vmcnt(11)\n"
"ds_read_b128 a[152:155], v3 offset:30720\n"
"v_mfma_f32_16x16x32_bf16 v[32:35], a[32:35], a[96:99], v[32:35]\n"
"s_xor_b32 s31, s79, s66\n"
"v_mfma_f32_16x16x32_bf16 v[32:35], a[36:39], a[100:103], v[32:35]\n"
"global_load_dwordx4 a[80:83], v0, s[34:35] offset:0 sc1\n"
"v_mfma_f32_16x16x32_bf16 v[60:63], a[44:47], a[100:103], v[60:63]\n"
"v_mfma_f32_16x16x32_bf16 v[60:63], a[40:43], a[96:99], v[60:63]\n"
"v_mfma_f32_16x16x32_bf16 v[64:67], a[40:43], a[104:107], v[64:67]\n"
"v_mfma_f32_16x16x32_bf16 v[64:67], a[44:47], a[108:111], v[64:67]\n"
"v_mfma_f32_16x16x32_bf16 v[36:39], a[36:39], a[108:111], v[36:39]\n"
"s_add_u32 m0, s64, 0\n"
"v_mfma_f32_16x16x32_bf16 v[36:39], a[32:35], a[104:107], v[36:39]\n"
"s_waitcnt lgkmcnt(8)\n"
"v_mfma_f32_16x16x32_bf16 v[40:43], a[32:35], a[112:115], v[40:43]\n"
"v_mfma_f32_16x16x32_bf16 v[40:43], a[36:39], a[116:119], v[40:43]\n"
"global_load_dwordx4 a[84:87], v0, s[34:35] offset:1024 sc1\n"
"v_mfma_f32_16x16x32_bf16 v[68:71], a[44:47], a[116:119], v[68:71]\n"
"v_mfma_f32_16x16x32_bf16 v[68:71], a[40:43], a[112:115], v[68:71]\n"
"v_mfma_f32_16x16x32_bf16 v[72:75], a[40:43], a[120:123], v[72:75]\n"
"v_mfma_f32_16x16x32_bf16 v[72:75], a[44:47], a[124:127], v[72:75]\n"
"v_and_b32 v10, 0x300, v0\n"
"v_mfma_f32_16x16x32_bf16 v[44:47], a[36:39], a[124:127], v[44:47]\n"
"v_mfma_f32_16x16x32_bf16 v[44:47], a[32:35], a[120:123], v[44:47]\n"
"s_waitcnt lgkmcnt(4)\n"
"v_mfma_f32_16x16x32_bf16 v[48:51], a[32:35], a[128:131], v[48:51]\n"
"v_and_b32 v11, 0x3c0, v0\n"
"v_mfma_f32_16x16x32_bf16 v[48:51], a[36:39], a[132:135], v[48:51]\n"
"global_load_dwordx4 a[88:91], v0, s[34:35] offset:2048 sc1\n"
"v_mfma_f32_16x16x32_bf16 v[76:79], a[44:47], a[132:135], v[76:79]\n"
"v_mfma_f32_16x16x32_bf16 v[76:79], a[40:43], a[128:131], v[76:79]\n"
"v_mfma_f32_16x16x32_bf16 v[80:83], a[40:43], a[136:139], v[80:83]\n"
"v_add_lshl_u32 v11, v10, v11, 1\n"
"v_mfma_f32_16x16x32_bf16 v[80:83], a[44:47], a[140:143], v[80:83]\n"
"buffer_load_dwordx4 v0, s[48:51], s75 offen offset:0 sc1 lds\n"
"v_mfma_f32_16x16x32_bf16 v[52:55], a[36:39], a[140:143], v[52:55]\n"
"v_mfma_f32_16x16x32_bf16 v[52:55], a[32:35], a[136:139], v[52:55]\n"
"s_waitcnt lgkmcnt(0)\n"
"v_mfma_f32_16x16x32_bf16 v[56:59], a[32:35], a[144:147], v[56:59]\n"
"v_lshrrev_b32 v10, 2, v0\n"
"v_mfma_f32_16x16x32_bf16 v[56:59], a[36:39], a[148:151], v[56:59]\n"
"L_st8533:\n"
"s_cbranch_execz L_retB\n"
"global_load_dwordx4 a[92:95], v0, s[34:35] offset:3072 sc1\n"
"v_mfma_f32_16x16x32_bf16 v[84:87], a[44:47], a[148:151], v[84:87]\n"
"v_mfma_f32_16x16x32_bf16 v[84:87], a[40:43], a[144:147], v[84:87]\n"
"v_xor_b32 v10, v10, v0\n"
"v_mfma_f32_16x16x32_bf16 a[4:7], a[40:43], a[152:155], a[4:7]\n"
"s_add_u32 s34, s34, 8192\n"
"v_mfma_f32_16x16x32_bf16 a[4:7], a[44:47], a[156:159], a[4:7]\n"
"v_mfma_f32_16x16x32_bf16 a[0:3], a[36:39], a[156:159], a[0:3]\n"
"v_mfma_f32_16x16x32_bf16 a[0:3], a[32:35], a[152:155], a[0:3]\n"
"s_waitcnt vmcnt(11)\n"
"v_and_b32 v10, 0x30, v10\n"
"v_mfma_f32_16x16x32_bf16 v[88:91], a[48:51], a[96:99], v[88:91]\n"
"v_mfma_f32_16x16x32_bf16 v[88:91], a[52:55], a[100:103], v[88:91]\n"
"global_load_dwordx4 a[32:35], v0, s[34:35] offset:-4096 sc1\n"
"v_mfma_f32_16x16x32_bf16 v[116:119], a[60:63], a[100:103], v[116:119]\n"
"v_add3_u32 v11, v10, v11, s64\n"
"v_mfma_f32_16x16x32_bf16 v[116:119], a[56:59], a[96:99], v[116:119]\n"
"buffer_load_dwordx4 v0, s[48:51], s75 offen offset:1024 sc1 lds\n"
"v_mfma_f32_16x16x32_bf16 v[120:123], a[56:59], a[104:107], v[120:123]\n"
"v_mfma_f32_16x16x32_bf16 v[120:123], a[60:63], a[108:111], v[120:123]\n"
"v_mfma_f32_16x16x32_bf16 v[92:95], a[52:55], a[108:111], v[92:95]\n"
"s_add_u32 s27, s27, 0x200000\n"
"v_mfma_f32_16x16x32_bf16 v[92:95], a[48:51], a[104:107], v[92:95]\n"
"v_mfma_f32_16x16x32_bf16 v[96:99], a[48:51], a[112:115], v[96:99]\n"
"v_mfma_f32_16x16x32_bf16 v[96:99], a[52:55], a[116:119], v[96:99]\n"
"global_load_dwordx4 a[36:39], v0, s[34:35] offset:-3072 sc1\n"
"v_mfma_f32_16x16x32_bf16 v[124:127], a[60:63], a[116:119], v[124:127]\n"
"ds_read_b64_tr_b16 v[24:25], v11 offset:12288\n"
"v_mfma_f32_16x16x32_bf16 v[124:127], a[56:59], a[112:115], v[124:127]\n"
"v_mfma_f32_16x16x32_bf16 v[128:131], a[56:59], a[120:123], v[128:131]\n"
"ds_read_b64_tr_b16 v[26:27], v11 offset:12864\n"
"v_mfma_f32_16x16x32_bf16 v[128:131], a[60:63], a[124:127], v[128:131]\n"
"v_mfma_f32_16x16x32_bf16 v[100:103], a[52:55], a[124:127], v[100:103]\n"
"ds_read_b64_tr_b16 v[28:29], v11 offset:12296\n"
"v_mfma_f32_16x16x32_bf16 v[100:103], a[48:51], a[120:123], v[100:103]\n"
"v_mfma_f32_16x16x32_bf16 v[104:107], a[48:51], a[128:131], v[104:107]\n"
"ds_read_b64_tr_b16 v[30:31], v11 offset:12872\n"
"v_mfma_f32_16x16x32_bf16 v[104:107], a[52:55], a[132:135], v[104:107]\n"
"global_load_dwordx4 a[40:43], v0, s[34:35] offset:-2048 sc1\n"
"v_mfma_f32_16x16x32_bf16 v[132:135], a[60:63], a[132:135], v[132:135]\n"
"buffer_load_dwordx4 v0, s[48:51], s75 offen offset:2048 sc1 lds\n"
"v_mfma_f32_16x16x32_bf16 v[132:135], a[56:59], a[128:131], v[132:135]\n"
"v_mfma_f32_16x16x32_bf16 v[136:139], a[56:59], a[136:139], v[136:139]\n"
"v_mfma_f32_16x16x32_bf16 v[136:139], a[60:63], a[140:143], v[136:139]\n"
"v_mfma_f32_16x16x32_bf16 v[108:111], a[52:55], a[140:143], v[108:111]\n"
"v_mfma_f32_16x16x32_bf16 v[108:111], a[48:51], a[136:139], v[108:111]\n"
"v_mfma_f32_16x16x32_bf16 v[112:115], a[48:51], a[144:147], v[112:115]\n"
"s_waitcnt lgkmcnt(0)\n"
"v_mfma_f32_16x16x32_bf16 v[112:115], a[52:55], a[148:151], v[112:115]\n"
"global_load_dwordx4 a[44:47], v0, s[34:35] offset:-1024 sc1\n"
"v_mfma_f32_16x16x32_bf16 v[140:143], a[60:63], a[148:151], v[140:143]\n"
"v_mfma_f32_16x16x32_bf16 v[140:143], a[56:59], a[144:147], v[140:143]\n"
"v_mfma_f32_16x16x32_bf16 a[12:15], a[56:59], a[152:155], a[12:15]\n"
"v_mfma_f32_16x16x32_bf16 a[12:15], a[60:63], a[156:159], a[12:15]\n"
"v_mfma_f32_16x16x32_bf16 a[8:11], a[52:55], a[156:159], a[8:11]\n"
"v_mfma_f32_16x16x32_bf16 a[8:11], a[48:51], a[152:155], a[8:11]\n"
"s_waitcnt vmcnt(11)\n"
"v_mfma_f32_16x16x32_bf16 v[144:147], a[64:67], a[96:99], v[144:147]\n"
"buffer_store_dwordx4 v[24:27], v0, s[36:39], s28 offen offset:0 sc1\n"
"buffer_load_dwordx4 v0, s[48:51], s75 offen offset:3072 sc1 lds\n"
"v_mfma_f32_16x16x32_bf16 v[144:147], a[68:71], a[100:103], v[144:147]\n"
"global_load_dwordx4 a[48:51], v0, s[34:35] offset:0 sc1\n"
"v_mfma_f32_16x16x32_bf16 v[172:175], a[76:79], a[100:103], v[172:175]\n"
"v_mfma_f32_16x16x32_bf16 v[172:175], a[72:75], a[96:99], v[172:175]\n"
"v_mfma_f32_16x16x32_bf16 v[176:179], a[72:75], a[104:107], v[176:179]\n"
"v_mfma_f32_16x16x32_bf16 v[176:179], a[76:79], a[108:111], v[176:179]\n"
"v_mfma_f32_16x16x32_bf16 v[148:151], a[68:71], a[108:111], v[148:151]\n"
"v_mfma_f32_16x16x32_bf16 v[148:151], a[64:67], a[104:107], v[148:151]\n"
"v_mfma_f32_16x16x32_bf16 v[152:155], a[64:67], a[112:115], v[152:155]\n"
"v_mfma_f32_16x16x32_bf16 v[152:155], a[68:71], a[116:119], v[152:155]\n"
"global_load_dwordx4 a[52:55], v0, s[34:35] offset:1024 sc1\n"
"v_mfma_f32_16x16x32_bf16 v[180:183], a[76:79], a[116:119], v[180:183]\n"
"v_mfma_f32_16x16x32_bf16 v[180:183], a[72:75], a[112:115], v[180:183]\n"
"v_mfma_f32_16x16x32_bf16 v[184:187], a[72:75], a[120:123], v[184:187]\n"
"v_mfma_f32_16x16x32_bf16 v[184:187], a[76:79], a[124:127], v[184:187]\n"
"v_mfma_f32_16x16x32_bf16 v[156:159], a[68:71], a[124:127], v[156:159]\n"
"v_mfma_f32_16x16x32_bf16 v[156:159], a[64:67], a[120:123], v[156:159]\n"
"s_add_u32 m0, s64, 4096\n"
"v_mfma_f32_16x16x32_bf16 v[160:163], a[64:67], a[128:131], v[160:163]\n"
"v_mfma_f32_16x16x32_bf16 v[160:163], a[68:71], a[132:135], v[160:163]\n"
"global_load_dwordx4 a[56:59], v0, s[34:35] offset:2048 sc1\n"
"v_mfma_f32_16x16x32_bf16 v[188:191], a[76:79], a[132:135], v[188:191]\n"
"v_mfma_f32_16x16x32_bf16 v[188:191], a[72:75], a[128:131], v[188:191]\n"
"v_mfma_f32_16x16x32_bf16 v[192:195], a[72:75], a[136:139], v[192:195]\n"
"v_mfma_f32_16x16x32_bf16 v[192:195], a[76:79], a[140:143], v[192:195]\n"
"v_mfma_f32_16x16x32_bf16 v[164:167], a[68:71], a[140:143], v[164:167]\n"
"v_mfma_f32_16x16x32_bf16 v[164:167], a[64:67], a[136:139], v[164:167]\n"
"v_mfma_f32_16x16x32_bf16 v[168:171], a[64:67], a[144:147], v[168:171]\n"
"v_mfma_f32_16x16x32_bf16 v[168:171], a[68:71], a[148:151], v[168:171]\n"
"global_load_dwordx4 a[60:63], v0, s[34:35] offset:3072 sc1\n"
"v_mfma_f32_16x16x32_bf16 v[196:199], a[76:79], a[148:151], v[196:199]\n"
"v_mfma_f32_16x16x32_bf16 v[196:199], a[72:75], a[144:147], v[196:199]\n"
"v_mfma_f32_16x16x32_bf16 a[20:23], a[72:75], a[152:155], a[20:23]\n"
"s_add_u32 s34, s34, 8192\n"
"v_mfma_f32_16x16x32_bf16 a[20:23], a[76:79], a[156:159], a[20:23]\n"
"v_mfma_f32_16x16x32_bf16 a[16:19], a[68:71], a[156:159], a[16:19]\n"
"buffer_load_dwordx4 v1, s[48:51], s75 offen offset:0 sc1 lds\n"
"v_mfma_f32_16x16x32_bf16 a[16:19], a[64:67], a[152:155], a[16:19]\n"
"s_waitcnt vmcnt(13)\n"
"v_mfma_f32_16x16x32_bf16 v[200:203], a[80:83], a[96:99], v[200:203]\n"
"v_mfma_f32_16x16x32_bf16 v[200:203], a[84:87], a[100:103], v[200:203]\n"
"global_load_dwordx4 a[64:67], v0, s[34:35] offset:-4096 sc1\n"
"s_add_u32 s65, s65, 1\n"
"v_mfma_f32_16x16x32_bf16 v[228:231], a[92:95], a[100:103], v[228:231]\n"
"ds_read_b128 a[100:103], v4 offset:17408\n"
"v_mfma_f32_16x16x32_bf16 v[228:231], a[88:91], a[96:99], v[228:231]\n"
"ds_read_b128 a[96:99], v4 offset:16384\n"
"v_mov_b32 v8, s65\n"
"v_mfma_f32_16x16x32_bf16 v[232:235], a[88:91], a[104:107], v[232:235]\n"
"v_mov_b32 v9, s60\n"
"v_mfma_f32_16x16x32_bf16 v[232:235], a[92:95], a[108:111], v[232:235]\n"
"v_mfma_f32_16x16x32_bf16 v[204:207], a[84:87], a[108:111], v[204:207]\n"
"buffer_store_dwordx2 v[8:9], off, s[56:59], %13 sc1\n"
"ds_read_b128 a[108:111], v4 offset:19456\n"
"v_mfma_f32_16x16x32_bf16 v[204:207], a[80:83], a[104:107], v[204:207]\n"
"ds_read_b128 a[104:107], v4 offset:18432\n"
"v_mfma_f32_16x16x32_bf16 v[208:211], a[80:83], a[112:115], v[208:211]\n"
"buffer_store_dwordx4 v[28:31], v0, s[36:39], s28 offen offset:2048 sc1\n"
"buffer_load_dwordx4 v1, s[48:51], s75 offen offset:1024 sc1 lds\n"
"v_mfma_f32_16x16x32_bf16 v[208:211], a[84:87], a[116:119], v[208:211]\n"
"global_load_dwordx4 a[68:71], v0, s[34:35] offset:-3072 sc1\n"
"v_mfma_f32_16x16x32_bf16 v[236:239], a[92:95], a[116:119], v[236:239]\n"
"ds_read_b128 a[116:119], v4 offset:21504\n"
"v_mfma_f32_16x16x32_bf16 v[236:239], a[88:91], a[112:115], v[236:239]\n"
"ds_read_b128 a[112:115], v4 offset:20480\n"
"v_mfma_f32_16x16x32_bf16 v[240:243], a[88:91], a[120:123], v[240:243]\n"
"v_mfma_f32_16x16x32_bf16 v[240:243], a[92:95], a[124:127], v[240:243]\n"
"v_mfma_f32_16x16x32_bf16 v[212:215], a[84:87], a[124:127], v[212:215]\n"
"ds_read_b128 a[124:127], v4 offset:23552\n"
"v_mfma_f32_16x16x32_bf16 v[212:215], a[80:83], a[120:123], v[212:215]\n"
"ds_read_b128 a[120:123], v4 offset:22528\n"
"v_mfma_f32_16x16x32_bf16 v[216:219], a[80:83], a[128:131], v[216:219]\n"
"ds_read_b64_tr_b16 v[24:25], v11 offset:12352\n"
"v_mfma_f32_16x16x32_bf16 v[216:219], a[84:87], a[132:135], v[216:219]\n"
"global_load_dwordx4 a[72:75], v0, s[34:35] offset:-2048 sc1\n"
"ds_read_b64_tr_b16 v[26:27], v11 offset:12800\n"
"v_mfma_f32_16x16x32_bf16 v[244:247], a[92:95], a[132:135], v[244:247]\n"
"ds_read_b128 a[132:135], v4 offset:25600\n"
"v_mfma_f32_16x16x32_bf16 v[244:247], a[88:91], a[128:131], v[244:247]\n"
"ds_read_b128 a[128:131], v4 offset:24576\n"
"v_mfma_f32_16x16x32_bf16 v[248:251], a[88:91], a[136:139], v[248:251]\n"
"ds_read_b64_tr_b16 v[28:29], v11 offset:12360\n"
"v_mfma_f32_16x16x32_bf16 v[248:251], a[92:95], a[140:143], v[248:251]\n"
"ds_read_b64_tr_b16 v[30:31], v11 offset:12808\n"
"v_mfma_f32_16x16x32_bf16 v[220:223], a[84:87], a[140:143], v[220:223]\n"
"ds_read_b128 a[140:143], v4 offset:27648\n"
"v_mfma_f32_16x16x32_bf16 v[220:223], a[80:83], a[136:139], v[220:223]\n"
"ds_read_b128 a[136:139], v4 offset:26624\n"
"v_mfma_f32_16x16x32_bf16 v[224:227], a[80:83], a[144:147], v[224:227]\n"
"v_mfma_f32_16x16x32_bf16 v[224:227], a[84:87], a[148:151], v[224:227]\n"
"global_load_dwordx4 a[76:79], v0, s[34:35] offset:-1024 sc1\n"
"v_mfma_f32_16x16x32_bf16 v[252:255], a[92:95], a[148:151], v[252:255]\n"
"ds_read_b128 a[148:151], v4 offset:29696\n"
"v_mfma_f32_16x16x32_bf16 v[252:255], a[88:91], a[144:147], v[252:255]\n"
"ds_read_b128 a[144:147], v4 offset:28672\n"
"v_mfma_f32_16x16x32_bf16 a[28:31], a[88:91], a[152:155], a[28:31]\n"
"buffer_load_dwordx4 v1, s[48:51], s75 offen offset:2048 sc1 lds\n"
"v_mfma_f32_16x16x32_bf16 a[28:31], a[92:95], a[156:159], a[28:31]\n"
"v_mfma_f32_16x16x32_bf16 a[24:27], a[84:87], a[156:159], a[24:27]\n"
"s_waitcnt lgkmcnt(4)\n"
"ds_read_b128 a[156:159], v4 offset:31744\n"
"v_mfma_f32_16x16x32_bf16 a[24:27], a[80:83], a[152:155], a[24:27]\n"
"s_waitcnt vmcnt(15)\n"
"ds_read_b128 a[152:155], v4 offset:30720\n"
"v_mfma_f32_16x16x32_bf16 v[32:35], a[32:35], a[96:99], v[32:35]\n"
"v_mfma_f32_16x16x32_bf16 v[32:35], a[36:39], a[100:103], v[32:35]\n"
"global_load_dwordx4 a[80:83], v0, s[34:35] offset:0 sc1\n"
"v_mfma_f32_16x16x32_bf16 v[60:63], a[44:47], a[100:103], v[60:63]\n"
"v_mfma_f32_16x16x32_bf16 v[60:63], a[40:43], a[96:99], v[60:63]\n"
"v_mfma_f32_16x16x32_bf16 v[64:67], a[40:43], a[104:107], v[64:67]\n"
"v_mfma_f32_16x16x32_bf16 v[64:67], a[44:47], a[108:111], v[64:67]\n"
"v_mfma_f32_16x16x32_bf16 v[36:39], a[36:39], a[108:111], v[36:39]\n"
"v_mfma_f32_16x16x32_bf16 v[36:39], a[32:35], a[104:107], v[36:39]\n"
"v_mfma_f32_16x16x32_bf16 v[40:43], a[32:35], a[112:115], v[40:43]\n"
"v_mfma_f32_16x16x32_bf16 v[40:43], a[36:39], a[116:119], v[40:43]\n"
"global_load_dwordx4 a[84:87], v0, s[34:35] offset:1024 sc1\n"
"v_mfma_f32_16x16x32_bf16 v[68:71], a[44:47], a[116:119], v[68:71]\n"
"buffer_load_dwordx4 v1, s[48:51], s75 offen offset:3072 sc1 lds\n"
"v_mfma_f32_16x16x32_bf16 v[68:71], a[40:43], a[112:115], v[68:71]\n"
"v_mfma_f32_16x16x32_bf16 v[72:75], a[40:43], a[120:123], v[72:75]\n"
"v_mfma_f32_16x16x32_bf16 v[72:75], a[44:47], a[124:127], v[72:75]\n"
"v_mfma_f32_16x16x32_bf16 v[44:47], a[36:39], a[124:127], v[44:47]\n"
"v_mfma_f32_16x16x32_bf16 v[44:47], a[32:35], a[120:123], v[44:47]\n"
"s_waitcnt lgkmcnt(4)\n"
"v_mfma_f32_16x16x32_bf16 v[48:51], a[32:35], a[128:131], v[48:51]\n"
"v_mfma_f32_16x16x32_bf16 v[48:51], a[36:39], a[132:135], v[48:51]\n"
"global_load_dwordx4 a[88:91], v0, s[34:35] offset:2048 sc1\n"
"v_mfma_f32_16x16x32_bf16 v[76:79], a[44:47], a[132:135], v[76:79]\n"
"v_mfma_f32_16x16x32_bf16 v[76:79], a[40:43], a[128:131], v[76:79]\n"
"v_mfma_f32_16x16x32_bf16 v[80:83], a[40:43], a[136:139], v[80:83]\n"
"v_mfma_f32_16x16x32_bf16 v[80:83], a[44:47], a[140:143], v[80:83]\n"
"v_mfma_f32_16x16x32_bf16 v[52:55], a[36:39], a[140:143], v[52:55]\n"
"v_mfma_f32_16x16x32_bf16 v[52:55], a[32:35], a[136:139], v[52:55]\n"
"s_waitcnt lgkmcnt(0)\n"
"v_mfma_f32_16x16x32_bf16 v[56:59], a[32:35], a[144:147], v[56:59]\n"
"v_mfma_f32_16x16x32_bf16 v[56:59], a[36:39], a[148:151], v[56:59]\n"
"s_add_u32 m0, s64, 8192\n"
"global_load_dwordx4 a[92:95], v0, s[34:35] offset:3072 sc1\n"
"v_mfma_f32_16x16x32_bf16 v[84:87], a[44:47], a[148:151], v[84:87]\n"
"v_mfma_f32_16x16x32_bf16 v[84:87], a[40:43], a[144:147], v[84:87]\n"
"v_mfma_f32_16x16x32_bf16 a[4:7], a[40:43], a[152:155], a[4:7]\n"
"s_add_u32 s34, s34, 8192\n"
"v_mfma_f32_16x16x32_bf16 a[4:7], a[44:47], a[156:159], a[4:7]\n"
"v_mfma_f32_16x16x32_bf16 a[0:3], a[36:39], a[156:159], a[0:3]\n"
"v_mfma_f32_16x16x32_bf16 a[0:3], a[32:35], a[152:155], a[0:3]\n"
"s_waitcnt vmcnt(14)\n"
"v_mfma_f32_16x16x32_bf16 v[88:91], a[48:51], a[96:99], v[88:91]\n"
"v_mfma_f32_16x16x32_bf16 v[88:91], a[52:55], a[100:103], v[88:91]\n"
"global_load_dwordx4 a[32:35], v0, s[34:35] offset:-4096 sc1\n"
"v_mfma_f32_16x16x32_bf16 v[116:119], a[60:63], a[100:103], v[116:119]\n"
"v_mfma_f32_16x16x32_bf16 v[116:119], a[56:59], a[96:99], v[116:119]\n"
"v_mfma_f32_16x16x32_bf16 v[120:123], a[56:59], a[104:107], v[120:123]\n"
"v_mfma_f32_16x16x32_bf16 v[120:123], a[60:63], a[108:111], v[120:123]\n"
"v_mfma_f32_16x16x32_bf16 v[92:95], a[52:55], a[108:111], v[92:95]\n"
"v_mfma_f32_16x16x32_bf16 v[92:95], a[48:51], a[104:107], v[92:95]\n"
"v_mfma_f32_16x16x32_bf16 v[96:99], a[48:51], a[112:115], v[96:99]\n"
"buffer_load_dwordx4 v0, s[48:51], s69 offen offset:0 sc1 lds\n"
"v_mfma_f32_16x16x32_bf16 v[96:99], a[52:55], a[116:119], v[96:99]\n"
"global_load_dwordx4 a[36:39], v0, s[34:35] offset:-3072 sc1\n"
"v_mfma_f32_16x16x32_bf16 v[124:127], a[60:63], a[116:119], v[124:127]\n"
"v_mfma_f32_16x16x32_bf16 v[124:127], a[56:59], a[112:115], v[124:127]\n"
"v_mfma_f32_16x16x32_bf16 v[128:131], a[56:59], a[120:123], v[128:131]\n"
"v_mfma_f32_16x16x32_bf16 v[128:131], a[60:63], a[124:127], v[128:131]\n"
"v_mfma_f32_16x16x32_bf16 v[100:103], a[52:55], a[124:127], v[100:103]\n"
"v_mfma_f32_16x16x32_bf16 v[100:103], a[48:51], a[120:123], v[100:103]\n"
"v_mfma_f32_16x16x32_bf16 v[104:107], a[48:51], a[128:131], v[104:107]\n"
"v_mfma_f32_16x16x32_bf16 v[104:107], a[52:55], a[132:135], v[104:107]\n"
"global_load_dwordx4 a[40:43], v0, s[34:35] offset:-2048 sc1\n"
"v_mfma_f32_16x16x32_bf16 v[132:135], a[60:63], a[132:135], v[132:135]\n"
"v_mfma_f32_16x16x32_bf16 v[132:135], a[56:59], a[128:131], v[132:135]\n"
"v_mfma_f32_16x16x32_bf16 v[136:139], a[56:59], a[136:139], v[136:139]\n"
"v_mfma_f32_16x16x32_bf16 v[136:139], a[60:63], a[140:143], v[136:139]\n"
"v_mfma_f32_16x16x32_bf16 v[108:111], a[52:55], a[140:143], v[108:111]\n"
"buffer_load_dwordx4 v0, s[48:51], s69 offen offset:1024 sc1 lds\n"
"v_mfma_f32_16x16x32_bf16 v[108:111], a[48:51], a[136:139], v[108:111]\n"
"v_mfma_f32_16x16x32_bf16 v[112:115], a[48:51], a[144:147], v[112:115]\n"
"v_mfma_f32_16x16x32_bf16 v[112:115], a[52:55], a[148:151], v[112:115]\n"
"global_load_dwordx4 a[44:47], v0, s[34:35] offset:-1024 sc1\n"
"v_mfma_f32_16x16x32_bf16 v[140:143], a[60:63], a[148:151], v[140:143]\n"
"v_mfma_f32_16x16x32_bf16 v[140:143], a[56:59], a[144:147], v[140:143]\n"
"v_mfma_f32_16x16x32_bf16 a[12:15], a[56:59], a[152:155], a[12:15]\n"
"v_mfma_f32_16x16x32_bf16 a[12:15], a[60:63], a[156:159], a[12:15]\n"
"v_mfma_f32_16x16x32_bf16 a[8:11], a[52:55], a[156:159], a[8:11]\n"
"v_mfma_f32_16x16x32_bf16 a[8:11], a[48:51], a[152:155], a[8:11]\n"
"s_waitcnt vmcnt(12)\n"
"v_mfma_f32_16x16x32_bf16 v[144:147], a[64:67], a[96:99], v[144:147]\n"
"v_mfma_f32_16x16x32_bf16 v[144:147], a[68:71], a[100:103], v[144:147]\n"
"global_load_dwordx4 a[48:51], v0, s[34:35] offset:0 sc1\n"
"v_mfma_f32_16x16x32_bf16 v[172:175], a[76:79], a[100:103], v[172:175]\n"
"v_mfma_f32_16x16x32_bf16 v[172:175], a[72:75], a[96:99], v[172:175]\n"
"v_mfma_f32_16x16x32_bf16 v[176:179], a[72:75], a[104:107], v[176:179]\n"
"v_mfma_f32_16x16x32_bf16 v[176:179], a[76:79], a[108:111], v[176:179]\n"
"buffer_load_dwordx4 v0, s[48:51], s69 offen offset:2048 sc1 lds\n"
"v_mfma_f32_16x16x32_bf16 v[148:151], a[68:71], a[108:111], v[148:151]\n"
"v_mfma_f32_16x16x32_bf16 v[148:151], a[64:67], a[104:107], v[148:151]\n"
"v_mfma_f32_16x16x32_bf16 v[152:155], a[64:67], a[112:115], v[152:155]\n"
"v_lshlrev_b32 v16, 16, v18\n"
"v_mfma_f32_16x16x32_bf16 v[152:155], a[68:71], a[116:119], v[152:155]\n"
"global_load_dwordx4 a[52:55], v0, s[34:35] offset:1024 sc1\n"
"v_and_b32 v17, s63, v18\n"
"v_mfma_f32_16x16x32_bf16 v[180:183], a[76:79], a[116:119], v[180:183]\n"
"v_lshlrev_b32 v18, 16, v19\n"
"v_mfma_f32_16x16x32_bf16 v[180:183], a[72:75], a[112:115], v[180:183]\n"
"v_and_b32 v19, s63, v19\n"
"v_mfma_f32_16x16x32_bf16 v[184:187], a[72:75], a[120:123], v[184:187]\n"
"v_add_f32 v32, v32, v16\n"
"v_mfma_f32_16x16x32_bf16 v[184:187], a[76:79], a[124:127], v[184:187]\n"
"v_add_f32 v33, v33, v17\n"
"v_add_f32 v34, v34, v18\n"
"v_mfma_f32_16x16x32_bf16 v[156:159], a[68:71], a[124:127], v[156:159]\n"
"v_add_f32 v35, v35, v19\n"
"v_mfma_f32_16x16x32_bf16 v[156:159], a[64:67], a[120:123], v[156:159]\n"
"v_add_f32 v36, v36, v16\n"
"v_mfma_f32_16x16x32_bf16 v[160:163], a[64:67], a[128:131], v[160:163]\n"
"v_add_f32 v37, v37, v17\n"
"v_mfma_f32_16x16x32_bf16 v[160:163], a[68:71], a[132:135], v[160:163]\n"
"global_load_dwordx4 a[56:59], v0, s[34:35] offset:2048 sc1\n"
"v_add_f32 v38, v38, v18\n"
"v_mfma_f32_16x16x32_bf16 v[188:191], a[76:79], a[132:135], v[188:191]\n"
"v_add_f32 v39, v39, v19\n"
"v_mfma_f32_16x16x32_bf16 v[188:191], a[72:75], a[128:131], v[188:191]\n"
"v_add_f32 v40, v40, v16\n"
"v_add_f32 v41, v41, v17\n"
"v_mfma_f32_16x16x32_bf16 v[192:195], a[72:75], a[136:139], v[192:195]\n"
"buffer_load_dwordx4 v0, s[48:51], s69 offen offset:3072 sc1 lds\n"
"v_mfma_f32_16x16x32_bf16 v[192:195], a[76:79], a[140:143], v[192:195]\n"
"v_add_f32 v42, v42, v18\n"
"v_mfma_f32_16x16x32_bf16 v[164:167], a[68:71], a[140:143], v[164:167]\n"
"v_add_f32 v43, v43, v19\n"
"v_mfma_f32_16x16x32_bf16 v[164:167], a[64:67], a[136:139], v[164:167]\n"
"v_add_f32 v44, v44, v16\n"
"v_add_f32 v45, v45, v17\n"
"v_mfma_f32_16x16x32_bf16 v[168:171], a[64:67], a[144:147], v[168:171]\n"
"v_add_f32 v46, v46, v18\n"
"v_mfma_f32_16x16x32_bf16 v[168:171], a[68:71], a[148:151], v[168:171]\n"
"global_load_dwordx4 a[60:63], v0, s[34:35] offset:3072 sc1\n"
"v_add_f32 v47, v47, v19\n"
"v_mfma_f32_16x16x32_bf16 v[196:199], a[76:79], a[148:151], v[196:199]\n"
"v_add_f32 v48, v48, v16\n"
"v_mfma_f32_16x16x32_bf16 v[196:199], a[72:75], a[144:147], v[196:199]\n"
"v_add_f32 v49, v49, v17\n"
"v_mfma_f32_16x16x32_bf16 a[20:23], a[72:75], a[152:155], a[20:23]\n"
"s_add_u32 s34, s34, 8192\n"
"v_add_f32 v50, v50, v18\n"
"v_mfma_f32_16x16x32_bf16 a[20:23], a[76:79], a[156:159], a[20:23]\n"
"v_add_f32 v51, v51, v19\n"
"v_mfma_f32_16x16x32_bf16 a[16:19], a[68:71], a[156:159], a[16:19]\n"
"v_add_f32 v52, v52, v16\n"
"v_mfma_f32_16x16x32_bf16 a[16:19], a[64:67], a[152:155], a[16:19]\n"
"s_waitcnt vmcnt(12)\n"
"v_add_f32 v53, v53, v17\n"
"v_mfma_f32_16x16x32_bf16 v[200:203], a[80:83], a[96:99], v[200:203]\n"
"v_add_f32 v54, v54, v18\n"
"v_add_f32 v55, v55, v19\n"
"v_mfma_f32_16x16x32_bf16 v[200:203], a[84:87], a[100:103], v[200:203]\n"
"global_load_dwordx4 a[64:67], v0, s[34:35] offset:-4096 sc1\n"
"v_add_f32 v56, v56, v16\n"
"v_mfma_f32_16x16x32_bf16 v[228:231], a[92:95], a[100:103], v[228:231]\n"
"s_add_u32 m0, s64, 12288\n"
"ds_read_b128 a[100:103], v5 offset:17408\n"
"v_add_f32 v57, v57, v17\n"
"v_mfma_f32_16x16x32_bf16 v[228:231], a[88:91], a[96:99], v[228:231]\n"
"ds_read_b128 a[96:99], v5 offset:16384\n"
"v_add_f32 v58, v58, v18\n"
"v_mfma_f32_16x16x32_bf16 v[232:235], a[88:91], a[104:107], v[232:235]\n"
"v_add_f32 v59, v59, v19\n"
"v_mfma_f32_16x16x32_bf16 v[232:235], a[92:95], a[108:111], v[232:235]\n"
"v_accvgpr_read_b32 v12, a0\n"
"v_mfma_f32_16x16x32_bf16 v[204:207], a[84:87], a[108:111], v[204:207]\n"
"ds_read_b128 a[108:111], v5 offset:19456\n"
"v_accvgpr_read_b32 v13, a1\n"
"v_mfma_f32_16x16x32_bf16 v[204:207], a[80:83], a[104:107], v[204:207]\n"
"ds_read_b128 a[104:107], v5 offset:18432\n"
"v_accvgpr_read_b32 v14, a2\n"
"v_mfma_f32_16x16x32_bf16 v[208:211], a[80:83], a[112:115], v[208:211]\n"
"v_accvgpr_read_b32 v15, a3\n"
"v_mfma_f32_16x16x32_bf16 v[208:211], a[84:87], a[116:119], v[208:211]\n"
"global_load_dwordx4 a[68:71], v0, s[34:35] offset:-3072 sc1\n"
"v_add_f32 v12, v12, v16\n"
"v_mfma_f32_16x16x32_bf16 v[236:239], a[92:95], a[116:119], v[236:239]\n"
"ds_read_b128 a[116:119], v5 offset:21504\n"
"v_add_f32 v13, v13, v17\n"
"v_mfma_f32_16x16x32_bf16 v[236:239], a[88:91], a[112:115], v[236:239]\n"
"ds_read_b128 a[112:115], v5 offset:20480\n"
"v_add_f32 v14, v14, v18\n"
"v_mfma_f32_16x16x32_bf16 v[240:243], a[88:91], a[120:123], v[240:243]\n"
"v_add_f32 v15, v15, v19\n"
"v_accvgpr_write_b32 a0, v12\n"
"v_mfma_f32_16x16x32_bf16 v[240:243], a[92:95], a[124:127], v[240:243]\n"
"v_accvgpr_write_b32 a1, v13\n"
"v_mfma_f32_16x16x32_bf16 v[212:215], a[84:87], a[124:127], v[212:215]\n"
"ds_read_b128 a[124:127], v5 offset:23552\n"
"v_accvgpr_write_b32 a2, v14\n"
"v_mfma_f32_16x16x32_bf16 v[212:215], a[80:83], a[120:123], v[212:215]\n"
"ds_read_b128 a[120:123], v5 offset:22528\n"
"v_accvgpr_write_b32 a3, v15\n"
"v_mfma_f32_16x16x32_bf16 v[216:219], a[80:83], a[128:131], v[216:219]\n"
"buffer_load_dwordx4 v1, s[48:51], s69 offen offset:0 sc1 lds\n"
"v_mfma_f32_16x16x32_bf16 v[216:219], a[84:87], a[132:135], v[216:219]\n"
"global_load_dwordx4 a[72:75], v0, s[34:35] offset:-2048 sc1\n"
"v_lshrrev_b32 v16, 4, v0\n"
"v_mfma_f32_16x16x32_bf16 v[244:247], a[92:95], a[132:135], v[244:247]\n"
"ds_read_b128 a[132:135], v5 offset:25600\n"
"v_and_b32 v16, 0x30, v16\n"
"v_mfma_f32_16x16x32_bf16 v[244:247], a[88:91], a[128:131], v[244:247]\n"
"ds_read_b128 a[128:131], v5 offset:24576\n"
"buffer_load_dwordx2 v[18:19], v16, s[44:47], s61 offen offset:64\n"
"v_mfma_f32_16x16x32_bf16 v[248:251], a[88:91], a[136:139], v[248:251]\n"
"s_getpc_b64 s[96:97]\n"
"L_c6_:\n"
"s_add_u32 s96, s96, L_last-L_c6_\n"
"s_addc_u32 s97, s97, 0\n"
"v_mfma_f32_16x16x32_bf16 v[248:251], a[92:95], a[140:143], v[248:251]\n"
"s_lshr_b32 s66, %26, 3\n"
"v_mfma_f32_16x16x32_bf16 v[220:223], a[84:87], a[140:143], v[220:223]\n"
"s_lshl2_add_u32 s66, s66, %16\n"
"ds_read_b128 a[140:143], v5 offset:27648\n"
"v_mfma_f32_16x16x32_bf16 v[220:223], a[80:83], a[136:139], v[220:223]\n"
"ds_read_b128 a[136:139], v5 offset:26624\n"
"v_mfma_f32_16x16x32_bf16 v[224:227], a[80:83], a[144:147], v[224:227]\n"
"s_mul_i32 s66, s66, 256\n"
"v_mfma_f32_16x16x32_bf16 v[224:227], a[84:87], a[148:151], v[224:227]\n"
"global_load_dwordx4 a[76:79], v0, s[34:35] offset:-1024 sc1\n"
"v_mfma_f32_16x16x32_bf16 v[252:255], a[92:95], a[148:151], v[252:255]\n"
"ds_read_b128 a[148:151], v5 offset:29696\n"
"v_mfma_f32_16x16x32_bf16 v[252:255], a[88:91], a[144:147], v[252:255]\n"
"s_min_u32 s66, s66, L_headF-L_last-256\n"
"ds_read_b128 a[144:147], v5 offset:28672\n"
"v_mfma_f32_16x16x32_bf16 a[28:31], a[88:91], a[152:155], a[28:31]\n"
"v_mfma_f32_16x16x32_bf16 a[28:31], a[92:95], a[156:159], a[28:31]\n"
"v_min_u32 v11, 16, v0\n"
"v_mfma_f32_16x16x32_bf16 a[24:27], a[84:87], a[156:159], a[24:27]\n"
"ds_read_b128 a[156:159], v5 offset:31744\n"
"v_mfma_f32_16x16x32_bf16 a[24:27], a[80:83], a[152:155], a[24:27]\n"
"s_waitcnt vmcnt(12) lgkmcnt(11)\n"
"ds_read_b128 a[152:155], v5 offset:30720\n"
"v_mfma_f32_16x16x32_bf16 v[32:35], a[32:35], a[96:99], v[32:35]\n"
"buffer_load_dwordx4 v1, s[48:51], s69 offen offset:1024 sc1 lds\n"
"v_mfma_f32_16x16x32_bf16 v[32:35], a[36:39], a[100:103], v[32:35]\n"
"global_load_dwordx4 a[80:83], v0, s[34:35] offset:0 sc1\n"
"v_mfma_f32_16x16x32_bf16 v[60:63], a[44:47], a[100:103], v[60:63]\n"
"v_lshl_add_u32 v11, v11, 3, s66\n"
"v_mfma_f32_16x16x32_bf16 v[60:63], a[40:43], a[96:99], v[60:63]\n"
"global_load_dword v10, v11, s[96:97]\n"
"v_mfma_f32_16x16x32_bf16 v[64:67], a[40:43], a[104:107], v[64:67]\n"
"v_mfma_f32_16x16x32_bf16 v[64:67], a[44:47], a[108:111], v[64:67]\n"
"v_mfma_f32_16x16x32_bf16 v[36:39], a[36:39], a[108:111], v[36:39]\n"
"v_mfma_f32_16x16x32_bf16 v[36:39], a[32:35], a[104:107], v[36:39]\n"
"s_waitcnt lgkmcnt(8)\n"
"v_mfma_f32_16x16x32_bf16 v[40:43], a[32:35], a[112:115], v[40:43]\n"
"v_mfma_f32_16x16x32_bf16 v[40:43], a[36:39], a[116:119], v[40:43]\n"
"global_load_dwordx4 a[84:87], v0, s[34:35] offset:1024 sc1\n"
"v_mfma_f32_16x16x32_bf16 v[68:71], a[44:47], a[116:119], v[68:71]\n"
"v_mfma_f32_16x16x32_bf16 v[68:71], a[40:43], a[112:115], v[68:71]\n"
"v_mfma_f32_16x16x32_bf16 v[72:75], a[40:43], a[120:123], v[72:75]\n"
"v_mfma_f32_16x16x32_bf16 v[72:75], a[44:47], a[124:127], v[72:75]\n"
"v_mfma_f32_16x16x32_bf16 v[44:47], a[36:39], a[124:127], v[44:47]\n"
"buffer_load_dwordx4 v1, s[48:51], s69 offen offset:2048 sc1 lds\n"
"v_mfma_f32_16x16x32_bf16 v[44:47], a[32:35], a[120:123], v[44:47]\n"
"s_waitcnt lgkmcnt(4)\n"
"v_mfma_f32_16x16x32_bf16 v[48:51], a[32:35], a[128:131], v[48:51]\n"
"v_mfma_f32_16x16x32_bf16 v[48:51], a[36:39], a[132:135], v[48:51]\n"
"global_load_dwordx4 a[88:91], v0, s[34:35] offset:2048 sc1\n"
"v_mfma_f32_16x16x32_bf16 v[76:79], a[44:47], a[132:135], v[76:79]\n"
"v_mfma_f32_16x16x32_bf16 v[76:79], a[40:43], a[128:131], v[76:79]\n"
"v_mfma_f32_16x16x32_bf16 v[80:83], a[40:43], a[136:139], v[80:83]\n"
"v_mfma_f32_16x16x32_bf16 v[80:83], a[44:47], a[140:143], v[80:83]\n"
"v_mfma_f32_16x16x32_bf16 v[52:55], a[36:39], a[140:143], v[52:55]\n"
"v_mfma_f32_16x16x32_bf16 v[52:55], a[32:35], a[136:139], v[52:55]\n"
"s_waitcnt lgkmcnt(0)\n"
"v_mfma_f32_16x16x32_bf16 v[56:59], a[32:35], a[144:147], v[56:59]\n"
"v_mfma_f32_16x16x32_bf16 v[56:59], a[36:39], a[148:151], v[56:59]\n"
"global_load_dwordx4 a[92:95], v0, s[34:35] offset:3072 sc1\n"
"v_mfma_f32_16x16x32_bf16 v[84:87], a[44:47], a[148:151], v[84:87]\n"
"v_mfma_f32_16x16x32_bf16 v[84:87], a[40:43], a[144:147], v[84:87]\n"
"v_mfma_f32_16x16x32_bf16 a[4:7], a[40:43], a[152:155], a[4:7]\n"
"s_add_u32 s34, s34, 8192\n"
"v_mfma_f32_16x16x32_bf16 a[4:7], a[44:47], a[156:159], a[4:7]\n"
"buffer_load_dwordx4 v1, s[48:51], s69 offen offset:3072 sc1 lds\n"
"v_mfma_f32_16x16x32_bf16 a[0:3], a[36:39], a[156:159], a[0:3]\n"
"v_mfma_f32_16x16x32_bf16 a[0:3], a[32:35], a[152:155], a[0:3]\n"
"s_waitcnt vmcnt(14)\n"
"v_mfma_f32_16x16x32_bf16 v[88:91], a[48:51], a[96:99], v[88:91]\n"
"v_mfma_f32_16x16x32_bf16 v[88:91], a[52:55], a[100:103], v[88:91]\n"
"global_load_dwordx4 a[32:35], v0, s[34:35] offset:-4096 sc1\n"
"v_mfma_f32_16x16x32_bf16 v[116:119], a[60:63], a[100:103], v[116:119]\n"
"v_mfma_f32_16x16x32_bf16 v[116:119], a[56:59], a[96:99], v[116:119]\n"
"v_mfma_f32_16x16x32_bf16 v[120:123], a[56:59], a[104:107], v[120:123]\n"
"v_mfma_f32_16x16x32_bf16 v[120:123], a[60:63], a[108:111], v[120:123]\n"
"v_mfma_f32_16x16x32_bf16 v[92:95], a[52:55], a[108:111], v[92:95]\n"
"v_mfma_f32_16x16x32_bf16 v[92:95], a[48:51], a[104:107], v[92:95]\n"
"v_mfma_f32_16x16x32_bf16 v[96:99], a[48:51], a[112:115], v[96:99]\n"
"v_mfma_f32_16x16x32_bf16 v[96:99], a[52:55], a[116:119], v[96:99]\n"
"global_load_dwordx4 a[36:39], v0, s[34:35] offset:-3072 sc1\n"
"v_mfma_f32_16x16x32_bf16 v[124:127], a[60:63], a[116:119], v[124:127]\n"
"v_mfma_f32_16x16x32_bf16 v[124:127], a[56:59], a[112:115], v[124:127]\n"
"v_mfma_f32_16x16x32_bf16 v[128:131], a[56:59], a[120:123], v[128:131]\n"
"v_mfma_f32_16x16x32_bf16 v[128:131], a[60:63], a[124:127], v[128:131]\n"
"v_mfma_f32_16x16x32_bf16 v[100:103], a[52:55], a[124:127], v[100:103]\n"
"v_mfma_f32_16x16x32_bf16 v[100:103], a[48:51], a[120:123], v[100:103]\n"
"v_mfma_f32_16x16x32_bf16 v[104:107], a[48:51], a[128:131], v[104:107]\n"
"buffer_store_dwordx4 v[24:27], v1, s[36:39], s28 offen offset:0 sc1\n"
"buffer_load_dwordx2 v[8:9], off, s[56:59], %14 sc1\n"
"v_mfma_f32_16x16x32_bf16 v[104:107], a[52:55], a[132:135], v[104:107]\n"
"global_load_dwordx4 a[40:43], v0, s[34:35] offset:-2048 sc1\n"
"v_mfma_f32_16x16x32_bf16 v[132:135], a[60:63], a[132:135], v[132:135]\n"
"v_mfma_f32_16x16x32_bf16 v[132:135], a[56:59], a[128:131], v[132:135]\n"
"v_mfma_f32_16x16x32_bf16 v[136:139], a[56:59], a[136:139], v[136:139]\n"
"v_mfma_f32_16x16x32_bf16 v[136:139], a[60:63], a[140:143], v[136:139]\n"
"v_mfma_f32_16x16x32_bf16 v[108:111], a[52:55], a[140:143], v[108:111]\n"
"v_mfma_f32_16x16x32_bf16 v[108:111], a[48:51], a[136:139], v[108:111]\n"
"v_mfma_f32_16x16x32_bf16 v[112:115], a[48:51], a[144:147], v[112:115]\n"
"v_mfma_f32_16x16x32_bf16 v[112:115], a[52:55], a[148:151], v[112:115]\n"
"global_load_dwordx4 a[44:47], v0, s[34:35] offset:-1024 sc1\n"
"v_mfma_f32_16x16x32_bf16 v[140:143], a[60:63], a[148:151], v[140:143]\n"
"v_mfma_f32_16x16x32_bf16 v[140:143], a[56:59], a[144:147], v[140:143]\n"
"v_mfma_f32_16x16x32_bf16 a[12:15], a[56:59], a[152:155], a[12:15]\n"
"v_mfma_f32_16x16x32_bf16 a[12:15], a[60:63], a[156:159], a[12:15]\n"
"v_mfma_f32_16x16x32_bf16 a[8:11], a[52:55], a[156:159], a[8:11]\n"
"v_mfma_f32_16x16x32_bf16 a[8:11], a[48:51], a[152:155], a[8:11]\n"
"s_waitcnt vmcnt(14)\n"
"v_mfma_f32_16x16x32_bf16 v[144:147], a[64:67], a[96:99], v[144:147]\n"
"v_mfma_f32_16x16x32_bf16 v[144:147], a[68:71], a[100:103], v[144:147]\n"
"global_load_dwordx4 a[48:51], v0, s[34:35] offset:0 sc1\n"
"v_mfma_f32_16x16x32_bf16 v[172:175], a[76:79], a[100:103], v[172:175]\n"
"v_mfma_f32_16x16x32_bf16 v[172:175], a[72:75], a[96:99], v[172:175]\n"
"v_mfma_f32_16x16x32_bf16 v[176:179], a[72:75], a[104:107], v[176:179]\n"
"v_mfma_f32_16x16x32_bf16 v[176:179], a[76:79], a[108:111], v[176:179]\n"
"v_mfma_f32_16x16x32_bf16 v[148:151], a[68:71], a[108:111], v[148:151]\n"
"v_mfma_f32_16x16x32_bf16 v[148:151], a[64:67], a[104:107], v[148:151]\n"
"v_mfma_f32_16x16x32_bf16 v[152:155], a[64:67], a[112:115], v[152:155]\n"
"v_lshlrev_b32 v20, 16, v22\n"
"v_mfma_f32_16x16x32_bf16 v[152:155], a[68:71], a[116:119], v[152:155]\n"
"global_load_dwordx4 a[52:55], v0, s[34:35] offset:1024 sc1\n"
"v_and_b32 v21, s63, v22\n"
"v_mfma_f32_16x16x32_bf16 v[180:183], a[76:79], a[116:119], v[180:183]\n"
"v_lshlrev_b32 v22, 16, v23\n"
"v_mfma_f32_16x16x32_bf16 v[180:183], a[72:75], a[112:115], v[180:183]\n"
"v_and_b32 v23, s63, v23\n"
"v_mfma_f32_16x16x32_bf16 v[184:187], a[72:75], a[120:123], v[184:187]\n"
"v_add_f32 v60, v60, v20\n"
"v_mfma_f32_16x16x32_bf16 v[184:187], a[76:79], a[124:127], v[184:187]\n"
"v_add_f32 v61, v61, v21\n"
"v_mfma_f32_16x16x32_bf16 v[156:159], a[68:71], a[124:127], v[156:159]\n"
"buffer_store_dwordx4 v[28:31], v1, s[36:39], s28 offen offset:2048 sc1\n"
"v_add_f32 v62, v62, v22\n"
"v_add_f32 v63, v63, v23\n"
"v_add_f32 v64, v64, v20\n"
"v_mfma_f32_16x16x32_bf16 v[156:159], a[64:67], a[120:123], v[156:159]\n"
"v_add_f32 v65, v65, v21\n"
"v_mfma_f32_16x16x32_bf16 v[160:163], a[64:67], a[128:131], v[160:163]\n"
"v_add_f32 v66, v66, v22\n"
"v_mfma_f32_16x16x32_bf16 v[160:163], a[68:71], a[132:135], v[160:163]\n"
"global_load_dwordx4 a[56:59], v0, s[34:35] offset:2048 sc1\n"
"v_mfma_f32_16x16x32_bf16 v[188:191], a[76:79], a[132:135], v[188:191]\n"
"v_add_f32 v67, v67, v23\n"
"v_mfma_f32_16x16x32_bf16 v[188:191], a[72:75], a[128:131], v[188:191]\n"
"v_add_f32 v68, v68, v20\n"
"v_mfma_f32_16x16x32_bf16 v[192:195], a[72:75], a[136:139], v[192:195]\n"
"v_add_f32 v69, v69, v21\n"
"v_mfma_f32_16x16x32_bf16 v[192:195], a[76:79], a[140:143], v[192:195]\n"
"v_add_f32 v70, v70, v22\n"
"v_mfma_f32_16x16x32_bf16 v[164:167], a[68:71], a[140:143], v[164:167]\n"
"v_add_f32 v71, v71, v23\n"
"v_mfma_f32_16x16x32_bf16 v[164:167], a[64:67], a[136:139], v[164:167]\n"
"v_add_f32 v72, v72, v20\n"
"v_add_f32 v73, v73, v21\n"
"v_mfma_f32_16x16x32_bf16 v[168:171], a[64:67], a[144:147], v[168:171]\n"
"v_add_f32 v74, v74, v22\n"
"v_mfma_f32_16x16x32_bf16 v[168:171], a[68:71], a[148:151], v[168:171]\n"
"global_load_dwordx4 a[60:63], v0, s[34:35] offset:3072 sc1\n"
"v_add_f32 v75, v75, v23\n"
"v_mfma_f32_16x16x32_bf16 v[196:199], a[76:79], a[148:151], v[196:199]\n"
"v_add_f32 v76, v76, v20\n"
"v_mfma_f32_16x16x32_bf16 v[196:199], a[72:75], a[144:147], v[196:199]\n"
"v_add_f32 v77, v77, v21\n"
"v_mfma_f32_16x16x32_bf16 a[20:23], a[72:75], a[152:155], a[20:23]\n"
"s_add_u32 s34, s34, 8192\n"
"v_add_f32 v78, v78, v22\n"
"v_mfma_f32_16x16x32_bf16 a[20:23], a[76:79], a[156:159], a[20:23]\n"
"v_add_f32 v79, v79, v23\n"
"v_mfma_f32_16x16x32_bf16 a[16:19], a[68:71], a[156:159], a[16:19]\n"
"v_add_f32 v80, v80, v20\n"
"v_mfma_f32_16x16x32_bf16 a[16:19], a[64:67], a[152:155], a[16:19]\n"
"s_waitcnt vmcnt(12)\n"
"v_add_f32 v81, v81, v21\n"
"v_mfma_f32_16x16x32_bf16 v[200:203], a[80:83], a[96:99], v[200:203]\n"
"v_add_f32 v82, v82, v22\n"
"v_add_f32 v83, v83, v23\n"
"v_mfma_f32_16x16x32_bf16 v[200:203], a[84:87], a[100:103], v[200:203]\n"
"global_load_dwordx4 a[64:67], v0, s[34:35] offset:-4096 sc1\n"
"v_add_f32 v84, v84, v20\n"
"v_mfma_f32_16x16x32_bf16 v[228:231], a[92:95], a[100:103], v[228:231]\n"
"s_waitcnt vmcnt(12)\n"
"ds_read_b128 a[100:103], v2 offset:1024\n"
"v_add_f32 v85, v85, v21\n"
"v_mfma_f32_16x16x32_bf16 v[228:231], a[88:91], a[96:99], v[228:231]\n"
"ds_read_b128 a[96:99], v2 offset:0\n"
"v_add_f32 v86, v86, v22\n"
"v_mfma_f32_16x16x32_bf16 v[232:235], a[88:91], a[104:107], v[232:235]\n"
"v_add_f32 v87, v87, v23\n"
"v_mfma_f32_16x16x32_bf16 v[232:235], a[92:95], a[108:111], v[232:235]\n"
"L_st9095:\n"
"s_cbranch_execz L_retB\n"
"v_accvgpr_read_b32 v12, a4\n"
"v_mfma_f32_16x16x32_bf16 v[204:207], a[84:87], a[108:111], v[204:207]\n"
"ds_read_b128 a[108:111], v2 offset:3072\n"
"v_accvgpr_read_b32 v13, a5\n"
"v_mfma_f32_16x16x32_bf16 v[204:207], a[80:83], a[104:107], v[204:207]\n"
"ds_read_b128 a[104:107], v2 offset:2048\n"
"v_accvgpr_read_b32 v14, a6\n"
"v_mfma_f32_16x16x32_bf16 v[208:211], a[80:83], a[112:115], v[208:211]\n"
"v_accvgpr_read_b32 v15, a7\n"
"v_mfma_f32_16x16x32_bf16 v[208:211], a[84:87], a[116:119], v[208:211]\n"
"global_load_dwordx4 a[68:71], v0, s[34:35] offset:-3072 sc1\n"
"v_add_f32 v12, v12, v20\n"
"v_mfma_f32_16x16x32_bf16 v[236:239], a[92:95], a[116:119], v[236:239]\n"
"ds_read_b128 a[116:119], v2 offset:5120\n"
"v_add_f32 v13, v13, v21\n"
"v_mfma_f32_16x16x32_bf16 v[236:239], a[88:91], a[112:115], v[236:239]\n"
"ds_read_b128 a[112:115], v2 offset:4096\n"
"v_add_f32 v14, v14, v22\n"
"v_mfma_f32_16x16x32_bf16 v[240:243], a[88:91], a[120:123], v[240:243]\n"
"v_add_f32 v15, v15, v23\n"
"v_accvgpr_write_b32 a4, v12\n"
"v_mfma_f32_16x16x32_bf16 v[240:243], a[92:95], a[124:127], v[240:243]\n"
"v_accvgpr_write_b32 a5, v13\n"
"v_mfma_f32_16x16x32_bf16 v[212:215], a[84:87], a[124:127], v[212:215]\n"
"ds_read_b128 a[124:127], v2 offset:7168\n"
"v_accvgpr_write_b32 a6, v14\n"
"v_mfma_f32_16x16x32_bf16 v[212:215], a[80:83], a[120:123], v[212:215]\n"
"ds_read_b128 a[120:123], v2 offset:6144\n"
"v_accvgpr_write_b32 a7, v15\n"
"v_mfma_f32_16x16x32_bf16 v[216:219], a[80:83], a[128:131], v[216:219]\n"
"s_add_u32 s28, s28, 0x40000\n"
"v_mfma_f32_16x16x32_bf16 v[216:219], a[84:87], a[132:135], v[216:219]\n"
"global_load_dwordx4 a[72:75], v0, s[34:35] offset:-2048 sc1\n"
"v_lshrrev_b32 v20, 4, v0\n"
"v_mfma_f32_16x16x32_bf16 v[244:247], a[92:95], a[132:135], v[244:247]\n"
"s_add_u32 s67, s78, s62\n"
"ds_read_b128 a[132:135], v2 offset:9216\n"
"v_and_b32 v20, 0x30, v20\n"
"v_mfma_f32_16x16x32_bf16 v[244:247], a[88:91], a[128:131], v[244:247]\n"
"ds_read_b128 a[128:131], v2 offset:8192\n"
"buffer_load_dwordx2 v[22:23], v20, s[44:47], s61 offen offset:72\n"
"v_mfma_f32_16x16x32_bf16 v[248:251], a[88:91], a[136:139], v[248:251]\n"
"s_add_u32 s67, s67, 1\n"
"v_mfma_f32_16x16x32_bf16 v[248:251], a[92:95], a[140:143], v[248:251]\n"
"v_mfma_f32_16x16x32_bf16 v[220:223], a[84:87], a[140:143], v[220:223]\n"
"ds_read_b128 a[140:143], v2 offset:11264\n"
"v_mov_b32 v24, s67\n"
"v_mfma_f32_16x16x32_bf16 v[220:223], a[80:83], a[136:139], v[220:223]\n"
"ds_read_b128 a[136:139], v2 offset:10240\n"
"v_mov_b32 v25, s79\n"
"v_mfma_f32_16x16x32_bf16 v[224:227], a[80:83], a[144:147], v[224:227]\n"
"v_mfma_f32_16x16x32_bf16 v[224:227], a[84:87], a[148:151], v[224:227]\n"
"s_waitcnt vmcnt(6)\n"
"global_load_dwordx4 a[76:79], v0, s[34:35] offset:-1024 sc1\n"
"buffer_store_dwordx2 v[24:25], off, s[56:59], %24 sc1\n"
"v_mfma_f32_16x16x32_bf16 v[252:255], a[92:95], a[148:151], v[252:255]\n"
"ds_read_b128 a[148:151], v2 offset:13312\n"
"v_mfma_f32_16x16x32_bf16 v[252:255], a[88:91], a[144:147], v[252:255]\n"
"ds_read_b128 a[144:147], v2 offset:12288\n"
"v_mfma_f32_16x16x32_bf16 a[28:31], a[88:91], a[152:155], a[28:31]\n"
"v_mfma_f32_16x16x32_bf16 a[28:31], a[92:95], a[156:159], a[28:31]\n"
"v_mfma_f32_16x16x32_bf16 a[24:27], a[84:87], a[156:159], a[24:27]\n"
"s_cmp_eq_u32 s62, 5\n"
"ds_read_b128 a[156:159], v2 offset:15360\n"
"v_mfma_f32_16x16x32_bf16 a[24:27], a[80:83], a[152:155], a[24:27]\n"
"ds_read_b128 a[152:155], v2 offset:14336\n"
"s_cbranch_scc1 L_last\n"
"s_branch L_P4\n"
"L_last:\n"
"L_st9164:\n"
"s_cbranch_execz L_retB\n"
"s_waitcnt vmcnt(9) lgkmcnt(12)\n"
"v_mfma_f32_16x16x32_bf16 v[32:35], a[32:35], a[96:99], v[32:35]\n"
"s_add_u32 s34, s36, 1454080\n"
"s_add_u32 s48, s48, 131072\n"
"s_addc_u32 s49, s49, 0\n"
"v_mfma_f32_16x16x32_bf16 v[32:35], a[36:39], a[100:103], v[32:35]\n"
"s_mov_b64 exec, 0\n"
"s_cmp_eq_u32 %16, 0\n"
"s_cbranch_scc1 L_st9674\n"
"s_cmp_eq_u32 %16, 1\n"
"s_cbranch_scc1 L_st10279\n"
"s_cmp_eq_u32 %16, 2\n"
"s_cbranch_scc1 L_st10887\n"
"s_branch L_st11493\n"
"L_retC:\n"
"s_mov_b64 exec, -1\n"
"s_nop 3\n"
"global_load_dwordx4 a[80:83], v0, s[34:35] offset:0 sc1\n"
"v_mfma_f32_16x16x32_bf16 v[60:63], a[44:47], a[100:103], v[60:63]\n"
"s_barrier\n"
"v_mfma_f32_16x16x32_bf16 v[60:63], a[40:43], a[96:99], v[60:63]\n"
"v_mfma_f32_16x16x32_bf16 v[64:67], a[40:43], a[104:107], v[64:67]\n"
"v_mfma_f32_16x16x32_bf16 v[64:67], a[44:47], a[108:111], v[64:67]\n"
"v_mfma_f32_16x16x32_bf16 v[36:39], a[36:39], a[108:111], v[36:39]\n"
"v_subrev_u32 v8, s65, v8\n"
"v_mfma_f32_16x16x32_bf16 v[36:39], a[32:35], a[104:107], v[36:39]\n"
"s_waitcnt lgkmcnt(8)\n"
"v_xor_b32 v9, s79, v9\n"
"v_mfma_f32_16x16x32_bf16 v[40:43], a[32:35], a[112:115], v[40:43]\n"
"v_or_b32 v8, v8, v9\n"
"v_mfma_f32_16x16x32_bf16 v[40:43], a[36:39], a[116:119], v[40:43]\n"
"global_load_dwordx4 a[84:87], v0, s[34:35] offset:1024 sc1\n"
"v_cmp_lt_u32 vcc, 15, v8\n"
"v_mfma_f32_16x16x32_bf16 v[68:71], a[44:47], a[116:119], v[68:71]\n"
"s_cbranch_vccnz L_slow4\n"
"L_back4:\n"
"v_mfma_f32_16x16x32_bf16 v[68:71], a[40:43], a[112:115], v[68:71]\n"
"v_mfma_f32_16x16x32_bf16 v[72:75], a[40:43], a[120:123], v[72:75]\n"
"v_mfma_f32_16x16x32_bf16 v[72:75], a[44:47], a[124:127], v[72:75]\n"
"v_mfma_f32_16x16x32_bf16 v[44:47], a[36:39], a[124:127], v[44:47]\n"
"v_mfma_f32_16x16x32_bf16 v[44:47], a[32:35], a[120:123], v[44:47]\n"
"s_add_u32 m0, s64, 16384\n"
"s_waitcnt lgkmcnt(4)\n"
"v_mfma_f32_16x16x32_bf16 v[48:51], a[32:35], a[128:131], v[48:51]\n"
"v_mfma_f32_16x16x32_bf16 v[48:51], a[36:39], a[132:135], v[48:51]\n"
"global_load_dwordx4 a[88:91], v0, s[34:35] offset:2048 sc1\n"
"v_mfma_f32_16x16x32_bf16 v[76:79], a[44:47], a[132:135], v[76:79]\n"
"v_mfma_f32_16x16x32_bf16 v[76:79], a[40:43], a[128:131], v[76:79]\n"
"v_mfma_f32_16x16x32_bf16 v[80:83], a[40:43], a[136:139], v[80:83]\n"
"s_mov_b32 s97, s37\n"
"v_mfma_f32_16x16x32_bf16 v[80:83], a[44:47], a[140:143], v[80:83]\n"
"v_mfma_f32_16x16x32_bf16 v[52:55], a[36:39], a[140:143], v[52:55]\n"
"v_mfma_f32_16x16x32_bf16 v[52:55], a[32:35], a[136:139], v[52:55]\n"
"s_waitcnt lgkmcnt(0)\n"
"v_mfma_f32_16x16x32_bf16 v[56:59], a[32:35], a[144:147], v[56:59]\n"
"buffer_load_dwordx4 v0, s[48:51], s75 offen offset:0 sc1 lds\n"
"v_mfma_f32_16x16x32_bf16 v[56:59], a[36:39], a[148:151], v[56:59]\n"
"global_load_dwordx4 a[92:95], v0, s[34:35] offset:3072 sc1\n"
"v_mfma_f32_16x16x32_bf16 v[84:87], a[44:47], a[148:151], v[84:87]\n"
"v_mfma_f32_16x16x32_bf16 v[84:87], a[40:43], a[144:147], v[84:87]\n"
"v_mfma_f32_16x16x32_bf16 a[4:7], a[40:43], a[152:155], a[4:7]\n"
"v_mfma_f32_16x16x32_bf16 a[4:7], a[44:47], a[156:159], a[4:7]\n"
"v_mfma_f32_16x16x32_bf16 a[0:3], a[36:39], a[156:159], a[0:3]\n"
"v_mfma_f32_16x16x32_bf16 a[0:3], a[32:35], a[152:155], a[0:3]\n"
"s_waitcnt vmcnt(10)\n"
"v_mfma_f32_16x16x32_bf16 v[88:91], a[48:51], a[96:99], v[88:91]\n"
"s_add_u32 s34, s36, 1458176\n"
"v_mfma_f32_16x16x32_bf16 v[88:91], a[52:55], a[100:103], v[88:91]\n"
"global_load_dwordx4 a[32:35], v0, s[34:35] offset:0 sc1\n"
"v_mfma_f32_16x16x32_bf16 v[116:119], a[60:63], a[100:103], v[116:119]\n"
"buffer_load_dwordx4 v0, s[48:51], s75 offen offset:1024 sc1 lds\n"
"v_mfma_f32_16x16x32_bf16 v[116:119], a[56:59], a[96:99], v[116:119]\n"
"v_mfma_f32_16x16x32_bf16 v[120:123], a[56:59], a[104:107], v[120:123]\n"
"v_mfma_f32_16x16x32_bf16 v[120:123], a[60:63], a[108:111], v[120:123]\n"
"v_mfma_f32_16x16x32_bf16 v[92:95], a[52:55], a[108:111], v[92:95]\n"
"v_mfma_f32_16x16x32_bf16 v[92:95], a[48:51], a[104:107], v[92:95]\n"
"v_mfma_f32_16x16x32_bf16 v[96:99], a[48:51], a[112:115], v[96:99]\n"
"v_mfma_f32_16x16x32_bf16 v[96:99], a[52:55], a[116:119], v[96:99]\n"
"global_load_dwordx4 a[36:39], v0, s[34:35] offset:1024 sc1\n"
"v_mfma_f32_16x16x32_bf16 v[124:127], a[60:63], a[116:119], v[124:127]\n"
"v_mfma_f32_16x16x32_bf16 v[124:127], a[56:59], a[112:115], v[124:127]\n"
"v_mfma_f32_16x16x32_bf16 v[128:131], a[56:59], a[120:123], v[128:131]\n"
"buffer_load_dwordx4 v0, s[48:51], s75 offen offset:2048 sc1 lds\n"
"v_mfma_f32_16x16x32_bf16 v[128:131], a[60:63], a[124:127], v[128:131]\n"
"v_mfma_f32_16x16x32_bf16 v[100:103], a[52:55], a[124:127], v[100:103]\n"
"v_mfma_f32_16x16x32_bf16 v[100:103], a[48:51], a[120:123], v[100:103]\n"
"v_mfma_f32_16x16x32_bf16 v[104:107], a[48:51], a[128:131], v[104:107]\n"
"v_mfma_f32_16x16x32_bf16 v[104:107], a[52:55], a[132:135], v[104:107]\n"
"global_load_dwordx4 a[40:43], v0, s[34:35] offset:2048 sc1\n"
"v_mfma_f32_16x16x32_bf16 v[132:135], a[60:63], a[132:135], v[132:135]\n"
"v_mfma_f32_16x16x32_bf16 v[132:135], a[56:59], a[128:131], v[132:135]\n"
"v_mfma_f32_16x16x32_bf16 v[136:139], a[56:59], a[136:139], v[136:139]\n"
"v_mfma_f32_16x16x32_bf16 v[136:139], a[60:63], a[140:143], v[136:139]\n"
"buffer_load_dwordx4 v0, s[48:51], s75 offen offset:3072 sc1 lds\n"
"v_mfma_f32_16x16x32_bf16 v[108:111], a[52:55], a[140:143], v[108:111]\n"
"v_mfma_f32_16x16x32_bf16 v[108:111], a[48:51], a[136:139], v[108:111]\n"
"v_mfma_f32_16x16x32_bf16 v[112:115], a[48:51], a[144:147], v[112:115]\n"
"v_mfma_f32_16x16x32_bf16 v[112:115], a[52:55], a[148:151], v[112:115]\n"
"global_load_dwordx4 a[44:47], v0, s[34:35] offset:3072 sc1\n"
"v_mfma_f32_16x16x32_bf16 v[140:143], a[60:63], a[148:151], v[140:143]\n"
"v_mfma_f32_16x16x32_bf16 v[140:143], a[56:59], a[144:147], v[140:143]\n"
"v_mfma_f32_16x16x32_bf16 a[12:15], a[56:59], a[152:155], a[12:15]\n"
"v_mfma_f32_16x16x32_bf16 a[12:15], a[60:63], a[156:159], a[12:15]\n"
"v_mfma_f32_16x16x32_bf16 a[8:11], a[52:55], a[156:159], a[8:11]\n"
"v_mfma_f32_16x16x32_bf16 a[8:11], a[48:51], a[152:155], a[8:11]\n"
"s_add_u32 m0, s64, 20480\n"
"s_waitcnt vmcnt(12)\n"
"v_mfma_f32_16x16x32_bf16 v[144:147], a[64:67], a[96:99], v[144:147]\n"
"s_add_u32 s34, s36, 1462272\n"
"v_mfma_f32_16x16x32_bf16 v[144:147], a[68:71], a[100:103], v[144:147]\n"
"global_load_dwordx4 a[48:51], v0, s[34:35] offset:0 sc1\n"
"v_mfma_f32_16x16x32_bf16 v[172:175], a[76:79], a[100:103], v[172:175]\n"
"v_mfma_f32_16x16x32_bf16 v[172:175], a[72:75], a[96:99], v[172:175]\n"
"v_mfma_f32_16x16x32_bf16 v[176:179], a[72:75], a[104:107], v[176:179]\n"
"v_mfma_f32_16x16x32_bf16 v[176:179], a[76:79], a[108:111], v[176:179]\n"
"v_mfma_f32_16x16x32_bf16 v[148:151], a[68:71], a[108:111], v[148:151]\n"
"v_lshlrev_b32 v16, 16, v18\n"
"v_and_b32 v17, s63, v18\n"
"v_mfma_f32_16x16x32_bf16 v[148:151], a[64:67], a[104:107], v[148:151]\n"
"v_lshlrev_b32 v18, 16, v19\n"
"v_and_b32 v19, s63, v19\n"
"v_mfma_f32_16x16x32_bf16 v[152:155], a[64:67], a[112:115], v[152:155]\n"
"buffer_load_dwordx4 v1, s[48:51], s75 offen offset:0 sc1 lds\n"
"v_mfma_f32_16x16x32_bf16 v[152:155], a[68:71], a[116:119], v[152:155]\n"
"global_load_dwordx4 a[52:55], v0, s[34:35] offset:1024 sc1\n"
"v_add_f32 v88, v88, v16\n"
"v_mfma_f32_16x16x32_bf16 v[180:183], a[76:79], a[116:119], v[180:183]\n"
"v_add_f32 v89, v89, v17\n"
"v_add_f32 v90, v90, v18\n"
"v_mfma_f32_16x16x32_bf16 v[180:183], a[72:75], a[112:115], v[180:183]\n"
"v_add_f32 v91, v91, v19\n"
"v_add_f32 v92, v92, v16\n"
"v_mfma_f32_16x16x32_bf16 v[184:187], a[72:75], a[120:123], v[184:187]\n"
"v_add_f32 v93, v93, v17\n"
"v_add_f32 v94, v94, v18\n"
"v_mfma_f32_16x16x32_bf16 v[184:187], a[76:79], a[124:127], v[184:187]\n"
"v_add_f32 v95, v95, v19\n"
"v_add_f32 v96, v96, v16\n"
"v_mfma_f32_16x16x32_bf16 v[156:159], a[68:71], a[124:127], v[156:159]\n"
"v_add_f32 v97, v97, v17\n"
"v_add_f32 v98, v98, v18\n"
"v_mfma_f32_16x16x32_bf16 v[156:159], a[64:67], a[120:123], v[156:159]\n"
"v_add_f32 v99, v99, v19\n"
"v_add_f32 v100, v100, v16\n"
"v_mfma_f32_16x16x32_bf16 v[160:163], a[64:67], a[128:131], v[160:163]\n"
"v_add_f32 v101, v101, v17\n"
"v_add_f32 v102, v102, v18\n"
"v_mfma_f32_16x16x32_bf16 v[160:163], a[68:71], a[132:135], v[160:163]\n"
"global_load_dwordx4 a[56:59], v0, s[34:35] offset:2048 sc1\n"
"v_add_f32 v103, v103, v19\n"
"v_mfma_f32_16x16x32_bf16 v[188:191], a[76:79], a[132:135], v[188:191]\n"
"buffer_load_dwordx4 v1, s[48:51], s75 offen offset:1024 sc1 lds\n"
"v_mfma_f32_16x16x32_bf16 v[188:191], a[72:75], a[128:131], v[188:191]\n"
"v_add_f32 v104, v104, v16\n"
"v_add_f32 v105, v105, v17\n"
"v_mfma_f32_16x16x32_bf16 v[192:195], a[72:75], a[136:139], v[192:195]\n"
"v_add_f32 v106, v106, v18\n"
"v_add_f32 v107, v107, v19\n"
"v_mfma_f32_16x16x32_bf16 v[192:195], a[76:79], a[140:143], v[192:195]\n"
"v_add_f32 v108, v108, v16\n"
"v_add_f32 v109, v109, v17\n"
"v_mfma_f32_16x16x32_bf16 v[164:167], a[68:71], a[140:143], v[164:167]\n"
"v_add_f32 v110, v110, v18\n"
"v_add_f32 v111, v111, v19\n"
"v_mfma_f32_16x16x32_bf16 v[164:167], a[64:67], a[136:139], v[164:167]\n"
"v_add_f32 v112, v112, v16\n"
"v_add_f32 v113, v113, v17\n"
"v_mfma_f32_16x16x32_bf16 v[168:171], a[64:67], a[144:147], v[168:171]\n"
"v_add_f32 v114, v114, v18\n"
"v_add_f32 v115, v115, v19\n"
"v_mfma_f32_16x16x32_bf16 v[168:171], a[68:71], a[148:151], v[168:171]\n"
"global_load_dwordx4 a[60:63], v0, s[34:35] offset:3072 sc1\n"
"v_accvgpr_read_b32 v12, a8\n"
"v_mfma_f32_16x16x32_bf16 v[196:199], a[76:79], a[148:151], v[196:199]\n"
"v_accvgpr_read_b32 v13, a9\n"
"v_accvgpr_read_b32 v14, a10\n"
"v_mfma_f32_16x16x32_bf16 v[196:199], a[72:75], a[144:147], v[196:199]\n"
"buffer_load_dwordx4 v1, s[48:51], s75 offen offset:2048 sc1 lds\n"
"v_mfma_f32_16x16x32_bf16 a[20:23], a[72:75], a[152:155], a[20:23]\n"
"v_accvgpr_read_b32 v15, a11\n"
"v_add_f32 v12, v12, v16\n"
"v_mfma_f32_16x16x32_bf16 a[20:23], a[76:79], a[156:159], a[20:23]\n"
"v_add_f32 v13, v13, v17\n"
"v_add_f32 v14, v14, v18\n"
"v_mfma_f32_16x16x32_bf16 a[16:19], a[68:71], a[156:159], a[16:19]\n"
"v_add_f32 v15, v15, v19\n"
"v_accvgpr_write_b32 a8, v12\n"
"v_mfma_f32_16x16x32_bf16 a[16:19], a[64:67], a[152:155], a[16:19]\n"
"s_waitcnt vmcnt(14)\n"
"v_accvgpr_write_b32 a9, v13\n"
"v_accvgpr_write_b32 a10, v14\n"
"v_accvgpr_write_b32 a11, v15\n"
"v_mfma_f32_16x16x32_bf16 v[200:203], a[80:83], a[96:99], v[200:203]\n"
"s_add_u32 s34, s36, 1474560\n"
"v_lshlrev_b32 v20, 16, v22\n"
"v_and_b32 v21, s63, v22\n"
"v_mfma_f32_16x16x32_bf16 v[200:203], a[84:87], a[100:103], v[200:203]\n"
"global_load_dwordx4 a[64:67], v0, s[34:35] offset:0 sc1\n"
"v_lshlrev_b32 v22, 16, v23\n"
"v_mfma_f32_16x16x32_bf16 v[228:231], a[92:95], a[100:103], v[228:231]\n"
"ds_read_b128 a[100:103], v3 offset:1024\n"
"v_and_b32 v23, s63, v23\n"
"v_mfma_f32_16x16x32_bf16 v[228:231], a[88:91], a[96:99], v[228:231]\n"
"ds_read_b128 a[96:99], v3 offset:0\n"
"v_add_f32 v116, v116, v20\n"
"v_mfma_f32_16x16x32_bf16 v[232:235], a[88:91], a[104:107], v[232:235]\n"
"v_add_f32 v117, v117, v21\n"
"v_add_f32 v118, v118, v22\n"
"v_mfma_f32_16x16x32_bf16 v[232:235], a[92:95], a[108:111], v[232:235]\n"
"buffer_load_dwordx4 v1, s[48:51], s75 offen offset:3072 sc1 lds\n"
"v_mfma_f32_16x16x32_bf16 v[204:207], a[84:87], a[108:111], v[204:207]\n"
"ds_read_b128 a[108:111], v3 offset:3072\n"
"v_add_f32 v119, v119, v23\n"
"v_add_f32 v120, v120, v20\n"
"v_mfma_f32_16x16x32_bf16 v[204:207], a[80:83], a[104:107], v[204:207]\n"
"ds_read_b128 a[104:107], v3 offset:2048\n"
"v_add_f32 v121, v121, v21\n"
"v_mfma_f32_16x16x32_bf16 v[208:211], a[80:83], a[112:115], v[208:211]\n"
"v_add_f32 v122, v122, v22\n"
"v_add_f32 v123, v123, v23\n"
"v_mfma_f32_16x16x32_bf16 v[208:211], a[84:87], a[116:119], v[208:211]\n"
"global_load_dwordx4 a[68:71], v0, s[34:35] offset:1024 sc1\n"
"v_add_f32 v124, v124, v20\n"
"v_add_f32 v125, v125, v21\n"
"v_mfma_f32_16x16x32_bf16 v[236:239], a[92:95], a[116:119], v[236:239]\n"
"ds_read_b128 a[116:119], v3 offset:5120\n"
"v_add_f32 v126, v126, v22\n"
"v_mfma_f32_16x16x32_bf16 v[236:239], a[88:91], a[112:115], v[236:239]\n"
"ds_read_b128 a[112:115], v3 offset:4096\n"
"v_add_f32 v127, v127, v23\n"
"v_mfma_f32_16x16x32_bf16 v[240:243], a[88:91], a[120:123], v[240:243]\n"
"v_add_f32 v128, v128, v20\n"
"v_add_f32 v129, v129, v21\n"
"v_mfma_f32_16x16x32_bf16 v[240:243], a[92:95], a[124:127], v[240:243]\n"
"v_add_f32 v130, v130, v22\n"
"v_add_f32 v131, v131, v23\n"
"v_mfma_f32_16x16x32_bf16 v[212:215], a[84:87], a[124:127], v[212:215]\n"
"s_add_u32 m0, s64, 24576\n"
"ds_read_b128 a[124:127], v3 offset:7168\n"
"v_add_f32 v132, v132, v20\n"
"v_add_f32 v133, v133, v21\n"
"v_mfma_f32_16x16x32_bf16 v[212:215], a[80:83], a[120:123], v[212:215]\n"
"ds_read_b128 a[120:123], v3 offset:6144\n"
"v_add_f32 v134, v134, v22\n"
"v_mfma_f32_16x16x32_bf16 v[216:219], a[80:83], a[128:131], v[216:219]\n"
"v_add_f32 v135, v135, v23\n"
"v_add_f32 v136, v136, v20\n"
"v_mfma_f32_16x16x32_bf16 v[216:219], a[84:87], a[132:135], v[216:219]\n"
"global_load_dwordx4 a[72:75], v0, s[34:35] offset:2048 sc1\n"
"v_add_f32 v137, v137, v21\n"
"v_mfma_f32_16x16x32_bf16 v[244:247], a[92:95], a[132:135], v[244:247]\n"
"ds_read_b128 a[132:135], v3 offset:9216\n"
"v_add_f32 v138, v138, v22\n"
"v_add_f32 v139, v139, v23\n"
"v_mfma_f32_16x16x32_bf16 v[244:247], a[88:91], a[128:131], v[244:247]\n"
"ds_read_b128 a[128:131], v3 offset:8192\n"
"v_add_f32 v140, v140, v20\n"
"v_mfma_f32_16x16x32_bf16 v[248:251], a[88:91], a[136:139], v[248:251]\n"
"v_add_f32 v141, v141, v21\n"
"v_add_f32 v142, v142, v22\n"
"v_mfma_f32_16x16x32_bf16 v[248:251], a[92:95], a[140:143], v[248:251]\n"
"v_add_f32 v143, v143, v23\n"
"v_accvgpr_read_b32 v12, a12\n"
"v_mfma_f32_16x16x32_bf16 v[220:223], a[84:87], a[140:143], v[220:223]\n"
"ds_read_b128 a[140:143], v3 offset:11264\n"
"v_accvgpr_read_b32 v13, a13\n"
"v_mfma_f32_16x16x32_bf16 v[220:223], a[80:83], a[136:139], v[220:223]\n"
"ds_read_b128 a[136:139], v3 offset:10240\n"
"v_accvgpr_read_b32 v14, a14\n"
"v_accvgpr_read_b32 v15, a15\n"
"v_mfma_f32_16x16x32_bf16 v[224:227], a[80:83], a[144:147], v[224:227]\n"
"buffer_load_dwordx4 v0, s[48:51], s69 offen offset:0 sc1 lds\n"
"v_add_f32 v12, v12, v20\n"
"v_mfma_f32_16x16x32_bf16 v[224:227], a[84:87], a[148:151], v[224:227]\n"
"global_load_dwordx4 a[76:79], v0, s[34:35] offset:3072 sc1\n"
"v_add_f32 v13, v13, v21\n"
"v_add_f32 v14, v14, v22\n"
"v_mfma_f32_16x16x32_bf16 v[252:255], a[92:95], a[148:151], v[252:255]\n"
"ds_read_b128 a[148:151], v3 offset:13312\n"
"v_add_f32 v15, v15, v23\n"
"v_accvgpr_write_b32 a12, v12\n"
"v_mfma_f32_16x16x32_bf16 v[252:255], a[88:91], a[144:147], v[252:255]\n"
"ds_read_b128 a[144:147], v3 offset:12288\n"
"v_accvgpr_write_b32 a13, v13\n"
"v_mfma_f32_16x16x32_bf16 a[28:31], a[88:91], a[152:155], a[28:31]\n"
"v_accvgpr_write_b32 a14, v14\n"
"v_accvgpr_write_b32 a15, v15\n"
"v_mfma_f32_16x16x32_bf16 a[28:31], a[92:95], a[156:159], a[28:31]\n"
"v_mfma_f32_16x16x32_bf16 a[24:27], a[84:87], a[156:159], a[24:27]\n"
"ds_read_b128 a[156:159], v3 offset:15360\n"
"v_mfma_f32_16x16x32_bf16 a[24:27], a[80:83], a[152:155], a[24:27]\n"
"s_waitcnt vmcnt(13) lgkmcnt(11)\n"
"ds_read_b128 a[152:155], v3 offset:14336\n"
"v_mfma_f32_16x16x32_bf16 v[32:35], a[32:35], a[96:99], v[32:35]\n"
"s_add_u32 s34, s36, 1478656\n"
"v_mfma_f32_16x16x32_bf16 v[32:35], a[36:39], a[100:103], v[32:35]\n"
"global_load_dwordx4 a[80:83], v0, s[34:35] offset:0 sc1\n"
"v_mfma_f32_16x16x32_bf16 v[60:63], a[44:47], a[100:103], v[60:63]\n"
"buffer_load_dwordx4 v0, s[48:51], s69 offen offset:1024 sc1 lds\n"
"v_mfma_f32_16x16x32_bf16 v[60:63], a[40:43], a[96:99], v[60:63]\n"
"v_lshrrev_b32 v16, 4, v0\n"
"v_mfma_f32_16x16x32_bf16 v[64:67], a[40:43], a[104:107], v[64:67]\n"
"v_mfma_f32_16x16x32_bf16 v[64:67], a[44:47], a[108:111], v[64:67]\n"
"v_mfma_f32_16x16x32_bf16 v[36:39], a[36:39], a[108:111], v[36:39]\n"
"v_mfma_f32_16x16x32_bf16 v[36:39], a[32:35], a[104:107], v[36:39]\n"
"s_waitcnt lgkmcnt(8)\n"
"v_and_b32 v16, 0x30, v16\n"
"v_mfma_f32_16x16x32_bf16 v[40:43], a[32:35], a[112:115], v[40:43]\n"
"v_mfma_f32_16x16x32_bf16 v[40:43], a[36:39], a[116:119], v[40:43]\n"
"global_load_dwordx4 a[84:87], v0, s[34:35] offset:1024 sc1\n"
"v_mfma_f32_16x16x32_bf16 v[68:71], a[44:47], a[116:119], v[68:71]\n"
"buffer_load_dwordx2 v[18:19], v16, s[44:47], s61 offen offset:128\n"
"v_mfma_f32_16x16x32_bf16 v[68:71], a[40:43], a[112:115], v[68:71]\n"
"buffer_load_dwordx4 v0, s[48:51], s69 offen offset:2048 sc1 lds\n"
"v_mfma_f32_16x16x32_bf16 v[72:75], a[40:43], a[120:123], v[72:75]\n"
"v_mfma_f32_16x16x32_bf16 v[72:75], a[44:47], a[124:127], v[72:75]\n"
"v_lshrrev_b32 v20, 4, v0\n"
"v_mfma_f32_16x16x32_bf16 v[44:47], a[36:39], a[124:127], v[44:47]\n"
"v_mfma_f32_16x16x32_bf16 v[44:47], a[32:35], a[120:123], v[44:47]\n"
"s_waitcnt lgkmcnt(4)\n"
"v_mfma_f32_16x16x32_bf16 v[48:51], a[32:35], a[128:131], v[48:51]\n"
"v_and_b32 v20, 0x30, v20\n"
"v_mfma_f32_16x16x32_bf16 v[48:51], a[36:39], a[132:135], v[48:51]\n"
"global_load_dwordx4 a[88:91], v0, s[34:35] offset:2048 sc1\n"
"v_mfma_f32_16x16x32_bf16 v[76:79], a[44:47], a[132:135], v[76:79]\n"
"v_mfma_f32_16x16x32_bf16 v[76:79], a[40:43], a[128:131], v[76:79]\n"
"v_mfma_f32_16x16x32_bf16 v[80:83], a[40:43], a[136:139], v[80:83]\n"
"buffer_load_dwordx4 v0, s[48:51], s69 offen offset:3072 sc1 lds\n"
"v_mfma_f32_16x16x32_bf16 v[80:83], a[44:47], a[140:143], v[80:83]\n"
"buffer_load_dwordx2 v[22:23], v20, s[44:47], s61 offen offset:136\n"
"v_mfma_f32_16x16x32_bf16 v[52:55], a[36:39], a[140:143], v[52:55]\n"
"v_mfma_f32_16x16x32_bf16 v[52:55], a[32:35], a[136:139], v[52:55]\n"
"s_waitcnt lgkmcnt(0)\n"
"v_mfma_f32_16x16x32_bf16 v[56:59], a[32:35], a[144:147], v[56:59]\n"
"v_lshrrev_b32 v24, 4, v0\n"
"v_mfma_f32_16x16x32_bf16 v[56:59], a[36:39], a[148:151], v[56:59]\n"
"global_load_dwordx4 a[92:95], v0, s[34:35] offset:3072 sc1\n"
"v_mfma_f32_16x16x32_bf16 v[84:87], a[44:47], a[148:151], v[84:87]\n"
"v_mfma_f32_16x16x32_bf16 v[84:87], a[40:43], a[144:147], v[84:87]\n"
"v_and_b32 v24, 0x30, v24\n"
"v_mfma_f32_16x16x32_bf16 a[4:7], a[40:43], a[152:155], a[4:7]\n"
"v_mfma_f32_16x16x32_bf16 a[4:7], a[44:47], a[156:159], a[4:7]\n"
"v_mfma_f32_16x16x32_bf16 a[0:3], a[36:39], a[156:159], a[0:3]\n"
"s_add_u32 m0, s64, 28672\n"
"v_mfma_f32_16x16x32_bf16 a[0:3], a[32:35], a[152:155], a[0:3]\n"
"s_waitcnt vmcnt(16)\n"
"buffer_load_dwordx2 v[26:27], v24, s[44:47], s61 offen offset:192\n"
"v_mfma_f32_16x16x32_bf16 v[88:91], a[48:51], a[96:99], v[88:91]\n"
"s_add_u32 s34, s36, 1490944\n"
"v_lshrrev_b32 v28, 4, v0\n"
"v_mfma_f32_16x16x32_bf16 v[88:91], a[52:55], a[100:103], v[88:91]\n"
"global_load_dwordx4 a[32:35], v0, s[34:35] offset:0 sc1\n"
"v_mfma_f32_16x16x32_bf16 v[116:119], a[60:63], a[100:103], v[116:119]\n"
"ds_read_b128 a[100:103], v4 offset:1024\n"
"v_mfma_f32_16x16x32_bf16 v[116:119], a[56:59], a[96:99], v[116:119]\n"
"ds_read_b128 a[96:99], v4 offset:0\n"
"v_mfma_f32_16x16x32_bf16 v[120:123], a[56:59], a[104:107], v[120:123]\n"
"v_and_b32 v28, 0x30, v28\n"
"v_mfma_f32_16x16x32_bf16 v[120:123], a[60:63], a[108:111], v[120:123]\n"
"buffer_load_dwordx2 v[30:31], v28, s[44:47], s61 offen offset:200\n"
"v_mfma_f32_16x16x32_bf16 v[92:95], a[52:55], a[108:111], v[92:95]\n"
"ds_read_b128 a[108:111], v4 offset:3072\n"
"v_mfma_f32_16x16x32_bf16 v[92:95], a[48:51], a[104:107], v[92:95]\n"
"ds_read_b128 a[104:107], v4 offset:2048\n"
"v_mfma_f32_16x16x32_bf16 v[96:99], a[48:51], a[112:115], v[96:99]\n"
"buffer_load_dwordx4 v1, s[48:51], s69 offen offset:0 sc1 lds\n"
"v_mfma_f32_16x16x32_bf16 v[96:99], a[52:55], a[116:119], v[96:99]\n"
"global_load_dwordx4 a[36:39], v0, s[34:35] offset:1024 sc1\n"
"v_mfma_f32_16x16x32_bf16 v[124:127], a[60:63], a[116:119], v[124:127]\n"
"ds_read_b128 a[116:119], v4 offset:5120\n"
"v_mfma_f32_16x16x32_bf16 v[124:127], a[56:59], a[112:115], v[124:127]\n"
"ds_read_b128 a[112:115], v4 offset:4096\n"
"v_mfma_f32_16x16x32_bf16 v[128:131], a[56:59], a[120:123], v[128:131]\n"
"v_mfma_f32_16x16x32_bf16 v[128:131], a[60:63], a[124:127], v[128:131]\n"
"v_mfma_f32_16x16x32_bf16 v[100:103], a[52:55], a[124:127], v[100:103]\n"
"ds_read_b128 a[124:127], v4 offset:7168\n"
"v_mfma_f32_16x16x32_bf16 v[100:103], a[48:51], a[120:123], v[100:103]\n"
"ds_read_b128 a[120:123], v4 offset:6144\n"
"v_mfma_f32_16x16x32_bf16 v[104:107], a[48:51], a[128:131], v[104:107]\n"
"buffer_load_dwordx4 v1, s[48:51], s69 offen offset:1024 sc1 lds\n"
"v_mfma_f32_16x16x32_bf16 v[104:107], a[52:55], a[132:135], v[104:107]\n"
"global_load_dwordx4 a[40:43], v0, s[34:35] offset:2048 sc1\n"
"v_mfma_f32_16x16x32_bf16 v[132:135], a[60:63], a[132:135], v[132:135]\n"
"ds_read_b128 a[132:135], v4 offset:9216\n"
"v_mfma_f32_16x16x32_bf16 v[132:135], a[56:59], a[128:131], v[132:135]\n"
"ds_read_b128 a[128:131], v4 offset:8192\n"
"v_mfma_f32_16x16x32_bf16 v[136:139], a[56:59], a[136:139], v[136:139]\n"
"v_mfma_f32_16x16x32_bf16 v[136:139], a[60:63], a[140:143], v[136:139]\n"
"v_mfma_f32_16x16x32_bf16 v[108:111], a[52:55], a[140:143], v[108:111]\n"
"ds_read_b128 a[140:143], v4 offset:11264\n"
"v_mfma_f32_16x16x32_bf16 v[108:111], a[48:51], a[136:139], v[108:111]\n"
"ds_read_b128 a[136:139], v4 offset:10240\n"
"v_mfma_f32_16x16x32_bf16 v[112:115], a[48:51], a[144:147], v[112:115]\n"
"v_mfma_f32_16x16x32_bf16 v[112:115], a[52:55], a[148:151], v[112:115]\n"
"global_load_dwordx4 a[44:47], v0, s[34:35] offset:3072 sc1\n"
"v_mfma_f32_16x16x32_bf16 v[140:143], a[60:63], a[148:151], v[140:143]\n"
"ds_read_b128 a[148:151], v4 offset:13312\n"
"v_mfma_f32_16x16x32_bf16 v[140:143], a[56:59], a[144:147], v[140:143]\n"
"ds_read_b128 a[144:147], v4 offset:12288\n"
"v_mfma_f32_16x16x32_bf16 a[12:15], a[56:59], a[152:155], a[12:15]\n"
"buffer_load_dwordx4 v1, s[48:51], s69 offen offset:2048 sc1 lds\n"
"v_mfma_f32_16x16x32_bf16 a[12:15], a[60:63], a[156:159], a[12:15]\n"
"v_mfma_f32_16x16x32_bf16 a[8:11], a[52:55], a[156:159], a[8:11]\n"
"ds_read_b128 a[156:159], v4 offset:15360\n"
"v_mfma_f32_16x16x32_bf16 a[8:11], a[48:51], a[152:155], a[8:11]\n"
"s_waitcnt vmcnt(18) lgkmcnt(11)\n"
"ds_read_b128 a[152:155], v4 offset:14336\n"
"v_mfma_f32_16x16x32_bf16 v[32:35], a[64:67], a[96:99], v[32:35]\n"
"s_add_u32 s34, s36, 1495040\n"
"v_mfma_f32_16x16x32_bf16 v[32:35], a[68:71], a[100:103], v[32:35]\n"
"global_load_dwordx4 a[48:51], v0, s[34:35] offset:0 sc1\n"
"v_mfma_f32_16x16x32_bf16 v[60:63], a[76:79], a[100:103], v[60:63]\n"
"v_mfma_f32_16x16x32_bf16 v[60:63], a[72:75], a[96:99], v[60:63]\n"
"v_mfma_f32_16x16x32_bf16 v[64:67], a[72:75], a[104:107], v[64:67]\n"
"buffer_load_dwordx4 v1, s[48:51], s69 offen offset:3072 sc1 lds\n"
"v_mfma_f32_16x16x32_bf16 v[64:67], a[76:79], a[108:111], v[64:67]\n"
"v_mfma_f32_16x16x32_bf16 v[36:39], a[68:71], a[108:111], v[36:39]\n"
"v_mfma_f32_16x16x32_bf16 v[36:39], a[64:67], a[104:107], v[36:39]\n"
"s_waitcnt lgkmcnt(8)\n"
"v_mfma_f32_16x16x32_bf16 v[40:43], a[64:67], a[112:115], v[40:43]\n"
"v_mfma_f32_16x16x32_bf16 v[40:43], a[68:71], a[116:119], v[40:43]\n"
"global_load_dwordx4 a[52:55], v0, s[34:35] offset:1024 sc1\n"
"v_mfma_f32_16x16x32_bf16 v[68:71], a[76:79], a[116:119], v[68:71]\n"
"v_mfma_f32_16x16x32_bf16 v[68:71], a[72:75], a[112:115], v[68:71]\n"
"v_mfma_f32_16x16x32_bf16 v[72:75], a[72:75], a[120:123], v[72:75]\n"
"v_mfma_f32_16x16x32_bf16 v[72:75], a[76:79], a[124:127], v[72:75]\n"
"v_mfma_f32_16x16x32_bf16 v[44:47], a[68:71], a[124:127], v[44:47]\n"
"v_mfma_f32_16x16x32_bf16 v[44:47], a[64:67], a[120:123], v[44:47]\n"
"s_waitcnt lgkmcnt(4)\n"
"v_mfma_f32_16x16x32_bf16 v[48:51], a[64:67], a[128:131], v[48:51]\n"
"v_mfma_f32_16x16x32_bf16 v[48:51], a[68:71], a[132:135], v[48:51]\n"
"global_load_dwordx4 a[56:59], v0, s[34:35] offset:2048 sc1\n"
"v_mfma_f32_16x16x32_bf16 v[76:79], a[76:79], a[132:135], v[76:79]\n"
"v_mfma_f32_16x16x32_bf16 v[76:79], a[72:75], a[128:131], v[76:79]\n"
"v_mfma_f32_16x16x32_bf16 v[80:83], a[72:75], a[136:139], v[80:83]\n"
"v_mfma_f32_16x16x32_bf16 v[80:83], a[76:79], a[140:143], v[80:83]\n"
"v_mfma_f32_16x16x32_bf16 v[52:55], a[68:71], a[140:143], v[52:55]\n"
"v_mfma_f32_16x16x32_bf16 v[52:55], a[64:67], a[136:139], v[52:55]\n"
"s_waitcnt lgkmcnt(0)\n"
"v_mfma_f32_16x16x32_bf16 v[56:59], a[64:67], a[144:147], v[56:59]\n"
"v_mfma_f32_16x16x32_bf16 v[56:59], a[68:71], a[148:151], v[56:59]\n"
"global_load_dwordx4 a[60:63], v0, s[34:35] offset:3072 sc1\n"
"v_mfma_f32_16x16x32_bf16 v[84:87], a[76:79], a[148:151], v[84:87]\n"
"v_mfma_f32_16x16x32_bf16 v[84:87], a[72:75], a[144:147], v[84:87]\n"
"v_mfma_f32_16x16x32_bf16 a[4:7], a[72:75], a[152:155], a[4:7]\n"
"v_mfma_f32_16x16x32_bf16 a[4:7], a[76:79], a[156:159], a[4:7]\n"
"v_mfma_f32_16x16x32_bf16 a[0:3], a[68:71], a[156:159], a[0:3]\n"
"v_mfma_f32_16x16x32_bf16 a[0:3], a[64:67], a[152:155], a[0:3]\n"
"s_waitcnt vmcnt(14)\n"
"v_mfma_f32_16x16x32_bf16 v[88:91], a[80:83], a[96:99], v[88:91]\n"
"s_add_u32 s34, s36, 1507328\n"
"v_mfma_f32_16x16x32_bf16 v[88:91], a[84:87], a[100:103], v[88:91]\n"
"global_load_dwordx4 a[64:67], v0, s[34:35] offset:0 sc1\n"
"v_mfma_f32_16x16x32_bf16 v[116:119], a[92:95], a[100:103], v[116:119]\n"
"ds_read_b128 a[100:103], v5 offset:1024\n"
"v_mfma_f32_16x16x32_bf16 v[116:119], a[88:91], a[96:99], v[116:119]\n"
"ds_read_b128 a[96:99], v5 offset:0\n"
"v_mfma_f32_16x16x32_bf16 v[120:123], a[88:91], a[104:107], v[120:123]\n"
"v_mfma_f32_16x16x32_bf16 v[120:123], a[92:95], a[108:111], v[120:123]\n"
"v_mfma_f32_16x16x32_bf16 v[92:95], a[84:87], a[108:111], v[92:95]\n"
"ds_read_b128 a[108:111], v5 offset:3072\n"
"v_mfma_f32_16x16x32_bf16 v[92:95], a[80:83], a[104:107], v[92:95]\n"
"ds_read_b128 a[104:107], v5 offset:2048\n"
"v_mfma_f32_16x16x32_bf16 v[96:99], a[80:83], a[112:115], v[96:99]\n"
"v_mfma_f32_16x16x32_bf16 v[96:99], a[84:87], a[116:119], v[96:99]\n"
"global_load_dwordx4 a[68:71], v0, s[34:35] offset:1024 sc1\n"
"v_mfma_f32_16x16x32_bf16 v[124:127], a[92:95], a[116:119], v[124:127]\n"
"ds_read_b128 a[116:119], v5 offset:5120\n"
"v_mfma_f32_16x16x32_bf16 v[124:127], a[88:91], a[112:115], v[124:127]\n"
"ds_read_b128 a[112:115], v5 offset:4096\n"
"v_mfma_f32_16x16x32_bf16 v[128:131], a[88:91], a[120:123], v[128:131]\n"
"v_mfma_f32_16x16x32_bf16 v[128:131], a[92:95], a[124:127], v[128:131]\n"
"v_mfma_f32_16x16x32_bf16 v[100:103], a[84:87], a[124:127], v[100:103]\n"
"ds_read_b128 a[124:127], v5 offset:7168\n"
"v_mfma_f32_16x16x32_bf16 v[100:103], a[80:83], a[120:123], v[100:103]\n"
"ds_read_b128 a[120:123], v5 offset:6144\n"
"v_mfma_f32_16x16x32_bf16 v[104:107], a[80:83], a[128:131], v[104:107]\n"
"v_mfma_f32_16x16x32_bf16 v[104:107], a[84:87], a[132:135], v[104:107]\n"
"global_load_dwordx4 a[72:75], v0, s[34:35] offset:2048 sc1\n"
"v_mfma_f32_16x16x32_bf16 v[132:135], a[92:95], a[132:135], v[132:135]\n"
"ds_read_b128 a[132:135], v5 offset:9216\n"
"v_mfma_f32_16x16x32_bf16 v[132:135], a[88:91], a[128:131], v[132:135]\n"
"ds_read_b128 a[128:131], v5 offset:8192\n"
"v_mfma_f32_16x16x32_bf16 v[136:139], a[88:91], a[136:139], v[136:139]\n"
"v_mfma_f32_16x16x32_bf16 v[136:139], a[92:95], a[140:143], v[136:139]\n"
"v_mfma_f32_16x16x32_bf16 v[108:111], a[84:87], a[140:143], v[108:111]\n"
"ds_read_b128 a[140:143], v5 offset:11264\n"
"v_mfma_f32_16x16x32_bf16 v[108:111], a[80:83], a[136:139], v[108:111]\n"
"ds_read_b128 a[136:139], v5 offset:10240\n"
"v_mfma_f32_16x16x32_bf16 v[112:115], a[80:83], a[144:147], v[112:115]\n"
"v_mfma_f32_16x16x32_bf16 v[112:115], a[84:87], a[148:151], v[112:115]\n"
"global_load_dwordx4 a[76:79], v0, s[34:35] offset:3072 sc1\n"
"v_mfma_f32_16x16x32_bf16 v[140:143], a[92:95], a[148:151], v[140:143]\n"
"ds_read_b128 a[148:151], v5 offset:13312\n"
"v_mfma_f32_16x16x32_bf16 v[140:143], a[88:91], a[144:147], v[140:143]\n"
"ds_read_b128 a[144:147], v5 offset:12288\n"
"v_mfma_f32_16x16x32_bf16 a[12:15], a[88:91], a[152:155], a[12:15]\n"
"v_mfma_f32_16x16x32_bf16 a[12:15], a[92:95], a[156:159], a[12:15]\n"
"v_mfma_f32_16x16x32_bf16 a[8:11], a[84:87], a[156:159], a[8:11]\n"
"ds_read_b128 a[156:159], v5 offset:15360\n"
"v_mfma_f32_16x16x32_bf16 a[8:11], a[80:83], a[152:155], a[8:11]\n"
"s_waitcnt vmcnt(10) lgkmcnt(11)\n"
"ds_read_b128 a[152:155], v5 offset:14336\n"
"v_mfma_f32_16x16x32_bf16 v[32:35], a[32:35], a[96:99], v[32:35]\n"
"s_add_u32 s34, s36, 1511424\n"
"v_mfma_f32_16x16x32_bf16 v[32:35], a[36:39], a[100:103], v[32:35]\n"
"global_load_dwordx4 a[80:83], v0, s[34:35] offset:0 sc1\n"
"v_mfma_f32_16x16x32_bf16 v[60:63], a[44:47], a[100:103], v[60:63]\n"
"v_mfma_f32_16x16x32_bf16 v[60:63], a[40:43], a[96:99], v[60:63]\n"
"v_mfma_f32_16x16x32_bf16 v[64:67], a[40:43], a[104:107], v[64:67]\n"
"L_st9674:\n"
"s_cbranch_execz L_retC\n"
"v_mfma_f32_16x16x32_bf16 v[64:67], a[44:47], a[108:111], v[64:67]\n"
"v_mfma_f32_16x16x32_bf16 v[36:39], a[36:39], a[108:111], v[36:39]\n"
"v_mfma_f32_16x16x32_bf16 v[36:39], a[32:35], a[104:107], v[36:39]\n"
"s_waitcnt lgkmcnt(8)\n"
"v_mfma_f32_16x16x32_bf16 v[40:43], a[32:35], a[112:115], v[40:43]\n"
"v_mfma_f32_16x16x32_bf16 v[40:43], a[36:39], a[116:119], v[40:43]\n"
"global_load_dwordx4 a[84:87], v0, s[34:35] offset:1024 sc1\n"
"v_mfma_f32_16x16x32_bf16 v[68:71], a[44:47], a[116:119], v[68:71]\n"
"v_mfma_f32_16x16x32_bf16 v[68:71], a[40:43], a[112:115], v[68:71]\n"
"v_mfma_f32_16x16x32_bf16 v[72:75], a[40:43], a[120:123], v[72:75]\n"
"v_mfma_f32_16x16x32_bf16 v[72:75], a[44:47], a[124:127], v[72:75]\n"
"v_mfma_f32_16x16x32_bf16 v[44:47], a[36:39], a[124:127], v[44:47]\n"
"v_mfma_f32_16x16x32_bf16 v[44:47], a[32:35], a[120:123], v[44:47]\n"
"s_waitcnt lgkmcnt(4)\n"
"v_mfma_f32_16x16x32_bf16 v[48:51], a[32:35], a[128:131], v[48:51]\n"
"v_mfma_f32_16x16x32_bf16 v[48:51], a[36:39], a[132:135], v[48:51]\n"
"global_load_dwordx4 a[88:91], v0, s[34:35] offset:2048 sc1\n"
"v_mfma_f32_16x16x32_bf16 v[76:79], a[44:47], a[132:135], v[76:79]\n"
"v_mfma_f32_16x16x32_bf16 v[76:79], a[40:43], a[128:131], v[76:79]\n"
"v_mfma_f32_16x16x32_bf16 v[80:83], a[40:43], a[136:139], v[80:83]\n"
"v_mfma_f32_16x16x32_bf16 v[80:83], a[44:47], a[140:143], v[80:83]\n"
"v_mfma_f32_16x16x32_bf16 v[52:55], a[36:39], a[140:143], v[52:55]\n"
"v_mfma_f32_16x16x32_bf16 v[52:55], a[32:35], a[136:139], v[52:55]\n"
"s_waitcnt lgkmcnt(0)\n"
"v_mfma_f32_16x16x32_bf16 v[56:59], a[32:35], a[144:147], v[56:59]\n"
"v_mfma_f32_16x16x32_bf16 v[56:59], a[36:39], a[148:151], v[56:59]\n"
"global_load_dwordx4 a[92:95], v0, s[34:35] offset:3072 sc1\n"
"v_mfma_f32_16x16x32_bf16 v[84:87], a[44:47], a[148:151], v[84:87]\n"
"v_mfma_f32_16x16x32_bf16 v[84:87], a[40:43], a[144:147], v[84:87]\n"
"v_mfma_f32_16x16x32_bf16 a[4:7], a[40:43], a[152:155], a[4:7]\n"
"v_mfma_f32_16x16x32_bf16 a[4:7], a[44:47], a[156:159], a[4:7]\n"
"v_mfma_f32_16x16x32_bf16 a[0:3], a[36:39], a[156:159], a[0:3]\n"
"v_mfma_f32_16x16x32_bf16 a[0:3], a[32:35], a[152:155], a[0:3]\n"
"s_waitcnt vmcnt(8)\n"
"v_mfma_f32_16x16x32_bf16 v[88:91], a[48:51], a[96:99], v[88:91]\n"
"s_add_u32 s34, s36, 1515520\n"
"v_mfma_f32_16x16x32_bf16 v[88:91], a[52:55], a[100:103], v[88:91]\n"
"global_load_dwordx4 a[32:35], v0, s[34:35] offset:0 sc1\n"
"v_mfma_f32_16x16x32_bf16 v[116:119], a[60:63], a[100:103], v[116:119]\n"
"ds_read_b128 a[100:103], v2 offset:17408\n"
"v_mfma_f32_16x16x32_bf16 v[116:119], a[56:59], a[96:99], v[116:119]\n"
"ds_read_b128 a[96:99], v2 offset:16384\n"
"v_mfma_f32_16x16x32_bf16 v[120:123], a[56:59], a[104:107], v[120:123]\n"
"v_mfma_f32_16x16x32_bf16 v[120:123], a[60:63], a[108:111], v[120:123]\n"
"v_mfma_f32_16x16x32_bf16 v[92:95], a[52:55], a[108:111], v[92:95]\n"
"ds_read_b128 a[108:111], v2 offset:19456\n"
"v_mfma_f32_16x16x32_bf16 v[92:95], a[48:51], a[104:107], v[92:95]\n"
"ds_read_b128 a[104:107], v2 offset:18432\n"
"v_mfma_f32_16x16x32_bf16 v[96:99], a[48:51], a[112:115], v[96:99]\n"
"v_mfma_f32_16x16x32_bf16 v[96:99], a[52:55], a[116:119], v[96:99]\n"
"global_load_dwordx4 a[36:39], v0, s[34:35] offset:1024 sc1\n"
"v_mfma_f32_16x16x32_bf16 v[124:127], a[60:63], a[116:119], v[124:127]\n"
"ds_read_b128 a[116:119], v2 offset:21504\n"
"v_mfma_f32_16x16x32_bf16 v[124:127], a[56:59], a[112:115], v[124:127]\n"
"ds_read_b128 a[112:115], v2 offset:20480\n"
"v_mfma_f32_16x16x32_bf16 v[128:131], a[56:59], a[120:123], v[128:131]\n"
"v_mfma_f32_16x16x32_bf16 v[128:131], a[60:63], a[124:127], v[128:131]\n"
"v_mfma_f32_16x16x32_bf16 v[100:103], a[52:55], a[124:127], v[100:103]\n"
"ds_read_b128 a[124:127], v2 offset:23552\n"
"v_mfma_f32_16x16x32_bf16 v[100:103], a[48:51], a[120:123], v[100:103]\n"
"ds_read_b128 a[120:123], v2 offset:22528\n"
"v_mfma_f32_16x16x32_bf16 v[104:107], a[48:51], a[128:131], v[104:107]\n"
"v_mfma_f32_16x16x32_bf16 v[104:107], a[52:55], a[132:135], v[104:107]\n"
"global_load_dwordx4 a[40:43], v0, s[34:35] offset:2048 sc1\n"
"v_mfma_f32_16x16x32_bf16 v[132:135], a[60:63], a[132:135], v[132:135]\n"
"ds_read_b128 a[132:135], v2 offset:25600\n"
"v_mfma_f32_16x16x32_bf16 v[132:135], a[56:59], a[128:131], v[132:135]\n"
"ds_read_b128 a[128:131], v2 offset:24576\n"
"v_mfma_f32_16x16x32_bf16 v[136:139], a[56:59], a[136:139], v[136:139]\n"
"v_mfma_f32_16x16x32_bf16 v[136:139], a[60:63], a[140:143], v[136:139]\n"
"v_mfma_f32_16x16x32_bf16 v[108:111], a[52:55], a[140:143], v[108:111]\n"
"ds_read_b128 a[140:143], v2 offset:27648\n"
"v_mfma_f32_16x16x32_bf16 v[108:111], a[48:51], a[136:139], v[108:111]\n"
"ds_read_b128 a[136:139], v2 offset:26624\n"
"v_mfma_f32_16x16x32_bf16 v[112:115], a[48:51], a[144:147], v[112:115]\n"
"v_mfma_f32_16x16x32_bf16 v[112:115], a[52:55], a[148:151], v[112:115]\n"
"global_load_dwordx4 a[44:47], v0, s[34:35] offset:3072 sc1\n"
"v_mfma_f32_16x16x32_bf16 v[140:143], a[60:63], a[148:151], v[140:143]\n"
"ds_read_b128 a[148:151], v2 offset:29696\n"
"v_mfma_f32_16x16x32_bf16 v[140:143], a[56:59], a[144:147], v[140:143]\n"
"ds_read_b128 a[144:147], v2 offset:28672\n"
"v_mfma_f32_16x16x32_bf16 a[12:15], a[56:59], a[152:155], a[12:15]\n"
"v_mfma_f32_16x16x32_bf16 a[12:15], a[60:63], a[156:159], a[12:15]\n"
"v_mfma_f32_16x16x32_bf16 a[8:11], a[52:55], a[156:159], a[8:11]\n"
"ds_read_b128 a[156:159], v2 offset:31744\n"
"v_mfma_f32_16x16x32_bf16 a[8:11], a[48:51], a[152:155], a[8:11]\n"
"s_waitcnt vmcnt(8) lgkmcnt(11)\n"
"ds_read_b128 a[152:155], v2 offset:30720\n"
"v_mfma_f32_16x16x32_bf16 v[32:35], a[64:67], a[96:99], v[32:35]\n"
"s_add_u32 s34, s36, 1519616\n"
"v_mfma_f32_16x16x32_bf16 v[32:35], a[68:71], a[100:103], v[32:35]\n"
"global_load_dwordx4 a[48:51], v0, s[34:35] offset:0 sc1\n"
"v_mfma_f32_16x16x32_bf16 v[60:63], a[76:79], a[100:103], v[60:63]\n"
"s_barrier\n"
"v_mfma_f32_16x16x32_bf16 v[60:63], a[72:75], a[96:99], v[60:63]\n"
"v_mfma_f32_16x16x32_bf16 v[64:67], a[72:75], a[104:107], v[64:67]\n"
"v_mfma_f32_16x16x32_bf16 v[64:67], a[76:79], a[108:111], v[64:67]\n"
"v_mfma_f32_16x16x32_bf16 v[36:39], a[68:71], a[108:111], v[36:39]\n"
"v_mfma_f32_16x16x32_bf16 v[36:39], a[64:67], a[104:107], v[36:39]\n"
"s_waitcnt lgkmcnt(8)\n"
"v_mfma_f32_16x16x32_bf16 v[40:43], a[64:67], a[112:115], v[40:43]\n"
"v_mfma_f32_16x16x32_bf16 v[40:43], a[68:71], a[116:119], v[40:43]\n"
"global_load_dwordx4 a[52:55], v0, s[34:35] offset:1024 sc1\n"
"v_mfma_f32_16x16x32_bf16 v[68:71], a[76:79], a[116:119], v[68:71]\n"
"v_mfma_f32_16x16x32_bf16 v[68:71], a[72:75], a[112:115], v[68:71]\n"
"v_mfma_f32_16x16x32_bf16 v[72:75], a[72:75], a[120:123], v[72:75]\n"
"v_mfma_f32_16x16x32_bf16 v[72:75], a[76:79], a[124:127], v[72:75]\n"
"v_mfma_f32_16x16x32_bf16 v[44:47], a[68:71], a[124:127], v[44:47]\n"
"v_mfma_f32_16x16x32_bf16 v[44:47], a[64:67], a[120:123], v[44:47]\n"
"s_waitcnt lgkmcnt(4)\n"
"v_mfma_f32_16x16x32_bf16 v[48:51], a[64:67], a[128:131], v[48:51]\n"
"v_mfma_f32_16x16x32_bf16 v[48:51], a[68:71], a[132:135], v[48:51]\n"
"global_load_dwordx4 a[56:59], v0, s[34:35] offset:2048 sc1\n"
"v_mfma_f32_16x16x32_bf16 v[76:79], a[76:79], a[132:135], v[76:79]\n"
"v_mfma_f32_16x16x32_bf16 v[76:79], a[72:75], a[128:131], v[76:79]\n"
"v_mfma_f32_16x16x32_bf16 v[80:83], a[72:75], a[136:139], v[80:83]\n"
"v_lshlrev_b32 v16, 16, v18\n"
"v_mfma_f32_16x16x32_bf16 v[80:83], a[76:79], a[140:143], v[80:83]\n"
"v_and_b32 v17, s63, v18\n"
"v_mfma_f32_16x16x32_bf16 v[52:55], a[68:71], a[140:143], v[52:55]\n"
"v_lshlrev_b32 v18, 16, v19\n"
"v_mfma_f32_16x16x32_bf16 v[52:55], a[64:67], a[136:139], v[52:55]\n"
"s_waitcnt lgkmcnt(0)\n"
"v_and_b32 v19, s63, v19\n"
"v_add_f32 v144, v144, v16\n"
"v_mfma_f32_16x16x32_bf16 v[56:59], a[64:67], a[144:147], v[56:59]\n"
"v_add_f32 v145, v145, v17\n"
"v_mfma_f32_16x16x32_bf16 v[56:59], a[68:71], a[148:151], v[56:59]\n"
"global_load_dwordx4 a[60:63], v0, s[34:35] offset:3072 sc1\n"
"v_add_f32 v146, v146, v18\n"
"v_mfma_f32_16x16x32_bf16 v[84:87], a[76:79], a[148:151], v[84:87]\n"
"v_add_f32 v147, v147, v19\n"
"v_mfma_f32_16x16x32_bf16 v[84:87], a[72:75], a[144:147], v[84:87]\n"
"v_add_f32 v148, v148, v16\n"
"v_mfma_f32_16x16x32_bf16 a[4:7], a[72:75], a[152:155], a[4:7]\n"
"v_add_f32 v149, v149, v17\n"
"v_mfma_f32_16x16x32_bf16 a[4:7], a[76:79], a[156:159], a[4:7]\n"
"v_add_f32 v150, v150, v18\n"
"v_mfma_f32_16x16x32_bf16 a[0:3], a[68:71], a[156:159], a[0:3]\n"
"v_add_f32 v151, v151, v19\n"
"v_mfma_f32_16x16x32_bf16 a[0:3], a[64:67], a[152:155], a[0:3]\n"
"s_waitcnt vmcnt(8)\n"
"v_add_f32 v152, v152, v16\n"
"v_add_f32 v153, v153, v17\n"
"v_mfma_f32_16x16x32_bf16 v[88:91], a[80:83], a[96:99], v[88:91]\n"
"s_add_u32 s34, s36, 1523712\n"
"v_add_f32 v154, v154, v18\n"
"v_mfma_f32_16x16x32_bf16 v[88:91], a[84:87], a[100:103], v[88:91]\n"
"global_load_dwordx4 a[64:67], v0, s[34:35] offset:0 sc1\n"
"v_add_f32 v155, v155, v19\n"
"v_mfma_f32_16x16x32_bf16 v[116:119], a[92:95], a[100:103], v[116:119]\n"
"ds_read_b128 a[100:103], v3 offset:17408\n"
"v_add_f32 v156, v156, v16\n"
"v_mfma_f32_16x16x32_bf16 v[116:119], a[88:91], a[96:99], v[116:119]\n"
"ds_read_b128 a[96:99], v3 offset:16384\n"
"v_add_f32 v157, v157, v17\n"
"v_mfma_f32_16x16x32_bf16 v[120:123], a[88:91], a[104:107], v[120:123]\n"
"v_add_f32 v158, v158, v18\n"
"v_mfma_f32_16x16x32_bf16 v[120:123], a[92:95], a[108:111], v[120:123]\n"
"v_add_f32 v159, v159, v19\n"
"v_add_f32 v160, v160, v16\n"
"v_mfma_f32_16x16x32_bf16 v[92:95], a[84:87], a[108:111], v[92:95]\n"
"ds_read_b128 a[108:111], v3 offset:19456\n"
"v_add_f32 v161, v161, v17\n"
"v_mfma_f32_16x16x32_bf16 v[92:95], a[80:83], a[104:107], v[92:95]\n"
"ds_read_b128 a[104:107], v3 offset:18432\n"
"v_add_f32 v162, v162, v18\n"
"v_mfma_f32_16x16x32_bf16 v[96:99], a[80:83], a[112:115], v[96:99]\n"
"v_add_f32 v163, v163, v19\n"
"v_mfma_f32_16x16x32_bf16 v[96:99], a[84:87], a[116:119], v[96:99]\n"
"global_load_dwordx4 a[68:71], v0, s[34:35] offset:1024 sc1\n"
"v_add_f32 v164, v164, v16\n"
"v_mfma_f32_16x16x32_bf16 v[124:127], a[92:95], a[116:119], v[124:127]\n"
"ds_read_b128 a[116:119], v3 offset:21504\n"
"v_add_f32 v165, v165, v17\n"
"v_mfma_f32_16x16x32_bf16 v[124:127], a[88:91], a[112:115], v[124:127]\n"
"ds_read_b128 a[112:115], v3 offset:20480\n"
"v_add_f32 v166, v166, v18\n"
"v_mfma_f32_16x16x32_bf16 v[128:131], a[88:91], a[120:123], v[128:131]\n"
"v_add_f32 v167, v167, v19\n"
"v_mfma_f32_16x16x32_bf16 v[128:131], a[92:95], a[124:127], v[128:131]\n"
"v_add_f32 v168, v168, v16\n"
"v_add_f32 v169, v169, v17\n"
"v_mfma_f32_16x16x32_bf16 v[100:103], a[84:87], a[124:127], v[100:103]\n"
"ds_read_b128 a[124:127], v3 offset:23552\n"
"v_add_f32 v170, v170, v18\n"
"v_mfma_f32_16x16x32_bf16 v[100:103], a[80:83], a[120:123], v[100:103]\n"
"ds_read_b128 a[120:123], v3 offset:22528\n"
"v_add_f32 v171, v171, v19\n"
"v_mfma_f32_16x16x32_bf16 v[104:107], a[80:83], a[128:131], v[104:107]\n"
"v_accvgpr_read_b32 v12, a16\n"
"v_mfma_f32_16x16x32_bf16 v[104:107], a[84:87], a[132:135], v[104:107]\n"
"global_load_dwordx4 a[72:75], v0, s[34:35] offset:2048 sc1\n"
"v_accvgpr_read_b32 v13, a17\n"
"v_mfma_f32_16x16x32_bf16 v[132:135], a[92:95], a[132:135], v[132:135]\n"
"ds_read_b128 a[132:135], v3 offset:25600\n"
"v_accvgpr_read_b32 v14, a18\n"
"v_mfma_f32_16x16x32_bf16 v[132:135], a[88:91], a[128:131], v[132:135]\n"
"ds_read_b128 a[128:131], v3 offset:24576\n"
"v_accvgpr_read_b32 v15, a19\n"
"v_mfma_f32_16x16x32_bf16 v[136:139], a[88:91], a[136:139], v[136:139]\n"
"v_add_f32 v12, v12, v16\n"
"v_mfma_f32_16x16x32_bf16 v[136:139], a[92:95], a[140:143], v[136:139]\n"
"v_add_f32 v13, v13, v17\n"
"v_add_f32 v14, v14, v18\n"
"v_mfma_f32_16x16x32_bf16 v[108:111], a[84:87], a[140:143], v[108:111]\n"
"ds_read_b128 a[140:143], v3 offset:27648\n"
"v_add_f32 v15, v15, v19\n"
"v_mfma_f32_16x16x32_bf16 v[108:111], a[80:83], a[136:139], v[108:111]\n"
"ds_read_b128 a[136:139], v3 offset:26624\n"
"v_accvgpr_write_b32 a16, v12\n"
"v_mfma_f32_16x16x32_bf16 v[112:115], a[80:83], a[144:147], v[112:115]\n"
"v_accvgpr_write_b32 a17, v13\n"
"v_mfma_f32_16x16x32_bf16 v[112:115], a[84:87], a[148:151], v[112:115]\n"
"global_load_dwordx4 a[76:79], v0, s[34:35] offset:3072 sc1\n"
"v_accvgpr_write_b32 a18, v14\n"
"v_mfma_f32_16x16x32_bf16 v[140:143], a[92:95], a[148:151], v[140:143]\n"
"ds_read_b128 a[148:151], v3 offset:29696\n"
"v_accvgpr_write_b32 a19, v15\n"
"v_mfma_f32_16x16x32_bf16 v[140:143], a[88:91], a[144:147], v[140:143]\n"
"ds_read_b128 a[144:147], v3 offset:28672\n"
"v_lshlrev_b32 v20, 16, v22\n"
"v_mfma_f32_16x16x32_bf16 a[12:15], a[88:91], a[152:155], a[12:15]\n"
"v_and_b32 v21, s63, v22\n"
"v_lshlrev_b32 v22, 16, v23\n"
"v_mfma_f32_16x16x32_bf16 a[12:15], a[92:95], a[156:159], a[12:15]\n"
"v_and_b32 v23, s63, v23\n"
"v_mfma_f32_16x16x32_bf16 a[8:11], a[84:87], a[156:159], a[8:11]\n"
"ds_read_b128 a[156:159], v3 offset:31744\n"
"v_add_f32 v172, v172, v20\n"
"v_mfma_f32_16x16x32_bf16 a[8:11], a[80:83], a[152:155], a[8:11]\n"
"s_waitcnt vmcnt(8) lgkmcnt(11)\n"
"ds_read_b128 a[152:155], v3 offset:30720\n"
"v_add_f32 v173, v173, v21\n"
"v_mfma_f32_16x16x32_bf16 v[32:35], a[32:35], a[96:99], v[32:35]\n"
"s_add_u32 s34, s36, 1527808\n"
"v_add_f32 v174, v174, v22\n"
"v_mfma_f32_16x16x32_bf16 v[32:35], a[36:39], a[100:103], v[32:35]\n"
"global_load_dwordx4 a[80:83], v0, s[34:35] offset:0 sc1\n"
"v_add_f32 v175, v175, v23\n"
"v_mfma_f32_16x16x32_bf16 v[60:63], a[44:47], a[100:103], v[60:63]\n"
"v_add_f32 v176, v176, v20\n"
"v_mfma_f32_16x16x32_bf16 v[60:63], a[40:43], a[96:99], v[60:63]\n"
"v_add_f32 v177, v177, v21\n"
"v_add_f32 v178, v178, v22\n"
"v_mfma_f32_16x16x32_bf16 v[64:67], a[40:43], a[104:107], v[64:67]\n"
"v_add_f32 v179, v179, v23\n"
"v_mfma_f32_16x16x32_bf16 v[64:67], a[44:47], a[108:111], v[64:67]\n"
"v_add_f32 v180, v180, v20\n"
"v_mfma_f32_16x16x32_bf16 v[36:39], a[36:39], a[108:111], v[36:39]\n"
"v_add_f32 v181, v181, v21\n"
"v_mfma_f32_16x16x32_bf16 v[36:39], a[32:35], a[104:107], v[36:39]\n"
"s_waitcnt lgkmcnt(8)\n"
"v_add_f32 v182, v182, v22\n"
"v_mfma_f32_16x16x32_bf16 v[40:43], a[32:35], a[112:115], v[40:43]\n"
"v_add_f32 v183, v183, v23\n"
"v_mfma_f32_16x16x32_bf16 v[40:43], a[36:39], a[116:119], v[40:43]\n"
"global_load_dwordx4 a[84:87], v0, s[34:35] offset:1024 sc1\n"
"v_add_f32 v184, v184, v20\n"
"v_mfma_f32_16x16x32_bf16 v[68:71], a[44:47], a[116:119], v[68:71]\n"
"v_add_f32 v185, v185, v21\n"
"v_mfma_f32_16x16x32_bf16 v[68:71], a[40:43], a[112:115], v[68:71]\n"
"v_add_f32 v186, v186, v22\n"
"v_add_f32 v187, v187, v23\n"
"v_mfma_f32_16x16x32_bf16 v[72:75], a[40:43], a[120:123], v[72:75]\n"
"v_add_f32 v188, v188, v20\n"
"v_mfma_f32_16x16x32_bf16 v[72:75], a[44:47], a[124:127], v[72:75]\n"
"v_add_f32 v189, v189, v21\n"
"v_mfma_f32_16x16x32_bf16 v[44:47], a[36:39], a[124:127], v[44:47]\n"
"v_add_f32 v190, v190, v22\n"
"v_mfma_f32_16x16x32_bf16 v[44:47], a[32:35], a[120:123], v[44:47]\n"
"s_waitcnt lgkmcnt(4)\n"
"v_add_f32 v191, v191, v23\n"
"v_mfma_f32_16x16x32_bf16 v[48:51], a[32:35], a[128:131], v[48:51]\n"
"v_add_f32 v192, v192, v20\n"
"v_mfma_f32_16x16x32_bf16 v[48:51], a[36:39], a[132:135], v[48:51]\n"
"global_load_dwordx4 a[88:91], v0, s[34:35] offset:2048 sc1\n"
"v_add_f32 v193, v193, v21\n"
"v_mfma_f32_16x16x32_bf16 v[76:79], a[44:47], a[132:135], v[76:79]\n"
"v_add_f32 v194, v194, v22\n"
"v_add_f32 v195, v195, v23\n"
"v_mfma_f32_16x16x32_bf16 v[76:79], a[40:43], a[128:131], v[76:79]\n"
"v_add_f32 v196, v196, v20\n"
"v_mfma_f32_16x16x32_bf16 v[80:83], a[40:43], a[136:139], v[80:83]\n"
"v_add_f32 v197, v197, v21\n"
"v_mfma_f32_16x16x32_bf16 v[80:83], a[44:47], a[140:143], v[80:83]\n"
"v_add_f32 v198, v198, v22\n"
"v_mfma_f32_16x16x32_bf16 v[52:55], a[36:39], a[140:143], v[52:55]\n"
"v_add_f32 v199, v199, v23\n"
"v_mfma_f32_16x16x32_bf16 v[52:55], a[32:35], a[136:139], v[52:55]\n"
"s_waitcnt lgkmcnt(0)\n"
"v_accvgpr_read_b32 v12, a20\n"
"v_mfma_f32_16x16x32_bf16 v[56:59], a[32:35], a[144:147], v[56:59]\n"
"v_accvgpr_read_b32 v13, a21\n"
"v_mfma_f32_16x16x32_bf16 v[56:59], a[36:39], a[148:151], v[56:59]\n"
"global_load_dwordx4 a[92:95], v0, s[34:35] offset:3072 sc1\n"
"v_accvgpr_read_b32 v14, a22\n"
"v_mfma_f32_16x16x32_bf16 v[84:87], a[44:47], a[148:151], v[84:87]\n"
"v_accvgpr_read_b32 v15, a23\n"
"v_add_f32 v12, v12, v20\n"
"v_mfma_f32_16x16x32_bf16 v[84:87], a[40:43], a[144:147], v[84:87]\n"
"v_add_f32 v13, v13, v21\n"
"v_mfma_f32_16x16x32_bf16 a[4:7], a[40:43], a[152:155], a[4:7]\n"
"v_add_f32 v14, v14, v22\n"
"v_mfma_f32_16x16x32_bf16 a[4:7], a[44:47], a[156:159], a[4:7]\n"
"v_add_f32 v15, v15, v23\n"
"v_mfma_f32_16x16x32_bf16 a[0:3], a[36:39], a[156:159], a[0:3]\n"
"v_accvgpr_write_b32 a20, v12\n"
"v_mfma_f32_16x16x32_bf16 a[0:3], a[32:35], a[152:155], a[0:3]\n"
"s_waitcnt vmcnt(8)\n"
"v_accvgpr_write_b32 a21, v13\n"
"v_mfma_f32_16x16x32_bf16 v[88:91], a[48:51], a[96:99], v[88:91]\n"
"s_add_u32 s34, s36, 1531904\n"
"v_accvgpr_write_b32 a22, v14\n"
"v_accvgpr_write_b32 a23, v15\n"
"v_mfma_f32_16x16x32_bf16 v[88:91], a[52:55], a[100:103], v[88:91]\n"
"global_load_dwordx4 a[32:35], v0, s[34:35] offset:0 sc1\n"
"v_lshlrev_b32 v24, 16, v26\n"
"v_mfma_f32_16x16x32_bf16 v[116:119], a[60:63], a[100:103], v[116:119]\n"
"ds_read_b128 a[100:103], v4 offset:17408\n"
"v_and_b32 v25, s63, v26\n"
"v_mfma_f32_16x16x32_bf16 v[116:119], a[56:59], a[96:99], v[116:119]\n"
"ds_read_b128 a[96:99], v4 offset:16384\n"
"v_lshlrev_b32 v26, 16, v27\n"
"v_mfma_f32_16x16x32_bf16 v[120:123], a[56:59], a[104:107], v[120:123]\n"
"v_and_b32 v27, s63, v27\n"
"v_mfma_f32_16x16x32_bf16 v[120:123], a[60:63], a[108:111], v[120:123]\n"
"v_add_f32 v200, v200, v24\n"
"v_mfma_f32_16x16x32_bf16 v[92:95], a[52:55], a[108:111], v[92:95]\n"
"ds_read_b128 a[108:111], v4 offset:19456\n"
"v_add_f32 v201, v201, v25\n"
"v_mfma_f32_16x16x32_bf16 v[92:95], a[48:51], a[104:107], v[92:95]\n"
"ds_read_b128 a[104:107], v4 offset:18432\n"
"v_add_f32 v202, v202, v26\n"
"v_mfma_f32_16x16x32_bf16 v[96:99], a[48:51], a[112:115], v[96:99]\n"
"v_add_f32 v203, v203, v27\n"
"v_add_f32 v204, v204, v24\n"
"v_mfma_f32_16x16x32_bf16 v[96:99], a[52:55], a[116:119], v[96:99]\n"
"global_load_dwordx4 a[36:39], v0, s[34:35] offset:1024 sc1\n"
"v_add_f32 v205, v205, v25\n"
"v_mfma_f32_16x16x32_bf16 v[124:127], a[60:63], a[116:119], v[124:127]\n"
"ds_read_b128 a[116:119], v4 offset:21504\n"
"v_add_f32 v206, v206, v26\n"
"v_mfma_f32_16x16x32_bf16 v[124:127], a[56:59], a[112:115], v[124:127]\n"
"ds_read_b128 a[112:115], v4 offset:20480\n"
"v_add_f32 v207, v207, v27\n"
"v_mfma_f32_16x16x32_bf16 v[128:131], a[56:59], a[120:123], v[128:131]\n"
"v_add_f32 v208, v208, v24\n"
"v_mfma_f32_16x16x32_bf16 v[128:131], a[60:63], a[124:127], v[128:131]\n"
"v_add_f32 v209, v209, v25\n"
"v_mfma_f32_16x16x32_bf16 v[100:103], a[52:55], a[124:127], v[100:103]\n"
"ds_read_b128 a[124:127], v4 offset:23552\n"
"v_add_f32 v210, v210, v26\n"
"v_mfma_f32_16x16x32_bf16 v[100:103], a[48:51], a[120:123], v[100:103]\n"
"ds_read_b128 a[120:123], v4 offset:22528\n"
"v_add_f32 v211, v211, v27\n"
"v_mfma_f32_16x16x32_bf16 v[104:107], a[48:51], a[128:131], v[104:107]\n"
"v_add_f32 v212, v212, v24\n"
"v_add_f32 v213, v213, v25\n"
"v_mfma_f32_16x16x32_bf16 v[104:107], a[52:55], a[132:135], v[104:107]\n"
"global_load_dwordx4 a[40:43], v0, s[34:35] offset:2048 sc1\n"
"v_add_f32 v214, v214, v26\n"
"v_mfma_f32_16x16x32_bf16 v[132:135], a[60:63], a[132:135], v[132:135]\n"
"ds_read_b128 a[132:135], v4 offset:25600\n"
"v_add_f32 v215, v215, v27\n"
"v_mfma_f32_16x16x32_bf16 v[132:135], a[56:59], a[128:131], v[132:135]\n"
"ds_read_b128 a[128:131], v4 offset:24576\n"
"v_add_f32 v216, v216, v24\n"
"v_mfma_f32_16x16x32_bf16 v[136:139], a[56:59], a[136:139], v[136:139]\n"
"v_add_f32 v217, v217, v25\n"
"v_mfma_f32_16x16x32_bf16 v[136:139], a[60:63], a[140:143], v[136:139]\n"
"v_add_f32 v218, v218, v26\n"
"v_mfma_f32_16x16x32_bf16 v[108:111], a[52:55], a[140:143], v[108:111]\n"
"ds_read_b128 a[140:143], v4 offset:27648\n"
"v_add_f32 v219, v219, v27\n"
"v_mfma_f32_16x16x32_bf16 v[108:111], a[48:51], a[136:139], v[108:111]\n"
"ds_read_b128 a[136:139], v4 offset:26624\n"
"v_add_f32 v220, v220, v24\n"
"v_mfma_f32_16x16x32_bf16 v[112:115], a[48:51], a[144:147], v[112:115]\n"
"v_add_f32 v221, v221, v25\n"
"v_add_f32 v222, v222, v26\n"
"v_mfma_f32_16x16x32_bf16 v[112:115], a[52:55], a[148:151], v[112:115]\n"
"global_load_dwordx4 a[44:47], v0, s[34:35] offset:3072 sc1\n"
"v_add_f32 v223, v223, v27\n"
"v_mfma_f32_16x16x32_bf16 v[140:143], a[60:63], a[148:151], v[140:143]\n"
"ds_read_b128 a[148:151], v4 offset:29696\n"
"v_add_f32 v224, v224, v24\n"
"v_mfma_f32_16x16x32_bf16 v[140:143], a[56:59], a[144:147], v[140:143]\n"
"ds_read_b128 a[144:147], v4 offset:28672\n"
"v_add_f32 v225, v225, v25\n"
"v_mfma_f32_16x16x32_bf16 a[12:15], a[56:59], a[152:155], a[12:15]\n"
"v_add_f32 v226, v226, v26\n"
"v_mfma_f32_16x16x32_bf16 a[12:15], a[60:63], a[156:159], a[12:15]\n"
"v_add_f32 v227, v227, v27\n"
"v_mfma_f32_16x16x32_bf16 a[8:11], a[52:55], a[156:159], a[8:11]\n"
"ds_read_b128 a[156:159], v4 offset:31744\n"
"v_accvgpr_read_b32 v12, a24\n"
"v_mfma_f32_16x16x32_bf16 a[8:11], a[48:51], a[152:155], a[8:11]\n"
"s_waitcnt vmcnt(8) lgkmcnt(11)\n"
"ds_read_b128 a[152:155], v4 offset:30720\n"
"v_accvgpr_read_b32 v13, a25\n"
"v_mfma_f32_16x16x32_bf16 v[32:35], a[64:67], a[96:99], v[32:35]\n"
"s_add_u32 s34, s36, 1536000\n"
"v_accvgpr_read_b32 v14, a26\n"
"v_accvgpr_read_b32 v15, a27\n"
"v_mfma_f32_16x16x32_bf16 v[32:35], a[68:71], a[100:103], v[32:35]\n"
"global_load_dwordx4 a[48:51], v0, s[34:35] offset:0 sc1\n"
"v_add_f32 v12, v12, v24\n"
"v_mfma_f32_16x16x32_bf16 v[60:63], a[76:79], a[100:103], v[60:63]\n"
"v_add_f32 v13, v13, v25\n"
"v_mfma_f32_16x16x32_bf16 v[60:63], a[72:75], a[96:99], v[60:63]\n"
"v_add_f32 v14, v14, v26\n"
"v_mfma_f32_16x16x32_bf16 v[64:67], a[72:75], a[104:107], v[64:67]\n"
"v_add_f32 v15, v15, v27\n"
"v_mfma_f32_16x16x32_bf16 v[64:67], a[76:79], a[108:111], v[64:67]\n"
"v_accvgpr_write_b32 a24, v12\n"
"v_mfma_f32_16x16x32_bf16 v[36:39], a[68:71], a[108:111], v[36:39]\n"
"v_accvgpr_write_b32 a25, v13\n"
"v_accvgpr_write_b32 a26, v14\n"
"v_mfma_f32_16x16x32_bf16 v[36:39], a[64:67], a[104:107], v[36:39]\n"
"s_waitcnt lgkmcnt(8)\n"
"v_accvgpr_write_b32 a27, v15\n"
"v_mfma_f32_16x16x32_bf16 v[40:43], a[64:67], a[112:115], v[40:43]\n"
"v_lshlrev_b32 v28, 16, v30\n"
"v_mfma_f32_16x16x32_bf16 v[40:43], a[68:71], a[116:119], v[40:43]\n"
"global_load_dwordx4 a[52:55], v0, s[34:35] offset:1024 sc1\n"
"v_and_b32 v29, s63, v30\n"
"v_mfma_f32_16x16x32_bf16 v[68:71], a[76:79], a[116:119], v[68:71]\n"
"v_lshlrev_b32 v30, 16, v31\n"
"v_mfma_f32_16x16x32_bf16 v[68:71], a[72:75], a[112:115], v[68:71]\n"
"v_and_b32 v31, s63, v31\n"
"v_mfma_f32_16x16x32_bf16 v[72:75], a[72:75], a[120:123], v[72:75]\n"
"v_add_f32 v228, v228, v28\n"
"v_mfma_f32_16x16x32_bf16 v[72:75], a[76:79], a[124:127], v[72:75]\n"
"v_add_f32 v229, v229, v29\n"
"v_add_f32 v230, v230, v30\n"
"v_mfma_f32_16x16x32_bf16 v[44:47], a[68:71], a[124:127], v[44:47]\n"
"v_add_f32 v231, v231, v31\n"
"v_mfma_f32_16x16x32_bf16 v[44:47], a[64:67], a[120:123], v[44:47]\n"
"s_waitcnt lgkmcnt(4)\n"
"v_add_f32 v232, v232, v28\n"
"v_mfma_f32_16x16x32_bf16 v[48:51], a[64:67], a[128:131], v[48:51]\n"
"v_add_f32 v233, v233, v29\n"
"v_mfma_f32_16x16x32_bf16 v[48:51], a[68:71], a[132:135], v[48:51]\n"
"global_load_dwordx4 a[56:59], v0, s[34:35] offset:2048 sc1\n"
"v_add_f32 v234, v234, v30\n"
"v_mfma_f32_16x16x32_bf16 v[76:79], a[76:79], a[132:135], v[76:79]\n"
"v_add_f32 v235, v235, v31\n"
"v_mfma_f32_16x16x32_bf16 v[76:79], a[72:75], a[128:131], v[76:79]\n"
"v_add_f32 v236, v236, v28\n"
"v_mfma_f32_16x16x32_bf16 v[80:83], a[72:75], a[136:139], v[80:83]\n"
"v_add_f32 v237, v237, v29\n"
"v_mfma_f32_16x16x32_bf16 v[80:83], a[76:79], a[140:143], v[80:83]\n"
"v_add_f32 v238, v238, v30\n"
"v_add_f32 v239, v239, v31\n"
"v_mfma_f32_16x16x32_bf16 v[52:55], a[68:71], a[140:143], v[52:55]\n"
"v_add_f32 v240, v240, v28\n"
"v_mfma_f32_16x16x32_bf16 v[52:55], a[64:67], a[136:139], v[52:55]\n"
"s_waitcnt lgkmcnt(0)\n"
"v_add_f32 v241, v241, v29\n"
"v_mfma_f32_16x16x32_bf16 v[56:59], a[64:67], a[144:147], v[56:59]\n"
"v_add_f32 v242, v242, v30\n"
"v_mfma_f32_16x16x32_bf16 v[56:59], a[68:71], a[148:151], v[56:59]\n"
"global_load_dwordx4 a[60:63], v0, s[34:35] offset:3072 sc1\n"
"v_add_f32 v243, v243, v31\n"
"v_mfma_f32_16x16x32_bf16 v[84:87], a[76:79], a[148:151], v[84:87]\n"
"v_add_f32 v244, v244, v28\n"
"v_mfma_f32_16x16x32_bf16 v[84:87], a[72:75], a[144:147], v[84:87]\n"
"v_add_f32 v245, v245, v29\n"
"v_mfma_f32_16x16x32_bf16 a[4:7], a[72:75], a[152:155], a[4:7]\n"
"v_add_f32 v246, v246, v30\n"
"v_add_f32 v247, v247, v31\n"
"v_mfma_f32_16x16x32_bf16 a[4:7], a[76:79], a[156:159], a[4:7]\n"
"v_add_f32 v248, v248, v28\n"
"v_mfma_f32_16x16x32_bf16 a[0:3], a[68:71], a[156:159], a[0:3]\n"
"v_add_f32 v249, v249, v29\n"
"v_mfma_f32_16x16x32_bf16 a[0:3], a[64:67], a[152:155], a[0:3]\n"
"s_waitcnt vmcnt(8)\n"
"v_add_f32 v250, v250, v30\n"
"v_mfma_f32_16x16x32_bf16 v[88:91], a[80:83], a[96:99], v[88:91]\n"
"s_add_u32 s34, s36, 1466368\n"
"v_add_f32 v251, v251, v31\n"
"v_mfma_f32_16x16x32_bf16 v[88:91], a[84:87], a[100:103], v[88:91]\n"
"global_load_dwordx4 a[64:67], v0, s[34:35] offset:0 sc1\n"
"v_add_f32 v252, v252, v28\n"
"v_mfma_f32_16x16x32_bf16 v[116:119], a[92:95], a[100:103], v[116:119]\n"
"ds_read_b128 a[100:103], v5 offset:17408\n"
"v_add_f32 v253, v253, v29\n"
"v_mfma_f32_16x16x32_bf16 v[116:119], a[88:91], a[96:99], v[116:119]\n"
"ds_read_b128 a[96:99], v5 offset:16384\n"
"v_add_f32 v254, v254, v30\n"
"v_mfma_f32_16x16x32_bf16 v[120:123], a[88:91], a[104:107], v[120:123]\n"
"v_add_f32 v255, v255, v31\n"
"v_accvgpr_read_b32 v12, a28\n"
"v_mfma_f32_16x16x32_bf16 v[120:123], a[92:95], a[108:111], v[120:123]\n"
"v_accvgpr_read_b32 v13, a29\n"
"v_mfma_f32_16x16x32_bf16 v[92:95], a[84:87], a[108:111], v[92:95]\n"
"ds_read_b128 a[108:111], v5 offset:19456\n"
"v_accvgpr_read_b32 v14, a30\n"
"v_mfma_f32_16x16x32_bf16 v[92:95], a[80:83], a[104:107], v[92:95]\n"
"ds_read_b128 a[104:107], v5 offset:18432\n"
"v_accvgpr_read_b32 v15, a31\n"
"v_mfma_f32_16x16x32_bf16 v[96:99], a[80:83], a[112:115], v[96:99]\n"
"v_add_f32 v12, v12, v28\n"
"v_mfma_f32_16x16x32_bf16 v[96:99], a[84:87], a[116:119], v[96:99]\n"
"global_load_dwordx4 a[68:71], v0, s[34:35] offset:1024 sc1\n"
"v_add_f32 v13, v13, v29\n"
"v_mfma_f32_16x16x32_bf16 v[124:127], a[92:95], a[116:119], v[124:127]\n"
"ds_read_b128 a[116:119], v5 offset:21504\n"
"v_add_f32 v14, v14, v30\n"
"v_mfma_f32_16x16x32_bf16 v[124:127], a[88:91], a[112:115], v[124:127]\n"
"ds_read_b128 a[112:115], v5 offset:20480\n"
"v_add_f32 v15, v15, v31\n"
"v_mfma_f32_16x16x32_bf16 v[128:131], a[88:91], a[120:123], v[128:131]\n"
"v_accvgpr_write_b32 a28, v12\n"
"v_accvgpr_write_b32 a29, v13\n"
"v_mfma_f32_16x16x32_bf16 v[128:131], a[92:95], a[124:127], v[128:131]\n"
"v_accvgpr_write_b32 a30, v14\n"
"v_mfma_f32_16x16x32_bf16 v[100:103], a[84:87], a[124:127], v[100:103]\n"
"ds_read_b128 a[124:127], v5 offset:23552\n"
"v_accvgpr_write_b32 a31, v15\n"
"v_mfma_f32_16x16x32_bf16 v[100:103], a[80:83], a[120:123], v[100:103]\n"
"ds_read_b128 a[120:123], v5 offset:22528\n"
"v_mfma_f32_16x16x32_bf16 v[104:107], a[80:83], a[128:131], v[104:107]\n"
"v_mfma_f32_16x16x32_bf16 v[104:107], a[84:87], a[132:135], v[104:107]\n"
"global_load_dwordx4 a[72:75], v0, s[34:35] offset:2048 sc1\n"
"v_mfma_f32_16x16x32_bf16 v[132:135], a[92:95], a[132:135], v[132:135]\n"
"ds_read_b128 a[132:135], v5 offset:25600\n"
"v_mfma_f32_16x16x32_bf16 v[132:135], a[88:91], a[128:131], v[132:135]\n"
"ds_read_b128 a[128:131], v5 offset:24576\n"
"v_mfma_f32_16x16x32_bf16 v[136:139], a[88:91], a[136:139], v[136:139]\n"
"ds_read_b128 v[16:19], v7 offset:32768\n"
"v_mfma_f32_16x16x32_bf16 v[136:139], a[92:95], a[140:143], v[136:139]\n"
"v_mfma_f32_16x16x32_bf16 v[108:111], a[84:87], a[140:143], v[108:111]\n"
"ds_read_b128 a[140:143], v5 offset:27648\n"
"v_mfma_f32_16x16x32_bf16 v[108:111], a[80:83], a[136:139], v[108:111]\n"
"ds_read_b128 a[136:139], v5 offset:26624\n"
"v_mfma_f32_16x16x32_bf16 v[112:115], a[80:83], a[144:147], v[112:115]\n"
"v_mfma_f32_16x16x32_bf16 v[112:115], a[84:87], a[148:151], v[112:115]\n"
"global_load_dwordx4 a[76:79], v0, s[34:35] offset:3072 sc1\n"
"v_mfma_f32_16x16x32_bf16 v[140:143], a[92:95], a[148:151], v[140:143]\n"
"ds_read_b128 a[148:151], v5 offset:29696\n"
"v_mfma_f32_16x16x32_bf16 v[140:143], a[88:91], a[144:147], v[140:143]\n"
"ds_read_b128 a[144:147], v5 offset:28672\n"
"v_mfma_f32_16x16x32_bf16 a[12:15], a[88:91], a[152:155], a[12:15]\n"
"v_mfma_f32_16x16x32_bf16 a[12:15], a[92:95], a[156:159], a[12:15]\n"
"v_mfma_f32_16x16x32_bf16 a[8:11], a[84:87], a[156:159], a[8:11]\n"
"ds_read_b128 a[156:159], v5 offset:31744\n"
"v_mfma_f32_16x16x32_bf16 a[8:11], a[80:83], a[152:155], a[8:11]\n"
"s_waitcnt vmcnt(8) lgkmcnt(12)\n"
"ds_read_b128 a[152:155], v5 offset:30720\n"
"v_mfma_f32_16x16x32_bf16 v[32:35], a[32:35], a[96:99], v[32:35]\n"
"s_add_u32 s34, s36, 1470464\n"
"v_mfma_f32_16x16x32_bf16 v[32:35], a[36:39], a[100:103], v[32:35]\n"
"global_load_dwordx4 a[80:83], v0, s[34:35] offset:0 sc1\n"
"v_mfma_f32_16x16x32_bf16 v[60:63], a[44:47], a[100:103], v[60:63]\n"
"v_mfma_f32_16x16x32_bf16 v[60:63], a[40:43], a[96:99], v[60:63]\n"
"v_mfma_f32_16x16x32_bf16 v[64:67], a[40:43], a[104:107], v[64:67]\n"
"v_mfma_f32_16x16x32_bf16 v[64:67], a[44:47], a[108:111], v[64:67]\n"
"v_mfma_f32_16x16x32_bf16 v[36:39], a[36:39], a[108:111], v[36:39]\n"
"v_mfma_f32_16x16x32_bf16 v[36:39], a[32:35], a[104:107], v[36:39]\n"
"s_waitcnt lgkmcnt(9)\n"
"v_mfma_f32_16x16x32_bf16 v[40:43], a[32:35], a[112:115], v[40:43]\n"
"s_mov_b32 s52, %4\n"
"s_and_b32 s53, %5, 0xffff\n"
"v_mfma_f32_16x16x32_bf16 v[40:43], a[36:39], a[116:119], v[40:43]\n"
"global_load_dwordx4 a[84:87], v0, s[34:35] offset:1024 sc1\n"
"v_mfma_f32_16x16x32_bf16 v[68:71], a[44:47], a[116:119], v[68:71]\n"
"s_mov_b32 s54, 260352\n"
"s_mov_b32 s55, 0x20000\n"
"v_mfma_f32_16x16x32_bf16 v[68:71], a[40:43], a[112:115], v[68:71]\n"
"v_xor_b32 v8, 64, v7\n"
"v_mfma_f32_16x16x32_bf16 v[72:75], a[40:43], a[120:123], v[72:75]\n"
"s_waitcnt lgkmcnt(6)\n"
"v_lshlrev_b32 v12, 16, v16\n"
"v_mfma_f32_16x16x32_bf16 v[72:75], a[44:47], a[124:127], v[72:75]\n"
"v_and_b32 v13, s63, v16\n"
"v_lshlrev_b32 v14, 16, v17\n"
"v_mfma_f32_16x16x32_bf16 v[44:47], a[36:39], a[124:127], v[44:47]\n"
"v_and_b32 v15, s63, v17\n"
"v_mfma_f32_16x16x32_bf16 v[44:47], a[32:35], a[120:123], v[44:47]\n"
"s_waitcnt lgkmcnt(4)\n"
"v_xor_b32 v20, 64, v7\n"
"v_mfma_f32_16x16x32_bf16 v[48:51], a[32:35], a[128:131], v[48:51]\n"
"ds_read_b128 v[20:23], v20 offset:32768\n"
"v_mul_f32 v12, v12, v32\n"
"v_mfma_f32_16x16x32_bf16 v[48:51], a[36:39], a[132:135], v[48:51]\n"
"global_load_dwordx4 a[88:91], v0, s[34:35] offset:2048 sc1\n"
"v_mul_f32 v13, v13, v33\n"
"v_mfma_f32_16x16x32_bf16 v[76:79], a[44:47], a[132:135], v[76:79]\n"
"v_mul_f32 v14, v14, v34\n"
"v_mfma_f32_16x16x32_bf16 v[76:79], a[40:43], a[128:131], v[76:79]\n"
"v_mul_f32 v15, v15, v35\n"
"v_mfma_f32_16x16x32_bf16 v[80:83], a[40:43], a[136:139], v[80:83]\n"
"v_cvt_pk_bf16_f32 v24, v12, v13\n"
"v_cvt_pk_bf16_f32 v25, v14, v15\n"
"v_mfma_f32_16x16x32_bf16 v[80:83], a[44:47], a[140:143], v[80:83]\n"
"ds_write_b64 v7, v[24:25] offset:32768\n"
"v_mfma_f32_16x16x32_bf16 v[52:55], a[36:39], a[140:143], v[52:55]\n"
"v_lshlrev_b32 v12, 16, v18\n"
"v_mfma_f32_16x16x32_bf16 v[52:55], a[32:35], a[136:139], v[52:55]\n"
"s_waitcnt lgkmcnt(2)\n"
"v_and_b32 v13, s63, v18\n"
"v_lshlrev_b32 v14, 16, v19\n"
"v_mfma_f32_16x16x32_bf16 v[56:59], a[32:35], a[144:147], v[56:59]\n"
"L_st10279:\n"
"s_cbranch_execz L_retC\n"
"v_and_b32 v15, s63, v19\n"
"v_mfma_f32_16x16x32_bf16 v[56:59], a[36:39], a[148:151], v[56:59]\n"
"global_load_dwordx4 a[92:95], v0, s[34:35] offset:3072 sc1\n"
"v_mul_f32 v12, v12, v60\n"
"v_mfma_f32_16x16x32_bf16 v[84:87], a[44:47], a[148:151], v[84:87]\n"
"v_mul_f32 v13, v13, v61\n"
"v_mfma_f32_16x16x32_bf16 v[84:87], a[40:43], a[144:147], v[84:87]\n"
"v_mul_f32 v14, v14, v62\n"
"v_mul_f32 v15, v15, v63\n"
"v_mfma_f32_16x16x32_bf16 a[4:7], a[40:43], a[152:155], a[4:7]\n"
"v_cvt_pk_bf16_f32 v26, v12, v13\n"
"v_mfma_f32_16x16x32_bf16 a[4:7], a[44:47], a[156:159], a[4:7]\n"
"v_cvt_pk_bf16_f32 v27, v14, v15\n"
"v_mfma_f32_16x16x32_bf16 a[0:3], a[36:39], a[156:159], a[0:3]\n"
"ds_write_b64 v7, v[26:27] offset:32776\n"
"v_mfma_f32_16x16x32_bf16 a[0:3], a[32:35], a[152:155], a[0:3]\n"
"s_waitcnt vmcnt(8) lgkmcnt(2)\n"
"v_lshlrev_b32 v12, 16, v20\n"
"v_mfma_f32_16x16x32_bf16 v[88:91], a[48:51], a[96:99], v[88:91]\n"
"s_add_u32 s34, s36, 1482752\n"
"v_and_b32 v13, s63, v20\n"
"v_mfma_f32_16x16x32_bf16 v[88:91], a[52:55], a[100:103], v[88:91]\n"
"global_load_dwordx4 a[32:35], v0, s[34:35] offset:0 sc1\n"
"v_mfma_f32_16x16x32_bf16 v[116:119], a[60:63], a[100:103], v[116:119]\n"
"ds_read_b128 a[100:103], v3 offset:1024\n"
"v_mfma_f32_16x16x32_bf16 v[116:119], a[56:59], a[96:99], v[116:119]\n"
"ds_read_b128 a[96:99], v3 offset:0\n"
"v_mfma_f32_16x16x32_bf16 v[120:123], a[56:59], a[104:107], v[120:123]\n"
"v_lshlrev_b32 v14, 16, v21\n"
"v_mfma_f32_16x16x32_bf16 v[120:123], a[60:63], a[108:111], v[120:123]\n"
"v_and_b32 v15, s63, v21\n"
"v_mfma_f32_16x16x32_bf16 v[92:95], a[52:55], a[108:111], v[92:95]\n"
"ds_read_b128 a[108:111], v3 offset:3072\n"
"v_mfma_f32_16x16x32_bf16 v[92:95], a[48:51], a[104:107], v[92:95]\n"
"ds_read_b128 a[104:107], v3 offset:2048\n"
"v_mfma_f32_16x16x32_bf16 v[96:99], a[48:51], a[112:115], v[96:99]\n"
"ds_read_b128 v[16:19], v7 offset:34816\n"
"v_mfma_f32_16x16x32_bf16 v[96:99], a[52:55], a[116:119], v[96:99]\n"
"global_load_dwordx4 a[36:39], v0, s[34:35] offset:1024 sc1\n"
"v_mul_f32 v12, v12, v88\n"
"v_mul_f32 v13, v13, v89\n"
"v_mfma_f32_16x16x32_bf16 v[124:127], a[60:63], a[116:119], v[124:127]\n"
"ds_read_b128 a[116:119], v3 offset:5120\n"
"v_mul_f32 v14, v14, v90\n"
"v_mul_f32 v15, v15, v91\n"
"v_mfma_f32_16x16x32_bf16 v[124:127], a[56:59], a[112:115], v[124:127]\n"
"ds_read_b128 a[112:115], v3 offset:4096\n"
"v_cvt_pk_bf16_f32 v28, v12, v13\n"
"v_cvt_pk_bf16_f32 v29, v14, v15\n"
"v_mfma_f32_16x16x32_bf16 v[128:131], a[56:59], a[120:123], v[128:131]\n"
"ds_write_b64 v8, v[28:29] offset:32768\n"
"v_mfma_f32_16x16x32_bf16 v[128:131], a[60:63], a[124:127], v[128:131]\n"
"v_lshlrev_b32 v12, 16, v22\n"
"v_and_b32 v13, s63, v22\n"
"v_lshlrev_b32 v14, 16, v23\n"
"v_mfma_f32_16x16x32_bf16 v[100:103], a[52:55], a[124:127], v[100:103]\n"
"ds_read_b128 a[124:127], v3 offset:7168\n"
"v_and_b32 v15, s63, v23\n"
"v_mfma_f32_16x16x32_bf16 v[100:103], a[48:51], a[120:123], v[100:103]\n"
"ds_read_b128 a[120:123], v3 offset:6144\n"
"v_mul_f32 v12, v12, v116\n"
"v_mfma_f32_16x16x32_bf16 v[104:107], a[48:51], a[128:131], v[104:107]\n"
"v_mul_f32 v13, v13, v117\n"
"v_mul_f32 v14, v14, v118\n"
"v_mfma_f32_16x16x32_bf16 v[104:107], a[52:55], a[132:135], v[104:107]\n"
"global_load_dwordx4 a[40:43], v0, s[34:35] offset:2048 sc1\n"
"v_mul_f32 v15, v15, v119\n"
"v_mfma_f32_16x16x32_bf16 v[132:135], a[60:63], a[132:135], v[132:135]\n"
"ds_read_b128 a[132:135], v3 offset:9216\n"
"v_cvt_pk_bf16_f32 v30, v12, v13\n"
"v_mfma_f32_16x16x32_bf16 v[132:135], a[56:59], a[128:131], v[132:135]\n"
"ds_read_b128 a[128:131], v3 offset:8192\n"
"v_cvt_pk_bf16_f32 v31, v14, v15\n"
"v_mfma_f32_16x16x32_bf16 v[136:139], a[56:59], a[136:139], v[136:139]\n"
"ds_write_b64 v8, v[30:31] offset:32776\n"
"v_mfma_f32_16x16x32_bf16 v[136:139], a[60:63], a[140:143], v[136:139]\n"
"ds_read_b128 v[32:35], v2 offset:32768\n"
"v_mfma_f32_16x16x32_bf16 v[108:111], a[52:55], a[140:143], v[108:111]\n"
"s_add_u32 s96, s36, 1552384\n"
"ds_read_b128 a[140:143], v3 offset:11264\n"
"v_mfma_f32_16x16x32_bf16 v[108:111], a[48:51], a[136:139], v[108:111]\n"
"ds_read_b128 a[136:139], v3 offset:10240\n"
"v_mfma_f32_16x16x32_bf16 v[112:115], a[48:51], a[144:147], v[112:115]\n"
"global_load_dwordx4 v[88:91], v0, s[96:97] offset:-4096 sc1\n"
"ds_read_b128 v[60:63], v2 offset:33792\n"
"v_mfma_f32_16x16x32_bf16 v[112:115], a[52:55], a[148:151], v[112:115]\n"
"s_waitcnt lgkmcnt(12)\n"
"global_load_dwordx4 a[44:47], v0, s[34:35] offset:3072 sc1\n"
"v_lshlrev_b32 v12, 16, v16\n"
"v_mfma_f32_16x16x32_bf16 v[140:143], a[60:63], a[148:151], v[140:143]\n"
"ds_read_b128 a[148:151], v3 offset:13312\n"
"v_and_b32 v13, s63, v16\n"
"v_mfma_f32_16x16x32_bf16 v[140:143], a[56:59], a[144:147], v[140:143]\n"
"ds_read_b128 a[144:147], v3 offset:12288\n"
"v_lshlrev_b32 v14, 16, v17\n"
"v_mfma_f32_16x16x32_bf16 a[12:15], a[56:59], a[152:155], a[12:15]\n"
"v_and_b32 v15, s63, v17\n"
"v_xor_b32 v20, 64, v7\n"
"v_mfma_f32_16x16x32_bf16 a[12:15], a[60:63], a[156:159], a[12:15]\n"
"ds_read_b128 v[20:23], v20 offset:34816\n"
"v_mul_f32 v12, v12, v36\n"
"v_mfma_f32_16x16x32_bf16 a[8:11], a[52:55], a[156:159], a[8:11]\n"
"s_waitcnt lgkmcnt(6)\n"
"buffer_store_dwordx4 v[32:35], v6, s[52:55], s80 offen offset:0 nt\n"
"ds_read_b128 a[156:159], v3 offset:15360\n"
"v_mul_f32 v13, v13, v37\n"
"v_mul_f32 v14, v14, v38\n"
"v_mfma_f32_16x16x32_bf16 a[8:11], a[48:51], a[152:155], a[8:11]\n"
"s_waitcnt vmcnt(10)\n"
"ds_read_b128 a[152:155], v3 offset:14336\n"
"v_mul_f32 v15, v15, v39\n"
"v_mfma_f32_16x16x32_bf16 v[144:147], a[64:67], a[96:99], v[144:147]\n"
"s_add_u32 s34, s36, 1486848\n"
"v_cvt_pk_bf16_f32 v24, v12, v13\n"
"v_cvt_pk_bf16_f32 v25, v14, v15\n"
"v_mfma_f32_16x16x32_bf16 v[144:147], a[68:71], a[100:103], v[144:147]\n"
"global_load_dwordx4 a[48:51], v0, s[34:35] offset:0 sc1\n"
"v_mfma_f32_16x16x32_bf16 v[172:175], a[76:79], a[100:103], v[172:175]\n"
"global_load_dwordx4 v[116:119], v0, s[96:97] offset:-3072 sc1\n"
"v_mfma_f32_16x16x32_bf16 v[172:175], a[72:75], a[96:99], v[172:175]\n"
"ds_write_b64 v7, v[24:25] offset:34816\n"
"v_mfma_f32_16x16x32_bf16 v[176:179], a[72:75], a[104:107], v[176:179]\n"
"v_lshlrev_b32 v12, 16, v18\n"
"v_and_b32 v13, s63, v18\n"
"v_mfma_f32_16x16x32_bf16 v[176:179], a[76:79], a[108:111], v[176:179]\n"
"v_lshlrev_b32 v14, 16, v19\n"
"v_and_b32 v15, s63, v19\n"
"v_mfma_f32_16x16x32_bf16 v[148:151], a[68:71], a[108:111], v[148:151]\n"
"v_mul_f32 v12, v12, v64\n"
"v_mul_f32 v13, v13, v65\n"
"v_mfma_f32_16x16x32_bf16 v[148:151], a[64:67], a[104:107], v[148:151]\n"
"v_mul_f32 v14, v14, v66\n"
"v_mul_f32 v15, v15, v67\n"
"v_mfma_f32_16x16x32_bf16 v[152:155], a[64:67], a[112:115], v[152:155]\n"
"v_cvt_pk_bf16_f32 v26, v12, v13\n"
"v_cvt_pk_bf16_f32 v27, v14, v15\n"
"v_mfma_f32_16x16x32_bf16 v[152:155], a[68:71], a[116:119], v[152:155]\n"
"global_load_dwordx4 a[52:55], v0, s[34:35] offset:1024 sc1\n"
"v_mfma_f32_16x16x32_bf16 v[180:183], a[76:79], a[116:119], v[180:183]\n"
"s_waitcnt lgkmcnt(6)\n"
"buffer_store_dwordx4 v[60:63], v6, s[52:55], s81 offen offset:1024 nt\n"
"ds_write_b64 v7, v[26:27] offset:34824\n"
"v_mfma_f32_16x16x32_bf16 v[180:183], a[72:75], a[112:115], v[180:183]\n"
"s_waitcnt lgkmcnt(4)\n"
"v_lshlrev_b32 v12, 16, v20\n"
"v_and_b32 v13, s63, v20\n"
"v_mfma_f32_16x16x32_bf16 v[184:187], a[72:75], a[120:123], v[184:187]\n"
"v_lshlrev_b32 v14, 16, v21\n"
"v_and_b32 v15, s63, v21\n"
"v_mfma_f32_16x16x32_bf16 v[184:187], a[76:79], a[124:127], v[184:187]\n"
"ds_read_b128 v[16:19], v7 offset:36864\n"
"v_mul_f32 v12, v12, v92\n"
"v_mfma_f32_16x16x32_bf16 v[156:159], a[68:71], a[124:127], v[156:159]\n"
"v_mul_f32 v13, v13, v93\n"
"v_mul_f32 v14, v14, v94\n"
"v_mfma_f32_16x16x32_bf16 v[156:159], a[64:67], a[120:123], v[156:159]\n"
"v_mul_f32 v15, v15, v95\n"
"v_cvt_pk_bf16_f32 v28, v12, v13\n"
"v_mfma_f32_16x16x32_bf16 v[160:163], a[64:67], a[128:131], v[160:163]\n"
"v_cvt_pk_bf16_f32 v29, v14, v15\n"
"v_mfma_f32_16x16x32_bf16 v[160:163], a[68:71], a[132:135], v[160:163]\n"
"global_load_dwordx4 a[56:59], v0, s[34:35] offset:2048 sc1\n"
"v_mfma_f32_16x16x32_bf16 v[188:191], a[76:79], a[132:135], v[188:191]\n"
"ds_write_b64 v8, v[28:29] offset:34816\n"
"v_mfma_f32_16x16x32_bf16 v[188:191], a[72:75], a[128:131], v[188:191]\n"
"v_lshlrev_b32 v12, 16, v22\n"
"v_and_b32 v13, s63, v22\n"
"v_mfma_f32_16x16x32_bf16 v[192:195], a[72:75], a[136:139], v[192:195]\n"
"v_lshlrev_b32 v14, 16, v23\n"
"v_and_b32 v15, s63, v23\n"
"v_mfma_f32_16x16x32_bf16 v[192:195], a[76:79], a[140:143], v[192:195]\n"
"v_mul_f32 v12, v12, v120\n"
"v_mul_f32 v13, v13, v121\n"
"v_mfma_f32_16x16x32_bf16 v[164:167], a[68:71], a[140:143], v[164:167]\n"
"v_mul_f32 v14, v14, v122\n"
"v_mul_f32 v15, v15, v123\n"
"v_mfma_f32_16x16x32_bf16 v[164:167], a[64:67], a[136:139], v[164:167]\n"
"s_waitcnt lgkmcnt(4)\n"
"v_cvt_pk_bf16_f32 v30, v12, v13\n"
"v_cvt_pk_bf16_f32 v31, v14, v15\n"
"v_mfma_f32_16x16x32_bf16 v[168:171], a[64:67], a[144:147], v[168:171]\n"
"ds_write_b64 v8, v[30:31] offset:34824\n"
"v_mfma_f32_16x16x32_bf16 v[168:171], a[68:71], a[148:151], v[168:171]\n"
"global_load_dwordx4 a[60:63], v0, s[34:35] offset:3072 sc1\n"
"ds_read_b128 v[36:39], v2 offset:34816\n"
"v_mfma_f32_16x16x32_bf16 v[196:199], a[76:79], a[148:151], v[196:199]\n"
"s_waitcnt lgkmcnt(3)\n"
"ds_read_b128 v[64:67], v2 offset:35840\n"
"v_lshlrev_b32 v12, 16, v16\n"
"v_mfma_f32_16x16x32_bf16 v[196:199], a[72:75], a[144:147], v[196:199]\n"
"v_and_b32 v13, s63, v16\n"
"v_lshlrev_b32 v14, 16, v17\n"
"v_mfma_f32_16x16x32_bf16 a[20:23], a[72:75], a[152:155], a[20:23]\n"
"v_and_b32 v15, s63, v17\n"
"v_xor_b32 v20, 64, v7\n"
"v_mfma_f32_16x16x32_bf16 a[20:23], a[76:79], a[156:159], a[20:23]\n"
"ds_read_b128 v[20:23], v20 offset:36864\n"
"v_mul_f32 v12, v12, v40\n"
"v_mfma_f32_16x16x32_bf16 a[16:19], a[68:71], a[156:159], a[16:19]\n"
"v_mul_f32 v13, v13, v41\n"
"v_mul_f32 v14, v14, v42\n"
"v_mfma_f32_16x16x32_bf16 a[16:19], a[64:67], a[152:155], a[16:19]\n"
"s_waitcnt vmcnt(12)\n"
"global_load_dwordx4 v[92:95], v0, s[96:97] offset:-2048 sc1\n"
"v_mul_f32 v15, v15, v43\n"
"v_mfma_f32_16x16x32_bf16 v[200:203], a[80:83], a[96:99], v[200:203]\n"
"s_add_u32 s34, s36, 1499136\n"
"v_cvt_pk_bf16_f32 v24, v12, v13\n"
"v_mfma_f32_16x16x32_bf16 v[200:203], a[84:87], a[100:103], v[200:203]\n"
"global_load_dwordx4 a[64:67], v0, s[34:35] offset:0 sc1\n"
"v_cvt_pk_bf16_f32 v25, v14, v15\n"
"v_mfma_f32_16x16x32_bf16 v[228:231], a[92:95], a[100:103], v[228:231]\n"
"ds_read_b128 a[100:103], v4 offset:1024\n"
"v_mfma_f32_16x16x32_bf16 v[228:231], a[88:91], a[96:99], v[228:231]\n"
"ds_read_b128 a[96:99], v4 offset:0\n"
"v_mfma_f32_16x16x32_bf16 v[232:235], a[88:91], a[104:107], v[232:235]\n"
"ds_write_b64 v7, v[24:25] offset:36864\n"
"v_mfma_f32_16x16x32_bf16 v[232:235], a[92:95], a[108:111], v[232:235]\n"
"v_lshlrev_b32 v12, 16, v18\n"
"v_and_b32 v13, s63, v18\n"
"v_mfma_f32_16x16x32_bf16 v[204:207], a[84:87], a[108:111], v[204:207]\n"
"ds_read_b128 a[108:111], v4 offset:3072\n"
"v_lshlrev_b32 v14, 16, v19\n"
"v_mfma_f32_16x16x32_bf16 v[204:207], a[80:83], a[104:107], v[204:207]\n"
"ds_read_b128 a[104:107], v4 offset:2048\n"
"v_and_b32 v15, s63, v19\n"
"v_mfma_f32_16x16x32_bf16 v[208:211], a[80:83], a[112:115], v[208:211]\n"
"s_waitcnt lgkmcnt(7)\n"
"buffer_store_dwordx4 v[36:39], v6, s[52:55], s82 offen offset:2048 nt\n"
"global_load_dwordx4 v[120:123], v0, s[96:97] offset:-1024 sc1\n"
"v_mul_f32 v12, v12, v68\n"
"v_mul_f32 v13, v13, v69\n"
"v_mul_f32 v14, v14, v70\n"
"v_mfma_f32_16x16x32_bf16 v[208:211], a[84:87], a[116:119], v[208:211]\n"
"global_load_dwordx4 a[68:71], v0, s[34:35] offset:1024 sc1\n"
"v_mul_f32 v15, v15, v71\n"
"v_mfma_f32_16x16x32_bf16 v[236:239], a[92:95], a[116:119], v[236:239]\n"
"ds_read_b128 a[116:119], v4 offset:5120\n"
"v_cvt_pk_bf16_f32 v26, v12, v13\n"
"v_mfma_f32_16x16x32_bf16 v[236:239], a[88:91], a[112:115], v[236:239]\n"
"ds_read_b128 a[112:115], v4 offset:4096\n"
"v_cvt_pk_bf16_f32 v27, v14, v15\n"
"v_mfma_f32_16x16x32_bf16 v[240:243], a[88:91], a[120:123], v[240:243]\n"
"ds_write_b64 v7, v[26:27] offset:36872\n"
"v_mfma_f32_16x16x32_bf16 v[240:243], a[92:95], a[124:127], v[240:243]\n"
"s_waitcnt lgkmcnt(8)\n"
"v_lshlrev_b32 v12, 16, v20\n"
"v_and_b32 v13, s63, v20\n"
"v_mfma_f32_16x16x32_bf16 v[212:215], a[84:87], a[124:127], v[212:215]\n"
"ds_read_b128 a[124:127], v4 offset:7168\n"
"v_lshlrev_b32 v14, 16, v21\n"
"v_mfma_f32_16x16x32_bf16 v[212:215], a[80:83], a[120:123], v[212:215]\n"
"ds_read_b128 a[120:123], v4 offset:6144\n"
"v_and_b32 v15, s63, v21\n"
"v_mfma_f32_16x16x32_bf16 v[216:219], a[80:83], a[128:131], v[216:219]\n"
"ds_read_b128 v[16:19], v7 offset:38912\n"
"v_mul_f32 v12, v12, v96\n"
"v_mfma_f32_16x16x32_bf16 v[216:219], a[84:87], a[132:135], v[216:219]\n"
"global_load_dwordx4 a[72:75], v0, s[34:35] offset:2048 sc1\n"
"v_mul_f32 v13, v13, v97\n"
"v_mfma_f32_16x16x32_bf16 v[244:247], a[92:95], a[132:135], v[244:247]\n"
"ds_read_b128 a[132:135], v4 offset:9216\n"
"v_mul_f32 v14, v14, v98\n"
"v_mfma_f32_16x16x32_bf16 v[244:247], a[88:91], a[128:131], v[244:247]\n"
"ds_read_b128 a[128:131], v4 offset:8192\n"
"v_mul_f32 v15, v15, v99\n"
"v_mfma_f32_16x16x32_bf16 v[248:251], a[88:91], a[136:139], v[248:251]\n"
"buffer_store_dwordx4 v[64:67], v6, s[52:55], s83 offen offset:3072 nt\n"
"global_load_dwordx4 v[32:35], v0, s[96:97] offset:0 sc1\n"
"v_cvt_pk_bf16_f32 v28, v12, v13\n"
"v_cvt_pk_bf16_f32 v29, v14, v15\n"
"v_mfma_f32_16x16x32_bf16 v[248:251], a[92:95], a[140:143], v[248:251]\n"
"ds_write_b64 v8, v[28:29] offset:36864\n"
"v_mfma_f32_16x16x32_bf16 v[220:223], a[84:87], a[140:143], v[220:223]\n"
"ds_read_b128 a[140:143], v4 offset:11264\n"
"v_lshlrev_b32 v12, 16, v22\n"
"v_mfma_f32_16x16x32_bf16 v[220:223], a[80:83], a[136:139], v[220:223]\n"
"ds_read_b128 a[136:139], v4 offset:10240\n"
"v_and_b32 v13, s63, v22\n"
"v_mfma_f32_16x16x32_bf16 v[224:227], a[80:83], a[144:147], v[224:227]\n"
"v_lshlrev_b32 v14, 16, v23\n"
"v_and_b32 v15, s63, v23\n"
"v_mfma_f32_16x16x32_bf16 v[224:227], a[84:87], a[148:151], v[224:227]\n"
"global_load_dwordx4 a[76:79], v0, s[34:35] offset:3072 sc1\n"
"v_mul_f32 v12, v12, v124\n"
"v_mfma_f32_16x16x32_bf16 v[252:255], a[92:95], a[148:151], v[252:255]\n"
"ds_read_b128 a[148:151], v4 offset:13312\n"
"v_mul_f32 v13, v13, v125\n"
"v_mfma_f32_16x16x32_bf16 v[252:255], a[88:91], a[144:147], v[252:255]\n"
"ds_read_b128 a[144:147], v4 offset:12288\n"
"v_mul_f32 v14, v14, v126\n"
"v_mfma_f32_16x16x32_bf16 a[28:31], a[88:91], a[152:155], a[28:31]\n"
"v_mul_f32 v15, v15, v127\n"
"v_cvt_pk_bf16_f32 v30, v12, v13\n"
"v_mfma_f32_16x16x32_bf16 a[28:31], a[92:95], a[156:159], a[28:31]\n"
"global_load_dwordx4 v[60:63], v0, s[96:97] offset:1024 sc1\n"
"v_cvt_pk_bf16_f32 v31, v14, v15\n"
"v_mfma_f32_16x16x32_bf16 a[24:27], a[84:87], a[156:159], a[24:27]\n"
"ds_read_b128 a[156:159], v4 offset:15360\n"
"v_mfma_f32_16x16x32_bf16 a[24:27], a[80:83], a[152:155], a[24:27]\n"
"s_waitcnt vmcnt(17) lgkmcnt(14)\n"
"ds_read_b128 a[152:155], v4 offset:14336\n"
"v_mfma_f32_16x16x32_bf16 v[144:147], a[32:35], a[96:99], v[144:147]\n"
"s_add_u32 s34, s36, 1503232\n"
"ds_write_b64 v8, v[30:31] offset:36872\n"
"v_mfma_f32_16x16x32_bf16 v[144:147], a[36:39], a[100:103], v[144:147]\n"
"global_load_dwordx4 a[80:83], v0, s[34:35] offset:0 sc1\n"
"ds_read_b128 v[40:43], v2 offset:36864\n"
"v_mfma_f32_16x16x32_bf16 v[172:175], a[44:47], a[100:103], v[172:175]\n"
"s_waitcnt lgkmcnt(11)\n"
"ds_read_b128 v[68:71], v2 offset:37888\n"
"v_lshlrev_b32 v12, 16, v16\n"
"v_mfma_f32_16x16x32_bf16 v[172:175], a[40:43], a[96:99], v[172:175]\n"
"v_and_b32 v13, s63, v16\n"
"v_lshlrev_b32 v14, 16, v17\n"
"v_mfma_f32_16x16x32_bf16 v[176:179], a[40:43], a[104:107], v[176:179]\n"
"v_and_b32 v15, s63, v17\n"
"v_xor_b32 v20, 64, v7\n"
"v_mfma_f32_16x16x32_bf16 v[176:179], a[44:47], a[108:111], v[176:179]\n"
"ds_read_b128 v[20:23], v20 offset:38912\n"
"v_mul_f32 v12, v12, v44\n"
"v_mfma_f32_16x16x32_bf16 v[148:151], a[36:39], a[108:111], v[148:151]\n"
"v_mul_f32 v13, v13, v45\n"
"v_mul_f32 v14, v14, v46\n"
"v_mfma_f32_16x16x32_bf16 v[148:151], a[32:35], a[104:107], v[148:151]\n"
"v_mul_f32 v15, v15, v47\n"
"v_cvt_pk_bf16_f32 v24, v12, v13\n"
"v_mfma_f32_16x16x32_bf16 v[152:155], a[32:35], a[112:115], v[152:155]\n"
"v_cvt_pk_bf16_f32 v25, v14, v15\n"
"v_mfma_f32_16x16x32_bf16 v[152:155], a[36:39], a[116:119], v[152:155]\n"
"global_load_dwordx4 a[84:87], v0, s[34:35] offset:1024 sc1\n"
"v_mfma_f32_16x16x32_bf16 v[180:183], a[44:47], a[116:119], v[180:183]\n"
"global_load_dwordx4 v[96:99], v0, s[96:97] offset:2048 sc1\n"
"v_mfma_f32_16x16x32_bf16 v[180:183], a[40:43], a[112:115], v[180:183]\n"
"ds_write_b64 v7, v[24:25] offset:38912\n"
"v_mfma_f32_16x16x32_bf16 v[184:187], a[40:43], a[120:123], v[184:187]\n"
"v_lshlrev_b32 v12, 16, v18\n"
"v_and_b32 v13, s63, v18\n"
"v_mfma_f32_16x16x32_bf16 v[184:187], a[44:47], a[124:127], v[184:187]\n"
"v_lshlrev_b32 v14, 16, v19\n"
"v_and_b32 v15, s63, v19\n"
"v_mfma_f32_16x16x32_bf16 v[156:159], a[36:39], a[124:127], v[156:159]\n"
"v_mul_f32 v12, v12, v72\n"
"v_mul_f32 v13, v13, v73\n"
"v_mfma_f32_16x16x32_bf16 v[156:159], a[32:35], a[120:123], v[156:159]\n"
"s_waitcnt lgkmcnt(9)\n"
"v_mul_f32 v14, v14, v74\n"
"v_mul_f32 v15, v15, v75\n"
"v_mfma_f32_16x16x32_bf16 v[160:163], a[32:35], a[128:131], v[160:163]\n"
"v_cvt_pk_bf16_f32 v26, v12, v13\n"
"v_cvt_pk_bf16_f32 v27, v14, v15\n"
"v_mfma_f32_16x16x32_bf16 v[160:163], a[36:39], a[132:135], v[160:163]\n"
"global_load_dwordx4 a[88:91], v0, s[34:35] offset:2048 sc1\n"
"v_mfma_f32_16x16x32_bf16 v[188:191], a[44:47], a[132:135], v[188:191]\n"
"s_waitcnt lgkmcnt(3)\n"
"buffer_store_dwordx4 v[40:43], v6, s[52:55], s84 offen offset:0 nt\n"
"ds_write_b64 v7, v[26:27] offset:38920\n"
"v_mfma_f32_16x16x32_bf16 v[188:191], a[40:43], a[128:131], v[188:191]\n"
"s_waitcnt lgkmcnt(2)\n"
"global_load_dwordx4 v[124:127], v0, s[96:97] offset:3072 sc1\n"
"v_lshlrev_b32 v12, 16, v20\n"
"v_mfma_f32_16x16x32_bf16 v[192:195], a[40:43], a[136:139], v[192:195]\n"
"v_and_b32 v13, s63, v20\n"
"v_lshlrev_b32 v14, 16, v21\n"
"v_mfma_f32_16x16x32_bf16 v[192:195], a[44:47], a[140:143], v[192:195]\n"
"v_and_b32 v15, s63, v21\n"
"v_mul_f32 v12, v12, v100\n"
"v_mfma_f32_16x16x32_bf16 v[164:167], a[36:39], a[140:143], v[164:167]\n"
"v_mul_f32 v13, v13, v101\n"
"v_mul_f32 v14, v14, v102\n"
"v_mfma_f32_16x16x32_bf16 v[164:167], a[32:35], a[136:139], v[164:167]\n"
"v_mul_f32 v15, v15, v103\n"
"v_cvt_pk_bf16_f32 v28, v12, v13\n"
"v_mfma_f32_16x16x32_bf16 v[168:171], a[32:35], a[144:147], v[168:171]\n"
"v_cvt_pk_bf16_f32 v29, v14, v15\n"
"v_mfma_f32_16x16x32_bf16 v[168:171], a[36:39], a[148:151], v[168:171]\n"
"global_load_dwordx4 a[92:95], v0, s[34:35] offset:3072 sc1\n"
"v_mfma_f32_16x16x32_bf16 v[196:199], a[44:47], a[148:151], v[196:199]\n"
"ds_write_b64 v8, v[28:29] offset:38912\n"
"v_mfma_f32_16x16x32_bf16 v[196:199], a[40:43], a[144:147], v[196:199]\n"
"v_lshlrev_b32 v12, 16, v22\n"
"v_and_b32 v13, s63, v22\n"
"v_mfma_f32_16x16x32_bf16 a[20:23], a[40:43], a[152:155], a[20:23]\n"
"v_lshlrev_b32 v14, 16, v23\n"
"v_and_b32 v15, s63, v23\n"
"v_mfma_f32_16x16x32_bf16 a[20:23], a[44:47], a[156:159], a[20:23]\n"
"v_mul_f32 v12, v12, v128\n"
"v_mfma_f32_16x16x32_bf16 a[16:19], a[36:39], a[156:159], a[16:19]\n"
"buffer_store_dwordx4 v[68:71], v6, s[52:55], s85 offen offset:1024 nt\n"
"v_mul_f32 v13, v13, v129\n"
"v_mul_f32 v14, v14, v130\n"
"v_mul_f32 v15, v15, v131\n"
"v_mfma_f32_16x16x32_bf16 a[16:19], a[32:35], a[152:155], a[16:19]\n"
"s_waitcnt vmcnt(18)\n"
"v_cvt_pk_bf16_f32 v30, v12, v13\n"
"v_cvt_pk_bf16_f32 v31, v14, v15\n"
"v_mfma_f32_16x16x32_bf16 v[200:203], a[48:51], a[96:99], v[200:203]\n"
"s_add_u32 s34, s36, 1540096\n"
"ds_write_b64 v8, v[30:31] offset:38920\n"
"v_mfma_f32_16x16x32_bf16 v[200:203], a[52:55], a[100:103], v[200:203]\n"
"global_load_dwordx4 a[32:35], v0, s[34:35] offset:0 sc1\n"
"ds_read_b128 v[44:47], v2 offset:38912\n"
"v_mfma_f32_16x16x32_bf16 v[228:231], a[60:63], a[100:103], v[228:231]\n"
"ds_read_b128 a[100:103], v5 offset:1024\n"
"v_mfma_f32_16x16x32_bf16 v[228:231], a[56:59], a[96:99], v[228:231]\n"
"ds_read_b128 a[96:99], v5 offset:0\n"
"v_mfma_f32_16x16x32_bf16 v[232:235], a[56:59], a[104:107], v[232:235]\n"
"ds_read_b128 v[72:75], v2 offset:39936\n"
"v_accvgpr_read_b32 v16, a160\n"
"v_mfma_f32_16x16x32_bf16 v[232:235], a[60:63], a[108:111], v[232:235]\n"
"v_accvgpr_read_b32 v17, a161\n"
"v_accvgpr_read_b32 v18, a162\n"
"v_mfma_f32_16x16x32_bf16 v[204:207], a[52:55], a[108:111], v[204:207]\n"
"ds_read_b128 a[108:111], v5 offset:3072\n"
"v_accvgpr_read_b32 v19, a163\n"
"v_mfma_f32_16x16x32_bf16 v[204:207], a[48:51], a[104:107], v[204:207]\n"
"ds_read_b128 a[104:107], v5 offset:2048\n"
"v_lshlrev_b32 v12, 16, v16\n"
"v_mfma_f32_16x16x32_bf16 v[208:211], a[48:51], a[112:115], v[208:211]\n"
"v_and_b32 v13, s63, v16\n"
"v_lshlrev_b32 v14, 16, v17\n"
"v_mfma_f32_16x16x32_bf16 v[208:211], a[52:55], a[116:119], v[208:211]\n"
"global_load_dwordx4 a[36:39], v0, s[34:35] offset:1024 sc1\n"
"v_and_b32 v15, s63, v17\n"
"v_mfma_f32_16x16x32_bf16 v[236:239], a[60:63], a[116:119], v[236:239]\n"
"ds_read_b128 a[116:119], v5 offset:5120\n"
"v_mul_f32 v12, v12, v48\n"
"v_mfma_f32_16x16x32_bf16 v[236:239], a[56:59], a[112:115], v[236:239]\n"
"ds_read_b128 a[112:115], v5 offset:4096\n"
"v_mul_f32 v13, v13, v49\n"
"v_mfma_f32_16x16x32_bf16 v[240:243], a[56:59], a[120:123], v[240:243]\n"
"v_mul_f32 v14, v14, v50\n"
"v_mul_f32 v15, v15, v51\n"
"v_mfma_f32_16x16x32_bf16 v[240:243], a[60:63], a[124:127], v[240:243]\n"
"v_cvt_pk_bf16_f32 v24, v12, v13\n"
"v_mfma_f32_16x16x32_bf16 v[212:215], a[52:55], a[124:127], v[212:215]\n"
"ds_read_b128 a[124:127], v5 offset:7168\n"
"v_cvt_pk_bf16_f32 v25, v14, v15\n"
"v_mfma_f32_16x16x32_bf16 v[212:215], a[48:51], a[120:123], v[212:215]\n"
"ds_read_b128 a[120:123], v5 offset:6144\n"
"v_mfma_f32_16x16x32_bf16 v[216:219], a[48:51], a[128:131], v[216:219]\n"
"ds_write_b64 v7, v[24:25] offset:32768\n"
"v_mfma_f32_16x16x32_bf16 v[216:219], a[52:55], a[132:135], v[216:219]\n"
"global_load_dwordx4 a[40:43], v0, s[34:35] offset:2048 sc1\n"
"v_lshlrev_b32 v12, 16, v18\n"
"v_mfma_f32_16x16x32_bf16 v[244:247], a[60:63], a[132:135], v[244:247]\n"
"ds_read_b128 a[132:135], v5 offset:9216\n"
"v_and_b32 v13, s63, v18\n"
"v_mfma_f32_16x16x32_bf16 v[244:247], a[56:59], a[128:131], v[244:247]\n"
"ds_read_b128 a[128:131], v5 offset:8192\n"
"v_lshlrev_b32 v14, 16, v19\n"
"v_mfma_f32_16x16x32_bf16 v[248:251], a[56:59], a[136:139], v[248:251]\n"
"s_add_u32 s96, s36, 1560576\n"
"v_and_b32 v15, s63, v19\n"
"v_mul_f32 v12, v12, v76\n"
"v_mfma_f32_16x16x32_bf16 v[248:251], a[60:63], a[140:143], v[248:251]\n"
"global_load_dwordx4 v[100:103], v0, s[96:97] offset:-4096 sc1\n"
"v_mul_f32 v13, v13, v77\n"
"v_mfma_f32_16x16x32_bf16 v[220:223], a[52:55], a[140:143], v[220:223]\n"
"ds_read_b128 a[140:143], v5 offset:11264\n"
"v_mul_f32 v14, v14, v78\n"
"v_mfma_f32_16x16x32_bf16 v[220:223], a[48:51], a[136:139], v[220:223]\n"
"ds_read_b128 a[136:139], v5 offset:10240\n"
"v_mul_f32 v15, v15, v79\n"
"v_mfma_f32_16x16x32_bf16 v[224:227], a[48:51], a[144:147], v[224:227]\n"
"v_cvt_pk_bf16_f32 v26, v12, v13\n"
"v_cvt_pk_bf16_f32 v27, v14, v15\n"
"v_mfma_f32_16x16x32_bf16 v[224:227], a[52:55], a[148:151], v[224:227]\n"
"global_load_dwordx4 a[44:47], v0, s[34:35] offset:3072 sc1\n"
"v_mfma_f32_16x16x32_bf16 v[252:255], a[60:63], a[148:151], v[252:255]\n"
"ds_read_b128 a[148:151], v5 offset:13312\n"
"v_mfma_f32_16x16x32_bf16 v[252:255], a[56:59], a[144:147], v[252:255]\n"
"ds_read_b128 a[144:147], v5 offset:12288\n"
"v_mfma_f32_16x16x32_bf16 a[28:31], a[56:59], a[152:155], a[28:31]\n"
"s_waitcnt lgkmcnt(15)\n"
"buffer_store_dwordx4 v[44:47], v6, s[52:55], s86 offen offset:2048 nt\n"
"ds_write_b64 v7, v[26:27] offset:32776\n"
"v_mfma_f32_16x16x32_bf16 a[28:31], a[60:63], a[156:159], a[28:31]\n"
"v_accvgpr_read_b32 v16, a164\n"
"v_accvgpr_read_b32 v17, a165\n"
"v_mfma_f32_16x16x32_bf16 a[24:27], a[52:55], a[156:159], a[24:27]\n"
"ds_read_b128 a[156:159], v5 offset:15360\n"
"v_accvgpr_read_b32 v18, a166\n"
"v_mfma_f32_16x16x32_bf16 a[24:27], a[48:51], a[152:155], a[24:27]\n"
"s_waitcnt vmcnt(15) lgkmcnt(13)\n"
"ds_read_b128 a[152:155], v5 offset:14336\n"
"v_accvgpr_read_b32 v19, a167\n"
"v_mfma_f32_16x16x32_bf16 v[144:147], a[64:67], a[96:99], v[144:147]\n"
"s_add_u32 s34, s36, 1544192\n"
"global_load_dwordx4 v[128:131], v0, s[96:97] offset:-3072 sc1\n"
"v_mfma_f32_16x16x32_bf16 v[144:147], a[68:71], a[100:103], v[144:147]\n"
"global_load_dwordx4 a[48:51], v0, s[34:35] offset:0 sc1\n"
"v_lshlrev_b32 v12, 16, v16\n"
"v_mfma_f32_16x16x32_bf16 v[172:175], a[76:79], a[100:103], v[172:175]\n"
"v_and_b32 v13, s63, v16\n"
"v_lshlrev_b32 v14, 16, v17\n"
"v_mfma_f32_16x16x32_bf16 v[172:175], a[72:75], a[96:99], v[172:175]\n"
"v_and_b32 v15, s63, v17\n"
"v_mul_f32 v12, v12, v104\n"
"v_mfma_f32_16x16x32_bf16 v[176:179], a[72:75], a[104:107], v[176:179]\n"
"v_mul_f32 v13, v13, v105\n"
"v_mul_f32 v14, v14, v106\n"
"v_mfma_f32_16x16x32_bf16 v[176:179], a[76:79], a[108:111], v[176:179]\n"
"v_mul_f32 v15, v15, v107\n"
"v_mfma_f32_16x16x32_bf16 v[148:151], a[68:71], a[108:111], v[148:151]\n"
"v_cvt_pk_bf16_f32 v28, v12, v13\n"
"v_mfma_f32_16x16x32_bf16 v[148:151], a[64:67], a[104:107], v[148:151]\n"
"s_waitcnt lgkmcnt(10)\n"
"v_cvt_pk_bf16_f32 v29, v14, v15\n"
"v_mfma_f32_16x16x32_bf16 v[152:155], a[64:67], a[112:115], v[152:155]\n"
"buffer_store_dwordx4 v[72:75], v6, s[52:55], s87 offen offset:3072 nt\n"
"ds_write_b64 v8, v[28:29] offset:32768\n"
"v_mfma_f32_16x16x32_bf16 v[152:155], a[68:71], a[116:119], v[152:155]\n"
"global_load_dwordx4 a[52:55], v0, s[34:35] offset:1024 sc1\n"
"v_lshlrev_b32 v12, 16, v18\n"
"v_mfma_f32_16x16x32_bf16 v[180:183], a[76:79], a[116:119], v[180:183]\n"
"global_load_dwordx4 v[36:39], v0, s[96:97] offset:-2048 sc1\n"
"v_and_b32 v13, s63, v18\n"
"v_mfma_f32_16x16x32_bf16 v[180:183], a[72:75], a[112:115], v[180:183]\n"
"v_lshlrev_b32 v14, 16, v19\n"
"v_and_b32 v15, s63, v19\n"
"v_mfma_f32_16x16x32_bf16 v[184:187], a[72:75], a[120:123], v[184:187]\n"
"v_mul_f32 v12, v12, v132\n"
"v_mul_f32 v13, v13, v133\n"
"v_mfma_f32_16x16x32_bf16 v[184:187], a[76:79], a[124:127], v[184:187]\n"
"v_mul_f32 v14, v14, v134\n"
"v_mul_f32 v15, v15, v135\n"
"v_mfma_f32_16x16x32_bf16 v[156:159], a[68:71], a[124:127], v[156:159]\n"
"v_cvt_pk_bf16_f32 v30, v12, v13\n"
"v_cvt_pk_bf16_f32 v31, v14, v15\n"
"v_mfma_f32_16x16x32_bf16 v[156:159], a[64:67], a[120:123], v[156:159]\n"
"s_waitcnt lgkmcnt(6)\n"
"ds_write_b64 v8, v[30:31] offset:32776\n"
"v_mfma_f32_16x16x32_bf16 v[160:163], a[64:67], a[128:131], v[160:163]\n"
"ds_read_b128 v[48:51], v2 offset:32768\n"
"v_mfma_f32_16x16x32_bf16 v[160:163], a[68:71], a[132:135], v[160:163]\n"
"global_load_dwordx4 a[56:59], v0, s[34:35] offset:2048 sc1\n"
"ds_read_b128 v[76:79], v2 offset:33792\n"
"v_mfma_f32_16x16x32_bf16 v[188:191], a[76:79], a[132:135], v[188:191]\n"
"v_accvgpr_read_b32 v16, a168\n"
"v_accvgpr_read_b32 v17, a169\n"
"v_mfma_f32_16x16x32_bf16 v[188:191], a[72:75], a[128:131], v[188:191]\n"
"v_accvgpr_read_b32 v18, a170\n"
"v_accvgpr_read_b32 v19, a171\n"
"v_mfma_f32_16x16x32_bf16 v[192:195], a[72:75], a[136:139], v[192:195]\n"
"global_load_dwordx4 v[64:67], v0, s[96:97] offset:-1024 sc1\n"
"v_lshlrev_b32 v12, 16, v16\n"
"v_mfma_f32_16x16x32_bf16 v[192:195], a[76:79], a[140:143], v[192:195]\n"
"v_and_b32 v13, s63, v16\n"
"v_lshlrev_b32 v14, 16, v17\n"
"v_mfma_f32_16x16x32_bf16 v[164:167], a[68:71], a[140:143], v[164:167]\n"
"v_and_b32 v15, s63, v17\n"
"v_mul_f32 v12, v12, v52\n"
"v_mfma_f32_16x16x32_bf16 v[164:167], a[64:67], a[136:139], v[164:167]\n"
"s_waitcnt lgkmcnt(4)\n"
"v_mul_f32 v13, v13, v53\n"
"v_mul_f32 v14, v14, v54\n"
"v_mfma_f32_16x16x32_bf16 v[168:171], a[64:67], a[144:147], v[168:171]\n"
"v_mul_f32 v15, v15, v55\n"
"v_cvt_pk_bf16_f32 v24, v12, v13\n"
"v_mfma_f32_16x16x32_bf16 v[168:171], a[68:71], a[148:151], v[168:171]\n"
"global_load_dwordx4 a[60:63], v0, s[34:35] offset:3072 sc1\n"
"v_cvt_pk_bf16_f32 v25, v14, v15\n"
"v_mfma_f32_16x16x32_bf16 v[196:199], a[76:79], a[148:151], v[196:199]\n"
"ds_write_b64 v7, v[24:25] offset:34816\n"
"v_mfma_f32_16x16x32_bf16 v[196:199], a[72:75], a[144:147], v[196:199]\n"
"v_lshlrev_b32 v12, 16, v18\n"
"v_and_b32 v13, s63, v18\n"
"v_mfma_f32_16x16x32_bf16 a[20:23], a[72:75], a[152:155], a[20:23]\n"
"v_lshlrev_b32 v14, 16, v19\n"
"v_and_b32 v15, s63, v19\n"
"v_mfma_f32_16x16x32_bf16 a[20:23], a[76:79], a[156:159], a[20:23]\n"
"v_mul_f32 v12, v12, v80\n"
"v_mfma_f32_16x16x32_bf16 a[16:19], a[68:71], a[156:159], a[16:19]\n"
"global_load_dwordx4 v[104:107], v0, s[96:97] offset:0 sc1\n"
"v_mul_f32 v13, v13, v81\n"
"v_mfma_f32_16x16x32_bf16 a[16:19], a[64:67], a[152:155], a[16:19]\n"
"s_waitcnt vmcnt(16)\n"
"v_mul_f32 v14, v14, v82\n"
"v_mul_f32 v15, v15, v83\n"
"v_mfma_f32_16x16x32_bf16 v[200:203], a[80:83], a[96:99], v[200:203]\n"
"s_add_u32 s34, s36, 1564672\n"
"v_cvt_pk_bf16_f32 v26, v12, v13\n"
"v_mfma_f32_16x16x32_bf16 v[200:203], a[84:87], a[100:103], v[200:203]\n"
"global_load_dwordx4 a[64:67], v0, s[34:35] offset:0 sc1\n"
"v_cvt_pk_bf16_f32 v27, v14, v15\n"
"v_mfma_f32_16x16x32_bf16 v[228:231], a[92:95], a[100:103], v[228:231]\n"
"ds_read_b128 a[100:103], v2 offset:17408\n"
"v_mfma_f32_16x16x32_bf16 v[228:231], a[88:91], a[96:99], v[228:231]\n"
"ds_read_b128 a[96:99], v2 offset:16384\n"
"v_mfma_f32_16x16x32_bf16 v[232:235], a[88:91], a[104:107], v[232:235]\n"
"ds_write_b64 v7, v[26:27] offset:34824\n"
"v_mfma_f32_16x16x32_bf16 v[232:235], a[92:95], a[108:111], v[232:235]\n"
"v_accvgpr_read_b32 v16, a172\n"
"v_accvgpr_read_b32 v17, a173\n"
"v_mfma_f32_16x16x32_bf16 v[204:207], a[84:87], a[108:111], v[204:207]\n"
"s_waitcnt lgkmcnt(5)\n"
"buffer_store_dwordx4 v[48:51], v6, s[52:55], s88 offen offset:0 nt\n"
"ds_read_b128 a[108:111], v2 offset:19456\n"
"v_accvgpr_read_b32 v18, a174\n"
"v_accvgpr_read_b32 v19, a175\n"
"v_mfma_f32_16x16x32_bf16 v[204:207], a[80:83], a[104:107], v[204:207]\n"
"ds_read_b128 a[104:107], v2 offset:18432\n"
"v_lshlrev_b32 v12, 16, v16\n"
"v_mfma_f32_16x16x32_bf16 v[208:211], a[80:83], a[112:115], v[208:211]\n"
"global_load_dwordx4 v[132:135], v0, s[96:97] offset:1024 sc1\n"
"v_and_b32 v13, s63, v16\n"
"v_mfma_f32_16x16x32_bf16 v[208:211], a[84:87], a[116:119], v[208:211]\n"
"L_st10887:\n"
"s_cbranch_execz L_retC\n"
"global_load_dwordx4 a[68:71], v0, s[34:35] offset:1024 sc1\n"
"v_lshlrev_b32 v14, 16, v17\n"
"v_mfma_f32_16x16x32_bf16 v[236:239], a[92:95], a[116:119], v[236:239]\n"
"ds_read_b128 a[116:119], v2 offset:21504\n"
"v_and_b32 v15, s63, v17\n"
"v_mfma_f32_16x16x32_bf16 v[236:239], a[88:91], a[112:115], v[236:239]\n"
"ds_read_b128 a[112:115], v2 offset:20480\n"
"v_mul_f32 v12, v12, v108\n"
"v_mfma_f32_16x16x32_bf16 v[240:243], a[88:91], a[120:123], v[240:243]\n"
"v_mul_f32 v13, v13, v109\n"
"v_mul_f32 v14, v14, v110\n"
"v_mfma_f32_16x16x32_bf16 v[240:243], a[92:95], a[124:127], v[240:243]\n"
"v_mul_f32 v15, v15, v111\n"
"v_cvt_pk_bf16_f32 v28, v12, v13\n"
"v_mfma_f32_16x16x32_bf16 v[212:215], a[84:87], a[124:127], v[212:215]\n"
"ds_read_b128 a[124:127], v2 offset:23552\n"
"v_cvt_pk_bf16_f32 v29, v14, v15\n"
"v_mfma_f32_16x16x32_bf16 v[212:215], a[80:83], a[120:123], v[212:215]\n"
"ds_read_b128 a[120:123], v2 offset:22528\n"
"v_mfma_f32_16x16x32_bf16 v[216:219], a[80:83], a[128:131], v[216:219]\n"
"ds_write_b64 v8, v[28:29] offset:34816\n"
"v_mfma_f32_16x16x32_bf16 v[216:219], a[84:87], a[132:135], v[216:219]\n"
"global_load_dwordx4 a[72:75], v0, s[34:35] offset:2048 sc1\n"
"v_lshlrev_b32 v12, 16, v18\n"
"v_mfma_f32_16x16x32_bf16 v[244:247], a[92:95], a[132:135], v[244:247]\n"
"s_waitcnt lgkmcnt(11)\n"
"buffer_store_dwordx4 v[76:79], v6, s[52:55], s89 offen offset:1024 nt\n"
"ds_read_b128 a[132:135], v2 offset:25600\n"
"v_and_b32 v13, s63, v18\n"
"v_lshlrev_b32 v14, 16, v19\n"
"v_mfma_f32_16x16x32_bf16 v[244:247], a[88:91], a[128:131], v[244:247]\n"
"ds_read_b128 a[128:131], v2 offset:24576\n"
"v_and_b32 v15, s63, v19\n"
"v_mfma_f32_16x16x32_bf16 v[248:251], a[88:91], a[136:139], v[248:251]\n"
"v_mul_f32 v12, v12, v136\n"
"v_mul_f32 v13, v13, v137\n"
"v_mfma_f32_16x16x32_bf16 v[248:251], a[92:95], a[140:143], v[248:251]\n"
"v_mul_f32 v14, v14, v138\n"
"v_mul_f32 v15, v15, v139\n"
"v_mfma_f32_16x16x32_bf16 v[220:223], a[84:87], a[140:143], v[220:223]\n"
"ds_read_b128 a[140:143], v2 offset:27648\n"
"v_cvt_pk_bf16_f32 v30, v12, v13\n"
"v_mfma_f32_16x16x32_bf16 v[220:223], a[80:83], a[136:139], v[220:223]\n"
"ds_read_b128 a[136:139], v2 offset:26624\n"
"v_cvt_pk_bf16_f32 v31, v14, v15\n"
"v_mfma_f32_16x16x32_bf16 v[224:227], a[80:83], a[144:147], v[224:227]\n"
"ds_write_b64 v8, v[30:31] offset:34824\n"
"v_mfma_f32_16x16x32_bf16 v[224:227], a[84:87], a[148:151], v[224:227]\n"
"global_load_dwordx4 a[76:79], v0, s[34:35] offset:3072 sc1\n"
"ds_read_b128 v[52:55], v2 offset:34816\n"
"v_mfma_f32_16x16x32_bf16 v[252:255], a[92:95], a[148:151], v[252:255]\n"
"ds_read_b128 a[148:151], v2 offset:29696\n"
"v_mfma_f32_16x16x32_bf16 v[252:255], a[88:91], a[144:147], v[252:255]\n"
"ds_read_b128 a[144:147], v2 offset:28672\n"
"v_mfma_f32_16x16x32_bf16 a[28:31], a[88:91], a[152:155], a[28:31]\n"
"ds_read_b128 v[80:83], v2 offset:35840\n"
"v_accvgpr_read_b32 v16, a176\n"
"v_mfma_f32_16x16x32_bf16 a[28:31], a[92:95], a[156:159], a[28:31]\n"
"v_accvgpr_read_b32 v17, a177\n"
"v_accvgpr_read_b32 v18, a178\n"
"v_mfma_f32_16x16x32_bf16 a[24:27], a[84:87], a[156:159], a[24:27]\n"
"ds_read_b128 a[156:159], v2 offset:31744\n"
"v_accvgpr_read_b32 v19, a179\n"
"v_mfma_f32_16x16x32_bf16 a[24:27], a[80:83], a[152:155], a[24:27]\n"
"s_waitcnt vmcnt(17) lgkmcnt(15)\n"
"ds_read_b128 a[152:155], v2 offset:30720\n"
"v_lshlrev_b32 v12, 16, v16\n"
"v_mfma_f32_16x16x32_bf16 v[144:147], a[32:35], a[96:99], v[144:147]\n"
"s_add_u32 s34, s36, 1568768\n"
"v_and_b32 v13, s63, v16\n"
"v_lshlrev_b32 v14, 16, v17\n"
"v_mfma_f32_16x16x32_bf16 v[144:147], a[36:39], a[100:103], v[144:147]\n"
"global_load_dwordx4 a[80:83], v0, s[34:35] offset:0 sc1\n"
"v_and_b32 v15, s63, v17\n"
"v_mfma_f32_16x16x32_bf16 v[172:175], a[44:47], a[100:103], v[172:175]\n"
"v_mul_f32 v12, v12, v56\n"
"v_mul_f32 v13, v13, v57\n"
"v_mfma_f32_16x16x32_bf16 v[172:175], a[40:43], a[96:99], v[172:175]\n"
"v_mul_f32 v14, v14, v58\n"
"v_mul_f32 v15, v15, v59\n"
"v_mfma_f32_16x16x32_bf16 v[176:179], a[40:43], a[104:107], v[176:179]\n"
"global_load_dwordx4 a[84:87], v0, s[34:35] offset:1024 sc1\n"
"v_cvt_pk_bf16_f32 v24, v12, v13\n"
"v_mfma_f32_16x16x32_bf16 v[176:179], a[44:47], a[108:111], v[176:179]\n"
"v_cvt_pk_bf16_f32 v25, v14, v15\n"
"v_mfma_f32_16x16x32_bf16 v[148:151], a[36:39], a[108:111], v[148:151]\n"
"ds_write_b64 v7, v[24:25] offset:36864\n"
"v_mfma_f32_16x16x32_bf16 v[148:151], a[32:35], a[104:107], v[148:151]\n"
"s_waitcnt lgkmcnt(13)\n"
"global_load_dwordx4 a[88:91], v0, s[34:35] offset:2048 sc1\n"
"v_lshlrev_b32 v12, 16, v18\n"
"v_mfma_f32_16x16x32_bf16 v[152:155], a[32:35], a[112:115], v[152:155]\n"
"global_load_dwordx4 v[108:111], v0, s[96:97] offset:2048 sc1\n"
"v_and_b32 v13, s63, v18\n"
"v_mfma_f32_16x16x32_bf16 v[152:155], a[36:39], a[116:119], v[152:155]\n"
"v_lshlrev_b32 v14, 16, v19\n"
"v_and_b32 v15, s63, v19\n"
"v_mfma_f32_16x16x32_bf16 v[180:183], a[44:47], a[116:119], v[180:183]\n"
"global_load_dwordx4 a[92:95], v0, s[34:35] offset:3072 sc1\n"
"v_mul_f32 v12, v12, v84\n"
"v_mfma_f32_16x16x32_bf16 v[180:183], a[40:43], a[112:115], v[180:183]\n"
"v_mul_f32 v13, v13, v85\n"
"v_mul_f32 v14, v14, v86\n"
"v_mfma_f32_16x16x32_bf16 v[184:187], a[40:43], a[120:123], v[184:187]\n"
"v_mul_f32 v15, v15, v87\n"
"v_cvt_pk_bf16_f32 v26, v12, v13\n"
"v_mfma_f32_16x16x32_bf16 v[184:187], a[44:47], a[124:127], v[184:187]\n"
"v_cvt_pk_bf16_f32 v27, v14, v15\n"
"v_mfma_f32_16x16x32_bf16 v[156:159], a[36:39], a[124:127], v[156:159]\n"
"ds_write_b64 v7, v[26:27] offset:36872\n"
"v_mfma_f32_16x16x32_bf16 v[156:159], a[32:35], a[120:123], v[156:159]\n"
"s_waitcnt lgkmcnt(9)\n"
"v_accvgpr_read_b32 v16, a180\n"
"v_accvgpr_read_b32 v17, a181\n"
"v_mfma_f32_16x16x32_bf16 v[160:163], a[32:35], a[128:131], v[160:163]\n"
"s_waitcnt lgkmcnt(7)\n"
"buffer_store_dwordx4 v[52:55], v6, s[52:55], s90 offen offset:2048 nt\n"
"v_accvgpr_read_b32 v18, a182\n"
"v_accvgpr_read_b32 v19, a183\n"
"v_lshlrev_b32 v12, 16, v16\n"
"v_mfma_f32_16x16x32_bf16 v[160:163], a[36:39], a[132:135], v[160:163]\n"
"global_load_dwordx4 v[136:139], v0, s[96:97] offset:3072 sc1\n"
"v_and_b32 v13, s63, v16\n"
"v_mfma_f32_16x16x32_bf16 v[188:191], a[44:47], a[132:135], v[188:191]\n"
"v_lshlrev_b32 v14, 16, v17\n"
"v_and_b32 v15, s63, v17\n"
"v_mfma_f32_16x16x32_bf16 v[188:191], a[40:43], a[128:131], v[188:191]\n"
"v_mul_f32 v12, v12, v112\n"
"v_mul_f32 v13, v13, v113\n"
"v_mfma_f32_16x16x32_bf16 v[192:195], a[40:43], a[136:139], v[192:195]\n"
"v_mul_f32 v14, v14, v114\n"
"v_mul_f32 v15, v15, v115\n"
"v_mfma_f32_16x16x32_bf16 v[192:195], a[44:47], a[140:143], v[192:195]\n"
"v_cvt_pk_bf16_f32 v28, v12, v13\n"
"v_cvt_pk_bf16_f32 v29, v14, v15\n"
"v_mfma_f32_16x16x32_bf16 v[164:167], a[36:39], a[140:143], v[164:167]\n"
"ds_write_b64 v8, v[28:29] offset:36864\n"
"v_mfma_f32_16x16x32_bf16 v[164:167], a[32:35], a[136:139], v[164:167]\n"
"s_waitcnt lgkmcnt(3)\n"
"v_lshlrev_b32 v12, 16, v18\n"
"v_and_b32 v13, s63, v18\n"
"v_mfma_f32_16x16x32_bf16 v[168:171], a[32:35], a[144:147], v[168:171]\n"
"v_lshlrev_b32 v14, 16, v19\n"
"v_and_b32 v15, s63, v19\n"
"v_mfma_f32_16x16x32_bf16 v[168:171], a[36:39], a[148:151], v[168:171]\n"
"v_mul_f32 v12, v12, v140\n"
"v_mul_f32 v13, v13, v141\n"
"v_mfma_f32_16x16x32_bf16 v[196:199], a[44:47], a[148:151], v[196:199]\n"
"v_mul_f32 v14, v14, v142\n"
"v_mul_f32 v15, v15, v143\n"
"v_mfma_f32_16x16x32_bf16 v[196:199], a[40:43], a[144:147], v[196:199]\n"
"v_cvt_pk_bf16_f32 v30, v12, v13\n"
"v_cvt_pk_bf16_f32 v31, v14, v15\n"
"v_mfma_f32_16x16x32_bf16 a[20:23], a[40:43], a[152:155], a[20:23]\n"
"buffer_store_dwordx4 v[80:83], v6, s[52:55], s91 offen offset:3072 nt\n"
"ds_write_b64 v8, v[30:31] offset:36872\n"
"v_mfma_f32_16x16x32_bf16 a[20:23], a[44:47], a[156:159], a[20:23]\n"
"ds_read_b128 v[56:59], v2 offset:36864\n"
"v_mfma_f32_16x16x32_bf16 a[16:19], a[36:39], a[156:159], a[16:19]\n"
"ds_read_b128 v[84:87], v2 offset:37888\n"
"v_mfma_f32_16x16x32_bf16 a[16:19], a[32:35], a[152:155], a[16:19]\n"
"s_waitcnt vmcnt(16)\n"
"v_accvgpr_read_b32 v16, a184\n"
"v_mfma_f32_16x16x32_bf16 v[200:203], a[48:51], a[96:99], v[200:203]\n"
"v_accvgpr_read_b32 v17, a185\n"
"v_mfma_f32_16x16x32_bf16 v[200:203], a[52:55], a[100:103], v[200:203]\n"
"v_accvgpr_read_b32 v18, a186\n"
"v_mfma_f32_16x16x32_bf16 v[228:231], a[60:63], a[100:103], v[228:231]\n"
"ds_read_b128 a[100:103], v3 offset:17408\n"
"v_mfma_f32_16x16x32_bf16 v[228:231], a[56:59], a[96:99], v[228:231]\n"
"ds_read_b128 a[96:99], v3 offset:16384\n"
"v_accvgpr_read_b32 v19, a187\n"
"v_mfma_f32_16x16x32_bf16 v[232:235], a[56:59], a[104:107], v[232:235]\n"
"v_lshlrev_b32 v12, 16, v16\n"
"v_and_b32 v13, s63, v16\n"
"v_mfma_f32_16x16x32_bf16 v[232:235], a[60:63], a[108:111], v[232:235]\n"
"v_lshlrev_b32 v14, 16, v17\n"
"v_and_b32 v15, s63, v17\n"
"v_mfma_f32_16x16x32_bf16 v[204:207], a[52:55], a[108:111], v[204:207]\n"
"ds_read_b128 a[108:111], v4 offset:17408\n"
"v_accvgpr_read_b32 v10, a0\n"
"v_mfma_f32_16x16x32_bf16 v[204:207], a[48:51], a[104:107], v[204:207]\n"
"ds_read_b128 a[104:107], v4 offset:16384\n"
"v_accvgpr_read_b32 v11, a1\n"
"v_mfma_f32_16x16x32_bf16 v[208:211], a[48:51], a[112:115], v[208:211]\n"
"v_mul_f32 v12, v12, v10\n"
"v_mul_f32 v13, v13, v11\n"
"v_mfma_f32_16x16x32_bf16 v[208:211], a[52:55], a[116:119], v[208:211]\n"
"v_accvgpr_read_b32 v10, a2\n"
"v_accvgpr_read_b32 v11, a3\n"
"v_mfma_f32_16x16x32_bf16 v[236:239], a[60:63], a[116:119], v[236:239]\n"
"ds_read_b128 a[116:119], v5 offset:17408\n"
"v_mul_f32 v14, v14, v10\n"
"v_mfma_f32_16x16x32_bf16 v[236:239], a[56:59], a[112:115], v[236:239]\n"
"ds_read_b128 a[112:115], v5 offset:16384\n"
"v_mul_f32 v15, v15, v11\n"
"v_mfma_f32_16x16x32_bf16 v[240:243], a[56:59], a[120:123], v[240:243]\n"
"v_cvt_pk_bf16_f32 v24, v12, v13\n"
"v_cvt_pk_bf16_f32 v25, v14, v15\n"
"v_mfma_f32_16x16x32_bf16 v[240:243], a[60:63], a[124:127], v[240:243]\n"
"ds_write_b64 v7, v[24:25] offset:38912\n"
"v_mfma_f32_16x16x32_bf16 v[212:215], a[52:55], a[124:127], v[212:215]\n"
"ds_read_b128 a[124:127], v3 offset:19456\n"
"v_lshlrev_b32 v12, 16, v18\n"
"v_mfma_f32_16x16x32_bf16 v[212:215], a[48:51], a[120:123], v[212:215]\n"
"ds_read_b128 a[120:123], v3 offset:18432\n"
"v_and_b32 v13, s63, v18\n"
"v_mfma_f32_16x16x32_bf16 v[216:219], a[48:51], a[128:131], v[216:219]\n"
"v_lshlrev_b32 v14, 16, v19\n"
"v_and_b32 v15, s63, v19\n"
"v_mfma_f32_16x16x32_bf16 v[216:219], a[52:55], a[132:135], v[216:219]\n"
"v_accvgpr_read_b32 v10, a4\n"
"v_accvgpr_read_b32 v11, a5\n"
"v_mfma_f32_16x16x32_bf16 v[244:247], a[60:63], a[132:135], v[244:247]\n"
"ds_read_b128 a[132:135], v4 offset:19456\n"
"v_mul_f32 v12, v12, v10\n"
"v_mfma_f32_16x16x32_bf16 v[244:247], a[56:59], a[128:131], v[244:247]\n"
"ds_read_b128 a[128:131], v4 offset:18432\n"
"v_mul_f32 v13, v13, v11\n"
"v_mfma_f32_16x16x32_bf16 v[248:251], a[56:59], a[136:139], v[248:251]\n"
"v_accvgpr_read_b32 v10, a6\n"
"v_accvgpr_read_b32 v11, a7\n"
"v_mfma_f32_16x16x32_bf16 v[248:251], a[60:63], a[140:143], v[248:251]\n"
"v_mul_f32 v14, v14, v10\n"
"v_mul_f32 v15, v15, v11\n"
"v_mfma_f32_16x16x32_bf16 v[220:223], a[52:55], a[140:143], v[220:223]\n"
"ds_read_b128 a[140:143], v5 offset:19456\n"
"v_cvt_pk_bf16_f32 v26, v12, v13\n"
"v_mfma_f32_16x16x32_bf16 v[220:223], a[48:51], a[136:139], v[220:223]\n"
"ds_read_b128 a[136:139], v5 offset:18432\n"
"v_cvt_pk_bf16_f32 v27, v14, v15\n"
"v_mfma_f32_16x16x32_bf16 v[224:227], a[48:51], a[144:147], v[224:227]\n"
"ds_write_b64 v7, v[26:27] offset:38920\n"
"v_mfma_f32_16x16x32_bf16 v[224:227], a[52:55], a[148:151], v[224:227]\n"
"v_accvgpr_read_b32 v16, a188\n"
"v_mfma_f32_16x16x32_bf16 v[252:255], a[60:63], a[148:151], v[252:255]\n"
"s_waitcnt lgkmcnt(15)\n"
"buffer_store_dwordx4 v[56:59], v6, s[52:55], s92 offen offset:0 nt\n"
"ds_read_b128 a[148:151], v3 offset:21504\n"
"v_accvgpr_read_b32 v17, a189\n"
"v_accvgpr_read_b32 v18, a190\n"
"v_mfma_f32_16x16x32_bf16 v[252:255], a[56:59], a[144:147], v[252:255]\n"
"ds_read_b128 a[144:147], v3 offset:20480\n"
"v_accvgpr_read_b32 v19, a191\n"
"v_mfma_f32_16x16x32_bf16 a[28:31], a[56:59], a[152:155], a[28:31]\n"
"v_lshlrev_b32 v12, 16, v16\n"
"v_and_b32 v13, s63, v16\n"
"v_mfma_f32_16x16x32_bf16 a[28:31], a[60:63], a[156:159], a[28:31]\n"
"v_lshlrev_b32 v14, 16, v17\n"
"v_and_b32 v15, s63, v17\n"
"v_mfma_f32_16x16x32_bf16 a[24:27], a[52:55], a[156:159], a[24:27]\n"
"ds_read_b128 a[156:159], v4 offset:21504\n"
"v_accvgpr_read_b32 v10, a8\n"
"v_mfma_f32_16x16x32_bf16 a[24:27], a[48:51], a[152:155], a[24:27]\n"
"s_waitcnt lgkmcnt(15)\n"
"ds_read_b128 a[152:155], v4 offset:20480\n"
"v_accvgpr_read_b32 v11, a9\n"
"v_mfma_f32_16x16x32_bf16 v[144:147], v[88:91], a[96:99], v[144:147]\n"
"v_mul_f32 v12, v12, v10\n"
"v_mul_f32 v13, v13, v11\n"
"v_mfma_f32_16x16x32_bf16 v[144:147], v[116:119], a[100:103], v[144:147]\n"
"v_accvgpr_read_b32 v10, a10\n"
"v_accvgpr_read_b32 v11, a11\n"
"v_mfma_f32_16x16x32_bf16 v[172:175], v[120:123], a[100:103], v[172:175]\n"
"v_mul_f32 v14, v14, v10\n"
"v_mul_f32 v15, v15, v11\n"
"v_mfma_f32_16x16x32_bf16 v[172:175], v[92:95], a[96:99], v[172:175]\n"
"v_cvt_pk_bf16_f32 v28, v12, v13\n"
"v_cvt_pk_bf16_f32 v29, v14, v15\n"
"v_mfma_f32_16x16x32_bf16 v[200:203], v[32:35], a[96:99], v[200:203]\n"
"buffer_store_dwordx4 v[84:87], v6, s[52:55], s93 offen offset:1024 nt\n"
"ds_write_b64 v8, v[28:29] offset:38912\n"
"v_mfma_f32_16x16x32_bf16 v[200:203], v[60:63], a[100:103], v[200:203]\n"
"v_lshlrev_b32 v12, 16, v18\n"
"v_mfma_f32_16x16x32_bf16 v[228:231], v[124:127], a[100:103], v[228:231]\n"
"ds_read_b128 a[100:103], v5 offset:21504\n"
"v_mfma_f32_16x16x32_bf16 v[228:231], v[96:99], a[96:99], v[228:231]\n"
"s_waitcnt vmcnt(3) lgkmcnt(15)\n"
"ds_read_b128 a[96:99], v5 offset:20480\n"
"v_mfma_f32_16x16x32_bf16 v[228:231], v[108:111], a[104:107], v[228:231]\n"
"v_and_b32 v13, s63, v18\n"
"v_mfma_f32_16x16x32_bf16 v[228:231], v[136:139], a[108:111], v[228:231]\n"
"v_lshlrev_b32 v14, 16, v19\n"
"v_mfma_f32_16x16x32_bf16 v[200:203], v[132:135], a[108:111], v[200:203]\n"
"v_and_b32 v15, s63, v19\n"
"v_mfma_f32_16x16x32_bf16 v[200:203], v[104:107], a[104:107], v[200:203]\n"
"v_accvgpr_read_b32 v10, a12\n"
"v_mfma_f32_16x16x32_bf16 v[172:175], v[36:39], a[104:107], v[172:175]\n"
"v_accvgpr_read_b32 v11, a13\n"
"v_mfma_f32_16x16x32_bf16 v[172:175], v[64:67], a[108:111], v[172:175]\n"
"v_mul_f32 v12, v12, v10\n"
"v_mfma_f32_16x16x32_bf16 v[144:147], v[128:131], a[108:111], v[144:147]\n"
"ds_read_b128 a[108:111], v3 offset:23552\n"
"v_mfma_f32_16x16x32_bf16 v[144:147], v[100:103], a[104:107], v[144:147]\n"
"s_waitcnt lgkmcnt(15)\n"
"ds_read_b128 a[104:107], v3 offset:22528\n"
"v_mfma_f32_16x16x32_bf16 v[144:147], a[64:67], a[112:115], v[144:147]\n"
"v_mul_f32 v13, v13, v11\n"
"v_mfma_f32_16x16x32_bf16 v[144:147], a[68:71], a[116:119], v[144:147]\n"
"v_accvgpr_read_b32 v10, a14\n"
"v_accvgpr_read_b32 v11, a15\n"
"v_mfma_f32_16x16x32_bf16 v[172:175], a[76:79], a[116:119], v[172:175]\n"
"v_mul_f32 v14, v14, v10\n"
"v_mul_f32 v15, v15, v11\n"
"v_mfma_f32_16x16x32_bf16 v[172:175], a[72:75], a[112:115], v[172:175]\n"
"v_cvt_pk_bf16_f32 v30, v12, v13\n"
"v_cvt_pk_bf16_f32 v31, v14, v15\n"
"v_mfma_f32_16x16x32_bf16 v[200:203], a[80:83], a[112:115], v[200:203]\n"
"ds_write_b64 v8, v[30:31] offset:38920\n"
"v_mfma_f32_16x16x32_bf16 v[200:203], a[84:87], a[116:119], v[200:203]\n"
"ds_read_b128 v[112:115], v2 offset:38912\n"
"v_mfma_f32_16x16x32_bf16 v[228:231], a[92:95], a[116:119], v[228:231]\n"
"ds_read_b128 a[116:119], v4 offset:23552\n"
"v_mfma_f32_16x16x32_bf16 v[228:231], a[88:91], a[112:115], v[228:231]\n"
"s_waitcnt lgkmcnt(15)\n"
"ds_read_b128 a[112:115], v4 offset:22528\n"
"v_mfma_f32_16x16x32_bf16 v[232:235], v[96:99], a[120:123], v[232:235]\n"
"ds_read_b128 v[140:143], v2 offset:39936\n"
"v_mfma_f32_16x16x32_bf16 v[232:235], v[124:127], a[124:127], v[232:235]\n"
"v_accvgpr_read_b32 v16, a192\n"
"v_accvgpr_read_b32 v17, a193\n"
"v_mfma_f32_16x16x32_bf16 v[204:207], v[60:63], a[124:127], v[204:207]\n"
"v_accvgpr_read_b32 v18, a194\n"
"v_accvgpr_read_b32 v19, a195\n"
"v_mfma_f32_16x16x32_bf16 v[204:207], v[32:35], a[120:123], v[204:207]\n"
"v_lshlrev_b32 v12, 16, v16\n"
"v_and_b32 v13, s63, v16\n"
"v_mfma_f32_16x16x32_bf16 v[176:179], v[92:95], a[120:123], v[176:179]\n"
"v_lshlrev_b32 v14, 16, v17\n"
"v_and_b32 v15, s63, v17\n"
"v_mfma_f32_16x16x32_bf16 v[176:179], v[120:123], a[124:127], v[176:179]\n"
"v_mul_f32 v12, v12, v144\n"
"v_mul_f32 v13, v13, v145\n"
"v_mfma_f32_16x16x32_bf16 v[148:151], v[116:119], a[124:127], v[148:151]\n"
"ds_read_b128 a[124:127], v5 offset:23552\n"
"v_mul_f32 v14, v14, v146\n"
"v_mfma_f32_16x16x32_bf16 v[148:151], v[88:91], a[120:123], v[148:151]\n"
"ds_read_b128 a[120:123], v5 offset:22528\n"
"v_mul_f32 v15, v15, v147\n"
"v_mfma_f32_16x16x32_bf16 v[148:151], v[100:103], a[128:131], v[148:151]\n"
"v_cvt_pk_bf16_f32 v24, v12, v13\n"
"v_cvt_pk_bf16_f32 v25, v14, v15\n"
"v_mfma_f32_16x16x32_bf16 v[148:151], v[128:131], a[132:135], v[148:151]\n"
"ds_write_b64 v7, v[24:25] offset:32768\n"
"v_mfma_f32_16x16x32_bf16 v[176:179], v[64:67], a[132:135], v[176:179]\n"
"v_lshlrev_b32 v12, 16, v18\n"
"v_and_b32 v13, s63, v18\n"
"v_mfma_f32_16x16x32_bf16 v[176:179], v[36:39], a[128:131], v[176:179]\n"
"v_lshlrev_b32 v14, 16, v19\n"
"v_and_b32 v15, s63, v19\n"
"v_mfma_f32_16x16x32_bf16 v[204:207], v[104:107], a[128:131], v[204:207]\n"
"s_waitcnt lgkmcnt(6)\n"
"buffer_store_dwordx4 v[112:115], v6, s[52:55], s94 offen offset:2048 nt\n"
"v_mul_f32 v12, v12, v172\n"
"v_mul_f32 v13, v13, v173\n"
"v_mul_f32 v14, v14, v174\n"
"v_mfma_f32_16x16x32_bf16 v[204:207], v[132:135], a[132:135], v[204:207]\n"
"v_mul_f32 v15, v15, v175\n"
"v_mfma_f32_16x16x32_bf16 v[232:235], v[136:139], a[132:135], v[232:235]\n"
"ds_read_b128 a[132:135], v3 offset:25600\n"
"v_cvt_pk_bf16_f32 v26, v12, v13\n"
"v_mfma_f32_16x16x32_bf16 v[232:235], v[108:111], a[128:131], v[232:235]\n"
"ds_read_b128 a[128:131], v3 offset:24576\n"
"v_cvt_pk_bf16_f32 v27, v14, v15\n"
"v_mfma_f32_16x16x32_bf16 v[232:235], a[88:91], a[136:139], v[232:235]\n"
"ds_write_b64 v7, v[26:27] offset:32776\n"
"v_mfma_f32_16x16x32_bf16 v[232:235], a[92:95], a[140:143], v[232:235]\n"
"v_accvgpr_read_b32 v16, a196\n"
"v_accvgpr_read_b32 v17, a197\n"
"v_mfma_f32_16x16x32_bf16 v[204:207], a[84:87], a[140:143], v[204:207]\n"
"v_accvgpr_read_b32 v18, a198\n"
"v_accvgpr_read_b32 v19, a199\n"
"v_mfma_f32_16x16x32_bf16 v[204:207], a[80:83], a[136:139], v[204:207]\n"
"v_lshlrev_b32 v12, 16, v16\n"
"v_and_b32 v13, s63, v16\n"
"v_mfma_f32_16x16x32_bf16 v[176:179], a[72:75], a[136:139], v[176:179]\n"
"v_lshlrev_b32 v14, 16, v17\n"
"v_and_b32 v15, s63, v17\n"
"v_mfma_f32_16x16x32_bf16 v[176:179], a[76:79], a[140:143], v[176:179]\n"
"v_mul_f32 v12, v12, v200\n"
"v_mul_f32 v13, v13, v201\n"
"v_mfma_f32_16x16x32_bf16 v[148:151], a[68:71], a[140:143], v[148:151]\n"
"s_waitcnt lgkmcnt(6)\n"
"buffer_store_dwordx4 v[140:143], v6, s[52:55], s95 offen offset:3072 nt\n"
"ds_read_b128 a[140:143], v4 offset:25600\n"
"v_mul_f32 v14, v14, v202\n"
"v_mul_f32 v15, v15, v203\n"
"v_mfma_f32_16x16x32_bf16 v[148:151], a[64:67], a[136:139], v[148:151]\n"
"ds_read_b128 a[136:139], v4 offset:24576\n"
"v_cvt_pk_bf16_f32 v28, v12, v13\n"
"v_mfma_f32_16x16x32_bf16 v[152:155], v[88:91], a[144:147], v[152:155]\n"
"v_cvt_pk_bf16_f32 v29, v14, v15\n"
"v_mfma_f32_16x16x32_bf16 v[152:155], v[116:119], a[148:151], v[152:155]\n"
"ds_write_b64 v8, v[28:29] offset:32768\n"
"v_mfma_f32_16x16x32_bf16 v[180:183], v[120:123], a[148:151], v[180:183]\n"
"v_lshlrev_b32 v12, 16, v18\n"
"v_and_b32 v13, s63, v18\n"
"v_mfma_f32_16x16x32_bf16 v[180:183], v[92:95], a[144:147], v[180:183]\n"
"v_lshlrev_b32 v14, 16, v19\n"
"v_and_b32 v15, s63, v19\n"
"v_mfma_f32_16x16x32_bf16 v[208:211], v[32:35], a[144:147], v[208:211]\n"
"v_mul_f32 v12, v12, v228\n"
"v_mul_f32 v13, v13, v229\n"
"v_mfma_f32_16x16x32_bf16 v[208:211], v[60:63], a[148:151], v[208:211]\n"
"v_mul_f32 v14, v14, v230\n"
"v_mul_f32 v15, v15, v231\n"
"v_mfma_f32_16x16x32_bf16 v[236:239], v[124:127], a[148:151], v[236:239]\n"
"ds_read_b128 a[148:151], v5 offset:25600\n"
"v_cvt_pk_bf16_f32 v30, v12, v13\n"
"v_mfma_f32_16x16x32_bf16 v[236:239], v[96:99], a[144:147], v[236:239]\n"
"ds_read_b128 a[144:147], v5 offset:24576\n"
"v_cvt_pk_bf16_f32 v31, v14, v15\n"
"v_mfma_f32_16x16x32_bf16 v[236:239], v[108:111], a[152:155], v[236:239]\n"
"ds_write_b64 v8, v[30:31] offset:32776\n"
"v_mfma_f32_16x16x32_bf16 v[236:239], v[136:139], a[156:159], v[236:239]\n"
"ds_read_b128 v[144:147], v2 offset:32768\n"
"v_mfma_f32_16x16x32_bf16 v[208:211], v[132:135], a[156:159], v[208:211]\n"
"ds_read_b128 v[172:175], v2 offset:33792\n"
"v_accvgpr_read_b32 v16, a200\n"
"v_accvgpr_read_b32 v17, a201\n"
"v_mfma_f32_16x16x32_bf16 v[208:211], v[104:107], a[152:155], v[208:211]\n"
"v_accvgpr_read_b32 v18, a202\n"
"v_accvgpr_read_b32 v19, a203\n"
"v_mfma_f32_16x16x32_bf16 v[180:183], v[36:39], a[152:155], v[180:183]\n"
"v_lshlrev_b32 v12, 16, v16\n"
"v_and_b32 v13, s63, v16\n"
"v_mfma_f32_16x16x32_bf16 v[180:183], v[64:67], a[156:159], v[180:183]\n"
"v_lshlrev_b32 v14, 16, v17\n"
"v_and_b32 v15, s63, v17\n"
"v_mfma_f32_16x16x32_bf16 v[152:155], v[128:131], a[156:159], v[152:155]\n"
"ds_read_b128 a[156:159], v3 offset:27648\n"
"v_mul_f32 v12, v12, v148\n"
"v_mfma_f32_16x16x32_bf16 v[152:155], v[100:103], a[152:155], v[152:155]\n"
"ds_read_b128 a[152:155], v3 offset:26624\n"
"v_mul_f32 v13, v13, v149\n"
"v_mfma_f32_16x16x32_bf16 v[152:155], a[64:67], a[96:99], v[152:155]\n"
"v_mul_f32 v14, v14, v150\n"
"v_mul_f32 v15, v15, v151\n"
"v_mfma_f32_16x16x32_bf16 v[152:155], a[68:71], a[100:103], v[152:155]\n"
"v_cvt_pk_bf16_f32 v24, v12, v13\n"
"v_cvt_pk_bf16_f32 v25, v14, v15\n"
"v_mfma_f32_16x16x32_bf16 v[180:183], a[76:79], a[100:103], v[180:183]\n"
"s_waitcnt lgkmcnt(3)\n"
"buffer_store_dwordx4 v[144:147], v6, s[52:55], s80 offen offset:128 nt\n"
"ds_write_b64 v7, v[24:25] offset:34816\n"
"v_mfma_f32_16x16x32_bf16 v[180:183], a[72:75], a[96:99], v[180:183]\n"
"v_lshlrev_b32 v12, 16, v18\n"
"v_and_b32 v13, s63, v18\n"
"v_mfma_f32_16x16x32_bf16 v[208:211], a[80:83], a[96:99], v[208:211]\n"
"v_lshlrev_b32 v14, 16, v19\n"
"v_and_b32 v15, s63, v19\n"
"v_mfma_f32_16x16x32_bf16 v[208:211], a[84:87], a[100:103], v[208:211]\n"
"v_mul_f32 v12, v12, v176\n"
"v_mul_f32 v13, v13, v177\n"
"v_mfma_f32_16x16x32_bf16 v[236:239], a[92:95], a[100:103], v[236:239]\n"
"ds_read_b128 a[100:103], v4 offset:27648\n"
"v_mul_f32 v14, v14, v178\n"
"v_mfma_f32_16x16x32_bf16 v[236:239], a[88:91], a[96:99], v[236:239]\n"
"ds_read_b128 a[96:99], v4 offset:26624\n"
"v_mul_f32 v15, v15, v179\n"
"v_mfma_f32_16x16x32_bf16 v[240:243], v[96:99], a[104:107], v[240:243]\n"
"v_cvt_pk_bf16_f32 v26, v12, v13\n"
"v_cvt_pk_bf16_f32 v27, v14, v15\n"
"v_mfma_f32_16x16x32_bf16 v[240:243], v[124:127], a[108:111], v[240:243]\n"
"ds_write_b64 v7, v[26:27] offset:34824\n"
"v_mfma_f32_16x16x32_bf16 v[212:215], v[60:63], a[108:111], v[212:215]\n"
"v_accvgpr_read_b32 v16, a204\n"
"v_accvgpr_read_b32 v17, a205\n"
"v_mfma_f32_16x16x32_bf16 v[212:215], v[32:35], a[104:107], v[212:215]\n"
"v_accvgpr_read_b32 v18, a206\n"
"v_accvgpr_read_b32 v19, a207\n"
"v_mfma_f32_16x16x32_bf16 v[184:187], v[92:95], a[104:107], v[184:187]\n"
"s_waitcnt lgkmcnt(6)\n"
"buffer_store_dwordx4 v[172:175], v6, s[52:55], s81 offen offset:1152 nt\n"
"v_lshlrev_b32 v12, 16, v16\n"
"v_and_b32 v13, s63, v16\n"
"v_lshlrev_b32 v14, 16, v17\n"
"v_and_b32 v15, s63, v17\n"
"v_mfma_f32_16x16x32_bf16 v[184:187], v[120:123], a[108:111], v[184:187]\n"
"v_mul_f32 v12, v12, v204\n"
"v_mul_f32 v13, v13, v205\n"
"v_mfma_f32_16x16x32_bf16 v[156:159], v[116:119], a[108:111], v[156:159]\n"
"ds_read_b128 a[108:111], v5 offset:27648\n"
"v_mul_f32 v14, v14, v206\n"
"v_mfma_f32_16x16x32_bf16 v[156:159], v[88:91], a[104:107], v[156:159]\n"
"ds_read_b128 a[104:107], v5 offset:26624\n"
"v_mul_f32 v15, v15, v207\n"
"v_mfma_f32_16x16x32_bf16 v[156:159], v[100:103], a[112:115], v[156:159]\n"
"v_cvt_pk_bf16_f32 v28, v12, v13\n"
"v_cvt_pk_bf16_f32 v29, v14, v15\n"
"v_mfma_f32_16x16x32_bf16 v[156:159], v[128:131], a[116:119], v[156:159]\n"
"ds_write_b64 v8, v[28:29] offset:34816\n"
"v_mfma_f32_16x16x32_bf16 v[184:187], v[64:67], a[116:119], v[184:187]\n"
"v_lshlrev_b32 v12, 16, v18\n"
"v_and_b32 v13, s63, v18\n"
"v_lshlrev_b32 v14, 16, v19\n"
"v_mfma_f32_16x16x32_bf16 v[184:187], v[36:39], a[112:115], v[184:187]\n"
"v_and_b32 v15, s63, v19\n"
"v_mul_f32 v12, v12, v232\n"
"v_mfma_f32_16x16x32_bf16 v[212:215], v[104:107], a[112:115], v[212:215]\n"
"v_mul_f32 v13, v13, v233\n"
"v_mul_f32 v14, v14, v234\n"
"v_mfma_f32_16x16x32_bf16 v[212:215], v[132:135], a[116:119], v[212:215]\n"
"v_mul_f32 v15, v15, v235\n"
"v_cvt_pk_bf16_f32 v30, v12, v13\n"
"v_mfma_f32_16x16x32_bf16 v[240:243], v[136:139], a[116:119], v[240:243]\n"
"ds_read_b128 a[116:119], v3 offset:29696\n"
"v_cvt_pk_bf16_f32 v31, v14, v15\n"
"v_mfma_f32_16x16x32_bf16 v[240:243], v[108:111], a[112:115], v[240:243]\n"
"ds_read_b128 a[112:115], v3 offset:28672\n"
"ds_write_b64 v8, v[30:31] offset:34824\n"
"v_mfma_f32_16x16x32_bf16 v[240:243], a[88:91], a[120:123], v[240:243]\n"
"ds_read_b128 v[148:151], v2 offset:34816\n"
"v_mfma_f32_16x16x32_bf16 v[240:243], a[92:95], a[124:127], v[240:243]\n"
"ds_read_b128 v[176:179], v2 offset:35840\n"
"v_accvgpr_read_b32 v16, a208\n"
"v_accvgpr_read_b32 v17, a209\n"
"v_mfma_f32_16x16x32_bf16 v[212:215], a[84:87], a[124:127], v[212:215]\n"
"v_accvgpr_read_b32 v18, a210\n"
"v_accvgpr_read_b32 v19, a211\n"
"v_mfma_f32_16x16x32_bf16 v[212:215], a[80:83], a[120:123], v[212:215]\n"
"v_lshlrev_b32 v12, 16, v16\n"
"v_and_b32 v13, s63, v16\n"
"v_mfma_f32_16x16x32_bf16 v[184:187], a[72:75], a[120:123], v[184:187]\n"
"v_lshlrev_b32 v14, 16, v17\n"
"v_and_b32 v15, s63, v17\n"
"v_mfma_f32_16x16x32_bf16 v[184:187], a[76:79], a[124:127], v[184:187]\n"
"v_mul_f32 v12, v12, v152\n"
"v_mul_f32 v13, v13, v153\n"
"v_mfma_f32_16x16x32_bf16 v[156:159], a[68:71], a[124:127], v[156:159]\n"
"ds_read_b128 a[124:127], v4 offset:29696\n"
"v_mul_f32 v14, v14, v154\n"
"v_mfma_f32_16x16x32_bf16 v[156:159], a[64:67], a[120:123], v[156:159]\n"
"ds_read_b128 a[120:123], v4 offset:28672\n"
"v_mul_f32 v15, v15, v155\n"
"v_mfma_f32_16x16x32_bf16 v[160:163], v[88:91], a[128:131], v[160:163]\n"
"v_cvt_pk_bf16_f32 v24, v12, v13\n"
"v_cvt_pk_bf16_f32 v25, v14, v15\n"
"v_mfma_f32_16x16x32_bf16 v[160:163], v[116:119], a[132:135], v[160:163]\n"
"s_waitcnt lgkmcnt(3)\n"
"buffer_store_dwordx4 v[148:151], v6, s[52:55], s82 offen offset:2176 nt\n"
"ds_write_b64 v7, v[24:25] offset:36864\n"
"v_mfma_f32_16x16x32_bf16 v[188:191], v[120:123], a[132:135], v[188:191]\n"
"v_lshlrev_b32 v12, 16, v18\n"
"v_and_b32 v13, s63, v18\n"
"v_mfma_f32_16x16x32_bf16 v[188:191], v[92:95], a[128:131], v[188:191]\n"
"v_lshlrev_b32 v14, 16, v19\n"
"v_and_b32 v15, s63, v19\n"
"v_mfma_f32_16x16x32_bf16 v[216:219], v[32:35], a[128:131], v[216:219]\n"
"v_mul_f32 v12, v12, v180\n"
"v_mul_f32 v13, v13, v181\n"
"v_mfma_f32_16x16x32_bf16 v[216:219], v[60:63], a[132:135], v[216:219]\n"
"v_mul_f32 v14, v14, v182\n"
"v_mul_f32 v15, v15, v183\n"
"v_mfma_f32_16x16x32_bf16 v[244:247], v[124:127], a[132:135], v[244:247]\n"
"ds_read_b128 a[132:135], v5 offset:29696\n"
"v_cvt_pk_bf16_f32 v26, v12, v13\n"
"v_mfma_f32_16x16x32_bf16 v[244:247], v[96:99], a[128:131], v[244:247]\n"
"ds_read_b128 a[128:131], v5 offset:28672\n"
"v_cvt_pk_bf16_f32 v27, v14, v15\n"
"v_mfma_f32_16x16x32_bf16 v[244:247], v[108:111], a[136:139], v[244:247]\n"
"ds_write_b64 v7, v[26:27] offset:36872\n"
"v_mfma_f32_16x16x32_bf16 v[244:247], v[136:139], a[140:143], v[244:247]\n"
"v_accvgpr_read_b32 v16, a212\n"
"v_accvgpr_read_b32 v17, a213\n"
"v_mfma_f32_16x16x32_bf16 v[216:219], v[132:135], a[140:143], v[216:219]\n"
"v_accvgpr_read_b32 v18, a214\n"
"v_accvgpr_read_b32 v19, a215\n"
"v_mfma_f32_16x16x32_bf16 v[216:219], v[104:107], a[136:139], v[216:219]\n"
"s_waitcnt lgkmcnt(6)\n"
"buffer_store_dwordx4 v[176:179], v6, s[52:55], s83 offen offset:3200 nt\n"
"v_lshlrev_b32 v12, 16, v16\n"
"v_and_b32 v13, s63, v16\n"
"v_lshlrev_b32 v14, 16, v17\n"
"v_and_b32 v15, s63, v17\n"
"v_mfma_f32_16x16x32_bf16 v[188:191], v[36:39], a[136:139], v[188:191]\n"
"v_mul_f32 v12, v12, v208\n"
"v_mul_f32 v13, v13, v209\n"
"v_mfma_f32_16x16x32_bf16 v[188:191], v[64:67], a[140:143], v[188:191]\n"
"v_mul_f32 v14, v14, v210\n"
"v_mul_f32 v15, v15, v211\n"
"v_mfma_f32_16x16x32_bf16 v[160:163], v[128:131], a[140:143], v[160:163]\n"
"ds_read_b128 a[140:143], v3 offset:31744\n"
"v_cvt_pk_bf16_f32 v28, v12, v13\n"
"v_mfma_f32_16x16x32_bf16 v[160:163], v[100:103], a[136:139], v[160:163]\n"
"ds_read_b128 a[136:139], v3 offset:30720\n"
"v_cvt_pk_bf16_f32 v29, v14, v15\n"
"v_mfma_f32_16x16x32_bf16 v[160:163], a[64:67], a[144:147], v[160:163]\n"
"ds_write_b64 v8, v[28:29] offset:36864\n"
"v_mfma_f32_16x16x32_bf16 v[160:163], a[68:71], a[148:151], v[160:163]\n"
"v_lshlrev_b32 v12, 16, v18\n"
"v_and_b32 v13, s63, v18\n"
"v_lshlrev_b32 v14, 16, v19\n"
"v_mfma_f32_16x16x32_bf16 v[188:191], a[76:79], a[148:151], v[188:191]\n"
"v_and_b32 v15, s63, v19\n"
"v_mul_f32 v12, v12, v236\n"
"v_mul_f32 v13, v13, v237\n"
"v_mfma_f32_16x16x32_bf16 v[188:191], a[72:75], a[144:147], v[188:191]\n"
"v_mul_f32 v14, v14, v238\n"
"v_mul_f32 v15, v15, v239\n"
"v_mfma_f32_16x16x32_bf16 v[216:219], a[80:83], a[144:147], v[216:219]\n"
"v_cvt_pk_bf16_f32 v30, v12, v13\n"
"v_cvt_pk_bf16_f32 v31, v14, v15\n"
"v_mfma_f32_16x16x32_bf16 v[216:219], a[84:87], a[148:151], v[216:219]\n"
"ds_write_b64 v8, v[30:31] offset:36872\n"
"v_mfma_f32_16x16x32_bf16 v[244:247], a[92:95], a[148:151], v[244:247]\n"
"L_st11493:\n"
"s_cbranch_execz L_retC\n"
"ds_read_b128 a[148:151], v4 offset:31744\n"
"ds_read_b128 v[152:155], v2 offset:36864\n"
"v_mfma_f32_16x16x32_bf16 v[244:247], a[88:91], a[144:147], v[244:247]\n"
"ds_read_b128 a[144:147], v4 offset:30720\n"
"v_mfma_f32_16x16x32_bf16 v[248:251], v[96:99], a[152:155], v[248:251]\n"
"ds_read_b128 v[180:183], v2 offset:37888\n"
"v_accvgpr_read_b32 v16, a216\n"
"v_mfma_f32_16x16x32_bf16 v[248:251], v[124:127], a[156:159], v[248:251]\n"
"v_accvgpr_read_b32 v17, a217\n"
"v_accvgpr_read_b32 v18, a218\n"
"v_mfma_f32_16x16x32_bf16 v[220:223], v[60:63], a[156:159], v[220:223]\n"
"v_accvgpr_read_b32 v19, a219\n"
"v_lshlrev_b32 v12, 16, v16\n"
"v_mfma_f32_16x16x32_bf16 v[220:223], v[32:35], a[152:155], v[220:223]\n"
"v_and_b32 v13, s63, v16\n"
"v_lshlrev_b32 v14, 16, v17\n"
"v_mfma_f32_16x16x32_bf16 v[192:195], v[92:95], a[152:155], v[192:195]\n"
"v_and_b32 v15, s63, v17\n"
"v_mul_f32 v12, v12, v156\n"
"v_mfma_f32_16x16x32_bf16 v[192:195], v[120:123], a[156:159], v[192:195]\n"
"v_mul_f32 v13, v13, v157\n"
"v_mul_f32 v14, v14, v158\n"
"v_mfma_f32_16x16x32_bf16 v[164:167], v[116:119], a[156:159], v[164:167]\n"
"ds_read_b128 a[156:159], v5 offset:31744\n"
"v_mul_f32 v15, v15, v159\n"
"v_mfma_f32_16x16x32_bf16 v[164:167], v[88:91], a[152:155], v[164:167]\n"
"s_waitcnt lgkmcnt(3)\n"
"buffer_store_dwordx4 v[152:155], v6, s[52:55], s84 offen offset:128 nt\n"
"ds_read_b128 a[152:155], v5 offset:30720\n"
"v_cvt_pk_bf16_f32 v24, v12, v13\n"
"v_cvt_pk_bf16_f32 v25, v14, v15\n"
"v_mfma_f32_16x16x32_bf16 v[164:167], v[100:103], a[96:99], v[164:167]\n"
"ds_write_b64 v7, v[24:25] offset:38912\n"
"v_mfma_f32_16x16x32_bf16 v[164:167], v[128:131], a[100:103], v[164:167]\n"
"v_lshlrev_b32 v12, 16, v18\n"
"v_and_b32 v13, s63, v18\n"
"v_mfma_f32_16x16x32_bf16 v[192:195], v[64:67], a[100:103], v[192:195]\n"
"v_lshlrev_b32 v14, 16, v19\n"
"v_and_b32 v15, s63, v19\n"
"v_mfma_f32_16x16x32_bf16 v[192:195], v[36:39], a[96:99], v[192:195]\n"
"v_mul_f32 v12, v12, v184\n"
"v_mul_f32 v13, v13, v185\n"
"v_mfma_f32_16x16x32_bf16 v[220:223], v[104:107], a[96:99], v[220:223]\n"
"v_mul_f32 v14, v14, v186\n"
"v_mul_f32 v15, v15, v187\n"
"v_mfma_f32_16x16x32_bf16 v[220:223], v[132:135], a[100:103], v[220:223]\n"
"v_cvt_pk_bf16_f32 v26, v12, v13\n"
"v_cvt_pk_bf16_f32 v27, v14, v15\n"
"v_mfma_f32_16x16x32_bf16 v[248:251], v[136:139], a[100:103], v[248:251]\n"
"ds_write_b64 v7, v[26:27] offset:38920\n"
"v_mfma_f32_16x16x32_bf16 v[248:251], v[108:111], a[96:99], v[248:251]\n"
"v_accvgpr_read_b32 v16, a220\n"
"v_accvgpr_read_b32 v17, a221\n"
"v_mfma_f32_16x16x32_bf16 v[248:251], a[88:91], a[104:107], v[248:251]\n"
"v_accvgpr_read_b32 v18, a222\n"
"v_accvgpr_read_b32 v19, a223\n"
"v_mfma_f32_16x16x32_bf16 v[248:251], a[92:95], a[108:111], v[248:251]\n"
"s_waitcnt lgkmcnt(4)\n"
"buffer_store_dwordx4 v[180:183], v6, s[52:55], s85 offen offset:1152 nt\n"
"v_lshlrev_b32 v12, 16, v16\n"
"v_and_b32 v13, s63, v16\n"
"v_lshlrev_b32 v14, 16, v17\n"
"v_mfma_f32_16x16x32_bf16 v[220:223], a[84:87], a[108:111], v[220:223]\n"
"v_and_b32 v15, s63, v17\n"
"v_mul_f32 v12, v12, v212\n"
"v_mfma_f32_16x16x32_bf16 v[220:223], a[80:83], a[104:107], v[220:223]\n"
"v_mul_f32 v13, v13, v213\n"
"v_mul_f32 v14, v14, v214\n"
"v_mfma_f32_16x16x32_bf16 v[192:195], a[72:75], a[104:107], v[192:195]\n"
"v_mul_f32 v15, v15, v215\n"
"v_cvt_pk_bf16_f32 v28, v12, v13\n"
"v_mfma_f32_16x16x32_bf16 v[192:195], a[76:79], a[108:111], v[192:195]\n"
"v_cvt_pk_bf16_f32 v29, v14, v15\n"
"v_mfma_f32_16x16x32_bf16 v[164:167], a[68:71], a[108:111], v[164:167]\n"
"ds_write_b64 v8, v[28:29] offset:38912\n"
"v_mfma_f32_16x16x32_bf16 v[164:167], a[64:67], a[104:107], v[164:167]\n"
"v_lshlrev_b32 v12, 16, v18\n"
"v_and_b32 v13, s63, v18\n"
"v_mfma_f32_16x16x32_bf16 v[168:171], v[88:91], a[112:115], v[168:171]\n"
"v_lshlrev_b32 v14, 16, v19\n"
"v_and_b32 v15, s63, v19\n"
"v_mfma_f32_16x16x32_bf16 v[168:171], v[116:119], a[116:119], v[168:171]\n"
"v_mul_f32 v12, v12, v240\n"
"v_mul_f32 v13, v13, v241\n"
"v_mfma_f32_16x16x32_bf16 v[196:199], v[120:123], a[116:119], v[196:199]\n"
"v_mul_f32 v14, v14, v242\n"
"v_mul_f32 v15, v15, v243\n"
"v_mfma_f32_16x16x32_bf16 v[196:199], v[92:95], a[112:115], v[196:199]\n"
"v_cvt_pk_bf16_f32 v30, v12, v13\n"
"v_cvt_pk_bf16_f32 v31, v14, v15\n"
"v_mfma_f32_16x16x32_bf16 v[224:227], v[32:35], a[112:115], v[224:227]\n"
"ds_write_b64 v8, v[30:31] offset:38920\n"
"v_mfma_f32_16x16x32_bf16 v[224:227], v[60:63], a[116:119], v[224:227]\n"
"ds_read_b128 v[156:159], v2 offset:38912\n"
"v_mfma_f32_16x16x32_bf16 v[252:255], v[124:127], a[116:119], v[252:255]\n"
"ds_read_b128 v[184:187], v2 offset:39936\n"
"v_accvgpr_read_b32 v16, a224\n"
"v_mfma_f32_16x16x32_bf16 v[252:255], v[96:99], a[112:115], v[252:255]\n"
"v_accvgpr_read_b32 v17, a225\n"
"v_accvgpr_read_b32 v18, a226\n"
"v_mfma_f32_16x16x32_bf16 v[252:255], v[108:111], a[120:123], v[252:255]\n"
"v_accvgpr_read_b32 v19, a227\n"
"v_lshlrev_b32 v12, 16, v16\n"
"v_mfma_f32_16x16x32_bf16 v[252:255], v[136:139], a[124:127], v[252:255]\n"
"v_and_b32 v13, s63, v16\n"
"v_lshlrev_b32 v14, 16, v17\n"
"v_mfma_f32_16x16x32_bf16 v[224:227], v[132:135], a[124:127], v[224:227]\n"
"v_and_b32 v15, s63, v17\n"
"v_mul_f32 v12, v12, v160\n"
"v_mfma_f32_16x16x32_bf16 v[224:227], v[104:107], a[120:123], v[224:227]\n"
"v_mul_f32 v13, v13, v161\n"
"v_mul_f32 v14, v14, v162\n"
"v_mfma_f32_16x16x32_bf16 v[196:199], v[36:39], a[120:123], v[196:199]\n"
"v_mul_f32 v15, v15, v163\n"
"v_cvt_pk_bf16_f32 v24, v12, v13\n"
"v_mfma_f32_16x16x32_bf16 v[196:199], v[64:67], a[124:127], v[196:199]\n"
"v_cvt_pk_bf16_f32 v25, v14, v15\n"
"v_mfma_f32_16x16x32_bf16 v[168:171], v[128:131], a[124:127], v[168:171]\n"
"s_waitcnt lgkmcnt(1)\n"
"buffer_store_dwordx4 v[156:159], v6, s[52:55], s86 offen offset:2176 nt\n"
"ds_write_b64 v7, v[24:25] offset:32768\n"
"v_mfma_f32_16x16x32_bf16 v[168:171], v[100:103], a[120:123], v[168:171]\n"
"v_lshlrev_b32 v12, 16, v18\n"
"v_and_b32 v13, s63, v18\n"
"v_mfma_f32_16x16x32_bf16 v[168:171], a[64:67], a[128:131], v[168:171]\n"
"v_lshlrev_b32 v14, 16, v19\n"
"v_and_b32 v15, s63, v19\n"
"v_mfma_f32_16x16x32_bf16 v[168:171], a[68:71], a[132:135], v[168:171]\n"
"v_mul_f32 v12, v12, v188\n"
"v_mul_f32 v13, v13, v189\n"
"v_mfma_f32_16x16x32_bf16 v[196:199], a[76:79], a[132:135], v[196:199]\n"
"v_mul_f32 v14, v14, v190\n"
"v_mul_f32 v15, v15, v191\n"
"v_mfma_f32_16x16x32_bf16 v[196:199], a[72:75], a[128:131], v[196:199]\n"
"v_cvt_pk_bf16_f32 v26, v12, v13\n"
"v_cvt_pk_bf16_f32 v27, v14, v15\n"
"v_mfma_f32_16x16x32_bf16 v[224:227], a[80:83], a[128:131], v[224:227]\n"
"ds_write_b64 v7, v[26:27] offset:32776\n"
"v_mfma_f32_16x16x32_bf16 v[224:227], a[84:87], a[132:135], v[224:227]\n"
"v_accvgpr_read_b32 v16, a228\n"
"v_accvgpr_read_b32 v17, a229\n"
"v_mfma_f32_16x16x32_bf16 v[252:255], a[92:95], a[132:135], v[252:255]\n"
"v_accvgpr_read_b32 v18, a230\n"
"v_accvgpr_read_b32 v19, a231\n"
"v_mfma_f32_16x16x32_bf16 v[252:255], a[88:91], a[128:131], v[252:255]\n"
"v_lshlrev_b32 v12, 16, v16\n"
"v_and_b32 v13, s63, v16\n"
"v_mfma_f32_16x16x32_bf16 a[28:31], v[96:99], a[136:139], a[28:31]\n"
"s_waitcnt lgkmcnt(2)\n"
"buffer_store_dwordx4 v[184:187], v6, s[52:55], s87 offen offset:3200 nt\n"
"v_lshlrev_b32 v14, 16, v17\n"
"v_and_b32 v15, s63, v17\n"
"v_mul_f32 v12, v12, v216\n"
"v_mul_f32 v13, v13, v217\n"
"v_mfma_f32_16x16x32_bf16 a[28:31], v[124:127], a[140:143], a[28:31]\n"
"v_mul_f32 v14, v14, v218\n"
"v_mul_f32 v15, v15, v219\n"
"v_mfma_f32_16x16x32_bf16 a[24:27], v[60:63], a[140:143], a[24:27]\n"
"v_cvt_pk_bf16_f32 v28, v12, v13\n"
"v_cvt_pk_bf16_f32 v29, v14, v15\n"
"v_mfma_f32_16x16x32_bf16 a[24:27], v[32:35], a[136:139], a[24:27]\n"
"ds_write_b64 v8, v[28:29] offset:32768\n"
"v_mfma_f32_16x16x32_bf16 a[20:23], v[92:95], a[136:139], a[20:23]\n"
"v_lshlrev_b32 v12, 16, v18\n"
"v_and_b32 v13, s63, v18\n"
"v_mfma_f32_16x16x32_bf16 a[20:23], v[120:123], a[140:143], a[20:23]\n"
"v_lshlrev_b32 v14, 16, v19\n"
"v_and_b32 v15, s63, v19\n"
"v_mfma_f32_16x16x32_bf16 a[16:19], v[116:119], a[140:143], a[16:19]\n"
"v_mul_f32 v12, v12, v244\n"
"v_mul_f32 v13, v13, v245\n"
"v_mfma_f32_16x16x32_bf16 a[16:19], v[88:91], a[136:139], a[16:19]\n"
"v_mul_f32 v14, v14, v246\n"
"v_mul_f32 v15, v15, v247\n"
"v_mfma_f32_16x16x32_bf16 a[16:19], v[100:103], a[144:147], a[16:19]\n"
"v_cvt_pk_bf16_f32 v30, v12, v13\n"
"v_cvt_pk_bf16_f32 v31, v14, v15\n"
"v_mfma_f32_16x16x32_bf16 a[16:19], v[128:131], a[148:151], a[16:19]\n"
"ds_write_b64 v8, v[30:31] offset:32776\n"
"v_mfma_f32_16x16x32_bf16 a[20:23], v[64:67], a[148:151], a[20:23]\n"
"ds_read_b128 v[160:163], v2 offset:32768\n"
"v_mfma_f32_16x16x32_bf16 a[20:23], v[36:39], a[144:147], a[20:23]\n"
"ds_read_b128 v[188:191], v2 offset:33792\n"
"v_accvgpr_read_b32 v16, a232\n"
"v_accvgpr_read_b32 v17, a233\n"
"v_mfma_f32_16x16x32_bf16 a[24:27], v[104:107], a[144:147], a[24:27]\n"
"v_accvgpr_read_b32 v18, a234\n"
"v_accvgpr_read_b32 v19, a235\n"
"v_mfma_f32_16x16x32_bf16 a[24:27], v[132:135], a[148:151], a[24:27]\n"
"v_lshlrev_b32 v12, 16, v16\n"
"v_and_b32 v13, s63, v16\n"
"v_mfma_f32_16x16x32_bf16 a[28:31], v[136:139], a[148:151], a[28:31]\n"
"v_lshlrev_b32 v14, 16, v17\n"
"v_and_b32 v15, s63, v17\n"
"v_mfma_f32_16x16x32_bf16 a[28:31], v[108:111], a[144:147], a[28:31]\n"
"v_mul_f32 v12, v12, v164\n"
"v_mul_f32 v13, v13, v165\n"
"v_mfma_f32_16x16x32_bf16 a[28:31], a[88:91], a[152:155], a[28:31]\n"
"v_mul_f32 v14, v14, v166\n"
"v_mul_f32 v15, v15, v167\n"
"v_mfma_f32_16x16x32_bf16 a[28:31], a[92:95], a[156:159], a[28:31]\n"
"v_cvt_pk_bf16_f32 v24, v12, v13\n"
"v_cvt_pk_bf16_f32 v25, v14, v15\n"
"v_mfma_f32_16x16x32_bf16 a[24:27], a[84:87], a[156:159], a[24:27]\n"
"ds_write_b64 v7, v[24:25] offset:34816\n"
"v_mfma_f32_16x16x32_bf16 a[24:27], a[80:83], a[152:155], a[24:27]\n"
"v_lshlrev_b32 v12, 16, v18\n"
"v_and_b32 v13, s63, v18\n"
"v_mfma_f32_16x16x32_bf16 a[20:23], a[72:75], a[152:155], a[20:23]\n"
"s_waitcnt lgkmcnt(2)\n"
"buffer_store_dwordx4 v[160:163], v6, s[52:55], s88 offen offset:128 nt\n"
"v_lshlrev_b32 v14, 16, v19\n"
"v_and_b32 v15, s63, v19\n"
"v_mul_f32 v12, v12, v192\n"
"v_mul_f32 v13, v13, v193\n"
"s_add_u32 s62, s62, 1\n"
"v_mfma_f32_16x16x32_bf16 a[20:23], a[76:79], a[156:159], a[20:23]\n"
"v_mul_f32 v14, v14, v194\n"
"v_mul_f32 v15, v15, v195\n"
"v_mfma_f32_16x16x32_bf16 a[16:19], a[68:71], a[156:159], a[16:19]\n"
"v_cvt_pk_bf16_f32 v26, v12, v13\n"
"v_cvt_pk_bf16_f32 v27, v14, v15\n"
"v_mfma_f32_16x16x32_bf16 a[16:19], a[64:67], a[152:155], a[16:19]\n"
"ds_write_b64 v7, v[26:27] offset:34824\n"
"s_nop 7\n"
"s_waitcnt lgkmcnt(2)\n"
"buffer_store_dwordx4 v[188:191], v6, s[52:55], s89 offen offset:1152 nt\n"
"v_accvgpr_read_b32 v16, a236\n"
"v_accvgpr_read_b32 v17, a237\n"
"v_accvgpr_read_b32 v18, a238\n"
"v_accvgpr_read_b32 v19, a239\n"
"v_lshlrev_b32 v12, 16, v16\n"
"v_and_b32 v13, s63, v16\n"
"v_lshlrev_b32 v14, 16, v17\n"
"v_and_b32 v15, s63, v17\n"
"v_pk_mul_f32 v[12:13], v[12:13], v[220:221]\n"
"v_pk_mul_f32 v[14:15], v[14:15], v[222:223]\n"
"v_cvt_pk_bf16_f32 v28, v12, v13\n"
"v_cvt_pk_bf16_f32 v29, v14, v15\n"
"v_lshlrev_b32 v12, 16, v18\n"
"v_and_b32 v13, s63, v18\n"
"v_lshlrev_b32 v14, 16, v19\n"
"v_and_b32 v15, s63, v19\n"
"v_pk_mul_f32 v[12:13], v[12:13], v[248:249]\n"
"v_pk_mul_f32 v[14:15], v[14:15], v[250:251]\n"
"v_cvt_pk_bf16_f32 v30, v12, v13\n"
"v_cvt_pk_bf16_f32 v31, v14, v15\n"
"ds_write_b128 v8, v[28:31] offset:34816\n"
"ds_read_b128 v[164:167], v2 offset:34816\n"
"ds_read_b128 v[192:195], v2 offset:35840\n"
"v_accvgpr_read_b32 v16, a240\n"
"v_accvgpr_read_b32 v17, a241\n"
"v_accvgpr_read_b32 v18, a242\n"
"v_accvgpr_read_b32 v19, a243\n"
"v_lshlrev_b32 v12, 16, v16\n"
"v_and_b32 v13, s63, v16\n"
"v_lshlrev_b32 v14, 16, v17\n"
"v_and_b32 v15, s63, v17\n"
"v_pk_mul_f32 v[12:13], v[12:13], v[168:169]\n"
"v_pk_mul_f32 v[14:15], v[14:15], v[170:171]\n"
"v_cvt_pk_bf16_f32 v24, v12, v13\n"
"v_cvt_pk_bf16_f32 v25, v14, v15\n"
"v_lshlrev_b32 v12, 16, v18\n"
"v_and_b32 v13, s63, v18\n"
"v_lshlrev_b32 v14, 16, v19\n"
"v_and_b32 v15, s63, v19\n"
"v_pk_mul_f32 v[12:13], v[12:13], v[196:197]\n"
"v_pk_mul_f32 v[14:15], v[14:15], v[198:199]\n"
"v_cvt_pk_bf16_f32 v26, v12, v13\n"
"v_cvt_pk_bf16_f32 v27, v14, v15\n"
"ds_write_b128 v7, v[24:27] offset:36864\n"
"s_waitcnt lgkmcnt(2)\n"
"buffer_store_dwordx4 v[164:167], v6, s[52:55], s90 offen offset:2176 nt\n"
"s_waitcnt lgkmcnt(1)\n"
"buffer_store_dwordx4 v[192:195], v6, s[52:55], s91 offen offset:3200 nt\n"
"v_accvgpr_read_b32 v16, a244\n"
"v_accvgpr_read_b32 v17, a245\n"
"v_accvgpr_read_b32 v18, a246\n"
"v_accvgpr_read_b32 v19, a247\n"
"v_lshlrev_b32 v12, 16, v16\n"
"v_and_b32 v13, s63, v16\n"
"v_lshlrev_b32 v14, 16, v17\n"
"v_and_b32 v15, s63, v17\n"
"v_pk_mul_f32 v[12:13], v[12:13], v[224:225]\n"
"v_pk_mul_f32 v[14:15], v[14:15], v[226:227]\n"
"v_cvt_pk_bf16_f32 v28, v12, v13\n"
"v_cvt_pk_bf16_f32 v29, v14, v15\n"
"v_lshlrev_b32 v12, 16, v18\n"
"v_and_b32 v13, s63, v18\n"
"v_lshlrev_b32 v14, 16, v19\n"
"v_and_b32 v15, s63, v19\n"
"v_pk_mul_f32 v[12:13], v[12:13], v[252:253]\n"
"v_pk_mul_f32 v[14:15], v[14:15], v[254:255]\n"
"v_cvt_pk_bf16_f32 v30, v12, v13\n"
"v_cvt_pk_bf16_f32 v31, v14, v15\n"
"ds_write_b128 v8, v[28:31] offset:36864\n"
"ds_read_b128 v[168:171], v2 offset:36864\n"
"ds_read_b128 v[196:199], v2 offset:37888\n"
"v_accvgpr_read_b32 v16, a248\n"
"v_accvgpr_read_b32 v17, a249\n"
"v_accvgpr_read_b32 v18, a250\n"
"v_accvgpr_read_b32 v19, a251\n"
"v_lshlrev_b32 v12, 16, v16\n"
"v_and_b32 v13, s63, v16\n"
"v_lshlrev_b32 v14, 16, v17\n"
"v_and_b32 v15, s63, v17\n"
"v_accvgpr_read_b32 v10, a16\n"
"v_accvgpr_read_b32 v11, a17\n"
"v_mul_f32 v12, v12, v10\n"
"v_mul_f32 v13, v13, v11\n"
"v_accvgpr_read_b32 v10, a18\n"
"v_accvgpr_read_b32 v11, a19\n"
"v_mul_f32 v14, v14, v10\n"
"v_mul_f32 v15, v15, v11\n"
"v_cvt_pk_bf16_f32 v24, v12, v13\n"
"v_cvt_pk_bf16_f32 v25, v14, v15\n"
"v_lshlrev_b32 v12, 16, v18\n"
"v_and_b32 v13, s63, v18\n"
"v_lshlrev_b32 v14, 16, v19\n"
"v_and_b32 v15, s63, v19\n"
"v_accvgpr_read_b32 v10, a20\n"
"v_accvgpr_read_b32 v11, a21\n"
"v_mul_f32 v12, v12, v10\n"
"v_mul_f32 v13, v13, v11\n"
"v_accvgpr_read_b32 v10, a22\n"
"v_accvgpr_read_b32 v11, a23\n"
"v_mul_f32 v14, v14, v10\n"
"v_mul_f32 v15, v15, v11\n"
"v_cvt_pk_bf16_f32 v26, v12, v13\n"
"v_cvt_pk_bf16_f32 v27, v14, v15\n"
"ds_write_b128 v7, v[24:27] offset:38912\n"
"s_waitcnt lgkmcnt(2)\n"
"buffer_store_dwordx4 v[168:171], v6, s[52:55], s92 offen offset:128 nt\n"
"s_waitcnt lgkmcnt(1)\n"
"buffer_store_dwordx4 v[196:199], v6, s[52:55], s93 offen offset:1152 nt\n"
"v_accvgpr_read_b32 v16, a252\n"
"v_accvgpr_read_b32 v17, a253\n"
"v_accvgpr_read_b32 v18, a254\n"
"v_accvgpr_read_b32 v19, a255\n"
"v_lshlrev_b32 v12, 16, v16\n"
"v_and_b32 v13, s63, v16\n"
"v_lshlrev_b32 v14, 16, v17\n"
"v_and_b32 v15, s63, v17\n"
"v_accvgpr_read_b32 v10, a24\n"
"v_accvgpr_read_b32 v11, a25\n"
"v_mul_f32 v12, v12, v10\n"
"v_mul_f32 v13, v13, v11\n"
"v_accvgpr_read_b32 v10, a26\n"
"v_accvgpr_read_b32 v11, a27\n"
"v_mul_f32 v14, v14, v10\n"
"v_mul_f32 v15, v15, v11\n"
"v_cvt_pk_bf16_f32 v28, v12, v13\n"
"v_cvt_pk_bf16_f32 v29, v14, v15\n"
"v_lshlrev_b32 v12, 16, v18\n"
"v_and_b32 v13, s63, v18\n"
"v_lshlrev_b32 v14, 16, v19\n"
"v_and_b32 v15, s63, v19\n"
"v_accvgpr_read_b32 v10, a28\n"
"v_accvgpr_read_b32 v11, a29\n"
"v_mul_f32 v12, v12, v10\n"
"v_mul_f32 v13, v13, v11\n"
"v_accvgpr_read_b32 v10, a30\n"
"v_accvgpr_read_b32 v11, a31\n"
"v_mul_f32 v14, v14, v10\n"
"v_mul_f32 v15, v15, v11\n"
"v_cvt_pk_bf16_f32 v30, v12, v13\n"
"v_cvt_pk_bf16_f32 v31, v14, v15\n"
"ds_write_b128 v8, v[28:31] offset:38912\n"
"ds_read_b128 v[224:227], v2 offset:38912\n"
"ds_read_b128 v[252:255], v2 offset:39936\n"
"s_waitcnt lgkmcnt(1)\n"
"buffer_store_dwordx4 v[224:227], v6, s[52:55], s94 offen offset:2176 nt\n"
"s_waitcnt lgkmcnt(0)\n"
"buffer_store_dwordx4 v[252:255], v6, s[52:55], s95 offen offset:3200 nt\n"
"s_or_b32 s66, %26, %16\n"
"s_cbranch_scc1 L_end\n"
"v_mov_b32 v10, s78\n"
"v_mov_b32 v11, s79\n"
"s_mov_b32 s66, 139264\n"
"buffer_store_dwordx2 v[10:11], off, s[56:59], s66 sc1\n"
"s_branch L_end\n"
"L_slow0:\n"
"s_cmp_eq_u32 s68, 0\n"
"s_cbranch_scc1 L_back0\n"
"s_sub_u32 s68, s68, 1\n"
"s_sleep 2\n"
"buffer_load_dwordx2 v[8:9], off, s[56:59], %14 sc1\n"
"s_waitcnt vmcnt(0)\n"
"v_subrev_u32 v8, s65, v8\n"
"v_xor_b32 v9, s79, v9\n"
"v_or_b32 v8, v8, v9\n"
"v_cmp_lt_u32 vcc, 15, v8\n"
"s_nop 4\n"
"s_cbranch_vccnz L_slow0\n"
"s_branch L_back0\n"
"L_slow1:\n"
"s_cmp_eq_u32 s68, 0\n"
"s_cbranch_scc1 L_back1\n"
"s_sub_u32 s68, s68, 1\n"
"s_sleep 2\n"
"buffer_load_dwordx2 v[8:9], off, s[56:59], %14 sc1\n"
"s_waitcnt vmcnt(0)\n"
"v_subrev_u32 v8, s65, v8\n"
"v_xor_b32 v9, s79, v9\n"
"v_or_b32 v8, v8, v9\n"
"v_cmp_lt_u32 vcc, 15, v8\n"
"s_nop 4\n"
"s_cbranch_vccnz L_slow1\n"
"s_branch L_back1\n"
"L_slow2:\n"
"s_cmp_eq_u32 s68, 0\n"
"s_cbranch_scc1 L_back2\n"
"s_sub_u32 s68, s68, 1\n"
"s_sleep 2\n"
"buffer_load_dwordx4 v[12:15], v0, s[56:59], %25 offen sc1\n"
"s_waitcnt vmcnt(0)\n"
"v_subrev_u32 v12, s67, v12\n"
"v_xor_b32 v13, s79, v13\n"
"v_subrev_u32 v14, s67, v14\n"
"v_xor_b32 v15, s79, v15\n"
"v_or_b32 v12, v12, v14\n"
"v_or_b32 v13, v13, v15\n"
"v_or_b32 v12, v12, v13\n"
"v_cmp_lt_u32 vcc, 15, v12\n"
"s_nop 4\n"
"s_cbranch_vccnz L_slow2\n"
"s_branch L_back2\n"
"L_slow3:\n"
"s_cmp_eq_u32 s68, 0\n"
"s_cbranch_scc1 L_back3\n"
"s_sub_u32 s68, s68, 1\n"
"s_sleep 2\n"
"buffer_load_dwordx4 v[24:27], v0, s[56:59], %25 offen sc1\n"
"s_waitcnt vmcnt(0)\n"
"v_subrev_u32 v24, s70, v24\n"
"v_xor_b32 v25, s79, v25\n"
"v_subrev_u32 v26, s70, v26\n"
"v_xor_b32 v27, s79, v27\n"
"v_or_b32 v24, v24, v26\n"
"v_or_b32 v25, v25, v27\n"
"v_or_b32 v24, v24, v25\n"
"v_cmp_lt_u32 vcc, 15, v24\n"
"s_nop 4\n"
"s_cbranch_vccnz L_slow3\n"
"s_branch L_back3\n"
"L_slow4:\n"
"s_cmp_eq_u32 s68, 0\n"
"s_cbranch_scc1 L_back4\n"
"s_sub_u32 s68, s68, 1\n"
"s_sleep 2\n"
"buffer_load_dwordx2 v[8:9], off, s[56:59], %14 sc1\n"
"s_waitcnt vmcnt(0)\n"
"v_subrev_u32 v8, s65, v8\n"
"v_xor_b32 v9, s79, v9\n"
"v_or_b32 v8, v8, v9\n"
"v_cmp_lt_u32 vcc, 15, v8\n"
"s_nop 4\n"
"s_cbranch_vccnz L_slow4\n"
"s_branch L_back4\n"
"L_slow7:\n"
"s_cmp_eq_u32 s68, 0\n"
"s_cbranch_scc1 L_back7\n"
"s_sub_u32 s68, s68, 1\n"
"s_sleep 2\n"
"buffer_load_dwordx2 v[8:9], off, s[56:59], %14 sc1\n"
"s_waitcnt vmcnt(0)\n"
"v_subrev_u32 v8, s65, v8\n"
"v_xor_b32 v9, s79, v9\n"
"v_or_b32 v8, v8, v9\n"
"v_cmp_lt_u32 vcc, 15, v8\n"
"s_nop 4\n"
"s_cbranch_vccnz L_slow7\n"
"s_branch L_back7\n"
"L_headF:\n"
"L_end:\n"
"s_waitcnt vmcnt(0) lgkmcnt(0)\n"
    :: LO(wsb), HI(wsb), LO(xo), HI(xo), LO(ob), HI(ob), LO(wr), HI(wr), LO(bb), HI(bb), "s"(wo), "s"(eo), "s"(ep), "s"(fo), "s"(fp),
       "s"(ldsa), "s"(w), "s"(col), "v"(threadIdx.x), "s"(db), "s"(rs1), "s"(rs2), "s"(rd1), "s"(rn1), "s"(wfo), "s"(wfa), "s"(b)
    : "memory", "scc", "vcc",
"v1","v2","v3","v4","v5","v6","v7","v8","v9","v10","v11","v12","v13","v14","v15","v16","v17","v18","v19","v20","v21","v22","v23","v24","v25","v26","v27","v28","v29","v30","v31","v32","v33","v34","v35","v36","v37","v38","v39","v40","v41","v42","v43","v44","v45","v46","v47","v48","v49","v50","v51","v52","v53","v54","v55","v56","v57","v58","v59","v60","v61","v62","v63","v64","v65","v66","v67","v68","v69","v70","v71","v72","v73","v74","v75","v76","v77","v78","v79","v80","v81","v82","v83","v84","v85","v86","v87","v88","v89","v90","v91","v92","v93","v94","v95","v96","v97","v98","v99","v100","v101","v102","v103","v104","v105","v106","v107","v108","v109","v110","v111","v112","v113","v114","v115","v116","v117","v118","v119","v120","v121","v122","v123","v124","v125","v126","v127","v128","v129","v130","v131","v132","v133","v134","v135","v136","v137","v138","v139","v140","v141","v142","v143","v144","v145","v146","v147","v148","v149","v150","v151","v152","v153","v154","v155","v156","v157","v158","v159","v160","v161","v162","v163","v164","v165","v166","v167","v168","v169","v170","v171","v172","v173","v174","v175","v176","v177","v178","v179","v180","v181","v182","v183","v184","v185","v186","v187","v188","v189","v190","v191","v192","v193","v194","v195","v196","v197","v198","v199","v200","v201","v202","v203","v204","v205","v206","v207","v208","v209","v210","v211","v212","v213","v214","v215","v216","v217","v218","v219","v220","v221","v222","v223","v224","v225","v226","v227","v228","v229","v230","v231","v232","v233","v234","v235","v236","v237","v238","v239","v240","v241","v242","v243","v244","v245","v246","v247","v248","v249","v250","v251","v252","v253","v254","v255",
"a0","a1","a2","a3","a4","a5","a6","a7","a8","a9","a10","a11","a12","a13","a14","a15","a16","a17","a18","a19","a20","a21","a22","a23","a24","a25","a26","a27","a28","a29","a30","a31","a32","a33","a34","a35","a36","a37","a38","a39","a40","a41","a42","a43","a44","a45","a46","a47","a48","a49","a50","a51","a52","a53","a54","a55","a56","a57","a58","a59","a60","a61","a62","a63","a64","a65","a66","a67","a68","a69","a70","a71","a72","a73","a74","a75","a76","a77","a78","a79","a80","a81","a82","a83","a84","a85","a86","a87","a88","a89","a90","a91","a92","a93","a94","a95","a96","a97","a98","a99","a100","a101","a102","a103","a104","a105","a106","a107","a108","a109","a110","a111","a112","a113","a114","a115","a116","a117","a118","a119","a120","a121","a122","a123","a124","a125","a126","a127","a128","a129","a130","a131","a132","a133","a134","a135","a136","a137","a138","a139","a140","a141","a142","a143","a144","a145","a146","a147","a148","a149","a150","a151","a152","a153","a154","a155","a156","a157","a158","a159","a160","a161","a162","a163","a164","a165","a166","a167","a168","a169","a170","a171","a172","a173","a174","a175","a176","a177","a178","a179","a180","a181","a182","a183","a184","a185","a186","a187","a188","a189","a190","a191","a192","a193","a194","a195","a196","a197","a198","a199","a200","a201","a202","a203","a204","a205","a206","a207","a208","a209","a210","a211","a212","a213","a214","a215","a216","a217","a218","a219","a220","a221","a222","a223","a224","a225","a226","a227","a228","a229","a230","a231","a232","a233","a234","a235","a236","a237","a238","a239","a240","a241","a242","a243","a244","a245","a246","a247","a248","a249","a250","a251","a252","a253","a254","a255",
"s27","s28","s29","s30","s31","s33","s34","s35","s36","s37","s38","s39","s40","s41","s42","s43","s44","s45","s46","s47","s48","s49","s50","s51","s52","s53","s54","s55","s56","s57","s58","s59","s60","s61","s62","s63","s64","s65","s66","s67","s68","s69","s70","s71","s72","s73","s74","s75","s76","s77","s78","s79","s80","s81","s82","s83","s84","s85","s86","s87","s88","s89","s90","s91","s92","s93","s94","s95","s96","s97","s98","s99");
}

extern "C" void kernel_launch(void* const* d_in, const int* in_sizes, int n_in,
                              void* d_out, int out_size, void* d_ws, size_t ws_size,
                              hipStream_t stream) {
  const unsigned short* x0 = (const unsigned short*)d_in[0];
  const unsigned short* ws = (const unsigned short*)d_in[1];
  const unsigned short* bs = (const unsigned short*)d_in[2];
  dcn_cross_kernel<<<256, 256, 0, stream>>>(x0, ws, bs, (unsigned char*)d_ws, (unsigned short*)d_out);
}
```

```cpp
#include <hip/hip_runtime.h>


constexpr int kD = 1024;
constexpr unsigned kWFlagOff = 131072, kWOff = 1u << 20, kWStream = 200 * 8192, kXchOff = 32u << 20, kDbgOff = 224u << 20;

__device__ __forceinline__ unsigned unit_src(unsigned v, unsigned h, unsigned w) {
  const unsigned l = v >> 5, u = v & 31;
  const unsigned split = ((0xff0000ffu >> u) & 1) - 1;
  const unsigned part = l ? u >> 4 : 0u, g = (0xff00ff00u >> u) & 1;
  const unsigned jh = (u >> (~split & 2)) & 1, c = (u >> (split & 1)) ^ (~split & (0u - jh));
  const unsigned kc = 8 * (h ^ part) + 2 * ((w + c) & 3) + g;
  return (l * 1024 + 64 * kc) * 2048 + jh * 128;
}

__global__ __launch_bounds__(256, 1) __attribute__((aligned(4096)))
void dcn_cross_kernel(const unsigned short* __restrict__ x0, const unsigned short* __restrict__ ws, const unsigned short* __restrict__ bs,
                      unsigned char* __restrict__ wsp, unsigned short* __restrict__ out) {
  __shared__ __attribute__((aligned(16))) unsigned char lds[163840];
  const unsigned b = blockIdx.x;
  constexpr unsigned kPx = 1 ? 1u : 8u;
  const unsigned h = 1 ? (b & 1) : ((b >> 3) & 1), rb = 1 ? (b >> 1) : ((b >> 4) * 8 + (b & 7));
  const unsigned w = __builtin_amdgcn_readfirstlane(threadIdx.x >> 6);
  unsigned col = (512 * h + 128 * w) * 2, rows = rb * 128 * kD * 2, oofs = rows + col;
  unsigned wo = (h * 4 + w) * kWStream, eo = kXchOff + b * 524288 + w * 32768, ep = kXchOff + (b ^ kPx) * 524288 + w * 32768;
  unsigned fo = b * 512 + w * 128, fp = (b ^ kPx) * 512 + w * 128;
  unsigned db = kDbgOff + (b * 4 + w) * 512;
  const unsigned v1 = 24 + (rb >> 1), v2 = 64 + ((rb >> 1) & 31), hk = rb & 1, h2 = rb >> 6;
  unsigned rs1 = unit_src(v1, 0, w) + hk * 65536, rd1 = v1 * 8192 + hk * 1024;
  unsigned rs2 = unit_src(v2, h2, w) + hk * 65536 + h2 * 1024 + h * ((4u << 20) - 1024), rn1 = v2 * 8192 + hk * 1024 + h2 * 4 * kWStream;
  unsigned wfa = kWFlagOff + w * 1024, wfo = wfa + h * 4096 + rb * 8;
  asm volatile("" : "+s"(col), "+s"(rows), "+s"(oofs), "+s"(wo), "+s"(eo), "+s"(ep), "+s"(fo), "+s"(fp), "+s"(db), "+s"(rs1), "+s"(rd1), "+s"(rs2), "+s"(rn1), "+s"(wfa), "+s"(wfo));
  __builtin_amdgcn_sched_barrier(0);
  const unsigned long long wsb = (unsigned long long)wsp, bb = (unsigned long long)bs;
  const unsigned long long xo = (unsigned long long)x0 + rows, ob = (unsigned long long)out + oofs, wr = (unsigned long long)ws + col;
  wo += (unsigned)wsb > ~(15u << 20) ? 16u << 20 : kWOff;
  const unsigned xa = (0u - (unsigned)wsb) & 0x1fffffu;
  eo += xa; ep += xa;
  const unsigned ldsa = (unsigned)(size_t)(__attribute__((address_space(3))) unsigned char*)lds;
#define LO(x) "s"((unsigned)(x))
#define HI(x) "s"((unsigned)((x) >> 32))
  asm volatile(
"L_main_entry:\n"
"v_and_b32 v12, 63, %18\n"
"v_lshrrev_b32 v13, 3, v12\n"
"v_and_b32 v15, 7, v12\n"
"v_lshlrev_b32 v10, 11, v13\n"
"s_andn2_b32 s60, 1024, %17\n"
"s_lshl_b32 s75, s60, 10\n"
"s_mov_b32 s52, %6\n"
"s_and_b32 s53, %7, 0xffff\n"
"s_mov_b32 s54, 12581120\n"
"s_mov_b32 s55, 0x20000\n"
"s_getpc_b64 s[96:97]\n"
"L_pc:\n"
"s_lshr_b32 s66, %26, 3\n"
"s_lshl_b32 s66, s66, 2\n"
"s_add_u32 s66, s66, %16\n"
"s_mul_i32 s66, s66, 704\n"
"s_and_b32 s97, s97, 0xffff\n"
"s_mov_b32 s98, L_end-L_pc\n"
"s_mov_b32 s99, 0x20000\n"
"s_min_u32 s66, s66, L_end-L_pc-704\n"
"v_min_u32 v9, 10, v12\n"
"v_lshlrev_b32 v9, 6, v9\n"
"buffer_load_dword a252, v9, s[96:99], s66 offen\n"
"v_xor_b32 v6, v15, v13\n"
"v_lshl_add_u32 v6, v6, 4, v10\n"
"s_mov_b32 s48, %2\n"
"s_and_b32 s49, %3, 0xffff\n"
"s_mov_b32 s50, 65536\n"
"s_mov_b32 s51, 0x20000\n"
"s_lshl_b32 s66, %16, 16\n"
"s_add_u32 s48, s48, s66\n"
"s_addc_u32 s49, s49, 0\n"
"s_lshl_b32 s72, %16, 12\n"
"s_add_u32 s72, s72, %15\n"
"s_mul_i32 s73, %16, 40960\n"
"s_add_u32 s64, s73, %15\n"
"v_lshrrev_b32 v11, 2, v12\n"
"v_and_b32 v11, 4, v11\n"
"v_xor_b32 v11, v11, v15\n"
"v_lshl_add_u32 v11, v11, 4, v10\n"
"s_bitcmp1_b32 %26, 0\n"
"s_cbranch_scc1 L_y\n"
"s_mov_b64 s[96:97], s[52:53]\n"
"s_cmp_lt_u32 %22, 524288\n"
"s_cselect_b32 s98, s54, 0\n"
"s_mov_b32 s99, 0x20000\n"
"s_xor_b32 s71, %20, 0x100000\n"
"s_add_u32 m0, s72, 139264\n"
"s_add_u32 s67, %20, 0\n"
"buffer_load_dwordx4 v11, s[96:99], s67 offen offset:0  lds\n"
"s_add_u32 s67, %20, 15360\n"
"buffer_load_dwordx4 v11, s[96:99], s67 offen offset:1024  lds\n"
"s_add_u32 s67, %20, 30720\n"
"buffer_load_dwordx4 v11, s[96:99], s67 offen offset:2048  lds\n"
"s_add_u32 s67, %20, 46080\n"
"buffer_load_dwordx4 v11, s[96:99], s67 offen offset:3072  lds\n"
"s_add_u32 m0, s72, 98304\n"
"s_add_u32 s67, s71, 1024\n"
"buffer_load_dwordx4 v11, s[96:99], s67 offen offset:0  lds\n"
"s_add_u32 s67, s71, 16384\n"
"buffer_load_dwordx4 v11, s[96:99], s67 offen offset:1024  lds\n"
"s_add_u32 s67, s71, 31744\n"
"buffer_load_dwordx4 v11, s[96:99], s67 offen offset:2048  lds\n"
"s_add_u32 s67, s71, 47104\n"
"buffer_load_dwordx4 v11, s[96:99], s67 offen offset:3072  lds\n"
"L_y:\n"
"s_add_u32 m0, s72, 16384\n"
"s_add_u32 s67, s60, 0\n"
"buffer_load_dwordx4 v6, s[48:51], s67 offen offset:0  lds\n"
"s_add_u32 s67, s60, 15360\n"
"buffer_load_dwordx4 v6, s[48:51], s67 offen offset:1024  lds\n"
"s_add_u32 s67, s60, 30720\n"
"buffer_load_dwordx4 v6, s[48:51], s67 offen offset:2048  lds\n"
"s_add_u32 s67, s60, 46080\n"
"buffer_load_dwordx4 v6, s[48:51], s67 offen offset:3072  lds\n"
"s_add_u32 m0, s64, 0\n"
"s_add_u32 s67, s75, 0\n"
"buffer_load_dwordx4 v11, s[52:55], s67 offen offset:0  lds\n"
"s_add_u32 s67, s75, 15360\n"
"buffer_load_dwordx4 v11, s[52:55], s67 offen offset:1024  lds\n"
"s_add_u32 s67, s75, 30720\n"
"buffer_load_dwordx4 v11, s[52:55], s67 offen offset:2048  lds\n"
"s_add_u32 s67, s75, 46080\n"
"buffer_load_dwordx4 v11, s[52:55], s67 offen offset:3072  lds\n"
"s_add_u32 m0, s64, 4096\n"
"s_add_u32 s67, s75, 65536\n"
"buffer_load_dwordx4 v11, s[52:55], s67 offen offset:0  lds\n"
"s_add_u32 s67, s75, 80896\n"
"buffer_load_dwordx4 v11, s[52:55], s67 offen offset:1024  lds\n"
"s_add_u32 s67, s75, 96256\n"
"buffer_load_dwordx4 v11, s[52:55], s67 offen offset:2048  lds\n"
"s_add_u32 s67, s75, 111616\n"
"buffer_load_dwordx4 v11, s[52:55], s67 offen offset:3072  lds\n"
"s_lshr_b32 s65, %26, 4\n"
"s_lshl_b32 s65, s65, 2\n"
"s_add_u32 s65, s65, %16\n"
"s_lshl_b32 s65, s65, 11\n"
"s_lshl_b32 s67, %16, 8\n"
"s_sub_u32 s65, s65, s67\n"
"v_and_b32 v14, 1, v13\n"
"v_lshlrev_b32 v14, 17, v14\n"
"v_lshl_add_u32 v14, v15, 7, v14\n"
"v_and_b32 v14, 0xfffffdff, v14\n"
"s_lshl_b32 s67, %26, 6\n"
"s_and_b32 s67, s67, 512\n"
"s_add_u32 s65, s65, s67\n"
"v_lshlrev_b32 v0, 4, v12\n"
"s_mov_b32 s56, %0\n"
"s_and_b32 s57, %1, 0xffff\n"
"s_mov_b32 s58, 171966464\n"
"s_mov_b32 s59, 0x20000\n"
"v_add_u32 v1, 4096, v0\n"
"s_add_u32 s36, s56, %10\n"
"s_addc_u32 s37, s57, 0\n"
"s_mov_b32 s38, 1572864\n"
"s_mov_b32 s39, 0x20000\n"
"s_add_u32 s34, s36, 200704\n"
"s_mov_b32 s35, s37\n"
"v_and_b32 v13, 15, v12\n"
"v_lshrrev_b32 v10, 4, v12\n"
"v_xor_b32 v15, v15, v10\n"
"v_lshlrev_b32 v7, 7, v13\n"
"v_lshl_add_u32 v7, v15, 4, v7\n"
"v_add_u32 v7, s64, v7\n"
"v_xor_b32 v8, 64, v7\n"
"v_bfe_u32 v13, v12, 2, 2\n"
"v_and_b32 v15, 3, v12\n"
"v_bfe_u32 v9, v12, 3, 1\n"
"v_lshl_add_u32 v15, v9, 2, v15\n"
"v_lshl_add_u32 v13, v10, 3, v13\n"
"v_lshlrev_b32 v13, 7, v13\n"
"v_lshl_add_u32 v9, v15, 4, v13\n"
"v_add_u32 v9, s64, v9\n"
"v_xor_b32 v15, 64, v9\n"
"s_mov_b32 s40, %2\n"
"s_and_b32 s41, %3, 0xffff\n"
"s_mov_b32 s42, 260352\n"
"s_mov_b32 s43, 0x20000\n"
"s_add_u32 s40, s40, %17\n"
"s_addc_u32 s41, s41, 0\n"
"s_mov_b32 s80, 0\n"
"s_mov_b32 s81, 15360\n"
"s_mov_b32 s82, 30720\n"
"s_mov_b32 s83, 46080\n"
"s_mov_b32 s84, 65536\n"
"s_mov_b32 s85, 80896\n"
"s_mov_b32 s86, 96256\n"
"s_mov_b32 s87, 111616\n"
"s_mov_b32 s88, 131072\n"
"s_mov_b32 s89, 146432\n"
"s_mov_b32 s90, 161792\n"
"s_mov_b32 s91, 177152\n"
"s_mov_b32 s92, 196608\n"
"s_mov_b32 s93, 211968\n"
"s_mov_b32 s94, 227328\n"
"s_mov_b32 s95, 242688\n"
"s_mov_b32 s62, 0\n"
"s_mov_b32 s63, 0xffff0000\n"
"s_mov_b32 s68, 0x8000\n"
"s_add_u32 s66, %16, 0\n"
"s_and_b32 s66, s66, 3\n"
"s_mul_i32 s66, s66, 40960\n"
"s_add_u32 s66, s66, %15\n"
"v_add_u32 v2, s66, v0\n"
"s_add_u32 s66, %16, 1\n"
"s_and_b32 s66, s66, 3\n"
"s_mul_i32 s66, s66, 40960\n"
"s_add_u32 s66, s66, %15\n"
"v_add_u32 v3, s66, v0\n"
"s_add_u32 s66, %16, 2\n"
"s_and_b32 s66, s66, 3\n"
"s_mul_i32 s66, s66, 40960\n"
"s_add_u32 s66, s66, %15\n"
"v_add_u32 v4, s66, v0\n"
"s_add_u32 s66, %16, 3\n"
"s_and_b32 s66, s66, 3\n"
"s_mul_i32 s66, s66, 40960\n"
"s_add_u32 s66, s66, %15\n"
"v_add_u32 v5, s66, v0\n"
"s_sub_u32 s66, 0, s73\n"
"v_add_u32 v12, s66, v7\n"
"v_add_u32 v13, s66, v8\n"
"s_mov_b64 exec, 0\n"
"s_cmp_eq_u32 %16, 0\n"
"s_cbranch_scc1 L_st531\n"
"s_cmp_eq_u32 %16, 1\n"
"s_cbranch_scc1 L_st1058\n"
"s_cmp_eq_u32 %16, 2\n"
"s_cbranch_scc1 L_st1588\n"
"s_branch L_st2130\n"
"L_retA:\n"
"s_mov_b64 exec, -1\n"
"s_nop 3\n"
"s_mov_b32 s44, %8\n"
"s_and_b32 s45, %9, 0xffff\n"
"s_mov_b32 s46, 12288\n"
"s_mov_b32 s47, 0x20000\n"
"s_mov_b32 s61, %17\n"
"v_lshrrev_b32 v16, 4, v0\n"
"v_and_b32 v16, 0x30, v16\n"
"buffer_load_dwordx2 v[18:19], v16, s[44:47], s61 offen offset:0\n"
"v_lshrrev_b32 v20, 4, v0\n"
"v_and_b32 v20, 0x30, v20\n"
"buffer_load_dwordx2 v[22:23], v20, s[44:47], s61 offen offset:8\n"
"v_lshrrev_b32 v24, 4, v0\n"
"v_and_b32 v24, 0x30, v24\n"
"buffer_load_dwordx2 v[26:27], v24, s[44:47], s61 offen offset:64\n"
"v_lshrrev_b32 v28, 4, v0\n"
"v_and_b32 v28, 0x30, v28\n"
"buffer_load_dwordx2 v[30:31], v28, s[44:47], s61 offen offset:72\n"
"s_add_u32 s70, s75, 131072\n"
"s_add_u32 s70, s70, s65\n"
"buffer_load_dword a252, v14, s[52:55], s70 offen\n"
"s_add_u32 m0, s64, 8192\n"
"s_add_u32 s67, s75, 128\n"
"buffer_load_dwordx4 v11, s[52:55], s67 offen offset:0  lds\n"
"s_add_u32 s67, s75, 15488\n"
"buffer_load_dwordx4 v11, s[52:55], s67 offen offset:1024  lds\n"
"s_add_u32 s67, s75, 30848\n"
"buffer_load_dwordx4 v11, s[52:55], s67 offen offset:2048  lds\n"
"s_add_u32 s67, s75, 46208\n"
"buffer_load_dwordx4 v11, s[52:55], s67 offen offset:3072  lds\n"
"s_add_u32 m0, s64, 12288\n"
"s_add_u32 s67, s75, 65664\n"
"buffer_load_dwordx4 v11, s[52:55], s67 offen offset:0  lds\n"
"s_add_u32 s67, s75, 81024\n"
"buffer_load_dwordx4 v11, s[52:55], s67 offen offset:1024  lds\n"
"s_add_u32 s67, s75, 96384\n"
"buffer_load_dwordx4 v11, s[52:55], s67 offen offset:2048  lds\n"
"s_add_u32 s67, s75, 111744\n"
"buffer_load_dwordx4 v11, s[52:55], s67 offen offset:3072  lds\n"
"s_mov_b32 s67, 139264\n"
"buffer_load_dwordx2 v[252:253], off, s[56:59], s67 sc1\n"
"s_waitcnt vmcnt(22)\n"
"s_barrier\n"
"ds_read_b128 a[100:103], v13 offset:16384\n"
"ds_read_b128 a[96:99], v12 offset:16384\n"
"ds_read_b128 a[108:111], v13 offset:18432\n"
"ds_read_b128 a[104:107], v12 offset:18432\n"
"ds_read_b128 a[116:119], v13 offset:20480\n"
"ds_read_b128 a[112:115], v12 offset:20480\n"
"ds_read_b128 a[124:127], v13 offset:22528\n"
"ds_read_b128 a[120:123], v12 offset:22528\n"
"ds_read_b128 a[132:135], v13 offset:24576\n"
"ds_read_b128 a[128:131], v12 offset:24576\n"
"ds_read_b128 a[140:143], v13 offset:26624\n"
"ds_read_b128 a[136:139], v12 offset:26624\n"
"ds_read_b128 a[148:151], v13 offset:28672\n"
"ds_read_b128 a[144:147], v12 offset:28672\n"
"ds_read_b128 a[156:159], v13 offset:30720\n"
"ds_read_b128 a[152:155], v12 offset:30720\n"
"s_add_u32 m0, s72, 57344\n"
"s_add_u32 s67, s60, 128\n"
"buffer_load_dwordx4 v6, s[48:51], s67 offen offset:0  lds\n"
"s_add_u32 s67, s60, 15488\n"
"buffer_load_dwordx4 v6, s[48:51], s67 offen offset:1024  lds\n"
"s_add_u32 s67, s60, 30848\n"
"buffer_load_dwordx4 v6, s[48:51], s67 offen offset:2048  lds\n"
"s_add_u32 s67, s60, 46208\n"
"buffer_load_dwordx4 v6, s[48:51], s67 offen offset:3072  lds\n"
"s_waitcnt vmcnt(18)\n"
"ds_read_b64_tr_b16 a[32:33], v9 offset:0\n"
"ds_read_b64_tr_b16 a[34:35], v9 offset:512\n"
"ds_read_b64_tr_b16 a[36:37], v9 offset:4096\n"
"ds_read_b64_tr_b16 a[38:39], v9 offset:4608\n"
"ds_read_b64_tr_b16 a[40:41], v9 offset:8\n"
"ds_read_b64_tr_b16 a[42:43], v9 offset:520\n"
"ds_read_b64_tr_b16 a[44:45], v9 offset:4104\n"
"ds_read_b64_tr_b16 a[46:47], v9 offset:4616\n"
"ds_read_b64_tr_b16 a[48:49], v15 offset:0\n"
"ds_read_b64_tr_b16 a[50:51], v15 offset:512\n"
"ds_read_b64_tr_b16 a[52:53], v15 offset:4096\n"
"ds_read_b64_tr_b16 a[54:55], v15 offset:4608\n"
"ds_read_b64_tr_b16 a[56:57], v15 offset:8\n"
"ds_read_b64_tr_b16 a[58:59], v15 offset:520\n"
"ds_read_b64_tr_b16 a[60:61], v15 offset:4104\n"
"ds_read_b64_tr_b16 a[62:63], v15 offset:4616\n"
"s_waitcnt vmcnt(14)\n"
"v_lshlrev_b32 v16, 16, v18\n"
"v_and_b32 v17, s63, v18\n"
"v_lshlrev_b32 v18, 16, v19\n"
"v_and_b32 v19, s63, v19\n"
"v_add_f32 v16, 1.0, v16\n"
"v_add_f32 v17, 1.0, v17\n"
"v_add_f32 v18, 1.0, v18\n"
"v_add_f32 v19, 1.0, v19\n"
"v_lshlrev_b32 v20, 16, v22\n"
"v_and_b32 v21, s63, v22\n"
"v_lshlrev_b32 v22, 16, v23\n"
"v_and_b32 v23, s63, v23\n"
"v_add_f32 v20, 1.0, v20\n"
"v_add_f32 v21, 1.0, v21\n"
"v_add_f32 v22, 1.0, v22\n"
"v_add_f32 v23, 1.0, v23\n"
"v_lshlrev_b32 v24, 16, v26\n"
"v_and_b32 v25, s63, v26\n"
"v_lshlrev_b32 v26, 16, v27\n"
"v_and_b32 v27, s63, v27\n"
"v_add_f32 v24, 1.0, v24\n"
"v_add_f32 v25, 1.0, v25\n"
"v_add_f32 v26, 1.0, v26\n"
"v_add_f32 v27, 1.0, v27\n"
"v_lshlrev_b32 v28, 16, v30\n"
"v_and_b32 v29, s63, v30\n"
"v_lshlrev_b32 v30, 16, v31\n"
"v_and_b32 v31, s63, v31\n"
"v_add_f32 v28, 1.0, v28\n"
"v_add_f32 v29, 1.0, v29\n"
"v_add_f32 v30, 1.0, v30\n"
"v_add_f32 v31, 1.0, v31\n"
"v_accvgpr_write_b32 a0, v16\n"
"v_accvgpr_write_b32 a1, v17\n"
"v_accvgpr_write_b32 a2, v18\n"
"v_accvgpr_write_b32 a3, v19\n"
"v_accvgpr_write_b32 a4, v20\n"
"v_accvgpr_write_b32 a5, v21\n"
"v_accvgpr_write_b32 a6, v22\n"
"v_accvgpr_write_b32 a7, v23\n"
"v_accvgpr_write_b32 a8, v24\n"
"v_accvgpr_write_b32 a9, v25\n"
"v_accvgpr_write_b32 a10, v26\n"
"v_accvgpr_write_b32 a11, v27\n"
"v_accvgpr_write_b32 a12, v28\n"
"v_accvgpr_write_b32 a13, v29\n"
"v_accvgpr_write_b32 a14, v30\n"
"v_accvgpr_write_b32 a15, v31\n"
"s_bitcmp1_b32 %26, 0\n"
"s_cbranch_scc1 L_z\n"
"s_cmp_lt_u32 %22, 524288\n"
"s_cselect_b32 s38, 8192000, 0\n"
"s_add_u32 s70, %22, 6553600\n"
"s_sub_u32 s67, s72, s64\n"
"s_add_u32 s67, s67, 98304\n"
"v_add_u32 v40, s67, v9\n"
"v_add_u32 v41, s67, v15\n"
"s_sleep 1\n"
"ds_read_b64_tr_b16 a[160:161], v40 offset:40960\n"
"ds_read_b64_tr_b16 a[162:163], v40 offset:41472\n"
"ds_read_b64_tr_b16 a[164:165], v40 offset:40968\n"
"ds_read_b64_tr_b16 a[166:167], v40 offset:41480\n"
"ds_read_b64_tr_b16 a[168:169], v41 offset:40960\n"
"ds_read_b64_tr_b16 a[170:171], v41 offset:41472\n"
"ds_read_b64_tr_b16 a[172:173], v41 offset:40968\n"
"ds_read_b64_tr_b16 a[174:175], v41 offset:41480\n"
"ds_read_b64_tr_b16 a[176:177], v40 offset:0\n"
"ds_read_b64_tr_b16 a[178:179], v40 offset:512\n"
"ds_read_b64_tr_b16 a[180:181], v40 offset:8\n"
"ds_read_b64_tr_b16 a[182:183], v40 offset:520\n"
"ds_read_b64_tr_b16 a[184:185], v41 offset:0\n"
"ds_read_b64_tr_b16 a[186:187], v41 offset:512\n"
"ds_read_b64_tr_b16 a[188:189], v41 offset:8\n"
"ds_read_b64_tr_b16 a[190:191], v41 offset:520\n"
"s_waitcnt lgkmcnt(14)\n"
"buffer_store_dwordx4 a[160:163], v0, s[36:39], %22 offen offset:0 sc1\n"
"s_waitcnt lgkmcnt(12)\n"
"buffer_store_dwordx4 a[164:167], v0, s[36:39], %22 offen offset:2048 sc1\n"
"s_waitcnt lgkmcnt(10)\n"
"buffer_store_dwordx4 a[168:171], v1, s[36:39], %22 offen offset:0 sc1\n"
"s_waitcnt lgkmcnt(8)\n"
"buffer_store_dwordx4 a[172:175], v1, s[36:39], %22 offen offset:2048 sc1\n"
"s_waitcnt lgkmcnt(6)\n"
"buffer_store_dwordx4 a[176:179], v0, s[36:39], s70 offen offset:0 sc1\n"
"s_waitcnt lgkmcnt(4)\n"
"buffer_store_dwordx4 a[180:183], v0, s[36:39], s70 offen offset:2048 sc1\n"
"s_waitcnt lgkmcnt(2)\n"
"buffer_store_dwordx4 a[184:187], v1, s[36:39], s70 offen offset:0 sc1\n"
"s_waitcnt lgkmcnt(0)\n"
"buffer_store_dwordx4 a[188:191], v1, s[36:39], s70 offen offset:2048 sc1\n"
"s_sleep 10\n"
"L_z:\n"
"s_nop 1\n"
"s_waitcnt lgkmcnt(8)\n"
"v_mfma_f32_16x16x32_bf16 v[32:35], a[32:35], a[96:99], v[16:19]\n"
"s_add_u32 s70, s75, 393216\n"
"v_mfma_f32_16x16x32_bf16 v[32:35], a[36:39], a[100:103], v[32:35]\n"
"s_add_u32 s70, s70, s65\n"
"v_mfma_f32_16x16x32_bf16 v[60:63], a[44:47], a[100:103], v[20:23]\n"
"buffer_load_dword a252, v14, s[52:55], s70 offen\n"
"v_mfma_f32_16x16x32_bf16 v[60:63], a[40:43], a[96:99], v[60:63]\n"
"s_add_u32 m0, s64, 32768\n"
"v_mfma_f32_16x16x32_bf16 v[64:67], a[40:43], a[104:107], v[20:23]\n"
"s_add_u32 s67, s75, 131072\n"
"v_mfma_f32_16x16x32_bf16 v[64:67], a[44:47], a[108:111], v[64:67]\n"
"buffer_load_dwordx4 v11, s[52:55], s67 offen offset:0  lds\n"
"v_mfma_f32_16x16x32_bf16 v[36:39], a[36:39], a[108:111], v[16:19]\n"
"s_add_u32 s67, s75, 146432\n"
"v_mfma_f32_16x16x32_bf16 v[36:39], a[32:35], a[104:107], v[36:39]\n"
"v_mfma_f32_16x16x32_bf16 v[40:43], a[32:35], a[112:115], v[16:19]\n"
"s_waitcnt lgkmcnt(0)\n"
"buffer_load_dwordx4 v11, s[52:55], s67 offen offset:1024  lds\n"
"v_mfma_f32_16x16x32_bf16 v[40:43], a[36:39], a[116:119], v[40:43]\n"
"v_mfma_f32_16x16x32_bf16 v[68:71], a[44:47], a[116:119], v[20:23]\n"
"s_add_u32 s67, s75, 161792\n"
"v_mfma_f32_16x16x32_bf16 v[68:71], a[40:43], a[112:115], v[68:71]\n"
"v_mfma_f32_16x16x32_bf16 v[72:75], a[40:43], a[120:123], v[20:23]\n"
"buffer_load_dwordx4 v11, s[52:55], s67 offen offset:2048  lds\n"
"v_mfma_f32_16x16x32_bf16 v[72:75], a[44:47], a[124:127], v[72:75]\n"
"v_mfma_f32_16x16x32_bf16 v[44:47], a[36:39], a[124:127], v[16:19]\n"
"s_add_u32 s67, s75, 177152\n"
"v_mfma_f32_16x16x32_bf16 v[44:47], a[32:35], a[120:123], v[44:47]\n"
"v_mfma_f32_16x16x32_bf16 v[48:51], a[32:35], a[128:131], v[16:19]\n"
"buffer_load_dwordx4 v11, s[52:55], s67 offen offset:3072  lds\n"
"v_mfma_f32_16x16x32_bf16 v[48:51], a[36:39], a[132:135], v[48:51]\n"
"v_mfma_f32_16x16x32_bf16 v[76:79], a[44:47], a[132:135], v[20:23]\n"
"s_add_u32 m0, s64, 36864\n"
"v_mfma_f32_16x16x32_bf16 v[76:79], a[40:43], a[128:131], v[76:79]\n"
"v_mfma_f32_16x16x32_bf16 v[80:83], a[40:43], a[136:139], v[20:23]\n"
"s_add_u32 s67, s75, 196608\n"
"v_mfma_f32_16x16x32_bf16 v[80:83], a[44:47], a[140:143], v[80:83]\n"
"v_mfma_f32_16x16x32_bf16 v[52:55], a[36:39], a[140:143], v[16:19]\n"
"buffer_load_dwordx4 v11, s[52:55], s67 offen offset:0  lds\n"
"v_mfma_f32_16x16x32_bf16 v[52:55], a[32:35], a[136:139], v[52:55]\n"
"v_mfma_f32_16x16x32_bf16 v[56:59], a[32:35], a[144:147], v[16:19]\n"
"s_mov_b64 exec, 0\n"
"s_cmp_eq_u32 %16, 0\n"
"s_cbranch_scc1 L_st5091\n"
"s_cmp_eq_u32 %16, 1\n"
"s_cbranch_scc1 L_st5703\n"
"s_cmp_eq_u32 %16, 2\n"
"s_cbranch_scc1 L_st6239\n"
"s_branch L_st6793\n"
"L_retB:\n"
"s_mov_b64 exec, -1\n"
"s_nop 3\n"
"s_add_u32 s67, s75, 211968\n"
"v_mfma_f32_16x16x32_bf16 v[56:59], a[36:39], a[148:151], v[56:59]\n"
"v_mfma_f32_16x16x32_bf16 v[84:87], a[44:47], a[148:151], v[20:23]\n"
"buffer_load_dwordx4 v11, s[52:55], s67 offen offset:1024  lds\n"
"v_mfma_f32_16x16x32_bf16 v[84:87], a[40:43], a[144:147], v[84:87]\n"
"v_mfma_f32_16x16x32_bf16 a[4:7], a[40:43], a[152:155], a[4:7]\n"
"s_add_u32 s67, s75, 227328\n"
"v_mfma_f32_16x16x32_bf16 a[4:7], a[44:47], a[156:159], a[4:7]\n"
"v_mfma_f32_16x16x32_bf16 a[0:3], a[36:39], a[156:159], a[0:3]\n"
"buffer_load_dwordx4 v11, s[52:55], s67 offen offset:2048  lds\n"
"v_mfma_f32_16x16x32_bf16 a[0:3], a[32:35], a[152:155], a[0:3]\n"
"v_mfma_f32_16x16x32_bf16 v[88:91], a[48:51], a[96:99], v[24:27]\n"
"s_add_u32 s67, s75, 242688\n"
"v_mfma_f32_16x16x32_bf16 v[88:91], a[52:55], a[100:103], v[88:91]\n"
"v_mfma_f32_16x16x32_bf16 v[116:119], a[60:63], a[100:103], v[28:31]\n"
"v_mfma_f32_16x16x32_bf16 v[116:119], a[56:59], a[96:99], v[116:119]\n"
"buffer_load_dwordx4 v11, s[52:55], s67 offen offset:3072  lds\n"
"v_mfma_f32_16x16x32_bf16 v[120:123], a[56:59], a[104:107], v[28:31]\n"
"s_add_u32 m0, s64, 0\n"
"v_mfma_f32_16x16x32_bf16 v[120:123], a[60:63], a[108:111], v[120:123]\n"
"v_mfma_f32_16x16x32_bf16 v[92:95], a[52:55], a[108:111], v[24:27]\n"
"s_add_u32 s67, s75, 131200\n"
"v_mfma_f32_16x16x32_bf16 v[92:95], a[48:51], a[104:107], v[92:95]\n"
"v_mfma_f32_16x16x32_bf16 v[96:99], a[48:51], a[112:115], v[24:27]\n"
"buffer_load_dwordx4 v11, s[52:55], s67 offen offset:0  lds\n"
"v_mfma_f32_16x16x32_bf16 v[96:99], a[52:55], a[116:119], v[96:99]\n"
"v_mfma_f32_16x16x32_bf16 v[124:127], a[60:63], a[116:119], v[28:31]\n"
"s_add_u32 s67, s75, 146560\n"
"v_mfma_f32_16x16x32_bf16 v[124:127], a[56:59], a[112:115], v[124:127]\n"
"buffer_load_dwordx4 v11, s[52:55], s67 offen offset:1024  lds\n"
"v_mfma_f32_16x16x32_bf16 v[128:131], a[56:59], a[120:123], v[28:31]\n"
"s_waitcnt vmcnt(16)\n"
"ds_read_b64_tr_b16 a[64:65], v9 offset:8192\n"
"v_mfma_f32_16x16x32_bf16 v[128:131], a[60:63], a[124:127], v[128:131]\n"
"s_add_u32 s67, s75, 161920\n"
"ds_read_b64_tr_b16 a[66:67], v9 offset:8704\n"
"v_mfma_f32_16x16x32_bf16 v[100:103], a[52:55], a[124:127], v[24:27]\n"
"ds_read_b64_tr_b16 a[68:69], v9 offset:12288\n"
"v_mfma_f32_16x16x32_bf16 v[100:103], a[48:51], a[120:123], v[100:103]\n"
"ds_read_b64_tr_b16 a[70:71], v9 offset:12800\n"
"v_mfma_f32_16x16x32_bf16 v[104:107], a[48:51], a[128:131], v[24:27]\n"
"ds_read_b64_tr_b16 a[72:73], v9 offset:8200\n"
"v_mfma_f32_16x16x32_bf16 v[104:107], a[52:55], a[132:135], v[104:107]\n"
"ds_read_b64_tr_b16 a[74:75], v9 offset:8712\n"
"buffer_load_dwordx4 v11, s[52:55], s67 offen offset:2048  lds\n"
"v_mfma_f32_16x16x32_bf16 v[132:135], a[60:63], a[132:135], v[28:31]\n"
"ds_read_b64_tr_b16 a[76:77], v9 offset:12296\n"
"v_mfma_f32_16x16x32_bf16 v[132:135], a[56:59], a[128:131], v[132:135]\n"
"s_add_u32 s67, s75, 177280\n"
"ds_read_b64_tr_b16 a[78:79], v9 offset:12808\n"
"v_mfma_f32_16x16x32_bf16 v[136:139], a[56:59], a[136:139], v[28:31]\n"
"buffer_load_dwordx4 v11, s[52:55], s67 offen offset:3072  lds\n"
"v_mfma_f32_16x16x32_bf16 v[136:139], a[60:63], a[140:143], v[136:139]\n"
"ds_read_b64_tr_b16 a[80:81], v15 offset:8192\n"
"v_mfma_f32_16x16x32_bf16 v[108:111], a[52:55], a[140:143], v[24:27]\n"
"s_add_u32 m0, s64, 4096\n"
"ds_read_b64_tr_b16 a[82:83], v15 offset:8704\n"
"v_mfma_f32_16x16x32_bf16 v[108:111], a[48:51], a[136:139], v[108:111]\n"
"ds_read_b64_tr_b16 a[84:85], v15 offset:12288\n"
"v_mfma_f32_16x16x32_bf16 v[112:115], a[48:51], a[144:147], v[24:27]\n"
"s_add_u32 s67, s75, 196736\n"
"ds_read_b64_tr_b16 a[86:87], v15 offset:12800\n"
"v_mfma_f32_16x16x32_bf16 v[112:115], a[52:55], a[148:151], v[112:115]\n"
"ds_read_b64_tr_b16 a[88:89], v15 offset:8200\n"
"v_mfma_f32_16x16x32_bf16 v[140:143], a[60:63], a[148:151], v[28:31]\n"
"ds_read_b64_tr_b16 a[90:91], v15 offset:8712\n"
"buffer_load_dwordx4 v11, s[52:55], s67 offen offset:0  lds\n"
"v_mfma_f32_16x16x32_bf16 v[140:143], a[56:59], a[144:147], v[140:143]\n"
"ds_read_b64_tr_b16 a[92:93], v15 offset:12296\n"
"v_mfma_f32_16x16x32_bf16 a[12:15], a[56:59], a[152:155], a[12:15]\n"
"s_add_u32 s67, s75, 212096\n"
"ds_read_b64_tr_b16 a[94:95], v15 offset:12808\n"
"v_mfma_f32_16x16x32_bf16 a[12:15], a[60:63], a[156:159], a[12:15]\n"
"v_mfma_f32_16x16x32_bf16 a[8:11], a[52:55], a[156:159], a[8:11]\n"
"buffer_load_dwordx4 v11, s[52:55], s67 offen offset:1024  lds\n"
"v_mfma_f32_16x16x32_bf16 a[8:11], a[48:51], a[152:155], a[8:11]\n"
"s_waitcnt lgkmcnt(8)\n"
"v_mfma_f32_16x16x32_bf16 v[144:147], a[64:67], a[96:99], 1.0\n"
"s_add_u32 s67, s75, 227456\n"
"v_mfma_f32_16x16x32_bf16 v[144:147], a[68:71], a[100:103], v[144:147]\n"
"v_mfma_f32_16x16x32_bf16 v[172:175], a[76:79], a[100:103], 1.0\n"
"s_waitcnt vmcnt(19) lgkmcnt(0)\n"
"v_readfirstlane_b32 s78, v252\n"
"v_readfirstlane_b32 s79, v253\n"
"v_mfma_f32_16x16x32_bf16 v[172:175], a[72:75], a[96:99], v[172:175]\n"
"buffer_load_dwordx4 v11, s[52:55], s67 offen offset:2048  lds\n"
"v_mfma_f32_16x16x32_bf16 v[176:179], a[72:75], a[104:107], 1.0\n"
"v_mfma_f32_16x16x32_bf16 v[176:179], a[76:79], a[108:111], v[176:179]\n"
"s_add_u32 s67, s75, 242816\n"
"v_mfma_f32_16x16x32_bf16 v[148:151], a[68:71], a[108:111], 1.0\n"
"s_add_u32 s78, s78, 16\n"
"v_mfma_f32_16x16x32_bf16 v[148:151], a[64:67], a[104:107], v[148:151]\n"
"s_add_u32 s79, s79, 16\n"
"v_mfma_f32_16x16x32_bf16 v[152:155], a[64:67], a[112:115], 1.0\n"
"s_sub_u32 s66, 40960, s73\n"
"v_mfma_f32_16x16x32_bf16 v[152:155], a[68:71], a[116:119], v[152:155]\n"
"buffer_load_dwordx4 v11, s[52:55], s67 offen offset:3072  lds\n"
"v_mfma_f32_16x16x32_bf16 v[180:183], a[76:79], a[116:119], 1.0\n"
"s_add_u32 m0, s72, 98304\n"
"v_add_u32 v12, s66, v7\n"
"v_mfma_f32_16x16x32_bf16 v[180:183], a[72:75], a[112:115], v[180:183]\n"
"v_mfma_f32_16x16x32_bf16 v[184:187], a[72:75], a[120:123], 1.0\n"
"s_add_u32 s67, s60, 256\n"
"v_add_u32 v13, s66, v8\n"
"v_mfma_f32_16x16x32_bf16 v[184:187], a[76:79], a[124:127], v[184:187]\n"
"buffer_load_dwordx4 v6, s[48:51], s67 offen offset:0  lds\n"
"v_mfma_f32_16x16x32_bf16 v[156:159], a[68:71], a[124:127], 1.0\n"
"L_st531:\n"
"s_cbranch_execz L_st2677\n"
"v_mfma_f32_16x16x32_bf16 v[156:159], a[64:67], a[120:123], v[156:159]\n"
"s_add_u32 s67, s60, 15616\n"
"v_mfma_f32_16x16x32_bf16 v[160:163], a[64:67], a[128:131], 1.0\n"
"v_mfma_f32_16x16x32_bf16 v[160:163], a[68:71], a[132:135], v[160:163]\n"
"buffer_load_dwordx4 v6, s[48:51], s67 offen offset:1024  lds\n"
"v_mfma_f32_16x16x32_bf16 v[188:191], a[76:79], a[132:135], 1.0\n"
"v_mfma_f32_16x16x32_bf16 v[188:191], a[72:75], a[128:131], v[188:191]\n"
"s_add_u32 s67, s60, 30976\n"
"v_mfma_f32_16x16x32_bf16 v[192:195], a[72:75], a[136:139], 1.0\n"
"v_mfma_f32_16x16x32_bf16 v[192:195], a[76:79], a[140:143], v[192:195]\n"
"buffer_load_dwordx4 v6, s[48:51], s67 offen offset:2048  lds\n"
"v_mfma_f32_16x16x32_bf16 v[164:167], a[68:71], a[140:143], 1.0\n"
"v_mfma_f32_16x16x32_bf16 v[164:167], a[64:67], a[136:139], v[164:167]\n"
"s_add_u32 s67, s60, 46336\n"
"v_mfma_f32_16x16x32_bf16 v[168:171], a[64:67], a[144:147], 1.0\n"
"v_mfma_f32_16x16x32_bf16 v[168:171], a[68:71], a[148:151], v[168:171]\n"
"v_mfma_f32_16x16x32_bf16 v[196:199], a[76:79], a[148:151], 1.0\n"
"buffer_load_dwordx4 v6, s[48:51], s67 offen offset:3072  lds\n"
"v_mfma_f32_16x16x32_bf16 v[196:199], a[72:75], a[144:147], v[196:199]\n"
"v_mfma_f32_16x16x32_bf16 a[20:23], a[72:75], a[152:155], 1.0\n"
"v_mfma_f32_16x16x32_bf16 a[20:23], a[76:79], a[156:159], a[20:23]\n"
"v_mfma_f32_16x16x32_bf16 a[16:19], a[68:71], a[156:159], 1.0\n"
"v_mfma_f32_16x16x32_bf16 a[16:19], a[64:67], a[152:155], a[16:19]\n"
"s_waitcnt vmcnt(21)\n"
"s_barrier\n"
"v_mfma_f32_16x16x32_bf16 v[200:203], a[80:83], a[96:99], 1.0\n"
"s_waitcnt vmcnt(12)\n"
"ds_read_b64_tr_b16 a[32:33], v9 offset:32768\n"
"v_mfma_f32_16x16x32_bf16 v[200:203], a[84:87], a[100:103], v[200:203]\n"
"s_add_u32 m0, s64, 8192\n"
"ds_read_b64_tr_b16 a[34:35], v9 offset:33280\n"
"v_mfma_f32_16x16x32_bf16 v[228:231], a[92:95], a[100:103], 1.0\n"
"ds_read_b128 a[100:103], v13 offset:16384\n"
"v_mfma_f32_16x16x32_bf16 v[228:231], a[88:91], a[96:99], v[228:231]\n"
"ds_read_b128 a[96:99], v12 offset:16384\n"
"v_mfma_f32_16x16x32_bf16 v[232:235], a[88:91], a[104:107], 1.0\n"
"ds_read_b64_tr_b16 a[36:37], v9 offset:36864\n"
"v_mfma_f32_16x16x32_bf16 v[232:235], a[92:95], a[108:111], v[232:235]\n"
"ds_read_b64_tr_b16 a[38:39], v9 offset:37376\n"
"v_mfma_f32_16x16x32_bf16 v[204:207], a[84:87], a[108:111], 1.0\n"
"ds_read_b128 a[108:111], v13 offset:18432\n"
"v_mfma_f32_16x16x32_bf16 v[204:207], a[80:83], a[104:107], v[204:207]\n"
"ds_read_b128 a[104:107], v12 offset:18432\n"
"ds_read_b64_tr_b16 a[40:41], v9 offset:32776\n"
"v_mfma_f32_16x16x32_bf16 v[208:211], a[80:83], a[112:115], 1.0\n"
"ds_read_b64_tr_b16 a[42:43], v9 offset:33288\n"
"v_mfma_f32_16x16x32_bf16 v[208:211], a[84:87], a[116:119], v[208:211]\n"
"ds_read_b64_tr_b16 a[44:45], v9 offset:36872\n"
"v_mfma_f32_16x16x32_bf16 v[236:239], a[92:95], a[116:119], 1.0\n"
"ds_read_b128 a[116:119], v13 offset:20480\n"
"v_mfma_f32_16x16x32_bf16 v[236:239], a[88:91], a[112:115], v[236:239]\n"
"ds_read_b128 a[112:115], v12 offset:20480\n"
"v_mfma_f32_16x16x32_bf16 v[240:243], a[88:91], a[120:123], 1.0\n"
"ds_read_b64_tr_b16 a[46:47], v9 offset:37384\n"
"v_mfma_f32_16x16x32_bf16 v[240:243], a[92:95], a[124:127], v[240:243]\n"
"ds_read_b64_tr_b16 a[48:49], v15 offset:32768\n"
"v_mfma_f32_16x16x32_bf16 v[212:215], a[84:87], a[124:127], 1.0\n"
"s_add_u32 s67, s75, 262144\n"
"ds_read_b128 a[124:127], v13 offset:22528\n"
"v_mfma_f32_16x16x32_bf16 v[212:215], a[80:83], a[120:123], v[212:215]\n"
"ds_read_b128 a[120:123], v12 offset:22528\n"
"v_mfma_f32_16x16x32_bf16 v[216:219], a[80:83], a[128:131], 1.0\n"
"ds_read_b64_tr_b16 a[50:51], v15 offset:33280\n"
"v_mfma_f32_16x16x32_bf16 v[216:219], a[84:87], a[132:135], v[216:219]\n"
"ds_read_b64_tr_b16 a[52:53], v15 offset:36864\n"
"v_mfma_f32_16x16x32_bf16 v[244:247], a[92:95], a[132:135], 1.0\n"
"ds_read_b128 a[132:135], v13 offset:24576\n"
"v_mfma_f32_16x16x32_bf16 v[244:247], a[88:91], a[128:131], v[244:247]\n"
"ds_read_b128 a[128:131], v12 offset:24576\n"
"v_mfma_f32_16x16x32_bf16 v[248:251], a[88:91], a[136:139], 1.0\n"
"ds_read_b64_tr_b16 a[54:55], v15 offset:37376\n"
"v_mfma_f32_16x16x32_bf16 v[248:251], a[92:95], a[140:143], v[248:251]\n"
"ds_read_b64_tr_b16 a[56:57], v15 offset:32776\n"
"v_mfma_f32_16x16x32_bf16 v[220:223], a[84:87], a[140:143], 1.0\n"
"ds_read_b128 a[140:143], v13 offset:26624\n"
"v_mfma_f32_16x16x32_bf16 v[220:223], a[80:83], a[136:139], v[220:223]\n"
"ds_read_b128 a[136:139], v12 offset:26624\n"
"v_mfma_f32_16x16x32_bf16 v[224:227], a[80:83], a[144:147], 1.0\n"
"ds_read_b64_tr_b16 a[58:59], v15 offset:33288\n"
"v_mfma_f32_16x16x32_bf16 v[224:227], a[84:87], a[148:151], v[224:227]\n"
"ds_read_b64_tr_b16 a[60:61], v15 offset:36872\n"
"v_mfma_f32_16x16x32_bf16 v[252:255], a[92:95], a[148:151], 1.0\n"
"ds_read_b128 a[148:151], v13 offset:28672\n"
"v_mfma_f32_16x16x32_bf16 v[252:255], a[88:91], a[144:147], v[252:255]\n"
"ds_read_b128 a[144:147], v12 offset:28672\n"
"ds_read_b64_tr_b16 a[62:63], v15 offset:37384\n"
"v_mfma_f32_16x16x32_bf16 a[28:31], a[88:91], a[152:155], 1.0\n"
"v_mfma_f32_16x16x32_bf16 a[28:31], a[92:95], a[156:159], a[28:31]\n"
"v_mfma_f32_16x16x32_bf16 a[24:27], a[84:87], a[156:159], 1.0\n"
"ds_read_b128 a[156:159], v13 offset:30720\n"
"v_mfma_f32_16x16x32_bf16 a[24:27], a[80:83], a[152:155], a[24:27]\n"
"s_waitcnt lgkmcnt(15)\n"
"ds_read_b128 a[152:155], v12 offset:30720\n"
"v_mfma_f32_16x16x32_bf16 v[32:35], a[32:35], a[96:99], v[32:35]\n"
"buffer_load_dwordx4 v11, s[52:55], s67 offen offset:0  lds\n"
"v_mfma_f32_16x16x32_bf16 v[32:35], a[36:39], a[100:103], v[32:35]\n"
"s_waitcnt lgkmcnt(2)\n"
"v_mfma_f32_16x16x32_bf16 v[60:63], a[44:47], a[100:103], v[60:63]\n"
"s_add_u32 s67, s75, 277504\n"
"v_mfma_f32_16x16x32_bf16 v[60:63], a[40:43], a[96:99], v[60:63]\n"
"v_mfma_f32_16x16x32_bf16 v[64:67], a[40:43], a[104:107], v[64:67]\n"
"v_mfma_f32_16x16x32_bf16 v[64:67], a[44:47], a[108:111], v[64:67]\n"
"buffer_load_dwordx4 v11, s[52:55], s67 offen offset:1024  lds\n"
"v_mfma_f32_16x16x32_bf16 v[36:39], a[36:39], a[108:111], v[36:39]\n"
"v_mfma_f32_16x16x32_bf16 v[36:39], a[32:35], a[104:107], v[36:39]\n"
"s_add_u32 s67, s75, 292864\n"
"v_mfma_f32_16x16x32_bf16 v[40:43], a[32:35], a[112:115], v[40:43]\n"
"v_mfma_f32_16x16x32_bf16 v[40:43], a[36:39], a[116:119], v[40:43]\n"
"v_mfma_f32_16x16x32_bf16 v[68:71], a[44:47], a[116:119], v[68:71]\n"
"buffer_load_dwordx4 v11, s[52:55], s67 offen offset:2048  lds\n"
"v_mfma_f32_16x16x32_bf16 v[68:71], a[40:43], a[112:115], v[68:71]\n"
"v_mfma_f32_16x16x32_bf16 v[72:75], a[40:43], a[120:123], v[72:75]\n"
"s_add_u32 s67, s75, 308224\n"
"v_mfma_f32_16x16x32_bf16 v[72:75], a[44:47], a[124:127], v[72:75]\n"
"v_mfma_f32_16x16x32_bf16 v[44:47], a[36:39], a[124:127], v[44:47]\n"
"v_mov_b32 v16, -1\n"
"v_mfma_f32_16x16x32_bf16 v[44:47], a[32:35], a[120:123], v[44:47]\n"
"buffer_load_dwordx4 v11, s[52:55], s67 offen offset:3072  lds\n"
"v_mfma_f32_16x16x32_bf16 v[48:51], a[32:35], a[128:131], v[48:51]\n"
"s_add_u32 m0, s64, 12288\n"
"v_mfma_f32_16x16x32_bf16 v[48:51], a[36:39], a[132:135], v[48:51]\n"
"v_mov_b32 v17, -1\n"
"v_mfma_f32_16x16x32_bf16 v[76:79], a[44:47], a[132:135], v[76:79]\n"
"s_waitcnt vmcnt(8)\n"
"ds_read_b64_tr_b16 a[64:65], v9 offset:0\n"
"v_mfma_f32_16x16x32_bf16 v[76:79], a[40:43], a[128:131], v[76:79]\n"
"s_add_u32 s67, s75, 327680\n"
"ds_read_b64_tr_b16 a[66:67], v9 offset:512\n"
"v_mfma_f32_16x16x32_bf16 v[80:83], a[40:43], a[136:139], v[80:83]\n"
"ds_read_b64_tr_b16 a[68:69], v9 offset:4096\n"
"buffer_load_dwordx4 v11, s[52:55], s67 offen offset:0  lds\n"
"v_mfma_f32_16x16x32_bf16 v[80:83], a[44:47], a[140:143], v[80:83]\n"
"ds_read_b64_tr_b16 a[70:71], v9 offset:4608\n"
"v_mfma_f32_16x16x32_bf16 v[52:55], a[36:39], a[140:143], v[52:55]\n"
"s_add_u32 s67, s75, 343040\n"
"ds_read_b64_tr_b16 a[72:73], v9 offset:8\n"
"v_mfma_f32_16x16x32_bf16 v[52:55], a[32:35], a[136:139], v[52:55]\n"
"s_waitcnt lgkmcnt(5)\n"
"ds_read_b64_tr_b16 a[74:75], v9 offset:520\n"
"v_mfma_f32_16x16x32_bf16 v[56:59], a[32:35], a[144:147], v[56:59]\n"
"ds_read_b64_tr_b16 a[76:77], v9 offset:4104\n"
"buffer_load_dwordx4 v11, s[52:55], s67 offen offset:1024  lds\n"
"v_mfma_f32_16x16x32_bf16 v[56:59], a[36:39], a[148:151], v[56:59]\n"
"s_add_u32 s67, s75, 358400\n"
"ds_read_b64_tr_b16 a[78:79], v9 offset:4616\n"
"v_mfma_f32_16x16x32_bf16 v[84:87], a[44:47], a[148:151], v[84:87]\n"
"s_add_u32 s69, %11, 8192\n"
"v_lshrrev_b32 v18, 1, v0\n"
"v_mfma_f32_16x16x32_bf16 v[84:87], a[40:43], a[144:147], v[84:87]\n"
"ds_read_b64_tr_b16 a[80:81], v15 offset:0\n"
"v_mfma_f32_16x16x32_bf16 a[4:7], a[40:43], a[152:155], a[4:7]\n"
"ds_read_b64_tr_b16 a[82:83], v15 offset:512\n"
"buffer_load_dwordx4 v11, s[52:55], s67 offen offset:2048  lds\n"
"v_mfma_f32_16x16x32_bf16 a[4:7], a[44:47], a[156:159], a[4:7]\n"
"ds_read_b64_tr_b16 a[84:85], v15 offset:4096\n"
"v_mfma_f32_16x16x32_bf16 a[0:3], a[36:39], a[156:159], a[0:3]\n"
"s_add_u32 s67, s75, 373760\n"
"ds_read_b64_tr_b16 a[86:87], v15 offset:4608\n"
"v_mfma_f32_16x16x32_bf16 a[0:3], a[32:35], a[152:155], a[0:3]\n"
"ds_read_b64_tr_b16 a[88:89], v15 offset:8\n"
"v_mfma_f32_16x16x32_bf16 v[88:91], a[48:51], a[96:99], v[88:91]\n"
"ds_read_b64_tr_b16 a[90:91], v15 offset:520\n"
"v_mfma_f32_16x16x32_bf16 v[88:91], a[52:55], a[100:103], v[88:91]\n"
"ds_read_b64_tr_b16 a[92:93], v15 offset:4104\n"
"buffer_load_dwordx4 v11, s[52:55], s67 offen offset:3072  lds\n"
"v_mfma_f32_16x16x32_bf16 v[116:119], a[60:63], a[100:103], v[116:119]\n"
"ds_read_b64_tr_b16 a[94:95], v15 offset:4616\n"
"v_mfma_f32_16x16x32_bf16 v[116:119], a[56:59], a[96:99], v[116:119]\n"
"s_add_u32 m0, s64, 0\n"
"v_mfma_f32_16x16x32_bf16 v[120:123], a[56:59], a[104:107], v[120:123]\n"
"v_mfma_f32_16x16x32_bf16 v[120:123], a[60:63], a[108:111], v[120:123]\n"
"v_mfma_f32_16x16x32_bf16 v[92:95], a[52:55], a[108:111], v[92:95]\n"
"s_add_u32 s67, s75, 262272\n"
"v_mfma_f32_16x16x32_bf16 v[92:95], a[48:51], a[104:107], v[92:95]\n"
"v_mfma_f32_16x16x32_bf16 v[96:99], a[48:51], a[112:115], v[96:99]\n"
"s_waitcnt lgkmcnt(0)\n"
"v_mfma_f32_16x16x32_bf16 v[96:99], a[52:55], a[116:119], v[96:99]\n"
"buffer_load_dwordx4 v11, s[52:55], s67 offen offset:0  lds\n"
"v_mfma_f32_16x16x32_bf16 v[124:127], a[60:63], a[116:119], v[124:127]\n"
"v_mfma_f32_16x16x32_bf16 v[124:127], a[56:59], a[112:115], v[124:127]\n"
"s_add_u32 s67, s75, 277632\n"
"v_mfma_f32_16x16x32_bf16 v[128:131], a[56:59], a[120:123], v[128:131]\n"
"v_mfma_f32_16x16x32_bf16 v[128:131], a[60:63], a[124:127], v[128:131]\n"
"v_mfma_f32_16x16x32_bf16 v[100:103], a[52:55], a[124:127], v[100:103]\n"
"buffer_load_dwordx4 v11, s[52:55], s67 offen offset:1024  lds\n"
"v_mfma_f32_16x16x32_bf16 v[100:103], a[48:51], a[120:123], v[100:103]\n"
"v_mfma_f32_16x16x32_bf16 v[104:107], a[48:51], a[128:131], v[104:107]\n"
"s_add_u32 s67, s75, 292992\n"
"v_mfma_f32_16x16x32_bf16 v[104:107], a[52:55], a[132:135], v[104:107]\n"
"v_mfma_f32_16x16x32_bf16 v[132:135], a[60:63], a[132:135], v[132:135]\n"
"buffer_load_dwordx4 v11, s[52:55], s67 offen offset:2048  lds\n"
"v_mfma_f32_16x16x32_bf16 v[132:135], a[56:59], a[128:131], v[132:135]\n"
"v_mfma_f32_16x16x32_bf16 v[136:139], a[56:59], a[136:139], v[136:139]\n"
"s_add_u32 s67, s75, 308352\n"
"v_mfma_f32_16x16x32_bf16 v[136:139], a[60:63], a[140:143], v[136:139]\n"
"v_mfma_f32_16x16x32_bf16 v[108:111], a[52:55], a[140:143], v[108:111]\n"
"v_mfma_f32_16x16x32_bf16 v[108:111], a[48:51], a[136:139], v[108:111]\n"
"buffer_load_dwordx4 v11, s[52:55], s67 offen offset:3072  lds\n"
"v_mfma_f32_16x16x32_bf16 v[112:115], a[48:51], a[144:147], v[112:115]\n"
"v_mfma_f32_16x16x32_bf16 v[112:115], a[52:55], a[148:151], v[112:115]\n"
"s_add_u32 m0, s64, 4096\n"
"v_mfma_f32_16x16x32_bf16 v[140:143], a[60:63], a[148:151], v[140:143]\n"
"v_mfma_f32_16x16x32_bf16 v[140:143], a[56:59], a[144:147], v[140:143]\n"
"s_add_u32 s67, s75, 327808\n"
"v_mfma_f32_16x16x32_bf16 a[12:15], a[56:59], a[152:155], a[12:15]\n"
"v_mfma_f32_16x16x32_bf16 a[12:15], a[60:63], a[156:159], a[12:15]\n"
"buffer_load_dwordx4 v11, s[52:55], s67 offen offset:0  lds\n"
"v_mfma_f32_16x16x32_bf16 a[8:11], a[52:55], a[156:159], a[8:11]\n"
"v_mfma_f32_16x16x32_bf16 a[8:11], a[48:51], a[152:155], a[8:11]\n"
"s_add_u32 s67, s75, 343168\n"
"v_mfma_f32_16x16x32_bf16 v[144:147], a[64:67], a[96:99], v[144:147]\n"
"v_mfma_f32_16x16x32_bf16 v[144:147], a[68:71], a[100:103], v[144:147]\n"
"buffer_load_dwordx4 v11, s[52:55], s67 offen offset:1024  lds\n"
"v_mfma_f32_16x16x32_bf16 v[172:175], a[76:79], a[100:103], v[172:175]\n"
"v_mfma_f32_16x16x32_bf16 v[172:175], a[72:75], a[96:99], v[172:175]\n"
"s_add_u32 s67, s75, 358528\n"
"v_mfma_f32_16x16x32_bf16 v[176:179], a[72:75], a[104:107], v[176:179]\n"
"v_mfma_f32_16x16x32_bf16 v[176:179], a[76:79], a[108:111], v[176:179]\n"
"buffer_load_dwordx4 v11, s[52:55], s67 offen offset:2048  lds\n"
"v_mfma_f32_16x16x32_bf16 v[148:151], a[68:71], a[108:111], v[148:151]\n"
"v_mfma_f32_16x16x32_bf16 v[148:151], a[64:67], a[104:107], v[148:151]\n"
"s_add_u32 s67, s75, 373888\n"
"v_mfma_f32_16x16x32_bf16 v[152:155], a[64:67], a[112:115], v[152:155]\n"
"s_sub_u32 s66, 81920, s73\n"
"v_mfma_f32_16x16x32_bf16 v[152:155], a[68:71], a[116:119], v[152:155]\n"
"buffer_load_dwordx4 v11, s[52:55], s67 offen offset:3072  lds\n"
"v_mfma_f32_16x16x32_bf16 v[180:183], a[76:79], a[116:119], v[180:183]\n"
"v_add_u32 v12, s66, v7\n"
"v_mfma_f32_16x16x32_bf16 v[180:183], a[72:75], a[112:115], v[180:183]\n"
"s_add_u32 m0, s72, 139264\n"
"v_mfma_f32_16x16x32_bf16 v[184:187], a[72:75], a[120:123], v[184:187]\n"
"v_add_u32 v13, s66, v8\n"
"v_mfma_f32_16x16x32_bf16 v[184:187], a[76:79], a[124:127], v[184:187]\n"
"s_add_u32 s67, s60, 384\n"
"v_mfma_f32_16x16x32_bf16 v[156:159], a[68:71], a[124:127], v[156:159]\n"
"v_mfma_f32_16x16x32_bf16 v[156:159], a[64:67], a[120:123], v[156:159]\n"
"buffer_load_dwordx4 v6, s[48:51], s67 offen offset:0  lds\n"
"v_mfma_f32_16x16x32_bf16 v[160:163], a[64:67], a[128:131], v[160:163]\n"
"v_mfma_f32_16x16x32_bf16 v[160:163], a[68:71], a[132:135], v[160:163]\n"
"s_add_u32 s67, s60, 15744\n"
"v_mfma_f32_16x16x32_bf16 v[188:191], a[76:79], a[132:135], v[188:191]\n"
"v_mfma_f32_16x16x32_bf16 v[188:191], a[72:75], a[128:131], v[188:191]\n"
"buffer_load_dwordx4 v6, s[48:51], s67 offen offset:1024  lds\n"
"v_mfma_f32_16x16x32_bf16 v[192:195], a[72:75], a[136:139], v[192:195]\n"
"v_mfma_f32_16x16x32_bf16 v[192:195], a[76:79], a[140:143], v[192:195]\n"
"s_add_u32 s67, s60, 31104\n"
"v_mfma_f32_16x16x32_bf16 v[164:167], a[68:71], a[140:143], v[164:167]\n"
"v_mfma_f32_16x16x32_bf16 v[164:167], a[64:67], a[136:139], v[164:167]\n"
"buffer_load_dwordx4 v6, s[48:51], s67 offen offset:2048  lds\n"
"v_mfma_f32_16x16x32_bf16 v[168:171], a[64:67], a[144:147], v[168:171]\n"
"v_mfma_f32_16x16x32_bf16 v[168:171], a[68:71], a[148:151], v[168:171]\n"
"s_add_u32 s67, s60, 46464\n"
"v_mfma_f32_16x16x32_bf16 v[196:199], a[76:79], a[148:151], v[196:199]\n"
"v_mfma_f32_16x16x32_bf16 v[196:199], a[72:75], a[144:147], v[196:199]\n"
"v_mfma_f32_16x16x32_bf16 a[20:23], a[72:75], a[152:155], a[20:23]\n"
"buffer_load_dwordx4 v6, s[48:51], s67 offen offset:3072  lds\n"
"v_mfma_f32_16x16x32_bf16 a[20:23], a[76:79], a[156:159], a[20:23]\n"
"s_add_u32 s70, s75, 655360\n"
"v_mfma_f32_16x16x32_bf16 a[16:19], a[68:71], a[156:159], a[16:19]\n"
"s_add_u32 s70, s70, s65\n"
"v_mfma_f32_16x16x32_bf16 a[16:19], a[64:67], a[152:155], a[16:19]\n"
"s_waitcnt vmcnt(20)\n"
"s_barrier\n"
"v_mfma_f32_16x16x32_bf16 v[200:203], a[80:83], a[96:99], v[200:203]\n"
"s_waitcnt vmcnt(12)\n"
"ds_read_b64_tr_b16 a[32:33], v9 offset:8192\n"
"v_mfma_f32_16x16x32_bf16 v[200:203], a[84:87], a[100:103], v[200:203]\n"
"ds_read_b64_tr_b16 a[34:35], v9 offset:8704\n"
"buffer_load_dword a252, v14, s[52:55], s70 offen\n"
"v_mfma_f32_16x16x32_bf16 v[228:231], a[92:95], a[100:103], v[228:231]\n"
"ds_read_b128 a[100:103], v13 offset:16384\n"
"v_mfma_f32_16x16x32_bf16 v[228:231], a[88:91], a[96:99], v[228:231]\n"
"ds_read_b128 a[96:99], v12 offset:16384\n"
"v_mfma_f32_16x16x32_bf16 v[232:235], a[88:91], a[104:107], v[232:235]\n"
"ds_read_b64_tr_b16 a[36:37], v9 offset:12288\n"
"v_mfma_f32_16x16x32_bf16 v[232:235], a[92:95], a[108:111], v[232:235]\n"
"ds_read_b64_tr_b16 a[38:39], v9 offset:12800\n"
"v_mfma_f32_16x16x32_bf16 v[204:207], a[84:87], a[108:111], v[204:207]\n"
"ds_read_b128 a[108:111], v13 offset:18432\n"
"v_mfma_f32_16x16x32_bf16 v[204:207], a[80:83], a[104:107], v[204:207]\n"
"ds_read_b128 a[104:107], v12 offset:18432\n"
"s_add_u32 m0, s64, 8192\n"
"ds_read_b64_tr_b16 a[40:41], v9 offset:8200\n"
"v_mfma_f32_16x16x32_bf16 v[208:211], a[80:83], a[112:115], v[208:211]\n"
"ds_read_b64_tr_b16 a[42:43], v9 offset:8712\n"
"v_mfma_f32_16x16x32_bf16 v[208:211], a[84:87], a[116:119], v[208:211]\n"
"ds_read_b64_tr_b16 a[44:45], v9 offset:12296\n"
"v_mfma_f32_16x16x32_bf16 v[236:239], a[92:95], a[116:119], v[236:239]\n"
"ds_read_b128 a[116:119], v13 offset:20480\n"
"v_mfma_f32_16x16x32_bf16 v[236:239], a[88:91], a[112:115], v[236:239]\n"
"ds_read_b128 a[112:115], v12 offset:20480\n"
"v_mfma_f32_16x16x32_bf16 v[240:243], a[88:91], a[120:123], v[240:243]\n"
"ds_read_b64_tr_b16 a[46:47], v9 offset:12808\n"
"v_mfma_f32_16x16x32_bf16 v[240:243], a[92:95], a[124:127], v[240:243]\n"
"ds_read_b64_tr_b16 a[48:49], v15 offset:8192\n"
"v_mfma_f32_16x16x32_bf16 v[212:215], a[84:87], a[124:127], v[212:215]\n"
"ds_read_b128 a[124:127], v13 offset:22528\n"
"v_mfma_f32_16x16x32_bf16 v[212:215], a[80:83], a[120:123], v[212:215]\n"
"ds_read_b128 a[120:123], v12 offset:22528\n"
"v_mfma_f32_16x16x32_bf16 v[216:219], a[80:83], a[128:131], v[216:219]\n"
"ds_read_b64_tr_b16 a[50:51], v15 offset:8704\n"
"v_mfma_f32_16x16x32_bf16 v[216:219], a[84:87], a[132:135], v[216:219]\n"
"ds_read_b64_tr_b16 a[52:53], v15 offset:12288\n"
"v_mfma_f32_16x16x32_bf16 v[244:247], a[92:95], a[132:135], v[244:247]\n"
"ds_read_b128 a[132:135], v13 offset:24576\n"
"v_mfma_f32_16x16x32_bf16 v[244:247], a[88:91], a[128:131], v[244:247]\n"
"s_add_u32 s67, s75, 393216\n"
"ds_read_b128 a[128:131], v12 offset:24576\n"
"v_mfma_f32_16x16x32_bf16 v[248:251], a[88:91], a[136:139], v[248:251]\n"
"ds_read_b64_tr_b16 a[54:55], v15 offset:12800\n"
"v_mfma_f32_16x16x32_bf16 v[248:251], a[92:95], a[140:143], v[248:251]\n"
"s_add_u32 s66, s78, 1\n"
"ds_read_b64_tr_b16 a[56:57], v15 offset:8200\n"
"v_mfma_f32_16x16x32_bf16 v[220:223], a[84:87], a[140:143], v[220:223]\n"
"ds_read_b128 a[140:143], v13 offset:26624\n"
"v_mfma_f32_16x16x32_bf16 v[220:223], a[80:83], a[136:139], v[220:223]\n"
"ds_read_b128 a[136:139], v12 offset:26624\n"
"v_mov_b32 v28, s66\n"
"v_mfma_f32_16x16x32_bf16 v[224:227], a[80:83], a[144:147], v[224:227]\n"
"ds_read_b64_tr_b16 a[58:59], v15 offset:8712\n"
"v_mov_b32 v29, s79\n"
"v_mfma_f32_16x16x32_bf16 v[224:227], a[84:87], a[148:151], v[224:227]\n"
"ds_read_b64_tr_b16 a[60:61], v15 offset:12296\n"
"v_mfma_f32_16x16x32_bf16 v[252:255], a[92:95], a[148:151], v[252:255]\n"
"buffer_store_dwordx2 v[28:29], off, s[56:59], %24 sc1\n"
"ds_read_b128 a[148:151], v13 offset:28672\n"
"v_mfma_f32_16x16x32_bf16 v[252:255], a[88:91], a[144:147], v[252:255]\n"
"ds_read_b128 a[144:147], v12 offset:28672\n"
"ds_read_b64_tr_b16 a[62:63], v15 offset:12808\n"
"v_mfma_f32_16x16x32_bf16 a[28:31], a[88:91], a[152:155], a[28:31]\n"
"v_mfma_f32_16x16x32_bf16 a[28:31], a[92:95], a[156:159], a[28:31]\n"
"v_mfma_f32_16x16x32_bf16 a[24:27], a[84:87], a[156:159], a[24:27]\n"
"ds_read_b128 a[156:159], v13 offset:30720\n"
"v_mfma_f32_16x16x32_bf16 a[24:27], a[80:83], a[152:155], a[24:27]\n"
"s_waitcnt lgkmcnt(15)\n"
"ds_read_b128 a[152:155], v12 offset:30720\n"
"v_mfma_f32_16x16x32_bf16 v[32:35], a[32:35], a[96:99], v[32:35]\n"
"v_mfma_f32_16x16x32_bf16 v[32:35], a[36:39], a[100:103], v[32:35]\n"
"s_waitcnt lgkmcnt(2)\n"
"v_mfma_f32_16x16x32_bf16 v[60:63], a[44:47], a[100:103], v[60:63]\n"
"buffer_load_dwordx4 v11, s[52:55], s67 offen offset:0  lds\n"
"v_mfma_f32_16x16x32_bf16 v[60:63], a[40:43], a[96:99], v[60:63]\n"
"v_mfma_f32_16x16x32_bf16 v[64:67], a[40:43], a[104:107], v[64:67]\n"
"s_add_u32 s67, s75, 408576\n"
"v_mfma_f32_16x16x32_bf16 v[64:67], a[44:47], a[108:111], v[64:67]\n"
"v_mfma_f32_16x16x32_bf16 v[36:39], a[36:39], a[108:111], v[36:39]\n"
"v_mfma_f32_16x16x32_bf16 v[36:39], a[32:35], a[104:107], v[36:39]\n"
"buffer_load_dwordx4 v11, s[52:55], s67 offen offset:1024  lds\n"
"v_mfma_f32_16x16x32_bf16 v[40:43], a[32:35], a[112:115], v[40:43]\n"
"v_mfma_f32_16x16x32_bf16 v[40:43], a[36:39], a[116:119], v[40:43]\n"
"s_add_u32 s67, s75, 423936\n"
"v_mfma_f32_16x16x32_bf16 v[68:71], a[44:47], a[116:119], v[68:71]\n"
"v_mfma_f32_16x16x32_bf16 v[68:71], a[40:43], a[112:115], v[68:71]\n"
"v_mfma_f32_16x16x32_bf16 v[72:75], a[40:43], a[120:123], v[72:75]\n"
"buffer_load_dwordx4 v11, s[52:55], s67 offen offset:2048  lds\n"
"v_mfma_f32_16x16x32_bf16 v[72:75], a[44:47], a[124:127], v[72:75]\n"
"v_mfma_f32_16x16x32_bf16 v[44:47], a[36:39], a[124:127], v[44:47]\n"
"s_add_u32 s67, s75, 439296\n"
"v_mfma_f32_16x16x32_bf16 v[44:47], a[32:35], a[120:123], v[44:47]\n"
"v_mfma_f32_16x16x32_bf16 v[48:51], a[32:35], a[128:131], v[48:51]\n"
"buffer_load_dwordx4 v11, s[52:55], s67 offen offset:3072  lds\n"
"v_mfma_f32_16x16x32_bf16 v[48:51], a[36:39], a[132:135], v[48:51]\n"
"s_add_u32 m0, s64, 12288\n"
"v_mfma_f32_16x16x32_bf16 v[76:79], a[44:47], a[132:135], v[76:79]\n"
"s_waitcnt vmcnt(10)\n"
"ds_read_b64_tr_b16 a[64:65], v9 offset:0\n"
"v_mfma_f32_16x16x32_bf16 v[76:79], a[40:43], a[128:131], v[76:79]\n"
"ds_read_b64_tr_b16 a[66:67], v9 offset:512\n"
"v_mfma_f32_16x16x32_bf16 v[80:83], a[40:43], a[136:139], v[80:83]\n"
"s_add_u32 s67, s75, 458752\n"
"ds_read_b64_tr_b16 a[68:69], v9 offset:4096\n"
"v_mfma_f32_16x16x32_bf16 v[80:83], a[44:47], a[140:143], v[80:83]\n"
"ds_read_b64_tr_b16 a[70:71], v9 offset:4608\n"
"v_mfma_f32_16x16x32_bf16 v[52:55], a[36:39], a[140:143], v[52:55]\n"
"ds_read_b64_tr_b16 a[72:73], v9 offset:8\n"
"buffer_load_dwordx4 v11, s[52:55], s67 offen offset:0  lds\n"
"v_mfma_f32_16x16x32_bf16 v[52:55], a[32:35], a[136:139], v[52:55]\n"
"s_waitcnt lgkmcnt(5)\n"
"ds_read_b64_tr_b16 a[74:75], v9 offset:520\n"
"v_mfma_f32_16x16x32_bf16 v[56:59], a[32:35], a[144:147], v[56:59]\n"
"s_add_u32 s67, s75, 474112\n"
"ds_read_b64_tr_b16 a[76:77], v9 offset:4104\n"
"v_mfma_f32_16x16x32_bf16 v[56:59], a[36:39], a[148:151], v[56:59]\n"
"ds_read_b64_tr_b16 a[78:79], v9 offset:4616\n"
"v_mfma_f32_16x16x32_bf16 v[84:87], a[44:47], a[148:151], v[84:87]\n"
"buffer_load_dwordx4 v11, s[52:55], s67 offen offset:1024  lds\n"
"v_mfma_f32_16x16x32_bf16 v[84:87], a[40:43], a[144:147], v[84:87]\n"
"ds_read_b64_tr_b16 a[80:81], v15 offset:0\n"
"v_mfma_f32_16x16x32_bf16 a[4:7], a[40:43], a[152:155], a[4:7]\n"
"s_add_u32 s67, s75, 489472\n"
"ds_read_b64_tr_b16 a[82:83], v15 offset:512\n"
"v_mfma_f32_16x16x32_bf16 a[4:7], a[44:47], a[156:159], a[4:7]\n"
"ds_read_b64_tr_b16 a[84:85], v15 offset:4096\n"
"v_mfma_f32_16x16x32_bf16 a[0:3], a[36:39], a[156:159], a[0:3]\n"
"ds_read_b64_tr_b16 a[86:87], v15 offset:4608\n"
"buffer_load_dwordx4 v11, s[52:55], s67 offen offset:2048  lds\n"
"v_mfma_f32_16x16x32_bf16 a[0:3], a[32:35], a[152:155], a[0:3]\n"
"ds_read_b64_tr_b16 a[88:89], v15 offset:8\n"
"v_mfma_f32_16x16x32_bf16 v[88:91], a[48:51], a[96:99], v[88:91]\n"
"s_add_u32 s67, s75, 504832\n"
"ds_read_b64_tr_b16 a[90:91], v15 offset:520\n"
"v_mfma_f32_16x16x32_bf16 v[88:91], a[52:55], a[100:103], v[88:91]\n"
"ds_read_b64_tr_b16 a[92:93], v15 offset:4104\n"
"v_mfma_f32_16x16x32_bf16 v[116:119], a[60:63], a[100:103], v[116:119]\n"
"ds_read_b64_tr_b16 a[94:95], v15 offset:4616\n"
"v_mfma_f32_16x16x32_bf16 v[116:119], a[56:59], a[96:99], v[116:119]\n"
"buffer_load_dwordx4 v11, s[52:55], s67 offen offset:3072  lds\n"
"v_mfma_f32_16x16x32_bf16 v[120:123], a[56:59], a[104:107], v[120:123]\n"
"v_mfma_f32_16x16x32_bf16 v[120:123], a[60:63], a[108:111], v[120:123]\n"
"s_add_u32 m0, s64, 0\n"
"v_mfma_f32_16x16x32_bf16 v[92:95], a[52:55], a[108:111], v[92:95]\n"
"v_mfma_f32_16x16x32_bf16 v[92:95], a[48:51], a[104:107], v[92:95]\n"
"s_add_u32 s67, s75, 393344\n"
"v_mfma_f32_16x16x32_bf16 v[96:99], a[48:51], a[112:115], v[96:99]\n"
"s_waitcnt lgkmcnt(0)\n"
"v_mfma_f32_16x16x32_bf16 v[96:99], a[52:55], a[116:119], v[96:99]\n"
"buffer_load_dwordx4 v11, s[52:55], s67 offen offset:0  lds\n"
"v_mfma_f32_16x16x32_bf16 v[124:127], a[60:63], a[116:119], v[124:127]\n"
"v_mfma_f32_16x16x32_bf16 v[124:127], a[56:59], a[112:115], v[124:127]\n"
"s_add_u32 s67, s75, 408704\n"
"v_mfma_f32_16x16x32_bf16 v[128:131], a[56:59], a[120:123], v[128:131]\n"
"v_mfma_f32_16x16x32_bf16 v[128:131], a[60:63], a[124:127], v[128:131]\n"
"v_mfma_f32_16x16x32_bf16 v[100:103], a[52:55], a[124:127], v[100:103]\n"
"buffer_load_dwordx4 v11, s[52:55], s67 offen offset:1024  lds\n"
"v_mfma_f32_16x16x32_bf16 v[100:103], a[48:51], a[120:123], v[100:103]\n"
"v_mfma_f32_16x16x32_bf16 v[104:107], a[48:51], a[128:131], v[104:107]\n"
"s_add_u32 s67, s75, 424064\n"
"v_mfma_f32_16x16x32_bf16 v[104:107], a[52:55], a[132:135], v[104:107]\n"
"v_mfma_f32_16x16x32_bf16 v[132:135], a[60:63], a[132:135], v[132:135]\n"
"v_mfma_f32_16x16x32_bf16 v[132:135], a[56:59], a[128:131], v[132:135]\n"
"buffer_load_dwordx4 v11, s[52:55], s67 offen offset:2048  lds\n"
"v_mfma_f32_16x16x32_bf16 v[136:139], a[56:59], a[136:139], v[136:139]\n"
"v_mfma_f32_16x16x32_bf16 v[136:139], a[60:63], a[140:143], v[136:139]\n"
"s_add_u32 s67, s75, 439424\n"
"v_mfma_f32_16x16x32_bf16 v[108:111], a[52:55], a[140:143], v[108:111]\n"
"v_mfma_f32_16x16x32_bf16 v[108:111], a[48:51], a[136:139], v[108:111]\n"
"buffer_load_dwordx4 v11, s[52:55], s67 offen offset:3072  lds\n"
"v_mfma_f32_16x16x32_bf16 v[112:115], a[48:51], a[144:147], v[112:115]\n"
"v_mfma_f32_16x16x32_bf16 v[112:115], a[52:55], a[148:151], v[112:115]\n"
"s_add_u32 m0, s64, 4096\n"
"v_mfma_f32_16x16x32_bf16 v[140:143], a[60:63], a[148:151], v[140:143]\n"
"v_mfma_f32_16x16x32_bf16 v[140:143], a[56:59], a[144:147], v[140:143]\n"
"s_add_u32 s67, s75, 458880\n"
"v_mfma_f32_16x16x32_bf16 a[12:15], a[56:59], a[152:155], a[12:15]\n"
"v_mfma_f32_16x16x32_bf16 a[12:15], a[60:63], a[156:159], a[12:15]\n"
"buffer_load_dwordx4 v11, s[52:55], s67 offen offset:0  lds\n"
"v_mfma_f32_16x16x32_bf16 a[8:11], a[52:55], a[156:159], a[8:11]\n"
"v_mfma_f32_16x16x32_bf16 a[8:11], a[48:51], a[152:155], a[8:11]\n"
"s_add_u32 s67, s75, 474240\n"
"v_mfma_f32_16x16x32_bf16 v[144:147], a[64:67], a[96:99], v[144:147]\n"
"v_mfma_f32_16x16x32_bf16 v[144:147], a[68:71], a[100:103], v[144:147]\n"
"buffer_load_dwordx4 v11, s[52:55], s67 offen offset:1024  lds\n"
"v_mfma_f32_16x16x32_bf16 v[172:175], a[76:79], a[100:103], v[172:175]\n"
"v_mfma_f32_16x16x32_bf16 v[172:175], a[72:75], a[96:99], v[172:175]\n"
"s_add_u32 s67, s75, 489600\n"
"v_mfma_f32_16x16x32_bf16 v[176:179], a[72:75], a[104:107], v[176:179]\n"
"v_mfma_f32_16x16x32_bf16 v[176:179], a[76:79], a[108:111], v[176:179]\n"
"buffer_load_dwordx4 v11, s[52:55], s67 offen offset:2048  lds\n"
"v_mfma_f32_16x16x32_bf16 v[148:151], a[68:71], a[108:111], v[148:151]\n"
"v_mfma_f32_16x16x32_bf16 v[148:151], a[64:67], a[104:107], v[148:151]\n"
"s_add_u32 s67, s75, 504960\n"
"v_mfma_f32_16x16x32_bf16 v[152:155], a[64:67], a[112:115], v[152:155]\n"
"s_sub_u32 s66, 122880, s73\n"
"v_mfma_f32_16x16x32_bf16 v[152:155], a[68:71], a[116:119], v[152:155]\n"
"v_mfma_f32_16x16x32_bf16 v[180:183], a[76:79], a[116:119], v[180:183]\n"
"v_add_u32 v12, s66, v7\n"
"v_mfma_f32_16x16x32_bf16 v[180:183], a[72:75], a[112:115], v[180:183]\n"
"buffer_load_dwordx4 v11, s[52:55], s67 offen offset:3072  lds\n"
"v_mfma_f32_16x16x32_bf16 v[184:187], a[72:75], a[120:123], v[184:187]\n"
"s_add_u32 m0, s72, 16384\n"
"v_add_u32 v13, s66, v8\n"
"v_mfma_f32_16x16x32_bf16 v[184:187], a[76:79], a[124:127], v[184:187]\n"
"v_mfma_f32_16x16x32_bf16 v[156:159], a[68:71], a[124:127], v[156:159]\n"
"s_add_u32 s67, s60, 512\n"
"v_mfma_f32_16x16x32_bf16 v[156:159], a[64:67], a[120:123], v[156:159]\n"
"buffer_load_dwordx4 v6, s[48:51], s67 offen offset:0  lds\n"
"v_mfma_f32_16x16x32_bf16 v[160:163], a[64:67], a[128:131], v[160:163]\n"
"v_mfma_f32_16x16x32_bf16 v[160:163], a[68:71], a[132:135], v[160:163]\n"
"s_add_u32 s67, s60, 15872\n"
"v_mfma_f32_16x16x32_bf16 v[188:191], a[76:79], a[132:135], v[188:191]\n"
"v_mfma_f32_16x16x32_bf16 v[188:191], a[72:75], a[128:131], v[188:191]\n"
"buffer_load_dwordx4 v6, s[48:51], s67 offen offset:1024  lds\n"
"v_mfma_f32_16x16x32_bf16 v[192:195], a[72:75], a[136:139], v[192:195]\n"
"v_mfma_f32_16x16x32_bf16 v[192:195], a[76:79], a[140:143], v[192:195]\n"
"s_add_u32 s67, s60, 31232\n"
"v_mfma_f32_16x16x32_bf16 v[164:167], a[68:71], a[140:143], v[164:167]\n"
"v_mfma_f32_16x16x32_bf16 v[164:167], a[64:67], a[136:139], v[164:167]\n"
"buffer_load_dwordx4 v6, s[48:51], s67 offen offset:2048  lds\n"
"v_mfma_f32_16x16x32_bf16 v[168:171], a[64:67], a[144:147], v[168:171]\n"
"v_mfma_f32_16x16x32_bf16 v[168:171], a[68:71], a[148:151], v[168:171]\n"
"s_add_u32 s67, s60, 46592\n"
"v_mfma_f32_16x16x32_bf16 v[196:199], a[76:79], a[148:151], v[196:199]\n"
"v_mfma_f32_16x16x32_bf16 v[196:199], a[72:75], a[144:147], v[196:199]\n"
"v_mfma_f32_16x16x32_bf16 a[20:23], a[72:75], a[152:155], a[20:23]\n"
"buffer_load_dwordx4 v6, s[48:51], s67 offen offset:3072  lds\n"
"v_mfma_f32_16x16x32_bf16 a[20:23], a[76:79], a[156:159], a[20:23]\n"
"v_mfma_f32_16x16x32_bf16 a[16:19], a[68:71], a[156:159], a[16:19]\n"
"s_add_u32 s70, s75, 786432\n"
"s_add_u32 s70, s70, s65\n"
"v_mfma_f32_16x16x32_bf16 a[16:19], a[64:67], a[152:155], a[16:19]\n"
"s_waitcnt vmcnt(22)\n"
"s_barrier\n"
"v_mfma_f32_16x16x32_bf16 v[200:203], a[80:83], a[96:99], v[200:203]\n"
"s_waitcnt vmcnt(12)\n"
"ds_read_b64_tr_b16 a[32:33], v9 offset:8192\n"
"v_mfma_f32_16x16x32_bf16 v[200:203], a[84:87], a[100:103], v[200:203]\n"
"ds_read_b64_tr_b16 a[34:35], v9 offset:8704\n"
"v_mfma_f32_16x16x32_bf16 v[228:231], a[92:95], a[100:103], v[228:231]\n"
"ds_read_b128 a[100:103], v13 offset:16384\n"
"buffer_load_dword a252, v14, s[52:55], s70 offen\n"
"v_mfma_f32_16x16x32_bf16 v[228:231], a[88:91], a[96:99], v[228:231]\n"
"ds_read_b128 a[96:99], v12 offset:16384\n"
"v_mfma_f32_16x16x32_bf16 v[232:235], a[88:91], a[104:107], v[232:235]\n"
"ds_read_b64_tr_b16 a[36:37], v9 offset:12288\n"
"v_mfma_f32_16x16x32_bf16 v[232:235], a[92:95], a[108:111], v[232:235]\n"
"ds_read_b64_tr_b16 a[38:39], v9 offset:12800\n"
"v_mfma_f32_16x16x32_bf16 v[204:207], a[84:87], a[108:111], v[204:207]\n"
"ds_read_b128 a[108:111], v13 offset:18432\n"
"v_mfma_f32_16x16x32_bf16 v[204:207], a[80:83], a[104:107], v[204:207]\n"
"ds_read_b128 a[104:107], v12 offset:18432\n"
"ds_read_b64_tr_b16 a[40:41], v9 offset:8200\n"
"v_mfma_f32_16x16x32_bf16 v[208:211], a[80:83], a[112:115], v[208:211]\n"
"s_add_u32 m0, s64, 8192\n"
"ds_read_b64_tr_b16 a[42:43], v9 offset:8712\n"
"v_mfma_f32_16x16x32_bf16 v[208:211], a[84:87], a[116:119], v[208:211]\n"
"ds_read_b64_tr_b16 a[44:45], v9 offset:12296\n"
"v_mfma_f32_16x16x32_bf16 v[236:239], a[92:95], a[116:119], v[236:239]\n"
"L_st1058:\n"
"s_cbranch_execz L_st3238\n"
"ds_read_b128 a[116:119], v13 offset:20480\n"
"v_mfma_f32_16x16x32_bf16 v[236:239], a[88:91], a[112:115], v[236:239]\n"
"ds_read_b128 a[112:115], v12 offset:20480\n"
"v_mfma_f32_16x16x32_bf16 v[240:243], a[88:91], a[120:123], v[240:243]\n"
"ds_read_b64_tr_b16 a[46:47], v9 offset:12808\n"
"v_mfma_f32_16x16x32_bf16 v[240:243], a[92:95], a[124:127], v[240:243]\n"
"ds_read_b64_tr_b16 a[48:49], v15 offset:8192\n"
"v_mfma_f32_16x16x32_bf16 v[212:215], a[84:87], a[124:127], v[212:215]\n"
"ds_read_b128 a[124:127], v13 offset:22528\n"
"v_mfma_f32_16x16x32_bf16 v[212:215], a[80:83], a[120:123], v[212:215]\n"
"ds_read_b128 a[120:123], v12 offset:22528\n"
"v_mfma_f32_16x16x32_bf16 v[216:219], a[80:83], a[128:131], v[216:219]\n"
"ds_read_b64_tr_b16 a[50:51], v15 offset:8704\n"
"v_mfma_f32_16x16x32_bf16 v[216:219], a[84:87], a[132:135], v[216:219]\n"
"ds_read_b64_tr_b16 a[52:53], v15 offset:12288\n"
"v_mfma_f32_16x16x32_bf16 v[244:247], a[92:95], a[132:135], v[244:247]\n"
"ds_read_b128 a[132:135], v13 offset:24576\n"
"v_mfma_f32_16x16x32_bf16 v[244:247], a[88:91], a[128:131], v[244:247]\n"
"s_add_u32 s67, s75, 524288\n"
"ds_read_b128 a[128:131], v12 offset:24576\n"
"v_mfma_f32_16x16x32_bf16 v[248:251], a[88:91], a[136:139], v[248:251]\n"
"ds_read_b64_tr_b16 a[54:55], v15 offset:12800\n"
"v_mfma_f32_16x16x32_bf16 v[248:251], a[92:95], a[140:143], v[248:251]\n"
"ds_read_b64_tr_b16 a[56:57], v15 offset:8200\n"
"v_mfma_f32_16x16x32_bf16 v[220:223], a[84:87], a[140:143], v[220:223]\n"
"ds_read_b128 a[140:143], v13 offset:26624\n"
"v_mfma_f32_16x16x32_bf16 v[220:223], a[80:83], a[136:139], v[220:223]\n"
"ds_read_b128 a[136:139], v12 offset:26624\n"
"v_mfma_f32_16x16x32_bf16 v[224:227], a[80:83], a[144:147], v[224:227]\n"
"ds_read_b64_tr_b16 a[58:59], v15 offset:8712\n"
"v_mfma_f32_16x16x32_bf16 v[224:227], a[84:87], a[148:151], v[224:227]\n"
"ds_read_b64_tr_b16 a[60:61], v15 offset:12296\n"
"v_mfma_f32_16x16x32_bf16 v[252:255], a[92:95], a[148:151], v[252:255]\n"
"ds_read_b128 a[148:151], v13 offset:28672\n"
"v_mfma_f32_16x16x32_bf16 v[252:255], a[88:91], a[144:147], v[252:255]\n"
"ds_read_b128 a[144:147], v12 offset:28672\n"
"ds_read_b64_tr_b16 a[62:63], v15 offset:12808\n"
"v_mfma_f32_16x16x32_bf16 a[28:31], a[88:91], a[152:155], a[28:31]\n"
"v_mfma_f32_16x16x32_bf16 a[28:31], a[92:95], a[156:159], a[28:31]\n"
"v_mfma_f32_16x16x32_bf16 a[24:27], a[84:87], a[156:159], a[24:27]\n"
"ds_read_b128 a[156:159], v13 offset:30720\n"
"v_mfma_f32_16x16x32_bf16 a[24:27], a[80:83], a[152:155], a[24:27]\n"
"s_waitcnt lgkmcnt(15)\n"
"ds_read_b128 a[152:155], v12 offset:30720\n"
"v_mfma_f32_16x16x32_bf16 v[32:35], a[32:35], a[96:99], v[32:35]\n"
"v_mfma_f32_16x16x32_bf16 v[32:35], a[36:39], a[100:103], v[32:35]\n"
"s_waitcnt lgkmcnt(2)\n"
"v_mfma_f32_16x16x32_bf16 v[60:63], a[44:47], a[100:103], v[60:63]\n"
"buffer_load_dwordx4 v11, s[52:55], s67 offen offset:0  lds\n"
"v_mfma_f32_16x16x32_bf16 v[60:63], a[40:43], a[96:99], v[60:63]\n"
"v_mfma_f32_16x16x32_bf16 v[64:67], a[40:43], a[104:107], v[64:67]\n"
"s_add_u32 s67, s75, 539648\n"
"v_mfma_f32_16x16x32_bf16 v[64:67], a[44:47], a[108:111], v[64:67]\n"
"v_mfma_f32_16x16x32_bf16 v[36:39], a[36:39], a[108:111], v[36:39]\n"
"buffer_load_dwordx4 v11, s[52:55], s67 offen offset:1024  lds\n"
"v_mfma_f32_16x16x32_bf16 v[36:39], a[32:35], a[104:107], v[36:39]\n"
"v_mfma_f32_16x16x32_bf16 v[40:43], a[32:35], a[112:115], v[40:43]\n"
"s_add_u32 s67, s75, 555008\n"
"v_mfma_f32_16x16x32_bf16 v[40:43], a[36:39], a[116:119], v[40:43]\n"
"v_mfma_f32_16x16x32_bf16 v[68:71], a[44:47], a[116:119], v[68:71]\n"
"v_mfma_f32_16x16x32_bf16 v[68:71], a[40:43], a[112:115], v[68:71]\n"
"buffer_load_dwordx4 v11, s[52:55], s67 offen offset:2048  lds\n"
"v_mfma_f32_16x16x32_bf16 v[72:75], a[40:43], a[120:123], v[72:75]\n"
"v_mfma_f32_16x16x32_bf16 v[72:75], a[44:47], a[124:127], v[72:75]\n"
"s_add_u32 s67, s75, 570368\n"
"v_mfma_f32_16x16x32_bf16 v[44:47], a[36:39], a[124:127], v[44:47]\n"
"v_mfma_f32_16x16x32_bf16 v[44:47], a[32:35], a[120:123], v[44:47]\n"
"buffer_load_dwordx4 v11, s[52:55], s67 offen offset:3072  lds\n"
"v_mfma_f32_16x16x32_bf16 v[48:51], a[32:35], a[128:131], v[48:51]\n"
"v_mfma_f32_16x16x32_bf16 v[48:51], a[36:39], a[132:135], v[48:51]\n"
"s_add_u32 m0, s64, 12288\n"
"v_mfma_f32_16x16x32_bf16 v[76:79], a[44:47], a[132:135], v[76:79]\n"
"s_waitcnt vmcnt(9)\n"
"ds_read_b64_tr_b16 a[64:65], v9 offset:0\n"
"v_mfma_f32_16x16x32_bf16 v[76:79], a[40:43], a[128:131], v[76:79]\n"
"s_add_u32 s67, s75, 589824\n"
"ds_read_b64_tr_b16 a[66:67], v9 offset:512\n"
"v_mfma_f32_16x16x32_bf16 v[80:83], a[40:43], a[136:139], v[80:83]\n"
"ds_read_b64_tr_b16 a[68:69], v9 offset:4096\n"
"v_mfma_f32_16x16x32_bf16 v[80:83], a[44:47], a[140:143], v[80:83]\n"
"ds_read_b64_tr_b16 a[70:71], v9 offset:4608\n"
"buffer_load_dwordx4 v11, s[52:55], s67 offen offset:0  lds\n"
"v_mfma_f32_16x16x32_bf16 v[52:55], a[36:39], a[140:143], v[52:55]\n"
"s_add_u32 s67, s75, 605184\n"
"ds_read_b64_tr_b16 a[72:73], v9 offset:8\n"
"v_mfma_f32_16x16x32_bf16 v[52:55], a[32:35], a[136:139], v[52:55]\n"
"s_waitcnt lgkmcnt(5)\n"
"ds_read_b64_tr_b16 a[74:75], v9 offset:520\n"
"v_mfma_f32_16x16x32_bf16 v[56:59], a[32:35], a[144:147], v[56:59]\n"
"ds_read_b64_tr_b16 a[76:77], v9 offset:4104\n"
"v_mfma_f32_16x16x32_bf16 v[56:59], a[36:39], a[148:151], v[56:59]\n"
"ds_read_b64_tr_b16 a[78:79], v9 offset:4616\n"
"v_mfma_f32_16x16x32_bf16 v[84:87], a[44:47], a[148:151], v[84:87]\n"
"buffer_load_dwordx4 v11, s[52:55], s67 offen offset:1024  lds\n"
"v_mfma_f32_16x16x32_bf16 v[84:87], a[40:43], a[144:147], v[84:87]\n"
"ds_read_b64_tr_b16 a[80:81], v15 offset:0\n"
"v_mfma_f32_16x16x32_bf16 a[4:7], a[40:43], a[152:155], a[4:7]\n"
"s_add_u32 s67, s75, 620544\n"
"ds_read_b64_tr_b16 a[82:83], v15 offset:512\n"
"v_mfma_f32_16x16x32_bf16 a[4:7], a[44:47], a[156:159], a[4:7]\n"
"ds_read_b64_tr_b16 a[84:85], v15 offset:4096\n"
"v_mfma_f32_16x16x32_bf16 a[0:3], a[36:39], a[156:159], a[0:3]\n"
"ds_read_b64_tr_b16 a[86:87], v15 offset:4608\n"
"buffer_load_dwordx4 v11, s[52:55], s67 offen offset:2048  lds\n"
"v_mfma_f32_16x16x32_bf16 a[0:3], a[32:35], a[152:155], a[0:3]\n"
"ds_read_b64_tr_b16 a[88:89], v15 offset:8\n"
"v_mfma_f32_16x16x32_bf16 v[88:91], a[48:51], a[96:99], v[88:91]\n"
"s_add_u32 s67, s75, 635904\n"
"ds_read_b64_tr_b16 a[90:91], v15 offset:520\n"
"v_mfma_f32_16x16x32_bf16 v[88:91], a[52:55], a[100:103], v[88:91]\n"
"ds_read_b64_tr_b16 a[92:93], v15 offset:4104\n"
"v_mfma_f32_16x16x32_bf16 v[116:119], a[60:63], a[100:103], v[116:119]\n"
"ds_read_b64_tr_b16 a[94:95], v15 offset:4616\n"
"v_mfma_f32_16x16x32_bf16 v[116:119], a[56:59], a[96:99], v[116:119]\n"
"buffer_load_dwordx4 v11, s[52:55], s67 offen offset:3072  lds\n"
"v_mfma_f32_16x16x32_bf16 v[120:123], a[56:59], a[104:107], v[120:123]\n"
"v_mfma_f32_16x16x32_bf16 v[120:123], a[60:63], a[108:111], v[120:123]\n"
"s_add_u32 m0, s64, 0\n"
"v_mfma_f32_16x16x32_bf16 v[92:95], a[52:55], a[108:111], v[92:95]\n"
"v_mfma_f32_16x16x32_bf16 v[92:95], a[48:51], a[104:107], v[92:95]\n"
"s_add_u32 s67, s75, 524416\n"
"v_mfma_f32_16x16x32_bf16 v[96:99], a[48:51], a[112:115], v[96:99]\n"
"s_waitcnt lgkmcnt(0)\n"
"v_mfma_f32_16x16x32_bf16 v[96:99], a[52:55], a[116:119], v[96:99]\n"
"buffer_load_dwordx4 v11, s[52:55], s67 offen offset:0  lds\n"
"v_mfma_f32_16x16x32_bf16 v[124:127], a[60:63], a[116:119], v[124:127]\n"
"v_mfma_f32_16x16x32_bf16 v[124:127], a[56:59], a[112:115], v[124:127]\n"
"s_add_u32 s67, s75, 539776\n"
"v_mfma_f32_16x16x32_bf16 v[128:131], a[56:59], a[120:123], v[128:131]\n"
"v_mfma_f32_16x16x32_bf16 v[128:131], a[60:63], a[124:127], v[128:131]\n"
"v_mfma_f32_16x16x32_bf16 v[100:103], a[52:55], a[124:127], v[100:103]\n"
"buffer_load_dwordx4 v11, s[52:55], s67 offen offset:1024  lds\n"
"v_mfma_f32_16x16x32_bf16 v[100:103], a[48:51], a[120:123], v[100:103]\n"
"v_mfma_f32_16x16x32_bf16 v[104:107], a[48:51], a[128:131], v[104:107]\n"
"s_add_u32 s67, s75, 555136\n"
"v_mfma_f32_16x16x32_bf16 v[104:107], a[52:55], a[132:135], v[104:107]\n"
"v_mfma_f32_16x16x32_bf16 v[132:135], a[60:63], a[132:135], v[132:135]\n"
"v_mfma_f32_16x16x32_bf16 v[132:135], a[56:59], a[128:131], v[132:135]\n"
"buffer_load_dwordx4 v11, s[52:55], s67 offen offset:2048  lds\n"
"v_mfma_f32_16x16x32_bf16 v[136:139], a[56:59], a[136:139], v[136:139]\n"
"v_mfma_f32_16x16x32_bf16 v[136:139], a[60:63], a[140:143], v[136:139]\n"
"s_add_u32 s67, s75, 570496\n"
"v_mfma_f32_16x16x32_bf16 v[108:111], a[52:55], a[140:143], v[108:111]\n"
"v_mfma_f32_16x16x32_bf16 v[108:111], a[48:51], a[136:139], v[108:111]\n"
"buffer_load_dwordx4 v11, s[52:55], s67 offen offset:3072  lds\n"
"v_mfma_f32_16x16x32_bf16 v[112:115], a[48:51], a[144:147], v[112:115]\n"
"v_mfma_f32_16x16x32_bf16 v[112:115], a[52:55], a[148:151], v[112:115]\n"
"s_add_u32 m0, s64, 4096\n"
"v_mfma_f32_16x16x32_bf16 v[140:143], a[60:63], a[148:151], v[140:143]\n"
"v_mfma_f32_16x16x32_bf16 v[140:143], a[56:59], a[144:147], v[140:143]\n"
"s_add_u32 s67, s75, 589952\n"
"v_mfma_f32_16x16x32_bf16 a[12:15], a[56:59], a[152:155], a[12:15]\n"
"v_mfma_f32_16x16x32_bf16 a[12:15], a[60:63], a[156:159], a[12:15]\n"
"buffer_load_dwordx4 v11, s[52:55], s67 offen offset:0  lds\n"
"v_mfma_f32_16x16x32_bf16 a[8:11], a[52:55], a[156:159], a[8:11]\n"
"v_mfma_f32_16x16x32_bf16 a[8:11], a[48:51], a[152:155], a[8:11]\n"
"s_add_u32 s67, s75, 605312\n"
"v_mfma_f32_16x16x32_bf16 v[144:147], a[64:67], a[96:99], v[144:147]\n"
"v_mfma_f32_16x16x32_bf16 v[144:147], a[68:71], a[100:103], v[144:147]\n"
"buffer_load_dwordx4 v11, s[52:55], s67 offen offset:1024  lds\n"
"v_mfma_f32_16x16x32_bf16 v[172:175], a[76:79], a[100:103], v[172:175]\n"
"v_mfma_f32_16x16x32_bf16 v[172:175], a[72:75], a[96:99], v[172:175]\n"
"s_add_u32 s67, s75, 620672\n"
"v_mfma_f32_16x16x32_bf16 v[176:179], a[72:75], a[104:107], v[176:179]\n"
"v_mfma_f32_16x16x32_bf16 v[176:179], a[76:79], a[108:111], v[176:179]\n"
"buffer_load_dwordx4 v11, s[52:55], s67 offen offset:2048  lds\n"
"v_mfma_f32_16x16x32_bf16 v[148:151], a[68:71], a[108:111], v[148:151]\n"
"v_mfma_f32_16x16x32_bf16 v[148:151], a[64:67], a[104:107], v[148:151]\n"
"s_add_u32 s67, s75, 636032\n"
"v_mfma_f32_16x16x32_bf16 v[152:155], a[64:67], a[112:115], v[152:155]\n"
"s_sub_u32 s66, 0, s73\n"
"v_mfma_f32_16x16x32_bf16 v[152:155], a[68:71], a[116:119], v[152:155]\n"
"v_mfma_f32_16x16x32_bf16 v[180:183], a[76:79], a[116:119], v[180:183]\n"
"v_add_u32 v12, s66, v7\n"
"v_mfma_f32_16x16x32_bf16 v[180:183], a[72:75], a[112:115], v[180:183]\n"
"buffer_load_dwordx4 v11, s[52:55], s67 offen offset:3072  lds\n"
"v_mfma_f32_16x16x32_bf16 v[184:187], a[72:75], a[120:123], v[184:187]\n"
"s_add_u32 m0, s72, 57344\n"
"v_add_u32 v13, s66, v8\n"
"v_mfma_f32_16x16x32_bf16 v[184:187], a[76:79], a[124:127], v[184:187]\n"
"v_mfma_f32_16x16x32_bf16 v[156:159], a[68:71], a[124:127], v[156:159]\n"
"s_add_u32 s67, s60, 640\n"
"v_mfma_f32_16x16x32_bf16 v[156:159], a[64:67], a[120:123], v[156:159]\n"
"buffer_load_dwordx4 v6, s[48:51], s67 offen offset:0  lds\n"
"v_mfma_f32_16x16x32_bf16 v[160:163], a[64:67], a[128:131], v[160:163]\n"
"v_mfma_f32_16x16x32_bf16 v[160:163], a[68:71], a[132:135], v[160:163]\n"
"s_add_u32 s67, s60, 16000\n"
"v_mfma_f32_16x16x32_bf16 v[188:191], a[76:79], a[132:135], v[188:191]\n"
"v_mfma_f32_16x16x32_bf16 v[188:191], a[72:75], a[128:131], v[188:191]\n"
"buffer_load_dwordx4 v6, s[48:51], s67 offen offset:1024  lds\n"
"v_mfma_f32_16x16x32_bf16 v[192:195], a[72:75], a[136:139], v[192:195]\n"
"v_mfma_f32_16x16x32_bf16 v[192:195], a[76:79], a[140:143], v[192:195]\n"
"s_add_u32 s67, s60, 31360\n"
"v_mfma_f32_16x16x32_bf16 v[164:167], a[68:71], a[140:143], v[164:167]\n"
"v_mfma_f32_16x16x32_bf16 v[164:167], a[64:67], a[136:139], v[164:167]\n"
"buffer_load_dwordx4 v6, s[48:51], s67 offen offset:2048  lds\n"
"v_mfma_f32_16x16x32_bf16 v[168:171], a[64:67], a[144:147], v[168:171]\n"
"v_mfma_f32_16x16x32_bf16 v[168:171], a[68:71], a[148:151], v[168:171]\n"
"s_add_u32 s67, s60, 46720\n"
"v_mfma_f32_16x16x32_bf16 v[196:199], a[76:79], a[148:151], v[196:199]\n"
"v_mfma_f32_16x16x32_bf16 v[196:199], a[72:75], a[144:147], v[196:199]\n"
"v_mfma_f32_16x16x32_bf16 a[20:23], a[72:75], a[152:155], a[20:23]\n"
"buffer_load_dwordx4 v6, s[48:51], s67 offen offset:3072  lds\n"
"v_mfma_f32_16x16x32_bf16 a[20:23], a[76:79], a[156:159], a[20:23]\n"
"s_sub_u32 s70, 1048576, s75\n"
"v_and_b32 v19, 0x20000, v14\n"
"v_mfma_f32_16x16x32_bf16 a[16:19], a[68:71], a[156:159], a[16:19]\n"
"v_add_u32 v14, v19, v14\n"
"v_mfma_f32_16x16x32_bf16 a[16:19], a[64:67], a[152:155], a[16:19]\n"
"s_waitcnt vmcnt(21)\n"
"s_barrier\n"
"v_mfma_f32_16x16x32_bf16 v[200:203], a[80:83], a[96:99], v[200:203]\n"
"s_waitcnt vmcnt(12)\n"
"ds_read_b64_tr_b16 a[32:33], v9 offset:8192\n"
"v_mfma_f32_16x16x32_bf16 v[200:203], a[84:87], a[100:103], v[200:203]\n"
"s_add_u32 s70, s70, s65\n"
"ds_read_b64_tr_b16 a[34:35], v9 offset:8704\n"
"s_getpc_b64 s[96:97]\n"
"L_c10_:\n"
"s_add_u32 s96, s96, L_H6S-L_c10_\n"
"s_addc_u32 s97, s97, 0\n"
"v_mfma_f32_16x16x32_bf16 v[228:231], a[92:95], a[100:103], v[228:231]\n"
"s_lshr_b32 s66, %26, 3\n"
"ds_read_b128 a[100:103], v13 offset:16384\n"
"buffer_load_dword a252, v14, s[52:55], s70 offen\n"
"v_mfma_f32_16x16x32_bf16 v[228:231], a[88:91], a[96:99], v[228:231]\n"
"s_lshl2_add_u32 s66, s66, %16\n"
"ds_read_b128 a[96:99], v12 offset:16384\n"
"v_mfma_f32_16x16x32_bf16 v[232:235], a[88:91], a[104:107], v[232:235]\n"
"s_mul_i32 s66, s66, 128\n"
"ds_read_b64_tr_b16 a[36:37], v9 offset:12288\n"
"v_mfma_f32_16x16x32_bf16 v[232:235], a[92:95], a[108:111], v[232:235]\n"
"s_min_u32 s66, s66, L_headF-L_H6S-128\n"
"ds_read_b64_tr_b16 a[38:39], v9 offset:12800\n"
"v_min_u32 v22, 0, v0\n"
"v_mfma_f32_16x16x32_bf16 v[204:207], a[84:87], a[108:111], v[204:207]\n"
"ds_read_b128 a[108:111], v13 offset:18432\n"
"v_lshl_add_u32 v22, v22, 3, s66\n"
"v_mfma_f32_16x16x32_bf16 v[204:207], a[80:83], a[104:107], v[204:207]\n"
"ds_read_b128 a[104:107], v12 offset:18432\n"
"global_load_dword v23, v22, s[96:97]\n"
"ds_read_b64_tr_b16 a[40:41], v9 offset:8200\n"
"v_mfma_f32_16x16x32_bf16 v[208:211], a[80:83], a[112:115], v[208:211]\n"
"s_add_u32 m0, s64, 8192\n"
"ds_read_b64_tr_b16 a[42:43], v9 offset:8712\n"
"v_mfma_f32_16x16x32_bf16 v[208:211], a[84:87], a[116:119], v[208:211]\n"
"ds_read_b64_tr_b16 a[44:45], v9 offset:12296\n"
"v_mfma_f32_16x16x32_bf16 v[236:239], a[92:95], a[116:119], v[236:239]\n"
"ds_read_b128 a[116:119], v13 offset:20480\n"
"v_mfma_f32_16x16x32_bf16 v[236:239], a[88:91], a[112:115], v[236:239]\n"
"ds_read_b128 a[112:115], v12 offset:20480\n"
"v_mfma_f32_16x16x32_bf16 v[240:243], a[88:91], a[120:123], v[240:243]\n"
"ds_read_b64_tr_b16 a[46:47], v9 offset:12808\n"
"v_mfma_f32_16x16x32_bf16 v[240:243], a[92:95], a[124:127], v[240:243]\n"
"ds_read_b64_tr_b16 a[48:49], v15 offset:8192\n"
"v_mfma_f32_16x16x32_bf16 v[212:215], a[84:87], a[124:127], v[212:215]\n"
"ds_read_b128 a[124:127], v13 offset:22528\n"
"v_mfma_f32_16x16x32_bf16 v[212:215], a[80:83], a[120:123], v[212:215]\n"
"ds_read_b128 a[120:123], v12 offset:22528\n"
"v_mfma_f32_16x16x32_bf16 v[216:219], a[80:83], a[128:131], v[216:219]\n"
"ds_read_b64_tr_b16 a[50:51], v15 offset:8704\n"
"v_mfma_f32_16x16x32_bf16 v[216:219], a[84:87], a[132:135], v[216:219]\n"
"ds_read_b64_tr_b16 a[52:53], v15 offset:12288\n"
"v_mfma_f32_16x16x32_bf16 v[244:247], a[92:95], a[132:135], v[244:247]\n"
"ds_read_b128 a[132:135], v13 offset:24576\n"
"v_mfma_f32_16x16x32_bf16 v[244:247], a[88:91], a[128:131], v[244:247]\n"
"s_add_u32 s67, s75, 655360\n"
"ds_read_b128 a[128:131], v12 offset:24576\n"
"v_mfma_f32_16x16x32_bf16 v[248:251], a[88:91], a[136:139], v[248:251]\n"
"ds_read_b64_tr_b16 a[54:55], v15 offset:12800\n"
"v_mfma_f32_16x16x32_bf16 v[248:251], a[92:95], a[140:143], v[248:251]\n"
"ds_read_b64_tr_b16 a[56:57], v15 offset:8200\n"
"v_mfma_f32_16x16x32_bf16 v[220:223], a[84:87], a[140:143], v[220:223]\n"
"ds_read_b128 a[140:143], v13 offset:26624\n"
"v_mfma_f32_16x16x32_bf16 v[220:223], a[80:83], a[136:139], v[220:223]\n"
"ds_read_b128 a[136:139], v12 offset:26624\n"
"v_mfma_f32_16x16x32_bf16 v[224:227], a[80:83], a[144:147], v[224:227]\n"
"ds_read_b64_tr_b16 a[58:59], v15 offset:8712\n"
"v_mfma_f32_16x16x32_bf16 v[224:227], a[84:87], a[148:151], v[224:227]\n"
"ds_read_b64_tr_b16 a[60:61], v15 offset:12296\n"
"v_mfma_f32_16x16x32_bf16 v[252:255], a[92:95], a[148:151], v[252:255]\n"
"ds_read_b128 a[148:151], v13 offset:28672\n"
"v_mfma_f32_16x16x32_bf16 v[252:255], a[88:91], a[144:147], v[252:255]\n"
"ds_read_b128 a[144:147], v12 offset:28672\n"
"ds_read_b64_tr_b16 a[62:63], v15 offset:12808\n"
"v_mfma_f32_16x16x32_bf16 a[28:31], a[88:91], a[152:155], a[28:31]\n"
"v_mfma_f32_16x16x32_bf16 a[28:31], a[92:95], a[156:159], a[28:31]\n"
"v_mfma_f32_16x16x32_bf16 a[24:27], a[84:87], a[156:159], a[24:27]\n"
"ds_read_b128 a[156:159], v13 offset:30720\n"
"v_mfma_f32_16x16x32_bf16 a[24:27], a[80:83], a[152:155], a[24:27]\n"
"s_waitcnt lgkmcnt(15)\n"
"ds_read_b128 a[152:155], v12 offset:30720\n"
"v_mfma_f32_16x16x32_bf16 v[32:35], a[32:35], a[96:99], v[32:35]\n"
"v_mfma_f32_16x16x32_bf16 v[32:35], a[36:39], a[100:103], v[32:35]\n"
"s_waitcnt lgkmcnt(2)\n"
"v_mfma_f32_16x16x32_bf16 v[60:63], a[44:47], a[100:103], v[60:63]\n"
"buffer_load_dwordx4 v11, s[52:55], s67 offen offset:0  lds\n"
"v_mfma_f32_16x16x32_bf16 v[60:63], a[40:43], a[96:99], v[60:63]\n"
"v_mfma_f32_16x16x32_bf16 v[64:67], a[40:43], a[104:107], v[64:67]\n"
"s_add_u32 s67, s75, 670720\n"
"v_mfma_f32_16x16x32_bf16 v[64:67], a[44:47], a[108:111], v[64:67]\n"
"v_mfma_f32_16x16x32_bf16 v[36:39], a[36:39], a[108:111], v[36:39]\n"
"buffer_load_dwordx4 v11, s[52:55], s67 offen offset:1024  lds\n"
"v_mfma_f32_16x16x32_bf16 v[36:39], a[32:35], a[104:107], v[36:39]\n"
"v_mfma_f32_16x16x32_bf16 v[40:43], a[32:35], a[112:115], v[40:43]\n"
"s_add_u32 s67, s75, 686080\n"
"v_mfma_f32_16x16x32_bf16 v[40:43], a[36:39], a[116:119], v[40:43]\n"
"v_mfma_f32_16x16x32_bf16 v[68:71], a[44:47], a[116:119], v[68:71]\n"
"v_mfma_f32_16x16x32_bf16 v[68:71], a[40:43], a[112:115], v[68:71]\n"
"buffer_load_dwordx4 v11, s[52:55], s67 offen offset:2048  lds\n"
"v_mfma_f32_16x16x32_bf16 v[72:75], a[40:43], a[120:123], v[72:75]\n"
"v_mfma_f32_16x16x32_bf16 v[72:75], a[44:47], a[124:127], v[72:75]\n"
"s_add_u32 s67, s75, 701440\n"
"v_mfma_f32_16x16x32_bf16 v[44:47], a[36:39], a[124:127], v[44:47]\n"
"v_mfma_f32_16x16x32_bf16 v[44:47], a[32:35], a[120:123], v[44:47]\n"
"buffer_load_dwordx4 v11, s[52:55], s67 offen offset:3072  lds\n"
"v_mfma_f32_16x16x32_bf16 v[48:51], a[32:35], a[128:131], v[48:51]\n"
"v_mfma_f32_16x16x32_bf16 v[48:51], a[36:39], a[132:135], v[48:51]\n"
"s_add_u32 m0, s64, 12288\n"
"v_mfma_f32_16x16x32_bf16 v[76:79], a[44:47], a[132:135], v[76:79]\n"
"s_waitcnt vmcnt(10)\n"
"ds_read_b64_tr_b16 a[64:65], v9 offset:0\n"
"v_mfma_f32_16x16x32_bf16 v[76:79], a[40:43], a[128:131], v[76:79]\n"
"s_add_u32 s67, s75, 720896\n"
"ds_read_b64_tr_b16 a[66:67], v9 offset:512\n"
"v_mfma_f32_16x16x32_bf16 v[80:83], a[40:43], a[136:139], v[80:83]\n"
"ds_read_b64_tr_b16 a[68:69], v9 offset:4096\n"
"v_mfma_f32_16x16x32_bf16 v[80:83], a[44:47], a[140:143], v[80:83]\n"
"ds_read_b64_tr_b16 a[70:71], v9 offset:4608\n"
"buffer_load_dwordx4 v11, s[52:55], s67 offen offset:0  lds\n"
"v_mfma_f32_16x16x32_bf16 v[52:55], a[36:39], a[140:143], v[52:55]\n"
"s_add_u32 s67, s75, 736256\n"
"ds_read_b64_tr_b16 a[72:73], v9 offset:8\n"
"v_mfma_f32_16x16x32_bf16 v[52:55], a[32:35], a[136:139], v[52:55]\n"
"s_waitcnt lgkmcnt(5)\n"
"ds_read_b64_tr_b16 a[74:75], v9 offset:520\n"
"v_mfma_f32_16x16x32_bf16 v[56:59], a[32:35], a[144:147], v[56:59]\n"
"ds_read_b64_tr_b16 a[76:77], v9 offset:4104\n"
"v_mfma_f32_16x16x32_bf16 v[56:59], a[36:39], a[148:151], v[56:59]\n"
"ds_read_b64_tr_b16 a[78:79], v9 offset:4616\n"
"v_mfma_f32_16x16x32_bf16 v[84:87], a[44:47], a[148:151], v[84:87]\n"
"buffer_load_dwordx4 v11, s[52:55], s67 offen offset:1024  lds\n"
"v_mfma_f32_16x16x32_bf16 v[84:87], a[40:43], a[144:147], v[84:87]\n"
"ds_read_b64_tr_b16 a[80:81], v15 offset:0\n"
"v_mfma_f32_16x16x32_bf16 a[4:7], a[40:43], a[152:155], a[4:7]\n"
"s_add_u32 s67, s75, 751616\n"
"ds_read_b64_tr_b16 a[82:83], v15 offset:512\n"
"v_mfma_f32_16x16x32_bf16 a[4:7], a[44:47], a[156:159], a[4:7]\n"
"ds_read_b64_tr_b16 a[84:85], v15 offset:4096\n"
"v_mfma_f32_16x16x32_bf16 a[0:3], a[36:39], a[156:159], a[0:3]\n"
"ds_read_b64_tr_b16 a[86:87], v15 offset:4608\n"
"buffer_load_dwordx4 v11, s[52:55], s67 offen offset:2048  lds\n"
"v_mfma_f32_16x16x32_bf16 a[0:3], a[32:35], a[152:155], a[0:3]\n"
"ds_read_b64_tr_b16 a[88:89], v15 offset:8\n"
"v_mfma_f32_16x16x32_bf16 v[88:91], a[48:51], a[96:99], v[88:91]\n"
"s_add_u32 s67, s75, 766976\n"
"ds_read_b64_tr_b16 a[90:91], v15 offset:520\n"
"v_mfma_f32_16x16x32_bf16 v[88:91], a[52:55], a[100:103], v[88:91]\n"
"ds_read_b64_tr_b16 a[92:93], v15 offset:4104\n"
"v_mfma_f32_16x16x32_bf16 v[116:119], a[60:63], a[100:103], v[116:119]\n"
"ds_read_b64_tr_b16 a[94:95], v15 offset:4616\n"
"v_mfma_f32_16x16x32_bf16 v[116:119], a[56:59], a[96:99], v[116:119]\n"
"buffer_load_dwordx4 v11, s[52:55], s67 offen offset:3072  lds\n"
"v_mfma_f32_16x16x32_bf16 v[120:123], a[56:59], a[104:107], v[120:123]\n"
"v_mfma_f32_16x16x32_bf16 v[120:123], a[60:63], a[108:111], v[120:123]\n"
"s_add_u32 m0, s64, 0\n"
"v_mfma_f32_16x16x32_bf16 v[92:95], a[52:55], a[108:111], v[92:95]\n"
"v_mfma_f32_16x16x32_bf16 v[92:95], a[48:51], a[104:107], v[92:95]\n"
"s_add_u32 s67, s75, 655488\n"
"v_mfma_f32_16x16x32_bf16 v[96:99], a[48:51], a[112:115], v[96:99]\n"
"s_waitcnt lgkmcnt(0)\n"
"v_mfma_f32_16x16x32_bf16 v[96:99], a[52:55], a[116:119], v[96:99]\n"
"buffer_load_dwordx4 v11, s[52:55], s67 offen offset:0  lds\n"
"v_mfma_f32_16x16x32_bf16 v[124:127], a[60:63], a[116:119], v[124:127]\n"
"v_mfma_f32_16x16x32_bf16 v[124:127], a[56:59], a[112:115], v[124:127]\n"
"s_add_u32 s67, s75, 670848\n"
"v_mfma_f32_16x16x32_bf16 v[128:131], a[56:59], a[120:123], v[128:131]\n"
"v_mfma_f32_16x16x32_bf16 v[128:131], a[60:63], a[124:127], v[128:131]\n"
"v_mfma_f32_16x16x32_bf16 v[100:103], a[52:55], a[124:127], v[100:103]\n"
"buffer_load_dwordx4 v11, s[52:55], s67 offen offset:1024  lds\n"
"v_mfma_f32_16x16x32_bf16 v[100:103], a[48:51], a[120:123], v[100:103]\n"
"v_mfma_f32_16x16x32_bf16 v[104:107], a[48:51], a[128:131], v[104:107]\n"
"s_add_u32 s67, s75, 686208\n"
"v_mfma_f32_16x16x32_bf16 v[104:107], a[52:55], a[132:135], v[104:107]\n"
"v_mfma_f32_16x16x32_bf16 v[132:135], a[60:63], a[132:135], v[132:135]\n"
"v_mfma_f32_16x16x32_bf16 v[132:135], a[56:59], a[128:131], v[132:135]\n"
"buffer_load_dwordx4 v11, s[52:55], s67 offen offset:2048  lds\n"
"v_mfma_f32_16x16x32_bf16 v[136:139], a[56:59], a[136:139], v[136:139]\n"
"v_mfma_f32_16x16x32_bf16 v[136:139], a[60:63], a[140:143], v[136:139]\n"
"s_add_u32 s67, s75, 701568\n"
"v_mfma_f32_16x16x32_bf16 v[108:111], a[52:55], a[140:143], v[108:111]\n"
"v_mfma_f32_16x16x32_bf16 v[108:111], a[48:51], a[136:139], v[108:111]\n"
"buffer_load_dwordx4 v11, s[52:55], s67 offen offset:3072  lds\n"
"v_mfma_f32_16x16x32_bf16 v[112:115], a[48:51], a[144:147], v[112:115]\n"
"v_mfma_f32_16x16x32_bf16 v[112:115], a[52:55], a[148:151], v[112:115]\n"
"s_add_u32 m0, s64, 4096\n"
"v_mfma_f32_16x16x32_bf16 v[140:143], a[60:63], a[148:151], v[140:143]\n"
"v_mfma_f32_16x16x32_bf16 v[140:143], a[56:59], a[144:147], v[140:143]\n"
"s_add_u32 s67, s75, 721024\n"
"v_mfma_f32_16x16x32_bf16 a[12:15], a[56:59], a[152:155], a[12:15]\n"
"v_mfma_f32_16x16x32_bf16 a[12:15], a[60:63], a[156:159], a[12:15]\n"
"buffer_load_dwordx4 v11, s[52:55], s67 offen offset:0  lds\n"
"v_mfma_f32_16x16x32_bf16 a[8:11], a[52:55], a[156:159], a[8:11]\n"
"v_mfma_f32_16x16x32_bf16 a[8:11], a[48:51], a[152:155], a[8:11]\n"
"s_add_u32 s67, s75, 736384\n"
"v_mfma_f32_16x16x32_bf16 v[144:147], a[64:67], a[96:99], v[144:147]\n"
"v_mfma_f32_16x16x32_bf16 v[144:147], a[68:71], a[100:103], v[144:147]\n"
"buffer_load_dwordx4 v11, s[52:55], s67 offen offset:1024  lds\n"
"v_mfma_f32_16x16x32_bf16 v[172:175], a[76:79], a[100:103], v[172:175]\n"
"v_mfma_f32_16x16x32_bf16 v[172:175], a[72:75], a[96:99], v[172:175]\n"
"s_add_u32 s67, s75, 751744\n"
"v_mfma_f32_16x16x32_bf16 v[176:179], a[72:75], a[104:107], v[176:179]\n"
"v_mfma_f32_16x16x32_bf16 v[176:179], a[76:79], a[108:111], v[176:179]\n"
"buffer_load_dwordx4 v11, s[52:55], s67 offen offset:2048  lds\n"
"v_mfma_f32_16x16x32_bf16 v[148:151], a[68:71], a[108:111], v[148:151]\n"
"v_mfma_f32_16x16x32_bf16 v[148:151], a[64:67], a[104:107], v[148:151]\n"
"s_add_u32 s67, s75, 767104\n"
"v_mfma_f32_16x16x32_bf16 v[152:155], a[64:67], a[112:115], v[152:155]\n"
"s_sub_u32 s66, 40960, s73\n"
"v_mfma_f32_16x16x32_bf16 v[152:155], a[68:71], a[116:119], v[152:155]\n"
"v_mfma_f32_16x16x32_bf16 v[180:183], a[76:79], a[116:119], v[180:183]\n"
"v_add_u32 v12, s66, v7\n"
"v_mfma_f32_16x16x32_bf16 v[180:183], a[72:75], a[112:115], v[180:183]\n"
"buffer_load_dwordx4 v11, s[52:55], s67 offen offset:3072  lds\n"
"v_mfma_f32_16x16x32_bf16 v[184:187], a[72:75], a[120:123], v[184:187]\n"
"s_add_u32 m0, s72, 98304\n"
"v_add_u32 v13, s66, v8\n"
"v_mfma_f32_16x16x32_bf16 v[184:187], a[76:79], a[124:127], v[184:187]\n"
"v_mfma_f32_16x16x32_bf16 v[156:159], a[68:71], a[124:127], v[156:159]\n"
"s_add_u32 s67, s60, 768\n"
"v_mfma_f32_16x16x32_bf16 v[156:159], a[64:67], a[120:123], v[156:159]\n"
"buffer_load_dwordx4 v6, s[48:51], s67 offen offset:0  lds\n"
"v_mfma_f32_16x16x32_bf16 v[160:163], a[64:67], a[128:131], v[160:163]\n"
"v_mfma_f32_16x16x32_bf16 v[160:163], a[68:71], a[132:135], v[160:163]\n"
"s_add_u32 s67, s60, 16128\n"
"v_mfma_f32_16x16x32_bf16 v[188:191], a[76:79], a[132:135], v[188:191]\n"
"v_mfma_f32_16x16x32_bf16 v[188:191], a[72:75], a[128:131], v[188:191]\n"
"buffer_load_dwordx4 v6, s[48:51], s67 offen offset:1024  lds\n"
"v_mfma_f32_16x16x32_bf16 v[192:195], a[72:75], a[136:139], v[192:195]\n"
"v_mfma_f32_16x16x32_bf16 v[192:195], a[76:79], a[140:143], v[192:195]\n"
"s_add_u32 s67, s60, 31488\n"
"v_mfma_f32_16x16x32_bf16 v[164:167], a[68:71], a[140:143], v[164:167]\n"
"v_mfma_f32_16x16x32_bf16 v[164:167], a[64:67], a[136:139], v[164:167]\n"
"buffer_load_dwordx4 v6, s[48:51], s67 offen offset:2048  lds\n"
"v_mfma_f32_16x16x32_bf16 v[168:171], a[64:67], a[144:147], v[168:171]\n"
"v_mfma_f32_16x16x32_bf16 v[168:171], a[68:71], a[148:151], v[168:171]\n"
"s_add_u32 s67, s60, 46848\n"
"v_mfma_f32_16x16x32_bf16 v[196:199], a[76:79], a[148:151], v[196:199]\n"
"v_mfma_f32_16x16x32_bf16 v[196:199], a[72:75], a[144:147], v[196:199]\n"
"v_mfma_f32_16x16x32_bf16 a[20:23], a[72:75], a[152:155], a[20:23]\n"
"buffer_load_dwordx4 v6, s[48:51], s67 offen offset:3072  lds\n"
"v_mfma_f32_16x16x32_bf16 a[20:23], a[76:79], a[156:159], a[20:23]\n"
"v_mfma_f32_16x16x32_bf16 a[16:19], a[68:71], a[156:159], a[16:19]\n"
"s_sub_u32 s70, 1572864, s75\n"
"s_add_u32 s70, s70, s65\n"
"v_mfma_f32_16x16x32_bf16 a[16:19], a[64:67], a[152:155], a[16:19]\n"
"s_waitcnt vmcnt(22)\n"
"s_barrier\n"
"v_mfma_f32_16x16x32_bf16 v[200:203], a[80:83], a[96:99], v[200:203]\n"
"s_waitcnt vmcnt(12)\n"
"ds_read_b64_tr_b16 a[32:33], v9 offset:8192\n"
"v_mfma_f32_16x16x32_bf16 v[200:203], a[84:87], a[100:103], v[200:203]\n"
"ds_read_b64_tr_b16 a[34:35], v9 offset:8704\n"
"v_mfma_f32_16x16x32_bf16 v[228:231], a[92:95], a[100:103], v[228:231]\n"
"ds_read_b128 a[100:103], v13 offset:16384\n"
"buffer_load_dword a252, v14, s[52:55], s70 offen\n"
"v_mfma_f32_16x16x32_bf16 v[228:231], a[88:91], a[96:99], v[228:231]\n"
"ds_read_b128 a[96:99], v12 offset:16384\n"
"v_mfma_f32_16x16x32_bf16 v[232:235], a[88:91], a[104:107], v[232:235]\n"
"buffer_store_dwordx2 v[16:17], v18, s[56:59], %11 offen sc1\n"
"ds_read_b64_tr_b16 a[36:37], v9 offset:12288\n"
"v_mfma_f32_16x16x32_bf16 v[232:235], a[92:95], a[108:111], v[232:235]\n"
"ds_read_b64_tr_b16 a[38:39], v9 offset:12800\n"
"v_mfma_f32_16x16x32_bf16 v[204:207], a[84:87], a[108:111], v[204:207]\n"
"ds_read_b128 a[108:111], v13 offset:18432\n"
"v_mfma_f32_16x16x32_bf16 v[204:207], a[80:83], a[104:107], v[204:207]\n"
"ds_read_b128 a[104:107], v12 offset:18432\n"
"buffer_store_dwordx2 v[16:17], v18, s[56:59], s69 offen sc1\n"
"ds_read_b64_tr_b16 a[40:41], v9 offset:8200\n"
"v_mfma_f32_16x16x32_bf16 v[208:211], a[80:83], a[112:115], v[208:211]\n"
"ds_read_b64_tr_b16 a[42:43], v9 offset:8712\n"
"v_mfma_f32_16x16x32_bf16 v[208:211], a[84:87], a[116:119], v[208:211]\n"
"ds_read_b64_tr_b16 a[44:45], v9 offset:12296\n"
"v_mfma_f32_16x16x32_bf16 v[236:239], a[92:95], a[116:119], v[236:239]\n"
"ds_read_b128 a[116:119], v13 offset:20480\n"
"v_mfma_f32_16x16x32_bf16 v[236:239], a[88:91], a[112:115], v[236:239]\n"
"ds_read_b128 a[112:115], v12 offset:20480\n"
"v_mfma_f32_16x16x32_bf16 v[240:243], a[88:91], a[120:123], v[240:243]\n"
"s_add_u32 m0, s64, 8192\n"
"ds_read_b64_tr_b16 a[46:47], v9 offset:12808\n"
"v_mfma_f32_16x16x32_bf16 v[240:243], a[92:95], a[124:127], v[240:243]\n"
"ds_read_b64_tr_b16 a[48:49], v15 offset:8192\n"
"v_mfma_f32_16x16x32_bf16 v[212:215], a[84:87], a[124:127], v[212:215]\n"
"ds_read_b128 a[124:127], v13 offset:22528\n"
"v_mfma_f32_16x16x32_bf16 v[212:215], a[80:83], a[120:123], v[212:215]\n"
"ds_read_b128 a[120:123], v12 offset:22528\n"
"v_mfma_f32_16x16x32_bf16 v[216:219], a[80:83], a[128:131], v[216:219]\n"
"ds_read_b64_tr_b16 a[50:51], v15 offset:8704\n"
"v_mfma_f32_16x16x32_bf16 v[216:219], a[84:87], a[132:135], v[216:219]\n"
"ds_read_b64_tr_b16 a[52:53], v15 offset:12288\n"
"v_mfma_f32_16x16x32_bf16 v[244:247], a[92:95], a[132:135], v[244:247]\n"
"ds_read_b128 a[132:135], v13 offset:24576\n"
"v_mfma_f32_16x16x32_bf16 v[244:247], a[88:91], a[128:131], v[244:247]\n"
"ds_read_b128 a[128:131], v12 offset:24576\n"
"v_mfma_f32_16x16x32_bf16 v[248:251], a[88:91], a[136:139], v[248:251]\n"
"ds_read_b64_tr_b16 a[54:55], v15 offset:12800\n"
"v_mfma_f32_16x16x32_bf16 v[248:251], a[92:95], a[140:143], v[248:251]\n"
"ds_read_b64_tr_b16 a[56:57], v15 offset:8200\n"
"v_mfma_f32_16x16x32_bf16 v[220:223], a[84:87], a[140:143], v[220:223]\n"
"s_add_u32 s67, s75, 786432\n"
"ds_read_b128 a[140:143], v13 offset:26624\n"
"v_mfma_f32_16x16x32_bf16 v[220:223], a[80:83], a[136:139], v[220:223]\n"
"ds_read_b128 a[136:139], v12 offset:26624\n"
"v_mfma_f32_16x16x32_bf16 v[224:227], a[80:83], a[144:147], v[224:227]\n"
"ds_read_b64_tr_b16 a[58:59], v15 offset:8712\n"
"v_mfma_f32_16x16x32_bf16 v[224:227], a[84:87], a[148:151], v[224:227]\n"
"ds_read_b64_tr_b16 a[60:61], v15 offset:12296\n"
"v_mfma_f32_16x16x32_bf16 v[252:255], a[92:95], a[148:151], v[252:255]\n"
"ds_read_b128 a[148:151], v13 offset:28672\n"
"v_mfma_f32_16x16x32_bf16 v[252:255], a[88:91], a[144:147], v[252:255]\n"
"ds_read_b128 a[144:147], v12 offset:28672\n"
"ds_read_b64_tr_b16 a[62:63], v15 offset:12808\n"
"v_mfma_f32_16x16x32_bf16 a[28:31], a[88:91], a[152:155], a[28:31]\n"
"v_mfma_f32_16x16x32_bf16 a[28:31], a[92:95], a[156:159], a[28:31]\n"
"v_mfma_f32_16x16x32_bf16 a[24:27], a[84:87], a[156:159], a[24:27]\n"
"ds_read_b128 a[156:159], v13 offset:30720\n"
"v_mfma_f32_16x16x32_bf16 a[24:27], a[80:83], a[152:155], a[24:27]\n"
"s_waitcnt lgkmcnt(15)\n"
"ds_read_b128 a[152:155], v12 offset:30720\n"
"v_mfma_f32_16x16x32_bf16 v[32:35], a[32:35], a[96:99], v[32:35]\n"
"L_st1588:\n"
"s_cbranch_execz L_st3861\n"
"v_mfma_f32_16x16x32_bf16 v[32:35], a[36:39], a[100:103], v[32:35]\n"
"s_waitcnt lgkmcnt(2)\n"
"v_mfma_f32_16x16x32_bf16 v[60:63], a[44:47], a[100:103], v[60:63]\n"
"buffer_load_dwordx4 v11, s[52:55], s67 offen offset:0  lds\n"
"v_mfma_f32_16x16x32_bf16 v[60:63], a[40:43], a[96:99], v[60:63]\n"
"v_mfma_f32_16x16x32_bf16 v[64:67], a[40:43], a[104:107], v[64:67]\n"
"s_add_u32 s67, s75, 801792\n"
"v_mfma_f32_16x16x32_bf16 v[64:67], a[44:47], a[108:111], v[64:67]\n"
"v_mfma_f32_16x16x32_bf16 v[36:39], a[36:39], a[108:111], v[36:39]\n"
"buffer_load_dwordx4 v11, s[52:55], s67 offen offset:1024  lds\n"
"v_mfma_f32_16x16x32_bf16 v[36:39], a[32:35], a[104:107], v[36:39]\n"
"v_mfma_f32_16x16x32_bf16 v[40:43], a[32:35], a[112:115], v[40:43]\n"
"s_add_u32 s67, s75, 817152\n"
"v_mfma_f32_16x16x32_bf16 v[40:43], a[36:39], a[116:119], v[40:43]\n"
"v_mfma_f32_16x16x32_bf16 v[68:71], a[44:47], a[116:119], v[68:71]\n"
"s_mov_b32 s30, 0\n"
"v_mfma_f32_16x16x32_bf16 v[68:71], a[40:43], a[112:115], v[68:71]\n"
"buffer_load_dwordx4 v11, s[52:55], s67 offen offset:2048  lds\n"
"v_mfma_f32_16x16x32_bf16 v[72:75], a[40:43], a[120:123], v[72:75]\n"
"s_mov_b32 s31, 0\n"
"v_mfma_f32_16x16x32_bf16 v[72:75], a[44:47], a[124:127], v[72:75]\n"
"s_add_u32 s67, s75, 832512\n"
"v_mfma_f32_16x16x32_bf16 v[44:47], a[36:39], a[124:127], v[44:47]\n"
"v_mfma_f32_16x16x32_bf16 v[44:47], a[32:35], a[120:123], v[44:47]\n"
"v_mfma_f32_16x16x32_bf16 v[48:51], a[32:35], a[128:131], v[48:51]\n"
"buffer_load_dwordx4 v11, s[52:55], s67 offen offset:3072  lds\n"
"v_mfma_f32_16x16x32_bf16 v[48:51], a[36:39], a[132:135], v[48:51]\n"
"s_add_u32 m0, s64, 12288\n"
"v_mfma_f32_16x16x32_bf16 v[76:79], a[44:47], a[132:135], v[76:79]\n"
"s_waitcnt vmcnt(11)\n"
"ds_read_b64_tr_b16 a[64:65], v9 offset:0\n"
"v_mfma_f32_16x16x32_bf16 v[76:79], a[40:43], a[128:131], v[76:79]\n"
"s_add_u32 s67, s75, 851968\n"
"ds_read_b64_tr_b16 a[66:67], v9 offset:512\n"
"v_mfma_f32_16x16x32_bf16 v[80:83], a[40:43], a[136:139], v[80:83]\n"
"ds_read_b64_tr_b16 a[68:69], v9 offset:4096\n"
"v_mfma_f32_16x16x32_bf16 v[80:83], a[44:47], a[140:143], v[80:83]\n"
"ds_read_b64_tr_b16 a[70:71], v9 offset:4608\n"
"buffer_load_dwordx4 v11, s[52:55], s67 offen offset:0  lds\n"
"v_mfma_f32_16x16x32_bf16 v[52:55], a[36:39], a[140:143], v[52:55]\n"
"s_add_u32 s67, s75, 867328\n"
"ds_read_b64_tr_b16 a[72:73], v9 offset:8\n"
"v_mfma_f32_16x16x32_bf16 v[52:55], a[32:35], a[136:139], v[52:55]\n"
"s_waitcnt lgkmcnt(5)\n"
"ds_read_b64_tr_b16 a[74:75], v9 offset:520\n"
"v_mfma_f32_16x16x32_bf16 v[56:59], a[32:35], a[144:147], v[56:59]\n"
"ds_read_b64_tr_b16 a[76:77], v9 offset:4104\n"
"v_mfma_f32_16x16x32_bf16 v[56:59], a[36:39], a[148:151], v[56:59]\n"
"ds_read_b64_tr_b16 a[78:79], v9 offset:4616\n"
"v_mfma_f32_16x16x32_bf16 v[84:87], a[44:47], a[148:151], v[84:87]\n"
"buffer_load_dwordx4 v11, s[52:55], s67 offen offset:1024  lds\n"
"v_mfma_f32_16x16x32_bf16 v[84:87], a[40:43], a[144:147], v[84:87]\n"
"ds_read_b64_tr_b16 a[80:81], v15 offset:0\n"
"v_mfma_f32_16x16x32_bf16 a[4:7], a[40:43], a[152:155], a[4:7]\n"
"s_add_u32 s67, s75, 882688\n"
"ds_read_b64_tr_b16 a[82:83], v15 offset:512\n"
"v_mfma_f32_16x16x32_bf16 a[4:7], a[44:47], a[156:159], a[4:7]\n"
"ds_read_b64_tr_b16 a[84:85], v15 offset:4096\n"
"buffer_load_dwordx4 v11, s[52:55], s67 offen offset:2048  lds\n"
"v_mfma_f32_16x16x32_bf16 a[0:3], a[36:39], a[156:159], a[0:3]\n"
"s_add_u32 s96, s56, %11\n"
"ds_read_b64_tr_b16 a[86:87], v15 offset:4608\n"
"s_addc_u32 s97, s57, 0\n"
"v_mfma_f32_16x16x32_bf16 a[0:3], a[32:35], a[152:155], a[0:3]\n"
"ds_read_b64_tr_b16 a[88:89], v15 offset:8\n"
"v_mfma_f32_16x16x32_bf16 v[88:91], a[48:51], a[96:99], v[88:91]\n"
"s_add_u32 s67, s75, 898048\n"
"ds_read_b64_tr_b16 a[90:91], v15 offset:520\n"
"v_mfma_f32_16x16x32_bf16 v[88:91], a[52:55], a[100:103], v[88:91]\n"
"ds_read_b64_tr_b16 a[92:93], v15 offset:4104\n"
"v_mfma_f32_16x16x32_bf16 v[116:119], a[60:63], a[100:103], v[116:119]\n"
"ds_read_b64_tr_b16 a[94:95], v15 offset:4616\n"
"v_mfma_f32_16x16x32_bf16 v[116:119], a[56:59], a[96:99], v[116:119]\n"
"buffer_load_dwordx4 v11, s[52:55], s67 offen offset:3072  lds\n"
"v_mfma_f32_16x16x32_bf16 v[120:123], a[56:59], a[104:107], v[120:123]\n"
"v_mfma_f32_16x16x32_bf16 v[120:123], a[60:63], a[108:111], v[120:123]\n"
"s_add_u32 m0, s64, 0\n"
"v_mfma_f32_16x16x32_bf16 v[92:95], a[52:55], a[108:111], v[92:95]\n"
"v_mfma_f32_16x16x32_bf16 v[92:95], a[48:51], a[104:107], v[92:95]\n"
"s_add_u32 s67, s75, 786560\n"
"v_mfma_f32_16x16x32_bf16 v[96:99], a[48:51], a[112:115], v[96:99]\n"
"s_waitcnt lgkmcnt(0)\n"
"v_mfma_f32_16x16x32_bf16 v[96:99], a[52:55], a[116:119], v[96:99]\n"
"buffer_load_dwordx4 v11, s[52:55], s67 offen offset:0  lds\n"
"v_mfma_f32_16x16x32_bf16 v[124:127], a[60:63], a[116:119], v[124:127]\n"
"s_load_dword s98, s[96:97], 512 glc\n"
"v_mfma_f32_16x16x32_bf16 v[124:127], a[56:59], a[112:115], v[124:127]\n"
"s_load_dword s99, s[96:97], 8704 glc\n"
"s_add_u32 s67, s75, 801920\n"
"v_mfma_f32_16x16x32_bf16 v[128:131], a[56:59], a[120:123], v[128:131]\n"
"v_mfma_f32_16x16x32_bf16 v[128:131], a[60:63], a[124:127], v[128:131]\n"
"buffer_load_dwordx4 v11, s[52:55], s67 offen offset:1024  lds\n"
"v_mfma_f32_16x16x32_bf16 v[100:103], a[52:55], a[124:127], v[100:103]\n"
"v_mfma_f32_16x16x32_bf16 v[100:103], a[48:51], a[120:123], v[100:103]\n"
"s_add_u32 s67, s75, 817280\n"
"v_mfma_f32_16x16x32_bf16 v[104:107], a[48:51], a[128:131], v[104:107]\n"
"v_mfma_f32_16x16x32_bf16 v[104:107], a[52:55], a[132:135], v[104:107]\n"
"v_mfma_f32_16x16x32_bf16 v[132:135], a[60:63], a[132:135], v[132:135]\n"
"buffer_load_dwordx4 v11, s[52:55], s67 offen offset:2048  lds\n"
"v_mfma_f32_16x16x32_bf16 v[132:135], a[56:59], a[128:131], v[132:135]\n"
"v_mfma_f32_16x16x32_bf16 v[136:139], a[56:59], a[136:139], v[136:139]\n"
"s_add_u32 s67, s75, 832640\n"
"v_mfma_f32_16x16x32_bf16 v[136:139], a[60:63], a[140:143], v[136:139]\n"
"v_mfma_f32_16x16x32_bf16 v[108:111], a[52:55], a[140:143], v[108:111]\n"
"buffer_load_dwordx4 v11, s[52:55], s67 offen offset:3072  lds\n"
"v_mfma_f32_16x16x32_bf16 v[108:111], a[48:51], a[136:139], v[108:111]\n"
"v_mfma_f32_16x16x32_bf16 v[112:115], a[48:51], a[144:147], v[112:115]\n"
"s_add_u32 m0, s64, 4096\n"
"v_mfma_f32_16x16x32_bf16 v[112:115], a[52:55], a[148:151], v[112:115]\n"
"v_mfma_f32_16x16x32_bf16 v[140:143], a[60:63], a[148:151], v[140:143]\n"
"s_add_u32 s67, s75, 852096\n"
"v_mfma_f32_16x16x32_bf16 v[140:143], a[56:59], a[144:147], v[140:143]\n"
"v_mfma_f32_16x16x32_bf16 a[12:15], a[56:59], a[152:155], a[12:15]\n"
"buffer_load_dwordx4 v11, s[52:55], s67 offen offset:0  lds\n"
"v_mfma_f32_16x16x32_bf16 a[12:15], a[60:63], a[156:159], a[12:15]\n"
"v_mfma_f32_16x16x32_bf16 a[8:11], a[52:55], a[156:159], a[8:11]\n"
"s_add_u32 s67, s75, 867456\n"
"v_mfma_f32_16x16x32_bf16 a[8:11], a[48:51], a[152:155], a[8:11]\n"
"v_mfma_f32_16x16x32_bf16 v[144:147], a[64:67], a[96:99], v[144:147]\n"
"buffer_load_dwordx4 v11, s[52:55], s67 offen offset:1024  lds\n"
"v_mfma_f32_16x16x32_bf16 v[144:147], a[68:71], a[100:103], v[144:147]\n"
"v_mfma_f32_16x16x32_bf16 v[172:175], a[76:79], a[100:103], v[172:175]\n"
"s_add_u32 s67, s75, 882816\n"
"v_mfma_f32_16x16x32_bf16 v[172:175], a[72:75], a[96:99], v[172:175]\n"
"v_mfma_f32_16x16x32_bf16 v[176:179], a[72:75], a[104:107], v[176:179]\n"
"buffer_load_dwordx4 v11, s[52:55], s67 offen offset:2048  lds\n"
"v_mfma_f32_16x16x32_bf16 v[176:179], a[76:79], a[108:111], v[176:179]\n"
"v_mfma_f32_16x16x32_bf16 v[148:151], a[68:71], a[108:111], v[148:151]\n"
"s_add_u32 s67, s75, 898176\n"
"v_mfma_f32_16x16x32_bf16 v[148:151], a[64:67], a[104:107], v[148:151]\n"
"v_mfma_f32_16x16x32_bf16 v[152:155], a[64:67], a[112:115], v[152:155]\n"
"s_sub_u32 s66, 81920, s73\n"
"v_mfma_f32_16x16x32_bf16 v[152:155], a[68:71], a[116:119], v[152:155]\n"
"buffer_load_dwordx4 v11, s[52:55], s67 offen offset:3072  lds\n"
"v_mfma_f32_16x16x32_bf16 v[180:183], a[76:79], a[116:119], v[180:183]\n"
"s_add_u32 m0, s72, 139264\n"
"v_add_u32 v12, s66, v7\n"
"v_mfma_f32_16x16x32_bf16 v[180:183], a[72:75], a[112:115], v[180:183]\n"
"v_mfma_f32_16x16x32_bf16 v[184:187], a[72:75], a[120:123], v[184:187]\n"
"s_add_u32 s67, s60, 896\n"
"v_add_u32 v13, s66, v8\n"
"v_mfma_f32_16x16x32_bf16 v[184:187], a[76:79], a[124:127], v[184:187]\n"
"v_mfma_f32_16x16x32_bf16 v[156:159], a[68:71], a[124:127], v[156:159]\n"
"buffer_load_dwordx4 v6, s[48:51], s67 offen offset:0  lds\n"
"v_mfma_f32_16x16x32_bf16 v[156:159], a[64:67], a[120:123], v[156:159]\n"
"v_mfma_f32_16x16x32_bf16 v[160:163], a[64:67], a[128:131], v[160:163]\n"
"s_add_u32 s67, s60, 16256\n"
"v_mfma_f32_16x16x32_bf16 v[160:163], a[68:71], a[132:135], v[160:163]\n"
"v_mfma_f32_16x16x32_bf16 v[188:191], a[76:79], a[132:135], v[188:191]\n"
"buffer_load_dwordx4 v6, s[48:51], s67 offen offset:1024  lds\n"
"v_mfma_f32_16x16x32_bf16 v[188:191], a[72:75], a[128:131], v[188:191]\n"
"v_mfma_f32_16x16x32_bf16 v[192:195], a[72:75], a[136:139], v[192:195]\n"
"s_add_u32 s67, s60, 31616\n"
"v_mfma_f32_16x16x32_bf16 v[192:195], a[76:79], a[140:143], v[192:195]\n"
"v_mfma_f32_16x16x32_bf16 v[164:167], a[68:71], a[140:143], v[164:167]\n"
"v_mfma_f32_16x16x32_bf16 v[164:167], a[64:67], a[136:139], v[164:167]\n"
"buffer_load_dwordx4 v6, s[48:51], s67 offen offset:2048  lds\n"
"v_mfma_f32_16x16x32_bf16 v[168:171], a[64:67], a[144:147], v[168:171]\n"
"v_mfma_f32_16x16x32_bf16 v[168:171], a[68:71], a[148:151], v[168:171]\n"
"s_add_u32 s67, s60, 46976\n"
"v_mfma_f32_16x16x32_bf16 v[196:199], a[76:79], a[148:151], v[196:199]\n"
"v_mfma_f32_16x16x32_bf16 v[196:199], a[72:75], a[144:147], v[196:199]\n"
"buffer_load_dwordx4 v6, s[48:51], s67 offen offset:3072  lds\n"
"v_mfma_f32_16x16x32_bf16 a[20:23], a[72:75], a[152:155], a[20:23]\n"
"v_mfma_f32_16x16x32_bf16 a[20:23], a[76:79], a[156:159], a[20:23]\n"
"s_waitcnt lgkmcnt(0)\n"
"v_mfma_f32_16x16x32_bf16 a[16:19], a[68:71], a[156:159], a[16:19]\n"
"v_mfma_f32_16x16x32_bf16 a[16:19], a[64:67], a[152:155], a[16:19]\n"
"s_waitcnt vmcnt(23)\n"
"s_barrier\n"
"v_mfma_f32_16x16x32_bf16 v[200:203], a[80:83], a[96:99], v[200:203]\n"
"s_waitcnt vmcnt(12)\n"
"ds_read_b64_tr_b16 a[32:33], v9 offset:8192\n"
"v_mfma_f32_16x16x32_bf16 v[200:203], a[84:87], a[100:103], v[200:203]\n"
"ds_read_b64_tr_b16 a[34:35], v9 offset:8704\n"
"v_mfma_f32_16x16x32_bf16 v[228:231], a[92:95], a[100:103], v[228:231]\n"
"s_add_u32 m0, s64, 8192\n"
"ds_read_b128 a[100:103], v13 offset:16384\n"
"v_mfma_f32_16x16x32_bf16 v[228:231], a[88:91], a[96:99], v[228:231]\n"
"ds_read_b128 a[96:99], v12 offset:16384\n"
"v_mfma_f32_16x16x32_bf16 v[232:235], a[88:91], a[104:107], v[232:235]\n"
"ds_read_b64_tr_b16 a[36:37], v9 offset:12288\n"
"v_mfma_f32_16x16x32_bf16 v[232:235], a[92:95], a[108:111], v[232:235]\n"
"ds_read_b64_tr_b16 a[38:39], v9 offset:12800\n"
"v_mfma_f32_16x16x32_bf16 v[204:207], a[84:87], a[108:111], v[204:207]\n"
"ds_read_b128 a[108:111], v13 offset:18432\n"
"v_mfma_f32_16x16x32_bf16 v[204:207], a[80:83], a[104:107], v[204:207]\n"
"ds_read_b128 a[104:107], v12 offset:18432\n"
"ds_read_b64_tr_b16 a[40:41], v9 offset:8200\n"
"v_mfma_f32_16x16x32_bf16 v[208:211], a[80:83], a[112:115], v[208:211]\n"
"ds_read_b64_tr_b16 a[42:43], v9 offset:8712\n"
"v_mfma_f32_16x16x32_bf16 v[208:211], a[84:87], a[116:119], v[208:211]\n"
"ds_read_b64_tr_b16 a[44:45], v9 offset:12296\n"
"v_mfma_f32_16x16x32_bf16 v[236:239], a[92:95], a[116:119], v[236:239]\n"
"ds_read_b128 a[116:119], v13 offset:20480\n"
"v_mfma_f32_16x16x32_bf16 v[236:239], a[88:91], a[112:115], v[236:239]\n"
"ds_read_b128 a[112:115], v12 offset:20480\n"
"v_mfma_f32_16x16x32_bf16 v[240:243], a[88:91], a[120:123], v[240:243]\n"
"ds_read_b64_tr_b16 a[46:47], v9 offset:12808\n"
"v_mfma_f32_16x16x32_bf16 v[240:243], a[92:95], a[124:127], v[240:243]\n"
"ds_read_b64_tr_b16 a[48:49], v15 offset:8192\n"
"v_mfma_f32_16x16x32_bf16 v[212:215], a[84:87], a[124:127], v[212:215]\n"
"ds_read_b128 a[124:127], v13 offset:22528\n"
"v_mfma_f32_16x16x32_bf16 v[212:215], a[80:83], a[120:123], v[212:215]\n"
"ds_read_b128 a[120:123], v12 offset:22528\n"
"v_mfma_f32_16x16x32_bf16 v[216:219], a[80:83], a[128:131], v[216:219]\n"
"s_add_u32 s67, s75, 917504\n"
"ds_read_b64_tr_b16 a[50:51], v15 offset:8704\n"
"v_mfma_f32_16x16x32_bf16 v[216:219], a[84:87], a[132:135], v[216:219]\n"
"ds_read_b64_tr_b16 a[52:53], v15 offset:12288\n"
"v_mfma_f32_16x16x32_bf16 v[244:247], a[92:95], a[132:135], v[244:247]\n"
"ds_read_b128 a[132:135], v13 offset:24576\n"
"v_mfma_f32_16x16x32_bf16 v[244:247], a[88:91], a[128:131], v[244:247]\n"
"ds_read_b128 a[128:131], v12 offset:24576\n"
"v_mfma_f32_16x16x32_bf16 v[248:251], a[88:91], a[136:139], v[248:251]\n"
"ds_read_b64_tr_b16 a[54:55], v15 offset:12800\n"
"v_mfma_f32_16x16x32_bf16 v[248:251], a[92:95], a[140:143], v[248:251]\n"
"ds_read_b64_tr_b16 a[56:57], v15 offset:8200\n"
"v_mfma_f32_16x16x32_bf16 v[220:223], a[84:87], a[140:143], v[220:223]\n"
"ds_read_b128 a[140:143], v13 offset:26624\n"
"v_mfma_f32_16x16x32_bf16 v[220:223], a[80:83], a[136:139], v[220:223]\n"
"ds_read_b128 a[136:139], v12 offset:26624\n"
"v_mfma_f32_16x16x32_bf16 v[224:227], a[80:83], a[144:147], v[224:227]\n"
"ds_read_b64_tr_b16 a[58:59], v15 offset:8712\n"
"v_mfma_f32_16x16x32_bf16 v[224:227], a[84:87], a[148:151], v[224:227]\n"
"ds_read_b64_tr_b16 a[60:61], v15 offset:12296\n"
"v_mfma_f32_16x16x32_bf16 v[252:255], a[92:95], a[148:151], v[252:255]\n"
"ds_read_b128 a[148:151], v13 offset:28672\n"
"v_mfma_f32_16x16x32_bf16 v[252:255], a[88:91], a[144:147], v[252:255]\n"
"ds_read_b128 a[144:147], v12 offset:28672\n"
"ds_read_b64_tr_b16 a[62:63], v15 offset:12808\n"
"v_mfma_f32_16x16x32_bf16 a[28:31], a[88:91], a[152:155], a[28:31]\n"
"v_mfma_f32_16x16x32_bf16 a[28:31], a[92:95], a[156:159], a[28:31]\n"
"v_mfma_f32_16x16x32_bf16 a[24:27], a[84:87], a[156:159], a[24:27]\n"
"ds_read_b128 a[156:159], v13 offset:30720\n"
"v_mfma_f32_16x16x32_bf16 a[24:27], a[80:83], a[152:155], a[24:27]\n"
"s_waitcnt lgkmcnt(15)\n"
"ds_read_b128 a[152:155], v12 offset:30720\n"
"v_mfma_f32_16x16x32_bf16 v[32:35], a[32:35], a[96:99], v[32:35]\n"
"v_mfma_f32_16x16x32_bf16 v[32:35], a[36:39], a[100:103], v[32:35]\n"
"s_waitcnt lgkmcnt(2)\n"
"L_H6S:\n"
"v_mfma_f32_16x16x32_bf16 v[60:63], a[44:47], a[100:103], v[60:63]\n"
"buffer_load_dwordx4 v11, s[52:55], s67 offen offset:0  lds\n"
"v_mfma_f32_16x16x32_bf16 v[60:63], a[40:43], a[96:99], v[60:63]\n"
"v_mfma_f32_16x16x32_bf16 v[64:67], a[40:43], a[104:107], v[64:67]\n"
"s_add_u32 s67, s75, 932864\n"
"v_mfma_f32_16x16x32_bf16 v[64:67], a[44:47], a[108:111], v[64:67]\n"
"v_mfma_f32_16x16x32_bf16 v[36:39], a[36:39], a[108:111], v[36:39]\n"
"buffer_load_dwordx4 v11, s[52:55], s67 offen offset:1024  lds\n"
"v_mfma_f32_16x16x32_bf16 v[36:39], a[32:35], a[104:107], v[36:39]\n"
"v_mfma_f32_16x16x32_bf16 v[40:43], a[32:35], a[112:115], v[40:43]\n"
"s_add_u32 s67, s75, 948224\n"
"v_mfma_f32_16x16x32_bf16 v[40:43], a[36:39], a[116:119], v[40:43]\n"
"v_mfma_f32_16x16x32_bf16 v[68:71], a[44:47], a[116:119], v[68:71]\n"
"v_mfma_f32_16x16x32_bf16 v[68:71], a[40:43], a[112:115], v[68:71]\n"
"buffer_load_dwordx4 v11, s[52:55], s67 offen offset:2048  lds\n"
"v_mfma_f32_16x16x32_bf16 v[72:75], a[40:43], a[120:123], v[72:75]\n"
"v_mfma_f32_16x16x32_bf16 v[72:75], a[44:47], a[124:127], v[72:75]\n"
"s_add_u32 s67, s75, 963584\n"
"v_mfma_f32_16x16x32_bf16 v[44:47], a[36:39], a[124:127], v[44:47]\n"
"v_mfma_f32_16x16x32_bf16 v[44:47], a[32:35], a[120:123], v[44:47]\n"
"v_mfma_f32_16x16x32_bf16 v[48:51], a[32:35], a[128:131], v[48:51]\n"
"buffer_load_dwordx4 v11, s[52:55], s67 offen offset:3072  lds\n"
"v_mfma_f32_16x16x32_bf16 v[48:51], a[36:39], a[132:135], v[48:51]\n"
"s_add_u32 m0, s64, 12288\n"
"v_mfma_f32_16x16x32_bf16 v[76:79], a[44:47], a[132:135], v[76:79]\n"
"s_waitcnt vmcnt(8)\n"
"ds_read_b64_tr_b16 a[64:65], v9 offset:0\n"
"v_mfma_f32_16x16x32_bf16 v[76:79], a[40:43], a[128:131], v[76:79]\n"
"s_add_u32 s67, s75, 983040\n"
"ds_read_b64_tr_b16 a[66:67], v9 offset:512\n"
"v_mfma_f32_16x16x32_bf16 v[80:83], a[40:43], a[136:139], v[80:83]\n"
"ds_read_b64_tr_b16 a[68:69], v9 offset:4096\n"
"v_mfma_f32_16x16x32_bf16 v[80:83], a[44:47], a[140:143], v[80:83]\n"
"ds_read_b64_tr_b16 a[70:71], v9 offset:4608\n"
"buffer_load_dwordx4 v11, s[52:55], s67 offen offset:0  lds\n"
"v_mfma_f32_16x16x32_bf16 v[52:55], a[36:39], a[140:143], v[52:55]\n"
"s_add_u32 s67, s75, 998400\n"
"ds_read_b64_tr_b16 a[72:73], v9 offset:8\n"
"v_mfma_f32_16x16x32_bf16 v[52:55], a[32:35], a[136:139], v[52:55]\n"
"s_waitcnt lgkmcnt(5)\n"
"ds_read_b64_tr_b16 a[74:75], v9 offset:520\n"
"v_mfma_f32_16x16x32_bf16 v[56:59], a[32:35], a[144:147], v[56:59]\n"
"ds_read_b64_tr_b16 a[76:77], v9 offset:4104\n"
"v_mfma_f32_16x16x32_bf16 v[56:59], a[36:39], a[148:151], v[56:59]\n"
"ds_read_b64_tr_b16 a[78:79], v9 offset:4616\n"
"v_mfma_f32_16x16x32_bf16 v[84:87], a[44:47], a[148:151], v[84:87]\n"
"buffer_load_dwordx4 v11, s[52:55], s67 offen offset:1024  lds\n"
"v_mfma_f32_16x16x32_bf16 v[84:87], a[40:43], a[144:147], v[84:87]\n"
"ds_read_b64_tr_b16 a[80:81], v15 offset:0\n"
"v_mfma_f32_16x16x32_bf16 a[4:7], a[40:43], a[152:155], a[4:7]\n"
"s_add_u32 s67, s75, 1013760\n"
"ds_read_b64_tr_b16 a[82:83], v15 offset:512\n"
"v_mfma_f32_16x16x32_bf16 a[4:7], a[44:47], a[156:159], a[4:7]\n"
"ds_read_b64_tr_b16 a[84:85], v15 offset:4096\n"
"v_mfma_f32_16x16x32_bf16 a[0:3], a[36:39], a[156:159], a[0:3]\n"
"ds_read_b64_tr_b16 a[86:87], v15 offset:4608\n"
"buffer_load_dwordx4 v11, s[52:55], s67 offen offset:2048  lds\n"
"v_mfma_f32_16x16x32_bf16 a[0:3], a[32:35], a[152:155], a[0:3]\n"
"ds_read_b64_tr_b16 a[88:89], v15 offset:8\n"
"v_mfma_f32_16x16x32_bf16 v[88:91], a[48:51], a[96:99], v[88:91]\n"
"s_add_u32 s67, s75, 1029120\n"
"ds_read_b64_tr_b16 a[90:91], v15 offset:520\n"
"v_mfma_f32_16x16x32_bf16 v[88:91], a[52:55], a[100:103], v[88:91]\n"
"ds_read_b64_tr_b16 a[92:93], v15 offset:4104\n"
"v_mfma_f32_16x16x32_bf16 v[116:119], a[60:63], a[100:103], v[116:119]\n"
"ds_read_b64_tr_b16 a[94:95], v15 offset:4616\n"
"v_mfma_f32_16x16x32_bf16 v[116:119], a[56:59], a[96:99], v[116:119]\n"
"buffer_load_dwordx4 v11, s[52:55], s67 offen offset:3072  lds\n"
"v_mfma_f32_16x16x32_bf16 v[120:123], a[56:59], a[104:107], v[120:123]\n"
"v_mfma_f32_16x16x32_bf16 v[120:123], a[60:63], a[108:111], v[120:123]\n"
"s_add_u32 m0, s64, 0\n"
"v_mfma_f32_16x16x32_bf16 v[92:95], a[52:55], a[108:111], v[92:95]\n"
"v_mfma_f32_16x16x32_bf16 v[92:95], a[48:51], a[104:107], v[92:95]\n"
"s_add_u32 s67, s75, 917632\n"
"v_mfma_f32_16x16x32_bf16 v[96:99], a[48:51], a[112:115], v[96:99]\n"
"s_waitcnt lgkmcnt(0)\n"
"s_mov_b32 s99, 0\n"
"v_mfma_f32_16x16x32_bf16 v[96:99], a[52:55], a[116:119], v[96:99]\n"
"s_mov_b32 s98, 0\n"
"buffer_load_dwordx4 v11, s[52:55], s67 offen offset:0  lds\n"
"v_mfma_f32_16x16x32_bf16 v[124:127], a[60:63], a[116:119], v[124:127]\n"
"s_memtime s[76:77]\n"
"s_load_dword s98, s[96:97], 8192 glc\n"
"v_mfma_f32_16x16x32_bf16 v[124:127], a[56:59], a[112:115], v[124:127]\n"
"s_add_u32 s67, s75, 932992\n"
"v_mfma_f32_16x16x32_bf16 v[128:131], a[56:59], a[120:123], v[128:131]\n"
"v_mfma_f32_16x16x32_bf16 v[128:131], a[60:63], a[124:127], v[128:131]\n"
"buffer_load_dwordx4 v11, s[52:55], s67 offen offset:1024  lds\n"
"v_mfma_f32_16x16x32_bf16 v[100:103], a[52:55], a[124:127], v[100:103]\n"
"v_mfma_f32_16x16x32_bf16 v[100:103], a[48:51], a[120:123], v[100:103]\n"
"s_add_u32 s67, s75, 948352\n"
"v_mfma_f32_16x16x32_bf16 v[104:107], a[48:51], a[128:131], v[104:107]\n"
"v_mfma_f32_16x16x32_bf16 v[104:107], a[52:55], a[132:135], v[104:107]\n"
"v_mfma_f32_16x16x32_bf16 v[132:135], a[60:63], a[132:135], v[132:135]\n"
"buffer_load_dwordx4 v11, s[52:55], s67 offen offset:2048  lds\n"
"v_mfma_f32_16x16x32_bf16 v[132:135], a[56:59], a[128:131], v[132:135]\n"
"s_mov_b32 s29, s76\n"
"v_mfma_f32_16x16x32_bf16 v[136:139], a[56:59], a[136:139], v[136:139]\n"
"s_add_u32 s67, s75, 963712\n"
"v_mfma_f32_16x16x32_bf16 v[136:139], a[60:63], a[140:143], v[136:139]\n"
"buffer_load_dwordx4 v11, s[52:55], s67 offen offset:3072  lds\n"
"v_mfma_f32_16x16x32_bf16 v[108:111], a[52:55], a[140:143], v[108:111]\n"
"s_sub_u32 s99, s99, s98\n"
"s_sub_u32 s99, s99, s98\n"
"v_mfma_f32_16x16x32_bf16 v[108:111], a[48:51], a[136:139], v[108:111]\n"
"s_sub_u32 s99, s99, s98\n"
"s_add_u32 m0, s64, 4096\n"
"v_mfma_f32_16x16x32_bf16 v[112:115], a[48:51], a[144:147], v[112:115]\n"
"s_sub_u32 s99, s99, s98\n"
"v_mfma_f32_16x16x32_bf16 v[112:115], a[52:55], a[148:151], v[112:115]\n"
"s_add_u32 s67, s75, 983168\n"
"v_mfma_f32_16x16x32_bf16 v[140:143], a[60:63], a[148:151], v[140:143]\n"
"s_sub_u32 s99, s99, s98\n"
"s_sub_u32 s99, s99, s98\n"
"v_mfma_f32_16x16x32_bf16 v[140:143], a[56:59], a[144:147], v[140:143]\n"
"s_sub_u32 s99, s99, s98\n"
"buffer_load_dwordx4 v11, s[52:55], s67 offen offset:0  lds\n"
"v_mfma_f32_16x16x32_bf16 a[12:15], a[56:59], a[152:155], a[12:15]\n"
"s_sub_u32 s99, s99, s98\n"
"v_mfma_f32_16x16x32_bf16 a[12:15], a[60:63], a[156:159], a[12:15]\n"
"s_add_u32 s67, s75, 998528\n"
"v_mfma_f32_16x16x32_bf16 a[8:11], a[52:55], a[156:159], a[8:11]\n"
"s_sub_u32 s99, s99, s98\n"
"s_sub_u32 s99, s99, s98\n"
"v_mfma_f32_16x16x32_bf16 a[8:11], a[48:51], a[152:155], a[8:11]\n"
"s_sub_u32 s99, s99, s98\n"
"v_mfma_f32_16x16x32_bf16 v[144:147], a[64:67], a[96:99], v[144:147]\n"
"s_sub_u32 s99, s99, s98\n"
"v_mfma_f32_16x16x32_bf16 v[144:147], a[68:71], a[100:103], v[144:147]\n"
"buffer_load_dwordx4 v11, s[52:55], s67 offen offset:1024  lds\n"
"v_mfma_f32_16x16x32_bf16 v[172:175], a[76:79], a[100:103], v[172:175]\n"
"s_sub_u32 s99, s99, s98\n"
"s_sub_u32 s99, s99, s98\n"
"v_mfma_f32_16x16x32_bf16 v[172:175], a[72:75], a[96:99], v[172:175]\n"
"s_sub_u32 s99, s99, s98\n"
"s_add_u32 s67, s75, 1013888\n"
"v_mfma_f32_16x16x32_bf16 v[176:179], a[72:75], a[104:107], v[176:179]\n"
"s_sub_u32 s99, s99, s98\n"
"v_mfma_f32_16x16x32_bf16 v[176:179], a[76:79], a[108:111], v[176:179]\n"
"buffer_load_dwordx4 v11, s[52:55], s67 offen offset:2048  lds\n"
"v_mfma_f32_16x16x32_bf16 v[148:151], a[68:71], a[108:111], v[148:151]\n"
"s_sub_u32 s99, s99, s98\n"
"s_sub_u32 s99, s99, s98\n"
"v_mfma_f32_16x16x32_bf16 v[148:151], a[64:67], a[104:107], v[148:151]\n"
"s_sub_u32 s99, s99, s98\n"
"s_add_u32 s67, s75, 1029248\n"
"v_mfma_f32_16x16x32_bf16 v[152:155], a[64:67], a[112:115], v[152:155]\n"
"s_sub_u32 s66, 122880, s73\n"
"s_sub_u32 s99, s99, s98\n"
"v_mfma_f32_16x16x32_bf16 v[152:155], a[68:71], a[116:119], v[152:155]\n"
"buffer_load_dwordx4 v11, s[52:55], s67 offen offset:3072  lds\n"
"v_mfma_f32_16x16x32_bf16 v[180:183], a[76:79], a[116:119], v[180:183]\n"
"s_sub_u32 s99, s99, s98\n"
"v_add_u32 v12, s66, v7\n"
"s_sub_u32 s99, s99, s98\n"
"v_mfma_f32_16x16x32_bf16 v[180:183], a[72:75], a[112:115], v[180:183]\n"
"s_sub_u32 s99, s99, s98\n"
"s_cmp_lt_u32 %16, 2\n"
"s_cselect_b32 s67, 16384, 0\n"
"v_mfma_f32_16x16x32_bf16 v[184:187], a[72:75], a[120:123], v[184:187]\n"
"s_sub_u32 s99, s99, s98\n"
"v_add_u32 v13, s66, v8\n"
"s_add_u32 s67, s67, s72\n"
"v_mfma_f32_16x16x32_bf16 v[184:187], a[76:79], a[124:127], v[184:187]\n"
"s_memtime s[76:77]\n"
"s_add_u32 m0, s67, 16384\n"
"s_sub_u32 s67, 1024, s60\n"
"v_mfma_f32_16x16x32_bf16 v[156:159], a[68:71], a[124:127], v[156:159]\n"
"buffer_load_dwordx4 v6, s[48:51], s67 offen offset:0  lds\n"
"v_mfma_f32_16x16x32_bf16 v[156:159], a[64:67], a[120:123], v[156:159]\n"
"s_waitcnt lgkmcnt(0)\n"
"v_mfma_f32_16x16x32_bf16 v[160:163], a[64:67], a[128:131], v[160:163]\n"
"s_sub_u32 s29, s76, s29\n"
"s_sub_u32 s67, 16384, s60\n"
"v_mfma_f32_16x16x32_bf16 v[160:163], a[68:71], a[132:135], v[160:163]\n"
"s_cmp_lt_u32 s29, 620\n"
"s_cselect_b32 s99, s99, 0\n"
"v_mfma_f32_16x16x32_bf16 v[188:191], a[76:79], a[132:135], v[188:191]\n"
"s_max_u32 s31, s31, s99\n"
"v_mfma_f32_16x16x32_bf16 v[188:191], a[72:75], a[128:131], v[188:191]\n"
"buffer_load_dwordx4 v6, s[48:51], s67 offen offset:1024  lds\n"
"v_mfma_f32_16x16x32_bf16 v[192:195], a[72:75], a[136:139], v[192:195]\n"
"v_mfma_f32_16x16x32_bf16 v[192:195], a[76:79], a[140:143], v[192:195]\n"
"s_sub_u32 s67, 31744, s60\n"
"v_mfma_f32_16x16x32_bf16 v[164:167], a[68:71], a[140:143], v[164:167]\n"
"v_mfma_f32_16x16x32_bf16 v[164:167], a[64:67], a[136:139], v[164:167]\n"
"buffer_load_dwordx4 v6, s[48:51], s67 offen offset:2048  lds\n"
"v_mfma_f32_16x16x32_bf16 v[168:171], a[64:67], a[144:147], v[168:171]\n"
"v_mfma_f32_16x16x32_bf16 v[168:171], a[68:71], a[148:151], v[168:171]\n"
"s_sub_u32 s67, 47104, s60\n"
"v_mfma_f32_16x16x32_bf16 v[196:199], a[76:79], a[148:151], v[196:199]\n"
"v_mfma_f32_16x16x32_bf16 v[196:199], a[72:75], a[144:147], v[196:199]\n"
"buffer_load_dwordx4 v6, s[48:51], s67 offen offset:3072  lds\n"
"v_mfma_f32_16x16x32_bf16 a[20:23], a[72:75], a[152:155], a[20:23]\n"
"v_mfma_f32_16x16x32_bf16 a[20:23], a[76:79], a[156:159], a[20:23]\n"
"s_add_u32 m0, s72, 57216\n"
"v_mfma_f32_16x16x32_bf16 a[16:19], a[68:71], a[156:159], a[16:19]\n"
"v_mfma_f32_16x16x32_bf16 a[16:19], a[64:67], a[152:155], a[16:19]\n"
"s_waitcnt vmcnt(20)\n"
"s_barrier\n"
"v_mfma_f32_16x16x32_bf16 v[200:203], a[80:83], a[96:99], v[200:203]\n"
"s_waitcnt vmcnt(12)\n"
"ds_read_b64_tr_b16 a[32:33], v9 offset:8192\n"
"buffer_load_dwordx4 v6, s[40:43], s80 offen offset:128  lds\n"
"v_mfma_f32_16x16x32_bf16 v[200:203], a[84:87], a[100:103], v[200:203]\n"
"ds_read_b64_tr_b16 a[34:35], v9 offset:8704\n"
"v_mfma_f32_16x16x32_bf16 v[228:231], a[92:95], a[100:103], v[228:231]\n"
"ds_read_b128 a[100:103], v13 offset:16384\n"
"v_mfma_f32_16x16x32_bf16 v[228:231], a[88:91], a[96:99], v[228:231]\n"
"ds_read_b128 a[96:99], v12 offset:16384\n"
"buffer_load_dwordx4 v6, s[40:43], s81 offen offset:1152  lds\n"
"v_mfma_f32_16x16x32_bf16 v[232:235], a[88:91], a[104:107], v[232:235]\n"
"ds_read_b64_tr_b16 a[36:37], v9 offset:12288\n"
"v_mfma_f32_16x16x32_bf16 v[232:235], a[92:95], a[108:111], v[232:235]\n"
"ds_read_b64_tr_b16 a[38:39], v9 offset:12800\n"
"v_mfma_f32_16x16x32_bf16 v[204:207], a[84:87], a[108:111], v[204:207]\n"
"ds_read_b128 a[108:111], v13 offset:18432\n"
"v_mfma_f32_16x16x32_bf16 v[204:207], a[80:83], a[104:107], v[204:207]\n"
"ds_read_b128 a[104:107], v12 offset:18432\n"
"ds_read_b64_tr_b16 a[40:41], v9 offset:8200\n"
"v_mfma_f32_16x16x32_bf16 v[208:211], a[80:83], a[112:115], v[208:211]\n"
"ds_read_b64_tr_b16 a[42:43], v9 offset:8712\n"
"buffer_load_dwordx4 v6, s[40:43], s82 offen offset:2176  lds\n"
"v_mfma_f32_16x16x32_bf16 v[208:211], a[84:87], a[116:119], v[208:211]\n"
"ds_read_b64_tr_b16 a[44:45], v9 offset:12296\n"
"v_mfma_f32_16x16x32_bf16 v[236:239], a[92:95], a[116:119], v[236:239]\n"
"ds_read_b128 a[116:119], v13 offset:20480\n"
"v_mfma_f32_16x16x32_bf16 v[236:239], a[88:91], a[112:115], v[236:239]\n"
"ds_read_b128 a[112:115], v12 offset:20480\n"
"v_mfma_f32_16x16x32_bf16 v[240:243], a[88:91], a[120:123], v[240:243]\n"
"ds_read_b64_tr_b16 a[46:47], v9 offset:12808\n"
"v_mfma_f32_16x16x32_bf16 v[240:243], a[92:95], a[124:127], v[240:243]\n"
"ds_read_b64_tr_b16 a[48:49], v15 offset:8192\n"
"buffer_load_dwordx4 v6, s[40:43], s83 offen offset:3200  lds\n"
"v_mfma_f32_16x16x32_bf16 v[212:215], a[84:87], a[124:127], v[212:215]\n"
"ds_read_b128 a[124:127], v13 offset:22528\n"
"v_mfma_f32_16x16x32_bf16 v[212:215], a[80:83], a[120:123], v[212:215]\n"
"ds_read_b128 a[120:123], v12 offset:22528\n"
"v_mfma_f32_16x16x32_bf16 v[216:219], a[80:83], a[128:131], v[216:219]\n"
"ds_read_b64_tr_b16 a[50:51], v15 offset:8704\n"
"v_mfma_f32_16x16x32_bf16 v[216:219], a[84:87], a[132:135], v[216:219]\n"
"s_add_u32 m0, s64, 8192\n"
"ds_read_b64_tr_b16 a[52:53], v15 offset:12288\n"
"v_mfma_f32_16x16x32_bf16 v[244:247], a[92:95], a[132:135], v[244:247]\n"
"ds_read_b128 a[132:135], v13 offset:24576\n"
"v_mfma_f32_16x16x32_bf16 v[244:247], a[88:91], a[128:131], v[244:247]\n"
"ds_read_b128 a[128:131], v12 offset:24576\n"
"v_mfma_f32_16x16x32_bf16 v[248:251], a[88:91], a[136:139], v[248:251]\n"
"ds_read_b64_tr_b16 a[54:55], v15 offset:12800\n"
"v_mfma_f32_16x16x32_bf16 v[248:251], a[92:95], a[140:143], v[248:251]\n"
"ds_read_b64_tr_b16 a[56:57], v15 offset:8200\n"
"v_mfma_f32_16x16x32_bf16 v[220:223], a[84:87], a[140:143], v[220:223]\n"
"ds_read_b128 a[140:143], v13 offset:26624\n"
"v_mfma_f32_16x16x32_bf16 v[220:223], a[80:83], a[136:139], v[220:223]\n"
"ds_read_b128 a[136:139], v12 offset:26624\n"
"v_mfma_f32_16x16x32_bf16 v[224:227], a[80:83], a[144:147], v[224:227]\n"
"ds_read_b64_tr_b16 a[58:59], v15 offset:8712\n"
"v_mfma_f32_16x16x32_bf16 v[224:227], a[84:87], a[148:151], v[224:227]\n"
"s_sub_u32 s67, 1048576, s75\n"
"ds_read_b64_tr_b16 a[60:61], v15 offset:12296\n"
"v_mfma_f32_16x16x32_bf16 v[252:255], a[92:95], a[148:151], v[252:255]\n"
"ds_read_b128 a[148:151], v13 offset:28672\n"
"v_mfma_f32_16x16x32_bf16 v[252:255], a[88:91], a[144:147], v[252:255]\n"
"ds_read_b128 a[144:147], v12 offset:28672\n"
"ds_read_b64_tr_b16 a[62:63], v15 offset:12808\n"
"v_mfma_f32_16x16x32_bf16 a[28:31], a[88:91], a[152:155], a[28:31]\n"
"v_mfma_f32_16x16x32_bf16 a[28:31], a[92:95], a[156:159], a[28:31]\n"
"v_mfma_f32_16x16x32_bf16 a[24:27], a[84:87], a[156:159], a[24:27]\n"
"ds_read_b128 a[156:159], v13 offset:30720\n"
"v_mfma_f32_16x16x32_bf16 a[24:27], a[80:83], a[152:155], a[24:27]\n"
"s_waitcnt lgkmcnt(15)\n"
"ds_read_b128 a[152:155], v12 offset:30720\n"
"v_mfma_f32_16x16x32_bf16 v[32:35], a[32:35], a[96:99], v[32:35]\n"
"v_mfma_f32_16x16x32_bf16 v[32:35], a[36:39], a[100:103], v[32:35]\n"
"s_waitcnt lgkmcnt(2)\n"
"v_mfma_f32_16x16x32_bf16 v[60:63], a[44:47], a[100:103], v[60:63]\n"
"buffer_load_dwordx4 v11, s[52:55], s67 offen offset:0  lds\n"
"v_mfma_f32_16x16x32_bf16 v[60:63], a[40:43], a[96:99], v[60:63]\n"
"v_mfma_f32_16x16x32_bf16 v[64:67], a[40:43], a[104:107], v[64:67]\n"
"s_sub_u32 s67, 1063936, s75\n"
"v_mfma_f32_16x16x32_bf16 v[64:67], a[44:47], a[108:111], v[64:67]\n"
"v_mfma_f32_16x16x32_bf16 v[36:39], a[36:39], a[108:111], v[36:39]\n"
"v_mfma_f32_16x16x32_bf16 v[36:39], a[32:35], a[104:107], v[36:39]\n"
"buffer_load_dwordx4 v11, s[52:55], s67 offen offset:1024  lds\n"
"v_mfma_f32_16x16x32_bf16 v[40:43], a[32:35], a[112:115], v[40:43]\n"
"v_mfma_f32_16x16x32_bf16 v[40:43], a[36:39], a[116:119], v[40:43]\n"
"s_sub_u32 s67, 1079296, s75\n"
"v_mfma_f32_16x16x32_bf16 v[68:71], a[44:47], a[116:119], v[68:71]\n"
"v_mfma_f32_16x16x32_bf16 v[68:71], a[40:43], a[112:115], v[68:71]\n"
"buffer_load_dwordx4 v11, s[52:55], s67 offen offset:2048  lds\n"
"v_mfma_f32_16x16x32_bf16 v[72:75], a[40:43], a[120:123], v[72:75]\n"
"v_mfma_f32_16x16x32_bf16 v[72:75], a[44:47], a[124:127], v[72:75]\n"
"s_sub_u32 s67, 1094656, s75\n"
"v_mfma_f32_16x16x32_bf16 v[44:47], a[36:39], a[124:127], v[44:47]\n"
"v_mfma_f32_16x16x32_bf16 v[44:47], a[32:35], a[120:123], v[44:47]\n"
"v_mfma_f32_16x16x32_bf16 v[48:51], a[32:35], a[128:131], v[48:51]\n"
"buffer_load_dwordx4 v11, s[52:55], s67 offen offset:3072  lds\n"
"v_mfma_f32_16x16x32_bf16 v[48:51], a[36:39], a[132:135], v[48:51]\n"
"s_add_u32 m0, s64, 12288\n"
"v_mfma_f32_16x16x32_bf16 v[76:79], a[44:47], a[132:135], v[76:79]\n"
"L_st2130:\n"
"s_cbranch_execz L_st4488\n"
"s_waitcnt vmcnt(12)\n"
"ds_read_b64_tr_b16 a[64:65], v9 offset:0\n"
"v_mfma_f32_16x16x32_bf16 v[76:79], a[40:43], a[128:131], v[76:79]\n"
"ds_read_b64_tr_b16 a[66:67], v9 offset:512\n"
"v_mfma_f32_16x16x32_bf16 v[80:83], a[40:43], a[136:139], v[80:83]\n"
"s_sub_u32 s67, 1114112, s75\n"
"ds_read_b64_tr_b16 a[68:69], v9 offset:4096\n"
"v_mfma_f32_16x16x32_bf16 v[80:83], a[44:47], a[140:143], v[80:83]\n"
"ds_read_b64_tr_b16 a[70:71], v9 offset:4608\n"
"v_mfma_f32_16x16x32_bf16 v[52:55], a[36:39], a[140:143], v[52:55]\n"
"ds_read_b64_tr_b16 a[72:73], v9 offset:8\n"
"buffer_load_dwordx4 v11, s[52:55], s67 offen offset:0  lds\n"
"v_mfma_f32_16x16x32_bf16 v[52:55], a[32:35], a[136:139], v[52:55]\n"
"s_waitcnt lgkmcnt(5)\n"
"ds_read_b64_tr_b16 a[74:75], v9 offset:520\n"
"v_mfma_f32_16x16x32_bf16 v[56:59], a[32:35], a[144:147], v[56:59]\n"
"s_sub_u32 s67, 1129472, s75\n"
"ds_read_b64_tr_b16 a[76:77], v9 offset:4104\n"
"v_mfma_f32_16x16x32_bf16 v[56:59], a[36:39], a[148:151], v[56:59]\n"
"ds_read_b64_tr_b16 a[78:79], v9 offset:4616\n"
"v_mfma_f32_16x16x32_bf16 v[84:87], a[44:47], a[148:151], v[84:87]\n"
"buffer_load_dwordx4 v11, s[52:55], s67 offen offset:1024  lds\n"
"v_mfma_f32_16x16x32_bf16 v[84:87], a[40:43], a[144:147], v[84:87]\n"
"ds_read_b64_tr_b16 a[80:81], v15 offset:0\n"
"v_mfma_f32_16x16x32_bf16 a[4:7], a[40:43], a[152:155], a[4:7]\n"
"s_sub_u32 s67, 1144832, s75\n"
"ds_read_b64_tr_b16 a[82:83], v15 offset:512\n"
"v_mfma_f32_16x16x32_bf16 a[4:7], a[44:47], a[156:159], a[4:7]\n"
"ds_read_b64_tr_b16 a[84:85], v15 offset:4096\n"
"v_mfma_f32_16x16x32_bf16 a[0:3], a[36:39], a[156:159], a[0:3]\n"
"ds_read_b64_tr_b16 a[86:87], v15 offset:4608\n"
"buffer_load_dwordx4 v11, s[52:55], s67 offen offset:2048  lds\n"
"v_mfma_f32_16x16x32_bf16 a[0:3], a[32:35], a[152:155], a[0:3]\n"
"ds_read_b64_tr_b16 a[88:89], v15 offset:8\n"
"v_mfma_f32_16x16x32_bf16 v[88:91], a[48:51], a[96:99], v[88:91]\n"
"s_sub_u32 s67, 1160192, s75\n"
"ds_read_b64_tr_b16 a[90:91], v15 offset:520\n"
"v_mfma_f32_16x16x32_bf16 v[88:91], a[52:55], a[100:103], v[88:91]\n"
"ds_read_b64_tr_b16 a[92:93], v15 offset:4104\n"
"v_mfma_f32_16x16x32_bf16 v[116:119], a[60:63], a[100:103], v[116:119]\n"
"ds_read_b64_tr_b16 a[94:95], v15 offset:4616\n"
"v_mfma_f32_16x16x32_bf16 v[116:119], a[56:59], a[96:99], v[116:119]\n"
"buffer_load_dwordx4 v11, s[52:55], s67 offen offset:3072  lds\n"
"v_mfma_f32_16x16x32_bf16 v[120:123], a[56:59], a[104:107], v[120:123]\n"
"v_mfma_f32_16x16x32_bf16 v[120:123], a[60:63], a[108:111], v[120:123]\n"
"s_add_u32 m0, s64, 0\n"
"v_mfma_f32_16x16x32_bf16 v[92:95], a[52:55], a[108:111], v[92:95]\n"
"v_mfma_f32_16x16x32_bf16 v[92:95], a[48:51], a[104:107], v[92:95]\n"
"s_sub_u32 s67, 1048704, s75\n"
"v_mfma_f32_16x16x32_bf16 v[96:99], a[48:51], a[112:115], v[96:99]\n"
"s_waitcnt lgkmcnt(0)\n"
"v_mfma_f32_16x16x32_bf16 v[96:99], a[52:55], a[116:119], v[96:99]\n"
"buffer_load_dwordx4 v11, s[52:55], s67 offen offset:0  lds\n"
"v_mfma_f32_16x16x32_bf16 v[124:127], a[60:63], a[116:119], v[124:127]\n"
"v_mfma_f32_16x16x32_bf16 v[124:127], a[56:59], a[112:115], v[124:127]\n"
"s_sub_u32 s67, 1064064, s75\n"
"v_mfma_f32_16x16x32_bf16 v[128:131], a[56:59], a[120:123], v[128:131]\n"
"v_mfma_f32_16x16x32_bf16 v[128:131], a[60:63], a[124:127], v[128:131]\n"
"v_mfma_f32_16x16x32_bf16 v[100:103], a[52:55], a[124:127], v[100:103]\n"
"buffer_load_dwordx4 v11, s[52:55], s67 offen offset:1024  lds\n"
"v_mfma_f32_16x16x32_bf16 v[100:103], a[48:51], a[120:123], v[100:103]\n"
"v_mfma_f32_16x16x32_bf16 v[104:107], a[48:51], a[128:131], v[104:107]\n"
"s_sub_u32 s67, 1079424, s75\n"
"v_mfma_f32_16x16x32_bf16 v[104:107], a[52:55], a[132:135], v[104:107]\n"
"v_mfma_f32_16x16x32_bf16 v[132:135], a[60:63], a[132:135], v[132:135]\n"
"buffer_load_dwordx4 v11, s[52:55], s67 offen offset:2048  lds\n"
"v_mfma_f32_16x16x32_bf16 v[132:135], a[56:59], a[128:131], v[132:135]\n"
"v_mfma_f32_16x16x32_bf16 v[136:139], a[56:59], a[136:139], v[136:139]\n"
"s_sub_u32 s67, 1094784, s75\n"
"v_mfma_f32_16x16x32_bf16 v[136:139], a[60:63], a[140:143], v[136:139]\n"
"v_mfma_f32_16x16x32_bf16 v[108:111], a[52:55], a[140:143], v[108:111]\n"
"v_mfma_f32_16x16x32_bf16 v[108:111], a[48:51], a[136:139], v[108:111]\n"
"buffer_load_dwordx4 v11, s[52:55], s67 offen offset:3072  lds\n"
"v_mfma_f32_16x16x32_bf16 v[112:115], a[48:51], a[144:147], v[112:115]\n"
"s_add_u32 m0, s64, 4096\n"
"v_mfma_f32_16x16x32_bf16 v[112:115], a[52:55], a[148:151], v[112:115]\n"
"v_mfma_f32_16x16x32_bf16 v[140:143], a[60:63], a[148:151], v[140:143]\n"
"s_sub_u32 s67, 1114240, s75\n"
"v_mfma_f32_16x16x32_bf16 v[140:143], a[56:59], a[144:147], v[140:143]\n"
"v_mfma_f32_16x16x32_bf16 a[12:15], a[56:59], a[152:155], a[12:15]\n"
"buffer_load_dwordx4 v11, s[52:55], s67 offen offset:0  lds\n"
"v_mfma_f32_16x16x32_bf16 a[12:15], a[60:63], a[156:159], a[12:15]\n"
"v_mfma_f32_16x16x32_bf16 a[8:11], a[52:55], a[156:159], a[8:11]\n"
"s_sub_u32 s67, 1129600, s75\n"
"v_mfma_f32_16x16x32_bf16 a[8:11], a[48:51], a[152:155], a[8:11]\n"
"v_mfma_f32_16x16x32_bf16 v[144:147], a[64:67], a[96:99], v[144:147]\n"
"buffer_load_dwordx4 v11, s[52:55], s67 offen offset:1024  lds\n"
"v_mfma_f32_16x16x32_bf16 v[144:147], a[68:71], a[100:103], v[144:147]\n"
"v_mfma_f32_16x16x32_bf16 v[172:175], a[76:79], a[100:103], v[172:175]\n"
"s_sub_u32 s67, 1144960, s75\n"
"v_mfma_f32_16x16x32_bf16 v[172:175], a[72:75], a[96:99], v[172:175]\n"
"v_mfma_f32_16x16x32_bf16 v[176:179], a[72:75], a[104:107], v[176:179]\n"
"v_mfma_f32_16x16x32_bf16 v[176:179], a[76:79], a[108:111], v[176:179]\n"
"s_sub_u32 s71, s72, s64\n"
"v_mfma_f32_16x16x32_bf16 v[148:151], a[68:71], a[108:111], v[148:151]\n"
"buffer_load_dwordx4 v11, s[52:55], s67 offen offset:2048  lds\n"
"v_mfma_f32_16x16x32_bf16 v[148:151], a[64:67], a[104:107], v[148:151]\n"
"s_sub_u32 s67, 1160320, s75\n"
"v_mfma_f32_16x16x32_bf16 v[152:155], a[64:67], a[112:115], v[152:155]\n"
"s_sub_u32 s66, 0, s73\n"
"s_add_u32 s71, s71, 57344\n"
"v_mfma_f32_16x16x32_bf16 v[152:155], a[68:71], a[116:119], v[152:155]\n"
"buffer_load_dwordx4 v11, s[52:55], s67 offen offset:3072  lds\n"
"v_mfma_f32_16x16x32_bf16 v[180:183], a[76:79], a[116:119], v[180:183]\n"
"v_add_u32 v12, s66, v7\n"
"v_mfma_f32_16x16x32_bf16 v[180:183], a[72:75], a[112:115], v[180:183]\n"
"v_add_u32 v20, s71, v7\n"
"v_mfma_f32_16x16x32_bf16 v[184:187], a[72:75], a[120:123], v[184:187]\n"
"v_add_u32 v13, s66, v8\n"
"v_mfma_f32_16x16x32_bf16 v[184:187], a[76:79], a[124:127], v[184:187]\n"
"v_add_u32 v21, s71, v8\n"
"v_mfma_f32_16x16x32_bf16 v[156:159], a[68:71], a[124:127], v[156:159]\n"
"s_cmp_lt_u32 %16, 2\n"
"s_cselect_b32 s67, 16384, 0\n"
"v_mfma_f32_16x16x32_bf16 v[156:159], a[64:67], a[120:123], v[156:159]\n"
"s_add_u32 s67, s67, s72\n"
"v_mfma_f32_16x16x32_bf16 v[160:163], a[64:67], a[128:131], v[160:163]\n"
"s_waitcnt vmcnt(16)\n"
"ds_read_b128 a[192:195], v20 offset:0\n"
"v_mfma_f32_16x16x32_bf16 v[160:163], a[68:71], a[132:135], v[160:163]\n"
"v_mfma_f32_16x16x32_bf16 v[188:191], a[76:79], a[132:135], v[188:191]\n"
"ds_read_b128 a[196:199], v21 offset:0\n"
"v_mfma_f32_16x16x32_bf16 v[188:191], a[72:75], a[128:131], v[188:191]\n"
"ds_read_b128 a[200:203], v20 offset:2048\n"
"v_mfma_f32_16x16x32_bf16 v[192:195], a[72:75], a[136:139], v[192:195]\n"
"s_add_u32 m0, s67, 57344\n"
"v_mfma_f32_16x16x32_bf16 v[192:195], a[76:79], a[140:143], v[192:195]\n"
"ds_read_b128 a[204:207], v21 offset:2048\n"
"v_mfma_f32_16x16x32_bf16 v[164:167], a[68:71], a[140:143], v[164:167]\n"
"v_mfma_f32_16x16x32_bf16 v[164:167], a[64:67], a[136:139], v[164:167]\n"
"v_mfma_f32_16x16x32_bf16 v[168:171], a[64:67], a[144:147], v[168:171]\n"
"s_sub_u32 s67, 1280, s60\n"
"v_mfma_f32_16x16x32_bf16 v[168:171], a[68:71], a[148:151], v[168:171]\n"
"v_mfma_f32_16x16x32_bf16 v[196:199], a[76:79], a[148:151], v[196:199]\n"
"v_mfma_f32_16x16x32_bf16 v[196:199], a[72:75], a[144:147], v[196:199]\n"
"s_waitcnt lgkmcnt(0)\n"
"v_mfma_f32_16x16x32_bf16 a[20:23], a[72:75], a[152:155], a[20:23]\n"
"buffer_load_dwordx4 v6, s[48:51], s67 offen offset:0  lds\n"
"v_mfma_f32_16x16x32_bf16 a[20:23], a[76:79], a[156:159], a[20:23]\n"
"v_mfma_f32_16x16x32_bf16 a[16:19], a[68:71], a[156:159], a[16:19]\n"
"s_sub_u32 s67, 16640, s60\n"
"v_mfma_f32_16x16x32_bf16 a[16:19], a[64:67], a[152:155], a[16:19]\n"
"s_barrier\n"
"s_waitcnt vmcnt(9)\n"
"ds_read_b64_tr_b16 a[32:33], v9 offset:8192\n"
"v_mfma_f32_16x16x32_bf16 v[200:203], a[80:83], a[96:99], v[200:203]\n"
"ds_read_b64_tr_b16 a[34:35], v9 offset:8704\n"
"buffer_load_dwordx4 v6, s[48:51], s67 offen offset:1024  lds\n"
"v_mfma_f32_16x16x32_bf16 v[200:203], a[84:87], a[100:103], v[200:203]\n"
"ds_read_b64_tr_b16 a[36:37], v9 offset:12288\n"
"v_mfma_f32_16x16x32_bf16 v[228:231], a[92:95], a[100:103], v[228:231]\n"
"s_sub_u32 s67, 32000, s60\n"
"ds_read_b128 a[100:103], v13 offset:32768\n"
"v_mfma_f32_16x16x32_bf16 v[228:231], a[88:91], a[96:99], v[228:231]\n"
"ds_read_b128 a[96:99], v12 offset:32768\n"
"v_mfma_f32_16x16x32_bf16 v[232:235], a[88:91], a[104:107], v[232:235]\n"
"ds_read_b64_tr_b16 a[38:39], v9 offset:12800\n"
"v_mfma_f32_16x16x32_bf16 v[232:235], a[92:95], a[108:111], v[232:235]\n"
"ds_read_b64_tr_b16 a[40:41], v9 offset:8200\n"
"buffer_load_dwordx4 v6, s[48:51], s67 offen offset:2048  lds\n"
"v_mfma_f32_16x16x32_bf16 v[204:207], a[84:87], a[108:111], v[204:207]\n"
"ds_read_b128 a[108:111], v13 offset:34816\n"
"v_mfma_f32_16x16x32_bf16 v[204:207], a[80:83], a[104:107], v[204:207]\n"
"s_sub_u32 s67, 47360, s60\n"
"ds_read_b128 a[104:107], v12 offset:34816\n"
"v_mfma_f32_16x16x32_bf16 v[208:211], a[80:83], a[112:115], v[208:211]\n"
"ds_read_b64_tr_b16 a[42:43], v9 offset:8712\n"
"v_mfma_f32_16x16x32_bf16 v[208:211], a[84:87], a[116:119], v[208:211]\n"
"ds_read_b64_tr_b16 a[44:45], v9 offset:12296\n"
"v_mfma_f32_16x16x32_bf16 v[236:239], a[92:95], a[116:119], v[236:239]\n"
"ds_read_b128 a[116:119], v13 offset:36864\n"
"buffer_load_dwordx4 v6, s[48:51], s67 offen offset:3072  lds\n"
"v_mfma_f32_16x16x32_bf16 v[236:239], a[88:91], a[112:115], v[236:239]\n"
"ds_read_b128 a[112:115], v12 offset:36864\n"
"v_mfma_f32_16x16x32_bf16 v[240:243], a[88:91], a[120:123], v[240:243]\n"
"s_add_u32 m0, s64, 16256\n"
"ds_read_b64_tr_b16 a[46:47], v9 offset:12808\n"
"v_mfma_f32_16x16x32_bf16 v[240:243], a[92:95], a[124:127], v[240:243]\n"
"ds_read_b64_tr_b16 a[48:49], v15 offset:8192\n"
"v_mfma_f32_16x16x32_bf16 v[212:215], a[84:87], a[124:127], v[212:215]\n"
"ds_read_b128 a[124:127], v13 offset:38912\n"
"buffer_load_dwordx4 v6, s[40:43], s84 offen offset:128  lds\n"
"v_mfma_f32_16x16x32_bf16 v[212:215], a[80:83], a[120:123], v[212:215]\n"
"ds_read_b128 a[120:123], v12 offset:38912\n"
"v_mfma_f32_16x16x32_bf16 v[216:219], a[80:83], a[128:131], v[216:219]\n"
"ds_read_b64_tr_b16 a[50:51], v15 offset:8704\n"
"v_mfma_f32_16x16x32_bf16 v[216:219], a[84:87], a[132:135], v[216:219]\n"
"ds_read_b64_tr_b16 a[52:53], v15 offset:12288\n"
"v_mfma_f32_16x16x32_bf16 v[244:247], a[92:95], a[132:135], v[244:247]\n"
"ds_read_b128 a[132:135], v13 offset:24576\n"
"buffer_load_dwordx4 v6, s[40:43], s85 offen offset:1152  lds\n"
"v_mfma_f32_16x16x32_bf16 v[244:247], a[88:91], a[128:131], v[244:247]\n"
"ds_read_b128 a[128:131], v12 offset:24576\n"
"v_mfma_f32_16x16x32_bf16 v[248:251], a[88:91], a[136:139], v[248:251]\n"
"ds_read_b64_tr_b16 a[54:55], v15 offset:12800\n"
"v_mfma_f32_16x16x32_bf16 v[248:251], a[92:95], a[140:143], v[248:251]\n"
"ds_read_b64_tr_b16 a[56:57], v15 offset:8200\n"
"v_mfma_f32_16x16x32_bf16 v[220:223], a[84:87], a[140:143], v[220:223]\n"
"ds_read_b128 a[140:143], v13 offset:26624\n"
"buffer_load_dwordx4 v6, s[40:43], s86 offen offset:2176  lds\n"
"v_mfma_f32_16x16x32_bf16 v[220:223], a[80:83], a[136:139], v[220:223]\n"
"ds_read_b128 a[136:139], v12 offset:26624\n"
"v_mfma_f32_16x16x32_bf16 v[224:227], a[80:83], a[144:147], v[224:227]\n"
"ds_read_b64_tr_b16 a[58:59], v15 offset:8712\n"
"v_mfma_f32_16x16x32_bf16 v[224:227], a[84:87], a[148:151], v[224:227]\n"
"ds_read_b64_tr_b16 a[60:61], v15 offset:12296\n"
"v_mfma_f32_16x16x32_bf16 v[252:255], a[92:95], a[148:151], v[252:255]\n"
"ds_read_b128 a[148:151], v13 offset:28672\n"
"v_mfma_f32_16x16x32_bf16 v[252:255], a[88:91], a[144:147], v[252:255]\n"
"ds_read_b128 a[144:147], v12 offset:28672\n"
"ds_read_b64_tr_b16 a[62:63], v15 offset:12808\n"
"v_mfma_f32_16x16x32_bf16 a[28:31], a[88:91], a[152:155], a[28:31]\n"
"buffer_load_dwordx4 v6, s[40:43], s87 offen offset:3200  lds\n"
"v_mfma_f32_16x16x32_bf16 a[28:31], a[92:95], a[156:159], a[28:31]\n"
"v_mfma_f32_16x16x32_bf16 a[24:27], a[84:87], a[156:159], a[24:27]\n"
"s_add_u32 m0, s64, 8192\n"
"ds_read_b128 a[156:159], v13 offset:30720\n"
"v_mfma_f32_16x16x32_bf16 a[24:27], a[80:83], a[152:155], a[24:27]\n"
"s_waitcnt lgkmcnt(15)\n"
"ds_read_b128 a[152:155], v12 offset:30720\n"
"v_mfma_f32_16x16x32_bf16 v[32:35], a[32:35], a[96:99], v[32:35]\n"
"s_sub_u32 s67, 1310720, s75\n"
"v_mfma_f32_16x16x32_bf16 v[32:35], a[36:39], a[100:103], v[32:35]\n"
"s_waitcnt lgkmcnt(2)\n"
"v_mfma_f32_16x16x32_bf16 v[60:63], a[44:47], a[100:103], v[60:63]\n"
"buffer_load_dwordx4 v11, s[52:55], s67 offen offset:0  lds\n"
"v_mfma_f32_16x16x32_bf16 v[60:63], a[40:43], a[96:99], v[60:63]\n"
"v_mfma_f32_16x16x32_bf16 v[64:67], a[40:43], a[104:107], v[64:67]\n"
"s_sub_u32 s67, 1326080, s75\n"
"v_mfma_f32_16x16x32_bf16 v[64:67], a[44:47], a[108:111], v[64:67]\n"
"v_mfma_f32_16x16x32_bf16 v[36:39], a[36:39], a[108:111], v[36:39]\n"
"buffer_load_dwordx4 v11, s[52:55], s67 offen offset:1024  lds\n"
"v_mfma_f32_16x16x32_bf16 v[36:39], a[32:35], a[104:107], v[36:39]\n"
"v_mfma_f32_16x16x32_bf16 v[40:43], a[32:35], a[112:115], v[40:43]\n"
"s_sub_u32 s67, 1341440, s75\n"
"v_mfma_f32_16x16x32_bf16 v[40:43], a[36:39], a[116:119], v[40:43]\n"
"v_mfma_f32_16x16x32_bf16 v[68:71], a[44:47], a[116:119], v[68:71]\n"
"v_mfma_f32_16x16x32_bf16 v[68:71], a[40:43], a[112:115], v[68:71]\n"
"buffer_load_dwordx4 v11, s[52:55], s67 offen offset:2048  lds\n"
"v_mfma_f32_16x16x32_bf16 v[72:75], a[40:43], a[120:123], v[72:75]\n"
"v_mfma_f32_16x16x32_bf16 v[72:75], a[44:47], a[124:127], v[72:75]\n"
"s_sub_u32 s67, 1356800, s75\n"
"v_mfma_f32_16x16x32_bf16 v[44:47], a[36:39], a[124:127], v[44:47]\n"
"v_mfma_f32_16x16x32_bf16 v[44:47], a[32:35], a[120:123], v[44:47]\n"
"buffer_load_dwordx4 v11, s[52:55], s67 offen offset:3072  lds\n"
"v_mfma_f32_16x16x32_bf16 v[48:51], a[32:35], a[128:131], v[48:51]\n"
"v_mfma_f32_16x16x32_bf16 v[48:51], a[36:39], a[132:135], v[48:51]\n"
"s_add_u32 m0, s64, 12288\n"
"v_mfma_f32_16x16x32_bf16 v[76:79], a[44:47], a[132:135], v[76:79]\n"
"s_waitcnt vmcnt(12)\n"
"ds_read_b64_tr_b16 a[64:65], v9 offset:0\n"
"v_mfma_f32_16x16x32_bf16 v[76:79], a[40:43], a[128:131], v[76:79]\n"
"s_sub_u32 s67, 1376256, s75\n"
"ds_read_b64_tr_b16 a[66:67], v9 offset:512\n"
"v_mfma_f32_16x16x32_bf16 v[80:83], a[40:43], a[136:139], v[80:83]\n"
"ds_read_b64_tr_b16 a[68:69], v9 offset:4096\n"
"v_mfma_f32_16x16x32_bf16 v[80:83], a[44:47], a[140:143], v[80:83]\n"
"ds_read_b64_tr_b16 a[70:71], v9 offset:4608\n"
"buffer_load_dwordx4 v11, s[52:55], s67 offen offset:0  lds\n"
"v_mfma_f32_16x16x32_bf16 v[52:55], a[36:39], a[140:143], v[52:55]\n"
"s_sub_u32 s67, 1391616, s75\n"
"ds_read_b64_tr_b16 a[72:73], v9 offset:8\n"
"v_mfma_f32_16x16x32_bf16 v[52:55], a[32:35], a[136:139], v[52:55]\n"
"s_waitcnt lgkmcnt(5)\n"
"ds_read_b64_tr_b16 a[74:75], v9 offset:520\n"
"v_mfma_f32_16x16x32_bf16 v[56:59], a[32:35], a[144:147], v[56:59]\n"
"ds_read_b64_tr_b16 a[76:77], v9 offset:4104\n"
"v_mfma_f32_16x16x32_bf16 v[56:59], a[36:39], a[148:151], v[56:59]\n"
"ds_read_b64_tr_b16 a[78:79], v9 offset:4616\n"
"v_mfma_f32_16x16x32_bf16 v[84:87], a[44:47], a[148:151], v[84:87]\n"
"buffer_load_dwordx4 v11, s[52:55], s67 offen offset:1024  lds\n"
"v_mfma_f32_16x16x32_bf16 v[84:87], a[40:43], a[144:147], v[84:87]\n"
"ds_read_b64_tr_b16 a[80:81], v15 offset:0\n"
"v_mfma_f32_16x16x32_bf16 a[4:7], a[40:43], a[152:155], a[4:7]\n"
"s_sub_u32 s67, 1406976, s75\n"
"ds_read_b64_tr_b16 a[82:83], v15 offset:512\n"
"v_mfma_f32_16x16x32_bf16 a[4:7], a[44:47], a[156:159], a[4:7]\n"
"ds_read_b64_tr_b16 a[84:85], v15 offset:4096\n"
"v_mfma_f32_16x16x32_bf16 a[0:3], a[36:39], a[156:159], a[0:3]\n"
"ds_read_b64_tr_b16 a[86:87], v15 offset:4608\n"
"buffer_load_dwordx4 v11, s[52:55], s67 offen offset:2048  lds\n"
"v_mfma_f32_16x16x32_bf16 a[0:3], a[32:35], a[152:155], a[0:3]\n"
"ds_read_b64_tr_b16 a[88:89], v15 offset:8\n"
"v_mfma_f32_16x16x32_bf16 v[88:91], a[48:51], a[96:99], v[88:91]\n"
"s_sub_u32 s67, 1422336, s75\n"
"ds_read_b64_tr_b16 a[90:91], v15 offset:520\n"
"v_mfma_f32_16x16x32_bf16 v[88:91], a[52:55], a[100:103], v[88:91]\n"
"ds_read_b64_tr_b16 a[92:93], v15 offset:4104\n"
"v_mfma_f32_16x16x32_bf16 v[116:119], a[60:63], a[100:103], v[116:119]\n"
"ds_read_b64_tr_b16 a[94:95], v15 offset:4616\n"
"v_mfma_f32_16x16x32_bf16 v[116:119], a[56:59], a[96:99], v[116:119]\n"
"buffer_load_dwordx4 v11, s[52:55], s67 offen offset:3072  lds\n"
"v_mfma_f32_16x16x32_bf16 v[120:123], a[56:59], a[104:107], v[120:123]\n"
"v_mfma_f32_16x16x32_bf16 v[120:123], a[60:63], a[108:111], v[120:123]\n"
"s_add_u32 m0, s64, 0\n"
"v_mfma_f32_16x16x32_bf16 v[92:95], a[52:55], a[108:111], v[92:95]\n"
"v_mfma_f32_16x16x32_bf16 v[92:95], a[48:51], a[104:107], v[92:95]\n"
"s_sub_u32 s67, 1310848, s75\n"
"v_mfma_f32_16x16x32_bf16 v[96:99], a[48:51], a[112:115], v[96:99]\n"
"s_waitcnt lgkmcnt(0)\n"
"s_mov_b32 s99, 0\n"
"v_mfma_f32_16x16x32_bf16 v[96:99], a[52:55], a[116:119], v[96:99]\n"
"s_mov_b32 s98, 0\n"
"buffer_load_dwordx4 v11, s[52:55], s67 offen offset:0  lds\n"
"v_mfma_f32_16x16x32_bf16 v[124:127], a[60:63], a[116:119], v[124:127]\n"
"s_memtime s[76:77]\n"
"s_load_dword s98, s[96:97], 0 glc\n"
"v_mfma_f32_16x16x32_bf16 v[124:127], a[56:59], a[112:115], v[124:127]\n"
"s_sub_u32 s67, 1326208, s75\n"
"v_mfma_f32_16x16x32_bf16 v[128:131], a[56:59], a[120:123], v[128:131]\n"
"v_mfma_f32_16x16x32_bf16 v[128:131], a[60:63], a[124:127], v[128:131]\n"
"v_mfma_f32_16x16x32_bf16 v[100:103], a[52:55], a[124:127], v[100:103]\n"
"buffer_load_dwordx4 v11, s[52:55], s67 offen offset:1024  lds\n"
"v_mfma_f32_16x16x32_bf16 v[100:103], a[48:51], a[120:123], v[100:103]\n"
"v_mfma_f32_16x16x32_bf16 v[104:107], a[48:51], a[128:131], v[104:107]\n"
"s_sub_u32 s67, 1341568, s75\n"
"v_mfma_f32_16x16x32_bf16 v[104:107], a[52:55], a[132:135], v[104:107]\n"
"v_mfma_f32_16x16x32_bf16 v[132:135], a[60:63], a[132:135], v[132:135]\n"
"v_mfma_f32_16x16x32_bf16 v[132:135], a[56:59], a[128:131], v[132:135]\n"
"s_mov_b32 s29, s76\n"
"v_mfma_f32_16x16x32_bf16 v[136:139], a[56:59], a[136:139], v[136:139]\n"
"buffer_load_dwordx4 v11, s[52:55], s67 offen offset:2048  lds\n"
"v_mfma_f32_16x16x32_bf16 v[136:139], a[60:63], a[140:143], v[136:139]\n"
"s_sub_u32 s67, 1356928, s75\n"
"v_mfma_f32_16x16x32_bf16 v[108:111], a[52:55], a[140:143], v[108:111]\n"
"s_sub_u32 s99, s99, s98\n"
"s_sub_u32 s99, s99, s98\n"
"v_mfma_f32_16x16x32_bf16 v[108:111], a[48:51], a[136:139], v[108:111]\n"
"s_sub_u32 s99, s99, s98\n"
"v_mfma_f32_16x16x32_bf16 v[112:115], a[48:51], a[144:147], v[112:115]\n"
"s_sub_u32 s99, s99, s98\n"
"buffer_load_dwordx4 v11, s[52:55], s67 offen offset:3072  lds\n"
"v_mfma_f32_16x16x32_bf16 v[112:115], a[52:55], a[148:151], v[112:115]\n"
"s_add_u32 m0, s64, 4096\n"
"v_mfma_f32_16x16x32_bf16 v[140:143], a[60:63], a[148:151], v[140:143]\n"
"s_sub_u32 s99, s99, s98\n"
"s_sub_u32 s99, s99, s98\n"
"v_mfma_f32_16x16x32_bf16 v[140:143], a[56:59], a[144:147], v[140:143]\n"
"s_sub_u32 s99, s99, s98\n"
"s_sub_u32 s67, 1376384, s75\n"
"v_mfma_f32_16x16x32_bf16 a[12:15], a[56:59], a[152:155], a[12:15]\n"
"s_sub_u32 s99, s99, s98\n"
"v_mfma_f32_16x16x32_bf16 a[12:15], a[60:63], a[156:159], a[12:15]\n"
"buffer_load_dwordx4 v11, s[52:55], s67 offen offset:0  lds\n"
"v_mfma_f32_16x16x32_bf16 a[8:11], a[52:55], a[156:159], a[8:11]\n"
"s_sub_u32 s99, s99, s98\n"
"s_sub_u32 s99, s99, s98\n"
"v_mfma_f32_16x16x32_bf16 a[8:11], a[48:51], a[152:155], a[8:11]\n"
"s_sub_u32 s99, s99, s98\n"
"s_sub_u32 s67, 1391744, s75\n"
"v_mfma_f32_16x16x32_bf16 v[144:147], a[64:67], a[96:99], v[144:147]\n"
"s_sub_u32 s99, s99, s98\n"
"v_mfma_f32_16x16x32_bf16 v[144:147], a[68:71], a[100:103], v[144:147]\n"
"buffer_load_dwordx4 v11, s[52:55], s67 offen offset:1024  lds\n"
"v_mfma_f32_16x16x32_bf16 v[172:175], a[76:79], a[100:103], v[172:175]\n"
"s_sub_u32 s99, s99, s98\n"
"s_sub_u32 s99, s99, s98\n"
"v_mfma_f32_16x16x32_bf16 v[172:175], a[72:75], a[96:99], v[172:175]\n"
"s_sub_u32 s99, s99, s98\n"
"s_sub_u32 s67, 1407104, s75\n"
"v_mfma_f32_16x16x32_bf16 v[176:179], a[72:75], a[104:107], v[176:179]\n"
"s_sub_u32 s99, s99, s98\n"
"v_mfma_f32_16x16x32_bf16 v[176:179], a[76:79], a[108:111], v[176:179]\n"
"buffer_load_dwordx4 v11, s[52:55], s67 offen offset:2048  lds\n"
"v_mfma_f32_16x16x32_bf16 v[148:151], a[68:71], a[108:111], v[148:151]\n"
"s_sub_u32 s99, s99, s98\n"
"s_sub_u32 s99, s99, s98\n"
"v_mfma_f32_16x16x32_bf16 v[148:151], a[64:67], a[104:107], v[148:151]\n"
"s_sub_u32 s99, s99, s98\n"
"s_sub_u32 s71, s64, s64\n"
"v_mfma_f32_16x16x32_bf16 v[152:155], a[64:67], a[112:115], v[152:155]\n"
"s_sub_u32 s66, 40960, s73\n"
"s_sub_u32 s99, s99, s98\n"
"v_mfma_f32_16x16x32_bf16 v[152:155], a[68:71], a[116:119], v[152:155]\n"
"s_add_u32 s71, s71, 16384\n"
"s_sub_u32 s67, 1422464, s75\n"
"v_mfma_f32_16x16x32_bf16 v[180:183], a[76:79], a[116:119], v[180:183]\n"
"s_sub_u32 s99, s99, s98\n"
"v_add_u32 v12, s66, v7\n"
"s_sub_u32 s99, s99, s98\n"
"v_mfma_f32_16x16x32_bf16 v[180:183], a[72:75], a[112:115], v[180:183]\n"
"s_sub_u32 s99, s99, s98\n"
"buffer_load_dwordx4 v11, s[52:55], s67 offen offset:3072  lds\n"
"v_mfma_f32_16x16x32_bf16 v[184:187], a[72:75], a[120:123], v[184:187]\n"
"s_sub_u32 s99, s99, s98\n"
"v_add_u32 v13, s66, v8\n"
"s_cmp_lt_u32 %16, 2\n"
"s_cselect_b32 s67, 16384, 0\n"
"v_mfma_f32_16x16x32_bf16 v[184:187], a[76:79], a[124:127], v[184:187]\n"
"s_memtime s[76:77]\n"
"v_add_u32 v20, s71, v7\n"
"s_add_u32 s67, s67, s72\n"
"v_mfma_f32_16x16x32_bf16 v[156:159], a[68:71], a[124:127], v[156:159]\n"
"s_add_u32 m0, s67, 98304\n"
"s_sub_u32 s67, 1536, s60\n"
"v_mfma_f32_16x16x32_bf16 v[156:159], a[64:67], a[120:123], v[156:159]\n"
"s_waitcnt lgkmcnt(0)\n"
"buffer_load_dwordx4 v6, s[48:51], s67 offen offset:0  lds\n"
"v_add_u32 v21, s71, v8\n"
"v_mfma_f32_16x16x32_bf16 v[160:163], a[64:67], a[128:131], v[160:163]\n"
"s_sub_u32 s29, s76, s29\n"
"s_sub_u32 s67, 16896, s60\n"
"v_mfma_f32_16x16x32_bf16 v[160:163], a[68:71], a[132:135], v[160:163]\n"
"s_cmp_lt_u32 s29, 620\n"
"s_cselect_b32 s99, s99, 0\n"
"v_mfma_f32_16x16x32_bf16 v[188:191], a[76:79], a[132:135], v[188:191]\n"
"s_max_u32 s30, s30, s99\n"
"v_mfma_f32_16x16x32_bf16 v[188:191], a[72:75], a[128:131], v[188:191]\n"
"s_waitcnt vmcnt(17)\n"
"ds_read_b128 a[208:211], v20 offset:0\n"
"buffer_load_dwordx4 v6, s[48:51], s67 offen offset:1024  lds\n"
"v_mfma_f32_16x16x32_bf16 v[192:195], a[72:75], a[136:139], v[192:195]\n"
"ds_read_b128 a[212:215], v21 offset:0\n"
"v_mfma_f32_16x16x32_bf16 v[192:195], a[76:79], a[140:143], v[192:195]\n"
"s_sub_u32 s67, 32256, s60\n"
"ds_read_b128 a[216:219], v20 offset:2048\n"
"v_mfma_f32_16x16x32_bf16 v[164:167], a[68:71], a[140:143], v[164:167]\n"
"v_mfma_f32_16x16x32_bf16 v[164:167], a[64:67], a[136:139], v[164:167]\n"
"ds_read_b128 a[220:223], v21 offset:2048\n"
"v_mfma_f32_16x16x32_bf16 v[168:171], a[64:67], a[144:147], v[168:171]\n"
"buffer_load_dwordx4 v6, s[48:51], s67 offen offset:2048  lds\n"
"v_mfma_f32_16x16x32_bf16 v[168:171], a[68:71], a[148:151], v[168:171]\n"
"v_mfma_f32_16x16x32_bf16 v[196:199], a[76:79], a[148:151], v[196:199]\n"
"s_sub_u32 s67, 47616, s60\n"
"v_mfma_f32_16x16x32_bf16 v[196:199], a[72:75], a[144:147], v[196:199]\n"
"v_mfma_f32_16x16x32_bf16 a[20:23], a[72:75], a[152:155], a[20:23]\n"
"buffer_load_dwordx4 v6, s[48:51], s67 offen offset:3072  lds\n"
"v_mfma_f32_16x16x32_bf16 a[20:23], a[76:79], a[156:159], a[20:23]\n"
"s_waitcnt lgkmcnt(0)\n"
"v_mfma_f32_16x16x32_bf16 a[16:19], a[68:71], a[156:159], a[16:19]\n"
"s_add_u32 m0, s64, 20352\n"
"v_mfma_f32_16x16x32_bf16 a[16:19], a[64:67], a[152:155], a[16:19]\n"
"s_barrier\n"
"s_waitcnt vmcnt(12)\n"
"ds_read_b64_tr_b16 a[32:33], v9 offset:8192\n"
"v_mfma_f32_16x16x32_bf16 v[200:203], a[80:83], a[96:99], v[200:203]\n"
"ds_read_b64_tr_b16 a[34:35], v9 offset:8704\n"
"v_mfma_f32_16x16x32_bf16 v[200:203], a[84:87], a[100:103], v[200:203]\n"
"ds_read_b64_tr_b16 a[36:37], v9 offset:12288\n"
"buffer_load_dwordx4 v6, s[40:43], s88 offen offset:128  lds\n"
"v_mfma_f32_16x16x32_bf16 v[228:231], a[92:95], a[100:103], v[228:231]\n"
"ds_read_b128 a[100:103], v13 offset:32768\n"
"v_mfma_f32_16x16x32_bf16 v[228:231], a[88:91], a[96:99], v[228:231]\n"
"ds_read_b128 a[96:99], v12 offset:32768\n"
"v_mfma_f32_16x16x32_bf16 v[232:235], a[88:91], a[104:107], v[232:235]\n"
"ds_read_b64_tr_b16 a[38:39], v9 offset:12800\n"
"buffer_load_dwordx4 v6, s[40:43], s89 offen offset:1152  lds\n"
"v_mfma_f32_16x16x32_bf16 v[232:235], a[92:95], a[108:111], v[232:235]\n"
"ds_read_b64_tr_b16 a[40:41], v9 offset:8200\n"
"v_mfma_f32_16x16x32_bf16 v[204:207], a[84:87], a[108:111], v[204:207]\n"
"ds_read_b128 a[108:111], v13 offset:34816\n"
"v_mfma_f32_16x16x32_bf16 v[204:207], a[80:83], a[104:107], v[204:207]\n"
"ds_read_b128 a[104:107], v12 offset:34816\n"
"v_mfma_f32_16x16x32_bf16 v[208:211], a[80:83], a[112:115], v[208:211]\n"
"ds_read_b64_tr_b16 a[42:43], v9 offset:8712\n"
"v_mfma_f32_16x16x32_bf16 v[208:211], a[84:87], a[116:119], v[208:211]\n"
"ds_read_b64_tr_b16 a[44:45], v9 offset:12296\n"
"buffer_load_dwordx4 v6, s[40:43], s90 offen offset:2176  lds\n"
"v_mfma_f32_16x16x32_bf16 v[236:239], a[92:95], a[116:119], v[236:239]\n"
"ds_read_b128 a[116:119], v13 offset:36864\n"
"v_mfma_f32_16x16x32_bf16 v[236:239], a[88:91], a[112:115], v[236:239]\n"
"ds_read_b128 a[112:115], v12 offset:36864\n"
"v_mfma_f32_16x16x32_bf16 v[240:243], a[88:91], a[120:123], v[240:243]\n"
"ds_read_b64_tr_b16 a[46:47], v9 offset:12808\n"
"v_mfma_f32_16x16x32_bf16 v[240:243], a[92:95], a[124:127], v[240:243]\n"
"ds_read_b64_tr_b16 a[48:49], v15 offset:8192\n"
"buffer_load_dwordx4 v6, s[40:43], s91 offen offset:3200  lds\n"
"v_mfma_f32_16x16x32_bf16 v[212:215], a[84:87], a[124:127], v[212:215]\n"
"ds_read_b128 a[124:127], v13 offset:38912\n"
"v_mfma_f32_16x16x32_bf16 v[212:215], a[80:83], a[120:123], v[212:215]\n"
"ds_read_b128 a[120:123], v12 offset:38912\n"
"v_mfma_f32_16x16x32_bf16 v[216:219], a[80:83], a[128:131], v[216:219]\n"
"ds_read_b64_tr_b16 a[50:51], v15 offset:8704\n"
"v_mfma_f32_16x16x32_bf16 v[216:219], a[84:87], a[132:135], v[216:219]\n"
"s_add_u32 m0, s64, 8192\n"
"ds_read_b64_tr_b16 a[52:53], v15 offset:12288\n"
"v_mfma_f32_16x16x32_bf16 v[244:247], a[92:95], a[132:135], v[244:247]\n"
"ds_read_b128 a[132:135], v13 offset:24576\n"
"v_mfma_f32_16x16x32_bf16 v[244:247], a[88:91], a[128:131], v[244:247]\n"
"ds_read_b128 a[128:131], v12 offset:24576\n"
"v_mfma_f32_16x16x32_bf16 v[248:251], a[88:91], a[136:139], v[248:251]\n"
"ds_read_b64_tr_b16 a[54:55], v15 offset:12800\n"
"v_mfma_f32_16x16x32_bf16 v[248:251], a[92:95], a[140:143], v[248:251]\n"
"ds_read_b64_tr_b16 a[56:57], v15 offset:8200\n"
"v_mfma_f32_16x16x32_bf16 v[220:223], a[84:87], a[140:143], v[220:223]\n"
"ds_read_b128 a[140:143], v13 offset:26624\n"
"v_mfma_f32_16x16x32_bf16 v[220:223], a[80:83], a[136:139], v[220:223]\n"
"ds_read_b128 a[136:139], v12 offset:26624\n"
"v_mfma_f32_16x16x32_bf16 v[224:227], a[80:83], a[144:147], v[224:227]\n"
"ds_read_b64_tr_b16 a[58:59], v15 offset:8712\n"
"v_mfma_f32_16x16x32_bf16 v[224:227], a[84:87], a[148:151], v[224:227]\n"
"s_sub_u32 s67, 1572864, s75\n"
"ds_read_b64_tr_b16 a[60:61], v15 offset:12296\n"
"v_mfma_f32_16x16x32_bf16 v[252:255], a[92:95], a[148:151], v[252:255]\n"
"ds_read_b128 a[148:151], v13 offset:28672\n"
"v_mfma_f32_16x16x32_bf16 v[252:255], a[88:91], a[144:147], v[252:255]\n"
"ds_read_b128 a[144:147], v12 offset:28672\n"
"ds_read_b64_tr_b16 a[62:63], v15 offset:12808\n"
"v_mfma_f32_16x16x32_bf16 a[28:31], a[88:91], a[152:155], a[28:31]\n"
"v_mfma_f32_16x16x32_bf16 a[28:31], a[92:95], a[156:159], a[28:31]\n"
"v_mfma_f32_16x16x32_bf16 a[24:27], a[84:87], a[156:159], a[24:27]\n"
"ds_read_b128 a[156:159], v13 offset:30720\n"
"v_mfma_f32_16x16x32_bf16 a[24:27], a[80:83], a[152:155], a[24:27]\n"
"s_waitcnt lgkmcnt(15)\n"
"ds_read_b128 a[152:155], v12 offset:30720\n"
"v_mfma_f32_16x16x32_bf16 v[32:35], a[32:35], a[96:99], v[32:35]\n"
"v_mfma_f32_16x16x32_bf16 v[32:35], a[36:39], a[100:103], v[32:35]\n"
"s_waitcnt lgkmcnt(2)\n"
"v_mfma_f32_16x16x32_bf16 v[60:63], a[44:47], a[100:103], v[60:63]\n"
"buffer_load_dwordx4 v11, s[52:55], s67 offen offset:0  lds\n"
"v_mfma_f32_16x16x32_bf16 v[60:63], a[40:43], a[96:99], v[60:63]\n"
"v_mfma_f32_16x16x32_bf16 v[64:67], a[40:43], a[104:107], v[64:67]\n"
"s_sub_u32 s67, 1588224, s75\n"
"v_mfma_f32_16x16x32_bf16 v[64:67], a[44:47], a[108:111], v[64:67]\n"
"v_mfma_f32_16x16x32_bf16 v[36:39], a[36:39], a[108:111], v[36:39]\n"
"v_mfma_f32_16x16x32_bf16 v[36:39], a[32:35], a[104:107], v[36:39]\n"
"buffer_load_dwordx4 v11, s[52:55], s67 offen offset:1024  lds\n"
"v_mfma_f32_16x16x32_bf16 v[40:43], a[32:35], a[112:115], v[40:43]\n"
"v_mfma_f32_16x16x32_bf16 v[40:43], a[36:39], a[116:119], v[40:43]\n"
"s_sub_u32 s67, 1603584, s75\n"
"v_mfma_f32_16x16x32_bf16 v[68:71], a[44:47], a[116:119], v[68:71]\n"
"v_mfma_f32_16x16x32_bf16 v[68:71], a[40:43], a[112:115], v[68:71]\n"
"buffer_load_dwordx4 v11, s[52:55], s67 offen offset:2048  lds\n"
"v_mfma_f32_16x16x32_bf16 v[72:75], a[40:43], a[120:123], v[72:75]\n"
"v_mfma_f32_16x16x32_bf16 v[72:75], a[44:47], a[124:127], v[72:75]\n"
"s_sub_u32 s67, 1618944, s75\n"
"v_mfma_f32_16x16x32_bf16 v[44:47], a[36:39], a[124:127], v[44:47]\n"
"v_mfma_f32_16x16x32_bf16 v[44:47], a[32:35], a[120:123], v[44:47]\n"
"v_mfma_f32_16x16x32_bf16 v[48:51], a[32:35], a[128:131], v[48:51]\n"
"buffer_load_dwordx4 v11, s[52:55], s67 offen offset:3072  lds\n"
"v_mfma_f32_16x16x32_bf16 v[48:51], a[36:39], a[132:135], v[48:51]\n"
"s_add_u32 m0, s64, 12288\n"
"v_mfma_f32_16x16x32_bf16 v[76:79], a[44:47], a[132:135], v[76:79]\n"
"s_waitcnt vmcnt(12)\n"
"ds_read_b64_tr_b16 a[64:65], v9 offset:0\n"
"v_mfma_f32_16x16x32_bf16 v[76:79], a[40:43], a[128:131], v[76:79]\n"
"s_sub_u32 s67, 1638400, s75\n"
"ds_read_b64_tr_b16 a[66:67], v9 offset:512\n"
"v_mfma_f32_16x16x32_bf16 v[80:83], a[40:43], a[136:139], v[80:83]\n"
"ds_read_b64_tr_b16 a[68:69], v9 offset:4096\n"
"v_mfma_f32_16x16x32_bf16 v[80:83], a[44:47], a[140:143], v[80:83]\n"
"ds_read_b64_tr_b16 a[70:71], v9 offset:4608\n"
"buffer_load_dwordx4 v11, s[52:55], s67 offen offset:0  lds\n"
"v_mfma_f32_16x16x32_bf16 v[52:55], a[36:39], a[140:143], v[52:55]\n"
"s_sub_u32 s67, 1653760, s75\n"
"ds_read_b64_tr_b16 a[72:73], v9 offset:8\n"
"v_mfma_f32_16x16x32_bf16 v[52:55], a[32:35], a[136:139], v[52:55]\n"
"L_st2677:\n"
"s_cbranch_execz L_retA\n"
"s_waitcnt lgkmcnt(5)\n"
"ds_read_b64_tr_b16 a[74:75], v9 offset:520\n"
"v_mfma_f32_16x16x32_bf16 v[56:59], a[32:35], a[144:147], v[56:59]\n"
"ds_read_b64_tr_b16 a[76:77], v9 offset:4104\n"
"v_mfma_f32_16x16x32_bf16 v[56:59], a[36:39], a[148:151], v[56:59]\n"
"ds_read_b64_tr_b16 a[78:79], v9 offset:4616\n"
"v_mfma_f32_16x16x32_bf16 v[84:87], a[44:47], a[148:151], v[84:87]\n"
"buffer_load_dwordx4 v11, s[52:55], s67 offen offset:1024  lds\n"
"v_mfma_f32_16x16x32_bf16 v[84:87], a[40:43], a[144:147], v[84:87]\n"
"ds_read_b64_tr_b16 a[80:81], v15 offset:0\n"
"v_mfma_f32_16x16x32_bf16 a[4:7], a[40:43], a[152:155], a[4:7]\n"
"s_sub_u32 s67, 1669120, s75\n"
"ds_read_b64_tr_b16 a[82:83], v15 offset:512\n"
"v_mfma_f32_16x16x32_bf16 a[4:7], a[44:47], a[156:159], a[4:7]\n"
"ds_read_b64_tr_b16 a[84:85], v15 offset:4096\n"
"v_mfma_f32_16x16x32_bf16 a[0:3], a[36:39], a[156:159], a[0:3]\n"
"ds_read_b64_tr_b16 a[86:87], v15 offset:4608\n"
"buffer_load_dwordx4 v11, s[52:55], s67 offen offset:2048  lds\n"
"v_mfma_f32_16x16x32_bf16 a[0:3], a[32:35], a[152:155], a[0:3]\n"
"ds_read_b64_tr_b16 a[88:89], v15 offset:8\n"
"v_mfma_f32_16x16x32_bf16 v[88:91], a[48:51], a[96:99], v[88:91]\n"
"s_sub_u32 s67, 1684480, s75\n"
"ds_read_b64_tr_b16 a[90:91], v15 offset:520\n"
"v_mfma_f32_16x16x32_bf16 v[88:91], a[52:55], a[100:103], v[88:91]\n"
"ds_read_b64_tr_b16 a[92:93], v15 offset:4104\n"
"v_mfma_f32_16x16x32_bf16 v[116:119], a[60:63], a[100:103], v[116:119]\n"
"ds_read_b64_tr_b16 a[94:95], v15 offset:4616\n"
"v_mfma_f32_16x16x32_bf16 v[116:119], a[56:59], a[96:99], v[116:119]\n"
"buffer_load_dwordx4 v11, s[52:55], s67 offen offset:3072  lds\n"
"v_mfma_f32_16x16x32_bf16 v[120:123], a[56:59], a[104:107], v[120:123]\n"
"v_mfma_f32_16x16x32_bf16 v[120:123], a[60:63], a[108:111], v[120:123]\n"
"s_add_u32 m0, s64, 0\n"
"v_mfma_f32_16x16x32_bf16 v[92:95], a[52:55], a[108:111], v[92:95]\n"
"v_mfma_f32_16x16x32_bf16 v[92:95], a[48:51], a[104:107], v[92:95]\n"
"s_sub_u32 s67, 1572992, s75\n"
"v_mfma_f32_16x16x32_bf16 v[96:99], a[48:51], a[112:115], v[96:99]\n"
"s_waitcnt lgkmcnt(0)\n"
"s_mov_b32 s99, 0\n"
"v_mfma_f32_16x16x32_bf16 v[96:99], a[52:55], a[116:119], v[96:99]\n"
"s_mov_b32 s98, 0\n"
"buffer_load_dwordx4 v11, s[52:55], s67 offen offset:0  lds\n"
"v_mfma_f32_16x16x32_bf16 v[124:127], a[60:63], a[116:119], v[124:127]\n"
"s_memtime s[76:77]\n"
"s_load_dword s98, s[96:97], 8320 glc\n"
"v_mfma_f32_16x16x32_bf16 v[124:127], a[56:59], a[112:115], v[124:127]\n"
"s_sub_u32 s67, 1588352, s75\n"
"v_mfma_f32_16x16x32_bf16 v[128:131], a[56:59], a[120:123], v[128:131]\n"
"v_mfma_f32_16x16x32_bf16 v[128:131], a[60:63], a[124:127], v[128:131]\n"
"buffer_load_dwordx4 v11, s[52:55], s67 offen offset:1024  lds\n"
"v_mfma_f32_16x16x32_bf16 v[100:103], a[52:55], a[124:127], v[100:103]\n"
"v_mfma_f32_16x16x32_bf16 v[100:103], a[48:51], a[120:123], v[100:103]\n"
"s_sub_u32 s67, 1603712, s75\n"
"v_mfma_f32_16x16x32_bf16 v[104:107], a[48:51], a[128:131], v[104:107]\n"
"v_mfma_f32_16x16x32_bf16 v[104:107], a[52:55], a[132:135], v[104:107]\n"
"v_mfma_f32_16x16x32_bf16 v[132:135], a[60:63], a[132:135], v[132:135]\n"
"buffer_load_dwordx4 v11, s[52:55], s67 offen offset:2048  lds\n"
"v_mfma_f32_16x16x32_bf16 v[132:135], a[56:59], a[128:131], v[132:135]\n"
"s_mov_b32 s29, s76\n"
"v_mfma_f32_16x16x32_bf16 v[136:139], a[56:59], a[136:139], v[136:139]\n"
"s_sub_u32 s67, 1619072, s75\n"
"v_mfma_f32_16x16x32_bf16 v[136:139], a[60:63], a[140:143], v[136:139]\n"
"buffer_load_dwordx4 v11, s[52:55], s67 offen offset:3072  lds\n"
"v_mfma_f32_16x16x32_bf16 v[108:111], a[52:55], a[140:143], v[108:111]\n"
"s_sub_u32 s99, s99, s98\n"
"s_sub_u32 s99, s99, s98\n"
"v_mfma_f32_16x16x32_bf16 v[108:111], a[48:51], a[136:139], v[108:111]\n"
"s_sub_u32 s99, s99, s98\n"
"s_add_u32 m0, s64, 4096\n"
"v_mfma_f32_16x16x32_bf16 v[112:115], a[48:51], a[144:147], v[112:115]\n"
"s_sub_u32 s99, s99, s98\n"
"v_mfma_f32_16x16x32_bf16 v[112:115], a[52:55], a[148:151], v[112:115]\n"
"s_sub_u32 s67, 1638528, s75\n"
"v_mfma_f32_16x16x32_bf16 v[140:143], a[60:63], a[148:151], v[140:143]\n"
"s_sub_u32 s99, s99, s98\n"
"s_sub_u32 s99, s99, s98\n"
"v_mfma_f32_16x16x32_bf16 v[140:143], a[56:59], a[144:147], v[140:143]\n"
"s_sub_u32 s99, s99, s98\n"
"buffer_load_dwordx4 v11, s[52:55], s67 offen offset:0  lds\n"
"v_mfma_f32_16x16x32_bf16 a[12:15], a[56:59], a[152:155], a[12:15]\n"
"s_sub_u32 s99, s99, s98\n"
"v_mfma_f32_16x16x32_bf16 a[12:15], a[60:63], a[156:159], a[12:15]\n"
"s_sub_u32 s67, 1653888, s75\n"
"v_mfma_f32_16x16x32_bf16 a[8:11], a[52:55], a[156:159], a[8:11]\n"
"s_sub_u32 s99, s99, s98\n"
"s_sub_u32 s99, s99, s98\n"
"v_mfma_f32_16x16x32_bf16 a[8:11], a[48:51], a[152:155], a[8:11]\n"
"s_sub_u32 s99, s99, s98\n"
"v_mfma_f32_16x16x32_bf16 v[144:147], a[64:67], a[96:99], v[144:147]\n"
"s_sub_u32 s99, s99, s98\n"
"v_mfma_f32_16x16x32_bf16 v[144:147], a[68:71], a[100:103], v[144:147]\n"
"buffer_load_dwordx4 v11, s[52:55], s67 offen offset:1024  lds\n"
"v_mfma_f32_16x16x32_bf16 v[172:175], a[76:79], a[100:103], v[172:175]\n"
"s_sub_u32 s99, s99, s98\n"
"s_sub_u32 s99, s99, s98\n"
"v_mfma_f32_16x16x32_bf16 v[172:175], a[72:75], a[96:99], v[172:175]\n"
"s_sub_u32 s99, s99, s98\n"
"s_sub_u32 s67, 1669248, s75\n"
"v_mfma_f32_16x16x32_bf16 v[176:179], a[72:75], a[104:107], v[176:179]\n"
"s_sub_u32 s99, s99, s98\n"
"v_mfma_f32_16x16x32_bf16 v[176:179], a[76:79], a[108:111], v[176:179]\n"
"buffer_load_dwordx4 v11, s[52:55], s67 offen offset:2048  lds\n"
"v_mfma_f32_16x16x32_bf16 v[148:151], a[68:71], a[108:111], v[148:151]\n"
"s_sub_u32 s99, s99, s98\n"
"s_sub_u32 s99, s99, s98\n"
"v_mfma_f32_16x16x32_bf16 v[148:151], a[64:67], a[104:107], v[148:151]\n"
"s_sub_u32 s99, s99, s98\n"
"s_sub_u32 s71, s64, s64\n"
"s_sub_u32 s67, 1684608, s75\n"
"v_mfma_f32_16x16x32_bf16 v[152:155], a[64:67], a[112:115], v[152:155]\n"
"s_sub_u32 s66, 81920, s73\n"
"s_sub_u32 s99, s99, s98\n"
"v_mfma_f32_16x16x32_bf16 v[152:155], a[68:71], a[116:119], v[152:155]\n"
"s_add_u32 s71, s71, 20480\n"
"buffer_load_dwordx4 v11, s[52:55], s67 offen offset:3072  lds\n"
"v_mfma_f32_16x16x32_bf16 v[180:183], a[76:79], a[116:119], v[180:183]\n"
"s_sub_u32 s99, s99, s98\n"
"v_add_u32 v12, s66, v7\n"
"s_sub_u32 s99, s99, s98\n"
"v_mfma_f32_16x16x32_bf16 v[180:183], a[72:75], a[112:115], v[180:183]\n"
"s_sub_u32 s99, s99, s98\n"
"s_cmp_lt_u32 %16, 2\n"
"s_cselect_b32 s67, 16384, 0\n"
"s_add_u32 s67, s67, s72\n"
"v_mfma_f32_16x16x32_bf16 v[184:187], a[72:75], a[120:123], v[184:187]\n"
"s_sub_u32 s99, s99, s98\n"
"v_add_u32 v13, s66, v8\n"
"s_add_u32 m0, s67, 139264\n"
"v_mfma_f32_16x16x32_bf16 v[184:187], a[76:79], a[124:127], v[184:187]\n"
"s_memtime s[76:77]\n"
"v_add_u32 v20, s71, v7\n"
"s_sub_u32 s67, 1792, s60\n"
"v_mfma_f32_16x16x32_bf16 v[156:159], a[68:71], a[124:127], v[156:159]\n"
"buffer_load_dwordx4 v6, s[48:51], s67 offen offset:0  lds\n"
"v_mfma_f32_16x16x32_bf16 v[156:159], a[64:67], a[120:123], v[156:159]\n"
"s_waitcnt lgkmcnt(0)\n"
"v_add_u32 v21, s71, v8\n"
"v_mfma_f32_16x16x32_bf16 v[160:163], a[64:67], a[128:131], v[160:163]\n"
"s_sub_u32 s29, s76, s29\n"
"s_sub_u32 s67, 17152, s60\n"
"v_mfma_f32_16x16x32_bf16 v[160:163], a[68:71], a[132:135], v[160:163]\n"
"s_cmp_lt_u32 s29, 620\n"
"s_cselect_b32 s99, s99, 0\n"
"v_mfma_f32_16x16x32_bf16 v[188:191], a[76:79], a[132:135], v[188:191]\n"
"s_max_u32 s31, s31, s99\n"
"buffer_load_dwordx4 v6, s[48:51], s67 offen offset:1024  lds\n"
"v_mfma_f32_16x16x32_bf16 v[188:191], a[72:75], a[128:131], v[188:191]\n"
"s_waitcnt vmcnt(18)\n"
"ds_read_b128 a[224:227], v20 offset:0\n"
"v_mfma_f32_16x16x32_bf16 v[192:195], a[72:75], a[136:139], v[192:195]\n"
"s_sub_u32 s67, 32512, s60\n"
"ds_read_b128 a[228:231], v21 offset:0\n"
"v_mfma_f32_16x16x32_bf16 v[192:195], a[76:79], a[140:143], v[192:195]\n"
"ds_read_b128 a[232:235], v20 offset:2048\n"
"v_mfma_f32_16x16x32_bf16 v[164:167], a[68:71], a[140:143], v[164:167]\n"
"v_mfma_f32_16x16x32_bf16 v[164:167], a[64:67], a[136:139], v[164:167]\n"
"ds_read_b128 a[236:239], v21 offset:2048\n"
"v_mfma_f32_16x16x32_bf16 v[168:171], a[64:67], a[144:147], v[168:171]\n"
"buffer_load_dwordx4 v6, s[48:51], s67 offen offset:2048  lds\n"
"v_mfma_f32_16x16x32_bf16 v[168:171], a[68:71], a[148:151], v[168:171]\n"
"v_mfma_f32_16x16x32_bf16 v[196:199], a[76:79], a[148:151], v[196:199]\n"
"s_sub_u32 s67, 47872, s60\n"
"v_mfma_f32_16x16x32_bf16 v[196:199], a[72:75], a[144:147], v[196:199]\n"
"v_mfma_f32_16x16x32_bf16 a[20:23], a[72:75], a[152:155], a[20:23]\n"
"buffer_load_dwordx4 v6, s[48:51], s67 offen offset:3072  lds\n"
"v_mfma_f32_16x16x32_bf16 a[20:23], a[76:79], a[156:159], a[20:23]\n"
"s_waitcnt lgkmcnt(0)\n"
"v_mfma_f32_16x16x32_bf16 a[16:19], a[68:71], a[156:159], a[16:19]\n"
"s_add_u32 m0, s64, 16256\n"
"v_mfma_f32_16x16x32_bf16 a[16:19], a[64:67], a[152:155], a[16:19]\n"
"s_barrier\n"
"s_waitcnt vmcnt(12)\n"
"ds_read_b64_tr_b16 a[32:33], v9 offset:8192\n"
"v_mfma_f32_16x16x32_bf16 v[200:203], a[80:83], a[96:99], v[200:203]\n"
"ds_read_b64_tr_b16 a[34:35], v9 offset:8704\n"
"buffer_load_dwordx4 v6, s[40:43], s92 offen offset:128  lds\n"
"v_mfma_f32_16x16x32_bf16 v[200:203], a[84:87], a[100:103], v[200:203]\n"
"ds_read_b64_tr_b16 a[36:37], v9 offset:12288\n"
"v_mfma_f32_16x16x32_bf16 v[228:231], a[92:95], a[100:103], v[228:231]\n"
"ds_read_b128 a[100:103], v13 offset:32768\n"
"v_mfma_f32_16x16x32_bf16 v[228:231], a[88:91], a[96:99], v[228:231]\n"
"ds_read_b128 a[96:99], v12 offset:32768\n"
"buffer_load_dwordx4 v6, s[40:43], s93 offen offset:1152  lds\n"
"v_mfma_f32_16x16x32_bf16 v[232:235], a[88:91], a[104:107], v[232:235]\n"
"ds_read_b64_tr_b16 a[38:39], v9 offset:12800\n"
"v_mfma_f32_16x16x32_bf16 v[232:235], a[92:95], a[108:111], v[232:235]\n"
"ds_read_b64_tr_b16 a[40:41], v9 offset:8200\n"
"v_mfma_f32_16x16x32_bf16 v[204:207], a[84:87], a[108:111], v[204:207]\n"
"ds_read_b128 a[108:111], v13 offset:34816\n"
"v_mfma_f32_16x16x32_bf16 v[204:207], a[80:83], a[104:107], v[204:207]\n"
"ds_read_b128 a[104:107], v12 offset:34816\n"
"v_mfma_f32_16x16x32_bf16 v[208:211], a[80:83], a[112:115], v[208:211]\n"
"ds_read_b64_tr_b16 a[42:43], v9 offset:8712\n"
"buffer_load_dwordx4 v6, s[40:43], s94 offen offset:2176  lds\n"
"v_mfma_f32_16x16x32_bf16 v[208:211], a[84:87], a[116:119], v[208:211]\n"
"ds_read_b64_tr_b16 a[44:45], v9 offset:12296\n"
"v_mfma_f32_16x16x32_bf16 v[236:239], a[92:95], a[116:119], v[236:239]\n"
"ds_read_b128 a[116:119], v13 offset:36864\n"
"v_mfma_f32_16x16x32_bf16 v[236:239], a[88:91], a[112:115], v[236:239]\n"
"ds_read_b128 a[112:115], v12 offset:36864\n"
"v_mfma_f32_16x16x32_bf16 v[240:243], a[88:91], a[120:123], v[240:243]\n"
"ds_read_b64_tr_b16 a[46:47], v9 offset:12808\n"
"v_mfma_f32_16x16x32_bf16 v[240:243], a[92:95], a[124:127], v[240:243]\n"
"ds_read_b64_tr_b16 a[48:49], v15 offset:8192\n"
"buffer_load_dwordx4 v6, s[40:43], s95 offen offset:3200  lds\n"
"v_mfma_f32_16x16x32_bf16 v[212:215], a[84:87], a[124:127], v[212:215]\n"
"ds_read_b128 a[124:127], v13 offset:38912\n"
"v_mfma_f32_16x16x32_bf16 v[212:215], a[80:83], a[120:123], v[212:215]\n"
"ds_read_b128 a[120:123], v12 offset:38912\n"
"v_mfma_f32_16x16x32_bf16 v[216:219], a[80:83], a[128:131], v[216:219]\n"
"ds_read_b64_tr_b16 a[50:51], v15 offset:8704\n"
"v_mfma_f32_16x16x32_bf16 v[216:219], a[84:87], a[132:135], v[216:219]\n"
"s_add_u32 m0, s64, 8192\n"
"ds_read_b64_tr_b16 a[52:53], v15 offset:12288\n"
"v_mfma_f32_16x16x32_bf16 v[244:247], a[92:95], a[132:135], v[244:247]\n"
"ds_read_b128 a[132:135], v13 offset:24576\n"
"v_mfma_f32_16x16x32_bf16 v[244:247], a[88:91], a[128:131], v[244:247]\n"
"ds_read_b128 a[128:131], v12 offset:24576\n"
"v_mfma_f32_16x16x32_bf16 v[248:251], a[88:91], a[136:139], v[248:251]\n"
"ds_read_b64_tr_b16 a[54:55], v15 offset:12800\n"
"v_mfma_f32_16x16x32_bf16 v[248:251], a[92:95], a[140:143], v[248:251]\n"
"ds_read_b64_tr_b16 a[56:57], v15 offset:8200\n"
"v_mfma_f32_16x16x32_bf16 v[220:223], a[84:87], a[140:143], v[220:223]\n"
"ds_read_b128 a[140:143], v13 offset:26624\n"
"v_mfma_f32_16x16x32_bf16 v[220:223], a[80:83], a[136:139], v[220:223]\n"
"ds_read_b128 a[136:139], v12 offset:26624\n"
"v_mfma_f32_16x16x32_bf16 v[224:227], a[80:83], a[144:147], v[224:227]\n"
"ds_read_b64_tr_b16 a[58:59], v15 offset:8712\n"
"v_mfma_f32_16x16x32_bf16 v[224:227], a[84:87], a[148:151], v[224:227]\n"
"s_sub_u32 s67, 1835008, s75\n"
"ds_read_b64_tr_b16 a[60:61], v15 offset:12296\n"
"v_mfma_f32_16x16x32_bf16 v[252:255], a[92:95], a[148:151], v[252:255]\n"
"ds_read_b128 a[148:151], v13 offset:28672\n"
"v_mfma_f32_16x16x32_bf16 v[252:255], a[88:91], a[144:147], v[252:255]\n"
"ds_read_b128 a[144:147], v12 offset:28672\n"
"ds_read_b64_tr_b16 a[62:63], v15 offset:12808\n"
"v_mfma_f32_16x16x32_bf16 a[28:31], a[88:91], a[152:155], a[28:31]\n"
"v_mfma_f32_16x16x32_bf16 a[28:31], a[92:95], a[156:159], a[28:31]\n"
"v_mfma_f32_16x16x32_bf16 a[24:27], a[84:87], a[156:159], a[24:27]\n"
"ds_read_b128 a[156:159], v13 offset:30720\n"
"v_mfma_f32_16x16x32_bf16 a[24:27], a[80:83], a[152:155], a[24:27]\n"
"s_waitcnt lgkmcnt(15)\n"
"ds_read_b128 a[152:155], v12 offset:30720\n"
"v_mfma_f32_16x16x32_bf16 v[32:35], a[32:35], a[96:99], v[32:35]\n"
"v_mfma_f32_16x16x32_bf16 v[32:35], a[36:39], a[100:103], v[32:35]\n"
"s_waitcnt lgkmcnt(2)\n"
"v_mfma_f32_16x16x32_bf16 v[60:63], a[44:47], a[100:103], v[60:63]\n"
"buffer_load_dwordx4 v11, s[52:55], s67 offen offset:0  lds\n"
"v_mfma_f32_16x16x32_bf16 v[60:63], a[40:43], a[96:99], v[60:63]\n"
"v_mfma_f32_16x16x32_bf16 v[64:67], a[40:43], a[104:107], v[64:67]\n"
"s_sub_u32 s67, 1850368, s75\n"
"v_mfma_f32_16x16x32_bf16 v[64:67], a[44:47], a[108:111], v[64:67]\n"
"v_mfma_f32_16x16x32_bf16 v[36:39], a[36:39], a[108:111], v[36:39]\n"
"v_mfma_f32_16x16x32_bf16 v[36:39], a[32:35], a[104:107], v[36:39]\n"
"buffer_load_dwordx4 v11, s[52:55], s67 offen offset:1024  lds\n"
"v_mfma_f32_16x16x32_bf16 v[40:43], a[32:35], a[112:115], v[40:43]\n"
"v_mfma_f32_16x16x32_bf16 v[40:43], a[36:39], a[116:119], v[40:43]\n"
"s_sub_u32 s67, 1865728, s75\n"
"v_mfma_f32_16x16x32_bf16 v[68:71], a[44:47], a[116:119], v[68:71]\n"
"v_mfma_f32_16x16x32_bf16 v[68:71], a[40:43], a[112:115], v[68:71]\n"
"buffer_load_dwordx4 v11, s[52:55], s67 offen offset:2048  lds\n"
"v_mfma_f32_16x16x32_bf16 v[72:75], a[40:43], a[120:123], v[72:75]\n"
"v_mfma_f32_16x16x32_bf16 v[72:75], a[44:47], a[124:127], v[72:75]\n"
"s_sub_u32 s67, 1881088, s75\n"
"v_mfma_f32_16x16x32_bf16 v[44:47], a[36:39], a[124:127], v[44:47]\n"
"v_mfma_f32_16x16x32_bf16 v[44:47], a[32:35], a[120:123], v[44:47]\n"
"v_mfma_f32_16x16x32_bf16 v[48:51], a[32:35], a[128:131], v[48:51]\n"
"buffer_load_dwordx4 v11, s[52:55], s67 offen offset:3072  lds\n"
"v_mfma_f32_16x16x32_bf16 v[48:51], a[36:39], a[132:135], v[48:51]\n"
"s_add_u32 m0, s64, 12288\n"
"v_mfma_f32_16x16x32_bf16 v[76:79], a[44:47], a[132:135], v[76:79]\n"
"s_waitcnt vmcnt(12)\n"
"ds_read_b64_tr_b16 a[64:65], v9 offset:0\n"
"v_mfma_f32_16x16x32_bf16 v[76:79], a[40:43], a[128:131], v[76:79]\n"
"ds_read_b64_tr_b16 a[66:67], v9 offset:512\n"
"v_mfma_f32_16x16x32_bf16 v[80:83], a[40:43], a[136:139], v[80:83]\n"
"s_sub_u32 s67, 1900544, s75\n"
"ds_read_b64_tr_b16 a[68:69], v9 offset:4096\n"
"v_mfma_f32_16x16x32_bf16 v[80:83], a[44:47], a[140:143], v[80:83]\n"
"ds_read_b64_tr_b16 a[70:71], v9 offset:4608\n"
"v_mfma_f32_16x16x32_bf16 v[52:55], a[36:39], a[140:143], v[52:55]\n"
"ds_read_b64_tr_b16 a[72:73], v9 offset:8\n"
"buffer_load_dwordx4 v11, s[52:55], s67 offen offset:0  lds\n"
"v_mfma_f32_16x16x32_bf16 v[52:55], a[32:35], a[136:139], v[52:55]\n"
"s_waitcnt lgkmcnt(5)\n"
"ds_read_b64_tr_b16 a[74:75], v9 offset:520\n"
"v_mfma_f32_16x16x32_bf16 v[56:59], a[32:35], a[144:147], v[56:59]\n"
"s_sub_u32 s67, 1915904, s75\n"
"ds_read_b64_tr_b16 a[76:77], v9 offset:4104\n"
"v_mfma_f32_16x16x32_bf16 v[56:59], a[36:39], a[148:151], v[56:59]\n"
"ds_read_b64_tr_b16 a[78:79], v9 offset:4616\n"
"v_mfma_f32_16x16x32_bf16 v[84:87], a[44:47], a[148:151], v[84:87]\n"
"buffer_load_dwordx4 v11, s[52:55], s67 offen offset:1024  lds\n"
"v_mfma_f32_16x16x32_bf16 v[84:87], a[40:43], a[144:147], v[84:87]\n"
"ds_read_b64_tr_b16 a[80:81], v15 offset:0\n"
"v_mfma_f32_16x16x32_bf16 a[4:7], a[40:43], a[152:155], a[4:7]\n"
"s_sub_u32 s67, 1931264, s75\n"
"ds_read_b64_tr_b16 a[82:83], v15 offset:512\n"
"v_mfma_f32_16x16x32_bf16 a[4:7], a[44:47], a[156:159], a[4:7]\n"
"ds_read_b64_tr_b16 a[84:85], v15 offset:4096\n"
"v_mfma_f32_16x16x32_bf16 a[0:3], a[36:39], a[156:159], a[0:3]\n"
"ds_read_b64_tr_b16 a[86:87], v15 offset:4608\n"
"buffer_load_dwordx4 v11, s[52:55], s67 offen offset:2048  lds\n"
"v_mfma_f32_16x16x32_bf16 a[0:3], a[32:35], a[152:155], a[0:3]\n"
"ds_read_b64_tr_b16 a[88:89], v15 offset:8\n"
"v_mfma_f32_16x16x32_bf16 v[88:91], a[48:51], a[96:99], v[88:91]\n"
"s_sub_u32 s67, 1946624, s75\n"
"ds_read_b64_tr_b16 a[90:91], v15 offset:520\n"
"v_mfma_f32_16x16x32_bf16 v[88:91], a[52:55], a[100:103], v[88:91]\n"
"ds_read_b64_tr_b16 a[92:93], v15 offset:4104\n"
"v_mfma_f32_16x16x32_bf16 v[116:119], a[60:63], a[100:103], v[116:119]\n"
"ds_read_b64_tr_b16 a[94:95], v15 offset:4616\n"
"v_mfma_f32_16x16x32_bf16 v[116:119], a[56:59], a[96:99], v[116:119]\n"
"buffer_load_dwordx4 v11, s[52:55], s67 offen offset:3072  lds\n"
"v_mfma_f32_16x16x32_bf16 v[120:123], a[56:59], a[104:107], v[120:123]\n"
"v_mfma_f32_16x16x32_bf16 v[120:123], a[60:63], a[108:111], v[120:123]\n"
"s_add_u32 m0, s64, 0\n"
"v_mfma_f32_16x16x32_bf16 v[92:95], a[52:55], a[108:111], v[92:95]\n"
"v_mfma_f32_16x16x32_bf16 v[92:95], a[48:51], a[104:107], v[92:95]\n"
"s_sub_u32 s67, 1835136, s75\n"
"v_mfma_f32_16x16x32_bf16 v[96:99], a[48:51], a[112:115], v[96:99]\n"
"s_waitcnt lgkmcnt(0)\n"
"s_mov_b32 s99, 0\n"
"v_mfma_f32_16x16x32_bf16 v[96:99], a[52:55], a[116:119], v[96:99]\n"
"s_mov_b32 s98, 0\n"
"buffer_load_dwordx4 v11, s[52:55], s67 offen offset:0  lds\n"
"v_mfma_f32_16x16x32_bf16 v[124:127], a[60:63], a[116:119], v[124:127]\n"
"s_memtime s[76:77]\n"
"s_load_dword s98, s[96:97], 128 glc\n"
"v_mfma_f32_16x16x32_bf16 v[124:127], a[56:59], a[112:115], v[124:127]\n"
"s_sub_u32 s67, 1850496, s75\n"
"v_mfma_f32_16x16x32_bf16 v[128:131], a[56:59], a[120:123], v[128:131]\n"
"v_mfma_f32_16x16x32_bf16 v[128:131], a[60:63], a[124:127], v[128:131]\n"
"v_mfma_f32_16x16x32_bf16 v[100:103], a[52:55], a[124:127], v[100:103]\n"
"buffer_load_dwordx4 v11, s[52:55], s67 offen offset:1024  lds\n"
"v_mfma_f32_16x16x32_bf16 v[100:103], a[48:51], a[120:123], v[100:103]\n"
"v_mfma_f32_16x16x32_bf16 v[104:107], a[48:51], a[128:131], v[104:107]\n"
"s_sub_u32 s67, 1865856, s75\n"
"v_mfma_f32_16x16x32_bf16 v[104:107], a[52:55], a[132:135], v[104:107]\n"
"v_mfma_f32_16x16x32_bf16 v[132:135], a[60:63], a[132:135], v[132:135]\n"
"buffer_load_dwordx4 v11, s[52:55], s67 offen offset:2048  lds\n"
"v_mfma_f32_16x16x32_bf16 v[132:135], a[56:59], a[128:131], v[132:135]\n"
"s_mov_b32 s29, s76\n"
"v_mfma_f32_16x16x32_bf16 v[136:139], a[56:59], a[136:139], v[136:139]\n"
"s_sub_u32 s67, 1881216, s75\n"
"v_mfma_f32_16x16x32_bf16 v[136:139], a[60:63], a[140:143], v[136:139]\n"
"v_mfma_f32_16x16x32_bf16 v[108:111], a[52:55], a[140:143], v[108:111]\n"
"s_sub_u32 s99, s99, s98\n"
"s_sub_u32 s99, s99, s98\n"
"v_mfma_f32_16x16x32_bf16 v[108:111], a[48:51], a[136:139], v[108:111]\n"
"s_sub_u32 s99, s99, s98\n"
"buffer_load_dwordx4 v11, s[52:55], s67 offen offset:3072  lds\n"
"v_mfma_f32_16x16x32_bf16 v[112:115], a[48:51], a[144:147], v[112:115]\n"
"s_sub_u32 s99, s99, s98\n"
"s_add_u32 m0, s64, 4096\n"
"v_mfma_f32_16x16x32_bf16 v[112:115], a[52:55], a[148:151], v[112:115]\n"
"v_mfma_f32_16x16x32_bf16 v[140:143], a[60:63], a[148:151], v[140:143]\n"
"s_sub_u32 s99, s99, s98\n"
"s_sub_u32 s99, s99, s98\n"
"buffer_load_dwordx4 v[24:27], v0, s[56:59], %25 offen sc1\n"
"v_mfma_f32_16x16x32_bf16 v[140:143], a[56:59], a[144:147], v[140:143]\n"
"s_sub_u32 s99, s99, s98\n"
"s_sub_u32 s67, 1900672, s75\n"
"v_mfma_f32_16x16x32_bf16 a[12:15], a[56:59], a[152:155], a[12:15]\n"
"s_sub_u32 s99, s99, s98\n"
"buffer_load_dwordx4 v11, s[52:55], s67 offen offset:0  lds\n"
"v_mfma_f32_16x16x32_bf16 a[12:15], a[60:63], a[156:159], a[12:15]\n"
"s_sub_u32 s67, 1916032, s75\n"
"v_mfma_f32_16x16x32_bf16 a[8:11], a[52:55], a[156:159], a[8:11]\n"
"s_sub_u32 s99, s99, s98\n"
"s_sub_u32 s99, s99, s98\n"
"v_mfma_f32_16x16x32_bf16 a[8:11], a[48:51], a[152:155], a[8:11]\n"
"s_sub_u32 s99, s99, s98\n"
"v_mfma_f32_16x16x32_bf16 v[144:147], a[64:67], a[96:99], v[144:147]\n"
"s_sub_u32 s99, s99, s98\n"
"v_mfma_f32_16x16x32_bf16 v[144:147], a[68:71], a[100:103], v[144:147]\n"
"buffer_load_dwordx4 v11, s[52:55], s67 offen offset:1024  lds\n"
"v_mfma_f32_16x16x32_bf16 v[172:175], a[76:79], a[100:103], v[172:175]\n"
"s_sub_u32 s99, s99, s98\n"
"s_sub_u32 s99, s99, s98\n"
"v_mfma_f32_16x16x32_bf16 v[172:175], a[72:75], a[96:99], v[172:175]\n"
"s_sub_u32 s99, s99, s98\n"
"s_sub_u32 s67, 1931392, s75\n"
"v_mfma_f32_16x16x32_bf16 v[176:179], a[72:75], a[104:107], v[176:179]\n"
"s_sub_u32 s99, s99, s98\n"
"v_mfma_f32_16x16x32_bf16 v[176:179], a[76:79], a[108:111], v[176:179]\n"
"buffer_load_dwordx4 v11, s[52:55], s67 offen offset:2048  lds\n"
"v_mfma_f32_16x16x32_bf16 v[148:151], a[68:71], a[108:111], v[148:151]\n"
"s_sub_u32 s99, s99, s98\n"
"s_sub_u32 s99, s99, s98\n"
"v_mfma_f32_16x16x32_bf16 v[148:151], a[64:67], a[104:107], v[148:151]\n"
"s_sub_u32 s99, s99, s98\n"
"s_sub_u32 s71, s64, s64\n"
"s_sub_u32 s67, 1946752, s75\n"
"v_mfma_f32_16x16x32_bf16 v[152:155], a[64:67], a[112:115], v[152:155]\n"
"s_sub_u32 s66, 122880, s73\n"
"s_sub_u32 s99, s99, s98\n"
"v_mfma_f32_16x16x32_bf16 v[152:155], a[68:71], a[116:119], v[152:155]\n"
"s_add_u32 s71, s71, 16384\n"
"buffer_load_dwordx4 v11, s[52:55], s67 offen offset:3072  lds\n"
"v_mfma_f32_16x16x32_bf16 v[180:183], a[76:79], a[116:119], v[180:183]\n"
"s_sub_u32 s99, s99, s98\n"
"v_add_u32 v12, s66, v7\n"
"s_sub_u32 s99, s99, s98\n"
"v_mfma_f32_16x16x32_bf16 v[180:183], a[72:75], a[112:115], v[180:183]\n"
"s_sub_u32 s99, s99, s98\n"
"v_min_u32 v19, 496, v0\n"
"v_mfma_f32_16x16x32_bf16 v[184:187], a[72:75], a[120:123], v[184:187]\n"
"s_sub_u32 s99, s99, s98\n"
"v_add_u32 v13, s66, v8\n"
"s_add_u32 s70, %21, 0\n"
"v_mfma_f32_16x16x32_bf16 v[184:187], a[76:79], a[124:127], v[184:187]\n"
"s_memtime s[76:77]\n"
"v_add_u32 v20, s71, v7\n"
"v_lshl_add_u32 v19, v19, 7, s70\n"
"v_mfma_f32_16x16x32_bf16 v[156:159], a[68:71], a[124:127], v[156:159]\n"
"buffer_load_dword v23, v19, s[52:55], 0 offen\n"
"v_mfma_f32_16x16x32_bf16 v[156:159], a[64:67], a[120:123], v[156:159]\n"
"s_waitcnt lgkmcnt(0)\n"
"v_add_u32 v21, s71, v8\n"
"v_min_u32 v19, 496, v0\n"
"v_mfma_f32_16x16x32_bf16 v[160:163], a[64:67], a[128:131], v[160:163]\n"
"s_sub_u32 s29, s76, s29\n"
"s_add_u32 s70, %21, 2097152\n"
"v_lshl_add_u32 v19, v19, 7, s70\n"
"v_mfma_f32_16x16x32_bf16 v[160:163], a[68:71], a[132:135], v[160:163]\n"
"s_cmp_lt_u32 s29, 620\n"
"s_cselect_b32 s99, s99, 0\n"
"buffer_load_dword v23, v19, s[52:55], 0 offen\n"
"v_mfma_f32_16x16x32_bf16 v[188:191], a[76:79], a[132:135], v[188:191]\n"
"s_max_u32 s30, s30, s99\n"
"v_mfma_f32_16x16x32_bf16 v[188:191], a[72:75], a[128:131], v[188:191]\n"
"s_waitcnt vmcnt(19)\n"
"ds_read_b128 a[240:243], v20 offset:0\n"
"v_mfma_f32_16x16x32_bf16 v[192:195], a[72:75], a[136:139], v[192:195]\n"
"ds_read_b128 a[244:247], v21 offset:0\n"
"v_mfma_f32_16x16x32_bf16 v[192:195], a[76:79], a[140:143], v[192:195]\n"
"ds_read_b128 a[248:251], v20 offset:2048\n"
"v_mfma_f32_16x16x32_bf16 v[164:167], a[68:71], a[140:143], v[164:167]\n"
"v_mfma_f32_16x16x32_bf16 v[164:167], a[64:67], a[136:139], v[164:167]\n"
"ds_read_b128 a[252:255], v21 offset:2048\n"
"v_mfma_f32_16x16x32_bf16 v[168:171], a[64:67], a[144:147], v[168:171]\n"
"v_mfma_f32_16x16x32_bf16 v[168:171], a[68:71], a[148:151], v[168:171]\n"
"v_mfma_f32_16x16x32_bf16 v[196:199], a[76:79], a[148:151], v[196:199]\n"
"v_mfma_f32_16x16x32_bf16 v[196:199], a[72:75], a[144:147], v[196:199]\n"
"v_mfma_f32_16x16x32_bf16 a[20:23], a[72:75], a[152:155], a[20:23]\n"
"v_mfma_f32_16x16x32_bf16 a[20:23], a[76:79], a[156:159], a[20:23]\n"
"s_waitcnt lgkmcnt(0)\n"
"v_mfma_f32_16x16x32_bf16 a[16:19], a[68:71], a[156:159], a[16:19]\n"
"v_mfma_f32_16x16x32_bf16 a[16:19], a[64:67], a[152:155], a[16:19]\n"
"s_barrier\n"
"s_waitcnt vmcnt(11)\n"
"ds_read_b64_tr_b16 a[32:33], v9 offset:8192\n"
"v_mfma_f32_16x16x32_bf16 v[200:203], a[80:83], a[96:99], v[200:203]\n"
"ds_read_b64_tr_b16 a[34:35], v9 offset:8704\n"
"v_mfma_f32_16x16x32_bf16 v[200:203], a[84:87], a[100:103], v[200:203]\n"
"ds_read_b64_tr_b16 a[36:37], v9 offset:12288\n"
"v_mfma_f32_16x16x32_bf16 v[228:231], a[92:95], a[100:103], v[228:231]\n"
"ds_read_b128 a[100:103], v13 offset:32768\n"
"v_mfma_f32_16x16x32_bf16 v[228:231], a[88:91], a[96:99], v[228:231]\n"
"ds_read_b128 a[96:99], v12 offset:32768\n"
"v_mfma_f32_16x16x32_bf16 v[232:235], a[88:91], a[104:107], v[232:235]\n"
"ds_read_b64_tr_b16 a[38:39], v9 offset:12800\n"
"v_mfma_f32_16x16x32_bf16 v[232:235], a[92:95], a[108:111], v[232:235]\n"
"ds_read_b64_tr_b16 a[40:41], v9 offset:8200\n"
"s_getpc_b64 s[70:71]\n"
"L_c11_:\n"
"s_add_u32 s70, s70, L_P6-L_c11_\n"
"s_addc_u32 s71, s71, 0\n"
"v_mfma_f32_16x16x32_bf16 v[204:207], a[84:87], a[108:111], v[204:207]\n"
"ds_read_b128 a[108:111], v13 offset:34816\n"
"v_mfma_f32_16x16x32_bf16 v[204:207], a[80:83], a[104:107], v[204:207]\n"
"s_lshr_b32 s66, %26, 3\n"
"ds_read_b128 a[104:107], v12 offset:34816\n"
"v_mfma_f32_16x16x32_bf16 v[208:211], a[80:83], a[112:115], v[208:211]\n"
"ds_read_b64_tr_b16 a[42:43], v9 offset:8712\n"
"v_mfma_f32_16x16x32_bf16 v[208:211], a[84:87], a[116:119], v[208:211]\n"
"ds_read_b64_tr_b16 a[44:45], v9 offset:12296\n"
"v_mfma_f32_16x16x32_bf16 v[236:239], a[92:95], a[116:119], v[236:239]\n"
"s_lshl2_add_u32 s66, s66, %16\n"
"ds_read_b128 a[116:119], v13 offset:36864\n"
"v_mfma_f32_16x16x32_bf16 v[236:239], a[88:91], a[112:115], v[236:239]\n"
"ds_read_b128 a[112:115], v12 offset:36864\n"
"v_mfma_f32_16x16x32_bf16 v[240:243], a[88:91], a[120:123], v[240:243]\n"
"ds_read_b64_tr_b16 a[46:47], v9 offset:12808\n"
"v_mfma_f32_16x16x32_bf16 v[240:243], a[92:95], a[124:127], v[240:243]\n"
"s_mul_i32 s66, s66, 384\n"
"ds_read_b64_tr_b16 a[48:49], v15 offset:8192\n"
"v_mfma_f32_16x16x32_bf16 v[212:215], a[84:87], a[124:127], v[212:215]\n"
"ds_read_b128 a[124:127], v13 offset:38912\n"
"v_mfma_f32_16x16x32_bf16 v[212:215], a[80:83], a[120:123], v[212:215]\n"
"s_min_u32 s66, s66, L_headF-L_P6-384\n"
"ds_read_b128 a[120:123], v12 offset:38912\n"
"v_mfma_f32_16x16x32_bf16 v[216:219], a[80:83], a[128:131], v[216:219]\n"
"ds_read_b64_tr_b16 a[50:51], v15 offset:8704\n"
"v_mfma_f32_16x16x32_bf16 v[216:219], a[84:87], a[132:135], v[216:219]\n"
"ds_read_b64_tr_b16 a[52:53], v15 offset:12288\n"
"v_mfma_f32_16x16x32_bf16 v[244:247], a[92:95], a[132:135], v[244:247]\n"
"ds_read_b128 a[132:135], v13 offset:24576\n"
"v_min_u32 v22, 32, v0\n"
"v_mfma_f32_16x16x32_bf16 v[244:247], a[88:91], a[128:131], v[244:247]\n"
"ds_read_b128 a[128:131], v12 offset:24576\n"
"v_mfma_f32_16x16x32_bf16 v[248:251], a[88:91], a[136:139], v[248:251]\n"
"ds_read_b64_tr_b16 a[54:55], v15 offset:12800\n"
"v_mfma_f32_16x16x32_bf16 v[248:251], a[92:95], a[140:143], v[248:251]\n"
"ds_read_b64_tr_b16 a[56:57], v15 offset:8200\n"
"v_lshl_add_u32 v22, v22, 3, s66\n"
"v_mfma_f32_16x16x32_bf16 v[220:223], a[84:87], a[140:143], v[220:223]\n"
"ds_read_b128 a[140:143], v13 offset:26624\n"
"v_mfma_f32_16x16x32_bf16 v[220:223], a[80:83], a[136:139], v[220:223]\n"
"ds_read_b128 a[136:139], v12 offset:26624\n"
"global_load_dword v23, v22, s[70:71]\n"
"v_mfma_f32_16x16x32_bf16 v[224:227], a[80:83], a[144:147], v[224:227]\n"
"ds_read_b64_tr_b16 a[58:59], v15 offset:8712\n"
"v_mfma_f32_16x16x32_bf16 v[224:227], a[84:87], a[148:151], v[224:227]\n"
"ds_read_b64_tr_b16 a[60:61], v15 offset:12296\n"
"v_mfma_f32_16x16x32_bf16 v[252:255], a[92:95], a[148:151], v[252:255]\n"
"ds_read_b128 a[148:151], v13 offset:28672\n"
"v_mfma_f32_16x16x32_bf16 v[252:255], a[88:91], a[144:147], v[252:255]\n"
"ds_read_b128 a[144:147], v12 offset:28672\n"
"ds_read_b64_tr_b16 a[62:63], v15 offset:12808\n"
"v_mfma_f32_16x16x32_bf16 a[28:31], a[88:91], a[152:155], a[28:31]\n"
"v_mfma_f32_16x16x32_bf16 a[28:31], a[92:95], a[156:159], a[28:31]\n"
"v_mfma_f32_16x16x32_bf16 a[24:27], a[84:87], a[156:159], a[24:27]\n"
"ds_read_b128 a[156:159], v13 offset:30720\n"
"v_mfma_f32_16x16x32_bf16 a[24:27], a[80:83], a[152:155], a[24:27]\n"
"s_waitcnt lgkmcnt(15)\n"
"ds_read_b128 a[152:155], v12 offset:30720\n"
"v_mfma_f32_16x16x32_bf16 v[32:35], a[32:35], a[96:99], v[32:35]\n"
"v_mfma_f32_16x16x32_bf16 v[32:35], a[36:39], a[100:103], v[32:35]\n"
"s_waitcnt lgkmcnt(2)\n"
"v_mfma_f32_16x16x32_bf16 v[60:63], a[44:47], a[100:103], v[60:63]\n"
"v_mfma_f32_16x16x32_bf16 v[60:63], a[40:43], a[96:99], v[60:63]\n"
"v_mfma_f32_16x16x32_bf16 v[64:67], a[40:43], a[104:107], v[64:67]\n"
"ds_read_b128 a[160:163], v7 offset:24576\n"
"v_mfma_f32_16x16x32_bf16 v[64:67], a[44:47], a[108:111], v[64:67]\n"
"v_mfma_f32_16x16x32_bf16 v[36:39], a[36:39], a[108:111], v[36:39]\n"
"ds_read_b128 a[164:167], v8 offset:24576\n"
"v_mfma_f32_16x16x32_bf16 v[36:39], a[32:35], a[104:107], v[36:39]\n"
"v_mfma_f32_16x16x32_bf16 v[40:43], a[32:35], a[112:115], v[40:43]\n"
"ds_read_b128 a[168:171], v7 offset:26624\n"
"v_mfma_f32_16x16x32_bf16 v[40:43], a[36:39], a[116:119], v[40:43]\n"
"ds_read_b128 a[172:175], v8 offset:26624\n"
"v_mfma_f32_16x16x32_bf16 v[68:71], a[44:47], a[116:119], v[68:71]\n"
"s_add_u32 s70, s78, 1\n"
"v_mfma_f32_16x16x32_bf16 v[68:71], a[40:43], a[112:115], v[68:71]\n"
"ds_read_b128 a[176:179], v7 offset:28672\n"
"v_mfma_f32_16x16x32_bf16 v[72:75], a[40:43], a[120:123], v[72:75]\n"
"ds_read_b128 a[180:183], v8 offset:28672\n"
"v_mfma_f32_16x16x32_bf16 v[72:75], a[44:47], a[124:127], v[72:75]\n"
"v_mfma_f32_16x16x32_bf16 v[44:47], a[36:39], a[124:127], v[44:47]\n"
"ds_read_b128 a[184:187], v7 offset:30720\n"
"v_mfma_f32_16x16x32_bf16 v[44:47], a[32:35], a[120:123], v[44:47]\n"
"v_mfma_f32_16x16x32_bf16 v[48:51], a[32:35], a[128:131], v[48:51]\n"
"ds_read_b128 a[188:191], v8 offset:30720\n"
"v_mfma_f32_16x16x32_bf16 v[48:51], a[36:39], a[132:135], v[48:51]\n"
"v_mfma_f32_16x16x32_bf16 v[76:79], a[44:47], a[132:135], v[76:79]\n"
"s_waitcnt vmcnt(3)\n"
"ds_read_b64_tr_b16 a[64:65], v9 offset:0\n"
"v_mfma_f32_16x16x32_bf16 v[76:79], a[40:43], a[128:131], v[76:79]\n"
"ds_read_b64_tr_b16 a[66:67], v9 offset:512\n"
"v_mfma_f32_16x16x32_bf16 v[80:83], a[40:43], a[136:139], v[80:83]\n"
"L_st3238:\n"
"s_cbranch_execz L_retA\n"
"ds_read_b64_tr_b16 a[68:69], v9 offset:4096\n"
"v_mfma_f32_16x16x32_bf16 v[80:83], a[44:47], a[140:143], v[80:83]\n"
"ds_read_b64_tr_b16 a[70:71], v9 offset:4608\n"
"v_mfma_f32_16x16x32_bf16 v[52:55], a[36:39], a[140:143], v[52:55]\n"
"ds_read_b64_tr_b16 a[72:73], v9 offset:8\n"
"v_subrev_u32 v24, s70, v24\n"
"v_mfma_f32_16x16x32_bf16 v[52:55], a[32:35], a[136:139], v[52:55]\n"
"s_waitcnt lgkmcnt(13)\n"
"ds_read_b64_tr_b16 a[74:75], v9 offset:520\n"
"v_xor_b32 v25, s79, v25\n"
"v_mfma_f32_16x16x32_bf16 v[56:59], a[32:35], a[144:147], v[56:59]\n"
"ds_read_b64_tr_b16 a[76:77], v9 offset:4104\n"
"v_subrev_u32 v26, s70, v26\n"
"v_mfma_f32_16x16x32_bf16 v[56:59], a[36:39], a[148:151], v[56:59]\n"
"ds_read_b64_tr_b16 a[78:79], v9 offset:4616\n"
"v_xor_b32 v27, s79, v27\n"
"v_mfma_f32_16x16x32_bf16 v[84:87], a[44:47], a[148:151], v[84:87]\n"
"ds_read_b64_tr_b16 a[80:81], v15 offset:0\n"
"v_or_b32 v24, v24, v26\n"
"v_mfma_f32_16x16x32_bf16 v[84:87], a[40:43], a[144:147], v[84:87]\n"
"ds_read_b64_tr_b16 a[82:83], v15 offset:512\n"
"v_or_b32 v25, v25, v27\n"
"v_mfma_f32_16x16x32_bf16 a[4:7], a[40:43], a[152:155], a[4:7]\n"
"ds_read_b64_tr_b16 a[84:85], v15 offset:4096\n"
"v_or_b32 v24, v24, v25\n"
"v_mfma_f32_16x16x32_bf16 a[4:7], a[44:47], a[156:159], a[4:7]\n"
"ds_read_b64_tr_b16 a[86:87], v15 offset:4608\n"
"v_cmp_lt_u32 vcc, 15, v24\n"
"v_mfma_f32_16x16x32_bf16 a[0:3], a[36:39], a[156:159], a[0:3]\n"
"ds_read_b64_tr_b16 a[88:89], v15 offset:8\n"
"s_cbranch_vccnz L_slow3\n"
"L_back3:\n"
"v_mfma_f32_16x16x32_bf16 a[0:3], a[32:35], a[152:155], a[0:3]\n"
"ds_read_b64_tr_b16 a[90:91], v15 offset:520\n"
"v_mfma_f32_16x16x32_bf16 v[88:91], a[48:51], a[96:99], v[88:91]\n"
"ds_read_b64_tr_b16 a[92:93], v15 offset:4104\n"
"v_mfma_f32_16x16x32_bf16 v[88:91], a[52:55], a[100:103], v[88:91]\n"
"global_load_dwordx4 a[32:35], v0, s[34:35] offset:-4096 sc1\n"
"s_barrier\n"
"v_mfma_f32_16x16x32_bf16 v[116:119], a[60:63], a[100:103], v[116:119]\n"
"ds_read_b64_tr_b16 a[94:95], v15 offset:4616\n"
"v_mfma_f32_16x16x32_bf16 v[116:119], a[56:59], a[96:99], v[116:119]\n"
"ds_write_b64 v2, a[192:193] offset:16384\n"
"v_mfma_f32_16x16x32_bf16 v[120:123], a[56:59], a[104:107], v[120:123]\n"
"v_mfma_f32_16x16x32_bf16 v[120:123], a[60:63], a[108:111], v[120:123]\n"
"ds_write_b64 v2, a[194:195] offset:16392\n"
"v_mfma_f32_16x16x32_bf16 v[92:95], a[52:55], a[108:111], v[92:95]\n"
"v_mfma_f32_16x16x32_bf16 v[92:95], a[48:51], a[104:107], v[92:95]\n"
"ds_write_b64 v2, a[196:197] offset:17408\n"
"v_mfma_f32_16x16x32_bf16 v[96:99], a[48:51], a[112:115], v[96:99]\n"
"s_waitcnt lgkmcnt(3)\n"
"v_mfma_f32_16x16x32_bf16 v[96:99], a[52:55], a[116:119], v[96:99]\n"
"global_load_dwordx4 a[36:39], v0, s[34:35] offset:-3072 sc1\n"
"v_mfma_f32_16x16x32_bf16 v[124:127], a[60:63], a[116:119], v[124:127]\n"
"ds_write_b64 v2, a[198:199] offset:17416\n"
"v_mfma_f32_16x16x32_bf16 v[124:127], a[56:59], a[112:115], v[124:127]\n"
"ds_write_b64 v2, a[200:201] offset:18432\n"
"v_mfma_f32_16x16x32_bf16 v[128:131], a[56:59], a[120:123], v[128:131]\n"
"v_mfma_f32_16x16x32_bf16 v[128:131], a[60:63], a[124:127], v[128:131]\n"
"ds_write_b64 v2, a[202:203] offset:18440\n"
"v_mfma_f32_16x16x32_bf16 v[100:103], a[52:55], a[124:127], v[100:103]\n"
"v_mfma_f32_16x16x32_bf16 v[100:103], a[48:51], a[120:123], v[100:103]\n"
"ds_write_b64 v2, a[204:205] offset:19456\n"
"v_mfma_f32_16x16x32_bf16 v[104:107], a[48:51], a[128:131], v[104:107]\n"
"ds_write_b64 v2, a[206:207] offset:19464\n"
"v_mfma_f32_16x16x32_bf16 v[104:107], a[52:55], a[132:135], v[104:107]\n"
"global_load_dwordx4 a[40:43], v0, s[34:35] offset:-2048 sc1\n"
"v_mfma_f32_16x16x32_bf16 v[132:135], a[60:63], a[132:135], v[132:135]\n"
"ds_write_b64 v2, a[208:209] offset:20480\n"
"v_mfma_f32_16x16x32_bf16 v[132:135], a[56:59], a[128:131], v[132:135]\n"
"v_mfma_f32_16x16x32_bf16 v[136:139], a[56:59], a[136:139], v[136:139]\n"
"ds_write_b64 v2, a[210:211] offset:20488\n"
"v_mfma_f32_16x16x32_bf16 v[136:139], a[60:63], a[140:143], v[136:139]\n"
"v_mfma_f32_16x16x32_bf16 v[108:111], a[52:55], a[140:143], v[108:111]\n"
"ds_write_b64 v2, a[212:213] offset:21504\n"
"v_mfma_f32_16x16x32_bf16 v[108:111], a[48:51], a[136:139], v[108:111]\n"
"v_mfma_f32_16x16x32_bf16 v[112:115], a[48:51], a[144:147], v[112:115]\n"
"ds_write_b64 v2, a[214:215] offset:21512\n"
"v_mfma_f32_16x16x32_bf16 v[112:115], a[52:55], a[148:151], v[112:115]\n"
"global_load_dwordx4 a[44:47], v0, s[34:35] offset:-1024 sc1\n"
"v_mfma_f32_16x16x32_bf16 v[140:143], a[60:63], a[148:151], v[140:143]\n"
"ds_write_b64 v2, a[216:217] offset:22528\n"
"v_mfma_f32_16x16x32_bf16 v[140:143], a[56:59], a[144:147], v[140:143]\n"
"v_mfma_f32_16x16x32_bf16 a[12:15], a[56:59], a[152:155], a[12:15]\n"
"ds_write_b64 v2, a[218:219] offset:22536\n"
"v_mfma_f32_16x16x32_bf16 a[12:15], a[60:63], a[156:159], a[12:15]\n"
"v_mfma_f32_16x16x32_bf16 a[8:11], a[52:55], a[156:159], a[8:11]\n"
"ds_write_b64 v2, a[220:221] offset:23552\n"
"v_mfma_f32_16x16x32_bf16 a[8:11], a[48:51], a[152:155], a[8:11]\n"
"v_mfma_f32_16x16x32_bf16 v[144:147], a[64:67], a[96:99], v[144:147]\n"
"ds_write_b64 v2, a[222:223] offset:23560\n"
"v_mfma_f32_16x16x32_bf16 v[144:147], a[68:71], a[100:103], v[144:147]\n"
"global_load_dwordx4 a[48:51], v0, s[34:35] offset:0 sc1\n"
"v_mfma_f32_16x16x32_bf16 v[172:175], a[76:79], a[100:103], v[172:175]\n"
"ds_write_b64 v2, a[224:225] offset:24576\n"
"v_mfma_f32_16x16x32_bf16 v[172:175], a[72:75], a[96:99], v[172:175]\n"
"ds_write_b64 v2, a[226:227] offset:24584\n"
"v_mfma_f32_16x16x32_bf16 v[176:179], a[72:75], a[104:107], v[176:179]\n"
"v_mfma_f32_16x16x32_bf16 v[176:179], a[76:79], a[108:111], v[176:179]\n"
"ds_write_b64 v2, a[228:229] offset:25600\n"
"v_mfma_f32_16x16x32_bf16 v[148:151], a[68:71], a[108:111], v[148:151]\n"
"v_mfma_f32_16x16x32_bf16 v[148:151], a[64:67], a[104:107], v[148:151]\n"
"ds_write_b64 v2, a[230:231] offset:25608\n"
"v_mfma_f32_16x16x32_bf16 v[152:155], a[64:67], a[112:115], v[152:155]\n"
"ds_write_b64 v2, a[232:233] offset:26624\n"
"v_mfma_f32_16x16x32_bf16 v[152:155], a[68:71], a[116:119], v[152:155]\n"
"global_load_dwordx4 a[52:55], v0, s[34:35] offset:1024 sc1\n"
"v_mfma_f32_16x16x32_bf16 v[180:183], a[76:79], a[116:119], v[180:183]\n"
"v_mfma_f32_16x16x32_bf16 v[180:183], a[72:75], a[112:115], v[180:183]\n"
"ds_write_b64 v2, a[234:235] offset:26632\n"
"v_mfma_f32_16x16x32_bf16 v[184:187], a[72:75], a[120:123], v[184:187]\n"
"v_mfma_f32_16x16x32_bf16 v[184:187], a[76:79], a[124:127], v[184:187]\n"
"ds_write_b64 v2, a[236:237] offset:27648\n"
"v_mfma_f32_16x16x32_bf16 v[156:159], a[68:71], a[124:127], v[156:159]\n"
"v_mfma_f32_16x16x32_bf16 v[156:159], a[64:67], a[120:123], v[156:159]\n"
"ds_write_b64 v2, a[238:239] offset:27656\n"
"v_mfma_f32_16x16x32_bf16 v[160:163], a[64:67], a[128:131], v[160:163]\n"
"ds_write_b64 v2, a[240:241] offset:28672\n"
"v_mfma_f32_16x16x32_bf16 v[160:163], a[68:71], a[132:135], v[160:163]\n"
"global_load_dwordx4 a[56:59], v0, s[34:35] offset:2048 sc1\n"
"v_mfma_f32_16x16x32_bf16 v[188:191], a[76:79], a[132:135], v[188:191]\n"
"ds_write_b64 v2, a[242:243] offset:28680\n"
"v_mfma_f32_16x16x32_bf16 v[188:191], a[72:75], a[128:131], v[188:191]\n"
"v_mfma_f32_16x16x32_bf16 v[192:195], a[72:75], a[136:139], v[192:195]\n"
"ds_write_b64 v2, a[244:245] offset:29696\n"
"v_mfma_f32_16x16x32_bf16 v[192:195], a[76:79], a[140:143], v[192:195]\n"
"v_mfma_f32_16x16x32_bf16 v[164:167], a[68:71], a[140:143], v[164:167]\n"
"ds_write_b64 v2, a[246:247] offset:29704\n"
"v_mfma_f32_16x16x32_bf16 v[164:167], a[64:67], a[136:139], v[164:167]\n"
"v_mfma_f32_16x16x32_bf16 v[168:171], a[64:67], a[144:147], v[168:171]\n"
"ds_write_b64 v2, a[248:249] offset:30720\n"
"v_mfma_f32_16x16x32_bf16 v[168:171], a[68:71], a[148:151], v[168:171]\n"
"global_load_dwordx4 a[60:63], v0, s[34:35] offset:3072 sc1\n"
"v_mfma_f32_16x16x32_bf16 v[196:199], a[76:79], a[148:151], v[196:199]\n"
"ds_write_b64 v2, a[250:251] offset:30728\n"
"v_mfma_f32_16x16x32_bf16 v[196:199], a[72:75], a[144:147], v[196:199]\n"
"v_mfma_f32_16x16x32_bf16 a[20:23], a[72:75], a[152:155], a[20:23]\n"
"s_add_u32 s34, s34, 8192\n"
"ds_write_b64 v2, a[252:253] offset:31744\n"
"v_mfma_f32_16x16x32_bf16 a[20:23], a[76:79], a[156:159], a[20:23]\n"
"v_mfma_f32_16x16x32_bf16 a[16:19], a[68:71], a[156:159], a[16:19]\n"
"ds_write_b64 v2, a[254:255] offset:31752\n"
"v_mfma_f32_16x16x32_bf16 a[16:19], a[64:67], a[152:155], a[16:19]\n"
"v_mfma_f32_16x16x32_bf16 v[200:203], a[80:83], a[96:99], v[200:203]\n"
"v_lshrrev_b32 v16, 4, v0\n"
"v_mfma_f32_16x16x32_bf16 v[200:203], a[84:87], a[100:103], v[200:203]\n"
"global_load_dwordx4 a[64:67], v0, s[34:35] offset:-4096 sc1\n"
"v_mfma_f32_16x16x32_bf16 v[228:231], a[92:95], a[100:103], v[228:231]\n"
"ds_read_b128 a[100:103], v2 offset:17408\n"
"v_mfma_f32_16x16x32_bf16 v[228:231], a[88:91], a[96:99], v[228:231]\n"
"ds_read_b128 a[96:99], v2 offset:16384\n"
"v_mfma_f32_16x16x32_bf16 v[232:235], a[88:91], a[104:107], v[232:235]\n"
"v_and_b32 v16, 0x30, v16\n"
"v_mfma_f32_16x16x32_bf16 v[232:235], a[92:95], a[108:111], v[232:235]\n"
"buffer_load_dwordx2 v[18:19], v16, s[44:47], s61 offen offset:128\n"
"v_mfma_f32_16x16x32_bf16 v[204:207], a[84:87], a[108:111], v[204:207]\n"
"ds_read_b128 a[108:111], v2 offset:19456\n"
"v_mfma_f32_16x16x32_bf16 v[204:207], a[80:83], a[104:107], v[204:207]\n"
"ds_read_b128 a[104:107], v2 offset:18432\n"
"v_mfma_f32_16x16x32_bf16 v[208:211], a[80:83], a[112:115], v[208:211]\n"
"v_lshrrev_b32 v20, 4, v0\n"
"v_mfma_f32_16x16x32_bf16 v[208:211], a[84:87], a[116:119], v[208:211]\n"
"global_load_dwordx4 a[68:71], v0, s[34:35] offset:-3072 sc1\n"
"v_mfma_f32_16x16x32_bf16 v[236:239], a[92:95], a[116:119], v[236:239]\n"
"ds_read_b128 a[116:119], v2 offset:21504\n"
"v_mfma_f32_16x16x32_bf16 v[236:239], a[88:91], a[112:115], v[236:239]\n"
"ds_read_b128 a[112:115], v2 offset:20480\n"
"v_mfma_f32_16x16x32_bf16 v[240:243], a[88:91], a[120:123], v[240:243]\n"
"v_and_b32 v20, 0x30, v20\n"
"v_mfma_f32_16x16x32_bf16 v[240:243], a[92:95], a[124:127], v[240:243]\n"
"buffer_load_dwordx2 v[22:23], v20, s[44:47], s61 offen offset:136\n"
"v_mfma_f32_16x16x32_bf16 v[212:215], a[84:87], a[124:127], v[212:215]\n"
"ds_read_b128 a[124:127], v2 offset:23552\n"
"v_mfma_f32_16x16x32_bf16 v[212:215], a[80:83], a[120:123], v[212:215]\n"
"ds_read_b128 a[120:123], v2 offset:22528\n"
"v_mfma_f32_16x16x32_bf16 v[216:219], a[80:83], a[128:131], v[216:219]\n"
"v_lshrrev_b32 v24, 4, v0\n"
"v_mfma_f32_16x16x32_bf16 v[216:219], a[84:87], a[132:135], v[216:219]\n"
"global_load_dwordx4 a[72:75], v0, s[34:35] offset:-2048 sc1\n"
"v_mfma_f32_16x16x32_bf16 v[244:247], a[92:95], a[132:135], v[244:247]\n"
"ds_read_b128 a[132:135], v2 offset:25600\n"
"v_and_b32 v24, 0x30, v24\n"
"v_mfma_f32_16x16x32_bf16 v[244:247], a[88:91], a[128:131], v[244:247]\n"
"ds_read_b128 a[128:131], v2 offset:24576\n"
"v_mfma_f32_16x16x32_bf16 v[248:251], a[88:91], a[136:139], v[248:251]\n"
"buffer_load_dwordx2 v[26:27], v24, s[44:47], s61 offen offset:192\n"
"v_mfma_f32_16x16x32_bf16 v[248:251], a[92:95], a[140:143], v[248:251]\n"
"v_lshrrev_b32 v28, 4, v0\n"
"v_mfma_f32_16x16x32_bf16 v[220:223], a[84:87], a[140:143], v[220:223]\n"
"ds_read_b128 a[140:143], v2 offset:27648\n"
"v_mfma_f32_16x16x32_bf16 v[220:223], a[80:83], a[136:139], v[220:223]\n"
"ds_read_b128 a[136:139], v2 offset:26624\n"
"v_mfma_f32_16x16x32_bf16 v[224:227], a[80:83], a[144:147], v[224:227]\n"
"v_and_b32 v28, 0x30, v28\n"
"v_mfma_f32_16x16x32_bf16 v[224:227], a[84:87], a[148:151], v[224:227]\n"
"global_load_dwordx4 a[76:79], v0, s[34:35] offset:-1024 sc1\n"
"v_mfma_f32_16x16x32_bf16 v[252:255], a[92:95], a[148:151], v[252:255]\n"
"ds_read_b128 a[148:151], v2 offset:29696\n"
"v_mfma_f32_16x16x32_bf16 v[252:255], a[88:91], a[144:147], v[252:255]\n"
"ds_read_b128 a[144:147], v2 offset:28672\n"
"v_mfma_f32_16x16x32_bf16 a[28:31], a[88:91], a[152:155], a[28:31]\n"
"buffer_load_dwordx2 v[30:31], v28, s[44:47], s61 offen offset:200\n"
"v_mfma_f32_16x16x32_bf16 a[28:31], a[92:95], a[156:159], a[28:31]\n"
"v_mfma_f32_16x16x32_bf16 a[24:27], a[84:87], a[156:159], a[24:27]\n"
"ds_read_b128 a[156:159], v2 offset:31744\n"
"v_mfma_f32_16x16x32_bf16 a[24:27], a[80:83], a[152:155], a[24:27]\n"
"s_waitcnt vmcnt(12) lgkmcnt(11)\n"
"ds_read_b128 a[152:155], v2 offset:30720\n"
"v_mfma_f32_16x16x32_bf16 v[32:35], a[32:35], a[96:99], v[32:35]\n"
"v_mfma_f32_16x16x32_bf16 v[32:35], a[36:39], a[100:103], v[32:35]\n"
"global_load_dwordx4 a[80:83], v0, s[34:35] offset:0 sc1\n"
"v_mfma_f32_16x16x32_bf16 v[60:63], a[44:47], a[100:103], v[60:63]\n"
"s_barrier\n"
"v_mfma_f32_16x16x32_bf16 v[60:63], a[40:43], a[96:99], v[60:63]\n"
"v_mfma_f32_16x16x32_bf16 v[64:67], a[40:43], a[104:107], v[64:67]\n"
"v_mfma_f32_16x16x32_bf16 v[64:67], a[44:47], a[108:111], v[64:67]\n"
"v_mfma_f32_16x16x32_bf16 v[36:39], a[36:39], a[108:111], v[36:39]\n"
"v_mfma_f32_16x16x32_bf16 v[36:39], a[32:35], a[104:107], v[36:39]\n"
"s_waitcnt lgkmcnt(8)\n"
"v_mfma_f32_16x16x32_bf16 v[40:43], a[32:35], a[112:115], v[40:43]\n"
"v_mfma_f32_16x16x32_bf16 v[40:43], a[36:39], a[116:119], v[40:43]\n"
"global_load_dwordx4 a[84:87], v0, s[34:35] offset:1024 sc1\n"
"v_mfma_f32_16x16x32_bf16 v[68:71], a[44:47], a[116:119], v[68:71]\n"
"v_mfma_f32_16x16x32_bf16 v[68:71], a[40:43], a[112:115], v[68:71]\n"
"v_mfma_f32_16x16x32_bf16 v[72:75], a[40:43], a[120:123], v[72:75]\n"
"v_mfma_f32_16x16x32_bf16 v[72:75], a[44:47], a[124:127], v[72:75]\n"
"s_add_u32 s61, s61, 2048\n"
"v_mfma_f32_16x16x32_bf16 v[44:47], a[36:39], a[124:127], v[44:47]\n"
"v_mfma_f32_16x16x32_bf16 v[44:47], a[32:35], a[120:123], v[44:47]\n"
"s_waitcnt lgkmcnt(4)\n"
"v_mfma_f32_16x16x32_bf16 v[48:51], a[32:35], a[128:131], v[48:51]\n"
"s_waitcnt vmcnt(8)\n"
"v_lshlrev_b32 v16, 16, v18\n"
"v_mfma_f32_16x16x32_bf16 v[48:51], a[36:39], a[132:135], v[48:51]\n"
"global_load_dwordx4 a[88:91], v0, s[34:35] offset:2048 sc1\n"
"v_and_b32 v17, s63, v18\n"
"v_mfma_f32_16x16x32_bf16 v[76:79], a[44:47], a[132:135], v[76:79]\n"
"v_lshlrev_b32 v18, 16, v19\n"
"v_and_b32 v19, s63, v19\n"
"v_mfma_f32_16x16x32_bf16 v[76:79], a[40:43], a[128:131], v[76:79]\n"
"v_add_f32 v144, v144, v16\n"
"v_mfma_f32_16x16x32_bf16 v[80:83], a[40:43], a[136:139], v[80:83]\n"
"v_add_f32 v145, v145, v17\n"
"v_add_f32 v146, v146, v18\n"
"v_mfma_f32_16x16x32_bf16 v[80:83], a[44:47], a[140:143], v[80:83]\n"
"v_add_f32 v147, v147, v19\n"
"v_mfma_f32_16x16x32_bf16 v[52:55], a[36:39], a[140:143], v[52:55]\n"
"v_add_f32 v148, v148, v16\n"
"v_mfma_f32_16x16x32_bf16 v[52:55], a[32:35], a[136:139], v[52:55]\n"
"s_mov_b32 s99, 0\n"
"v_add_f32 v149, v149, v17\n"
"s_waitcnt lgkmcnt(0)\n"
"v_mfma_f32_16x16x32_bf16 v[56:59], a[32:35], a[144:147], v[56:59]\n"
"s_mov_b32 s98, 0\n"
"v_add_f32 v150, v150, v18\n"
"v_add_f32 v151, v151, v19\n"
"v_mfma_f32_16x16x32_bf16 v[56:59], a[36:39], a[148:151], v[56:59]\n"
"global_load_dwordx4 a[92:95], v0, s[34:35] offset:3072 sc1\n"
"s_memtime s[76:77]\n"
"s_load_dword s98, s[96:97], 8448 glc\n"
"v_mfma_f32_16x16x32_bf16 v[84:87], a[44:47], a[148:151], v[84:87]\n"
"v_add_f32 v152, v152, v16\n"
"v_add_f32 v153, v153, v17\n"
"v_mfma_f32_16x16x32_bf16 v[84:87], a[40:43], a[144:147], v[84:87]\n"
"v_add_f32 v154, v154, v18\n"
"v_add_f32 v155, v155, v19\n"
"v_mfma_f32_16x16x32_bf16 a[4:7], a[40:43], a[152:155], a[4:7]\n"
"s_add_u32 s34, s34, 8192\n"
"v_add_f32 v156, v156, v16\n"
"v_mfma_f32_16x16x32_bf16 a[4:7], a[44:47], a[156:159], a[4:7]\n"
"v_add_f32 v157, v157, v17\n"
"v_mfma_f32_16x16x32_bf16 a[0:3], a[36:39], a[156:159], a[0:3]\n"
"v_add_f32 v158, v158, v18\n"
"v_add_f32 v159, v159, v19\n"
"v_mfma_f32_16x16x32_bf16 a[0:3], a[32:35], a[152:155], a[0:3]\n"
"v_add_f32 v160, v160, v16\n"
"v_mfma_f32_16x16x32_bf16 v[88:91], a[48:51], a[96:99], v[88:91]\n"
"v_add_f32 v161, v161, v17\n"
"v_add_f32 v162, v162, v18\n"
"v_mfma_f32_16x16x32_bf16 v[88:91], a[52:55], a[100:103], v[88:91]\n"
"global_load_dwordx4 a[32:35], v0, s[34:35] offset:-4096 sc1\n"
"v_add_f32 v163, v163, v19\n"
"v_mfma_f32_16x16x32_bf16 v[116:119], a[60:63], a[100:103], v[116:119]\n"
"s_mov_b32 s29, s76\n"
"ds_read_b128 a[100:103], v3 offset:17408\n"
"v_add_f32 v164, v164, v16\n"
"v_mfma_f32_16x16x32_bf16 v[116:119], a[56:59], a[96:99], v[116:119]\n"
"ds_read_b128 a[96:99], v3 offset:16384\n"
"v_add_f32 v165, v165, v17\n"
"v_mfma_f32_16x16x32_bf16 v[120:123], a[56:59], a[104:107], v[120:123]\n"
"s_sub_u32 s99, s99, s98\n"
"v_add_f32 v166, v166, v18\n"
"v_add_f32 v167, v167, v19\n"
"v_mfma_f32_16x16x32_bf16 v[120:123], a[60:63], a[108:111], v[120:123]\n"
"s_sub_u32 s99, s99, s98\n"
"v_add_f32 v168, v168, v16\n"
"v_add_f32 v169, v169, v17\n"
"v_mfma_f32_16x16x32_bf16 v[92:95], a[52:55], a[108:111], v[92:95]\n"
"s_sub_u32 s99, s99, s98\n"
"ds_read_b128 a[108:111], v3 offset:19456\n"
"v_add_f32 v170, v170, v18\n"
"v_mfma_f32_16x16x32_bf16 v[92:95], a[48:51], a[104:107], v[92:95]\n"
"s_sub_u32 s99, s99, s98\n"
"ds_read_b128 a[104:107], v3 offset:18432\n"
"v_add_f32 v171, v171, v19\n"
"v_mfma_f32_16x16x32_bf16 v[96:99], a[48:51], a[112:115], v[96:99]\n"
"s_sub_u32 s99, s99, s98\n"
"v_accvgpr_read_b32 v12, a16\n"
"v_accvgpr_read_b32 v13, a17\n"
"v_mfma_f32_16x16x32_bf16 v[96:99], a[52:55], a[116:119], v[96:99]\n"
"global_load_dwordx4 a[36:39], v0, s[34:35] offset:-3072 sc1\n"
"s_sub_u32 s99, s99, s98\n"
"v_mfma_f32_16x16x32_bf16 v[124:127], a[60:63], a[116:119], v[124:127]\n"
"s_sub_u32 s99, s99, s98\n"
"ds_read_b128 a[116:119], v3 offset:21504\n"
"v_accvgpr_read_b32 v14, a18\n"
"v_mfma_f32_16x16x32_bf16 v[124:127], a[56:59], a[112:115], v[124:127]\n"
"s_sub_u32 s99, s99, s98\n"
"ds_read_b128 a[112:115], v3 offset:20480\n"
"v_accvgpr_read_b32 v15, a19\n"
"v_mfma_f32_16x16x32_bf16 v[128:131], a[56:59], a[120:123], v[128:131]\n"
"s_sub_u32 s99, s99, s98\n"
"v_add_f32 v12, v12, v16\n"
"v_add_f32 v13, v13, v17\n"
"v_mfma_f32_16x16x32_bf16 v[128:131], a[60:63], a[124:127], v[128:131]\n"
"s_sub_u32 s99, s99, s98\n"
"v_add_f32 v14, v14, v18\n"
"v_add_f32 v15, v15, v19\n"
"v_mfma_f32_16x16x32_bf16 v[100:103], a[52:55], a[124:127], v[100:103]\n"
"s_sub_u32 s99, s99, s98\n"
"ds_read_b128 a[124:127], v3 offset:23552\n"
"v_accvgpr_write_b32 a16, v12\n"
"v_mfma_f32_16x16x32_bf16 v[100:103], a[48:51], a[120:123], v[100:103]\n"
"s_sub_u32 s99, s99, s98\n"
"ds_read_b128 a[120:123], v3 offset:22528\n"
"v_accvgpr_write_b32 a17, v13\n"
"v_mfma_f32_16x16x32_bf16 v[104:107], a[48:51], a[128:131], v[104:107]\n"
"s_sub_u32 s99, s99, s98\n"
"v_accvgpr_write_b32 a18, v14\n"
"v_accvgpr_write_b32 a19, v15\n"
"v_mfma_f32_16x16x32_bf16 v[104:107], a[52:55], a[132:135], v[104:107]\n"
"s_sub_u32 s99, s99, s98\n"
"s_waitcnt vmcnt(10)\n"
"global_load_dwordx4 a[40:43], v0, s[34:35] offset:-2048 sc1\n"
"v_mfma_f32_16x16x32_bf16 v[132:135], a[60:63], a[132:135], v[132:135]\n"
"s_sub_u32 s99, s99, s98\n"
"ds_read_b128 a[132:135], v3 offset:25600\n"
"v_lshlrev_b32 v20, 16, v22\n"
"v_mfma_f32_16x16x32_bf16 v[132:135], a[56:59], a[128:131], v[132:135]\n"
"s_sub_u32 s99, s99, s98\n"
"ds_read_b128 a[128:131], v3 offset:24576\n"
"v_and_b32 v21, s63, v22\n"
"v_mfma_f32_16x16x32_bf16 v[136:139], a[56:59], a[136:139], v[136:139]\n"
"s_sub_u32 s99, s99, s98\n"
"v_lshlrev_b32 v22, 16, v23\n"
"v_and_b32 v23, s63, v23\n"
"v_mfma_f32_16x16x32_bf16 v[136:139], a[60:63], a[140:143], v[136:139]\n"
"s_sub_u32 s99, s99, s98\n"
"v_add_f32 v172, v172, v20\n"
"v_add_f32 v173, v173, v21\n"
"v_mfma_f32_16x16x32_bf16 v[108:111], a[52:55], a[140:143], v[108:111]\n"
"s_sub_u32 s99, s99, s98\n"
"ds_read_b128 a[140:143], v3 offset:27648\n"
"v_add_f32 v174, v174, v22\n"
"v_mfma_f32_16x16x32_bf16 v[108:111], a[48:51], a[136:139], v[108:111]\n"
"s_sub_u32 s99, s99, s98\n"
"ds_read_b128 a[136:139], v3 offset:26624\n"
"v_add_f32 v175, v175, v23\n"
"v_mfma_f32_16x16x32_bf16 v[112:115], a[48:51], a[144:147], v[112:115]\n"
"s_sub_u32 s99, s99, s98\n"
"v_add_f32 v176, v176, v20\n"
"v_add_f32 v177, v177, v21\n"
"v_mfma_f32_16x16x32_bf16 v[112:115], a[52:55], a[148:151], v[112:115]\n"
"global_load_dwordx4 a[44:47], v0, s[34:35] offset:-1024 sc1\n"
"s_sub_u32 s99, s99, s98\n"
"v_mfma_f32_16x16x32_bf16 v[140:143], a[60:63], a[148:151], v[140:143]\n"
"s_sub_u32 s99, s99, s98\n"
"ds_read_b128 a[148:151], v3 offset:29696\n"
"v_add_f32 v178, v178, v22\n"
"v_mfma_f32_16x16x32_bf16 v[140:143], a[56:59], a[144:147], v[140:143]\n"
"s_sub_u32 s99, s99, s98\n"
"ds_read_b128 a[144:147], v3 offset:28672\n"
"v_add_f32 v179, v179, v23\n"
"v_mfma_f32_16x16x32_bf16 a[12:15], a[56:59], a[152:155], a[12:15]\n"
"s_memtime s[76:77]\n"
"v_add_f32 v180, v180, v20\n"
"v_add_f32 v181, v181, v21\n"
"v_mfma_f32_16x16x32_bf16 a[12:15], a[60:63], a[156:159], a[12:15]\n"
"v_add_f32 v182, v182, v22\n"
"v_add_f32 v183, v183, v23\n"
"v_mfma_f32_16x16x32_bf16 a[8:11], a[52:55], a[156:159], a[8:11]\n"
"ds_read_b128 a[156:159], v3 offset:31744\n"
"v_add_f32 v184, v184, v20\n"
"v_mfma_f32_16x16x32_bf16 a[8:11], a[48:51], a[152:155], a[8:11]\n"
"s_waitcnt vmcnt(9) lgkmcnt(11)\n"
"ds_read_b128 a[152:155], v3 offset:30720\n"
"v_add_f32 v185, v185, v21\n"
"v_mfma_f32_16x16x32_bf16 v[32:35], a[64:67], a[96:99], v[32:35]\n"
"v_add_f32 v186, v186, v22\n"
"v_add_f32 v187, v187, v23\n"
"v_mfma_f32_16x16x32_bf16 v[32:35], a[68:71], a[100:103], v[32:35]\n"
"global_load_dwordx4 a[48:51], v0, s[34:35] offset:0 sc1\n"
"v_add_f32 v188, v188, v20\n"
"v_mfma_f32_16x16x32_bf16 v[60:63], a[76:79], a[100:103], v[60:63]\n"
"v_add_f32 v189, v189, v21\n"
"v_mfma_f32_16x16x32_bf16 v[60:63], a[72:75], a[96:99], v[60:63]\n"
"v_add_f32 v190, v190, v22\n"
"v_add_f32 v191, v191, v23\n"
"v_mfma_f32_16x16x32_bf16 v[64:67], a[72:75], a[104:107], v[64:67]\n"
"v_add_f32 v192, v192, v20\n"
"v_mfma_f32_16x16x32_bf16 v[64:67], a[76:79], a[108:111], v[64:67]\n"
"v_add_f32 v193, v193, v21\n"
"v_mfma_f32_16x16x32_bf16 v[36:39], a[68:71], a[108:111], v[36:39]\n"
"v_add_f32 v194, v194, v22\n"
"v_add_f32 v195, v195, v23\n"
"v_mfma_f32_16x16x32_bf16 v[36:39], a[64:67], a[104:107], v[36:39]\n"
"s_waitcnt lgkmcnt(8)\n"
"v_add_f32 v196, v196, v20\n"
"v_mfma_f32_16x16x32_bf16 v[40:43], a[64:67], a[112:115], v[40:43]\n"
"v_add_f32 v197, v197, v21\n"
"v_mfma_f32_16x16x32_bf16 v[40:43], a[68:71], a[116:119], v[40:43]\n"
"global_load_dwordx4 a[52:55], v0, s[34:35] offset:1024 sc1\n"
"v_add_f32 v198, v198, v22\n"
"v_mfma_f32_16x16x32_bf16 v[68:71], a[76:79], a[116:119], v[68:71]\n"
"v_add_f32 v199, v199, v23\n"
"v_accvgpr_read_b32 v12, a20\n"
"v_mfma_f32_16x16x32_bf16 v[68:71], a[72:75], a[112:115], v[68:71]\n"
"v_accvgpr_read_b32 v13, a21\n"
"v_mfma_f32_16x16x32_bf16 v[72:75], a[72:75], a[120:123], v[72:75]\n"
"v_accvgpr_read_b32 v14, a22\n"
"v_accvgpr_read_b32 v15, a23\n"
"v_mfma_f32_16x16x32_bf16 v[72:75], a[76:79], a[124:127], v[72:75]\n"
"v_add_f32 v12, v12, v20\n"
"v_mfma_f32_16x16x32_bf16 v[44:47], a[68:71], a[124:127], v[44:47]\n"
"v_add_f32 v13, v13, v21\n"
"v_mfma_f32_16x16x32_bf16 v[44:47], a[64:67], a[120:123], v[44:47]\n"
"s_waitcnt lgkmcnt(4)\n"
"v_add_f32 v14, v14, v22\n"
"v_mfma_f32_16x16x32_bf16 v[48:51], a[64:67], a[128:131], v[48:51]\n"
"v_add_f32 v15, v15, v23\n"
"v_accvgpr_write_b32 a20, v12\n"
"v_mfma_f32_16x16x32_bf16 v[48:51], a[68:71], a[132:135], v[48:51]\n"
"global_load_dwordx4 a[56:59], v0, s[34:35] offset:2048 sc1\n"
"v_accvgpr_write_b32 a21, v13\n"
"v_mfma_f32_16x16x32_bf16 v[76:79], a[76:79], a[132:135], v[76:79]\n"
"s_waitcnt lgkmcnt(0)\n"
"v_accvgpr_write_b32 a22, v14\n"
"v_mfma_f32_16x16x32_bf16 v[76:79], a[72:75], a[128:131], v[76:79]\n"
"s_sub_u32 s29, s76, s29\n"
"v_accvgpr_write_b32 a23, v15\n"
"v_lshlrev_b32 v24, 16, v26\n"
"v_mfma_f32_16x16x32_bf16 v[80:83], a[72:75], a[136:139], v[80:83]\n"
"s_cmp_lt_u32 s29, 620\n"
"s_cselect_b32 s99, s99, 0\n"
"v_and_b32 v25, s63, v26\n"
"v_mfma_f32_16x16x32_bf16 v[80:83], a[76:79], a[140:143], v[80:83]\n"
"s_max_u32 s31, s31, s99\n"
"v_lshlrev_b32 v26, 16, v27\n"
"v_mfma_f32_16x16x32_bf16 v[52:55], a[68:71], a[140:143], v[52:55]\n"
"v_and_b32 v27, s63, v27\n"
"v_add_f32 v200, v200, v24\n"
"v_mfma_f32_16x16x32_bf16 v[52:55], a[64:67], a[136:139], v[52:55]\n"
"s_mov_b32 s99, 0\n"
"v_add_f32 v201, v201, v25\n"
"v_mfma_f32_16x16x32_bf16 v[56:59], a[64:67], a[144:147], v[56:59]\n"
"s_mov_b32 s98, 0\n"
"v_add_f32 v202, v202, v26\n"
"v_add_f32 v203, v203, v27\n"
"v_mfma_f32_16x16x32_bf16 v[56:59], a[68:71], a[148:151], v[56:59]\n"
"global_load_dwordx4 a[60:63], v0, s[34:35] offset:3072 sc1\n"
"s_memtime s[76:77]\n"
"s_load_dword s98, s[96:97], 256 glc\n"
"v_mfma_f32_16x16x32_bf16 v[84:87], a[76:79], a[148:151], v[84:87]\n"
"v_add_f32 v204, v204, v24\n"
"v_add_f32 v205, v205, v25\n"
"v_mfma_f32_16x16x32_bf16 v[84:87], a[72:75], a[144:147], v[84:87]\n"
"v_add_f32 v206, v206, v26\n"
"v_mfma_f32_16x16x32_bf16 a[4:7], a[72:75], a[152:155], a[4:7]\n"
"s_add_u32 s34, s34, 8192\n"
"v_add_f32 v207, v207, v27\n"
"v_mfma_f32_16x16x32_bf16 a[4:7], a[76:79], a[156:159], a[4:7]\n"
"v_add_f32 v208, v208, v24\n"
"v_add_f32 v209, v209, v25\n"
"v_mfma_f32_16x16x32_bf16 a[0:3], a[68:71], a[156:159], a[0:3]\n"
"v_add_f32 v210, v210, v26\n"
"v_mfma_f32_16x16x32_bf16 a[0:3], a[64:67], a[152:155], a[0:3]\n"
"s_waitcnt vmcnt(8)\n"
"v_add_f32 v211, v211, v27\n"
"v_mfma_f32_16x16x32_bf16 v[88:91], a[80:83], a[96:99], v[88:91]\n"
"v_add_f32 v212, v212, v24\n"
"v_add_f32 v213, v213, v25\n"
"v_mfma_f32_16x16x32_bf16 v[88:91], a[84:87], a[100:103], v[88:91]\n"
"global_load_dwordx4 a[64:67], v0, s[34:35] offset:-4096 sc1\n"
"v_add_f32 v214, v214, v26\n"
"v_mfma_f32_16x16x32_bf16 v[116:119], a[92:95], a[100:103], v[116:119]\n"
"s_mov_b32 s29, s76\n"
"ds_read_b128 a[100:103], v4 offset:17408\n"
"v_add_f32 v215, v215, v27\n"
"v_mfma_f32_16x16x32_bf16 v[116:119], a[88:91], a[96:99], v[116:119]\n"
"ds_read_b128 a[96:99], v4 offset:16384\n"
"v_add_f32 v216, v216, v24\n"
"v_mfma_f32_16x16x32_bf16 v[120:123], a[88:91], a[104:107], v[120:123]\n"
"s_sub_u32 s99, s99, s98\n"
"v_add_f32 v217, v217, v25\n"
"v_add_f32 v218, v218, v26\n"
"v_mfma_f32_16x16x32_bf16 v[120:123], a[92:95], a[108:111], v[120:123]\n"
"s_sub_u32 s99, s99, s98\n"
"v_add_f32 v219, v219, v27\n"
"v_add_f32 v220, v220, v24\n"
"v_mfma_f32_16x16x32_bf16 v[92:95], a[84:87], a[108:111], v[92:95]\n"
"s_sub_u32 s99, s99, s98\n"
"ds_read_b128 a[108:111], v4 offset:19456\n"
"v_add_f32 v221, v221, v25\n"
"v_mfma_f32_16x16x32_bf16 v[92:95], a[80:83], a[104:107], v[92:95]\n"
"s_sub_u32 s99, s99, s98\n"
"ds_read_b128 a[104:107], v4 offset:18432\n"
"v_add_f32 v222, v222, v26\n"
"v_mfma_f32_16x16x32_bf16 v[96:99], a[80:83], a[112:115], v[96:99]\n"
"s_sub_u32 s99, s99, s98\n"
"v_add_f32 v223, v223, v27\n"
"v_add_f32 v224, v224, v24\n"
"v_mfma_f32_16x16x32_bf16 v[96:99], a[84:87], a[116:119], v[96:99]\n"
"global_load_dwordx4 a[68:71], v0, s[34:35] offset:-3072 sc1\n"
"s_sub_u32 s99, s99, s98\n"
"v_mfma_f32_16x16x32_bf16 v[124:127], a[92:95], a[116:119], v[124:127]\n"
"s_sub_u32 s99, s99, s98\n"
"ds_read_b128 a[116:119], v4 offset:21504\n"
"v_add_f32 v225, v225, v25\n"
"v_mfma_f32_16x16x32_bf16 v[124:127], a[88:91], a[112:115], v[124:127]\n"
"s_sub_u32 s99, s99, s98\n"
"ds_read_b128 a[112:115], v4 offset:20480\n"
"v_add_f32 v226, v226, v26\n"
"v_mfma_f32_16x16x32_bf16 v[128:131], a[88:91], a[120:123], v[128:131]\n"
"s_sub_u32 s99, s99, s98\n"
"v_add_f32 v227, v227, v27\n"
"v_accvgpr_read_b32 v12, a24\n"
"v_mfma_f32_16x16x32_bf16 v[128:131], a[92:95], a[124:127], v[128:131]\n"
"s_sub_u32 s99, s99, s98\n"
"v_accvgpr_read_b32 v13, a25\n"
"v_accvgpr_read_b32 v14, a26\n"
"v_mfma_f32_16x16x32_bf16 v[100:103], a[84:87], a[124:127], v[100:103]\n"
"s_sub_u32 s99, s99, s98\n"
"ds_read_b128 a[124:127], v4 offset:23552\n"
"v_accvgpr_read_b32 v15, a27\n"
"v_mfma_f32_16x16x32_bf16 v[100:103], a[80:83], a[120:123], v[100:103]\n"
"s_sub_u32 s99, s99, s98\n"
"ds_read_b128 a[120:123], v4 offset:22528\n"
"v_add_f32 v12, v12, v24\n"
"v_mfma_f32_16x16x32_bf16 v[104:107], a[80:83], a[128:131], v[104:107]\n"
"s_sub_u32 s99, s99, s98\n"
"v_add_f32 v13, v13, v25\n"
"v_add_f32 v14, v14, v26\n"
"v_mfma_f32_16x16x32_bf16 v[104:107], a[84:87], a[132:135], v[104:107]\n"
"global_load_dwordx4 a[72:75], v0, s[34:35] offset:-2048 sc1\n"
"s_sub_u32 s99, s99, s98\n"
"v_mfma_f32_16x16x32_bf16 v[132:135], a[92:95], a[132:135], v[132:135]\n"
"s_sub_u32 s99, s99, s98\n"
"ds_read_b128 a[132:135], v4 offset:25600\n"
"v_add_f32 v15, v15, v27\n"
"v_mfma_f32_16x16x32_bf16 v[132:135], a[88:91], a[128:131], v[132:135]\n"
"s_sub_u32 s99, s99, s98\n"
"ds_read_b128 a[128:131], v4 offset:24576\n"
"v_accvgpr_write_b32 a24, v12\n"
"v_mfma_f32_16x16x32_bf16 v[136:139], a[88:91], a[136:139], v[136:139]\n"
"s_sub_u32 s99, s99, s98\n"
"v_accvgpr_write_b32 a25, v13\n"
"v_accvgpr_write_b32 a26, v14\n"
"v_mfma_f32_16x16x32_bf16 v[136:139], a[92:95], a[140:143], v[136:139]\n"
"s_sub_u32 s99, s99, s98\n"
"v_accvgpr_write_b32 a27, v15\n"
"v_lshlrev_b32 v28, 16, v30\n"
"v_mfma_f32_16x16x32_bf16 v[108:111], a[84:87], a[140:143], v[108:111]\n"
"s_sub_u32 s99, s99, s98\n"
"ds_read_b128 a[140:143], v4 offset:27648\n"
"v_and_b32 v29, s63, v30\n"
"v_mfma_f32_16x16x32_bf16 v[108:111], a[80:83], a[136:139], v[108:111]\n"
"s_sub_u32 s99, s99, s98\n"
"ds_read_b128 a[136:139], v4 offset:26624\n"
"v_lshlrev_b32 v30, 16, v31\n"
"v_mfma_f32_16x16x32_bf16 v[112:115], a[80:83], a[144:147], v[112:115]\n"
"s_sub_u32 s99, s99, s98\n"
"v_and_b32 v31, s63, v31\n"
"v_add_f32 v228, v228, v28\n"
"v_mfma_f32_16x16x32_bf16 v[112:115], a[84:87], a[148:151], v[112:115]\n"
"global_load_dwordx4 a[76:79], v0, s[34:35] offset:-1024 sc1\n"
"s_sub_u32 s99, s99, s98\n"
"v_mfma_f32_16x16x32_bf16 v[140:143], a[92:95], a[148:151], v[140:143]\n"
"s_sub_u32 s99, s99, s98\n"
"ds_read_b128 a[148:151], v4 offset:29696\n"
"v_add_f32 v229, v229, v29\n"
"v_mfma_f32_16x16x32_bf16 v[140:143], a[88:91], a[144:147], v[140:143]\n"
"s_sub_u32 s99, s99, s98\n"
"ds_read_b128 a[144:147], v4 offset:28672\n"
"v_add_f32 v230, v230, v30\n"
"v_mfma_f32_16x16x32_bf16 a[12:15], a[88:91], a[152:155], a[12:15]\n"
"s_memtime s[76:77]\n"
"v_add_f32 v231, v231, v31\n"
"v_add_f32 v232, v232, v28\n"
"v_mfma_f32_16x16x32_bf16 a[12:15], a[92:95], a[156:159], a[12:15]\n"
"v_add_f32 v233, v233, v29\n"
"v_add_f32 v234, v234, v30\n"
"v_mfma_f32_16x16x32_bf16 a[8:11], a[84:87], a[156:159], a[8:11]\n"
"ds_read_b128 a[156:159], v4 offset:31744\n"
"v_add_f32 v235, v235, v31\n"
"v_mfma_f32_16x16x32_bf16 a[8:11], a[80:83], a[152:155], a[8:11]\n"
"s_waitcnt vmcnt(8) lgkmcnt(11)\n"
"ds_read_b128 a[152:155], v4 offset:30720\n"
"v_add_f32 v236, v236, v28\n"
"v_mfma_f32_16x16x32_bf16 v[32:35], a[32:35], a[96:99], v[32:35]\n"
"v_add_f32 v237, v237, v29\n"
"v_add_f32 v238, v238, v30\n"
"v_mfma_f32_16x16x32_bf16 v[32:35], a[36:39], a[100:103], v[32:35]\n"
"global_load_dwordx4 a[80:83], v0, s[34:35] offset:0 sc1\n"
"v_add_f32 v239, v239, v31\n"
"v_mfma_f32_16x16x32_bf16 v[60:63], a[44:47], a[100:103], v[60:63]\n"
"v_add_f32 v240, v240, v28\n"
"v_add_f32 v241, v241, v29\n"
"v_mfma_f32_16x16x32_bf16 v[60:63], a[40:43], a[96:99], v[60:63]\n"
"v_add_f32 v242, v242, v30\n"
"v_mfma_f32_16x16x32_bf16 v[64:67], a[40:43], a[104:107], v[64:67]\n"
"v_add_f32 v243, v243, v31\n"
"v_add_f32 v244, v244, v28\n"
"v_mfma_f32_16x16x32_bf16 v[64:67], a[44:47], a[108:111], v[64:67]\n"
"L_st3861:\n"
"s_cbranch_execz L_retA\n"
"v_add_f32 v245, v245, v29\n"
"v_mfma_f32_16x16x32_bf16 v[36:39], a[36:39], a[108:111], v[36:39]\n"
"v_add_f32 v246, v246, v30\n"
"v_mfma_f32_16x16x32_bf16 v[36:39], a[32:35], a[104:107], v[36:39]\n"
"s_waitcnt lgkmcnt(8)\n"
"v_add_f32 v247, v247, v31\n"
"v_mfma_f32_16x16x32_bf16 v[40:43], a[32:35], a[112:115], v[40:43]\n"
"v_add_f32 v248, v248, v28\n"
"v_add_f32 v249, v249, v29\n"
"v_mfma_f32_16x16x32_bf16 v[40:43], a[36:39], a[116:119], v[40:43]\n"
"global_load_dwordx4 a[84:87], v0, s[34:35] offset:1024 sc1\n"
"s_add_u32 s48, s56, %12\n"
"s_addc_u32 s49, s57, 0\n"
"v_mfma_f32_16x16x32_bf16 v[68:71], a[44:47], a[116:119], v[68:71]\n"
"v_add_f32 v250, v250, v30\n"
"v_add_f32 v251, v251, v31\n"
"v_mfma_f32_16x16x32_bf16 v[68:71], a[40:43], a[112:115], v[68:71]\n"
"s_mov_b32 s50, 32768\n"
"v_add_f32 v252, v252, v28\n"
"v_add_f32 v253, v253, v29\n"
"v_mfma_f32_16x16x32_bf16 v[72:75], a[40:43], a[120:123], v[72:75]\n"
"v_add_f32 v254, v254, v30\n"
"v_mfma_f32_16x16x32_bf16 v[72:75], a[44:47], a[124:127], v[72:75]\n"
"s_mov_b32 s65, s78\n"
"v_add_f32 v255, v255, v31\n"
"v_mfma_f32_16x16x32_bf16 v[44:47], a[36:39], a[124:127], v[44:47]\n"
"v_accvgpr_read_b32 v12, a28\n"
"v_accvgpr_read_b32 v13, a29\n"
"v_mfma_f32_16x16x32_bf16 v[44:47], a[32:35], a[120:123], v[44:47]\n"
"s_mov_b64 s[40:41], s[52:53]\n"
"v_accvgpr_read_b32 v14, a30\n"
"s_waitcnt lgkmcnt(4)\n"
"v_mfma_f32_16x16x32_bf16 v[48:51], a[32:35], a[128:131], v[48:51]\n"
"s_mov_b32 s27, %21\n"
"v_accvgpr_read_b32 v15, a31\n"
"v_add_f32 v12, v12, v28\n"
"v_mfma_f32_16x16x32_bf16 v[48:51], a[36:39], a[132:135], v[48:51]\n"
"global_load_dwordx4 a[88:91], v0, s[34:35] offset:2048 sc1\n"
"s_mov_b32 s28, %23\n"
"v_mfma_f32_16x16x32_bf16 v[76:79], a[44:47], a[132:135], v[76:79]\n"
"s_waitcnt lgkmcnt(0)\n"
"v_add_f32 v13, v13, v29\n"
"v_add_f32 v14, v14, v30\n"
"v_mfma_f32_16x16x32_bf16 v[76:79], a[40:43], a[128:131], v[76:79]\n"
"s_sub_u32 s29, s76, s29\n"
"v_add_f32 v15, v15, v31\n"
"v_mfma_f32_16x16x32_bf16 v[80:83], a[40:43], a[136:139], v[80:83]\n"
"s_cmp_lt_u32 s29, 620\n"
"s_cselect_b32 s99, s99, 0\n"
"v_accvgpr_write_b32 a28, v12\n"
"v_mfma_f32_16x16x32_bf16 v[80:83], a[44:47], a[140:143], v[80:83]\n"
"s_max_u32 s30, s30, s99\n"
"v_accvgpr_write_b32 a29, v13\n"
"v_accvgpr_write_b32 a30, v14\n"
"v_mfma_f32_16x16x32_bf16 v[52:55], a[36:39], a[140:143], v[52:55]\n"
"s_cmp_eq_u32 s30, 0\n"
"s_cselect_b32 s30, 10, s30\n"
"v_accvgpr_write_b32 a31, v15\n"
"v_mfma_f32_16x16x32_bf16 v[52:55], a[32:35], a[136:139], v[52:55]\n"
"s_cmp_eq_u32 s31, 0\n"
"s_cselect_b32 s31, 10, s31\n"
"v_mfma_f32_16x16x32_bf16 v[56:59], a[32:35], a[144:147], v[56:59]\n"
"s_sub_u32 s69, s31, s30\n"
"v_mfma_f32_16x16x32_bf16 v[56:59], a[36:39], a[148:151], v[56:59]\n"
"global_load_dwordx4 a[92:95], v0, s[34:35] offset:3072 sc1\n"
"v_mov_b32 v20, s69\n"
"v_mfma_f32_16x16x32_bf16 v[84:87], a[44:47], a[148:151], v[84:87]\n"
"ds_write_b32 v2, v20 offset:8192\n"
"v_mfma_f32_16x16x32_bf16 v[84:87], a[40:43], a[144:147], v[84:87]\n"
"v_mfma_f32_16x16x32_bf16 a[4:7], a[40:43], a[152:155], a[4:7]\n"
"s_add_u32 s34, s34, 8192\n"
"v_mfma_f32_16x16x32_bf16 a[4:7], a[44:47], a[156:159], a[4:7]\n"
"v_mfma_f32_16x16x32_bf16 a[0:3], a[36:39], a[156:159], a[0:3]\n"
"v_mfma_f32_16x16x32_bf16 a[0:3], a[32:35], a[152:155], a[0:3]\n"
"s_waitcnt lgkmcnt(0)\n"
"s_barrier\n"
"s_waitcnt vmcnt(8)\n"
"v_mfma_f32_16x16x32_bf16 v[88:91], a[48:51], a[96:99], v[88:91]\n"
"ds_read_b32 v21, v3 offset:8192\n"
"v_mfma_f32_16x16x32_bf16 v[88:91], a[52:55], a[100:103], v[88:91]\n"
"global_load_dwordx4 a[32:35], v0, s[34:35] offset:-4096 sc1\n"
"v_mfma_f32_16x16x32_bf16 v[116:119], a[60:63], a[100:103], v[116:119]\n"
"ds_read_b128 a[100:103], v5 offset:17408\n"
"v_mfma_f32_16x16x32_bf16 v[116:119], a[56:59], a[96:99], v[116:119]\n"
"ds_read_b128 a[96:99], v5 offset:16384\n"
"v_mfma_f32_16x16x32_bf16 v[120:123], a[56:59], a[104:107], v[120:123]\n"
"ds_read_b32 v22, v4 offset:8192\n"
"v_mfma_f32_16x16x32_bf16 v[120:123], a[60:63], a[108:111], v[120:123]\n"
"ds_read_b32 v23, v5 offset:8192\n"
"v_mfma_f32_16x16x32_bf16 v[92:95], a[52:55], a[108:111], v[92:95]\n"
"ds_read_b128 a[108:111], v5 offset:19456\n"
"v_mfma_f32_16x16x32_bf16 v[92:95], a[48:51], a[104:107], v[92:95]\n"
"ds_read_b128 a[104:107], v5 offset:18432\n"
"v_mfma_f32_16x16x32_bf16 v[96:99], a[48:51], a[112:115], v[96:99]\n"
"v_mfma_f32_16x16x32_bf16 v[96:99], a[52:55], a[116:119], v[96:99]\n"
"global_load_dwordx4 a[36:39], v0, s[34:35] offset:-3072 sc1\n"
"v_mfma_f32_16x16x32_bf16 v[124:127], a[60:63], a[116:119], v[124:127]\n"
"s_waitcnt lgkmcnt(2)\n"
"ds_read_b128 a[116:119], v5 offset:21504\n"
"v_add_u32 v20, v20, v21\n"
"v_mfma_f32_16x16x32_bf16 v[124:127], a[56:59], a[112:115], v[124:127]\n"
"ds_read_b128 a[112:115], v5 offset:20480\n"
"v_mfma_f32_16x16x32_bf16 v[128:131], a[56:59], a[120:123], v[128:131]\n"
"v_add3_u32 v20, v20, v22, v23\n"
"v_mfma_f32_16x16x32_bf16 v[128:131], a[60:63], a[124:127], v[128:131]\n"
"v_mfma_f32_16x16x32_bf16 v[100:103], a[52:55], a[124:127], v[100:103]\n"
"ds_read_b128 a[124:127], v5 offset:23552\n"
"v_readfirstlane_b32 s69, v20\n"
"v_mfma_f32_16x16x32_bf16 v[100:103], a[48:51], a[120:123], v[100:103]\n"
"ds_read_b128 a[120:123], v5 offset:22528\n"
"v_mfma_f32_16x16x32_bf16 v[104:107], a[48:51], a[128:131], v[104:107]\n"
"v_mfma_f32_16x16x32_bf16 v[104:107], a[52:55], a[132:135], v[104:107]\n"
"s_cmp_le_i32 s69, -2\n"
"global_load_dwordx4 a[40:43], v0, s[34:35] offset:-2048 sc1\n"
"s_cselect_b32 s66, 2, 1\n"
"v_mfma_f32_16x16x32_bf16 v[132:135], a[60:63], a[132:135], v[132:135]\n"
"ds_read_b128 a[132:135], v5 offset:25600\n"
"v_mfma_f32_16x16x32_bf16 v[132:135], a[56:59], a[128:131], v[132:135]\n"
"s_cmp_ge_i32 s69, 2\n"
"ds_read_b128 a[128:131], v5 offset:24576\n"
"s_cselect_b32 s66, 3, s66\n"
"v_mfma_f32_16x16x32_bf16 v[136:139], a[56:59], a[136:139], v[136:139]\n"
"ds_read_b128 v[16:19], v7 offset:32768\n"
"v_mfma_f32_16x16x32_bf16 v[136:139], a[60:63], a[140:143], v[136:139]\n"
"s_lshl_b32 s73, s66, 13\n"
"s_xor_b32 s60, s79, s66\n"
"v_mfma_f32_16x16x32_bf16 v[108:111], a[52:55], a[140:143], v[108:111]\n"
"ds_read_b128 a[140:143], v5 offset:27648\n"
"v_mfma_f32_16x16x32_bf16 v[108:111], a[48:51], a[136:139], v[108:111]\n"
"s_cmp_eq_u32 s66, 3\n"
"ds_read_b128 a[136:139], v5 offset:26624\n"
"s_cselect_b32 s72, 8192, 0\n"
"v_mfma_f32_16x16x32_bf16 v[112:115], a[48:51], a[144:147], v[112:115]\n"
"s_abs_i32 s30, s69\n"
"v_mfma_f32_16x16x32_bf16 v[112:115], a[52:55], a[148:151], v[112:115]\n"
"global_load_dwordx4 a[44:47], v0, s[34:35] offset:-1024 sc1\n"
"s_add_u32 s52, s56, %11\n"
"s_addc_u32 s53, s57, 0\n"
"v_mfma_f32_16x16x32_bf16 v[140:143], a[60:63], a[148:151], v[140:143]\n"
"s_cmp_lt_u32 s30, 26\n"
"s_cselect_b32 s30, 2, 0\n"
"ds_read_b128 a[148:151], v5 offset:29696\n"
"v_mfma_f32_16x16x32_bf16 v[140:143], a[56:59], a[144:147], v[140:143]\n"
"s_add_u32 s52, s52, 4096\n"
"ds_read_b128 a[144:147], v5 offset:28672\n"
"s_addc_u32 s53, s53, 0\n"
"v_mfma_f32_16x16x32_bf16 a[12:15], a[56:59], a[152:155], a[12:15]\n"
"v_mfma_f32_16x16x32_bf16 a[12:15], a[60:63], a[156:159], a[12:15]\n"
"s_add_u32 s54, s52, s73\n"
"s_addc_u32 s55, s53, 0\n"
"v_mfma_f32_16x16x32_bf16 a[8:11], a[52:55], a[156:159], a[8:11]\n"
"ds_read_b128 a[156:159], v5 offset:31744\n"
"v_mfma_f32_16x16x32_bf16 a[8:11], a[48:51], a[152:155], a[8:11]\n"
"s_add_u32 s52, s52, s72\n"
"ds_read_b128 a[152:155], v5 offset:30720\n"
"s_addc_u32 s53, s53, 0\n"
"L_P6:\n"
"s_waitcnt vmcnt(8) lgkmcnt(13)\n"
"v_mfma_f32_16x16x32_bf16 v[32:35], a[64:67], a[96:99], v[32:35]\n"
"v_mfma_f32_16x16x32_bf16 v[32:35], a[68:71], a[100:103], v[32:35]\n"
"global_load_dwordx4 a[48:51], v0, s[34:35] offset:0 sc1\n"
"v_mfma_f32_16x16x32_bf16 v[60:63], a[76:79], a[100:103], v[60:63]\n"
"v_mfma_f32_16x16x32_bf16 v[60:63], a[72:75], a[96:99], v[60:63]\n"
"v_mfma_f32_16x16x32_bf16 v[64:67], a[72:75], a[104:107], v[64:67]\n"
"v_mfma_f32_16x16x32_bf16 v[64:67], a[76:79], a[108:111], v[64:67]\n"
"v_mfma_f32_16x16x32_bf16 v[36:39], a[68:71], a[108:111], v[36:39]\n"
"s_waitcnt lgkmcnt(6)\n"
"v_lshlrev_b32 v12, 16, v16\n"
"v_mfma_f32_16x16x32_bf16 v[36:39], a[64:67], a[104:107], v[36:39]\n"
"v_and_b32 v13, s63, v16\n"
"v_lshlrev_b32 v14, 16, v17\n"
"v_mfma_f32_16x16x32_bf16 v[40:43], a[64:67], a[112:115], v[40:43]\n"
"v_and_b32 v15, s63, v17\n"
"v_mfma_f32_16x16x32_bf16 v[40:43], a[68:71], a[116:119], v[40:43]\n"
"global_load_dwordx4 a[52:55], v0, s[34:35] offset:1024 sc1\n"
"ds_read_b128 v[20:23], v7 offset:34816\n"
"v_mfma_f32_16x16x32_bf16 v[68:71], a[76:79], a[116:119], v[68:71]\n"
"v_mul_f32 v12, v12, v32\n"
"v_mul_f32 v13, v13, v33\n"
"v_mfma_f32_16x16x32_bf16 v[68:71], a[72:75], a[112:115], v[68:71]\n"
"v_mul_f32 v14, v14, v34\n"
"v_mfma_f32_16x16x32_bf16 v[72:75], a[72:75], a[120:123], v[72:75]\n"
"v_mul_f32 v15, v15, v35\n"
"v_mfma_f32_16x16x32_bf16 v[72:75], a[76:79], a[124:127], v[72:75]\n"
"v_cvt_pk_bf16_f32 v24, v12, v13\n"
"v_cvt_pk_bf16_f32 v25, v14, v15\n"
"v_mfma_f32_16x16x32_bf16 v[44:47], a[68:71], a[124:127], v[44:47]\n"
"ds_write_b64 v2, v[24:25] offset:0\n"
"v_mfma_f32_16x16x32_bf16 v[44:47], a[64:67], a[120:123], v[44:47]\n"
"s_waitcnt lgkmcnt(6)\n"
"v_lshlrev_b32 v12, 16, v18\n"
"v_mfma_f32_16x16x32_bf16 v[48:51], a[64:67], a[128:131], v[48:51]\n"
"v_and_b32 v13, s63, v18\n"
"v_lshlrev_b32 v14, 16, v19\n"
"v_mfma_f32_16x16x32_bf16 v[48:51], a[68:71], a[132:135], v[48:51]\n"
"global_load_dwordx4 a[56:59], v0, s[34:35] offset:2048 sc1\n"
"v_and_b32 v15, s63, v19\n"
"v_mfma_f32_16x16x32_bf16 v[76:79], a[76:79], a[132:135], v[76:79]\n"
"v_mul_f32 v12, v12, v60\n"
"v_mfma_f32_16x16x32_bf16 v[76:79], a[72:75], a[128:131], v[76:79]\n"
"v_mul_f32 v13, v13, v61\n"
"v_mul_f32 v14, v14, v62\n"
"v_mfma_f32_16x16x32_bf16 v[80:83], a[72:75], a[136:139], v[80:83]\n"
"v_mul_f32 v15, v15, v63\n"
"v_mfma_f32_16x16x32_bf16 v[80:83], a[76:79], a[140:143], v[80:83]\n"
"v_cvt_pk_bf16_f32 v26, v12, v13\n"
"v_cvt_pk_bf16_f32 v27, v14, v15\n"
"v_mfma_f32_16x16x32_bf16 v[52:55], a[68:71], a[140:143], v[52:55]\n"
"ds_write_b64 v2, v[26:27] offset:8\n"
"v_mfma_f32_16x16x32_bf16 v[52:55], a[64:67], a[136:139], v[52:55]\n"
"s_waitcnt lgkmcnt(2)\n"
"v_lshlrev_b32 v12, 16, v20\n"
"v_mfma_f32_16x16x32_bf16 v[56:59], a[64:67], a[144:147], v[56:59]\n"
"v_and_b32 v13, s63, v20\n"
"v_mfma_f32_16x16x32_bf16 v[56:59], a[68:71], a[148:151], v[56:59]\n"
"global_load_dwordx4 a[60:63], v0, s[34:35] offset:3072 sc1\n"
"v_lshlrev_b32 v14, 16, v21\n"
"v_mfma_f32_16x16x32_bf16 v[84:87], a[76:79], a[148:151], v[84:87]\n"
"v_and_b32 v15, s63, v21\n"
"v_xor_b32 v16, 64, v7\n"
"v_mfma_f32_16x16x32_bf16 v[84:87], a[72:75], a[144:147], v[84:87]\n"
"ds_read_b128 v[16:19], v16 offset:32768\n"
"v_mfma_f32_16x16x32_bf16 a[4:7], a[72:75], a[152:155], a[4:7]\n"
"s_add_u32 s34, s34, 8192\n"
"v_mul_f32 v12, v12, v36\n"
"v_mul_f32 v13, v13, v37\n"
"v_mfma_f32_16x16x32_bf16 a[4:7], a[76:79], a[156:159], a[4:7]\n"
"v_mul_f32 v14, v14, v38\n"
"v_mfma_f32_16x16x32_bf16 a[0:3], a[68:71], a[156:159], a[0:3]\n"
"v_mul_f32 v15, v15, v39\n"
"v_mfma_f32_16x16x32_bf16 a[0:3], a[64:67], a[152:155], a[0:3]\n"
"s_waitcnt vmcnt(8)\n"
"v_cvt_pk_bf16_f32 v28, v12, v13\n"
"v_cvt_pk_bf16_f32 v29, v14, v15\n"
"v_mfma_f32_16x16x32_bf16 v[88:91], a[80:83], a[96:99], v[88:91]\n"
"v_lshlrev_b32 v12, 16, v22\n"
"v_mfma_f32_16x16x32_bf16 v[88:91], a[84:87], a[100:103], v[88:91]\n"
"global_load_dwordx4 a[64:67], v0, s[34:35] offset:-4096 sc1\n"
"v_and_b32 v13, s63, v22\n"
"v_mfma_f32_16x16x32_bf16 v[116:119], a[92:95], a[100:103], v[116:119]\n"
"v_lshlrev_b32 v14, 16, v23\n"
"v_and_b32 v15, s63, v23\n"
"v_mfma_f32_16x16x32_bf16 v[116:119], a[88:91], a[96:99], v[116:119]\n"
"v_mul_f32 v12, v12, v64\n"
"v_mfma_f32_16x16x32_bf16 v[120:123], a[88:91], a[104:107], v[120:123]\n"
"v_mul_f32 v13, v13, v65\n"
"v_mfma_f32_16x16x32_bf16 v[120:123], a[92:95], a[108:111], v[120:123]\n"
"v_mul_f32 v14, v14, v66\n"
"v_mul_f32 v15, v15, v67\n"
"v_mfma_f32_16x16x32_bf16 v[92:95], a[84:87], a[108:111], v[92:95]\n"
"v_cvt_pk_bf16_f32 v30, v12, v13\n"
"v_mfma_f32_16x16x32_bf16 v[92:95], a[80:83], a[104:107], v[92:95]\n"
"v_cvt_pk_bf16_f32 v31, v14, v15\n"
"v_mfma_f32_16x16x32_bf16 v[96:99], a[80:83], a[112:115], v[96:99]\n"
"global_store_dwordx4 v0, v[28:31], s[52:53] offset:-2048 sc1\n"
"ds_write_b128 v2, v[28:31] offset:2048\n"
"v_mfma_f32_16x16x32_bf16 v[96:99], a[84:87], a[116:119], v[96:99]\n"
"s_waitcnt lgkmcnt(1)\n"
"global_load_dwordx4 a[68:71], v0, s[34:35] offset:-3072 sc1\n"
"v_lshlrev_b32 v12, 16, v16\n"
"v_mfma_f32_16x16x32_bf16 v[124:127], a[92:95], a[116:119], v[124:127]\n"
"v_and_b32 v13, s63, v16\n"
"v_mfma_f32_16x16x32_bf16 v[124:127], a[88:91], a[112:115], v[124:127]\n"
"v_lshlrev_b32 v14, 16, v17\n"
"v_and_b32 v15, s63, v17\n"
"v_mfma_f32_16x16x32_bf16 v[128:131], a[88:91], a[120:123], v[128:131]\n"
"v_xor_b32 v20, 64, v7\n"
"v_mfma_f32_16x16x32_bf16 v[128:131], a[92:95], a[124:127], v[128:131]\n"
"ds_read_b128 v[20:23], v20 offset:34816\n"
"v_mfma_f32_16x16x32_bf16 v[100:103], a[84:87], a[124:127], v[100:103]\n"
"v_mul_f32 v12, v12, v88\n"
"v_mul_f32 v13, v13, v89\n"
"v_mfma_f32_16x16x32_bf16 v[100:103], a[80:83], a[120:123], v[100:103]\n"
"v_mul_f32 v14, v14, v90\n"
"v_mfma_f32_16x16x32_bf16 v[104:107], a[80:83], a[128:131], v[104:107]\n"
"v_mul_f32 v15, v15, v91\n"
"v_mfma_f32_16x16x32_bf16 v[104:107], a[84:87], a[132:135], v[104:107]\n"
"global_load_dwordx4 a[72:75], v0, s[34:35] offset:-2048 sc1\n"
"v_cvt_pk_bf16_f32 v24, v12, v13\n"
"v_mfma_f32_16x16x32_bf16 v[132:135], a[92:95], a[132:135], v[132:135]\n"
"v_cvt_pk_bf16_f32 v25, v14, v15\n"
"v_lshlrev_b32 v12, 16, v18\n"
"v_mfma_f32_16x16x32_bf16 v[132:135], a[88:91], a[128:131], v[132:135]\n"
"v_and_b32 v13, s63, v18\n"
"v_mfma_f32_16x16x32_bf16 v[136:139], a[88:91], a[136:139], v[136:139]\n"
"v_lshlrev_b32 v14, 16, v19\n"
"v_and_b32 v15, s63, v19\n"
"v_mfma_f32_16x16x32_bf16 v[136:139], a[92:95], a[140:143], v[136:139]\n"
"v_mul_f32 v12, v12, v116\n"
"v_mfma_f32_16x16x32_bf16 v[108:111], a[84:87], a[140:143], v[108:111]\n"
"v_mul_f32 v13, v13, v117\n"
"v_mfma_f32_16x16x32_bf16 v[108:111], a[80:83], a[136:139], v[108:111]\n"
"v_mul_f32 v14, v14, v118\n"
"v_mul_f32 v15, v15, v119\n"
"v_mfma_f32_16x16x32_bf16 v[112:115], a[80:83], a[144:147], v[112:115]\n"
"v_cvt_pk_bf16_f32 v26, v12, v13\n"
"v_mfma_f32_16x16x32_bf16 v[112:115], a[84:87], a[148:151], v[112:115]\n"
"global_load_dwordx4 a[76:79], v0, s[34:35] offset:-1024 sc1\n"
"v_cvt_pk_bf16_f32 v27, v14, v15\n"
"v_mfma_f32_16x16x32_bf16 v[140:143], a[92:95], a[148:151], v[140:143]\n"
"global_store_dwordx4 v0, v[24:27], s[52:53] offset:-3072 sc1\n"
"ds_write_b128 v2, v[24:27] offset:1024\n"
"v_mfma_f32_16x16x32_bf16 v[140:143], a[88:91], a[144:147], v[140:143]\n"
"s_waitcnt lgkmcnt(1)\n"
"v_lshlrev_b32 v12, 16, v20\n"
"v_mfma_f32_16x16x32_bf16 a[12:15], a[88:91], a[152:155], a[12:15]\n"
"v_and_b32 v13, s63, v20\n"
"v_mfma_f32_16x16x32_bf16 a[12:15], a[92:95], a[156:159], a[12:15]\n"
"v_lshlrev_b32 v14, 16, v21\n"
"v_mfma_f32_16x16x32_bf16 a[8:11], a[84:87], a[156:159], a[8:11]\n"
"ds_read_b128 v[16:19], v7 offset:36864\n"
"v_and_b32 v15, s63, v21\n"
"v_mfma_f32_16x16x32_bf16 a[8:11], a[80:83], a[152:155], a[8:11]\n"
"s_waitcnt vmcnt(10)\n"
"v_mul_f32 v12, v12, v92\n"
"v_mfma_f32_16x16x32_bf16 v[144:147], a[32:35], a[96:99], v[144:147]\n"
"v_mul_f32 v13, v13, v93\n"
"v_mul_f32 v14, v14, v94\n"
"v_mfma_f32_16x16x32_bf16 v[144:147], a[36:39], a[100:103], v[144:147]\n"
"global_load_dwordx4 a[80:83], v0, s[34:35] offset:0 sc1\n"
"v_mul_f32 v15, v15, v95\n"
"v_mfma_f32_16x16x32_bf16 v[172:175], a[44:47], a[100:103], v[172:175]\n"
"v_cvt_pk_bf16_f32 v28, v12, v13\n"
"v_mfma_f32_16x16x32_bf16 v[172:175], a[40:43], a[96:99], v[172:175]\n"
"v_cvt_pk_bf16_f32 v29, v14, v15\n"
"v_mfma_f32_16x16x32_bf16 v[176:179], a[40:43], a[104:107], v[176:179]\n"
"ds_write_b64 v2, v[28:29] offset:3072\n"
"v_mfma_f32_16x16x32_bf16 v[176:179], a[44:47], a[108:111], v[176:179]\n"
"v_lshlrev_b32 v12, 16, v22\n"
"v_and_b32 v13, s63, v22\n"
"v_mfma_f32_16x16x32_bf16 v[148:151], a[36:39], a[108:111], v[148:151]\n"
"v_lshlrev_b32 v14, 16, v23\n"
"v_mfma_f32_16x16x32_bf16 v[148:151], a[32:35], a[104:107], v[148:151]\n"
"v_and_b32 v15, s63, v23\n"
"v_mul_f32 v12, v12, v120\n"
"v_mfma_f32_16x16x32_bf16 v[152:155], a[32:35], a[112:115], v[152:155]\n"
"v_mul_f32 v13, v13, v121\n"
"v_mfma_f32_16x16x32_bf16 v[152:155], a[36:39], a[116:119], v[152:155]\n"
"global_load_dwordx4 a[84:87], v0, s[34:35] offset:1024 sc1\n"
"v_mul_f32 v14, v14, v122\n"
"v_mfma_f32_16x16x32_bf16 v[180:183], a[44:47], a[116:119], v[180:183]\n"
"v_mul_f32 v15, v15, v123\n"
"v_cvt_pk_bf16_f32 v30, v12, v13\n"
"v_mfma_f32_16x16x32_bf16 v[180:183], a[40:43], a[112:115], v[180:183]\n"
"v_cvt_pk_bf16_f32 v31, v14, v15\n"
"v_mfma_f32_16x16x32_bf16 v[184:187], a[40:43], a[120:123], v[184:187]\n"
"ds_write_b64 v2, v[30:31] offset:3080\n"
"v_mfma_f32_16x16x32_bf16 v[184:187], a[44:47], a[124:127], v[184:187]\n"
"s_waitcnt lgkmcnt(2)\n"
"v_lshlrev_b32 v12, 16, v16\n"
"v_and_b32 v13, s63, v16\n"
"v_mfma_f32_16x16x32_bf16 v[156:159], a[36:39], a[124:127], v[156:159]\n"
"v_lshlrev_b32 v14, 16, v17\n"
"v_mfma_f32_16x16x32_bf16 v[156:159], a[32:35], a[120:123], v[156:159]\n"
"v_and_b32 v15, s63, v17\n"
"v_xor_b32 v20, 64, v7\n"
"v_mfma_f32_16x16x32_bf16 v[160:163], a[32:35], a[128:131], v[160:163]\n"
"ds_read_b128 v[20:23], v20 offset:36864\n"
"v_mfma_f32_16x16x32_bf16 v[160:163], a[36:39], a[132:135], v[160:163]\n"
"global_load_dwordx4 a[88:91], v0, s[34:35] offset:2048 sc1\n"
"v_mul_f32 v12, v12, v40\n"
"v_mfma_f32_16x16x32_bf16 v[188:191], a[44:47], a[132:135], v[188:191]\n"
"v_mul_f32 v13, v13, v41\n"
"v_mfma_f32_16x16x32_bf16 v[188:191], a[40:43], a[128:131], v[188:191]\n"
"v_mul_f32 v14, v14, v42\n"
"v_mul_f32 v15, v15, v43\n"
"v_mfma_f32_16x16x32_bf16 v[192:195], a[40:43], a[136:139], v[192:195]\n"
"v_cvt_pk_bf16_f32 v24, v12, v13\n"
"v_mfma_f32_16x16x32_bf16 v[192:195], a[44:47], a[140:143], v[192:195]\n"
"v_cvt_pk_bf16_f32 v25, v14, v15\n"
"v_mfma_f32_16x16x32_bf16 v[164:167], a[36:39], a[140:143], v[164:167]\n"
"v_lshlrev_b32 v12, 16, v18\n"
"v_and_b32 v13, s63, v18\n"
"v_mfma_f32_16x16x32_bf16 v[164:167], a[32:35], a[136:139], v[164:167]\n"
"v_lshlrev_b32 v14, 16, v19\n"
"v_mfma_f32_16x16x32_bf16 v[168:171], a[32:35], a[144:147], v[168:171]\n"
"v_and_b32 v15, s63, v19\n"
"v_mul_f32 v12, v12, v68\n"
"v_mfma_f32_16x16x32_bf16 v[168:171], a[36:39], a[148:151], v[168:171]\n"
"global_load_dwordx4 a[92:95], v0, s[34:35] offset:3072 sc1\n"
"v_mul_f32 v13, v13, v69\n"
"v_mfma_f32_16x16x32_bf16 v[196:199], a[44:47], a[148:151], v[196:199]\n"
"v_mul_f32 v14, v14, v70\n"
"v_mfma_f32_16x16x32_bf16 v[196:199], a[40:43], a[144:147], v[196:199]\n"
"v_mul_f32 v15, v15, v71\n"
"v_mfma_f32_16x16x32_bf16 a[20:23], a[40:43], a[152:155], a[20:23]\n"
"s_add_u32 s34, s34, 8192\n"
"v_cvt_pk_bf16_f32 v26, v12, v13\n"
"v_cvt_pk_bf16_f32 v27, v14, v15\n"
"v_mfma_f32_16x16x32_bf16 a[20:23], a[44:47], a[156:159], a[20:23]\n"
"global_store_dwordx4 v0, v[24:27], s[52:53] offset:0 sc1\n"
"ds_write_b128 v2, v[24:27] offset:4096\n"
"v_mfma_f32_16x16x32_bf16 a[16:19], a[36:39], a[156:159], a[16:19]\n"
"s_waitcnt lgkmcnt(1)\n"
"v_lshlrev_b32 v12, 16, v20\n"
"v_mfma_f32_16x16x32_bf16 a[16:19], a[32:35], a[152:155], a[16:19]\n"
"s_waitcnt vmcnt(11)\n"
"v_and_b32 v13, s63, v20\n"
"v_mfma_f32_16x16x32_bf16 v[200:203], a[48:51], a[96:99], v[200:203]\n"
"v_lshlrev_b32 v14, 16, v21\n"
"v_and_b32 v15, s63, v21\n"
"v_mfma_f32_16x16x32_bf16 v[200:203], a[52:55], a[100:103], v[200:203]\n"
"global_load_dwordx4 a[32:35], v0, s[34:35] offset:-4096 sc1\n"
"ds_read_b128 v[16:19], v7 offset:38912\n"
"v_mfma_f32_16x16x32_bf16 v[228:231], a[60:63], a[100:103], v[228:231]\n"
"ds_read_b128 a[100:103], v4 offset:17408\n"
"v_mul_f32 v12, v12, v96\n"
"v_mfma_f32_16x16x32_bf16 v[228:231], a[56:59], a[96:99], v[228:231]\n"
"ds_read_b128 a[96:99], v4 offset:16384\n"
"v_mul_f32 v13, v13, v97\n"
"v_mfma_f32_16x16x32_bf16 v[232:235], a[56:59], a[104:107], v[232:235]\n"
"v_mul_f32 v14, v14, v98\n"
"v_mfma_f32_16x16x32_bf16 v[232:235], a[60:63], a[108:111], v[232:235]\n"
"v_mul_f32 v15, v15, v99\n"
"v_cvt_pk_bf16_f32 v28, v12, v13\n"
"v_mfma_f32_16x16x32_bf16 v[204:207], a[52:55], a[108:111], v[204:207]\n"
"ds_read_b128 a[108:111], v4 offset:19456\n"
"v_cvt_pk_bf16_f32 v29, v14, v15\n"
"v_mfma_f32_16x16x32_bf16 v[204:207], a[48:51], a[104:107], v[204:207]\n"
"ds_read_b128 a[104:107], v4 offset:18432\n"
"v_lshlrev_b32 v12, 16, v22\n"
"v_mfma_f32_16x16x32_bf16 v[208:211], a[48:51], a[112:115], v[208:211]\n"
"v_and_b32 v13, s63, v22\n"
"v_lshlrev_b32 v14, 16, v23\n"
"v_mfma_f32_16x16x32_bf16 v[208:211], a[52:55], a[116:119], v[208:211]\n"
"global_load_dwordx4 a[36:39], v0, s[34:35] offset:-3072 sc1\n"
"v_and_b32 v15, s63, v23\n"
"v_mfma_f32_16x16x32_bf16 v[236:239], a[60:63], a[116:119], v[236:239]\n"
"ds_read_b128 a[116:119], v4 offset:21504\n"
"v_mul_f32 v12, v12, v124\n"
"v_mfma_f32_16x16x32_bf16 v[236:239], a[56:59], a[112:115], v[236:239]\n"
"ds_read_b128 a[112:115], v4 offset:20480\n"
"v_mul_f32 v13, v13, v125\n"
"v_mfma_f32_16x16x32_bf16 v[240:243], a[56:59], a[120:123], v[240:243]\n"
"v_mul_f32 v14, v14, v126\n"
"v_mul_f32 v15, v15, v127\n"
"v_mfma_f32_16x16x32_bf16 v[240:243], a[60:63], a[124:127], v[240:243]\n"
"v_cvt_pk_bf16_f32 v30, v12, v13\n"
"v_mfma_f32_16x16x32_bf16 v[212:215], a[52:55], a[124:127], v[212:215]\n"
"ds_read_b128 a[124:127], v4 offset:23552\n"
"v_cvt_pk_bf16_f32 v31, v14, v15\n"
"v_mfma_f32_16x16x32_bf16 v[212:215], a[48:51], a[120:123], v[212:215]\n"
"ds_read_b128 a[120:123], v4 offset:22528\n"
"v_mfma_f32_16x16x32_bf16 v[216:219], a[48:51], a[128:131], v[216:219]\n"
"global_store_dwordx4 v0, v[28:31], s[52:53] offset:1024 sc1\n"
"ds_write_b128 v2, v[28:31] offset:5120\n"
"v_mfma_f32_16x16x32_bf16 v[216:219], a[52:55], a[132:135], v[216:219]\n"
"s_waitcnt lgkmcnt(9)\n"
"global_load_dwordx4 a[40:43], v0, s[34:35] offset:-2048 sc1\n"
"v_lshlrev_b32 v12, 16, v16\n"
"v_mfma_f32_16x16x32_bf16 v[244:247], a[60:63], a[132:135], v[244:247]\n"
"ds_read_b128 a[132:135], v4 offset:25600\n"
"v_and_b32 v13, s63, v16\n"
"v_mfma_f32_16x16x32_bf16 v[244:247], a[56:59], a[128:131], v[244:247]\n"
"ds_read_b128 a[128:131], v4 offset:24576\n"
"v_lshlrev_b32 v14, 16, v17\n"
"v_mfma_f32_16x16x32_bf16 v[248:251], a[56:59], a[136:139], v[248:251]\n"
"v_and_b32 v15, s63, v17\n"
"v_xor_b32 v20, 64, v7\n"
"v_mfma_f32_16x16x32_bf16 v[248:251], a[60:63], a[140:143], v[248:251]\n"
"ds_read_b128 v[20:23], v20 offset:38912\n"
"v_mul_f32 v12, v12, v44\n"
"v_mfma_f32_16x16x32_bf16 v[220:223], a[52:55], a[140:143], v[220:223]\n"
"ds_read_b128 a[140:143], v4 offset:27648\n"
"v_mul_f32 v13, v13, v45\n"
"v_mfma_f32_16x16x32_bf16 v[220:223], a[48:51], a[136:139], v[220:223]\n"
"ds_read_b128 a[136:139], v4 offset:26624\n"
"v_mul_f32 v14, v14, v46\n"
"v_mfma_f32_16x16x32_bf16 v[224:227], a[48:51], a[144:147], v[224:227]\n"
"v_mul_f32 v15, v15, v47\n"
"v_cvt_pk_bf16_f32 v24, v12, v13\n"
"v_mfma_f32_16x16x32_bf16 v[224:227], a[52:55], a[148:151], v[224:227]\n"
"global_load_dwordx4 a[44:47], v0, s[34:35] offset:-1024 sc1\n"
"v_cvt_pk_bf16_f32 v25, v14, v15\n"
"v_mfma_f32_16x16x32_bf16 v[252:255], a[60:63], a[148:151], v[252:255]\n"
"ds_read_b128 a[148:151], v4 offset:29696\n"
"v_mfma_f32_16x16x32_bf16 v[252:255], a[56:59], a[144:147], v[252:255]\n"
"ds_read_b128 a[144:147], v4 offset:28672\n"
"v_mfma_f32_16x16x32_bf16 a[28:31], a[56:59], a[152:155], a[28:31]\n"
"ds_write_b64 v2, v[24:25] offset:6144\n"
"v_mfma_f32_16x16x32_bf16 a[28:31], a[60:63], a[156:159], a[28:31]\n"
"v_lshlrev_b32 v12, 16, v18\n"
"v_and_b32 v13, s63, v18\n"
"v_mfma_f32_16x16x32_bf16 a[24:27], a[52:55], a[156:159], a[24:27]\n"
"ds_read_b128 a[156:159], v4 offset:31744\n"
"v_lshlrev_b32 v14, 16, v19\n"
"v_mfma_f32_16x16x32_bf16 a[24:27], a[48:51], a[152:155], a[24:27]\n"
"s_waitcnt vmcnt(11) lgkmcnt(14)\n"
"ds_read_b128 a[152:155], v4 offset:30720\n"
"v_and_b32 v15, s63, v19\n"
"v_mfma_f32_16x16x32_bf16 v[144:147], a[64:67], a[96:99], v[144:147]\n"
"v_mul_f32 v12, v12, v72\n"
"v_mul_f32 v13, v13, v73\n"
"v_mfma_f32_16x16x32_bf16 v[144:147], a[68:71], a[100:103], v[144:147]\n"
"global_load_dwordx4 a[48:51], v0, s[34:35] offset:0 sc1\n"
"v_mul_f32 v14, v14, v74\n"
"v_mfma_f32_16x16x32_bf16 v[172:175], a[76:79], a[100:103], v[172:175]\n"
"v_mul_f32 v15, v15, v75\n"
"v_cvt_pk_bf16_f32 v26, v12, v13\n"
"v_mfma_f32_16x16x32_bf16 v[172:175], a[72:75], a[96:99], v[172:175]\n"
"v_cvt_pk_bf16_f32 v27, v14, v15\n"
"v_mfma_f32_16x16x32_bf16 v[176:179], a[72:75], a[104:107], v[176:179]\n"
"ds_write_b64 v2, v[26:27] offset:6152\n"
"v_mfma_f32_16x16x32_bf16 v[176:179], a[76:79], a[108:111], v[176:179]\n"
"s_waitcnt lgkmcnt(8)\n"
"v_lshlrev_b32 v12, 16, v20\n"
"v_and_b32 v13, s63, v20\n"
"v_mfma_f32_16x16x32_bf16 v[148:151], a[68:71], a[108:111], v[148:151]\n"
"v_lshlrev_b32 v14, 16, v21\n"
"v_and_b32 v15, s63, v21\n"
"v_mfma_f32_16x16x32_bf16 v[148:151], a[64:67], a[104:107], v[148:151]\n"
"v_mul_f32 v12, v12, v100\n"
"v_mul_f32 v13, v13, v101\n"
"v_mfma_f32_16x16x32_bf16 v[152:155], a[64:67], a[112:115], v[152:155]\n"
"v_mul_f32 v14, v14, v102\n"
"v_mul_f32 v15, v15, v103\n"
"v_mfma_f32_16x16x32_bf16 v[152:155], a[68:71], a[116:119], v[152:155]\n"
"global_load_dwordx4 a[52:55], v0, s[34:35] offset:1024 sc1\n"
"v_cvt_pk_bf16_f32 v28, v12, v13\n"
"v_mfma_f32_16x16x32_bf16 v[180:183], a[76:79], a[116:119], v[180:183]\n"
"v_cvt_pk_bf16_f32 v29, v14, v15\n"
"v_lshlrev_b32 v12, 16, v22\n"
"v_mfma_f32_16x16x32_bf16 v[180:183], a[72:75], a[112:115], v[180:183]\n"
"v_and_b32 v13, s63, v22\n"
"v_lshlrev_b32 v14, 16, v23\n"
"v_mfma_f32_16x16x32_bf16 v[184:187], a[72:75], a[120:123], v[184:187]\n"
"v_and_b32 v15, s63, v23\n"
"v_mul_f32 v12, v12, v128\n"
"v_mfma_f32_16x16x32_bf16 v[184:187], a[76:79], a[124:127], v[184:187]\n"
"v_mul_f32 v13, v13, v129\n"
"v_mfma_f32_16x16x32_bf16 v[156:159], a[68:71], a[124:127], v[156:159]\n"
"v_mul_f32 v14, v14, v130\n"
"v_mfma_f32_16x16x32_bf16 v[156:159], a[64:67], a[120:123], v[156:159]\n"
"s_waitcnt lgkmcnt(6)\n"
"v_mul_f32 v15, v15, v131\n"
"v_cvt_pk_bf16_f32 v30, v12, v13\n"
"v_mfma_f32_16x16x32_bf16 v[160:163], a[64:67], a[128:131], v[160:163]\n"
"v_cvt_pk_bf16_f32 v31, v14, v15\n"
"v_mfma_f32_16x16x32_bf16 v[160:163], a[68:71], a[132:135], v[160:163]\n"
"global_load_dwordx4 a[56:59], v0, s[34:35] offset:2048 sc1\n"
"v_mfma_f32_16x16x32_bf16 v[188:191], a[76:79], a[132:135], v[188:191]\n"
"global_store_dwordx4 v0, v[28:31], s[52:53] offset:3072 sc1\n"
"ds_write_b128 v2, v[28:31] offset:7168\n"
"v_mfma_f32_16x16x32_bf16 v[188:191], a[72:75], a[128:131], v[188:191]\n"
"v_accvgpr_read_b32 v16, a160\n"
"v_accvgpr_read_b32 v17, a161\n"
"v_mfma_f32_16x16x32_bf16 v[192:195], a[72:75], a[136:139], v[192:195]\n"
"v_accvgpr_read_b32 v18, a162\n"
"v_mfma_f32_16x16x32_bf16 v[192:195], a[76:79], a[140:143], v[192:195]\n"
"v_accvgpr_read_b32 v19, a163\n"
"v_lshlrev_b32 v12, 16, v16\n"
"v_mfma_f32_16x16x32_bf16 v[164:167], a[68:71], a[140:143], v[164:167]\n"
"v_and_b32 v13, s63, v16\n"
"v_mfma_f32_16x16x32_bf16 v[164:167], a[64:67], a[136:139], v[164:167]\n"
"s_waitcnt lgkmcnt(2)\n"
"v_lshlrev_b32 v14, 16, v17\n"
"v_mfma_f32_16x16x32_bf16 v[168:171], a[64:67], a[144:147], v[168:171]\n"
"v_and_b32 v15, s63, v17\n"
"v_mul_f32 v12, v12, v48\n"
"v_mfma_f32_16x16x32_bf16 v[168:171], a[68:71], a[148:151], v[168:171]\n"
"global_load_dwordx4 a[60:63], v0, s[34:35] offset:3072 sc1\n"
"v_mul_f32 v13, v13, v49\n"
"v_mfma_f32_16x16x32_bf16 v[196:199], a[76:79], a[148:151], v[196:199]\n"
"v_mul_f32 v14, v14, v50\n"
"v_mfma_f32_16x16x32_bf16 v[196:199], a[72:75], a[144:147], v[196:199]\n"
"v_mul_f32 v15, v15, v51\n"
"v_cvt_pk_bf16_f32 v24, v12, v13\n"
"v_mfma_f32_16x16x32_bf16 a[20:23], a[72:75], a[152:155], a[20:23]\n"
"s_add_u32 s34, s34, 8192\n"
"v_cvt_pk_bf16_f32 v25, v14, v15\n"
"v_mfma_f32_16x16x32_bf16 a[20:23], a[76:79], a[156:159], a[20:23]\n"
"v_lshlrev_b32 v12, 16, v18\n"
"v_mfma_f32_16x16x32_bf16 a[16:19], a[68:71], a[156:159], a[16:19]\n"
"v_and_b32 v13, s63, v18\n"
"v_lshlrev_b32 v14, 16, v19\n"
"v_mfma_f32_16x16x32_bf16 a[16:19], a[64:67], a[152:155], a[16:19]\n"
"s_waitcnt vmcnt(11)\n"
"v_and_b32 v15, s63, v19\n"
"v_mul_f32 v12, v12, v76\n"
"v_mfma_f32_16x16x32_bf16 v[200:203], a[80:83], a[96:99], v[200:203]\n"
"v_mul_f32 v13, v13, v77\n"
"v_mul_f32 v14, v14, v78\n"
"v_mfma_f32_16x16x32_bf16 v[200:203], a[84:87], a[100:103], v[200:203]\n"
"global_load_dwordx4 a[64:67], v0, s[34:35] offset:-4096 sc1\n"
"v_mul_f32 v15, v15, v79\n"
"v_mfma_f32_16x16x32_bf16 v[228:231], a[92:95], a[100:103], v[228:231]\n"
"ds_read_b128 a[100:103], v3 offset:17408\n"
"v_cvt_pk_bf16_f32 v26, v12, v13\n"
"v_mfma_f32_16x16x32_bf16 v[228:231], a[88:91], a[96:99], v[228:231]\n"
"ds_read_b128 a[96:99], v3 offset:16384\n"
"v_cvt_pk_bf16_f32 v27, v14, v15\n"
"v_mfma_f32_16x16x32_bf16 v[232:235], a[88:91], a[104:107], v[232:235]\n"
"global_store_dwordx4 v0, v[24:27], s[54:55] offset:-4096 sc1\n"
"ds_write_b128 v2, v[24:27] offset:8192\n"
"v_mfma_f32_16x16x32_bf16 v[232:235], a[92:95], a[108:111], v[232:235]\n"
"v_accvgpr_read_b32 v16, a164\n"
"v_mfma_f32_16x16x32_bf16 v[204:207], a[84:87], a[108:111], v[204:207]\n"
"ds_read_b128 a[108:111], v3 offset:19456\n"
"v_accvgpr_read_b32 v17, a165\n"
"v_mfma_f32_16x16x32_bf16 v[204:207], a[80:83], a[104:107], v[204:207]\n"
"ds_read_b128 a[104:107], v3 offset:18432\n"
"v_accvgpr_read_b32 v18, a166\n"
"v_mfma_f32_16x16x32_bf16 v[208:211], a[80:83], a[112:115], v[208:211]\n"
"v_accvgpr_read_b32 v19, a167\n"
"v_lshlrev_b32 v12, 16, v16\n"
"v_mfma_f32_16x16x32_bf16 v[208:211], a[84:87], a[116:119], v[208:211]\n"
"global_load_dwordx4 a[68:71], v0, s[34:35] offset:-3072 sc1\n"
"v_and_b32 v13, s63, v16\n"
"v_mfma_f32_16x16x32_bf16 v[236:239], a[92:95], a[116:119], v[236:239]\n"
"ds_read_b128 a[116:119], v3 offset:21504\n"
"v_lshlrev_b32 v14, 16, v17\n"
"v_mfma_f32_16x16x32_bf16 v[236:239], a[88:91], a[112:115], v[236:239]\n"
"ds_read_b128 a[112:115], v3 offset:20480\n"
"v_and_b32 v15, s63, v17\n"
"v_mfma_f32_16x16x32_bf16 v[240:243], a[88:91], a[120:123], v[240:243]\n"
"v_mul_f32 v12, v12, v104\n"
"v_mul_f32 v13, v13, v105\n"
"v_mfma_f32_16x16x32_bf16 v[240:243], a[92:95], a[124:127], v[240:243]\n"
"v_mul_f32 v14, v14, v106\n"
"v_mul_f32 v15, v15, v107\n"
"v_mfma_f32_16x16x32_bf16 v[212:215], a[84:87], a[124:127], v[212:215]\n"
"ds_read_b128 a[124:127], v3 offset:23552\n"
"v_cvt_pk_bf16_f32 v28, v12, v13\n"
"v_mfma_f32_16x16x32_bf16 v[212:215], a[80:83], a[120:123], v[212:215]\n"
"ds_read_b128 a[120:123], v3 offset:22528\n"
"v_cvt_pk_bf16_f32 v29, v14, v15\n"
"v_mfma_f32_16x16x32_bf16 v[216:219], a[80:83], a[128:131], v[216:219]\n"
"L_st4488:\n"
"s_cbranch_execz L_retA\n"
"ds_write_b64 v2, v[28:29] offset:9216\n"
"v_mfma_f32_16x16x32_bf16 v[216:219], a[84:87], a[132:135], v[216:219]\n"
"global_load_dwordx4 a[72:75], v0, s[34:35] offset:-2048 sc1\n"
"v_lshlrev_b32 v12, 16, v18\n"
"v_mfma_f32_16x16x32_bf16 v[244:247], a[92:95], a[132:135], v[244:247]\n"
"ds_read_b128 a[132:135], v3 offset:25600\n"
"v_and_b32 v13, s63, v18\n"
"v_mfma_f32_16x16x32_bf16 v[244:247], a[88:91], a[128:131], v[244:247]\n"
"ds_read_b128 a[128:131], v3 offset:24576\n"
"v_lshlrev_b32 v14, 16, v19\n"
"v_mfma_f32_16x16x32_bf16 v[248:251], a[88:91], a[136:139], v[248:251]\n"
"v_and_b32 v15, s63, v19\n"
"v_mul_f32 v12, v12, v132\n"
"v_mfma_f32_16x16x32_bf16 v[248:251], a[92:95], a[140:143], v[248:251]\n"
"v_mul_f32 v13, v13, v133\n"
"v_mfma_f32_16x16x32_bf16 v[220:223], a[84:87], a[140:143], v[220:223]\n"
"ds_read_b128 a[140:143], v3 offset:27648\n"
"v_mul_f32 v14, v14, v134\n"
"v_mfma_f32_16x16x32_bf16 v[220:223], a[80:83], a[136:139], v[220:223]\n"
"ds_read_b128 a[136:139], v3 offset:26624\n"
"v_mul_f32 v15, v15, v135\n"
"v_mfma_f32_16x16x32_bf16 v[224:227], a[80:83], a[144:147], v[224:227]\n"
"v_cvt_pk_bf16_f32 v30, v12, v13\n"
"v_cvt_pk_bf16_f32 v31, v14, v15\n"
"v_mfma_f32_16x16x32_bf16 v[224:227], a[84:87], a[148:151], v[224:227]\n"
"global_load_dwordx4 a[76:79], v0, s[34:35] offset:-1024 sc1\n"
"v_mfma_f32_16x16x32_bf16 v[252:255], a[92:95], a[148:151], v[252:255]\n"
"ds_read_b128 a[148:151], v3 offset:29696\n"
"v_mfma_f32_16x16x32_bf16 v[252:255], a[88:91], a[144:147], v[252:255]\n"
"ds_read_b128 a[144:147], v3 offset:28672\n"
"v_mfma_f32_16x16x32_bf16 a[28:31], a[88:91], a[152:155], a[28:31]\n"
"ds_write_b64 v2, v[30:31] offset:9224\n"
"v_mfma_f32_16x16x32_bf16 a[28:31], a[92:95], a[156:159], a[28:31]\n"
"v_accvgpr_read_b32 v16, a168\n"
"v_accvgpr_read_b32 v17, a169\n"
"v_mfma_f32_16x16x32_bf16 a[24:27], a[84:87], a[156:159], a[24:27]\n"
"ds_read_b128 a[156:159], v3 offset:31744\n"
"v_accvgpr_read_b32 v18, a170\n"
"v_mfma_f32_16x16x32_bf16 a[24:27], a[80:83], a[152:155], a[24:27]\n"
"s_waitcnt vmcnt(10) lgkmcnt(13)\n"
"ds_read_b128 a[152:155], v3 offset:30720\n"
"v_accvgpr_read_b32 v19, a171\n"
"v_mfma_f32_16x16x32_bf16 v[144:147], a[32:35], a[96:99], v[144:147]\n"
"v_lshlrev_b32 v12, 16, v16\n"
"v_and_b32 v13, s63, v16\n"
"v_mfma_f32_16x16x32_bf16 v[144:147], a[36:39], a[100:103], v[144:147]\n"
"global_load_dwordx4 a[80:83], v0, s[34:35] offset:0 sc1\n"
"v_lshlrev_b32 v14, 16, v17\n"
"v_mfma_f32_16x16x32_bf16 v[172:175], a[44:47], a[100:103], v[172:175]\n"
"v_and_b32 v15, s63, v17\n"
"v_mul_f32 v12, v12, v52\n"
"v_mfma_f32_16x16x32_bf16 v[172:175], a[40:43], a[96:99], v[172:175]\n"
"v_mul_f32 v13, v13, v53\n"
"v_mul_f32 v14, v14, v54\n"
"v_mfma_f32_16x16x32_bf16 v[176:179], a[40:43], a[104:107], v[176:179]\n"
"v_mul_f32 v15, v15, v55\n"
"v_cvt_pk_bf16_f32 v24, v12, v13\n"
"v_mfma_f32_16x16x32_bf16 v[176:179], a[44:47], a[108:111], v[176:179]\n"
"v_cvt_pk_bf16_f32 v25, v14, v15\n"
"v_lshlrev_b32 v12, 16, v18\n"
"v_mfma_f32_16x16x32_bf16 v[148:151], a[36:39], a[108:111], v[148:151]\n"
"v_and_b32 v13, s63, v18\n"
"v_lshlrev_b32 v14, 16, v19\n"
"v_mfma_f32_16x16x32_bf16 v[148:151], a[32:35], a[104:107], v[148:151]\n"
"s_waitcnt lgkmcnt(10)\n"
"v_and_b32 v15, s63, v19\n"
"v_mul_f32 v12, v12, v80\n"
"v_mfma_f32_16x16x32_bf16 v[152:155], a[32:35], a[112:115], v[152:155]\n"
"v_mul_f32 v13, v13, v81\n"
"v_mul_f32 v14, v14, v82\n"
"v_mfma_f32_16x16x32_bf16 v[152:155], a[36:39], a[116:119], v[152:155]\n"
"global_load_dwordx4 a[84:87], v0, s[34:35] offset:1024 sc1\n"
"v_mul_f32 v15, v15, v83\n"
"v_mfma_f32_16x16x32_bf16 v[180:183], a[44:47], a[116:119], v[180:183]\n"
"v_cvt_pk_bf16_f32 v26, v12, v13\n"
"v_mfma_f32_16x16x32_bf16 v[180:183], a[40:43], a[112:115], v[180:183]\n"
"v_cvt_pk_bf16_f32 v27, v14, v15\n"
"v_mfma_f32_16x16x32_bf16 v[184:187], a[40:43], a[120:123], v[184:187]\n"
"global_store_dwordx4 v0, v[24:27], s[54:55] offset:-2048 sc1\n"
"ds_write_b128 v2, v[24:27] offset:10240\n"
"v_mfma_f32_16x16x32_bf16 v[184:187], a[44:47], a[124:127], v[184:187]\n"
"v_accvgpr_read_b32 v16, a172\n"
"v_mfma_f32_16x16x32_bf16 v[156:159], a[36:39], a[124:127], v[156:159]\n"
"v_accvgpr_read_b32 v17, a173\n"
"v_mfma_f32_16x16x32_bf16 v[156:159], a[32:35], a[120:123], v[156:159]\n"
"s_waitcnt lgkmcnt(6)\n"
"v_accvgpr_read_b32 v18, a174\n"
"v_accvgpr_read_b32 v19, a175\n"
"v_mfma_f32_16x16x32_bf16 v[160:163], a[32:35], a[128:131], v[160:163]\n"
"v_lshlrev_b32 v12, 16, v16\n"
"v_mfma_f32_16x16x32_bf16 v[160:163], a[36:39], a[132:135], v[160:163]\n"
"global_load_dwordx4 a[88:91], v0, s[34:35] offset:2048 sc1\n"
"v_and_b32 v13, s63, v16\n"
"v_mfma_f32_16x16x32_bf16 v[188:191], a[44:47], a[132:135], v[188:191]\n"
"v_lshlrev_b32 v14, 16, v17\n"
"v_and_b32 v15, s63, v17\n"
"v_mfma_f32_16x16x32_bf16 v[188:191], a[40:43], a[128:131], v[188:191]\n"
"v_mul_f32 v12, v12, v108\n"
"v_mfma_f32_16x16x32_bf16 v[192:195], a[40:43], a[136:139], v[192:195]\n"
"v_mul_f32 v13, v13, v109\n"
"v_mfma_f32_16x16x32_bf16 v[192:195], a[44:47], a[140:143], v[192:195]\n"
"v_mul_f32 v14, v14, v110\n"
"v_mul_f32 v15, v15, v111\n"
"v_mfma_f32_16x16x32_bf16 v[164:167], a[36:39], a[140:143], v[164:167]\n"
"v_cvt_pk_bf16_f32 v28, v12, v13\n"
"v_mfma_f32_16x16x32_bf16 v[164:167], a[32:35], a[136:139], v[164:167]\n"
"s_waitcnt lgkmcnt(1)\n"
"v_cvt_pk_bf16_f32 v29, v14, v15\n"
"v_mfma_f32_16x16x32_bf16 v[168:171], a[32:35], a[144:147], v[168:171]\n"
"v_lshlrev_b32 v12, 16, v18\n"
"v_and_b32 v13, s63, v18\n"
"v_mfma_f32_16x16x32_bf16 v[168:171], a[36:39], a[148:151], v[168:171]\n"
"global_load_dwordx4 a[92:95], v0, s[34:35] offset:3072 sc1\n"
"v_lshlrev_b32 v14, 16, v19\n"
"v_mfma_f32_16x16x32_bf16 v[196:199], a[44:47], a[148:151], v[196:199]\n"
"v_and_b32 v15, s63, v19\n"
"v_mfma_f32_16x16x32_bf16 v[196:199], a[40:43], a[144:147], v[196:199]\n"
"v_mul_f32 v12, v12, v136\n"
"v_mul_f32 v13, v13, v137\n"
"v_mfma_f32_16x16x32_bf16 a[20:23], a[40:43], a[152:155], a[20:23]\n"
"s_add_u32 s34, s34, 8192\n"
"v_mul_f32 v14, v14, v138\n"
"v_mfma_f32_16x16x32_bf16 a[20:23], a[44:47], a[156:159], a[20:23]\n"
"v_mul_f32 v15, v15, v139\n"
"v_mfma_f32_16x16x32_bf16 a[16:19], a[36:39], a[156:159], a[16:19]\n"
"v_cvt_pk_bf16_f32 v30, v12, v13\n"
"v_cvt_pk_bf16_f32 v31, v14, v15\n"
"v_mfma_f32_16x16x32_bf16 a[16:19], a[32:35], a[152:155], a[16:19]\n"
"s_waitcnt vmcnt(10)\n"
"global_store_dwordx4 v0, v[28:31], s[54:55] offset:-1024 sc1\n"
"ds_write_b128 v2, v[28:31] offset:11264\n"
"v_mfma_f32_16x16x32_bf16 v[200:203], a[48:51], a[96:99], v[200:203]\n"
"v_accvgpr_read_b32 v16, a176\n"
"v_mfma_f32_16x16x32_bf16 v[200:203], a[52:55], a[100:103], v[200:203]\n"
"global_load_dwordx4 a[32:35], v0, s[34:35] offset:-4096 sc1\n"
"v_accvgpr_read_b32 v17, a177\n"
"v_mfma_f32_16x16x32_bf16 v[228:231], a[60:63], a[100:103], v[228:231]\n"
"ds_read_b128 a[100:103], v2 offset:17408\n"
"v_accvgpr_read_b32 v18, a178\n"
"v_mfma_f32_16x16x32_bf16 v[228:231], a[56:59], a[96:99], v[228:231]\n"
"ds_read_b128 a[96:99], v2 offset:16384\n"
"v_accvgpr_read_b32 v19, a179\n"
"v_mfma_f32_16x16x32_bf16 v[232:235], a[56:59], a[104:107], v[232:235]\n"
"v_lshlrev_b32 v12, 16, v16\n"
"v_and_b32 v13, s63, v16\n"
"v_mfma_f32_16x16x32_bf16 v[232:235], a[60:63], a[108:111], v[232:235]\n"
"v_lshlrev_b32 v14, 16, v17\n"
"v_mfma_f32_16x16x32_bf16 v[204:207], a[52:55], a[108:111], v[204:207]\n"
"ds_read_b128 a[108:111], v2 offset:19456\n"
"v_and_b32 v15, s63, v17\n"
"v_mfma_f32_16x16x32_bf16 v[204:207], a[48:51], a[104:107], v[204:207]\n"
"ds_read_b128 a[104:107], v2 offset:18432\n"
"v_mul_f32 v12, v12, v56\n"
"v_mfma_f32_16x16x32_bf16 v[208:211], a[48:51], a[112:115], v[208:211]\n"
"v_mul_f32 v13, v13, v57\n"
"v_mul_f32 v14, v14, v58\n"
"v_mfma_f32_16x16x32_bf16 v[208:211], a[52:55], a[116:119], v[208:211]\n"
"global_load_dwordx4 a[36:39], v0, s[34:35] offset:-3072 sc1\n"
"v_mul_f32 v15, v15, v59\n"
"v_mfma_f32_16x16x32_bf16 v[236:239], a[60:63], a[116:119], v[236:239]\n"
"ds_read_b128 a[116:119], v2 offset:21504\n"
"v_cvt_pk_bf16_f32 v24, v12, v13\n"
"v_mfma_f32_16x16x32_bf16 v[236:239], a[56:59], a[112:115], v[236:239]\n"
"ds_read_b128 a[112:115], v2 offset:20480\n"
"v_cvt_pk_bf16_f32 v25, v14, v15\n"
"v_mfma_f32_16x16x32_bf16 v[240:243], a[56:59], a[120:123], v[240:243]\n"
"ds_write_b64 v2, v[24:25] offset:12288\n"
"v_mfma_f32_16x16x32_bf16 v[240:243], a[60:63], a[124:127], v[240:243]\n"
"v_lshlrev_b32 v12, 16, v18\n"
"v_and_b32 v13, s63, v18\n"
"v_mfma_f32_16x16x32_bf16 v[212:215], a[52:55], a[124:127], v[212:215]\n"
"ds_read_b128 a[124:127], v2 offset:23552\n"
"v_lshlrev_b32 v14, 16, v19\n"
"v_mfma_f32_16x16x32_bf16 v[212:215], a[48:51], a[120:123], v[212:215]\n"
"ds_read_b128 a[120:123], v2 offset:22528\n"
"v_and_b32 v15, s63, v19\n"
"v_mfma_f32_16x16x32_bf16 v[216:219], a[48:51], a[128:131], v[216:219]\n"
"v_mul_f32 v12, v12, v84\n"
"v_mul_f32 v13, v13, v85\n"
"v_mfma_f32_16x16x32_bf16 v[216:219], a[52:55], a[132:135], v[216:219]\n"
"global_load_dwordx4 a[40:43], v0, s[34:35] offset:-2048 sc1\n"
"v_mul_f32 v14, v14, v86\n"
"v_mfma_f32_16x16x32_bf16 v[244:247], a[60:63], a[132:135], v[244:247]\n"
"ds_read_b128 a[132:135], v2 offset:25600\n"
"v_mul_f32 v15, v15, v87\n"
"v_mfma_f32_16x16x32_bf16 v[244:247], a[56:59], a[128:131], v[244:247]\n"
"ds_read_b128 a[128:131], v2 offset:24576\n"
"v_cvt_pk_bf16_f32 v26, v12, v13\n"
"v_mfma_f32_16x16x32_bf16 v[248:251], a[56:59], a[136:139], v[248:251]\n"
"v_cvt_pk_bf16_f32 v27, v14, v15\n"
"v_mfma_f32_16x16x32_bf16 v[248:251], a[60:63], a[140:143], v[248:251]\n"
"ds_write_b64 v2, v[26:27] offset:12296\n"
"v_mfma_f32_16x16x32_bf16 v[220:223], a[52:55], a[140:143], v[220:223]\n"
"ds_read_b128 a[140:143], v2 offset:27648\n"
"v_accvgpr_read_b32 v16, a180\n"
"v_mfma_f32_16x16x32_bf16 v[220:223], a[48:51], a[136:139], v[220:223]\n"
"ds_read_b128 a[136:139], v2 offset:26624\n"
"v_accvgpr_read_b32 v17, a181\n"
"v_mfma_f32_16x16x32_bf16 v[224:227], a[48:51], a[144:147], v[224:227]\n"
"v_accvgpr_read_b32 v18, a182\n"
"v_accvgpr_read_b32 v19, a183\n"
"v_mfma_f32_16x16x32_bf16 v[224:227], a[52:55], a[148:151], v[224:227]\n"
"global_load_dwordx4 a[44:47], v0, s[34:35] offset:-1024 sc1\n"
"v_lshlrev_b32 v12, 16, v16\n"
"v_mfma_f32_16x16x32_bf16 v[252:255], a[60:63], a[148:151], v[252:255]\n"
"ds_read_b128 a[148:151], v2 offset:29696\n"
"v_and_b32 v13, s63, v16\n"
"v_mfma_f32_16x16x32_bf16 v[252:255], a[56:59], a[144:147], v[252:255]\n"
"ds_read_b128 a[144:147], v2 offset:28672\n"
"v_lshlrev_b32 v14, 16, v17\n"
"v_mfma_f32_16x16x32_bf16 a[28:31], a[56:59], a[152:155], a[28:31]\n"
"v_and_b32 v15, s63, v17\n"
"v_mul_f32 v12, v12, v112\n"
"v_mfma_f32_16x16x32_bf16 a[28:31], a[60:63], a[156:159], a[28:31]\n"
"v_mul_f32 v13, v13, v113\n"
"v_mul_f32 v14, v14, v114\n"
"v_mfma_f32_16x16x32_bf16 a[24:27], a[52:55], a[156:159], a[24:27]\n"
"ds_read_b128 a[156:159], v2 offset:31744\n"
"v_mul_f32 v15, v15, v115\n"
"v_mfma_f32_16x16x32_bf16 a[24:27], a[48:51], a[152:155], a[24:27]\n"
"s_waitcnt vmcnt(10) lgkmcnt(13)\n"
"ds_read_b128 a[152:155], v2 offset:30720\n"
"v_cvt_pk_bf16_f32 v28, v12, v13\n"
"v_mfma_f32_16x16x32_bf16 v[144:147], a[64:67], a[96:99], v[144:147]\n"
"v_cvt_pk_bf16_f32 v29, v14, v15\n"
"v_lshlrev_b32 v12, 16, v18\n"
"v_mfma_f32_16x16x32_bf16 v[144:147], a[68:71], a[100:103], v[144:147]\n"
"global_load_dwordx4 a[48:51], v0, s[34:35] offset:0 sc1\n"
"v_and_b32 v13, s63, v18\n"
"v_mfma_f32_16x16x32_bf16 v[172:175], a[76:79], a[100:103], v[172:175]\n"
"v_lshlrev_b32 v14, 16, v19\n"
"v_and_b32 v15, s63, v19\n"
"v_mfma_f32_16x16x32_bf16 v[172:175], a[72:75], a[96:99], v[172:175]\n"
"v_mul_f32 v12, v12, v140\n"
"v_mul_f32 v13, v13, v141\n"
"v_mfma_f32_16x16x32_bf16 v[176:179], a[72:75], a[104:107], v[176:179]\n"
"v_mul_f32 v14, v14, v142\n"
"v_mul_f32 v15, v15, v143\n"
"v_mfma_f32_16x16x32_bf16 v[176:179], a[76:79], a[108:111], v[176:179]\n"
"v_cvt_pk_bf16_f32 v30, v12, v13\n"
"v_mfma_f32_16x16x32_bf16 v[148:151], a[68:71], a[108:111], v[148:151]\n"
"v_cvt_pk_bf16_f32 v31, v14, v15\n"
"v_mfma_f32_16x16x32_bf16 v[148:151], a[64:67], a[104:107], v[148:151]\n"
"s_waitcnt lgkmcnt(9)\n"
"global_store_dwordx4 v0, v[28:31], s[54:55] offset:1024 sc1\n"
"ds_write_b128 v2, v[28:31] offset:13312\n"
"v_mfma_f32_16x16x32_bf16 v[152:155], a[64:67], a[112:115], v[152:155]\n"
"v_accvgpr_read_b32 v16, a184\n"
"v_accvgpr_read_b32 v17, a185\n"
"v_mfma_f32_16x16x32_bf16 v[152:155], a[68:71], a[116:119], v[152:155]\n"
"global_load_dwordx4 a[52:55], v0, s[34:35] offset:1024 sc1\n"
"v_accvgpr_read_b32 v18, a186\n"
"v_mfma_f32_16x16x32_bf16 v[180:183], a[76:79], a[116:119], v[180:183]\n"
"v_accvgpr_read_b32 v19, a187\n"
"v_mfma_f32_16x16x32_bf16 v[180:183], a[72:75], a[112:115], v[180:183]\n"
"v_lshlrev_b32 v12, 16, v16\n"
"v_mfma_f32_16x16x32_bf16 v[184:187], a[72:75], a[120:123], v[184:187]\n"
"v_and_b32 v13, s63, v16\n"
"v_lshlrev_b32 v14, 16, v17\n"
"v_mfma_f32_16x16x32_bf16 v[184:187], a[76:79], a[124:127], v[184:187]\n"
"v_and_b32 v15, s63, v17\n"
"v_mfma_f32_16x16x32_bf16 v[156:159], a[68:71], a[124:127], v[156:159]\n"
"v_accvgpr_read_b32 v10, a0\n"
"v_mfma_f32_16x16x32_bf16 v[156:159], a[64:67], a[120:123], v[156:159]\n"
"s_waitcnt lgkmcnt(5)\n"
"v_accvgpr_read_b32 v11, a1\n"
"v_mul_f32 v12, v12, v10\n"
"v_mfma_f32_16x16x32_bf16 v[160:163], a[64:67], a[128:131], v[160:163]\n"
"v_mul_f32 v13, v13, v11\n"
"v_mfma_f32_16x16x32_bf16 v[160:163], a[68:71], a[132:135], v[160:163]\n"
"global_load_dwordx4 a[56:59], v0, s[34:35] offset:2048 sc1\n"
"v_accvgpr_read_b32 v10, a2\n"
"v_mfma_f32_16x16x32_bf16 v[188:191], a[76:79], a[132:135], v[188:191]\n"
"v_accvgpr_read_b32 v11, a3\n"
"v_mul_f32 v14, v14, v10\n"
"v_mfma_f32_16x16x32_bf16 v[188:191], a[72:75], a[128:131], v[188:191]\n"
"v_mul_f32 v15, v15, v11\n"
"v_mfma_f32_16x16x32_bf16 v[192:195], a[72:75], a[136:139], v[192:195]\n"
"v_cvt_pk_bf16_f32 v24, v12, v13\n"
"v_mfma_f32_16x16x32_bf16 v[192:195], a[76:79], a[140:143], v[192:195]\n"
"v_cvt_pk_bf16_f32 v25, v14, v15\n"
"v_lshlrev_b32 v12, 16, v18\n"
"v_mfma_f32_16x16x32_bf16 v[164:167], a[68:71], a[140:143], v[164:167]\n"
"v_and_b32 v13, s63, v18\n"
"v_mfma_f32_16x16x32_bf16 v[164:167], a[64:67], a[136:139], v[164:167]\n"
"s_waitcnt lgkmcnt(1)\n"
"v_lshlrev_b32 v14, 16, v19\n"
"v_mfma_f32_16x16x32_bf16 v[168:171], a[64:67], a[144:147], v[168:171]\n"
"v_and_b32 v15, s63, v19\n"
"v_accvgpr_read_b32 v10, a4\n"
"v_mfma_f32_16x16x32_bf16 v[168:171], a[68:71], a[148:151], v[168:171]\n"
"global_load_dwordx4 a[60:63], v0, s[34:35] offset:3072 sc1\n"
"v_accvgpr_read_b32 v11, a5\n"
"v_mfma_f32_16x16x32_bf16 v[196:199], a[76:79], a[148:151], v[196:199]\n"
"v_mul_f32 v12, v12, v10\n"
"v_mfma_f32_16x16x32_bf16 v[196:199], a[72:75], a[144:147], v[196:199]\n"
"v_mul_f32 v13, v13, v11\n"
"v_accvgpr_read_b32 v10, a6\n"
"v_mfma_f32_16x16x32_bf16 a[20:23], a[72:75], a[152:155], a[20:23]\n"
"s_add_u32 s34, s34, 8192\n"
"v_accvgpr_read_b32 v11, a7\n"
"v_mfma_f32_16x16x32_bf16 a[20:23], a[76:79], a[156:159], a[20:23]\n"
"v_mul_f32 v14, v14, v10\n"
"v_mul_f32 v15, v15, v11\n"
"v_mfma_f32_16x16x32_bf16 a[16:19], a[68:71], a[156:159], a[16:19]\n"
"v_cvt_pk_bf16_f32 v26, v12, v13\n"
"v_cvt_pk_bf16_f32 v27, v14, v15\n"
"v_mfma_f32_16x16x32_bf16 a[16:19], a[64:67], a[152:155], a[16:19]\n"
"s_waitcnt vmcnt(10)\n"
"global_store_dwordx4 v0, v[24:27], s[54:55] offset:2048 sc1\n"
"ds_write_b128 v2, v[24:27] offset:14336\n"
"v_mfma_f32_16x16x32_bf16 v[200:203], a[80:83], a[96:99], v[200:203]\n"
"v_accvgpr_read_b32 v16, a188\n"
"v_mfma_f32_16x16x32_bf16 v[200:203], a[84:87], a[100:103], v[200:203]\n"
"global_load_dwordx4 a[64:67], v0, s[34:35] offset:-4096 sc1\n"
"v_accvgpr_read_b32 v17, a189\n"
"v_mfma_f32_16x16x32_bf16 v[228:231], a[92:95], a[100:103], v[228:231]\n"
"ds_read_b128 a[100:103], v2 offset:1024\n"
"v_accvgpr_read_b32 v18, a190\n"
"v_mfma_f32_16x16x32_bf16 v[228:231], a[88:91], a[96:99], v[228:231]\n"
"ds_read_b128 a[96:99], v2 offset:0\n"
"v_accvgpr_read_b32 v19, a191\n"
"v_mfma_f32_16x16x32_bf16 v[232:235], a[88:91], a[104:107], v[232:235]\n"
"v_lshlrev_b32 v12, 16, v16\n"
"v_and_b32 v13, s63, v16\n"
"v_mfma_f32_16x16x32_bf16 v[232:235], a[92:95], a[108:111], v[232:235]\n"
"v_lshlrev_b32 v14, 16, v17\n"
"v_and_b32 v15, s63, v17\n"
"v_mfma_f32_16x16x32_bf16 v[204:207], a[84:87], a[108:111], v[204:207]\n"
"ds_read_b128 a[108:111], v2 offset:3072\n"
"v_accvgpr_read_b32 v10, a8\n"
"v_mfma_f32_16x16x32_bf16 v[204:207], a[80:83], a[104:107], v[204:207]\n"
"ds_read_b128 a[104:107], v2 offset:2048\n"
"v_accvgpr_read_b32 v11, a9\n"
"v_mfma_f32_16x16x32_bf16 v[208:211], a[80:83], a[112:115], v[208:211]\n"
"v_mul_f32 v12, v12, v10\n"
"v_mul_f32 v13, v13, v11\n"
"v_mfma_f32_16x16x32_bf16 v[208:211], a[84:87], a[116:119], v[208:211]\n"
"global_load_dwordx4 a[68:71], v0, s[34:35] offset:-3072 sc1\n"
"v_accvgpr_read_b32 v10, a10\n"
"v_mfma_f32_16x16x32_bf16 v[236:239], a[92:95], a[116:119], v[236:239]\n"
"ds_read_b128 a[116:119], v2 offset:5120\n"
"v_accvgpr_read_b32 v11, a11\n"
"v_mfma_f32_16x16x32_bf16 v[236:239], a[88:91], a[112:115], v[236:239]\n"
"ds_read_b128 a[112:115], v2 offset:4096\n"
"v_mul_f32 v14, v14, v10\n"
"v_mfma_f32_16x16x32_bf16 v[240:243], a[88:91], a[120:123], v[240:243]\n"
"v_mul_f32 v15, v15, v11\n"
"v_cvt_pk_bf16_f32 v28, v12, v13\n"
"v_mfma_f32_16x16x32_bf16 v[240:243], a[92:95], a[124:127], v[240:243]\n"
"v_cvt_pk_bf16_f32 v29, v14, v15\n"
"v_lshlrev_b32 v12, 16, v18\n"
"v_mfma_f32_16x16x32_bf16 v[212:215], a[84:87], a[124:127], v[212:215]\n"
"ds_read_b128 a[124:127], v2 offset:7168\n"
"v_and_b32 v13, s63, v18\n"
"v_mfma_f32_16x16x32_bf16 v[212:215], a[80:83], a[120:123], v[212:215]\n"
"ds_read_b128 a[120:123], v2 offset:6144\n"
"v_lshlrev_b32 v14, 16, v19\n"
"v_mfma_f32_16x16x32_bf16 v[216:219], a[80:83], a[128:131], v[216:219]\n"
"v_and_b32 v15, s63, v19\n"
"v_accvgpr_read_b32 v10, a12\n"
"v_mfma_f32_16x16x32_bf16 v[216:219], a[84:87], a[132:135], v[216:219]\n"
"global_load_dwordx4 a[72:75], v0, s[34:35] offset:-2048 sc1\n"
"v_accvgpr_read_b32 v11, a13\n"
"v_mfma_f32_16x16x32_bf16 v[244:247], a[92:95], a[132:135], v[244:247]\n"
"ds_read_b128 a[132:135], v2 offset:9216\n"
"v_mul_f32 v12, v12, v10\n"
"v_mfma_f32_16x16x32_bf16 v[244:247], a[88:91], a[128:131], v[244:247]\n"
"ds_read_b128 a[128:131], v2 offset:8192\n"
"v_mul_f32 v13, v13, v11\n"
"v_mfma_f32_16x16x32_bf16 v[248:251], a[88:91], a[136:139], v[248:251]\n"
"v_accvgpr_read_b32 v10, a14\n"
"v_accvgpr_read_b32 v11, a15\n"
"v_mfma_f32_16x16x32_bf16 v[248:251], a[92:95], a[140:143], v[248:251]\n"
"v_mul_f32 v14, v14, v10\n"
"v_mul_f32 v15, v15, v11\n"
"v_mfma_f32_16x16x32_bf16 v[220:223], a[84:87], a[140:143], v[220:223]\n"
"ds_read_b128 a[140:143], v2 offset:11264\n"
"v_cvt_pk_bf16_f32 v30, v12, v13\n"
"v_mfma_f32_16x16x32_bf16 v[220:223], a[80:83], a[136:139], v[220:223]\n"
"ds_read_b128 a[136:139], v2 offset:10240\n"
"v_cvt_pk_bf16_f32 v31, v14, v15\n"
"v_mfma_f32_16x16x32_bf16 v[224:227], a[80:83], a[144:147], v[224:227]\n"
"global_store_dwordx4 v0, v[28:31], s[54:55] offset:3072 sc1\n"
"ds_write_b128 v2, v[28:31] offset:15360\n"
"v_mfma_f32_16x16x32_bf16 v[224:227], a[84:87], a[148:151], v[224:227]\n"
"global_load_dwordx4 a[76:79], v0, s[34:35] offset:-1024 sc1\n"
"v_mfma_f32_16x16x32_bf16 v[252:255], a[92:95], a[148:151], v[252:255]\n"
"ds_read_b128 a[148:151], v2 offset:13312\n"
"v_mfma_f32_16x16x32_bf16 v[252:255], a[88:91], a[144:147], v[252:255]\n"
"ds_read_b128 a[144:147], v2 offset:12288\n"
"v_mfma_f32_16x16x32_bf16 a[28:31], a[88:91], a[152:155], a[28:31]\n"
"s_add_u32 s62, s62, 1\n"
"v_mfma_f32_16x16x32_bf16 a[28:31], a[92:95], a[156:159], a[28:31]\n"
"v_mfma_f32_16x16x32_bf16 a[24:27], a[84:87], a[156:159], a[24:27]\n"
"ds_read_b128 a[156:159], v2 offset:15360\n"
"v_mfma_f32_16x16x32_bf16 a[24:27], a[80:83], a[152:155], a[24:27]\n"
"ds_read_b128 a[152:155], v2 offset:14336\n"
"L_layer:\n"
"s_waitcnt vmcnt(11) lgkmcnt(13)\n"
"v_mfma_f32_16x16x32_bf16 v[32:35], a[32:35], a[96:99], v[32:35]\n"
"s_mov_b64 s[96:97], s[52:53]\n"
"v_accvgpr_read_b32 v16, a192\n"
"v_accvgpr_read_b32 v17, a193\n"
"v_mfma_f32_16x16x32_bf16 v[32:35], a[36:39], a[100:103], v[32:35]\n"
"global_load_dwordx4 a[80:83], v0, s[34:35] offset:0 sc1\n"
"v_accvgpr_read_b32 v18, a194\n"
"v_mfma_f32_16x16x32_bf16 v[60:63], a[44:47], a[100:103], v[60:63]\n"
"s_mov_b64 s[98:99], s[54:55]\n"
"v_accvgpr_read_b32 v19, a195\n"
"v_mfma_f32_16x16x32_bf16 v[60:63], a[40:43], a[96:99], v[60:63]\n"
"v_lshlrev_b32 v12, 16, v16\n"
"v_and_b32 v13, s63, v16\n"
"v_mfma_f32_16x16x32_bf16 v[64:67], a[40:43], a[104:107], v[64:67]\n"
"s_add_u32 s52, s52, 131072\n"
"s_addc_u32 s53, s53, 0\n"
"v_lshlrev_b32 v14, 16, v17\n"
"v_mfma_f32_16x16x32_bf16 v[64:67], a[44:47], a[108:111], v[64:67]\n"
"v_and_b32 v15, s63, v17\n"
"v_mul_f32 v12, v12, v144\n"
"v_mfma_f32_16x16x32_bf16 v[36:39], a[36:39], a[108:111], v[36:39]\n"
"s_add_u32 s54, s54, 131072\n"
"s_addc_u32 s55, s55, 0\n"
"v_mul_f32 v13, v13, v145\n"
"v_mfma_f32_16x16x32_bf16 v[36:39], a[32:35], a[104:107], v[36:39]\n"
"s_waitcnt lgkmcnt(9)\n"
"v_mul_f32 v14, v14, v146\n"
"v_mul_f32 v15, v15, v147\n"
"v_mfma_f32_16x16x32_bf16 v[40:43], a[32:35], a[112:115], v[40:43]\n"
"ds_read_b128 v[20:23], v2 offset:0\n"
"v_cvt_pk_bf16_f32 v24, v12, v13\n"
"v_mfma_f32_16x16x32_bf16 v[40:43], a[36:39], a[116:119], v[40:43]\n"
"global_load_dwordx4 a[84:87], v0, s[34:35] offset:1024 sc1\n"
"v_cvt_pk_bf16_f32 v25, v14, v15\n"
"v_mfma_f32_16x16x32_bf16 v[68:71], a[44:47], a[116:119], v[68:71]\n"
"ds_write_b64 v2, v[24:25] offset:16384\n"
"v_mfma_f32_16x16x32_bf16 v[68:71], a[40:43], a[112:115], v[68:71]\n"
"v_lshlrev_b32 v12, 16, v18\n"
"v_and_b32 v13, s63, v18\n"
"v_mfma_f32_16x16x32_bf16 v[72:75], a[40:43], a[120:123], v[72:75]\n"
"s_waitcnt lgkmcnt(6)\n"
"s_barrier\n"
"v_mfma_f32_16x16x32_bf16 v[72:75], a[44:47], a[124:127], v[72:75]\n"
"v_lshlrev_b32 v14, 16, v19\n"
"v_and_b32 v15, s63, v19\n"
"v_mfma_f32_16x16x32_bf16 v[44:47], a[36:39], a[124:127], v[44:47]\n"
"v_mul_f32 v12, v12, v172\n"
"v_mul_f32 v13, v13, v173\n"
"v_mfma_f32_16x16x32_bf16 v[44:47], a[32:35], a[120:123], v[44:47]\n"
"v_mul_f32 v14, v14, v174\n"
"v_mfma_f32_16x16x32_bf16 v[48:51], a[32:35], a[128:131], v[48:51]\n"
"v_mul_f32 v15, v15, v175\n"
"v_mfma_f32_16x16x32_bf16 v[48:51], a[36:39], a[132:135], v[48:51]\n"
"global_load_dwordx4 a[88:91], v0, s[34:35] offset:2048 sc1\n"
"v_mfma_f32_16x16x32_bf16 v[76:79], a[44:47], a[132:135], v[76:79]\n"
"v_cvt_pk_bf16_f32 v26, v12, v13\n"
"v_mfma_f32_16x16x32_bf16 v[76:79], a[40:43], a[128:131], v[76:79]\n"
"v_cvt_pk_bf16_f32 v27, v14, v15\n"
"v_mfma_f32_16x16x32_bf16 v[80:83], a[40:43], a[136:139], v[80:83]\n"
"s_waitcnt lgkmcnt(1)\n"
"global_store_dwordx4 v0, v[20:23], s[96:97] offset:-4096 sc1\n"
"ds_write_b64 v2, v[26:27] offset:16392\n"
"v_accvgpr_read_b32 v16, a196\n"
"v_mfma_f32_16x16x32_bf16 v[80:83], a[44:47], a[140:143], v[80:83]\n"
"v_accvgpr_read_b32 v17, a197\n"
"v_accvgpr_read_b32 v18, a198\n"
"v_mfma_f32_16x16x32_bf16 v[52:55], a[36:39], a[140:143], v[52:55]\n"
"v_accvgpr_read_b32 v19, a199\n"
"v_lshlrev_b32 v12, 16, v16\n"
"v_mfma_f32_16x16x32_bf16 v[52:55], a[32:35], a[136:139], v[52:55]\n"
"v_and_b32 v13, s63, v16\n"
"v_lshlrev_b32 v14, 16, v17\n"
"v_mfma_f32_16x16x32_bf16 v[56:59], a[32:35], a[144:147], v[56:59]\n"
"v_and_b32 v15, s63, v17\n"
"v_mul_f32 v12, v12, v200\n"
"v_mfma_f32_16x16x32_bf16 v[56:59], a[36:39], a[148:151], v[56:59]\n"
"global_load_dwordx4 a[92:95], v0, s[34:35] offset:3072 sc1\n"
"v_mul_f32 v13, v13, v201\n"
"v_mfma_f32_16x16x32_bf16 v[84:87], a[44:47], a[148:151], v[84:87]\n"
"v_mul_f32 v14, v14, v202\n"
"v_mul_f32 v15, v15, v203\n"
"v_mfma_f32_16x16x32_bf16 v[84:87], a[40:43], a[144:147], v[84:87]\n"
"v_cvt_pk_bf16_f32 v28, v12, v13\n"
"v_cvt_pk_bf16_f32 v29, v14, v15\n"
"v_mfma_f32_16x16x32_bf16 a[4:7], a[40:43], a[152:155], a[4:7]\n"
"s_add_u32 s34, s34, 8192\n"
"ds_write_b64 v2, v[28:29] offset:17408\n"
"v_mfma_f32_16x16x32_bf16 a[4:7], a[44:47], a[156:159], a[4:7]\n"
"v_lshlrev_b32 v12, 16, v18\n"
"v_mfma_f32_16x16x32_bf16 a[0:3], a[36:39], a[156:159], a[0:3]\n"
"v_and_b32 v13, s63, v18\n"
"v_lshlrev_b32 v14, 16, v19\n"
"v_mfma_f32_16x16x32_bf16 a[0:3], a[32:35], a[152:155], a[0:3]\n"
"s_waitcnt vmcnt(11)\n"
"v_and_b32 v15, s63, v19\n"
"v_mul_f32 v12, v12, v228\n"
"v_mfma_f32_16x16x32_bf16 v[88:91], a[48:51], a[96:99], v[88:91]\n"
"v_mul_f32 v13, v13, v229\n"
"v_mul_f32 v14, v14, v230\n"
"v_mfma_f32_16x16x32_bf16 v[88:91], a[52:55], a[100:103], v[88:91]\n"
"global_load_dwordx4 a[32:35], v0, s[34:35] offset:-4096 sc1\n"
"v_mul_f32 v15, v15, v231\n"
"v_mfma_f32_16x16x32_bf16 v[116:119], a[60:63], a[100:103], v[116:119]\n"
"ds_read_b128 a[100:103], v3 offset:1024\n"
"v_cvt_pk_bf16_f32 v30, v12, v13\n"
"v_mfma_f32_16x16x32_bf16 v[116:119], a[56:59], a[96:99], v[116:119]\n"
"ds_read_b128 a[96:99], v3 offset:0\n"
"v_cvt_pk_bf16_f32 v31, v14, v15\n"
"v_mfma_f32_16x16x32_bf16 v[120:123], a[56:59], a[104:107], v[120:123]\n"
"ds_write_b64 v2, v[30:31] offset:17416\n"
"v_mfma_f32_16x16x32_bf16 v[120:123], a[60:63], a[108:111], v[120:123]\n"
"ds_read_b128 v[20:23], v2 offset:3072\n"
"v_accvgpr_read_b32 v16, a200\n"
"v_mfma_f32_16x16x32_bf16 v[92:95], a[52:55], a[108:111], v[92:95]\n"
"ds_read_b128 a[108:111], v3 offset:3072\n"
"v_accvgpr_read_b32 v17, a201\n"
"v_mfma_f32_16x16x32_bf16 v[92:95], a[48:51], a[104:107], v[92:95]\n"
"ds_read_b128 a[104:107], v3 offset:2048\n"
"v_accvgpr_read_b32 v18, a202\n"
"v_mfma_f32_16x16x32_bf16 v[96:99], a[48:51], a[112:115], v[96:99]\n"
"v_accvgpr_read_b32 v19, a203\n"
"v_lshlrev_b32 v12, 16, v16\n"
"v_mfma_f32_16x16x32_bf16 v[96:99], a[52:55], a[116:119], v[96:99]\n"
"global_load_dwordx4 a[36:39], v0, s[34:35] offset:-3072 sc1\n"
"v_and_b32 v13, s63, v16\n"
"v_mfma_f32_16x16x32_bf16 v[124:127], a[60:63], a[116:119], v[124:127]\n"
"ds_read_b128 a[116:119], v3 offset:5120\n"
"v_lshlrev_b32 v14, 16, v17\n"
"v_mfma_f32_16x16x32_bf16 v[124:127], a[56:59], a[112:115], v[124:127]\n"
"ds_read_b128 a[112:115], v3 offset:4096\n"
"v_and_b32 v15, s63, v17\n"
"v_mfma_f32_16x16x32_bf16 v[128:131], a[56:59], a[120:123], v[128:131]\n"
"v_mul_f32 v12, v12, v148\n"
"v_mul_f32 v13, v13, v149\n"
"v_mfma_f32_16x16x32_bf16 v[128:131], a[60:63], a[124:127], v[128:131]\n"
"v_mul_f32 v14, v14, v150\n"
"v_mul_f32 v15, v15, v151\n"
"v_mfma_f32_16x16x32_bf16 v[100:103], a[52:55], a[124:127], v[100:103]\n"
"ds_read_b128 a[124:127], v3 offset:7168\n"
"v_cvt_pk_bf16_f32 v24, v12, v13\n"
"v_mfma_f32_16x16x32_bf16 v[100:103], a[48:51], a[120:123], v[100:103]\n"
"ds_read_b128 a[120:123], v3 offset:6144\n"
"v_cvt_pk_bf16_f32 v25, v14, v15\n"
"v_mfma_f32_16x16x32_bf16 v[104:107], a[48:51], a[128:131], v[104:107]\n"
"s_waitcnt lgkmcnt(6)\n"
"global_store_dwordx4 v0, v[20:23], s[96:97] offset:-1024 sc1\n"
"ds_write_b64 v2, v[24:25] offset:18432\n"
"v_lshlrev_b32 v12, 16, v18\n"
"v_mfma_f32_16x16x32_bf16 v[104:107], a[52:55], a[132:135], v[104:107]\n"
"global_load_dwordx4 a[40:43], v0, s[34:35] offset:-2048 sc1\n"
"v_and_b32 v13, s63, v18\n"
"v_mfma_f32_16x16x32_bf16 v[132:135], a[60:63], a[132:135], v[132:135]\n"
"ds_read_b128 a[132:135], v3 offset:9216\n"
"v_lshlrev_b32 v14, 16, v19\n"
"v_mfma_f32_16x16x32_bf16 v[132:135], a[56:59], a[128:131], v[132:135]\n"
"ds_read_b128 a[128:131], v3 offset:8192\n"
"v_and_b32 v15, s63, v19\n"
"v_mfma_f32_16x16x32_bf16 v[136:139], a[56:59], a[136:139], v[136:139]\n"
"v_mul_f32 v12, v12, v176\n"
"v_mul_f32 v13, v13, v177\n"
"v_mfma_f32_16x16x32_bf16 v[136:139], a[60:63], a[140:143], v[136:139]\n"
"v_mul_f32 v14, v14, v178\n"
"v_mul_f32 v15, v15, v179\n"
"v_mfma_f32_16x16x32_bf16 v[108:111], a[52:55], a[140:143], v[108:111]\n"
"ds_read_b128 a[140:143], v3 offset:11264\n"
"v_cvt_pk_bf16_f32 v26, v12, v13\n"
"v_mfma_f32_16x16x32_bf16 v[108:111], a[48:51], a[136:139], v[108:111]\n"
"ds_read_b128 a[136:139], v3 offset:10240\n"
"v_cvt_pk_bf16_f32 v27, v14, v15\n"
"v_mfma_f32_16x16x32_bf16 v[112:115], a[48:51], a[144:147], v[112:115]\n"
"ds_write_b64 v2, v[26:27] offset:18440\n"
"v_mfma_f32_16x16x32_bf16 v[112:115], a[52:55], a[148:151], v[112:115]\n"
"global_load_dwordx4 a[44:47], v0, s[34:35] offset:-1024 sc1\n"
"v_accvgpr_read_b32 v16, a204\n"
"v_mfma_f32_16x16x32_bf16 v[140:143], a[60:63], a[148:151], v[140:143]\n"
"ds_read_b128 a[148:151], v3 offset:13312\n"
"v_accvgpr_read_b32 v17, a205\n"
"v_mfma_f32_16x16x32_bf16 v[140:143], a[56:59], a[144:147], v[140:143]\n"
"ds_read_b128 a[144:147], v3 offset:12288\n"
"v_accvgpr_read_b32 v18, a206\n"
"v_mfma_f32_16x16x32_bf16 a[12:15], a[56:59], a[152:155], a[12:15]\n"
"v_accvgpr_read_b32 v19, a207\n"
"v_lshlrev_b32 v12, 16, v16\n"
"v_mfma_f32_16x16x32_bf16 a[12:15], a[60:63], a[156:159], a[12:15]\n"
"v_and_b32 v13, s63, v16\n"
"v_lshlrev_b32 v14, 16, v17\n"
"v_mfma_f32_16x16x32_bf16 a[8:11], a[52:55], a[156:159], a[8:11]\n"
"ds_read_b128 a[156:159], v3 offset:15360\n"
"v_and_b32 v15, s63, v17\n"
"v_mfma_f32_16x16x32_bf16 a[8:11], a[48:51], a[152:155], a[8:11]\n"
"s_waitcnt vmcnt(10) lgkmcnt(13)\n"
"ds_read_b128 a[152:155], v3 offset:14336\n"
"v_mul_f32 v12, v12, v204\n"
"v_mfma_f32_16x16x32_bf16 v[32:35], a[64:67], a[96:99], v[32:35]\n"
"ds_read_b128 v[20:23], v2 offset:6144\n"
"v_mul_f32 v13, v13, v205\n"
"v_mfma_f32_16x16x32_bf16 v[32:35], a[68:71], a[100:103], v[32:35]\n"
"global_load_dwordx4 a[48:51], v0, s[34:35] offset:0 sc1\n"
"v_mul_f32 v14, v14, v206\n"
"v_mfma_f32_16x16x32_bf16 v[60:63], a[76:79], a[100:103], v[60:63]\n"
"v_mul_f32 v15, v15, v207\n"
"v_cvt_pk_bf16_f32 v28, v12, v13\n"
"v_mfma_f32_16x16x32_bf16 v[60:63], a[72:75], a[96:99], v[60:63]\n"
"L_st5091:\n"
"s_cbranch_execz L_st7374\n"
"v_cvt_pk_bf16_f32 v29, v14, v15\n"
"v_mfma_f32_16x16x32_bf16 v[64:67], a[72:75], a[104:107], v[64:67]\n"
"ds_write_b64 v2, v[28:29] offset:19456\n"
"v_mfma_f32_16x16x32_bf16 v[64:67], a[76:79], a[108:111], v[64:67]\n"
"v_lshlrev_b32 v12, 16, v18\n"
"v_and_b32 v13, s63, v18\n"
"v_mfma_f32_16x16x32_bf16 v[36:39], a[68:71], a[108:111], v[36:39]\n"
"v_lshlrev_b32 v14, 16, v19\n"
"v_and_b32 v15, s63, v19\n"
"v_mfma_f32_16x16x32_bf16 v[36:39], a[64:67], a[104:107], v[36:39]\n"
"s_waitcnt lgkmcnt(12)\n"
"v_mul_f32 v12, v12, v232\n"
"v_mul_f32 v13, v13, v233\n"
"v_mfma_f32_16x16x32_bf16 v[40:43], a[64:67], a[112:115], v[40:43]\n"
"v_mul_f32 v14, v14, v234\n"
"v_mfma_f32_16x16x32_bf16 v[40:43], a[68:71], a[116:119], v[40:43]\n"
"global_load_dwordx4 a[52:55], v0, s[34:35] offset:1024 sc1\n"
"v_mul_f32 v15, v15, v235\n"
"v_mfma_f32_16x16x32_bf16 v[68:71], a[76:79], a[116:119], v[68:71]\n"
"v_cvt_pk_bf16_f32 v30, v12, v13\n"
"v_mfma_f32_16x16x32_bf16 v[68:71], a[72:75], a[112:115], v[68:71]\n"
"v_cvt_pk_bf16_f32 v31, v14, v15\n"
"v_mfma_f32_16x16x32_bf16 v[72:75], a[72:75], a[120:123], v[72:75]\n"
"s_waitcnt lgkmcnt(1)\n"
"global_store_dwordx4 v0, v[20:23], s[96:97] offset:2048 sc1\n"
"ds_write_b64 v2, v[30:31] offset:19464\n"
"v_accvgpr_read_b32 v16, a208\n"
"v_mfma_f32_16x16x32_bf16 v[72:75], a[76:79], a[124:127], v[72:75]\n"
"v_accvgpr_read_b32 v17, a209\n"
"v_accvgpr_read_b32 v18, a210\n"
"v_mfma_f32_16x16x32_bf16 v[44:47], a[68:71], a[124:127], v[44:47]\n"
"v_accvgpr_read_b32 v19, a211\n"
"v_mfma_f32_16x16x32_bf16 v[44:47], a[64:67], a[120:123], v[44:47]\n"
"v_lshlrev_b32 v12, 16, v16\n"
"v_and_b32 v13, s63, v16\n"
"v_mfma_f32_16x16x32_bf16 v[48:51], a[64:67], a[128:131], v[48:51]\n"
"v_lshlrev_b32 v14, 16, v17\n"
"v_mfma_f32_16x16x32_bf16 v[48:51], a[68:71], a[132:135], v[48:51]\n"
"global_load_dwordx4 a[56:59], v0, s[34:35] offset:2048 sc1\n"
"v_and_b32 v15, s63, v17\n"
"v_mfma_f32_16x16x32_bf16 v[76:79], a[76:79], a[132:135], v[76:79]\n"
"v_mul_f32 v12, v12, v152\n"
"v_mul_f32 v13, v13, v153\n"
"v_mfma_f32_16x16x32_bf16 v[76:79], a[72:75], a[128:131], v[76:79]\n"
"v_mul_f32 v14, v14, v154\n"
"v_mul_f32 v15, v15, v155\n"
"v_mfma_f32_16x16x32_bf16 v[80:83], a[72:75], a[136:139], v[80:83]\n"
"v_cvt_pk_bf16_f32 v24, v12, v13\n"
"v_cvt_pk_bf16_f32 v25, v14, v15\n"
"v_mfma_f32_16x16x32_bf16 v[80:83], a[76:79], a[140:143], v[80:83]\n"
"ds_write_b64 v2, v[24:25] offset:20480\n"
"v_mfma_f32_16x16x32_bf16 v[52:55], a[68:71], a[140:143], v[52:55]\n"
"v_lshlrev_b32 v12, 16, v18\n"
"v_mfma_f32_16x16x32_bf16 v[52:55], a[64:67], a[136:139], v[52:55]\n"
"v_and_b32 v13, s63, v18\n"
"v_lshlrev_b32 v14, 16, v19\n"
"v_mfma_f32_16x16x32_bf16 v[56:59], a[64:67], a[144:147], v[56:59]\n"
"v_and_b32 v15, s63, v19\n"
"v_mul_f32 v12, v12, v180\n"
"v_mfma_f32_16x16x32_bf16 v[56:59], a[68:71], a[148:151], v[56:59]\n"
"global_load_dwordx4 a[60:63], v0, s[34:35] offset:3072 sc1\n"
"v_mul_f32 v13, v13, v181\n"
"v_mfma_f32_16x16x32_bf16 v[84:87], a[76:79], a[148:151], v[84:87]\n"
"v_mul_f32 v14, v14, v182\n"
"v_mul_f32 v15, v15, v183\n"
"v_mfma_f32_16x16x32_bf16 v[84:87], a[72:75], a[144:147], v[84:87]\n"
"v_cvt_pk_bf16_f32 v26, v12, v13\n"
"v_cvt_pk_bf16_f32 v27, v14, v15\n"
"v_mfma_f32_16x16x32_bf16 a[4:7], a[72:75], a[152:155], a[4:7]\n"
"s_add_u32 s34, s34, 8192\n"
"ds_write_b64 v2, v[26:27] offset:20488\n"
"v_mfma_f32_16x16x32_bf16 a[4:7], a[76:79], a[156:159], a[4:7]\n"
"ds_read_b128 v[20:23], v2 offset:9216\n"
"v_mfma_f32_16x16x32_bf16 a[0:3], a[68:71], a[156:159], a[0:3]\n"
"v_accvgpr_read_b32 v16, a212\n"
"v_mfma_f32_16x16x32_bf16 a[0:3], a[64:67], a[152:155], a[0:3]\n"
"s_waitcnt vmcnt(10)\n"
"v_accvgpr_read_b32 v17, a213\n"
"v_accvgpr_read_b32 v18, a214\n"
"v_mfma_f32_16x16x32_bf16 v[88:91], a[80:83], a[96:99], v[88:91]\n"
"v_accvgpr_read_b32 v19, a215\n"
"v_lshlrev_b32 v12, 16, v16\n"
"v_mfma_f32_16x16x32_bf16 v[88:91], a[84:87], a[100:103], v[88:91]\n"
"global_load_dwordx4 a[64:67], v0, s[34:35] offset:-4096 sc1\n"
"v_and_b32 v13, s63, v16\n"
"v_mfma_f32_16x16x32_bf16 v[116:119], a[92:95], a[100:103], v[116:119]\n"
"ds_read_b128 a[100:103], v4 offset:1024\n"
"v_lshlrev_b32 v14, 16, v17\n"
"v_mfma_f32_16x16x32_bf16 v[116:119], a[88:91], a[96:99], v[116:119]\n"
"ds_read_b128 a[96:99], v4 offset:0\n"
"v_and_b32 v15, s63, v17\n"
"v_mfma_f32_16x16x32_bf16 v[120:123], a[88:91], a[104:107], v[120:123]\n"
"v_mul_f32 v12, v12, v208\n"
"v_mul_f32 v13, v13, v209\n"
"v_mfma_f32_16x16x32_bf16 v[120:123], a[92:95], a[108:111], v[120:123]\n"
"v_mul_f32 v14, v14, v210\n"
"v_mul_f32 v15, v15, v211\n"
"v_mfma_f32_16x16x32_bf16 v[92:95], a[84:87], a[108:111], v[92:95]\n"
"ds_read_b128 a[108:111], v4 offset:3072\n"
"v_cvt_pk_bf16_f32 v28, v12, v13\n"
"v_mfma_f32_16x16x32_bf16 v[92:95], a[80:83], a[104:107], v[92:95]\n"
"ds_read_b128 a[104:107], v4 offset:2048\n"
"v_cvt_pk_bf16_f32 v29, v14, v15\n"
"v_mfma_f32_16x16x32_bf16 v[96:99], a[80:83], a[112:115], v[96:99]\n"
"s_waitcnt lgkmcnt(4)\n"
"global_store_dwordx4 v0, v[20:23], s[98:99] offset:-3072 sc1\n"
"ds_write_b64 v2, v[28:29] offset:21504\n"
"v_lshlrev_b32 v12, 16, v18\n"
"v_mfma_f32_16x16x32_bf16 v[96:99], a[84:87], a[116:119], v[96:99]\n"
"global_load_dwordx4 a[68:71], v0, s[34:35] offset:-3072 sc1\n"
"v_and_b32 v13, s63, v18\n"
"v_mfma_f32_16x16x32_bf16 v[124:127], a[92:95], a[116:119], v[124:127]\n"
"ds_read_b128 a[116:119], v4 offset:5120\n"
"v_lshlrev_b32 v14, 16, v19\n"
"v_mfma_f32_16x16x32_bf16 v[124:127], a[88:91], a[112:115], v[124:127]\n"
"ds_read_b128 a[112:115], v4 offset:4096\n"
"v_and_b32 v15, s63, v19\n"
"v_mfma_f32_16x16x32_bf16 v[128:131], a[88:91], a[120:123], v[128:131]\n"
"v_mul_f32 v12, v12, v236\n"
"v_mul_f32 v13, v13, v237\n"
"v_mfma_f32_16x16x32_bf16 v[128:131], a[92:95], a[124:127], v[128:131]\n"
"v_mul_f32 v14, v14, v238\n"
"v_mul_f32 v15, v15, v239\n"
"v_mfma_f32_16x16x32_bf16 v[100:103], a[84:87], a[124:127], v[100:103]\n"
"ds_read_b128 a[124:127], v4 offset:7168\n"
"v_cvt_pk_bf16_f32 v30, v12, v13\n"
"v_mfma_f32_16x16x32_bf16 v[100:103], a[80:83], a[120:123], v[100:103]\n"
"ds_read_b128 a[120:123], v4 offset:6144\n"
"v_cvt_pk_bf16_f32 v31, v14, v15\n"
"v_mfma_f32_16x16x32_bf16 v[104:107], a[80:83], a[128:131], v[104:107]\n"
"ds_write_b64 v2, v[30:31] offset:21512\n"
"v_mfma_f32_16x16x32_bf16 v[104:107], a[84:87], a[132:135], v[104:107]\n"
"global_load_dwordx4 a[72:75], v0, s[34:35] offset:-2048 sc1\n"
"v_accvgpr_read_b32 v16, a216\n"
"v_mfma_f32_16x16x32_bf16 v[132:135], a[92:95], a[132:135], v[132:135]\n"
"ds_read_b128 a[132:135], v4 offset:9216\n"
"v_accvgpr_read_b32 v17, a217\n"
"v_mfma_f32_16x16x32_bf16 v[132:135], a[88:91], a[128:131], v[132:135]\n"
"ds_read_b128 a[128:131], v4 offset:8192\n"
"v_accvgpr_read_b32 v18, a218\n"
"v_mfma_f32_16x16x32_bf16 v[136:139], a[88:91], a[136:139], v[136:139]\n"
"v_accvgpr_read_b32 v19, a219\n"
"v_mfma_f32_16x16x32_bf16 v[136:139], a[92:95], a[140:143], v[136:139]\n"
"v_lshlrev_b32 v12, 16, v16\n"
"v_and_b32 v13, s63, v16\n"
"v_mfma_f32_16x16x32_bf16 v[108:111], a[84:87], a[140:143], v[108:111]\n"
"ds_read_b128 a[140:143], v4 offset:11264\n"
"v_lshlrev_b32 v14, 16, v17\n"
"v_mfma_f32_16x16x32_bf16 v[108:111], a[80:83], a[136:139], v[108:111]\n"
"ds_read_b128 a[136:139], v4 offset:10240\n"
"v_and_b32 v15, s63, v17\n"
"v_mfma_f32_16x16x32_bf16 v[112:115], a[80:83], a[144:147], v[112:115]\n"
"ds_read_b128 v[20:23], v2 offset:12288\n"
"v_mul_f32 v12, v12, v156\n"
"v_mfma_f32_16x16x32_bf16 v[112:115], a[84:87], a[148:151], v[112:115]\n"
"global_load_dwordx4 a[76:79], v0, s[34:35] offset:-1024 sc1\n"
"v_mfma_f32_16x16x32_bf16 v[140:143], a[92:95], a[148:151], v[140:143]\n"
"ds_read_b128 a[148:151], v4 offset:13312\n"
"v_mfma_f32_16x16x32_bf16 v[140:143], a[88:91], a[144:147], v[140:143]\n"
"ds_read_b128 a[144:147], v4 offset:12288\n"
"v_mfma_f32_16x16x32_bf16 a[12:15], a[88:91], a[152:155], a[12:15]\n"
"v_mul_f32 v13, v13, v157\n"
"v_mfma_f32_16x16x32_bf16 a[12:15], a[92:95], a[156:159], a[12:15]\n"
"v_mul_f32 v14, v14, v158\n"
"v_mfma_f32_16x16x32_bf16 a[8:11], a[84:87], a[156:159], a[8:11]\n"
"ds_read_b128 a[156:159], v4 offset:15360\n"
"v_mfma_f32_16x16x32_bf16 a[8:11], a[80:83], a[152:155], a[8:11]\n"
"s_waitcnt vmcnt(10) lgkmcnt(14)\n"
"ds_read_b128 a[152:155], v4 offset:14336\n"
"v_mfma_f32_16x16x32_bf16 v[32:35], a[32:35], a[96:99], v[32:35]\n"
"v_mul_f32 v15, v15, v159\n"
"v_mfma_f32_16x16x32_bf16 v[32:35], a[36:39], a[100:103], v[32:35]\n"
"global_load_dwordx4 a[80:83], v0, s[34:35] offset:0 sc1\n"
"v_mfma_f32_16x16x32_bf16 v[60:63], a[44:47], a[100:103], v[60:63]\n"
"s_add_u32 s65, s65, 1\n"
"v_cvt_pk_bf16_f32 v24, v12, v13\n"
"v_mfma_f32_16x16x32_bf16 v[60:63], a[40:43], a[96:99], v[60:63]\n"
"v_cvt_pk_bf16_f32 v25, v14, v15\n"
"v_mfma_f32_16x16x32_bf16 v[64:67], a[40:43], a[104:107], v[64:67]\n"
"s_waitcnt lgkmcnt(4)\n"
"global_store_dwordx4 v0, v[20:23], s[98:99] offset:0 sc1\n"
"ds_write_b64 v2, v[24:25] offset:22528\n"
"v_lshlrev_b32 v12, 16, v18\n"
"v_mfma_f32_16x16x32_bf16 v[64:67], a[44:47], a[108:111], v[64:67]\n"
"v_and_b32 v13, s63, v18\n"
"v_lshlrev_b32 v14, 16, v19\n"
"v_mfma_f32_16x16x32_bf16 v[36:39], a[36:39], a[108:111], v[36:39]\n"
"v_and_b32 v15, s63, v19\n"
"v_mul_f32 v12, v12, v184\n"
"v_mfma_f32_16x16x32_bf16 v[36:39], a[32:35], a[104:107], v[36:39]\n"
"v_mul_f32 v13, v13, v185\n"
"v_mul_f32 v14, v14, v186\n"
"v_mfma_f32_16x16x32_bf16 v[40:43], a[32:35], a[112:115], v[40:43]\n"
"v_mul_f32 v15, v15, v187\n"
"v_cvt_pk_bf16_f32 v26, v12, v13\n"
"v_mfma_f32_16x16x32_bf16 v[40:43], a[36:39], a[116:119], v[40:43]\n"
"global_load_dwordx4 a[84:87], v0, s[34:35] offset:1024 sc1\n"
"v_cvt_pk_bf16_f32 v27, v14, v15\n"
"v_mfma_f32_16x16x32_bf16 v[68:71], a[44:47], a[116:119], v[68:71]\n"
"ds_write_b64 v2, v[26:27] offset:22536\n"
"v_mfma_f32_16x16x32_bf16 v[68:71], a[40:43], a[112:115], v[68:71]\n"
"v_accvgpr_read_b32 v16, a220\n"
"v_accvgpr_read_b32 v17, a221\n"
"v_mfma_f32_16x16x32_bf16 v[72:75], a[40:43], a[120:123], v[72:75]\n"
"v_accvgpr_read_b32 v18, a222\n"
"v_accvgpr_read_b32 v19, a223\n"
"v_mfma_f32_16x16x32_bf16 v[72:75], a[44:47], a[124:127], v[72:75]\n"
"v_lshlrev_b32 v12, 16, v16\n"
"v_and_b32 v13, s63, v16\n"
"v_mfma_f32_16x16x32_bf16 v[44:47], a[36:39], a[124:127], v[44:47]\n"
"v_lshlrev_b32 v14, 16, v17\n"
"v_and_b32 v15, s63, v17\n"
"v_mfma_f32_16x16x32_bf16 v[44:47], a[32:35], a[120:123], v[44:47]\n"
"v_mul_f32 v12, v12, v212\n"
"v_mul_f32 v13, v13, v213\n"
"v_mfma_f32_16x16x32_bf16 v[48:51], a[32:35], a[128:131], v[48:51]\n"
"v_mul_f32 v14, v14, v214\n"
"v_mul_f32 v15, v15, v215\n"
"v_mfma_f32_16x16x32_bf16 v[48:51], a[36:39], a[132:135], v[48:51]\n"
"global_load_dwordx4 a[88:91], v0, s[34:35] offset:2048 sc1\n"
"v_cvt_pk_bf16_f32 v28, v12, v13\n"
"v_mfma_f32_16x16x32_bf16 v[76:79], a[44:47], a[132:135], v[76:79]\n"
"v_cvt_pk_bf16_f32 v29, v14, v15\n"
"v_mfma_f32_16x16x32_bf16 v[76:79], a[40:43], a[128:131], v[76:79]\n"
"ds_write_b64 v2, v[28:29] offset:23552\n"
"v_mfma_f32_16x16x32_bf16 v[80:83], a[40:43], a[136:139], v[80:83]\n"
"v_lshlrev_b32 v12, 16, v18\n"
"v_and_b32 v13, s63, v18\n"
"v_mfma_f32_16x16x32_bf16 v[80:83], a[44:47], a[140:143], v[80:83]\n"
"v_lshlrev_b32 v14, 16, v19\n"
"v_and_b32 v15, s63, v19\n"
"v_mfma_f32_16x16x32_bf16 v[52:55], a[36:39], a[140:143], v[52:55]\n"
"v_mul_f32 v12, v12, v240\n"
"v_mul_f32 v13, v13, v241\n"
"v_mfma_f32_16x16x32_bf16 v[52:55], a[32:35], a[136:139], v[52:55]\n"
"s_waitcnt lgkmcnt(3)\n"
"v_mul_f32 v14, v14, v242\n"
"v_mul_f32 v15, v15, v243\n"
"v_mfma_f32_16x16x32_bf16 v[56:59], a[32:35], a[144:147], v[56:59]\n"
"v_cvt_pk_bf16_f32 v30, v12, v13\n"
"v_cvt_pk_bf16_f32 v31, v14, v15\n"
"v_mfma_f32_16x16x32_bf16 v[56:59], a[36:39], a[148:151], v[56:59]\n"
"global_load_dwordx4 a[92:95], v0, s[34:35] offset:3072 sc1\n"
"v_mfma_f32_16x16x32_bf16 v[84:87], a[44:47], a[148:151], v[84:87]\n"
"ds_write_b64 v2, v[30:31] offset:23560\n"
"v_mfma_f32_16x16x32_bf16 v[84:87], a[40:43], a[144:147], v[84:87]\n"
"v_accvgpr_read_b32 v16, a224\n"
"v_accvgpr_read_b32 v17, a225\n"
"v_mfma_f32_16x16x32_bf16 a[4:7], a[40:43], a[152:155], a[4:7]\n"
"s_add_u32 s34, s34, 8192\n"
"v_accvgpr_read_b32 v18, a226\n"
"v_accvgpr_read_b32 v19, a227\n"
"v_mfma_f32_16x16x32_bf16 a[4:7], a[44:47], a[156:159], a[4:7]\n"
"v_lshlrev_b32 v12, 16, v16\n"
"v_and_b32 v13, s63, v16\n"
"v_mfma_f32_16x16x32_bf16 a[0:3], a[36:39], a[156:159], a[0:3]\n"
"v_lshlrev_b32 v14, 16, v17\n"
"v_and_b32 v15, s63, v17\n"
"v_mfma_f32_16x16x32_bf16 a[0:3], a[32:35], a[152:155], a[0:3]\n"
"s_waitcnt vmcnt(10)\n"
"v_mul_f32 v12, v12, v160\n"
"v_mul_f32 v13, v13, v161\n"
"v_mfma_f32_16x16x32_bf16 v[88:91], a[48:51], a[96:99], v[88:91]\n"
"v_mul_f32 v14, v14, v162\n"
"v_mul_f32 v15, v15, v163\n"
"v_mfma_f32_16x16x32_bf16 v[88:91], a[52:55], a[100:103], v[88:91]\n"
"global_load_dwordx4 a[32:35], v0, s[34:35] offset:-4096 sc1\n"
"v_cvt_pk_bf16_f32 v24, v12, v13\n"
"v_mfma_f32_16x16x32_bf16 v[116:119], a[60:63], a[100:103], v[116:119]\n"
"ds_read_b128 a[100:103], v5 offset:1024\n"
"v_cvt_pk_bf16_f32 v25, v14, v15\n"
"v_mfma_f32_16x16x32_bf16 v[116:119], a[56:59], a[96:99], v[116:119]\n"
"ds_read_b128 a[96:99], v5 offset:0\n"
"v_mfma_f32_16x16x32_bf16 v[120:123], a[56:59], a[104:107], v[120:123]\n"
"ds_write_b64 v2, v[24:25] offset:24576\n"
"v_mfma_f32_16x16x32_bf16 v[120:123], a[60:63], a[108:111], v[120:123]\n"
"v_lshlrev_b32 v12, 16, v18\n"
"v_and_b32 v13, s63, v18\n"
"v_mfma_f32_16x16x32_bf16 v[92:95], a[52:55], a[108:111], v[92:95]\n"
"ds_read_b128 a[108:111], v5 offset:3072\n"
"v_lshlrev_b32 v14, 16, v19\n"
"v_mfma_f32_16x16x32_bf16 v[92:95], a[48:51], a[104:107], v[92:95]\n"
"ds_read_b128 a[104:107], v5 offset:2048\n"
"v_and_b32 v15, s63, v19\n"
"v_mfma_f32_16x16x32_bf16 v[96:99], a[48:51], a[112:115], v[96:99]\n"
"v_mul_f32 v12, v12, v188\n"
"v_mul_f32 v13, v13, v189\n"
"v_mfma_f32_16x16x32_bf16 v[96:99], a[52:55], a[116:119], v[96:99]\n"
"global_load_dwordx4 a[36:39], v0, s[34:35] offset:-3072 sc1\n"
"v_mul_f32 v14, v14, v190\n"
"v_mfma_f32_16x16x32_bf16 v[124:127], a[60:63], a[116:119], v[124:127]\n"
"ds_read_b128 a[116:119], v5 offset:5120\n"
"v_mul_f32 v15, v15, v191\n"
"v_mfma_f32_16x16x32_bf16 v[124:127], a[56:59], a[112:115], v[124:127]\n"
"ds_read_b128 a[112:115], v5 offset:4096\n"
"v_cvt_pk_bf16_f32 v26, v12, v13\n"
"v_mfma_f32_16x16x32_bf16 v[128:131], a[56:59], a[120:123], v[128:131]\n"
"v_cvt_pk_bf16_f32 v27, v14, v15\n"
"v_mfma_f32_16x16x32_bf16 v[128:131], a[60:63], a[124:127], v[128:131]\n"
"ds_write_b64 v2, v[26:27] offset:24584\n"
"v_mfma_f32_16x16x32_bf16 v[100:103], a[52:55], a[124:127], v[100:103]\n"
"ds_read_b128 a[124:127], v5 offset:7168\n"
"v_accvgpr_read_b32 v16, a228\n"
"v_mfma_f32_16x16x32_bf16 v[100:103], a[48:51], a[120:123], v[100:103]\n"
"ds_read_b128 a[120:123], v5 offset:6144\n"
"v_accvgpr_read_b32 v17, a229\n"
"v_mfma_f32_16x16x32_bf16 v[104:107], a[48:51], a[128:131], v[104:107]\n"
"v_accvgpr_read_b32 v18, a230\n"
"v_accvgpr_read_b32 v19, a231\n"
"v_mfma_f32_16x16x32_bf16 v[104:107], a[52:55], a[132:135], v[104:107]\n"
"global_load_dwordx4 a[40:43], v0, s[34:35] offset:-2048 sc1\n"
"v_lshlrev_b32 v12, 16, v16\n"
"v_mfma_f32_16x16x32_bf16 v[132:135], a[60:63], a[132:135], v[132:135]\n"
"ds_read_b128 a[132:135], v5 offset:9216\n"
"v_and_b32 v13, s63, v16\n"
"v_mfma_f32_16x16x32_bf16 v[132:135], a[56:59], a[128:131], v[132:135]\n"
"ds_read_b128 a[128:131], v5 offset:8192\n"
"v_lshlrev_b32 v14, 16, v17\n"
"v_mfma_f32_16x16x32_bf16 v[136:139], a[56:59], a[136:139], v[136:139]\n"
"v_and_b32 v15, s63, v17\n"
"v_mul_f32 v12, v12, v216\n"
"v_mfma_f32_16x16x32_bf16 v[136:139], a[60:63], a[140:143], v[136:139]\n"
"v_mul_f32 v13, v13, v217\n"
"v_mul_f32 v14, v14, v218\n"
"v_mfma_f32_16x16x32_bf16 v[108:111], a[52:55], a[140:143], v[108:111]\n"
"ds_read_b128 a[140:143], v5 offset:11264\n"
"v_mul_f32 v15, v15, v219\n"
"v_mfma_f32_16x16x32_bf16 v[108:111], a[48:51], a[136:139], v[108:111]\n"
"ds_read_b128 a[136:139], v5 offset:10240\n"
"v_cvt_pk_bf16_f32 v28, v12, v13\n"
"v_mfma_f32_16x16x32_bf16 v[112:115], a[48:51], a[144:147], v[112:115]\n"
"v_cvt_pk_bf16_f32 v29, v14, v15\n"
"v_lshlrev_b32 v12, 16, v18\n"
"v_mfma_f32_16x16x32_bf16 v[112:115], a[52:55], a[148:151], v[112:115]\n"
"global_load_dwordx4 a[44:47], v0, s[34:35] offset:-1024 sc1\n"
"v_and_b32 v13, s63, v18\n"
"v_mfma_f32_16x16x32_bf16 v[140:143], a[60:63], a[148:151], v[140:143]\n"
"ds_read_b128 a[148:151], v5 offset:13312\n"
"v_lshlrev_b32 v14, 16, v19\n"
"v_mfma_f32_16x16x32_bf16 v[140:143], a[56:59], a[144:147], v[140:143]\n"
"ds_read_b128 a[144:147], v5 offset:12288\n"
"v_and_b32 v15, s63, v19\n"
"v_mfma_f32_16x16x32_bf16 a[12:15], a[56:59], a[152:155], a[12:15]\n"
"v_mul_f32 v12, v12, v244\n"
"v_mul_f32 v13, v13, v245\n"
"v_mfma_f32_16x16x32_bf16 a[12:15], a[60:63], a[156:159], a[12:15]\n"
"v_mul_f32 v14, v14, v246\n"
"v_mul_f32 v15, v15, v247\n"
"v_mfma_f32_16x16x32_bf16 a[8:11], a[52:55], a[156:159], a[8:11]\n"
"ds_read_b128 a[156:159], v5 offset:15360\n"
"v_cvt_pk_bf16_f32 v30, v12, v13\n"
"v_mfma_f32_16x16x32_bf16 a[8:11], a[48:51], a[152:155], a[8:11]\n"
"s_waitcnt vmcnt(9) lgkmcnt(12)\n"
"ds_read_b128 a[152:155], v5 offset:14336\n"
"v_cvt_pk_bf16_f32 v31, v14, v15\n"
"v_mfma_f32_16x16x32_bf16 v[32:35], a[64:67], a[96:99], v[32:35]\n"
"global_store_dwordx4 v0, v[28:31], s[54:55] offset:-3072 sc1\n"
"ds_write_b128 v2, v[28:31] offset:25600\n"
"v_mfma_f32_16x16x32_bf16 v[32:35], a[68:71], a[100:103], v[32:35]\n"
"global_load_dwordx4 a[48:51], v0, s[34:35] offset:0 sc1\n"
"v_accvgpr_read_b32 v16, a232\n"
"v_mfma_f32_16x16x32_bf16 v[60:63], a[76:79], a[100:103], v[60:63]\n"
"v_accvgpr_read_b32 v17, a233\n"
"v_accvgpr_read_b32 v18, a234\n"
"v_mfma_f32_16x16x32_bf16 v[60:63], a[72:75], a[96:99], v[60:63]\n"
"v_accvgpr_read_b32 v19, a235\n"
"v_lshlrev_b32 v12, 16, v16\n"
"v_mfma_f32_16x16x32_bf16 v[64:67], a[72:75], a[104:107], v[64:67]\n"
"v_and_b32 v13, s63, v16\n"
"v_lshlrev_b32 v14, 16, v17\n"
"v_mfma_f32_16x16x32_bf16 v[64:67], a[76:79], a[108:111], v[64:67]\n"
"v_and_b32 v15, s63, v17\n"
"v_mul_f32 v12, v12, v164\n"
"v_mfma_f32_16x16x32_bf16 v[36:39], a[68:71], a[108:111], v[36:39]\n"
"v_mul_f32 v13, v13, v165\n"
"v_mul_f32 v14, v14, v166\n"
"v_mfma_f32_16x16x32_bf16 v[36:39], a[64:67], a[104:107], v[36:39]\n"
"s_waitcnt lgkmcnt(9)\n"
"v_mul_f32 v15, v15, v167\n"
"v_cvt_pk_bf16_f32 v24, v12, v13\n"
"v_mfma_f32_16x16x32_bf16 v[40:43], a[64:67], a[112:115], v[40:43]\n"
"v_cvt_pk_bf16_f32 v25, v14, v15\n"
"v_mfma_f32_16x16x32_bf16 v[40:43], a[68:71], a[116:119], v[40:43]\n"
"global_load_dwordx4 a[52:55], v0, s[34:35] offset:1024 sc1\n"
"v_mfma_f32_16x16x32_bf16 v[68:71], a[76:79], a[116:119], v[68:71]\n"
"ds_write_b64 v2, v[24:25] offset:26624\n"
"v_mfma_f32_16x16x32_bf16 v[68:71], a[72:75], a[112:115], v[68:71]\n"
"v_lshlrev_b32 v12, 16, v18\n"
"v_and_b32 v13, s63, v18\n"
"v_mfma_f32_16x16x32_bf16 v[72:75], a[72:75], a[120:123], v[72:75]\n"
"v_lshlrev_b32 v14, 16, v19\n"
"v_and_b32 v15, s63, v19\n"
"v_mfma_f32_16x16x32_bf16 v[72:75], a[76:79], a[124:127], v[72:75]\n"
"v_mul_f32 v12, v12, v192\n"
"v_mul_f32 v13, v13, v193\n"
"v_mfma_f32_16x16x32_bf16 v[44:47], a[68:71], a[124:127], v[44:47]\n"
"v_mul_f32 v14, v14, v194\n"
"v_mul_f32 v15, v15, v195\n"
"v_mfma_f32_16x16x32_bf16 v[44:47], a[64:67], a[120:123], v[44:47]\n"
"s_waitcnt lgkmcnt(6)\n"
"v_cvt_pk_bf16_f32 v26, v12, v13\n"
"v_cvt_pk_bf16_f32 v27, v14, v15\n"
"v_mfma_f32_16x16x32_bf16 v[48:51], a[64:67], a[128:131], v[48:51]\n"
"ds_write_b64 v2, v[26:27] offset:26632\n"
"v_mfma_f32_16x16x32_bf16 v[48:51], a[68:71], a[132:135], v[48:51]\n"
"global_load_dwordx4 a[56:59], v0, s[34:35] offset:2048 sc1\n"
"v_accvgpr_read_b32 v16, a236\n"
"v_mfma_f32_16x16x32_bf16 v[76:79], a[76:79], a[132:135], v[76:79]\n"
"v_accvgpr_read_b32 v17, a237\n"
"v_accvgpr_read_b32 v18, a238\n"
"v_mfma_f32_16x16x32_bf16 v[76:79], a[72:75], a[128:131], v[76:79]\n"
"v_accvgpr_read_b32 v19, a239\n"
"v_lshlrev_b32 v12, 16, v16\n"
"v_mfma_f32_16x16x32_bf16 v[80:83], a[72:75], a[136:139], v[80:83]\n"
"v_and_b32 v13, s63, v16\n"
"v_mfma_f32_16x16x32_bf16 v[80:83], a[76:79], a[140:143], v[80:83]\n"
"v_lshlrev_b32 v14, 16, v17\n"
"v_mfma_f32_16x16x32_bf16 v[52:55], a[68:71], a[140:143], v[52:55]\n"
"v_and_b32 v15, s63, v17\n"
"v_mul_f32 v12, v12, v220\n"
"v_mfma_f32_16x16x32_bf16 v[52:55], a[64:67], a[136:139], v[52:55]\n"
"s_waitcnt lgkmcnt(3)\n"
"v_mul_f32 v13, v13, v221\n"
"v_mfma_f32_16x16x32_bf16 v[56:59], a[64:67], a[144:147], v[56:59]\n"
"v_mul_f32 v14, v14, v222\n"
"v_mul_f32 v15, v15, v223\n"
"v_mfma_f32_16x16x32_bf16 v[56:59], a[68:71], a[148:151], v[56:59]\n"
"global_load_dwordx4 a[60:63], v0, s[34:35] offset:3072 sc1\n"
"v_cvt_pk_bf16_f32 v28, v12, v13\n"
"v_mfma_f32_16x16x32_bf16 v[84:87], a[76:79], a[148:151], v[84:87]\n"
"v_cvt_pk_bf16_f32 v29, v14, v15\n"
"v_mfma_f32_16x16x32_bf16 v[84:87], a[72:75], a[144:147], v[84:87]\n"
"v_lshlrev_b32 v12, 16, v18\n"
"v_and_b32 v13, s63, v18\n"
"v_mfma_f32_16x16x32_bf16 a[4:7], a[72:75], a[152:155], a[4:7]\n"
"s_add_u32 s34, s34, 8192\n"
"v_lshlrev_b32 v14, 16, v19\n"
"v_mfma_f32_16x16x32_bf16 a[4:7], a[76:79], a[156:159], a[4:7]\n"
"v_and_b32 v15, s63, v19\n"
"v_mfma_f32_16x16x32_bf16 a[0:3], a[68:71], a[156:159], a[0:3]\n"
"v_mul_f32 v12, v12, v248\n"
"v_mul_f32 v13, v13, v249\n"
"v_mfma_f32_16x16x32_bf16 a[0:3], a[64:67], a[152:155], a[0:3]\n"
"s_waitcnt vmcnt(9)\n"
"v_mul_f32 v14, v14, v250\n"
"v_mfma_f32_16x16x32_bf16 v[88:91], a[80:83], a[96:99], v[88:91]\n"
"v_mul_f32 v15, v15, v251\n"
"v_cvt_pk_bf16_f32 v30, v12, v13\n"
"v_mfma_f32_16x16x32_bf16 v[88:91], a[84:87], a[100:103], v[88:91]\n"
"global_load_dwordx4 a[64:67], v0, s[34:35] offset:-4096 sc1\n"
"v_cvt_pk_bf16_f32 v31, v14, v15\n"
"v_mfma_f32_16x16x32_bf16 v[116:119], a[92:95], a[100:103], v[116:119]\n"
"global_store_dwordx4 v0, v[28:31], s[54:55] offset:-1024 sc1\n"
"ds_write_b128 v2, v[28:31] offset:27648\n"
"v_mfma_f32_16x16x32_bf16 v[116:119], a[88:91], a[96:99], v[116:119]\n"
"v_accvgpr_read_b32 v16, a240\n"
"v_mfma_f32_16x16x32_bf16 v[120:123], a[88:91], a[104:107], v[120:123]\n"
"v_accvgpr_read_b32 v17, a241\n"
"v_mfma_f32_16x16x32_bf16 v[120:123], a[92:95], a[108:111], v[120:123]\n"
"v_accvgpr_read_b32 v18, a242\n"
"v_mfma_f32_16x16x32_bf16 v[92:95], a[84:87], a[108:111], v[92:95]\n"
"v_accvgpr_read_b32 v19, a243\n"
"v_lshlrev_b32 v12, 16, v16\n"
"v_mfma_f32_16x16x32_bf16 v[92:95], a[80:83], a[104:107], v[92:95]\n"
"v_and_b32 v13, s63, v16\n"
"v_mfma_f32_16x16x32_bf16 v[96:99], a[80:83], a[112:115], v[96:99]\n"
"v_lshlrev_b32 v14, 16, v17\n"
"v_and_b32 v15, s63, v17\n"
"v_mfma_f32_16x16x32_bf16 v[96:99], a[84:87], a[116:119], v[96:99]\n"
"global_load_dwordx4 a[68:71], v0, s[34:35] offset:-3072 sc1\n"
"v_mul_f32 v12, v12, v168\n"
"v_mfma_f32_16x16x32_bf16 v[124:127], a[92:95], a[116:119], v[124:127]\n"
"v_mul_f32 v13, v13, v169\n"
"v_mfma_f32_16x16x32_bf16 v[124:127], a[88:91], a[112:115], v[124:127]\n"
"v_mul_f32 v14, v14, v170\n"
"v_mul_f32 v15, v15, v171\n"
"v_mfma_f32_16x16x32_bf16 v[128:131], a[88:91], a[120:123], v[128:131]\n"
"v_cvt_pk_bf16_f32 v24, v12, v13\n"
"v_mfma_f32_16x16x32_bf16 v[128:131], a[92:95], a[124:127], v[128:131]\n"
"v_cvt_pk_bf16_f32 v25, v14, v15\n"
"v_lshlrev_b32 v12, 16, v18\n"
"v_mfma_f32_16x16x32_bf16 v[100:103], a[84:87], a[124:127], v[100:103]\n"
"v_and_b32 v13, s63, v18\n"
"v_lshlrev_b32 v14, 16, v19\n"
"v_mfma_f32_16x16x32_bf16 v[100:103], a[80:83], a[120:123], v[100:103]\n"
"v_mov_b32 v8, s65\n"
"v_and_b32 v15, s63, v19\n"
"v_mfma_f32_16x16x32_bf16 v[104:107], a[80:83], a[128:131], v[104:107]\n"
"v_mov_b32 v9, s60\n"
"v_mul_f32 v12, v12, v196\n"
"v_mfma_f32_16x16x32_bf16 v[104:107], a[84:87], a[132:135], v[104:107]\n"
"global_load_dwordx4 a[72:75], v0, s[34:35] offset:-2048 sc1\n"
"v_mul_f32 v13, v13, v197\n"
"v_mfma_f32_16x16x32_bf16 v[132:135], a[92:95], a[132:135], v[132:135]\n"
"buffer_store_dwordx2 v[8:9], off, s[56:59], %13 sc1\n"
"v_mul_f32 v14, v14, v198\n"
"v_mfma_f32_16x16x32_bf16 v[132:135], a[88:91], a[128:131], v[132:135]\n"
"v_mul_f32 v15, v15, v199\n"
"v_cvt_pk_bf16_f32 v26, v12, v13\n"
"v_mfma_f32_16x16x32_bf16 v[136:139], a[88:91], a[136:139], v[136:139]\n"
"v_cvt_pk_bf16_f32 v27, v14, v15\n"
"v_mfma_f32_16x16x32_bf16 v[136:139], a[92:95], a[140:143], v[136:139]\n"
"global_store_dwordx4 v0, v[24:27], s[54:55] offset:0 sc1\n"
"ds_write_b128 v2, v[24:27] offset:28672\n"
"v_mfma_f32_16x16x32_bf16 v[108:111], a[84:87], a[140:143], v[108:111]\n"
"v_accvgpr_read_b32 v16, a248\n"
"v_mfma_f32_16x16x32_bf16 v[108:111], a[80:83], a[136:139], v[108:111]\n"
"v_accvgpr_read_b32 v17, a249\n"
"v_mfma_f32_16x16x32_bf16 v[112:115], a[80:83], a[144:147], v[112:115]\n"
"v_accvgpr_read_b32 v18, a250\n"
"v_accvgpr_read_b32 v19, a251\n"
"v_mfma_f32_16x16x32_bf16 v[112:115], a[84:87], a[148:151], v[112:115]\n"
"global_load_dwordx4 a[76:79], v0, s[34:35] offset:-1024 sc1\n"
"v_lshlrev_b32 v12, 16, v16\n"
"v_mfma_f32_16x16x32_bf16 v[140:143], a[92:95], a[148:151], v[140:143]\n"
"v_and_b32 v13, s63, v16\n"
"v_mfma_f32_16x16x32_bf16 v[140:143], a[88:91], a[144:147], v[140:143]\n"
"v_lshlrev_b32 v14, 16, v17\n"
"v_and_b32 v15, s63, v17\n"
"v_mfma_f32_16x16x32_bf16 a[12:15], a[88:91], a[152:155], a[12:15]\n"
"v_accvgpr_read_b32 v10, a16\n"
"v_mfma_f32_16x16x32_bf16 a[12:15], a[92:95], a[156:159], a[12:15]\n"
"v_accvgpr_read_b32 v11, a17\n"
"v_mfma_f32_16x16x32_bf16 a[8:11], a[84:87], a[156:159], a[8:11]\n"
"v_mul_f32 v12, v12, v10\n"
"v_mul_f32 v13, v13, v11\n"
"v_mfma_f32_16x16x32_bf16 a[8:11], a[80:83], a[152:155], a[8:11]\n"
"s_waitcnt vmcnt(12)\n"
"v_accvgpr_read_b32 v10, a18\n"
"v_mfma_f32_16x16x32_bf16 v[144:147], a[32:35], a[96:99], v[144:147]\n"
"v_accvgpr_read_b32 v11, a19\n"
"v_mul_f32 v14, v14, v10\n"
"v_mfma_f32_16x16x32_bf16 v[144:147], a[36:39], a[100:103], v[144:147]\n"
"global_load_dwordx4 a[80:83], v0, s[34:35] offset:0 sc1\n"
"v_mul_f32 v15, v15, v11\n"
"v_mfma_f32_16x16x32_bf16 v[172:175], a[44:47], a[100:103], v[172:175]\n"
"v_cvt_pk_bf16_f32 v28, v12, v13\n"
"v_mfma_f32_16x16x32_bf16 v[172:175], a[40:43], a[96:99], v[172:175]\n"
"v_cvt_pk_bf16_f32 v29, v14, v15\n"
"v_lshlrev_b32 v12, 16, v18\n"
"v_mfma_f32_16x16x32_bf16 v[176:179], a[40:43], a[104:107], v[176:179]\n"
"v_and_b32 v13, s63, v18\n"
"v_mfma_f32_16x16x32_bf16 v[176:179], a[44:47], a[108:111], v[176:179]\n"
"v_lshlrev_b32 v14, 16, v19\n"
"v_mfma_f32_16x16x32_bf16 v[148:151], a[36:39], a[108:111], v[148:151]\n"
"v_and_b32 v15, s63, v19\n"
"v_accvgpr_read_b32 v10, a20\n"
"v_mfma_f32_16x16x32_bf16 v[148:151], a[32:35], a[104:107], v[148:151]\n"
"v_accvgpr_read_b32 v11, a21\n"
"v_mfma_f32_16x16x32_bf16 v[152:155], a[32:35], a[112:115], v[152:155]\n"
"v_mul_f32 v12, v12, v10\n"
"v_mul_f32 v13, v13, v11\n"
"v_mfma_f32_16x16x32_bf16 v[152:155], a[36:39], a[116:119], v[152:155]\n"
"global_load_dwordx4 a[84:87], v0, s[34:35] offset:1024 sc1\n"
"v_accvgpr_read_b32 v10, a22\n"
"v_mfma_f32_16x16x32_bf16 v[180:183], a[44:47], a[116:119], v[180:183]\n"
"v_accvgpr_read_b32 v11, a23\n"
"v_mfma_f32_16x16x32_bf16 v[180:183], a[40:43], a[112:115], v[180:183]\n"
"v_mul_f32 v14, v14, v10\n"
"v_mul_f32 v15, v15, v11\n"
"v_mfma_f32_16x16x32_bf16 v[184:187], a[40:43], a[120:123], v[184:187]\n"
"v_cvt_pk_bf16_f32 v30, v12, v13\n"
"v_mfma_f32_16x16x32_bf16 v[184:187], a[44:47], a[124:127], v[184:187]\n"
"v_cvt_pk_bf16_f32 v31, v14, v15\n"
"v_mfma_f32_16x16x32_bf16 v[156:159], a[36:39], a[124:127], v[156:159]\n"
"global_store_dwordx4 v0, v[28:31], s[54:55] offset:2048 sc1\n"
"ds_write_b128 v2, v[28:31] offset:30720\n"
"v_mfma_f32_16x16x32_bf16 v[156:159], a[32:35], a[120:123], v[156:159]\n"
"v_accvgpr_read_b32 v16, a244\n"
"v_mfma_f32_16x16x32_bf16 v[160:163], a[32:35], a[128:131], v[160:163]\n"
"v_accvgpr_read_b32 v17, a245\n"
"v_accvgpr_read_b32 v18, a246\n"
"v_mfma_f32_16x16x32_bf16 v[160:163], a[36:39], a[132:135], v[160:163]\n"
"global_load_dwordx4 a[88:91], v0, s[34:35] offset:2048 sc1\n"
"v_accvgpr_read_b32 v19, a247\n"
"v_mfma_f32_16x16x32_bf16 v[188:191], a[44:47], a[132:135], v[188:191]\n"
"v_lshlrev_b32 v12, 16, v16\n"
"v_mfma_f32_16x16x32_bf16 v[188:191], a[40:43], a[128:131], v[188:191]\n"
"v_and_b32 v13, s63, v16\n"
"v_lshlrev_b32 v14, 16, v17\n"
"v_mfma_f32_16x16x32_bf16 v[192:195], a[40:43], a[136:139], v[192:195]\n"
"v_and_b32 v15, s63, v17\n"
"v_mfma_f32_16x16x32_bf16 v[192:195], a[44:47], a[140:143], v[192:195]\n"
"v_mul_f32 v12, v12, v224\n"
"v_mul_f32 v13, v13, v225\n"
"v_mfma_f32_16x16x32_bf16 v[164:167], a[36:39], a[140:143], v[164:167]\n"
"v_mul_f32 v14, v14, v226\n"
"v_mul_f32 v15, v15, v227\n"
"v_mfma_f32_16x16x32_bf16 v[164:167], a[32:35], a[136:139], v[164:167]\n"
"v_cvt_pk_bf16_f32 v24, v12, v13\n"
"v_cvt_pk_bf16_f32 v25, v14, v15\n"
"v_mfma_f32_16x16x32_bf16 v[168:171], a[32:35], a[144:147], v[168:171]\n"
"ds_write_b64 v2, v[24:25] offset:29696\n"
"v_mfma_f32_16x16x32_bf16 v[168:171], a[36:39], a[148:151], v[168:171]\n"
"global_load_dwordx4 a[92:95], v0, s[34:35] offset:3072 sc1\n"
"v_mfma_f32_16x16x32_bf16 v[196:199], a[44:47], a[148:151], v[196:199]\n"
"v_lshlrev_b32 v12, 16, v18\n"
"v_and_b32 v13, s63, v18\n"
"v_mfma_f32_16x16x32_bf16 v[196:199], a[40:43], a[144:147], v[196:199]\n"
"v_lshlrev_b32 v14, 16, v19\n"
"v_and_b32 v15, s63, v19\n"
"v_mfma_f32_16x16x32_bf16 a[20:23], a[40:43], a[152:155], a[20:23]\n"
"s_add_u32 s34, s34, 8192\n"
"v_mul_f32 v12, v12, v252\n"
"v_mul_f32 v13, v13, v253\n"
"v_mfma_f32_16x16x32_bf16 a[20:23], a[44:47], a[156:159], a[20:23]\n"
"v_mul_f32 v14, v14, v254\n"
"v_mul_f32 v15, v15, v255\n"
"v_mfma_f32_16x16x32_bf16 a[16:19], a[36:39], a[156:159], a[16:19]\n"
"v_cvt_pk_bf16_f32 v26, v12, v13\n"
"v_cvt_pk_bf16_f32 v27, v14, v15\n"
"v_mfma_f32_16x16x32_bf16 a[16:19], a[32:35], a[152:155], a[16:19]\n"
"L_st5703:\n"
"s_cbranch_execz L_st7945\n"
"s_waitcnt vmcnt(12)\n"
"ds_write_b64 v2, v[26:27] offset:29704\n"
"v_mfma_f32_16x16x32_bf16 v[200:203], a[48:51], a[96:99], v[200:203]\n"
"v_accvgpr_read_b32 v16, a252\n"
"v_mfma_f32_16x16x32_bf16 v[200:203], a[52:55], a[100:103], v[200:203]\n"
"global_load_dwordx4 a[32:35], v0, s[34:35] offset:-4096 sc1\n"
"v_accvgpr_read_b32 v17, a253\n"
"v_mfma_f32_16x16x32_bf16 v[228:231], a[60:63], a[100:103], v[228:231]\n"
"ds_read_b128 a[100:103], v4 offset:1024\n"
"v_accvgpr_read_b32 v18, a254\n"
"v_mfma_f32_16x16x32_bf16 v[228:231], a[56:59], a[96:99], v[228:231]\n"
"ds_read_b128 a[96:99], v4 offset:0\n"
"v_accvgpr_read_b32 v19, a255\n"
"v_mfma_f32_16x16x32_bf16 v[232:235], a[56:59], a[104:107], v[232:235]\n"
"v_lshlrev_b32 v12, 16, v16\n"
"v_and_b32 v13, s63, v16\n"
"v_mfma_f32_16x16x32_bf16 v[232:235], a[60:63], a[108:111], v[232:235]\n"
"v_lshlrev_b32 v14, 16, v17\n"
"v_and_b32 v15, s63, v17\n"
"v_mfma_f32_16x16x32_bf16 v[204:207], a[52:55], a[108:111], v[204:207]\n"
"ds_read_b128 a[108:111], v4 offset:3072\n"
"v_accvgpr_read_b32 v10, a24\n"
"v_mfma_f32_16x16x32_bf16 v[204:207], a[48:51], a[104:107], v[204:207]\n"
"ds_read_b128 a[104:107], v4 offset:2048\n"
"v_accvgpr_read_b32 v11, a25\n"
"v_mfma_f32_16x16x32_bf16 v[208:211], a[48:51], a[112:115], v[208:211]\n"
"v_mul_f32 v12, v12, v10\n"
"v_mul_f32 v13, v13, v11\n"
"v_mfma_f32_16x16x32_bf16 v[208:211], a[52:55], a[116:119], v[208:211]\n"
"global_load_dwordx4 a[36:39], v0, s[34:35] offset:-3072 sc1\n"
"v_accvgpr_read_b32 v10, a26\n"
"v_mfma_f32_16x16x32_bf16 v[236:239], a[60:63], a[116:119], v[236:239]\n"
"ds_read_b128 a[116:119], v4 offset:5120\n"
"v_accvgpr_read_b32 v11, a27\n"
"v_mfma_f32_16x16x32_bf16 v[236:239], a[56:59], a[112:115], v[236:239]\n"
"ds_read_b128 a[112:115], v4 offset:4096\n"
"v_mul_f32 v14, v14, v10\n"
"v_mfma_f32_16x16x32_bf16 v[240:243], a[56:59], a[120:123], v[240:243]\n"
"v_mul_f32 v15, v15, v11\n"
"v_cvt_pk_bf16_f32 v28, v12, v13\n"
"v_mfma_f32_16x16x32_bf16 v[240:243], a[60:63], a[124:127], v[240:243]\n"
"v_cvt_pk_bf16_f32 v29, v14, v15\n"
"v_lshlrev_b32 v12, 16, v18\n"
"v_mfma_f32_16x16x32_bf16 v[212:215], a[52:55], a[124:127], v[212:215]\n"
"ds_read_b128 a[124:127], v4 offset:7168\n"
"v_and_b32 v13, s63, v18\n"
"v_mfma_f32_16x16x32_bf16 v[212:215], a[48:51], a[120:123], v[212:215]\n"
"ds_read_b128 a[120:123], v4 offset:6144\n"
"v_lshlrev_b32 v14, 16, v19\n"
"v_mfma_f32_16x16x32_bf16 v[216:219], a[48:51], a[128:131], v[216:219]\n"
"v_and_b32 v15, s63, v19\n"
"v_accvgpr_read_b32 v10, a28\n"
"v_mfma_f32_16x16x32_bf16 v[216:219], a[52:55], a[132:135], v[216:219]\n"
"global_load_dwordx4 a[40:43], v0, s[34:35] offset:-2048 sc1\n"
"v_accvgpr_read_b32 v11, a29\n"
"v_mfma_f32_16x16x32_bf16 v[244:247], a[60:63], a[132:135], v[244:247]\n"
"ds_read_b128 a[132:135], v4 offset:9216\n"
"v_mul_f32 v12, v12, v10\n"
"v_mfma_f32_16x16x32_bf16 v[244:247], a[56:59], a[128:131], v[244:247]\n"
"ds_read_b128 a[128:131], v4 offset:8192\n"
"v_mul_f32 v13, v13, v11\n"
"v_mfma_f32_16x16x32_bf16 v[248:251], a[56:59], a[136:139], v[248:251]\n"
"v_accvgpr_read_b32 v10, a30\n"
"v_accvgpr_read_b32 v11, a31\n"
"v_mfma_f32_16x16x32_bf16 v[248:251], a[60:63], a[140:143], v[248:251]\n"
"v_mul_f32 v14, v14, v10\n"
"v_mul_f32 v15, v15, v11\n"
"v_mfma_f32_16x16x32_bf16 v[220:223], a[52:55], a[140:143], v[220:223]\n"
"ds_read_b128 a[140:143], v4 offset:11264\n"
"v_cvt_pk_bf16_f32 v30, v12, v13\n"
"v_mfma_f32_16x16x32_bf16 v[220:223], a[48:51], a[136:139], v[220:223]\n"
"ds_read_b128 a[136:139], v4 offset:10240\n"
"v_cvt_pk_bf16_f32 v31, v14, v15\n"
"v_mfma_f32_16x16x32_bf16 v[224:227], a[48:51], a[144:147], v[224:227]\n"
"global_store_dwordx4 v0, v[28:31], s[54:55] offset:3072 sc1\n"
"ds_write_b128 v2, v[28:31] offset:31744\n"
"v_mfma_f32_16x16x32_bf16 v[224:227], a[52:55], a[148:151], v[224:227]\n"
"global_load_dwordx4 a[44:47], v0, s[34:35] offset:-1024 sc1\n"
"v_mfma_f32_16x16x32_bf16 v[252:255], a[60:63], a[148:151], v[252:255]\n"
"ds_read_b128 a[148:151], v4 offset:13312\n"
"v_mfma_f32_16x16x32_bf16 v[252:255], a[56:59], a[144:147], v[252:255]\n"
"ds_read_b128 a[144:147], v4 offset:12288\n"
"v_mfma_f32_16x16x32_bf16 a[28:31], a[56:59], a[152:155], a[28:31]\n"
"v_mfma_f32_16x16x32_bf16 a[28:31], a[60:63], a[156:159], a[28:31]\n"
"v_mfma_f32_16x16x32_bf16 a[24:27], a[52:55], a[156:159], a[24:27]\n"
"ds_read_b128 a[156:159], v4 offset:15360\n"
"v_mfma_f32_16x16x32_bf16 a[24:27], a[48:51], a[152:155], a[24:27]\n"
"s_waitcnt vmcnt(10) lgkmcnt(12)\n"
"ds_read_b128 a[152:155], v4 offset:14336\n"
"v_mfma_f32_16x16x32_bf16 v[144:147], a[64:67], a[96:99], v[144:147]\n"
"ds_read_b128 v[24:27], v2 offset:16384\n"
"v_mfma_f32_16x16x32_bf16 v[144:147], a[68:71], a[100:103], v[144:147]\n"
"global_load_dwordx4 a[48:51], v0, s[34:35] offset:0 sc1\n"
"v_mfma_f32_16x16x32_bf16 v[172:175], a[76:79], a[100:103], v[172:175]\n"
"v_mfma_f32_16x16x32_bf16 v[172:175], a[72:75], a[96:99], v[172:175]\n"
"v_mfma_f32_16x16x32_bf16 v[176:179], a[72:75], a[104:107], v[176:179]\n"
"v_mfma_f32_16x16x32_bf16 v[176:179], a[76:79], a[108:111], v[176:179]\n"
"v_mfma_f32_16x16x32_bf16 v[148:151], a[68:71], a[108:111], v[148:151]\n"
"v_mfma_f32_16x16x32_bf16 v[148:151], a[64:67], a[104:107], v[148:151]\n"
"s_waitcnt lgkmcnt(10)\n"
"v_mfma_f32_16x16x32_bf16 v[152:155], a[64:67], a[112:115], v[152:155]\n"
"v_mfma_f32_16x16x32_bf16 v[152:155], a[68:71], a[116:119], v[152:155]\n"
"global_load_dwordx4 a[52:55], v0, s[34:35] offset:1024 sc1\n"
"v_mfma_f32_16x16x32_bf16 v[180:183], a[76:79], a[116:119], v[180:183]\n"
"v_mfma_f32_16x16x32_bf16 v[180:183], a[72:75], a[112:115], v[180:183]\n"
"v_mfma_f32_16x16x32_bf16 v[184:187], a[72:75], a[120:123], v[184:187]\n"
"s_waitcnt lgkmcnt(0)\n"
"global_store_dwordx4 v0, v[24:27], s[52:53] offset:-4096 sc1\n"
"v_mfma_f32_16x16x32_bf16 v[184:187], a[76:79], a[124:127], v[184:187]\n"
"v_mfma_f32_16x16x32_bf16 v[156:159], a[68:71], a[124:127], v[156:159]\n"
"v_mfma_f32_16x16x32_bf16 v[156:159], a[64:67], a[120:123], v[156:159]\n"
"v_mfma_f32_16x16x32_bf16 v[160:163], a[64:67], a[128:131], v[160:163]\n"
"v_mfma_f32_16x16x32_bf16 v[160:163], a[68:71], a[132:135], v[160:163]\n"
"global_load_dwordx4 a[56:59], v0, s[34:35] offset:2048 sc1\n"
"v_mfma_f32_16x16x32_bf16 v[188:191], a[76:79], a[132:135], v[188:191]\n"
"v_mfma_f32_16x16x32_bf16 v[188:191], a[72:75], a[128:131], v[188:191]\n"
"v_mfma_f32_16x16x32_bf16 v[192:195], a[72:75], a[136:139], v[192:195]\n"
"v_mfma_f32_16x16x32_bf16 v[192:195], a[76:79], a[140:143], v[192:195]\n"
"v_mfma_f32_16x16x32_bf16 v[164:167], a[68:71], a[140:143], v[164:167]\n"
"v_mfma_f32_16x16x32_bf16 v[164:167], a[64:67], a[136:139], v[164:167]\n"
"v_mfma_f32_16x16x32_bf16 v[168:171], a[64:67], a[144:147], v[168:171]\n"
"v_mfma_f32_16x16x32_bf16 v[168:171], a[68:71], a[148:151], v[168:171]\n"
"global_load_dwordx4 a[60:63], v0, s[34:35] offset:3072 sc1\n"
"v_mfma_f32_16x16x32_bf16 v[196:199], a[76:79], a[148:151], v[196:199]\n"
"v_mfma_f32_16x16x32_bf16 v[196:199], a[72:75], a[144:147], v[196:199]\n"
"v_mfma_f32_16x16x32_bf16 a[20:23], a[72:75], a[152:155], a[20:23]\n"
"s_add_u32 s34, s34, 8192\n"
"v_mfma_f32_16x16x32_bf16 a[20:23], a[76:79], a[156:159], a[20:23]\n"
"v_mfma_f32_16x16x32_bf16 a[16:19], a[68:71], a[156:159], a[16:19]\n"
"v_mfma_f32_16x16x32_bf16 a[16:19], a[64:67], a[152:155], a[16:19]\n"
"s_waitcnt vmcnt(10)\n"
"ds_read_b128 v[28:31], v2 offset:17408\n"
"v_mfma_f32_16x16x32_bf16 v[200:203], a[80:83], a[96:99], v[200:203]\n"
"v_mfma_f32_16x16x32_bf16 v[200:203], a[84:87], a[100:103], v[200:203]\n"
"global_load_dwordx4 a[64:67], v0, s[34:35] offset:-4096 sc1\n"
"v_mfma_f32_16x16x32_bf16 v[228:231], a[92:95], a[100:103], v[228:231]\n"
"ds_read_b128 a[100:103], v3 offset:1024\n"
"v_mfma_f32_16x16x32_bf16 v[228:231], a[88:91], a[96:99], v[228:231]\n"
"ds_read_b128 a[96:99], v3 offset:0\n"
"v_mfma_f32_16x16x32_bf16 v[232:235], a[88:91], a[104:107], v[232:235]\n"
"v_mfma_f32_16x16x32_bf16 v[232:235], a[92:95], a[108:111], v[232:235]\n"
"v_mfma_f32_16x16x32_bf16 v[204:207], a[84:87], a[108:111], v[204:207]\n"
"ds_read_b128 a[108:111], v3 offset:3072\n"
"v_mfma_f32_16x16x32_bf16 v[204:207], a[80:83], a[104:107], v[204:207]\n"
"ds_read_b128 a[104:107], v3 offset:2048\n"
"v_mfma_f32_16x16x32_bf16 v[208:211], a[80:83], a[112:115], v[208:211]\n"
"v_mfma_f32_16x16x32_bf16 v[208:211], a[84:87], a[116:119], v[208:211]\n"
"global_load_dwordx4 a[68:71], v0, s[34:35] offset:-3072 sc1\n"
"v_mfma_f32_16x16x32_bf16 v[236:239], a[92:95], a[116:119], v[236:239]\n"
"s_waitcnt lgkmcnt(4)\n"
"global_store_dwordx4 v0, v[28:31], s[52:53] offset:-3072 sc1\n"
"ds_read_b128 a[116:119], v3 offset:5120\n"
"v_mfma_f32_16x16x32_bf16 v[236:239], a[88:91], a[112:115], v[236:239]\n"
"ds_read_b128 a[112:115], v3 offset:4096\n"
"v_mfma_f32_16x16x32_bf16 v[240:243], a[88:91], a[120:123], v[240:243]\n"
"v_mfma_f32_16x16x32_bf16 v[240:243], a[92:95], a[124:127], v[240:243]\n"
"v_mfma_f32_16x16x32_bf16 v[212:215], a[84:87], a[124:127], v[212:215]\n"
"ds_read_b128 a[124:127], v3 offset:7168\n"
"v_mfma_f32_16x16x32_bf16 v[212:215], a[80:83], a[120:123], v[212:215]\n"
"ds_read_b128 a[120:123], v3 offset:6144\n"
"v_mfma_f32_16x16x32_bf16 v[216:219], a[80:83], a[128:131], v[216:219]\n"
"v_mfma_f32_16x16x32_bf16 v[216:219], a[84:87], a[132:135], v[216:219]\n"
"global_load_dwordx4 a[72:75], v0, s[34:35] offset:-2048 sc1\n"
"v_mfma_f32_16x16x32_bf16 v[244:247], a[92:95], a[132:135], v[244:247]\n"
"ds_read_b128 a[132:135], v3 offset:9216\n"
"v_mfma_f32_16x16x32_bf16 v[244:247], a[88:91], a[128:131], v[244:247]\n"
"ds_read_b128 a[128:131], v3 offset:8192\n"
"v_mfma_f32_16x16x32_bf16 v[248:251], a[88:91], a[136:139], v[248:251]\n"
"v_mfma_f32_16x16x32_bf16 v[248:251], a[92:95], a[140:143], v[248:251]\n"
"v_mfma_f32_16x16x32_bf16 v[220:223], a[84:87], a[140:143], v[220:223]\n"
"ds_read_b128 a[140:143], v3 offset:11264\n"
"v_mfma_f32_16x16x32_bf16 v[220:223], a[80:83], a[136:139], v[220:223]\n"
"ds_read_b128 a[136:139], v3 offset:10240\n"
"v_mfma_f32_16x16x32_bf16 v[224:227], a[80:83], a[144:147], v[224:227]\n"
"v_mfma_f32_16x16x32_bf16 v[224:227], a[84:87], a[148:151], v[224:227]\n"
"global_load_dwordx4 a[76:79], v0, s[34:35] offset:-1024 sc1\n"
"v_mfma_f32_16x16x32_bf16 v[252:255], a[92:95], a[148:151], v[252:255]\n"
"ds_read_b128 a[148:151], v3 offset:13312\n"
"v_mfma_f32_16x16x32_bf16 v[252:255], a[88:91], a[144:147], v[252:255]\n"
"ds_read_b128 a[144:147], v3 offset:12288\n"
"v_mfma_f32_16x16x32_bf16 a[28:31], a[88:91], a[152:155], a[28:31]\n"
"v_mfma_f32_16x16x32_bf16 a[28:31], a[92:95], a[156:159], a[28:31]\n"
"ds_read_b128 v[24:27], v2 offset:18432\n"
"v_mfma_f32_16x16x32_bf16 a[24:27], a[84:87], a[156:159], a[24:27]\n"
"ds_read_b128 a[156:159], v3 offset:15360\n"
"v_mfma_f32_16x16x32_bf16 a[24:27], a[80:83], a[152:155], a[24:27]\n"
"s_waitcnt vmcnt(10) lgkmcnt(12)\n"
"ds_read_b128 a[152:155], v3 offset:14336\n"
"v_mfma_f32_16x16x32_bf16 v[144:147], a[32:35], a[96:99], v[144:147]\n"
"v_mfma_f32_16x16x32_bf16 v[144:147], a[36:39], a[100:103], v[144:147]\n"
"global_load_dwordx4 a[80:83], v0, s[34:35] offset:0 sc1\n"
"v_mfma_f32_16x16x32_bf16 v[172:175], a[44:47], a[100:103], v[172:175]\n"
"v_mfma_f32_16x16x32_bf16 v[172:175], a[40:43], a[96:99], v[172:175]\n"
"v_mfma_f32_16x16x32_bf16 v[176:179], a[40:43], a[104:107], v[176:179]\n"
"v_mfma_f32_16x16x32_bf16 v[176:179], a[44:47], a[108:111], v[176:179]\n"
"v_mfma_f32_16x16x32_bf16 v[148:151], a[36:39], a[108:111], v[148:151]\n"
"v_mfma_f32_16x16x32_bf16 v[148:151], a[32:35], a[104:107], v[148:151]\n"
"s_waitcnt lgkmcnt(9)\n"
"v_mfma_f32_16x16x32_bf16 v[152:155], a[32:35], a[112:115], v[152:155]\n"
"s_waitcnt lgkmcnt(2)\n"
"global_store_dwordx4 v0, v[24:27], s[52:53] offset:-2048 sc1\n"
"v_mfma_f32_16x16x32_bf16 v[152:155], a[36:39], a[116:119], v[152:155]\n"
"global_load_dwordx4 a[84:87], v0, s[34:35] offset:1024 sc1\n"
"v_mfma_f32_16x16x32_bf16 v[180:183], a[44:47], a[116:119], v[180:183]\n"
"v_mfma_f32_16x16x32_bf16 v[180:183], a[40:43], a[112:115], v[180:183]\n"
"v_mfma_f32_16x16x32_bf16 v[184:187], a[40:43], a[120:123], v[184:187]\n"
"v_mfma_f32_16x16x32_bf16 v[184:187], a[44:47], a[124:127], v[184:187]\n"
"v_mfma_f32_16x16x32_bf16 v[156:159], a[36:39], a[124:127], v[156:159]\n"
"v_mfma_f32_16x16x32_bf16 v[156:159], a[32:35], a[120:123], v[156:159]\n"
"v_mfma_f32_16x16x32_bf16 v[160:163], a[32:35], a[128:131], v[160:163]\n"
"v_mfma_f32_16x16x32_bf16 v[160:163], a[36:39], a[132:135], v[160:163]\n"
"global_load_dwordx4 a[88:91], v0, s[34:35] offset:2048 sc1\n"
"v_mfma_f32_16x16x32_bf16 v[188:191], a[44:47], a[132:135], v[188:191]\n"
"v_mfma_f32_16x16x32_bf16 v[188:191], a[40:43], a[128:131], v[188:191]\n"
"v_mfma_f32_16x16x32_bf16 v[192:195], a[40:43], a[136:139], v[192:195]\n"
"v_mfma_f32_16x16x32_bf16 v[192:195], a[44:47], a[140:143], v[192:195]\n"
"v_mfma_f32_16x16x32_bf16 v[164:167], a[36:39], a[140:143], v[164:167]\n"
"v_mfma_f32_16x16x32_bf16 v[164:167], a[32:35], a[136:139], v[164:167]\n"
"s_waitcnt lgkmcnt(0)\n"
"v_mfma_f32_16x16x32_bf16 v[168:171], a[32:35], a[144:147], v[168:171]\n"
"v_mfma_f32_16x16x32_bf16 v[168:171], a[36:39], a[148:151], v[168:171]\n"
"global_load_dwordx4 a[92:95], v0, s[34:35] offset:3072 sc1\n"
"v_mfma_f32_16x16x32_bf16 v[196:199], a[44:47], a[148:151], v[196:199]\n"
"v_mfma_f32_16x16x32_bf16 v[196:199], a[40:43], a[144:147], v[196:199]\n"
"ds_read_b128 v[28:31], v2 offset:19456\n"
"v_mfma_f32_16x16x32_bf16 a[20:23], a[40:43], a[152:155], a[20:23]\n"
"s_add_u32 s34, s34, 8192\n"
"v_mfma_f32_16x16x32_bf16 a[20:23], a[44:47], a[156:159], a[20:23]\n"
"v_mfma_f32_16x16x32_bf16 a[16:19], a[36:39], a[156:159], a[16:19]\n"
"v_mfma_f32_16x16x32_bf16 a[16:19], a[32:35], a[152:155], a[16:19]\n"
"s_waitcnt vmcnt(10)\n"
"v_mfma_f32_16x16x32_bf16 v[200:203], a[48:51], a[96:99], v[200:203]\n"
"v_mfma_f32_16x16x32_bf16 v[200:203], a[52:55], a[100:103], v[200:203]\n"
"global_load_dwordx4 a[32:35], v0, s[34:35] offset:-4096 sc1\n"
"v_mfma_f32_16x16x32_bf16 v[228:231], a[60:63], a[100:103], v[228:231]\n"
"ds_read_b128 a[100:103], v2 offset:1024\n"
"v_mfma_f32_16x16x32_bf16 v[228:231], a[56:59], a[96:99], v[228:231]\n"
"ds_read_b128 a[96:99], v2 offset:0\n"
"v_mfma_f32_16x16x32_bf16 v[232:235], a[56:59], a[104:107], v[232:235]\n"
"v_mfma_f32_16x16x32_bf16 v[232:235], a[60:63], a[108:111], v[232:235]\n"
"v_mfma_f32_16x16x32_bf16 v[204:207], a[52:55], a[108:111], v[204:207]\n"
"s_waitcnt lgkmcnt(2)\n"
"global_store_dwordx4 v0, v[28:31], s[52:53] offset:-1024 sc1\n"
"ds_read_b128 a[108:111], v2 offset:3072\n"
"v_mfma_f32_16x16x32_bf16 v[204:207], a[48:51], a[104:107], v[204:207]\n"
"ds_read_b128 a[104:107], v2 offset:2048\n"
"v_mfma_f32_16x16x32_bf16 v[208:211], a[48:51], a[112:115], v[208:211]\n"
"v_mfma_f32_16x16x32_bf16 v[208:211], a[52:55], a[116:119], v[208:211]\n"
"global_load_dwordx4 a[36:39], v0, s[34:35] offset:-3072 sc1\n"
"v_mfma_f32_16x16x32_bf16 v[236:239], a[60:63], a[116:119], v[236:239]\n"
"ds_read_b128 a[116:119], v2 offset:5120\n"
"v_mfma_f32_16x16x32_bf16 v[236:239], a[56:59], a[112:115], v[236:239]\n"
"ds_read_b128 a[112:115], v2 offset:4096\n"
"v_mfma_f32_16x16x32_bf16 v[240:243], a[56:59], a[120:123], v[240:243]\n"
"v_mfma_f32_16x16x32_bf16 v[240:243], a[60:63], a[124:127], v[240:243]\n"
"v_mfma_f32_16x16x32_bf16 v[212:215], a[52:55], a[124:127], v[212:215]\n"
"ds_read_b128 a[124:127], v2 offset:7168\n"
"v_mfma_f32_16x16x32_bf16 v[212:215], a[48:51], a[120:123], v[212:215]\n"
"ds_read_b128 a[120:123], v2 offset:6144\n"
"v_mfma_f32_16x16x32_bf16 v[216:219], a[48:51], a[128:131], v[216:219]\n"
"v_mfma_f32_16x16x32_bf16 v[216:219], a[52:55], a[132:135], v[216:219]\n"
"global_load_dwordx4 a[40:43], v0, s[34:35] offset:-2048 sc1\n"
"v_mfma_f32_16x16x32_bf16 v[244:247], a[60:63], a[132:135], v[244:247]\n"
"ds_read_b128 a[132:135], v2 offset:9216\n"
"v_mfma_f32_16x16x32_bf16 v[244:247], a[56:59], a[128:131], v[244:247]\n"
"ds_read_b128 a[128:131], v2 offset:8192\n"
"v_mfma_f32_16x16x32_bf16 v[248:251], a[56:59], a[136:139], v[248:251]\n"
"v_mfma_f32_16x16x32_bf16 v[248:251], a[60:63], a[140:143], v[248:251]\n"
"v_mfma_f32_16x16x32_bf16 v[220:223], a[52:55], a[140:143], v[220:223]\n"
"ds_read_b128 a[140:143], v2 offset:11264\n"
"v_mfma_f32_16x16x32_bf16 v[220:223], a[48:51], a[136:139], v[220:223]\n"
"ds_read_b128 a[136:139], v2 offset:10240\n"
"v_mfma_f32_16x16x32_bf16 v[224:227], a[48:51], a[144:147], v[224:227]\n"
"ds_read_b128 v[24:27], v2 offset:20480\n"
"v_mfma_f32_16x16x32_bf16 v[224:227], a[52:55], a[148:151], v[224:227]\n"
"global_load_dwordx4 a[44:47], v0, s[34:35] offset:-1024 sc1\n"
"v_mfma_f32_16x16x32_bf16 v[252:255], a[60:63], a[148:151], v[252:255]\n"
"ds_read_b128 a[148:151], v2 offset:13312\n"
"v_mfma_f32_16x16x32_bf16 v[252:255], a[56:59], a[144:147], v[252:255]\n"
"ds_read_b128 a[144:147], v2 offset:12288\n"
"v_mfma_f32_16x16x32_bf16 a[28:31], a[56:59], a[152:155], a[28:31]\n"
"v_mfma_f32_16x16x32_bf16 a[28:31], a[60:63], a[156:159], a[28:31]\n"
"v_mfma_f32_16x16x32_bf16 a[24:27], a[52:55], a[156:159], a[24:27]\n"
"ds_read_b128 a[156:159], v2 offset:15360\n"
"v_mfma_f32_16x16x32_bf16 a[24:27], a[48:51], a[152:155], a[24:27]\n"
"s_waitcnt vmcnt(10) lgkmcnt(12)\n"
"ds_read_b128 a[152:155], v2 offset:14336\n"
"v_mfma_f32_16x16x32_bf16 v[144:147], a[64:67], a[96:99], v[144:147]\n"
"v_mfma_f32_16x16x32_bf16 v[144:147], a[68:71], a[100:103], v[144:147]\n"
"global_load_dwordx4 a[48:51], v0, s[34:35] offset:0 sc1\n"
"v_mfma_f32_16x16x32_bf16 v[172:175], a[76:79], a[100:103], v[172:175]\n"
"v_mfma_f32_16x16x32_bf16 v[172:175], a[72:75], a[96:99], v[172:175]\n"
"v_mfma_f32_16x16x32_bf16 v[176:179], a[72:75], a[104:107], v[176:179]\n"
"s_waitcnt lgkmcnt(4)\n"
"global_store_dwordx4 v0, v[24:27], s[52:53] offset:0 sc1\n"
"v_mfma_f32_16x16x32_bf16 v[176:179], a[76:79], a[108:111], v[176:179]\n"
"v_mfma_f32_16x16x32_bf16 v[148:151], a[68:71], a[108:111], v[148:151]\n"
"v_mfma_f32_16x16x32_bf16 v[148:151], a[64:67], a[104:107], v[148:151]\n"
"v_mfma_f32_16x16x32_bf16 v[152:155], a[64:67], a[112:115], v[152:155]\n"
"v_mfma_f32_16x16x32_bf16 v[152:155], a[68:71], a[116:119], v[152:155]\n"
"global_load_dwordx4 a[52:55], v0, s[34:35] offset:1024 sc1\n"
"v_mfma_f32_16x16x32_bf16 v[180:183], a[76:79], a[116:119], v[180:183]\n"
"v_mfma_f32_16x16x32_bf16 v[180:183], a[72:75], a[112:115], v[180:183]\n"
"v_mfma_f32_16x16x32_bf16 v[184:187], a[72:75], a[120:123], v[184:187]\n"
"v_mfma_f32_16x16x32_bf16 v[184:187], a[76:79], a[124:127], v[184:187]\n"
"v_mfma_f32_16x16x32_bf16 v[156:159], a[68:71], a[124:127], v[156:159]\n"
"v_mfma_f32_16x16x32_bf16 v[156:159], a[64:67], a[120:123], v[156:159]\n"
"v_mfma_f32_16x16x32_bf16 v[160:163], a[64:67], a[128:131], v[160:163]\n"
"v_mfma_f32_16x16x32_bf16 v[160:163], a[68:71], a[132:135], v[160:163]\n"
"global_load_dwordx4 a[56:59], v0, s[34:35] offset:2048 sc1\n"
"v_mfma_f32_16x16x32_bf16 v[188:191], a[76:79], a[132:135], v[188:191]\n"
"v_mfma_f32_16x16x32_bf16 v[188:191], a[72:75], a[128:131], v[188:191]\n"
"v_mfma_f32_16x16x32_bf16 v[192:195], a[72:75], a[136:139], v[192:195]\n"
"v_mfma_f32_16x16x32_bf16 v[192:195], a[76:79], a[140:143], v[192:195]\n"
"v_mfma_f32_16x16x32_bf16 v[164:167], a[68:71], a[140:143], v[164:167]\n"
"v_mfma_f32_16x16x32_bf16 v[164:167], a[64:67], a[136:139], v[164:167]\n"
"s_waitcnt lgkmcnt(0)\n"
"ds_read_b128 v[28:31], v2 offset:21504\n"
"v_mfma_f32_16x16x32_bf16 v[168:171], a[64:67], a[144:147], v[168:171]\n"
"v_mfma_f32_16x16x32_bf16 v[168:171], a[68:71], a[148:151], v[168:171]\n"
"global_load_dwordx4 a[60:63], v0, s[34:35] offset:3072 sc1\n"
"v_mfma_f32_16x16x32_bf16 v[196:199], a[76:79], a[148:151], v[196:199]\n"
"v_mfma_f32_16x16x32_bf16 v[196:199], a[72:75], a[144:147], v[196:199]\n"
"v_mfma_f32_16x16x32_bf16 a[20:23], a[72:75], a[152:155], a[20:23]\n"
"s_add_u32 s34, s34, 8192\n"
"v_mfma_f32_16x16x32_bf16 a[20:23], a[76:79], a[156:159], a[20:23]\n"
"v_mfma_f32_16x16x32_bf16 a[16:19], a[68:71], a[156:159], a[16:19]\n"
"v_mfma_f32_16x16x32_bf16 a[16:19], a[64:67], a[152:155], a[16:19]\n"
"s_waitcnt vmcnt(10)\n"
"v_mfma_f32_16x16x32_bf16 v[200:203], a[80:83], a[96:99], v[200:203]\n"
"v_mfma_f32_16x16x32_bf16 v[200:203], a[84:87], a[100:103], v[200:203]\n"
"global_load_dwordx4 a[64:67], v0, s[34:35] offset:-4096 sc1\n"
"v_mfma_f32_16x16x32_bf16 v[228:231], a[92:95], a[100:103], v[228:231]\n"
"s_waitcnt lgkmcnt(0)\n"
"global_store_dwordx4 v0, v[28:31], s[52:53] offset:1024 sc1\n"
"ds_read_b128 a[100:103], v2 offset:17408\n"
"v_mfma_f32_16x16x32_bf16 v[228:231], a[88:91], a[96:99], v[228:231]\n"
"ds_read_b128 a[96:99], v2 offset:16384\n"
"v_mfma_f32_16x16x32_bf16 v[232:235], a[88:91], a[104:107], v[232:235]\n"
"v_mfma_f32_16x16x32_bf16 v[232:235], a[92:95], a[108:111], v[232:235]\n"
"v_mfma_f32_16x16x32_bf16 v[204:207], a[84:87], a[108:111], v[204:207]\n"
"ds_read_b128 a[108:111], v2 offset:19456\n"
"v_mfma_f32_16x16x32_bf16 v[204:207], a[80:83], a[104:107], v[204:207]\n"
"ds_read_b128 a[104:107], v2 offset:18432\n"
"v_mfma_f32_16x16x32_bf16 v[208:211], a[80:83], a[112:115], v[208:211]\n"
"v_mfma_f32_16x16x32_bf16 v[208:211], a[84:87], a[116:119], v[208:211]\n"
"global_load_dwordx4 a[68:71], v0, s[34:35] offset:-3072 sc1\n"
"v_mfma_f32_16x16x32_bf16 v[236:239], a[92:95], a[116:119], v[236:239]\n"
"ds_read_b128 a[116:119], v2 offset:21504\n"
"v_mfma_f32_16x16x32_bf16 v[236:239], a[88:91], a[112:115], v[236:239]\n"
"ds_read_b128 a[112:115], v2 offset:20480\n"
"v_mfma_f32_16x16x32_bf16 v[240:243], a[88:91], a[120:123], v[240:243]\n"
"v_mfma_f32_16x16x32_bf16 v[240:243], a[92:95], a[124:127], v[240:243]\n"
"v_mfma_f32_16x16x32_bf16 v[212:215], a[84:87], a[124:127], v[212:215]\n"
"ds_read_b128 a[124:127], v2 offset:23552\n"
"v_mfma_f32_16x16x32_bf16 v[212:215], a[80:83], a[120:123], v[212:215]\n"
"ds_read_b128 a[120:123], v2 offset:22528\n"
"v_mfma_f32_16x16x32_bf16 v[216:219], a[80:83], a[128:131], v[216:219]\n"
"v_mfma_f32_16x16x32_bf16 v[216:219], a[84:87], a[132:135], v[216:219]\n"
"global_load_dwordx4 a[72:75], v0, s[34:35] offset:-2048 sc1\n"
"v_mfma_f32_16x16x32_bf16 v[244:247], a[92:95], a[132:135], v[244:247]\n"
"ds_read_b128 a[132:135], v2 offset:25600\n"
"v_mfma_f32_16x16x32_bf16 v[244:247], a[88:91], a[128:131], v[244:247]\n"
"ds_read_b128 a[128:131], v2 offset:24576\n"
"v_mfma_f32_16x16x32_bf16 v[248:251], a[88:91], a[136:139], v[248:251]\n"
"v_mfma_f32_16x16x32_bf16 v[248:251], a[92:95], a[140:143], v[248:251]\n"
"ds_read_b128 v[24:27], v2 offset:22528\n"
"v_mfma_f32_16x16x32_bf16 v[220:223], a[84:87], a[140:143], v[220:223]\n"
"ds_read_b128 a[140:143], v2 offset:27648\n"
"v_mfma_f32_16x16x32_bf16 v[220:223], a[80:83], a[136:139], v[220:223]\n"
"ds_read_b128 a[136:139], v2 offset:26624\n"
"v_mfma_f32_16x16x32_bf16 v[224:227], a[80:83], a[144:147], v[224:227]\n"
"v_mfma_f32_16x16x32_bf16 v[224:227], a[84:87], a[148:151], v[224:227]\n"
"global_load_dwordx4 a[76:79], v0, s[34:35] offset:-1024 sc1\n"
"v_mfma_f32_16x16x32_bf16 v[252:255], a[92:95], a[148:151], v[252:255]\n"
"ds_read_b128 a[148:151], v2 offset:29696\n"
"v_mfma_f32_16x16x32_bf16 v[252:255], a[88:91], a[144:147], v[252:255]\n"
"ds_read_b128 a[144:147], v2 offset:28672\n"
"v_mfma_f32_16x16x32_bf16 a[28:31], a[88:91], a[152:155], a[28:31]\n"
"v_mfma_f32_16x16x32_bf16 a[28:31], a[92:95], a[156:159], a[28:31]\n"
"v_mfma_f32_16x16x32_bf16 a[24:27], a[84:87], a[156:159], a[24:27]\n"
"s_bitcmp0_b32 %26, 0\n"
"ds_read_b128 a[156:159], v2 offset:31744\n"
"v_mfma_f32_16x16x32_bf16 a[24:27], a[80:83], a[152:155], a[24:27]\n"
"ds_read_b128 a[152:155], v2 offset:30720\n"
"s_cbranch_scc1 L_P3F\n"
"s_waitcnt vmcnt(10) lgkmcnt(13)\n"
"v_mfma_f32_16x16x32_bf16 v[32:35], a[32:35], a[96:99], v[32:35]\n"
"s_waitcnt lgkmcnt(6)\n"
"global_store_dwordx4 v0, v[24:27], s[52:53] offset:2048 sc1\n"
"v_mfma_f32_16x16x32_bf16 v[32:35], a[36:39], a[100:103], v[32:35]\n"
"global_load_dwordx4 a[80:83], v0, s[34:35] offset:0 sc1\n"
"v_mfma_f32_16x16x32_bf16 v[60:63], a[44:47], a[100:103], v[60:63]\n"
"s_barrier\n"
"v_mfma_f32_16x16x32_bf16 v[60:63], a[40:43], a[96:99], v[60:63]\n"
"v_mfma_f32_16x16x32_bf16 v[64:67], a[40:43], a[104:107], v[64:67]\n"
"v_mfma_f32_16x16x32_bf16 v[64:67], a[44:47], a[108:111], v[64:67]\n"
"v_mfma_f32_16x16x32_bf16 v[36:39], a[36:39], a[108:111], v[36:39]\n"
"v_mfma_f32_16x16x32_bf16 v[36:39], a[32:35], a[104:107], v[36:39]\n"
"v_mfma_f32_16x16x32_bf16 v[40:43], a[32:35], a[112:115], v[40:43]\n"
"v_mfma_f32_16x16x32_bf16 v[40:43], a[36:39], a[116:119], v[40:43]\n"
"global_load_dwordx4 a[84:87], v0, s[34:35] offset:1024 sc1\n"
"v_mfma_f32_16x16x32_bf16 v[68:71], a[44:47], a[116:119], v[68:71]\n"
"v_mfma_f32_16x16x32_bf16 v[68:71], a[40:43], a[112:115], v[68:71]\n"
"v_mfma_f32_16x16x32_bf16 v[72:75], a[40:43], a[120:123], v[72:75]\n"
"v_mfma_f32_16x16x32_bf16 v[72:75], a[44:47], a[124:127], v[72:75]\n"
"v_mfma_f32_16x16x32_bf16 v[44:47], a[36:39], a[124:127], v[44:47]\n"
"v_mfma_f32_16x16x32_bf16 v[44:47], a[32:35], a[120:123], v[44:47]\n"
"s_waitcnt lgkmcnt(4)\n"
"v_mfma_f32_16x16x32_bf16 v[48:51], a[32:35], a[128:131], v[48:51]\n"
"v_mfma_f32_16x16x32_bf16 v[48:51], a[36:39], a[132:135], v[48:51]\n"
"global_load_dwordx4 a[88:91], v0, s[34:35] offset:2048 sc1\n"
"v_lshrrev_b32 v16, 4, v0\n"
"v_mfma_f32_16x16x32_bf16 v[76:79], a[44:47], a[132:135], v[76:79]\n"
"v_and_b32 v16, 0x30, v16\n"
"v_mfma_f32_16x16x32_bf16 v[76:79], a[40:43], a[128:131], v[76:79]\n"
"ds_read_b128 v[28:31], v2 offset:23552\n"
"buffer_load_dwordx2 v[18:19], v16, s[44:47], s61 offen offset:0\n"
"v_mfma_f32_16x16x32_bf16 v[80:83], a[40:43], a[136:139], v[80:83]\n"
"buffer_load_dwordx2 v[8:9], off, s[56:59], %14 sc1\n"
"v_mfma_f32_16x16x32_bf16 v[80:83], a[44:47], a[140:143], v[80:83]\n"
"v_mfma_f32_16x16x32_bf16 v[52:55], a[36:39], a[140:143], v[52:55]\n"
"v_mfma_f32_16x16x32_bf16 v[52:55], a[32:35], a[136:139], v[52:55]\n"
"s_waitcnt lgkmcnt(1)\n"
"v_mfma_f32_16x16x32_bf16 v[56:59], a[32:35], a[144:147], v[56:59]\n"
"v_mfma_f32_16x16x32_bf16 v[56:59], a[36:39], a[148:151], v[56:59]\n"
"global_load_dwordx4 a[92:95], v0, s[34:35] offset:3072 sc1\n"
"v_lshrrev_b32 v20, 4, v0\n"
"v_mfma_f32_16x16x32_bf16 v[84:87], a[44:47], a[148:151], v[84:87]\n"
"v_and_b32 v20, 0x30, v20\n"
"v_mfma_f32_16x16x32_bf16 v[84:87], a[40:43], a[144:147], v[84:87]\n"
"buffer_load_dwordx2 v[22:23], v20, s[44:47], s61 offen offset:8\n"
"v_mfma_f32_16x16x32_bf16 a[4:7], a[40:43], a[152:155], a[4:7]\n"
"s_add_u32 s34, s34, 8192\n"
"v_mfma_f32_16x16x32_bf16 a[4:7], a[44:47], a[156:159], a[4:7]\n"
"v_mfma_f32_16x16x32_bf16 a[0:3], a[36:39], a[156:159], a[0:3]\n"
"s_waitcnt lgkmcnt(0)\n"
"global_store_dwordx4 v0, v[28:31], s[52:53] offset:3072 sc1\n"
"v_mfma_f32_16x16x32_bf16 a[0:3], a[32:35], a[152:155], a[0:3]\n"
"s_waitcnt vmcnt(14)\n"
"v_mfma_f32_16x16x32_bf16 v[88:91], a[48:51], a[96:99], v[88:91]\n"
"v_mfma_f32_16x16x32_bf16 v[88:91], a[52:55], a[100:103], v[88:91]\n"
"global_load_dwordx4 a[32:35], v0, s[34:35] offset:-4096 sc1\n"
"v_mfma_f32_16x16x32_bf16 v[116:119], a[60:63], a[100:103], v[116:119]\n"
"v_mfma_f32_16x16x32_bf16 v[116:119], a[56:59], a[96:99], v[116:119]\n"
"v_mfma_f32_16x16x32_bf16 v[120:123], a[56:59], a[104:107], v[120:123]\n"
"v_mfma_f32_16x16x32_bf16 v[120:123], a[60:63], a[108:111], v[120:123]\n"
"v_mfma_f32_16x16x32_bf16 v[92:95], a[52:55], a[108:111], v[92:95]\n"
"v_mfma_f32_16x16x32_bf16 v[92:95], a[48:51], a[104:107], v[92:95]\n"
"v_mfma_f32_16x16x32_bf16 v[96:99], a[48:51], a[112:115], v[96:99]\n"
"v_mfma_f32_16x16x32_bf16 v[96:99], a[52:55], a[116:119], v[96:99]\n"
"global_load_dwordx4 a[36:39], v0, s[34:35] offset:-3072 sc1\n"
"v_mfma_f32_16x16x32_bf16 v[124:127], a[60:63], a[116:119], v[124:127]\n"
"v_mfma_f32_16x16x32_bf16 v[124:127], a[56:59], a[112:115], v[124:127]\n"
"v_mfma_f32_16x16x32_bf16 v[128:131], a[56:59], a[120:123], v[128:131]\n"
"v_mfma_f32_16x16x32_bf16 v[128:131], a[60:63], a[124:127], v[128:131]\n"
"v_mfma_f32_16x16x32_bf16 v[100:103], a[52:55], a[124:127], v[100:103]\n"
"v_mfma_f32_16x16x32_bf16 v[100:103], a[48:51], a[120:123], v[100:103]\n"
"v_mfma_f32_16x16x32_bf16 v[104:107], a[48:51], a[128:131], v[104:107]\n"
"ds_read_b128 v[24:27], v2 offset:24576\n"
"v_mfma_f32_16x16x32_bf16 v[104:107], a[52:55], a[132:135], v[104:107]\n"
"global_load_dwordx4 a[40:43], v0, s[34:35] offset:-2048 sc1\n"
"v_mfma_f32_16x16x32_bf16 v[132:135], a[60:63], a[132:135], v[132:135]\n"
"v_mfma_f32_16x16x32_bf16 v[132:135], a[56:59], a[128:131], v[132:135]\n"
"v_mfma_f32_16x16x32_bf16 v[136:139], a[56:59], a[136:139], v[136:139]\n"
"v_mfma_f32_16x16x32_bf16 v[136:139], a[60:63], a[140:143], v[136:139]\n"
"v_mfma_f32_16x16x32_bf16 v[108:111], a[52:55], a[140:143], v[108:111]\n"
"v_mfma_f32_16x16x32_bf16 v[108:111], a[48:51], a[136:139], v[108:111]\n"
"v_mfma_f32_16x16x32_bf16 v[112:115], a[48:51], a[144:147], v[112:115]\n"
"v_mfma_f32_16x16x32_bf16 v[112:115], a[52:55], a[148:151], v[112:115]\n"
"global_load_dwordx4 a[44:47], v0, s[34:35] offset:-1024 sc1\n"
"v_mfma_f32_16x16x32_bf16 v[140:143], a[60:63], a[148:151], v[140:143]\n"
"v_mfma_f32_16x16x32_bf16 v[140:143], a[56:59], a[144:147], v[140:143]\n"
"v_mfma_f32_16x16x32_bf16 a[12:15], a[56:59], a[152:155], a[12:15]\n"
"s_waitcnt lgkmcnt(0)\n"
"global_store_dwordx4 v0, v[24:27], s[54:55] offset:-4096 sc1\n"
"v_mfma_f32_16x16x32_bf16 a[12:15], a[60:63], a[156:159], a[12:15]\n"
"v_mfma_f32_16x16x32_bf16 a[8:11], a[52:55], a[156:159], a[8:11]\n"
"v_mfma_f32_16x16x32_bf16 a[8:11], a[48:51], a[152:155], a[8:11]\n"
"s_waitcnt vmcnt(14)\n"
"v_mfma_f32_16x16x32_bf16 v[144:147], a[64:67], a[96:99], v[144:147]\n"
"v_mfma_f32_16x16x32_bf16 v[144:147], a[68:71], a[100:103], v[144:147]\n"
"global_load_dwordx4 a[48:51], v0, s[34:35] offset:0 sc1\n"
"v_mfma_f32_16x16x32_bf16 v[172:175], a[76:79], a[100:103], v[172:175]\n"
"v_mfma_f32_16x16x32_bf16 v[172:175], a[72:75], a[96:99], v[172:175]\n"
"v_mfma_f32_16x16x32_bf16 v[176:179], a[72:75], a[104:107], v[176:179]\n"
"v_mfma_f32_16x16x32_bf16 v[176:179], a[76:79], a[108:111], v[176:179]\n"
"v_mfma_f32_16x16x32_bf16 v[148:151], a[68:71], a[108:111], v[148:151]\n"
"v_mfma_f32_16x16x32_bf16 v[148:151], a[64:67], a[104:107], v[148:151]\n"
"v_mfma_f32_16x16x32_bf16 v[152:155], a[64:67], a[112:115], v[152:155]\n"
"v_mfma_f32_16x16x32_bf16 v[152:155], a[68:71], a[116:119], v[152:155]\n"
"global_load_dwordx4 a[52:55], v0, s[34:35] offset:1024 sc1\n"
"v_mfma_f32_16x16x32_bf16 v[180:183], a[76:79], a[116:119], v[180:183]\n"
"v_mfma_f32_16x16x32_bf16 v[180:183], a[72:75], a[112:115], v[180:183]\n"
"v_mfma_f32_16x16x32_bf16 v[184:187], a[72:75], a[120:123], v[184:187]\n"
"v_mfma_f32_16x16x32_bf16 v[184:187], a[76:79], a[124:127], v[184:187]\n"
"v_mfma_f32_16x16x32_bf16 v[156:159], a[68:71], a[124:127], v[156:159]\n"
"v_mfma_f32_16x16x32_bf16 v[156:159], a[64:67], a[120:123], v[156:159]\n"
"ds_read_b128 v[28:31], v2 offset:26624\n"
"v_mfma_f32_16x16x32_bf16 v[160:163], a[64:67], a[128:131], v[160:163]\n"
"v_mfma_f32_16x16x32_bf16 v[160:163], a[68:71], a[132:135], v[160:163]\n"
"global_load_dwordx4 a[56:59], v0, s[34:35] offset:2048 sc1\n"
"v_mfma_f32_16x16x32_bf16 v[188:191], a[76:79], a[132:135], v[188:191]\n"
"v_mfma_f32_16x16x32_bf16 v[188:191], a[72:75], a[128:131], v[188:191]\n"
"v_mfma_f32_16x16x32_bf16 v[192:195], a[72:75], a[136:139], v[192:195]\n"
"v_mfma_f32_16x16x32_bf16 v[192:195], a[76:79], a[140:143], v[192:195]\n"
"v_mfma_f32_16x16x32_bf16 v[164:167], a[68:71], a[140:143], v[164:167]\n"
"v_mfma_f32_16x16x32_bf16 v[164:167], a[64:67], a[136:139], v[164:167]\n"
"v_mfma_f32_16x16x32_bf16 v[168:171], a[64:67], a[144:147], v[168:171]\n"
"v_mfma_f32_16x16x32_bf16 v[168:171], a[68:71], a[148:151], v[168:171]\n"
"global_load_dwordx4 a[60:63], v0, s[34:35] offset:3072 sc1\n"
"v_mfma_f32_16x16x32_bf16 v[196:199], a[76:79], a[148:151], v[196:199]\n"
"s_waitcnt lgkmcnt(0)\n"
"global_store_dwordx4 v0, v[28:31], s[54:55] offset:-2048 sc1\n"
"v_mfma_f32_16x16x32_bf16 v[196:199], a[72:75], a[144:147], v[196:199]\n"
"v_mfma_f32_16x16x32_bf16 a[20:23], a[72:75], a[152:155], a[20:23]\n"
"s_add_u32 s34, s34, 8192\n"
"v_mfma_f32_16x16x32_bf16 a[20:23], a[76:79], a[156:159], a[20:23]\n"
"v_mfma_f32_16x16x32_bf16 a[16:19], a[68:71], a[156:159], a[16:19]\n"
"v_mfma_f32_16x16x32_bf16 a[16:19], a[64:67], a[152:155], a[16:19]\n"
"s_waitcnt vmcnt(12)\n"
"v_mfma_f32_16x16x32_bf16 v[200:203], a[80:83], a[96:99], v[200:203]\n"
"v_mfma_f32_16x16x32_bf16 v[200:203], a[84:87], a[100:103], v[200:203]\n"
"global_load_dwordx4 a[64:67], v0, s[34:35] offset:-4096 sc1\n"
"v_mfma_f32_16x16x32_bf16 v[228:231], a[92:95], a[100:103], v[228:231]\n"
"ds_read_b128 a[100:103], v3 offset:17408\n"
"v_mfma_f32_16x16x32_bf16 v[228:231], a[88:91], a[96:99], v[228:231]\n"
"ds_read_b128 a[96:99], v3 offset:16384\n"
"v_mfma_f32_16x16x32_bf16 v[232:235], a[88:91], a[104:107], v[232:235]\n"
"v_mfma_f32_16x16x32_bf16 v[232:235], a[92:95], a[108:111], v[232:235]\n"
"v_mfma_f32_16x16x32_bf16 v[204:207], a[84:87], a[108:111], v[204:207]\n"
"ds_read_b128 a[108:111], v3 offset:19456\n"
"v_mfma_f32_16x16x32_bf16 v[204:207], a[80:83], a[104:107], v[204:207]\n"
"ds_read_b128 a[104:107], v3 offset:18432\n"
"v_mfma_f32_16x16x32_bf16 v[208:211], a[80:83], a[112:115], v[208:211]\n"
"v_mfma_f32_16x16x32_bf16 v[208:211], a[84:87], a[116:119], v[208:211]\n"
"L_st6239:\n"
"s_cbranch_execz L_st8529\n"
"global_load_dwordx4 a[68:71], v0, s[34:35] offset:-3072 sc1\n"
"v_mfma_f32_16x16x32_bf16 v[236:239], a[92:95], a[116:119], v[236:239]\n"
"ds_read_b128 a[116:119], v3 offset:21504\n"
"v_mfma_f32_16x16x32_bf16 v[236:239], a[88:91], a[112:115], v[236:239]\n"
"ds_read_b128 a[112:115], v3 offset:20480\n"
"v_mfma_f32_16x16x32_bf16 v[240:243], a[88:91], a[120:123], v[240:243]\n"
"v_mfma_f32_16x16x32_bf16 v[240:243], a[92:95], a[124:127], v[240:243]\n"
"ds_read_b128 v[24:27], v2 offset:29696\n"
"v_mfma_f32_16x16x32_bf16 v[212:215], a[84:87], a[124:127], v[212:215]\n"
"ds_read_b128 a[124:127], v3 offset:23552\n"
"v_mfma_f32_16x16x32_bf16 v[212:215], a[80:83], a[120:123], v[212:215]\n"
"ds_read_b128 a[120:123], v3 offset:22528\n"
"v_mfma_f32_16x16x32_bf16 v[216:219], a[80:83], a[128:131], v[216:219]\n"
"v_mfma_f32_16x16x32_bf16 v[216:219], a[84:87], a[132:135], v[216:219]\n"
"global_load_dwordx4 a[72:75], v0, s[34:35] offset:-2048 sc1\n"
"v_mfma_f32_16x16x32_bf16 v[244:247], a[92:95], a[132:135], v[244:247]\n"
"ds_read_b128 a[132:135], v3 offset:25600\n"
"v_mfma_f32_16x16x32_bf16 v[244:247], a[88:91], a[128:131], v[244:247]\n"
"ds_read_b128 a[128:131], v3 offset:24576\n"
"v_mfma_f32_16x16x32_bf16 v[248:251], a[88:91], a[136:139], v[248:251]\n"
"v_subrev_u32 v8, s65, v8\n"
"v_mfma_f32_16x16x32_bf16 v[248:251], a[92:95], a[140:143], v[248:251]\n"
"v_xor_b32 v9, s79, v9\n"
"v_mfma_f32_16x16x32_bf16 v[220:223], a[84:87], a[140:143], v[220:223]\n"
"ds_read_b128 a[140:143], v3 offset:27648\n"
"v_or_b32 v8, v8, v9\n"
"v_mfma_f32_16x16x32_bf16 v[220:223], a[80:83], a[136:139], v[220:223]\n"
"ds_read_b128 a[136:139], v3 offset:26624\n"
"v_cmp_lt_u32 vcc, 15, v8\n"
"v_mfma_f32_16x16x32_bf16 v[224:227], a[80:83], a[144:147], v[224:227]\n"
"s_waitcnt lgkmcnt(6)\n"
"global_store_dwordx4 v0, v[24:27], s[54:55] offset:1024 sc1\n"
"s_cbranch_vccnz L_slow0\n"
"L_back0:\n"
"v_mfma_f32_16x16x32_bf16 v[224:227], a[84:87], a[148:151], v[224:227]\n"
"global_load_dwordx4 a[76:79], v0, s[34:35] offset:-1024 sc1\n"
"v_readfirstlane_b32 s66, v9\n"
"v_mfma_f32_16x16x32_bf16 v[252:255], a[92:95], a[148:151], v[252:255]\n"
"ds_read_b128 a[148:151], v3 offset:29696\n"
"v_mfma_f32_16x16x32_bf16 v[252:255], a[88:91], a[144:147], v[252:255]\n"
"s_and_b32 s66, s66, 3\n"
"ds_read_b128 a[144:147], v3 offset:28672\n"
"v_mfma_f32_16x16x32_bf16 a[28:31], a[88:91], a[152:155], a[28:31]\n"
"s_lshl_b32 s69, s66, 13\n"
"v_mfma_f32_16x16x32_bf16 a[28:31], a[92:95], a[156:159], a[28:31]\n"
"s_cmp_eq_u32 s66, 3\n"
"s_cselect_b32 s75, 8192, 0\n"
"v_mfma_f32_16x16x32_bf16 a[24:27], a[84:87], a[156:159], a[24:27]\n"
"s_and_b32 s29, s66, s30\n"
"ds_read_b128 a[156:159], v3 offset:31744\n"
"v_mfma_f32_16x16x32_bf16 a[24:27], a[80:83], a[152:155], a[24:27]\n"
"s_waitcnt vmcnt(11)\n"
"ds_read_b128 a[152:155], v3 offset:30720\n"
"v_mfma_f32_16x16x32_bf16 v[32:35], a[32:35], a[96:99], v[32:35]\n"
"s_xor_b32 s31, s79, s66\n"
"v_mfma_f32_16x16x32_bf16 v[32:35], a[36:39], a[100:103], v[32:35]\n"
"global_load_dwordx4 a[80:83], v0, s[34:35] offset:0 sc1\n"
"v_mfma_f32_16x16x32_bf16 v[60:63], a[44:47], a[100:103], v[60:63]\n"
"v_mfma_f32_16x16x32_bf16 v[60:63], a[40:43], a[96:99], v[60:63]\n"
"v_mfma_f32_16x16x32_bf16 v[64:67], a[40:43], a[104:107], v[64:67]\n"
"v_mfma_f32_16x16x32_bf16 v[64:67], a[44:47], a[108:111], v[64:67]\n"
"v_mfma_f32_16x16x32_bf16 v[36:39], a[36:39], a[108:111], v[36:39]\n"
"s_add_u32 m0, s64, 0\n"
"v_mfma_f32_16x16x32_bf16 v[36:39], a[32:35], a[104:107], v[36:39]\n"
"s_waitcnt lgkmcnt(8)\n"
"v_mfma_f32_16x16x32_bf16 v[40:43], a[32:35], a[112:115], v[40:43]\n"
"v_mfma_f32_16x16x32_bf16 v[40:43], a[36:39], a[116:119], v[40:43]\n"
"global_load_dwordx4 a[84:87], v0, s[34:35] offset:1024 sc1\n"
"v_mfma_f32_16x16x32_bf16 v[68:71], a[44:47], a[116:119], v[68:71]\n"
"v_mfma_f32_16x16x32_bf16 v[68:71], a[40:43], a[112:115], v[68:71]\n"
"v_mfma_f32_16x16x32_bf16 v[72:75], a[40:43], a[120:123], v[72:75]\n"
"v_mfma_f32_16x16x32_bf16 v[72:75], a[44:47], a[124:127], v[72:75]\n"
"v_mfma_f32_16x16x32_bf16 v[44:47], a[36:39], a[124:127], v[44:47]\n"
"v_mfma_f32_16x16x32_bf16 v[44:47], a[32:35], a[120:123], v[44:47]\n"
"s_waitcnt lgkmcnt(4)\n"
"v_mfma_f32_16x16x32_bf16 v[48:51], a[32:35], a[128:131], v[48:51]\n"
"v_mfma_f32_16x16x32_bf16 v[48:51], a[36:39], a[132:135], v[48:51]\n"
"global_load_dwordx4 a[88:91], v0, s[34:35] offset:2048 sc1\n"
"v_mfma_f32_16x16x32_bf16 v[76:79], a[44:47], a[132:135], v[76:79]\n"
"v_mfma_f32_16x16x32_bf16 v[76:79], a[40:43], a[128:131], v[76:79]\n"
"v_mfma_f32_16x16x32_bf16 v[80:83], a[40:43], a[136:139], v[80:83]\n"
"v_mfma_f32_16x16x32_bf16 v[80:83], a[44:47], a[140:143], v[80:83]\n"
"buffer_load_dwordx4 v0, s[48:51], s75 offen offset:0 sc1 lds\n"
"v_mfma_f32_16x16x32_bf16 v[52:55], a[36:39], a[140:143], v[52:55]\n"
"v_mfma_f32_16x16x32_bf16 v[52:55], a[32:35], a[136:139], v[52:55]\n"
"s_waitcnt lgkmcnt(0)\n"
"v_mfma_f32_16x16x32_bf16 v[56:59], a[32:35], a[144:147], v[56:59]\n"
"v_mfma_f32_16x16x32_bf16 v[56:59], a[36:39], a[148:151], v[56:59]\n"
"global_load_dwordx4 a[92:95], v0, s[34:35] offset:3072 sc1\n"
"v_mfma_f32_16x16x32_bf16 v[84:87], a[44:47], a[148:151], v[84:87]\n"
"v_mfma_f32_16x16x32_bf16 v[84:87], a[40:43], a[144:147], v[84:87]\n"
"v_mfma_f32_16x16x32_bf16 a[4:7], a[40:43], a[152:155], a[4:7]\n"
"s_add_u32 s34, s34, 8192\n"
"v_mfma_f32_16x16x32_bf16 a[4:7], a[44:47], a[156:159], a[4:7]\n"
"v_mfma_f32_16x16x32_bf16 a[0:3], a[36:39], a[156:159], a[0:3]\n"
"v_mfma_f32_16x16x32_bf16 a[0:3], a[32:35], a[152:155], a[0:3]\n"
"s_waitcnt vmcnt(11)\n"
"v_mfma_f32_16x16x32_bf16 v[88:91], a[48:51], a[96:99], v[88:91]\n"
"v_mfma_f32_16x16x32_bf16 v[88:91], a[52:55], a[100:103], v[88:91]\n"
"global_load_dwordx4 a[32:35], v0, s[34:35] offset:-4096 sc1\n"
"v_mfma_f32_16x16x32_bf16 v[116:119], a[60:63], a[100:103], v[116:119]\n"
"v_mfma_f32_16x16x32_bf16 v[116:119], a[56:59], a[96:99], v[116:119]\n"
"buffer_load_dwordx4 v0, s[48:51], s75 offen offset:1024 sc1 lds\n"
"v_mfma_f32_16x16x32_bf16 v[120:123], a[56:59], a[104:107], v[120:123]\n"
"v_mfma_f32_16x16x32_bf16 v[120:123], a[60:63], a[108:111], v[120:123]\n"
"v_mfma_f32_16x16x32_bf16 v[92:95], a[52:55], a[108:111], v[92:95]\n"
"v_mfma_f32_16x16x32_bf16 v[92:95], a[48:51], a[104:107], v[92:95]\n"
"v_mfma_f32_16x16x32_bf16 v[96:99], a[48:51], a[112:115], v[96:99]\n"
"v_mfma_f32_16x16x32_bf16 v[96:99], a[52:55], a[116:119], v[96:99]\n"
"global_load_dwordx4 a[36:39], v0, s[34:35] offset:-3072 sc1\n"
"v_mfma_f32_16x16x32_bf16 v[124:127], a[60:63], a[116:119], v[124:127]\n"
"v_mfma_f32_16x16x32_bf16 v[124:127], a[56:59], a[112:115], v[124:127]\n"
"v_mfma_f32_16x16x32_bf16 v[128:131], a[56:59], a[120:123], v[128:131]\n"
"v_mfma_f32_16x16x32_bf16 v[128:131], a[60:63], a[124:127], v[128:131]\n"
"v_mfma_f32_16x16x32_bf16 v[100:103], a[52:55], a[124:127], v[100:103]\n"
"v_mfma_f32_16x16x32_bf16 v[100:103], a[48:51], a[120:123], v[100:103]\n"
"v_mfma_f32_16x16x32_bf16 v[104:107], a[48:51], a[128:131], v[104:107]\n"
"v_mfma_f32_16x16x32_bf16 v[104:107], a[52:55], a[132:135], v[104:107]\n"
"global_load_dwordx4 a[40:43], v0, s[34:35] offset:-2048 sc1\n"
"v_mfma_f32_16x16x32_bf16 v[132:135], a[60:63], a[132:135], v[132:135]\n"
"buffer_load_dwordx4 v0, s[48:51], s75 offen offset:2048 sc1 lds\n"
"v_mfma_f32_16x16x32_bf16 v[132:135], a[56:59], a[128:131], v[132:135]\n"
"v_mfma_f32_16x16x32_bf16 v[136:139], a[56:59], a[136:139], v[136:139]\n"
"v_mfma_f32_16x16x32_bf16 v[136:139], a[60:63], a[140:143], v[136:139]\n"
"v_mfma_f32_16x16x32_bf16 v[108:111], a[52:55], a[140:143], v[108:111]\n"
"v_mfma_f32_16x16x32_bf16 v[108:111], a[48:51], a[136:139], v[108:111]\n"
"v_mfma_f32_16x16x32_bf16 v[112:115], a[48:51], a[144:147], v[112:115]\n"
"v_mfma_f32_16x16x32_bf16 v[112:115], a[52:55], a[148:151], v[112:115]\n"
"global_load_dwordx4 a[44:47], v0, s[34:35] offset:-1024 sc1\n"
"v_mfma_f32_16x16x32_bf16 v[140:143], a[60:63], a[148:151], v[140:143]\n"
"v_mfma_f32_16x16x32_bf16 v[140:143], a[56:59], a[144:147], v[140:143]\n"
"v_mfma_f32_16x16x32_bf16 a[12:15], a[56:59], a[152:155], a[12:15]\n"
"v_mfma_f32_16x16x32_bf16 a[12:15], a[60:63], a[156:159], a[12:15]\n"
"v_mfma_f32_16x16x32_bf16 a[8:11], a[52:55], a[156:159], a[8:11]\n"
"v_mfma_f32_16x16x32_bf16 a[8:11], a[48:51], a[152:155], a[8:11]\n"
"s_waitcnt vmcnt(11)\n"
"v_mfma_f32_16x16x32_bf16 v[144:147], a[64:67], a[96:99], v[144:147]\n"
"buffer_load_dwordx4 v0, s[48:51], s75 offen offset:3072 sc1 lds\n"
"v_mfma_f32_16x16x32_bf16 v[144:147], a[68:71], a[100:103], v[144:147]\n"
"global_load_dwordx4 a[48:51], v0, s[34:35] offset:0 sc1\n"
"v_mfma_f32_16x16x32_bf16 v[172:175], a[76:79], a[100:103], v[172:175]\n"
"v_mfma_f32_16x16x32_bf16 v[172:175], a[72:75], a[96:99], v[172:175]\n"
"v_mfma_f32_16x16x32_bf16 v[176:179], a[72:75], a[104:107], v[176:179]\n"
"v_mfma_f32_16x16x32_bf16 v[176:179], a[76:79], a[108:111], v[176:179]\n"
"v_mfma_f32_16x16x32_bf16 v[148:151], a[68:71], a[108:111], v[148:151]\n"
"v_mfma_f32_16x16x32_bf16 v[148:151], a[64:67], a[104:107], v[148:151]\n"
"v_mfma_f32_16x16x32_bf16 v[152:155], a[64:67], a[112:115], v[152:155]\n"
"v_mfma_f32_16x16x32_bf16 v[152:155], a[68:71], a[116:119], v[152:155]\n"
"global_load_dwordx4 a[52:55], v0, s[34:35] offset:1024 sc1\n"
"v_mfma_f32_16x16x32_bf16 v[180:183], a[76:79], a[116:119], v[180:183]\n"
"v_mfma_f32_16x16x32_bf16 v[180:183], a[72:75], a[112:115], v[180:183]\n"
"v_mfma_f32_16x16x32_bf16 v[184:187], a[72:75], a[120:123], v[184:187]\n"
"v_mfma_f32_16x16x32_bf16 v[184:187], a[76:79], a[124:127], v[184:187]\n"
"v_mfma_f32_16x16x32_bf16 v[156:159], a[68:71], a[124:127], v[156:159]\n"
"v_mfma_f32_16x16x32_bf16 v[156:159], a[64:67], a[120:123], v[156:159]\n"
"s_add_u32 m0, s64, 4096\n"
"v_mfma_f32_16x16x32_bf16 v[160:163], a[64:67], a[128:131], v[160:163]\n"
"v_mfma_f32_16x16x32_bf16 v[160:163], a[68:71], a[132:135], v[160:163]\n"
"global_load_dwordx4 a[56:59], v0, s[34:35] offset:2048 sc1\n"
"v_mfma_f32_16x16x32_bf16 v[188:191], a[76:79], a[132:135], v[188:191]\n"
"v_mfma_f32_16x16x32_bf16 v[188:191], a[72:75], a[128:131], v[188:191]\n"
"v_mfma_f32_16x16x32_bf16 v[192:195], a[72:75], a[136:139], v[192:195]\n"
"v_mfma_f32_16x16x32_bf16 v[192:195], a[76:79], a[140:143], v[192:195]\n"
"v_mfma_f32_16x16x32_bf16 v[164:167], a[68:71], a[140:143], v[164:167]\n"
"v_mfma_f32_16x16x32_bf16 v[164:167], a[64:67], a[136:139], v[164:167]\n"
"v_mfma_f32_16x16x32_bf16 v[168:171], a[64:67], a[144:147], v[168:171]\n"
"v_mfma_f32_16x16x32_bf16 v[168:171], a[68:71], a[148:151], v[168:171]\n"
"global_load_dwordx4 a[60:63], v0, s[34:35] offset:3072 sc1\n"
"v_mfma_f32_16x16x32_bf16 v[196:199], a[76:79], a[148:151], v[196:199]\n"
"v_mfma_f32_16x16x32_bf16 v[196:199], a[72:75], a[144:147], v[196:199]\n"
"v_mfma_f32_16x16x32_bf16 a[20:23], a[72:75], a[152:155], a[20:23]\n"
"s_add_u32 s34, s34, 8192\n"
"v_mfma_f32_16x16x32_bf16 a[20:23], a[76:79], a[156:159], a[20:23]\n"
"v_mfma_f32_16x16x32_bf16 a[16:19], a[68:71], a[156:159], a[16:19]\n"
"buffer_load_dwordx4 v1, s[48:51], s75 offen offset:0 sc1 lds\n"
"v_mfma_f32_16x16x32_bf16 a[16:19], a[64:67], a[152:155], a[16:19]\n"
"s_waitcnt vmcnt(12)\n"
"v_mfma_f32_16x16x32_bf16 v[200:203], a[80:83], a[96:99], v[200:203]\n"
"v_mfma_f32_16x16x32_bf16 v[200:203], a[84:87], a[100:103], v[200:203]\n"
"global_load_dwordx4 a[64:67], v0, s[34:35] offset:-4096 sc1\n"
"s_add_u32 s65, s65, 1\n"
"v_mfma_f32_16x16x32_bf16 v[228:231], a[92:95], a[100:103], v[228:231]\n"
"ds_read_b128 a[100:103], v4 offset:17408\n"
"v_mfma_f32_16x16x32_bf16 v[228:231], a[88:91], a[96:99], v[228:231]\n"
"ds_read_b128 a[96:99], v4 offset:16384\n"
"v_mov_b32 v8, s65\n"
"v_mfma_f32_16x16x32_bf16 v[232:235], a[88:91], a[104:107], v[232:235]\n"
"v_mov_b32 v9, s60\n"
"v_mfma_f32_16x16x32_bf16 v[232:235], a[92:95], a[108:111], v[232:235]\n"
"v_mfma_f32_16x16x32_bf16 v[204:207], a[84:87], a[108:111], v[204:207]\n"
"buffer_store_dwordx2 v[8:9], off, s[56:59], %13 sc1\n"
"ds_read_b128 a[108:111], v4 offset:19456\n"
"v_mfma_f32_16x16x32_bf16 v[204:207], a[80:83], a[104:107], v[204:207]\n"
"ds_read_b128 a[104:107], v4 offset:18432\n"
"v_mfma_f32_16x16x32_bf16 v[208:211], a[80:83], a[112:115], v[208:211]\n"
"v_mfma_f32_16x16x32_bf16 v[208:211], a[84:87], a[116:119], v[208:211]\n"
"global_load_dwordx4 a[68:71], v0, s[34:35] offset:-3072 sc1\n"
"v_mfma_f32_16x16x32_bf16 v[236:239], a[92:95], a[116:119], v[236:239]\n"
"ds_read_b128 a[116:119], v4 offset:21504\n"
"v_mfma_f32_16x16x32_bf16 v[236:239], a[88:91], a[112:115], v[236:239]\n"
"ds_read_b128 a[112:115], v4 offset:20480\n"
"v_mfma_f32_16x16x32_bf16 v[240:243], a[88:91], a[120:123], v[240:243]\n"
"v_mfma_f32_16x16x32_bf16 v[240:243], a[92:95], a[124:127], v[240:243]\n"
"buffer_load_dwordx4 v1, s[48:51], s75 offen offset:1024 sc1 lds\n"
"v_mfma_f32_16x16x32_bf16 v[212:215], a[84:87], a[124:127], v[212:215]\n"
"ds_read_b128 a[124:127], v4 offset:23552\n"
"v_mfma_f32_16x16x32_bf16 v[212:215], a[80:83], a[120:123], v[212:215]\n"
"ds_read_b128 a[120:123], v4 offset:22528\n"
"v_mfma_f32_16x16x32_bf16 v[216:219], a[80:83], a[128:131], v[216:219]\n"
"v_mfma_f32_16x16x32_bf16 v[216:219], a[84:87], a[132:135], v[216:219]\n"
"global_load_dwordx4 a[72:75], v0, s[34:35] offset:-2048 sc1\n"
"v_mfma_f32_16x16x32_bf16 v[244:247], a[92:95], a[132:135], v[244:247]\n"
"ds_read_b128 a[132:135], v4 offset:25600\n"
"v_mfma_f32_16x16x32_bf16 v[244:247], a[88:91], a[128:131], v[244:247]\n"
"ds_read_b128 a[128:131], v4 offset:24576\n"
"v_mfma_f32_16x16x32_bf16 v[248:251], a[88:91], a[136:139], v[248:251]\n"
"v_mfma_f32_16x16x32_bf16 v[248:251], a[92:95], a[140:143], v[248:251]\n"
"v_mfma_f32_16x16x32_bf16 v[220:223], a[84:87], a[140:143], v[220:223]\n"
"ds_read_b128 a[140:143], v4 offset:27648\n"
"v_mfma_f32_16x16x32_bf16 v[220:223], a[80:83], a[136:139], v[220:223]\n"
"ds_read_b128 a[136:139], v4 offset:26624\n"
"v_mfma_f32_16x16x32_bf16 v[224:227], a[80:83], a[144:147], v[224:227]\n"
"v_mfma_f32_16x16x32_bf16 v[224:227], a[84:87], a[148:151], v[224:227]\n"
"global_load_dwordx4 a[76:79], v0, s[34:35] offset:-1024 sc1\n"
"v_mfma_f32_16x16x32_bf16 v[252:255], a[92:95], a[148:151], v[252:255]\n"
"ds_read_b128 a[148:151], v4 offset:29696\n"
"v_mfma_f32_16x16x32_bf16 v[252:255], a[88:91], a[144:147], v[252:255]\n"
"ds_read_b128 a[144:147], v4 offset:28672\n"
"v_mfma_f32_16x16x32_bf16 a[28:31], a[88:91], a[152:155], a[28:31]\n"
"buffer_load_dwordx4 v1, s[48:51], s75 offen offset:2048 sc1 lds\n"
"v_mfma_f32_16x16x32_bf16 a[28:31], a[92:95], a[156:159], a[28:31]\n"
"v_mfma_f32_16x16x32_bf16 a[24:27], a[84:87], a[156:159], a[24:27]\n"
"ds_read_b128 a[156:159], v4 offset:31744\n"
"v_mfma_f32_16x16x32_bf16 a[24:27], a[80:83], a[152:155], a[24:27]\n"
"s_waitcnt vmcnt(13) lgkmcnt(11)\n"
"ds_read_b128 a[152:155], v4 offset:30720\n"
"v_mfma_f32_16x16x32_bf16 v[32:35], a[32:35], a[96:99], v[32:35]\n"
"v_mfma_f32_16x16x32_bf16 v[32:35], a[36:39], a[100:103], v[32:35]\n"
"global_load_dwordx4 a[80:83], v0, s[34:35] offset:0 sc1\n"
"v_mfma_f32_16x16x32_bf16 v[60:63], a[44:47], a[100:103], v[60:63]\n"
"v_mfma_f32_16x16x32_bf16 v[60:63], a[40:43], a[96:99], v[60:63]\n"
"v_mfma_f32_16x16x32_bf16 v[64:67], a[40:43], a[104:107], v[64:67]\n"
"v_mfma_f32_16x16x32_bf16 v[64:67], a[44:47], a[108:111], v[64:67]\n"
"v_mfma_f32_16x16x32_bf16 v[36:39], a[36:39], a[108:111], v[36:39]\n"
"v_mfma_f32_16x16x32_bf16 v[36:39], a[32:35], a[104:107], v[36:39]\n"
"s_waitcnt lgkmcnt(8)\n"
"v_mfma_f32_16x16x32_bf16 v[40:43], a[32:35], a[112:115], v[40:43]\n"
"v_mfma_f32_16x16x32_bf16 v[40:43], a[36:39], a[116:119], v[40:43]\n"
"global_load_dwordx4 a[84:87], v0, s[34:35] offset:1024 sc1\n"
"v_mfma_f32_16x16x32_bf16 v[68:71], a[44:47], a[116:119], v[68:71]\n"
"buffer_load_dwordx4 v1, s[48:51], s75 offen offset:3072 sc1 lds\n"
"v_mfma_f32_16x16x32_bf16 v[68:71], a[40:43], a[112:115], v[68:71]\n"
"v_mfma_f32_16x16x32_bf16 v[72:75], a[40:43], a[120:123], v[72:75]\n"
"v_mfma_f32_16x16x32_bf16 v[72:75], a[44:47], a[124:127], v[72:75]\n"
"v_mfma_f32_16x16x32_bf16 v[44:47], a[36:39], a[124:127], v[44:47]\n"
"v_mfma_f32_16x16x32_bf16 v[44:47], a[32:35], a[120:123], v[44:47]\n"
"s_waitcnt lgkmcnt(4)\n"
"v_mfma_f32_16x16x32_bf16 v[48:51], a[32:35], a[128:131], v[48:51]\n"
"v_mfma_f32_16x16x32_bf16 v[48:51], a[36:39], a[132:135], v[48:51]\n"
"global_load_dwordx4 a[88:91], v0, s[34:35] offset:2048 sc1\n"
"v_mfma_f32_16x16x32_bf16 v[76:79], a[44:47], a[132:135], v[76:79]\n"
"v_mfma_f32_16x16x32_bf16 v[76:79], a[40:43], a[128:131], v[76:79]\n"
"v_mfma_f32_16x16x32_bf16 v[80:83], a[40:43], a[136:139], v[80:83]\n"
"v_mfma_f32_16x16x32_bf16 v[80:83], a[44:47], a[140:143], v[80:83]\n"
"v_mfma_f32_16x16x32_bf16 v[52:55], a[36:39], a[140:143], v[52:55]\n"
"v_mfma_f32_16x16x32_bf16 v[52:55], a[32:35], a[136:139], v[52:55]\n"
"s_waitcnt lgkmcnt(0)\n"
"v_mfma_f32_16x16x32_bf16 v[56:59], a[32:35], a[144:147], v[56:59]\n"
"v_mfma_f32_16x16x32_bf16 v[56:59], a[36:39], a[148:151], v[56:59]\n"
"s_add_u32 m0, s64, 8192\n"
"global_load_dwordx4 a[92:95], v0, s[34:35] offset:3072 sc1\n"
"v_mfma_f32_16x16x32_bf16 v[84:87], a[44:47], a[148:151], v[84:87]\n"
"v_mfma_f32_16x16x32_bf16 v[84:87], a[40:43], a[144:147], v[84:87]\n"
"v_mfma_f32_16x16x32_bf16 a[4:7], a[40:43], a[152:155], a[4:7]\n"
"s_add_u32 s34, s34, 8192\n"
"v_mfma_f32_16x16x32_bf16 a[4:7], a[44:47], a[156:159], a[4:7]\n"
"v_mfma_f32_16x16x32_bf16 a[0:3], a[36:39], a[156:159], a[0:3]\n"
"v_mfma_f32_16x16x32_bf16 a[0:3], a[32:35], a[152:155], a[0:3]\n"
"s_waitcnt vmcnt(13)\n"
"v_mfma_f32_16x16x32_bf16 v[88:91], a[48:51], a[96:99], v[88:91]\n"
"v_mfma_f32_16x16x32_bf16 v[88:91], a[52:55], a[100:103], v[88:91]\n"
"global_load_dwordx4 a[32:35], v0, s[34:35] offset:-4096 sc1\n"
"v_mfma_f32_16x16x32_bf16 v[116:119], a[60:63], a[100:103], v[116:119]\n"
"v_mfma_f32_16x16x32_bf16 v[116:119], a[56:59], a[96:99], v[116:119]\n"
"v_mfma_f32_16x16x32_bf16 v[120:123], a[56:59], a[104:107], v[120:123]\n"
"v_mfma_f32_16x16x32_bf16 v[120:123], a[60:63], a[108:111], v[120:123]\n"
"v_mfma_f32_16x16x32_bf16 v[92:95], a[52:55], a[108:111], v[92:95]\n"
"v_mfma_f32_16x16x32_bf16 v[92:95], a[48:51], a[104:107], v[92:95]\n"
"v_mfma_f32_16x16x32_bf16 v[96:99], a[48:51], a[112:115], v[96:99]\n"
"buffer_load_dwordx4 v0, s[48:51], s69 offen offset:0 sc1 lds\n"
"v_mfma_f32_16x16x32_bf16 v[96:99], a[52:55], a[116:119], v[96:99]\n"
"global_load_dwordx4 a[36:39], v0, s[34:35] offset:-3072 sc1\n"
"v_mfma_f32_16x16x32_bf16 v[124:127], a[60:63], a[116:119], v[124:127]\n"
"v_mfma_f32_16x16x32_bf16 v[124:127], a[56:59], a[112:115], v[124:127]\n"
"v_mfma_f32_16x16x32_bf16 v[128:131], a[56:59], a[120:123], v[128:131]\n"
"v_mfma_f32_16x16x32_bf16 v[128:131], a[60:63], a[124:127], v[128:131]\n"
"v_mfma_f32_16x16x32_bf16 v[100:103], a[52:55], a[124:127], v[100:103]\n"
"v_mfma_f32_16x16x32_bf16 v[100:103], a[48:51], a[120:123], v[100:103]\n"
"v_mfma_f32_16x16x32_bf16 v[104:107], a[48:51], a[128:131], v[104:107]\n"
"v_mfma_f32_16x16x32_bf16 v[104:107], a[52:55], a[132:135], v[104:107]\n"
"global_load_dwordx4 a[40:43], v0, s[34:35] offset:-2048 sc1\n"
"v_mfma_f32_16x16x32_bf16 v[132:135], a[60:63], a[132:135], v[132:135]\n"
"v_mfma_f32_16x16x32_bf16 v[132:135], a[56:59], a[128:131], v[132:135]\n"
"v_mfma_f32_16x16x32_bf16 v[136:139], a[56:59], a[136:139], v[136:139]\n"
"v_mfma_f32_16x16x32_bf16 v[136:139], a[60:63], a[140:143], v[136:139]\n"
"v_mfma_f32_16x16x32_bf16 v[108:111], a[52:55], a[140:143], v[108:111]\n"
"buffer_load_dwordx4 v0, s[48:51], s69 offen offset:1024 sc1 lds\n"
"v_mfma_f32_16x16x32_bf16 v[108:111], a[48:51], a[136:139], v[108:111]\n"
"v_mfma_f32_16x16x32_bf16 v[112:115], a[48:51], a[144:147], v[112:115]\n"
"v_mfma_f32_16x16x32_bf16 v[112:115], a[52:55], a[148:151], v[112:115]\n"
"global_load_dwordx4 a[44:47], v0, s[34:35] offset:-1024 sc1\n"
"v_mfma_f32_16x16x32_bf16 v[140:143], a[60:63], a[148:151], v[140:143]\n"
"v_mfma_f32_16x16x32_bf16 v[140:143], a[56:59], a[144:147], v[140:143]\n"
"v_mfma_f32_16x16x32_bf16 a[12:15], a[56:59], a[152:155], a[12:15]\n"
"v_mfma_f32_16x16x32_bf16 a[12:15], a[60:63], a[156:159], a[12:15]\n"
"v_mfma_f32_16x16x32_bf16 a[8:11], a[52:55], a[156:159], a[8:11]\n"
"v_mfma_f32_16x16x32_bf16 a[8:11], a[48:51], a[152:155], a[8:11]\n"
"s_waitcnt vmcnt(12)\n"
"v_mfma_f32_16x16x32_bf16 v[144:147], a[64:67], a[96:99], v[144:147]\n"
"v_mfma_f32_16x16x32_bf16 v[144:147], a[68:71], a[100:103], v[144:147]\n"
"global_load_dwordx4 a[48:51], v0, s[34:35] offset:0 sc1\n"
"v_mfma_f32_16x16x32_bf16 v[172:175], a[76:79], a[100:103], v[172:175]\n"
"v_mfma_f32_16x16x32_bf16 v[172:175], a[72:75], a[96:99], v[172:175]\n"
"v_mfma_f32_16x16x32_bf16 v[176:179], a[72:75], a[104:107], v[176:179]\n"
"v_mfma_f32_16x16x32_bf16 v[176:179], a[76:79], a[108:111], v[176:179]\n"
"buffer_load_dwordx4 v0, s[48:51], s69 offen offset:2048 sc1 lds\n"
"v_mfma_f32_16x16x32_bf16 v[148:151], a[68:71], a[108:111], v[148:151]\n"
"v_mfma_f32_16x16x32_bf16 v[148:151], a[64:67], a[104:107], v[148:151]\n"
"v_mfma_f32_16x16x32_bf16 v[152:155], a[64:67], a[112:115], v[152:155]\n"
"v_lshlrev_b32 v16, 16, v18\n"
"v_mfma_f32_16x16x32_bf16 v[152:155], a[68:71], a[116:119], v[152:155]\n"
"global_load_dwordx4 a[52:55], v0, s[34:35] offset:1024 sc1\n"
"v_and_b32 v17, s63, v18\n"
"v_mfma_f32_16x16x32_bf16 v[180:183], a[76:79], a[116:119], v[180:183]\n"
"v_lshlrev_b32 v18, 16, v19\n"
"v_mfma_f32_16x16x32_bf16 v[180:183], a[72:75], a[112:115], v[180:183]\n"
"v_and_b32 v19, s63, v19\n"
"v_mfma_f32_16x16x32_bf16 v[184:187], a[72:75], a[120:123], v[184:187]\n"
"v_add_f32 v32, v32, v16\n"
"v_mfma_f32_16x16x32_bf16 v[184:187], a[76:79], a[124:127], v[184:187]\n"
"v_add_f32 v33, v33, v17\n"
"v_add_f32 v34, v34, v18\n"
"v_mfma_f32_16x16x32_bf16 v[156:159], a[68:71], a[124:127], v[156:159]\n"
"v_add_f32 v35, v35, v19\n"
"v_mfma_f32_16x16x32_bf16 v[156:159], a[64:67], a[120:123], v[156:159]\n"
"v_add_f32 v36, v36, v16\n"
"v_mfma_f32_16x16x32_bf16 v[160:163], a[64:67], a[128:131], v[160:163]\n"
"v_add_f32 v37, v37, v17\n"
"v_mfma_f32_16x16x32_bf16 v[160:163], a[68:71], a[132:135], v[160:163]\n"
"global_load_dwordx4 a[56:59], v0, s[34:35] offset:2048 sc1\n"
"v_add_f32 v38, v38, v18\n"
"v_mfma_f32_16x16x32_bf16 v[188:191], a[76:79], a[132:135], v[188:191]\n"
"v_add_f32 v39, v39, v19\n"
"v_mfma_f32_16x16x32_bf16 v[188:191], a[72:75], a[128:131], v[188:191]\n"
"v_add_f32 v40, v40, v16\n"
"v_add_f32 v41, v41, v17\n"
"v_mfma_f32_16x16x32_bf16 v[192:195], a[72:75], a[136:139], v[192:195]\n"
"buffer_load_dwordx4 v0, s[48:51], s69 offen offset:3072 sc1 lds\n"
"v_mfma_f32_16x16x32_bf16 v[192:195], a[76:79], a[140:143], v[192:195]\n"
"v_add_f32 v42, v42, v18\n"
"v_mfma_f32_16x16x32_bf16 v[164:167], a[68:71], a[140:143], v[164:167]\n"
"v_add_f32 v43, v43, v19\n"
"v_mfma_f32_16x16x32_bf16 v[164:167], a[64:67], a[136:139], v[164:167]\n"
"v_add_f32 v44, v44, v16\n"
"v_add_f32 v45, v45, v17\n"
"v_mfma_f32_16x16x32_bf16 v[168:171], a[64:67], a[144:147], v[168:171]\n"
"v_add_f32 v46, v46, v18\n"
"v_mfma_f32_16x16x32_bf16 v[168:171], a[68:71], a[148:151], v[168:171]\n"
"global_load_dwordx4 a[60:63], v0, s[34:35] offset:3072 sc1\n"
"v_add_f32 v47, v47, v19\n"
"v_mfma_f32_16x16x32_bf16 v[196:199], a[76:79], a[148:151], v[196:199]\n"
"v_add_f32 v48, v48, v16\n"
"v_mfma_f32_16x16x32_bf16 v[196:199], a[72:75], a[144:147], v[196:199]\n"
"v_add_f32 v49, v49, v17\n"
"v_mfma_f32_16x16x32_bf16 a[20:23], a[72:75], a[152:155], a[20:23]\n"
"s_add_u32 s34, s34, 8192\n"
"v_add_f32 v50, v50, v18\n"
"v_mfma_f32_16x16x32_bf16 a[20:23], a[76:79], a[156:159], a[20:23]\n"
"v_add_f32 v51, v51, v19\n"
"v_mfma_f32_16x16x32_bf16 a[16:19], a[68:71], a[156:159], a[16:19]\n"
"v_add_f32 v52, v52, v16\n"
"v_mfma_f32_16x16x32_bf16 a[16:19], a[64:67], a[152:155], a[16:19]\n"
"s_waitcnt vmcnt(12)\n"
"v_add_f32 v53, v53, v17\n"
"v_mfma_f32_16x16x32_bf16 v[200:203], a[80:83], a[96:99], v[200:203]\n"
"v_add_f32 v54, v54, v18\n"
"v_add_f32 v55, v55, v19\n"
"v_mfma_f32_16x16x32_bf16 v[200:203], a[84:87], a[100:103], v[200:203]\n"
"global_load_dwordx4 a[64:67], v0, s[34:35] offset:-4096 sc1\n"
"v_add_f32 v56, v56, v16\n"
"v_mfma_f32_16x16x32_bf16 v[228:231], a[92:95], a[100:103], v[228:231]\n"
"s_add_u32 m0, s64, 12288\n"
"ds_read_b128 a[100:103], v5 offset:17408\n"
"v_add_f32 v57, v57, v17\n"
"v_mfma_f32_16x16x32_bf16 v[228:231], a[88:91], a[96:99], v[228:231]\n"
"ds_read_b128 a[96:99], v5 offset:16384\n"
"v_add_f32 v58, v58, v18\n"
"v_mfma_f32_16x16x32_bf16 v[232:235], a[88:91], a[104:107], v[232:235]\n"
"v_add_f32 v59, v59, v19\n"
"v_mfma_f32_16x16x32_bf16 v[232:235], a[92:95], a[108:111], v[232:235]\n"
"v_accvgpr_read_b32 v12, a0\n"
"v_mfma_f32_16x16x32_bf16 v[204:207], a[84:87], a[108:111], v[204:207]\n"
"ds_read_b128 a[108:111], v5 offset:19456\n"
"v_accvgpr_read_b32 v13, a1\n"
"v_mfma_f32_16x16x32_bf16 v[204:207], a[80:83], a[104:107], v[204:207]\n"
"ds_read_b128 a[104:107], v5 offset:18432\n"
"v_accvgpr_read_b32 v14, a2\n"
"v_mfma_f32_16x16x32_bf16 v[208:211], a[80:83], a[112:115], v[208:211]\n"
"v_accvgpr_read_b32 v15, a3\n"
"v_mfma_f32_16x16x32_bf16 v[208:211], a[84:87], a[116:119], v[208:211]\n"
"global_load_dwordx4 a[68:71], v0, s[34:35] offset:-3072 sc1\n"
"v_add_f32 v12, v12, v16\n"
"v_mfma_f32_16x16x32_bf16 v[236:239], a[92:95], a[116:119], v[236:239]\n"
"ds_read_b128 a[116:119], v5 offset:21504\n"
"v_add_f32 v13, v13, v17\n"
"v_mfma_f32_16x16x32_bf16 v[236:239], a[88:91], a[112:115], v[236:239]\n"
"ds_read_b128 a[112:115], v5 offset:20480\n"
"v_add_f32 v14, v14, v18\n"
"v_mfma_f32_16x16x32_bf16 v[240:243], a[88:91], a[120:123], v[240:243]\n"
"v_add_f32 v15, v15, v19\n"
"v_accvgpr_write_b32 a0, v12\n"
"v_mfma_f32_16x16x32_bf16 v[240:243], a[92:95], a[124:127], v[240:243]\n"
"v_accvgpr_write_b32 a1, v13\n"
"v_mfma_f32_16x16x32_bf16 v[212:215], a[84:87], a[124:127], v[212:215]\n"
"ds_read_b128 a[124:127], v5 offset:23552\n"
"v_accvgpr_write_b32 a2, v14\n"
"v_mfma_f32_16x16x32_bf16 v[212:215], a[80:83], a[120:123], v[212:215]\n"
"ds_read_b128 a[120:123], v5 offset:22528\n"
"v_accvgpr_write_b32 a3, v15\n"
"v_mfma_f32_16x16x32_bf16 v[216:219], a[80:83], a[128:131], v[216:219]\n"
"buffer_load_dwordx4 v1, s[48:51], s69 offen offset:0 sc1 lds\n"
"v_mfma_f32_16x16x32_bf16 v[216:219], a[84:87], a[132:135], v[216:219]\n"
"global_load_dwordx4 a[72:75], v0, s[34:35] offset:-2048 sc1\n"
"v_lshrrev_b32 v16, 4, v0\n"
"v_mfma_f32_16x16x32_bf16 v[244:247], a[92:95], a[132:135], v[244:247]\n"
"ds_read_b128 a[132:135], v5 offset:25600\n"
"v_and_b32 v16, 0x30, v16\n"
"v_mfma_f32_16x16x32_bf16 v[244:247], a[88:91], a[128:131], v[244:247]\n"
"ds_read_b128 a[128:131], v5 offset:24576\n"
"buffer_load_dwordx2 v[18:19], v16, s[44:47], s61 offen offset:64\n"
"v_mfma_f32_16x16x32_bf16 v[248:251], a[88:91], a[136:139], v[248:251]\n"
"s_getpc_b64 s[96:97]\n"
"L_c3_:\n"
"s_add_u32 s96, s96, L_last-L_c3_\n"
"s_addc_u32 s97, s97, 0\n"
"v_mfma_f32_16x16x32_bf16 v[248:251], a[92:95], a[140:143], v[248:251]\n"
"s_lshr_b32 s66, %26, 3\n"
"v_mfma_f32_16x16x32_bf16 v[220:223], a[84:87], a[140:143], v[220:223]\n"
"s_lshl2_add_u32 s66, s66, %16\n"
"ds_read_b128 a[140:143], v5 offset:27648\n"
"v_mfma_f32_16x16x32_bf16 v[220:223], a[80:83], a[136:139], v[220:223]\n"
"ds_read_b128 a[136:139], v5 offset:26624\n"
"v_mfma_f32_16x16x32_bf16 v[224:227], a[80:83], a[144:147], v[224:227]\n"
"s_mul_i32 s66, s66, 256\n"
"v_mfma_f32_16x16x32_bf16 v[224:227], a[84:87], a[148:151], v[224:227]\n"
"global_load_dwordx4 a[76:79], v0, s[34:35] offset:-1024 sc1\n"
"v_mfma_f32_16x16x32_bf16 v[252:255], a[92:95], a[148:151], v[252:255]\n"
"ds_read_b128 a[148:151], v5 offset:29696\n"
"v_mfma_f32_16x16x32_bf16 v[252:255], a[88:91], a[144:147], v[252:255]\n"
"s_min_u32 s66, s66, L_headF-L_last-256\n"
"ds_read_b128 a[144:147], v5 offset:28672\n"
"v_mfma_f32_16x16x32_bf16 a[28:31], a[88:91], a[152:155], a[28:31]\n"
"v_mfma_f32_16x16x32_bf16 a[28:31], a[92:95], a[156:159], a[28:31]\n"
"v_min_u32 v11, 16, v0\n"
"v_mfma_f32_16x16x32_bf16 a[24:27], a[84:87], a[156:159], a[24:27]\n"
"ds_read_b128 a[156:159], v5 offset:31744\n"
"v_mfma_f32_16x16x32_bf16 a[24:27], a[80:83], a[152:155], a[24:27]\n"
"s_waitcnt vmcnt(12) lgkmcnt(11)\n"
"ds_read_b128 a[152:155], v5 offset:30720\n"
"v_mfma_f32_16x16x32_bf16 v[32:35], a[32:35], a[96:99], v[32:35]\n"
"buffer_load_dwordx4 v1, s[48:51], s69 offen offset:1024 sc1 lds\n"
"v_mfma_f32_16x16x32_bf16 v[32:35], a[36:39], a[100:103], v[32:35]\n"
"global_load_dwordx4 a[80:83], v0, s[34:35] offset:0 sc1\n"
"v_mfma_f32_16x16x32_bf16 v[60:63], a[44:47], a[100:103], v[60:63]\n"
"v_lshl_add_u32 v11, v11, 3, s66\n"
"v_mfma_f32_16x16x32_bf16 v[60:63], a[40:43], a[96:99], v[60:63]\n"
"global_load_dword v10, v11, s[96:97]\n"
"v_mfma_f32_16x16x32_bf16 v[64:67], a[40:43], a[104:107], v[64:67]\n"
"v_mfma_f32_16x16x32_bf16 v[64:67], a[44:47], a[108:111], v[64:67]\n"
"v_mfma_f32_16x16x32_bf16 v[36:39], a[36:39], a[108:111], v[36:39]\n"
"v_mfma_f32_16x16x32_bf16 v[36:39], a[32:35], a[104:107], v[36:39]\n"
"s_waitcnt lgkmcnt(8)\n"
"v_mfma_f32_16x16x32_bf16 v[40:43], a[32:35], a[112:115], v[40:43]\n"
"v_mfma_f32_16x16x32_bf16 v[40:43], a[36:39], a[116:119], v[40:43]\n"
"global_load_dwordx4 a[84:87], v0, s[34:35] offset:1024 sc1\n"
"v_mfma_f32_16x16x32_bf16 v[68:71], a[44:47], a[116:119], v[68:71]\n"
"v_mfma_f32_16x16x32_bf16 v[68:71], a[40:43], a[112:115], v[68:71]\n"
"v_mfma_f32_16x16x32_bf16 v[72:75], a[40:43], a[120:123], v[72:75]\n"
"v_mfma_f32_16x16x32_bf16 v[72:75], a[44:47], a[124:127], v[72:75]\n"
"v_mfma_f32_16x16x32_bf16 v[44:47], a[36:39], a[124:127], v[44:47]\n"
"buffer_load_dwordx4 v1, s[48:51], s69 offen offset:2048 sc1 lds\n"
"v_mfma_f32_16x16x32_bf16 v[44:47], a[32:35], a[120:123], v[44:47]\n"
"s_waitcnt lgkmcnt(4)\n"
"v_mfma_f32_16x16x32_bf16 v[48:51], a[32:35], a[128:131], v[48:51]\n"
"v_mfma_f32_16x16x32_bf16 v[48:51], a[36:39], a[132:135], v[48:51]\n"
"global_load_dwordx4 a[88:91], v0, s[34:35] offset:2048 sc1\n"
"v_mfma_f32_16x16x32_bf16 v[76:79], a[44:47], a[132:135], v[76:79]\n"
"v_mfma_f32_16x16x32_bf16 v[76:79], a[40:43], a[128:131], v[76:79]\n"
"v_mfma_f32_16x16x32_bf16 v[80:83], a[40:43], a[136:139], v[80:83]\n"
"v_mfma_f32_16x16x32_bf16 v[80:83], a[44:47], a[140:143], v[80:83]\n"
"v_mfma_f32_16x16x32_bf16 v[52:55], a[36:39], a[140:143], v[52:55]\n"
"v_mfma_f32_16x16x32_bf16 v[52:55], a[32:35], a[136:139], v[52:55]\n"
"s_waitcnt lgkmcnt(0)\n"
"v_mfma_f32_16x16x32_bf16 v[56:59], a[32:35], a[144:147], v[56:59]\n"
"v_mfma_f32_16x16x32_bf16 v[56:59], a[36:39], a[148:151], v[56:59]\n"
"global_load_dwordx4 a[92:95], v0, s[34:35] offset:3072 sc1\n"
"v_mfma_f32_16x16x32_bf16 v[84:87], a[44:47], a[148:151], v[84:87]\n"
"v_mfma_f32_16x16x32_bf16 v[84:87], a[40:43], a[144:147], v[84:87]\n"
"v_mfma_f32_16x16x32_bf16 a[4:7], a[40:43], a[152:155], a[4:7]\n"
"s_add_u32 s34, s34, 8192\n"
"v_mfma_f32_16x16x32_bf16 a[4:7], a[44:47], a[156:159], a[4:7]\n"
"buffer_load_dwordx4 v1, s[48:51], s69 offen offset:3072 sc1 lds\n"
"v_mfma_f32_16x16x32_bf16 a[0:3], a[36:39], a[156:159], a[0:3]\n"
"v_mfma_f32_16x16x32_bf16 a[0:3], a[32:35], a[152:155], a[0:3]\n"
"s_waitcnt vmcnt(14)\n"
"v_mfma_f32_16x16x32_bf16 v[88:91], a[48:51], a[96:99], v[88:91]\n"
"v_mfma_f32_16x16x32_bf16 v[88:91], a[52:55], a[100:103], v[88:91]\n"
"global_load_dwordx4 a[32:35], v0, s[34:35] offset:-4096 sc1\n"
"v_mfma_f32_16x16x32_bf16 v[116:119], a[60:63], a[100:103], v[116:119]\n"
"v_mfma_f32_16x16x32_bf16 v[116:119], a[56:59], a[96:99], v[116:119]\n"
"v_mfma_f32_16x16x32_bf16 v[120:123], a[56:59], a[104:107], v[120:123]\n"
"v_mfma_f32_16x16x32_bf16 v[120:123], a[60:63], a[108:111], v[120:123]\n"
"v_mfma_f32_16x16x32_bf16 v[92:95], a[52:55], a[108:111], v[92:95]\n"
"v_mfma_f32_16x16x32_bf16 v[92:95], a[48:51], a[104:107], v[92:95]\n"
"v_mfma_f32_16x16x32_bf16 v[96:99], a[48:51], a[112:115], v[96:99]\n"
"v_mfma_f32_16x16x32_bf16 v[96:99], a[52:55], a[116:119], v[96:99]\n"
"global_load_dwordx4 a[36:39], v0, s[34:35] offset:-3072 sc1\n"
"v_mfma_f32_16x16x32_bf16 v[124:127], a[60:63], a[116:119], v[124:127]\n"
"v_mfma_f32_16x16x32_bf16 v[124:127], a[56:59], a[112:115], v[124:127]\n"
"v_mfma_f32_16x16x32_bf16 v[128:131], a[56:59], a[120:123], v[128:131]\n"
"v_mfma_f32_16x16x32_bf16 v[128:131], a[60:63], a[124:127], v[128:131]\n"
"v_mfma_f32_16x16x32_bf16 v[100:103], a[52:55], a[124:127], v[100:103]\n"
"v_mfma_f32_16x16x32_bf16 v[100:103], a[48:51], a[120:123], v[100:103]\n"
"v_mfma_f32_16x16x32_bf16 v[104:107], a[48:51], a[128:131], v[104:107]\n"
"buffer_load_dwordx2 v[8:9], off, s[56:59], %14 sc1\n"
"v_mfma_f32_16x16x32_bf16 v[104:107], a[52:55], a[132:135], v[104:107]\n"
"global_load_dwordx4 a[40:43], v0, s[34:35] offset:-2048 sc1\n"
"v_mfma_f32_16x16x32_bf16 v[132:135], a[60:63], a[132:135], v[132:135]\n"
"v_mfma_f32_16x16x32_bf16 v[132:135], a[56:59], a[128:131], v[132:135]\n"
"v_mfma_f32_16x16x32_bf16 v[136:139], a[56:59], a[136:139], v[136:139]\n"
"v_mfma_f32_16x16x32_bf16 v[136:139], a[60:63], a[140:143], v[136:139]\n"
"v_mfma_f32_16x16x32_bf16 v[108:111], a[52:55], a[140:143], v[108:111]\n"
"v_mfma_f32_16x16x32_bf16 v[108:111], a[48:51], a[136:139], v[108:111]\n"
"v_mfma_f32_16x16x32_bf16 v[112:115], a[48:51], a[144:147], v[112:115]\n"
"v_mfma_f32_16x16x32_bf16 v[112:115], a[52:55], a[148:151], v[112:115]\n"
"global_load_dwordx4 a[44:47], v0, s[34:35] offset:-1024 sc1\n"
"v_mfma_f32_16x16x32_bf16 v[140:143], a[60:63], a[148:151], v[140:143]\n"
"v_mfma_f32_16x16x32_bf16 v[140:143], a[56:59], a[144:147], v[140:143]\n"
"v_mfma_f32_16x16x32_bf16 a[12:15], a[56:59], a[152:155], a[12:15]\n"
"v_mfma_f32_16x16x32_bf16 a[12:15], a[60:63], a[156:159], a[12:15]\n"
"v_mfma_f32_16x16x32_bf16 a[8:11], a[52:55], a[156:159], a[8:11]\n"
"v_mfma_f32_16x16x32_bf16 a[8:11], a[48:51], a[152:155], a[8:11]\n"
"s_waitcnt vmcnt(13)\n"
"v_mfma_f32_16x16x32_bf16 v[144:147], a[64:67], a[96:99], v[144:147]\n"
"L_st6793:\n"
"s_cbranch_execz L_st9091\n"
"v_mfma_f32_16x16x32_bf16 v[144:147], a[68:71], a[100:103], v[144:147]\n"
"global_load_dwordx4 a[48:51], v0, s[34:35] offset:0 sc1\n"
"v_mfma_f32_16x16x32_bf16 v[172:175], a[76:79], a[100:103], v[172:175]\n"
"v_mfma_f32_16x16x32_bf16 v[172:175], a[72:75], a[96:99], v[172:175]\n"
"v_mfma_f32_16x16x32_bf16 v[176:179], a[72:75], a[104:107], v[176:179]\n"
"v_mfma_f32_16x16x32_bf16 v[176:179], a[76:79], a[108:111], v[176:179]\n"
"v_mfma_f32_16x16x32_bf16 v[148:151], a[68:71], a[108:111], v[148:151]\n"
"v_mfma_f32_16x16x32_bf16 v[148:151], a[64:67], a[104:107], v[148:151]\n"
"v_mfma_f32_16x16x32_bf16 v[152:155], a[64:67], a[112:115], v[152:155]\n"
"v_lshlrev_b32 v20, 16, v22\n"
"v_mfma_f32_16x16x32_bf16 v[152:155], a[68:71], a[116:119], v[152:155]\n"
"global_load_dwordx4 a[52:55], v0, s[34:35] offset:1024 sc1\n"
"v_and_b32 v21, s63, v22\n"
"v_mfma_f32_16x16x32_bf16 v[180:183], a[76:79], a[116:119], v[180:183]\n"
"v_lshlrev_b32 v22, 16, v23\n"
"v_mfma_f32_16x16x32_bf16 v[180:183], a[72:75], a[112:115], v[180:183]\n"
"v_and_b32 v23, s63, v23\n"
"v_mfma_f32_16x16x32_bf16 v[184:187], a[72:75], a[120:123], v[184:187]\n"
"v_add_f32 v60, v60, v20\n"
"v_mfma_f32_16x16x32_bf16 v[184:187], a[76:79], a[124:127], v[184:187]\n"
"v_add_f32 v61, v61, v21\n"
"v_add_f32 v62, v62, v22\n"
"v_mfma_f32_16x16x32_bf16 v[156:159], a[68:71], a[124:127], v[156:159]\n"
"v_add_f32 v63, v63, v23\n"
"v_mfma_f32_16x16x32_bf16 v[156:159], a[64:67], a[120:123], v[156:159]\n"
"v_add_f32 v64, v64, v20\n"
"v_mfma_f32_16x16x32_bf16 v[160:163], a[64:67], a[128:131], v[160:163]\n"
"v_add_f32 v65, v65, v21\n"
"v_mfma_f32_16x16x32_bf16 v[160:163], a[68:71], a[132:135], v[160:163]\n"
"global_load_dwordx4 a[56:59], v0, s[34:35] offset:2048 sc1\n"
"v_add_f32 v66, v66, v22\n"
"v_mfma_f32_16x16x32_bf16 v[188:191], a[76:79], a[132:135], v[188:191]\n"
"v_add_f32 v67, v67, v23\n"
"v_mfma_f32_16x16x32_bf16 v[188:191], a[72:75], a[128:131], v[188:191]\n"
"v_add_f32 v68, v68, v20\n"
"v_mfma_f32_16x16x32_bf16 v[192:195], a[72:75], a[136:139], v[192:195]\n"
"v_add_f32 v69, v69, v21\n"
"v_mfma_f32_16x16x32_bf16 v[192:195], a[76:79], a[140:143], v[192:195]\n"
"v_add_f32 v70, v70, v22\n"
"v_mfma_f32_16x16x32_bf16 v[164:167], a[68:71], a[140:143], v[164:167]\n"
"v_add_f32 v71, v71, v23\n"
"v_mfma_f32_16x16x32_bf16 v[164:167], a[64:67], a[136:139], v[164:167]\n"
"v_add_f32 v72, v72, v20\n"
"v_add_f32 v73, v73, v21\n"
"v_mfma_f32_16x16x32_bf16 v[168:171], a[64:67], a[144:147], v[168:171]\n"
"v_add_f32 v74, v74, v22\n"
"v_mfma_f32_16x16x32_bf16 v[168:171], a[68:71], a[148:151], v[168:171]\n"
"global_load_dwordx4 a[60:63], v0, s[34:35] offset:3072 sc1\n"
"v_add_f32 v75, v75, v23\n"
"v_mfma_f32_16x16x32_bf16 v[196:199], a[76:79], a[148:151], v[196:199]\n"
"v_add_f32 v76, v76, v20\n"
"v_mfma_f32_16x16x32_bf16 v[196:199], a[72:75], a[144:147], v[196:199]\n"
"v_add_f32 v77, v77, v21\n"
"v_mfma_f32_16x16x32_bf16 a[20:23], a[72:75], a[152:155], a[20:23]\n"
"s_add_u32 s34, s34, 8192\n"
"v_add_f32 v78, v78, v22\n"
"v_mfma_f32_16x16x32_bf16 a[20:23], a[76:79], a[156:159], a[20:23]\n"
"v_add_f32 v79, v79, v23\n"
"v_mfma_f32_16x16x32_bf16 a[16:19], a[68:71], a[156:159], a[16:19]\n"
"v_add_f32 v80, v80, v20\n"
"v_mfma_f32_16x16x32_bf16 a[16:19], a[64:67], a[152:155], a[16:19]\n"
"s_waitcnt vmcnt(10)\n"
"v_add_f32 v81, v81, v21\n"
"v_mfma_f32_16x16x32_bf16 v[200:203], a[80:83], a[96:99], v[200:203]\n"
"v_add_f32 v82, v82, v22\n"
"v_add_f32 v83, v83, v23\n"
"v_mfma_f32_16x16x32_bf16 v[200:203], a[84:87], a[100:103], v[200:203]\n"
"global_load_dwordx4 a[64:67], v0, s[34:35] offset:-4096 sc1\n"
"v_add_f32 v84, v84, v20\n"
"v_mfma_f32_16x16x32_bf16 v[228:231], a[92:95], a[100:103], v[228:231]\n"
"s_waitcnt vmcnt(10)\n"
"ds_read_b128 a[100:103], v2 offset:1024\n"
"v_add_f32 v85, v85, v21\n"
"v_mfma_f32_16x16x32_bf16 v[228:231], a[88:91], a[96:99], v[228:231]\n"
"ds_read_b128 a[96:99], v2 offset:0\n"
"v_add_f32 v86, v86, v22\n"
"v_mfma_f32_16x16x32_bf16 v[232:235], a[88:91], a[104:107], v[232:235]\n"
"v_add_f32 v87, v87, v23\n"
"v_mfma_f32_16x16x32_bf16 v[232:235], a[92:95], a[108:111], v[232:235]\n"
"v_accvgpr_read_b32 v12, a4\n"
"v_mfma_f32_16x16x32_bf16 v[204:207], a[84:87], a[108:111], v[204:207]\n"
"ds_read_b128 a[108:111], v2 offset:3072\n"
"v_accvgpr_read_b32 v13, a5\n"
"v_mfma_f32_16x16x32_bf16 v[204:207], a[80:83], a[104:107], v[204:207]\n"
"ds_read_b128 a[104:107], v2 offset:2048\n"
"v_accvgpr_read_b32 v14, a6\n"
"v_mfma_f32_16x16x32_bf16 v[208:211], a[80:83], a[112:115], v[208:211]\n"
"v_accvgpr_read_b32 v15, a7\n"
"v_mfma_f32_16x16x32_bf16 v[208:211], a[84:87], a[116:119], v[208:211]\n"
"global_load_dwordx4 a[68:71], v0, s[34:35] offset:-3072 sc1\n"
"v_add_f32 v12, v12, v20\n"
"v_mfma_f32_16x16x32_bf16 v[236:239], a[92:95], a[116:119], v[236:239]\n"
"ds_read_b128 a[116:119], v2 offset:5120\n"
"v_add_f32 v13, v13, v21\n"
"v_mfma_f32_16x16x32_bf16 v[236:239], a[88:91], a[112:115], v[236:239]\n"
"ds_read_b128 a[112:115], v2 offset:4096\n"
"v_add_f32 v14, v14, v22\n"
"v_mfma_f32_16x16x32_bf16 v[240:243], a[88:91], a[120:123], v[240:243]\n"
"v_add_f32 v15, v15, v23\n"
"v_accvgpr_write_b32 a4, v12\n"
"v_mfma_f32_16x16x32_bf16 v[240:243], a[92:95], a[124:127], v[240:243]\n"
"v_accvgpr_write_b32 a5, v13\n"
"v_mfma_f32_16x16x32_bf16 v[212:215], a[84:87], a[124:127], v[212:215]\n"
"ds_read_b128 a[124:127], v2 offset:7168\n"
"v_accvgpr_write_b32 a6, v14\n"
"v_mfma_f32_16x16x32_bf16 v[212:215], a[80:83], a[120:123], v[212:215]\n"
"ds_read_b128 a[120:123], v2 offset:6144\n"
"v_accvgpr_write_b32 a7, v15\n"
"v_mfma_f32_16x16x32_bf16 v[216:219], a[80:83], a[128:131], v[216:219]\n"
"v_mfma_f32_16x16x32_bf16 v[216:219], a[84:87], a[132:135], v[216:219]\n"
"global_load_dwordx4 a[72:75], v0, s[34:35] offset:-2048 sc1\n"
"v_lshrrev_b32 v20, 4, v0\n"
"v_mfma_f32_16x16x32_bf16 v[244:247], a[92:95], a[132:135], v[244:247]\n"
"ds_read_b128 a[132:135], v2 offset:9216\n"
"v_and_b32 v20, 0x30, v20\n"
"v_mfma_f32_16x16x32_bf16 v[244:247], a[88:91], a[128:131], v[244:247]\n"
"ds_read_b128 a[128:131], v2 offset:8192\n"
"buffer_load_dwordx2 v[22:23], v20, s[44:47], s61 offen offset:72\n"
"v_mfma_f32_16x16x32_bf16 v[248:251], a[88:91], a[136:139], v[248:251]\n"
"v_mfma_f32_16x16x32_bf16 v[248:251], a[92:95], a[140:143], v[248:251]\n"
"v_mfma_f32_16x16x32_bf16 v[220:223], a[84:87], a[140:143], v[220:223]\n"
"ds_read_b128 a[140:143], v2 offset:11264\n"
"v_mfma_f32_16x16x32_bf16 v[220:223], a[80:83], a[136:139], v[220:223]\n"
"ds_read_b128 a[136:139], v2 offset:10240\n"
"v_mfma_f32_16x16x32_bf16 v[224:227], a[80:83], a[144:147], v[224:227]\n"
"v_mfma_f32_16x16x32_bf16 v[224:227], a[84:87], a[148:151], v[224:227]\n"
"global_load_dwordx4 a[76:79], v0, s[34:35] offset:-1024 sc1\n"
"v_mfma_f32_16x16x32_bf16 v[252:255], a[92:95], a[148:151], v[252:255]\n"
"ds_read_b128 a[148:151], v2 offset:13312\n"
"v_mfma_f32_16x16x32_bf16 v[252:255], a[88:91], a[144:147], v[252:255]\n"
"ds_read_b128 a[144:147], v2 offset:12288\n"
"v_mfma_f32_16x16x32_bf16 a[28:31], a[88:91], a[152:155], a[28:31]\n"
"v_mfma_f32_16x16x32_bf16 a[28:31], a[92:95], a[156:159], a[28:31]\n"
"v_mfma_f32_16x16x32_bf16 a[24:27], a[84:87], a[156:159], a[24:27]\n"
"s_cmp_eq_u32 s62, 5\n"
"ds_read_b128 a[156:159], v2 offset:15360\n"
"v_mfma_f32_16x16x32_bf16 a[24:27], a[80:83], a[152:155], a[24:27]\n"
"ds_read_b128 a[152:155], v2 offset:14336\n"
"s_cbranch_scc1 L_last\n"
"L_P4:\n"
"s_waitcnt vmcnt(9) lgkmcnt(12)\n"
"v_mfma_f32_16x16x32_bf16 v[32:35], a[32:35], a[96:99], v[32:35]\n"
"s_add_u32 s48, s48, 131072\n"
"s_addc_u32 s49, s49, 0\n"
"v_mfma_f32_16x16x32_bf16 v[32:35], a[36:39], a[100:103], v[32:35]\n"
"global_load_dwordx4 a[80:83], v0, s[34:35] offset:0 sc1\n"
"v_mfma_f32_16x16x32_bf16 v[60:63], a[44:47], a[100:103], v[60:63]\n"
"s_barrier\n"
"v_mfma_f32_16x16x32_bf16 v[60:63], a[40:43], a[96:99], v[60:63]\n"
"v_mfma_f32_16x16x32_bf16 v[64:67], a[40:43], a[104:107], v[64:67]\n"
"v_mfma_f32_16x16x32_bf16 v[64:67], a[44:47], a[108:111], v[64:67]\n"
"v_mfma_f32_16x16x32_bf16 v[36:39], a[36:39], a[108:111], v[36:39]\n"
"v_subrev_u32 v8, s65, v8\n"
"v_mfma_f32_16x16x32_bf16 v[36:39], a[32:35], a[104:107], v[36:39]\n"
"s_waitcnt lgkmcnt(8)\n"
"v_xor_b32 v9, s79, v9\n"
"v_mfma_f32_16x16x32_bf16 v[40:43], a[32:35], a[112:115], v[40:43]\n"
"v_or_b32 v8, v8, v9\n"
"v_mfma_f32_16x16x32_bf16 v[40:43], a[36:39], a[116:119], v[40:43]\n"
"global_load_dwordx4 a[84:87], v0, s[34:35] offset:1024 sc1\n"
"v_cmp_lt_u32 vcc, 15, v8\n"
"v_mfma_f32_16x16x32_bf16 v[68:71], a[44:47], a[116:119], v[68:71]\n"
"s_cbranch_vccnz L_slow1\n"
"L_back1:\n"
"v_mfma_f32_16x16x32_bf16 v[68:71], a[40:43], a[112:115], v[68:71]\n"
"v_mfma_f32_16x16x32_bf16 v[72:75], a[40:43], a[120:123], v[72:75]\n"
"v_mfma_f32_16x16x32_bf16 v[72:75], a[44:47], a[124:127], v[72:75]\n"
"v_mfma_f32_16x16x32_bf16 v[44:47], a[36:39], a[124:127], v[44:47]\n"
"v_mfma_f32_16x16x32_bf16 v[44:47], a[32:35], a[120:123], v[44:47]\n"
"s_waitcnt lgkmcnt(4)\n"
"v_mfma_f32_16x16x32_bf16 v[48:51], a[32:35], a[128:131], v[48:51]\n"
"v_mfma_f32_16x16x32_bf16 v[48:51], a[36:39], a[132:135], v[48:51]\n"
"global_load_dwordx4 a[88:91], v0, s[34:35] offset:2048 sc1\n"
"v_mfma_f32_16x16x32_bf16 v[76:79], a[44:47], a[132:135], v[76:79]\n"
"v_mfma_f32_16x16x32_bf16 v[76:79], a[40:43], a[128:131], v[76:79]\n"
"s_add_u32 m0, s64, 16384\n"
"v_mfma_f32_16x16x32_bf16 v[80:83], a[40:43], a[136:139], v[80:83]\n"
"v_mfma_f32_16x16x32_bf16 v[80:83], a[44:47], a[140:143], v[80:83]\n"
"v_mfma_f32_16x16x32_bf16 v[52:55], a[36:39], a[140:143], v[52:55]\n"
"v_mfma_f32_16x16x32_bf16 v[52:55], a[32:35], a[136:139], v[52:55]\n"
"s_waitcnt lgkmcnt(0)\n"
"v_mfma_f32_16x16x32_bf16 v[56:59], a[32:35], a[144:147], v[56:59]\n"
"v_mfma_f32_16x16x32_bf16 v[56:59], a[36:39], a[148:151], v[56:59]\n"
"global_load_dwordx4 a[92:95], v0, s[34:35] offset:3072 sc1\n"
"v_mfma_f32_16x16x32_bf16 v[84:87], a[44:47], a[148:151], v[84:87]\n"
"v_mfma_f32_16x16x32_bf16 v[84:87], a[40:43], a[144:147], v[84:87]\n"
"v_mfma_f32_16x16x32_bf16 a[4:7], a[40:43], a[152:155], a[4:7]\n"
"s_add_u32 s34, s34, 8192\n"
"v_mfma_f32_16x16x32_bf16 a[4:7], a[44:47], a[156:159], a[4:7]\n"
"v_mfma_f32_16x16x32_bf16 a[0:3], a[36:39], a[156:159], a[0:3]\n"
"v_mfma_f32_16x16x32_bf16 a[0:3], a[32:35], a[152:155], a[0:3]\n"
"s_waitcnt vmcnt(9)\n"
"v_mfma_f32_16x16x32_bf16 v[88:91], a[48:51], a[96:99], v[88:91]\n"
"v_mfma_f32_16x16x32_bf16 v[88:91], a[52:55], a[100:103], v[88:91]\n"
"global_load_dwordx4 a[32:35], v0, s[34:35] offset:-4096 sc1\n"
"v_mfma_f32_16x16x32_bf16 v[116:119], a[60:63], a[100:103], v[116:119]\n"
"buffer_load_dwordx4 v0, s[48:51], s75 offen offset:0 sc1 lds\n"
"v_mfma_f32_16x16x32_bf16 v[116:119], a[56:59], a[96:99], v[116:119]\n"
"v_mfma_f32_16x16x32_bf16 v[120:123], a[56:59], a[104:107], v[120:123]\n"
"v_mfma_f32_16x16x32_bf16 v[120:123], a[60:63], a[108:111], v[120:123]\n"
"v_mfma_f32_16x16x32_bf16 v[92:95], a[52:55], a[108:111], v[92:95]\n"
"v_mfma_f32_16x16x32_bf16 v[92:95], a[48:51], a[104:107], v[92:95]\n"
"v_mfma_f32_16x16x32_bf16 v[96:99], a[48:51], a[112:115], v[96:99]\n"
"v_mfma_f32_16x16x32_bf16 v[96:99], a[52:55], a[116:119], v[96:99]\n"
"global_load_dwordx4 a[36:39], v0, s[34:35] offset:-3072 sc1\n"
"v_mfma_f32_16x16x32_bf16 v[124:127], a[60:63], a[116:119], v[124:127]\n"
"v_mfma_f32_16x16x32_bf16 v[124:127], a[56:59], a[112:115], v[124:127]\n"
"v_mfma_f32_16x16x32_bf16 v[128:131], a[56:59], a[120:123], v[128:131]\n"
"v_mfma_f32_16x16x32_bf16 v[128:131], a[60:63], a[124:127], v[128:131]\n"
"v_mfma_f32_16x16x32_bf16 v[100:103], a[52:55], a[124:127], v[100:103]\n"
"v_mfma_f32_16x16x32_bf16 v[100:103], a[48:51], a[120:123], v[100:103]\n"
"v_mfma_f32_16x16x32_bf16 v[104:107], a[48:51], a[128:131], v[104:107]\n"
"v_mfma_f32_16x16x32_bf16 v[104:107], a[52:55], a[132:135], v[104:107]\n"
"global_load_dwordx4 a[40:43], v0, s[34:35] offset:-2048 sc1\n"
"v_mfma_f32_16x16x32_bf16 v[132:135], a[60:63], a[132:135], v[132:135]\n"
"buffer_load_dwordx4 v0, s[48:51], s75 offen offset:1024 sc1 lds\n"
"v_mfma_f32_16x16x32_bf16 v[132:135], a[56:59], a[128:131], v[132:135]\n"
"v_mfma_f32_16x16x32_bf16 v[136:139], a[56:59], a[136:139], v[136:139]\n"
"v_mfma_f32_16x16x32_bf16 v[136:139], a[60:63], a[140:143], v[136:139]\n"
"v_mfma_f32_16x16x32_bf16 v[108:111], a[52:55], a[140:143], v[108:111]\n"
"v_mfma_f32_16x16x32_bf16 v[108:111], a[48:51], a[136:139], v[108:111]\n"
"v_mfma_f32_16x16x32_bf16 v[112:115], a[48:51], a[144:147], v[112:115]\n"
"v_mfma_f32_16x16x32_bf16 v[112:115], a[52:55], a[148:151], v[112:115]\n"
"global_load_dwordx4 a[44:47], v0, s[34:35] offset:-1024 sc1\n"
"v_mfma_f32_16x16x32_bf16 v[140:143], a[60:63], a[148:151], v[140:143]\n"
"v_mfma_f32_16x16x32_bf16 v[140:143], a[56:59], a[144:147], v[140:143]\n"
"v_mfma_f32_16x16x32_bf16 a[12:15], a[56:59], a[152:155], a[12:15]\n"
"v_mfma_f32_16x16x32_bf16 a[12:15], a[60:63], a[156:159], a[12:15]\n"
"v_mfma_f32_16x16x32_bf16 a[8:11], a[52:55], a[156:159], a[8:11]\n"
"v_mfma_f32_16x16x32_bf16 a[8:11], a[48:51], a[152:155], a[8:11]\n"
"s_waitcnt vmcnt(10)\n"
"v_mfma_f32_16x16x32_bf16 v[144:147], a[64:67], a[96:99], v[144:147]\n"
"v_mfma_f32_16x16x32_bf16 v[144:147], a[68:71], a[100:103], v[144:147]\n"
"global_load_dwordx4 a[48:51], v0, s[34:35] offset:0 sc1\n"
"v_mfma_f32_16x16x32_bf16 v[172:175], a[76:79], a[100:103], v[172:175]\n"
"v_mfma_f32_16x16x32_bf16 v[172:175], a[72:75], a[96:99], v[172:175]\n"
"buffer_load_dwordx4 v0, s[48:51], s75 offen offset:2048 sc1 lds\n"
"v_mfma_f32_16x16x32_bf16 v[176:179], a[72:75], a[104:107], v[176:179]\n"
"v_mfma_f32_16x16x32_bf16 v[176:179], a[76:79], a[108:111], v[176:179]\n"
"v_mfma_f32_16x16x32_bf16 v[148:151], a[68:71], a[108:111], v[148:151]\n"
"v_mfma_f32_16x16x32_bf16 v[148:151], a[64:67], a[104:107], v[148:151]\n"
"v_mfma_f32_16x16x32_bf16 v[152:155], a[64:67], a[112:115], v[152:155]\n"
"v_lshlrev_b32 v16, 16, v18\n"
"v_mfma_f32_16x16x32_bf16 v[152:155], a[68:71], a[116:119], v[152:155]\n"
"global_load_dwordx4 a[52:55], v0, s[34:35] offset:1024 sc1\n"
"v_and_b32 v17, s63, v18\n"
"v_mfma_f32_16x16x32_bf16 v[180:183], a[76:79], a[116:119], v[180:183]\n"
"v_lshlrev_b32 v18, 16, v19\n"
"v_mfma_f32_16x16x32_bf16 v[180:183], a[72:75], a[112:115], v[180:183]\n"
"v_and_b32 v19, s63, v19\n"
"v_mfma_f32_16x16x32_bf16 v[184:187], a[72:75], a[120:123], v[184:187]\n"
"v_add_f32 v88, v88, v16\n"
"v_mfma_f32_16x16x32_bf16 v[184:187], a[76:79], a[124:127], v[184:187]\n"
"v_add_f32 v89, v89, v17\n"
"v_add_f32 v90, v90, v18\n"
"v_mfma_f32_16x16x32_bf16 v[156:159], a[68:71], a[124:127], v[156:159]\n"
"v_add_f32 v91, v91, v19\n"
"v_mfma_f32_16x16x32_bf16 v[156:159], a[64:67], a[120:123], v[156:159]\n"
"v_add_f32 v92, v92, v16\n"
"v_mfma_f32_16x16x32_bf16 v[160:163], a[64:67], a[128:131], v[160:163]\n"
"v_add_f32 v93, v93, v17\n"
"v_mfma_f32_16x16x32_bf16 v[160:163], a[68:71], a[132:135], v[160:163]\n"
"global_load_dwordx4 a[56:59], v0, s[34:35] offset:2048 sc1\n"
"v_add_f32 v94, v94, v18\n"
"v_mfma_f32_16x16x32_bf16 v[188:191], a[76:79], a[132:135], v[188:191]\n"
"buffer_load_dwordx4 v0, s[48:51], s75 offen offset:3072 sc1 lds\n"
"v_mfma_f32_16x16x32_bf16 v[188:191], a[72:75], a[128:131], v[188:191]\n"
"v_add_f32 v95, v95, v19\n"
"v_add_f32 v96, v96, v16\n"
"v_mfma_f32_16x16x32_bf16 v[192:195], a[72:75], a[136:139], v[192:195]\n"
"v_add_f32 v97, v97, v17\n"
"v_mfma_f32_16x16x32_bf16 v[192:195], a[76:79], a[140:143], v[192:195]\n"
"v_add_f32 v98, v98, v18\n"
"v_mfma_f32_16x16x32_bf16 v[164:167], a[68:71], a[140:143], v[164:167]\n"
"v_add_f32 v99, v99, v19\n"
"v_mfma_f32_16x16x32_bf16 v[164:167], a[64:67], a[136:139], v[164:167]\n"
"v_add_f32 v100, v100, v16\n"
"v_add_f32 v101, v101, v17\n"
"v_mfma_f32_16x16x32_bf16 v[168:171], a[64:67], a[144:147], v[168:171]\n"
"v_add_f32 v102, v102, v18\n"
"v_mfma_f32_16x16x32_bf16 v[168:171], a[68:71], a[148:151], v[168:171]\n"
"global_load_dwordx4 a[60:63], v0, s[34:35] offset:3072 sc1\n"
"v_add_f32 v103, v103, v19\n"
"v_mfma_f32_16x16x32_bf16 v[196:199], a[76:79], a[148:151], v[196:199]\n"
"v_add_f32 v104, v104, v16\n"
"v_mfma_f32_16x16x32_bf16 v[196:199], a[72:75], a[144:147], v[196:199]\n"
"v_add_f32 v105, v105, v17\n"
"v_mfma_f32_16x16x32_bf16 a[20:23], a[72:75], a[152:155], a[20:23]\n"
"s_add_u32 s34, s34, 8192\n"
"v_add_f32 v106, v106, v18\n"
"v_mfma_f32_16x16x32_bf16 a[20:23], a[76:79], a[156:159], a[20:23]\n"
"v_add_f32 v107, v107, v19\n"
"v_mfma_f32_16x16x32_bf16 a[16:19], a[68:71], a[156:159], a[16:19]\n"
"v_add_f32 v108, v108, v16\n"
"v_mfma_f32_16x16x32_bf16 a[16:19], a[64:67], a[152:155], a[16:19]\n"
"s_waitcnt vmcnt(12)\n"
"v_add_f32 v109, v109, v17\n"
"v_mfma_f32_16x16x32_bf16 v[200:203], a[80:83], a[96:99], v[200:203]\n"
"v_add_f32 v110, v110, v18\n"
"v_add_f32 v111, v111, v19\n"
"v_mfma_f32_16x16x32_bf16 v[200:203], a[84:87], a[100:103], v[200:203]\n"
"global_load_dwordx4 a[64:67], v0, s[34:35] offset:-4096 sc1\n"
"v_add_f32 v112, v112, v16\n"
"v_mfma_f32_16x16x32_bf16 v[228:231], a[92:95], a[100:103], v[228:231]\n"
"ds_read_b128 a[100:103], v3 offset:1024\n"
"v_add_f32 v113, v113, v17\n"
"v_mfma_f32_16x16x32_bf16 v[228:231], a[88:91], a[96:99], v[228:231]\n"
"s_add_u32 m0, s64, 20480\n"
"ds_read_b128 a[96:99], v3 offset:0\n"
"v_add_f32 v114, v114, v18\n"
"v_mfma_f32_16x16x32_bf16 v[232:235], a[88:91], a[104:107], v[232:235]\n"
"v_add_f32 v115, v115, v19\n"
"v_mfma_f32_16x16x32_bf16 v[232:235], a[92:95], a[108:111], v[232:235]\n"
"v_accvgpr_read_b32 v12, a8\n"
"v_mfma_f32_16x16x32_bf16 v[204:207], a[84:87], a[108:111], v[204:207]\n"
"ds_read_b128 a[108:111], v3 offset:3072\n"
"v_accvgpr_read_b32 v13, a9\n"
"v_mfma_f32_16x16x32_bf16 v[204:207], a[80:83], a[104:107], v[204:207]\n"
"ds_read_b128 a[104:107], v3 offset:2048\n"
"v_accvgpr_read_b32 v14, a10\n"
"v_mfma_f32_16x16x32_bf16 v[208:211], a[80:83], a[112:115], v[208:211]\n"
"v_accvgpr_read_b32 v15, a11\n"
"v_mfma_f32_16x16x32_bf16 v[208:211], a[84:87], a[116:119], v[208:211]\n"
"global_load_dwordx4 a[68:71], v0, s[34:35] offset:-3072 sc1\n"
"v_add_f32 v12, v12, v16\n"
"v_mfma_f32_16x16x32_bf16 v[236:239], a[92:95], a[116:119], v[236:239]\n"
"ds_read_b128 a[116:119], v3 offset:5120\n"
"v_add_f32 v13, v13, v17\n"
"v_mfma_f32_16x16x32_bf16 v[236:239], a[88:91], a[112:115], v[236:239]\n"
"ds_read_b128 a[112:115], v3 offset:4096\n"
"v_add_f32 v14, v14, v18\n"
"v_mfma_f32_16x16x32_bf16 v[240:243], a[88:91], a[120:123], v[240:243]\n"
"v_add_f32 v15, v15, v19\n"
"v_accvgpr_write_b32 a8, v12\n"
"v_mfma_f32_16x16x32_bf16 v[240:243], a[92:95], a[124:127], v[240:243]\n"
"v_accvgpr_write_b32 a9, v13\n"
"v_mfma_f32_16x16x32_bf16 v[212:215], a[84:87], a[124:127], v[212:215]\n"
"ds_read_b128 a[124:127], v3 offset:7168\n"
"v_accvgpr_write_b32 a10, v14\n"
"v_mfma_f32_16x16x32_bf16 v[212:215], a[80:83], a[120:123], v[212:215]\n"
"ds_read_b128 a[120:123], v3 offset:6144\n"
"v_accvgpr_write_b32 a11, v15\n"
"v_mfma_f32_16x16x32_bf16 v[216:219], a[80:83], a[128:131], v[216:219]\n"
"v_mfma_f32_16x16x32_bf16 v[216:219], a[84:87], a[132:135], v[216:219]\n"
"global_load_dwordx4 a[72:75], v0, s[34:35] offset:-2048 sc1\n"
"v_mfma_f32_16x16x32_bf16 v[244:247], a[92:95], a[132:135], v[244:247]\n"
"ds_read_b128 a[132:135], v3 offset:9216\n"
"v_mfma_f32_16x16x32_bf16 v[244:247], a[88:91], a[128:131], v[244:247]\n"
"ds_read_b128 a[128:131], v3 offset:8192\n"
"v_mfma_f32_16x16x32_bf16 v[248:251], a[88:91], a[136:139], v[248:251]\n"
"buffer_load_dwordx4 v1, s[48:51], s75 offen offset:0 sc1 lds\n"
"v_mfma_f32_16x16x32_bf16 v[248:251], a[92:95], a[140:143], v[248:251]\n"
"v_mfma_f32_16x16x32_bf16 v[220:223], a[84:87], a[140:143], v[220:223]\n"
"ds_read_b128 a[140:143], v3 offset:11264\n"
"v_mfma_f32_16x16x32_bf16 v[220:223], a[80:83], a[136:139], v[220:223]\n"
"ds_read_b128 a[136:139], v3 offset:10240\n"
"v_mfma_f32_16x16x32_bf16 v[224:227], a[80:83], a[144:147], v[224:227]\n"
"v_mfma_f32_16x16x32_bf16 v[224:227], a[84:87], a[148:151], v[224:227]\n"
"global_load_dwordx4 a[76:79], v0, s[34:35] offset:-1024 sc1\n"
"v_mfma_f32_16x16x32_bf16 v[252:255], a[92:95], a[148:151], v[252:255]\n"
"ds_read_b128 a[148:151], v3 offset:13312\n"
"v_mfma_f32_16x16x32_bf16 v[252:255], a[88:91], a[144:147], v[252:255]\n"
"ds_read_b128 a[144:147], v3 offset:12288\n"
"v_mfma_f32_16x16x32_bf16 a[28:31], a[88:91], a[152:155], a[28:31]\n"
"v_mfma_f32_16x16x32_bf16 a[28:31], a[92:95], a[156:159], a[28:31]\n"
"v_mfma_f32_16x16x32_bf16 a[24:27], a[84:87], a[156:159], a[24:27]\n"
"ds_read_b128 a[156:159], v3 offset:15360\n"
"v_mfma_f32_16x16x32_bf16 a[24:27], a[80:83], a[152:155], a[24:27]\n"
"s_waitcnt vmcnt(11) lgkmcnt(11)\n"
"ds_read_b128 a[152:155], v3 offset:14336\n"
"v_mfma_f32_16x16x32_bf16 v[32:35], a[32:35], a[96:99], v[32:35]\n"
"v_mfma_f32_16x16x32_bf16 v[32:35], a[36:39], a[100:103], v[32:35]\n"
"global_load_dwordx4 a[80:83], v0, s[34:35] offset:0 sc1\n"
"v_mfma_f32_16x16x32_bf16 v[60:63], a[44:47], a[100:103], v[60:63]\n"
"v_mfma_f32_16x16x32_bf16 v[60:63], a[40:43], a[96:99], v[60:63]\n"
"buffer_load_dwordx4 v1, s[48:51], s75 offen offset:1024 sc1 lds\n"
"v_mfma_f32_16x16x32_bf16 v[64:67], a[40:43], a[104:107], v[64:67]\n"
"v_mfma_f32_16x16x32_bf16 v[64:67], a[44:47], a[108:111], v[64:67]\n"
"v_mfma_f32_16x16x32_bf16 v[36:39], a[36:39], a[108:111], v[36:39]\n"
"v_mfma_f32_16x16x32_bf16 v[36:39], a[32:35], a[104:107], v[36:39]\n"
"s_waitcnt lgkmcnt(8)\n"
"v_mfma_f32_16x16x32_bf16 v[40:43], a[32:35], a[112:115], v[40:43]\n"
"v_mfma_f32_16x16x32_bf16 v[40:43], a[36:39], a[116:119], v[40:43]\n"
"global_load_dwordx4 a[84:87], v0, s[34:35] offset:1024 sc1\n"
"v_mfma_f32_16x16x32_bf16 v[68:71], a[44:47], a[116:119], v[68:71]\n"
"v_mfma_f32_16x16x32_bf16 v[68:71], a[40:43], a[112:115], v[68:71]\n"
"v_mfma_f32_16x16x32_bf16 v[72:75], a[40:43], a[120:123], v[72:75]\n"
"v_mfma_f32_16x16x32_bf16 v[72:75], a[44:47], a[124:127], v[72:75]\n"
"v_mfma_f32_16x16x32_bf16 v[44:47], a[36:39], a[124:127], v[44:47]\n"
"v_mfma_f32_16x16x32_bf16 v[44:47], a[32:35], a[120:123], v[44:47]\n"
"s_waitcnt lgkmcnt(4)\n"
"v_mfma_f32_16x16x32_bf16 v[48:51], a[32:35], a[128:131], v[48:51]\n"
"v_mfma_f32_16x16x32_bf16 v[48:51], a[36:39], a[132:135], v[48:51]\n"
"global_load_dwordx4 a[88:91], v0, s[34:35] offset:2048 sc1\n"
"v_mfma_f32_16x16x32_bf16 v[76:79], a[44:47], a[132:135], v[76:79]\n"
"v_mfma_f32_16x16x32_bf16 v[76:79], a[40:43], a[128:131], v[76:79]\n"
"buffer_load_dwordx4 v1, s[48:51], s75 offen offset:2048 sc1 lds\n"
"v_mfma_f32_16x16x32_bf16 v[80:83], a[40:43], a[136:139], v[80:83]\n"
"v_mfma_f32_16x16x32_bf16 v[80:83], a[44:47], a[140:143], v[80:83]\n"
"v_mfma_f32_16x16x32_bf16 v[52:55], a[36:39], a[140:143], v[52:55]\n"
"v_mfma_f32_16x16x32_bf16 v[52:55], a[32:35], a[136:139], v[52:55]\n"
"s_waitcnt lgkmcnt(0)\n"
"v_mfma_f32_16x16x32_bf16 v[56:59], a[32:35], a[144:147], v[56:59]\n"
"v_mfma_f32_16x16x32_bf16 v[56:59], a[36:39], a[148:151], v[56:59]\n"
"global_load_dwordx4 a[92:95], v0, s[34:35] offset:3072 sc1\n"
"v_mfma_f32_16x16x32_bf16 v[84:87], a[44:47], a[148:151], v[84:87]\n"
"v_mfma_f32_16x16x32_bf16 v[84:87], a[40:43], a[144:147], v[84:87]\n"
"v_mfma_f32_16x16x32_bf16 a[4:7], a[40:43], a[152:155], a[4:7]\n"
"s_add_u32 s34, s34, 8192\n"
"v_mfma_f32_16x16x32_bf16 a[4:7], a[44:47], a[156:159], a[4:7]\n"
"v_mfma_f32_16x16x32_bf16 a[0:3], a[36:39], a[156:159], a[0:3]\n"
"v_mfma_f32_16x16x32_bf16 a[0:3], a[32:35], a[152:155], a[0:3]\n"
"s_waitcnt vmcnt(11)\n"
"v_mfma_f32_16x16x32_bf16 v[88:91], a[48:51], a[96:99], v[88:91]\n"
"v_mfma_f32_16x16x32_bf16 v[88:91], a[52:55], a[100:103], v[88:91]\n"
"global_load_dwordx4 a[32:35], v0, s[34:35] offset:-4096 sc1\n"
"v_mfma_f32_16x16x32_bf16 v[116:119], a[60:63], a[100:103], v[116:119]\n"
"v_mfma_f32_16x16x32_bf16 v[116:119], a[56:59], a[96:99], v[116:119]\n"
"buffer_load_dwordx4 v1, s[48:51], s75 offen offset:3072 sc1 lds\n"
"v_mfma_f32_16x16x32_bf16 v[120:123], a[56:59], a[104:107], v[120:123]\n"
"v_mfma_f32_16x16x32_bf16 v[120:123], a[60:63], a[108:111], v[120:123]\n"
"v_mfma_f32_16x16x32_bf16 v[92:95], a[52:55], a[108:111], v[92:95]\n"
"v_mfma_f32_16x16x32_bf16 v[92:95], a[48:51], a[104:107], v[92:95]\n"
"v_mfma_f32_16x16x32_bf16 v[96:99], a[48:51], a[112:115], v[96:99]\n"
"v_mfma_f32_16x16x32_bf16 v[96:99], a[52:55], a[116:119], v[96:99]\n"
"global_load_dwordx4 a[36:39], v0, s[34:35] offset:-3072 sc1\n"
"v_mfma_f32_16x16x32_bf16 v[124:127], a[60:63], a[116:119], v[124:127]\n"
"v_mfma_f32_16x16x32_bf16 v[124:127], a[56:59], a[112:115], v[124:127]\n"
"v_mfma_f32_16x16x32_bf16 v[128:131], a[56:59], a[120:123], v[128:131]\n"
"v_mfma_f32_16x16x32_bf16 v[128:131], a[60:63], a[124:127], v[128:131]\n"
"v_mfma_f32_16x16x32_bf16 v[100:103], a[52:55], a[124:127], v[100:103]\n"
"v_mfma_f32_16x16x32_bf16 v[100:103], a[48:51], a[120:123], v[100:103]\n"
"v_mfma_f32_16x16x32_bf16 v[104:107], a[48:51], a[128:131], v[104:107]\n"
"v_mfma_f32_16x16x32_bf16 v[104:107], a[52:55], a[132:135], v[104:107]\n"
"global_load_dwordx4 a[40:43], v0, s[34:35] offset:-2048 sc1\n"
"v_mfma_f32_16x16x32_bf16 v[132:135], a[60:63], a[132:135], v[132:135]\n"
"v_mfma_f32_16x16x32_bf16 v[132:135], a[56:59], a[128:131], v[132:135]\n"
"s_add_u32 m0, s64, 24576\n"
"v_mfma_f32_16x16x32_bf16 v[136:139], a[56:59], a[136:139], v[136:139]\n"
"v_mfma_f32_16x16x32_bf16 v[136:139], a[60:63], a[140:143], v[136:139]\n"
"v_mfma_f32_16x16x32_bf16 v[108:111], a[52:55], a[140:143], v[108:111]\n"
"v_mfma_f32_16x16x32_bf16 v[108:111], a[48:51], a[136:139], v[108:111]\n"
"v_mfma_f32_16x16x32_bf16 v[112:115], a[48:51], a[144:147], v[112:115]\n"
"v_mfma_f32_16x16x32_bf16 v[112:115], a[52:55], a[148:151], v[112:115]\n"
"global_load_dwordx4 a[44:47], v0, s[34:35] offset:-1024 sc1\n"
"v_mfma_f32_16x16x32_bf16 v[140:143], a[60:63], a[148:151], v[140:143]\n"
"v_mfma_f32_16x16x32_bf16 v[140:143], a[56:59], a[144:147], v[140:143]\n"
"v_mfma_f32_16x16x32_bf16 a[12:15], a[56:59], a[152:155], a[12:15]\n"
"v_mfma_f32_16x16x32_bf16 a[12:15], a[60:63], a[156:159], a[12:15]\n"
"v_mfma_f32_16x16x32_bf16 a[8:11], a[52:55], a[156:159], a[8:11]\n"
"v_mfma_f32_16x16x32_bf16 a[8:11], a[48:51], a[152:155], a[8:11]\n"
"s_waitcnt vmcnt(11)\n"
"v_mfma_f32_16x16x32_bf16 v[144:147], a[64:67], a[96:99], v[144:147]\n"
"v_mfma_f32_16x16x32_bf16 v[144:147], a[68:71], a[100:103], v[144:147]\n"
"global_load_dwordx4 a[48:51], v0, s[34:35] offset:0 sc1\n"
"v_mfma_f32_16x16x32_bf16 v[172:175], a[76:79], a[100:103], v[172:175]\n"
"v_mfma_f32_16x16x32_bf16 v[172:175], a[72:75], a[96:99], v[172:175]\n"
"buffer_load_dwordx4 v0, s[48:51], s69 offen offset:0 sc1 lds\n"
"v_mfma_f32_16x16x32_bf16 v[176:179], a[72:75], a[104:107], v[176:179]\n"
"v_mfma_f32_16x16x32_bf16 v[176:179], a[76:79], a[108:111], v[176:179]\n"
"v_mfma_f32_16x16x32_bf16 v[148:151], a[68:71], a[108:111], v[148:151]\n"
"v_mfma_f32_16x16x32_bf16 v[148:151], a[64:67], a[104:107], v[148:151]\n"
"v_mfma_f32_16x16x32_bf16 v[152:155], a[64:67], a[112:115], v[152:155]\n"
"v_lshlrev_b32 v20, 16, v22\n"
"v_mfma_f32_16x16x32_bf16 v[152:155], a[68:71], a[116:119], v[152:155]\n"
"global_load_dwordx4 a[52:55], v0, s[34:35] offset:1024 sc1\n"
"v_and_b32 v21, s63, v22\n"
"v_mfma_f32_16x16x32_bf16 v[180:183], a[76:79], a[116:119], v[180:183]\n"
"v_lshlrev_b32 v22, 16, v23\n"
"v_mfma_f32_16x16x32_bf16 v[180:183], a[72:75], a[112:115], v[180:183]\n"
"v_and_b32 v23, s63, v23\n"
"v_mfma_f32_16x16x32_bf16 v[184:187], a[72:75], a[120:123], v[184:187]\n"
"v_add_f32 v116, v116, v20\n"
"v_mfma_f32_16x16x32_bf16 v[184:187], a[76:79], a[124:127], v[184:187]\n"
"v_add_f32 v117, v117, v21\n"
"v_add_f32 v118, v118, v22\n"
"v_mfma_f32_16x16x32_bf16 v[156:159], a[68:71], a[124:127], v[156:159]\n"
"v_add_f32 v119, v119, v23\n"
"v_mfma_f32_16x16x32_bf16 v[156:159], a[64:67], a[120:123], v[156:159]\n"
"v_add_f32 v120, v120, v20\n"
"v_mfma_f32_16x16x32_bf16 v[160:163], a[64:67], a[128:131], v[160:163]\n"
"v_add_f32 v121, v121, v21\n"
"v_mfma_f32_16x16x32_bf16 v[160:163], a[68:71], a[132:135], v[160:163]\n"
"global_load_dwordx4 a[56:59], v0, s[34:35] offset:2048 sc1\n"
"v_add_f32 v122, v122, v22\n"
"v_mfma_f32_16x16x32_bf16 v[188:191], a[76:79], a[132:135], v[188:191]\n"
"v_add_f32 v123, v123, v23\n"
"v_add_f32 v124, v124, v20\n"
"v_mfma_f32_16x16x32_bf16 v[188:191], a[72:75], a[128:131], v[188:191]\n"
"buffer_load_dwordx4 v0, s[48:51], s69 offen offset:1024 sc1 lds\n"
"v_mfma_f32_16x16x32_bf16 v[192:195], a[72:75], a[136:139], v[192:195]\n"
"v_add_f32 v125, v125, v21\n"
"v_mfma_f32_16x16x32_bf16 v[192:195], a[76:79], a[140:143], v[192:195]\n"
"v_add_f32 v126, v126, v22\n"
"v_mfma_f32_16x16x32_bf16 v[164:167], a[68:71], a[140:143], v[164:167]\n"
"v_add_f32 v127, v127, v23\n"
"v_mfma_f32_16x16x32_bf16 v[164:167], a[64:67], a[136:139], v[164:167]\n"
"v_add_f32 v128, v128, v20\n"
"v_add_f32 v129, v129, v21\n"
"v_mfma_f32_16x16x32_bf16 v[168:171], a[64:67], a[144:147], v[168:171]\n"
"v_add_f32 v130, v130, v22\n"
"v_mfma_f32_16x16x32_bf16 v[168:171], a[68:71], a[148:151], v[168:171]\n"
"global_load_dwordx4 a[60:63], v0, s[34:35] offset:3072 sc1\n"
"v_add_f32 v131, v131, v23\n"
"v_mfma_f32_16x16x32_bf16 v[196:199], a[76:79], a[148:151], v[196:199]\n"
"v_add_f32 v132, v132, v20\n"
"v_mfma_f32_16x16x32_bf16 v[196:199], a[72:75], a[144:147], v[196:199]\n"
"v_add_f32 v133, v133, v21\n"
"v_mfma_f32_16x16x32_bf16 a[20:23], a[72:75], a[152:155], a[20:23]\n"
"s_add_u32 s34, s34, 8192\n"
"v_add_f32 v134, v134, v22\n"
"v_mfma_f32_16x16x32_bf16 a[20:23], a[76:79], a[156:159], a[20:23]\n"
"v_add_f32 v135, v135, v23\n"
"v_mfma_f32_16x16x32_bf16 a[16:19], a[68:71], a[156:159], a[16:19]\n"
"v_add_f32 v136, v136, v20\n"
"v_mfma_f32_16x16x32_bf16 a[16:19], a[64:67], a[152:155], a[16:19]\n"
"s_waitcnt vmcnt(11)\n"
"v_add_f32 v137, v137, v21\n"
"v_mfma_f32_16x16x32_bf16 v[200:203], a[80:83], a[96:99], v[200:203]\n"
"v_add_f32 v138, v138, v22\n"
"v_add_f32 v139, v139, v23\n"
"v_mfma_f32_16x16x32_bf16 v[200:203], a[84:87], a[100:103], v[200:203]\n"
"global_load_dwordx4 a[64:67], v0, s[34:35] offset:-4096 sc1\n"
"v_add_f32 v140, v140, v20\n"
"v_mfma_f32_16x16x32_bf16 v[228:231], a[92:95], a[100:103], v[228:231]\n"
"ds_read_b128 a[100:103], v4 offset:1024\n"
"v_add_f32 v141, v141, v21\n"
"v_mfma_f32_16x16x32_bf16 v[228:231], a[88:91], a[96:99], v[228:231]\n"
"ds_read_b128 a[96:99], v4 offset:0\n"
"v_add_f32 v142, v142, v22\n"
"v_mfma_f32_16x16x32_bf16 v[232:235], a[88:91], a[104:107], v[232:235]\n"
"buffer_load_dwordx4 v0, s[48:51], s69 offen offset:2048 sc1 lds\n"
"v_mfma_f32_16x16x32_bf16 v[232:235], a[92:95], a[108:111], v[232:235]\n"
"v_add_f32 v143, v143, v23\n"
"v_accvgpr_read_b32 v12, a12\n"
"v_mfma_f32_16x16x32_bf16 v[204:207], a[84:87], a[108:111], v[204:207]\n"
"ds_read_b128 a[108:111], v4 offset:3072\n"
"v_accvgpr_read_b32 v13, a13\n"
"v_mfma_f32_16x16x32_bf16 v[204:207], a[80:83], a[104:107], v[204:207]\n"
"ds_read_b128 a[104:107], v4 offset:2048\n"
"v_accvgpr_read_b32 v14, a14\n"
"v_mfma_f32_16x16x32_bf16 v[208:211], a[80:83], a[112:115], v[208:211]\n"
"v_accvgpr_read_b32 v15, a15\n"
"v_mfma_f32_16x16x32_bf16 v[208:211], a[84:87], a[116:119], v[208:211]\n"
"global_load_dwordx4 a[68:71], v0, s[34:35] offset:-3072 sc1\n"
"v_add_f32 v12, v12, v20\n"
"v_mfma_f32_16x16x32_bf16 v[236:239], a[92:95], a[116:119], v[236:239]\n"
"ds_read_b128 a[116:119], v4 offset:5120\n"
"v_add_f32 v13, v13, v21\n"
"v_mfma_f32_16x16x32_bf16 v[236:239], a[88:91], a[112:115], v[236:239]\n"
"ds_read_b128 a[112:115], v4 offset:4096\n"
"v_add_f32 v14, v14, v22\n"
"v_mfma_f32_16x16x32_bf16 v[240:243], a[88:91], a[120:123], v[240:243]\n"
"v_add_f32 v15, v15, v23\n"
"v_accvgpr_write_b32 a12, v12\n"
"v_mfma_f32_16x16x32_bf16 v[240:243], a[92:95], a[124:127], v[240:243]\n"
"v_accvgpr_write_b32 a13, v13\n"
"v_mfma_f32_16x16x32_bf16 v[212:215], a[84:87], a[124:127], v[212:215]\n"
"ds_read_b128 a[124:127], v4 offset:7168\n"
"v_accvgpr_write_b32 a14, v14\n"
"v_mfma_f32_16x16x32_bf16 v[212:215], a[80:83], a[120:123], v[212:215]\n"
"ds_read_b128 a[120:123], v4 offset:6144\n"
"v_accvgpr_write_b32 a15, v15\n"
"v_mfma_f32_16x16x32_bf16 v[216:219], a[80:83], a[128:131], v[216:219]\n"
"v_mfma_f32_16x16x32_bf16 v[216:219], a[84:87], a[132:135], v[216:219]\n"
"global_load_dwordx4 a[72:75], v0, s[34:35] offset:-2048 sc1\n"
"v_mfma_f32_16x16x32_bf16 v[244:247], a[92:95], a[132:135], v[244:247]\n"
"ds_read_b128 a[132:135], v4 offset:9216\n"
"v_mfma_f32_16x16x32_bf16 v[244:247], a[88:91], a[128:131], v[244:247]\n"
"ds_read_b128 a[128:131], v4 offset:8192\n"
"v_mfma_f32_16x16x32_bf16 v[248:251], a[88:91], a[136:139], v[248:251]\n"
"buffer_load_dwordx4 v0, s[48:51], s69 offen offset:3072 sc1 lds\n"
"v_mfma_f32_16x16x32_bf16 v[248:251], a[92:95], a[140:143], v[248:251]\n"
"v_mfma_f32_16x16x32_bf16 v[220:223], a[84:87], a[140:143], v[220:223]\n"
"ds_read_b128 a[140:143], v4 offset:11264\n"
"v_mfma_f32_16x16x32_bf16 v[220:223], a[80:83], a[136:139], v[220:223]\n"
"ds_read_b128 a[136:139], v4 offset:10240\n"
"v_mfma_f32_16x16x32_bf16 v[224:227], a[80:83], a[144:147], v[224:227]\n"
"v_mfma_f32_16x16x32_bf16 v[224:227], a[84:87], a[148:151], v[224:227]\n"
"global_load_dwordx4 a[76:79], v0, s[34:35] offset:-1024 sc1\n"
"v_mfma_f32_16x16x32_bf16 v[252:255], a[92:95], a[148:151], v[252:255]\n"
"L_st7374:\n"
"s_cbranch_execz L_st8243\n"
"ds_read_b128 a[148:151], v4 offset:13312\n"
"v_mfma_f32_16x16x32_bf16 v[252:255], a[88:91], a[144:147], v[252:255]\n"
"ds_read_b128 a[144:147], v4 offset:12288\n"
"v_mfma_f32_16x16x32_bf16 a[28:31], a[88:91], a[152:155], a[28:31]\n"
"v_mfma_f32_16x16x32_bf16 a[28:31], a[92:95], a[156:159], a[28:31]\n"
"v_mfma_f32_16x16x32_bf16 a[24:27], a[84:87], a[156:159], a[24:27]\n"
"ds_read_b128 a[156:159], v4 offset:15360\n"
"v_mfma_f32_16x16x32_bf16 a[24:27], a[80:83], a[152:155], a[24:27]\n"
"s_waitcnt vmcnt(12) lgkmcnt(11)\n"
"ds_read_b128 a[152:155], v4 offset:14336\n"
"v_mfma_f32_16x16x32_bf16 v[32:35], a[32:35], a[96:99], v[32:35]\n"
"v_mfma_f32_16x16x32_bf16 v[32:35], a[36:39], a[100:103], v[32:35]\n"
"global_load_dwordx4 a[80:83], v0, s[34:35] offset:0 sc1\n"
"v_lshrrev_b32 v16, 4, v0\n"
"v_mfma_f32_16x16x32_bf16 v[60:63], a[44:47], a[100:103], v[60:63]\n"
"v_and_b32 v16, 0x30, v16\n"
"v_mfma_f32_16x16x32_bf16 v[60:63], a[40:43], a[96:99], v[60:63]\n"
"s_add_u32 m0, s64, 28672\n"
"buffer_load_dwordx2 v[18:19], v16, s[44:47], s61 offen offset:128\n"
"v_mfma_f32_16x16x32_bf16 v[64:67], a[40:43], a[104:107], v[64:67]\n"
"v_mfma_f32_16x16x32_bf16 v[64:67], a[44:47], a[108:111], v[64:67]\n"
"v_mfma_f32_16x16x32_bf16 v[36:39], a[36:39], a[108:111], v[36:39]\n"
"v_mfma_f32_16x16x32_bf16 v[36:39], a[32:35], a[104:107], v[36:39]\n"
"s_waitcnt lgkmcnt(8)\n"
"v_mfma_f32_16x16x32_bf16 v[40:43], a[32:35], a[112:115], v[40:43]\n"
"v_mfma_f32_16x16x32_bf16 v[40:43], a[36:39], a[116:119], v[40:43]\n"
"global_load_dwordx4 a[84:87], v0, s[34:35] offset:1024 sc1\n"
"v_lshrrev_b32 v20, 4, v0\n"
"v_mfma_f32_16x16x32_bf16 v[68:71], a[44:47], a[116:119], v[68:71]\n"
"v_and_b32 v20, 0x30, v20\n"
"v_mfma_f32_16x16x32_bf16 v[68:71], a[40:43], a[112:115], v[68:71]\n"
"buffer_load_dwordx2 v[22:23], v20, s[44:47], s61 offen offset:136\n"
"v_mfma_f32_16x16x32_bf16 v[72:75], a[40:43], a[120:123], v[72:75]\n"
"v_mfma_f32_16x16x32_bf16 v[72:75], a[44:47], a[124:127], v[72:75]\n"
"v_mfma_f32_16x16x32_bf16 v[44:47], a[36:39], a[124:127], v[44:47]\n"
"v_mfma_f32_16x16x32_bf16 v[44:47], a[32:35], a[120:123], v[44:47]\n"
"s_waitcnt lgkmcnt(4)\n"
"v_mfma_f32_16x16x32_bf16 v[48:51], a[32:35], a[128:131], v[48:51]\n"
"v_mfma_f32_16x16x32_bf16 v[48:51], a[36:39], a[132:135], v[48:51]\n"
"global_load_dwordx4 a[88:91], v0, s[34:35] offset:2048 sc1\n"
"v_lshrrev_b32 v24, 4, v0\n"
"v_mfma_f32_16x16x32_bf16 v[76:79], a[44:47], a[132:135], v[76:79]\n"
"v_and_b32 v24, 0x30, v24\n"
"v_mfma_f32_16x16x32_bf16 v[76:79], a[40:43], a[128:131], v[76:79]\n"
"buffer_load_dwordx4 v1, s[48:51], s69 offen offset:0 sc1 lds\n"
"buffer_load_dwordx2 v[26:27], v24, s[44:47], s61 offen offset:192\n"
"v_mfma_f32_16x16x32_bf16 v[80:83], a[40:43], a[136:139], v[80:83]\n"
"v_mfma_f32_16x16x32_bf16 v[80:83], a[44:47], a[140:143], v[80:83]\n"
"v_mfma_f32_16x16x32_bf16 v[52:55], a[36:39], a[140:143], v[52:55]\n"
"v_mfma_f32_16x16x32_bf16 v[52:55], a[32:35], a[136:139], v[52:55]\n"
"s_waitcnt lgkmcnt(0)\n"
"v_mfma_f32_16x16x32_bf16 v[56:59], a[32:35], a[144:147], v[56:59]\n"
"v_mfma_f32_16x16x32_bf16 v[56:59], a[36:39], a[148:151], v[56:59]\n"
"global_load_dwordx4 a[92:95], v0, s[34:35] offset:3072 sc1\n"
"v_lshrrev_b32 v28, 4, v0\n"
"v_mfma_f32_16x16x32_bf16 v[84:87], a[44:47], a[148:151], v[84:87]\n"
"v_and_b32 v28, 0x30, v28\n"
"v_mfma_f32_16x16x32_bf16 v[84:87], a[40:43], a[144:147], v[84:87]\n"
"buffer_load_dwordx2 v[30:31], v28, s[44:47], s61 offen offset:200\n"
"v_mfma_f32_16x16x32_bf16 a[4:7], a[40:43], a[152:155], a[4:7]\n"
"s_add_u32 s34, s34, 8192\n"
"v_mfma_f32_16x16x32_bf16 a[4:7], a[44:47], a[156:159], a[4:7]\n"
"v_mfma_f32_16x16x32_bf16 a[0:3], a[36:39], a[156:159], a[0:3]\n"
"v_mfma_f32_16x16x32_bf16 a[0:3], a[32:35], a[152:155], a[0:3]\n"
"s_waitcnt vmcnt(15)\n"
"v_mfma_f32_16x16x32_bf16 v[88:91], a[48:51], a[96:99], v[88:91]\n"
"v_mfma_f32_16x16x32_bf16 v[88:91], a[52:55], a[100:103], v[88:91]\n"
"global_load_dwordx4 a[32:35], v0, s[34:35] offset:-4096 sc1\n"
"v_mfma_f32_16x16x32_bf16 v[116:119], a[60:63], a[100:103], v[116:119]\n"
"v_mfma_f32_16x16x32_bf16 v[116:119], a[56:59], a[96:99], v[116:119]\n"
"buffer_load_dwordx4 v1, s[48:51], s69 offen offset:1024 sc1 lds\n"
"v_mfma_f32_16x16x32_bf16 v[120:123], a[56:59], a[104:107], v[120:123]\n"
"v_mfma_f32_16x16x32_bf16 v[120:123], a[60:63], a[108:111], v[120:123]\n"
"v_mfma_f32_16x16x32_bf16 v[92:95], a[52:55], a[108:111], v[92:95]\n"
"v_mfma_f32_16x16x32_bf16 v[92:95], a[48:51], a[104:107], v[92:95]\n"
"v_mfma_f32_16x16x32_bf16 v[96:99], a[48:51], a[112:115], v[96:99]\n"
"v_mfma_f32_16x16x32_bf16 v[96:99], a[52:55], a[116:119], v[96:99]\n"
"global_load_dwordx4 a[36:39], v0, s[34:35] offset:-3072 sc1\n"
"v_mfma_f32_16x16x32_bf16 v[124:127], a[60:63], a[116:119], v[124:127]\n"
"v_mfma_f32_16x16x32_bf16 v[124:127], a[56:59], a[112:115], v[124:127]\n"
"v_mfma_f32_16x16x32_bf16 v[128:131], a[56:59], a[120:123], v[128:131]\n"
"v_mfma_f32_16x16x32_bf16 v[128:131], a[60:63], a[124:127], v[128:131]\n"
"v_mfma_f32_16x16x32_bf16 v[100:103], a[52:55], a[124:127], v[100:103]\n"
"v_mfma_f32_16x16x32_bf16 v[100:103], a[48:51], a[120:123], v[100:103]\n"
"v_mfma_f32_16x16x32_bf16 v[104:107], a[48:51], a[128:131], v[104:107]\n"
"v_mfma_f32_16x16x32_bf16 v[104:107], a[52:55], a[132:135], v[104:107]\n"
"global_load_dwordx4 a[40:43], v0, s[34:35] offset:-2048 sc1\n"
"v_mfma_f32_16x16x32_bf16 v[132:135], a[60:63], a[132:135], v[132:135]\n"
"v_mfma_f32_16x16x32_bf16 v[132:135], a[56:59], a[128:131], v[132:135]\n"
"buffer_load_dwordx4 v1, s[48:51], s69 offen offset:2048 sc1 lds\n"
"v_mfma_f32_16x16x32_bf16 v[136:139], a[56:59], a[136:139], v[136:139]\n"
"v_mfma_f32_16x16x32_bf16 v[136:139], a[60:63], a[140:143], v[136:139]\n"
"v_mfma_f32_16x16x32_bf16 v[108:111], a[52:55], a[140:143], v[108:111]\n"
"v_mfma_f32_16x16x32_bf16 v[108:111], a[48:51], a[136:139], v[108:111]\n"
"v_mfma_f32_16x16x32_bf16 v[112:115], a[48:51], a[144:147], v[112:115]\n"
"v_mfma_f32_16x16x32_bf16 v[112:115], a[52:55], a[148:151], v[112:115]\n"
"global_load_dwordx4 a[44:47], v0, s[34:35] offset:-1024 sc1\n"
"v_mfma_f32_16x16x32_bf16 v[140:143], a[60:63], a[148:151], v[140:143]\n"
"v_mfma_f32_16x16x32_bf16 v[140:143], a[56:59], a[144:147], v[140:143]\n"
"v_mfma_f32_16x16x32_bf16 a[12:15], a[56:59], a[152:155], a[12:15]\n"
"v_mfma_f32_16x16x32_bf16 a[12:15], a[60:63], a[156:159], a[12:15]\n"
"v_mfma_f32_16x16x32_bf16 a[8:11], a[52:55], a[156:159], a[8:11]\n"
"v_mfma_f32_16x16x32_bf16 a[8:11], a[48:51], a[152:155], a[8:11]\n"
"s_waitcnt vmcnt(15)\n"
"v_mfma_f32_16x16x32_bf16 v[144:147], a[64:67], a[96:99], v[144:147]\n"
"v_mfma_f32_16x16x32_bf16 v[144:147], a[68:71], a[100:103], v[144:147]\n"
"global_load_dwordx4 a[48:51], v0, s[34:35] offset:0 sc1\n"
"v_mfma_f32_16x16x32_bf16 v[172:175], a[76:79], a[100:103], v[172:175]\n"
"v_mfma_f32_16x16x32_bf16 v[172:175], a[72:75], a[96:99], v[172:175]\n"
"buffer_load_dwordx4 v1, s[48:51], s69 offen offset:3072 sc1 lds\n"
"v_mfma_f32_16x16x32_bf16 v[176:179], a[72:75], a[104:107], v[176:179]\n"
"v_mfma_f32_16x16x32_bf16 v[176:179], a[76:79], a[108:111], v[176:179]\n"
"v_mfma_f32_16x16x32_bf16 v[148:151], a[68:71], a[108:111], v[148:151]\n"
"v_mfma_f32_16x16x32_bf16 v[148:151], a[64:67], a[104:107], v[148:151]\n"
"v_mfma_f32_16x16x32_bf16 v[152:155], a[64:67], a[112:115], v[152:155]\n"
"v_mfma_f32_16x16x32_bf16 v[152:155], a[68:71], a[116:119], v[152:155]\n"
"global_load_dwordx4 a[52:55], v0, s[34:35] offset:1024 sc1\n"
"v_mfma_f32_16x16x32_bf16 v[180:183], a[76:79], a[116:119], v[180:183]\n"
"v_mfma_f32_16x16x32_bf16 v[180:183], a[72:75], a[112:115], v[180:183]\n"
"v_mfma_f32_16x16x32_bf16 v[184:187], a[72:75], a[120:123], v[184:187]\n"
"v_mfma_f32_16x16x32_bf16 v[184:187], a[76:79], a[124:127], v[184:187]\n"
"v_mfma_f32_16x16x32_bf16 v[156:159], a[68:71], a[124:127], v[156:159]\n"
"v_mfma_f32_16x16x32_bf16 v[156:159], a[64:67], a[120:123], v[156:159]\n"
"v_mfma_f32_16x16x32_bf16 v[160:163], a[64:67], a[128:131], v[160:163]\n"
"v_mfma_f32_16x16x32_bf16 v[160:163], a[68:71], a[132:135], v[160:163]\n"
"global_load_dwordx4 a[56:59], v0, s[34:35] offset:2048 sc1\n"
"v_mfma_f32_16x16x32_bf16 v[188:191], a[76:79], a[132:135], v[188:191]\n"
"v_mfma_f32_16x16x32_bf16 v[188:191], a[72:75], a[128:131], v[188:191]\n"
"v_mfma_f32_16x16x32_bf16 v[192:195], a[72:75], a[136:139], v[192:195]\n"
"s_cmp_lg_u32 s29, 0\n"
"s_cselect_b32 s60, s31, s60\n"
"s_cselect_b32 s30, 0, s30\n"
"v_mfma_f32_16x16x32_bf16 v[192:195], a[76:79], a[140:143], v[192:195]\n"
"v_mfma_f32_16x16x32_bf16 v[164:167], a[68:71], a[140:143], v[164:167]\n"
"s_cmp_lg_u32 s29, 0\n"
"s_cselect_b32 s31, s75, s72\n"
"v_mfma_f32_16x16x32_bf16 v[164:167], a[64:67], a[136:139], v[164:167]\n"
"s_sub_u32 s52, s52, s72\n"
"s_subb_u32 s53, s53, 0\n"
"v_mfma_f32_16x16x32_bf16 v[168:171], a[64:67], a[144:147], v[168:171]\n"
"s_add_u32 s52, s52, s31\n"
"s_addc_u32 s53, s53, 0\n"
"v_mfma_f32_16x16x32_bf16 v[168:171], a[68:71], a[148:151], v[168:171]\n"
"global_load_dwordx4 a[60:63], v0, s[34:35] offset:3072 sc1\n"
"v_mfma_f32_16x16x32_bf16 v[196:199], a[76:79], a[148:151], v[196:199]\n"
"s_mov_b32 s72, s31\n"
"v_mfma_f32_16x16x32_bf16 v[196:199], a[72:75], a[144:147], v[196:199]\n"
"v_mfma_f32_16x16x32_bf16 a[20:23], a[72:75], a[152:155], a[20:23]\n"
"s_add_u32 s34, s34, 8192\n"
"v_mfma_f32_16x16x32_bf16 a[20:23], a[76:79], a[156:159], a[20:23]\n"
"v_mfma_f32_16x16x32_bf16 a[16:19], a[68:71], a[156:159], a[16:19]\n"
"s_cmp_lg_u32 s29, 0\n"
"s_cselect_b32 s31, s69, s73\n"
"v_mfma_f32_16x16x32_bf16 a[16:19], a[64:67], a[152:155], a[16:19]\n"
"s_sub_u32 s54, s54, s73\n"
"s_subb_u32 s55, s55, 0\n"
"s_waitcnt vmcnt(12)\n"
"v_mfma_f32_16x16x32_bf16 v[200:203], a[80:83], a[96:99], v[200:203]\n"
"s_add_u32 s54, s54, s31\n"
"s_addc_u32 s55, s55, 0\n"
"v_mfma_f32_16x16x32_bf16 v[200:203], a[84:87], a[100:103], v[200:203]\n"
"global_load_dwordx4 a[64:67], v0, s[34:35] offset:-4096 sc1\n"
"v_mfma_f32_16x16x32_bf16 v[228:231], a[92:95], a[100:103], v[228:231]\n"
"s_mov_b32 s73, s31\n"
"ds_read_b128 a[100:103], v5 offset:1024\n"
"v_mfma_f32_16x16x32_bf16 v[228:231], a[88:91], a[96:99], v[228:231]\n"
"ds_read_b128 a[96:99], v5 offset:0\n"
"v_mfma_f32_16x16x32_bf16 v[232:235], a[88:91], a[104:107], v[232:235]\n"
"v_mfma_f32_16x16x32_bf16 v[232:235], a[92:95], a[108:111], v[232:235]\n"
"v_mfma_f32_16x16x32_bf16 v[204:207], a[84:87], a[108:111], v[204:207]\n"
"ds_read_b128 a[108:111], v5 offset:3072\n"
"v_mfma_f32_16x16x32_bf16 v[204:207], a[80:83], a[104:107], v[204:207]\n"
"ds_read_b128 a[104:107], v5 offset:2048\n"
"v_mfma_f32_16x16x32_bf16 v[208:211], a[80:83], a[112:115], v[208:211]\n"
"v_mfma_f32_16x16x32_bf16 v[208:211], a[84:87], a[116:119], v[208:211]\n"
"global_load_dwordx4 a[68:71], v0, s[34:35] offset:-3072 sc1\n"
"v_mfma_f32_16x16x32_bf16 v[236:239], a[92:95], a[116:119], v[236:239]\n"
"ds_read_b128 a[116:119], v5 offset:5120\n"
"v_mfma_f32_16x16x32_bf16 v[236:239], a[88:91], a[112:115], v[236:239]\n"
"ds_read_b128 a[112:115], v5 offset:4096\n"
"v_mfma_f32_16x16x32_bf16 v[240:243], a[88:91], a[120:123], v[240:243]\n"
"v_mfma_f32_16x16x32_bf16 v[240:243], a[92:95], a[124:127], v[240:243]\n"
"v_mfma_f32_16x16x32_bf16 v[212:215], a[84:87], a[124:127], v[212:215]\n"
"ds_read_b128 a[124:127], v5 offset:7168\n"
"v_mfma_f32_16x16x32_bf16 v[212:215], a[80:83], a[120:123], v[212:215]\n"
"ds_read_b128 a[120:123], v5 offset:6144\n"
"v_mfma_f32_16x16x32_bf16 v[216:219], a[80:83], a[128:131], v[216:219]\n"
"v_mfma_f32_16x16x32_bf16 v[216:219], a[84:87], a[132:135], v[216:219]\n"
"global_load_dwordx4 a[72:75], v0, s[34:35] offset:-2048 sc1\n"
"v_mfma_f32_16x16x32_bf16 v[244:247], a[92:95], a[132:135], v[244:247]\n"
"ds_read_b128 a[132:135], v5 offset:9216\n"
"v_mfma_f32_16x16x32_bf16 v[244:247], a[88:91], a[128:131], v[244:247]\n"
"ds_read_b128 a[128:131], v5 offset:8192\n"
"v_mfma_f32_16x16x32_bf16 v[248:251], a[88:91], a[136:139], v[248:251]\n"
"v_mfma_f32_16x16x32_bf16 v[248:251], a[92:95], a[140:143], v[248:251]\n"
"v_mfma_f32_16x16x32_bf16 v[220:223], a[84:87], a[140:143], v[220:223]\n"
"ds_read_b128 a[140:143], v5 offset:11264\n"
"v_mfma_f32_16x16x32_bf16 v[220:223], a[80:83], a[136:139], v[220:223]\n"
"ds_read_b128 a[136:139], v5 offset:10240\n"
"v_mfma_f32_16x16x32_bf16 v[224:227], a[80:83], a[144:147], v[224:227]\n"
"v_mfma_f32_16x16x32_bf16 v[224:227], a[84:87], a[148:151], v[224:227]\n"
"global_load_dwordx4 a[76:79], v0, s[34:35] offset:-1024 sc1\n"
"v_mfma_f32_16x16x32_bf16 v[252:255], a[92:95], a[148:151], v[252:255]\n"
"ds_read_b128 a[148:151], v5 offset:13312\n"
"v_mfma_f32_16x16x32_bf16 v[252:255], a[88:91], a[144:147], v[252:255]\n"
"ds_read_b128 a[144:147], v5 offset:12288\n"
"v_mfma_f32_16x16x32_bf16 a[28:31], a[88:91], a[152:155], a[28:31]\n"
"v_mfma_f32_16x16x32_bf16 a[28:31], a[92:95], a[156:159], a[28:31]\n"
"v_mfma_f32_16x16x32_bf16 a[24:27], a[84:87], a[156:159], a[24:27]\n"
"ds_read_b128 a[156:159], v5 offset:15360\n"
"v_mfma_f32_16x16x32_bf16 a[24:27], a[80:83], a[152:155], a[24:27]\n"
"s_waitcnt vmcnt(9) lgkmcnt(11)\n"
"ds_read_b128 a[152:155], v5 offset:14336\n"
"v_mfma_f32_16x16x32_bf16 v[32:35], a[32:35], a[96:99], v[32:35]\n"
"v_mfma_f32_16x16x32_bf16 v[32:35], a[36:39], a[100:103], v[32:35]\n"
"global_load_dwordx4 a[80:83], v0, s[34:35] offset:0 sc1\n"
"v_mfma_f32_16x16x32_bf16 v[60:63], a[44:47], a[100:103], v[60:63]\n"
"v_mfma_f32_16x16x32_bf16 v[60:63], a[40:43], a[96:99], v[60:63]\n"
"v_mfma_f32_16x16x32_bf16 v[64:67], a[40:43], a[104:107], v[64:67]\n"
"v_mfma_f32_16x16x32_bf16 v[64:67], a[44:47], a[108:111], v[64:67]\n"
"v_mfma_f32_16x16x32_bf16 v[36:39], a[36:39], a[108:111], v[36:39]\n"
"v_mfma_f32_16x16x32_bf16 v[36:39], a[32:35], a[104:107], v[36:39]\n"
"s_waitcnt lgkmcnt(8)\n"
"v_mfma_f32_16x16x32_bf16 v[40:43], a[32:35], a[112:115], v[40:43]\n"
"v_mfma_f32_16x16x32_bf16 v[40:43], a[36:39], a[116:119], v[40:43]\n"
"global_load_dwordx4 a[84:87], v0, s[34:35] offset:1024 sc1\n"
"v_mfma_f32_16x16x32_bf16 v[68:71], a[44:47], a[116:119], v[68:71]\n"
"v_mfma_f32_16x16x32_bf16 v[68:71], a[40:43], a[112:115], v[68:71]\n"
"v_mfma_f32_16x16x32_bf16 v[72:75], a[40:43], a[120:123], v[72:75]\n"
"v_mfma_f32_16x16x32_bf16 v[72:75], a[44:47], a[124:127], v[72:75]\n"
"v_mfma_f32_16x16x32_bf16 v[44:47], a[36:39], a[124:127], v[44:47]\n"
"v_mfma_f32_16x16x32_bf16 v[44:47], a[32:35], a[120:123], v[44:47]\n"
"s_waitcnt lgkmcnt(4)\n"
"v_mfma_f32_16x16x32_bf16 v[48:51], a[32:35], a[128:131], v[48:51]\n"
"s_lshr_b32 s66, 20, s62\n"
"v_mfma_f32_16x16x32_bf16 v[48:51], a[36:39], a[132:135], v[48:51]\n"
"global_load_dwordx4 a[88:91], v0, s[34:35] offset:2048 sc1\n"
"v_mfma_f32_16x16x32_bf16 v[76:79], a[44:47], a[132:135], v[76:79]\n"
"s_and_b32 s66, s66, 1\n"
"s_cselect_b32 s70, 4294574080, 0\n"
"s_cselect_b32 s71, -1, 0\n"
"s_cselect_b32 s66, 0, 131072\n"
"v_mfma_f32_16x16x32_bf16 v[76:79], a[40:43], a[128:131], v[76:79]\n"
"v_mfma_f32_16x16x32_bf16 v[80:83], a[40:43], a[136:139], v[80:83]\n"
"s_add_u32 s70, s70, s66\n"
"v_mfma_f32_16x16x32_bf16 v[80:83], a[44:47], a[140:143], v[80:83]\n"
"v_mfma_f32_16x16x32_bf16 v[52:55], a[36:39], a[140:143], v[52:55]\n"
"v_mfma_f32_16x16x32_bf16 v[52:55], a[32:35], a[136:139], v[52:55]\n"
"s_waitcnt lgkmcnt(0)\n"
"v_mfma_f32_16x16x32_bf16 v[56:59], a[32:35], a[144:147], v[56:59]\n"
"v_mfma_f32_16x16x32_bf16 v[56:59], a[36:39], a[148:151], v[56:59]\n"
"global_load_dwordx4 a[92:95], v0, s[34:35] offset:3072 sc1\n"
"v_mfma_f32_16x16x32_bf16 v[84:87], a[44:47], a[148:151], v[84:87]\n"
"v_mfma_f32_16x16x32_bf16 v[84:87], a[40:43], a[144:147], v[84:87]\n"
"v_mfma_f32_16x16x32_bf16 a[4:7], a[40:43], a[152:155], a[4:7]\n"
"s_add_u32 s34, s34, 8192\n"
"v_mfma_f32_16x16x32_bf16 a[4:7], a[44:47], a[156:159], a[4:7]\n"
"v_mfma_f32_16x16x32_bf16 a[0:3], a[36:39], a[156:159], a[0:3]\n"
"v_mfma_f32_16x16x32_bf16 a[0:3], a[32:35], a[152:155], a[0:3]\n"
"s_waitcnt vmcnt(8)\n"
"v_mfma_f32_16x16x32_bf16 v[88:91], a[48:51], a[96:99], v[88:91]\n"
"v_mfma_f32_16x16x32_bf16 v[88:91], a[52:55], a[100:103], v[88:91]\n"
"global_load_dwordx4 a[32:35], v0, s[34:35] offset:-4096 sc1\n"
"v_mfma_f32_16x16x32_bf16 v[116:119], a[60:63], a[100:103], v[116:119]\n"
"v_mfma_f32_16x16x32_bf16 v[116:119], a[56:59], a[96:99], v[116:119]\n"
"v_mfma_f32_16x16x32_bf16 v[120:123], a[56:59], a[104:107], v[120:123]\n"
"v_mfma_f32_16x16x32_bf16 v[120:123], a[60:63], a[108:111], v[120:123]\n"
"v_mfma_f32_16x16x32_bf16 v[92:95], a[52:55], a[108:111], v[92:95]\n"
"v_mfma_f32_16x16x32_bf16 v[92:95], a[48:51], a[104:107], v[92:95]\n"
"v_mfma_f32_16x16x32_bf16 v[96:99], a[48:51], a[112:115], v[96:99]\n"
"v_mfma_f32_16x16x32_bf16 v[96:99], a[52:55], a[116:119], v[96:99]\n"
"global_load_dwordx4 a[36:39], v0, s[34:35] offset:-3072 sc1\n"
"v_mfma_f32_16x16x32_bf16 v[124:127], a[60:63], a[116:119], v[124:127]\n"
"buffer_load_dwordx4 v[12:15], v0, s[56:59], %25 offen sc1\n"
"v_mfma_f32_16x16x32_bf16 v[124:127], a[56:59], a[112:115], v[124:127]\n"
"v_mfma_f32_16x16x32_bf16 v[128:131], a[56:59], a[120:123], v[128:131]\n"
"v_mfma_f32_16x16x32_bf16 v[128:131], a[60:63], a[124:127], v[128:131]\n"
"v_mfma_f32_16x16x32_bf16 v[100:103], a[52:55], a[124:127], v[100:103]\n"
"v_mfma_f32_16x16x32_bf16 v[100:103], a[48:51], a[120:123], v[100:103]\n"
"v_mfma_f32_16x16x32_bf16 v[104:107], a[48:51], a[128:131], v[104:107]\n"
"v_mfma_f32_16x16x32_bf16 v[104:107], a[52:55], a[132:135], v[104:107]\n"
"global_load_dwordx4 a[40:43], v0, s[34:35] offset:-2048 sc1\n"
"v_mfma_f32_16x16x32_bf16 v[132:135], a[60:63], a[132:135], v[132:135]\n"
"v_mfma_f32_16x16x32_bf16 v[132:135], a[56:59], a[128:131], v[132:135]\n"
"v_mfma_f32_16x16x32_bf16 v[136:139], a[56:59], a[136:139], v[136:139]\n"
"v_mfma_f32_16x16x32_bf16 v[136:139], a[60:63], a[140:143], v[136:139]\n"
"v_mfma_f32_16x16x32_bf16 v[108:111], a[52:55], a[140:143], v[108:111]\n"
"v_mfma_f32_16x16x32_bf16 v[108:111], a[48:51], a[136:139], v[108:111]\n"
"v_mfma_f32_16x16x32_bf16 v[112:115], a[48:51], a[144:147], v[112:115]\n"
"v_mfma_f32_16x16x32_bf16 v[112:115], a[52:55], a[148:151], v[112:115]\n"
"global_load_dwordx4 a[44:47], v0, s[34:35] offset:-1024 sc1\n"
"v_mfma_f32_16x16x32_bf16 v[140:143], a[60:63], a[148:151], v[140:143]\n"
"v_mfma_f32_16x16x32_bf16 v[140:143], a[56:59], a[144:147], v[140:143]\n"
"v_mfma_f32_16x16x32_bf16 a[12:15], a[56:59], a[152:155], a[12:15]\n"
"v_mfma_f32_16x16x32_bf16 a[12:15], a[60:63], a[156:159], a[12:15]\n"
"v_mfma_f32_16x16x32_bf16 a[8:11], a[52:55], a[156:159], a[8:11]\n"
"v_mfma_f32_16x16x32_bf16 a[8:11], a[48:51], a[152:155], a[8:11]\n"
"s_waitcnt vmcnt(9)\n"
"v_mfma_f32_16x16x32_bf16 v[144:147], a[64:67], a[96:99], v[144:147]\n"
"v_mfma_f32_16x16x32_bf16 v[144:147], a[68:71], a[100:103], v[144:147]\n"
"global_load_dwordx4 a[48:51], v0, s[34:35] offset:0 sc1\n"
"v_mfma_f32_16x16x32_bf16 v[172:175], a[76:79], a[100:103], v[172:175]\n"
"v_mfma_f32_16x16x32_bf16 v[172:175], a[72:75], a[96:99], v[172:175]\n"
"v_mfma_f32_16x16x32_bf16 v[176:179], a[72:75], a[104:107], v[176:179]\n"
"v_mfma_f32_16x16x32_bf16 v[176:179], a[76:79], a[108:111], v[176:179]\n"
"v_mfma_f32_16x16x32_bf16 v[148:151], a[68:71], a[108:111], v[148:151]\n"
"v_mfma_f32_16x16x32_bf16 v[148:151], a[64:67], a[104:107], v[148:151]\n"
"v_mfma_f32_16x16x32_bf16 v[152:155], a[64:67], a[112:115], v[152:155]\n"
"v_mfma_f32_16x16x32_bf16 v[152:155], a[68:71], a[116:119], v[152:155]\n"
"global_load_dwordx4 a[52:55], v0, s[34:35] offset:1024 sc1\n"
"v_mfma_f32_16x16x32_bf16 v[180:183], a[76:79], a[116:119], v[180:183]\n"
"v_mfma_f32_16x16x32_bf16 v[180:183], a[72:75], a[112:115], v[180:183]\n"
"v_mfma_f32_16x16x32_bf16 v[184:187], a[72:75], a[120:123], v[184:187]\n"
"v_mfma_f32_16x16x32_bf16 v[184:187], a[76:79], a[124:127], v[184:187]\n"
"v_mfma_f32_16x16x32_bf16 v[156:159], a[68:71], a[124:127], v[156:159]\n"
"v_mfma_f32_16x16x32_bf16 v[156:159], a[64:67], a[120:123], v[156:159]\n"
"v_mfma_f32_16x16x32_bf16 v[160:163], a[64:67], a[128:131], v[160:163]\n"
"v_mfma_f32_16x16x32_bf16 v[160:163], a[68:71], a[132:135], v[160:163]\n"
"global_load_dwordx4 a[56:59], v0, s[34:35] offset:2048 sc1\n"
"v_mfma_f32_16x16x32_bf16 v[188:191], a[76:79], a[132:135], v[188:191]\n"
"v_mfma_f32_16x16x32_bf16 v[188:191], a[72:75], a[128:131], v[188:191]\n"
"v_mfma_f32_16x16x32_bf16 v[192:195], a[72:75], a[136:139], v[192:195]\n"
"v_mfma_f32_16x16x32_bf16 v[192:195], a[76:79], a[140:143], v[192:195]\n"
"v_mfma_f32_16x16x32_bf16 v[164:167], a[68:71], a[140:143], v[164:167]\n"
"s_add_u32 s67, s78, s62\n"
"v_mfma_f32_16x16x32_bf16 v[164:167], a[64:67], a[136:139], v[164:167]\n"
"v_mfma_f32_16x16x32_bf16 v[168:171], a[64:67], a[144:147], v[168:171]\n"
"s_add_u32 s67, s67, 1\n"
"v_mfma_f32_16x16x32_bf16 v[168:171], a[68:71], a[148:151], v[168:171]\n"
"global_load_dwordx4 a[60:63], v0, s[34:35] offset:3072 sc1\n"
"v_mfma_f32_16x16x32_bf16 v[196:199], a[76:79], a[148:151], v[196:199]\n"
"v_mfma_f32_16x16x32_bf16 v[196:199], a[72:75], a[144:147], v[196:199]\n"
"v_mfma_f32_16x16x32_bf16 a[20:23], a[72:75], a[152:155], a[20:23]\n"
"s_add_u32 s34, s34, 8192\n"
"v_mfma_f32_16x16x32_bf16 a[20:23], a[76:79], a[156:159], a[20:23]\n"
"v_mfma_f32_16x16x32_bf16 a[16:19], a[68:71], a[156:159], a[16:19]\n"
"v_mfma_f32_16x16x32_bf16 a[16:19], a[64:67], a[152:155], a[16:19]\n"
"s_waitcnt vmcnt(9)\n"
"v_mfma_f32_16x16x32_bf16 v[200:203], a[80:83], a[96:99], v[200:203]\n"
"v_mfma_f32_16x16x32_bf16 v[200:203], a[84:87], a[100:103], v[200:203]\n"
"global_load_dwordx4 a[64:67], v0, s[34:35] offset:-4096 sc1\n"
"v_mfma_f32_16x16x32_bf16 v[228:231], a[92:95], a[100:103], v[228:231]\n"
"ds_read_b128 a[100:103], v2 offset:17408\n"
"v_mfma_f32_16x16x32_bf16 v[228:231], a[88:91], a[96:99], v[228:231]\n"
"ds_read_b128 a[96:99], v2 offset:16384\n"
"v_mfma_f32_16x16x32_bf16 v[232:235], a[88:91], a[104:107], v[232:235]\n"
"v_mfma_f32_16x16x32_bf16 v[232:235], a[92:95], a[108:111], v[232:235]\n"
"v_mfma_f32_16x16x32_bf16 v[204:207], a[84:87], a[108:111], v[204:207]\n"
"ds_read_b128 a[108:111], v2 offset:19456\n"
"v_mfma_f32_16x16x32_bf16 v[204:207], a[80:83], a[104:107], v[204:207]\n"
"ds_read_b128 a[104:107], v2 offset:18432\n"
"v_mfma_f32_16x16x32_bf16 v[208:211], a[80:83], a[112:115], v[208:211]\n"
"v_mfma_f32_16x16x32_bf16 v[208:211], a[84:87], a[116:119], v[208:211]\n"
"global_load_dwordx4 a[68:71], v0, s[34:35] offset:-3072 sc1\n"
"v_mfma_f32_16x16x32_bf16 v[236:239], a[92:95], a[116:119], v[236:239]\n"
"ds_read_b128 a[116:119], v2 offset:21504\n"
"v_mfma_f32_16x16x32_bf16 v[236:239], a[88:91], a[112:115], v[236:239]\n"
"ds_read_b128 a[112:115], v2 offset:20480\n"
"v_mfma_f32_16x16x32_bf16 v[240:243], a[88:91], a[120:123], v[240:243]\n"
"v_mfma_f32_16x16x32_bf16 v[240:243], a[92:95], a[124:127], v[240:243]\n"
"v_mfma_f32_16x16x32_bf16 v[212:215], a[84:87], a[124:127], v[212:215]\n"
"ds_read_b128 a[124:127], v2 offset:23552\n"
"v_mfma_f32_16x16x32_bf16 v[212:215], a[80:83], a[120:123], v[212:215]\n"
"ds_read_b128 a[120:123], v2 offset:22528\n"
"v_mfma_f32_16x16x32_bf16 v[216:219], a[80:83], a[128:131], v[216:219]\n"
"v_mfma_f32_16x16x32_bf16 v[216:219], a[84:87], a[132:135], v[216:219]\n"
"global_load_dwordx4 a[72:75], v0, s[34:35] offset:-2048 sc1\n"
"v_mfma_f32_16x16x32_bf16 v[244:247], a[92:95], a[132:135], v[244:247]\n"
"ds_read_b128 a[132:135], v2 offset:25600\n"
"v_mfma_f32_16x16x32_bf16 v[244:247], a[88:91], a[128:131], v[244:247]\n"
"ds_read_b128 a[128:131], v2 offset:24576\n"
"v_mfma_f32_16x16x32_bf16 v[248:251], a[88:91], a[136:139], v[248:251]\n"
"v_mfma_f32_16x16x32_bf16 v[248:251], a[92:95], a[140:143], v[248:251]\n"
"v_mfma_f32_16x16x32_bf16 v[220:223], a[84:87], a[140:143], v[220:223]\n"
"ds_read_b128 a[140:143], v2 offset:27648\n"
"v_mfma_f32_16x16x32_bf16 v[220:223], a[80:83], a[136:139], v[220:223]\n"
"ds_read_b128 a[136:139], v2 offset:26624\n"
"v_mfma_f32_16x16x32_bf16 v[224:227], a[80:83], a[144:147], v[224:227]\n"
"v_mfma_f32_16x16x32_bf16 v[224:227], a[84:87], a[148:151], v[224:227]\n"
"global_load_dwordx4 a[76:79], v0, s[34:35] offset:-1024 sc1\n"
"v_mfma_f32_16x16x32_bf16 v[252:255], a[92:95], a[148:151], v[252:255]\n"
"ds_read_b128 a[148:151], v2 offset:29696\n"
"v_mfma_f32_16x16x32_bf16 v[252:255], a[88:91], a[144:147], v[252:255]\n"
"ds_read_b128 a[144:147], v2 offset:28672\n"
"v_mfma_f32_16x16x32_bf16 a[28:31], a[88:91], a[152:155], a[28:31]\n"
"v_mfma_f32_16x16x32_bf16 a[28:31], a[92:95], a[156:159], a[28:31]\n"
"v_mfma_f32_16x16x32_bf16 a[24:27], a[84:87], a[156:159], a[24:27]\n"
"ds_read_b128 a[156:159], v2 offset:31744\n"
"v_mfma_f32_16x16x32_bf16 a[24:27], a[80:83], a[152:155], a[24:27]\n"
"ds_read_b128 a[152:155], v2 offset:30720\n"
"s_waitcnt vmcnt(8) lgkmcnt(12)\n"
"v_mfma_f32_16x16x32_bf16 v[32:35], a[32:35], a[96:99], v[32:35]\n"
"v_mfma_f32_16x16x32_bf16 v[32:35], a[36:39], a[100:103], v[32:35]\n"
"global_load_dwordx4 a[80:83], v0, s[34:35] offset:0 sc1\n"
"v_mfma_f32_16x16x32_bf16 v[60:63], a[44:47], a[100:103], v[60:63]\n"
"s_barrier\n"
"v_mfma_f32_16x16x32_bf16 v[60:63], a[40:43], a[96:99], v[60:63]\n"
"s_add_u32 s48, s48, s70\n"
"s_addc_u32 s49, s49, s71\n"
"v_mfma_f32_16x16x32_bf16 v[64:67], a[40:43], a[104:107], v[64:67]\n"
"v_mfma_f32_16x16x32_bf16 v[64:67], a[44:47], a[108:111], v[64:67]\n"
"v_mfma_f32_16x16x32_bf16 v[36:39], a[36:39], a[108:111], v[36:39]\n"
"s_add_u32 s52, s52, s70\n"
"s_addc_u32 s53, s53, s71\n"
"v_subrev_u32 v12, s67, v12\n"
"v_mfma_f32_16x16x32_bf16 v[36:39], a[32:35], a[104:107], v[36:39]\n"
"s_waitcnt lgkmcnt(8)\n"
"v_xor_b32 v13, s79, v13\n"
"v_mfma_f32_16x16x32_bf16 v[40:43], a[32:35], a[112:115], v[40:43]\n"
"s_add_u32 s54, s54, s70\n"
"s_addc_u32 s55, s55, s71\n"
"v_subrev_u32 v14, s67, v14\n"
"v_mfma_f32_16x16x32_bf16 v[40:43], a[36:39], a[116:119], v[40:43]\n"
"global_load_dwordx4 a[84:87], v0, s[34:35] offset:1024 sc1\n"
"v_xor_b32 v15, s79, v15\n"
"v_mfma_f32_16x16x32_bf16 v[68:71], a[44:47], a[116:119], v[68:71]\n"
"v_or_b32 v12, v12, v14\n"
"v_mfma_f32_16x16x32_bf16 v[68:71], a[40:43], a[112:115], v[68:71]\n"
"v_or_b32 v13, v13, v15\n"
"v_mfma_f32_16x16x32_bf16 v[72:75], a[40:43], a[120:123], v[72:75]\n"
"v_or_b32 v12, v12, v13\n"
"v_mfma_f32_16x16x32_bf16 v[72:75], a[44:47], a[124:127], v[72:75]\n"
"s_add_u32 s61, s61, 2048\n"
"v_cmp_lt_u32 vcc, 15, v12\n"
"v_mfma_f32_16x16x32_bf16 v[44:47], a[36:39], a[124:127], v[44:47]\n"
"s_cbranch_vccnz L_slow2\n"
"L_back2:\n"
"v_mfma_f32_16x16x32_bf16 v[44:47], a[32:35], a[120:123], v[44:47]\n"
"s_waitcnt lgkmcnt(4)\n"
"v_mfma_f32_16x16x32_bf16 v[48:51], a[32:35], a[128:131], v[48:51]\n"
"v_lshlrev_b32 v16, 16, v18\n"
"v_mfma_f32_16x16x32_bf16 v[48:51], a[36:39], a[132:135], v[48:51]\n"
"global_load_dwordx4 a[88:91], v0, s[34:35] offset:2048 sc1\n"
"v_and_b32 v17, s63, v18\n"
"v_mfma_f32_16x16x32_bf16 v[76:79], a[44:47], a[132:135], v[76:79]\n"
"v_lshlrev_b32 v18, 16, v19\n"
"v_mfma_f32_16x16x32_bf16 v[76:79], a[40:43], a[128:131], v[76:79]\n"
"v_and_b32 v19, s63, v19\n"
"v_mfma_f32_16x16x32_bf16 v[80:83], a[40:43], a[136:139], v[80:83]\n"
"v_add_f32 v144, v144, v16\n"
"v_add_f32 v145, v145, v17\n"
"v_mfma_f32_16x16x32_bf16 v[80:83], a[44:47], a[140:143], v[80:83]\n"
"v_add_f32 v146, v146, v18\n"
"v_mfma_f32_16x16x32_bf16 v[52:55], a[36:39], a[140:143], v[52:55]\n"
"v_add_f32 v147, v147, v19\n"
"v_mfma_f32_16x16x32_bf16 v[52:55], a[32:35], a[136:139], v[52:55]\n"
"s_waitcnt lgkmcnt(0)\n"
"v_add_f32 v148, v148, v16\n"
"v_mfma_f32_16x16x32_bf16 v[56:59], a[32:35], a[144:147], v[56:59]\n"
"v_add_f32 v149, v149, v17\n"
"v_mfma_f32_16x16x32_bf16 v[56:59], a[36:39], a[148:151], v[56:59]\n"
"global_load_dwordx4 a[92:95], v0, s[34:35] offset:3072 sc1\n"
"v_add_f32 v150, v150, v18\n"
"v_mfma_f32_16x16x32_bf16 v[84:87], a[44:47], a[148:151], v[84:87]\n"
"v_add_f32 v151, v151, v19\n"
"v_mfma_f32_16x16x32_bf16 v[84:87], a[40:43], a[144:147], v[84:87]\n"
"v_add_f32 v152, v152, v16\n"
"v_mfma_f32_16x16x32_bf16 a[4:7], a[40:43], a[152:155], a[4:7]\n"
"s_add_u32 s34, s34, 8192\n"
"v_add_f32 v153, v153, v17\n"
"v_mfma_f32_16x16x32_bf16 a[4:7], a[44:47], a[156:159], a[4:7]\n"
"v_add_f32 v154, v154, v18\n"
"v_mfma_f32_16x16x32_bf16 a[0:3], a[36:39], a[156:159], a[0:3]\n"
"v_add_f32 v155, v155, v19\n"
"v_add_f32 v156, v156, v16\n"
"v_mfma_f32_16x16x32_bf16 a[0:3], a[32:35], a[152:155], a[0:3]\n"
"s_waitcnt vmcnt(8)\n"
"v_add_f32 v157, v157, v17\n"
"v_mfma_f32_16x16x32_bf16 v[88:91], a[48:51], a[96:99], v[88:91]\n"
"v_add_f32 v158, v158, v18\n"
"v_mfma_f32_16x16x32_bf16 v[88:91], a[52:55], a[100:103], v[88:91]\n"
"global_load_dwordx4 a[32:35], v0, s[34:35] offset:-4096 sc1\n"
"v_add_f32 v159, v159, v19\n"
"v_mfma_f32_16x16x32_bf16 v[116:119], a[60:63], a[100:103], v[116:119]\n"
"ds_read_b128 a[100:103], v3 offset:17408\n"
"v_add_f32 v160, v160, v16\n"
"v_mfma_f32_16x16x32_bf16 v[116:119], a[56:59], a[96:99], v[116:119]\n"
"ds_read_b128 a[96:99], v3 offset:16384\n"
"v_add_f32 v161, v161, v17\n"
"v_mfma_f32_16x16x32_bf16 v[120:123], a[56:59], a[104:107], v[120:123]\n"
"v_add_f32 v162, v162, v18\n"
"v_mfma_f32_16x16x32_bf16 v[120:123], a[60:63], a[108:111], v[120:123]\n"
"v_add_f32 v163, v163, v19\n"
"v_mfma_f32_16x16x32_bf16 v[92:95], a[52:55], a[108:111], v[92:95]\n"
"ds_read_b128 a[108:111], v3 offset:19456\n"
"v_add_f32 v164, v164, v16\n"
"v_mfma_f32_16x16x32_bf16 v[92:95], a[48:51], a[104:107], v[92:95]\n"
"ds_read_b128 a[104:107], v3 offset:18432\n"
"v_add_f32 v165, v165, v17\n"
"v_mfma_f32_16x16x32_bf16 v[96:99], a[48:51], a[112:115], v[96:99]\n"
"v_add_f32 v166, v166, v18\n"
"v_add_f32 v167, v167, v19\n"
"v_mfma_f32_16x16x32_bf16 v[96:99], a[52:55], a[116:119], v[96:99]\n"
"global_load_dwordx4 a[36:39], v0, s[34:35] offset:-3072 sc1\n"
"v_add_f32 v168, v168, v16\n"
"v_mfma_f32_16x16x32_bf16 v[124:127], a[60:63], a[116:119], v[124:127]\n"
"ds_read_b128 a[116:119], v3 offset:21504\n"
"v_add_f32 v169, v169, v17\n"
"v_mfma_f32_16x16x32_bf16 v[124:127], a[56:59], a[112:115], v[124:127]\n"
"ds_read_b128 a[112:115], v3 offset:20480\n"
"v_add_f32 v170, v170, v18\n"
"v_mfma_f32_16x16x32_bf16 v[128:131], a[56:59], a[120:123], v[128:131]\n"
"v_add_f32 v171, v171, v19\n"
"v_mfma_f32_16x16x32_bf16 v[128:131], a[60:63], a[124:127], v[128:131]\n"
"v_accvgpr_read_b32 v12, a16\n"
"v_mfma_f32_16x16x32_bf16 v[100:103], a[52:55], a[124:127], v[100:103]\n"
"ds_read_b128 a[124:127], v3 offset:23552\n"
"v_accvgpr_read_b32 v13, a17\n"
"v_mfma_f32_16x16x32_bf16 v[100:103], a[48:51], a[120:123], v[100:103]\n"
"ds_read_b128 a[120:123], v3 offset:22528\n"
"v_accvgpr_read_b32 v14, a18\n"
"v_mfma_f32_16x16x32_bf16 v[104:107], a[48:51], a[128:131], v[104:107]\n"
"v_accvgpr_read_b32 v15, a19\n"
"v_add_f32 v12, v12, v16\n"
"v_mfma_f32_16x16x32_bf16 v[104:107], a[52:55], a[132:135], v[104:107]\n"
"global_load_dwordx4 a[40:43], v0, s[34:35] offset:-2048 sc1\n"
"v_add_f32 v13, v13, v17\n"
"v_mfma_f32_16x16x32_bf16 v[132:135], a[60:63], a[132:135], v[132:135]\n"
"ds_read_b128 a[132:135], v3 offset:25600\n"
"v_add_f32 v14, v14, v18\n"
"v_mfma_f32_16x16x32_bf16 v[132:135], a[56:59], a[128:131], v[132:135]\n"
"ds_read_b128 a[128:131], v3 offset:24576\n"
"v_add_f32 v15, v15, v19\n"
"v_mfma_f32_16x16x32_bf16 v[136:139], a[56:59], a[136:139], v[136:139]\n"
"v_accvgpr_write_b32 a16, v12\n"
"v_mfma_f32_16x16x32_bf16 v[136:139], a[60:63], a[140:143], v[136:139]\n"
"v_accvgpr_write_b32 a17, v13\n"
"v_mfma_f32_16x16x32_bf16 v[108:111], a[52:55], a[140:143], v[108:111]\n"
"ds_read_b128 a[140:143], v3 offset:27648\n"
"v_accvgpr_write_b32 a18, v14\n"
"v_mfma_f32_16x16x32_bf16 v[108:111], a[48:51], a[136:139], v[108:111]\n"
"ds_read_b128 a[136:139], v3 offset:26624\n"
"v_accvgpr_write_b32 a19, v15\n"
"v_mfma_f32_16x16x32_bf16 v[112:115], a[48:51], a[144:147], v[112:115]\n"
"v_lshlrev_b32 v20, 16, v22\n"
"v_and_b32 v21, s63, v22\n"
"v_mfma_f32_16x16x32_bf16 v[112:115], a[52:55], a[148:151], v[112:115]\n"
"global_load_dwordx4 a[44:47], v0, s[34:35] offset:-1024 sc1\n"
"v_lshlrev_b32 v22, 16, v23\n"
"v_mfma_f32_16x16x32_bf16 v[140:143], a[60:63], a[148:151], v[140:143]\n"
"ds_read_b128 a[148:151], v3 offset:29696\n"
"v_and_b32 v23, s63, v23\n"
"v_mfma_f32_16x16x32_bf16 v[140:143], a[56:59], a[144:147], v[140:143]\n"
"ds_read_b128 a[144:147], v3 offset:28672\n"
"v_add_f32 v172, v172, v20\n"
"v_mfma_f32_16x16x32_bf16 a[12:15], a[56:59], a[152:155], a[12:15]\n"
"v_add_f32 v173, v173, v21\n"
"v_mfma_f32_16x16x32_bf16 a[12:15], a[60:63], a[156:159], a[12:15]\n"
"v_add_f32 v174, v174, v22\n"
"v_mfma_f32_16x16x32_bf16 a[8:11], a[52:55], a[156:159], a[8:11]\n"
"ds_read_b128 a[156:159], v3 offset:31744\n"
"v_add_f32 v175, v175, v23\n"
"v_mfma_f32_16x16x32_bf16 a[8:11], a[48:51], a[152:155], a[8:11]\n"
"s_waitcnt vmcnt(8) lgkmcnt(11)\n"
"ds_read_b128 a[152:155], v3 offset:30720\n"
"v_add_f32 v176, v176, v20\n"
"v_mfma_f32_16x16x32_bf16 v[32:35], a[64:67], a[96:99], v[32:35]\n"
"v_add_f32 v177, v177, v21\n"
"v_mfma_f32_16x16x32_bf16 v[32:35], a[68:71], a[100:103], v[32:35]\n"
"global_load_dwordx4 a[48:51], v0, s[34:35] offset:0 sc1\n"
"v_add_f32 v178, v178, v22\n"
"v_mfma_f32_16x16x32_bf16 v[60:63], a[76:79], a[100:103], v[60:63]\n"
"v_add_f32 v179, v179, v23\n"
"v_mfma_f32_16x16x32_bf16 v[60:63], a[72:75], a[96:99], v[60:63]\n"
"v_add_f32 v180, v180, v20\n"
"v_add_f32 v181, v181, v21\n"
"v_mfma_f32_16x16x32_bf16 v[64:67], a[72:75], a[104:107], v[64:67]\n"
"v_add_f32 v182, v182, v22\n"
"v_mfma_f32_16x16x32_bf16 v[64:67], a[76:79], a[108:111], v[64:67]\n"
"v_add_f32 v183, v183, v23\n"
"v_mfma_f32_16x16x32_bf16 v[36:39], a[68:71], a[108:111], v[36:39]\n"
"L_st7945:\n"
"s_cbranch_execz L_st9128\n"
"v_add_f32 v184, v184, v20\n"
"v_mfma_f32_16x16x32_bf16 v[36:39], a[64:67], a[104:107], v[36:39]\n"
"s_waitcnt lgkmcnt(8)\n"
"v_add_f32 v185, v185, v21\n"
"v_mfma_f32_16x16x32_bf16 v[40:43], a[64:67], a[112:115], v[40:43]\n"
"v_add_f32 v186, v186, v22\n"
"v_mfma_f32_16x16x32_bf16 v[40:43], a[68:71], a[116:119], v[40:43]\n"
"global_load_dwordx4 a[52:55], v0, s[34:35] offset:1024 sc1\n"
"v_add_f32 v187, v187, v23\n"
"v_mfma_f32_16x16x32_bf16 v[68:71], a[76:79], a[116:119], v[68:71]\n"
"v_add_f32 v188, v188, v20\n"
"v_mfma_f32_16x16x32_bf16 v[68:71], a[72:75], a[112:115], v[68:71]\n"
"v_add_f32 v189, v189, v21\n"
"v_mfma_f32_16x16x32_bf16 v[72:75], a[72:75], a[120:123], v[72:75]\n"
"v_add_f32 v190, v190, v22\n"
"v_add_f32 v191, v191, v23\n"
"v_mfma_f32_16x16x32_bf16 v[72:75], a[76:79], a[124:127], v[72:75]\n"
"v_add_f32 v192, v192, v20\n"
"v_mfma_f32_16x16x32_bf16 v[44:47], a[68:71], a[124:127], v[44:47]\n"
"v_add_f32 v193, v193, v21\n"
"v_mfma_f32_16x16x32_bf16 v[44:47], a[64:67], a[120:123], v[44:47]\n"
"s_waitcnt lgkmcnt(4)\n"
"v_add_f32 v194, v194, v22\n"
"v_mfma_f32_16x16x32_bf16 v[48:51], a[64:67], a[128:131], v[48:51]\n"
"v_add_f32 v195, v195, v23\n"
"v_mfma_f32_16x16x32_bf16 v[48:51], a[68:71], a[132:135], v[48:51]\n"
"global_load_dwordx4 a[56:59], v0, s[34:35] offset:2048 sc1\n"
"v_add_f32 v196, v196, v20\n"
"v_mfma_f32_16x16x32_bf16 v[76:79], a[76:79], a[132:135], v[76:79]\n"
"v_add_f32 v197, v197, v21\n"
"v_mfma_f32_16x16x32_bf16 v[76:79], a[72:75], a[128:131], v[76:79]\n"
"v_add_f32 v198, v198, v22\n"
"v_mfma_f32_16x16x32_bf16 v[80:83], a[72:75], a[136:139], v[80:83]\n"
"v_add_f32 v199, v199, v23\n"
"v_mfma_f32_16x16x32_bf16 v[80:83], a[76:79], a[140:143], v[80:83]\n"
"v_accvgpr_read_b32 v12, a20\n"
"v_mfma_f32_16x16x32_bf16 v[52:55], a[68:71], a[140:143], v[52:55]\n"
"v_accvgpr_read_b32 v13, a21\n"
"v_accvgpr_read_b32 v14, a22\n"
"v_mfma_f32_16x16x32_bf16 v[52:55], a[64:67], a[136:139], v[52:55]\n"
"s_waitcnt lgkmcnt(0)\n"
"v_accvgpr_read_b32 v15, a23\n"
"v_mfma_f32_16x16x32_bf16 v[56:59], a[64:67], a[144:147], v[56:59]\n"
"v_add_f32 v12, v12, v20\n"
"v_mfma_f32_16x16x32_bf16 v[56:59], a[68:71], a[148:151], v[56:59]\n"
"global_load_dwordx4 a[60:63], v0, s[34:35] offset:3072 sc1\n"
"v_add_f32 v13, v13, v21\n"
"v_mfma_f32_16x16x32_bf16 v[84:87], a[76:79], a[148:151], v[84:87]\n"
"v_add_f32 v14, v14, v22\n"
"v_mfma_f32_16x16x32_bf16 v[84:87], a[72:75], a[144:147], v[84:87]\n"
"v_add_f32 v15, v15, v23\n"
"v_mfma_f32_16x16x32_bf16 a[4:7], a[72:75], a[152:155], a[4:7]\n"
"s_add_u32 s34, s34, 8192\n"
"v_accvgpr_write_b32 a20, v12\n"
"v_mfma_f32_16x16x32_bf16 a[4:7], a[76:79], a[156:159], a[4:7]\n"
"v_accvgpr_write_b32 a21, v13\n"
"v_mfma_f32_16x16x32_bf16 a[0:3], a[68:71], a[156:159], a[0:3]\n"
"v_accvgpr_write_b32 a22, v14\n"
"v_mfma_f32_16x16x32_bf16 a[0:3], a[64:67], a[152:155], a[0:3]\n"
"s_waitcnt vmcnt(8)\n"
"v_accvgpr_write_b32 a23, v15\n"
"v_lshlrev_b32 v24, 16, v26\n"
"v_mfma_f32_16x16x32_bf16 v[88:91], a[80:83], a[96:99], v[88:91]\n"
"v_and_b32 v25, s63, v26\n"
"v_mfma_f32_16x16x32_bf16 v[88:91], a[84:87], a[100:103], v[88:91]\n"
"global_load_dwordx4 a[64:67], v0, s[34:35] offset:-4096 sc1\n"
"v_lshlrev_b32 v26, 16, v27\n"
"v_mfma_f32_16x16x32_bf16 v[116:119], a[92:95], a[100:103], v[116:119]\n"
"ds_read_b128 a[100:103], v4 offset:17408\n"
"v_and_b32 v27, s63, v27\n"
"v_mfma_f32_16x16x32_bf16 v[116:119], a[88:91], a[96:99], v[116:119]\n"
"ds_read_b128 a[96:99], v4 offset:16384\n"
"v_add_f32 v200, v200, v24\n"
"v_mfma_f32_16x16x32_bf16 v[120:123], a[88:91], a[104:107], v[120:123]\n"
"v_add_f32 v201, v201, v25\n"
"v_mfma_f32_16x16x32_bf16 v[120:123], a[92:95], a[108:111], v[120:123]\n"
"v_add_f32 v202, v202, v26\n"
"v_mfma_f32_16x16x32_bf16 v[92:95], a[84:87], a[108:111], v[92:95]\n"
"ds_read_b128 a[108:111], v4 offset:19456\n"
"v_add_f32 v203, v203, v27\n"
"v_mfma_f32_16x16x32_bf16 v[92:95], a[80:83], a[104:107], v[92:95]\n"
"ds_read_b128 a[104:107], v4 offset:18432\n"
"v_add_f32 v204, v204, v24\n"
"v_mfma_f32_16x16x32_bf16 v[96:99], a[80:83], a[112:115], v[96:99]\n"
"v_add_f32 v205, v205, v25\n"
"v_add_f32 v206, v206, v26\n"
"v_mfma_f32_16x16x32_bf16 v[96:99], a[84:87], a[116:119], v[96:99]\n"
"global_load_dwordx4 a[68:71], v0, s[34:35] offset:-3072 sc1\n"
"v_add_f32 v207, v207, v27\n"
"v_mfma_f32_16x16x32_bf16 v[124:127], a[92:95], a[116:119], v[124:127]\n"
"ds_read_b128 a[116:119], v4 offset:21504\n"
"v_add_f32 v208, v208, v24\n"
"v_mfma_f32_16x16x32_bf16 v[124:127], a[88:91], a[112:115], v[124:127]\n"
"ds_read_b128 a[112:115], v4 offset:20480\n"
"v_add_f32 v209, v209, v25\n"
"v_mfma_f32_16x16x32_bf16 v[128:131], a[88:91], a[120:123], v[128:131]\n"
"v_add_f32 v210, v210, v26\n"
"v_mfma_f32_16x16x32_bf16 v[128:131], a[92:95], a[124:127], v[128:131]\n"
"v_add_f32 v211, v211, v27\n"
"v_mfma_f32_16x16x32_bf16 v[100:103], a[84:87], a[124:127], v[100:103]\n"
"ds_read_b128 a[124:127], v4 offset:23552\n"
"v_add_f32 v212, v212, v24\n"
"v_mfma_f32_16x16x32_bf16 v[100:103], a[80:83], a[120:123], v[100:103]\n"
"ds_read_b128 a[120:123], v4 offset:22528\n"
"v_add_f32 v213, v213, v25\n"
"v_mfma_f32_16x16x32_bf16 v[104:107], a[80:83], a[128:131], v[104:107]\n"
"v_add_f32 v214, v214, v26\n"
"v_add_f32 v215, v215, v27\n"
"v_mfma_f32_16x16x32_bf16 v[104:107], a[84:87], a[132:135], v[104:107]\n"
"global_load_dwordx4 a[72:75], v0, s[34:35] offset:-2048 sc1\n"
"v_add_f32 v216, v216, v24\n"
"v_mfma_f32_16x16x32_bf16 v[132:135], a[92:95], a[132:135], v[132:135]\n"
"ds_read_b128 a[132:135], v4 offset:25600\n"
"v_add_f32 v217, v217, v25\n"
"v_mfma_f32_16x16x32_bf16 v[132:135], a[88:91], a[128:131], v[132:135]\n"
"ds_read_b128 a[128:131], v4 offset:24576\n"
"v_add_f32 v218, v218, v26\n"
"v_mfma_f32_16x16x32_bf16 v[136:139], a[88:91], a[136:139], v[136:139]\n"
"v_add_f32 v219, v219, v27\n"
"v_mfma_f32_16x16x32_bf16 v[136:139], a[92:95], a[140:143], v[136:139]\n"
"v_add_f32 v220, v220, v24\n"
"v_mfma_f32_16x16x32_bf16 v[108:111], a[84:87], a[140:143], v[108:111]\n"
"ds_read_b128 a[140:143], v4 offset:27648\n"
"v_add_f32 v221, v221, v25\n"
"v_mfma_f32_16x16x32_bf16 v[108:111], a[80:83], a[136:139], v[108:111]\n"
"ds_read_b128 a[136:139], v4 offset:26624\n"
"v_add_f32 v222, v222, v26\n"
"v_mfma_f32_16x16x32_bf16 v[112:115], a[80:83], a[144:147], v[112:115]\n"
"v_add_f32 v223, v223, v27\n"
"v_mfma_f32_16x16x32_bf16 v[112:115], a[84:87], a[148:151], v[112:115]\n"
"global_load_dwordx4 a[76:79], v0, s[34:35] offset:-1024 sc1\n"
"v_add_f32 v224, v224, v24\n"
"v_mfma_f32_16x16x32_bf16 v[140:143], a[92:95], a[148:151], v[140:143]\n"
"ds_read_b128 a[148:151], v4 offset:29696\n"
"v_add_f32 v225, v225, v25\n"
"v_mfma_f32_16x16x32_bf16 v[140:143], a[88:91], a[144:147], v[140:143]\n"
"ds_read_b128 a[144:147], v4 offset:28672\n"
"v_add_f32 v226, v226, v26\n"
"v_mfma_f32_16x16x32_bf16 a[12:15], a[88:91], a[152:155], a[12:15]\n"
"v_add_f32 v227, v227, v27\n"
"v_accvgpr_read_b32 v12, a24\n"
"v_mfma_f32_16x16x32_bf16 a[12:15], a[92:95], a[156:159], a[12:15]\n"
"v_accvgpr_read_b32 v13, a25\n"
"v_mfma_f32_16x16x32_bf16 a[8:11], a[84:87], a[156:159], a[8:11]\n"
"ds_read_b128 a[156:159], v4 offset:31744\n"
"v_accvgpr_read_b32 v14, a26\n"
"v_mfma_f32_16x16x32_bf16 a[8:11], a[80:83], a[152:155], a[8:11]\n"
"s_waitcnt vmcnt(8) lgkmcnt(11)\n"
"ds_read_b128 a[152:155], v4 offset:30720\n"
"v_accvgpr_read_b32 v15, a27\n"
"v_mfma_f32_16x16x32_bf16 v[32:35], a[32:35], a[96:99], v[32:35]\n"
"v_add_f32 v12, v12, v24\n"
"v_mfma_f32_16x16x32_bf16 v[32:35], a[36:39], a[100:103], v[32:35]\n"
"global_load_dwordx4 a[80:83], v0, s[34:35] offset:0 sc1\n"
"v_add_f32 v13, v13, v25\n"
"v_mfma_f32_16x16x32_bf16 v[60:63], a[44:47], a[100:103], v[60:63]\n"
"v_add_f32 v14, v14, v26\n"
"v_mfma_f32_16x16x32_bf16 v[60:63], a[40:43], a[96:99], v[60:63]\n"
"v_add_f32 v15, v15, v27\n"
"v_mfma_f32_16x16x32_bf16 v[64:67], a[40:43], a[104:107], v[64:67]\n"
"v_accvgpr_write_b32 a24, v12\n"
"v_accvgpr_write_b32 a25, v13\n"
"v_mfma_f32_16x16x32_bf16 v[64:67], a[44:47], a[108:111], v[64:67]\n"
"v_accvgpr_write_b32 a26, v14\n"
"v_mfma_f32_16x16x32_bf16 v[36:39], a[36:39], a[108:111], v[36:39]\n"
"v_accvgpr_write_b32 a27, v15\n"
"v_mfma_f32_16x16x32_bf16 v[36:39], a[32:35], a[104:107], v[36:39]\n"
"s_waitcnt lgkmcnt(8)\n"
"v_lshlrev_b32 v28, 16, v30\n"
"v_mfma_f32_16x16x32_bf16 v[40:43], a[32:35], a[112:115], v[40:43]\n"
"v_and_b32 v29, s63, v30\n"
"v_mfma_f32_16x16x32_bf16 v[40:43], a[36:39], a[116:119], v[40:43]\n"
"global_load_dwordx4 a[84:87], v0, s[34:35] offset:1024 sc1\n"
"v_lshlrev_b32 v30, 16, v31\n"
"v_mfma_f32_16x16x32_bf16 v[68:71], a[44:47], a[116:119], v[68:71]\n"
"v_and_b32 v31, s63, v31\n"
"v_mfma_f32_16x16x32_bf16 v[68:71], a[40:43], a[112:115], v[68:71]\n"
"v_add_f32 v228, v228, v28\n"
"v_mfma_f32_16x16x32_bf16 v[72:75], a[40:43], a[120:123], v[72:75]\n"
"v_add_f32 v229, v229, v29\n"
"v_mfma_f32_16x16x32_bf16 v[72:75], a[44:47], a[124:127], v[72:75]\n"
"v_add_f32 v230, v230, v30\n"
"v_mfma_f32_16x16x32_bf16 v[44:47], a[36:39], a[124:127], v[44:47]\n"
"v_add_f32 v231, v231, v31\n"
"v_add_f32 v232, v232, v28\n"
"v_mfma_f32_16x16x32_bf16 v[44:47], a[32:35], a[120:123], v[44:47]\n"
"s_waitcnt lgkmcnt(4)\n"
"v_add_f32 v233, v233, v29\n"
"v_mfma_f32_16x16x32_bf16 v[48:51], a[32:35], a[128:131], v[48:51]\n"
"v_add_f32 v234, v234, v30\n"
"v_mfma_f32_16x16x32_bf16 v[48:51], a[36:39], a[132:135], v[48:51]\n"
"global_load_dwordx4 a[88:91], v0, s[34:35] offset:2048 sc1\n"
"v_add_f32 v235, v235, v31\n"
"v_mfma_f32_16x16x32_bf16 v[76:79], a[44:47], a[132:135], v[76:79]\n"
"v_add_f32 v236, v236, v28\n"
"v_mfma_f32_16x16x32_bf16 v[76:79], a[40:43], a[128:131], v[76:79]\n"
"v_add_f32 v237, v237, v29\n"
"v_mfma_f32_16x16x32_bf16 v[80:83], a[40:43], a[136:139], v[80:83]\n"
"v_add_f32 v238, v238, v30\n"
"v_mfma_f32_16x16x32_bf16 v[80:83], a[44:47], a[140:143], v[80:83]\n"
"v_add_f32 v239, v239, v31\n"
"v_mfma_f32_16x16x32_bf16 v[52:55], a[36:39], a[140:143], v[52:55]\n"
"v_add_f32 v240, v240, v28\n"
"v_mfma_f32_16x16x32_bf16 v[52:55], a[32:35], a[136:139], v[52:55]\n"
"s_waitcnt lgkmcnt(0)\n"
"v_add_f32 v241, v241, v29\n"
"v_add_f32 v242, v242, v30\n"
"v_mfma_f32_16x16x32_bf16 v[56:59], a[32:35], a[144:147], v[56:59]\n"
"v_add_f32 v243, v243, v31\n"
"v_mfma_f32_16x16x32_bf16 v[56:59], a[36:39], a[148:151], v[56:59]\n"
"global_load_dwordx4 a[92:95], v0, s[34:35] offset:3072 sc1\n"
"v_add_f32 v244, v244, v28\n"
"v_mfma_f32_16x16x32_bf16 v[84:87], a[44:47], a[148:151], v[84:87]\n"
"v_add_f32 v245, v245, v29\n"
"v_mfma_f32_16x16x32_bf16 v[84:87], a[40:43], a[144:147], v[84:87]\n"
"v_add_f32 v246, v246, v30\n"
"v_mfma_f32_16x16x32_bf16 a[4:7], a[40:43], a[152:155], a[4:7]\n"
"s_add_u32 s34, s34, 8192\n"
"v_add_f32 v247, v247, v31\n"
"v_mfma_f32_16x16x32_bf16 a[4:7], a[44:47], a[156:159], a[4:7]\n"
"v_add_f32 v248, v248, v28\n"
"v_mfma_f32_16x16x32_bf16 a[0:3], a[36:39], a[156:159], a[0:3]\n"
"v_add_f32 v249, v249, v29\n"
"v_mfma_f32_16x16x32_bf16 a[0:3], a[32:35], a[152:155], a[0:3]\n"
"s_waitcnt vmcnt(8)\n"
"v_add_f32 v250, v250, v30\n"
"v_mfma_f32_16x16x32_bf16 v[88:91], a[48:51], a[96:99], v[88:91]\n"
"v_add_f32 v251, v251, v31\n"
"v_add_f32 v252, v252, v28\n"
"v_mfma_f32_16x16x32_bf16 v[88:91], a[52:55], a[100:103], v[88:91]\n"
"global_load_dwordx4 a[32:35], v0, s[34:35] offset:-4096 sc1\n"
"v_add_f32 v253, v253, v29\n"
"v_mfma_f32_16x16x32_bf16 v[116:119], a[60:63], a[100:103], v[116:119]\n"
"ds_read_b128 a[100:103], v5 offset:17408\n"
"v_add_f32 v254, v254, v30\n"
"v_mfma_f32_16x16x32_bf16 v[116:119], a[56:59], a[96:99], v[116:119]\n"
"ds_read_b128 a[96:99], v5 offset:16384\n"
"v_add_f32 v255, v255, v31\n"
"v_mfma_f32_16x16x32_bf16 v[120:123], a[56:59], a[104:107], v[120:123]\n"
"v_accvgpr_read_b32 v12, a28\n"
"v_mfma_f32_16x16x32_bf16 v[120:123], a[60:63], a[108:111], v[120:123]\n"
"v_accvgpr_read_b32 v13, a29\n"
"v_mfma_f32_16x16x32_bf16 v[92:95], a[52:55], a[108:111], v[92:95]\n"
"ds_read_b128 a[108:111], v5 offset:19456\n"
"v_accvgpr_read_b32 v14, a30\n"
"v_mfma_f32_16x16x32_bf16 v[92:95], a[48:51], a[104:107], v[92:95]\n"
"ds_read_b128 a[104:107], v5 offset:18432\n"
"v_accvgpr_read_b32 v15, a31\n"
"v_mfma_f32_16x16x32_bf16 v[96:99], a[48:51], a[112:115], v[96:99]\n"
"v_add_f32 v12, v12, v28\n"
"v_add_f32 v13, v13, v29\n"
"v_mfma_f32_16x16x32_bf16 v[96:99], a[52:55], a[116:119], v[96:99]\n"
"global_load_dwordx4 a[36:39], v0, s[34:35] offset:-3072 sc1\n"
"v_add_f32 v14, v14, v30\n"
"v_mfma_f32_16x16x32_bf16 v[124:127], a[60:63], a[116:119], v[124:127]\n"
"ds_read_b128 a[116:119], v5 offset:21504\n"
"v_add_f32 v15, v15, v31\n"
"v_mfma_f32_16x16x32_bf16 v[124:127], a[56:59], a[112:115], v[124:127]\n"
"ds_read_b128 a[112:115], v5 offset:20480\n"
"v_accvgpr_write_b32 a28, v12\n"
"v_mfma_f32_16x16x32_bf16 v[128:131], a[56:59], a[120:123], v[128:131]\n"
"v_accvgpr_write_b32 a29, v13\n"
"v_mfma_f32_16x16x32_bf16 v[128:131], a[60:63], a[124:127], v[128:131]\n"
"v_accvgpr_write_b32 a30, v14\n"
"v_mfma_f32_16x16x32_bf16 v[100:103], a[52:55], a[124:127], v[100:103]\n"
"ds_read_b128 a[124:127], v5 offset:23552\n"
"v_accvgpr_write_b32 a31, v15\n"
"v_mfma_f32_16x16x32_bf16 v[100:103], a[48:51], a[120:123], v[100:103]\n"
"ds_read_b128 a[120:123], v5 offset:22528\n"
"v_mfma_f32_16x16x32_bf16 v[104:107], a[48:51], a[128:131], v[104:107]\n"
"v_mfma_f32_16x16x32_bf16 v[104:107], a[52:55], a[132:135], v[104:107]\n"
"global_load_dwordx4 a[40:43], v0, s[34:35] offset:-2048 sc1\n"
"v_mfma_f32_16x16x32_bf16 v[132:135], a[60:63], a[132:135], v[132:135]\n"
"ds_read_b128 a[132:135], v5 offset:25600\n"
"v_mfma_f32_16x16x32_bf16 v[132:135], a[56:59], a[128:131], v[132:135]\n"
"ds_read_b128 a[128:131], v5 offset:24576\n"
"v_mfma_f32_16x16x32_bf16 v[136:139], a[56:59], a[136:139], v[136:139]\n"
"ds_read_b128 v[16:19], v7 offset:32768\n"
"v_mfma_f32_16x16x32_bf16 v[136:139], a[60:63], a[140:143], v[136:139]\n"
"v_mfma_f32_16x16x32_bf16 v[108:111], a[52:55], a[140:143], v[108:111]\n"
"ds_read_b128 a[140:143], v5 offset:27648\n"
"v_mfma_f32_16x16x32_bf16 v[108:111], a[48:51], a[136:139], v[108:111]\n"
"ds_read_b128 a[136:139], v5 offset:26624\n"
"v_mfma_f32_16x16x32_bf16 v[112:115], a[48:51], a[144:147], v[112:115]\n"
"v_mfma_f32_16x16x32_bf16 v[112:115], a[52:55], a[148:151], v[112:115]\n"
"global_load_dwordx4 a[44:47], v0, s[34:35] offset:-1024 sc1\n"
"v_mfma_f32_16x16x32_bf16 v[140:143], a[60:63], a[148:151], v[140:143]\n"
"ds_read_b128 a[148:151], v5 offset:29696\n"
"v_mfma_f32_16x16x32_bf16 v[140:143], a[56:59], a[144:147], v[140:143]\n"
"ds_read_b128 a[144:147], v5 offset:28672\n"
"v_mfma_f32_16x16x32_bf16 a[12:15], a[56:59], a[152:155], a[12:15]\n"
"v_mfma_f32_16x16x32_bf16 a[12:15], a[60:63], a[156:159], a[12:15]\n"
"v_mfma_f32_16x16x32_bf16 a[8:11], a[52:55], a[156:159], a[8:11]\n"
"ds_read_b128 a[156:159], v5 offset:31744\n"
"v_mfma_f32_16x16x32_bf16 a[8:11], a[48:51], a[152:155], a[8:11]\n"
"ds_read_b128 a[152:155], v5 offset:30720\n"
"s_branch L_P6\n"
"L_P3F:\n"
"L_st8243:\n"
"s_cbranch_execz L_retB\n"
"s_waitcnt vmcnt(10) lgkmcnt(13)\n"
"v_mfma_f32_16x16x32_bf16 v[32:35], a[32:35], a[96:99], v[32:35]\n"
"s_waitcnt lgkmcnt(6)\n"
"global_store_dwordx4 v0, v[24:27], s[52:53] offset:2048 sc1\n"
"s_cmp_lt_u32 s27, 12582912\n"
"s_cselect_b32 s38, 8192000, 0\n"
"s_cselect_b32 s42, 12582144, 0\n"
"v_mfma_f32_16x16x32_bf16 v[32:35], a[36:39], a[100:103], v[32:35]\n"
"global_load_dwordx4 a[80:83], v0, s[34:35] offset:0 sc1\n"
"v_mfma_f32_16x16x32_bf16 v[60:63], a[44:47], a[100:103], v[60:63]\n"
"s_barrier\n"
"v_mfma_f32_16x16x32_bf16 v[60:63], a[40:43], a[96:99], v[60:63]\n"
"s_add_u32 m0, s64, 12288\n"
"v_mfma_f32_16x16x32_bf16 v[64:67], a[40:43], a[104:107], v[64:67]\n"
"v_mfma_f32_16x16x32_bf16 v[64:67], a[44:47], a[108:111], v[64:67]\n"
"s_add_u32 s67, s27, 0\n"
"v_mfma_f32_16x16x32_bf16 v[36:39], a[36:39], a[108:111], v[36:39]\n"
"v_mfma_f32_16x16x32_bf16 v[36:39], a[32:35], a[104:107], v[36:39]\n"
"v_mfma_f32_16x16x32_bf16 v[40:43], a[32:35], a[112:115], v[40:43]\n"
"buffer_load_dwordx4 v6, s[40:43], s67 offen offset:0 lds\n"
"v_mfma_f32_16x16x32_bf16 v[40:43], a[36:39], a[116:119], v[40:43]\n"
"global_load_dwordx4 a[84:87], v0, s[34:35] offset:1024 sc1\n"
"v_mfma_f32_16x16x32_bf16 v[68:71], a[44:47], a[116:119], v[68:71]\n"
"v_mfma_f32_16x16x32_bf16 v[68:71], a[40:43], a[112:115], v[68:71]\n"
"s_add_u32 s67, s27, 15360\n"
"v_mfma_f32_16x16x32_bf16 v[72:75], a[40:43], a[120:123], v[72:75]\n"
"v_mfma_f32_16x16x32_bf16 v[72:75], a[44:47], a[124:127], v[72:75]\n"
"v_mfma_f32_16x16x32_bf16 v[44:47], a[36:39], a[124:127], v[44:47]\n"
"buffer_load_dwordx4 v6, s[40:43], s67 offen offset:1024 lds\n"
"v_mfma_f32_16x16x32_bf16 v[44:47], a[32:35], a[120:123], v[44:47]\n"
"s_waitcnt lgkmcnt(4)\n"
"v_mfma_f32_16x16x32_bf16 v[48:51], a[32:35], a[128:131], v[48:51]\n"
"v_mfma_f32_16x16x32_bf16 v[48:51], a[36:39], a[132:135], v[48:51]\n"
"global_load_dwordx4 a[88:91], v0, s[34:35] offset:2048 sc1\n"
"v_lshrrev_b32 v16, 4, v0\n"
"v_mfma_f32_16x16x32_bf16 v[76:79], a[44:47], a[132:135], v[76:79]\n"
"s_add_u32 s67, s27, 30720\n"
"v_and_b32 v16, 0x30, v16\n"
"v_mfma_f32_16x16x32_bf16 v[76:79], a[40:43], a[128:131], v[76:79]\n"
"ds_read_b128 v[28:31], v2 offset:23552\n"
"buffer_load_dwordx2 v[18:19], v16, s[44:47], s61 offen offset:0\n"
"v_mfma_f32_16x16x32_bf16 v[80:83], a[40:43], a[136:139], v[80:83]\n"
"buffer_load_dwordx2 v[8:9], off, s[56:59], %14 sc1\n"
"v_mfma_f32_16x16x32_bf16 v[80:83], a[44:47], a[140:143], v[80:83]\n"
"buffer_load_dwordx4 v6, s[40:43], s67 offen offset:2048 lds\n"
"v_mfma_f32_16x16x32_bf16 v[52:55], a[36:39], a[140:143], v[52:55]\n"
"v_mfma_f32_16x16x32_bf16 v[52:55], a[32:35], a[136:139], v[52:55]\n"
"s_waitcnt lgkmcnt(1)\n"
"v_mfma_f32_16x16x32_bf16 v[56:59], a[32:35], a[144:147], v[56:59]\n"
"s_add_u32 s67, s27, 46080\n"
"v_mfma_f32_16x16x32_bf16 v[56:59], a[36:39], a[148:151], v[56:59]\n"
"global_load_dwordx4 a[92:95], v0, s[34:35] offset:3072 sc1\n"
"v_lshrrev_b32 v20, 4, v0\n"
"v_mfma_f32_16x16x32_bf16 v[84:87], a[44:47], a[148:151], v[84:87]\n"
"v_and_b32 v20, 0x30, v20\n"
"v_mfma_f32_16x16x32_bf16 v[84:87], a[40:43], a[144:147], v[84:87]\n"
"buffer_load_dwordx4 v6, s[40:43], s67 offen offset:3072 lds\n"
"buffer_load_dwordx2 v[22:23], v20, s[44:47], s61 offen offset:8\n"
"v_mfma_f32_16x16x32_bf16 a[4:7], a[40:43], a[152:155], a[4:7]\n"
"s_add_u32 s34, s34, 8192\n"
"v_mfma_f32_16x16x32_bf16 a[4:7], a[44:47], a[156:159], a[4:7]\n"
"v_mfma_f32_16x16x32_bf16 a[0:3], a[36:39], a[156:159], a[0:3]\n"
"s_waitcnt lgkmcnt(0)\n"
"global_store_dwordx4 v0, v[28:31], s[52:53] offset:3072 sc1\n"
"v_mfma_f32_16x16x32_bf16 a[0:3], a[32:35], a[152:155], a[0:3]\n"
"s_waitcnt vmcnt(18)\n"
"v_mfma_f32_16x16x32_bf16 v[88:91], a[48:51], a[96:99], v[88:91]\n"
"v_mfma_f32_16x16x32_bf16 v[88:91], a[52:55], a[100:103], v[88:91]\n"
"global_load_dwordx4 a[32:35], v0, s[34:35] offset:-4096 sc1\n"
"v_mfma_f32_16x16x32_bf16 v[116:119], a[60:63], a[100:103], v[116:119]\n"
"v_mfma_f32_16x16x32_bf16 v[116:119], a[56:59], a[96:99], v[116:119]\n"
"v_mfma_f32_16x16x32_bf16 v[120:123], a[56:59], a[104:107], v[120:123]\n"
"v_mfma_f32_16x16x32_bf16 v[120:123], a[60:63], a[108:111], v[120:123]\n"
"v_mfma_f32_16x16x32_bf16 v[92:95], a[52:55], a[108:111], v[92:95]\n"
"v_mfma_f32_16x16x32_bf16 v[92:95], a[48:51], a[104:107], v[92:95]\n"
"v_mfma_f32_16x16x32_bf16 v[96:99], a[48:51], a[112:115], v[96:99]\n"
"v_mfma_f32_16x16x32_bf16 v[96:99], a[52:55], a[116:119], v[96:99]\n"
"global_load_dwordx4 a[36:39], v0, s[34:35] offset:-3072 sc1\n"
"v_mfma_f32_16x16x32_bf16 v[124:127], a[60:63], a[116:119], v[124:127]\n"
"v_mfma_f32_16x16x32_bf16 v[124:127], a[56:59], a[112:115], v[124:127]\n"
"v_mfma_f32_16x16x32_bf16 v[128:131], a[56:59], a[120:123], v[128:131]\n"
"v_mfma_f32_16x16x32_bf16 v[128:131], a[60:63], a[124:127], v[128:131]\n"
"v_mfma_f32_16x16x32_bf16 v[100:103], a[52:55], a[124:127], v[100:103]\n"
"v_mfma_f32_16x16x32_bf16 v[100:103], a[48:51], a[120:123], v[100:103]\n"
"v_mfma_f32_16x16x32_bf16 v[104:107], a[48:51], a[128:131], v[104:107]\n"
"ds_read_b128 v[24:27], v2 offset:24576\n"
"v_mfma_f32_16x16x32_bf16 v[104:107], a[52:55], a[132:135], v[104:107]\n"
"global_load_dwordx4 a[40:43], v0, s[34:35] offset:-2048 sc1\n"
"v_mfma_f32_16x16x32_bf16 v[132:135], a[60:63], a[132:135], v[132:135]\n"
"v_mfma_f32_16x16x32_bf16 v[132:135], a[56:59], a[128:131], v[132:135]\n"
"v_mfma_f32_16x16x32_bf16 v[136:139], a[56:59], a[136:139], v[136:139]\n"
"v_mfma_f32_16x16x32_bf16 v[136:139], a[60:63], a[140:143], v[136:139]\n"
"v_mfma_f32_16x16x32_bf16 v[108:111], a[52:55], a[140:143], v[108:111]\n"
"v_mfma_f32_16x16x32_bf16 v[108:111], a[48:51], a[136:139], v[108:111]\n"
"v_mfma_f32_16x16x32_bf16 v[112:115], a[48:51], a[144:147], v[112:115]\n"
"v_mfma_f32_16x16x32_bf16 v[112:115], a[52:55], a[148:151], v[112:115]\n"
"global_load_dwordx4 a[44:47], v0, s[34:35] offset:-1024 sc1\n"
"v_mfma_f32_16x16x32_bf16 v[140:143], a[60:63], a[148:151], v[140:143]\n"
"v_mfma_f32_16x16x32_bf16 v[140:143], a[56:59], a[144:147], v[140:143]\n"
"v_mfma_f32_16x16x32_bf16 a[12:15], a[56:59], a[152:155], a[12:15]\n"
"s_waitcnt lgkmcnt(0)\n"
"global_store_dwordx4 v0, v[24:27], s[54:55] offset:-4096 sc1\n"
"v_mfma_f32_16x16x32_bf16 a[12:15], a[60:63], a[156:159], a[12:15]\n"
"v_mfma_f32_16x16x32_bf16 a[8:11], a[52:55], a[156:159], a[8:11]\n"
"v_mfma_f32_16x16x32_bf16 a[8:11], a[48:51], a[152:155], a[8:11]\n"
"s_waitcnt vmcnt(18)\n"
"v_mfma_f32_16x16x32_bf16 v[144:147], a[64:67], a[96:99], v[144:147]\n"
"v_mfma_f32_16x16x32_bf16 v[144:147], a[68:71], a[100:103], v[144:147]\n"
"global_load_dwordx4 a[48:51], v0, s[34:35] offset:0 sc1\n"
"v_mfma_f32_16x16x32_bf16 v[172:175], a[76:79], a[100:103], v[172:175]\n"
"v_mfma_f32_16x16x32_bf16 v[172:175], a[72:75], a[96:99], v[172:175]\n"
"v_mfma_f32_16x16x32_bf16 v[176:179], a[72:75], a[104:107], v[176:179]\n"
"v_mfma_f32_16x16x32_bf16 v[176:179], a[76:79], a[108:111], v[176:179]\n"
"v_mfma_f32_16x16x32_bf16 v[148:151], a[68:71], a[108:111], v[148:151]\n"
"v_mfma_f32_16x16x32_bf16 v[148:151], a[64:67], a[104:107], v[148:151]\n"
"v_mfma_f32_16x16x32_bf16 v[152:155], a[64:67], a[112:115], v[152:155]\n"
"v_mfma_f32_16x16x32_bf16 v[152:155], a[68:71], a[116:119], v[152:155]\n"
"global_load_dwordx4 a[52:55], v0, s[34:35] offset:1024 sc1\n"
"v_mfma_f32_16x16x32_bf16 v[180:183], a[76:79], a[116:119], v[180:183]\n"
"v_mfma_f32_16x16x32_bf16 v[180:183], a[72:75], a[112:115], v[180:183]\n"
"v_mfma_f32_16x16x32_bf16 v[184:187], a[72:75], a[120:123], v[184:187]\n"
"v_mfma_f32_16x16x32_bf16 v[184:187], a[76:79], a[124:127], v[184:187]\n"
"v_mfma_f32_16x16x32_bf16 v[156:159], a[68:71], a[124:127], v[156:159]\n"
"v_mfma_f32_16x16x32_bf16 v[156:159], a[64:67], a[120:123], v[156:159]\n"
"ds_read_b128 v[28:31], v2 offset:26624\n"
"v_mfma_f32_16x16x32_bf16 v[160:163], a[64:67], a[128:131], v[160:163]\n"
"v_mfma_f32_16x16x32_bf16 v[160:163], a[68:71], a[132:135], v[160:163]\n"
"global_load_dwordx4 a[56:59], v0, s[34:35] offset:2048 sc1\n"
"v_mfma_f32_16x16x32_bf16 v[188:191], a[76:79], a[132:135], v[188:191]\n"
"v_mfma_f32_16x16x32_bf16 v[188:191], a[72:75], a[128:131], v[188:191]\n"
"v_mfma_f32_16x16x32_bf16 v[192:195], a[72:75], a[136:139], v[192:195]\n"
"v_mfma_f32_16x16x32_bf16 v[192:195], a[76:79], a[140:143], v[192:195]\n"
"v_mfma_f32_16x16x32_bf16 v[164:167], a[68:71], a[140:143], v[164:167]\n"
"v_mfma_f32_16x16x32_bf16 v[164:167], a[64:67], a[136:139], v[164:167]\n"
"v_mfma_f32_16x16x32_bf16 v[168:171], a[64:67], a[144:147], v[168:171]\n"
"v_mfma_f32_16x16x32_bf16 v[168:171], a[68:71], a[148:151], v[168:171]\n"
"global_load_dwordx4 a[60:63], v0, s[34:35] offset:3072 sc1\n"
"v_mfma_f32_16x16x32_bf16 v[196:199], a[76:79], a[148:151], v[196:199]\n"
"s_waitcnt lgkmcnt(0)\n"
"global_store_dwordx4 v0, v[28:31], s[54:55] offset:-2048 sc1\n"
"v_mfma_f32_16x16x32_bf16 v[196:199], a[72:75], a[144:147], v[196:199]\n"
"v_mfma_f32_16x16x32_bf16 a[20:23], a[72:75], a[152:155], a[20:23]\n"
"s_add_u32 s34, s34, 8192\n"
"v_mfma_f32_16x16x32_bf16 a[20:23], a[76:79], a[156:159], a[20:23]\n"
"v_mfma_f32_16x16x32_bf16 a[16:19], a[68:71], a[156:159], a[16:19]\n"
"v_mfma_f32_16x16x32_bf16 a[16:19], a[64:67], a[152:155], a[16:19]\n"
"s_waitcnt vmcnt(13)\n"
"v_mfma_f32_16x16x32_bf16 v[200:203], a[80:83], a[96:99], v[200:203]\n"
"v_mfma_f32_16x16x32_bf16 v[200:203], a[84:87], a[100:103], v[200:203]\n"
"global_load_dwordx4 a[64:67], v0, s[34:35] offset:-4096 sc1\n"
"v_mfma_f32_16x16x32_bf16 v[228:231], a[92:95], a[100:103], v[228:231]\n"
"ds_read_b128 a[100:103], v3 offset:17408\n"
"v_mfma_f32_16x16x32_bf16 v[228:231], a[88:91], a[96:99], v[228:231]\n"
"ds_read_b128 a[96:99], v3 offset:16384\n"
"v_mfma_f32_16x16x32_bf16 v[232:235], a[88:91], a[104:107], v[232:235]\n"
"v_mfma_f32_16x16x32_bf16 v[232:235], a[92:95], a[108:111], v[232:235]\n"
"v_mfma_f32_16x16x32_bf16 v[204:207], a[84:87], a[108:111], v[204:207]\n"
"ds_read_b128 a[108:111], v3 offset:19456\n"
"v_mfma_f32_16x16x32_bf16 v[204:207], a[80:83], a[104:107], v[204:207]\n"
"ds_read_b128 a[104:107], v3 offset:18432\n"
"v_mfma_f32_16x16x32_bf16 v[208:211], a[80:83], a[112:115], v[208:211]\n"
"v_mfma_f32_16x16x32_bf16 v[208:211], a[84:87], a[116:119], v[208:211]\n"
"global_load_dwordx4 a[68:71], v0, s[34:35] offset:-3072 sc1\n"
"v_mfma_f32_16x16x32_bf16 v[236:239], a[92:95], a[116:119], v[236:239]\n"
"ds_read_b128 a[116:119], v3 offset:21504\n"
"v_mfma_f32_16x16x32_bf16 v[236:239], a[88:91], a[112:115], v[236:239]\n"
"ds_read_b128 a[112:115], v3 offset:20480\n"
"v_mfma_f32_16x16x32_bf16 v[240:243], a[88:91], a[120:123], v[240:243]\n"
"v_mfma_f32_16x16x32_bf16 v[240:243], a[92:95], a[124:127], v[240:243]\n"
"ds_read_b128 v[24:27], v2 offset:29696\n"
"v_mfma_f32_16x16x32_bf16 v[212:215], a[84:87], a[124:127], v[212:215]\n"
"ds_read_b128 a[124:127], v3 offset:23552\n"
"v_mfma_f32_16x16x32_bf16 v[212:215], a[80:83], a[120:123], v[212:215]\n"
"ds_read_b128 a[120:123], v3 offset:22528\n"
"v_mfma_f32_16x16x32_bf16 v[216:219], a[80:83], a[128:131], v[216:219]\n"
"v_mfma_f32_16x16x32_bf16 v[216:219], a[84:87], a[132:135], v[216:219]\n"
"global_load_dwordx4 a[72:75], v0, s[34:35] offset:-2048 sc1\n"
"v_mfma_f32_16x16x32_bf16 v[244:247], a[92:95], a[132:135], v[244:247]\n"
"ds_read_b128 a[132:135], v3 offset:25600\n"
"v_mfma_f32_16x16x32_bf16 v[244:247], a[88:91], a[128:131], v[244:247]\n"
"ds_read_b128 a[128:131], v3 offset:24576\n"
"v_mfma_f32_16x16x32_bf16 v[248:251], a[88:91], a[136:139], v[248:251]\n"
"v_subrev_u32 v8, s65, v8\n"
"v_mfma_f32_16x16x32_bf16 v[248:251], a[92:95], a[140:143], v[248:251]\n"
"v_xor_b32 v9, s79, v9\n"
"v_mfma_f32_16x16x32_bf16 v[220:223], a[84:87], a[140:143], v[220:223]\n"
"ds_read_b128 a[140:143], v3 offset:27648\n"
"v_or_b32 v8, v8, v9\n"
"v_mfma_f32_16x16x32_bf16 v[220:223], a[80:83], a[136:139], v[220:223]\n"
"ds_read_b128 a[136:139], v3 offset:26624\n"
"v_cmp_lt_u32 vcc, 15, v8\n"
"v_mfma_f32_16x16x32_bf16 v[224:227], a[80:83], a[144:147], v[224:227]\n"
"s_waitcnt lgkmcnt(6)\n"
"global_store_dwordx4 v0, v[24:27], s[54:55] offset:1024 sc1\n"
"s_cbranch_vccnz L_slow7\n"
"L_back7:\n"
"v_mfma_f32_16x16x32_bf16 v[224:227], a[84:87], a[148:151], v[224:227]\n"
"global_load_dwordx4 a[76:79], v0, s[34:35] offset:-1024 sc1\n"
"v_readfirstlane_b32 s66, v9\n"
"v_mfma_f32_16x16x32_bf16 v[252:255], a[92:95], a[148:151], v[252:255]\n"
"ds_read_b128 a[148:151], v3 offset:29696\n"
"v_mfma_f32_16x16x32_bf16 v[252:255], a[88:91], a[144:147], v[252:255]\n"
"s_and_b32 s66, s66, 3\n"
"ds_read_b128 a[144:147], v3 offset:28672\n"
"v_mfma_f32_16x16x32_bf16 a[28:31], a[88:91], a[152:155], a[28:31]\n"
"s_lshl_b32 s69, s66, 13\n"
"v_mfma_f32_16x16x32_bf16 a[28:31], a[92:95], a[156:159], a[28:31]\n"
"s_cmp_eq_u32 s66, 3\n"
"s_cselect_b32 s75, 8192, 0\n"
"v_mfma_f32_16x16x32_bf16 a[24:27], a[84:87], a[156:159], a[24:27]\n"
"s_and_b32 s29, s66, s30\n"
"ds_read_b128 a[156:159], v3 offset:31744\n"
"v_mfma_f32_16x16x32_bf16 a[24:27], a[80:83], a[152:155], a[24:27]\n"
"s_waitcnt vmcnt(11)\n"
"ds_read_b128 a[152:155], v3 offset:30720\n"
"v_mfma_f32_16x16x32_bf16 v[32:35], a[32:35], a[96:99], v[32:35]\n"
"s_xor_b32 s31, s79, s66\n"
"v_mfma_f32_16x16x32_bf16 v[32:35], a[36:39], a[100:103], v[32:35]\n"
"global_load_dwordx4 a[80:83], v0, s[34:35] offset:0 sc1\n"
"v_mfma_f32_16x16x32_bf16 v[60:63], a[44:47], a[100:103], v[60:63]\n"
"v_mfma_f32_16x16x32_bf16 v[60:63], a[40:43], a[96:99], v[60:63]\n"
"v_mfma_f32_16x16x32_bf16 v[64:67], a[40:43], a[104:107], v[64:67]\n"
"v_mfma_f32_16x16x32_bf16 v[64:67], a[44:47], a[108:111], v[64:67]\n"
"v_mfma_f32_16x16x32_bf16 v[36:39], a[36:39], a[108:111], v[36:39]\n"
"s_add_u32 m0, s64, 0\n"
"v_mfma_f32_16x16x32_bf16 v[36:39], a[32:35], a[104:107], v[36:39]\n"
"s_waitcnt lgkmcnt(8)\n"
"v_mfma_f32_16x16x32_bf16 v[40:43], a[32:35], a[112:115], v[40:43]\n"
"v_mfma_f32_16x16x32_bf16 v[40:43], a[36:39], a[116:119], v[40:43]\n"
"global_load_dwordx4 a[84:87], v0, s[34:35] offset:1024 sc1\n"
"v_mfma_f32_16x16x32_bf16 v[68:71], a[44:47], a[116:119], v[68:71]\n"
"v_mfma_f32_16x16x32_bf16 v[68:71], a[40:43], a[112:115], v[68:71]\n"
"v_mfma_f32_16x16x32_bf16 v[72:75], a[40:43], a[120:123], v[72:75]\n"
"v_mfma_f32_16x16x32_bf16 v[72:75], a[44:47], a[124:127], v[72:75]\n"
"v_and_b32 v10, 0x300, v0\n"
"v_mfma_f32_16x16x32_bf16 v[44:47], a[36:39], a[124:127], v[44:47]\n"
"v_mfma_f32_16x16x32_bf16 v[44:47], a[32:35], a[120:123], v[44:47]\n"
"s_waitcnt lgkmcnt(4)\n"
"v_mfma_f32_16x16x32_bf16 v[48:51], a[32:35], a[128:131], v[48:51]\n"
"v_and_b32 v11, 0x3c0, v0\n"
"v_mfma_f32_16x16x32_bf16 v[48:51], a[36:39], a[132:135], v[48:51]\n"
"global_load_dwordx4 a[88:91], v0, s[34:35] offset:2048 sc1\n"
"v_mfma_f32_16x16x32_bf16 v[76:79], a[44:47], a[132:135], v[76:79]\n"
"v_mfma_f32_16x16x32_bf16 v[76:79], a[40:43], a[128:131], v[76:79]\n"
"v_mfma_f32_16x16x32_bf16 v[80:83], a[40:43], a[136:139], v[80:83]\n"
"v_add_lshl_u32 v11, v10, v11, 1\n"
"v_mfma_f32_16x16x32_bf16 v[80:83], a[44:47], a[140:143], v[80:83]\n"
"buffer_load_dwordx4 v0, s[48:51], s75 offen offset:0 sc1 lds\n"
"v_mfma_f32_16x16x32_bf16 v[52:55], a[36:39], a[140:143], v[52:55]\n"
"v_mfma_f32_16x16x32_bf16 v[52:55], a[32:35], a[136:139], v[52:55]\n"
"s_waitcnt lgkmcnt(0)\n"
"v_mfma_f32_16x16x32_bf16 v[56:59], a[32:35], a[144:147], v[56:59]\n"
"v_lshrrev_b32 v10, 2, v0\n"
"v_mfma_f32_16x16x32_bf16 v[56:59], a[36:39], a[148:151], v[56:59]\n"
"global_load_dwordx4 a[92:95], v0, s[34:35] offset:3072 sc1\n"
"v_mfma_f32_16x16x32_bf16 v[84:87], a[44:47], a[148:151], v[84:87]\n"
"v_mfma_f32_16x16x32_bf16 v[84:87], a[40:43], a[144:147], v[84:87]\n"
"v_xor_b32 v10, v10, v0\n"
"v_mfma_f32_16x16x32_bf16 a[4:7], a[40:43], a[152:155], a[4:7]\n"
"s_add_u32 s34, s34, 8192\n"
"v_mfma_f32_16x16x32_bf16 a[4:7], a[44:47], a[156:159], a[4:7]\n"
"v_mfma_f32_16x16x32_bf16 a[0:3], a[36:39], a[156:159], a[0:3]\n"
"v_mfma_f32_16x16x32_bf16 a[0:3], a[32:35], a[152:155], a[0:3]\n"
"s_waitcnt vmcnt(11)\n"
"v_and_b32 v10, 0x30, v10\n"
"v_mfma_f32_16x16x32_bf16 v[88:91], a[48:51], a[96:99], v[88:91]\n"
"v_mfma_f32_16x16x32_bf16 v[88:91], a[52:55], a[100:103], v[88:91]\n"
"global_load_dwordx4 a[32:35], v0, s[34:35] offset:-4096 sc1\n"
"v_mfma_f32_16x16x32_bf16 v[116:119], a[60:63], a[100:103], v[116:119]\n"
"v_add3_u32 v11, v10, v11, s64\n"
"v_mfma_f32_16x16x32_bf16 v[116:119], a[56:59], a[96:99], v[116:119]\n"
"buffer_load_dwordx4 v0, s[48:51], s75 offen offset:1024 sc1 lds\n"
"v_mfma_f32_16x16x32_bf16 v[120:123], a[56:59], a[104:107], v[120:123]\n"
"v_mfma_f32_16x16x32_bf16 v[120:123], a[60:63], a[108:111], v[120:123]\n"
"v_mfma_f32_16x16x32_bf16 v[92:95], a[52:55], a[108:111], v[92:95]\n"
"s_add_u32 s27, s27, 0x200000\n"
"v_mfma_f32_16x16x32_bf16 v[92:95], a[48:51], a[104:107], v[92:95]\n"
"v_mfma_f32_16x16x32_bf16 v[96:99], a[48:51], a[112:115], v[96:99]\n"
"v_mfma_f32_16x16x32_bf16 v[96:99], a[52:55], a[116:119], v[96:99]\n"
"global_load_dwordx4 a[36:39], v0, s[34:35] offset:-3072 sc1\n"
"v_mfma_f32_16x16x32_bf16 v[124:127], a[60:63], a[116:119], v[124:127]\n"
"ds_read_b64_tr_b16 v[24:25], v11 offset:12288\n"
"v_mfma_f32_16x16x32_bf16 v[124:127], a[56:59], a[112:115], v[124:127]\n"
"v_mfma_f32_16x16x32_bf16 v[128:131], a[56:59], a[120:123], v[128:131]\n"
"ds_read_b64_tr_b16 v[26:27], v11 offset:12864\n"
"v_mfma_f32_16x16x32_bf16 v[128:131], a[60:63], a[124:127], v[128:131]\n"
"L_st8529:\n"
"s_cbranch_execz L_retB\n"
"v_mfma_f32_16x16x32_bf16 v[100:103], a[52:55], a[124:127], v[100:103]\n"
"ds_read_b64_tr_b16 v[28:29], v11 offset:12296\n"
"v_mfma_f32_16x16x32_bf16 v[100:103], a[48:51], a[120:123], v[100:103]\n"
"v_mfma_f32_16x16x32_bf16 v[104:107], a[48:51], a[128:131], v[104:107]\n"
"ds_read_b64_tr_b16 v[30:31], v11 offset:12872\n"
"v_mfma_f32_16x16x32_bf16 v[104:107], a[52:55], a[132:135], v[104:107]\n"
"global_load_dwordx4 a[40:43], v0, s[34:35] offset:-2048 sc1\n"
"v_mfma_f32_16x16x32_bf16 v[132:135], a[60:63], a[132:135], v[132:135]\n"
"buffer_load_dwordx4 v0, s[48:51], s75 offen offset:2048 sc1 lds\n"
"v_mfma_f32_16x16x32_bf16 v[132:135], a[56:59], a[128:131], v[132:135]\n"
"v_mfma_f32_16x16x32_bf16 v[136:139], a[56:59], a[136:139], v[136:139]\n"
"v_mfma_f32_16x16x32_bf16 v[136:139], a[60:63], a[140:143], v[136:139]\n"
"v_mfma_f32_16x16x32_bf16 v[108:111], a[52:55], a[140:143], v[108:111]\n"
"v_mfma_f32_16x16x32_bf16 v[108:111], a[48:51], a[136:139], v[108:111]\n"
"v_mfma_f32_16x16x32_bf16 v[112:115], a[48:51], a[144:147], v[112:115]\n"
"s_waitcnt lgkmcnt(0)\n"
"v_mfma_f32_16x16x32_bf16 v[112:115], a[52:55], a[148:151], v[112:115]\n"
"global_load_dwordx4 a[44:47], v0, s[34:35] offset:-1024 sc1\n"
"v_mfma_f32_16x16x32_bf16 v[140:143], a[60:63], a[148:151], v[140:143]\n"
"v_mfma_f32_16x16x32_bf16 v[140:143], a[56:59], a[144:147], v[140:143]\n"
"v_mfma_f32_16x16x32_bf16 a[12:15], a[56:59], a[152:155], a[12:15]\n"
"v_mfma_f32_16x16x32_bf16 a[12:15], a[60:63], a[156:159], a[12:15]\n"
"v_mfma_f32_16x16x32_bf16 a[8:11], a[52:55], a[156:159], a[8:11]\n"
"v_mfma_f32_16x16x32_bf16 a[8:11], a[48:51], a[152:155], a[8:11]\n"
"s_waitcnt vmcnt(11)\n"
"v_mfma_f32_16x16x32_bf16 v[144:147], a[64:67], a[96:99], v[144:147]\n"
"buffer_store_dwordx4 v[24:27], v0, s[36:39], s28 offen offset:0 sc1\n"
"buffer_load_dwordx4 v0, s[48:51], s75 offen offset:3072 sc1 lds\n"
"v_mfma_f32_16x16x32_bf16 v[144:147], a[68:71], a[100:103], v[144:147]\n"
"global_load_dwordx4 a[48:51], v0, s[34:35] offset:0 sc1\n"
"v_mfma_f32_16x16x32_bf16 v[172:175], a[76:79], a[100:103], v[172:175]\n"
"v_mfma_f32_16x16x32_bf16 v[172:175], a[72:75], a[96:99], v[172:175]\n"
"v_mfma_f32_16x16x32_bf16 v[176:179], a[72:75], a[104:107], v[176:179]\n"
"v_mfma_f32_16x16x32_bf16 v[176:179], a[76:79], a[108:111], v[176:179]\n"
"v_mfma_f32_16x16x32_bf16 v[148:151], a[68:71], a[108:111], v[148:151]\n"
"v_mfma_f32_16x16x32_bf16 v[148:151], a[64:67], a[104:107], v[148:151]\n"
"v_mfma_f32_16x16x32_bf16 v[152:155], a[64:67], a[112:115], v[152:155]\n"
"v_mfma_f32_16x16x32_bf16 v[152:155], a[68:71], a[116:119], v[152:155]\n"
"global_load_dwordx4 a[52:55], v0, s[34:35] offset:1024 sc1\n"
"v_mfma_f32_16x16x32_bf16 v[180:183], a[76:79], a[116:119], v[180:183]\n"
"v_mfma_f32_16x16x32_bf16 v[180:183], a[72:75], a[112:115], v[180:183]\n"
"v_mfma_f32_16x16x32_bf16 v[184:187], a[72:75], a[120:123], v[184:187]\n"
"v_mfma_f32_16x16x32_bf16 v[184:187], a[76:79], a[124:127], v[184:187]\n"
"v_mfma_f32_16x16x32_bf16 v[156:159], a[68:71], a[124:127], v[156:159]\n"
"v_mfma_f32_16x16x32_bf16 v[156:159], a[64:67], a[120:123], v[156:159]\n"
"s_add_u32 m0, s64, 4096\n"
"v_mfma_f32_16x16x32_bf16 v[160:163], a[64:67], a[128:131], v[160:163]\n"
"v_mfma_f32_16x16x32_bf16 v[160:163], a[68:71], a[132:135], v[160:163]\n"
"global_load_dwordx4 a[56:59], v0, s[34:35] offset:2048 sc1\n"
"v_mfma_f32_16x16x32_bf16 v[188:191], a[76:79], a[132:135], v[188:191]\n"
"v_mfma_f32_16x16x32_bf16 v[188:191], a[72:75], a[128:131], v[188:191]\n"
"v_mfma_f32_16x16x32_bf16 v[192:195], a[72:75], a[136:139], v[192:195]\n"
"v_mfma_f32_16x16x32_bf16 v[192:195], a[76:79], a[140:143], v[192:195]\n"
"v_mfma_f32_16x16x32_bf16 v[164:167], a[68:71], a[140:143], v[164:167]\n"
"v_mfma_f32_16x16x32_bf16 v[164:167], a[64:67], a[136:139], v[164:167]\n"
"v_mfma_f32_16x16x32_bf16 v[168:171], a[64:67], a[144:147], v[168:171]\n"
"v_mfma_f32_16x16x32_bf16 v[168:171], a[68:71], a[148:151], v[168:171]\n"
"global_load_dwordx4 a[60:63], v0, s[34:35] offset:3072 sc1\n"
"v_mfma_f32_16x16x32_bf16 v[196:199], a[76:79], a[148:151], v[196:199]\n"
"v_mfma_f32_16x16x32_bf16 v[196:199], a[72:75], a[144:147], v[196:199]\n"
"v_mfma_f32_16x16x32_bf16 a[20:23], a[72:75], a[152:155], a[20:23]\n"
"s_add_u32 s34, s34, 8192\n"
"v_mfma_f32_16x16x32_bf16 a[20:23], a[76:79], a[156:159], a[20:23]\n"
"v_mfma_f32_16x16x32_bf16 a[16:19], a[68:71], a[156:159], a[16:19]\n"
"buffer_load_dwordx4 v1, s[48:51], s75 offen offset:0 sc1 lds\n"
"v_mfma_f32_16x16x32_bf16 a[16:19], a[64:67], a[152:155], a[16:19]\n"
"s_waitcnt vmcnt(13)\n"
"v_mfma_f32_16x16x32_bf16 v[200:203], a[80:83], a[96:99], v[200:203]\n"
"v_mfma_f32_16x16x32_bf16 v[200:203], a[84:87], a[100:103], v[200:203]\n"
"global_load_dwordx4 a[64:67], v0, s[34:35] offset:-4096 sc1\n"
"s_add_u32 s65, s65, 1\n"
"v_mfma_f32_16x16x32_bf16 v[228:231], a[92:95], a[100:103], v[228:231]\n"
"ds_read_b128 a[100:103], v4 offset:17408\n"
"v_mfma_f32_16x16x32_bf16 v[228:231], a[88:91], a[96:99], v[228:231]\n"
"ds_read_b128 a[96:99], v4 offset:16384\n"
"v_mov_b32 v8, s65\n"
"v_mfma_f32_16x16x32_bf16 v[232:235], a[88:91], a[104:107], v[232:235]\n"
"v_mov_b32 v9, s60\n"
"v_mfma_f32_16x16x32_bf16 v[232:235], a[92:95], a[108:111], v[232:235]\n"
"v_mfma_f32_16x16x32_bf16 v[204:207], a[84:87], a[108:111], v[204:207]\n"
"buffer_store_dwordx2 v[8:9], off, s[56:59], %13 sc1\n"
"ds_read_b128 a[108:111], v4 offset:19456\n"
"v_mfma_f32_16x16x32_bf16 v[204:207], a[80:83], a[104:107], v[204:207]\n"
"ds_read_b128 a[104:107], v4 offset:18432\n"
"v_mfma_f32_16x16x32_bf16 v[208:211], a[80:83], a[112:115], v[208:211]\n"
"buffer_store_dwordx4 v[28:31], v0, s[36:39], s28 offen offset:2048 sc1\n"
"buffer_load_dwordx4 v1, s[48:51], s75 offen offset:1024 sc1 lds\n"
"v_mfma_f32_16x16x32_bf16 v[208:211], a[84:87], a[116:119], v[208:211]\n"
"global_load_dwordx4 a[68:71], v0, s[34:35] offset:-3072 sc1\n"
"v_mfma_f32_16x16x32_bf16 v[236:239], a[92:95], a[116:119], v[236:239]\n"
"ds_read_b128 a[116:119], v4 offset:21504\n"
"v_mfma_f32_16x16x32_bf16 v[236:239], a[88:91], a[112:115], v[236:239]\n"
"ds_read_b128 a[112:115], v4 offset:20480\n"
"v_mfma_f32_16x16x32_bf16 v[240:243], a[88:91], a[120:123], v[240:243]\n"
"v_mfma_f32_16x16x32_bf16 v[240:243], a[92:95], a[124:127], v[240:243]\n"
"v_mfma_f32_16x16x32_bf16 v[212:215], a[84:87], a[124:127], v[212:215]\n"
"ds_read_b128 a[124:127], v4 offset:23552\n"
"v_mfma_f32_16x16x32_bf16 v[212:215], a[80:83], a[120:123], v[212:215]\n"
"ds_read_b128 a[120:123], v4 offset:22528\n"
"v_mfma_f32_16x16x32_bf16 v[216:219], a[80:83], a[128:131], v[216:219]\n"
"ds_read_b64_tr_b16 v[24:25], v11 offset:12352\n"
"v_mfma_f32_16x16x32_bf16 v[216:219], a[84:87], a[132:135], v[216:219]\n"
"global_load_dwordx4 a[72:75], v0, s[34:35] offset:-2048 sc1\n"
"ds_read_b64_tr_b16 v[26:27], v11 offset:12800\n"
"v_mfma_f32_16x16x32_bf16 v[244:247], a[92:95], a[132:135], v[244:247]\n"
"ds_read_b128 a[132:135], v4 offset:25600\n"
"v_mfma_f32_16x16x32_bf16 v[244:247], a[88:91], a[128:131], v[244:247]\n"
"ds_read_b128 a[128:131], v4 offset:24576\n"
"v_mfma_f32_16x16x32_bf16 v[248:251], a[88:91], a[136:139], v[248:251]\n"
"ds_read_b64_tr_b16 v[28:29], v11 offset:12360\n"
"v_mfma_f32_16x16x32_bf16 v[248:251], a[92:95], a[140:143], v[248:251]\n"
"ds_read_b64_tr_b16 v[30:31], v11 offset:12808\n"
"v_mfma_f32_16x16x32_bf16 v[220:223], a[84:87], a[140:143], v[220:223]\n"
"ds_read_b128 a[140:143], v4 offset:27648\n"
"v_mfma_f32_16x16x32_bf16 v[220:223], a[80:83], a[136:139], v[220:223]\n"
"ds_read_b128 a[136:139], v4 offset:26624\n"
"v_mfma_f32_16x16x32_bf16 v[224:227], a[80:83], a[144:147], v[224:227]\n"
"v_mfma_f32_16x16x32_bf16 v[224:227], a[84:87], a[148:151], v[224:227]\n"
"global_load_dwordx4 a[76:79], v0, s[34:35] offset:-1024 sc1\n"
"v_mfma_f32_16x16x32_bf16 v[252:255], a[92:95], a[148:151], v[252:255]\n"
"ds_read_b128 a[148:151], v4 offset:29696\n"
"v_mfma_f32_16x16x32_bf16 v[252:255], a[88:91], a[144:147], v[252:255]\n"
"ds_read_b128 a[144:147], v4 offset:28672\n"
"v_mfma_f32_16x16x32_bf16 a[28:31], a[88:91], a[152:155], a[28:31]\n"
"buffer_load_dwordx4 v1, s[48:51], s75 offen offset:2048 sc1 lds\n"
"v_mfma_f32_16x16x32_bf16 a[28:31], a[92:95], a[156:159], a[28:31]\n"
"v_mfma_f32_16x16x32_bf16 a[24:27], a[84:87], a[156:159], a[24:27]\n"
"s_waitcnt lgkmcnt(4)\n"
"ds_read_b128 a[156:159], v4 offset:31744\n"
"v_mfma_f32_16x16x32_bf16 a[24:27], a[80:83], a[152:155], a[24:27]\n"
"s_waitcnt vmcnt(15)\n"
"ds_read_b128 a[152:155], v4 offset:30720\n"
"v_mfma_f32_16x16x32_bf16 v[32:35], a[32:35], a[96:99], v[32:35]\n"
"v_mfma_f32_16x16x32_bf16 v[32:35], a[36:39], a[100:103], v[32:35]\n"
"global_load_dwordx4 a[80:83], v0, s[34:35] offset:0 sc1\n"
"v_mfma_f32_16x16x32_bf16 v[60:63], a[44:47], a[100:103], v[60:63]\n"
"v_mfma_f32_16x16x32_bf16 v[60:63], a[40:43], a[96:99], v[60:63]\n"
"v_mfma_f32_16x16x32_bf16 v[64:67], a[40:43], a[104:107], v[64:67]\n"
"v_mfma_f32_16x16x32_bf16 v[64:67], a[44:47], a[108:111], v[64:67]\n"
"v_mfma_f32_16x16x32_bf16 v[36:39], a[36:39], a[108:111], v[36:39]\n"
"v_mfma_f32_16x16x32_bf16 v[36:39], a[32:35], a[104:107], v[36:39]\n"
"v_mfma_f32_16x16x32_bf16 v[40:43], a[32:35], a[112:115], v[40:43]\n"
"v_mfma_f32_16x16x32_bf16 v[40:43], a[36:39], a[116:119], v[40:43]\n"
"global_load_dwordx4 a[84:87], v0, s[34:35] offset:1024 sc1\n"
"v_mfma_f32_16x16x32_bf16 v[68:71], a[44:47], a[116:119], v[68:71]\n"
"buffer_load_dwordx4 v1, s[48:51], s75 offen offset:3072 sc1 lds\n"
"v_mfma_f32_16x16x32_bf16 v[68:71], a[40:43], a[112:115], v[68:71]\n"
"v_mfma_f32_16x16x32_bf16 v[72:75], a[40:43], a[120:123], v[72:75]\n"
"v_mfma_f32_16x16x32_bf16 v[72:75], a[44:47], a[124:127], v[72:75]\n"
"v_mfma_f32_16x16x32_bf16 v[44:47], a[36:39], a[124:127], v[44:47]\n"
"v_mfma_f32_16x16x32_bf16 v[44:47], a[32:35], a[120:123], v[44:47]\n"
"s_waitcnt lgkmcnt(4)\n"
"v_mfma_f32_16x16x32_bf16 v[48:51], a[32:35], a[128:131], v[48:51]\n"
"v_mfma_f32_16x16x32_bf16 v[48:51], a[36:39], a[132:135], v[48:51]\n"
"global_load_dwordx4 a[88:91], v0, s[34:35] offset:2048 sc1\n"
"v_mfma_f32_16x16x32_bf16 v[76:79], a[44:47], a[132:135], v[76:79]\n"
"v_mfma_f32_16x16x32_bf16 v[76:79], a[40:43], a[128:131], v[76:79]\n"
"v_mfma_f32_16x16x32_bf16 v[80:83], a[40:43], a[136:139], v[80:83]\n"
"v_mfma_f32_16x16x32_bf16 v[80:83], a[44:47], a[140:143], v[80:83]\n"
"v_mfma_f32_16x16x32_bf16 v[52:55], a[36:39], a[140:143], v[52:55]\n"
"v_mfma_f32_16x16x32_bf16 v[52:55], a[32:35], a[136:139], v[52:55]\n"
"s_waitcnt lgkmcnt(0)\n"
"v_mfma_f32_16x16x32_bf16 v[56:59], a[32:35], a[144:147], v[56:59]\n"
"v_mfma_f32_16x16x32_bf16 v[56:59], a[36:39], a[148:151], v[56:59]\n"
"s_add_u32 m0, s64, 8192\n"
"global_load_dwordx4 a[92:95], v0, s[34:35] offset:3072 sc1\n"
"v_mfma_f32_16x16x32_bf16 v[84:87], a[44:47], a[148:151], v[84:87]\n"
"v_mfma_f32_16x16x32_bf16 v[84:87], a[40:43], a[144:147], v[84:87]\n"
"v_mfma_f32_16x16x32_bf16 a[4:7], a[40:43], a[152:155], a[4:7]\n"
"s_add_u32 s34, s34, 8192\n"
"v_mfma_f32_16x16x32_bf16 a[4:7], a[44:47], a[156:159], a[4:7]\n"
"v_mfma_f32_16x16x32_bf16 a[0:3], a[36:39], a[156:159], a[0:3]\n"
"v_mfma_f32_16x16x32_bf16 a[0:3], a[32:35], a[152:155], a[0:3]\n"
"s_waitcnt vmcnt(14)\n"
"v_mfma_f32_16x16x32_bf16 v[88:91], a[48:51], a[96:99], v[88:91]\n"
"v_mfma_f32_16x16x32_bf16 v[88:91], a[52:55], a[100:103], v[88:91]\n"
"global_load_dwordx4 a[32:35], v0, s[34:35] offset:-4096 sc1\n"
"v_mfma_f32_16x16x32_bf16 v[116:119], a[60:63], a[100:103], v[116:119]\n"
"v_mfma_f32_16x16x32_bf16 v[116:119], a[56:59], a[96:99], v[116:119]\n"
"v_mfma_f32_16x16x32_bf16 v[120:123], a[56:59], a[104:107], v[120:123]\n"
"v_mfma_f32_16x16x32_bf16 v[120:123], a[60:63], a[108:111], v[120:123]\n"
"v_mfma_f32_16x16x32_bf16 v[92:95], a[52:55], a[108:111], v[92:95]\n"
"v_mfma_f32_16x16x32_bf16 v[92:95], a[48:51], a[104:107], v[92:95]\n"
"v_mfma_f32_16x16x32_bf16 v[96:99], a[48:51], a[112:115], v[96:99]\n"
"buffer_load_dwordx4 v0, s[48:51], s69 offen offset:0 sc1 lds\n"
"v_mfma_f32_16x16x32_bf16 v[96:99], a[52:55], a[116:119], v[96:99]\n"
"global_load_dwordx4 a[36:39], v0, s[34:35] offset:-3072 sc1\n"
"v_mfma_f32_16x16x32_bf16 v[124:127], a[60:63], a[116:119], v[124:127]\n"
"v_mfma_f32_16x16x32_bf16 v[124:127], a[56:59], a[112:115], v[124:127]\n"
"v_mfma_f32_16x16x32_bf16 v[128:131], a[56:59], a[120:123], v[128:131]\n"
"v_mfma_f32_16x16x32_bf16 v[128:131], a[60:63], a[124:127], v[128:131]\n"
"v_mfma_f32_16x16x32_bf16 v[100:103], a[52:55], a[124:127], v[100:103]\n"
"v_mfma_f32_16x16x32_bf16 v[100:103], a[48:51], a[120:123], v[100:103]\n"
"v_mfma_f32_16x16x32_bf16 v[104:107], a[48:51], a[128:131], v[104:107]\n"
"v_mfma_f32_16x16x32_bf16 v[104:107], a[52:55], a[132:135], v[104:107]\n"
"global_load_dwordx4 a[40:43], v0, s[34:35] offset:-2048 sc1\n"
"v_mfma_f32_16x16x32_bf16 v[132:135], a[60:63], a[132:135], v[132:135]\n"
"v_mfma_f32_16x16x32_bf16 v[132:135], a[56:59], a[128:131], v[132:135]\n"
"v_mfma_f32_16x16x32_bf16 v[136:139], a[56:59], a[136:139], v[136:139]\n"
"v_mfma_f32_16x16x32_bf16 v[136:139], a[60:63], a[140:143], v[136:139]\n"
"v_mfma_f32_16x16x32_bf16 v[108:111], a[52:55], a[140:143], v[108:111]\n"
"buffer_load_dwordx4 v0, s[48:51], s69 offen offset:1024 sc1 lds\n"
"v_mfma_f32_16x16x32_bf16 v[108:111], a[48:51], a[136:139], v[108:111]\n"
"v_mfma_f32_16x16x32_bf16 v[112:115], a[48:51], a[144:147], v[112:115]\n"
"v_mfma_f32_16x16x32_bf16 v[112:115], a[52:55], a[148:151], v[112:115]\n"
"global_load_dwordx4 a[44:47], v0, s[34:35] offset:-1024 sc1\n"
"v_mfma_f32_16x16x32_bf16 v[140:143], a[60:63], a[148:151], v[140:143]\n"
"v_mfma_f32_16x16x32_bf16 v[140:143], a[56:59], a[144:147], v[140:143]\n"
"v_mfma_f32_16x16x32_bf16 a[12:15], a[56:59], a[152:155], a[12:15]\n"
"v_mfma_f32_16x16x32_bf16 a[12:15], a[60:63], a[156:159], a[12:15]\n"
"v_mfma_f32_16x16x32_bf16 a[8:11], a[52:55], a[156:159], a[8:11]\n"
"v_mfma_f32_16x16x32_bf16 a[8:11], a[48:51], a[152:155], a[8:11]\n"
"s_waitcnt vmcnt(12)\n"
"v_mfma_f32_16x16x32_bf16 v[144:147], a[64:67], a[96:99], v[144:147]\n"
"v_mfma_f32_16x16x32_bf16 v[144:147], a[68:71], a[100:103], v[144:147]\n"
"global_load_dwordx4 a[48:51], v0, s[34:35] offset:0 sc1\n"
"v_mfma_f32_16x16x32_bf16 v[172:175], a[76:79], a[100:103], v[172:175]\n"
"v_mfma_f32_16x16x32_bf16 v[172:175], a[72:75], a[96:99], v[172:175]\n"
"v_mfma_f32_16x16x32_bf16 v[176:179], a[72:75], a[104:107], v[176:179]\n"
"v_mfma_f32_16x16x32_bf16 v[176:179], a[76:79], a[108:111], v[176:179]\n"
"buffer_load_dwordx4 v0, s[48:51], s69 offen offset:2048 sc1 lds\n"
"v_mfma_f32_16x16x32_bf16 v[148:151], a[68:71], a[108:111], v[148:151]\n"
"v_mfma_f32_16x16x32_bf16 v[148:151], a[64:67], a[104:107], v[148:151]\n"
"v_mfma_f32_16x16x32_bf16 v[152:155], a[64:67], a[112:115], v[152:155]\n"
"v_lshlrev_b32 v16, 16, v18\n"
"v_mfma_f32_16x16x32_bf16 v[152:155], a[68:71], a[116:119], v[152:155]\n"
"global_load_dwordx4 a[52:55], v0, s[34:35] offset:1024 sc1\n"
"v_and_b32 v17, s63, v18\n"
"v_mfma_f32_16x16x32_bf16 v[180:183], a[76:79], a[116:119], v[180:183]\n"
"v_lshlrev_b32 v18, 16, v19\n"
"v_mfma_f32_16x16x32_bf16 v[180:183], a[72:75], a[112:115], v[180:183]\n"
"v_and_b32 v19, s63, v19\n"
"v_mfma_f32_16x16x32_bf16 v[184:187], a[72:75], a[120:123], v[184:187]\n"
"v_add_f32 v32, v32, v16\n"
"v_mfma_f32_16x16x32_bf16 v[184:187], a[76:79], a[124:127], v[184:187]\n"
"v_add_f32 v33, v33, v17\n"
"v_add_f32 v34, v34, v18\n"
"v_mfma_f32_16x16x32_bf16 v[156:159], a[68:71], a[124:127], v[156:159]\n"
"v_add_f32 v35, v35, v19\n"
"v_mfma_f32_16x16x32_bf16 v[156:159], a[64:67], a[120:123], v[156:159]\n"
"v_add_f32 v36, v36, v16\n"
"v_mfma_f32_16x16x32_bf16 v[160:163], a[64:67], a[128:131], v[160:163]\n"
"v_add_f32 v37, v37, v17\n"
"v_mfma_f32_16x16x32_bf16 v[160:163], a[68:71], a[132:135], v[160:163]\n"
"global_load_dwordx4 a[56:59], v0, s[34:35] offset:2048 sc1\n"
"v_add_f32 v38, v38, v18\n"
"v_mfma_f32_16x16x32_bf16 v[188:191], a[76:79], a[132:135], v[188:191]\n"
"v_add_f32 v39, v39, v19\n"
"v_mfma_f32_16x16x32_bf16 v[188:191], a[72:75], a[128:131], v[188:191]\n"
"v_add_f32 v40, v40, v16\n"
"v_add_f32 v41, v41, v17\n"
"v_mfma_f32_16x16x32_bf16 v[192:195], a[72:75], a[136:139], v[192:195]\n"
"buffer_load_dwordx4 v0, s[48:51], s69 offen offset:3072 sc1 lds\n"
"v_mfma_f32_16x16x32_bf16 v[192:195], a[76:79], a[140:143], v[192:195]\n"
"v_add_f32 v42, v42, v18\n"
"v_mfma_f32_16x16x32_bf16 v[164:167], a[68:71], a[140:143], v[164:167]\n"
"v_add_f32 v43, v43, v19\n"
"v_mfma_f32_16x16x32_bf16 v[164:167], a[64:67], a[136:139], v[164:167]\n"
"v_add_f32 v44, v44, v16\n"
"v_add_f32 v45, v45, v17\n"
"v_mfma_f32_16x16x32_bf16 v[168:171], a[64:67], a[144:147], v[168:171]\n"
"v_add_f32 v46, v46, v18\n"
"v_mfma_f32_16x16x32_bf16 v[168:171], a[68:71], a[148:151], v[168:171]\n"
"global_load_dwordx4 a[60:63], v0, s[34:35] offset:3072 sc1\n"
"v_add_f32 v47, v47, v19\n"
"v_mfma_f32_16x16x32_bf16 v[196:199], a[76:79], a[148:151], v[196:199]\n"
"v_add_f32 v48, v48, v16\n"
"v_mfma_f32_16x16x32_bf16 v[196:199], a[72:75], a[144:147], v[196:199]\n"
"v_add_f32 v49, v49, v17\n"
"v_mfma_f32_16x16x32_bf16 a[20:23], a[72:75], a[152:155], a[20:23]\n"
"s_add_u32 s34, s34, 8192\n"
"v_add_f32 v50, v50, v18\n"
"v_mfma_f32_16x16x32_bf16 a[20:23], a[76:79], a[156:159], a[20:23]\n"
"v_add_f32 v51, v51, v19\n"
"v_mfma_f32_16x16x32_bf16 a[16:19], a[68:71], a[156:159], a[16:19]\n"
"v_add_f32 v52, v52, v16\n"
"v_mfma_f32_16x16x32_bf16 a[16:19], a[64:67], a[152:155], a[16:19]\n"
"s_waitcnt vmcnt(12)\n"
"v_add_f32 v53, v53, v17\n"
"v_mfma_f32_16x16x32_bf16 v[200:203], a[80:83], a[96:99], v[200:203]\n"
"v_add_f32 v54, v54, v18\n"
"v_add_f32 v55, v55, v19\n"
"v_mfma_f32_16x16x32_bf16 v[200:203], a[84:87], a[100:103], v[200:203]\n"
"global_load_dwordx4 a[64:67], v0, s[34:35] offset:-4096 sc1\n"
"v_add_f32 v56, v56, v16\n"
"v_mfma_f32_16x16x32_bf16 v[228:231], a[92:95], a[100:103], v[228:231]\n"
"s_add_u32 m0, s64, 12288\n"
"ds_read_b128 a[100:103], v5 offset:17408\n"
"v_add_f32 v57, v57, v17\n"
"v_mfma_f32_16x16x32_bf16 v[228:231], a[88:91], a[96:99], v[228:231]\n"
"ds_read_b128 a[96:99], v5 offset:16384\n"
"v_add_f32 v58, v58, v18\n"
"v_mfma_f32_16x16x32_bf16 v[232:235], a[88:91], a[104:107], v[232:235]\n"
"v_add_f32 v59, v59, v19\n"
"v_mfma_f32_16x16x32_bf16 v[232:235], a[92:95], a[108:111], v[232:235]\n"
"v_accvgpr_read_b32 v12, a0\n"
"v_mfma_f32_16x16x32_bf16 v[204:207], a[84:87], a[108:111], v[204:207]\n"
"ds_read_b128 a[108:111], v5 offset:19456\n"
"v_accvgpr_read_b32 v13, a1\n"
"v_mfma_f32_16x16x32_bf16 v[204:207], a[80:83], a[104:107], v[204:207]\n"
"ds_read_b128 a[104:107], v5 offset:18432\n"
"v_accvgpr_read_b32 v14, a2\n"
"v_mfma_f32_16x16x32_bf16 v[208:211], a[80:83], a[112:115], v[208:211]\n"
"v_accvgpr_read_b32 v15, a3\n"
"v_mfma_f32_16x16x32_bf16 v[208:211], a[84:87], a[116:119], v[208:211]\n"
"global_load_dwordx4 a[68:71], v0, s[34:35] offset:-3072 sc1\n"
"v_add_f32 v12, v12, v16\n"
"v_mfma_f32_16x16x32_bf16 v[236:239], a[92:95], a[116:119], v[236:239]\n"
"ds_read_b128 a[116:119], v5 offset:21504\n"
"v_add_f32 v13, v13, v17\n"
"v_mfma_f32_16x16x32_bf16 v[236:239], a[88:91], a[112:115], v[236:239]\n"
"ds_read_b128 a[112:115], v5 offset:20480\n"
"v_add_f32 v14, v14, v18\n"
"v_mfma_f32_16x16x32_bf16 v[240:243], a[88:91], a[120:123], v[240:243]\n"
"v_add_f32 v15, v15, v19\n"
"v_accvgpr_write_b32 a0, v12\n"
"v_mfma_f32_16x16x32_bf16 v[240:243], a[92:95], a[124:127], v[240:243]\n"
"v_accvgpr_write_b32 a1, v13\n"
"v_mfma_f32_16x16x32_bf16 v[212:215], a[84:87], a[124:127], v[212:215]\n"
"ds_read_b128 a[124:127], v5 offset:23552\n"
"v_accvgpr_write_b32 a2, v14\n"
"v_mfma_f32_16x16x32_bf16 v[212:215], a[80:83], a[120:123], v[212:215]\n"
"ds_read_b128 a[120:123], v5 offset:22528\n"
"v_accvgpr_write_b32 a3, v15\n"
"v_mfma_f32_16x16x32_bf16 v[216:219], a[80:83], a[128:131], v[216:219]\n"
"buffer_load_dwordx4 v1, s[48:51], s69 offen offset:0 sc1 lds\n"
"v_mfma_f32_16x16x32_bf16 v[216:219], a[84:87], a[132:135], v[216:219]\n"
"global_load_dwordx4 a[72:75], v0, s[34:35] offset:-2048 sc1\n"
"v_lshrrev_b32 v16, 4, v0\n"
"v_mfma_f32_16x16x32_bf16 v[244:247], a[92:95], a[132:135], v[244:247]\n"
"ds_read_b128 a[132:135], v5 offset:25600\n"
"v_and_b32 v16, 0x30, v16\n"
"v_mfma_f32_16x16x32_bf16 v[244:247], a[88:91], a[128:131], v[244:247]\n"
"ds_read_b128 a[128:131], v5 offset:24576\n"
"buffer_load_dwordx2 v[18:19], v16, s[44:47], s61 offen offset:64\n"
"v_mfma_f32_16x16x32_bf16 v[248:251], a[88:91], a[136:139], v[248:251]\n"
"s_getpc_b64 s[96:97]\n"
"L_c6_:\n"
"s_add_u32 s96, s96, L_last-L_c6_\n"
"s_addc_u32 s97, s97, 0\n"
"v_mfma_f32_16x16x32_bf16 v[248:251], a[92:95], a[140:143], v[248:251]\n"
"s_lshr_b32 s66, %26, 3\n"
"v_mfma_f32_16x16x32_bf16 v[220:223], a[84:87], a[140:143], v[220:223]\n"
"s_lshl2_add_u32 s66, s66, %16\n"
"ds_read_b128 a[140:143], v5 offset:27648\n"
"v_mfma_f32_16x16x32_bf16 v[220:223], a[80:83], a[136:139], v[220:223]\n"
"ds_read_b128 a[136:139], v5 offset:26624\n"
"v_mfma_f32_16x16x32_bf16 v[224:227], a[80:83], a[144:147], v[224:227]\n"
"s_mul_i32 s66, s66, 256\n"
"v_mfma_f32_16x16x32_bf16 v[224:227], a[84:87], a[148:151], v[224:227]\n"
"global_load_dwordx4 a[76:79], v0, s[34:35] offset:-1024 sc1\n"
"v_mfma_f32_16x16x32_bf16 v[252:255], a[92:95], a[148:151], v[252:255]\n"
"ds_read_b128 a[148:151], v5 offset:29696\n"
"v_mfma_f32_16x16x32_bf16 v[252:255], a[88:91], a[144:147], v[252:255]\n"
"s_min_u32 s66, s66, L_headF-L_last-256\n"
"ds_read_b128 a[144:147], v5 offset:28672\n"
"v_mfma_f32_16x16x32_bf16 a[28:31], a[88:91], a[152:155], a[28:31]\n"
"v_mfma_f32_16x16x32_bf16 a[28:31], a[92:95], a[156:159], a[28:31]\n"
"v_min_u32 v11, 16, v0\n"
"v_mfma_f32_16x16x32_bf16 a[24:27], a[84:87], a[156:159], a[24:27]\n"
"ds_read_b128 a[156:159], v5 offset:31744\n"
"v_mfma_f32_16x16x32_bf16 a[24:27], a[80:83], a[152:155], a[24:27]\n"
"s_waitcnt vmcnt(12) lgkmcnt(11)\n"
"ds_read_b128 a[152:155], v5 offset:30720\n"
"v_mfma_f32_16x16x32_bf16 v[32:35], a[32:35], a[96:99], v[32:35]\n"
"buffer_load_dwordx4 v1, s[48:51], s69 offen offset:1024 sc1 lds\n"
"v_mfma_f32_16x16x32_bf16 v[32:35], a[36:39], a[100:103], v[32:35]\n"
"global_load_dwordx4 a[80:83], v0, s[34:35] offset:0 sc1\n"
"v_mfma_f32_16x16x32_bf16 v[60:63], a[44:47], a[100:103], v[60:63]\n"
"v_lshl_add_u32 v11, v11, 3, s66\n"
"v_mfma_f32_16x16x32_bf16 v[60:63], a[40:43], a[96:99], v[60:63]\n"
"global_load_dword v10, v11, s[96:97]\n"
"v_mfma_f32_16x16x32_bf16 v[64:67], a[40:43], a[104:107], v[64:67]\n"
"v_mfma_f32_16x16x32_bf16 v[64:67], a[44:47], a[108:111], v[64:67]\n"
"v_mfma_f32_16x16x32_bf16 v[36:39], a[36:39], a[108:111], v[36:39]\n"
"v_mfma_f32_16x16x32_bf16 v[36:39], a[32:35], a[104:107], v[36:39]\n"
"s_waitcnt lgkmcnt(8)\n"
"v_mfma_f32_16x16x32_bf16 v[40:43], a[32:35], a[112:115], v[40:43]\n"
"v_mfma_f32_16x16x32_bf16 v[40:43], a[36:39], a[116:119], v[40:43]\n"
"global_load_dwordx4 a[84:87], v0, s[34:35] offset:1024 sc1\n"
"v_mfma_f32_16x16x32_bf16 v[68:71], a[44:47], a[116:119], v[68:71]\n"
"v_mfma_f32_16x16x32_bf16 v[68:71], a[40:43], a[112:115], v[68:71]\n"
"v_mfma_f32_16x16x32_bf16 v[72:75], a[40:43], a[120:123], v[72:75]\n"
"v_mfma_f32_16x16x32_bf16 v[72:75], a[44:47], a[124:127], v[72:75]\n"
"v_mfma_f32_16x16x32_bf16 v[44:47], a[36:39], a[124:127], v[44:47]\n"
"buffer_load_dwordx4 v1, s[48:51], s69 offen offset:2048 sc1 lds\n"
"v_mfma_f32_16x16x32_bf16 v[44:47], a[32:35], a[120:123], v[44:47]\n"
"s_waitcnt lgkmcnt(4)\n"
"v_mfma_f32_16x16x32_bf16 v[48:51], a[32:35], a[128:131], v[48:51]\n"
"v_mfma_f32_16x16x32_bf16 v[48:51], a[36:39], a[132:135], v[48:51]\n"
"global_load_dwordx4 a[88:91], v0, s[34:35] offset:2048 sc1\n"
"v_mfma_f32_16x16x32_bf16 v[76:79], a[44:47], a[132:135], v[76:79]\n"
"v_mfma_f32_16x16x32_bf16 v[76:79], a[40:43], a[128:131], v[76:79]\n"
"v_mfma_f32_16x16x32_bf16 v[80:83], a[40:43], a[136:139], v[80:83]\n"
"v_mfma_f32_16x16x32_bf16 v[80:83], a[44:47], a[140:143], v[80:83]\n"
"v_mfma_f32_16x16x32_bf16 v[52:55], a[36:39], a[140:143], v[52:55]\n"
"v_mfma_f32_16x16x32_bf16 v[52:55], a[32:35], a[136:139], v[52:55]\n"
"s_waitcnt lgkmcnt(0)\n"
"v_mfma_f32_16x16x32_bf16 v[56:59], a[32:35], a[144:147], v[56:59]\n"
"v_mfma_f32_16x16x32_bf16 v[56:59], a[36:39], a[148:151], v[56:59]\n"
"global_load_dwordx4 a[92:95], v0, s[34:35] offset:3072 sc1\n"
"v_mfma_f32_16x16x32_bf16 v[84:87], a[44:47], a[148:151], v[84:87]\n"
"v_mfma_f32_16x16x32_bf16 v[84:87], a[40:43], a[144:147], v[84:87]\n"
"v_mfma_f32_16x16x32_bf16 a[4:7], a[40:43], a[152:155], a[4:7]\n"
"s_add_u32 s34, s34, 8192\n"
"v_mfma_f32_16x16x32_bf16 a[4:7], a[44:47], a[156:159], a[4:7]\n"
"buffer_load_dwordx4 v1, s[48:51], s69 offen offset:3072 sc1 lds\n"
"v_mfma_f32_16x16x32_bf16 a[0:3], a[36:39], a[156:159], a[0:3]\n"
"v_mfma_f32_16x16x32_bf16 a[0:3], a[32:35], a[152:155], a[0:3]\n"
"s_waitcnt vmcnt(14)\n"
"v_mfma_f32_16x16x32_bf16 v[88:91], a[48:51], a[96:99], v[88:91]\n"
"v_mfma_f32_16x16x32_bf16 v[88:91], a[52:55], a[100:103], v[88:91]\n"
"global_load_dwordx4 a[32:35], v0, s[34:35] offset:-4096 sc1\n"
"v_mfma_f32_16x16x32_bf16 v[116:119], a[60:63], a[100:103], v[116:119]\n"
"v_mfma_f32_16x16x32_bf16 v[116:119], a[56:59], a[96:99], v[116:119]\n"
"v_mfma_f32_16x16x32_bf16 v[120:123], a[56:59], a[104:107], v[120:123]\n"
"v_mfma_f32_16x16x32_bf16 v[120:123], a[60:63], a[108:111], v[120:123]\n"
"v_mfma_f32_16x16x32_bf16 v[92:95], a[52:55], a[108:111], v[92:95]\n"
"v_mfma_f32_16x16x32_bf16 v[92:95], a[48:51], a[104:107], v[92:95]\n"
"v_mfma_f32_16x16x32_bf16 v[96:99], a[48:51], a[112:115], v[96:99]\n"
"v_mfma_f32_16x16x32_bf16 v[96:99], a[52:55], a[116:119], v[96:99]\n"
"global_load_dwordx4 a[36:39], v0, s[34:35] offset:-3072 sc1\n"
"v_mfma_f32_16x16x32_bf16 v[124:127], a[60:63], a[116:119], v[124:127]\n"
"v_mfma_f32_16x16x32_bf16 v[124:127], a[56:59], a[112:115], v[124:127]\n"
"v_mfma_f32_16x16x32_bf16 v[128:131], a[56:59], a[120:123], v[128:131]\n"
"v_mfma_f32_16x16x32_bf16 v[128:131], a[60:63], a[124:127], v[128:131]\n"
"v_mfma_f32_16x16x32_bf16 v[100:103], a[52:55], a[124:127], v[100:103]\n"
"v_mfma_f32_16x16x32_bf16 v[100:103], a[48:51], a[120:123], v[100:103]\n"
"v_mfma_f32_16x16x32_bf16 v[104:107], a[48:51], a[128:131], v[104:107]\n"
"buffer_store_dwordx4 v[24:27], v1, s[36:39], s28 offen offset:0 sc1\n"
"buffer_load_dwordx2 v[8:9], off, s[56:59], %14 sc1\n"
"v_mfma_f32_16x16x32_bf16 v[104:107], a[52:55], a[132:135], v[104:107]\n"
"global_load_dwordx4 a[40:43], v0, s[34:35] offset:-2048 sc1\n"
"v_mfma_f32_16x16x32_bf16 v[132:135], a[60:63], a[132:135], v[132:135]\n"
"v_mfma_f32_16x16x32_bf16 v[132:135], a[56:59], a[128:131], v[132:135]\n"
"v_mfma_f32_16x16x32_bf16 v[136:139], a[56:59], a[136:139], v[136:139]\n"
"v_mfma_f32_16x16x32_bf16 v[136:139], a[60:63], a[140:143], v[136:139]\n"
"v_mfma_f32_16x16x32_bf16 v[108:111], a[52:55], a[140:143], v[108:111]\n"
"v_mfma_f32_16x16x32_bf16 v[108:111], a[48:51], a[136:139], v[108:111]\n"
"v_mfma_f32_16x16x32_bf16 v[112:115], a[48:51], a[144:147], v[112:115]\n"
"v_mfma_f32_16x16x32_bf16 v[112:115], a[52:55], a[148:151], v[112:115]\n"
"global_load_dwordx4 a[44:47], v0, s[34:35] offset:-1024 sc1\n"
"v_mfma_f32_16x16x32_bf16 v[140:143], a[60:63], a[148:151], v[140:143]\n"
"v_mfma_f32_16x16x32_bf16 v[140:143], a[56:59], a[144:147], v[140:143]\n"
"v_mfma_f32_16x16x32_bf16 a[12:15], a[56:59], a[152:155], a[12:15]\n"
"v_mfma_f32_16x16x32_bf16 a[12:15], a[60:63], a[156:159], a[12:15]\n"
"v_mfma_f32_16x16x32_bf16 a[8:11], a[52:55], a[156:159], a[8:11]\n"
"v_mfma_f32_16x16x32_bf16 a[8:11], a[48:51], a[152:155], a[8:11]\n"
"s_waitcnt vmcnt(14)\n"
"v_mfma_f32_16x16x32_bf16 v[144:147], a[64:67], a[96:99], v[144:147]\n"
"v_mfma_f32_16x16x32_bf16 v[144:147], a[68:71], a[100:103], v[144:147]\n"
"global_load_dwordx4 a[48:51], v0, s[34:35] offset:0 sc1\n"
"v_mfma_f32_16x16x32_bf16 v[172:175], a[76:79], a[100:103], v[172:175]\n"
"v_mfma_f32_16x16x32_bf16 v[172:175], a[72:75], a[96:99], v[172:175]\n"
"v_mfma_f32_16x16x32_bf16 v[176:179], a[72:75], a[104:107], v[176:179]\n"
"v_mfma_f32_16x16x32_bf16 v[176:179], a[76:79], a[108:111], v[176:179]\n"
"v_mfma_f32_16x16x32_bf16 v[148:151], a[68:71], a[108:111], v[148:151]\n"
"v_mfma_f32_16x16x32_bf16 v[148:151], a[64:67], a[104:107], v[148:151]\n"
"v_mfma_f32_16x16x32_bf16 v[152:155], a[64:67], a[112:115], v[152:155]\n"
"v_lshlrev_b32 v20, 16, v22\n"
"v_mfma_f32_16x16x32_bf16 v[152:155], a[68:71], a[116:119], v[152:155]\n"
"global_load_dwordx4 a[52:55], v0, s[34:35] offset:1024 sc1\n"
"v_and_b32 v21, s63, v22\n"
"v_mfma_f32_16x16x32_bf16 v[180:183], a[76:79], a[116:119], v[180:183]\n"
"v_lshlrev_b32 v22, 16, v23\n"
"v_mfma_f32_16x16x32_bf16 v[180:183], a[72:75], a[112:115], v[180:183]\n"
"v_and_b32 v23, s63, v23\n"
"v_mfma_f32_16x16x32_bf16 v[184:187], a[72:75], a[120:123], v[184:187]\n"
"v_add_f32 v60, v60, v20\n"
"v_mfma_f32_16x16x32_bf16 v[184:187], a[76:79], a[124:127], v[184:187]\n"
"v_add_f32 v61, v61, v21\n"
"v_mfma_f32_16x16x32_bf16 v[156:159], a[68:71], a[124:127], v[156:159]\n"
"buffer_store_dwordx4 v[28:31], v1, s[36:39], s28 offen offset:2048 sc1\n"
"v_add_f32 v62, v62, v22\n"
"v_add_f32 v63, v63, v23\n"
"v_add_f32 v64, v64, v20\n"
"v_mfma_f32_16x16x32_bf16 v[156:159], a[64:67], a[120:123], v[156:159]\n"
"v_add_f32 v65, v65, v21\n"
"v_mfma_f32_16x16x32_bf16 v[160:163], a[64:67], a[128:131], v[160:163]\n"
"v_add_f32 v66, v66, v22\n"
"v_mfma_f32_16x16x32_bf16 v[160:163], a[68:71], a[132:135], v[160:163]\n"
"global_load_dwordx4 a[56:59], v0, s[34:35] offset:2048 sc1\n"
"v_mfma_f32_16x16x32_bf16 v[188:191], a[76:79], a[132:135], v[188:191]\n"
"v_add_f32 v67, v67, v23\n"
"v_mfma_f32_16x16x32_bf16 v[188:191], a[72:75], a[128:131], v[188:191]\n"
"v_add_f32 v68, v68, v20\n"
"v_mfma_f32_16x16x32_bf16 v[192:195], a[72:75], a[136:139], v[192:195]\n"
"v_add_f32 v69, v69, v21\n"
"v_mfma_f32_16x16x32_bf16 v[192:195], a[76:79], a[140:143], v[192:195]\n"
"v_add_f32 v70, v70, v22\n"
"v_mfma_f32_16x16x32_bf16 v[164:167], a[68:71], a[140:143], v[164:167]\n"
"v_add_f32 v71, v71, v23\n"
"v_mfma_f32_16x16x32_bf16 v[164:167], a[64:67], a[136:139], v[164:167]\n"
"v_add_f32 v72, v72, v20\n"
"v_add_f32 v73, v73, v21\n"
"v_mfma_f32_16x16x32_bf16 v[168:171], a[64:67], a[144:147], v[168:171]\n"
"v_add_f32 v74, v74, v22\n"
"v_mfma_f32_16x16x32_bf16 v[168:171], a[68:71], a[148:151], v[168:171]\n"
"global_load_dwordx4 a[60:63], v0, s[34:35] offset:3072 sc1\n"
"v_add_f32 v75, v75, v23\n"
"v_mfma_f32_16x16x32_bf16 v[196:199], a[76:79], a[148:151], v[196:199]\n"
"v_add_f32 v76, v76, v20\n"
"v_mfma_f32_16x16x32_bf16 v[196:199], a[72:75], a[144:147], v[196:199]\n"
"v_add_f32 v77, v77, v21\n"
"v_mfma_f32_16x16x32_bf16 a[20:23], a[72:75], a[152:155], a[20:23]\n"
"s_add_u32 s34, s34, 8192\n"
"v_add_f32 v78, v78, v22\n"
"v_mfma_f32_16x16x32_bf16 a[20:23], a[76:79], a[156:159], a[20:23]\n"
"v_add_f32 v79, v79, v23\n"
"v_mfma_f32_16x16x32_bf16 a[16:19], a[68:71], a[156:159], a[16:19]\n"
"v_add_f32 v80, v80, v20\n"
"v_mfma_f32_16x16x32_bf16 a[16:19], a[64:67], a[152:155], a[16:19]\n"
"s_waitcnt vmcnt(12)\n"
"v_add_f32 v81, v81, v21\n"
"v_mfma_f32_16x16x32_bf16 v[200:203], a[80:83], a[96:99], v[200:203]\n"
"v_add_f32 v82, v82, v22\n"
"v_add_f32 v83, v83, v23\n"
"v_mfma_f32_16x16x32_bf16 v[200:203], a[84:87], a[100:103], v[200:203]\n"
"global_load_dwordx4 a[64:67], v0, s[34:35] offset:-4096 sc1\n"
"v_add_f32 v84, v84, v20\n"
"v_mfma_f32_16x16x32_bf16 v[228:231], a[92:95], a[100:103], v[228:231]\n"
"s_waitcnt vmcnt(12)\n"
"ds_read_b128 a[100:103], v2 offset:1024\n"
"v_add_f32 v85, v85, v21\n"
"v_mfma_f32_16x16x32_bf16 v[228:231], a[88:91], a[96:99], v[228:231]\n"
"ds_read_b128 a[96:99], v2 offset:0\n"
"v_add_f32 v86, v86, v22\n"
"v_mfma_f32_16x16x32_bf16 v[232:235], a[88:91], a[104:107], v[232:235]\n"
"v_add_f32 v87, v87, v23\n"
"v_mfma_f32_16x16x32_bf16 v[232:235], a[92:95], a[108:111], v[232:235]\n"
"v_accvgpr_read_b32 v12, a4\n"
"v_mfma_f32_16x16x32_bf16 v[204:207], a[84:87], a[108:111], v[204:207]\n"
"ds_read_b128 a[108:111], v2 offset:3072\n"
"v_accvgpr_read_b32 v13, a5\n"
"v_mfma_f32_16x16x32_bf16 v[204:207], a[80:83], a[104:107], v[204:207]\n"
"ds_read_b128 a[104:107], v2 offset:2048\n"
"v_accvgpr_read_b32 v14, a6\n"
"v_mfma_f32_16x16x32_bf16 v[208:211], a[80:83], a[112:115], v[208:211]\n"
"v_accvgpr_read_b32 v15, a7\n"
"v_mfma_f32_16x16x32_bf16 v[208:211], a[84:87], a[116:119], v[208:211]\n"
"global_load_dwordx4 a[68:71], v0, s[34:35] offset:-3072 sc1\n"
"v_add_f32 v12, v12, v20\n"
"v_mfma_f32_16x16x32_bf16 v[236:239], a[92:95], a[116:119], v[236:239]\n"
"ds_read_b128 a[116:119], v2 offset:5120\n"
"v_add_f32 v13, v13, v21\n"
"v_mfma_f32_16x16x32_bf16 v[236:239], a[88:91], a[112:115], v[236:239]\n"
"ds_read_b128 a[112:115], v2 offset:4096\n"
"v_add_f32 v14, v14, v22\n"
"v_mfma_f32_16x16x32_bf16 v[240:243], a[88:91], a[120:123], v[240:243]\n"
"v_add_f32 v15, v15, v23\n"
"v_accvgpr_write_b32 a4, v12\n"
"v_mfma_f32_16x16x32_bf16 v[240:243], a[92:95], a[124:127], v[240:243]\n"
"v_accvgpr_write_b32 a5, v13\n"
"v_mfma_f32_16x16x32_bf16 v[212:215], a[84:87], a[124:127], v[212:215]\n"
"ds_read_b128 a[124:127], v2 offset:7168\n"
"v_accvgpr_write_b32 a6, v14\n"
"v_mfma_f32_16x16x32_bf16 v[212:215], a[80:83], a[120:123], v[212:215]\n"
"ds_read_b128 a[120:123], v2 offset:6144\n"
"v_accvgpr_write_b32 a7, v15\n"
"v_mfma_f32_16x16x32_bf16 v[216:219], a[80:83], a[128:131], v[216:219]\n"
"s_add_u32 s28, s28, 0x40000\n"
"v_mfma_f32_16x16x32_bf16 v[216:219], a[84:87], a[132:135], v[216:219]\n"
"L_st9091:\n"
"s_cbranch_execz L_retB\n"
"global_load_dwordx4 a[72:75], v0, s[34:35] offset:-2048 sc1\n"
"v_lshrrev_b32 v20, 4, v0\n"
"v_mfma_f32_16x16x32_bf16 v[244:247], a[92:95], a[132:135], v[244:247]\n"
"s_add_u32 s67, s78, s62\n"
"ds_read_b128 a[132:135], v2 offset:9216\n"
"v_and_b32 v20, 0x30, v20\n"
"v_mfma_f32_16x16x32_bf16 v[244:247], a[88:91], a[128:131], v[244:247]\n"
"ds_read_b128 a[128:131], v2 offset:8192\n"
"buffer_load_dwordx2 v[22:23], v20, s[44:47], s61 offen offset:72\n"
"v_mfma_f32_16x16x32_bf16 v[248:251], a[88:91], a[136:139], v[248:251]\n"
"s_add_u32 s67, s67, 1\n"
"v_mfma_f32_16x16x32_bf16 v[248:251], a[92:95], a[140:143], v[248:251]\n"
"v_mfma_f32_16x16x32_bf16 v[220:223], a[84:87], a[140:143], v[220:223]\n"
"ds_read_b128 a[140:143], v2 offset:11264\n"
"v_mov_b32 v24, s67\n"
"v_mfma_f32_16x16x32_bf16 v[220:223], a[80:83], a[136:139], v[220:223]\n"
"ds_read_b128 a[136:139], v2 offset:10240\n"
"v_mov_b32 v25, s79\n"
"v_mfma_f32_16x16x32_bf16 v[224:227], a[80:83], a[144:147], v[224:227]\n"
"v_mfma_f32_16x16x32_bf16 v[224:227], a[84:87], a[148:151], v[224:227]\n"
"s_waitcnt vmcnt(6)\n"
"global_load_dwordx4 a[76:79], v0, s[34:35] offset:-1024 sc1\n"
"buffer_store_dwordx2 v[24:25], off, s[56:59], %24 sc1\n"
"v_mfma_f32_16x16x32_bf16 v[252:255], a[92:95], a[148:151], v[252:255]\n"
"ds_read_b128 a[148:151], v2 offset:13312\n"
"v_mfma_f32_16x16x32_bf16 v[252:255], a[88:91], a[144:147], v[252:255]\n"
"ds_read_b128 a[144:147], v2 offset:12288\n"
"v_mfma_f32_16x16x32_bf16 a[28:31], a[88:91], a[152:155], a[28:31]\n"
"v_mfma_f32_16x16x32_bf16 a[28:31], a[92:95], a[156:159], a[28:31]\n"
"v_mfma_f32_16x16x32_bf16 a[24:27], a[84:87], a[156:159], a[24:27]\n"
"s_cmp_eq_u32 s62, 5\n"
"ds_read_b128 a[156:159], v2 offset:15360\n"
"v_mfma_f32_16x16x32_bf16 a[24:27], a[80:83], a[152:155], a[24:27]\n"
"ds_read_b128 a[152:155], v2 offset:14336\n"
"s_cbranch_scc1 L_last\n"
"s_branch L_P4\n"
"L_last:\n"
"L_st9128:\n"
"s_cbranch_execz L_retB\n"
"s_waitcnt vmcnt(9) lgkmcnt(12)\n"
"v_mfma_f32_16x16x32_bf16 v[32:35], a[32:35], a[96:99], v[32:35]\n"
"s_add_u32 s34, s36, 1454080\n"
"s_add_u32 s48, s48, 131072\n"
"s_addc_u32 s49, s49, 0\n"
"v_mfma_f32_16x16x32_bf16 v[32:35], a[36:39], a[100:103], v[32:35]\n"
"s_mov_b64 exec, 0\n"
"s_cmp_eq_u32 %16, 0\n"
"s_cbranch_scc1 L_st9670\n"
"s_cmp_eq_u32 %16, 1\n"
"s_cbranch_scc1 L_st10278\n"
"s_cmp_eq_u32 %16, 2\n"
"s_cbranch_scc1 L_st10888\n"
"s_branch L_st11498\n"
"L_retC:\n"
"s_mov_b64 exec, -1\n"
"s_nop 3\n"
"global_load_dwordx4 a[80:83], v0, s[34:35] offset:0 sc1\n"
"v_mfma_f32_16x16x32_bf16 v[60:63], a[44:47], a[100:103], v[60:63]\n"
"s_barrier\n"
"v_mfma_f32_16x16x32_bf16 v[60:63], a[40:43], a[96:99], v[60:63]\n"
"v_mfma_f32_16x16x32_bf16 v[64:67], a[40:43], a[104:107], v[64:67]\n"
"v_mfma_f32_16x16x32_bf16 v[64:67], a[44:47], a[108:111], v[64:67]\n"
"v_mfma_f32_16x16x32_bf16 v[36:39], a[36:39], a[108:111], v[36:39]\n"
"v_subrev_u32 v8, s65, v8\n"
"v_mfma_f32_16x16x32_bf16 v[36:39], a[32:35], a[104:107], v[36:39]\n"
"s_waitcnt lgkmcnt(8)\n"
"v_xor_b32 v9, s79, v9\n"
"v_mfma_f32_16x16x32_bf16 v[40:43], a[32:35], a[112:115], v[40:43]\n"
"v_or_b32 v8, v8, v9\n"
"v_mfma_f32_16x16x32_bf16 v[40:43], a[36:39], a[116:119], v[40:43]\n"
"global_load_dwordx4 a[84:87], v0, s[34:35] offset:1024 sc1\n"
"v_cmp_lt_u32 vcc, 15, v8\n"
"v_mfma_f32_16x16x32_bf16 v[68:71], a[44:47], a[116:119], v[68:71]\n"
"s_cbranch_vccnz L_slow4\n"
"L_back4:\n"
"v_mfma_f32_16x16x32_bf16 v[68:71], a[40:43], a[112:115], v[68:71]\n"
"v_mfma_f32_16x16x32_bf16 v[72:75], a[40:43], a[120:123], v[72:75]\n"
"v_mfma_f32_16x16x32_bf16 v[72:75], a[44:47], a[124:127], v[72:75]\n"
"v_mfma_f32_16x16x32_bf16 v[44:47], a[36:39], a[124:127], v[44:47]\n"
"v_mfma_f32_16x16x32_bf16 v[44:47], a[32:35], a[120:123], v[44:47]\n"
"s_add_u32 m0, s64, 16384\n"
"s_waitcnt lgkmcnt(4)\n"
"v_mfma_f32_16x16x32_bf16 v[48:51], a[32:35], a[128:131], v[48:51]\n"
"v_mfma_f32_16x16x32_bf16 v[48:51], a[36:39], a[132:135], v[48:51]\n"
"global_load_dwordx4 a[88:91], v0, s[34:35] offset:2048 sc1\n"
"v_mfma_f32_16x16x32_bf16 v[76:79], a[44:47], a[132:135], v[76:79]\n"
"v_mfma_f32_16x16x32_bf16 v[76:79], a[40:43], a[128:131], v[76:79]\n"
"v_mfma_f32_16x16x32_bf16 v[80:83], a[40:43], a[136:139], v[80:83]\n"
"s_mov_b32 s97, s37\n"
"v_mfma_f32_16x16x32_bf16 v[80:83], a[44:47], a[140:143], v[80:83]\n"
"v_mfma_f32_16x16x32_bf16 v[52:55], a[36:39], a[140:143], v[52:55]\n"
"v_mfma_f32_16x16x32_bf16 v[52:55], a[32:35], a[136:139], v[52:55]\n"
"s_waitcnt lgkmcnt(0)\n"
"v_mfma_f32_16x16x32_bf16 v[56:59], a[32:35], a[144:147], v[56:59]\n"
"buffer_load_dwordx4 v0, s[48:51], s75 offen offset:0 sc1 lds\n"
"v_mfma_f32_16x16x32_bf16 v[56:59], a[36:39], a[148:151], v[56:59]\n"
"global_load_dwordx4 a[92:95], v0, s[34:35] offset:3072 sc1\n"
"v_mfma_f32_16x16x32_bf16 v[84:87], a[44:47], a[148:151], v[84:87]\n"
"v_mfma_f32_16x16x32_bf16 v[84:87], a[40:43], a[144:147], v[84:87]\n"
"v_mfma_f32_16x16x32_bf16 a[4:7], a[40:43], a[152:155], a[4:7]\n"
"v_mfma_f32_16x16x32_bf16 a[4:7], a[44:47], a[156:159], a[4:7]\n"
"v_mfma_f32_16x16x32_bf16 a[0:3], a[36:39], a[156:159], a[0:3]\n"
"v_mfma_f32_16x16x32_bf16 a[0:3], a[32:35], a[152:155], a[0:3]\n"
"s_waitcnt vmcnt(10)\n"
"v_mfma_f32_16x16x32_bf16 v[88:91], a[48:51], a[96:99], v[88:91]\n"
"s_add_u32 s34, s36, 1458176\n"
"v_mfma_f32_16x16x32_bf16 v[88:91], a[52:55], a[100:103], v[88:91]\n"
"global_load_dwordx4 a[32:35], v0, s[34:35] offset:0 sc1\n"
"v_mfma_f32_16x16x32_bf16 v[116:119], a[60:63], a[100:103], v[116:119]\n"
"buffer_load_dwordx4 v0, s[48:51], s75 offen offset:1024 sc1 lds\n"
"v_mfma_f32_16x16x32_bf16 v[116:119], a[56:59], a[96:99], v[116:119]\n"
"v_mfma_f32_16x16x32_bf16 v[120:123], a[56:59], a[104:107], v[120:123]\n"
"v_mfma_f32_16x16x32_bf16 v[120:123], a[60:63], a[108:111], v[120:123]\n"
"v_mfma_f32_16x16x32_bf16 v[92:95], a[52:55], a[108:111], v[92:95]\n"
"v_mfma_f32_16x16x32_bf16 v[92:95], a[48:51], a[104:107], v[92:95]\n"
"v_mfma_f32_16x16x32_bf16 v[96:99], a[48:51], a[112:115], v[96:99]\n"
"v_mfma_f32_16x16x32_bf16 v[96:99], a[52:55], a[116:119], v[96:99]\n"
"global_load_dwordx4 a[36:39], v0, s[34:35] offset:1024 sc1\n"
"v_mfma_f32_16x16x32_bf16 v[124:127], a[60:63], a[116:119], v[124:127]\n"
"v_mfma_f32_16x16x32_bf16 v[124:127], a[56:59], a[112:115], v[124:127]\n"
"v_mfma_f32_16x16x32_bf16 v[128:131], a[56:59], a[120:123], v[128:131]\n"
"buffer_load_dwordx4 v0, s[48:51], s75 offen offset:2048 sc1 lds\n"
"v_mfma_f32_16x16x32_bf16 v[128:131], a[60:63], a[124:127], v[128:131]\n"
"v_mfma_f32_16x16x32_bf16 v[100:103], a[52:55], a[124:127], v[100:103]\n"
"v_mfma_f32_16x16x32_bf16 v[100:103], a[48:51], a[120:123], v[100:103]\n"
"v_mfma_f32_16x16x32_bf16 v[104:107], a[48:51], a[128:131], v[104:107]\n"
"v_mfma_f32_16x16x32_bf16 v[104:107], a[52:55], a[132:135], v[104:107]\n"
"global_load_dwordx4 a[40:43], v0, s[34:35] offset:2048 sc1\n"
"v_mfma_f32_16x16x32_bf16 v[132:135], a[60:63], a[132:135], v[132:135]\n"
"v_mfma_f32_16x16x32_bf16 v[132:135], a[56:59], a[128:131], v[132:135]\n"
"v_mfma_f32_16x16x32_bf16 v[136:139], a[56:59], a[136:139], v[136:139]\n"
"v_mfma_f32_16x16x32_bf16 v[136:139], a[60:63], a[140:143], v[136:139]\n"
"buffer_load_dwordx4 v0, s[48:51], s75 offen offset:3072 sc1 lds\n"
"v_mfma_f32_16x16x32_bf16 v[108:111], a[52:55], a[140:143], v[108:111]\n"
"v_mfma_f32_16x16x32_bf16 v[108:111], a[48:51], a[136:139], v[108:111]\n"
"v_mfma_f32_16x16x32_bf16 v[112:115], a[48:51], a[144:147], v[112:115]\n"
"v_mfma_f32_16x16x32_bf16 v[112:115], a[52:55], a[148:151], v[112:115]\n"
"global_load_dwordx4 a[44:47], v0, s[34:35] offset:3072 sc1\n"
"v_mfma_f32_16x16x32_bf16 v[140:143], a[60:63], a[148:151], v[140:143]\n"
"v_mfma_f32_16x16x32_bf16 v[140:143], a[56:59], a[144:147], v[140:143]\n"
"v_mfma_f32_16x16x32_bf16 a[12:15], a[56:59], a[152:155], a[12:15]\n"
"v_mfma_f32_16x16x32_bf16 a[12:15], a[60:63], a[156:159], a[12:15]\n"
"v_mfma_f32_16x16x32_bf16 a[8:11], a[52:55], a[156:159], a[8:11]\n"
"v_mfma_f32_16x16x32_bf16 a[8:11], a[48:51], a[152:155], a[8:11]\n"
"s_add_u32 m0, s64, 20480\n"
"s_waitcnt vmcnt(12)\n"
"v_mfma_f32_16x16x32_bf16 v[144:147], a[64:67], a[96:99], v[144:147]\n"
"s_add_u32 s34, s36, 1462272\n"
"v_mfma_f32_16x16x32_bf16 v[144:147], a[68:71], a[100:103], v[144:147]\n"
"global_load_dwordx4 a[48:51], v0, s[34:35] offset:0 sc1\n"
"v_mfma_f32_16x16x32_bf16 v[172:175], a[76:79], a[100:103], v[172:175]\n"
"v_mfma_f32_16x16x32_bf16 v[172:175], a[72:75], a[96:99], v[172:175]\n"
"v_mfma_f32_16x16x32_bf16 v[176:179], a[72:75], a[104:107], v[176:179]\n"
"v_mfma_f32_16x16x32_bf16 v[176:179], a[76:79], a[108:111], v[176:179]\n"
"v_mfma_f32_16x16x32_bf16 v[148:151], a[68:71], a[108:111], v[148:151]\n"
"v_lshlrev_b32 v16, 16, v18\n"
"v_and_b32 v17, s63, v18\n"
"v_mfma_f32_16x16x32_bf16 v[148:151], a[64:67], a[104:107], v[148:151]\n"
"v_lshlrev_b32 v18, 16, v19\n"
"v_and_b32 v19, s63, v19\n"
"v_mfma_f32_16x16x32_bf16 v[152:155], a[64:67], a[112:115], v[152:155]\n"
"buffer_load_dwordx4 v1, s[48:51], s75 offen offset:0 sc1 lds\n"
"v_mfma_f32_16x16x32_bf16 v[152:155], a[68:71], a[116:119], v[152:155]\n"
"global_load_dwordx4 a[52:55], v0, s[34:35] offset:1024 sc1\n"
"v_add_f32 v88, v88, v16\n"
"v_mfma_f32_16x16x32_bf16 v[180:183], a[76:79], a[116:119], v[180:183]\n"
"v_add_f32 v89, v89, v17\n"
"v_add_f32 v90, v90, v18\n"
"v_mfma_f32_16x16x32_bf16 v[180:183], a[72:75], a[112:115], v[180:183]\n"
"v_add_f32 v91, v91, v19\n"
"v_add_f32 v92, v92, v16\n"
"v_mfma_f32_16x16x32_bf16 v[184:187], a[72:75], a[120:123], v[184:187]\n"
"v_add_f32 v93, v93, v17\n"
"v_add_f32 v94, v94, v18\n"
"v_mfma_f32_16x16x32_bf16 v[184:187], a[76:79], a[124:127], v[184:187]\n"
"v_add_f32 v95, v95, v19\n"
"v_add_f32 v96, v96, v16\n"
"v_mfma_f32_16x16x32_bf16 v[156:159], a[68:71], a[124:127], v[156:159]\n"
"v_add_f32 v97, v97, v17\n"
"v_add_f32 v98, v98, v18\n"
"v_mfma_f32_16x16x32_bf16 v[156:159], a[64:67], a[120:123], v[156:159]\n"
"v_add_f32 v99, v99, v19\n"
"v_add_f32 v100, v100, v16\n"
"v_mfma_f32_16x16x32_bf16 v[160:163], a[64:67], a[128:131], v[160:163]\n"
"v_add_f32 v101, v101, v17\n"
"v_add_f32 v102, v102, v18\n"
"v_mfma_f32_16x16x32_bf16 v[160:163], a[68:71], a[132:135], v[160:163]\n"
"global_load_dwordx4 a[56:59], v0, s[34:35] offset:2048 sc1\n"
"v_add_f32 v103, v103, v19\n"
"v_mfma_f32_16x16x32_bf16 v[188:191], a[76:79], a[132:135], v[188:191]\n"
"buffer_load_dwordx4 v1, s[48:51], s75 offen offset:1024 sc1 lds\n"
"v_mfma_f32_16x16x32_bf16 v[188:191], a[72:75], a[128:131], v[188:191]\n"
"v_add_f32 v104, v104, v16\n"
"v_add_f32 v105, v105, v17\n"
"v_mfma_f32_16x16x32_bf16 v[192:195], a[72:75], a[136:139], v[192:195]\n"
"v_add_f32 v106, v106, v18\n"
"v_add_f32 v107, v107, v19\n"
"v_mfma_f32_16x16x32_bf16 v[192:195], a[76:79], a[140:143], v[192:195]\n"
"v_add_f32 v108, v108, v16\n"
"v_add_f32 v109, v109, v17\n"
"v_mfma_f32_16x16x32_bf16 v[164:167], a[68:71], a[140:143], v[164:167]\n"
"v_add_f32 v110, v110, v18\n"
"v_add_f32 v111, v111, v19\n"
"v_mfma_f32_16x16x32_bf16 v[164:167], a[64:67], a[136:139], v[164:167]\n"
"v_add_f32 v112, v112, v16\n"
"v_add_f32 v113, v113, v17\n"
"v_mfma_f32_16x16x32_bf16 v[168:171], a[64:67], a[144:147], v[168:171]\n"
"v_add_f32 v114, v114, v18\n"
"v_add_f32 v115, v115, v19\n"
"v_mfma_f32_16x16x32_bf16 v[168:171], a[68:71], a[148:151], v[168:171]\n"
"global_load_dwordx4 a[60:63], v0, s[34:35] offset:3072 sc1\n"
"v_accvgpr_read_b32 v12, a8\n"
"v_mfma_f32_16x16x32_bf16 v[196:199], a[76:79], a[148:151], v[196:199]\n"
"v_accvgpr_read_b32 v13, a9\n"
"v_accvgpr_read_b32 v14, a10\n"
"v_mfma_f32_16x16x32_bf16 v[196:199], a[72:75], a[144:147], v[196:199]\n"
"buffer_load_dwordx4 v1, s[48:51], s75 offen offset:2048 sc1 lds\n"
"v_mfma_f32_16x16x32_bf16 a[20:23], a[72:75], a[152:155], a[20:23]\n"
"v_accvgpr_read_b32 v15, a11\n"
"v_add_f32 v12, v12, v16\n"
"v_mfma_f32_16x16x32_bf16 a[20:23], a[76:79], a[156:159], a[20:23]\n"
"v_add_f32 v13, v13, v17\n"
"v_add_f32 v14, v14, v18\n"
"v_mfma_f32_16x16x32_bf16 a[16:19], a[68:71], a[156:159], a[16:19]\n"
"v_add_f32 v15, v15, v19\n"
"v_accvgpr_write_b32 a8, v12\n"
"v_mfma_f32_16x16x32_bf16 a[16:19], a[64:67], a[152:155], a[16:19]\n"
"s_waitcnt vmcnt(14)\n"
"v_accvgpr_write_b32 a9, v13\n"
"v_accvgpr_write_b32 a10, v14\n"
"v_accvgpr_write_b32 a11, v15\n"
"v_mfma_f32_16x16x32_bf16 v[200:203], a[80:83], a[96:99], v[200:203]\n"
"s_add_u32 s34, s36, 1474560\n"
"v_lshlrev_b32 v20, 16, v22\n"
"v_and_b32 v21, s63, v22\n"
"v_mfma_f32_16x16x32_bf16 v[200:203], a[84:87], a[100:103], v[200:203]\n"
"global_load_dwordx4 a[64:67], v0, s[34:35] offset:0 sc1\n"
"v_lshlrev_b32 v22, 16, v23\n"
"v_mfma_f32_16x16x32_bf16 v[228:231], a[92:95], a[100:103], v[228:231]\n"
"ds_read_b128 a[100:103], v3 offset:1024\n"
"v_and_b32 v23, s63, v23\n"
"v_mfma_f32_16x16x32_bf16 v[228:231], a[88:91], a[96:99], v[228:231]\n"
"ds_read_b128 a[96:99], v3 offset:0\n"
"v_add_f32 v116, v116, v20\n"
"v_mfma_f32_16x16x32_bf16 v[232:235], a[88:91], a[104:107], v[232:235]\n"
"v_add_f32 v117, v117, v21\n"
"v_add_f32 v118, v118, v22\n"
"v_mfma_f32_16x16x32_bf16 v[232:235], a[92:95], a[108:111], v[232:235]\n"
"buffer_load_dwordx4 v1, s[48:51], s75 offen offset:3072 sc1 lds\n"
"v_mfma_f32_16x16x32_bf16 v[204:207], a[84:87], a[108:111], v[204:207]\n"
"ds_read_b128 a[108:111], v3 offset:3072\n"
"v_add_f32 v119, v119, v23\n"
"v_add_f32 v120, v120, v20\n"
"v_mfma_f32_16x16x32_bf16 v[204:207], a[80:83], a[104:107], v[204:207]\n"
"ds_read_b128 a[104:107], v3 offset:2048\n"
"v_add_f32 v121, v121, v21\n"
"v_mfma_f32_16x16x32_bf16 v[208:211], a[80:83], a[112:115], v[208:211]\n"
"v_add_f32 v122, v122, v22\n"
"v_add_f32 v123, v123, v23\n"
"v_mfma_f32_16x16x32_bf16 v[208:211], a[84:87], a[116:119], v[208:211]\n"
"global_load_dwordx4 a[68:71], v0, s[34:35] offset:1024 sc1\n"
"v_add_f32 v124, v124, v20\n"
"v_add_f32 v125, v125, v21\n"
"v_mfma_f32_16x16x32_bf16 v[236:239], a[92:95], a[116:119], v[236:239]\n"
"ds_read_b128 a[116:119], v3 offset:5120\n"
"v_add_f32 v126, v126, v22\n"
"v_mfma_f32_16x16x32_bf16 v[236:239], a[88:91], a[112:115], v[236:239]\n"
"ds_read_b128 a[112:115], v3 offset:4096\n"
"v_add_f32 v127, v127, v23\n"
"v_mfma_f32_16x16x32_bf16 v[240:243], a[88:91], a[120:123], v[240:243]\n"
"v_add_f32 v128, v128, v20\n"
"v_add_f32 v129, v129, v21\n"
"v_mfma_f32_16x16x32_bf16 v[240:243], a[92:95], a[124:127], v[240:243]\n"
"v_add_f32 v130, v130, v22\n"
"v_add_f32 v131, v131, v23\n"
"v_mfma_f32_16x16x32_bf16 v[212:215], a[84:87], a[124:127], v[212:215]\n"
"s_add_u32 m0, s64, 24576\n"
"ds_read_b128 a[124:127], v3 offset:7168\n"
"v_add_f32 v132, v132, v20\n"
"v_add_f32 v133, v133, v21\n"
"v_mfma_f32_16x16x32_bf16 v[212:215], a[80:83], a[120:123], v[212:215]\n"
"ds_read_b128 a[120:123], v3 offset:6144\n"
"v_add_f32 v134, v134, v22\n"
"v_mfma_f32_16x16x32_bf16 v[216:219], a[80:83], a[128:131], v[216:219]\n"
"v_add_f32 v135, v135, v23\n"
"v_add_f32 v136, v136, v20\n"
"v_mfma_f32_16x16x32_bf16 v[216:219], a[84:87], a[132:135], v[216:219]\n"
"global_load_dwordx4 a[72:75], v0, s[34:35] offset:2048 sc1\n"
"v_add_f32 v137, v137, v21\n"
"v_mfma_f32_16x16x32_bf16 v[244:247], a[92:95], a[132:135], v[244:247]\n"
"ds_read_b128 a[132:135], v3 offset:9216\n"
"v_add_f32 v138, v138, v22\n"
"v_add_f32 v139, v139, v23\n"
"v_mfma_f32_16x16x32_bf16 v[244:247], a[88:91], a[128:131], v[244:247]\n"
"ds_read_b128 a[128:131], v3 offset:8192\n"
"v_add_f32 v140, v140, v20\n"
"v_mfma_f32_16x16x32_bf16 v[248:251], a[88:91], a[136:139], v[248:251]\n"
"v_add_f32 v141, v141, v21\n"
"v_add_f32 v142, v142, v22\n"
"v_mfma_f32_16x16x32_bf16 v[248:251], a[92:95], a[140:143], v[248:251]\n"
"v_add_f32 v143, v143, v23\n"
"v_accvgpr_read_b32 v12, a12\n"
"v_mfma_f32_16x16x32_bf16 v[220:223], a[84:87], a[140:143], v[220:223]\n"
"ds_read_b128 a[140:143], v3 offset:11264\n"
"v_accvgpr_read_b32 v13, a13\n"
"v_mfma_f32_16x16x32_bf16 v[220:223], a[80:83], a[136:139], v[220:223]\n"
"ds_read_b128 a[136:139], v3 offset:10240\n"
"v_accvgpr_read_b32 v14, a14\n"
"v_accvgpr_read_b32 v15, a15\n"
"v_mfma_f32_16x16x32_bf16 v[224:227], a[80:83], a[144:147], v[224:227]\n"
"buffer_load_dwordx4 v0, s[48:51], s69 offen offset:0 sc1 lds\n"
"v_add_f32 v12, v12, v20\n"
"v_mfma_f32_16x16x32_bf16 v[224:227], a[84:87], a[148:151], v[224:227]\n"
"global_load_dwordx4 a[76:79], v0, s[34:35] offset:3072 sc1\n"
"v_add_f32 v13, v13, v21\n"
"v_add_f32 v14, v14, v22\n"
"v_mfma_f32_16x16x32_bf16 v[252:255], a[92:95], a[148:151], v[252:255]\n"
"ds_read_b128 a[148:151], v3 offset:13312\n"
"v_add_f32 v15, v15, v23\n"
"v_accvgpr_write_b32 a12, v12\n"
"v_mfma_f32_16x16x32_bf16 v[252:255], a[88:91], a[144:147], v[252:255]\n"
"ds_read_b128 a[144:147], v3 offset:12288\n"
"v_accvgpr_write_b32 a13, v13\n"
"v_mfma_f32_16x16x32_bf16 a[28:31], a[88:91], a[152:155], a[28:31]\n"
"v_accvgpr_write_b32 a14, v14\n"
"v_accvgpr_write_b32 a15, v15\n"
"v_mfma_f32_16x16x32_bf16 a[28:31], a[92:95], a[156:159], a[28:31]\n"
"v_mfma_f32_16x16x32_bf16 a[24:27], a[84:87], a[156:159], a[24:27]\n"
"ds_read_b128 a[156:159], v3 offset:15360\n"
"v_mfma_f32_16x16x32_bf16 a[24:27], a[80:83], a[152:155], a[24:27]\n"
"s_waitcnt vmcnt(13) lgkmcnt(11)\n"
"ds_read_b128 a[152:155], v3 offset:14336\n"
"v_mfma_f32_16x16x32_bf16 v[32:35], a[32:35], a[96:99], v[32:35]\n"
"s_add_u32 s34, s36, 1478656\n"
"v_mfma_f32_16x16x32_bf16 v[32:35], a[36:39], a[100:103], v[32:35]\n"
"global_load_dwordx4 a[80:83], v0, s[34:35] offset:0 sc1\n"
"v_mfma_f32_16x16x32_bf16 v[60:63], a[44:47], a[100:103], v[60:63]\n"
"buffer_load_dwordx4 v0, s[48:51], s69 offen offset:1024 sc1 lds\n"
"v_mfma_f32_16x16x32_bf16 v[60:63], a[40:43], a[96:99], v[60:63]\n"
"v_lshrrev_b32 v16, 4, v0\n"
"v_mfma_f32_16x16x32_bf16 v[64:67], a[40:43], a[104:107], v[64:67]\n"
"v_mfma_f32_16x16x32_bf16 v[64:67], a[44:47], a[108:111], v[64:67]\n"
"v_mfma_f32_16x16x32_bf16 v[36:39], a[36:39], a[108:111], v[36:39]\n"
"v_mfma_f32_16x16x32_bf16 v[36:39], a[32:35], a[104:107], v[36:39]\n"
"s_waitcnt lgkmcnt(8)\n"
"v_and_b32 v16, 0x30, v16\n"
"v_mfma_f32_16x16x32_bf16 v[40:43], a[32:35], a[112:115], v[40:43]\n"
"v_mfma_f32_16x16x32_bf16 v[40:43], a[36:39], a[116:119], v[40:43]\n"
"global_load_dwordx4 a[84:87], v0, s[34:35] offset:1024 sc1\n"
"v_mfma_f32_16x16x32_bf16 v[68:71], a[44:47], a[116:119], v[68:71]\n"
"buffer_load_dwordx2 v[18:19], v16, s[44:47], s61 offen offset:128\n"
"v_mfma_f32_16x16x32_bf16 v[68:71], a[40:43], a[112:115], v[68:71]\n"
"buffer_load_dwordx4 v0, s[48:51], s69 offen offset:2048 sc1 lds\n"
"v_mfma_f32_16x16x32_bf16 v[72:75], a[40:43], a[120:123], v[72:75]\n"
"v_mfma_f32_16x16x32_bf16 v[72:75], a[44:47], a[124:127], v[72:75]\n"
"v_lshrrev_b32 v20, 4, v0\n"
"v_mfma_f32_16x16x32_bf16 v[44:47], a[36:39], a[124:127], v[44:47]\n"
"v_mfma_f32_16x16x32_bf16 v[44:47], a[32:35], a[120:123], v[44:47]\n"
"s_waitcnt lgkmcnt(4)\n"
"v_mfma_f32_16x16x32_bf16 v[48:51], a[32:35], a[128:131], v[48:51]\n"
"v_and_b32 v20, 0x30, v20\n"
"v_mfma_f32_16x16x32_bf16 v[48:51], a[36:39], a[132:135], v[48:51]\n"
"global_load_dwordx4 a[88:91], v0, s[34:35] offset:2048 sc1\n"
"v_mfma_f32_16x16x32_bf16 v[76:79], a[44:47], a[132:135], v[76:79]\n"
"v_mfma_f32_16x16x32_bf16 v[76:79], a[40:43], a[128:131], v[76:79]\n"
"v_mfma_f32_16x16x32_bf16 v[80:83], a[40:43], a[136:139], v[80:83]\n"
"buffer_load_dwordx4 v0, s[48:51], s69 offen offset:3072 sc1 lds\n"
"v_mfma_f32_16x16x32_bf16 v[80:83], a[44:47], a[140:143], v[80:83]\n"
"buffer_load_dwordx2 v[22:23], v20, s[44:47], s61 offen offset:136\n"
"v_mfma_f32_16x16x32_bf16 v[52:55], a[36:39], a[140:143], v[52:55]\n"
"v_mfma_f32_16x16x32_bf16 v[52:55], a[32:35], a[136:139], v[52:55]\n"
"s_waitcnt lgkmcnt(0)\n"
"v_mfma_f32_16x16x32_bf16 v[56:59], a[32:35], a[144:147], v[56:59]\n"
"v_lshrrev_b32 v24, 4, v0\n"
"v_mfma_f32_16x16x32_bf16 v[56:59], a[36:39], a[148:151], v[56:59]\n"
"global_load_dwordx4 a[92:95], v0, s[34:35] offset:3072 sc1\n"
"v_mfma_f32_16x16x32_bf16 v[84:87], a[44:47], a[148:151], v[84:87]\n"
"v_mfma_f32_16x16x32_bf16 v[84:87], a[40:43], a[144:147], v[84:87]\n"
"v_and_b32 v24, 0x30, v24\n"
"v_mfma_f32_16x16x32_bf16 a[4:7], a[40:43], a[152:155], a[4:7]\n"
"v_mfma_f32_16x16x32_bf16 a[4:7], a[44:47], a[156:159], a[4:7]\n"
"v_mfma_f32_16x16x32_bf16 a[0:3], a[36:39], a[156:159], a[0:3]\n"
"s_add_u32 m0, s64, 28672\n"
"v_mfma_f32_16x16x32_bf16 a[0:3], a[32:35], a[152:155], a[0:3]\n"
"s_waitcnt vmcnt(16)\n"
"buffer_load_dwordx2 v[26:27], v24, s[44:47], s61 offen offset:192\n"
"v_mfma_f32_16x16x32_bf16 v[88:91], a[48:51], a[96:99], v[88:91]\n"
"s_add_u32 s34, s36, 1490944\n"
"v_lshrrev_b32 v28, 4, v0\n"
"v_mfma_f32_16x16x32_bf16 v[88:91], a[52:55], a[100:103], v[88:91]\n"
"global_load_dwordx4 a[32:35], v0, s[34:35] offset:0 sc1\n"
"v_mfma_f32_16x16x32_bf16 v[116:119], a[60:63], a[100:103], v[116:119]\n"
"ds_read_b128 a[100:103], v4 offset:1024\n"
"v_mfma_f32_16x16x32_bf16 v[116:119], a[56:59], a[96:99], v[116:119]\n"
"ds_read_b128 a[96:99], v4 offset:0\n"
"v_mfma_f32_16x16x32_bf16 v[120:123], a[56:59], a[104:107], v[120:123]\n"
"v_and_b32 v28, 0x30, v28\n"
"v_mfma_f32_16x16x32_bf16 v[120:123], a[60:63], a[108:111], v[120:123]\n"
"buffer_load_dwordx2 v[30:31], v28, s[44:47], s61 offen offset:200\n"
"v_mfma_f32_16x16x32_bf16 v[92:95], a[52:55], a[108:111], v[92:95]\n"
"ds_read_b128 a[108:111], v4 offset:3072\n"
"v_mfma_f32_16x16x32_bf16 v[92:95], a[48:51], a[104:107], v[92:95]\n"
"ds_read_b128 a[104:107], v4 offset:2048\n"
"v_mfma_f32_16x16x32_bf16 v[96:99], a[48:51], a[112:115], v[96:99]\n"
"buffer_load_dwordx4 v1, s[48:51], s69 offen offset:0 sc1 lds\n"
"v_mfma_f32_16x16x32_bf16 v[96:99], a[52:55], a[116:119], v[96:99]\n"
"global_load_dwordx4 a[36:39], v0, s[34:35] offset:1024 sc1\n"
"v_mfma_f32_16x16x32_bf16 v[124:127], a[60:63], a[116:119], v[124:127]\n"
"ds_read_b128 a[116:119], v4 offset:5120\n"
"v_mfma_f32_16x16x32_bf16 v[124:127], a[56:59], a[112:115], v[124:127]\n"
"ds_read_b128 a[112:115], v4 offset:4096\n"
"v_mfma_f32_16x16x32_bf16 v[128:131], a[56:59], a[120:123], v[128:131]\n"
"v_mfma_f32_16x16x32_bf16 v[128:131], a[60:63], a[124:127], v[128:131]\n"
"v_mfma_f32_16x16x32_bf16 v[100:103], a[52:55], a[124:127], v[100:103]\n"
"ds_read_b128 a[124:127], v4 offset:7168\n"
"v_mfma_f32_16x16x32_bf16 v[100:103], a[48:51], a[120:123], v[100:103]\n"
"ds_read_b128 a[120:123], v4 offset:6144\n"
"v_mfma_f32_16x16x32_bf16 v[104:107], a[48:51], a[128:131], v[104:107]\n"
"buffer_load_dwordx4 v1, s[48:51], s69 offen offset:1024 sc1 lds\n"
"v_mfma_f32_16x16x32_bf16 v[104:107], a[52:55], a[132:135], v[104:107]\n"
"global_load_dwordx4 a[40:43], v0, s[34:35] offset:2048 sc1\n"
"v_mfma_f32_16x16x32_bf16 v[132:135], a[60:63], a[132:135], v[132:135]\n"
"ds_read_b128 a[132:135], v4 offset:9216\n"
"v_mfma_f32_16x16x32_bf16 v[132:135], a[56:59], a[128:131], v[132:135]\n"
"ds_read_b128 a[128:131], v4 offset:8192\n"
"v_mfma_f32_16x16x32_bf16 v[136:139], a[56:59], a[136:139], v[136:139]\n"
"v_mfma_f32_16x16x32_bf16 v[136:139], a[60:63], a[140:143], v[136:139]\n"
"v_mfma_f32_16x16x32_bf16 v[108:111], a[52:55], a[140:143], v[108:111]\n"
"ds_read_b128 a[140:143], v4 offset:11264\n"
"v_mfma_f32_16x16x32_bf16 v[108:111], a[48:51], a[136:139], v[108:111]\n"
"ds_read_b128 a[136:139], v4 offset:10240\n"
"v_mfma_f32_16x16x32_bf16 v[112:115], a[48:51], a[144:147], v[112:115]\n"
"v_mfma_f32_16x16x32_bf16 v[112:115], a[52:55], a[148:151], v[112:115]\n"
"global_load_dwordx4 a[44:47], v0, s[34:35] offset:3072 sc1\n"
"v_mfma_f32_16x16x32_bf16 v[140:143], a[60:63], a[148:151], v[140:143]\n"
"ds_read_b128 a[148:151], v4 offset:13312\n"
"v_mfma_f32_16x16x32_bf16 v[140:143], a[56:59], a[144:147], v[140:143]\n"
"ds_read_b128 a[144:147], v4 offset:12288\n"
"v_mfma_f32_16x16x32_bf16 a[12:15], a[56:59], a[152:155], a[12:15]\n"
"buffer_load_dwordx4 v1, s[48:51], s69 offen offset:2048 sc1 lds\n"
"v_mfma_f32_16x16x32_bf16 a[12:15], a[60:63], a[156:159], a[12:15]\n"
"v_mfma_f32_16x16x32_bf16 a[8:11], a[52:55], a[156:159], a[8:11]\n"
"ds_read_b128 a[156:159], v4 offset:15360\n"
"v_mfma_f32_16x16x32_bf16 a[8:11], a[48:51], a[152:155], a[8:11]\n"
"s_waitcnt vmcnt(18) lgkmcnt(11)\n"
"ds_read_b128 a[152:155], v4 offset:14336\n"
"v_mfma_f32_16x16x32_bf16 v[32:35], a[64:67], a[96:99], v[32:35]\n"
"s_add_u32 s34, s36, 1495040\n"
"v_mfma_f32_16x16x32_bf16 v[32:35], a[68:71], a[100:103], v[32:35]\n"
"global_load_dwordx4 a[48:51], v0, s[34:35] offset:0 sc1\n"
"v_mfma_f32_16x16x32_bf16 v[60:63], a[76:79], a[100:103], v[60:63]\n"
"v_mfma_f32_16x16x32_bf16 v[60:63], a[72:75], a[96:99], v[60:63]\n"
"v_mfma_f32_16x16x32_bf16 v[64:67], a[72:75], a[104:107], v[64:67]\n"
"buffer_load_dwordx4 v1, s[48:51], s69 offen offset:3072 sc1 lds\n"
"v_mfma_f32_16x16x32_bf16 v[64:67], a[76:79], a[108:111], v[64:67]\n"
"v_mfma_f32_16x16x32_bf16 v[36:39], a[68:71], a[108:111], v[36:39]\n"
"v_mfma_f32_16x16x32_bf16 v[36:39], a[64:67], a[104:107], v[36:39]\n"
"s_waitcnt lgkmcnt(8)\n"
"v_mfma_f32_16x16x32_bf16 v[40:43], a[64:67], a[112:115], v[40:43]\n"
"v_mfma_f32_16x16x32_bf16 v[40:43], a[68:71], a[116:119], v[40:43]\n"
"global_load_dwordx4 a[52:55], v0, s[34:35] offset:1024 sc1\n"
"v_mfma_f32_16x16x32_bf16 v[68:71], a[76:79], a[116:119], v[68:71]\n"
"v_mfma_f32_16x16x32_bf16 v[68:71], a[72:75], a[112:115], v[68:71]\n"
"v_mfma_f32_16x16x32_bf16 v[72:75], a[72:75], a[120:123], v[72:75]\n"
"v_mfma_f32_16x16x32_bf16 v[72:75], a[76:79], a[124:127], v[72:75]\n"
"v_mfma_f32_16x16x32_bf16 v[44:47], a[68:71], a[124:127], v[44:47]\n"
"v_mfma_f32_16x16x32_bf16 v[44:47], a[64:67], a[120:123], v[44:47]\n"
"s_waitcnt lgkmcnt(4)\n"
"v_mfma_f32_16x16x32_bf16 v[48:51], a[64:67], a[128:131], v[48:51]\n"
"v_mfma_f32_16x16x32_bf16 v[48:51], a[68:71], a[132:135], v[48:51]\n"
"global_load_dwordx4 a[56:59], v0, s[34:35] offset:2048 sc1\n"
"v_mfma_f32_16x16x32_bf16 v[76:79], a[76:79], a[132:135], v[76:79]\n"
"v_mfma_f32_16x16x32_bf16 v[76:79], a[72:75], a[128:131], v[76:79]\n"
"v_mfma_f32_16x16x32_bf16 v[80:83], a[72:75], a[136:139], v[80:83]\n"
"v_mfma_f32_16x16x32_bf16 v[80:83], a[76:79], a[140:143], v[80:83]\n"
"v_mfma_f32_16x16x32_bf16 v[52:55], a[68:71], a[140:143], v[52:55]\n"
"v_mfma_f32_16x16x32_bf16 v[52:55], a[64:67], a[136:139], v[52:55]\n"
"s_waitcnt lgkmcnt(0)\n"
"v_mfma_f32_16x16x32_bf16 v[56:59], a[64:67], a[144:147], v[56:59]\n"
"v_mfma_f32_16x16x32_bf16 v[56:59], a[68:71], a[148:151], v[56:59]\n"
"global_load_dwordx4 a[60:63], v0, s[34:35] offset:3072 sc1\n"
"v_mfma_f32_16x16x32_bf16 v[84:87], a[76:79], a[148:151], v[84:87]\n"
"v_mfma_f32_16x16x32_bf16 v[84:87], a[72:75], a[144:147], v[84:87]\n"
"v_mfma_f32_16x16x32_bf16 a[4:7], a[72:75], a[152:155], a[4:7]\n"
"v_mfma_f32_16x16x32_bf16 a[4:7], a[76:79], a[156:159], a[4:7]\n"
"v_mfma_f32_16x16x32_bf16 a[0:3], a[68:71], a[156:159], a[0:3]\n"
"v_mfma_f32_16x16x32_bf16 a[0:3], a[64:67], a[152:155], a[0:3]\n"
"s_waitcnt vmcnt(14)\n"
"v_mfma_f32_16x16x32_bf16 v[88:91], a[80:83], a[96:99], v[88:91]\n"
"s_add_u32 s34, s36, 1507328\n"
"v_mfma_f32_16x16x32_bf16 v[88:91], a[84:87], a[100:103], v[88:91]\n"
"global_load_dwordx4 a[64:67], v0, s[34:35] offset:0 sc1\n"
"v_mfma_f32_16x16x32_bf16 v[116:119], a[92:95], a[100:103], v[116:119]\n"
"ds_read_b128 a[100:103], v5 offset:1024\n"
"v_mfma_f32_16x16x32_bf16 v[116:119], a[88:91], a[96:99], v[116:119]\n"
"ds_read_b128 a[96:99], v5 offset:0\n"
"v_mfma_f32_16x16x32_bf16 v[120:123], a[88:91], a[104:107], v[120:123]\n"
"v_mfma_f32_16x16x32_bf16 v[120:123], a[92:95], a[108:111], v[120:123]\n"
"v_mfma_f32_16x16x32_bf16 v[92:95], a[84:87], a[108:111], v[92:95]\n"
"ds_read_b128 a[108:111], v5 offset:3072\n"
"v_mfma_f32_16x16x32_bf16 v[92:95], a[80:83], a[104:107], v[92:95]\n"
"ds_read_b128 a[104:107], v5 offset:2048\n"
"v_mfma_f32_16x16x32_bf16 v[96:99], a[80:83], a[112:115], v[96:99]\n"
"v_mfma_f32_16x16x32_bf16 v[96:99], a[84:87], a[116:119], v[96:99]\n"
"global_load_dwordx4 a[68:71], v0, s[34:35] offset:1024 sc1\n"
"v_mfma_f32_16x16x32_bf16 v[124:127], a[92:95], a[116:119], v[124:127]\n"
"ds_read_b128 a[116:119], v5 offset:5120\n"
"v_mfma_f32_16x16x32_bf16 v[124:127], a[88:91], a[112:115], v[124:127]\n"
"ds_read_b128 a[112:115], v5 offset:4096\n"
"v_mfma_f32_16x16x32_bf16 v[128:131], a[88:91], a[120:123], v[128:131]\n"
"v_mfma_f32_16x16x32_bf16 v[128:131], a[92:95], a[124:127], v[128:131]\n"
"v_mfma_f32_16x16x32_bf16 v[100:103], a[84:87], a[124:127], v[100:103]\n"
"ds_read_b128 a[124:127], v5 offset:7168\n"
"v_mfma_f32_16x16x32_bf16 v[100:103], a[80:83], a[120:123], v[100:103]\n"
"ds_read_b128 a[120:123], v5 offset:6144\n"
"v_mfma_f32_16x16x32_bf16 v[104:107], a[80:83], a[128:131], v[104:107]\n"
"v_mfma_f32_16x16x32_bf16 v[104:107], a[84:87], a[132:135], v[104:107]\n"
"global_load_dwordx4 a[72:75], v0, s[34:35] offset:2048 sc1\n"
"v_mfma_f32_16x16x32_bf16 v[132:135], a[92:95], a[132:135], v[132:135]\n"
"ds_read_b128 a[132:135], v5 offset:9216\n"
"v_mfma_f32_16x16x32_bf16 v[132:135], a[88:91], a[128:131], v[132:135]\n"
"ds_read_b128 a[128:131], v5 offset:8192\n"
"v_mfma_f32_16x16x32_bf16 v[136:139], a[88:91], a[136:139], v[136:139]\n"
"v_mfma_f32_16x16x32_bf16 v[136:139], a[92:95], a[140:143], v[136:139]\n"
"v_mfma_f32_16x16x32_bf16 v[108:111], a[84:87], a[140:143], v[108:111]\n"
"ds_read_b128 a[140:143], v5 offset:11264\n"
"v_mfma_f32_16x16x32_bf16 v[108:111], a[80:83], a[136:139], v[108:111]\n"
"ds_read_b128 a[136:139], v5 offset:10240\n"
"v_mfma_f32_16x16x32_bf16 v[112:115], a[80:83], a[144:147], v[112:115]\n"
"v_mfma_f32_16x16x32_bf16 v[112:115], a[84:87], a[148:151], v[112:115]\n"
"global_load_dwordx4 a[76:79], v0, s[34:35] offset:3072 sc1\n"
"v_mfma_f32_16x16x32_bf16 v[140:143], a[92:95], a[148:151], v[140:143]\n"
"ds_read_b128 a[148:151], v5 offset:13312\n"
"v_mfma_f32_16x16x32_bf16 v[140:143], a[88:91], a[144:147], v[140:143]\n"
"ds_read_b128 a[144:147], v5 offset:12288\n"
"v_mfma_f32_16x16x32_bf16 a[12:15], a[88:91], a[152:155], a[12:15]\n"
"v_mfma_f32_16x16x32_bf16 a[12:15], a[92:95], a[156:159], a[12:15]\n"
"v_mfma_f32_16x16x32_bf16 a[8:11], a[84:87], a[156:159], a[8:11]\n"
"ds_read_b128 a[156:159], v5 offset:15360\n"
"v_mfma_f32_16x16x32_bf16 a[8:11], a[80:83], a[152:155], a[8:11]\n"
"s_waitcnt vmcnt(10) lgkmcnt(11)\n"
"ds_read_b128 a[152:155], v5 offset:14336\n"
"v_mfma_f32_16x16x32_bf16 v[32:35], a[32:35], a[96:99], v[32:35]\n"
"s_add_u32 s34, s36, 1511424\n"
"v_mfma_f32_16x16x32_bf16 v[32:35], a[36:39], a[100:103], v[32:35]\n"
"global_load_dwordx4 a[80:83], v0, s[34:35] offset:0 sc1\n"
"v_mfma_f32_16x16x32_bf16 v[60:63], a[44:47], a[100:103], v[60:63]\n"
"v_mfma_f32_16x16x32_bf16 v[60:63], a[40:43], a[96:99], v[60:63]\n"
"v_mfma_f32_16x16x32_bf16 v[64:67], a[40:43], a[104:107], v[64:67]\n"
"v_mfma_f32_16x16x32_bf16 v[64:67], a[44:47], a[108:111], v[64:67]\n"
"v_mfma_f32_16x16x32_bf16 v[36:39], a[36:39], a[108:111], v[36:39]\n"
"v_mfma_f32_16x16x32_bf16 v[36:39], a[32:35], a[104:107], v[36:39]\n"
"s_waitcnt lgkmcnt(8)\n"
"v_mfma_f32_16x16x32_bf16 v[40:43], a[32:35], a[112:115], v[40:43]\n"
"v_mfma_f32_16x16x32_bf16 v[40:43], a[36:39], a[116:119], v[40:43]\n"
"global_load_dwordx4 a[84:87], v0, s[34:35] offset:1024 sc1\n"
"v_mfma_f32_16x16x32_bf16 v[68:71], a[44:47], a[116:119], v[68:71]\n"
"v_mfma_f32_16x16x32_bf16 v[68:71], a[40:43], a[112:115], v[68:71]\n"
"v_mfma_f32_16x16x32_bf16 v[72:75], a[40:43], a[120:123], v[72:75]\n"
"v_mfma_f32_16x16x32_bf16 v[72:75], a[44:47], a[124:127], v[72:75]\n"
"v_mfma_f32_16x16x32_bf16 v[44:47], a[36:39], a[124:127], v[44:47]\n"
"v_mfma_f32_16x16x32_bf16 v[44:47], a[32:35], a[120:123], v[44:47]\n"
"s_waitcnt lgkmcnt(4)\n"
"v_mfma_f32_16x16x32_bf16 v[48:51], a[32:35], a[128:131], v[48:51]\n"
"v_mfma_f32_16x16x32_bf16 v[48:51], a[36:39], a[132:135], v[48:51]\n"
"global_load_dwordx4 a[88:91], v0, s[34:35] offset:2048 sc1\n"
"v_mfma_f32_16x16x32_bf16 v[76:79], a[44:47], a[132:135], v[76:79]\n"
"v_mfma_f32_16x16x32_bf16 v[76:79], a[40:43], a[128:131], v[76:79]\n"
"v_mfma_f32_16x16x32_bf16 v[80:83], a[40:43], a[136:139], v[80:83]\n"
"v_mfma_f32_16x16x32_bf16 v[80:83], a[44:47], a[140:143], v[80:83]\n"
"v_mfma_f32_16x16x32_bf16 v[52:55], a[36:39], a[140:143], v[52:55]\n"
"v_mfma_f32_16x16x32_bf16 v[52:55], a[32:35], a[136:139], v[52:55]\n"
"s_waitcnt lgkmcnt(0)\n"
"v_mfma_f32_16x16x32_bf16 v[56:59], a[32:35], a[144:147], v[56:59]\n"
"v_mfma_f32_16x16x32_bf16 v[56:59], a[36:39], a[148:151], v[56:59]\n"
"global_load_dwordx4 a[92:95], v0, s[34:35] offset:3072 sc1\n"
"v_mfma_f32_16x16x32_bf16 v[84:87], a[44:47], a[148:151], v[84:87]\n"
"v_mfma_f32_16x16x32_bf16 v[84:87], a[40:43], a[144:147], v[84:87]\n"
"v_mfma_f32_16x16x32_bf16 a[4:7], a[40:43], a[152:155], a[4:7]\n"
"v_mfma_f32_16x16x32_bf16 a[4:7], a[44:47], a[156:159], a[4:7]\n"
"v_mfma_f32_16x16x32_bf16 a[0:3], a[36:39], a[156:159], a[0:3]\n"
"L_st9670:\n"
"s_cbranch_execz L_retC\n"
"v_mfma_f32_16x16x32_bf16 a[0:3], a[32:35], a[152:155], a[0:3]\n"
"s_waitcnt vmcnt(8)\n"
"v_mfma_f32_16x16x32_bf16 v[88:91], a[48:51], a[96:99], v[88:91]\n"
"s_add_u32 s34, s36, 1515520\n"
"v_mfma_f32_16x16x32_bf16 v[88:91], a[52:55], a[100:103], v[88:91]\n"
"global_load_dwordx4 a[32:35], v0, s[34:35] offset:0 sc1\n"
"v_mfma_f32_16x16x32_bf16 v[116:119], a[60:63], a[100:103], v[116:119]\n"
"ds_read_b128 a[100:103], v2 offset:17408\n"
"v_mfma_f32_16x16x32_bf16 v[116:119], a[56:59], a[96:99], v[116:119]\n"
"ds_read_b128 a[96:99], v2 offset:16384\n"
"v_mfma_f32_16x16x32_bf16 v[120:123], a[56:59], a[104:107], v[120:123]\n"
"v_mfma_f32_16x16x32_bf16 v[120:123], a[60:63], a[108:111], v[120:123]\n"
"v_mfma_f32_16x16x32_bf16 v[92:95], a[52:55], a[108:111], v[92:95]\n"
"ds_read_b128 a[108:111], v2 offset:19456\n"
"v_mfma_f32_16x16x32_bf16 v[92:95], a[48:51], a[104:107], v[92:95]\n"
"ds_read_b128 a[104:107], v2 offset:18432\n"
"v_mfma_f32_16x16x32_bf16 v[96:99], a[48:51], a[112:115], v[96:99]\n"
"v_mfma_f32_16x16x32_bf16 v[96:99], a[52:55], a[116:119], v[96:99]\n"
"global_load_dwordx4 a[36:39], v0, s[34:35] offset:1024 sc1\n"
"v_mfma_f32_16x16x32_bf16 v[124:127], a[60:63], a[116:119], v[124:127]\n"
"ds_read_b128 a[116:119], v2 offset:21504\n"
"v_mfma_f32_16x16x32_bf16 v[124:127], a[56:59], a[112:115], v[124:127]\n"
"ds_read_b128 a[112:115], v2 offset:20480\n"
"v_mfma_f32_16x16x32_bf16 v[128:131], a[56:59], a[120:123], v[128:131]\n"
"v_mfma_f32_16x16x32_bf16 v[128:131], a[60:63], a[124:127], v[128:131]\n"
"v_mfma_f32_16x16x32_bf16 v[100:103], a[52:55], a[124:127], v[100:103]\n"
"ds_read_b128 a[124:127], v2 offset:23552\n"
"v_mfma_f32_16x16x32_bf16 v[100:103], a[48:51], a[120:123], v[100:103]\n"
"ds_read_b128 a[120:123], v2 offset:22528\n"
"v_mfma_f32_16x16x32_bf16 v[104:107], a[48:51], a[128:131], v[104:107]\n"
"v_mfma_f32_16x16x32_bf16 v[104:107], a[52:55], a[132:135], v[104:107]\n"
"global_load_dwordx4 a[40:43], v0, s[34:35] offset:2048 sc1\n"
"v_mfma_f32_16x16x32_bf16 v[132:135], a[60:63], a[132:135], v[132:135]\n"
"ds_read_b128 a[132:135], v2 offset:25600\n"
"v_mfma_f32_16x16x32_bf16 v[132:135], a[56:59], a[128:131], v[132:135]\n"
"ds_read_b128 a[128:131], v2 offset:24576\n"
"v_mfma_f32_16x16x32_bf16 v[136:139], a[56:59], a[136:139], v[136:139]\n"
"v_mfma_f32_16x16x32_bf16 v[136:139], a[60:63], a[140:143], v[136:139]\n"
"v_mfma_f32_16x16x32_bf16 v[108:111], a[52:55], a[140:143], v[108:111]\n"
"ds_read_b128 a[140:143], v2 offset:27648\n"
"v_mfma_f32_16x16x32_bf16 v[108:111], a[48:51], a[136:139], v[108:111]\n"
"ds_read_b128 a[136:139], v2 offset:26624\n"
"v_mfma_f32_16x16x32_bf16 v[112:115], a[48:51], a[144:147], v[112:115]\n"
"v_mfma_f32_16x16x32_bf16 v[112:115], a[52:55], a[148:151], v[112:115]\n"
"global_load_dwordx4 a[44:47], v0, s[34:35] offset:3072 sc1\n"
"v_mfma_f32_16x16x32_bf16 v[140:143], a[60:63], a[148:151], v[140:143]\n"
"ds_read_b128 a[148:151], v2 offset:29696\n"
"v_mfma_f32_16x16x32_bf16 v[140:143], a[56:59], a[144:147], v[140:143]\n"
"ds_read_b128 a[144:147], v2 offset:28672\n"
"v_mfma_f32_16x16x32_bf16 a[12:15], a[56:59], a[152:155], a[12:15]\n"
"v_mfma_f32_16x16x32_bf16 a[12:15], a[60:63], a[156:159], a[12:15]\n"
"v_mfma_f32_16x16x32_bf16 a[8:11], a[52:55], a[156:159], a[8:11]\n"
"ds_read_b128 a[156:159], v2 offset:31744\n"
"v_mfma_f32_16x16x32_bf16 a[8:11], a[48:51], a[152:155], a[8:11]\n"
"s_waitcnt vmcnt(8) lgkmcnt(11)\n"
"ds_read_b128 a[152:155], v2 offset:30720\n"
"v_mfma_f32_16x16x32_bf16 v[32:35], a[64:67], a[96:99], v[32:35]\n"
"s_add_u32 s34, s36, 1519616\n"
"v_mfma_f32_16x16x32_bf16 v[32:35], a[68:71], a[100:103], v[32:35]\n"
"global_load_dwordx4 a[48:51], v0, s[34:35] offset:0 sc1\n"
"v_mfma_f32_16x16x32_bf16 v[60:63], a[76:79], a[100:103], v[60:63]\n"
"s_barrier\n"
"v_mfma_f32_16x16x32_bf16 v[60:63], a[72:75], a[96:99], v[60:63]\n"
"v_mfma_f32_16x16x32_bf16 v[64:67], a[72:75], a[104:107], v[64:67]\n"
"v_mfma_f32_16x16x32_bf16 v[64:67], a[76:79], a[108:111], v[64:67]\n"
"v_mfma_f32_16x16x32_bf16 v[36:39], a[68:71], a[108:111], v[36:39]\n"
"v_mfma_f32_16x16x32_bf16 v[36:39], a[64:67], a[104:107], v[36:39]\n"
"s_waitcnt lgkmcnt(8)\n"
"v_mfma_f32_16x16x32_bf16 v[40:43], a[64:67], a[112:115], v[40:43]\n"
"v_mfma_f32_16x16x32_bf16 v[40:43], a[68:71], a[116:119], v[40:43]\n"
"global_load_dwordx4 a[52:55], v0, s[34:35] offset:1024 sc1\n"
"v_mfma_f32_16x16x32_bf16 v[68:71], a[76:79], a[116:119], v[68:71]\n"
"v_mfma_f32_16x16x32_bf16 v[68:71], a[72:75], a[112:115], v[68:71]\n"
"v_mfma_f32_16x16x32_bf16 v[72:75], a[72:75], a[120:123], v[72:75]\n"
"v_mfma_f32_16x16x32_bf16 v[72:75], a[76:79], a[124:127], v[72:75]\n"
"v_mfma_f32_16x16x32_bf16 v[44:47], a[68:71], a[124:127], v[44:47]\n"
"v_mfma_f32_16x16x32_bf16 v[44:47], a[64:67], a[120:123], v[44:47]\n"
"s_waitcnt lgkmcnt(4)\n"
"v_mfma_f32_16x16x32_bf16 v[48:51], a[64:67], a[128:131], v[48:51]\n"
"v_mfma_f32_16x16x32_bf16 v[48:51], a[68:71], a[132:135], v[48:51]\n"
"global_load_dwordx4 a[56:59], v0, s[34:35] offset:2048 sc1\n"
"v_mfma_f32_16x16x32_bf16 v[76:79], a[76:79], a[132:135], v[76:79]\n"
"v_mfma_f32_16x16x32_bf16 v[76:79], a[72:75], a[128:131], v[76:79]\n"
"v_mfma_f32_16x16x32_bf16 v[80:83], a[72:75], a[136:139], v[80:83]\n"
"v_lshlrev_b32 v16, 16, v18\n"
"v_mfma_f32_16x16x32_bf16 v[80:83], a[76:79], a[140:143], v[80:83]\n"
"v_and_b32 v17, s63, v18\n"
"v_mfma_f32_16x16x32_bf16 v[52:55], a[68:71], a[140:143], v[52:55]\n"
"v_lshlrev_b32 v18, 16, v19\n"
"v_mfma_f32_16x16x32_bf16 v[52:55], a[64:67], a[136:139], v[52:55]\n"
"s_waitcnt lgkmcnt(0)\n"
"v_and_b32 v19, s63, v19\n"
"v_add_f32 v144, v144, v16\n"
"v_mfma_f32_16x16x32_bf16 v[56:59], a[64:67], a[144:147], v[56:59]\n"
"v_add_f32 v145, v145, v17\n"
"v_mfma_f32_16x16x32_bf16 v[56:59], a[68:71], a[148:151], v[56:59]\n"
"global_load_dwordx4 a[60:63], v0, s[34:35] offset:3072 sc1\n"
"v_add_f32 v146, v146, v18\n"
"v_mfma_f32_16x16x32_bf16 v[84:87], a[76:79], a[148:151], v[84:87]\n"
"v_add_f32 v147, v147, v19\n"
"v_mfma_f32_16x16x32_bf16 v[84:87], a[72:75], a[144:147], v[84:87]\n"
"v_add_f32 v148, v148, v16\n"
"v_mfma_f32_16x16x32_bf16 a[4:7], a[72:75], a[152:155], a[4:7]\n"
"v_add_f32 v149, v149, v17\n"
"v_mfma_f32_16x16x32_bf16 a[4:7], a[76:79], a[156:159], a[4:7]\n"
"v_add_f32 v150, v150, v18\n"
"v_mfma_f32_16x16x32_bf16 a[0:3], a[68:71], a[156:159], a[0:3]\n"
"v_add_f32 v151, v151, v19\n"
"v_mfma_f32_16x16x32_bf16 a[0:3], a[64:67], a[152:155], a[0:3]\n"
"s_waitcnt vmcnt(8)\n"
"v_add_f32 v152, v152, v16\n"
"v_add_f32 v153, v153, v17\n"
"v_mfma_f32_16x16x32_bf16 v[88:91], a[80:83], a[96:99], v[88:91]\n"
"s_add_u32 s34, s36, 1523712\n"
"v_add_f32 v154, v154, v18\n"
"v_mfma_f32_16x16x32_bf16 v[88:91], a[84:87], a[100:103], v[88:91]\n"
"global_load_dwordx4 a[64:67], v0, s[34:35] offset:0 sc1\n"
"v_add_f32 v155, v155, v19\n"
"v_mfma_f32_16x16x32_bf16 v[116:119], a[92:95], a[100:103], v[116:119]\n"
"ds_read_b128 a[100:103], v3 offset:17408\n"
"v_add_f32 v156, v156, v16\n"
"v_mfma_f32_16x16x32_bf16 v[116:119], a[88:91], a[96:99], v[116:119]\n"
"ds_read_b128 a[96:99], v3 offset:16384\n"
"v_add_f32 v157, v157, v17\n"
"v_mfma_f32_16x16x32_bf16 v[120:123], a[88:91], a[104:107], v[120:123]\n"
"v_add_f32 v158, v158, v18\n"
"v_mfma_f32_16x16x32_bf16 v[120:123], a[92:95], a[108:111], v[120:123]\n"
"v_add_f32 v159, v159, v19\n"
"v_add_f32 v160, v160, v16\n"
"v_mfma_f32_16x16x32_bf16 v[92:95], a[84:87], a[108:111], v[92:95]\n"
"ds_read_b128 a[108:111], v3 offset:19456\n"
"v_add_f32 v161, v161, v17\n"
"v_mfma_f32_16x16x32_bf16 v[92:95], a[80:83], a[104:107], v[92:95]\n"
"ds_read_b128 a[104:107], v3 offset:18432\n"
"v_add_f32 v162, v162, v18\n"
"v_mfma_f32_16x16x32_bf16 v[96:99], a[80:83], a[112:115], v[96:99]\n"
"v_add_f32 v163, v163, v19\n"
"v_mfma_f32_16x16x32_bf16 v[96:99], a[84:87], a[116:119], v[96:99]\n"
"global_load_dwordx4 a[68:71], v0, s[34:35] offset:1024 sc1\n"
"v_add_f32 v164, v164, v16\n"
"v_mfma_f32_16x16x32_bf16 v[124:127], a[92:95], a[116:119], v[124:127]\n"
"ds_read_b128 a[116:119], v3 offset:21504\n"
"v_add_f32 v165, v165, v17\n"
"v_mfma_f32_16x16x32_bf16 v[124:127], a[88:91], a[112:115], v[124:127]\n"
"ds_read_b128 a[112:115], v3 offset:20480\n"
"v_add_f32 v166, v166, v18\n"
"v_mfma_f32_16x16x32_bf16 v[128:131], a[88:91], a[120:123], v[128:131]\n"
"v_add_f32 v167, v167, v19\n"
"v_mfma_f32_16x16x32_bf16 v[128:131], a[92:95], a[124:127], v[128:131]\n"
"v_add_f32 v168, v168, v16\n"
"v_add_f32 v169, v169, v17\n"
"v_mfma_f32_16x16x32_bf16 v[100:103], a[84:87], a[124:127], v[100:103]\n"
"ds_read_b128 a[124:127], v3 offset:23552\n"
"v_add_f32 v170, v170, v18\n"
"v_mfma_f32_16x16x32_bf16 v[100:103], a[80:83], a[120:123], v[100:103]\n"
"ds_read_b128 a[120:123], v3 offset:22528\n"
"v_add_f32 v171, v171, v19\n"
"v_mfma_f32_16x16x32_bf16 v[104:107], a[80:83], a[128:131], v[104:107]\n"
"v_accvgpr_read_b32 v12, a16\n"
"v_mfma_f32_16x16x32_bf16 v[104:107], a[84:87], a[132:135], v[104:107]\n"
"global_load_dwordx4 a[72:75], v0, s[34:35] offset:2048 sc1\n"
"v_accvgpr_read_b32 v13, a17\n"
"v_mfma_f32_16x16x32_bf16 v[132:135], a[92:95], a[132:135], v[132:135]\n"
"ds_read_b128 a[132:135], v3 offset:25600\n"
"v_accvgpr_read_b32 v14, a18\n"
"v_mfma_f32_16x16x32_bf16 v[132:135], a[88:91], a[128:131], v[132:135]\n"
"ds_read_b128 a[128:131], v3 offset:24576\n"
"v_accvgpr_read_b32 v15, a19\n"
"v_mfma_f32_16x16x32_bf16 v[136:139], a[88:91], a[136:139], v[136:139]\n"
"v_add_f32 v12, v12, v16\n"
"v_mfma_f32_16x16x32_bf16 v[136:139], a[92:95], a[140:143], v[136:139]\n"
"v_add_f32 v13, v13, v17\n"
"v_add_f32 v14, v14, v18\n"
"v_mfma_f32_16x16x32_bf16 v[108:111], a[84:87], a[140:143], v[108:111]\n"
"ds_read_b128 a[140:143], v3 offset:27648\n"
"v_add_f32 v15, v15, v19\n"
"v_mfma_f32_16x16x32_bf16 v[108:111], a[80:83], a[136:139], v[108:111]\n"
"ds_read_b128 a[136:139], v3 offset:26624\n"
"v_accvgpr_write_b32 a16, v12\n"
"v_mfma_f32_16x16x32_bf16 v[112:115], a[80:83], a[144:147], v[112:115]\n"
"v_accvgpr_write_b32 a17, v13\n"
"v_mfma_f32_16x16x32_bf16 v[112:115], a[84:87], a[148:151], v[112:115]\n"
"global_load_dwordx4 a[76:79], v0, s[34:35] offset:3072 sc1\n"
"v_accvgpr_write_b32 a18, v14\n"
"v_mfma_f32_16x16x32_bf16 v[140:143], a[92:95], a[148:151], v[140:143]\n"
"ds_read_b128 a[148:151], v3 offset:29696\n"
"v_accvgpr_write_b32 a19, v15\n"
"v_mfma_f32_16x16x32_bf16 v[140:143], a[88:91], a[144:147], v[140:143]\n"
"ds_read_b128 a[144:147], v3 offset:28672\n"
"v_lshlrev_b32 v20, 16, v22\n"
"v_mfma_f32_16x16x32_bf16 a[12:15], a[88:91], a[152:155], a[12:15]\n"
"v_and_b32 v21, s63, v22\n"
"v_lshlrev_b32 v22, 16, v23\n"
"v_mfma_f32_16x16x32_bf16 a[12:15], a[92:95], a[156:159], a[12:15]\n"
"v_and_b32 v23, s63, v23\n"
"v_mfma_f32_16x16x32_bf16 a[8:11], a[84:87], a[156:159], a[8:11]\n"
"ds_read_b128 a[156:159], v3 offset:31744\n"
"v_add_f32 v172, v172, v20\n"
"v_mfma_f32_16x16x32_bf16 a[8:11], a[80:83], a[152:155], a[8:11]\n"
"s_waitcnt vmcnt(8) lgkmcnt(11)\n"
"ds_read_b128 a[152:155], v3 offset:30720\n"
"v_add_f32 v173, v173, v21\n"
"v_mfma_f32_16x16x32_bf16 v[32:35], a[32:35], a[96:99], v[32:35]\n"
"s_add_u32 s34, s36, 1527808\n"
"v_add_f32 v174, v174, v22\n"
"v_mfma_f32_16x16x32_bf16 v[32:35], a[36:39], a[100:103], v[32:35]\n"
"global_load_dwordx4 a[80:83], v0, s[34:35] offset:0 sc1\n"
"v_add_f32 v175, v175, v23\n"
"v_mfma_f32_16x16x32_bf16 v[60:63], a[44:47], a[100:103], v[60:63]\n"
"v_add_f32 v176, v176, v20\n"
"v_mfma_f32_16x16x32_bf16 v[60:63], a[40:43], a[96:99], v[60:63]\n"
"v_add_f32 v177, v177, v21\n"
"v_add_f32 v178, v178, v22\n"
"v_mfma_f32_16x16x32_bf16 v[64:67], a[40:43], a[104:107], v[64:67]\n"
"v_add_f32 v179, v179, v23\n"
"v_mfma_f32_16x16x32_bf16 v[64:67], a[44:47], a[108:111], v[64:67]\n"
"v_add_f32 v180, v180, v20\n"
"v_mfma_f32_16x16x32_bf16 v[36:39], a[36:39], a[108:111], v[36:39]\n"
"v_add_f32 v181, v181, v21\n"
"v_mfma_f32_16x16x32_bf16 v[36:39], a[32:35], a[104:107], v[36:39]\n"
"s_waitcnt lgkmcnt(8)\n"
"v_add_f32 v182, v182, v22\n"
"v_mfma_f32_16x16x32_bf16 v[40:43], a[32:35], a[112:115], v[40:43]\n"
"v_add_f32 v183, v183, v23\n"
"v_mfma_f32_16x16x32_bf16 v[40:43], a[36:39], a[116:119], v[40:43]\n"
"global_load_dwordx4 a[84:87], v0, s[34:35] offset:1024 sc1\n"
"v_add_f32 v184, v184, v20\n"
"v_mfma_f32_16x16x32_bf16 v[68:71], a[44:47], a[116:119], v[68:71]\n"
"v_add_f32 v185, v185, v21\n"
"v_mfma_f32_16x16x32_bf16 v[68:71], a[40:43], a[112:115], v[68:71]\n"
"v_add_f32 v186, v186, v22\n"
"v_add_f32 v187, v187, v23\n"
"v_mfma_f32_16x16x32_bf16 v[72:75], a[40:43], a[120:123], v[72:75]\n"
"v_add_f32 v188, v188, v20\n"
"v_mfma_f32_16x16x32_bf16 v[72:75], a[44:47], a[124:127], v[72:75]\n"
"v_add_f32 v189, v189, v21\n"
"v_mfma_f32_16x16x32_bf16 v[44:47], a[36:39], a[124:127], v[44:47]\n"
"v_add_f32 v190, v190, v22\n"
"v_mfma_f32_16x16x32_bf16 v[44:47], a[32:35], a[120:123], v[44:47]\n"
"s_waitcnt lgkmcnt(4)\n"
"v_add_f32 v191, v191, v23\n"
"v_mfma_f32_16x16x32_bf16 v[48:51], a[32:35], a[128:131], v[48:51]\n"
"v_add_f32 v192, v192, v20\n"
"v_mfma_f32_16x16x32_bf16 v[48:51], a[36:39], a[132:135], v[48:51]\n"
"global_load_dwordx4 a[88:91], v0, s[34:35] offset:2048 sc1\n"
"v_add_f32 v193, v193, v21\n"
"v_mfma_f32_16x16x32_bf16 v[76:79], a[44:47], a[132:135], v[76:79]\n"
"v_add_f32 v194, v194, v22\n"
"v_add_f32 v195, v195, v23\n"
"v_mfma_f32_16x16x32_bf16 v[76:79], a[40:43], a[128:131], v[76:79]\n"
"v_add_f32 v196, v196, v20\n"
"v_mfma_f32_16x16x32_bf16 v[80:83], a[40:43], a[136:139], v[80:83]\n"
"v_add_f32 v197, v197, v21\n"
"v_mfma_f32_16x16x32_bf16 v[80:83], a[44:47], a[140:143], v[80:83]\n"
"v_add_f32 v198, v198, v22\n"
"v_mfma_f32_16x16x32_bf16 v[52:55], a[36:39], a[140:143], v[52:55]\n"
"v_add_f32 v199, v199, v23\n"
"v_mfma_f32_16x16x32_bf16 v[52:55], a[32:35], a[136:139], v[52:55]\n"
"s_waitcnt lgkmcnt(0)\n"
"v_accvgpr_read_b32 v12, a20\n"
"v_mfma_f32_16x16x32_bf16 v[56:59], a[32:35], a[144:147], v[56:59]\n"
"v_accvgpr_read_b32 v13, a21\n"
"v_mfma_f32_16x16x32_bf16 v[56:59], a[36:39], a[148:151], v[56:59]\n"
"global_load_dwordx4 a[92:95], v0, s[34:35] offset:3072 sc1\n"
"v_accvgpr_read_b32 v14, a22\n"
"v_mfma_f32_16x16x32_bf16 v[84:87], a[44:47], a[148:151], v[84:87]\n"
"v_accvgpr_read_b32 v15, a23\n"
"v_add_f32 v12, v12, v20\n"
"v_mfma_f32_16x16x32_bf16 v[84:87], a[40:43], a[144:147], v[84:87]\n"
"v_add_f32 v13, v13, v21\n"
"v_mfma_f32_16x16x32_bf16 a[4:7], a[40:43], a[152:155], a[4:7]\n"
"v_add_f32 v14, v14, v22\n"
"v_mfma_f32_16x16x32_bf16 a[4:7], a[44:47], a[156:159], a[4:7]\n"
"v_add_f32 v15, v15, v23\n"
"v_mfma_f32_16x16x32_bf16 a[0:3], a[36:39], a[156:159], a[0:3]\n"
"v_accvgpr_write_b32 a20, v12\n"
"v_mfma_f32_16x16x32_bf16 a[0:3], a[32:35], a[152:155], a[0:3]\n"
"s_waitcnt vmcnt(8)\n"
"v_accvgpr_write_b32 a21, v13\n"
"v_mfma_f32_16x16x32_bf16 v[88:91], a[48:51], a[96:99], v[88:91]\n"
"s_add_u32 s34, s36, 1531904\n"
"v_accvgpr_write_b32 a22, v14\n"
"v_accvgpr_write_b32 a23, v15\n"
"v_mfma_f32_16x16x32_bf16 v[88:91], a[52:55], a[100:103], v[88:91]\n"
"global_load_dwordx4 a[32:35], v0, s[34:35] offset:0 sc1\n"
"v_lshlrev_b32 v24, 16, v26\n"
"v_mfma_f32_16x16x32_bf16 v[116:119], a[60:63], a[100:103], v[116:119]\n"
"ds_read_b128 a[100:103], v4 offset:17408\n"
"v_and_b32 v25, s63, v26\n"
"v_mfma_f32_16x16x32_bf16 v[116:119], a[56:59], a[96:99], v[116:119]\n"
"ds_read_b128 a[96:99], v4 offset:16384\n"
"v_lshlrev_b32 v26, 16, v27\n"
"v_mfma_f32_16x16x32_bf16 v[120:123], a[56:59], a[104:107], v[120:123]\n"
"v_and_b32 v27, s63, v27\n"
"v_mfma_f32_16x16x32_bf16 v[120:123], a[60:63], a[108:111], v[120:123]\n"
"v_add_f32 v200, v200, v24\n"
"v_mfma_f32_16x16x32_bf16 v[92:95], a[52:55], a[108:111], v[92:95]\n"
"ds_read_b128 a[108:111], v4 offset:19456\n"
"v_add_f32 v201, v201, v25\n"
"v_mfma_f32_16x16x32_bf16 v[92:95], a[48:51], a[104:107], v[92:95]\n"
"ds_read_b128 a[104:107], v4 offset:18432\n"
"v_add_f32 v202, v202, v26\n"
"v_mfma_f32_16x16x32_bf16 v[96:99], a[48:51], a[112:115], v[96:99]\n"
"v_add_f32 v203, v203, v27\n"
"v_add_f32 v204, v204, v24\n"
"v_mfma_f32_16x16x32_bf16 v[96:99], a[52:55], a[116:119], v[96:99]\n"
"global_load_dwordx4 a[36:39], v0, s[34:35] offset:1024 sc1\n"
"v_add_f32 v205, v205, v25\n"
"v_mfma_f32_16x16x32_bf16 v[124:127], a[60:63], a[116:119], v[124:127]\n"
"ds_read_b128 a[116:119], v4 offset:21504\n"
"v_add_f32 v206, v206, v26\n"
"v_mfma_f32_16x16x32_bf16 v[124:127], a[56:59], a[112:115], v[124:127]\n"
"ds_read_b128 a[112:115], v4 offset:20480\n"
"v_add_f32 v207, v207, v27\n"
"v_mfma_f32_16x16x32_bf16 v[128:131], a[56:59], a[120:123], v[128:131]\n"
"v_add_f32 v208, v208, v24\n"
"v_mfma_f32_16x16x32_bf16 v[128:131], a[60:63], a[124:127], v[128:131]\n"
"v_add_f32 v209, v209, v25\n"
"v_mfma_f32_16x16x32_bf16 v[100:103], a[52:55], a[124:127], v[100:103]\n"
"ds_read_b128 a[124:127], v4 offset:23552\n"
"v_add_f32 v210, v210, v26\n"
"v_mfma_f32_16x16x32_bf16 v[100:103], a[48:51], a[120:123], v[100:103]\n"
"ds_read_b128 a[120:123], v4 offset:22528\n"
"v_add_f32 v211, v211, v27\n"
"v_mfma_f32_16x16x32_bf16 v[104:107], a[48:51], a[128:131], v[104:107]\n"
"v_add_f32 v212, v212, v24\n"
"v_add_f32 v213, v213, v25\n"
"v_mfma_f32_16x16x32_bf16 v[104:107], a[52:55], a[132:135], v[104:107]\n"
"global_load_dwordx4 a[40:43], v0, s[34:35] offset:2048 sc1\n"
"v_add_f32 v214, v214, v26\n"
"v_mfma_f32_16x16x32_bf16 v[132:135], a[60:63], a[132:135], v[132:135]\n"
"ds_read_b128 a[132:135], v4 offset:25600\n"
"v_add_f32 v215, v215, v27\n"
"v_mfma_f32_16x16x32_bf16 v[132:135], a[56:59], a[128:131], v[132:135]\n"
"ds_read_b128 a[128:131], v4 offset:24576\n"
"v_add_f32 v216, v216, v24\n"
"v_mfma_f32_16x16x32_bf16 v[136:139], a[56:59], a[136:139], v[136:139]\n"
"v_add_f32 v217, v217, v25\n"
"v_mfma_f32_16x16x32_bf16 v[136:139], a[60:63], a[140:143], v[136:139]\n"
"v_add_f32 v218, v218, v26\n"
"v_mfma_f32_16x16x32_bf16 v[108:111], a[52:55], a[140:143], v[108:111]\n"
"ds_read_b128 a[140:143], v4 offset:27648\n"
"v_add_f32 v219, v219, v27\n"
"v_mfma_f32_16x16x32_bf16 v[108:111], a[48:51], a[136:139], v[108:111]\n"
"ds_read_b128 a[136:139], v4 offset:26624\n"
"v_add_f32 v220, v220, v24\n"
"v_mfma_f32_16x16x32_bf16 v[112:115], a[48:51], a[144:147], v[112:115]\n"
"v_add_f32 v221, v221, v25\n"
"v_add_f32 v222, v222, v26\n"
"v_mfma_f32_16x16x32_bf16 v[112:115], a[52:55], a[148:151], v[112:115]\n"
"global_load_dwordx4 a[44:47], v0, s[34:35] offset:3072 sc1\n"
"v_add_f32 v223, v223, v27\n"
"v_mfma_f32_16x16x32_bf16 v[140:143], a[60:63], a[148:151], v[140:143]\n"
"ds_read_b128 a[148:151], v4 offset:29696\n"
"v_add_f32 v224, v224, v24\n"
"v_mfma_f32_16x16x32_bf16 v[140:143], a[56:59], a[144:147], v[140:143]\n"
"ds_read_b128 a[144:147], v4 offset:28672\n"
"v_add_f32 v225, v225, v25\n"
"v_mfma_f32_16x16x32_bf16 a[12:15], a[56:59], a[152:155], a[12:15]\n"
"v_add_f32 v226, v226, v26\n"
"v_mfma_f32_16x16x32_bf16 a[12:15], a[60:63], a[156:159], a[12:15]\n"
"v_add_f32 v227, v227, v27\n"
"v_mfma_f32_16x16x32_bf16 a[8:11], a[52:55], a[156:159], a[8:11]\n"
"ds_read_b128 a[156:159], v4 offset:31744\n"
"v_accvgpr_read_b32 v12, a24\n"
"v_mfma_f32_16x16x32_bf16 a[8:11], a[48:51], a[152:155], a[8:11]\n"
"s_waitcnt vmcnt(8) lgkmcnt(11)\n"
"ds_read_b128 a[152:155], v4 offset:30720\n"
"v_accvgpr_read_b32 v13, a25\n"
"v_mfma_f32_16x16x32_bf16 v[32:35], a[64:67], a[96:99], v[32:35]\n"
"s_add_u32 s34, s36, 1536000\n"
"v_accvgpr_read_b32 v14, a26\n"
"v_accvgpr_read_b32 v15, a27\n"
"v_mfma_f32_16x16x32_bf16 v[32:35], a[68:71], a[100:103], v[32:35]\n"
"global_load_dwordx4 a[48:51], v0, s[34:35] offset:0 sc1\n"
"v_add_f32 v12, v12, v24\n"
"v_mfma_f32_16x16x32_bf16 v[60:63], a[76:79], a[100:103], v[60:63]\n"
"v_add_f32 v13, v13, v25\n"
"v_mfma_f32_16x16x32_bf16 v[60:63], a[72:75], a[96:99], v[60:63]\n"
"v_add_f32 v14, v14, v26\n"
"v_mfma_f32_16x16x32_bf16 v[64:67], a[72:75], a[104:107], v[64:67]\n"
"v_add_f32 v15, v15, v27\n"
"v_mfma_f32_16x16x32_bf16 v[64:67], a[76:79], a[108:111], v[64:67]\n"
"v_accvgpr_write_b32 a24, v12\n"
"v_mfma_f32_16x16x32_bf16 v[36:39], a[68:71], a[108:111], v[36:39]\n"
"v_accvgpr_write_b32 a25, v13\n"
"v_accvgpr_write_b32 a26, v14\n"
"v_mfma_f32_16x16x32_bf16 v[36:39], a[64:67], a[104:107], v[36:39]\n"
"s_waitcnt lgkmcnt(8)\n"
"v_accvgpr_write_b32 a27, v15\n"
"v_mfma_f32_16x16x32_bf16 v[40:43], a[64:67], a[112:115], v[40:43]\n"
"v_lshlrev_b32 v28, 16, v30\n"
"v_mfma_f32_16x16x32_bf16 v[40:43], a[68:71], a[116:119], v[40:43]\n"
"global_load_dwordx4 a[52:55], v0, s[34:35] offset:1024 sc1\n"
"v_and_b32 v29, s63, v30\n"
"v_mfma_f32_16x16x32_bf16 v[68:71], a[76:79], a[116:119], v[68:71]\n"
"v_lshlrev_b32 v30, 16, v31\n"
"v_mfma_f32_16x16x32_bf16 v[68:71], a[72:75], a[112:115], v[68:71]\n"
"v_and_b32 v31, s63, v31\n"
"v_mfma_f32_16x16x32_bf16 v[72:75], a[72:75], a[120:123], v[72:75]\n"
"v_add_f32 v228, v228, v28\n"
"v_mfma_f32_16x16x32_bf16 v[72:75], a[76:79], a[124:127], v[72:75]\n"
"v_add_f32 v229, v229, v29\n"
"v_add_f32 v230, v230, v30\n"
"v_mfma_f32_16x16x32_bf16 v[44:47], a[68:71], a[124:127], v[44:47]\n"
"v_add_f32 v231, v231, v31\n"
"v_mfma_f32_16x16x32_bf16 v[44:47], a[64:67], a[120:123], v[44:47]\n"
"s_waitcnt lgkmcnt(4)\n"
"v_add_f32 v232, v232, v28\n"
"v_mfma_f32_16x16x32_bf16 v[48:51], a[64:67], a[128:131], v[48:51]\n"
"v_add_f32 v233, v233, v29\n"
"v_mfma_f32_16x16x32_bf16 v[48:51], a[68:71], a[132:135], v[48:51]\n"
"global_load_dwordx4 a[56:59], v0, s[34:35] offset:2048 sc1\n"
"v_add_f32 v234, v234, v30\n"
"v_mfma_f32_16x16x32_bf16 v[76:79], a[76:79], a[132:135], v[76:79]\n"
"v_add_f32 v235, v235, v31\n"
"v_mfma_f32_16x16x32_bf16 v[76:79], a[72:75], a[128:131], v[76:79]\n"
"v_add_f32 v236, v236, v28\n"
"v_mfma_f32_16x16x32_bf16 v[80:83], a[72:75], a[136:139], v[80:83]\n"
"v_add_f32 v237, v237, v29\n"
"v_mfma_f32_16x16x32_bf16 v[80:83], a[76:79], a[140:143], v[80:83]\n"
"v_add_f32 v238, v238, v30\n"
"v_add_f32 v239, v239, v31\n"
"v_mfma_f32_16x16x32_bf16 v[52:55], a[68:71], a[140:143], v[52:55]\n"
"v_add_f32 v240, v240, v28\n"
"v_mfma_f32_16x16x32_bf16 v[52:55], a[64:67], a[136:139], v[52:55]\n"
"s_waitcnt lgkmcnt(0)\n"
"v_add_f32 v241, v241, v29\n"
"v_mfma_f32_16x16x32_bf16 v[56:59], a[64:67], a[144:147], v[56:59]\n"
"v_add_f32 v242, v242, v30\n"
"v_mfma_f32_16x16x32_bf16 v[56:59], a[68:71], a[148:151], v[56:59]\n"
"global_load_dwordx4 a[60:63], v0, s[34:35] offset:3072 sc1\n"
"v_add_f32 v243, v243, v31\n"
"v_mfma_f32_16x16x32_bf16 v[84:87], a[76:79], a[148:151], v[84:87]\n"
"v_add_f32 v244, v244, v28\n"
"v_mfma_f32_16x16x32_bf16 v[84:87], a[72:75], a[144:147], v[84:87]\n"
"v_add_f32 v245, v245, v29\n"
"v_mfma_f32_16x16x32_bf16 a[4:7], a[72:75], a[152:155], a[4:7]\n"
"v_add_f32 v246, v246, v30\n"
"v_add_f32 v247, v247, v31\n"
"v_mfma_f32_16x16x32_bf16 a[4:7], a[76:79], a[156:159], a[4:7]\n"
"v_add_f32 v248, v248, v28\n"
"v_mfma_f32_16x16x32_bf16 a[0:3], a[68:71], a[156:159], a[0:3]\n"
"v_add_f32 v249, v249, v29\n"
"v_mfma_f32_16x16x32_bf16 a[0:3], a[64:67], a[152:155], a[0:3]\n"
"s_waitcnt vmcnt(8)\n"
"v_add_f32 v250, v250, v30\n"
"v_mfma_f32_16x16x32_bf16 v[88:91], a[80:83], a[96:99], v[88:91]\n"
"s_add_u32 s34, s36, 1540096\n"
"v_add_f32 v251, v251, v31\n"
"v_mfma_f32_16x16x32_bf16 v[88:91], a[84:87], a[100:103], v[88:91]\n"
"global_load_dwordx4 a[64:67], v0, s[34:35] offset:0 sc1\n"
"v_add_f32 v252, v252, v28\n"
"v_mfma_f32_16x16x32_bf16 v[116:119], a[92:95], a[100:103], v[116:119]\n"
"ds_read_b128 a[100:103], v5 offset:17408\n"
"v_add_f32 v253, v253, v29\n"
"v_mfma_f32_16x16x32_bf16 v[116:119], a[88:91], a[96:99], v[116:119]\n"
"ds_read_b128 a[96:99], v5 offset:16384\n"
"v_add_f32 v254, v254, v30\n"
"v_mfma_f32_16x16x32_bf16 v[120:123], a[88:91], a[104:107], v[120:123]\n"
"v_add_f32 v255, v255, v31\n"
"v_accvgpr_read_b32 v12, a28\n"
"v_mfma_f32_16x16x32_bf16 v[120:123], a[92:95], a[108:111], v[120:123]\n"
"v_accvgpr_read_b32 v13, a29\n"
"v_mfma_f32_16x16x32_bf16 v[92:95], a[84:87], a[108:111], v[92:95]\n"
"ds_read_b128 a[108:111], v5 offset:19456\n"
"v_accvgpr_read_b32 v14, a30\n"
"v_mfma_f32_16x16x32_bf16 v[92:95], a[80:83], a[104:107], v[92:95]\n"
"ds_read_b128 a[104:107], v5 offset:18432\n"
"v_accvgpr_read_b32 v15, a31\n"
"v_mfma_f32_16x16x32_bf16 v[96:99], a[80:83], a[112:115], v[96:99]\n"
"v_add_f32 v12, v12, v28\n"
"v_mfma_f32_16x16x32_bf16 v[96:99], a[84:87], a[116:119], v[96:99]\n"
"global_load_dwordx4 a[68:71], v0, s[34:35] offset:1024 sc1\n"
"v_add_f32 v13, v13, v29\n"
"v_mfma_f32_16x16x32_bf16 v[124:127], a[92:95], a[116:119], v[124:127]\n"
"ds_read_b128 a[116:119], v5 offset:21504\n"
"v_add_f32 v14, v14, v30\n"
"v_mfma_f32_16x16x32_bf16 v[124:127], a[88:91], a[112:115], v[124:127]\n"
"ds_read_b128 a[112:115], v5 offset:20480\n"
"v_add_f32 v15, v15, v31\n"
"v_mfma_f32_16x16x32_bf16 v[128:131], a[88:91], a[120:123], v[128:131]\n"
"v_accvgpr_write_b32 a28, v12\n"
"v_accvgpr_write_b32 a29, v13\n"
"v_mfma_f32_16x16x32_bf16 v[128:131], a[92:95], a[124:127], v[128:131]\n"
"v_accvgpr_write_b32 a30, v14\n"
"v_mfma_f32_16x16x32_bf16 v[100:103], a[84:87], a[124:127], v[100:103]\n"
"ds_read_b128 a[124:127], v5 offset:23552\n"
"v_accvgpr_write_b32 a31, v15\n"
"v_mfma_f32_16x16x32_bf16 v[100:103], a[80:83], a[120:123], v[100:103]\n"
"ds_read_b128 a[120:123], v5 offset:22528\n"
"v_mfma_f32_16x16x32_bf16 v[104:107], a[80:83], a[128:131], v[104:107]\n"
"v_mfma_f32_16x16x32_bf16 v[104:107], a[84:87], a[132:135], v[104:107]\n"
"global_load_dwordx4 a[72:75], v0, s[34:35] offset:2048 sc1\n"
"v_mfma_f32_16x16x32_bf16 v[132:135], a[92:95], a[132:135], v[132:135]\n"
"ds_read_b128 a[132:135], v5 offset:25600\n"
"v_mfma_f32_16x16x32_bf16 v[132:135], a[88:91], a[128:131], v[132:135]\n"
"ds_read_b128 a[128:131], v5 offset:24576\n"
"v_mfma_f32_16x16x32_bf16 v[136:139], a[88:91], a[136:139], v[136:139]\n"
"ds_read_b128 v[16:19], v7 offset:32768\n"
"v_mfma_f32_16x16x32_bf16 v[136:139], a[92:95], a[140:143], v[136:139]\n"
"v_mfma_f32_16x16x32_bf16 v[108:111], a[84:87], a[140:143], v[108:111]\n"
"ds_read_b128 a[140:143], v5 offset:27648\n"
"v_mfma_f32_16x16x32_bf16 v[108:111], a[80:83], a[136:139], v[108:111]\n"
"ds_read_b128 a[136:139], v5 offset:26624\n"
"v_mfma_f32_16x16x32_bf16 v[112:115], a[80:83], a[144:147], v[112:115]\n"
"v_mfma_f32_16x16x32_bf16 v[112:115], a[84:87], a[148:151], v[112:115]\n"
"global_load_dwordx4 a[76:79], v0, s[34:35] offset:3072 sc1\n"
"v_mfma_f32_16x16x32_bf16 v[140:143], a[92:95], a[148:151], v[140:143]\n"
"ds_read_b128 a[148:151], v5 offset:29696\n"
"v_mfma_f32_16x16x32_bf16 v[140:143], a[88:91], a[144:147], v[140:143]\n"
"ds_read_b128 a[144:147], v5 offset:28672\n"
"v_mfma_f32_16x16x32_bf16 a[12:15], a[88:91], a[152:155], a[12:15]\n"
"v_mfma_f32_16x16x32_bf16 a[12:15], a[92:95], a[156:159], a[12:15]\n"
"v_mfma_f32_16x16x32_bf16 a[8:11], a[84:87], a[156:159], a[8:11]\n"
"ds_read_b128 a[156:159], v5 offset:31744\n"
"v_mfma_f32_16x16x32_bf16 a[8:11], a[80:83], a[152:155], a[8:11]\n"
"s_waitcnt vmcnt(8) lgkmcnt(12)\n"
"ds_read_b128 a[152:155], v5 offset:30720\n"
"v_mfma_f32_16x16x32_bf16 v[32:35], a[32:35], a[96:99], v[32:35]\n"
"s_add_u32 s34, s36, 1544192\n"
"v_mfma_f32_16x16x32_bf16 v[32:35], a[36:39], a[100:103], v[32:35]\n"
"global_load_dwordx4 a[80:83], v0, s[34:35] offset:0 sc1\n"
"v_mfma_f32_16x16x32_bf16 v[60:63], a[44:47], a[100:103], v[60:63]\n"
"v_mfma_f32_16x16x32_bf16 v[60:63], a[40:43], a[96:99], v[60:63]\n"
"v_mfma_f32_16x16x32_bf16 v[64:67], a[40:43], a[104:107], v[64:67]\n"
"v_mfma_f32_16x16x32_bf16 v[64:67], a[44:47], a[108:111], v[64:67]\n"
"v_mfma_f32_16x16x32_bf16 v[36:39], a[36:39], a[108:111], v[36:39]\n"
"v_mfma_f32_16x16x32_bf16 v[36:39], a[32:35], a[104:107], v[36:39]\n"
"s_waitcnt lgkmcnt(9)\n"
"v_mfma_f32_16x16x32_bf16 v[40:43], a[32:35], a[112:115], v[40:43]\n"
"s_mov_b32 s52, %4\n"
"s_and_b32 s53, %5, 0xffff\n"
"v_mfma_f32_16x16x32_bf16 v[40:43], a[36:39], a[116:119], v[40:43]\n"
"global_load_dwordx4 a[84:87], v0, s[34:35] offset:1024 sc1\n"
"v_mfma_f32_16x16x32_bf16 v[68:71], a[44:47], a[116:119], v[68:71]\n"
"s_mov_b32 s54, 260352\n"
"s_mov_b32 s55, 0x20000\n"
"v_mfma_f32_16x16x32_bf16 v[68:71], a[40:43], a[112:115], v[68:71]\n"
"v_xor_b32 v8, 64, v7\n"
"v_mfma_f32_16x16x32_bf16 v[72:75], a[40:43], a[120:123], v[72:75]\n"
"s_waitcnt lgkmcnt(6)\n"
"v_lshlrev_b32 v12, 16, v16\n"
"v_mfma_f32_16x16x32_bf16 v[72:75], a[44:47], a[124:127], v[72:75]\n"
"v_and_b32 v13, s63, v16\n"
"v_lshlrev_b32 v14, 16, v17\n"
"v_mfma_f32_16x16x32_bf16 v[44:47], a[36:39], a[124:127], v[44:47]\n"
"v_and_b32 v15, s63, v17\n"
"v_mfma_f32_16x16x32_bf16 v[44:47], a[32:35], a[120:123], v[44:47]\n"
"s_waitcnt lgkmcnt(4)\n"
"v_xor_b32 v20, 64, v7\n"
"v_mfma_f32_16x16x32_bf16 v[48:51], a[32:35], a[128:131], v[48:51]\n"
"ds_read_b128 v[20:23], v20 offset:32768\n"
"v_mul_f32 v12, v12, v32\n"
"v_mfma_f32_16x16x32_bf16 v[48:51], a[36:39], a[132:135], v[48:51]\n"
"global_load_dwordx4 a[88:91], v0, s[34:35] offset:2048 sc1\n"
"v_mul_f32 v13, v13, v33\n"
"v_mfma_f32_16x16x32_bf16 v[76:79], a[44:47], a[132:135], v[76:79]\n"
"v_mul_f32 v14, v14, v34\n"
"v_mfma_f32_16x16x32_bf16 v[76:79], a[40:43], a[128:131], v[76:79]\n"
"v_mul_f32 v15, v15, v35\n"
"v_mfma_f32_16x16x32_bf16 v[80:83], a[40:43], a[136:139], v[80:83]\n"
"v_cvt_pk_bf16_f32 v24, v12, v13\n"
"v_mfma_f32_16x16x32_bf16 v[80:83], a[44:47], a[140:143], v[80:83]\n"
"v_cvt_pk_bf16_f32 v25, v14, v15\n"
"v_mfma_f32_16x16x32_bf16 v[52:55], a[36:39], a[140:143], v[52:55]\n"
"ds_write_b64 v7, v[24:25] offset:32768\n"
"v_mfma_f32_16x16x32_bf16 v[52:55], a[32:35], a[136:139], v[52:55]\n"
"s_waitcnt lgkmcnt(2)\n"
"v_lshlrev_b32 v12, 16, v18\n"
"v_mfma_f32_16x16x32_bf16 v[56:59], a[32:35], a[144:147], v[56:59]\n"
"v_and_b32 v13, s63, v18\n"
"v_mfma_f32_16x16x32_bf16 v[56:59], a[36:39], a[148:151], v[56:59]\n"
"global_load_dwordx4 a[92:95], v0, s[34:35] offset:3072 sc1\n"
"v_mfma_f32_16x16x32_bf16 v[84:87], a[44:47], a[148:151], v[84:87]\n"
"v_lshlrev_b32 v14, 16, v19\n"
"v_mfma_f32_16x16x32_bf16 v[84:87], a[40:43], a[144:147], v[84:87]\n"
"v_and_b32 v15, s63, v19\n"
"v_mfma_f32_16x16x32_bf16 a[4:7], a[40:43], a[152:155], a[4:7]\n"
"v_mul_f32 v12, v12, v60\n"
"v_mfma_f32_16x16x32_bf16 a[4:7], a[44:47], a[156:159], a[4:7]\n"
"v_mul_f32 v13, v13, v61\n"
"v_mfma_f32_16x16x32_bf16 a[0:3], a[36:39], a[156:159], a[0:3]\n"
"v_mul_f32 v14, v14, v62\n"
"v_mfma_f32_16x16x32_bf16 a[0:3], a[32:35], a[152:155], a[0:3]\n"
"s_waitcnt vmcnt(8)\n"
"v_mul_f32 v15, v15, v63\n"
"v_mfma_f32_16x16x32_bf16 v[88:91], a[48:51], a[96:99], v[88:91]\n"
"s_add_u32 s34, s36, 1466368\n"
"v_cvt_pk_bf16_f32 v26, v12, v13\n"
"v_mfma_f32_16x16x32_bf16 v[88:91], a[52:55], a[100:103], v[88:91]\n"
"global_load_dwordx4 a[32:35], v0, s[34:35] offset:0 sc1\n"
"v_mfma_f32_16x16x32_bf16 v[116:119], a[60:63], a[100:103], v[116:119]\n"
"v_cvt_pk_bf16_f32 v27, v14, v15\n"
"v_mfma_f32_16x16x32_bf16 v[116:119], a[56:59], a[96:99], v[116:119]\n"
"ds_write_b64 v7, v[26:27] offset:32776\n"
"v_mfma_f32_16x16x32_bf16 v[120:123], a[56:59], a[104:107], v[120:123]\n"
"s_waitcnt lgkmcnt(2)\n"
"v_lshlrev_b32 v12, 16, v20\n"
"v_mfma_f32_16x16x32_bf16 v[120:123], a[60:63], a[108:111], v[120:123]\n"
"v_and_b32 v13, s63, v20\n"
"v_mfma_f32_16x16x32_bf16 v[92:95], a[52:55], a[108:111], v[92:95]\n"
"v_lshlrev_b32 v14, 16, v21\n"
"v_mfma_f32_16x16x32_bf16 v[92:95], a[48:51], a[104:107], v[92:95]\n"
"v_and_b32 v15, s63, v21\n"
"v_mfma_f32_16x16x32_bf16 v[96:99], a[48:51], a[112:115], v[96:99]\n"
"ds_read_b128 v[16:19], v7 offset:34816\n"
"v_mfma_f32_16x16x32_bf16 v[96:99], a[52:55], a[116:119], v[96:99]\n"
"L_st10278:\n"
"s_cbranch_execz L_retC\n"
"global_load_dwordx4 a[36:39], v0, s[34:35] offset:1024 sc1\n"
"v_mul_f32 v12, v12, v88\n"
"v_mul_f32 v13, v13, v89\n"
"v_mfma_f32_16x16x32_bf16 v[124:127], a[60:63], a[116:119], v[124:127]\n"
"v_mul_f32 v14, v14, v90\n"
"v_mul_f32 v15, v15, v91\n"
"v_mfma_f32_16x16x32_bf16 v[124:127], a[56:59], a[112:115], v[124:127]\n"
"v_cvt_pk_bf16_f32 v28, v12, v13\n"
"v_cvt_pk_bf16_f32 v29, v14, v15\n"
"v_mfma_f32_16x16x32_bf16 v[128:131], a[56:59], a[120:123], v[128:131]\n"
"ds_write_b64 v8, v[28:29] offset:32768\n"
"v_mfma_f32_16x16x32_bf16 v[128:131], a[60:63], a[124:127], v[128:131]\n"
"v_lshlrev_b32 v12, 16, v22\n"
"v_and_b32 v13, s63, v22\n"
"v_mfma_f32_16x16x32_bf16 v[100:103], a[52:55], a[124:127], v[100:103]\n"
"v_lshlrev_b32 v14, 16, v23\n"
"v_and_b32 v15, s63, v23\n"
"v_mfma_f32_16x16x32_bf16 v[100:103], a[48:51], a[120:123], v[100:103]\n"
"v_mul_f32 v12, v12, v116\n"
"v_mul_f32 v13, v13, v117\n"
"v_mfma_f32_16x16x32_bf16 v[104:107], a[48:51], a[128:131], v[104:107]\n"
"v_mul_f32 v14, v14, v118\n"
"v_mul_f32 v15, v15, v119\n"
"v_mfma_f32_16x16x32_bf16 v[104:107], a[52:55], a[132:135], v[104:107]\n"
"global_load_dwordx4 a[40:43], v0, s[34:35] offset:2048 sc1\n"
"v_cvt_pk_bf16_f32 v30, v12, v13\n"
"v_mfma_f32_16x16x32_bf16 v[132:135], a[60:63], a[132:135], v[132:135]\n"
"v_cvt_pk_bf16_f32 v31, v14, v15\n"
"v_mfma_f32_16x16x32_bf16 v[132:135], a[56:59], a[128:131], v[132:135]\n"
"ds_write_b64 v8, v[30:31] offset:32776\n"
"v_mfma_f32_16x16x32_bf16 v[136:139], a[56:59], a[136:139], v[136:139]\n"
"ds_read_b128 v[32:35], v2 offset:32768\n"
"v_mfma_f32_16x16x32_bf16 v[136:139], a[60:63], a[140:143], v[136:139]\n"
"s_waitcnt lgkmcnt(3)\n"
"ds_read_b128 v[60:63], v2 offset:33792\n"
"v_lshlrev_b32 v12, 16, v16\n"
"v_mfma_f32_16x16x32_bf16 v[108:111], a[52:55], a[140:143], v[108:111]\n"
"s_add_u32 s96, s36, 1568768\n"
"v_and_b32 v13, s63, v16\n"
"v_lshlrev_b32 v14, 16, v17\n"
"v_mfma_f32_16x16x32_bf16 v[108:111], a[48:51], a[136:139], v[108:111]\n"
"v_and_b32 v15, s63, v17\n"
"v_xor_b32 v20, 64, v7\n"
"v_mfma_f32_16x16x32_bf16 v[112:115], a[48:51], a[144:147], v[112:115]\n"
"global_load_dwordx4 v[88:91], v0, s[96:97] offset:-4096 sc1\n"
"ds_read_b128 v[20:23], v20 offset:34816\n"
"v_mfma_f32_16x16x32_bf16 v[112:115], a[52:55], a[148:151], v[112:115]\n"
"global_load_dwordx4 a[44:47], v0, s[34:35] offset:3072 sc1\n"
"v_mul_f32 v12, v12, v36\n"
"v_mfma_f32_16x16x32_bf16 v[140:143], a[60:63], a[148:151], v[140:143]\n"
"v_mul_f32 v13, v13, v37\n"
"v_mul_f32 v14, v14, v38\n"
"v_mfma_f32_16x16x32_bf16 v[140:143], a[56:59], a[144:147], v[140:143]\n"
"v_mul_f32 v15, v15, v39\n"
"v_mfma_f32_16x16x32_bf16 a[12:15], a[56:59], a[152:155], a[12:15]\n"
"v_cvt_pk_bf16_f32 v24, v12, v13\n"
"v_mfma_f32_16x16x32_bf16 a[12:15], a[60:63], a[156:159], a[12:15]\n"
"v_cvt_pk_bf16_f32 v25, v14, v15\n"
"v_mfma_f32_16x16x32_bf16 a[8:11], a[52:55], a[156:159], a[8:11]\n"
"s_waitcnt lgkmcnt(2)\n"
"buffer_store_dwordx4 v[32:35], v6, s[52:55], s80 offen offset:0 nt\n"
"ds_write_b64 v7, v[24:25] offset:34816\n"
"v_mfma_f32_16x16x32_bf16 a[8:11], a[48:51], a[152:155], a[8:11]\n"
"s_waitcnt vmcnt(10)\n"
"v_lshlrev_b32 v12, 16, v18\n"
"v_and_b32 v13, s63, v18\n"
"v_mfma_f32_16x16x32_bf16 v[144:147], a[64:67], a[96:99], v[144:147]\n"
"s_add_u32 s34, s36, 1470464\n"
"v_lshlrev_b32 v14, 16, v19\n"
"v_and_b32 v15, s63, v19\n"
"v_mfma_f32_16x16x32_bf16 v[144:147], a[68:71], a[100:103], v[144:147]\n"
"global_load_dwordx4 a[48:51], v0, s[34:35] offset:0 sc1\n"
"v_mfma_f32_16x16x32_bf16 v[172:175], a[76:79], a[100:103], v[172:175]\n"
"global_load_dwordx4 v[116:119], v0, s[96:97] offset:-3072 sc1\n"
"v_mfma_f32_16x16x32_bf16 v[172:175], a[72:75], a[96:99], v[172:175]\n"
"v_mul_f32 v12, v12, v64\n"
"v_mfma_f32_16x16x32_bf16 v[176:179], a[72:75], a[104:107], v[176:179]\n"
"v_mul_f32 v13, v13, v65\n"
"v_mfma_f32_16x16x32_bf16 v[176:179], a[76:79], a[108:111], v[176:179]\n"
"v_mul_f32 v14, v14, v66\n"
"v_mfma_f32_16x16x32_bf16 v[148:151], a[68:71], a[108:111], v[148:151]\n"
"v_mul_f32 v15, v15, v67\n"
"v_mfma_f32_16x16x32_bf16 v[148:151], a[64:67], a[104:107], v[148:151]\n"
"v_cvt_pk_bf16_f32 v26, v12, v13\n"
"v_mfma_f32_16x16x32_bf16 v[152:155], a[64:67], a[112:115], v[152:155]\n"
"v_cvt_pk_bf16_f32 v27, v14, v15\n"
"v_mfma_f32_16x16x32_bf16 v[152:155], a[68:71], a[116:119], v[152:155]\n"
"global_load_dwordx4 a[52:55], v0, s[34:35] offset:1024 sc1\n"
"v_mfma_f32_16x16x32_bf16 v[180:183], a[76:79], a[116:119], v[180:183]\n"
"s_waitcnt lgkmcnt(2)\n"
"buffer_store_dwordx4 v[60:63], v6, s[52:55], s81 offen offset:1024 nt\n"
"ds_write_b64 v7, v[26:27] offset:34824\n"
"v_mfma_f32_16x16x32_bf16 v[180:183], a[72:75], a[112:115], v[180:183]\n"
"s_waitcnt lgkmcnt(2)\n"
"v_lshlrev_b32 v12, 16, v20\n"
"v_and_b32 v13, s63, v20\n"
"v_mfma_f32_16x16x32_bf16 v[184:187], a[72:75], a[120:123], v[184:187]\n"
"v_lshlrev_b32 v14, 16, v21\n"
"v_and_b32 v15, s63, v21\n"
"v_mfma_f32_16x16x32_bf16 v[184:187], a[76:79], a[124:127], v[184:187]\n"
"ds_read_b128 v[16:19], v7 offset:36864\n"
"v_mul_f32 v12, v12, v92\n"
"v_mfma_f32_16x16x32_bf16 v[156:159], a[68:71], a[124:127], v[156:159]\n"
"v_mul_f32 v13, v13, v93\n"
"v_mul_f32 v14, v14, v94\n"
"v_mfma_f32_16x16x32_bf16 v[156:159], a[64:67], a[120:123], v[156:159]\n"
"v_mul_f32 v15, v15, v95\n"
"v_cvt_pk_bf16_f32 v28, v12, v13\n"
"v_mfma_f32_16x16x32_bf16 v[160:163], a[64:67], a[128:131], v[160:163]\n"
"v_cvt_pk_bf16_f32 v29, v14, v15\n"
"v_mfma_f32_16x16x32_bf16 v[160:163], a[68:71], a[132:135], v[160:163]\n"
"global_load_dwordx4 a[56:59], v0, s[34:35] offset:2048 sc1\n"
"v_mfma_f32_16x16x32_bf16 v[188:191], a[76:79], a[132:135], v[188:191]\n"
"ds_write_b64 v8, v[28:29] offset:34816\n"
"v_mfma_f32_16x16x32_bf16 v[188:191], a[72:75], a[128:131], v[188:191]\n"
"v_lshlrev_b32 v12, 16, v22\n"
"v_and_b32 v13, s63, v22\n"
"v_mfma_f32_16x16x32_bf16 v[192:195], a[72:75], a[136:139], v[192:195]\n"
"v_lshlrev_b32 v14, 16, v23\n"
"v_and_b32 v15, s63, v23\n"
"v_mfma_f32_16x16x32_bf16 v[192:195], a[76:79], a[140:143], v[192:195]\n"
"v_mul_f32 v12, v12, v120\n"
"v_mul_f32 v13, v13, v121\n"
"v_mfma_f32_16x16x32_bf16 v[164:167], a[68:71], a[140:143], v[164:167]\n"
"v_mul_f32 v14, v14, v122\n"
"v_mul_f32 v15, v15, v123\n"
"v_mfma_f32_16x16x32_bf16 v[164:167], a[64:67], a[136:139], v[164:167]\n"
"v_cvt_pk_bf16_f32 v30, v12, v13\n"
"v_cvt_pk_bf16_f32 v31, v14, v15\n"
"v_mfma_f32_16x16x32_bf16 v[168:171], a[64:67], a[144:147], v[168:171]\n"
"ds_write_b64 v8, v[30:31] offset:34824\n"
"v_mfma_f32_16x16x32_bf16 v[168:171], a[68:71], a[148:151], v[168:171]\n"
"global_load_dwordx4 a[60:63], v0, s[34:35] offset:3072 sc1\n"
"ds_read_b128 v[36:39], v2 offset:34816\n"
"v_mfma_f32_16x16x32_bf16 v[196:199], a[76:79], a[148:151], v[196:199]\n"
"s_waitcnt lgkmcnt(3)\n"
"ds_read_b128 v[64:67], v2 offset:35840\n"
"v_lshlrev_b32 v12, 16, v16\n"
"v_mfma_f32_16x16x32_bf16 v[196:199], a[72:75], a[144:147], v[196:199]\n"
"v_and_b32 v13, s63, v16\n"
"v_lshlrev_b32 v14, 16, v17\n"
"v_mfma_f32_16x16x32_bf16 a[20:23], a[72:75], a[152:155], a[20:23]\n"
"v_and_b32 v15, s63, v17\n"
"v_xor_b32 v20, 64, v7\n"
"v_mfma_f32_16x16x32_bf16 a[20:23], a[76:79], a[156:159], a[20:23]\n"
"ds_read_b128 v[20:23], v20 offset:36864\n"
"v_mul_f32 v12, v12, v40\n"
"v_mfma_f32_16x16x32_bf16 a[16:19], a[68:71], a[156:159], a[16:19]\n"
"v_mul_f32 v13, v13, v41\n"
"v_mul_f32 v14, v14, v42\n"
"v_mfma_f32_16x16x32_bf16 a[16:19], a[64:67], a[152:155], a[16:19]\n"
"s_waitcnt vmcnt(12)\n"
"global_load_dwordx4 v[92:95], v0, s[96:97] offset:-2048 sc1\n"
"v_mul_f32 v15, v15, v43\n"
"v_mfma_f32_16x16x32_bf16 v[200:203], a[80:83], a[96:99], v[200:203]\n"
"s_add_u32 s34, s36, 1482752\n"
"v_cvt_pk_bf16_f32 v24, v12, v13\n"
"v_mfma_f32_16x16x32_bf16 v[200:203], a[84:87], a[100:103], v[200:203]\n"
"global_load_dwordx4 a[64:67], v0, s[34:35] offset:0 sc1\n"
"v_cvt_pk_bf16_f32 v25, v14, v15\n"
"v_mfma_f32_16x16x32_bf16 v[228:231], a[92:95], a[100:103], v[228:231]\n"
"ds_read_b128 a[100:103], v3 offset:1024\n"
"v_mfma_f32_16x16x32_bf16 v[228:231], a[88:91], a[96:99], v[228:231]\n"
"ds_read_b128 a[96:99], v3 offset:0\n"
"v_mfma_f32_16x16x32_bf16 v[232:235], a[88:91], a[104:107], v[232:235]\n"
"ds_write_b64 v7, v[24:25] offset:36864\n"
"v_mfma_f32_16x16x32_bf16 v[232:235], a[92:95], a[108:111], v[232:235]\n"
"v_lshlrev_b32 v12, 16, v18\n"
"v_and_b32 v13, s63, v18\n"
"v_mfma_f32_16x16x32_bf16 v[204:207], a[84:87], a[108:111], v[204:207]\n"
"ds_read_b128 a[108:111], v3 offset:3072\n"
"v_lshlrev_b32 v14, 16, v19\n"
"v_mfma_f32_16x16x32_bf16 v[204:207], a[80:83], a[104:107], v[204:207]\n"
"ds_read_b128 a[104:107], v3 offset:2048\n"
"v_and_b32 v15, s63, v19\n"
"v_mfma_f32_16x16x32_bf16 v[208:211], a[80:83], a[112:115], v[208:211]\n"
"s_waitcnt lgkmcnt(7)\n"
"buffer_store_dwordx4 v[36:39], v6, s[52:55], s82 offen offset:2048 nt\n"
"global_load_dwordx4 v[120:123], v0, s[96:97] offset:-1024 sc1\n"
"v_mul_f32 v12, v12, v68\n"
"v_mul_f32 v13, v13, v69\n"
"v_mul_f32 v14, v14, v70\n"
"v_mfma_f32_16x16x32_bf16 v[208:211], a[84:87], a[116:119], v[208:211]\n"
"global_load_dwordx4 a[68:71], v0, s[34:35] offset:1024 sc1\n"
"v_mul_f32 v15, v15, v71\n"
"v_mfma_f32_16x16x32_bf16 v[236:239], a[92:95], a[116:119], v[236:239]\n"
"ds_read_b128 a[116:119], v3 offset:5120\n"
"v_cvt_pk_bf16_f32 v26, v12, v13\n"
"v_mfma_f32_16x16x32_bf16 v[236:239], a[88:91], a[112:115], v[236:239]\n"
"ds_read_b128 a[112:115], v3 offset:4096\n"
"v_cvt_pk_bf16_f32 v27, v14, v15\n"
"v_mfma_f32_16x16x32_bf16 v[240:243], a[88:91], a[120:123], v[240:243]\n"
"ds_write_b64 v7, v[26:27] offset:36872\n"
"v_mfma_f32_16x16x32_bf16 v[240:243], a[92:95], a[124:127], v[240:243]\n"
"s_waitcnt lgkmcnt(8)\n"
"v_lshlrev_b32 v12, 16, v20\n"
"v_and_b32 v13, s63, v20\n"
"v_mfma_f32_16x16x32_bf16 v[212:215], a[84:87], a[124:127], v[212:215]\n"
"ds_read_b128 a[124:127], v3 offset:7168\n"
"v_lshlrev_b32 v14, 16, v21\n"
"v_mfma_f32_16x16x32_bf16 v[212:215], a[80:83], a[120:123], v[212:215]\n"
"ds_read_b128 a[120:123], v3 offset:6144\n"
"v_and_b32 v15, s63, v21\n"
"v_mfma_f32_16x16x32_bf16 v[216:219], a[80:83], a[128:131], v[216:219]\n"
"ds_read_b128 v[16:19], v7 offset:38912\n"
"v_mul_f32 v12, v12, v96\n"
"v_mfma_f32_16x16x32_bf16 v[216:219], a[84:87], a[132:135], v[216:219]\n"
"global_load_dwordx4 a[72:75], v0, s[34:35] offset:2048 sc1\n"
"v_mul_f32 v13, v13, v97\n"
"v_mfma_f32_16x16x32_bf16 v[244:247], a[92:95], a[132:135], v[244:247]\n"
"ds_read_b128 a[132:135], v3 offset:9216\n"
"v_mul_f32 v14, v14, v98\n"
"v_mfma_f32_16x16x32_bf16 v[244:247], a[88:91], a[128:131], v[244:247]\n"
"ds_read_b128 a[128:131], v3 offset:8192\n"
"v_mul_f32 v15, v15, v99\n"
"v_mfma_f32_16x16x32_bf16 v[248:251], a[88:91], a[136:139], v[248:251]\n"
"buffer_store_dwordx4 v[64:67], v6, s[52:55], s83 offen offset:3072 nt\n"
"global_load_dwordx4 v[32:35], v0, s[96:97] offset:0 sc1\n"
"v_cvt_pk_bf16_f32 v28, v12, v13\n"
"v_cvt_pk_bf16_f32 v29, v14, v15\n"
"v_mfma_f32_16x16x32_bf16 v[248:251], a[92:95], a[140:143], v[248:251]\n"
"ds_write_b64 v8, v[28:29] offset:36864\n"
"v_mfma_f32_16x16x32_bf16 v[220:223], a[84:87], a[140:143], v[220:223]\n"
"ds_read_b128 a[140:143], v3 offset:11264\n"
"v_lshlrev_b32 v12, 16, v22\n"
"v_mfma_f32_16x16x32_bf16 v[220:223], a[80:83], a[136:139], v[220:223]\n"
"ds_read_b128 a[136:139], v3 offset:10240\n"
"v_and_b32 v13, s63, v22\n"
"v_mfma_f32_16x16x32_bf16 v[224:227], a[80:83], a[144:147], v[224:227]\n"
"v_lshlrev_b32 v14, 16, v23\n"
"v_and_b32 v15, s63, v23\n"
"v_mfma_f32_16x16x32_bf16 v[224:227], a[84:87], a[148:151], v[224:227]\n"
"global_load_dwordx4 a[76:79], v0, s[34:35] offset:3072 sc1\n"
"v_mul_f32 v12, v12, v124\n"
"v_mfma_f32_16x16x32_bf16 v[252:255], a[92:95], a[148:151], v[252:255]\n"
"ds_read_b128 a[148:151], v3 offset:13312\n"
"v_mul_f32 v13, v13, v125\n"
"v_mfma_f32_16x16x32_bf16 v[252:255], a[88:91], a[144:147], v[252:255]\n"
"ds_read_b128 a[144:147], v3 offset:12288\n"
"v_mul_f32 v14, v14, v126\n"
"v_mfma_f32_16x16x32_bf16 a[28:31], a[88:91], a[152:155], a[28:31]\n"
"v_mul_f32 v15, v15, v127\n"
"v_cvt_pk_bf16_f32 v30, v12, v13\n"
"v_mfma_f32_16x16x32_bf16 a[28:31], a[92:95], a[156:159], a[28:31]\n"
"global_load_dwordx4 v[60:63], v0, s[96:97] offset:1024 sc1\n"
"v_cvt_pk_bf16_f32 v31, v14, v15\n"
"v_mfma_f32_16x16x32_bf16 a[24:27], a[84:87], a[156:159], a[24:27]\n"
"ds_read_b128 a[156:159], v3 offset:15360\n"
"v_mfma_f32_16x16x32_bf16 a[24:27], a[80:83], a[152:155], a[24:27]\n"
"s_waitcnt vmcnt(17) lgkmcnt(14)\n"
"ds_read_b128 a[152:155], v3 offset:14336\n"
"v_mfma_f32_16x16x32_bf16 v[144:147], a[32:35], a[96:99], v[144:147]\n"
"s_add_u32 s34, s36, 1486848\n"
"ds_write_b64 v8, v[30:31] offset:36872\n"
"v_mfma_f32_16x16x32_bf16 v[144:147], a[36:39], a[100:103], v[144:147]\n"
"global_load_dwordx4 a[80:83], v0, s[34:35] offset:0 sc1\n"
"ds_read_b128 v[40:43], v2 offset:36864\n"
"v_mfma_f32_16x16x32_bf16 v[172:175], a[44:47], a[100:103], v[172:175]\n"
"s_waitcnt lgkmcnt(11)\n"
"ds_read_b128 v[68:71], v2 offset:37888\n"
"v_lshlrev_b32 v12, 16, v16\n"
"v_mfma_f32_16x16x32_bf16 v[172:175], a[40:43], a[96:99], v[172:175]\n"
"v_and_b32 v13, s63, v16\n"
"v_lshlrev_b32 v14, 16, v17\n"
"v_mfma_f32_16x16x32_bf16 v[176:179], a[40:43], a[104:107], v[176:179]\n"
"v_and_b32 v15, s63, v17\n"
"v_xor_b32 v20, 64, v7\n"
"v_mfma_f32_16x16x32_bf16 v[176:179], a[44:47], a[108:111], v[176:179]\n"
"ds_read_b128 v[20:23], v20 offset:38912\n"
"v_mul_f32 v12, v12, v44\n"
"v_mfma_f32_16x16x32_bf16 v[148:151], a[36:39], a[108:111], v[148:151]\n"
"v_mul_f32 v13, v13, v45\n"
"v_mul_f32 v14, v14, v46\n"
"v_mfma_f32_16x16x32_bf16 v[148:151], a[32:35], a[104:107], v[148:151]\n"
"v_mul_f32 v15, v15, v47\n"
"v_cvt_pk_bf16_f32 v24, v12, v13\n"
"v_mfma_f32_16x16x32_bf16 v[152:155], a[32:35], a[112:115], v[152:155]\n"
"v_cvt_pk_bf16_f32 v25, v14, v15\n"
"v_mfma_f32_16x16x32_bf16 v[152:155], a[36:39], a[116:119], v[152:155]\n"
"global_load_dwordx4 a[84:87], v0, s[34:35] offset:1024 sc1\n"
"v_mfma_f32_16x16x32_bf16 v[180:183], a[44:47], a[116:119], v[180:183]\n"
"global_load_dwordx4 v[96:99], v0, s[96:97] offset:2048 sc1\n"
"v_mfma_f32_16x16x32_bf16 v[180:183], a[40:43], a[112:115], v[180:183]\n"
"ds_write_b64 v7, v[24:25] offset:38912\n"
"v_mfma_f32_16x16x32_bf16 v[184:187], a[40:43], a[120:123], v[184:187]\n"
"v_lshlrev_b32 v12, 16, v18\n"
"v_and_b32 v13, s63, v18\n"
"v_mfma_f32_16x16x32_bf16 v[184:187], a[44:47], a[124:127], v[184:187]\n"
"v_lshlrev_b32 v14, 16, v19\n"
"v_and_b32 v15, s63, v19\n"
"v_mfma_f32_16x16x32_bf16 v[156:159], a[36:39], a[124:127], v[156:159]\n"
"v_mul_f32 v12, v12, v72\n"
"v_mul_f32 v13, v13, v73\n"
"v_mfma_f32_16x16x32_bf16 v[156:159], a[32:35], a[120:123], v[156:159]\n"
"s_waitcnt lgkmcnt(9)\n"
"v_mul_f32 v14, v14, v74\n"
"v_mul_f32 v15, v15, v75\n"
"v_mfma_f32_16x16x32_bf16 v[160:163], a[32:35], a[128:131], v[160:163]\n"
"v_cvt_pk_bf16_f32 v26, v12, v13\n"
"v_cvt_pk_bf16_f32 v27, v14, v15\n"
"v_mfma_f32_16x16x32_bf16 v[160:163], a[36:39], a[132:135], v[160:163]\n"
"global_load_dwordx4 a[88:91], v0, s[34:35] offset:2048 sc1\n"
"v_mfma_f32_16x16x32_bf16 v[188:191], a[44:47], a[132:135], v[188:191]\n"
"s_waitcnt lgkmcnt(3)\n"
"buffer_store_dwordx4 v[40:43], v6, s[52:55], s84 offen offset:0 nt\n"
"ds_write_b64 v7, v[26:27] offset:38920\n"
"v_mfma_f32_16x16x32_bf16 v[188:191], a[40:43], a[128:131], v[188:191]\n"
"s_waitcnt lgkmcnt(2)\n"
"global_load_dwordx4 v[124:127], v0, s[96:97] offset:3072 sc1\n"
"v_lshlrev_b32 v12, 16, v20\n"
"v_mfma_f32_16x16x32_bf16 v[192:195], a[40:43], a[136:139], v[192:195]\n"
"v_and_b32 v13, s63, v20\n"
"v_lshlrev_b32 v14, 16, v21\n"
"v_mfma_f32_16x16x32_bf16 v[192:195], a[44:47], a[140:143], v[192:195]\n"
"v_and_b32 v15, s63, v21\n"
"v_mul_f32 v12, v12, v100\n"
"v_mfma_f32_16x16x32_bf16 v[164:167], a[36:39], a[140:143], v[164:167]\n"
"v_mul_f32 v13, v13, v101\n"
"v_mul_f32 v14, v14, v102\n"
"v_mfma_f32_16x16x32_bf16 v[164:167], a[32:35], a[136:139], v[164:167]\n"
"v_mul_f32 v15, v15, v103\n"
"v_cvt_pk_bf16_f32 v28, v12, v13\n"
"v_mfma_f32_16x16x32_bf16 v[168:171], a[32:35], a[144:147], v[168:171]\n"
"v_cvt_pk_bf16_f32 v29, v14, v15\n"
"v_mfma_f32_16x16x32_bf16 v[168:171], a[36:39], a[148:151], v[168:171]\n"
"global_load_dwordx4 a[92:95], v0, s[34:35] offset:3072 sc1\n"
"v_mfma_f32_16x16x32_bf16 v[196:199], a[44:47], a[148:151], v[196:199]\n"
"ds_write_b64 v8, v[28:29] offset:38912\n"
"v_mfma_f32_16x16x32_bf16 v[196:199], a[40:43], a[144:147], v[196:199]\n"
"v_lshlrev_b32 v12, 16, v22\n"
"v_and_b32 v13, s63, v22\n"
"v_mfma_f32_16x16x32_bf16 a[20:23], a[40:43], a[152:155], a[20:23]\n"
"v_lshlrev_b32 v14, 16, v23\n"
"v_and_b32 v15, s63, v23\n"
"v_mfma_f32_16x16x32_bf16 a[20:23], a[44:47], a[156:159], a[20:23]\n"
"v_mul_f32 v12, v12, v128\n"
"v_mfma_f32_16x16x32_bf16 a[16:19], a[36:39], a[156:159], a[16:19]\n"
"buffer_store_dwordx4 v[68:71], v6, s[52:55], s85 offen offset:1024 nt\n"
"v_mul_f32 v13, v13, v129\n"
"v_mul_f32 v14, v14, v130\n"
"v_mul_f32 v15, v15, v131\n"
"v_mfma_f32_16x16x32_bf16 a[16:19], a[32:35], a[152:155], a[16:19]\n"
"s_waitcnt vmcnt(18)\n"
"v_cvt_pk_bf16_f32 v30, v12, v13\n"
"v_cvt_pk_bf16_f32 v31, v14, v15\n"
"v_mfma_f32_16x16x32_bf16 v[200:203], a[48:51], a[96:99], v[200:203]\n"
"s_add_u32 s34, s36, 1499136\n"
"ds_write_b64 v8, v[30:31] offset:38920\n"
"v_mfma_f32_16x16x32_bf16 v[200:203], a[52:55], a[100:103], v[200:203]\n"
"global_load_dwordx4 a[32:35], v0, s[34:35] offset:0 sc1\n"
"ds_read_b128 v[44:47], v2 offset:38912\n"
"v_mfma_f32_16x16x32_bf16 v[228:231], a[60:63], a[100:103], v[228:231]\n"
"ds_read_b128 a[100:103], v4 offset:1024\n"
"v_mfma_f32_16x16x32_bf16 v[228:231], a[56:59], a[96:99], v[228:231]\n"
"ds_read_b128 a[96:99], v4 offset:0\n"
"v_mfma_f32_16x16x32_bf16 v[232:235], a[56:59], a[104:107], v[232:235]\n"
"ds_read_b128 v[72:75], v2 offset:39936\n"
"v_accvgpr_read_b32 v16, a160\n"
"v_mfma_f32_16x16x32_bf16 v[232:235], a[60:63], a[108:111], v[232:235]\n"
"v_accvgpr_read_b32 v17, a161\n"
"v_accvgpr_read_b32 v18, a162\n"
"v_mfma_f32_16x16x32_bf16 v[204:207], a[52:55], a[108:111], v[204:207]\n"
"ds_read_b128 a[108:111], v4 offset:3072\n"
"v_accvgpr_read_b32 v19, a163\n"
"v_mfma_f32_16x16x32_bf16 v[204:207], a[48:51], a[104:107], v[204:207]\n"
"ds_read_b128 a[104:107], v4 offset:2048\n"
"v_lshlrev_b32 v12, 16, v16\n"
"v_mfma_f32_16x16x32_bf16 v[208:211], a[48:51], a[112:115], v[208:211]\n"
"v_and_b32 v13, s63, v16\n"
"v_lshlrev_b32 v14, 16, v17\n"
"v_mfma_f32_16x16x32_bf16 v[208:211], a[52:55], a[116:119], v[208:211]\n"
"global_load_dwordx4 a[36:39], v0, s[34:35] offset:1024 sc1\n"
"v_and_b32 v15, s63, v17\n"
"v_mfma_f32_16x16x32_bf16 v[236:239], a[60:63], a[116:119], v[236:239]\n"
"ds_read_b128 a[116:119], v4 offset:5120\n"
"v_mul_f32 v12, v12, v48\n"
"v_mfma_f32_16x16x32_bf16 v[236:239], a[56:59], a[112:115], v[236:239]\n"
"ds_read_b128 a[112:115], v4 offset:4096\n"
"v_mul_f32 v13, v13, v49\n"
"v_mfma_f32_16x16x32_bf16 v[240:243], a[56:59], a[120:123], v[240:243]\n"
"v_mul_f32 v14, v14, v50\n"
"v_mul_f32 v15, v15, v51\n"
"v_mfma_f32_16x16x32_bf16 v[240:243], a[60:63], a[124:127], v[240:243]\n"
"v_cvt_pk_bf16_f32 v24, v12, v13\n"
"v_mfma_f32_16x16x32_bf16 v[212:215], a[52:55], a[124:127], v[212:215]\n"
"ds_read_b128 a[124:127], v4 offset:7168\n"
"v_cvt_pk_bf16_f32 v25, v14, v15\n"
"v_mfma_f32_16x16x32_bf16 v[212:215], a[48:51], a[120:123], v[212:215]\n"
"ds_read_b128 a[120:123], v4 offset:6144\n"
"v_mfma_f32_16x16x32_bf16 v[216:219], a[48:51], a[128:131], v[216:219]\n"
"ds_write_b64 v7, v[24:25] offset:32768\n"
"v_mfma_f32_16x16x32_bf16 v[216:219], a[52:55], a[132:135], v[216:219]\n"
"global_load_dwordx4 a[40:43], v0, s[34:35] offset:2048 sc1\n"
"v_lshlrev_b32 v12, 16, v18\n"
"v_mfma_f32_16x16x32_bf16 v[244:247], a[60:63], a[132:135], v[244:247]\n"
"ds_read_b128 a[132:135], v4 offset:9216\n"
"v_and_b32 v13, s63, v18\n"
"v_mfma_f32_16x16x32_bf16 v[244:247], a[56:59], a[128:131], v[244:247]\n"
"ds_read_b128 a[128:131], v4 offset:8192\n"
"v_lshlrev_b32 v14, 16, v19\n"
"v_mfma_f32_16x16x32_bf16 v[248:251], a[56:59], a[136:139], v[248:251]\n"
"s_add_u32 s96, s36, 1560576\n"
"v_and_b32 v15, s63, v19\n"
"v_mul_f32 v12, v12, v76\n"
"v_mfma_f32_16x16x32_bf16 v[248:251], a[60:63], a[140:143], v[248:251]\n"
"global_load_dwordx4 v[100:103], v0, s[96:97] offset:-4096 sc1\n"
"v_mul_f32 v13, v13, v77\n"
"v_mfma_f32_16x16x32_bf16 v[220:223], a[52:55], a[140:143], v[220:223]\n"
"ds_read_b128 a[140:143], v4 offset:11264\n"
"v_mul_f32 v14, v14, v78\n"
"v_mfma_f32_16x16x32_bf16 v[220:223], a[48:51], a[136:139], v[220:223]\n"
"ds_read_b128 a[136:139], v4 offset:10240\n"
"v_mul_f32 v15, v15, v79\n"
"v_mfma_f32_16x16x32_bf16 v[224:227], a[48:51], a[144:147], v[224:227]\n"
"v_cvt_pk_bf16_f32 v26, v12, v13\n"
"v_cvt_pk_bf16_f32 v27, v14, v15\n"
"v_mfma_f32_16x16x32_bf16 v[224:227], a[52:55], a[148:151], v[224:227]\n"
"global_load_dwordx4 a[44:47], v0, s[34:35] offset:3072 sc1\n"
"v_mfma_f32_16x16x32_bf16 v[252:255], a[60:63], a[148:151], v[252:255]\n"
"ds_read_b128 a[148:151], v4 offset:13312\n"
"v_mfma_f32_16x16x32_bf16 v[252:255], a[56:59], a[144:147], v[252:255]\n"
"ds_read_b128 a[144:147], v4 offset:12288\n"
"v_mfma_f32_16x16x32_bf16 a[28:31], a[56:59], a[152:155], a[28:31]\n"
"s_waitcnt lgkmcnt(15)\n"
"buffer_store_dwordx4 v[44:47], v6, s[52:55], s86 offen offset:2048 nt\n"
"ds_write_b64 v7, v[26:27] offset:32776\n"
"v_mfma_f32_16x16x32_bf16 a[28:31], a[60:63], a[156:159], a[28:31]\n"
"v_accvgpr_read_b32 v16, a164\n"
"v_accvgpr_read_b32 v17, a165\n"
"v_mfma_f32_16x16x32_bf16 a[24:27], a[52:55], a[156:159], a[24:27]\n"
"ds_read_b128 a[156:159], v4 offset:15360\n"
"v_accvgpr_read_b32 v18, a166\n"
"v_mfma_f32_16x16x32_bf16 a[24:27], a[48:51], a[152:155], a[24:27]\n"
"s_waitcnt vmcnt(15) lgkmcnt(13)\n"
"ds_read_b128 a[152:155], v4 offset:14336\n"
"v_accvgpr_read_b32 v19, a167\n"
"v_mfma_f32_16x16x32_bf16 v[144:147], a[64:67], a[96:99], v[144:147]\n"
"s_add_u32 s34, s36, 1503232\n"
"global_load_dwordx4 v[128:131], v0, s[96:97] offset:-3072 sc1\n"
"v_mfma_f32_16x16x32_bf16 v[144:147], a[68:71], a[100:103], v[144:147]\n"
"global_load_dwordx4 a[48:51], v0, s[34:35] offset:0 sc1\n"
"v_lshlrev_b32 v12, 16, v16\n"
"v_mfma_f32_16x16x32_bf16 v[172:175], a[76:79], a[100:103], v[172:175]\n"
"v_and_b32 v13, s63, v16\n"
"v_lshlrev_b32 v14, 16, v17\n"
"v_mfma_f32_16x16x32_bf16 v[172:175], a[72:75], a[96:99], v[172:175]\n"
"v_and_b32 v15, s63, v17\n"
"v_mul_f32 v12, v12, v104\n"
"v_mfma_f32_16x16x32_bf16 v[176:179], a[72:75], a[104:107], v[176:179]\n"
"v_mul_f32 v13, v13, v105\n"
"v_mul_f32 v14, v14, v106\n"
"v_mfma_f32_16x16x32_bf16 v[176:179], a[76:79], a[108:111], v[176:179]\n"
"v_mul_f32 v15, v15, v107\n"
"v_mfma_f32_16x16x32_bf16 v[148:151], a[68:71], a[108:111], v[148:151]\n"
"v_cvt_pk_bf16_f32 v28, v12, v13\n"
"v_mfma_f32_16x16x32_bf16 v[148:151], a[64:67], a[104:107], v[148:151]\n"
"s_waitcnt lgkmcnt(10)\n"
"v_cvt_pk_bf16_f32 v29, v14, v15\n"
"v_mfma_f32_16x16x32_bf16 v[152:155], a[64:67], a[112:115], v[152:155]\n"
"buffer_store_dwordx4 v[72:75], v6, s[52:55], s87 offen offset:3072 nt\n"
"ds_write_b64 v8, v[28:29] offset:32768\n"
"v_mfma_f32_16x16x32_bf16 v[152:155], a[68:71], a[116:119], v[152:155]\n"
"global_load_dwordx4 a[52:55], v0, s[34:35] offset:1024 sc1\n"
"v_lshlrev_b32 v12, 16, v18\n"
"v_mfma_f32_16x16x32_bf16 v[180:183], a[76:79], a[116:119], v[180:183]\n"
"global_load_dwordx4 v[36:39], v0, s[96:97] offset:-2048 sc1\n"
"v_and_b32 v13, s63, v18\n"
"v_mfma_f32_16x16x32_bf16 v[180:183], a[72:75], a[112:115], v[180:183]\n"
"v_lshlrev_b32 v14, 16, v19\n"
"v_and_b32 v15, s63, v19\n"
"v_mfma_f32_16x16x32_bf16 v[184:187], a[72:75], a[120:123], v[184:187]\n"
"v_mul_f32 v12, v12, v132\n"
"v_mul_f32 v13, v13, v133\n"
"v_mfma_f32_16x16x32_bf16 v[184:187], a[76:79], a[124:127], v[184:187]\n"
"v_mul_f32 v14, v14, v134\n"
"v_mul_f32 v15, v15, v135\n"
"v_mfma_f32_16x16x32_bf16 v[156:159], a[68:71], a[124:127], v[156:159]\n"
"v_cvt_pk_bf16_f32 v30, v12, v13\n"
"v_cvt_pk_bf16_f32 v31, v14, v15\n"
"v_mfma_f32_16x16x32_bf16 v[156:159], a[64:67], a[120:123], v[156:159]\n"
"s_waitcnt lgkmcnt(6)\n"
"ds_write_b64 v8, v[30:31] offset:32776\n"
"v_mfma_f32_16x16x32_bf16 v[160:163], a[64:67], a[128:131], v[160:163]\n"
"ds_read_b128 v[48:51], v2 offset:32768\n"
"v_mfma_f32_16x16x32_bf16 v[160:163], a[68:71], a[132:135], v[160:163]\n"
"global_load_dwordx4 a[56:59], v0, s[34:35] offset:2048 sc1\n"
"ds_read_b128 v[76:79], v2 offset:33792\n"
"v_mfma_f32_16x16x32_bf16 v[188:191], a[76:79], a[132:135], v[188:191]\n"
"v_accvgpr_read_b32 v16, a168\n"
"v_accvgpr_read_b32 v17, a169\n"
"v_mfma_f32_16x16x32_bf16 v[188:191], a[72:75], a[128:131], v[188:191]\n"
"v_accvgpr_read_b32 v18, a170\n"
"v_accvgpr_read_b32 v19, a171\n"
"v_mfma_f32_16x16x32_bf16 v[192:195], a[72:75], a[136:139], v[192:195]\n"
"global_load_dwordx4 v[64:67], v0, s[96:97] offset:-1024 sc1\n"
"v_lshlrev_b32 v12, 16, v16\n"
"v_mfma_f32_16x16x32_bf16 v[192:195], a[76:79], a[140:143], v[192:195]\n"
"v_and_b32 v13, s63, v16\n"
"v_lshlrev_b32 v14, 16, v17\n"
"v_mfma_f32_16x16x32_bf16 v[164:167], a[68:71], a[140:143], v[164:167]\n"
"v_and_b32 v15, s63, v17\n"
"v_mul_f32 v12, v12, v52\n"
"v_mfma_f32_16x16x32_bf16 v[164:167], a[64:67], a[136:139], v[164:167]\n"
"s_waitcnt lgkmcnt(4)\n"
"v_mul_f32 v13, v13, v53\n"
"v_mul_f32 v14, v14, v54\n"
"v_mfma_f32_16x16x32_bf16 v[168:171], a[64:67], a[144:147], v[168:171]\n"
"v_mul_f32 v15, v15, v55\n"
"v_cvt_pk_bf16_f32 v24, v12, v13\n"
"v_mfma_f32_16x16x32_bf16 v[168:171], a[68:71], a[148:151], v[168:171]\n"
"global_load_dwordx4 a[60:63], v0, s[34:35] offset:3072 sc1\n"
"v_cvt_pk_bf16_f32 v25, v14, v15\n"
"v_mfma_f32_16x16x32_bf16 v[196:199], a[76:79], a[148:151], v[196:199]\n"
"ds_write_b64 v7, v[24:25] offset:34816\n"
"v_mfma_f32_16x16x32_bf16 v[196:199], a[72:75], a[144:147], v[196:199]\n"
"v_lshlrev_b32 v12, 16, v18\n"
"v_and_b32 v13, s63, v18\n"
"v_mfma_f32_16x16x32_bf16 a[20:23], a[72:75], a[152:155], a[20:23]\n"
"v_lshlrev_b32 v14, 16, v19\n"
"v_and_b32 v15, s63, v19\n"
"v_mfma_f32_16x16x32_bf16 a[20:23], a[76:79], a[156:159], a[20:23]\n"
"v_mul_f32 v12, v12, v80\n"
"v_mfma_f32_16x16x32_bf16 a[16:19], a[68:71], a[156:159], a[16:19]\n"
"global_load_dwordx4 v[104:107], v0, s[96:97] offset:0 sc1\n"
"v_mul_f32 v13, v13, v81\n"
"v_mfma_f32_16x16x32_bf16 a[16:19], a[64:67], a[152:155], a[16:19]\n"
"s_waitcnt vmcnt(16)\n"
"v_mul_f32 v14, v14, v82\n"
"v_mul_f32 v15, v15, v83\n"
"v_mfma_f32_16x16x32_bf16 v[200:203], a[80:83], a[96:99], v[200:203]\n"
"s_add_u32 s34, s36, 1548288\n"
"v_cvt_pk_bf16_f32 v26, v12, v13\n"
"v_mfma_f32_16x16x32_bf16 v[200:203], a[84:87], a[100:103], v[200:203]\n"
"global_load_dwordx4 a[64:67], v0, s[34:35] offset:0 sc1\n"
"v_cvt_pk_bf16_f32 v27, v14, v15\n"
"v_mfma_f32_16x16x32_bf16 v[228:231], a[92:95], a[100:103], v[228:231]\n"
"ds_read_b128 a[100:103], v5 offset:1024\n"
"v_mfma_f32_16x16x32_bf16 v[228:231], a[88:91], a[96:99], v[228:231]\n"
"ds_read_b128 a[96:99], v5 offset:0\n"
"v_mfma_f32_16x16x32_bf16 v[232:235], a[88:91], a[104:107], v[232:235]\n"
"ds_write_b64 v7, v[26:27] offset:34824\n"
"v_mfma_f32_16x16x32_bf16 v[232:235], a[92:95], a[108:111], v[232:235]\n"
"v_accvgpr_read_b32 v16, a172\n"
"v_accvgpr_read_b32 v17, a173\n"
"v_mfma_f32_16x16x32_bf16 v[204:207], a[84:87], a[108:111], v[204:207]\n"
"s_waitcnt lgkmcnt(5)\n"
"buffer_store_dwordx4 v[48:51], v6, s[52:55], s88 offen offset:0 nt\n"
"ds_read_b128 a[108:111], v5 offset:3072\n"
"v_accvgpr_read_b32 v18, a174\n"
"v_accvgpr_read_b32 v19, a175\n"
"v_mfma_f32_16x16x32_bf16 v[204:207], a[80:83], a[104:107], v[204:207]\n"
"ds_read_b128 a[104:107], v5 offset:2048\n"
"v_lshlrev_b32 v12, 16, v16\n"
"v_mfma_f32_16x16x32_bf16 v[208:211], a[80:83], a[112:115], v[208:211]\n"
"global_load_dwordx4 v[132:135], v0, s[96:97] offset:1024 sc1\n"
"v_and_b32 v13, s63, v16\n"
"v_mfma_f32_16x16x32_bf16 v[208:211], a[84:87], a[116:119], v[208:211]\n"
"global_load_dwordx4 a[68:71], v0, s[34:35] offset:1024 sc1\n"
"v_lshlrev_b32 v14, 16, v17\n"
"v_mfma_f32_16x16x32_bf16 v[236:239], a[92:95], a[116:119], v[236:239]\n"
"ds_read_b128 a[116:119], v5 offset:5120\n"
"v_and_b32 v15, s63, v17\n"
"v_mfma_f32_16x16x32_bf16 v[236:239], a[88:91], a[112:115], v[236:239]\n"
"ds_read_b128 a[112:115], v5 offset:4096\n"
"v_mul_f32 v12, v12, v108\n"
"v_mfma_f32_16x16x32_bf16 v[240:243], a[88:91], a[120:123], v[240:243]\n"
"v_mul_f32 v13, v13, v109\n"
"v_mul_f32 v14, v14, v110\n"
"v_mfma_f32_16x16x32_bf16 v[240:243], a[92:95], a[124:127], v[240:243]\n"
"v_mul_f32 v15, v15, v111\n"
"v_cvt_pk_bf16_f32 v28, v12, v13\n"
"v_mfma_f32_16x16x32_bf16 v[212:215], a[84:87], a[124:127], v[212:215]\n"
"ds_read_b128 a[124:127], v5 offset:7168\n"
"v_cvt_pk_bf16_f32 v29, v14, v15\n"
"v_mfma_f32_16x16x32_bf16 v[212:215], a[80:83], a[120:123], v[212:215]\n"
"ds_read_b128 a[120:123], v5 offset:6144\n"
"v_mfma_f32_16x16x32_bf16 v[216:219], a[80:83], a[128:131], v[216:219]\n"
"ds_write_b64 v8, v[28:29] offset:34816\n"
"v_mfma_f32_16x16x32_bf16 v[216:219], a[84:87], a[132:135], v[216:219]\n"
"global_load_dwordx4 a[72:75], v0, s[34:35] offset:2048 sc1\n"
"v_lshlrev_b32 v12, 16, v18\n"
"v_mfma_f32_16x16x32_bf16 v[244:247], a[92:95], a[132:135], v[244:247]\n"
"s_waitcnt lgkmcnt(11)\n"
"buffer_store_dwordx4 v[76:79], v6, s[52:55], s89 offen offset:1024 nt\n"
"ds_read_b128 a[132:135], v5 offset:9216\n"
"v_and_b32 v13, s63, v18\n"
"v_lshlrev_b32 v14, 16, v19\n"
"v_mfma_f32_16x16x32_bf16 v[244:247], a[88:91], a[128:131], v[244:247]\n"
"ds_read_b128 a[128:131], v5 offset:8192\n"
"v_and_b32 v15, s63, v19\n"
"v_mfma_f32_16x16x32_bf16 v[248:251], a[88:91], a[136:139], v[248:251]\n"
"v_mul_f32 v12, v12, v136\n"
"v_mul_f32 v13, v13, v137\n"
"v_mfma_f32_16x16x32_bf16 v[248:251], a[92:95], a[140:143], v[248:251]\n"
"v_mul_f32 v14, v14, v138\n"
"v_mul_f32 v15, v15, v139\n"
"v_mfma_f32_16x16x32_bf16 v[220:223], a[84:87], a[140:143], v[220:223]\n"
"ds_read_b128 a[140:143], v5 offset:11264\n"
"v_cvt_pk_bf16_f32 v30, v12, v13\n"
"v_mfma_f32_16x16x32_bf16 v[220:223], a[80:83], a[136:139], v[220:223]\n"
"ds_read_b128 a[136:139], v5 offset:10240\n"
"v_cvt_pk_bf16_f32 v31, v14, v15\n"
"v_mfma_f32_16x16x32_bf16 v[224:227], a[80:83], a[144:147], v[224:227]\n"
"ds_write_b64 v8, v[30:31] offset:34824\n"
"v_mfma_f32_16x16x32_bf16 v[224:227], a[84:87], a[148:151], v[224:227]\n"
"global_load_dwordx4 a[76:79], v0, s[34:35] offset:3072 sc1\n"
"ds_read_b128 v[52:55], v2 offset:34816\n"
"v_mfma_f32_16x16x32_bf16 v[252:255], a[92:95], a[148:151], v[252:255]\n"
"ds_read_b128 a[148:151], v5 offset:13312\n"
"v_mfma_f32_16x16x32_bf16 v[252:255], a[88:91], a[144:147], v[252:255]\n"
"L_st10888:\n"
"s_cbranch_execz L_retC\n"
"ds_read_b128 a[144:147], v5 offset:12288\n"
"v_mfma_f32_16x16x32_bf16 a[28:31], a[88:91], a[152:155], a[28:31]\n"
"ds_read_b128 v[80:83], v2 offset:35840\n"
"v_accvgpr_read_b32 v16, a176\n"
"v_mfma_f32_16x16x32_bf16 a[28:31], a[92:95], a[156:159], a[28:31]\n"
"v_accvgpr_read_b32 v17, a177\n"
"v_accvgpr_read_b32 v18, a178\n"
"v_mfma_f32_16x16x32_bf16 a[24:27], a[84:87], a[156:159], a[24:27]\n"
"ds_read_b128 a[156:159], v5 offset:15360\n"
"v_accvgpr_read_b32 v19, a179\n"
"v_mfma_f32_16x16x32_bf16 a[24:27], a[80:83], a[152:155], a[24:27]\n"
"s_waitcnt vmcnt(17) lgkmcnt(15)\n"
"ds_read_b128 a[152:155], v5 offset:14336\n"
"v_lshlrev_b32 v12, 16, v16\n"
"v_mfma_f32_16x16x32_bf16 v[144:147], a[32:35], a[96:99], v[144:147]\n"
"s_add_u32 s34, s36, 1552384\n"
"v_and_b32 v13, s63, v16\n"
"v_lshlrev_b32 v14, 16, v17\n"
"v_mfma_f32_16x16x32_bf16 v[144:147], a[36:39], a[100:103], v[144:147]\n"
"global_load_dwordx4 a[80:83], v0, s[34:35] offset:0 sc1\n"
"v_and_b32 v15, s63, v17\n"
"v_mfma_f32_16x16x32_bf16 v[172:175], a[44:47], a[100:103], v[172:175]\n"
"v_mul_f32 v12, v12, v56\n"
"v_mul_f32 v13, v13, v57\n"
"v_mfma_f32_16x16x32_bf16 v[172:175], a[40:43], a[96:99], v[172:175]\n"
"v_mul_f32 v14, v14, v58\n"
"v_mul_f32 v15, v15, v59\n"
"v_mfma_f32_16x16x32_bf16 v[176:179], a[40:43], a[104:107], v[176:179]\n"
"global_load_dwordx4 a[84:87], v0, s[34:35] offset:1024 sc1\n"
"v_cvt_pk_bf16_f32 v24, v12, v13\n"
"v_mfma_f32_16x16x32_bf16 v[176:179], a[44:47], a[108:111], v[176:179]\n"
"v_cvt_pk_bf16_f32 v25, v14, v15\n"
"v_mfma_f32_16x16x32_bf16 v[148:151], a[36:39], a[108:111], v[148:151]\n"
"ds_write_b64 v7, v[24:25] offset:36864\n"
"v_mfma_f32_16x16x32_bf16 v[148:151], a[32:35], a[104:107], v[148:151]\n"
"s_waitcnt lgkmcnt(13)\n"
"global_load_dwordx4 a[88:91], v0, s[34:35] offset:2048 sc1\n"
"v_lshlrev_b32 v12, 16, v18\n"
"v_mfma_f32_16x16x32_bf16 v[152:155], a[32:35], a[112:115], v[152:155]\n"
"global_load_dwordx4 v[108:111], v0, s[96:97] offset:2048 sc1\n"
"v_and_b32 v13, s63, v18\n"
"v_mfma_f32_16x16x32_bf16 v[152:155], a[36:39], a[116:119], v[152:155]\n"
"v_lshlrev_b32 v14, 16, v19\n"
"v_and_b32 v15, s63, v19\n"
"v_mfma_f32_16x16x32_bf16 v[180:183], a[44:47], a[116:119], v[180:183]\n"
"global_load_dwordx4 a[92:95], v0, s[34:35] offset:3072 sc1\n"
"v_mul_f32 v12, v12, v84\n"
"v_mfma_f32_16x16x32_bf16 v[180:183], a[40:43], a[112:115], v[180:183]\n"
"v_mul_f32 v13, v13, v85\n"
"v_mul_f32 v14, v14, v86\n"
"v_mfma_f32_16x16x32_bf16 v[184:187], a[40:43], a[120:123], v[184:187]\n"
"v_mul_f32 v15, v15, v87\n"
"v_cvt_pk_bf16_f32 v26, v12, v13\n"
"v_mfma_f32_16x16x32_bf16 v[184:187], a[44:47], a[124:127], v[184:187]\n"
"v_cvt_pk_bf16_f32 v27, v14, v15\n"
"v_mfma_f32_16x16x32_bf16 v[156:159], a[36:39], a[124:127], v[156:159]\n"
"ds_write_b64 v7, v[26:27] offset:36872\n"
"v_mfma_f32_16x16x32_bf16 v[156:159], a[32:35], a[120:123], v[156:159]\n"
"s_waitcnt lgkmcnt(9)\n"
"v_accvgpr_read_b32 v16, a180\n"
"v_accvgpr_read_b32 v17, a181\n"
"v_mfma_f32_16x16x32_bf16 v[160:163], a[32:35], a[128:131], v[160:163]\n"
"s_waitcnt lgkmcnt(7)\n"
"buffer_store_dwordx4 v[52:55], v6, s[52:55], s90 offen offset:2048 nt\n"
"v_accvgpr_read_b32 v18, a182\n"
"v_accvgpr_read_b32 v19, a183\n"
"v_lshlrev_b32 v12, 16, v16\n"
"v_mfma_f32_16x16x32_bf16 v[160:163], a[36:39], a[132:135], v[160:163]\n"
"global_load_dwordx4 v[136:139], v0, s[96:97] offset:3072 sc1\n"
"v_and_b32 v13, s63, v16\n"
"v_mfma_f32_16x16x32_bf16 v[188:191], a[44:47], a[132:135], v[188:191]\n"
"v_lshlrev_b32 v14, 16, v17\n"
"v_and_b32 v15, s63, v17\n"
"v_mfma_f32_16x16x32_bf16 v[188:191], a[40:43], a[128:131], v[188:191]\n"
"v_mul_f32 v12, v12, v112\n"
"v_mul_f32 v13, v13, v113\n"
"v_mfma_f32_16x16x32_bf16 v[192:195], a[40:43], a[136:139], v[192:195]\n"
"v_mul_f32 v14, v14, v114\n"
"v_mul_f32 v15, v15, v115\n"
"v_mfma_f32_16x16x32_bf16 v[192:195], a[44:47], a[140:143], v[192:195]\n"
"v_cvt_pk_bf16_f32 v28, v12, v13\n"
"v_cvt_pk_bf16_f32 v29, v14, v15\n"
"v_mfma_f32_16x16x32_bf16 v[164:167], a[36:39], a[140:143], v[164:167]\n"
"ds_write_b64 v8, v[28:29] offset:36864\n"
"v_mfma_f32_16x16x32_bf16 v[164:167], a[32:35], a[136:139], v[164:167]\n"
"s_waitcnt lgkmcnt(3)\n"
"v_lshlrev_b32 v12, 16, v18\n"
"v_and_b32 v13, s63, v18\n"
"v_mfma_f32_16x16x32_bf16 v[168:171], a[32:35], a[144:147], v[168:171]\n"
"v_lshlrev_b32 v14, 16, v19\n"
"v_and_b32 v15, s63, v19\n"
"v_mfma_f32_16x16x32_bf16 v[168:171], a[36:39], a[148:151], v[168:171]\n"
"v_mul_f32 v12, v12, v140\n"
"v_mul_f32 v13, v13, v141\n"
"v_mfma_f32_16x16x32_bf16 v[196:199], a[44:47], a[148:151], v[196:199]\n"
"v_mul_f32 v14, v14, v142\n"
"v_mul_f32 v15, v15, v143\n"
"v_mfma_f32_16x16x32_bf16 v[196:199], a[40:43], a[144:147], v[196:199]\n"
"v_cvt_pk_bf16_f32 v30, v12, v13\n"
"v_cvt_pk_bf16_f32 v31, v14, v15\n"
"v_mfma_f32_16x16x32_bf16 a[20:23], a[40:43], a[152:155], a[20:23]\n"
"buffer_store_dwordx4 v[80:83], v6, s[52:55], s91 offen offset:3072 nt\n"
"ds_write_b64 v8, v[30:31] offset:36872\n"
"v_mfma_f32_16x16x32_bf16 a[20:23], a[44:47], a[156:159], a[20:23]\n"
"ds_read_b128 v[56:59], v2 offset:36864\n"
"v_mfma_f32_16x16x32_bf16 a[16:19], a[36:39], a[156:159], a[16:19]\n"
"ds_read_b128 v[84:87], v2 offset:37888\n"
"v_mfma_f32_16x16x32_bf16 a[16:19], a[32:35], a[152:155], a[16:19]\n"
"s_waitcnt vmcnt(16)\n"
"v_accvgpr_read_b32 v16, a184\n"
"v_mfma_f32_16x16x32_bf16 v[200:203], a[48:51], a[96:99], v[200:203]\n"
"v_accvgpr_read_b32 v17, a185\n"
"v_mfma_f32_16x16x32_bf16 v[200:203], a[52:55], a[100:103], v[200:203]\n"
"v_accvgpr_read_b32 v18, a186\n"
"v_mfma_f32_16x16x32_bf16 v[228:231], a[60:63], a[100:103], v[228:231]\n"
"ds_read_b128 a[100:103], v2 offset:17408\n"
"v_mfma_f32_16x16x32_bf16 v[228:231], a[56:59], a[96:99], v[228:231]\n"
"ds_read_b128 a[96:99], v2 offset:16384\n"
"v_accvgpr_read_b32 v19, a187\n"
"v_mfma_f32_16x16x32_bf16 v[232:235], a[56:59], a[104:107], v[232:235]\n"
"v_lshlrev_b32 v12, 16, v16\n"
"v_and_b32 v13, s63, v16\n"
"v_mfma_f32_16x16x32_bf16 v[232:235], a[60:63], a[108:111], v[232:235]\n"
"v_lshlrev_b32 v14, 16, v17\n"
"v_and_b32 v15, s63, v17\n"
"v_mfma_f32_16x16x32_bf16 v[204:207], a[52:55], a[108:111], v[204:207]\n"
"ds_read_b128 a[108:111], v3 offset:17408\n"
"v_accvgpr_read_b32 v10, a0\n"
"v_mfma_f32_16x16x32_bf16 v[204:207], a[48:51], a[104:107], v[204:207]\n"
"ds_read_b128 a[104:107], v3 offset:16384\n"
"v_accvgpr_read_b32 v11, a1\n"
"v_mfma_f32_16x16x32_bf16 v[208:211], a[48:51], a[112:115], v[208:211]\n"
"v_mul_f32 v12, v12, v10\n"
"v_mul_f32 v13, v13, v11\n"
"v_mfma_f32_16x16x32_bf16 v[208:211], a[52:55], a[116:119], v[208:211]\n"
"v_accvgpr_read_b32 v10, a2\n"
"v_accvgpr_read_b32 v11, a3\n"
"v_mfma_f32_16x16x32_bf16 v[236:239], a[60:63], a[116:119], v[236:239]\n"
"ds_read_b128 a[116:119], v4 offset:17408\n"
"v_mul_f32 v14, v14, v10\n"
"v_mfma_f32_16x16x32_bf16 v[236:239], a[56:59], a[112:115], v[236:239]\n"
"ds_read_b128 a[112:115], v4 offset:16384\n"
"v_mul_f32 v15, v15, v11\n"
"v_mfma_f32_16x16x32_bf16 v[240:243], a[56:59], a[120:123], v[240:243]\n"
"v_cvt_pk_bf16_f32 v24, v12, v13\n"
"v_cvt_pk_bf16_f32 v25, v14, v15\n"
"v_mfma_f32_16x16x32_bf16 v[240:243], a[60:63], a[124:127], v[240:243]\n"
"ds_write_b64 v7, v[24:25] offset:38912\n"
"v_mfma_f32_16x16x32_bf16 v[212:215], a[52:55], a[124:127], v[212:215]\n"
"ds_read_b128 a[124:127], v2 offset:19456\n"
"v_lshlrev_b32 v12, 16, v18\n"
"v_mfma_f32_16x16x32_bf16 v[212:215], a[48:51], a[120:123], v[212:215]\n"
"ds_read_b128 a[120:123], v2 offset:18432\n"
"v_and_b32 v13, s63, v18\n"
"v_mfma_f32_16x16x32_bf16 v[216:219], a[48:51], a[128:131], v[216:219]\n"
"v_lshlrev_b32 v14, 16, v19\n"
"v_and_b32 v15, s63, v19\n"
"v_mfma_f32_16x16x32_bf16 v[216:219], a[52:55], a[132:135], v[216:219]\n"
"v_accvgpr_read_b32 v10, a4\n"
"v_accvgpr_read_b32 v11, a5\n"
"v_mfma_f32_16x16x32_bf16 v[244:247], a[60:63], a[132:135], v[244:247]\n"
"ds_read_b128 a[132:135], v3 offset:19456\n"
"v_mul_f32 v12, v12, v10\n"
"v_mfma_f32_16x16x32_bf16 v[244:247], a[56:59], a[128:131], v[244:247]\n"
"ds_read_b128 a[128:131], v3 offset:18432\n"
"v_mul_f32 v13, v13, v11\n"
"v_mfma_f32_16x16x32_bf16 v[248:251], a[56:59], a[136:139], v[248:251]\n"
"v_accvgpr_read_b32 v10, a6\n"
"v_accvgpr_read_b32 v11, a7\n"
"v_mfma_f32_16x16x32_bf16 v[248:251], a[60:63], a[140:143], v[248:251]\n"
"v_mul_f32 v14, v14, v10\n"
"v_mul_f32 v15, v15, v11\n"
"v_mfma_f32_16x16x32_bf16 v[220:223], a[52:55], a[140:143], v[220:223]\n"
"ds_read_b128 a[140:143], v4 offset:19456\n"
"v_cvt_pk_bf16_f32 v26, v12, v13\n"
"v_mfma_f32_16x16x32_bf16 v[220:223], a[48:51], a[136:139], v[220:223]\n"
"ds_read_b128 a[136:139], v4 offset:18432\n"
"v_cvt_pk_bf16_f32 v27, v14, v15\n"
"v_mfma_f32_16x16x32_bf16 v[224:227], a[48:51], a[144:147], v[224:227]\n"
"ds_write_b64 v7, v[26:27] offset:38920\n"
"v_mfma_f32_16x16x32_bf16 v[224:227], a[52:55], a[148:151], v[224:227]\n"
"v_accvgpr_read_b32 v16, a188\n"
"v_mfma_f32_16x16x32_bf16 v[252:255], a[60:63], a[148:151], v[252:255]\n"
"s_waitcnt lgkmcnt(15)\n"
"buffer_store_dwordx4 v[56:59], v6, s[52:55], s92 offen offset:0 nt\n"
"ds_read_b128 a[148:151], v2 offset:21504\n"
"v_accvgpr_read_b32 v17, a189\n"
"v_accvgpr_read_b32 v18, a190\n"
"v_mfma_f32_16x16x32_bf16 v[252:255], a[56:59], a[144:147], v[252:255]\n"
"ds_read_b128 a[144:147], v2 offset:20480\n"
"v_accvgpr_read_b32 v19, a191\n"
"v_mfma_f32_16x16x32_bf16 a[28:31], a[56:59], a[152:155], a[28:31]\n"
"v_lshlrev_b32 v12, 16, v16\n"
"v_and_b32 v13, s63, v16\n"
"v_mfma_f32_16x16x32_bf16 a[28:31], a[60:63], a[156:159], a[28:31]\n"
"v_lshlrev_b32 v14, 16, v17\n"
"v_and_b32 v15, s63, v17\n"
"v_mfma_f32_16x16x32_bf16 a[24:27], a[52:55], a[156:159], a[24:27]\n"
"ds_read_b128 a[156:159], v3 offset:21504\n"
"v_accvgpr_read_b32 v10, a8\n"
"v_mfma_f32_16x16x32_bf16 a[24:27], a[48:51], a[152:155], a[24:27]\n"
"s_waitcnt lgkmcnt(15)\n"
"ds_read_b128 a[152:155], v3 offset:20480\n"
"v_accvgpr_read_b32 v11, a9\n"
"v_mfma_f32_16x16x32_bf16 v[144:147], v[88:91], a[96:99], v[144:147]\n"
"v_mul_f32 v12, v12, v10\n"
"v_mul_f32 v13, v13, v11\n"
"v_mfma_f32_16x16x32_bf16 v[144:147], v[116:119], a[100:103], v[144:147]\n"
"v_accvgpr_read_b32 v10, a10\n"
"v_accvgpr_read_b32 v11, a11\n"
"v_mfma_f32_16x16x32_bf16 v[172:175], v[120:123], a[100:103], v[172:175]\n"
"v_mul_f32 v14, v14, v10\n"
"v_mul_f32 v15, v15, v11\n"
"v_mfma_f32_16x16x32_bf16 v[172:175], v[92:95], a[96:99], v[172:175]\n"
"v_cvt_pk_bf16_f32 v28, v12, v13\n"
"v_cvt_pk_bf16_f32 v29, v14, v15\n"
"v_mfma_f32_16x16x32_bf16 v[200:203], v[32:35], a[96:99], v[200:203]\n"
"buffer_store_dwordx4 v[84:87], v6, s[52:55], s93 offen offset:1024 nt\n"
"ds_write_b64 v8, v[28:29] offset:38912\n"
"v_mfma_f32_16x16x32_bf16 v[200:203], v[60:63], a[100:103], v[200:203]\n"
"v_lshlrev_b32 v12, 16, v18\n"
"v_mfma_f32_16x16x32_bf16 v[228:231], v[124:127], a[100:103], v[228:231]\n"
"ds_read_b128 a[100:103], v4 offset:21504\n"
"v_mfma_f32_16x16x32_bf16 v[228:231], v[96:99], a[96:99], v[228:231]\n"
"s_waitcnt vmcnt(3) lgkmcnt(15)\n"
"ds_read_b128 a[96:99], v4 offset:20480\n"
"v_mfma_f32_16x16x32_bf16 v[228:231], v[108:111], a[104:107], v[228:231]\n"
"v_and_b32 v13, s63, v18\n"
"v_mfma_f32_16x16x32_bf16 v[228:231], v[136:139], a[108:111], v[228:231]\n"
"v_lshlrev_b32 v14, 16, v19\n"
"v_mfma_f32_16x16x32_bf16 v[200:203], v[132:135], a[108:111], v[200:203]\n"
"v_and_b32 v15, s63, v19\n"
"v_mfma_f32_16x16x32_bf16 v[200:203], v[104:107], a[104:107], v[200:203]\n"
"v_accvgpr_read_b32 v10, a12\n"
"v_mfma_f32_16x16x32_bf16 v[172:175], v[36:39], a[104:107], v[172:175]\n"
"v_accvgpr_read_b32 v11, a13\n"
"v_mfma_f32_16x16x32_bf16 v[172:175], v[64:67], a[108:111], v[172:175]\n"
"v_mul_f32 v12, v12, v10\n"
"v_mfma_f32_16x16x32_bf16 v[144:147], v[128:131], a[108:111], v[144:147]\n"
"ds_read_b128 a[108:111], v2 offset:23552\n"
"v_mfma_f32_16x16x32_bf16 v[144:147], v[100:103], a[104:107], v[144:147]\n"
"s_waitcnt lgkmcnt(15)\n"
"ds_read_b128 a[104:107], v2 offset:22528\n"
"v_mfma_f32_16x16x32_bf16 v[144:147], a[64:67], a[112:115], v[144:147]\n"
"v_mul_f32 v13, v13, v11\n"
"v_mfma_f32_16x16x32_bf16 v[144:147], a[68:71], a[116:119], v[144:147]\n"
"v_accvgpr_read_b32 v10, a14\n"
"v_accvgpr_read_b32 v11, a15\n"
"v_mfma_f32_16x16x32_bf16 v[172:175], a[76:79], a[116:119], v[172:175]\n"
"v_mul_f32 v14, v14, v10\n"
"v_mul_f32 v15, v15, v11\n"
"v_mfma_f32_16x16x32_bf16 v[172:175], a[72:75], a[112:115], v[172:175]\n"
"v_cvt_pk_bf16_f32 v30, v12, v13\n"
"v_cvt_pk_bf16_f32 v31, v14, v15\n"
"v_mfma_f32_16x16x32_bf16 v[200:203], a[80:83], a[112:115], v[200:203]\n"
"ds_write_b64 v8, v[30:31] offset:38920\n"
"v_mfma_f32_16x16x32_bf16 v[200:203], a[84:87], a[116:119], v[200:203]\n"
"ds_read_b128 v[112:115], v2 offset:38912\n"
"v_mfma_f32_16x16x32_bf16 v[228:231], a[92:95], a[116:119], v[228:231]\n"
"ds_read_b128 a[116:119], v3 offset:23552\n"
"v_mfma_f32_16x16x32_bf16 v[228:231], a[88:91], a[112:115], v[228:231]\n"
"s_waitcnt lgkmcnt(15)\n"
"ds_read_b128 a[112:115], v3 offset:22528\n"
"v_mfma_f32_16x16x32_bf16 v[232:235], v[96:99], a[120:123], v[232:235]\n"
"ds_read_b128 v[140:143], v2 offset:39936\n"
"v_mfma_f32_16x16x32_bf16 v[232:235], v[124:127], a[124:127], v[232:235]\n"
"v_accvgpr_read_b32 v16, a192\n"
"v_accvgpr_read_b32 v17, a193\n"
"v_mfma_f32_16x16x32_bf16 v[204:207], v[60:63], a[124:127], v[204:207]\n"
"v_accvgpr_read_b32 v18, a194\n"
"v_accvgpr_read_b32 v19, a195\n"
"v_mfma_f32_16x16x32_bf16 v[204:207], v[32:35], a[120:123], v[204:207]\n"
"v_lshlrev_b32 v12, 16, v16\n"
"v_and_b32 v13, s63, v16\n"
"v_mfma_f32_16x16x32_bf16 v[176:179], v[92:95], a[120:123], v[176:179]\n"
"v_lshlrev_b32 v14, 16, v17\n"
"v_and_b32 v15, s63, v17\n"
"v_mfma_f32_16x16x32_bf16 v[176:179], v[120:123], a[124:127], v[176:179]\n"
"v_mul_f32 v12, v12, v144\n"
"v_mul_f32 v13, v13, v145\n"
"v_mfma_f32_16x16x32_bf16 v[148:151], v[116:119], a[124:127], v[148:151]\n"
"ds_read_b128 a[124:127], v4 offset:23552\n"
"v_mul_f32 v14, v14, v146\n"
"v_mfma_f32_16x16x32_bf16 v[148:151], v[88:91], a[120:123], v[148:151]\n"
"ds_read_b128 a[120:123], v4 offset:22528\n"
"v_mul_f32 v15, v15, v147\n"
"v_mfma_f32_16x16x32_bf16 v[148:151], v[100:103], a[128:131], v[148:151]\n"
"v_cvt_pk_bf16_f32 v24, v12, v13\n"
"v_cvt_pk_bf16_f32 v25, v14, v15\n"
"v_mfma_f32_16x16x32_bf16 v[148:151], v[128:131], a[132:135], v[148:151]\n"
"ds_write_b64 v7, v[24:25] offset:32768\n"
"v_mfma_f32_16x16x32_bf16 v[176:179], v[64:67], a[132:135], v[176:179]\n"
"v_lshlrev_b32 v12, 16, v18\n"
"v_and_b32 v13, s63, v18\n"
"v_mfma_f32_16x16x32_bf16 v[176:179], v[36:39], a[128:131], v[176:179]\n"
"v_lshlrev_b32 v14, 16, v19\n"
"v_and_b32 v15, s63, v19\n"
"v_mfma_f32_16x16x32_bf16 v[204:207], v[104:107], a[128:131], v[204:207]\n"
"s_waitcnt lgkmcnt(6)\n"
"buffer_store_dwordx4 v[112:115], v6, s[52:55], s94 offen offset:2048 nt\n"
"v_mul_f32 v12, v12, v172\n"
"v_mul_f32 v13, v13, v173\n"
"v_mul_f32 v14, v14, v174\n"
"v_mfma_f32_16x16x32_bf16 v[204:207], v[132:135], a[132:135], v[204:207]\n"
"v_mul_f32 v15, v15, v175\n"
"v_mfma_f32_16x16x32_bf16 v[232:235], v[136:139], a[132:135], v[232:235]\n"
"ds_read_b128 a[132:135], v2 offset:25600\n"
"v_cvt_pk_bf16_f32 v26, v12, v13\n"
"v_mfma_f32_16x16x32_bf16 v[232:235], v[108:111], a[128:131], v[232:235]\n"
"ds_read_b128 a[128:131], v2 offset:24576\n"
"v_cvt_pk_bf16_f32 v27, v14, v15\n"
"v_mfma_f32_16x16x32_bf16 v[232:235], a[88:91], a[136:139], v[232:235]\n"
"ds_write_b64 v7, v[26:27] offset:32776\n"
"v_mfma_f32_16x16x32_bf16 v[232:235], a[92:95], a[140:143], v[232:235]\n"
"v_accvgpr_read_b32 v16, a196\n"
"v_accvgpr_read_b32 v17, a197\n"
"v_mfma_f32_16x16x32_bf16 v[204:207], a[84:87], a[140:143], v[204:207]\n"
"v_accvgpr_read_b32 v18, a198\n"
"v_accvgpr_read_b32 v19, a199\n"
"v_mfma_f32_16x16x32_bf16 v[204:207], a[80:83], a[136:139], v[204:207]\n"
"v_lshlrev_b32 v12, 16, v16\n"
"v_and_b32 v13, s63, v16\n"
"v_mfma_f32_16x16x32_bf16 v[176:179], a[72:75], a[136:139], v[176:179]\n"
"v_lshlrev_b32 v14, 16, v17\n"
"v_and_b32 v15, s63, v17\n"
"v_mfma_f32_16x16x32_bf16 v[176:179], a[76:79], a[140:143], v[176:179]\n"
"v_mul_f32 v12, v12, v200\n"
"v_mul_f32 v13, v13, v201\n"
"v_mfma_f32_16x16x32_bf16 v[148:151], a[68:71], a[140:143], v[148:151]\n"
"s_waitcnt lgkmcnt(6)\n"
"buffer_store_dwordx4 v[140:143], v6, s[52:55], s95 offen offset:3072 nt\n"
"ds_read_b128 a[140:143], v3 offset:25600\n"
"v_mul_f32 v14, v14, v202\n"
"v_mul_f32 v15, v15, v203\n"
"v_mfma_f32_16x16x32_bf16 v[148:151], a[64:67], a[136:139], v[148:151]\n"
"ds_read_b128 a[136:139], v3 offset:24576\n"
"v_cvt_pk_bf16_f32 v28, v12, v13\n"
"v_mfma_f32_16x16x32_bf16 v[152:155], v[88:91], a[144:147], v[152:155]\n"
"v_cvt_pk_bf16_f32 v29, v14, v15\n"
"v_mfma_f32_16x16x32_bf16 v[152:155], v[116:119], a[148:151], v[152:155]\n"
"ds_write_b64 v8, v[28:29] offset:32768\n"
"v_mfma_f32_16x16x32_bf16 v[180:183], v[120:123], a[148:151], v[180:183]\n"
"v_lshlrev_b32 v12, 16, v18\n"
"v_and_b32 v13, s63, v18\n"
"v_mfma_f32_16x16x32_bf16 v[180:183], v[92:95], a[144:147], v[180:183]\n"
"v_lshlrev_b32 v14, 16, v19\n"
"v_and_b32 v15, s63, v19\n"
"v_mfma_f32_16x16x32_bf16 v[208:211], v[32:35], a[144:147], v[208:211]\n"
"v_mul_f32 v12, v12, v228\n"
"v_mul_f32 v13, v13, v229\n"
"v_mfma_f32_16x16x32_bf16 v[208:211], v[60:63], a[148:151], v[208:211]\n"
"v_mul_f32 v14, v14, v230\n"
"v_mul_f32 v15, v15, v231\n"
"v_mfma_f32_16x16x32_bf16 v[236:239], v[124:127], a[148:151], v[236:239]\n"
"ds_read_b128 a[148:151], v4 offset:25600\n"
"v_cvt_pk_bf16_f32 v30, v12, v13\n"
"v_mfma_f32_16x16x32_bf16 v[236:239], v[96:99], a[144:147], v[236:239]\n"
"ds_read_b128 a[144:147], v4 offset:24576\n"
"v_cvt_pk_bf16_f32 v31, v14, v15\n"
"v_mfma_f32_16x16x32_bf16 v[236:239], v[108:111], a[152:155], v[236:239]\n"
"ds_write_b64 v8, v[30:31] offset:32776\n"
"v_mfma_f32_16x16x32_bf16 v[236:239], v[136:139], a[156:159], v[236:239]\n"
"ds_read_b128 v[144:147], v2 offset:32768\n"
"v_mfma_f32_16x16x32_bf16 v[208:211], v[132:135], a[156:159], v[208:211]\n"
"ds_read_b128 v[172:175], v2 offset:33792\n"
"v_accvgpr_read_b32 v16, a200\n"
"v_accvgpr_read_b32 v17, a201\n"
"v_mfma_f32_16x16x32_bf16 v[208:211], v[104:107], a[152:155], v[208:211]\n"
"v_accvgpr_read_b32 v18, a202\n"
"v_accvgpr_read_b32 v19, a203\n"
"v_mfma_f32_16x16x32_bf16 v[180:183], v[36:39], a[152:155], v[180:183]\n"
"v_lshlrev_b32 v12, 16, v16\n"
"v_and_b32 v13, s63, v16\n"
"v_mfma_f32_16x16x32_bf16 v[180:183], v[64:67], a[156:159], v[180:183]\n"
"v_lshlrev_b32 v14, 16, v17\n"
"v_and_b32 v15, s63, v17\n"
"v_mfma_f32_16x16x32_bf16 v[152:155], v[128:131], a[156:159], v[152:155]\n"
"ds_read_b128 a[156:159], v2 offset:27648\n"
"v_mul_f32 v12, v12, v148\n"
"v_mfma_f32_16x16x32_bf16 v[152:155], v[100:103], a[152:155], v[152:155]\n"
"ds_read_b128 a[152:155], v2 offset:26624\n"
"v_mul_f32 v13, v13, v149\n"
"v_mfma_f32_16x16x32_bf16 v[152:155], a[64:67], a[96:99], v[152:155]\n"
"v_mul_f32 v14, v14, v150\n"
"v_mul_f32 v15, v15, v151\n"
"v_mfma_f32_16x16x32_bf16 v[152:155], a[68:71], a[100:103], v[152:155]\n"
"v_cvt_pk_bf16_f32 v24, v12, v13\n"
"v_cvt_pk_bf16_f32 v25, v14, v15\n"
"v_mfma_f32_16x16x32_bf16 v[180:183], a[76:79], a[100:103], v[180:183]\n"
"s_waitcnt lgkmcnt(3)\n"
"buffer_store_dwordx4 v[144:147], v6, s[52:55], s80 offen offset:128 nt\n"
"ds_write_b64 v7, v[24:25] offset:34816\n"
"v_mfma_f32_16x16x32_bf16 v[180:183], a[72:75], a[96:99], v[180:183]\n"
"v_lshlrev_b32 v12, 16, v18\n"
"v_and_b32 v13, s63, v18\n"
"v_mfma_f32_16x16x32_bf16 v[208:211], a[80:83], a[96:99], v[208:211]\n"
"v_lshlrev_b32 v14, 16, v19\n"
"v_and_b32 v15, s63, v19\n"
"v_mfma_f32_16x16x32_bf16 v[208:211], a[84:87], a[100:103], v[208:211]\n"
"v_mul_f32 v12, v12, v176\n"
"v_mul_f32 v13, v13, v177\n"
"v_mfma_f32_16x16x32_bf16 v[236:239], a[92:95], a[100:103], v[236:239]\n"
"ds_read_b128 a[100:103], v3 offset:27648\n"
"v_mul_f32 v14, v14, v178\n"
"v_mfma_f32_16x16x32_bf16 v[236:239], a[88:91], a[96:99], v[236:239]\n"
"ds_read_b128 a[96:99], v3 offset:26624\n"
"v_mul_f32 v15, v15, v179\n"
"v_mfma_f32_16x16x32_bf16 v[240:243], v[96:99], a[104:107], v[240:243]\n"
"v_cvt_pk_bf16_f32 v26, v12, v13\n"
"v_cvt_pk_bf16_f32 v27, v14, v15\n"
"v_mfma_f32_16x16x32_bf16 v[240:243], v[124:127], a[108:111], v[240:243]\n"
"ds_write_b64 v7, v[26:27] offset:34824\n"
"v_mfma_f32_16x16x32_bf16 v[212:215], v[60:63], a[108:111], v[212:215]\n"
"v_accvgpr_read_b32 v16, a204\n"
"v_accvgpr_read_b32 v17, a205\n"
"v_mfma_f32_16x16x32_bf16 v[212:215], v[32:35], a[104:107], v[212:215]\n"
"v_accvgpr_read_b32 v18, a206\n"
"v_accvgpr_read_b32 v19, a207\n"
"v_mfma_f32_16x16x32_bf16 v[184:187], v[92:95], a[104:107], v[184:187]\n"
"s_waitcnt lgkmcnt(6)\n"
"buffer_store_dwordx4 v[172:175], v6, s[52:55], s81 offen offset:1152 nt\n"
"v_lshlrev_b32 v12, 16, v16\n"
"v_and_b32 v13, s63, v16\n"
"v_lshlrev_b32 v14, 16, v17\n"
"v_and_b32 v15, s63, v17\n"
"v_mfma_f32_16x16x32_bf16 v[184:187], v[120:123], a[108:111], v[184:187]\n"
"v_mul_f32 v12, v12, v204\n"
"v_mul_f32 v13, v13, v205\n"
"v_mfma_f32_16x16x32_bf16 v[156:159], v[116:119], a[108:111], v[156:159]\n"
"ds_read_b128 a[108:111], v4 offset:27648\n"
"v_mul_f32 v14, v14, v206\n"
"v_mfma_f32_16x16x32_bf16 v[156:159], v[88:91], a[104:107], v[156:159]\n"
"ds_read_b128 a[104:107], v4 offset:26624\n"
"v_mul_f32 v15, v15, v207\n"
"v_mfma_f32_16x16x32_bf16 v[156:159], v[100:103], a[112:115], v[156:159]\n"
"v_cvt_pk_bf16_f32 v28, v12, v13\n"
"v_cvt_pk_bf16_f32 v29, v14, v15\n"
"v_mfma_f32_16x16x32_bf16 v[156:159], v[128:131], a[116:119], v[156:159]\n"
"ds_write_b64 v8, v[28:29] offset:34816\n"
"v_mfma_f32_16x16x32_bf16 v[184:187], v[64:67], a[116:119], v[184:187]\n"
"v_lshlrev_b32 v12, 16, v18\n"
"v_and_b32 v13, s63, v18\n"
"v_lshlrev_b32 v14, 16, v19\n"
"v_mfma_f32_16x16x32_bf16 v[184:187], v[36:39], a[112:115], v[184:187]\n"
"v_and_b32 v15, s63, v19\n"
"v_mul_f32 v12, v12, v232\n"
"v_mfma_f32_16x16x32_bf16 v[212:215], v[104:107], a[112:115], v[212:215]\n"
"v_mul_f32 v13, v13, v233\n"
"v_mul_f32 v14, v14, v234\n"
"v_mfma_f32_16x16x32_bf16 v[212:215], v[132:135], a[116:119], v[212:215]\n"
"v_mul_f32 v15, v15, v235\n"
"v_cvt_pk_bf16_f32 v30, v12, v13\n"
"v_mfma_f32_16x16x32_bf16 v[240:243], v[136:139], a[116:119], v[240:243]\n"
"ds_read_b128 a[116:119], v2 offset:29696\n"
"v_cvt_pk_bf16_f32 v31, v14, v15\n"
"v_mfma_f32_16x16x32_bf16 v[240:243], v[108:111], a[112:115], v[240:243]\n"
"ds_read_b128 a[112:115], v2 offset:28672\n"
"ds_write_b64 v8, v[30:31] offset:34824\n"
"v_mfma_f32_16x16x32_bf16 v[240:243], a[88:91], a[120:123], v[240:243]\n"
"ds_read_b128 v[148:151], v2 offset:34816\n"
"v_mfma_f32_16x16x32_bf16 v[240:243], a[92:95], a[124:127], v[240:243]\n"
"ds_read_b128 v[176:179], v2 offset:35840\n"
"v_accvgpr_read_b32 v16, a208\n"
"v_accvgpr_read_b32 v17, a209\n"
"v_mfma_f32_16x16x32_bf16 v[212:215], a[84:87], a[124:127], v[212:215]\n"
"v_accvgpr_read_b32 v18, a210\n"
"v_accvgpr_read_b32 v19, a211\n"
"v_mfma_f32_16x16x32_bf16 v[212:215], a[80:83], a[120:123], v[212:215]\n"
"v_lshlrev_b32 v12, 16, v16\n"
"v_and_b32 v13, s63, v16\n"
"v_mfma_f32_16x16x32_bf16 v[184:187], a[72:75], a[120:123], v[184:187]\n"
"v_lshlrev_b32 v14, 16, v17\n"
"v_and_b32 v15, s63, v17\n"
"v_mfma_f32_16x16x32_bf16 v[184:187], a[76:79], a[124:127], v[184:187]\n"
"v_mul_f32 v12, v12, v152\n"
"v_mul_f32 v13, v13, v153\n"
"v_mfma_f32_16x16x32_bf16 v[156:159], a[68:71], a[124:127], v[156:159]\n"
"ds_read_b128 a[124:127], v3 offset:29696\n"
"v_mul_f32 v14, v14, v154\n"
"v_mfma_f32_16x16x32_bf16 v[156:159], a[64:67], a[120:123], v[156:159]\n"
"ds_read_b128 a[120:123], v3 offset:28672\n"
"v_mul_f32 v15, v15, v155\n"
"v_mfma_f32_16x16x32_bf16 v[160:163], v[88:91], a[128:131], v[160:163]\n"
"v_cvt_pk_bf16_f32 v24, v12, v13\n"
"v_cvt_pk_bf16_f32 v25, v14, v15\n"
"v_mfma_f32_16x16x32_bf16 v[160:163], v[116:119], a[132:135], v[160:163]\n"
"s_waitcnt lgkmcnt(3)\n"
"buffer_store_dwordx4 v[148:151], v6, s[52:55], s82 offen offset:2176 nt\n"
"ds_write_b64 v7, v[24:25] offset:36864\n"
"v_mfma_f32_16x16x32_bf16 v[188:191], v[120:123], a[132:135], v[188:191]\n"
"v_lshlrev_b32 v12, 16, v18\n"
"v_and_b32 v13, s63, v18\n"
"v_mfma_f32_16x16x32_bf16 v[188:191], v[92:95], a[128:131], v[188:191]\n"
"v_lshlrev_b32 v14, 16, v19\n"
"v_and_b32 v15, s63, v19\n"
"v_mfma_f32_16x16x32_bf16 v[216:219], v[32:35], a[128:131], v[216:219]\n"
"v_mul_f32 v12, v12, v180\n"
"v_mul_f32 v13, v13, v181\n"
"v_mfma_f32_16x16x32_bf16 v[216:219], v[60:63], a[132:135], v[216:219]\n"
"v_mul_f32 v14, v14, v182\n"
"v_mul_f32 v15, v15, v183\n"
"v_mfma_f32_16x16x32_bf16 v[244:247], v[124:127], a[132:135], v[244:247]\n"
"ds_read_b128 a[132:135], v4 offset:29696\n"
"v_cvt_pk_bf16_f32 v26, v12, v13\n"
"v_mfma_f32_16x16x32_bf16 v[244:247], v[96:99], a[128:131], v[244:247]\n"
"ds_read_b128 a[128:131], v4 offset:28672\n"
"v_cvt_pk_bf16_f32 v27, v14, v15\n"
"v_mfma_f32_16x16x32_bf16 v[244:247], v[108:111], a[136:139], v[244:247]\n"
"ds_write_b64 v7, v[26:27] offset:36872\n"
"v_mfma_f32_16x16x32_bf16 v[244:247], v[136:139], a[140:143], v[244:247]\n"
"v_accvgpr_read_b32 v16, a212\n"
"v_accvgpr_read_b32 v17, a213\n"
"v_mfma_f32_16x16x32_bf16 v[216:219], v[132:135], a[140:143], v[216:219]\n"
"v_accvgpr_read_b32 v18, a214\n"
"v_accvgpr_read_b32 v19, a215\n"
"v_mfma_f32_16x16x32_bf16 v[216:219], v[104:107], a[136:139], v[216:219]\n"
"s_waitcnt lgkmcnt(6)\n"
"buffer_store_dwordx4 v[176:179], v6, s[52:55], s83 offen offset:3200 nt\n"
"v_lshlrev_b32 v12, 16, v16\n"
"v_and_b32 v13, s63, v16\n"
"v_lshlrev_b32 v14, 16, v17\n"
"v_and_b32 v15, s63, v17\n"
"v_mfma_f32_16x16x32_bf16 v[188:191], v[36:39], a[136:139], v[188:191]\n"
"v_mul_f32 v12, v12, v208\n"
"v_mul_f32 v13, v13, v209\n"
"v_mfma_f32_16x16x32_bf16 v[188:191], v[64:67], a[140:143], v[188:191]\n"
"v_mul_f32 v14, v14, v210\n"
"v_mul_f32 v15, v15, v211\n"
"v_mfma_f32_16x16x32_bf16 v[160:163], v[128:131], a[140:143], v[160:163]\n"
"ds_read_b128 a[140:143], v2 offset:31744\n"
"v_cvt_pk_bf16_f32 v28, v12, v13\n"
"v_mfma_f32_16x16x32_bf16 v[160:163], v[100:103], a[136:139], v[160:163]\n"
"ds_read_b128 a[136:139], v2 offset:30720\n"
"v_cvt_pk_bf16_f32 v29, v14, v15\n"
"v_mfma_f32_16x16x32_bf16 v[160:163], a[64:67], a[144:147], v[160:163]\n"
"ds_write_b64 v8, v[28:29] offset:36864\n"
"v_mfma_f32_16x16x32_bf16 v[160:163], a[68:71], a[148:151], v[160:163]\n"
"v_lshlrev_b32 v12, 16, v18\n"
"v_and_b32 v13, s63, v18\n"
"v_lshlrev_b32 v14, 16, v19\n"
"v_mfma_f32_16x16x32_bf16 v[188:191], a[76:79], a[148:151], v[188:191]\n"
"v_and_b32 v15, s63, v19\n"
"v_mul_f32 v12, v12, v236\n"
"v_mul_f32 v13, v13, v237\n"
"v_mfma_f32_16x16x32_bf16 v[188:191], a[72:75], a[144:147], v[188:191]\n"
"v_mul_f32 v14, v14, v238\n"
"v_mul_f32 v15, v15, v239\n"
"v_mfma_f32_16x16x32_bf16 v[216:219], a[80:83], a[144:147], v[216:219]\n"
"v_cvt_pk_bf16_f32 v30, v12, v13\n"
"v_cvt_pk_bf16_f32 v31, v14, v15\n"
"v_mfma_f32_16x16x32_bf16 v[216:219], a[84:87], a[148:151], v[216:219]\n"
"ds_write_b64 v8, v[30:31] offset:36872\n"
"v_mfma_f32_16x16x32_bf16 v[244:247], a[92:95], a[148:151], v[244:247]\n"
"ds_read_b128 a[148:151], v3 offset:31744\n"
"ds_read_b128 v[152:155], v2 offset:36864\n"
"v_mfma_f32_16x16x32_bf16 v[244:247], a[88:91], a[144:147], v[244:247]\n"
"ds_read_b128 a[144:147], v3 offset:30720\n"
"v_mfma_f32_16x16x32_bf16 v[248:251], v[96:99], a[152:155], v[248:251]\n"
"ds_read_b128 v[180:183], v2 offset:37888\n"
"v_accvgpr_read_b32 v16, a216\n"
"v_mfma_f32_16x16x32_bf16 v[248:251], v[124:127], a[156:159], v[248:251]\n"
"v_accvgpr_read_b32 v17, a217\n"
"v_accvgpr_read_b32 v18, a218\n"
"v_mfma_f32_16x16x32_bf16 v[220:223], v[60:63], a[156:159], v[220:223]\n"
"v_accvgpr_read_b32 v19, a219\n"
"v_lshlrev_b32 v12, 16, v16\n"
"v_mfma_f32_16x16x32_bf16 v[220:223], v[32:35], a[152:155], v[220:223]\n"
"v_and_b32 v13, s63, v16\n"
"v_lshlrev_b32 v14, 16, v17\n"
"v_mfma_f32_16x16x32_bf16 v[192:195], v[92:95], a[152:155], v[192:195]\n"
"v_and_b32 v15, s63, v17\n"
"v_mul_f32 v12, v12, v156\n"
"v_mfma_f32_16x16x32_bf16 v[192:195], v[120:123], a[156:159], v[192:195]\n"
"v_mul_f32 v13, v13, v157\n"
"v_mul_f32 v14, v14, v158\n"
"v_mfma_f32_16x16x32_bf16 v[164:167], v[116:119], a[156:159], v[164:167]\n"
"ds_read_b128 a[156:159], v4 offset:31744\n"
"v_mul_f32 v15, v15, v159\n"
"v_mfma_f32_16x16x32_bf16 v[164:167], v[88:91], a[152:155], v[164:167]\n"
"s_waitcnt lgkmcnt(3)\n"
"buffer_store_dwordx4 v[152:155], v6, s[52:55], s84 offen offset:128 nt\n"
"ds_read_b128 a[152:155], v4 offset:30720\n"
"v_cvt_pk_bf16_f32 v24, v12, v13\n"
"v_cvt_pk_bf16_f32 v25, v14, v15\n"
"v_mfma_f32_16x16x32_bf16 v[164:167], v[100:103], a[96:99], v[164:167]\n"
"ds_write_b64 v7, v[24:25] offset:38912\n"
"v_mfma_f32_16x16x32_bf16 v[164:167], v[128:131], a[100:103], v[164:167]\n"
"v_lshlrev_b32 v12, 16, v18\n"
"v_and_b32 v13, s63, v18\n"
"v_mfma_f32_16x16x32_bf16 v[192:195], v[64:67], a[100:103], v[192:195]\n"
"v_lshlrev_b32 v14, 16, v19\n"
"v_and_b32 v15, s63, v19\n"
"v_mfma_f32_16x16x32_bf16 v[192:195], v[36:39], a[96:99], v[192:195]\n"
"v_mul_f32 v12, v12, v184\n"
"v_mul_f32 v13, v13, v185\n"
"v_mfma_f32_16x16x32_bf16 v[220:223], v[104:107], a[96:99], v[220:223]\n"
"v_mul_f32 v14, v14, v186\n"
"v_mul_f32 v15, v15, v187\n"
"v_mfma_f32_16x16x32_bf16 v[220:223], v[132:135], a[100:103], v[220:223]\n"
"v_cvt_pk_bf16_f32 v26, v12, v13\n"
"v_cvt_pk_bf16_f32 v27, v14, v15\n"
"v_mfma_f32_16x16x32_bf16 v[248:251], v[136:139], a[100:103], v[248:251]\n"
"ds_write_b64 v7, v[26:27] offset:38920\n"
"v_mfma_f32_16x16x32_bf16 v[248:251], v[108:111], a[96:99], v[248:251]\n"
"v_accvgpr_read_b32 v16, a220\n"
"v_accvgpr_read_b32 v17, a221\n"
"v_mfma_f32_16x16x32_bf16 v[248:251], a[88:91], a[104:107], v[248:251]\n"
"v_accvgpr_read_b32 v18, a222\n"
"v_accvgpr_read_b32 v19, a223\n"
"v_mfma_f32_16x16x32_bf16 v[248:251], a[92:95], a[108:111], v[248:251]\n"
"L_st11498:\n"
"s_cbranch_execz L_retC\n"
"s_waitcnt lgkmcnt(4)\n"
"buffer_store_dwordx4 v[180:183], v6, s[52:55], s85 offen offset:1152 nt\n"
"v_lshlrev_b32 v12, 16, v16\n"
"v_and_b32 v13, s63, v16\n"
"v_lshlrev_b32 v14, 16, v17\n"
"v_mfma_f32_16x16x32_bf16 v[220:223], a[84:87], a[108:111], v[220:223]\n"
"v_and_b32 v15, s63, v17\n"
"v_mul_f32 v12, v12, v212\n"
"v_mfma_f32_16x16x32_bf16 v[220:223], a[80:83], a[104:107], v[220:223]\n"
"v_mul_f32 v13, v13, v213\n"
"v_mul_f32 v14, v14, v214\n"
"v_mfma_f32_16x16x32_bf16 v[192:195], a[72:75], a[104:107], v[192:195]\n"
"v_mul_f32 v15, v15, v215\n"
"v_cvt_pk_bf16_f32 v28, v12, v13\n"
"v_mfma_f32_16x16x32_bf16 v[192:195], a[76:79], a[108:111], v[192:195]\n"
"v_cvt_pk_bf16_f32 v29, v14, v15\n"
"v_mfma_f32_16x16x32_bf16 v[164:167], a[68:71], a[108:111], v[164:167]\n"
"ds_write_b64 v8, v[28:29] offset:38912\n"
"v_mfma_f32_16x16x32_bf16 v[164:167], a[64:67], a[104:107], v[164:167]\n"
"v_lshlrev_b32 v12, 16, v18\n"
"v_and_b32 v13, s63, v18\n"
"v_mfma_f32_16x16x32_bf16 v[168:171], v[88:91], a[112:115], v[168:171]\n"
"v_lshlrev_b32 v14, 16, v19\n"
"v_and_b32 v15, s63, v19\n"
"v_mfma_f32_16x16x32_bf16 v[168:171], v[116:119], a[116:119], v[168:171]\n"
"v_mul_f32 v12, v12, v240\n"
"v_mul_f32 v13, v13, v241\n"
"v_mfma_f32_16x16x32_bf16 v[196:199], v[120:123], a[116:119], v[196:199]\n"
"v_mul_f32 v14, v14, v242\n"
"v_mul_f32 v15, v15, v243\n"
"v_mfma_f32_16x16x32_bf16 v[196:199], v[92:95], a[112:115], v[196:199]\n"
"v_cvt_pk_bf16_f32 v30, v12, v13\n"
"v_cvt_pk_bf16_f32 v31, v14, v15\n"
"v_mfma_f32_16x16x32_bf16 v[224:227], v[32:35], a[112:115], v[224:227]\n"
"ds_write_b64 v8, v[30:31] offset:38920\n"
"v_mfma_f32_16x16x32_bf16 v[224:227], v[60:63], a[116:119], v[224:227]\n"
"ds_read_b128 v[156:159], v2 offset:38912\n"
"v_mfma_f32_16x16x32_bf16 v[252:255], v[124:127], a[116:119], v[252:255]\n"
"ds_read_b128 v[184:187], v2 offset:39936\n"
"v_accvgpr_read_b32 v16, a224\n"
"v_mfma_f32_16x16x32_bf16 v[252:255], v[96:99], a[112:115], v[252:255]\n"
"v_accvgpr_read_b32 v17, a225\n"
"v_accvgpr_read_b32 v18, a226\n"
"v_mfma_f32_16x16x32_bf16 v[252:255], v[108:111], a[120:123], v[252:255]\n"
"v_accvgpr_read_b32 v19, a227\n"
"v_lshlrev_b32 v12, 16, v16\n"
"v_mfma_f32_16x16x32_bf16 v[252:255], v[136:139], a[124:127], v[252:255]\n"
"v_and_b32 v13, s63, v16\n"
"v_lshlrev_b32 v14, 16, v17\n"
"v_mfma_f32_16x16x32_bf16 v[224:227], v[132:135], a[124:127], v[224:227]\n"
"v_and_b32 v15, s63, v17\n"
"v_mul_f32 v12, v12, v160\n"
"v_mfma_f32_16x16x32_bf16 v[224:227], v[104:107], a[120:123], v[224:227]\n"
"v_mul_f32 v13, v13, v161\n"
"v_mul_f32 v14, v14, v162\n"
"v_mfma_f32_16x16x32_bf16 v[196:199], v[36:39], a[120:123], v[196:199]\n"
"v_mul_f32 v15, v15, v163\n"
"v_cvt_pk_bf16_f32 v24, v12, v13\n"
"v_mfma_f32_16x16x32_bf16 v[196:199], v[64:67], a[124:127], v[196:199]\n"
"v_cvt_pk_bf16_f32 v25, v14, v15\n"
"v_mfma_f32_16x16x32_bf16 v[168:171], v[128:131], a[124:127], v[168:171]\n"
"s_waitcnt lgkmcnt(1)\n"
"buffer_store_dwordx4 v[156:159], v6, s[52:55], s86 offen offset:2176 nt\n"
"ds_write_b64 v7, v[24:25] offset:32768\n"
"v_mfma_f32_16x16x32_bf16 v[168:171], v[100:103], a[120:123], v[168:171]\n"
"v_lshlrev_b32 v12, 16, v18\n"
"v_and_b32 v13, s63, v18\n"
"v_mfma_f32_16x16x32_bf16 v[168:171], a[64:67], a[128:131], v[168:171]\n"
"v_lshlrev_b32 v14, 16, v19\n"
"v_and_b32 v15, s63, v19\n"
"v_mfma_f32_16x16x32_bf16 v[168:171], a[68:71], a[132:135], v[168:171]\n"
"v_mul_f32 v12, v12, v188\n"
"v_mul_f32 v13, v13, v189\n"
"v_mfma_f32_16x16x32_bf16 v[196:199], a[76:79], a[132:135], v[196:199]\n"
"v_mul_f32 v14, v14, v190\n"
"v_mul_f32 v15, v15, v191\n"
"v_mfma_f32_16x16x32_bf16 v[196:199], a[72:75], a[128:131], v[196:199]\n"
"v_cvt_pk_bf16_f32 v26, v12, v13\n"
"v_cvt_pk_bf16_f32 v27, v14, v15\n"
"v_mfma_f32_16x16x32_bf16 v[224:227], a[80:83], a[128:131], v[224:227]\n"
"ds_write_b64 v7, v[26:27] offset:32776\n"
"v_mfma_f32_16x16x32_bf16 v[224:227], a[84:87], a[132:135], v[224:227]\n"
"v_accvgpr_read_b32 v16, a228\n"
"v_accvgpr_read_b32 v17, a229\n"
"v_mfma_f32_16x16x32_bf16 v[252:255], a[92:95], a[132:135], v[252:255]\n"
"v_accvgpr_read_b32 v18, a230\n"
"v_accvgpr_read_b32 v19, a231\n"
"v_mfma_f32_16x16x32_bf16 v[252:255], a[88:91], a[128:131], v[252:255]\n"
"v_lshlrev_b32 v12, 16, v16\n"
"v_and_b32 v13, s63, v16\n"
"v_mfma_f32_16x16x32_bf16 a[28:31], v[96:99], a[136:139], a[28:31]\n"
"s_waitcnt lgkmcnt(2)\n"
"buffer_store_dwordx4 v[184:187], v6, s[52:55], s87 offen offset:3200 nt\n"
"v_lshlrev_b32 v14, 16, v17\n"
"v_and_b32 v15, s63, v17\n"
"v_mul_f32 v12, v12, v216\n"
"v_mul_f32 v13, v13, v217\n"
"v_mfma_f32_16x16x32_bf16 a[28:31], v[124:127], a[140:143], a[28:31]\n"
"v_mul_f32 v14, v14, v218\n"
"v_mul_f32 v15, v15, v219\n"
"v_mfma_f32_16x16x32_bf16 a[24:27], v[60:63], a[140:143], a[24:27]\n"
"v_cvt_pk_bf16_f32 v28, v12, v13\n"
"v_cvt_pk_bf16_f32 v29, v14, v15\n"
"v_mfma_f32_16x16x32_bf16 a[24:27], v[32:35], a[136:139], a[24:27]\n"
"ds_write_b64 v8, v[28:29] offset:32768\n"
"v_mfma_f32_16x16x32_bf16 a[20:23], v[92:95], a[136:139], a[20:23]\n"
"v_lshlrev_b32 v12, 16, v18\n"
"v_and_b32 v13, s63, v18\n"
"v_mfma_f32_16x16x32_bf16 a[20:23], v[120:123], a[140:143], a[20:23]\n"
"v_lshlrev_b32 v14, 16, v19\n"
"v_and_b32 v15, s63, v19\n"
"v_mfma_f32_16x16x32_bf16 a[16:19], v[116:119], a[140:143], a[16:19]\n"
"v_mul_f32 v12, v12, v244\n"
"v_mul_f32 v13, v13, v245\n"
"v_mfma_f32_16x16x32_bf16 a[16:19], v[88:91], a[136:139], a[16:19]\n"
"v_mul_f32 v14, v14, v246\n"
"v_mul_f32 v15, v15, v247\n"
"v_mfma_f32_16x16x32_bf16 a[16:19], v[100:103], a[144:147], a[16:19]\n"
"v_cvt_pk_bf16_f32 v30, v12, v13\n"
"v_cvt_pk_bf16_f32 v31, v14, v15\n"
"v_mfma_f32_16x16x32_bf16 a[16:19], v[128:131], a[148:151], a[16:19]\n"
"ds_write_b64 v8, v[30:31] offset:32776\n"
"v_mfma_f32_16x16x32_bf16 a[20:23], v[64:67], a[148:151], a[20:23]\n"
"ds_read_b128 v[160:163], v2 offset:32768\n"
"v_mfma_f32_16x16x32_bf16 a[20:23], v[36:39], a[144:147], a[20:23]\n"
"ds_read_b128 v[188:191], v2 offset:33792\n"
"v_accvgpr_read_b32 v16, a232\n"
"v_accvgpr_read_b32 v17, a233\n"
"v_mfma_f32_16x16x32_bf16 a[24:27], v[104:107], a[144:147], a[24:27]\n"
"v_accvgpr_read_b32 v18, a234\n"
"v_accvgpr_read_b32 v19, a235\n"
"v_mfma_f32_16x16x32_bf16 a[24:27], v[132:135], a[148:151], a[24:27]\n"
"v_lshlrev_b32 v12, 16, v16\n"
"v_and_b32 v13, s63, v16\n"
"v_mfma_f32_16x16x32_bf16 a[28:31], v[136:139], a[148:151], a[28:31]\n"
"v_lshlrev_b32 v14, 16, v17\n"
"v_and_b32 v15, s63, v17\n"
"v_mfma_f32_16x16x32_bf16 a[28:31], v[108:111], a[144:147], a[28:31]\n"
"v_mul_f32 v12, v12, v164\n"
"v_mul_f32 v13, v13, v165\n"
"v_mfma_f32_16x16x32_bf16 a[28:31], a[88:91], a[152:155], a[28:31]\n"
"v_mul_f32 v14, v14, v166\n"
"v_mul_f32 v15, v15, v167\n"
"v_mfma_f32_16x16x32_bf16 a[28:31], a[92:95], a[156:159], a[28:31]\n"
"v_cvt_pk_bf16_f32 v24, v12, v13\n"
"v_cvt_pk_bf16_f32 v25, v14, v15\n"
"v_mfma_f32_16x16x32_bf16 a[24:27], a[84:87], a[156:159], a[24:27]\n"
"ds_write_b64 v7, v[24:25] offset:34816\n"
"v_mfma_f32_16x16x32_bf16 a[24:27], a[80:83], a[152:155], a[24:27]\n"
"v_lshlrev_b32 v12, 16, v18\n"
"v_and_b32 v13, s63, v18\n"
"v_mfma_f32_16x16x32_bf16 a[20:23], a[72:75], a[152:155], a[20:23]\n"
"s_waitcnt lgkmcnt(2)\n"
"buffer_store_dwordx4 v[160:163], v6, s[52:55], s88 offen offset:128 nt\n"
"v_lshlrev_b32 v14, 16, v19\n"
"v_and_b32 v15, s63, v19\n"
"v_mul_f32 v12, v12, v192\n"
"v_mul_f32 v13, v13, v193\n"
"s_add_u32 s62, s62, 1\n"
"v_mfma_f32_16x16x32_bf16 a[20:23], a[76:79], a[156:159], a[20:23]\n"
"v_mul_f32 v14, v14, v194\n"
"v_mul_f32 v15, v15, v195\n"
"v_mfma_f32_16x16x32_bf16 a[16:19], a[68:71], a[156:159], a[16:19]\n"
"v_cvt_pk_bf16_f32 v26, v12, v13\n"
"v_cvt_pk_bf16_f32 v27, v14, v15\n"
"v_mfma_f32_16x16x32_bf16 a[16:19], a[64:67], a[152:155], a[16:19]\n"
"ds_write_b64 v7, v[26:27] offset:34824\n"
"s_nop 7\n"
"s_waitcnt lgkmcnt(2)\n"
"buffer_store_dwordx4 v[188:191], v6, s[52:55], s89 offen offset:1152 nt\n"
"v_accvgpr_read_b32 v16, a236\n"
"v_accvgpr_read_b32 v17, a237\n"
"v_accvgpr_read_b32 v18, a238\n"
"v_accvgpr_read_b32 v19, a239\n"
"v_lshlrev_b32 v12, 16, v16\n"
"v_and_b32 v13, s63, v16\n"
"v_lshlrev_b32 v14, 16, v17\n"
"v_and_b32 v15, s63, v17\n"
"v_pk_mul_f32 v[12:13], v[12:13], v[220:221]\n"
"v_pk_mul_f32 v[14:15], v[14:15], v[222:223]\n"
"v_cvt_pk_bf16_f32 v28, v12, v13\n"
"v_cvt_pk_bf16_f32 v29, v14, v15\n"
"v_lshlrev_b32 v12, 16, v18\n"
"v_and_b32 v13, s63, v18\n"
"v_lshlrev_b32 v14, 16, v19\n"
"v_and_b32 v15, s63, v19\n"
"v_pk_mul_f32 v[12:13], v[12:13], v[248:249]\n"
"v_pk_mul_f32 v[14:15], v[14:15], v[250:251]\n"
"v_cvt_pk_bf16_f32 v30, v12, v13\n"
"v_cvt_pk_bf16_f32 v31, v14, v15\n"
"ds_write_b128 v8, v[28:31] offset:34816\n"
"ds_read_b128 v[164:167], v2 offset:34816\n"
"ds_read_b128 v[192:195], v2 offset:35840\n"
"v_accvgpr_read_b32 v16, a240\n"
"v_accvgpr_read_b32 v17, a241\n"
"v_accvgpr_read_b32 v18, a242\n"
"v_accvgpr_read_b32 v19, a243\n"
"v_lshlrev_b32 v12, 16, v16\n"
"v_and_b32 v13, s63, v16\n"
"v_lshlrev_b32 v14, 16, v17\n"
"v_and_b32 v15, s63, v17\n"
"v_pk_mul_f32 v[12:13], v[12:13], v[168:169]\n"
"v_pk_mul_f32 v[14:15], v[14:15], v[170:171]\n"
"v_cvt_pk_bf16_f32 v24, v12, v13\n"
"v_cvt_pk_bf16_f32 v25, v14, v15\n"
"v_lshlrev_b32 v12, 16, v18\n"
"v_and_b32 v13, s63, v18\n"
"v_lshlrev_b32 v14, 16, v19\n"
"v_and_b32 v15, s63, v19\n"
"v_pk_mul_f32 v[12:13], v[12:13], v[196:197]\n"
"v_pk_mul_f32 v[14:15], v[14:15], v[198:199]\n"
"v_cvt_pk_bf16_f32 v26, v12, v13\n"
"v_cvt_pk_bf16_f32 v27, v14, v15\n"
"ds_write_b128 v7, v[24:27] offset:36864\n"
"s_waitcnt lgkmcnt(2)\n"
"buffer_store_dwordx4 v[164:167], v6, s[52:55], s90 offen offset:2176 nt\n"
"s_waitcnt lgkmcnt(1)\n"
"buffer_store_dwordx4 v[192:195], v6, s[52:55], s91 offen offset:3200 nt\n"
"v_accvgpr_read_b32 v16, a244\n"
"v_accvgpr_read_b32 v17, a245\n"
"v_accvgpr_read_b32 v18, a246\n"
"v_accvgpr_read_b32 v19, a247\n"
"v_lshlrev_b32 v12, 16, v16\n"
"v_and_b32 v13, s63, v16\n"
"v_lshlrev_b32 v14, 16, v17\n"
"v_and_b32 v15, s63, v17\n"
"v_pk_mul_f32 v[12:13], v[12:13], v[224:225]\n"
"v_pk_mul_f32 v[14:15], v[14:15], v[226:227]\n"
"v_cvt_pk_bf16_f32 v28, v12, v13\n"
"v_cvt_pk_bf16_f32 v29, v14, v15\n"
"v_lshlrev_b32 v12, 16, v18\n"
"v_and_b32 v13, s63, v18\n"
"v_lshlrev_b32 v14, 16, v19\n"
"v_and_b32 v15, s63, v19\n"
"v_pk_mul_f32 v[12:13], v[12:13], v[252:253]\n"
"v_pk_mul_f32 v[14:15], v[14:15], v[254:255]\n"
"v_cvt_pk_bf16_f32 v30, v12, v13\n"
"v_cvt_pk_bf16_f32 v31, v14, v15\n"
"ds_write_b128 v8, v[28:31] offset:36864\n"
"ds_read_b128 v[168:171], v2 offset:36864\n"
"ds_read_b128 v[196:199], v2 offset:37888\n"
"v_accvgpr_read_b32 v16, a248\n"
"v_accvgpr_read_b32 v17, a249\n"
"v_accvgpr_read_b32 v18, a250\n"
"v_accvgpr_read_b32 v19, a251\n"
"v_lshlrev_b32 v12, 16, v16\n"
"v_and_b32 v13, s63, v16\n"
"v_lshlrev_b32 v14, 16, v17\n"
"v_and_b32 v15, s63, v17\n"
"v_accvgpr_read_b32 v10, a16\n"
"v_accvgpr_read_b32 v11, a17\n"
"v_mul_f32 v12, v12, v10\n"
"v_mul_f32 v13, v13, v11\n"
"v_accvgpr_read_b32 v10, a18\n"
"v_accvgpr_read_b32 v11, a19\n"
"v_mul_f32 v14, v14, v10\n"
"v_mul_f32 v15, v15, v11\n"
"v_cvt_pk_bf16_f32 v24, v12, v13\n"
"v_cvt_pk_bf16_f32 v25, v14, v15\n"
"v_lshlrev_b32 v12, 16, v18\n"
"v_and_b32 v13, s63, v18\n"
"v_lshlrev_b32 v14, 16, v19\n"
"v_and_b32 v15, s63, v19\n"
"v_accvgpr_read_b32 v10, a20\n"
"v_accvgpr_read_b32 v11, a21\n"
"v_mul_f32 v12, v12, v10\n"
"v_mul_f32 v13, v13, v11\n"
"v_accvgpr_read_b32 v10, a22\n"
"v_accvgpr_read_b32 v11, a23\n"
"v_mul_f32 v14, v14, v10\n"
"v_mul_f32 v15, v15, v11\n"
"v_cvt_pk_bf16_f32 v26, v12, v13\n"
"v_cvt_pk_bf16_f32 v27, v14, v15\n"
"ds_write_b128 v7, v[24:27] offset:38912\n"
"s_waitcnt lgkmcnt(2)\n"
"buffer_store_dwordx4 v[168:171], v6, s[52:55], s92 offen offset:128 nt\n"
"s_waitcnt lgkmcnt(1)\n"
"buffer_store_dwordx4 v[196:199], v6, s[52:55], s93 offen offset:1152 nt\n"
"v_accvgpr_read_b32 v16, a252\n"
"v_accvgpr_read_b32 v17, a253\n"
"v_accvgpr_read_b32 v18, a254\n"
"v_accvgpr_read_b32 v19, a255\n"
"v_lshlrev_b32 v12, 16, v16\n"
"v_and_b32 v13, s63, v16\n"
"v_lshlrev_b32 v14, 16, v17\n"
"v_and_b32 v15, s63, v17\n"
"v_accvgpr_read_b32 v10, a24\n"
"v_accvgpr_read_b32 v11, a25\n"
"v_mul_f32 v12, v12, v10\n"
"v_mul_f32 v13, v13, v11\n"
"v_accvgpr_read_b32 v10, a26\n"
"v_accvgpr_read_b32 v11, a27\n"
"v_mul_f32 v14, v14, v10\n"
"v_mul_f32 v15, v15, v11\n"
"v_cvt_pk_bf16_f32 v28, v12, v13\n"
"v_cvt_pk_bf16_f32 v29, v14, v15\n"
"v_lshlrev_b32 v12, 16, v18\n"
"v_and_b32 v13, s63, v18\n"
"v_lshlrev_b32 v14, 16, v19\n"
"v_and_b32 v15, s63, v19\n"
"v_accvgpr_read_b32 v10, a28\n"
"v_accvgpr_read_b32 v11, a29\n"
"v_mul_f32 v12, v12, v10\n"
"v_mul_f32 v13, v13, v11\n"
"v_accvgpr_read_b32 v10, a30\n"
"v_accvgpr_read_b32 v11, a31\n"
"v_mul_f32 v14, v14, v10\n"
"v_mul_f32 v15, v15, v11\n"
"v_cvt_pk_bf16_f32 v30, v12, v13\n"
"v_cvt_pk_bf16_f32 v31, v14, v15\n"
"ds_write_b128 v8, v[28:31] offset:38912\n"
"ds_read_b128 v[224:227], v2 offset:38912\n"
"ds_read_b128 v[252:255], v2 offset:39936\n"
"s_waitcnt lgkmcnt(1)\n"
"buffer_store_dwordx4 v[224:227], v6, s[52:55], s94 offen offset:2176 nt\n"
"s_waitcnt lgkmcnt(0)\n"
"buffer_store_dwordx4 v[252:255], v6, s[52:55], s95 offen offset:3200 nt\n"
"s_or_b32 s66, %26, %16\n"
"s_cbranch_scc1 L_end\n"
"v_mov_b32 v10, s78\n"
"v_mov_b32 v11, s79\n"
"s_mov_b32 s66, 139264\n"
"buffer_store_dwordx2 v[10:11], off, s[56:59], s66 sc1\n"
"s_branch L_end\n"
"L_slow0:\n"
"s_cmp_eq_u32 s68, 0\n"
"s_cbranch_scc1 L_back0\n"
"s_sub_u32 s68, s68, 1\n"
"s_sleep 2\n"
"buffer_load_dwordx2 v[8:9], off, s[56:59], %14 sc1\n"
"s_waitcnt vmcnt(0)\n"
"v_subrev_u32 v8, s65, v8\n"
"v_xor_b32 v9, s79, v9\n"
"v_or_b32 v8, v8, v9\n"
"v_cmp_lt_u32 vcc, 15, v8\n"
"s_nop 4\n"
"s_cbranch_vccnz L_slow0\n"
"s_branch L_back0\n"
"L_slow1:\n"
"s_cmp_eq_u32 s68, 0\n"
"s_cbranch_scc1 L_back1\n"
"s_sub_u32 s68, s68, 1\n"
"s_sleep 2\n"
"buffer_load_dwordx2 v[8:9], off, s[56:59], %14 sc1\n"
"s_waitcnt vmcnt(0)\n"
"v_subrev_u32 v8, s65, v8\n"
"v_xor_b32 v9, s79, v9\n"
"v_or_b32 v8, v8, v9\n"
"v_cmp_lt_u32 vcc, 15, v8\n"
"s_nop 4\n"
"s_cbranch_vccnz L_slow1\n"
"s_branch L_back1\n"
"L_slow2:\n"
"s_cmp_eq_u32 s68, 0\n"
"s_cbranch_scc1 L_back2\n"
"s_sub_u32 s68, s68, 1\n"
"s_sleep 2\n"
"buffer_load_dwordx4 v[12:15], v0, s[56:59], %25 offen sc1\n"
"s_waitcnt vmcnt(0)\n"
"v_subrev_u32 v12, s67, v12\n"
"v_xor_b32 v13, s79, v13\n"
"v_subrev_u32 v14, s67, v14\n"
"v_xor_b32 v15, s79, v15\n"
"v_or_b32 v12, v12, v14\n"
"v_or_b32 v13, v13, v15\n"
"v_or_b32 v12, v12, v13\n"
"v_cmp_lt_u32 vcc, 15, v12\n"
"s_nop 4\n"
"s_cbranch_vccnz L_slow2\n"
"s_branch L_back2\n"
"L_slow3:\n"
"s_cmp_eq_u32 s68, 0\n"
"s_cbranch_scc1 L_back3\n"
"s_sub_u32 s68, s68, 1\n"
"s_sleep 2\n"
"buffer_load_dwordx4 v[24:27], v0, s[56:59], %25 offen sc1\n"
"s_waitcnt vmcnt(0)\n"
"v_subrev_u32 v24, s70, v24\n"
"v_xor_b32 v25, s79, v25\n"
"v_subrev_u32 v26, s70, v26\n"
"v_xor_b32 v27, s79, v27\n"
"v_or_b32 v24, v24, v26\n"
"v_or_b32 v25, v25, v27\n"
"v_or_b32 v24, v24, v25\n"
"v_cmp_lt_u32 vcc, 15, v24\n"
"s_nop 4\n"
"s_cbranch_vccnz L_slow3\n"
"s_branch L_back3\n"
"L_slow4:\n"
"s_cmp_eq_u32 s68, 0\n"
"s_cbranch_scc1 L_back4\n"
"s_sub_u32 s68, s68, 1\n"
"s_sleep 2\n"
"buffer_load_dwordx2 v[8:9], off, s[56:59], %14 sc1\n"
"s_waitcnt vmcnt(0)\n"
"v_subrev_u32 v8, s65, v8\n"
"v_xor_b32 v9, s79, v9\n"
"v_or_b32 v8, v8, v9\n"
"v_cmp_lt_u32 vcc, 15, v8\n"
"s_nop 4\n"
"s_cbranch_vccnz L_slow4\n"
"s_branch L_back4\n"
"L_slow7:\n"
"s_cmp_eq_u32 s68, 0\n"
"s_cbranch_scc1 L_back7\n"
"s_sub_u32 s68, s68, 1\n"
"s_sleep 2\n"
"buffer_load_dwordx2 v[8:9], off, s[56:59], %14 sc1\n"
"s_waitcnt vmcnt(0)\n"
"v_subrev_u32 v8, s65, v8\n"
"v_xor_b32 v9, s79, v9\n"
"v_or_b32 v8, v8, v9\n"
"v_cmp_lt_u32 vcc, 15, v8\n"
"s_nop 4\n"
"s_cbranch_vccnz L_slow7\n"
"s_branch L_back7\n"
"L_headF:\n"
"L_end:\n"
"s_waitcnt vmcnt(0) lgkmcnt(0)\n"
    :: LO(wsb), HI(wsb), LO(xo), HI(xo), LO(ob), HI(ob), LO(wr), HI(wr), LO(bb), HI(bb), "s"(wo), "s"(eo), "s"(ep), "s"(fo), "s"(fp),
       "s"(ldsa), "s"(w), "s"(col), "v"(threadIdx.x), "s"(db), "s"(rs1), "s"(rs2), "s"(rd1), "s"(rn1), "s"(wfo), "s"(wfa), "s"(b)
    : "memory", "scc", "vcc",
"v1","v2","v3","v4","v5","v6","v7","v8","v9","v10","v11","v12","v13","v14","v15","v16","v17","v18","v19","v20","v21","v22","v23","v24","v25","v26","v27","v28","v29","v30","v31","v32","v33","v34","v35","v36","v37","v38","v39","v40","v41","v42","v43","v44","v45","v46","v47","v48","v49","v50","v51","v52","v53","v54","v55","v56","v57","v58","v59","v60","v61","v62","v63","v64","v65","v66","v67","v68","v69","v70","v71","v72","v73","v74","v75","v76","v77","v78","v79","v80","v81","v82","v83","v84","v85","v86","v87","v88","v89","v90","v91","v92","v93","v94","v95","v96","v97","v98","v99","v100","v101","v102","v103","v104","v105","v106","v107","v108","v109","v110","v111","v112","v113","v114","v115","v116","v117","v118","v119","v120","v121","v122","v123","v124","v125","v126","v127","v128","v129","v130","v131","v132","v133","v134","v135","v136","v137","v138","v139","v140","v141","v142","v143","v144","v145","v146","v147","v148","v149","v150","v151","v152","v153","v154","v155","v156","v157","v158","v159","v160","v161","v162","v163","v164","v165","v166","v167","v168","v169","v170","v171","v172","v173","v174","v175","v176","v177","v178","v179","v180","v181","v182","v183","v184","v185","v186","v187","v188","v189","v190","v191","v192","v193","v194","v195","v196","v197","v198","v199","v200","v201","v202","v203","v204","v205","v206","v207","v208","v209","v210","v211","v212","v213","v214","v215","v216","v217","v218","v219","v220","v221","v222","v223","v224","v225","v226","v227","v228","v229","v230","v231","v232","v233","v234","v235","v236","v237","v238","v239","v240","v241","v242","v243","v244","v245","v246","v247","v248","v249","v250","v251","v252","v253","v254","v255",
"a0","a1","a2","a3","a4","a5","a6","a7","a8","a9","a10","a11","a12","a13","a14","a15","a16","a17","a18","a19","a20","a21","a22","a23","a24","a25","a26","a27","a28","a29","a30","a31","a32","a33","a34","a35","a36","a37","a38","a39","a40","a41","a42","a43","a44","a45","a46","a47","a48","a49","a50","a51","a52","a53","a54","a55","a56","a57","a58","a59","a60","a61","a62","a63","a64","a65","a66","a67","a68","a69","a70","a71","a72","a73","a74","a75","a76","a77","a78","a79","a80","a81","a82","a83","a84","a85","a86","a87","a88","a89","a90","a91","a92","a93","a94","a95","a96","a97","a98","a99","a100","a101","a102","a103","a104","a105","a106","a107","a108","a109","a110","a111","a112","a113","a114","a115","a116","a117","a118","a119","a120","a121","a122","a123","a124","a125","a126","a127","a128","a129","a130","a131","a132","a133","a134","a135","a136","a137","a138","a139","a140","a141","a142","a143","a144","a145","a146","a147","a148","a149","a150","a151","a152","a153","a154","a155","a156","a157","a158","a159","a160","a161","a162","a163","a164","a165","a166","a167","a168","a169","a170","a171","a172","a173","a174","a175","a176","a177","a178","a179","a180","a181","a182","a183","a184","a185","a186","a187","a188","a189","a190","a191","a192","a193","a194","a195","a196","a197","a198","a199","a200","a201","a202","a203","a204","a205","a206","a207","a208","a209","a210","a211","a212","a213","a214","a215","a216","a217","a218","a219","a220","a221","a222","a223","a224","a225","a226","a227","a228","a229","a230","a231","a232","a233","a234","a235","a236","a237","a238","a239","a240","a241","a242","a243","a244","a245","a246","a247","a248","a249","a250","a251","a252","a253","a254","a255",
"s27","s28","s29","s30","s31","s33","s34","s35","s36","s37","s38","s39","s40","s41","s42","s43","s44","s45","s46","s47","s48","s49","s50","s51","s52","s53","s54","s55","s56","s57","s58","s59","s60","s61","s62","s63","s64","s65","s66","s67","s68","s69","s70","s71","s72","s73","s74","s75","s76","s77","s78","s79","s80","s81","s82","s83","s84","s85","s86","s87","s88","s89","s90","s91","s92","s93","s94","s95","s96","s97","s98","s99");
}

extern "C" void kernel_launch(void* const* d_in, const int* in_sizes, int n_in,
                              void* d_out, int out_size, void* d_ws, size_t ws_size,
                              hipStream_t stream) {
  const unsigned short* x0 = (const unsigned short*)d_in[0];
  const unsigned short* ws = (const unsigned short*)d_in[1];
  const unsigned short* bs = (const unsigned short*)d_in[2];
  dcn_cross_kernel<<<256, 256, 0, stream>>>(x0, ws, bs, (unsigned char*)d_ws, (unsigned short*)d_out);
}
```

```cpp
#include <hip/hip_runtime.h>


constexpr int kD = 1024;
constexpr unsigned kWFlagOff = 131072, kWOff = 1u << 20, kWStream = 200 * 8192, kXchOff = 32u << 20, kDbgOff = 224u << 20;

__device__ __forceinline__ unsigned unit_src(unsigned v, unsigned h, unsigned w) {
  const unsigned l = v >> 5, u = v & 31;
  const unsigned split = ((0xff0000ffu >> u) & 1) - 1;
  const unsigned part = l ? u >> 4 : 0u, g = (0xff00ff00u >> u) & 1;
  const unsigned jh = (u >> (~split & 2)) & 1, c = (u >> (split & 1)) ^ (~split & (0u - jh));
  const unsigned kc = 8 * (h ^ part) + 2 * ((w + c) & 3) + g;
  return (l * 1024 + 64 * kc) * 2048 + jh * 128;
}

__global__ __launch_bounds__(256, 1) __attribute__((aligned(4096)))
void dcn_cross_kernel(const unsigned short* __restrict__ x0, const unsigned short* __restrict__ ws, const unsigned short* __restrict__ bs,
                      unsigned char* __restrict__ wsp, unsigned short* __restrict__ out) {
  __shared__ __attribute__((aligned(16))) unsigned char lds[163840];
  __builtin_amdgcn_sched_barrier(0);
  const unsigned b = blockIdx.x;
  constexpr unsigned kPx = 1 ? 1u : 8u;
  const unsigned h = 1 ? (b & 1) : ((b >> 3) & 1), rb = 1 ? (b >> 1) : ((b >> 4) * 8 + (b & 7));
  const unsigned w = __builtin_amdgcn_readfirstlane(threadIdx.x >> 6);
  asm volatile("s_getpc_b64 s[96:97]\n" "L_pc:\n" "s_lshr_b32 s95, %0, 3\n" "s_lshl2_add_u32 s95, s95, %1\n" "s_mul_i32 s95, s95, 704\n" "s_and_b32 s97, s97, 0xffff\n" "s_mov_b32 s98, L_end-L_pc\n" "s_mov_b32 s99, 0x20000\n" "s_min_u32 s95, s95, L_end-L_pc-704\n" "v_and_b32 v9, 63, %2\n" "v_min_u32 v9, 10, v9\n" "v_lshlrev_b32 v9, 6, v9\n" "buffer_load_dword a252, v9, s[96:99], s95 offen\n"
    :: "s"(b), "s"(w), "v"(threadIdx.x) : "memory", "scc", "v9", "a252", "s95", "s96", "s97", "s98", "s99");
  __builtin_amdgcn_sched_barrier(0);
  unsigned col = (512 * h + 128 * w) * 2, rows = rb * 128 * kD * 2, oofs = rows + col;
  unsigned wo = (h * 4 + w) * kWStream, eo = kXchOff + b * 524288 + w * 32768, ep = kXchOff + (b ^ kPx) * 524288 + w * 32768;
  unsigned fo = b * 512 + w * 128, fp = (b ^ kPx) * 512 + w * 128;
  unsigned db = kDbgOff + (b * 4 + w) * 512;
  const unsigned v1 = 24 + (rb >> 1), v2 = 64 + ((rb >> 1) & 31), hk = rb & 1, h2 = rb >> 6;
  unsigned rs1 = unit_src(v1, 0, w) + hk * 65536, rd1 = v1 * 8192 + hk * 1024;
  unsigned rs2 = unit_src(v2, h2, w) + hk * 65536 + h2 * 1024 + h * ((4u << 20) - 1024), rn1 = v2 * 8192 + hk * 1024 + h2 * 4 * kWStream;
  unsigned wfa = kWFlagOff + w * 1024, wfo = wfa + h * 4096 + rb * 8;
  asm volatile("" : "+s"(col), "+s"(rows), "+s"(oofs), "+s"(wo), "+s"(eo), "+s"(ep), "+s"(fo), "+s"(fp), "+s"(db), "+s"(rs1), "+s"(rd1), "+s"(rs2), "+s"(rn1), "+s"(wfa), "+s"(wfo));
  __builtin_amdgcn_sched_barrier(0);
  const unsigned long long wsb = (unsigned long long)wsp, bb = (unsigned long long)bs;
  const unsigned long long xo = (unsigned long long)x0 + rows, ob = (unsigned long long)out + oofs, wr = (unsigned long long)ws + col;
  wo += (unsigned)wsb > ~(15u << 20) ? 16u << 20 : kWOff;
  const unsigned xa = (0u - (unsigned)wsb) & 0x1fffffu;
  eo += xa; ep += xa;
  const unsigned ldsa = (unsigned)(size_t)(__attribute__((address_space(3))) unsigned char*)lds;
#define LO(x) "s"((unsigned)(x))
#define HI(x) "s"((unsigned)((x) >> 32))
  asm volatile(
"L_main_entry:\n"
"v_and_b32 v12, 63, %18\n"
"v_lshrrev_b32 v13, 3, v12\n"
"v_and_b32 v15, 7, v12\n"
"v_lshlrev_b32 v10, 11, v13\n"
"s_andn2_b32 s60, 1024, %17\n"
"s_lshl_b32 s75, s60, 10\n"
"s_mov_b32 s52, %6\n"
"s_and_b32 s53, %7, 0xffff\n"
"s_mov_b32 s54, 12581120\n"
"s_mov_b32 s55, 0x20000\n"
"v_xor_b32 v6, v15, v13\n"
"v_lshl_add_u32 v6, v6, 4, v10\n"
"s_mov_b32 s48, %2\n"
"s_and_b32 s49, %3, 0xffff\n"
"s_mov_b32 s50, 65536\n"
"s_mov_b32 s51, 0x20000\n"
"s_lshl_b32 s66, %16, 16\n"
"s_add_u32 s48, s48, s66\n"
"s_addc_u32 s49, s49, 0\n"
"s_lshl_b32 s72, %16, 12\n"
"s_add_u32 s72, s72, %15\n"
"s_mul_i32 s73, %16, 40960\n"
"s_add_u32 s64, s73, %15\n"
"v_lshrrev_b32 v11, 2, v12\n"
"v_and_b32 v11, 4, v11\n"
"v_xor_b32 v11, v11, v15\n"
"v_lshl_add_u32 v11, v11, 4, v10\n"
"s_bitcmp1_b32 %26, 0\n"
"s_cbranch_scc1 L_y\n"
"s_mov_b64 s[96:97], s[52:53]\n"
"s_cmp_lt_u32 %22, 524288\n"
"s_cselect_b32 s98, s54, 0\n"
"s_mov_b32 s99, 0x20000\n"
"s_xor_b32 s71, %20, 0x100000\n"
"s_add_u32 m0, s72, 139264\n"
"s_add_u32 s67, %20, 0\n"
"buffer_load_dwordx4 v11, s[96:99], s67 offen offset:0  lds\n"
"s_add_u32 s67, %20, 15360\n"
"buffer_load_dwordx4 v11, s[96:99], s67 offen offset:1024  lds\n"
"s_add_u32 s67, %20, 30720\n"
"buffer_load_dwordx4 v11, s[96:99], s67 offen offset:2048  lds\n"
"s_add_u32 s67, %20, 46080\n"
"buffer_load_dwordx4 v11, s[96:99], s67 offen offset:3072  lds\n"
"s_add_u32 m0, s72, 98304\n"
"s_add_u32 s67, s71, 1024\n"
"buffer_load_dwordx4 v11, s[96:99], s67 offen offset:0  lds\n"
"s_add_u32 s67, s71, 16384\n"
"buffer_load_dwordx4 v11, s[96:99], s67 offen offset:1024  lds\n"
"s_add_u32 s67, s71, 31744\n"
"buffer_load_dwordx4 v11, s[96:99], s67 offen offset:2048  lds\n"
"s_add_u32 s67, s71, 47104\n"
"buffer_load_dwordx4 v11, s[96:99], s67 offen offset:3072  lds\n"
"L_y:\n"
"s_add_u32 m0, s72, 16384\n"
"s_add_u32 s67, s60, 0\n"
"buffer_load_dwordx4 v6, s[48:51], s67 offen offset:0  lds\n"
"s_add_u32 s67, s60, 15360\n"
"buffer_load_dwordx4 v6, s[48:51], s67 offen offset:1024  lds\n"
"s_add_u32 s67, s60, 30720\n"
"buffer_load_dwordx4 v6, s[48:51], s67 offen offset:2048  lds\n"
"s_add_u32 s67, s60, 46080\n"
"buffer_load_dwordx4 v6, s[48:51], s67 offen offset:3072  lds\n"
"s_add_u32 m0, s64, 0\n"
"s_add_u32 s67, s75, 0\n"
"buffer_load_dwordx4 v11, s[52:55], s67 offen offset:0  lds\n"
"s_add_u32 s67, s75, 15360\n"
"buffer_load_dwordx4 v11, s[52:55], s67 offen offset:1024  lds\n"
"s_add_u32 s67, s75, 30720\n"
"buffer_load_dwordx4 v11, s[52:55], s67 offen offset:2048  lds\n"
"s_add_u32 s67, s75, 46080\n"
"buffer_load_dwordx4 v11, s[52:55], s67 offen offset:3072  lds\n"
"s_add_u32 m0, s64, 4096\n"
"s_add_u32 s67, s75, 65536\n"
"buffer_load_dwordx4 v11, s[52:55], s67 offen offset:0  lds\n"
"s_add_u32 s67, s75, 80896\n"
"buffer_load_dwordx4 v11, s[52:55], s67 offen offset:1024  lds\n"
"s_add_u32 s67, s75, 96256\n"
"buffer_load_dwordx4 v11, s[52:55], s67 offen offset:2048  lds\n"
"s_add_u32 s67, s75, 111616\n"
"buffer_load_dwordx4 v11, s[52:55], s67 offen offset:3072  lds\n"
"s_lshr_b32 s65, %26, 4\n"
"s_lshl_b32 s65, s65, 2\n"
"s_add_u32 s65, s65, %16\n"
"s_lshl_b32 s65, s65, 11\n"
"s_lshl_b32 s67, %16, 8\n"
"s_sub_u32 s65, s65, s67\n"
"v_and_b32 v14, 1, v13\n"
"v_lshlrev_b32 v14, 17, v14\n"
"v_lshl_add_u32 v14, v15, 7, v14\n"
"v_and_b32 v14, 0xfffffdff, v14\n"
"s_lshl_b32 s67, %26, 6\n"
"s_and_b32 s67, s67, 512\n"
"s_add_u32 s65, s65, s67\n"
"v_lshlrev_b32 v0, 4, v12\n"
"s_mov_b32 s56, %0\n"
"s_and_b32 s57, %1, 0xffff\n"
"s_mov_b32 s58, 171966464\n"
"s_mov_b32 s59, 0x20000\n"
"v_add_u32 v1, 4096, v0\n"
"s_add_u32 s36, s56, %10\n"
"s_addc_u32 s37, s57, 0\n"
"s_mov_b32 s38, 1572864\n"
"s_mov_b32 s39, 0x20000\n"
"s_add_u32 s34, s36, 200704\n"
"s_mov_b32 s35, s37\n"
"v_and_b32 v13, 15, v12\n"
"v_lshrrev_b32 v10, 4, v12\n"
"v_xor_b32 v15, v15, v10\n"
"v_lshlrev_b32 v7, 7, v13\n"
"v_lshl_add_u32 v7, v15, 4, v7\n"
"v_add_u32 v7, s64, v7\n"
"v_xor_b32 v8, 64, v7\n"
"v_bfe_u32 v13, v12, 2, 2\n"
"v_and_b32 v15, 3, v12\n"
"v_bfe_u32 v9, v12, 3, 1\n"
"v_lshl_add_u32 v15, v9, 2, v15\n"
"v_lshl_add_u32 v13, v10, 3, v13\n"
"v_lshlrev_b32 v13, 7, v13\n"
"v_lshl_add_u32 v9, v15, 4, v13\n"
"v_add_u32 v9, s64, v9\n"
"v_xor_b32 v15, 64, v9\n"
"s_mov_b32 s40, %2\n"
"s_and_b32 s41, %3, 0xffff\n"
"s_mov_b32 s42, 260352\n"
"s_mov_b32 s43, 0x20000\n"
"s_add_u32 s40, s40, %17\n"
"s_addc_u32 s41, s41, 0\n"
"s_mov_b32 s80, 0\n"
"s_mov_b32 s81, 15360\n"
"s_mov_b32 s82, 30720\n"
"s_mov_b32 s83, 46080\n"
"s_mov_b32 s84, 65536\n"
"s_mov_b32 s85, 80896\n"
"s_mov_b32 s86, 96256\n"
"s_mov_b32 s87, 111616\n"
"s_mov_b32 s88, 131072\n"
"s_mov_b32 s89, 146432\n"
"s_mov_b32 s90, 161792\n"
"s_mov_b32 s91, 177152\n"
"s_mov_b32 s92, 196608\n"
"s_mov_b32 s93, 211968\n"
"s_mov_b32 s94, 227328\n"
"s_mov_b32 s95, 242688\n"
"s_mov_b32 s62, 0\n"
"s_mov_b32 s63, 0xffff0000\n"
"s_mov_b32 s68, 0x8000\n"
"s_add_u32 s66, %16, 0\n"
"s_and_b32 s66, s66, 3\n"
"s_mul_i32 s66, s66, 40960\n"
"s_add_u32 s66, s66, %15\n"
"v_add_u32 v2, s66, v0\n"
"s_add_u32 s66, %16, 1\n"
"s_and_b32 s66, s66, 3\n"
"s_mul_i32 s66, s66, 40960\n"
"s_add_u32 s66, s66, %15\n"
"v_add_u32 v3, s66, v0\n"
"s_add_u32 s66, %16, 2\n"
"s_and_b32 s66, s66, 3\n"
"s_mul_i32 s66, s66, 40960\n"
"s_add_u32 s66, s66, %15\n"
"v_add_u32 v4, s66, v0\n"
"s_add_u32 s66, %16, 3\n"
"s_and_b32 s66, s66, 3\n"
"s_mul_i32 s66, s66, 40960\n"
"s_add_u32 s66, s66, %15\n"
"v_add_u32 v5, s66, v0\n"
"s_sub_u32 s66, 0, s73\n"
"v_add_u32 v12, s66, v7\n"
"v_add_u32 v13, s66, v8\n"
"s_mov_b64 exec, 0\n"
"s_cmp_eq_u32 %16, 0\n"
"s_cbranch_scc1 L_st518\n"
"s_cmp_eq_u32 %16, 1\n"
"s_cbranch_scc1 L_st1045\n"
"s_cmp_eq_u32 %16, 2\n"
"s_cbranch_scc1 L_st1575\n"
"s_branch L_st2117\n"
"L_retA:\n"
"s_mov_b64 exec, -1\n"
"s_nop 3\n"
"s_mov_b32 s44, %8\n"
"s_and_b32 s45, %9, 0xffff\n"
"s_mov_b32 s46, 12288\n"
"s_mov_b32 s47, 0x20000\n"
"s_mov_b32 s61, %17\n"
"v_lshrrev_b32 v16, 4, v0\n"
"v_and_b32 v16, 0x30, v16\n"
"buffer_load_dwordx2 v[18:19], v16, s[44:47], s61 offen offset:0\n"
"v_lshrrev_b32 v20, 4, v0\n"
"v_and_b32 v20, 0x30, v20\n"
"buffer_load_dwordx2 v[22:23], v20, s[44:47], s61 offen offset:8\n"
"v_lshrrev_b32 v24, 4, v0\n"
"v_and_b32 v24, 0x30, v24\n"
"buffer_load_dwordx2 v[26:27], v24, s[44:47], s61 offen offset:64\n"
"v_lshrrev_b32 v28, 4, v0\n"
"v_and_b32 v28, 0x30, v28\n"
"buffer_load_dwordx2 v[30:31], v28, s[44:47], s61 offen offset:72\n"
"s_add_u32 s70, s75, 131072\n"
"s_add_u32 s70, s70, s65\n"
"buffer_load_dword a252, v14, s[52:55], s70 offen\n"
"s_add_u32 m0, s64, 8192\n"
"s_add_u32 s67, s75, 128\n"
"buffer_load_dwordx4 v11, s[52:55], s67 offen offset:0  lds\n"
"s_add_u32 s67, s75, 15488\n"
"buffer_load_dwordx4 v11, s[52:55], s67 offen offset:1024  lds\n"
"s_add_u32 s67, s75, 30848\n"
"buffer_load_dwordx4 v11, s[52:55], s67 offen offset:2048  lds\n"
"s_add_u32 s67, s75, 46208\n"
"buffer_load_dwordx4 v11, s[52:55], s67 offen offset:3072  lds\n"
"s_add_u32 m0, s64, 12288\n"
"s_add_u32 s67, s75, 65664\n"
"buffer_load_dwordx4 v11, s[52:55], s67 offen offset:0  lds\n"
"s_add_u32 s67, s75, 81024\n"
"buffer_load_dwordx4 v11, s[52:55], s67 offen offset:1024  lds\n"
"s_add_u32 s67, s75, 96384\n"
"buffer_load_dwordx4 v11, s[52:55], s67 offen offset:2048  lds\n"
"s_add_u32 s67, s75, 111744\n"
"buffer_load_dwordx4 v11, s[52:55], s67 offen offset:3072  lds\n"
"s_mov_b32 s67, 139264\n"
"buffer_load_dwordx2 v[252:253], off, s[56:59], s67 sc1\n"
"s_waitcnt vmcnt(22)\n"
"s_barrier\n"
"ds_read_b128 a[100:103], v13 offset:16384\n"
"ds_read_b128 a[96:99], v12 offset:16384\n"
"ds_read_b128 a[108:111], v13 offset:18432\n"
"ds_read_b128 a[104:107], v12 offset:18432\n"
"ds_read_b128 a[116:119], v13 offset:20480\n"
"ds_read_b128 a[112:115], v12 offset:20480\n"
"ds_read_b128 a[124:127], v13 offset:22528\n"
"ds_read_b128 a[120:123], v12 offset:22528\n"
"ds_read_b128 a[132:135], v13 offset:24576\n"
"ds_read_b128 a[128:131], v12 offset:24576\n"
"ds_read_b128 a[140:143], v13 offset:26624\n"
"ds_read_b128 a[136:139], v12 offset:26624\n"
"ds_read_b128 a[148:151], v13 offset:28672\n"
"ds_read_b128 a[144:147], v12 offset:28672\n"
"ds_read_b128 a[156:159], v13 offset:30720\n"
"ds_read_b128 a[152:155], v12 offset:30720\n"
"s_add_u32 m0, s72, 57344\n"
"s_add_u32 s67, s60, 128\n"
"buffer_load_dwordx4 v6, s[48:51], s67 offen offset:0  lds\n"
"s_add_u32 s67, s60, 15488\n"
"buffer_load_dwordx4 v6, s[48:51], s67 offen offset:1024  lds\n"
"s_add_u32 s67, s60, 30848\n"
"buffer_load_dwordx4 v6, s[48:51], s67 offen offset:2048  lds\n"
"s_add_u32 s67, s60, 46208\n"
"buffer_load_dwordx4 v6, s[48:51], s67 offen offset:3072  lds\n"
"s_waitcnt vmcnt(18)\n"
"ds_read_b64_tr_b16 a[32:33], v9 offset:0\n"
"ds_read_b64_tr_b16 a[34:35], v9 offset:512\n"
"ds_read_b64_tr_b16 a[36:37], v9 offset:4096\n"
"ds_read_b64_tr_b16 a[38:39], v9 offset:4608\n"
"ds_read_b64_tr_b16 a[40:41], v9 offset:8\n"
"ds_read_b64_tr_b16 a[42:43], v9 offset:520\n"
"ds_read_b64_tr_b16 a[44:45], v9 offset:4104\n"
"ds_read_b64_tr_b16 a[46:47], v9 offset:4616\n"
"ds_read_b64_tr_b16 a[48:49], v15 offset:0\n"
"ds_read_b64_tr_b16 a[50:51], v15 offset:512\n"
"ds_read_b64_tr_b16 a[52:53], v15 offset:4096\n"
"ds_read_b64_tr_b16 a[54:55], v15 offset:4608\n"
"ds_read_b64_tr_b16 a[56:57], v15 offset:8\n"
"ds_read_b64_tr_b16 a[58:59], v15 offset:520\n"
"ds_read_b64_tr_b16 a[60:61], v15 offset:4104\n"
"ds_read_b64_tr_b16 a[62:63], v15 offset:4616\n"
"s_waitcnt vmcnt(14)\n"
"v_lshlrev_b32 v16, 16, v18\n"
"v_and_b32 v17, s63, v18\n"
"v_lshlrev_b32 v18, 16, v19\n"
"v_and_b32 v19, s63, v19\n"
"v_add_f32 v16, 1.0, v16\n"
"v_add_f32 v17, 1.0, v17\n"
"v_add_f32 v18, 1.0, v18\n"
"v_add_f32 v19, 1.0, v19\n"
"v_lshlrev_b32 v20, 16, v22\n"
"v_and_b32 v21, s63, v22\n"
"v_lshlrev_b32 v22, 16, v23\n"
"v_and_b32 v23, s63, v23\n"
"v_add_f32 v20, 1.0, v20\n"
"v_add_f32 v21, 1.0, v21\n"
"v_add_f32 v22, 1.0, v22\n"
"v_add_f32 v23, 1.0, v23\n"
"v_lshlrev_b32 v24, 16, v26\n"
"v_and_b32 v25, s63, v26\n"
"v_lshlrev_b32 v26, 16, v27\n"
"v_and_b32 v27, s63, v27\n"
"v_add_f32 v24, 1.0, v24\n"
"v_add_f32 v25, 1.0, v25\n"
"v_add_f32 v26, 1.0, v26\n"
"v_add_f32 v27, 1.0, v27\n"
"v_lshlrev_b32 v28, 16, v30\n"
"v_and_b32 v29, s63, v30\n"
"v_lshlrev_b32 v30, 16, v31\n"
"v_and_b32 v31, s63, v31\n"
"v_add_f32 v28, 1.0, v28\n"
"v_add_f32 v29, 1.0, v29\n"
"v_add_f32 v30, 1.0, v30\n"
"v_add_f32 v31, 1.0, v31\n"
"v_accvgpr_write_b32 a0, v16\n"
"v_accvgpr_write_b32 a1, v17\n"
"v_accvgpr_write_b32 a2, v18\n"
"v_accvgpr_write_b32 a3, v19\n"
"v_accvgpr_write_b32 a4, v20\n"
"v_accvgpr_write_b32 a5, v21\n"
"v_accvgpr_write_b32 a6, v22\n"
"v_accvgpr_write_b32 a7, v23\n"
"v_accvgpr_write_b32 a8, v24\n"
"v_accvgpr_write_b32 a9, v25\n"
"v_accvgpr_write_b32 a10, v26\n"
"v_accvgpr_write_b32 a11, v27\n"
"v_accvgpr_write_b32 a12, v28\n"
"v_accvgpr_write_b32 a13, v29\n"
"v_accvgpr_write_b32 a14, v30\n"
"v_accvgpr_write_b32 a15, v31\n"
"s_bitcmp1_b32 %26, 0\n"
"s_cbranch_scc1 L_z\n"
"s_cmp_lt_u32 %22, 524288\n"
"s_cselect_b32 s38, 8192000, 0\n"
"s_add_u32 s70, %22, 6553600\n"
"s_sub_u32 s67, s72, s64\n"
"s_add_u32 s67, s67, 98304\n"
"v_add_u32 v40, s67, v9\n"
"v_add_u32 v41, s67, v15\n"
"s_sleep 1\n"
"ds_read_b64_tr_b16 a[160:161], v40 offset:40960\n"
"ds_read_b64_tr_b16 a[162:163], v40 offset:41472\n"
"ds_read_b64_tr_b16 a[164:165], v40 offset:40968\n"
"ds_read_b64_tr_b16 a[166:167], v40 offset:41480\n"
"ds_read_b64_tr_b16 a[168:169], v41 offset:40960\n"
"ds_read_b64_tr_b16 a[170:171], v41 offset:41472\n"
"ds_read_b64_tr_b16 a[172:173], v41 offset:40968\n"
"ds_read_b64_tr_b16 a[174:175], v41 offset:41480\n"
"ds_read_b64_tr_b16 a[176:177], v40 offset:0\n"
"ds_read_b64_tr_b16 a[178:179], v40 offset:512\n"
"ds_read_b64_tr_b16 a[180:181], v40 offset:8\n"
"ds_read_b64_tr_b16 a[182:183], v40 offset:520\n"
"ds_read_b64_tr_b16 a[184:185], v41 offset:0\n"
"ds_read_b64_tr_b16 a[186:187], v41 offset:512\n"
"ds_read_b64_tr_b16 a[188:189], v41 offset:8\n"
"ds_read_b64_tr_b16 a[190:191], v41 offset:520\n"
"s_waitcnt lgkmcnt(14)\n"
"buffer_store_dwordx4 a[160:163], v0, s[36:39], %22 offen offset:0 sc1\n"
"s_waitcnt lgkmcnt(12)\n"
"buffer_store_dwordx4 a[164:167], v0, s[36:39], %22 offen offset:2048 sc1\n"
"s_waitcnt lgkmcnt(10)\n"
"buffer_store_dwordx4 a[168:171], v1, s[36:39], %22 offen offset:0 sc1\n"
"s_waitcnt lgkmcnt(8)\n"
"buffer_store_dwordx4 a[172:175], v1, s[36:39], %22 offen offset:2048 sc1\n"
"s_waitcnt lgkmcnt(6)\n"
"buffer_store_dwordx4 a[176:179], v0, s[36:39], s70 offen offset:0 sc1\n"
"s_waitcnt lgkmcnt(4)\n"
"buffer_store_dwordx4 a[180:183], v0, s[36:39], s70 offen offset:2048 sc1\n"
"s_waitcnt lgkmcnt(2)\n"
"buffer_store_dwordx4 a[184:187], v1, s[36:39], s70 offen offset:0 sc1\n"
"s_waitcnt lgkmcnt(0)\n"
"buffer_store_dwordx4 a[188:191], v1, s[36:39], s70 offen offset:2048 sc1\n"
"s_sleep 10\n"
"L_z:\n"
"s_nop 1\n"
"s_waitcnt lgkmcnt(8)\n"
"v_mfma_f32_16x16x32_bf16 v[32:35], a[32:35], a[96:99], v[16:19]\n"
"s_add_u32 s70, s75, 393216\n"
"v_mfma_f32_16x16x32_bf16 v[32:35], a[36:39], a[100:103], v[32:35]\n"
"s_add_u32 s70, s70, s65\n"
"v_mfma_f32_16x16x32_bf16 v[60:63], a[44:47], a[100:103], v[20:23]\n"
"buffer_load_dword a252, v14, s[52:55], s70 offen\n"
"v_mfma_f32_16x16x32_bf16 v[60:63], a[40:43], a[96:99], v[60:63]\n"
"s_add_u32 m0, s64, 32768\n"
"v_mfma_f32_16x16x32_bf16 v[64:67], a[40:43], a[104:107], v[20:23]\n"
"s_add_u32 s67, s75, 131072\n"
"v_mfma_f32_16x16x32_bf16 v[64:67], a[44:47], a[108:111], v[64:67]\n"
"buffer_load_dwordx4 v11, s[52:55], s67 offen offset:0  lds\n"
"v_mfma_f32_16x16x32_bf16 v[36:39], a[36:39], a[108:111], v[16:19]\n"
"s_add_u32 s67, s75, 146432\n"
"v_mfma_f32_16x16x32_bf16 v[36:39], a[32:35], a[104:107], v[36:39]\n"
"v_mfma_f32_16x16x32_bf16 v[40:43], a[32:35], a[112:115], v[16:19]\n"
"s_waitcnt lgkmcnt(0)\n"
"buffer_load_dwordx4 v11, s[52:55], s67 offen offset:1024  lds\n"
"v_mfma_f32_16x16x32_bf16 v[40:43], a[36:39], a[116:119], v[40:43]\n"
"v_mfma_f32_16x16x32_bf16 v[68:71], a[44:47], a[116:119], v[20:23]\n"
"s_add_u32 s67, s75, 161792\n"
"v_mfma_f32_16x16x32_bf16 v[68:71], a[40:43], a[112:115], v[68:71]\n"
"v_mfma_f32_16x16x32_bf16 v[72:75], a[40:43], a[120:123], v[20:23]\n"
"buffer_load_dwordx4 v11, s[52:55], s67 offen offset:2048  lds\n"
"v_mfma_f32_16x16x32_bf16 v[72:75], a[44:47], a[124:127], v[72:75]\n"
"v_mfma_f32_16x16x32_bf16 v[44:47], a[36:39], a[124:127], v[16:19]\n"
"s_add_u32 s67, s75, 177152\n"
"v_mfma_f32_16x16x32_bf16 v[44:47], a[32:35], a[120:123], v[44:47]\n"
"v_mfma_f32_16x16x32_bf16 v[48:51], a[32:35], a[128:131], v[16:19]\n"
"buffer_load_dwordx4 v11, s[52:55], s67 offen offset:3072  lds\n"
"v_mfma_f32_16x16x32_bf16 v[48:51], a[36:39], a[132:135], v[48:51]\n"
"v_mfma_f32_16x16x32_bf16 v[76:79], a[44:47], a[132:135], v[20:23]\n"
"s_add_u32 m0, s64, 36864\n"
"v_mfma_f32_16x16x32_bf16 v[76:79], a[40:43], a[128:131], v[76:79]\n"
"v_mfma_f32_16x16x32_bf16 v[80:83], a[40:43], a[136:139], v[20:23]\n"
"s_add_u32 s67, s75, 196608\n"
"v_mfma_f32_16x16x32_bf16 v[80:83], a[44:47], a[140:143], v[80:83]\n"
"v_mfma_f32_16x16x32_bf16 v[52:55], a[36:39], a[140:143], v[16:19]\n"
"buffer_load_dwordx4 v11, s[52:55], s67 offen offset:0  lds\n"
"v_mfma_f32_16x16x32_bf16 v[52:55], a[32:35], a[136:139], v[52:55]\n"
"v_mfma_f32_16x16x32_bf16 v[56:59], a[32:35], a[144:147], v[16:19]\n"
"s_mov_b64 exec, 0\n"
"s_cmp_eq_u32 %16, 0\n"
"s_cbranch_scc1 L_st5078\n"
"s_cmp_eq_u32 %16, 1\n"
"s_cbranch_scc1 L_st5690\n"
"s_cmp_eq_u32 %16, 2\n"
"s_cbranch_scc1 L_st6226\n"
"s_branch L_st6780\n"
"L_retB:\n"
"s_mov_b64 exec, -1\n"
"s_nop 3\n"
"s_add_u32 s67, s75, 211968\n"
"v_mfma_f32_16x16x32_bf16 v[56:59], a[36:39], a[148:151], v[56:59]\n"
"v_mfma_f32_16x16x32_bf16 v[84:87], a[44:47], a[148:151], v[20:23]\n"
"buffer_load_dwordx4 v11, s[52:55], s67 offen offset:1024  lds\n"
"v_mfma_f32_16x16x32_bf16 v[84:87], a[40:43], a[144:147], v[84:87]\n"
"v_mfma_f32_16x16x32_bf16 a[4:7], a[40:43], a[152:155], a[4:7]\n"
"s_add_u32 s67, s75, 227328\n"
"v_mfma_f32_16x16x32_bf16 a[4:7], a[44:47], a[156:159], a[4:7]\n"
"v_mfma_f32_16x16x32_bf16 a[0:3], a[36:39], a[156:159], a[0:3]\n"
"buffer_load_dwordx4 v11, s[52:55], s67 offen offset:2048  lds\n"
"v_mfma_f32_16x16x32_bf16 a[0:3], a[32:35], a[152:155], a[0:3]\n"
"v_mfma_f32_16x16x32_bf16 v[88:91], a[48:51], a[96:99], v[24:27]\n"
"s_add_u32 s67, s75, 242688\n"
"v_mfma_f32_16x16x32_bf16 v[88:91], a[52:55], a[100:103], v[88:91]\n"
"v_mfma_f32_16x16x32_bf16 v[116:119], a[60:63], a[100:103], v[28:31]\n"
"v_mfma_f32_16x16x32_bf16 v[116:119], a[56:59], a[96:99], v[116:119]\n"
"buffer_load_dwordx4 v11, s[52:55], s67 offen offset:3072  lds\n"
"v_mfma_f32_16x16x32_bf16 v[120:123], a[56:59], a[104:107], v[28:31]\n"
"s_add_u32 m0, s64, 0\n"
"v_mfma_f32_16x16x32_bf16 v[120:123], a[60:63], a[108:111], v[120:123]\n"
"v_mfma_f32_16x16x32_bf16 v[92:95], a[52:55], a[108:111], v[24:27]\n"
"s_add_u32 s67, s75, 131200\n"
"v_mfma_f32_16x16x32_bf16 v[92:95], a[48:51], a[104:107], v[92:95]\n"
"v_mfma_f32_16x16x32_bf16 v[96:99], a[48:51], a[112:115], v[24:27]\n"
"buffer_load_dwordx4 v11, s[52:55], s67 offen offset:0  lds\n"
"v_mfma_f32_16x16x32_bf16 v[96:99], a[52:55], a[116:119], v[96:99]\n"
"v_mfma_f32_16x16x32_bf16 v[124:127], a[60:63], a[116:119], v[28:31]\n"
"s_add_u32 s67, s75, 146560\n"
"v_mfma_f32_16x16x32_bf16 v[124:127], a[56:59], a[112:115], v[124:127]\n"
"buffer_load_dwordx4 v11, s[52:55], s67 offen offset:1024  lds\n"
"v_mfma_f32_16x16x32_bf16 v[128:131], a[56:59], a[120:123], v[28:31]\n"
"s_waitcnt vmcnt(16)\n"
"ds_read_b64_tr_b16 a[64:65], v9 offset:8192\n"
"v_mfma_f32_16x16x32_bf16 v[128:131], a[60:63], a[124:127], v[128:131]\n"
"s_add_u32 s67, s75, 161920\n"
"ds_read_b64_tr_b16 a[66:67], v9 offset:8704\n"
"v_mfma_f32_16x16x32_bf16 v[100:103], a[52:55], a[124:127], v[24:27]\n"
"ds_read_b64_tr_b16 a[68:69], v9 offset:12288\n"
"v_mfma_f32_16x16x32_bf16 v[100:103], a[48:51], a[120:123], v[100:103]\n"
"ds_read_b64_tr_b16 a[70:71], v9 offset:12800\n"
"v_mfma_f32_16x16x32_bf16 v[104:107], a[48:51], a[128:131], v[24:27]\n"
"ds_read_b64_tr_b16 a[72:73], v9 offset:8200\n"
"v_mfma_f32_16x16x32_bf16 v[104:107], a[52:55], a[132:135], v[104:107]\n"
"ds_read_b64_tr_b16 a[74:75], v9 offset:8712\n"
"buffer_load_dwordx4 v11, s[52:55], s67 offen offset:2048  lds\n"
"v_mfma_f32_16x16x32_bf16 v[132:135], a[60:63], a[132:135], v[28:31]\n"
"ds_read_b64_tr_b16 a[76:77], v9 offset:12296\n"
"v_mfma_f32_16x16x32_bf16 v[132:135], a[56:59], a[128:131], v[132:135]\n"
"s_add_u32 s67, s75, 177280\n"
"ds_read_b64_tr_b16 a[78:79], v9 offset:12808\n"
"v_mfma_f32_16x16x32_bf16 v[136:139], a[56:59], a[136:139], v[28:31]\n"
"buffer_load_dwordx4 v11, s[52:55], s67 offen offset:3072  lds\n"
"v_mfma_f32_16x16x32_bf16 v[136:139], a[60:63], a[140:143], v[136:139]\n"
"ds_read_b64_tr_b16 a[80:81], v15 offset:8192\n"
"v_mfma_f32_16x16x32_bf16 v[108:111], a[52:55], a[140:143], v[24:27]\n"
"s_add_u32 m0, s64, 4096\n"
"ds_read_b64_tr_b16 a[82:83], v15 offset:8704\n"
"v_mfma_f32_16x16x32_bf16 v[108:111], a[48:51], a[136:139], v[108:111]\n"
"ds_read_b64_tr_b16 a[84:85], v15 offset:12288\n"
"v_mfma_f32_16x16x32_bf16 v[112:115], a[48:51], a[144:147], v[24:27]\n"
"s_add_u32 s67, s75, 196736\n"
"ds_read_b64_tr_b16 a[86:87], v15 offset:12800\n"
"v_mfma_f32_16x16x32_bf16 v[112:115], a[52:55], a[148:151], v[112:115]\n"
"ds_read_b64_tr_b16 a[88:89], v15 offset:8200\n"
"v_mfma_f32_16x16x32_bf16 v[140:143], a[60:63], a[148:151], v[28:31]\n"
"ds_read_b64_tr_b16 a[90:91], v15 offset:8712\n"
"buffer_load_dwordx4 v11, s[52:55], s67 offen offset:0  lds\n"
"v_mfma_f32_16x16x32_bf16 v[140:143], a[56:59], a[144:147], v[140:143]\n"
"ds_read_b64_tr_b16 a[92:93], v15 offset:12296\n"
"v_mfma_f32_16x16x32_bf16 a[12:15], a[56:59], a[152:155], a[12:15]\n"
"s_add_u32 s67, s75, 212096\n"
"ds_read_b64_tr_b16 a[94:95], v15 offset:12808\n"
"v_mfma_f32_16x16x32_bf16 a[12:15], a[60:63], a[156:159], a[12:15]\n"
"v_mfma_f32_16x16x32_bf16 a[8:11], a[52:55], a[156:159], a[8:11]\n"
"buffer_load_dwordx4 v11, s[52:55], s67 offen offset:1024  lds\n"
"v_mfma_f32_16x16x32_bf16 a[8:11], a[48:51], a[152:155], a[8:11]\n"
"s_waitcnt lgkmcnt(8)\n"
"v_mfma_f32_16x16x32_bf16 v[144:147], a[64:67], a[96:99], 1.0\n"
"s_add_u32 s67, s75, 227456\n"
"v_mfma_f32_16x16x32_bf16 v[144:147], a[68:71], a[100:103], v[144:147]\n"
"v_mfma_f32_16x16x32_bf16 v[172:175], a[76:79], a[100:103], 1.0\n"
"s_waitcnt vmcnt(19) lgkmcnt(0)\n"
"v_readfirstlane_b32 s78, v252\n"
"v_readfirstlane_b32 s79, v253\n"
"v_mfma_f32_16x16x32_bf16 v[172:175], a[72:75], a[96:99], v[172:175]\n"
"buffer_load_dwordx4 v11, s[52:55], s67 offen offset:2048  lds\n"
"v_mfma_f32_16x16x32_bf16 v[176:179], a[72:75], a[104:107], 1.0\n"
"v_mfma_f32_16x16x32_bf16 v[176:179], a[76:79], a[108:111], v[176:179]\n"
"s_add_u32 s67, s75, 242816\n"
"v_mfma_f32_16x16x32_bf16 v[148:151], a[68:71], a[108:111], 1.0\n"
"s_add_u32 s78, s78, 16\n"
"v_mfma_f32_16x16x32_bf16 v[148:151], a[64:67], a[104:107], v[148:151]\n"
"s_add_u32 s79, s79, 16\n"
"v_mfma_f32_16x16x32_bf16 v[152:155], a[64:67], a[112:115], 1.0\n"
"s_sub_u32 s66, 40960, s73\n"
"v_mfma_f32_16x16x32_bf16 v[152:155], a[68:71], a[116:119], v[152:155]\n"
"buffer_load_dwordx4 v11, s[52:55], s67 offen offset:3072  lds\n"
"v_mfma_f32_16x16x32_bf16 v[180:183], a[76:79], a[116:119], 1.0\n"
"s_add_u32 m0, s72, 98304\n"
"v_add_u32 v12, s66, v7\n"
"v_mfma_f32_16x16x32_bf16 v[180:183], a[72:75], a[112:115], v[180:183]\n"
"v_mfma_f32_16x16x32_bf16 v[184:187], a[72:75], a[120:123], 1.0\n"
"s_add_u32 s67, s60, 256\n"
"v_add_u32 v13, s66, v8\n"
"v_mfma_f32_16x16x32_bf16 v[184:187], a[76:79], a[124:127], v[184:187]\n"
"buffer_load_dwordx4 v6, s[48:51], s67 offen offset:0  lds\n"
"v_mfma_f32_16x16x32_bf16 v[156:159], a[68:71], a[124:127], 1.0\n"
"L_st518:\n"
"s_cbranch_execz L_st2664\n"
"v_mfma_f32_16x16x32_bf16 v[156:159], a[64:67], a[120:123], v[156:159]\n"
"s_add_u32 s67, s60, 15616\n"
"v_mfma_f32_16x16x32_bf16 v[160:163], a[64:67], a[128:131], 1.0\n"
"v_mfma_f32_16x16x32_bf16 v[160:163], a[68:71], a[132:135], v[160:163]\n"
"buffer_load_dwordx4 v6, s[48:51], s67 offen offset:1024  lds\n"
"v_mfma_f32_16x16x32_bf16 v[188:191], a[76:79], a[132:135], 1.0\n"
"v_mfma_f32_16x16x32_bf16 v[188:191], a[72:75], a[128:131], v[188:191]\n"
"s_add_u32 s67, s60, 30976\n"
"v_mfma_f32_16x16x32_bf16 v[192:195], a[72:75], a[136:139], 1.0\n"
"v_mfma_f32_16x16x32_bf16 v[192:195], a[76:79], a[140:143], v[192:195]\n"
"buffer_load_dwordx4 v6, s[48:51], s67 offen offset:2048  lds\n"
"v_mfma_f32_16x16x32_bf16 v[164:167], a[68:71], a[140:143], 1.0\n"
"v_mfma_f32_16x16x32_bf16 v[164:167], a[64:67], a[136:139], v[164:167]\n"
"s_add_u32 s67, s60, 46336\n"
"v_mfma_f32_16x16x32_bf16 v[168:171], a[64:67], a[144:147], 1.0\n"
"v_mfma_f32_16x16x32_bf16 v[168:171], a[68:71], a[148:151], v[168:171]\n"
"v_mfma_f32_16x16x32_bf16 v[196:199], a[76:79], a[148:151], 1.0\n"
"buffer_load_dwordx4 v6, s[48:51], s67 offen offset:3072  lds\n"
"v_mfma_f32_16x16x32_bf16 v[196:199], a[72:75], a[144:147], v[196:199]\n"
"v_mfma_f32_16x16x32_bf16 a[20:23], a[72:75], a[152:155], 1.0\n"
"v_mfma_f32_16x16x32_bf16 a[20:23], a[76:79], a[156:159], a[20:23]\n"
"v_mfma_f32_16x16x32_bf16 a[16:19], a[68:71], a[156:159], 1.0\n"
"v_mfma_f32_16x16x32_bf16 a[16:19], a[64:67], a[152:155], a[16:19]\n"
"s_waitcnt vmcnt(21)\n"
"s_barrier\n"
"v_mfma_f32_16x16x32_bf16 v[200:203], a[80:83], a[96:99], 1.0\n"
"s_waitcnt vmcnt(12)\n"
"ds_read_b64_tr_b16 a[32:33], v9 offset:32768\n"
"v_mfma_f32_16x16x32_bf16 v[200:203], a[84:87], a[100:103], v[200:203]\n"
"s_add_u32 m0, s64, 8192\n"
"ds_read_b64_tr_b16 a[34:35], v9 offset:33280\n"
"v_mfma_f32_16x16x32_bf16 v[228:231], a[92:95], a[100:103], 1.0\n"
"ds_read_b128 a[100:103], v13 offset:16384\n"
"v_mfma_f32_16x16x32_bf16 v[228:231], a[88:91], a[96:99], v[228:231]\n"
"ds_read_b128 a[96:99], v12 offset:16384\n"
"v_mfma_f32_16x16x32_bf16 v[232:235], a[88:91], a[104:107], 1.0\n"
"ds_read_b64_tr_b16 a[36:37], v9 offset:36864\n"
"v_mfma_f32_16x16x32_bf16 v[232:235], a[92:95], a[108:111], v[232:235]\n"
"ds_read_b64_tr_b16 a[38:39], v9 offset:37376\n"
"v_mfma_f32_16x16x32_bf16 v[204:207], a[84:87], a[108:111], 1.0\n"
"ds_read_b128 a[108:111], v13 offset:18432\n"
"v_mfma_f32_16x16x32_bf16 v[204:207], a[80:83], a[104:107], v[204:207]\n"
"ds_read_b128 a[104:107], v12 offset:18432\n"
"ds_read_b64_tr_b16 a[40:41], v9 offset:32776\n"
"v_mfma_f32_16x16x32_bf16 v[208:211], a[80:83], a[112:115], 1.0\n"
"ds_read_b64_tr_b16 a[42:43], v9 offset:33288\n"
"v_mfma_f32_16x16x32_bf16 v[208:211], a[84:87], a[116:119], v[208:211]\n"
"ds_read_b64_tr_b16 a[44:45], v9 offset:36872\n"
"v_mfma_f32_16x16x32_bf16 v[236:239], a[92:95], a[116:119], 1.0\n"
"ds_read_b128 a[116:119], v13 offset:20480\n"
"v_mfma_f32_16x16x32_bf16 v[236:239], a[88:91], a[112:115], v[236:239]\n"
"ds_read_b128 a[112:115], v12 offset:20480\n"
"v_mfma_f32_16x16x32_bf16 v[240:243], a[88:91], a[120:123], 1.0\n"
"ds_read_b64_tr_b16 a[46:47], v9 offset:37384\n"
"v_mfma_f32_16x16x32_bf16 v[240:243], a[92:95], a[124:127], v[240:243]\n"
"ds_read_b64_tr_b16 a[48:49], v15 offset:32768\n"
"v_mfma_f32_16x16x32_bf16 v[212:215], a[84:87], a[124:127], 1.0\n"
"s_add_u32 s67, s75, 262144\n"
"ds_read_b128 a[124:127], v13 offset:22528\n"
"v_mfma_f32_16x16x32_bf16 v[212:215], a[80:83], a[120:123], v[212:215]\n"
"ds_read_b128 a[120:123], v12 offset:22528\n"
"v_mfma_f32_16x16x32_bf16 v[216:219], a[80:83], a[128:131], 1.0\n"
"ds_read_b64_tr_b16 a[50:51], v15 offset:33280\n"
"v_mfma_f32_16x16x32_bf16 v[216:219], a[84:87], a[132:135], v[216:219]\n"
"ds_read_b64_tr_b16 a[52:53], v15 offset:36864\n"
"v_mfma_f32_16x16x32_bf16 v[244:247], a[92:95], a[132:135], 1.0\n"
"ds_read_b128 a[132:135], v13 offset:24576\n"
"v_mfma_f32_16x16x32_bf16 v[244:247], a[88:91], a[128:131], v[244:247]\n"
"ds_read_b128 a[128:131], v12 offset:24576\n"
"v_mfma_f32_16x16x32_bf16 v[248:251], a[88:91], a[136:139], 1.0\n"
"ds_read_b64_tr_b16 a[54:55], v15 offset:37376\n"
"v_mfma_f32_16x16x32_bf16 v[248:251], a[92:95], a[140:143], v[248:251]\n"
"ds_read_b64_tr_b16 a[56:57], v15 offset:32776\n"
"v_mfma_f32_16x16x32_bf16 v[220:223], a[84:87], a[140:143], 1.0\n"
"ds_read_b128 a[140:143], v13 offset:26624\n"
"v_mfma_f32_16x16x32_bf16 v[220:223], a[80:83], a[136:139], v[220:223]\n"
"ds_read_b128 a[136:139], v12 offset:26624\n"
"v_mfma_f32_16x16x32_bf16 v[224:227], a[80:83], a[144:147], 1.0\n"
"ds_read_b64_tr_b16 a[58:59], v15 offset:33288\n"
"v_mfma_f32_16x16x32_bf16 v[224:227], a[84:87], a[148:151], v[224:227]\n"
"ds_read_b64_tr_b16 a[60:61], v15 offset:36872\n"
"v_mfma_f32_16x16x32_bf16 v[252:255], a[92:95], a[148:151], 1.0\n"
"ds_read_b128 a[148:151], v13 offset:28672\n"
"v_mfma_f32_16x16x32_bf16 v[252:255], a[88:91], a[144:147], v[252:255]\n"
"ds_read_b128 a[144:147], v12 offset:28672\n"
"ds_read_b64_tr_b16 a[62:63], v15 offset:37384\n"
"v_mfma_f32_16x16x32_bf16 a[28:31], a[88:91], a[152:155], 1.0\n"
"v_mfma_f32_16x16x32_bf16 a[28:31], a[92:95], a[156:159], a[28:31]\n"
"v_mfma_f32_16x16x32_bf16 a[24:27], a[84:87], a[156:159], 1.0\n"
"ds_read_b128 a[156:159], v13 offset:30720\n"
"v_mfma_f32_16x16x32_bf16 a[24:27], a[80:83], a[152:155], a[24:27]\n"
"s_waitcnt lgkmcnt(15)\n"
"ds_read_b128 a[152:155], v12 offset:30720\n"
"v_mfma_f32_16x16x32_bf16 v[32:35], a[32:35], a[96:99], v[32:35]\n"
"buffer_load_dwordx4 v11, s[52:55], s67 offen offset:0  lds\n"
"v_mfma_f32_16x16x32_bf16 v[32:35], a[36:39], a[100:103], v[32:35]\n"
"s_waitcnt lgkmcnt(2)\n"
"v_mfma_f32_16x16x32_bf16 v[60:63], a[44:47], a[100:103], v[60:63]\n"
"s_add_u32 s67, s75, 277504\n"
"v_mfma_f32_16x16x32_bf16 v[60:63], a[40:43], a[96:99], v[60:63]\n"
"v_mfma_f32_16x16x32_bf16 v[64:67], a[40:43], a[104:107], v[64:67]\n"
"v_mfma_f32_16x16x32_bf16 v[64:67], a[44:47], a[108:111], v[64:67]\n"
"buffer_load_dwordx4 v11, s[52:55], s67 offen offset:1024  lds\n"
"v_mfma_f32_16x16x32_bf16 v[36:39], a[36:39], a[108:111], v[36:39]\n"
"v_mfma_f32_16x16x32_bf16 v[36:39], a[32:35], a[104:107], v[36:39]\n"
"s_add_u32 s67, s75, 292864\n"
"v_mfma_f32_16x16x32_bf16 v[40:43], a[32:35], a[112:115], v[40:43]\n"
"v_mfma_f32_16x16x32_bf16 v[40:43], a[36:39], a[116:119], v[40:43]\n"
"v_mfma_f32_16x16x32_bf16 v[68:71], a[44:47], a[116:119], v[68:71]\n"
"buffer_load_dwordx4 v11, s[52:55], s67 offen offset:2048  lds\n"
"v_mfma_f32_16x16x32_bf16 v[68:71], a[40:43], a[112:115], v[68:71]\n"
"v_mfma_f32_16x16x32_bf16 v[72:75], a[40:43], a[120:123], v[72:75]\n"
"s_add_u32 s67, s75, 308224\n"
"v_mfma_f32_16x16x32_bf16 v[72:75], a[44:47], a[124:127], v[72:75]\n"
"v_mfma_f32_16x16x32_bf16 v[44:47], a[36:39], a[124:127], v[44:47]\n"
"v_mov_b32 v16, -1\n"
"v_mfma_f32_16x16x32_bf16 v[44:47], a[32:35], a[120:123], v[44:47]\n"
"buffer_load_dwordx4 v11, s[52:55], s67 offen offset:3072  lds\n"
"v_mfma_f32_16x16x32_bf16 v[48:51], a[32:35], a[128:131], v[48:51]\n"
"s_add_u32 m0, s64, 12288\n"
"v_mfma_f32_16x16x32_bf16 v[48:51], a[36:39], a[132:135], v[48:51]\n"
"v_mov_b32 v17, -1\n"
"v_mfma_f32_16x16x32_bf16 v[76:79], a[44:47], a[132:135], v[76:79]\n"
"s_waitcnt vmcnt(8)\n"
"ds_read_b64_tr_b16 a[64:65], v9 offset:0\n"
"v_mfma_f32_16x16x32_bf16 v[76:79], a[40:43], a[128:131], v[76:79]\n"
"s_add_u32 s67, s75, 327680\n"
"ds_read_b64_tr_b16 a[66:67], v9 offset:512\n"
"v_mfma_f32_16x16x32_bf16 v[80:83], a[40:43], a[136:139], v[80:83]\n"
"ds_read_b64_tr_b16 a[68:69], v9 offset:4096\n"
"buffer_load_dwordx4 v11, s[52:55], s67 offen offset:0  lds\n"
"v_mfma_f32_16x16x32_bf16 v[80:83], a[44:47], a[140:143], v[80:83]\n"
"ds_read_b64_tr_b16 a[70:71], v9 offset:4608\n"
"v_mfma_f32_16x16x32_bf16 v[52:55], a[36:39], a[140:143], v[52:55]\n"
"s_add_u32 s67, s75, 343040\n"
"ds_read_b64_tr_b16 a[72:73], v9 offset:8\n"
"v_mfma_f32_16x16x32_bf16 v[52:55], a[32:35], a[136:139], v[52:55]\n"
"s_waitcnt lgkmcnt(5)\n"
"ds_read_b64_tr_b16 a[74:75], v9 offset:520\n"
"v_mfma_f32_16x16x32_bf16 v[56:59], a[32:35], a[144:147], v[56:59]\n"
"ds_read_b64_tr_b16 a[76:77], v9 offset:4104\n"
"buffer_load_dwordx4 v11, s[52:55], s67 offen offset:1024  lds\n"
"v_mfma_f32_16x16x32_bf16 v[56:59], a[36:39], a[148:151], v[56:59]\n"
"s_add_u32 s67, s75, 358400\n"
"ds_read_b64_tr_b16 a[78:79], v9 offset:4616\n"
"v_mfma_f32_16x16x32_bf16 v[84:87], a[44:47], a[148:151], v[84:87]\n"
"s_add_u32 s69, %11, 8192\n"
"v_lshrrev_b32 v18, 1, v0\n"
"v_mfma_f32_16x16x32_bf16 v[84:87], a[40:43], a[144:147], v[84:87]\n"
"ds_read_b64_tr_b16 a[80:81], v15 offset:0\n"
"v_mfma_f32_16x16x32_bf16 a[4:7], a[40:43], a[152:155], a[4:7]\n"
"ds_read_b64_tr_b16 a[82:83], v15 offset:512\n"
"buffer_load_dwordx4 v11, s[52:55], s67 offen offset:2048  lds\n"
"v_mfma_f32_16x16x32_bf16 a[4:7], a[44:47], a[156:159], a[4:7]\n"
"ds_read_b64_tr_b16 a[84:85], v15 offset:4096\n"
"v_mfma_f32_16x16x32_bf16 a[0:3], a[36:39], a[156:159], a[0:3]\n"
"s_add_u32 s67, s75, 373760\n"
"ds_read_b64_tr_b16 a[86:87], v15 offset:4608\n"
"v_mfma_f32_16x16x32_bf16 a[0:3], a[32:35], a[152:155], a[0:3]\n"
"ds_read_b64_tr_b16 a[88:89], v15 offset:8\n"
"v_mfma_f32_16x16x32_bf16 v[88:91], a[48:51], a[96:99], v[88:91]\n"
"ds_read_b64_tr_b16 a[90:91], v15 offset:520\n"
"v_mfma_f32_16x16x32_bf16 v[88:91], a[52:55], a[100:103], v[88:91]\n"
"ds_read_b64_tr_b16 a[92:93], v15 offset:4104\n"
"buffer_load_dwordx4 v11, s[52:55], s67 offen offset:3072  lds\n"
"v_mfma_f32_16x16x32_bf16 v[116:119], a[60:63], a[100:103], v[116:119]\n"
"ds_read_b64_tr_b16 a[94:95], v15 offset:4616\n"
"v_mfma_f32_16x16x32_bf16 v[116:119], a[56:59], a[96:99], v[116:119]\n"
"s_add_u32 m0, s64, 0\n"
"v_mfma_f32_16x16x32_bf16 v[120:123], a[56:59], a[104:107], v[120:123]\n"
"v_mfma_f32_16x16x32_bf16 v[120:123], a[60:63], a[108:111], v[120:123]\n"
"v_mfma_f32_16x16x32_bf16 v[92:95], a[52:55], a[108:111], v[92:95]\n"
"s_add_u32 s67, s75, 262272\n"
"v_mfma_f32_16x16x32_bf16 v[92:95], a[48:51], a[104:107], v[92:95]\n"
"v_mfma_f32_16x16x32_bf16 v[96:99], a[48:51], a[112:115], v[96:99]\n"
"s_waitcnt lgkmcnt(0)\n"
"v_mfma_f32_16x16x32_bf16 v[96:99], a[52:55], a[116:119], v[96:99]\n"
"buffer_load_dwordx4 v11, s[52:55], s67 offen offset:0  lds\n"
"v_mfma_f32_16x16x32_bf16 v[124:127], a[60:63], a[116:119], v[124:127]\n"
"v_mfma_f32_16x16x32_bf16 v[124:127], a[56:59], a[112:115], v[124:127]\n"
"s_add_u32 s67, s75, 277632\n"
"v_mfma_f32_16x16x32_bf16 v[128:131], a[56:59], a[120:123], v[128:131]\n"
"v_mfma_f32_16x16x32_bf16 v[128:131], a[60:63], a[124:127], v[128:131]\n"
"v_mfma_f32_16x16x32_bf16 v[100:103], a[52:55], a[124:127], v[100:103]\n"
"buffer_load_dwordx4 v11, s[52:55], s67 offen offset:1024  lds\n"
"v_mfma_f32_16x16x32_bf16 v[100:103], a[48:51], a[120:123], v[100:103]\n"
"v_mfma_f32_16x16x32_bf16 v[104:107], a[48:51], a[128:131], v[104:107]\n"
"s_add_u32 s67, s75, 292992\n"
"v_mfma_f32_16x16x32_bf16 v[104:107], a[52:55], a[132:135], v[104:107]\n"
"v_mfma_f32_16x16x32_bf16 v[132:135], a[60:63], a[132:135], v[132:135]\n"
"buffer_load_dwordx4 v11, s[52:55], s67 offen offset:2048  lds\n"
"v_mfma_f32_16x16x32_bf16 v[132:135], a[56:59], a[128:131], v[132:135]\n"
"v_mfma_f32_16x16x32_bf16 v[136:139], a[56:59], a[136:139], v[136:139]\n"
"s_add_u32 s67, s75, 308352\n"
"v_mfma_f32_16x16x32_bf16 v[136:139], a[60:63], a[140:143], v[136:139]\n"
"v_mfma_f32_16x16x32_bf16 v[108:111], a[52:55], a[140:143], v[108:111]\n"
"v_mfma_f32_16x16x32_bf16 v[108:111], a[48:51], a[136:139], v[108:111]\n"
"buffer_load_dwordx4 v11, s[52:55], s67 offen offset:3072  lds\n"
"v_mfma_f32_16x16x32_bf16 v[112:115], a[48:51], a[144:147], v[112:115]\n"
"v_mfma_f32_16x16x32_bf16 v[112:115], a[52:55], a[148:151], v[112:115]\n"
"s_add_u32 m0, s64, 4096\n"
"v_mfma_f32_16x16x32_bf16 v[140:143], a[60:63], a[148:151], v[140:143]\n"
"v_mfma_f32_16x16x32_bf16 v[140:143], a[56:59], a[144:147], v[140:143]\n"
"s_add_u32 s67, s75, 327808\n"
"v_mfma_f32_16x16x32_bf16 a[12:15], a[56:59], a[152:155], a[12:15]\n"
"v_mfma_f32_16x16x32_bf16 a[12:15], a[60:63], a[156:159], a[12:15]\n"
"buffer_load_dwordx4 v11, s[52:55], s67 offen offset:0  lds\n"
"v_mfma_f32_16x16x32_bf16 a[8:11], a[52:55], a[156:159], a[8:11]\n"
"v_mfma_f32_16x16x32_bf16 a[8:11], a[48:51], a[152:155], a[8:11]\n"
"s_add_u32 s67, s75, 343168\n"
"v_mfma_f32_16x16x32_bf16 v[144:147], a[64:67], a[96:99], v[144:147]\n"
"v_mfma_f32_16x16x32_bf16 v[144:147], a[68:71], a[100:103], v[144:147]\n"
"buffer_load_dwordx4 v11, s[52:55], s67 offen offset:1024  lds\n"
"v_mfma_f32_16x16x32_bf16 v[172:175], a[76:79], a[100:103], v[172:175]\n"
"v_mfma_f32_16x16x32_bf16 v[172:175], a[72:75], a[96:99], v[172:175]\n"
"s_add_u32 s67, s75, 358528\n"
"v_mfma_f32_16x16x32_bf16 v[176:179], a[72:75], a[104:107], v[176:179]\n"
"v_mfma_f32_16x16x32_bf16 v[176:179], a[76:79], a[108:111], v[176:179]\n"
"buffer_load_dwordx4 v11, s[52:55], s67 offen offset:2048  lds\n"
"v_mfma_f32_16x16x32_bf16 v[148:151], a[68:71], a[108:111], v[148:151]\n"
"v_mfma_f32_16x16x32_bf16 v[148:151], a[64:67], a[104:107], v[148:151]\n"
"s_add_u32 s67, s75, 373888\n"
"v_mfma_f32_16x16x32_bf16 v[152:155], a[64:67], a[112:115], v[152:155]\n"
"s_sub_u32 s66, 81920, s73\n"
"v_mfma_f32_16x16x32_bf16 v[152:155], a[68:71], a[116:119], v[152:155]\n"
"buffer_load_dwordx4 v11, s[52:55], s67 offen offset:3072  lds\n"
"v_mfma_f32_16x16x32_bf16 v[180:183], a[76:79], a[116:119], v[180:183]\n"
"v_add_u32 v12, s66, v7\n"
"v_mfma_f32_16x16x32_bf16 v[180:183], a[72:75], a[112:115], v[180:183]\n"
"s_add_u32 m0, s72, 139264\n"
"v_mfma_f32_16x16x32_bf16 v[184:187], a[72:75], a[120:123], v[184:187]\n"
"v_add_u32 v13, s66, v8\n"
"v_mfma_f32_16x16x32_bf16 v[184:187], a[76:79], a[124:127], v[184:187]\n"
"s_add_u32 s67, s60, 384\n"
"v_mfma_f32_16x16x32_bf16 v[156:159], a[68:71], a[124:127], v[156:159]\n"
"v_mfma_f32_16x16x32_bf16 v[156:159], a[64:67], a[120:123], v[156:159]\n"
"buffer_load_dwordx4 v6, s[48:51], s67 offen offset:0  lds\n"
"v_mfma_f32_16x16x32_bf16 v[160:163], a[64:67], a[128:131], v[160:163]\n"
"v_mfma_f32_16x16x32_bf16 v[160:163], a[68:71], a[132:135], v[160:163]\n"
"s_add_u32 s67, s60, 15744\n"
"v_mfma_f32_16x16x32_bf16 v[188:191], a[76:79], a[132:135], v[188:191]\n"
"v_mfma_f32_16x16x32_bf16 v[188:191], a[72:75], a[128:131], v[188:191]\n"
"buffer_load_dwordx4 v6, s[48:51], s67 offen offset:1024  lds\n"
"v_mfma_f32_16x16x32_bf16 v[192:195], a[72:75], a[136:139], v[192:195]\n"
"v_mfma_f32_16x16x32_bf16 v[192:195], a[76:79], a[140:143], v[192:195]\n"
"s_add_u32 s67, s60, 31104\n"
"v_mfma_f32_16x16x32_bf16 v[164:167], a[68:71], a[140:143], v[164:167]\n"
"v_mfma_f32_16x16x32_bf16 v[164:167], a[64:67], a[136:139], v[164:167]\n"
"buffer_load_dwordx4 v6, s[48:51], s67 offen offset:2048  lds\n"
"v_mfma_f32_16x16x32_bf16 v[168:171], a[64:67], a[144:147], v[168:171]\n"
"v_mfma_f32_16x16x32_bf16 v[168:171], a[68:71], a[148:151], v[168:171]\n"
"s_add_u32 s67, s60, 46464\n"
"v_mfma_f32_16x16x32_bf16 v[196:199], a[76:79], a[148:151], v[196:199]\n"
"v_mfma_f32_16x16x32_bf16 v[196:199], a[72:75], a[144:147], v[196:199]\n"
"v_mfma_f32_16x16x32_bf16 a[20:23], a[72:75], a[152:155], a[20:23]\n"
"buffer_load_dwordx4 v6, s[48:51], s67 offen offset:3072  lds\n"
"v_mfma_f32_16x16x32_bf16 a[20:23], a[76:79], a[156:159], a[20:23]\n"
"s_add_u32 s70, s75, 655360\n"
"v_mfma_f32_16x16x32_bf16 a[16:19], a[68:71], a[156:159], a[16:19]\n"
"s_add_u32 s70, s70, s65\n"
"v_mfma_f32_16x16x32_bf16 a[16:19], a[64:67], a[152:155], a[16:19]\n"
"s_waitcnt vmcnt(20)\n"
"s_barrier\n"
"v_mfma_f32_16x16x32_bf16 v[200:203], a[80:83], a[96:99], v[200:203]\n"
"s_waitcnt vmcnt(12)\n"
"ds_read_b64_tr_b16 a[32:33], v9 offset:8192\n"
"v_mfma_f32_16x16x32_bf16 v[200:203], a[84:87], a[100:103], v[200:203]\n"
"ds_read_b64_tr_b16 a[34:35], v9 offset:8704\n"
"buffer_load_dword a252, v14, s[52:55], s70 offen\n"
"v_mfma_f32_16x16x32_bf16 v[228:231], a[92:95], a[100:103], v[228:231]\n"
"ds_read_b128 a[100:103], v13 offset:16384\n"
"v_mfma_f32_16x16x32_bf16 v[228:231], a[88:91], a[96:99], v[228:231]\n"
"ds_read_b128 a[96:99], v12 offset:16384\n"
"v_mfma_f32_16x16x32_bf16 v[232:235], a[88:91], a[104:107], v[232:235]\n"
"ds_read_b64_tr_b16 a[36:37], v9 offset:12288\n"
"v_mfma_f32_16x16x32_bf16 v[232:235], a[92:95], a[108:111], v[232:235]\n"
"ds_read_b64_tr_b16 a[38:39], v9 offset:12800\n"
"v_mfma_f32_16x16x32_bf16 v[204:207], a[84:87], a[108:111], v[204:207]\n"
"ds_read_b128 a[108:111], v13 offset:18432\n"
"v_mfma_f32_16x16x32_bf16 v[204:207], a[80:83], a[104:107], v[204:207]\n"
"ds_read_b128 a[104:107], v12 offset:18432\n"
"s_add_u32 m0, s64, 8192\n"
"ds_read_b64_tr_b16 a[40:41], v9 offset:8200\n"
"v_mfma_f32_16x16x32_bf16 v[208:211], a[80:83], a[112:115], v[208:211]\n"
"ds_read_b64_tr_b16 a[42:43], v9 offset:8712\n"
"v_mfma_f32_16x16x32_bf16 v[208:211], a[84:87], a[116:119], v[208:211]\n"
"ds_read_b64_tr_b16 a[44:45], v9 offset:12296\n"
"v_mfma_f32_16x16x32_bf16 v[236:239], a[92:95], a[116:119], v[236:239]\n"
"ds_read_b128 a[116:119], v13 offset:20480\n"
"v_mfma_f32_16x16x32_bf16 v[236:239], a[88:91], a[112:115], v[236:239]\n"
"ds_read_b128 a[112:115], v12 offset:20480\n"
"v_mfma_f32_16x16x32_bf16 v[240:243], a[88:91], a[120:123], v[240:243]\n"
"ds_read_b64_tr_b16 a[46:47], v9 offset:12808\n"
"v_mfma_f32_16x16x32_bf16 v[240:243], a[92:95], a[124:127], v[240:243]\n"
"ds_read_b64_tr_b16 a[48:49], v15 offset:8192\n"
"v_mfma_f32_16x16x32_bf16 v[212:215], a[84:87], a[124:127], v[212:215]\n"
"ds_read_b128 a[124:127], v13 offset:22528\n"
"v_mfma_f32_16x16x32_bf16 v[212:215], a[80:83], a[120:123], v[212:215]\n"
"ds_read_b128 a[120:123], v12 offset:22528\n"
"v_mfma_f32_16x16x32_bf16 v[216:219], a[80:83], a[128:131], v[216:219]\n"
"ds_read_b64_tr_b16 a[50:51], v15 offset:8704\n"
"v_mfma_f32_16x16x32_bf16 v[216:219], a[84:87], a[132:135], v[216:219]\n"
"ds_read_b64_tr_b16 a[52:53], v15 offset:12288\n"
"v_mfma_f32_16x16x32_bf16 v[244:247], a[92:95], a[132:135], v[244:247]\n"
"ds_read_b128 a[132:135], v13 offset:24576\n"
"v_mfma_f32_16x16x32_bf16 v[244:247], a[88:91], a[128:131], v[244:247]\n"
"s_add_u32 s67, s75, 393216\n"
"ds_read_b128 a[128:131], v12 offset:24576\n"
"v_mfma_f32_16x16x32_bf16 v[248:251], a[88:91], a[136:139], v[248:251]\n"
"ds_read_b64_tr_b16 a[54:55], v15 offset:12800\n"
"v_mfma_f32_16x16x32_bf16 v[248:251], a[92:95], a[140:143], v[248:251]\n"
"s_add_u32 s66, s78, 1\n"
"ds_read_b64_tr_b16 a[56:57], v15 offset:8200\n"
"v_mfma_f32_16x16x32_bf16 v[220:223], a[84:87], a[140:143], v[220:223]\n"
"ds_read_b128 a[140:143], v13 offset:26624\n"
"v_mfma_f32_16x16x32_bf16 v[220:223], a[80:83], a[136:139], v[220:223]\n"
"ds_read_b128 a[136:139], v12 offset:26624\n"
"v_mov_b32 v28, s66\n"
"v_mfma_f32_16x16x32_bf16 v[224:227], a[80:83], a[144:147], v[224:227]\n"
"ds_read_b64_tr_b16 a[58:59], v15 offset:8712\n"
"v_mov_b32 v29, s79\n"
"v_mfma_f32_16x16x32_bf16 v[224:227], a[84:87], a[148:151], v[224:227]\n"
"ds_read_b64_tr_b16 a[60:61], v15 offset:12296\n"
"v_mfma_f32_16x16x32_bf16 v[252:255], a[92:95], a[148:151], v[252:255]\n"
"buffer_store_dwordx2 v[28:29], off, s[56:59], %24 sc1\n"
"ds_read_b128 a[148:151], v13 offset:28672\n"
"v_mfma_f32_16x16x32_bf16 v[252:255], a[88:91], a[144:147], v[252:255]\n"
"ds_read_b128 a[144:147], v12 offset:28672\n"
"ds_read_b64_tr_b16 a[62:63], v15 offset:12808\n"
"v_mfma_f32_16x16x32_bf16 a[28:31], a[88:91], a[152:155], a[28:31]\n"
"v_mfma_f32_16x16x32_bf16 a[28:31], a[92:95], a[156:159], a[28:31]\n"
"v_mfma_f32_16x16x32_bf16 a[24:27], a[84:87], a[156:159], a[24:27]\n"
"ds_read_b128 a[156:159], v13 offset:30720\n"
"v_mfma_f32_16x16x32_bf16 a[24:27], a[80:83], a[152:155], a[24:27]\n"
"s_waitcnt lgkmcnt(15)\n"
"ds_read_b128 a[152:155], v12 offset:30720\n"
"v_mfma_f32_16x16x32_bf16 v[32:35], a[32:35], a[96:99], v[32:35]\n"
"v_mfma_f32_16x16x32_bf16 v[32:35], a[36:39], a[100:103], v[32:35]\n"
"s_waitcnt lgkmcnt(2)\n"
"v_mfma_f32_16x16x32_bf16 v[60:63], a[44:47], a[100:103], v[60:63]\n"
"buffer_load_dwordx4 v11, s[52:55], s67 offen offset:0  lds\n"
"v_mfma_f32_16x16x32_bf16 v[60:63], a[40:43], a[96:99], v[60:63]\n"
"v_mfma_f32_16x16x32_bf16 v[64:67], a[40:43], a[104:107], v[64:67]\n"
"s_add_u32 s67, s75, 408576\n"
"v_mfma_f32_16x16x32_bf16 v[64:67], a[44:47], a[108:111], v[64:67]\n"
"v_mfma_f32_16x16x32_bf16 v[36:39], a[36:39], a[108:111], v[36:39]\n"
"v_mfma_f32_16x16x32_bf16 v[36:39], a[32:35], a[104:107], v[36:39]\n"
"buffer_load_dwordx4 v11, s[52:55], s67 offen offset:1024  lds\n"
"v_mfma_f32_16x16x32_bf16 v[40:43], a[32:35], a[112:115], v[40:43]\n"
"v_mfma_f32_16x16x32_bf16 v[40:43], a[36:39], a[116:119], v[40:43]\n"
"s_add_u32 s67, s75, 423936\n"
"v_mfma_f32_16x16x32_bf16 v[68:71], a[44:47], a[116:119], v[68:71]\n"
"v_mfma_f32_16x16x32_bf16 v[68:71], a[40:43], a[112:115], v[68:71]\n"
"v_mfma_f32_16x16x32_bf16 v[72:75], a[40:43], a[120:123], v[72:75]\n"
"buffer_load_dwordx4 v11, s[52:55], s67 offen offset:2048  lds\n"
"v_mfma_f32_16x16x32_bf16 v[72:75], a[44:47], a[124:127], v[72:75]\n"
"v_mfma_f32_16x16x32_bf16 v[44:47], a[36:39], a[124:127], v[44:47]\n"
"s_add_u32 s67, s75, 439296\n"
"v_mfma_f32_16x16x32_bf16 v[44:47], a[32:35], a[120:123], v[44:47]\n"
"v_mfma_f32_16x16x32_bf16 v[48:51], a[32:35], a[128:131], v[48:51]\n"
"buffer_load_dwordx4 v11, s[52:55], s67 offen offset:3072  lds\n"
"v_mfma_f32_16x16x32_bf16 v[48:51], a[36:39], a[132:135], v[48:51]\n"
"s_add_u32 m0, s64, 12288\n"
"v_mfma_f32_16x16x32_bf16 v[76:79], a[44:47], a[132:135], v[76:79]\n"
"s_waitcnt vmcnt(10)\n"
"ds_read_b64_tr_b16 a[64:65], v9 offset:0\n"
"v_mfma_f32_16x16x32_bf16 v[76:79], a[40:43], a[128:131], v[76:79]\n"
"ds_read_b64_tr_b16 a[66:67], v9 offset:512\n"
"v_mfma_f32_16x16x32_bf16 v[80:83], a[40:43], a[136:139], v[80:83]\n"
"s_add_u32 s67, s75, 458752\n"
"ds_read_b64_tr_b16 a[68:69], v9 offset:4096\n"
"v_mfma_f32_16x16x32_bf16 v[80:83], a[44:47], a[140:143], v[80:83]\n"
"ds_read_b64_tr_b16 a[70:71], v9 offset:4608\n"
"v_mfma_f32_16x16x32_bf16 v[52:55], a[36:39], a[140:143], v[52:55]\n"
"ds_read_b64_tr_b16 a[72:73], v9 offset:8\n"
"buffer_load_dwordx4 v11, s[52:55], s67 offen offset:0  lds\n"
"v_mfma_f32_16x16x32_bf16 v[52:55], a[32:35], a[136:139], v[52:55]\n"
"s_waitcnt lgkmcnt(5)\n"
"ds_read_b64_tr_b16 a[74:75], v9 offset:520\n"
"v_mfma_f32_16x16x32_bf16 v[56:59], a[32:35], a[144:147], v[56:59]\n"
"s_add_u32 s67, s75, 474112\n"
"ds_read_b64_tr_b16 a[76:77], v9 offset:4104\n"
"v_mfma_f32_16x16x32_bf16 v[56:59], a[36:39], a[148:151], v[56:59]\n"
"ds_read_b64_tr_b16 a[78:79], v9 offset:4616\n"
"v_mfma_f32_16x16x32_bf16 v[84:87], a[44:47], a[148:151], v[84:87]\n"
"buffer_load_dwordx4 v11, s[52:55], s67 offen offset:1024  lds\n"
"v_mfma_f32_16x16x32_bf16 v[84:87], a[40:43], a[144:147], v[84:87]\n"
"ds_read_b64_tr_b16 a[80:81], v15 offset:0\n"
"v_mfma_f32_16x16x32_bf16 a[4:7], a[40:43], a[152:155], a[4:7]\n"
"s_add_u32 s67, s75, 489472\n"
"ds_read_b64_tr_b16 a[82:83], v15 offset:512\n"
"v_mfma_f32_16x16x32_bf16 a[4:7], a[44:47], a[156:159], a[4:7]\n"
"ds_read_b64_tr_b16 a[84:85], v15 offset:4096\n"
"v_mfma_f32_16x16x32_bf16 a[0:3], a[36:39], a[156:159], a[0:3]\n"
"ds_read_b64_tr_b16 a[86:87], v15 offset:4608\n"
"buffer_load_dwordx4 v11, s[52:55], s67 offen offset:2048  lds\n"
"v_mfma_f32_16x16x32_bf16 a[0:3], a[32:35], a[152:155], a[0:3]\n"
"ds_read_b64_tr_b16 a[88:89], v15 offset:8\n"
"v_mfma_f32_16x16x32_bf16 v[88:91], a[48:51], a[96:99], v[88:91]\n"
"s_add_u32 s67, s75, 504832\n"
"ds_read_b64_tr_b16 a[90:91], v15 offset:520\n"
"v_mfma_f32_16x16x32_bf16 v[88:91], a[52:55], a[100:103], v[88:91]\n"
"ds_read_b64_tr_b16 a[92:93], v15 offset:4104\n"
"v_mfma_f32_16x16x32_bf16 v[116:119], a[60:63], a[100:103], v[116:119]\n"
"ds_read_b64_tr_b16 a[94:95], v15 offset:4616\n"
"v_mfma_f32_16x16x32_bf16 v[116:119], a[56:59], a[96:99], v[116:119]\n"
"buffer_load_dwordx4 v11, s[52:55], s67 offen offset:3072  lds\n"
"v_mfma_f32_16x16x32_bf16 v[120:123], a[56:59], a[104:107], v[120:123]\n"
"v_mfma_f32_16x16x32_bf16 v[120:123], a[60:63], a[108:111], v[120:123]\n"
"s_add_u32 m0, s64, 0\n"
"v_mfma_f32_16x16x32_bf16 v[92:95], a[52:55], a[108:111], v[92:95]\n"
"v_mfma_f32_16x16x32_bf16 v[92:95], a[48:51], a[104:107], v[92:95]\n"
"s_add_u32 s67, s75, 393344\n"
"v_mfma_f32_16x16x32_bf16 v[96:99], a[48:51], a[112:115], v[96:99]\n"
"s_waitcnt lgkmcnt(0)\n"
"v_mfma_f32_16x16x32_bf16 v[96:99], a[52:55], a[116:119], v[96:99]\n"
"buffer_load_dwordx4 v11, s[52:55], s67 offen offset:0  lds\n"
"v_mfma_f32_16x16x32_bf16 v[124:127], a[60:63], a[116:119], v[124:127]\n"
"v_mfma_f32_16x16x32_bf16 v[124:127], a[56:59], a[112:115], v[124:127]\n"
"s_add_u32 s67, s75, 408704\n"
"v_mfma_f32_16x16x32_bf16 v[128:131], a[56:59], a[120:123], v[128:131]\n"
"v_mfma_f32_16x16x32_bf16 v[128:131], a[60:63], a[124:127], v[128:131]\n"
"v_mfma_f32_16x16x32_bf16 v[100:103], a[52:55], a[124:127], v[100:103]\n"
"buffer_load_dwordx4 v11, s[52:55], s67 offen offset:1024  lds\n"
"v_mfma_f32_16x16x32_bf16 v[100:103], a[48:51], a[120:123], v[100:103]\n"
"v_mfma_f32_16x16x32_bf16 v[104:107], a[48:51], a[128:131], v[104:107]\n"
"s_add_u32 s67, s75, 424064\n"
"v_mfma_f32_16x16x32_bf16 v[104:107], a[52:55], a[132:135], v[104:107]\n"
"v_mfma_f32_16x16x32_bf16 v[132:135], a[60:63], a[132:135], v[132:135]\n"
"v_mfma_f32_16x16x32_bf16 v[132:135], a[56:59], a[128:131], v[132:135]\n"
"buffer_load_dwordx4 v11, s[52:55], s67 offen offset:2048  lds\n"
"v_mfma_f32_16x16x32_bf16 v[136:139], a[56:59], a[136:139], v[136:139]\n"
"v_mfma_f32_16x16x32_bf16 v[136:139], a[60:63], a[140:143], v[136:139]\n"
"s_add_u32 s67, s75, 439424\n"
"v_mfma_f32_16x16x32_bf16 v[108:111], a[52:55], a[140:143], v[108:111]\n"
"v_mfma_f32_16x16x32_bf16 v[108:111], a[48:51], a[136:139], v[108:111]\n"
"buffer_load_dwordx4 v11, s[52:55], s67 offen offset:3072  lds\n"
"v_mfma_f32_16x16x32_bf16 v[112:115], a[48:51], a[144:147], v[112:115]\n"
"v_mfma_f32_16x16x32_bf16 v[112:115], a[52:55], a[148:151], v[112:115]\n"
"s_add_u32 m0, s64, 4096\n"
"v_mfma_f32_16x16x32_bf16 v[140:143], a[60:63], a[148:151], v[140:143]\n"
"v_mfma_f32_16x16x32_bf16 v[140:143], a[56:59], a[144:147], v[140:143]\n"
"s_add_u32 s67, s75, 458880\n"
"v_mfma_f32_16x16x32_bf16 a[12:15], a[56:59], a[152:155], a[12:15]\n"
"v_mfma_f32_16x16x32_bf16 a[12:15], a[60:63], a[156:159], a[12:15]\n"
"buffer_load_dwordx4 v11, s[52:55], s67 offen offset:0  lds\n"
"v_mfma_f32_16x16x32_bf16 a[8:11], a[52:55], a[156:159], a[8:11]\n"
"v_mfma_f32_16x16x32_bf16 a[8:11], a[48:51], a[152:155], a[8:11]\n"
"s_add_u32 s67, s75, 474240\n"
"v_mfma_f32_16x16x32_bf16 v[144:147], a[64:67], a[96:99], v[144:147]\n"
"v_mfma_f32_16x16x32_bf16 v[144:147], a[68:71], a[100:103], v[144:147]\n"
"buffer_load_dwordx4 v11, s[52:55], s67 offen offset:1024  lds\n"
"v_mfma_f32_16x16x32_bf16 v[172:175], a[76:79], a[100:103], v[172:175]\n"
"v_mfma_f32_16x16x32_bf16 v[172:175], a[72:75], a[96:99], v[172:175]\n"
"s_add_u32 s67, s75, 489600\n"
"v_mfma_f32_16x16x32_bf16 v[176:179], a[72:75], a[104:107], v[176:179]\n"
"v_mfma_f32_16x16x32_bf16 v[176:179], a[76:79], a[108:111], v[176:179]\n"
"buffer_load_dwordx4 v11, s[52:55], s67 offen offset:2048  lds\n"
"v_mfma_f32_16x16x32_bf16 v[148:151], a[68:71], a[108:111], v[148:151]\n"
"v_mfma_f32_16x16x32_bf16 v[148:151], a[64:67], a[104:107], v[148:151]\n"
"s_add_u32 s67, s75, 504960\n"
"v_mfma_f32_16x16x32_bf16 v[152:155], a[64:67], a[112:115], v[152:155]\n"
"s_sub_u32 s66, 122880, s73\n"
"v_mfma_f32_16x16x32_bf16 v[152:155], a[68:71], a[116:119], v[152:155]\n"
"v_mfma_f32_16x16x32_bf16 v[180:183], a[76:79], a[116:119], v[180:183]\n"
"v_add_u32 v12, s66, v7\n"
"v_mfma_f32_16x16x32_bf16 v[180:183], a[72:75], a[112:115], v[180:183]\n"
"buffer_load_dwordx4 v11, s[52:55], s67 offen offset:3072  lds\n"
"v_mfma_f32_16x16x32_bf16 v[184:187], a[72:75], a[120:123], v[184:187]\n"
"s_add_u32 m0, s72, 16384\n"
"v_add_u32 v13, s66, v8\n"
"v_mfma_f32_16x16x32_bf16 v[184:187], a[76:79], a[124:127], v[184:187]\n"
"v_mfma_f32_16x16x32_bf16 v[156:159], a[68:71], a[124:127], v[156:159]\n"
"s_add_u32 s67, s60, 512\n"
"v_mfma_f32_16x16x32_bf16 v[156:159], a[64:67], a[120:123], v[156:159]\n"
"buffer_load_dwordx4 v6, s[48:51], s67 offen offset:0  lds\n"
"v_mfma_f32_16x16x32_bf16 v[160:163], a[64:67], a[128:131], v[160:163]\n"
"v_mfma_f32_16x16x32_bf16 v[160:163], a[68:71], a[132:135], v[160:163]\n"
"s_add_u32 s67, s60, 15872\n"
"v_mfma_f32_16x16x32_bf16 v[188:191], a[76:79], a[132:135], v[188:191]\n"
"v_mfma_f32_16x16x32_bf16 v[188:191], a[72:75], a[128:131], v[188:191]\n"
"buffer_load_dwordx4 v6, s[48:51], s67 offen offset:1024  lds\n"
"v_mfma_f32_16x16x32_bf16 v[192:195], a[72:75], a[136:139], v[192:195]\n"
"v_mfma_f32_16x16x32_bf16 v[192:195], a[76:79], a[140:143], v[192:195]\n"
"s_add_u32 s67, s60, 31232\n"
"v_mfma_f32_16x16x32_bf16 v[164:167], a[68:71], a[140:143], v[164:167]\n"
"v_mfma_f32_16x16x32_bf16 v[164:167], a[64:67], a[136:139], v[164:167]\n"
"buffer_load_dwordx4 v6, s[48:51], s67 offen offset:2048  lds\n"
"v_mfma_f32_16x16x32_bf16 v[168:171], a[64:67], a[144:147], v[168:171]\n"
"v_mfma_f32_16x16x32_bf16 v[168:171], a[68:71], a[148:151], v[168:171]\n"
"s_add_u32 s67, s60, 46592\n"
"v_mfma_f32_16x16x32_bf16 v[196:199], a[76:79], a[148:151], v[196:199]\n"
"v_mfma_f32_16x16x32_bf16 v[196:199], a[72:75], a[144:147], v[196:199]\n"
"v_mfma_f32_16x16x32_bf16 a[20:23], a[72:75], a[152:155], a[20:23]\n"
"buffer_load_dwordx4 v6, s[48:51], s67 offen offset:3072  lds\n"
"v_mfma_f32_16x16x32_bf16 a[20:23], a[76:79], a[156:159], a[20:23]\n"
"v_mfma_f32_16x16x32_bf16 a[16:19], a[68:71], a[156:159], a[16:19]\n"
"s_add_u32 s70, s75, 786432\n"
"s_add_u32 s70, s70, s65\n"
"v_mfma_f32_16x16x32_bf16 a[16:19], a[64:67], a[152:155], a[16:19]\n"
"s_waitcnt vmcnt(22)\n"
"s_barrier\n"
"v_mfma_f32_16x16x32_bf16 v[200:203], a[80:83], a[96:99], v[200:203]\n"
"s_waitcnt vmcnt(12)\n"
"ds_read_b64_tr_b16 a[32:33], v9 offset:8192\n"
"v_mfma_f32_16x16x32_bf16 v[200:203], a[84:87], a[100:103], v[200:203]\n"
"ds_read_b64_tr_b16 a[34:35], v9 offset:8704\n"
"v_mfma_f32_16x16x32_bf16 v[228:231], a[92:95], a[100:103], v[228:231]\n"
"ds_read_b128 a[100:103], v13 offset:16384\n"
"buffer_load_dword a252, v14, s[52:55], s70 offen\n"
"v_mfma_f32_16x16x32_bf16 v[228:231], a[88:91], a[96:99], v[228:231]\n"
"ds_read_b128 a[96:99], v12 offset:16384\n"
"v_mfma_f32_16x16x32_bf16 v[232:235], a[88:91], a[104:107], v[232:235]\n"
"ds_read_b64_tr_b16 a[36:37], v9 offset:12288\n"
"v_mfma_f32_16x16x32_bf16 v[232:235], a[92:95], a[108:111], v[232:235]\n"
"ds_read_b64_tr_b16 a[38:39], v9 offset:12800\n"
"v_mfma_f32_16x16x32_bf16 v[204:207], a[84:87], a[108:111], v[204:207]\n"
"ds_read_b128 a[108:111], v13 offset:18432\n"
"v_mfma_f32_16x16x32_bf16 v[204:207], a[80:83], a[104:107], v[204:207]\n"
"ds_read_b128 a[104:107], v12 offset:18432\n"
"ds_read_b64_tr_b16 a[40:41], v9 offset:8200\n"
"v_mfma_f32_16x16x32_bf16 v[208:211], a[80:83], a[112:115], v[208:211]\n"
"s_add_u32 m0, s64, 8192\n"
"ds_read_b64_tr_b16 a[42:43], v9 offset:8712\n"
"v_mfma_f32_16x16x32_bf16 v[208:211], a[84:87], a[116:119], v[208:211]\n"
"ds_read_b64_tr_b16 a[44:45], v9 offset:12296\n"
"v_mfma_f32_16x16x32_bf16 v[236:239], a[92:95], a[116:119], v[236:239]\n"
"L_st1045:\n"
"s_cbranch_execz L_st3225\n"
"ds_read_b128 a[116:119], v13 offset:20480\n"
"v_mfma_f32_16x16x32_bf16 v[236:239], a[88:91], a[112:115], v[236:239]\n"
"ds_read_b128 a[112:115], v12 offset:20480\n"
"v_mfma_f32_16x16x32_bf16 v[240:243], a[88:91], a[120:123], v[240:243]\n"
"ds_read_b64_tr_b16 a[46:47], v9 offset:12808\n"
"v_mfma_f32_16x16x32_bf16 v[240:243], a[92:95], a[124:127], v[240:243]\n"
"ds_read_b64_tr_b16 a[48:49], v15 offset:8192\n"
"v_mfma_f32_16x16x32_bf16 v[212:215], a[84:87], a[124:127], v[212:215]\n"
"ds_read_b128 a[124:127], v13 offset:22528\n"
"v_mfma_f32_16x16x32_bf16 v[212:215], a[80:83], a[120:123], v[212:215]\n"
"ds_read_b128 a[120:123], v12 offset:22528\n"
"v_mfma_f32_16x16x32_bf16 v[216:219], a[80:83], a[128:131], v[216:219]\n"
"ds_read_b64_tr_b16 a[50:51], v15 offset:8704\n"
"v_mfma_f32_16x16x32_bf16 v[216:219], a[84:87], a[132:135], v[216:219]\n"
"ds_read_b64_tr_b16 a[52:53], v15 offset:12288\n"
"v_mfma_f32_16x16x32_bf16 v[244:247], a[92:95], a[132:135], v[244:247]\n"
"ds_read_b128 a[132:135], v13 offset:24576\n"
"v_mfma_f32_16x16x32_bf16 v[244:247], a[88:91], a[128:131], v[244:247]\n"
"s_add_u32 s67, s75, 524288\n"
"ds_read_b128 a[128:131], v12 offset:24576\n"
"v_mfma_f32_16x16x32_bf16 v[248:251], a[88:91], a[136:139], v[248:251]\n"
"ds_read_b64_tr_b16 a[54:55], v15 offset:12800\n"
"v_mfma_f32_16x16x32_bf16 v[248:251], a[92:95], a[140:143], v[248:251]\n"
"ds_read_b64_tr_b16 a[56:57], v15 offset:8200\n"
"v_mfma_f32_16x16x32_bf16 v[220:223], a[84:87], a[140:143], v[220:223]\n"
"ds_read_b128 a[140:143], v13 offset:26624\n"
"v_mfma_f32_16x16x32_bf16 v[220:223], a[80:83], a[136:139], v[220:223]\n"
"ds_read_b128 a[136:139], v12 offset:26624\n"
"v_mfma_f32_16x16x32_bf16 v[224:227], a[80:83], a[144:147], v[224:227]\n"
"ds_read_b64_tr_b16 a[58:59], v15 offset:8712\n"
"v_mfma_f32_16x16x32_bf16 v[224:227], a[84:87], a[148:151], v[224:227]\n"
"ds_read_b64_tr_b16 a[60:61], v15 offset:12296\n"
"v_mfma_f32_16x16x32_bf16 v[252:255], a[92:95], a[148:151], v[252:255]\n"
"ds_read_b128 a[148:151], v13 offset:28672\n"
"v_mfma_f32_16x16x32_bf16 v[252:255], a[88:91], a[144:147], v[252:255]\n"
"ds_read_b128 a[144:147], v12 offset:28672\n"
"ds_read_b64_tr_b16 a[62:63], v15 offset:12808\n"
"v_mfma_f32_16x16x32_bf16 a[28:31], a[88:91], a[152:155], a[28:31]\n"
"v_mfma_f32_16x16x32_bf16 a[28:31], a[92:95], a[156:159], a[28:31]\n"
"v_mfma_f32_16x16x32_bf16 a[24:27], a[84:87], a[156:159], a[24:27]\n"
"ds_read_b128 a[156:159], v13 offset:30720\n"
"v_mfma_f32_16x16x32_bf16 a[24:27], a[80:83], a[152:155], a[24:27]\n"
"s_waitcnt lgkmcnt(15)\n"
"ds_read_b128 a[152:155], v12 offset:30720\n"
"v_mfma_f32_16x16x32_bf16 v[32:35], a[32:35], a[96:99], v[32:35]\n"
"v_mfma_f32_16x16x32_bf16 v[32:35], a[36:39], a[100:103], v[32:35]\n"
"s_waitcnt lgkmcnt(2)\n"
"v_mfma_f32_16x16x32_bf16 v[60:63], a[44:47], a[100:103], v[60:63]\n"
"buffer_load_dwordx4 v11, s[52:55], s67 offen offset:0  lds\n"
"v_mfma_f32_16x16x32_bf16 v[60:63], a[40:43], a[96:99], v[60:63]\n"
"v_mfma_f32_16x16x32_bf16 v[64:67], a[40:43], a[104:107], v[64:67]\n"
"s_add_u32 s67, s75, 539648\n"
"v_mfma_f32_16x16x32_bf16 v[64:67], a[44:47], a[108:111], v[64:67]\n"
"v_mfma_f32_16x16x32_bf16 v[36:39], a[36:39], a[108:111], v[36:39]\n"
"buffer_load_dwordx4 v11, s[52:55], s67 offen offset:1024  lds\n"
"v_mfma_f32_16x16x32_bf16 v[36:39], a[32:35], a[104:107], v[36:39]\n"
"v_mfma_f32_16x16x32_bf16 v[40:43], a[32:35], a[112:115], v[40:43]\n"
"s_add_u32 s67, s75, 555008\n"
"v_mfma_f32_16x16x32_bf16 v[40:43], a[36:39], a[116:119], v[40:43]\n"
"v_mfma_f32_16x16x32_bf16 v[68:71], a[44:47], a[116:119], v[68:71]\n"
"v_mfma_f32_16x16x32_bf16 v[68:71], a[40:43], a[112:115], v[68:71]\n"
"buffer_load_dwordx4 v11, s[52:55], s67 offen offset:2048  lds\n"
"v_mfma_f32_16x16x32_bf16 v[72:75], a[40:43], a[120:123], v[72:75]\n"
"v_mfma_f32_16x16x32_bf16 v[72:75], a[44:47], a[124:127], v[72:75]\n"
"s_add_u32 s67, s75, 570368\n"
"v_mfma_f32_16x16x32_bf16 v[44:47], a[36:39], a[124:127], v[44:47]\n"
"v_mfma_f32_16x16x32_bf16 v[44:47], a[32:35], a[120:123], v[44:47]\n"
"buffer_load_dwordx4 v11, s[52:55], s67 offen offset:3072  lds\n"
"v_mfma_f32_16x16x32_bf16 v[48:51], a[32:35], a[128:131], v[48:51]\n"
"v_mfma_f32_16x16x32_bf16 v[48:51], a[36:39], a[132:135], v[48:51]\n"
"s_add_u32 m0, s64, 12288\n"
"v_mfma_f32_16x16x32_bf16 v[76:79], a[44:47], a[132:135], v[76:79]\n"
"s_waitcnt vmcnt(9)\n"
"ds_read_b64_tr_b16 a[64:65], v9 offset:0\n"
"v_mfma_f32_16x16x32_bf16 v[76:79], a[40:43], a[128:131], v[76:79]\n"
"s_add_u32 s67, s75, 589824\n"
"ds_read_b64_tr_b16 a[66:67], v9 offset:512\n"
"v_mfma_f32_16x16x32_bf16 v[80:83], a[40:43], a[136:139], v[80:83]\n"
"ds_read_b64_tr_b16 a[68:69], v9 offset:4096\n"
"v_mfma_f32_16x16x32_bf16 v[80:83], a[44:47], a[140:143], v[80:83]\n"
"ds_read_b64_tr_b16 a[70:71], v9 offset:4608\n"
"buffer_load_dwordx4 v11, s[52:55], s67 offen offset:0  lds\n"
"v_mfma_f32_16x16x32_bf16 v[52:55], a[36:39], a[140:143], v[52:55]\n"
"s_add_u32 s67, s75, 605184\n"
"ds_read_b64_tr_b16 a[72:73], v9 offset:8\n"
"v_mfma_f32_16x16x32_bf16 v[52:55], a[32:35], a[136:139], v[52:55]\n"
"s_waitcnt lgkmcnt(5)\n"
"ds_read_b64_tr_b16 a[74:75], v9 offset:520\n"
"v_mfma_f32_16x16x32_bf16 v[56:59], a[32:35], a[144:147], v[56:59]\n"
"ds_read_b64_tr_b16 a[76:77], v9 offset:4104\n"
"v_mfma_f32_16x16x32_bf16 v[56:59], a[36:39], a[148:151], v[56:59]\n"
"ds_read_b64_tr_b16 a[78:79], v9 offset:4616\n"
"v_mfma_f32_16x16x32_bf16 v[84:87], a[44:47], a[148:151], v[84:87]\n"
"buffer_load_dwordx4 v11, s[52:55], s67 offen offset:1024  lds\n"
"v_mfma_f32_16x16x32_bf16 v[84:87], a[40:43], a[144:147], v[84:87]\n"
"ds_read_b64_tr_b16 a[80:81], v15 offset:0\n"
"v_mfma_f32_16x16x32_bf16 a[4:7], a[40:43], a[152:155], a[4:7]\n"
"s_add_u32 s67, s75, 620544\n"
"ds_read_b64_tr_b16 a[82:83], v15 offset:512\n"
"v_mfma_f32_16x16x32_bf16 a[4:7], a[44:47], a[156:159], a[4:7]\n"
"ds_read_b64_tr_b16 a[84:85], v15 offset:4096\n"
"v_mfma_f32_16x16x32_bf16 a[0:3], a[36:39], a[156:159], a[0:3]\n"
"ds_read_b64_tr_b16 a[86:87], v15 offset:4608\n"
"buffer_load_dwordx4 v11, s[52:55], s67 offen offset:2048  lds\n"
"v_mfma_f32_16x16x32_bf16 a[0:3], a[32:35], a[152:155], a[0:3]\n"
"ds_read_b64_tr_b16 a[88:89], v15 offset:8\n"
"v_mfma_f32_16x16x32_bf16 v[88:91], a[48:51], a[96:99], v[88:91]\n"
"s_add_u32 s67, s75, 635904\n"
"ds_read_b64_tr_b16 a[90:91], v15 offset:520\n"
"v_mfma_f32_16x16x32_bf16 v[88:91], a[52:55], a[100:103], v[88:91]\n"
"ds_read_b64_tr_b16 a[92:93], v15 offset:4104\n"
"v_mfma_f32_16x16x32_bf16 v[116:119], a[60:63], a[100:103], v[116:119]\n"
"ds_read_b64_tr_b16 a[94:95], v15 offset:4616\n"
"v_mfma_f32_16x16x32_bf16 v[116:119], a[56:59], a[96:99], v[116:119]\n"
"buffer_load_dwordx4 v11, s[52:55], s67 offen offset:3072  lds\n"
"v_mfma_f32_16x16x32_bf16 v[120:123], a[56:59], a[104:107], v[120:123]\n"
"v_mfma_f32_16x16x32_bf16 v[120:123], a[60:63], a[108:111], v[120:123]\n"
"s_add_u32 m0, s64, 0\n"
"v_mfma_f32_16x16x32_bf16 v[92:95], a[52:55], a[108:111], v[92:95]\n"
"v_mfma_f32_16x16x32_bf16 v[92:95], a[48:51], a[104:107], v[92:95]\n"
"s_add_u32 s67, s75, 524416\n"
"v_mfma_f32_16x16x32_bf16 v[96:99], a[48:51], a[112:115], v[96:99]\n"
"s_waitcnt lgkmcnt(0)\n"
"v_mfma_f32_16x16x32_bf16 v[96:99], a[52:55], a[116:119], v[96:99]\n"
"buffer_load_dwordx4 v11, s[52:55], s67 offen offset:0  lds\n"
"v_mfma_f32_16x16x32_bf16 v[124:127], a[60:63], a[116:119], v[124:127]\n"
"v_mfma_f32_16x16x32_bf16 v[124:127], a[56:59], a[112:115], v[124:127]\n"
"s_add_u32 s67, s75, 539776\n"
"v_mfma_f32_16x16x32_bf16 v[128:131], a[56:59], a[120:123], v[128:131]\n"
"v_mfma_f32_16x16x32_bf16 v[128:131], a[60:63], a[124:127], v[128:131]\n"
"v_mfma_f32_16x16x32_bf16 v[100:103], a[52:55], a[124:127], v[100:103]\n"
"buffer_load_dwordx4 v11, s[52:55], s67 offen offset:1024  lds\n"
"v_mfma_f32_16x16x32_bf16 v[100:103], a[48:51], a[120:123], v[100:103]\n"
"v_mfma_f32_16x16x32_bf16 v[104:107], a[48:51], a[128:131], v[104:107]\n"
"s_add_u32 s67, s75, 555136\n"
"v_mfma_f32_16x16x32_bf16 v[104:107], a[52:55], a[132:135], v[104:107]\n"
"v_mfma_f32_16x16x32_bf16 v[132:135], a[60:63], a[132:135], v[132:135]\n"
"v_mfma_f32_16x16x32_bf16 v[132:135], a[56:59], a[128:131], v[132:135]\n"
"buffer_load_dwordx4 v11, s[52:55], s67 offen offset:2048  lds\n"
"v_mfma_f32_16x16x32_bf16 v[136:139], a[56:59], a[136:139], v[136:139]\n"
"v_mfma_f32_16x16x32_bf16 v[136:139], a[60:63], a[140:143], v[136:139]\n"
"s_add_u32 s67, s75, 570496\n"
"v_mfma_f32_16x16x32_bf16 v[108:111], a[52:55], a[140:143], v[108:111]\n"
"v_mfma_f32_16x16x32_bf16 v[108:111], a[48:51], a[136:139], v[108:111]\n"
"buffer_load_dwordx4 v11, s[52:55], s67 offen offset:3072  lds\n"
"v_mfma_f32_16x16x32_bf16 v[112:115], a[48:51], a[144:147], v[112:115]\n"
"v_mfma_f32_16x16x32_bf16 v[112:115], a[52:55], a[148:151], v[112:115]\n"
"s_add_u32 m0, s64, 4096\n"
"v_mfma_f32_16x16x32_bf16 v[140:143], a[60:63], a[148:151], v[140:143]\n"
"v_mfma_f32_16x16x32_bf16 v[140:143], a[56:59], a[144:147], v[140:143]\n"
"s_add_u32 s67, s75, 589952\n"
"v_mfma_f32_16x16x32_bf16 a[12:15], a[56:59], a[152:155], a[12:15]\n"
"v_mfma_f32_16x16x32_bf16 a[12:15], a[60:63], a[156:159], a[12:15]\n"
"buffer_load_dwordx4 v11, s[52:55], s67 offen offset:0  lds\n"
"v_mfma_f32_16x16x32_bf16 a[8:11], a[52:55], a[156:159], a[8:11]\n"
"v_mfma_f32_16x16x32_bf16 a[8:11], a[48:51], a[152:155], a[8:11]\n"
"s_add_u32 s67, s75, 605312\n"
"v_mfma_f32_16x16x32_bf16 v[144:147], a[64:67], a[96:99], v[144:147]\n"
"v_mfma_f32_16x16x32_bf16 v[144:147], a[68:71], a[100:103], v[144:147]\n"
"buffer_load_dwordx4 v11, s[52:55], s67 offen offset:1024  lds\n"
"v_mfma_f32_16x16x32_bf16 v[172:175], a[76:79], a[100:103], v[172:175]\n"
"v_mfma_f32_16x16x32_bf16 v[172:175], a[72:75], a[96:99], v[172:175]\n"
"s_add_u32 s67, s75, 620672\n"
"v_mfma_f32_16x16x32_bf16 v[176:179], a[72:75], a[104:107], v[176:179]\n"
"v_mfma_f32_16x16x32_bf16 v[176:179], a[76:79], a[108:111], v[176:179]\n"
"buffer_load_dwordx4 v11, s[52:55], s67 offen offset:2048  lds\n"
"v_mfma_f32_16x16x32_bf16 v[148:151], a[68:71], a[108:111], v[148:151]\n"
"v_mfma_f32_16x16x32_bf16 v[148:151], a[64:67], a[104:107], v[148:151]\n"
"s_add_u32 s67, s75, 636032\n"
"v_mfma_f32_16x16x32_bf16 v[152:155], a[64:67], a[112:115], v[152:155]\n"
"s_sub_u32 s66, 0, s73\n"
"v_mfma_f32_16x16x32_bf16 v[152:155], a[68:71], a[116:119], v[152:155]\n"
"v_mfma_f32_16x16x32_bf16 v[180:183], a[76:79], a[116:119], v[180:183]\n"
"v_add_u32 v12, s66, v7\n"
"v_mfma_f32_16x16x32_bf16 v[180:183], a[72:75], a[112:115], v[180:183]\n"
"buffer_load_dwordx4 v11, s[52:55], s67 offen offset:3072  lds\n"
"v_mfma_f32_16x16x32_bf16 v[184:187], a[72:75], a[120:123], v[184:187]\n"
"s_add_u32 m0, s72, 57344\n"
"v_add_u32 v13, s66, v8\n"
"v_mfma_f32_16x16x32_bf16 v[184:187], a[76:79], a[124:127], v[184:187]\n"
"v_mfma_f32_16x16x32_bf16 v[156:159], a[68:71], a[124:127], v[156:159]\n"
"s_add_u32 s67, s60, 640\n"
"v_mfma_f32_16x16x32_bf16 v[156:159], a[64:67], a[120:123], v[156:159]\n"
"buffer_load_dwordx4 v6, s[48:51], s67 offen offset:0  lds\n"
"v_mfma_f32_16x16x32_bf16 v[160:163], a[64:67], a[128:131], v[160:163]\n"
"v_mfma_f32_16x16x32_bf16 v[160:163], a[68:71], a[132:135], v[160:163]\n"
"s_add_u32 s67, s60, 16000\n"
"v_mfma_f32_16x16x32_bf16 v[188:191], a[76:79], a[132:135], v[188:191]\n"
"v_mfma_f32_16x16x32_bf16 v[188:191], a[72:75], a[128:131], v[188:191]\n"
"buffer_load_dwordx4 v6, s[48:51], s67 offen offset:1024  lds\n"
"v_mfma_f32_16x16x32_bf16 v[192:195], a[72:75], a[136:139], v[192:195]\n"
"v_mfma_f32_16x16x32_bf16 v[192:195], a[76:79], a[140:143], v[192:195]\n"
"s_add_u32 s67, s60, 31360\n"
"v_mfma_f32_16x16x32_bf16 v[164:167], a[68:71], a[140:143], v[164:167]\n"
"v_mfma_f32_16x16x32_bf16 v[164:167], a[64:67], a[136:139], v[164:167]\n"
"buffer_load_dwordx4 v6, s[48:51], s67 offen offset:2048  lds\n"
"v_mfma_f32_16x16x32_bf16 v[168:171], a[64:67], a[144:147], v[168:171]\n"
"v_mfma_f32_16x16x32_bf16 v[168:171], a[68:71], a[148:151], v[168:171]\n"
"s_add_u32 s67, s60, 46720\n"
"v_mfma_f32_16x16x32_bf16 v[196:199], a[76:79], a[148:151], v[196:199]\n"
"v_mfma_f32_16x16x32_bf16 v[196:199], a[72:75], a[144:147], v[196:199]\n"
"v_mfma_f32_16x16x32_bf16 a[20:23], a[72:75], a[152:155], a[20:23]\n"
"buffer_load_dwordx4 v6, s[48:51], s67 offen offset:3072  lds\n"
"v_mfma_f32_16x16x32_bf16 a[20:23], a[76:79], a[156:159], a[20:23]\n"
"s_sub_u32 s70, 1048576, s75\n"
"v_and_b32 v19, 0x20000, v14\n"
"v_mfma_f32_16x16x32_bf16 a[16:19], a[68:71], a[156:159], a[16:19]\n"
"v_add_u32 v14, v19, v14\n"
"v_mfma_f32_16x16x32_bf16 a[16:19], a[64:67], a[152:155], a[16:19]\n"
"s_waitcnt vmcnt(21)\n"
"s_barrier\n"
"v_mfma_f32_16x16x32_bf16 v[200:203], a[80:83], a[96:99], v[200:203]\n"
"s_waitcnt vmcnt(12)\n"
"ds_read_b64_tr_b16 a[32:33], v9 offset:8192\n"
"v_mfma_f32_16x16x32_bf16 v[200:203], a[84:87], a[100:103], v[200:203]\n"
"s_add_u32 s70, s70, s65\n"
"ds_read_b64_tr_b16 a[34:35], v9 offset:8704\n"
"s_getpc_b64 s[96:97]\n"
"L_c10_:\n"
"s_add_u32 s96, s96, L_H6S-L_c10_\n"
"s_addc_u32 s97, s97, 0\n"
"v_mfma_f32_16x16x32_bf16 v[228:231], a[92:95], a[100:103], v[228:231]\n"
"s_lshr_b32 s66, %26, 3\n"
"ds_read_b128 a[100:103], v13 offset:16384\n"
"buffer_load_dword a252, v14, s[52:55], s70 offen\n"
"v_mfma_f32_16x16x32_bf16 v[228:231], a[88:91], a[96:99], v[228:231]\n"
"s_lshl2_add_u32 s66, s66, %16\n"
"ds_read_b128 a[96:99], v12 offset:16384\n"
"v_mfma_f32_16x16x32_bf16 v[232:235], a[88:91], a[104:107], v[232:235]\n"
"s_mul_i32 s66, s66, 128\n"
"ds_read_b64_tr_b16 a[36:37], v9 offset:12288\n"
"v_mfma_f32_16x16x32_bf16 v[232:235], a[92:95], a[108:111], v[232:235]\n"
"s_min_u32 s66, s66, L_headF-L_H6S-128\n"
"ds_read_b64_tr_b16 a[38:39], v9 offset:12800\n"
"v_min_u32 v22, 0, v0\n"
"v_mfma_f32_16x16x32_bf16 v[204:207], a[84:87], a[108:111], v[204:207]\n"
"ds_read_b128 a[108:111], v13 offset:18432\n"
"v_lshl_add_u32 v22, v22, 3, s66\n"
"v_mfma_f32_16x16x32_bf16 v[204:207], a[80:83], a[104:107], v[204:207]\n"
"ds_read_b128 a[104:107], v12 offset:18432\n"
"global_load_dword v23, v22, s[96:97]\n"
"ds_read_b64_tr_b16 a[40:41], v9 offset:8200\n"
"v_mfma_f32_16x16x32_bf16 v[208:211], a[80:83], a[112:115], v[208:211]\n"
"s_add_u32 m0, s64, 8192\n"
"ds_read_b64_tr_b16 a[42:43], v9 offset:8712\n"
"v_mfma_f32_16x16x32_bf16 v[208:211], a[84:87], a[116:119], v[208:211]\n"
"ds_read_b64_tr_b16 a[44:45], v9 offset:12296\n"
"v_mfma_f32_16x16x32_bf16 v[236:239], a[92:95], a[116:119], v[236:239]\n"
"ds_read_b128 a[116:119], v13 offset:20480\n"
"v_mfma_f32_16x16x32_bf16 v[236:239], a[88:91], a[112:115], v[236:239]\n"
"ds_read_b128 a[112:115], v12 offset:20480\n"
"v_mfma_f32_16x16x32_bf16 v[240:243], a[88:91], a[120:123], v[240:243]\n"
"ds_read_b64_tr_b16 a[46:47], v9 offset:12808\n"
"v_mfma_f32_16x16x32_bf16 v[240:243], a[92:95], a[124:127], v[240:243]\n"
"ds_read_b64_tr_b16 a[48:49], v15 offset:8192\n"
"v_mfma_f32_16x16x32_bf16 v[212:215], a[84:87], a[124:127], v[212:215]\n"
"ds_read_b128 a[124:127], v13 offset:22528\n"
"v_mfma_f32_16x16x32_bf16 v[212:215], a[80:83], a[120:123], v[212:215]\n"
"ds_read_b128 a[120:123], v12 offset:22528\n"
"v_mfma_f32_16x16x32_bf16 v[216:219], a[80:83], a[128:131], v[216:219]\n"
"ds_read_b64_tr_b16 a[50:51], v15 offset:8704\n"
"v_mfma_f32_16x16x32_bf16 v[216:219], a[84:87], a[132:135], v[216:219]\n"
"ds_read_b64_tr_b16 a[52:53], v15 offset:12288\n"
"v_mfma_f32_16x16x32_bf16 v[244:247], a[92:95], a[132:135], v[244:247]\n"
"ds_read_b128 a[132:135], v13 offset:24576\n"
"v_mfma_f32_16x16x32_bf16 v[244:247], a[88:91], a[128:131], v[244:247]\n"
"s_add_u32 s67, s75, 655360\n"
"ds_read_b128 a[128:131], v12 offset:24576\n"
"v_mfma_f32_16x16x32_bf16 v[248:251], a[88:91], a[136:139], v[248:251]\n"
"ds_read_b64_tr_b16 a[54:55], v15 offset:12800\n"
"v_mfma_f32_16x16x32_bf16 v[248:251], a[92:95], a[140:143], v[248:251]\n"
"ds_read_b64_tr_b16 a[56:57], v15 offset:8200\n"
"v_mfma_f32_16x16x32_bf16 v[220:223], a[84:87], a[140:143], v[220:223]\n"
"ds_read_b128 a[140:143], v13 offset:26624\n"
"v_mfma_f32_16x16x32_bf16 v[220:223], a[80:83], a[136:139], v[220:223]\n"
"ds_read_b128 a[136:139], v12 offset:26624\n"
"v_mfma_f32_16x16x32_bf16 v[224:227], a[80:83], a[144:147], v[224:227]\n"
"ds_read_b64_tr_b16 a[58:59], v15 offset:8712\n"
"v_mfma_f32_16x16x32_bf16 v[224:227], a[84:87], a[148:151], v[224:227]\n"
"ds_read_b64_tr_b16 a[60:61], v15 offset:12296\n"
"v_mfma_f32_16x16x32_bf16 v[252:255], a[92:95], a[148:151], v[252:255]\n"
"ds_read_b128 a[148:151], v13 offset:28672\n"
"v_mfma_f32_16x16x32_bf16 v[252:255], a[88:91], a[144:147], v[252:255]\n"
"ds_read_b128 a[144:147], v12 offset:28672\n"
"ds_read_b64_tr_b16 a[62:63], v15 offset:12808\n"
"v_mfma_f32_16x16x32_bf16 a[28:31], a[88:91], a[152:155], a[28:31]\n"
"v_mfma_f32_16x16x32_bf16 a[28:31], a[92:95], a[156:159], a[28:31]\n"
"v_mfma_f32_16x16x32_bf16 a[24:27], a[84:87], a[156:159], a[24:27]\n"
"ds_read_b128 a[156:159], v13 offset:30720\n"
"v_mfma_f32_16x16x32_bf16 a[24:27], a[80:83], a[152:155], a[24:27]\n"
"s_waitcnt lgkmcnt(15)\n"
"ds_read_b128 a[152:155], v12 offset:30720\n"
"v_mfma_f32_16x16x32_bf16 v[32:35], a[32:35], a[96:99], v[32:35]\n"
"v_mfma_f32_16x16x32_bf16 v[32:35], a[36:39], a[100:103], v[32:35]\n"
"s_waitcnt lgkmcnt(2)\n"
"v_mfma_f32_16x16x32_bf16 v[60:63], a[44:47], a[100:103], v[60:63]\n"
"buffer_load_dwordx4 v11, s[52:55], s67 offen offset:0  lds\n"
"v_mfma_f32_16x16x32_bf16 v[60:63], a[40:43], a[96:99], v[60:63]\n"
"v_mfma_f32_16x16x32_bf16 v[64:67], a[40:43], a[104:107], v[64:67]\n"
"s_add_u32 s67, s75, 670720\n"
"v_mfma_f32_16x16x32_bf16 v[64:67], a[44:47], a[108:111], v[64:67]\n"
"v_mfma_f32_16x16x32_bf16 v[36:39], a[36:39], a[108:111], v[36:39]\n"
"buffer_load_dwordx4 v11, s[52:55], s67 offen offset:1024  lds\n"
"v_mfma_f32_16x16x32_bf16 v[36:39], a[32:35], a[104:107], v[36:39]\n"
"v_mfma_f32_16x16x32_bf16 v[40:43], a[32:35], a[112:115], v[40:43]\n"
"s_add_u32 s67, s75, 686080\n"
"v_mfma_f32_16x16x32_bf16 v[40:43], a[36:39], a[116:119], v[40:43]\n"
"v_mfma_f32_16x16x32_bf16 v[68:71], a[44:47], a[116:119], v[68:71]\n"
"v_mfma_f32_16x16x32_bf16 v[68:71], a[40:43], a[112:115], v[68:71]\n"
"buffer_load_dwordx4 v11, s[52:55], s67 offen offset:2048  lds\n"
"v_mfma_f32_16x16x32_bf16 v[72:75], a[40:43], a[120:123], v[72:75]\n"
"v_mfma_f32_16x16x32_bf16 v[72:75], a[44:47], a[124:127], v[72:75]\n"
"s_add_u32 s67, s75, 701440\n"
"v_mfma_f32_16x16x32_bf16 v[44:47], a[36:39], a[124:127], v[44:47]\n"
"v_mfma_f32_16x16x32_bf16 v[44:47], a[32:35], a[120:123], v[44:47]\n"
"buffer_load_dwordx4 v11, s[52:55], s67 offen offset:3072  lds\n"
"v_mfma_f32_16x16x32_bf16 v[48:51], a[32:35], a[128:131], v[48:51]\n"
"v_mfma_f32_16x16x32_bf16 v[48:51], a[36:39], a[132:135], v[48:51]\n"
"s_add_u32 m0, s64, 12288\n"
"v_mfma_f32_16x16x32_bf16 v[76:79], a[44:47], a[132:135], v[76:79]\n"
"s_waitcnt vmcnt(10)\n"
"ds_read_b64_tr_b16 a[64:65], v9 offset:0\n"
"v_mfma_f32_16x16x32_bf16 v[76:79], a[40:43], a[128:131], v[76:79]\n"
"s_add_u32 s67, s75, 720896\n"
"ds_read_b64_tr_b16 a[66:67], v9 offset:512\n"
"v_mfma_f32_16x16x32_bf16 v[80:83], a[40:43], a[136:139], v[80:83]\n"
"ds_read_b64_tr_b16 a[68:69], v9 offset:4096\n"
"v_mfma_f32_16x16x32_bf16 v[80:83], a[44:47], a[140:143], v[80:83]\n"
"ds_read_b64_tr_b16 a[70:71], v9 offset:4608\n"
"buffer_load_dwordx4 v11, s[52:55], s67 offen offset:0  lds\n"
"v_mfma_f32_16x16x32_bf16 v[52:55], a[36:39], a[140:143], v[52:55]\n"
"s_add_u32 s67, s75, 736256\n"
"ds_read_b64_tr_b16 a[72:73], v9 offset:8\n"
"v_mfma_f32_16x16x32_bf16 v[52:55], a[32:35], a[136:139], v[52:55]\n"
"s_waitcnt lgkmcnt(5)\n"
"ds_read_b64_tr_b16 a[74:75], v9 offset:520\n"
"v_mfma_f32_16x16x32_bf16 v[56:59], a[32:35], a[144:147], v[56:59]\n"
"ds_read_b64_tr_b16 a[76:77], v9 offset:4104\n"
"v_mfma_f32_16x16x32_bf16 v[56:59], a[36:39], a[148:151], v[56:59]\n"
"ds_read_b64_tr_b16 a[78:79], v9 offset:4616\n"
"v_mfma_f32_16x16x32_bf16 v[84:87], a[44:47], a[148:151], v[84:87]\n"
"buffer_load_dwordx4 v11, s[52:55], s67 offen offset:1024  lds\n"
"v_mfma_f32_16x16x32_bf16 v[84:87], a[40:43], a[144:147], v[84:87]\n"
"ds_read_b64_tr_b16 a[80:81], v15 offset:0\n"
"v_mfma_f32_16x16x32_bf16 a[4:7], a[40:43], a[152:155], a[4:7]\n"
"s_add_u32 s67, s75, 751616\n"
"ds_read_b64_tr_b16 a[82:83], v15 offset:512\n"
"v_mfma_f32_16x16x32_bf16 a[4:7], a[44:47], a[156:159], a[4:7]\n"
"ds_read_b64_tr_b16 a[84:85], v15 offset:4096\n"
"v_mfma_f32_16x16x32_bf16 a[0:3], a[36:39], a[156:159], a[0:3]\n"
"ds_read_b64_tr_b16 a[86:87], v15 offset:4608\n"
"buffer_load_dwordx4 v11, s[52:55], s67 offen offset:2048  lds\n"
"v_mfma_f32_16x16x32_bf16 a[0:3], a[32:35], a[152:155], a[0:3]\n"
"ds_read_b64_tr_b16 a[88:89], v15 offset:8\n"
"v_mfma_f32_16x16x32_bf16 v[88:91], a[48:51], a[96:99], v[88:91]\n"
"s_add_u32 s67, s75, 766976\n"
"ds_read_b64_tr_b16 a[90:91], v15 offset:520\n"
"v_mfma_f32_16x16x32_bf16 v[88:91], a[52:55], a[100:103], v[88:91]\n"
"ds_read_b64_tr_b16 a[92:93], v15 offset:4104\n"
"v_mfma_f32_16x16x32_bf16 v[116:119], a[60:63], a[100:103], v[116:119]\n"
"ds_read_b64_tr_b16 a[94:95], v15 offset:4616\n"
"v_mfma_f32_16x16x32_bf16 v[116:119], a[56:59], a[96:99], v[116:119]\n"
"buffer_load_dwordx4 v11, s[52:55], s67 offen offset:3072  lds\n"
"v_mfma_f32_16x16x32_bf16 v[120:123], a[56:59], a[104:107], v[120:123]\n"
"v_mfma_f32_16x16x32_bf16 v[120:123], a[60:63], a[108:111], v[120:123]\n"
"s_add_u32 m0, s64, 0\n"
"v_mfma_f32_16x16x32_bf16 v[92:95], a[52:55], a[108:111], v[92:95]\n"
"v_mfma_f32_16x16x32_bf16 v[92:95], a[48:51], a[104:107], v[92:95]\n"
"s_add_u32 s67, s75, 655488\n"
"v_mfma_f32_16x16x32_bf16 v[96:99], a[48:51], a[112:115], v[96:99]\n"
"s_waitcnt lgkmcnt(0)\n"
"v_mfma_f32_16x16x32_bf16 v[96:99], a[52:55], a[116:119], v[96:99]\n"
"buffer_load_dwordx4 v11, s[52:55], s67 offen offset:0  lds\n"
"v_mfma_f32_16x16x32_bf16 v[124:127], a[60:63], a[116:119], v[124:127]\n"
"v_mfma_f32_16x16x32_bf16 v[124:127], a[56:59], a[112:115], v[124:127]\n"
"s_add_u32 s67, s75, 670848\n"
"v_mfma_f32_16x16x32_bf16 v[128:131], a[56:59], a[120:123], v[128:131]\n"
"v_mfma_f32_16x16x32_bf16 v[128:131], a[60:63], a[124:127], v[128:131]\n"
"v_mfma_f32_16x16x32_bf16 v[100:103], a[52:55], a[124:127], v[100:103]\n"
"buffer_load_dwordx4 v11, s[52:55], s67 offen offset:1024  lds\n"
"v_mfma_f32_16x16x32_bf16 v[100:103], a[48:51], a[120:123], v[100:103]\n"
"v_mfma_f32_16x16x32_bf16 v[104:107], a[48:51], a[128:131], v[104:107]\n"
"s_add_u32 s67, s75, 686208\n"
"v_mfma_f32_16x16x32_bf16 v[104:107], a[52:55], a[132:135], v[104:107]\n"
"v_mfma_f32_16x16x32_bf16 v[132:135], a[60:63], a[132:135], v[132:135]\n"
"v_mfma_f32_16x16x32_bf16 v[132:135], a[56:59], a[128:131], v[132:135]\n"
"buffer_load_dwordx4 v11, s[52:55], s67 offen offset:2048  lds\n"
"v_mfma_f32_16x16x32_bf16 v[136:139], a[56:59], a[136:139], v[136:139]\n"
"v_mfma_f32_16x16x32_bf16 v[136:139], a[60:63], a[140:143], v[136:139]\n"
"s_add_u32 s67, s75, 701568\n"
"v_mfma_f32_16x16x32_bf16 v[108:111], a[52:55], a[140:143], v[108:111]\n"
"v_mfma_f32_16x16x32_bf16 v[108:111], a[48:51], a[136:139], v[108:111]\n"
"buffer_load_dwordx4 v11, s[52:55], s67 offen offset:3072  lds\n"
"v_mfma_f32_16x16x32_bf16 v[112:115], a[48:51], a[144:147], v[112:115]\n"
"v_mfma_f32_16x16x32_bf16 v[112:115], a[52:55], a[148:151], v[112:115]\n"
"s_add_u32 m0, s64, 4096\n"
"v_mfma_f32_16x16x32_bf16 v[140:143], a[60:63], a[148:151], v[140:143]\n"
"v_mfma_f32_16x16x32_bf16 v[140:143], a[56:59], a[144:147], v[140:143]\n"
"s_add_u32 s67, s75, 721024\n"
"v_mfma_f32_16x16x32_bf16 a[12:15], a[56:59], a[152:155], a[12:15]\n"
"v_mfma_f32_16x16x32_bf16 a[12:15], a[60:63], a[156:159], a[12:15]\n"
"buffer_load_dwordx4 v11, s[52:55], s67 offen offset:0  lds\n"
"v_mfma_f32_16x16x32_bf16 a[8:11], a[52:55], a[156:159], a[8:11]\n"
"v_mfma_f32_16x16x32_bf16 a[8:11], a[48:51], a[152:155], a[8:11]\n"
"s_add_u32 s67, s75, 736384\n"
"v_mfma_f32_16x16x32_bf16 v[144:147], a[64:67], a[96:99], v[144:147]\n"
"v_mfma_f32_16x16x32_bf16 v[144:147], a[68:71], a[100:103], v[144:147]\n"
"buffer_load_dwordx4 v11, s[52:55], s67 offen offset:1024  lds\n"
"v_mfma_f32_16x16x32_bf16 v[172:175], a[76:79], a[100:103], v[172:175]\n"
"v_mfma_f32_16x16x32_bf16 v[172:175], a[72:75], a[96:99], v[172:175]\n"
"s_add_u32 s67, s75, 751744\n"
"v_mfma_f32_16x16x32_bf16 v[176:179], a[72:75], a[104:107], v[176:179]\n"
"v_mfma_f32_16x16x32_bf16 v[176:179], a[76:79], a[108:111], v[176:179]\n"
"buffer_load_dwordx4 v11, s[52:55], s67 offen offset:2048  lds\n"
"v_mfma_f32_16x16x32_bf16 v[148:151], a[68:71], a[108:111], v[148:151]\n"
"v_mfma_f32_16x16x32_bf16 v[148:151], a[64:67], a[104:107], v[148:151]\n"
"s_add_u32 s67, s75, 767104\n"
"v_mfma_f32_16x16x32_bf16 v[152:155], a[64:67], a[112:115], v[152:155]\n"
"s_sub_u32 s66, 40960, s73\n"
"v_mfma_f32_16x16x32_bf16 v[152:155], a[68:71], a[116:119], v[152:155]\n"
"v_mfma_f32_16x16x32_bf16 v[180:183], a[76:79], a[116:119], v[180:183]\n"
"v_add_u32 v12, s66, v7\n"
"v_mfma_f32_16x16x32_bf16 v[180:183], a[72:75], a[112:115], v[180:183]\n"
"buffer_load_dwordx4 v11, s[52:55], s67 offen offset:3072  lds\n"
"v_mfma_f32_16x16x32_bf16 v[184:187], a[72:75], a[120:123], v[184:187]\n"
"s_add_u32 m0, s72, 98304\n"
"v_add_u32 v13, s66, v8\n"
"v_mfma_f32_16x16x32_bf16 v[184:187], a[76:79], a[124:127], v[184:187]\n"
"v_mfma_f32_16x16x32_bf16 v[156:159], a[68:71], a[124:127], v[156:159]\n"
"s_add_u32 s67, s60, 768\n"
"v_mfma_f32_16x16x32_bf16 v[156:159], a[64:67], a[120:123], v[156:159]\n"
"buffer_load_dwordx4 v6, s[48:51], s67 offen offset:0  lds\n"
"v_mfma_f32_16x16x32_bf16 v[160:163], a[64:67], a[128:131], v[160:163]\n"
"v_mfma_f32_16x16x32_bf16 v[160:163], a[68:71], a[132:135], v[160:163]\n"
"s_add_u32 s67, s60, 16128\n"
"v_mfma_f32_16x16x32_bf16 v[188:191], a[76:79], a[132:135], v[188:191]\n"
"v_mfma_f32_16x16x32_bf16 v[188:191], a[72:75], a[128:131], v[188:191]\n"
"buffer_load_dwordx4 v6, s[48:51], s67 offen offset:1024  lds\n"
"v_mfma_f32_16x16x32_bf16 v[192:195], a[72:75], a[136:139], v[192:195]\n"
"v_mfma_f32_16x16x32_bf16 v[192:195], a[76:79], a[140:143], v[192:195]\n"
"s_add_u32 s67, s60, 31488\n"
"v_mfma_f32_16x16x32_bf16 v[164:167], a[68:71], a[140:143], v[164:167]\n"
"v_mfma_f32_16x16x32_bf16 v[164:167], a[64:67], a[136:139], v[164:167]\n"
"buffer_load_dwordx4 v6, s[48:51], s67 offen offset:2048  lds\n"
"v_mfma_f32_16x16x32_bf16 v[168:171], a[64:67], a[144:147], v[168:171]\n"
"v_mfma_f32_16x16x32_bf16 v[168:171], a[68:71], a[148:151], v[168:171]\n"
"s_add_u32 s67, s60, 46848\n"
"v_mfma_f32_16x16x32_bf16 v[196:199], a[76:79], a[148:151], v[196:199]\n"
"v_mfma_f32_16x16x32_bf16 v[196:199], a[72:75], a[144:147], v[196:199]\n"
"v_mfma_f32_16x16x32_bf16 a[20:23], a[72:75], a[152:155], a[20:23]\n"
"buffer_load_dwordx4 v6, s[48:51], s67 offen offset:3072  lds\n"
"v_mfma_f32_16x16x32_bf16 a[20:23], a[76:79], a[156:159], a[20:23]\n"
"v_mfma_f32_16x16x32_bf16 a[16:19], a[68:71], a[156:159], a[16:19]\n"
"s_sub_u32 s70, 1572864, s75\n"
"s_add_u32 s70, s70, s65\n"
"v_mfma_f32_16x16x32_bf16 a[16:19], a[64:67], a[152:155], a[16:19]\n"
"s_waitcnt vmcnt(22)\n"
"s_barrier\n"
"v_mfma_f32_16x16x32_bf16 v[200:203], a[80:83], a[96:99], v[200:203]\n"
"s_waitcnt vmcnt(12)\n"
"ds_read_b64_tr_b16 a[32:33], v9 offset:8192\n"
"v_mfma_f32_16x16x32_bf16 v[200:203], a[84:87], a[100:103], v[200:203]\n"
"ds_read_b64_tr_b16 a[34:35], v9 offset:8704\n"
"v_mfma_f32_16x16x32_bf16 v[228:231], a[92:95], a[100:103], v[228:231]\n"
"ds_read_b128 a[100:103], v13 offset:16384\n"
"buffer_load_dword a252, v14, s[52:55], s70 offen\n"
"v_mfma_f32_16x16x32_bf16 v[228:231], a[88:91], a[96:99], v[228:231]\n"
"ds_read_b128 a[96:99], v12 offset:16384\n"
"v_mfma_f32_16x16x32_bf16 v[232:235], a[88:91], a[104:107], v[232:235]\n"
"buffer_store_dwordx2 v[16:17], v18, s[56:59], %11 offen sc1\n"
"ds_read_b64_tr_b16 a[36:37], v9 offset:12288\n"
"v_mfma_f32_16x16x32_bf16 v[232:235], a[92:95], a[108:111], v[232:235]\n"
"ds_read_b64_tr_b16 a[38:39], v9 offset:12800\n"
"v_mfma_f32_16x16x32_bf16 v[204:207], a[84:87], a[108:111], v[204:207]\n"
"ds_read_b128 a[108:111], v13 offset:18432\n"
"v_mfma_f32_16x16x32_bf16 v[204:207], a[80:83], a[104:107], v[204:207]\n"
"ds_read_b128 a[104:107], v12 offset:18432\n"
"buffer_store_dwordx2 v[16:17], v18, s[56:59], s69 offen sc1\n"
"ds_read_b64_tr_b16 a[40:41], v9 offset:8200\n"
"v_mfma_f32_16x16x32_bf16 v[208:211], a[80:83], a[112:115], v[208:211]\n"
"ds_read_b64_tr_b16 a[42:43], v9 offset:8712\n"
"v_mfma_f32_16x16x32_bf16 v[208:211], a[84:87], a[116:119], v[208:211]\n"
"ds_read_b64_tr_b16 a[44:45], v9 offset:12296\n"
"v_mfma_f32_16x16x32_bf16 v[236:239], a[92:95], a[116:119], v[236:239]\n"
"ds_read_b128 a[116:119], v13 offset:20480\n"
"v_mfma_f32_16x16x32_bf16 v[236:239], a[88:91], a[112:115], v[236:239]\n"
"ds_read_b128 a[112:115], v12 offset:20480\n"
"v_mfma_f32_16x16x32_bf16 v[240:243], a[88:91], a[120:123], v[240:243]\n"
"s_add_u32 m0, s64, 8192\n"
"ds_read_b64_tr_b16 a[46:47], v9 offset:12808\n"
"v_mfma_f32_16x16x32_bf16 v[240:243], a[92:95], a[124:127], v[240:243]\n"
"ds_read_b64_tr_b16 a[48:49], v15 offset:8192\n"
"v_mfma_f32_16x16x32_bf16 v[212:215], a[84:87], a[124:127], v[212:215]\n"
"ds_read_b128 a[124:127], v13 offset:22528\n"
"v_mfma_f32_16x16x32_bf16 v[212:215], a[80:83], a[120:123], v[212:215]\n"
"ds_read_b128 a[120:123], v12 offset:22528\n"
"v_mfma_f32_16x16x32_bf16 v[216:219], a[80:83], a[128:131], v[216:219]\n"
"ds_read_b64_tr_b16 a[50:51], v15 offset:8704\n"
"v_mfma_f32_16x16x32_bf16 v[216:219], a[84:87], a[132:135], v[216:219]\n"
"ds_read_b64_tr_b16 a[52:53], v15 offset:12288\n"
"v_mfma_f32_16x16x32_bf16 v[244:247], a[92:95], a[132:135], v[244:247]\n"
"ds_read_b128 a[132:135], v13 offset:24576\n"
"v_mfma_f32_16x16x32_bf16 v[244:247], a[88:91], a[128:131], v[244:247]\n"
"ds_read_b128 a[128:131], v12 offset:24576\n"
"v_mfma_f32_16x16x32_bf16 v[248:251], a[88:91], a[136:139], v[248:251]\n"
"ds_read_b64_tr_b16 a[54:55], v15 offset:12800\n"
"v_mfma_f32_16x16x32_bf16 v[248:251], a[92:95], a[140:143], v[248:251]\n"
"ds_read_b64_tr_b16 a[56:57], v15 offset:8200\n"
"v_mfma_f32_16x16x32_bf16 v[220:223], a[84:87], a[140:143], v[220:223]\n"
"s_add_u32 s67, s75, 786432\n"
"ds_read_b128 a[140:143], v13 offset:26624\n"
"v_mfma_f32_16x16x32_bf16 v[220:223], a[80:83], a[136:139], v[220:223]\n"
"ds_read_b128 a[136:139], v12 offset:26624\n"
"v_mfma_f32_16x16x32_bf16 v[224:227], a[80:83], a[144:147], v[224:227]\n"
"ds_read_b64_tr_b16 a[58:59], v15 offset:8712\n"
"v_mfma_f32_16x16x32_bf16 v[224:227], a[84:87], a[148:151], v[224:227]\n"
"ds_read_b64_tr_b16 a[60:61], v15 offset:12296\n"
"v_mfma_f32_16x16x32_bf16 v[252:255], a[92:95], a[148:151], v[252:255]\n"
"ds_read_b128 a[148:151], v13 offset:28672\n"
"v_mfma_f32_16x16x32_bf16 v[252:255], a[88:91], a[144:147], v[252:255]\n"
"ds_read_b128 a[144:147], v12 offset:28672\n"
"ds_read_b64_tr_b16 a[62:63], v15 offset:12808\n"
"v_mfma_f32_16x16x32_bf16 a[28:31], a[88:91], a[152:155], a[28:31]\n"
"v_mfma_f32_16x16x32_bf16 a[28:31], a[92:95], a[156:159], a[28:31]\n"
"v_mfma_f32_16x16x32_bf16 a[24:27], a[84:87], a[156:159], a[24:27]\n"
"ds_read_b128 a[156:159], v13 offset:30720\n"
"v_mfma_f32_16x16x32_bf16 a[24:27], a[80:83], a[152:155], a[24:27]\n"
"s_waitcnt lgkmcnt(15)\n"
"ds_read_b128 a[152:155], v12 offset:30720\n"
"v_mfma_f32_16x16x32_bf16 v[32:35], a[32:35], a[96:99], v[32:35]\n"
"L_st1575:\n"
"s_cbranch_execz L_st3848\n"
"v_mfma_f32_16x16x32_bf16 v[32:35], a[36:39], a[100:103], v[32:35]\n"
"s_waitcnt lgkmcnt(2)\n"
"v_mfma_f32_16x16x32_bf16 v[60:63], a[44:47], a[100:103], v[60:63]\n"
"buffer_load_dwordx4 v11, s[52:55], s67 offen offset:0  lds\n"
"v_mfma_f32_16x16x32_bf16 v[60:63], a[40:43], a[96:99], v[60:63]\n"
"v_mfma_f32_16x16x32_bf16 v[64:67], a[40:43], a[104:107], v[64:67]\n"
"s_add_u32 s67, s75, 801792\n"
"v_mfma_f32_16x16x32_bf16 v[64:67], a[44:47], a[108:111], v[64:67]\n"
"v_mfma_f32_16x16x32_bf16 v[36:39], a[36:39], a[108:111], v[36:39]\n"
"buffer_load_dwordx4 v11, s[52:55], s67 offen offset:1024  lds\n"
"v_mfma_f32_16x16x32_bf16 v[36:39], a[32:35], a[104:107], v[36:39]\n"
"v_mfma_f32_16x16x32_bf16 v[40:43], a[32:35], a[112:115], v[40:43]\n"
"s_add_u32 s67, s75, 817152\n"
"v_mfma_f32_16x16x32_bf16 v[40:43], a[36:39], a[116:119], v[40:43]\n"
"v_mfma_f32_16x16x32_bf16 v[68:71], a[44:47], a[116:119], v[68:71]\n"
"s_mov_b32 s30, 0\n"
"v_mfma_f32_16x16x32_bf16 v[68:71], a[40:43], a[112:115], v[68:71]\n"
"buffer_load_dwordx4 v11, s[52:55], s67 offen offset:2048  lds\n"
"v_mfma_f32_16x16x32_bf16 v[72:75], a[40:43], a[120:123], v[72:75]\n"
"s_mov_b32 s31, 0\n"
"v_mfma_f32_16x16x32_bf16 v[72:75], a[44:47], a[124:127], v[72:75]\n"
"s_add_u32 s67, s75, 832512\n"
"v_mfma_f32_16x16x32_bf16 v[44:47], a[36:39], a[124:127], v[44:47]\n"
"v_mfma_f32_16x16x32_bf16 v[44:47], a[32:35], a[120:123], v[44:47]\n"
"v_mfma_f32_16x16x32_bf16 v[48:51], a[32:35], a[128:131], v[48:51]\n"
"buffer_load_dwordx4 v11, s[52:55], s67 offen offset:3072  lds\n"
"v_mfma_f32_16x16x32_bf16 v[48:51], a[36:39], a[132:135], v[48:51]\n"
"s_add_u32 m0, s64, 12288\n"
"v_mfma_f32_16x16x32_bf16 v[76:79], a[44:47], a[132:135], v[76:79]\n"
"s_waitcnt vmcnt(11)\n"
"ds_read_b64_tr_b16 a[64:65], v9 offset:0\n"
"v_mfma_f32_16x16x32_bf16 v[76:79], a[40:43], a[128:131], v[76:79]\n"
"s_add_u32 s67, s75, 851968\n"
"ds_read_b64_tr_b16 a[66:67], v9 offset:512\n"
"v_mfma_f32_16x16x32_bf16 v[80:83], a[40:43], a[136:139], v[80:83]\n"
"ds_read_b64_tr_b16 a[68:69], v9 offset:4096\n"
"v_mfma_f32_16x16x32_bf16 v[80:83], a[44:47], a[140:143], v[80:83]\n"
"ds_read_b64_tr_b16 a[70:71], v9 offset:4608\n"
"buffer_load_dwordx4 v11, s[52:55], s67 offen offset:0  lds\n"
"v_mfma_f32_16x16x32_bf16 v[52:55], a[36:39], a[140:143], v[52:55]\n"
"s_add_u32 s67, s75, 867328\n"
"ds_read_b64_tr_b16 a[72:73], v9 offset:8\n"
"v_mfma_f32_16x16x32_bf16 v[52:55], a[32:35], a[136:139], v[52:55]\n"
"s_waitcnt lgkmcnt(5)\n"
"ds_read_b64_tr_b16 a[74:75], v9 offset:520\n"
"v_mfma_f32_16x16x32_bf16 v[56:59], a[32:35], a[144:147], v[56:59]\n"
"ds_read_b64_tr_b16 a[76:77], v9 offset:4104\n"
"v_mfma_f32_16x16x32_bf16 v[56:59], a[36:39], a[148:151], v[56:59]\n"
"ds_read_b64_tr_b16 a[78:79], v9 offset:4616\n"
"v_mfma_f32_16x16x32_bf16 v[84:87], a[44:47], a[148:151], v[84:87]\n"
"buffer_load_dwordx4 v11, s[52:55], s67 offen offset:1024  lds\n"
"v_mfma_f32_16x16x32_bf16 v[84:87], a[40:43], a[144:147], v[84:87]\n"
"ds_read_b64_tr_b16 a[80:81], v15 offset:0\n"
"v_mfma_f32_16x16x32_bf16 a[4:7], a[40:43], a[152:155], a[4:7]\n"
"s_add_u32 s67, s75, 882688\n"
"ds_read_b64_tr_b16 a[82:83], v15 offset:512\n"
"v_mfma_f32_16x16x32_bf16 a[4:7], a[44:47], a[156:159], a[4:7]\n"
"ds_read_b64_tr_b16 a[84:85], v15 offset:4096\n"
"buffer_load_dwordx4 v11, s[52:55], s67 offen offset:2048  lds\n"
"v_mfma_f32_16x16x32_bf16 a[0:3], a[36:39], a[156:159], a[0:3]\n"
"s_add_u32 s96, s56, %11\n"
"ds_read_b64_tr_b16 a[86:87], v15 offset:4608\n"
"s_addc_u32 s97, s57, 0\n"
"v_mfma_f32_16x16x32_bf16 a[0:3], a[32:35], a[152:155], a[0:3]\n"
"ds_read_b64_tr_b16 a[88:89], v15 offset:8\n"
"v_mfma_f32_16x16x32_bf16 v[88:91], a[48:51], a[96:99], v[88:91]\n"
"s_add_u32 s67, s75, 898048\n"
"ds_read_b64_tr_b16 a[90:91], v15 offset:520\n"
"v_mfma_f32_16x16x32_bf16 v[88:91], a[52:55], a[100:103], v[88:91]\n"
"ds_read_b64_tr_b16 a[92:93], v15 offset:4104\n"
"v_mfma_f32_16x16x32_bf16 v[116:119], a[60:63], a[100:103], v[116:119]\n"
"ds_read_b64_tr_b16 a[94:95], v15 offset:4616\n"
"v_mfma_f32_16x16x32_bf16 v[116:119], a[56:59], a[96:99], v[116:119]\n"
"buffer_load_dwordx4 v11, s[52:55], s67 offen offset:3072  lds\n"
"v_mfma_f32_16x16x32_bf16 v[120:123], a[56:59], a[104:107], v[120:123]\n"
"v_mfma_f32_16x16x32_bf16 v[120:123], a[60:63], a[108:111], v[120:123]\n"
"s_add_u32 m0, s64, 0\n"
"v_mfma_f32_16x16x32_bf16 v[92:95], a[52:55], a[108:111], v[92:95]\n"
"v_mfma_f32_16x16x32_bf16 v[92:95], a[48:51], a[104:107], v[92:95]\n"
"s_add_u32 s67, s75, 786560\n"
"v_mfma_f32_16x16x32_bf16 v[96:99], a[48:51], a[112:115], v[96:99]\n"
"s_waitcnt lgkmcnt(0)\n"
"v_mfma_f32_16x16x32_bf16 v[96:99], a[52:55], a[116:119], v[96:99]\n"
"buffer_load_dwordx4 v11, s[52:55], s67 offen offset:0  lds\n"
"v_mfma_f32_16x16x32_bf16 v[124:127], a[60:63], a[116:119], v[124:127]\n"
"s_load_dword s98, s[96:97], 512 glc\n"
"v_mfma_f32_16x16x32_bf16 v[124:127], a[56:59], a[112:115], v[124:127]\n"
"s_load_dword s99, s[96:97], 8704 glc\n"
"s_add_u32 s67, s75, 801920\n"
"v_mfma_f32_16x16x32_bf16 v[128:131], a[56:59], a[120:123], v[128:131]\n"
"v_mfma_f32_16x16x32_bf16 v[128:131], a[60:63], a[124:127], v[128:131]\n"
"buffer_load_dwordx4 v11, s[52:55], s67 offen offset:1024  lds\n"
"v_mfma_f32_16x16x32_bf16 v[100:103], a[52:55], a[124:127], v[100:103]\n"
"v_mfma_f32_16x16x32_bf16 v[100:103], a[48:51], a[120:123], v[100:103]\n"
"s_add_u32 s67, s75, 817280\n"
"v_mfma_f32_16x16x32_bf16 v[104:107], a[48:51], a[128:131], v[104:107]\n"
"v_mfma_f32_16x16x32_bf16 v[104:107], a[52:55], a[132:135], v[104:107]\n"
"v_mfma_f32_16x16x32_bf16 v[132:135], a[60:63], a[132:135], v[132:135]\n"
"buffer_load_dwordx4 v11, s[52:55], s67 offen offset:2048  lds\n"
"v_mfma_f32_16x16x32_bf16 v[132:135], a[56:59], a[128:131], v[132:135]\n"
"v_mfma_f32_16x16x32_bf16 v[136:139], a[56:59], a[136:139], v[136:139]\n"
"s_add_u32 s67, s75, 832640\n"
"v_mfma_f32_16x16x32_bf16 v[136:139], a[60:63], a[140:143], v[136:139]\n"
"v_mfma_f32_16x16x32_bf16 v[108:111], a[52:55], a[140:143], v[108:111]\n"
"buffer_load_dwordx4 v11, s[52:55], s67 offen offset:3072  lds\n"
"v_mfma_f32_16x16x32_bf16 v[108:111], a[48:51], a[136:139], v[108:111]\n"
"v_mfma_f32_16x16x32_bf16 v[112:115], a[48:51], a[144:147], v[112:115]\n"
"s_add_u32 m0, s64, 4096\n"
"v_mfma_f32_16x16x32_bf16 v[112:115], a[52:55], a[148:151], v[112:115]\n"
"v_mfma_f32_16x16x32_bf16 v[140:143], a[60:63], a[148:151], v[140:143]\n"
"s_add_u32 s67, s75, 852096\n"
"v_mfma_f32_16x16x32_bf16 v[140:143], a[56:59], a[144:147], v[140:143]\n"
"v_mfma_f32_16x16x32_bf16 a[12:15], a[56:59], a[152:155], a[12:15]\n"
"buffer_load_dwordx4 v11, s[52:55], s67 offen offset:0  lds\n"
"v_mfma_f32_16x16x32_bf16 a[12:15], a[60:63], a[156:159], a[12:15]\n"
"v_mfma_f32_16x16x32_bf16 a[8:11], a[52:55], a[156:159], a[8:11]\n"
"s_add_u32 s67, s75, 867456\n"
"v_mfma_f32_16x16x32_bf16 a[8:11], a[48:51], a[152:155], a[8:11]\n"
"v_mfma_f32_16x16x32_bf16 v[144:147], a[64:67], a[96:99], v[144:147]\n"
"buffer_load_dwordx4 v11, s[52:55], s67 offen offset:1024  lds\n"
"v_mfma_f32_16x16x32_bf16 v[144:147], a[68:71], a[100:103], v[144:147]\n"
"v_mfma_f32_16x16x32_bf16 v[172:175], a[76:79], a[100:103], v[172:175]\n"
"s_add_u32 s67, s75, 882816\n"
"v_mfma_f32_16x16x32_bf16 v[172:175], a[72:75], a[96:99], v[172:175]\n"
"v_mfma_f32_16x16x32_bf16 v[176:179], a[72:75], a[104:107], v[176:179]\n"
"buffer_load_dwordx4 v11, s[52:55], s67 offen offset:2048  lds\n"
"v_mfma_f32_16x16x32_bf16 v[176:179], a[76:79], a[108:111], v[176:179]\n"
"v_mfma_f32_16x16x32_bf16 v[148:151], a[68:71], a[108:111], v[148:151]\n"
"s_add_u32 s67, s75, 898176\n"
"v_mfma_f32_16x16x32_bf16 v[148:151], a[64:67], a[104:107], v[148:151]\n"
"v_mfma_f32_16x16x32_bf16 v[152:155], a[64:67], a[112:115], v[152:155]\n"
"s_sub_u32 s66, 81920, s73\n"
"v_mfma_f32_16x16x32_bf16 v[152:155], a[68:71], a[116:119], v[152:155]\n"
"buffer_load_dwordx4 v11, s[52:55], s67 offen offset:3072  lds\n"
"v_mfma_f32_16x16x32_bf16 v[180:183], a[76:79], a[116:119], v[180:183]\n"
"s_add_u32 m0, s72, 139264\n"
"v_add_u32 v12, s66, v7\n"
"v_mfma_f32_16x16x32_bf16 v[180:183], a[72:75], a[112:115], v[180:183]\n"
"v_mfma_f32_16x16x32_bf16 v[184:187], a[72:75], a[120:123], v[184:187]\n"
"s_add_u32 s67, s60, 896\n"
"v_add_u32 v13, s66, v8\n"
"v_mfma_f32_16x16x32_bf16 v[184:187], a[76:79], a[124:127], v[184:187]\n"
"v_mfma_f32_16x16x32_bf16 v[156:159], a[68:71], a[124:127], v[156:159]\n"
"buffer_load_dwordx4 v6, s[48:51], s67 offen offset:0  lds\n"
"v_mfma_f32_16x16x32_bf16 v[156:159], a[64:67], a[120:123], v[156:159]\n"
"v_mfma_f32_16x16x32_bf16 v[160:163], a[64:67], a[128:131], v[160:163]\n"
"s_add_u32 s67, s60, 16256\n"
"v_mfma_f32_16x16x32_bf16 v[160:163], a[68:71], a[132:135], v[160:163]\n"
"v_mfma_f32_16x16x32_bf16 v[188:191], a[76:79], a[132:135], v[188:191]\n"
"buffer_load_dwordx4 v6, s[48:51], s67 offen offset:1024  lds\n"
"v_mfma_f32_16x16x32_bf16 v[188:191], a[72:75], a[128:131], v[188:191]\n"
"v_mfma_f32_16x16x32_bf16 v[192:195], a[72:75], a[136:139], v[192:195]\n"
"s_add_u32 s67, s60, 31616\n"
"v_mfma_f32_16x16x32_bf16 v[192:195], a[76:79], a[140:143], v[192:195]\n"
"v_mfma_f32_16x16x32_bf16 v[164:167], a[68:71], a[140:143], v[164:167]\n"
"v_mfma_f32_16x16x32_bf16 v[164:167], a[64:67], a[136:139], v[164:167]\n"
"buffer_load_dwordx4 v6, s[48:51], s67 offen offset:2048  lds\n"
"v_mfma_f32_16x16x32_bf16 v[168:171], a[64:67], a[144:147], v[168:171]\n"
"v_mfma_f32_16x16x32_bf16 v[168:171], a[68:71], a[148:151], v[168:171]\n"
"s_add_u32 s67, s60, 46976\n"
"v_mfma_f32_16x16x32_bf16 v[196:199], a[76:79], a[148:151], v[196:199]\n"
"v_mfma_f32_16x16x32_bf16 v[196:199], a[72:75], a[144:147], v[196:199]\n"
"buffer_load_dwordx4 v6, s[48:51], s67 offen offset:3072  lds\n"
"v_mfma_f32_16x16x32_bf16 a[20:23], a[72:75], a[152:155], a[20:23]\n"
"v_mfma_f32_16x16x32_bf16 a[20:23], a[76:79], a[156:159], a[20:23]\n"
"s_waitcnt lgkmcnt(0)\n"
"v_mfma_f32_16x16x32_bf16 a[16:19], a[68:71], a[156:159], a[16:19]\n"
"v_mfma_f32_16x16x32_bf16 a[16:19], a[64:67], a[152:155], a[16:19]\n"
"s_waitcnt vmcnt(23)\n"
"s_barrier\n"
"v_mfma_f32_16x16x32_bf16 v[200:203], a[80:83], a[96:99], v[200:203]\n"
"s_waitcnt vmcnt(12)\n"
"ds_read_b64_tr_b16 a[32:33], v9 offset:8192\n"
"v_mfma_f32_16x16x32_bf16 v[200:203], a[84:87], a[100:103], v[200:203]\n"
"ds_read_b64_tr_b16 a[34:35], v9 offset:8704\n"
"v_mfma_f32_16x16x32_bf16 v[228:231], a[92:95], a[100:103], v[228:231]\n"
"s_add_u32 m0, s64, 8192\n"
"ds_read_b128 a[100:103], v13 offset:16384\n"
"v_mfma_f32_16x16x32_bf16 v[228:231], a[88:91], a[96:99], v[228:231]\n"
"ds_read_b128 a[96:99], v12 offset:16384\n"
"v_mfma_f32_16x16x32_bf16 v[232:235], a[88:91], a[104:107], v[232:235]\n"
"ds_read_b64_tr_b16 a[36:37], v9 offset:12288\n"
"v_mfma_f32_16x16x32_bf16 v[232:235], a[92:95], a[108:111], v[232:235]\n"
"ds_read_b64_tr_b16 a[38:39], v9 offset:12800\n"
"v_mfma_f32_16x16x32_bf16 v[204:207], a[84:87], a[108:111], v[204:207]\n"
"ds_read_b128 a[108:111], v13 offset:18432\n"
"v_mfma_f32_16x16x32_bf16 v[204:207], a[80:83], a[104:107], v[204:207]\n"
"ds_read_b128 a[104:107], v12 offset:18432\n"
"ds_read_b64_tr_b16 a[40:41], v9 offset:8200\n"
"v_mfma_f32_16x16x32_bf16 v[208:211], a[80:83], a[112:115], v[208:211]\n"
"ds_read_b64_tr_b16 a[42:43], v9 offset:8712\n"
"v_mfma_f32_16x16x32_bf16 v[208:211], a[84:87], a[116:119], v[208:211]\n"
"ds_read_b64_tr_b16 a[44:45], v9 offset:12296\n"
"v_mfma_f32_16x16x32_bf16 v[236:239], a[92:95], a[116:119], v[236:239]\n"
"ds_read_b128 a[116:119], v13 offset:20480\n"
"v_mfma_f32_16x16x32_bf16 v[236:239], a[88:91], a[112:115], v[236:239]\n"
"ds_read_b128 a[112:115], v12 offset:20480\n"
"v_mfma_f32_16x16x32_bf16 v[240:243], a[88:91], a[120:123], v[240:243]\n"
"ds_read_b64_tr_b16 a[46:47], v9 offset:12808\n"
"v_mfma_f32_16x16x32_bf16 v[240:243], a[92:95], a[124:127], v[240:243]\n"
"ds_read_b64_tr_b16 a[48:49], v15 offset:8192\n"
"v_mfma_f32_16x16x32_bf16 v[212:215], a[84:87], a[124:127], v[212:215]\n"
"ds_read_b128 a[124:127], v13 offset:22528\n"
"v_mfma_f32_16x16x32_bf16 v[212:215], a[80:83], a[120:123], v[212:215]\n"
"ds_read_b128 a[120:123], v12 offset:22528\n"
"v_mfma_f32_16x16x32_bf16 v[216:219], a[80:83], a[128:131], v[216:219]\n"
"s_add_u32 s67, s75, 917504\n"
"ds_read_b64_tr_b16 a[50:51], v15 offset:8704\n"
"v_mfma_f32_16x16x32_bf16 v[216:219], a[84:87], a[132:135], v[216:219]\n"
"ds_read_b64_tr_b16 a[52:53], v15 offset:12288\n"
"v_mfma_f32_16x16x32_bf16 v[244:247], a[92:95], a[132:135], v[244:247]\n"
"ds_read_b128 a[132:135], v13 offset:24576\n"
"v_mfma_f32_16x16x32_bf16 v[244:247], a[88:91], a[128:131], v[244:247]\n"
"ds_read_b128 a[128:131], v12 offset:24576\n"
"v_mfma_f32_16x16x32_bf16 v[248:251], a[88:91], a[136:139], v[248:251]\n"
"ds_read_b64_tr_b16 a[54:55], v15 offset:12800\n"
"v_mfma_f32_16x16x32_bf16 v[248:251], a[92:95], a[140:143], v[248:251]\n"
"ds_read_b64_tr_b16 a[56:57], v15 offset:8200\n"
"v_mfma_f32_16x16x32_bf16 v[220:223], a[84:87], a[140:143], v[220:223]\n"
"ds_read_b128 a[140:143], v13 offset:26624\n"
"v_mfma_f32_16x16x32_bf16 v[220:223], a[80:83], a[136:139], v[220:223]\n"
"ds_read_b128 a[136:139], v12 offset:26624\n"
"v_mfma_f32_16x16x32_bf16 v[224:227], a[80:83], a[144:147], v[224:227]\n"
"ds_read_b64_tr_b16 a[58:59], v15 offset:8712\n"
"v_mfma_f32_16x16x32_bf16 v[224:227], a[84:87], a[148:151], v[224:227]\n"
"ds_read_b64_tr_b16 a[60:61], v15 offset:12296\n"
"v_mfma_f32_16x16x32_bf16 v[252:255], a[92:95], a[148:151], v[252:255]\n"
"ds_read_b128 a[148:151], v13 offset:28672\n"
"v_mfma_f32_16x16x32_bf16 v[252:255], a[88:91], a[144:147], v[252:255]\n"
"ds_read_b128 a[144:147], v12 offset:28672\n"
"ds_read_b64_tr_b16 a[62:63], v15 offset:12808\n"
"v_mfma_f32_16x16x32_bf16 a[28:31], a[88:91], a[152:155], a[28:31]\n"
"v_mfma_f32_16x16x32_bf16 a[28:31], a[92:95], a[156:159], a[28:31]\n"
"v_mfma_f32_16x16x32_bf16 a[24:27], a[84:87], a[156:159], a[24:27]\n"
"ds_read_b128 a[156:159], v13 offset:30720\n"
"v_mfma_f32_16x16x32_bf16 a[24:27], a[80:83], a[152:155], a[24:27]\n"
"s_waitcnt lgkmcnt(15)\n"
"ds_read_b128 a[152:155], v12 offset:30720\n"
"v_mfma_f32_16x16x32_bf16 v[32:35], a[32:35], a[96:99], v[32:35]\n"
"v_mfma_f32_16x16x32_bf16 v[32:35], a[36:39], a[100:103], v[32:35]\n"
"s_waitcnt lgkmcnt(2)\n"
"L_H6S:\n"
"v_mfma_f32_16x16x32_bf16 v[60:63], a[44:47], a[100:103], v[60:63]\n"
"buffer_load_dwordx4 v11, s[52:55], s67 offen offset:0  lds\n"
"v_mfma_f32_16x16x32_bf16 v[60:63], a[40:43], a[96:99], v[60:63]\n"
"v_mfma_f32_16x16x32_bf16 v[64:67], a[40:43], a[104:107], v[64:67]\n"
"s_add_u32 s67, s75, 932864\n"
"v_mfma_f32_16x16x32_bf16 v[64:67], a[44:47], a[108:111], v[64:67]\n"
"v_mfma_f32_16x16x32_bf16 v[36:39], a[36:39], a[108:111], v[36:39]\n"
"buffer_load_dwordx4 v11, s[52:55], s67 offen offset:1024  lds\n"
"v_mfma_f32_16x16x32_bf16 v[36:39], a[32:35], a[104:107], v[36:39]\n"
"v_mfma_f32_16x16x32_bf16 v[40:43], a[32:35], a[112:115], v[40:43]\n"
"s_add_u32 s67, s75, 948224\n"
"v_mfma_f32_16x16x32_bf16 v[40:43], a[36:39], a[116:119], v[40:43]\n"
"v_mfma_f32_16x16x32_bf16 v[68:71], a[44:47], a[116:119], v[68:71]\n"
"v_mfma_f32_16x16x32_bf16 v[68:71], a[40:43], a[112:115], v[68:71]\n"
"buffer_load_dwordx4 v11, s[52:55], s67 offen offset:2048  lds\n"
"v_mfma_f32_16x16x32_bf16 v[72:75], a[40:43], a[120:123], v[72:75]\n"
"v_mfma_f32_16x16x32_bf16 v[72:75], a[44:47], a[124:127], v[72:75]\n"
"s_add_u32 s67, s75, 963584\n"
"v_mfma_f32_16x16x32_bf16 v[44:47], a[36:39], a[124:127], v[44:47]\n"
"v_mfma_f32_16x16x32_bf16 v[44:47], a[32:35], a[120:123], v[44:47]\n"
"v_mfma_f32_16x16x32_bf16 v[48:51], a[32:35], a[128:131], v[48:51]\n"
"buffer_load_dwordx4 v11, s[52:55], s67 offen offset:3072  lds\n"
"v_mfma_f32_16x16x32_bf16 v[48:51], a[36:39], a[132:135], v[48:51]\n"
"s_add_u32 m0, s64, 12288\n"
"v_mfma_f32_16x16x32_bf16 v[76:79], a[44:47], a[132:135], v[76:79]\n"
"s_waitcnt vmcnt(8)\n"
"ds_read_b64_tr_b16 a[64:65], v9 offset:0\n"
"v_mfma_f32_16x16x32_bf16 v[76:79], a[40:43], a[128:131], v[76:79]\n"
"s_add_u32 s67, s75, 983040\n"
"ds_read_b64_tr_b16 a[66:67], v9 offset:512\n"
"v_mfma_f32_16x16x32_bf16 v[80:83], a[40:43], a[136:139], v[80:83]\n"
"ds_read_b64_tr_b16 a[68:69], v9 offset:4096\n"
"v_mfma_f32_16x16x32_bf16 v[80:83], a[44:47], a[140:143], v[80:83]\n"
"ds_read_b64_tr_b16 a[70:71], v9 offset:4608\n"
"buffer_load_dwordx4 v11, s[52:55], s67 offen offset:0  lds\n"
"v_mfma_f32_16x16x32_bf16 v[52:55], a[36:39], a[140:143], v[52:55]\n"
"s_add_u32 s67, s75, 998400\n"
"ds_read_b64_tr_b16 a[72:73], v9 offset:8\n"
"v_mfma_f32_16x16x32_bf16 v[52:55], a[32:35], a[136:139], v[52:55]\n"
"s_waitcnt lgkmcnt(5)\n"
"ds_read_b64_tr_b16 a[74:75], v9 offset:520\n"
"v_mfma_f32_16x16x32_bf16 v[56:59], a[32:35], a[144:147], v[56:59]\n"
"ds_read_b64_tr_b16 a[76:77], v9 offset:4104\n"
"v_mfma_f32_16x16x32_bf16 v[56:59], a[36:39], a[148:151], v[56:59]\n"
"ds_read_b64_tr_b16 a[78:79], v9 offset:4616\n"
"v_mfma_f32_16x16x32_bf16 v[84:87], a[44:47], a[148:151], v[84:87]\n"
"buffer_load_dwordx4 v11, s[52:55], s67 offen offset:1024  lds\n"
"v_mfma_f32_16x16x32_bf16 v[84:87], a[40:43], a[144:147], v[84:87]\n"
"ds_read_b64_tr_b16 a[80:81], v15 offset:0\n"
"v_mfma_f32_16x16x32_bf16 a[4:7], a[40:43], a[152:155], a[4:7]\n"
"s_add_u32 s67, s75, 1013760\n"
"ds_read_b64_tr_b16 a[82:83], v15 offset:512\n"
"v_mfma_f32_16x16x32_bf16 a[4:7], a[44:47], a[156:159], a[4:7]\n"
"ds_read_b64_tr_b16 a[84:85], v15 offset:4096\n"
"v_mfma_f32_16x16x32_bf16 a[0:3], a[36:39], a[156:159], a[0:3]\n"
"ds_read_b64_tr_b16 a[86:87], v15 offset:4608\n"
"buffer_load_dwordx4 v11, s[52:55], s67 offen offset:2048  lds\n"
"v_mfma_f32_16x16x32_bf16 a[0:3], a[32:35], a[152:155], a[0:3]\n"
"ds_read_b64_tr_b16 a[88:89], v15 offset:8\n"
"v_mfma_f32_16x16x32_bf16 v[88:91], a[48:51], a[96:99], v[88:91]\n"
"s_add_u32 s67, s75, 1029120\n"
"ds_read_b64_tr_b16 a[90:91], v15 offset:520\n"
"v_mfma_f32_16x16x32_bf16 v[88:91], a[52:55], a[100:103], v[88:91]\n"
"ds_read_b64_tr_b16 a[92:93], v15 offset:4104\n"
"v_mfma_f32_16x16x32_bf16 v[116:119], a[60:63], a[100:103], v[116:119]\n"
"ds_read_b64_tr_b16 a[94:95], v15 offset:4616\n"
"v_mfma_f32_16x16x32_bf16 v[116:119], a[56:59], a[96:99], v[116:119]\n"
"buffer_load_dwordx4 v11, s[52:55], s67 offen offset:3072  lds\n"
"v_mfma_f32_16x16x32_bf16 v[120:123], a[56:59], a[104:107], v[120:123]\n"
"v_mfma_f32_16x16x32_bf16 v[120:123], a[60:63], a[108:111], v[120:123]\n"
"s_add_u32 m0, s64, 0\n"
"v_mfma_f32_16x16x32_bf16 v[92:95], a[52:55], a[108:111], v[92:95]\n"
"v_mfma_f32_16x16x32_bf16 v[92:95], a[48:51], a[104:107], v[92:95]\n"
"s_add_u32 s67, s75, 917632\n"
"v_mfma_f32_16x16x32_bf16 v[96:99], a[48:51], a[112:115], v[96:99]\n"
"s_waitcnt lgkmcnt(0)\n"
"s_mov_b32 s99, 0\n"
"v_mfma_f32_16x16x32_bf16 v[96:99], a[52:55], a[116:119], v[96:99]\n"
"s_mov_b32 s98, 0\n"
"buffer_load_dwordx4 v11, s[52:55], s67 offen offset:0  lds\n"
"v_mfma_f32_16x16x32_bf16 v[124:127], a[60:63], a[116:119], v[124:127]\n"
"s_memtime s[76:77]\n"
"s_load_dword s98, s[96:97], 8192 glc\n"
"v_mfma_f32_16x16x32_bf16 v[124:127], a[56:59], a[112:115], v[124:127]\n"
"s_add_u32 s67, s75, 932992\n"
"v_mfma_f32_16x16x32_bf16 v[128:131], a[56:59], a[120:123], v[128:131]\n"
"v_mfma_f32_16x16x32_bf16 v[128:131], a[60:63], a[124:127], v[128:131]\n"
"buffer_load_dwordx4 v11, s[52:55], s67 offen offset:1024  lds\n"
"v_mfma_f32_16x16x32_bf16 v[100:103], a[52:55], a[124:127], v[100:103]\n"
"v_mfma_f32_16x16x32_bf16 v[100:103], a[48:51], a[120:123], v[100:103]\n"
"s_add_u32 s67, s75, 948352\n"
"v_mfma_f32_16x16x32_bf16 v[104:107], a[48:51], a[128:131], v[104:107]\n"
"v_mfma_f32_16x16x32_bf16 v[104:107], a[52:55], a[132:135], v[104:107]\n"
"v_mfma_f32_16x16x32_bf16 v[132:135], a[60:63], a[132:135], v[132:135]\n"
"buffer_load_dwordx4 v11, s[52:55], s67 offen offset:2048  lds\n"
"v_mfma_f32_16x16x32_bf16 v[132:135], a[56:59], a[128:131], v[132:135]\n"
"s_mov_b32 s29, s76\n"
"v_mfma_f32_16x16x32_bf16 v[136:139], a[56:59], a[136:139], v[136:139]\n"
"s_add_u32 s67, s75, 963712\n"
"v_mfma_f32_16x16x32_bf16 v[136:139], a[60:63], a[140:143], v[136:139]\n"
"buffer_load_dwordx4 v11, s[52:55], s67 offen offset:3072  lds\n"
"v_mfma_f32_16x16x32_bf16 v[108:111], a[52:55], a[140:143], v[108:111]\n"
"s_sub_u32 s99, s99, s98\n"
"s_sub_u32 s99, s99, s98\n"
"v_mfma_f32_16x16x32_bf16 v[108:111], a[48:51], a[136:139], v[108:111]\n"
"s_sub_u32 s99, s99, s98\n"
"s_add_u32 m0, s64, 4096\n"
"v_mfma_f32_16x16x32_bf16 v[112:115], a[48:51], a[144:147], v[112:115]\n"
"s_sub_u32 s99, s99, s98\n"
"v_mfma_f32_16x16x32_bf16 v[112:115], a[52:55], a[148:151], v[112:115]\n"
"s_add_u32 s67, s75, 983168\n"
"v_mfma_f32_16x16x32_bf16 v[140:143], a[60:63], a[148:151], v[140:143]\n"
"s_sub_u32 s99, s99, s98\n"
"s_sub_u32 s99, s99, s98\n"
"v_mfma_f32_16x16x32_bf16 v[140:143], a[56:59], a[144:147], v[140:143]\n"
"s_sub_u32 s99, s99, s98\n"
"buffer_load_dwordx4 v11, s[52:55], s67 offen offset:0  lds\n"
"v_mfma_f32_16x16x32_bf16 a[12:15], a[56:59], a[152:155], a[12:15]\n"
"s_sub_u32 s99, s99, s98\n"
"v_mfma_f32_16x16x32_bf16 a[12:15], a[60:63], a[156:159], a[12:15]\n"
"s_add_u32 s67, s75, 998528\n"
"v_mfma_f32_16x16x32_bf16 a[8:11], a[52:55], a[156:159], a[8:11]\n"
"s_sub_u32 s99, s99, s98\n"
"s_sub_u32 s99, s99, s98\n"
"v_mfma_f32_16x16x32_bf16 a[8:11], a[48:51], a[152:155], a[8:11]\n"
"s_sub_u32 s99, s99, s98\n"
"v_mfma_f32_16x16x32_bf16 v[144:147], a[64:67], a[96:99], v[144:147]\n"
"s_sub_u32 s99, s99, s98\n"
"v_mfma_f32_16x16x32_bf16 v[144:147], a[68:71], a[100:103], v[144:147]\n"
"buffer_load_dwordx4 v11, s[52:55], s67 offen offset:1024  lds\n"
"v_mfma_f32_16x16x32_bf16 v[172:175], a[76:79], a[100:103], v[172:175]\n"
"s_sub_u32 s99, s99, s98\n"
"s_sub_u32 s99, s99, s98\n"
"v_mfma_f32_16x16x32_bf16 v[172:175], a[72:75], a[96:99], v[172:175]\n"
"s_sub_u32 s99, s99, s98\n"
"s_add_u32 s67, s75, 1013888\n"
"v_mfma_f32_16x16x32_bf16 v[176:179], a[72:75], a[104:107], v[176:179]\n"
"s_sub_u32 s99, s99, s98\n"
"v_mfma_f32_16x16x32_bf16 v[176:179], a[76:79], a[108:111], v[176:179]\n"
"buffer_load_dwordx4 v11, s[52:55], s67 offen offset:2048  lds\n"
"v_mfma_f32_16x16x32_bf16 v[148:151], a[68:71], a[108:111], v[148:151]\n"
"s_sub_u32 s99, s99, s98\n"
"s_sub_u32 s99, s99, s98\n"
"v_mfma_f32_16x16x32_bf16 v[148:151], a[64:67], a[104:107], v[148:151]\n"
"s_sub_u32 s99, s99, s98\n"
"s_add_u32 s67, s75, 1029248\n"
"v_mfma_f32_16x16x32_bf16 v[152:155], a[64:67], a[112:115], v[152:155]\n"
"s_sub_u32 s66, 122880, s73\n"
"s_sub_u32 s99, s99, s98\n"
"v_mfma_f32_16x16x32_bf16 v[152:155], a[68:71], a[116:119], v[152:155]\n"
"buffer_load_dwordx4 v11, s[52:55], s67 offen offset:3072  lds\n"
"v_mfma_f32_16x16x32_bf16 v[180:183], a[76:79], a[116:119], v[180:183]\n"
"s_sub_u32 s99, s99, s98\n"
"v_add_u32 v12, s66, v7\n"
"s_sub_u32 s99, s99, s98\n"
"v_mfma_f32_16x16x32_bf16 v[180:183], a[72:75], a[112:115], v[180:183]\n"
"s_sub_u32 s99, s99, s98\n"
"s_cmp_lt_u32 %16, 2\n"
"s_cselect_b32 s67, 16384, 0\n"
"v_mfma_f32_16x16x32_bf16 v[184:187], a[72:75], a[120:123], v[184:187]\n"
"s_sub_u32 s99, s99, s98\n"
"v_add_u32 v13, s66, v8\n"
"s_add_u32 s67, s67, s72\n"
"v_mfma_f32_16x16x32_bf16 v[184:187], a[76:79], a[124:127], v[184:187]\n"
"s_memtime s[76:77]\n"
"s_add_u32 m0, s67, 16384\n"
"s_sub_u32 s67, 1024, s60\n"
"v_mfma_f32_16x16x32_bf16 v[156:159], a[68:71], a[124:127], v[156:159]\n"
"buffer_load_dwordx4 v6, s[48:51], s67 offen offset:0  lds\n"
"v_mfma_f32_16x16x32_bf16 v[156:159], a[64:67], a[120:123], v[156:159]\n"
"s_waitcnt lgkmcnt(0)\n"
"v_mfma_f32_16x16x32_bf16 v[160:163], a[64:67], a[128:131], v[160:163]\n"
"s_sub_u32 s29, s76, s29\n"
"s_sub_u32 s67, 16384, s60\n"
"v_mfma_f32_16x16x32_bf16 v[160:163], a[68:71], a[132:135], v[160:163]\n"
"s_cmp_lt_u32 s29, 620\n"
"s_cselect_b32 s99, s99, 0\n"
"v_mfma_f32_16x16x32_bf16 v[188:191], a[76:79], a[132:135], v[188:191]\n"
"s_max_u32 s31, s31, s99\n"
"v_mfma_f32_16x16x32_bf16 v[188:191], a[72:75], a[128:131], v[188:191]\n"
"buffer_load_dwordx4 v6, s[48:51], s67 offen offset:1024  lds\n"
"v_mfma_f32_16x16x32_bf16 v[192:195], a[72:75], a[136:139], v[192:195]\n"
"v_mfma_f32_16x16x32_bf16 v[192:195], a[76:79], a[140:143], v[192:195]\n"
"s_sub_u32 s67, 31744, s60\n"
"v_mfma_f32_16x16x32_bf16 v[164:167], a[68:71], a[140:143], v[164:167]\n"
"v_mfma_f32_16x16x32_bf16 v[164:167], a[64:67], a[136:139], v[164:167]\n"
"buffer_load_dwordx4 v6, s[48:51], s67 offen offset:2048  lds\n"
"v_mfma_f32_16x16x32_bf16 v[168:171], a[64:67], a[144:147], v[168:171]\n"
"v_mfma_f32_16x16x32_bf16 v[168:171], a[68:71], a[148:151], v[168:171]\n"
"s_sub_u32 s67, 47104, s60\n"
"v_mfma_f32_16x16x32_bf16 v[196:199], a[76:79], a[148:151], v[196:199]\n"
"v_mfma_f32_16x16x32_bf16 v[196:199], a[72:75], a[144:147], v[196:199]\n"
"buffer_load_dwordx4 v6, s[48:51], s67 offen offset:3072  lds\n"
"v_mfma_f32_16x16x32_bf16 a[20:23], a[72:75], a[152:155], a[20:23]\n"
"v_mfma_f32_16x16x32_bf16 a[20:23], a[76:79], a[156:159], a[20:23]\n"
"s_add_u32 m0, s72, 57216\n"
"v_mfma_f32_16x16x32_bf16 a[16:19], a[68:71], a[156:159], a[16:19]\n"
"v_mfma_f32_16x16x32_bf16 a[16:19], a[64:67], a[152:155], a[16:19]\n"
"s_waitcnt vmcnt(20)\n"
"s_barrier\n"
"v_mfma_f32_16x16x32_bf16 v[200:203], a[80:83], a[96:99], v[200:203]\n"
"s_waitcnt vmcnt(12)\n"
"ds_read_b64_tr_b16 a[32:33], v9 offset:8192\n"
"buffer_load_dwordx4 v6, s[40:43], s80 offen offset:128  lds\n"
"v_mfma_f32_16x16x32_bf16 v[200:203], a[84:87], a[100:103], v[200:203]\n"
"ds_read_b64_tr_b16 a[34:35], v9 offset:8704\n"
"v_mfma_f32_16x16x32_bf16 v[228:231], a[92:95], a[100:103], v[228:231]\n"
"ds_read_b128 a[100:103], v13 offset:16384\n"
"v_mfma_f32_16x16x32_bf16 v[228:231], a[88:91], a[96:99], v[228:231]\n"
"ds_read_b128 a[96:99], v12 offset:16384\n"
"buffer_load_dwordx4 v6, s[40:43], s81 offen offset:1152  lds\n"
"v_mfma_f32_16x16x32_bf16 v[232:235], a[88:91], a[104:107], v[232:235]\n"
"ds_read_b64_tr_b16 a[36:37], v9 offset:12288\n"
"v_mfma_f32_16x16x32_bf16 v[232:235], a[92:95], a[108:111], v[232:235]\n"
"ds_read_b64_tr_b16 a[38:39], v9 offset:12800\n"
"v_mfma_f32_16x16x32_bf16 v[204:207], a[84:87], a[108:111], v[204:207]\n"
"ds_read_b128 a[108:111], v13 offset:18432\n"
"v_mfma_f32_16x16x32_bf16 v[204:207], a[80:83], a[104:107], v[204:207]\n"
"ds_read_b128 a[104:107], v12 offset:18432\n"
"ds_read_b64_tr_b16 a[40:41], v9 offset:8200\n"
"v_mfma_f32_16x16x32_bf16 v[208:211], a[80:83], a[112:115], v[208:211]\n"
"ds_read_b64_tr_b16 a[42:43], v9 offset:8712\n"
"buffer_load_dwordx4 v6, s[40:43], s82 offen offset:2176  lds\n"
"v_mfma_f32_16x16x32_bf16 v[208:211], a[84:87], a[116:119], v[208:211]\n"
"ds_read_b64_tr_b16 a[44:45], v9 offset:12296\n"
"v_mfma_f32_16x16x32_bf16 v[236:239], a[92:95], a[116:119], v[236:239]\n"
"ds_read_b128 a[116:119], v13 offset:20480\n"
"v_mfma_f32_16x16x32_bf16 v[236:239], a[88:91], a[112:115], v[236:239]\n"
"ds_read_b128 a[112:115], v12 offset:20480\n"
"v_mfma_f32_16x16x32_bf16 v[240:243], a[88:91], a[120:123], v[240:243]\n"
"ds_read_b64_tr_b16 a[46:47], v9 offset:12808\n"
"v_mfma_f32_16x16x32_bf16 v[240:243], a[92:95], a[124:127], v[240:243]\n"
"ds_read_b64_tr_b16 a[48:49], v15 offset:8192\n"
"buffer_load_dwordx4 v6, s[40:43], s83 offen offset:3200  lds\n"
"v_mfma_f32_16x16x32_bf16 v[212:215], a[84:87], a[124:127], v[212:215]\n"
"ds_read_b128 a[124:127], v13 offset:22528\n"
"v_mfma_f32_16x16x32_bf16 v[212:215], a[80:83], a[120:123], v[212:215]\n"
"ds_read_b128 a[120:123], v12 offset:22528\n"
"v_mfma_f32_16x16x32_bf16 v[216:219], a[80:83], a[128:131], v[216:219]\n"
"ds_read_b64_tr_b16 a[50:51], v15 offset:8704\n"
"v_mfma_f32_16x16x32_bf16 v[216:219], a[84:87], a[132:135], v[216:219]\n"
"s_add_u32 m0, s64, 8192\n"
"ds_read_b64_tr_b16 a[52:53], v15 offset:12288\n"
"v_mfma_f32_16x16x32_bf16 v[244:247], a[92:95], a[132:135], v[244:247]\n"
"ds_read_b128 a[132:135], v13 offset:24576\n"
"v_mfma_f32_16x16x32_bf16 v[244:247], a[88:91], a[128:131], v[244:247]\n"
"ds_read_b128 a[128:131], v12 offset:24576\n"
"v_mfma_f32_16x16x32_bf16 v[248:251], a[88:91], a[136:139], v[248:251]\n"
"ds_read_b64_tr_b16 a[54:55], v15 offset:12800\n"
"v_mfma_f32_16x16x32_bf16 v[248:251], a[92:95], a[140:143], v[248:251]\n"
"ds_read_b64_tr_b16 a[56:57], v15 offset:8200\n"
"v_mfma_f32_16x16x32_bf16 v[220:223], a[84:87], a[140:143], v[220:223]\n"
"ds_read_b128 a[140:143], v13 offset:26624\n"
"v_mfma_f32_16x16x32_bf16 v[220:223], a[80:83], a[136:139], v[220:223]\n"
"ds_read_b128 a[136:139], v12 offset:26624\n"
"v_mfma_f32_16x16x32_bf16 v[224:227], a[80:83], a[144:147], v[224:227]\n"
"ds_read_b64_tr_b16 a[58:59], v15 offset:8712\n"
"v_mfma_f32_16x16x32_bf16 v[224:227], a[84:87], a[148:151], v[224:227]\n"
"s_sub_u32 s67, 1048576, s75\n"
"ds_read_b64_tr_b16 a[60:61], v15 offset:12296\n"
"v_mfma_f32_16x16x32_bf16 v[252:255], a[92:95], a[148:151], v[252:255]\n"
"ds_read_b128 a[148:151], v13 offset:28672\n"
"v_mfma_f32_16x16x32_bf16 v[252:255], a[88:91], a[144:147], v[252:255]\n"
"ds_read_b128 a[144:147], v12 offset:28672\n"
"ds_read_b64_tr_b16 a[62:63], v15 offset:12808\n"
"v_mfma_f32_16x16x32_bf16 a[28:31], a[88:91], a[152:155], a[28:31]\n"
"v_mfma_f32_16x16x32_bf16 a[28:31], a[92:95], a[156:159], a[28:31]\n"
"v_mfma_f32_16x16x32_bf16 a[24:27], a[84:87], a[156:159], a[24:27]\n"
"ds_read_b128 a[156:159], v13 offset:30720\n"
"v_mfma_f32_16x16x32_bf16 a[24:27], a[80:83], a[152:155], a[24:27]\n"
"s_waitcnt lgkmcnt(15)\n"
"ds_read_b128 a[152:155], v12 offset:30720\n"
"v_mfma_f32_16x16x32_bf16 v[32:35], a[32:35], a[96:99], v[32:35]\n"
"v_mfma_f32_16x16x32_bf16 v[32:35], a[36:39], a[100:103], v[32:35]\n"
"s_waitcnt lgkmcnt(2)\n"
"v_mfma_f32_16x16x32_bf16 v[60:63], a[44:47], a[100:103], v[60:63]\n"
"buffer_load_dwordx4 v11, s[52:55], s67 offen offset:0  lds\n"
"v_mfma_f32_16x16x32_bf16 v[60:63], a[40:43], a[96:99], v[60:63]\n"
"v_mfma_f32_16x16x32_bf16 v[64:67], a[40:43], a[104:107], v[64:67]\n"
"s_sub_u32 s67, 1063936, s75\n"
"v_mfma_f32_16x16x32_bf16 v[64:67], a[44:47], a[108:111], v[64:67]\n"
"v_mfma_f32_16x16x32_bf16 v[36:39], a[36:39], a[108:111], v[36:39]\n"
"v_mfma_f32_16x16x32_bf16 v[36:39], a[32:35], a[104:107], v[36:39]\n"
"buffer_load_dwordx4 v11, s[52:55], s67 offen offset:1024  lds\n"
"v_mfma_f32_16x16x32_bf16 v[40:43], a[32:35], a[112:115], v[40:43]\n"
"v_mfma_f32_16x16x32_bf16 v[40:43], a[36:39], a[116:119], v[40:43]\n"
"s_sub_u32 s67, 1079296, s75\n"
"v_mfma_f32_16x16x32_bf16 v[68:71], a[44:47], a[116:119], v[68:71]\n"
"v_mfma_f32_16x16x32_bf16 v[68:71], a[40:43], a[112:115], v[68:71]\n"
"buffer_load_dwordx4 v11, s[52:55], s67 offen offset:2048  lds\n"
"v_mfma_f32_16x16x32_bf16 v[72:75], a[40:43], a[120:123], v[72:75]\n"
"v_mfma_f32_16x16x32_bf16 v[72:75], a[44:47], a[124:127], v[72:75]\n"
"s_sub_u32 s67, 1094656, s75\n"
"v_mfma_f32_16x16x32_bf16 v[44:47], a[36:39], a[124:127], v[44:47]\n"
"v_mfma_f32_16x16x32_bf16 v[44:47], a[32:35], a[120:123], v[44:47]\n"
"v_mfma_f32_16x16x32_bf16 v[48:51], a[32:35], a[128:131], v[48:51]\n"
"buffer_load_dwordx4 v11, s[52:55], s67 offen offset:3072  lds\n"
"v_mfma_f32_16x16x32_bf16 v[48:51], a[36:39], a[132:135], v[48:51]\n"
"s_add_u32 m0, s64, 12288\n"
"v_mfma_f32_16x16x32_bf16 v[76:79], a[44:47], a[132:135], v[76:79]\n"
"L_st2117:\n"
"s_cbranch_execz L_st4475\n"
"s_waitcnt vmcnt(12)\n"
"ds_read_b64_tr_b16 a[64:65], v9 offset:0\n"
"v_mfma_f32_16x16x32_bf16 v[76:79], a[40:43], a[128:131], v[76:79]\n"
"ds_read_b64_tr_b16 a[66:67], v9 offset:512\n"
"v_mfma_f32_16x16x32_bf16 v[80:83], a[40:43], a[136:139], v[80:83]\n"
"s_sub_u32 s67, 1114112, s75\n"
"ds_read_b64_tr_b16 a[68:69], v9 offset:4096\n"
"v_mfma_f32_16x16x32_bf16 v[80:83], a[44:47], a[140:143], v[80:83]\n"
"ds_read_b64_tr_b16 a[70:71], v9 offset:4608\n"
"v_mfma_f32_16x16x32_bf16 v[52:55], a[36:39], a[140:143], v[52:55]\n"
"ds_read_b64_tr_b16 a[72:73], v9 offset:8\n"
"buffer_load_dwordx4 v11, s[52:55], s67 offen offset:0  lds\n"
"v_mfma_f32_16x16x32_bf16 v[52:55], a[32:35], a[136:139], v[52:55]\n"
"s_waitcnt lgkmcnt(5)\n"
"ds_read_b64_tr_b16 a[74:75], v9 offset:520\n"
"v_mfma_f32_16x16x32_bf16 v[56:59], a[32:35], a[144:147], v[56:59]\n"
"s_sub_u32 s67, 1129472, s75\n"
"ds_read_b64_tr_b16 a[76:77], v9 offset:4104\n"
"v_mfma_f32_16x16x32_bf16 v[56:59], a[36:39], a[148:151], v[56:59]\n"
"ds_read_b64_tr_b16 a[78:79], v9 offset:4616\n"
"v_mfma_f32_16x16x32_bf16 v[84:87], a[44:47], a[148:151], v[84:87]\n"
"buffer_load_dwordx4 v11, s[52:55], s67 offen offset:1024  lds\n"
"v_mfma_f32_16x16x32_bf16 v[84:87], a[40:43], a[144:147], v[84:87]\n"
"ds_read_b64_tr_b16 a[80:81], v15 offset:0\n"
"v_mfma_f32_16x16x32_bf16 a[4:7], a[40:43], a[152:155], a[4:7]\n"
"s_sub_u32 s67, 1144832, s75\n"
"ds_read_b64_tr_b16 a[82:83], v15 offset:512\n"
"v_mfma_f32_16x16x32_bf16 a[4:7], a[44:47], a[156:159], a[4:7]\n"
"ds_read_b64_tr_b16 a[84:85], v15 offset:4096\n"
"v_mfma_f32_16x16x32_bf16 a[0:3], a[36:39], a[156:159], a[0:3]\n"
"ds_read_b64_tr_b16 a[86:87], v15 offset:4608\n"
"buffer_load_dwordx4 v11, s[52:55], s67 offen offset:2048  lds\n"
"v_mfma_f32_16x16x32_bf16 a[0:3], a[32:35], a[152:155], a[0:3]\n"
"ds_read_b64_tr_b16 a[88:89], v15 offset:8\n"
"v_mfma_f32_16x16x32_bf16 v[88:91], a[48:51], a[96:99], v[88:91]\n"
"s_sub_u32 s67, 1160192, s75\n"
"ds_read_b64_tr_b16 a[90:91], v15 offset:520\n"
"v_mfma_f32_16x16x32_bf16 v[88:91], a[52:55], a[100:103], v[88:91]\n"
"ds_read_b64_tr_b16 a[92:93], v15 offset:4104\n"
"v_mfma_f32_16x16x32_bf16 v[116:119], a[60:63], a[100:103], v[116:119]\n"
"ds_read_b64_tr_b16 a[94:95], v15 offset:4616\n"
"v_mfma_f32_16x16x32_bf16 v[116:119], a[56:59], a[96:99], v[116:119]\n"
"buffer_load_dwordx4 v11, s[52:55], s67 offen offset:3072  lds\n"
"v_mfma_f32_16x16x32_bf16 v[120:123], a[56:59], a[104:107], v[120:123]\n"
"v_mfma_f32_16x16x32_bf16 v[120:123], a[60:63], a[108:111], v[120:123]\n"
"s_add_u32 m0, s64, 0\n"
"v_mfma_f32_16x16x32_bf16 v[92:95], a[52:55], a[108:111], v[92:95]\n"
"v_mfma_f32_16x16x32_bf16 v[92:95], a[48:51], a[104:107], v[92:95]\n"
"s_sub_u32 s67, 1048704, s75\n"
"v_mfma_f32_16x16x32_bf16 v[96:99], a[48:51], a[112:115], v[96:99]\n"
"s_waitcnt lgkmcnt(0)\n"
"v_mfma_f32_16x16x32_bf16 v[96:99], a[52:55], a[116:119], v[96:99]\n"
"buffer_load_dwordx4 v11, s[52:55], s67 offen offset:0  lds\n"
"v_mfma_f32_16x16x32_bf16 v[124:127], a[60:63], a[116:119], v[124:127]\n"
"v_mfma_f32_16x16x32_bf16 v[124:127], a[56:59], a[112:115], v[124:127]\n"
"s_sub_u32 s67, 1064064, s75\n"
"v_mfma_f32_16x16x32_bf16 v[128:131], a[56:59], a[120:123], v[128:131]\n"
"v_mfma_f32_16x16x32_bf16 v[128:131], a[60:63], a[124:127], v[128:131]\n"
"v_mfma_f32_16x16x32_bf16 v[100:103], a[52:55], a[124:127], v[100:103]\n"
"buffer_load_dwordx4 v11, s[52:55], s67 offen offset:1024  lds\n"
"v_mfma_f32_16x16x32_bf16 v[100:103], a[48:51], a[120:123], v[100:103]\n"
"v_mfma_f32_16x16x32_bf16 v[104:107], a[48:51], a[128:131], v[104:107]\n"
"s_sub_u32 s67, 1079424, s75\n"
"v_mfma_f32_16x16x32_bf16 v[104:107], a[52:55], a[132:135], v[104:107]\n"
"v_mfma_f32_16x16x32_bf16 v[132:135], a[60:63], a[132:135], v[132:135]\n"
"buffer_load_dwordx4 v11, s[52:55], s67 offen offset:2048  lds\n"
"v_mfma_f32_16x16x32_bf16 v[132:135], a[56:59], a[128:131], v[132:135]\n"
"v_mfma_f32_16x16x32_bf16 v[136:139], a[56:59], a[136:139], v[136:139]\n"
"s_sub_u32 s67, 1094784, s75\n"
"v_mfma_f32_16x16x32_bf16 v[136:139], a[60:63], a[140:143], v[136:139]\n"
"v_mfma_f32_16x16x32_bf16 v[108:111], a[52:55], a[140:143], v[108:111]\n"
"v_mfma_f32_16x16x32_bf16 v[108:111], a[48:51], a[136:139], v[108:111]\n"
"buffer_load_dwordx4 v11, s[52:55], s67 offen offset:3072  lds\n"
"v_mfma_f32_16x16x32_bf16 v[112:115], a[48:51], a[144:147], v[112:115]\n"
"s_add_u32 m0, s64, 4096\n"
"v_mfma_f32_16x16x32_bf16 v[112:115], a[52:55], a[148:151], v[112:115]\n"
"v_mfma_f32_16x16x32_bf16 v[140:143], a[60:63], a[148:151], v[140:143]\n"
"s_sub_u32 s67, 1114240, s75\n"
"v_mfma_f32_16x16x32_bf16 v[140:143], a[56:59], a[144:147], v[140:143]\n"
"v_mfma_f32_16x16x32_bf16 a[12:15], a[56:59], a[152:155], a[12:15]\n"
"buffer_load_dwordx4 v11, s[52:55], s67 offen offset:0  lds\n"
"v_mfma_f32_16x16x32_bf16 a[12:15], a[60:63], a[156:159], a[12:15]\n"
"v_mfma_f32_16x16x32_bf16 a[8:11], a[52:55], a[156:159], a[8:11]\n"
"s_sub_u32 s67, 1129600, s75\n"
"v_mfma_f32_16x16x32_bf16 a[8:11], a[48:51], a[152:155], a[8:11]\n"
"v_mfma_f32_16x16x32_bf16 v[144:147], a[64:67], a[96:99], v[144:147]\n"
"buffer_load_dwordx4 v11, s[52:55], s67 offen offset:1024  lds\n"
"v_mfma_f32_16x16x32_bf16 v[144:147], a[68:71], a[100:103], v[144:147]\n"
"v_mfma_f32_16x16x32_bf16 v[172:175], a[76:79], a[100:103], v[172:175]\n"
"s_sub_u32 s67, 1144960, s75\n"
"v_mfma_f32_16x16x32_bf16 v[172:175], a[72:75], a[96:99], v[172:175]\n"
"v_mfma_f32_16x16x32_bf16 v[176:179], a[72:75], a[104:107], v[176:179]\n"
"v_mfma_f32_16x16x32_bf16 v[176:179], a[76:79], a[108:111], v[176:179]\n"
"s_sub_u32 s71, s72, s64\n"
"v_mfma_f32_16x16x32_bf16 v[148:151], a[68:71], a[108:111], v[148:151]\n"
"buffer_load_dwordx4 v11, s[52:55], s67 offen offset:2048  lds\n"
"v_mfma_f32_16x16x32_bf16 v[148:151], a[64:67], a[104:107], v[148:151]\n"
"s_sub_u32 s67, 1160320, s75\n"
"v_mfma_f32_16x16x32_bf16 v[152:155], a[64:67], a[112:115], v[152:155]\n"
"s_sub_u32 s66, 0, s73\n"
"s_add_u32 s71, s71, 57344\n"
"v_mfma_f32_16x16x32_bf16 v[152:155], a[68:71], a[116:119], v[152:155]\n"
"buffer_load_dwordx4 v11, s[52:55], s67 offen offset:3072  lds\n"
"v_mfma_f32_16x16x32_bf16 v[180:183], a[76:79], a[116:119], v[180:183]\n"
"v_add_u32 v12, s66, v7\n"
"v_mfma_f32_16x16x32_bf16 v[180:183], a[72:75], a[112:115], v[180:183]\n"
"v_add_u32 v20, s71, v7\n"
"v_mfma_f32_16x16x32_bf16 v[184:187], a[72:75], a[120:123], v[184:187]\n"
"v_add_u32 v13, s66, v8\n"
"v_mfma_f32_16x16x32_bf16 v[184:187], a[76:79], a[124:127], v[184:187]\n"
"v_add_u32 v21, s71, v8\n"
"v_mfma_f32_16x16x32_bf16 v[156:159], a[68:71], a[124:127], v[156:159]\n"
"s_cmp_lt_u32 %16, 2\n"
"s_cselect_b32 s67, 16384, 0\n"
"v_mfma_f32_16x16x32_bf16 v[156:159], a[64:67], a[120:123], v[156:159]\n"
"s_add_u32 s67, s67, s72\n"
"v_mfma_f32_16x16x32_bf16 v[160:163], a[64:67], a[128:131], v[160:163]\n"
"s_waitcnt vmcnt(16)\n"
"ds_read_b128 a[192:195], v20 offset:0\n"
"v_mfma_f32_16x16x32_bf16 v[160:163], a[68:71], a[132:135], v[160:163]\n"
"v_mfma_f32_16x16x32_bf16 v[188:191], a[76:79], a[132:135], v[188:191]\n"
"ds_read_b128 a[196:199], v21 offset:0\n"
"v_mfma_f32_16x16x32_bf16 v[188:191], a[72:75], a[128:131], v[188:191]\n"
"ds_read_b128 a[200:203], v20 offset:2048\n"
"v_mfma_f32_16x16x32_bf16 v[192:195], a[72:75], a[136:139], v[192:195]\n"
"s_add_u32 m0, s67, 57344\n"
"v_mfma_f32_16x16x32_bf16 v[192:195], a[76:79], a[140:143], v[192:195]\n"
"ds_read_b128 a[204:207], v21 offset:2048\n"
"v_mfma_f32_16x16x32_bf16 v[164:167], a[68:71], a[140:143], v[164:167]\n"
"v_mfma_f32_16x16x32_bf16 v[164:167], a[64:67], a[136:139], v[164:167]\n"
"v_mfma_f32_16x16x32_bf16 v[168:171], a[64:67], a[144:147], v[168:171]\n"
"s_sub_u32 s67, 1280, s60\n"
"v_mfma_f32_16x16x32_bf16 v[168:171], a[68:71], a[148:151], v[168:171]\n"
"v_mfma_f32_16x16x32_bf16 v[196:199], a[76:79], a[148:151], v[196:199]\n"
"v_mfma_f32_16x16x32_bf16 v[196:199], a[72:75], a[144:147], v[196:199]\n"
"s_waitcnt lgkmcnt(0)\n"
"v_mfma_f32_16x16x32_bf16 a[20:23], a[72:75], a[152:155], a[20:23]\n"
"buffer_load_dwordx4 v6, s[48:51], s67 offen offset:0  lds\n"
"v_mfma_f32_16x16x32_bf16 a[20:23], a[76:79], a[156:159], a[20:23]\n"
"v_mfma_f32_16x16x32_bf16 a[16:19], a[68:71], a[156:159], a[16:19]\n"
"s_sub_u32 s67, 16640, s60\n"
"v_mfma_f32_16x16x32_bf16 a[16:19], a[64:67], a[152:155], a[16:19]\n"
"s_barrier\n"
"s_waitcnt vmcnt(9)\n"
"ds_read_b64_tr_b16 a[32:33], v9 offset:8192\n"
"v_mfma_f32_16x16x32_bf16 v[200:203], a[80:83], a[96:99], v[200:203]\n"
"ds_read_b64_tr_b16 a[34:35], v9 offset:8704\n"
"buffer_load_dwordx4 v6, s[48:51], s67 offen offset:1024  lds\n"
"v_mfma_f32_16x16x32_bf16 v[200:203], a[84:87], a[100:103], v[200:203]\n"
"ds_read_b64_tr_b16 a[36:37], v9 offset:12288\n"
"v_mfma_f32_16x16x32_bf16 v[228:231], a[92:95], a[100:103], v[228:231]\n"
"s_sub_u32 s67, 32000, s60\n"
"ds_read_b128 a[100:103], v13 offset:32768\n"
"v_mfma_f32_16x16x32_bf16 v[228:231], a[88:91], a[96:99], v[228:231]\n"
"ds_read_b128 a[96:99], v12 offset:32768\n"
"v_mfma_f32_16x16x32_bf16 v[232:235], a[88:91], a[104:107], v[232:235]\n"
"ds_read_b64_tr_b16 a[38:39], v9 offset:12800\n"
"v_mfma_f32_16x16x32_bf16 v[232:235], a[92:95], a[108:111], v[232:235]\n"
"ds_read_b64_tr_b16 a[40:41], v9 offset:8200\n"
"buffer_load_dwordx4 v6, s[48:51], s67 offen offset:2048  lds\n"
"v_mfma_f32_16x16x32_bf16 v[204:207], a[84:87], a[108:111], v[204:207]\n"
"ds_read_b128 a[108:111], v13 offset:34816\n"
"v_mfma_f32_16x16x32_bf16 v[204:207], a[80:83], a[104:107], v[204:207]\n"
"s_sub_u32 s67, 47360, s60\n"
"ds_read_b128 a[104:107], v12 offset:34816\n"
"v_mfma_f32_16x16x32_bf16 v[208:211], a[80:83], a[112:115], v[208:211]\n"
"ds_read_b64_tr_b16 a[42:43], v9 offset:8712\n"
"v_mfma_f32_16x16x32_bf16 v[208:211], a[84:87], a[116:119], v[208:211]\n"
"ds_read_b64_tr_b16 a[44:45], v9 offset:12296\n"
"v_mfma_f32_16x16x32_bf16 v[236:239], a[92:95], a[116:119], v[236:239]\n"
"ds_read_b128 a[116:119], v13 offset:36864\n"
"buffer_load_dwordx4 v6, s[48:51], s67 offen offset:3072  lds\n"
"v_mfma_f32_16x16x32_bf16 v[236:239], a[88:91], a[112:115], v[236:239]\n"
"ds_read_b128 a[112:115], v12 offset:36864\n"
"v_mfma_f32_16x16x32_bf16 v[240:243], a[88:91], a[120:123], v[240:243]\n"
"s_add_u32 m0, s64, 16256\n"
"ds_read_b64_tr_b16 a[46:47], v9 offset:12808\n"
"v_mfma_f32_16x16x32_bf16 v[240:243], a[92:95], a[124:127], v[240:243]\n"
"ds_read_b64_tr_b16 a[48:49], v15 offset:8192\n"
"v_mfma_f32_16x16x32_bf16 v[212:215], a[84:87], a[124:127], v[212:215]\n"
"ds_read_b128 a[124:127], v13 offset:38912\n"
"buffer_load_dwordx4 v6, s[40:43], s84 offen offset:128  lds\n"
"v_mfma_f32_16x16x32_bf16 v[212:215], a[80:83], a[120:123], v[212:215]\n"
"ds_read_b128 a[120:123], v12 offset:38912\n"
"v_mfma_f32_16x16x32_bf16 v[216:219], a[80:83], a[128:131], v[216:219]\n"
"ds_read_b64_tr_b16 a[50:51], v15 offset:8704\n"
"v_mfma_f32_16x16x32_bf16 v[216:219], a[84:87], a[132:135], v[216:219]\n"
"ds_read_b64_tr_b16 a[52:53], v15 offset:12288\n"
"v_mfma_f32_16x16x32_bf16 v[244:247], a[92:95], a[132:135], v[244:247]\n"
"ds_read_b128 a[132:135], v13 offset:24576\n"
"buffer_load_dwordx4 v6, s[40:43], s85 offen offset:1152  lds\n"
"v_mfma_f32_16x16x32_bf16 v[244:247], a[88:91], a[128:131], v[244:247]\n"
"ds_read_b128 a[128:131], v12 offset:24576\n"
"v_mfma_f32_16x16x32_bf16 v[248:251], a[88:91], a[136:139], v[248:251]\n"
"ds_read_b64_tr_b16 a[54:55], v15 offset:12800\n"
"v_mfma_f32_16x16x32_bf16 v[248:251], a[92:95], a[140:143], v[248:251]\n"
"ds_read_b64_tr_b16 a[56:57], v15 offset:8200\n"
"v_mfma_f32_16x16x32_bf16 v[220:223], a[84:87], a[140:143], v[220:223]\n"
"ds_read_b128 a[140:143], v13 offset:26624\n"
"buffer_load_dwordx4 v6, s[40:43], s86 offen offset:2176  lds\n"
"v_mfma_f32_16x16x32_bf16 v[220:223], a[80:83], a[136:139], v[220:223]\n"
"ds_read_b128 a[136:139], v12 offset:26624\n"
"v_mfma_f32_16x16x32_bf16 v[224:227], a[80:83], a[144:147], v[224:227]\n"
"ds_read_b64_tr_b16 a[58:59], v15 offset:8712\n"
"v_mfma_f32_16x16x32_bf16 v[224:227], a[84:87], a[148:151], v[224:227]\n"
"ds_read_b64_tr_b16 a[60:61], v15 offset:12296\n"
"v_mfma_f32_16x16x32_bf16 v[252:255], a[92:95], a[148:151], v[252:255]\n"
"ds_read_b128 a[148:151], v13 offset:28672\n"
"v_mfma_f32_16x16x32_bf16 v[252:255], a[88:91], a[144:147], v[252:255]\n"
"ds_read_b128 a[144:147], v12 offset:28672\n"
"ds_read_b64_tr_b16 a[62:63], v15 offset:12808\n"
"v_mfma_f32_16x16x32_bf16 a[28:31], a[88:91], a[152:155], a[28:31]\n"
"buffer_load_dwordx4 v6, s[40:43], s87 offen offset:3200  lds\n"
"v_mfma_f32_16x16x32_bf16 a[28:31], a[92:95], a[156:159], a[28:31]\n"
"v_mfma_f32_16x16x32_bf16 a[24:27], a[84:87], a[156:159], a[24:27]\n"
"s_add_u32 m0, s64, 8192\n"
"ds_read_b128 a[156:159], v13 offset:30720\n"
"v_mfma_f32_16x16x32_bf16 a[24:27], a[80:83], a[152:155], a[24:27]\n"
"s_waitcnt lgkmcnt(15)\n"
"ds_read_b128 a[152:155], v12 offset:30720\n"
"v_mfma_f32_16x16x32_bf16 v[32:35], a[32:35], a[96:99], v[32:35]\n"
"s_sub_u32 s67, 1310720, s75\n"
"v_mfma_f32_16x16x32_bf16 v[32:35], a[36:39], a[100:103], v[32:35]\n"
"s_waitcnt lgkmcnt(2)\n"
"v_mfma_f32_16x16x32_bf16 v[60:63], a[44:47], a[100:103], v[60:63]\n"
"buffer_load_dwordx4 v11, s[52:55], s67 offen offset:0  lds\n"
"v_mfma_f32_16x16x32_bf16 v[60:63], a[40:43], a[96:99], v[60:63]\n"
"v_mfma_f32_16x16x32_bf16 v[64:67], a[40:43], a[104:107], v[64:67]\n"
"s_sub_u32 s67, 1326080, s75\n"
"v_mfma_f32_16x16x32_bf16 v[64:67], a[44:47], a[108:111], v[64:67]\n"
"v_mfma_f32_16x16x32_bf16 v[36:39], a[36:39], a[108:111], v[36:39]\n"
"buffer_load_dwordx4 v11, s[52:55], s67 offen offset:1024  lds\n"
"v_mfma_f32_16x16x32_bf16 v[36:39], a[32:35], a[104:107], v[36:39]\n"
"v_mfma_f32_16x16x32_bf16 v[40:43], a[32:35], a[112:115], v[40:43]\n"
"s_sub_u32 s67, 1341440, s75\n"
"v_mfma_f32_16x16x32_bf16 v[40:43], a[36:39], a[116:119], v[40:43]\n"
"v_mfma_f32_16x16x32_bf16 v[68:71], a[44:47], a[116:119], v[68:71]\n"
"v_mfma_f32_16x16x32_bf16 v[68:71], a[40:43], a[112:115], v[68:71]\n"
"buffer_load_dwordx4 v11, s[52:55], s67 offen offset:2048  lds\n"
"v_mfma_f32_16x16x32_bf16 v[72:75], a[40:43], a[120:123], v[72:75]\n"
"v_mfma_f32_16x16x32_bf16 v[72:75], a[44:47], a[124:127], v[72:75]\n"
"s_sub_u32 s67, 1356800, s75\n"
"v_mfma_f32_16x16x32_bf16 v[44:47], a[36:39], a[124:127], v[44:47]\n"
"v_mfma_f32_16x16x32_bf16 v[44:47], a[32:35], a[120:123], v[44:47]\n"
"buffer_load_dwordx4 v11, s[52:55], s67 offen offset:3072  lds\n"
"v_mfma_f32_16x16x32_bf16 v[48:51], a[32:35], a[128:131], v[48:51]\n"
"v_mfma_f32_16x16x32_bf16 v[48:51], a[36:39], a[132:135], v[48:51]\n"
"s_add_u32 m0, s64, 12288\n"
"v_mfma_f32_16x16x32_bf16 v[76:79], a[44:47], a[132:135], v[76:79]\n"
"s_waitcnt vmcnt(12)\n"
"ds_read_b64_tr_b16 a[64:65], v9 offset:0\n"
"v_mfma_f32_16x16x32_bf16 v[76:79], a[40:43], a[128:131], v[76:79]\n"
"s_sub_u32 s67, 1376256, s75\n"
"ds_read_b64_tr_b16 a[66:67], v9 offset:512\n"
"v_mfma_f32_16x16x32_bf16 v[80:83], a[40:43], a[136:139], v[80:83]\n"
"ds_read_b64_tr_b16 a[68:69], v9 offset:4096\n"
"v_mfma_f32_16x16x32_bf16 v[80:83], a[44:47], a[140:143], v[80:83]\n"
"ds_read_b64_tr_b16 a[70:71], v9 offset:4608\n"
"buffer_load_dwordx4 v11, s[52:55], s67 offen offset:0  lds\n"
"v_mfma_f32_16x16x32_bf16 v[52:55], a[36:39], a[140:143], v[52:55]\n"
"s_sub_u32 s67, 1391616, s75\n"
"ds_read_b64_tr_b16 a[72:73], v9 offset:8\n"
"v_mfma_f32_16x16x32_bf16 v[52:55], a[32:35], a[136:139], v[52:55]\n"
"s_waitcnt lgkmcnt(5)\n"
"ds_read_b64_tr_b16 a[74:75], v9 offset:520\n"
"v_mfma_f32_16x16x32_bf16 v[56:59], a[32:35], a[144:147], v[56:59]\n"
"ds_read_b64_tr_b16 a[76:77], v9 offset:4104\n"
"v_mfma_f32_16x16x32_bf16 v[56:59], a[36:39], a[148:151], v[56:59]\n"
"ds_read_b64_tr_b16 a[78:79], v9 offset:4616\n"
"v_mfma_f32_16x16x32_bf16 v[84:87], a[44:47], a[148:151], v[84:87]\n"
"buffer_load_dwordx4 v11, s[52:55], s67 offen offset:1024  lds\n"
"v_mfma_f32_16x16x32_bf16 v[84:87], a[40:43], a[144:147], v[84:87]\n"
"ds_read_b64_tr_b16 a[80:81], v15 offset:0\n"
"v_mfma_f32_16x16x32_bf16 a[4:7], a[40:43], a[152:155], a[4:7]\n"
"s_sub_u32 s67, 1406976, s75\n"
"ds_read_b64_tr_b16 a[82:83], v15 offset:512\n"
"v_mfma_f32_16x16x32_bf16 a[4:7], a[44:47], a[156:159], a[4:7]\n"
"ds_read_b64_tr_b16 a[84:85], v15 offset:4096\n"
"v_mfma_f32_16x16x32_bf16 a[0:3], a[36:39], a[156:159], a[0:3]\n"
"ds_read_b64_tr_b16 a[86:87], v15 offset:4608\n"
"buffer_load_dwordx4 v11, s[52:55], s67 offen offset:2048  lds\n"
"v_mfma_f32_16x16x32_bf16 a[0:3], a[32:35], a[152:155], a[0:3]\n"
"ds_read_b64_tr_b16 a[88:89], v15 offset:8\n"
"v_mfma_f32_16x16x32_bf16 v[88:91], a[48:51], a[96:99], v[88:91]\n"
"s_sub_u32 s67, 1422336, s75\n"
"ds_read_b64_tr_b16 a[90:91], v15 offset:520\n"
"v_mfma_f32_16x16x32_bf16 v[88:91], a[52:55], a[100:103], v[88:91]\n"
"ds_read_b64_tr_b16 a[92:93], v15 offset:4104\n"
"v_mfma_f32_16x16x32_bf16 v[116:119], a[60:63], a[100:103], v[116:119]\n"
"ds_read_b64_tr_b16 a[94:95], v15 offset:4616\n"
"v_mfma_f32_16x16x32_bf16 v[116:119], a[56:59], a[96:99], v[116:119]\n"
"buffer_load_dwordx4 v11, s[52:55], s67 offen offset:3072  lds\n"
"v_mfma_f32_16x16x32_bf16 v[120:123], a[56:59], a[104:107], v[120:123]\n"
"v_mfma_f32_16x16x32_bf16 v[120:123], a[60:63], a[108:111], v[120:123]\n"
"s_add_u32 m0, s64, 0\n"
"v_mfma_f32_16x16x32_bf16 v[92:95], a[52:55], a[108:111], v[92:95]\n"
"v_mfma_f32_16x16x32_bf16 v[92:95], a[48:51], a[104:107], v[92:95]\n"
"s_sub_u32 s67, 1310848, s75\n"
"v_mfma_f32_16x16x32_bf16 v[96:99], a[48:51], a[112:115], v[96:99]\n"
"s_waitcnt lgkmcnt(0)\n"
"s_mov_b32 s99, 0\n"
"v_mfma_f32_16x16x32_bf16 v[96:99], a[52:55], a[116:119], v[96:99]\n"
"s_mov_b32 s98, 0\n"
"buffer_load_dwordx4 v11, s[52:55], s67 offen offset:0  lds\n"
"v_mfma_f32_16x16x32_bf16 v[124:127], a[60:63], a[116:119], v[124:127]\n"
"s_memtime s[76:77]\n"
"s_load_dword s98, s[96:97], 0 glc\n"
"v_mfma_f32_16x16x32_bf16 v[124:127], a[56:59], a[112:115], v[124:127]\n"
"s_sub_u32 s67, 1326208, s75\n"
"v_mfma_f32_16x16x32_bf16 v[128:131], a[56:59], a[120:123], v[128:131]\n"
"v_mfma_f32_16x16x32_bf16 v[128:131], a[60:63], a[124:127], v[128:131]\n"
"v_mfma_f32_16x16x32_bf16 v[100:103], a[52:55], a[124:127], v[100:103]\n"
"buffer_load_dwordx4 v11, s[52:55], s67 offen offset:1024  lds\n"
"v_mfma_f32_16x16x32_bf16 v[100:103], a[48:51], a[120:123], v[100:103]\n"
"v_mfma_f32_16x16x32_bf16 v[104:107], a[48:51], a[128:131], v[104:107]\n"
"s_sub_u32 s67, 1341568, s75\n"
"v_mfma_f32_16x16x32_bf16 v[104:107], a[52:55], a[132:135], v[104:107]\n"
"v_mfma_f32_16x16x32_bf16 v[132:135], a[60:63], a[132:135], v[132:135]\n"
"v_mfma_f32_16x16x32_bf16 v[132:135], a[56:59], a[128:131], v[132:135]\n"
"s_mov_b32 s29, s76\n"
"v_mfma_f32_16x16x32_bf16 v[136:139], a[56:59], a[136:139], v[136:139]\n"
"buffer_load_dwordx4 v11, s[52:55], s67 offen offset:2048  lds\n"
"v_mfma_f32_16x16x32_bf16 v[136:139], a[60:63], a[140:143], v[136:139]\n"
"s_sub_u32 s67, 1356928, s75\n"
"v_mfma_f32_16x16x32_bf16 v[108:111], a[52:55], a[140:143], v[108:111]\n"
"s_sub_u32 s99, s99, s98\n"
"s_sub_u32 s99, s99, s98\n"
"v_mfma_f32_16x16x32_bf16 v[108:111], a[48:51], a[136:139], v[108:111]\n"
"s_sub_u32 s99, s99, s98\n"
"v_mfma_f32_16x16x32_bf16 v[112:115], a[48:51], a[144:147], v[112:115]\n"
"s_sub_u32 s99, s99, s98\n"
"buffer_load_dwordx4 v11, s[52:55], s67 offen offset:3072  lds\n"
"v_mfma_f32_16x16x32_bf16 v[112:115], a[52:55], a[148:151], v[112:115]\n"
"s_add_u32 m0, s64, 4096\n"
"v_mfma_f32_16x16x32_bf16 v[140:143], a[60:63], a[148:151], v[140:143]\n"
"s_sub_u32 s99, s99, s98\n"
"s_sub_u32 s99, s99, s98\n"
"v_mfma_f32_16x16x32_bf16 v[140:143], a[56:59], a[144:147], v[140:143]\n"
"s_sub_u32 s99, s99, s98\n"
"s_sub_u32 s67, 1376384, s75\n"
"v_mfma_f32_16x16x32_bf16 a[12:15], a[56:59], a[152:155], a[12:15]\n"
"s_sub_u32 s99, s99, s98\n"
"v_mfma_f32_16x16x32_bf16 a[12:15], a[60:63], a[156:159], a[12:15]\n"
"buffer_load_dwordx4 v11, s[52:55], s67 offen offset:0  lds\n"
"v_mfma_f32_16x16x32_bf16 a[8:11], a[52:55], a[156:159], a[8:11]\n"
"s_sub_u32 s99, s99, s98\n"
"s_sub_u32 s99, s99, s98\n"
"v_mfma_f32_16x16x32_bf16 a[8:11], a[48:51], a[152:155], a[8:11]\n"
"s_sub_u32 s99, s99, s98\n"
"s_sub_u32 s67, 1391744, s75\n"
"v_mfma_f32_16x16x32_bf16 v[144:147], a[64:67], a[96:99], v[144:147]\n"
"s_sub_u32 s99, s99, s98\n"
"v_mfma_f32_16x16x32_bf16 v[144:147], a[68:71], a[100:103], v[144:147]\n"
"buffer_load_dwordx4 v11, s[52:55], s67 offen offset:1024  lds\n"
"v_mfma_f32_16x16x32_bf16 v[172:175], a[76:79], a[100:103], v[172:175]\n"
"s_sub_u32 s99, s99, s98\n"
"s_sub_u32 s99, s99, s98\n"
"v_mfma_f32_16x16x32_bf16 v[172:175], a[72:75], a[96:99], v[172:175]\n"
"s_sub_u32 s99, s99, s98\n"
"s_sub_u32 s67, 1407104, s75\n"
"v_mfma_f32_16x16x32_bf16 v[176:179], a[72:75], a[104:107], v[176:179]\n"
"s_sub_u32 s99, s99, s98\n"
"v_mfma_f32_16x16x32_bf16 v[176:179], a[76:79], a[108:111], v[176:179]\n"
"buffer_load_dwordx4 v11, s[52:55], s67 offen offset:2048  lds\n"
"v_mfma_f32_16x16x32_bf16 v[148:151], a[68:71], a[108:111], v[148:151]\n"
"s_sub_u32 s99, s99, s98\n"
"s_sub_u32 s99, s99, s98\n"
"v_mfma_f32_16x16x32_bf16 v[148:151], a[64:67], a[104:107], v[148:151]\n"
"s_sub_u32 s99, s99, s98\n"
"s_sub_u32 s71, s64, s64\n"
"v_mfma_f32_16x16x32_bf16 v[152:155], a[64:67], a[112:115], v[152:155]\n"
"s_sub_u32 s66, 40960, s73\n"
"s_sub_u32 s99, s99, s98\n"
"v_mfma_f32_16x16x32_bf16 v[152:155], a[68:71], a[116:119], v[152:155]\n"
"s_add_u32 s71, s71, 16384\n"
"s_sub_u32 s67, 1422464, s75\n"
"v_mfma_f32_16x16x32_bf16 v[180:183], a[76:79], a[116:119], v[180:183]\n"
"s_sub_u32 s99, s99, s98\n"
"v_add_u32 v12, s66, v7\n"
"s_sub_u32 s99, s99, s98\n"
"v_mfma_f32_16x16x32_bf16 v[180:183], a[72:75], a[112:115], v[180:183]\n"
"s_sub_u32 s99, s99, s98\n"
"buffer_load_dwordx4 v11, s[52:55], s67 offen offset:3072  lds\n"
"v_mfma_f32_16x16x32_bf16 v[184:187], a[72:75], a[120:123], v[184:187]\n"
"s_sub_u32 s99, s99, s98\n"
"v_add_u32 v13, s66, v8\n"
"s_cmp_lt_u32 %16, 2\n"
"s_cselect_b32 s67, 16384, 0\n"
"v_mfma_f32_16x16x32_bf16 v[184:187], a[76:79], a[124:127], v[184:187]\n"
"s_memtime s[76:77]\n"
"v_add_u32 v20, s71, v7\n"
"s_add_u32 s67, s67, s72\n"
"v_mfma_f32_16x16x32_bf16 v[156:159], a[68:71], a[124:127], v[156:159]\n"
"s_add_u32 m0, s67, 98304\n"
"s_sub_u32 s67, 1536, s60\n"
"v_mfma_f32_16x16x32_bf16 v[156:159], a[64:67], a[120:123], v[156:159]\n"
"s_waitcnt lgkmcnt(0)\n"
"buffer_load_dwordx4 v6, s[48:51], s67 offen offset:0  lds\n"
"v_add_u32 v21, s71, v8\n"
"v_mfma_f32_16x16x32_bf16 v[160:163], a[64:67], a[128:131], v[160:163]\n"
"s_sub_u32 s29, s76, s29\n"
"s_sub_u32 s67, 16896, s60\n"
"v_mfma_f32_16x16x32_bf16 v[160:163], a[68:71], a[132:135], v[160:163]\n"
"s_cmp_lt_u32 s29, 620\n"
"s_cselect_b32 s99, s99, 0\n"
"v_mfma_f32_16x16x32_bf16 v[188:191], a[76:79], a[132:135], v[188:191]\n"
"s_max_u32 s30, s30, s99\n"
"v_mfma_f32_16x16x32_bf16 v[188:191], a[72:75], a[128:131], v[188:191]\n"
"s_waitcnt vmcnt(17)\n"
"ds_read_b128 a[208:211], v20 offset:0\n"
"buffer_load_dwordx4 v6, s[48:51], s67 offen offset:1024  lds\n"
"v_mfma_f32_16x16x32_bf16 v[192:195], a[72:75], a[136:139], v[192:195]\n"
"ds_read_b128 a[212:215], v21 offset:0\n"
"v_mfma_f32_16x16x32_bf16 v[192:195], a[76:79], a[140:143], v[192:195]\n"
"s_sub_u32 s67, 32256, s60\n"
"ds_read_b128 a[216:219], v20 offset:2048\n"
"v_mfma_f32_16x16x32_bf16 v[164:167], a[68:71], a[140:143], v[164:167]\n"
"v_mfma_f32_16x16x32_bf16 v[164:167], a[64:67], a[136:139], v[164:167]\n"
"ds_read_b128 a[220:223], v21 offset:2048\n"
"v_mfma_f32_16x16x32_bf16 v[168:171], a[64:67], a[144:147], v[168:171]\n"
"buffer_load_dwordx4 v6, s[48:51], s67 offen offset:2048  lds\n"
"v_mfma_f32_16x16x32_bf16 v[168:171], a[68:71], a[148:151], v[168:171]\n"
"v_mfma_f32_16x16x32_bf16 v[196:199], a[76:79], a[148:151], v[196:199]\n"
"s_sub_u32 s67, 47616, s60\n"
"v_mfma_f32_16x16x32_bf16 v[196:199], a[72:75], a[144:147], v[196:199]\n"
"v_mfma_f32_16x16x32_bf16 a[20:23], a[72:75], a[152:155], a[20:23]\n"
"buffer_load_dwordx4 v6, s[48:51], s67 offen offset:3072  lds\n"
"v_mfma_f32_16x16x32_bf16 a[20:23], a[76:79], a[156:159], a[20:23]\n"
"s_waitcnt lgkmcnt(0)\n"
"v_mfma_f32_16x16x32_bf16 a[16:19], a[68:71], a[156:159], a[16:19]\n"
"s_add_u32 m0, s64, 20352\n"
"v_mfma_f32_16x16x32_bf16 a[16:19], a[64:67], a[152:155], a[16:19]\n"
"s_barrier\n"
"s_waitcnt vmcnt(12)\n"
"ds_read_b64_tr_b16 a[32:33], v9 offset:8192\n"
"v_mfma_f32_16x16x32_bf16 v[200:203], a[80:83], a[96:99], v[200:203]\n"
"ds_read_b64_tr_b16 a[34:35], v9 offset:8704\n"
"v_mfma_f32_16x16x32_bf16 v[200:203], a[84:87], a[100:103], v[200:203]\n"
"ds_read_b64_tr_b16 a[36:37], v9 offset:12288\n"
"buffer_load_dwordx4 v6, s[40:43], s88 offen offset:128  lds\n"
"v_mfma_f32_16x16x32_bf16 v[228:231], a[92:95], a[100:103], v[228:231]\n"
"ds_read_b128 a[100:103], v13 offset:32768\n"
"v_mfma_f32_16x16x32_bf16 v[228:231], a[88:91], a[96:99], v[228:231]\n"
"ds_read_b128 a[96:99], v12 offset:32768\n"
"v_mfma_f32_16x16x32_bf16 v[232:235], a[88:91], a[104:107], v[232:235]\n"
"ds_read_b64_tr_b16 a[38:39], v9 offset:12800\n"
"buffer_load_dwordx4 v6, s[40:43], s89 offen offset:1152  lds\n"
"v_mfma_f32_16x16x32_bf16 v[232:235], a[92:95], a[108:111], v[232:235]\n"
"ds_read_b64_tr_b16 a[40:41], v9 offset:8200\n"
"v_mfma_f32_16x16x32_bf16 v[204:207], a[84:87], a[108:111], v[204:207]\n"
"ds_read_b128 a[108:111], v13 offset:34816\n"
"v_mfma_f32_16x16x32_bf16 v[204:207], a[80:83], a[104:107], v[204:207]\n"
"ds_read_b128 a[104:107], v12 offset:34816\n"
"v_mfma_f32_16x16x32_bf16 v[208:211], a[80:83], a[112:115], v[208:211]\n"
"ds_read_b64_tr_b16 a[42:43], v9 offset:8712\n"
"v_mfma_f32_16x16x32_bf16 v[208:211], a[84:87], a[116:119], v[208:211]\n"
"ds_read_b64_tr_b16 a[44:45], v9 offset:12296\n"
"buffer_load_dwordx4 v6, s[40:43], s90 offen offset:2176  lds\n"
"v_mfma_f32_16x16x32_bf16 v[236:239], a[92:95], a[116:119], v[236:239]\n"
"ds_read_b128 a[116:119], v13 offset:36864\n"
"v_mfma_f32_16x16x32_bf16 v[236:239], a[88:91], a[112:115], v[236:239]\n"
"ds_read_b128 a[112:115], v12 offset:36864\n"
"v_mfma_f32_16x16x32_bf16 v[240:243], a[88:91], a[120:123], v[240:243]\n"
"ds_read_b64_tr_b16 a[46:47], v9 offset:12808\n"
"v_mfma_f32_16x16x32_bf16 v[240:243], a[92:95], a[124:127], v[240:243]\n"
"ds_read_b64_tr_b16 a[48:49], v15 offset:8192\n"
"buffer_load_dwordx4 v6, s[40:43], s91 offen offset:3200  lds\n"
"v_mfma_f32_16x16x32_bf16 v[212:215], a[84:87], a[124:127], v[212:215]\n"
"ds_read_b128 a[124:127], v13 offset:38912\n"
"v_mfma_f32_16x16x32_bf16 v[212:215], a[80:83], a[120:123], v[212:215]\n"
"ds_read_b128 a[120:123], v12 offset:38912\n"
"v_mfma_f32_16x16x32_bf16 v[216:219], a[80:83], a[128:131], v[216:219]\n"
"ds_read_b64_tr_b16 a[50:51], v15 offset:8704\n"
"v_mfma_f32_16x16x32_bf16 v[216:219], a[84:87], a[132:135], v[216:219]\n"
"s_add_u32 m0, s64, 8192\n"
"ds_read_b64_tr_b16 a[52:53], v15 offset:12288\n"
"v_mfma_f32_16x16x32_bf16 v[244:247], a[92:95], a[132:135], v[244:247]\n"
"ds_read_b128 a[132:135], v13 offset:24576\n"
"v_mfma_f32_16x16x32_bf16 v[244:247], a[88:91], a[128:131], v[244:247]\n"
"ds_read_b128 a[128:131], v12 offset:24576\n"
"v_mfma_f32_16x16x32_bf16 v[248:251], a[88:91], a[136:139], v[248:251]\n"
"ds_read_b64_tr_b16 a[54:55], v15 offset:12800\n"
"v_mfma_f32_16x16x32_bf16 v[248:251], a[92:95], a[140:143], v[248:251]\n"
"ds_read_b64_tr_b16 a[56:57], v15 offset:8200\n"
"v_mfma_f32_16x16x32_bf16 v[220:223], a[84:87], a[140:143], v[220:223]\n"
"ds_read_b128 a[140:143], v13 offset:26624\n"
"v_mfma_f32_16x16x32_bf16 v[220:223], a[80:83], a[136:139], v[220:223]\n"
"ds_read_b128 a[136:139], v12 offset:26624\n"
"v_mfma_f32_16x16x32_bf16 v[224:227], a[80:83], a[144:147], v[224:227]\n"
"ds_read_b64_tr_b16 a[58:59], v15 offset:8712\n"
"v_mfma_f32_16x16x32_bf16 v[224:227], a[84:87], a[148:151], v[224:227]\n"
"s_sub_u32 s67, 1572864, s75\n"
"ds_read_b64_tr_b16 a[60:61], v15 offset:12296\n"
"v_mfma_f32_16x16x32_bf16 v[252:255], a[92:95], a[148:151], v[252:255]\n"
"ds_read_b128 a[148:151], v13 offset:28672\n"
"v_mfma_f32_16x16x32_bf16 v[252:255], a[88:91], a[144:147], v[252:255]\n"
"ds_read_b128 a[144:147], v12 offset:28672\n"
"ds_read_b64_tr_b16 a[62:63], v15 offset:12808\n"
"v_mfma_f32_16x16x32_bf16 a[28:31], a[88:91], a[152:155], a[28:31]\n"
"v_mfma_f32_16x16x32_bf16 a[28:31], a[92:95], a[156:159], a[28:31]\n"
"v_mfma_f32_16x16x32_bf16 a[24:27], a[84:87], a[156:159], a[24:27]\n"
"ds_read_b128 a[156:159], v13 offset:30720\n"
"v_mfma_f32_16x16x32_bf16 a[24:27], a[80:83], a[152:155], a[24:27]\n"
"s_waitcnt lgkmcnt(15)\n"
"ds_read_b128 a[152:155], v12 offset:30720\n"
"v_mfma_f32_16x16x32_bf16 v[32:35], a[32:35], a[96:99], v[32:35]\n"
"v_mfma_f32_16x16x32_bf16 v[32:35], a[36:39], a[100:103], v[32:35]\n"
"s_waitcnt lgkmcnt(2)\n"
"v_mfma_f32_16x16x32_bf16 v[60:63], a[44:47], a[100:103], v[60:63]\n"
"buffer_load_dwordx4 v11, s[52:55], s67 offen offset:0  lds\n"
"v_mfma_f32_16x16x32_bf16 v[60:63], a[40:43], a[96:99], v[60:63]\n"
"v_mfma_f32_16x16x32_bf16 v[64:67], a[40:43], a[104:107], v[64:67]\n"
"s_sub_u32 s67, 1588224, s75\n"
"v_mfma_f32_16x16x32_bf16 v[64:67], a[44:47], a[108:111], v[64:67]\n"
"v_mfma_f32_16x16x32_bf16 v[36:39], a[36:39], a[108:111], v[36:39]\n"
"v_mfma_f32_16x16x32_bf16 v[36:39], a[32:35], a[104:107], v[36:39]\n"
"buffer_load_dwordx4 v11, s[52:55], s67 offen offset:1024  lds\n"
"v_mfma_f32_16x16x32_bf16 v[40:43], a[32:35], a[112:115], v[40:43]\n"
"v_mfma_f32_16x16x32_bf16 v[40:43], a[36:39], a[116:119], v[40:43]\n"
"s_sub_u32 s67, 1603584, s75\n"
"v_mfma_f32_16x16x32_bf16 v[68:71], a[44:47], a[116:119], v[68:71]\n"
"v_mfma_f32_16x16x32_bf16 v[68:71], a[40:43], a[112:115], v[68:71]\n"
"buffer_load_dwordx4 v11, s[52:55], s67 offen offset:2048  lds\n"
"v_mfma_f32_16x16x32_bf16 v[72:75], a[40:43], a[120:123], v[72:75]\n"
"v_mfma_f32_16x16x32_bf16 v[72:75], a[44:47], a[124:127], v[72:75]\n"
"s_sub_u32 s67, 1618944, s75\n"
"v_mfma_f32_16x16x32_bf16 v[44:47], a[36:39], a[124:127], v[44:47]\n"
"v_mfma_f32_16x16x32_bf16 v[44:47], a[32:35], a[120:123], v[44:47]\n"
"v_mfma_f32_16x16x32_bf16 v[48:51], a[32:35], a[128:131], v[48:51]\n"
"buffer_load_dwordx4 v11, s[52:55], s67 offen offset:3072  lds\n"
"v_mfma_f32_16x16x32_bf16 v[48:51], a[36:39], a[132:135], v[48:51]\n"
"s_add_u32 m0, s64, 12288\n"
"v_mfma_f32_16x16x32_bf16 v[76:79], a[44:47], a[132:135], v[76:79]\n"
"s_waitcnt vmcnt(12)\n"
"ds_read_b64_tr_b16 a[64:65], v9 offset:0\n"
"v_mfma_f32_16x16x32_bf16 v[76:79], a[40:43], a[128:131], v[76:79]\n"
"s_sub_u32 s67, 1638400, s75\n"
"ds_read_b64_tr_b16 a[66:67], v9 offset:512\n"
"v_mfma_f32_16x16x32_bf16 v[80:83], a[40:43], a[136:139], v[80:83]\n"
"ds_read_b64_tr_b16 a[68:69], v9 offset:4096\n"
"v_mfma_f32_16x16x32_bf16 v[80:83], a[44:47], a[140:143], v[80:83]\n"
"ds_read_b64_tr_b16 a[70:71], v9 offset:4608\n"
"buffer_load_dwordx4 v11, s[52:55], s67 offen offset:0  lds\n"
"v_mfma_f32_16x16x32_bf16 v[52:55], a[36:39], a[140:143], v[52:55]\n"
"s_sub_u32 s67, 1653760, s75\n"
"ds_read_b64_tr_b16 a[72:73], v9 offset:8\n"
"v_mfma_f32_16x16x32_bf16 v[52:55], a[32:35], a[136:139], v[52:55]\n"
"L_st2664:\n"
"s_cbranch_execz L_retA\n"
"s_waitcnt lgkmcnt(5)\n"
"ds_read_b64_tr_b16 a[74:75], v9 offset:520\n"
"v_mfma_f32_16x16x32_bf16 v[56:59], a[32:35], a[144:147], v[56:59]\n"
"ds_read_b64_tr_b16 a[76:77], v9 offset:4104\n"
"v_mfma_f32_16x16x32_bf16 v[56:59], a[36:39], a[148:151], v[56:59]\n"
"ds_read_b64_tr_b16 a[78:79], v9 offset:4616\n"
"v_mfma_f32_16x16x32_bf16 v[84:87], a[44:47], a[148:151], v[84:87]\n"
"buffer_load_dwordx4 v11, s[52:55], s67 offen offset:1024  lds\n"
"v_mfma_f32_16x16x32_bf16 v[84:87], a[40:43], a[144:147], v[84:87]\n"
"ds_read_b64_tr_b16 a[80:81], v15 offset:0\n"
"v_mfma_f32_16x16x32_bf16 a[4:7], a[40:43], a[152:155], a[4:7]\n"
"s_sub_u32 s67, 1669120, s75\n"
"ds_read_b64_tr_b16 a[82:83], v15 offset:512\n"
"v_mfma_f32_16x16x32_bf16 a[4:7], a[44:47], a[156:159], a[4:7]\n"
"ds_read_b64_tr_b16 a[84:85], v15 offset:4096\n"
"v_mfma_f32_16x16x32_bf16 a[0:3], a[36:39], a[156:159], a[0:3]\n"
"ds_read_b64_tr_b16 a[86:87], v15 offset:4608\n"
"buffer_load_dwordx4 v11, s[52:55], s67 offen offset:2048  lds\n"
"v_mfma_f32_16x16x32_bf16 a[0:3], a[32:35], a[152:155], a[0:3]\n"
"ds_read_b64_tr_b16 a[88:89], v15 offset:8\n"
"v_mfma_f32_16x16x32_bf16 v[88:91], a[48:51], a[96:99], v[88:91]\n"
"s_sub_u32 s67, 1684480, s75\n"
"ds_read_b64_tr_b16 a[90:91], v15 offset:520\n"
"v_mfma_f32_16x16x32_bf16 v[88:91], a[52:55], a[100:103], v[88:91]\n"
"ds_read_b64_tr_b16 a[92:93], v15 offset:4104\n"
"v_mfma_f32_16x16x32_bf16 v[116:119], a[60:63], a[100:103], v[116:119]\n"
"ds_read_b64_tr_b16 a[94:95], v15 offset:4616\n"
"v_mfma_f32_16x16x32_bf16 v[116:119], a[56:59], a[96:99], v[116:119]\n"
"buffer_load_dwordx4 v11, s[52:55], s67 offen offset:3072  lds\n"
"v_mfma_f32_16x16x32_bf16 v[120:123], a[56:59], a[104:107], v[120:123]\n"
"v_mfma_f32_16x16x32_bf16 v[120:123], a[60:63], a[108:111], v[120:123]\n"
"s_add_u32 m0, s64, 0\n"
"v_mfma_f32_16x16x32_bf16 v[92:95], a[52:55], a[108:111], v[92:95]\n"
"v_mfma_f32_16x16x32_bf16 v[92:95], a[48:51], a[104:107], v[92:95]\n"
"s_sub_u32 s67, 1572992, s75\n"
"v_mfma_f32_16x16x32_bf16 v[96:99], a[48:51], a[112:115], v[96:99]\n"
"s_waitcnt lgkmcnt(0)\n"
"s_mov_b32 s99, 0\n"
"v_mfma_f32_16x16x32_bf16 v[96:99], a[52:55], a[116:119], v[96:99]\n"
"s_mov_b32 s98, 0\n"
"buffer_load_dwordx4 v11, s[52:55], s67 offen offset:0  lds\n"
"v_mfma_f32_16x16x32_bf16 v[124:127], a[60:63], a[116:119], v[124:127]\n"
"s_memtime s[76:77]\n"
"s_load_dword s98, s[96:97], 8320 glc\n"
"v_mfma_f32_16x16x32_bf16 v[124:127], a[56:59], a[112:115], v[124:127]\n"
"s_sub_u32 s67, 1588352, s75\n"
"v_mfma_f32_16x16x32_bf16 v[128:131], a[56:59], a[120:123], v[128:131]\n"
"v_mfma_f32_16x16x32_bf16 v[128:131], a[60:63], a[124:127], v[128:131]\n"
"buffer_load_dwordx4 v11, s[52:55], s67 offen offset:1024  lds\n"
"v_mfma_f32_16x16x32_bf16 v[100:103], a[52:55], a[124:127], v[100:103]\n"
"v_mfma_f32_16x16x32_bf16 v[100:103], a[48:51], a[120:123], v[100:103]\n"
"s_sub_u32 s67, 1603712, s75\n"
"v_mfma_f32_16x16x32_bf16 v[104:107], a[48:51], a[128:131], v[104:107]\n"
"v_mfma_f32_16x16x32_bf16 v[104:107], a[52:55], a[132:135], v[104:107]\n"
"v_mfma_f32_16x16x32_bf16 v[132:135], a[60:63], a[132:135], v[132:135]\n"
"buffer_load_dwordx4 v11, s[52:55], s67 offen offset:2048  lds\n"
"v_mfma_f32_16x16x32_bf16 v[132:135], a[56:59], a[128:131], v[132:135]\n"
"s_mov_b32 s29, s76\n"
"v_mfma_f32_16x16x32_bf16 v[136:139], a[56:59], a[136:139], v[136:139]\n"
"s_sub_u32 s67, 1619072, s75\n"
"v_mfma_f32_16x16x32_bf16 v[136:139], a[60:63], a[140:143], v[136:139]\n"
"buffer_load_dwordx4 v11, s[52:55], s67 offen offset:3072  lds\n"
"v_mfma_f32_16x16x32_bf16 v[108:111], a[52:55], a[140:143], v[108:111]\n"
"s_sub_u32 s99, s99, s98\n"
"s_sub_u32 s99, s99, s98\n"
"v_mfma_f32_16x16x32_bf16 v[108:111], a[48:51], a[136:139], v[108:111]\n"
"s_sub_u32 s99, s99, s98\n"
"s_add_u32 m0, s64, 4096\n"
"v_mfma_f32_16x16x32_bf16 v[112:115], a[48:51], a[144:147], v[112:115]\n"
"s_sub_u32 s99, s99, s98\n"
"v_mfma_f32_16x16x32_bf16 v[112:115], a[52:55], a[148:151], v[112:115]\n"
"s_sub_u32 s67, 1638528, s75\n"
"v_mfma_f32_16x16x32_bf16 v[140:143], a[60:63], a[148:151], v[140:143]\n"
"s_sub_u32 s99, s99, s98\n"
"s_sub_u32 s99, s99, s98\n"
"v_mfma_f32_16x16x32_bf16 v[140:143], a[56:59], a[144:147], v[140:143]\n"
"s_sub_u32 s99, s99, s98\n"
"buffer_load_dwordx4 v11, s[52:55], s67 offen offset:0  lds\n"
"v_mfma_f32_16x16x32_bf16 a[12:15], a[56:59], a[152:155], a[12:15]\n"
"s_sub_u32 s99, s99, s98\n"
"v_mfma_f32_16x16x32_bf16 a[12:15], a[60:63], a[156:159], a[12:15]\n"
"s_sub_u32 s67, 1653888, s75\n"
"v_mfma_f32_16x16x32_bf16 a[8:11], a[52:55], a[156:159], a[8:11]\n"
"s_sub_u32 s99, s99, s98\n"
"s_sub_u32 s99, s99, s98\n"
"v_mfma_f32_16x16x32_bf16 a[8:11], a[48:51], a[152:155], a[8:11]\n"
"s_sub_u32 s99, s99, s98\n"
"v_mfma_f32_16x16x32_bf16 v[144:147], a[64:67], a[96:99], v[144:147]\n"
"s_sub_u32 s99, s99, s98\n"
"v_mfma_f32_16x16x32_bf16 v[144:147], a[68:71], a[100:103], v[144:147]\n"
"buffer_load_dwordx4 v11, s[52:55], s67 offen offset:1024  lds\n"
"v_mfma_f32_16x16x32_bf16 v[172:175], a[76:79], a[100:103], v[172:175]\n"
"s_sub_u32 s99, s99, s98\n"
"s_sub_u32 s99, s99, s98\n"
"v_mfma_f32_16x16x32_bf16 v[172:175], a[72:75], a[96:99], v[172:175]\n"
"s_sub_u32 s99, s99, s98\n"
"s_sub_u32 s67, 1669248, s75\n"
"v_mfma_f32_16x16x32_bf16 v[176:179], a[72:75], a[104:107], v[176:179]\n"
"s_sub_u32 s99, s99, s98\n"
"v_mfma_f32_16x16x32_bf16 v[176:179], a[76:79], a[108:111], v[176:179]\n"
"buffer_load_dwordx4 v11, s[52:55], s67 offen offset:2048  lds\n"
"v_mfma_f32_16x16x32_bf16 v[148:151], a[68:71], a[108:111], v[148:151]\n"
"s_sub_u32 s99, s99, s98\n"
"s_sub_u32 s99, s99, s98\n"
"v_mfma_f32_16x16x32_bf16 v[148:151], a[64:67], a[104:107], v[148:151]\n"
"s_sub_u32 s99, s99, s98\n"
"s_sub_u32 s71, s64, s64\n"
"s_sub_u32 s67, 1684608, s75\n"
"v_mfma_f32_16x16x32_bf16 v[152:155], a[64:67], a[112:115], v[152:155]\n"
"s_sub_u32 s66, 81920, s73\n"
"s_sub_u32 s99, s99, s98\n"
"v_mfma_f32_16x16x32_bf16 v[152:155], a[68:71], a[116:119], v[152:155]\n"
"s_add_u32 s71, s71, 20480\n"
"buffer_load_dwordx4 v11, s[52:55], s67 offen offset:3072  lds\n"
"v_mfma_f32_16x16x32_bf16 v[180:183], a[76:79], a[116:119], v[180:183]\n"
"s_sub_u32 s99, s99, s98\n"
"v_add_u32 v12, s66, v7\n"
"s_sub_u32 s99, s99, s98\n"
"v_mfma_f32_16x16x32_bf16 v[180:183], a[72:75], a[112:115], v[180:183]\n"
"s_sub_u32 s99, s99, s98\n"
"s_cmp_lt_u32 %16, 2\n"
"s_cselect_b32 s67, 16384, 0\n"
"s_add_u32 s67, s67, s72\n"
"v_mfma_f32_16x16x32_bf16 v[184:187], a[72:75], a[120:123], v[184:187]\n"
"s_sub_u32 s99, s99, s98\n"
"v_add_u32 v13, s66, v8\n"
"s_add_u32 m0, s67, 139264\n"
"v_mfma_f32_16x16x32_bf16 v[184:187], a[76:79], a[124:127], v[184:187]\n"
"s_memtime s[76:77]\n"
"v_add_u32 v20, s71, v7\n"
"s_sub_u32 s67, 1792, s60\n"
"v_mfma_f32_16x16x32_bf16 v[156:159], a[68:71], a[124:127], v[156:159]\n"
"buffer_load_dwordx4 v6, s[48:51], s67 offen offset:0  lds\n"
"v_mfma_f32_16x16x32_bf16 v[156:159], a[64:67], a[120:123], v[156:159]\n"
"s_waitcnt lgkmcnt(0)\n"
"v_add_u32 v21, s71, v8\n"
"v_mfma_f32_16x16x32_bf16 v[160:163], a[64:67], a[128:131], v[160:163]\n"
"s_sub_u32 s29, s76, s29\n"
"s_sub_u32 s67, 17152, s60\n"
"v_mfma_f32_16x16x32_bf16 v[160:163], a[68:71], a[132:135], v[160:163]\n"
"s_cmp_lt_u32 s29, 620\n"
"s_cselect_b32 s99, s99, 0\n"
"v_mfma_f32_16x16x32_bf16 v[188:191], a[76:79], a[132:135], v[188:191]\n"
"s_max_u32 s31, s31, s99\n"
"buffer_load_dwordx4 v6, s[48:51], s67 offen offset:1024  lds\n"
"v_mfma_f32_16x16x32_bf16 v[188:191], a[72:75], a[128:131], v[188:191]\n"
"s_waitcnt vmcnt(18)\n"
"ds_read_b128 a[224:227], v20 offset:0\n"
"v_mfma_f32_16x16x32_bf16 v[192:195], a[72:75], a[136:139], v[192:195]\n"
"s_sub_u32 s67, 32512, s60\n"
"ds_read_b128 a[228:231], v21 offset:0\n"
"v_mfma_f32_16x16x32_bf16 v[192:195], a[76:79], a[140:143], v[192:195]\n"
"ds_read_b128 a[232:235], v20 offset:2048\n"
"v_mfma_f32_16x16x32_bf16 v[164:167], a[68:71], a[140:143], v[164:167]\n"
"v_mfma_f32_16x16x32_bf16 v[164:167], a[64:67], a[136:139], v[164:167]\n"
"ds_read_b128 a[236:239], v21 offset:2048\n"
"v_mfma_f32_16x16x32_bf16 v[168:171], a[64:67], a[144:147], v[168:171]\n"
"buffer_load_dwordx4 v6, s[48:51], s67 offen offset:2048  lds\n"
"v_mfma_f32_16x16x32_bf16 v[168:171], a[68:71], a[148:151], v[168:171]\n"
"v_mfma_f32_16x16x32_bf16 v[196:199], a[76:79], a[148:151], v[196:199]\n"
"s_sub_u32 s67, 47872, s60\n"
"v_mfma_f32_16x16x32_bf16 v[196:199], a[72:75], a[144:147], v[196:199]\n"
"v_mfma_f32_16x16x32_bf16 a[20:23], a[72:75], a[152:155], a[20:23]\n"
"buffer_load_dwordx4 v6, s[48:51], s67 offen offset:3072  lds\n"
"v_mfma_f32_16x16x32_bf16 a[20:23], a[76:79], a[156:159], a[20:23]\n"
"s_waitcnt lgkmcnt(0)\n"
"v_mfma_f32_16x16x32_bf16 a[16:19], a[68:71], a[156:159], a[16:19]\n"
"s_add_u32 m0, s64, 16256\n"
"v_mfma_f32_16x16x32_bf16 a[16:19], a[64:67], a[152:155], a[16:19]\n"
"s_barrier\n"
"s_waitcnt vmcnt(12)\n"
"ds_read_b64_tr_b16 a[32:33], v9 offset:8192\n"
"v_mfma_f32_16x16x32_bf16 v[200:203], a[80:83], a[96:99], v[200:203]\n"
"ds_read_b64_tr_b16 a[34:35], v9 offset:8704\n"
"buffer_load_dwordx4 v6, s[40:43], s92 offen offset:128  lds\n"
"v_mfma_f32_16x16x32_bf16 v[200:203], a[84:87], a[100:103], v[200:203]\n"
"ds_read_b64_tr_b16 a[36:37], v9 offset:12288\n"
"v_mfma_f32_16x16x32_bf16 v[228:231], a[92:95], a[100:103], v[228:231]\n"
"ds_read_b128 a[100:103], v13 offset:32768\n"
"v_mfma_f32_16x16x32_bf16 v[228:231], a[88:91], a[96:99], v[228:231]\n"
"ds_read_b128 a[96:99], v12 offset:32768\n"
"buffer_load_dwordx4 v6, s[40:43], s93 offen offset:1152  lds\n"
"v_mfma_f32_16x16x32_bf16 v[232:235], a[88:91], a[104:107], v[232:235]\n"
"ds_read_b64_tr_b16 a[38:39], v9 offset:12800\n"
"v_mfma_f32_16x16x32_bf16 v[232:235], a[92:95], a[108:111], v[232:235]\n"
"ds_read_b64_tr_b16 a[40:41], v9 offset:8200\n"
"v_mfma_f32_16x16x32_bf16 v[204:207], a[84:87], a[108:111], v[204:207]\n"
"ds_read_b128 a[108:111], v13 offset:34816\n"
"v_mfma_f32_16x16x32_bf16 v[204:207], a[80:83], a[104:107], v[204:207]\n"
"ds_read_b128 a[104:107], v12 offset:34816\n"
"v_mfma_f32_16x16x32_bf16 v[208:211], a[80:83], a[112:115], v[208:211]\n"
"ds_read_b64_tr_b16 a[42:43], v9 offset:8712\n"
"buffer_load_dwordx4 v6, s[40:43], s94 offen offset:2176  lds\n"
"v_mfma_f32_16x16x32_bf16 v[208:211], a[84:87], a[116:119], v[208:211]\n"
"ds_read_b64_tr_b16 a[44:45], v9 offset:12296\n"
"v_mfma_f32_16x16x32_bf16 v[236:239], a[92:95], a[116:119], v[236:239]\n"
"ds_read_b128 a[116:119], v13 offset:36864\n"
"v_mfma_f32_16x16x32_bf16 v[236:239], a[88:91], a[112:115], v[236:239]\n"
"ds_read_b128 a[112:115], v12 offset:36864\n"
"v_mfma_f32_16x16x32_bf16 v[240:243], a[88:91], a[120:123], v[240:243]\n"
"ds_read_b64_tr_b16 a[46:47], v9 offset:12808\n"
"v_mfma_f32_16x16x32_bf16 v[240:243], a[92:95], a[124:127], v[240:243]\n"
"ds_read_b64_tr_b16 a[48:49], v15 offset:8192\n"
"buffer_load_dwordx4 v6, s[40:43], s95 offen offset:3200  lds\n"
"v_mfma_f32_16x16x32_bf16 v[212:215], a[84:87], a[124:127], v[212:215]\n"
"ds_read_b128 a[124:127], v13 offset:38912\n"
"v_mfma_f32_16x16x32_bf16 v[212:215], a[80:83], a[120:123], v[212:215]\n"
"ds_read_b128 a[120:123], v12 offset:38912\n"
"v_mfma_f32_16x16x32_bf16 v[216:219], a[80:83], a[128:131], v[216:219]\n"
"ds_read_b64_tr_b16 a[50:51], v15 offset:8704\n"
"v_mfma_f32_16x16x32_bf16 v[216:219], a[84:87], a[132:135], v[216:219]\n"
"s_add_u32 m0, s64, 8192\n"
"ds_read_b64_tr_b16 a[52:53], v15 offset:12288\n"
"v_mfma_f32_16x16x32_bf16 v[244:247], a[92:95], a[132:135], v[244:247]\n"
"ds_read_b128 a[132:135], v13 offset:24576\n"
"v_mfma_f32_16x16x32_bf16 v[244:247], a[88:91], a[128:131], v[244:247]\n"
"ds_read_b128 a[128:131], v12 offset:24576\n"
"v_mfma_f32_16x16x32_bf16 v[248:251], a[88:91], a[136:139], v[248:251]\n"
"ds_read_b64_tr_b16 a[54:55], v15 offset:12800\n"
"v_mfma_f32_16x16x32_bf16 v[248:251], a[92:95], a[140:143], v[248:251]\n"
"ds_read_b64_tr_b16 a[56:57], v15 offset:8200\n"
"v_mfma_f32_16x16x32_bf16 v[220:223], a[84:87], a[140:143], v[220:223]\n"
"ds_read_b128 a[140:143], v13 offset:26624\n"
"v_mfma_f32_16x16x32_bf16 v[220:223], a[80:83], a[136:139], v[220:223]\n"
"ds_read_b128 a[136:139], v12 offset:26624\n"
"v_mfma_f32_16x16x32_bf16 v[224:227], a[80:83], a[144:147], v[224:227]\n"
"ds_read_b64_tr_b16 a[58:59], v15 offset:8712\n"
"v_mfma_f32_16x16x32_bf16 v[224:227], a[84:87], a[148:151], v[224:227]\n"
"s_sub_u32 s67, 1835008, s75\n"
"ds_read_b64_tr_b16 a[60:61], v15 offset:12296\n"
"v_mfma_f32_16x16x32_bf16 v[252:255], a[92:95], a[148:151], v[252:255]\n"
"ds_read_b128 a[148:151], v13 offset:28672\n"
"v_mfma_f32_16x16x32_bf16 v[252:255], a[88:91], a[144:147], v[252:255]\n"
"ds_read_b128 a[144:147], v12 offset:28672\n"
"ds_read_b64_tr_b16 a[62:63], v15 offset:12808\n"
"v_mfma_f32_16x16x32_bf16 a[28:31], a[88:91], a[152:155], a[28:31]\n"
"v_mfma_f32_16x16x32_bf16 a[28:31], a[92:95], a[156:159], a[28:31]\n"
"v_mfma_f32_16x16x32_bf16 a[24:27], a[84:87], a[156:159], a[24:27]\n"
"ds_read_b128 a[156:159], v13 offset:30720\n"
"v_mfma_f32_16x16x32_bf16 a[24:27], a[80:83], a[152:155], a[24:27]\n"
"s_waitcnt lgkmcnt(15)\n"
"ds_read_b128 a[152:155], v12 offset:30720\n"
"v_mfma_f32_16x16x32_bf16 v[32:35], a[32:35], a[96:99], v[32:35]\n"
"v_mfma_f32_16x16x32_bf16 v[32:35], a[36:39], a[100:103], v[32:35]\n"
"s_waitcnt lgkmcnt(2)\n"
"v_mfma_f32_16x16x32_bf16 v[60:63], a[44:47], a[100:103], v[60:63]\n"
"buffer_load_dwordx4 v11, s[52:55], s67 offen offset:0  lds\n"
"v_mfma_f32_16x16x32_bf16 v[60:63], a[40:43], a[96:99], v[60:63]\n"
"v_mfma_f32_16x16x32_bf16 v[64:67], a[40:43], a[104:107], v[64:67]\n"
"s_sub_u32 s67, 1850368, s75\n"
"v_mfma_f32_16x16x32_bf16 v[64:67], a[44:47], a[108:111], v[64:67]\n"
"v_mfma_f32_16x16x32_bf16 v[36:39], a[36:39], a[108:111], v[36:39]\n"
"v_mfma_f32_16x16x32_bf16 v[36:39], a[32:35], a[104:107], v[36:39]\n"
"buffer_load_dwordx4 v11, s[52:55], s67 offen offset:1024  lds\n"
"v_mfma_f32_16x16x32_bf16 v[40:43], a[32:35], a[112:115], v[40:43]\n"
"v_mfma_f32_16x16x32_bf16 v[40:43], a[36:39], a[116:119], v[40:43]\n"
"s_sub_u32 s67, 1865728, s75\n"
"v_mfma_f32_16x16x32_bf16 v[68:71], a[44:47], a[116:119], v[68:71]\n"
"v_mfma_f32_16x16x32_bf16 v[68:71], a[40:43], a[112:115], v[68:71]\n"
"buffer_load_dwordx4 v11, s[52:55], s67 offen offset:2048  lds\n"
"v_mfma_f32_16x16x32_bf16 v[72:75], a[40:43], a[120:123], v[72:75]\n"
"v_mfma_f32_16x16x32_bf16 v[72:75], a[44:47], a[124:127], v[72:75]\n"
"s_sub_u32 s67, 1881088, s75\n"
"v_mfma_f32_16x16x32_bf16 v[44:47], a[36:39], a[124:127], v[44:47]\n"
"v_mfma_f32_16x16x32_bf16 v[44:47], a[32:35], a[120:123], v[44:47]\n"
"v_mfma_f32_16x16x32_bf16 v[48:51], a[32:35], a[128:131], v[48:51]\n"
"buffer_load_dwordx4 v11, s[52:55], s67 offen offset:3072  lds\n"
"v_mfma_f32_16x16x32_bf16 v[48:51], a[36:39], a[132:135], v[48:51]\n"
"s_add_u32 m0, s64, 12288\n"
"v_mfma_f32_16x16x32_bf16 v[76:79], a[44:47], a[132:135], v[76:79]\n"
"s_waitcnt vmcnt(12)\n"
"ds_read_b64_tr_b16 a[64:65], v9 offset:0\n"
"v_mfma_f32_16x16x32_bf16 v[76:79], a[40:43], a[128:131], v[76:79]\n"
"ds_read_b64_tr_b16 a[66:67], v9 offset:512\n"
"v_mfma_f32_16x16x32_bf16 v[80:83], a[40:43], a[136:139], v[80:83]\n"
"s_sub_u32 s67, 1900544, s75\n"
"ds_read_b64_tr_b16 a[68:69], v9 offset:4096\n"
"v_mfma_f32_16x16x32_bf16 v[80:83], a[44:47], a[140:143], v[80:83]\n"
"ds_read_b64_tr_b16 a[70:71], v9 offset:4608\n"
"v_mfma_f32_16x16x32_bf16 v[52:55], a[36:39], a[140:143], v[52:55]\n"
"ds_read_b64_tr_b16 a[72:73], v9 offset:8\n"
"buffer_load_dwordx4 v11, s[52:55], s67 offen offset:0  lds\n"
"v_mfma_f32_16x16x32_bf16 v[52:55], a[32:35], a[136:139], v[52:55]\n"
"s_waitcnt lgkmcnt(5)\n"
"ds_read_b64_tr_b16 a[74:75], v9 offset:520\n"
"v_mfma_f32_16x16x32_bf16 v[56:59], a[32:35], a[144:147], v[56:59]\n"
"s_sub_u32 s67, 1915904, s75\n"
"ds_read_b64_tr_b16 a[76:77], v9 offset:4104\n"
"v_mfma_f32_16x16x32_bf16 v[56:59], a[36:39], a[148:151], v[56:59]\n"
"ds_read_b64_tr_b16 a[78:79], v9 offset:4616\n"
"v_mfma_f32_16x16x32_bf16 v[84:87], a[44:47], a[148:151], v[84:87]\n"
"buffer_load_dwordx4 v11, s[52:55], s67 offen offset:1024  lds\n"
"v_mfma_f32_16x16x32_bf16 v[84:87], a[40:43], a[144:147], v[84:87]\n"
"ds_read_b64_tr_b16 a[80:81], v15 offset:0\n"
"v_mfma_f32_16x16x32_bf16 a[4:7], a[40:43], a[152:155], a[4:7]\n"
"s_sub_u32 s67, 1931264, s75\n"
"ds_read_b64_tr_b16 a[82:83], v15 offset:512\n"
"v_mfma_f32_16x16x32_bf16 a[4:7], a[44:47], a[156:159], a[4:7]\n"
"ds_read_b64_tr_b16 a[84:85], v15 offset:4096\n"
"v_mfma_f32_16x16x32_bf16 a[0:3], a[36:39], a[156:159], a[0:3]\n"
"ds_read_b64_tr_b16 a[86:87], v15 offset:4608\n"
"buffer_load_dwordx4 v11, s[52:55], s67 offen offset:2048  lds\n"
"v_mfma_f32_16x16x32_bf16 a[0:3], a[32:35], a[152:155], a[0:3]\n"
"ds_read_b64_tr_b16 a[88:89], v15 offset:8\n"
"v_mfma_f32_16x16x32_bf16 v[88:91], a[48:51], a[96:99], v[88:91]\n"
"s_sub_u32 s67, 1946624, s75\n"
"ds_read_b64_tr_b16 a[90:91], v15 offset:520\n"
"v_mfma_f32_16x16x32_bf16 v[88:91], a[52:55], a[100:103], v[88:91]\n"
"ds_read_b64_tr_b16 a[92:93], v15 offset:4104\n"
"v_mfma_f32_16x16x32_bf16 v[116:119], a[60:63], a[100:103], v[116:119]\n"
"ds_read_b64_tr_b16 a[94:95], v15 offset:4616\n"
"v_mfma_f32_16x16x32_bf16 v[116:119], a[56:59], a[96:99], v[116:119]\n"
"buffer_load_dwordx4 v11, s[52:55], s67 offen offset:3072  lds\n"
"v_mfma_f32_16x16x32_bf16 v[120:123], a[56:59], a[104:107], v[120:123]\n"
"v_mfma_f32_16x16x32_bf16 v[120:123], a[60:63], a[108:111], v[120:123]\n"
"s_add_u32 m0, s64, 0\n"
"v_mfma_f32_16x16x32_bf16 v[92:95], a[52:55], a[108:111], v[92:95]\n"
"v_mfma_f32_16x16x32_bf16 v[92:95], a[48:51], a[104:107], v[92:95]\n"
"s_sub_u32 s67, 1835136, s75\n"
"v_mfma_f32_16x16x32_bf16 v[96:99], a[48:51], a[112:115], v[96:99]\n"
"s_waitcnt lgkmcnt(0)\n"
"s_mov_b32 s99, 0\n"
"v_mfma_f32_16x16x32_bf16 v[96:99], a[52:55], a[116:119], v[96:99]\n"
"s_mov_b32 s98, 0\n"
"buffer_load_dwordx4 v11, s[52:55], s67 offen offset:0  lds\n"
"v_mfma_f32_16x16x32_bf16 v[124:127], a[60:63], a[116:119], v[124:127]\n"
"s_memtime s[76:77]\n"
"s_load_dword s98, s[96:97], 128 glc\n"
"v_mfma_f32_16x16x32_bf16 v[124:127], a[56:59], a[112:115], v[124:127]\n"
"s_sub_u32 s67, 1850496, s75\n"
"v_mfma_f32_16x16x32_bf16 v[128:131], a[56:59], a[120:123], v[128:131]\n"
"v_mfma_f32_16x16x32_bf16 v[128:131], a[60:63], a[124:127], v[128:131]\n"
"v_mfma_f32_16x16x32_bf16 v[100:103], a[52:55], a[124:127], v[100:103]\n"
"buffer_load_dwordx4 v11, s[52:55], s67 offen offset:1024  lds\n"
"v_mfma_f32_16x16x32_bf16 v[100:103], a[48:51], a[120:123], v[100:103]\n"
"v_mfma_f32_16x16x32_bf16 v[104:107], a[48:51], a[128:131], v[104:107]\n"
"s_sub_u32 s67, 1865856, s75\n"
"v_mfma_f32_16x16x32_bf16 v[104:107], a[52:55], a[132:135], v[104:107]\n"
"v_mfma_f32_16x16x32_bf16 v[132:135], a[60:63], a[132:135], v[132:135]\n"
"buffer_load_dwordx4 v11, s[52:55], s67 offen offset:2048  lds\n"
"v_mfma_f32_16x16x32_bf16 v[132:135], a[56:59], a[128:131], v[132:135]\n"
"s_mov_b32 s29, s76\n"
"v_mfma_f32_16x16x32_bf16 v[136:139], a[56:59], a[136:139], v[136:139]\n"
"s_sub_u32 s67, 1881216, s75\n"
"v_mfma_f32_16x16x32_bf16 v[136:139], a[60:63], a[140:143], v[136:139]\n"
"v_mfma_f32_16x16x32_bf16 v[108:111], a[52:55], a[140:143], v[108:111]\n"
"s_sub_u32 s99, s99, s98\n"
"s_sub_u32 s99, s99, s98\n"
"v_mfma_f32_16x16x32_bf16 v[108:111], a[48:51], a[136:139], v[108:111]\n"
"s_sub_u32 s99, s99, s98\n"
"buffer_load_dwordx4 v11, s[52:55], s67 offen offset:3072  lds\n"
"v_mfma_f32_16x16x32_bf16 v[112:115], a[48:51], a[144:147], v[112:115]\n"
"s_sub_u32 s99, s99, s98\n"
"s_add_u32 m0, s64, 4096\n"
"v_mfma_f32_16x16x32_bf16 v[112:115], a[52:55], a[148:151], v[112:115]\n"
"v_mfma_f32_16x16x32_bf16 v[140:143], a[60:63], a[148:151], v[140:143]\n"
"s_sub_u32 s99, s99, s98\n"
"s_sub_u32 s99, s99, s98\n"
"buffer_load_dwordx4 v[24:27], v0, s[56:59], %25 offen sc1\n"
"v_mfma_f32_16x16x32_bf16 v[140:143], a[56:59], a[144:147], v[140:143]\n"
"s_sub_u32 s99, s99, s98\n"
"s_sub_u32 s67, 1900672, s75\n"
"v_mfma_f32_16x16x32_bf16 a[12:15], a[56:59], a[152:155], a[12:15]\n"
"s_sub_u32 s99, s99, s98\n"
"buffer_load_dwordx4 v11, s[52:55], s67 offen offset:0  lds\n"
"v_mfma_f32_16x16x32_bf16 a[12:15], a[60:63], a[156:159], a[12:15]\n"
"s_sub_u32 s67, 1916032, s75\n"
"v_mfma_f32_16x16x32_bf16 a[8:11], a[52:55], a[156:159], a[8:11]\n"
"s_sub_u32 s99, s99, s98\n"
"s_sub_u32 s99, s99, s98\n"
"v_mfma_f32_16x16x32_bf16 a[8:11], a[48:51], a[152:155], a[8:11]\n"
"s_sub_u32 s99, s99, s98\n"
"v_mfma_f32_16x16x32_bf16 v[144:147], a[64:67], a[96:99], v[144:147]\n"
"s_sub_u32 s99, s99, s98\n"
"v_mfma_f32_16x16x32_bf16 v[144:147], a[68:71], a[100:103], v[144:147]\n"
"buffer_load_dwordx4 v11, s[52:55], s67 offen offset:1024  lds\n"
"v_mfma_f32_16x16x32_bf16 v[172:175], a[76:79], a[100:103], v[172:175]\n"
"s_sub_u32 s99, s99, s98\n"
"s_sub_u32 s99, s99, s98\n"
"v_mfma_f32_16x16x32_bf16 v[172:175], a[72:75], a[96:99], v[172:175]\n"
"s_sub_u32 s99, s99, s98\n"
"s_sub_u32 s67, 1931392, s75\n"
"v_mfma_f32_16x16x32_bf16 v[176:179], a[72:75], a[104:107], v[176:179]\n"
"s_sub_u32 s99, s99, s98\n"
"v_mfma_f32_16x16x32_bf16 v[176:179], a[76:79], a[108:111], v[176:179]\n"
"buffer_load_dwordx4 v11, s[52:55], s67 offen offset:2048  lds\n"
"v_mfma_f32_16x16x32_bf16 v[148:151], a[68:71], a[108:111], v[148:151]\n"
"s_sub_u32 s99, s99, s98\n"
"s_sub_u32 s99, s99, s98\n"
"v_mfma_f32_16x16x32_bf16 v[148:151], a[64:67], a[104:107], v[148:151]\n"
"s_sub_u32 s99, s99, s98\n"
"s_sub_u32 s71, s64, s64\n"
"s_sub_u32 s67, 1946752, s75\n"
"v_mfma_f32_16x16x32_bf16 v[152:155], a[64:67], a[112:115], v[152:155]\n"
"s_sub_u32 s66, 122880, s73\n"
"s_sub_u32 s99, s99, s98\n"
"v_mfma_f32_16x16x32_bf16 v[152:155], a[68:71], a[116:119], v[152:155]\n"
"s_add_u32 s71, s71, 16384\n"
"buffer_load_dwordx4 v11, s[52:55], s67 offen offset:3072  lds\n"
"v_mfma_f32_16x16x32_bf16 v[180:183], a[76:79], a[116:119], v[180:183]\n"
"s_sub_u32 s99, s99, s98\n"
"v_add_u32 v12, s66, v7\n"
"s_sub_u32 s99, s99, s98\n"
"v_mfma_f32_16x16x32_bf16 v[180:183], a[72:75], a[112:115], v[180:183]\n"
"s_sub_u32 s99, s99, s98\n"
"v_min_u32 v19, 496, v0\n"
"v_mfma_f32_16x16x32_bf16 v[184:187], a[72:75], a[120:123], v[184:187]\n"
"s_sub_u32 s99, s99, s98\n"
"v_add_u32 v13, s66, v8\n"
"s_add_u32 s70, %21, 0\n"
"v_mfma_f32_16x16x32_bf16 v[184:187], a[76:79], a[124:127], v[184:187]\n"
"s_memtime s[76:77]\n"
"v_add_u32 v20, s71, v7\n"
"v_lshl_add_u32 v19, v19, 7, s70\n"
"v_mfma_f32_16x16x32_bf16 v[156:159], a[68:71], a[124:127], v[156:159]\n"
"buffer_load_dword v23, v19, s[52:55], 0 offen\n"
"v_mfma_f32_16x16x32_bf16 v[156:159], a[64:67], a[120:123], v[156:159]\n"
"s_waitcnt lgkmcnt(0)\n"
"v_add_u32 v21, s71, v8\n"
"v_min_u32 v19, 496, v0\n"
"v_mfma_f32_16x16x32_bf16 v[160:163], a[64:67], a[128:131], v[160:163]\n"
"s_sub_u32 s29, s76, s29\n"
"s_add_u32 s70, %21, 2097152\n"
"v_lshl_add_u32 v19, v19, 7, s70\n"
"v_mfma_f32_16x16x32_bf16 v[160:163], a[68:71], a[132:135], v[160:163]\n"
"s_cmp_lt_u32 s29, 620\n"
"s_cselect_b32 s99, s99, 0\n"
"buffer_load_dword v23, v19, s[52:55], 0 offen\n"
"v_mfma_f32_16x16x32_bf16 v[188:191], a[76:79], a[132:135], v[188:191]\n"
"s_max_u32 s30, s30, s99\n"
"v_mfma_f32_16x16x32_bf16 v[188:191], a[72:75], a[128:131], v[188:191]\n"
"s_waitcnt vmcnt(19)\n"
"ds_read_b128 a[240:243], v20 offset:0\n"
"v_mfma_f32_16x16x32_bf16 v[192:195], a[72:75], a[136:139], v[192:195]\n"
"ds_read_b128 a[244:247], v21 offset:0\n"
"v_mfma_f32_16x16x32_bf16 v[192:195], a[76:79], a[140:143], v[192:195]\n"
"ds_read_b128 a[248:251], v20 offset:2048\n"
"v_mfma_f32_16x16x32_bf16 v[164:167], a[68:71], a[140:143], v[164:167]\n"
"v_mfma_f32_16x16x32_bf16 v[164:167], a[64:67], a[136:139], v[164:167]\n"
"ds_read_b128 a[252:255], v21 offset:2048\n"
"v_mfma_f32_16x16x32_bf16 v[168:171], a[64:67], a[144:147], v[168:171]\n"
"v_mfma_f32_16x16x32_bf16 v[168:171], a[68:71], a[148:151], v[168:171]\n"
"v_mfma_f32_16x16x32_bf16 v[196:199], a[76:79], a[148:151], v[196:199]\n"
"v_mfma_f32_16x16x32_bf16 v[196:199], a[72:75], a[144:147], v[196:199]\n"
"v_mfma_f32_16x16x32_bf16 a[20:23], a[72:75], a[152:155], a[20:23]\n"
"v_mfma_f32_16x16x32_bf16 a[20:23], a[76:79], a[156:159], a[20:23]\n"
"s_waitcnt lgkmcnt(0)\n"
"v_mfma_f32_16x16x32_bf16 a[16:19], a[68:71], a[156:159], a[16:19]\n"
"v_mfma_f32_16x16x32_bf16 a[16:19], a[64:67], a[152:155], a[16:19]\n"
"s_barrier\n"
"s_waitcnt vmcnt(11)\n"
"ds_read_b64_tr_b16 a[32:33], v9 offset:8192\n"
"v_mfma_f32_16x16x32_bf16 v[200:203], a[80:83], a[96:99], v[200:203]\n"
"ds_read_b64_tr_b16 a[34:35], v9 offset:8704\n"
"v_mfma_f32_16x16x32_bf16 v[200:203], a[84:87], a[100:103], v[200:203]\n"
"ds_read_b64_tr_b16 a[36:37], v9 offset:12288\n"
"v_mfma_f32_16x16x32_bf16 v[228:231], a[92:95], a[100:103], v[228:231]\n"
"ds_read_b128 a[100:103], v13 offset:32768\n"
"v_mfma_f32_16x16x32_bf16 v[228:231], a[88:91], a[96:99], v[228:231]\n"
"ds_read_b128 a[96:99], v12 offset:32768\n"
"v_mfma_f32_16x16x32_bf16 v[232:235], a[88:91], a[104:107], v[232:235]\n"
"ds_read_b64_tr_b16 a[38:39], v9 offset:12800\n"
"v_mfma_f32_16x16x32_bf16 v[232:235], a[92:95], a[108:111], v[232:235]\n"
"ds_read_b64_tr_b16 a[40:41], v9 offset:8200\n"
"s_getpc_b64 s[70:71]\n"
"L_c11_:\n"
"s_add_u32 s70, s70, L_P6-L_c11_\n"
"s_addc_u32 s71, s71, 0\n"
"v_mfma_f32_16x16x32_bf16 v[204:207], a[84:87], a[108:111], v[204:207]\n"
"ds_read_b128 a[108:111], v13 offset:34816\n"
"v_mfma_f32_16x16x32_bf16 v[204:207], a[80:83], a[104:107], v[204:207]\n"
"s_lshr_b32 s66, %26, 3\n"
"ds_read_b128 a[104:107], v12 offset:34816\n"
"v_mfma_f32_16x16x32_bf16 v[208:211], a[80:83], a[112:115], v[208:211]\n"
"ds_read_b64_tr_b16 a[42:43], v9 offset:8712\n"
"v_mfma_f32_16x16x32_bf16 v[208:211], a[84:87], a[116:119], v[208:211]\n"
"ds_read_b64_tr_b16 a[44:45], v9 offset:12296\n"
"v_mfma_f32_16x16x32_bf16 v[236:239], a[92:95], a[116:119], v[236:239]\n"
"s_lshl2_add_u32 s66, s66, %16\n"
"ds_read_b128 a[116:119], v13 offset:36864\n"
"v_mfma_f32_16x16x32_bf16 v[236:239], a[88:91], a[112:115], v[236:239]\n"
"ds_read_b128 a[112:115], v12 offset:36864\n"
"v_mfma_f32_16x16x32_bf16 v[240:243], a[88:91], a[120:123], v[240:243]\n"
"ds_read_b64_tr_b16 a[46:47], v9 offset:12808\n"
"v_mfma_f32_16x16x32_bf16 v[240:243], a[92:95], a[124:127], v[240:243]\n"
"s_mul_i32 s66, s66, 384\n"
"ds_read_b64_tr_b16 a[48:49], v15 offset:8192\n"
"v_mfma_f32_16x16x32_bf16 v[212:215], a[84:87], a[124:127], v[212:215]\n"
"ds_read_b128 a[124:127], v13 offset:38912\n"
"v_mfma_f32_16x16x32_bf16 v[212:215], a[80:83], a[120:123], v[212:215]\n"
"s_min_u32 s66, s66, L_headF-L_P6-384\n"
"ds_read_b128 a[120:123], v12 offset:38912\n"
"v_mfma_f32_16x16x32_bf16 v[216:219], a[80:83], a[128:131], v[216:219]\n"
"ds_read_b64_tr_b16 a[50:51], v15 offset:8704\n"
"v_mfma_f32_16x16x32_bf16 v[216:219], a[84:87], a[132:135], v[216:219]\n"
"ds_read_b64_tr_b16 a[52:53], v15 offset:12288\n"
"v_mfma_f32_16x16x32_bf16 v[244:247], a[92:95], a[132:135], v[244:247]\n"
"ds_read_b128 a[132:135], v13 offset:24576\n"
"v_min_u32 v22, 32, v0\n"
"v_mfma_f32_16x16x32_bf16 v[244:247], a[88:91], a[128:131], v[244:247]\n"
"ds_read_b128 a[128:131], v12 offset:24576\n"
"v_mfma_f32_16x16x32_bf16 v[248:251], a[88:91], a[136:139], v[248:251]\n"
"ds_read_b64_tr_b16 a[54:55], v15 offset:12800\n"
"v_mfma_f32_16x16x32_bf16 v[248:251], a[92:95], a[140:143], v[248:251]\n"
"ds_read_b64_tr_b16 a[56:57], v15 offset:8200\n"
"v_lshl_add_u32 v22, v22, 3, s66\n"
"v_mfma_f32_16x16x32_bf16 v[220:223], a[84:87], a[140:143], v[220:223]\n"
"ds_read_b128 a[140:143], v13 offset:26624\n"
"v_mfma_f32_16x16x32_bf16 v[220:223], a[80:83], a[136:139], v[220:223]\n"
"ds_read_b128 a[136:139], v12 offset:26624\n"
"global_load_dword v23, v22, s[70:71]\n"
"v_mfma_f32_16x16x32_bf16 v[224:227], a[80:83], a[144:147], v[224:227]\n"
"ds_read_b64_tr_b16 a[58:59], v15 offset:8712\n"
"v_mfma_f32_16x16x32_bf16 v[224:227], a[84:87], a[148:151], v[224:227]\n"
"ds_read_b64_tr_b16 a[60:61], v15 offset:12296\n"
"v_mfma_f32_16x16x32_bf16 v[252:255], a[92:95], a[148:151], v[252:255]\n"
"ds_read_b128 a[148:151], v13 offset:28672\n"
"v_mfma_f32_16x16x32_bf16 v[252:255], a[88:91], a[144:147], v[252:255]\n"
"ds_read_b128 a[144:147], v12 offset:28672\n"
"ds_read_b64_tr_b16 a[62:63], v15 offset:12808\n"
"v_mfma_f32_16x16x32_bf16 a[28:31], a[88:91], a[152:155], a[28:31]\n"
"v_mfma_f32_16x16x32_bf16 a[28:31], a[92:95], a[156:159], a[28:31]\n"
"v_mfma_f32_16x16x32_bf16 a[24:27], a[84:87], a[156:159], a[24:27]\n"
"ds_read_b128 a[156:159], v13 offset:30720\n"
"v_mfma_f32_16x16x32_bf16 a[24:27], a[80:83], a[152:155], a[24:27]\n"
"s_waitcnt lgkmcnt(15)\n"
"ds_read_b128 a[152:155], v12 offset:30720\n"
"v_mfma_f32_16x16x32_bf16 v[32:35], a[32:35], a[96:99], v[32:35]\n"
"v_mfma_f32_16x16x32_bf16 v[32:35], a[36:39], a[100:103], v[32:35]\n"
"s_waitcnt lgkmcnt(2)\n"
"v_mfma_f32_16x16x32_bf16 v[60:63], a[44:47], a[100:103], v[60:63]\n"
"v_mfma_f32_16x16x32_bf16 v[60:63], a[40:43], a[96:99], v[60:63]\n"
"v_mfma_f32_16x16x32_bf16 v[64:67], a[40:43], a[104:107], v[64:67]\n"
"ds_read_b128 a[160:163], v7 offset:24576\n"
"v_mfma_f32_16x16x32_bf16 v[64:67], a[44:47], a[108:111], v[64:67]\n"
"v_mfma_f32_16x16x32_bf16 v[36:39], a[36:39], a[108:111], v[36:39]\n"
"ds_read_b128 a[164:167], v8 offset:24576\n"
"v_mfma_f32_16x16x32_bf16 v[36:39], a[32:35], a[104:107], v[36:39]\n"
"v_mfma_f32_16x16x32_bf16 v[40:43], a[32:35], a[112:115], v[40:43]\n"
"ds_read_b128 a[168:171], v7 offset:26624\n"
"v_mfma_f32_16x16x32_bf16 v[40:43], a[36:39], a[116:119], v[40:43]\n"
"ds_read_b128 a[172:175], v8 offset:26624\n"
"v_mfma_f32_16x16x32_bf16 v[68:71], a[44:47], a[116:119], v[68:71]\n"
"s_add_u32 s70, s78, 1\n"
"v_mfma_f32_16x16x32_bf16 v[68:71], a[40:43], a[112:115], v[68:71]\n"
"ds_read_b128 a[176:179], v7 offset:28672\n"
"v_mfma_f32_16x16x32_bf16 v[72:75], a[40:43], a[120:123], v[72:75]\n"
"ds_read_b128 a[180:183], v8 offset:28672\n"
"v_mfma_f32_16x16x32_bf16 v[72:75], a[44:47], a[124:127], v[72:75]\n"
"v_mfma_f32_16x16x32_bf16 v[44:47], a[36:39], a[124:127], v[44:47]\n"
"ds_read_b128 a[184:187], v7 offset:30720\n"
"v_mfma_f32_16x16x32_bf16 v[44:47], a[32:35], a[120:123], v[44:47]\n"
"v_mfma_f32_16x16x32_bf16 v[48:51], a[32:35], a[128:131], v[48:51]\n"
"ds_read_b128 a[188:191], v8 offset:30720\n"
"v_mfma_f32_16x16x32_bf16 v[48:51], a[36:39], a[132:135], v[48:51]\n"
"v_mfma_f32_16x16x32_bf16 v[76:79], a[44:47], a[132:135], v[76:79]\n"
"s_waitcnt vmcnt(3)\n"
"ds_read_b64_tr_b16 a[64:65], v9 offset:0\n"
"v_mfma_f32_16x16x32_bf16 v[76:79], a[40:43], a[128:131], v[76:79]\n"
"ds_read_b64_tr_b16 a[66:67], v9 offset:512\n"
"v_mfma_f32_16x16x32_bf16 v[80:83], a[40:43], a[136:139], v[80:83]\n"
"L_st3225:\n"
"s_cbranch_execz L_retA\n"
"ds_read_b64_tr_b16 a[68:69], v9 offset:4096\n"
"v_mfma_f32_16x16x32_bf16 v[80:83], a[44:47], a[140:143], v[80:83]\n"
"ds_read_b64_tr_b16 a[70:71], v9 offset:4608\n"
"v_mfma_f32_16x16x32_bf16 v[52:55], a[36:39], a[140:143], v[52:55]\n"
"ds_read_b64_tr_b16 a[72:73], v9 offset:8\n"
"v_subrev_u32 v24, s70, v24\n"
"v_mfma_f32_16x16x32_bf16 v[52:55], a[32:35], a[136:139], v[52:55]\n"
"s_waitcnt lgkmcnt(13)\n"
"ds_read_b64_tr_b16 a[74:75], v9 offset:520\n"
"v_xor_b32 v25, s79, v25\n"
"v_mfma_f32_16x16x32_bf16 v[56:59], a[32:35], a[144:147], v[56:59]\n"
"ds_read_b64_tr_b16 a[76:77], v9 offset:4104\n"
"v_subrev_u32 v26, s70, v26\n"
"v_mfma_f32_16x16x32_bf16 v[56:59], a[36:39], a[148:151], v[56:59]\n"
"ds_read_b64_tr_b16 a[78:79], v9 offset:4616\n"
"v_xor_b32 v27, s79, v27\n"
"v_mfma_f32_16x16x32_bf16 v[84:87], a[44:47], a[148:151], v[84:87]\n"
"ds_read_b64_tr_b16 a[80:81], v15 offset:0\n"
"v_or_b32 v24, v24, v26\n"
"v_mfma_f32_16x16x32_bf16 v[84:87], a[40:43], a[144:147], v[84:87]\n"
"ds_read_b64_tr_b16 a[82:83], v15 offset:512\n"
"v_or_b32 v25, v25, v27\n"
"v_mfma_f32_16x16x32_bf16 a[4:7], a[40:43], a[152:155], a[4:7]\n"
"ds_read_b64_tr_b16 a[84:85], v15 offset:4096\n"
"v_or_b32 v24, v24, v25\n"
"v_mfma_f32_16x16x32_bf16 a[4:7], a[44:47], a[156:159], a[4:7]\n"
"ds_read_b64_tr_b16 a[86:87], v15 offset:4608\n"
"v_cmp_lt_u32 vcc, 15, v24\n"
"v_mfma_f32_16x16x32_bf16 a[0:3], a[36:39], a[156:159], a[0:3]\n"
"ds_read_b64_tr_b16 a[88:89], v15 offset:8\n"
"s_cbranch_vccnz L_slow3\n"
"L_back3:\n"
"v_mfma_f32_16x16x32_bf16 a[0:3], a[32:35], a[152:155], a[0:3]\n"
"ds_read_b64_tr_b16 a[90:91], v15 offset:520\n"
"v_mfma_f32_16x16x32_bf16 v[88:91], a[48:51], a[96:99], v[88:91]\n"
"ds_read_b64_tr_b16 a[92:93], v15 offset:4104\n"
"v_mfma_f32_16x16x32_bf16 v[88:91], a[52:55], a[100:103], v[88:91]\n"
"global_load_dwordx4 a[32:35], v0, s[34:35] offset:-4096 sc1\n"
"s_barrier\n"
"v_mfma_f32_16x16x32_bf16 v[116:119], a[60:63], a[100:103], v[116:119]\n"
"ds_read_b64_tr_b16 a[94:95], v15 offset:4616\n"
"v_mfma_f32_16x16x32_bf16 v[116:119], a[56:59], a[96:99], v[116:119]\n"
"ds_write_b64 v2, a[192:193] offset:16384\n"
"v_mfma_f32_16x16x32_bf16 v[120:123], a[56:59], a[104:107], v[120:123]\n"
"v_mfma_f32_16x16x32_bf16 v[120:123], a[60:63], a[108:111], v[120:123]\n"
"ds_write_b64 v2, a[194:195] offset:16392\n"
"v_mfma_f32_16x16x32_bf16 v[92:95], a[52:55], a[108:111], v[92:95]\n"
"v_mfma_f32_16x16x32_bf16 v[92:95], a[48:51], a[104:107], v[92:95]\n"
"ds_write_b64 v2, a[196:197] offset:17408\n"
"v_mfma_f32_16x16x32_bf16 v[96:99], a[48:51], a[112:115], v[96:99]\n"
"s_waitcnt lgkmcnt(3)\n"
"v_mfma_f32_16x16x32_bf16 v[96:99], a[52:55], a[116:119], v[96:99]\n"
"global_load_dwordx4 a[36:39], v0, s[34:35] offset:-3072 sc1\n"
"v_mfma_f32_16x16x32_bf16 v[124:127], a[60:63], a[116:119], v[124:127]\n"
"ds_write_b64 v2, a[198:199] offset:17416\n"
"v_mfma_f32_16x16x32_bf16 v[124:127], a[56:59], a[112:115], v[124:127]\n"
"ds_write_b64 v2, a[200:201] offset:18432\n"
"v_mfma_f32_16x16x32_bf16 v[128:131], a[56:59], a[120:123], v[128:131]\n"
"v_mfma_f32_16x16x32_bf16 v[128:131], a[60:63], a[124:127], v[128:131]\n"
"ds_write_b64 v2, a[202:203] offset:18440\n"
"v_mfma_f32_16x16x32_bf16 v[100:103], a[52:55], a[124:127], v[100:103]\n"
"v_mfma_f32_16x16x32_bf16 v[100:103], a[48:51], a[120:123], v[100:103]\n"
"ds_write_b64 v2, a[204:205] offset:19456\n"
"v_mfma_f32_16x16x32_bf16 v[104:107], a[48:51], a[128:131], v[104:107]\n"
"ds_write_b64 v2, a[206:207] offset:19464\n"
"v_mfma_f32_16x16x32_bf16 v[104:107], a[52:55], a[132:135], v[104:107]\n"
"global_load_dwordx4 a[40:43], v0, s[34:35] offset:-2048 sc1\n"
"v_mfma_f32_16x16x32_bf16 v[132:135], a[60:63], a[132:135], v[132:135]\n"
"ds_write_b64 v2, a[208:209] offset:20480\n"
"v_mfma_f32_16x16x32_bf16 v[132:135], a[56:59], a[128:131], v[132:135]\n"
"v_mfma_f32_16x16x32_bf16 v[136:139], a[56:59], a[136:139], v[136:139]\n"
"ds_write_b64 v2, a[210:211] offset:20488\n"
"v_mfma_f32_16x16x32_bf16 v[136:139], a[60:63], a[140:143], v[136:139]\n"
"v_mfma_f32_16x16x32_bf16 v[108:111], a[52:55], a[140:143], v[108:111]\n"
"ds_write_b64 v2, a[212:213] offset:21504\n"
"v_mfma_f32_16x16x32_bf16 v[108:111], a[48:51], a[136:139], v[108:111]\n"
"v_mfma_f32_16x16x32_bf16 v[112:115], a[48:51], a[144:147], v[112:115]\n"
"ds_write_b64 v2, a[214:215] offset:21512\n"
"v_mfma_f32_16x16x32_bf16 v[112:115], a[52:55], a[148:151], v[112:115]\n"
"global_load_dwordx4 a[44:47], v0, s[34:35] offset:-1024 sc1\n"
"v_mfma_f32_16x16x32_bf16 v[140:143], a[60:63], a[148:151], v[140:143]\n"
"ds_write_b64 v2, a[216:217] offset:22528\n"
"v_mfma_f32_16x16x32_bf16 v[140:143], a[56:59], a[144:147], v[140:143]\n"
"v_mfma_f32_16x16x32_bf16 a[12:15], a[56:59], a[152:155], a[12:15]\n"
"ds_write_b64 v2, a[218:219] offset:22536\n"
"v_mfma_f32_16x16x32_bf16 a[12:15], a[60:63], a[156:159], a[12:15]\n"
"v_mfma_f32_16x16x32_bf16 a[8:11], a[52:55], a[156:159], a[8:11]\n"
"ds_write_b64 v2, a[220:221] offset:23552\n"
"v_mfma_f32_16x16x32_bf16 a[8:11], a[48:51], a[152:155], a[8:11]\n"
"v_mfma_f32_16x16x32_bf16 v[144:147], a[64:67], a[96:99], v[144:147]\n"
"ds_write_b64 v2, a[222:223] offset:23560\n"
"v_mfma_f32_16x16x32_bf16 v[144:147], a[68:71], a[100:103], v[144:147]\n"
"global_load_dwordx4 a[48:51], v0, s[34:35] offset:0 sc1\n"
"v_mfma_f32_16x16x32_bf16 v[172:175], a[76:79], a[100:103], v[172:175]\n"
"ds_write_b64 v2, a[224:225] offset:24576\n"
"v_mfma_f32_16x16x32_bf16 v[172:175], a[72:75], a[96:99], v[172:175]\n"
"ds_write_b64 v2, a[226:227] offset:24584\n"
"v_mfma_f32_16x16x32_bf16 v[176:179], a[72:75], a[104:107], v[176:179]\n"
"v_mfma_f32_16x16x32_bf16 v[176:179], a[76:79], a[108:111], v[176:179]\n"
"ds_write_b64 v2, a[228:229] offset:25600\n"
"v_mfma_f32_16x16x32_bf16 v[148:151], a[68:71], a[108:111], v[148:151]\n"
"v_mfma_f32_16x16x32_bf16 v[148:151], a[64:67], a[104:107], v[148:151]\n"
"ds_write_b64 v2, a[230:231] offset:25608\n"
"v_mfma_f32_16x16x32_bf16 v[152:155], a[64:67], a[112:115], v[152:155]\n"
"ds_write_b64 v2, a[232:233] offset:26624\n"
"v_mfma_f32_16x16x32_bf16 v[152:155], a[68:71], a[116:119], v[152:155]\n"
"global_load_dwordx4 a[52:55], v0, s[34:35] offset:1024 sc1\n"
"v_mfma_f32_16x16x32_bf16 v[180:183], a[76:79], a[116:119], v[180:183]\n"
"v_mfma_f32_16x16x32_bf16 v[180:183], a[72:75], a[112:115], v[180:183]\n"
"ds_write_b64 v2, a[234:235] offset:26632\n"
"v_mfma_f32_16x16x32_bf16 v[184:187], a[72:75], a[120:123], v[184:187]\n"
"v_mfma_f32_16x16x32_bf16 v[184:187], a[76:79], a[124:127], v[184:187]\n"
"ds_write_b64 v2, a[236:237] offset:27648\n"
"v_mfma_f32_16x16x32_bf16 v[156:159], a[68:71], a[124:127], v[156:159]\n"
"v_mfma_f32_16x16x32_bf16 v[156:159], a[64:67], a[120:123], v[156:159]\n"
"ds_write_b64 v2, a[238:239] offset:27656\n"
"v_mfma_f32_16x16x32_bf16 v[160:163], a[64:67], a[128:131], v[160:163]\n"
"ds_write_b64 v2, a[240:241] offset:28672\n"
"v_mfma_f32_16x16x32_bf16 v[160:163], a[68:71], a[132:135], v[160:163]\n"
"global_load_dwordx4 a[56:59], v0, s[34:35] offset:2048 sc1\n"
"v_mfma_f32_16x16x32_bf16 v[188:191], a[76:79], a[132:135], v[188:191]\n"
"ds_write_b64 v2, a[242:243] offset:28680\n"
"v_mfma_f32_16x16x32_bf16 v[188:191], a[72:75], a[128:131], v[188:191]\n"
"v_mfma_f32_16x16x32_bf16 v[192:195], a[72:75], a[136:139], v[192:195]\n"
"ds_write_b64 v2, a[244:245] offset:29696\n"
"v_mfma_f32_16x16x32_bf16 v[192:195], a[76:79], a[140:143], v[192:195]\n"
"v_mfma_f32_16x16x32_bf16 v[164:167], a[68:71], a[140:143], v[164:167]\n"
"ds_write_b64 v2, a[246:247] offset:29704\n"
"v_mfma_f32_16x16x32_bf16 v[164:167], a[64:67], a[136:139], v[164:167]\n"
"v_mfma_f32_16x16x32_bf16 v[168:171], a[64:67], a[144:147], v[168:171]\n"
"ds_write_b64 v2, a[248:249] offset:30720\n"
"v_mfma_f32_16x16x32_bf16 v[168:171], a[68:71], a[148:151], v[168:171]\n"
"global_load_dwordx4 a[60:63], v0, s[34:35] offset:3072 sc1\n"
"v_mfma_f32_16x16x32_bf16 v[196:199], a[76:79], a[148:151], v[196:199]\n"
"ds_write_b64 v2, a[250:251] offset:30728\n"
"v_mfma_f32_16x16x32_bf16 v[196:199], a[72:75], a[144:147], v[196:199]\n"
"v_mfma_f32_16x16x32_bf16 a[20:23], a[72:75], a[152:155], a[20:23]\n"
"s_add_u32 s34, s34, 8192\n"
"ds_write_b64 v2, a[252:253] offset:31744\n"
"v_mfma_f32_16x16x32_bf16 a[20:23], a[76:79], a[156:159], a[20:23]\n"
"v_mfma_f32_16x16x32_bf16 a[16:19], a[68:71], a[156:159], a[16:19]\n"
"ds_write_b64 v2, a[254:255] offset:31752\n"
"v_mfma_f32_16x16x32_bf16 a[16:19], a[64:67], a[152:155], a[16:19]\n"
"v_mfma_f32_16x16x32_bf16 v[200:203], a[80:83], a[96:99], v[200:203]\n"
"v_lshrrev_b32 v16, 4, v0\n"
"v_mfma_f32_16x16x32_bf16 v[200:203], a[84:87], a[100:103], v[200:203]\n"
"global_load_dwordx4 a[64:67], v0, s[34:35] offset:-4096 sc1\n"
"v_mfma_f32_16x16x32_bf16 v[228:231], a[92:95], a[100:103], v[228:231]\n"
"ds_read_b128 a[100:103], v2 offset:17408\n"
"v_mfma_f32_16x16x32_bf16 v[228:231], a[88:91], a[96:99], v[228:231]\n"
"ds_read_b128 a[96:99], v2 offset:16384\n"
"v_mfma_f32_16x16x32_bf16 v[232:235], a[88:91], a[104:107], v[232:235]\n"
"v_and_b32 v16, 0x30, v16\n"
"v_mfma_f32_16x16x32_bf16 v[232:235], a[92:95], a[108:111], v[232:235]\n"
"buffer_load_dwordx2 v[18:19], v16, s[44:47], s61 offen offset:128\n"
"v_mfma_f32_16x16x32_bf16 v[204:207], a[84:87], a[108:111], v[204:207]\n"
"ds_read_b128 a[108:111], v2 offset:19456\n"
"v_mfma_f32_16x16x32_bf16 v[204:207], a[80:83], a[104:107], v[204:207]\n"
"ds_read_b128 a[104:107], v2 offset:18432\n"
"v_mfma_f32_16x16x32_bf16 v[208:211], a[80:83], a[112:115], v[208:211]\n"
"v_lshrrev_b32 v20, 4, v0\n"
"v_mfma_f32_16x16x32_bf16 v[208:211], a[84:87], a[116:119], v[208:211]\n"
"global_load_dwordx4 a[68:71], v0, s[34:35] offset:-3072 sc1\n"
"v_mfma_f32_16x16x32_bf16 v[236:239], a[92:95], a[116:119], v[236:239]\n"
"ds_read_b128 a[116:119], v2 offset:21504\n"
"v_mfma_f32_16x16x32_bf16 v[236:239], a[88:91], a[112:115], v[236:239]\n"
"ds_read_b128 a[112:115], v2 offset:20480\n"
"v_mfma_f32_16x16x32_bf16 v[240:243], a[88:91], a[120:123], v[240:243]\n"
"v_and_b32 v20, 0x30, v20\n"
"v_mfma_f32_16x16x32_bf16 v[240:243], a[92:95], a[124:127], v[240:243]\n"
"buffer_load_dwordx2 v[22:23], v20, s[44:47], s61 offen offset:136\n"
"v_mfma_f32_16x16x32_bf16 v[212:215], a[84:87], a[124:127], v[212:215]\n"
"ds_read_b128 a[124:127], v2 offset:23552\n"
"v_mfma_f32_16x16x32_bf16 v[212:215], a[80:83], a[120:123], v[212:215]\n"
"ds_read_b128 a[120:123], v2 offset:22528\n"
"v_mfma_f32_16x16x32_bf16 v[216:219], a[80:83], a[128:131], v[216:219]\n"
"v_lshrrev_b32 v24, 4, v0\n"
"v_mfma_f32_16x16x32_bf16 v[216:219], a[84:87], a[132:135], v[216:219]\n"
"global_load_dwordx4 a[72:75], v0, s[34:35] offset:-2048 sc1\n"
"v_mfma_f32_16x16x32_bf16 v[244:247], a[92:95], a[132:135], v[244:247]\n"
"ds_read_b128 a[132:135], v2 offset:25600\n"
"v_and_b32 v24, 0x30, v24\n"
"v_mfma_f32_16x16x32_bf16 v[244:247], a[88:91], a[128:131], v[244:247]\n"
"ds_read_b128 a[128:131], v2 offset:24576\n"
"v_mfma_f32_16x16x32_bf16 v[248:251], a[88:91], a[136:139], v[248:251]\n"
"buffer_load_dwordx2 v[26:27], v24, s[44:47], s61 offen offset:192\n"
"v_mfma_f32_16x16x32_bf16 v[248:251], a[92:95], a[140:143], v[248:251]\n"
"v_lshrrev_b32 v28, 4, v0\n"
"v_mfma_f32_16x16x32_bf16 v[220:223], a[84:87], a[140:143], v[220:223]\n"
"ds_read_b128 a[140:143], v2 offset:27648\n"
"v_mfma_f32_16x16x32_bf16 v[220:223], a[80:83], a[136:139], v[220:223]\n"
"ds_read_b128 a[136:139], v2 offset:26624\n"
"v_mfma_f32_16x16x32_bf16 v[224:227], a[80:83], a[144:147], v[224:227]\n"
"v_and_b32 v28, 0x30, v28\n"
"v_mfma_f32_16x16x32_bf16 v[224:227], a[84:87], a[148:151], v[224:227]\n"
"global_load_dwordx4 a[76:79], v0, s[34:35] offset:-1024 sc1\n"
"v_mfma_f32_16x16x32_bf16 v[252:255], a[92:95], a[148:151], v[252:255]\n"
"ds_read_b128 a[148:151], v2 offset:29696\n"
"v_mfma_f32_16x16x32_bf16 v[252:255], a[88:91], a[144:147], v[252:255]\n"
"ds_read_b128 a[144:147], v2 offset:28672\n"
"v_mfma_f32_16x16x32_bf16 a[28:31], a[88:91], a[152:155], a[28:31]\n"
"buffer_load_dwordx2 v[30:31], v28, s[44:47], s61 offen offset:200\n"
"v_mfma_f32_16x16x32_bf16 a[28:31], a[92:95], a[156:159], a[28:31]\n"
"v_mfma_f32_16x16x32_bf16 a[24:27], a[84:87], a[156:159], a[24:27]\n"
"ds_read_b128 a[156:159], v2 offset:31744\n"
"v_mfma_f32_16x16x32_bf16 a[24:27], a[80:83], a[152:155], a[24:27]\n"
"s_waitcnt vmcnt(12) lgkmcnt(11)\n"
"ds_read_b128 a[152:155], v2 offset:30720\n"
"v_mfma_f32_16x16x32_bf16 v[32:35], a[32:35], a[96:99], v[32:35]\n"
"v_mfma_f32_16x16x32_bf16 v[32:35], a[36:39], a[100:103], v[32:35]\n"
"global_load_dwordx4 a[80:83], v0, s[34:35] offset:0 sc1\n"
"v_mfma_f32_16x16x32_bf16 v[60:63], a[44:47], a[100:103], v[60:63]\n"
"s_barrier\n"
"v_mfma_f32_16x16x32_bf16 v[60:63], a[40:43], a[96:99], v[60:63]\n"
"v_mfma_f32_16x16x32_bf16 v[64:67], a[40:43], a[104:107], v[64:67]\n"
"v_mfma_f32_16x16x32_bf16 v[64:67], a[44:47], a[108:111], v[64:67]\n"
"v_mfma_f32_16x16x32_bf16 v[36:39], a[36:39], a[108:111], v[36:39]\n"
"v_mfma_f32_16x16x32_bf16 v[36:39], a[32:35], a[104:107], v[36:39]\n"
"s_waitcnt lgkmcnt(8)\n"
"v_mfma_f32_16x16x32_bf16 v[40:43], a[32:35], a[112:115], v[40:43]\n"
"v_mfma_f32_16x16x32_bf16 v[40:43], a[36:39], a[116:119], v[40:43]\n"
"global_load_dwordx4 a[84:87], v0, s[34:35] offset:1024 sc1\n"
"v_mfma_f32_16x16x32_bf16 v[68:71], a[44:47], a[116:119], v[68:71]\n"
"v_mfma_f32_16x16x32_bf16 v[68:71], a[40:43], a[112:115], v[68:71]\n"
"v_mfma_f32_16x16x32_bf16 v[72:75], a[40:43], a[120:123], v[72:75]\n"
"v_mfma_f32_16x16x32_bf16 v[72:75], a[44:47], a[124:127], v[72:75]\n"
"s_add_u32 s61, s61, 2048\n"
"v_mfma_f32_16x16x32_bf16 v[44:47], a[36:39], a[124:127], v[44:47]\n"
"v_mfma_f32_16x16x32_bf16 v[44:47], a[32:35], a[120:123], v[44:47]\n"
"s_waitcnt lgkmcnt(4)\n"
"v_mfma_f32_16x16x32_bf16 v[48:51], a[32:35], a[128:131], v[48:51]\n"
"s_waitcnt vmcnt(8)\n"
"v_lshlrev_b32 v16, 16, v18\n"
"v_mfma_f32_16x16x32_bf16 v[48:51], a[36:39], a[132:135], v[48:51]\n"
"global_load_dwordx4 a[88:91], v0, s[34:35] offset:2048 sc1\n"
"v_and_b32 v17, s63, v18\n"
"v_mfma_f32_16x16x32_bf16 v[76:79], a[44:47], a[132:135], v[76:79]\n"
"v_lshlrev_b32 v18, 16, v19\n"
"v_and_b32 v19, s63, v19\n"
"v_mfma_f32_16x16x32_bf16 v[76:79], a[40:43], a[128:131], v[76:79]\n"
"v_add_f32 v144, v144, v16\n"
"v_mfma_f32_16x16x32_bf16 v[80:83], a[40:43], a[136:139], v[80:83]\n"
"v_add_f32 v145, v145, v17\n"
"v_add_f32 v146, v146, v18\n"
"v_mfma_f32_16x16x32_bf16 v[80:83], a[44:47], a[140:143], v[80:83]\n"
"v_add_f32 v147, v147, v19\n"
"v_mfma_f32_16x16x32_bf16 v[52:55], a[36:39], a[140:143], v[52:55]\n"
"v_add_f32 v148, v148, v16\n"
"v_mfma_f32_16x16x32_bf16 v[52:55], a[32:35], a[136:139], v[52:55]\n"
"s_mov_b32 s99, 0\n"
"v_add_f32 v149, v149, v17\n"
"s_waitcnt lgkmcnt(0)\n"
"v_mfma_f32_16x16x32_bf16 v[56:59], a[32:35], a[144:147], v[56:59]\n"
"s_mov_b32 s98, 0\n"
"v_add_f32 v150, v150, v18\n"
"v_add_f32 v151, v151, v19\n"
"v_mfma_f32_16x16x32_bf16 v[56:59], a[36:39], a[148:151], v[56:59]\n"
"global_load_dwordx4 a[92:95], v0, s[34:35] offset:3072 sc1\n"
"s_memtime s[76:77]\n"
"s_load_dword s98, s[96:97], 8448 glc\n"
"v_mfma_f32_16x16x32_bf16 v[84:87], a[44:47], a[148:151], v[84:87]\n"
"v_add_f32 v152, v152, v16\n"
"v_add_f32 v153, v153, v17\n"
"v_mfma_f32_16x16x32_bf16 v[84:87], a[40:43], a[144:147], v[84:87]\n"
"v_add_f32 v154, v154, v18\n"
"v_add_f32 v155, v155, v19\n"
"v_mfma_f32_16x16x32_bf16 a[4:7], a[40:43], a[152:155], a[4:7]\n"
"s_add_u32 s34, s34, 8192\n"
"v_add_f32 v156, v156, v16\n"
"v_mfma_f32_16x16x32_bf16 a[4:7], a[44:47], a[156:159], a[4:7]\n"
"v_add_f32 v157, v157, v17\n"
"v_mfma_f32_16x16x32_bf16 a[0:3], a[36:39], a[156:159], a[0:3]\n"
"v_add_f32 v158, v158, v18\n"
"v_add_f32 v159, v159, v19\n"
"v_mfma_f32_16x16x32_bf16 a[0:3], a[32:35], a[152:155], a[0:3]\n"
"v_add_f32 v160, v160, v16\n"
"v_mfma_f32_16x16x32_bf16 v[88:91], a[48:51], a[96:99], v[88:91]\n"
"v_add_f32 v161, v161, v17\n"
"v_add_f32 v162, v162, v18\n"
"v_mfma_f32_16x16x32_bf16 v[88:91], a[52:55], a[100:103], v[88:91]\n"
"global_load_dwordx4 a[32:35], v0, s[34:35] offset:-4096 sc1\n"
"v_add_f32 v163, v163, v19\n"
"v_mfma_f32_16x16x32_bf16 v[116:119], a[60:63], a[100:103], v[116:119]\n"
"s_mov_b32 s29, s76\n"
"ds_read_b128 a[100:103], v3 offset:17408\n"
"v_add_f32 v164, v164, v16\n"
"v_mfma_f32_16x16x32_bf16 v[116:119], a[56:59], a[96:99], v[116:119]\n"
"ds_read_b128 a[96:99], v3 offset:16384\n"
"v_add_f32 v165, v165, v17\n"
"v_mfma_f32_16x16x32_bf16 v[120:123], a[56:59], a[104:107], v[120:123]\n"
"s_sub_u32 s99, s99, s98\n"
"v_add_f32 v166, v166, v18\n"
"v_add_f32 v167, v167, v19\n"
"v_mfma_f32_16x16x32_bf16 v[120:123], a[60:63], a[108:111], v[120:123]\n"
"s_sub_u32 s99, s99, s98\n"
"v_add_f32 v168, v168, v16\n"
"v_add_f32 v169, v169, v17\n"
"v_mfma_f32_16x16x32_bf16 v[92:95], a[52:55], a[108:111], v[92:95]\n"
"s_sub_u32 s99, s99, s98\n"
"ds_read_b128 a[108:111], v3 offset:19456\n"
"v_add_f32 v170, v170, v18\n"
"v_mfma_f32_16x16x32_bf16 v[92:95], a[48:51], a[104:107], v[92:95]\n"
"s_sub_u32 s99, s99, s98\n"
"ds_read_b128 a[104:107], v3 offset:18432\n"
"v_add_f32 v171, v171, v19\n"
"v_mfma_f32_16x16x32_bf16 v[96:99], a[48:51], a[112:115], v[96:99]\n"
"s_sub_u32 s99, s99, s98\n"
"v_accvgpr_read_b32 v12, a16\n"
"v_accvgpr_read_b32 v13, a17\n"
"v_mfma_f32_16x16x32_bf16 v[96:99], a[52:55], a[116:119], v[96:99]\n"
"global_load_dwordx4 a[36:39], v0, s[34:35] offset:-3072 sc1\n"
"s_sub_u32 s99, s99, s98\n"
"v_mfma_f32_16x16x32_bf16 v[124:127], a[60:63], a[116:119], v[124:127]\n"
"s_sub_u32 s99, s99, s98\n"
"ds_read_b128 a[116:119], v3 offset:21504\n"
"v_accvgpr_read_b32 v14, a18\n"
"v_mfma_f32_16x16x32_bf16 v[124:127], a[56:59], a[112:115], v[124:127]\n"
"s_sub_u32 s99, s99, s98\n"
"ds_read_b128 a[112:115], v3 offset:20480\n"
"v_accvgpr_read_b32 v15, a19\n"
"v_mfma_f32_16x16x32_bf16 v[128:131], a[56:59], a[120:123], v[128:131]\n"
"s_sub_u32 s99, s99, s98\n"
"v_add_f32 v12, v12, v16\n"
"v_add_f32 v13, v13, v17\n"
"v_mfma_f32_16x16x32_bf16 v[128:131], a[60:63], a[124:127], v[128:131]\n"
"s_sub_u32 s99, s99, s98\n"
"v_add_f32 v14, v14, v18\n"
"v_add_f32 v15, v15, v19\n"
"v_mfma_f32_16x16x32_bf16 v[100:103], a[52:55], a[124:127], v[100:103]\n"
"s_sub_u32 s99, s99, s98\n"
"ds_read_b128 a[124:127], v3 offset:23552\n"
"v_accvgpr_write_b32 a16, v12\n"
"v_mfma_f32_16x16x32_bf16 v[100:103], a[48:51], a[120:123], v[100:103]\n"
"s_sub_u32 s99, s99, s98\n"
"ds_read_b128 a[120:123], v3 offset:22528\n"
"v_accvgpr_write_b32 a17, v13\n"
"v_mfma_f32_16x16x32_bf16 v[104:107], a[48:51], a[128:131], v[104:107]\n"
"s_sub_u32 s99, s99, s98\n"
"v_accvgpr_write_b32 a18, v14\n"
"v_accvgpr_write_b32 a19, v15\n"
"v_mfma_f32_16x16x32_bf16 v[104:107], a[52:55], a[132:135], v[104:107]\n"
"s_sub_u32 s99, s99, s98\n"
"s_waitcnt vmcnt(10)\n"
"global_load_dwordx4 a[40:43], v0, s[34:35] offset:-2048 sc1\n"
"v_mfma_f32_16x16x32_bf16 v[132:135], a[60:63], a[132:135], v[132:135]\n"
"s_sub_u32 s99, s99, s98\n"
"ds_read_b128 a[132:135], v3 offset:25600\n"
"v_lshlrev_b32 v20, 16, v22\n"
"v_mfma_f32_16x16x32_bf16 v[132:135], a[56:59], a[128:131], v[132:135]\n"
"s_sub_u32 s99, s99, s98\n"
"ds_read_b128 a[128:131], v3 offset:24576\n"
"v_and_b32 v21, s63, v22\n"
"v_mfma_f32_16x16x32_bf16 v[136:139], a[56:59], a[136:139], v[136:139]\n"
"s_sub_u32 s99, s99, s98\n"
"v_lshlrev_b32 v22, 16, v23\n"
"v_and_b32 v23, s63, v23\n"
"v_mfma_f32_16x16x32_bf16 v[136:139], a[60:63], a[140:143], v[136:139]\n"
"s_sub_u32 s99, s99, s98\n"
"v_add_f32 v172, v172, v20\n"
"v_add_f32 v173, v173, v21\n"
"v_mfma_f32_16x16x32_bf16 v[108:111], a[52:55], a[140:143], v[108:111]\n"
"s_sub_u32 s99, s99, s98\n"
"ds_read_b128 a[140:143], v3 offset:27648\n"
"v_add_f32 v174, v174, v22\n"
"v_mfma_f32_16x16x32_bf16 v[108:111], a[48:51], a[136:139], v[108:111]\n"
"s_sub_u32 s99, s99, s98\n"
"ds_read_b128 a[136:139], v3 offset:26624\n"
"v_add_f32 v175, v175, v23\n"
"v_mfma_f32_16x16x32_bf16 v[112:115], a[48:51], a[144:147], v[112:115]\n"
"s_sub_u32 s99, s99, s98\n"
"v_add_f32 v176, v176, v20\n"
"v_add_f32 v177, v177, v21\n"
"v_mfma_f32_16x16x32_bf16 v[112:115], a[52:55], a[148:151], v[112:115]\n"
"global_load_dwordx4 a[44:47], v0, s[34:35] offset:-1024 sc1\n"
"s_sub_u32 s99, s99, s98\n"
"v_mfma_f32_16x16x32_bf16 v[140:143], a[60:63], a[148:151], v[140:143]\n"
"s_sub_u32 s99, s99, s98\n"
"ds_read_b128 a[148:151], v3 offset:29696\n"
"v_add_f32 v178, v178, v22\n"
"v_mfma_f32_16x16x32_bf16 v[140:143], a[56:59], a[144:147], v[140:143]\n"
"s_sub_u32 s99, s99, s98\n"
"ds_read_b128 a[144:147], v3 offset:28672\n"
"v_add_f32 v179, v179, v23\n"
"v_mfma_f32_16x16x32_bf16 a[12:15], a[56:59], a[152:155], a[12:15]\n"
"s_memtime s[76:77]\n"
"v_add_f32 v180, v180, v20\n"
"v_add_f32 v181, v181, v21\n"
"v_mfma_f32_16x16x32_bf16 a[12:15], a[60:63], a[156:159], a[12:15]\n"
"v_add_f32 v182, v182, v22\n"
"v_add_f32 v183, v183, v23\n"
"v_mfma_f32_16x16x32_bf16 a[8:11], a[52:55], a[156:159], a[8:11]\n"
"ds_read_b128 a[156:159], v3 offset:31744\n"
"v_add_f32 v184, v184, v20\n"
"v_mfma_f32_16x16x32_bf16 a[8:11], a[48:51], a[152:155], a[8:11]\n"
"s_waitcnt vmcnt(9) lgkmcnt(11)\n"
"ds_read_b128 a[152:155], v3 offset:30720\n"
"v_add_f32 v185, v185, v21\n"
"v_mfma_f32_16x16x32_bf16 v[32:35], a[64:67], a[96:99], v[32:35]\n"
"v_add_f32 v186, v186, v22\n"
"v_add_f32 v187, v187, v23\n"
"v_mfma_f32_16x16x32_bf16 v[32:35], a[68:71], a[100:103], v[32:35]\n"
"global_load_dwordx4 a[48:51], v0, s[34:35] offset:0 sc1\n"
"v_add_f32 v188, v188, v20\n"
"v_mfma_f32_16x16x32_bf16 v[60:63], a[76:79], a[100:103], v[60:63]\n"
"v_add_f32 v189, v189, v21\n"
"v_mfma_f32_16x16x32_bf16 v[60:63], a[72:75], a[96:99], v[60:63]\n"
"v_add_f32 v190, v190, v22\n"
"v_add_f32 v191, v191, v23\n"
"v_mfma_f32_16x16x32_bf16 v[64:67], a[72:75], a[104:107], v[64:67]\n"
"v_add_f32 v192, v192, v20\n"
"v_mfma_f32_16x16x32_bf16 v[64:67], a[76:79], a[108:111], v[64:67]\n"
"v_add_f32 v193, v193, v21\n"
"v_mfma_f32_16x16x32_bf16 v[36:39], a[68:71], a[108:111], v[36:39]\n"
"v_add_f32 v194, v194, v22\n"
"v_add_f32 v195, v195, v23\n"
"v_mfma_f32_16x16x32_bf16 v[36:39], a[64:67], a[104:107], v[36:39]\n"
"s_waitcnt lgkmcnt(8)\n"
"v_add_f32 v196, v196, v20\n"
"v_mfma_f32_16x16x32_bf16 v[40:43], a[64:67], a[112:115], v[40:43]\n"
"v_add_f32 v197, v197, v21\n"
"v_mfma_f32_16x16x32_bf16 v[40:43], a[68:71], a[116:119], v[40:43]\n"
"global_load_dwordx4 a[52:55], v0, s[34:35] offset:1024 sc1\n"
"v_add_f32 v198, v198, v22\n"
"v_mfma_f32_16x16x32_bf16 v[68:71], a[76:79], a[116:119], v[68:71]\n"
"v_add_f32 v199, v199, v23\n"
"v_accvgpr_read_b32 v12, a20\n"
"v_mfma_f32_16x16x32_bf16 v[68:71], a[72:75], a[112:115], v[68:71]\n"
"v_accvgpr_read_b32 v13, a21\n"
"v_mfma_f32_16x16x32_bf16 v[72:75], a[72:75], a[120:123], v[72:75]\n"
"v_accvgpr_read_b32 v14, a22\n"
"v_accvgpr_read_b32 v15, a23\n"
"v_mfma_f32_16x16x32_bf16 v[72:75], a[76:79], a[124:127], v[72:75]\n"
"v_add_f32 v12, v12, v20\n"
"v_mfma_f32_16x16x32_bf16 v[44:47], a[68:71], a[124:127], v[44:47]\n"
"v_add_f32 v13, v13, v21\n"
"v_mfma_f32_16x16x32_bf16 v[44:47], a[64:67], a[120:123], v[44:47]\n"
"s_waitcnt lgkmcnt(4)\n"
"v_add_f32 v14, v14, v22\n"
"v_mfma_f32_16x16x32_bf16 v[48:51], a[64:67], a[128:131], v[48:51]\n"
"v_add_f32 v15, v15, v23\n"
"v_accvgpr_write_b32 a20, v12\n"
"v_mfma_f32_16x16x32_bf16 v[48:51], a[68:71], a[132:135], v[48:51]\n"
"global_load_dwordx4 a[56:59], v0, s[34:35] offset:2048 sc1\n"
"v_accvgpr_write_b32 a21, v13\n"
"v_mfma_f32_16x16x32_bf16 v[76:79], a[76:79], a[132:135], v[76:79]\n"
"s_waitcnt lgkmcnt(0)\n"
"v_accvgpr_write_b32 a22, v14\n"
"v_mfma_f32_16x16x32_bf16 v[76:79], a[72:75], a[128:131], v[76:79]\n"
"s_sub_u32 s29, s76, s29\n"
"v_accvgpr_write_b32 a23, v15\n"
"v_lshlrev_b32 v24, 16, v26\n"
"v_mfma_f32_16x16x32_bf16 v[80:83], a[72:75], a[136:139], v[80:83]\n"
"s_cmp_lt_u32 s29, 620\n"
"s_cselect_b32 s99, s99, 0\n"
"v_and_b32 v25, s63, v26\n"
"v_mfma_f32_16x16x32_bf16 v[80:83], a[76:79], a[140:143], v[80:83]\n"
"s_max_u32 s31, s31, s99\n"
"v_lshlrev_b32 v26, 16, v27\n"
"v_mfma_f32_16x16x32_bf16 v[52:55], a[68:71], a[140:143], v[52:55]\n"
"v_and_b32 v27, s63, v27\n"
"v_add_f32 v200, v200, v24\n"
"v_mfma_f32_16x16x32_bf16 v[52:55], a[64:67], a[136:139], v[52:55]\n"
"s_mov_b32 s99, 0\n"
"v_add_f32 v201, v201, v25\n"
"v_mfma_f32_16x16x32_bf16 v[56:59], a[64:67], a[144:147], v[56:59]\n"
"s_mov_b32 s98, 0\n"
"v_add_f32 v202, v202, v26\n"
"v_add_f32 v203, v203, v27\n"
"v_mfma_f32_16x16x32_bf16 v[56:59], a[68:71], a[148:151], v[56:59]\n"
"global_load_dwordx4 a[60:63], v0, s[34:35] offset:3072 sc1\n"
"s_memtime s[76:77]\n"
"s_load_dword s98, s[96:97], 256 glc\n"
"v_mfma_f32_16x16x32_bf16 v[84:87], a[76:79], a[148:151], v[84:87]\n"
"v_add_f32 v204, v204, v24\n"
"v_add_f32 v205, v205, v25\n"
"v_mfma_f32_16x16x32_bf16 v[84:87], a[72:75], a[144:147], v[84:87]\n"
"v_add_f32 v206, v206, v26\n"
"v_mfma_f32_16x16x32_bf16 a[4:7], a[72:75], a[152:155], a[4:7]\n"
"s_add_u32 s34, s34, 8192\n"
"v_add_f32 v207, v207, v27\n"
"v_mfma_f32_16x16x32_bf16 a[4:7], a[76:79], a[156:159], a[4:7]\n"
"v_add_f32 v208, v208, v24\n"
"v_add_f32 v209, v209, v25\n"
"v_mfma_f32_16x16x32_bf16 a[0:3], a[68:71], a[156:159], a[0:3]\n"
"v_add_f32 v210, v210, v26\n"
"v_mfma_f32_16x16x32_bf16 a[0:3], a[64:67], a[152:155], a[0:3]\n"
"s_waitcnt vmcnt(8)\n"
"v_add_f32 v211, v211, v27\n"
"v_mfma_f32_16x16x32_bf16 v[88:91], a[80:83], a[96:99], v[88:91]\n"
"v_add_f32 v212, v212, v24\n"
"v_add_f32 v213, v213, v25\n"
"v_mfma_f32_16x16x32_bf16 v[88:91], a[84:87], a[100:103], v[88:91]\n"
"global_load_dwordx4 a[64:67], v0, s[34:35] offset:-4096 sc1\n"
"v_add_f32 v214, v214, v26\n"
"v_mfma_f32_16x16x32_bf16 v[116:119], a[92:95], a[100:103], v[116:119]\n"
"s_mov_b32 s29, s76\n"
"ds_read_b128 a[100:103], v4 offset:17408\n"
"v_add_f32 v215, v215, v27\n"
"v_mfma_f32_16x16x32_bf16 v[116:119], a[88:91], a[96:99], v[116:119]\n"
"ds_read_b128 a[96:99], v4 offset:16384\n"
"v_add_f32 v216, v216, v24\n"
"v_mfma_f32_16x16x32_bf16 v[120:123], a[88:91], a[104:107], v[120:123]\n"
"s_sub_u32 s99, s99, s98\n"
"v_add_f32 v217, v217, v25\n"
"v_add_f32 v218, v218, v26\n"
"v_mfma_f32_16x16x32_bf16 v[120:123], a[92:95], a[108:111], v[120:123]\n"
"s_sub_u32 s99, s99, s98\n"
"v_add_f32 v219, v219, v27\n"
"v_add_f32 v220, v220, v24\n"
"v_mfma_f32_16x16x32_bf16 v[92:95], a[84:87], a[108:111], v[92:95]\n"
"s_sub_u32 s99, s99, s98\n"
"ds_read_b128 a[108:111], v4 offset:19456\n"
"v_add_f32 v221, v221, v25\n"
"v_mfma_f32_16x16x32_bf16 v[92:95], a[80:83], a[104:107], v[92:95]\n"
"s_sub_u32 s99, s99, s98\n"
"ds_read_b128 a[104:107], v4 offset:18432\n"
"v_add_f32 v222, v222, v26\n"
"v_mfma_f32_16x16x32_bf16 v[96:99], a[80:83], a[112:115], v[96:99]\n"
"s_sub_u32 s99, s99, s98\n"
"v_add_f32 v223, v223, v27\n"
"v_add_f32 v224, v224, v24\n"
"v_mfma_f32_16x16x32_bf16 v[96:99], a[84:87], a[116:119], v[96:99]\n"
"global_load_dwordx4 a[68:71], v0, s[34:35] offset:-3072 sc1\n"
"s_sub_u32 s99, s99, s98\n"
"v_mfma_f32_16x16x32_bf16 v[124:127], a[92:95], a[116:119], v[124:127]\n"
"s_sub_u32 s99, s99, s98\n"
"ds_read_b128 a[116:119], v4 offset:21504\n"
"v_add_f32 v225, v225, v25\n"
"v_mfma_f32_16x16x32_bf16 v[124:127], a[88:91], a[112:115], v[124:127]\n"
"s_sub_u32 s99, s99, s98\n"
"ds_read_b128 a[112:115], v4 offset:20480\n"
"v_add_f32 v226, v226, v26\n"
"v_mfma_f32_16x16x32_bf16 v[128:131], a[88:91], a[120:123], v[128:131]\n"
"s_sub_u32 s99, s99, s98\n"
"v_add_f32 v227, v227, v27\n"
"v_accvgpr_read_b32 v12, a24\n"
"v_mfma_f32_16x16x32_bf16 v[128:131], a[92:95], a[124:127], v[128:131]\n"
"s_sub_u32 s99, s99, s98\n"
"v_accvgpr_read_b32 v13, a25\n"
"v_accvgpr_read_b32 v14, a26\n"
"v_mfma_f32_16x16x32_bf16 v[100:103], a[84:87], a[124:127], v[100:103]\n"
"s_sub_u32 s99, s99, s98\n"
"ds_read_b128 a[124:127], v4 offset:23552\n"
"v_accvgpr_read_b32 v15, a27\n"
"v_mfma_f32_16x16x32_bf16 v[100:103], a[80:83], a[120:123], v[100:103]\n"
"s_sub_u32 s99, s99, s98\n"
"ds_read_b128 a[120:123], v4 offset:22528\n"
"v_add_f32 v12, v12, v24\n"
"v_mfma_f32_16x16x32_bf16 v[104:107], a[80:83], a[128:131], v[104:107]\n"
"s_sub_u32 s99, s99, s98\n"
"v_add_f32 v13, v13, v25\n"
"v_add_f32 v14, v14, v26\n"
"v_mfma_f32_16x16x32_bf16 v[104:107], a[84:87], a[132:135], v[104:107]\n"
"global_load_dwordx4 a[72:75], v0, s[34:35] offset:-2048 sc1\n"
"s_sub_u32 s99, s99, s98\n"
"v_mfma_f32_16x16x32_bf16 v[132:135], a[92:95], a[132:135], v[132:135]\n"
"s_sub_u32 s99, s99, s98\n"
"ds_read_b128 a[132:135], v4 offset:25600\n"
"v_add_f32 v15, v15, v27\n"
"v_mfma_f32_16x16x32_bf16 v[132:135], a[88:91], a[128:131], v[132:135]\n"
"s_sub_u32 s99, s99, s98\n"
"ds_read_b128 a[128:131], v4 offset:24576\n"
"v_accvgpr_write_b32 a24, v12\n"
"v_mfma_f32_16x16x32_bf16 v[136:139], a[88:91], a[136:139], v[136:139]\n"
"s_sub_u32 s99, s99, s98\n"
"v_accvgpr_write_b32 a25, v13\n"
"v_accvgpr_write_b32 a26, v14\n"
"v_mfma_f32_16x16x32_bf16 v[136:139], a[92:95], a[140:143], v[136:139]\n"
"s_sub_u32 s99, s99, s98\n"
"v_accvgpr_write_b32 a27, v15\n"
"v_lshlrev_b32 v28, 16, v30\n"
"v_mfma_f32_16x16x32_bf16 v[108:111], a[84:87], a[140:143], v[108:111]\n"
"s_sub_u32 s99, s99, s98\n"
"ds_read_b128 a[140:143], v4 offset:27648\n"
"v_and_b32 v29, s63, v30\n"
"v_mfma_f32_16x16x32_bf16 v[108:111], a[80:83], a[136:139], v[108:111]\n"
"s_sub_u32 s99, s99, s98\n"
"ds_read_b128 a[136:139], v4 offset:26624\n"
"v_lshlrev_b32 v30, 16, v31\n"
"v_mfma_f32_16x16x32_bf16 v[112:115], a[80:83], a[144:147], v[112:115]\n"
"s_sub_u32 s99, s99, s98\n"
"v_and_b32 v31, s63, v31\n"
"v_add_f32 v228, v228, v28\n"
"v_mfma_f32_16x16x32_bf16 v[112:115], a[84:87], a[148:151], v[112:115]\n"
"global_load_dwordx4 a[76:79], v0, s[34:35] offset:-1024 sc1\n"
"s_sub_u32 s99, s99, s98\n"
"v_mfma_f32_16x16x32_bf16 v[140:143], a[92:95], a[148:151], v[140:143]\n"
"s_sub_u32 s99, s99, s98\n"
"ds_read_b128 a[148:151], v4 offset:29696\n"
"v_add_f32 v229, v229, v29\n"
"v_mfma_f32_16x16x32_bf16 v[140:143], a[88:91], a[144:147], v[140:143]\n"
"s_sub_u32 s99, s99, s98\n"
"ds_read_b128 a[144:147], v4 offset:28672\n"
"v_add_f32 v230, v230, v30\n"
"v_mfma_f32_16x16x32_bf16 a[12:15], a[88:91], a[152:155], a[12:15]\n"
"s_memtime s[76:77]\n"
"v_add_f32 v231, v231, v31\n"
"v_add_f32 v232, v232, v28\n"
"v_mfma_f32_16x16x32_bf16 a[12:15], a[92:95], a[156:159], a[12:15]\n"
"v_add_f32 v233, v233, v29\n"
"v_add_f32 v234, v234, v30\n"
"v_mfma_f32_16x16x32_bf16 a[8:11], a[84:87], a[156:159], a[8:11]\n"
"ds_read_b128 a[156:159], v4 offset:31744\n"
"v_add_f32 v235, v235, v31\n"
"v_mfma_f32_16x16x32_bf16 a[8:11], a[80:83], a[152:155], a[8:11]\n"
"s_waitcnt vmcnt(8) lgkmcnt(11)\n"
"ds_read_b128 a[152:155], v4 offset:30720\n"
"v_add_f32 v236, v236, v28\n"
"v_mfma_f32_16x16x32_bf16 v[32:35], a[32:35], a[96:99], v[32:35]\n"
"v_add_f32 v237, v237, v29\n"
"v_add_f32 v238, v238, v30\n"
"v_mfma_f32_16x16x32_bf16 v[32:35], a[36:39], a[100:103], v[32:35]\n"
"global_load_dwordx4 a[80:83], v0, s[34:35] offset:0 sc1\n"
"v_add_f32 v239, v239, v31\n"
"v_mfma_f32_16x16x32_bf16 v[60:63], a[44:47], a[100:103], v[60:63]\n"
"v_add_f32 v240, v240, v28\n"
"v_add_f32 v241, v241, v29\n"
"v_mfma_f32_16x16x32_bf16 v[60:63], a[40:43], a[96:99], v[60:63]\n"
"v_add_f32 v242, v242, v30\n"
"v_mfma_f32_16x16x32_bf16 v[64:67], a[40:43], a[104:107], v[64:67]\n"
"v_add_f32 v243, v243, v31\n"
"v_add_f32 v244, v244, v28\n"
"v_mfma_f32_16x16x32_bf16 v[64:67], a[44:47], a[108:111], v[64:67]\n"
"L_st3848:\n"
"s_cbranch_execz L_retA\n"
"v_add_f32 v245, v245, v29\n"
"v_mfma_f32_16x16x32_bf16 v[36:39], a[36:39], a[108:111], v[36:39]\n"
"v_add_f32 v246, v246, v30\n"
"v_mfma_f32_16x16x32_bf16 v[36:39], a[32:35], a[104:107], v[36:39]\n"
"s_waitcnt lgkmcnt(8)\n"
"v_add_f32 v247, v247, v31\n"
"v_mfma_f32_16x16x32_bf16 v[40:43], a[32:35], a[112:115], v[40:43]\n"
"v_add_f32 v248, v248, v28\n"
"v_add_f32 v249, v249, v29\n"
"v_mfma_f32_16x16x32_bf16 v[40:43], a[36:39], a[116:119], v[40:43]\n"
"global_load_dwordx4 a[84:87], v0, s[34:35] offset:1024 sc1\n"
"s_add_u32 s48, s56, %12\n"
"s_addc_u32 s49, s57, 0\n"
"v_mfma_f32_16x16x32_bf16 v[68:71], a[44:47], a[116:119], v[68:71]\n"
"v_add_f32 v250, v250, v30\n"
"v_add_f32 v251, v251, v31\n"
"v_mfma_f32_16x16x32_bf16 v[68:71], a[40:43], a[112:115], v[68:71]\n"
"s_mov_b32 s50, 32768\n"
"v_add_f32 v252, v252, v28\n"
"v_add_f32 v253, v253, v29\n"
"v_mfma_f32_16x16x32_bf16 v[72:75], a[40:43], a[120:123], v[72:75]\n"
"v_add_f32 v254, v254, v30\n"
"v_mfma_f32_16x16x32_bf16 v[72:75], a[44:47], a[124:127], v[72:75]\n"
"s_mov_b32 s65, s78\n"
"v_add_f32 v255, v255, v31\n"
"v_mfma_f32_16x16x32_bf16 v[44:47], a[36:39], a[124:127], v[44:47]\n"
"v_accvgpr_read_b32 v12, a28\n"
"v_accvgpr_read_b32 v13, a29\n"
"v_mfma_f32_16x16x32_bf16 v[44:47], a[32:35], a[120:123], v[44:47]\n"
"s_mov_b64 s[40:41], s[52:53]\n"
"v_accvgpr_read_b32 v14, a30\n"
"s_waitcnt lgkmcnt(4)\n"
"v_mfma_f32_16x16x32_bf16 v[48:51], a[32:35], a[128:131], v[48:51]\n"
"s_mov_b32 s27, %21\n"
"v_accvgpr_read_b32 v15, a31\n"
"v_add_f32 v12, v12, v28\n"
"v_mfma_f32_16x16x32_bf16 v[48:51], a[36:39], a[132:135], v[48:51]\n"
"global_load_dwordx4 a[88:91], v0, s[34:35] offset:2048 sc1\n"
"s_mov_b32 s28, %23\n"
"v_mfma_f32_16x16x32_bf16 v[76:79], a[44:47], a[132:135], v[76:79]\n"
"s_waitcnt lgkmcnt(0)\n"
"v_add_f32 v13, v13, v29\n"
"v_add_f32 v14, v14, v30\n"
"v_mfma_f32_16x16x32_bf16 v[76:79], a[40:43], a[128:131], v[76:79]\n"
"s_sub_u32 s29, s76, s29\n"
"v_add_f32 v15, v15, v31\n"
"v_mfma_f32_16x16x32_bf16 v[80:83], a[40:43], a[136:139], v[80:83]\n"
"s_cmp_lt_u32 s29, 620\n"
"s_cselect_b32 s99, s99, 0\n"
"v_accvgpr_write_b32 a28, v12\n"
"v_mfma_f32_16x16x32_bf16 v[80:83], a[44:47], a[140:143], v[80:83]\n"
"s_max_u32 s30, s30, s99\n"
"v_accvgpr_write_b32 a29, v13\n"
"v_accvgpr_write_b32 a30, v14\n"
"v_mfma_f32_16x16x32_bf16 v[52:55], a[36:39], a[140:143], v[52:55]\n"
"s_cmp_eq_u32 s30, 0\n"
"s_cselect_b32 s30, 10, s30\n"
"v_accvgpr_write_b32 a31, v15\n"
"v_mfma_f32_16x16x32_bf16 v[52:55], a[32:35], a[136:139], v[52:55]\n"
"s_cmp_eq_u32 s31, 0\n"
"s_cselect_b32 s31, 10, s31\n"
"v_mfma_f32_16x16x32_bf16 v[56:59], a[32:35], a[144:147], v[56:59]\n"
"s_sub_u32 s69, s31, s30\n"
"v_mfma_f32_16x16x32_bf16 v[56:59], a[36:39], a[148:151], v[56:59]\n"
"global_load_dwordx4 a[92:95], v0, s[34:35] offset:3072 sc1\n"
"v_mov_b32 v20, s69\n"
"v_mfma_f32_16x16x32_bf16 v[84:87], a[44:47], a[148:151], v[84:87]\n"
"ds_write_b32 v2, v20 offset:8192\n"
"v_mfma_f32_16x16x32_bf16 v[84:87], a[40:43], a[144:147], v[84:87]\n"
"v_mfma_f32_16x16x32_bf16 a[4:7], a[40:43], a[152:155], a[4:7]\n"
"s_add_u32 s34, s34, 8192\n"
"v_mfma_f32_16x16x32_bf16 a[4:7], a[44:47], a[156:159], a[4:7]\n"
"v_mfma_f32_16x16x32_bf16 a[0:3], a[36:39], a[156:159], a[0:3]\n"
"v_mfma_f32_16x16x32_bf16 a[0:3], a[32:35], a[152:155], a[0:3]\n"
"s_waitcnt lgkmcnt(0)\n"
"s_barrier\n"
"s_waitcnt vmcnt(8)\n"
"v_mfma_f32_16x16x32_bf16 v[88:91], a[48:51], a[96:99], v[88:91]\n"
"ds_read_b32 v21, v3 offset:8192\n"
"v_mfma_f32_16x16x32_bf16 v[88:91], a[52:55], a[100:103], v[88:91]\n"
"global_load_dwordx4 a[32:35], v0, s[34:35] offset:-4096 sc1\n"
"v_mfma_f32_16x16x32_bf16 v[116:119], a[60:63], a[100:103], v[116:119]\n"
"ds_read_b128 a[100:103], v5 offset:17408\n"
"v_mfma_f32_16x16x32_bf16 v[116:119], a[56:59], a[96:99], v[116:119]\n"
"ds_read_b128 a[96:99], v5 offset:16384\n"
"v_mfma_f32_16x16x32_bf16 v[120:123], a[56:59], a[104:107], v[120:123]\n"
"ds_read_b32 v22, v4 offset:8192\n"
"v_mfma_f32_16x16x32_bf16 v[120:123], a[60:63], a[108:111], v[120:123]\n"
"ds_read_b32 v23, v5 offset:8192\n"
"v_mfma_f32_16x16x32_bf16 v[92:95], a[52:55], a[108:111], v[92:95]\n"
"ds_read_b128 a[108:111], v5 offset:19456\n"
"v_mfma_f32_16x16x32_bf16 v[92:95], a[48:51], a[104:107], v[92:95]\n"
"ds_read_b128 a[104:107], v5 offset:18432\n"
"v_mfma_f32_16x16x32_bf16 v[96:99], a[48:51], a[112:115], v[96:99]\n"
"v_mfma_f32_16x16x32_bf16 v[96:99], a[52:55], a[116:119], v[96:99]\n"
"global_load_dwordx4 a[36:39], v0, s[34:35] offset:-3072 sc1\n"
"v_mfma_f32_16x16x32_bf16 v[124:127], a[60:63], a[116:119], v[124:127]\n"
"s_waitcnt lgkmcnt(2)\n"
"ds_read_b128 a[116:119], v5 offset:21504\n"
"v_add_u32 v20, v20, v21\n"
"v_mfma_f32_16x16x32_bf16 v[124:127], a[56:59], a[112:115], v[124:127]\n"
"ds_read_b128 a[112:115], v5 offset:20480\n"
"v_mfma_f32_16x16x32_bf16 v[128:131], a[56:59], a[120:123], v[128:131]\n"
"v_add3_u32 v20, v20, v22, v23\n"
"v_mfma_f32_16x16x32_bf16 v[128:131], a[60:63], a[124:127], v[128:131]\n"
"v_mfma_f32_16x16x32_bf16 v[100:103], a[52:55], a[124:127], v[100:103]\n"
"ds_read_b128 a[124:127], v5 offset:23552\n"
"v_readfirstlane_b32 s69, v20\n"
"v_mfma_f32_16x16x32_bf16 v[100:103], a[48:51], a[120:123], v[100:103]\n"
"ds_read_b128 a[120:123], v5 offset:22528\n"
"v_mfma_f32_16x16x32_bf16 v[104:107], a[48:51], a[128:131], v[104:107]\n"
"v_mfma_f32_16x16x32_bf16 v[104:107], a[52:55], a[132:135], v[104:107]\n"
"s_cmp_le_i32 s69, -2\n"
"global_load_dwordx4 a[40:43], v0, s[34:35] offset:-2048 sc1\n"
"s_cselect_b32 s66, 2, 1\n"
"v_mfma_f32_16x16x32_bf16 v[132:135], a[60:63], a[132:135], v[132:135]\n"
"ds_read_b128 a[132:135], v5 offset:25600\n"
"v_mfma_f32_16x16x32_bf16 v[132:135], a[56:59], a[128:131], v[132:135]\n"
"s_cmp_ge_i32 s69, 2\n"
"ds_read_b128 a[128:131], v5 offset:24576\n"
"s_cselect_b32 s66, 3, s66\n"
"v_mfma_f32_16x16x32_bf16 v[136:139], a[56:59], a[136:139], v[136:139]\n"
"ds_read_b128 v[16:19], v7 offset:32768\n"
"v_mfma_f32_16x16x32_bf16 v[136:139], a[60:63], a[140:143], v[136:139]\n"
"s_lshl_b32 s73, s66, 13\n"
"s_xor_b32 s60, s79, s66\n"
"v_mfma_f32_16x16x32_bf16 v[108:111], a[52:55], a[140:143], v[108:111]\n"
"ds_read_b128 a[140:143], v5 offset:27648\n"
"v_mfma_f32_16x16x32_bf16 v[108:111], a[48:51], a[136:139], v[108:111]\n"
"s_cmp_eq_u32 s66, 3\n"
"ds_read_b128 a[136:139], v5 offset:26624\n"
"s_cselect_b32 s72, 8192, 0\n"
"v_mfma_f32_16x16x32_bf16 v[112:115], a[48:51], a[144:147], v[112:115]\n"
"s_abs_i32 s30, s69\n"
"v_mfma_f32_16x16x32_bf16 v[112:115], a[52:55], a[148:151], v[112:115]\n"
"global_load_dwordx4 a[44:47], v0, s[34:35] offset:-1024 sc1\n"
"s_add_u32 s52, s56, %11\n"
"s_addc_u32 s53, s57, 0\n"
"v_mfma_f32_16x16x32_bf16 v[140:143], a[60:63], a[148:151], v[140:143]\n"
"s_cmp_lt_u32 s30, 26\n"
"s_cselect_b32 s30, 2, 0\n"
"ds_read_b128 a[148:151], v5 offset:29696\n"
"v_mfma_f32_16x16x32_bf16 v[140:143], a[56:59], a[144:147], v[140:143]\n"
"s_add_u32 s52, s52, 4096\n"
"ds_read_b128 a[144:147], v5 offset:28672\n"
"s_addc_u32 s53, s53, 0\n"
"v_mfma_f32_16x16x32_bf16 a[12:15], a[56:59], a[152:155], a[12:15]\n"
"v_mfma_f32_16x16x32_bf16 a[12:15], a[60:63], a[156:159], a[12:15]\n"
"s_add_u32 s54, s52, s73\n"
"s_addc_u32 s55, s53, 0\n"
"v_mfma_f32_16x16x32_bf16 a[8:11], a[52:55], a[156:159], a[8:11]\n"
"ds_read_b128 a[156:159], v5 offset:31744\n"
"v_mfma_f32_16x16x32_bf16 a[8:11], a[48:51], a[152:155], a[8:11]\n"
"s_add_u32 s52, s52, s72\n"
"ds_read_b128 a[152:155], v5 offset:30720\n"
"s_addc_u32 s53, s53, 0\n"
"L_P6:\n"
"s_waitcnt vmcnt(8) lgkmcnt(13)\n"
"v_mfma_f32_16x16x32_bf16 v[32:35], a[64:67], a[96:99], v[32:35]\n"
"v_mfma_f32_16x16x32_bf16 v[32:35], a[68:71], a[100:103], v[32:35]\n"
"global_load_dwordx4 a[48:51], v0, s[34:35] offset:0 sc1\n"
"v_mfma_f32_16x16x32_bf16 v[60:63], a[76:79], a[100:103], v[60:63]\n"
"v_mfma_f32_16x16x32_bf16 v[60:63], a[72:75], a[96:99], v[60:63]\n"
"v_mfma_f32_16x16x32_bf16 v[64:67], a[72:75], a[104:107], v[64:67]\n"
"v_mfma_f32_16x16x32_bf16 v[64:67], a[76:79], a[108:111], v[64:67]\n"
"v_mfma_f32_16x16x32_bf16 v[36:39], a[68:71], a[108:111], v[36:39]\n"
"s_waitcnt lgkmcnt(6)\n"
"v_lshlrev_b32 v12, 16, v16\n"
"v_mfma_f32_16x16x32_bf16 v[36:39], a[64:67], a[104:107], v[36:39]\n"
"v_and_b32 v13, s63, v16\n"
"v_lshlrev_b32 v14, 16, v17\n"
"v_mfma_f32_16x16x32_bf16 v[40:43], a[64:67], a[112:115], v[40:43]\n"
"v_and_b32 v15, s63, v17\n"
"v_mfma_f32_16x16x32_bf16 v[40:43], a[68:71], a[116:119], v[40:43]\n"
"global_load_dwordx4 a[52:55], v0, s[34:35] offset:1024 sc1\n"
"ds_read_b128 v[20:23], v7 offset:34816\n"
"v_mfma_f32_16x16x32_bf16 v[68:71], a[76:79], a[116:119], v[68:71]\n"
"v_mul_f32 v12, v12, v32\n"
"v_mul_f32 v13, v13, v33\n"
"v_mfma_f32_16x16x32_bf16 v[68:71], a[72:75], a[112:115], v[68:71]\n"
"v_mul_f32 v14, v14, v34\n"
"v_mfma_f32_16x16x32_bf16 v[72:75], a[72:75], a[120:123], v[72:75]\n"
"v_mul_f32 v15, v15, v35\n"
"v_mfma_f32_16x16x32_bf16 v[72:75], a[76:79], a[124:127], v[72:75]\n"
"v_cvt_pk_bf16_f32 v24, v12, v13\n"
"v_cvt_pk_bf16_f32 v25, v14, v15\n"
"v_mfma_f32_16x16x32_bf16 v[44:47], a[68:71], a[124:127], v[44:47]\n"
"ds_write_b64 v2, v[24:25] offset:0\n"
"v_mfma_f32_16x16x32_bf16 v[44:47], a[64:67], a[120:123], v[44:47]\n"
"s_waitcnt lgkmcnt(6)\n"
"v_lshlrev_b32 v12, 16, v18\n"
"v_mfma_f32_16x16x32_bf16 v[48:51], a[64:67], a[128:131], v[48:51]\n"
"v_and_b32 v13, s63, v18\n"
"v_lshlrev_b32 v14, 16, v19\n"
"v_mfma_f32_16x16x32_bf16 v[48:51], a[68:71], a[132:135], v[48:51]\n"
"global_load_dwordx4 a[56:59], v0, s[34:35] offset:2048 sc1\n"
"v_and_b32 v15, s63, v19\n"
"v_mfma_f32_16x16x32_bf16 v[76:79], a[76:79], a[132:135], v[76:79]\n"
"v_mul_f32 v12, v12, v60\n"
"v_mfma_f32_16x16x32_bf16 v[76:79], a[72:75], a[128:131], v[76:79]\n"
"v_mul_f32 v13, v13, v61\n"
"v_mul_f32 v14, v14, v62\n"
"v_mfma_f32_16x16x32_bf16 v[80:83], a[72:75], a[136:139], v[80:83]\n"
"v_mul_f32 v15, v15, v63\n"
"v_mfma_f32_16x16x32_bf16 v[80:83], a[76:79], a[140:143], v[80:83]\n"
"v_cvt_pk_bf16_f32 v26, v12, v13\n"
"v_cvt_pk_bf16_f32 v27, v14, v15\n"
"v_mfma_f32_16x16x32_bf16 v[52:55], a[68:71], a[140:143], v[52:55]\n"
"ds_write_b64 v2, v[26:27] offset:8\n"
"v_mfma_f32_16x16x32_bf16 v[52:55], a[64:67], a[136:139], v[52:55]\n"
"s_waitcnt lgkmcnt(2)\n"
"v_lshlrev_b32 v12, 16, v20\n"
"v_mfma_f32_16x16x32_bf16 v[56:59], a[64:67], a[144:147], v[56:59]\n"
"v_and_b32 v13, s63, v20\n"
"v_mfma_f32_16x16x32_bf16 v[56:59], a[68:71], a[148:151], v[56:59]\n"
"global_load_dwordx4 a[60:63], v0, s[34:35] offset:3072 sc1\n"
"v_lshlrev_b32 v14, 16, v21\n"
"v_mfma_f32_16x16x32_bf16 v[84:87], a[76:79], a[148:151], v[84:87]\n"
"v_and_b32 v15, s63, v21\n"
"v_xor_b32 v16, 64, v7\n"
"v_mfma_f32_16x16x32_bf16 v[84:87], a[72:75], a[144:147], v[84:87]\n"
"ds_read_b128 v[16:19], v16 offset:32768\n"
"v_mfma_f32_16x16x32_bf16 a[4:7], a[72:75], a[152:155], a[4:7]\n"
"s_add_u32 s34, s34, 8192\n"
"v_mul_f32 v12, v12, v36\n"
"v_mul_f32 v13, v13, v37\n"
"v_mfma_f32_16x16x32_bf16 a[4:7], a[76:79], a[156:159], a[4:7]\n"
"v_mul_f32 v14, v14, v38\n"
"v_mfma_f32_16x16x32_bf16 a[0:3], a[68:71], a[156:159], a[0:3]\n"
"v_mul_f32 v15, v15, v39\n"
"v_mfma_f32_16x16x32_bf16 a[0:3], a[64:67], a[152:155], a[0:3]\n"
"s_waitcnt vmcnt(8)\n"
"v_cvt_pk_bf16_f32 v28, v12, v13\n"
"v_cvt_pk_bf16_f32 v29, v14, v15\n"
"v_mfma_f32_16x16x32_bf16 v[88:91], a[80:83], a[96:99], v[88:91]\n"
"v_lshlrev_b32 v12, 16, v22\n"
"v_mfma_f32_16x16x32_bf16 v[88:91], a[84:87], a[100:103], v[88:91]\n"
"global_load_dwordx4 a[64:67], v0, s[34:35] offset:-4096 sc1\n"
"v_and_b32 v13, s63, v22\n"
"v_mfma_f32_16x16x32_bf16 v[116:119], a[92:95], a[100:103], v[116:119]\n"
"v_lshlrev_b32 v14, 16, v23\n"
"v_and_b32 v15, s63, v23\n"
"v_mfma_f32_16x16x32_bf16 v[116:119], a[88:91], a[96:99], v[116:119]\n"
"v_mul_f32 v12, v12, v64\n"
"v_mfma_f32_16x16x32_bf16 v[120:123], a[88:91], a[104:107], v[120:123]\n"
"v_mul_f32 v13, v13, v65\n"
"v_mfma_f32_16x16x32_bf16 v[120:123], a[92:95], a[108:111], v[120:123]\n"
"v_mul_f32 v14, v14, v66\n"
"v_mul_f32 v15, v15, v67\n"
"v_mfma_f32_16x16x32_bf16 v[92:95], a[84:87], a[108:111], v[92:95]\n"
"v_cvt_pk_bf16_f32 v30, v12, v13\n"
"v_mfma_f32_16x16x32_bf16 v[92:95], a[80:83], a[104:107], v[92:95]\n"
"v_cvt_pk_bf16_f32 v31, v14, v15\n"
"v_mfma_f32_16x16x32_bf16 v[96:99], a[80:83], a[112:115], v[96:99]\n"
"global_store_dwordx4 v0, v[28:31], s[52:53] offset:-2048 sc1\n"
"ds_write_b128 v2, v[28:31] offset:2048\n"
"v_mfma_f32_16x16x32_bf16 v[96:99], a[84:87], a[116:119], v[96:99]\n"
"s_waitcnt lgkmcnt(1)\n"
"global_load_dwordx4 a[68:71], v0, s[34:35] offset:-3072 sc1\n"
"v_lshlrev_b32 v12, 16, v16\n"
"v_mfma_f32_16x16x32_bf16 v[124:127], a[92:95], a[116:119], v[124:127]\n"
"v_and_b32 v13, s63, v16\n"
"v_mfma_f32_16x16x32_bf16 v[124:127], a[88:91], a[112:115], v[124:127]\n"
"v_lshlrev_b32 v14, 16, v17\n"
"v_and_b32 v15, s63, v17\n"
"v_mfma_f32_16x16x32_bf16 v[128:131], a[88:91], a[120:123], v[128:131]\n"
"v_xor_b32 v20, 64, v7\n"
"v_mfma_f32_16x16x32_bf16 v[128:131], a[92:95], a[124:127], v[128:131]\n"
"ds_read_b128 v[20:23], v20 offset:34816\n"
"v_mfma_f32_16x16x32_bf16 v[100:103], a[84:87], a[124:127], v[100:103]\n"
"v_mul_f32 v12, v12, v88\n"
"v_mul_f32 v13, v13, v89\n"
"v_mfma_f32_16x16x32_bf16 v[100:103], a[80:83], a[120:123], v[100:103]\n"
"v_mul_f32 v14, v14, v90\n"
"v_mfma_f32_16x16x32_bf16 v[104:107], a[80:83], a[128:131], v[104:107]\n"
"v_mul_f32 v15, v15, v91\n"
"v_mfma_f32_16x16x32_bf16 v[104:107], a[84:87], a[132:135], v[104:107]\n"
"global_load_dwordx4 a[72:75], v0, s[34:35] offset:-2048 sc1\n"
"v_cvt_pk_bf16_f32 v24, v12, v13\n"
"v_mfma_f32_16x16x32_bf16 v[132:135], a[92:95], a[132:135], v[132:135]\n"
"v_cvt_pk_bf16_f32 v25, v14, v15\n"
"v_lshlrev_b32 v12, 16, v18\n"
"v_mfma_f32_16x16x32_bf16 v[132:135], a[88:91], a[128:131], v[132:135]\n"
"v_and_b32 v13, s63, v18\n"
"v_mfma_f32_16x16x32_bf16 v[136:139], a[88:91], a[136:139], v[136:139]\n"
"v_lshlrev_b32 v14, 16, v19\n"
"v_and_b32 v15, s63, v19\n"
"v_mfma_f32_16x16x32_bf16 v[136:139], a[92:95], a[140:143], v[136:139]\n"
"v_mul_f32 v12, v12, v116\n"
"v_mfma_f32_16x16x32_bf16 v[108:111], a[84:87], a[140:143], v[108:111]\n"
"v_mul_f32 v13, v13, v117\n"
"v_mfma_f32_16x16x32_bf16 v[108:111], a[80:83], a[136:139], v[108:111]\n"
"v_mul_f32 v14, v14, v118\n"
"v_mul_f32 v15, v15, v119\n"
"v_mfma_f32_16x16x32_bf16 v[112:115], a[80:83], a[144:147], v[112:115]\n"
"v_cvt_pk_bf16_f32 v26, v12, v13\n"
"v_mfma_f32_16x16x32_bf16 v[112:115], a[84:87], a[148:151], v[112:115]\n"
"global_load_dwordx4 a[76:79], v0, s[34:35] offset:-1024 sc1\n"
"v_cvt_pk_bf16_f32 v27, v14, v15\n"
"v_mfma_f32_16x16x32_bf16 v[140:143], a[92:95], a[148:151], v[140:143]\n"
"global_store_dwordx4 v0, v[24:27], s[52:53] offset:-3072 sc1\n"
"ds_write_b128 v2, v[24:27] offset:1024\n"
"v_mfma_f32_16x16x32_bf16 v[140:143], a[88:91], a[144:147], v[140:143]\n"
"s_waitcnt lgkmcnt(1)\n"
"v_lshlrev_b32 v12, 16, v20\n"
"v_mfma_f32_16x16x32_bf16 a[12:15], a[88:91], a[152:155], a[12:15]\n"
"v_and_b32 v13, s63, v20\n"
"v_mfma_f32_16x16x32_bf16 a[12:15], a[92:95], a[156:159], a[12:15]\n"
"v_lshlrev_b32 v14, 16, v21\n"
"v_mfma_f32_16x16x32_bf16 a[8:11], a[84:87], a[156:159], a[8:11]\n"
"ds_read_b128 v[16:19], v7 offset:36864\n"
"v_and_b32 v15, s63, v21\n"
"v_mfma_f32_16x16x32_bf16 a[8:11], a[80:83], a[152:155], a[8:11]\n"
"s_waitcnt vmcnt(10)\n"
"v_mul_f32 v12, v12, v92\n"
"v_mfma_f32_16x16x32_bf16 v[144:147], a[32:35], a[96:99], v[144:147]\n"
"v_mul_f32 v13, v13, v93\n"
"v_mul_f32 v14, v14, v94\n"
"v_mfma_f32_16x16x32_bf16 v[144:147], a[36:39], a[100:103], v[144:147]\n"
"global_load_dwordx4 a[80:83], v0, s[34:35] offset:0 sc1\n"
"v_mul_f32 v15, v15, v95\n"
"v_mfma_f32_16x16x32_bf16 v[172:175], a[44:47], a[100:103], v[172:175]\n"
"v_cvt_pk_bf16_f32 v28, v12, v13\n"
"v_mfma_f32_16x16x32_bf16 v[172:175], a[40:43], a[96:99], v[172:175]\n"
"v_cvt_pk_bf16_f32 v29, v14, v15\n"
"v_mfma_f32_16x16x32_bf16 v[176:179], a[40:43], a[104:107], v[176:179]\n"
"ds_write_b64 v2, v[28:29] offset:3072\n"
"v_mfma_f32_16x16x32_bf16 v[176:179], a[44:47], a[108:111], v[176:179]\n"
"v_lshlrev_b32 v12, 16, v22\n"
"v_and_b32 v13, s63, v22\n"
"v_mfma_f32_16x16x32_bf16 v[148:151], a[36:39], a[108:111], v[148:151]\n"
"v_lshlrev_b32 v14, 16, v23\n"
"v_mfma_f32_16x16x32_bf16 v[148:151], a[32:35], a[104:107], v[148:151]\n"
"v_and_b32 v15, s63, v23\n"
"v_mul_f32 v12, v12, v120\n"
"v_mfma_f32_16x16x32_bf16 v[152:155], a[32:35], a[112:115], v[152:155]\n"
"v_mul_f32 v13, v13, v121\n"
"v_mfma_f32_16x16x32_bf16 v[152:155], a[36:39], a[116:119], v[152:155]\n"
"global_load_dwordx4 a[84:87], v0, s[34:35] offset:1024 sc1\n"
"v_mul_f32 v14, v14, v122\n"
"v_mfma_f32_16x16x32_bf16 v[180:183], a[44:47], a[116:119], v[180:183]\n"
"v_mul_f32 v15, v15, v123\n"
"v_cvt_pk_bf16_f32 v30, v12, v13\n"
"v_mfma_f32_16x16x32_bf16 v[180:183], a[40:43], a[112:115], v[180:183]\n"
"v_cvt_pk_bf16_f32 v31, v14, v15\n"
"v_mfma_f32_16x16x32_bf16 v[184:187], a[40:43], a[120:123], v[184:187]\n"
"ds_write_b64 v2, v[30:31] offset:3080\n"
"v_mfma_f32_16x16x32_bf16 v[184:187], a[44:47], a[124:127], v[184:187]\n"
"s_waitcnt lgkmcnt(2)\n"
"v_lshlrev_b32 v12, 16, v16\n"
"v_and_b32 v13, s63, v16\n"
"v_mfma_f32_16x16x32_bf16 v[156:159], a[36:39], a[124:127], v[156:159]\n"
"v_lshlrev_b32 v14, 16, v17\n"
"v_mfma_f32_16x16x32_bf16 v[156:159], a[32:35], a[120:123], v[156:159]\n"
"v_and_b32 v15, s63, v17\n"
"v_xor_b32 v20, 64, v7\n"
"v_mfma_f32_16x16x32_bf16 v[160:163], a[32:35], a[128:131], v[160:163]\n"
"ds_read_b128 v[20:23], v20 offset:36864\n"
"v_mfma_f32_16x16x32_bf16 v[160:163], a[36:39], a[132:135], v[160:163]\n"
"global_load_dwordx4 a[88:91], v0, s[34:35] offset:2048 sc1\n"
"v_mul_f32 v12, v12, v40\n"
"v_mfma_f32_16x16x32_bf16 v[188:191], a[44:47], a[132:135], v[188:191]\n"
"v_mul_f32 v13, v13, v41\n"
"v_mfma_f32_16x16x32_bf16 v[188:191], a[40:43], a[128:131], v[188:191]\n"
"v_mul_f32 v14, v14, v42\n"
"v_mul_f32 v15, v15, v43\n"
"v_mfma_f32_16x16x32_bf16 v[192:195], a[40:43], a[136:139], v[192:195]\n"
"v_cvt_pk_bf16_f32 v24, v12, v13\n"
"v_mfma_f32_16x16x32_bf16 v[192:195], a[44:47], a[140:143], v[192:195]\n"
"v_cvt_pk_bf16_f32 v25, v14, v15\n"
"v_mfma_f32_16x16x32_bf16 v[164:167], a[36:39], a[140:143], v[164:167]\n"
"v_lshlrev_b32 v12, 16, v18\n"
"v_and_b32 v13, s63, v18\n"
"v_mfma_f32_16x16x32_bf16 v[164:167], a[32:35], a[136:139], v[164:167]\n"
"v_lshlrev_b32 v14, 16, v19\n"
"v_mfma_f32_16x16x32_bf16 v[168:171], a[32:35], a[144:147], v[168:171]\n"
"v_and_b32 v15, s63, v19\n"
"v_mul_f32 v12, v12, v68\n"
"v_mfma_f32_16x16x32_bf16 v[168:171], a[36:39], a[148:151], v[168:171]\n"
"global_load_dwordx4 a[92:95], v0, s[34:35] offset:3072 sc1\n"
"v_mul_f32 v13, v13, v69\n"
"v_mfma_f32_16x16x32_bf16 v[196:199], a[44:47], a[148:151], v[196:199]\n"
"v_mul_f32 v14, v14, v70\n"
"v_mfma_f32_16x16x32_bf16 v[196:199], a[40:43], a[144:147], v[196:199]\n"
"v_mul_f32 v15, v15, v71\n"
"v_mfma_f32_16x16x32_bf16 a[20:23], a[40:43], a[152:155], a[20:23]\n"
"s_add_u32 s34, s34, 8192\n"
"v_cvt_pk_bf16_f32 v26, v12, v13\n"
"v_cvt_pk_bf16_f32 v27, v14, v15\n"
"v_mfma_f32_16x16x32_bf16 a[20:23], a[44:47], a[156:159], a[20:23]\n"
"global_store_dwordx4 v0, v[24:27], s[52:53] offset:0 sc1\n"
"ds_write_b128 v2, v[24:27] offset:4096\n"
"v_mfma_f32_16x16x32_bf16 a[16:19], a[36:39], a[156:159], a[16:19]\n"
"s_waitcnt lgkmcnt(1)\n"
"v_lshlrev_b32 v12, 16, v20\n"
"v_mfma_f32_16x16x32_bf16 a[16:19], a[32:35], a[152:155], a[16:19]\n"
"s_waitcnt vmcnt(11)\n"
"v_and_b32 v13, s63, v20\n"
"v_mfma_f32_16x16x32_bf16 v[200:203], a[48:51], a[96:99], v[200:203]\n"
"v_lshlrev_b32 v14, 16, v21\n"
"v_and_b32 v15, s63, v21\n"
"v_mfma_f32_16x16x32_bf16 v[200:203], a[52:55], a[100:103], v[200:203]\n"
"global_load_dwordx4 a[32:35], v0, s[34:35] offset:-4096 sc1\n"
"ds_read_b128 v[16:19], v7 offset:38912\n"
"v_mfma_f32_16x16x32_bf16 v[228:231], a[60:63], a[100:103], v[228:231]\n"
"ds_read_b128 a[100:103], v4 offset:17408\n"
"v_mul_f32 v12, v12, v96\n"
"v_mfma_f32_16x16x32_bf16 v[228:231], a[56:59], a[96:99], v[228:231]\n"
"ds_read_b128 a[96:99], v4 offset:16384\n"
"v_mul_f32 v13, v13, v97\n"
"v_mfma_f32_16x16x32_bf16 v[232:235], a[56:59], a[104:107], v[232:235]\n"
"v_mul_f32 v14, v14, v98\n"
"v_mfma_f32_16x16x32_bf16 v[232:235], a[60:63], a[108:111], v[232:235]\n"
"v_mul_f32 v15, v15, v99\n"
"v_cvt_pk_bf16_f32 v28, v12, v13\n"
"v_mfma_f32_16x16x32_bf16 v[204:207], a[52:55], a[108:111], v[204:207]\n"
"ds_read_b128 a[108:111], v4 offset:19456\n"
"v_cvt_pk_bf16_f32 v29, v14, v15\n"
"v_mfma_f32_16x16x32_bf16 v[204:207], a[48:51], a[104:107], v[204:207]\n"
"ds_read_b128 a[104:107], v4 offset:18432\n"
"v_lshlrev_b32 v12, 16, v22\n"
"v_mfma_f32_16x16x32_bf16 v[208:211], a[48:51], a[112:115], v[208:211]\n"
"v_and_b32 v13, s63, v22\n"
"v_lshlrev_b32 v14, 16, v23\n"
"v_mfma_f32_16x16x32_bf16 v[208:211], a[52:55], a[116:119], v[208:211]\n"
"global_load_dwordx4 a[36:39], v0, s[34:35] offset:-3072 sc1\n"
"v_and_b32 v15, s63, v23\n"
"v_mfma_f32_16x16x32_bf16 v[236:239], a[60:63], a[116:119], v[236:239]\n"
"ds_read_b128 a[116:119], v4 offset:21504\n"
"v_mul_f32 v12, v12, v124\n"
"v_mfma_f32_16x16x32_bf16 v[236:239], a[56:59], a[112:115], v[236:239]\n"
"ds_read_b128 a[112:115], v4 offset:20480\n"
"v_mul_f32 v13, v13, v125\n"
"v_mfma_f32_16x16x32_bf16 v[240:243], a[56:59], a[120:123], v[240:243]\n"
"v_mul_f32 v14, v14, v126\n"
"v_mul_f32 v15, v15, v127\n"
"v_mfma_f32_16x16x32_bf16 v[240:243], a[60:63], a[124:127], v[240:243]\n"
"v_cvt_pk_bf16_f32 v30, v12, v13\n"
"v_mfma_f32_16x16x32_bf16 v[212:215], a[52:55], a[124:127], v[212:215]\n"
"ds_read_b128 a[124:127], v4 offset:23552\n"
"v_cvt_pk_bf16_f32 v31, v14, v15\n"
"v_mfma_f32_16x16x32_bf16 v[212:215], a[48:51], a[120:123], v[212:215]\n"
"ds_read_b128 a[120:123], v4 offset:22528\n"
"v_mfma_f32_16x16x32_bf16 v[216:219], a[48:51], a[128:131], v[216:219]\n"
"global_store_dwordx4 v0, v[28:31], s[52:53] offset:1024 sc1\n"
"ds_write_b128 v2, v[28:31] offset:5120\n"
"v_mfma_f32_16x16x32_bf16 v[216:219], a[52:55], a[132:135], v[216:219]\n"
"s_waitcnt lgkmcnt(9)\n"
"global_load_dwordx4 a[40:43], v0, s[34:35] offset:-2048 sc1\n"
"v_lshlrev_b32 v12, 16, v16\n"
"v_mfma_f32_16x16x32_bf16 v[244:247], a[60:63], a[132:135], v[244:247]\n"
"ds_read_b128 a[132:135], v4 offset:25600\n"
"v_and_b32 v13, s63, v16\n"
"v_mfma_f32_16x16x32_bf16 v[244:247], a[56:59], a[128:131], v[244:247]\n"
"ds_read_b128 a[128:131], v4 offset:24576\n"
"v_lshlrev_b32 v14, 16, v17\n"
"v_mfma_f32_16x16x32_bf16 v[248:251], a[56:59], a[136:139], v[248:251]\n"
"v_and_b32 v15, s63, v17\n"
"v_xor_b32 v20, 64, v7\n"
"v_mfma_f32_16x16x32_bf16 v[248:251], a[60:63], a[140:143], v[248:251]\n"
"ds_read_b128 v[20:23], v20 offset:38912\n"
"v_mul_f32 v12, v12, v44\n"
"v_mfma_f32_16x16x32_bf16 v[220:223], a[52:55], a[140:143], v[220:223]\n"
"ds_read_b128 a[140:143], v4 offset:27648\n"
"v_mul_f32 v13, v13, v45\n"
"v_mfma_f32_16x16x32_bf16 v[220:223], a[48:51], a[136:139], v[220:223]\n"
"ds_read_b128 a[136:139], v4 offset:26624\n"
"v_mul_f32 v14, v14, v46\n"
"v_mfma_f32_16x16x32_bf16 v[224:227], a[48:51], a[144:147], v[224:227]\n"
"v_mul_f32 v15, v15, v47\n"
"v_cvt_pk_bf16_f32 v24, v12, v13\n"
"v_mfma_f32_16x16x32_bf16 v[224:227], a[52:55], a[148:151], v[224:227]\n"
"global_load_dwordx4 a[44:47], v0, s[34:35] offset:-1024 sc1\n"
"v_cvt_pk_bf16_f32 v25, v14, v15\n"
"v_mfma_f32_16x16x32_bf16 v[252:255], a[60:63], a[148:151], v[252:255]\n"
"ds_read_b128 a[148:151], v4 offset:29696\n"
"v_mfma_f32_16x16x32_bf16 v[252:255], a[56:59], a[144:147], v[252:255]\n"
"ds_read_b128 a[144:147], v4 offset:28672\n"
"v_mfma_f32_16x16x32_bf16 a[28:31], a[56:59], a[152:155], a[28:31]\n"
"ds_write_b64 v2, v[24:25] offset:6144\n"
"v_mfma_f32_16x16x32_bf16 a[28:31], a[60:63], a[156:159], a[28:31]\n"
"v_lshlrev_b32 v12, 16, v18\n"
"v_and_b32 v13, s63, v18\n"
"v_mfma_f32_16x16x32_bf16 a[24:27], a[52:55], a[156:159], a[24:27]\n"
"ds_read_b128 a[156:159], v4 offset:31744\n"
"v_lshlrev_b32 v14, 16, v19\n"
"v_mfma_f32_16x16x32_bf16 a[24:27], a[48:51], a[152:155], a[24:27]\n"
"s_waitcnt vmcnt(11) lgkmcnt(14)\n"
"ds_read_b128 a[152:155], v4 offset:30720\n"
"v_and_b32 v15, s63, v19\n"
"v_mfma_f32_16x16x32_bf16 v[144:147], a[64:67], a[96:99], v[144:147]\n"
"v_mul_f32 v12, v12, v72\n"
"v_mul_f32 v13, v13, v73\n"
"v_mfma_f32_16x16x32_bf16 v[144:147], a[68:71], a[100:103], v[144:147]\n"
"global_load_dwordx4 a[48:51], v0, s[34:35] offset:0 sc1\n"
"v_mul_f32 v14, v14, v74\n"
"v_mfma_f32_16x16x32_bf16 v[172:175], a[76:79], a[100:103], v[172:175]\n"
"v_mul_f32 v15, v15, v75\n"
"v_cvt_pk_bf16_f32 v26, v12, v13\n"
"v_mfma_f32_16x16x32_bf16 v[172:175], a[72:75], a[96:99], v[172:175]\n"
"v_cvt_pk_bf16_f32 v27, v14, v15\n"
"v_mfma_f32_16x16x32_bf16 v[176:179], a[72:75], a[104:107], v[176:179]\n"
"ds_write_b64 v2, v[26:27] offset:6152\n"
"v_mfma_f32_16x16x32_bf16 v[176:179], a[76:79], a[108:111], v[176:179]\n"
"s_waitcnt lgkmcnt(8)\n"
"v_lshlrev_b32 v12, 16, v20\n"
"v_and_b32 v13, s63, v20\n"
"v_mfma_f32_16x16x32_bf16 v[148:151], a[68:71], a[108:111], v[148:151]\n"
"v_lshlrev_b32 v14, 16, v21\n"
"v_and_b32 v15, s63, v21\n"
"v_mfma_f32_16x16x32_bf16 v[148:151], a[64:67], a[104:107], v[148:151]\n"
"v_mul_f32 v12, v12, v100\n"
"v_mul_f32 v13, v13, v101\n"
"v_mfma_f32_16x16x32_bf16 v[152:155], a[64:67], a[112:115], v[152:155]\n"
"v_mul_f32 v14, v14, v102\n"
"v_mul_f32 v15, v15, v103\n"
"v_mfma_f32_16x16x32_bf16 v[152:155], a[68:71], a[116:119], v[152:155]\n"
"global_load_dwordx4 a[52:55], v0, s[34:35] offset:1024 sc1\n"
"v_cvt_pk_bf16_f32 v28, v12, v13\n"
"v_mfma_f32_16x16x32_bf16 v[180:183], a[76:79], a[116:119], v[180:183]\n"
"v_cvt_pk_bf16_f32 v29, v14, v15\n"
"v_lshlrev_b32 v12, 16, v22\n"
"v_mfma_f32_16x16x32_bf16 v[180:183], a[72:75], a[112:115], v[180:183]\n"
"v_and_b32 v13, s63, v22\n"
"v_lshlrev_b32 v14, 16, v23\n"
"v_mfma_f32_16x16x32_bf16 v[184:187], a[72:75], a[120:123], v[184:187]\n"
"v_and_b32 v15, s63, v23\n"
"v_mul_f32 v12, v12, v128\n"
"v_mfma_f32_16x16x32_bf16 v[184:187], a[76:79], a[124:127], v[184:187]\n"
"v_mul_f32 v13, v13, v129\n"
"v_mfma_f32_16x16x32_bf16 v[156:159], a[68:71], a[124:127], v[156:159]\n"
"v_mul_f32 v14, v14, v130\n"
"v_mfma_f32_16x16x32_bf16 v[156:159], a[64:67], a[120:123], v[156:159]\n"
"s_waitcnt lgkmcnt(6)\n"
"v_mul_f32 v15, v15, v131\n"
"v_cvt_pk_bf16_f32 v30, v12, v13\n"
"v_mfma_f32_16x16x32_bf16 v[160:163], a[64:67], a[128:131], v[160:163]\n"
"v_cvt_pk_bf16_f32 v31, v14, v15\n"
"v_mfma_f32_16x16x32_bf16 v[160:163], a[68:71], a[132:135], v[160:163]\n"
"global_load_dwordx4 a[56:59], v0, s[34:35] offset:2048 sc1\n"
"v_mfma_f32_16x16x32_bf16 v[188:191], a[76:79], a[132:135], v[188:191]\n"
"global_store_dwordx4 v0, v[28:31], s[52:53] offset:3072 sc1\n"
"ds_write_b128 v2, v[28:31] offset:7168\n"
"v_mfma_f32_16x16x32_bf16 v[188:191], a[72:75], a[128:131], v[188:191]\n"
"v_accvgpr_read_b32 v16, a160\n"
"v_accvgpr_read_b32 v17, a161\n"
"v_mfma_f32_16x16x32_bf16 v[192:195], a[72:75], a[136:139], v[192:195]\n"
"v_accvgpr_read_b32 v18, a162\n"
"v_mfma_f32_16x16x32_bf16 v[192:195], a[76:79], a[140:143], v[192:195]\n"
"v_accvgpr_read_b32 v19, a163\n"
"v_lshlrev_b32 v12, 16, v16\n"
"v_mfma_f32_16x16x32_bf16 v[164:167], a[68:71], a[140:143], v[164:167]\n"
"v_and_b32 v13, s63, v16\n"
"v_mfma_f32_16x16x32_bf16 v[164:167], a[64:67], a[136:139], v[164:167]\n"
"s_waitcnt lgkmcnt(2)\n"
"v_lshlrev_b32 v14, 16, v17\n"
"v_mfma_f32_16x16x32_bf16 v[168:171], a[64:67], a[144:147], v[168:171]\n"
"v_and_b32 v15, s63, v17\n"
"v_mul_f32 v12, v12, v48\n"
"v_mfma_f32_16x16x32_bf16 v[168:171], a[68:71], a[148:151], v[168:171]\n"
"global_load_dwordx4 a[60:63], v0, s[34:35] offset:3072 sc1\n"
"v_mul_f32 v13, v13, v49\n"
"v_mfma_f32_16x16x32_bf16 v[196:199], a[76:79], a[148:151], v[196:199]\n"
"v_mul_f32 v14, v14, v50\n"
"v_mfma_f32_16x16x32_bf16 v[196:199], a[72:75], a[144:147], v[196:199]\n"
"v_mul_f32 v15, v15, v51\n"
"v_cvt_pk_bf16_f32 v24, v12, v13\n"
"v_mfma_f32_16x16x32_bf16 a[20:23], a[72:75], a[152:155], a[20:23]\n"
"s_add_u32 s34, s34, 8192\n"
"v_cvt_pk_bf16_f32 v25, v14, v15\n"
"v_mfma_f32_16x16x32_bf16 a[20:23], a[76:79], a[156:159], a[20:23]\n"
"v_lshlrev_b32 v12, 16, v18\n"
"v_mfma_f32_16x16x32_bf16 a[16:19], a[68:71], a[156:159], a[16:19]\n"
"v_and_b32 v13, s63, v18\n"
"v_lshlrev_b32 v14, 16, v19\n"
"v_mfma_f32_16x16x32_bf16 a[16:19], a[64:67], a[152:155], a[16:19]\n"
"s_waitcnt vmcnt(11)\n"
"v_and_b32 v15, s63, v19\n"
"v_mul_f32 v12, v12, v76\n"
"v_mfma_f32_16x16x32_bf16 v[200:203], a[80:83], a[96:99], v[200:203]\n"
"v_mul_f32 v13, v13, v77\n"
"v_mul_f32 v14, v14, v78\n"
"v_mfma_f32_16x16x32_bf16 v[200:203], a[84:87], a[100:103], v[200:203]\n"
"global_load_dwordx4 a[64:67], v0, s[34:35] offset:-4096 sc1\n"
"v_mul_f32 v15, v15, v79\n"
"v_mfma_f32_16x16x32_bf16 v[228:231], a[92:95], a[100:103], v[228:231]\n"
"ds_read_b128 a[100:103], v3 offset:17408\n"
"v_cvt_pk_bf16_f32 v26, v12, v13\n"
"v_mfma_f32_16x16x32_bf16 v[228:231], a[88:91], a[96:99], v[228:231]\n"
"ds_read_b128 a[96:99], v3 offset:16384\n"
"v_cvt_pk_bf16_f32 v27, v14, v15\n"
"v_mfma_f32_16x16x32_bf16 v[232:235], a[88:91], a[104:107], v[232:235]\n"
"global_store_dwordx4 v0, v[24:27], s[54:55] offset:-4096 sc1\n"
"ds_write_b128 v2, v[24:27] offset:8192\n"
"v_mfma_f32_16x16x32_bf16 v[232:235], a[92:95], a[108:111], v[232:235]\n"
"v_accvgpr_read_b32 v16, a164\n"
"v_mfma_f32_16x16x32_bf16 v[204:207], a[84:87], a[108:111], v[204:207]\n"
"ds_read_b128 a[108:111], v3 offset:19456\n"
"v_accvgpr_read_b32 v17, a165\n"
"v_mfma_f32_16x16x32_bf16 v[204:207], a[80:83], a[104:107], v[204:207]\n"
"ds_read_b128 a[104:107], v3 offset:18432\n"
"v_accvgpr_read_b32 v18, a166\n"
"v_mfma_f32_16x16x32_bf16 v[208:211], a[80:83], a[112:115], v[208:211]\n"
"v_accvgpr_read_b32 v19, a167\n"
"v_lshlrev_b32 v12, 16, v16\n"
"v_mfma_f32_16x16x32_bf16 v[208:211], a[84:87], a[116:119], v[208:211]\n"
"global_load_dwordx4 a[68:71], v0, s[34:35] offset:-3072 sc1\n"
"v_and_b32 v13, s63, v16\n"
"v_mfma_f32_16x16x32_bf16 v[236:239], a[92:95], a[116:119], v[236:239]\n"
"ds_read_b128 a[116:119], v3 offset:21504\n"
"v_lshlrev_b32 v14, 16, v17\n"
"v_mfma_f32_16x16x32_bf16 v[236:239], a[88:91], a[112:115], v[236:239]\n"
"ds_read_b128 a[112:115], v3 offset:20480\n"
"v_and_b32 v15, s63, v17\n"
"v_mfma_f32_16x16x32_bf16 v[240:243], a[88:91], a[120:123], v[240:243]\n"
"v_mul_f32 v12, v12, v104\n"
"v_mul_f32 v13, v13, v105\n"
"v_mfma_f32_16x16x32_bf16 v[240:243], a[92:95], a[124:127], v[240:243]\n"
"v_mul_f32 v14, v14, v106\n"
"v_mul_f32 v15, v15, v107\n"
"v_mfma_f32_16x16x32_bf16 v[212:215], a[84:87], a[124:127], v[212:215]\n"
"ds_read_b128 a[124:127], v3 offset:23552\n"
"v_cvt_pk_bf16_f32 v28, v12, v13\n"
"v_mfma_f32_16x16x32_bf16 v[212:215], a[80:83], a[120:123], v[212:215]\n"
"ds_read_b128 a[120:123], v3 offset:22528\n"
"v_cvt_pk_bf16_f32 v29, v14, v15\n"
"v_mfma_f32_16x16x32_bf16 v[216:219], a[80:83], a[128:131], v[216:219]\n"
"L_st4475:\n"
"s_cbranch_execz L_retA\n"
"ds_write_b64 v2, v[28:29] offset:9216\n"
"v_mfma_f32_16x16x32_bf16 v[216:219], a[84:87], a[132:135], v[216:219]\n"
"global_load_dwordx4 a[72:75], v0, s[34:35] offset:-2048 sc1\n"
"v_lshlrev_b32 v12, 16, v18\n"
"v_mfma_f32_16x16x32_bf16 v[244:247], a[92:95], a[132:135], v[244:247]\n"
"ds_read_b128 a[132:135], v3 offset:25600\n"
"v_and_b32 v13, s63, v18\n"
"v_mfma_f32_16x16x32_bf16 v[244:247], a[88:91], a[128:131], v[244:247]\n"
"ds_read_b128 a[128:131], v3 offset:24576\n"
"v_lshlrev_b32 v14, 16, v19\n"
"v_mfma_f32_16x16x32_bf16 v[248:251], a[88:91], a[136:139], v[248:251]\n"
"v_and_b32 v15, s63, v19\n"
"v_mul_f32 v12, v12, v132\n"
"v_mfma_f32_16x16x32_bf16 v[248:251], a[92:95], a[140:143], v[248:251]\n"
"v_mul_f32 v13, v13, v133\n"
"v_mfma_f32_16x16x32_bf16 v[220:223], a[84:87], a[140:143], v[220:223]\n"
"ds_read_b128 a[140:143], v3 offset:27648\n"
"v_mul_f32 v14, v14, v134\n"
"v_mfma_f32_16x16x32_bf16 v[220:223], a[80:83], a[136:139], v[220:223]\n"
"ds_read_b128 a[136:139], v3 offset:26624\n"
"v_mul_f32 v15, v15, v135\n"
"v_mfma_f32_16x16x32_bf16 v[224:227], a[80:83], a[144:147], v[224:227]\n"
"v_cvt_pk_bf16_f32 v30, v12, v13\n"
"v_cvt_pk_bf16_f32 v31, v14, v15\n"
"v_mfma_f32_16x16x32_bf16 v[224:227], a[84:87], a[148:151], v[224:227]\n"
"global_load_dwordx4 a[76:79], v0, s[34:35] offset:-1024 sc1\n"
"v_mfma_f32_16x16x32_bf16 v[252:255], a[92:95], a[148:151], v[252:255]\n"
"ds_read_b128 a[148:151], v3 offset:29696\n"
"v_mfma_f32_16x16x32_bf16 v[252:255], a[88:91], a[144:147], v[252:255]\n"
"ds_read_b128 a[144:147], v3 offset:28672\n"
"v_mfma_f32_16x16x32_bf16 a[28:31], a[88:91], a[152:155], a[28:31]\n"
"ds_write_b64 v2, v[30:31] offset:9224\n"
"v_mfma_f32_16x16x32_bf16 a[28:31], a[92:95], a[156:159], a[28:31]\n"
"v_accvgpr_read_b32 v16, a168\n"
"v_accvgpr_read_b32 v17, a169\n"
"v_mfma_f32_16x16x32_bf16 a[24:27], a[84:87], a[156:159], a[24:27]\n"
"ds_read_b128 a[156:159], v3 offset:31744\n"
"v_accvgpr_read_b32 v18, a170\n"
"v_mfma_f32_16x16x32_bf16 a[24:27], a[80:83], a[152:155], a[24:27]\n"
"s_waitcnt vmcnt(10) lgkmcnt(13)\n"
"ds_read_b128 a[152:155], v3 offset:30720\n"
"v_accvgpr_read_b32 v19, a171\n"
"v_mfma_f32_16x16x32_bf16 v[144:147], a[32:35], a[96:99], v[144:147]\n"
"v_lshlrev_b32 v12, 16, v16\n"
"v_and_b32 v13, s63, v16\n"
"v_mfma_f32_16x16x32_bf16 v[144:147], a[36:39], a[100:103], v[144:147]\n"
"global_load_dwordx4 a[80:83], v0, s[34:35] offset:0 sc1\n"
"v_lshlrev_b32 v14, 16, v17\n"
"v_mfma_f32_16x16x32_bf16 v[172:175], a[44:47], a[100:103], v[172:175]\n"
"v_and_b32 v15, s63, v17\n"
"v_mul_f32 v12, v12, v52\n"
"v_mfma_f32_16x16x32_bf16 v[172:175], a[40:43], a[96:99], v[172:175]\n"
"v_mul_f32 v13, v13, v53\n"
"v_mul_f32 v14, v14, v54\n"
"v_mfma_f32_16x16x32_bf16 v[176:179], a[40:43], a[104:107], v[176:179]\n"
"v_mul_f32 v15, v15, v55\n"
"v_cvt_pk_bf16_f32 v24, v12, v13\n"
"v_mfma_f32_16x16x32_bf16 v[176:179], a[44:47], a[108:111], v[176:179]\n"
"v_cvt_pk_bf16_f32 v25, v14, v15\n"
"v_lshlrev_b32 v12, 16, v18\n"
"v_mfma_f32_16x16x32_bf16 v[148:151], a[36:39], a[108:111], v[148:151]\n"
"v_and_b32 v13, s63, v18\n"
"v_lshlrev_b32 v14, 16, v19\n"
"v_mfma_f32_16x16x32_bf16 v[148:151], a[32:35], a[104:107], v[148:151]\n"
"s_waitcnt lgkmcnt(10)\n"
"v_and_b32 v15, s63, v19\n"
"v_mul_f32 v12, v12, v80\n"
"v_mfma_f32_16x16x32_bf16 v[152:155], a[32:35], a[112:115], v[152:155]\n"
"v_mul_f32 v13, v13, v81\n"
"v_mul_f32 v14, v14, v82\n"
"v_mfma_f32_16x16x32_bf16 v[152:155], a[36:39], a[116:119], v[152:155]\n"
"global_load_dwordx4 a[84:87], v0, s[34:35] offset:1024 sc1\n"
"v_mul_f32 v15, v15, v83\n"
"v_mfma_f32_16x16x32_bf16 v[180:183], a[44:47], a[116:119], v[180:183]\n"
"v_cvt_pk_bf16_f32 v26, v12, v13\n"
"v_mfma_f32_16x16x32_bf16 v[180:183], a[40:43], a[112:115], v[180:183]\n"
"v_cvt_pk_bf16_f32 v27, v14, v15\n"
"v_mfma_f32_16x16x32_bf16 v[184:187], a[40:43], a[120:123], v[184:187]\n"
"global_store_dwordx4 v0, v[24:27], s[54:55] offset:-2048 sc1\n"
"ds_write_b128 v2, v[24:27] offset:10240\n"
"v_mfma_f32_16x16x32_bf16 v[184:187], a[44:47], a[124:127], v[184:187]\n"
"v_accvgpr_read_b32 v16, a172\n"
"v_mfma_f32_16x16x32_bf16 v[156:159], a[36:39], a[124:127], v[156:159]\n"
"v_accvgpr_read_b32 v17, a173\n"
"v_mfma_f32_16x16x32_bf16 v[156:159], a[32:35], a[120:123], v[156:159]\n"
"s_waitcnt lgkmcnt(6)\n"
"v_accvgpr_read_b32 v18, a174\n"
"v_accvgpr_read_b32 v19, a175\n"
"v_mfma_f32_16x16x32_bf16 v[160:163], a[32:35], a[128:131], v[160:163]\n"
"v_lshlrev_b32 v12, 16, v16\n"
"v_mfma_f32_16x16x32_bf16 v[160:163], a[36:39], a[132:135], v[160:163]\n"
"global_load_dwordx4 a[88:91], v0, s[34:35] offset:2048 sc1\n"
"v_and_b32 v13, s63, v16\n"
"v_mfma_f32_16x16x32_bf16 v[188:191], a[44:47], a[132:135], v[188:191]\n"
"v_lshlrev_b32 v14, 16, v17\n"
"v_and_b32 v15, s63, v17\n"
"v_mfma_f32_16x16x32_bf16 v[188:191], a[40:43], a[128:131], v[188:191]\n"
"v_mul_f32 v12, v12, v108\n"
"v_mfma_f32_16x16x32_bf16 v[192:195], a[40:43], a[136:139], v[192:195]\n"
"v_mul_f32 v13, v13, v109\n"
"v_mfma_f32_16x16x32_bf16 v[192:195], a[44:47], a[140:143], v[192:195]\n"
"v_mul_f32 v14, v14, v110\n"
"v_mul_f32 v15, v15, v111\n"
"v_mfma_f32_16x16x32_bf16 v[164:167], a[36:39], a[140:143], v[164:167]\n"
"v_cvt_pk_bf16_f32 v28, v12, v13\n"
"v_mfma_f32_16x16x32_bf16 v[164:167], a[32:35], a[136:139], v[164:167]\n"
"s_waitcnt lgkmcnt(1)\n"
"v_cvt_pk_bf16_f32 v29, v14, v15\n"
"v_mfma_f32_16x16x32_bf16 v[168:171], a[32:35], a[144:147], v[168:171]\n"
"v_lshlrev_b32 v12, 16, v18\n"
"v_and_b32 v13, s63, v18\n"
"v_mfma_f32_16x16x32_bf16 v[168:171], a[36:39], a[148:151], v[168:171]\n"
"global_load_dwordx4 a[92:95], v0, s[34:35] offset:3072 sc1\n"
"v_lshlrev_b32 v14, 16, v19\n"
"v_mfma_f32_16x16x32_bf16 v[196:199], a[44:47], a[148:151], v[196:199]\n"
"v_and_b32 v15, s63, v19\n"
"v_mfma_f32_16x16x32_bf16 v[196:199], a[40:43], a[144:147], v[196:199]\n"
"v_mul_f32 v12, v12, v136\n"
"v_mul_f32 v13, v13, v137\n"
"v_mfma_f32_16x16x32_bf16 a[20:23], a[40:43], a[152:155], a[20:23]\n"
"s_add_u32 s34, s34, 8192\n"
"v_mul_f32 v14, v14, v138\n"
"v_mfma_f32_16x16x32_bf16 a[20:23], a[44:47], a[156:159], a[20:23]\n"
"v_mul_f32 v15, v15, v139\n"
"v_mfma_f32_16x16x32_bf16 a[16:19], a[36:39], a[156:159], a[16:19]\n"
"v_cvt_pk_bf16_f32 v30, v12, v13\n"
"v_cvt_pk_bf16_f32 v31, v14, v15\n"
"v_mfma_f32_16x16x32_bf16 a[16:19], a[32:35], a[152:155], a[16:19]\n"
"s_waitcnt vmcnt(10)\n"
"global_store_dwordx4 v0, v[28:31], s[54:55] offset:-1024 sc1\n"
"ds_write_b128 v2, v[28:31] offset:11264\n"
"v_mfma_f32_16x16x32_bf16 v[200:203], a[48:51], a[96:99], v[200:203]\n"
"v_accvgpr_read_b32 v16, a176\n"
"v_mfma_f32_16x16x32_bf16 v[200:203], a[52:55], a[100:103], v[200:203]\n"
"global_load_dwordx4 a[32:35], v0, s[34:35] offset:-4096 sc1\n"
"v_accvgpr_read_b32 v17, a177\n"
"v_mfma_f32_16x16x32_bf16 v[228:231], a[60:63], a[100:103], v[228:231]\n"
"ds_read_b128 a[100:103], v2 offset:17408\n"
"v_accvgpr_read_b32 v18, a178\n"
"v_mfma_f32_16x16x32_bf16 v[228:231], a[56:59], a[96:99], v[228:231]\n"
"ds_read_b128 a[96:99], v2 offset:16384\n"
"v_accvgpr_read_b32 v19, a179\n"
"v_mfma_f32_16x16x32_bf16 v[232:235], a[56:59], a[104:107], v[232:235]\n"
"v_lshlrev_b32 v12, 16, v16\n"
"v_and_b32 v13, s63, v16\n"
"v_mfma_f32_16x16x32_bf16 v[232:235], a[60:63], a[108:111], v[232:235]\n"
"v_lshlrev_b32 v14, 16, v17\n"
"v_mfma_f32_16x16x32_bf16 v[204:207], a[52:55], a[108:111], v[204:207]\n"
"ds_read_b128 a[108:111], v2 offset:19456\n"
"v_and_b32 v15, s63, v17\n"
"v_mfma_f32_16x16x32_bf16 v[204:207], a[48:51], a[104:107], v[204:207]\n"
"ds_read_b128 a[104:107], v2 offset:18432\n"
"v_mul_f32 v12, v12, v56\n"
"v_mfma_f32_16x16x32_bf16 v[208:211], a[48:51], a[112:115], v[208:211]\n"
"v_mul_f32 v13, v13, v57\n"
"v_mul_f32 v14, v14, v58\n"
"v_mfma_f32_16x16x32_bf16 v[208:211], a[52:55], a[116:119], v[208:211]\n"
"global_load_dwordx4 a[36:39], v0, s[34:35] offset:-3072 sc1\n"
"v_mul_f32 v15, v15, v59\n"
"v_mfma_f32_16x16x32_bf16 v[236:239], a[60:63], a[116:119], v[236:239]\n"
"ds_read_b128 a[116:119], v2 offset:21504\n"
"v_cvt_pk_bf16_f32 v24, v12, v13\n"
"v_mfma_f32_16x16x32_bf16 v[236:239], a[56:59], a[112:115], v[236:239]\n"
"ds_read_b128 a[112:115], v2 offset:20480\n"
"v_cvt_pk_bf16_f32 v25, v14, v15\n"
"v_mfma_f32_16x16x32_bf16 v[240:243], a[56:59], a[120:123], v[240:243]\n"
"ds_write_b64 v2, v[24:25] offset:12288\n"
"v_mfma_f32_16x16x32_bf16 v[240:243], a[60:63], a[124:127], v[240:243]\n"
"v_lshlrev_b32 v12, 16, v18\n"
"v_and_b32 v13, s63, v18\n"
"v_mfma_f32_16x16x32_bf16 v[212:215], a[52:55], a[124:127], v[212:215]\n"
"ds_read_b128 a[124:127], v2 offset:23552\n"
"v_lshlrev_b32 v14, 16, v19\n"
"v_mfma_f32_16x16x32_bf16 v[212:215], a[48:51], a[120:123], v[212:215]\n"
"ds_read_b128 a[120:123], v2 offset:22528\n"
"v_and_b32 v15, s63, v19\n"
"v_mfma_f32_16x16x32_bf16 v[216:219], a[48:51], a[128:131], v[216:219]\n"
"v_mul_f32 v12, v12, v84\n"
"v_mul_f32 v13, v13, v85\n"
"v_mfma_f32_16x16x32_bf16 v[216:219], a[52:55], a[132:135], v[216:219]\n"
"global_load_dwordx4 a[40:43], v0, s[34:35] offset:-2048 sc1\n"
"v_mul_f32 v14, v14, v86\n"
"v_mfma_f32_16x16x32_bf16 v[244:247], a[60:63], a[132:135], v[244:247]\n"
"ds_read_b128 a[132:135], v2 offset:25600\n"
"v_mul_f32 v15, v15, v87\n"
"v_mfma_f32_16x16x32_bf16 v[244:247], a[56:59], a[128:131], v[244:247]\n"
"ds_read_b128 a[128:131], v2 offset:24576\n"
"v_cvt_pk_bf16_f32 v26, v12, v13\n"
"v_mfma_f32_16x16x32_bf16 v[248:251], a[56:59], a[136:139], v[248:251]\n"
"v_cvt_pk_bf16_f32 v27, v14, v15\n"
"v_mfma_f32_16x16x32_bf16 v[248:251], a[60:63], a[140:143], v[248:251]\n"
"ds_write_b64 v2, v[26:27] offset:12296\n"
"v_mfma_f32_16x16x32_bf16 v[220:223], a[52:55], a[140:143], v[220:223]\n"
"ds_read_b128 a[140:143], v2 offset:27648\n"
"v_accvgpr_read_b32 v16, a180\n"
"v_mfma_f32_16x16x32_bf16 v[220:223], a[48:51], a[136:139], v[220:223]\n"
"ds_read_b128 a[136:139], v2 offset:26624\n"
"v_accvgpr_read_b32 v17, a181\n"
"v_mfma_f32_16x16x32_bf16 v[224:227], a[48:51], a[144:147], v[224:227]\n"
"v_accvgpr_read_b32 v18, a182\n"
"v_accvgpr_read_b32 v19, a183\n"
"v_mfma_f32_16x16x32_bf16 v[224:227], a[52:55], a[148:151], v[224:227]\n"
"global_load_dwordx4 a[44:47], v0, s[34:35] offset:-1024 sc1\n"
"v_lshlrev_b32 v12, 16, v16\n"
"v_mfma_f32_16x16x32_bf16 v[252:255], a[60:63], a[148:151], v[252:255]\n"
"ds_read_b128 a[148:151], v2 offset:29696\n"
"v_and_b32 v13, s63, v16\n"
"v_mfma_f32_16x16x32_bf16 v[252:255], a[56:59], a[144:147], v[252:255]\n"
"ds_read_b128 a[144:147], v2 offset:28672\n"
"v_lshlrev_b32 v14, 16, v17\n"
"v_mfma_f32_16x16x32_bf16 a[28:31], a[56:59], a[152:155], a[28:31]\n"
"v_and_b32 v15, s63, v17\n"
"v_mul_f32 v12, v12, v112\n"
"v_mfma_f32_16x16x32_bf16 a[28:31], a[60:63], a[156:159], a[28:31]\n"
"v_mul_f32 v13, v13, v113\n"
"v_mul_f32 v14, v14, v114\n"
"v_mfma_f32_16x16x32_bf16 a[24:27], a[52:55], a[156:159], a[24:27]\n"
"ds_read_b128 a[156:159], v2 offset:31744\n"
"v_mul_f32 v15, v15, v115\n"
"v_mfma_f32_16x16x32_bf16 a[24:27], a[48:51], a[152:155], a[24:27]\n"
"s_waitcnt vmcnt(10) lgkmcnt(13)\n"
"ds_read_b128 a[152:155], v2 offset:30720\n"
"v_cvt_pk_bf16_f32 v28, v12, v13\n"
"v_mfma_f32_16x16x32_bf16 v[144:147], a[64:67], a[96:99], v[144:147]\n"
"v_cvt_pk_bf16_f32 v29, v14, v15\n"
"v_lshlrev_b32 v12, 16, v18\n"
"v_mfma_f32_16x16x32_bf16 v[144:147], a[68:71], a[100:103], v[144:147]\n"
"global_load_dwordx4 a[48:51], v0, s[34:35] offset:0 sc1\n"
"v_and_b32 v13, s63, v18\n"
"v_mfma_f32_16x16x32_bf16 v[172:175], a[76:79], a[100:103], v[172:175]\n"
"v_lshlrev_b32 v14, 16, v19\n"
"v_and_b32 v15, s63, v19\n"
"v_mfma_f32_16x16x32_bf16 v[172:175], a[72:75], a[96:99], v[172:175]\n"
"v_mul_f32 v12, v12, v140\n"
"v_mul_f32 v13, v13, v141\n"
"v_mfma_f32_16x16x32_bf16 v[176:179], a[72:75], a[104:107], v[176:179]\n"
"v_mul_f32 v14, v14, v142\n"
"v_mul_f32 v15, v15, v143\n"
"v_mfma_f32_16x16x32_bf16 v[176:179], a[76:79], a[108:111], v[176:179]\n"
"v_cvt_pk_bf16_f32 v30, v12, v13\n"
"v_mfma_f32_16x16x32_bf16 v[148:151], a[68:71], a[108:111], v[148:151]\n"
"v_cvt_pk_bf16_f32 v31, v14, v15\n"
"v_mfma_f32_16x16x32_bf16 v[148:151], a[64:67], a[104:107], v[148:151]\n"
"s_waitcnt lgkmcnt(9)\n"
"global_store_dwordx4 v0, v[28:31], s[54:55] offset:1024 sc1\n"
"ds_write_b128 v2, v[28:31] offset:13312\n"
"v_mfma_f32_16x16x32_bf16 v[152:155], a[64:67], a[112:115], v[152:155]\n"
"v_accvgpr_read_b32 v16, a184\n"
"v_accvgpr_read_b32 v17, a185\n"
"v_mfma_f32_16x16x32_bf16 v[152:155], a[68:71], a[116:119], v[152:155]\n"
"global_load_dwordx4 a[52:55], v0, s[34:35] offset:1024 sc1\n"
"v_accvgpr_read_b32 v18, a186\n"
"v_mfma_f32_16x16x32_bf16 v[180:183], a[76:79], a[116:119], v[180:183]\n"
"v_accvgpr_read_b32 v19, a187\n"
"v_mfma_f32_16x16x32_bf16 v[180:183], a[72:75], a[112:115], v[180:183]\n"
"v_lshlrev_b32 v12, 16, v16\n"
"v_mfma_f32_16x16x32_bf16 v[184:187], a[72:75], a[120:123], v[184:187]\n"
"v_and_b32 v13, s63, v16\n"
"v_lshlrev_b32 v14, 16, v17\n"
"v_mfma_f32_16x16x32_bf16 v[184:187], a[76:79], a[124:127], v[184:187]\n"
"v_and_b32 v15, s63, v17\n"
"v_mfma_f32_16x16x32_bf16 v[156:159], a[68:71], a[124:127], v[156:159]\n"
"v_accvgpr_read_b32 v10, a0\n"
"v_mfma_f32_16x16x32_bf16 v[156:159], a[64:67], a[120:123], v[156:159]\n"
"s_waitcnt lgkmcnt(5)\n"
"v_accvgpr_read_b32 v11, a1\n"
"v_mul_f32 v12, v12, v10\n"
"v_mfma_f32_16x16x32_bf16 v[160:163], a[64:67], a[128:131], v[160:163]\n"
"v_mul_f32 v13, v13, v11\n"
"v_mfma_f32_16x16x32_bf16 v[160:163], a[68:71], a[132:135], v[160:163]\n"
"global_load_dwordx4 a[56:59], v0, s[34:35] offset:2048 sc1\n"
"v_accvgpr_read_b32 v10, a2\n"
"v_mfma_f32_16x16x32_bf16 v[188:191], a[76:79], a[132:135], v[188:191]\n"
"v_accvgpr_read_b32 v11, a3\n"
"v_mul_f32 v14, v14, v10\n"
"v_mfma_f32_16x16x32_bf16 v[188:191], a[72:75], a[128:131], v[188:191]\n"
"v_mul_f32 v15, v15, v11\n"
"v_mfma_f32_16x16x32_bf16 v[192:195], a[72:75], a[136:139], v[192:195]\n"
"v_cvt_pk_bf16_f32 v24, v12, v13\n"
"v_mfma_f32_16x16x32_bf16 v[192:195], a[76:79], a[140:143], v[192:195]\n"
"v_cvt_pk_bf16_f32 v25, v14, v15\n"
"v_lshlrev_b32 v12, 16, v18\n"
"v_mfma_f32_16x16x32_bf16 v[164:167], a[68:71], a[140:143], v[164:167]\n"
"v_and_b32 v13, s63, v18\n"
"v_mfma_f32_16x16x32_bf16 v[164:167], a[64:67], a[136:139], v[164:167]\n"
"s_waitcnt lgkmcnt(1)\n"
"v_lshlrev_b32 v14, 16, v19\n"
"v_mfma_f32_16x16x32_bf16 v[168:171], a[64:67], a[144:147], v[168:171]\n"
"v_and_b32 v15, s63, v19\n"
"v_accvgpr_read_b32 v10, a4\n"
"v_mfma_f32_16x16x32_bf16 v[168:171], a[68:71], a[148:151], v[168:171]\n"
"global_load_dwordx4 a[60:63], v0, s[34:35] offset:3072 sc1\n"
"v_accvgpr_read_b32 v11, a5\n"
"v_mfma_f32_16x16x32_bf16 v[196:199], a[76:79], a[148:151], v[196:199]\n"
"v_mul_f32 v12, v12, v10\n"
"v_mfma_f32_16x16x32_bf16 v[196:199], a[72:75], a[144:147], v[196:199]\n"
"v_mul_f32 v13, v13, v11\n"
"v_accvgpr_read_b32 v10, a6\n"
"v_mfma_f32_16x16x32_bf16 a[20:23], a[72:75], a[152:155], a[20:23]\n"
"s_add_u32 s34, s34, 8192\n"
"v_accvgpr_read_b32 v11, a7\n"
"v_mfma_f32_16x16x32_bf16 a[20:23], a[76:79], a[156:159], a[20:23]\n"
"v_mul_f32 v14, v14, v10\n"
"v_mul_f32 v15, v15, v11\n"
"v_mfma_f32_16x16x32_bf16 a[16:19], a[68:71], a[156:159], a[16:19]\n"
"v_cvt_pk_bf16_f32 v26, v12, v13\n"
"v_cvt_pk_bf16_f32 v27, v14, v15\n"
"v_mfma_f32_16x16x32_bf16 a[16:19], a[64:67], a[152:155], a[16:19]\n"
"s_waitcnt vmcnt(10)\n"
"global_store_dwordx4 v0, v[24:27], s[54:55] offset:2048 sc1\n"
"ds_write_b128 v2, v[24:27] offset:14336\n"
"v_mfma_f32_16x16x32_bf16 v[200:203], a[80:83], a[96:99], v[200:203]\n"
"v_accvgpr_read_b32 v16, a188\n"
"v_mfma_f32_16x16x32_bf16 v[200:203], a[84:87], a[100:103], v[200:203]\n"
"global_load_dwordx4 a[64:67], v0, s[34:35] offset:-4096 sc1\n"
"v_accvgpr_read_b32 v17, a189\n"
"v_mfma_f32_16x16x32_bf16 v[228:231], a[92:95], a[100:103], v[228:231]\n"
"ds_read_b128 a[100:103], v2 offset:1024\n"
"v_accvgpr_read_b32 v18, a190\n"
"v_mfma_f32_16x16x32_bf16 v[228:231], a[88:91], a[96:99], v[228:231]\n"
"ds_read_b128 a[96:99], v2 offset:0\n"
"v_accvgpr_read_b32 v19, a191\n"
"v_mfma_f32_16x16x32_bf16 v[232:235], a[88:91], a[104:107], v[232:235]\n"
"v_lshlrev_b32 v12, 16, v16\n"
"v_and_b32 v13, s63, v16\n"
"v_mfma_f32_16x16x32_bf16 v[232:235], a[92:95], a[108:111], v[232:235]\n"
"v_lshlrev_b32 v14, 16, v17\n"
"v_and_b32 v15, s63, v17\n"
"v_mfma_f32_16x16x32_bf16 v[204:207], a[84:87], a[108:111], v[204:207]\n"
"ds_read_b128 a[108:111], v2 offset:3072\n"
"v_accvgpr_read_b32 v10, a8\n"
"v_mfma_f32_16x16x32_bf16 v[204:207], a[80:83], a[104:107], v[204:207]\n"
"ds_read_b128 a[104:107], v2 offset:2048\n"
"v_accvgpr_read_b32 v11, a9\n"
"v_mfma_f32_16x16x32_bf16 v[208:211], a[80:83], a[112:115], v[208:211]\n"
"v_mul_f32 v12, v12, v10\n"
"v_mul_f32 v13, v13, v11\n"
"v_mfma_f32_16x16x32_bf16 v[208:211], a[84:87], a[116:119], v[208:211]\n"
"global_load_dwordx4 a[68:71], v0, s[34:35] offset:-3072 sc1\n"
"v_accvgpr_read_b32 v10, a10\n"
"v_mfma_f32_16x16x32_bf16 v[236:239], a[92:95], a[116:119], v[236:239]\n"
"ds_read_b128 a[116:119], v2 offset:5120\n"
"v_accvgpr_read_b32 v11, a11\n"
"v_mfma_f32_16x16x32_bf16 v[236:239], a[88:91], a[112:115], v[236:239]\n"
"ds_read_b128 a[112:115], v2 offset:4096\n"
"v_mul_f32 v14, v14, v10\n"
"v_mfma_f32_16x16x32_bf16 v[240:243], a[88:91], a[120:123], v[240:243]\n"
"v_mul_f32 v15, v15, v11\n"
"v_cvt_pk_bf16_f32 v28, v12, v13\n"
"v_mfma_f32_16x16x32_bf16 v[240:243], a[92:95], a[124:127], v[240:243]\n"
"v_cvt_pk_bf16_f32 v29, v14, v15\n"
"v_lshlrev_b32 v12, 16, v18\n"
"v_mfma_f32_16x16x32_bf16 v[212:215], a[84:87], a[124:127], v[212:215]\n"
"ds_read_b128 a[124:127], v2 offset:7168\n"
"v_and_b32 v13, s63, v18\n"
"v_mfma_f32_16x16x32_bf16 v[212:215], a[80:83], a[120:123], v[212:215]\n"
"ds_read_b128 a[120:123], v2 offset:6144\n"
"v_lshlrev_b32 v14, 16, v19\n"
"v_mfma_f32_16x16x32_bf16 v[216:219], a[80:83], a[128:131], v[216:219]\n"
"v_and_b32 v15, s63, v19\n"
"v_accvgpr_read_b32 v10, a12\n"
"v_mfma_f32_16x16x32_bf16 v[216:219], a[84:87], a[132:135], v[216:219]\n"
"global_load_dwordx4 a[72:75], v0, s[34:35] offset:-2048 sc1\n"
"v_accvgpr_read_b32 v11, a13\n"
"v_mfma_f32_16x16x32_bf16 v[244:247], a[92:95], a[132:135], v[244:247]\n"
"ds_read_b128 a[132:135], v2 offset:9216\n"
"v_mul_f32 v12, v12, v10\n"
"v_mfma_f32_16x16x32_bf16 v[244:247], a[88:91], a[128:131], v[244:247]\n"
"ds_read_b128 a[128:131], v2 offset:8192\n"
"v_mul_f32 v13, v13, v11\n"
"v_mfma_f32_16x16x32_bf16 v[248:251], a[88:91], a[136:139], v[248:251]\n"
"v_accvgpr_read_b32 v10, a14\n"
"v_accvgpr_read_b32 v11, a15\n"
"v_mfma_f32_16x16x32_bf16 v[248:251], a[92:95], a[140:143], v[248:251]\n"
"v_mul_f32 v14, v14, v10\n"
"v_mul_f32 v15, v15, v11\n"
"v_mfma_f32_16x16x32_bf16 v[220:223], a[84:87], a[140:143], v[220:223]\n"
"ds_read_b128 a[140:143], v2 offset:11264\n"
"v_cvt_pk_bf16_f32 v30, v12, v13\n"
"v_mfma_f32_16x16x32_bf16 v[220:223], a[80:83], a[136:139], v[220:223]\n"
"ds_read_b128 a[136:139], v2 offset:10240\n"
"v_cvt_pk_bf16_f32 v31, v14, v15\n"
"v_mfma_f32_16x16x32_bf16 v[224:227], a[80:83], a[144:147], v[224:227]\n"
"global_store_dwordx4 v0, v[28:31], s[54:55] offset:3072 sc1\n"
"ds_write_b128 v2, v[28:31] offset:15360\n"
"v_mfma_f32_16x16x32_bf16 v[224:227], a[84:87], a[148:151], v[224:227]\n"
"global_load_dwordx4 a[76:79], v0, s[34:35] offset:-1024 sc1\n"
"v_mfma_f32_16x16x32_bf16 v[252:255], a[92:95], a[148:151], v[252:255]\n"
"ds_read_b128 a[148:151], v2 offset:13312\n"
"v_mfma_f32_16x16x32_bf16 v[252:255], a[88:91], a[144:147], v[252:255]\n"
"ds_read_b128 a[144:147], v2 offset:12288\n"
"v_mfma_f32_16x16x32_bf16 a[28:31], a[88:91], a[152:155], a[28:31]\n"
"s_add_u32 s62, s62, 1\n"
"v_mfma_f32_16x16x32_bf16 a[28:31], a[92:95], a[156:159], a[28:31]\n"
"v_mfma_f32_16x16x32_bf16 a[24:27], a[84:87], a[156:159], a[24:27]\n"
"ds_read_b128 a[156:159], v2 offset:15360\n"
"v_mfma_f32_16x16x32_bf16 a[24:27], a[80:83], a[152:155], a[24:27]\n"
"ds_read_b128 a[152:155], v2 offset:14336\n"
"L_layer:\n"
"s_waitcnt vmcnt(11) lgkmcnt(13)\n"
"v_mfma_f32_16x16x32_bf16 v[32:35], a[32:35], a[96:99], v[32:35]\n"
"s_mov_b64 s[96:97], s[52:53]\n"
"v_accvgpr_read_b32 v16, a192\n"
"v_accvgpr_read_b32 v17, a193\n"
"v_mfma_f32_16x16x32_bf16 v[32:35], a[36:39], a[100:103], v[32:35]\n"
"global_load_dwordx4 a[80:83], v0, s[34:35] offset:0 sc1\n"
"v_accvgpr_read_b32 v18, a194\n"
"v_mfma_f32_16x16x32_bf16 v[60:63], a[44:47], a[100:103], v[60:63]\n"
"s_mov_b64 s[98:99], s[54:55]\n"
"v_accvgpr_read_b32 v19, a195\n"
"v_mfma_f32_16x16x32_bf16 v[60:63], a[40:43], a[96:99], v[60:63]\n"
"v_lshlrev_b32 v12, 16, v16\n"
"v_and_b32 v13, s63, v16\n"
"v_mfma_f32_16x16x32_bf16 v[64:67], a[40:43], a[104:107], v[64:67]\n"
"s_add_u32 s52, s52, 131072\n"
"s_addc_u32 s53, s53, 0\n"
"v_lshlrev_b32 v14, 16, v17\n"
"v_mfma_f32_16x16x32_bf16 v[64:67], a[44:47], a[108:111], v[64:67]\n"
"v_and_b32 v15, s63, v17\n"
"v_mul_f32 v12, v12, v144\n"
"v_mfma_f32_16x16x32_bf16 v[36:39], a[36:39], a[108:111], v[36:39]\n"
"s_add_u32 s54, s54, 131072\n"
"s_addc_u32 s55, s55, 0\n"
"v_mul_f32 v13, v13, v145\n"
"v_mfma_f32_16x16x32_bf16 v[36:39], a[32:35], a[104:107], v[36:39]\n"
"s_waitcnt lgkmcnt(9)\n"
"v_mul_f32 v14, v14, v146\n"
"v_mul_f32 v15, v15, v147\n"
"v_mfma_f32_16x16x32_bf16 v[40:43], a[32:35], a[112:115], v[40:43]\n"
"ds_read_b128 v[20:23], v2 offset:0\n"
"v_cvt_pk_bf16_f32 v24, v12, v13\n"
"v_mfma_f32_16x16x32_bf16 v[40:43], a[36:39], a[116:119], v[40:43]\n"
"global_load_dwordx4 a[84:87], v0, s[34:35] offset:1024 sc1\n"
"v_cvt_pk_bf16_f32 v25, v14, v15\n"
"v_mfma_f32_16x16x32_bf16 v[68:71], a[44:47], a[116:119], v[68:71]\n"
"ds_write_b64 v2, v[24:25] offset:16384\n"
"v_mfma_f32_16x16x32_bf16 v[68:71], a[40:43], a[112:115], v[68:71]\n"
"v_lshlrev_b32 v12, 16, v18\n"
"v_and_b32 v13, s63, v18\n"
"v_mfma_f32_16x16x32_bf16 v[72:75], a[40:43], a[120:123], v[72:75]\n"
"s_waitcnt lgkmcnt(6)\n"
"s_barrier\n"
"v_mfma_f32_16x16x32_bf16 v[72:75], a[44:47], a[124:127], v[72:75]\n"
"v_lshlrev_b32 v14, 16, v19\n"
"v_and_b32 v15, s63, v19\n"
"v_mfma_f32_16x16x32_bf16 v[44:47], a[36:39], a[124:127], v[44:47]\n"
"v_mul_f32 v12, v12, v172\n"
"v_mul_f32 v13, v13, v173\n"
"v_mfma_f32_16x16x32_bf16 v[44:47], a[32:35], a[120:123], v[44:47]\n"
"v_mul_f32 v14, v14, v174\n"
"v_mfma_f32_16x16x32_bf16 v[48:51], a[32:35], a[128:131], v[48:51]\n"
"v_mul_f32 v15, v15, v175\n"
"v_mfma_f32_16x16x32_bf16 v[48:51], a[36:39], a[132:135], v[48:51]\n"
"global_load_dwordx4 a[88:91], v0, s[34:35] offset:2048 sc1\n"
"v_mfma_f32_16x16x32_bf16 v[76:79], a[44:47], a[132:135], v[76:79]\n"
"v_cvt_pk_bf16_f32 v26, v12, v13\n"
"v_mfma_f32_16x16x32_bf16 v[76:79], a[40:43], a[128:131], v[76:79]\n"
"v_cvt_pk_bf16_f32 v27, v14, v15\n"
"v_mfma_f32_16x16x32_bf16 v[80:83], a[40:43], a[136:139], v[80:83]\n"
"s_waitcnt lgkmcnt(1)\n"
"global_store_dwordx4 v0, v[20:23], s[96:97] offset:-4096 sc1\n"
"ds_write_b64 v2, v[26:27] offset:16392\n"
"v_accvgpr_read_b32 v16, a196\n"
"v_mfma_f32_16x16x32_bf16 v[80:83], a[44:47], a[140:143], v[80:83]\n"
"v_accvgpr_read_b32 v17, a197\n"
"v_accvgpr_read_b32 v18, a198\n"
"v_mfma_f32_16x16x32_bf16 v[52:55], a[36:39], a[140:143], v[52:55]\n"
"v_accvgpr_read_b32 v19, a199\n"
"v_lshlrev_b32 v12, 16, v16\n"
"v_mfma_f32_16x16x32_bf16 v[52:55], a[32:35], a[136:139], v[52:55]\n"
"v_and_b32 v13, s63, v16\n"
"v_lshlrev_b32 v14, 16, v17\n"
"v_mfma_f32_16x16x32_bf16 v[56:59], a[32:35], a[144:147], v[56:59]\n"
"v_and_b32 v15, s63, v17\n"
"v_mul_f32 v12, v12, v200\n"
"v_mfma_f32_16x16x32_bf16 v[56:59], a[36:39], a[148:151], v[56:59]\n"
"global_load_dwordx4 a[92:95], v0, s[34:35] offset:3072 sc1\n"
"v_mul_f32 v13, v13, v201\n"
"v_mfma_f32_16x16x32_bf16 v[84:87], a[44:47], a[148:151], v[84:87]\n"
"v_mul_f32 v14, v14, v202\n"
"v_mul_f32 v15, v15, v203\n"
"v_mfma_f32_16x16x32_bf16 v[84:87], a[40:43], a[144:147], v[84:87]\n"
"v_cvt_pk_bf16_f32 v28, v12, v13\n"
"v_cvt_pk_bf16_f32 v29, v14, v15\n"
"v_mfma_f32_16x16x32_bf16 a[4:7], a[40:43], a[152:155], a[4:7]\n"
"s_add_u32 s34, s34, 8192\n"
"ds_write_b64 v2, v[28:29] offset:17408\n"
"v_mfma_f32_16x16x32_bf16 a[4:7], a[44:47], a[156:159], a[4:7]\n"
"v_lshlrev_b32 v12, 16, v18\n"
"v_mfma_f32_16x16x32_bf16 a[0:3], a[36:39], a[156:159], a[0:3]\n"
"v_and_b32 v13, s63, v18\n"
"v_lshlrev_b32 v14, 16, v19\n"
"v_mfma_f32_16x16x32_bf16 a[0:3], a[32:35], a[152:155], a[0:3]\n"
"s_waitcnt vmcnt(11)\n"
"v_and_b32 v15, s63, v19\n"
"v_mul_f32 v12, v12, v228\n"
"v_mfma_f32_16x16x32_bf16 v[88:91], a[48:51], a[96:99], v[88:91]\n"
"v_mul_f32 v13, v13, v229\n"
"v_mul_f32 v14, v14, v230\n"
"v_mfma_f32_16x16x32_bf16 v[88:91], a[52:55], a[100:103], v[88:91]\n"
"global_load_dwordx4 a[32:35], v0, s[34:35] offset:-4096 sc1\n"
"v_mul_f32 v15, v15, v231\n"
"v_mfma_f32_16x16x32_bf16 v[116:119], a[60:63], a[100:103], v[116:119]\n"
"ds_read_b128 a[100:103], v3 offset:1024\n"
"v_cvt_pk_bf16_f32 v30, v12, v13\n"
"v_mfma_f32_16x16x32_bf16 v[116:119], a[56:59], a[96:99], v[116:119]\n"
"ds_read_b128 a[96:99], v3 offset:0\n"
"v_cvt_pk_bf16_f32 v31, v14, v15\n"
"v_mfma_f32_16x16x32_bf16 v[120:123], a[56:59], a[104:107], v[120:123]\n"
"ds_write_b64 v2, v[30:31] offset:17416\n"
"v_mfma_f32_16x16x32_bf16 v[120:123], a[60:63], a[108:111], v[120:123]\n"
"ds_read_b128 v[20:23], v2 offset:3072\n"
"v_accvgpr_read_b32 v16, a200\n"
"v_mfma_f32_16x16x32_bf16 v[92:95], a[52:55], a[108:111], v[92:95]\n"
"ds_read_b128 a[108:111], v3 offset:3072\n"
"v_accvgpr_read_b32 v17, a201\n"
"v_mfma_f32_16x16x32_bf16 v[92:95], a[48:51], a[104:107], v[92:95]\n"
"ds_read_b128 a[104:107], v3 offset:2048\n"
"v_accvgpr_read_b32 v18, a202\n"
"v_mfma_f32_16x16x32_bf16 v[96:99], a[48:51], a[112:115], v[96:99]\n"
"v_accvgpr_read_b32 v19, a203\n"
"v_lshlrev_b32 v12, 16, v16\n"
"v_mfma_f32_16x16x32_bf16 v[96:99], a[52:55], a[116:119], v[96:99]\n"
"global_load_dwordx4 a[36:39], v0, s[34:35] offset:-3072 sc1\n"
"v_and_b32 v13, s63, v16\n"
"v_mfma_f32_16x16x32_bf16 v[124:127], a[60:63], a[116:119], v[124:127]\n"
"ds_read_b128 a[116:119], v3 offset:5120\n"
"v_lshlrev_b32 v14, 16, v17\n"
"v_mfma_f32_16x16x32_bf16 v[124:127], a[56:59], a[112:115], v[124:127]\n"
"ds_read_b128 a[112:115], v3 offset:4096\n"
"v_and_b32 v15, s63, v17\n"
"v_mfma_f32_16x16x32_bf16 v[128:131], a[56:59], a[120:123], v[128:131]\n"
"v_mul_f32 v12, v12, v148\n"
"v_mul_f32 v13, v13, v149\n"
"v_mfma_f32_16x16x32_bf16 v[128:131], a[60:63], a[124:127], v[128:131]\n"
"v_mul_f32 v14, v14, v150\n"
"v_mul_f32 v15, v15, v151\n"
"v_mfma_f32_16x16x32_bf16 v[100:103], a[52:55], a[124:127], v[100:103]\n"
"ds_read_b128 a[124:127], v3 offset:7168\n"
"v_cvt_pk_bf16_f32 v24, v12, v13\n"
"v_mfma_f32_16x16x32_bf16 v[100:103], a[48:51], a[120:123], v[100:103]\n"
"ds_read_b128 a[120:123], v3 offset:6144\n"
"v_cvt_pk_bf16_f32 v25, v14, v15\n"
"v_mfma_f32_16x16x32_bf16 v[104:107], a[48:51], a[128:131], v[104:107]\n"
"s_waitcnt lgkmcnt(6)\n"
"global_store_dwordx4 v0, v[20:23], s[96:97] offset:-1024 sc1\n"
"ds_write_b64 v2, v[24:25] offset:18432\n"
"v_lshlrev_b32 v12, 16, v18\n"
"v_mfma_f32_16x16x32_bf16 v[104:107], a[52:55], a[132:135], v[104:107]\n"
"global_load_dwordx4 a[40:43], v0, s[34:35] offset:-2048 sc1\n"
"v_and_b32 v13, s63, v18\n"
"v_mfma_f32_16x16x32_bf16 v[132:135], a[60:63], a[132:135], v[132:135]\n"
"ds_read_b128 a[132:135], v3 offset:9216\n"
"v_lshlrev_b32 v14, 16, v19\n"
"v_mfma_f32_16x16x32_bf16 v[132:135], a[56:59], a[128:131], v[132:135]\n"
"ds_read_b128 a[128:131], v3 offset:8192\n"
"v_and_b32 v15, s63, v19\n"
"v_mfma_f32_16x16x32_bf16 v[136:139], a[56:59], a[136:139], v[136:139]\n"
"v_mul_f32 v12, v12, v176\n"
"v_mul_f32 v13, v13, v177\n"
"v_mfma_f32_16x16x32_bf16 v[136:139], a[60:63], a[140:143], v[136:139]\n"
"v_mul_f32 v14, v14, v178\n"
"v_mul_f32 v15, v15, v179\n"
"v_mfma_f32_16x16x32_bf16 v[108:111], a[52:55], a[140:143], v[108:111]\n"
"ds_read_b128 a[140:143], v3 offset:11264\n"
"v_cvt_pk_bf16_f32 v26, v12, v13\n"
"v_mfma_f32_16x16x32_bf16 v[108:111], a[48:51], a[136:139], v[108:111]\n"
"ds_read_b128 a[136:139], v3 offset:10240\n"
"v_cvt_pk_bf16_f32 v27, v14, v15\n"
"v_mfma_f32_16x16x32_bf16 v[112:115], a[48:51], a[144:147], v[112:115]\n"
"ds_write_b64 v2, v[26:27] offset:18440\n"
"v_mfma_f32_16x16x32_bf16 v[112:115], a[52:55], a[148:151], v[112:115]\n"
"global_load_dwordx4 a[44:47], v0, s[34:35] offset:-1024 sc1\n"
"v_accvgpr_read_b32 v16, a204\n"
"v_mfma_f32_16x16x32_bf16 v[140:143], a[60:63], a[148:151], v[140:143]\n"
"ds_read_b128 a[148:151], v3 offset:13312\n"
"v_accvgpr_read_b32 v17, a205\n"
"v_mfma_f32_16x16x32_bf16 v[140:143], a[56:59], a[144:147], v[140:143]\n"
"ds_read_b128 a[144:147], v3 offset:12288\n"
"v_accvgpr_read_b32 v18, a206\n"
"v_mfma_f32_16x16x32_bf16 a[12:15], a[56:59], a[152:155], a[12:15]\n"
"v_accvgpr_read_b32 v19, a207\n"
"v_lshlrev_b32 v12, 16, v16\n"
"v_mfma_f32_16x16x32_bf16 a[12:15], a[60:63], a[156:159], a[12:15]\n"
"v_and_b32 v13, s63, v16\n"
"v_lshlrev_b32 v14, 16, v17\n"
"v_mfma_f32_16x16x32_bf16 a[8:11], a[52:55], a[156:159], a[8:11]\n"
"ds_read_b128 a[156:159], v3 offset:15360\n"
"v_and_b32 v15, s63, v17\n"
"v_mfma_f32_16x16x32_bf16 a[8:11], a[48:51], a[152:155], a[8:11]\n"
"s_waitcnt vmcnt(10) lgkmcnt(13)\n"
"ds_read_b128 a[152:155], v3 offset:14336\n"
"v_mul_f32 v12, v12, v204\n"
"v_mfma_f32_16x16x32_bf16 v[32:35], a[64:67], a[96:99], v[32:35]\n"
"ds_read_b128 v[20:23], v2 offset:6144\n"
"v_mul_f32 v13, v13, v205\n"
"v_mfma_f32_16x16x32_bf16 v[32:35], a[68:71], a[100:103], v[32:35]\n"
"global_load_dwordx4 a[48:51], v0, s[34:35] offset:0 sc1\n"
"v_mul_f32 v14, v14, v206\n"
"v_mfma_f32_16x16x32_bf16 v[60:63], a[76:79], a[100:103], v[60:63]\n"
"v_mul_f32 v15, v15, v207\n"
"v_cvt_pk_bf16_f32 v28, v12, v13\n"
"v_mfma_f32_16x16x32_bf16 v[60:63], a[72:75], a[96:99], v[60:63]\n"
"L_st5078:\n"
"s_cbranch_execz L_st7361\n"
"v_cvt_pk_bf16_f32 v29, v14, v15\n"
"v_mfma_f32_16x16x32_bf16 v[64:67], a[72:75], a[104:107], v[64:67]\n"
"ds_write_b64 v2, v[28:29] offset:19456\n"
"v_mfma_f32_16x16x32_bf16 v[64:67], a[76:79], a[108:111], v[64:67]\n"
"v_lshlrev_b32 v12, 16, v18\n"
"v_and_b32 v13, s63, v18\n"
"v_mfma_f32_16x16x32_bf16 v[36:39], a[68:71], a[108:111], v[36:39]\n"
"v_lshlrev_b32 v14, 16, v19\n"
"v_and_b32 v15, s63, v19\n"
"v_mfma_f32_16x16x32_bf16 v[36:39], a[64:67], a[104:107], v[36:39]\n"
"s_waitcnt lgkmcnt(12)\n"
"v_mul_f32 v12, v12, v232\n"
"v_mul_f32 v13, v13, v233\n"
"v_mfma_f32_16x16x32_bf16 v[40:43], a[64:67], a[112:115], v[40:43]\n"
"v_mul_f32 v14, v14, v234\n"
"v_mfma_f32_16x16x32_bf16 v[40:43], a[68:71], a[116:119], v[40:43]\n"
"global_load_dwordx4 a[52:55], v0, s[34:35] offset:1024 sc1\n"
"v_mul_f32 v15, v15, v235\n"
"v_mfma_f32_16x16x32_bf16 v[68:71], a[76:79], a[116:119], v[68:71]\n"
"v_cvt_pk_bf16_f32 v30, v12, v13\n"
"v_mfma_f32_16x16x32_bf16 v[68:71], a[72:75], a[112:115], v[68:71]\n"
"v_cvt_pk_bf16_f32 v31, v14, v15\n"
"v_mfma_f32_16x16x32_bf16 v[72:75], a[72:75], a[120:123], v[72:75]\n"
"s_waitcnt lgkmcnt(1)\n"
"global_store_dwordx4 v0, v[20:23], s[96:97] offset:2048 sc1\n"
"ds_write_b64 v2, v[30:31] offset:19464\n"
"v_accvgpr_read_b32 v16, a208\n"
"v_mfma_f32_16x16x32_bf16 v[72:75], a[76:79], a[124:127], v[72:75]\n"
"v_accvgpr_read_b32 v17, a209\n"
"v_accvgpr_read_b32 v18, a210\n"
"v_mfma_f32_16x16x32_bf16 v[44:47], a[68:71], a[124:127], v[44:47]\n"
"v_accvgpr_read_b32 v19, a211\n"
"v_mfma_f32_16x16x32_bf16 v[44:47], a[64:67], a[120:123], v[44:47]\n"
"v_lshlrev_b32 v12, 16, v16\n"
"v_and_b32 v13, s63, v16\n"
"v_mfma_f32_16x16x32_bf16 v[48:51], a[64:67], a[128:131], v[48:51]\n"
"v_lshlrev_b32 v14, 16, v17\n"
"v_mfma_f32_16x16x32_bf16 v[48:51], a[68:71], a[132:135], v[48:51]\n"
"global_load_dwordx4 a[56:59], v0, s[34:35] offset:2048 sc1\n"
"v_and_b32 v15, s63, v17\n"
"v_mfma_f32_16x16x32_bf16 v[76:79], a[76:79], a[132:135], v[76:79]\n"
"v_mul_f32 v12, v12, v152\n"
"v_mul_f32 v13, v13, v153\n"
"v_mfma_f32_16x16x32_bf16 v[76:79], a[72:75], a[128:131], v[76:79]\n"
"v_mul_f32 v14, v14, v154\n"
"v_mul_f32 v15, v15, v155\n"
"v_mfma_f32_16x16x32_bf16 v[80:83], a[72:75], a[136:139], v[80:83]\n"
"v_cvt_pk_bf16_f32 v24, v12, v13\n"
"v_cvt_pk_bf16_f32 v25, v14, v15\n"
"v_mfma_f32_16x16x32_bf16 v[80:83], a[76:79], a[140:143], v[80:83]\n"
"ds_write_b64 v2, v[24:25] offset:20480\n"
"v_mfma_f32_16x16x32_bf16 v[52:55], a[68:71], a[140:143], v[52:55]\n"
"v_lshlrev_b32 v12, 16, v18\n"
"v_mfma_f32_16x16x32_bf16 v[52:55], a[64:67], a[136:139], v[52:55]\n"
"v_and_b32 v13, s63, v18\n"
"v_lshlrev_b32 v14, 16, v19\n"
"v_mfma_f32_16x16x32_bf16 v[56:59], a[64:67], a[144:147], v[56:59]\n"
"v_and_b32 v15, s63, v19\n"
"v_mul_f32 v12, v12, v180\n"
"v_mfma_f32_16x16x32_bf16 v[56:59], a[68:71], a[148:151], v[56:59]\n"
"global_load_dwordx4 a[60:63], v0, s[34:35] offset:3072 sc1\n"
"v_mul_f32 v13, v13, v181\n"
"v_mfma_f32_16x16x32_bf16 v[84:87], a[76:79], a[148:151], v[84:87]\n"
"v_mul_f32 v14, v14, v182\n"
"v_mul_f32 v15, v15, v183\n"
"v_mfma_f32_16x16x32_bf16 v[84:87], a[72:75], a[144:147], v[84:87]\n"
"v_cvt_pk_bf16_f32 v26, v12, v13\n"
"v_cvt_pk_bf16_f32 v27, v14, v15\n"
"v_mfma_f32_16x16x32_bf16 a[4:7], a[72:75], a[152:155], a[4:7]\n"
"s_add_u32 s34, s34, 8192\n"
"ds_write_b64 v2, v[26:27] offset:20488\n"
"v_mfma_f32_16x16x32_bf16 a[4:7], a[76:79], a[156:159], a[4:7]\n"
"ds_read_b128 v[20:23], v2 offset:9216\n"
"v_mfma_f32_16x16x32_bf16 a[0:3], a[68:71], a[156:159], a[0:3]\n"
"v_accvgpr_read_b32 v16, a212\n"
"v_mfma_f32_16x16x32_bf16 a[0:3], a[64:67], a[152:155], a[0:3]\n"
"s_waitcnt vmcnt(10)\n"
"v_accvgpr_read_b32 v17, a213\n"
"v_accvgpr_read_b32 v18, a214\n"
"v_mfma_f32_16x16x32_bf16 v[88:91], a[80:83], a[96:99], v[88:91]\n"
"v_accvgpr_read_b32 v19, a215\n"
"v_lshlrev_b32 v12, 16, v16\n"
"v_mfma_f32_16x16x32_bf16 v[88:91], a[84:87], a[100:103], v[88:91]\n"
"global_load_dwordx4 a[64:67], v0, s[34:35] offset:-4096 sc1\n"
"v_and_b32 v13, s63, v16\n"
"v_mfma_f32_16x16x32_bf16 v[116:119], a[92:95], a[100:103], v[116:119]\n"
"ds_read_b128 a[100:103], v4 offset:1024\n"
"v_lshlrev_b32 v14, 16, v17\n"
"v_mfma_f32_16x16x32_bf16 v[116:119], a[88:91], a[96:99], v[116:119]\n"
"ds_read_b128 a[96:99], v4 offset:0\n"
"v_and_b32 v15, s63, v17\n"
"v_mfma_f32_16x16x32_bf16 v[120:123], a[88:91], a[104:107], v[120:123]\n"
"v_mul_f32 v12, v12, v208\n"
"v_mul_f32 v13, v13, v209\n"
"v_mfma_f32_16x16x32_bf16 v[120:123], a[92:95], a[108:111], v[120:123]\n"
"v_mul_f32 v14, v14, v210\n"
"v_mul_f32 v15, v15, v211\n"
"v_mfma_f32_16x16x32_bf16 v[92:95], a[84:87], a[108:111], v[92:95]\n"
"ds_read_b128 a[108:111], v4 offset:3072\n"
"v_cvt_pk_bf16_f32 v28, v12, v13\n"
"v_mfma_f32_16x16x32_bf16 v[92:95], a[80:83], a[104:107], v[92:95]\n"
"ds_read_b128 a[104:107], v4 offset:2048\n"
"v_cvt_pk_bf16_f32 v29, v14, v15\n"
"v_mfma_f32_16x16x32_bf16 v[96:99], a[80:83], a[112:115], v[96:99]\n"
"s_waitcnt lgkmcnt(4)\n"
"global_store_dwordx4 v0, v[20:23], s[98:99] offset:-3072 sc1\n"
"ds_write_b64 v2, v[28:29] offset:21504\n"
"v_lshlrev_b32 v12, 16, v18\n"
"v_mfma_f32_16x16x32_bf16 v[96:99], a[84:87], a[116:119], v[96:99]\n"
"global_load_dwordx4 a[68:71], v0, s[34:35] offset:-3072 sc1\n"
"v_and_b32 v13, s63, v18\n"
"v_mfma_f32_16x16x32_bf16 v[124:127], a[92:95], a[116:119], v[124:127]\n"
"ds_read_b128 a[116:119], v4 offset:5120\n"
"v_lshlrev_b32 v14, 16, v19\n"
"v_mfma_f32_16x16x32_bf16 v[124:127], a[88:91], a[112:115], v[124:127]\n"
"ds_read_b128 a[112:115], v4 offset:4096\n"
"v_and_b32 v15, s63, v19\n"
"v_mfma_f32_16x16x32_bf16 v[128:131], a[88:91], a[120:123], v[128:131]\n"
"v_mul_f32 v12, v12, v236\n"
"v_mul_f32 v13, v13, v237\n"
"v_mfma_f32_16x16x32_bf16 v[128:131], a[92:95], a[124:127], v[128:131]\n"
"v_mul_f32 v14, v14, v238\n"
"v_mul_f32 v15, v15, v239\n"
"v_mfma_f32_16x16x32_bf16 v[100:103], a[84:87], a[124:127], v[100:103]\n"
"ds_read_b128 a[124:127], v4 offset:7168\n"
"v_cvt_pk_bf16_f32 v30, v12, v13\n"
"v_mfma_f32_16x16x32_bf16 v[100:103], a[80:83], a[120:123], v[100:103]\n"
"ds_read_b128 a[120:123], v4 offset:6144\n"
"v_cvt_pk_bf16_f32 v31, v14, v15\n"
"v_mfma_f32_16x16x32_bf16 v[104:107], a[80:83], a[128:131], v[104:107]\n"
"ds_write_b64 v2, v[30:31] offset:21512\n"
"v_mfma_f32_16x16x32_bf16 v[104:107], a[84:87], a[132:135], v[104:107]\n"
"global_load_dwordx4 a[72:75], v0, s[34:35] offset:-2048 sc1\n"
"v_accvgpr_read_b32 v16, a216\n"
"v_mfma_f32_16x16x32_bf16 v[132:135], a[92:95], a[132:135], v[132:135]\n"
"ds_read_b128 a[132:135], v4 offset:9216\n"
"v_accvgpr_read_b32 v17, a217\n"
"v_mfma_f32_16x16x32_bf16 v[132:135], a[88:91], a[128:131], v[132:135]\n"
"ds_read_b128 a[128:131], v4 offset:8192\n"
"v_accvgpr_read_b32 v18, a218\n"
"v_mfma_f32_16x16x32_bf16 v[136:139], a[88:91], a[136:139], v[136:139]\n"
"v_accvgpr_read_b32 v19, a219\n"
"v_mfma_f32_16x16x32_bf16 v[136:139], a[92:95], a[140:143], v[136:139]\n"
"v_lshlrev_b32 v12, 16, v16\n"
"v_and_b32 v13, s63, v16\n"
"v_mfma_f32_16x16x32_bf16 v[108:111], a[84:87], a[140:143], v[108:111]\n"
"ds_read_b128 a[140:143], v4 offset:11264\n"
"v_lshlrev_b32 v14, 16, v17\n"
"v_mfma_f32_16x16x32_bf16 v[108:111], a[80:83], a[136:139], v[108:111]\n"
"ds_read_b128 a[136:139], v4 offset:10240\n"
"v_and_b32 v15, s63, v17\n"
"v_mfma_f32_16x16x32_bf16 v[112:115], a[80:83], a[144:147], v[112:115]\n"
"ds_read_b128 v[20:23], v2 offset:12288\n"
"v_mul_f32 v12, v12, v156\n"
"v_mfma_f32_16x16x32_bf16 v[112:115], a[84:87], a[148:151], v[112:115]\n"
"global_load_dwordx4 a[76:79], v0, s[34:35] offset:-1024 sc1\n"
"v_mfma_f32_16x16x32_bf16 v[140:143], a[92:95], a[148:151], v[140:143]\n"
"ds_read_b128 a[148:151], v4 offset:13312\n"
"v_mfma_f32_16x16x32_bf16 v[140:143], a[88:91], a[144:147], v[140:143]\n"
"ds_read_b128 a[144:147], v4 offset:12288\n"
"v_mfma_f32_16x16x32_bf16 a[12:15], a[88:91], a[152:155], a[12:15]\n"
"v_mul_f32 v13, v13, v157\n"
"v_mfma_f32_16x16x32_bf16 a[12:15], a[92:95], a[156:159], a[12:15]\n"
"v_mul_f32 v14, v14, v158\n"
"v_mfma_f32_16x16x32_bf16 a[8:11], a[84:87], a[156:159], a[8:11]\n"
"ds_read_b128 a[156:159], v4 offset:15360\n"
"v_mfma_f32_16x16x32_bf16 a[8:11], a[80:83], a[152:155], a[8:11]\n"
"s_waitcnt vmcnt(10) lgkmcnt(14)\n"
"ds_read_b128 a[152:155], v4 offset:14336\n"
"v_mfma_f32_16x16x32_bf16 v[32:35], a[32:35], a[96:99], v[32:35]\n"
"v_mul_f32 v15, v15, v159\n"
"v_mfma_f32_16x16x32_bf16 v[32:35], a[36:39], a[100:103], v[32:35]\n"
"global_load_dwordx4 a[80:83], v0, s[34:35] offset:0 sc1\n"
"v_mfma_f32_16x16x32_bf16 v[60:63], a[44:47], a[100:103], v[60:63]\n"
"s_add_u32 s65, s65, 1\n"
"v_cvt_pk_bf16_f32 v24, v12, v13\n"
"v_mfma_f32_16x16x32_bf16 v[60:63], a[40:43], a[96:99], v[60:63]\n"
"v_cvt_pk_bf16_f32 v25, v14, v15\n"
"v_mfma_f32_16x16x32_bf16 v[64:67], a[40:43], a[104:107], v[64:67]\n"
"s_waitcnt lgkmcnt(4)\n"
"global_store_dwordx4 v0, v[20:23], s[98:99] offset:0 sc1\n"
"ds_write_b64 v2, v[24:25] offset:22528\n"
"v_lshlrev_b32 v12, 16, v18\n"
"v_mfma_f32_16x16x32_bf16 v[64:67], a[44:47], a[108:111], v[64:67]\n"
"v_and_b32 v13, s63, v18\n"
"v_lshlrev_b32 v14, 16, v19\n"
"v_mfma_f32_16x16x32_bf16 v[36:39], a[36:39], a[108:111], v[36:39]\n"
"v_and_b32 v15, s63, v19\n"
"v_mul_f32 v12, v12, v184\n"
"v_mfma_f32_16x16x32_bf16 v[36:39], a[32:35], a[104:107], v[36:39]\n"
"v_mul_f32 v13, v13, v185\n"
"v_mul_f32 v14, v14, v186\n"
"v_mfma_f32_16x16x32_bf16 v[40:43], a[32:35], a[112:115], v[40:43]\n"
"v_mul_f32 v15, v15, v187\n"
"v_cvt_pk_bf16_f32 v26, v12, v13\n"
"v_mfma_f32_16x16x32_bf16 v[40:43], a[36:39], a[116:119], v[40:43]\n"
"global_load_dwordx4 a[84:87], v0, s[34:35] offset:1024 sc1\n"
"v_cvt_pk_bf16_f32 v27, v14, v15\n"
"v_mfma_f32_16x16x32_bf16 v[68:71], a[44:47], a[116:119], v[68:71]\n"
"ds_write_b64 v2, v[26:27] offset:22536\n"
"v_mfma_f32_16x16x32_bf16 v[68:71], a[40:43], a[112:115], v[68:71]\n"
"v_accvgpr_read_b32 v16, a220\n"
"v_accvgpr_read_b32 v17, a221\n"
"v_mfma_f32_16x16x32_bf16 v[72:75], a[40:43], a[120:123], v[72:75]\n"
"v_accvgpr_read_b32 v18, a222\n"
"v_accvgpr_read_b32 v19, a223\n"
"v_mfma_f32_16x16x32_bf16 v[72:75], a[44:47], a[124:127], v[72:75]\n"
"v_lshlrev_b32 v12, 16, v16\n"
"v_and_b32 v13, s63, v16\n"
"v_mfma_f32_16x16x32_bf16 v[44:47], a[36:39], a[124:127], v[44:47]\n"
"v_lshlrev_b32 v14, 16, v17\n"
"v_and_b32 v15, s63, v17\n"
"v_mfma_f32_16x16x32_bf16 v[44:47], a[32:35], a[120:123], v[44:47]\n"
"v_mul_f32 v12, v12, v212\n"
"v_mul_f32 v13, v13, v213\n"
"v_mfma_f32_16x16x32_bf16 v[48:51], a[32:35], a[128:131], v[48:51]\n"
"v_mul_f32 v14, v14, v214\n"
"v_mul_f32 v15, v15, v215\n"
"v_mfma_f32_16x16x32_bf16 v[48:51], a[36:39], a[132:135], v[48:51]\n"
"global_load_dwordx4 a[88:91], v0, s[34:35] offset:2048 sc1\n"
"v_cvt_pk_bf16_f32 v28, v12, v13\n"
"v_mfma_f32_16x16x32_bf16 v[76:79], a[44:47], a[132:135], v[76:79]\n"
"v_cvt_pk_bf16_f32 v29, v14, v15\n"
"v_mfma_f32_16x16x32_bf16 v[76:79], a[40:43], a[128:131], v[76:79]\n"
"ds_write_b64 v2, v[28:29] offset:23552\n"
"v_mfma_f32_16x16x32_bf16 v[80:83], a[40:43], a[136:139], v[80:83]\n"
"v_lshlrev_b32 v12, 16, v18\n"
"v_and_b32 v13, s63, v18\n"
"v_mfma_f32_16x16x32_bf16 v[80:83], a[44:47], a[140:143], v[80:83]\n"
"v_lshlrev_b32 v14, 16, v19\n"
"v_and_b32 v15, s63, v19\n"
"v_mfma_f32_16x16x32_bf16 v[52:55], a[36:39], a[140:143], v[52:55]\n"
"v_mul_f32 v12, v12, v240\n"
"v_mul_f32 v13, v13, v241\n"
"v_mfma_f32_16x16x32_bf16 v[52:55], a[32:35], a[136:139], v[52:55]\n"
"s_waitcnt lgkmcnt(3)\n"
"v_mul_f32 v14, v14, v242\n"
"v_mul_f32 v15, v15, v243\n"
"v_mfma_f32_16x16x32_bf16 v[56:59], a[32:35], a[144:147], v[56:59]\n"
"v_cvt_pk_bf16_f32 v30, v12, v13\n"
"v_cvt_pk_bf16_f32 v31, v14, v15\n"
"v_mfma_f32_16x16x32_bf16 v[56:59], a[36:39], a[148:151], v[56:59]\n"
"global_load_dwordx4 a[92:95], v0, s[34:35] offset:3072 sc1\n"
"v_mfma_f32_16x16x32_bf16 v[84:87], a[44:47], a[148:151], v[84:87]\n"
"ds_write_b64 v2, v[30:31] offset:23560\n"
"v_mfma_f32_16x16x32_bf16 v[84:87], a[40:43], a[144:147], v[84:87]\n"
"v_accvgpr_read_b32 v16, a224\n"
"v_accvgpr_read_b32 v17, a225\n"
"v_mfma_f32_16x16x32_bf16 a[4:7], a[40:43], a[152:155], a[4:7]\n"
"s_add_u32 s34, s34, 8192\n"
"v_accvgpr_read_b32 v18, a226\n"
"v_accvgpr_read_b32 v19, a227\n"
"v_mfma_f32_16x16x32_bf16 a[4:7], a[44:47], a[156:159], a[4:7]\n"
"v_lshlrev_b32 v12, 16, v16\n"
"v_and_b32 v13, s63, v16\n"
"v_mfma_f32_16x16x32_bf16 a[0:3], a[36:39], a[156:159], a[0:3]\n"
"v_lshlrev_b32 v14, 16, v17\n"
"v_and_b32 v15, s63, v17\n"
"v_mfma_f32_16x16x32_bf16 a[0:3], a[32:35], a[152:155], a[0:3]\n"
"s_waitcnt vmcnt(10)\n"
"v_mul_f32 v12, v12, v160\n"
"v_mul_f32 v13, v13, v161\n"
"v_mfma_f32_16x16x32_bf16 v[88:91], a[48:51], a[96:99], v[88:91]\n"
"v_mul_f32 v14, v14, v162\n"
"v_mul_f32 v15, v15, v163\n"
"v_mfma_f32_16x16x32_bf16 v[88:91], a[52:55], a[100:103], v[88:91]\n"
"global_load_dwordx4 a[32:35], v0, s[34:35] offset:-4096 sc1\n"
"v_cvt_pk_bf16_f32 v24, v12, v13\n"
"v_mfma_f32_16x16x32_bf16 v[116:119], a[60:63], a[100:103], v[116:119]\n"
"ds_read_b128 a[100:103], v5 offset:1024\n"
"v_cvt_pk_bf16_f32 v25, v14, v15\n"
"v_mfma_f32_16x16x32_bf16 v[116:119], a[56:59], a[96:99], v[116:119]\n"
"ds_read_b128 a[96:99], v5 offset:0\n"
"v_mfma_f32_16x16x32_bf16 v[120:123], a[56:59], a[104:107], v[120:123]\n"
"ds_write_b64 v2, v[24:25] offset:24576\n"
"v_mfma_f32_16x16x32_bf16 v[120:123], a[60:63], a[108:111], v[120:123]\n"
"v_lshlrev_b32 v12, 16, v18\n"
"v_and_b32 v13, s63, v18\n"
"v_mfma_f32_16x16x32_bf16 v[92:95], a[52:55], a[108:111], v[92:95]\n"
"ds_read_b128 a[108:111], v5 offset:3072\n"
"v_lshlrev_b32 v14, 16, v19\n"
"v_mfma_f32_16x16x32_bf16 v[92:95], a[48:51], a[104:107], v[92:95]\n"
"ds_read_b128 a[104:107], v5 offset:2048\n"
"v_and_b32 v15, s63, v19\n"
"v_mfma_f32_16x16x32_bf16 v[96:99], a[48:51], a[112:115], v[96:99]\n"
"v_mul_f32 v12, v12, v188\n"
"v_mul_f32 v13, v13, v189\n"
"v_mfma_f32_16x16x32_bf16 v[96:99], a[52:55], a[116:119], v[96:99]\n"
"global_load_dwordx4 a[36:39], v0, s[34:35] offset:-3072 sc1\n"
"v_mul_f32 v14, v14, v190\n"
"v_mfma_f32_16x16x32_bf16 v[124:127], a[60:63], a[116:119], v[124:127]\n"
"ds_read_b128 a[116:119], v5 offset:5120\n"
"v_mul_f32 v15, v15, v191\n"
"v_mfma_f32_16x16x32_bf16 v[124:127], a[56:59], a[112:115], v[124:127]\n"
"ds_read_b128 a[112:115], v5 offset:4096\n"
"v_cvt_pk_bf16_f32 v26, v12, v13\n"
"v_mfma_f32_16x16x32_bf16 v[128:131], a[56:59], a[120:123], v[128:131]\n"
"v_cvt_pk_bf16_f32 v27, v14, v15\n"
"v_mfma_f32_16x16x32_bf16 v[128:131], a[60:63], a[124:127], v[128:131]\n"
"ds_write_b64 v2, v[26:27] offset:24584\n"
"v_mfma_f32_16x16x32_bf16 v[100:103], a[52:55], a[124:127], v[100:103]\n"
"ds_read_b128 a[124:127], v5 offset:7168\n"
"v_accvgpr_read_b32 v16, a228\n"
"v_mfma_f32_16x16x32_bf16 v[100:103], a[48:51], a[120:123], v[100:103]\n"
"ds_read_b128 a[120:123], v5 offset:6144\n"
"v_accvgpr_read_b32 v17, a229\n"
"v_mfma_f32_16x16x32_bf16 v[104:107], a[48:51], a[128:131], v[104:107]\n"
"v_accvgpr_read_b32 v18, a230\n"
"v_accvgpr_read_b32 v19, a231\n"
"v_mfma_f32_16x16x32_bf16 v[104:107], a[52:55], a[132:135], v[104:107]\n"
"global_load_dwordx4 a[40:43], v0, s[34:35] offset:-2048 sc1\n"
"v_lshlrev_b32 v12, 16, v16\n"
"v_mfma_f32_16x16x32_bf16 v[132:135], a[60:63], a[132:135], v[132:135]\n"
"ds_read_b128 a[132:135], v5 offset:9216\n"
"v_and_b32 v13, s63, v16\n"
"v_mfma_f32_16x16x32_bf16 v[132:135], a[56:59], a[128:131], v[132:135]\n"
"ds_read_b128 a[128:131], v5 offset:8192\n"
"v_lshlrev_b32 v14, 16, v17\n"
"v_mfma_f32_16x16x32_bf16 v[136:139], a[56:59], a[136:139], v[136:139]\n"
"v_and_b32 v15, s63, v17\n"
"v_mul_f32 v12, v12, v216\n"
"v_mfma_f32_16x16x32_bf16 v[136:139], a[60:63], a[140:143], v[136:139]\n"
"v_mul_f32 v13, v13, v217\n"
"v_mul_f32 v14, v14, v218\n"
"v_mfma_f32_16x16x32_bf16 v[108:111], a[52:55], a[140:143], v[108:111]\n"
"ds_read_b128 a[140:143], v5 offset:11264\n"
"v_mul_f32 v15, v15, v219\n"
"v_mfma_f32_16x16x32_bf16 v[108:111], a[48:51], a[136:139], v[108:111]\n"
"ds_read_b128 a[136:139], v5 offset:10240\n"
"v_cvt_pk_bf16_f32 v28, v12, v13\n"
"v_mfma_f32_16x16x32_bf16 v[112:115], a[48:51], a[144:147], v[112:115]\n"
"v_cvt_pk_bf16_f32 v29, v14, v15\n"
"v_lshlrev_b32 v12, 16, v18\n"
"v_mfma_f32_16x16x32_bf16 v[112:115], a[52:55], a[148:151], v[112:115]\n"
"global_load_dwordx4 a[44:47], v0, s[34:35] offset:-1024 sc1\n"
"v_and_b32 v13, s63, v18\n"
"v_mfma_f32_16x16x32_bf16 v[140:143], a[60:63], a[148:151], v[140:143]\n"
"ds_read_b128 a[148:151], v5 offset:13312\n"
"v_lshlrev_b32 v14, 16, v19\n"
"v_mfma_f32_16x16x32_bf16 v[140:143], a[56:59], a[144:147], v[140:143]\n"
"ds_read_b128 a[144:147], v5 offset:12288\n"
"v_and_b32 v15, s63, v19\n"
"v_mfma_f32_16x16x32_bf16 a[12:15], a[56:59], a[152:155], a[12:15]\n"
"v_mul_f32 v12, v12, v244\n"
"v_mul_f32 v13, v13, v245\n"
"v_mfma_f32_16x16x32_bf16 a[12:15], a[60:63], a[156:159], a[12:15]\n"
"v_mul_f32 v14, v14, v246\n"
"v_mul_f32 v15, v15, v247\n"
"v_mfma_f32_16x16x32_bf16 a[8:11], a[52:55], a[156:159], a[8:11]\n"
"ds_read_b128 a[156:159], v5 offset:15360\n"
"v_cvt_pk_bf16_f32 v30, v12, v13\n"
"v_mfma_f32_16x16x32_bf16 a[8:11], a[48:51], a[152:155], a[8:11]\n"
"s_waitcnt vmcnt(9) lgkmcnt(12)\n"
"ds_read_b128 a[152:155], v5 offset:14336\n"
"v_cvt_pk_bf16_f32 v31, v14, v15\n"
"v_mfma_f32_16x16x32_bf16 v[32:35], a[64:67], a[96:99], v[32:35]\n"
"global_store_dwordx4 v0, v[28:31], s[54:55] offset:-3072 sc1\n"
"ds_write_b128 v2, v[28:31] offset:25600\n"
"v_mfma_f32_16x16x32_bf16 v[32:35], a[68:71], a[100:103], v[32:35]\n"
"global_load_dwordx4 a[48:51], v0, s[34:35] offset:0 sc1\n"
"v_accvgpr_read_b32 v16, a232\n"
"v_mfma_f32_16x16x32_bf16 v[60:63], a[76:79], a[100:103], v[60:63]\n"
"v_accvgpr_read_b32 v17, a233\n"
"v_accvgpr_read_b32 v18, a234\n"
"v_mfma_f32_16x16x32_bf16 v[60:63], a[72:75], a[96:99], v[60:63]\n"
"v_accvgpr_read_b32 v19, a235\n"
"v_lshlrev_b32 v12, 16, v16\n"
"v_mfma_f32_16x16x32_bf16 v[64:67], a[72:75], a[104:107], v[64:67]\n"
"v_and_b32 v13, s63, v16\n"
"v_lshlrev_b32 v14, 16, v17\n"
"v_mfma_f32_16x16x32_bf16 v[64:67], a[76:79], a[108:111], v[64:67]\n"
"v_and_b32 v15, s63, v17\n"
"v_mul_f32 v12, v12, v164\n"
"v_mfma_f32_16x16x32_bf16 v[36:39], a[68:71], a[108:111], v[36:39]\n"
"v_mul_f32 v13, v13, v165\n"
"v_mul_f32 v14, v14, v166\n"
"v_mfma_f32_16x16x32_bf16 v[36:39], a[64:67], a[104:107], v[36:39]\n"
"s_waitcnt lgkmcnt(9)\n"
"v_mul_f32 v15, v15, v167\n"
"v_cvt_pk_bf16_f32 v24, v12, v13\n"
"v_mfma_f32_16x16x32_bf16 v[40:43], a[64:67], a[112:115], v[40:43]\n"
"v_cvt_pk_bf16_f32 v25, v14, v15\n"
"v_mfma_f32_16x16x32_bf16 v[40:43], a[68:71], a[116:119], v[40:43]\n"
"global_load_dwordx4 a[52:55], v0, s[34:35] offset:1024 sc1\n"
"v_mfma_f32_16x16x32_bf16 v[68:71], a[76:79], a[116:119], v[68:71]\n"
"ds_write_b64 v2, v[24:25] offset:26624\n"
"v_mfma_f32_16x16x32_bf16 v[68:71], a[72:75], a[112:115], v[68:71]\n"
"v_lshlrev_b32 v12, 16, v18\n"
"v_and_b32 v13, s63, v18\n"
"v_mfma_f32_16x16x32_bf16 v[72:75], a[72:75], a[120:123], v[72:75]\n"
"v_lshlrev_b32 v14, 16, v19\n"
"v_and_b32 v15, s63, v19\n"
"v_mfma_f32_16x16x32_bf16 v[72:75], a[76:79], a[124:127], v[72:75]\n"
"v_mul_f32 v12, v12, v192\n"
"v_mul_f32 v13, v13, v193\n"
"v_mfma_f32_16x16x32_bf16 v[44:47], a[68:71], a[124:127], v[44:47]\n"
"v_mul_f32 v14, v14, v194\n"
"v_mul_f32 v15, v15, v195\n"
"v_mfma_f32_16x16x32_bf16 v[44:47], a[64:67], a[120:123], v[44:47]\n"
"s_waitcnt lgkmcnt(6)\n"
"v_cvt_pk_bf16_f32 v26, v12, v13\n"
"v_cvt_pk_bf16_f32 v27, v14, v15\n"
"v_mfma_f32_16x16x32_bf16 v[48:51], a[64:67], a[128:131], v[48:51]\n"
"ds_write_b64 v2, v[26:27] offset:26632\n"
"v_mfma_f32_16x16x32_bf16 v[48:51], a[68:71], a[132:135], v[48:51]\n"
"global_load_dwordx4 a[56:59], v0, s[34:35] offset:2048 sc1\n"
"v_accvgpr_read_b32 v16, a236\n"
"v_mfma_f32_16x16x32_bf16 v[76:79], a[76:79], a[132:135], v[76:79]\n"
"v_accvgpr_read_b32 v17, a237\n"
"v_accvgpr_read_b32 v18, a238\n"
"v_mfma_f32_16x16x32_bf16 v[76:79], a[72:75], a[128:131], v[76:79]\n"
"v_accvgpr_read_b32 v19, a239\n"
"v_lshlrev_b32 v12, 16, v16\n"
"v_mfma_f32_16x16x32_bf16 v[80:83], a[72:75], a[136:139], v[80:83]\n"
"v_and_b32 v13, s63, v16\n"
"v_mfma_f32_16x16x32_bf16 v[80:83], a[76:79], a[140:143], v[80:83]\n"
"v_lshlrev_b32 v14, 16, v17\n"
"v_mfma_f32_16x16x32_bf16 v[52:55], a[68:71], a[140:143], v[52:55]\n"
"v_and_b32 v15, s63, v17\n"
"v_mul_f32 v12, v12, v220\n"
"v_mfma_f32_16x16x32_bf16 v[52:55], a[64:67], a[136:139], v[52:55]\n"
"s_waitcnt lgkmcnt(3)\n"
"v_mul_f32 v13, v13, v221\n"
"v_mfma_f32_16x16x32_bf16 v[56:59], a[64:67], a[144:147], v[56:59]\n"
"v_mul_f32 v14, v14, v222\n"
"v_mul_f32 v15, v15, v223\n"
"v_mfma_f32_16x16x32_bf16 v[56:59], a[68:71], a[148:151], v[56:59]\n"
"global_load_dwordx4 a[60:63], v0, s[34:35] offset:3072 sc1\n"
"v_cvt_pk_bf16_f32 v28, v12, v13\n"
"v_mfma_f32_16x16x32_bf16 v[84:87], a[76:79], a[148:151], v[84:87]\n"
"v_cvt_pk_bf16_f32 v29, v14, v15\n"
"v_mfma_f32_16x16x32_bf16 v[84:87], a[72:75], a[144:147], v[84:87]\n"
"v_lshlrev_b32 v12, 16, v18\n"
"v_and_b32 v13, s63, v18\n"
"v_mfma_f32_16x16x32_bf16 a[4:7], a[72:75], a[152:155], a[4:7]\n"
"s_add_u32 s34, s34, 8192\n"
"v_lshlrev_b32 v14, 16, v19\n"
"v_mfma_f32_16x16x32_bf16 a[4:7], a[76:79], a[156:159], a[4:7]\n"
"v_and_b32 v15, s63, v19\n"
"v_mfma_f32_16x16x32_bf16 a[0:3], a[68:71], a[156:159], a[0:3]\n"
"v_mul_f32 v12, v12, v248\n"
"v_mul_f32 v13, v13, v249\n"
"v_mfma_f32_16x16x32_bf16 a[0:3], a[64:67], a[152:155], a[0:3]\n"
"s_waitcnt vmcnt(9)\n"
"v_mul_f32 v14, v14, v250\n"
"v_mfma_f32_16x16x32_bf16 v[88:91], a[80:83], a[96:99], v[88:91]\n"
"v_mul_f32 v15, v15, v251\n"
"v_cvt_pk_bf16_f32 v30, v12, v13\n"
"v_mfma_f32_16x16x32_bf16 v[88:91], a[84:87], a[100:103], v[88:91]\n"
"global_load_dwordx4 a[64:67], v0, s[34:35] offset:-4096 sc1\n"
"v_cvt_pk_bf16_f32 v31, v14, v15\n"
"v_mfma_f32_16x16x32_bf16 v[116:119], a[92:95], a[100:103], v[116:119]\n"
"global_store_dwordx4 v0, v[28:31], s[54:55] offset:-1024 sc1\n"
"ds_write_b128 v2, v[28:31] offset:27648\n"
"v_mfma_f32_16x16x32_bf16 v[116:119], a[88:91], a[96:99], v[116:119]\n"
"v_accvgpr_read_b32 v16, a240\n"
"v_mfma_f32_16x16x32_bf16 v[120:123], a[88:91], a[104:107], v[120:123]\n"
"v_accvgpr_read_b32 v17, a241\n"
"v_mfma_f32_16x16x32_bf16 v[120:123], a[92:95], a[108:111], v[120:123]\n"
"v_accvgpr_read_b32 v18, a242\n"
"v_mfma_f32_16x16x32_bf16 v[92:95], a[84:87], a[108:111], v[92:95]\n"
"v_accvgpr_read_b32 v19, a243\n"
"v_lshlrev_b32 v12, 16, v16\n"
"v_mfma_f32_16x16x32_bf16 v[92:95], a[80:83], a[104:107], v[92:95]\n"
"v_and_b32 v13, s63, v16\n"
"v_mfma_f32_16x16x32_bf16 v[96:99], a[80:83], a[112:115], v[96:99]\n"
"v_lshlrev_b32 v14, 16, v17\n"
"v_and_b32 v15, s63, v17\n"
"v_mfma_f32_16x16x32_bf16 v[96:99], a[84:87], a[116:119], v[96:99]\n"
"global_load_dwordx4 a[68:71], v0, s[34:35] offset:-3072 sc1\n"
"v_mul_f32 v12, v12, v168\n"
"v_mfma_f32_16x16x32_bf16 v[124:127], a[92:95], a[116:119], v[124:127]\n"
"v_mul_f32 v13, v13, v169\n"
"v_mfma_f32_16x16x32_bf16 v[124:127], a[88:91], a[112:115], v[124:127]\n"
"v_mul_f32 v14, v14, v170\n"
"v_mul_f32 v15, v15, v171\n"
"v_mfma_f32_16x16x32_bf16 v[128:131], a[88:91], a[120:123], v[128:131]\n"
"v_cvt_pk_bf16_f32 v24, v12, v13\n"
"v_mfma_f32_16x16x32_bf16 v[128:131], a[92:95], a[124:127], v[128:131]\n"
"v_cvt_pk_bf16_f32 v25, v14, v15\n"
"v_lshlrev_b32 v12, 16, v18\n"
"v_mfma_f32_16x16x32_bf16 v[100:103], a[84:87], a[124:127], v[100:103]\n"
"v_and_b32 v13, s63, v18\n"
"v_lshlrev_b32 v14, 16, v19\n"
"v_mfma_f32_16x16x32_bf16 v[100:103], a[80:83], a[120:123], v[100:103]\n"
"v_mov_b32 v8, s65\n"
"v_and_b32 v15, s63, v19\n"
"v_mfma_f32_16x16x32_bf16 v[104:107], a[80:83], a[128:131], v[104:107]\n"
"v_mov_b32 v9, s60\n"
"v_mul_f32 v12, v12, v196\n"
"v_mfma_f32_16x16x32_bf16 v[104:107], a[84:87], a[132:135], v[104:107]\n"
"global_load_dwordx4 a[72:75], v0, s[34:35] offset:-2048 sc1\n"
"v_mul_f32 v13, v13, v197\n"
"v_mfma_f32_16x16x32_bf16 v[132:135], a[92:95], a[132:135], v[132:135]\n"
"buffer_store_dwordx2 v[8:9], off, s[56:59], %13 sc1\n"
"v_mul_f32 v14, v14, v198\n"
"v_mfma_f32_16x16x32_bf16 v[132:135], a[88:91], a[128:131], v[132:135]\n"
"v_mul_f32 v15, v15, v199\n"
"v_cvt_pk_bf16_f32 v26, v12, v13\n"
"v_mfma_f32_16x16x32_bf16 v[136:139], a[88:91], a[136:139], v[136:139]\n"
"v_cvt_pk_bf16_f32 v27, v14, v15\n"
"v_mfma_f32_16x16x32_bf16 v[136:139], a[92:95], a[140:143], v[136:139]\n"
"global_store_dwordx4 v0, v[24:27], s[54:55] offset:0 sc1\n"
"ds_write_b128 v2, v[24:27] offset:28672\n"
"v_mfma_f32_16x16x32_bf16 v[108:111], a[84:87], a[140:143], v[108:111]\n"
"v_accvgpr_read_b32 v16, a248\n"
"v_mfma_f32_16x16x32_bf16 v[108:111], a[80:83], a[136:139], v[108:111]\n"
"v_accvgpr_read_b32 v17, a249\n"
"v_mfma_f32_16x16x32_bf16 v[112:115], a[80:83], a[144:147], v[112:115]\n"
"v_accvgpr_read_b32 v18, a250\n"
"v_accvgpr_read_b32 v19, a251\n"
"v_mfma_f32_16x16x32_bf16 v[112:115], a[84:87], a[148:151], v[112:115]\n"
"global_load_dwordx4 a[76:79], v0, s[34:35] offset:-1024 sc1\n"
"v_lshlrev_b32 v12, 16, v16\n"
"v_mfma_f32_16x16x32_bf16 v[140:143], a[92:95], a[148:151], v[140:143]\n"
"v_and_b32 v13, s63, v16\n"
"v_mfma_f32_16x16x32_bf16 v[140:143], a[88:91], a[144:147], v[140:143]\n"
"v_lshlrev_b32 v14, 16, v17\n"
"v_and_b32 v15, s63, v17\n"
"v_mfma_f32_16x16x32_bf16 a[12:15], a[88:91], a[152:155], a[12:15]\n"
"v_accvgpr_read_b32 v10, a16\n"
"v_mfma_f32_16x16x32_bf16 a[12:15], a[92:95], a[156:159], a[12:15]\n"
"v_accvgpr_read_b32 v11, a17\n"
"v_mfma_f32_16x16x32_bf16 a[8:11], a[84:87], a[156:159], a[8:11]\n"
"v_mul_f32 v12, v12, v10\n"
"v_mul_f32 v13, v13, v11\n"
"v_mfma_f32_16x16x32_bf16 a[8:11], a[80:83], a[152:155], a[8:11]\n"
"s_waitcnt vmcnt(12)\n"
"v_accvgpr_read_b32 v10, a18\n"
"v_mfma_f32_16x16x32_bf16 v[144:147], a[32:35], a[96:99], v[144:147]\n"
"v_accvgpr_read_b32 v11, a19\n"
"v_mul_f32 v14, v14, v10\n"
"v_mfma_f32_16x16x32_bf16 v[144:147], a[36:39], a[100:103], v[144:147]\n"
"global_load_dwordx4 a[80:83], v0, s[34:35] offset:0 sc1\n"
"v_mul_f32 v15, v15, v11\n"
"v_mfma_f32_16x16x32_bf16 v[172:175], a[44:47], a[100:103], v[172:175]\n"
"v_cvt_pk_bf16_f32 v28, v12, v13\n"
"v_mfma_f32_16x16x32_bf16 v[172:175], a[40:43], a[96:99], v[172:175]\n"
"v_cvt_pk_bf16_f32 v29, v14, v15\n"
"v_lshlrev_b32 v12, 16, v18\n"
"v_mfma_f32_16x16x32_bf16 v[176:179], a[40:43], a[104:107], v[176:179]\n"
"v_and_b32 v13, s63, v18\n"
"v_mfma_f32_16x16x32_bf16 v[176:179], a[44:47], a[108:111], v[176:179]\n"
"v_lshlrev_b32 v14, 16, v19\n"
"v_mfma_f32_16x16x32_bf16 v[148:151], a[36:39], a[108:111], v[148:151]\n"
"v_and_b32 v15, s63, v19\n"
"v_accvgpr_read_b32 v10, a20\n"
"v_mfma_f32_16x16x32_bf16 v[148:151], a[32:35], a[104:107], v[148:151]\n"
"v_accvgpr_read_b32 v11, a21\n"
"v_mfma_f32_16x16x32_bf16 v[152:155], a[32:35], a[112:115], v[152:155]\n"
"v_mul_f32 v12, v12, v10\n"
"v_mul_f32 v13, v13, v11\n"
"v_mfma_f32_16x16x32_bf16 v[152:155], a[36:39], a[116:119], v[152:155]\n"
"global_load_dwordx4 a[84:87], v0, s[34:35] offset:1024 sc1\n"
"v_accvgpr_read_b32 v10, a22\n"
"v_mfma_f32_16x16x32_bf16 v[180:183], a[44:47], a[116:119], v[180:183]\n"
"v_accvgpr_read_b32 v11, a23\n"
"v_mfma_f32_16x16x32_bf16 v[180:183], a[40:43], a[112:115], v[180:183]\n"
"v_mul_f32 v14, v14, v10\n"
"v_mul_f32 v15, v15, v11\n"
"v_mfma_f32_16x16x32_bf16 v[184:187], a[40:43], a[120:123], v[184:187]\n"
"v_cvt_pk_bf16_f32 v30, v12, v13\n"
"v_mfma_f32_16x16x32_bf16 v[184:187], a[44:47], a[124:127], v[184:187]\n"
"v_cvt_pk_bf16_f32 v31, v14, v15\n"
"v_mfma_f32_16x16x32_bf16 v[156:159], a[36:39], a[124:127], v[156:159]\n"
"global_store_dwordx4 v0, v[28:31], s[54:55] offset:2048 sc1\n"
"ds_write_b128 v2, v[28:31] offset:30720\n"
"v_mfma_f32_16x16x32_bf16 v[156:159], a[32:35], a[120:123], v[156:159]\n"
"v_accvgpr_read_b32 v16, a244\n"
"v_mfma_f32_16x16x32_bf16 v[160:163], a[32:35], a[128:131], v[160:163]\n"
"v_accvgpr_read_b32 v17, a245\n"
"v_accvgpr_read_b32 v18, a246\n"
"v_mfma_f32_16x16x32_bf16 v[160:163], a[36:39], a[132:135], v[160:163]\n"
"global_load_dwordx4 a[88:91], v0, s[34:35] offset:2048 sc1\n"
"v_accvgpr_read_b32 v19, a247\n"
"v_mfma_f32_16x16x32_bf16 v[188:191], a[44:47], a[132:135], v[188:191]\n"
"v_lshlrev_b32 v12, 16, v16\n"
"v_mfma_f32_16x16x32_bf16 v[188:191], a[40:43], a[128:131], v[188:191]\n"
"v_and_b32 v13, s63, v16\n"
"v_lshlrev_b32 v14, 16, v17\n"
"v_mfma_f32_16x16x32_bf16 v[192:195], a[40:43], a[136:139], v[192:195]\n"
"v_and_b32 v15, s63, v17\n"
"v_mfma_f32_16x16x32_bf16 v[192:195], a[44:47], a[140:143], v[192:195]\n"
"v_mul_f32 v12, v12, v224\n"
"v_mul_f32 v13, v13, v225\n"
"v_mfma_f32_16x16x32_bf16 v[164:167], a[36:39], a[140:143], v[164:167]\n"
"v_mul_f32 v14, v14, v226\n"
"v_mul_f32 v15, v15, v227\n"
"v_mfma_f32_16x16x32_bf16 v[164:167], a[32:35], a[136:139], v[164:167]\n"
"v_cvt_pk_bf16_f32 v24, v12, v13\n"
"v_cvt_pk_bf16_f32 v25, v14, v15\n"
"v_mfma_f32_16x16x32_bf16 v[168:171], a[32:35], a[144:147], v[168:171]\n"
"ds_write_b64 v2, v[24:25] offset:29696\n"
"v_mfma_f32_16x16x32_bf16 v[168:171], a[36:39], a[148:151], v[168:171]\n"
"global_load_dwordx4 a[92:95], v0, s[34:35] offset:3072 sc1\n"
"v_mfma_f32_16x16x32_bf16 v[196:199], a[44:47], a[148:151], v[196:199]\n"
"v_lshlrev_b32 v12, 16, v18\n"
"v_and_b32 v13, s63, v18\n"
"v_mfma_f32_16x16x32_bf16 v[196:199], a[40:43], a[144:147], v[196:199]\n"
"v_lshlrev_b32 v14, 16, v19\n"
"v_and_b32 v15, s63, v19\n"
"v_mfma_f32_16x16x32_bf16 a[20:23], a[40:43], a[152:155], a[20:23]\n"
"s_add_u32 s34, s34, 8192\n"
"v_mul_f32 v12, v12, v252\n"
"v_mul_f32 v13, v13, v253\n"
"v_mfma_f32_16x16x32_bf16 a[20:23], a[44:47], a[156:159], a[20:23]\n"
"v_mul_f32 v14, v14, v254\n"
"v_mul_f32 v15, v15, v255\n"
"v_mfma_f32_16x16x32_bf16 a[16:19], a[36:39], a[156:159], a[16:19]\n"
"v_cvt_pk_bf16_f32 v26, v12, v13\n"
"v_cvt_pk_bf16_f32 v27, v14, v15\n"
"v_mfma_f32_16x16x32_bf16 a[16:19], a[32:35], a[152:155], a[16:19]\n"
"L_st5690:\n"
"s_cbranch_execz L_st7932\n"
"s_waitcnt vmcnt(12)\n"
"ds_write_b64 v2, v[26:27] offset:29704\n"
"v_mfma_f32_16x16x32_bf16 v[200:203], a[48:51], a[96:99], v[200:203]\n"
"v_accvgpr_read_b32 v16, a252\n"
"v_mfma_f32_16x16x32_bf16 v[200:203], a[52:55], a[100:103], v[200:203]\n"
"global_load_dwordx4 a[32:35], v0, s[34:35] offset:-4096 sc1\n"
"v_accvgpr_read_b32 v17, a253\n"
"v_mfma_f32_16x16x32_bf16 v[228:231], a[60:63], a[100:103], v[228:231]\n"
"ds_read_b128 a[100:103], v4 offset:1024\n"
"v_accvgpr_read_b32 v18, a254\n"
"v_mfma_f32_16x16x32_bf16 v[228:231], a[56:59], a[96:99], v[228:231]\n"
"ds_read_b128 a[96:99], v4 offset:0\n"
"v_accvgpr_read_b32 v19, a255\n"
"v_mfma_f32_16x16x32_bf16 v[232:235], a[56:59], a[104:107], v[232:235]\n"
"v_lshlrev_b32 v12, 16, v16\n"
"v_and_b32 v13, s63, v16\n"
"v_mfma_f32_16x16x32_bf16 v[232:235], a[60:63], a[108:111], v[232:235]\n"
"v_lshlrev_b32 v14, 16, v17\n"
"v_and_b32 v15, s63, v17\n"
"v_mfma_f32_16x16x32_bf16 v[204:207], a[52:55], a[108:111], v[204:207]\n"
"ds_read_b128 a[108:111], v4 offset:3072\n"
"v_accvgpr_read_b32 v10, a24\n"
"v_mfma_f32_16x16x32_bf16 v[204:207], a[48:51], a[104:107], v[204:207]\n"
"ds_read_b128 a[104:107], v4 offset:2048\n"
"v_accvgpr_read_b32 v11, a25\n"
"v_mfma_f32_16x16x32_bf16 v[208:211], a[48:51], a[112:115], v[208:211]\n"
"v_mul_f32 v12, v12, v10\n"
"v_mul_f32 v13, v13, v11\n"
"v_mfma_f32_16x16x32_bf16 v[208:211], a[52:55], a[116:119], v[208:211]\n"
"global_load_dwordx4 a[36:39], v0, s[34:35] offset:-3072 sc1\n"
"v_accvgpr_read_b32 v10, a26\n"
"v_mfma_f32_16x16x32_bf16 v[236:239], a[60:63], a[116:119], v[236:239]\n"
"ds_read_b128 a[116:119], v4 offset:5120\n"
"v_accvgpr_read_b32 v11, a27\n"
"v_mfma_f32_16x16x32_bf16 v[236:239], a[56:59], a[112:115], v[236:239]\n"
"ds_read_b128 a[112:115], v4 offset:4096\n"
"v_mul_f32 v14, v14, v10\n"
"v_mfma_f32_16x16x32_bf16 v[240:243], a[56:59], a[120:123], v[240:243]\n"
"v_mul_f32 v15, v15, v11\n"
"v_cvt_pk_bf16_f32 v28, v12, v13\n"
"v_mfma_f32_16x16x32_bf16 v[240:243], a[60:63], a[124:127], v[240:243]\n"
"v_cvt_pk_bf16_f32 v29, v14, v15\n"
"v_lshlrev_b32 v12, 16, v18\n"
"v_mfma_f32_16x16x32_bf16 v[212:215], a[52:55], a[124:127], v[212:215]\n"
"ds_read_b128 a[124:127], v4 offset:7168\n"
"v_and_b32 v13, s63, v18\n"
"v_mfma_f32_16x16x32_bf16 v[212:215], a[48:51], a[120:123], v[212:215]\n"
"ds_read_b128 a[120:123], v4 offset:6144\n"
"v_lshlrev_b32 v14, 16, v19\n"
"v_mfma_f32_16x16x32_bf16 v[216:219], a[48:51], a[128:131], v[216:219]\n"
"v_and_b32 v15, s63, v19\n"
"v_accvgpr_read_b32 v10, a28\n"
"v_mfma_f32_16x16x32_bf16 v[216:219], a[52:55], a[132:135], v[216:219]\n"
"global_load_dwordx4 a[40:43], v0, s[34:35] offset:-2048 sc1\n"
"v_accvgpr_read_b32 v11, a29\n"
"v_mfma_f32_16x16x32_bf16 v[244:247], a[60:63], a[132:135], v[244:247]\n"
"ds_read_b128 a[132:135], v4 offset:9216\n"
"v_mul_f32 v12, v12, v10\n"
"v_mfma_f32_16x16x32_bf16 v[244:247], a[56:59], a[128:131], v[244:247]\n"
"ds_read_b128 a[128:131], v4 offset:8192\n"
"v_mul_f32 v13, v13, v11\n"
"v_mfma_f32_16x16x32_bf16 v[248:251], a[56:59], a[136:139], v[248:251]\n"
"v_accvgpr_read_b32 v10, a30\n"
"v_accvgpr_read_b32 v11, a31\n"
"v_mfma_f32_16x16x32_bf16 v[248:251], a[60:63], a[140:143], v[248:251]\n"
"v_mul_f32 v14, v14, v10\n"
"v_mul_f32 v15, v15, v11\n"
"v_mfma_f32_16x16x32_bf16 v[220:223], a[52:55], a[140:143], v[220:223]\n"
"ds_read_b128 a[140:143], v4 offset:11264\n"
"v_cvt_pk_bf16_f32 v30, v12, v13\n"
"v_mfma_f32_16x16x32_bf16 v[220:223], a[48:51], a[136:139], v[220:223]\n"
"ds_read_b128 a[136:139], v4 offset:10240\n"
"v_cvt_pk_bf16_f32 v31, v14, v15\n"
"v_mfma_f32_16x16x32_bf16 v[224:227], a[48:51], a[144:147], v[224:227]\n"
"global_store_dwordx4 v0, v[28:31], s[54:55] offset:3072 sc1\n"
"ds_write_b128 v2, v[28:31] offset:31744\n"
"v_mfma_f32_16x16x32_bf16 v[224:227], a[52:55], a[148:151], v[224:227]\n"
"global_load_dwordx4 a[44:47], v0, s[34:35] offset:-1024 sc1\n"
"v_mfma_f32_16x16x32_bf16 v[252:255], a[60:63], a[148:151], v[252:255]\n"
"ds_read_b128 a[148:151], v4 offset:13312\n"
"v_mfma_f32_16x16x32_bf16 v[252:255], a[56:59], a[144:147], v[252:255]\n"
"ds_read_b128 a[144:147], v4 offset:12288\n"
"v_mfma_f32_16x16x32_bf16 a[28:31], a[56:59], a[152:155], a[28:31]\n"
"v_mfma_f32_16x16x32_bf16 a[28:31], a[60:63], a[156:159], a[28:31]\n"
"v_mfma_f32_16x16x32_bf16 a[24:27], a[52:55], a[156:159], a[24:27]\n"
"ds_read_b128 a[156:159], v4 offset:15360\n"
"v_mfma_f32_16x16x32_bf16 a[24:27], a[48:51], a[152:155], a[24:27]\n"
"s_waitcnt vmcnt(10) lgkmcnt(12)\n"
"ds_read_b128 a[152:155], v4 offset:14336\n"
"v_mfma_f32_16x16x32_bf16 v[144:147], a[64:67], a[96:99], v[144:147]\n"
"ds_read_b128 v[24:27], v2 offset:16384\n"
"v_mfma_f32_16x16x32_bf16 v[144:147], a[68:71], a[100:103], v[144:147]\n"
"global_load_dwordx4 a[48:51], v0, s[34:35] offset:0 sc1\n"
"v_mfma_f32_16x16x32_bf16 v[172:175], a[76:79], a[100:103], v[172:175]\n"
"v_mfma_f32_16x16x32_bf16 v[172:175], a[72:75], a[96:99], v[172:175]\n"
"v_mfma_f32_16x16x32_bf16 v[176:179], a[72:75], a[104:107], v[176:179]\n"
"v_mfma_f32_16x16x32_bf16 v[176:179], a[76:79], a[108:111], v[176:179]\n"
"v_mfma_f32_16x16x32_bf16 v[148:151], a[68:71], a[108:111], v[148:151]\n"
"v_mfma_f32_16x16x32_bf16 v[148:151], a[64:67], a[104:107], v[148:151]\n"
"s_waitcnt lgkmcnt(10)\n"
"v_mfma_f32_16x16x32_bf16 v[152:155], a[64:67], a[112:115], v[152:155]\n"
"v_mfma_f32_16x16x32_bf16 v[152:155], a[68:71], a[116:119], v[152:155]\n"
"global_load_dwordx4 a[52:55], v0, s[34:35] offset:1024 sc1\n"
"v_mfma_f32_16x16x32_bf16 v[180:183], a[76:79], a[116:119], v[180:183]\n"
"v_mfma_f32_16x16x32_bf16 v[180:183], a[72:75], a[112:115], v[180:183]\n"
"v_mfma_f32_16x16x32_bf16 v[184:187], a[72:75], a[120:123], v[184:187]\n"
"s_waitcnt lgkmcnt(0)\n"
"global_store_dwordx4 v0, v[24:27], s[52:53] offset:-4096 sc1\n"
"v_mfma_f32_16x16x32_bf16 v[184:187], a[76:79], a[124:127], v[184:187]\n"
"v_mfma_f32_16x16x32_bf16 v[156:159], a[68:71], a[124:127], v[156:159]\n"
"v_mfma_f32_16x16x32_bf16 v[156:159], a[64:67], a[120:123], v[156:159]\n"
"v_mfma_f32_16x16x32_bf16 v[160:163], a[64:67], a[128:131], v[160:163]\n"
"v_mfma_f32_16x16x32_bf16 v[160:163], a[68:71], a[132:135], v[160:163]\n"
"global_load_dwordx4 a[56:59], v0, s[34:35] offset:2048 sc1\n"
"v_mfma_f32_16x16x32_bf16 v[188:191], a[76:79], a[132:135], v[188:191]\n"
"v_mfma_f32_16x16x32_bf16 v[188:191], a[72:75], a[128:131], v[188:191]\n"
"v_mfma_f32_16x16x32_bf16 v[192:195], a[72:75], a[136:139], v[192:195]\n"
"v_mfma_f32_16x16x32_bf16 v[192:195], a[76:79], a[140:143], v[192:195]\n"
"v_mfma_f32_16x16x32_bf16 v[164:167], a[68:71], a[140:143], v[164:167]\n"
"v_mfma_f32_16x16x32_bf16 v[164:167], a[64:67], a[136:139], v[164:167]\n"
"v_mfma_f32_16x16x32_bf16 v[168:171], a[64:67], a[144:147], v[168:171]\n"
"v_mfma_f32_16x16x32_bf16 v[168:171], a[68:71], a[148:151], v[168:171]\n"
"global_load_dwordx4 a[60:63], v0, s[34:35] offset:3072 sc1\n"
"v_mfma_f32_16x16x32_bf16 v[196:199], a[76:79], a[148:151], v[196:199]\n"
"v_mfma_f32_16x16x32_bf16 v[196:199], a[72:75], a[144:147], v[196:199]\n"
"v_mfma_f32_16x16x32_bf16 a[20:23], a[72:75], a[152:155], a[20:23]\n"
"s_add_u32 s34, s34, 8192\n"
"v_mfma_f32_16x16x32_bf16 a[20:23], a[76:79], a[156:159], a[20:23]\n"
"v_mfma_f32_16x16x32_bf16 a[16:19], a[68:71], a[156:159], a[16:19]\n"
"v_mfma_f32_16x16x32_bf16 a[16:19], a[64:67], a[152:155], a[16:19]\n"
"s_waitcnt vmcnt(10)\n"
"ds_read_b128 v[28:31], v2 offset:17408\n"
"v_mfma_f32_16x16x32_bf16 v[200:203], a[80:83], a[96:99], v[200:203]\n"
"v_mfma_f32_16x16x32_bf16 v[200:203], a[84:87], a[100:103], v[200:203]\n"
"global_load_dwordx4 a[64:67], v0, s[34:35] offset:-4096 sc1\n"
"v_mfma_f32_16x16x32_bf16 v[228:231], a[92:95], a[100:103], v[228:231]\n"
"ds_read_b128 a[100:103], v3 offset:1024\n"
"v_mfma_f32_16x16x32_bf16 v[228:231], a[88:91], a[96:99], v[228:231]\n"
"ds_read_b128 a[96:99], v3 offset:0\n"
"v_mfma_f32_16x16x32_bf16 v[232:235], a[88:91], a[104:107], v[232:235]\n"
"v_mfma_f32_16x16x32_bf16 v[232:235], a[92:95], a[108:111], v[232:235]\n"
"v_mfma_f32_16x16x32_bf16 v[204:207], a[84:87], a[108:111], v[204:207]\n"
"ds_read_b128 a[108:111], v3 offset:3072\n"
"v_mfma_f32_16x16x32_bf16 v[204:207], a[80:83], a[104:107], v[204:207]\n"
"ds_read_b128 a[104:107], v3 offset:2048\n"
"v_mfma_f32_16x16x32_bf16 v[208:211], a[80:83], a[112:115], v[208:211]\n"
"v_mfma_f32_16x16x32_bf16 v[208:211], a[84:87], a[116:119], v[208:211]\n"
"global_load_dwordx4 a[68:71], v0, s[34:35] offset:-3072 sc1\n"
"v_mfma_f32_16x16x32_bf16 v[236:239], a[92:95], a[116:119], v[236:239]\n"
"s_waitcnt lgkmcnt(4)\n"
"global_store_dwordx4 v0, v[28:31], s[52:53] offset:-3072 sc1\n"
"ds_read_b128 a[116:119], v3 offset:5120\n"
"v_mfma_f32_16x16x32_bf16 v[236:239], a[88:91], a[112:115], v[236:239]\n"
"ds_read_b128 a[112:115], v3 offset:4096\n"
"v_mfma_f32_16x16x32_bf16 v[240:243], a[88:91], a[120:123], v[240:243]\n"
"v_mfma_f32_16x16x32_bf16 v[240:243], a[92:95], a[124:127], v[240:243]\n"
"v_mfma_f32_16x16x32_bf16 v[212:215], a[84:87], a[124:127], v[212:215]\n"
"ds_read_b128 a[124:127], v3 offset:7168\n"
"v_mfma_f32_16x16x32_bf16 v[212:215], a[80:83], a[120:123], v[212:215]\n"
"ds_read_b128 a[120:123], v3 offset:6144\n"
"v_mfma_f32_16x16x32_bf16 v[216:219], a[80:83], a[128:131], v[216:219]\n"
"v_mfma_f32_16x16x32_bf16 v[216:219], a[84:87], a[132:135], v[216:219]\n"
"global_load_dwordx4 a[72:75], v0, s[34:35] offset:-2048 sc1\n"
"v_mfma_f32_16x16x32_bf16 v[244:247], a[92:95], a[132:135], v[244:247]\n"
"ds_read_b128 a[132:135], v3 offset:9216\n"
"v_mfma_f32_16x16x32_bf16 v[244:247], a[88:91], a[128:131], v[244:247]\n"
"ds_read_b128 a[128:131], v3 offset:8192\n"
"v_mfma_f32_16x16x32_bf16 v[248:251], a[88:91], a[136:139], v[248:251]\n"
"v_mfma_f32_16x16x32_bf16 v[248:251], a[92:95], a[140:143], v[248:251]\n"
"v_mfma_f32_16x16x32_bf16 v[220:223], a[84:87], a[140:143], v[220:223]\n"
"ds_read_b128 a[140:143], v3 offset:11264\n"
"v_mfma_f32_16x16x32_bf16 v[220:223], a[80:83], a[136:139], v[220:223]\n"
"ds_read_b128 a[136:139], v3 offset:10240\n"
"v_mfma_f32_16x16x32_bf16 v[224:227], a[80:83], a[144:147], v[224:227]\n"
"v_mfma_f32_16x16x32_bf16 v[224:227], a[84:87], a[148:151], v[224:227]\n"
"global_load_dwordx4 a[76:79], v0, s[34:35] offset:-1024 sc1\n"
"v_mfma_f32_16x16x32_bf16 v[252:255], a[92:95], a[148:151], v[252:255]\n"
"ds_read_b128 a[148:151], v3 offset:13312\n"
"v_mfma_f32_16x16x32_bf16 v[252:255], a[88:91], a[144:147], v[252:255]\n"
"ds_read_b128 a[144:147], v3 offset:12288\n"
"v_mfma_f32_16x16x32_bf16 a[28:31], a[88:91], a[152:155], a[28:31]\n"
"v_mfma_f32_16x16x32_bf16 a[28:31], a[92:95], a[156:159], a[28:31]\n"
"ds_read_b128 v[24:27], v2 offset:18432\n"
"v_mfma_f32_16x16x32_bf16 a[24:27], a[84:87], a[156:159], a[24:27]\n"
"ds_read_b128 a[156:159], v3 offset:15360\n"
"v_mfma_f32_16x16x32_bf16 a[24:27], a[80:83], a[152:155], a[24:27]\n"
"s_waitcnt vmcnt(10) lgkmcnt(12)\n"
"ds_read_b128 a[152:155], v3 offset:14336\n"
"v_mfma_f32_16x16x32_bf16 v[144:147], a[32:35], a[96:99], v[144:147]\n"
"v_mfma_f32_16x16x32_bf16 v[144:147], a[36:39], a[100:103], v[144:147]\n"
"global_load_dwordx4 a[80:83], v0, s[34:35] offset:0 sc1\n"
"v_mfma_f32_16x16x32_bf16 v[172:175], a[44:47], a[100:103], v[172:175]\n"
"v_mfma_f32_16x16x32_bf16 v[172:175], a[40:43], a[96:99], v[172:175]\n"
"v_mfma_f32_16x16x32_bf16 v[176:179], a[40:43], a[104:107], v[176:179]\n"
"v_mfma_f32_16x16x32_bf16 v[176:179], a[44:47], a[108:111], v[176:179]\n"
"v_mfma_f32_16x16x32_bf16 v[148:151], a[36:39], a[108:111], v[148:151]\n"
"v_mfma_f32_16x16x32_bf16 v[148:151], a[32:35], a[104:107], v[148:151]\n"
"s_waitcnt lgkmcnt(9)\n"
"v_mfma_f32_16x16x32_bf16 v[152:155], a[32:35], a[112:115], v[152:155]\n"
"s_waitcnt lgkmcnt(2)\n"
"global_store_dwordx4 v0, v[24:27], s[52:53] offset:-2048 sc1\n"
"v_mfma_f32_16x16x32_bf16 v[152:155], a[36:39], a[116:119], v[152:155]\n"
"global_load_dwordx4 a[84:87], v0, s[34:35] offset:1024 sc1\n"
"v_mfma_f32_16x16x32_bf16 v[180:183], a[44:47], a[116:119], v[180:183]\n"
"v_mfma_f32_16x16x32_bf16 v[180:183], a[40:43], a[112:115], v[180:183]\n"
"v_mfma_f32_16x16x32_bf16 v[184:187], a[40:43], a[120:123], v[184:187]\n"
"v_mfma_f32_16x16x32_bf16 v[184:187], a[44:47], a[124:127], v[184:187]\n"
"v_mfma_f32_16x16x32_bf16 v[156:159], a[36:39], a[124:127], v[156:159]\n"
"v_mfma_f32_16x16x32_bf16 v[156:159], a[32:35], a[120:123], v[156:159]\n"
"v_mfma_f32_16x16x32_bf16 v[160:163], a[32:35], a[128:131], v[160:163]\n"
"v_mfma_f32_16x16x32_bf16 v[160:163], a[36:39], a[132:135], v[160:163]\n"
"global_load_dwordx4 a[88:91], v0, s[34:35] offset:2048 sc1\n"
"v_mfma_f32_16x16x32_bf16 v[188:191], a[44:47], a[132:135], v[188:191]\n"
"v_mfma_f32_16x16x32_bf16 v[188:191], a[40:43], a[128:131], v[188:191]\n"
"v_mfma_f32_16x16x32_bf16 v[192:195], a[40:43], a[136:139], v[192:195]\n"
"v_mfma_f32_16x16x32_bf16 v[192:195], a[44:47], a[140:143], v[192:195]\n"
"v_mfma_f32_16x16x32_bf16 v[164:167], a[36:39], a[140:143], v[164:167]\n"
"v_mfma_f32_16x16x32_bf16 v[164:167], a[32:35], a[136:139], v[164:167]\n"
"s_waitcnt lgkmcnt(0)\n"
"v_mfma_f32_16x16x32_bf16 v[168:171], a[32:35], a[144:147], v[168:171]\n"
"v_mfma_f32_16x16x32_bf16 v[168:171], a[36:39], a[148:151], v[168:171]\n"
"global_load_dwordx4 a[92:95], v0, s[34:35] offset:3072 sc1\n"
"v_mfma_f32_16x16x32_bf16 v[196:199], a[44:47], a[148:151], v[196:199]\n"
"v_mfma_f32_16x16x32_bf16 v[196:199], a[40:43], a[144:147], v[196:199]\n"
"ds_read_b128 v[28:31], v2 offset:19456\n"
"v_mfma_f32_16x16x32_bf16 a[20:23], a[40:43], a[152:155], a[20:23]\n"
"s_add_u32 s34, s34, 8192\n"
"v_mfma_f32_16x16x32_bf16 a[20:23], a[44:47], a[156:159], a[20:23]\n"
"v_mfma_f32_16x16x32_bf16 a[16:19], a[36:39], a[156:159], a[16:19]\n"
"v_mfma_f32_16x16x32_bf16 a[16:19], a[32:35], a[152:155], a[16:19]\n"
"s_waitcnt vmcnt(10)\n"
"v_mfma_f32_16x16x32_bf16 v[200:203], a[48:51], a[96:99], v[200:203]\n"
"v_mfma_f32_16x16x32_bf16 v[200:203], a[52:55], a[100:103], v[200:203]\n"
"global_load_dwordx4 a[32:35], v0, s[34:35] offset:-4096 sc1\n"
"v_mfma_f32_16x16x32_bf16 v[228:231], a[60:63], a[100:103], v[228:231]\n"
"ds_read_b128 a[100:103], v2 offset:1024\n"
"v_mfma_f32_16x16x32_bf16 v[228:231], a[56:59], a[96:99], v[228:231]\n"
"ds_read_b128 a[96:99], v2 offset:0\n"
"v_mfma_f32_16x16x32_bf16 v[232:235], a[56:59], a[104:107], v[232:235]\n"
"v_mfma_f32_16x16x32_bf16 v[232:235], a[60:63], a[108:111], v[232:235]\n"
"v_mfma_f32_16x16x32_bf16 v[204:207], a[52:55], a[108:111], v[204:207]\n"
"s_waitcnt lgkmcnt(2)\n"
"global_store_dwordx4 v0, v[28:31], s[52:53] offset:-1024 sc1\n"
"ds_read_b128 a[108:111], v2 offset:3072\n"
"v_mfma_f32_16x16x32_bf16 v[204:207], a[48:51], a[104:107], v[204:207]\n"
"ds_read_b128 a[104:107], v2 offset:2048\n"
"v_mfma_f32_16x16x32_bf16 v[208:211], a[48:51], a[112:115], v[208:211]\n"
"v_mfma_f32_16x16x32_bf16 v[208:211], a[52:55], a[116:119], v[208:211]\n"
"global_load_dwordx4 a[36:39], v0, s[34:35] offset:-3072 sc1\n"
"v_mfma_f32_16x16x32_bf16 v[236:239], a[60:63], a[116:119], v[236:239]\n"
"ds_read_b128 a[116:119], v2 offset:5120\n"
"v_mfma_f32_16x16x32_bf16 v[236:239], a[56:59], a[112:115], v[236:239]\n"
"ds_read_b128 a[112:115], v2 offset:4096\n"
"v_mfma_f32_16x16x32_bf16 v[240:243], a[56:59], a[120:123], v[240:243]\n"
"v_mfma_f32_16x16x32_bf16 v[240:243], a[60:63], a[124:127], v[240:243]\n"
"v_mfma_f32_16x16x32_bf16 v[212:215], a[52:55], a[124:127], v[212:215]\n"
"ds_read_b128 a[124:127], v2 offset:7168\n"
"v_mfma_f32_16x16x32_bf16 v[212:215], a[48:51], a[120:123], v[212:215]\n"
"ds_read_b128 a[120:123], v2 offset:6144\n"
"v_mfma_f32_16x16x32_bf16 v[216:219], a[48:51], a[128:131], v[216:219]\n"
"v_mfma_f32_16x16x32_bf16 v[216:219], a[52:55], a[132:135], v[216:219]\n"
"global_load_dwordx4 a[40:43], v0, s[34:35] offset:-2048 sc1\n"
"v_mfma_f32_16x16x32_bf16 v[244:247], a[60:63], a[132:135], v[244:247]\n"
"ds_read_b128 a[132:135], v2 offset:9216\n"
"v_mfma_f32_16x16x32_bf16 v[244:247], a[56:59], a[128:131], v[244:247]\n"
"ds_read_b128 a[128:131], v2 offset:8192\n"
"v_mfma_f32_16x16x32_bf16 v[248:251], a[56:59], a[136:139], v[248:251]\n"
"v_mfma_f32_16x16x32_bf16 v[248:251], a[60:63], a[140:143], v[248:251]\n"
"v_mfma_f32_16x16x32_bf16 v[220:223], a[52:55], a[140:143], v[220:223]\n"
"ds_read_b128 a[140:143], v2 offset:11264\n"
"v_mfma_f32_16x16x32_bf16 v[220:223], a[48:51], a[136:139], v[220:223]\n"
"ds_read_b128 a[136:139], v2 offset:10240\n"
"v_mfma_f32_16x16x32_bf16 v[224:227], a[48:51], a[144:147], v[224:227]\n"
"ds_read_b128 v[24:27], v2 offset:20480\n"
"v_mfma_f32_16x16x32_bf16 v[224:227], a[52:55], a[148:151], v[224:227]\n"
"global_load_dwordx4 a[44:47], v0, s[34:35] offset:-1024 sc1\n"
"v_mfma_f32_16x16x32_bf16 v[252:255], a[60:63], a[148:151], v[252:255]\n"
"ds_read_b128 a[148:151], v2 offset:13312\n"
"v_mfma_f32_16x16x32_bf16 v[252:255], a[56:59], a[144:147], v[252:255]\n"
"ds_read_b128 a[144:147], v2 offset:12288\n"
"v_mfma_f32_16x16x32_bf16 a[28:31], a[56:59], a[152:155], a[28:31]\n"
"v_mfma_f32_16x16x32_bf16 a[28:31], a[60:63], a[156:159], a[28:31]\n"
"v_mfma_f32_16x16x32_bf16 a[24:27], a[52:55], a[156:159], a[24:27]\n"
"ds_read_b128 a[156:159], v2 offset:15360\n"
"v_mfma_f32_16x16x32_bf16 a[24:27], a[48:51], a[152:155], a[24:27]\n"
"s_waitcnt vmcnt(10) lgkmcnt(12)\n"
"ds_read_b128 a[152:155], v2 offset:14336\n"
"v_mfma_f32_16x16x32_bf16 v[144:147], a[64:67], a[96:99], v[144:147]\n"
"v_mfma_f32_16x16x32_bf16 v[144:147], a[68:71], a[100:103], v[144:147]\n"
"global_load_dwordx4 a[48:51], v0, s[34:35] offset:0 sc1\n"
"v_mfma_f32_16x16x32_bf16 v[172:175], a[76:79], a[100:103], v[172:175]\n"
"v_mfma_f32_16x16x32_bf16 v[172:175], a[72:75], a[96:99], v[172:175]\n"
"v_mfma_f32_16x16x32_bf16 v[176:179], a[72:75], a[104:107], v[176:179]\n"
"s_waitcnt lgkmcnt(4)\n"
"global_store_dwordx4 v0, v[24:27], s[52:53] offset:0 sc1\n"
"v_mfma_f32_16x16x32_bf16 v[176:179], a[76:79], a[108:111], v[176:179]\n"
"v_mfma_f32_16x16x32_bf16 v[148:151], a[68:71], a[108:111], v[148:151]\n"
"v_mfma_f32_16x16x32_bf16 v[148:151], a[64:67], a[104:107], v[148:151]\n"
"v_mfma_f32_16x16x32_bf16 v[152:155], a[64:67], a[112:115], v[152:155]\n"
"v_mfma_f32_16x16x32_bf16 v[152:155], a[68:71], a[116:119], v[152:155]\n"
"global_load_dwordx4 a[52:55], v0, s[34:35] offset:1024 sc1\n"
"v_mfma_f32_16x16x32_bf16 v[180:183], a[76:79], a[116:119], v[180:183]\n"
"v_mfma_f32_16x16x32_bf16 v[180:183], a[72:75], a[112:115], v[180:183]\n"
"v_mfma_f32_16x16x32_bf16 v[184:187], a[72:75], a[120:123], v[184:187]\n"
"v_mfma_f32_16x16x32_bf16 v[184:187], a[76:79], a[124:127], v[184:187]\n"
"v_mfma_f32_16x16x32_bf16 v[156:159], a[68:71], a[124:127], v[156:159]\n"
"v_mfma_f32_16x16x32_bf16 v[156:159], a[64:67], a[120:123], v[156:159]\n"
"v_mfma_f32_16x16x32_bf16 v[160:163], a[64:67], a[128:131], v[160:163]\n"
"v_mfma_f32_16x16x32_bf16 v[160:163], a[68:71], a[132:135], v[160:163]\n"
"global_load_dwordx4 a[56:59], v0, s[34:35] offset:2048 sc1\n"
"v_mfma_f32_16x16x32_bf16 v[188:191], a[76:79], a[132:135], v[188:191]\n"
"v_mfma_f32_16x16x32_bf16 v[188:191], a[72:75], a[128:131], v[188:191]\n"
"v_mfma_f32_16x16x32_bf16 v[192:195], a[72:75], a[136:139], v[192:195]\n"
"v_mfma_f32_16x16x32_bf16 v[192:195], a[76:79], a[140:143], v[192:195]\n"
"v_mfma_f32_16x16x32_bf16 v[164:167], a[68:71], a[140:143], v[164:167]\n"
"v_mfma_f32_16x16x32_bf16 v[164:167], a[64:67], a[136:139], v[164:167]\n"
"s_waitcnt lgkmcnt(0)\n"
"ds_read_b128 v[28:31], v2 offset:21504\n"
"v_mfma_f32_16x16x32_bf16 v[168:171], a[64:67], a[144:147], v[168:171]\n"
"v_mfma_f32_16x16x32_bf16 v[168:171], a[68:71], a[148:151], v[168:171]\n"
"global_load_dwordx4 a[60:63], v0, s[34:35] offset:3072 sc1\n"
"v_mfma_f32_16x16x32_bf16 v[196:199], a[76:79], a[148:151], v[196:199]\n"
"v_mfma_f32_16x16x32_bf16 v[196:199], a[72:75], a[144:147], v[196:199]\n"
"v_mfma_f32_16x16x32_bf16 a[20:23], a[72:75], a[152:155], a[20:23]\n"
"s_add_u32 s34, s34, 8192\n"
"v_mfma_f32_16x16x32_bf16 a[20:23], a[76:79], a[156:159], a[20:23]\n"
"v_mfma_f32_16x16x32_bf16 a[16:19], a[68:71], a[156:159], a[16:19]\n"
"v_mfma_f32_16x16x32_bf16 a[16:19], a[64:67], a[152:155], a[16:19]\n"
"s_waitcnt vmcnt(10)\n"
"v_mfma_f32_16x16x32_bf16 v[200:203], a[80:83], a[96:99], v[200:203]\n"
"v_mfma_f32_16x16x32_bf16 v[200:203], a[84:87], a[100:103], v[200:203]\n"
"global_load_dwordx4 a[64:67], v0, s[34:35] offset:-4096 sc1\n"
"v_mfma_f32_16x16x32_bf16 v[228:231], a[92:95], a[100:103], v[228:231]\n"
"s_waitcnt lgkmcnt(0)\n"
"global_store_dwordx4 v0, v[28:31], s[52:53] offset:1024 sc1\n"
"ds_read_b128 a[100:103], v2 offset:17408\n"
"v_mfma_f32_16x16x32_bf16 v[228:231], a[88:91], a[96:99], v[228:231]\n"
"ds_read_b128 a[96:99], v2 offset:16384\n"
"v_mfma_f32_16x16x32_bf16 v[232:235], a[88:91], a[104:107], v[232:235]\n"
"v_mfma_f32_16x16x32_bf16 v[232:235], a[92:95], a[108:111], v[232:235]\n"
"v_mfma_f32_16x16x32_bf16 v[204:207], a[84:87], a[108:111], v[204:207]\n"
"ds_read_b128 a[108:111], v2 offset:19456\n"
"v_mfma_f32_16x16x32_bf16 v[204:207], a[80:83], a[104:107], v[204:207]\n"
"ds_read_b128 a[104:107], v2 offset:18432\n"
"v_mfma_f32_16x16x32_bf16 v[208:211], a[80:83], a[112:115], v[208:211]\n"
"v_mfma_f32_16x16x32_bf16 v[208:211], a[84:87], a[116:119], v[208:211]\n"
"global_load_dwordx4 a[68:71], v0, s[34:35] offset:-3072 sc1\n"
"v_mfma_f32_16x16x32_bf16 v[236:239], a[92:95], a[116:119], v[236:239]\n"
"ds_read_b128 a[116:119], v2 offset:21504\n"
"v_mfma_f32_16x16x32_bf16 v[236:239], a[88:91], a[112:115], v[236:239]\n"
"ds_read_b128 a[112:115], v2 offset:20480\n"
"v_mfma_f32_16x16x32_bf16 v[240:243], a[88:91], a[120:123], v[240:243]\n"
"v_mfma_f32_16x16x32_bf16 v[240:243], a[92:95], a[124:127], v[240:243]\n"
"v_mfma_f32_16x16x32_bf16 v[212:215], a[84:87], a[124:127], v[212:215]\n"
"ds_read_b128 a[124:127], v2 offset:23552\n"
"v_mfma_f32_16x16x32_bf16 v[212:215], a[80:83], a[120:123], v[212:215]\n"
"ds_read_b128 a[120:123], v2 offset:22528\n"
"v_mfma_f32_16x16x32_bf16 v[216:219], a[80:83], a[128:131], v[216:219]\n"
"v_mfma_f32_16x16x32_bf16 v[216:219], a[84:87], a[132:135], v[216:219]\n"
"global_load_dwordx4 a[72:75], v0, s[34:35] offset:-2048 sc1\n"
"v_mfma_f32_16x16x32_bf16 v[244:247], a[92:95], a[132:135], v[244:247]\n"
"ds_read_b128 a[132:135], v2 offset:25600\n"
"v_mfma_f32_16x16x32_bf16 v[244:247], a[88:91], a[128:131], v[244:247]\n"
"ds_read_b128 a[128:131], v2 offset:24576\n"
"v_mfma_f32_16x16x32_bf16 v[248:251], a[88:91], a[136:139], v[248:251]\n"
"v_mfma_f32_16x16x32_bf16 v[248:251], a[92:95], a[140:143], v[248:251]\n"
"ds_read_b128 v[24:27], v2 offset:22528\n"
"v_mfma_f32_16x16x32_bf16 v[220:223], a[84:87], a[140:143], v[220:223]\n"
"ds_read_b128 a[140:143], v2 offset:27648\n"
"v_mfma_f32_16x16x32_bf16 v[220:223], a[80:83], a[136:139], v[220:223]\n"
"ds_read_b128 a[136:139], v2 offset:26624\n"
"v_mfma_f32_16x16x32_bf16 v[224:227], a[80:83], a[144:147], v[224:227]\n"
"v_mfma_f32_16x16x32_bf16 v[224:227], a[84:87], a[148:151], v[224:227]\n"
"global_load_dwordx4 a[76:79], v0, s[34:35] offset:-1024 sc1\n"
"v_mfma_f32_16x16x32_bf16 v[252:255], a[92:95], a[148:151], v[252:255]\n"
"ds_read_b128 a[148:151], v2 offset:29696\n"
"v_mfma_f32_16x16x32_bf16 v[252:255], a[88:91], a[144:147], v[252:255]\n"
"ds_read_b128 a[144:147], v2 offset:28672\n"
"v_mfma_f32_16x16x32_bf16 a[28:31], a[88:91], a[152:155], a[28:31]\n"
"v_mfma_f32_16x16x32_bf16 a[28:31], a[92:95], a[156:159], a[28:31]\n"
"v_mfma_f32_16x16x32_bf16 a[24:27], a[84:87], a[156:159], a[24:27]\n"
"s_bitcmp0_b32 %26, 0\n"
"ds_read_b128 a[156:159], v2 offset:31744\n"
"v_mfma_f32_16x16x32_bf16 a[24:27], a[80:83], a[152:155], a[24:27]\n"
"ds_read_b128 a[152:155], v2 offset:30720\n"
"s_cbranch_scc1 L_P3F\n"
"s_waitcnt vmcnt(10) lgkmcnt(13)\n"
"v_mfma_f32_16x16x32_bf16 v[32:35], a[32:35], a[96:99], v[32:35]\n"
"s_waitcnt lgkmcnt(6)\n"
"global_store_dwordx4 v0, v[24:27], s[52:53] offset:2048 sc1\n"
"v_mfma_f32_16x16x32_bf16 v[32:35], a[36:39], a[100:103], v[32:35]\n"
"global_load_dwordx4 a[80:83], v0, s[34:35] offset:0 sc1\n"
"v_mfma_f32_16x16x32_bf16 v[60:63], a[44:47], a[100:103], v[60:63]\n"
"s_barrier\n"
"v_mfma_f32_16x16x32_bf16 v[60:63], a[40:43], a[96:99], v[60:63]\n"
"v_mfma_f32_16x16x32_bf16 v[64:67], a[40:43], a[104:107], v[64:67]\n"
"v_mfma_f32_16x16x32_bf16 v[64:67], a[44:47], a[108:111], v[64:67]\n"
"v_mfma_f32_16x16x32_bf16 v[36:39], a[36:39], a[108:111], v[36:39]\n"
"v_mfma_f32_16x16x32_bf16 v[36:39], a[32:35], a[104:107], v[36:39]\n"
"v_mfma_f32_16x16x32_bf16 v[40:43], a[32:35], a[112:115], v[40:43]\n"
"v_mfma_f32_16x16x32_bf16 v[40:43], a[36:39], a[116:119], v[40:43]\n"
"global_load_dwordx4 a[84:87], v0, s[34:35] offset:1024 sc1\n"
"v_mfma_f32_16x16x32_bf16 v[68:71], a[44:47], a[116:119], v[68:71]\n"
"v_mfma_f32_16x16x32_bf16 v[68:71], a[40:43], a[112:115], v[68:71]\n"
"v_mfma_f32_16x16x32_bf16 v[72:75], a[40:43], a[120:123], v[72:75]\n"
"v_mfma_f32_16x16x32_bf16 v[72:75], a[44:47], a[124:127], v[72:75]\n"
"v_mfma_f32_16x16x32_bf16 v[44:47], a[36:39], a[124:127], v[44:47]\n"
"v_mfma_f32_16x16x32_bf16 v[44:47], a[32:35], a[120:123], v[44:47]\n"
"s_waitcnt lgkmcnt(4)\n"
"v_mfma_f32_16x16x32_bf16 v[48:51], a[32:35], a[128:131], v[48:51]\n"
"v_mfma_f32_16x16x32_bf16 v[48:51], a[36:39], a[132:135], v[48:51]\n"
"global_load_dwordx4 a[88:91], v0, s[34:35] offset:2048 sc1\n"
"v_lshrrev_b32 v16, 4, v0\n"
"v_mfma_f32_16x16x32_bf16 v[76:79], a[44:47], a[132:135], v[76:79]\n"
"v_and_b32 v16, 0x30, v16\n"
"v_mfma_f32_16x16x32_bf16 v[76:79], a[40:43], a[128:131], v[76:79]\n"
"ds_read_b128 v[28:31], v2 offset:23552\n"
"buffer_load_dwordx2 v[18:19], v16, s[44:47], s61 offen offset:0\n"
"v_mfma_f32_16x16x32_bf16 v[80:83], a[40:43], a[136:139], v[80:83]\n"
"buffer_load_dwordx2 v[8:9], off, s[56:59], %14 sc1\n"
"v_mfma_f32_16x16x32_bf16 v[80:83], a[44:47], a[140:143], v[80:83]\n"
"v_mfma_f32_16x16x32_bf16 v[52:55], a[36:39], a[140:143], v[52:55]\n"
"v_mfma_f32_16x16x32_bf16 v[52:55], a[32:35], a[136:139], v[52:55]\n"
"s_waitcnt lgkmcnt(1)\n"
"v_mfma_f32_16x16x32_bf16 v[56:59], a[32:35], a[144:147], v[56:59]\n"
"v_mfma_f32_16x16x32_bf16 v[56:59], a[36:39], a[148:151], v[56:59]\n"
"global_load_dwordx4 a[92:95], v0, s[34:35] offset:3072 sc1\n"
"v_lshrrev_b32 v20, 4, v0\n"
"v_mfma_f32_16x16x32_bf16 v[84:87], a[44:47], a[148:151], v[84:87]\n"
"v_and_b32 v20, 0x30, v20\n"
"v_mfma_f32_16x16x32_bf16 v[84:87], a[40:43], a[144:147], v[84:87]\n"
"buffer_load_dwordx2 v[22:23], v20, s[44:47], s61 offen offset:8\n"
"v_mfma_f32_16x16x32_bf16 a[4:7], a[40:43], a[152:155], a[4:7]\n"
"s_add_u32 s34, s34, 8192\n"
"v_mfma_f32_16x16x32_bf16 a[4:7], a[44:47], a[156:159], a[4:7]\n"
"v_mfma_f32_16x16x32_bf16 a[0:3], a[36:39], a[156:159], a[0:3]\n"
"s_waitcnt lgkmcnt(0)\n"
"global_store_dwordx4 v0, v[28:31], s[52:53] offset:3072 sc1\n"
"v_mfma_f32_16x16x32_bf16 a[0:3], a[32:35], a[152:155], a[0:3]\n"
"s_waitcnt vmcnt(14)\n"
"v_mfma_f32_16x16x32_bf16 v[88:91], a[48:51], a[96:99], v[88:91]\n"
"v_mfma_f32_16x16x32_bf16 v[88:91], a[52:55], a[100:103], v[88:91]\n"
"global_load_dwordx4 a[32:35], v0, s[34:35] offset:-4096 sc1\n"
"v_mfma_f32_16x16x32_bf16 v[116:119], a[60:63], a[100:103], v[116:119]\n"
"v_mfma_f32_16x16x32_bf16 v[116:119], a[56:59], a[96:99], v[116:119]\n"
"v_mfma_f32_16x16x32_bf16 v[120:123], a[56:59], a[104:107], v[120:123]\n"
"v_mfma_f32_16x16x32_bf16 v[120:123], a[60:63], a[108:111], v[120:123]\n"
"v_mfma_f32_16x16x32_bf16 v[92:95], a[52:55], a[108:111], v[92:95]\n"
"v_mfma_f32_16x16x32_bf16 v[92:95], a[48:51], a[104:107], v[92:95]\n"
"v_mfma_f32_16x16x32_bf16 v[96:99], a[48:51], a[112:115], v[96:99]\n"
"v_mfma_f32_16x16x32_bf16 v[96:99], a[52:55], a[116:119], v[96:99]\n"
"global_load_dwordx4 a[36:39], v0, s[34:35] offset:-3072 sc1\n"
"v_mfma_f32_16x16x32_bf16 v[124:127], a[60:63], a[116:119], v[124:127]\n"
"v_mfma_f32_16x16x32_bf16 v[124:127], a[56:59], a[112:115], v[124:127]\n"
"v_mfma_f32_16x16x32_bf16 v[128:131], a[56:59], a[120:123], v[128:131]\n"
"v_mfma_f32_16x16x32_bf16 v[128:131], a[60:63], a[124:127], v[128:131]\n"
"v_mfma_f32_16x16x32_bf16 v[100:103], a[52:55], a[124:127], v[100:103]\n"
"v_mfma_f32_16x16x32_bf16 v[100:103], a[48:51], a[120:123], v[100:103]\n"
"v_mfma_f32_16x16x32_bf16 v[104:107], a[48:51], a[128:131], v[104:107]\n"
"ds_read_b128 v[24:27], v2 offset:24576\n"
"v_mfma_f32_16x16x32_bf16 v[104:107], a[52:55], a[132:135], v[104:107]\n"
"global_load_dwordx4 a[40:43], v0, s[34:35] offset:-2048 sc1\n"
"v_mfma_f32_16x16x32_bf16 v[132:135], a[60:63], a[132:135], v[132:135]\n"
"v_mfma_f32_16x16x32_bf16 v[132:135], a[56:59], a[128:131], v[132:135]\n"
"v_mfma_f32_16x16x32_bf16 v[136:139], a[56:59], a[136:139], v[136:139]\n"
"v_mfma_f32_16x16x32_bf16 v[136:139], a[60:63], a[140:143], v[136:139]\n"
"v_mfma_f32_16x16x32_bf16 v[108:111], a[52:55], a[140:143], v[108:111]\n"
"v_mfma_f32_16x16x32_bf16 v[108:111], a[48:51], a[136:139], v[108:111]\n"
"v_mfma_f32_16x16x32_bf16 v[112:115], a[48:51], a[144:147], v[112:115]\n"
"v_mfma_f32_16x16x32_bf16 v[112:115], a[52:55], a[148:151], v[112:115]\n"
"global_load_dwordx4 a[44:47], v0, s[34:35] offset:-1024 sc1\n"
"v_mfma_f32_16x16x32_bf16 v[140:143], a[60:63], a[148:151], v[140:143]\n"
"v_mfma_f32_16x16x32_bf16 v[140:143], a[56:59], a[144:147], v[140:143]\n"
"v_mfma_f32_16x16x32_bf16 a[12:15], a[56:59], a[152:155], a[12:15]\n"
"s_waitcnt lgkmcnt(0)\n"
"global_store_dwordx4 v0, v[24:27], s[54:55] offset:-4096 sc1\n"
"v_mfma_f32_16x16x32_bf16 a[12:15], a[60:63], a[156:159], a[12:15]\n"
"v_mfma_f32_16x16x32_bf16 a[8:11], a[52:55], a[156:159], a[8:11]\n"
"v_mfma_f32_16x16x32_bf16 a[8:11], a[48:51], a[152:155], a[8:11]\n"
"s_waitcnt vmcnt(14)\n"
"v_mfma_f32_16x16x32_bf16 v[144:147], a[64:67], a[96:99], v[144:147]\n"
"v_mfma_f32_16x16x32_bf16 v[144:147], a[68:71], a[100:103], v[144:147]\n"
"global_load_dwordx4 a[48:51], v0, s[34:35] offset:0 sc1\n"
"v_mfma_f32_16x16x32_bf16 v[172:175], a[76:79], a[100:103], v[172:175]\n"
"v_mfma_f32_16x16x32_bf16 v[172:175], a[72:75], a[96:99], v[172:175]\n"
"v_mfma_f32_16x16x32_bf16 v[176:179], a[72:75], a[104:107], v[176:179]\n"
"v_mfma_f32_16x16x32_bf16 v[176:179], a[76:79], a[108:111], v[176:179]\n"
"v_mfma_f32_16x16x32_bf16 v[148:151], a[68:71], a[108:111], v[148:151]\n"
"v_mfma_f32_16x16x32_bf16 v[148:151], a[64:67], a[104:107], v[148:151]\n"
"v_mfma_f32_16x16x32_bf16 v[152:155], a[64:67], a[112:115], v[152:155]\n"
"v_mfma_f32_16x16x32_bf16 v[152:155], a[68:71], a[116:119], v[152:155]\n"
"global_load_dwordx4 a[52:55], v0, s[34:35] offset:1024 sc1\n"
"v_mfma_f32_16x16x32_bf16 v[180:183], a[76:79], a[116:119], v[180:183]\n"
"v_mfma_f32_16x16x32_bf16 v[180:183], a[72:75], a[112:115], v[180:183]\n"
"v_mfma_f32_16x16x32_bf16 v[184:187], a[72:75], a[120:123], v[184:187]\n"
"v_mfma_f32_16x16x32_bf16 v[184:187], a[76:79], a[124:127], v[184:187]\n"
"v_mfma_f32_16x16x32_bf16 v[156:159], a[68:71], a[124:127], v[156:159]\n"
"v_mfma_f32_16x16x32_bf16 v[156:159], a[64:67], a[120:123], v[156:159]\n"
"ds_read_b128 v[28:31], v2 offset:26624\n"
"v_mfma_f32_16x16x32_bf16 v[160:163], a[64:67], a[128:131], v[160:163]\n"
"v_mfma_f32_16x16x32_bf16 v[160:163], a[68:71], a[132:135], v[160:163]\n"
"global_load_dwordx4 a[56:59], v0, s[34:35] offset:2048 sc1\n"
"v_mfma_f32_16x16x32_bf16 v[188:191], a[76:79], a[132:135], v[188:191]\n"
"v_mfma_f32_16x16x32_bf16 v[188:191], a[72:75], a[128:131], v[188:191]\n"
"v_mfma_f32_16x16x32_bf16 v[192:195], a[72:75], a[136:139], v[192:195]\n"
"v_mfma_f32_16x16x32_bf16 v[192:195], a[76:79], a[140:143], v[192:195]\n"
"v_mfma_f32_16x16x32_bf16 v[164:167], a[68:71], a[140:143], v[164:167]\n"
"v_mfma_f32_16x16x32_bf16 v[164:167], a[64:67], a[136:139], v[164:167]\n"
"v_mfma_f32_16x16x32_bf16 v[168:171], a[64:67], a[144:147], v[168:171]\n"
"v_mfma_f32_16x16x32_bf16 v[168:171], a[68:71], a[148:151], v[168:171]\n"
"global_load_dwordx4 a[60:63], v0, s[34:35] offset:3072 sc1\n"
"v_mfma_f32_16x16x32_bf16 v[196:199], a[76:79], a[148:151], v[196:199]\n"
"s_waitcnt lgkmcnt(0)\n"
"global_store_dwordx4 v0, v[28:31], s[54:55] offset:-2048 sc1\n"
"v_mfma_f32_16x16x32_bf16 v[196:199], a[72:75], a[144:147], v[196:199]\n"
"v_mfma_f32_16x16x32_bf16 a[20:23], a[72:75], a[152:155], a[20:23]\n"
"s_add_u32 s34, s34, 8192\n"
"v_mfma_f32_16x16x32_bf16 a[20:23], a[76:79], a[156:159], a[20:23]\n"
"v_mfma_f32_16x16x32_bf16 a[16:19], a[68:71], a[156:159], a[16:19]\n"
"v_mfma_f32_16x16x32_bf16 a[16:19], a[64:67], a[152:155], a[16:19]\n"
"s_waitcnt vmcnt(12)\n"
"v_mfma_f32_16x16x32_bf16 v[200:203], a[80:83], a[96:99], v[200:203]\n"
"v_mfma_f32_16x16x32_bf16 v[200:203], a[84:87], a[100:103], v[200:203]\n"
"global_load_dwordx4 a[64:67], v0, s[34:35] offset:-4096 sc1\n"
"v_mfma_f32_16x16x32_bf16 v[228:231], a[92:95], a[100:103], v[228:231]\n"
"ds_read_b128 a[100:103], v3 offset:17408\n"
"v_mfma_f32_16x16x32_bf16 v[228:231], a[88:91], a[96:99], v[228:231]\n"
"ds_read_b128 a[96:99], v3 offset:16384\n"
"v_mfma_f32_16x16x32_bf16 v[232:235], a[88:91], a[104:107], v[232:235]\n"
"v_mfma_f32_16x16x32_bf16 v[232:235], a[92:95], a[108:111], v[232:235]\n"
"v_mfma_f32_16x16x32_bf16 v[204:207], a[84:87], a[108:111], v[204:207]\n"
"ds_read_b128 a[108:111], v3 offset:19456\n"
"v_mfma_f32_16x16x32_bf16 v[204:207], a[80:83], a[104:107], v[204:207]\n"
"ds_read_b128 a[104:107], v3 offset:18432\n"
"v_mfma_f32_16x16x32_bf16 v[208:211], a[80:83], a[112:115], v[208:211]\n"
"v_mfma_f32_16x16x32_bf16 v[208:211], a[84:87], a[116:119], v[208:211]\n"
"L_st6226:\n"
"s_cbranch_execz L_st8516\n"
"global_load_dwordx4 a[68:71], v0, s[34:35] offset:-3072 sc1\n"
"v_mfma_f32_16x16x32_bf16 v[236:239], a[92:95], a[116:119], v[236:239]\n"
"ds_read_b128 a[116:119], v3 offset:21504\n"
"v_mfma_f32_16x16x32_bf16 v[236:239], a[88:91], a[112:115], v[236:239]\n"
"ds_read_b128 a[112:115], v3 offset:20480\n"
"v_mfma_f32_16x16x32_bf16 v[240:243], a[88:91], a[120:123], v[240:243]\n"
"v_mfma_f32_16x16x32_bf16 v[240:243], a[92:95], a[124:127], v[240:243]\n"
"ds_read_b128 v[24:27], v2 offset:29696\n"
"v_mfma_f32_16x16x32_bf16 v[212:215], a[84:87], a[124:127], v[212:215]\n"
"ds_read_b128 a[124:127], v3 offset:23552\n"
"v_mfma_f32_16x16x32_bf16 v[212:215], a[80:83], a[120:123], v[212:215]\n"
"ds_read_b128 a[120:123], v3 offset:22528\n"
"v_mfma_f32_16x16x32_bf16 v[216:219], a[80:83], a[128:131], v[216:219]\n"
"v_mfma_f32_16x16x32_bf16 v[216:219], a[84:87], a[132:135], v[216:219]\n"
"global_load_dwordx4 a[72:75], v0, s[34:35] offset:-2048 sc1\n"
"v_mfma_f32_16x16x32_bf16 v[244:247], a[92:95], a[132:135], v[244:247]\n"
"ds_read_b128 a[132:135], v3 offset:25600\n"
"v_mfma_f32_16x16x32_bf16 v[244:247], a[88:91], a[128:131], v[244:247]\n"
"ds_read_b128 a[128:131], v3 offset:24576\n"
"v_mfma_f32_16x16x32_bf16 v[248:251], a[88:91], a[136:139], v[248:251]\n"
"v_subrev_u32 v8, s65, v8\n"
"v_mfma_f32_16x16x32_bf16 v[248:251], a[92:95], a[140:143], v[248:251]\n"
"v_xor_b32 v9, s79, v9\n"
"v_mfma_f32_16x16x32_bf16 v[220:223], a[84:87], a[140:143], v[220:223]\n"
"ds_read_b128 a[140:143], v3 offset:27648\n"
"v_or_b32 v8, v8, v9\n"
"v_mfma_f32_16x16x32_bf16 v[220:223], a[80:83], a[136:139], v[220:223]\n"
"ds_read_b128 a[136:139], v3 offset:26624\n"
"v_cmp_lt_u32 vcc, 15, v8\n"
"v_mfma_f32_16x16x32_bf16 v[224:227], a[80:83], a[144:147], v[224:227]\n"
"s_waitcnt lgkmcnt(6)\n"
"global_store_dwordx4 v0, v[24:27], s[54:55] offset:1024 sc1\n"
"s_cbranch_vccnz L_slow0\n"
"L_back0:\n"
"v_mfma_f32_16x16x32_bf16 v[224:227], a[84:87], a[148:151], v[224:227]\n"
"global_load_dwordx4 a[76:79], v0, s[34:35] offset:-1024 sc1\n"
"v_readfirstlane_b32 s66, v9\n"
"v_mfma_f32_16x16x32_bf16 v[252:255], a[92:95], a[148:151], v[252:255]\n"
"ds_read_b128 a[148:151], v3 offset:29696\n"
"v_mfma_f32_16x16x32_bf16 v[252:255], a[88:91], a[144:147], v[252:255]\n"
"s_and_b32 s66, s66, 3\n"
"ds_read_b128 a[144:147], v3 offset:28672\n"
"v_mfma_f32_16x16x32_bf16 a[28:31], a[88:91], a[152:155], a[28:31]\n"
"s_lshl_b32 s69, s66, 13\n"
"v_mfma_f32_16x16x32_bf16 a[28:31], a[92:95], a[156:159], a[28:31]\n"
"s_cmp_eq_u32 s66, 3\n"
"s_cselect_b32 s75, 8192, 0\n"
"v_mfma_f32_16x16x32_bf16 a[24:27], a[84:87], a[156:159], a[24:27]\n"
"s_and_b32 s29, s66, s30\n"
"ds_read_b128 a[156:159], v3 offset:31744\n"
"v_mfma_f32_16x16x32_bf16 a[24:27], a[80:83], a[152:155], a[24:27]\n"
"s_waitcnt vmcnt(11)\n"
"ds_read_b128 a[152:155], v3 offset:30720\n"
"v_mfma_f32_16x16x32_bf16 v[32:35], a[32:35], a[96:99], v[32:35]\n"
"s_xor_b32 s31, s79, s66\n"
"v_mfma_f32_16x16x32_bf16 v[32:35], a[36:39], a[100:103], v[32:35]\n"
"global_load_dwordx4 a[80:83], v0, s[34:35] offset:0 sc1\n"
"v_mfma_f32_16x16x32_bf16 v[60:63], a[44:47], a[100:103], v[60:63]\n"
"v_mfma_f32_16x16x32_bf16 v[60:63], a[40:43], a[96:99], v[60:63]\n"
"v_mfma_f32_16x16x32_bf16 v[64:67], a[40:43], a[104:107], v[64:67]\n"
"v_mfma_f32_16x16x32_bf16 v[64:67], a[44:47], a[108:111], v[64:67]\n"
"v_mfma_f32_16x16x32_bf16 v[36:39], a[36:39], a[108:111], v[36:39]\n"
"s_add_u32 m0, s64, 0\n"
"v_mfma_f32_16x16x32_bf16 v[36:39], a[32:35], a[104:107], v[36:39]\n"
"s_waitcnt lgkmcnt(8)\n"
"v_mfma_f32_16x16x32_bf16 v[40:43], a[32:35], a[112:115], v[40:43]\n"
"v_mfma_f32_16x16x32_bf16 v[40:43], a[36:39], a[116:119], v[40:43]\n"
"global_load_dwordx4 a[84:87], v0, s[34:35] offset:1024 sc1\n"
"v_mfma_f32_16x16x32_bf16 v[68:71], a[44:47], a[116:119], v[68:71]\n"
"v_mfma_f32_16x16x32_bf16 v[68:71], a[40:43], a[112:115], v[68:71]\n"
"v_mfma_f32_16x16x32_bf16 v[72:75], a[40:43], a[120:123], v[72:75]\n"
"v_mfma_f32_16x16x32_bf16 v[72:75], a[44:47], a[124:127], v[72:75]\n"
"v_mfma_f32_16x16x32_bf16 v[44:47], a[36:39], a[124:127], v[44:47]\n"
"v_mfma_f32_16x16x32_bf16 v[44:47], a[32:35], a[120:123], v[44:47]\n"
"s_waitcnt lgkmcnt(4)\n"
"v_mfma_f32_16x16x32_bf16 v[48:51], a[32:35], a[128:131], v[48:51]\n"
"v_mfma_f32_16x16x32_bf16 v[48:51], a[36:39], a[132:135], v[48:51]\n"
"global_load_dwordx4 a[88:91], v0, s[34:35] offset:2048 sc1\n"
"v_mfma_f32_16x16x32_bf16 v[76:79], a[44:47], a[132:135], v[76:79]\n"
"v_mfma_f32_16x16x32_bf16 v[76:79], a[40:43], a[128:131], v[76:79]\n"
"v_mfma_f32_16x16x32_bf16 v[80:83], a[40:43], a[136:139], v[80:83]\n"
"v_mfma_f32_16x16x32_bf16 v[80:83], a[44:47], a[140:143], v[80:83]\n"
"buffer_load_dwordx4 v0, s[48:51], s75 offen offset:0 sc1 lds\n"
"v_mfma_f32_16x16x32_bf16 v[52:55], a[36:39], a[140:143], v[52:55]\n"
"v_mfma_f32_16x16x32_bf16 v[52:55], a[32:35], a[136:139], v[52:55]\n"
"s_waitcnt lgkmcnt(0)\n"
"v_mfma_f32_16x16x32_bf16 v[56:59], a[32:35], a[144:147], v[56:59]\n"
"v_mfma_f32_16x16x32_bf16 v[56:59], a[36:39], a[148:151], v[56:59]\n"
"global_load_dwordx4 a[92:95], v0, s[34:35] offset:3072 sc1\n"
"v_mfma_f32_16x16x32_bf16 v[84:87], a[44:47], a[148:151], v[84:87]\n"
"v_mfma_f32_16x16x32_bf16 v[84:87], a[40:43], a[144:147], v[84:87]\n"
"v_mfma_f32_16x16x32_bf16 a[4:7], a[40:43], a[152:155], a[4:7]\n"
"s_add_u32 s34, s34, 8192\n"
"v_mfma_f32_16x16x32_bf16 a[4:7], a[44:47], a[156:159], a[4:7]\n"
"v_mfma_f32_16x16x32_bf16 a[0:3], a[36:39], a[156:159], a[0:3]\n"
"v_mfma_f32_16x16x32_bf16 a[0:3], a[32:35], a[152:155], a[0:3]\n"
"s_waitcnt vmcnt(11)\n"
"v_mfma_f32_16x16x32_bf16 v[88:91], a[48:51], a[96:99], v[88:91]\n"
"v_mfma_f32_16x16x32_bf16 v[88:91], a[52:55], a[100:103], v[88:91]\n"
"global_load_dwordx4 a[32:35], v0, s[34:35] offset:-4096 sc1\n"
"v_mfma_f32_16x16x32_bf16 v[116:119], a[60:63], a[100:103], v[116:119]\n"
"v_mfma_f32_16x16x32_bf16 v[116:119], a[56:59], a[96:99], v[116:119]\n"
"buffer_load_dwordx4 v0, s[48:51], s75 offen offset:1024 sc1 lds\n"
"v_mfma_f32_16x16x32_bf16 v[120:123], a[56:59], a[104:107], v[120:123]\n"
"v_mfma_f32_16x16x32_bf16 v[120:123], a[60:63], a[108:111], v[120:123]\n"
"v_mfma_f32_16x16x32_bf16 v[92:95], a[52:55], a[108:111], v[92:95]\n"
"v_mfma_f32_16x16x32_bf16 v[92:95], a[48:51], a[104:107], v[92:95]\n"
"v_mfma_f32_16x16x32_bf16 v[96:99], a[48:51], a[112:115], v[96:99]\n"
"v_mfma_f32_16x16x32_bf16 v[96:99], a[52:55], a[116:119], v[96:99]\n"
"global_load_dwordx4 a[36:39], v0, s[34:35] offset:-3072 sc1\n"
"v_mfma_f32_16x16x32_bf16 v[124:127], a[60:63], a[116:119], v[124:127]\n"
"v_mfma_f32_16x16x32_bf16 v[124:127], a[56:59], a[112:115], v[124:127]\n"
"v_mfma_f32_16x16x32_bf16 v[128:131], a[56:59], a[120:123], v[128:131]\n"
"v_mfma_f32_16x16x32_bf16 v[128:131], a[60:63], a[124:127], v[128:131]\n"
"v_mfma_f32_16x16x32_bf16 v[100:103], a[52:55], a[124:127], v[100:103]\n"
"v_mfma_f32_16x16x32_bf16 v[100:103], a[48:51], a[120:123], v[100:103]\n"
"v_mfma_f32_16x16x32_bf16 v[104:107], a[48:51], a[128:131], v[104:107]\n"
"v_mfma_f32_16x16x32_bf16 v[104:107], a[52:55], a[132:135], v[104:107]\n"
"global_load_dwordx4 a[40:43], v0, s[34:35] offset:-2048 sc1\n"
"v_mfma_f32_16x16x32_bf16 v[132:135], a[60:63], a[132:135], v[132:135]\n"
"buffer_load_dwordx4 v0, s[48:51], s75 offen offset:2048 sc1 lds\n"
"v_mfma_f32_16x16x32_bf16 v[132:135], a[56:59], a[128:131], v[132:135]\n"
"v_mfma_f32_16x16x32_bf16 v[136:139], a[56:59], a[136:139], v[136:139]\n"
"v_mfma_f32_16x16x32_bf16 v[136:139], a[60:63], a[140:143], v[136:139]\n"
"v_mfma_f32_16x16x32_bf16 v[108:111], a[52:55], a[140:143], v[108:111]\n"
"v_mfma_f32_16x16x32_bf16 v[108:111], a[48:51], a[136:139], v[108:111]\n"
"v_mfma_f32_16x16x32_bf16 v[112:115], a[48:51], a[144:147], v[112:115]\n"
"v_mfma_f32_16x16x32_bf16 v[112:115], a[52:55], a[148:151], v[112:115]\n"
"global_load_dwordx4 a[44:47], v0, s[34:35] offset:-1024 sc1\n"
"v_mfma_f32_16x16x32_bf16 v[140:143], a[60:63], a[148:151], v[140:143]\n"
"v_mfma_f32_16x16x32_bf16 v[140:143], a[56:59], a[144:147], v[140:143]\n"
"v_mfma_f32_16x16x32_bf16 a[12:15], a[56:59], a[152:155], a[12:15]\n"
"v_mfma_f32_16x16x32_bf16 a[12:15], a[60:63], a[156:159], a[12:15]\n"
"v_mfma_f32_16x16x32_bf16 a[8:11], a[52:55], a[156:159], a[8:11]\n"
"v_mfma_f32_16x16x32_bf16 a[8:11], a[48:51], a[152:155], a[8:11]\n"
"s_waitcnt vmcnt(11)\n"
"v_mfma_f32_16x16x32_bf16 v[144:147], a[64:67], a[96:99], v[144:147]\n"
"buffer_load_dwordx4 v0, s[48:51], s75 offen offset:3072 sc1 lds\n"
"v_mfma_f32_16x16x32_bf16 v[144:147], a[68:71], a[100:103], v[144:147]\n"
"global_load_dwordx4 a[48:51], v0, s[34:35] offset:0 sc1\n"
"v_mfma_f32_16x16x32_bf16 v[172:175], a[76:79], a[100:103], v[172:175]\n"
"v_mfma_f32_16x16x32_bf16 v[172:175], a[72:75], a[96:99], v[172:175]\n"
"v_mfma_f32_16x16x32_bf16 v[176:179], a[72:75], a[104:107], v[176:179]\n"
"v_mfma_f32_16x16x32_bf16 v[176:179], a[76:79], a[108:111], v[176:179]\n"
"v_mfma_f32_16x16x32_bf16 v[148:151], a[68:71], a[108:111], v[148:151]\n"
"v_mfma_f32_16x16x32_bf16 v[148:151], a[64:67], a[104:107], v[148:151]\n"
"v_mfma_f32_16x16x32_bf16 v[152:155], a[64:67], a[112:115], v[152:155]\n"
"v_mfma_f32_16x16x32_bf16 v[152:155], a[68:71], a[116:119], v[152:155]\n"
"global_load_dwordx4 a[52:55], v0, s[34:35] offset:1024 sc1\n"
"v_mfma_f32_16x16x32_bf16 v[180:183], a[76:79], a[116:119], v[180:183]\n"
"v_mfma_f32_16x16x32_bf16 v[180:183], a[72:75], a[112:115], v[180:183]\n"
"v_mfma_f32_16x16x32_bf16 v[184:187], a[72:75], a[120:123], v[184:187]\n"
"v_mfma_f32_16x16x32_bf16 v[184:187], a[76:79], a[124:127], v[184:187]\n"
"v_mfma_f32_16x16x32_bf16 v[156:159], a[68:71], a[124:127], v[156:159]\n"
"v_mfma_f32_16x16x32_bf16 v[156:159], a[64:67], a[120:123], v[156:159]\n"
"s_add_u32 m0, s64, 4096\n"
"v_mfma_f32_16x16x32_bf16 v[160:163], a[64:67], a[128:131], v[160:163]\n"
"v_mfma_f32_16x16x32_bf16 v[160:163], a[68:71], a[132:135], v[160:163]\n"
"global_load_dwordx4 a[56:59], v0, s[34:35] offset:2048 sc1\n"
"v_mfma_f32_16x16x32_bf16 v[188:191], a[76:79], a[132:135], v[188:191]\n"
"v_mfma_f32_16x16x32_bf16 v[188:191], a[72:75], a[128:131], v[188:191]\n"
"v_mfma_f32_16x16x32_bf16 v[192:195], a[72:75], a[136:139], v[192:195]\n"
"v_mfma_f32_16x16x32_bf16 v[192:195], a[76:79], a[140:143], v[192:195]\n"
"v_mfma_f32_16x16x32_bf16 v[164:167], a[68:71], a[140:143], v[164:167]\n"
"v_mfma_f32_16x16x32_bf16 v[164:167], a[64:67], a[136:139], v[164:167]\n"
"v_mfma_f32_16x16x32_bf16 v[168:171], a[64:67], a[144:147], v[168:171]\n"
"v_mfma_f32_16x16x32_bf16 v[168:171], a[68:71], a[148:151], v[168:171]\n"
"global_load_dwordx4 a[60:63], v0, s[34:35] offset:3072 sc1\n"
"v_mfma_f32_16x16x32_bf16 v[196:199], a[76:79], a[148:151], v[196:199]\n"
"v_mfma_f32_16x16x32_bf16 v[196:199], a[72:75], a[144:147], v[196:199]\n"
"v_mfma_f32_16x16x32_bf16 a[20:23], a[72:75], a[152:155], a[20:23]\n"
"s_add_u32 s34, s34, 8192\n"
"v_mfma_f32_16x16x32_bf16 a[20:23], a[76:79], a[156:159], a[20:23]\n"
"v_mfma_f32_16x16x32_bf16 a[16:19], a[68:71], a[156:159], a[16:19]\n"
"buffer_load_dwordx4 v1, s[48:51], s75 offen offset:0 sc1 lds\n"
"v_mfma_f32_16x16x32_bf16 a[16:19], a[64:67], a[152:155], a[16:19]\n"
"s_waitcnt vmcnt(12)\n"
"v_mfma_f32_16x16x32_bf16 v[200:203], a[80:83], a[96:99], v[200:203]\n"
"v_mfma_f32_16x16x32_bf16 v[200:203], a[84:87], a[100:103], v[200:203]\n"
"global_load_dwordx4 a[64:67], v0, s[34:35] offset:-4096 sc1\n"
"s_add_u32 s65, s65, 1\n"
"v_mfma_f32_16x16x32_bf16 v[228:231], a[92:95], a[100:103], v[228:231]\n"
"ds_read_b128 a[100:103], v4 offset:17408\n"
"v_mfma_f32_16x16x32_bf16 v[228:231], a[88:91], a[96:99], v[228:231]\n"
"ds_read_b128 a[96:99], v4 offset:16384\n"
"v_mov_b32 v8, s65\n"
"v_mfma_f32_16x16x32_bf16 v[232:235], a[88:91], a[104:107], v[232:235]\n"
"v_mov_b32 v9, s60\n"
"v_mfma_f32_16x16x32_bf16 v[232:235], a[92:95], a[108:111], v[232:235]\n"
"v_mfma_f32_16x16x32_bf16 v[204:207], a[84:87], a[108:111], v[204:207]\n"
"buffer_store_dwordx2 v[8:9], off, s[56:59], %13 sc1\n"
"ds_read_b128 a[108:111], v4 offset:19456\n"
"v_mfma_f32_16x16x32_bf16 v[204:207], a[80:83], a[104:107], v[204:207]\n"
"ds_read_b128 a[104:107], v4 offset:18432\n"
"v_mfma_f32_16x16x32_bf16 v[208:211], a[80:83], a[112:115], v[208:211]\n"
"v_mfma_f32_16x16x32_bf16 v[208:211], a[84:87], a[116:119], v[208:211]\n"
"global_load_dwordx4 a[68:71], v0, s[34:35] offset:-3072 sc1\n"
"v_mfma_f32_16x16x32_bf16 v[236:239], a[92:95], a[116:119], v[236:239]\n"
"ds_read_b128 a[116:119], v4 offset:21504\n"
"v_mfma_f32_16x16x32_bf16 v[236:239], a[88:91], a[112:115], v[236:239]\n"
"ds_read_b128 a[112:115], v4 offset:20480\n"
"v_mfma_f32_16x16x32_bf16 v[240:243], a[88:91], a[120:123], v[240:243]\n"
"v_mfma_f32_16x16x32_bf16 v[240:243], a[92:95], a[124:127], v[240:243]\n"
"buffer_load_dwordx4 v1, s[48:51], s75 offen offset:1024 sc1 lds\n"
"v_mfma_f32_16x16x32_bf16 v[212:215], a[84:87], a[124:127], v[212:215]\n"
"ds_read_b128 a[124:127], v4 offset:23552\n"
"v_mfma_f32_16x16x32_bf16 v[212:215], a[80:83], a[120:123], v[212:215]\n"
"ds_read_b128 a[120:123], v4 offset:22528\n"
"v_mfma_f32_16x16x32_bf16 v[216:219], a[80:83], a[128:131], v[216:219]\n"
"v_mfma_f32_16x16x32_bf16 v[216:219], a[84:87], a[132:135], v[216:219]\n"
"global_load_dwordx4 a[72:75], v0, s[34:35] offset:-2048 sc1\n"
"v_mfma_f32_16x16x32_bf16 v[244:247], a[92:95], a[132:135], v[244:247]\n"
"ds_read_b128 a[132:135], v4 offset:25600\n"
"v_mfma_f32_16x16x32_bf16 v[244:247], a[88:91], a[128:131], v[244:247]\n"
"ds_read_b128 a[128:131], v4 offset:24576\n"
"v_mfma_f32_16x16x32_bf16 v[248:251], a[88:91], a[136:139], v[248:251]\n"
"v_mfma_f32_16x16x32_bf16 v[248:251], a[92:95], a[140:143], v[248:251]\n"
"v_mfma_f32_16x16x32_bf16 v[220:223], a[84:87], a[140:143], v[220:223]\n"
"ds_read_b128 a[140:143], v4 offset:27648\n"
"v_mfma_f32_16x16x32_bf16 v[220:223], a[80:83], a[136:139], v[220:223]\n"
"ds_read_b128 a[136:139], v4 offset:26624\n"
"v_mfma_f32_16x16x32_bf16 v[224:227], a[80:83], a[144:147], v[224:227]\n"
"v_mfma_f32_16x16x32_bf16 v[224:227], a[84:87], a[148:151], v[224:227]\n"
"global_load_dwordx4 a[76:79], v0, s[34:35] offset:-1024 sc1\n"
"v_mfma_f32_16x16x32_bf16 v[252:255], a[92:95], a[148:151], v[252:255]\n"
"ds_read_b128 a[148:151], v4 offset:29696\n"
"v_mfma_f32_16x16x32_bf16 v[252:255], a[88:91], a[144:147], v[252:255]\n"
"ds_read_b128 a[144:147], v4 offset:28672\n"
"v_mfma_f32_16x16x32_bf16 a[28:31], a[88:91], a[152:155], a[28:31]\n"
"buffer_load_dwordx4 v1, s[48:51], s75 offen offset:2048 sc1 lds\n"
"v_mfma_f32_16x16x32_bf16 a[28:31], a[92:95], a[156:159], a[28:31]\n"
"v_mfma_f32_16x16x32_bf16 a[24:27], a[84:87], a[156:159], a[24:27]\n"
"ds_read_b128 a[156:159], v4 offset:31744\n"
"v_mfma_f32_16x16x32_bf16 a[24:27], a[80:83], a[152:155], a[24:27]\n"
"s_waitcnt vmcnt(13) lgkmcnt(11)\n"
"ds_read_b128 a[152:155], v4 offset:30720\n"
"v_mfma_f32_16x16x32_bf16 v[32:35], a[32:35], a[96:99], v[32:35]\n"
"v_mfma_f32_16x16x32_bf16 v[32:35], a[36:39], a[100:103], v[32:35]\n"
"global_load_dwordx4 a[80:83], v0, s[34:35] offset:0 sc1\n"
"v_mfma_f32_16x16x32_bf16 v[60:63], a[44:47], a[100:103], v[60:63]\n"
"v_mfma_f32_16x16x32_bf16 v[60:63], a[40:43], a[96:99], v[60:63]\n"
"v_mfma_f32_16x16x32_bf16 v[64:67], a[40:43], a[104:107], v[64:67]\n"
"v_mfma_f32_16x16x32_bf16 v[64:67], a[44:47], a[108:111], v[64:67]\n"
"v_mfma_f32_16x16x32_bf16 v[36:39], a[36:39], a[108:111], v[36:39]\n"
"v_mfma_f32_16x16x32_bf16 v[36:39], a[32:35], a[104:107], v[36:39]\n"
"s_waitcnt lgkmcnt(8)\n"
"v_mfma_f32_16x16x32_bf16 v[40:43], a[32:35], a[112:115], v[40:43]\n"
"v_mfma_f32_16x16x32_bf16 v[40:43], a[36:39], a[116:119], v[40:43]\n"
"global_load_dwordx4 a[84:87], v0, s[34:35] offset:1024 sc1\n"
"v_mfma_f32_16x16x32_bf16 v[68:71], a[44:47], a[116:119], v[68:71]\n"
"buffer_load_dwordx4 v1, s[48:51], s75 offen offset:3072 sc1 lds\n"
"v_mfma_f32_16x16x32_bf16 v[68:71], a[40:43], a[112:115], v[68:71]\n"
"v_mfma_f32_16x16x32_bf16 v[72:75], a[40:43], a[120:123], v[72:75]\n"
"v_mfma_f32_16x16x32_bf16 v[72:75], a[44:47], a[124:127], v[72:75]\n"
"v_mfma_f32_16x16x32_bf16 v[44:47], a[36:39], a[124:127], v[44:47]\n"
"v_mfma_f32_16x16x32_bf16 v[44:47], a[32:35], a[120:123], v[44:47]\n"
"s_waitcnt lgkmcnt(4)\n"
"v_mfma_f32_16x16x32_bf16 v[48:51], a[32:35], a[128:131], v[48:51]\n"
"v_mfma_f32_16x16x32_bf16 v[48:51], a[36:39], a[132:135], v[48:51]\n"
"global_load_dwordx4 a[88:91], v0, s[34:35] offset:2048 sc1\n"
"v_mfma_f32_16x16x32_bf16 v[76:79], a[44:47], a[132:135], v[76:79]\n"
"v_mfma_f32_16x16x32_bf16 v[76:79], a[40:43], a[128:131], v[76:79]\n"
"v_mfma_f32_16x16x32_bf16 v[80:83], a[40:43], a[136:139], v[80:83]\n"
"v_mfma_f32_16x16x32_bf16 v[80:83], a[44:47], a[140:143], v[80:83]\n"
"v_mfma_f32_16x16x32_bf16 v[52:55], a[36:39], a[140:143], v[52:55]\n"
"v_mfma_f32_16x16x32_bf16 v[52:55], a[32:35], a[136:139], v[52:55]\n"
"s_waitcnt lgkmcnt(0)\n"
"v_mfma_f32_16x16x32_bf16 v[56:59], a[32:35], a[144:147], v[56:59]\n"
"v_mfma_f32_16x16x32_bf16 v[56:59], a[36:39], a[148:151], v[56:59]\n"
"s_add_u32 m0, s64, 8192\n"
"global_load_dwordx4 a[92:95], v0, s[34:35] offset:3072 sc1\n"
"v_mfma_f32_16x16x32_bf16 v[84:87], a[44:47], a[148:151], v[84:87]\n"
"v_mfma_f32_16x16x32_bf16 v[84:87], a[40:43], a[144:147], v[84:87]\n"
"v_mfma_f32_16x16x32_bf16 a[4:7], a[40:43], a[152:155], a[4:7]\n"
"s_add_u32 s34, s34, 8192\n"
"v_mfma_f32_16x16x32_bf16 a[4:7], a[44:47], a[156:159], a[4:7]\n"
"v_mfma_f32_16x16x32_bf16 a[0:3], a[36:39], a[156:159], a[0:3]\n"
"v_mfma_f32_16x16x32_bf16 a[0:3], a[32:35], a[152:155], a[0:3]\n"
"s_waitcnt vmcnt(13)\n"
"v_mfma_f32_16x16x32_bf16 v[88:91], a[48:51], a[96:99], v[88:91]\n"
"v_mfma_f32_16x16x32_bf16 v[88:91], a[52:55], a[100:103], v[88:91]\n"
"global_load_dwordx4 a[32:35], v0, s[34:35] offset:-4096 sc1\n"
"v_mfma_f32_16x16x32_bf16 v[116:119], a[60:63], a[100:103], v[116:119]\n"
"v_mfma_f32_16x16x32_bf16 v[116:119], a[56:59], a[96:99], v[116:119]\n"
"v_mfma_f32_16x16x32_bf16 v[120:123], a[56:59], a[104:107], v[120:123]\n"
"v_mfma_f32_16x16x32_bf16 v[120:123], a[60:63], a[108:111], v[120:123]\n"
"v_mfma_f32_16x16x32_bf16 v[92:95], a[52:55], a[108:111], v[92:95]\n"
"v_mfma_f32_16x16x32_bf16 v[92:95], a[48:51], a[104:107], v[92:95]\n"
"v_mfma_f32_16x16x32_bf16 v[96:99], a[48:51], a[112:115], v[96:99]\n"
"buffer_load_dwordx4 v0, s[48:51], s69 offen offset:0 sc1 lds\n"
"v_mfma_f32_16x16x32_bf16 v[96:99], a[52:55], a[116:119], v[96:99]\n"
"global_load_dwordx4 a[36:39], v0, s[34:35] offset:-3072 sc1\n"
"v_mfma_f32_16x16x32_bf16 v[124:127], a[60:63], a[116:119], v[124:127]\n"
"v_mfma_f32_16x16x32_bf16 v[124:127], a[56:59], a[112:115], v[124:127]\n"
"v_mfma_f32_16x16x32_bf16 v[128:131], a[56:59], a[120:123], v[128:131]\n"
"v_mfma_f32_16x16x32_bf16 v[128:131], a[60:63], a[124:127], v[128:131]\n"
"v_mfma_f32_16x16x32_bf16 v[100:103], a[52:55], a[124:127], v[100:103]\n"
"v_mfma_f32_16x16x32_bf16 v[100:103], a[48:51], a[120:123], v[100:103]\n"
"v_mfma_f32_16x16x32_bf16 v[104:107], a[48:51], a[128:131], v[104:107]\n"
"v_mfma_f32_16x16x32_bf16 v[104:107], a[52:55], a[132:135], v[104:107]\n"
"global_load_dwordx4 a[40:43], v0, s[34:35] offset:-2048 sc1\n"
"v_mfma_f32_16x16x32_bf16 v[132:135], a[60:63], a[132:135], v[132:135]\n"
"v_mfma_f32_16x16x32_bf16 v[132:135], a[56:59], a[128:131], v[132:135]\n"
"v_mfma_f32_16x16x32_bf16 v[136:139], a[56:59], a[136:139], v[136:139]\n"
"v_mfma_f32_16x16x32_bf16 v[136:139], a[60:63], a[140:143], v[136:139]\n"
"v_mfma_f32_16x16x32_bf16 v[108:111], a[52:55], a[140:143], v[108:111]\n"
"buffer_load_dwordx4 v0, s[48:51], s69 offen offset:1024 sc1 lds\n"
"v_mfma_f32_16x16x32_bf16 v[108:111], a[48:51], a[136:139], v[108:111]\n"
"v_mfma_f32_16x16x32_bf16 v[112:115], a[48:51], a[144:147], v[112:115]\n"
"v_mfma_f32_16x16x32_bf16 v[112:115], a[52:55], a[148:151], v[112:115]\n"
"global_load_dwordx4 a[44:47], v0, s[34:35] offset:-1024 sc1\n"
"v_mfma_f32_16x16x32_bf16 v[140:143], a[60:63], a[148:151], v[140:143]\n"
"v_mfma_f32_16x16x32_bf16 v[140:143], a[56:59], a[144:147], v[140:143]\n"
"v_mfma_f32_16x16x32_bf16 a[12:15], a[56:59], a[152:155], a[12:15]\n"
"v_mfma_f32_16x16x32_bf16 a[12:15], a[60:63], a[156:159], a[12:15]\n"
"v_mfma_f32_16x16x32_bf16 a[8:11], a[52:55], a[156:159], a[8:11]\n"
"v_mfma_f32_16x16x32_bf16 a[8:11], a[48:51], a[152:155], a[8:11]\n"
"s_waitcnt vmcnt(12)\n"
"v_mfma_f32_16x16x32_bf16 v[144:147], a[64:67], a[96:99], v[144:147]\n"
"v_mfma_f32_16x16x32_bf16 v[144:147], a[68:71], a[100:103], v[144:147]\n"
"global_load_dwordx4 a[48:51], v0, s[34:35] offset:0 sc1\n"
"v_mfma_f32_16x16x32_bf16 v[172:175], a[76:79], a[100:103], v[172:175]\n"
"v_mfma_f32_16x16x32_bf16 v[172:175], a[72:75], a[96:99], v[172:175]\n"
"v_mfma_f32_16x16x32_bf16 v[176:179], a[72:75], a[104:107], v[176:179]\n"
"v_mfma_f32_16x16x32_bf16 v[176:179], a[76:79], a[108:111], v[176:179]\n"
"buffer_load_dwordx4 v0, s[48:51], s69 offen offset:2048 sc1 lds\n"
"v_mfma_f32_16x16x32_bf16 v[148:151], a[68:71], a[108:111], v[148:151]\n"
"v_mfma_f32_16x16x32_bf16 v[148:151], a[64:67], a[104:107], v[148:151]\n"
"v_mfma_f32_16x16x32_bf16 v[152:155], a[64:67], a[112:115], v[152:155]\n"
"v_lshlrev_b32 v16, 16, v18\n"
"v_mfma_f32_16x16x32_bf16 v[152:155], a[68:71], a[116:119], v[152:155]\n"
"global_load_dwordx4 a[52:55], v0, s[34:35] offset:1024 sc1\n"
"v_and_b32 v17, s63, v18\n"
"v_mfma_f32_16x16x32_bf16 v[180:183], a[76:79], a[116:119], v[180:183]\n"
"v_lshlrev_b32 v18, 16, v19\n"
"v_mfma_f32_16x16x32_bf16 v[180:183], a[72:75], a[112:115], v[180:183]\n"
"v_and_b32 v19, s63, v19\n"
"v_mfma_f32_16x16x32_bf16 v[184:187], a[72:75], a[120:123], v[184:187]\n"
"v_add_f32 v32, v32, v16\n"
"v_mfma_f32_16x16x32_bf16 v[184:187], a[76:79], a[124:127], v[184:187]\n"
"v_add_f32 v33, v33, v17\n"
"v_add_f32 v34, v34, v18\n"
"v_mfma_f32_16x16x32_bf16 v[156:159], a[68:71], a[124:127], v[156:159]\n"
"v_add_f32 v35, v35, v19\n"
"v_mfma_f32_16x16x32_bf16 v[156:159], a[64:67], a[120:123], v[156:159]\n"
"v_add_f32 v36, v36, v16\n"
"v_mfma_f32_16x16x32_bf16 v[160:163], a[64:67], a[128:131], v[160:163]\n"
"v_add_f32 v37, v37, v17\n"
"v_mfma_f32_16x16x32_bf16 v[160:163], a[68:71], a[132:135], v[160:163]\n"
"global_load_dwordx4 a[56:59], v0, s[34:35] offset:2048 sc1\n"
"v_add_f32 v38, v38, v18\n"
"v_mfma_f32_16x16x32_bf16 v[188:191], a[76:79], a[132:135], v[188:191]\n"
"v_add_f32 v39, v39, v19\n"
"v_mfma_f32_16x16x32_bf16 v[188:191], a[72:75], a[128:131], v[188:191]\n"
"v_add_f32 v40, v40, v16\n"
"v_add_f32 v41, v41, v17\n"
"v_mfma_f32_16x16x32_bf16 v[192:195], a[72:75], a[136:139], v[192:195]\n"
"buffer_load_dwordx4 v0, s[48:51], s69 offen offset:3072 sc1 lds\n"
"v_mfma_f32_16x16x32_bf16 v[192:195], a[76:79], a[140:143], v[192:195]\n"
"v_add_f32 v42, v42, v18\n"
"v_mfma_f32_16x16x32_bf16 v[164:167], a[68:71], a[140:143], v[164:167]\n"
"v_add_f32 v43, v43, v19\n"
"v_mfma_f32_16x16x32_bf16 v[164:167], a[64:67], a[136:139], v[164:167]\n"
"v_add_f32 v44, v44, v16\n"
"v_add_f32 v45, v45, v17\n"
"v_mfma_f32_16x16x32_bf16 v[168:171], a[64:67], a[144:147], v[168:171]\n"
"v_add_f32 v46, v46, v18\n"
"v_mfma_f32_16x16x32_bf16 v[168:171], a[68:71], a[148:151], v[168:171]\n"
"global_load_dwordx4 a[60:63], v0, s[34:35] offset:3072 sc1\n"
"v_add_f32 v47, v47, v19\n"
"v_mfma_f32_16x16x32_bf16 v[196:199], a[76:79], a[148:151], v[196:199]\n"
"v_add_f32 v48, v48, v16\n"
"v_mfma_f32_16x16x32_bf16 v[196:199], a[72:75], a[144:147], v[196:199]\n"
"v_add_f32 v49, v49, v17\n"
"v_mfma_f32_16x16x32_bf16 a[20:23], a[72:75], a[152:155], a[20:23]\n"
"s_add_u32 s34, s34, 8192\n"
"v_add_f32 v50, v50, v18\n"
"v_mfma_f32_16x16x32_bf16 a[20:23], a[76:79], a[156:159], a[20:23]\n"
"v_add_f32 v51, v51, v19\n"
"v_mfma_f32_16x16x32_bf16 a[16:19], a[68:71], a[156:159], a[16:19]\n"
"v_add_f32 v52, v52, v16\n"
"v_mfma_f32_16x16x32_bf16 a[16:19], a[64:67], a[152:155], a[16:19]\n"
"s_waitcnt vmcnt(12)\n"
"v_add_f32 v53, v53, v17\n"
"v_mfma_f32_16x16x32_bf16 v[200:203], a[80:83], a[96:99], v[200:203]\n"
"v_add_f32 v54, v54, v18\n"
"v_add_f32 v55, v55, v19\n"
"v_mfma_f32_16x16x32_bf16 v[200:203], a[84:87], a[100:103], v[200:203]\n"
"global_load_dwordx4 a[64:67], v0, s[34:35] offset:-4096 sc1\n"
"v_add_f32 v56, v56, v16\n"
"v_mfma_f32_16x16x32_bf16 v[228:231], a[92:95], a[100:103], v[228:231]\n"
"s_add_u32 m0, s64, 12288\n"
"ds_read_b128 a[100:103], v5 offset:17408\n"
"v_add_f32 v57, v57, v17\n"
"v_mfma_f32_16x16x32_bf16 v[228:231], a[88:91], a[96:99], v[228:231]\n"
"ds_read_b128 a[96:99], v5 offset:16384\n"
"v_add_f32 v58, v58, v18\n"
"v_mfma_f32_16x16x32_bf16 v[232:235], a[88:91], a[104:107], v[232:235]\n"
"v_add_f32 v59, v59, v19\n"
"v_mfma_f32_16x16x32_bf16 v[232:235], a[92:95], a[108:111], v[232:235]\n"
"v_accvgpr_read_b32 v12, a0\n"
"v_mfma_f32_16x16x32_bf16 v[204:207], a[84:87], a[108:111], v[204:207]\n"
"ds_read_b128 a[108:111], v5 offset:19456\n"
"v_accvgpr_read_b32 v13, a1\n"
"v_mfma_f32_16x16x32_bf16 v[204:207], a[80:83], a[104:107], v[204:207]\n"
"ds_read_b128 a[104:107], v5 offset:18432\n"
"v_accvgpr_read_b32 v14, a2\n"
"v_mfma_f32_16x16x32_bf16 v[208:211], a[80:83], a[112:115], v[208:211]\n"
"v_accvgpr_read_b32 v15, a3\n"
"v_mfma_f32_16x16x32_bf16 v[208:211], a[84:87], a[116:119], v[208:211]\n"
"global_load_dwordx4 a[68:71], v0, s[34:35] offset:-3072 sc1\n"
"v_add_f32 v12, v12, v16\n"
"v_mfma_f32_16x16x32_bf16 v[236:239], a[92:95], a[116:119], v[236:239]\n"
"ds_read_b128 a[116:119], v5 offset:21504\n"
"v_add_f32 v13, v13, v17\n"
"v_mfma_f32_16x16x32_bf16 v[236:239], a[88:91], a[112:115], v[236:239]\n"
"ds_read_b128 a[112:115], v5 offset:20480\n"
"v_add_f32 v14, v14, v18\n"
"v_mfma_f32_16x16x32_bf16 v[240:243], a[88:91], a[120:123], v[240:243]\n"
"v_add_f32 v15, v15, v19\n"
"v_accvgpr_write_b32 a0, v12\n"
"v_mfma_f32_16x16x32_bf16 v[240:243], a[92:95], a[124:127], v[240:243]\n"
"v_accvgpr_write_b32 a1, v13\n"
"v_mfma_f32_16x16x32_bf16 v[212:215], a[84:87], a[124:127], v[212:215]\n"
"ds_read_b128 a[124:127], v5 offset:23552\n"
"v_accvgpr_write_b32 a2, v14\n"
"v_mfma_f32_16x16x32_bf16 v[212:215], a[80:83], a[120:123], v[212:215]\n"
"ds_read_b128 a[120:123], v5 offset:22528\n"
"v_accvgpr_write_b32 a3, v15\n"
"v_mfma_f32_16x16x32_bf16 v[216:219], a[80:83], a[128:131], v[216:219]\n"
"buffer_load_dwordx4 v1, s[48:51], s69 offen offset:0 sc1 lds\n"
"v_mfma_f32_16x16x32_bf16 v[216:219], a[84:87], a[132:135], v[216:219]\n"
"global_load_dwordx4 a[72:75], v0, s[34:35] offset:-2048 sc1\n"
"v_lshrrev_b32 v16, 4, v0\n"
"v_mfma_f32_16x16x32_bf16 v[244:247], a[92:95], a[132:135], v[244:247]\n"
"ds_read_b128 a[132:135], v5 offset:25600\n"
"v_and_b32 v16, 0x30, v16\n"
"v_mfma_f32_16x16x32_bf16 v[244:247], a[88:91], a[128:131], v[244:247]\n"
"ds_read_b128 a[128:131], v5 offset:24576\n"
"buffer_load_dwordx2 v[18:19], v16, s[44:47], s61 offen offset:64\n"
"v_mfma_f32_16x16x32_bf16 v[248:251], a[88:91], a[136:139], v[248:251]\n"
"s_getpc_b64 s[96:97]\n"
"L_c3_:\n"
"s_add_u32 s96, s96, L_last-L_c3_\n"
"s_addc_u32 s97, s97, 0\n"
"v_mfma_f32_16x16x32_bf16 v[248:251], a[92:95], a[140:143], v[248:251]\n"
"s_lshr_b32 s66, %26, 3\n"
"v_mfma_f32_16x16x32_bf16 v[220:223], a[84:87], a[140:143], v[220:223]\n"
"s_lshl2_add_u32 s66, s66, %16\n"
"ds_read_b128 a[140:143], v5 offset:27648\n"
"v_mfma_f32_16x16x32_bf16 v[220:223], a[80:83], a[136:139], v[220:223]\n"
"ds_read_b128 a[136:139], v5 offset:26624\n"
"v_mfma_f32_16x16x32_bf16 v[224:227], a[80:83], a[144:147], v[224:227]\n"
"s_mul_i32 s66, s66, 256\n"
"v_mfma_f32_16x16x32_bf16 v[224:227], a[84:87], a[148:151], v[224:227]\n"
"global_load_dwordx4 a[76:79], v0, s[34:35] offset:-1024 sc1\n"
"v_mfma_f32_16x16x32_bf16 v[252:255], a[92:95], a[148:151], v[252:255]\n"
"ds_read_b128 a[148:151], v5 offset:29696\n"
"v_mfma_f32_16x16x32_bf16 v[252:255], a[88:91], a[144:147], v[252:255]\n"
"s_min_u32 s66, s66, L_headF-L_last-256\n"
"ds_read_b128 a[144:147], v5 offset:28672\n"
"v_mfma_f32_16x16x32_bf16 a[28:31], a[88:91], a[152:155], a[28:31]\n"
"v_mfma_f32_16x16x32_bf16 a[28:31], a[92:95], a[156:159], a[28:31]\n"
"v_min_u32 v11, 16, v0\n"
"v_mfma_f32_16x16x32_bf16 a[24:27], a[84:87], a[156:159], a[24:27]\n"
"ds_read_b128 a[156:159], v5 offset:31744\n"
"v_mfma_f32_16x16x32_bf16 a[24:27], a[80:83], a[152:155], a[24:27]\n"
"s_waitcnt vmcnt(12) lgkmcnt(11)\n"
"ds_read_b128 a[152:155], v5 offset:30720\n"
"v_mfma_f32_16x16x32_bf16 v[32:35], a[32:35], a[96:99], v[32:35]\n"
"buffer_load_dwordx4 v1, s[48:51], s69 offen offset:1024 sc1 lds\n"
"v_mfma_f32_16x16x32_bf16 v[32:35], a[36:39], a[100:103], v[32:35]\n"
"global_load_dwordx4 a[80:83], v0, s[34:35] offset:0 sc1\n"
"v_mfma_f32_16x16x32_bf16 v[60:63], a[44:47], a[100:103], v[60:63]\n"
"v_lshl_add_u32 v11, v11, 3, s66\n"
"v_mfma_f32_16x16x32_bf16 v[60:63], a[40:43], a[96:99], v[60:63]\n"
"global_load_dword v10, v11, s[96:97]\n"
"v_mfma_f32_16x16x32_bf16 v[64:67], a[40:43], a[104:107], v[64:67]\n"
"v_mfma_f32_16x16x32_bf16 v[64:67], a[44:47], a[108:111], v[64:67]\n"
"v_mfma_f32_16x16x32_bf16 v[36:39], a[36:39], a[108:111], v[36:39]\n"
"v_mfma_f32_16x16x32_bf16 v[36:39], a[32:35], a[104:107], v[36:39]\n"
"s_waitcnt lgkmcnt(8)\n"
"v_mfma_f32_16x16x32_bf16 v[40:43], a[32:35], a[112:115], v[40:43]\n"
"v_mfma_f32_16x16x32_bf16 v[40:43], a[36:39], a[116:119], v[40:43]\n"
"global_load_dwordx4 a[84:87], v0, s[34:35] offset:1024 sc1\n"
"v_mfma_f32_16x16x32_bf16 v[68:71], a[44:47], a[116:119], v[68:71]\n"
"v_mfma_f32_16x16x32_bf16 v[68:71], a[40:43], a[112:115], v[68:71]\n"
"v_mfma_f32_16x16x32_bf16 v[72:75], a[40:43], a[120:123], v[72:75]\n"
"v_mfma_f32_16x16x32_bf16 v[72:75], a[44:47], a[124:127], v[72:75]\n"
"v_mfma_f32_16x16x32_bf16 v[44:47], a[36:39], a[124:127], v[44:47]\n"
"buffer_load_dwordx4 v1, s[48:51], s69 offen offset:2048 sc1 lds\n"
"v_mfma_f32_16x16x32_bf16 v[44:47], a[32:35], a[120:123], v[44:47]\n"
"s_waitcnt lgkmcnt(4)\n"
"v_mfma_f32_16x16x32_bf16 v[48:51], a[32:35], a[128:131], v[48:51]\n"
"v_mfma_f32_16x16x32_bf16 v[48:51], a[36:39], a[132:135], v[48:51]\n"
"global_load_dwordx4 a[88:91], v0, s[34:35] offset:2048 sc1\n"
"v_mfma_f32_16x16x32_bf16 v[76:79], a[44:47], a[132:135], v[76:79]\n"
"v_mfma_f32_16x16x32_bf16 v[76:79], a[40:43], a[128:131], v[76:79]\n"
"v_mfma_f32_16x16x32_bf16 v[80:83], a[40:43], a[136:139], v[80:83]\n"
"v_mfma_f32_16x16x32_bf16 v[80:83], a[44:47], a[140:143], v[80:83]\n"
"v_mfma_f32_16x16x32_bf16 v[52:55], a[36:39], a[140:143], v[52:55]\n"
"v_mfma_f32_16x16x32_bf16 v[52:55], a[32:35], a[136:139], v[52:55]\n"
"s_waitcnt lgkmcnt(0)\n"
"v_mfma_f32_16x16x32_bf16 v[56:59], a[32:35], a[144:147], v[56:59]\n"
"v_mfma_f32_16x16x32_bf16 v[56:59], a[36:39], a[148:151], v[56:59]\n"
"global_load_dwordx4 a[92:95], v0, s[34:35] offset:3072 sc1\n"
"v_mfma_f32_16x16x32_bf16 v[84:87], a[44:47], a[148:151], v[84:87]\n"
"v_mfma_f32_16x16x32_bf16 v[84:87], a[40:43], a[144:147], v[84:87]\n"
"v_mfma_f32_16x16x32_bf16 a[4:7], a[40:43], a[152:155], a[4:7]\n"
"s_add_u32 s34, s34, 8192\n"
"v_mfma_f32_16x16x32_bf16 a[4:7], a[44:47], a[156:159], a[4:7]\n"
"buffer_load_dwordx4 v1, s[48:51], s69 offen offset:3072 sc1 lds\n"
"v_mfma_f32_16x16x32_bf16 a[0:3], a[36:39], a[156:159], a[0:3]\n"
"v_mfma_f32_16x16x32_bf16 a[0:3], a[32:35], a[152:155], a[0:3]\n"
"s_waitcnt vmcnt(14)\n"
"v_mfma_f32_16x16x32_bf16 v[88:91], a[48:51], a[96:99], v[88:91]\n"
"v_mfma_f32_16x16x32_bf16 v[88:91], a[52:55], a[100:103], v[88:91]\n"
"global_load_dwordx4 a[32:35], v0, s[34:35] offset:-4096 sc1\n"
"v_mfma_f32_16x16x32_bf16 v[116:119], a[60:63], a[100:103], v[116:119]\n"
"v_mfma_f32_16x16x32_bf16 v[116:119], a[56:59], a[96:99], v[116:119]\n"
"v_mfma_f32_16x16x32_bf16 v[120:123], a[56:59], a[104:107], v[120:123]\n"
"v_mfma_f32_16x16x32_bf16 v[120:123], a[60:63], a[108:111], v[120:123]\n"
"v_mfma_f32_16x16x32_bf16 v[92:95], a[52:55], a[108:111], v[92:95]\n"
"v_mfma_f32_16x16x32_bf16 v[92:95], a[48:51], a[104:107], v[92:95]\n"
"v_mfma_f32_16x16x32_bf16 v[96:99], a[48:51], a[112:115], v[96:99]\n"
"v_mfma_f32_16x16x32_bf16 v[96:99], a[52:55], a[116:119], v[96:99]\n"
"global_load_dwordx4 a[36:39], v0, s[34:35] offset:-3072 sc1\n"
"v_mfma_f32_16x16x32_bf16 v[124:127], a[60:63], a[116:119], v[124:127]\n"
"v_mfma_f32_16x16x32_bf16 v[124:127], a[56:59], a[112:115], v[124:127]\n"
"v_mfma_f32_16x16x32_bf16 v[128:131], a[56:59], a[120:123], v[128:131]\n"
"v_mfma_f32_16x16x32_bf16 v[128:131], a[60:63], a[124:127], v[128:131]\n"
"v_mfma_f32_16x16x32_bf16 v[100:103], a[52:55], a[124:127], v[100:103]\n"
"v_mfma_f32_16x16x32_bf16 v[100:103], a[48:51], a[120:123], v[100:103]\n"
"v_mfma_f32_16x16x32_bf16 v[104:107], a[48:51], a[128:131], v[104:107]\n"
"buffer_load_dwordx2 v[8:9], off, s[56:59], %14 sc1\n"
"v_mfma_f32_16x16x32_bf16 v[104:107], a[52:55], a[132:135], v[104:107]\n"
"global_load_dwordx4 a[40:43], v0, s[34:35] offset:-2048 sc1\n"
"v_mfma_f32_16x16x32_bf16 v[132:135], a[60:63], a[132:135], v[132:135]\n"
"v_mfma_f32_16x16x32_bf16 v[132:135], a[56:59], a[128:131], v[132:135]\n"
"v_mfma_f32_16x16x32_bf16 v[136:139], a[56:59], a[136:139], v[136:139]\n"
"v_mfma_f32_16x16x32_bf16 v[136:139], a[60:63], a[140:143], v[136:139]\n"
"v_mfma_f32_16x16x32_bf16 v[108:111], a[52:55], a[140:143], v[108:111]\n"
"v_mfma_f32_16x16x32_bf16 v[108:111], a[48:51], a[136:139], v[108:111]\n"
"v_mfma_f32_16x16x32_bf16 v[112:115], a[48:51], a[144:147], v[112:115]\n"
"v_mfma_f32_16x16x32_bf16 v[112:115], a[52:55], a[148:151], v[112:115]\n"
"global_load_dwordx4 a[44:47], v0, s[34:35] offset:-1024 sc1\n"
"v_mfma_f32_16x16x32_bf16 v[140:143], a[60:63], a[148:151], v[140:143]\n"
"v_mfma_f32_16x16x32_bf16 v[140:143], a[56:59], a[144:147], v[140:143]\n"
"v_mfma_f32_16x16x32_bf16 a[12:15], a[56:59], a[152:155], a[12:15]\n"
"v_mfma_f32_16x16x32_bf16 a[12:15], a[60:63], a[156:159], a[12:15]\n"
"v_mfma_f32_16x16x32_bf16 a[8:11], a[52:55], a[156:159], a[8:11]\n"
"v_mfma_f32_16x16x32_bf16 a[8:11], a[48:51], a[152:155], a[8:11]\n"
"s_waitcnt vmcnt(13)\n"
"v_mfma_f32_16x16x32_bf16 v[144:147], a[64:67], a[96:99], v[144:147]\n"
"L_st6780:\n"
"s_cbranch_execz L_st9078\n"
"v_mfma_f32_16x16x32_bf16 v[144:147], a[68:71], a[100:103], v[144:147]\n"
"global_load_dwordx4 a[48:51], v0, s[34:35] offset:0 sc1\n"
"v_mfma_f32_16x16x32_bf16 v[172:175], a[76:79], a[100:103], v[172:175]\n"
"v_mfma_f32_16x16x32_bf16 v[172:175], a[72:75], a[96:99], v[172:175]\n"
"v_mfma_f32_16x16x32_bf16 v[176:179], a[72:75], a[104:107], v[176:179]\n"
"v_mfma_f32_16x16x32_bf16 v[176:179], a[76:79], a[108:111], v[176:179]\n"
"v_mfma_f32_16x16x32_bf16 v[148:151], a[68:71], a[108:111], v[148:151]\n"
"v_mfma_f32_16x16x32_bf16 v[148:151], a[64:67], a[104:107], v[148:151]\n"
"v_mfma_f32_16x16x32_bf16 v[152:155], a[64:67], a[112:115], v[152:155]\n"
"v_lshlrev_b32 v20, 16, v22\n"
"v_mfma_f32_16x16x32_bf16 v[152:155], a[68:71], a[116:119], v[152:155]\n"
"global_load_dwordx4 a[52:55], v0, s[34:35] offset:1024 sc1\n"
"v_and_b32 v21, s63, v22\n"
"v_mfma_f32_16x16x32_bf16 v[180:183], a[76:79], a[116:119], v[180:183]\n"
"v_lshlrev_b32 v22, 16, v23\n"
"v_mfma_f32_16x16x32_bf16 v[180:183], a[72:75], a[112:115], v[180:183]\n"
"v_and_b32 v23, s63, v23\n"
"v_mfma_f32_16x16x32_bf16 v[184:187], a[72:75], a[120:123], v[184:187]\n"
"v_add_f32 v60, v60, v20\n"
"v_mfma_f32_16x16x32_bf16 v[184:187], a[76:79], a[124:127], v[184:187]\n"
"v_add_f32 v61, v61, v21\n"
"v_add_f32 v62, v62, v22\n"
"v_mfma_f32_16x16x32_bf16 v[156:159], a[68:71], a[124:127], v[156:159]\n"
"v_add_f32 v63, v63, v23\n"
"v_mfma_f32_16x16x32_bf16 v[156:159], a[64:67], a[120:123], v[156:159]\n"
"v_add_f32 v64, v64, v20\n"
"v_mfma_f32_16x16x32_bf16 v[160:163], a[64:67], a[128:131], v[160:163]\n"
"v_add_f32 v65, v65, v21\n"
"v_mfma_f32_16x16x32_bf16 v[160:163], a[68:71], a[132:135], v[160:163]\n"
"global_load_dwordx4 a[56:59], v0, s[34:35] offset:2048 sc1\n"
"v_add_f32 v66, v66, v22\n"
"v_mfma_f32_16x16x32_bf16 v[188:191], a[76:79], a[132:135], v[188:191]\n"
"v_add_f32 v67, v67, v23\n"
"v_mfma_f32_16x16x32_bf16 v[188:191], a[72:75], a[128:131], v[188:191]\n"
"v_add_f32 v68, v68, v20\n"
"v_mfma_f32_16x16x32_bf16 v[192:195], a[72:75], a[136:139], v[192:195]\n"
"v_add_f32 v69, v69, v21\n"
"v_mfma_f32_16x16x32_bf16 v[192:195], a[76:79], a[140:143], v[192:195]\n"
"v_add_f32 v70, v70, v22\n"
"v_mfma_f32_16x16x32_bf16 v[164:167], a[68:71], a[140:143], v[164:167]\n"
"v_add_f32 v71, v71, v23\n"
"v_mfma_f32_16x16x32_bf16 v[164:167], a[64:67], a[136:139], v[164:167]\n"
"v_add_f32 v72, v72, v20\n"
"v_add_f32 v73, v73, v21\n"
"v_mfma_f32_16x16x32_bf16 v[168:171], a[64:67], a[144:147], v[168:171]\n"
"v_add_f32 v74, v74, v22\n"
"v_mfma_f32_16x16x32_bf16 v[168:171], a[68:71], a[148:151], v[168:171]\n"
"global_load_dwordx4 a[60:63], v0, s[34:35] offset:3072 sc1\n"
"v_add_f32 v75, v75, v23\n"
"v_mfma_f32_16x16x32_bf16 v[196:199], a[76:79], a[148:151], v[196:199]\n"
"v_add_f32 v76, v76, v20\n"
"v_mfma_f32_16x16x32_bf16 v[196:199], a[72:75], a[144:147], v[196:199]\n"
"v_add_f32 v77, v77, v21\n"
"v_mfma_f32_16x16x32_bf16 a[20:23], a[72:75], a[152:155], a[20:23]\n"
"s_add_u32 s34, s34, 8192\n"
"v_add_f32 v78, v78, v22\n"
"v_mfma_f32_16x16x32_bf16 a[20:23], a[76:79], a[156:159], a[20:23]\n"
"v_add_f32 v79, v79, v23\n"
"v_mfma_f32_16x16x32_bf16 a[16:19], a[68:71], a[156:159], a[16:19]\n"
"v_add_f32 v80, v80, v20\n"
"v_mfma_f32_16x16x32_bf16 a[16:19], a[64:67], a[152:155], a[16:19]\n"
"s_waitcnt vmcnt(10)\n"
"v_add_f32 v81, v81, v21\n"
"v_mfma_f32_16x16x32_bf16 v[200:203], a[80:83], a[96:99], v[200:203]\n"
"v_add_f32 v82, v82, v22\n"
"v_add_f32 v83, v83, v23\n"
"v_mfma_f32_16x16x32_bf16 v[200:203], a[84:87], a[100:103], v[200:203]\n"
"global_load_dwordx4 a[64:67], v0, s[34:35] offset:-4096 sc1\n"
"v_add_f32 v84, v84, v20\n"
"v_mfma_f32_16x16x32_bf16 v[228:231], a[92:95], a[100:103], v[228:231]\n"
"s_waitcnt vmcnt(10)\n"
"ds_read_b128 a[100:103], v2 offset:1024\n"
"v_add_f32 v85, v85, v21\n"
"v_mfma_f32_16x16x32_bf16 v[228:231], a[88:91], a[96:99], v[228:231]\n"
"ds_read_b128 a[96:99], v2 offset:0\n"
"v_add_f32 v86, v86, v22\n"
"v_mfma_f32_16x16x32_bf16 v[232:235], a[88:91], a[104:107], v[232:235]\n"
"v_add_f32 v87, v87, v23\n"
"v_mfma_f32_16x16x32_bf16 v[232:235], a[92:95], a[108:111], v[232:235]\n"
"v_accvgpr_read_b32 v12, a4\n"
"v_mfma_f32_16x16x32_bf16 v[204:207], a[84:87], a[108:111], v[204:207]\n"
"ds_read_b128 a[108:111], v2 offset:3072\n"
"v_accvgpr_read_b32 v13, a5\n"
"v_mfma_f32_16x16x32_bf16 v[204:207], a[80:83], a[104:107], v[204:207]\n"
"ds_read_b128 a[104:107], v2 offset:2048\n"
"v_accvgpr_read_b32 v14, a6\n"
"v_mfma_f32_16x16x32_bf16 v[208:211], a[80:83], a[112:115], v[208:211]\n"
"v_accvgpr_read_b32 v15, a7\n"
"v_mfma_f32_16x16x32_bf16 v[208:211], a[84:87], a[116:119], v[208:211]\n"
"global_load_dwordx4 a[68:71], v0, s[34:35] offset:-3072 sc1\n"
"v_add_f32 v12, v12, v20\n"
"v_mfma_f32_16x16x32_bf16 v[236:239], a[92:95], a[116:119], v[236:239]\n"
"ds_read_b128 a[116:119], v2 offset:5120\n"
"v_add_f32 v13, v13, v21\n"
"v_mfma_f32_16x16x32_bf16 v[236:239], a[88:91], a[112:115], v[236:239]\n"
"ds_read_b128 a[112:115], v2 offset:4096\n"
"v_add_f32 v14, v14, v22\n"
"v_mfma_f32_16x16x32_bf16 v[240:243], a[88:91], a[120:123], v[240:243]\n"
"v_add_f32 v15, v15, v23\n"
"v_accvgpr_write_b32 a4, v12\n"
"v_mfma_f32_16x16x32_bf16 v[240:243], a[92:95], a[124:127], v[240:243]\n"
"v_accvgpr_write_b32 a5, v13\n"
"v_mfma_f32_16x16x32_bf16 v[212:215], a[84:87], a[124:127], v[212:215]\n"
"ds_read_b128 a[124:127], v2 offset:7168\n"
"v_accvgpr_write_b32 a6, v14\n"
"v_mfma_f32_16x16x32_bf16 v[212:215], a[80:83], a[120:123], v[212:215]\n"
"ds_read_b128 a[120:123], v2 offset:6144\n"
"v_accvgpr_write_b32 a7, v15\n"
"v_mfma_f32_16x16x32_bf16 v[216:219], a[80:83], a[128:131], v[216:219]\n"
"v_mfma_f32_16x16x32_bf16 v[216:219], a[84:87], a[132:135], v[216:219]\n"
"global_load_dwordx4 a[72:75], v0, s[34:35] offset:-2048 sc1\n"
"v_lshrrev_b32 v20, 4, v0\n"
"v_mfma_f32_16x16x32_bf16 v[244:247], a[92:95], a[132:135], v[244:247]\n"
"ds_read_b128 a[132:135], v2 offset:9216\n"
"v_and_b32 v20, 0x30, v20\n"
"v_mfma_f32_16x16x32_bf16 v[244:247], a[88:91], a[128:131], v[244:247]\n"
"ds_read_b128 a[128:131], v2 offset:8192\n"
"buffer_load_dwordx2 v[22:23], v20, s[44:47], s61 offen offset:72\n"
"v_mfma_f32_16x16x32_bf16 v[248:251], a[88:91], a[136:139], v[248:251]\n"
"v_mfma_f32_16x16x32_bf16 v[248:251], a[92:95], a[140:143], v[248:251]\n"
"v_mfma_f32_16x16x32_bf16 v[220:223], a[84:87], a[140:143], v[220:223]\n"
"ds_read_b128 a[140:143], v2 offset:11264\n"
"v_mfma_f32_16x16x32_bf16 v[220:223], a[80:83], a[136:139], v[220:223]\n"
"ds_read_b128 a[136:139], v2 offset:10240\n"
"v_mfma_f32_16x16x32_bf16 v[224:227], a[80:83], a[144:147], v[224:227]\n"
"v_mfma_f32_16x16x32_bf16 v[224:227], a[84:87], a[148:151], v[224:227]\n"
"global_load_dwordx4 a[76:79], v0, s[34:35] offset:-1024 sc1\n"
"v_mfma_f32_16x16x32_bf16 v[252:255], a[92:95], a[148:151], v[252:255]\n"
"ds_read_b128 a[148:151], v2 offset:13312\n"
"v_mfma_f32_16x16x32_bf16 v[252:255], a[88:91], a[144:147], v[252:255]\n"
"ds_read_b128 a[144:147], v2 offset:12288\n"
"v_mfma_f32_16x16x32_bf16 a[28:31], a[88:91], a[152:155], a[28:31]\n"
"v_mfma_f32_16x16x32_bf16 a[28:31], a[92:95], a[156:159], a[28:31]\n"
"v_mfma_f32_16x16x32_bf16 a[24:27], a[84:87], a[156:159], a[24:27]\n"
"s_cmp_eq_u32 s62, 5\n"
"ds_read_b128 a[156:159], v2 offset:15360\n"
"v_mfma_f32_16x16x32_bf16 a[24:27], a[80:83], a[152:155], a[24:27]\n"
"ds_read_b128 a[152:155], v2 offset:14336\n"
"s_cbranch_scc1 L_last\n"
"L_P4:\n"
"s_waitcnt vmcnt(9) lgkmcnt(12)\n"
"v_mfma_f32_16x16x32_bf16 v[32:35], a[32:35], a[96:99], v[32:35]\n"
"s_add_u32 s48, s48, 131072\n"
"s_addc_u32 s49, s49, 0\n"
"v_mfma_f32_16x16x32_bf16 v[32:35], a[36:39], a[100:103], v[32:35]\n"
"global_load_dwordx4 a[80:83], v0, s[34:35] offset:0 sc1\n"
"v_mfma_f32_16x16x32_bf16 v[60:63], a[44:47], a[100:103], v[60:63]\n"
"s_barrier\n"
"v_mfma_f32_16x16x32_bf16 v[60:63], a[40:43], a[96:99], v[60:63]\n"
"v_mfma_f32_16x16x32_bf16 v[64:67], a[40:43], a[104:107], v[64:67]\n"
"v_mfma_f32_16x16x32_bf16 v[64:67], a[44:47], a[108:111], v[64:67]\n"
"v_mfma_f32_16x16x32_bf16 v[36:39], a[36:39], a[108:111], v[36:39]\n"
"v_subrev_u32 v8, s65, v8\n"
"v_mfma_f32_16x16x32_bf16 v[36:39], a[32:35], a[104:107], v[36:39]\n"
"s_waitcnt lgkmcnt(8)\n"
"v_xor_b32 v9, s79, v9\n"
"v_mfma_f32_16x16x32_bf16 v[40:43], a[32:35], a[112:115], v[40:43]\n"
"v_or_b32 v8, v8, v9\n"
"v_mfma_f32_16x16x32_bf16 v[40:43], a[36:39], a[116:119], v[40:43]\n"
"global_load_dwordx4 a[84:87], v0, s[34:35] offset:1024 sc1\n"
"v_cmp_lt_u32 vcc, 15, v8\n"
"v_mfma_f32_16x16x32_bf16 v[68:71], a[44:47], a[116:119], v[68:71]\n"
"s_cbranch_vccnz L_slow1\n"
"L_back1:\n"
"v_mfma_f32_16x16x32_bf16 v[68:71], a[40:43], a[112:115], v[68:71]\n"
"v_mfma_f32_16x16x32_bf16 v[72:75], a[40:43], a[120:123], v[72:75]\n"
"v_mfma_f32_16x16x32_bf16 v[72:75], a[44:47], a[124:127], v[72:75]\n"
"v_mfma_f32_16x16x32_bf16 v[44:47], a[36:39], a[124:127], v[44:47]\n"
"v_mfma_f32_16x16x32_bf16 v[44:47], a[32:35], a[120:123], v[44:47]\n"
"s_waitcnt lgkmcnt(4)\n"
"v_mfma_f32_16x16x32_bf16 v[48:51], a[32:35], a[128:131], v[48:51]\n"
"v_mfma_f32_16x16x32_bf16 v[48:51], a[36:39], a[132:135], v[48:51]\n"
"global_load_dwordx4 a[88:91], v0, s[34:35] offset:2048 sc1\n"
"v_mfma_f32_16x16x32_bf16 v[76:79], a[44:47], a[132:135], v[76:79]\n"
"v_mfma_f32_16x16x32_bf16 v[76:79], a[40:43], a[128:131], v[76:79]\n"
"s_add_u32 m0, s64, 16384\n"
"v_mfma_f32_16x16x32_bf16 v[80:83], a[40:43], a[136:139], v[80:83]\n"
"v_mfma_f32_16x16x32_bf16 v[80:83], a[44:47], a[140:143], v[80:83]\n"
"v_mfma_f32_16x16x32_bf16 v[52:55], a[36:39], a[140:143], v[52:55]\n"
"v_mfma_f32_16x16x32_bf16 v[52:55], a[32:35], a[136:139], v[52:55]\n"
"s_waitcnt lgkmcnt(0)\n"
"v_mfma_f32_16x16x32_bf16 v[56:59], a[32:35], a[144:147], v[56:59]\n"
"v_mfma_f32_16x16x32_bf16 v[56:59], a[36:39], a[148:151], v[56:59]\n"
"global_load_dwordx4 a[92:95], v0, s[34:35] offset:3072 sc1\n"
"v_mfma_f32_16x16x32_bf16 v[84:87], a[44:47], a[148:151], v[84:87]\n"
"v_mfma_f32_16x16x32_bf16 v[84:87], a[40:43], a[144:147], v[84:87]\n"
"v_mfma_f32_16x16x32_bf16 a[4:7], a[40:43], a[152:155], a[4:7]\n"
"s_add_u32 s34, s34, 8192\n"
"v_mfma_f32_16x16x32_bf16 a[4:7], a[44:47], a[156:159], a[4:7]\n"
"v_mfma_f32_16x16x32_bf16 a[0:3], a[36:39], a[156:159], a[0:3]\n"
"v_mfma_f32_16x16x32_bf16 a[0:3], a[32:35], a[152:155], a[0:3]\n"
"s_waitcnt vmcnt(9)\n"
"v_mfma_f32_16x16x32_bf16 v[88:91], a[48:51], a[96:99], v[88:91]\n"
"v_mfma_f32_16x16x32_bf16 v[88:91], a[52:55], a[100:103], v[88:91]\n"
"global_load_dwordx4 a[32:35], v0, s[34:35] offset:-4096 sc1\n"
"v_mfma_f32_16x16x32_bf16 v[116:119], a[60:63], a[100:103], v[116:119]\n"
"buffer_load_dwordx4 v0, s[48:51], s75 offen offset:0 sc1 lds\n"
"v_mfma_f32_16x16x32_bf16 v[116:119], a[56:59], a[96:99], v[116:119]\n"
"v_mfma_f32_16x16x32_bf16 v[120:123], a[56:59], a[104:107], v[120:123]\n"
"v_mfma_f32_16x16x32_bf16 v[120:123], a[60:63], a[108:111], v[120:123]\n"
"v_mfma_f32_16x16x32_bf16 v[92:95], a[52:55], a[108:111], v[92:95]\n"
"v_mfma_f32_16x16x32_bf16 v[92:95], a[48:51], a[104:107], v[92:95]\n"
"v_mfma_f32_16x16x32_bf16 v[96:99], a[48:51], a[112:115], v[96:99]\n"
"v_mfma_f32_16x16x32_bf16 v[96:99], a[52:55], a[116:119], v[96:99]\n"
"global_load_dwordx4 a[36:39], v0, s[34:35] offset:-3072 sc1\n"
"v_mfma_f32_16x16x32_bf16 v[124:127], a[60:63], a[116:119], v[124:127]\n"
"v_mfma_f32_16x16x32_bf16 v[124:127], a[56:59], a[112:115], v[124:127]\n"
"v_mfma_f32_16x16x32_bf16 v[128:131], a[56:59], a[120:123], v[128:131]\n"
"v_mfma_f32_16x16x32_bf16 v[128:131], a[60:63], a[124:127], v[128:131]\n"
"v_mfma_f32_16x16x32_bf16 v[100:103], a[52:55], a[124:127], v[100:103]\n"
"v_mfma_f32_16x16x32_bf16 v[100:103], a[48:51], a[120:123], v[100:103]\n"
"v_mfma_f32_16x16x32_bf16 v[104:107], a[48:51], a[128:131], v[104:107]\n"
"v_mfma_f32_16x16x32_bf16 v[104:107], a[52:55], a[132:135], v[104:107]\n"
"global_load_dwordx4 a[40:43], v0, s[34:35] offset:-2048 sc1\n"
"v_mfma_f32_16x16x32_bf16 v[132:135], a[60:63], a[132:135], v[132:135]\n"
"buffer_load_dwordx4 v0, s[48:51], s75 offen offset:1024 sc1 lds\n"
"v_mfma_f32_16x16x32_bf16 v[132:135], a[56:59], a[128:131], v[132:135]\n"
"v_mfma_f32_16x16x32_bf16 v[136:139], a[56:59], a[136:139], v[136:139]\n"
"v_mfma_f32_16x16x32_bf16 v[136:139], a[60:63], a[140:143], v[136:139]\n"
"v_mfma_f32_16x16x32_bf16 v[108:111], a[52:55], a[140:143], v[108:111]\n"
"v_mfma_f32_16x16x32_bf16 v[108:111], a[48:51], a[136:139], v[108:111]\n"
"v_mfma_f32_16x16x32_bf16 v[112:115], a[48:51], a[144:147], v[112:115]\n"
"v_mfma_f32_16x16x32_bf16 v[112:115], a[52:55], a[148:151], v[112:115]\n"
"global_load_dwordx4 a[44:47], v0, s[34:35] offset:-1024 sc1\n"
"v_mfma_f32_16x16x32_bf16 v[140:143], a[60:63], a[148:151], v[140:143]\n"
"v_mfma_f32_16x16x32_bf16 v[140:143], a[56:59], a[144:147], v[140:143]\n"
"v_mfma_f32_16x16x32_bf16 a[12:15], a[56:59], a[152:155], a[12:15]\n"
"v_mfma_f32_16x16x32_bf16 a[12:15], a[60:63], a[156:159], a[12:15]\n"
"v_mfma_f32_16x16x32_bf16 a[8:11], a[52:55], a[156:159], a[8:11]\n"
"v_mfma_f32_16x16x32_bf16 a[8:11], a[48:51], a[152:155], a[8:11]\n"
"s_waitcnt vmcnt(10)\n"
"v_mfma_f32_16x16x32_bf16 v[144:147], a[64:67], a[96:99], v[144:147]\n"
"v_mfma_f32_16x16x32_bf16 v[144:147], a[68:71], a[100:103], v[144:147]\n"
"global_load_dwordx4 a[48:51], v0, s[34:35] offset:0 sc1\n"
"v_mfma_f32_16x16x32_bf16 v[172:175], a[76:79], a[100:103], v[172:175]\n"
"v_mfma_f32_16x16x32_bf16 v[172:175], a[72:75], a[96:99], v[172:175]\n"
"buffer_load_dwordx4 v0, s[48:51], s75 offen offset:2048 sc1 lds\n"
"v_mfma_f32_16x16x32_bf16 v[176:179], a[72:75], a[104:107], v[176:179]\n"
"v_mfma_f32_16x16x32_bf16 v[176:179], a[76:79], a[108:111], v[176:179]\n"
"v_mfma_f32_16x16x32_bf16 v[148:151], a[68:71], a[108:111], v[148:151]\n"
"v_mfma_f32_16x16x32_bf16 v[148:151], a[64:67], a[104:107], v[148:151]\n"
"v_mfma_f32_16x16x32_bf16 v[152:155], a[64:67], a[112:115], v[152:155]\n"
"v_lshlrev_b32 v16, 16, v18\n"
"v_mfma_f32_16x16x32_bf16 v[152:155], a[68:71], a[116:119], v[152:155]\n"
"global_load_dwordx4 a[52:55], v0, s[34:35] offset:1024 sc1\n"
"v_and_b32 v17, s63, v18\n"
"v_mfma_f32_16x16x32_bf16 v[180:183], a[76:79], a[116:119], v[180:183]\n"
"v_lshlrev_b32 v18, 16, v19\n"
"v_mfma_f32_16x16x32_bf16 v[180:183], a[72:75], a[112:115], v[180:183]\n"
"v_and_b32 v19, s63, v19\n"
"v_mfma_f32_16x16x32_bf16 v[184:187], a[72:75], a[120:123], v[184:187]\n"
"v_add_f32 v88, v88, v16\n"
"v_mfma_f32_16x16x32_bf16 v[184:187], a[76:79], a[124:127], v[184:187]\n"
"v_add_f32 v89, v89, v17\n"
"v_add_f32 v90, v90, v18\n"
"v_mfma_f32_16x16x32_bf16 v[156:159], a[68:71], a[124:127], v[156:159]\n"
"v_add_f32 v91, v91, v19\n"
"v_mfma_f32_16x16x32_bf16 v[156:159], a[64:67], a[120:123], v[156:159]\n"
"v_add_f32 v92, v92, v16\n"
"v_mfma_f32_16x16x32_bf16 v[160:163], a[64:67], a[128:131], v[160:163]\n"
"v_add_f32 v93, v93, v17\n"
"v_mfma_f32_16x16x32_bf16 v[160:163], a[68:71], a[132:135], v[160:163]\n"
"global_load_dwordx4 a[56:59], v0, s[34:35] offset:2048 sc1\n"
"v_add_f32 v94, v94, v18\n"
"v_mfma_f32_16x16x32_bf16 v[188:191], a[76:79], a[132:135], v[188:191]\n"
"buffer_load_dwordx4 v0, s[48:51], s75 offen offset:3072 sc1 lds\n"
"v_mfma_f32_16x16x32_bf16 v[188:191], a[72:75], a[128:131], v[188:191]\n"
"v_add_f32 v95, v95, v19\n"
"v_add_f32 v96, v96, v16\n"
"v_mfma_f32_16x16x32_bf16 v[192:195], a[72:75], a[136:139], v[192:195]\n"
"v_add_f32 v97, v97, v17\n"
"v_mfma_f32_16x16x32_bf16 v[192:195], a[76:79], a[140:143], v[192:195]\n"
"v_add_f32 v98, v98, v18\n"
"v_mfma_f32_16x16x32_bf16 v[164:167], a[68:71], a[140:143], v[164:167]\n"
"v_add_f32 v99, v99, v19\n"
"v_mfma_f32_16x16x32_bf16 v[164:167], a[64:67], a[136:139], v[164:167]\n"
"v_add_f32 v100, v100, v16\n"
"v_add_f32 v101, v101, v17\n"
"v_mfma_f32_16x16x32_bf16 v[168:171], a[64:67], a[144:147], v[168:171]\n"
"v_add_f32 v102, v102, v18\n"
"v_mfma_f32_16x16x32_bf16 v[168:171], a[68:71], a[148:151], v[168:171]\n"
"global_load_dwordx4 a[60:63], v0, s[34:35] offset:3072 sc1\n"
"v_add_f32 v103, v103, v19\n"
"v_mfma_f32_16x16x32_bf16 v[196:199], a[76:79], a[148:151], v[196:199]\n"
"v_add_f32 v104, v104, v16\n"
"v_mfma_f32_16x16x32_bf16 v[196:199], a[72:75], a[144:147], v[196:199]\n"
"v_add_f32 v105, v105, v17\n"
"v_mfma_f32_16x16x32_bf16 a[20:23], a[72:75], a[152:155], a[20:23]\n"
"s_add_u32 s34, s34, 8192\n"
"v_add_f32 v106, v106, v18\n"
"v_mfma_f32_16x16x32_bf16 a[20:23], a[76:79], a[156:159], a[20:23]\n"
"v_add_f32 v107, v107, v19\n"
"v_mfma_f32_16x16x32_bf16 a[16:19], a[68:71], a[156:159], a[16:19]\n"
"v_add_f32 v108, v108, v16\n"
"v_mfma_f32_16x16x32_bf16 a[16:19], a[64:67], a[152:155], a[16:19]\n"
"s_waitcnt vmcnt(12)\n"
"v_add_f32 v109, v109, v17\n"
"v_mfma_f32_16x16x32_bf16 v[200:203], a[80:83], a[96:99], v[200:203]\n"
"v_add_f32 v110, v110, v18\n"
"v_add_f32 v111, v111, v19\n"
"v_mfma_f32_16x16x32_bf16 v[200:203], a[84:87], a[100:103], v[200:203]\n"
"global_load_dwordx4 a[64:67], v0, s[34:35] offset:-4096 sc1\n"
"v_add_f32 v112, v112, v16\n"
"v_mfma_f32_16x16x32_bf16 v[228:231], a[92:95], a[100:103], v[228:231]\n"
"ds_read_b128 a[100:103], v3 offset:1024\n"
"v_add_f32 v113, v113, v17\n"
"v_mfma_f32_16x16x32_bf16 v[228:231], a[88:91], a[96:99], v[228:231]\n"
"s_add_u32 m0, s64, 20480\n"
"ds_read_b128 a[96:99], v3 offset:0\n"
"v_add_f32 v114, v114, v18\n"
"v_mfma_f32_16x16x32_bf16 v[232:235], a[88:91], a[104:107], v[232:235]\n"
"v_add_f32 v115, v115, v19\n"
"v_mfma_f32_16x16x32_bf16 v[232:235], a[92:95], a[108:111], v[232:235]\n"
"v_accvgpr_read_b32 v12, a8\n"
"v_mfma_f32_16x16x32_bf16 v[204:207], a[84:87], a[108:111], v[204:207]\n"
"ds_read_b128 a[108:111], v3 offset:3072\n"
"v_accvgpr_read_b32 v13, a9\n"
"v_mfma_f32_16x16x32_bf16 v[204:207], a[80:83], a[104:107], v[204:207]\n"
"ds_read_b128 a[104:107], v3 offset:2048\n"
"v_accvgpr_read_b32 v14, a10\n"
"v_mfma_f32_16x16x32_bf16 v[208:211], a[80:83], a[112:115], v[208:211]\n"
"v_accvgpr_read_b32 v15, a11\n"
"v_mfma_f32_16x16x32_bf16 v[208:211], a[84:87], a[116:119], v[208:211]\n"
"global_load_dwordx4 a[68:71], v0, s[34:35] offset:-3072 sc1\n"
"v_add_f32 v12, v12, v16\n"
"v_mfma_f32_16x16x32_bf16 v[236:239], a[92:95], a[116:119], v[236:239]\n"
"ds_read_b128 a[116:119], v3 offset:5120\n"
"v_add_f32 v13, v13, v17\n"
"v_mfma_f32_16x16x32_bf16 v[236:239], a[88:91], a[112:115], v[236:239]\n"
"ds_read_b128 a[112:115], v3 offset:4096\n"
"v_add_f32 v14, v14, v18\n"
"v_mfma_f32_16x16x32_bf16 v[240:243], a[88:91], a[120:123], v[240:243]\n"
"v_add_f32 v15, v15, v19\n"
"v_accvgpr_write_b32 a8, v12\n"
"v_mfma_f32_16x16x32_bf16 v[240:243], a[92:95], a[124:127], v[240:243]\n"
"v_accvgpr_write_b32 a9, v13\n"
"v_mfma_f32_16x16x32_bf16 v[212:215], a[84:87], a[124:127], v[212:215]\n"
"ds_read_b128 a[124:127], v3 offset:7168\n"
"v_accvgpr_write_b32 a10, v14\n"
"v_mfma_f32_16x16x32_bf16 v[212:215], a[80:83], a[120:123], v[212:215]\n"
"ds_read_b128 a[120:123], v3 offset:6144\n"
"v_accvgpr_write_b32 a11, v15\n"
"v_mfma_f32_16x16x32_bf16 v[216:219], a[80:83], a[128:131], v[216:219]\n"
"v_mfma_f32_16x16x32_bf16 v[216:219], a[84:87], a[132:135], v[216:219]\n"
"global_load_dwordx4 a[72:75], v0, s[34:35] offset:-2048 sc1\n"
"v_mfma_f32_16x16x32_bf16 v[244:247], a[92:95], a[132:135], v[244:247]\n"
"ds_read_b128 a[132:135], v3 offset:9216\n"
"v_mfma_f32_16x16x32_bf16 v[244:247], a[88:91], a[128:131], v[244:247]\n"
"ds_read_b128 a[128:131], v3 offset:8192\n"
"v_mfma_f32_16x16x32_bf16 v[248:251], a[88:91], a[136:139], v[248:251]\n"
"buffer_load_dwordx4 v1, s[48:51], s75 offen offset:0 sc1 lds\n"
"v_mfma_f32_16x16x32_bf16 v[248:251], a[92:95], a[140:143], v[248:251]\n"
"v_mfma_f32_16x16x32_bf16 v[220:223], a[84:87], a[140:143], v[220:223]\n"
"ds_read_b128 a[140:143], v3 offset:11264\n"
"v_mfma_f32_16x16x32_bf16 v[220:223], a[80:83], a[136:139], v[220:223]\n"
"ds_read_b128 a[136:139], v3 offset:10240\n"
"v_mfma_f32_16x16x32_bf16 v[224:227], a[80:83], a[144:147], v[224:227]\n"
"v_mfma_f32_16x16x32_bf16 v[224:227], a[84:87], a[148:151], v[224:227]\n"
"global_load_dwordx4 a[76:79], v0, s[34:35] offset:-1024 sc1\n"
"v_mfma_f32_16x16x32_bf16 v[252:255], a[92:95], a[148:151], v[252:255]\n"
"ds_read_b128 a[148:151], v3 offset:13312\n"
"v_mfma_f32_16x16x32_bf16 v[252:255], a[88:91], a[144:147], v[252:255]\n"
"ds_read_b128 a[144:147], v3 offset:12288\n"
"v_mfma_f32_16x16x32_bf16 a[28:31], a[88:91], a[152:155], a[28:31]\n"
"v_mfma_f32_16x16x32_bf16 a[28:31], a[92:95], a[156:159], a[28:31]\n"
"v_mfma_f32_16x16x32_bf16 a[24:27], a[84:87], a[156:159], a[24:27]\n"
"ds_read_b128 a[156:159], v3 offset:15360\n"
"v_mfma_f32_16x16x32_bf16 a[24:27], a[80:83], a[152:155], a[24:27]\n"
"s_waitcnt vmcnt(11) lgkmcnt(11)\n"
"ds_read_b128 a[152:155], v3 offset:14336\n"
"v_mfma_f32_16x16x32_bf16 v[32:35], a[32:35], a[96:99], v[32:35]\n"
"v_mfma_f32_16x16x32_bf16 v[32:35], a[36:39], a[100:103], v[32:35]\n"
"global_load_dwordx4 a[80:83], v0, s[34:35] offset:0 sc1\n"
"v_mfma_f32_16x16x32_bf16 v[60:63], a[44:47], a[100:103], v[60:63]\n"
"v_mfma_f32_16x16x32_bf16 v[60:63], a[40:43], a[96:99], v[60:63]\n"
"buffer_load_dwordx4 v1, s[48:51], s75 offen offset:1024 sc1 lds\n"
"v_mfma_f32_16x16x32_bf16 v[64:67], a[40:43], a[104:107], v[64:67]\n"
"v_mfma_f32_16x16x32_bf16 v[64:67], a[44:47], a[108:111], v[64:67]\n"
"v_mfma_f32_16x16x32_bf16 v[36:39], a[36:39], a[108:111], v[36:39]\n"
"v_mfma_f32_16x16x32_bf16 v[36:39], a[32:35], a[104:107], v[36:39]\n"
"s_waitcnt lgkmcnt(8)\n"
"v_mfma_f32_16x16x32_bf16 v[40:43], a[32:35], a[112:115], v[40:43]\n"
"v_mfma_f32_16x16x32_bf16 v[40:43], a[36:39], a[116:119], v[40:43]\n"
"global_load_dwordx4 a[84:87], v0, s[34:35] offset:1024 sc1\n"
"v_mfma_f32_16x16x32_bf16 v[68:71], a[44:47], a[116:119], v[68:71]\n"
"v_mfma_f32_16x16x32_bf16 v[68:71], a[40:43], a[112:115], v[68:71]\n"
"v_mfma_f32_16x16x32_bf16 v[72:75], a[40:43], a[120:123], v[72:75]\n"
"v_mfma_f32_16x16x32_bf16 v[72:75], a[44:47], a[124:127], v[72:75]\n"
"v_mfma_f32_16x16x32_bf16 v[44:47], a[36:39], a[124:127], v[44:47]\n"
"v_mfma_f32_16x16x32_bf16 v[44:47], a[32:35], a[120:123], v[44:47]\n"
"s_waitcnt lgkmcnt(4)\n"
"v_mfma_f32_16x16x32_bf16 v[48:51], a[32:35], a[128:131], v[48:51]\n"
"v_mfma_f32_16x16x32_bf16 v[48:51], a[36:39], a[132:135], v[48:51]\n"
"global_load_dwordx4 a[88:91], v0, s[34:35] offset:2048 sc1\n"
"v_mfma_f32_16x16x32_bf16 v[76:79], a[44:47], a[132:135], v[76:79]\n"
"v_mfma_f32_16x16x32_bf16 v[76:79], a[40:43], a[128:131], v[76:79]\n"
"buffer_load_dwordx4 v1, s[48:51], s75 offen offset:2048 sc1 lds\n"
"v_mfma_f32_16x16x32_bf16 v[80:83], a[40:43], a[136:139], v[80:83]\n"
"v_mfma_f32_16x16x32_bf16 v[80:83], a[44:47], a[140:143], v[80:83]\n"
"v_mfma_f32_16x16x32_bf16 v[52:55], a[36:39], a[140:143], v[52:55]\n"
"v_mfma_f32_16x16x32_bf16 v[52:55], a[32:35], a[136:139], v[52:55]\n"
"s_waitcnt lgkmcnt(0)\n"
"v_mfma_f32_16x16x32_bf16 v[56:59], a[32:35], a[144:147], v[56:59]\n"
"v_mfma_f32_16x16x32_bf16 v[56:59], a[36:39], a[148:151], v[56:59]\n"
"global_load_dwordx4 a[92:95], v0, s[34:35] offset:3072 sc1\n"
"v_mfma_f32_16x16x32_bf16 v[84:87], a[44:47], a[148:151], v[84:87]\n"
"v_mfma_f32_16x16x32_bf16 v[84:87], a[40:43], a[144:147], v[84:87]\n"
"v_mfma_f32_16x16x32_bf16 a[4:7], a[40:43], a[152:155], a[4:7]\n"
"s_add_u32 s34, s34, 8192\n"
"v_mfma_f32_16x16x32_bf16 a[4:7], a[44:47], a[156:159], a[4:7]\n"
"v_mfma_f32_16x16x32_bf16 a[0:3], a[36:39], a[156:159], a[0:3]\n"
"v_mfma_f32_16x16x32_bf16 a[0:3], a[32:35], a[152:155], a[0:3]\n"
"s_waitcnt vmcnt(11)\n"
"v_mfma_f32_16x16x32_bf16 v[88:91], a[48:51], a[96:99], v[88:91]\n"
"v_mfma_f32_16x16x32_bf16 v[88:91], a[52:55], a[100:103], v[88:91]\n"
"global_load_dwordx4 a[32:35], v0, s[34:35] offset:-4096 sc1\n"
"v_mfma_f32_16x16x32_bf16 v[116:119], a[60:63], a[100:103], v[116:119]\n"
"v_mfma_f32_16x16x32_bf16 v[116:119], a[56:59], a[96:99], v[116:119]\n"
"buffer_load_dwordx4 v1, s[48:51], s75 offen offset:3072 sc1 lds\n"
"v_mfma_f32_16x16x32_bf16 v[120:123], a[56:59], a[104:107], v[120:123]\n"
"v_mfma_f32_16x16x32_bf16 v[120:123], a[60:63], a[108:111], v[120:123]\n"
"v_mfma_f32_16x16x32_bf16 v[92:95], a[52:55], a[108:111], v[92:95]\n"
"v_mfma_f32_16x16x32_bf16 v[92:95], a[48:51], a[104:107], v[92:95]\n"
"v_mfma_f32_16x16x32_bf16 v[96:99], a[48:51], a[112:115], v[96:99]\n"
"v_mfma_f32_16x16x32_bf16 v[96:99], a[52:55], a[116:119], v[96:99]\n"
"global_load_dwordx4 a[36:39], v0, s[34:35] offset:-3072 sc1\n"
"v_mfma_f32_16x16x32_bf16 v[124:127], a[60:63], a[116:119], v[124:127]\n"
"v_mfma_f32_16x16x32_bf16 v[124:127], a[56:59], a[112:115], v[124:127]\n"
"v_mfma_f32_16x16x32_bf16 v[128:131], a[56:59], a[120:123], v[128:131]\n"
"v_mfma_f32_16x16x32_bf16 v[128:131], a[60:63], a[124:127], v[128:131]\n"
"v_mfma_f32_16x16x32_bf16 v[100:103], a[52:55], a[124:127], v[100:103]\n"
"v_mfma_f32_16x16x32_bf16 v[100:103], a[48:51], a[120:123], v[100:103]\n"
"v_mfma_f32_16x16x32_bf16 v[104:107], a[48:51], a[128:131], v[104:107]\n"
"v_mfma_f32_16x16x32_bf16 v[104:107], a[52:55], a[132:135], v[104:107]\n"
"global_load_dwordx4 a[40:43], v0, s[34:35] offset:-2048 sc1\n"
"v_mfma_f32_16x16x32_bf16 v[132:135], a[60:63], a[132:135], v[132:135]\n"
"v_mfma_f32_16x16x32_bf16 v[132:135], a[56:59], a[128:131], v[132:135]\n"
"s_add_u32 m0, s64, 24576\n"
"v_mfma_f32_16x16x32_bf16 v[136:139], a[56:59], a[136:139], v[136:139]\n"
"v_mfma_f32_16x16x32_bf16 v[136:139], a[60:63], a[140:143], v[136:139]\n"
"v_mfma_f32_16x16x32_bf16 v[108:111], a[52:55], a[140:143], v[108:111]\n"
"v_mfma_f32_16x16x32_bf16 v[108:111], a[48:51], a[136:139], v[108:111]\n"
"v_mfma_f32_16x16x32_bf16 v[112:115], a[48:51], a[144:147], v[112:115]\n"
"v_mfma_f32_16x16x32_bf16 v[112:115], a[52:55], a[148:151], v[112:115]\n"
"global_load_dwordx4 a[44:47], v0, s[34:35] offset:-1024 sc1\n"
"v_mfma_f32_16x16x32_bf16 v[140:143], a[60:63], a[148:151], v[140:143]\n"
"v_mfma_f32_16x16x32_bf16 v[140:143], a[56:59], a[144:147], v[140:143]\n"
"v_mfma_f32_16x16x32_bf16 a[12:15], a[56:59], a[152:155], a[12:15]\n"
"v_mfma_f32_16x16x32_bf16 a[12:15], a[60:63], a[156:159], a[12:15]\n"
"v_mfma_f32_16x16x32_bf16 a[8:11], a[52:55], a[156:159], a[8:11]\n"
"v_mfma_f32_16x16x32_bf16 a[8:11], a[48:51], a[152:155], a[8:11]\n"
"s_waitcnt vmcnt(11)\n"
"v_mfma_f32_16x16x32_bf16 v[144:147], a[64:67], a[96:99], v[144:147]\n"
"v_mfma_f32_16x16x32_bf16 v[144:147], a[68:71], a[100:103], v[144:147]\n"
"global_load_dwordx4 a[48:51], v0, s[34:35] offset:0 sc1\n"
"v_mfma_f32_16x16x32_bf16 v[172:175], a[76:79], a[100:103], v[172:175]\n"
"v_mfma_f32_16x16x32_bf16 v[172:175], a[72:75], a[96:99], v[172:175]\n"
"buffer_load_dwordx4 v0, s[48:51], s69 offen offset:0 sc1 lds\n"
"v_mfma_f32_16x16x32_bf16 v[176:179], a[72:75], a[104:107], v[176:179]\n"
"v_mfma_f32_16x16x32_bf16 v[176:179], a[76:79], a[108:111], v[176:179]\n"
"v_mfma_f32_16x16x32_bf16 v[148:151], a[68:71], a[108:111], v[148:151]\n"
"v_mfma_f32_16x16x32_bf16 v[148:151], a[64:67], a[104:107], v[148:151]\n"
"v_mfma_f32_16x16x32_bf16 v[152:155], a[64:67], a[112:115], v[152:155]\n"
"v_lshlrev_b32 v20, 16, v22\n"
"v_mfma_f32_16x16x32_bf16 v[152:155], a[68:71], a[116:119], v[152:155]\n"
"global_load_dwordx4 a[52:55], v0, s[34:35] offset:1024 sc1\n"
"v_and_b32 v21, s63, v22\n"
"v_mfma_f32_16x16x32_bf16 v[180:183], a[76:79], a[116:119], v[180:183]\n"
"v_lshlrev_b32 v22, 16, v23\n"
"v_mfma_f32_16x16x32_bf16 v[180:183], a[72:75], a[112:115], v[180:183]\n"
"v_and_b32 v23, s63, v23\n"
"v_mfma_f32_16x16x32_bf16 v[184:187], a[72:75], a[120:123], v[184:187]\n"
"v_add_f32 v116, v116, v20\n"
"v_mfma_f32_16x16x32_bf16 v[184:187], a[76:79], a[124:127], v[184:187]\n"
"v_add_f32 v117, v117, v21\n"
"v_add_f32 v118, v118, v22\n"
"v_mfma_f32_16x16x32_bf16 v[156:159], a[68:71], a[124:127], v[156:159]\n"
"v_add_f32 v119, v119, v23\n"
"v_mfma_f32_16x16x32_bf16 v[156:159], a[64:67], a[120:123], v[156:159]\n"
"v_add_f32 v120, v120, v20\n"
"v_mfma_f32_16x16x32_bf16 v[160:163], a[64:67], a[128:131], v[160:163]\n"
"v_add_f32 v121, v121, v21\n"
"v_mfma_f32_16x16x32_bf16 v[160:163], a[68:71], a[132:135], v[160:163]\n"
"global_load_dwordx4 a[56:59], v0, s[34:35] offset:2048 sc1\n"
"v_add_f32 v122, v122, v22\n"
"v_mfma_f32_16x16x32_bf16 v[188:191], a[76:79], a[132:135], v[188:191]\n"
"v_add_f32 v123, v123, v23\n"
"v_add_f32 v124, v124, v20\n"
"v_mfma_f32_16x16x32_bf16 v[188:191], a[72:75], a[128:131], v[188:191]\n"
"buffer_load_dwordx4 v0, s[48:51], s69 offen offset:1024 sc1 lds\n"
"v_mfma_f32_16x16x32_bf16 v[192:195], a[72:75], a[136:139], v[192:195]\n"
"v_add_f32 v125, v125, v21\n"
"v_mfma_f32_16x16x32_bf16 v[192:195], a[76:79], a[140:143], v[192:195]\n"
"v_add_f32 v126, v126, v22\n"
"v_mfma_f32_16x16x32_bf16 v[164:167], a[68:71], a[140:143], v[164:167]\n"
"v_add_f32 v127, v127, v23\n"
"v_mfma_f32_16x16x32_bf16 v[164:167], a[64:67], a[136:139], v[164:167]\n"
"v_add_f32 v128, v128, v20\n"
"v_add_f32 v129, v129, v21\n"
"v_mfma_f32_16x16x32_bf16 v[168:171], a[64:67], a[144:147], v[168:171]\n"
"v_add_f32 v130, v130, v22\n"
"v_mfma_f32_16x16x32_bf16 v[168:171], a[68:71], a[148:151], v[168:171]\n"
"global_load_dwordx4 a[60:63], v0, s[34:35] offset:3072 sc1\n"
"v_add_f32 v131, v131, v23\n"
"v_mfma_f32_16x16x32_bf16 v[196:199], a[76:79], a[148:151], v[196:199]\n"
"v_add_f32 v132, v132, v20\n"
"v_mfma_f32_16x16x32_bf16 v[196:199], a[72:75], a[144:147], v[196:199]\n"
"v_add_f32 v133, v133, v21\n"
"v_mfma_f32_16x16x32_bf16 a[20:23], a[72:75], a[152:155], a[20:23]\n"
"s_add_u32 s34, s34, 8192\n"
"v_add_f32 v134, v134, v22\n"
"v_mfma_f32_16x16x32_bf16 a[20:23], a[76:79], a[156:159], a[20:23]\n"
"v_add_f32 v135, v135, v23\n"
"v_mfma_f32_16x16x32_bf16 a[16:19], a[68:71], a[156:159], a[16:19]\n"
"v_add_f32 v136, v136, v20\n"
"v_mfma_f32_16x16x32_bf16 a[16:19], a[64:67], a[152:155], a[16:19]\n"
"s_waitcnt vmcnt(11)\n"
"v_add_f32 v137, v137, v21\n"
"v_mfma_f32_16x16x32_bf16 v[200:203], a[80:83], a[96:99], v[200:203]\n"
"v_add_f32 v138, v138, v22\n"
"v_add_f32 v139, v139, v23\n"
"v_mfma_f32_16x16x32_bf16 v[200:203], a[84:87], a[100:103], v[200:203]\n"
"global_load_dwordx4 a[64:67], v0, s[34:35] offset:-4096 sc1\n"
"v_add_f32 v140, v140, v20\n"
"v_mfma_f32_16x16x32_bf16 v[228:231], a[92:95], a[100:103], v[228:231]\n"
"ds_read_b128 a[100:103], v4 offset:1024\n"
"v_add_f32 v141, v141, v21\n"
"v_mfma_f32_16x16x32_bf16 v[228:231], a[88:91], a[96:99], v[228:231]\n"
"ds_read_b128 a[96:99], v4 offset:0\n"
"v_add_f32 v142, v142, v22\n"
"v_mfma_f32_16x16x32_bf16 v[232:235], a[88:91], a[104:107], v[232:235]\n"
"buffer_load_dwordx4 v0, s[48:51], s69 offen offset:2048 sc1 lds\n"
"v_mfma_f32_16x16x32_bf16 v[232:235], a[92:95], a[108:111], v[232:235]\n"
"v_add_f32 v143, v143, v23\n"
"v_accvgpr_read_b32 v12, a12\n"
"v_mfma_f32_16x16x32_bf16 v[204:207], a[84:87], a[108:111], v[204:207]\n"
"ds_read_b128 a[108:111], v4 offset:3072\n"
"v_accvgpr_read_b32 v13, a13\n"
"v_mfma_f32_16x16x32_bf16 v[204:207], a[80:83], a[104:107], v[204:207]\n"
"ds_read_b128 a[104:107], v4 offset:2048\n"
"v_accvgpr_read_b32 v14, a14\n"
"v_mfma_f32_16x16x32_bf16 v[208:211], a[80:83], a[112:115], v[208:211]\n"
"v_accvgpr_read_b32 v15, a15\n"
"v_mfma_f32_16x16x32_bf16 v[208:211], a[84:87], a[116:119], v[208:211]\n"
"global_load_dwordx4 a[68:71], v0, s[34:35] offset:-3072 sc1\n"
"v_add_f32 v12, v12, v20\n"
"v_mfma_f32_16x16x32_bf16 v[236:239], a[92:95], a[116:119], v[236:239]\n"
"ds_read_b128 a[116:119], v4 offset:5120\n"
"v_add_f32 v13, v13, v21\n"
"v_mfma_f32_16x16x32_bf16 v[236:239], a[88:91], a[112:115], v[236:239]\n"
"ds_read_b128 a[112:115], v4 offset:4096\n"
"v_add_f32 v14, v14, v22\n"
"v_mfma_f32_16x16x32_bf16 v[240:243], a[88:91], a[120:123], v[240:243]\n"
"v_add_f32 v15, v15, v23\n"
"v_accvgpr_write_b32 a12, v12\n"
"v_mfma_f32_16x16x32_bf16 v[240:243], a[92:95], a[124:127], v[240:243]\n"
"v_accvgpr_write_b32 a13, v13\n"
"v_mfma_f32_16x16x32_bf16 v[212:215], a[84:87], a[124:127], v[212:215]\n"
"ds_read_b128 a[124:127], v4 offset:7168\n"
"v_accvgpr_write_b32 a14, v14\n"
"v_mfma_f32_16x16x32_bf16 v[212:215], a[80:83], a[120:123], v[212:215]\n"
"ds_read_b128 a[120:123], v4 offset:6144\n"
"v_accvgpr_write_b32 a15, v15\n"
"v_mfma_f32_16x16x32_bf16 v[216:219], a[80:83], a[128:131], v[216:219]\n"
"v_mfma_f32_16x16x32_bf16 v[216:219], a[84:87], a[132:135], v[216:219]\n"
"global_load_dwordx4 a[72:75], v0, s[34:35] offset:-2048 sc1\n"
"v_mfma_f32_16x16x32_bf16 v[244:247], a[92:95], a[132:135], v[244:247]\n"
"ds_read_b128 a[132:135], v4 offset:9216\n"
"v_mfma_f32_16x16x32_bf16 v[244:247], a[88:91], a[128:131], v[244:247]\n"
"ds_read_b128 a[128:131], v4 offset:8192\n"
"v_mfma_f32_16x16x32_bf16 v[248:251], a[88:91], a[136:139], v[248:251]\n"
"buffer_load_dwordx4 v0, s[48:51], s69 offen offset:3072 sc1 lds\n"
"v_mfma_f32_16x16x32_bf16 v[248:251], a[92:95], a[140:143], v[248:251]\n"
"v_mfma_f32_16x16x32_bf16 v[220:223], a[84:87], a[140:143], v[220:223]\n"
"ds_read_b128 a[140:143], v4 offset:11264\n"
"v_mfma_f32_16x16x32_bf16 v[220:223], a[80:83], a[136:139], v[220:223]\n"
"ds_read_b128 a[136:139], v4 offset:10240\n"
"v_mfma_f32_16x16x32_bf16 v[224:227], a[80:83], a[144:147], v[224:227]\n"
"v_mfma_f32_16x16x32_bf16 v[224:227], a[84:87], a[148:151], v[224:227]\n"
"global_load_dwordx4 a[76:79], v0, s[34:35] offset:-1024 sc1\n"
"v_mfma_f32_16x16x32_bf16 v[252:255], a[92:95], a[148:151], v[252:255]\n"
"L_st7361:\n"
"s_cbranch_execz L_st8230\n"
"ds_read_b128 a[148:151], v4 offset:13312\n"
"v_mfma_f32_16x16x32_bf16 v[252:255], a[88:91], a[144:147], v[252:255]\n"
"ds_read_b128 a[144:147], v4 offset:12288\n"
"v_mfma_f32_16x16x32_bf16 a[28:31], a[88:91], a[152:155], a[28:31]\n"
"v_mfma_f32_16x16x32_bf16 a[28:31], a[92:95], a[156:159], a[28:31]\n"
"v_mfma_f32_16x16x32_bf16 a[24:27], a[84:87], a[156:159], a[24:27]\n"
"ds_read_b128 a[156:159], v4 offset:15360\n"
"v_mfma_f32_16x16x32_bf16 a[24:27], a[80:83], a[152:155], a[24:27]\n"
"s_waitcnt vmcnt(12) lgkmcnt(11)\n"
"ds_read_b128 a[152:155], v4 offset:14336\n"
"v_mfma_f32_16x16x32_bf16 v[32:35], a[32:35], a[96:99], v[32:35]\n"
"v_mfma_f32_16x16x32_bf16 v[32:35], a[36:39], a[100:103], v[32:35]\n"
"global_load_dwordx4 a[80:83], v0, s[34:35] offset:0 sc1\n"
"v_lshrrev_b32 v16, 4, v0\n"
"v_mfma_f32_16x16x32_bf16 v[60:63], a[44:47], a[100:103], v[60:63]\n"
"v_and_b32 v16, 0x30, v16\n"
"v_mfma_f32_16x16x32_bf16 v[60:63], a[40:43], a[96:99], v[60:63]\n"
"s_add_u32 m0, s64, 28672\n"
"buffer_load_dwordx2 v[18:19], v16, s[44:47], s61 offen offset:128\n"
"v_mfma_f32_16x16x32_bf16 v[64:67], a[40:43], a[104:107], v[64:67]\n"
"v_mfma_f32_16x16x32_bf16 v[64:67], a[44:47], a[108:111], v[64:67]\n"
"v_mfma_f32_16x16x32_bf16 v[36:39], a[36:39], a[108:111], v[36:39]\n"
"v_mfma_f32_16x16x32_bf16 v[36:39], a[32:35], a[104:107], v[36:39]\n"
"s_waitcnt lgkmcnt(8)\n"
"v_mfma_f32_16x16x32_bf16 v[40:43], a[32:35], a[112:115], v[40:43]\n"
"v_mfma_f32_16x16x32_bf16 v[40:43], a[36:39], a[116:119], v[40:43]\n"
"global_load_dwordx4 a[84:87], v0, s[34:35] offset:1024 sc1\n"
"v_lshrrev_b32 v20, 4, v0\n"
"v_mfma_f32_16x16x32_bf16 v[68:71], a[44:47], a[116:119], v[68:71]\n"
"v_and_b32 v20, 0x30, v20\n"
"v_mfma_f32_16x16x32_bf16 v[68:71], a[40:43], a[112:115], v[68:71]\n"
"buffer_load_dwordx2 v[22:23], v20, s[44:47], s61 offen offset:136\n"
"v_mfma_f32_16x16x32_bf16 v[72:75], a[40:43], a[120:123], v[72:75]\n"
"v_mfma_f32_16x16x32_bf16 v[72:75], a[44:47], a[124:127], v[72:75]\n"
"v_mfma_f32_16x16x32_bf16 v[44:47], a[36:39], a[124:127], v[44:47]\n"
"v_mfma_f32_16x16x32_bf16 v[44:47], a[32:35], a[120:123], v[44:47]\n"
"s_waitcnt lgkmcnt(4)\n"
"v_mfma_f32_16x16x32_bf16 v[48:51], a[32:35], a[128:131], v[48:51]\n"
"v_mfma_f32_16x16x32_bf16 v[48:51], a[36:39], a[132:135], v[48:51]\n"
"global_load_dwordx4 a[88:91], v0, s[34:35] offset:2048 sc1\n"
"v_lshrrev_b32 v24, 4, v0\n"
"v_mfma_f32_16x16x32_bf16 v[76:79], a[44:47], a[132:135], v[76:79]\n"
"v_and_b32 v24, 0x30, v24\n"
"v_mfma_f32_16x16x32_bf16 v[76:79], a[40:43], a[128:131], v[76:79]\n"
"buffer_load_dwordx4 v1, s[48:51], s69 offen offset:0 sc1 lds\n"
"buffer_load_dwordx2 v[26:27], v24, s[44:47], s61 offen offset:192\n"
"v_mfma_f32_16x16x32_bf16 v[80:83], a[40:43], a[136:139], v[80:83]\n"
"v_mfma_f32_16x16x32_bf16 v[80:83], a[44:47], a[140:143], v[80:83]\n"
"v_mfma_f32_16x16x32_bf16 v[52:55], a[36:39], a[140:143], v[52:55]\n"
"v_mfma_f32_16x16x32_bf16 v[52:55], a[32:35], a[136:139], v[52:55]\n"
"s_waitcnt lgkmcnt(0)\n"
"v_mfma_f32_16x16x32_bf16 v[56:59], a[32:35], a[144:147], v[56:59]\n"
"v_mfma_f32_16x16x32_bf16 v[56:59], a[36:39], a[148:151], v[56:59]\n"
"global_load_dwordx4 a[92:95], v0, s[34:35] offset:3072 sc1\n"
"v_lshrrev_b32 v28, 4, v0\n"
"v_mfma_f32_16x16x32_bf16 v[84:87], a[44:47], a[148:151], v[84:87]\n"
"v_and_b32 v28, 0x30, v28\n"
"v_mfma_f32_16x16x32_bf16 v[84:87], a[40:43], a[144:147], v[84:87]\n"
"buffer_load_dwordx2 v[30:31], v28, s[44:47], s61 offen offset:200\n"
"v_mfma_f32_16x16x32_bf16 a[4:7], a[40:43], a[152:155], a[4:7]\n"
"s_add_u32 s34, s34, 8192\n"
"v_mfma_f32_16x16x32_bf16 a[4:7], a[44:47], a[156:159], a[4:7]\n"
"v_mfma_f32_16x16x32_bf16 a[0:3], a[36:39], a[156:159], a[0:3]\n"
"v_mfma_f32_16x16x32_bf16 a[0:3], a[32:35], a[152:155], a[0:3]\n"
"s_waitcnt vmcnt(15)\n"
"v_mfma_f32_16x16x32_bf16 v[88:91], a[48:51], a[96:99], v[88:91]\n"
"v_mfma_f32_16x16x32_bf16 v[88:91], a[52:55], a[100:103], v[88:91]\n"
"global_load_dwordx4 a[32:35], v0, s[34:35] offset:-4096 sc1\n"
"v_mfma_f32_16x16x32_bf16 v[116:119], a[60:63], a[100:103], v[116:119]\n"
"v_mfma_f32_16x16x32_bf16 v[116:119], a[56:59], a[96:99], v[116:119]\n"
"buffer_load_dwordx4 v1, s[48:51], s69 offen offset:1024 sc1 lds\n"
"v_mfma_f32_16x16x32_bf16 v[120:123], a[56:59], a[104:107], v[120:123]\n"
"v_mfma_f32_16x16x32_bf16 v[120:123], a[60:63], a[108:111], v[120:123]\n"
"v_mfma_f32_16x16x32_bf16 v[92:95], a[52:55], a[108:111], v[92:95]\n"
"v_mfma_f32_16x16x32_bf16 v[92:95], a[48:51], a[104:107], v[92:95]\n"
"v_mfma_f32_16x16x32_bf16 v[96:99], a[48:51], a[112:115], v[96:99]\n"
"v_mfma_f32_16x16x32_bf16 v[96:99], a[52:55], a[116:119], v[96:99]\n"
"global_load_dwordx4 a[36:39], v0, s[34:35] offset:-3072 sc1\n"
"v_mfma_f32_16x16x32_bf16 v[124:127], a[60:63], a[116:119], v[124:127]\n"
"v_mfma_f32_16x16x32_bf16 v[124:127], a[56:59], a[112:115], v[124:127]\n"
"v_mfma_f32_16x16x32_bf16 v[128:131], a[56:59], a[120:123], v[128:131]\n"
"v_mfma_f32_16x16x32_bf16 v[128:131], a[60:63], a[124:127], v[128:131]\n"
"v_mfma_f32_16x16x32_bf16 v[100:103], a[52:55], a[124:127], v[100:103]\n"
"v_mfma_f32_16x16x32_bf16 v[100:103], a[48:51], a[120:123], v[100:103]\n"
"v_mfma_f32_16x16x32_bf16 v[104:107], a[48:51], a[128:131], v[104:107]\n"
"v_mfma_f32_16x16x32_bf16 v[104:107], a[52:55], a[132:135], v[104:107]\n"
"global_load_dwordx4 a[40:43], v0, s[34:35] offset:-2048 sc1\n"
"v_mfma_f32_16x16x32_bf16 v[132:135], a[60:63], a[132:135], v[132:135]\n"
"v_mfma_f32_16x16x32_bf16 v[132:135], a[56:59], a[128:131], v[132:135]\n"
"buffer_load_dwordx4 v1, s[48:51], s69 offen offset:2048 sc1 lds\n"
"v_mfma_f32_16x16x32_bf16 v[136:139], a[56:59], a[136:139], v[136:139]\n"
"v_mfma_f32_16x16x32_bf16 v[136:139], a[60:63], a[140:143], v[136:139]\n"
"v_mfma_f32_16x16x32_bf16 v[108:111], a[52:55], a[140:143], v[108:111]\n"
"v_mfma_f32_16x16x32_bf16 v[108:111], a[48:51], a[136:139], v[108:111]\n"
"v_mfma_f32_16x16x32_bf16 v[112:115], a[48:51], a[144:147], v[112:115]\n"
"v_mfma_f32_16x16x32_bf16 v[112:115], a[52:55], a[148:151], v[112:115]\n"
"global_load_dwordx4 a[44:47], v0, s[34:35] offset:-1024 sc1\n"
"v_mfma_f32_16x16x32_bf16 v[140:143], a[60:63], a[148:151], v[140:143]\n"
"v_mfma_f32_16x16x32_bf16 v[140:143], a[56:59], a[144:147], v[140:143]\n"
"v_mfma_f32_16x16x32_bf16 a[12:15], a[56:59], a[152:155], a[12:15]\n"
"v_mfma_f32_16x16x32_bf16 a[12:15], a[60:63], a[156:159], a[12:15]\n"
"v_mfma_f32_16x16x32_bf16 a[8:11], a[52:55], a[156:159], a[8:11]\n"
"v_mfma_f32_16x16x32_bf16 a[8:11], a[48:51], a[152:155], a[8:11]\n"
"s_waitcnt vmcnt(15)\n"
"v_mfma_f32_16x16x32_bf16 v[144:147], a[64:67], a[96:99], v[144:147]\n"
"v_mfma_f32_16x16x32_bf16 v[144:147], a[68:71], a[100:103], v[144:147]\n"
"global_load_dwordx4 a[48:51], v0, s[34:35] offset:0 sc1\n"
"v_mfma_f32_16x16x32_bf16 v[172:175], a[76:79], a[100:103], v[172:175]\n"
"v_mfma_f32_16x16x32_bf16 v[172:175], a[72:75], a[96:99], v[172:175]\n"
"buffer_load_dwordx4 v1, s[48:51], s69 offen offset:3072 sc1 lds\n"
"v_mfma_f32_16x16x32_bf16 v[176:179], a[72:75], a[104:107], v[176:179]\n"
"v_mfma_f32_16x16x32_bf16 v[176:179], a[76:79], a[108:111], v[176:179]\n"
"v_mfma_f32_16x16x32_bf16 v[148:151], a[68:71], a[108:111], v[148:151]\n"
"v_mfma_f32_16x16x32_bf16 v[148:151], a[64:67], a[104:107], v[148:151]\n"
"v_mfma_f32_16x16x32_bf16 v[152:155], a[64:67], a[112:115], v[152:155]\n"
"v_mfma_f32_16x16x32_bf16 v[152:155], a[68:71], a[116:119], v[152:155]\n"
"global_load_dwordx4 a[52:55], v0, s[34:35] offset:1024 sc1\n"
"v_mfma_f32_16x16x32_bf16 v[180:183], a[76:79], a[116:119], v[180:183]\n"
"v_mfma_f32_16x16x32_bf16 v[180:183], a[72:75], a[112:115], v[180:183]\n"
"v_mfma_f32_16x16x32_bf16 v[184:187], a[72:75], a[120:123], v[184:187]\n"
"v_mfma_f32_16x16x32_bf16 v[184:187], a[76:79], a[124:127], v[184:187]\n"
"v_mfma_f32_16x16x32_bf16 v[156:159], a[68:71], a[124:127], v[156:159]\n"
"v_mfma_f32_16x16x32_bf16 v[156:159], a[64:67], a[120:123], v[156:159]\n"
"v_mfma_f32_16x16x32_bf16 v[160:163], a[64:67], a[128:131], v[160:163]\n"
"v_mfma_f32_16x16x32_bf16 v[160:163], a[68:71], a[132:135], v[160:163]\n"
"global_load_dwordx4 a[56:59], v0, s[34:35] offset:2048 sc1\n"
"v_mfma_f32_16x16x32_bf16 v[188:191], a[76:79], a[132:135], v[188:191]\n"
"v_mfma_f32_16x16x32_bf16 v[188:191], a[72:75], a[128:131], v[188:191]\n"
"v_mfma_f32_16x16x32_bf16 v[192:195], a[72:75], a[136:139], v[192:195]\n"
"s_cmp_lg_u32 s29, 0\n"
"s_cselect_b32 s60, s31, s60\n"
"s_cselect_b32 s30, 0, s30\n"
"v_mfma_f32_16x16x32_bf16 v[192:195], a[76:79], a[140:143], v[192:195]\n"
"v_mfma_f32_16x16x32_bf16 v[164:167], a[68:71], a[140:143], v[164:167]\n"
"s_cmp_lg_u32 s29, 0\n"
"s_cselect_b32 s31, s75, s72\n"
"v_mfma_f32_16x16x32_bf16 v[164:167], a[64:67], a[136:139], v[164:167]\n"
"s_sub_u32 s52, s52, s72\n"
"s_subb_u32 s53, s53, 0\n"
"v_mfma_f32_16x16x32_bf16 v[168:171], a[64:67], a[144:147], v[168:171]\n"
"s_add_u32 s52, s52, s31\n"
"s_addc_u32 s53, s53, 0\n"
"v_mfma_f32_16x16x32_bf16 v[168:171], a[68:71], a[148:151], v[168:171]\n"
"global_load_dwordx4 a[60:63], v0, s[34:35] offset:3072 sc1\n"
"v_mfma_f32_16x16x32_bf16 v[196:199], a[76:79], a[148:151], v[196:199]\n"
"s_mov_b32 s72, s31\n"
"v_mfma_f32_16x16x32_bf16 v[196:199], a[72:75], a[144:147], v[196:199]\n"
"v_mfma_f32_16x16x32_bf16 a[20:23], a[72:75], a[152:155], a[20:23]\n"
"s_add_u32 s34, s34, 8192\n"
"v_mfma_f32_16x16x32_bf16 a[20:23], a[76:79], a[156:159], a[20:23]\n"
"v_mfma_f32_16x16x32_bf16 a[16:19], a[68:71], a[156:159], a[16:19]\n"
"s_cmp_lg_u32 s29, 0\n"
"s_cselect_b32 s31, s69, s73\n"
"v_mfma_f32_16x16x32_bf16 a[16:19], a[64:67], a[152:155], a[16:19]\n"
"s_sub_u32 s54, s54, s73\n"
"s_subb_u32 s55, s55, 0\n"
"s_waitcnt vmcnt(12)\n"
"v_mfma_f32_16x16x32_bf16 v[200:203], a[80:83], a[96:99], v[200:203]\n"
"s_add_u32 s54, s54, s31\n"
"s_addc_u32 s55, s55, 0\n"
"v_mfma_f32_16x16x32_bf16 v[200:203], a[84:87], a[100:103], v[200:203]\n"
"global_load_dwordx4 a[64:67], v0, s[34:35] offset:-4096 sc1\n"
"v_mfma_f32_16x16x32_bf16 v[228:231], a[92:95], a[100:103], v[228:231]\n"
"s_mov_b32 s73, s31\n"
"ds_read_b128 a[100:103], v5 offset:1024\n"
"v_mfma_f32_16x16x32_bf16 v[228:231], a[88:91], a[96:99], v[228:231]\n"
"ds_read_b128 a[96:99], v5 offset:0\n"
"v_mfma_f32_16x16x32_bf16 v[232:235], a[88:91], a[104:107], v[232:235]\n"
"v_mfma_f32_16x16x32_bf16 v[232:235], a[92:95], a[108:111], v[232:235]\n"
"v_mfma_f32_16x16x32_bf16 v[204:207], a[84:87], a[108:111], v[204:207]\n"
"ds_read_b128 a[108:111], v5 offset:3072\n"
"v_mfma_f32_16x16x32_bf16 v[204:207], a[80:83], a[104:107], v[204:207]\n"
"ds_read_b128 a[104:107], v5 offset:2048\n"
"v_mfma_f32_16x16x32_bf16 v[208:211], a[80:83], a[112:115], v[208:211]\n"
"v_mfma_f32_16x16x32_bf16 v[208:211], a[84:87], a[116:119], v[208:211]\n"
"global_load_dwordx4 a[68:71], v0, s[34:35] offset:-3072 sc1\n"
"v_mfma_f32_16x16x32_bf16 v[236:239], a[92:95], a[116:119], v[236:239]\n"
"ds_read_b128 a[116:119], v5 offset:5120\n"
"v_mfma_f32_16x16x32_bf16 v[236:239], a[88:91], a[112:115], v[236:239]\n"
"ds_read_b128 a[112:115], v5 offset:4096\n"
"v_mfma_f32_16x16x32_bf16 v[240:243], a[88:91], a[120:123], v[240:243]\n"
"v_mfma_f32_16x16x32_bf16 v[240:243], a[92:95], a[124:127], v[240:243]\n"
"v_mfma_f32_16x16x32_bf16 v[212:215], a[84:87], a[124:127], v[212:215]\n"
"ds_read_b128 a[124:127], v5 offset:7168\n"
"v_mfma_f32_16x16x32_bf16 v[212:215], a[80:83], a[120:123], v[212:215]\n"
"ds_read_b128 a[120:123], v5 offset:6144\n"
"v_mfma_f32_16x16x32_bf16 v[216:219], a[80:83], a[128:131], v[216:219]\n"
"v_mfma_f32_16x16x32_bf16 v[216:219], a[84:87], a[132:135], v[216:219]\n"
"global_load_dwordx4 a[72:75], v0, s[34:35] offset:-2048 sc1\n"
"v_mfma_f32_16x16x32_bf16 v[244:247], a[92:95], a[132:135], v[244:247]\n"
"ds_read_b128 a[132:135], v5 offset:9216\n"
"v_mfma_f32_16x16x32_bf16 v[244:247], a[88:91], a[128:131], v[244:247]\n"
"ds_read_b128 a[128:131], v5 offset:8192\n"
"v_mfma_f32_16x16x32_bf16 v[248:251], a[88:91], a[136:139], v[248:251]\n"
"v_mfma_f32_16x16x32_bf16 v[248:251], a[92:95], a[140:143], v[248:251]\n"
"v_mfma_f32_16x16x32_bf16 v[220:223], a[84:87], a[140:143], v[220:223]\n"
"ds_read_b128 a[140:143], v5 offset:11264\n"
"v_mfma_f32_16x16x32_bf16 v[220:223], a[80:83], a[136:139], v[220:223]\n"
"ds_read_b128 a[136:139], v5 offset:10240\n"
"v_mfma_f32_16x16x32_bf16 v[224:227], a[80:83], a[144:147], v[224:227]\n"
"v_mfma_f32_16x16x32_bf16 v[224:227], a[84:87], a[148:151], v[224:227]\n"
"global_load_dwordx4 a[76:79], v0, s[34:35] offset:-1024 sc1\n"
"v_mfma_f32_16x16x32_bf16 v[252:255], a[92:95], a[148:151], v[252:255]\n"
"ds_read_b128 a[148:151], v5 offset:13312\n"
"v_mfma_f32_16x16x32_bf16 v[252:255], a[88:91], a[144:147], v[252:255]\n"
"ds_read_b128 a[144:147], v5 offset:12288\n"
"v_mfma_f32_16x16x32_bf16 a[28:31], a[88:91], a[152:155], a[28:31]\n"
"v_mfma_f32_16x16x32_bf16 a[28:31], a[92:95], a[156:159], a[28:31]\n"
"v_mfma_f32_16x16x32_bf16 a[24:27], a[84:87], a[156:159], a[24:27]\n"
"ds_read_b128 a[156:159], v5 offset:15360\n"
"v_mfma_f32_16x16x32_bf16 a[24:27], a[80:83], a[152:155], a[24:27]\n"
"s_waitcnt vmcnt(9) lgkmcnt(11)\n"
"ds_read_b128 a[152:155], v5 offset:14336\n"
"v_mfma_f32_16x16x32_bf16 v[32:35], a[32:35], a[96:99], v[32:35]\n"
"v_mfma_f32_16x16x32_bf16 v[32:35], a[36:39], a[100:103], v[32:35]\n"
"global_load_dwordx4 a[80:83], v0, s[34:35] offset:0 sc1\n"
"v_mfma_f32_16x16x32_bf16 v[60:63], a[44:47], a[100:103], v[60:63]\n"
"v_mfma_f32_16x16x32_bf16 v[60:63], a[40:43], a[96:99], v[60:63]\n"
"v_mfma_f32_16x16x32_bf16 v[64:67], a[40:43], a[104:107], v[64:67]\n"
"v_mfma_f32_16x16x32_bf16 v[64:67], a[44:47], a[108:111], v[64:67]\n"
"v_mfma_f32_16x16x32_bf16 v[36:39], a[36:39], a[108:111], v[36:39]\n"
"v_mfma_f32_16x16x32_bf16 v[36:39], a[32:35], a[104:107], v[36:39]\n"
"s_waitcnt lgkmcnt(8)\n"
"v_mfma_f32_16x16x32_bf16 v[40:43], a[32:35], a[112:115], v[40:43]\n"
"v_mfma_f32_16x16x32_bf16 v[40:43], a[36:39], a[116:119], v[40:43]\n"
"global_load_dwordx4 a[84:87], v0, s[34:35] offset:1024 sc1\n"
"v_mfma_f32_16x16x32_bf16 v[68:71], a[44:47], a[116:119], v[68:71]\n"
"v_mfma_f32_16x16x32_bf16 v[68:71], a[40:43], a[112:115], v[68:71]\n"
"v_mfma_f32_16x16x32_bf16 v[72:75], a[40:43], a[120:123], v[72:75]\n"
"v_mfma_f32_16x16x32_bf16 v[72:75], a[44:47], a[124:127], v[72:75]\n"
"v_mfma_f32_16x16x32_bf16 v[44:47], a[36:39], a[124:127], v[44:47]\n"
"v_mfma_f32_16x16x32_bf16 v[44:47], a[32:35], a[120:123], v[44:47]\n"
"s_waitcnt lgkmcnt(4)\n"
"v_mfma_f32_16x16x32_bf16 v[48:51], a[32:35], a[128:131], v[48:51]\n"
"s_lshr_b32 s66, 20, s62\n"
"v_mfma_f32_16x16x32_bf16 v[48:51], a[36:39], a[132:135], v[48:51]\n"
"global_load_dwordx4 a[88:91], v0, s[34:35] offset:2048 sc1\n"
"v_mfma_f32_16x16x32_bf16 v[76:79], a[44:47], a[132:135], v[76:79]\n"
"s_and_b32 s66, s66, 1\n"
"s_cselect_b32 s70, 4294574080, 0\n"
"s_cselect_b32 s71, -1, 0\n"
"s_cselect_b32 s66, 0, 131072\n"
"v_mfma_f32_16x16x32_bf16 v[76:79], a[40:43], a[128:131], v[76:79]\n"
"v_mfma_f32_16x16x32_bf16 v[80:83], a[40:43], a[136:139], v[80:83]\n"
"s_add_u32 s70, s70, s66\n"
"v_mfma_f32_16x16x32_bf16 v[80:83], a[44:47], a[140:143], v[80:83]\n"
"v_mfma_f32_16x16x32_bf16 v[52:55], a[36:39], a[140:143], v[52:55]\n"
"v_mfma_f32_16x16x32_bf16 v[52:55], a[32:35], a[136:139], v[52:55]\n"
"s_waitcnt lgkmcnt(0)\n"
"v_mfma_f32_16x16x32_bf16 v[56:59], a[32:35], a[144:147], v[56:59]\n"
"v_mfma_f32_16x16x32_bf16 v[56:59], a[36:39], a[148:151], v[56:59]\n"
"global_load_dwordx4 a[92:95], v0, s[34:35] offset:3072 sc1\n"
"v_mfma_f32_16x16x32_bf16 v[84:87], a[44:47], a[148:151], v[84:87]\n"
"v_mfma_f32_16x16x32_bf16 v[84:87], a[40:43], a[144:147], v[84:87]\n"
"v_mfma_f32_16x16x32_bf16 a[4:7], a[40:43], a[152:155], a[4:7]\n"
"s_add_u32 s34, s34, 8192\n"
"v_mfma_f32_16x16x32_bf16 a[4:7], a[44:47], a[156:159], a[4:7]\n"
"v_mfma_f32_16x16x32_bf16 a[0:3], a[36:39], a[156:159], a[0:3]\n"
"v_mfma_f32_16x16x32_bf16 a[0:3], a[32:35], a[152:155], a[0:3]\n"
"s_waitcnt vmcnt(8)\n"
"v_mfma_f32_16x16x32_bf16 v[88:91], a[48:51], a[96:99], v[88:91]\n"
"v_mfma_f32_16x16x32_bf16 v[88:91], a[52:55], a[100:103], v[88:91]\n"
"global_load_dwordx4 a[32:35], v0, s[34:35] offset:-4096 sc1\n"
"v_mfma_f32_16x16x32_bf16 v[116:119], a[60:63], a[100:103], v[116:119]\n"
"v_mfma_f32_16x16x32_bf16 v[116:119], a[56:59], a[96:99], v[116:119]\n"
"v_mfma_f32_16x16x32_bf16 v[120:123], a[56:59], a[104:107], v[120:123]\n"
"v_mfma_f32_16x16x32_bf16 v[120:123], a[60:63], a[108:111], v[120:123]\n"
"v_mfma_f32_16x16x32_bf16 v[92:95], a[52:55], a[108:111], v[92:95]\n"
"v_mfma_f32_16x16x32_bf16 v[92:95], a[48:51], a[104:107], v[92:95]\n"
"v_mfma_f32_16x16x32_bf16 v[96:99], a[48:51], a[112:115], v[96:99]\n"
"v_mfma_f32_16x16x32_bf16 v[96:99], a[52:55], a[116:119], v[96:99]\n"
"global_load_dwordx4 a[36:39], v0, s[34:35] offset:-3072 sc1\n"
"v_mfma_f32_16x16x32_bf16 v[124:127], a[60:63], a[116:119], v[124:127]\n"
"buffer_load_dwordx4 v[12:15], v0, s[56:59], %25 offen sc1\n"
"v_mfma_f32_16x16x32_bf16 v[124:127], a[56:59], a[112:115], v[124:127]\n"
"v_mfma_f32_16x16x32_bf16 v[128:131], a[56:59], a[120:123], v[128:131]\n"
"v_mfma_f32_16x16x32_bf16 v[128:131], a[60:63], a[124:127], v[128:131]\n"
"v_mfma_f32_16x16x32_bf16 v[100:103], a[52:55], a[124:127], v[100:103]\n"
"v_mfma_f32_16x16x32_bf16 v[100:103], a[48:51], a[120:123], v[100:103]\n"
"v_mfma_f32_16x16x32_bf16 v[104:107], a[48:51], a[128:131], v[104:107]\n"
"v_mfma_f32_16x16x32_bf16 v[104:107], a[52:55], a[132:135], v[104:107]\n"
"global_load_dwordx4 a[40:43], v0, s[34:35] offset:-2048 sc1\n"
"v_mfma_f32_16x16x32_bf16 v[132:135], a[60:63], a[132:135], v[132:135]\n"
"v_mfma_f32_16x16x32_bf16 v[132:135], a[56:59], a[128:131], v[132:135]\n"
"v_mfma_f32_16x16x32_bf16 v[136:139], a[56:59], a[136:139], v[136:139]\n"
"v_mfma_f32_16x16x32_bf16 v[136:139], a[60:63], a[140:143], v[136:139]\n"
"v_mfma_f32_16x16x32_bf16 v[108:111], a[52:55], a[140:143], v[108:111]\n"
"v_mfma_f32_16x16x32_bf16 v[108:111], a[48:51], a[136:139], v[108:111]\n"
"v_mfma_f32_16x16x32_bf16 v[112:115], a[48:51], a[144:147], v[112:115]\n"
"v_mfma_f32_16x16x32_bf16 v[112:115], a[52:55], a[148:151], v[112:115]\n"
"global_load_dwordx4 a[44:47], v0, s[34:35] offset:-1024 sc1\n"
"v_mfma_f32_16x16x32_bf16 v[140:143], a[60:63], a[148:151], v[140:143]\n"
"v_mfma_f32_16x16x32_bf16 v[140:143], a[56:59], a[144:147], v[140:143]\n"
"v_mfma_f32_16x16x32_bf16 a[12:15], a[56:59], a[152:155], a[12:15]\n"
"v_mfma_f32_16x16x32_bf16 a[12:15], a[60:63], a[156:159], a[12:15]\n"
"v_mfma_f32_16x16x32_bf16 a[8:11], a[52:55], a[156:159], a[8:11]\n"
"v_mfma_f32_16x16x32_bf16 a[8:11], a[48:51], a[152:155], a[8:11]\n"
"s_waitcnt vmcnt(9)\n"
"v_mfma_f32_16x16x32_bf16 v[144:147], a[64:67], a[96:99], v[144:147]\n"
"v_mfma_f32_16x16x32_bf16 v[144:147], a[68:71], a[100:103], v[144:147]\n"
"global_load_dwordx4 a[48:51], v0, s[34:35] offset:0 sc1\n"
"v_mfma_f32_16x16x32_bf16 v[172:175], a[76:79], a[100:103], v[172:175]\n"
"v_mfma_f32_16x16x32_bf16 v[172:175], a[72:75], a[96:99], v[172:175]\n"
"v_mfma_f32_16x16x32_bf16 v[176:179], a[72:75], a[104:107], v[176:179]\n"
"v_mfma_f32_16x16x32_bf16 v[176:179], a[76:79], a[108:111], v[176:179]\n"
"v_mfma_f32_16x16x32_bf16 v[148:151], a[68:71], a[108:111], v[148:151]\n"
"v_mfma_f32_16x16x32_bf16 v[148:151], a[64:67], a[104:107], v[148:151]\n"
"v_mfma_f32_16x16x32_bf16 v[152:155], a[64:67], a[112:115], v[152:155]\n"
"v_mfma_f32_16x16x32_bf16 v[152:155], a[68:71], a[116:119], v[152:155]\n"
"global_load_dwordx4 a[52:55], v0, s[34:35] offset:1024 sc1\n"
"v_mfma_f32_16x16x32_bf16 v[180:183], a[76:79], a[116:119], v[180:183]\n"
"v_mfma_f32_16x16x32_bf16 v[180:183], a[72:75], a[112:115], v[180:183]\n"
"v_mfma_f32_16x16x32_bf16 v[184:187], a[72:75], a[120:123], v[184:187]\n"
"v_mfma_f32_16x16x32_bf16 v[184:187], a[76:79], a[124:127], v[184:187]\n"
"v_mfma_f32_16x16x32_bf16 v[156:159], a[68:71], a[124:127], v[156:159]\n"
"v_mfma_f32_16x16x32_bf16 v[156:159], a[64:67], a[120:123], v[156:159]\n"
"v_mfma_f32_16x16x32_bf16 v[160:163], a[64:67], a[128:131], v[160:163]\n"
"v_mfma_f32_16x16x32_bf16 v[160:163], a[68:71], a[132:135], v[160:163]\n"
"global_load_dwordx4 a[56:59], v0, s[34:35] offset:2048 sc1\n"
"v_mfma_f32_16x16x32_bf16 v[188:191], a[76:79], a[132:135], v[188:191]\n"
"v_mfma_f32_16x16x32_bf16 v[188:191], a[72:75], a[128:131], v[188:191]\n"
"v_mfma_f32_16x16x32_bf16 v[192:195], a[72:75], a[136:139], v[192:195]\n"
"v_mfma_f32_16x16x32_bf16 v[192:195], a[76:79], a[140:143], v[192:195]\n"
"v_mfma_f32_16x16x32_bf16 v[164:167], a[68:71], a[140:143], v[164:167]\n"
"s_add_u32 s67, s78, s62\n"
"v_mfma_f32_16x16x32_bf16 v[164:167], a[64:67], a[136:139], v[164:167]\n"
"v_mfma_f32_16x16x32_bf16 v[168:171], a[64:67], a[144:147], v[168:171]\n"
"s_add_u32 s67, s67, 1\n"
"v_mfma_f32_16x16x32_bf16 v[168:171], a[68:71], a[148:151], v[168:171]\n"
"global_load_dwordx4 a[60:63], v0, s[34:35] offset:3072 sc1\n"
"v_mfma_f32_16x16x32_bf16 v[196:199], a[76:79], a[148:151], v[196:199]\n"
"v_mfma_f32_16x16x32_bf16 v[196:199], a[72:75], a[144:147], v[196:199]\n"
"v_mfma_f32_16x16x32_bf16 a[20:23], a[72:75], a[152:155], a[20:23]\n"
"s_add_u32 s34, s34, 8192\n"
"v_mfma_f32_16x16x32_bf16 a[20:23], a[76:79], a[156:159], a[20:23]\n"
"v_mfma_f32_16x16x32_bf16 a[16:19], a[68:71], a[156:159], a[16:19]\n"
"v_mfma_f32_16x16x32_bf16 a[16:19], a[64:67], a[152:155], a[16:19]\n"
"s_waitcnt vmcnt(9)\n"
"v_mfma_f32_16x16x32_bf16 v[200:203], a[80:83], a[96:99], v[200:203]\n"
"v_mfma_f32_16x16x32_bf16 v[200:203], a[84:87], a[100:103], v[200:203]\n"
"global_load_dwordx4 a[64:67], v0, s[34:35] offset:-4096 sc1\n"
"v_mfma_f32_16x16x32_bf16 v[228:231], a[92:95], a[100:103], v[228:231]\n"
"ds_read_b128 a[100:103], v2 offset:17408\n"
"v_mfma_f32_16x16x32_bf16 v[228:231], a[88:91], a[96:99], v[228:231]\n"
"ds_read_b128 a[96:99], v2 offset:16384\n"
"v_mfma_f32_16x16x32_bf16 v[232:235], a[88:91], a[104:107], v[232:235]\n"
"v_mfma_f32_16x16x32_bf16 v[232:235], a[92:95], a[108:111], v[232:235]\n"
"v_mfma_f32_16x16x32_bf16 v[204:207], a[84:87], a[108:111], v[204:207]\n"
"ds_read_b128 a[108:111], v2 offset:19456\n"
"v_mfma_f32_16x16x32_bf16 v[204:207], a[80:83], a[104:107], v[204:207]\n"
"ds_read_b128 a[104:107], v2 offset:18432\n"
"v_mfma_f32_16x16x32_bf16 v[208:211], a[80:83], a[112:115], v[208:211]\n"
"v_mfma_f32_16x16x32_bf16 v[208:211], a[84:87], a[116:119], v[208:211]\n"
"global_load_dwordx4 a[68:71], v0, s[34:35] offset:-3072 sc1\n"
"v_mfma_f32_16x16x32_bf16 v[236:239], a[92:95], a[116:119], v[236:239]\n"
"ds_read_b128 a[116:119], v2 offset:21504\n"
"v_mfma_f32_16x16x32_bf16 v[236:239], a[88:91], a[112:115], v[236:239]\n"
"ds_read_b128 a[112:115], v2 offset:20480\n"
"v_mfma_f32_16x16x32_bf16 v[240:243], a[88:91], a[120:123], v[240:243]\n"
"v_mfma_f32_16x16x32_bf16 v[240:243], a[92:95], a[124:127], v[240:243]\n"
"v_mfma_f32_16x16x32_bf16 v[212:215], a[84:87], a[124:127], v[212:215]\n"
"ds_read_b128 a[124:127], v2 offset:23552\n"
"v_mfma_f32_16x16x32_bf16 v[212:215], a[80:83], a[120:123], v[212:215]\n"
"ds_read_b128 a[120:123], v2 offset:22528\n"
"v_mfma_f32_16x16x32_bf16 v[216:219], a[80:83], a[128:131], v[216:219]\n"
"v_mfma_f32_16x16x32_bf16 v[216:219], a[84:87], a[132:135], v[216:219]\n"
"global_load_dwordx4 a[72:75], v0, s[34:35] offset:-2048 sc1\n"
"v_mfma_f32_16x16x32_bf16 v[244:247], a[92:95], a[132:135], v[244:247]\n"
"ds_read_b128 a[132:135], v2 offset:25600\n"
"v_mfma_f32_16x16x32_bf16 v[244:247], a[88:91], a[128:131], v[244:247]\n"
"ds_read_b128 a[128:131], v2 offset:24576\n"
"v_mfma_f32_16x16x32_bf16 v[248:251], a[88:91], a[136:139], v[248:251]\n"
"v_mfma_f32_16x16x32_bf16 v[248:251], a[92:95], a[140:143], v[248:251]\n"
"v_mfma_f32_16x16x32_bf16 v[220:223], a[84:87], a[140:143], v[220:223]\n"
"ds_read_b128 a[140:143], v2 offset:27648\n"
"v_mfma_f32_16x16x32_bf16 v[220:223], a[80:83], a[136:139], v[220:223]\n"
"ds_read_b128 a[136:139], v2 offset:26624\n"
"v_mfma_f32_16x16x32_bf16 v[224:227], a[80:83], a[144:147], v[224:227]\n"
"v_mfma_f32_16x16x32_bf16 v[224:227], a[84:87], a[148:151], v[224:227]\n"
"global_load_dwordx4 a[76:79], v0, s[34:35] offset:-1024 sc1\n"
"v_mfma_f32_16x16x32_bf16 v[252:255], a[92:95], a[148:151], v[252:255]\n"
"ds_read_b128 a[148:151], v2 offset:29696\n"
"v_mfma_f32_16x16x32_bf16 v[252:255], a[88:91], a[144:147], v[252:255]\n"
"ds_read_b128 a[144:147], v2 offset:28672\n"
"v_mfma_f32_16x16x32_bf16 a[28:31], a[88:91], a[152:155], a[28:31]\n"
"v_mfma_f32_16x16x32_bf16 a[28:31], a[92:95], a[156:159], a[28:31]\n"
"v_mfma_f32_16x16x32_bf16 a[24:27], a[84:87], a[156:159], a[24:27]\n"
"ds_read_b128 a[156:159], v2 offset:31744\n"
"v_mfma_f32_16x16x32_bf16 a[24:27], a[80:83], a[152:155], a[24:27]\n"
"ds_read_b128 a[152:155], v2 offset:30720\n"
"s_waitcnt vmcnt(8) lgkmcnt(12)\n"
"v_mfma_f32_16x16x32_bf16 v[32:35], a[32:35], a[96:99], v[32:35]\n"
"v_mfma_f32_16x16x32_bf16 v[32:35], a[36:39], a[100:103], v[32:35]\n"
"global_load_dwordx4 a[80:83], v0, s[34:35] offset:0 sc1\n"
"v_mfma_f32_16x16x32_bf16 v[60:63], a[44:47], a[100:103], v[60:63]\n"
"s_barrier\n"
"v_mfma_f32_16x16x32_bf16 v[60:63], a[40:43], a[96:99], v[60:63]\n"
"s_add_u32 s48, s48, s70\n"
"s_addc_u32 s49, s49, s71\n"
"v_mfma_f32_16x16x32_bf16 v[64:67], a[40:43], a[104:107], v[64:67]\n"
"v_mfma_f32_16x16x32_bf16 v[64:67], a[44:47], a[108:111], v[64:67]\n"
"v_mfma_f32_16x16x32_bf16 v[36:39], a[36:39], a[108:111], v[36:39]\n"
"s_add_u32 s52, s52, s70\n"
"s_addc_u32 s53, s53, s71\n"
"v_subrev_u32 v12, s67, v12\n"
"v_mfma_f32_16x16x32_bf16 v[36:39], a[32:35], a[104:107], v[36:39]\n"
"s_waitcnt lgkmcnt(8)\n"
"v_xor_b32 v13, s79, v13\n"
"v_mfma_f32_16x16x32_bf16 v[40:43], a[32:35], a[112:115], v[40:43]\n"
"s_add_u32 s54, s54, s70\n"
"s_addc_u32 s55, s55, s71\n"
"v_subrev_u32 v14, s67, v14\n"
"v_mfma_f32_16x16x32_bf16 v[40:43], a[36:39], a[116:119], v[40:43]\n"
"global_load_dwordx4 a[84:87], v0, s[34:35] offset:1024 sc1\n"
"v_xor_b32 v15, s79, v15\n"
"v_mfma_f32_16x16x32_bf16 v[68:71], a[44:47], a[116:119], v[68:71]\n"
"v_or_b32 v12, v12, v14\n"
"v_mfma_f32_16x16x32_bf16 v[68:71], a[40:43], a[112:115], v[68:71]\n"
"v_or_b32 v13, v13, v15\n"
"v_mfma_f32_16x16x32_bf16 v[72:75], a[40:43], a[120:123], v[72:75]\n"
"v_or_b32 v12, v12, v13\n"
"v_mfma_f32_16x16x32_bf16 v[72:75], a[44:47], a[124:127], v[72:75]\n"
"s_add_u32 s61, s61, 2048\n"
"v_cmp_lt_u32 vcc, 15, v12\n"
"v_mfma_f32_16x16x32_bf16 v[44:47], a[36:39], a[124:127], v[44:47]\n"
"s_cbranch_vccnz L_slow2\n"
"L_back2:\n"
"v_mfma_f32_16x16x32_bf16 v[44:47], a[32:35], a[120:123], v[44:47]\n"
"s_waitcnt lgkmcnt(4)\n"
"v_mfma_f32_16x16x32_bf16 v[48:51], a[32:35], a[128:131], v[48:51]\n"
"v_lshlrev_b32 v16, 16, v18\n"
"v_mfma_f32_16x16x32_bf16 v[48:51], a[36:39], a[132:135], v[48:51]\n"
"global_load_dwordx4 a[88:91], v0, s[34:35] offset:2048 sc1\n"
"v_and_b32 v17, s63, v18\n"
"v_mfma_f32_16x16x32_bf16 v[76:79], a[44:47], a[132:135], v[76:79]\n"
"v_lshlrev_b32 v18, 16, v19\n"
"v_mfma_f32_16x16x32_bf16 v[76:79], a[40:43], a[128:131], v[76:79]\n"
"v_and_b32 v19, s63, v19\n"
"v_mfma_f32_16x16x32_bf16 v[80:83], a[40:43], a[136:139], v[80:83]\n"
"v_add_f32 v144, v144, v16\n"
"v_add_f32 v145, v145, v17\n"
"v_mfma_f32_16x16x32_bf16 v[80:83], a[44:47], a[140:143], v[80:83]\n"
"v_add_f32 v146, v146, v18\n"
"v_mfma_f32_16x16x32_bf16 v[52:55], a[36:39], a[140:143], v[52:55]\n"
"v_add_f32 v147, v147, v19\n"
"v_mfma_f32_16x16x32_bf16 v[52:55], a[32:35], a[136:139], v[52:55]\n"
"s_waitcnt lgkmcnt(0)\n"
"v_add_f32 v148, v148, v16\n"
"v_mfma_f32_16x16x32_bf16 v[56:59], a[32:35], a[144:147], v[56:59]\n"
"v_add_f32 v149, v149, v17\n"
"v_mfma_f32_16x16x32_bf16 v[56:59], a[36:39], a[148:151], v[56:59]\n"
"global_load_dwordx4 a[92:95], v0, s[34:35] offset:3072 sc1\n"
"v_add_f32 v150, v150, v18\n"
"v_mfma_f32_16x16x32_bf16 v[84:87], a[44:47], a[148:151], v[84:87]\n"
"v_add_f32 v151, v151, v19\n"
"v_mfma_f32_16x16x32_bf16 v[84:87], a[40:43], a[144:147], v[84:87]\n"
"v_add_f32 v152, v152, v16\n"
"v_mfma_f32_16x16x32_bf16 a[4:7], a[40:43], a[152:155], a[4:7]\n"
"s_add_u32 s34, s34, 8192\n"
"v_add_f32 v153, v153, v17\n"
"v_mfma_f32_16x16x32_bf16 a[4:7], a[44:47], a[156:159], a[4:7]\n"
"v_add_f32 v154, v154, v18\n"
"v_mfma_f32_16x16x32_bf16 a[0:3], a[36:39], a[156:159], a[0:3]\n"
"v_add_f32 v155, v155, v19\n"
"v_add_f32 v156, v156, v16\n"
"v_mfma_f32_16x16x32_bf16 a[0:3], a[32:35], a[152:155], a[0:3]\n"
"s_waitcnt vmcnt(8)\n"
"v_add_f32 v157, v157, v17\n"
"v_mfma_f32_16x16x32_bf16 v[88:91], a[48:51], a[96:99], v[88:91]\n"
"v_add_f32 v158, v158, v18\n"
"v_mfma_f32_16x16x32_bf16 v[88:91], a[52:55], a[100:103], v[88:91]\n"
"global_load_dwordx4 a[32:35], v0, s[34:35] offset:-4096 sc1\n"
"v_add_f32 v159, v159, v19\n"
"v_mfma_f32_16x16x32_bf16 v[116:119], a[60:63], a[100:103], v[116:119]\n"
"ds_read_b128 a[100:103], v3 offset:17408\n"
"v_add_f32 v160, v160, v16\n"
"v_mfma_f32_16x16x32_bf16 v[116:119], a[56:59], a[96:99], v[116:119]\n"
"ds_read_b128 a[96:99], v3 offset:16384\n"
"v_add_f32 v161, v161, v17\n"
"v_mfma_f32_16x16x32_bf16 v[120:123], a[56:59], a[104:107], v[120:123]\n"
"v_add_f32 v162, v162, v18\n"
"v_mfma_f32_16x16x32_bf16 v[120:123], a[60:63], a[108:111], v[120:123]\n"
"v_add_f32 v163, v163, v19\n"
"v_mfma_f32_16x16x32_bf16 v[92:95], a[52:55], a[108:111], v[92:95]\n"
"ds_read_b128 a[108:111], v3 offset:19456\n"
"v_add_f32 v164, v164, v16\n"
"v_mfma_f32_16x16x32_bf16 v[92:95], a[48:51], a[104:107], v[92:95]\n"
"ds_read_b128 a[104:107], v3 offset:18432\n"
"v_add_f32 v165, v165, v17\n"
"v_mfma_f32_16x16x32_bf16 v[96:99], a[48:51], a[112:115], v[96:99]\n"
"v_add_f32 v166, v166, v18\n"
"v_add_f32 v167, v167, v19\n"
"v_mfma_f32_16x16x32_bf16 v[96:99], a[52:55], a[116:119], v[96:99]\n"
"global_load_dwordx4 a[36:39], v0, s[34:35] offset:-3072 sc1\n"
"v_add_f32 v168, v168, v16\n"
"v_mfma_f32_16x16x32_bf16 v[124:127], a[60:63], a[116:119], v[124:127]\n"
"ds_read_b128 a[116:119], v3 offset:21504\n"
"v_add_f32 v169, v169, v17\n"
"v_mfma_f32_16x16x32_bf16 v[124:127], a[56:59], a[112:115], v[124:127]\n"
"ds_read_b128 a[112:115], v3 offset:20480\n"
"v_add_f32 v170, v170, v18\n"
"v_mfma_f32_16x16x32_bf16 v[128:131], a[56:59], a[120:123], v[128:131]\n"
"v_add_f32 v171, v171, v19\n"
"v_mfma_f32_16x16x32_bf16 v[128:131], a[60:63], a[124:127], v[128:131]\n"
"v_accvgpr_read_b32 v12, a16\n"
"v_mfma_f32_16x16x32_bf16 v[100:103], a[52:55], a[124:127], v[100:103]\n"
"ds_read_b128 a[124:127], v3 offset:23552\n"
"v_accvgpr_read_b32 v13, a17\n"
"v_mfma_f32_16x16x32_bf16 v[100:103], a[48:51], a[120:123], v[100:103]\n"
"ds_read_b128 a[120:123], v3 offset:22528\n"
"v_accvgpr_read_b32 v14, a18\n"
"v_mfma_f32_16x16x32_bf16 v[104:107], a[48:51], a[128:131], v[104:107]\n"
"v_accvgpr_read_b32 v15, a19\n"
"v_add_f32 v12, v12, v16\n"
"v_mfma_f32_16x16x32_bf16 v[104:107], a[52:55], a[132:135], v[104:107]\n"
"global_load_dwordx4 a[40:43], v0, s[34:35] offset:-2048 sc1\n"
"v_add_f32 v13, v13, v17\n"
"v_mfma_f32_16x16x32_bf16 v[132:135], a[60:63], a[132:135], v[132:135]\n"
"ds_read_b128 a[132:135], v3 offset:25600\n"
"v_add_f32 v14, v14, v18\n"
"v_mfma_f32_16x16x32_bf16 v[132:135], a[56:59], a[128:131], v[132:135]\n"
"ds_read_b128 a[128:131], v3 offset:24576\n"
"v_add_f32 v15, v15, v19\n"
"v_mfma_f32_16x16x32_bf16 v[136:139], a[56:59], a[136:139], v[136:139]\n"
"v_accvgpr_write_b32 a16, v12\n"
"v_mfma_f32_16x16x32_bf16 v[136:139], a[60:63], a[140:143], v[136:139]\n"
"v_accvgpr_write_b32 a17, v13\n"
"v_mfma_f32_16x16x32_bf16 v[108:111], a[52:55], a[140:143], v[108:111]\n"
"ds_read_b128 a[140:143], v3 offset:27648\n"
"v_accvgpr_write_b32 a18, v14\n"
"v_mfma_f32_16x16x32_bf16 v[108:111], a[48:51], a[136:139], v[108:111]\n"
"ds_read_b128 a[136:139], v3 offset:26624\n"
"v_accvgpr_write_b32 a19, v15\n"
"v_mfma_f32_16x16x32_bf16 v[112:115], a[48:51], a[144:147], v[112:115]\n"
"v_lshlrev_b32 v20, 16, v22\n"
"v_and_b32 v21, s63, v22\n"
"v_mfma_f32_16x16x32_bf16 v[112:115], a[52:55], a[148:151], v[112:115]\n"
"global_load_dwordx4 a[44:47], v0, s[34:35] offset:-1024 sc1\n"
"v_lshlrev_b32 v22, 16, v23\n"
"v_mfma_f32_16x16x32_bf16 v[140:143], a[60:63], a[148:151], v[140:143]\n"
"ds_read_b128 a[148:151], v3 offset:29696\n"
"v_and_b32 v23, s63, v23\n"
"v_mfma_f32_16x16x32_bf16 v[140:143], a[56:59], a[144:147], v[140:143]\n"
"ds_read_b128 a[144:147], v3 offset:28672\n"
"v_add_f32 v172, v172, v20\n"
"v_mfma_f32_16x16x32_bf16 a[12:15], a[56:59], a[152:155], a[12:15]\n"
"v_add_f32 v173, v173, v21\n"
"v_mfma_f32_16x16x32_bf16 a[12:15], a[60:63], a[156:159], a[12:15]\n"
"v_add_f32 v174, v174, v22\n"
"v_mfma_f32_16x16x32_bf16 a[8:11], a[52:55], a[156:159], a[8:11]\n"
"ds_read_b128 a[156:159], v3 offset:31744\n"
"v_add_f32 v175, v175, v23\n"
"v_mfma_f32_16x16x32_bf16 a[8:11], a[48:51], a[152:155], a[8:11]\n"
"s_waitcnt vmcnt(8) lgkmcnt(11)\n"
"ds_read_b128 a[152:155], v3 offset:30720\n"
"v_add_f32 v176, v176, v20\n"
"v_mfma_f32_16x16x32_bf16 v[32:35], a[64:67], a[96:99], v[32:35]\n"
"v_add_f32 v177, v177, v21\n"
"v_mfma_f32_16x16x32_bf16 v[32:35], a[68:71], a[100:103], v[32:35]\n"
"global_load_dwordx4 a[48:51], v0, s[34:35] offset:0 sc1\n"
"v_add_f32 v178, v178, v22\n"
"v_mfma_f32_16x16x32_bf16 v[60:63], a[76:79], a[100:103], v[60:63]\n"
"v_add_f32 v179, v179, v23\n"
"v_mfma_f32_16x16x32_bf16 v[60:63], a[72:75], a[96:99], v[60:63]\n"
"v_add_f32 v180, v180, v20\n"
"v_add_f32 v181, v181, v21\n"
"v_mfma_f32_16x16x32_bf16 v[64:67], a[72:75], a[104:107], v[64:67]\n"
"v_add_f32 v182, v182, v22\n"
"v_mfma_f32_16x16x32_bf16 v[64:67], a[76:79], a[108:111], v[64:67]\n"
"v_add_f32 v183, v183, v23\n"
"v_mfma_f32_16x16x32_bf16 v[36:39], a[68:71], a[108:111], v[36:39]\n"
"L_st7932:\n"
"s_cbranch_execz L_st9115\n"
"v_add_f32 v184, v184, v20\n"
"v_mfma_f32_16x16x32_bf16 v[36:39], a[64:67], a[104:107], v[36:39]\n"
"s_waitcnt lgkmcnt(8)\n"
"v_add_f32 v185, v185, v21\n"
"v_mfma_f32_16x16x32_bf16 v[40:43], a[64:67], a[112:115], v[40:43]\n"
"v_add_f32 v186, v186, v22\n"
"v_mfma_f32_16x16x32_bf16 v[40:43], a[68:71], a[116:119], v[40:43]\n"
"global_load_dwordx4 a[52:55], v0, s[34:35] offset:1024 sc1\n"
"v_add_f32 v187, v187, v23\n"
"v_mfma_f32_16x16x32_bf16 v[68:71], a[76:79], a[116:119], v[68:71]\n"
"v_add_f32 v188, v188, v20\n"
"v_mfma_f32_16x16x32_bf16 v[68:71], a[72:75], a[112:115], v[68:71]\n"
"v_add_f32 v189, v189, v21\n"
"v_mfma_f32_16x16x32_bf16 v[72:75], a[72:75], a[120:123], v[72:75]\n"
"v_add_f32 v190, v190, v22\n"
"v_add_f32 v191, v191, v23\n"
"v_mfma_f32_16x16x32_bf16 v[72:75], a[76:79], a[124:127], v[72:75]\n"
"v_add_f32 v192, v192, v20\n"
"v_mfma_f32_16x16x32_bf16 v[44:47], a[68:71], a[124:127], v[44:47]\n"
"v_add_f32 v193, v193, v21\n"
"v_mfma_f32_16x16x32_bf16 v[44:47], a[64:67], a[120:123], v[44:47]\n"
"s_waitcnt lgkmcnt(4)\n"
"v_add_f32 v194, v194, v22\n"
"v_mfma_f32_16x16x32_bf16 v[48:51], a[64:67], a[128:131], v[48:51]\n"
"v_add_f32 v195, v195, v23\n"
"v_mfma_f32_16x16x32_bf16 v[48:51], a[68:71], a[132:135], v[48:51]\n"
"global_load_dwordx4 a[56:59], v0, s[34:35] offset:2048 sc1\n"
"v_add_f32 v196, v196, v20\n"
"v_mfma_f32_16x16x32_bf16 v[76:79], a[76:79], a[132:135], v[76:79]\n"
"v_add_f32 v197, v197, v21\n"
"v_mfma_f32_16x16x32_bf16 v[76:79], a[72:75], a[128:131], v[76:79]\n"
"v_add_f32 v198, v198, v22\n"
"v_mfma_f32_16x16x32_bf16 v[80:83], a[72:75], a[136:139], v[80:83]\n"
"v_add_f32 v199, v199, v23\n"
"v_mfma_f32_16x16x32_bf16 v[80:83], a[76:79], a[140:143], v[80:83]\n"
"v_accvgpr_read_b32 v12, a20\n"
"v_mfma_f32_16x16x32_bf16 v[52:55], a[68:71], a[140:143], v[52:55]\n"
"v_accvgpr_read_b32 v13, a21\n"
"v_accvgpr_read_b32 v14, a22\n"
"v_mfma_f32_16x16x32_bf16 v[52:55], a[64:67], a[136:139], v[52:55]\n"
"s_waitcnt lgkmcnt(0)\n"
"v_accvgpr_read_b32 v15, a23\n"
"v_mfma_f32_16x16x32_bf16 v[56:59], a[64:67], a[144:147], v[56:59]\n"
"v_add_f32 v12, v12, v20\n"
"v_mfma_f32_16x16x32_bf16 v[56:59], a[68:71], a[148:151], v[56:59]\n"
"global_load_dwordx4 a[60:63], v0, s[34:35] offset:3072 sc1\n"
"v_add_f32 v13, v13, v21\n"
"v_mfma_f32_16x16x32_bf16 v[84:87], a[76:79], a[148:151], v[84:87]\n"
"v_add_f32 v14, v14, v22\n"
"v_mfma_f32_16x16x32_bf16 v[84:87], a[72:75], a[144:147], v[84:87]\n"
"v_add_f32 v15, v15, v23\n"
"v_mfma_f32_16x16x32_bf16 a[4:7], a[72:75], a[152:155], a[4:7]\n"
"s_add_u32 s34, s34, 8192\n"
"v_accvgpr_write_b32 a20, v12\n"
"v_mfma_f32_16x16x32_bf16 a[4:7], a[76:79], a[156:159], a[4:7]\n"
"v_accvgpr_write_b32 a21, v13\n"
"v_mfma_f32_16x16x32_bf16 a[0:3], a[68:71], a[156:159], a[0:3]\n"
"v_accvgpr_write_b32 a22, v14\n"
"v_mfma_f32_16x16x32_bf16 a[0:3], a[64:67], a[152:155], a[0:3]\n"
"s_waitcnt vmcnt(8)\n"
"v_accvgpr_write_b32 a23, v15\n"
"v_lshlrev_b32 v24, 16, v26\n"
"v_mfma_f32_16x16x32_bf16 v[88:91], a[80:83], a[96:99], v[88:91]\n"
"v_and_b32 v25, s63, v26\n"
"v_mfma_f32_16x16x32_bf16 v[88:91], a[84:87], a[100:103], v[88:91]\n"
"global_load_dwordx4 a[64:67], v0, s[34:35] offset:-4096 sc1\n"
"v_lshlrev_b32 v26, 16, v27\n"
"v_mfma_f32_16x16x32_bf16 v[116:119], a[92:95], a[100:103], v[116:119]\n"
"ds_read_b128 a[100:103], v4 offset:17408\n"
"v_and_b32 v27, s63, v27\n"
"v_mfma_f32_16x16x32_bf16 v[116:119], a[88:91], a[96:99], v[116:119]\n"
"ds_read_b128 a[96:99], v4 offset:16384\n"
"v_add_f32 v200, v200, v24\n"
"v_mfma_f32_16x16x32_bf16 v[120:123], a[88:91], a[104:107], v[120:123]\n"
"v_add_f32 v201, v201, v25\n"
"v_mfma_f32_16x16x32_bf16 v[120:123], a[92:95], a[108:111], v[120:123]\n"
"v_add_f32 v202, v202, v26\n"
"v_mfma_f32_16x16x32_bf16 v[92:95], a[84:87], a[108:111], v[92:95]\n"
"ds_read_b128 a[108:111], v4 offset:19456\n"
"v_add_f32 v203, v203, v27\n"
"v_mfma_f32_16x16x32_bf16 v[92:95], a[80:83], a[104:107], v[92:95]\n"
"ds_read_b128 a[104:107], v4 offset:18432\n"
"v_add_f32 v204, v204, v24\n"
"v_mfma_f32_16x16x32_bf16 v[96:99], a[80:83], a[112:115], v[96:99]\n"
"v_add_f32 v205, v205, v25\n"
"v_add_f32 v206, v206, v26\n"
"v_mfma_f32_16x16x32_bf16 v[96:99], a[84:87], a[116:119], v[96:99]\n"
"global_load_dwordx4 a[68:71], v0, s[34:35] offset:-3072 sc1\n"
"v_add_f32 v207, v207, v27\n"
"v_mfma_f32_16x16x32_bf16 v[124:127], a[92:95], a[116:119], v[124:127]\n"
"ds_read_b128 a[116:119], v4 offset:21504\n"
"v_add_f32 v208, v208, v24\n"
"v_mfma_f32_16x16x32_bf16 v[124:127], a[88:91], a[112:115], v[124:127]\n"
"ds_read_b128 a[112:115], v4 offset:20480\n"
"v_add_f32 v209, v209, v25\n"
"v_mfma_f32_16x16x32_bf16 v[128:131], a[88:91], a[120:123], v[128:131]\n"
"v_add_f32 v210, v210, v26\n"
"v_mfma_f32_16x16x32_bf16 v[128:131], a[92:95], a[124:127], v[128:131]\n"
"v_add_f32 v211, v211, v27\n"
"v_mfma_f32_16x16x32_bf16 v[100:103], a[84:87], a[124:127], v[100:103]\n"
"ds_read_b128 a[124:127], v4 offset:23552\n"
"v_add_f32 v212, v212, v24\n"
"v_mfma_f32_16x16x32_bf16 v[100:103], a[80:83], a[120:123], v[100:103]\n"
"ds_read_b128 a[120:123], v4 offset:22528\n"
"v_add_f32 v213, v213, v25\n"
"v_mfma_f32_16x16x32_bf16 v[104:107], a[80:83], a[128:131], v[104:107]\n"
"v_add_f32 v214, v214, v26\n"
"v_add_f32 v215, v215, v27\n"
"v_mfma_f32_16x16x32_bf16 v[104:107], a[84:87], a[132:135], v[104:107]\n"
"global_load_dwordx4 a[72:75], v0, s[34:35] offset:-2048 sc1\n"
"v_add_f32 v216, v216, v24\n"
"v_mfma_f32_16x16x32_bf16 v[132:135], a[92:95], a[132:135], v[132:135]\n"
"ds_read_b128 a[132:135], v4 offset:25600\n"
"v_add_f32 v217, v217, v25\n"
"v_mfma_f32_16x16x32_bf16 v[132:135], a[88:91], a[128:131], v[132:135]\n"
"ds_read_b128 a[128:131], v4 offset:24576\n"
"v_add_f32 v218, v218, v26\n"
"v_mfma_f32_16x16x32_bf16 v[136:139], a[88:91], a[136:139], v[136:139]\n"
"v_add_f32 v219, v219, v27\n"
"v_mfma_f32_16x16x32_bf16 v[136:139], a[92:95], a[140:143], v[136:139]\n"
"v_add_f32 v220, v220, v24\n"
"v_mfma_f32_16x16x32_bf16 v[108:111], a[84:87], a[140:143], v[108:111]\n"
"ds_read_b128 a[140:143], v4 offset:27648\n"
"v_add_f32 v221, v221, v25\n"
"v_mfma_f32_16x16x32_bf16 v[108:111], a[80:83], a[136:139], v[108:111]\n"
"ds_read_b128 a[136:139], v4 offset:26624\n"
"v_add_f32 v222, v222, v26\n"
"v_mfma_f32_16x16x32_bf16 v[112:115], a[80:83], a[144:147], v[112:115]\n"
"v_add_f32 v223, v223, v27\n"
"v_mfma_f32_16x16x32_bf16 v[112:115], a[84:87], a[148:151], v[112:115]\n"
"global_load_dwordx4 a[76:79], v0, s[34:35] offset:-1024 sc1\n"
"v_add_f32 v224, v224, v24\n"
"v_mfma_f32_16x16x32_bf16 v[140:143], a[92:95], a[148:151], v[140:143]\n"
"ds_read_b128 a[148:151], v4 offset:29696\n"
"v_add_f32 v225, v225, v25\n"
"v_mfma_f32_16x16x32_bf16 v[140:143], a[88:91], a[144:147], v[140:143]\n"
"ds_read_b128 a[144:147], v4 offset:28672\n"
"v_add_f32 v226, v226, v26\n"
"v_mfma_f32_16x16x32_bf16 a[12:15], a[88:91], a[152:155], a[12:15]\n"
"v_add_f32 v227, v227, v27\n"
"v_accvgpr_read_b32 v12, a24\n"
"v_mfma_f32_16x16x32_bf16 a[12:15], a[92:95], a[156:159], a[12:15]\n"
"v_accvgpr_read_b32 v13, a25\n"
"v_mfma_f32_16x16x32_bf16 a[8:11], a[84:87], a[156:159], a[8:11]\n"
"ds_read_b128 a[156:159], v4 offset:31744\n"
"v_accvgpr_read_b32 v14, a26\n"
"v_mfma_f32_16x16x32_bf16 a[8:11], a[80:83], a[152:155], a[8:11]\n"
"s_waitcnt vmcnt(8) lgkmcnt(11)\n"
"ds_read_b128 a[152:155], v4 offset:30720\n"
"v_accvgpr_read_b32 v15, a27\n"
"v_mfma_f32_16x16x32_bf16 v[32:35], a[32:35], a[96:99], v[32:35]\n"
"v_add_f32 v12, v12, v24\n"
"v_mfma_f32_16x16x32_bf16 v[32:35], a[36:39], a[100:103], v[32:35]\n"
"global_load_dwordx4 a[80:83], v0, s[34:35] offset:0 sc1\n"
"v_add_f32 v13, v13, v25\n"
"v_mfma_f32_16x16x32_bf16 v[60:63], a[44:47], a[100:103], v[60:63]\n"
"v_add_f32 v14, v14, v26\n"
"v_mfma_f32_16x16x32_bf16 v[60:63], a[40:43], a[96:99], v[60:63]\n"
"v_add_f32 v15, v15, v27\n"
"v_mfma_f32_16x16x32_bf16 v[64:67], a[40:43], a[104:107], v[64:67]\n"
"v_accvgpr_write_b32 a24, v12\n"
"v_accvgpr_write_b32 a25, v13\n"
"v_mfma_f32_16x16x32_bf16 v[64:67], a[44:47], a[108:111], v[64:67]\n"
"v_accvgpr_write_b32 a26, v14\n"
"v_mfma_f32_16x16x32_bf16 v[36:39], a[36:39], a[108:111], v[36:39]\n"
"v_accvgpr_write_b32 a27, v15\n"
"v_mfma_f32_16x16x32_bf16 v[36:39], a[32:35], a[104:107], v[36:39]\n"
"s_waitcnt lgkmcnt(8)\n"
"v_lshlrev_b32 v28, 16, v30\n"
"v_mfma_f32_16x16x32_bf16 v[40:43], a[32:35], a[112:115], v[40:43]\n"
"v_and_b32 v29, s63, v30\n"
"v_mfma_f32_16x16x32_bf16 v[40:43], a[36:39], a[116:119], v[40:43]\n"
"global_load_dwordx4 a[84:87], v0, s[34:35] offset:1024 sc1\n"
"v_lshlrev_b32 v30, 16, v31\n"
"v_mfma_f32_16x16x32_bf16 v[68:71], a[44:47], a[116:119], v[68:71]\n"
"v_and_b32 v31, s63, v31\n"
"v_mfma_f32_16x16x32_bf16 v[68:71], a[40:43], a[112:115], v[68:71]\n"
"v_add_f32 v228, v228, v28\n"
"v_mfma_f32_16x16x32_bf16 v[72:75], a[40:43], a[120:123], v[72:75]\n"
"v_add_f32 v229, v229, v29\n"
"v_mfma_f32_16x16x32_bf16 v[72:75], a[44:47], a[124:127], v[72:75]\n"
"v_add_f32 v230, v230, v30\n"
"v_mfma_f32_16x16x32_bf16 v[44:47], a[36:39], a[124:127], v[44:47]\n"
"v_add_f32 v231, v231, v31\n"
"v_add_f32 v232, v232, v28\n"
"v_mfma_f32_16x16x32_bf16 v[44:47], a[32:35], a[120:123], v[44:47]\n"
"s_waitcnt lgkmcnt(4)\n"
"v_add_f32 v233, v233, v29\n"
"v_mfma_f32_16x16x32_bf16 v[48:51], a[32:35], a[128:131], v[48:51]\n"
"v_add_f32 v234, v234, v30\n"
"v_mfma_f32_16x16x32_bf16 v[48:51], a[36:39], a[132:135], v[48:51]\n"
"global_load_dwordx4 a[88:91], v0, s[34:35] offset:2048 sc1\n"
"v_add_f32 v235, v235, v31\n"
"v_mfma_f32_16x16x32_bf16 v[76:79], a[44:47], a[132:135], v[76:79]\n"
"v_add_f32 v236, v236, v28\n"
"v_mfma_f32_16x16x32_bf16 v[76:79], a[40:43], a[128:131], v[76:79]\n"
"v_add_f32 v237, v237, v29\n"
"v_mfma_f32_16x16x32_bf16 v[80:83], a[40:43], a[136:139], v[80:83]\n"
"v_add_f32 v238, v238, v30\n"
"v_mfma_f32_16x16x32_bf16 v[80:83], a[44:47], a[140:143], v[80:83]\n"
"v_add_f32 v239, v239, v31\n"
"v_mfma_f32_16x16x32_bf16 v[52:55], a[36:39], a[140:143], v[52:55]\n"
"v_add_f32 v240, v240, v28\n"
"v_mfma_f32_16x16x32_bf16 v[52:55], a[32:35], a[136:139], v[52:55]\n"
"s_waitcnt lgkmcnt(0)\n"
"v_add_f32 v241, v241, v29\n"
"v_add_f32 v242, v242, v30\n"
"v_mfma_f32_16x16x32_bf16 v[56:59], a[32:35], a[144:147], v[56:59]\n"
"v_add_f32 v243, v243, v31\n"
"v_mfma_f32_16x16x32_bf16 v[56:59], a[36:39], a[148:151], v[56:59]\n"
"global_load_dwordx4 a[92:95], v0, s[34:35] offset:3072 sc1\n"
"v_add_f32 v244, v244, v28\n"
"v_mfma_f32_16x16x32_bf16 v[84:87], a[44:47], a[148:151], v[84:87]\n"
"v_add_f32 v245, v245, v29\n"
"v_mfma_f32_16x16x32_bf16 v[84:87], a[40:43], a[144:147], v[84:87]\n"
"v_add_f32 v246, v246, v30\n"
"v_mfma_f32_16x16x32_bf16 a[4:7], a[40:43], a[152:155], a[4:7]\n"
"s_add_u32 s34, s34, 8192\n"
"v_add_f32 v247, v247, v31\n"
"v_mfma_f32_16x16x32_bf16 a[4:7], a[44:47], a[156:159], a[4:7]\n"
"v_add_f32 v248, v248, v28\n"
"v_mfma_f32_16x16x32_bf16 a[0:3], a[36:39], a[156:159], a[0:3]\n"
"v_add_f32 v249, v249, v29\n"
"v_mfma_f32_16x16x32_bf16 a[0:3], a[32:35], a[152:155], a[0:3]\n"
"s_waitcnt vmcnt(8)\n"
"v_add_f32 v250, v250, v30\n"
"v_mfma_f32_16x16x32_bf16 v[88:91], a[48:51], a[96:99], v[88:91]\n"
"v_add_f32 v251, v251, v31\n"
"v_add_f32 v252, v252, v28\n"
"v_mfma_f32_16x16x32_bf16 v[88:91], a[52:55], a[100:103], v[88:91]\n"
"global_load_dwordx4 a[32:35], v0, s[34:35] offset:-4096 sc1\n"
"v_add_f32 v253, v253, v29\n"
"v_mfma_f32_16x16x32_bf16 v[116:119], a[60:63], a[100:103], v[116:119]\n"
"ds_read_b128 a[100:103], v5 offset:17408\n"
"v_add_f32 v254, v254, v30\n"
"v_mfma_f32_16x16x32_bf16 v[116:119], a[56:59], a[96:99], v[116:119]\n"
"ds_read_b128 a[96:99], v5 offset:16384\n"
"v_add_f32 v255, v255, v31\n"
"v_mfma_f32_16x16x32_bf16 v[120:123], a[56:59], a[104:107], v[120:123]\n"
"v_accvgpr_read_b32 v12, a28\n"
"v_mfma_f32_16x16x32_bf16 v[120:123], a[60:63], a[108:111], v[120:123]\n"
"v_accvgpr_read_b32 v13, a29\n"
"v_mfma_f32_16x16x32_bf16 v[92:95], a[52:55], a[108:111], v[92:95]\n"
"ds_read_b128 a[108:111], v5 offset:19456\n"
"v_accvgpr_read_b32 v14, a30\n"
"v_mfma_f32_16x16x32_bf16 v[92:95], a[48:51], a[104:107], v[92:95]\n"
"ds_read_b128 a[104:107], v5 offset:18432\n"
"v_accvgpr_read_b32 v15, a31\n"
"v_mfma_f32_16x16x32_bf16 v[96:99], a[48:51], a[112:115], v[96:99]\n"
"v_add_f32 v12, v12, v28\n"
"v_add_f32 v13, v13, v29\n"
"v_mfma_f32_16x16x32_bf16 v[96:99], a[52:55], a[116:119], v[96:99]\n"
"global_load_dwordx4 a[36:39], v0, s[34:35] offset:-3072 sc1\n"
"v_add_f32 v14, v14, v30\n"
"v_mfma_f32_16x16x32_bf16 v[124:127], a[60:63], a[116:119], v[124:127]\n"
"ds_read_b128 a[116:119], v5 offset:21504\n"
"v_add_f32 v15, v15, v31\n"
"v_mfma_f32_16x16x32_bf16 v[124:127], a[56:59], a[112:115], v[124:127]\n"
"ds_read_b128 a[112:115], v5 offset:20480\n"
"v_accvgpr_write_b32 a28, v12\n"
"v_mfma_f32_16x16x32_bf16 v[128:131], a[56:59], a[120:123], v[128:131]\n"
"v_accvgpr_write_b32 a29, v13\n"
"v_mfma_f32_16x16x32_bf16 v[128:131], a[60:63], a[124:127], v[128:131]\n"
"v_accvgpr_write_b32 a30, v14\n"
"v_mfma_f32_16x16x32_bf16 v[100:103], a[52:55], a[124:127], v[100:103]\n"
"ds_read_b128 a[124:127], v5 offset:23552\n"
"v_accvgpr_write_b32 a31, v15\n"
"v_mfma_f32_16x16x32_bf16 v[100:103], a[48:51], a[120:123], v[100:103]\n"
"ds_read_b128 a[120:123], v5 offset:22528\n"
"v_mfma_f32_16x16x32_bf16 v[104:107], a[48:51], a[128:131], v[104:107]\n"
"v_mfma_f32_16x16x32_bf16 v[104:107], a[52:55], a[132:135], v[104:107]\n"
"global_load_dwordx4 a[40:43], v0, s[34:35] offset:-2048 sc1\n"
"v_mfma_f32_16x16x32_bf16 v[132:135], a[60:63], a[132:135], v[132:135]\n"
"ds_read_b128 a[132:135], v5 offset:25600\n"
"v_mfma_f32_16x16x32_bf16 v[132:135], a[56:59], a[128:131], v[132:135]\n"
"ds_read_b128 a[128:131], v5 offset:24576\n"
"v_mfma_f32_16x16x32_bf16 v[136:139], a[56:59], a[136:139], v[136:139]\n"
"ds_read_b128 v[16:19], v7 offset:32768\n"
"v_mfma_f32_16x16x32_bf16 v[136:139], a[60:63], a[140:143], v[136:139]\n"
"v_mfma_f32_16x16x32_bf16 v[108:111], a[52:55], a[140:143], v[108:111]\n"
"ds_read_b128 a[140:143], v5 offset:27648\n"
"v_mfma_f32_16x16x32_bf16 v[108:111], a[48:51], a[136:139], v[108:111]\n"
"ds_read_b128 a[136:139], v5 offset:26624\n"
"v_mfma_f32_16x16x32_bf16 v[112:115], a[48:51], a[144:147], v[112:115]\n"
"v_mfma_f32_16x16x32_bf16 v[112:115], a[52:55], a[148:151], v[112:115]\n"
"global_load_dwordx4 a[44:47], v0, s[34:35] offset:-1024 sc1\n"
"v_mfma_f32_16x16x32_bf16 v[140:143], a[60:63], a[148:151], v[140:143]\n"
"ds_read_b128 a[148:151], v5 offset:29696\n"
"v_mfma_f32_16x16x32_bf16 v[140:143], a[56:59], a[144:147], v[140:143]\n"
"ds_read_b128 a[144:147], v5 offset:28672\n"
"v_mfma_f32_16x16x32_bf16 a[12:15], a[56:59], a[152:155], a[12:15]\n"
"v_mfma_f32_16x16x32_bf16 a[12:15], a[60:63], a[156:159], a[12:15]\n"
"v_mfma_f32_16x16x32_bf16 a[8:11], a[52:55], a[156:159], a[8:11]\n"
"ds_read_b128 a[156:159], v5 offset:31744\n"
"v_mfma_f32_16x16x32_bf16 a[8:11], a[48:51], a[152:155], a[8:11]\n"
"ds_read_b128 a[152:155], v5 offset:30720\n"
"s_branch L_P6\n"
"L_P3F:\n"
"L_st8230:\n"
"s_cbranch_execz L_retB\n"
"s_waitcnt vmcnt(10) lgkmcnt(13)\n"
"v_mfma_f32_16x16x32_bf16 v[32:35], a[32:35], a[96:99], v[32:35]\n"
"s_waitcnt lgkmcnt(6)\n"
"global_store_dwordx4 v0, v[24:27], s[52:53] offset:2048 sc1\n"
"s_cmp_lt_u32 s27, 12582912\n"
"s_cselect_b32 s38, 8192000, 0\n"
"s_cselect_b32 s42, 12582144, 0\n"
"v_mfma_f32_16x16x32_bf16 v[32:35], a[36:39], a[100:103], v[32:35]\n"
"global_load_dwordx4 a[80:83], v0, s[34:35] offset:0 sc1\n"
"v_mfma_f32_16x16x32_bf16 v[60:63], a[44:47], a[100:103], v[60:63]\n"
"s_barrier\n"
"v_mfma_f32_16x16x32_bf16 v[60:63], a[40:43], a[96:99], v[60:63]\n"
"s_add_u32 m0, s64, 12288\n"
"v_mfma_f32_16x16x32_bf16 v[64:67], a[40:43], a[104:107], v[64:67]\n"
"v_mfma_f32_16x16x32_bf16 v[64:67], a[44:47], a[108:111], v[64:67]\n"
"s_add_u32 s67, s27, 0\n"
"v_mfma_f32_16x16x32_bf16 v[36:39], a[36:39], a[108:111], v[36:39]\n"
"v_mfma_f32_16x16x32_bf16 v[36:39], a[32:35], a[104:107], v[36:39]\n"
"v_mfma_f32_16x16x32_bf16 v[40:43], a[32:35], a[112:115], v[40:43]\n"
"buffer_load_dwordx4 v6, s[40:43], s67 offen offset:0 lds\n"
"v_mfma_f32_16x16x32_bf16 v[40:43], a[36:39], a[116:119], v[40:43]\n"
"global_load_dwordx4 a[84:87], v0, s[34:35] offset:1024 sc1\n"
"v_mfma_f32_16x16x32_bf16 v[68:71], a[44:47], a[116:119], v[68:71]\n"
"v_mfma_f32_16x16x32_bf16 v[68:71], a[40:43], a[112:115], v[68:71]\n"
"s_add_u32 s67, s27, 15360\n"
"v_mfma_f32_16x16x32_bf16 v[72:75], a[40:43], a[120:123], v[72:75]\n"
"v_mfma_f32_16x16x32_bf16 v[72:75], a[44:47], a[124:127], v[72:75]\n"
"v_mfma_f32_16x16x32_bf16 v[44:47], a[36:39], a[124:127], v[44:47]\n"
"buffer_load_dwordx4 v6, s[40:43], s67 offen offset:1024 lds\n"
"v_mfma_f32_16x16x32_bf16 v[44:47], a[32:35], a[120:123], v[44:47]\n"
"s_waitcnt lgkmcnt(4)\n"
"v_mfma_f32_16x16x32_bf16 v[48:51], a[32:35], a[128:131], v[48:51]\n"
"v_mfma_f32_16x16x32_bf16 v[48:51], a[36:39], a[132:135], v[48:51]\n"
"global_load_dwordx4 a[88:91], v0, s[34:35] offset:2048 sc1\n"
"v_lshrrev_b32 v16, 4, v0\n"
"v_mfma_f32_16x16x32_bf16 v[76:79], a[44:47], a[132:135], v[76:79]\n"
"s_add_u32 s67, s27, 30720\n"
"v_and_b32 v16, 0x30, v16\n"
"v_mfma_f32_16x16x32_bf16 v[76:79], a[40:43], a[128:131], v[76:79]\n"
"ds_read_b128 v[28:31], v2 offset:23552\n"
"buffer_load_dwordx2 v[18:19], v16, s[44:47], s61 offen offset:0\n"
"v_mfma_f32_16x16x32_bf16 v[80:83], a[40:43], a[136:139], v[80:83]\n"
"buffer_load_dwordx2 v[8:9], off, s[56:59], %14 sc1\n"
"v_mfma_f32_16x16x32_bf16 v[80:83], a[44:47], a[140:143], v[80:83]\n"
"buffer_load_dwordx4 v6, s[40:43], s67 offen offset:2048 lds\n"
"v_mfma_f32_16x16x32_bf16 v[52:55], a[36:39], a[140:143], v[52:55]\n"
"v_mfma_f32_16x16x32_bf16 v[52:55], a[32:35], a[136:139], v[52:55]\n"
"s_waitcnt lgkmcnt(1)\n"
"v_mfma_f32_16x16x32_bf16 v[56:59], a[32:35], a[144:147], v[56:59]\n"
"s_add_u32 s67, s27, 46080\n"
"v_mfma_f32_16x16x32_bf16 v[56:59], a[36:39], a[148:151], v[56:59]\n"
"global_load_dwordx4 a[92:95], v0, s[34:35] offset:3072 sc1\n"
"v_lshrrev_b32 v20, 4, v0\n"
"v_mfma_f32_16x16x32_bf16 v[84:87], a[44:47], a[148:151], v[84:87]\n"
"v_and_b32 v20, 0x30, v20\n"
"v_mfma_f32_16x16x32_bf16 v[84:87], a[40:43], a[144:147], v[84:87]\n"
"buffer_load_dwordx4 v6, s[40:43], s67 offen offset:3072 lds\n"
"buffer_load_dwordx2 v[22:23], v20, s[44:47], s61 offen offset:8\n"
"v_mfma_f32_16x16x32_bf16 a[4:7], a[40:43], a[152:155], a[4:7]\n"
"s_add_u32 s34, s34, 8192\n"
"v_mfma_f32_16x16x32_bf16 a[4:7], a[44:47], a[156:159], a[4:7]\n"
"v_mfma_f32_16x16x32_bf16 a[0:3], a[36:39], a[156:159], a[0:3]\n"
"s_waitcnt lgkmcnt(0)\n"
"global_store_dwordx4 v0, v[28:31], s[52:53] offset:3072 sc1\n"
"v_mfma_f32_16x16x32_bf16 a[0:3], a[32:35], a[152:155], a[0:3]\n"
"s_waitcnt vmcnt(18)\n"
"v_mfma_f32_16x16x32_bf16 v[88:91], a[48:51], a[96:99], v[88:91]\n"
"v_mfma_f32_16x16x32_bf16 v[88:91], a[52:55], a[100:103], v[88:91]\n"
"global_load_dwordx4 a[32:35], v0, s[34:35] offset:-4096 sc1\n"
"v_mfma_f32_16x16x32_bf16 v[116:119], a[60:63], a[100:103], v[116:119]\n"
"v_mfma_f32_16x16x32_bf16 v[116:119], a[56:59], a[96:99], v[116:119]\n"
"v_mfma_f32_16x16x32_bf16 v[120:123], a[56:59], a[104:107], v[120:123]\n"
"v_mfma_f32_16x16x32_bf16 v[120:123], a[60:63], a[108:111], v[120:123]\n"
"v_mfma_f32_16x16x32_bf16 v[92:95], a[52:55], a[108:111], v[92:95]\n"
"v_mfma_f32_16x16x32_bf16 v[92:95], a[48:51], a[104:107], v[92:95]\n"
"v_mfma_f32_16x16x32_bf16 v[96:99], a[48:51], a[112:115], v[96:99]\n"
"v_mfma_f32_16x16x32_bf16 v[96:99], a[52:55], a[116:119], v[96:99]\n"
"global_load_dwordx4 a[36:39], v0, s[34:35] offset:-3072 sc1\n"
"v_mfma_f32_16x16x32_bf16 v[124:127], a[60:63], a[116:119], v[124:127]\n"
"v_mfma_f32_16x16x32_bf16 v[124:127], a[56:59], a[112:115], v[124:127]\n"
"v_mfma_f32_16x16x32_bf16 v[128:131], a[56:59], a[120:123], v[128:131]\n"
"v_mfma_f32_16x16x32_bf16 v[128:131], a[60:63], a[124:127], v[128:131]\n"
"v_mfma_f32_16x16x32_bf16 v[100:103], a[52:55], a[124:127], v[100:103]\n"
"v_mfma_f32_16x16x32_bf16 v[100:103], a[48:51], a[120:123], v[100:103]\n"
"v_mfma_f32_16x16x32_bf16 v[104:107], a[48:51], a[128:131], v[104:107]\n"
"ds_read_b128 v[24:27], v2 offset:24576\n"
"v_mfma_f32_16x16x32_bf16 v[104:107], a[52:55], a[132:135], v[104:107]\n"
"global_load_dwordx4 a[40:43], v0, s[34:35] offset:-2048 sc1\n"
"v_mfma_f32_16x16x32_bf16 v[132:135], a[60:63], a[132:135], v[132:135]\n"
"v_mfma_f32_16x16x32_bf16 v[132:135], a[56:59], a[128:131], v[132:135]\n"
"v_mfma_f32_16x16x32_bf16 v[136:139], a[56:59], a[136:139], v[136:139]\n"
"v_mfma_f32_16x16x32_bf16 v[136:139], a[60:63], a[140:143], v[136:139]\n"
"v_mfma_f32_16x16x32_bf16 v[108:111], a[52:55], a[140:143], v[108:111]\n"
"v_mfma_f32_16x16x32_bf16 v[108:111], a[48:51], a[136:139], v[108:111]\n"
"v_mfma_f32_16x16x32_bf16 v[112:115], a[48:51], a[144:147], v[112:115]\n"
"v_mfma_f32_16x16x32_bf16 v[112:115], a[52:55], a[148:151], v[112:115]\n"
"global_load_dwordx4 a[44:47], v0, s[34:35] offset:-1024 sc1\n"
"v_mfma_f32_16x16x32_bf16 v[140:143], a[60:63], a[148:151], v[140:143]\n"
"v_mfma_f32_16x16x32_bf16 v[140:143], a[56:59], a[144:147], v[140:143]\n"
"v_mfma_f32_16x16x32_bf16 a[12:15], a[56:59], a[152:155], a[12:15]\n"
"s_waitcnt lgkmcnt(0)\n"
"global_store_dwordx4 v0, v[24:27], s[54:55] offset:-4096 sc1\n"
"v_mfma_f32_16x16x32_bf16 a[12:15], a[60:63], a[156:159], a[12:15]\n"
"v_mfma_f32_16x16x32_bf16 a[8:11], a[52:55], a[156:159], a[8:11]\n"
"v_mfma_f32_16x16x32_bf16 a[8:11], a[48:51], a[152:155], a[8:11]\n"
"s_waitcnt vmcnt(18)\n"
"v_mfma_f32_16x16x32_bf16 v[144:147], a[64:67], a[96:99], v[144:147]\n"
"v_mfma_f32_16x16x32_bf16 v[144:147], a[68:71], a[100:103], v[144:147]\n"
"global_load_dwordx4 a[48:51], v0, s[34:35] offset:0 sc1\n"
"v_mfma_f32_16x16x32_bf16 v[172:175], a[76:79], a[100:103], v[172:175]\n"
"v_mfma_f32_16x16x32_bf16 v[172:175], a[72:75], a[96:99], v[172:175]\n"
"v_mfma_f32_16x16x32_bf16 v[176:179], a[72:75], a[104:107], v[176:179]\n"
"v_mfma_f32_16x16x32_bf16 v[176:179], a[76:79], a[108:111], v[176:179]\n"
"v_mfma_f32_16x16x32_bf16 v[148:151], a[68:71], a[108:111], v[148:151]\n"
"v_mfma_f32_16x16x32_bf16 v[148:151], a[64:67], a[104:107], v[148:151]\n"
"v_mfma_f32_16x16x32_bf16 v[152:155], a[64:67], a[112:115], v[152:155]\n"
"v_mfma_f32_16x16x32_bf16 v[152:155], a[68:71], a[116:119], v[152:155]\n"
"global_load_dwordx4 a[52:55], v0, s[34:35] offset:1024 sc1\n"
"v_mfma_f32_16x16x32_bf16 v[180:183], a[76:79], a[116:119], v[180:183]\n"
"v_mfma_f32_16x16x32_bf16 v[180:183], a[72:75], a[112:115], v[180:183]\n"
"v_mfma_f32_16x16x32_bf16 v[184:187], a[72:75], a[120:123], v[184:187]\n"
"v_mfma_f32_16x16x32_bf16 v[184:187], a[76:79], a[124:127], v[184:187]\n"
"v_mfma_f32_16x16x32_bf16 v[156:159], a[68:71], a[124:127], v[156:159]\n"
"v_mfma_f32_16x16x32_bf16 v[156:159], a[64:67], a[120:123], v[156:159]\n"
"ds_read_b128 v[28:31], v2 offset:26624\n"
"v_mfma_f32_16x16x32_bf16 v[160:163], a[64:67], a[128:131], v[160:163]\n"
"v_mfma_f32_16x16x32_bf16 v[160:163], a[68:71], a[132:135], v[160:163]\n"
"global_load_dwordx4 a[56:59], v0, s[34:35] offset:2048 sc1\n"
"v_mfma_f32_16x16x32_bf16 v[188:191], a[76:79], a[132:135], v[188:191]\n"
"v_mfma_f32_16x16x32_bf16 v[188:191], a[72:75], a[128:131], v[188:191]\n"
"v_mfma_f32_16x16x32_bf16 v[192:195], a[72:75], a[136:139], v[192:195]\n"
"v_mfma_f32_16x16x32_bf16 v[192:195], a[76:79], a[140:143], v[192:195]\n"
"v_mfma_f32_16x16x32_bf16 v[164:167], a[68:71], a[140:143], v[164:167]\n"
"v_mfma_f32_16x16x32_bf16 v[164:167], a[64:67], a[136:139], v[164:167]\n"
"v_mfma_f32_16x16x32_bf16 v[168:171], a[64:67], a[144:147], v[168:171]\n"
"v_mfma_f32_16x16x32_bf16 v[168:171], a[68:71], a[148:151], v[168:171]\n"
"global_load_dwordx4 a[60:63], v0, s[34:35] offset:3072 sc1\n"
"v_mfma_f32_16x16x32_bf16 v[196:199], a[76:79], a[148:151], v[196:199]\n"
"s_waitcnt lgkmcnt(0)\n"
"global_store_dwordx4 v0, v[28:31], s[54:55] offset:-2048 sc1\n"
"v_mfma_f32_16x16x32_bf16 v[196:199], a[72:75], a[144:147], v[196:199]\n"
"v_mfma_f32_16x16x32_bf16 a[20:23], a[72:75], a[152:155], a[20:23]\n"
"s_add_u32 s34, s34, 8192\n"
"v_mfma_f32_16x16x32_bf16 a[20:23], a[76:79], a[156:159], a[20:23]\n"
"v_mfma_f32_16x16x32_bf16 a[16:19], a[68:71], a[156:159], a[16:19]\n"
"v_mfma_f32_16x16x32_bf16 a[16:19], a[64:67], a[152:155], a[16:19]\n"
"s_waitcnt vmcnt(13)\n"
"v_mfma_f32_16x16x32_bf16 v[200:203], a[80:83], a[96:99], v[200:203]\n"
"v_mfma_f32_16x16x32_bf16 v[200:203], a[84:87], a[100:103], v[200:203]\n"
"global_load_dwordx4 a[64:67], v0, s[34:35] offset:-4096 sc1\n"
"v_mfma_f32_16x16x32_bf16 v[228:231], a[92:95], a[100:103], v[228:231]\n"
"ds_read_b128 a[100:103], v3 offset:17408\n"
"v_mfma_f32_16x16x32_bf16 v[228:231], a[88:91], a[96:99], v[228:231]\n"
"ds_read_b128 a[96:99], v3 offset:16384\n"
"v_mfma_f32_16x16x32_bf16 v[232:235], a[88:91], a[104:107], v[232:235]\n"
"v_mfma_f32_16x16x32_bf16 v[232:235], a[92:95], a[108:111], v[232:235]\n"
"v_mfma_f32_16x16x32_bf16 v[204:207], a[84:87], a[108:111], v[204:207]\n"
"ds_read_b128 a[108:111], v3 offset:19456\n"
"v_mfma_f32_16x16x32_bf16 v[204:207], a[80:83], a[104:107], v[204:207]\n"
"ds_read_b128 a[104:107], v3 offset:18432\n"
"v_mfma_f32_16x16x32_bf16 v[208:211], a[80:83], a[112:115], v[208:211]\n"
"v_mfma_f32_16x16x32_bf16 v[208:211], a[84:87], a[116:119], v[208:211]\n"
"global_load_dwordx4 a[68:71], v0, s[34:35] offset:-3072 sc1\n"
"v_mfma_f32_16x16x32_bf16 v[236:239], a[92:95], a[116:119], v[236:239]\n"
"ds_read_b128 a[116:119], v3 offset:21504\n"
"v_mfma_f32_16x16x32_bf16 v[236:239], a[88:91], a[112:115], v[236:239]\n"
"ds_read_b128 a[112:115], v3 offset:20480\n"
"v_mfma_f32_16x16x32_bf16 v[240:243], a[88:91], a[120:123], v[240:243]\n"
"v_mfma_f32_16x16x32_bf16 v[240:243], a[92:95], a[124:127], v[240:243]\n"
"ds_read_b128 v[24:27], v2 offset:29696\n"
"v_mfma_f32_16x16x32_bf16 v[212:215], a[84:87], a[124:127], v[212:215]\n"
"ds_read_b128 a[124:127], v3 offset:23552\n"
"v_mfma_f32_16x16x32_bf16 v[212:215], a[80:83], a[120:123], v[212:215]\n"
"ds_read_b128 a[120:123], v3 offset:22528\n"
"v_mfma_f32_16x16x32_bf16 v[216:219], a[80:83], a[128:131], v[216:219]\n"
"v_mfma_f32_16x16x32_bf16 v[216:219], a[84:87], a[132:135], v[216:219]\n"
"global_load_dwordx4 a[72:75], v0, s[34:35] offset:-2048 sc1\n"
"v_mfma_f32_16x16x32_bf16 v[244:247], a[92:95], a[132:135], v[244:247]\n"
"ds_read_b128 a[132:135], v3 offset:25600\n"
"v_mfma_f32_16x16x32_bf16 v[244:247], a[88:91], a[128:131], v[244:247]\n"
"ds_read_b128 a[128:131], v3 offset:24576\n"
"v_mfma_f32_16x16x32_bf16 v[248:251], a[88:91], a[136:139], v[248:251]\n"
"v_subrev_u32 v8, s65, v8\n"
"v_mfma_f32_16x16x32_bf16 v[248:251], a[92:95], a[140:143], v[248:251]\n"
"v_xor_b32 v9, s79, v9\n"
"v_mfma_f32_16x16x32_bf16 v[220:223], a[84:87], a[140:143], v[220:223]\n"
"ds_read_b128 a[140:143], v3 offset:27648\n"
"v_or_b32 v8, v8, v9\n"
"v_mfma_f32_16x16x32_bf16 v[220:223], a[80:83], a[136:139], v[220:223]\n"
"ds_read_b128 a[136:139], v3 offset:26624\n"
"v_cmp_lt_u32 vcc, 15, v8\n"
"v_mfma_f32_16x16x32_bf16 v[224:227], a[80:83], a[144:147], v[224:227]\n"
"s_waitcnt lgkmcnt(6)\n"
"global_store_dwordx4 v0, v[24:27], s[54:55] offset:1024 sc1\n"
"s_cbranch_vccnz L_slow7\n"
"L_back7:\n"
"v_mfma_f32_16x16x32_bf16 v[224:227], a[84:87], a[148:151], v[224:227]\n"
"global_load_dwordx4 a[76:79], v0, s[34:35] offset:-1024 sc1\n"
"v_readfirstlane_b32 s66, v9\n"
"v_mfma_f32_16x16x32_bf16 v[252:255], a[92:95], a[148:151], v[252:255]\n"
"ds_read_b128 a[148:151], v3 offset:29696\n"
"v_mfma_f32_16x16x32_bf16 v[252:255], a[88:91], a[144:147], v[252:255]\n"
"s_and_b32 s66, s66, 3\n"
"ds_read_b128 a[144:147], v3 offset:28672\n"
"v_mfma_f32_16x16x32_bf16 a[28:31], a[88:91], a[152:155], a[28:31]\n"
"s_lshl_b32 s69, s66, 13\n"
"v_mfma_f32_16x16x32_bf16 a[28:31], a[92:95], a[156:159], a[28:31]\n"
"s_cmp_eq_u32 s66, 3\n"
"s_cselect_b32 s75, 8192, 0\n"
"v_mfma_f32_16x16x32_bf16 a[24:27], a[84:87], a[156:159], a[24:27]\n"
"s_and_b32 s29, s66, s30\n"
"ds_read_b128 a[156:159], v3 offset:31744\n"
"v_mfma_f32_16x16x32_bf16 a[24:27], a[80:83], a[152:155], a[24:27]\n"
"s_waitcnt vmcnt(11)\n"
"ds_read_b128 a[152:155], v3 offset:30720\n"
"v_mfma_f32_16x16x32_bf16 v[32:35], a[32:35], a[96:99], v[32:35]\n"
"s_xor_b32 s31, s79, s66\n"
"v_mfma_f32_16x16x32_bf16 v[32:35], a[36:39], a[100:103], v[32:35]\n"
"global_load_dwordx4 a[80:83], v0, s[34:35] offset:0 sc1\n"
"v_mfma_f32_16x16x32_bf16 v[60:63], a[44:47], a[100:103], v[60:63]\n"
"v_mfma_f32_16x16x32_bf16 v[60:63], a[40:43], a[96:99], v[60:63]\n"
"v_mfma_f32_16x16x32_bf16 v[64:67], a[40:43], a[104:107], v[64:67]\n"
"v_mfma_f32_16x16x32_bf16 v[64:67], a[44:47], a[108:111], v[64:67]\n"
"v_mfma_f32_16x16x32_bf16 v[36:39], a[36:39], a[108:111], v[36:39]\n"
"s_add_u32 m0, s64, 0\n"
"v_mfma_f32_16x16x32_bf16 v[36:39], a[32:35], a[104:107], v[36:39]\n"
"s_waitcnt lgkmcnt(8)\n"
"v_mfma_f32_16x16x32_bf16 v[40:43], a[32:35], a[112:115], v[40:43]\n"
"v_mfma_f32_16x16x32_bf16 v[40:43], a[36:39], a[116:119], v[40:43]\n"
"global_load_dwordx4 a[84:87], v0, s[34:35] offset:1024 sc1\n"
"v_mfma_f32_16x16x32_bf16 v[68:71], a[44:47], a[116:119], v[68:71]\n"
"v_mfma_f32_16x16x32_bf16 v[68:71], a[40:43], a[112:115], v[68:71]\n"
"v_mfma_f32_16x16x32_bf16 v[72:75], a[40:43], a[120:123], v[72:75]\n"
"v_mfma_f32_16x16x32_bf16 v[72:75], a[44:47], a[124:127], v[72:75]\n"
"v_and_b32 v10, 0x300, v0\n"
"v_mfma_f32_16x16x32_bf16 v[44:47], a[36:39], a[124:127], v[44:47]\n"
"v_mfma_f32_16x16x32_bf16 v[44:47], a[32:35], a[120:123], v[44:47]\n"
"s_waitcnt lgkmcnt(4)\n"
"v_mfma_f32_16x16x32_bf16 v[48:51], a[32:35], a[128:131], v[48:51]\n"
"v_and_b32 v11, 0x3c0, v0\n"
"v_mfma_f32_16x16x32_bf16 v[48:51], a[36:39], a[132:135], v[48:51]\n"
"global_load_dwordx4 a[88:91], v0, s[34:35] offset:2048 sc1\n"
"v_mfma_f32_16x16x32_bf16 v[76:79], a[44:47], a[132:135], v[76:79]\n"
"v_mfma_f32_16x16x32_bf16 v[76:79], a[40:43], a[128:131], v[76:79]\n"
"v_mfma_f32_16x16x32_bf16 v[80:83], a[40:43], a[136:139], v[80:83]\n"
"v_add_lshl_u32 v11, v10, v11, 1\n"
"v_mfma_f32_16x16x32_bf16 v[80:83], a[44:47], a[140:143], v[80:83]\n"
"buffer_load_dwordx4 v0, s[48:51], s75 offen offset:0 sc1 lds\n"
"v_mfma_f32_16x16x32_bf16 v[52:55], a[36:39], a[140:143], v[52:55]\n"
"v_mfma_f32_16x16x32_bf16 v[52:55], a[32:35], a[136:139], v[52:55]\n"
"s_waitcnt lgkmcnt(0)\n"
"v_mfma_f32_16x16x32_bf16 v[56:59], a[32:35], a[144:147], v[56:59]\n"
"v_lshrrev_b32 v10, 2, v0\n"
"v_mfma_f32_16x16x32_bf16 v[56:59], a[36:39], a[148:151], v[56:59]\n"
"global_load_dwordx4 a[92:95], v0, s[34:35] offset:3072 sc1\n"
"v_mfma_f32_16x16x32_bf16 v[84:87], a[44:47], a[148:151], v[84:87]\n"
"v_mfma_f32_16x16x32_bf16 v[84:87], a[40:43], a[144:147], v[84:87]\n"
"v_xor_b32 v10, v10, v0\n"
"v_mfma_f32_16x16x32_bf16 a[4:7], a[40:43], a[152:155], a[4:7]\n"
"s_add_u32 s34, s34, 8192\n"
"v_mfma_f32_16x16x32_bf16 a[4:7], a[44:47], a[156:159], a[4:7]\n"
"v_mfma_f32_16x16x32_bf16 a[0:3], a[36:39], a[156:159], a[0:3]\n"
"v_mfma_f32_16x16x32_bf16 a[0:3], a[32:35], a[152:155], a[0:3]\n"
"s_waitcnt vmcnt(11)\n"
"v_and_b32 v10, 0x30, v10\n"
"v_mfma_f32_16x16x32_bf16 v[88:91], a[48:51], a[96:99], v[88:91]\n"
"v_mfma_f32_16x16x32_bf16 v[88:91], a[52:55], a[100:103], v[88:91]\n"
"global_load_dwordx4 a[32:35], v0, s[34:35] offset:-4096 sc1\n"
"v_mfma_f32_16x16x32_bf16 v[116:119], a[60:63], a[100:103], v[116:119]\n"
"v_add3_u32 v11, v10, v11, s64\n"
"v_mfma_f32_16x16x32_bf16 v[116:119], a[56:59], a[96:99], v[116:119]\n"
"buffer_load_dwordx4 v0, s[48:51], s75 offen offset:1024 sc1 lds\n"
"v_mfma_f32_16x16x32_bf16 v[120:123], a[56:59], a[104:107], v[120:123]\n"
"v_mfma_f32_16x16x32_bf16 v[120:123], a[60:63], a[108:111], v[120:123]\n"
"v_mfma_f32_16x16x32_bf16 v[92:95], a[52:55], a[108:111], v[92:95]\n"
"s_add_u32 s27, s27, 0x200000\n"
"v_mfma_f32_16x16x32_bf16 v[92:95], a[48:51], a[104:107], v[92:95]\n"
"v_mfma_f32_16x16x32_bf16 v[96:99], a[48:51], a[112:115], v[96:99]\n"
"v_mfma_f32_16x16x32_bf16 v[96:99], a[52:55], a[116:119], v[96:99]\n"
"global_load_dwordx4 a[36:39], v0, s[34:35] offset:-3072 sc1\n"
"v_mfma_f32_16x16x32_bf16 v[124:127], a[60:63], a[116:119], v[124:127]\n"
"ds_read_b64_tr_b16 v[24:25], v11 offset:12288\n"
"v_mfma_f32_16x16x32_bf16 v[124:127], a[56:59], a[112:115], v[124:127]\n"
"v_mfma_f32_16x16x32_bf16 v[128:131], a[56:59], a[120:123], v[128:131]\n"
"ds_read_b64_tr_b16 v[26:27], v11 offset:12864\n"
"v_mfma_f32_16x16x32_bf16 v[128:131], a[60:63], a[124:127], v[128:131]\n"
"L_st8516:\n"
"s_cbranch_execz L_retB\n"
"v_mfma_f32_16x16x32_bf16 v[100:103], a[52:55], a[124:127], v[100:103]\n"
"ds_read_b64_tr_b16 v[28:29], v11 offset:12296\n"
"v_mfma_f32_16x16x32_bf16 v[100:103], a[48:51], a[120:123], v[100:103]\n"
"v_mfma_f32_16x16x32_bf16 v[104:107], a[48:51], a[128:131], v[104:107]\n"
"ds_read_b64_tr_b16 v[30:31], v11 offset:12872\n"
"v_mfma_f32_16x16x32_bf16 v[104:107], a[52:55], a[132:135], v[104:107]\n"
"global_load_dwordx4 a[40:43], v0, s[34:35] offset:-2048 sc1\n"
"v_mfma_f32_16x16x32_bf16 v[132:135], a[60:63], a[132:135], v[132:135]\n"
"buffer_load_dwordx4 v0, s[48:51], s75 offen offset:2048 sc1 lds\n"
"v_mfma_f32_16x16x32_bf16 v[132:135], a[56:59], a[128:131], v[132:135]\n"
"v_mfma_f32_16x16x32_bf16 v[136:139], a[56:59], a[136:139], v[136:139]\n"
"v_mfma_f32_16x16x32_bf16 v[136:139], a[60:63], a[140:143], v[136:139]\n"
"v_mfma_f32_16x16x32_bf16 v[108:111], a[52:55], a[140:143], v[108:111]\n"
"v_mfma_f32_16x16x32_bf16 v[108:111], a[48:51], a[136:139], v[108:111]\n"
"v_mfma_f32_16x16x32_bf16 v[112:115], a[48:51], a[144:147], v[112:115]\n"
"s_waitcnt lgkmcnt(0)\n"
"v_mfma_f32_16x16x32_bf16 v[112:115], a[52:55], a[148:151], v[112:115]\n"
"global_load_dwordx4 a[44:47], v0, s[34:35] offset:-1024 sc1\n"
"v_mfma_f32_16x16x32_bf16 v[140:143], a[60:63], a[148:151], v[140:143]\n"
"v_mfma_f32_16x16x32_bf16 v[140:143], a[56:59], a[144:147], v[140:143]\n"
"v_mfma_f32_16x16x32_bf16 a[12:15], a[56:59], a[152:155], a[12:15]\n"
"v_mfma_f32_16x16x32_bf16 a[12:15], a[60:63], a[156:159], a[12:15]\n"
"v_mfma_f32_16x16x32_bf16 a[8:11], a[52:55], a[156:159], a[8:11]\n"
"v_mfma_f32_16x16x32_bf16 a[8:11], a[48:51], a[152:155], a[8:11]\n"
"s_waitcnt vmcnt(11)\n"
"v_mfma_f32_16x16x32_bf16 v[144:147], a[64:67], a[96:99], v[144:147]\n"
"buffer_store_dwordx4 v[24:27], v0, s[36:39], s28 offen offset:0 sc1\n"
"buffer_load_dwordx4 v0, s[48:51], s75 offen offset:3072 sc1 lds\n"
"v_mfma_f32_16x16x32_bf16 v[144:147], a[68:71], a[100:103], v[144:147]\n"
"global_load_dwordx4 a[48:51], v0, s[34:35] offset:0 sc1\n"
"v_mfma_f32_16x16x32_bf16 v[172:175], a[76:79], a[100:103], v[172:175]\n"
"v_mfma_f32_16x16x32_bf16 v[172:175], a[72:75], a[96:99], v[172:175]\n"
"v_mfma_f32_16x16x32_bf16 v[176:179], a[72:75], a[104:107], v[176:179]\n"
"v_mfma_f32_16x16x32_bf16 v[176:179], a[76:79], a[108:111], v[176:179]\n"
"v_mfma_f32_16x16x32_bf16 v[148:151], a[68:71], a[108:111], v[148:151]\n"
"v_mfma_f32_16x16x32_bf16 v[148:151], a[64:67], a[104:107], v[148:151]\n"
"v_mfma_f32_16x16x32_bf16 v[152:155], a[64:67], a[112:115], v[152:155]\n"
"v_mfma_f32_16x16x32_bf16 v[152:155], a[68:71], a[116:119], v[152:155]\n"
"global_load_dwordx4 a[52:55], v0, s[34:35] offset:1024 sc1\n"
"v_mfma_f32_16x16x32_bf16 v[180:183], a[76:79], a[116:119], v[180:183]\n"
"v_mfma_f32_16x16x32_bf16 v[180:183], a[72:75], a[112:115], v[180:183]\n"
"v_mfma_f32_16x16x32_bf16 v[184:187], a[72:75], a[120:123], v[184:187]\n"
"v_mfma_f32_16x16x32_bf16 v[184:187], a[76:79], a[124:127], v[184:187]\n"
"v_mfma_f32_16x16x32_bf16 v[156:159], a[68:71], a[124:127], v[156:159]\n"
"v_mfma_f32_16x16x32_bf16 v[156:159], a[64:67], a[120:123], v[156:159]\n"
"s_add_u32 m0, s64, 4096\n"
"v_mfma_f32_16x16x32_bf16 v[160:163], a[64:67], a[128:131], v[160:163]\n"
"v_mfma_f32_16x16x32_bf16 v[160:163], a[68:71], a[132:135], v[160:163]\n"
"global_load_dwordx4 a[56:59], v0, s[34:35] offset:2048 sc1\n"
"v_mfma_f32_16x16x32_bf16 v[188:191], a[76:79], a[132:135], v[188:191]\n"
"v_mfma_f32_16x16x32_bf16 v[188:191], a[72:75], a[128:131], v[188:191]\n"
"v_mfma_f32_16x16x32_bf16 v[192:195], a[72:75], a[136:139], v[192:195]\n"
"v_mfma_f32_16x16x32_bf16 v[192:195], a[76:79], a[140:143], v[192:195]\n"
"v_mfma_f32_16x16x32_bf16 v[164:167], a[68:71], a[140:143], v[164:167]\n"
"v_mfma_f32_16x16x32_bf16 v[164:167], a[64:67], a[136:139], v[164:167]\n"
"v_mfma_f32_16x16x32_bf16 v[168:171], a[64:67], a[144:147], v[168:171]\n"
"v_mfma_f32_16x16x32_bf16 v[168:171], a[68:71], a[148:151], v[168:171]\n"
"global_load_dwordx4 a[60:63], v0, s[34:35] offset:3072 sc1\n"
"v_mfma_f32_16x16x32_bf16 v[196:199], a[76:79], a[148:151], v[196:199]\n"
"v_mfma_f32_16x16x32_bf16 v[196:199], a[72:75], a[144:147], v[196:199]\n"
"v_mfma_f32_16x16x32_bf16 a[20:23], a[72:75], a[152:155], a[20:23]\n"
"s_add_u32 s34, s34, 8192\n"
"v_mfma_f32_16x16x32_bf16 a[20:23], a[76:79], a[156:159], a[20:23]\n"
"v_mfma_f32_16x16x32_bf16 a[16:19], a[68:71], a[156:159], a[16:19]\n"
"buffer_load_dwordx4 v1, s[48:51], s75 offen offset:0 sc1 lds\n"
"v_mfma_f32_16x16x32_bf16 a[16:19], a[64:67], a[152:155], a[16:19]\n"
"s_waitcnt vmcnt(13)\n"
"v_mfma_f32_16x16x32_bf16 v[200:203], a[80:83], a[96:99], v[200:203]\n"
"v_mfma_f32_16x16x32_bf16 v[200:203], a[84:87], a[100:103], v[200:203]\n"
"global_load_dwordx4 a[64:67], v0, s[34:35] offset:-4096 sc1\n"
"s_add_u32 s65, s65, 1\n"
"v_mfma_f32_16x16x32_bf16 v[228:231], a[92:95], a[100:103], v[228:231]\n"
"ds_read_b128 a[100:103], v4 offset:17408\n"
"v_mfma_f32_16x16x32_bf16 v[228:231], a[88:91], a[96:99], v[228:231]\n"
"ds_read_b128 a[96:99], v4 offset:16384\n"
"v_mov_b32 v8, s65\n"
"v_mfma_f32_16x16x32_bf16 v[232:235], a[88:91], a[104:107], v[232:235]\n"
"v_mov_b32 v9, s60\n"
"v_mfma_f32_16x16x32_bf16 v[232:235], a[92:95], a[108:111], v[232:235]\n"
"v_mfma_f32_16x16x32_bf16 v[204:207], a[84:87], a[108:111], v[204:207]\n"
"buffer_store_dwordx2 v[8:9], off, s[56:59], %13 sc1\n"
"ds_read_b128 a[108:111], v4 offset:19456\n"
"v_mfma_f32_16x16x32_bf16 v[204:207], a[80:83], a[104:107], v[204:207]\n"
"ds_read_b128 a[104:107], v4 offset:18432\n"
"v_mfma_f32_16x16x32_bf16 v[208:211], a[80:83], a[112:115], v[208:211]\n"
"buffer_store_dwordx4 v[28:31], v0, s[36:39], s28 offen offset:2048 sc1\n"
"buffer_load_dwordx4 v1, s[48:51], s75 offen offset:1024 sc1 lds\n"
"v_mfma_f32_16x16x32_bf16 v[208:211], a[84:87], a[116:119], v[208:211]\n"
"global_load_dwordx4 a[68:71], v0, s[34:35] offset:-3072 sc1\n"
"v_mfma_f32_16x16x32_bf16 v[236:239], a[92:95], a[116:119], v[236:239]\n"
"ds_read_b128 a[116:119], v4 offset:21504\n"
"v_mfma_f32_16x16x32_bf16 v[236:239], a[88:91], a[112:115], v[236:239]\n"
"ds_read_b128 a[112:115], v4 offset:20480\n"
"v_mfma_f32_16x16x32_bf16 v[240:243], a[88:91], a[120:123], v[240:243]\n"
"v_mfma_f32_16x16x32_bf16 v[240:243], a[92:95], a[124:127], v[240:243]\n"
"v_mfma_f32_16x16x32_bf16 v[212:215], a[84:87], a[124:127], v[212:215]\n"
"ds_read_b128 a[124:127], v4 offset:23552\n"
"v_mfma_f32_16x16x32_bf16 v[212:215], a[80:83], a[120:123], v[212:215]\n"
"ds_read_b128 a[120:123], v4 offset:22528\n"
"v_mfma_f32_16x16x32_bf16 v[216:219], a[80:83], a[128:131], v[216:219]\n"
"ds_read_b64_tr_b16 v[24:25], v11 offset:12352\n"
"v_mfma_f32_16x16x32_bf16 v[216:219], a[84:87], a[132:135], v[216:219]\n"
"global_load_dwordx4 a[72:75], v0, s[34:35] offset:-2048 sc1\n"
"ds_read_b64_tr_b16 v[26:27], v11 offset:12800\n"
"v_mfma_f32_16x16x32_bf16 v[244:247], a[92:95], a[132:135], v[244:247]\n"
"ds_read_b128 a[132:135], v4 offset:25600\n"
"v_mfma_f32_16x16x32_bf16 v[244:247], a[88:91], a[128:131], v[244:247]\n"
"ds_read_b128 a[128:131], v4 offset:24576\n"
"v_mfma_f32_16x16x32_bf16 v[248:251], a[88:91], a[136:139], v[248:251]\n"
"ds_read_b64_tr_b16 v[28:29], v11 offset:12360\n"
"v_mfma_f32_16x16x32_bf16 v[248:251], a[92:95], a[140:143], v[248:251]\n"
"ds_read_b64_tr_b16 v[30:31], v11 offset:12808\n"
"v_mfma_f32_16x16x32_bf16 v[220:223], a[84:87], a[140:143], v[220:223]\n"
"ds_read_b128 a[140:143], v4 offset:27648\n"
"v_mfma_f32_16x16x32_bf16 v[220:223], a[80:83], a[136:139], v[220:223]\n"
"ds_read_b128 a[136:139], v4 offset:26624\n"
"v_mfma_f32_16x16x32_bf16 v[224:227], a[80:83], a[144:147], v[224:227]\n"
"v_mfma_f32_16x16x32_bf16 v[224:227], a[84:87], a[148:151], v[224:227]\n"
"global_load_dwordx4 a[76:79], v0, s[34:35] offset:-1024 sc1\n"
"v_mfma_f32_16x16x32_bf16 v[252:255], a[92:95], a[148:151], v[252:255]\n"
"ds_read_b128 a[148:151], v4 offset:29696\n"
"v_mfma_f32_16x16x32_bf16 v[252:255], a[88:91], a[144:147], v[252:255]\n"
"ds_read_b128 a[144:147], v4 offset:28672\n"
"v_mfma_f32_16x16x32_bf16 a[28:31], a[88:91], a[152:155], a[28:31]\n"
"buffer_load_dwordx4 v1, s[48:51], s75 offen offset:2048 sc1 lds\n"
"v_mfma_f32_16x16x32_bf16 a[28:31], a[92:95], a[156:159], a[28:31]\n"
"v_mfma_f32_16x16x32_bf16 a[24:27], a[84:87], a[156:159], a[24:27]\n"
"s_waitcnt lgkmcnt(4)\n"
"ds_read_b128 a[156:159], v4 offset:31744\n"
"v_mfma_f32_16x16x32_bf16 a[24:27], a[80:83], a[152:155], a[24:27]\n"
"s_waitcnt vmcnt(15)\n"
"ds_read_b128 a[152:155], v4 offset:30720\n"
"v_mfma_f32_16x16x32_bf16 v[32:35], a[32:35], a[96:99], v[32:35]\n"
"v_mfma_f32_16x16x32_bf16 v[32:35], a[36:39], a[100:103], v[32:35]\n"
"global_load_dwordx4 a[80:83], v0, s[34:35] offset:0 sc1\n"
"v_mfma_f32_16x16x32_bf16 v[60:63], a[44:47], a[100:103], v[60:63]\n"
"v_mfma_f32_16x16x32_bf16 v[60:63], a[40:43], a[96:99], v[60:63]\n"
"v_mfma_f32_16x16x32_bf16 v[64:67], a[40:43], a[104:107], v[64:67]\n"
"v_mfma_f32_16x16x32_bf16 v[64:67], a[44:47], a[108:111], v[64:67]\n"
"v_mfma_f32_16x16x32_bf16 v[36:39], a[36:39], a[108:111], v[36:39]\n"
"v_mfma_f32_16x16x32_bf16 v[36:39], a[32:35], a[104:107], v[36:39]\n"
"v_mfma_f32_16x16x32_bf16 v[40:43], a[32:35], a[112:115], v[40:43]\n"
"v_mfma_f32_16x16x32_bf16 v[40:43], a[36:39], a[116:119], v[40:43]\n"
"global_load_dwordx4 a[84:87], v0, s[34:35] offset:1024 sc1\n"
"v_mfma_f32_16x16x32_bf16 v[68:71], a[44:47], a[116:119], v[68:71]\n"
"buffer_load_dwordx4 v1, s[48:51], s75 offen offset:3072 sc1 lds\n"
"v_mfma_f32_16x16x32_bf16 v[68:71], a[40:43], a[112:115], v[68:71]\n"
"v_mfma_f32_16x16x32_bf16 v[72:75], a[40:43], a[120:123], v[72:75]\n"
"v_mfma_f32_16x16x32_bf16 v[72:75], a[44:47], a[124:127], v[72:75]\n"
"v_mfma_f32_16x16x32_bf16 v[44:47], a[36:39], a[124:127], v[44:47]\n"
"v_mfma_f32_16x16x32_bf16 v[44:47], a[32:35], a[120:123], v[44:47]\n"
"s_waitcnt lgkmcnt(4)\n"
"v_mfma_f32_16x16x32_bf16 v[48:51], a[32:35], a[128:131], v[48:51]\n"
"v_mfma_f32_16x16x32_bf16 v[48:51], a[36:39], a[132:135], v[48:51]\n"
"global_load_dwordx4 a[88:91], v0, s[34:35] offset:2048 sc1\n"
"v_mfma_f32_16x16x32_bf16 v[76:79], a[44:47], a[132:135], v[76:79]\n"
"v_mfma_f32_16x16x32_bf16 v[76:79], a[40:43], a[128:131], v[76:79]\n"
"v_mfma_f32_16x16x32_bf16 v[80:83], a[40:43], a[136:139], v[80:83]\n"
"v_mfma_f32_16x16x32_bf16 v[80:83], a[44:47], a[140:143], v[80:83]\n"
"v_mfma_f32_16x16x32_bf16 v[52:55], a[36:39], a[140:143], v[52:55]\n"
"v_mfma_f32_16x16x32_bf16 v[52:55], a[32:35], a[136:139], v[52:55]\n"
"s_waitcnt lgkmcnt(0)\n"
"v_mfma_f32_16x16x32_bf16 v[56:59], a[32:35], a[144:147], v[56:59]\n"
"v_mfma_f32_16x16x32_bf16 v[56:59], a[36:39], a[148:151], v[56:59]\n"
"s_add_u32 m0, s64, 8192\n"
"global_load_dwordx4 a[92:95], v0, s[34:35] offset:3072 sc1\n"
"v_mfma_f32_16x16x32_bf16 v[84:87], a[44:47], a[148:151], v[84:87]\n"
"v_mfma_f32_16x16x32_bf16 v[84:87], a[40:43], a[144:147], v[84:87]\n"
"v_mfma_f32_16x16x32_bf16 a[4:7], a[40:43], a[152:155], a[4:7]\n"
"s_add_u32 s34, s34, 8192\n"
"v_mfma_f32_16x16x32_bf16 a[4:7], a[44:47], a[156:159], a[4:7]\n"
"v_mfma_f32_16x16x32_bf16 a[0:3], a[36:39], a[156:159], a[0:3]\n"
"v_mfma_f32_16x16x32_bf16 a[0:3], a[32:35], a[152:155], a[0:3]\n"
"s_waitcnt vmcnt(14)\n"
"v_mfma_f32_16x16x32_bf16 v[88:91], a[48:51], a[96:99], v[88:91]\n"
"v_mfma_f32_16x16x32_bf16 v[88:91], a[52:55], a[100:103], v[88:91]\n"
"global_load_dwordx4 a[32:35], v0, s[34:35] offset:-4096 sc1\n"
"v_mfma_f32_16x16x32_bf16 v[116:119], a[60:63], a[100:103], v[116:119]\n"
"v_mfma_f32_16x16x32_bf16 v[116:119], a[56:59], a[96:99], v[116:119]\n"
"v_mfma_f32_16x16x32_bf16 v[120:123], a[56:59], a[104:107], v[120:123]\n"
"v_mfma_f32_16x16x32_bf16 v[120:123], a[60:63], a[108:111], v[120:123]\n"
"v_mfma_f32_16x16x32_bf16 v[92:95], a[52:55], a[108:111], v[92:95]\n"
"v_mfma_f32_16x16x32_bf16 v[92:95], a[48:51], a[104:107], v[92:95]\n"
"v_mfma_f32_16x16x32_bf16 v[96:99], a[48:51], a[112:115], v[96:99]\n"
"buffer_load_dwordx4 v0, s[48:51], s69 offen offset:0 sc1 lds\n"
"v_mfma_f32_16x16x32_bf16 v[96:99], a[52:55], a[116:119], v[96:99]\n"
"global_load_dwordx4 a[36:39], v0, s[34:35] offset:-3072 sc1\n"
"v_mfma_f32_16x16x32_bf16 v[124:127], a[60:63], a[116:119], v[124:127]\n"
"v_mfma_f32_16x16x32_bf16 v[124:127], a[56:59], a[112:115], v[124:127]\n"
"v_mfma_f32_16x16x32_bf16 v[128:131], a[56:59], a[120:123], v[128:131]\n"
"v_mfma_f32_16x16x32_bf16 v[128:131], a[60:63], a[124:127], v[128:131]\n"
"v_mfma_f32_16x16x32_bf16 v[100:103], a[52:55], a[124:127], v[100:103]\n"
"v_mfma_f32_16x16x32_bf16 v[100:103], a[48:51], a[120:123], v[100:103]\n"
"v_mfma_f32_16x16x32_bf16 v[104:107], a[48:51], a[128:131], v[104:107]\n"
"v_mfma_f32_16x16x32_bf16 v[104:107], a[52:55], a[132:135], v[104:107]\n"
"global_load_dwordx4 a[40:43], v0, s[34:35] offset:-2048 sc1\n"
"v_mfma_f32_16x16x32_bf16 v[132:135], a[60:63], a[132:135], v[132:135]\n"
"v_mfma_f32_16x16x32_bf16 v[132:135], a[56:59], a[128:131], v[132:135]\n"
"v_mfma_f32_16x16x32_bf16 v[136:139], a[56:59], a[136:139], v[136:139]\n"
"v_mfma_f32_16x16x32_bf16 v[136:139], a[60:63], a[140:143], v[136:139]\n"
"v_mfma_f32_16x16x32_bf16 v[108:111], a[52:55], a[140:143], v[108:111]\n"
"buffer_load_dwordx4 v0, s[48:51], s69 offen offset:1024 sc1 lds\n"
"v_mfma_f32_16x16x32_bf16 v[108:111], a[48:51], a[136:139], v[108:111]\n"
"v_mfma_f32_16x16x32_bf16 v[112:115], a[48:51], a[144:147], v[112:115]\n"
"v_mfma_f32_16x16x32_bf16 v[112:115], a[52:55], a[148:151], v[112:115]\n"
"global_load_dwordx4 a[44:47], v0, s[34:35] offset:-1024 sc1\n"
"v_mfma_f32_16x16x32_bf16 v[140:143], a[60:63], a[148:151], v[140:143]\n"
"v_mfma_f32_16x16x32_bf16 v[140:143], a[56:59], a[144:147], v[140:143]\n"
"v_mfma_f32_16x16x32_bf16 a[12:15], a[56:59], a[152:155], a[12:15]\n"
"v_mfma_f32_16x16x32_bf16 a[12:15], a[60:63], a[156:159], a[12:15]\n"
"v_mfma_f32_16x16x32_bf16 a[8:11], a[52:55], a[156:159], a[8:11]\n"
"v_mfma_f32_16x16x32_bf16 a[8:11], a[48:51], a[152:155], a[8:11]\n"
"s_waitcnt vmcnt(12)\n"
"v_mfma_f32_16x16x32_bf16 v[144:147], a[64:67], a[96:99], v[144:147]\n"
"v_mfma_f32_16x16x32_bf16 v[144:147], a[68:71], a[100:103], v[144:147]\n"
"global_load_dwordx4 a[48:51], v0, s[34:35] offset:0 sc1\n"
"v_mfma_f32_16x16x32_bf16 v[172:175], a[76:79], a[100:103], v[172:175]\n"
"v_mfma_f32_16x16x32_bf16 v[172:175], a[72:75], a[96:99], v[172:175]\n"
"v_mfma_f32_16x16x32_bf16 v[176:179], a[72:75], a[104:107], v[176:179]\n"
"v_mfma_f32_16x16x32_bf16 v[176:179], a[76:79], a[108:111], v[176:179]\n"
"buffer_load_dwordx4 v0, s[48:51], s69 offen offset:2048 sc1 lds\n"
"v_mfma_f32_16x16x32_bf16 v[148:151], a[68:71], a[108:111], v[148:151]\n"
"v_mfma_f32_16x16x32_bf16 v[148:151], a[64:67], a[104:107], v[148:151]\n"
"v_mfma_f32_16x16x32_bf16 v[152:155], a[64:67], a[112:115], v[152:155]\n"
"v_lshlrev_b32 v16, 16, v18\n"
"v_mfma_f32_16x16x32_bf16 v[152:155], a[68:71], a[116:119], v[152:155]\n"
"global_load_dwordx4 a[52:55], v0, s[34:35] offset:1024 sc1\n"
"v_and_b32 v17, s63, v18\n"
"v_mfma_f32_16x16x32_bf16 v[180:183], a[76:79], a[116:119], v[180:183]\n"
"v_lshlrev_b32 v18, 16, v19\n"
"v_mfma_f32_16x16x32_bf16 v[180:183], a[72:75], a[112:115], v[180:183]\n"
"v_and_b32 v19, s63, v19\n"
"v_mfma_f32_16x16x32_bf16 v[184:187], a[72:75], a[120:123], v[184:187]\n"
"v_add_f32 v32, v32, v16\n"
"v_mfma_f32_16x16x32_bf16 v[184:187], a[76:79], a[124:127], v[184:187]\n"
"v_add_f32 v33, v33, v17\n"
"v_add_f32 v34, v34, v18\n"
"v_mfma_f32_16x16x32_bf16 v[156:159], a[68:71], a[124:127], v[156:159]\n"
"v_add_f32 v35, v35, v19\n"
"v_mfma_f32_16x16x32_bf16 v[156:159], a[64:67], a[120:123], v[156:159]\n"
"v_add_f32 v36, v36, v16\n"
"v_mfma_f32_16x16x32_bf16 v[160:163], a[64:67], a[128:131], v[160:163]\n"
"v_add_f32 v37, v37, v17\n"
"v_mfma_f32_16x16x32_bf16 v[160:163], a[68:71], a[132:135], v[160:163]\n"
"global_load_dwordx4 a[56:59], v0, s[34:35] offset:2048 sc1\n"
"v_add_f32 v38, v38, v18\n"
"v_mfma_f32_16x16x32_bf16 v[188:191], a[76:79], a[132:135], v[188:191]\n"
"v_add_f32 v39, v39, v19\n"
"v_mfma_f32_16x16x32_bf16 v[188:191], a[72:75], a[128:131], v[188:191]\n"
"v_add_f32 v40, v40, v16\n"
"v_add_f32 v41, v41, v17\n"
"v_mfma_f32_16x16x32_bf16 v[192:195], a[72:75], a[136:139], v[192:195]\n"
"buffer_load_dwordx4 v0, s[48:51], s69 offen offset:3072 sc1 lds\n"
"v_mfma_f32_16x16x32_bf16 v[192:195], a[76:79], a[140:143], v[192:195]\n"
"v_add_f32 v42, v42, v18\n"
"v_mfma_f32_16x16x32_bf16 v[164:167], a[68:71], a[140:143], v[164:167]\n"
"v_add_f32 v43, v43, v19\n"
"v_mfma_f32_16x16x32_bf16 v[164:167], a[64:67], a[136:139], v[164:167]\n"
"v_add_f32 v44, v44, v16\n"
"v_add_f32 v45, v45, v17\n"
"v_mfma_f32_16x16x32_bf16 v[168:171], a[64:67], a[144:147], v[168:171]\n"
"v_add_f32 v46, v46, v18\n"
"v_mfma_f32_16x16x32_bf16 v[168:171], a[68:71], a[148:151], v[168:171]\n"
"global_load_dwordx4 a[60:63], v0, s[34:35] offset:3072 sc1\n"
"v_add_f32 v47, v47, v19\n"
"v_mfma_f32_16x16x32_bf16 v[196:199], a[76:79], a[148:151], v[196:199]\n"
"v_add_f32 v48, v48, v16\n"
"v_mfma_f32_16x16x32_bf16 v[196:199], a[72:75], a[144:147], v[196:199]\n"
"v_add_f32 v49, v49, v17\n"
"v_mfma_f32_16x16x32_bf16 a[20:23], a[72:75], a[152:155], a[20:23]\n"
"s_add_u32 s34, s34, 8192\n"
"v_add_f32 v50, v50, v18\n"
"v_mfma_f32_16x16x32_bf16 a[20:23], a[76:79], a[156:159], a[20:23]\n"
"v_add_f32 v51, v51, v19\n"
"v_mfma_f32_16x16x32_bf16 a[16:19], a[68:71], a[156:159], a[16:19]\n"
"v_add_f32 v52, v52, v16\n"
"v_mfma_f32_16x16x32_bf16 a[16:19], a[64:67], a[152:155], a[16:19]\n"
"s_waitcnt vmcnt(12)\n"
"v_add_f32 v53, v53, v17\n"
"v_mfma_f32_16x16x32_bf16 v[200:203], a[80:83], a[96:99], v[200:203]\n"
"v_add_f32 v54, v54, v18\n"
"v_add_f32 v55, v55, v19\n"
"v_mfma_f32_16x16x32_bf16 v[200:203], a[84:87], a[100:103], v[200:203]\n"
"global_load_dwordx4 a[64:67], v0, s[34:35] offset:-4096 sc1\n"
"v_add_f32 v56, v56, v16\n"
"v_mfma_f32_16x16x32_bf16 v[228:231], a[92:95], a[100:103], v[228:231]\n"
"s_add_u32 m0, s64, 12288\n"
"ds_read_b128 a[100:103], v5 offset:17408\n"
"v_add_f32 v57, v57, v17\n"
"v_mfma_f32_16x16x32_bf16 v[228:231], a[88:91], a[96:99], v[228:231]\n"
"ds_read_b128 a[96:99], v5 offset:16384\n"
"v_add_f32 v58, v58, v18\n"
"v_mfma_f32_16x16x32_bf16 v[232:235], a[88:91], a[104:107], v[232:235]\n"
"v_add_f32 v59, v59, v19\n"
"v_mfma_f32_16x16x32_bf16 v[232:235], a[92:95], a[108:111], v[232:235]\n"
"v_accvgpr_read_b32 v12, a0\n"
"v_mfma_f32_16x16x32_bf16 v[204:207], a[84:87], a[108:111], v[204:207]\n"
"ds_read_b128 a[108:111], v5 offset:19456\n"
"v_accvgpr_read_b32 v13, a1\n"
"v_mfma_f32_16x16x32_bf16 v[204:207], a[80:83], a[104:107], v[204:207]\n"
"ds_read_b128 a[104:107], v5 offset:18432\n"
"v_accvgpr_read_b32 v14, a2\n"
"v_mfma_f32_16x16x32_bf16 v[208:211], a[80:83], a[112:115], v[208:211]\n"
"v_accvgpr_read_b32 v15, a3\n"
"v_mfma_f32_16x16x32_bf16 v[208:211], a[84:87], a[116:119], v[208:211]\n"
"global_load_dwordx4 a[68:71], v0, s[34:35] offset:-3072 sc1\n"
"v_add_f32 v12, v12, v16\n"
"v_mfma_f32_16x16x32_bf16 v[236:239], a[92:95], a[116:119], v[236:239]\n"
"ds_read_b128 a[116:119], v5 offset:21504\n"
"v_add_f32 v13, v13, v17\n"
"v_mfma_f32_16x16x32_bf16 v[236:239], a[88:91], a[112:115], v[236:239]\n"
"ds_read_b128 a[112:115], v5 offset:20480\n"
"v_add_f32 v14, v14, v18\n"
"v_mfma_f32_16x16x32_bf16 v[240:243], a[88:91], a[120:123], v[240:243]\n"
"v_add_f32 v15, v15, v19\n"
"v_accvgpr_write_b32 a0, v12\n"
"v_mfma_f32_16x16x32_bf16 v[240:243], a[92:95], a[124:127], v[240:243]\n"
"v_accvgpr_write_b32 a1, v13\n"
"v_mfma_f32_16x16x32_bf16 v[212:215], a[84:87], a[124:127], v[212:215]\n"
"ds_read_b128 a[124:127], v5 offset:23552\n"
"v_accvgpr_write_b32 a2, v14\n"
"v_mfma_f32_16x16x32_bf16 v[212:215], a[80:83], a[120:123], v[212:215]\n"
"ds_read_b128 a[120:123], v5 offset:22528\n"
"v_accvgpr_write_b32 a3, v15\n"
"v_mfma_f32_16x16x32_bf16 v[216:219], a[80:83], a[128:131], v[216:219]\n"
"buffer_load_dwordx4 v1, s[48:51], s69 offen offset:0 sc1 lds\n"
"v_mfma_f32_16x16x32_bf16 v[216:219], a[84:87], a[132:135], v[216:219]\n"
"global_load_dwordx4 a[72:75], v0, s[34:35] offset:-2048 sc1\n"
"v_lshrrev_b32 v16, 4, v0\n"
"v_mfma_f32_16x16x32_bf16 v[244:247], a[92:95], a[132:135], v[244:247]\n"
"ds_read_b128 a[132:135], v5 offset:25600\n"
"v_and_b32 v16, 0x30, v16\n"
"v_mfma_f32_16x16x32_bf16 v[244:247], a[88:91], a[128:131], v[244:247]\n"
"ds_read_b128 a[128:131], v5 offset:24576\n"
"buffer_load_dwordx2 v[18:19], v16, s[44:47], s61 offen offset:64\n"
"v_mfma_f32_16x16x32_bf16 v[248:251], a[88:91], a[136:139], v[248:251]\n"
"s_getpc_b64 s[96:97]\n"
"L_c6_:\n"
"s_add_u32 s96, s96, L_last-L_c6_\n"
"s_addc_u32 s97, s97, 0\n"
"v_mfma_f32_16x16x32_bf16 v[248:251], a[92:95], a[140:143], v[248:251]\n"
"s_lshr_b32 s66, %26, 3\n"
"v_mfma_f32_16x16x32_bf16 v[220:223], a[84:87], a[140:143], v[220:223]\n"
"s_lshl2_add_u32 s66, s66, %16\n"
"ds_read_b128 a[140:143], v5 offset:27648\n"
"v_mfma_f32_16x16x32_bf16 v[220:223], a[80:83], a[136:139], v[220:223]\n"
"ds_read_b128 a[136:139], v5 offset:26624\n"
"v_mfma_f32_16x16x32_bf16 v[224:227], a[80:83], a[144:147], v[224:227]\n"
"s_mul_i32 s66, s66, 256\n"
"v_mfma_f32_16x16x32_bf16 v[224:227], a[84:87], a[148:151], v[224:227]\n"
"global_load_dwordx4 a[76:79], v0, s[34:35] offset:-1024 sc1\n"
"v_mfma_f32_16x16x32_bf16 v[252:255], a[92:95], a[148:151], v[252:255]\n"
"ds_read_b128 a[148:151], v5 offset:29696\n"
"v_mfma_f32_16x16x32_bf16 v[252:255], a[88:91], a[144:147], v[252:255]\n"
"s_min_u32 s66, s66, L_headF-L_last-256\n"
"ds_read_b128 a[144:147], v5 offset:28672\n"
"v_mfma_f32_16x16x32_bf16 a[28:31], a[88:91], a[152:155], a[28:31]\n"
"v_mfma_f32_16x16x32_bf16 a[28:31], a[92:95], a[156:159], a[28:31]\n"
"v_min_u32 v11, 16, v0\n"
"v_mfma_f32_16x16x32_bf16 a[24:27], a[84:87], a[156:159], a[24:27]\n"
"ds_read_b128 a[156:159], v5 offset:31744\n"
"v_mfma_f32_16x16x32_bf16 a[24:27], a[80:83], a[152:155], a[24:27]\n"
"s_waitcnt vmcnt(12) lgkmcnt(11)\n"
"ds_read_b128 a[152:155], v5 offset:30720\n"
"v_mfma_f32_16x16x32_bf16 v[32:35], a[32:35], a[96:99], v[32:35]\n"
"buffer_load_dwordx4 v1, s[48:51], s69 offen offset:1024 sc1 lds\n"
"v_mfma_f32_16x16x32_bf16 v[32:35], a[36:39], a[100:103], v[32:35]\n"
"global_load_dwordx4 a[80:83], v0, s[34:35] offset:0 sc1\n"
"v_mfma_f32_16x16x32_bf16 v[60:63], a[44:47], a[100:103], v[60:63]\n"
"v_lshl_add_u32 v11, v11, 3, s66\n"
"v_mfma_f32_16x16x32_bf16 v[60:63], a[40:43], a[96:99], v[60:63]\n"
"global_load_dword v10, v11, s[96:97]\n"
"v_mfma_f32_16x16x32_bf16 v[64:67], a[40:43], a[104:107], v[64:67]\n"
"v_mfma_f32_16x16x32_bf16 v[64:67], a[44:47], a[108:111], v[64:67]\n"
"v_mfma_f32_16x16x32_bf16 v[36:39], a[36:39], a[108:111], v[36:39]\n"
"v_mfma_f32_16x16x32_bf16 v[36:39], a[32:35], a[104:107], v[36:39]\n"
"s_waitcnt lgkmcnt(8)\n"
"v_mfma_f32_16x16x32_bf16 v[40:43], a[32:35], a[112:115], v[40:43]\n"
"v_mfma_f32_16x16x32_bf16 v[40:43], a[36:39], a[116:119], v[40:43]\n"
"global_load_dwordx4 a[84:87], v0, s[34:35] offset:1024 sc1\n"
"v_mfma_f32_16x16x32_bf16 v[68:71], a[44:47], a[116:119], v[68:71]\n"
"v_mfma_f32_16x16x32_bf16 v[68:71], a[40:43], a[112:115], v[68:71]\n"
"v_mfma_f32_16x16x32_bf16 v[72:75], a[40:43], a[120:123], v[72:75]\n"
"v_mfma_f32_16x16x32_bf16 v[72:75], a[44:47], a[124:127], v[72:75]\n"
"v_mfma_f32_16x16x32_bf16 v[44:47], a[36:39], a[124:127], v[44:47]\n"
"buffer_load_dwordx4 v1, s[48:51], s69 offen offset:2048 sc1 lds\n"
"v_mfma_f32_16x16x32_bf16 v[44:47], a[32:35], a[120:123], v[44:47]\n"
"s_waitcnt lgkmcnt(4)\n"
"v_mfma_f32_16x16x32_bf16 v[48:51], a[32:35], a[128:131], v[48:51]\n"
"v_mfma_f32_16x16x32_bf16 v[48:51], a[36:39], a[132:135], v[48:51]\n"
"global_load_dwordx4 a[88:91], v0, s[34:35] offset:2048 sc1\n"
"v_mfma_f32_16x16x32_bf16 v[76:79], a[44:47], a[132:135], v[76:79]\n"
"v_mfma_f32_16x16x32_bf16 v[76:79], a[40:43], a[128:131], v[76:79]\n"
"v_mfma_f32_16x16x32_bf16 v[80:83], a[40:43], a[136:139], v[80:83]\n"
"v_mfma_f32_16x16x32_bf16 v[80:83], a[44:47], a[140:143], v[80:83]\n"
"v_mfma_f32_16x16x32_bf16 v[52:55], a[36:39], a[140:143], v[52:55]\n"
"v_mfma_f32_16x16x32_bf16 v[52:55], a[32:35], a[136:139], v[52:55]\n"
"s_waitcnt lgkmcnt(0)\n"
"v_mfma_f32_16x16x32_bf16 v[56:59], a[32:35], a[144:147], v[56:59]\n"
"v_mfma_f32_16x16x32_bf16 v[56:59], a[36:39], a[148:151], v[56:59]\n"
"global_load_dwordx4 a[92:95], v0, s[34:35] offset:3072 sc1\n"
"v_mfma_f32_16x16x32_bf16 v[84:87], a[44:47], a[148:151], v[84:87]\n"
"v_mfma_f32_16x16x32_bf16 v[84:87], a[40:43], a[144:147], v[84:87]\n"
"v_mfma_f32_16x16x32_bf16 a[4:7], a[40:43], a[152:155], a[4:7]\n"
"s_add_u32 s34, s34, 8192\n"
"v_mfma_f32_16x16x32_bf16 a[4:7], a[44:47], a[156:159], a[4:7]\n"
"buffer_load_dwordx4 v1, s[48:51], s69 offen offset:3072 sc1 lds\n"
"v_mfma_f32_16x16x32_bf16 a[0:3], a[36:39], a[156:159], a[0:3]\n"
"v_mfma_f32_16x16x32_bf16 a[0:3], a[32:35], a[152:155], a[0:3]\n"
"s_waitcnt vmcnt(14)\n"
"v_mfma_f32_16x16x32_bf16 v[88:91], a[48:51], a[96:99], v[88:91]\n"
"v_mfma_f32_16x16x32_bf16 v[88:91], a[52:55], a[100:103], v[88:91]\n"
"global_load_dwordx4 a[32:35], v0, s[34:35] offset:-4096 sc1\n"
"v_mfma_f32_16x16x32_bf16 v[116:119], a[60:63], a[100:103], v[116:119]\n"
"v_mfma_f32_16x16x32_bf16 v[116:119], a[56:59], a[96:99], v[116:119]\n"
"v_mfma_f32_16x16x32_bf16 v[120:123], a[56:59], a[104:107], v[120:123]\n"
"v_mfma_f32_16x16x32_bf16 v[120:123], a[60:63], a[108:111], v[120:123]\n"
"v_mfma_f32_16x16x32_bf16 v[92:95], a[52:55], a[108:111], v[92:95]\n"
"v_mfma_f32_16x16x32_bf16 v[92:95], a[48:51], a[104:107], v[92:95]\n"
"v_mfma_f32_16x16x32_bf16 v[96:99], a[48:51], a[112:115], v[96:99]\n"
"v_mfma_f32_16x16x32_bf16 v[96:99], a[52:55], a[116:119], v[96:99]\n"
"global_load_dwordx4 a[36:39], v0, s[34:35] offset:-3072 sc1\n"
"v_mfma_f32_16x16x32_bf16 v[124:127], a[60:63], a[116:119], v[124:127]\n"
"v_mfma_f32_16x16x32_bf16 v[124:127], a[56:59], a[112:115], v[124:127]\n"
"v_mfma_f32_16x16x32_bf16 v[128:131], a[56:59], a[120:123], v[128:131]\n"
"v_mfma_f32_16x16x32_bf16 v[128:131], a[60:63], a[124:127], v[128:131]\n"
"v_mfma_f32_16x16x32_bf16 v[100:103], a[52:55], a[124:127], v[100:103]\n"
"v_mfma_f32_16x16x32_bf16 v[100:103], a[48:51], a[120:123], v[100:103]\n"
"v_mfma_f32_16x16x32_bf16 v[104:107], a[48:51], a[128:131], v[104:107]\n"
"buffer_store_dwordx4 v[24:27], v1, s[36:39], s28 offen offset:0 sc1\n"
"buffer_load_dwordx2 v[8:9], off, s[56:59], %14 sc1\n"
"v_mfma_f32_16x16x32_bf16 v[104:107], a[52:55], a[132:135], v[104:107]\n"
"global_load_dwordx4 a[40:43], v0, s[34:35] offset:-2048 sc1\n"
"v_mfma_f32_16x16x32_bf16 v[132:135], a[60:63], a[132:135], v[132:135]\n"
"v_mfma_f32_16x16x32_bf16 v[132:135], a[56:59], a[128:131], v[132:135]\n"
"v_mfma_f32_16x16x32_bf16 v[136:139], a[56:59], a[136:139], v[136:139]\n"
"v_mfma_f32_16x16x32_bf16 v[136:139], a[60:63], a[140:143], v[136:139]\n"
"v_mfma_f32_16x16x32_bf16 v[108:111], a[52:55], a[140:143], v[108:111]\n"
"v_mfma_f32_16x16x32_bf16 v[108:111], a[48:51], a[136:139], v[108:111]\n"
"v_mfma_f32_16x16x32_bf16 v[112:115], a[48:51], a[144:147], v[112:115]\n"
"v_mfma_f32_16x16x32_bf16 v[112:115], a[52:55], a[148:151], v[112:115]\n"
"global_load_dwordx4 a[44:47], v0, s[34:35] offset:-1024 sc1\n"
"v_mfma_f32_16x16x32_bf16 v[140:143], a[60:63], a[148:151], v[140:143]\n"
"v_mfma_f32_16x16x32_bf16 v[140:143], a[56:59], a[144:147], v[140:143]\n"
"v_mfma_f32_16x16x32_bf16 a[12:15], a[56:59], a[152:155], a[12:15]\n"
"v_mfma_f32_16x16x32_bf16 a[12:15], a[60:63], a[156:159], a[12:15]\n"
"v_mfma_f32_16x16x32_bf16 a[8:11], a[52:55], a[156:159], a[8:11]\n"
"v_mfma_f32_16x16x32_bf16 a[8:11], a[48:51], a[152:155], a[8:11]\n"
"s_waitcnt vmcnt(14)\n"
"v_mfma_f32_16x16x32_bf16 v[144:147], a[64:67], a[96:99], v[144:147]\n"
"v_mfma_f32_16x16x32_bf16 v[144:147], a[68:71], a[100:103], v[144:147]\n"
"global_load_dwordx4 a[48:51], v0, s[34:35] offset:0 sc1\n"
"v_mfma_f32_16x16x32_bf16 v[172:175], a[76:79], a[100:103], v[172:175]\n"
"v_mfma_f32_16x16x32_bf16 v[172:175], a[72:75], a[96:99], v[172:175]\n"
"v_mfma_f32_16x16x32_bf16 v[176:179], a[72:75], a[104:107], v[176:179]\n"
"v_mfma_f32_16x16x32_bf16 v[176:179], a[76:79], a[108:111], v[176:179]\n"
"v_mfma_f32_16x16x32_bf16 v[148:151], a[68:71], a[108:111], v[148:151]\n"
"v_mfma_f32_16x16x32_bf16 v[148:151], a[64:67], a[104:107], v[148:151]\n"
"v_mfma_f32_16x16x32_bf16 v[152:155], a[64:67], a[112:115], v[152:155]\n"
"v_lshlrev_b32 v20, 16, v22\n"
"v_mfma_f32_16x16x32_bf16 v[152:155], a[68:71], a[116:119], v[152:155]\n"
"global_load_dwordx4 a[52:55], v0, s[34:35] offset:1024 sc1\n"
"v_and_b32 v21, s63, v22\n"
"v_mfma_f32_16x16x32_bf16 v[180:183], a[76:79], a[116:119], v[180:183]\n"
"v_lshlrev_b32 v22, 16, v23\n"
"v_mfma_f32_16x16x32_bf16 v[180:183], a[72:75], a[112:115], v[180:183]\n"
"v_and_b32 v23, s63, v23\n"
"v_mfma_f32_16x16x32_bf16 v[184:187], a[72:75], a[120:123], v[184:187]\n"
"v_add_f32 v60, v60, v20\n"
"v_mfma_f32_16x16x32_bf16 v[184:187], a[76:79], a[124:127], v[184:187]\n"
"v_add_f32 v61, v61, v21\n"
"v_mfma_f32_16x16x32_bf16 v[156:159], a[68:71], a[124:127], v[156:159]\n"
"buffer_store_dwordx4 v[28:31], v1, s[36:39], s28 offen offset:2048 sc1\n"
"v_add_f32 v62, v62, v22\n"
"v_add_f32 v63, v63, v23\n"
"v_add_f32 v64, v64, v20\n"
"v_mfma_f32_16x16x32_bf16 v[156:159], a[64:67], a[120:123], v[156:159]\n"
"v_add_f32 v65, v65, v21\n"
"v_mfma_f32_16x16x32_bf16 v[160:163], a[64:67], a[128:131], v[160:163]\n"
"v_add_f32 v66, v66, v22\n"
"v_mfma_f32_16x16x32_bf16 v[160:163], a[68:71], a[132:135], v[160:163]\n"
"global_load_dwordx4 a[56:59], v0, s[34:35] offset:2048 sc1\n"
"v_mfma_f32_16x16x32_bf16 v[188:191], a[76:79], a[132:135], v[188:191]\n"
"v_add_f32 v67, v67, v23\n"
"v_mfma_f32_16x16x32_bf16 v[188:191], a[72:75], a[128:131], v[188:191]\n"
"v_add_f32 v68, v68, v20\n"
"v_mfma_f32_16x16x32_bf16 v[192:195], a[72:75], a[136:139], v[192:195]\n"
"v_add_f32 v69, v69, v21\n"
"v_mfma_f32_16x16x32_bf16 v[192:195], a[76:79], a[140:143], v[192:195]\n"
"v_add_f32 v70, v70, v22\n"
"v_mfma_f32_16x16x32_bf16 v[164:167], a[68:71], a[140:143], v[164:167]\n"
"v_add_f32 v71, v71, v23\n"
"v_mfma_f32_16x16x32_bf16 v[164:167], a[64:67], a[136:139], v[164:167]\n"
"v_add_f32 v72, v72, v20\n"
"v_add_f32 v73, v73, v21\n"
"v_mfma_f32_16x16x32_bf16 v[168:171], a[64:67], a[144:147], v[168:171]\n"
"v_add_f32 v74, v74, v22\n"
"v_mfma_f32_16x16x32_bf16 v[168:171], a[68:71], a[148:151], v[168:171]\n"
"global_load_dwordx4 a[60:63], v0, s[34:35] offset:3072 sc1\n"
"v_add_f32 v75, v75, v23\n"
"v_mfma_f32_16x16x32_bf16 v[196:199], a[76:79], a[148:151], v[196:199]\n"
"v_add_f32 v76, v76, v20\n"
"v_mfma_f32_16x16x32_bf16 v[196:199], a[72:75], a[144:147], v[196:199]\n"
"v_add_f32 v77, v77, v21\n"
"v_mfma_f32_16x16x32_bf16 a[20:23], a[72:75], a[152:155], a[20:23]\n"
"s_add_u32 s34, s34, 8192\n"
"v_add_f32 v78, v78, v22\n"
"v_mfma_f32_16x16x32_bf16 a[20:23], a[76:79], a[156:159], a[20:23]\n"
"v_add_f32 v79, v79, v23\n"
"v_mfma_f32_16x16x32_bf16 a[16:19], a[68:71], a[156:159], a[16:19]\n"
"v_add_f32 v80, v80, v20\n"
"v_mfma_f32_16x16x32_bf16 a[16:19], a[64:67], a[152:155], a[16:19]\n"
"s_waitcnt vmcnt(12)\n"
"v_add_f32 v81, v81, v21\n"
"v_mfma_f32_16x16x32_bf16 v[200:203], a[80:83], a[96:99], v[200:203]\n"
"v_add_f32 v82, v82, v22\n"
"v_add_f32 v83, v83, v23\n"
"v_mfma_f32_16x16x32_bf16 v[200:203], a[84:87], a[100:103], v[200:203]\n"
"global_load_dwordx4 a[64:67], v0, s[34:35] offset:-4096 sc1\n"
"v_add_f32 v84, v84, v20\n"
"v_mfma_f32_16x16x32_bf16 v[228:231], a[92:95], a[100:103], v[228:231]\n"
"s_waitcnt vmcnt(12)\n"
"ds_read_b128 a[100:103], v2 offset:1024\n"
"v_add_f32 v85, v85, v21\n"
"v_mfma_f32_16x16x32_bf16 v[228:231], a[88:91], a[96:99], v[228:231]\n"
"ds_read_b128 a[96:99], v2 offset:0\n"
"v_add_f32 v86, v86, v22\n"
"v_mfma_f32_16x16x32_bf16 v[232:235], a[88:91], a[104:107], v[232:235]\n"
"v_add_f32 v87, v87, v23\n"
"v_mfma_f32_16x16x32_bf16 v[232:235], a[92:95], a[108:111], v[232:235]\n"
"v_accvgpr_read_b32 v12, a4\n"
"v_mfma_f32_16x16x32_bf16 v[204:207], a[84:87], a[108:111], v[204:207]\n"
"ds_read_b128 a[108:111], v2 offset:3072\n"
"v_accvgpr_read_b32 v13, a5\n"
"v_mfma_f32_16x16x32_bf16 v[204:207], a[80:83], a[104:107], v[204:207]\n"
"ds_read_b128 a[104:107], v2 offset:2048\n"
"v_accvgpr_read_b32 v14, a6\n"
"v_mfma_f32_16x16x32_bf16 v[208:211], a[80:83], a[112:115], v[208:211]\n"
"v_accvgpr_read_b32 v15, a7\n"
"v_mfma_f32_16x16x32_bf16 v[208:211], a[84:87], a[116:119], v[208:211]\n"
"global_load_dwordx4 a[68:71], v0, s[34:35] offset:-3072 sc1\n"
"v_add_f32 v12, v12, v20\n"
"v_mfma_f32_16x16x32_bf16 v[236:239], a[92:95], a[116:119], v[236:239]\n"
"ds_read_b128 a[116:119], v2 offset:5120\n"
"v_add_f32 v13, v13, v21\n"
"v_mfma_f32_16x16x32_bf16 v[236:239], a[88:91], a[112:115], v[236:239]\n"
"ds_read_b128 a[112:115], v2 offset:4096\n"
"v_add_f32 v14, v14, v22\n"
"v_mfma_f32_16x16x32_bf16 v[240:243], a[88:91], a[120:123], v[240:243]\n"
"v_add_f32 v15, v15, v23\n"
"v_accvgpr_write_b32 a4, v12\n"
"v_mfma_f32_16x16x32_bf16 v[240:243], a[92:95], a[124:127], v[240:243]\n"
"v_accvgpr_write_b32 a5, v13\n"
"v_mfma_f32_16x16x32_bf16 v[212:215], a[84:87], a[124:127], v[212:215]\n"
"ds_read_b128 a[124:127], v2 offset:7168\n"
"v_accvgpr_write_b32 a6, v14\n"
"v_mfma_f32_16x16x32_bf16 v[212:215], a[80:83], a[120:123], v[212:215]\n"
"ds_read_b128 a[120:123], v2 offset:6144\n"
"v_accvgpr_write_b32 a7, v15\n"
"v_mfma_f32_16x16x32_bf16 v[216:219], a[80:83], a[128:131], v[216:219]\n"
"s_add_u32 s28, s28, 0x40000\n"
"v_mfma_f32_16x16x32_bf16 v[216:219], a[84:87], a[132:135], v[216:219]\n"
"L_st9078:\n"
"s_cbranch_execz L_retB\n"
"global_load_dwordx4 a[72:75], v0, s[34:35] offset:-2048 sc1\n"
"v_lshrrev_b32 v20, 4, v0\n"
"v_mfma_f32_16x16x32_bf16 v[244:247], a[92:95], a[132:135], v[244:247]\n"
"s_add_u32 s67, s78, s62\n"
"ds_read_b128 a[132:135], v2 offset:9216\n"
"v_and_b32 v20, 0x30, v20\n"
"v_mfma_f32_16x16x32_bf16 v[244:247], a[88:91], a[128:131], v[244:247]\n"
"ds_read_b128 a[128:131], v2 offset:8192\n"
"buffer_load_dwordx2 v[22:23], v20, s[44:47], s61 offen offset:72\n"
"v_mfma_f32_16x16x32_bf16 v[248:251], a[88:91], a[136:139], v[248:251]\n"
"s_add_u32 s67, s67, 1\n"
"v_mfma_f32_16x16x32_bf16 v[248:251], a[92:95], a[140:143], v[248:251]\n"
"v_mfma_f32_16x16x32_bf16 v[220:223], a[84:87], a[140:143], v[220:223]\n"
"ds_read_b128 a[140:143], v2 offset:11264\n"
"v_mov_b32 v24, s67\n"
"v_mfma_f32_16x16x32_bf16 v[220:223], a[80:83], a[136:139], v[220:223]\n"
"ds_read_b128 a[136:139], v2 offset:10240\n"
"v_mov_b32 v25, s79\n"
"v_mfma_f32_16x16x32_bf16 v[224:227], a[80:83], a[144:147], v[224:227]\n"
"v_mfma_f32_16x16x32_bf16 v[224:227], a[84:87], a[148:151], v[224:227]\n"
"s_waitcnt vmcnt(6)\n"
"global_load_dwordx4 a[76:79], v0, s[34:35] offset:-1024 sc1\n"
"buffer_store_dwordx2 v[24:25], off, s[56:59], %24 sc1\n"
"v_mfma_f32_16x16x32_bf16 v[252:255], a[92:95], a[148:151], v[252:255]\n"
"ds_read_b128 a[148:151], v2 offset:13312\n"
"v_mfma_f32_16x16x32_bf16 v[252:255], a[88:91], a[144:147], v[252:255]\n"
"ds_read_b128 a[144:147], v2 offset:12288\n"
"v_mfma_f32_16x16x32_bf16 a[28:31], a[88:91], a[152:155], a[28:31]\n"
"v_mfma_f32_16x16x32_bf16 a[28:31], a[92:95], a[156:159], a[28:31]\n"
"v_mfma_f32_16x16x32_bf16 a[24:27], a[84:87], a[156:159], a[24:27]\n"
"s_cmp_eq_u32 s62, 5\n"
"ds_read_b128 a[156:159], v2 offset:15360\n"
"v_mfma_f32_16x16x32_bf16 a[24:27], a[80:83], a[152:155], a[24:27]\n"
"ds_read_b128 a[152:155], v2 offset:14336\n"
"s_cbranch_scc1 L_last\n"
"s_branch L_P4\n"
"L_last:\n"
"L_st9115:\n"
"s_cbranch_execz L_retB\n"
"s_waitcnt vmcnt(9) lgkmcnt(12)\n"
"v_mfma_f32_16x16x32_bf16 v[32:35], a[32:35], a[96:99], v[32:35]\n"
"s_add_u32 s34, s36, 1454080\n"
"s_add_u32 s48, s48, 131072\n"
"s_addc_u32 s49, s49, 0\n"
"v_mfma_f32_16x16x32_bf16 v[32:35], a[36:39], a[100:103], v[32:35]\n"
"s_mov_b64 exec, 0\n"
"s_cmp_eq_u32 %16, 0\n"
"s_cbranch_scc1 L_st9657\n"
"s_cmp_eq_u32 %16, 1\n"
"s_cbranch_scc1 L_st10265\n"
"s_cmp_eq_u32 %16, 2\n"
"s_cbranch_scc1 L_st10875\n"
"s_branch L_st11485\n"
"L_retC:\n"
"s_mov_b64 exec, -1\n"
"s_nop 3\n"
"global_load_dwordx4 a[80:83], v0, s[34:35] offset:0 sc1\n"
"v_mfma_f32_16x16x32_bf16 v[60:63], a[44:47], a[100:103], v[60:63]\n"
"s_barrier\n"
"v_mfma_f32_16x16x32_bf16 v[60:63], a[40:43], a[96:99], v[60:63]\n"
"v_mfma_f32_16x16x32_bf16 v[64:67], a[40:43], a[104:107], v[64:67]\n"
"v_mfma_f32_16x16x32_bf16 v[64:67], a[44:47], a[108:111], v[64:67]\n"
"v_mfma_f32_16x16x32_bf16 v[36:39], a[36:39], a[108:111], v[36:39]\n"
"v_subrev_u32 v8, s65, v8\n"
"v_mfma_f32_16x16x32_bf16 v[36:39], a[32:35], a[104:107], v[36:39]\n"
"s_waitcnt lgkmcnt(8)\n"
"v_xor_b32 v9, s79, v9\n"
"v_mfma_f32_16x16x32_bf16 v[40:43], a[32:35], a[112:115], v[40:43]\n"
"v_or_b32 v8, v8, v9\n"
"v_mfma_f32_16x16x32_bf16 v[40:43], a[36:39], a[116:119], v[40:43]\n"
"global_load_dwordx4 a[84:87], v0, s[34:35] offset:1024 sc1\n"
"v_cmp_lt_u32 vcc, 15, v8\n"
"v_mfma_f32_16x16x32_bf16 v[68:71], a[44:47], a[116:119], v[68:71]\n"
"s_cbranch_vccnz L_slow4\n"
"L_back4:\n"
"v_mfma_f32_16x16x32_bf16 v[68:71], a[40:43], a[112:115], v[68:71]\n"
"v_mfma_f32_16x16x32_bf16 v[72:75], a[40:43], a[120:123], v[72:75]\n"
"v_mfma_f32_16x16x32_bf16 v[72:75], a[44:47], a[124:127], v[72:75]\n"
"v_mfma_f32_16x16x32_bf16 v[44:47], a[36:39], a[124:127], v[44:47]\n"
"v_mfma_f32_16x16x32_bf16 v[44:47], a[32:35], a[120:123], v[44:47]\n"
"s_add_u32 m0, s64, 16384\n"
"s_waitcnt lgkmcnt(4)\n"
"v_mfma_f32_16x16x32_bf16 v[48:51], a[32:35], a[128:131], v[48:51]\n"
"v_mfma_f32_16x16x32_bf16 v[48:51], a[36:39], a[132:135], v[48:51]\n"
"global_load_dwordx4 a[88:91], v0, s[34:35] offset:2048 sc1\n"
"v_mfma_f32_16x16x32_bf16 v[76:79], a[44:47], a[132:135], v[76:79]\n"
"v_mfma_f32_16x16x32_bf16 v[76:79], a[40:43], a[128:131], v[76:79]\n"
"v_mfma_f32_16x16x32_bf16 v[80:83], a[40:43], a[136:139], v[80:83]\n"
"s_mov_b32 s97, s37\n"
"v_mfma_f32_16x16x32_bf16 v[80:83], a[44:47], a[140:143], v[80:83]\n"
"v_mfma_f32_16x16x32_bf16 v[52:55], a[36:39], a[140:143], v[52:55]\n"
"v_mfma_f32_16x16x32_bf16 v[52:55], a[32:35], a[136:139], v[52:55]\n"
"s_waitcnt lgkmcnt(0)\n"
"v_mfma_f32_16x16x32_bf16 v[56:59], a[32:35], a[144:147], v[56:59]\n"
"buffer_load_dwordx4 v0, s[48:51], s75 offen offset:0 sc1 lds\n"
"v_mfma_f32_16x16x32_bf16 v[56:59], a[36:39], a[148:151], v[56:59]\n"
"global_load_dwordx4 a[92:95], v0, s[34:35] offset:3072 sc1\n"
"v_mfma_f32_16x16x32_bf16 v[84:87], a[44:47], a[148:151], v[84:87]\n"
"v_mfma_f32_16x16x32_bf16 v[84:87], a[40:43], a[144:147], v[84:87]\n"
"v_mfma_f32_16x16x32_bf16 a[4:7], a[40:43], a[152:155], a[4:7]\n"
"v_mfma_f32_16x16x32_bf16 a[4:7], a[44:47], a[156:159], a[4:7]\n"
"v_mfma_f32_16x16x32_bf16 a[0:3], a[36:39], a[156:159], a[0:3]\n"
"v_mfma_f32_16x16x32_bf16 a[0:3], a[32:35], a[152:155], a[0:3]\n"
"s_waitcnt vmcnt(10)\n"
"v_mfma_f32_16x16x32_bf16 v[88:91], a[48:51], a[96:99], v[88:91]\n"
"s_add_u32 s34, s36, 1458176\n"
"v_mfma_f32_16x16x32_bf16 v[88:91], a[52:55], a[100:103], v[88:91]\n"
"global_load_dwordx4 a[32:35], v0, s[34:35] offset:0 sc1\n"
"v_mfma_f32_16x16x32_bf16 v[116:119], a[60:63], a[100:103], v[116:119]\n"
"buffer_load_dwordx4 v0, s[48:51], s75 offen offset:1024 sc1 lds\n"
"v_mfma_f32_16x16x32_bf16 v[116:119], a[56:59], a[96:99], v[116:119]\n"
"v_mfma_f32_16x16x32_bf16 v[120:123], a[56:59], a[104:107], v[120:123]\n"
"v_mfma_f32_16x16x32_bf16 v[120:123], a[60:63], a[108:111], v[120:123]\n"
"v_mfma_f32_16x16x32_bf16 v[92:95], a[52:55], a[108:111], v[92:95]\n"
"v_mfma_f32_16x16x32_bf16 v[92:95], a[48:51], a[104:107], v[92:95]\n"
"v_mfma_f32_16x16x32_bf16 v[96:99], a[48:51], a[112:115], v[96:99]\n"
"v_mfma_f32_16x16x32_bf16 v[96:99], a[52:55], a[116:119], v[96:99]\n"
"global_load_dwordx4 a[36:39], v0, s[34:35] offset:1024 sc1\n"
"v_mfma_f32_16x16x32_bf16 v[124:127], a[60:63], a[116:119], v[124:127]\n"
"v_mfma_f32_16x16x32_bf16 v[124:127], a[56:59], a[112:115], v[124:127]\n"
"v_mfma_f32_16x16x32_bf16 v[128:131], a[56:59], a[120:123], v[128:131]\n"
"buffer_load_dwordx4 v0, s[48:51], s75 offen offset:2048 sc1 lds\n"
"v_mfma_f32_16x16x32_bf16 v[128:131], a[60:63], a[124:127], v[128:131]\n"
"v_mfma_f32_16x16x32_bf16 v[100:103], a[52:55], a[124:127], v[100:103]\n"
"v_mfma_f32_16x16x32_bf16 v[100:103], a[48:51], a[120:123], v[100:103]\n"
"v_mfma_f32_16x16x32_bf16 v[104:107], a[48:51], a[128:131], v[104:107]\n"
"v_mfma_f32_16x16x32_bf16 v[104:107], a[52:55], a[132:135], v[104:107]\n"
"global_load_dwordx4 a[40:43], v0, s[34:35] offset:2048 sc1\n"
"v_mfma_f32_16x16x32_bf16 v[132:135], a[60:63], a[132:135], v[132:135]\n"
"v_mfma_f32_16x16x32_bf16 v[132:135], a[56:59], a[128:131], v[132:135]\n"
"v_mfma_f32_16x16x32_bf16 v[136:139], a[56:59], a[136:139], v[136:139]\n"
"v_mfma_f32_16x16x32_bf16 v[136:139], a[60:63], a[140:143], v[136:139]\n"
"buffer_load_dwordx4 v0, s[48:51], s75 offen offset:3072 sc1 lds\n"
"v_mfma_f32_16x16x32_bf16 v[108:111], a[52:55], a[140:143], v[108:111]\n"
"v_mfma_f32_16x16x32_bf16 v[108:111], a[48:51], a[136:139], v[108:111]\n"
"v_mfma_f32_16x16x32_bf16 v[112:115], a[48:51], a[144:147], v[112:115]\n"
"v_mfma_f32_16x16x32_bf16 v[112:115], a[52:55], a[148:151], v[112:115]\n"
"global_load_dwordx4 a[44:47], v0, s[34:35] offset:3072 sc1\n"
"v_mfma_f32_16x16x32_bf16 v[140:143], a[60:63], a[148:151], v[140:143]\n"
"v_mfma_f32_16x16x32_bf16 v[140:143], a[56:59], a[144:147], v[140:143]\n"
"v_mfma_f32_16x16x32_bf16 a[12:15], a[56:59], a[152:155], a[12:15]\n"
"v_mfma_f32_16x16x32_bf16 a[12:15], a[60:63], a[156:159], a[12:15]\n"
"v_mfma_f32_16x16x32_bf16 a[8:11], a[52:55], a[156:159], a[8:11]\n"
"v_mfma_f32_16x16x32_bf16 a[8:11], a[48:51], a[152:155], a[8:11]\n"
"s_add_u32 m0, s64, 20480\n"
"s_waitcnt vmcnt(12)\n"
"v_mfma_f32_16x16x32_bf16 v[144:147], a[64:67], a[96:99], v[144:147]\n"
"s_add_u32 s34, s36, 1462272\n"
"v_mfma_f32_16x16x32_bf16 v[144:147], a[68:71], a[100:103], v[144:147]\n"
"global_load_dwordx4 a[48:51], v0, s[34:35] offset:0 sc1\n"
"v_mfma_f32_16x16x32_bf16 v[172:175], a[76:79], a[100:103], v[172:175]\n"
"v_mfma_f32_16x16x32_bf16 v[172:175], a[72:75], a[96:99], v[172:175]\n"
"v_mfma_f32_16x16x32_bf16 v[176:179], a[72:75], a[104:107], v[176:179]\n"
"v_mfma_f32_16x16x32_bf16 v[176:179], a[76:79], a[108:111], v[176:179]\n"
"v_mfma_f32_16x16x32_bf16 v[148:151], a[68:71], a[108:111], v[148:151]\n"
"v_lshlrev_b32 v16, 16, v18\n"
"v_and_b32 v17, s63, v18\n"
"v_mfma_f32_16x16x32_bf16 v[148:151], a[64:67], a[104:107], v[148:151]\n"
"v_lshlrev_b32 v18, 16, v19\n"
"v_and_b32 v19, s63, v19\n"
"v_mfma_f32_16x16x32_bf16 v[152:155], a[64:67], a[112:115], v[152:155]\n"
"buffer_load_dwordx4 v1, s[48:51], s75 offen offset:0 sc1 lds\n"
"v_mfma_f32_16x16x32_bf16 v[152:155], a[68:71], a[116:119], v[152:155]\n"
"global_load_dwordx4 a[52:55], v0, s[34:35] offset:1024 sc1\n"
"v_add_f32 v88, v88, v16\n"
"v_mfma_f32_16x16x32_bf16 v[180:183], a[76:79], a[116:119], v[180:183]\n"
"v_add_f32 v89, v89, v17\n"
"v_add_f32 v90, v90, v18\n"
"v_mfma_f32_16x16x32_bf16 v[180:183], a[72:75], a[112:115], v[180:183]\n"
"v_add_f32 v91, v91, v19\n"
"v_add_f32 v92, v92, v16\n"
"v_mfma_f32_16x16x32_bf16 v[184:187], a[72:75], a[120:123], v[184:187]\n"
"v_add_f32 v93, v93, v17\n"
"v_add_f32 v94, v94, v18\n"
"v_mfma_f32_16x16x32_bf16 v[184:187], a[76:79], a[124:127], v[184:187]\n"
"v_add_f32 v95, v95, v19\n"
"v_add_f32 v96, v96, v16\n"
"v_mfma_f32_16x16x32_bf16 v[156:159], a[68:71], a[124:127], v[156:159]\n"
"v_add_f32 v97, v97, v17\n"
"v_add_f32 v98, v98, v18\n"
"v_mfma_f32_16x16x32_bf16 v[156:159], a[64:67], a[120:123], v[156:159]\n"
"v_add_f32 v99, v99, v19\n"
"v_add_f32 v100, v100, v16\n"
"v_mfma_f32_16x16x32_bf16 v[160:163], a[64:67], a[128:131], v[160:163]\n"
"v_add_f32 v101, v101, v17\n"
"v_add_f32 v102, v102, v18\n"
"v_mfma_f32_16x16x32_bf16 v[160:163], a[68:71], a[132:135], v[160:163]\n"
"global_load_dwordx4 a[56:59], v0, s[34:35] offset:2048 sc1\n"
"v_add_f32 v103, v103, v19\n"
"v_mfma_f32_16x16x32_bf16 v[188:191], a[76:79], a[132:135], v[188:191]\n"
"buffer_load_dwordx4 v1, s[48:51], s75 offen offset:1024 sc1 lds\n"
"v_mfma_f32_16x16x32_bf16 v[188:191], a[72:75], a[128:131], v[188:191]\n"
"v_add_f32 v104, v104, v16\n"
"v_add_f32 v105, v105, v17\n"
"v_mfma_f32_16x16x32_bf16 v[192:195], a[72:75], a[136:139], v[192:195]\n"
"v_add_f32 v106, v106, v18\n"
"v_add_f32 v107, v107, v19\n"
"v_mfma_f32_16x16x32_bf16 v[192:195], a[76:79], a[140:143], v[192:195]\n"
"v_add_f32 v108, v108, v16\n"
"v_add_f32 v109, v109, v17\n"
"v_mfma_f32_16x16x32_bf16 v[164:167], a[68:71], a[140:143], v[164:167]\n"
"v_add_f32 v110, v110, v18\n"
"v_add_f32 v111, v111, v19\n"
"v_mfma_f32_16x16x32_bf16 v[164:167], a[64:67], a[136:139], v[164:167]\n"
"v_add_f32 v112, v112, v16\n"
"v_add_f32 v113, v113, v17\n"
"v_mfma_f32_16x16x32_bf16 v[168:171], a[64:67], a[144:147], v[168:171]\n"
"v_add_f32 v114, v114, v18\n"
"v_add_f32 v115, v115, v19\n"
"v_mfma_f32_16x16x32_bf16 v[168:171], a[68:71], a[148:151], v[168:171]\n"
"global_load_dwordx4 a[60:63], v0, s[34:35] offset:3072 sc1\n"
"v_accvgpr_read_b32 v12, a8\n"
"v_mfma_f32_16x16x32_bf16 v[196:199], a[76:79], a[148:151], v[196:199]\n"
"v_accvgpr_read_b32 v13, a9\n"
"v_accvgpr_read_b32 v14, a10\n"
"v_mfma_f32_16x16x32_bf16 v[196:199], a[72:75], a[144:147], v[196:199]\n"
"buffer_load_dwordx4 v1, s[48:51], s75 offen offset:2048 sc1 lds\n"
"v_mfma_f32_16x16x32_bf16 a[20:23], a[72:75], a[152:155], a[20:23]\n"
"v_accvgpr_read_b32 v15, a11\n"
"v_add_f32 v12, v12, v16\n"
"v_mfma_f32_16x16x32_bf16 a[20:23], a[76:79], a[156:159], a[20:23]\n"
"v_add_f32 v13, v13, v17\n"
"v_add_f32 v14, v14, v18\n"
"v_mfma_f32_16x16x32_bf16 a[16:19], a[68:71], a[156:159], a[16:19]\n"
"v_add_f32 v15, v15, v19\n"
"v_accvgpr_write_b32 a8, v12\n"
"v_mfma_f32_16x16x32_bf16 a[16:19], a[64:67], a[152:155], a[16:19]\n"
"s_waitcnt vmcnt(14)\n"
"v_accvgpr_write_b32 a9, v13\n"
"v_accvgpr_write_b32 a10, v14\n"
"v_accvgpr_write_b32 a11, v15\n"
"v_mfma_f32_16x16x32_bf16 v[200:203], a[80:83], a[96:99], v[200:203]\n"
"s_add_u32 s34, s36, 1474560\n"
"v_lshlrev_b32 v20, 16, v22\n"
"v_and_b32 v21, s63, v22\n"
"v_mfma_f32_16x16x32_bf16 v[200:203], a[84:87], a[100:103], v[200:203]\n"
"global_load_dwordx4 a[64:67], v0, s[34:35] offset:0 sc1\n"
"v_lshlrev_b32 v22, 16, v23\n"
"v_mfma_f32_16x16x32_bf16 v[228:231], a[92:95], a[100:103], v[228:231]\n"
"ds_read_b128 a[100:103], v3 offset:1024\n"
"v_and_b32 v23, s63, v23\n"
"v_mfma_f32_16x16x32_bf16 v[228:231], a[88:91], a[96:99], v[228:231]\n"
"ds_read_b128 a[96:99], v3 offset:0\n"
"v_add_f32 v116, v116, v20\n"
"v_mfma_f32_16x16x32_bf16 v[232:235], a[88:91], a[104:107], v[232:235]\n"
"v_add_f32 v117, v117, v21\n"
"v_add_f32 v118, v118, v22\n"
"v_mfma_f32_16x16x32_bf16 v[232:235], a[92:95], a[108:111], v[232:235]\n"
"buffer_load_dwordx4 v1, s[48:51], s75 offen offset:3072 sc1 lds\n"
"v_mfma_f32_16x16x32_bf16 v[204:207], a[84:87], a[108:111], v[204:207]\n"
"ds_read_b128 a[108:111], v3 offset:3072\n"
"v_add_f32 v119, v119, v23\n"
"v_add_f32 v120, v120, v20\n"
"v_mfma_f32_16x16x32_bf16 v[204:207], a[80:83], a[104:107], v[204:207]\n"
"ds_read_b128 a[104:107], v3 offset:2048\n"
"v_add_f32 v121, v121, v21\n"
"v_mfma_f32_16x16x32_bf16 v[208:211], a[80:83], a[112:115], v[208:211]\n"
"v_add_f32 v122, v122, v22\n"
"v_add_f32 v123, v123, v23\n"
"v_mfma_f32_16x16x32_bf16 v[208:211], a[84:87], a[116:119], v[208:211]\n"
"global_load_dwordx4 a[68:71], v0, s[34:35] offset:1024 sc1\n"
"v_add_f32 v124, v124, v20\n"
"v_add_f32 v125, v125, v21\n"
"v_mfma_f32_16x16x32_bf16 v[236:239], a[92:95], a[116:119], v[236:239]\n"
"ds_read_b128 a[116:119], v3 offset:5120\n"
"v_add_f32 v126, v126, v22\n"
"v_mfma_f32_16x16x32_bf16 v[236:239], a[88:91], a[112:115], v[236:239]\n"
"ds_read_b128 a[112:115], v3 offset:4096\n"
"v_add_f32 v127, v127, v23\n"
"v_mfma_f32_16x16x32_bf16 v[240:243], a[88:91], a[120:123], v[240:243]\n"
"v_add_f32 v128, v128, v20\n"
"v_add_f32 v129, v129, v21\n"
"v_mfma_f32_16x16x32_bf16 v[240:243], a[92:95], a[124:127], v[240:243]\n"
"v_add_f32 v130, v130, v22\n"
"v_add_f32 v131, v131, v23\n"
"v_mfma_f32_16x16x32_bf16 v[212:215], a[84:87], a[124:127], v[212:215]\n"
"s_add_u32 m0, s64, 24576\n"
"ds_read_b128 a[124:127], v3 offset:7168\n"
"v_add_f32 v132, v132, v20\n"
"v_add_f32 v133, v133, v21\n"
"v_mfma_f32_16x16x32_bf16 v[212:215], a[80:83], a[120:123], v[212:215]\n"
"ds_read_b128 a[120:123], v3 offset:6144\n"
"v_add_f32 v134, v134, v22\n"
"v_mfma_f32_16x16x32_bf16 v[216:219], a[80:83], a[128:131], v[216:219]\n"
"v_add_f32 v135, v135, v23\n"
"v_add_f32 v136, v136, v20\n"
"v_mfma_f32_16x16x32_bf16 v[216:219], a[84:87], a[132:135], v[216:219]\n"
"global_load_dwordx4 a[72:75], v0, s[34:35] offset:2048 sc1\n"
"v_add_f32 v137, v137, v21\n"
"v_mfma_f32_16x16x32_bf16 v[244:247], a[92:95], a[132:135], v[244:247]\n"
"ds_read_b128 a[132:135], v3 offset:9216\n"
"v_add_f32 v138, v138, v22\n"
"v_add_f32 v139, v139, v23\n"
"v_mfma_f32_16x16x32_bf16 v[244:247], a[88:91], a[128:131], v[244:247]\n"
"ds_read_b128 a[128:131], v3 offset:8192\n"
"v_add_f32 v140, v140, v20\n"
"v_mfma_f32_16x16x32_bf16 v[248:251], a[88:91], a[136:139], v[248:251]\n"
"v_add_f32 v141, v141, v21\n"
"v_add_f32 v142, v142, v22\n"
"v_mfma_f32_16x16x32_bf16 v[248:251], a[92:95], a[140:143], v[248:251]\n"
"v_add_f32 v143, v143, v23\n"
"v_accvgpr_read_b32 v12, a12\n"
"v_mfma_f32_16x16x32_bf16 v[220:223], a[84:87], a[140:143], v[220:223]\n"
"ds_read_b128 a[140:143], v3 offset:11264\n"
"v_accvgpr_read_b32 v13, a13\n"
"v_mfma_f32_16x16x32_bf16 v[220:223], a[80:83], a[136:139], v[220:223]\n"
"ds_read_b128 a[136:139], v3 offset:10240\n"
"v_accvgpr_read_b32 v14, a14\n"
"v_accvgpr_read_b32 v15, a15\n"
"v_mfma_f32_16x16x32_bf16 v[224:227], a[80:83], a[144:147], v[224:227]\n"
"buffer_load_dwordx4 v0, s[48:51], s69 offen offset:0 sc1 lds\n"
"v_add_f32 v12, v12, v20\n"
"v_mfma_f32_16x16x32_bf16 v[224:227], a[84:87], a[148:151], v[224:227]\n"
"global_load_dwordx4 a[76:79], v0, s[34:35] offset:3072 sc1\n"
"v_add_f32 v13, v13, v21\n"
"v_add_f32 v14, v14, v22\n"
"v_mfma_f32_16x16x32_bf16 v[252:255], a[92:95], a[148:151], v[252:255]\n"
"ds_read_b128 a[148:151], v3 offset:13312\n"
"v_add_f32 v15, v15, v23\n"
"v_accvgpr_write_b32 a12, v12\n"
"v_mfma_f32_16x16x32_bf16 v[252:255], a[88:91], a[144:147], v[252:255]\n"
"ds_read_b128 a[144:147], v3 offset:12288\n"
"v_accvgpr_write_b32 a13, v13\n"
"v_mfma_f32_16x16x32_bf16 a[28:31], a[88:91], a[152:155], a[28:31]\n"
"v_accvgpr_write_b32 a14, v14\n"
"v_accvgpr_write_b32 a15, v15\n"
"v_mfma_f32_16x16x32_bf16 a[28:31], a[92:95], a[156:159], a[28:31]\n"
"v_mfma_f32_16x16x32_bf16 a[24:27], a[84:87], a[156:159], a[24:27]\n"
"ds_read_b128 a[156:159], v3 offset:15360\n"
"v_mfma_f32_16x16x32_bf16 a[24:27], a[80:83], a[152:155], a[24:27]\n"
"s_waitcnt vmcnt(13) lgkmcnt(11)\n"
"ds_read_b128 a[152:155], v3 offset:14336\n"
"v_mfma_f32_16x16x32_bf16 v[32:35], a[32:35], a[96:99], v[32:35]\n"
"s_add_u32 s34, s36, 1478656\n"
"v_mfma_f32_16x16x32_bf16 v[32:35], a[36:39], a[100:103], v[32:35]\n"
"global_load_dwordx4 a[80:83], v0, s[34:35] offset:0 sc1\n"
"v_mfma_f32_16x16x32_bf16 v[60:63], a[44:47], a[100:103], v[60:63]\n"
"buffer_load_dwordx4 v0, s[48:51], s69 offen offset:1024 sc1 lds\n"
"v_mfma_f32_16x16x32_bf16 v[60:63], a[40:43], a[96:99], v[60:63]\n"
"v_lshrrev_b32 v16, 4, v0\n"
"v_mfma_f32_16x16x32_bf16 v[64:67], a[40:43], a[104:107], v[64:67]\n"
"v_mfma_f32_16x16x32_bf16 v[64:67], a[44:47], a[108:111], v[64:67]\n"
"v_mfma_f32_16x16x32_bf16 v[36:39], a[36:39], a[108:111], v[36:39]\n"
"v_mfma_f32_16x16x32_bf16 v[36:39], a[32:35], a[104:107], v[36:39]\n"
"s_waitcnt lgkmcnt(8)\n"
"v_and_b32 v16, 0x30, v16\n"
"v_mfma_f32_16x16x32_bf16 v[40:43], a[32:35], a[112:115], v[40:43]\n"
"v_mfma_f32_16x16x32_bf16 v[40:43], a[36:39], a[116:119], v[40:43]\n"
"global_load_dwordx4 a[84:87], v0, s[34:35] offset:1024 sc1\n"
"v_mfma_f32_16x16x32_bf16 v[68:71], a[44:47], a[116:119], v[68:71]\n"
"buffer_load_dwordx2 v[18:19], v16, s[44:47], s61 offen offset:128\n"
"v_mfma_f32_16x16x32_bf16 v[68:71], a[40:43], a[112:115], v[68:71]\n"
"buffer_load_dwordx4 v0, s[48:51], s69 offen offset:2048 sc1 lds\n"
"v_mfma_f32_16x16x32_bf16 v[72:75], a[40:43], a[120:123], v[72:75]\n"
"v_mfma_f32_16x16x32_bf16 v[72:75], a[44:47], a[124:127], v[72:75]\n"
"v_lshrrev_b32 v20, 4, v0\n"
"v_mfma_f32_16x16x32_bf16 v[44:47], a[36:39], a[124:127], v[44:47]\n"
"v_mfma_f32_16x16x32_bf16 v[44:47], a[32:35], a[120:123], v[44:47]\n"
"s_waitcnt lgkmcnt(4)\n"
"v_mfma_f32_16x16x32_bf16 v[48:51], a[32:35], a[128:131], v[48:51]\n"
"v_and_b32 v20, 0x30, v20\n"
"v_mfma_f32_16x16x32_bf16 v[48:51], a[36:39], a[132:135], v[48:51]\n"
"global_load_dwordx4 a[88:91], v0, s[34:35] offset:2048 sc1\n"
"v_mfma_f32_16x16x32_bf16 v[76:79], a[44:47], a[132:135], v[76:79]\n"
"v_mfma_f32_16x16x32_bf16 v[76:79], a[40:43], a[128:131], v[76:79]\n"
"v_mfma_f32_16x16x32_bf16 v[80:83], a[40:43], a[136:139], v[80:83]\n"
"buffer_load_dwordx4 v0, s[48:51], s69 offen offset:3072 sc1 lds\n"
"v_mfma_f32_16x16x32_bf16 v[80:83], a[44:47], a[140:143], v[80:83]\n"
"buffer_load_dwordx2 v[22:23], v20, s[44:47], s61 offen offset:136\n"
"v_mfma_f32_16x16x32_bf16 v[52:55], a[36:39], a[140:143], v[52:55]\n"
"v_mfma_f32_16x16x32_bf16 v[52:55], a[32:35], a[136:139], v[52:55]\n"
"s_waitcnt lgkmcnt(0)\n"
"v_mfma_f32_16x16x32_bf16 v[56:59], a[32:35], a[144:147], v[56:59]\n"
"v_lshrrev_b32 v24, 4, v0\n"
"v_mfma_f32_16x16x32_bf16 v[56:59], a[36:39], a[148:151], v[56:59]\n"
"global_load_dwordx4 a[92:95], v0, s[34:35] offset:3072 sc1\n"
"v_mfma_f32_16x16x32_bf16 v[84:87], a[44:47], a[148:151], v[84:87]\n"
"v_mfma_f32_16x16x32_bf16 v[84:87], a[40:43], a[144:147], v[84:87]\n"
"v_and_b32 v24, 0x30, v24\n"
"v_mfma_f32_16x16x32_bf16 a[4:7], a[40:43], a[152:155], a[4:7]\n"
"v_mfma_f32_16x16x32_bf16 a[4:7], a[44:47], a[156:159], a[4:7]\n"
"v_mfma_f32_16x16x32_bf16 a[0:3], a[36:39], a[156:159], a[0:3]\n"
"s_add_u32 m0, s64, 28672\n"
"v_mfma_f32_16x16x32_bf16 a[0:3], a[32:35], a[152:155], a[0:3]\n"
"s_waitcnt vmcnt(16)\n"
"buffer_load_dwordx2 v[26:27], v24, s[44:47], s61 offen offset:192\n"
"v_mfma_f32_16x16x32_bf16 v[88:91], a[48:51], a[96:99], v[88:91]\n"
"s_add_u32 s34, s36, 1490944\n"
"v_lshrrev_b32 v28, 4, v0\n"
"v_mfma_f32_16x16x32_bf16 v[88:91], a[52:55], a[100:103], v[88:91]\n"
"global_load_dwordx4 a[32:35], v0, s[34:35] offset:0 sc1\n"
"v_mfma_f32_16x16x32_bf16 v[116:119], a[60:63], a[100:103], v[116:119]\n"
"ds_read_b128 a[100:103], v4 offset:1024\n"
"v_mfma_f32_16x16x32_bf16 v[116:119], a[56:59], a[96:99], v[116:119]\n"
"ds_read_b128 a[96:99], v4 offset:0\n"
"v_mfma_f32_16x16x32_bf16 v[120:123], a[56:59], a[104:107], v[120:123]\n"
"v_and_b32 v28, 0x30, v28\n"
"v_mfma_f32_16x16x32_bf16 v[120:123], a[60:63], a[108:111], v[120:123]\n"
"buffer_load_dwordx2 v[30:31], v28, s[44:47], s61 offen offset:200\n"
"v_mfma_f32_16x16x32_bf16 v[92:95], a[52:55], a[108:111], v[92:95]\n"
"ds_read_b128 a[108:111], v4 offset:3072\n"
"v_mfma_f32_16x16x32_bf16 v[92:95], a[48:51], a[104:107], v[92:95]\n"
"ds_read_b128 a[104:107], v4 offset:2048\n"
"v_mfma_f32_16x16x32_bf16 v[96:99], a[48:51], a[112:115], v[96:99]\n"
"buffer_load_dwordx4 v1, s[48:51], s69 offen offset:0 sc1 lds\n"
"v_mfma_f32_16x16x32_bf16 v[96:99], a[52:55], a[116:119], v[96:99]\n"
"global_load_dwordx4 a[36:39], v0, s[34:35] offset:1024 sc1\n"
"v_mfma_f32_16x16x32_bf16 v[124:127], a[60:63], a[116:119], v[124:127]\n"
"ds_read_b128 a[116:119], v4 offset:5120\n"
"v_mfma_f32_16x16x32_bf16 v[124:127], a[56:59], a[112:115], v[124:127]\n"
"ds_read_b128 a[112:115], v4 offset:4096\n"
"v_mfma_f32_16x16x32_bf16 v[128:131], a[56:59], a[120:123], v[128:131]\n"
"v_mfma_f32_16x16x32_bf16 v[128:131], a[60:63], a[124:127], v[128:131]\n"
"v_mfma_f32_16x16x32_bf16 v[100:103], a[52:55], a[124:127], v[100:103]\n"
"ds_read_b128 a[124:127], v4 offset:7168\n"
"v_mfma_f32_16x16x32_bf16 v[100:103], a[48:51], a[120:123], v[100:103]\n"
"ds_read_b128 a[120:123], v4 offset:6144\n"
"v_mfma_f32_16x16x32_bf16 v[104:107], a[48:51], a[128:131], v[104:107]\n"
"buffer_load_dwordx4 v1, s[48:51], s69 offen offset:1024 sc1 lds\n"
"v_mfma_f32_16x16x32_bf16 v[104:107], a[52:55], a[132:135], v[104:107]\n"
"global_load_dwordx4 a[40:43], v0, s[34:35] offset:2048 sc1\n"
"v_mfma_f32_16x16x32_bf16 v[132:135], a[60:63], a[132:135], v[132:135]\n"
"ds_read_b128 a[132:135], v4 offset:9216\n"
"v_mfma_f32_16x16x32_bf16 v[132:135], a[56:59], a[128:131], v[132:135]\n"
"ds_read_b128 a[128:131], v4 offset:8192\n"
"v_mfma_f32_16x16x32_bf16 v[136:139], a[56:59], a[136:139], v[136:139]\n"
"v_mfma_f32_16x16x32_bf16 v[136:139], a[60:63], a[140:143], v[136:139]\n"
"v_mfma_f32_16x16x32_bf16 v[108:111], a[52:55], a[140:143], v[108:111]\n"
"ds_read_b128 a[140:143], v4 offset:11264\n"
"v_mfma_f32_16x16x32_bf16 v[108:111], a[48:51], a[136:139], v[108:111]\n"
"ds_read_b128 a[136:139], v4 offset:10240\n"
"v_mfma_f32_16x16x32_bf16 v[112:115], a[48:51], a[144:147], v[112:115]\n"
"v_mfma_f32_16x16x32_bf16 v[112:115], a[52:55], a[148:151], v[112:115]\n"
"global_load_dwordx4 a[44:47], v0, s[34:35] offset:3072 sc1\n"
"v_mfma_f32_16x16x32_bf16 v[140:143], a[60:63], a[148:151], v[140:143]\n"
"ds_read_b128 a[148:151], v4 offset:13312\n"
"v_mfma_f32_16x16x32_bf16 v[140:143], a[56:59], a[144:147], v[140:143]\n"
"ds_read_b128 a[144:147], v4 offset:12288\n"
"v_mfma_f32_16x16x32_bf16 a[12:15], a[56:59], a[152:155], a[12:15]\n"
"buffer_load_dwordx4 v1, s[48:51], s69 offen offset:2048 sc1 lds\n"
"v_mfma_f32_16x16x32_bf16 a[12:15], a[60:63], a[156:159], a[12:15]\n"
"v_mfma_f32_16x16x32_bf16 a[8:11], a[52:55], a[156:159], a[8:11]\n"
"ds_read_b128 a[156:159], v4 offset:15360\n"
"v_mfma_f32_16x16x32_bf16 a[8:11], a[48:51], a[152:155], a[8:11]\n"
"s_waitcnt vmcnt(18) lgkmcnt(11)\n"
"ds_read_b128 a[152:155], v4 offset:14336\n"
"v_mfma_f32_16x16x32_bf16 v[32:35], a[64:67], a[96:99], v[32:35]\n"
"s_add_u32 s34, s36, 1495040\n"
"v_mfma_f32_16x16x32_bf16 v[32:35], a[68:71], a[100:103], v[32:35]\n"
"global_load_dwordx4 a[48:51], v0, s[34:35] offset:0 sc1\n"
"v_mfma_f32_16x16x32_bf16 v[60:63], a[76:79], a[100:103], v[60:63]\n"
"v_mfma_f32_16x16x32_bf16 v[60:63], a[72:75], a[96:99], v[60:63]\n"
"v_mfma_f32_16x16x32_bf16 v[64:67], a[72:75], a[104:107], v[64:67]\n"
"buffer_load_dwordx4 v1, s[48:51], s69 offen offset:3072 sc1 lds\n"
"v_mfma_f32_16x16x32_bf16 v[64:67], a[76:79], a[108:111], v[64:67]\n"
"v_mfma_f32_16x16x32_bf16 v[36:39], a[68:71], a[108:111], v[36:39]\n"
"v_mfma_f32_16x16x32_bf16 v[36:39], a[64:67], a[104:107], v[36:39]\n"
"s_waitcnt lgkmcnt(8)\n"
"v_mfma_f32_16x16x32_bf16 v[40:43], a[64:67], a[112:115], v[40:43]\n"
"v_mfma_f32_16x16x32_bf16 v[40:43], a[68:71], a[116:119], v[40:43]\n"
"global_load_dwordx4 a[52:55], v0, s[34:35] offset:1024 sc1\n"
"v_mfma_f32_16x16x32_bf16 v[68:71], a[76:79], a[116:119], v[68:71]\n"
"v_mfma_f32_16x16x32_bf16 v[68:71], a[72:75], a[112:115], v[68:71]\n"
"v_mfma_f32_16x16x32_bf16 v[72:75], a[72:75], a[120:123], v[72:75]\n"
"v_mfma_f32_16x16x32_bf16 v[72:75], a[76:79], a[124:127], v[72:75]\n"
"v_mfma_f32_16x16x32_bf16 v[44:47], a[68:71], a[124:127], v[44:47]\n"
"v_mfma_f32_16x16x32_bf16 v[44:47], a[64:67], a[120:123], v[44:47]\n"
"s_waitcnt lgkmcnt(4)\n"
"v_mfma_f32_16x16x32_bf16 v[48:51], a[64:67], a[128:131], v[48:51]\n"
"v_mfma_f32_16x16x32_bf16 v[48:51], a[68:71], a[132:135], v[48:51]\n"
"global_load_dwordx4 a[56:59], v0, s[34:35] offset:2048 sc1\n"
"v_mfma_f32_16x16x32_bf16 v[76:79], a[76:79], a[132:135], v[76:79]\n"
"v_mfma_f32_16x16x32_bf16 v[76:79], a[72:75], a[128:131], v[76:79]\n"
"v_mfma_f32_16x16x32_bf16 v[80:83], a[72:75], a[136:139], v[80:83]\n"
"v_mfma_f32_16x16x32_bf16 v[80:83], a[76:79], a[140:143], v[80:83]\n"
"v_mfma_f32_16x16x32_bf16 v[52:55], a[68:71], a[140:143], v[52:55]\n"
"v_mfma_f32_16x16x32_bf16 v[52:55], a[64:67], a[136:139], v[52:55]\n"
"s_waitcnt lgkmcnt(0)\n"
"v_mfma_f32_16x16x32_bf16 v[56:59], a[64:67], a[144:147], v[56:59]\n"
"v_mfma_f32_16x16x32_bf16 v[56:59], a[68:71], a[148:151], v[56:59]\n"
"global_load_dwordx4 a[60:63], v0, s[34:35] offset:3072 sc1\n"
"v_mfma_f32_16x16x32_bf16 v[84:87], a[76:79], a[148:151], v[84:87]\n"
"v_mfma_f32_16x16x32_bf16 v[84:87], a[72:75], a[144:147], v[84:87]\n"
"v_mfma_f32_16x16x32_bf16 a[4:7], a[72:75], a[152:155], a[4:7]\n"
"v_mfma_f32_16x16x32_bf16 a[4:7], a[76:79], a[156:159], a[4:7]\n"
"v_mfma_f32_16x16x32_bf16 a[0:3], a[68:71], a[156:159], a[0:3]\n"
"v_mfma_f32_16x16x32_bf16 a[0:3], a[64:67], a[152:155], a[0:3]\n"
"s_waitcnt vmcnt(14)\n"
"v_mfma_f32_16x16x32_bf16 v[88:91], a[80:83], a[96:99], v[88:91]\n"
"s_add_u32 s34, s36, 1507328\n"
"v_mfma_f32_16x16x32_bf16 v[88:91], a[84:87], a[100:103], v[88:91]\n"
"global_load_dwordx4 a[64:67], v0, s[34:35] offset:0 sc1\n"
"v_mfma_f32_16x16x32_bf16 v[116:119], a[92:95], a[100:103], v[116:119]\n"
"ds_read_b128 a[100:103], v5 offset:1024\n"
"v_mfma_f32_16x16x32_bf16 v[116:119], a[88:91], a[96:99], v[116:119]\n"
"ds_read_b128 a[96:99], v5 offset:0\n"
"v_mfma_f32_16x16x32_bf16 v[120:123], a[88:91], a[104:107], v[120:123]\n"
"v_mfma_f32_16x16x32_bf16 v[120:123], a[92:95], a[108:111], v[120:123]\n"
"v_mfma_f32_16x16x32_bf16 v[92:95], a[84:87], a[108:111], v[92:95]\n"
"ds_read_b128 a[108:111], v5 offset:3072\n"
"v_mfma_f32_16x16x32_bf16 v[92:95], a[80:83], a[104:107], v[92:95]\n"
"ds_read_b128 a[104:107], v5 offset:2048\n"
"v_mfma_f32_16x16x32_bf16 v[96:99], a[80:83], a[112:115], v[96:99]\n"
"v_mfma_f32_16x16x32_bf16 v[96:99], a[84:87], a[116:119], v[96:99]\n"
"global_load_dwordx4 a[68:71], v0, s[34:35] offset:1024 sc1\n"
"v_mfma_f32_16x16x32_bf16 v[124:127], a[92:95], a[116:119], v[124:127]\n"
"ds_read_b128 a[116:119], v5 offset:5120\n"
"v_mfma_f32_16x16x32_bf16 v[124:127], a[88:91], a[112:115], v[124:127]\n"
"ds_read_b128 a[112:115], v5 offset:4096\n"
"v_mfma_f32_16x16x32_bf16 v[128:131], a[88:91], a[120:123], v[128:131]\n"
"v_mfma_f32_16x16x32_bf16 v[128:131], a[92:95], a[124:127], v[128:131]\n"
"v_mfma_f32_16x16x32_bf16 v[100:103], a[84:87], a[124:127], v[100:103]\n"
"ds_read_b128 a[124:127], v5 offset:7168\n"
"v_mfma_f32_16x16x32_bf16 v[100:103], a[80:83], a[120:123], v[100:103]\n"
"ds_read_b128 a[120:123], v5 offset:6144\n"
"v_mfma_f32_16x16x32_bf16 v[104:107], a[80:83], a[128:131], v[104:107]\n"
"v_mfma_f32_16x16x32_bf16 v[104:107], a[84:87], a[132:135], v[104:107]\n"
"global_load_dwordx4 a[72:75], v0, s[34:35] offset:2048 sc1\n"
"v_mfma_f32_16x16x32_bf16 v[132:135], a[92:95], a[132:135], v[132:135]\n"
"ds_read_b128 a[132:135], v5 offset:9216\n"
"v_mfma_f32_16x16x32_bf16 v[132:135], a[88:91], a[128:131], v[132:135]\n"
"ds_read_b128 a[128:131], v5 offset:8192\n"
"v_mfma_f32_16x16x32_bf16 v[136:139], a[88:91], a[136:139], v[136:139]\n"
"v_mfma_f32_16x16x32_bf16 v[136:139], a[92:95], a[140:143], v[136:139]\n"
"v_mfma_f32_16x16x32_bf16 v[108:111], a[84:87], a[140:143], v[108:111]\n"
"ds_read_b128 a[140:143], v5 offset:11264\n"
"v_mfma_f32_16x16x32_bf16 v[108:111], a[80:83], a[136:139], v[108:111]\n"
"ds_read_b128 a[136:139], v5 offset:10240\n"
"v_mfma_f32_16x16x32_bf16 v[112:115], a[80:83], a[144:147], v[112:115]\n"
"v_mfma_f32_16x16x32_bf16 v[112:115], a[84:87], a[148:151], v[112:115]\n"
"global_load_dwordx4 a[76:79], v0, s[34:35] offset:3072 sc1\n"
"v_mfma_f32_16x16x32_bf16 v[140:143], a[92:95], a[148:151], v[140:143]\n"
"ds_read_b128 a[148:151], v5 offset:13312\n"
"v_mfma_f32_16x16x32_bf16 v[140:143], a[88:91], a[144:147], v[140:143]\n"
"ds_read_b128 a[144:147], v5 offset:12288\n"
"v_mfma_f32_16x16x32_bf16 a[12:15], a[88:91], a[152:155], a[12:15]\n"
"v_mfma_f32_16x16x32_bf16 a[12:15], a[92:95], a[156:159], a[12:15]\n"
"v_mfma_f32_16x16x32_bf16 a[8:11], a[84:87], a[156:159], a[8:11]\n"
"ds_read_b128 a[156:159], v5 offset:15360\n"
"v_mfma_f32_16x16x32_bf16 a[8:11], a[80:83], a[152:155], a[8:11]\n"
"s_waitcnt vmcnt(10) lgkmcnt(11)\n"
"ds_read_b128 a[152:155], v5 offset:14336\n"
"v_mfma_f32_16x16x32_bf16 v[32:35], a[32:35], a[96:99], v[32:35]\n"
"s_add_u32 s34, s36, 1511424\n"
"v_mfma_f32_16x16x32_bf16 v[32:35], a[36:39], a[100:103], v[32:35]\n"
"global_load_dwordx4 a[80:83], v0, s[34:35] offset:0 sc1\n"
"v_mfma_f32_16x16x32_bf16 v[60:63], a[44:47], a[100:103], v[60:63]\n"
"v_mfma_f32_16x16x32_bf16 v[60:63], a[40:43], a[96:99], v[60:63]\n"
"v_mfma_f32_16x16x32_bf16 v[64:67], a[40:43], a[104:107], v[64:67]\n"
"v_mfma_f32_16x16x32_bf16 v[64:67], a[44:47], a[108:111], v[64:67]\n"
"v_mfma_f32_16x16x32_bf16 v[36:39], a[36:39], a[108:111], v[36:39]\n"
"v_mfma_f32_16x16x32_bf16 v[36:39], a[32:35], a[104:107], v[36:39]\n"
"s_waitcnt lgkmcnt(8)\n"
"v_mfma_f32_16x16x32_bf16 v[40:43], a[32:35], a[112:115], v[40:43]\n"
"v_mfma_f32_16x16x32_bf16 v[40:43], a[36:39], a[116:119], v[40:43]\n"
"global_load_dwordx4 a[84:87], v0, s[34:35] offset:1024 sc1\n"
"v_mfma_f32_16x16x32_bf16 v[68:71], a[44:47], a[116:119], v[68:71]\n"
"v_mfma_f32_16x16x32_bf16 v[68:71], a[40:43], a[112:115], v[68:71]\n"
"v_mfma_f32_16x16x32_bf16 v[72:75], a[40:43], a[120:123], v[72:75]\n"
"v_mfma_f32_16x16x32_bf16 v[72:75], a[44:47], a[124:127], v[72:75]\n"
"v_mfma_f32_16x16x32_bf16 v[44:47], a[36:39], a[124:127], v[44:47]\n"
"v_mfma_f32_16x16x32_bf16 v[44:47], a[32:35], a[120:123], v[44:47]\n"
"s_waitcnt lgkmcnt(4)\n"
"v_mfma_f32_16x16x32_bf16 v[48:51], a[32:35], a[128:131], v[48:51]\n"
"v_mfma_f32_16x16x32_bf16 v[48:51], a[36:39], a[132:135], v[48:51]\n"
"global_load_dwordx4 a[88:91], v0, s[34:35] offset:2048 sc1\n"
"v_mfma_f32_16x16x32_bf16 v[76:79], a[44:47], a[132:135], v[76:79]\n"
"v_mfma_f32_16x16x32_bf16 v[76:79], a[40:43], a[128:131], v[76:79]\n"
"v_mfma_f32_16x16x32_bf16 v[80:83], a[40:43], a[136:139], v[80:83]\n"
"v_mfma_f32_16x16x32_bf16 v[80:83], a[44:47], a[140:143], v[80:83]\n"
"v_mfma_f32_16x16x32_bf16 v[52:55], a[36:39], a[140:143], v[52:55]\n"
"v_mfma_f32_16x16x32_bf16 v[52:55], a[32:35], a[136:139], v[52:55]\n"
"s_waitcnt lgkmcnt(0)\n"
"v_mfma_f32_16x16x32_bf16 v[56:59], a[32:35], a[144:147], v[56:59]\n"
"v_mfma_f32_16x16x32_bf16 v[56:59], a[36:39], a[148:151], v[56:59]\n"
"global_load_dwordx4 a[92:95], v0, s[34:35] offset:3072 sc1\n"
"v_mfma_f32_16x16x32_bf16 v[84:87], a[44:47], a[148:151], v[84:87]\n"
"v_mfma_f32_16x16x32_bf16 v[84:87], a[40:43], a[144:147], v[84:87]\n"
"v_mfma_f32_16x16x32_bf16 a[4:7], a[40:43], a[152:155], a[4:7]\n"
"v_mfma_f32_16x16x32_bf16 a[4:7], a[44:47], a[156:159], a[4:7]\n"
"v_mfma_f32_16x16x32_bf16 a[0:3], a[36:39], a[156:159], a[0:3]\n"
"L_st9657:\n"
"s_cbranch_execz L_retC\n"
"v_mfma_f32_16x16x32_bf16 a[0:3], a[32:35], a[152:155], a[0:3]\n"
"s_waitcnt vmcnt(8)\n"
"v_mfma_f32_16x16x32_bf16 v[88:91], a[48:51], a[96:99], v[88:91]\n"
"s_add_u32 s34, s36, 1515520\n"
"v_mfma_f32_16x16x32_bf16 v[88:91], a[52:55], a[100:103], v[88:91]\n"
"global_load_dwordx4 a[32:35], v0, s[34:35] offset:0 sc1\n"
"v_mfma_f32_16x16x32_bf16 v[116:119], a[60:63], a[100:103], v[116:119]\n"
"ds_read_b128 a[100:103], v2 offset:17408\n"
"v_mfma_f32_16x16x32_bf16 v[116:119], a[56:59], a[96:99], v[116:119]\n"
"ds_read_b128 a[96:99], v2 offset:16384\n"
"v_mfma_f32_16x16x32_bf16 v[120:123], a[56:59], a[104:107], v[120:123]\n"
"v_mfma_f32_16x16x32_bf16 v[120:123], a[60:63], a[108:111], v[120:123]\n"
"v_mfma_f32_16x16x32_bf16 v[92:95], a[52:55], a[108:111], v[92:95]\n"
"ds_read_b128 a[108:111], v2 offset:19456\n"
"v_mfma_f32_16x16x32_bf16 v[92:95], a[48:51], a[104:107], v[92:95]\n"
"ds_read_b128 a[104:107], v2 offset:18432\n"
"v_mfma_f32_16x16x32_bf16 v[96:99], a[48:51], a[112:115], v[96:99]\n"
"v_mfma_f32_16x16x32_bf16 v[96:99], a[52:55], a[116:119], v[96:99]\n"
"global_load_dwordx4 a[36:39], v0, s[34:35] offset:1024 sc1\n"
"v_mfma_f32_16x16x32_bf16 v[124:127], a[60:63], a[116:119], v[124:127]\n"
"ds_read_b128 a[116:119], v2 offset:21504\n"
"v_mfma_f32_16x16x32_bf16 v[124:127], a[56:59], a[112:115], v[124:127]\n"
"ds_read_b128 a[112:115], v2 offset:20480\n"
"v_mfma_f32_16x16x32_bf16 v[128:131], a[56:59], a[120:123], v[128:131]\n"
"v_mfma_f32_16x16x32_bf16 v[128:131], a[60:63], a[124:127], v[128:131]\n"
"v_mfma_f32_16x16x32_bf16 v[100:103], a[52:55], a[124:127], v[100:103]\n"
"ds_read_b128 a[124:127], v2 offset:23552\n"
"v_mfma_f32_16x16x32_bf16 v[100:103], a[48:51], a[120:123], v[100:103]\n"
"ds_read_b128 a[120:123], v2 offset:22528\n"
"v_mfma_f32_16x16x32_bf16 v[104:107], a[48:51], a[128:131], v[104:107]\n"
"v_mfma_f32_16x16x32_bf16 v[104:107], a[52:55], a[132:135], v[104:107]\n"
"global_load_dwordx4 a[40:43], v0, s[34:35] offset:2048 sc1\n"
"v_mfma_f32_16x16x32_bf16 v[132:135], a[60:63], a[132:135], v[132:135]\n"
"ds_read_b128 a[132:135], v2 offset:25600\n"
"v_mfma_f32_16x16x32_bf16 v[132:135], a[56:59], a[128:131], v[132:135]\n"
"ds_read_b128 a[128:131], v2 offset:24576\n"
"v_mfma_f32_16x16x32_bf16 v[136:139], a[56:59], a[136:139], v[136:139]\n"
"v_mfma_f32_16x16x32_bf16 v[136:139], a[60:63], a[140:143], v[136:139]\n"
"v_mfma_f32_16x16x32_bf16 v[108:111], a[52:55], a[140:143], v[108:111]\n"
"ds_read_b128 a[140:143], v2 offset:27648\n"
"v_mfma_f32_16x16x32_bf16 v[108:111], a[48:51], a[136:139], v[108:111]\n"
"ds_read_b128 a[136:139], v2 offset:26624\n"
"v_mfma_f32_16x16x32_bf16 v[112:115], a[48:51], a[144:147], v[112:115]\n"
"v_mfma_f32_16x16x32_bf16 v[112:115], a[52:55], a[148:151], v[112:115]\n"
"global_load_dwordx4 a[44:47], v0, s[34:35] offset:3072 sc1\n"
"v_mfma_f32_16x16x32_bf16 v[140:143], a[60:63], a[148:151], v[140:143]\n"
"ds_read_b128 a[148:151], v2 offset:29696\n"
"v_mfma_f32_16x16x32_bf16 v[140:143], a[56:59], a[144:147], v[140:143]\n"
"ds_read_b128 a[144:147], v2 offset:28672\n"
"v_mfma_f32_16x16x32_bf16 a[12:15], a[56:59], a[152:155], a[12:15]\n"
"v_mfma_f32_16x16x32_bf16 a[12:15], a[60:63], a[156:159], a[12:15]\n"
"v_mfma_f32_16x16x32_bf16 a[8:11], a[52:55], a[156:159], a[8:11]\n"
"ds_read_b128 a[156:159], v2 offset:31744\n"
"v_mfma_f32_16x16x32_bf16 a[8:11], a[48:51], a[152:155], a[8:11]\n"
"s_waitcnt vmcnt(8) lgkmcnt(11)\n"
"ds_read_b128 a[152:155], v2 offset:30720\n"
"v_mfma_f32_16x16x32_bf16 v[32:35], a[64:67], a[96:99], v[32:35]\n"
"s_add_u32 s34, s36, 1519616\n"
"v_mfma_f32_16x16x32_bf16 v[32:35], a[68:71], a[100:103], v[32:35]\n"
"global_load_dwordx4 a[48:51], v0, s[34:35] offset:0 sc1\n"
"v_mfma_f32_16x16x32_bf16 v[60:63], a[76:79], a[100:103], v[60:63]\n"
"s_barrier\n"
"v_mfma_f32_16x16x32_bf16 v[60:63], a[72:75], a[96:99], v[60:63]\n"
"v_mfma_f32_16x16x32_bf16 v[64:67], a[72:75], a[104:107], v[64:67]\n"
"v_mfma_f32_16x16x32_bf16 v[64:67], a[76:79], a[108:111], v[64:67]\n"
"v_mfma_f32_16x16x32_bf16 v[36:39], a[68:71], a[108:111], v[36:39]\n"
"v_mfma_f32_16x16x32_bf16 v[36:39], a[64:67], a[104:107], v[36:39]\n"
"s_waitcnt lgkmcnt(8)\n"
"v_mfma_f32_16x16x32_bf16 v[40:43], a[64:67], a[112:115], v[40:43]\n"
"v_mfma_f32_16x16x32_bf16 v[40:43], a[68:71], a[116:119], v[40:43]\n"
"global_load_dwordx4 a[52:55], v0, s[34:35] offset:1024 sc1\n"
"v_mfma_f32_16x16x32_bf16 v[68:71], a[76:79], a[116:119], v[68:71]\n"
"v_mfma_f32_16x16x32_bf16 v[68:71], a[72:75], a[112:115], v[68:71]\n"
"v_mfma_f32_16x16x32_bf16 v[72:75], a[72:75], a[120:123], v[72:75]\n"
"v_mfma_f32_16x16x32_bf16 v[72:75], a[76:79], a[124:127], v[72:75]\n"
"v_mfma_f32_16x16x32_bf16 v[44:47], a[68:71], a[124:127], v[44:47]\n"
"v_mfma_f32_16x16x32_bf16 v[44:47], a[64:67], a[120:123], v[44:47]\n"
"s_waitcnt lgkmcnt(4)\n"
"v_mfma_f32_16x16x32_bf16 v[48:51], a[64:67], a[128:131], v[48:51]\n"
"v_mfma_f32_16x16x32_bf16 v[48:51], a[68:71], a[132:135], v[48:51]\n"
"global_load_dwordx4 a[56:59], v0, s[34:35] offset:2048 sc1\n"
"v_mfma_f32_16x16x32_bf16 v[76:79], a[76:79], a[132:135], v[76:79]\n"
"v_mfma_f32_16x16x32_bf16 v[76:79], a[72:75], a[128:131], v[76:79]\n"
"v_mfma_f32_16x16x32_bf16 v[80:83], a[72:75], a[136:139], v[80:83]\n"
"v_lshlrev_b32 v16, 16, v18\n"
"v_mfma_f32_16x16x32_bf16 v[80:83], a[76:79], a[140:143], v[80:83]\n"
"v_and_b32 v17, s63, v18\n"
"v_mfma_f32_16x16x32_bf16 v[52:55], a[68:71], a[140:143], v[52:55]\n"
"v_lshlrev_b32 v18, 16, v19\n"
"v_mfma_f32_16x16x32_bf16 v[52:55], a[64:67], a[136:139], v[52:55]\n"
"s_waitcnt lgkmcnt(0)\n"
"v_and_b32 v19, s63, v19\n"
"v_add_f32 v144, v144, v16\n"
"v_mfma_f32_16x16x32_bf16 v[56:59], a[64:67], a[144:147], v[56:59]\n"
"v_add_f32 v145, v145, v17\n"
"v_mfma_f32_16x16x32_bf16 v[56:59], a[68:71], a[148:151], v[56:59]\n"
"global_load_dwordx4 a[60:63], v0, s[34:35] offset:3072 sc1\n"
"v_add_f32 v146, v146, v18\n"
"v_mfma_f32_16x16x32_bf16 v[84:87], a[76:79], a[148:151], v[84:87]\n"
"v_add_f32 v147, v147, v19\n"
"v_mfma_f32_16x16x32_bf16 v[84:87], a[72:75], a[144:147], v[84:87]\n"
"v_add_f32 v148, v148, v16\n"
"v_mfma_f32_16x16x32_bf16 a[4:7], a[72:75], a[152:155], a[4:7]\n"
"v_add_f32 v149, v149, v17\n"
"v_mfma_f32_16x16x32_bf16 a[4:7], a[76:79], a[156:159], a[4:7]\n"
"v_add_f32 v150, v150, v18\n"
"v_mfma_f32_16x16x32_bf16 a[0:3], a[68:71], a[156:159], a[0:3]\n"
"v_add_f32 v151, v151, v19\n"
"v_mfma_f32_16x16x32_bf16 a[0:3], a[64:67], a[152:155], a[0:3]\n"
"s_waitcnt vmcnt(8)\n"
"v_add_f32 v152, v152, v16\n"
"v_add_f32 v153, v153, v17\n"
"v_mfma_f32_16x16x32_bf16 v[88:91], a[80:83], a[96:99], v[88:91]\n"
"s_add_u32 s34, s36, 1523712\n"
"v_add_f32 v154, v154, v18\n"
"v_mfma_f32_16x16x32_bf16 v[88:91], a[84:87], a[100:103], v[88:91]\n"
"global_load_dwordx4 a[64:67], v0, s[34:35] offset:0 sc1\n"
"v_add_f32 v155, v155, v19\n"
"v_mfma_f32_16x16x32_bf16 v[116:119], a[92:95], a[100:103], v[116:119]\n"
"ds_read_b128 a[100:103], v3 offset:17408\n"
"v_add_f32 v156, v156, v16\n"
"v_mfma_f32_16x16x32_bf16 v[116:119], a[88:91], a[96:99], v[116:119]\n"
"ds_read_b128 a[96:99], v3 offset:16384\n"
"v_add_f32 v157, v157, v17\n"
"v_mfma_f32_16x16x32_bf16 v[120:123], a[88:91], a[104:107], v[120:123]\n"
"v_add_f32 v158, v158, v18\n"
"v_mfma_f32_16x16x32_bf16 v[120:123], a[92:95], a[108:111], v[120:123]\n"
"v_add_f32 v159, v159, v19\n"
"v_add_f32 v160, v160, v16\n"
"v_mfma_f32_16x16x32_bf16 v[92:95], a[84:87], a[108:111], v[92:95]\n"
"ds_read_b128 a[108:111], v3 offset:19456\n"
"v_add_f32 v161, v161, v17\n"
"v_mfma_f32_16x16x32_bf16 v[92:95], a[80:83], a[104:107], v[92:95]\n"
"ds_read_b128 a[104:107], v3 offset:18432\n"
"v_add_f32 v162, v162, v18\n"
"v_mfma_f32_16x16x32_bf16 v[96:99], a[80:83], a[112:115], v[96:99]\n"
"v_add_f32 v163, v163, v19\n"
"v_mfma_f32_16x16x32_bf16 v[96:99], a[84:87], a[116:119], v[96:99]\n"
"global_load_dwordx4 a[68:71], v0, s[34:35] offset:1024 sc1\n"
"v_add_f32 v164, v164, v16\n"
"v_mfma_f32_16x16x32_bf16 v[124:127], a[92:95], a[116:119], v[124:127]\n"
"ds_read_b128 a[116:119], v3 offset:21504\n"
"v_add_f32 v165, v165, v17\n"
"v_mfma_f32_16x16x32_bf16 v[124:127], a[88:91], a[112:115], v[124:127]\n"
"ds_read_b128 a[112:115], v3 offset:20480\n"
"v_add_f32 v166, v166, v18\n"
"v_mfma_f32_16x16x32_bf16 v[128:131], a[88:91], a[120:123], v[128:131]\n"
"v_add_f32 v167, v167, v19\n"
"v_mfma_f32_16x16x32_bf16 v[128:131], a[92:95], a[124:127], v[128:131]\n"
"v_add_f32 v168, v168, v16\n"
"v_add_f32 v169, v169, v17\n"
"v_mfma_f32_16x16x32_bf16 v[100:103], a[84:87], a[124:127], v[100:103]\n"
"ds_read_b128 a[124:127], v3 offset:23552\n"
"v_add_f32 v170, v170, v18\n"
"v_mfma_f32_16x16x32_bf16 v[100:103], a[80:83], a[120:123], v[100:103]\n"
"ds_read_b128 a[120:123], v3 offset:22528\n"
"v_add_f32 v171, v171, v19\n"
"v_mfma_f32_16x16x32_bf16 v[104:107], a[80:83], a[128:131], v[104:107]\n"
"v_accvgpr_read_b32 v12, a16\n"
"v_mfma_f32_16x16x32_bf16 v[104:107], a[84:87], a[132:135], v[104:107]\n"
"global_load_dwordx4 a[72:75], v0, s[34:35] offset:2048 sc1\n"
"v_accvgpr_read_b32 v13, a17\n"
"v_mfma_f32_16x16x32_bf16 v[132:135], a[92:95], a[132:135], v[132:135]\n"
"ds_read_b128 a[132:135], v3 offset:25600\n"
"v_accvgpr_read_b32 v14, a18\n"
"v_mfma_f32_16x16x32_bf16 v[132:135], a[88:91], a[128:131], v[132:135]\n"
"ds_read_b128 a[128:131], v3 offset:24576\n"
"v_accvgpr_read_b32 v15, a19\n"
"v_mfma_f32_16x16x32_bf16 v[136:139], a[88:91], a[136:139], v[136:139]\n"
"v_add_f32 v12, v12, v16\n"
"v_mfma_f32_16x16x32_bf16 v[136:139], a[92:95], a[140:143], v[136:139]\n"
"v_add_f32 v13, v13, v17\n"
"v_add_f32 v14, v14, v18\n"
"v_mfma_f32_16x16x32_bf16 v[108:111], a[84:87], a[140:143], v[108:111]\n"
"ds_read_b128 a[140:143], v3 offset:27648\n"
"v_add_f32 v15, v15, v19\n"
"v_mfma_f32_16x16x32_bf16 v[108:111], a[80:83], a[136:139], v[108:111]\n"
"ds_read_b128 a[136:139], v3 offset:26624\n"
"v_accvgpr_write_b32 a16, v12\n"
"v_mfma_f32_16x16x32_bf16 v[112:115], a[80:83], a[144:147], v[112:115]\n"
"v_accvgpr_write_b32 a17, v13\n"
"v_mfma_f32_16x16x32_bf16 v[112:115], a[84:87], a[148:151], v[112:115]\n"
"global_load_dwordx4 a[76:79], v0, s[34:35] offset:3072 sc1\n"
"v_accvgpr_write_b32 a18, v14\n"
"v_mfma_f32_16x16x32_bf16 v[140:143], a[92:95], a[148:151], v[140:143]\n"
"ds_read_b128 a[148:151], v3 offset:29696\n"
"v_accvgpr_write_b32 a19, v15\n"
"v_mfma_f32_16x16x32_bf16 v[140:143], a[88:91], a[144:147], v[140:143]\n"
"ds_read_b128 a[144:147], v3 offset:28672\n"
"v_lshlrev_b32 v20, 16, v22\n"
"v_mfma_f32_16x16x32_bf16 a[12:15], a[88:91], a[152:155], a[12:15]\n"
"v_and_b32 v21, s63, v22\n"
"v_lshlrev_b32 v22, 16, v23\n"
"v_mfma_f32_16x16x32_bf16 a[12:15], a[92:95], a[156:159], a[12:15]\n"
"v_and_b32 v23, s63, v23\n"
"v_mfma_f32_16x16x32_bf16 a[8:11], a[84:87], a[156:159], a[8:11]\n"
"ds_read_b128 a[156:159], v3 offset:31744\n"
"v_add_f32 v172, v172, v20\n"
"v_mfma_f32_16x16x32_bf16 a[8:11], a[80:83], a[152:155], a[8:11]\n"
"s_waitcnt vmcnt(8) lgkmcnt(11)\n"
"ds_read_b128 a[152:155], v3 offset:30720\n"
"v_add_f32 v173, v173, v21\n"
"v_mfma_f32_16x16x32_bf16 v[32:35], a[32:35], a[96:99], v[32:35]\n"
"s_add_u32 s34, s36, 1527808\n"
"v_add_f32 v174, v174, v22\n"
"v_mfma_f32_16x16x32_bf16 v[32:35], a[36:39], a[100:103], v[32:35]\n"
"global_load_dwordx4 a[80:83], v0, s[34:35] offset:0 sc1\n"
"v_add_f32 v175, v175, v23\n"
"v_mfma_f32_16x16x32_bf16 v[60:63], a[44:47], a[100:103], v[60:63]\n"
"v_add_f32 v176, v176, v20\n"
"v_mfma_f32_16x16x32_bf16 v[60:63], a[40:43], a[96:99], v[60:63]\n"
"v_add_f32 v177, v177, v21\n"
"v_add_f32 v178, v178, v22\n"
"v_mfma_f32_16x16x32_bf16 v[64:67], a[40:43], a[104:107], v[64:67]\n"
"v_add_f32 v179, v179, v23\n"
"v_mfma_f32_16x16x32_bf16 v[64:67], a[44:47], a[108:111], v[64:67]\n"
"v_add_f32 v180, v180, v20\n"
"v_mfma_f32_16x16x32_bf16 v[36:39], a[36:39], a[108:111], v[36:39]\n"
"v_add_f32 v181, v181, v21\n"
"v_mfma_f32_16x16x32_bf16 v[36:39], a[32:35], a[104:107], v[36:39]\n"
"s_waitcnt lgkmcnt(8)\n"
"v_add_f32 v182, v182, v22\n"
"v_mfma_f32_16x16x32_bf16 v[40:43], a[32:35], a[112:115], v[40:43]\n"
"v_add_f32 v183, v183, v23\n"
"v_mfma_f32_16x16x32_bf16 v[40:43], a[36:39], a[116:119], v[40:43]\n"
"global_load_dwordx4 a[84:87], v0, s[34:35] offset:1024 sc1\n"
"v_add_f32 v184, v184, v20\n"
"v_mfma_f32_16x16x32_bf16 v[68:71], a[44:47], a[116:119], v[68:71]\n"
"v_add_f32 v185, v185, v21\n"
"v_mfma_f32_16x16x32_bf16 v[68:71], a[40:43], a[112:115], v[68:71]\n"
"v_add_f32 v186, v186, v22\n"
"v_add_f32 v187, v187, v23\n"
"v_mfma_f32_16x16x32_bf16 v[72:75], a[40:43], a[120:123], v[72:75]\n"
"v_add_f32 v188, v188, v20\n"
"v_mfma_f32_16x16x32_bf16 v[72:75], a[44:47], a[124:127], v[72:75]\n"
"v_add_f32 v189, v189, v21\n"
"v_mfma_f32_16x16x32_bf16 v[44:47], a[36:39], a[124:127], v[44:47]\n"
"v_add_f32 v190, v190, v22\n"
"v_mfma_f32_16x16x32_bf16 v[44:47], a[32:35], a[120:123], v[44:47]\n"
"s_waitcnt lgkmcnt(4)\n"
"v_add_f32 v191, v191, v23\n"
"v_mfma_f32_16x16x32_bf16 v[48:51], a[32:35], a[128:131], v[48:51]\n"
"v_add_f32 v192, v192, v20\n"
"v_mfma_f32_16x16x32_bf16 v[48:51], a[36:39], a[132:135], v[48:51]\n"
"global_load_dwordx4 a[88:91], v0, s[34:35] offset:2048 sc1\n"
"v_add_f32 v193, v193, v21\n"
"v_mfma_f32_16x16x32_bf16 v[76:79], a[44:47], a[132:135], v[76:79]\n"
"v_add_f32 v194, v194, v22\n"
"v_add_f32 v195, v195, v23\n"
"v_mfma_f32_16x16x32_bf16 v[76:79], a[40:43], a[128:131], v[76:79]\n"
"v_add_f32 v196, v196, v20\n"
"v_mfma_f32_16x16x32_bf16 v[80:83], a[40:43], a[136:139], v[80:83]\n"
"v_add_f32 v197, v197, v21\n"
"v_mfma_f32_16x16x32_bf16 v[80:83], a[44:47], a[140:143], v[80:83]\n"
"v_add_f32 v198, v198, v22\n"
"v_mfma_f32_16x16x32_bf16 v[52:55], a[36:39], a[140:143], v[52:55]\n"
"v_add_f32 v199, v199, v23\n"
"v_mfma_f32_16x16x32_bf16 v[52:55], a[32:35], a[136:139], v[52:55]\n"
"s_waitcnt lgkmcnt(0)\n"
"v_accvgpr_read_b32 v12, a20\n"
"v_mfma_f32_16x16x32_bf16 v[56:59], a[32:35], a[144:147], v[56:59]\n"
"v_accvgpr_read_b32 v13, a21\n"
"v_mfma_f32_16x16x32_bf16 v[56:59], a[36:39], a[148:151], v[56:59]\n"
"global_load_dwordx4 a[92:95], v0, s[34:35] offset:3072 sc1\n"
"v_accvgpr_read_b32 v14, a22\n"
"v_mfma_f32_16x16x32_bf16 v[84:87], a[44:47], a[148:151], v[84:87]\n"
"v_accvgpr_read_b32 v15, a23\n"
"v_add_f32 v12, v12, v20\n"
"v_mfma_f32_16x16x32_bf16 v[84:87], a[40:43], a[144:147], v[84:87]\n"
"v_add_f32 v13, v13, v21\n"
"v_mfma_f32_16x16x32_bf16 a[4:7], a[40:43], a[152:155], a[4:7]\n"
"v_add_f32 v14, v14, v22\n"
"v_mfma_f32_16x16x32_bf16 a[4:7], a[44:47], a[156:159], a[4:7]\n"
"v_add_f32 v15, v15, v23\n"
"v_mfma_f32_16x16x32_bf16 a[0:3], a[36:39], a[156:159], a[0:3]\n"
"v_accvgpr_write_b32 a20, v12\n"
"v_mfma_f32_16x16x32_bf16 a[0:3], a[32:35], a[152:155], a[0:3]\n"
"s_waitcnt vmcnt(8)\n"
"v_accvgpr_write_b32 a21, v13\n"
"v_mfma_f32_16x16x32_bf16 v[88:91], a[48:51], a[96:99], v[88:91]\n"
"s_add_u32 s34, s36, 1531904\n"
"v_accvgpr_write_b32 a22, v14\n"
"v_accvgpr_write_b32 a23, v15\n"
"v_mfma_f32_16x16x32_bf16 v[88:91], a[52:55], a[100:103], v[88:91]\n"
"global_load_dwordx4 a[32:35], v0, s[34:35] offset:0 sc1\n"
"v_lshlrev_b32 v24, 16, v26\n"
"v_mfma_f32_16x16x32_bf16 v[116:119], a[60:63], a[100:103], v[116:119]\n"
"ds_read_b128 a[100:103], v4 offset:17408\n"
"v_and_b32 v25, s63, v26\n"
"v_mfma_f32_16x16x32_bf16 v[116:119], a[56:59], a[96:99], v[116:119]\n"
"ds_read_b128 a[96:99], v4 offset:16384\n"
"v_lshlrev_b32 v26, 16, v27\n"
"v_mfma_f32_16x16x32_bf16 v[120:123], a[56:59], a[104:107], v[120:123]\n"
"v_and_b32 v27, s63, v27\n"
"v_mfma_f32_16x16x32_bf16 v[120:123], a[60:63], a[108:111], v[120:123]\n"
"v_add_f32 v200, v200, v24\n"
"v_mfma_f32_16x16x32_bf16 v[92:95], a[52:55], a[108:111], v[92:95]\n"
"ds_read_b128 a[108:111], v4 offset:19456\n"
"v_add_f32 v201, v201, v25\n"
"v_mfma_f32_16x16x32_bf16 v[92:95], a[48:51], a[104:107], v[92:95]\n"
"ds_read_b128 a[104:107], v4 offset:18432\n"
"v_add_f32 v202, v202, v26\n"
"v_mfma_f32_16x16x32_bf16 v[96:99], a[48:51], a[112:115], v[96:99]\n"
"v_add_f32 v203, v203, v27\n"
"v_add_f32 v204, v204, v24\n"
"v_mfma_f32_16x16x32_bf16 v[96:99], a[52:55], a[116:119], v[96:99]\n"
"global_load_dwordx4 a[36:39], v0, s[34:35] offset:1024 sc1\n"
"v_add_f32 v205, v205, v25\n"
"v_mfma_f32_16x16x32_bf16 v[124:127], a[60:63], a[116:119], v[124:127]\n"
"ds_read_b128 a[116:119], v4 offset:21504\n"
"v_add_f32 v206, v206, v26\n"
"v_mfma_f32_16x16x32_bf16 v[124:127], a[56:59], a[112:115], v[124:127]\n"
"ds_read_b128 a[112:115], v4 offset:20480\n"
"v_add_f32 v207, v207, v27\n"
"v_mfma_f32_16x16x32_bf16 v[128:131], a[56:59], a[120:123], v[128:131]\n"
"v_add_f32 v208, v208, v24\n"
"v_mfma_f32_16x16x32_bf16 v[128:131], a[60:63], a[124:127], v[128:131]\n"
"v_add_f32 v209, v209, v25\n"
"v_mfma_f32_16x16x32_bf16 v[100:103], a[52:55], a[124:127], v[100:103]\n"
"ds_read_b128 a[124:127], v4 offset:23552\n"
"v_add_f32 v210, v210, v26\n"
"v_mfma_f32_16x16x32_bf16 v[100:103], a[48:51], a[120:123], v[100:103]\n"
"ds_read_b128 a[120:123], v4 offset:22528\n"
"v_add_f32 v211, v211, v27\n"
"v_mfma_f32_16x16x32_bf16 v[104:107], a[48:51], a[128:131], v[104:107]\n"
"v_add_f32 v212, v212, v24\n"
"v_add_f32 v213, v213, v25\n"
"v_mfma_f32_16x16x32_bf16 v[104:107], a[52:55], a[132:135], v[104:107]\n"
"global_load_dwordx4 a[40:43], v0, s[34:35] offset:2048 sc1\n"
"v_add_f32 v214, v214, v26\n"
"v_mfma_f32_16x16x32_bf16 v[132:135], a[60:63], a[132:135], v[132:135]\n"
"ds_read_b128 a[132:135], v4 offset:25600\n"
"v_add_f32 v215, v215, v27\n"
"v_mfma_f32_16x16x32_bf16 v[132:135], a[56:59], a[128:131], v[132:135]\n"
"ds_read_b128 a[128:131], v4 offset:24576\n"
"v_add_f32 v216, v216, v24\n"
"v_mfma_f32_16x16x32_bf16 v[136:139], a[56:59], a[136:139], v[136:139]\n"
"v_add_f32 v217, v217, v25\n"
"v_mfma_f32_16x16x32_bf16 v[136:139], a[60:63], a[140:143], v[136:139]\n"
"v_add_f32 v218, v218, v26\n"
"v_mfma_f32_16x16x32_bf16 v[108:111], a[52:55], a[140:143], v[108:111]\n"
"ds_read_b128 a[140:143], v4 offset:27648\n"
"v_add_f32 v219, v219, v27\n"
"v_mfma_f32_16x16x32_bf16 v[108:111], a[48:51], a[136:139], v[108:111]\n"
"ds_read_b128 a[136:139], v4 offset:26624\n"
"v_add_f32 v220, v220, v24\n"
"v_mfma_f32_16x16x32_bf16 v[112:115], a[48:51], a[144:147], v[112:115]\n"
"v_add_f32 v221, v221, v25\n"
"v_add_f32 v222, v222, v26\n"
"v_mfma_f32_16x16x32_bf16 v[112:115], a[52:55], a[148:151], v[112:115]\n"
"global_load_dwordx4 a[44:47], v0, s[34:35] offset:3072 sc1\n"
"v_add_f32 v223, v223, v27\n"
"v_mfma_f32_16x16x32_bf16 v[140:143], a[60:63], a[148:151], v[140:143]\n"
"ds_read_b128 a[148:151], v4 offset:29696\n"
"v_add_f32 v224, v224, v24\n"
"v_mfma_f32_16x16x32_bf16 v[140:143], a[56:59], a[144:147], v[140:143]\n"
"ds_read_b128 a[144:147], v4 offset:28672\n"
"v_add_f32 v225, v225, v25\n"
"v_mfma_f32_16x16x32_bf16 a[12:15], a[56:59], a[152:155], a[12:15]\n"
"v_add_f32 v226, v226, v26\n"
"v_mfma_f32_16x16x32_bf16 a[12:15], a[60:63], a[156:159], a[12:15]\n"
"v_add_f32 v227, v227, v27\n"
"v_mfma_f32_16x16x32_bf16 a[8:11], a[52:55], a[156:159], a[8:11]\n"
"ds_read_b128 a[156:159], v4 offset:31744\n"
"v_accvgpr_read_b32 v12, a24\n"
"v_mfma_f32_16x16x32_bf16 a[8:11], a[48:51], a[152:155], a[8:11]\n"
"s_waitcnt vmcnt(8) lgkmcnt(11)\n"
"ds_read_b128 a[152:155], v4 offset:30720\n"
"v_accvgpr_read_b32 v13, a25\n"
"v_mfma_f32_16x16x32_bf16 v[32:35], a[64:67], a[96:99], v[32:35]\n"
"s_add_u32 s34, s36, 1536000\n"
"v_accvgpr_read_b32 v14, a26\n"
"v_accvgpr_read_b32 v15, a27\n"
"v_mfma_f32_16x16x32_bf16 v[32:35], a[68:71], a[100:103], v[32:35]\n"
"global_load_dwordx4 a[48:51], v0, s[34:35] offset:0 sc1\n"
"v_add_f32 v12, v12, v24\n"
"v_mfma_f32_16x16x32_bf16 v[60:63], a[76:79], a[100:103], v[60:63]\n"
"v_add_f32 v13, v13, v25\n"
"v_mfma_f32_16x16x32_bf16 v[60:63], a[72:75], a[96:99], v[60:63]\n"
"v_add_f32 v14, v14, v26\n"
"v_mfma_f32_16x16x32_bf16 v[64:67], a[72:75], a[104:107], v[64:67]\n"
"v_add_f32 v15, v15, v27\n"
"v_mfma_f32_16x16x32_bf16 v[64:67], a[76:79], a[108:111], v[64:67]\n"
"v_accvgpr_write_b32 a24, v12\n"
"v_mfma_f32_16x16x32_bf16 v[36:39], a[68:71], a[108:111], v[36:39]\n"
"v_accvgpr_write_b32 a25, v13\n"
"v_accvgpr_write_b32 a26, v14\n"
"v_mfma_f32_16x16x32_bf16 v[36:39], a[64:67], a[104:107], v[36:39]\n"
"s_waitcnt lgkmcnt(8)\n"
"v_accvgpr_write_b32 a27, v15\n"
"v_mfma_f32_16x16x32_bf16 v[40:43], a[64:67], a[112:115], v[40:43]\n"
"v_lshlrev_b32 v28, 16, v30\n"
"v_mfma_f32_16x16x32_bf16 v[40:43], a[68:71], a[116:119], v[40:43]\n"
"global_load_dwordx4 a[52:55], v0, s[34:35] offset:1024 sc1\n"
"v_and_b32 v29, s63, v30\n"
"v_mfma_f32_16x16x32_bf16 v[68:71], a[76:79], a[116:119], v[68:71]\n"
"v_lshlrev_b32 v30, 16, v31\n"
"v_mfma_f32_16x16x32_bf16 v[68:71], a[72:75], a[112:115], v[68:71]\n"
"v_and_b32 v31, s63, v31\n"
"v_mfma_f32_16x16x32_bf16 v[72:75], a[72:75], a[120:123], v[72:75]\n"
"v_add_f32 v228, v228, v28\n"
"v_mfma_f32_16x16x32_bf16 v[72:75], a[76:79], a[124:127], v[72:75]\n"
"v_add_f32 v229, v229, v29\n"
"v_add_f32 v230, v230, v30\n"
"v_mfma_f32_16x16x32_bf16 v[44:47], a[68:71], a[124:127], v[44:47]\n"
"v_add_f32 v231, v231, v31\n"
"v_mfma_f32_16x16x32_bf16 v[44:47], a[64:67], a[120:123], v[44:47]\n"
"s_waitcnt lgkmcnt(4)\n"
"v_add_f32 v232, v232, v28\n"
"v_mfma_f32_16x16x32_bf16 v[48:51], a[64:67], a[128:131], v[48:51]\n"
"v_add_f32 v233, v233, v29\n"
"v_mfma_f32_16x16x32_bf16 v[48:51], a[68:71], a[132:135], v[48:51]\n"
"global_load_dwordx4 a[56:59], v0, s[34:35] offset:2048 sc1\n"
"v_add_f32 v234, v234, v30\n"
"v_mfma_f32_16x16x32_bf16 v[76:79], a[76:79], a[132:135], v[76:79]\n"
"v_add_f32 v235, v235, v31\n"
"v_mfma_f32_16x16x32_bf16 v[76:79], a[72:75], a[128:131], v[76:79]\n"
"v_add_f32 v236, v236, v28\n"
"v_mfma_f32_16x16x32_bf16 v[80:83], a[72:75], a[136:139], v[80:83]\n"
"v_add_f32 v237, v237, v29\n"
"v_mfma_f32_16x16x32_bf16 v[80:83], a[76:79], a[140:143], v[80:83]\n"
"v_add_f32 v238, v238, v30\n"
"v_add_f32 v239, v239, v31\n"
"v_mfma_f32_16x16x32_bf16 v[52:55], a[68:71], a[140:143], v[52:55]\n"
"v_add_f32 v240, v240, v28\n"
"v_mfma_f32_16x16x32_bf16 v[52:55], a[64:67], a[136:139], v[52:55]\n"
"s_waitcnt lgkmcnt(0)\n"
"v_add_f32 v241, v241, v29\n"
"v_mfma_f32_16x16x32_bf16 v[56:59], a[64:67], a[144:147], v[56:59]\n"
"v_add_f32 v242, v242, v30\n"
"v_mfma_f32_16x16x32_bf16 v[56:59], a[68:71], a[148:151], v[56:59]\n"
"global_load_dwordx4 a[60:63], v0, s[34:35] offset:3072 sc1\n"
"v_add_f32 v243, v243, v31\n"
"v_mfma_f32_16x16x32_bf16 v[84:87], a[76:79], a[148:151], v[84:87]\n"
"v_add_f32 v244, v244, v28\n"
"v_mfma_f32_16x16x32_bf16 v[84:87], a[72:75], a[144:147], v[84:87]\n"
"v_add_f32 v245, v245, v29\n"
"v_mfma_f32_16x16x32_bf16 a[4:7], a[72:75], a[152:155], a[4:7]\n"
"v_add_f32 v246, v246, v30\n"
"v_add_f32 v247, v247, v31\n"
"v_mfma_f32_16x16x32_bf16 a[4:7], a[76:79], a[156:159], a[4:7]\n"
"v_add_f32 v248, v248, v28\n"
"v_mfma_f32_16x16x32_bf16 a[0:3], a[68:71], a[156:159], a[0:3]\n"
"v_add_f32 v249, v249, v29\n"
"v_mfma_f32_16x16x32_bf16 a[0:3], a[64:67], a[152:155], a[0:3]\n"
"s_waitcnt vmcnt(8)\n"
"v_add_f32 v250, v250, v30\n"
"v_mfma_f32_16x16x32_bf16 v[88:91], a[80:83], a[96:99], v[88:91]\n"
"s_add_u32 s34, s36, 1540096\n"
"v_add_f32 v251, v251, v31\n"
"v_mfma_f32_16x16x32_bf16 v[88:91], a[84:87], a[100:103], v[88:91]\n"
"global_load_dwordx4 a[64:67], v0, s[34:35] offset:0 sc1\n"
"v_add_f32 v252, v252, v28\n"
"v_mfma_f32_16x16x32_bf16 v[116:119], a[92:95], a[100:103], v[116:119]\n"
"ds_read_b128 a[100:103], v5 offset:17408\n"
"v_add_f32 v253, v253, v29\n"
"v_mfma_f32_16x16x32_bf16 v[116:119], a[88:91], a[96:99], v[116:119]\n"
"ds_read_b128 a[96:99], v5 offset:16384\n"
"v_add_f32 v254, v254, v30\n"
"v_mfma_f32_16x16x32_bf16 v[120:123], a[88:91], a[104:107], v[120:123]\n"
"v_add_f32 v255, v255, v31\n"
"v_accvgpr_read_b32 v12, a28\n"
"v_mfma_f32_16x16x32_bf16 v[120:123], a[92:95], a[108:111], v[120:123]\n"
"v_accvgpr_read_b32 v13, a29\n"
"v_mfma_f32_16x16x32_bf16 v[92:95], a[84:87], a[108:111], v[92:95]\n"
"ds_read_b128 a[108:111], v5 offset:19456\n"
"v_accvgpr_read_b32 v14, a30\n"
"v_mfma_f32_16x16x32_bf16 v[92:95], a[80:83], a[104:107], v[92:95]\n"
"ds_read_b128 a[104:107], v5 offset:18432\n"
"v_accvgpr_read_b32 v15, a31\n"
"v_mfma_f32_16x16x32_bf16 v[96:99], a[80:83], a[112:115], v[96:99]\n"
"v_add_f32 v12, v12, v28\n"
"v_mfma_f32_16x16x32_bf16 v[96:99], a[84:87], a[116:119], v[96:99]\n"
"global_load_dwordx4 a[68:71], v0, s[34:35] offset:1024 sc1\n"
"v_add_f32 v13, v13, v29\n"
"v_mfma_f32_16x16x32_bf16 v[124:127], a[92:95], a[116:119], v[124:127]\n"
"ds_read_b128 a[116:119], v5 offset:21504\n"
"v_add_f32 v14, v14, v30\n"
"v_mfma_f32_16x16x32_bf16 v[124:127], a[88:91], a[112:115], v[124:127]\n"
"ds_read_b128 a[112:115], v5 offset:20480\n"
"v_add_f32 v15, v15, v31\n"
"v_mfma_f32_16x16x32_bf16 v[128:131], a[88:91], a[120:123], v[128:131]\n"
"v_accvgpr_write_b32 a28, v12\n"
"v_accvgpr_write_b32 a29, v13\n"
"v_mfma_f32_16x16x32_bf16 v[128:131], a[92:95], a[124:127], v[128:131]\n"
"v_accvgpr_write_b32 a30, v14\n"
"v_mfma_f32_16x16x32_bf16 v[100:103], a[84:87], a[124:127], v[100:103]\n"
"ds_read_b128 a[124:127], v5 offset:23552\n"
"v_accvgpr_write_b32 a31, v15\n"
"v_mfma_f32_16x16x32_bf16 v[100:103], a[80:83], a[120:123], v[100:103]\n"
"ds_read_b128 a[120:123], v5 offset:22528\n"
"v_mfma_f32_16x16x32_bf16 v[104:107], a[80:83], a[128:131], v[104:107]\n"
"v_mfma_f32_16x16x32_bf16 v[104:107], a[84:87], a[132:135], v[104:107]\n"
"global_load_dwordx4 a[72:75], v0, s[34:35] offset:2048 sc1\n"
"v_mfma_f32_16x16x32_bf16 v[132:135], a[92:95], a[132:135], v[132:135]\n"
"ds_read_b128 a[132:135], v5 offset:25600\n"
"v_mfma_f32_16x16x32_bf16 v[132:135], a[88:91], a[128:131], v[132:135]\n"
"ds_read_b128 a[128:131], v5 offset:24576\n"
"v_mfma_f32_16x16x32_bf16 v[136:139], a[88:91], a[136:139], v[136:139]\n"
"ds_read_b128 v[16:19], v7 offset:32768\n"
"v_mfma_f32_16x16x32_bf16 v[136:139], a[92:95], a[140:143], v[136:139]\n"
"v_mfma_f32_16x16x32_bf16 v[108:111], a[84:87], a[140:143], v[108:111]\n"
"ds_read_b128 a[140:143], v5 offset:27648\n"
"v_mfma_f32_16x16x32_bf16 v[108:111], a[80:83], a[136:139], v[108:111]\n"
"ds_read_b128 a[136:139], v5 offset:26624\n"
"v_mfma_f32_16x16x32_bf16 v[112:115], a[80:83], a[144:147], v[112:115]\n"
"v_mfma_f32_16x16x32_bf16 v[112:115], a[84:87], a[148:151], v[112:115]\n"
"global_load_dwordx4 a[76:79], v0, s[34:35] offset:3072 sc1\n"
"v_mfma_f32_16x16x32_bf16 v[140:143], a[92:95], a[148:151], v[140:143]\n"
"ds_read_b128 a[148:151], v5 offset:29696\n"
"v_mfma_f32_16x16x32_bf16 v[140:143], a[88:91], a[144:147], v[140:143]\n"
"ds_read_b128 a[144:147], v5 offset:28672\n"
"v_mfma_f32_16x16x32_bf16 a[12:15], a[88:91], a[152:155], a[12:15]\n"
"v_mfma_f32_16x16x32_bf16 a[12:15], a[92:95], a[156:159], a[12:15]\n"
"v_mfma_f32_16x16x32_bf16 a[8:11], a[84:87], a[156:159], a[8:11]\n"
"ds_read_b128 a[156:159], v5 offset:31744\n"
"v_mfma_f32_16x16x32_bf16 a[8:11], a[80:83], a[152:155], a[8:11]\n"
"s_waitcnt vmcnt(8) lgkmcnt(12)\n"
"ds_read_b128 a[152:155], v5 offset:30720\n"
"v_mfma_f32_16x16x32_bf16 v[32:35], a[32:35], a[96:99], v[32:35]\n"
"s_add_u32 s34, s36, 1544192\n"
"v_mfma_f32_16x16x32_bf16 v[32:35], a[36:39], a[100:103], v[32:35]\n"
"global_load_dwordx4 a[80:83], v0, s[34:35] offset:0 sc1\n"
"v_mfma_f32_16x16x32_bf16 v[60:63], a[44:47], a[100:103], v[60:63]\n"
"v_mfma_f32_16x16x32_bf16 v[60:63], a[40:43], a[96:99], v[60:63]\n"
"v_mfma_f32_16x16x32_bf16 v[64:67], a[40:43], a[104:107], v[64:67]\n"
"v_mfma_f32_16x16x32_bf16 v[64:67], a[44:47], a[108:111], v[64:67]\n"
"v_mfma_f32_16x16x32_bf16 v[36:39], a[36:39], a[108:111], v[36:39]\n"
"v_mfma_f32_16x16x32_bf16 v[36:39], a[32:35], a[104:107], v[36:39]\n"
"s_waitcnt lgkmcnt(9)\n"
"v_mfma_f32_16x16x32_bf16 v[40:43], a[32:35], a[112:115], v[40:43]\n"
"s_mov_b32 s52, %4\n"
"s_and_b32 s53, %5, 0xffff\n"
"v_mfma_f32_16x16x32_bf16 v[40:43], a[36:39], a[116:119], v[40:43]\n"
"global_load_dwordx4 a[84:87], v0, s[34:35] offset:1024 sc1\n"
"v_mfma_f32_16x16x32_bf16 v[68:71], a[44:47], a[116:119], v[68:71]\n"
"s_mov_b32 s54, 260352\n"
"s_mov_b32 s55, 0x20000\n"
"v_mfma_f32_16x16x32_bf16 v[68:71], a[40:43], a[112:115], v[68:71]\n"
"v_xor_b32 v8, 64, v7\n"
"v_mfma_f32_16x16x32_bf16 v[72:75], a[40:43], a[120:123], v[72:75]\n"
"s_waitcnt lgkmcnt(6)\n"
"v_lshlrev_b32 v12, 16, v16\n"
"v_mfma_f32_16x16x32_bf16 v[72:75], a[44:47], a[124:127], v[72:75]\n"
"v_and_b32 v13, s63, v16\n"
"v_lshlrev_b32 v14, 16, v17\n"
"v_mfma_f32_16x16x32_bf16 v[44:47], a[36:39], a[124:127], v[44:47]\n"
"v_and_b32 v15, s63, v17\n"
"v_mfma_f32_16x16x32_bf16 v[44:47], a[32:35], a[120:123], v[44:47]\n"
"s_waitcnt lgkmcnt(4)\n"
"v_xor_b32 v20, 64, v7\n"
"v_mfma_f32_16x16x32_bf16 v[48:51], a[32:35], a[128:131], v[48:51]\n"
"ds_read_b128 v[20:23], v20 offset:32768\n"
"v_mul_f32 v12, v12, v32\n"
"v_mfma_f32_16x16x32_bf16 v[48:51], a[36:39], a[132:135], v[48:51]\n"
"global_load_dwordx4 a[88:91], v0, s[34:35] offset:2048 sc1\n"
"v_mul_f32 v13, v13, v33\n"
"v_mfma_f32_16x16x32_bf16 v[76:79], a[44:47], a[132:135], v[76:79]\n"
"v_mul_f32 v14, v14, v34\n"
"v_mfma_f32_16x16x32_bf16 v[76:79], a[40:43], a[128:131], v[76:79]\n"
"v_mul_f32 v15, v15, v35\n"
"v_mfma_f32_16x16x32_bf16 v[80:83], a[40:43], a[136:139], v[80:83]\n"
"v_cvt_pk_bf16_f32 v24, v12, v13\n"
"v_mfma_f32_16x16x32_bf16 v[80:83], a[44:47], a[140:143], v[80:83]\n"
"v_cvt_pk_bf16_f32 v25, v14, v15\n"
"v_mfma_f32_16x16x32_bf16 v[52:55], a[36:39], a[140:143], v[52:55]\n"
"ds_write_b64 v7, v[24:25] offset:32768\n"
"v_mfma_f32_16x16x32_bf16 v[52:55], a[32:35], a[136:139], v[52:55]\n"
"s_waitcnt lgkmcnt(2)\n"
"v_lshlrev_b32 v12, 16, v18\n"
"v_mfma_f32_16x16x32_bf16 v[56:59], a[32:35], a[144:147], v[56:59]\n"
"v_and_b32 v13, s63, v18\n"
"v_mfma_f32_16x16x32_bf16 v[56:59], a[36:39], a[148:151], v[56:59]\n"
"global_load_dwordx4 a[92:95], v0, s[34:35] offset:3072 sc1\n"
"v_mfma_f32_16x16x32_bf16 v[84:87], a[44:47], a[148:151], v[84:87]\n"
"v_lshlrev_b32 v14, 16, v19\n"
"v_mfma_f32_16x16x32_bf16 v[84:87], a[40:43], a[144:147], v[84:87]\n"
"v_and_b32 v15, s63, v19\n"
"v_mfma_f32_16x16x32_bf16 a[4:7], a[40:43], a[152:155], a[4:7]\n"
"v_mul_f32 v12, v12, v60\n"
"v_mfma_f32_16x16x32_bf16 a[4:7], a[44:47], a[156:159], a[4:7]\n"
"v_mul_f32 v13, v13, v61\n"
"v_mfma_f32_16x16x32_bf16 a[0:3], a[36:39], a[156:159], a[0:3]\n"
"v_mul_f32 v14, v14, v62\n"
"v_mfma_f32_16x16x32_bf16 a[0:3], a[32:35], a[152:155], a[0:3]\n"
"s_waitcnt vmcnt(8)\n"
"v_mul_f32 v15, v15, v63\n"
"v_mfma_f32_16x16x32_bf16 v[88:91], a[48:51], a[96:99], v[88:91]\n"
"s_add_u32 s34, s36, 1466368\n"
"v_cvt_pk_bf16_f32 v26, v12, v13\n"
"v_mfma_f32_16x16x32_bf16 v[88:91], a[52:55], a[100:103], v[88:91]\n"
"global_load_dwordx4 a[32:35], v0, s[34:35] offset:0 sc1\n"
"v_mfma_f32_16x16x32_bf16 v[116:119], a[60:63], a[100:103], v[116:119]\n"
"v_cvt_pk_bf16_f32 v27, v14, v15\n"
"v_mfma_f32_16x16x32_bf16 v[116:119], a[56:59], a[96:99], v[116:119]\n"
"ds_write_b64 v7, v[26:27] offset:32776\n"
"v_mfma_f32_16x16x32_bf16 v[120:123], a[56:59], a[104:107], v[120:123]\n"
"s_waitcnt lgkmcnt(2)\n"
"v_lshlrev_b32 v12, 16, v20\n"
"v_mfma_f32_16x16x32_bf16 v[120:123], a[60:63], a[108:111], v[120:123]\n"
"v_and_b32 v13, s63, v20\n"
"v_mfma_f32_16x16x32_bf16 v[92:95], a[52:55], a[108:111], v[92:95]\n"
"v_lshlrev_b32 v14, 16, v21\n"
"v_mfma_f32_16x16x32_bf16 v[92:95], a[48:51], a[104:107], v[92:95]\n"
"v_and_b32 v15, s63, v21\n"
"v_mfma_f32_16x16x32_bf16 v[96:99], a[48:51], a[112:115], v[96:99]\n"
"ds_read_b128 v[16:19], v7 offset:34816\n"
"v_mfma_f32_16x16x32_bf16 v[96:99], a[52:55], a[116:119], v[96:99]\n"
"L_st10265:\n"
"s_cbranch_execz L_retC\n"
"global_load_dwordx4 a[36:39], v0, s[34:35] offset:1024 sc1\n"
"v_mul_f32 v12, v12, v88\n"
"v_mul_f32 v13, v13, v89\n"
"v_mfma_f32_16x16x32_bf16 v[124:127], a[60:63], a[116:119], v[124:127]\n"
"v_mul_f32 v14, v14, v90\n"
"v_mul_f32 v15, v15, v91\n"
"v_mfma_f32_16x16x32_bf16 v[124:127], a[56:59], a[112:115], v[124:127]\n"
"v_cvt_pk_bf16_f32 v28, v12, v13\n"
"v_cvt_pk_bf16_f32 v29, v14, v15\n"
"v_mfma_f32_16x16x32_bf16 v[128:131], a[56:59], a[120:123], v[128:131]\n"
"ds_write_b64 v8, v[28:29] offset:32768\n"
"v_mfma_f32_16x16x32_bf16 v[128:131], a[60:63], a[124:127], v[128:131]\n"
"v_lshlrev_b32 v12, 16, v22\n"
"v_and_b32 v13, s63, v22\n"
"v_mfma_f32_16x16x32_bf16 v[100:103], a[52:55], a[124:127], v[100:103]\n"
"v_lshlrev_b32 v14, 16, v23\n"
"v_and_b32 v15, s63, v23\n"
"v_mfma_f32_16x16x32_bf16 v[100:103], a[48:51], a[120:123], v[100:103]\n"
"v_mul_f32 v12, v12, v116\n"
"v_mul_f32 v13, v13, v117\n"
"v_mfma_f32_16x16x32_bf16 v[104:107], a[48:51], a[128:131], v[104:107]\n"
"v_mul_f32 v14, v14, v118\n"
"v_mul_f32 v15, v15, v119\n"
"v_mfma_f32_16x16x32_bf16 v[104:107], a[52:55], a[132:135], v[104:107]\n"
"global_load_dwordx4 a[40:43], v0, s[34:35] offset:2048 sc1\n"
"v_cvt_pk_bf16_f32 v30, v12, v13\n"
"v_mfma_f32_16x16x32_bf16 v[132:135], a[60:63], a[132:135], v[132:135]\n"
"v_cvt_pk_bf16_f32 v31, v14, v15\n"
"v_mfma_f32_16x16x32_bf16 v[132:135], a[56:59], a[128:131], v[132:135]\n"
"ds_write_b64 v8, v[30:31] offset:32776\n"
"v_mfma_f32_16x16x32_bf16 v[136:139], a[56:59], a[136:139], v[136:139]\n"
"ds_read_b128 v[32:35], v2 offset:32768\n"
"v_mfma_f32_16x16x32_bf16 v[136:139], a[60:63], a[140:143], v[136:139]\n"
"s_waitcnt lgkmcnt(3)\n"
"ds_read_b128 v[60:63], v2 offset:33792\n"
"v_lshlrev_b32 v12, 16, v16\n"
"v_mfma_f32_16x16x32_bf16 v[108:111], a[52:55], a[140:143], v[108:111]\n"
"s_add_u32 s96, s36, 1568768\n"
"v_and_b32 v13, s63, v16\n"
"v_lshlrev_b32 v14, 16, v17\n"
"v_mfma_f32_16x16x32_bf16 v[108:111], a[48:51], a[136:139], v[108:111]\n"
"v_and_b32 v15, s63, v17\n"
"v_xor_b32 v20, 64, v7\n"
"v_mfma_f32_16x16x32_bf16 v[112:115], a[48:51], a[144:147], v[112:115]\n"
"global_load_dwordx4 v[88:91], v0, s[96:97] offset:-4096 sc1\n"
"ds_read_b128 v[20:23], v20 offset:34816\n"
"v_mfma_f32_16x16x32_bf16 v[112:115], a[52:55], a[148:151], v[112:115]\n"
"global_load_dwordx4 a[44:47], v0, s[34:35] offset:3072 sc1\n"
"v_mul_f32 v12, v12, v36\n"
"v_mfma_f32_16x16x32_bf16 v[140:143], a[60:63], a[148:151], v[140:143]\n"
"v_mul_f32 v13, v13, v37\n"
"v_mul_f32 v14, v14, v38\n"
"v_mfma_f32_16x16x32_bf16 v[140:143], a[56:59], a[144:147], v[140:143]\n"
"v_mul_f32 v15, v15, v39\n"
"v_mfma_f32_16x16x32_bf16 a[12:15], a[56:59], a[152:155], a[12:15]\n"
"v_cvt_pk_bf16_f32 v24, v12, v13\n"
"v_mfma_f32_16x16x32_bf16 a[12:15], a[60:63], a[156:159], a[12:15]\n"
"v_cvt_pk_bf16_f32 v25, v14, v15\n"
"v_mfma_f32_16x16x32_bf16 a[8:11], a[52:55], a[156:159], a[8:11]\n"
"s_waitcnt lgkmcnt(2)\n"
"buffer_store_dwordx4 v[32:35], v6, s[52:55], s80 offen offset:0 nt\n"
"ds_write_b64 v7, v[24:25] offset:34816\n"
"v_mfma_f32_16x16x32_bf16 a[8:11], a[48:51], a[152:155], a[8:11]\n"
"s_waitcnt vmcnt(10)\n"
"v_lshlrev_b32 v12, 16, v18\n"
"v_and_b32 v13, s63, v18\n"
"v_mfma_f32_16x16x32_bf16 v[144:147], a[64:67], a[96:99], v[144:147]\n"
"s_add_u32 s34, s36, 1470464\n"
"v_lshlrev_b32 v14, 16, v19\n"
"v_and_b32 v15, s63, v19\n"
"v_mfma_f32_16x16x32_bf16 v[144:147], a[68:71], a[100:103], v[144:147]\n"
"global_load_dwordx4 a[48:51], v0, s[34:35] offset:0 sc1\n"
"v_mfma_f32_16x16x32_bf16 v[172:175], a[76:79], a[100:103], v[172:175]\n"
"global_load_dwordx4 v[116:119], v0, s[96:97] offset:-3072 sc1\n"
"v_mfma_f32_16x16x32_bf16 v[172:175], a[72:75], a[96:99], v[172:175]\n"
"v_mul_f32 v12, v12, v64\n"
"v_mfma_f32_16x16x32_bf16 v[176:179], a[72:75], a[104:107], v[176:179]\n"
"v_mul_f32 v13, v13, v65\n"
"v_mfma_f32_16x16x32_bf16 v[176:179], a[76:79], a[108:111], v[176:179]\n"
"v_mul_f32 v14, v14, v66\n"
"v_mfma_f32_16x16x32_bf16 v[148:151], a[68:71], a[108:111], v[148:151]\n"
"v_mul_f32 v15, v15, v67\n"
"v_mfma_f32_16x16x32_bf16 v[148:151], a[64:67], a[104:107], v[148:151]\n"
"v_cvt_pk_bf16_f32 v26, v12, v13\n"
"v_mfma_f32_16x16x32_bf16 v[152:155], a[64:67], a[112:115], v[152:155]\n"
"v_cvt_pk_bf16_f32 v27, v14, v15\n"
"v_mfma_f32_16x16x32_bf16 v[152:155], a[68:71], a[116:119], v[152:155]\n"
"global_load_dwordx4 a[52:55], v0, s[34:35] offset:1024 sc1\n"
"v_mfma_f32_16x16x32_bf16 v[180:183], a[76:79], a[116:119], v[180:183]\n"
"s_waitcnt lgkmcnt(2)\n"
"buffer_store_dwordx4 v[60:63], v6, s[52:55], s81 offen offset:1024 nt\n"
"ds_write_b64 v7, v[26:27] offset:34824\n"
"v_mfma_f32_16x16x32_bf16 v[180:183], a[72:75], a[112:115], v[180:183]\n"
"s_waitcnt lgkmcnt(2)\n"
"v_lshlrev_b32 v12, 16, v20\n"
"v_and_b32 v13, s63, v20\n"
"v_mfma_f32_16x16x32_bf16 v[184:187], a[72:75], a[120:123], v[184:187]\n"
"v_lshlrev_b32 v14, 16, v21\n"
"v_and_b32 v15, s63, v21\n"
"v_mfma_f32_16x16x32_bf16 v[184:187], a[76:79], a[124:127], v[184:187]\n"
"ds_read_b128 v[16:19], v7 offset:36864\n"
"v_mul_f32 v12, v12, v92\n"
"v_mfma_f32_16x16x32_bf16 v[156:159], a[68:71], a[124:127], v[156:159]\n"
"v_mul_f32 v13, v13, v93\n"
"v_mul_f32 v14, v14, v94\n"
"v_mfma_f32_16x16x32_bf16 v[156:159], a[64:67], a[120:123], v[156:159]\n"
"v_mul_f32 v15, v15, v95\n"
"v_cvt_pk_bf16_f32 v28, v12, v13\n"
"v_mfma_f32_16x16x32_bf16 v[160:163], a[64:67], a[128:131], v[160:163]\n"
"v_cvt_pk_bf16_f32 v29, v14, v15\n"
"v_mfma_f32_16x16x32_bf16 v[160:163], a[68:71], a[132:135], v[160:163]\n"
"global_load_dwordx4 a[56:59], v0, s[34:35] offset:2048 sc1\n"
"v_mfma_f32_16x16x32_bf16 v[188:191], a[76:79], a[132:135], v[188:191]\n"
"ds_write_b64 v8, v[28:29] offset:34816\n"
"v_mfma_f32_16x16x32_bf16 v[188:191], a[72:75], a[128:131], v[188:191]\n"
"v_lshlrev_b32 v12, 16, v22\n"
"v_and_b32 v13, s63, v22\n"
"v_mfma_f32_16x16x32_bf16 v[192:195], a[72:75], a[136:139], v[192:195]\n"
"v_lshlrev_b32 v14, 16, v23\n"
"v_and_b32 v15, s63, v23\n"
"v_mfma_f32_16x16x32_bf16 v[192:195], a[76:79], a[140:143], v[192:195]\n"
"v_mul_f32 v12, v12, v120\n"
"v_mul_f32 v13, v13, v121\n"
"v_mfma_f32_16x16x32_bf16 v[164:167], a[68:71], a[140:143], v[164:167]\n"
"v_mul_f32 v14, v14, v122\n"
"v_mul_f32 v15, v15, v123\n"
"v_mfma_f32_16x16x32_bf16 v[164:167], a[64:67], a[136:139], v[164:167]\n"
"v_cvt_pk_bf16_f32 v30, v12, v13\n"
"v_cvt_pk_bf16_f32 v31, v14, v15\n"
"v_mfma_f32_16x16x32_bf16 v[168:171], a[64:67], a[144:147], v[168:171]\n"
"ds_write_b64 v8, v[30:31] offset:34824\n"
"v_mfma_f32_16x16x32_bf16 v[168:171], a[68:71], a[148:151], v[168:171]\n"
"global_load_dwordx4 a[60:63], v0, s[34:35] offset:3072 sc1\n"
"ds_read_b128 v[36:39], v2 offset:34816\n"
"v_mfma_f32_16x16x32_bf16 v[196:199], a[76:79], a[148:151], v[196:199]\n"
"s_waitcnt lgkmcnt(3)\n"
"ds_read_b128 v[64:67], v2 offset:35840\n"
"v_lshlrev_b32 v12, 16, v16\n"
"v_mfma_f32_16x16x32_bf16 v[196:199], a[72:75], a[144:147], v[196:199]\n"
"v_and_b32 v13, s63, v16\n"
"v_lshlrev_b32 v14, 16, v17\n"
"v_mfma_f32_16x16x32_bf16 a[20:23], a[72:75], a[152:155], a[20:23]\n"
"v_and_b32 v15, s63, v17\n"
"v_xor_b32 v20, 64, v7\n"
"v_mfma_f32_16x16x32_bf16 a[20:23], a[76:79], a[156:159], a[20:23]\n"
"ds_read_b128 v[20:23], v20 offset:36864\n"
"v_mul_f32 v12, v12, v40\n"
"v_mfma_f32_16x16x32_bf16 a[16:19], a[68:71], a[156:159], a[16:19]\n"
"v_mul_f32 v13, v13, v41\n"
"v_mul_f32 v14, v14, v42\n"
"v_mfma_f32_16x16x32_bf16 a[16:19], a[64:67], a[152:155], a[16:19]\n"
"s_waitcnt vmcnt(12)\n"
"global_load_dwordx4 v[92:95], v0, s[96:97] offset:-2048 sc1\n"
"v_mul_f32 v15, v15, v43\n"
"v_mfma_f32_16x16x32_bf16 v[200:203], a[80:83], a[96:99], v[200:203]\n"
"s_add_u32 s34, s36, 1482752\n"
"v_cvt_pk_bf16_f32 v24, v12, v13\n"
"v_mfma_f32_16x16x32_bf16 v[200:203], a[84:87], a[100:103], v[200:203]\n"
"global_load_dwordx4 a[64:67], v0, s[34:35] offset:0 sc1\n"
"v_cvt_pk_bf16_f32 v25, v14, v15\n"
"v_mfma_f32_16x16x32_bf16 v[228:231], a[92:95], a[100:103], v[228:231]\n"
"ds_read_b128 a[100:103], v3 offset:1024\n"
"v_mfma_f32_16x16x32_bf16 v[228:231], a[88:91], a[96:99], v[228:231]\n"
"ds_read_b128 a[96:99], v3 offset:0\n"
"v_mfma_f32_16x16x32_bf16 v[232:235], a[88:91], a[104:107], v[232:235]\n"
"ds_write_b64 v7, v[24:25] offset:36864\n"
"v_mfma_f32_16x16x32_bf16 v[232:235], a[92:95], a[108:111], v[232:235]\n"
"v_lshlrev_b32 v12, 16, v18\n"
"v_and_b32 v13, s63, v18\n"
"v_mfma_f32_16x16x32_bf16 v[204:207], a[84:87], a[108:111], v[204:207]\n"
"ds_read_b128 a[108:111], v3 offset:3072\n"
"v_lshlrev_b32 v14, 16, v19\n"
"v_mfma_f32_16x16x32_bf16 v[204:207], a[80:83], a[104:107], v[204:207]\n"
"ds_read_b128 a[104:107], v3 offset:2048\n"
"v_and_b32 v15, s63, v19\n"
"v_mfma_f32_16x16x32_bf16 v[208:211], a[80:83], a[112:115], v[208:211]\n"
"s_waitcnt lgkmcnt(7)\n"
"buffer_store_dwordx4 v[36:39], v6, s[52:55], s82 offen offset:2048 nt\n"
"global_load_dwordx4 v[120:123], v0, s[96:97] offset:-1024 sc1\n"
"v_mul_f32 v12, v12, v68\n"
"v_mul_f32 v13, v13, v69\n"
"v_mul_f32 v14, v14, v70\n"
"v_mfma_f32_16x16x32_bf16 v[208:211], a[84:87], a[116:119], v[208:211]\n"
"global_load_dwordx4 a[68:71], v0, s[34:35] offset:1024 sc1\n"
"v_mul_f32 v15, v15, v71\n"
"v_mfma_f32_16x16x32_bf16 v[236:239], a[92:95], a[116:119], v[236:239]\n"
"ds_read_b128 a[116:119], v3 offset:5120\n"
"v_cvt_pk_bf16_f32 v26, v12, v13\n"
"v_mfma_f32_16x16x32_bf16 v[236:239], a[88:91], a[112:115], v[236:239]\n"
"ds_read_b128 a[112:115], v3 offset:4096\n"
"v_cvt_pk_bf16_f32 v27, v14, v15\n"
"v_mfma_f32_16x16x32_bf16 v[240:243], a[88:91], a[120:123], v[240:243]\n"
"ds_write_b64 v7, v[26:27] offset:36872\n"
"v_mfma_f32_16x16x32_bf16 v[240:243], a[92:95], a[124:127], v[240:243]\n"
"s_waitcnt lgkmcnt(8)\n"
"v_lshlrev_b32 v12, 16, v20\n"
"v_and_b32 v13, s63, v20\n"
"v_mfma_f32_16x16x32_bf16 v[212:215], a[84:87], a[124:127], v[212:215]\n"
"ds_read_b128 a[124:127], v3 offset:7168\n"
"v_lshlrev_b32 v14, 16, v21\n"
"v_mfma_f32_16x16x32_bf16 v[212:215], a[80:83], a[120:123], v[212:215]\n"
"ds_read_b128 a[120:123], v3 offset:6144\n"
"v_and_b32 v15, s63, v21\n"
"v_mfma_f32_16x16x32_bf16 v[216:219], a[80:83], a[128:131], v[216:219]\n"
"ds_read_b128 v[16:19], v7 offset:38912\n"
"v_mul_f32 v12, v12, v96\n"
"v_mfma_f32_16x16x32_bf16 v[216:219], a[84:87], a[132:135], v[216:219]\n"
"global_load_dwordx4 a[72:75], v0, s[34:35] offset:2048 sc1\n"
"v_mul_f32 v13, v13, v97\n"
"v_mfma_f32_16x16x32_bf16 v[244:247], a[92:95], a[132:135], v[244:247]\n"
"ds_read_b128 a[132:135], v3 offset:9216\n"
"v_mul_f32 v14, v14, v98\n"
"v_mfma_f32_16x16x32_bf16 v[244:247], a[88:91], a[128:131], v[244:247]\n"
"ds_read_b128 a[128:131], v3 offset:8192\n"
"v_mul_f32 v15, v15, v99\n"
"v_mfma_f32_16x16x32_bf16 v[248:251], a[88:91], a[136:139], v[248:251]\n"
"buffer_store_dwordx4 v[64:67], v6, s[52:55], s83 offen offset:3072 nt\n"
"global_load_dwordx4 v[32:35], v0, s[96:97] offset:0 sc1\n"
"v_cvt_pk_bf16_f32 v28, v12, v13\n"
"v_cvt_pk_bf16_f32 v29, v14, v15\n"
"v_mfma_f32_16x16x32_bf16 v[248:251], a[92:95], a[140:143], v[248:251]\n"
"ds_write_b64 v8, v[28:29] offset:36864\n"
"v_mfma_f32_16x16x32_bf16 v[220:223], a[84:87], a[140:143], v[220:223]\n"
"ds_read_b128 a[140:143], v3 offset:11264\n"
"v_lshlrev_b32 v12, 16, v22\n"
"v_mfma_f32_16x16x32_bf16 v[220:223], a[80:83], a[136:139], v[220:223]\n"
"ds_read_b128 a[136:139], v3 offset:10240\n"
"v_and_b32 v13, s63, v22\n"
"v_mfma_f32_16x16x32_bf16 v[224:227], a[80:83], a[144:147], v[224:227]\n"
"v_lshlrev_b32 v14, 16, v23\n"
"v_and_b32 v15, s63, v23\n"
"v_mfma_f32_16x16x32_bf16 v[224:227], a[84:87], a[148:151], v[224:227]\n"
"global_load_dwordx4 a[76:79], v0, s[34:35] offset:3072 sc1\n"
"v_mul_f32 v12, v12, v124\n"
"v_mfma_f32_16x16x32_bf16 v[252:255], a[92:95], a[148:151], v[252:255]\n"
"ds_read_b128 a[148:151], v3 offset:13312\n"
"v_mul_f32 v13, v13, v125\n"
"v_mfma_f32_16x16x32_bf16 v[252:255], a[88:91], a[144:147], v[252:255]\n"
"ds_read_b128 a[144:147], v3 offset:12288\n"
"v_mul_f32 v14, v14, v126\n"
"v_mfma_f32_16x16x32_bf16 a[28:31], a[88:91], a[152:155], a[28:31]\n"
"v_mul_f32 v15, v15, v127\n"
"v_cvt_pk_bf16_f32 v30, v12, v13\n"
"v_mfma_f32_16x16x32_bf16 a[28:31], a[92:95], a[156:159], a[28:31]\n"
"global_load_dwordx4 v[60:63], v0, s[96:97] offset:1024 sc1\n"
"v_cvt_pk_bf16_f32 v31, v14, v15\n"
"v_mfma_f32_16x16x32_bf16 a[24:27], a[84:87], a[156:159], a[24:27]\n"
"ds_read_b128 a[156:159], v3 offset:15360\n"
"v_mfma_f32_16x16x32_bf16 a[24:27], a[80:83], a[152:155], a[24:27]\n"
"s_waitcnt vmcnt(17) lgkmcnt(14)\n"
"ds_read_b128 a[152:155], v3 offset:14336\n"
"v_mfma_f32_16x16x32_bf16 v[144:147], a[32:35], a[96:99], v[144:147]\n"
"s_add_u32 s34, s36, 1486848\n"
"ds_write_b64 v8, v[30:31] offset:36872\n"
"v_mfma_f32_16x16x32_bf16 v[144:147], a[36:39], a[100:103], v[144:147]\n"
"global_load_dwordx4 a[80:83], v0, s[34:35] offset:0 sc1\n"
"ds_read_b128 v[40:43], v2 offset:36864\n"
"v_mfma_f32_16x16x32_bf16 v[172:175], a[44:47], a[100:103], v[172:175]\n"
"s_waitcnt lgkmcnt(11)\n"
"ds_read_b128 v[68:71], v2 offset:37888\n"
"v_lshlrev_b32 v12, 16, v16\n"
"v_mfma_f32_16x16x32_bf16 v[172:175], a[40:43], a[96:99], v[172:175]\n"
"v_and_b32 v13, s63, v16\n"
"v_lshlrev_b32 v14, 16, v17\n"
"v_mfma_f32_16x16x32_bf16 v[176:179], a[40:43], a[104:107], v[176:179]\n"
"v_and_b32 v15, s63, v17\n"
"v_xor_b32 v20, 64, v7\n"
"v_mfma_f32_16x16x32_bf16 v[176:179], a[44:47], a[108:111], v[176:179]\n"
"ds_read_b128 v[20:23], v20 offset:38912\n"
"v_mul_f32 v12, v12, v44\n"
"v_mfma_f32_16x16x32_bf16 v[148:151], a[36:39], a[108:111], v[148:151]\n"
"v_mul_f32 v13, v13, v45\n"
"v_mul_f32 v14, v14, v46\n"
"v_mfma_f32_16x16x32_bf16 v[148:151], a[32:35], a[104:107], v[148:151]\n"
"v_mul_f32 v15, v15, v47\n"
"v_cvt_pk_bf16_f32 v24, v12, v13\n"
"v_mfma_f32_16x16x32_bf16 v[152:155], a[32:35], a[112:115], v[152:155]\n"
"v_cvt_pk_bf16_f32 v25, v14, v15\n"
"v_mfma_f32_16x16x32_bf16 v[152:155], a[36:39], a[116:119], v[152:155]\n"
"global_load_dwordx4 a[84:87], v0, s[34:35] offset:1024 sc1\n"
"v_mfma_f32_16x16x32_bf16 v[180:183], a[44:47], a[116:119], v[180:183]\n"
"global_load_dwordx4 v[96:99], v0, s[96:97] offset:2048 sc1\n"
"v_mfma_f32_16x16x32_bf16 v[180:183], a[40:43], a[112:115], v[180:183]\n"
"ds_write_b64 v7, v[24:25] offset:38912\n"
"v_mfma_f32_16x16x32_bf16 v[184:187], a[40:43], a[120:123], v[184:187]\n"
"v_lshlrev_b32 v12, 16, v18\n"
"v_and_b32 v13, s63, v18\n"
"v_mfma_f32_16x16x32_bf16 v[184:187], a[44:47], a[124:127], v[184:187]\n"
"v_lshlrev_b32 v14, 16, v19\n"
"v_and_b32 v15, s63, v19\n"
"v_mfma_f32_16x16x32_bf16 v[156:159], a[36:39], a[124:127], v[156:159]\n"
"v_mul_f32 v12, v12, v72\n"
"v_mul_f32 v13, v13, v73\n"
"v_mfma_f32_16x16x32_bf16 v[156:159], a[32:35], a[120:123], v[156:159]\n"
"s_waitcnt lgkmcnt(9)\n"
"v_mul_f32 v14, v14, v74\n"
"v_mul_f32 v15, v15, v75\n"
"v_mfma_f32_16x16x32_bf16 v[160:163], a[32:35], a[128:131], v[160:163]\n"
"v_cvt_pk_bf16_f32 v26, v12, v13\n"
"v_cvt_pk_bf16_f32 v27, v14, v15\n"
"v_mfma_f32_16x16x32_bf16 v[160:163], a[36:39], a[132:135], v[160:163]\n"
"global_load_dwordx4 a[88:91], v0, s[34:35] offset:2048 sc1\n"
"v_mfma_f32_16x16x32_bf16 v[188:191], a[44:47], a[132:135], v[188:191]\n"
"s_waitcnt lgkmcnt(3)\n"
"buffer_store_dwordx4 v[40:43], v6, s[52:55], s84 offen offset:0 nt\n"
"ds_write_b64 v7, v[26:27] offset:38920\n"
"v_mfma_f32_16x16x32_bf16 v[188:191], a[40:43], a[128:131], v[188:191]\n"
"s_waitcnt lgkmcnt(2)\n"
"global_load_dwordx4 v[124:127], v0, s[96:97] offset:3072 sc1\n"
"v_lshlrev_b32 v12, 16, v20\n"
"v_mfma_f32_16x16x32_bf16 v[192:195], a[40:43], a[136:139], v[192:195]\n"
"v_and_b32 v13, s63, v20\n"
"v_lshlrev_b32 v14, 16, v21\n"
"v_mfma_f32_16x16x32_bf16 v[192:195], a[44:47], a[140:143], v[192:195]\n"
"v_and_b32 v15, s63, v21\n"
"v_mul_f32 v12, v12, v100\n"
"v_mfma_f32_16x16x32_bf16 v[164:167], a[36:39], a[140:143], v[164:167]\n"
"v_mul_f32 v13, v13, v101\n"
"v_mul_f32 v14, v14, v102\n"
"v_mfma_f32_16x16x32_bf16 v[164:167], a[32:35], a[136:139], v[164:167]\n"
"v_mul_f32 v15, v15, v103\n"
"v_cvt_pk_bf16_f32 v28, v12, v13\n"
"v_mfma_f32_16x16x32_bf16 v[168:171], a[32:35], a[144:147], v[168:171]\n"
"v_cvt_pk_bf16_f32 v29, v14, v15\n"
"v_mfma_f32_16x16x32_bf16 v[168:171], a[36:39], a[148:151], v[168:171]\n"
"global_load_dwordx4 a[92:95], v0, s[34:35] offset:3072 sc1\n"
"v_mfma_f32_16x16x32_bf16 v[196:199], a[44:47], a[148:151], v[196:199]\n"
"ds_write_b64 v8, v[28:29] offset:38912\n"
"v_mfma_f32_16x16x32_bf16 v[196:199], a[40:43], a[144:147], v[196:199]\n"
"v_lshlrev_b32 v12, 16, v22\n"
"v_and_b32 v13, s63, v22\n"
"v_mfma_f32_16x16x32_bf16 a[20:23], a[40:43], a[152:155], a[20:23]\n"
"v_lshlrev_b32 v14, 16, v23\n"
"v_and_b32 v15, s63, v23\n"
"v_mfma_f32_16x16x32_bf16 a[20:23], a[44:47], a[156:159], a[20:23]\n"
"v_mul_f32 v12, v12, v128\n"
"v_mfma_f32_16x16x32_bf16 a[16:19], a[36:39], a[156:159], a[16:19]\n"
"buffer_store_dwordx4 v[68:71], v6, s[52:55], s85 offen offset:1024 nt\n"
"v_mul_f32 v13, v13, v129\n"
"v_mul_f32 v14, v14, v130\n"
"v_mul_f32 v15, v15, v131\n"
"v_mfma_f32_16x16x32_bf16 a[16:19], a[32:35], a[152:155], a[16:19]\n"
"s_waitcnt vmcnt(18)\n"
"v_cvt_pk_bf16_f32 v30, v12, v13\n"
"v_cvt_pk_bf16_f32 v31, v14, v15\n"
"v_mfma_f32_16x16x32_bf16 v[200:203], a[48:51], a[96:99], v[200:203]\n"
"s_add_u32 s34, s36, 1499136\n"
"ds_write_b64 v8, v[30:31] offset:38920\n"
"v_mfma_f32_16x16x32_bf16 v[200:203], a[52:55], a[100:103], v[200:203]\n"
"global_load_dwordx4 a[32:35], v0, s[34:35] offset:0 sc1\n"
"ds_read_b128 v[44:47], v2 offset:38912\n"
"v_mfma_f32_16x16x32_bf16 v[228:231], a[60:63], a[100:103], v[228:231]\n"
"ds_read_b128 a[100:103], v4 offset:1024\n"
"v_mfma_f32_16x16x32_bf16 v[228:231], a[56:59], a[96:99], v[228:231]\n"
"ds_read_b128 a[96:99], v4 offset:0\n"
"v_mfma_f32_16x16x32_bf16 v[232:235], a[56:59], a[104:107], v[232:235]\n"
"ds_read_b128 v[72:75], v2 offset:39936\n"
"v_accvgpr_read_b32 v16, a160\n"
"v_mfma_f32_16x16x32_bf16 v[232:235], a[60:63], a[108:111], v[232:235]\n"
"v_accvgpr_read_b32 v17, a161\n"
"v_accvgpr_read_b32 v18, a162\n"
"v_mfma_f32_16x16x32_bf16 v[204:207], a[52:55], a[108:111], v[204:207]\n"
"ds_read_b128 a[108:111], v4 offset:3072\n"
"v_accvgpr_read_b32 v19, a163\n"
"v_mfma_f32_16x16x32_bf16 v[204:207], a[48:51], a[104:107], v[204:207]\n"
"ds_read_b128 a[104:107], v4 offset:2048\n"
"v_lshlrev_b32 v12, 16, v16\n"
"v_mfma_f32_16x16x32_bf16 v[208:211], a[48:51], a[112:115], v[208:211]\n"
"v_and_b32 v13, s63, v16\n"
"v_lshlrev_b32 v14, 16, v17\n"
"v_mfma_f32_16x16x32_bf16 v[208:211], a[52:55], a[116:119], v[208:211]\n"
"global_load_dwordx4 a[36:39], v0, s[34:35] offset:1024 sc1\n"
"v_and_b32 v15, s63, v17\n"
"v_mfma_f32_16x16x32_bf16 v[236:239], a[60:63], a[116:119], v[236:239]\n"
"ds_read_b128 a[116:119], v4 offset:5120\n"
"v_mul_f32 v12, v12, v48\n"
"v_mfma_f32_16x16x32_bf16 v[236:239], a[56:59], a[112:115], v[236:239]\n"
"ds_read_b128 a[112:115], v4 offset:4096\n"
"v_mul_f32 v13, v13, v49\n"
"v_mfma_f32_16x16x32_bf16 v[240:243], a[56:59], a[120:123], v[240:243]\n"
"v_mul_f32 v14, v14, v50\n"
"v_mul_f32 v15, v15, v51\n"
"v_mfma_f32_16x16x32_bf16 v[240:243], a[60:63], a[124:127], v[240:243]\n"
"v_cvt_pk_bf16_f32 v24, v12, v13\n"
"v_mfma_f32_16x16x32_bf16 v[212:215], a[52:55], a[124:127], v[212:215]\n"
"ds_read_b128 a[124:127], v4 offset:7168\n"
"v_cvt_pk_bf16_f32 v25, v14, v15\n"
"v_mfma_f32_16x16x32_bf16 v[212:215], a[48:51], a[120:123], v[212:215]\n"
"ds_read_b128 a[120:123], v4 offset:6144\n"
"v_mfma_f32_16x16x32_bf16 v[216:219], a[48:51], a[128:131], v[216:219]\n"
"ds_write_b64 v7, v[24:25] offset:32768\n"
"v_mfma_f32_16x16x32_bf16 v[216:219], a[52:55], a[132:135], v[216:219]\n"
"global_load_dwordx4 a[40:43], v0, s[34:35] offset:2048 sc1\n"
"v_lshlrev_b32 v12, 16, v18\n"
"v_mfma_f32_16x16x32_bf16 v[244:247], a[60:63], a[132:135], v[244:247]\n"
"ds_read_b128 a[132:135], v4 offset:9216\n"
"v_and_b32 v13, s63, v18\n"
"v_mfma_f32_16x16x32_bf16 v[244:247], a[56:59], a[128:131], v[244:247]\n"
"ds_read_b128 a[128:131], v4 offset:8192\n"
"v_lshlrev_b32 v14, 16, v19\n"
"v_mfma_f32_16x16x32_bf16 v[248:251], a[56:59], a[136:139], v[248:251]\n"
"s_add_u32 s96, s36, 1560576\n"
"v_and_b32 v15, s63, v19\n"
"v_mul_f32 v12, v12, v76\n"
"v_mfma_f32_16x16x32_bf16 v[248:251], a[60:63], a[140:143], v[248:251]\n"
"global_load_dwordx4 v[100:103], v0, s[96:97] offset:-4096 sc1\n"
"v_mul_f32 v13, v13, v77\n"
"v_mfma_f32_16x16x32_bf16 v[220:223], a[52:55], a[140:143], v[220:223]\n"
"ds_read_b128 a[140:143], v4 offset:11264\n"
"v_mul_f32 v14, v14, v78\n"
"v_mfma_f32_16x16x32_bf16 v[220:223], a[48:51], a[136:139], v[220:223]\n"
"ds_read_b128 a[136:139], v4 offset:10240\n"
"v_mul_f32 v15, v15, v79\n"
"v_mfma_f32_16x16x32_bf16 v[224:227], a[48:51], a[144:147], v[224:227]\n"
"v_cvt_pk_bf16_f32 v26, v12, v13\n"
"v_cvt_pk_bf16_f32 v27, v14, v15\n"
"v_mfma_f32_16x16x32_bf16 v[224:227], a[52:55], a[148:151], v[224:227]\n"
"global_load_dwordx4 a[44:47], v0, s[34:35] offset:3072 sc1\n"
"v_mfma_f32_16x16x32_bf16 v[252:255], a[60:63], a[148:151], v[252:255]\n"
"ds_read_b128 a[148:151], v4 offset:13312\n"
"v_mfma_f32_16x16x32_bf16 v[252:255], a[56:59], a[144:147], v[252:255]\n"
"ds_read_b128 a[144:147], v4 offset:12288\n"
"v_mfma_f32_16x16x32_bf16 a[28:31], a[56:59], a[152:155], a[28:31]\n"
"s_waitcnt lgkmcnt(15)\n"
"buffer_store_dwordx4 v[44:47], v6, s[52:55], s86 offen offset:2048 nt\n"
"ds_write_b64 v7, v[26:27] offset:32776\n"
"v_mfma_f32_16x16x32_bf16 a[28:31], a[60:63], a[156:159], a[28:31]\n"
"v_accvgpr_read_b32 v16, a164\n"
"v_accvgpr_read_b32 v17, a165\n"
"v_mfma_f32_16x16x32_bf16 a[24:27], a[52:55], a[156:159], a[24:27]\n"
"ds_read_b128 a[156:159], v4 offset:15360\n"
"v_accvgpr_read_b32 v18, a166\n"
"v_mfma_f32_16x16x32_bf16 a[24:27], a[48:51], a[152:155], a[24:27]\n"
"s_waitcnt vmcnt(15) lgkmcnt(13)\n"
"ds_read_b128 a[152:155], v4 offset:14336\n"
"v_accvgpr_read_b32 v19, a167\n"
"v_mfma_f32_16x16x32_bf16 v[144:147], a[64:67], a[96:99], v[144:147]\n"
"s_add_u32 s34, s36, 1503232\n"
"global_load_dwordx4 v[128:131], v0, s[96:97] offset:-3072 sc1\n"
"v_mfma_f32_16x16x32_bf16 v[144:147], a[68:71], a[100:103], v[144:147]\n"
"global_load_dwordx4 a[48:51], v0, s[34:35] offset:0 sc1\n"
"v_lshlrev_b32 v12, 16, v16\n"
"v_mfma_f32_16x16x32_bf16 v[172:175], a[76:79], a[100:103], v[172:175]\n"
"v_and_b32 v13, s63, v16\n"
"v_lshlrev_b32 v14, 16, v17\n"
"v_mfma_f32_16x16x32_bf16 v[172:175], a[72:75], a[96:99], v[172:175]\n"
"v_and_b32 v15, s63, v17\n"
"v_mul_f32 v12, v12, v104\n"
"v_mfma_f32_16x16x32_bf16 v[176:179], a[72:75], a[104:107], v[176:179]\n"
"v_mul_f32 v13, v13, v105\n"
"v_mul_f32 v14, v14, v106\n"
"v_mfma_f32_16x16x32_bf16 v[176:179], a[76:79], a[108:111], v[176:179]\n"
"v_mul_f32 v15, v15, v107\n"
"v_mfma_f32_16x16x32_bf16 v[148:151], a[68:71], a[108:111], v[148:151]\n"
"v_cvt_pk_bf16_f32 v28, v12, v13\n"
"v_mfma_f32_16x16x32_bf16 v[148:151], a[64:67], a[104:107], v[148:151]\n"
"s_waitcnt lgkmcnt(10)\n"
"v_cvt_pk_bf16_f32 v29, v14, v15\n"
"v_mfma_f32_16x16x32_bf16 v[152:155], a[64:67], a[112:115], v[152:155]\n"
"buffer_store_dwordx4 v[72:75], v6, s[52:55], s87 offen offset:3072 nt\n"
"ds_write_b64 v8, v[28:29] offset:32768\n"
"v_mfma_f32_16x16x32_bf16 v[152:155], a[68:71], a[116:119], v[152:155]\n"
"global_load_dwordx4 a[52:55], v0, s[34:35] offset:1024 sc1\n"
"v_lshlrev_b32 v12, 16, v18\n"
"v_mfma_f32_16x16x32_bf16 v[180:183], a[76:79], a[116:119], v[180:183]\n"
"global_load_dwordx4 v[36:39], v0, s[96:97] offset:-2048 sc1\n"
"v_and_b32 v13, s63, v18\n"
"v_mfma_f32_16x16x32_bf16 v[180:183], a[72:75], a[112:115], v[180:183]\n"
"v_lshlrev_b32 v14, 16, v19\n"
"v_and_b32 v15, s63, v19\n"
"v_mfma_f32_16x16x32_bf16 v[184:187], a[72:75], a[120:123], v[184:187]\n"
"v_mul_f32 v12, v12, v132\n"
"v_mul_f32 v13, v13, v133\n"
"v_mfma_f32_16x16x32_bf16 v[184:187], a[76:79], a[124:127], v[184:187]\n"
"v_mul_f32 v14, v14, v134\n"
"v_mul_f32 v15, v15, v135\n"
"v_mfma_f32_16x16x32_bf16 v[156:159], a[68:71], a[124:127], v[156:159]\n"
"v_cvt_pk_bf16_f32 v30, v12, v13\n"
"v_cvt_pk_bf16_f32 v31, v14, v15\n"
"v_mfma_f32_16x16x32_bf16 v[156:159], a[64:67], a[120:123], v[156:159]\n"
"s_waitcnt lgkmcnt(6)\n"
"ds_write_b64 v8, v[30:31] offset:32776\n"
"v_mfma_f32_16x16x32_bf16 v[160:163], a[64:67], a[128:131], v[160:163]\n"
"ds_read_b128 v[48:51], v2 offset:32768\n"
"v_mfma_f32_16x16x32_bf16 v[160:163], a[68:71], a[132:135], v[160:163]\n"
"global_load_dwordx4 a[56:59], v0, s[34:35] offset:2048 sc1\n"
"ds_read_b128 v[76:79], v2 offset:33792\n"
"v_mfma_f32_16x16x32_bf16 v[188:191], a[76:79], a[132:135], v[188:191]\n"
"v_accvgpr_read_b32 v16, a168\n"
"v_accvgpr_read_b32 v17, a169\n"
"v_mfma_f32_16x16x32_bf16 v[188:191], a[72:75], a[128:131], v[188:191]\n"
"v_accvgpr_read_b32 v18, a170\n"
"v_accvgpr_read_b32 v19, a171\n"
"v_mfma_f32_16x16x32_bf16 v[192:195], a[72:75], a[136:139], v[192:195]\n"
"global_load_dwordx4 v[64:67], v0, s[96:97] offset:-1024 sc1\n"
"v_lshlrev_b32 v12, 16, v16\n"
"v_mfma_f32_16x16x32_bf16 v[192:195], a[76:79], a[140:143], v[192:195]\n"
"v_and_b32 v13, s63, v16\n"
"v_lshlrev_b32 v14, 16, v17\n"
"v_mfma_f32_16x16x32_bf16 v[164:167], a[68:71], a[140:143], v[164:167]\n"
"v_and_b32 v15, s63, v17\n"
"v_mul_f32 v12, v12, v52\n"
"v_mfma_f32_16x16x32_bf16 v[164:167], a[64:67], a[136:139], v[164:167]\n"
"s_waitcnt lgkmcnt(4)\n"
"v_mul_f32 v13, v13, v53\n"
"v_mul_f32 v14, v14, v54\n"
"v_mfma_f32_16x16x32_bf16 v[168:171], a[64:67], a[144:147], v[168:171]\n"
"v_mul_f32 v15, v15, v55\n"
"v_cvt_pk_bf16_f32 v24, v12, v13\n"
"v_mfma_f32_16x16x32_bf16 v[168:171], a[68:71], a[148:151], v[168:171]\n"
"global_load_dwordx4 a[60:63], v0, s[34:35] offset:3072 sc1\n"
"v_cvt_pk_bf16_f32 v25, v14, v15\n"
"v_mfma_f32_16x16x32_bf16 v[196:199], a[76:79], a[148:151], v[196:199]\n"
"ds_write_b64 v7, v[24:25] offset:34816\n"
"v_mfma_f32_16x16x32_bf16 v[196:199], a[72:75], a[144:147], v[196:199]\n"
"v_lshlrev_b32 v12, 16, v18\n"
"v_and_b32 v13, s63, v18\n"
"v_mfma_f32_16x16x32_bf16 a[20:23], a[72:75], a[152:155], a[20:23]\n"
"v_lshlrev_b32 v14, 16, v19\n"
"v_and_b32 v15, s63, v19\n"
"v_mfma_f32_16x16x32_bf16 a[20:23], a[76:79], a[156:159], a[20:23]\n"
"v_mul_f32 v12, v12, v80\n"
"v_mfma_f32_16x16x32_bf16 a[16:19], a[68:71], a[156:159], a[16:19]\n"
"global_load_dwordx4 v[104:107], v0, s[96:97] offset:0 sc1\n"
"v_mul_f32 v13, v13, v81\n"
"v_mfma_f32_16x16x32_bf16 a[16:19], a[64:67], a[152:155], a[16:19]\n"
"s_waitcnt vmcnt(16)\n"
"v_mul_f32 v14, v14, v82\n"
"v_mul_f32 v15, v15, v83\n"
"v_mfma_f32_16x16x32_bf16 v[200:203], a[80:83], a[96:99], v[200:203]\n"
"s_add_u32 s34, s36, 1548288\n"
"v_cvt_pk_bf16_f32 v26, v12, v13\n"
"v_mfma_f32_16x16x32_bf16 v[200:203], a[84:87], a[100:103], v[200:203]\n"
"global_load_dwordx4 a[64:67], v0, s[34:35] offset:0 sc1\n"
"v_cvt_pk_bf16_f32 v27, v14, v15\n"
"v_mfma_f32_16x16x32_bf16 v[228:231], a[92:95], a[100:103], v[228:231]\n"
"ds_read_b128 a[100:103], v5 offset:1024\n"
"v_mfma_f32_16x16x32_bf16 v[228:231], a[88:91], a[96:99], v[228:231]\n"
"ds_read_b128 a[96:99], v5 offset:0\n"
"v_mfma_f32_16x16x32_bf16 v[232:235], a[88:91], a[104:107], v[232:235]\n"
"ds_write_b64 v7, v[26:27] offset:34824\n"
"v_mfma_f32_16x16x32_bf16 v[232:235], a[92:95], a[108:111], v[232:235]\n"
"v_accvgpr_read_b32 v16, a172\n"
"v_accvgpr_read_b32 v17, a173\n"
"v_mfma_f32_16x16x32_bf16 v[204:207], a[84:87], a[108:111], v[204:207]\n"
"s_waitcnt lgkmcnt(5)\n"
"buffer_store_dwordx4 v[48:51], v6, s[52:55], s88 offen offset:0 nt\n"
"ds_read_b128 a[108:111], v5 offset:3072\n"
"v_accvgpr_read_b32 v18, a174\n"
"v_accvgpr_read_b32 v19, a175\n"
"v_mfma_f32_16x16x32_bf16 v[204:207], a[80:83], a[104:107], v[204:207]\n"
"ds_read_b128 a[104:107], v5 offset:2048\n"
"v_lshlrev_b32 v12, 16, v16\n"
"v_mfma_f32_16x16x32_bf16 v[208:211], a[80:83], a[112:115], v[208:211]\n"
"global_load_dwordx4 v[132:135], v0, s[96:97] offset:1024 sc1\n"
"v_and_b32 v13, s63, v16\n"
"v_mfma_f32_16x16x32_bf16 v[208:211], a[84:87], a[116:119], v[208:211]\n"
"global_load_dwordx4 a[68:71], v0, s[34:35] offset:1024 sc1\n"
"v_lshlrev_b32 v14, 16, v17\n"
"v_mfma_f32_16x16x32_bf16 v[236:239], a[92:95], a[116:119], v[236:239]\n"
"ds_read_b128 a[116:119], v5 offset:5120\n"
"v_and_b32 v15, s63, v17\n"
"v_mfma_f32_16x16x32_bf16 v[236:239], a[88:91], a[112:115], v[236:239]\n"
"ds_read_b128 a[112:115], v5 offset:4096\n"
"v_mul_f32 v12, v12, v108\n"
"v_mfma_f32_16x16x32_bf16 v[240:243], a[88:91], a[120:123], v[240:243]\n"
"v_mul_f32 v13, v13, v109\n"
"v_mul_f32 v14, v14, v110\n"
"v_mfma_f32_16x16x32_bf16 v[240:243], a[92:95], a[124:127], v[240:243]\n"
"v_mul_f32 v15, v15, v111\n"
"v_cvt_pk_bf16_f32 v28, v12, v13\n"
"v_mfma_f32_16x16x32_bf16 v[212:215], a[84:87], a[124:127], v[212:215]\n"
"ds_read_b128 a[124:127], v5 offset:7168\n"
"v_cvt_pk_bf16_f32 v29, v14, v15\n"
"v_mfma_f32_16x16x32_bf16 v[212:215], a[80:83], a[120:123], v[212:215]\n"
"ds_read_b128 a[120:123], v5 offset:6144\n"
"v_mfma_f32_16x16x32_bf16 v[216:219], a[80:83], a[128:131], v[216:219]\n"
"ds_write_b64 v8, v[28:29] offset:34816\n"
"v_mfma_f32_16x16x32_bf16 v[216:219], a[84:87], a[132:135], v[216:219]\n"
"global_load_dwordx4 a[72:75], v0, s[34:35] offset:2048 sc1\n"
"v_lshlrev_b32 v12, 16, v18\n"
"v_mfma_f32_16x16x32_bf16 v[244:247], a[92:95], a[132:135], v[244:247]\n"
"s_waitcnt lgkmcnt(11)\n"
"buffer_store_dwordx4 v[76:79], v6, s[52:55], s89 offen offset:1024 nt\n"
"ds_read_b128 a[132:135], v5 offset:9216\n"
"v_and_b32 v13, s63, v18\n"
"v_lshlrev_b32 v14, 16, v19\n"
"v_mfma_f32_16x16x32_bf16 v[244:247], a[88:91], a[128:131], v[244:247]\n"
"ds_read_b128 a[128:131], v5 offset:8192\n"
"v_and_b32 v15, s63, v19\n"
"v_mfma_f32_16x16x32_bf16 v[248:251], a[88:91], a[136:139], v[248:251]\n"
"v_mul_f32 v12, v12, v136\n"
"v_mul_f32 v13, v13, v137\n"
"v_mfma_f32_16x16x32_bf16 v[248:251], a[92:95], a[140:143], v[248:251]\n"
"v_mul_f32 v14, v14, v138\n"
"v_mul_f32 v15, v15, v139\n"
"v_mfma_f32_16x16x32_bf16 v[220:223], a[84:87], a[140:143], v[220:223]\n"
"ds_read_b128 a[140:143], v5 offset:11264\n"
"v_cvt_pk_bf16_f32 v30, v12, v13\n"
"v_mfma_f32_16x16x32_bf16 v[220:223], a[80:83], a[136:139], v[220:223]\n"
"ds_read_b128 a[136:139], v5 offset:10240\n"
"v_cvt_pk_bf16_f32 v31, v14, v15\n"
"v_mfma_f32_16x16x32_bf16 v[224:227], a[80:83], a[144:147], v[224:227]\n"
"ds_write_b64 v8, v[30:31] offset:34824\n"
"v_mfma_f32_16x16x32_bf16 v[224:227], a[84:87], a[148:151], v[224:227]\n"
"global_load_dwordx4 a[76:79], v0, s[34:35] offset:3072 sc1\n"
"ds_read_b128 v[52:55], v2 offset:34816\n"
"v_mfma_f32_16x16x32_bf16 v[252:255], a[92:95], a[148:151], v[252:255]\n"
"ds_read_b128 a[148:151], v5 offset:13312\n"
"v_mfma_f32_16x16x32_bf16 v[252:255], a[88:91], a[144:147], v[252:255]\n"
"L_st10875:\n"
"s_cbranch_execz L_retC\n"
"ds_read_b128 a[144:147], v5 offset:12288\n"
"v_mfma_f32_16x16x32_bf16 a[28:31], a[88:91], a[152:155], a[28:31]\n"
"ds_read_b128 v[80:83], v2 offset:35840\n"
"v_accvgpr_read_b32 v16, a176\n"
"v_mfma_f32_16x16x32_bf16 a[28:31], a[92:95], a[156:159], a[28:31]\n"
"v_accvgpr_read_b32 v17, a177\n"
"v_accvgpr_read_b32 v18, a178\n"
"v_mfma_f32_16x16x32_bf16 a[24:27], a[84:87], a[156:159], a[24:27]\n"
"ds_read_b128 a[156:159], v5 offset:15360\n"
"v_accvgpr_read_b32 v19, a179\n"
"v_mfma_f32_16x16x32_bf16 a[24:27], a[80:83], a[152:155], a[24:27]\n"
"s_waitcnt vmcnt(17) lgkmcnt(15)\n"
"ds_read_b128 a[152:155], v5 offset:14336\n"
"v_lshlrev_b32 v12, 16, v16\n"
"v_mfma_f32_16x16x32_bf16 v[144:147], a[32:35], a[96:99], v[144:147]\n"
"s_add_u32 s34, s36, 1552384\n"
"v_and_b32 v13, s63, v16\n"
"v_lshlrev_b32 v14, 16, v17\n"
"v_mfma_f32_16x16x32_bf16 v[144:147], a[36:39], a[100:103], v[144:147]\n"
"global_load_dwordx4 a[80:83], v0, s[34:35] offset:0 sc1\n"
"v_and_b32 v15, s63, v17\n"
"v_mfma_f32_16x16x32_bf16 v[172:175], a[44:47], a[100:103], v[172:175]\n"
"v_mul_f32 v12, v12, v56\n"
"v_mul_f32 v13, v13, v57\n"
"v_mfma_f32_16x16x32_bf16 v[172:175], a[40:43], a[96:99], v[172:175]\n"
"v_mul_f32 v14, v14, v58\n"
"v_mul_f32 v15, v15, v59\n"
"v_mfma_f32_16x16x32_bf16 v[176:179], a[40:43], a[104:107], v[176:179]\n"
"global_load_dwordx4 a[84:87], v0, s[34:35] offset:1024 sc1\n"
"v_cvt_pk_bf16_f32 v24, v12, v13\n"
"v_mfma_f32_16x16x32_bf16 v[176:179], a[44:47], a[108:111], v[176:179]\n"
"v_cvt_pk_bf16_f32 v25, v14, v15\n"
"v_mfma_f32_16x16x32_bf16 v[148:151], a[36:39], a[108:111], v[148:151]\n"
"ds_write_b64 v7, v[24:25] offset:36864\n"
"v_mfma_f32_16x16x32_bf16 v[148:151], a[32:35], a[104:107], v[148:151]\n"
"s_waitcnt lgkmcnt(13)\n"
"global_load_dwordx4 a[88:91], v0, s[34:35] offset:2048 sc1\n"
"v_lshlrev_b32 v12, 16, v18\n"
"v_mfma_f32_16x16x32_bf16 v[152:155], a[32:35], a[112:115], v[152:155]\n"
"global_load_dwordx4 v[108:111], v0, s[96:97] offset:2048 sc1\n"
"v_and_b32 v13, s63, v18\n"
"v_mfma_f32_16x16x32_bf16 v[152:155], a[36:39], a[116:119], v[152:155]\n"
"v_lshlrev_b32 v14, 16, v19\n"
"v_and_b32 v15, s63, v19\n"
"v_mfma_f32_16x16x32_bf16 v[180:183], a[44:47], a[116:119], v[180:183]\n"
"global_load_dwordx4 a[92:95], v0, s[34:35] offset:3072 sc1\n"
"v_mul_f32 v12, v12, v84\n"
"v_mfma_f32_16x16x32_bf16 v[180:183], a[40:43], a[112:115], v[180:183]\n"
"v_mul_f32 v13, v13, v85\n"
"v_mul_f32 v14, v14, v86\n"
"v_mfma_f32_16x16x32_bf16 v[184:187], a[40:43], a[120:123], v[184:187]\n"
"v_mul_f32 v15, v15, v87\n"
"v_cvt_pk_bf16_f32 v26, v12, v13\n"
"v_mfma_f32_16x16x32_bf16 v[184:187], a[44:47], a[124:127], v[184:187]\n"
"v_cvt_pk_bf16_f32 v27, v14, v15\n"
"v_mfma_f32_16x16x32_bf16 v[156:159], a[36:39], a[124:127], v[156:159]\n"
"ds_write_b64 v7, v[26:27] offset:36872\n"
"v_mfma_f32_16x16x32_bf16 v[156:159], a[32:35], a[120:123], v[156:159]\n"
"s_waitcnt lgkmcnt(9)\n"
"v_accvgpr_read_b32 v16, a180\n"
"v_accvgpr_read_b32 v17, a181\n"
"v_mfma_f32_16x16x32_bf16 v[160:163], a[32:35], a[128:131], v[160:163]\n"
"s_waitcnt lgkmcnt(7)\n"
"buffer_store_dwordx4 v[52:55], v6, s[52:55], s90 offen offset:2048 nt\n"
"v_accvgpr_read_b32 v18, a182\n"
"v_accvgpr_read_b32 v19, a183\n"
"v_lshlrev_b32 v12, 16, v16\n"
"v_mfma_f32_16x16x32_bf16 v[160:163], a[36:39], a[132:135], v[160:163]\n"
"global_load_dwordx4 v[136:139], v0, s[96:97] offset:3072 sc1\n"
"v_and_b32 v13, s63, v16\n"
"v_mfma_f32_16x16x32_bf16 v[188:191], a[44:47], a[132:135], v[188:191]\n"
"v_lshlrev_b32 v14, 16, v17\n"
"v_and_b32 v15, s63, v17\n"
"v_mfma_f32_16x16x32_bf16 v[188:191], a[40:43], a[128:131], v[188:191]\n"
"v_mul_f32 v12, v12, v112\n"
"v_mul_f32 v13, v13, v113\n"
"v_mfma_f32_16x16x32_bf16 v[192:195], a[40:43], a[136:139], v[192:195]\n"
"v_mul_f32 v14, v14, v114\n"
"v_mul_f32 v15, v15, v115\n"
"v_mfma_f32_16x16x32_bf16 v[192:195], a[44:47], a[140:143], v[192:195]\n"
"v_cvt_pk_bf16_f32 v28, v12, v13\n"
"v_cvt_pk_bf16_f32 v29, v14, v15\n"
"v_mfma_f32_16x16x32_bf16 v[164:167], a[36:39], a[140:143], v[164:167]\n"
"ds_write_b64 v8, v[28:29] offset:36864\n"
"v_mfma_f32_16x16x32_bf16 v[164:167], a[32:35], a[136:139], v[164:167]\n"
"s_waitcnt lgkmcnt(3)\n"
"v_lshlrev_b32 v12, 16, v18\n"
"v_and_b32 v13, s63, v18\n"
"v_mfma_f32_16x16x32_bf16 v[168:171], a[32:35], a[144:147], v[168:171]\n"
"v_lshlrev_b32 v14, 16, v19\n"
"v_and_b32 v15, s63, v19\n"
"v_mfma_f32_16x16x32_bf16 v[168:171], a[36:39], a[148:151], v[168:171]\n"
"v_mul_f32 v12, v12, v140\n"
"v_mul_f32 v13, v13, v141\n"
"v_mfma_f32_16x16x32_bf16 v[196:199], a[44:47], a[148:151], v[196:199]\n"
"v_mul_f32 v14, v14, v142\n"
"v_mul_f32 v15, v15, v143\n"
"v_mfma_f32_16x16x32_bf16 v[196:199], a[40:43], a[144:147], v[196:199]\n"
"v_cvt_pk_bf16_f32 v30, v12, v13\n"
"v_cvt_pk_bf16_f32 v31, v14, v15\n"
"v_mfma_f32_16x16x32_bf16 a[20:23], a[40:43], a[152:155], a[20:23]\n"
"buffer_store_dwordx4 v[80:83], v6, s[52:55], s91 offen offset:3072 nt\n"
"ds_write_b64 v8, v[30:31] offset:36872\n"
"v_mfma_f32_16x16x32_bf16 a[20:23], a[44:47], a[156:159], a[20:23]\n"
"ds_read_b128 v[56:59], v2 offset:36864\n"
"v_mfma_f32_16x16x32_bf16 a[16:19], a[36:39], a[156:159], a[16:19]\n"
"ds_read_b128 v[84:87], v2 offset:37888\n"
"v_mfma_f32_16x16x32_bf16 a[16:19], a[32:35], a[152:155], a[16:19]\n"
"s_waitcnt vmcnt(16)\n"
"v_accvgpr_read_b32 v16, a184\n"
"v_mfma_f32_16x16x32_bf16 v[200:203], a[48:51], a[96:99], v[200:203]\n"
"v_accvgpr_read_b32 v17, a185\n"
"v_mfma_f32_16x16x32_bf16 v[200:203], a[52:55], a[100:103], v[200:203]\n"
"v_accvgpr_read_b32 v18, a186\n"
"v_mfma_f32_16x16x32_bf16 v[228:231], a[60:63], a[100:103], v[228:231]\n"
"ds_read_b128 a[100:103], v2 offset:17408\n"
"v_mfma_f32_16x16x32_bf16 v[228:231], a[56:59], a[96:99], v[228:231]\n"
"ds_read_b128 a[96:99], v2 offset:16384\n"
"v_accvgpr_read_b32 v19, a187\n"
"v_mfma_f32_16x16x32_bf16 v[232:235], a[56:59], a[104:107], v[232:235]\n"
"v_lshlrev_b32 v12, 16, v16\n"
"v_and_b32 v13, s63, v16\n"
"v_mfma_f32_16x16x32_bf16 v[232:235], a[60:63], a[108:111], v[232:235]\n"
"v_lshlrev_b32 v14, 16, v17\n"
"v_and_b32 v15, s63, v17\n"
"v_mfma_f32_16x16x32_bf16 v[204:207], a[52:55], a[108:111], v[204:207]\n"
"ds_read_b128 a[108:111], v3 offset:17408\n"
"v_accvgpr_read_b32 v10, a0\n"
"v_mfma_f32_16x16x32_bf16 v[204:207], a[48:51], a[104:107], v[204:207]\n"
"ds_read_b128 a[104:107], v3 offset:16384\n"
"v_accvgpr_read_b32 v11, a1\n"
"v_mfma_f32_16x16x32_bf16 v[208:211], a[48:51], a[112:115], v[208:211]\n"
"v_mul_f32 v12, v12, v10\n"
"v_mul_f32 v13, v13, v11\n"
"v_mfma_f32_16x16x32_bf16 v[208:211], a[52:55], a[116:119], v[208:211]\n"
"v_accvgpr_read_b32 v10, a2\n"
"v_accvgpr_read_b32 v11, a3\n"
"v_mfma_f32_16x16x32_bf16 v[236:239], a[60:63], a[116:119], v[236:239]\n"
"ds_read_b128 a[116:119], v4 offset:17408\n"
"v_mul_f32 v14, v14, v10\n"
"v_mfma_f32_16x16x32_bf16 v[236:239], a[56:59], a[112:115], v[236:239]\n"
"ds_read_b128 a[112:115], v4 offset:16384\n"
"v_mul_f32 v15, v15, v11\n"
"v_mfma_f32_16x16x32_bf16 v[240:243], a[56:59], a[120:123], v[240:243]\n"
"v_cvt_pk_bf16_f32 v24, v12, v13\n"
"v_cvt_pk_bf16_f32 v25, v14, v15\n"
"v_mfma_f32_16x16x32_bf16 v[240:243], a[60:63], a[124:127], v[240:243]\n"
"ds_write_b64 v7, v[24:25] offset:38912\n"
"v_mfma_f32_16x16x32_bf16 v[212:215], a[52:55], a[124:127], v[212:215]\n"
"ds_read_b128 a[124:127], v2 offset:19456\n"
"v_lshlrev_b32 v12, 16, v18\n"
"v_mfma_f32_16x16x32_bf16 v[212:215], a[48:51], a[120:123], v[212:215]\n"
"ds_read_b128 a[120:123], v2 offset:18432\n"
"v_and_b32 v13, s63, v18\n"
"v_mfma_f32_16x16x32_bf16 v[216:219], a[48:51], a[128:131], v[216:219]\n"
"v_lshlrev_b32 v14, 16, v19\n"
"v_and_b32 v15, s63, v19\n"
"v_mfma_f32_16x16x32_bf16 v[216:219], a[52:55], a[132:135], v[216:219]\n"
"v_accvgpr_read_b32 v10, a4\n"
"v_accvgpr_read_b32 v11, a5\n"
"v_mfma_f32_16x16x32_bf16 v[244:247], a[60:63], a[132:135], v[244:247]\n"
"ds_read_b128 a[132:135], v3 offset:19456\n"
"v_mul_f32 v12, v12, v10\n"
"v_mfma_f32_16x16x32_bf16 v[244:247], a[56:59], a[128:131], v[244:247]\n"
"ds_read_b128 a[128:131], v3 offset:18432\n"
"v_mul_f32 v13, v13, v11\n"
"v_mfma_f32_16x16x32_bf16 v[248:251], a[56:59], a[136:139], v[248:251]\n"
"v_accvgpr_read_b32 v10, a6\n"
"v_accvgpr_read_b32 v11, a7\n"
"v_mfma_f32_16x16x32_bf16 v[248:251], a[60:63], a[140:143], v[248:251]\n"
"v_mul_f32 v14, v14, v10\n"
"v_mul_f32 v15, v15, v11\n"
"v_mfma_f32_16x16x32_bf16 v[220:223], a[52:55], a[140:143], v[220:223]\n"
"ds_read_b128 a[140:143], v4 offset:19456\n"
"v_cvt_pk_bf16_f32 v26, v12, v13\n"
"v_mfma_f32_16x16x32_bf16 v[220:223], a[48:51], a[136:139], v[220:223]\n"
"ds_read_b128 a[136:139], v4 offset:18432\n"
"v_cvt_pk_bf16_f32 v27, v14, v15\n"
"v_mfma_f32_16x16x32_bf16 v[224:227], a[48:51], a[144:147], v[224:227]\n"
"ds_write_b64 v7, v[26:27] offset:38920\n"
"v_mfma_f32_16x16x32_bf16 v[224:227], a[52:55], a[148:151], v[224:227]\n"
"v_accvgpr_read_b32 v16, a188\n"
"v_mfma_f32_16x16x32_bf16 v[252:255], a[60:63], a[148:151], v[252:255]\n"
"s_waitcnt lgkmcnt(15)\n"
"buffer_store_dwordx4 v[56:59], v6, s[52:55], s92 offen offset:0 nt\n"
"ds_read_b128 a[148:151], v2 offset:21504\n"
"v_accvgpr_read_b32 v17, a189\n"
"v_accvgpr_read_b32 v18, a190\n"
"v_mfma_f32_16x16x32_bf16 v[252:255], a[56:59], a[144:147], v[252:255]\n"
"ds_read_b128 a[144:147], v2 offset:20480\n"
"v_accvgpr_read_b32 v19, a191\n"
"v_mfma_f32_16x16x32_bf16 a[28:31], a[56:59], a[152:155], a[28:31]\n"
"v_lshlrev_b32 v12, 16, v16\n"
"v_and_b32 v13, s63, v16\n"
"v_mfma_f32_16x16x32_bf16 a[28:31], a[60:63], a[156:159], a[28:31]\n"
"v_lshlrev_b32 v14, 16, v17\n"
"v_and_b32 v15, s63, v17\n"
"v_mfma_f32_16x16x32_bf16 a[24:27], a[52:55], a[156:159], a[24:27]\n"
"ds_read_b128 a[156:159], v3 offset:21504\n"
"v_accvgpr_read_b32 v10, a8\n"
"v_mfma_f32_16x16x32_bf16 a[24:27], a[48:51], a[152:155], a[24:27]\n"
"s_waitcnt lgkmcnt(15)\n"
"ds_read_b128 a[152:155], v3 offset:20480\n"
"v_accvgpr_read_b32 v11, a9\n"
"v_mfma_f32_16x16x32_bf16 v[144:147], v[88:91], a[96:99], v[144:147]\n"
"v_mul_f32 v12, v12, v10\n"
"v_mul_f32 v13, v13, v11\n"
"v_mfma_f32_16x16x32_bf16 v[144:147], v[116:119], a[100:103], v[144:147]\n"
"v_accvgpr_read_b32 v10, a10\n"
"v_accvgpr_read_b32 v11, a11\n"
"v_mfma_f32_16x16x32_bf16 v[172:175], v[120:123], a[100:103], v[172:175]\n"
"v_mul_f32 v14, v14, v10\n"
"v_mul_f32 v15, v15, v11\n"
"v_mfma_f32_16x16x32_bf16 v[172:175], v[92:95], a[96:99], v[172:175]\n"
"v_cvt_pk_bf16_f32 v28, v12, v13\n"
"v_cvt_pk_bf16_f32 v29, v14, v15\n"
"v_mfma_f32_16x16x32_bf16 v[200:203], v[32:35], a[96:99], v[200:203]\n"
"buffer_store_dwordx4 v[84:87], v6, s[52:55], s93 offen offset:1024 nt\n"
"ds_write_b64 v8, v[28:29] offset:38912\n"
"v_mfma_f32_16x16x32_bf16 v[200:203], v[60:63], a[100:103], v[200:203]\n"
"v_lshlrev_b32 v12, 16, v18\n"
"v_mfma_f32_16x16x32_bf16 v[228:231], v[124:127], a[100:103], v[228:231]\n"
"ds_read_b128 a[100:103], v4 offset:21504\n"
"v_mfma_f32_16x16x32_bf16 v[228:231], v[96:99], a[96:99], v[228:231]\n"
"s_waitcnt vmcnt(3) lgkmcnt(15)\n"
"ds_read_b128 a[96:99], v4 offset:20480\n"
"v_mfma_f32_16x16x32_bf16 v[228:231], v[108:111], a[104:107], v[228:231]\n"
"v_and_b32 v13, s63, v18\n"
"v_mfma_f32_16x16x32_bf16 v[228:231], v[136:139], a[108:111], v[228:231]\n"
"v_lshlrev_b32 v14, 16, v19\n"
"v_mfma_f32_16x16x32_bf16 v[200:203], v[132:135], a[108:111], v[200:203]\n"
"v_and_b32 v15, s63, v19\n"
"v_mfma_f32_16x16x32_bf16 v[200:203], v[104:107], a[104:107], v[200:203]\n"
"v_accvgpr_read_b32 v10, a12\n"
"v_mfma_f32_16x16x32_bf16 v[172:175], v[36:39], a[104:107], v[172:175]\n"
"v_accvgpr_read_b32 v11, a13\n"
"v_mfma_f32_16x16x32_bf16 v[172:175], v[64:67], a[108:111], v[172:175]\n"
"v_mul_f32 v12, v12, v10\n"
"v_mfma_f32_16x16x32_bf16 v[144:147], v[128:131], a[108:111], v[144:147]\n"
"ds_read_b128 a[108:111], v2 offset:23552\n"
"v_mfma_f32_16x16x32_bf16 v[144:147], v[100:103], a[104:107], v[144:147]\n"
"s_waitcnt lgkmcnt(15)\n"
"ds_read_b128 a[104:107], v2 offset:22528\n"
"v_mfma_f32_16x16x32_bf16 v[144:147], a[64:67], a[112:115], v[144:147]\n"
"v_mul_f32 v13, v13, v11\n"
"v_mfma_f32_16x16x32_bf16 v[144:147], a[68:71], a[116:119], v[144:147]\n"
"v_accvgpr_read_b32 v10, a14\n"
"v_accvgpr_read_b32 v11, a15\n"
"v_mfma_f32_16x16x32_bf16 v[172:175], a[76:79], a[116:119], v[172:175]\n"
"v_mul_f32 v14, v14, v10\n"
"v_mul_f32 v15, v15, v11\n"
"v_mfma_f32_16x16x32_bf16 v[172:175], a[72:75], a[112:115], v[172:175]\n"
"v_cvt_pk_bf16_f32 v30, v12, v13\n"
"v_cvt_pk_bf16_f32 v31, v14, v15\n"
"v_mfma_f32_16x16x32_bf16 v[200:203], a[80:83], a[112:115], v[200:203]\n"
"ds_write_b64 v8, v[30:31] offset:38920\n"
"v_mfma_f32_16x16x32_bf16 v[200:203], a[84:87], a[116:119], v[200:203]\n"
"ds_read_b128 v[112:115], v2 offset:38912\n"
"v_mfma_f32_16x16x32_bf16 v[228:231], a[92:95], a[116:119], v[228:231]\n"
"ds_read_b128 a[116:119], v3 offset:23552\n"
"v_mfma_f32_16x16x32_bf16 v[228:231], a[88:91], a[112:115], v[228:231]\n"
"s_waitcnt lgkmcnt(15)\n"
"ds_read_b128 a[112:115], v3 offset:22528\n"
"v_mfma_f32_16x16x32_bf16 v[232:235], v[96:99], a[120:123], v[232:235]\n"
"ds_read_b128 v[140:143], v2 offset:39936\n"
"v_mfma_f32_16x16x32_bf16 v[232:235], v[124:127], a[124:127], v[232:235]\n"
"v_accvgpr_read_b32 v16, a192\n"
"v_accvgpr_read_b32 v17, a193\n"
"v_mfma_f32_16x16x32_bf16 v[204:207], v[60:63], a[124:127], v[204:207]\n"
"v_accvgpr_read_b32 v18, a194\n"
"v_accvgpr_read_b32 v19, a195\n"
"v_mfma_f32_16x16x32_bf16 v[204:207], v[32:35], a[120:123], v[204:207]\n"
"v_lshlrev_b32 v12, 16, v16\n"
"v_and_b32 v13, s63, v16\n"
"v_mfma_f32_16x16x32_bf16 v[176:179], v[92:95], a[120:123], v[176:179]\n"
"v_lshlrev_b32 v14, 16, v17\n"
"v_and_b32 v15, s63, v17\n"
"v_mfma_f32_16x16x32_bf16 v[176:179], v[120:123], a[124:127], v[176:179]\n"
"v_mul_f32 v12, v12, v144\n"
"v_mul_f32 v13, v13, v145\n"
"v_mfma_f32_16x16x32_bf16 v[148:151], v[116:119], a[124:127], v[148:151]\n"
"ds_read_b128 a[124:127], v4 offset:23552\n"
"v_mul_f32 v14, v14, v146\n"
"v_mfma_f32_16x16x32_bf16 v[148:151], v[88:91], a[120:123], v[148:151]\n"
"ds_read_b128 a[120:123], v4 offset:22528\n"
"v_mul_f32 v15, v15, v147\n"
"v_mfma_f32_16x16x32_bf16 v[148:151], v[100:103], a[128:131], v[148:151]\n"
"v_cvt_pk_bf16_f32 v24, v12, v13\n"
"v_cvt_pk_bf16_f32 v25, v14, v15\n"
"v_mfma_f32_16x16x32_bf16 v[148:151], v[128:131], a[132:135], v[148:151]\n"
"ds_write_b64 v7, v[24:25] offset:32768\n"
"v_mfma_f32_16x16x32_bf16 v[176:179], v[64:67], a[132:135], v[176:179]\n"
"v_lshlrev_b32 v12, 16, v18\n"
"v_and_b32 v13, s63, v18\n"
"v_mfma_f32_16x16x32_bf16 v[176:179], v[36:39], a[128:131], v[176:179]\n"
"v_lshlrev_b32 v14, 16, v19\n"
"v_and_b32 v15, s63, v19\n"
"v_mfma_f32_16x16x32_bf16 v[204:207], v[104:107], a[128:131], v[204:207]\n"
"s_waitcnt lgkmcnt(6)\n"
"buffer_store_dwordx4 v[112:115], v6, s[52:55], s94 offen offset:2048 nt\n"
"v_mul_f32 v12, v12, v172\n"
"v_mul_f32 v13, v13, v173\n"
"v_mul_f32 v14, v14, v174\n"
"v_mfma_f32_16x16x32_bf16 v[204:207], v[132:135], a[132:135], v[204:207]\n"
"v_mul_f32 v15, v15, v175\n"
"v_mfma_f32_16x16x32_bf16 v[232:235], v[136:139], a[132:135], v[232:235]\n"
"ds_read_b128 a[132:135], v2 offset:25600\n"
"v_cvt_pk_bf16_f32 v26, v12, v13\n"
"v_mfma_f32_16x16x32_bf16 v[232:235], v[108:111], a[128:131], v[232:235]\n"
"ds_read_b128 a[128:131], v2 offset:24576\n"
"v_cvt_pk_bf16_f32 v27, v14, v15\n"
"v_mfma_f32_16x16x32_bf16 v[232:235], a[88:91], a[136:139], v[232:235]\n"
"ds_write_b64 v7, v[26:27] offset:32776\n"
"v_mfma_f32_16x16x32_bf16 v[232:235], a[92:95], a[140:143], v[232:235]\n"
"v_accvgpr_read_b32 v16, a196\n"
"v_accvgpr_read_b32 v17, a197\n"
"v_mfma_f32_16x16x32_bf16 v[204:207], a[84:87], a[140:143], v[204:207]\n"
"v_accvgpr_read_b32 v18, a198\n"
"v_accvgpr_read_b32 v19, a199\n"
"v_mfma_f32_16x16x32_bf16 v[204:207], a[80:83], a[136:139], v[204:207]\n"
"v_lshlrev_b32 v12, 16, v16\n"
"v_and_b32 v13, s63, v16\n"
"v_mfma_f32_16x16x32_bf16 v[176:179], a[72:75], a[136:139], v[176:179]\n"
"v_lshlrev_b32 v14, 16, v17\n"
"v_and_b32 v15, s63, v17\n"
"v_mfma_f32_16x16x32_bf16 v[176:179], a[76:79], a[140:143], v[176:179]\n"
"v_mul_f32 v12, v12, v200\n"
"v_mul_f32 v13, v13, v201\n"
"v_mfma_f32_16x16x32_bf16 v[148:151], a[68:71], a[140:143], v[148:151]\n"
"s_waitcnt lgkmcnt(6)\n"
"buffer_store_dwordx4 v[140:143], v6, s[52:55], s95 offen offset:3072 nt\n"
"ds_read_b128 a[140:143], v3 offset:25600\n"
"v_mul_f32 v14, v14, v202\n"
"v_mul_f32 v15, v15, v203\n"
"v_mfma_f32_16x16x32_bf16 v[148:151], a[64:67], a[136:139], v[148:151]\n"
"ds_read_b128 a[136:139], v3 offset:24576\n"
"v_cvt_pk_bf16_f32 v28, v12, v13\n"
"v_mfma_f32_16x16x32_bf16 v[152:155], v[88:91], a[144:147], v[152:155]\n"
"v_cvt_pk_bf16_f32 v29, v14, v15\n"
"v_mfma_f32_16x16x32_bf16 v[152:155], v[116:119], a[148:151], v[152:155]\n"
"ds_write_b64 v8, v[28:29] offset:32768\n"
"v_mfma_f32_16x16x32_bf16 v[180:183], v[120:123], a[148:151], v[180:183]\n"
"v_lshlrev_b32 v12, 16, v18\n"
"v_and_b32 v13, s63, v18\n"
"v_mfma_f32_16x16x32_bf16 v[180:183], v[92:95], a[144:147], v[180:183]\n"
"v_lshlrev_b32 v14, 16, v19\n"
"v_and_b32 v15, s63, v19\n"
"v_mfma_f32_16x16x32_bf16 v[208:211], v[32:35], a[144:147], v[208:211]\n"
"v_mul_f32 v12, v12, v228\n"
"v_mul_f32 v13, v13, v229\n"
"v_mfma_f32_16x16x32_bf16 v[208:211], v[60:63], a[148:151], v[208:211]\n"
"v_mul_f32 v14, v14, v230\n"
"v_mul_f32 v15, v15, v231\n"
"v_mfma_f32_16x16x32_bf16 v[236:239], v[124:127], a[148:151], v[236:239]\n"
"ds_read_b128 a[148:151], v4 offset:25600\n"
"v_cvt_pk_bf16_f32 v30, v12, v13\n"
"v_mfma_f32_16x16x32_bf16 v[236:239], v[96:99], a[144:147], v[236:239]\n"
"ds_read_b128 a[144:147], v4 offset:24576\n"
"v_cvt_pk_bf16_f32 v31, v14, v15\n"
"v_mfma_f32_16x16x32_bf16 v[236:239], v[108:111], a[152:155], v[236:239]\n"
"ds_write_b64 v8, v[30:31] offset:32776\n"
"v_mfma_f32_16x16x32_bf16 v[236:239], v[136:139], a[156:159], v[236:239]\n"
"ds_read_b128 v[144:147], v2 offset:32768\n"
"v_mfma_f32_16x16x32_bf16 v[208:211], v[132:135], a[156:159], v[208:211]\n"
"ds_read_b128 v[172:175], v2 offset:33792\n"
"v_accvgpr_read_b32 v16, a200\n"
"v_accvgpr_read_b32 v17, a201\n"
"v_mfma_f32_16x16x32_bf16 v[208:211], v[104:107], a[152:155], v[208:211]\n"
"v_accvgpr_read_b32 v18, a202\n"
"v_accvgpr_read_b32 v19, a203\n"
"v_mfma_f32_16x16x32_bf16 v[180:183], v[36:39], a[152:155], v[180:183]\n"
"v_lshlrev_b32 v12, 16, v16\n"
"v_and_b32 v13, s63, v16\n"
"v_mfma_f32_16x16x32_bf16 v[180:183], v[64:67], a[156:159], v[180:183]\n"
"v_lshlrev_b32 v14, 16, v17\n"
"v_and_b32 v15, s63, v17\n"
"v_mfma_f32_16x16x32_bf16 v[152:155], v[128:131], a[156:159], v[152:155]\n"
"ds_read_b128 a[156:159], v2 offset:27648\n"
"v_mul_f32 v12, v12, v148\n"
"v_mfma_f32_16x16x32_bf16 v[152:155], v[100:103], a[152:155], v[152:155]\n"
"ds_read_b128 a[152:155], v2 offset:26624\n"
"v_mul_f32 v13, v13, v149\n"
"v_mfma_f32_16x16x32_bf16 v[152:155], a[64:67], a[96:99], v[152:155]\n"
"v_mul_f32 v14, v14, v150\n"
"v_mul_f32 v15, v15, v151\n"
"v_mfma_f32_16x16x32_bf16 v[152:155], a[68:71], a[100:103], v[152:155]\n"
"v_cvt_pk_bf16_f32 v24, v12, v13\n"
"v_cvt_pk_bf16_f32 v25, v14, v15\n"
"v_mfma_f32_16x16x32_bf16 v[180:183], a[76:79], a[100:103], v[180:183]\n"
"s_waitcnt lgkmcnt(3)\n"
"buffer_store_dwordx4 v[144:147], v6, s[52:55], s80 offen offset:128 nt\n"
"ds_write_b64 v7, v[24:25] offset:34816\n"
"v_mfma_f32_16x16x32_bf16 v[180:183], a[72:75], a[96:99], v[180:183]\n"
"v_lshlrev_b32 v12, 16, v18\n"
"v_and_b32 v13, s63, v18\n"
"v_mfma_f32_16x16x32_bf16 v[208:211], a[80:83], a[96:99], v[208:211]\n"
"v_lshlrev_b32 v14, 16, v19\n"
"v_and_b32 v15, s63, v19\n"
"v_mfma_f32_16x16x32_bf16 v[208:211], a[84:87], a[100:103], v[208:211]\n"
"v_mul_f32 v12, v12, v176\n"
"v_mul_f32 v13, v13, v177\n"
"v_mfma_f32_16x16x32_bf16 v[236:239], a[92:95], a[100:103], v[236:239]\n"
"ds_read_b128 a[100:103], v3 offset:27648\n"
"v_mul_f32 v14, v14, v178\n"
"v_mfma_f32_16x16x32_bf16 v[236:239], a[88:91], a[96:99], v[236:239]\n"
"ds_read_b128 a[96:99], v3 offset:26624\n"
"v_mul_f32 v15, v15, v179\n"
"v_mfma_f32_16x16x32_bf16 v[240:243], v[96:99], a[104:107], v[240:243]\n"
"v_cvt_pk_bf16_f32 v26, v12, v13\n"
"v_cvt_pk_bf16_f32 v27, v14, v15\n"
"v_mfma_f32_16x16x32_bf16 v[240:243], v[124:127], a[108:111], v[240:243]\n"
"ds_write_b64 v7, v[26:27] offset:34824\n"
"v_mfma_f32_16x16x32_bf16 v[212:215], v[60:63], a[108:111], v[212:215]\n"
"v_accvgpr_read_b32 v16, a204\n"
"v_accvgpr_read_b32 v17, a205\n"
"v_mfma_f32_16x16x32_bf16 v[212:215], v[32:35], a[104:107], v[212:215]\n"
"v_accvgpr_read_b32 v18, a206\n"
"v_accvgpr_read_b32 v19, a207\n"
"v_mfma_f32_16x16x32_bf16 v[184:187], v[92:95], a[104:107], v[184:187]\n"
"s_waitcnt lgkmcnt(6)\n"
"buffer_store_dwordx4 v[172:175], v6, s[52:55], s81 offen offset:1152 nt\n"
"v_lshlrev_b32 v12, 16, v16\n"
"v_and_b32 v13, s63, v16\n"
"v_lshlrev_b32 v14, 16, v17\n"
"v_and_b32 v15, s63, v17\n"
"v_mfma_f32_16x16x32_bf16 v[184:187], v[120:123], a[108:111], v[184:187]\n"
"v_mul_f32 v12, v12, v204\n"
"v_mul_f32 v13, v13, v205\n"
"v_mfma_f32_16x16x32_bf16 v[156:159], v[116:119], a[108:111], v[156:159]\n"
"ds_read_b128 a[108:111], v4 offset:27648\n"
"v_mul_f32 v14, v14, v206\n"
"v_mfma_f32_16x16x32_bf16 v[156:159], v[88:91], a[104:107], v[156:159]\n"
"ds_read_b128 a[104:107], v4 offset:26624\n"
"v_mul_f32 v15, v15, v207\n"
"v_mfma_f32_16x16x32_bf16 v[156:159], v[100:103], a[112:115], v[156:159]\n"
"v_cvt_pk_bf16_f32 v28, v12, v13\n"
"v_cvt_pk_bf16_f32 v29, v14, v15\n"
"v_mfma_f32_16x16x32_bf16 v[156:159], v[128:131], a[116:119], v[156:159]\n"
"ds_write_b64 v8, v[28:29] offset:34816\n"
"v_mfma_f32_16x16x32_bf16 v[184:187], v[64:67], a[116:119], v[184:187]\n"
"v_lshlrev_b32 v12, 16, v18\n"
"v_and_b32 v13, s63, v18\n"
"v_lshlrev_b32 v14, 16, v19\n"
"v_mfma_f32_16x16x32_bf16 v[184:187], v[36:39], a[112:115], v[184:187]\n"
"v_and_b32 v15, s63, v19\n"
"v_mul_f32 v12, v12, v232\n"
"v_mfma_f32_16x16x32_bf16 v[212:215], v[104:107], a[112:115], v[212:215]\n"
"v_mul_f32 v13, v13, v233\n"
"v_mul_f32 v14, v14, v234\n"
"v_mfma_f32_16x16x32_bf16 v[212:215], v[132:135], a[116:119], v[212:215]\n"
"v_mul_f32 v15, v15, v235\n"
"v_cvt_pk_bf16_f32 v30, v12, v13\n"
"v_mfma_f32_16x16x32_bf16 v[240:243], v[136:139], a[116:119], v[240:243]\n"
"ds_read_b128 a[116:119], v2 offset:29696\n"
"v_cvt_pk_bf16_f32 v31, v14, v15\n"
"v_mfma_f32_16x16x32_bf16 v[240:243], v[108:111], a[112:115], v[240:243]\n"
"ds_read_b128 a[112:115], v2 offset:28672\n"
"ds_write_b64 v8, v[30:31] offset:34824\n"
"v_mfma_f32_16x16x32_bf16 v[240:243], a[88:91], a[120:123], v[240:243]\n"
"ds_read_b128 v[148:151], v2 offset:34816\n"
"v_mfma_f32_16x16x32_bf16 v[240:243], a[92:95], a[124:127], v[240:243]\n"
"ds_read_b128 v[176:179], v2 offset:35840\n"
"v_accvgpr_read_b32 v16, a208\n"
"v_accvgpr_read_b32 v17, a209\n"
"v_mfma_f32_16x16x32_bf16 v[212:215], a[84:87], a[124:127], v[212:215]\n"
"v_accvgpr_read_b32 v18, a210\n"
"v_accvgpr_read_b32 v19, a211\n"
"v_mfma_f32_16x16x32_bf16 v[212:215], a[80:83], a[120:123], v[212:215]\n"
"v_lshlrev_b32 v12, 16, v16\n"
"v_and_b32 v13, s63, v16\n"
"v_mfma_f32_16x16x32_bf16 v[184:187], a[72:75], a[120:123], v[184:187]\n"
"v_lshlrev_b32 v14, 16, v17\n"
"v_and_b32 v15, s63, v17\n"
"v_mfma_f32_16x16x32_bf16 v[184:187], a[76:79], a[124:127], v[184:187]\n"
"v_mul_f32 v12, v12, v152\n"
"v_mul_f32 v13, v13, v153\n"
"v_mfma_f32_16x16x32_bf16 v[156:159], a[68:71], a[124:127], v[156:159]\n"
"ds_read_b128 a[124:127], v3 offset:29696\n"
"v_mul_f32 v14, v14, v154\n"
"v_mfma_f32_16x16x32_bf16 v[156:159], a[64:67], a[120:123], v[156:159]\n"
"ds_read_b128 a[120:123], v3 offset:28672\n"
"v_mul_f32 v15, v15, v155\n"
"v_mfma_f32_16x16x32_bf16 v[160:163], v[88:91], a[128:131], v[160:163]\n"
"v_cvt_pk_bf16_f32 v24, v12, v13\n"
"v_cvt_pk_bf16_f32 v25, v14, v15\n"
"v_mfma_f32_16x16x32_bf16 v[160:163], v[116:119], a[132:135], v[160:163]\n"
"s_waitcnt lgkmcnt(3)\n"
"buffer_store_dwordx4 v[148:151], v6, s[52:55], s82 offen offset:2176 nt\n"
"ds_write_b64 v7, v[24:25] offset:36864\n"
"v_mfma_f32_16x16x32_bf16 v[188:191], v[120:123], a[132:135], v[188:191]\n"
"v_lshlrev_b32 v12, 16, v18\n"
"v_and_b32 v13, s63, v18\n"
"v_mfma_f32_16x16x32_bf16 v[188:191], v[92:95], a[128:131], v[188:191]\n"
"v_lshlrev_b32 v14, 16, v19\n"
"v_and_b32 v15, s63, v19\n"
"v_mfma_f32_16x16x32_bf16 v[216:219], v[32:35], a[128:131], v[216:219]\n"
"v_mul_f32 v12, v12, v180\n"
"v_mul_f32 v13, v13, v181\n"
"v_mfma_f32_16x16x32_bf16 v[216:219], v[60:63], a[132:135], v[216:219]\n"
"v_mul_f32 v14, v14, v182\n"
"v_mul_f32 v15, v15, v183\n"
"v_mfma_f32_16x16x32_bf16 v[244:247], v[124:127], a[132:135], v[244:247]\n"
"ds_read_b128 a[132:135], v4 offset:29696\n"
"v_cvt_pk_bf16_f32 v26, v12, v13\n"
"v_mfma_f32_16x16x32_bf16 v[244:247], v[96:99], a[128:131], v[244:247]\n"
"ds_read_b128 a[128:131], v4 offset:28672\n"
"v_cvt_pk_bf16_f32 v27, v14, v15\n"
"v_mfma_f32_16x16x32_bf16 v[244:247], v[108:111], a[136:139], v[244:247]\n"
"ds_write_b64 v7, v[26:27] offset:36872\n"
"v_mfma_f32_16x16x32_bf16 v[244:247], v[136:139], a[140:143], v[244:247]\n"
"v_accvgpr_read_b32 v16, a212\n"
"v_accvgpr_read_b32 v17, a213\n"
"v_mfma_f32_16x16x32_bf16 v[216:219], v[132:135], a[140:143], v[216:219]\n"
"v_accvgpr_read_b32 v18, a214\n"
"v_accvgpr_read_b32 v19, a215\n"
"v_mfma_f32_16x16x32_bf16 v[216:219], v[104:107], a[136:139], v[216:219]\n"
"s_waitcnt lgkmcnt(6)\n"
"buffer_store_dwordx4 v[176:179], v6, s[52:55], s83 offen offset:3200 nt\n"
"v_lshlrev_b32 v12, 16, v16\n"
"v_and_b32 v13, s63, v16\n"
"v_lshlrev_b32 v14, 16, v17\n"
"v_and_b32 v15, s63, v17\n"
"v_mfma_f32_16x16x32_bf16 v[188:191], v[36:39], a[136:139], v[188:191]\n"
"v_mul_f32 v12, v12, v208\n"
"v_mul_f32 v13, v13, v209\n"
"v_mfma_f32_16x16x32_bf16 v[188:191], v[64:67], a[140:143], v[188:191]\n"
"v_mul_f32 v14, v14, v210\n"
"v_mul_f32 v15, v15, v211\n"
"v_mfma_f32_16x16x32_bf16 v[160:163], v[128:131], a[140:143], v[160:163]\n"
"ds_read_b128 a[140:143], v2 offset:31744\n"
"v_cvt_pk_bf16_f32 v28, v12, v13\n"
"v_mfma_f32_16x16x32_bf16 v[160:163], v[100:103], a[136:139], v[160:163]\n"
"ds_read_b128 a[136:139], v2 offset:30720\n"
"v_cvt_pk_bf16_f32 v29, v14, v15\n"
"v_mfma_f32_16x16x32_bf16 v[160:163], a[64:67], a[144:147], v[160:163]\n"
"ds_write_b64 v8, v[28:29] offset:36864\n"
"v_mfma_f32_16x16x32_bf16 v[160:163], a[68:71], a[148:151], v[160:163]\n"
"v_lshlrev_b32 v12, 16, v18\n"
"v_and_b32 v13, s63, v18\n"
"v_lshlrev_b32 v14, 16, v19\n"
"v_mfma_f32_16x16x32_bf16 v[188:191], a[76:79], a[148:151], v[188:191]\n"
"v_and_b32 v15, s63, v19\n"
"v_mul_f32 v12, v12, v236\n"
"v_mul_f32 v13, v13, v237\n"
"v_mfma_f32_16x16x32_bf16 v[188:191], a[72:75], a[144:147], v[188:191]\n"
"v_mul_f32 v14, v14, v238\n"
"v_mul_f32 v15, v15, v239\n"
"v_mfma_f32_16x16x32_bf16 v[216:219], a[80:83], a[144:147], v[216:219]\n"
"v_cvt_pk_bf16_f32 v30, v12, v13\n"
"v_cvt_pk_bf16_f32 v31, v14, v15\n"
"v_mfma_f32_16x16x32_bf16 v[216:219], a[84:87], a[148:151], v[216:219]\n"
"ds_write_b64 v8, v[30:31] offset:36872\n"
"v_mfma_f32_16x16x32_bf16 v[244:247], a[92:95], a[148:151], v[244:247]\n"
"ds_read_b128 a[148:151], v3 offset:31744\n"
"ds_read_b128 v[152:155], v2 offset:36864\n"
"v_mfma_f32_16x16x32_bf16 v[244:247], a[88:91], a[144:147], v[244:247]\n"
"ds_read_b128 a[144:147], v3 offset:30720\n"
"v_mfma_f32_16x16x32_bf16 v[248:251], v[96:99], a[152:155], v[248:251]\n"
"ds_read_b128 v[180:183], v2 offset:37888\n"
"v_accvgpr_read_b32 v16, a216\n"
"v_mfma_f32_16x16x32_bf16 v[248:251], v[124:127], a[156:159], v[248:251]\n"
"v_accvgpr_read_b32 v17, a217\n"
"v_accvgpr_read_b32 v18, a218\n"
"v_mfma_f32_16x16x32_bf16 v[220:223], v[60:63], a[156:159], v[220:223]\n"
"v_accvgpr_read_b32 v19, a219\n"
"v_lshlrev_b32 v12, 16, v16\n"
"v_mfma_f32_16x16x32_bf16 v[220:223], v[32:35], a[152:155], v[220:223]\n"
"v_and_b32 v13, s63, v16\n"
"v_lshlrev_b32 v14, 16, v17\n"
"v_mfma_f32_16x16x32_bf16 v[192:195], v[92:95], a[152:155], v[192:195]\n"
"v_and_b32 v15, s63, v17\n"
"v_mul_f32 v12, v12, v156\n"
"v_mfma_f32_16x16x32_bf16 v[192:195], v[120:123], a[156:159], v[192:195]\n"
"v_mul_f32 v13, v13, v157\n"
"v_mul_f32 v14, v14, v158\n"
"v_mfma_f32_16x16x32_bf16 v[164:167], v[116:119], a[156:159], v[164:167]\n"
"ds_read_b128 a[156:159], v4 offset:31744\n"
"v_mul_f32 v15, v15, v159\n"
"v_mfma_f32_16x16x32_bf16 v[164:167], v[88:91], a[152:155], v[164:167]\n"
"s_waitcnt lgkmcnt(3)\n"
"buffer_store_dwordx4 v[152:155], v6, s[52:55], s84 offen offset:128 nt\n"
"ds_read_b128 a[152:155], v4 offset:30720\n"
"v_cvt_pk_bf16_f32 v24, v12, v13\n"
"v_cvt_pk_bf16_f32 v25, v14, v15\n"
"v_mfma_f32_16x16x32_bf16 v[164:167], v[100:103], a[96:99], v[164:167]\n"
"ds_write_b64 v7, v[24:25] offset:38912\n"
"v_mfma_f32_16x16x32_bf16 v[164:167], v[128:131], a[100:103], v[164:167]\n"
"v_lshlrev_b32 v12, 16, v18\n"
"v_and_b32 v13, s63, v18\n"
"v_mfma_f32_16x16x32_bf16 v[192:195], v[64:67], a[100:103], v[192:195]\n"
"v_lshlrev_b32 v14, 16, v19\n"
"v_and_b32 v15, s63, v19\n"
"v_mfma_f32_16x16x32_bf16 v[192:195], v[36:39], a[96:99], v[192:195]\n"
"v_mul_f32 v12, v12, v184\n"
"v_mul_f32 v13, v13, v185\n"
"v_mfma_f32_16x16x32_bf16 v[220:223], v[104:107], a[96:99], v[220:223]\n"
"v_mul_f32 v14, v14, v186\n"
"v_mul_f32 v15, v15, v187\n"
"v_mfma_f32_16x16x32_bf16 v[220:223], v[132:135], a[100:103], v[220:223]\n"
"v_cvt_pk_bf16_f32 v26, v12, v13\n"
"v_cvt_pk_bf16_f32 v27, v14, v15\n"
"v_mfma_f32_16x16x32_bf16 v[248:251], v[136:139], a[100:103], v[248:251]\n"
"ds_write_b64 v7, v[26:27] offset:38920\n"
"v_mfma_f32_16x16x32_bf16 v[248:251], v[108:111], a[96:99], v[248:251]\n"
"v_accvgpr_read_b32 v16, a220\n"
"v_accvgpr_read_b32 v17, a221\n"
"v_mfma_f32_16x16x32_bf16 v[248:251], a[88:91], a[104:107], v[248:251]\n"
"v_accvgpr_read_b32 v18, a222\n"
"v_accvgpr_read_b32 v19, a223\n"
"v_mfma_f32_16x16x32_bf16 v[248:251], a[92:95], a[108:111], v[248:251]\n"
"L_st11485:\n"
"s_cbranch_execz L_retC\n"
"s_waitcnt lgkmcnt(4)\n"
"buffer_store_dwordx4 v[180:183], v6, s[52:55], s85 offen offset:1152 nt\n"
"v_lshlrev_b32 v12, 16, v16\n"
"v_and_b32 v13, s63, v16\n"
"v_lshlrev_b32 v14, 16, v17\n"
"v_mfma_f32_16x16x32_bf16 v[220:223], a[84:87], a[108:111], v[220:223]\n"
"v_and_b32 v15, s63, v17\n"
"v_mul_f32 v12, v12, v212\n"
"v_mfma_f32_16x16x32_bf16 v[220:223], a[80:83], a[104:107], v[220:223]\n"
"v_mul_f32 v13, v13, v213\n"
"v_mul_f32 v14, v14, v214\n"
"v_mfma_f32_16x16x32_bf16 v[192:195], a[72:75], a[104:107], v[192:195]\n"
"v_mul_f32 v15, v15, v215\n"
"v_cvt_pk_bf16_f32 v28, v12, v13\n"
"v_mfma_f32_16x16x32_bf16 v[192:195], a[76:79], a[108:111], v[192:195]\n"
"v_cvt_pk_bf16_f32 v29, v14, v15\n"
"v_mfma_f32_16x16x32_bf16 v[164:167], a[68:71], a[108:111], v[164:167]\n"
"ds_write_b64 v8, v[28:29] offset:38912\n"
"v_mfma_f32_16x16x32_bf16 v[164:167], a[64:67], a[104:107], v[164:167]\n"
"v_lshlrev_b32 v12, 16, v18\n"
"v_and_b32 v13, s63, v18\n"
"v_mfma_f32_16x16x32_bf16 v[168:171], v[88:91], a[112:115], v[168:171]\n"
"v_lshlrev_b32 v14, 16, v19\n"
"v_and_b32 v15, s63, v19\n"
"v_mfma_f32_16x16x32_bf16 v[168:171], v[116:119], a[116:119], v[168:171]\n"
"v_mul_f32 v12, v12, v240\n"
"v_mul_f32 v13, v13, v241\n"
"v_mfma_f32_16x16x32_bf16 v[196:199], v[120:123], a[116:119], v[196:199]\n"
"v_mul_f32 v14, v14, v242\n"
"v_mul_f32 v15, v15, v243\n"
"v_mfma_f32_16x16x32_bf16 v[196:199], v[92:95], a[112:115], v[196:199]\n"
"v_cvt_pk_bf16_f32 v30, v12, v13\n"
"v_cvt_pk_bf16_f32 v31, v14, v15\n"
"v_mfma_f32_16x16x32_bf16 v[224:227], v[32:35], a[112:115], v[224:227]\n"
"ds_write_b64 v8, v[30:31] offset:38920\n"
"v_mfma_f32_16x16x32_bf16 v[224:227], v[60:63], a[116:119], v[224:227]\n"
"ds_read_b128 v[156:159], v2 offset:38912\n"
"v_mfma_f32_16x16x32_bf16 v[252:255], v[124:127], a[116:119], v[252:255]\n"
"ds_read_b128 v[184:187], v2 offset:39936\n"
"v_accvgpr_read_b32 v16, a224\n"
"v_mfma_f32_16x16x32_bf16 v[252:255], v[96:99], a[112:115], v[252:255]\n"
"v_accvgpr_read_b32 v17, a225\n"
"v_accvgpr_read_b32 v18, a226\n"
"v_mfma_f32_16x16x32_bf16 v[252:255], v[108:111], a[120:123], v[252:255]\n"
"v_accvgpr_read_b32 v19, a227\n"
"v_lshlrev_b32 v12, 16, v16\n"
"v_mfma_f32_16x16x32_bf16 v[252:255], v[136:139], a[124:127], v[252:255]\n"
"v_and_b32 v13, s63, v16\n"
"v_lshlrev_b32 v14, 16, v17\n"
"v_mfma_f32_16x16x32_bf16 v[224:227], v[132:135], a[124:127], v[224:227]\n"
"v_and_b32 v15, s63, v17\n"
"v_mul_f32 v12, v12, v160\n"
"v_mfma_f32_16x16x32_bf16 v[224:227], v[104:107], a[120:123], v[224:227]\n"
"v_mul_f32 v13, v13, v161\n"
"v_mul_f32 v14, v14, v162\n"
"v_mfma_f32_16x16x32_bf16 v[196:199], v[36:39], a[120:123], v[196:199]\n"
"v_mul_f32 v15, v15, v163\n"
"v_cvt_pk_bf16_f32 v24, v12, v13\n"
"v_mfma_f32_16x16x32_bf16 v[196:199], v[64:67], a[124:127], v[196:199]\n"
"v_cvt_pk_bf16_f32 v25, v14, v15\n"
"v_mfma_f32_16x16x32_bf16 v[168:171], v[128:131], a[124:127], v[168:171]\n"
"s_waitcnt lgkmcnt(1)\n"
"buffer_store_dwordx4 v[156:159], v6, s[52:55], s86 offen offset:2176 nt\n"
"ds_write_b64 v7, v[24:25] offset:32768\n"
"v_mfma_f32_16x16x32_bf16 v[168:171], v[100:103], a[120:123], v[168:171]\n"
"v_lshlrev_b32 v12, 16, v18\n"
"v_and_b32 v13, s63, v18\n"
"v_mfma_f32_16x16x32_bf16 v[168:171], a[64:67], a[128:131], v[168:171]\n"
"v_lshlrev_b32 v14, 16, v19\n"
"v_and_b32 v15, s63, v19\n"
"v_mfma_f32_16x16x32_bf16 v[168:171], a[68:71], a[132:135], v[168:171]\n"
"v_mul_f32 v12, v12, v188\n"
"v_mul_f32 v13, v13, v189\n"
"v_mfma_f32_16x16x32_bf16 v[196:199], a[76:79], a[132:135], v[196:199]\n"
"v_mul_f32 v14, v14, v190\n"
"v_mul_f32 v15, v15, v191\n"
"v_mfma_f32_16x16x32_bf16 v[196:199], a[72:75], a[128:131], v[196:199]\n"
"v_cvt_pk_bf16_f32 v26, v12, v13\n"
"v_cvt_pk_bf16_f32 v27, v14, v15\n"
"v_mfma_f32_16x16x32_bf16 v[224:227], a[80:83], a[128:131], v[224:227]\n"
"ds_write_b64 v7, v[26:27] offset:32776\n"
"v_mfma_f32_16x16x32_bf16 v[224:227], a[84:87], a[132:135], v[224:227]\n"
"v_accvgpr_read_b32 v16, a228\n"
"v_accvgpr_read_b32 v17, a229\n"
"v_mfma_f32_16x16x32_bf16 v[252:255], a[92:95], a[132:135], v[252:255]\n"
"v_accvgpr_read_b32 v18, a230\n"
"v_accvgpr_read_b32 v19, a231\n"
"v_mfma_f32_16x16x32_bf16 v[252:255], a[88:91], a[128:131], v[252:255]\n"
"v_lshlrev_b32 v12, 16, v16\n"
"v_and_b32 v13, s63, v16\n"
"v_mfma_f32_16x16x32_bf16 a[28:31], v[96:99], a[136:139], a[28:31]\n"
"s_waitcnt lgkmcnt(2)\n"
"buffer_store_dwordx4 v[184:187], v6, s[52:55], s87 offen offset:3200 nt\n"
"v_lshlrev_b32 v14, 16, v17\n"
"v_and_b32 v15, s63, v17\n"
"v_mul_f32 v12, v12, v216\n"
"v_mul_f32 v13, v13, v217\n"
"v_mfma_f32_16x16x32_bf16 a[28:31], v[124:127], a[140:143], a[28:31]\n"
"v_mul_f32 v14, v14, v218\n"
"v_mul_f32 v15, v15, v219\n"
"v_mfma_f32_16x16x32_bf16 a[24:27], v[60:63], a[140:143], a[24:27]\n"
"v_cvt_pk_bf16_f32 v28, v12, v13\n"
"v_cvt_pk_bf16_f32 v29, v14, v15\n"
"v_mfma_f32_16x16x32_bf16 a[24:27], v[32:35], a[136:139], a[24:27]\n"
"ds_write_b64 v8, v[28:29] offset:32768\n"
"v_mfma_f32_16x16x32_bf16 a[20:23], v[92:95], a[136:139], a[20:23]\n"
"v_lshlrev_b32 v12, 16, v18\n"
"v_and_b32 v13, s63, v18\n"
"v_mfma_f32_16x16x32_bf16 a[20:23], v[120:123], a[140:143], a[20:23]\n"
"v_lshlrev_b32 v14, 16, v19\n"
"v_and_b32 v15, s63, v19\n"
"v_mfma_f32_16x16x32_bf16 a[16:19], v[116:119], a[140:143], a[16:19]\n"
"v_mul_f32 v12, v12, v244\n"
"v_mul_f32 v13, v13, v245\n"
"v_mfma_f32_16x16x32_bf16 a[16:19], v[88:91], a[136:139], a[16:19]\n"
"v_mul_f32 v14, v14, v246\n"
"v_mul_f32 v15, v15, v247\n"
"v_mfma_f32_16x16x32_bf16 a[16:19], v[100:103], a[144:147], a[16:19]\n"
"v_cvt_pk_bf16_f32 v30, v12, v13\n"
"v_cvt_pk_bf16_f32 v31, v14, v15\n"
"v_mfma_f32_16x16x32_bf16 a[16:19], v[128:131], a[148:151], a[16:19]\n"
"ds_write_b64 v8, v[30:31] offset:32776\n"
"v_mfma_f32_16x16x32_bf16 a[20:23], v[64:67], a[148:151], a[20:23]\n"
"ds_read_b128 v[160:163], v2 offset:32768\n"
"v_mfma_f32_16x16x32_bf16 a[20:23], v[36:39], a[144:147], a[20:23]\n"
"ds_read_b128 v[188:191], v2 offset:33792\n"
"v_accvgpr_read_b32 v16, a232\n"
"v_accvgpr_read_b32 v17, a233\n"
"v_mfma_f32_16x16x32_bf16 a[24:27], v[104:107], a[144:147], a[24:27]\n"
"v_accvgpr_read_b32 v18, a234\n"
"v_accvgpr_read_b32 v19, a235\n"
"v_mfma_f32_16x16x32_bf16 a[24:27], v[132:135], a[148:151], a[24:27]\n"
"v_lshlrev_b32 v12, 16, v16\n"
"v_and_b32 v13, s63, v16\n"
"v_mfma_f32_16x16x32_bf16 a[28:31], v[136:139], a[148:151], a[28:31]\n"
"v_lshlrev_b32 v14, 16, v17\n"
"v_and_b32 v15, s63, v17\n"
"v_mfma_f32_16x16x32_bf16 a[28:31], v[108:111], a[144:147], a[28:31]\n"
"v_mul_f32 v12, v12, v164\n"
"v_mul_f32 v13, v13, v165\n"
"v_mfma_f32_16x16x32_bf16 a[28:31], a[88:91], a[152:155], a[28:31]\n"
"v_mul_f32 v14, v14, v166\n"
"v_mul_f32 v15, v15, v167\n"
"v_mfma_f32_16x16x32_bf16 a[28:31], a[92:95], a[156:159], a[28:31]\n"
"v_cvt_pk_bf16_f32 v24, v12, v13\n"
"v_cvt_pk_bf16_f32 v25, v14, v15\n"
"v_mfma_f32_16x16x32_bf16 a[24:27], a[84:87], a[156:159], a[24:27]\n"
"ds_write_b64 v7, v[24:25] offset:34816\n"
"v_mfma_f32_16x16x32_bf16 a[24:27], a[80:83], a[152:155], a[24:27]\n"
"v_lshlrev_b32 v12, 16, v18\n"
"v_and_b32 v13, s63, v18\n"
"v_mfma_f32_16x16x32_bf16 a[20:23], a[72:75], a[152:155], a[20:23]\n"
"s_waitcnt lgkmcnt(2)\n"
"buffer_store_dwordx4 v[160:163], v6, s[52:55], s88 offen offset:128 nt\n"
"v_lshlrev_b32 v14, 16, v19\n"
"v_and_b32 v15, s63, v19\n"
"v_mul_f32 v12, v12, v192\n"
"v_mul_f32 v13, v13, v193\n"
"s_add_u32 s62, s62, 1\n"
"v_mfma_f32_16x16x32_bf16 a[20:23], a[76:79], a[156:159], a[20:23]\n"
"v_mul_f32 v14, v14, v194\n"
"v_mul_f32 v15, v15, v195\n"
"v_mfma_f32_16x16x32_bf16 a[16:19], a[68:71], a[156:159], a[16:19]\n"
"v_cvt_pk_bf16_f32 v26, v12, v13\n"
"v_cvt_pk_bf16_f32 v27, v14, v15\n"
"v_mfma_f32_16x16x32_bf16 a[16:19], a[64:67], a[152:155], a[16:19]\n"
"ds_write_b64 v7, v[26:27] offset:34824\n"
"s_nop 7\n"
"s_waitcnt lgkmcnt(2)\n"
"buffer_store_dwordx4 v[188:191], v6, s[52:55], s89 offen offset:1152 nt\n"
"v_accvgpr_read_b32 v16, a236\n"
"v_accvgpr_read_b32 v17, a237\n"
"v_accvgpr_read_b32 v18, a238\n"
"v_accvgpr_read_b32 v19, a239\n"
"v_lshlrev_b32 v12, 16, v16\n"
"v_and_b32 v13, s63, v16\n"
"v_lshlrev_b32 v14, 16, v17\n"
"v_and_b32 v15, s63, v17\n"
"v_pk_mul_f32 v[12:13], v[12:13], v[220:221]\n"
"v_pk_mul_f32 v[14:15], v[14:15], v[222:223]\n"
"v_cvt_pk_bf16_f32 v28, v12, v13\n"
"v_cvt_pk_bf16_f32 v29, v14, v15\n"
"v_lshlrev_b32 v12, 16, v18\n"
"v_and_b32 v13, s63, v18\n"
"v_lshlrev_b32 v14, 16, v19\n"
"v_and_b32 v15, s63, v19\n"
"v_pk_mul_f32 v[12:13], v[12:13], v[248:249]\n"
"v_pk_mul_f32 v[14:15], v[14:15], v[250:251]\n"
"v_cvt_pk_bf16_f32 v30, v12, v13\n"
"v_cvt_pk_bf16_f32 v31, v14, v15\n"
"ds_write_b128 v8, v[28:31] offset:34816\n"
"ds_read_b128 v[164:167], v2 offset:34816\n"
"ds_read_b128 v[192:195], v2 offset:35840\n"
"v_accvgpr_read_b32 v16, a240\n"
"v_accvgpr_read_b32 v17, a241\n"
"v_accvgpr_read_b32 v18, a242\n"
"v_accvgpr_read_b32 v19, a243\n"
"v_lshlrev_b32 v12, 16, v16\n"
"v_and_b32 v13, s63, v16\n"
"v_lshlrev_b32 v14, 16, v17\n"
"v_and_b32 v15, s63, v17\n"
"v_pk_mul_f32 v[12:13], v[12:13], v[168:169]\n"
"v_pk_mul_f32 v[14:15], v[14:15], v[170:171]\n"
"v_cvt_pk_bf16_f32 v24, v12, v13\n"
"v_cvt_pk_bf16_f32 v25, v14, v15\n"
"v_lshlrev_b32 v12, 16, v18\n"
"v_and_b32 v13, s63, v18\n"
"v_lshlrev_b32 v14, 16, v19\n"
"v_and_b32 v15, s63, v19\n"
"v_pk_mul_f32 v[12:13], v[12:13], v[196:197]\n"
"v_pk_mul_f32 v[14:15], v[14:15], v[198:199]\n"
"v_cvt_pk_bf16_f32 v26, v12, v13\n"
"v_cvt_pk_bf16_f32 v27, v14, v15\n"
"ds_write_b128 v7, v[24:27] offset:36864\n"
"s_waitcnt lgkmcnt(2)\n"
"buffer_store_dwordx4 v[164:167], v6, s[52:55], s90 offen offset:2176 nt\n"
"s_waitcnt lgkmcnt(1)\n"
"buffer_store_dwordx4 v[192:195], v6, s[52:55], s91 offen offset:3200 nt\n"
"v_accvgpr_read_b32 v16, a244\n"
"v_accvgpr_read_b32 v17, a245\n"
"v_accvgpr_read_b32 v18, a246\n"
"v_accvgpr_read_b32 v19, a247\n"
"v_lshlrev_b32 v12, 16, v16\n"
"v_and_b32 v13, s63, v16\n"
"v_lshlrev_b32 v14, 16, v17\n"
"v_and_b32 v15, s63, v17\n"
"v_pk_mul_f32 v[12:13], v[12:13], v[224:225]\n"
"v_pk_mul_f32 v[14:15], v[14:15], v[226:227]\n"
"v_cvt_pk_bf16_f32 v28, v12, v13\n"
"v_cvt_pk_bf16_f32 v29, v14, v15\n"
"v_lshlrev_b32 v12, 16, v18\n"
"v_and_b32 v13, s63, v18\n"
"v_lshlrev_b32 v14, 16, v19\n"
"v_and_b32 v15, s63, v19\n"
"v_pk_mul_f32 v[12:13], v[12:13], v[252:253]\n"
"v_pk_mul_f32 v[14:15], v[14:15], v[254:255]\n"
"v_cvt_pk_bf16_f32 v30, v12, v13\n"
"v_cvt_pk_bf16_f32 v31, v14, v15\n"
"ds_write_b128 v8, v[28:31] offset:36864\n"
"ds_read_b128 v[168:171], v2 offset:36864\n"
"ds_read_b128 v[196:199], v2 offset:37888\n"
"v_accvgpr_read_b32 v16, a248\n"
"v_accvgpr_read_b32 v17, a249\n"
"v_accvgpr_read_b32 v18, a250\n"
"v_accvgpr_read_b32 v19, a251\n"
"v_lshlrev_b32 v12, 16, v16\n"
"v_and_b32 v13, s63, v16\n"
"v_lshlrev_b32 v14, 16, v17\n"
"v_and_b32 v15, s63, v17\n"
"v_accvgpr_read_b32 v10, a16\n"
"v_accvgpr_read_b32 v11, a17\n"
"v_mul_f32 v12, v12, v10\n"
"v_mul_f32 v13, v13, v11\n"
"v_accvgpr_read_b32 v10, a18\n"
"v_accvgpr_read_b32 v11, a19\n"
"v_mul_f32 v14, v14, v10\n"
"v_mul_f32 v15, v15, v11\n"
"v_cvt_pk_bf16_f32 v24, v12, v13\n"
"v_cvt_pk_bf16_f32 v25, v14, v15\n"
"v_lshlrev_b32 v12, 16, v18\n"
"v_and_b32 v13, s63, v18\n"
"v_lshlrev_b32 v14, 16, v19\n"
"v_and_b32 v15, s63, v19\n"
"v_accvgpr_read_b32 v10, a20\n"
"v_accvgpr_read_b32 v11, a21\n"
"v_mul_f32 v12, v12, v10\n"
"v_mul_f32 v13, v13, v11\n"
"v_accvgpr_read_b32 v10, a22\n"
"v_accvgpr_read_b32 v11, a23\n"
"v_mul_f32 v14, v14, v10\n"
"v_mul_f32 v15, v15, v11\n"
"v_cvt_pk_bf16_f32 v26, v12, v13\n"
"v_cvt_pk_bf16_f32 v27, v14, v15\n"
"ds_write_b128 v7, v[24:27] offset:38912\n"
"s_waitcnt lgkmcnt(2)\n"
"buffer_store_dwordx4 v[168:171], v6, s[52:55], s92 offen offset:128 nt\n"
"s_waitcnt lgkmcnt(1)\n"
"buffer_store_dwordx4 v[196:199], v6, s[52:55], s93 offen offset:1152 nt\n"
"v_accvgpr_read_b32 v16, a252\n"
"v_accvgpr_read_b32 v17, a253\n"
"v_accvgpr_read_b32 v18, a254\n"
"v_accvgpr_read_b32 v19, a255\n"
"v_lshlrev_b32 v12, 16, v16\n"
"v_and_b32 v13, s63, v16\n"
"v_lshlrev_b32 v14, 16, v17\n"
"v_and_b32 v15, s63, v17\n"
"v_accvgpr_read_b32 v10, a24\n"
"v_accvgpr_read_b32 v11, a25\n"
"v_mul_f32 v12, v12, v10\n"
"v_mul_f32 v13, v13, v11\n"
"v_accvgpr_read_b32 v10, a26\n"
"v_accvgpr_read_b32 v11, a27\n"
"v_mul_f32 v14, v14, v10\n"
"v_mul_f32 v15, v15, v11\n"
"v_cvt_pk_bf16_f32 v28, v12, v13\n"
"v_cvt_pk_bf16_f32 v29, v14, v15\n"
"v_lshlrev_b32 v12, 16, v18\n"
"v_and_b32 v13, s63, v18\n"
"v_lshlrev_b32 v14, 16, v19\n"
"v_and_b32 v15, s63, v19\n"
"v_accvgpr_read_b32 v10, a28\n"
"v_accvgpr_read_b32 v11, a29\n"
"v_mul_f32 v12, v12, v10\n"
"v_mul_f32 v13, v13, v11\n"
"v_accvgpr_read_b32 v10, a30\n"
"v_accvgpr_read_b32 v11, a31\n"
"v_mul_f32 v14, v14, v10\n"
"v_mul_f32 v15, v15, v11\n"
"v_cvt_pk_bf16_f32 v30, v12, v13\n"
"v_cvt_pk_bf16_f32 v31, v14, v15\n"
"ds_write_b128 v8, v[28:31] offset:38912\n"
"ds_read_b128 v[224:227], v2 offset:38912\n"
"ds_read_b128 v[252:255], v2 offset:39936\n"
"s_waitcnt lgkmcnt(1)\n"
"buffer_store_dwordx4 v[224:227], v6, s[52:55], s94 offen offset:2176 nt\n"
"s_waitcnt lgkmcnt(0)\n"
"buffer_store_dwordx4 v[252:255], v6, s[52:55], s95 offen offset:3200 nt\n"
"s_or_b32 s66, %26, %16\n"
"s_cbranch_scc1 L_end\n"
"v_mov_b32 v10, s78\n"
"v_mov_b32 v11, s79\n"
"s_mov_b32 s66, 139264\n"
"buffer_store_dwordx2 v[10:11], off, s[56:59], s66 sc1\n"
"s_branch L_end\n"
"L_slow0:\n"
"s_cmp_eq_u32 s68, 0\n"
"s_cbranch_scc1 L_back0\n"
"s_sub_u32 s68, s68, 1\n"
"s_sleep 2\n"
"buffer_load_dwordx2 v[8:9], off, s[56:59], %14 sc1\n"
"s_waitcnt vmcnt(0)\n"
"v_subrev_u32 v8, s65, v8\n"
"v_xor_b32 v9, s79, v9\n"
"v_or_b32 v8, v8, v9\n"
"v_cmp_lt_u32 vcc, 15, v8\n"
"s_nop 4\n"
"s_cbranch_vccnz L_slow0\n"
"s_branch L_back0\n"
"L_slow1:\n"
"s_cmp_eq_u32 s68, 0\n"
"s_cbranch_scc1 L_back1\n"
"s_sub_u32 s68, s68, 1\n"
"s_sleep 2\n"
"buffer_load_dwordx2 v[8:9], off, s[56:59], %14 sc1\n"
"s_waitcnt vmcnt(0)\n"
"v_subrev_u32 v8, s65, v8\n"
"v_xor_b32 v9, s79, v9\n"
"v_or_b32 v8, v8, v9\n"
"v_cmp_lt_u32 vcc, 15, v8\n"
"s_nop 4\n"
"s_cbranch_vccnz L_slow1\n"
"s_branch L_back1\n"
"L_slow2:\n"
"s_cmp_eq_u32 s68, 0\n"
"s_cbranch_scc1 L_back2\n"
"s_sub_u32 s68, s68, 1\n"
"s_sleep 2\n"
"buffer_load_dwordx4 v[12:15], v0, s[56:59], %25 offen sc1\n"
"s_waitcnt vmcnt(0)\n"
"v_subrev_u32 v12, s67, v12\n"
"v_xor_b32 v13, s79, v13\n"
"v_subrev_u32 v14, s67, v14\n"
"v_xor_b32 v15, s79, v15\n"
"v_or_b32 v12, v12, v14\n"
"v_or_b32 v13, v13, v15\n"
"v_or_b32 v12, v12, v13\n"
"v_cmp_lt_u32 vcc, 15, v12\n"
"s_nop 4\n"
"s_cbranch_vccnz L_slow2\n"
"s_branch L_back2\n"
"L_slow3:\n"
"s_cmp_eq_u32 s68, 0\n"
"s_cbranch_scc1 L_back3\n"
"s_sub_u32 s68, s68, 1\n"
"s_sleep 2\n"
"buffer_load_dwordx4 v[24:27], v0, s[56:59], %25 offen sc1\n"
"s_waitcnt vmcnt(0)\n"
"v_subrev_u32 v24, s70, v24\n"
"v_xor_b32 v25, s79, v25\n"
"v_subrev_u32 v26, s70, v26\n"
"v_xor_b32 v27, s79, v27\n"
"v_or_b32 v24, v24, v26\n"
"v_or_b32 v25, v25, v27\n"
"v_or_b32 v24, v24, v25\n"
"v_cmp_lt_u32 vcc, 15, v24\n"
"s_nop 4\n"
"s_cbranch_vccnz L_slow3\n"
"s_branch L_back3\n"
"L_slow4:\n"
"s_cmp_eq_u32 s68, 0\n"
"s_cbranch_scc1 L_back4\n"
"s_sub_u32 s68, s68, 1\n"
"s_sleep 2\n"
"buffer_load_dwordx2 v[8:9], off, s[56:59], %14 sc1\n"
"s_waitcnt vmcnt(0)\n"
"v_subrev_u32 v8, s65, v8\n"
"v_xor_b32 v9, s79, v9\n"
"v_or_b32 v8, v8, v9\n"
"v_cmp_lt_u32 vcc, 15, v8\n"
"s_nop 4\n"
"s_cbranch_vccnz L_slow4\n"
"s_branch L_back4\n"
"L_slow7:\n"
"s_cmp_eq_u32 s68, 0\n"
"s_cbranch_scc1 L_back7\n"
"s_sub_u32 s68, s68, 1\n"
"s_sleep 2\n"
"buffer_load_dwordx2 v[8:9], off, s[56:59], %14 sc1\n"
"s_waitcnt vmcnt(0)\n"
"v_subrev_u32 v8, s65, v8\n"
"v_xor_b32 v9, s79, v9\n"
"v_or_b32 v8, v8, v9\n"
"v_cmp_lt_u32 vcc, 15, v8\n"
"s_nop 4\n"
"s_cbranch_vccnz L_slow7\n"
"s_branch L_back7\n"
"L_headF:\n"
"L_end:\n"
"s_waitcnt vmcnt(0) lgkmcnt(0)\n"
    :: LO(wsb), HI(wsb), LO(xo), HI(xo), LO(ob), HI(ob), LO(wr), HI(wr), LO(bb), HI(bb), "s"(wo), "s"(eo), "s"(ep), "s"(fo), "s"(fp),
       "s"(ldsa), "s"(w), "s"(col), "v"(threadIdx.x), "s"(db), "s"(rs1), "s"(rs2), "s"(rd1), "s"(rn1), "s"(wfo), "s"(wfa), "s"(b)
    : "memory", "scc", "vcc",
"v1","v2","v3","v4","v5","v6","v7","v8","v9","v10","v11","v12","v13","v14","v15","v16","v17","v18","v19","v20","v21","v22","v23","v24","v25","v26","v27","v28","v29","v30","v31","v32","v33","v34","v35","v36","v37","v38","v39","v40","v41","v42","v43","v44","v45","v46","v47","v48","v49","v50","v51","v52","v53","v54","v55","v56","v57","v58","v59","v60","v61","v62","v63","v64","v65","v66","v67","v68","v69","v70","v71","v72","v73","v74","v75","v76","v77","v78","v79","v80","v81","v82","v83","v84","v85","v86","v87","v88","v89","v90","v91","v92","v93","v94","v95","v96","v97","v98","v99","v100","v101","v102","v103","v104","v105","v106","v107","v108","v109","v110","v111","v112","v113","v114","v115","v116","v117","v118","v119","v120","v121","v122","v123","v124","v125","v126","v127","v128","v129","v130","v131","v132","v133","v134","v135","v136","v137","v138","v139","v140","v141","v142","v143","v144","v145","v146","v147","v148","v149","v150","v151","v152","v153","v154","v155","v156","v157","v158","v159","v160","v161","v162","v163","v164","v165","v166","v167","v168","v169","v170","v171","v172","v173","v174","v175","v176","v177","v178","v179","v180","v181","v182","v183","v184","v185","v186","v187","v188","v189","v190","v191","v192","v193","v194","v195","v196","v197","v198","v199","v200","v201","v202","v203","v204","v205","v206","v207","v208","v209","v210","v211","v212","v213","v214","v215","v216","v217","v218","v219","v220","v221","v222","v223","v224","v225","v226","v227","v228","v229","v230","v231","v232","v233","v234","v235","v236","v237","v238","v239","v240","v241","v242","v243","v244","v245","v246","v247","v248","v249","v250","v251","v252","v253","v254","v255",
"a0","a1","a2","a3","a4","a5","a6","a7","a8","a9","a10","a11","a12","a13","a14","a15","a16","a17","a18","a19","a20","a21","a22","a23","a24","a25","a26","a27","a28","a29","a30","a31","a32","a33","a34","a35","a36","a37","a38","a39","a40","a41","a42","a43","a44","a45","a46","a47","a48","a49","a50","a51","a52","a53","a54","a55","a56","a57","a58","a59","a60","a61","a62","a63","a64","a65","a66","a67","a68","a69","a70","a71","a72","a73","a74","a75","a76","a77","a78","a79","a80","a81","a82","a83","a84","a85","a86","a87","a88","a89","a90","a91","a92","a93","a94","a95","a96","a97","a98","a99","a100","a101","a102","a103","a104","a105","a106","a107","a108","a109","a110","a111","a112","a113","a114","a115","a116","a117","a118","a119","a120","a121","a122","a123","a124","a125","a126","a127","a128","a129","a130","a131","a132","a133","a134","a135","a136","a137","a138","a139","a140","a141","a142","a143","a144","a145","a146","a147","a148","a149","a150","a151","a152","a153","a154","a155","a156","a157","a158","a159","a160","a161","a162","a163","a164","a165","a166","a167","a168","a169","a170","a171","a172","a173","a174","a175","a176","a177","a178","a179","a180","a181","a182","a183","a184","a185","a186","a187","a188","a189","a190","a191","a192","a193","a194","a195","a196","a197","a198","a199","a200","a201","a202","a203","a204","a205","a206","a207","a208","a209","a210","a211","a212","a213","a214","a215","a216","a217","a218","a219","a220","a221","a222","a223","a224","a225","a226","a227","a228","a229","a230","a231","a232","a233","a234","a235","a236","a237","a238","a239","a240","a241","a242","a243","a244","a245","a246","a247","a248","a249","a250","a251","a252","a253","a254","a255",
"s27","s28","s29","s30","s31","s33","s34","s35","s36","s37","s38","s39","s40","s41","s42","s43","s44","s45","s46","s47","s48","s49","s50","s51","s52","s53","s54","s55","s56","s57","s58","s59","s60","s61","s62","s63","s64","s65","s66","s67","s68","s69","s70","s71","s72","s73","s74","s75","s76","s77","s78","s79","s80","s81","s82","s83","s84","s85","s86","s87","s88","s89","s90","s91","s92","s93","s94","s95","s96","s97","s98","s99");
}

extern "C" void kernel_launch(void* const* d_in, const int* in_sizes, int n_in,
                              void* d_out, int out_size, void* d_ws, size_t ws_size,
                              hipStream_t stream) {
  const unsigned short* x0 = (const unsigned short*)d_in[0];
  const unsigned short* ws = (const unsigned short*)d_in[1];
  const unsigned short* bs = (const unsigned short*)d_in[2];
  dcn_cross_kernel<<<256, 256, 0, stream>>>(x0, ws, bs, (unsigned char*)d_ws, (unsigned short*)d_out);
}
```

```cpp
#include <hip/hip_runtime.h>


constexpr int kD = 1024;
constexpr unsigned kWFlagOff = 131072, kWOff = 1u << 20, kWStream = 200 * 8192, kXchOff = 32u << 20, kDbgOff = 224u << 20;

__device__ __forceinline__ unsigned unit_src(unsigned v, unsigned h, unsigned w) {
  const unsigned l = v >> 5, u = v & 31;
  const unsigned split = ((0xff0000ffu >> u) & 1) - 1;
  const unsigned part = l ? u >> 4 : 0u, g = (0xff00ff00u >> u) & 1;
  const unsigned jh = (u >> (~split & 2)) & 1, c = (u >> (split & 1)) ^ (~split & (0u - jh));
  const unsigned kc = 8 * (h ^ part) + 2 * ((w + c) & 3) + g;
  return (l * 1024 + 64 * kc) * 2048 + jh * 128;
}

__global__ __launch_bounds__(256, 1) __attribute__((aligned(4096)))
void dcn_cross_kernel(const unsigned short* __restrict__ x0, const unsigned short* __restrict__ ws, const unsigned short* __restrict__ bs,
                      unsigned char* __restrict__ wsp, unsigned short* __restrict__ out) {
  __shared__ __attribute__((aligned(16))) unsigned char lds[163840];
  __builtin_amdgcn_sched_barrier(0);
  const unsigned b = blockIdx.x;
  constexpr unsigned kPx = 1 ? 1u : 8u;
  const unsigned h = 1 ? (b & 1) : ((b >> 3) & 1), rb = 1 ? (b >> 1) : ((b >> 4) * 8 + (b & 7));
  const unsigned w = __builtin_amdgcn_readfirstlane(threadIdx.x >> 6);
  asm volatile("s_getpc_b64 s[96:97]\n" "L_pc:\n" "s_lshr_b32 s95, %0, 3\n" "s_lshl2_add_u32 s95, s95, %1\n" "s_mul_i32 s95, s95, 704\n" "s_and_b32 s97, s97, 0xffff\n" "s_mov_b32 s98, L_end-L_pc\n" "s_mov_b32 s99, 0x20000\n" "s_min_u32 s95, s95, L_end-L_pc-704\n" "v_and_b32 v9, 63, %2\n" "v_min_u32 v9, 10, v9\n" "v_lshlrev_b32 v9, 6, v9\n" "buffer_load_dword a252, v9, s[96:99], s95 offen\n"
    :: "s"(b), "s"(w), "v"(threadIdx.x) : "memory", "scc", "v9", "a252", "s95", "s96", "s97", "s98", "s99");
  __builtin_amdgcn_sched_barrier(0);
  unsigned col = (512 * h + 128 * w) * 2, rows = rb * 128 * kD * 2, oofs = rows + col;
  unsigned wo = (h * 4 + w) * kWStream, eo = kXchOff + b * 524288 + w * 32768, ep = kXchOff + (b ^ kPx) * 524288 + w * 32768;
  unsigned fo = b * 512 + w * 128, fp = (b ^ kPx) * 512 + w * 128;
  unsigned db = kDbgOff + (b * 4 + w) * 512;
  const unsigned v1 = 24 + (rb >> 1), v2 = 64 + ((rb >> 1) & 31), hk = rb & 1, h2 = rb >> 6;
  unsigned rs1 = unit_src(v1, 0, w) + hk * 65536, rd1 = v1 * 8192 + hk * 1024;
  unsigned rs2 = unit_src(v2, h2, w) + hk * 65536 + h2 * 1024 + h * ((4u << 20) - 1024), rn1 = v2 * 8192 + hk * 1024 + h2 * 4 * kWStream;
  unsigned wfa = kWFlagOff + w * 1024, wfo = wfa + h * 4096 + rb * 8;
  asm volatile("" : "+s"(col), "+s"(rows), "+s"(oofs), "+s"(wo), "+s"(eo), "+s"(ep), "+s"(fo), "+s"(fp), "+s"(db), "+s"(rs1), "+s"(rd1), "+s"(rs2), "+s"(rn1), "+s"(wfa), "+s"(wfo));
  __builtin_amdgcn_sched_barrier(0);
  const unsigned long long wsb = (unsigned long long)wsp, bb = (unsigned long long)bs;
  const unsigned long long xo = (unsigned long long)x0 + rows, ob = (unsigned long long)out + oofs, wr = (unsigned long long)ws + col;
  wo += (unsigned)wsb > ~(15u << 20) ? 16u << 20 : kWOff;
  const unsigned xa = (0u - (unsigned)wsb) & 0x1fffffu;
  eo += xa; ep += xa;
  const unsigned ldsa = (unsigned)(size_t)(__attribute__((address_space(3))) unsigned char*)lds;
#define LO(x) "s"((unsigned)(x))
#define HI(x) "s"((unsigned)((x) >> 32))
  asm volatile(
"L_main_entry:\n"
"v_and_b32 v12, 63, %18\n"
"v_lshrrev_b32 v13, 3, v12\n"
"v_and_b32 v15, 7, v12\n"
"v_lshlrev_b32 v10, 11, v13\n"
"s_andn2_b32 s60, 1024, %17\n"
"s_lshl_b32 s75, s60, 10\n"
"s_mov_b32 s52, %6\n"
"s_and_b32 s53, %7, 0xffff\n"
"s_mov_b32 s54, 12581120\n"
"s_mov_b32 s55, 0x20000\n"
"v_xor_b32 v6, v15, v13\n"
"v_lshl_add_u32 v6, v6, 4, v10\n"
"s_mov_b32 s48, %2\n"
"s_and_b32 s49, %3, 0xffff\n"
"s_mov_b32 s50, 65536\n"
"s_mov_b32 s51, 0x20000\n"
"s_lshl_b32 s66, %16, 16\n"
"s_add_u32 s48, s48, s66\n"
"s_addc_u32 s49, s49, 0\n"
"s_lshl_b32 s72, %16, 12\n"
"s_add_u32 s72, s72, %15\n"
"s_mul_i32 s73, %16, 40960\n"
"s_add_u32 s64, s73, %15\n"
"v_lshrrev_b32 v11, 2, v12\n"
"v_and_b32 v11, 4, v11\n"
"v_xor_b32 v11, v11, v15\n"
"v_lshl_add_u32 v11, v11, 4, v10\n"
"s_bitcmp1_b32 %26, 0\n"
"s_cbranch_scc1 L_y\n"
"s_mov_b64 s[96:97], s[52:53]\n"
"s_cmp_lt_u32 %22, 524288\n"
"s_cselect_b32 s98, s54, 0\n"
"s_mov_b32 s99, 0x20000\n"
"s_xor_b32 s71, %20, 0x100000\n"
"s_add_u32 m0, s72, 139264\n"
"s_add_u32 s67, %20, 0\n"
"buffer_load_dwordx4 v11, s[96:99], s67 offen offset:0  lds\n"
"s_add_u32 s67, %20, 15360\n"
"buffer_load_dwordx4 v11, s[96:99], s67 offen offset:1024  lds\n"
"s_add_u32 s67, %20, 30720\n"
"buffer_load_dwordx4 v11, s[96:99], s67 offen offset:2048  lds\n"
"s_add_u32 s67, %20, 46080\n"
"buffer_load_dwordx4 v11, s[96:99], s67 offen offset:3072  lds\n"
"s_add_u32 m0, s72, 98304\n"
"s_add_u32 s67, s71, 1024\n"
"buffer_load_dwordx4 v11, s[96:99], s67 offen offset:0  lds\n"
"s_add_u32 s67, s71, 16384\n"
"buffer_load_dwordx4 v11, s[96:99], s67 offen offset:1024  lds\n"
"s_add_u32 s67, s71, 31744\n"
"buffer_load_dwordx4 v11, s[96:99], s67 offen offset:2048  lds\n"
"s_add_u32 s67, s71, 47104\n"
"buffer_load_dwordx4 v11, s[96:99], s67 offen offset:3072  lds\n"
"L_y:\n"
"s_add_u32 m0, s72, 16384\n"
"s_add_u32 s67, s60, 0\n"
"buffer_load_dwordx4 v6, s[48:51], s67 offen offset:0  lds\n"
"s_add_u32 s67, s60, 15360\n"
"buffer_load_dwordx4 v6, s[48:51], s67 offen offset:1024  lds\n"
"s_add_u32 s67, s60, 30720\n"
"buffer_load_dwordx4 v6, s[48:51], s67 offen offset:2048  lds\n"
"s_add_u32 s67, s60, 46080\n"
"buffer_load_dwordx4 v6, s[48:51], s67 offen offset:3072  lds\n"
"s_add_u32 m0, s64, 0\n"
"s_add_u32 s67, s75, 0\n"
"buffer_load_dwordx4 v11, s[52:55], s67 offen offset:0  lds\n"
"s_add_u32 s67, s75, 15360\n"
"buffer_load_dwordx4 v11, s[52:55], s67 offen offset:1024  lds\n"
"s_add_u32 s67, s75, 30720\n"
"buffer_load_dwordx4 v11, s[52:55], s67 offen offset:2048  lds\n"
"s_add_u32 s67, s75, 46080\n"
"buffer_load_dwordx4 v11, s[52:55], s67 offen offset:3072  lds\n"
"s_add_u32 m0, s64, 4096\n"
"s_add_u32 s67, s75, 65536\n"
"buffer_load_dwordx4 v11, s[52:55], s67 offen offset:0  lds\n"
"s_add_u32 s67, s75, 80896\n"
"buffer_load_dwordx4 v11, s[52:55], s67 offen offset:1024  lds\n"
"s_add_u32 s67, s75, 96256\n"
"buffer_load_dwordx4 v11, s[52:55], s67 offen offset:2048  lds\n"
"s_add_u32 s67, s75, 111616\n"
"buffer_load_dwordx4 v11, s[52:55], s67 offen offset:3072  lds\n"
"s_lshr_b32 s65, %26, 4\n"
"s_lshl_b32 s65, s65, 2\n"
"s_add_u32 s65, s65, %16\n"
"s_lshl_b32 s65, s65, 11\n"
"s_lshl_b32 s67, %16, 8\n"
"s_sub_u32 s65, s65, s67\n"
"v_and_b32 v14, 1, v13\n"
"v_lshlrev_b32 v14, 17, v14\n"
"v_lshl_add_u32 v14, v15, 7, v14\n"
"v_and_b32 v14, 0xfffffdff, v14\n"
"s_lshl_b32 s67, %26, 6\n"
"s_and_b32 s67, s67, 512\n"
"s_add_u32 s65, s65, s67\n"
"v_lshlrev_b32 v0, 4, v12\n"
"s_mov_b32 s56, %0\n"
"s_and_b32 s57, %1, 0xffff\n"
"s_mov_b32 s58, 171966464\n"
"s_mov_b32 s59, 0x20000\n"
"v_add_u32 v1, 4096, v0\n"
"s_add_u32 s36, s56, %10\n"
"s_addc_u32 s37, s57, 0\n"
"s_mov_b32 s38, 1572864\n"
"s_mov_b32 s39, 0x20000\n"
"s_add_u32 s34, s36, 200704\n"
"s_mov_b32 s35, s37\n"
"v_and_b32 v13, 15, v12\n"
"v_lshrrev_b32 v10, 4, v12\n"
"v_xor_b32 v15, v15, v10\n"
"v_lshlrev_b32 v7, 7, v13\n"
"v_lshl_add_u32 v7, v15, 4, v7\n"
"v_add_u32 v7, s64, v7\n"
"v_xor_b32 v8, 64, v7\n"
"v_bfe_u32 v13, v12, 2, 2\n"
"v_and_b32 v15, 3, v12\n"
"v_bfe_u32 v9, v12, 3, 1\n"
"v_lshl_add_u32 v15, v9, 2, v15\n"
"v_lshl_add_u32 v13, v10, 3, v13\n"
"v_lshlrev_b32 v13, 7, v13\n"
"v_lshl_add_u32 v9, v15, 4, v13\n"
"v_add_u32 v9, s64, v9\n"
"v_xor_b32 v15, 64, v9\n"
"s_mov_b32 s40, %2\n"
"s_and_b32 s41, %3, 0xffff\n"
"s_mov_b32 s42, 260352\n"
"s_mov_b32 s43, 0x20000\n"
"s_add_u32 s40, s40, %17\n"
"s_addc_u32 s41, s41, 0\n"
"s_mov_b32 s80, 0\n"
"s_mov_b32 s81, 15360\n"
"s_mov_b32 s82, 30720\n"
"s_mov_b32 s83, 46080\n"
"s_mov_b32 s84, 65536\n"
"s_mov_b32 s85, 80896\n"
"s_mov_b32 s86, 96256\n"
"s_mov_b32 s87, 111616\n"
"s_mov_b32 s88, 131072\n"
"s_mov_b32 s89, 146432\n"
"s_mov_b32 s90, 161792\n"
"s_mov_b32 s91, 177152\n"
"s_mov_b32 s92, 196608\n"
"s_mov_b32 s93, 211968\n"
"s_mov_b32 s94, 227328\n"
"s_mov_b32 s95, 242688\n"
"s_mov_b32 s62, 0\n"
"s_mov_b32 s63, 0xffff0000\n"
"s_mov_b32 s68, 0x8000\n"
"s_add_u32 s66, %16, 0\n"
"s_and_b32 s66, s66, 3\n"
"s_mul_i32 s66, s66, 40960\n"
"s_add_u32 s66, s66, %15\n"
"v_add_u32 v2, s66, v0\n"
"s_add_u32 s66, %16, 1\n"
"s_and_b32 s66, s66, 3\n"
"s_mul_i32 s66, s66, 40960\n"
"s_add_u32 s66, s66, %15\n"
"v_add_u32 v3, s66, v0\n"
"s_add_u32 s66, %16, 2\n"
"s_and_b32 s66, s66, 3\n"
"s_mul_i32 s66, s66, 40960\n"
"s_add_u32 s66, s66, %15\n"
"v_add_u32 v4, s66, v0\n"
"s_add_u32 s66, %16, 3\n"
"s_and_b32 s66, s66, 3\n"
"s_mul_i32 s66, s66, 40960\n"
"s_add_u32 s66, s66, %15\n"
"v_add_u32 v5, s66, v0\n"
"s_sub_u32 s66, 0, s73\n"
"v_add_u32 v12, s66, v7\n"
"v_add_u32 v13, s66, v8\n"
"s_mov_b64 exec, 0\n"
"s_cmp_eq_u32 %16, 0\n"
"s_cbranch_scc1 L_st518\n"
"s_cmp_eq_u32 %16, 1\n"
"s_cbranch_scc1 L_st1045\n"
"s_cmp_eq_u32 %16, 2\n"
"s_cbranch_scc1 L_st1575\n"
"s_branch L_st2117\n"
"L_retA:\n"
"s_mov_b64 exec, -1\n"
"s_nop 3\n"
"s_mov_b32 s44, %8\n"
"s_and_b32 s45, %9, 0xffff\n"
"s_mov_b32 s46, 12288\n"
"s_mov_b32 s47, 0x20000\n"
"s_mov_b32 s61, %17\n"
"v_lshrrev_b32 v16, 4, v0\n"
"v_and_b32 v16, 0x30, v16\n"
"buffer_load_dwordx2 v[18:19], v16, s[44:47], s61 offen offset:0\n"
"v_lshrrev_b32 v20, 4, v0\n"
"v_and_b32 v20, 0x30, v20\n"
"buffer_load_dwordx2 v[22:23], v20, s[44:47], s61 offen offset:8\n"
"v_lshrrev_b32 v24, 4, v0\n"
"v_and_b32 v24, 0x30, v24\n"
"buffer_load_dwordx2 v[26:27], v24, s[44:47], s61 offen offset:64\n"
"v_lshrrev_b32 v28, 4, v0\n"
"v_and_b32 v28, 0x30, v28\n"
"buffer_load_dwordx2 v[30:31], v28, s[44:47], s61 offen offset:72\n"
"s_add_u32 s70, s75, 131072\n"
"s_add_u32 s70, s70, s65\n"
"buffer_load_dword a252, v14, s[52:55], s70 offen\n"
"s_add_u32 m0, s64, 8192\n"
"s_add_u32 s67, s75, 128\n"
"buffer_load_dwordx4 v11, s[52:55], s67 offen offset:0  lds\n"
"s_add_u32 s67, s75, 15488\n"
"buffer_load_dwordx4 v11, s[52:55], s67 offen offset:1024  lds\n"
"s_add_u32 s67, s75, 30848\n"
"buffer_load_dwordx4 v11, s[52:55], s67 offen offset:2048  lds\n"
"s_add_u32 s67, s75, 46208\n"
"buffer_load_dwordx4 v11, s[52:55], s67 offen offset:3072  lds\n"
"s_add_u32 m0, s64, 12288\n"
"s_add_u32 s67, s75, 65664\n"
"buffer_load_dwordx4 v11, s[52:55], s67 offen offset:0  lds\n"
"s_add_u32 s67, s75, 81024\n"
"buffer_load_dwordx4 v11, s[52:55], s67 offen offset:1024  lds\n"
"s_add_u32 s67, s75, 96384\n"
"buffer_load_dwordx4 v11, s[52:55], s67 offen offset:2048  lds\n"
"s_add_u32 s67, s75, 111744\n"
"buffer_load_dwordx4 v11, s[52:55], s67 offen offset:3072  lds\n"
"s_mov_b32 s67, 139264\n"
"buffer_load_dwordx2 v[252:253], off, s[56:59], s67 sc1\n"
"s_waitcnt vmcnt(22)\n"
"s_barrier\n"
"ds_read_b128 a[100:103], v13 offset:16384\n"
"ds_read_b128 a[96:99], v12 offset:16384\n"
"ds_read_b128 a[108:111], v13 offset:18432\n"
"ds_read_b128 a[104:107], v12 offset:18432\n"
"ds_read_b128 a[116:119], v13 offset:20480\n"
"ds_read_b128 a[112:115], v12 offset:20480\n"
"ds_read_b128 a[124:127], v13 offset:22528\n"
"ds_read_b128 a[120:123], v12 offset:22528\n"
"ds_read_b128 a[132:135], v13 offset:24576\n"
"ds_read_b128 a[128:131], v12 offset:24576\n"
"ds_read_b128 a[140:143], v13 offset:26624\n"
"ds_read_b128 a[136:139], v12 offset:26624\n"
"ds_read_b128 a[148:151], v13 offset:28672\n"
"ds_read_b128 a[144:147], v12 offset:28672\n"
"ds_read_b128 a[156:159], v13 offset:30720\n"
"ds_read_b128 a[152:155], v12 offset:30720\n"
"s_add_u32 m0, s72, 57344\n"
"s_add_u32 s67, s60, 128\n"
"buffer_load_dwordx4 v6, s[48:51], s67 offen offset:0  lds\n"
"s_add_u32 s67, s60, 15488\n"
"buffer_load_dwordx4 v6, s[48:51], s67 offen offset:1024  lds\n"
"s_add_u32 s67, s60, 30848\n"
"buffer_load_dwordx4 v6, s[48:51], s67 offen offset:2048  lds\n"
"s_add_u32 s67, s60, 46208\n"
"buffer_load_dwordx4 v6, s[48:51], s67 offen offset:3072  lds\n"
"s_waitcnt vmcnt(18)\n"
"ds_read_b64_tr_b16 a[32:33], v9 offset:0\n"
"ds_read_b64_tr_b16 a[34:35], v9 offset:512\n"
"ds_read_b64_tr_b16 a[36:37], v9 offset:4096\n"
"ds_read_b64_tr_b16 a[38:39], v9 offset:4608\n"
"ds_read_b64_tr_b16 a[40:41], v9 offset:8\n"
"ds_read_b64_tr_b16 a[42:43], v9 offset:520\n"
"ds_read_b64_tr_b16 a[44:45], v9 offset:4104\n"
"ds_read_b64_tr_b16 a[46:47], v9 offset:4616\n"
"ds_read_b64_tr_b16 a[48:49], v15 offset:0\n"
"ds_read_b64_tr_b16 a[50:51], v15 offset:512\n"
"ds_read_b64_tr_b16 a[52:53], v15 offset:4096\n"
"ds_read_b64_tr_b16 a[54:55], v15 offset:4608\n"
"ds_read_b64_tr_b16 a[56:57], v15 offset:8\n"
"ds_read_b64_tr_b16 a[58:59], v15 offset:520\n"
"ds_read_b64_tr_b16 a[60:61], v15 offset:4104\n"
"ds_read_b64_tr_b16 a[62:63], v15 offset:4616\n"
"s_waitcnt vmcnt(14)\n"
"v_lshlrev_b32 v16, 16, v18\n"
"v_and_b32 v17, s63, v18\n"
"v_lshlrev_b32 v18, 16, v19\n"
"v_and_b32 v19, s63, v19\n"
"v_add_f32 v16, 1.0, v16\n"
"v_add_f32 v17, 1.0, v17\n"
"v_add_f32 v18, 1.0, v18\n"
"v_add_f32 v19, 1.0, v19\n"
"v_lshlrev_b32 v20, 16, v22\n"
"v_and_b32 v21, s63, v22\n"
"v_lshlrev_b32 v22, 16, v23\n"
"v_and_b32 v23, s63, v23\n"
"v_add_f32 v20, 1.0, v20\n"
"v_add_f32 v21, 1.0, v21\n"
"v_add_f32 v22, 1.0, v22\n"
"v_add_f32 v23, 1.0, v23\n"
"v_lshlrev_b32 v24, 16, v26\n"
"v_and_b32 v25, s63, v26\n"
"v_lshlrev_b32 v26, 16, v27\n"
"v_and_b32 v27, s63, v27\n"
"v_add_f32 v24, 1.0, v24\n"
"v_add_f32 v25, 1.0, v25\n"
"v_add_f32 v26, 1.0, v26\n"
"v_add_f32 v27, 1.0, v27\n"
"v_lshlrev_b32 v28, 16, v30\n"
"v_and_b32 v29, s63, v30\n"
"v_lshlrev_b32 v30, 16, v31\n"
"v_and_b32 v31, s63, v31\n"
"v_add_f32 v28, 1.0, v28\n"
"v_add_f32 v29, 1.0, v29\n"
"v_add_f32 v30, 1.0, v30\n"
"v_add_f32 v31, 1.0, v31\n"
"v_accvgpr_write_b32 a0, v16\n"
"v_accvgpr_write_b32 a1, v17\n"
"v_accvgpr_write_b32 a2, v18\n"
"v_accvgpr_write_b32 a3, v19\n"
"v_accvgpr_write_b32 a4, v20\n"
"v_accvgpr_write_b32 a5, v21\n"
"v_accvgpr_write_b32 a6, v22\n"
"v_accvgpr_write_b32 a7, v23\n"
"v_accvgpr_write_b32 a8, v24\n"
"v_accvgpr_write_b32 a9, v25\n"
"v_accvgpr_write_b32 a10, v26\n"
"v_accvgpr_write_b32 a11, v27\n"
"v_accvgpr_write_b32 a12, v28\n"
"v_accvgpr_write_b32 a13, v29\n"
"v_accvgpr_write_b32 a14, v30\n"
"v_accvgpr_write_b32 a15, v31\n"
"s_bitcmp1_b32 %26, 0\n"
"s_cbranch_scc1 L_z\n"
"s_cmp_lt_u32 %22, 524288\n"
"s_cselect_b32 s38, 8192000, 0\n"
"s_add_u32 s70, %22, 6553600\n"
"s_sub_u32 s67, s72, s64\n"
"s_add_u32 s67, s67, 98304\n"
"v_add_u32 v40, s67, v9\n"
"v_add_u32 v41, s67, v15\n"
"s_sleep 1\n"
"ds_read_b64_tr_b16 a[160:161], v40 offset:40960\n"
"ds_read_b64_tr_b16 a[162:163], v40 offset:41472\n"
"ds_read_b64_tr_b16 a[164:165], v40 offset:40968\n"
"ds_read_b64_tr_b16 a[166:167], v40 offset:41480\n"
"ds_read_b64_tr_b16 a[168:169], v41 offset:40960\n"
"ds_read_b64_tr_b16 a[170:171], v41 offset:41472\n"
"ds_read_b64_tr_b16 a[172:173], v41 offset:40968\n"
"ds_read_b64_tr_b16 a[174:175], v41 offset:41480\n"
"ds_read_b64_tr_b16 a[176:177], v40 offset:0\n"
"ds_read_b64_tr_b16 a[178:179], v40 offset:512\n"
"ds_read_b64_tr_b16 a[180:181], v40 offset:8\n"
"ds_read_b64_tr_b16 a[182:183], v40 offset:520\n"
"ds_read_b64_tr_b16 a[184:185], v41 offset:0\n"
"ds_read_b64_tr_b16 a[186:187], v41 offset:512\n"
"ds_read_b64_tr_b16 a[188:189], v41 offset:8\n"
"ds_read_b64_tr_b16 a[190:191], v41 offset:520\n"
"s_waitcnt lgkmcnt(14)\n"
"buffer_store_dwordx4 a[160:163], v0, s[36:39], %22 offen offset:0 sc1\n"
"s_waitcnt lgkmcnt(12)\n"
"buffer_store_dwordx4 a[164:167], v0, s[36:39], %22 offen offset:2048 sc1\n"
"s_waitcnt lgkmcnt(10)\n"
"buffer_store_dwordx4 a[168:171], v1, s[36:39], %22 offen offset:0 sc1\n"
"s_waitcnt lgkmcnt(8)\n"
"buffer_store_dwordx4 a[172:175], v1, s[36:39], %22 offen offset:2048 sc1\n"
"s_waitcnt lgkmcnt(6)\n"
"buffer_store_dwordx4 a[176:179], v0, s[36:39], s70 offen offset:0 sc1\n"
"s_waitcnt lgkmcnt(4)\n"
"buffer_store_dwordx4 a[180:183], v0, s[36:39], s70 offen offset:2048 sc1\n"
"s_waitcnt lgkmcnt(2)\n"
"buffer_store_dwordx4 a[184:187], v1, s[36:39], s70 offen offset:0 sc1\n"
"s_waitcnt lgkmcnt(0)\n"
"buffer_store_dwordx4 a[188:191], v1, s[36:39], s70 offen offset:2048 sc1\n"
"s_sleep 10\n"
"L_z:\n"
"s_nop 1\n"
"s_waitcnt lgkmcnt(8)\n"
"v_mfma_f32_16x16x32_bf16 v[32:35], a[32:35], a[96:99], v[16:19]\n"
"s_add_u32 s70, s75, 393216\n"
"v_mfma_f32_16x16x32_bf16 v[32:35], a[36:39], a[100:103], v[32:35]\n"
"s_add_u32 s70, s70, s65\n"
"v_mfma_f32_16x16x32_bf16 v[60:63], a[44:47], a[100:103], v[20:23]\n"
"buffer_load_dword a252, v14, s[52:55], s70 offen\n"
"v_mfma_f32_16x16x32_bf16 v[60:63], a[40:43], a[96:99], v[60:63]\n"
"s_add_u32 m0, s64, 32768\n"
"v_mfma_f32_16x16x32_bf16 v[64:67], a[40:43], a[104:107], v[20:23]\n"
"s_add_u32 s67, s75, 131072\n"
"v_mfma_f32_16x16x32_bf16 v[64:67], a[44:47], a[108:111], v[64:67]\n"
"buffer_load_dwordx4 v11, s[52:55], s67 offen offset:0  lds\n"
"v_mfma_f32_16x16x32_bf16 v[36:39], a[36:39], a[108:111], v[16:19]\n"
"s_add_u32 s67, s75, 146432\n"
"v_mfma_f32_16x16x32_bf16 v[36:39], a[32:35], a[104:107], v[36:39]\n"
"v_mfma_f32_16x16x32_bf16 v[40:43], a[32:35], a[112:115], v[16:19]\n"
"s_waitcnt lgkmcnt(0)\n"
"buffer_load_dwordx4 v11, s[52:55], s67 offen offset:1024  lds\n"
"v_mfma_f32_16x16x32_bf16 v[40:43], a[36:39], a[116:119], v[40:43]\n"
"v_mfma_f32_16x16x32_bf16 v[68:71], a[44:47], a[116:119], v[20:23]\n"
"s_add_u32 s67, s75, 161792\n"
"v_mfma_f32_16x16x32_bf16 v[68:71], a[40:43], a[112:115], v[68:71]\n"
"v_mfma_f32_16x16x32_bf16 v[72:75], a[40:43], a[120:123], v[20:23]\n"
"buffer_load_dwordx4 v11, s[52:55], s67 offen offset:2048  lds\n"
"v_mfma_f32_16x16x32_bf16 v[72:75], a[44:47], a[124:127], v[72:75]\n"
"v_mfma_f32_16x16x32_bf16 v[44:47], a[36:39], a[124:127], v[16:19]\n"
"s_add_u32 s67, s75, 177152\n"
"v_mfma_f32_16x16x32_bf16 v[44:47], a[32:35], a[120:123], v[44:47]\n"
"v_mfma_f32_16x16x32_bf16 v[48:51], a[32:35], a[128:131], v[16:19]\n"
"buffer_load_dwordx4 v11, s[52:55], s67 offen offset:3072  lds\n"
"v_mfma_f32_16x16x32_bf16 v[48:51], a[36:39], a[132:135], v[48:51]\n"
"v_mfma_f32_16x16x32_bf16 v[76:79], a[44:47], a[132:135], v[20:23]\n"
"s_add_u32 m0, s64, 36864\n"
"v_mfma_f32_16x16x32_bf16 v[76:79], a[40:43], a[128:131], v[76:79]\n"
"v_mfma_f32_16x16x32_bf16 v[80:83], a[40:43], a[136:139], v[20:23]\n"
"s_add_u32 s67, s75, 196608\n"
"v_mfma_f32_16x16x32_bf16 v[80:83], a[44:47], a[140:143], v[80:83]\n"
"v_mfma_f32_16x16x32_bf16 v[52:55], a[36:39], a[140:143], v[16:19]\n"
"buffer_load_dwordx4 v11, s[52:55], s67 offen offset:0  lds\n"
"v_mfma_f32_16x16x32_bf16 v[52:55], a[32:35], a[136:139], v[52:55]\n"
"v_mfma_f32_16x16x32_bf16 v[56:59], a[32:35], a[144:147], v[16:19]\n"
"s_mov_b64 exec, 0\n"
"s_cmp_eq_u32 %16, 0\n"
"s_cbranch_scc1 L_st5078\n"
"s_cmp_eq_u32 %16, 1\n"
"s_cbranch_scc1 L_st5690\n"
"s_cmp_eq_u32 %16, 2\n"
"s_cbranch_scc1 L_st6226\n"
"s_branch L_st6780\n"
"L_retB:\n"
"s_mov_b64 exec, -1\n"
"s_nop 3\n"
"s_add_u32 s67, s75, 211968\n"
"v_mfma_f32_16x16x32_bf16 v[56:59], a[36:39], a[148:151], v[56:59]\n"
"v_mfma_f32_16x16x32_bf16 v[84:87], a[44:47], a[148:151], v[20:23]\n"
"buffer_load_dwordx4 v11, s[52:55], s67 offen offset:1024  lds\n"
"v_mfma_f32_16x16x32_bf16 v[84:87], a[40:43], a[144:147], v[84:87]\n"
"v_mfma_f32_16x16x32_bf16 a[4:7], a[40:43], a[152:155], a[4:7]\n"
"s_add_u32 s67, s75, 227328\n"
"v_mfma_f32_16x16x32_bf16 a[4:7], a[44:47], a[156:159], a[4:7]\n"
"v_mfma_f32_16x16x32_bf16 a[0:3], a[36:39], a[156:159], a[0:3]\n"
"buffer_load_dwordx4 v11, s[52:55], s67 offen offset:2048  lds\n"
"v_mfma_f32_16x16x32_bf16 a[0:3], a[32:35], a[152:155], a[0:3]\n"
"v_mfma_f32_16x16x32_bf16 v[88:91], a[48:51], a[96:99], v[24:27]\n"
"s_add_u32 s67, s75, 242688\n"
"v_mfma_f32_16x16x32_bf16 v[88:91], a[52:55], a[100:103], v[88:91]\n"
"v_mfma_f32_16x16x32_bf16 v[116:119], a[60:63], a[100:103], v[28:31]\n"
"v_mfma_f32_16x16x32_bf16 v[116:119], a[56:59], a[96:99], v[116:119]\n"
"buffer_load_dwordx4 v11, s[52:55], s67 offen offset:3072  lds\n"
"v_mfma_f32_16x16x32_bf16 v[120:123], a[56:59], a[104:107], v[28:31]\n"
"s_add_u32 m0, s64, 0\n"
"v_mfma_f32_16x16x32_bf16 v[120:123], a[60:63], a[108:111], v[120:123]\n"
"v_mfma_f32_16x16x32_bf16 v[92:95], a[52:55], a[108:111], v[24:27]\n"
"s_add_u32 s67, s75, 131200\n"
"v_mfma_f32_16x16x32_bf16 v[92:95], a[48:51], a[104:107], v[92:95]\n"
"v_mfma_f32_16x16x32_bf16 v[96:99], a[48:51], a[112:115], v[24:27]\n"
"buffer_load_dwordx4 v11, s[52:55], s67 offen offset:0  lds\n"
"v_mfma_f32_16x16x32_bf16 v[96:99], a[52:55], a[116:119], v[96:99]\n"
"v_mfma_f32_16x16x32_bf16 v[124:127], a[60:63], a[116:119], v[28:31]\n"
"s_add_u32 s67, s75, 146560\n"
"v_mfma_f32_16x16x32_bf16 v[124:127], a[56:59], a[112:115], v[124:127]\n"
"buffer_load_dwordx4 v11, s[52:55], s67 offen offset:1024  lds\n"
"v_mfma_f32_16x16x32_bf16 v[128:131], a[56:59], a[120:123], v[28:31]\n"
"s_waitcnt vmcnt(16)\n"
"ds_read_b64_tr_b16 a[64:65], v9 offset:8192\n"
"v_mfma_f32_16x16x32_bf16 v[128:131], a[60:63], a[124:127], v[128:131]\n"
"s_add_u32 s67, s75, 161920\n"
"ds_read_b64_tr_b16 a[66:67], v9 offset:8704\n"
"v_mfma_f32_16x16x32_bf16 v[100:103], a[52:55], a[124:127], v[24:27]\n"
"ds_read_b64_tr_b16 a[68:69], v9 offset:12288\n"
"v_mfma_f32_16x16x32_bf16 v[100:103], a[48:51], a[120:123], v[100:103]\n"
"ds_read_b64_tr_b16 a[70:71], v9 offset:12800\n"
"v_mfma_f32_16x16x32_bf16 v[104:107], a[48:51], a[128:131], v[24:27]\n"
"ds_read_b64_tr_b16 a[72:73], v9 offset:8200\n"
"v_mfma_f32_16x16x32_bf16 v[104:107], a[52:55], a[132:135], v[104:107]\n"
"ds_read_b64_tr_b16 a[74:75], v9 offset:8712\n"
"buffer_load_dwordx4 v11, s[52:55], s67 offen offset:2048  lds\n"
"v_mfma_f32_16x16x32_bf16 v[132:135], a[60:63], a[132:135], v[28:31]\n"
"ds_read_b64_tr_b16 a[76:77], v9 offset:12296\n"
"v_mfma_f32_16x16x32_bf16 v[132:135], a[56:59], a[128:131], v[132:135]\n"
"s_add_u32 s67, s75, 177280\n"
"ds_read_b64_tr_b16 a[78:79], v9 offset:12808\n"
"v_mfma_f32_16x16x32_bf16 v[136:139], a[56:59], a[136:139], v[28:31]\n"
"buffer_load_dwordx4 v11, s[52:55], s67 offen offset:3072  lds\n"
"v_mfma_f32_16x16x32_bf16 v[136:139], a[60:63], a[140:143], v[136:139]\n"
"ds_read_b64_tr_b16 a[80:81], v15 offset:8192\n"
"v_mfma_f32_16x16x32_bf16 v[108:111], a[52:55], a[140:143], v[24:27]\n"
"s_add_u32 m0, s64, 4096\n"
"ds_read_b64_tr_b16 a[82:83], v15 offset:8704\n"
"v_mfma_f32_16x16x32_bf16 v[108:111], a[48:51], a[136:139], v[108:111]\n"
"ds_read_b64_tr_b16 a[84:85], v15 offset:12288\n"
"v_mfma_f32_16x16x32_bf16 v[112:115], a[48:51], a[144:147], v[24:27]\n"
"s_add_u32 s67, s75, 196736\n"
"ds_read_b64_tr_b16 a[86:87], v15 offset:12800\n"
"v_mfma_f32_16x16x32_bf16 v[112:115], a[52:55], a[148:151], v[112:115]\n"
"ds_read_b64_tr_b16 a[88:89], v15 offset:8200\n"
"v_mfma_f32_16x16x32_bf16 v[140:143], a[60:63], a[148:151], v[28:31]\n"
"ds_read_b64_tr_b16 a[90:91], v15 offset:8712\n"
"buffer_load_dwordx4 v11, s[52:55], s67 offen offset:0  lds\n"
"v_mfma_f32_16x16x32_bf16 v[140:143], a[56:59], a[144:147], v[140:143]\n"
"ds_read_b64_tr_b16 a[92:93], v15 offset:12296\n"
"v_mfma_f32_16x16x32_bf16 a[12:15], a[56:59], a[152:155], a[12:15]\n"
"s_add_u32 s67, s75, 212096\n"
"ds_read_b64_tr_b16 a[94:95], v15 offset:12808\n"
"v_mfma_f32_16x16x32_bf16 a[12:15], a[60:63], a[156:159], a[12:15]\n"
"v_mfma_f32_16x16x32_bf16 a[8:11], a[52:55], a[156:159], a[8:11]\n"
"buffer_load_dwordx4 v11, s[52:55], s67 offen offset:1024  lds\n"
"v_mfma_f32_16x16x32_bf16 a[8:11], a[48:51], a[152:155], a[8:11]\n"
"s_waitcnt lgkmcnt(8)\n"
"v_mfma_f32_16x16x32_bf16 v[144:147], a[64:67], a[96:99], 1.0\n"
"s_add_u32 s67, s75, 227456\n"
"v_mfma_f32_16x16x32_bf16 v[144:147], a[68:71], a[100:103], v[144:147]\n"
"v_mfma_f32_16x16x32_bf16 v[172:175], a[76:79], a[100:103], 1.0\n"
"s_waitcnt vmcnt(19) lgkmcnt(0)\n"
"v_readfirstlane_b32 s78, v252\n"
"v_readfirstlane_b32 s79, v253\n"
"v_mfma_f32_16x16x32_bf16 v[172:175], a[72:75], a[96:99], v[172:175]\n"
"buffer_load_dwordx4 v11, s[52:55], s67 offen offset:2048  lds\n"
"v_mfma_f32_16x16x32_bf16 v[176:179], a[72:75], a[104:107], 1.0\n"
"v_mfma_f32_16x16x32_bf16 v[176:179], a[76:79], a[108:111], v[176:179]\n"
"s_add_u32 s67, s75, 242816\n"
"v_mfma_f32_16x16x32_bf16 v[148:151], a[68:71], a[108:111], 1.0\n"
"s_add_u32 s78, s78, 16\n"
"v_mfma_f32_16x16x32_bf16 v[148:151], a[64:67], a[104:107], v[148:151]\n"
"s_add_u32 s79, s79, 16\n"
"v_mfma_f32_16x16x32_bf16 v[152:155], a[64:67], a[112:115], 1.0\n"
"s_sub_u32 s66, 40960, s73\n"
"v_mfma_f32_16x16x32_bf16 v[152:155], a[68:71], a[116:119], v[152:155]\n"
"buffer_load_dwordx4 v11, s[52:55], s67 offen offset:3072  lds\n"
"v_mfma_f32_16x16x32_bf16 v[180:183], a[76:79], a[116:119], 1.0\n"
"s_add_u32 m0, s72, 98304\n"
"v_add_u32 v12, s66, v7\n"
"v_mfma_f32_16x16x32_bf16 v[180:183], a[72:75], a[112:115], v[180:183]\n"
"v_mfma_f32_16x16x32_bf16 v[184:187], a[72:75], a[120:123], 1.0\n"
"s_add_u32 s67, s60, 256\n"
"v_add_u32 v13, s66, v8\n"
"v_mfma_f32_16x16x32_bf16 v[184:187], a[76:79], a[124:127], v[184:187]\n"
"buffer_load_dwordx4 v6, s[48:51], s67 offen offset:0  lds\n"
"v_mfma_f32_16x16x32_bf16 v[156:159], a[68:71], a[124:127], 1.0\n"
"L_st518:\n"
"s_cbranch_execz L_st2664\n"
"v_mfma_f32_16x16x32_bf16 v[156:159], a[64:67], a[120:123], v[156:159]\n"
"s_add_u32 s67, s60, 15616\n"
"v_mfma_f32_16x16x32_bf16 v[160:163], a[64:67], a[128:131], 1.0\n"
"v_mfma_f32_16x16x32_bf16 v[160:163], a[68:71], a[132:135], v[160:163]\n"
"buffer_load_dwordx4 v6, s[48:51], s67 offen offset:1024  lds\n"
"v_mfma_f32_16x16x32_bf16 v[188:191], a[76:79], a[132:135], 1.0\n"
"v_mfma_f32_16x16x32_bf16 v[188:191], a[72:75], a[128:131], v[188:191]\n"
"s_add_u32 s67, s60, 30976\n"
"v_mfma_f32_16x16x32_bf16 v[192:195], a[72:75], a[136:139], 1.0\n"
"v_mfma_f32_16x16x32_bf16 v[192:195], a[76:79], a[140:143], v[192:195]\n"
"buffer_load_dwordx4 v6, s[48:51], s67 offen offset:2048  lds\n"
"v_mfma_f32_16x16x32_bf16 v[164:167], a[68:71], a[140:143], 1.0\n"
"v_mfma_f32_16x16x32_bf16 v[164:167], a[64:67], a[136:139], v[164:167]\n"
"s_add_u32 s67, s60, 46336\n"
"v_mfma_f32_16x16x32_bf16 v[168:171], a[64:67], a[144:147], 1.0\n"
"v_mfma_f32_16x16x32_bf16 v[168:171], a[68:71], a[148:151], v[168:171]\n"
"v_mfma_f32_16x16x32_bf16 v[196:199], a[76:79], a[148:151], 1.0\n"
"buffer_load_dwordx4 v6, s[48:51], s67 offen offset:3072  lds\n"
"v_mfma_f32_16x16x32_bf16 v[196:199], a[72:75], a[144:147], v[196:199]\n"
"v_mfma_f32_16x16x32_bf16 a[20:23], a[72:75], a[152:155], 1.0\n"
"v_mfma_f32_16x16x32_bf16 a[20:23], a[76:79], a[156:159], a[20:23]\n"
"v_mfma_f32_16x16x32_bf16 a[16:19], a[68:71], a[156:159], 1.0\n"
"v_mfma_f32_16x16x32_bf16 a[16:19], a[64:67], a[152:155], a[16:19]\n"
"s_waitcnt vmcnt(21)\n"
"s_barrier\n"
"v_mfma_f32_16x16x32_bf16 v[200:203], a[80:83], a[96:99], 1.0\n"
"s_waitcnt vmcnt(12)\n"
"ds_read_b64_tr_b16 a[32:33], v9 offset:32768\n"
"v_mfma_f32_16x16x32_bf16 v[200:203], a[84:87], a[100:103], v[200:203]\n"
"s_add_u32 m0, s64, 8192\n"
"ds_read_b64_tr_b16 a[34:35], v9 offset:33280\n"
"v_mfma_f32_16x16x32_bf16 v[228:231], a[92:95], a[100:103], 1.0\n"
"ds_read_b128 a[100:103], v13 offset:16384\n"
"v_mfma_f32_16x16x32_bf16 v[228:231], a[88:91], a[96:99], v[228:231]\n"
"ds_read_b128 a[96:99], v12 offset:16384\n"
"v_mfma_f32_16x16x32_bf16 v[232:235], a[88:91], a[104:107], 1.0\n"
"ds_read_b64_tr_b16 a[36:37], v9 offset:36864\n"
"v_mfma_f32_16x16x32_bf16 v[232:235], a[92:95], a[108:111], v[232:235]\n"
"ds_read_b64_tr_b16 a[38:39], v9 offset:37376\n"
"v_mfma_f32_16x16x32_bf16 v[204:207], a[84:87], a[108:111], 1.0\n"
"ds_read_b128 a[108:111], v13 offset:18432\n"
"v_mfma_f32_16x16x32_bf16 v[204:207], a[80:83], a[104:107], v[204:207]\n"
"ds_read_b128 a[104:107], v12 offset:18432\n"
"ds_read_b64_tr_b16 a[40:41], v9 offset:32776\n"
"v_mfma_f32_16x16x32_bf16 v[208:211], a[80:83], a[112:115], 1.0\n"
"ds_read_b64_tr_b16 a[42:43], v9 offset:33288\n"
"v_mfma_f32_16x16x32_bf16 v[208:211], a[84:87], a[116:119], v[208:211]\n"
"ds_read_b64_tr_b16 a[44:45], v9 offset:36872\n"
"v_mfma_f32_16x16x32_bf16 v[236:239], a[92:95], a[116:119], 1.0\n"
"ds_read_b128 a[116:119], v13 offset:20480\n"
"v_mfma_f32_16x16x32_bf16 v[236:239], a[88:91], a[112:115], v[236:239]\n"
"ds_read_b128 a[112:115], v12 offset:20480\n"
"v_mfma_f32_16x16x32_bf16 v[240:243], a[88:91], a[120:123], 1.0\n"
"ds_read_b64_tr_b16 a[46:47], v9 offset:37384\n"
"v_mfma_f32_16x16x32_bf16 v[240:243], a[92:95], a[124:127], v[240:243]\n"
"ds_read_b64_tr_b16 a[48:49], v15 offset:32768\n"
"v_mfma_f32_16x16x32_bf16 v[212:215], a[84:87], a[124:127], 1.0\n"
"s_add_u32 s67, s75, 262144\n"
"ds_read_b128 a[124:127], v13 offset:22528\n"
"v_mfma_f32_16x16x32_bf16 v[212:215], a[80:83], a[120:123], v[212:215]\n"
"ds_read_b128 a[120:123], v12 offset:22528\n"
"v_mfma_f32_16x16x32_bf16 v[216:219], a[80:83], a[128:131], 1.0\n"
"ds_read_b64_tr_b16 a[50:51], v15 offset:33280\n"
"v_mfma_f32_16x16x32_bf16 v[216:219], a[84:87], a[132:135], v[216:219]\n"
"ds_read_b64_tr_b16 a[52:53], v15 offset:36864\n"
"v_mfma_f32_16x16x32_bf16 v[244:247], a[92:95], a[132:135], 1.0\n"
"ds_read_b128 a[132:135], v13 offset:24576\n"
"v_mfma_f32_16x16x32_bf16 v[244:247], a[88:91], a[128:131], v[244:247]\n"
"ds_read_b128 a[128:131], v12 offset:24576\n"
"v_mfma_f32_16x16x32_bf16 v[248:251], a[88:91], a[136:139], 1.0\n"
"ds_read_b64_tr_b16 a[54:55], v15 offset:37376\n"
"v_mfma_f32_16x16x32_bf16 v[248:251], a[92:95], a[140:143], v[248:251]\n"
"ds_read_b64_tr_b16 a[56:57], v15 offset:32776\n"
"v_mfma_f32_16x16x32_bf16 v[220:223], a[84:87], a[140:143], 1.0\n"
"ds_read_b128 a[140:143], v13 offset:26624\n"
"v_mfma_f32_16x16x32_bf16 v[220:223], a[80:83], a[136:139], v[220:223]\n"
"ds_read_b128 a[136:139], v12 offset:26624\n"
"v_mfma_f32_16x16x32_bf16 v[224:227], a[80:83], a[144:147], 1.0\n"
"ds_read_b64_tr_b16 a[58:59], v15 offset:33288\n"
"v_mfma_f32_16x16x32_bf16 v[224:227], a[84:87], a[148:151], v[224:227]\n"
"ds_read_b64_tr_b16 a[60:61], v15 offset:36872\n"
"v_mfma_f32_16x16x32_bf16 v[252:255], a[92:95], a[148:151], 1.0\n"
"ds_read_b128 a[148:151], v13 offset:28672\n"
"v_mfma_f32_16x16x32_bf16 v[252:255], a[88:91], a[144:147], v[252:255]\n"
"ds_read_b128 a[144:147], v12 offset:28672\n"
"ds_read_b64_tr_b16 a[62:63], v15 offset:37384\n"
"v_mfma_f32_16x16x32_bf16 a[28:31], a[88:91], a[152:155], 1.0\n"
"v_mfma_f32_16x16x32_bf16 a[28:31], a[92:95], a[156:159], a[28:31]\n"
"v_mfma_f32_16x16x32_bf16 a[24:27], a[84:87], a[156:159], 1.0\n"
"ds_read_b128 a[156:159], v13 offset:30720\n"
"v_mfma_f32_16x16x32_bf16 a[24:27], a[80:83], a[152:155], a[24:27]\n"
"s_waitcnt lgkmcnt(15)\n"
"ds_read_b128 a[152:155], v12 offset:30720\n"
"v_mfma_f32_16x16x32_bf16 v[32:35], a[32:35], a[96:99], v[32:35]\n"
"buffer_load_dwordx4 v11, s[52:55], s67 offen offset:0  lds\n"
"v_mfma_f32_16x16x32_bf16 v[32:35], a[36:39], a[100:103], v[32:35]\n"
"s_waitcnt lgkmcnt(2)\n"
"v_mfma_f32_16x16x32_bf16 v[60:63], a[44:47], a[100:103], v[60:63]\n"
"s_add_u32 s67, s75, 277504\n"
"v_mfma_f32_16x16x32_bf16 v[60:63], a[40:43], a[96:99], v[60:63]\n"
"v_mfma_f32_16x16x32_bf16 v[64:67], a[40:43], a[104:107], v[64:67]\n"
"v_mfma_f32_16x16x32_bf16 v[64:67], a[44:47], a[108:111], v[64:67]\n"
"buffer_load_dwordx4 v11, s[52:55], s67 offen offset:1024  lds\n"
"v_mfma_f32_16x16x32_bf16 v[36:39], a[36:39], a[108:111], v[36:39]\n"
"v_mfma_f32_16x16x32_bf16 v[36:39], a[32:35], a[104:107], v[36:39]\n"
"s_add_u32 s67, s75, 292864\n"
"v_mfma_f32_16x16x32_bf16 v[40:43], a[32:35], a[112:115], v[40:43]\n"
"v_mfma_f32_16x16x32_bf16 v[40:43], a[36:39], a[116:119], v[40:43]\n"
"v_mfma_f32_16x16x32_bf16 v[68:71], a[44:47], a[116:119], v[68:71]\n"
"buffer_load_dwordx4 v11, s[52:55], s67 offen offset:2048  lds\n"
"v_mfma_f32_16x16x32_bf16 v[68:71], a[40:43], a[112:115], v[68:71]\n"
"v_mfma_f32_16x16x32_bf16 v[72:75], a[40:43], a[120:123], v[72:75]\n"
"s_add_u32 s67, s75, 308224\n"
"v_mfma_f32_16x16x32_bf16 v[72:75], a[44:47], a[124:127], v[72:75]\n"
"v_mfma_f32_16x16x32_bf16 v[44:47], a[36:39], a[124:127], v[44:47]\n"
"v_mov_b32 v16, -1\n"
"v_mfma_f32_16x16x32_bf16 v[44:47], a[32:35], a[120:123], v[44:47]\n"
"buffer_load_dwordx4 v11, s[52:55], s67 offen offset:3072  lds\n"
"v_mfma_f32_16x16x32_bf16 v[48:51], a[32:35], a[128:131], v[48:51]\n"
"s_add_u32 m0, s64, 12288\n"
"v_mfma_f32_16x16x32_bf16 v[48:51], a[36:39], a[132:135], v[48:51]\n"
"v_mov_b32 v17, -1\n"
"v_mfma_f32_16x16x32_bf16 v[76:79], a[44:47], a[132:135], v[76:79]\n"
"s_waitcnt vmcnt(8)\n"
"ds_read_b64_tr_b16 a[64:65], v9 offset:0\n"
"v_mfma_f32_16x16x32_bf16 v[76:79], a[40:43], a[128:131], v[76:79]\n"
"s_add_u32 s67, s75, 327680\n"
"ds_read_b64_tr_b16 a[66:67], v9 offset:512\n"
"v_mfma_f32_16x16x32_bf16 v[80:83], a[40:43], a[136:139], v[80:83]\n"
"ds_read_b64_tr_b16 a[68:69], v9 offset:4096\n"
"buffer_load_dwordx4 v11, s[52:55], s67 offen offset:0  lds\n"
"v_mfma_f32_16x16x32_bf16 v[80:83], a[44:47], a[140:143], v[80:83]\n"
"ds_read_b64_tr_b16 a[70:71], v9 offset:4608\n"
"v_mfma_f32_16x16x32_bf16 v[52:55], a[36:39], a[140:143], v[52:55]\n"
"s_add_u32 s67, s75, 343040\n"
"ds_read_b64_tr_b16 a[72:73], v9 offset:8\n"
"v_mfma_f32_16x16x32_bf16 v[52:55], a[32:35], a[136:139], v[52:55]\n"
"s_waitcnt lgkmcnt(5)\n"
"ds_read_b64_tr_b16 a[74:75], v9 offset:520\n"
"v_mfma_f32_16x16x32_bf16 v[56:59], a[32:35], a[144:147], v[56:59]\n"
"ds_read_b64_tr_b16 a[76:77], v9 offset:4104\n"
"buffer_load_dwordx4 v11, s[52:55], s67 offen offset:1024  lds\n"
"v_mfma_f32_16x16x32_bf16 v[56:59], a[36:39], a[148:151], v[56:59]\n"
"s_add_u32 s67, s75, 358400\n"
"ds_read_b64_tr_b16 a[78:79], v9 offset:4616\n"
"v_mfma_f32_16x16x32_bf16 v[84:87], a[44:47], a[148:151], v[84:87]\n"
"s_add_u32 s69, %11, 8192\n"
"v_lshrrev_b32 v18, 1, v0\n"
"v_mfma_f32_16x16x32_bf16 v[84:87], a[40:43], a[144:147], v[84:87]\n"
"ds_read_b64_tr_b16 a[80:81], v15 offset:0\n"
"v_mfma_f32_16x16x32_bf16 a[4:7], a[40:43], a[152:155], a[4:7]\n"
"ds_read_b64_tr_b16 a[82:83], v15 offset:512\n"
"buffer_load_dwordx4 v11, s[52:55], s67 offen offset:2048  lds\n"
"v_mfma_f32_16x16x32_bf16 a[4:7], a[44:47], a[156:159], a[4:7]\n"
"ds_read_b64_tr_b16 a[84:85], v15 offset:4096\n"
"v_mfma_f32_16x16x32_bf16 a[0:3], a[36:39], a[156:159], a[0:3]\n"
"s_add_u32 s67, s75, 373760\n"
"ds_read_b64_tr_b16 a[86:87], v15 offset:4608\n"
"v_mfma_f32_16x16x32_bf16 a[0:3], a[32:35], a[152:155], a[0:3]\n"
"ds_read_b64_tr_b16 a[88:89], v15 offset:8\n"
"v_mfma_f32_16x16x32_bf16 v[88:91], a[48:51], a[96:99], v[88:91]\n"
"ds_read_b64_tr_b16 a[90:91], v15 offset:520\n"
"v_mfma_f32_16x16x32_bf16 v[88:91], a[52:55], a[100:103], v[88:91]\n"
"ds_read_b64_tr_b16 a[92:93], v15 offset:4104\n"
"buffer_load_dwordx4 v11, s[52:55], s67 offen offset:3072  lds\n"
"v_mfma_f32_16x16x32_bf16 v[116:119], a[60:63], a[100:103], v[116:119]\n"
"ds_read_b64_tr_b16 a[94:95], v15 offset:4616\n"
"v_mfma_f32_16x16x32_bf16 v[116:119], a[56:59], a[96:99], v[116:119]\n"
"s_add_u32 m0, s64, 0\n"
"v_mfma_f32_16x16x32_bf16 v[120:123], a[56:59], a[104:107], v[120:123]\n"
"v_mfma_f32_16x16x32_bf16 v[120:123], a[60:63], a[108:111], v[120:123]\n"
"v_mfma_f32_16x16x32_bf16 v[92:95], a[52:55], a[108:111], v[92:95]\n"
"s_add_u32 s67, s75, 262272\n"
"v_mfma_f32_16x16x32_bf16 v[92:95], a[48:51], a[104:107], v[92:95]\n"
"v_mfma_f32_16x16x32_bf16 v[96:99], a[48:51], a[112:115], v[96:99]\n"
"s_waitcnt lgkmcnt(0)\n"
"v_mfma_f32_16x16x32_bf16 v[96:99], a[52:55], a[116:119], v[96:99]\n"
"buffer_load_dwordx4 v11, s[52:55], s67 offen offset:0  lds\n"
"v_mfma_f32_16x16x32_bf16 v[124:127], a[60:63], a[116:119], v[124:127]\n"
"v_mfma_f32_16x16x32_bf16 v[124:127], a[56:59], a[112:115], v[124:127]\n"
"s_add_u32 s67, s75, 277632\n"
"v_mfma_f32_16x16x32_bf16 v[128:131], a[56:59], a[120:123], v[128:131]\n"
"v_mfma_f32_16x16x32_bf16 v[128:131], a[60:63], a[124:127], v[128:131]\n"
"v_mfma_f32_16x16x32_bf16 v[100:103], a[52:55], a[124:127], v[100:103]\n"
"buffer_load_dwordx4 v11, s[52:55], s67 offen offset:1024  lds\n"
"v_mfma_f32_16x16x32_bf16 v[100:103], a[48:51], a[120:123], v[100:103]\n"
"v_mfma_f32_16x16x32_bf16 v[104:107], a[48:51], a[128:131], v[104:107]\n"
"s_add_u32 s67, s75, 292992\n"
"v_mfma_f32_16x16x32_bf16 v[104:107], a[52:55], a[132:135], v[104:107]\n"
"v_mfma_f32_16x16x32_bf16 v[132:135], a[60:63], a[132:135], v[132:135]\n"
"buffer_load_dwordx4 v11, s[52:55], s67 offen offset:2048  lds\n"
"v_mfma_f32_16x16x32_bf16 v[132:135], a[56:59], a[128:131], v[132:135]\n"
"v_mfma_f32_16x16x32_bf16 v[136:139], a[56:59], a[136:139], v[136:139]\n"
"s_add_u32 s67, s75, 308352\n"
"v_mfma_f32_16x16x32_bf16 v[136:139], a[60:63], a[140:143], v[136:139]\n"
"v_mfma_f32_16x16x32_bf16 v[108:111], a[52:55], a[140:143], v[108:111]\n"
"v_mfma_f32_16x16x32_bf16 v[108:111], a[48:51], a[136:139], v[108:111]\n"
"buffer_load_dwordx4 v11, s[52:55], s67 offen offset:3072  lds\n"
"v_mfma_f32_16x16x32_bf16 v[112:115], a[48:51], a[144:147], v[112:115]\n"
"v_mfma_f32_16x16x32_bf16 v[112:115], a[52:55], a[148:151], v[112:115]\n"
"s_add_u32 m0, s64, 4096\n"
"v_mfma_f32_16x16x32_bf16 v[140:143], a[60:63], a[148:151], v[140:143]\n"
"v_mfma_f32_16x16x32_bf16 v[140:143], a[56:59], a[144:147], v[140:143]\n"
"s_add_u32 s67, s75, 327808\n"
"v_mfma_f32_16x16x32_bf16 a[12:15], a[56:59], a[152:155], a[12:15]\n"
"v_mfma_f32_16x16x32_bf16 a[12:15], a[60:63], a[156:159], a[12:15]\n"
"buffer_load_dwordx4 v11, s[52:55], s67 offen offset:0  lds\n"
"v_mfma_f32_16x16x32_bf16 a[8:11], a[52:55], a[156:159], a[8:11]\n"
"v_mfma_f32_16x16x32_bf16 a[8:11], a[48:51], a[152:155], a[8:11]\n"
"s_add_u32 s67, s75, 343168\n"
"v_mfma_f32_16x16x32_bf16 v[144:147], a[64:67], a[96:99], v[144:147]\n"
"v_mfma_f32_16x16x32_bf16 v[144:147], a[68:71], a[100:103], v[144:147]\n"
"buffer_load_dwordx4 v11, s[52:55], s67 offen offset:1024  lds\n"
"v_mfma_f32_16x16x32_bf16 v[172:175], a[76:79], a[100:103], v[172:175]\n"
"v_mfma_f32_16x16x32_bf16 v[172:175], a[72:75], a[96:99], v[172:175]\n"
"s_add_u32 s67, s75, 358528\n"
"v_mfma_f32_16x16x32_bf16 v[176:179], a[72:75], a[104:107], v[176:179]\n"
"v_mfma_f32_16x16x32_bf16 v[176:179], a[76:79], a[108:111], v[176:179]\n"
"buffer_load_dwordx4 v11, s[52:55], s67 offen offset:2048  lds\n"
"v_mfma_f32_16x16x32_bf16 v[148:151], a[68:71], a[108:111], v[148:151]\n"
"v_mfma_f32_16x16x32_bf16 v[148:151], a[64:67], a[104:107], v[148:151]\n"
"s_add_u32 s67, s75, 373888\n"
"v_mfma_f32_16x16x32_bf16 v[152:155], a[64:67], a[112:115], v[152:155]\n"
"s_sub_u32 s66, 81920, s73\n"
"v_mfma_f32_16x16x32_bf16 v[152:155], a[68:71], a[116:119], v[152:155]\n"
"buffer_load_dwordx4 v11, s[52:55], s67 offen offset:3072  lds\n"
"v_mfma_f32_16x16x32_bf16 v[180:183], a[76:79], a[116:119], v[180:183]\n"
"v_add_u32 v12, s66, v7\n"
"v_mfma_f32_16x16x32_bf16 v[180:183], a[72:75], a[112:115], v[180:183]\n"
"s_add_u32 m0, s72, 139264\n"
"v_mfma_f32_16x16x32_bf16 v[184:187], a[72:75], a[120:123], v[184:187]\n"
"v_add_u32 v13, s66, v8\n"
"v_mfma_f32_16x16x32_bf16 v[184:187], a[76:79], a[124:127], v[184:187]\n"
"s_add_u32 s67, s60, 384\n"
"v_mfma_f32_16x16x32_bf16 v[156:159], a[68:71], a[124:127], v[156:159]\n"
"v_mfma_f32_16x16x32_bf16 v[156:159], a[64:67], a[120:123], v[156:159]\n"
"buffer_load_dwordx4 v6, s[48:51], s67 offen offset:0  lds\n"
"v_mfma_f32_16x16x32_bf16 v[160:163], a[64:67], a[128:131], v[160:163]\n"
"v_mfma_f32_16x16x32_bf16 v[160:163], a[68:71], a[132:135], v[160:163]\n"
"s_add_u32 s67, s60, 15744\n"
"v_mfma_f32_16x16x32_bf16 v[188:191], a[76:79], a[132:135], v[188:191]\n"
"v_mfma_f32_16x16x32_bf16 v[188:191], a[72:75], a[128:131], v[188:191]\n"
"buffer_load_dwordx4 v6, s[48:51], s67 offen offset:1024  lds\n"
"v_mfma_f32_16x16x32_bf16 v[192:195], a[72:75], a[136:139], v[192:195]\n"
"v_mfma_f32_16x16x32_bf16 v[192:195], a[76:79], a[140:143], v[192:195]\n"
"s_add_u32 s67, s60, 31104\n"
"v_mfma_f32_16x16x32_bf16 v[164:167], a[68:71], a[140:143], v[164:167]\n"
"v_mfma_f32_16x16x32_bf16 v[164:167], a[64:67], a[136:139], v[164:167]\n"
"buffer_load_dwordx4 v6, s[48:51], s67 offen offset:2048  lds\n"
"v_mfma_f32_16x16x32_bf16 v[168:171], a[64:67], a[144:147], v[168:171]\n"
"v_mfma_f32_16x16x32_bf16 v[168:171], a[68:71], a[148:151], v[168:171]\n"
"s_add_u32 s67, s60, 46464\n"
"v_mfma_f32_16x16x32_bf16 v[196:199], a[76:79], a[148:151], v[196:199]\n"
"v_mfma_f32_16x16x32_bf16 v[196:199], a[72:75], a[144:147], v[196:199]\n"
"v_mfma_f32_16x16x32_bf16 a[20:23], a[72:75], a[152:155], a[20:23]\n"
"buffer_load_dwordx4 v6, s[48:51], s67 offen offset:3072  lds\n"
"v_mfma_f32_16x16x32_bf16 a[20:23], a[76:79], a[156:159], a[20:23]\n"
"s_add_u32 s70, s75, 655360\n"
"v_mfma_f32_16x16x32_bf16 a[16:19], a[68:71], a[156:159], a[16:19]\n"
"s_add_u32 s70, s70, s65\n"
"v_mfma_f32_16x16x32_bf16 a[16:19], a[64:67], a[152:155], a[16:19]\n"
"s_waitcnt vmcnt(20)\n"
"s_barrier\n"
"v_mfma_f32_16x16x32_bf16 v[200:203], a[80:83], a[96:99], v[200:203]\n"
"s_waitcnt vmcnt(12)\n"
"ds_read_b64_tr_b16 a[32:33], v9 offset:8192\n"
"v_mfma_f32_16x16x32_bf16 v[200:203], a[84:87], a[100:103], v[200:203]\n"
"ds_read_b64_tr_b16 a[34:35], v9 offset:8704\n"
"buffer_load_dword a252, v14, s[52:55], s70 offen\n"
"v_mfma_f32_16x16x32_bf16 v[228:231], a[92:95], a[100:103], v[228:231]\n"
"ds_read_b128 a[100:103], v13 offset:16384\n"
"v_mfma_f32_16x16x32_bf16 v[228:231], a[88:91], a[96:99], v[228:231]\n"
"ds_read_b128 a[96:99], v12 offset:16384\n"
"v_mfma_f32_16x16x32_bf16 v[232:235], a[88:91], a[104:107], v[232:235]\n"
"ds_read_b64_tr_b16 a[36:37], v9 offset:12288\n"
"v_mfma_f32_16x16x32_bf16 v[232:235], a[92:95], a[108:111], v[232:235]\n"
"ds_read_b64_tr_b16 a[38:39], v9 offset:12800\n"
"v_mfma_f32_16x16x32_bf16 v[204:207], a[84:87], a[108:111], v[204:207]\n"
"ds_read_b128 a[108:111], v13 offset:18432\n"
"v_mfma_f32_16x16x32_bf16 v[204:207], a[80:83], a[104:107], v[204:207]\n"
"ds_read_b128 a[104:107], v12 offset:18432\n"
"s_add_u32 m0, s64, 8192\n"
"ds_read_b64_tr_b16 a[40:41], v9 offset:8200\n"
"v_mfma_f32_16x16x32_bf16 v[208:211], a[80:83], a[112:115], v[208:211]\n"
"ds_read_b64_tr_b16 a[42:43], v9 offset:8712\n"
"v_mfma_f32_16x16x32_bf16 v[208:211], a[84:87], a[116:119], v[208:211]\n"
"ds_read_b64_tr_b16 a[44:45], v9 offset:12296\n"
"v_mfma_f32_16x16x32_bf16 v[236:239], a[92:95], a[116:119], v[236:239]\n"
"ds_read_b128 a[116:119], v13 offset:20480\n"
"v_mfma_f32_16x16x32_bf16 v[236:239], a[88:91], a[112:115], v[236:239]\n"
"ds_read_b128 a[112:115], v12 offset:20480\n"
"v_mfma_f32_16x16x32_bf16 v[240:243], a[88:91], a[120:123], v[240:243]\n"
"ds_read_b64_tr_b16 a[46:47], v9 offset:12808\n"
"v_mfma_f32_16x16x32_bf16 v[240:243], a[92:95], a[124:127], v[240:243]\n"
"ds_read_b64_tr_b16 a[48:49], v15 offset:8192\n"
"v_mfma_f32_16x16x32_bf16 v[212:215], a[84:87], a[124:127], v[212:215]\n"
"ds_read_b128 a[124:127], v13 offset:22528\n"
"v_mfma_f32_16x16x32_bf16 v[212:215], a[80:83], a[120:123], v[212:215]\n"
"ds_read_b128 a[120:123], v12 offset:22528\n"
"v_mfma_f32_16x16x32_bf16 v[216:219], a[80:83], a[128:131], v[216:219]\n"
"ds_read_b64_tr_b16 a[50:51], v15 offset:8704\n"
"v_mfma_f32_16x16x32_bf16 v[216:219], a[84:87], a[132:135], v[216:219]\n"
"ds_read_b64_tr_b16 a[52:53], v15 offset:12288\n"
"v_mfma_f32_16x16x32_bf16 v[244:247], a[92:95], a[132:135], v[244:247]\n"
"ds_read_b128 a[132:135], v13 offset:24576\n"
"v_mfma_f32_16x16x32_bf16 v[244:247], a[88:91], a[128:131], v[244:247]\n"
"s_add_u32 s67, s75, 393216\n"
"ds_read_b128 a[128:131], v12 offset:24576\n"
"v_mfma_f32_16x16x32_bf16 v[248:251], a[88:91], a[136:139], v[248:251]\n"
"ds_read_b64_tr_b16 a[54:55], v15 offset:12800\n"
"v_mfma_f32_16x16x32_bf16 v[248:251], a[92:95], a[140:143], v[248:251]\n"
"s_add_u32 s66, s78, 1\n"
"ds_read_b64_tr_b16 a[56:57], v15 offset:8200\n"
"v_mfma_f32_16x16x32_bf16 v[220:223], a[84:87], a[140:143], v[220:223]\n"
"ds_read_b128 a[140:143], v13 offset:26624\n"
"v_mfma_f32_16x16x32_bf16 v[220:223], a[80:83], a[136:139], v[220:223]\n"
"ds_read_b128 a[136:139], v12 offset:26624\n"
"v_mov_b32 v28, s66\n"
"v_mfma_f32_16x16x32_bf16 v[224:227], a[80:83], a[144:147], v[224:227]\n"
"ds_read_b64_tr_b16 a[58:59], v15 offset:8712\n"
"v_mov_b32 v29, s79\n"
"v_mfma_f32_16x16x32_bf16 v[224:227], a[84:87], a[148:151], v[224:227]\n"
"ds_read_b64_tr_b16 a[60:61], v15 offset:12296\n"
"v_mfma_f32_16x16x32_bf16 v[252:255], a[92:95], a[148:151], v[252:255]\n"
"buffer_store_dwordx2 v[28:29], off, s[56:59], %24 sc1\n"
"ds_read_b128 a[148:151], v13 offset:28672\n"
"v_mfma_f32_16x16x32_bf16 v[252:255], a[88:91], a[144:147], v[252:255]\n"
"ds_read_b128 a[144:147], v12 offset:28672\n"
"ds_read_b64_tr_b16 a[62:63], v15 offset:12808\n"
"v_mfma_f32_16x16x32_bf16 a[28:31], a[88:91], a[152:155], a[28:31]\n"
"v_mfma_f32_16x16x32_bf16 a[28:31], a[92:95], a[156:159], a[28:31]\n"
"v_mfma_f32_16x16x32_bf16 a[24:27], a[84:87], a[156:159], a[24:27]\n"
"ds_read_b128 a[156:159], v13 offset:30720\n"
"v_mfma_f32_16x16x32_bf16 a[24:27], a[80:83], a[152:155], a[24:27]\n"
"s_waitcnt lgkmcnt(15)\n"
"ds_read_b128 a[152:155], v12 offset:30720\n"
"v_mfma_f32_16x16x32_bf16 v[32:35], a[32:35], a[96:99], v[32:35]\n"
"v_mfma_f32_16x16x32_bf16 v[32:35], a[36:39], a[100:103], v[32:35]\n"
"s_waitcnt lgkmcnt(2)\n"
"v_mfma_f32_16x16x32_bf16 v[60:63], a[44:47], a[100:103], v[60:63]\n"
"buffer_load_dwordx4 v11, s[52:55], s67 offen offset:0  lds\n"
"v_mfma_f32_16x16x32_bf16 v[60:63], a[40:43], a[96:99], v[60:63]\n"
"v_mfma_f32_16x16x32_bf16 v[64:67], a[40:43], a[104:107], v[64:67]\n"
"s_add_u32 s67, s75, 408576\n"
"v_mfma_f32_16x16x32_bf16 v[64:67], a[44:47], a[108:111], v[64:67]\n"
"v_mfma_f32_16x16x32_bf16 v[36:39], a[36:39], a[108:111], v[36:39]\n"
"v_mfma_f32_16x16x32_bf16 v[36:39], a[32:35], a[104:107], v[36:39]\n"
"buffer_load_dwordx4 v11, s[52:55], s67 offen offset:1024  lds\n"
"v_mfma_f32_16x16x32_bf16 v[40:43], a[32:35], a[112:115], v[40:43]\n"
"v_mfma_f32_16x16x32_bf16 v[40:43], a[36:39], a[116:119], v[40:43]\n"
"s_add_u32 s67, s75, 423936\n"
"v_mfma_f32_16x16x32_bf16 v[68:71], a[44:47], a[116:119], v[68:71]\n"
"v_mfma_f32_16x16x32_bf16 v[68:71], a[40:43], a[112:115], v[68:71]\n"
"v_mfma_f32_16x16x32_bf16 v[72:75], a[40:43], a[120:123], v[72:75]\n"
"buffer_load_dwordx4 v11, s[52:55], s67 offen offset:2048  lds\n"
"v_mfma_f32_16x16x32_bf16 v[72:75], a[44:47], a[124:127], v[72:75]\n"
"v_mfma_f32_16x16x32_bf16 v[44:47], a[36:39], a[124:127], v[44:47]\n"
"s_add_u32 s67, s75, 439296\n"
"v_mfma_f32_16x16x32_bf16 v[44:47], a[32:35], a[120:123], v[44:47]\n"
"v_mfma_f32_16x16x32_bf16 v[48:51], a[32:35], a[128:131], v[48:51]\n"
"buffer_load_dwordx4 v11, s[52:55], s67 offen offset:3072  lds\n"
"v_mfma_f32_16x16x32_bf16 v[48:51], a[36:39], a[132:135], v[48:51]\n"
"s_add_u32 m0, s64, 12288\n"
"v_mfma_f32_16x16x32_bf16 v[76:79], a[44:47], a[132:135], v[76:79]\n"
"s_waitcnt vmcnt(10)\n"
"ds_read_b64_tr_b16 a[64:65], v9 offset:0\n"
"v_mfma_f32_16x16x32_bf16 v[76:79], a[40:43], a[128:131], v[76:79]\n"
"ds_read_b64_tr_b16 a[66:67], v9 offset:512\n"
"v_mfma_f32_16x16x32_bf16 v[80:83], a[40:43], a[136:139], v[80:83]\n"
"s_add_u32 s67, s75, 458752\n"
"ds_read_b64_tr_b16 a[68:69], v9 offset:4096\n"
"v_mfma_f32_16x16x32_bf16 v[80:83], a[44:47], a[140:143], v[80:83]\n"
"ds_read_b64_tr_b16 a[70:71], v9 offset:4608\n"
"v_mfma_f32_16x16x32_bf16 v[52:55], a[36:39], a[140:143], v[52:55]\n"
"ds_read_b64_tr_b16 a[72:73], v9 offset:8\n"
"buffer_load_dwordx4 v11, s[52:55], s67 offen offset:0  lds\n"
"v_mfma_f32_16x16x32_bf16 v[52:55], a[32:35], a[136:139], v[52:55]\n"
"s_waitcnt lgkmcnt(5)\n"
"ds_read_b64_tr_b16 a[74:75], v9 offset:520\n"
"v_mfma_f32_16x16x32_bf16 v[56:59], a[32:35], a[144:147], v[56:59]\n"
"s_add_u32 s67, s75, 474112\n"
"ds_read_b64_tr_b16 a[76:77], v9 offset:4104\n"
"v_mfma_f32_16x16x32_bf16 v[56:59], a[36:39], a[148:151], v[56:59]\n"
"ds_read_b64_tr_b16 a[78:79], v9 offset:4616\n"
"v_mfma_f32_16x16x32_bf16 v[84:87], a[44:47], a[148:151], v[84:87]\n"
"buffer_load_dwordx4 v11, s[52:55], s67 offen offset:1024  lds\n"
"v_mfma_f32_16x16x32_bf16 v[84:87], a[40:43], a[144:147], v[84:87]\n"
"ds_read_b64_tr_b16 a[80:81], v15 offset:0\n"
"v_mfma_f32_16x16x32_bf16 a[4:7], a[40:43], a[152:155], a[4:7]\n"
"s_add_u32 s67, s75, 489472\n"
"ds_read_b64_tr_b16 a[82:83], v15 offset:512\n"
"v_mfma_f32_16x16x32_bf16 a[4:7], a[44:47], a[156:159], a[4:7]\n"
"ds_read_b64_tr_b16 a[84:85], v15 offset:4096\n"
"v_mfma_f32_16x16x32_bf16 a[0:3], a[36:39], a[156:159], a[0:3]\n"
"ds_read_b64_tr_b16 a[86:87], v15 offset:4608\n"
"buffer_load_dwordx4 v11, s[52:55], s67 offen offset:2048  lds\n"
"v_mfma_f32_16x16x32_bf16 a[0:3], a[32:35], a[152:155], a[0:3]\n"
"ds_read_b64_tr_b16 a[88:89], v15 offset:8\n"
"v_mfma_f32_16x16x32_bf16 v[88:91], a[48:51], a[96:99], v[88:91]\n"
"s_add_u32 s67, s75, 504832\n"
"ds_read_b64_tr_b16 a[90:91], v15 offset:520\n"
"v_mfma_f32_16x16x32_bf16 v[88:91], a[52:55], a[100:103], v[88:91]\n"
"ds_read_b64_tr_b16 a[92:93], v15 offset:4104\n"
"v_mfma_f32_16x16x32_bf16 v[116:119], a[60:63], a[100:103], v[116:119]\n"
"ds_read_b64_tr_b16 a[94:95], v15 offset:4616\n"
"v_mfma_f32_16x16x32_bf16 v[116:119], a[56:59], a[96:99], v[116:119]\n"
"buffer_load_dwordx4 v11, s[52:55], s67 offen offset:3072  lds\n"
"v_mfma_f32_16x16x32_bf16 v[120:123], a[56:59], a[104:107], v[120:123]\n"
"v_mfma_f32_16x16x32_bf16 v[120:123], a[60:63], a[108:111], v[120:123]\n"
"s_add_u32 m0, s64, 0\n"
"v_mfma_f32_16x16x32_bf16 v[92:95], a[52:55], a[108:111], v[92:95]\n"
"v_mfma_f32_16x16x32_bf16 v[92:95], a[48:51], a[104:107], v[92:95]\n"
"s_add_u32 s67, s75, 393344\n"
"v_mfma_f32_16x16x32_bf16 v[96:99], a[48:51], a[112:115], v[96:99]\n"
"s_waitcnt lgkmcnt(0)\n"
"v_mfma_f32_16x16x32_bf16 v[96:99], a[52:55], a[116:119], v[96:99]\n"
"buffer_load_dwordx4 v11, s[52:55], s67 offen offset:0  lds\n"
"v_mfma_f32_16x16x32_bf16 v[124:127], a[60:63], a[116:119], v[124:127]\n"
"v_mfma_f32_16x16x32_bf16 v[124:127], a[56:59], a[112:115], v[124:127]\n"
"s_add_u32 s67, s75, 408704\n"
"v_mfma_f32_16x16x32_bf16 v[128:131], a[56:59], a[120:123], v[128:131]\n"
"v_mfma_f32_16x16x32_bf16 v[128:131], a[60:63], a[124:127], v[128:131]\n"
"v_mfma_f32_16x16x32_bf16 v[100:103], a[52:55], a[124:127], v[100:103]\n"
"buffer_load_dwordx4 v11, s[52:55], s67 offen offset:1024  lds\n"
"v_mfma_f32_16x16x32_bf16 v[100:103], a[48:51], a[120:123], v[100:103]\n"
"v_mfma_f32_16x16x32_bf16 v[104:107], a[48:51], a[128:131], v[104:107]\n"
"s_add_u32 s67, s75, 424064\n"
"v_mfma_f32_16x16x32_bf16 v[104:107], a[52:55], a[132:135], v[104:107]\n"
"v_mfma_f32_16x16x32_bf16 v[132:135], a[60:63], a[132:135], v[132:135]\n"
"v_mfma_f32_16x16x32_bf16 v[132:135], a[56:59], a[128:131], v[132:135]\n"
"buffer_load_dwordx4 v11, s[52:55], s67 offen offset:2048  lds\n"
"v_mfma_f32_16x16x32_bf16 v[136:139], a[56:59], a[136:139], v[136:139]\n"
"v_mfma_f32_16x16x32_bf16 v[136:139], a[60:63], a[140:143], v[136:139]\n"
"s_add_u32 s67, s75, 439424\n"
"v_mfma_f32_16x16x32_bf16 v[108:111], a[52:55], a[140:143], v[108:111]\n"
"v_mfma_f32_16x16x32_bf16 v[108:111], a[48:51], a[136:139], v[108:111]\n"
"buffer_load_dwordx4 v11, s[52:55], s67 offen offset:3072  lds\n"
"v_mfma_f32_16x16x32_bf16 v[112:115], a[48:51], a[144:147], v[112:115]\n"
"v_mfma_f32_16x16x32_bf16 v[112:115], a[52:55], a[148:151], v[112:115]\n"
"s_add_u32 m0, s64, 4096\n"
"v_mfma_f32_16x16x32_bf16 v[140:143], a[60:63], a[148:151], v[140:143]\n"
"v_mfma_f32_16x16x32_bf16 v[140:143], a[56:59], a[144:147], v[140:143]\n"
"s_add_u32 s67, s75, 458880\n"
"v_mfma_f32_16x16x32_bf16 a[12:15], a[56:59], a[152:155], a[12:15]\n"
"v_mfma_f32_16x16x32_bf16 a[12:15], a[60:63], a[156:159], a[12:15]\n"
"buffer_load_dwordx4 v11, s[52:55], s67 offen offset:0  lds\n"
"v_mfma_f32_16x16x32_bf16 a[8:11], a[52:55], a[156:159], a[8:11]\n"
"v_mfma_f32_16x16x32_bf16 a[8:11], a[48:51], a[152:155], a[8:11]\n"
"s_add_u32 s67, s75, 474240\n"
"v_mfma_f32_16x16x32_bf16 v[144:147], a[64:67], a[96:99], v[144:147]\n"
"v_mfma_f32_16x16x32_bf16 v[144:147], a[68:71], a[100:103], v[144:147]\n"
"buffer_load_dwordx4 v11, s[52:55], s67 offen offset:1024  lds\n"
"v_mfma_f32_16x16x32_bf16 v[172:175], a[76:79], a[100:103], v[172:175]\n"
"v_mfma_f32_16x16x32_bf16 v[172:175], a[72:75], a[96:99], v[172:175]\n"
"s_add_u32 s67, s75, 489600\n"
"v_mfma_f32_16x16x32_bf16 v[176:179], a[72:75], a[104:107], v[176:179]\n"
"v_mfma_f32_16x16x32_bf16 v[176:179], a[76:79], a[108:111], v[176:179]\n"
"buffer_load_dwordx4 v11, s[52:55], s67 offen offset:2048  lds\n"
"v_mfma_f32_16x16x32_bf16 v[148:151], a[68:71], a[108:111], v[148:151]\n"
"v_mfma_f32_16x16x32_bf16 v[148:151], a[64:67], a[104:107], v[148:151]\n"
"s_add_u32 s67, s75, 504960\n"
"v_mfma_f32_16x16x32_bf16 v[152:155], a[64:67], a[112:115], v[152:155]\n"
"s_sub_u32 s66, 122880, s73\n"
"v_mfma_f32_16x16x32_bf16 v[152:155], a[68:71], a[116:119], v[152:155]\n"
"v_mfma_f32_16x16x32_bf16 v[180:183], a[76:79], a[116:119], v[180:183]\n"
"v_add_u32 v12, s66, v7\n"
"v_mfma_f32_16x16x32_bf16 v[180:183], a[72:75], a[112:115], v[180:183]\n"
"buffer_load_dwordx4 v11, s[52:55], s67 offen offset:3072  lds\n"
"v_mfma_f32_16x16x32_bf16 v[184:187], a[72:75], a[120:123], v[184:187]\n"
"s_add_u32 m0, s72, 16384\n"
"v_add_u32 v13, s66, v8\n"
"v_mfma_f32_16x16x32_bf16 v[184:187], a[76:79], a[124:127], v[184:187]\n"
"v_mfma_f32_16x16x32_bf16 v[156:159], a[68:71], a[124:127], v[156:159]\n"
"s_add_u32 s67, s60, 512\n"
"v_mfma_f32_16x16x32_bf16 v[156:159], a[64:67], a[120:123], v[156:159]\n"
"buffer_load_dwordx4 v6, s[48:51], s67 offen offset:0  lds\n"
"v_mfma_f32_16x16x32_bf16 v[160:163], a[64:67], a[128:131], v[160:163]\n"
"v_mfma_f32_16x16x32_bf16 v[160:163], a[68:71], a[132:135], v[160:163]\n"
"s_add_u32 s67, s60, 15872\n"
"v_mfma_f32_16x16x32_bf16 v[188:191], a[76:79], a[132:135], v[188:191]\n"
"v_mfma_f32_16x16x32_bf16 v[188:191], a[72:75], a[128:131], v[188:191]\n"
"buffer_load_dwordx4 v6, s[48:51], s67 offen offset:1024  lds\n"
"v_mfma_f32_16x16x32_bf16 v[192:195], a[72:75], a[136:139], v[192:195]\n"
"v_mfma_f32_16x16x32_bf16 v[192:195], a[76:79], a[140:143], v[192:195]\n"
"s_add_u32 s67, s60, 31232\n"
"v_mfma_f32_16x16x32_bf16 v[164:167], a[68:71], a[140:143], v[164:167]\n"
"v_mfma_f32_16x16x32_bf16 v[164:167], a[64:67], a[136:139], v[164:167]\n"
"buffer_load_dwordx4 v6, s[48:51], s67 offen offset:2048  lds\n"
"v_mfma_f32_16x16x32_bf16 v[168:171], a[64:67], a[144:147], v[168:171]\n"
"v_mfma_f32_16x16x32_bf16 v[168:171], a[68:71], a[148:151], v[168:171]\n"
"s_add_u32 s67, s60, 46592\n"
"v_mfma_f32_16x16x32_bf16 v[196:199], a[76:79], a[148:151], v[196:199]\n"
"v_mfma_f32_16x16x32_bf16 v[196:199], a[72:75], a[144:147], v[196:199]\n"
"v_mfma_f32_16x16x32_bf16 a[20:23], a[72:75], a[152:155], a[20:23]\n"
"buffer_load_dwordx4 v6, s[48:51], s67 offen offset:3072  lds\n"
"v_mfma_f32_16x16x32_bf16 a[20:23], a[76:79], a[156:159], a[20:23]\n"
"v_mfma_f32_16x16x32_bf16 a[16:19], a[68:71], a[156:159], a[16:19]\n"
"s_add_u32 s70, s75, 786432\n"
"s_add_u32 s70, s70, s65\n"
"v_mfma_f32_16x16x32_bf16 a[16:19], a[64:67], a[152:155], a[16:19]\n"
"s_waitcnt vmcnt(22)\n"
"s_barrier\n"
"v_mfma_f32_16x16x32_bf16 v[200:203], a[80:83], a[96:99], v[200:203]\n"
"s_waitcnt vmcnt(12)\n"
"ds_read_b64_tr_b16 a[32:33], v9 offset:8192\n"
"v_mfma_f32_16x16x32_bf16 v[200:203], a[84:87], a[100:103], v[200:203]\n"
"ds_read_b64_tr_b16 a[34:35], v9 offset:8704\n"
"v_mfma_f32_16x16x32_bf16 v[228:231], a[92:95], a[100:103], v[228:231]\n"
"ds_read_b128 a[100:103], v13 offset:16384\n"
"buffer_load_dword a252, v14, s[52:55], s70 offen\n"
"v_mfma_f32_16x16x32_bf16 v[228:231], a[88:91], a[96:99], v[228:231]\n"
"ds_read_b128 a[96:99], v12 offset:16384\n"
"v_mfma_f32_16x16x32_bf16 v[232:235], a[88:91], a[104:107], v[232:235]\n"
"ds_read_b64_tr_b16 a[36:37], v9 offset:12288\n"
"v_mfma_f32_16x16x32_bf16 v[232:235], a[92:95], a[108:111], v[232:235]\n"
"ds_read_b64_tr_b16 a[38:39], v9 offset:12800\n"
"v_mfma_f32_16x16x32_bf16 v[204:207], a[84:87], a[108:111], v[204:207]\n"
"ds_read_b128 a[108:111], v13 offset:18432\n"
"v_mfma_f32_16x16x32_bf16 v[204:207], a[80:83], a[104:107], v[204:207]\n"
"ds_read_b128 a[104:107], v12 offset:18432\n"
"ds_read_b64_tr_b16 a[40:41], v9 offset:8200\n"
"v_mfma_f32_16x16x32_bf16 v[208:211], a[80:83], a[112:115], v[208:211]\n"
"s_add_u32 m0, s64, 8192\n"
"ds_read_b64_tr_b16 a[42:43], v9 offset:8712\n"
"v_mfma_f32_16x16x32_bf16 v[208:211], a[84:87], a[116:119], v[208:211]\n"
"ds_read_b64_tr_b16 a[44:45], v9 offset:12296\n"
"v_mfma_f32_16x16x32_bf16 v[236:239], a[92:95], a[116:119], v[236:239]\n"
"L_st1045:\n"
"s_cbranch_execz L_st3225\n"
"ds_read_b128 a[116:119], v13 offset:20480\n"
"v_mfma_f32_16x16x32_bf16 v[236:239], a[88:91], a[112:115], v[236:239]\n"
"ds_read_b128 a[112:115], v12 offset:20480\n"
"v_mfma_f32_16x16x32_bf16 v[240:243], a[88:91], a[120:123], v[240:243]\n"
"ds_read_b64_tr_b16 a[46:47], v9 offset:12808\n"
"v_mfma_f32_16x16x32_bf16 v[240:243], a[92:95], a[124:127], v[240:243]\n"
"ds_read_b64_tr_b16 a[48:49], v15 offset:8192\n"
"v_mfma_f32_16x16x32_bf16 v[212:215], a[84:87], a[124:127], v[212:215]\n"
"ds_read_b128 a[124:127], v13 offset:22528\n"
"v_mfma_f32_16x16x32_bf16 v[212:215], a[80:83], a[120:123], v[212:215]\n"
"ds_read_b128 a[120:123], v12 offset:22528\n"
"v_mfma_f32_16x16x32_bf16 v[216:219], a[80:83], a[128:131], v[216:219]\n"
"ds_read_b64_tr_b16 a[50:51], v15 offset:8704\n"
"v_mfma_f32_16x16x32_bf16 v[216:219], a[84:87], a[132:135], v[216:219]\n"
"ds_read_b64_tr_b16 a[52:53], v15 offset:12288\n"
"v_mfma_f32_16x16x32_bf16 v[244:247], a[92:95], a[132:135], v[244:247]\n"
"ds_read_b128 a[132:135], v13 offset:24576\n"
"v_mfma_f32_16x16x32_bf16 v[244:247], a[88:91], a[128:131], v[244:247]\n"
"s_add_u32 s67, s75, 524288\n"
"ds_read_b128 a[128:131], v12 offset:24576\n"
"v_mfma_f32_16x16x32_bf16 v[248:251], a[88:91], a[136:139], v[248:251]\n"
"ds_read_b64_tr_b16 a[54:55], v15 offset:12800\n"
"v_mfma_f32_16x16x32_bf16 v[248:251], a[92:95], a[140:143], v[248:251]\n"
"ds_read_b64_tr_b16 a[56:57], v15 offset:8200\n"
"v_mfma_f32_16x16x32_bf16 v[220:223], a[84:87], a[140:143], v[220:223]\n"
"ds_read_b128 a[140:143], v13 offset:26624\n"
"v_mfma_f32_16x16x32_bf16 v[220:223], a[80:83], a[136:139], v[220:223]\n"
"ds_read_b128 a[136:139], v12 offset:26624\n"
"v_mfma_f32_16x16x32_bf16 v[224:227], a[80:83], a[144:147], v[224:227]\n"
"ds_read_b64_tr_b16 a[58:59], v15 offset:8712\n"
"v_mfma_f32_16x16x32_bf16 v[224:227], a[84:87], a[148:151], v[224:227]\n"
"ds_read_b64_tr_b16 a[60:61], v15 offset:12296\n"
"v_mfma_f32_16x16x32_bf16 v[252:255], a[92:95], a[148:151], v[252:255]\n"
"ds_read_b128 a[148:151], v13 offset:28672\n"
"v_mfma_f32_16x16x32_bf16 v[252:255], a[88:91], a[144:147], v[252:255]\n"
"ds_read_b128 a[144:147], v12 offset:28672\n"
"ds_read_b64_tr_b16 a[62:63], v15 offset:12808\n"
"v_mfma_f32_16x16x32_bf16 a[28:31], a[88:91], a[152:155], a[28:31]\n"
"v_mfma_f32_16x16x32_bf16 a[28:31], a[92:95], a[156:159], a[28:31]\n"
"v_mfma_f32_16x16x32_bf16 a[24:27], a[84:87], a[156:159], a[24:27]\n"
"ds_read_b128 a[156:159], v13 offset:30720\n"
"v_mfma_f32_16x16x32_bf16 a[24:27], a[80:83], a[152:155], a[24:27]\n"
"s_waitcnt lgkmcnt(15)\n"
"ds_read_b128 a[152:155], v12 offset:30720\n"
"v_mfma_f32_16x16x32_bf16 v[32:35], a[32:35], a[96:99], v[32:35]\n"
"v_mfma_f32_16x16x32_bf16 v[32:35], a[36:39], a[100:103], v[32:35]\n"
"s_waitcnt lgkmcnt(2)\n"
"v_mfma_f32_16x16x32_bf16 v[60:63], a[44:47], a[100:103], v[60:63]\n"
"buffer_load_dwordx4 v11, s[52:55], s67 offen offset:0  lds\n"
"v_mfma_f32_16x16x32_bf16 v[60:63], a[40:43], a[96:99], v[60:63]\n"
"v_mfma_f32_16x16x32_bf16 v[64:67], a[40:43], a[104:107], v[64:67]\n"
"s_add_u32 s67, s75, 539648\n"
"v_mfma_f32_16x16x32_bf16 v[64:67], a[44:47], a[108:111], v[64:67]\n"
"v_mfma_f32_16x16x32_bf16 v[36:39], a[36:39], a[108:111], v[36:39]\n"
"buffer_load_dwordx4 v11, s[52:55], s67 offen offset:1024  lds\n"
"v_mfma_f32_16x16x32_bf16 v[36:39], a[32:35], a[104:107], v[36:39]\n"
"v_mfma_f32_16x16x32_bf16 v[40:43], a[32:35], a[112:115], v[40:43]\n"
"s_add_u32 s67, s75, 555008\n"
"v_mfma_f32_16x16x32_bf16 v[40:43], a[36:39], a[116:119], v[40:43]\n"
"v_mfma_f32_16x16x32_bf16 v[68:71], a[44:47], a[116:119], v[68:71]\n"
"v_mfma_f32_16x16x32_bf16 v[68:71], a[40:43], a[112:115], v[68:71]\n"
"buffer_load_dwordx4 v11, s[52:55], s67 offen offset:2048  lds\n"
"v_mfma_f32_16x16x32_bf16 v[72:75], a[40:43], a[120:123], v[72:75]\n"
"v_mfma_f32_16x16x32_bf16 v[72:75], a[44:47], a[124:127], v[72:75]\n"
"s_add_u32 s67, s75, 570368\n"
"v_mfma_f32_16x16x32_bf16 v[44:47], a[36:39], a[124:127], v[44:47]\n"
"v_mfma_f32_16x16x32_bf16 v[44:47], a[32:35], a[120:123], v[44:47]\n"
"buffer_load_dwordx4 v11, s[52:55], s67 offen offset:3072  lds\n"
"v_mfma_f32_16x16x32_bf16 v[48:51], a[32:35], a[128:131], v[48:51]\n"
"v_mfma_f32_16x16x32_bf16 v[48:51], a[36:39], a[132:135], v[48:51]\n"
"s_add_u32 m0, s64, 12288\n"
"v_mfma_f32_16x16x32_bf16 v[76:79], a[44:47], a[132:135], v[76:79]\n"
"s_waitcnt vmcnt(9)\n"
"ds_read_b64_tr_b16 a[64:65], v9 offset:0\n"
"v_mfma_f32_16x16x32_bf16 v[76:79], a[40:43], a[128:131], v[76:79]\n"
"s_add_u32 s67, s75, 589824\n"
"ds_read_b64_tr_b16 a[66:67], v9 offset:512\n"
"v_mfma_f32_16x16x32_bf16 v[80:83], a[40:43], a[136:139], v[80:83]\n"
"ds_read_b64_tr_b16 a[68:69], v9 offset:4096\n"
"v_mfma_f32_16x16x32_bf16 v[80:83], a[44:47], a[140:143], v[80:83]\n"
"ds_read_b64_tr_b16 a[70:71], v9 offset:4608\n"
"buffer_load_dwordx4 v11, s[52:55], s67 offen offset:0  lds\n"
"v_mfma_f32_16x16x32_bf16 v[52:55], a[36:39], a[140:143], v[52:55]\n"
"s_add_u32 s67, s75, 605184\n"
"ds_read_b64_tr_b16 a[72:73], v9 offset:8\n"
"v_mfma_f32_16x16x32_bf16 v[52:55], a[32:35], a[136:139], v[52:55]\n"
"s_waitcnt lgkmcnt(5)\n"
"ds_read_b64_tr_b16 a[74:75], v9 offset:520\n"
"v_mfma_f32_16x16x32_bf16 v[56:59], a[32:35], a[144:147], v[56:59]\n"
"ds_read_b64_tr_b16 a[76:77], v9 offset:4104\n"
"v_mfma_f32_16x16x32_bf16 v[56:59], a[36:39], a[148:151], v[56:59]\n"
"ds_read_b64_tr_b16 a[78:79], v9 offset:4616\n"
"v_mfma_f32_16x16x32_bf16 v[84:87], a[44:47], a[148:151], v[84:87]\n"
"buffer_load_dwordx4 v11, s[52:55], s67 offen offset:1024  lds\n"
"v_mfma_f32_16x16x32_bf16 v[84:87], a[40:43], a[144:147], v[84:87]\n"
"ds_read_b64_tr_b16 a[80:81], v15 offset:0\n"
"v_mfma_f32_16x16x32_bf16 a[4:7], a[40:43], a[152:155], a[4:7]\n"
"s_add_u32 s67, s75, 620544\n"
"ds_read_b64_tr_b16 a[82:83], v15 offset:512\n"
"v_mfma_f32_16x16x32_bf16 a[4:7], a[44:47], a[156:159], a[4:7]\n"
"ds_read_b64_tr_b16 a[84:85], v15 offset:4096\n"
"v_mfma_f32_16x16x32_bf16 a[0:3], a[36:39], a[156:159], a[0:3]\n"
"ds_read_b64_tr_b16 a[86:87], v15 offset:4608\n"
"buffer_load_dwordx4 v11, s[52:55], s67 offen offset:2048  lds\n"
"v_mfma_f32_16x16x32_bf16 a[0:3], a[32:35], a[152:155], a[0:3]\n"
"ds_read_b64_tr_b16 a[88:89], v15 offset:8\n"
"v_mfma_f32_16x16x32_bf16 v[88:91], a[48:51], a[96:99], v[88:91]\n"
"s_add_u32 s67, s75, 635904\n"
"ds_read_b64_tr_b16 a[90:91], v15 offset:520\n"
"v_mfma_f32_16x16x32_bf16 v[88:91], a[52:55], a[100:103], v[88:91]\n"
"ds_read_b64_tr_b16 a[92:93], v15 offset:4104\n"
"v_mfma_f32_16x16x32_bf16 v[116:119], a[60:63], a[100:103], v[116:119]\n"
"ds_read_b64_tr_b16 a[94:95], v15 offset:4616\n"
"v_mfma_f32_16x16x32_bf16 v[116:119], a[56:59], a[96:99], v[116:119]\n"
"buffer_load_dwordx4 v11, s[52:55], s67 offen offset:3072  lds\n"
"v_mfma_f32_16x16x32_bf16 v[120:123], a[56:59], a[104:107], v[120:123]\n"
"v_mfma_f32_16x16x32_bf16 v[120:123], a[60:63], a[108:111], v[120:123]\n"
"s_add_u32 m0, s64, 0\n"
"v_mfma_f32_16x16x32_bf16 v[92:95], a[52:55], a[108:111], v[92:95]\n"
"v_mfma_f32_16x16x32_bf16 v[92:95], a[48:51], a[104:107], v[92:95]\n"
"s_add_u32 s67, s75, 524416\n"
"v_mfma_f32_16x16x32_bf16 v[96:99], a[48:51], a[112:115], v[96:99]\n"
"s_waitcnt lgkmcnt(0)\n"
"v_mfma_f32_16x16x32_bf16 v[96:99], a[52:55], a[116:119], v[96:99]\n"
"buffer_load_dwordx4 v11, s[52:55], s67 offen offset:0  lds\n"
"v_mfma_f32_16x16x32_bf16 v[124:127], a[60:63], a[116:119], v[124:127]\n"
"v_mfma_f32_16x16x32_bf16 v[124:127], a[56:59], a[112:115], v[124:127]\n"
"s_add_u32 s67, s75, 539776\n"
"v_mfma_f32_16x16x32_bf16 v[128:131], a[56:59], a[120:123], v[128:131]\n"
"v_mfma_f32_16x16x32_bf16 v[128:131], a[60:63], a[124:127], v[128:131]\n"
"v_mfma_f32_16x16x32_bf16 v[100:103], a[52:55], a[124:127], v[100:103]\n"
"buffer_load_dwordx4 v11, s[52:55], s67 offen offset:1024  lds\n"
"v_mfma_f32_16x16x32_bf16 v[100:103], a[48:51], a[120:123], v[100:103]\n"
"v_mfma_f32_16x16x32_bf16 v[104:107], a[48:51], a[128:131], v[104:107]\n"
"s_add_u32 s67, s75, 555136\n"
"v_mfma_f32_16x16x32_bf16 v[104:107], a[52:55], a[132:135], v[104:107]\n"
"v_mfma_f32_16x16x32_bf16 v[132:135], a[60:63], a[132:135], v[132:135]\n"
"v_mfma_f32_16x16x32_bf16 v[132:135], a[56:59], a[128:131], v[132:135]\n"
"buffer_load_dwordx4 v11, s[52:55], s67 offen offset:2048  lds\n"
"v_mfma_f32_16x16x32_bf16 v[136:139], a[56:59], a[136:139], v[136:139]\n"
"v_mfma_f32_16x16x32_bf16 v[136:139], a[60:63], a[140:143], v[136:139]\n"
"s_add_u32 s67, s75, 570496\n"
"v_mfma_f32_16x16x32_bf16 v[108:111], a[52:55], a[140:143], v[108:111]\n"
"v_mfma_f32_16x16x32_bf16 v[108:111], a[48:51], a[136:139], v[108:111]\n"
"buffer_load_dwordx4 v11, s[52:55], s67 offen offset:3072  lds\n"
"v_mfma_f32_16x16x32_bf16 v[112:115], a[48:51], a[144:147], v[112:115]\n"
"v_mfma_f32_16x16x32_bf16 v[112:115], a[52:55], a[148:151], v[112:115]\n"
"s_add_u32 m0, s64, 4096\n"
"v_mfma_f32_16x16x32_bf16 v[140:143], a[60:63], a[148:151], v[140:143]\n"
"v_mfma_f32_16x16x32_bf16 v[140:143], a[56:59], a[144:147], v[140:143]\n"
"s_add_u32 s67, s75, 589952\n"
"v_mfma_f32_16x16x32_bf16 a[12:15], a[56:59], a[152:155], a[12:15]\n"
"v_mfma_f32_16x16x32_bf16 a[12:15], a[60:63], a[156:159], a[12:15]\n"
"buffer_load_dwordx4 v11, s[52:55], s67 offen offset:0  lds\n"
"v_mfma_f32_16x16x32_bf16 a[8:11], a[52:55], a[156:159], a[8:11]\n"
"v_mfma_f32_16x16x32_bf16 a[8:11], a[48:51], a[152:155], a[8:11]\n"
"s_add_u32 s67, s75, 605312\n"
"v_mfma_f32_16x16x32_bf16 v[144:147], a[64:67], a[96:99], v[144:147]\n"
"v_mfma_f32_16x16x32_bf16 v[144:147], a[68:71], a[100:103], v[144:147]\n"
"buffer_load_dwordx4 v11, s[52:55], s67 offen offset:1024  lds\n"
"v_mfma_f32_16x16x32_bf16 v[172:175], a[76:79], a[100:103], v[172:175]\n"
"v_mfma_f32_16x16x32_bf16 v[172:175], a[72:75], a[96:99], v[172:175]\n"
"s_add_u32 s67, s75, 620672\n"
"v_mfma_f32_16x16x32_bf16 v[176:179], a[72:75], a[104:107], v[176:179]\n"
"v_mfma_f32_16x16x32_bf16 v[176:179], a[76:79], a[108:111], v[176:179]\n"
"buffer_load_dwordx4 v11, s[52:55], s67 offen offset:2048  lds\n"
"v_mfma_f32_16x16x32_bf16 v[148:151], a[68:71], a[108:111], v[148:151]\n"
"v_mfma_f32_16x16x32_bf16 v[148:151], a[64:67], a[104:107], v[148:151]\n"
"s_add_u32 s67, s75, 636032\n"
"v_mfma_f32_16x16x32_bf16 v[152:155], a[64:67], a[112:115], v[152:155]\n"
"s_sub_u32 s66, 0, s73\n"
"v_mfma_f32_16x16x32_bf16 v[152:155], a[68:71], a[116:119], v[152:155]\n"
"v_mfma_f32_16x16x32_bf16 v[180:183], a[76:79], a[116:119], v[180:183]\n"
"v_add_u32 v12, s66, v7\n"
"v_mfma_f32_16x16x32_bf16 v[180:183], a[72:75], a[112:115], v[180:183]\n"
"buffer_load_dwordx4 v11, s[52:55], s67 offen offset:3072  lds\n"
"v_mfma_f32_16x16x32_bf16 v[184:187], a[72:75], a[120:123], v[184:187]\n"
"s_add_u32 m0, s72, 57344\n"
"v_add_u32 v13, s66, v8\n"
"v_mfma_f32_16x16x32_bf16 v[184:187], a[76:79], a[124:127], v[184:187]\n"
"v_mfma_f32_16x16x32_bf16 v[156:159], a[68:71], a[124:127], v[156:159]\n"
"s_add_u32 s67, s60, 640\n"
"v_mfma_f32_16x16x32_bf16 v[156:159], a[64:67], a[120:123], v[156:159]\n"
"buffer_load_dwordx4 v6, s[48:51], s67 offen offset:0  lds\n"
"v_mfma_f32_16x16x32_bf16 v[160:163], a[64:67], a[128:131], v[160:163]\n"
"v_mfma_f32_16x16x32_bf16 v[160:163], a[68:71], a[132:135], v[160:163]\n"
"s_add_u32 s67, s60, 16000\n"
"v_mfma_f32_16x16x32_bf16 v[188:191], a[76:79], a[132:135], v[188:191]\n"
"v_mfma_f32_16x16x32_bf16 v[188:191], a[72:75], a[128:131], v[188:191]\n"
"buffer_load_dwordx4 v6, s[48:51], s67 offen offset:1024  lds\n"
"v_mfma_f32_16x16x32_bf16 v[192:195], a[72:75], a[136:139], v[192:195]\n"
"v_mfma_f32_16x16x32_bf16 v[192:195], a[76:79], a[140:143], v[192:195]\n"
"s_add_u32 s67, s60, 31360\n"
"v_mfma_f32_16x16x32_bf16 v[164:167], a[68:71], a[140:143], v[164:167]\n"
"v_mfma_f32_16x16x32_bf16 v[164:167], a[64:67], a[136:139], v[164:167]\n"
"buffer_load_dwordx4 v6, s[48:51], s67 offen offset:2048  lds\n"
"v_mfma_f32_16x16x32_bf16 v[168:171], a[64:67], a[144:147], v[168:171]\n"
"v_mfma_f32_16x16x32_bf16 v[168:171], a[68:71], a[148:151], v[168:171]\n"
"s_add_u32 s67, s60, 46720\n"
"v_mfma_f32_16x16x32_bf16 v[196:199], a[76:79], a[148:151], v[196:199]\n"
"v_mfma_f32_16x16x32_bf16 v[196:199], a[72:75], a[144:147], v[196:199]\n"
"v_mfma_f32_16x16x32_bf16 a[20:23], a[72:75], a[152:155], a[20:23]\n"
"buffer_load_dwordx4 v6, s[48:51], s67 offen offset:3072  lds\n"
"v_mfma_f32_16x16x32_bf16 a[20:23], a[76:79], a[156:159], a[20:23]\n"
"s_sub_u32 s70, 1048576, s75\n"
"v_and_b32 v19, 0x20000, v14\n"
"v_mfma_f32_16x16x32_bf16 a[16:19], a[68:71], a[156:159], a[16:19]\n"
"v_add_u32 v14, v19, v14\n"
"v_mfma_f32_16x16x32_bf16 a[16:19], a[64:67], a[152:155], a[16:19]\n"
"s_waitcnt vmcnt(21)\n"
"s_barrier\n"
"v_mfma_f32_16x16x32_bf16 v[200:203], a[80:83], a[96:99], v[200:203]\n"
"s_waitcnt vmcnt(12)\n"
"ds_read_b64_tr_b16 a[32:33], v9 offset:8192\n"
"v_mfma_f32_16x16x32_bf16 v[200:203], a[84:87], a[100:103], v[200:203]\n"
"s_add_u32 s70, s70, s65\n"
"ds_read_b64_tr_b16 a[34:35], v9 offset:8704\n"
"s_getpc_b64 s[96:97]\n"
"L_c10_:\n"
"s_add_u32 s96, s96, L_H6S-L_c10_\n"
"s_addc_u32 s97, s97, 0\n"
"v_mfma_f32_16x16x32_bf16 v[228:231], a[92:95], a[100:103], v[228:231]\n"
"s_lshr_b32 s66, %26, 3\n"
"ds_read_b128 a[100:103], v13 offset:16384\n"
"buffer_load_dword a252, v14, s[52:55], s70 offen\n"
"v_mfma_f32_16x16x32_bf16 v[228:231], a[88:91], a[96:99], v[228:231]\n"
"s_lshl2_add_u32 s66, s66, %16\n"
"ds_read_b128 a[96:99], v12 offset:16384\n"
"v_mfma_f32_16x16x32_bf16 v[232:235], a[88:91], a[104:107], v[232:235]\n"
"s_mul_i32 s66, s66, 128\n"
"ds_read_b64_tr_b16 a[36:37], v9 offset:12288\n"
"v_mfma_f32_16x16x32_bf16 v[232:235], a[92:95], a[108:111], v[232:235]\n"
"s_min_u32 s66, s66, L_headF-L_H6S-128\n"
"ds_read_b64_tr_b16 a[38:39], v9 offset:12800\n"
"v_min_u32 v22, 0, v0\n"
"v_mfma_f32_16x16x32_bf16 v[204:207], a[84:87], a[108:111], v[204:207]\n"
"ds_read_b128 a[108:111], v13 offset:18432\n"
"v_lshl_add_u32 v22, v22, 3, s66\n"
"v_mfma_f32_16x16x32_bf16 v[204:207], a[80:83], a[104:107], v[204:207]\n"
"ds_read_b128 a[104:107], v12 offset:18432\n"
"global_load_dword v23, v22, s[96:97]\n"
"ds_read_b64_tr_b16 a[40:41], v9 offset:8200\n"
"v_mfma_f32_16x16x32_bf16 v[208:211], a[80:83], a[112:115], v[208:211]\n"
"s_add_u32 m0, s64, 8192\n"
"ds_read_b64_tr_b16 a[42:43], v9 offset:8712\n"
"v_mfma_f32_16x16x32_bf16 v[208:211], a[84:87], a[116:119], v[208:211]\n"
"ds_read_b64_tr_b16 a[44:45], v9 offset:12296\n"
"v_mfma_f32_16x16x32_bf16 v[236:239], a[92:95], a[116:119], v[236:239]\n"
"ds_read_b128 a[116:119], v13 offset:20480\n"
"v_mfma_f32_16x16x32_bf16 v[236:239], a[88:91], a[112:115], v[236:239]\n"
"ds_read_b128 a[112:115], v12 offset:20480\n"
"v_mfma_f32_16x16x32_bf16 v[240:243], a[88:91], a[120:123], v[240:243]\n"
"ds_read_b64_tr_b16 a[46:47], v9 offset:12808\n"
"v_mfma_f32_16x16x32_bf16 v[240:243], a[92:95], a[124:127], v[240:243]\n"
"ds_read_b64_tr_b16 a[48:49], v15 offset:8192\n"
"v_mfma_f32_16x16x32_bf16 v[212:215], a[84:87], a[124:127], v[212:215]\n"
"ds_read_b128 a[124:127], v13 offset:22528\n"
"v_mfma_f32_16x16x32_bf16 v[212:215], a[80:83], a[120:123], v[212:215]\n"
"ds_read_b128 a[120:123], v12 offset:22528\n"
"v_mfma_f32_16x16x32_bf16 v[216:219], a[80:83], a[128:131], v[216:219]\n"
"ds_read_b64_tr_b16 a[50:51], v15 offset:8704\n"
"v_mfma_f32_16x16x32_bf16 v[216:219], a[84:87], a[132:135], v[216:219]\n"
"ds_read_b64_tr_b16 a[52:53], v15 offset:12288\n"
"v_mfma_f32_16x16x32_bf16 v[244:247], a[92:95], a[132:135], v[244:247]\n"
"ds_read_b128 a[132:135], v13 offset:24576\n"
"v_mfma_f32_16x16x32_bf16 v[244:247], a[88:91], a[128:131], v[244:247]\n"
"s_add_u32 s67, s75, 655360\n"
"ds_read_b128 a[128:131], v12 offset:24576\n"
"v_mfma_f32_16x16x32_bf16 v[248:251], a[88:91], a[136:139], v[248:251]\n"
"ds_read_b64_tr_b16 a[54:55], v15 offset:12800\n"
"v_mfma_f32_16x16x32_bf16 v[248:251], a[92:95], a[140:143], v[248:251]\n"
"ds_read_b64_tr_b16 a[56:57], v15 offset:8200\n"
"v_mfma_f32_16x16x32_bf16 v[220:223], a[84:87], a[140:143], v[220:223]\n"
"ds_read_b128 a[140:143], v13 offset:26624\n"
"v_mfma_f32_16x16x32_bf16 v[220:223], a[80:83], a[136:139], v[220:223]\n"
"ds_read_b128 a[136:139], v12 offset:26624\n"
"v_mfma_f32_16x16x32_bf16 v[224:227], a[80:83], a[144:147], v[224:227]\n"
"ds_read_b64_tr_b16 a[58:59], v15 offset:8712\n"
"v_mfma_f32_16x16x32_bf16 v[224:227], a[84:87], a[148:151], v[224:227]\n"
"ds_read_b64_tr_b16 a[60:61], v15 offset:12296\n"
"v_mfma_f32_16x16x32_bf16 v[252:255], a[92:95], a[148:151], v[252:255]\n"
"ds_read_b128 a[148:151], v13 offset:28672\n"
"v_mfma_f32_16x16x32_bf16 v[252:255], a[88:91], a[144:147], v[252:255]\n"
"ds_read_b128 a[144:147], v12 offset:28672\n"
"ds_read_b64_tr_b16 a[62:63], v15 offset:12808\n"
"v_mfma_f32_16x16x32_bf16 a[28:31], a[88:91], a[152:155], a[28:31]\n"
"v_mfma_f32_16x16x32_bf16 a[28:31], a[92:95], a[156:159], a[28:31]\n"
"v_mfma_f32_16x16x32_bf16 a[24:27], a[84:87], a[156:159], a[24:27]\n"
"ds_read_b128 a[156:159], v13 offset:30720\n"
"v_mfma_f32_16x16x32_bf16 a[24:27], a[80:83], a[152:155], a[24:27]\n"
"s_waitcnt lgkmcnt(15)\n"
"ds_read_b128 a[152:155], v12 offset:30720\n"
"v_mfma_f32_16x16x32_bf16 v[32:35], a[32:35], a[96:99], v[32:35]\n"
"v_mfma_f32_16x16x32_bf16 v[32:35], a[36:39], a[100:103], v[32:35]\n"
"s_waitcnt lgkmcnt(2)\n"
"v_mfma_f32_16x16x32_bf16 v[60:63], a[44:47], a[100:103], v[60:63]\n"
"buffer_load_dwordx4 v11, s[52:55], s67 offen offset:0  lds\n"
"v_mfma_f32_16x16x32_bf16 v[60:63], a[40:43], a[96:99], v[60:63]\n"
"v_mfma_f32_16x16x32_bf16 v[64:67], a[40:43], a[104:107], v[64:67]\n"
"s_add_u32 s67, s75, 670720\n"
"v_mfma_f32_16x16x32_bf16 v[64:67], a[44:47], a[108:111], v[64:67]\n"
"v_mfma_f32_16x16x32_bf16 v[36:39], a[36:39], a[108:111], v[36:39]\n"
"buffer_load_dwordx4 v11, s[52:55], s67 offen offset:1024  lds\n"
"v_mfma_f32_16x16x32_bf16 v[36:39], a[32:35], a[104:107], v[36:39]\n"
"v_mfma_f32_16x16x32_bf16 v[40:43], a[32:35], a[112:115], v[40:43]\n"
"s_add_u32 s67, s75, 686080\n"
"v_mfma_f32_16x16x32_bf16 v[40:43], a[36:39], a[116:119], v[40:43]\n"
"v_mfma_f32_16x16x32_bf16 v[68:71], a[44:47], a[116:119], v[68:71]\n"
"v_mfma_f32_16x16x32_bf16 v[68:71], a[40:43], a[112:115], v[68:71]\n"
"buffer_load_dwordx4 v11, s[52:55], s67 offen offset:2048  lds\n"
"v_mfma_f32_16x16x32_bf16 v[72:75], a[40:43], a[120:123], v[72:75]\n"
"v_mfma_f32_16x16x32_bf16 v[72:75], a[44:47], a[124:127], v[72:75]\n"
"s_add_u32 s67, s75, 701440\n"
"v_mfma_f32_16x16x32_bf16 v[44:47], a[36:39], a[124:127], v[44:47]\n"
"v_mfma_f32_16x16x32_bf16 v[44:47], a[32:35], a[120:123], v[44:47]\n"
"buffer_load_dwordx4 v11, s[52:55], s67 offen offset:3072  lds\n"
"v_mfma_f32_16x16x32_bf16 v[48:51], a[32:35], a[128:131], v[48:51]\n"
"v_mfma_f32_16x16x32_bf16 v[48:51], a[36:39], a[132:135], v[48:51]\n"
"s_add_u32 m0, s64, 12288\n"
"v_mfma_f32_16x16x32_bf16 v[76:79], a[44:47], a[132:135], v[76:79]\n"
"s_waitcnt vmcnt(10)\n"
"ds_read_b64_tr_b16 a[64:65], v9 offset:0\n"
"v_mfma_f32_16x16x32_bf16 v[76:79], a[40:43], a[128:131], v[76:79]\n"
"s_add_u32 s67, s75, 720896\n"
"ds_read_b64_tr_b16 a[66:67], v9 offset:512\n"
"v_mfma_f32_16x16x32_bf16 v[80:83], a[40:43], a[136:139], v[80:83]\n"
"ds_read_b64_tr_b16 a[68:69], v9 offset:4096\n"
"v_mfma_f32_16x16x32_bf16 v[80:83], a[44:47], a[140:143], v[80:83]\n"
"ds_read_b64_tr_b16 a[70:71], v9 offset:4608\n"
"buffer_load_dwordx4 v11, s[52:55], s67 offen offset:0  lds\n"
"v_mfma_f32_16x16x32_bf16 v[52:55], a[36:39], a[140:143], v[52:55]\n"
"s_add_u32 s67, s75, 736256\n"
"ds_read_b64_tr_b16 a[72:73], v9 offset:8\n"
"v_mfma_f32_16x16x32_bf16 v[52:55], a[32:35], a[136:139], v[52:55]\n"
"s_waitcnt lgkmcnt(5)\n"
"ds_read_b64_tr_b16 a[74:75], v9 offset:520\n"
"v_mfma_f32_16x16x32_bf16 v[56:59], a[32:35], a[144:147], v[56:59]\n"
"ds_read_b64_tr_b16 a[76:77], v9 offset:4104\n"
"v_mfma_f32_16x16x32_bf16 v[56:59], a[36:39], a[148:151], v[56:59]\n"
"ds_read_b64_tr_b16 a[78:79], v9 offset:4616\n"
"v_mfma_f32_16x16x32_bf16 v[84:87], a[44:47], a[148:151], v[84:87]\n"
"buffer_load_dwordx4 v11, s[52:55], s67 offen offset:1024  lds\n"
"v_mfma_f32_16x16x32_bf16 v[84:87], a[40:43], a[144:147], v[84:87]\n"
"ds_read_b64_tr_b16 a[80:81], v15 offset:0\n"
"v_mfma_f32_16x16x32_bf16 a[4:7], a[40:43], a[152:155], a[4:7]\n"
"s_add_u32 s67, s75, 751616\n"
"ds_read_b64_tr_b16 a[82:83], v15 offset:512\n"
"v_mfma_f32_16x16x32_bf16 a[4:7], a[44:47], a[156:159], a[4:7]\n"
"ds_read_b64_tr_b16 a[84:85], v15 offset:4096\n"
"v_mfma_f32_16x16x32_bf16 a[0:3], a[36:39], a[156:159], a[0:3]\n"
"ds_read_b64_tr_b16 a[86:87], v15 offset:4608\n"
"buffer_load_dwordx4 v11, s[52:55], s67 offen offset:2048  lds\n"
"v_mfma_f32_16x16x32_bf16 a[0:3], a[32:35], a[152:155], a[0:3]\n"
"ds_read_b64_tr_b16 a[88:89], v15 offset:8\n"
"v_mfma_f32_16x16x32_bf16 v[88:91], a[48:51], a[96:99], v[88:91]\n"
"s_add_u32 s67, s75, 766976\n"
"ds_read_b64_tr_b16 a[90:91], v15 offset:520\n"
"v_mfma_f32_16x16x32_bf16 v[88:91], a[52:55], a[100:103], v[88:91]\n"
"ds_read_b64_tr_b16 a[92:93], v15 offset:4104\n"
"v_mfma_f32_16x16x32_bf16 v[116:119], a[60:63], a[100:103], v[116:119]\n"
"ds_read_b64_tr_b16 a[94:95], v15 offset:4616\n"
"v_mfma_f32_16x16x32_bf16 v[116:119], a[56:59], a[96:99], v[116:119]\n"
"buffer_load_dwordx4 v11, s[52:55], s67 offen offset:3072  lds\n"
"v_mfma_f32_16x16x32_bf16 v[120:123], a[56:59], a[104:107], v[120:123]\n"
"v_mfma_f32_16x16x32_bf16 v[120:123], a[60:63], a[108:111], v[120:123]\n"
"s_add_u32 m0, s64, 0\n"
"v_mfma_f32_16x16x32_bf16 v[92:95], a[52:55], a[108:111], v[92:95]\n"
"v_mfma_f32_16x16x32_bf16 v[92:95], a[48:51], a[104:107], v[92:95]\n"
"s_add_u32 s67, s75, 655488\n"
"v_mfma_f32_16x16x32_bf16 v[96:99], a[48:51], a[112:115], v[96:99]\n"
"s_waitcnt lgkmcnt(0)\n"
"v_mfma_f32_16x16x32_bf16 v[96:99], a[52:55], a[116:119], v[96:99]\n"
"buffer_load_dwordx4 v11, s[52:55], s67 offen offset:0  lds\n"
"v_mfma_f32_16x16x32_bf16 v[124:127], a[60:63], a[116:119], v[124:127]\n"
"v_mfma_f32_16x16x32_bf16 v[124:127], a[56:59], a[112:115], v[124:127]\n"
"s_add_u32 s67, s75, 670848\n"
"v_mfma_f32_16x16x32_bf16 v[128:131], a[56:59], a[120:123], v[128:131]\n"
"v_mfma_f32_16x16x32_bf16 v[128:131], a[60:63], a[124:127], v[128:131]\n"
"v_mfma_f32_16x16x32_bf16 v[100:103], a[52:55], a[124:127], v[100:103]\n"
"buffer_load_dwordx4 v11, s[52:55], s67 offen offset:1024  lds\n"
"v_mfma_f32_16x16x32_bf16 v[100:103], a[48:51], a[120:123], v[100:103]\n"
"v_mfma_f32_16x16x32_bf16 v[104:107], a[48:51], a[128:131], v[104:107]\n"
"s_add_u32 s67, s75, 686208\n"
"v_mfma_f32_16x16x32_bf16 v[104:107], a[52:55], a[132:135], v[104:107]\n"
"v_mfma_f32_16x16x32_bf16 v[132:135], a[60:63], a[132:135], v[132:135]\n"
"v_mfma_f32_16x16x32_bf16 v[132:135], a[56:59], a[128:131], v[132:135]\n"
"buffer_load_dwordx4 v11, s[52:55], s67 offen offset:2048  lds\n"
"v_mfma_f32_16x16x32_bf16 v[136:139], a[56:59], a[136:139], v[136:139]\n"
"v_mfma_f32_16x16x32_bf16 v[136:139], a[60:63], a[140:143], v[136:139]\n"
"s_add_u32 s67, s75, 701568\n"
"v_mfma_f32_16x16x32_bf16 v[108:111], a[52:55], a[140:143], v[108:111]\n"
"v_mfma_f32_16x16x32_bf16 v[108:111], a[48:51], a[136:139], v[108:111]\n"
"buffer_load_dwordx4 v11, s[52:55], s67 offen offset:3072  lds\n"
"v_mfma_f32_16x16x32_bf16 v[112:115], a[48:51], a[144:147], v[112:115]\n"
"v_mfma_f32_16x16x32_bf16 v[112:115], a[52:55], a[148:151], v[112:115]\n"
"s_add_u32 m0, s64, 4096\n"
"v_mfma_f32_16x16x32_bf16 v[140:143], a[60:63], a[148:151], v[140:143]\n"
"v_mfma_f32_16x16x32_bf16 v[140:143], a[56:59], a[144:147], v[140:143]\n"
"s_add_u32 s67, s75, 721024\n"
"v_mfma_f32_16x16x32_bf16 a[12:15], a[56:59], a[152:155], a[12:15]\n"
"v_mfma_f32_16x16x32_bf16 a[12:15], a[60:63], a[156:159], a[12:15]\n"
"buffer_load_dwordx4 v11, s[52:55], s67 offen offset:0  lds\n"
"v_mfma_f32_16x16x32_bf16 a[8:11], a[52:55], a[156:159], a[8:11]\n"
"v_mfma_f32_16x16x32_bf16 a[8:11], a[48:51], a[152:155], a[8:11]\n"
"s_add_u32 s67, s75, 736384\n"
"v_mfma_f32_16x16x32_bf16 v[144:147], a[64:67], a[96:99], v[144:147]\n"
"v_mfma_f32_16x16x32_bf16 v[144:147], a[68:71], a[100:103], v[144:147]\n"
"buffer_load_dwordx4 v11, s[52:55], s67 offen offset:1024  lds\n"
"v_mfma_f32_16x16x32_bf16 v[172:175], a[76:79], a[100:103], v[172:175]\n"
"v_mfma_f32_16x16x32_bf16 v[172:175], a[72:75], a[96:99], v[172:175]\n"
"s_add_u32 s67, s75, 751744\n"
"v_mfma_f32_16x16x32_bf16 v[176:179], a[72:75], a[104:107], v[176:179]\n"
"v_mfma_f32_16x16x32_bf16 v[176:179], a[76:79], a[108:111], v[176:179]\n"
"buffer_load_dwordx4 v11, s[52:55], s67 offen offset:2048  lds\n"
"v_mfma_f32_16x16x32_bf16 v[148:151], a[68:71], a[108:111], v[148:151]\n"
"v_mfma_f32_16x16x32_bf16 v[148:151], a[64:67], a[104:107], v[148:151]\n"
"s_add_u32 s67, s75, 767104\n"
"v_mfma_f32_16x16x32_bf16 v[152:155], a[64:67], a[112:115], v[152:155]\n"
"s_sub_u32 s66, 40960, s73\n"
"v_mfma_f32_16x16x32_bf16 v[152:155], a[68:71], a[116:119], v[152:155]\n"
"v_mfma_f32_16x16x32_bf16 v[180:183], a[76:79], a[116:119], v[180:183]\n"
"v_add_u32 v12, s66, v7\n"
"v_mfma_f32_16x16x32_bf16 v[180:183], a[72:75], a[112:115], v[180:183]\n"
"buffer_load_dwordx4 v11, s[52:55], s67 offen offset:3072  lds\n"
"v_mfma_f32_16x16x32_bf16 v[184:187], a[72:75], a[120:123], v[184:187]\n"
"s_add_u32 m0, s72, 98304\n"
"v_add_u32 v13, s66, v8\n"
"v_mfma_f32_16x16x32_bf16 v[184:187], a[76:79], a[124:127], v[184:187]\n"
"v_mfma_f32_16x16x32_bf16 v[156:159], a[68:71], a[124:127], v[156:159]\n"
"s_add_u32 s67, s60, 768\n"
"v_mfma_f32_16x16x32_bf16 v[156:159], a[64:67], a[120:123], v[156:159]\n"
"buffer_load_dwordx4 v6, s[48:51], s67 offen offset:0  lds\n"
"v_mfma_f32_16x16x32_bf16 v[160:163], a[64:67], a[128:131], v[160:163]\n"
"v_mfma_f32_16x16x32_bf16 v[160:163], a[68:71], a[132:135], v[160:163]\n"
"s_add_u32 s67, s60, 16128\n"
"v_mfma_f32_16x16x32_bf16 v[188:191], a[76:79], a[132:135], v[188:191]\n"
"v_mfma_f32_16x16x32_bf16 v[188:191], a[72:75], a[128:131], v[188:191]\n"
"buffer_load_dwordx4 v6, s[48:51], s67 offen offset:1024  lds\n"
"v_mfma_f32_16x16x32_bf16 v[192:195], a[72:75], a[136:139], v[192:195]\n"
"v_mfma_f32_16x16x32_bf16 v[192:195], a[76:79], a[140:143], v[192:195]\n"
"s_add_u32 s67, s60, 31488\n"
"v_mfma_f32_16x16x32_bf16 v[164:167], a[68:71], a[140:143], v[164:167]\n"
"v_mfma_f32_16x16x32_bf16 v[164:167], a[64:67], a[136:139], v[164:167]\n"
"buffer_load_dwordx4 v6, s[48:51], s67 offen offset:2048  lds\n"
"v_mfma_f32_16x16x32_bf16 v[168:171], a[64:67], a[144:147], v[168:171]\n"
"v_mfma_f32_16x16x32_bf16 v[168:171], a[68:71], a[148:151], v[168:171]\n"
"s_add_u32 s67, s60, 46848\n"
"v_mfma_f32_16x16x32_bf16 v[196:199], a[76:79], a[148:151], v[196:199]\n"
"v_mfma_f32_16x16x32_bf16 v[196:199], a[72:75], a[144:147], v[196:199]\n"
"v_mfma_f32_16x16x32_bf16 a[20:23], a[72:75], a[152:155], a[20:23]\n"
"buffer_load_dwordx4 v6, s[48:51], s67 offen offset:3072  lds\n"
"v_mfma_f32_16x16x32_bf16 a[20:23], a[76:79], a[156:159], a[20:23]\n"
"v_mfma_f32_16x16x32_bf16 a[16:19], a[68:71], a[156:159], a[16:19]\n"
"s_sub_u32 s70, 1572864, s75\n"
"s_add_u32 s70, s70, s65\n"
"v_mfma_f32_16x16x32_bf16 a[16:19], a[64:67], a[152:155], a[16:19]\n"
"s_waitcnt vmcnt(22)\n"
"s_barrier\n"
"v_mfma_f32_16x16x32_bf16 v[200:203], a[80:83], a[96:99], v[200:203]\n"
"s_waitcnt vmcnt(12)\n"
"ds_read_b64_tr_b16 a[32:33], v9 offset:8192\n"
"v_mfma_f32_16x16x32_bf16 v[200:203], a[84:87], a[100:103], v[200:203]\n"
"ds_read_b64_tr_b16 a[34:35], v9 offset:8704\n"
"v_mfma_f32_16x16x32_bf16 v[228:231], a[92:95], a[100:103], v[228:231]\n"
"ds_read_b128 a[100:103], v13 offset:16384\n"
"buffer_load_dword a252, v14, s[52:55], s70 offen\n"
"v_mfma_f32_16x16x32_bf16 v[228:231], a[88:91], a[96:99], v[228:231]\n"
"ds_read_b128 a[96:99], v12 offset:16384\n"
"v_mfma_f32_16x16x32_bf16 v[232:235], a[88:91], a[104:107], v[232:235]\n"
"buffer_store_dwordx2 v[16:17], v18, s[56:59], %11 offen sc1\n"
"ds_read_b64_tr_b16 a[36:37], v9 offset:12288\n"
"v_mfma_f32_16x16x32_bf16 v[232:235], a[92:95], a[108:111], v[232:235]\n"
"ds_read_b64_tr_b16 a[38:39], v9 offset:12800\n"
"v_mfma_f32_16x16x32_bf16 v[204:207], a[84:87], a[108:111], v[204:207]\n"
"ds_read_b128 a[108:111], v13 offset:18432\n"
"v_mfma_f32_16x16x32_bf16 v[204:207], a[80:83], a[104:107], v[204:207]\n"
"ds_read_b128 a[104:107], v12 offset:18432\n"
"buffer_store_dwordx2 v[16:17], v18, s[56:59], s69 offen sc1\n"
"ds_read_b64_tr_b16 a[40:41], v9 offset:8200\n"
"v_mfma_f32_16x16x32_bf16 v[208:211], a[80:83], a[112:115], v[208:211]\n"
"ds_read_b64_tr_b16 a[42:43], v9 offset:8712\n"
"v_mfma_f32_16x16x32_bf16 v[208:211], a[84:87], a[116:119], v[208:211]\n"
"ds_read_b64_tr_b16 a[44:45], v9 offset:12296\n"
"v_mfma_f32_16x16x32_bf16 v[236:239], a[92:95], a[116:119], v[236:239]\n"
"ds_read_b128 a[116:119], v13 offset:20480\n"
"v_mfma_f32_16x16x32_bf16 v[236:239], a[88:91], a[112:115], v[236:239]\n"
"ds_read_b128 a[112:115], v12 offset:20480\n"
"v_mfma_f32_16x16x32_bf16 v[240:243], a[88:91], a[120:123], v[240:243]\n"
"s_add_u32 m0, s64, 8192\n"
"ds_read_b64_tr_b16 a[46:47], v9 offset:12808\n"
"v_mfma_f32_16x16x32_bf16 v[240:243], a[92:95], a[124:127], v[240:243]\n"
"ds_read_b64_tr_b16 a[48:49], v15 offset:8192\n"
"v_mfma_f32_16x16x32_bf16 v[212:215], a[84:87], a[124:127], v[212:215]\n"
"ds_read_b128 a[124:127], v13 offset:22528\n"
"v_mfma_f32_16x16x32_bf16 v[212:215], a[80:83], a[120:123], v[212:215]\n"
"ds_read_b128 a[120:123], v12 offset:22528\n"
"v_mfma_f32_16x16x32_bf16 v[216:219], a[80:83], a[128:131], v[216:219]\n"
"ds_read_b64_tr_b16 a[50:51], v15 offset:8704\n"
"v_mfma_f32_16x16x32_bf16 v[216:219], a[84:87], a[132:135], v[216:219]\n"
"ds_read_b64_tr_b16 a[52:53], v15 offset:12288\n"
"v_mfma_f32_16x16x32_bf16 v[244:247], a[92:95], a[132:135], v[244:247]\n"
"ds_read_b128 a[132:135], v13 offset:24576\n"
"v_mfma_f32_16x16x32_bf16 v[244:247], a[88:91], a[128:131], v[244:247]\n"
"ds_read_b128 a[128:131], v12 offset:24576\n"
"v_mfma_f32_16x16x32_bf16 v[248:251], a[88:91], a[136:139], v[248:251]\n"
"ds_read_b64_tr_b16 a[54:55], v15 offset:12800\n"
"v_mfma_f32_16x16x32_bf16 v[248:251], a[92:95], a[140:143], v[248:251]\n"
"ds_read_b64_tr_b16 a[56:57], v15 offset:8200\n"
"v_mfma_f32_16x16x32_bf16 v[220:223], a[84:87], a[140:143], v[220:223]\n"
"s_add_u32 s67, s75, 786432\n"
"ds_read_b128 a[140:143], v13 offset:26624\n"
"v_mfma_f32_16x16x32_bf16 v[220:223], a[80:83], a[136:139], v[220:223]\n"
"ds_read_b128 a[136:139], v12 offset:26624\n"
"v_mfma_f32_16x16x32_bf16 v[224:227], a[80:83], a[144:147], v[224:227]\n"
"ds_read_b64_tr_b16 a[58:59], v15 offset:8712\n"
"v_mfma_f32_16x16x32_bf16 v[224:227], a[84:87], a[148:151], v[224:227]\n"
"ds_read_b64_tr_b16 a[60:61], v15 offset:12296\n"
"v_mfma_f32_16x16x32_bf16 v[252:255], a[92:95], a[148:151], v[252:255]\n"
"ds_read_b128 a[148:151], v13 offset:28672\n"
"v_mfma_f32_16x16x32_bf16 v[252:255], a[88:91], a[144:147], v[252:255]\n"
"ds_read_b128 a[144:147], v12 offset:28672\n"
"ds_read_b64_tr_b16 a[62:63], v15 offset:12808\n"
"v_mfma_f32_16x16x32_bf16 a[28:31], a[88:91], a[152:155], a[28:31]\n"
"v_mfma_f32_16x16x32_bf16 a[28:31], a[92:95], a[156:159], a[28:31]\n"
"v_mfma_f32_16x16x32_bf16 a[24:27], a[84:87], a[156:159], a[24:27]\n"
"ds_read_b128 a[156:159], v13 offset:30720\n"
"v_mfma_f32_16x16x32_bf16 a[24:27], a[80:83], a[152:155], a[24:27]\n"
"s_waitcnt lgkmcnt(15)\n"
"ds_read_b128 a[152:155], v12 offset:30720\n"
"v_mfma_f32_16x16x32_bf16 v[32:35], a[32:35], a[96:99], v[32:35]\n"
"L_st1575:\n"
"s_cbranch_execz L_st3848\n"
"v_mfma_f32_16x16x32_bf16 v[32:35], a[36:39], a[100:103], v[32:35]\n"
"s_waitcnt lgkmcnt(2)\n"
"v_mfma_f32_16x16x32_bf16 v[60:63], a[44:47], a[100:103], v[60:63]\n"
"buffer_load_dwordx4 v11, s[52:55], s67 offen offset:0  lds\n"
"v_mfma_f32_16x16x32_bf16 v[60:63], a[40:43], a[96:99], v[60:63]\n"
"v_mfma_f32_16x16x32_bf16 v[64:67], a[40:43], a[104:107], v[64:67]\n"
"s_add_u32 s67, s75, 801792\n"
"v_mfma_f32_16x16x32_bf16 v[64:67], a[44:47], a[108:111], v[64:67]\n"
"v_mfma_f32_16x16x32_bf16 v[36:39], a[36:39], a[108:111], v[36:39]\n"
"buffer_load_dwordx4 v11, s[52:55], s67 offen offset:1024  lds\n"
"v_mfma_f32_16x16x32_bf16 v[36:39], a[32:35], a[104:107], v[36:39]\n"
"v_mfma_f32_16x16x32_bf16 v[40:43], a[32:35], a[112:115], v[40:43]\n"
"s_add_u32 s67, s75, 817152\n"
"v_mfma_f32_16x16x32_bf16 v[40:43], a[36:39], a[116:119], v[40:43]\n"
"v_mfma_f32_16x16x32_bf16 v[68:71], a[44:47], a[116:119], v[68:71]\n"
"s_mov_b32 s30, 0\n"
"v_mfma_f32_16x16x32_bf16 v[68:71], a[40:43], a[112:115], v[68:71]\n"
"buffer_load_dwordx4 v11, s[52:55], s67 offen offset:2048  lds\n"
"v_mfma_f32_16x16x32_bf16 v[72:75], a[40:43], a[120:123], v[72:75]\n"
"s_mov_b32 s31, 0\n"
"v_mfma_f32_16x16x32_bf16 v[72:75], a[44:47], a[124:127], v[72:75]\n"
"s_add_u32 s67, s75, 832512\n"
"v_mfma_f32_16x16x32_bf16 v[44:47], a[36:39], a[124:127], v[44:47]\n"
"v_mfma_f32_16x16x32_bf16 v[44:47], a[32:35], a[120:123], v[44:47]\n"
"v_mfma_f32_16x16x32_bf16 v[48:51], a[32:35], a[128:131], v[48:51]\n"
"buffer_load_dwordx4 v11, s[52:55], s67 offen offset:3072  lds\n"
"v_mfma_f32_16x16x32_bf16 v[48:51], a[36:39], a[132:135], v[48:51]\n"
"s_add_u32 m0, s64, 12288\n"
"v_mfma_f32_16x16x32_bf16 v[76:79], a[44:47], a[132:135], v[76:79]\n"
"s_waitcnt vmcnt(11)\n"
"ds_read_b64_tr_b16 a[64:65], v9 offset:0\n"
"v_mfma_f32_16x16x32_bf16 v[76:79], a[40:43], a[128:131], v[76:79]\n"
"s_add_u32 s67, s75, 851968\n"
"ds_read_b64_tr_b16 a[66:67], v9 offset:512\n"
"v_mfma_f32_16x16x32_bf16 v[80:83], a[40:43], a[136:139], v[80:83]\n"
"ds_read_b64_tr_b16 a[68:69], v9 offset:4096\n"
"v_mfma_f32_16x16x32_bf16 v[80:83], a[44:47], a[140:143], v[80:83]\n"
"ds_read_b64_tr_b16 a[70:71], v9 offset:4608\n"
"buffer_load_dwordx4 v11, s[52:55], s67 offen offset:0  lds\n"
"v_mfma_f32_16x16x32_bf16 v[52:55], a[36:39], a[140:143], v[52:55]\n"
"s_add_u32 s67, s75, 867328\n"
"ds_read_b64_tr_b16 a[72:73], v9 offset:8\n"
"v_mfma_f32_16x16x32_bf16 v[52:55], a[32:35], a[136:139], v[52:55]\n"
"s_waitcnt lgkmcnt(5)\n"
"ds_read_b64_tr_b16 a[74:75], v9 offset:520\n"
"v_mfma_f32_16x16x32_bf16 v[56:59], a[32:35], a[144:147], v[56:59]\n"
"ds_read_b64_tr_b16 a[76:77], v9 offset:4104\n"
"v_mfma_f32_16x16x32_bf16 v[56:59], a[36:39], a[148:151], v[56:59]\n"
"ds_read_b64_tr_b16 a[78:79], v9 offset:4616\n"
"v_mfma_f32_16x16x32_bf16 v[84:87], a[44:47], a[148:151], v[84:87]\n"
"buffer_load_dwordx4 v11, s[52:55], s67 offen offset:1024  lds\n"
"v_mfma_f32_16x16x32_bf16 v[84:87], a[40:43], a[144:147], v[84:87]\n"
"ds_read_b64_tr_b16 a[80:81], v15 offset:0\n"
"v_mfma_f32_16x16x32_bf16 a[4:7], a[40:43], a[152:155], a[4:7]\n"
"s_add_u32 s67, s75, 882688\n"
"ds_read_b64_tr_b16 a[82:83], v15 offset:512\n"
"v_mfma_f32_16x16x32_bf16 a[4:7], a[44:47], a[156:159], a[4:7]\n"
"ds_read_b64_tr_b16 a[84:85], v15 offset:4096\n"
"buffer_load_dwordx4 v11, s[52:55], s67 offen offset:2048  lds\n"
"v_mfma_f32_16x16x32_bf16 a[0:3], a[36:39], a[156:159], a[0:3]\n"
"s_add_u32 s96, s56, %11\n"
"ds_read_b64_tr_b16 a[86:87], v15 offset:4608\n"
"s_addc_u32 s97, s57, 0\n"
"v_mfma_f32_16x16x32_bf16 a[0:3], a[32:35], a[152:155], a[0:3]\n"
"ds_read_b64_tr_b16 a[88:89], v15 offset:8\n"
"v_mfma_f32_16x16x32_bf16 v[88:91], a[48:51], a[96:99], v[88:91]\n"
"s_add_u32 s67, s75, 898048\n"
"ds_read_b64_tr_b16 a[90:91], v15 offset:520\n"
"v_mfma_f32_16x16x32_bf16 v[88:91], a[52:55], a[100:103], v[88:91]\n"
"ds_read_b64_tr_b16 a[92:93], v15 offset:4104\n"
"v_mfma_f32_16x16x32_bf16 v[116:119], a[60:63], a[100:103], v[116:119]\n"
"ds_read_b64_tr_b16 a[94:95], v15 offset:4616\n"
"v_mfma_f32_16x16x32_bf16 v[116:119], a[56:59], a[96:99], v[116:119]\n"
"buffer_load_dwordx4 v11, s[52:55], s67 offen offset:3072  lds\n"
"v_mfma_f32_16x16x32_bf16 v[120:123], a[56:59], a[104:107], v[120:123]\n"
"v_mfma_f32_16x16x32_bf16 v[120:123], a[60:63], a[108:111], v[120:123]\n"
"s_add_u32 m0, s64, 0\n"
"v_mfma_f32_16x16x32_bf16 v[92:95], a[52:55], a[108:111], v[92:95]\n"
"v_mfma_f32_16x16x32_bf16 v[92:95], a[48:51], a[104:107], v[92:95]\n"
"s_add_u32 s67, s75, 786560\n"
"v_mfma_f32_16x16x32_bf16 v[96:99], a[48:51], a[112:115], v[96:99]\n"
"s_waitcnt lgkmcnt(0)\n"
"v_mfma_f32_16x16x32_bf16 v[96:99], a[52:55], a[116:119], v[96:99]\n"
"buffer_load_dwordx4 v11, s[52:55], s67 offen offset:0  lds\n"
"v_mfma_f32_16x16x32_bf16 v[124:127], a[60:63], a[116:119], v[124:127]\n"
"s_load_dword s98, s[96:97], 512 glc\n"
"v_mfma_f32_16x16x32_bf16 v[124:127], a[56:59], a[112:115], v[124:127]\n"
"s_load_dword s99, s[96:97], 8704 glc\n"
"s_add_u32 s67, s75, 801920\n"
"v_mfma_f32_16x16x32_bf16 v[128:131], a[56:59], a[120:123], v[128:131]\n"
"v_mfma_f32_16x16x32_bf16 v[128:131], a[60:63], a[124:127], v[128:131]\n"
"buffer_load_dwordx4 v11, s[52:55], s67 offen offset:1024  lds\n"
"v_mfma_f32_16x16x32_bf16 v[100:103], a[52:55], a[124:127], v[100:103]\n"
"v_mfma_f32_16x16x32_bf16 v[100:103], a[48:51], a[120:123], v[100:103]\n"
"s_add_u32 s67, s75, 817280\n"
"v_mfma_f32_16x16x32_bf16 v[104:107], a[48:51], a[128:131], v[104:107]\n"
"v_mfma_f32_16x16x32_bf16 v[104:107], a[52:55], a[132:135], v[104:107]\n"
"v_mfma_f32_16x16x32_bf16 v[132:135], a[60:63], a[132:135], v[132:135]\n"
"buffer_load_dwordx4 v11, s[52:55], s67 offen offset:2048  lds\n"
"v_mfma_f32_16x16x32_bf16 v[132:135], a[56:59], a[128:131], v[132:135]\n"
"v_mfma_f32_16x16x32_bf16 v[136:139], a[56:59], a[136:139], v[136:139]\n"
"s_add_u32 s67, s75, 832640\n"
"v_mfma_f32_16x16x32_bf16 v[136:139], a[60:63], a[140:143], v[136:139]\n"
"v_mfma_f32_16x16x32_bf16 v[108:111], a[52:55], a[140:143], v[108:111]\n"
"buffer_load_dwordx4 v11, s[52:55], s67 offen offset:3072  lds\n"
"v_mfma_f32_16x16x32_bf16 v[108:111], a[48:51], a[136:139], v[108:111]\n"
"v_mfma_f32_16x16x32_bf16 v[112:115], a[48:51], a[144:147], v[112:115]\n"
"s_add_u32 m0, s64, 4096\n"
"v_mfma_f32_16x16x32_bf16 v[112:115], a[52:55], a[148:151], v[112:115]\n"
"v_mfma_f32_16x16x32_bf16 v[140:143], a[60:63], a[148:151], v[140:143]\n"
"s_add_u32 s67, s75, 852096\n"
"v_mfma_f32_16x16x32_bf16 v[140:143], a[56:59], a[144:147], v[140:143]\n"
"v_mfma_f32_16x16x32_bf16 a[12:15], a[56:59], a[152:155], a[12:15]\n"
"buffer_load_dwordx4 v11, s[52:55], s67 offen offset:0  lds\n"
"v_mfma_f32_16x16x32_bf16 a[12:15], a[60:63], a[156:159], a[12:15]\n"
"v_mfma_f32_16x16x32_bf16 a[8:11], a[52:55], a[156:159], a[8:11]\n"
"s_add_u32 s67, s75, 867456\n"
"v_mfma_f32_16x16x32_bf16 a[8:11], a[48:51], a[152:155], a[8:11]\n"
"v_mfma_f32_16x16x32_bf16 v[144:147], a[64:67], a[96:99], v[144:147]\n"
"buffer_load_dwordx4 v11, s[52:55], s67 offen offset:1024  lds\n"
"v_mfma_f32_16x16x32_bf16 v[144:147], a[68:71], a[100:103], v[144:147]\n"
"v_mfma_f32_16x16x32_bf16 v[172:175], a[76:79], a[100:103], v[172:175]\n"
"s_add_u32 s67, s75, 882816\n"
"v_mfma_f32_16x16x32_bf16 v[172:175], a[72:75], a[96:99], v[172:175]\n"
"v_mfma_f32_16x16x32_bf16 v[176:179], a[72:75], a[104:107], v[176:179]\n"
"buffer_load_dwordx4 v11, s[52:55], s67 offen offset:2048  lds\n"
"v_mfma_f32_16x16x32_bf16 v[176:179], a[76:79], a[108:111], v[176:179]\n"
"v_mfma_f32_16x16x32_bf16 v[148:151], a[68:71], a[108:111], v[148:151]\n"
"s_add_u32 s67, s75, 898176\n"
"v_mfma_f32_16x16x32_bf16 v[148:151], a[64:67], a[104:107], v[148:151]\n"
"v_mfma_f32_16x16x32_bf16 v[152:155], a[64:67], a[112:115], v[152:155]\n"
"s_sub_u32 s66, 81920, s73\n"
"v_mfma_f32_16x16x32_bf16 v[152:155], a[68:71], a[116:119], v[152:155]\n"
"buffer_load_dwordx4 v11, s[52:55], s67 offen offset:3072  lds\n"
"v_mfma_f32_16x16x32_bf16 v[180:183], a[76:79], a[116:119], v[180:183]\n"
"s_add_u32 m0, s72, 139264\n"
"v_add_u32 v12, s66, v7\n"
"v_mfma_f32_16x16x32_bf16 v[180:183], a[72:75], a[112:115], v[180:183]\n"
"v_mfma_f32_16x16x32_bf16 v[184:187], a[72:75], a[120:123], v[184:187]\n"
"s_add_u32 s67, s60, 896\n"
"v_add_u32 v13, s66, v8\n"
"v_mfma_f32_16x16x32_bf16 v[184:187], a[76:79], a[124:127], v[184:187]\n"
"v_mfma_f32_16x16x32_bf16 v[156:159], a[68:71], a[124:127], v[156:159]\n"
"buffer_load_dwordx4 v6, s[48:51], s67 offen offset:0  lds\n"
"v_mfma_f32_16x16x32_bf16 v[156:159], a[64:67], a[120:123], v[156:159]\n"
"v_mfma_f32_16x16x32_bf16 v[160:163], a[64:67], a[128:131], v[160:163]\n"
"s_add_u32 s67, s60, 16256\n"
"v_mfma_f32_16x16x32_bf16 v[160:163], a[68:71], a[132:135], v[160:163]\n"
"v_mfma_f32_16x16x32_bf16 v[188:191], a[76:79], a[132:135], v[188:191]\n"
"buffer_load_dwordx4 v6, s[48:51], s67 offen offset:1024  lds\n"
"v_mfma_f32_16x16x32_bf16 v[188:191], a[72:75], a[128:131], v[188:191]\n"
"v_mfma_f32_16x16x32_bf16 v[192:195], a[72:75], a[136:139], v[192:195]\n"
"s_add_u32 s67, s60, 31616\n"
"v_mfma_f32_16x16x32_bf16 v[192:195], a[76:79], a[140:143], v[192:195]\n"
"v_mfma_f32_16x16x32_bf16 v[164:167], a[68:71], a[140:143], v[164:167]\n"
"v_mfma_f32_16x16x32_bf16 v[164:167], a[64:67], a[136:139], v[164:167]\n"
"buffer_load_dwordx4 v6, s[48:51], s67 offen offset:2048  lds\n"
"v_mfma_f32_16x16x32_bf16 v[168:171], a[64:67], a[144:147], v[168:171]\n"
"v_mfma_f32_16x16x32_bf16 v[168:171], a[68:71], a[148:151], v[168:171]\n"
"s_add_u32 s67, s60, 46976\n"
"v_mfma_f32_16x16x32_bf16 v[196:199], a[76:79], a[148:151], v[196:199]\n"
"v_mfma_f32_16x16x32_bf16 v[196:199], a[72:75], a[144:147], v[196:199]\n"
"buffer_load_dwordx4 v6, s[48:51], s67 offen offset:3072  lds\n"
"v_mfma_f32_16x16x32_bf16 a[20:23], a[72:75], a[152:155], a[20:23]\n"
"v_mfma_f32_16x16x32_bf16 a[20:23], a[76:79], a[156:159], a[20:23]\n"
"s_waitcnt lgkmcnt(0)\n"
"v_mfma_f32_16x16x32_bf16 a[16:19], a[68:71], a[156:159], a[16:19]\n"
"v_mfma_f32_16x16x32_bf16 a[16:19], a[64:67], a[152:155], a[16:19]\n"
"s_waitcnt vmcnt(23)\n"
"s_barrier\n"
"v_mfma_f32_16x16x32_bf16 v[200:203], a[80:83], a[96:99], v[200:203]\n"
"s_waitcnt vmcnt(12)\n"
"ds_read_b64_tr_b16 a[32:33], v9 offset:8192\n"
"v_mfma_f32_16x16x32_bf16 v[200:203], a[84:87], a[100:103], v[200:203]\n"
"ds_read_b64_tr_b16 a[34:35], v9 offset:8704\n"
"v_mfma_f32_16x16x32_bf16 v[228:231], a[92:95], a[100:103], v[228:231]\n"
"s_add_u32 m0, s64, 8192\n"
"ds_read_b128 a[100:103], v13 offset:16384\n"
"v_mfma_f32_16x16x32_bf16 v[228:231], a[88:91], a[96:99], v[228:231]\n"
"ds_read_b128 a[96:99], v12 offset:16384\n"
"v_mfma_f32_16x16x32_bf16 v[232:235], a[88:91], a[104:107], v[232:235]\n"
"ds_read_b64_tr_b16 a[36:37], v9 offset:12288\n"
"v_mfma_f32_16x16x32_bf16 v[232:235], a[92:95], a[108:111], v[232:235]\n"
"ds_read_b64_tr_b16 a[38:39], v9 offset:12800\n"
"v_mfma_f32_16x16x32_bf16 v[204:207], a[84:87], a[108:111], v[204:207]\n"
"ds_read_b128 a[108:111], v13 offset:18432\n"
"v_mfma_f32_16x16x32_bf16 v[204:207], a[80:83], a[104:107], v[204:207]\n"
"ds_read_b128 a[104:107], v12 offset:18432\n"
"ds_read_b64_tr_b16 a[40:41], v9 offset:8200\n"
"v_mfma_f32_16x16x32_bf16 v[208:211], a[80:83], a[112:115], v[208:211]\n"
"ds_read_b64_tr_b16 a[42:43], v9 offset:8712\n"
"v_mfma_f32_16x16x32_bf16 v[208:211], a[84:87], a[116:119], v[208:211]\n"
"ds_read_b64_tr_b16 a[44:45], v9 offset:12296\n"
"v_mfma_f32_16x16x32_bf16 v[236:239], a[92:95], a[116:119], v[236:239]\n"
"ds_read_b128 a[116:119], v13 offset:20480\n"
"v_mfma_f32_16x16x32_bf16 v[236:239], a[88:91], a[112:115], v[236:239]\n"
"ds_read_b128 a[112:115], v12 offset:20480\n"
"v_mfma_f32_16x16x32_bf16 v[240:243], a[88:91], a[120:123], v[240:243]\n"
"ds_read_b64_tr_b16 a[46:47], v9 offset:12808\n"
"v_mfma_f32_16x16x32_bf16 v[240:243], a[92:95], a[124:127], v[240:243]\n"
"ds_read_b64_tr_b16 a[48:49], v15 offset:8192\n"
"v_mfma_f32_16x16x32_bf16 v[212:215], a[84:87], a[124:127], v[212:215]\n"
"ds_read_b128 a[124:127], v13 offset:22528\n"
"v_mfma_f32_16x16x32_bf16 v[212:215], a[80:83], a[120:123], v[212:215]\n"
"ds_read_b128 a[120:123], v12 offset:22528\n"
"v_mfma_f32_16x16x32_bf16 v[216:219], a[80:83], a[128:131], v[216:219]\n"
"s_add_u32 s67, s75, 917504\n"
"ds_read_b64_tr_b16 a[50:51], v15 offset:8704\n"
"v_mfma_f32_16x16x32_bf16 v[216:219], a[84:87], a[132:135], v[216:219]\n"
"ds_read_b64_tr_b16 a[52:53], v15 offset:12288\n"
"v_mfma_f32_16x16x32_bf16 v[244:247], a[92:95], a[132:135], v[244:247]\n"
"ds_read_b128 a[132:135], v13 offset:24576\n"
"v_mfma_f32_16x16x32_bf16 v[244:247], a[88:91], a[128:131], v[244:247]\n"
"ds_read_b128 a[128:131], v12 offset:24576\n"
"v_mfma_f32_16x16x32_bf16 v[248:251], a[88:91], a[136:139], v[248:251]\n"
"ds_read_b64_tr_b16 a[54:55], v15 offset:12800\n"
"v_mfma_f32_16x16x32_bf16 v[248:251], a[92:95], a[140:143], v[248:251]\n"
"ds_read_b64_tr_b16 a[56:57], v15 offset:8200\n"
"v_mfma_f32_16x16x32_bf16 v[220:223], a[84:87], a[140:143], v[220:223]\n"
"ds_read_b128 a[140:143], v13 offset:26624\n"
"v_mfma_f32_16x16x32_bf16 v[220:223], a[80:83], a[136:139], v[220:223]\n"
"ds_read_b128 a[136:139], v12 offset:26624\n"
"v_mfma_f32_16x16x32_bf16 v[224:227], a[80:83], a[144:147], v[224:227]\n"
"ds_read_b64_tr_b16 a[58:59], v15 offset:8712\n"
"v_mfma_f32_16x16x32_bf16 v[224:227], a[84:87], a[148:151], v[224:227]\n"
"ds_read_b64_tr_b16 a[60:61], v15 offset:12296\n"
"v_mfma_f32_16x16x32_bf16 v[252:255], a[92:95], a[148:151], v[252:255]\n"
"ds_read_b128 a[148:151], v13 offset:28672\n"
"v_mfma_f32_16x16x32_bf16 v[252:255], a[88:91], a[144:147], v[252:255]\n"
"ds_read_b128 a[144:147], v12 offset:28672\n"
"ds_read_b64_tr_b16 a[62:63], v15 offset:12808\n"
"v_mfma_f32_16x16x32_bf16 a[28:31], a[88:91], a[152:155], a[28:31]\n"
"v_mfma_f32_16x16x32_bf16 a[28:31], a[92:95], a[156:159], a[28:31]\n"
"v_mfma_f32_16x16x32_bf16 a[24:27], a[84:87], a[156:159], a[24:27]\n"
"ds_read_b128 a[156:159], v13 offset:30720\n"
"v_mfma_f32_16x16x32_bf16 a[24:27], a[80:83], a[152:155], a[24:27]\n"
"s_waitcnt lgkmcnt(15)\n"
"ds_read_b128 a[152:155], v12 offset:30720\n"
"v_mfma_f32_16x16x32_bf16 v[32:35], a[32:35], a[96:99], v[32:35]\n"
"v_mfma_f32_16x16x32_bf16 v[32:35], a[36:39], a[100:103], v[32:35]\n"
"s_waitcnt lgkmcnt(2)\n"
"L_H6S:\n"
"v_mfma_f32_16x16x32_bf16 v[60:63], a[44:47], a[100:103], v[60:63]\n"
"buffer_load_dwordx4 v11, s[52:55], s67 offen offset:0  lds\n"
"v_mfma_f32_16x16x32_bf16 v[60:63], a[40:43], a[96:99], v[60:63]\n"
"v_mfma_f32_16x16x32_bf16 v[64:67], a[40:43], a[104:107], v[64:67]\n"
"s_add_u32 s67, s75, 932864\n"
"v_mfma_f32_16x16x32_bf16 v[64:67], a[44:47], a[108:111], v[64:67]\n"
"v_mfma_f32_16x16x32_bf16 v[36:39], a[36:39], a[108:111], v[36:39]\n"
"buffer_load_dwordx4 v11, s[52:55], s67 offen offset:1024  lds\n"
"v_mfma_f32_16x16x32_bf16 v[36:39], a[32:35], a[104:107], v[36:39]\n"
"v_mfma_f32_16x16x32_bf16 v[40:43], a[32:35], a[112:115], v[40:43]\n"
"s_add_u32 s67, s75, 948224\n"
"v_mfma_f32_16x16x32_bf16 v[40:43], a[36:39], a[116:119], v[40:43]\n"
"v_mfma_f32_16x16x32_bf16 v[68:71], a[44:47], a[116:119], v[68:71]\n"
"v_mfma_f32_16x16x32_bf16 v[68:71], a[40:43], a[112:115], v[68:71]\n"
"buffer_load_dwordx4 v11, s[52:55], s67 offen offset:2048  lds\n"
"v_mfma_f32_16x16x32_bf16 v[72:75], a[40:43], a[120:123], v[72:75]\n"
"v_mfma_f32_16x16x32_bf16 v[72:75], a[44:47], a[124:127], v[72:75]\n"
"s_add_u32 s67, s75, 963584\n"
"v_mfma_f32_16x16x32_bf16 v[44:47], a[36:39], a[124:127], v[44:47]\n"
"v_mfma_f32_16x16x32_bf16 v[44:47], a[32:35], a[120:123], v[44:47]\n"
"v_mfma_f32_16x16x32_bf16 v[48:51], a[32:35], a[128:131], v[48:51]\n"
"buffer_load_dwordx4 v11, s[52:55], s67 offen offset:3072  lds\n"
"v_mfma_f32_16x16x32_bf16 v[48:51], a[36:39], a[132:135], v[48:51]\n"
"s_add_u32 m0, s64, 12288\n"
"v_mfma_f32_16x16x32_bf16 v[76:79], a[44:47], a[132:135], v[76:79]\n"
"s_waitcnt vmcnt(8)\n"
"ds_read_b64_tr_b16 a[64:65], v9 offset:0\n"
"v_mfma_f32_16x16x32_bf16 v[76:79], a[40:43], a[128:131], v[76:79]\n"
"s_add_u32 s67, s75, 983040\n"
"ds_read_b64_tr_b16 a[66:67], v9 offset:512\n"
"v_mfma_f32_16x16x32_bf16 v[80:83], a[40:43], a[136:139], v[80:83]\n"
"ds_read_b64_tr_b16 a[68:69], v9 offset:4096\n"
"v_mfma_f32_16x16x32_bf16 v[80:83], a[44:47], a[140:143], v[80:83]\n"
"ds_read_b64_tr_b16 a[70:71], v9 offset:4608\n"
"buffer_load_dwordx4 v11, s[52:55], s67 offen offset:0  lds\n"
"v_mfma_f32_16x16x32_bf16 v[52:55], a[36:39], a[140:143], v[52:55]\n"
"s_add_u32 s67, s75, 998400\n"
"ds_read_b64_tr_b16 a[72:73], v9 offset:8\n"
"v_mfma_f32_16x16x32_bf16 v[52:55], a[32:35], a[136:139], v[52:55]\n"
"s_waitcnt lgkmcnt(5)\n"
"ds_read_b64_tr_b16 a[74:75], v9 offset:520\n"
"v_mfma_f32_16x16x32_bf16 v[56:59], a[32:35], a[144:147], v[56:59]\n"
"ds_read_b64_tr_b16 a[76:77], v9 offset:4104\n"
"v_mfma_f32_16x16x32_bf16 v[56:59], a[36:39], a[148:151], v[56:59]\n"
"ds_read_b64_tr_b16 a[78:79], v9 offset:4616\n"
"v_mfma_f32_16x16x32_bf16 v[84:87], a[44:47], a[148:151], v[84:87]\n"
"buffer_load_dwordx4 v11, s[52:55], s67 offen offset:1024  lds\n"
"v_mfma_f32_16x16x32_bf16 v[84:87], a[40:43], a[144:147], v[84:87]\n"
"ds_read_b64_tr_b16 a[80:81], v15 offset:0\n"
"v_mfma_f32_16x16x32_bf16 a[4:7], a[40:43], a[152:155], a[4:7]\n"
"s_add_u32 s67, s75, 1013760\n"
"ds_read_b64_tr_b16 a[82:83], v15 offset:512\n"
"v_mfma_f32_16x16x32_bf16 a[4:7], a[44:47], a[156:159], a[4:7]\n"
"ds_read_b64_tr_b16 a[84:85], v15 offset:4096\n"
"v_mfma_f32_16x16x32_bf16 a[0:3], a[36:39], a[156:159], a[0:3]\n"
"ds_read_b64_tr_b16 a[86:87], v15 offset:4608\n"
"buffer_load_dwordx4 v11, s[52:55], s67 offen offset:2048  lds\n"
"v_mfma_f32_16x16x32_bf16 a[0:3], a[32:35], a[152:155], a[0:3]\n"
"ds_read_b64_tr_b16 a[88:89], v15 offset:8\n"
"v_mfma_f32_16x16x32_bf16 v[88:91], a[48:51], a[96:99], v[88:91]\n"
"s_add_u32 s67, s75, 1029120\n"
"ds_read_b64_tr_b16 a[90:91], v15 offset:520\n"
"v_mfma_f32_16x16x32_bf16 v[88:91], a[52:55], a[100:103], v[88:91]\n"
"ds_read_b64_tr_b16 a[92:93], v15 offset:4104\n"
"v_mfma_f32_16x16x32_bf16 v[116:119], a[60:63], a[100:103], v[116:119]\n"
"ds_read_b64_tr_b16 a[94:95], v15 offset:4616\n"
"v_mfma_f32_16x16x32_bf16 v[116:119], a[56:59], a[96:99], v[116:119]\n"
"buffer_load_dwordx4 v11, s[52:55], s67 offen offset:3072  lds\n"
"v_mfma_f32_16x16x32_bf16 v[120:123], a[56:59], a[104:107], v[120:123]\n"
"v_mfma_f32_16x16x32_bf16 v[120:123], a[60:63], a[108:111], v[120:123]\n"
"s_add_u32 m0, s64, 0\n"
"v_mfma_f32_16x16x32_bf16 v[92:95], a[52:55], a[108:111], v[92:95]\n"
"v_mfma_f32_16x16x32_bf16 v[92:95], a[48:51], a[104:107], v[92:95]\n"
"s_add_u32 s67, s75, 917632\n"
"v_mfma_f32_16x16x32_bf16 v[96:99], a[48:51], a[112:115], v[96:99]\n"
"s_waitcnt lgkmcnt(0)\n"
"s_mov_b32 s99, 0\n"
"v_mfma_f32_16x16x32_bf16 v[96:99], a[52:55], a[116:119], v[96:99]\n"
"s_mov_b32 s98, 0\n"
"buffer_load_dwordx4 v11, s[52:55], s67 offen offset:0  lds\n"
"v_mfma_f32_16x16x32_bf16 v[124:127], a[60:63], a[116:119], v[124:127]\n"
"s_memtime s[76:77]\n"
"s_load_dword s98, s[96:97], 8192 glc\n"
"v_mfma_f32_16x16x32_bf16 v[124:127], a[56:59], a[112:115], v[124:127]\n"
"s_add_u32 s67, s75, 932992\n"
"v_mfma_f32_16x16x32_bf16 v[128:131], a[56:59], a[120:123], v[128:131]\n"
"v_mfma_f32_16x16x32_bf16 v[128:131], a[60:63], a[124:127], v[128:131]\n"
"buffer_load_dwordx4 v11, s[52:55], s67 offen offset:1024  lds\n"
"v_mfma_f32_16x16x32_bf16 v[100:103], a[52:55], a[124:127], v[100:103]\n"
"v_mfma_f32_16x16x32_bf16 v[100:103], a[48:51], a[120:123], v[100:103]\n"
"s_add_u32 s67, s75, 948352\n"
"v_mfma_f32_16x16x32_bf16 v[104:107], a[48:51], a[128:131], v[104:107]\n"
"v_mfma_f32_16x16x32_bf16 v[104:107], a[52:55], a[132:135], v[104:107]\n"
"v_mfma_f32_16x16x32_bf16 v[132:135], a[60:63], a[132:135], v[132:135]\n"
"buffer_load_dwordx4 v11, s[52:55], s67 offen offset:2048  lds\n"
"v_mfma_f32_16x16x32_bf16 v[132:135], a[56:59], a[128:131], v[132:135]\n"
"s_mov_b32 s29, s76\n"
"v_mfma_f32_16x16x32_bf16 v[136:139], a[56:59], a[136:139], v[136:139]\n"
"s_add_u32 s67, s75, 963712\n"
"v_mfma_f32_16x16x32_bf16 v[136:139], a[60:63], a[140:143], v[136:139]\n"
"buffer_load_dwordx4 v11, s[52:55], s67 offen offset:3072  lds\n"
"v_mfma_f32_16x16x32_bf16 v[108:111], a[52:55], a[140:143], v[108:111]\n"
"s_sub_u32 s99, s99, s98\n"
"s_sub_u32 s99, s99, s98\n"
"v_mfma_f32_16x16x32_bf16 v[108:111], a[48:51], a[136:139], v[108:111]\n"
"s_sub_u32 s99, s99, s98\n"
"s_add_u32 m0, s64, 4096\n"
"v_mfma_f32_16x16x32_bf16 v[112:115], a[48:51], a[144:147], v[112:115]\n"
"s_sub_u32 s99, s99, s98\n"
"v_mfma_f32_16x16x32_bf16 v[112:115], a[52:55], a[148:151], v[112:115]\n"
"s_add_u32 s67, s75, 983168\n"
"v_mfma_f32_16x16x32_bf16 v[140:143], a[60:63], a[148:151], v[140:143]\n"
"s_sub_u32 s99, s99, s98\n"
"s_sub_u32 s99, s99, s98\n"
"v_mfma_f32_16x16x32_bf16 v[140:143], a[56:59], a[144:147], v[140:143]\n"
"s_sub_u32 s99, s99, s98\n"
"buffer_load_dwordx4 v11, s[52:55], s67 offen offset:0  lds\n"
"v_mfma_f32_16x16x32_bf16 a[12:15], a[56:59], a[152:155], a[12:15]\n"
"s_sub_u32 s99, s99, s98\n"
"v_mfma_f32_16x16x32_bf16 a[12:15], a[60:63], a[156:159], a[12:15]\n"
"s_add_u32 s67, s75, 998528\n"
"v_mfma_f32_16x16x32_bf16 a[8:11], a[52:55], a[156:159], a[8:11]\n"
"s_sub_u32 s99, s99, s98\n"
"s_sub_u32 s99, s99, s98\n"
"v_mfma_f32_16x16x32_bf16 a[8:11], a[48:51], a[152:155], a[8:11]\n"
"s_sub_u32 s99, s99, s98\n"
"v_mfma_f32_16x16x32_bf16 v[144:147], a[64:67], a[96:99], v[144:147]\n"
"s_sub_u32 s99, s99, s98\n"
"v_mfma_f32_16x16x32_bf16 v[144:147], a[68:71], a[100:103], v[144:147]\n"
"buffer_load_dwordx4 v11, s[52:55], s67 offen offset:1024  lds\n"
"v_mfma_f32_16x16x32_bf16 v[172:175], a[76:79], a[100:103], v[172:175]\n"
"s_sub_u32 s99, s99, s98\n"
"s_sub_u32 s99, s99, s98\n"
"v_mfma_f32_16x16x32_bf16 v[172:175], a[72:75], a[96:99], v[172:175]\n"
"s_sub_u32 s99, s99, s98\n"
"s_add_u32 s67, s75, 1013888\n"
"v_mfma_f32_16x16x32_bf16 v[176:179], a[72:75], a[104:107], v[176:179]\n"
"s_sub_u32 s99, s99, s98\n"
"v_mfma_f32_16x16x32_bf16 v[176:179], a[76:79], a[108:111], v[176:179]\n"
"buffer_load_dwordx4 v11, s[52:55], s67 offen offset:2048  lds\n"
"v_mfma_f32_16x16x32_bf16 v[148:151], a[68:71], a[108:111], v[148:151]\n"
"s_sub_u32 s99, s99, s98\n"
"s_sub_u32 s99, s99, s98\n"
"v_mfma_f32_16x16x32_bf16 v[148:151], a[64:67], a[104:107], v[148:151]\n"
"s_sub_u32 s99, s99, s98\n"
"s_add_u32 s67, s75, 1029248\n"
"v_mfma_f32_16x16x32_bf16 v[152:155], a[64:67], a[112:115], v[152:155]\n"
"s_sub_u32 s66, 122880, s73\n"
"s_sub_u32 s99, s99, s98\n"
"v_mfma_f32_16x16x32_bf16 v[152:155], a[68:71], a[116:119], v[152:155]\n"
"buffer_load_dwordx4 v11, s[52:55], s67 offen offset:3072  lds\n"
"v_mfma_f32_16x16x32_bf16 v[180:183], a[76:79], a[116:119], v[180:183]\n"
"s_sub_u32 s99, s99, s98\n"
"v_add_u32 v12, s66, v7\n"
"s_sub_u32 s99, s99, s98\n"
"v_mfma_f32_16x16x32_bf16 v[180:183], a[72:75], a[112:115], v[180:183]\n"
"s_sub_u32 s99, s99, s98\n"
"s_cmp_lt_u32 %16, 2\n"
"s_cselect_b32 s67, 16384, 0\n"
"v_mfma_f32_16x16x32_bf16 v[184:187], a[72:75], a[120:123], v[184:187]\n"
"s_sub_u32 s99, s99, s98\n"
"v_add_u32 v13, s66, v8\n"
"s_add_u32 s67, s67, s72\n"
"v_mfma_f32_16x16x32_bf16 v[184:187], a[76:79], a[124:127], v[184:187]\n"
"s_memtime s[76:77]\n"
"s_add_u32 m0, s67, 16384\n"
"s_sub_u32 s67, 1024, s60\n"
"v_mfma_f32_16x16x32_bf16 v[156:159], a[68:71], a[124:127], v[156:159]\n"
"buffer_load_dwordx4 v6, s[48:51], s67 offen offset:0  lds\n"
"v_mfma_f32_16x16x32_bf16 v[156:159], a[64:67], a[120:123], v[156:159]\n"
"s_waitcnt lgkmcnt(0)\n"
"v_mfma_f32_16x16x32_bf16 v[160:163], a[64:67], a[128:131], v[160:163]\n"
"s_sub_u32 s29, s76, s29\n"
"s_sub_u32 s67, 16384, s60\n"
"v_mfma_f32_16x16x32_bf16 v[160:163], a[68:71], a[132:135], v[160:163]\n"
"s_cmp_lt_u32 s29, 620\n"
"s_cselect_b32 s99, s99, 0\n"
"v_mfma_f32_16x16x32_bf16 v[188:191], a[76:79], a[132:135], v[188:191]\n"
"s_max_u32 s31, s31, s99\n"
"v_mfma_f32_16x16x32_bf16 v[188:191], a[72:75], a[128:131], v[188:191]\n"
"buffer_load_dwordx4 v6, s[48:51], s67 offen offset:1024  lds\n"
"v_mfma_f32_16x16x32_bf16 v[192:195], a[72:75], a[136:139], v[192:195]\n"
"v_mfma_f32_16x16x32_bf16 v[192:195], a[76:79], a[140:143], v[192:195]\n"
"s_sub_u32 s67, 31744, s60\n"
"v_mfma_f32_16x16x32_bf16 v[164:167], a[68:71], a[140:143], v[164:167]\n"
"v_mfma_f32_16x16x32_bf16 v[164:167], a[64:67], a[136:139], v[164:167]\n"
"buffer_load_dwordx4 v6, s[48:51], s67 offen offset:2048  lds\n"
"v_mfma_f32_16x16x32_bf16 v[168:171], a[64:67], a[144:147], v[168:171]\n"
"v_mfma_f32_16x16x32_bf16 v[168:171], a[68:71], a[148:151], v[168:171]\n"
"s_sub_u32 s67, 47104, s60\n"
"v_mfma_f32_16x16x32_bf16 v[196:199], a[76:79], a[148:151], v[196:199]\n"
"v_mfma_f32_16x16x32_bf16 v[196:199], a[72:75], a[144:147], v[196:199]\n"
"buffer_load_dwordx4 v6, s[48:51], s67 offen offset:3072  lds\n"
"v_mfma_f32_16x16x32_bf16 a[20:23], a[72:75], a[152:155], a[20:23]\n"
"v_mfma_f32_16x16x32_bf16 a[20:23], a[76:79], a[156:159], a[20:23]\n"
"s_add_u32 m0, s72, 57216\n"
"v_mfma_f32_16x16x32_bf16 a[16:19], a[68:71], a[156:159], a[16:19]\n"
"v_mfma_f32_16x16x32_bf16 a[16:19], a[64:67], a[152:155], a[16:19]\n"
"s_waitcnt vmcnt(20)\n"
"s_barrier\n"
"v_mfma_f32_16x16x32_bf16 v[200:203], a[80:83], a[96:99], v[200:203]\n"
"s_waitcnt vmcnt(12)\n"
"ds_read_b64_tr_b16 a[32:33], v9 offset:8192\n"
"buffer_load_dwordx4 v6, s[40:43], s80 offen offset:128  lds\n"
"v_mfma_f32_16x16x32_bf16 v[200:203], a[84:87], a[100:103], v[200:203]\n"
"ds_read_b64_tr_b16 a[34:35], v9 offset:8704\n"
"v_mfma_f32_16x16x32_bf16 v[228:231], a[92:95], a[100:103], v[228:231]\n"
"ds_read_b128 a[100:103], v13 offset:16384\n"
"v_mfma_f32_16x16x32_bf16 v[228:231], a[88:91], a[96:99], v[228:231]\n"
"ds_read_b128 a[96:99], v12 offset:16384\n"
"buffer_load_dwordx4 v6, s[40:43], s81 offen offset:1152  lds\n"
"v_mfma_f32_16x16x32_bf16 v[232:235], a[88:91], a[104:107], v[232:235]\n"
"ds_read_b64_tr_b16 a[36:37], v9 offset:12288\n"
"v_mfma_f32_16x16x32_bf16 v[232:235], a[92:95], a[108:111], v[232:235]\n"
"ds_read_b64_tr_b16 a[38:39], v9 offset:12800\n"
"v_mfma_f32_16x16x32_bf16 v[204:207], a[84:87], a[108:111], v[204:207]\n"
"ds_read_b128 a[108:111], v13 offset:18432\n"
"v_mfma_f32_16x16x32_bf16 v[204:207], a[80:83], a[104:107], v[204:207]\n"
"ds_read_b128 a[104:107], v12 offset:18432\n"
"ds_read_b64_tr_b16 a[40:41], v9 offset:8200\n"
"v_mfma_f32_16x16x32_bf16 v[208:211], a[80:83], a[112:115], v[208:211]\n"
"ds_read_b64_tr_b16 a[42:43], v9 offset:8712\n"
"buffer_load_dwordx4 v6, s[40:43], s82 offen offset:2176  lds\n"
"v_mfma_f32_16x16x32_bf16 v[208:211], a[84:87], a[116:119], v[208:211]\n"
"ds_read_b64_tr_b16 a[44:45], v9 offset:12296\n"
"v_mfma_f32_16x16x32_bf16 v[236:239], a[92:95], a[116:119], v[236:239]\n"
"ds_read_b128 a[116:119], v13 offset:20480\n"
"v_mfma_f32_16x16x32_bf16 v[236:239], a[88:91], a[112:115], v[236:239]\n"
"ds_read_b128 a[112:115], v12 offset:20480\n"
"v_mfma_f32_16x16x32_bf16 v[240:243], a[88:91], a[120:123], v[240:243]\n"
"ds_read_b64_tr_b16 a[46:47], v9 offset:12808\n"
"v_mfma_f32_16x16x32_bf16 v[240:243], a[92:95], a[124:127], v[240:243]\n"
"ds_read_b64_tr_b16 a[48:49], v15 offset:8192\n"
"buffer_load_dwordx4 v6, s[40:43], s83 offen offset:3200  lds\n"
"v_mfma_f32_16x16x32_bf16 v[212:215], a[84:87], a[124:127], v[212:215]\n"
"ds_read_b128 a[124:127], v13 offset:22528\n"
"v_mfma_f32_16x16x32_bf16 v[212:215], a[80:83], a[120:123], v[212:215]\n"
"ds_read_b128 a[120:123], v12 offset:22528\n"
"v_mfma_f32_16x16x32_bf16 v[216:219], a[80:83], a[128:131], v[216:219]\n"
"ds_read_b64_tr_b16 a[50:51], v15 offset:8704\n"
"v_mfma_f32_16x16x32_bf16 v[216:219], a[84:87], a[132:135], v[216:219]\n"
"s_add_u32 m0, s64, 8192\n"
"ds_read_b64_tr_b16 a[52:53], v15 offset:12288\n"
"v_mfma_f32_16x16x32_bf16 v[244:247], a[92:95], a[132:135], v[244:247]\n"
"ds_read_b128 a[132:135], v13 offset:24576\n"
"v_mfma_f32_16x16x32_bf16 v[244:247], a[88:91], a[128:131], v[244:247]\n"
"ds_read_b128 a[128:131], v12 offset:24576\n"
"v_mfma_f32_16x16x32_bf16 v[248:251], a[88:91], a[136:139], v[248:251]\n"
"ds_read_b64_tr_b16 a[54:55], v15 offset:12800\n"
"v_mfma_f32_16x16x32_bf16 v[248:251], a[92:95], a[140:143], v[248:251]\n"
"ds_read_b64_tr_b16 a[56:57], v15 offset:8200\n"
"v_mfma_f32_16x16x32_bf16 v[220:223], a[84:87], a[140:143], v[220:223]\n"
"ds_read_b128 a[140:143], v13 offset:26624\n"
"v_mfma_f32_16x16x32_bf16 v[220:223], a[80:83], a[136:139], v[220:223]\n"
"ds_read_b128 a[136:139], v12 offset:26624\n"
"v_mfma_f32_16x16x32_bf16 v[224:227], a[80:83], a[144:147], v[224:227]\n"
"ds_read_b64_tr_b16 a[58:59], v15 offset:8712\n"
"v_mfma_f32_16x16x32_bf16 v[224:227], a[84:87], a[148:151], v[224:227]\n"
"s_sub_u32 s67, 1048576, s75\n"
"ds_read_b64_tr_b16 a[60:61], v15 offset:12296\n"
"v_mfma_f32_16x16x32_bf16 v[252:255], a[92:95], a[148:151], v[252:255]\n"
"ds_read_b128 a[148:151], v13 offset:28672\n"
"v_mfma_f32_16x16x32_bf16 v[252:255], a[88:91], a[144:147], v[252:255]\n"
"ds_read_b128 a[144:147], v12 offset:28672\n"
"ds_read_b64_tr_b16 a[62:63], v15 offset:12808\n"
"v_mfma_f32_16x16x32_bf16 a[28:31], a[88:91], a[152:155], a[28:31]\n"
"v_mfma_f32_16x16x32_bf16 a[28:31], a[92:95], a[156:159], a[28:31]\n"
"v_mfma_f32_16x16x32_bf16 a[24:27], a[84:87], a[156:159], a[24:27]\n"
"ds_read_b128 a[156:159], v13 offset:30720\n"
"v_mfma_f32_16x16x32_bf16 a[24:27], a[80:83], a[152:155], a[24:27]\n"
"s_waitcnt lgkmcnt(15)\n"
"ds_read_b128 a[152:155], v12 offset:30720\n"
"v_mfma_f32_16x16x32_bf16 v[32:35], a[32:35], a[96:99], v[32:35]\n"
"v_mfma_f32_16x16x32_bf16 v[32:35], a[36:39], a[100:103], v[32:35]\n"
"s_waitcnt lgkmcnt(2)\n"
"v_mfma_f32_16x16x32_bf16 v[60:63], a[44:47], a[100:103], v[60:63]\n"
"buffer_load_dwordx4 v11, s[52:55], s67 offen offset:0  lds\n"
"v_mfma_f32_16x16x32_bf16 v[60:63], a[40:43], a[96:99], v[60:63]\n"
"v_mfma_f32_16x16x32_bf16 v[64:67], a[40:43], a[104:107], v[64:67]\n"
"s_sub_u32 s67, 1063936, s75\n"
"v_mfma_f32_16x16x32_bf16 v[64:67], a[44:47], a[108:111], v[64:67]\n"
"v_mfma_f32_16x16x32_bf16 v[36:39], a[36:39], a[108:111], v[36:39]\n"
"v_mfma_f32_16x16x32_bf16 v[36:39], a[32:35], a[104:107], v[36:39]\n"
"buffer_load_dwordx4 v11, s[52:55], s67 offen offset:1024  lds\n"
"v_mfma_f32_16x16x32_bf16 v[40:43], a[32:35], a[112:115], v[40:43]\n"
"v_mfma_f32_16x16x32_bf16 v[40:43], a[36:39], a[116:119], v[40:43]\n"
"s_sub_u32 s67, 1079296, s75\n"
"v_mfma_f32_16x16x32_bf16 v[68:71], a[44:47], a[116:119], v[68:71]\n"
"v_mfma_f32_16x16x32_bf16 v[68:71], a[40:43], a[112:115], v[68:71]\n"
"buffer_load_dwordx4 v11, s[52:55], s67 offen offset:2048  lds\n"
"v_mfma_f32_16x16x32_bf16 v[72:75], a[40:43], a[120:123], v[72:75]\n"
"v_mfma_f32_16x16x32_bf16 v[72:75], a[44:47], a[124:127], v[72:75]\n"
"s_sub_u32 s67, 1094656, s75\n"
"v_mfma_f32_16x16x32_bf16 v[44:47], a[36:39], a[124:127], v[44:47]\n"
"v_mfma_f32_16x16x32_bf16 v[44:47], a[32:35], a[120:123], v[44:47]\n"
"v_mfma_f32_16x16x32_bf16 v[48:51], a[32:35], a[128:131], v[48:51]\n"
"buffer_load_dwordx4 v11, s[52:55], s67 offen offset:3072  lds\n"
"v_mfma_f32_16x16x32_bf16 v[48:51], a[36:39], a[132:135], v[48:51]\n"
"s_add_u32 m0, s64, 12288\n"
"v_mfma_f32_16x16x32_bf16 v[76:79], a[44:47], a[132:135], v[76:79]\n"
"L_st2117:\n"
"s_cbranch_execz L_st4475\n"
"s_waitcnt vmcnt(12)\n"
"ds_read_b64_tr_b16 a[64:65], v9 offset:0\n"
"v_mfma_f32_16x16x32_bf16 v[76:79], a[40:43], a[128:131], v[76:79]\n"
"ds_read_b64_tr_b16 a[66:67], v9 offset:512\n"
"v_mfma_f32_16x16x32_bf16 v[80:83], a[40:43], a[136:139], v[80:83]\n"
"s_sub_u32 s67, 1114112, s75\n"
"ds_read_b64_tr_b16 a[68:69], v9 offset:4096\n"
"v_mfma_f32_16x16x32_bf16 v[80:83], a[44:47], a[140:143], v[80:83]\n"
"ds_read_b64_tr_b16 a[70:71], v9 offset:4608\n"
"v_mfma_f32_16x16x32_bf16 v[52:55], a[36:39], a[140:143], v[52:55]\n"
"ds_read_b64_tr_b16 a[72:73], v9 offset:8\n"
"buffer_load_dwordx4 v11, s[52:55], s67 offen offset:0  lds\n"
"v_mfma_f32_16x16x32_bf16 v[52:55], a[32:35], a[136:139], v[52:55]\n"
"s_waitcnt lgkmcnt(5)\n"
"ds_read_b64_tr_b16 a[74:75], v9 offset:520\n"
"v_mfma_f32_16x16x32_bf16 v[56:59], a[32:35], a[144:147], v[56:59]\n"
"s_sub_u32 s67, 1129472, s75\n"
"ds_read_b64_tr_b16 a[76:77], v9 offset:4104\n"
"v_mfma_f32_16x16x32_bf16 v[56:59], a[36:39], a[148:151], v[56:59]\n"
"ds_read_b64_tr_b16 a[78:79], v9 offset:4616\n"
"v_mfma_f32_16x16x32_bf16 v[84:87], a[44:47], a[148:151], v[84:87]\n"
"buffer_load_dwordx4 v11, s[52:55], s67 offen offset:1024  lds\n"
"v_mfma_f32_16x16x32_bf16 v[84:87], a[40:43], a[144:147], v[84:87]\n"
"ds_read_b64_tr_b16 a[80:81], v15 offset:0\n"
"v_mfma_f32_16x16x32_bf16 a[4:7], a[40:43], a[152:155], a[4:7]\n"
"s_sub_u32 s67, 1144832, s75\n"
"ds_read_b64_tr_b16 a[82:83], v15 offset:512\n"
"v_mfma_f32_16x16x32_bf16 a[4:7], a[44:47], a[156:159], a[4:7]\n"
"ds_read_b64_tr_b16 a[84:85], v15 offset:4096\n"
"v_mfma_f32_16x16x32_bf16 a[0:3], a[36:39], a[156:159], a[0:3]\n"
"ds_read_b64_tr_b16 a[86:87], v15 offset:4608\n"
"buffer_load_dwordx4 v11, s[52:55], s67 offen offset:2048  lds\n"
"v_mfma_f32_16x16x32_bf16 a[0:3], a[32:35], a[152:155], a[0:3]\n"
"ds_read_b64_tr_b16 a[88:89], v15 offset:8\n"
"v_mfma_f32_16x16x32_bf16 v[88:91], a[48:51], a[96:99], v[88:91]\n"
"s_sub_u32 s67, 1160192, s75\n"
"ds_read_b64_tr_b16 a[90:91], v15 offset:520\n"
"v_mfma_f32_16x16x32_bf16 v[88:91], a[52:55], a[100:103], v[88:91]\n"
"ds_read_b64_tr_b16 a[92:93], v15 offset:4104\n"
"v_mfma_f32_16x16x32_bf16 v[116:119], a[60:63], a[100:103], v[116:119]\n"
"ds_read_b64_tr_b16 a[94:95], v15 offset:4616\n"
"v_mfma_f32_16x16x32_bf16 v[116:119], a[56:59], a[96:99], v[116:119]\n"
"buffer_load_dwordx4 v11, s[52:55], s67 offen offset:3072  lds\n"
"v_mfma_f32_16x16x32_bf16 v[120:123], a[56:59], a[104:107], v[120:123]\n"
"v_mfma_f32_16x16x32_bf16 v[120:123], a[60:63], a[108:111], v[120:123]\n"
"s_add_u32 m0, s64, 0\n"
"v_mfma_f32_16x16x32_bf16 v[92:95], a[52:55], a[108:111], v[92:95]\n"
"v_mfma_f32_16x16x32_bf16 v[92:95], a[48:51], a[104:107], v[92:95]\n"
"s_sub_u32 s67, 1048704, s75\n"
"v_mfma_f32_16x16x32_bf16 v[96:99], a[48:51], a[112:115], v[96:99]\n"
"s_waitcnt lgkmcnt(0)\n"
"v_mfma_f32_16x16x32_bf16 v[96:99], a[52:55], a[116:119], v[96:99]\n"
"buffer_load_dwordx4 v11, s[52:55], s67 offen offset:0  lds\n"
"v_mfma_f32_16x16x32_bf16 v[124:127], a[60:63], a[116:119], v[124:127]\n"
"v_mfma_f32_16x16x32_bf16 v[124:127], a[56:59], a[112:115], v[124:127]\n"
"s_sub_u32 s67, 1064064, s75\n"
"v_mfma_f32_16x16x32_bf16 v[128:131], a[56:59], a[120:123], v[128:131]\n"
"v_mfma_f32_16x16x32_bf16 v[128:131], a[60:63], a[124:127], v[128:131]\n"
"v_mfma_f32_16x16x32_bf16 v[100:103], a[52:55], a[124:127], v[100:103]\n"
"buffer_load_dwordx4 v11, s[52:55], s67 offen offset:1024  lds\n"
"v_mfma_f32_16x16x32_bf16 v[100:103], a[48:51], a[120:123], v[100:103]\n"
"v_mfma_f32_16x16x32_bf16 v[104:107], a[48:51], a[128:131], v[104:107]\n"
"s_sub_u32 s67, 1079424, s75\n"
"v_mfma_f32_16x16x32_bf16 v[104:107], a[52:55], a[132:135], v[104:107]\n"
"v_mfma_f32_16x16x32_bf16 v[132:135], a[60:63], a[132:135], v[132:135]\n"
"buffer_load_dwordx4 v11, s[52:55], s67 offen offset:2048  lds\n"
"v_mfma_f32_16x16x32_bf16 v[132:135], a[56:59], a[128:131], v[132:135]\n"
"v_mfma_f32_16x16x32_bf16 v[136:139], a[56:59], a[136:139], v[136:139]\n"
"s_sub_u32 s67, 1094784, s75\n"
"v_mfma_f32_16x16x32_bf16 v[136:139], a[60:63], a[140:143], v[136:139]\n"
"v_mfma_f32_16x16x32_bf16 v[108:111], a[52:55], a[140:143], v[108:111]\n"
"v_mfma_f32_16x16x32_bf16 v[108:111], a[48:51], a[136:139], v[108:111]\n"
"buffer_load_dwordx4 v11, s[52:55], s67 offen offset:3072  lds\n"
"v_mfma_f32_16x16x32_bf16 v[112:115], a[48:51], a[144:147], v[112:115]\n"
"s_add_u32 m0, s64, 4096\n"
"v_mfma_f32_16x16x32_bf16 v[112:115], a[52:55], a[148:151], v[112:115]\n"
"v_mfma_f32_16x16x32_bf16 v[140:143], a[60:63], a[148:151], v[140:143]\n"
"s_sub_u32 s67, 1114240, s75\n"
"v_mfma_f32_16x16x32_bf16 v[140:143], a[56:59], a[144:147], v[140:143]\n"
"v_mfma_f32_16x16x32_bf16 a[12:15], a[56:59], a[152:155], a[12:15]\n"
"buffer_load_dwordx4 v11, s[52:55], s67 offen offset:0  lds\n"
"v_mfma_f32_16x16x32_bf16 a[12:15], a[60:63], a[156:159], a[12:15]\n"
"v_mfma_f32_16x16x32_bf16 a[8:11], a[52:55], a[156:159], a[8:11]\n"
"s_sub_u32 s67, 1129600, s75\n"
"v_mfma_f32_16x16x32_bf16 a[8:11], a[48:51], a[152:155], a[8:11]\n"
"v_mfma_f32_16x16x32_bf16 v[144:147], a[64:67], a[96:99], v[144:147]\n"
"buffer_load_dwordx4 v11, s[52:55], s67 offen offset:1024  lds\n"
"v_mfma_f32_16x16x32_bf16 v[144:147], a[68:71], a[100:103], v[144:147]\n"
"v_mfma_f32_16x16x32_bf16 v[172:175], a[76:79], a[100:103], v[172:175]\n"
"s_sub_u32 s67, 1144960, s75\n"
"v_mfma_f32_16x16x32_bf16 v[172:175], a[72:75], a[96:99], v[172:175]\n"
"v_mfma_f32_16x16x32_bf16 v[176:179], a[72:75], a[104:107], v[176:179]\n"
"v_mfma_f32_16x16x32_bf16 v[176:179], a[76:79], a[108:111], v[176:179]\n"
"s_sub_u32 s71, s72, s64\n"
"v_mfma_f32_16x16x32_bf16 v[148:151], a[68:71], a[108:111], v[148:151]\n"
"buffer_load_dwordx4 v11, s[52:55], s67 offen offset:2048  lds\n"
"v_mfma_f32_16x16x32_bf16 v[148:151], a[64:67], a[104:107], v[148:151]\n"
"s_sub_u32 s67, 1160320, s75\n"
"v_mfma_f32_16x16x32_bf16 v[152:155], a[64:67], a[112:115], v[152:155]\n"
"s_sub_u32 s66, 0, s73\n"
"s_add_u32 s71, s71, 57344\n"
"v_mfma_f32_16x16x32_bf16 v[152:155], a[68:71], a[116:119], v[152:155]\n"
"buffer_load_dwordx4 v11, s[52:55], s67 offen offset:3072  lds\n"
"v_mfma_f32_16x16x32_bf16 v[180:183], a[76:79], a[116:119], v[180:183]\n"
"v_add_u32 v12, s66, v7\n"
"v_mfma_f32_16x16x32_bf16 v[180:183], a[72:75], a[112:115], v[180:183]\n"
"v_add_u32 v20, s71, v7\n"
"v_mfma_f32_16x16x32_bf16 v[184:187], a[72:75], a[120:123], v[184:187]\n"
"v_add_u32 v13, s66, v8\n"
"v_mfma_f32_16x16x32_bf16 v[184:187], a[76:79], a[124:127], v[184:187]\n"
"v_add_u32 v21, s71, v8\n"
"v_mfma_f32_16x16x32_bf16 v[156:159], a[68:71], a[124:127], v[156:159]\n"
"s_cmp_lt_u32 %16, 2\n"
"s_cselect_b32 s67, 16384, 0\n"
"v_mfma_f32_16x16x32_bf16 v[156:159], a[64:67], a[120:123], v[156:159]\n"
"s_add_u32 s67, s67, s72\n"
"v_mfma_f32_16x16x32_bf16 v[160:163], a[64:67], a[128:131], v[160:163]\n"
"s_waitcnt vmcnt(16)\n"
"ds_read_b128 a[192:195], v20 offset:0\n"
"v_mfma_f32_16x16x32_bf16 v[160:163], a[68:71], a[132:135], v[160:163]\n"
"v_mfma_f32_16x16x32_bf16 v[188:191], a[76:79], a[132:135], v[188:191]\n"
"ds_read_b128 a[196:199], v21 offset:0\n"
"v_mfma_f32_16x16x32_bf16 v[188:191], a[72:75], a[128:131], v[188:191]\n"
"ds_read_b128 a[200:203], v20 offset:2048\n"
"v_mfma_f32_16x16x32_bf16 v[192:195], a[72:75], a[136:139], v[192:195]\n"
"s_add_u32 m0, s67, 57344\n"
"v_mfma_f32_16x16x32_bf16 v[192:195], a[76:79], a[140:143], v[192:195]\n"
"ds_read_b128 a[204:207], v21 offset:2048\n"
"v_mfma_f32_16x16x32_bf16 v[164:167], a[68:71], a[140:143], v[164:167]\n"
"v_mfma_f32_16x16x32_bf16 v[164:167], a[64:67], a[136:139], v[164:167]\n"
"v_mfma_f32_16x16x32_bf16 v[168:171], a[64:67], a[144:147], v[168:171]\n"
"s_sub_u32 s67, 1280, s60\n"
"v_mfma_f32_16x16x32_bf16 v[168:171], a[68:71], a[148:151], v[168:171]\n"
"v_mfma_f32_16x16x32_bf16 v[196:199], a[76:79], a[148:151], v[196:199]\n"
"v_mfma_f32_16x16x32_bf16 v[196:199], a[72:75], a[144:147], v[196:199]\n"
"s_waitcnt lgkmcnt(0)\n"
"v_mfma_f32_16x16x32_bf16 a[20:23], a[72:75], a[152:155], a[20:23]\n"
"buffer_load_dwordx4 v6, s[48:51], s67 offen offset:0  lds\n"
"v_mfma_f32_16x16x32_bf16 a[20:23], a[76:79], a[156:159], a[20:23]\n"
"v_mfma_f32_16x16x32_bf16 a[16:19], a[68:71], a[156:159], a[16:19]\n"
"s_sub_u32 s67, 16640, s60\n"
"v_mfma_f32_16x16x32_bf16 a[16:19], a[64:67], a[152:155], a[16:19]\n"
"s_barrier\n"
"s_waitcnt vmcnt(9)\n"
"ds_read_b64_tr_b16 a[32:33], v9 offset:8192\n"
"v_mfma_f32_16x16x32_bf16 v[200:203], a[80:83], a[96:99], v[200:203]\n"
"ds_read_b64_tr_b16 a[34:35], v9 offset:8704\n"
"buffer_load_dwordx4 v6, s[48:51], s67 offen offset:1024  lds\n"
"v_mfma_f32_16x16x32_bf16 v[200:203], a[84:87], a[100:103], v[200:203]\n"
"ds_read_b64_tr_b16 a[36:37], v9 offset:12288\n"
"v_mfma_f32_16x16x32_bf16 v[228:231], a[92:95], a[100:103], v[228:231]\n"
"s_sub_u32 s67, 32000, s60\n"
"ds_read_b128 a[100:103], v13 offset:32768\n"
"v_mfma_f32_16x16x32_bf16 v[228:231], a[88:91], a[96:99], v[228:231]\n"
"ds_read_b128 a[96:99], v12 offset:32768\n"
"v_mfma_f32_16x16x32_bf16 v[232:235], a[88:91], a[104:107], v[232:235]\n"
"ds_read_b64_tr_b16 a[38:39], v9 offset:12800\n"
"v_mfma_f32_16x16x32_bf16 v[232:235], a[92:95], a[108:111], v[232:235]\n"
"ds_read_b64_tr_b16 a[40:41], v9 offset:8200\n"
"buffer_load_dwordx4 v6, s[48:51], s67 offen offset:2048  lds\n"
"v_mfma_f32_16x16x32_bf16 v[204:207], a[84:87], a[108:111], v[204:207]\n"
"ds_read_b128 a[108:111], v13 offset:34816\n"
"v_mfma_f32_16x16x32_bf16 v[204:207], a[80:83], a[104:107], v[204:207]\n"
"s_sub_u32 s67, 47360, s60\n"
"ds_read_b128 a[104:107], v12 offset:34816\n"
"v_mfma_f32_16x16x32_bf16 v[208:211], a[80:83], a[112:115], v[208:211]\n"
"ds_read_b64_tr_b16 a[42:43], v9 offset:8712\n"
"v_mfma_f32_16x16x32_bf16 v[208:211], a[84:87], a[116:119], v[208:211]\n"
"ds_read_b64_tr_b16 a[44:45], v9 offset:12296\n"
"v_mfma_f32_16x16x32_bf16 v[236:239], a[92:95], a[116:119], v[236:239]\n"
"ds_read_b128 a[116:119], v13 offset:36864\n"
"buffer_load_dwordx4 v6, s[48:51], s67 offen offset:3072  lds\n"
"v_mfma_f32_16x16x32_bf16 v[236:239], a[88:91], a[112:115], v[236:239]\n"
"ds_read_b128 a[112:115], v12 offset:36864\n"
"v_mfma_f32_16x16x32_bf16 v[240:243], a[88:91], a[120:123], v[240:243]\n"
"s_add_u32 m0, s64, 16256\n"
"ds_read_b64_tr_b16 a[46:47], v9 offset:12808\n"
"v_mfma_f32_16x16x32_bf16 v[240:243], a[92:95], a[124:127], v[240:243]\n"
"ds_read_b64_tr_b16 a[48:49], v15 offset:8192\n"
"v_mfma_f32_16x16x32_bf16 v[212:215], a[84:87], a[124:127], v[212:215]\n"
"ds_read_b128 a[124:127], v13 offset:38912\n"
"buffer_load_dwordx4 v6, s[40:43], s84 offen offset:128  lds\n"
"v_mfma_f32_16x16x32_bf16 v[212:215], a[80:83], a[120:123], v[212:215]\n"
"ds_read_b128 a[120:123], v12 offset:38912\n"
"v_mfma_f32_16x16x32_bf16 v[216:219], a[80:83], a[128:131], v[216:219]\n"
"ds_read_b64_tr_b16 a[50:51], v15 offset:8704\n"
"v_mfma_f32_16x16x32_bf16 v[216:219], a[84:87], a[132:135], v[216:219]\n"
"ds_read_b64_tr_b16 a[52:53], v15 offset:12288\n"
"v_mfma_f32_16x16x32_bf16 v[244:247], a[92:95], a[132:135], v[244:247]\n"
"ds_read_b128 a[132:135], v13 offset:24576\n"
"buffer_load_dwordx4 v6, s[40:43], s85 offen offset:1152  lds\n"
"v_mfma_f32_16x16x32_bf16 v[244:247], a[88:91], a[128:131], v[244:247]\n"
"ds_read_b128 a[128:131], v12 offset:24576\n"
"v_mfma_f32_16x16x32_bf16 v[248:251], a[88:91], a[136:139], v[248:251]\n"
"ds_read_b64_tr_b16 a[54:55], v15 offset:12800\n"
"v_mfma_f32_16x16x32_bf16 v[248:251], a[92:95], a[140:143], v[248:251]\n"
"ds_read_b64_tr_b16 a[56:57], v15 offset:8200\n"
"v_mfma_f32_16x16x32_bf16 v[220:223], a[84:87], a[140:143], v[220:223]\n"
"ds_read_b128 a[140:143], v13 offset:26624\n"
"buffer_load_dwordx4 v6, s[40:43], s86 offen offset:2176  lds\n"
"v_mfma_f32_16x16x32_bf16 v[220:223], a[80:83], a[136:139], v[220:223]\n"
"ds_read_b128 a[136:139], v12 offset:26624\n"
"v_mfma_f32_16x16x32_bf16 v[224:227], a[80:83], a[144:147], v[224:227]\n"
"ds_read_b64_tr_b16 a[58:59], v15 offset:8712\n"
"v_mfma_f32_16x16x32_bf16 v[224:227], a[84:87], a[148:151], v[224:227]\n"
"ds_read_b64_tr_b16 a[60:61], v15 offset:12296\n"
"v_mfma_f32_16x16x32_bf16 v[252:255], a[92:95], a[148:151], v[252:255]\n"
"ds_read_b128 a[148:151], v13 offset:28672\n"
"v_mfma_f32_16x16x32_bf16 v[252:255], a[88:91], a[144:147], v[252:255]\n"
"ds_read_b128 a[144:147], v12 offset:28672\n"
"ds_read_b64_tr_b16 a[62:63], v15 offset:12808\n"
"v_mfma_f32_16x16x32_bf16 a[28:31], a[88:91], a[152:155], a[28:31]\n"
"buffer_load_dwordx4 v6, s[40:43], s87 offen offset:3200  lds\n"
"v_mfma_f32_16x16x32_bf16 a[28:31], a[92:95], a[156:159], a[28:31]\n"
"v_mfma_f32_16x16x32_bf16 a[24:27], a[84:87], a[156:159], a[24:27]\n"
"s_add_u32 m0, s64, 8192\n"
"ds_read_b128 a[156:159], v13 offset:30720\n"
"v_mfma_f32_16x16x32_bf16 a[24:27], a[80:83], a[152:155], a[24:27]\n"
"s_waitcnt lgkmcnt(15)\n"
"ds_read_b128 a[152:155], v12 offset:30720\n"
"v_mfma_f32_16x16x32_bf16 v[32:35], a[32:35], a[96:99], v[32:35]\n"
"s_sub_u32 s67, 1310720, s75\n"
"v_mfma_f32_16x16x32_bf16 v[32:35], a[36:39], a[100:103], v[32:35]\n"
"s_waitcnt lgkmcnt(2)\n"
"v_mfma_f32_16x16x32_bf16 v[60:63], a[44:47], a[100:103], v[60:63]\n"
"buffer_load_dwordx4 v11, s[52:55], s67 offen offset:0  lds\n"
"v_mfma_f32_16x16x32_bf16 v[60:63], a[40:43], a[96:99], v[60:63]\n"
"v_mfma_f32_16x16x32_bf16 v[64:67], a[40:43], a[104:107], v[64:67]\n"
"s_sub_u32 s67, 1326080, s75\n"
"v_mfma_f32_16x16x32_bf16 v[64:67], a[44:47], a[108:111], v[64:67]\n"
"v_mfma_f32_16x16x32_bf16 v[36:39], a[36:39], a[108:111], v[36:39]\n"
"buffer_load_dwordx4 v11, s[52:55], s67 offen offset:1024  lds\n"
"v_mfma_f32_16x16x32_bf16 v[36:39], a[32:35], a[104:107], v[36:39]\n"
"v_mfma_f32_16x16x32_bf16 v[40:43], a[32:35], a[112:115], v[40:43]\n"
"s_sub_u32 s67, 1341440, s75\n"
"v_mfma_f32_16x16x32_bf16 v[40:43], a[36:39], a[116:119], v[40:43]\n"
"v_mfma_f32_16x16x32_bf16 v[68:71], a[44:47], a[116:119], v[68:71]\n"
"v_mfma_f32_16x16x32_bf16 v[68:71], a[40:43], a[112:115], v[68:71]\n"
"buffer_load_dwordx4 v11, s[52:55], s67 offen offset:2048  lds\n"
"v_mfma_f32_16x16x32_bf16 v[72:75], a[40:43], a[120:123], v[72:75]\n"
"v_mfma_f32_16x16x32_bf16 v[72:75], a[44:47], a[124:127], v[72:75]\n"
"s_sub_u32 s67, 1356800, s75\n"
"v_mfma_f32_16x16x32_bf16 v[44:47], a[36:39], a[124:127], v[44:47]\n"
"v_mfma_f32_16x16x32_bf16 v[44:47], a[32:35], a[120:123], v[44:47]\n"
"buffer_load_dwordx4 v11, s[52:55], s67 offen offset:3072  lds\n"
"v_mfma_f32_16x16x32_bf16 v[48:51], a[32:35], a[128:131], v[48:51]\n"
"v_mfma_f32_16x16x32_bf16 v[48:51], a[36:39], a[132:135], v[48:51]\n"
"s_add_u32 m0, s64, 12288\n"
"v_mfma_f32_16x16x32_bf16 v[76:79], a[44:47], a[132:135], v[76:79]\n"
"s_waitcnt vmcnt(12)\n"
"ds_read_b64_tr_b16 a[64:65], v9 offset:0\n"
"v_mfma_f32_16x16x32_bf16 v[76:79], a[40:43], a[128:131], v[76:79]\n"
"s_sub_u32 s67, 1376256, s75\n"
"ds_read_b64_tr_b16 a[66:67], v9 offset:512\n"
"v_mfma_f32_16x16x32_bf16 v[80:83], a[40:43], a[136:139], v[80:83]\n"
"ds_read_b64_tr_b16 a[68:69], v9 offset:4096\n"
"v_mfma_f32_16x16x32_bf16 v[80:83], a[44:47], a[140:143], v[80:83]\n"
"ds_read_b64_tr_b16 a[70:71], v9 offset:4608\n"
"buffer_load_dwordx4 v11, s[52:55], s67 offen offset:0  lds\n"
"v_mfma_f32_16x16x32_bf16 v[52:55], a[36:39], a[140:143], v[52:55]\n"
"s_sub_u32 s67, 1391616, s75\n"
"ds_read_b64_tr_b16 a[72:73], v9 offset:8\n"
"v_mfma_f32_16x16x32_bf16 v[52:55], a[32:35], a[136:139], v[52:55]\n"
"s_waitcnt lgkmcnt(5)\n"
"ds_read_b64_tr_b16 a[74:75], v9 offset:520\n"
"v_mfma_f32_16x16x32_bf16 v[56:59], a[32:35], a[144:147], v[56:59]\n"
"ds_read_b64_tr_b16 a[76:77], v9 offset:4104\n"
"v_mfma_f32_16x16x32_bf16 v[56:59], a[36:39], a[148:151], v[56:59]\n"
"ds_read_b64_tr_b16 a[78:79], v9 offset:4616\n"
"v_mfma_f32_16x16x32_bf16 v[84:87], a[44:47], a[148:151], v[84:87]\n"
"buffer_load_dwordx4 v11, s[52:55], s67 offen offset:1024  lds\n"
"v_mfma_f32_16x16x32_bf16 v[84:87], a[40:43], a[144:147], v[84:87]\n"
"ds_read_b64_tr_b16 a[80:81], v15 offset:0\n"
"v_mfma_f32_16x16x32_bf16 a[4:7], a[40:43], a[152:155], a[4:7]\n"
"s_sub_u32 s67, 1406976, s75\n"
"ds_read_b64_tr_b16 a[82:83], v15 offset:512\n"
"v_mfma_f32_16x16x32_bf16 a[4:7], a[44:47], a[156:159], a[4:7]\n"
"ds_read_b64_tr_b16 a[84:85], v15 offset:4096\n"
"v_mfma_f32_16x16x32_bf16 a[0:3], a[36:39], a[156:159], a[0:3]\n"
"ds_read_b64_tr_b16 a[86:87], v15 offset:4608\n"
"buffer_load_dwordx4 v11, s[52:55], s67 offen offset:2048  lds\n"
"v_mfma_f32_16x16x32_bf16 a[0:3], a[32:35], a[152:155], a[0:3]\n"
"ds_read_b64_tr_b16 a[88:89], v15 offset:8\n"
"v_mfma_f32_16x16x32_bf16 v[88:91], a[48:51], a[96:99], v[88:91]\n"
"s_sub_u32 s67, 1422336, s75\n"
"ds_read_b64_tr_b16 a[90:91], v15 offset:520\n"
"v_mfma_f32_16x16x32_bf16 v[88:91], a[52:55], a[100:103], v[88:91]\n"
"ds_read_b64_tr_b16 a[92:93], v15 offset:4104\n"
"v_mfma_f32_16x16x32_bf16 v[116:119], a[60:63], a[100:103], v[116:119]\n"
"ds_read_b64_tr_b16 a[94:95], v15 offset:4616\n"
"v_mfma_f32_16x16x32_bf16 v[116:119], a[56:59], a[96:99], v[116:119]\n"
"buffer_load_dwordx4 v11, s[52:55], s67 offen offset:3072  lds\n"
"v_mfma_f32_16x16x32_bf16 v[120:123], a[56:59], a[104:107], v[120:123]\n"
"v_mfma_f32_16x16x32_bf16 v[120:123], a[60:63], a[108:111], v[120:123]\n"
"s_add_u32 m0, s64, 0\n"
"v_mfma_f32_16x16x32_bf16 v[92:95], a[52:55], a[108:111], v[92:95]\n"
"v_mfma_f32_16x16x32_bf16 v[92:95], a[48:51], a[104:107], v[92:95]\n"
"s_sub_u32 s67, 1310848, s75\n"
"v_mfma_f32_16x16x32_bf16 v[96:99], a[48:51], a[112:115], v[96:99]\n"
"s_waitcnt lgkmcnt(0)\n"
"s_mov_b32 s99, 0\n"
"v_mfma_f32_16x16x32_bf16 v[96:99], a[52:55], a[116:119], v[96:99]\n"
"s_mov_b32 s98, 0\n"
"buffer_load_dwordx4 v11, s[52:55], s67 offen offset:0  lds\n"
"v_mfma_f32_16x16x32_bf16 v[124:127], a[60:63], a[116:119], v[124:127]\n"
"s_memtime s[76:77]\n"
"s_load_dword s98, s[96:97], 0 glc\n"
"v_mfma_f32_16x16x32_bf16 v[124:127], a[56:59], a[112:115], v[124:127]\n"
"s_sub_u32 s67, 1326208, s75\n"
"v_mfma_f32_16x16x32_bf16 v[128:131], a[56:59], a[120:123], v[128:131]\n"
"v_mfma_f32_16x16x32_bf16 v[128:131], a[60:63], a[124:127], v[128:131]\n"
"v_mfma_f32_16x16x32_bf16 v[100:103], a[52:55], a[124:127], v[100:103]\n"
"buffer_load_dwordx4 v11, s[52:55], s67 offen offset:1024  lds\n"
"v_mfma_f32_16x16x32_bf16 v[100:103], a[48:51], a[120:123], v[100:103]\n"
"v_mfma_f32_16x16x32_bf16 v[104:107], a[48:51], a[128:131], v[104:107]\n"
"s_sub_u32 s67, 1341568, s75\n"
"v_mfma_f32_16x16x32_bf16 v[104:107], a[52:55], a[132:135], v[104:107]\n"
"v_mfma_f32_16x16x32_bf16 v[132:135], a[60:63], a[132:135], v[132:135]\n"
"v_mfma_f32_16x16x32_bf16 v[132:135], a[56:59], a[128:131], v[132:135]\n"
"s_mov_b32 s29, s76\n"
"v_mfma_f32_16x16x32_bf16 v[136:139], a[56:59], a[136:139], v[136:139]\n"
"buffer_load_dwordx4 v11, s[52:55], s67 offen offset:2048  lds\n"
"v_mfma_f32_16x16x32_bf16 v[136:139], a[60:63], a[140:143], v[136:139]\n"
"s_sub_u32 s67, 1356928, s75\n"
"v_mfma_f32_16x16x32_bf16 v[108:111], a[52:55], a[140:143], v[108:111]\n"
"s_sub_u32 s99, s99, s98\n"
"s_sub_u32 s99, s99, s98\n"
"v_mfma_f32_16x16x32_bf16 v[108:111], a[48:51], a[136:139], v[108:111]\n"
"s_sub_u32 s99, s99, s98\n"
"v_mfma_f32_16x16x32_bf16 v[112:115], a[48:51], a[144:147], v[112:115]\n"
"s_sub_u32 s99, s99, s98\n"
"buffer_load_dwordx4 v11, s[52:55], s67 offen offset:3072  lds\n"
"v_mfma_f32_16x16x32_bf16 v[112:115], a[52:55], a[148:151], v[112:115]\n"
"s_add_u32 m0, s64, 4096\n"
"v_mfma_f32_16x16x32_bf16 v[140:143], a[60:63], a[148:151], v[140:143]\n"
"s_sub_u32 s99, s99, s98\n"
"s_sub_u32 s99, s99, s98\n"
"v_mfma_f32_16x16x32_bf16 v[140:143], a[56:59], a[144:147], v[140:143]\n"
"s_sub_u32 s99, s99, s98\n"
"s_sub_u32 s67, 1376384, s75\n"
"v_mfma_f32_16x16x32_bf16 a[12:15], a[56:59], a[152:155], a[12:15]\n"
"s_sub_u32 s99, s99, s98\n"
"v_mfma_f32_16x16x32_bf16 a[12:15], a[60:63], a[156:159], a[12:15]\n"
"buffer_load_dwordx4 v11, s[52:55], s67 offen offset:0  lds\n"
"v_mfma_f32_16x16x32_bf16 a[8:11], a[52:55], a[156:159], a[8:11]\n"
"s_sub_u32 s99, s99, s98\n"
"s_sub_u32 s99, s99, s98\n"
"v_mfma_f32_16x16x32_bf16 a[8:11], a[48:51], a[152:155], a[8:11]\n"
"s_sub_u32 s99, s99, s98\n"
"s_sub_u32 s67, 1391744, s75\n"
"v_mfma_f32_16x16x32_bf16 v[144:147], a[64:67], a[96:99], v[144:147]\n"
"s_sub_u32 s99, s99, s98\n"
"v_mfma_f32_16x16x32_bf16 v[144:147], a[68:71], a[100:103], v[144:147]\n"
"buffer_load_dwordx4 v11, s[52:55], s67 offen offset:1024  lds\n"
"v_mfma_f32_16x16x32_bf16 v[172:175], a[76:79], a[100:103], v[172:175]\n"
"s_sub_u32 s99, s99, s98\n"
"s_sub_u32 s99, s99, s98\n"
"v_mfma_f32_16x16x32_bf16 v[172:175], a[72:75], a[96:99], v[172:175]\n"
"s_sub_u32 s99, s99, s98\n"
"s_sub_u32 s67, 1407104, s75\n"
"v_mfma_f32_16x16x32_bf16 v[176:179], a[72:75], a[104:107], v[176:179]\n"
"s_sub_u32 s99, s99, s98\n"
"v_mfma_f32_16x16x32_bf16 v[176:179], a[76:79], a[108:111], v[176:179]\n"
"buffer_load_dwordx4 v11, s[52:55], s67 offen offset:2048  lds\n"
"v_mfma_f32_16x16x32_bf16 v[148:151], a[68:71], a[108:111], v[148:151]\n"
"s_sub_u32 s99, s99, s98\n"
"s_sub_u32 s99, s99, s98\n"
"v_mfma_f32_16x16x32_bf16 v[148:151], a[64:67], a[104:107], v[148:151]\n"
"s_sub_u32 s99, s99, s98\n"
"s_sub_u32 s71, s64, s64\n"
"v_mfma_f32_16x16x32_bf16 v[152:155], a[64:67], a[112:115], v[152:155]\n"
"s_sub_u32 s66, 40960, s73\n"
"s_sub_u32 s99, s99, s98\n"
"v_mfma_f32_16x16x32_bf16 v[152:155], a[68:71], a[116:119], v[152:155]\n"
"s_add_u32 s71, s71, 16384\n"
"s_sub_u32 s67, 1422464, s75\n"
"v_mfma_f32_16x16x32_bf16 v[180:183], a[76:79], a[116:119], v[180:183]\n"
"s_sub_u32 s99, s99, s98\n"
"v_add_u32 v12, s66, v7\n"
"s_sub_u32 s99, s99, s98\n"
"v_mfma_f32_16x16x32_bf16 v[180:183], a[72:75], a[112:115], v[180:183]\n"
"s_sub_u32 s99, s99, s98\n"
"buffer_load_dwordx4 v11, s[52:55], s67 offen offset:3072  lds\n"
"v_mfma_f32_16x16x32_bf16 v[184:187], a[72:75], a[120:123], v[184:187]\n"
"s_sub_u32 s99, s99, s98\n"
"v_add_u32 v13, s66, v8\n"
"s_cmp_lt_u32 %16, 2\n"
"s_cselect_b32 s67, 16384, 0\n"
"v_mfma_f32_16x16x32_bf16 v[184:187], a[76:79], a[124:127], v[184:187]\n"
"s_memtime s[76:77]\n"
"v_add_u32 v20, s71, v7\n"
"s_add_u32 s67, s67, s72\n"
"v_mfma_f32_16x16x32_bf16 v[156:159], a[68:71], a[124:127], v[156:159]\n"
"s_add_u32 m0, s67, 98304\n"
"s_sub_u32 s67, 1536, s60\n"
"v_mfma_f32_16x16x32_bf16 v[156:159], a[64:67], a[120:123], v[156:159]\n"
"s_waitcnt lgkmcnt(0)\n"
"buffer_load_dwordx4 v6, s[48:51], s67 offen offset:0  lds\n"
"v_add_u32 v21, s71, v8\n"
"v_mfma_f32_16x16x32_bf16 v[160:163], a[64:67], a[128:131], v[160:163]\n"
"s_sub_u32 s29, s76, s29\n"
"s_sub_u32 s67, 16896, s60\n"
"v_mfma_f32_16x16x32_bf16 v[160:163], a[68:71], a[132:135], v[160:163]\n"
"s_cmp_lt_u32 s29, 620\n"
"s_cselect_b32 s99, s99, 0\n"
"v_mfma_f32_16x16x32_bf16 v[188:191], a[76:79], a[132:135], v[188:191]\n"
"s_max_u32 s30, s30, s99\n"
"v_mfma_f32_16x16x32_bf16 v[188:191], a[72:75], a[128:131], v[188:191]\n"
"s_waitcnt vmcnt(17)\n"
"ds_read_b128 a[208:211], v20 offset:0\n"
"buffer_load_dwordx4 v6, s[48:51], s67 offen offset:1024  lds\n"
"v_mfma_f32_16x16x32_bf16 v[192:195], a[72:75], a[136:139], v[192:195]\n"
"ds_read_b128 a[212:215], v21 offset:0\n"
"v_mfma_f32_16x16x32_bf16 v[192:195], a[76:79], a[140:143], v[192:195]\n"
"s_sub_u32 s67, 32256, s60\n"
"ds_read_b128 a[216:219], v20 offset:2048\n"
"v_mfma_f32_16x16x32_bf16 v[164:167], a[68:71], a[140:143], v[164:167]\n"
"v_mfma_f32_16x16x32_bf16 v[164:167], a[64:67], a[136:139], v[164:167]\n"
"ds_read_b128 a[220:223], v21 offset:2048\n"
"v_mfma_f32_16x16x32_bf16 v[168:171], a[64:67], a[144:147], v[168:171]\n"
"buffer_load_dwordx4 v6, s[48:51], s67 offen offset:2048  lds\n"
"v_mfma_f32_16x16x32_bf16 v[168:171], a[68:71], a[148:151], v[168:171]\n"
"v_mfma_f32_16x16x32_bf16 v[196:199], a[76:79], a[148:151], v[196:199]\n"
"s_sub_u32 s67, 47616, s60\n"
"v_mfma_f32_16x16x32_bf16 v[196:199], a[72:75], a[144:147], v[196:199]\n"
"v_mfma_f32_16x16x32_bf16 a[20:23], a[72:75], a[152:155], a[20:23]\n"
"buffer_load_dwordx4 v6, s[48:51], s67 offen offset:3072  lds\n"
"v_mfma_f32_16x16x32_bf16 a[20:23], a[76:79], a[156:159], a[20:23]\n"
"s_waitcnt lgkmcnt(0)\n"
"v_mfma_f32_16x16x32_bf16 a[16:19], a[68:71], a[156:159], a[16:19]\n"
"s_add_u32 m0, s64, 20352\n"
"v_mfma_f32_16x16x32_bf16 a[16:19], a[64:67], a[152:155], a[16:19]\n"
"s_barrier\n"
"s_waitcnt vmcnt(12)\n"
"ds_read_b64_tr_b16 a[32:33], v9 offset:8192\n"
"v_mfma_f32_16x16x32_bf16 v[200:203], a[80:83], a[96:99], v[200:203]\n"
"ds_read_b64_tr_b16 a[34:35], v9 offset:8704\n"
"v_mfma_f32_16x16x32_bf16 v[200:203], a[84:87], a[100:103], v[200:203]\n"
"ds_read_b64_tr_b16 a[36:37], v9 offset:12288\n"
"buffer_load_dwordx4 v6, s[40:43], s88 offen offset:128  lds\n"
"v_mfma_f32_16x16x32_bf16 v[228:231], a[92:95], a[100:103], v[228:231]\n"
"ds_read_b128 a[100:103], v13 offset:32768\n"
"v_mfma_f32_16x16x32_bf16 v[228:231], a[88:91], a[96:99], v[228:231]\n"
"ds_read_b128 a[96:99], v12 offset:32768\n"
"v_mfma_f32_16x16x32_bf16 v[232:235], a[88:91], a[104:107], v[232:235]\n"
"ds_read_b64_tr_b16 a[38:39], v9 offset:12800\n"
"buffer_load_dwordx4 v6, s[40:43], s89 offen offset:1152  lds\n"
"v_mfma_f32_16x16x32_bf16 v[232:235], a[92:95], a[108:111], v[232:235]\n"
"ds_read_b64_tr_b16 a[40:41], v9 offset:8200\n"
"v_mfma_f32_16x16x32_bf16 v[204:207], a[84:87], a[108:111], v[204:207]\n"
"ds_read_b128 a[108:111], v13 offset:34816\n"
"v_mfma_f32_16x16x32_bf16 v[204:207], a[80:83], a[104:107], v[204:207]\n"
"ds_read_b128 a[104:107], v12 offset:34816\n"
"v_mfma_f32_16x16x32_bf16 v[208:211], a[80:83], a[112:115], v[208:211]\n"
"ds_read_b64_tr_b16 a[42:43], v9 offset:8712\n"
"v_mfma_f32_16x16x32_bf16 v[208:211], a[84:87], a[116:119], v[208:211]\n"
"ds_read_b64_tr_b16 a[44:45], v9 offset:12296\n"
"buffer_load_dwordx4 v6, s[40:43], s90 offen offset:2176  lds\n"
"v_mfma_f32_16x16x32_bf16 v[236:239], a[92:95], a[116:119], v[236:239]\n"
"ds_read_b128 a[116:119], v13 offset:36864\n"
"v_mfma_f32_16x16x32_bf16 v[236:239], a[88:91], a[112:115], v[236:239]\n"
"ds_read_b128 a[112:115], v12 offset:36864\n"
"v_mfma_f32_16x16x32_bf16 v[240:243], a[88:91], a[120:123], v[240:243]\n"
"ds_read_b64_tr_b16 a[46:47], v9 offset:12808\n"
"v_mfma_f32_16x16x32_bf16 v[240:243], a[92:95], a[124:127], v[240:243]\n"
"ds_read_b64_tr_b16 a[48:49], v15 offset:8192\n"
"buffer_load_dwordx4 v6, s[40:43], s91 offen offset:3200  lds\n"
"v_mfma_f32_16x16x32_bf16 v[212:215], a[84:87], a[124:127], v[212:215]\n"
"ds_read_b128 a[124:127], v13 offset:38912\n"
"v_mfma_f32_16x16x32_bf16 v[212:215], a[80:83], a[120:123], v[212:215]\n"
"ds_read_b128 a[120:123], v12 offset:38912\n"
"v_mfma_f32_16x16x32_bf16 v[216:219], a[80:83], a[128:131], v[216:219]\n"
"ds_read_b64_tr_b16 a[50:51], v15 offset:8704\n"
"v_mfma_f32_16x16x32_bf16 v[216:219], a[84:87], a[132:135], v[216:219]\n"
"s_add_u32 m0, s64, 8192\n"
"ds_read_b64_tr_b16 a[52:53], v15 offset:12288\n"
"v_mfma_f32_16x16x32_bf16 v[244:247], a[92:95], a[132:135], v[244:247]\n"
"ds_read_b128 a[132:135], v13 offset:24576\n"
"v_mfma_f32_16x16x32_bf16 v[244:247], a[88:91], a[128:131], v[244:247]\n"
"ds_read_b128 a[128:131], v12 offset:24576\n"
"v_mfma_f32_16x16x32_bf16 v[248:251], a[88:91], a[136:139], v[248:251]\n"
"ds_read_b64_tr_b16 a[54:55], v15 offset:12800\n"
"v_mfma_f32_16x16x32_bf16 v[248:251], a[92:95], a[140:143], v[248:251]\n"
"ds_read_b64_tr_b16 a[56:57], v15 offset:8200\n"
"v_mfma_f32_16x16x32_bf16 v[220:223], a[84:87], a[140:143], v[220:223]\n"
"ds_read_b128 a[140:143], v13 offset:26624\n"
"v_mfma_f32_16x16x32_bf16 v[220:223], a[80:83], a[136:139], v[220:223]\n"
"ds_read_b128 a[136:139], v12 offset:26624\n"
"v_mfma_f32_16x16x32_bf16 v[224:227], a[80:83], a[144:147], v[224:227]\n"
"ds_read_b64_tr_b16 a[58:59], v15 offset:8712\n"
"v_mfma_f32_16x16x32_bf16 v[224:227], a[84:87], a[148:151], v[224:227]\n"
"s_sub_u32 s67, 1572864, s75\n"
"ds_read_b64_tr_b16 a[60:61], v15 offset:12296\n"
"v_mfma_f32_16x16x32_bf16 v[252:255], a[92:95], a[148:151], v[252:255]\n"
"ds_read_b128 a[148:151], v13 offset:28672\n"
"v_mfma_f32_16x16x32_bf16 v[252:255], a[88:91], a[144:147], v[252:255]\n"
"ds_read_b128 a[144:147], v12 offset:28672\n"
"ds_read_b64_tr_b16 a[62:63], v15 offset:12808\n"
"v_mfma_f32_16x16x32_bf16 a[28:31], a[88:91], a[152:155], a[28:31]\n"
"v_mfma_f32_16x16x32_bf16 a[28:31], a[92:95], a[156:159], a[28:31]\n"
"v_mfma_f32_16x16x32_bf16 a[24:27], a[84:87], a[156:159], a[24:27]\n"
"ds_read_b128 a[156:159], v13 offset:30720\n"
"v_mfma_f32_16x16x32_bf16 a[24:27], a[80:83], a[152:155], a[24:27]\n"
"s_waitcnt lgkmcnt(15)\n"
"ds_read_b128 a[152:155], v12 offset:30720\n"
"v_mfma_f32_16x16x32_bf16 v[32:35], a[32:35], a[96:99], v[32:35]\n"
"v_mfma_f32_16x16x32_bf16 v[32:35], a[36:39], a[100:103], v[32:35]\n"
"s_waitcnt lgkmcnt(2)\n"
"v_mfma_f32_16x16x32_bf16 v[60:63], a[44:47], a[100:103], v[60:63]\n"
"buffer_load_dwordx4 v11, s[52:55], s67 offen offset:0  lds\n"
"v_mfma_f32_16x16x32_bf16 v[60:63], a[40:43], a[96:99], v[60:63]\n"
"v_mfma_f32_16x16x32_bf16 v[64:67], a[40:43], a[104:107], v[64:67]\n"
"s_sub_u32 s67, 1588224, s75\n"
"v_mfma_f32_16x16x32_bf16 v[64:67], a[44:47], a[108:111], v[64:67]\n"
"v_mfma_f32_16x16x32_bf16 v[36:39], a[36:39], a[108:111], v[36:39]\n"
"v_mfma_f32_16x16x32_bf16 v[36:39], a[32:35], a[104:107], v[36:39]\n"
"buffer_load_dwordx4 v11, s[52:55], s67 offen offset:1024  lds\n"
"v_mfma_f32_16x16x32_bf16 v[40:43], a[32:35], a[112:115], v[40:43]\n"
"v_mfma_f32_16x16x32_bf16 v[40:43], a[36:39], a[116:119], v[40:43]\n"
"s_sub_u32 s67, 1603584, s75\n"
"v_mfma_f32_16x16x32_bf16 v[68:71], a[44:47], a[116:119], v[68:71]\n"
"v_mfma_f32_16x16x32_bf16 v[68:71], a[40:43], a[112:115], v[68:71]\n"
"buffer_load_dwordx4 v11, s[52:55], s67 offen offset:2048  lds\n"
"v_mfma_f32_16x16x32_bf16 v[72:75], a[40:43], a[120:123], v[72:75]\n"
"v_mfma_f32_16x16x32_bf16 v[72:75], a[44:47], a[124:127], v[72:75]\n"
"s_sub_u32 s67, 1618944, s75\n"
"v_mfma_f32_16x16x32_bf16 v[44:47], a[36:39], a[124:127], v[44:47]\n"
"v_mfma_f32_16x16x32_bf16 v[44:47], a[32:35], a[120:123], v[44:47]\n"
"v_mfma_f32_16x16x32_bf16 v[48:51], a[32:35], a[128:131], v[48:51]\n"
"buffer_load_dwordx4 v11, s[52:55], s67 offen offset:3072  lds\n"
"v_mfma_f32_16x16x32_bf16 v[48:51], a[36:39], a[132:135], v[48:51]\n"
"s_add_u32 m0, s64, 12288\n"
"v_mfma_f32_16x16x32_bf16 v[76:79], a[44:47], a[132:135], v[76:79]\n"
"s_waitcnt vmcnt(12)\n"
"ds_read_b64_tr_b16 a[64:65], v9 offset:0\n"
"v_mfma_f32_16x16x32_bf16 v[76:79], a[40:43], a[128:131], v[76:79]\n"
"s_sub_u32 s67, 1638400, s75\n"
"ds_read_b64_tr_b16 a[66:67], v9 offset:512\n"
"v_mfma_f32_16x16x32_bf16 v[80:83], a[40:43], a[136:139], v[80:83]\n"
"ds_read_b64_tr_b16 a[68:69], v9 offset:4096\n"
"v_mfma_f32_16x16x32_bf16 v[80:83], a[44:47], a[140:143], v[80:83]\n"
"ds_read_b64_tr_b16 a[70:71], v9 offset:4608\n"
"buffer_load_dwordx4 v11, s[52:55], s67 offen offset:0  lds\n"
"v_mfma_f32_16x16x32_bf16 v[52:55], a[36:39], a[140:143], v[52:55]\n"
"s_sub_u32 s67, 1653760, s75\n"
"ds_read_b64_tr_b16 a[72:73], v9 offset:8\n"
"v_mfma_f32_16x16x32_bf16 v[52:55], a[32:35], a[136:139], v[52:55]\n"
"L_st2664:\n"
"s_cbranch_execz L_retA\n"
"s_waitcnt lgkmcnt(5)\n"
"ds_read_b64_tr_b16 a[74:75], v9 offset:520\n"
"v_mfma_f32_16x16x32_bf16 v[56:59], a[32:35], a[144:147], v[56:59]\n"
"ds_read_b64_tr_b16 a[76:77], v9 offset:4104\n"
"v_mfma_f32_16x16x32_bf16 v[56:59], a[36:39], a[148:151], v[56:59]\n"
"ds_read_b64_tr_b16 a[78:79], v9 offset:4616\n"
"v_mfma_f32_16x16x32_bf16 v[84:87], a[44:47], a[148:151], v[84:87]\n"
"buffer_load_dwordx4 v11, s[52:55], s67 offen offset:1024  lds\n"
"v_mfma_f32_16x16x32_bf16 v[84:87], a[40:43], a[144:147], v[84:87]\n"
"ds_read_b64_tr_b16 a[80:81], v15 offset:0\n"
"v_mfma_f32_16x16x32_bf16 a[4:7], a[40:43], a[152:155], a[4:7]\n"
"s_sub_u32 s67, 1669120, s75\n"
"ds_read_b64_tr_b16 a[82:83], v15 offset:512\n"
"v_mfma_f32_16x16x32_bf16 a[4:7], a[44:47], a[156:159], a[4:7]\n"
"ds_read_b64_tr_b16 a[84:85], v15 offset:4096\n"
"v_mfma_f32_16x16x32_bf16 a[0:3], a[36:39], a[156:159], a[0:3]\n"
"ds_read_b64_tr_b16 a[86:87], v15 offset:4608\n"
"buffer_load_dwordx4 v11, s[52:55], s67 offen offset:2048  lds\n"
"v_mfma_f32_16x16x32_bf16 a[0:3], a[32:35], a[152:155], a[0:3]\n"
"ds_read_b64_tr_b16 a[88:89], v15 offset:8\n"
"v_mfma_f32_16x16x32_bf16 v[88:91], a[48:51], a[96:99], v[88:91]\n"
"s_sub_u32 s67, 1684480, s75\n"
"ds_read_b64_tr_b16 a[90:91], v15 offset:520\n"
"v_mfma_f32_16x16x32_bf16 v[88:91], a[52:55], a[100:103], v[88:91]\n"
"ds_read_b64_tr_b16 a[92:93], v15 offset:4104\n"
"v_mfma_f32_16x16x32_bf16 v[116:119], a[60:63], a[100:103], v[116:119]\n"
"ds_read_b64_tr_b16 a[94:95], v15 offset:4616\n"
"v_mfma_f32_16x16x32_bf16 v[116:119], a[56:59], a[96:99], v[116:119]\n"
"buffer_load_dwordx4 v11, s[52:55], s67 offen offset:3072  lds\n"
"v_mfma_f32_16x16x32_bf16 v[120:123], a[56:59], a[104:107], v[120:123]\n"
"v_mfma_f32_16x16x32_bf16 v[120:123], a[60:63], a[108:111], v[120:123]\n"
"s_add_u32 m0, s64, 0\n"
"v_mfma_f32_16x16x32_bf16 v[92:95], a[52:55], a[108:111], v[92:95]\n"
"v_mfma_f32_16x16x32_bf16 v[92:95], a[48:51], a[104:107], v[92:95]\n"
"s_sub_u32 s67, 1572992, s75\n"
"v_mfma_f32_16x16x32_bf16 v[96:99], a[48:51], a[112:115], v[96:99]\n"
"s_waitcnt lgkmcnt(0)\n"
"s_mov_b32 s99, 0\n"
"v_mfma_f32_16x16x32_bf16 v[96:99], a[52:55], a[116:119], v[96:99]\n"
"s_mov_b32 s98, 0\n"
"buffer_load_dwordx4 v11, s[52:55], s67 offen offset:0  lds\n"
"v_mfma_f32_16x16x32_bf16 v[124:127], a[60:63], a[116:119], v[124:127]\n"
"s_memtime s[76:77]\n"
"s_load_dword s98, s[96:97], 8320 glc\n"
"v_mfma_f32_16x16x32_bf16 v[124:127], a[56:59], a[112:115], v[124:127]\n"
"s_sub_u32 s67, 1588352, s75\n"
"v_mfma_f32_16x16x32_bf16 v[128:131], a[56:59], a[120:123], v[128:131]\n"
"v_mfma_f32_16x16x32_bf16 v[128:131], a[60:63], a[124:127], v[128:131]\n"
"buffer_load_dwordx4 v11, s[52:55], s67 offen offset:1024  lds\n"
"v_mfma_f32_16x16x32_bf16 v[100:103], a[52:55], a[124:127], v[100:103]\n"
"v_mfma_f32_16x16x32_bf16 v[100:103], a[48:51], a[120:123], v[100:103]\n"
"s_sub_u32 s67, 1603712, s75\n"
"v_mfma_f32_16x16x32_bf16 v[104:107], a[48:51], a[128:131], v[104:107]\n"
"v_mfma_f32_16x16x32_bf16 v[104:107], a[52:55], a[132:135], v[104:107]\n"
"v_mfma_f32_16x16x32_bf16 v[132:135], a[60:63], a[132:135], v[132:135]\n"
"buffer_load_dwordx4 v11, s[52:55], s67 offen offset:2048  lds\n"
"v_mfma_f32_16x16x32_bf16 v[132:135], a[56:59], a[128:131], v[132:135]\n"
"s_mov_b32 s29, s76\n"
"v_mfma_f32_16x16x32_bf16 v[136:139], a[56:59], a[136:139], v[136:139]\n"
"s_sub_u32 s67, 1619072, s75\n"
"v_mfma_f32_16x16x32_bf16 v[136:139], a[60:63], a[140:143], v[136:139]\n"
"buffer_load_dwordx4 v11, s[52:55], s67 offen offset:3072  lds\n"
"v_mfma_f32_16x16x32_bf16 v[108:111], a[52:55], a[140:143], v[108:111]\n"
"s_sub_u32 s99, s99, s98\n"
"s_sub_u32 s99, s99, s98\n"
"v_mfma_f32_16x16x32_bf16 v[108:111], a[48:51], a[136:139], v[108:111]\n"
"s_sub_u32 s99, s99, s98\n"
"s_add_u32 m0, s64, 4096\n"
"v_mfma_f32_16x16x32_bf16 v[112:115], a[48:51], a[144:147], v[112:115]\n"
"s_sub_u32 s99, s99, s98\n"
"v_mfma_f32_16x16x32_bf16 v[112:115], a[52:55], a[148:151], v[112:115]\n"
"s_sub_u32 s67, 1638528, s75\n"
"v_mfma_f32_16x16x32_bf16 v[140:143], a[60:63], a[148:151], v[140:143]\n"
"s_sub_u32 s99, s99, s98\n"
"s_sub_u32 s99, s99, s98\n"
"v_mfma_f32_16x16x32_bf16 v[140:143], a[56:59], a[144:147], v[140:143]\n"
"s_sub_u32 s99, s99, s98\n"
"buffer_load_dwordx4 v11, s[52:55], s67 offen offset:0  lds\n"
"v_mfma_f32_16x16x32_bf16 a[12:15], a[56:59], a[152:155], a[12:15]\n"
"s_sub_u32 s99, s99, s98\n"
"v_mfma_f32_16x16x32_bf16 a[12:15], a[60:63], a[156:159], a[12:15]\n"
"s_sub_u32 s67, 1653888, s75\n"
"v_mfma_f32_16x16x32_bf16 a[8:11], a[52:55], a[156:159], a[8:11]\n"
"s_sub_u32 s99, s99, s98\n"
"s_sub_u32 s99, s99, s98\n"
"v_mfma_f32_16x16x32_bf16 a[8:11], a[48:51], a[152:155], a[8:11]\n"
"s_sub_u32 s99, s99, s98\n"
"v_mfma_f32_16x16x32_bf16 v[144:147], a[64:67], a[96:99], v[144:147]\n"
"s_sub_u32 s99, s99, s98\n"
"v_mfma_f32_16x16x32_bf16 v[144:147], a[68:71], a[100:103], v[144:147]\n"
"buffer_load_dwordx4 v11, s[52:55], s67 offen offset:1024  lds\n"
"v_mfma_f32_16x16x32_bf16 v[172:175], a[76:79], a[100:103], v[172:175]\n"
"s_sub_u32 s99, s99, s98\n"
"s_sub_u32 s99, s99, s98\n"
"v_mfma_f32_16x16x32_bf16 v[172:175], a[72:75], a[96:99], v[172:175]\n"
"s_sub_u32 s99, s99, s98\n"
"s_sub_u32 s67, 1669248, s75\n"
"v_mfma_f32_16x16x32_bf16 v[176:179], a[72:75], a[104:107], v[176:179]\n"
"s_sub_u32 s99, s99, s98\n"
"v_mfma_f32_16x16x32_bf16 v[176:179], a[76:79], a[108:111], v[176:179]\n"
"buffer_load_dwordx4 v11, s[52:55], s67 offen offset:2048  lds\n"
"v_mfma_f32_16x16x32_bf16 v[148:151], a[68:71], a[108:111], v[148:151]\n"
"s_sub_u32 s99, s99, s98\n"
"s_sub_u32 s99, s99, s98\n"
"v_mfma_f32_16x16x32_bf16 v[148:151], a[64:67], a[104:107], v[148:151]\n"
"s_sub_u32 s99, s99, s98\n"
"s_sub_u32 s71, s64, s64\n"
"s_sub_u32 s67, 1684608, s75\n"
"v_mfma_f32_16x16x32_bf16 v[152:155], a[64:67], a[112:115], v[152:155]\n"
"s_sub_u32 s66, 81920, s73\n"
"s_sub_u32 s99, s99, s98\n"
"v_mfma_f32_16x16x32_bf16 v[152:155], a[68:71], a[116:119], v[152:155]\n"
"s_add_u32 s71, s71, 20480\n"
"buffer_load_dwordx4 v11, s[52:55], s67 offen offset:3072  lds\n"
"v_mfma_f32_16x16x32_bf16 v[180:183], a[76:79], a[116:119], v[180:183]\n"
"s_sub_u32 s99, s99, s98\n"
"v_add_u32 v12, s66, v7\n"
"s_sub_u32 s99, s99, s98\n"
"v_mfma_f32_16x16x32_bf16 v[180:183], a[72:75], a[112:115], v[180:183]\n"
"s_sub_u32 s99, s99, s98\n"
"s_cmp_lt_u32 %16, 2\n"
"s_cselect_b32 s67, 16384, 0\n"
"s_add_u32 s67, s67, s72\n"
"v_mfma_f32_16x16x32_bf16 v[184:187], a[72:75], a[120:123], v[184:187]\n"
"s_sub_u32 s99, s99, s98\n"
"v_add_u32 v13, s66, v8\n"
"s_add_u32 m0, s67, 139264\n"
"v_mfma_f32_16x16x32_bf16 v[184:187], a[76:79], a[124:127], v[184:187]\n"
"s_memtime s[76:77]\n"
"v_add_u32 v20, s71, v7\n"
"s_sub_u32 s67, 1792, s60\n"
"v_mfma_f32_16x16x32_bf16 v[156:159], a[68:71], a[124:127], v[156:159]\n"
"buffer_load_dwordx4 v6, s[48:51], s67 offen offset:0  lds\n"
"v_mfma_f32_16x16x32_bf16 v[156:159], a[64:67], a[120:123], v[156:159]\n"
"s_waitcnt lgkmcnt(0)\n"
"v_add_u32 v21, s71, v8\n"
"v_mfma_f32_16x16x32_bf16 v[160:163], a[64:67], a[128:131], v[160:163]\n"
"s_sub_u32 s29, s76, s29\n"
"s_sub_u32 s67, 17152, s60\n"
"v_mfma_f32_16x16x32_bf16 v[160:163], a[68:71], a[132:135], v[160:163]\n"
"s_cmp_lt_u32 s29, 620\n"
"s_cselect_b32 s99, s99, 0\n"
"v_mfma_f32_16x16x32_bf16 v[188:191], a[76:79], a[132:135], v[188:191]\n"
"s_max_u32 s31, s31, s99\n"
"buffer_load_dwordx4 v6, s[48:51], s67 offen offset:1024  lds\n"
"v_mfma_f32_16x16x32_bf16 v[188:191], a[72:75], a[128:131], v[188:191]\n"
"s_waitcnt vmcnt(18)\n"
"ds_read_b128 a[224:227], v20 offset:0\n"
"v_mfma_f32_16x16x32_bf16 v[192:195], a[72:75], a[136:139], v[192:195]\n"
"s_sub_u32 s67, 32512, s60\n"
"ds_read_b128 a[228:231], v21 offset:0\n"
"v_mfma_f32_16x16x32_bf16 v[192:195], a[76:79], a[140:143], v[192:195]\n"
"ds_read_b128 a[232:235], v20 offset:2048\n"
"v_mfma_f32_16x16x32_bf16 v[164:167], a[68:71], a[140:143], v[164:167]\n"
"v_mfma_f32_16x16x32_bf16 v[164:167], a[64:67], a[136:139], v[164:167]\n"
"ds_read_b128 a[236:239], v21 offset:2048\n"
"v_mfma_f32_16x16x32_bf16 v[168:171], a[64:67], a[144:147], v[168:171]\n"
"buffer_load_dwordx4 v6, s[48:51], s67 offen offset:2048  lds\n"
"v_mfma_f32_16x16x32_bf16 v[168:171], a[68:71], a[148:151], v[168:171]\n"
"v_mfma_f32_16x16x32_bf16 v[196:199], a[76:79], a[148:151], v[196:199]\n"
"s_sub_u32 s67, 47872, s60\n"
"v_mfma_f32_16x16x32_bf16 v[196:199], a[72:75], a[144:147], v[196:199]\n"
"v_mfma_f32_16x16x32_bf16 a[20:23], a[72:75], a[152:155], a[20:23]\n"
"buffer_load_dwordx4 v6, s[48:51], s67 offen offset:3072  lds\n"
"v_mfma_f32_16x16x32_bf16 a[20:23], a[76:79], a[156:159], a[20:23]\n"
"s_waitcnt lgkmcnt(0)\n"
"v_mfma_f32_16x16x32_bf16 a[16:19], a[68:71], a[156:159], a[16:19]\n"
"s_add_u32 m0, s64, 16256\n"
"v_mfma_f32_16x16x32_bf16 a[16:19], a[64:67], a[152:155], a[16:19]\n"
"s_barrier\n"
"s_waitcnt vmcnt(12)\n"
"ds_read_b64_tr_b16 a[32:33], v9 offset:8192\n"
"v_mfma_f32_16x16x32_bf16 v[200:203], a[80:83], a[96:99], v[200:203]\n"
"ds_read_b64_tr_b16 a[34:35], v9 offset:8704\n"
"buffer_load_dwordx4 v6, s[40:43], s92 offen offset:128  lds\n"
"v_mfma_f32_16x16x32_bf16 v[200:203], a[84:87], a[100:103], v[200:203]\n"
"ds_read_b64_tr_b16 a[36:37], v9 offset:12288\n"
"v_mfma_f32_16x16x32_bf16 v[228:231], a[92:95], a[100:103], v[228:231]\n"
"ds_read_b128 a[100:103], v13 offset:32768\n"
"v_mfma_f32_16x16x32_bf16 v[228:231], a[88:91], a[96:99], v[228:231]\n"
"ds_read_b128 a[96:99], v12 offset:32768\n"
"buffer_load_dwordx4 v6, s[40:43], s93 offen offset:1152  lds\n"
"v_mfma_f32_16x16x32_bf16 v[232:235], a[88:91], a[104:107], v[232:235]\n"
"ds_read_b64_tr_b16 a[38:39], v9 offset:12800\n"
"v_mfma_f32_16x16x32_bf16 v[232:235], a[92:95], a[108:111], v[232:235]\n"
"ds_read_b64_tr_b16 a[40:41], v9 offset:8200\n"
"v_mfma_f32_16x16x32_bf16 v[204:207], a[84:87], a[108:111], v[204:207]\n"
"ds_read_b128 a[108:111], v13 offset:34816\n"
"v_mfma_f32_16x16x32_bf16 v[204:207], a[80:83], a[104:107], v[204:207]\n"
"ds_read_b128 a[104:107], v12 offset:34816\n"
"v_mfma_f32_16x16x32_bf16 v[208:211], a[80:83], a[112:115], v[208:211]\n"
"ds_read_b64_tr_b16 a[42:43], v9 offset:8712\n"
"buffer_load_dwordx4 v6, s[40:43], s94 offen offset:2176  lds\n"
"v_mfma_f32_16x16x32_bf16 v[208:211], a[84:87], a[116:119], v[208:211]\n"
"ds_read_b64_tr_b16 a[44:45], v9 offset:12296\n"
"v_mfma_f32_16x16x32_bf16 v[236:239], a[92:95], a[116:119], v[236:239]\n"
"ds_read_b128 a[116:119], v13 offset:36864\n"
"v_mfma_f32_16x16x32_bf16 v[236:239], a[88:91], a[112:115], v[236:239]\n"
"ds_read_b128 a[112:115], v12 offset:36864\n"
"v_mfma_f32_16x16x32_bf16 v[240:243], a[88:91], a[120:123], v[240:243]\n"
"ds_read_b64_tr_b16 a[46:47], v9 offset:12808\n"
"v_mfma_f32_16x16x32_bf16 v[240:243], a[92:95], a[124:127], v[240:243]\n"
"ds_read_b64_tr_b16 a[48:49], v15 offset:8192\n"
"buffer_load_dwordx4 v6, s[40:43], s95 offen offset:3200  lds\n"
"v_mfma_f32_16x16x32_bf16 v[212:215], a[84:87], a[124:127], v[212:215]\n"
"ds_read_b128 a[124:127], v13 offset:38912\n"
"v_mfma_f32_16x16x32_bf16 v[212:215], a[80:83], a[120:123], v[212:215]\n"
"ds_read_b128 a[120:123], v12 offset:38912\n"
"v_mfma_f32_16x16x32_bf16 v[216:219], a[80:83], a[128:131], v[216:219]\n"
"ds_read_b64_tr_b16 a[50:51], v15 offset:8704\n"
"v_mfma_f32_16x16x32_bf16 v[216:219], a[84:87], a[132:135], v[216:219]\n"
"s_add_u32 m0, s64, 8192\n"
"ds_read_b64_tr_b16 a[52:53], v15 offset:12288\n"
"v_mfma_f32_16x16x32_bf16 v[244:247], a[92:95], a[132:135], v[244:247]\n"
"ds_read_b128 a[132:135], v13 offset:24576\n"
"v_mfma_f32_16x16x32_bf16 v[244:247], a[88:91], a[128:131], v[244:247]\n"
"ds_read_b128 a[128:131], v12 offset:24576\n"
"v_mfma_f32_16x16x32_bf16 v[248:251], a[88:91], a[136:139], v[248:251]\n"
"ds_read_b64_tr_b16 a[54:55], v15 offset:12800\n"
"v_mfma_f32_16x16x32_bf16 v[248:251], a[92:95], a[140:143], v[248:251]\n"
"ds_read_b64_tr_b16 a[56:57], v15 offset:8200\n"
"v_mfma_f32_16x16x32_bf16 v[220:223], a[84:87], a[140:143], v[220:223]\n"
"ds_read_b128 a[140:143], v13 offset:26624\n"
"v_mfma_f32_16x16x32_bf16 v[220:223], a[80:83], a[136:139], v[220:223]\n"
"ds_read_b128 a[136:139], v12 offset:26624\n"
"v_mfma_f32_16x16x32_bf16 v[224:227], a[80:83], a[144:147], v[224:227]\n"
"ds_read_b64_tr_b16 a[58:59], v15 offset:8712\n"
"v_mfma_f32_16x16x32_bf16 v[224:227], a[84:87], a[148:151], v[224:227]\n"
"s_sub_u32 s67, 1835008, s75\n"
"ds_read_b64_tr_b16 a[60:61], v15 offset:12296\n"
"v_mfma_f32_16x16x32_bf16 v[252:255], a[92:95], a[148:151], v[252:255]\n"
"ds_read_b128 a[148:151], v13 offset:28672\n"
"v_mfma_f32_16x16x32_bf16 v[252:255], a[88:91], a[144:147], v[252:255]\n"
"ds_read_b128 a[144:147], v12 offset:28672\n"
"ds_read_b64_tr_b16 a[62:63], v15 offset:12808\n"
"v_mfma_f32_16x16x32_bf16 a[28:31], a[88:91], a[152:155], a[28:31]\n"
"v_mfma_f32_16x16x32_bf16 a[28:31], a[92:95], a[156:159], a[28:31]\n"
"v_mfma_f32_16x16x32_bf16 a[24:27], a[84:87], a[156:159], a[24:27]\n"
"ds_read_b128 a[156:159], v13 offset:30720\n"
"v_mfma_f32_16x16x32_bf16 a[24:27], a[80:83], a[152:155], a[24:27]\n"
"s_waitcnt lgkmcnt(15)\n"
"ds_read_b128 a[152:155], v12 offset:30720\n"
"v_mfma_f32_16x16x32_bf16 v[32:35], a[32:35], a[96:99], v[32:35]\n"
"v_mfma_f32_16x16x32_bf16 v[32:35], a[36:39], a[100:103], v[32:35]\n"
"s_waitcnt lgkmcnt(2)\n"
"v_mfma_f32_16x16x32_bf16 v[60:63], a[44:47], a[100:103], v[60:63]\n"
"buffer_load_dwordx4 v11, s[52:55], s67 offen offset:0  lds\n"
"v_mfma_f32_16x16x32_bf16 v[60:63], a[40:43], a[96:99], v[60:63]\n"
"v_mfma_f32_16x16x32_bf16 v[64:67], a[40:43], a[104:107], v[64:67]\n"
"s_sub_u32 s67, 1850368, s75\n"
"v_mfma_f32_16x16x32_bf16 v[64:67], a[44:47], a[108:111], v[64:67]\n"
"v_mfma_f32_16x16x32_bf16 v[36:39], a[36:39], a[108:111], v[36:39]\n"
"v_mfma_f32_16x16x32_bf16 v[36:39], a[32:35], a[104:107], v[36:39]\n"
"buffer_load_dwordx4 v11, s[52:55], s67 offen offset:1024  lds\n"
"v_mfma_f32_16x16x32_bf16 v[40:43], a[32:35], a[112:115], v[40:43]\n"
"v_mfma_f32_16x16x32_bf16 v[40:43], a[36:39], a[116:119], v[40:43]\n"
"s_sub_u32 s67, 1865728, s75\n"
"v_mfma_f32_16x16x32_bf16 v[68:71], a[44:47], a[116:119], v[68:71]\n"
"v_mfma_f32_16x16x32_bf16 v[68:71], a[40:43], a[112:115], v[68:71]\n"
"buffer_load_dwordx4 v11, s[52:55], s67 offen offset:2048  lds\n"
"v_mfma_f32_16x16x32_bf16 v[72:75], a[40:43], a[120:123], v[72:75]\n"
"v_mfma_f32_16x16x32_bf16 v[72:75], a[44:47], a[124:127], v[72:75]\n"
"s_sub_u32 s67, 1881088, s75\n"
"v_mfma_f32_16x16x32_bf16 v[44:47], a[36:39], a[124:127], v[44:47]\n"
"v_mfma_f32_16x16x32_bf16 v[44:47], a[32:35], a[120:123], v[44:47]\n"
"v_mfma_f32_16x16x32_bf16 v[48:51], a[32:35], a[128:131], v[48:51]\n"
"buffer_load_dwordx4 v11, s[52:55], s67 offen offset:3072  lds\n"
"v_mfma_f32_16x16x32_bf16 v[48:51], a[36:39], a[132:135], v[48:51]\n"
"s_add_u32 m0, s64, 12288\n"
"v_mfma_f32_16x16x32_bf16 v[76:79], a[44:47], a[132:135], v[76:79]\n"
"s_waitcnt vmcnt(12)\n"
"ds_read_b64_tr_b16 a[64:65], v9 offset:0\n"
"v_mfma_f32_16x16x32_bf16 v[76:79], a[40:43], a[128:131], v[76:79]\n"
"ds_read_b64_tr_b16 a[66:67], v9 offset:512\n"
"v_mfma_f32_16x16x32_bf16 v[80:83], a[40:43], a[136:139], v[80:83]\n"
"s_sub_u32 s67, 1900544, s75\n"
"ds_read_b64_tr_b16 a[68:69], v9 offset:4096\n"
"v_mfma_f32_16x16x32_bf16 v[80:83], a[44:47], a[140:143], v[80:83]\n"
"ds_read_b64_tr_b16 a[70:71], v9 offset:4608\n"
"v_mfma_f32_16x16x32_bf16 v[52:55], a[36:39], a[140:143], v[52:55]\n"
"ds_read_b64_tr_b16 a[72:73], v9 offset:8\n"
"buffer_load_dwordx4 v11, s[52:55], s67 offen offset:0  lds\n"
"v_mfma_f32_16x16x32_bf16 v[52:55], a[32:35], a[136:139], v[52:55]\n"
"s_waitcnt lgkmcnt(5)\n"
"ds_read_b64_tr_b16 a[74:75], v9 offset:520\n"
"v_mfma_f32_16x16x32_bf16 v[56:59], a[32:35], a[144:147], v[56:59]\n"
"s_sub_u32 s67, 1915904, s75\n"
"ds_read_b64_tr_b16 a[76:77], v9 offset:4104\n"
"v_mfma_f32_16x16x32_bf16 v[56:59], a[36:39], a[148:151], v[56:59]\n"
"ds_read_b64_tr_b16 a[78:79], v9 offset:4616\n"
"v_mfma_f32_16x16x32_bf16 v[84:87], a[44:47], a[148:151], v[84:87]\n"
"buffer_load_dwordx4 v11, s[52:55], s67 offen offset:1024  lds\n"
"v_mfma_f32_16x16x32_bf16 v[84:87], a[40:43], a[144:147], v[84:87]\n"
"ds_read_b64_tr_b16 a[80:81], v15 offset:0\n"
"v_mfma_f32_16x16x32_bf16 a[4:7], a[40:43], a[152:155], a[4:7]\n"
"s_sub_u32 s67, 1931264, s75\n"
"ds_read_b64_tr_b16 a[82:83], v15 offset:512\n"
"v_mfma_f32_16x16x32_bf16 a[4:7], a[44:47], a[156:159], a[4:7]\n"
"ds_read_b64_tr_b16 a[84:85], v15 offset:4096\n"
"v_mfma_f32_16x16x32_bf16 a[0:3], a[36:39], a[156:159], a[0:3]\n"
"ds_read_b64_tr_b16 a[86:87], v15 offset:4608\n"
"buffer_load_dwordx4 v11, s[52:55], s67 offen offset:2048  lds\n"
"v_mfma_f32_16x16x32_bf16 a[0:3], a[32:35], a[152:155], a[0:3]\n"
"ds_read_b64_tr_b16 a[88:89], v15 offset:8\n"
"v_mfma_f32_16x16x32_bf16 v[88:91], a[48:51], a[96:99], v[88:91]\n"
"s_sub_u32 s67, 1946624, s75\n"
"ds_read_b64_tr_b16 a[90:91], v15 offset:520\n"
"v_mfma_f32_16x16x32_bf16 v[88:91], a[52:55], a[100:103], v[88:91]\n"
"ds_read_b64_tr_b16 a[92:93], v15 offset:4104\n"
"v_mfma_f32_16x16x32_bf16 v[116:119], a[60:63], a[100:103], v[116:119]\n"
"ds_read_b64_tr_b16 a[94:95], v15 offset:4616\n"
"v_mfma_f32_16x16x32_bf16 v[116:119], a[56:59], a[96:99], v[116:119]\n"
"buffer_load_dwordx4 v11, s[52:55], s67 offen offset:3072  lds\n"
"v_mfma_f32_16x16x32_bf16 v[120:123], a[56:59], a[104:107], v[120:123]\n"
"v_mfma_f32_16x16x32_bf16 v[120:123], a[60:63], a[108:111], v[120:123]\n"
"s_add_u32 m0, s64, 0\n"
"v_mfma_f32_16x16x32_bf16 v[92:95], a[52:55], a[108:111], v[92:95]\n"
"v_mfma_f32_16x16x32_bf16 v[92:95], a[48:51], a[104:107], v[92:95]\n"
"s_sub_u32 s67, 1835136, s75\n"
"v_mfma_f32_16x16x32_bf16 v[96:99], a[48:51], a[112:115], v[96:99]\n"
"s_waitcnt lgkmcnt(0)\n"
"s_mov_b32 s99, 0\n"
"v_mfma_f32_16x16x32_bf16 v[96:99], a[52:55], a[116:119], v[96:99]\n"
"s_mov_b32 s98, 0\n"
"buffer_load_dwordx4 v11, s[52:55], s67 offen offset:0  lds\n"
"v_mfma_f32_16x16x32_bf16 v[124:127], a[60:63], a[116:119], v[124:127]\n"
"s_memtime s[76:77]\n"
"s_load_dword s98, s[96:97], 128 glc\n"
"v_mfma_f32_16x16x32_bf16 v[124:127], a[56:59], a[112:115], v[124:127]\n"
"s_sub_u32 s67, 1850496, s75\n"
"v_mfma_f32_16x16x32_bf16 v[128:131], a[56:59], a[120:123], v[128:131]\n"
"v_mfma_f32_16x16x32_bf16 v[128:131], a[60:63], a[124:127], v[128:131]\n"
"v_mfma_f32_16x16x32_bf16 v[100:103], a[52:55], a[124:127], v[100:103]\n"
"buffer_load_dwordx4 v11, s[52:55], s67 offen offset:1024  lds\n"
"v_mfma_f32_16x16x32_bf16 v[100:103], a[48:51], a[120:123], v[100:103]\n"
"v_mfma_f32_16x16x32_bf16 v[104:107], a[48:51], a[128:131], v[104:107]\n"
"s_sub_u32 s67, 1865856, s75\n"
"v_mfma_f32_16x16x32_bf16 v[104:107], a[52:55], a[132:135], v[104:107]\n"
"v_mfma_f32_16x16x32_bf16 v[132:135], a[60:63], a[132:135], v[132:135]\n"
"buffer_load_dwordx4 v11, s[52:55], s67 offen offset:2048  lds\n"
"v_mfma_f32_16x16x32_bf16 v[132:135], a[56:59], a[128:131], v[132:135]\n"
"s_mov_b32 s29, s76\n"
"v_mfma_f32_16x16x32_bf16 v[136:139], a[56:59], a[136:139], v[136:139]\n"
"s_sub_u32 s67, 1881216, s75\n"
"v_mfma_f32_16x16x32_bf16 v[136:139], a[60:63], a[140:143], v[136:139]\n"
"v_mfma_f32_16x16x32_bf16 v[108:111], a[52:55], a[140:143], v[108:111]\n"
"s_sub_u32 s99, s99, s98\n"
"s_sub_u32 s99, s99, s98\n"
"v_mfma_f32_16x16x32_bf16 v[108:111], a[48:51], a[136:139], v[108:111]\n"
"s_sub_u32 s99, s99, s98\n"
"buffer_load_dwordx4 v11, s[52:55], s67 offen offset:3072  lds\n"
"v_mfma_f32_16x16x32_bf16 v[112:115], a[48:51], a[144:147], v[112:115]\n"
"s_sub_u32 s99, s99, s98\n"
"s_add_u32 m0, s64, 4096\n"
"v_mfma_f32_16x16x32_bf16 v[112:115], a[52:55], a[148:151], v[112:115]\n"
"v_mfma_f32_16x16x32_bf16 v[140:143], a[60:63], a[148:151], v[140:143]\n"
"s_sub_u32 s99, s99, s98\n"
"s_sub_u32 s99, s99, s98\n"
"buffer_load_dwordx4 v[24:27], v0, s[56:59], %25 offen sc1\n"
"v_mfma_f32_16x16x32_bf16 v[140:143], a[56:59], a[144:147], v[140:143]\n"
"s_sub_u32 s99, s99, s98\n"
"s_sub_u32 s67, 1900672, s75\n"
"v_mfma_f32_16x16x32_bf16 a[12:15], a[56:59], a[152:155], a[12:15]\n"
"s_sub_u32 s99, s99, s98\n"
"buffer_load_dwordx4 v11, s[52:55], s67 offen offset:0  lds\n"
"v_mfma_f32_16x16x32_bf16 a[12:15], a[60:63], a[156:159], a[12:15]\n"
"s_sub_u32 s67, 1916032, s75\n"
"v_mfma_f32_16x16x32_bf16 a[8:11], a[52:55], a[156:159], a[8:11]\n"
"s_sub_u32 s99, s99, s98\n"
"s_sub_u32 s99, s99, s98\n"
"v_mfma_f32_16x16x32_bf16 a[8:11], a[48:51], a[152:155], a[8:11]\n"
"s_sub_u32 s99, s99, s98\n"
"v_mfma_f32_16x16x32_bf16 v[144:147], a[64:67], a[96:99], v[144:147]\n"
"s_sub_u32 s99, s99, s98\n"
"v_mfma_f32_16x16x32_bf16 v[144:147], a[68:71], a[100:103], v[144:147]\n"
"buffer_load_dwordx4 v11, s[52:55], s67 offen offset:1024  lds\n"
"v_mfma_f32_16x16x32_bf16 v[172:175], a[76:79], a[100:103], v[172:175]\n"
"s_sub_u32 s99, s99, s98\n"
"s_sub_u32 s99, s99, s98\n"
"v_mfma_f32_16x16x32_bf16 v[172:175], a[72:75], a[96:99], v[172:175]\n"
"s_sub_u32 s99, s99, s98\n"
"s_sub_u32 s67, 1931392, s75\n"
"v_mfma_f32_16x16x32_bf16 v[176:179], a[72:75], a[104:107], v[176:179]\n"
"s_sub_u32 s99, s99, s98\n"
"v_mfma_f32_16x16x32_bf16 v[176:179], a[76:79], a[108:111], v[176:179]\n"
"buffer_load_dwordx4 v11, s[52:55], s67 offen offset:2048  lds\n"
"v_mfma_f32_16x16x32_bf16 v[148:151], a[68:71], a[108:111], v[148:151]\n"
"s_sub_u32 s99, s99, s98\n"
"s_sub_u32 s99, s99, s98\n"
"v_mfma_f32_16x16x32_bf16 v[148:151], a[64:67], a[104:107], v[148:151]\n"
"s_sub_u32 s99, s99, s98\n"
"s_sub_u32 s71, s64, s64\n"
"s_sub_u32 s67, 1946752, s75\n"
"v_mfma_f32_16x16x32_bf16 v[152:155], a[64:67], a[112:115], v[152:155]\n"
"s_sub_u32 s66, 122880, s73\n"
"s_sub_u32 s99, s99, s98\n"
"v_mfma_f32_16x16x32_bf16 v[152:155], a[68:71], a[116:119], v[152:155]\n"
"s_add_u32 s71, s71, 16384\n"
"buffer_load_dwordx4 v11, s[52:55], s67 offen offset:3072  lds\n"
"v_mfma_f32_16x16x32_bf16 v[180:183], a[76:79], a[116:119], v[180:183]\n"
"s_sub_u32 s99, s99, s98\n"
"v_add_u32 v12, s66, v7\n"
"s_sub_u32 s99, s99, s98\n"
"v_mfma_f32_16x16x32_bf16 v[180:183], a[72:75], a[112:115], v[180:183]\n"
"s_sub_u32 s99, s99, s98\n"
"v_min_u32 v19, 496, v0\n"
"v_mfma_f32_16x16x32_bf16 v[184:187], a[72:75], a[120:123], v[184:187]\n"
"s_sub_u32 s99, s99, s98\n"
"v_add_u32 v13, s66, v8\n"
"s_add_u32 s70, %21, 0\n"
"v_mfma_f32_16x16x32_bf16 v[184:187], a[76:79], a[124:127], v[184:187]\n"
"s_memtime s[76:77]\n"
"v_add_u32 v20, s71, v7\n"
"v_lshl_add_u32 v19, v19, 7, s70\n"
"v_mfma_f32_16x16x32_bf16 v[156:159], a[68:71], a[124:127], v[156:159]\n"
"buffer_load_dword v23, v19, s[52:55], 0 offen\n"
"v_mfma_f32_16x16x32_bf16 v[156:159], a[64:67], a[120:123], v[156:159]\n"
"s_waitcnt lgkmcnt(0)\n"
"v_add_u32 v21, s71, v8\n"
"v_min_u32 v19, 496, v0\n"
"v_mfma_f32_16x16x32_bf16 v[160:163], a[64:67], a[128:131], v[160:163]\n"
"s_sub_u32 s29, s76, s29\n"
"s_add_u32 s70, %21, 2097152\n"
"v_lshl_add_u32 v19, v19, 7, s70\n"
"v_mfma_f32_16x16x32_bf16 v[160:163], a[68:71], a[132:135], v[160:163]\n"
"s_cmp_lt_u32 s29, 620\n"
"s_cselect_b32 s99, s99, 0\n"
"buffer_load_dword v23, v19, s[52:55], 0 offen\n"
"v_mfma_f32_16x16x32_bf16 v[188:191], a[76:79], a[132:135], v[188:191]\n"
"s_max_u32 s30, s30, s99\n"
"v_mfma_f32_16x16x32_bf16 v[188:191], a[72:75], a[128:131], v[188:191]\n"
"s_waitcnt vmcnt(19)\n"
"ds_read_b128 a[240:243], v20 offset:0\n"
"v_mfma_f32_16x16x32_bf16 v[192:195], a[72:75], a[136:139], v[192:195]\n"
"ds_read_b128 a[244:247], v21 offset:0\n"
"v_mfma_f32_16x16x32_bf16 v[192:195], a[76:79], a[140:143], v[192:195]\n"
"ds_read_b128 a[248:251], v20 offset:2048\n"
"v_mfma_f32_16x16x32_bf16 v[164:167], a[68:71], a[140:143], v[164:167]\n"
"v_mfma_f32_16x16x32_bf16 v[164:167], a[64:67], a[136:139], v[164:167]\n"
"ds_read_b128 a[252:255], v21 offset:2048\n"
"v_mfma_f32_16x16x32_bf16 v[168:171], a[64:67], a[144:147], v[168:171]\n"
"v_mfma_f32_16x16x32_bf16 v[168:171], a[68:71], a[148:151], v[168:171]\n"
"v_mfma_f32_16x16x32_bf16 v[196:199], a[76:79], a[148:151], v[196:199]\n"
"v_mfma_f32_16x16x32_bf16 v[196:199], a[72:75], a[144:147], v[196:199]\n"
"v_mfma_f32_16x16x32_bf16 a[20:23], a[72:75], a[152:155], a[20:23]\n"
"v_mfma_f32_16x16x32_bf16 a[20:23], a[76:79], a[156:159], a[20:23]\n"
"s_waitcnt lgkmcnt(0)\n"
"v_mfma_f32_16x16x32_bf16 a[16:19], a[68:71], a[156:159], a[16:19]\n"
"v_mfma_f32_16x16x32_bf16 a[16:19], a[64:67], a[152:155], a[16:19]\n"
"s_barrier\n"
"s_waitcnt vmcnt(11)\n"
"ds_read_b64_tr_b16 a[32:33], v9 offset:8192\n"
"v_mfma_f32_16x16x32_bf16 v[200:203], a[80:83], a[96:99], v[200:203]\n"
"ds_read_b64_tr_b16 a[34:35], v9 offset:8704\n"
"v_mfma_f32_16x16x32_bf16 v[200:203], a[84:87], a[100:103], v[200:203]\n"
"ds_read_b64_tr_b16 a[36:37], v9 offset:12288\n"
"v_mfma_f32_16x16x32_bf16 v[228:231], a[92:95], a[100:103], v[228:231]\n"
"ds_read_b128 a[100:103], v13 offset:32768\n"
"v_mfma_f32_16x16x32_bf16 v[228:231], a[88:91], a[96:99], v[228:231]\n"
"ds_read_b128 a[96:99], v12 offset:32768\n"
"v_mfma_f32_16x16x32_bf16 v[232:235], a[88:91], a[104:107], v[232:235]\n"
"ds_read_b64_tr_b16 a[38:39], v9 offset:12800\n"
"v_mfma_f32_16x16x32_bf16 v[232:235], a[92:95], a[108:111], v[232:235]\n"
"ds_read_b64_tr_b16 a[40:41], v9 offset:8200\n"
"s_getpc_b64 s[70:71]\n"
"L_c11_:\n"
"s_add_u32 s70, s70, L_P6-L_c11_\n"
"s_addc_u32 s71, s71, 0\n"
"v_mfma_f32_16x16x32_bf16 v[204:207], a[84:87], a[108:111], v[204:207]\n"
"ds_read_b128 a[108:111], v13 offset:34816\n"
"v_mfma_f32_16x16x32_bf16 v[204:207], a[80:83], a[104:107], v[204:207]\n"
"s_lshr_b32 s66, %26, 3\n"
"ds_read_b128 a[104:107], v12 offset:34816\n"
"v_mfma_f32_16x16x32_bf16 v[208:211], a[80:83], a[112:115], v[208:211]\n"
"ds_read_b64_tr_b16 a[42:43], v9 offset:8712\n"
"v_mfma_f32_16x16x32_bf16 v[208:211], a[84:87], a[116:119], v[208:211]\n"
"ds_read_b64_tr_b16 a[44:45], v9 offset:12296\n"
"v_mfma_f32_16x16x32_bf16 v[236:239], a[92:95], a[116:119], v[236:239]\n"
"s_lshl2_add_u32 s66, s66, %16\n"
"ds_read_b128 a[116:119], v13 offset:36864\n"
"v_mfma_f32_16x16x32_bf16 v[236:239], a[88:91], a[112:115], v[236:239]\n"
"ds_read_b128 a[112:115], v12 offset:36864\n"
"v_mfma_f32_16x16x32_bf16 v[240:243], a[88:91], a[120:123], v[240:243]\n"
"ds_read_b64_tr_b16 a[46:47], v9 offset:12808\n"
"v_mfma_f32_16x16x32_bf16 v[240:243], a[92:95], a[124:127], v[240:243]\n"
"s_mul_i32 s66, s66, 384\n"
"ds_read_b64_tr_b16 a[48:49], v15 offset:8192\n"
"v_mfma_f32_16x16x32_bf16 v[212:215], a[84:87], a[124:127], v[212:215]\n"
"ds_read_b128 a[124:127], v13 offset:38912\n"
"v_mfma_f32_16x16x32_bf16 v[212:215], a[80:83], a[120:123], v[212:215]\n"
"s_min_u32 s66, s66, L_headF-L_P6-384\n"
"ds_read_b128 a[120:123], v12 offset:38912\n"
"v_mfma_f32_16x16x32_bf16 v[216:219], a[80:83], a[128:131], v[216:219]\n"
"ds_read_b64_tr_b16 a[50:51], v15 offset:8704\n"
"v_mfma_f32_16x16x32_bf16 v[216:219], a[84:87], a[132:135], v[216:219]\n"
"ds_read_b64_tr_b16 a[52:53], v15 offset:12288\n"
"v_mfma_f32_16x16x32_bf16 v[244:247], a[92:95], a[132:135], v[244:247]\n"
"ds_read_b128 a[132:135], v13 offset:24576\n"
"v_min_u32 v22, 32, v0\n"
"v_mfma_f32_16x16x32_bf16 v[244:247], a[88:91], a[128:131], v[244:247]\n"
"ds_read_b128 a[128:131], v12 offset:24576\n"
"v_mfma_f32_16x16x32_bf16 v[248:251], a[88:91], a[136:139], v[248:251]\n"
"ds_read_b64_tr_b16 a[54:55], v15 offset:12800\n"
"v_mfma_f32_16x16x32_bf16 v[248:251], a[92:95], a[140:143], v[248:251]\n"
"ds_read_b64_tr_b16 a[56:57], v15 offset:8200\n"
"v_lshl_add_u32 v22, v22, 3, s66\n"
"v_mfma_f32_16x16x32_bf16 v[220:223], a[84:87], a[140:143], v[220:223]\n"
"ds_read_b128 a[140:143], v13 offset:26624\n"
"v_mfma_f32_16x16x32_bf16 v[220:223], a[80:83], a[136:139], v[220:223]\n"
"ds_read_b128 a[136:139], v12 offset:26624\n"
"global_load_dword v23, v22, s[70:71]\n"
"v_mfma_f32_16x16x32_bf16 v[224:227], a[80:83], a[144:147], v[224:227]\n"
"ds_read_b64_tr_b16 a[58:59], v15 offset:8712\n"
"v_mfma_f32_16x16x32_bf16 v[224:227], a[84:87], a[148:151], v[224:227]\n"
"ds_read_b64_tr_b16 a[60:61], v15 offset:12296\n"
"v_mfma_f32_16x16x32_bf16 v[252:255], a[92:95], a[148:151], v[252:255]\n"
"ds_read_b128 a[148:151], v13 offset:28672\n"
"v_mfma_f32_16x16x32_bf16 v[252:255], a[88:91], a[144:147], v[252:255]\n"
"ds_read_b128 a[144:147], v12 offset:28672\n"
"ds_read_b64_tr_b16 a[62:63], v15 offset:12808\n"
"v_mfma_f32_16x16x32_bf16 a[28:31], a[88:91], a[152:155], a[28:31]\n"
"v_mfma_f32_16x16x32_bf16 a[28:31], a[92:95], a[156:159], a[28:31]\n"
"v_mfma_f32_16x16x32_bf16 a[24:27], a[84:87], a[156:159], a[24:27]\n"
"ds_read_b128 a[156:159], v13 offset:30720\n"
"v_mfma_f32_16x16x32_bf16 a[24:27], a[80:83], a[152:155], a[24:27]\n"
"s_waitcnt lgkmcnt(15)\n"
"ds_read_b128 a[152:155], v12 offset:30720\n"
"v_mfma_f32_16x16x32_bf16 v[32:35], a[32:35], a[96:99], v[32:35]\n"
"v_mfma_f32_16x16x32_bf16 v[32:35], a[36:39], a[100:103], v[32:35]\n"
"s_waitcnt lgkmcnt(2)\n"
"v_mfma_f32_16x16x32_bf16 v[60:63], a[44:47], a[100:103], v[60:63]\n"
"v_mfma_f32_16x16x32_bf16 v[60:63], a[40:43], a[96:99], v[60:63]\n"
"v_mfma_f32_16x16x32_bf16 v[64:67], a[40:43], a[104:107], v[64:67]\n"
"ds_read_b128 a[160:163], v7 offset:24576\n"
"v_mfma_f32_16x16x32_bf16 v[64:67], a[44:47], a[108:111], v[64:67]\n"
"v_mfma_f32_16x16x32_bf16 v[36:39], a[36:39], a[108:111], v[36:39]\n"
"ds_read_b128 a[164:167], v8 offset:24576\n"
"v_mfma_f32_16x16x32_bf16 v[36:39], a[32:35], a[104:107], v[36:39]\n"
"v_mfma_f32_16x16x32_bf16 v[40:43], a[32:35], a[112:115], v[40:43]\n"
"ds_read_b128 a[168:171], v7 offset:26624\n"
"v_mfma_f32_16x16x32_bf16 v[40:43], a[36:39], a[116:119], v[40:43]\n"
"ds_read_b128 a[172:175], v8 offset:26624\n"
"v_mfma_f32_16x16x32_bf16 v[68:71], a[44:47], a[116:119], v[68:71]\n"
"s_add_u32 s70, s78, 1\n"
"v_mfma_f32_16x16x32_bf16 v[68:71], a[40:43], a[112:115], v[68:71]\n"
"ds_read_b128 a[176:179], v7 offset:28672\n"
"v_mfma_f32_16x16x32_bf16 v[72:75], a[40:43], a[120:123], v[72:75]\n"
"ds_read_b128 a[180:183], v8 offset:28672\n"
"v_mfma_f32_16x16x32_bf16 v[72:75], a[44:47], a[124:127], v[72:75]\n"
"v_mfma_f32_16x16x32_bf16 v[44:47], a[36:39], a[124:127], v[44:47]\n"
"ds_read_b128 a[184:187], v7 offset:30720\n"
"v_mfma_f32_16x16x32_bf16 v[44:47], a[32:35], a[120:123], v[44:47]\n"
"v_mfma_f32_16x16x32_bf16 v[48:51], a[32:35], a[128:131], v[48:51]\n"
"ds_read_b128 a[188:191], v8 offset:30720\n"
"v_mfma_f32_16x16x32_bf16 v[48:51], a[36:39], a[132:135], v[48:51]\n"
"v_mfma_f32_16x16x32_bf16 v[76:79], a[44:47], a[132:135], v[76:79]\n"
"s_waitcnt vmcnt(3)\n"
"ds_read_b64_tr_b16 a[64:65], v9 offset:0\n"
"v_mfma_f32_16x16x32_bf16 v[76:79], a[40:43], a[128:131], v[76:79]\n"
"ds_read_b64_tr_b16 a[66:67], v9 offset:512\n"
"v_mfma_f32_16x16x32_bf16 v[80:83], a[40:43], a[136:139], v[80:83]\n"
"L_st3225:\n"
"s_cbranch_execz L_retA\n"
"ds_read_b64_tr_b16 a[68:69], v9 offset:4096\n"
"v_mfma_f32_16x16x32_bf16 v[80:83], a[44:47], a[140:143], v[80:83]\n"
"ds_read_b64_tr_b16 a[70:71], v9 offset:4608\n"
"v_mfma_f32_16x16x32_bf16 v[52:55], a[36:39], a[140:143], v[52:55]\n"
"ds_read_b64_tr_b16 a[72:73], v9 offset:8\n"
"v_subrev_u32 v24, s70, v24\n"
"v_mfma_f32_16x16x32_bf16 v[52:55], a[32:35], a[136:139], v[52:55]\n"
"s_waitcnt lgkmcnt(13)\n"
"ds_read_b64_tr_b16 a[74:75], v9 offset:520\n"
"v_xor_b32 v25, s79, v25\n"
"v_mfma_f32_16x16x32_bf16 v[56:59], a[32:35], a[144:147], v[56:59]\n"
"ds_read_b64_tr_b16 a[76:77], v9 offset:4104\n"
"v_subrev_u32 v26, s70, v26\n"
"v_mfma_f32_16x16x32_bf16 v[56:59], a[36:39], a[148:151], v[56:59]\n"
"ds_read_b64_tr_b16 a[78:79], v9 offset:4616\n"
"v_xor_b32 v27, s79, v27\n"
"v_mfma_f32_16x16x32_bf16 v[84:87], a[44:47], a[148:151], v[84:87]\n"
"ds_read_b64_tr_b16 a[80:81], v15 offset:0\n"
"v_or_b32 v24, v24, v26\n"
"v_mfma_f32_16x16x32_bf16 v[84:87], a[40:43], a[144:147], v[84:87]\n"
"ds_read_b64_tr_b16 a[82:83], v15 offset:512\n"
"v_or_b32 v25, v25, v27\n"
"v_mfma_f32_16x16x32_bf16 a[4:7], a[40:43], a[152:155], a[4:7]\n"
"ds_read_b64_tr_b16 a[84:85], v15 offset:4096\n"
"v_or_b32 v24, v24, v25\n"
"v_mfma_f32_16x16x32_bf16 a[4:7], a[44:47], a[156:159], a[4:7]\n"
"ds_read_b64_tr_b16 a[86:87], v15 offset:4608\n"
"v_cmp_lt_u32 vcc, 15, v24\n"
"v_mfma_f32_16x16x32_bf16 a[0:3], a[36:39], a[156:159], a[0:3]\n"
"ds_read_b64_tr_b16 a[88:89], v15 offset:8\n"
"s_cbranch_vccnz L_slow3\n"
"L_back3:\n"
"v_mfma_f32_16x16x32_bf16 a[0:3], a[32:35], a[152:155], a[0:3]\n"
"ds_read_b64_tr_b16 a[90:91], v15 offset:520\n"
"v_mfma_f32_16x16x32_bf16 v[88:91], a[48:51], a[96:99], v[88:91]\n"
"ds_read_b64_tr_b16 a[92:93], v15 offset:4104\n"
"v_mfma_f32_16x16x32_bf16 v[88:91], a[52:55], a[100:103], v[88:91]\n"
"global_load_dwordx4 a[32:35], v0, s[34:35] offset:-4096 sc1\n"
"s_barrier\n"
"v_mfma_f32_16x16x32_bf16 v[116:119], a[60:63], a[100:103], v[116:119]\n"
"ds_read_b64_tr_b16 a[94:95], v15 offset:4616\n"
"v_mfma_f32_16x16x32_bf16 v[116:119], a[56:59], a[96:99], v[116:119]\n"
"ds_write_b64 v2, a[192:193] offset:16384\n"
"v_mfma_f32_16x16x32_bf16 v[120:123], a[56:59], a[104:107], v[120:123]\n"
"v_mfma_f32_16x16x32_bf16 v[120:123], a[60:63], a[108:111], v[120:123]\n"
"ds_write_b64 v2, a[194:195] offset:16392\n"
"v_mfma_f32_16x16x32_bf16 v[92:95], a[52:55], a[108:111], v[92:95]\n"
"v_mfma_f32_16x16x32_bf16 v[92:95], a[48:51], a[104:107], v[92:95]\n"
"ds_write_b64 v2, a[196:197] offset:17408\n"
"v_mfma_f32_16x16x32_bf16 v[96:99], a[48:51], a[112:115], v[96:99]\n"
"s_waitcnt lgkmcnt(3)\n"
"v_mfma_f32_16x16x32_bf16 v[96:99], a[52:55], a[116:119], v[96:99]\n"
"global_load_dwordx4 a[36:39], v0, s[34:35] offset:-3072 sc1\n"
"v_mfma_f32_16x16x32_bf16 v[124:127], a[60:63], a[116:119], v[124:127]\n"
"ds_write_b64 v2, a[198:199] offset:17416\n"
"v_mfma_f32_16x16x32_bf16 v[124:127], a[56:59], a[112:115], v[124:127]\n"
"ds_write_b64 v2, a[200:201] offset:18432\n"
"v_mfma_f32_16x16x32_bf16 v[128:131], a[56:59], a[120:123], v[128:131]\n"
"v_mfma_f32_16x16x32_bf16 v[128:131], a[60:63], a[124:127], v[128:131]\n"
"ds_write_b64 v2, a[202:203] offset:18440\n"
"v_mfma_f32_16x16x32_bf16 v[100:103], a[52:55], a[124:127], v[100:103]\n"
"v_mfma_f32_16x16x32_bf16 v[100:103], a[48:51], a[120:123], v[100:103]\n"
"ds_write_b64 v2, a[204:205] offset:19456\n"
"v_mfma_f32_16x16x32_bf16 v[104:107], a[48:51], a[128:131], v[104:107]\n"
"ds_write_b64 v2, a[206:207] offset:19464\n"
"v_mfma_f32_16x16x32_bf16 v[104:107], a[52:55], a[132:135], v[104:107]\n"
"global_load_dwordx4 a[40:43], v0, s[34:35] offset:-2048 sc1\n"
"v_mfma_f32_16x16x32_bf16 v[132:135], a[60:63], a[132:135], v[132:135]\n"
"ds_write_b64 v2, a[208:209] offset:20480\n"
"v_mfma_f32_16x16x32_bf16 v[132:135], a[56:59], a[128:131], v[132:135]\n"
"v_mfma_f32_16x16x32_bf16 v[136:139], a[56:59], a[136:139], v[136:139]\n"
"ds_write_b64 v2, a[210:211] offset:20488\n"
"v_mfma_f32_16x16x32_bf16 v[136:139], a[60:63], a[140:143], v[136:139]\n"
"v_mfma_f32_16x16x32_bf16 v[108:111], a[52:55], a[140:143], v[108:111]\n"
"ds_write_b64 v2, a[212:213] offset:21504\n"
"v_mfma_f32_16x16x32_bf16 v[108:111], a[48:51], a[136:139], v[108:111]\n"
"v_mfma_f32_16x16x32_bf16 v[112:115], a[48:51], a[144:147], v[112:115]\n"
"ds_write_b64 v2, a[214:215] offset:21512\n"
"v_mfma_f32_16x16x32_bf16 v[112:115], a[52:55], a[148:151], v[112:115]\n"
"global_load_dwordx4 a[44:47], v0, s[34:35] offset:-1024 sc1\n"
"v_mfma_f32_16x16x32_bf16 v[140:143], a[60:63], a[148:151], v[140:143]\n"
"ds_write_b64 v2, a[216:217] offset:22528\n"
"v_mfma_f32_16x16x32_bf16 v[140:143], a[56:59], a[144:147], v[140:143]\n"
"v_mfma_f32_16x16x32_bf16 a[12:15], a[56:59], a[152:155], a[12:15]\n"
"ds_write_b64 v2, a[218:219] offset:22536\n"
"v_mfma_f32_16x16x32_bf16 a[12:15], a[60:63], a[156:159], a[12:15]\n"
"v_mfma_f32_16x16x32_bf16 a[8:11], a[52:55], a[156:159], a[8:11]\n"
"ds_write_b64 v2, a[220:221] offset:23552\n"
"v_mfma_f32_16x16x32_bf16 a[8:11], a[48:51], a[152:155], a[8:11]\n"
"v_mfma_f32_16x16x32_bf16 v[144:147], a[64:67], a[96:99], v[144:147]\n"
"ds_write_b64 v2, a[222:223] offset:23560\n"
"v_mfma_f32_16x16x32_bf16 v[144:147], a[68:71], a[100:103], v[144:147]\n"
"global_load_dwordx4 a[48:51], v0, s[34:35] offset:0 sc1\n"
"v_mfma_f32_16x16x32_bf16 v[172:175], a[76:79], a[100:103], v[172:175]\n"
"ds_write_b64 v2, a[224:225] offset:24576\n"
"v_mfma_f32_16x16x32_bf16 v[172:175], a[72:75], a[96:99], v[172:175]\n"
"ds_write_b64 v2, a[226:227] offset:24584\n"
"v_mfma_f32_16x16x32_bf16 v[176:179], a[72:75], a[104:107], v[176:179]\n"
"v_mfma_f32_16x16x32_bf16 v[176:179], a[76:79], a[108:111], v[176:179]\n"
"ds_write_b64 v2, a[228:229] offset:25600\n"
"v_mfma_f32_16x16x32_bf16 v[148:151], a[68:71], a[108:111], v[148:151]\n"
"v_mfma_f32_16x16x32_bf16 v[148:151], a[64:67], a[104:107], v[148:151]\n"
"ds_write_b64 v2, a[230:231] offset:25608\n"
"v_mfma_f32_16x16x32_bf16 v[152:155], a[64:67], a[112:115], v[152:155]\n"
"ds_write_b64 v2, a[232:233] offset:26624\n"
"v_mfma_f32_16x16x32_bf16 v[152:155], a[68:71], a[116:119], v[152:155]\n"
"global_load_dwordx4 a[52:55], v0, s[34:35] offset:1024 sc1\n"
"v_mfma_f32_16x16x32_bf16 v[180:183], a[76:79], a[116:119], v[180:183]\n"
"v_mfma_f32_16x16x32_bf16 v[180:183], a[72:75], a[112:115], v[180:183]\n"
"ds_write_b64 v2, a[234:235] offset:26632\n"
"v_mfma_f32_16x16x32_bf16 v[184:187], a[72:75], a[120:123], v[184:187]\n"
"v_mfma_f32_16x16x32_bf16 v[184:187], a[76:79], a[124:127], v[184:187]\n"
"ds_write_b64 v2, a[236:237] offset:27648\n"
"v_mfma_f32_16x16x32_bf16 v[156:159], a[68:71], a[124:127], v[156:159]\n"
"v_mfma_f32_16x16x32_bf16 v[156:159], a[64:67], a[120:123], v[156:159]\n"
"ds_write_b64 v2, a[238:239] offset:27656\n"
"v_mfma_f32_16x16x32_bf16 v[160:163], a[64:67], a[128:131], v[160:163]\n"
"ds_write_b64 v2, a[240:241] offset:28672\n"
"v_mfma_f32_16x16x32_bf16 v[160:163], a[68:71], a[132:135], v[160:163]\n"
"global_load_dwordx4 a[56:59], v0, s[34:35] offset:2048 sc1\n"
"v_mfma_f32_16x16x32_bf16 v[188:191], a[76:79], a[132:135], v[188:191]\n"
"ds_write_b64 v2, a[242:243] offset:28680\n"
"v_mfma_f32_16x16x32_bf16 v[188:191], a[72:75], a[128:131], v[188:191]\n"
"v_mfma_f32_16x16x32_bf16 v[192:195], a[72:75], a[136:139], v[192:195]\n"
"ds_write_b64 v2, a[244:245] offset:29696\n"
"v_mfma_f32_16x16x32_bf16 v[192:195], a[76:79], a[140:143], v[192:195]\n"
"v_mfma_f32_16x16x32_bf16 v[164:167], a[68:71], a[140:143], v[164:167]\n"
"ds_write_b64 v2, a[246:247] offset:29704\n"
"v_mfma_f32_16x16x32_bf16 v[164:167], a[64:67], a[136:139], v[164:167]\n"
"v_mfma_f32_16x16x32_bf16 v[168:171], a[64:67], a[144:147], v[168:171]\n"
"ds_write_b64 v2, a[248:249] offset:30720\n"
"v_mfma_f32_16x16x32_bf16 v[168:171], a[68:71], a[148:151], v[168:171]\n"
"global_load_dwordx4 a[60:63], v0, s[34:35] offset:3072 sc1\n"
"v_mfma_f32_16x16x32_bf16 v[196:199], a[76:79], a[148:151], v[196:199]\n"
"ds_write_b64 v2, a[250:251] offset:30728\n"
"v_mfma_f32_16x16x32_bf16 v[196:199], a[72:75], a[144:147], v[196:199]\n"
"v_mfma_f32_16x16x32_bf16 a[20:23], a[72:75], a[152:155], a[20:23]\n"
"s_add_u32 s34, s34, 8192\n"
"ds_write_b64 v2, a[252:253] offset:31744\n"
"v_mfma_f32_16x16x32_bf16 a[20:23], a[76:79], a[156:159], a[20:23]\n"
"v_mfma_f32_16x16x32_bf16 a[16:19], a[68:71], a[156:159], a[16:19]\n"
"ds_write_b64 v2, a[254:255] offset:31752\n"
"v_mfma_f32_16x16x32_bf16 a[16:19], a[64:67], a[152:155], a[16:19]\n"
"v_mfma_f32_16x16x32_bf16 v[200:203], a[80:83], a[96:99], v[200:203]\n"
"v_lshrrev_b32 v16, 4, v0\n"
"v_mfma_f32_16x16x32_bf16 v[200:203], a[84:87], a[100:103], v[200:203]\n"
"global_load_dwordx4 a[64:67], v0, s[34:35] offset:-4096 sc1\n"
"v_mfma_f32_16x16x32_bf16 v[228:231], a[92:95], a[100:103], v[228:231]\n"
"ds_read_b128 a[100:103], v2 offset:17408\n"
"v_mfma_f32_16x16x32_bf16 v[228:231], a[88:91], a[96:99], v[228:231]\n"
"ds_read_b128 a[96:99], v2 offset:16384\n"
"v_mfma_f32_16x16x32_bf16 v[232:235], a[88:91], a[104:107], v[232:235]\n"
"v_and_b32 v16, 0x30, v16\n"
"v_mfma_f32_16x16x32_bf16 v[232:235], a[92:95], a[108:111], v[232:235]\n"
"buffer_load_dwordx2 v[18:19], v16, s[44:47], s61 offen offset:128\n"
"v_mfma_f32_16x16x32_bf16 v[204:207], a[84:87], a[108:111], v[204:207]\n"
"ds_read_b128 a[108:111], v2 offset:19456\n"
"v_mfma_f32_16x16x32_bf16 v[204:207], a[80:83], a[104:107], v[204:207]\n"
"ds_read_b128 a[104:107], v2 offset:18432\n"
"v_mfma_f32_16x16x32_bf16 v[208:211], a[80:83], a[112:115], v[208:211]\n"
"v_lshrrev_b32 v20, 4, v0\n"
"v_mfma_f32_16x16x32_bf16 v[208:211], a[84:87], a[116:119], v[208:211]\n"
"global_load_dwordx4 a[68:71], v0, s[34:35] offset:-3072 sc1\n"
"v_mfma_f32_16x16x32_bf16 v[236:239], a[92:95], a[116:119], v[236:239]\n"
"ds_read_b128 a[116:119], v2 offset:21504\n"
"v_mfma_f32_16x16x32_bf16 v[236:239], a[88:91], a[112:115], v[236:239]\n"
"ds_read_b128 a[112:115], v2 offset:20480\n"
"v_mfma_f32_16x16x32_bf16 v[240:243], a[88:91], a[120:123], v[240:243]\n"
"v_and_b32 v20, 0x30, v20\n"
"v_mfma_f32_16x16x32_bf16 v[240:243], a[92:95], a[124:127], v[240:243]\n"
"buffer_load_dwordx2 v[22:23], v20, s[44:47], s61 offen offset:136\n"
"v_mfma_f32_16x16x32_bf16 v[212:215], a[84:87], a[124:127], v[212:215]\n"
"ds_read_b128 a[124:127], v2 offset:23552\n"
"v_mfma_f32_16x16x32_bf16 v[212:215], a[80:83], a[120:123], v[212:215]\n"
"ds_read_b128 a[120:123], v2 offset:22528\n"
"v_mfma_f32_16x16x32_bf16 v[216:219], a[80:83], a[128:131], v[216:219]\n"
"v_lshrrev_b32 v24, 4, v0\n"
"v_mfma_f32_16x16x32_bf16 v[216:219], a[84:87], a[132:135], v[216:219]\n"
"global_load_dwordx4 a[72:75], v0, s[34:35] offset:-2048 sc1\n"
"v_mfma_f32_16x16x32_bf16 v[244:247], a[92:95], a[132:135], v[244:247]\n"
"ds_read_b128 a[132:135], v2 offset:25600\n"
"v_and_b32 v24, 0x30, v24\n"
"v_mfma_f32_16x16x32_bf16 v[244:247], a[88:91], a[128:131], v[244:247]\n"
"ds_read_b128 a[128:131], v2 offset:24576\n"
"v_mfma_f32_16x16x32_bf16 v[248:251], a[88:91], a[136:139], v[248:251]\n"
"buffer_load_dwordx2 v[26:27], v24, s[44:47], s61 offen offset:192\n"
"v_mfma_f32_16x16x32_bf16 v[248:251], a[92:95], a[140:143], v[248:251]\n"
"v_lshrrev_b32 v28, 4, v0\n"
"v_mfma_f32_16x16x32_bf16 v[220:223], a[84:87], a[140:143], v[220:223]\n"
"ds_read_b128 a[140:143], v2 offset:27648\n"
"v_mfma_f32_16x16x32_bf16 v[220:223], a[80:83], a[136:139], v[220:223]\n"
"ds_read_b128 a[136:139], v2 offset:26624\n"
"v_mfma_f32_16x16x32_bf16 v[224:227], a[80:83], a[144:147], v[224:227]\n"
"v_and_b32 v28, 0x30, v28\n"
"v_mfma_f32_16x16x32_bf16 v[224:227], a[84:87], a[148:151], v[224:227]\n"
"global_load_dwordx4 a[76:79], v0, s[34:35] offset:-1024 sc1\n"
"v_mfma_f32_16x16x32_bf16 v[252:255], a[92:95], a[148:151], v[252:255]\n"
"ds_read_b128 a[148:151], v2 offset:29696\n"
"v_mfma_f32_16x16x32_bf16 v[252:255], a[88:91], a[144:147], v[252:255]\n"
"ds_read_b128 a[144:147], v2 offset:28672\n"
"v_mfma_f32_16x16x32_bf16 a[28:31], a[88:91], a[152:155], a[28:31]\n"
"buffer_load_dwordx2 v[30:31], v28, s[44:47], s61 offen offset:200\n"
"v_mfma_f32_16x16x32_bf16 a[28:31], a[92:95], a[156:159], a[28:31]\n"
"v_mfma_f32_16x16x32_bf16 a[24:27], a[84:87], a[156:159], a[24:27]\n"
"ds_read_b128 a[156:159], v2 offset:31744\n"
"v_mfma_f32_16x16x32_bf16 a[24:27], a[80:83], a[152:155], a[24:27]\n"
"s_waitcnt vmcnt(12) lgkmcnt(11)\n"
"ds_read_b128 a[152:155], v2 offset:30720\n"
"v_mfma_f32_16x16x32_bf16 v[32:35], a[32:35], a[96:99], v[32:35]\n"
"v_mfma_f32_16x16x32_bf16 v[32:35], a[36:39], a[100:103], v[32:35]\n"
"global_load_dwordx4 a[80:83], v0, s[34:35] offset:0 sc1\n"
"v_mfma_f32_16x16x32_bf16 v[60:63], a[44:47], a[100:103], v[60:63]\n"
"s_barrier\n"
"v_mfma_f32_16x16x32_bf16 v[60:63], a[40:43], a[96:99], v[60:63]\n"
"v_mfma_f32_16x16x32_bf16 v[64:67], a[40:43], a[104:107], v[64:67]\n"
"v_mfma_f32_16x16x32_bf16 v[64:67], a[44:47], a[108:111], v[64:67]\n"
"v_mfma_f32_16x16x32_bf16 v[36:39], a[36:39], a[108:111], v[36:39]\n"
"v_mfma_f32_16x16x32_bf16 v[36:39], a[32:35], a[104:107], v[36:39]\n"
"s_waitcnt lgkmcnt(8)\n"
"v_mfma_f32_16x16x32_bf16 v[40:43], a[32:35], a[112:115], v[40:43]\n"
"v_mfma_f32_16x16x32_bf16 v[40:43], a[36:39], a[116:119], v[40:43]\n"
"global_load_dwordx4 a[84:87], v0, s[34:35] offset:1024 sc1\n"
"v_mfma_f32_16x16x32_bf16 v[68:71], a[44:47], a[116:119], v[68:71]\n"
"v_mfma_f32_16x16x32_bf16 v[68:71], a[40:43], a[112:115], v[68:71]\n"
"v_mfma_f32_16x16x32_bf16 v[72:75], a[40:43], a[120:123], v[72:75]\n"
"v_mfma_f32_16x16x32_bf16 v[72:75], a[44:47], a[124:127], v[72:75]\n"
"s_add_u32 s61, s61, 2048\n"
"v_mfma_f32_16x16x32_bf16 v[44:47], a[36:39], a[124:127], v[44:47]\n"
"v_mfma_f32_16x16x32_bf16 v[44:47], a[32:35], a[120:123], v[44:47]\n"
"s_waitcnt lgkmcnt(4)\n"
"v_mfma_f32_16x16x32_bf16 v[48:51], a[32:35], a[128:131], v[48:51]\n"
"s_waitcnt vmcnt(8)\n"
"v_lshlrev_b32 v16, 16, v18\n"
"v_mfma_f32_16x16x32_bf16 v[48:51], a[36:39], a[132:135], v[48:51]\n"
"global_load_dwordx4 a[88:91], v0, s[34:35] offset:2048 sc1\n"
"v_and_b32 v17, s63, v18\n"
"v_mfma_f32_16x16x32_bf16 v[76:79], a[44:47], a[132:135], v[76:79]\n"
"v_lshlrev_b32 v18, 16, v19\n"
"v_and_b32 v19, s63, v19\n"
"v_mfma_f32_16x16x32_bf16 v[76:79], a[40:43], a[128:131], v[76:79]\n"
"v_add_f32 v144, v144, v16\n"
"v_mfma_f32_16x16x32_bf16 v[80:83], a[40:43], a[136:139], v[80:83]\n"
"v_add_f32 v145, v145, v17\n"
"v_add_f32 v146, v146, v18\n"
"v_mfma_f32_16x16x32_bf16 v[80:83], a[44:47], a[140:143], v[80:83]\n"
"v_add_f32 v147, v147, v19\n"
"v_mfma_f32_16x16x32_bf16 v[52:55], a[36:39], a[140:143], v[52:55]\n"
"v_add_f32 v148, v148, v16\n"
"v_mfma_f32_16x16x32_bf16 v[52:55], a[32:35], a[136:139], v[52:55]\n"
"s_mov_b32 s99, 0\n"
"v_add_f32 v149, v149, v17\n"
"s_waitcnt lgkmcnt(0)\n"
"v_mfma_f32_16x16x32_bf16 v[56:59], a[32:35], a[144:147], v[56:59]\n"
"s_mov_b32 s98, 0\n"
"v_add_f32 v150, v150, v18\n"
"v_add_f32 v151, v151, v19\n"
"v_mfma_f32_16x16x32_bf16 v[56:59], a[36:39], a[148:151], v[56:59]\n"
"global_load_dwordx4 a[92:95], v0, s[34:35] offset:3072 sc1\n"
"s_memtime s[76:77]\n"
"s_load_dword s98, s[96:97], 8448 glc\n"
"v_mfma_f32_16x16x32_bf16 v[84:87], a[44:47], a[148:151], v[84:87]\n"
"v_add_f32 v152, v152, v16\n"
"v_add_f32 v153, v153, v17\n"
"v_mfma_f32_16x16x32_bf16 v[84:87], a[40:43], a[144:147], v[84:87]\n"
"v_add_f32 v154, v154, v18\n"
"v_add_f32 v155, v155, v19\n"
"v_mfma_f32_16x16x32_bf16 a[4:7], a[40:43], a[152:155], a[4:7]\n"
"s_add_u32 s34, s34, 8192\n"
"v_add_f32 v156, v156, v16\n"
"v_mfma_f32_16x16x32_bf16 a[4:7], a[44:47], a[156:159], a[4:7]\n"
"v_add_f32 v157, v157, v17\n"
"v_mfma_f32_16x16x32_bf16 a[0:3], a[36:39], a[156:159], a[0:3]\n"
"v_add_f32 v158, v158, v18\n"
"v_add_f32 v159, v159, v19\n"
"v_mfma_f32_16x16x32_bf16 a[0:3], a[32:35], a[152:155], a[0:3]\n"
"v_add_f32 v160, v160, v16\n"
"v_mfma_f32_16x16x32_bf16 v[88:91], a[48:51], a[96:99], v[88:91]\n"
"v_add_f32 v161, v161, v17\n"
"v_add_f32 v162, v162, v18\n"
"v_mfma_f32_16x16x32_bf16 v[88:91], a[52:55], a[100:103], v[88:91]\n"
"global_load_dwordx4 a[32:35], v0, s[34:35] offset:-4096 sc1\n"
"v_add_f32 v163, v163, v19\n"
"v_mfma_f32_16x16x32_bf16 v[116:119], a[60:63], a[100:103], v[116:119]\n"
"s_mov_b32 s29, s76\n"
"ds_read_b128 a[100:103], v3 offset:17408\n"
"v_add_f32 v164, v164, v16\n"
"v_mfma_f32_16x16x32_bf16 v[116:119], a[56:59], a[96:99], v[116:119]\n"
"ds_read_b128 a[96:99], v3 offset:16384\n"
"v_add_f32 v165, v165, v17\n"
"v_mfma_f32_16x16x32_bf16 v[120:123], a[56:59], a[104:107], v[120:123]\n"
"s_sub_u32 s99, s99, s98\n"
"v_add_f32 v166, v166, v18\n"
"v_add_f32 v167, v167, v19\n"
"v_mfma_f32_16x16x32_bf16 v[120:123], a[60:63], a[108:111], v[120:123]\n"
"s_sub_u32 s99, s99, s98\n"
"v_add_f32 v168, v168, v16\n"
"v_add_f32 v169, v169, v17\n"
"v_mfma_f32_16x16x32_bf16 v[92:95], a[52:55], a[108:111], v[92:95]\n"
"s_sub_u32 s99, s99, s98\n"
"ds_read_b128 a[108:111], v3 offset:19456\n"
"v_add_f32 v170, v170, v18\n"
"v_mfma_f32_16x16x32_bf16 v[92:95], a[48:51], a[104:107], v[92:95]\n"
"s_sub_u32 s99, s99, s98\n"
"ds_read_b128 a[104:107], v3 offset:18432\n"
"v_add_f32 v171, v171, v19\n"
"v_mfma_f32_16x16x32_bf16 v[96:99], a[48:51], a[112:115], v[96:99]\n"
"s_sub_u32 s99, s99, s98\n"
"v_accvgpr_read_b32 v12, a16\n"
"v_accvgpr_read_b32 v13, a17\n"
"v_mfma_f32_16x16x32_bf16 v[96:99], a[52:55], a[116:119], v[96:99]\n"
"global_load_dwordx4 a[36:39], v0, s[34:35] offset:-3072 sc1\n"
"s_sub_u32 s99, s99, s98\n"
"v_mfma_f32_16x16x32_bf16 v[124:127], a[60:63], a[116:119], v[124:127]\n"
"s_sub_u32 s99, s99, s98\n"
"ds_read_b128 a[116:119], v3 offset:21504\n"
"v_accvgpr_read_b32 v14, a18\n"
"v_mfma_f32_16x16x32_bf16 v[124:127], a[56:59], a[112:115], v[124:127]\n"
"s_sub_u32 s99, s99, s98\n"
"ds_read_b128 a[112:115], v3 offset:20480\n"
"v_accvgpr_read_b32 v15, a19\n"
"v_mfma_f32_16x16x32_bf16 v[128:131], a[56:59], a[120:123], v[128:131]\n"
"s_sub_u32 s99, s99, s98\n"
"v_add_f32 v12, v12, v16\n"
"v_add_f32 v13, v13, v17\n"
"v_mfma_f32_16x16x32_bf16 v[128:131], a[60:63], a[124:127], v[128:131]\n"
"s_sub_u32 s99, s99, s98\n"
"v_add_f32 v14, v14, v18\n"
"v_add_f32 v15, v15, v19\n"
"v_mfma_f32_16x16x32_bf16 v[100:103], a[52:55], a[124:127], v[100:103]\n"
"s_sub_u32 s99, s99, s98\n"
"ds_read_b128 a[124:127], v3 offset:23552\n"
"v_accvgpr_write_b32 a16, v12\n"
"v_mfma_f32_16x16x32_bf16 v[100:103], a[48:51], a[120:123], v[100:103]\n"
"s_sub_u32 s99, s99, s98\n"
"ds_read_b128 a[120:123], v3 offset:22528\n"
"v_accvgpr_write_b32 a17, v13\n"
"v_mfma_f32_16x16x32_bf16 v[104:107], a[48:51], a[128:131], v[104:107]\n"
"s_sub_u32 s99, s99, s98\n"
"v_accvgpr_write_b32 a18, v14\n"
"v_accvgpr_write_b32 a19, v15\n"
"v_mfma_f32_16x16x32_bf16 v[104:107], a[52:55], a[132:135], v[104:107]\n"
"s_sub_u32 s99, s99, s98\n"
"s_waitcnt vmcnt(10)\n"
"global_load_dwordx4 a[40:43], v0, s[34:35] offset:-2048 sc1\n"
"v_mfma_f32_16x16x32_bf16 v[132:135], a[60:63], a[132:135], v[132:135]\n"
"s_sub_u32 s99, s99, s98\n"
"ds_read_b128 a[132:135], v3 offset:25600\n"
"v_lshlrev_b32 v20, 16, v22\n"
"v_mfma_f32_16x16x32_bf16 v[132:135], a[56:59], a[128:131], v[132:135]\n"
"s_sub_u32 s99, s99, s98\n"
"ds_read_b128 a[128:131], v3 offset:24576\n"
"v_and_b32 v21, s63, v22\n"
"v_mfma_f32_16x16x32_bf16 v[136:139], a[56:59], a[136:139], v[136:139]\n"
"s_sub_u32 s99, s99, s98\n"
"v_lshlrev_b32 v22, 16, v23\n"
"v_and_b32 v23, s63, v23\n"
"v_mfma_f32_16x16x32_bf16 v[136:139], a[60:63], a[140:143], v[136:139]\n"
"s_sub_u32 s99, s99, s98\n"
"v_add_f32 v172, v172, v20\n"
"v_add_f32 v173, v173, v21\n"
"v_mfma_f32_16x16x32_bf16 v[108:111], a[52:55], a[140:143], v[108:111]\n"
"s_sub_u32 s99, s99, s98\n"
"ds_read_b128 a[140:143], v3 offset:27648\n"
"v_add_f32 v174, v174, v22\n"
"v_mfma_f32_16x16x32_bf16 v[108:111], a[48:51], a[136:139], v[108:111]\n"
"s_sub_u32 s99, s99, s98\n"
"ds_read_b128 a[136:139], v3 offset:26624\n"
"v_add_f32 v175, v175, v23\n"
"v_mfma_f32_16x16x32_bf16 v[112:115], a[48:51], a[144:147], v[112:115]\n"
"s_sub_u32 s99, s99, s98\n"
"v_add_f32 v176, v176, v20\n"
"v_add_f32 v177, v177, v21\n"
"v_mfma_f32_16x16x32_bf16 v[112:115], a[52:55], a[148:151], v[112:115]\n"
"global_load_dwordx4 a[44:47], v0, s[34:35] offset:-1024 sc1\n"
"s_sub_u32 s99, s99, s98\n"
"v_mfma_f32_16x16x32_bf16 v[140:143], a[60:63], a[148:151], v[140:143]\n"
"s_sub_u32 s99, s99, s98\n"
"ds_read_b128 a[148:151], v3 offset:29696\n"
"v_add_f32 v178, v178, v22\n"
"v_mfma_f32_16x16x32_bf16 v[140:143], a[56:59], a[144:147], v[140:143]\n"
"s_sub_u32 s99, s99, s98\n"
"ds_read_b128 a[144:147], v3 offset:28672\n"
"v_add_f32 v179, v179, v23\n"
"v_mfma_f32_16x16x32_bf16 a[12:15], a[56:59], a[152:155], a[12:15]\n"
"s_memtime s[76:77]\n"
"v_add_f32 v180, v180, v20\n"
"v_add_f32 v181, v181, v21\n"
"v_mfma_f32_16x16x32_bf16 a[12:15], a[60:63], a[156:159], a[12:15]\n"
"v_add_f32 v182, v182, v22\n"
"v_add_f32 v183, v183, v23\n"
"v_mfma_f32_16x16x32_bf16 a[8:11], a[52:55], a[156:159], a[8:11]\n"
"ds_read_b128 a[156:159], v3 offset:31744\n"
"v_add_f32 v184, v184, v20\n"
"v_mfma_f32_16x16x32_bf16 a[8:11], a[48:51], a[152:155], a[8:11]\n"
"s_waitcnt vmcnt(9) lgkmcnt(11)\n"
"ds_read_b128 a[152:155], v3 offset:30720\n"
"v_add_f32 v185, v185, v21\n"
"v_mfma_f32_16x16x32_bf16 v[32:35], a[64:67], a[96:99], v[32:35]\n"
"v_add_f32 v186, v186, v22\n"
"v_add_f32 v187, v187, v23\n"
"v_mfma_f32_16x16x32_bf16 v[32:35], a[68:71], a[100:103], v[32:35]\n"
"global_load_dwordx4 a[48:51], v0, s[34:35] offset:0 sc1\n"
"v_add_f32 v188, v188, v20\n"
"v_mfma_f32_16x16x32_bf16 v[60:63], a[76:79], a[100:103], v[60:63]\n"
"v_add_f32 v189, v189, v21\n"
"v_mfma_f32_16x16x32_bf16 v[60:63], a[72:75], a[96:99], v[60:63]\n"
"v_add_f32 v190, v190, v22\n"
"v_add_f32 v191, v191, v23\n"
"v_mfma_f32_16x16x32_bf16 v[64:67], a[72:75], a[104:107], v[64:67]\n"
"v_add_f32 v192, v192, v20\n"
"v_mfma_f32_16x16x32_bf16 v[64:67], a[76:79], a[108:111], v[64:67]\n"
"v_add_f32 v193, v193, v21\n"
"v_mfma_f32_16x16x32_bf16 v[36:39], a[68:71], a[108:111], v[36:39]\n"
"v_add_f32 v194, v194, v22\n"
"v_add_f32 v195, v195, v23\n"
"v_mfma_f32_16x16x32_bf16 v[36:39], a[64:67], a[104:107], v[36:39]\n"
"s_waitcnt lgkmcnt(8)\n"
"v_add_f32 v196, v196, v20\n"
"v_mfma_f32_16x16x32_bf16 v[40:43], a[64:67], a[112:115], v[40:43]\n"
"v_add_f32 v197, v197, v21\n"
"v_mfma_f32_16x16x32_bf16 v[40:43], a[68:71], a[116:119], v[40:43]\n"
"global_load_dwordx4 a[52:55], v0, s[34:35] offset:1024 sc1\n"
"v_add_f32 v198, v198, v22\n"
"v_mfma_f32_16x16x32_bf16 v[68:71], a[76:79], a[116:119], v[68:71]\n"
"v_add_f32 v199, v199, v23\n"
"v_accvgpr_read_b32 v12, a20\n"
"v_mfma_f32_16x16x32_bf16 v[68:71], a[72:75], a[112:115], v[68:71]\n"
"v_accvgpr_read_b32 v13, a21\n"
"v_mfma_f32_16x16x32_bf16 v[72:75], a[72:75], a[120:123], v[72:75]\n"
"v_accvgpr_read_b32 v14, a22\n"
"v_accvgpr_read_b32 v15, a23\n"
"v_mfma_f32_16x16x32_bf16 v[72:75], a[76:79], a[124:127], v[72:75]\n"
"v_add_f32 v12, v12, v20\n"
"v_mfma_f32_16x16x32_bf16 v[44:47], a[68:71], a[124:127], v[44:47]\n"
"v_add_f32 v13, v13, v21\n"
"v_mfma_f32_16x16x32_bf16 v[44:47], a[64:67], a[120:123], v[44:47]\n"
"s_waitcnt lgkmcnt(4)\n"
"v_add_f32 v14, v14, v22\n"
"v_mfma_f32_16x16x32_bf16 v[48:51], a[64:67], a[128:131], v[48:51]\n"
"v_add_f32 v15, v15, v23\n"
"v_accvgpr_write_b32 a20, v12\n"
"v_mfma_f32_16x16x32_bf16 v[48:51], a[68:71], a[132:135], v[48:51]\n"
"global_load_dwordx4 a[56:59], v0, s[34:35] offset:2048 sc1\n"
"v_accvgpr_write_b32 a21, v13\n"
"v_mfma_f32_16x16x32_bf16 v[76:79], a[76:79], a[132:135], v[76:79]\n"
"s_waitcnt lgkmcnt(0)\n"
"v_accvgpr_write_b32 a22, v14\n"
"v_mfma_f32_16x16x32_bf16 v[76:79], a[72:75], a[128:131], v[76:79]\n"
"s_sub_u32 s29, s76, s29\n"
"v_accvgpr_write_b32 a23, v15\n"
"v_lshlrev_b32 v24, 16, v26\n"
"v_mfma_f32_16x16x32_bf16 v[80:83], a[72:75], a[136:139], v[80:83]\n"
"s_cmp_lt_u32 s29, 620\n"
"s_cselect_b32 s99, s99, 0\n"
"v_and_b32 v25, s63, v26\n"
"v_mfma_f32_16x16x32_bf16 v[80:83], a[76:79], a[140:143], v[80:83]\n"
"s_max_u32 s31, s31, s99\n"
"v_lshlrev_b32 v26, 16, v27\n"
"v_mfma_f32_16x16x32_bf16 v[52:55], a[68:71], a[140:143], v[52:55]\n"
"v_and_b32 v27, s63, v27\n"
"v_add_f32 v200, v200, v24\n"
"v_mfma_f32_16x16x32_bf16 v[52:55], a[64:67], a[136:139], v[52:55]\n"
"s_mov_b32 s99, 0\n"
"v_add_f32 v201, v201, v25\n"
"v_mfma_f32_16x16x32_bf16 v[56:59], a[64:67], a[144:147], v[56:59]\n"
"s_mov_b32 s98, 0\n"
"v_add_f32 v202, v202, v26\n"
"v_add_f32 v203, v203, v27\n"
"v_mfma_f32_16x16x32_bf16 v[56:59], a[68:71], a[148:151], v[56:59]\n"
"global_load_dwordx4 a[60:63], v0, s[34:35] offset:3072 sc1\n"
"s_memtime s[76:77]\n"
"s_load_dword s98, s[96:97], 256 glc\n"
"v_mfma_f32_16x16x32_bf16 v[84:87], a[76:79], a[148:151], v[84:87]\n"
"v_add_f32 v204, v204, v24\n"
"v_add_f32 v205, v205, v25\n"
"v_mfma_f32_16x16x32_bf16 v[84:87], a[72:75], a[144:147], v[84:87]\n"
"v_add_f32 v206, v206, v26\n"
"v_mfma_f32_16x16x32_bf16 a[4:7], a[72:75], a[152:155], a[4:7]\n"
"s_add_u32 s34, s34, 8192\n"
"v_add_f32 v207, v207, v27\n"
"v_mfma_f32_16x16x32_bf16 a[4:7], a[76:79], a[156:159], a[4:7]\n"
"v_add_f32 v208, v208, v24\n"
"v_add_f32 v209, v209, v25\n"
"v_mfma_f32_16x16x32_bf16 a[0:3], a[68:71], a[156:159], a[0:3]\n"
"v_add_f32 v210, v210, v26\n"
"v_mfma_f32_16x16x32_bf16 a[0:3], a[64:67], a[152:155], a[0:3]\n"
"s_waitcnt vmcnt(8)\n"
"v_add_f32 v211, v211, v27\n"
"v_mfma_f32_16x16x32_bf16 v[88:91], a[80:83], a[96:99], v[88:91]\n"
"v_add_f32 v212, v212, v24\n"
"v_add_f32 v213, v213, v25\n"
"v_mfma_f32_16x16x32_bf16 v[88:91], a[84:87], a[100:103], v[88:91]\n"
"global_load_dwordx4 a[64:67], v0, s[34:35] offset:-4096 sc1\n"
"v_add_f32 v214, v214, v26\n"
"v_mfma_f32_16x16x32_bf16 v[116:119], a[92:95], a[100:103], v[116:119]\n"
"s_mov_b32 s29, s76\n"
"ds_read_b128 a[100:103], v4 offset:17408\n"
"v_add_f32 v215, v215, v27\n"
"v_mfma_f32_16x16x32_bf16 v[116:119], a[88:91], a[96:99], v[116:119]\n"
"ds_read_b128 a[96:99], v4 offset:16384\n"
"v_add_f32 v216, v216, v24\n"
"v_mfma_f32_16x16x32_bf16 v[120:123], a[88:91], a[104:107], v[120:123]\n"
"s_sub_u32 s99, s99, s98\n"
"v_add_f32 v217, v217, v25\n"
"v_add_f32 v218, v218, v26\n"
"v_mfma_f32_16x16x32_bf16 v[120:123], a[92:95], a[108:111], v[120:123]\n"
"s_sub_u32 s99, s99, s98\n"
"v_add_f32 v219, v219, v27\n"
"v_add_f32 v220, v220, v24\n"
"v_mfma_f32_16x16x32_bf16 v[92:95], a[84:87], a[108:111], v[92:95]\n"
"s_sub_u32 s99, s99, s98\n"
"ds_read_b128 a[108:111], v4 offset:19456\n"
"v_add_f32 v221, v221, v25\n"
"v_mfma_f32_16x16x32_bf16 v[92:95], a[80:83], a[104:107], v[92:95]\n"
"s_sub_u32 s99, s99, s98\n"
"ds_read_b128 a[104:107], v4 offset:18432\n"
"v_add_f32 v222, v222, v26\n"
"v_mfma_f32_16x16x32_bf16 v[96:99], a[80:83], a[112:115], v[96:99]\n"
"s_sub_u32 s99, s99, s98\n"
"v_add_f32 v223, v223, v27\n"
"v_add_f32 v224, v224, v24\n"
"v_mfma_f32_16x16x32_bf16 v[96:99], a[84:87], a[116:119], v[96:99]\n"
"global_load_dwordx4 a[68:71], v0, s[34:35] offset:-3072 sc1\n"
"s_sub_u32 s99, s99, s98\n"
"v_mfma_f32_16x16x32_bf16 v[124:127], a[92:95], a[116:119], v[124:127]\n"
"s_sub_u32 s99, s99, s98\n"
"ds_read_b128 a[116:119], v4 offset:21504\n"
"v_add_f32 v225, v225, v25\n"
"v_mfma_f32_16x16x32_bf16 v[124:127], a[88:91], a[112:115], v[124:127]\n"
"s_sub_u32 s99, s99, s98\n"
"ds_read_b128 a[112:115], v4 offset:20480\n"
"v_add_f32 v226, v226, v26\n"
"v_mfma_f32_16x16x32_bf16 v[128:131], a[88:91], a[120:123], v[128:131]\n"
"s_sub_u32 s99, s99, s98\n"
"v_add_f32 v227, v227, v27\n"
"v_accvgpr_read_b32 v12, a24\n"
"v_mfma_f32_16x16x32_bf16 v[128:131], a[92:95], a[124:127], v[128:131]\n"
"s_sub_u32 s99, s99, s98\n"
"v_accvgpr_read_b32 v13, a25\n"
"v_accvgpr_read_b32 v14, a26\n"
"v_mfma_f32_16x16x32_bf16 v[100:103], a[84:87], a[124:127], v[100:103]\n"
"s_sub_u32 s99, s99, s98\n"
"ds_read_b128 a[124:127], v4 offset:23552\n"
"v_accvgpr_read_b32 v15, a27\n"
"v_mfma_f32_16x16x32_bf16 v[100:103], a[80:83], a[120:123], v[100:103]\n"
"s_sub_u32 s99, s99, s98\n"
"ds_read_b128 a[120:123], v4 offset:22528\n"
"v_add_f32 v12, v12, v24\n"
"v_mfma_f32_16x16x32_bf16 v[104:107], a[80:83], a[128:131], v[104:107]\n"
"s_sub_u32 s99, s99, s98\n"
"v_add_f32 v13, v13, v25\n"
"v_add_f32 v14, v14, v26\n"
"v_mfma_f32_16x16x32_bf16 v[104:107], a[84:87], a[132:135], v[104:107]\n"
"global_load_dwordx4 a[72:75], v0, s[34:35] offset:-2048 sc1\n"
"s_sub_u32 s99, s99, s98\n"
"v_mfma_f32_16x16x32_bf16 v[132:135], a[92:95], a[132:135], v[132:135]\n"
"s_sub_u32 s99, s99, s98\n"
"ds_read_b128 a[132:135], v4 offset:25600\n"
"v_add_f32 v15, v15, v27\n"
"v_mfma_f32_16x16x32_bf16 v[132:135], a[88:91], a[128:131], v[132:135]\n"
"s_sub_u32 s99, s99, s98\n"
"ds_read_b128 a[128:131], v4 offset:24576\n"
"v_accvgpr_write_b32 a24, v12\n"
"v_mfma_f32_16x16x32_bf16 v[136:139], a[88:91], a[136:139], v[136:139]\n"
"s_sub_u32 s99, s99, s98\n"
"v_accvgpr_write_b32 a25, v13\n"
"v_accvgpr_write_b32 a26, v14\n"
"v_mfma_f32_16x16x32_bf16 v[136:139], a[92:95], a[140:143], v[136:139]\n"
"s_sub_u32 s99, s99, s98\n"
"v_accvgpr_write_b32 a27, v15\n"
"v_lshlrev_b32 v28, 16, v30\n"
"v_mfma_f32_16x16x32_bf16 v[108:111], a[84:87], a[140:143], v[108:111]\n"
"s_sub_u32 s99, s99, s98\n"
"ds_read_b128 a[140:143], v4 offset:27648\n"
"v_and_b32 v29, s63, v30\n"
"v_mfma_f32_16x16x32_bf16 v[108:111], a[80:83], a[136:139], v[108:111]\n"
"s_sub_u32 s99, s99, s98\n"
"ds_read_b128 a[136:139], v4 offset:26624\n"
"v_lshlrev_b32 v30, 16, v31\n"
"v_mfma_f32_16x16x32_bf16 v[112:115], a[80:83], a[144:147], v[112:115]\n"
"s_sub_u32 s99, s99, s98\n"
"v_and_b32 v31, s63, v31\n"
"v_add_f32 v228, v228, v28\n"
"v_mfma_f32_16x16x32_bf16 v[112:115], a[84:87], a[148:151], v[112:115]\n"
"global_load_dwordx4 a[76:79], v0, s[34:35] offset:-1024 sc1\n"
"s_sub_u32 s99, s99, s98\n"
"v_mfma_f32_16x16x32_bf16 v[140:143], a[92:95], a[148:151], v[140:143]\n"
"s_sub_u32 s99, s99, s98\n"
"ds_read_b128 a[148:151], v4 offset:29696\n"
"v_add_f32 v229, v229, v29\n"
"v_mfma_f32_16x16x32_bf16 v[140:143], a[88:91], a[144:147], v[140:143]\n"
"s_sub_u32 s99, s99, s98\n"
"ds_read_b128 a[144:147], v4 offset:28672\n"
"v_add_f32 v230, v230, v30\n"
"v_mfma_f32_16x16x32_bf16 a[12:15], a[88:91], a[152:155], a[12:15]\n"
"s_memtime s[76:77]\n"
"v_add_f32 v231, v231, v31\n"
"v_add_f32 v232, v232, v28\n"
"v_mfma_f32_16x16x32_bf16 a[12:15], a[92:95], a[156:159], a[12:15]\n"
"v_add_f32 v233, v233, v29\n"
"v_add_f32 v234, v234, v30\n"
"v_mfma_f32_16x16x32_bf16 a[8:11], a[84:87], a[156:159], a[8:11]\n"
"ds_read_b128 a[156:159], v4 offset:31744\n"
"v_add_f32 v235, v235, v31\n"
"v_mfma_f32_16x16x32_bf16 a[8:11], a[80:83], a[152:155], a[8:11]\n"
"s_waitcnt vmcnt(8) lgkmcnt(11)\n"
"ds_read_b128 a[152:155], v4 offset:30720\n"
"v_add_f32 v236, v236, v28\n"
"v_mfma_f32_16x16x32_bf16 v[32:35], a[32:35], a[96:99], v[32:35]\n"
"v_add_f32 v237, v237, v29\n"
"v_add_f32 v238, v238, v30\n"
"v_mfma_f32_16x16x32_bf16 v[32:35], a[36:39], a[100:103], v[32:35]\n"
"global_load_dwordx4 a[80:83], v0, s[34:35] offset:0 sc1\n"
"v_add_f32 v239, v239, v31\n"
"v_mfma_f32_16x16x32_bf16 v[60:63], a[44:47], a[100:103], v[60:63]\n"
"v_add_f32 v240, v240, v28\n"
"v_add_f32 v241, v241, v29\n"
"v_mfma_f32_16x16x32_bf16 v[60:63], a[40:43], a[96:99], v[60:63]\n"
"v_add_f32 v242, v242, v30\n"
"v_mfma_f32_16x16x32_bf16 v[64:67], a[40:43], a[104:107], v[64:67]\n"
"v_add_f32 v243, v243, v31\n"
"v_add_f32 v244, v244, v28\n"
"v_mfma_f32_16x16x32_bf16 v[64:67], a[44:47], a[108:111], v[64:67]\n"
"L_st3848:\n"
"s_cbranch_execz L_retA\n"
"v_add_f32 v245, v245, v29\n"
"v_mfma_f32_16x16x32_bf16 v[36:39], a[36:39], a[108:111], v[36:39]\n"
"v_add_f32 v246, v246, v30\n"
"v_mfma_f32_16x16x32_bf16 v[36:39], a[32:35], a[104:107], v[36:39]\n"
"s_waitcnt lgkmcnt(8)\n"
"v_add_f32 v247, v247, v31\n"
"v_mfma_f32_16x16x32_bf16 v[40:43], a[32:35], a[112:115], v[40:43]\n"
"v_add_f32 v248, v248, v28\n"
"v_add_f32 v249, v249, v29\n"
"v_mfma_f32_16x16x32_bf16 v[40:43], a[36:39], a[116:119], v[40:43]\n"
"global_load_dwordx4 a[84:87], v0, s[34:35] offset:1024 sc1\n"
"s_add_u32 s48, s56, %12\n"
"s_addc_u32 s49, s57, 0\n"
"v_mfma_f32_16x16x32_bf16 v[68:71], a[44:47], a[116:119], v[68:71]\n"
"v_add_f32 v250, v250, v30\n"
"v_add_f32 v251, v251, v31\n"
"v_mfma_f32_16x16x32_bf16 v[68:71], a[40:43], a[112:115], v[68:71]\n"
"s_mov_b32 s50, 32768\n"
"v_add_f32 v252, v252, v28\n"
"v_add_f32 v253, v253, v29\n"
"v_mfma_f32_16x16x32_bf16 v[72:75], a[40:43], a[120:123], v[72:75]\n"
"v_add_f32 v254, v254, v30\n"
"v_mfma_f32_16x16x32_bf16 v[72:75], a[44:47], a[124:127], v[72:75]\n"
"s_mov_b32 s65, s78\n"
"v_add_f32 v255, v255, v31\n"
"v_mfma_f32_16x16x32_bf16 v[44:47], a[36:39], a[124:127], v[44:47]\n"
"v_accvgpr_read_b32 v12, a28\n"
"v_accvgpr_read_b32 v13, a29\n"
"v_mfma_f32_16x16x32_bf16 v[44:47], a[32:35], a[120:123], v[44:47]\n"
"s_mov_b64 s[40:41], s[52:53]\n"
"v_accvgpr_read_b32 v14, a30\n"
"s_waitcnt lgkmcnt(4)\n"
"v_mfma_f32_16x16x32_bf16 v[48:51], a[32:35], a[128:131], v[48:51]\n"
"s_mov_b32 s27, %21\n"
"v_accvgpr_read_b32 v15, a31\n"
"v_add_f32 v12, v12, v28\n"
"v_mfma_f32_16x16x32_bf16 v[48:51], a[36:39], a[132:135], v[48:51]\n"
"global_load_dwordx4 a[88:91], v0, s[34:35] offset:2048 sc1\n"
"s_mov_b32 s28, %23\n"
"v_mfma_f32_16x16x32_bf16 v[76:79], a[44:47], a[132:135], v[76:79]\n"
"s_waitcnt lgkmcnt(0)\n"
"v_add_f32 v13, v13, v29\n"
"v_add_f32 v14, v14, v30\n"
"v_mfma_f32_16x16x32_bf16 v[76:79], a[40:43], a[128:131], v[76:79]\n"
"s_sub_u32 s29, s76, s29\n"
"v_add_f32 v15, v15, v31\n"
"v_mfma_f32_16x16x32_bf16 v[80:83], a[40:43], a[136:139], v[80:83]\n"
"s_cmp_lt_u32 s29, 620\n"
"s_cselect_b32 s99, s99, 0\n"
"v_accvgpr_write_b32 a28, v12\n"
"v_mfma_f32_16x16x32_bf16 v[80:83], a[44:47], a[140:143], v[80:83]\n"
"s_max_u32 s30, s30, s99\n"
"v_accvgpr_write_b32 a29, v13\n"
"v_accvgpr_write_b32 a30, v14\n"
"v_mfma_f32_16x16x32_bf16 v[52:55], a[36:39], a[140:143], v[52:55]\n"
"s_cmp_eq_u32 s30, 0\n"
"s_cselect_b32 s30, 10, s30\n"
"v_accvgpr_write_b32 a31, v15\n"
"v_mfma_f32_16x16x32_bf16 v[52:55], a[32:35], a[136:139], v[52:55]\n"
"s_cmp_eq_u32 s31, 0\n"
"s_cselect_b32 s31, 10, s31\n"
"v_mfma_f32_16x16x32_bf16 v[56:59], a[32:35], a[144:147], v[56:59]\n"
"s_sub_u32 s69, s31, s30\n"
"v_mfma_f32_16x16x32_bf16 v[56:59], a[36:39], a[148:151], v[56:59]\n"
"global_load_dwordx4 a[92:95], v0, s[34:35] offset:3072 sc1\n"
"v_mov_b32 v20, s69\n"
"v_mfma_f32_16x16x32_bf16 v[84:87], a[44:47], a[148:151], v[84:87]\n"
"ds_write_b32 v2, v20 offset:8192\n"
"v_mfma_f32_16x16x32_bf16 v[84:87], a[40:43], a[144:147], v[84:87]\n"
"v_mfma_f32_16x16x32_bf16 a[4:7], a[40:43], a[152:155], a[4:7]\n"
"s_add_u32 s34, s34, 8192\n"
"v_mfma_f32_16x16x32_bf16 a[4:7], a[44:47], a[156:159], a[4:7]\n"
"v_mfma_f32_16x16x32_bf16 a[0:3], a[36:39], a[156:159], a[0:3]\n"
"v_mfma_f32_16x16x32_bf16 a[0:3], a[32:35], a[152:155], a[0:3]\n"
"s_waitcnt lgkmcnt(0)\n"
"s_barrier\n"
"s_waitcnt vmcnt(8)\n"
"v_mfma_f32_16x16x32_bf16 v[88:91], a[48:51], a[96:99], v[88:91]\n"
"ds_read_b32 v21, v3 offset:8192\n"
"v_mfma_f32_16x16x32_bf16 v[88:91], a[52:55], a[100:103], v[88:91]\n"
"global_load_dwordx4 a[32:35], v0, s[34:35] offset:-4096 sc1\n"
"v_mfma_f32_16x16x32_bf16 v[116:119], a[60:63], a[100:103], v[116:119]\n"
"ds_read_b128 a[100:103], v5 offset:17408\n"
"v_mfma_f32_16x16x32_bf16 v[116:119], a[56:59], a[96:99], v[116:119]\n"
"ds_read_b128 a[96:99], v5 offset:16384\n"
"v_mfma_f32_16x16x32_bf16 v[120:123], a[56:59], a[104:107], v[120:123]\n"
"ds_read_b32 v22, v4 offset:8192\n"
"v_mfma_f32_16x16x32_bf16 v[120:123], a[60:63], a[108:111], v[120:123]\n"
"ds_read_b32 v23, v5 offset:8192\n"
"v_mfma_f32_16x16x32_bf16 v[92:95], a[52:55], a[108:111], v[92:95]\n"
"ds_read_b128 a[108:111], v5 offset:19456\n"
"v_mfma_f32_16x16x32_bf16 v[92:95], a[48:51], a[104:107], v[92:95]\n"
"ds_read_b128 a[104:107], v5 offset:18432\n"
"v_mfma_f32_16x16x32_bf16 v[96:99], a[48:51], a[112:115], v[96:99]\n"
"v_mfma_f32_16x16x32_bf16 v[96:99], a[52:55], a[116:119], v[96:99]\n"
"global_load_dwordx4 a[36:39], v0, s[34:35] offset:-3072 sc1\n"
"v_mfma_f32_16x16x32_bf16 v[124:127], a[60:63], a[116:119], v[124:127]\n"
"s_waitcnt lgkmcnt(2)\n"
"ds_read_b128 a[116:119], v5 offset:21504\n"
"v_add_u32 v20, v20, v21\n"
"v_mfma_f32_16x16x32_bf16 v[124:127], a[56:59], a[112:115], v[124:127]\n"
"ds_read_b128 a[112:115], v5 offset:20480\n"
"v_mfma_f32_16x16x32_bf16 v[128:131], a[56:59], a[120:123], v[128:131]\n"
"v_add3_u32 v20, v20, v22, v23\n"
"v_mfma_f32_16x16x32_bf16 v[128:131], a[60:63], a[124:127], v[128:131]\n"
"v_mfma_f32_16x16x32_bf16 v[100:103], a[52:55], a[124:127], v[100:103]\n"
"ds_read_b128 a[124:127], v5 offset:23552\n"
"v_readfirstlane_b32 s69, v20\n"
"v_mfma_f32_16x16x32_bf16 v[100:103], a[48:51], a[120:123], v[100:103]\n"
"ds_read_b128 a[120:123], v5 offset:22528\n"
"v_mfma_f32_16x16x32_bf16 v[104:107], a[48:51], a[128:131], v[104:107]\n"
"v_mfma_f32_16x16x32_bf16 v[104:107], a[52:55], a[132:135], v[104:107]\n"
"s_cmp_le_i32 s69, -2\n"
"global_load_dwordx4 a[40:43], v0, s[34:35] offset:-2048 sc1\n"
"s_cselect_b32 s66, 2, 1\n"
"v_mfma_f32_16x16x32_bf16 v[132:135], a[60:63], a[132:135], v[132:135]\n"
"ds_read_b128 a[132:135], v5 offset:25600\n"
"v_mfma_f32_16x16x32_bf16 v[132:135], a[56:59], a[128:131], v[132:135]\n"
"s_cmp_ge_i32 s69, 2\n"
"ds_read_b128 a[128:131], v5 offset:24576\n"
"s_cselect_b32 s66, 3, s66\n"
"v_mfma_f32_16x16x32_bf16 v[136:139], a[56:59], a[136:139], v[136:139]\n"
"ds_read_b128 v[16:19], v7 offset:32768\n"
"v_mfma_f32_16x16x32_bf16 v[136:139], a[60:63], a[140:143], v[136:139]\n"
"s_lshl_b32 s73, s66, 13\n"
"s_xor_b32 s60, s79, s66\n"
"v_mfma_f32_16x16x32_bf16 v[108:111], a[52:55], a[140:143], v[108:111]\n"
"ds_read_b128 a[140:143], v5 offset:27648\n"
"v_mfma_f32_16x16x32_bf16 v[108:111], a[48:51], a[136:139], v[108:111]\n"
"s_cmp_eq_u32 s66, 3\n"
"ds_read_b128 a[136:139], v5 offset:26624\n"
"s_cselect_b32 s72, 8192, 0\n"
"v_mfma_f32_16x16x32_bf16 v[112:115], a[48:51], a[144:147], v[112:115]\n"
"s_abs_i32 s30, s69\n"
"v_mfma_f32_16x16x32_bf16 v[112:115], a[52:55], a[148:151], v[112:115]\n"
"global_load_dwordx4 a[44:47], v0, s[34:35] offset:-1024 sc1\n"
"s_add_u32 s52, s56, %11\n"
"s_addc_u32 s53, s57, 0\n"
"v_mfma_f32_16x16x32_bf16 v[140:143], a[60:63], a[148:151], v[140:143]\n"
"s_cmp_lt_u32 s30, 26\n"
"s_cselect_b32 s30, 2, 0\n"
"ds_read_b128 a[148:151], v5 offset:29696\n"
"v_mfma_f32_16x16x32_bf16 v[140:143], a[56:59], a[144:147], v[140:143]\n"
"s_add_u32 s52, s52, 4096\n"
"ds_read_b128 a[144:147], v5 offset:28672\n"
"s_addc_u32 s53, s53, 0\n"
"v_mfma_f32_16x16x32_bf16 a[12:15], a[56:59], a[152:155], a[12:15]\n"
"v_mfma_f32_16x16x32_bf16 a[12:15], a[60:63], a[156:159], a[12:15]\n"
"s_add_u32 s54, s52, s73\n"
"s_addc_u32 s55, s53, 0\n"
"v_mfma_f32_16x16x32_bf16 a[8:11], a[52:55], a[156:159], a[8:11]\n"
"ds_read_b128 a[156:159], v5 offset:31744\n"
"v_mfma_f32_16x16x32_bf16 a[8:11], a[48:51], a[152:155], a[8:11]\n"
"s_add_u32 s52, s52, s72\n"
"ds_read_b128 a[152:155], v5 offset:30720\n"
"s_addc_u32 s53, s53, 0\n"
"L_P6:\n"
"s_waitcnt vmcnt(8) lgkmcnt(13)\n"
"v_mfma_f32_16x16x32_bf16 v[32:35], a[64:67], a[96:99], v[32:35]\n"
"v_mfma_f32_16x16x32_bf16 v[32:35], a[68:71], a[100:103], v[32:35]\n"
"global_load_dwordx4 a[48:51], v0, s[34:35] offset:0 sc1\n"
"v_mfma_f32_16x16x32_bf16 v[60:63], a[76:79], a[100:103], v[60:63]\n"
"v_mfma_f32_16x16x32_bf16 v[60:63], a[72:75], a[96:99], v[60:63]\n"
"v_mfma_f32_16x16x32_bf16 v[64:67], a[72:75], a[104:107], v[64:67]\n"
"v_mfma_f32_16x16x32_bf16 v[64:67], a[76:79], a[108:111], v[64:67]\n"
"v_mfma_f32_16x16x32_bf16 v[36:39], a[68:71], a[108:111], v[36:39]\n"
"s_waitcnt lgkmcnt(6)\n"
"v_lshlrev_b32 v12, 16, v16\n"
"v_mfma_f32_16x16x32_bf16 v[36:39], a[64:67], a[104:107], v[36:39]\n"
"v_and_b32 v13, s63, v16\n"
"v_lshlrev_b32 v14, 16, v17\n"
"v_mfma_f32_16x16x32_bf16 v[40:43], a[64:67], a[112:115], v[40:43]\n"
"v_and_b32 v15, s63, v17\n"
"v_mfma_f32_16x16x32_bf16 v[40:43], a[68:71], a[116:119], v[40:43]\n"
"global_load_dwordx4 a[52:55], v0, s[34:35] offset:1024 sc1\n"
"ds_read_b128 v[20:23], v7 offset:34816\n"
"v_mfma_f32_16x16x32_bf16 v[68:71], a[76:79], a[116:119], v[68:71]\n"
"v_mul_f32 v12, v12, v32\n"
"v_mul_f32 v13, v13, v33\n"
"v_mfma_f32_16x16x32_bf16 v[68:71], a[72:75], a[112:115], v[68:71]\n"
"v_mul_f32 v14, v14, v34\n"
"v_mfma_f32_16x16x32_bf16 v[72:75], a[72:75], a[120:123], v[72:75]\n"
"v_mul_f32 v15, v15, v35\n"
"v_mfma_f32_16x16x32_bf16 v[72:75], a[76:79], a[124:127], v[72:75]\n"
"v_cvt_pk_bf16_f32 v24, v12, v13\n"
"v_cvt_pk_bf16_f32 v25, v14, v15\n"
"v_mfma_f32_16x16x32_bf16 v[44:47], a[68:71], a[124:127], v[44:47]\n"
"ds_write_b64 v2, v[24:25] offset:0\n"
"v_mfma_f32_16x16x32_bf16 v[44:47], a[64:67], a[120:123], v[44:47]\n"
"s_waitcnt lgkmcnt(6)\n"
"v_lshlrev_b32 v12, 16, v18\n"
"v_mfma_f32_16x16x32_bf16 v[48:51], a[64:67], a[128:131], v[48:51]\n"
"v_and_b32 v13, s63, v18\n"
"v_lshlrev_b32 v14, 16, v19\n"
"v_mfma_f32_16x16x32_bf16 v[48:51], a[68:71], a[132:135], v[48:51]\n"
"global_load_dwordx4 a[56:59], v0, s[34:35] offset:2048 sc1\n"
"v_and_b32 v15, s63, v19\n"
"v_mfma_f32_16x16x32_bf16 v[76:79], a[76:79], a[132:135], v[76:79]\n"
"v_mul_f32 v12, v12, v60\n"
"v_mfma_f32_16x16x32_bf16 v[76:79], a[72:75], a[128:131], v[76:79]\n"
"v_mul_f32 v13, v13, v61\n"
"v_mul_f32 v14, v14, v62\n"
"v_mfma_f32_16x16x32_bf16 v[80:83], a[72:75], a[136:139], v[80:83]\n"
"v_mul_f32 v15, v15, v63\n"
"v_mfma_f32_16x16x32_bf16 v[80:83], a[76:79], a[140:143], v[80:83]\n"
"v_cvt_pk_bf16_f32 v26, v12, v13\n"
"v_cvt_pk_bf16_f32 v27, v14, v15\n"
"v_mfma_f32_16x16x32_bf16 v[52:55], a[68:71], a[140:143], v[52:55]\n"
"ds_write_b64 v2, v[26:27] offset:8\n"
"v_mfma_f32_16x16x32_bf16 v[52:55], a[64:67], a[136:139], v[52:55]\n"
"s_waitcnt lgkmcnt(2)\n"
"v_lshlrev_b32 v12, 16, v20\n"
"v_mfma_f32_16x16x32_bf16 v[56:59], a[64:67], a[144:147], v[56:59]\n"
"v_and_b32 v13, s63, v20\n"
"v_mfma_f32_16x16x32_bf16 v[56:59], a[68:71], a[148:151], v[56:59]\n"
"global_load_dwordx4 a[60:63], v0, s[34:35] offset:3072 sc1\n"
"v_lshlrev_b32 v14, 16, v21\n"
"v_mfma_f32_16x16x32_bf16 v[84:87], a[76:79], a[148:151], v[84:87]\n"
"v_and_b32 v15, s63, v21\n"
"v_xor_b32 v16, 64, v7\n"
"v_mfma_f32_16x16x32_bf16 v[84:87], a[72:75], a[144:147], v[84:87]\n"
"ds_read_b128 v[16:19], v16 offset:32768\n"
"v_mfma_f32_16x16x32_bf16 a[4:7], a[72:75], a[152:155], a[4:7]\n"
"s_add_u32 s34, s34, 8192\n"
"v_mul_f32 v12, v12, v36\n"
"v_mul_f32 v13, v13, v37\n"
"v_mfma_f32_16x16x32_bf16 a[4:7], a[76:79], a[156:159], a[4:7]\n"
"v_mul_f32 v14, v14, v38\n"
"v_mfma_f32_16x16x32_bf16 a[0:3], a[68:71], a[156:159], a[0:3]\n"
"v_mul_f32 v15, v15, v39\n"
"v_mfma_f32_16x16x32_bf16 a[0:3], a[64:67], a[152:155], a[0:3]\n"
"s_waitcnt vmcnt(8)\n"
"v_cvt_pk_bf16_f32 v28, v12, v13\n"
"v_cvt_pk_bf16_f32 v29, v14, v15\n"
"v_mfma_f32_16x16x32_bf16 v[88:91], a[80:83], a[96:99], v[88:91]\n"
"v_lshlrev_b32 v12, 16, v22\n"
"v_mfma_f32_16x16x32_bf16 v[88:91], a[84:87], a[100:103], v[88:91]\n"
"global_load_dwordx4 a[64:67], v0, s[34:35] offset:-4096 sc1\n"
"v_and_b32 v13, s63, v22\n"
"v_mfma_f32_16x16x32_bf16 v[116:119], a[92:95], a[100:103], v[116:119]\n"
"v_lshlrev_b32 v14, 16, v23\n"
"v_and_b32 v15, s63, v23\n"
"v_mfma_f32_16x16x32_bf16 v[116:119], a[88:91], a[96:99], v[116:119]\n"
"v_mul_f32 v12, v12, v64\n"
"v_mfma_f32_16x16x32_bf16 v[120:123], a[88:91], a[104:107], v[120:123]\n"
"v_mul_f32 v13, v13, v65\n"
"v_mfma_f32_16x16x32_bf16 v[120:123], a[92:95], a[108:111], v[120:123]\n"
"v_mul_f32 v14, v14, v66\n"
"v_mul_f32 v15, v15, v67\n"
"v_mfma_f32_16x16x32_bf16 v[92:95], a[84:87], a[108:111], v[92:95]\n"
"v_cvt_pk_bf16_f32 v30, v12, v13\n"
"v_mfma_f32_16x16x32_bf16 v[92:95], a[80:83], a[104:107], v[92:95]\n"
"v_cvt_pk_bf16_f32 v31, v14, v15\n"
"v_mfma_f32_16x16x32_bf16 v[96:99], a[80:83], a[112:115], v[96:99]\n"
"global_store_dwordx4 v0, v[28:31], s[52:53] offset:-2048 sc1\n"
"ds_write_b128 v2, v[28:31] offset:2048\n"
"v_mfma_f32_16x16x32_bf16 v[96:99], a[84:87], a[116:119], v[96:99]\n"
"s_waitcnt lgkmcnt(1)\n"
"global_load_dwordx4 a[68:71], v0, s[34:35] offset:-3072 sc1\n"
"v_lshlrev_b32 v12, 16, v16\n"
"v_mfma_f32_16x16x32_bf16 v[124:127], a[92:95], a[116:119], v[124:127]\n"
"v_and_b32 v13, s63, v16\n"
"v_mfma_f32_16x16x32_bf16 v[124:127], a[88:91], a[112:115], v[124:127]\n"
"v_lshlrev_b32 v14, 16, v17\n"
"v_and_b32 v15, s63, v17\n"
"v_mfma_f32_16x16x32_bf16 v[128:131], a[88:91], a[120:123], v[128:131]\n"
"v_xor_b32 v20, 64, v7\n"
"v_mfma_f32_16x16x32_bf16 v[128:131], a[92:95], a[124:127], v[128:131]\n"
"ds_read_b128 v[20:23], v20 offset:34816\n"
"v_mfma_f32_16x16x32_bf16 v[100:103], a[84:87], a[124:127], v[100:103]\n"
"v_mul_f32 v12, v12, v88\n"
"v_mul_f32 v13, v13, v89\n"
"v_mfma_f32_16x16x32_bf16 v[100:103], a[80:83], a[120:123], v[100:103]\n"
"v_mul_f32 v14, v14, v90\n"
"v_mfma_f32_16x16x32_bf16 v[104:107], a[80:83], a[128:131], v[104:107]\n"
"v_mul_f32 v15, v15, v91\n"
"v_mfma_f32_16x16x32_bf16 v[104:107], a[84:87], a[132:135], v[104:107]\n"
"global_load_dwordx4 a[72:75], v0, s[34:35] offset:-2048 sc1\n"
"v_cvt_pk_bf16_f32 v24, v12, v13\n"
"v_mfma_f32_16x16x32_bf16 v[132:135], a[92:95], a[132:135], v[132:135]\n"
"v_cvt_pk_bf16_f32 v25, v14, v15\n"
"v_lshlrev_b32 v12, 16, v18\n"
"v_mfma_f32_16x16x32_bf16 v[132:135], a[88:91], a[128:131], v[132:135]\n"
"v_and_b32 v13, s63, v18\n"
"v_mfma_f32_16x16x32_bf16 v[136:139], a[88:91], a[136:139], v[136:139]\n"
"v_lshlrev_b32 v14, 16, v19\n"
"v_and_b32 v15, s63, v19\n"
"v_mfma_f32_16x16x32_bf16 v[136:139], a[92:95], a[140:143], v[136:139]\n"
"v_mul_f32 v12, v12, v116\n"
"v_mfma_f32_16x16x32_bf16 v[108:111], a[84:87], a[140:143], v[108:111]\n"
"v_mul_f32 v13, v13, v117\n"
"v_mfma_f32_16x16x32_bf16 v[108:111], a[80:83], a[136:139], v[108:111]\n"
"v_mul_f32 v14, v14, v118\n"
"v_mul_f32 v15, v15, v119\n"
"v_mfma_f32_16x16x32_bf16 v[112:115], a[80:83], a[144:147], v[112:115]\n"
"v_cvt_pk_bf16_f32 v26, v12, v13\n"
"v_mfma_f32_16x16x32_bf16 v[112:115], a[84:87], a[148:151], v[112:115]\n"
"global_load_dwordx4 a[76:79], v0, s[34:35] offset:-1024 sc1\n"
"v_cvt_pk_bf16_f32 v27, v14, v15\n"
"v_mfma_f32_16x16x32_bf16 v[140:143], a[92:95], a[148:151], v[140:143]\n"
"global_store_dwordx4 v0, v[24:27], s[52:53] offset:-3072 sc1\n"
"ds_write_b128 v2, v[24:27] offset:1024\n"
"v_mfma_f32_16x16x32_bf16 v[140:143], a[88:91], a[144:147], v[140:143]\n"
"s_waitcnt lgkmcnt(1)\n"
"v_lshlrev_b32 v12, 16, v20\n"
"v_mfma_f32_16x16x32_bf16 a[12:15], a[88:91], a[152:155], a[12:15]\n"
"v_and_b32 v13, s63, v20\n"
"v_mfma_f32_16x16x32_bf16 a[12:15], a[92:95], a[156:159], a[12:15]\n"
"v_lshlrev_b32 v14, 16, v21\n"
"v_mfma_f32_16x16x32_bf16 a[8:11], a[84:87], a[156:159], a[8:11]\n"
"ds_read_b128 v[16:19], v7 offset:36864\n"
"v_and_b32 v15, s63, v21\n"
"v_mfma_f32_16x16x32_bf16 a[8:11], a[80:83], a[152:155], a[8:11]\n"
"s_waitcnt vmcnt(10)\n"
"v_mul_f32 v12, v12, v92\n"
"v_mfma_f32_16x16x32_bf16 v[144:147], a[32:35], a[96:99], v[144:147]\n"
"v_mul_f32 v13, v13, v93\n"
"v_mul_f32 v14, v14, v94\n"
"v_mfma_f32_16x16x32_bf16 v[144:147], a[36:39], a[100:103], v[144:147]\n"
"global_load_dwordx4 a[80:83], v0, s[34:35] offset:0 sc1\n"
"v_mul_f32 v15, v15, v95\n"
"v_mfma_f32_16x16x32_bf16 v[172:175], a[44:47], a[100:103], v[172:175]\n"
"v_cvt_pk_bf16_f32 v28, v12, v13\n"
"v_mfma_f32_16x16x32_bf16 v[172:175], a[40:43], a[96:99], v[172:175]\n"
"v_cvt_pk_bf16_f32 v29, v14, v15\n"
"v_mfma_f32_16x16x32_bf16 v[176:179], a[40:43], a[104:107], v[176:179]\n"
"ds_write_b64 v2, v[28:29] offset:3072\n"
"v_mfma_f32_16x16x32_bf16 v[176:179], a[44:47], a[108:111], v[176:179]\n"
"v_lshlrev_b32 v12, 16, v22\n"
"v_and_b32 v13, s63, v22\n"
"v_mfma_f32_16x16x32_bf16 v[148:151], a[36:39], a[108:111], v[148:151]\n"
"v_lshlrev_b32 v14, 16, v23\n"
"v_mfma_f32_16x16x32_bf16 v[148:151], a[32:35], a[104:107], v[148:151]\n"
"v_and_b32 v15, s63, v23\n"
"v_mul_f32 v12, v12, v120\n"
"v_mfma_f32_16x16x32_bf16 v[152:155], a[32:35], a[112:115], v[152:155]\n"
"v_mul_f32 v13, v13, v121\n"
"v_mfma_f32_16x16x32_bf16 v[152:155], a[36:39], a[116:119], v[152:155]\n"
"global_load_dwordx4 a[84:87], v0, s[34:35] offset:1024 sc1\n"
"v_mul_f32 v14, v14, v122\n"
"v_mfma_f32_16x16x32_bf16 v[180:183], a[44:47], a[116:119], v[180:183]\n"
"v_mul_f32 v15, v15, v123\n"
"v_cvt_pk_bf16_f32 v30, v12, v13\n"
"v_mfma_f32_16x16x32_bf16 v[180:183], a[40:43], a[112:115], v[180:183]\n"
"v_cvt_pk_bf16_f32 v31, v14, v15\n"
"v_mfma_f32_16x16x32_bf16 v[184:187], a[40:43], a[120:123], v[184:187]\n"
"ds_write_b64 v2, v[30:31] offset:3080\n"
"v_mfma_f32_16x16x32_bf16 v[184:187], a[44:47], a[124:127], v[184:187]\n"
"s_waitcnt lgkmcnt(2)\n"
"v_lshlrev_b32 v12, 16, v16\n"
"v_and_b32 v13, s63, v16\n"
"v_mfma_f32_16x16x32_bf16 v[156:159], a[36:39], a[124:127], v[156:159]\n"
"v_lshlrev_b32 v14, 16, v17\n"
"v_mfma_f32_16x16x32_bf16 v[156:159], a[32:35], a[120:123], v[156:159]\n"
"v_and_b32 v15, s63, v17\n"
"v_xor_b32 v20, 64, v7\n"
"v_mfma_f32_16x16x32_bf16 v[160:163], a[32:35], a[128:131], v[160:163]\n"
"ds_read_b128 v[20:23], v20 offset:36864\n"
"v_mfma_f32_16x16x32_bf16 v[160:163], a[36:39], a[132:135], v[160:163]\n"
"global_load_dwordx4 a[88:91], v0, s[34:35] offset:2048 sc1\n"
"v_mul_f32 v12, v12, v40\n"
"v_mfma_f32_16x16x32_bf16 v[188:191], a[44:47], a[132:135], v[188:191]\n"
"v_mul_f32 v13, v13, v41\n"
"v_mfma_f32_16x16x32_bf16 v[188:191], a[40:43], a[128:131], v[188:191]\n"
"v_mul_f32 v14, v14, v42\n"
"v_mul_f32 v15, v15, v43\n"
"v_mfma_f32_16x16x32_bf16 v[192:195], a[40:43], a[136:139], v[192:195]\n"
"v_cvt_pk_bf16_f32 v24, v12, v13\n"
"v_mfma_f32_16x16x32_bf16 v[192:195], a[44:47], a[140:143], v[192:195]\n"
"v_cvt_pk_bf16_f32 v25, v14, v15\n"
"v_mfma_f32_16x16x32_bf16 v[164:167], a[36:39], a[140:143], v[164:167]\n"
"v_lshlrev_b32 v12, 16, v18\n"
"v_and_b32 v13, s63, v18\n"
"v_mfma_f32_16x16x32_bf16 v[164:167], a[32:35], a[136:139], v[164:167]\n"
"v_lshlrev_b32 v14, 16, v19\n"
"v_mfma_f32_16x16x32_bf16 v[168:171], a[32:35], a[144:147], v[168:171]\n"
"v_and_b32 v15, s63, v19\n"
"v_mul_f32 v12, v12, v68\n"
"v_mfma_f32_16x16x32_bf16 v[168:171], a[36:39], a[148:151], v[168:171]\n"
"global_load_dwordx4 a[92:95], v0, s[34:35] offset:3072 sc1\n"
"v_mul_f32 v13, v13, v69\n"
"v_mfma_f32_16x16x32_bf16 v[196:199], a[44:47], a[148:151], v[196:199]\n"
"v_mul_f32 v14, v14, v70\n"
"v_mfma_f32_16x16x32_bf16 v[196:199], a[40:43], a[144:147], v[196:199]\n"
"v_mul_f32 v15, v15, v71\n"
"v_mfma_f32_16x16x32_bf16 a[20:23], a[40:43], a[152:155], a[20:23]\n"
"s_add_u32 s34, s34, 8192\n"
"v_cvt_pk_bf16_f32 v26, v12, v13\n"
"v_cvt_pk_bf16_f32 v27, v14, v15\n"
"v_mfma_f32_16x16x32_bf16 a[20:23], a[44:47], a[156:159], a[20:23]\n"
"global_store_dwordx4 v0, v[24:27], s[52:53] offset:0 sc1\n"
"ds_write_b128 v2, v[24:27] offset:4096\n"
"v_mfma_f32_16x16x32_bf16 a[16:19], a[36:39], a[156:159], a[16:19]\n"
"s_waitcnt lgkmcnt(1)\n"
"v_lshlrev_b32 v12, 16, v20\n"
"v_mfma_f32_16x16x32_bf16 a[16:19], a[32:35], a[152:155], a[16:19]\n"
"s_waitcnt vmcnt(11)\n"
"v_and_b32 v13, s63, v20\n"
"v_mfma_f32_16x16x32_bf16 v[200:203], a[48:51], a[96:99], v[200:203]\n"
"v_lshlrev_b32 v14, 16, v21\n"
"v_and_b32 v15, s63, v21\n"
"v_mfma_f32_16x16x32_bf16 v[200:203], a[52:55], a[100:103], v[200:203]\n"
"global_load_dwordx4 a[32:35], v0, s[34:35] offset:-4096 sc1\n"
"ds_read_b128 v[16:19], v7 offset:38912\n"
"v_mfma_f32_16x16x32_bf16 v[228:231], a[60:63], a[100:103], v[228:231]\n"
"ds_read_b128 a[100:103], v4 offset:17408\n"
"v_mul_f32 v12, v12, v96\n"
"v_mfma_f32_16x16x32_bf16 v[228:231], a[56:59], a[96:99], v[228:231]\n"
"ds_read_b128 a[96:99], v4 offset:16384\n"
"v_mul_f32 v13, v13, v97\n"
"v_mfma_f32_16x16x32_bf16 v[232:235], a[56:59], a[104:107], v[232:235]\n"
"v_mul_f32 v14, v14, v98\n"
"v_mfma_f32_16x16x32_bf16 v[232:235], a[60:63], a[108:111], v[232:235]\n"
"v_mul_f32 v15, v15, v99\n"
"v_cvt_pk_bf16_f32 v28, v12, v13\n"
"v_mfma_f32_16x16x32_bf16 v[204:207], a[52:55], a[108:111], v[204:207]\n"
"ds_read_b128 a[108:111], v4 offset:19456\n"
"v_cvt_pk_bf16_f32 v29, v14, v15\n"
"v_mfma_f32_16x16x32_bf16 v[204:207], a[48:51], a[104:107], v[204:207]\n"
"ds_read_b128 a[104:107], v4 offset:18432\n"
"v_lshlrev_b32 v12, 16, v22\n"
"v_mfma_f32_16x16x32_bf16 v[208:211], a[48:51], a[112:115], v[208:211]\n"
"v_and_b32 v13, s63, v22\n"
"v_lshlrev_b32 v14, 16, v23\n"
"v_mfma_f32_16x16x32_bf16 v[208:211], a[52:55], a[116:119], v[208:211]\n"
"global_load_dwordx4 a[36:39], v0, s[34:35] offset:-3072 sc1\n"
"v_and_b32 v15, s63, v23\n"
"v_mfma_f32_16x16x32_bf16 v[236:239], a[60:63], a[116:119], v[236:239]\n"
"ds_read_b128 a[116:119], v4 offset:21504\n"
"v_mul_f32 v12, v12, v124\n"
"v_mfma_f32_16x16x32_bf16 v[236:239], a[56:59], a[112:115], v[236:239]\n"
"ds_read_b128 a[112:115], v4 offset:20480\n"
"v_mul_f32 v13, v13, v125\n"
"v_mfma_f32_16x16x32_bf16 v[240:243], a[56:59], a[120:123], v[240:243]\n"
"v_mul_f32 v14, v14, v126\n"
"v_mul_f32 v15, v15, v127\n"
"v_mfma_f32_16x16x32_bf16 v[240:243], a[60:63], a[124:127], v[240:243]\n"
"v_cvt_pk_bf16_f32 v30, v12, v13\n"
"v_mfma_f32_16x16x32_bf16 v[212:215], a[52:55], a[124:127], v[212:215]\n"
"ds_read_b128 a[124:127], v4 offset:23552\n"
"v_cvt_pk_bf16_f32 v31, v14, v15\n"
"v_mfma_f32_16x16x32_bf16 v[212:215], a[48:51], a[120:123], v[212:215]\n"
"ds_read_b128 a[120:123], v4 offset:22528\n"
"v_mfma_f32_16x16x32_bf16 v[216:219], a[48:51], a[128:131], v[216:219]\n"
"global_store_dwordx4 v0, v[28:31], s[52:53] offset:1024 sc1\n"
"ds_write_b128 v2, v[28:31] offset:5120\n"
"v_mfma_f32_16x16x32_bf16 v[216:219], a[52:55], a[132:135], v[216:219]\n"
"s_waitcnt lgkmcnt(9)\n"
"global_load_dwordx4 a[40:43], v0, s[34:35] offset:-2048 sc1\n"
"v_lshlrev_b32 v12, 16, v16\n"
"v_mfma_f32_16x16x32_bf16 v[244:247], a[60:63], a[132:135], v[244:247]\n"
"ds_read_b128 a[132:135], v4 offset:25600\n"
"v_and_b32 v13, s63, v16\n"
"v_mfma_f32_16x16x32_bf16 v[244:247], a[56:59], a[128:131], v[244:247]\n"
"ds_read_b128 a[128:131], v4 offset:24576\n"
"v_lshlrev_b32 v14, 16, v17\n"
"v_mfma_f32_16x16x32_bf16 v[248:251], a[56:59], a[136:139], v[248:251]\n"
"v_and_b32 v15, s63, v17\n"
"v_xor_b32 v20, 64, v7\n"
"v_mfma_f32_16x16x32_bf16 v[248:251], a[60:63], a[140:143], v[248:251]\n"
"ds_read_b128 v[20:23], v20 offset:38912\n"
"v_mul_f32 v12, v12, v44\n"
"v_mfma_f32_16x16x32_bf16 v[220:223], a[52:55], a[140:143], v[220:223]\n"
"ds_read_b128 a[140:143], v4 offset:27648\n"
"v_mul_f32 v13, v13, v45\n"
"v_mfma_f32_16x16x32_bf16 v[220:223], a[48:51], a[136:139], v[220:223]\n"
"ds_read_b128 a[136:139], v4 offset:26624\n"
"v_mul_f32 v14, v14, v46\n"
"v_mfma_f32_16x16x32_bf16 v[224:227], a[48:51], a[144:147], v[224:227]\n"
"v_mul_f32 v15, v15, v47\n"
"v_cvt_pk_bf16_f32 v24, v12, v13\n"
"v_mfma_f32_16x16x32_bf16 v[224:227], a[52:55], a[148:151], v[224:227]\n"
"global_load_dwordx4 a[44:47], v0, s[34:35] offset:-1024 sc1\n"
"v_cvt_pk_bf16_f32 v25, v14, v15\n"
"v_mfma_f32_16x16x32_bf16 v[252:255], a[60:63], a[148:151], v[252:255]\n"
"ds_read_b128 a[148:151], v4 offset:29696\n"
"v_mfma_f32_16x16x32_bf16 v[252:255], a[56:59], a[144:147], v[252:255]\n"
"ds_read_b128 a[144:147], v4 offset:28672\n"
"v_mfma_f32_16x16x32_bf16 a[28:31], a[56:59], a[152:155], a[28:31]\n"
"ds_write_b64 v2, v[24:25] offset:6144\n"
"v_mfma_f32_16x16x32_bf16 a[28:31], a[60:63], a[156:159], a[28:31]\n"
"v_lshlrev_b32 v12, 16, v18\n"
"v_and_b32 v13, s63, v18\n"
"v_mfma_f32_16x16x32_bf16 a[24:27], a[52:55], a[156:159], a[24:27]\n"
"ds_read_b128 a[156:159], v4 offset:31744\n"
"v_lshlrev_b32 v14, 16, v19\n"
"v_mfma_f32_16x16x32_bf16 a[24:27], a[48:51], a[152:155], a[24:27]\n"
"s_waitcnt vmcnt(11) lgkmcnt(14)\n"
"ds_read_b128 a[152:155], v4 offset:30720\n"
"v_and_b32 v15, s63, v19\n"
"v_mfma_f32_16x16x32_bf16 v[144:147], a[64:67], a[96:99], v[144:147]\n"
"v_mul_f32 v12, v12, v72\n"
"v_mul_f32 v13, v13, v73\n"
"v_mfma_f32_16x16x32_bf16 v[144:147], a[68:71], a[100:103], v[144:147]\n"
"global_load_dwordx4 a[48:51], v0, s[34:35] offset:0 sc1\n"
"v_mul_f32 v14, v14, v74\n"
"v_mfma_f32_16x16x32_bf16 v[172:175], a[76:79], a[100:103], v[172:175]\n"
"v_mul_f32 v15, v15, v75\n"
"v_cvt_pk_bf16_f32 v26, v12, v13\n"
"v_mfma_f32_16x16x32_bf16 v[172:175], a[72:75], a[96:99], v[172:175]\n"
"v_cvt_pk_bf16_f32 v27, v14, v15\n"
"v_mfma_f32_16x16x32_bf16 v[176:179], a[72:75], a[104:107], v[176:179]\n"
"ds_write_b64 v2, v[26:27] offset:6152\n"
"v_mfma_f32_16x16x32_bf16 v[176:179], a[76:79], a[108:111], v[176:179]\n"
"s_waitcnt lgkmcnt(8)\n"
"v_lshlrev_b32 v12, 16, v20\n"
"v_and_b32 v13, s63, v20\n"
"v_mfma_f32_16x16x32_bf16 v[148:151], a[68:71], a[108:111], v[148:151]\n"
"v_lshlrev_b32 v14, 16, v21\n"
"v_and_b32 v15, s63, v21\n"
"v_mfma_f32_16x16x32_bf16 v[148:151], a[64:67], a[104:107], v[148:151]\n"
"v_mul_f32 v12, v12, v100\n"
"v_mul_f32 v13, v13, v101\n"
"v_mfma_f32_16x16x32_bf16 v[152:155], a[64:67], a[112:115], v[152:155]\n"
"v_mul_f32 v14, v14, v102\n"
"v_mul_f32 v15, v15, v103\n"
"v_mfma_f32_16x16x32_bf16 v[152:155], a[68:71], a[116:119], v[152:155]\n"
"global_load_dwordx4 a[52:55], v0, s[34:35] offset:1024 sc1\n"
"v_cvt_pk_bf16_f32 v28, v12, v13\n"
"v_mfma_f32_16x16x32_bf16 v[180:183], a[76:79], a[116:119], v[180:183]\n"
"v_cvt_pk_bf16_f32 v29, v14, v15\n"
"v_lshlrev_b32 v12, 16, v22\n"
"v_mfma_f32_16x16x32_bf16 v[180:183], a[72:75], a[112:115], v[180:183]\n"
"v_and_b32 v13, s63, v22\n"
"v_lshlrev_b32 v14, 16, v23\n"
"v_mfma_f32_16x16x32_bf16 v[184:187], a[72:75], a[120:123], v[184:187]\n"
"v_and_b32 v15, s63, v23\n"
"v_mul_f32 v12, v12, v128\n"
"v_mfma_f32_16x16x32_bf16 v[184:187], a[76:79], a[124:127], v[184:187]\n"
"v_mul_f32 v13, v13, v129\n"
"v_mfma_f32_16x16x32_bf16 v[156:159], a[68:71], a[124:127], v[156:159]\n"
"v_mul_f32 v14, v14, v130\n"
"v_mfma_f32_16x16x32_bf16 v[156:159], a[64:67], a[120:123], v[156:159]\n"
"s_waitcnt lgkmcnt(6)\n"
"v_mul_f32 v15, v15, v131\n"
"v_cvt_pk_bf16_f32 v30, v12, v13\n"
"v_mfma_f32_16x16x32_bf16 v[160:163], a[64:67], a[128:131], v[160:163]\n"
"v_cvt_pk_bf16_f32 v31, v14, v15\n"
"v_mfma_f32_16x16x32_bf16 v[160:163], a[68:71], a[132:135], v[160:163]\n"
"global_load_dwordx4 a[56:59], v0, s[34:35] offset:2048 sc1\n"
"v_mfma_f32_16x16x32_bf16 v[188:191], a[76:79], a[132:135], v[188:191]\n"
"global_store_dwordx4 v0, v[28:31], s[52:53] offset:3072 sc1\n"
"ds_write_b128 v2, v[28:31] offset:7168\n"
"v_mfma_f32_16x16x32_bf16 v[188:191], a[72:75], a[128:131], v[188:191]\n"
"v_accvgpr_read_b32 v16, a160\n"
"v_accvgpr_read_b32 v17, a161\n"
"v_mfma_f32_16x16x32_bf16 v[192:195], a[72:75], a[136:139], v[192:195]\n"
"v_accvgpr_read_b32 v18, a162\n"
"v_mfma_f32_16x16x32_bf16 v[192:195], a[76:79], a[140:143], v[192:195]\n"
"v_accvgpr_read_b32 v19, a163\n"
"v_lshlrev_b32 v12, 16, v16\n"
"v_mfma_f32_16x16x32_bf16 v[164:167], a[68:71], a[140:143], v[164:167]\n"
"v_and_b32 v13, s63, v16\n"
"v_mfma_f32_16x16x32_bf16 v[164:167], a[64:67], a[136:139], v[164:167]\n"
"s_waitcnt lgkmcnt(2)\n"
"v_lshlrev_b32 v14, 16, v17\n"
"v_mfma_f32_16x16x32_bf16 v[168:171], a[64:67], a[144:147], v[168:171]\n"
"v_and_b32 v15, s63, v17\n"
"v_mul_f32 v12, v12, v48\n"
"v_mfma_f32_16x16x32_bf16 v[168:171], a[68:71], a[148:151], v[168:171]\n"
"global_load_dwordx4 a[60:63], v0, s[34:35] offset:3072 sc1\n"
"v_mul_f32 v13, v13, v49\n"
"v_mfma_f32_16x16x32_bf16 v[196:199], a[76:79], a[148:151], v[196:199]\n"
"v_mul_f32 v14, v14, v50\n"
"v_mfma_f32_16x16x32_bf16 v[196:199], a[72:75], a[144:147], v[196:199]\n"
"v_mul_f32 v15, v15, v51\n"
"v_cvt_pk_bf16_f32 v24, v12, v13\n"
"v_mfma_f32_16x16x32_bf16 a[20:23], a[72:75], a[152:155], a[20:23]\n"
"s_add_u32 s34, s34, 8192\n"
"v_cvt_pk_bf16_f32 v25, v14, v15\n"
"v_mfma_f32_16x16x32_bf16 a[20:23], a[76:79], a[156:159], a[20:23]\n"
"v_lshlrev_b32 v12, 16, v18\n"
"v_mfma_f32_16x16x32_bf16 a[16:19], a[68:71], a[156:159], a[16:19]\n"
"v_and_b32 v13, s63, v18\n"
"v_lshlrev_b32 v14, 16, v19\n"
"v_mfma_f32_16x16x32_bf16 a[16:19], a[64:67], a[152:155], a[16:19]\n"
"s_waitcnt vmcnt(11)\n"
"v_and_b32 v15, s63, v19\n"
"v_mul_f32 v12, v12, v76\n"
"v_mfma_f32_16x16x32_bf16 v[200:203], a[80:83], a[96:99], v[200:203]\n"
"v_mul_f32 v13, v13, v77\n"
"v_mul_f32 v14, v14, v78\n"
"v_mfma_f32_16x16x32_bf16 v[200:203], a[84:87], a[100:103], v[200:203]\n"
"global_load_dwordx4 a[64:67], v0, s[34:35] offset:-4096 sc1\n"
"v_mul_f32 v15, v15, v79\n"
"v_mfma_f32_16x16x32_bf16 v[228:231], a[92:95], a[100:103], v[228:231]\n"
"ds_read_b128 a[100:103], v3 offset:17408\n"
"v_cvt_pk_bf16_f32 v26, v12, v13\n"
"v_mfma_f32_16x16x32_bf16 v[228:231], a[88:91], a[96:99], v[228:231]\n"
"ds_read_b128 a[96:99], v3 offset:16384\n"
"v_cvt_pk_bf16_f32 v27, v14, v15\n"
"v_mfma_f32_16x16x32_bf16 v[232:235], a[88:91], a[104:107], v[232:235]\n"
"global_store_dwordx4 v0, v[24:27], s[54:55] offset:-4096 sc1\n"
"ds_write_b128 v2, v[24:27] offset:8192\n"
"v_mfma_f32_16x16x32_bf16 v[232:235], a[92:95], a[108:111], v[232:235]\n"
"v_accvgpr_read_b32 v16, a164\n"
"v_mfma_f32_16x16x32_bf16 v[204:207], a[84:87], a[108:111], v[204:207]\n"
"ds_read_b128 a[108:111], v3 offset:19456\n"
"v_accvgpr_read_b32 v17, a165\n"
"v_mfma_f32_16x16x32_bf16 v[204:207], a[80:83], a[104:107], v[204:207]\n"
"ds_read_b128 a[104:107], v3 offset:18432\n"
"v_accvgpr_read_b32 v18, a166\n"
"v_mfma_f32_16x16x32_bf16 v[208:211], a[80:83], a[112:115], v[208:211]\n"
"v_accvgpr_read_b32 v19, a167\n"
"v_lshlrev_b32 v12, 16, v16\n"
"v_mfma_f32_16x16x32_bf16 v[208:211], a[84:87], a[116:119], v[208:211]\n"
"global_load_dwordx4 a[68:71], v0, s[34:35] offset:-3072 sc1\n"
"v_and_b32 v13, s63, v16\n"
"v_mfma_f32_16x16x32_bf16 v[236:239], a[92:95], a[116:119], v[236:239]\n"
"ds_read_b128 a[116:119], v3 offset:21504\n"
"v_lshlrev_b32 v14, 16, v17\n"
"v_mfma_f32_16x16x32_bf16 v[236:239], a[88:91], a[112:115], v[236:239]\n"
"ds_read_b128 a[112:115], v3 offset:20480\n"
"v_and_b32 v15, s63, v17\n"
"v_mfma_f32_16x16x32_bf16 v[240:243], a[88:91], a[120:123], v[240:243]\n"
"v_mul_f32 v12, v12, v104\n"
"v_mul_f32 v13, v13, v105\n"
"v_mfma_f32_16x16x32_bf16 v[240:243], a[92:95], a[124:127], v[240:243]\n"
"v_mul_f32 v14, v14, v106\n"
"v_mul_f32 v15, v15, v107\n"
"v_mfma_f32_16x16x32_bf16 v[212:215], a[84:87], a[124:127], v[212:215]\n"
"ds_read_b128 a[124:127], v3 offset:23552\n"
"v_cvt_pk_bf16_f32 v28, v12, v13\n"
"v_mfma_f32_16x16x32_bf16 v[212:215], a[80:83], a[120:123], v[212:215]\n"
"ds_read_b128 a[120:123], v3 offset:22528\n"
"v_cvt_pk_bf16_f32 v29, v14, v15\n"
"v_mfma_f32_16x16x32_bf16 v[216:219], a[80:83], a[128:131], v[216:219]\n"
"L_st4475:\n"
"s_cbranch_execz L_retA\n"
"ds_write_b64 v2, v[28:29] offset:9216\n"
"v_mfma_f32_16x16x32_bf16 v[216:219], a[84:87], a[132:135], v[216:219]\n"
"global_load_dwordx4 a[72:75], v0, s[34:35] offset:-2048 sc1\n"
"v_lshlrev_b32 v12, 16, v18\n"
"v_mfma_f32_16x16x32_bf16 v[244:247], a[92:95], a[132:135], v[244:247]\n"
"ds_read_b128 a[132:135], v3 offset:25600\n"
"v_and_b32 v13, s63, v18\n"
"v_mfma_f32_16x16x32_bf16 v[244:247], a[88:91], a[128:131], v[244:247]\n"
"ds_read_b128 a[128:131], v3 offset:24576\n"
"v_lshlrev_b32 v14, 16, v19\n"
"v_mfma_f32_16x16x32_bf16 v[248:251], a[88:91], a[136:139], v[248:251]\n"
"v_and_b32 v15, s63, v19\n"
"v_mul_f32 v12, v12, v132\n"
"v_mfma_f32_16x16x32_bf16 v[248:251], a[92:95], a[140:143], v[248:251]\n"
"v_mul_f32 v13, v13, v133\n"
"v_mfma_f32_16x16x32_bf16 v[220:223], a[84:87], a[140:143], v[220:223]\n"
"ds_read_b128 a[140:143], v3 offset:27648\n"
"v_mul_f32 v14, v14, v134\n"
"v_mfma_f32_16x16x32_bf16 v[220:223], a[80:83], a[136:139], v[220:223]\n"
"ds_read_b128 a[136:139], v3 offset:26624\n"
"v_mul_f32 v15, v15, v135\n"
"v_mfma_f32_16x16x32_bf16 v[224:227], a[80:83], a[144:147], v[224:227]\n"
"v_cvt_pk_bf16_f32 v30, v12, v13\n"
"v_cvt_pk_bf16_f32 v31, v14, v15\n"
"v_mfma_f32_16x16x32_bf16 v[224:227], a[84:87], a[148:151], v[224:227]\n"
"global_load_dwordx4 a[76:79], v0, s[34:35] offset:-1024 sc1\n"
"v_mfma_f32_16x16x32_bf16 v[252:255], a[92:95], a[148:151], v[252:255]\n"
"ds_read_b128 a[148:151], v3 offset:29696\n"
"v_mfma_f32_16x16x32_bf16 v[252:255], a[88:91], a[144:147], v[252:255]\n"
"ds_read_b128 a[144:147], v3 offset:28672\n"
"v_mfma_f32_16x16x32_bf16 a[28:31], a[88:91], a[152:155], a[28:31]\n"
"ds_write_b64 v2, v[30:31] offset:9224\n"
"v_mfma_f32_16x16x32_bf16 a[28:31], a[92:95], a[156:159], a[28:31]\n"
"v_accvgpr_read_b32 v16, a168\n"
"v_accvgpr_read_b32 v17, a169\n"
"v_mfma_f32_16x16x32_bf16 a[24:27], a[84:87], a[156:159], a[24:27]\n"
"ds_read_b128 a[156:159], v3 offset:31744\n"
"v_accvgpr_read_b32 v18, a170\n"
"v_mfma_f32_16x16x32_bf16 a[24:27], a[80:83], a[152:155], a[24:27]\n"
"s_waitcnt vmcnt(10) lgkmcnt(13)\n"
"ds_read_b128 a[152:155], v3 offset:30720\n"
"v_accvgpr_read_b32 v19, a171\n"
"v_mfma_f32_16x16x32_bf16 v[144:147], a[32:35], a[96:99], v[144:147]\n"
"v_lshlrev_b32 v12, 16, v16\n"
"v_and_b32 v13, s63, v16\n"
"v_mfma_f32_16x16x32_bf16 v[144:147], a[36:39], a[100:103], v[144:147]\n"
"global_load_dwordx4 a[80:83], v0, s[34:35] offset:0 sc1\n"
"v_lshlrev_b32 v14, 16, v17\n"
"v_mfma_f32_16x16x32_bf16 v[172:175], a[44:47], a[100:103], v[172:175]\n"
"v_and_b32 v15, s63, v17\n"
"v_mul_f32 v12, v12, v52\n"
"v_mfma_f32_16x16x32_bf16 v[172:175], a[40:43], a[96:99], v[172:175]\n"
"v_mul_f32 v13, v13, v53\n"
"v_mul_f32 v14, v14, v54\n"
"v_mfma_f32_16x16x32_bf16 v[176:179], a[40:43], a[104:107], v[176:179]\n"
"v_mul_f32 v15, v15, v55\n"
"v_cvt_pk_bf16_f32 v24, v12, v13\n"
"v_mfma_f32_16x16x32_bf16 v[176:179], a[44:47], a[108:111], v[176:179]\n"
"v_cvt_pk_bf16_f32 v25, v14, v15\n"
"v_lshlrev_b32 v12, 16, v18\n"
"v_mfma_f32_16x16x32_bf16 v[148:151], a[36:39], a[108:111], v[148:151]\n"
"v_and_b32 v13, s63, v18\n"
"v_lshlrev_b32 v14, 16, v19\n"
"v_mfma_f32_16x16x32_bf16 v[148:151], a[32:35], a[104:107], v[148:151]\n"
"s_waitcnt lgkmcnt(10)\n"
"v_and_b32 v15, s63, v19\n"
"v_mul_f32 v12, v12, v80\n"
"v_mfma_f32_16x16x32_bf16 v[152:155], a[32:35], a[112:115], v[152:155]\n"
"v_mul_f32 v13, v13, v81\n"
"v_mul_f32 v14, v14, v82\n"
"v_mfma_f32_16x16x32_bf16 v[152:155], a[36:39], a[116:119], v[152:155]\n"
"global_load_dwordx4 a[84:87], v0, s[34:35] offset:1024 sc1\n"
"v_mul_f32 v15, v15, v83\n"
"v_mfma_f32_16x16x32_bf16 v[180:183], a[44:47], a[116:119], v[180:183]\n"
"v_cvt_pk_bf16_f32 v26, v12, v13\n"
"v_mfma_f32_16x16x32_bf16 v[180:183], a[40:43], a[112:115], v[180:183]\n"
"v_cvt_pk_bf16_f32 v27, v14, v15\n"
"v_mfma_f32_16x16x32_bf16 v[184:187], a[40:43], a[120:123], v[184:187]\n"
"global_store_dwordx4 v0, v[24:27], s[54:55] offset:-2048 sc1\n"
"ds_write_b128 v2, v[24:27] offset:10240\n"
"v_mfma_f32_16x16x32_bf16 v[184:187], a[44:47], a[124:127], v[184:187]\n"
"v_accvgpr_read_b32 v16, a172\n"
"v_mfma_f32_16x16x32_bf16 v[156:159], a[36:39], a[124:127], v[156:159]\n"
"v_accvgpr_read_b32 v17, a173\n"
"v_mfma_f32_16x16x32_bf16 v[156:159], a[32:35], a[120:123], v[156:159]\n"
"s_waitcnt lgkmcnt(6)\n"
"v_accvgpr_read_b32 v18, a174\n"
"v_accvgpr_read_b32 v19, a175\n"
"v_mfma_f32_16x16x32_bf16 v[160:163], a[32:35], a[128:131], v[160:163]\n"
"v_lshlrev_b32 v12, 16, v16\n"
"v_mfma_f32_16x16x32_bf16 v[160:163], a[36:39], a[132:135], v[160:163]\n"
"global_load_dwordx4 a[88:91], v0, s[34:35] offset:2048 sc1\n"
"v_and_b32 v13, s63, v16\n"
"v_mfma_f32_16x16x32_bf16 v[188:191], a[44:47], a[132:135], v[188:191]\n"
"v_lshlrev_b32 v14, 16, v17\n"
"v_and_b32 v15, s63, v17\n"
"v_mfma_f32_16x16x32_bf16 v[188:191], a[40:43], a[128:131], v[188:191]\n"
"v_mul_f32 v12, v12, v108\n"
"v_mfma_f32_16x16x32_bf16 v[192:195], a[40:43], a[136:139], v[192:195]\n"
"v_mul_f32 v13, v13, v109\n"
"v_mfma_f32_16x16x32_bf16 v[192:195], a[44:47], a[140:143], v[192:195]\n"
"v_mul_f32 v14, v14, v110\n"
"v_mul_f32 v15, v15, v111\n"
"v_mfma_f32_16x16x32_bf16 v[164:167], a[36:39], a[140:143], v[164:167]\n"
"v_cvt_pk_bf16_f32 v28, v12, v13\n"
"v_mfma_f32_16x16x32_bf16 v[164:167], a[32:35], a[136:139], v[164:167]\n"
"s_waitcnt lgkmcnt(1)\n"
"v_cvt_pk_bf16_f32 v29, v14, v15\n"
"v_mfma_f32_16x16x32_bf16 v[168:171], a[32:35], a[144:147], v[168:171]\n"
"v_lshlrev_b32 v12, 16, v18\n"
"v_and_b32 v13, s63, v18\n"
"v_mfma_f32_16x16x32_bf16 v[168:171], a[36:39], a[148:151], v[168:171]\n"
"global_load_dwordx4 a[92:95], v0, s[34:35] offset:3072 sc1\n"
"v_lshlrev_b32 v14, 16, v19\n"
"v_mfma_f32_16x16x32_bf16 v[196:199], a[44:47], a[148:151], v[196:199]\n"
"v_and_b32 v15, s63, v19\n"
"v_mfma_f32_16x16x32_bf16 v[196:199], a[40:43], a[144:147], v[196:199]\n"
"v_mul_f32 v12, v12, v136\n"
"v_mul_f32 v13, v13, v137\n"
"v_mfma_f32_16x16x32_bf16 a[20:23], a[40:43], a[152:155], a[20:23]\n"
"s_add_u32 s34, s34, 8192\n"
"v_mul_f32 v14, v14, v138\n"
"v_mfma_f32_16x16x32_bf16 a[20:23], a[44:47], a[156:159], a[20:23]\n"
"v_mul_f32 v15, v15, v139\n"
"v_mfma_f32_16x16x32_bf16 a[16:19], a[36:39], a[156:159], a[16:19]\n"
"v_cvt_pk_bf16_f32 v30, v12, v13\n"
"v_cvt_pk_bf16_f32 v31, v14, v15\n"
"v_mfma_f32_16x16x32_bf16 a[16:19], a[32:35], a[152:155], a[16:19]\n"
"s_waitcnt vmcnt(10)\n"
"global_store_dwordx4 v0, v[28:31], s[54:55] offset:-1024 sc1\n"
"ds_write_b128 v2, v[28:31] offset:11264\n"
"v_mfma_f32_16x16x32_bf16 v[200:203], a[48:51], a[96:99], v[200:203]\n"
"v_accvgpr_read_b32 v16, a176\n"
"v_mfma_f32_16x16x32_bf16 v[200:203], a[52:55], a[100:103], v[200:203]\n"
"global_load_dwordx4 a[32:35], v0, s[34:35] offset:-4096 sc1\n"
"v_accvgpr_read_b32 v17, a177\n"
"v_mfma_f32_16x16x32_bf16 v[228:231], a[60:63], a[100:103], v[228:231]\n"
"ds_read_b128 a[100:103], v2 offset:17408\n"
"v_accvgpr_read_b32 v18, a178\n"
"v_mfma_f32_16x16x32_bf16 v[228:231], a[56:59], a[96:99], v[228:231]\n"
"ds_read_b128 a[96:99], v2 offset:16384\n"
"v_accvgpr_read_b32 v19, a179\n"
"v_mfma_f32_16x16x32_bf16 v[232:235], a[56:59], a[104:107], v[232:235]\n"
"v_lshlrev_b32 v12, 16, v16\n"
"v_and_b32 v13, s63, v16\n"
"v_mfma_f32_16x16x32_bf16 v[232:235], a[60:63], a[108:111], v[232:235]\n"
"v_lshlrev_b32 v14, 16, v17\n"
"v_mfma_f32_16x16x32_bf16 v[204:207], a[52:55], a[108:111], v[204:207]\n"
"ds_read_b128 a[108:111], v2 offset:19456\n"
"v_and_b32 v15, s63, v17\n"
"v_mfma_f32_16x16x32_bf16 v[204:207], a[48:51], a[104:107], v[204:207]\n"
"ds_read_b128 a[104:107], v2 offset:18432\n"
"v_mul_f32 v12, v12, v56\n"
"v_mfma_f32_16x16x32_bf16 v[208:211], a[48:51], a[112:115], v[208:211]\n"
"v_mul_f32 v13, v13, v57\n"
"v_mul_f32 v14, v14, v58\n"
"v_mfma_f32_16x16x32_bf16 v[208:211], a[52:55], a[116:119], v[208:211]\n"
"global_load_dwordx4 a[36:39], v0, s[34:35] offset:-3072 sc1\n"
"v_mul_f32 v15, v15, v59\n"
"v_mfma_f32_16x16x32_bf16 v[236:239], a[60:63], a[116:119], v[236:239]\n"
"ds_read_b128 a[116:119], v2 offset:21504\n"
"v_cvt_pk_bf16_f32 v24, v12, v13\n"
"v_mfma_f32_16x16x32_bf16 v[236:239], a[56:59], a[112:115], v[236:239]\n"
"ds_read_b128 a[112:115], v2 offset:20480\n"
"v_cvt_pk_bf16_f32 v25, v14, v15\n"
"v_mfma_f32_16x16x32_bf16 v[240:243], a[56:59], a[120:123], v[240:243]\n"
"ds_write_b64 v2, v[24:25] offset:12288\n"
"v_mfma_f32_16x16x32_bf16 v[240:243], a[60:63], a[124:127], v[240:243]\n"
"v_lshlrev_b32 v12, 16, v18\n"
"v_and_b32 v13, s63, v18\n"
"v_mfma_f32_16x16x32_bf16 v[212:215], a[52:55], a[124:127], v[212:215]\n"
"ds_read_b128 a[124:127], v2 offset:23552\n"
"v_lshlrev_b32 v14, 16, v19\n"
"v_mfma_f32_16x16x32_bf16 v[212:215], a[48:51], a[120:123], v[212:215]\n"
"ds_read_b128 a[120:123], v2 offset:22528\n"
"v_and_b32 v15, s63, v19\n"
"v_mfma_f32_16x16x32_bf16 v[216:219], a[48:51], a[128:131], v[216:219]\n"
"v_mul_f32 v12, v12, v84\n"
"v_mul_f32 v13, v13, v85\n"
"v_mfma_f32_16x16x32_bf16 v[216:219], a[52:55], a[132:135], v[216:219]\n"
"global_load_dwordx4 a[40:43], v0, s[34:35] offset:-2048 sc1\n"
"v_mul_f32 v14, v14, v86\n"
"v_mfma_f32_16x16x32_bf16 v[244:247], a[60:63], a[132:135], v[244:247]\n"
"ds_read_b128 a[132:135], v2 offset:25600\n"
"v_mul_f32 v15, v15, v87\n"
"v_mfma_f32_16x16x32_bf16 v[244:247], a[56:59], a[128:131], v[244:247]\n"
"ds_read_b128 a[128:131], v2 offset:24576\n"
"v_cvt_pk_bf16_f32 v26, v12, v13\n"
"v_mfma_f32_16x16x32_bf16 v[248:251], a[56:59], a[136:139], v[248:251]\n"
"v_cvt_pk_bf16_f32 v27, v14, v15\n"
"v_mfma_f32_16x16x32_bf16 v[248:251], a[60:63], a[140:143], v[248:251]\n"
"ds_write_b64 v2, v[26:27] offset:12296\n"
"v_mfma_f32_16x16x32_bf16 v[220:223], a[52:55], a[140:143], v[220:223]\n"
"ds_read_b128 a[140:143], v2 offset:27648\n"
"v_accvgpr_read_b32 v16, a180\n"
"v_mfma_f32_16x16x32_bf16 v[220:223], a[48:51], a[136:139], v[220:223]\n"
"ds_read_b128 a[136:139], v2 offset:26624\n"
"v_accvgpr_read_b32 v17, a181\n"
"v_mfma_f32_16x16x32_bf16 v[224:227], a[48:51], a[144:147], v[224:227]\n"
"v_accvgpr_read_b32 v18, a182\n"
"v_accvgpr_read_b32 v19, a183\n"
"v_mfma_f32_16x16x32_bf16 v[224:227], a[52:55], a[148:151], v[224:227]\n"
"global_load_dwordx4 a[44:47], v0, s[34:35] offset:-1024 sc1\n"
"v_lshlrev_b32 v12, 16, v16\n"
"v_mfma_f32_16x16x32_bf16 v[252:255], a[60:63], a[148:151], v[252:255]\n"
"ds_read_b128 a[148:151], v2 offset:29696\n"
"v_and_b32 v13, s63, v16\n"
"v_mfma_f32_16x16x32_bf16 v[252:255], a[56:59], a[144:147], v[252:255]\n"
"ds_read_b128 a[144:147], v2 offset:28672\n"
"v_lshlrev_b32 v14, 16, v17\n"
"v_mfma_f32_16x16x32_bf16 a[28:31], a[56:59], a[152:155], a[28:31]\n"
"v_and_b32 v15, s63, v17\n"
"v_mul_f32 v12, v12, v112\n"
"v_mfma_f32_16x16x32_bf16 a[28:31], a[60:63], a[156:159], a[28:31]\n"
"v_mul_f32 v13, v13, v113\n"
"v_mul_f32 v14, v14, v114\n"
"v_mfma_f32_16x16x32_bf16 a[24:27], a[52:55], a[156:159], a[24:27]\n"
"ds_read_b128 a[156:159], v2 offset:31744\n"
"v_mul_f32 v15, v15, v115\n"
"v_mfma_f32_16x16x32_bf16 a[24:27], a[48:51], a[152:155], a[24:27]\n"
"s_waitcnt vmcnt(10) lgkmcnt(13)\n"
"ds_read_b128 a[152:155], v2 offset:30720\n"
"v_cvt_pk_bf16_f32 v28, v12, v13\n"
"v_mfma_f32_16x16x32_bf16 v[144:147], a[64:67], a[96:99], v[144:147]\n"
"v_cvt_pk_bf16_f32 v29, v14, v15\n"
"v_lshlrev_b32 v12, 16, v18\n"
"v_mfma_f32_16x16x32_bf16 v[144:147], a[68:71], a[100:103], v[144:147]\n"
"global_load_dwordx4 a[48:51], v0, s[34:35] offset:0 sc1\n"
"v_and_b32 v13, s63, v18\n"
"v_mfma_f32_16x16x32_bf16 v[172:175], a[76:79], a[100:103], v[172:175]\n"
"v_lshlrev_b32 v14, 16, v19\n"
"v_and_b32 v15, s63, v19\n"
"v_mfma_f32_16x16x32_bf16 v[172:175], a[72:75], a[96:99], v[172:175]\n"
"v_mul_f32 v12, v12, v140\n"
"v_mul_f32 v13, v13, v141\n"
"v_mfma_f32_16x16x32_bf16 v[176:179], a[72:75], a[104:107], v[176:179]\n"
"v_mul_f32 v14, v14, v142\n"
"v_mul_f32 v15, v15, v143\n"
"v_mfma_f32_16x16x32_bf16 v[176:179], a[76:79], a[108:111], v[176:179]\n"
"v_cvt_pk_bf16_f32 v30, v12, v13\n"
"v_mfma_f32_16x16x32_bf16 v[148:151], a[68:71], a[108:111], v[148:151]\n"
"v_cvt_pk_bf16_f32 v31, v14, v15\n"
"v_mfma_f32_16x16x32_bf16 v[148:151], a[64:67], a[104:107], v[148:151]\n"
"s_waitcnt lgkmcnt(9)\n"
"global_store_dwordx4 v0, v[28:31], s[54:55] offset:1024 sc1\n"
"ds_write_b128 v2, v[28:31] offset:13312\n"
"v_mfma_f32_16x16x32_bf16 v[152:155], a[64:67], a[112:115], v[152:155]\n"
"v_accvgpr_read_b32 v16, a184\n"
"v_accvgpr_read_b32 v17, a185\n"
"v_mfma_f32_16x16x32_bf16 v[152:155], a[68:71], a[116:119], v[152:155]\n"
"global_load_dwordx4 a[52:55], v0, s[34:35] offset:1024 sc1\n"
"v_accvgpr_read_b32 v18, a186\n"
"v_mfma_f32_16x16x32_bf16 v[180:183], a[76:79], a[116:119], v[180:183]\n"
"v_accvgpr_read_b32 v19, a187\n"
"v_mfma_f32_16x16x32_bf16 v[180:183], a[72:75], a[112:115], v[180:183]\n"
"v_lshlrev_b32 v12, 16, v16\n"
"v_mfma_f32_16x16x32_bf16 v[184:187], a[72:75], a[120:123], v[184:187]\n"
"v_and_b32 v13, s63, v16\n"
"v_lshlrev_b32 v14, 16, v17\n"
"v_mfma_f32_16x16x32_bf16 v[184:187], a[76:79], a[124:127], v[184:187]\n"
"v_and_b32 v15, s63, v17\n"
"v_mfma_f32_16x16x32_bf16 v[156:159], a[68:71], a[124:127], v[156:159]\n"
"v_accvgpr_read_b32 v10, a0\n"
"v_mfma_f32_16x16x32_bf16 v[156:159], a[64:67], a[120:123], v[156:159]\n"
"s_waitcnt lgkmcnt(5)\n"
"v_accvgpr_read_b32 v11, a1\n"
"v_mul_f32 v12, v12, v10\n"
"v_mfma_f32_16x16x32_bf16 v[160:163], a[64:67], a[128:131], v[160:163]\n"
"v_mul_f32 v13, v13, v11\n"
"v_mfma_f32_16x16x32_bf16 v[160:163], a[68:71], a[132:135], v[160:163]\n"
"global_load_dwordx4 a[56:59], v0, s[34:35] offset:2048 sc1\n"
"v_accvgpr_read_b32 v10, a2\n"
"v_mfma_f32_16x16x32_bf16 v[188:191], a[76:79], a[132:135], v[188:191]\n"
"v_accvgpr_read_b32 v11, a3\n"
"v_mul_f32 v14, v14, v10\n"
"v_mfma_f32_16x16x32_bf16 v[188:191], a[72:75], a[128:131], v[188:191]\n"
"v_mul_f32 v15, v15, v11\n"
"v_mfma_f32_16x16x32_bf16 v[192:195], a[72:75], a[136:139], v[192:195]\n"
"v_cvt_pk_bf16_f32 v24, v12, v13\n"
"v_mfma_f32_16x16x32_bf16 v[192:195], a[76:79], a[140:143], v[192:195]\n"
"v_cvt_pk_bf16_f32 v25, v14, v15\n"
"v_lshlrev_b32 v12, 16, v18\n"
"v_mfma_f32_16x16x32_bf16 v[164:167], a[68:71], a[140:143], v[164:167]\n"
"v_and_b32 v13, s63, v18\n"
"v_mfma_f32_16x16x32_bf16 v[164:167], a[64:67], a[136:139], v[164:167]\n"
"s_waitcnt lgkmcnt(1)\n"
"v_lshlrev_b32 v14, 16, v19\n"
"v_mfma_f32_16x16x32_bf16 v[168:171], a[64:67], a[144:147], v[168:171]\n"
"v_and_b32 v15, s63, v19\n"
"v_accvgpr_read_b32 v10, a4\n"
"v_mfma_f32_16x16x32_bf16 v[168:171], a[68:71], a[148:151], v[168:171]\n"
"global_load_dwordx4 a[60:63], v0, s[34:35] offset:3072 sc1\n"
"v_accvgpr_read_b32 v11, a5\n"
"v_mfma_f32_16x16x32_bf16 v[196:199], a[76:79], a[148:151], v[196:199]\n"
"v_mul_f32 v12, v12, v10\n"
"v_mfma_f32_16x16x32_bf16 v[196:199], a[72:75], a[144:147], v[196:199]\n"
"v_mul_f32 v13, v13, v11\n"
"v_accvgpr_read_b32 v10, a6\n"
"v_mfma_f32_16x16x32_bf16 a[20:23], a[72:75], a[152:155], a[20:23]\n"
"s_add_u32 s34, s34, 8192\n"
"v_accvgpr_read_b32 v11, a7\n"
"v_mfma_f32_16x16x32_bf16 a[20:23], a[76:79], a[156:159], a[20:23]\n"
"v_mul_f32 v14, v14, v10\n"
"v_mul_f32 v15, v15, v11\n"
"v_mfma_f32_16x16x32_bf16 a[16:19], a[68:71], a[156:159], a[16:19]\n"
"v_cvt_pk_bf16_f32 v26, v12, v13\n"
"v_cvt_pk_bf16_f32 v27, v14, v15\n"
"v_mfma_f32_16x16x32_bf16 a[16:19], a[64:67], a[152:155], a[16:19]\n"
"s_waitcnt vmcnt(10)\n"
"global_store_dwordx4 v0, v[24:27], s[54:55] offset:2048 sc1\n"
"ds_write_b128 v2, v[24:27] offset:14336\n"
"v_mfma_f32_16x16x32_bf16 v[200:203], a[80:83], a[96:99], v[200:203]\n"
"v_accvgpr_read_b32 v16, a188\n"
"v_mfma_f32_16x16x32_bf16 v[200:203], a[84:87], a[100:103], v[200:203]\n"
"global_load_dwordx4 a[64:67], v0, s[34:35] offset:-4096 sc1\n"
"v_accvgpr_read_b32 v17, a189\n"
"v_mfma_f32_16x16x32_bf16 v[228:231], a[92:95], a[100:103], v[228:231]\n"
"ds_read_b128 a[100:103], v2 offset:1024\n"
"v_accvgpr_read_b32 v18, a190\n"
"v_mfma_f32_16x16x32_bf16 v[228:231], a[88:91], a[96:99], v[228:231]\n"
"ds_read_b128 a[96:99], v2 offset:0\n"
"v_accvgpr_read_b32 v19, a191\n"
"v_mfma_f32_16x16x32_bf16 v[232:235], a[88:91], a[104:107], v[232:235]\n"
"v_lshlrev_b32 v12, 16, v16\n"
"v_and_b32 v13, s63, v16\n"
"v_mfma_f32_16x16x32_bf16 v[232:235], a[92:95], a[108:111], v[232:235]\n"
"v_lshlrev_b32 v14, 16, v17\n"
"v_and_b32 v15, s63, v17\n"
"v_mfma_f32_16x16x32_bf16 v[204:207], a[84:87], a[108:111], v[204:207]\n"
"ds_read_b128 a[108:111], v2 offset:3072\n"
"v_accvgpr_read_b32 v10, a8\n"
"v_mfma_f32_16x16x32_bf16 v[204:207], a[80:83], a[104:107], v[204:207]\n"
"ds_read_b128 a[104:107], v2 offset:2048\n"
"v_accvgpr_read_b32 v11, a9\n"
"v_mfma_f32_16x16x32_bf16 v[208:211], a[80:83], a[112:115], v[208:211]\n"
"v_mul_f32 v12, v12, v10\n"
"v_mul_f32 v13, v13, v11\n"
"v_mfma_f32_16x16x32_bf16 v[208:211], a[84:87], a[116:119], v[208:211]\n"
"global_load_dwordx4 a[68:71], v0, s[34:35] offset:-3072 sc1\n"
"v_accvgpr_read_b32 v10, a10\n"
"v_mfma_f32_16x16x32_bf16 v[236:239], a[92:95], a[116:119], v[236:239]\n"
"ds_read_b128 a[116:119], v2 offset:5120\n"
"v_accvgpr_read_b32 v11, a11\n"
"v_mfma_f32_16x16x32_bf16 v[236:239], a[88:91], a[112:115], v[236:239]\n"
"ds_read_b128 a[112:115], v2 offset:4096\n"
"v_mul_f32 v14, v14, v10\n"
"v_mfma_f32_16x16x32_bf16 v[240:243], a[88:91], a[120:123], v[240:243]\n"
"v_mul_f32 v15, v15, v11\n"
"v_cvt_pk_bf16_f32 v28, v12, v13\n"
"v_mfma_f32_16x16x32_bf16 v[240:243], a[92:95], a[124:127], v[240:243]\n"
"v_cvt_pk_bf16_f32 v29, v14, v15\n"
"v_lshlrev_b32 v12, 16, v18\n"
"v_mfma_f32_16x16x32_bf16 v[212:215], a[84:87], a[124:127], v[212:215]\n"
"ds_read_b128 a[124:127], v2 offset:7168\n"
"v_and_b32 v13, s63, v18\n"
"v_mfma_f32_16x16x32_bf16 v[212:215], a[80:83], a[120:123], v[212:215]\n"
"ds_read_b128 a[120:123], v2 offset:6144\n"
"v_lshlrev_b32 v14, 16, v19\n"
"v_mfma_f32_16x16x32_bf16 v[216:219], a[80:83], a[128:131], v[216:219]\n"
"v_and_b32 v15, s63, v19\n"
"v_accvgpr_read_b32 v10, a12\n"
"v_mfma_f32_16x16x32_bf16 v[216:219], a[84:87], a[132:135], v[216:219]\n"
"global_load_dwordx4 a[72:75], v0, s[34:35] offset:-2048 sc1\n"
"v_accvgpr_read_b32 v11, a13\n"
"v_mfma_f32_16x16x32_bf16 v[244:247], a[92:95], a[132:135], v[244:247]\n"
"ds_read_b128 a[132:135], v2 offset:9216\n"
"v_mul_f32 v12, v12, v10\n"
"v_mfma_f32_16x16x32_bf16 v[244:247], a[88:91], a[128:131], v[244:247]\n"
"ds_read_b128 a[128:131], v2 offset:8192\n"
"v_mul_f32 v13, v13, v11\n"
"v_mfma_f32_16x16x32_bf16 v[248:251], a[88:91], a[136:139], v[248:251]\n"
"v_accvgpr_read_b32 v10, a14\n"
"v_accvgpr_read_b32 v11, a15\n"
"v_mfma_f32_16x16x32_bf16 v[248:251], a[92:95], a[140:143], v[248:251]\n"
"v_mul_f32 v14, v14, v10\n"
"v_mul_f32 v15, v15, v11\n"
"v_mfma_f32_16x16x32_bf16 v[220:223], a[84:87], a[140:143], v[220:223]\n"
"ds_read_b128 a[140:143], v2 offset:11264\n"
"v_cvt_pk_bf16_f32 v30, v12, v13\n"
"v_mfma_f32_16x16x32_bf16 v[220:223], a[80:83], a[136:139], v[220:223]\n"
"ds_read_b128 a[136:139], v2 offset:10240\n"
"v_cvt_pk_bf16_f32 v31, v14, v15\n"
"v_mfma_f32_16x16x32_bf16 v[224:227], a[80:83], a[144:147], v[224:227]\n"
"global_store_dwordx4 v0, v[28:31], s[54:55] offset:3072 sc1\n"
"ds_write_b128 v2, v[28:31] offset:15360\n"
"v_mfma_f32_16x16x32_bf16 v[224:227], a[84:87], a[148:151], v[224:227]\n"
"global_load_dwordx4 a[76:79], v0, s[34:35] offset:-1024 sc1\n"
"v_mfma_f32_16x16x32_bf16 v[252:255], a[92:95], a[148:151], v[252:255]\n"
"ds_read_b128 a[148:151], v2 offset:13312\n"
"v_mfma_f32_16x16x32_bf16 v[252:255], a[88:91], a[144:147], v[252:255]\n"
"ds_read_b128 a[144:147], v2 offset:12288\n"
"v_mfma_f32_16x16x32_bf16 a[28:31], a[88:91], a[152:155], a[28:31]\n"
"s_add_u32 s62, s62, 1\n"
"v_mfma_f32_16x16x32_bf16 a[28:31], a[92:95], a[156:159], a[28:31]\n"
"v_mfma_f32_16x16x32_bf16 a[24:27], a[84:87], a[156:159], a[24:27]\n"
"ds_read_b128 a[156:159], v2 offset:15360\n"
"v_mfma_f32_16x16x32_bf16 a[24:27], a[80:83], a[152:155], a[24:27]\n"
"ds_read_b128 a[152:155], v2 offset:14336\n"
"L_layer:\n"
"s_waitcnt vmcnt(11) lgkmcnt(13)\n"
"v_mfma_f32_16x16x32_bf16 v[32:35], a[32:35], a[96:99], v[32:35]\n"
"s_mov_b64 s[96:97], s[52:53]\n"
"v_accvgpr_read_b32 v16, a192\n"
"v_accvgpr_read_b32 v17, a193\n"
"v_mfma_f32_16x16x32_bf16 v[32:35], a[36:39], a[100:103], v[32:35]\n"
"global_load_dwordx4 a[80:83], v0, s[34:35] offset:0 sc1\n"
"v_accvgpr_read_b32 v18, a194\n"
"v_mfma_f32_16x16x32_bf16 v[60:63], a[44:47], a[100:103], v[60:63]\n"
"s_mov_b64 s[98:99], s[54:55]\n"
"v_accvgpr_read_b32 v19, a195\n"
"v_mfma_f32_16x16x32_bf16 v[60:63], a[40:43], a[96:99], v[60:63]\n"
"v_lshlrev_b32 v12, 16, v16\n"
"v_and_b32 v13, s63, v16\n"
"v_mfma_f32_16x16x32_bf16 v[64:67], a[40:43], a[104:107], v[64:67]\n"
"s_add_u32 s52, s52, 131072\n"
"s_addc_u32 s53, s53, 0\n"
"v_lshlrev_b32 v14, 16, v17\n"
"v_mfma_f32_16x16x32_bf16 v[64:67], a[44:47], a[108:111], v[64:67]\n"
"v_and_b32 v15, s63, v17\n"
"v_mul_f32 v12, v12, v144\n"
"v_mfma_f32_16x16x32_bf16 v[36:39], a[36:39], a[108:111], v[36:39]\n"
"s_add_u32 s54, s54, 131072\n"
"s_addc_u32 s55, s55, 0\n"
"v_mul_f32 v13, v13, v145\n"
"v_mfma_f32_16x16x32_bf16 v[36:39], a[32:35], a[104:107], v[36:39]\n"
"s_waitcnt lgkmcnt(9)\n"
"v_mul_f32 v14, v14, v146\n"
"v_mul_f32 v15, v15, v147\n"
"v_mfma_f32_16x16x32_bf16 v[40:43], a[32:35], a[112:115], v[40:43]\n"
"ds_read_b128 v[20:23], v2 offset:0\n"
"v_cvt_pk_bf16_f32 v24, v12, v13\n"
"v_mfma_f32_16x16x32_bf16 v[40:43], a[36:39], a[116:119], v[40:43]\n"
"global_load_dwordx4 a[84:87], v0, s[34:35] offset:1024 sc1\n"
"v_cvt_pk_bf16_f32 v25, v14, v15\n"
"v_mfma_f32_16x16x32_bf16 v[68:71], a[44:47], a[116:119], v[68:71]\n"
"ds_write_b64 v2, v[24:25] offset:16384\n"
"v_mfma_f32_16x16x32_bf16 v[68:71], a[40:43], a[112:115], v[68:71]\n"
"v_lshlrev_b32 v12, 16, v18\n"
"v_and_b32 v13, s63, v18\n"
"v_mfma_f32_16x16x32_bf16 v[72:75], a[40:43], a[120:123], v[72:75]\n"
"s_waitcnt lgkmcnt(6)\n"
"s_barrier\n"
"v_mfma_f32_16x16x32_bf16 v[72:75], a[44:47], a[124:127], v[72:75]\n"
"v_lshlrev_b32 v14, 16, v19\n"
"v_and_b32 v15, s63, v19\n"
"v_mfma_f32_16x16x32_bf16 v[44:47], a[36:39], a[124:127], v[44:47]\n"
"v_mul_f32 v12, v12, v172\n"
"v_mul_f32 v13, v13, v173\n"
"v_mfma_f32_16x16x32_bf16 v[44:47], a[32:35], a[120:123], v[44:47]\n"
"v_mul_f32 v14, v14, v174\n"
"v_mfma_f32_16x16x32_bf16 v[48:51], a[32:35], a[128:131], v[48:51]\n"
"v_mul_f32 v15, v15, v175\n"
"v_mfma_f32_16x16x32_bf16 v[48:51], a[36:39], a[132:135], v[48:51]\n"
"global_load_dwordx4 a[88:91], v0, s[34:35] offset:2048 sc1\n"
"v_mfma_f32_16x16x32_bf16 v[76:79], a[44:47], a[132:135], v[76:79]\n"
"v_cvt_pk_bf16_f32 v26, v12, v13\n"
"v_mfma_f32_16x16x32_bf16 v[76:79], a[40:43], a[128:131], v[76:79]\n"
"v_cvt_pk_bf16_f32 v27, v14, v15\n"
"v_mfma_f32_16x16x32_bf16 v[80:83], a[40:43], a[136:139], v[80:83]\n"
"s_waitcnt lgkmcnt(1)\n"
"global_store_dwordx4 v0, v[20:23], s[96:97] offset:-4096 sc1\n"
"ds_write_b64 v2, v[26:27] offset:16392\n"
"v_accvgpr_read_b32 v16, a196\n"
"v_mfma_f32_16x16x32_bf16 v[80:83], a[44:47], a[140:143], v[80:83]\n"
"v_accvgpr_read_b32 v17, a197\n"
"v_accvgpr_read_b32 v18, a198\n"
"v_mfma_f32_16x16x32_bf16 v[52:55], a[36:39], a[140:143], v[52:55]\n"
"v_accvgpr_read_b32 v19, a199\n"
"v_lshlrev_b32 v12, 16, v16\n"
"v_mfma_f32_16x16x32_bf16 v[52:55], a[32:35], a[136:139], v[52:55]\n"
"v_and_b32 v13, s63, v16\n"
"v_lshlrev_b32 v14, 16, v17\n"
"v_mfma_f32_16x16x32_bf16 v[56:59], a[32:35], a[144:147], v[56:59]\n"
"v_and_b32 v15, s63, v17\n"
"v_mul_f32 v12, v12, v200\n"
"v_mfma_f32_16x16x32_bf16 v[56:59], a[36:39], a[148:151], v[56:59]\n"
"global_load_dwordx4 a[92:95], v0, s[34:35] offset:3072 sc1\n"
"v_mul_f32 v13, v13, v201\n"
"v_mfma_f32_16x16x32_bf16 v[84:87], a[44:47], a[148:151], v[84:87]\n"
"v_mul_f32 v14, v14, v202\n"
"v_mul_f32 v15, v15, v203\n"
"v_mfma_f32_16x16x32_bf16 v[84:87], a[40:43], a[144:147], v[84:87]\n"
"v_cvt_pk_bf16_f32 v28, v12, v13\n"
"v_cvt_pk_bf16_f32 v29, v14, v15\n"
"v_mfma_f32_16x16x32_bf16 a[4:7], a[40:43], a[152:155], a[4:7]\n"
"s_add_u32 s34, s34, 8192\n"
"ds_write_b64 v2, v[28:29] offset:17408\n"
"v_mfma_f32_16x16x32_bf16 a[4:7], a[44:47], a[156:159], a[4:7]\n"
"v_lshlrev_b32 v12, 16, v18\n"
"v_mfma_f32_16x16x32_bf16 a[0:3], a[36:39], a[156:159], a[0:3]\n"
"v_and_b32 v13, s63, v18\n"
"v_lshlrev_b32 v14, 16, v19\n"
"v_mfma_f32_16x16x32_bf16 a[0:3], a[32:35], a[152:155], a[0:3]\n"
"s_waitcnt vmcnt(11)\n"
"v_and_b32 v15, s63, v19\n"
"v_mul_f32 v12, v12, v228\n"
"v_mfma_f32_16x16x32_bf16 v[88:91], a[48:51], a[96:99], v[88:91]\n"
"v_mul_f32 v13, v13, v229\n"
"v_mul_f32 v14, v14, v230\n"
"v_mfma_f32_16x16x32_bf16 v[88:91], a[52:55], a[100:103], v[88:91]\n"
"global_load_dwordx4 a[32:35], v0, s[34:35] offset:-4096 sc1\n"
"v_mul_f32 v15, v15, v231\n"
"v_mfma_f32_16x16x32_bf16 v[116:119], a[60:63], a[100:103], v[116:119]\n"
"ds_read_b128 a[100:103], v3 offset:1024\n"
"v_cvt_pk_bf16_f32 v30, v12, v13\n"
"v_mfma_f32_16x16x32_bf16 v[116:119], a[56:59], a[96:99], v[116:119]\n"
"ds_read_b128 a[96:99], v3 offset:0\n"
"v_cvt_pk_bf16_f32 v31, v14, v15\n"
"v_mfma_f32_16x16x32_bf16 v[120:123], a[56:59], a[104:107], v[120:123]\n"
"ds_write_b64 v2, v[30:31] offset:17416\n"
"v_mfma_f32_16x16x32_bf16 v[120:123], a[60:63], a[108:111], v[120:123]\n"
"ds_read_b128 v[20:23], v2 offset:3072\n"
"v_accvgpr_read_b32 v16, a200\n"
"v_mfma_f32_16x16x32_bf16 v[92:95], a[52:55], a[108:111], v[92:95]\n"
"ds_read_b128 a[108:111], v3 offset:3072\n"
"v_accvgpr_read_b32 v17, a201\n"
"v_mfma_f32_16x16x32_bf16 v[92:95], a[48:51], a[104:107], v[92:95]\n"
"ds_read_b128 a[104:107], v3 offset:2048\n"
"v_accvgpr_read_b32 v18, a202\n"
"v_mfma_f32_16x16x32_bf16 v[96:99], a[48:51], a[112:115], v[96:99]\n"
"v_accvgpr_read_b32 v19, a203\n"
"v_lshlrev_b32 v12, 16, v16\n"
"v_mfma_f32_16x16x32_bf16 v[96:99], a[52:55], a[116:119], v[96:99]\n"
"global_load_dwordx4 a[36:39], v0, s[34:35] offset:-3072 sc1\n"
"v_and_b32 v13, s63, v16\n"
"v_mfma_f32_16x16x32_bf16 v[124:127], a[60:63], a[116:119], v[124:127]\n"
"ds_read_b128 a[116:119], v3 offset:5120\n"
"v_lshlrev_b32 v14, 16, v17\n"
"v_mfma_f32_16x16x32_bf16 v[124:127], a[56:59], a[112:115], v[124:127]\n"
"ds_read_b128 a[112:115], v3 offset:4096\n"
"v_and_b32 v15, s63, v17\n"
"v_mfma_f32_16x16x32_bf16 v[128:131], a[56:59], a[120:123], v[128:131]\n"
"v_mul_f32 v12, v12, v148\n"
"v_mul_f32 v13, v13, v149\n"
"v_mfma_f32_16x16x32_bf16 v[128:131], a[60:63], a[124:127], v[128:131]\n"
"v_mul_f32 v14, v14, v150\n"
"v_mul_f32 v15, v15, v151\n"
"v_mfma_f32_16x16x32_bf16 v[100:103], a[52:55], a[124:127], v[100:103]\n"
"ds_read_b128 a[124:127], v3 offset:7168\n"
"v_cvt_pk_bf16_f32 v24, v12, v13\n"
"v_mfma_f32_16x16x32_bf16 v[100:103], a[48:51], a[120:123], v[100:103]\n"
"ds_read_b128 a[120:123], v3 offset:6144\n"
"v_cvt_pk_bf16_f32 v25, v14, v15\n"
"v_mfma_f32_16x16x32_bf16 v[104:107], a[48:51], a[128:131], v[104:107]\n"
"s_waitcnt lgkmcnt(6)\n"
"global_store_dwordx4 v0, v[20:23], s[96:97] offset:-1024 sc1\n"
"ds_write_b64 v2, v[24:25] offset:18432\n"
"v_lshlrev_b32 v12, 16, v18\n"
"v_mfma_f32_16x16x32_bf16 v[104:107], a[52:55], a[132:135], v[104:107]\n"
"global_load_dwordx4 a[40:43], v0, s[34:35] offset:-2048 sc1\n"
"v_and_b32 v13, s63, v18\n"
"v_mfma_f32_16x16x32_bf16 v[132:135], a[60:63], a[132:135], v[132:135]\n"
"ds_read_b128 a[132:135], v3 offset:9216\n"
"v_lshlrev_b32 v14, 16, v19\n"
"v_mfma_f32_16x16x32_bf16 v[132:135], a[56:59], a[128:131], v[132:135]\n"
"ds_read_b128 a[128:131], v3 offset:8192\n"
"v_and_b32 v15, s63, v19\n"
"v_mfma_f32_16x16x32_bf16 v[136:139], a[56:59], a[136:139], v[136:139]\n"
"v_mul_f32 v12, v12, v176\n"
"v_mul_f32 v13, v13, v177\n"
"v_mfma_f32_16x16x32_bf16 v[136:139], a[60:63], a[140:143], v[136:139]\n"
"v_mul_f32 v14, v14, v178\n"
"v_mul_f32 v15, v15, v179\n"
"v_mfma_f32_16x16x32_bf16 v[108:111], a[52:55], a[140:143], v[108:111]\n"
"ds_read_b128 a[140:143], v3 offset:11264\n"
"v_cvt_pk_bf16_f32 v26, v12, v13\n"
"v_mfma_f32_16x16x32_bf16 v[108:111], a[48:51], a[136:139], v[108:111]\n"
"ds_read_b128 a[136:139], v3 offset:10240\n"
"v_cvt_pk_bf16_f32 v27, v14, v15\n"
"v_mfma_f32_16x16x32_bf16 v[112:115], a[48:51], a[144:147], v[112:115]\n"
"ds_write_b64 v2, v[26:27] offset:18440\n"
"v_mfma_f32_16x16x32_bf16 v[112:115], a[52:55], a[148:151], v[112:115]\n"
"global_load_dwordx4 a[44:47], v0, s[34:35] offset:-1024 sc1\n"
"v_accvgpr_read_b32 v16, a204\n"
"v_mfma_f32_16x16x32_bf16 v[140:143], a[60:63], a[148:151], v[140:143]\n"
"ds_read_b128 a[148:151], v3 offset:13312\n"
"v_accvgpr_read_b32 v17, a205\n"
"v_mfma_f32_16x16x32_bf16 v[140:143], a[56:59], a[144:147], v[140:143]\n"
"ds_read_b128 a[144:147], v3 offset:12288\n"
"v_accvgpr_read_b32 v18, a206\n"
"v_mfma_f32_16x16x32_bf16 a[12:15], a[56:59], a[152:155], a[12:15]\n"
"v_accvgpr_read_b32 v19, a207\n"
"v_lshlrev_b32 v12, 16, v16\n"
"v_mfma_f32_16x16x32_bf16 a[12:15], a[60:63], a[156:159], a[12:15]\n"
"v_and_b32 v13, s63, v16\n"
"v_lshlrev_b32 v14, 16, v17\n"
"v_mfma_f32_16x16x32_bf16 a[8:11], a[52:55], a[156:159], a[8:11]\n"
"ds_read_b128 a[156:159], v3 offset:15360\n"
"v_and_b32 v15, s63, v17\n"
"v_mfma_f32_16x16x32_bf16 a[8:11], a[48:51], a[152:155], a[8:11]\n"
"s_waitcnt vmcnt(10) lgkmcnt(13)\n"
"ds_read_b128 a[152:155], v3 offset:14336\n"
"v_mul_f32 v12, v12, v204\n"
"v_mfma_f32_16x16x32_bf16 v[32:35], a[64:67], a[96:99], v[32:35]\n"
"ds_read_b128 v[20:23], v2 offset:6144\n"
"v_mul_f32 v13, v13, v205\n"
"v_mfma_f32_16x16x32_bf16 v[32:35], a[68:71], a[100:103], v[32:35]\n"
"global_load_dwordx4 a[48:51], v0, s[34:35] offset:0 sc1\n"
"v_mul_f32 v14, v14, v206\n"
"v_mfma_f32_16x16x32_bf16 v[60:63], a[76:79], a[100:103], v[60:63]\n"
"v_mul_f32 v15, v15, v207\n"
"v_cvt_pk_bf16_f32 v28, v12, v13\n"
"v_mfma_f32_16x16x32_bf16 v[60:63], a[72:75], a[96:99], v[60:63]\n"
"L_st5078:\n"
"s_cbranch_execz L_st7361\n"
"v_cvt_pk_bf16_f32 v29, v14, v15\n"
"v_mfma_f32_16x16x32_bf16 v[64:67], a[72:75], a[104:107], v[64:67]\n"
"ds_write_b64 v2, v[28:29] offset:19456\n"
"v_mfma_f32_16x16x32_bf16 v[64:67], a[76:79], a[108:111], v[64:67]\n"
"v_lshlrev_b32 v12, 16, v18\n"
"v_and_b32 v13, s63, v18\n"
"v_mfma_f32_16x16x32_bf16 v[36:39], a[68:71], a[108:111], v[36:39]\n"
"v_lshlrev_b32 v14, 16, v19\n"
"v_and_b32 v15, s63, v19\n"
"v_mfma_f32_16x16x32_bf16 v[36:39], a[64:67], a[104:107], v[36:39]\n"
"s_waitcnt lgkmcnt(12)\n"
"v_mul_f32 v12, v12, v232\n"
"v_mul_f32 v13, v13, v233\n"
"v_mfma_f32_16x16x32_bf16 v[40:43], a[64:67], a[112:115], v[40:43]\n"
"v_mul_f32 v14, v14, v234\n"
"v_mfma_f32_16x16x32_bf16 v[40:43], a[68:71], a[116:119], v[40:43]\n"
"global_load_dwordx4 a[52:55], v0, s[34:35] offset:1024 sc1\n"
"v_mul_f32 v15, v15, v235\n"
"v_mfma_f32_16x16x32_bf16 v[68:71], a[76:79], a[116:119], v[68:71]\n"
"v_cvt_pk_bf16_f32 v30, v12, v13\n"
"v_mfma_f32_16x16x32_bf16 v[68:71], a[72:75], a[112:115], v[68:71]\n"
"v_cvt_pk_bf16_f32 v31, v14, v15\n"
"v_mfma_f32_16x16x32_bf16 v[72:75], a[72:75], a[120:123], v[72:75]\n"
"s_waitcnt lgkmcnt(1)\n"
"global_store_dwordx4 v0, v[20:23], s[96:97] offset:2048 sc1\n"
"ds_write_b64 v2, v[30:31] offset:19464\n"
"v_accvgpr_read_b32 v16, a208\n"
"v_mfma_f32_16x16x32_bf16 v[72:75], a[76:79], a[124:127], v[72:75]\n"
"v_accvgpr_read_b32 v17, a209\n"
"v_accvgpr_read_b32 v18, a210\n"
"v_mfma_f32_16x16x32_bf16 v[44:47], a[68:71], a[124:127], v[44:47]\n"
"v_accvgpr_read_b32 v19, a211\n"
"v_mfma_f32_16x16x32_bf16 v[44:47], a[64:67], a[120:123], v[44:47]\n"
"v_lshlrev_b32 v12, 16, v16\n"
"v_and_b32 v13, s63, v16\n"
"v_mfma_f32_16x16x32_bf16 v[48:51], a[64:67], a[128:131], v[48:51]\n"
"v_lshlrev_b32 v14, 16, v17\n"
"v_mfma_f32_16x16x32_bf16 v[48:51], a[68:71], a[132:135], v[48:51]\n"
"global_load_dwordx4 a[56:59], v0, s[34:35] offset:2048 sc1\n"
"v_and_b32 v15, s63, v17\n"
"v_mfma_f32_16x16x32_bf16 v[76:79], a[76:79], a[132:135], v[76:79]\n"
"v_mul_f32 v12, v12, v152\n"
"v_mul_f32 v13, v13, v153\n"
"v_mfma_f32_16x16x32_bf16 v[76:79], a[72:75], a[128:131], v[76:79]\n"
"v_mul_f32 v14, v14, v154\n"
"v_mul_f32 v15, v15, v155\n"
"v_mfma_f32_16x16x32_bf16 v[80:83], a[72:75], a[136:139], v[80:83]\n"
"v_cvt_pk_bf16_f32 v24, v12, v13\n"
"v_cvt_pk_bf16_f32 v25, v14, v15\n"
"v_mfma_f32_16x16x32_bf16 v[80:83], a[76:79], a[140:143], v[80:83]\n"
"ds_write_b64 v2, v[24:25] offset:20480\n"
"v_mfma_f32_16x16x32_bf16 v[52:55], a[68:71], a[140:143], v[52:55]\n"
"v_lshlrev_b32 v12, 16, v18\n"
"v_mfma_f32_16x16x32_bf16 v[52:55], a[64:67], a[136:139], v[52:55]\n"
"v_and_b32 v13, s63, v18\n"
"v_lshlrev_b32 v14, 16, v19\n"
"v_mfma_f32_16x16x32_bf16 v[56:59], a[64:67], a[144:147], v[56:59]\n"
"v_and_b32 v15, s63, v19\n"
"v_mul_f32 v12, v12, v180\n"
"v_mfma_f32_16x16x32_bf16 v[56:59], a[68:71], a[148:151], v[56:59]\n"
"global_load_dwordx4 a[60:63], v0, s[34:35] offset:3072 sc1\n"
"v_mul_f32 v13, v13, v181\n"
"v_mfma_f32_16x16x32_bf16 v[84:87], a[76:79], a[148:151], v[84:87]\n"
"v_mul_f32 v14, v14, v182\n"
"v_mul_f32 v15, v15, v183\n"
"v_mfma_f32_16x16x32_bf16 v[84:87], a[72:75], a[144:147], v[84:87]\n"
"v_cvt_pk_bf16_f32 v26, v12, v13\n"
"v_cvt_pk_bf16_f32 v27, v14, v15\n"
"v_mfma_f32_16x16x32_bf16 a[4:7], a[72:75], a[152:155], a[4:7]\n"
"s_add_u32 s34, s34, 8192\n"
"ds_write_b64 v2, v[26:27] offset:20488\n"
"v_mfma_f32_16x16x32_bf16 a[4:7], a[76:79], a[156:159], a[4:7]\n"
"ds_read_b128 v[20:23], v2 offset:9216\n"
"v_mfma_f32_16x16x32_bf16 a[0:3], a[68:71], a[156:159], a[0:3]\n"
"v_accvgpr_read_b32 v16, a212\n"
"v_mfma_f32_16x16x32_bf16 a[0:3], a[64:67], a[152:155], a[0:3]\n"
"s_waitcnt vmcnt(10)\n"
"v_accvgpr_read_b32 v17, a213\n"
"v_accvgpr_read_b32 v18, a214\n"
"v_mfma_f32_16x16x32_bf16 v[88:91], a[80:83], a[96:99], v[88:91]\n"
"v_accvgpr_read_b32 v19, a215\n"
"v_lshlrev_b32 v12, 16, v16\n"
"v_mfma_f32_16x16x32_bf16 v[88:91], a[84:87], a[100:103], v[88:91]\n"
"global_load_dwordx4 a[64:67], v0, s[34:35] offset:-4096 sc1\n"
"v_and_b32 v13, s63, v16\n"
"v_mfma_f32_16x16x32_bf16 v[116:119], a[92:95], a[100:103], v[116:119]\n"
"ds_read_b128 a[100:103], v4 offset:1024\n"
"v_lshlrev_b32 v14, 16, v17\n"
"v_mfma_f32_16x16x32_bf16 v[116:119], a[88:91], a[96:99], v[116:119]\n"
"ds_read_b128 a[96:99], v4 offset:0\n"
"v_and_b32 v15, s63, v17\n"
"v_mfma_f32_16x16x32_bf16 v[120:123], a[88:91], a[104:107], v[120:123]\n"
"v_mul_f32 v12, v12, v208\n"
"v_mul_f32 v13, v13, v209\n"
"v_mfma_f32_16x16x32_bf16 v[120:123], a[92:95], a[108:111], v[120:123]\n"
"v_mul_f32 v14, v14, v210\n"
"v_mul_f32 v15, v15, v211\n"
"v_mfma_f32_16x16x32_bf16 v[92:95], a[84:87], a[108:111], v[92:95]\n"
"ds_read_b128 a[108:111], v4 offset:3072\n"
"v_cvt_pk_bf16_f32 v28, v12, v13\n"
"v_mfma_f32_16x16x32_bf16 v[92:95], a[80:83], a[104:107], v[92:95]\n"
"ds_read_b128 a[104:107], v4 offset:2048\n"
"v_cvt_pk_bf16_f32 v29, v14, v15\n"
"v_mfma_f32_16x16x32_bf16 v[96:99], a[80:83], a[112:115], v[96:99]\n"
"s_waitcnt lgkmcnt(4)\n"
"global_store_dwordx4 v0, v[20:23], s[98:99] offset:-3072 sc1\n"
"ds_write_b64 v2, v[28:29] offset:21504\n"
"v_lshlrev_b32 v12, 16, v18\n"
"v_mfma_f32_16x16x32_bf16 v[96:99], a[84:87], a[116:119], v[96:99]\n"
"global_load_dwordx4 a[68:71], v0, s[34:35] offset:-3072 sc1\n"
"v_and_b32 v13, s63, v18\n"
"v_mfma_f32_16x16x32_bf16 v[124:127], a[92:95], a[116:119], v[124:127]\n"
"ds_read_b128 a[116:119], v4 offset:5120\n"
"v_lshlrev_b32 v14, 16, v19\n"
"v_mfma_f32_16x16x32_bf16 v[124:127], a[88:91], a[112:115], v[124:127]\n"
"ds_read_b128 a[112:115], v4 offset:4096\n"
"v_and_b32 v15, s63, v19\n"
"v_mfma_f32_16x16x32_bf16 v[128:131], a[88:91], a[120:123], v[128:131]\n"
"v_mul_f32 v12, v12, v236\n"
"v_mul_f32 v13, v13, v237\n"
"v_mfma_f32_16x16x32_bf16 v[128:131], a[92:95], a[124:127], v[128:131]\n"
"v_mul_f32 v14, v14, v238\n"
"v_mul_f32 v15, v15, v239\n"
"v_mfma_f32_16x16x32_bf16 v[100:103], a[84:87], a[124:127], v[100:103]\n"
"ds_read_b128 a[124:127], v4 offset:7168\n"
"v_cvt_pk_bf16_f32 v30, v12, v13\n"
"v_mfma_f32_16x16x32_bf16 v[100:103], a[80:83], a[120:123], v[100:103]\n"
"ds_read_b128 a[120:123], v4 offset:6144\n"
"v_cvt_pk_bf16_f32 v31, v14, v15\n"
"v_mfma_f32_16x16x32_bf16 v[104:107], a[80:83], a[128:131], v[104:107]\n"
"ds_write_b64 v2, v[30:31] offset:21512\n"
"v_mfma_f32_16x16x32_bf16 v[104:107], a[84:87], a[132:135], v[104:107]\n"
"global_load_dwordx4 a[72:75], v0, s[34:35] offset:-2048 sc1\n"
"v_accvgpr_read_b32 v16, a216\n"
"v_mfma_f32_16x16x32_bf16 v[132:135], a[92:95], a[132:135], v[132:135]\n"
"ds_read_b128 a[132:135], v4 offset:9216\n"
"v_accvgpr_read_b32 v17, a217\n"
"v_mfma_f32_16x16x32_bf16 v[132:135], a[88:91], a[128:131], v[132:135]\n"
"ds_read_b128 a[128:131], v4 offset:8192\n"
"v_accvgpr_read_b32 v18, a218\n"
"v_mfma_f32_16x16x32_bf16 v[136:139], a[88:91], a[136:139], v[136:139]\n"
"v_accvgpr_read_b32 v19, a219\n"
"v_mfma_f32_16x16x32_bf16 v[136:139], a[92:95], a[140:143], v[136:139]\n"
"v_lshlrev_b32 v12, 16, v16\n"
"v_and_b32 v13, s63, v16\n"
"v_mfma_f32_16x16x32_bf16 v[108:111], a[84:87], a[140:143], v[108:111]\n"
"ds_read_b128 a[140:143], v4 offset:11264\n"
"v_lshlrev_b32 v14, 16, v17\n"
"v_mfma_f32_16x16x32_bf16 v[108:111], a[80:83], a[136:139], v[108:111]\n"
"ds_read_b128 a[136:139], v4 offset:10240\n"
"v_and_b32 v15, s63, v17\n"
"v_mfma_f32_16x16x32_bf16 v[112:115], a[80:83], a[144:147], v[112:115]\n"
"ds_read_b128 v[20:23], v2 offset:12288\n"
"v_mul_f32 v12, v12, v156\n"
"v_mfma_f32_16x16x32_bf16 v[112:115], a[84:87], a[148:151], v[112:115]\n"
"global_load_dwordx4 a[76:79], v0, s[34:35] offset:-1024 sc1\n"
"v_mfma_f32_16x16x32_bf16 v[140:143], a[92:95], a[148:151], v[140:143]\n"
"ds_read_b128 a[148:151], v4 offset:13312\n"
"v_mfma_f32_16x16x32_bf16 v[140:143], a[88:91], a[144:147], v[140:143]\n"
"ds_read_b128 a[144:147], v4 offset:12288\n"
"v_mfma_f32_16x16x32_bf16 a[12:15], a[88:91], a[152:155], a[12:15]\n"
"v_mul_f32 v13, v13, v157\n"
"v_mfma_f32_16x16x32_bf16 a[12:15], a[92:95], a[156:159], a[12:15]\n"
"v_mul_f32 v14, v14, v158\n"
"v_mfma_f32_16x16x32_bf16 a[8:11], a[84:87], a[156:159], a[8:11]\n"
"ds_read_b128 a[156:159], v4 offset:15360\n"
"v_mfma_f32_16x16x32_bf16 a[8:11], a[80:83], a[152:155], a[8:11]\n"
"s_waitcnt vmcnt(10) lgkmcnt(14)\n"
"ds_read_b128 a[152:155], v4 offset:14336\n"
"v_mfma_f32_16x16x32_bf16 v[32:35], a[32:35], a[96:99], v[32:35]\n"
"v_mul_f32 v15, v15, v159\n"
"v_mfma_f32_16x16x32_bf16 v[32:35], a[36:39], a[100:103], v[32:35]\n"
"global_load_dwordx4 a[80:83], v0, s[34:35] offset:0 sc1\n"
"v_mfma_f32_16x16x32_bf16 v[60:63], a[44:47], a[100:103], v[60:63]\n"
"s_add_u32 s65, s65, 1\n"
"v_cvt_pk_bf16_f32 v24, v12, v13\n"
"v_mfma_f32_16x16x32_bf16 v[60:63], a[40:43], a[96:99], v[60:63]\n"
"v_cvt_pk_bf16_f32 v25, v14, v15\n"
"v_mfma_f32_16x16x32_bf16 v[64:67], a[40:43], a[104:107], v[64:67]\n"
"s_waitcnt lgkmcnt(4)\n"
"global_store_dwordx4 v0, v[20:23], s[98:99] offset:0 sc1\n"
"ds_write_b64 v2, v[24:25] offset:22528\n"
"v_lshlrev_b32 v12, 16, v18\n"
"v_mfma_f32_16x16x32_bf16 v[64:67], a[44:47], a[108:111], v[64:67]\n"
"v_and_b32 v13, s63, v18\n"
"v_lshlrev_b32 v14, 16, v19\n"
"v_mfma_f32_16x16x32_bf16 v[36:39], a[36:39], a[108:111], v[36:39]\n"
"v_and_b32 v15, s63, v19\n"
"v_mul_f32 v12, v12, v184\n"
"v_mfma_f32_16x16x32_bf16 v[36:39], a[32:35], a[104:107], v[36:39]\n"
"v_mul_f32 v13, v13, v185\n"
"v_mul_f32 v14, v14, v186\n"
"v_mfma_f32_16x16x32_bf16 v[40:43], a[32:35], a[112:115], v[40:43]\n"
"v_mul_f32 v15, v15, v187\n"
"v_cvt_pk_bf16_f32 v26, v12, v13\n"
"v_mfma_f32_16x16x32_bf16 v[40:43], a[36:39], a[116:119], v[40:43]\n"
"global_load_dwordx4 a[84:87], v0, s[34:35] offset:1024 sc1\n"
"v_cvt_pk_bf16_f32 v27, v14, v15\n"
"v_mfma_f32_16x16x32_bf16 v[68:71], a[44:47], a[116:119], v[68:71]\n"
"ds_write_b64 v2, v[26:27] offset:22536\n"
"v_mfma_f32_16x16x32_bf16 v[68:71], a[40:43], a[112:115], v[68:71]\n"
"v_accvgpr_read_b32 v16, a220\n"
"v_accvgpr_read_b32 v17, a221\n"
"v_mfma_f32_16x16x32_bf16 v[72:75], a[40:43], a[120:123], v[72:75]\n"
"v_accvgpr_read_b32 v18, a222\n"
"v_accvgpr_read_b32 v19, a223\n"
"v_mfma_f32_16x16x32_bf16 v[72:75], a[44:47], a[124:127], v[72:75]\n"
"v_lshlrev_b32 v12, 16, v16\n"
"v_and_b32 v13, s63, v16\n"
"v_mfma_f32_16x16x32_bf16 v[44:47], a[36:39], a[124:127], v[44:47]\n"
"v_lshlrev_b32 v14, 16, v17\n"
"v_and_b32 v15, s63, v17\n"
"v_mfma_f32_16x16x32_bf16 v[44:47], a[32:35], a[120:123], v[44:47]\n"
"v_mul_f32 v12, v12, v212\n"
"v_mul_f32 v13, v13, v213\n"
"v_mfma_f32_16x16x32_bf16 v[48:51], a[32:35], a[128:131], v[48:51]\n"
"v_mul_f32 v14, v14, v214\n"
"v_mul_f32 v15, v15, v215\n"
"v_mfma_f32_16x16x32_bf16 v[48:51], a[36:39], a[132:135], v[48:51]\n"
"global_load_dwordx4 a[88:91], v0, s[34:35] offset:2048 sc1\n"
"v_cvt_pk_bf16_f32 v28, v12, v13\n"
"v_mfma_f32_16x16x32_bf16 v[76:79], a[44:47], a[132:135], v[76:79]\n"
"v_cvt_pk_bf16_f32 v29, v14, v15\n"
"v_mfma_f32_16x16x32_bf16 v[76:79], a[40:43], a[128:131], v[76:79]\n"
"ds_write_b64 v2, v[28:29] offset:23552\n"
"v_mfma_f32_16x16x32_bf16 v[80:83], a[40:43], a[136:139], v[80:83]\n"
"v_lshlrev_b32 v12, 16, v18\n"
"v_and_b32 v13, s63, v18\n"
"v_mfma_f32_16x16x32_bf16 v[80:83], a[44:47], a[140:143], v[80:83]\n"
"v_lshlrev_b32 v14, 16, v19\n"
"v_and_b32 v15, s63, v19\n"
"v_mfma_f32_16x16x32_bf16 v[52:55], a[36:39], a[140:143], v[52:55]\n"
"v_mul_f32 v12, v12, v240\n"
"v_mul_f32 v13, v13, v241\n"
"v_mfma_f32_16x16x32_bf16 v[52:55], a[32:35], a[136:139], v[52:55]\n"
"s_waitcnt lgkmcnt(3)\n"
"v_mul_f32 v14, v14, v242\n"
"v_mul_f32 v15, v15, v243\n"
"v_mfma_f32_16x16x32_bf16 v[56:59], a[32:35], a[144:147], v[56:59]\n"
"v_cvt_pk_bf16_f32 v30, v12, v13\n"
"v_cvt_pk_bf16_f32 v31, v14, v15\n"
"v_mfma_f32_16x16x32_bf16 v[56:59], a[36:39], a[148:151], v[56:59]\n"
"global_load_dwordx4 a[92:95], v0, s[34:35] offset:3072 sc1\n"
"v_mfma_f32_16x16x32_bf16 v[84:87], a[44:47], a[148:151], v[84:87]\n"
"ds_write_b64 v2, v[30:31] offset:23560\n"
"v_mfma_f32_16x16x32_bf16 v[84:87], a[40:43], a[144:147], v[84:87]\n"
"v_accvgpr_read_b32 v16, a224\n"
"v_accvgpr_read_b32 v17, a225\n"
"v_mfma_f32_16x16x32_bf16 a[4:7], a[40:43], a[152:155], a[4:7]\n"
"s_add_u32 s34, s34, 8192\n"
"v_accvgpr_read_b32 v18, a226\n"
"v_accvgpr_read_b32 v19, a227\n"
"v_mfma_f32_16x16x32_bf16 a[4:7], a[44:47], a[156:159], a[4:7]\n"
"v_lshlrev_b32 v12, 16, v16\n"
"v_and_b32 v13, s63, v16\n"
"v_mfma_f32_16x16x32_bf16 a[0:3], a[36:39], a[156:159], a[0:3]\n"
"v_lshlrev_b32 v14, 16, v17\n"
"v_and_b32 v15, s63, v17\n"
"v_mfma_f32_16x16x32_bf16 a[0:3], a[32:35], a[152:155], a[0:3]\n"
"s_waitcnt vmcnt(10)\n"
"v_mul_f32 v12, v12, v160\n"
"v_mul_f32 v13, v13, v161\n"
"v_mfma_f32_16x16x32_bf16 v[88:91], a[48:51], a[96:99], v[88:91]\n"
"v_mul_f32 v14, v14, v162\n"
"v_mul_f32 v15, v15, v163\n"
"v_mfma_f32_16x16x32_bf16 v[88:91], a[52:55], a[100:103], v[88:91]\n"
"global_load_dwordx4 a[32:35], v0, s[34:35] offset:-4096 sc1\n"
"v_cvt_pk_bf16_f32 v24, v12, v13\n"
"v_mfma_f32_16x16x32_bf16 v[116:119], a[60:63], a[100:103], v[116:119]\n"
"ds_read_b128 a[100:103], v5 offset:1024\n"
"v_cvt_pk_bf16_f32 v25, v14, v15\n"
"v_mfma_f32_16x16x32_bf16 v[116:119], a[56:59], a[96:99], v[116:119]\n"
"ds_read_b128 a[96:99], v5 offset:0\n"
"v_mfma_f32_16x16x32_bf16 v[120:123], a[56:59], a[104:107], v[120:123]\n"
"ds_write_b64 v2, v[24:25] offset:24576\n"
"v_mfma_f32_16x16x32_bf16 v[120:123], a[60:63], a[108:111], v[120:123]\n"
"v_lshlrev_b32 v12, 16, v18\n"
"v_and_b32 v13, s63, v18\n"
"v_mfma_f32_16x16x32_bf16 v[92:95], a[52:55], a[108:111], v[92:95]\n"
"ds_read_b128 a[108:111], v5 offset:3072\n"
"v_lshlrev_b32 v14, 16, v19\n"
"v_mfma_f32_16x16x32_bf16 v[92:95], a[48:51], a[104:107], v[92:95]\n"
"ds_read_b128 a[104:107], v5 offset:2048\n"
"v_and_b32 v15, s63, v19\n"
"v_mfma_f32_16x16x32_bf16 v[96:99], a[48:51], a[112:115], v[96:99]\n"
"v_mul_f32 v12, v12, v188\n"
"v_mul_f32 v13, v13, v189\n"
"v_mfma_f32_16x16x32_bf16 v[96:99], a[52:55], a[116:119], v[96:99]\n"
"global_load_dwordx4 a[36:39], v0, s[34:35] offset:-3072 sc1\n"
"v_mul_f32 v14, v14, v190\n"
"v_mfma_f32_16x16x32_bf16 v[124:127], a[60:63], a[116:119], v[124:127]\n"
"ds_read_b128 a[116:119], v5 offset:5120\n"
"v_mul_f32 v15, v15, v191\n"
"v_mfma_f32_16x16x32_bf16 v[124:127], a[56:59], a[112:115], v[124:127]\n"
"ds_read_b128 a[112:115], v5 offset:4096\n"
"v_cvt_pk_bf16_f32 v26, v12, v13\n"
"v_mfma_f32_16x16x32_bf16 v[128:131], a[56:59], a[120:123], v[128:131]\n"
"v_cvt_pk_bf16_f32 v27, v14, v15\n"
"v_mfma_f32_16x16x32_bf16 v[128:131], a[60:63], a[124:127], v[128:131]\n"
"ds_write_b64 v2, v[26:27] offset:24584\n"
"v_mfma_f32_16x16x32_bf16 v[100:103], a[52:55], a[124:127], v[100:103]\n"
"ds_read_b128 a[124:127], v5 offset:7168\n"
"v_accvgpr_read_b32 v16, a228\n"
"v_mfma_f32_16x16x32_bf16 v[100:103], a[48:51], a[120:123], v[100:103]\n"
"ds_read_b128 a[120:123], v5 offset:6144\n"
"v_accvgpr_read_b32 v17, a229\n"
"v_mfma_f32_16x16x32_bf16 v[104:107], a[48:51], a[128:131], v[104:107]\n"
"v_accvgpr_read_b32 v18, a230\n"
"v_accvgpr_read_b32 v19, a231\n"
"v_mfma_f32_16x16x32_bf16 v[104:107], a[52:55], a[132:135], v[104:107]\n"
"global_load_dwordx4 a[40:43], v0, s[34:35] offset:-2048 sc1\n"
"v_lshlrev_b32 v12, 16, v16\n"
"v_mfma_f32_16x16x32_bf16 v[132:135], a[60:63], a[132:135], v[132:135]\n"
"ds_read_b128 a[132:135], v5 offset:9216\n"
"v_and_b32 v13, s63, v16\n"
"v_mfma_f32_16x16x32_bf16 v[132:135], a[56:59], a[128:131], v[132:135]\n"
"ds_read_b128 a[128:131], v5 offset:8192\n"
"v_lshlrev_b32 v14, 16, v17\n"
"v_mfma_f32_16x16x32_bf16 v[136:139], a[56:59], a[136:139], v[136:139]\n"
"v_and_b32 v15, s63, v17\n"
"v_mul_f32 v12, v12, v216\n"
"v_mfma_f32_16x16x32_bf16 v[136:139], a[60:63], a[140:143], v[136:139]\n"
"v_mul_f32 v13, v13, v217\n"
"v_mul_f32 v14, v14, v218\n"
"v_mfma_f32_16x16x32_bf16 v[108:111], a[52:55], a[140:143], v[108:111]\n"
"ds_read_b128 a[140:143], v5 offset:11264\n"
"v_mul_f32 v15, v15, v219\n"
"v_mfma_f32_16x16x32_bf16 v[108:111], a[48:51], a[136:139], v[108:111]\n"
"ds_read_b128 a[136:139], v5 offset:10240\n"
"v_cvt_pk_bf16_f32 v28, v12, v13\n"
"v_mfma_f32_16x16x32_bf16 v[112:115], a[48:51], a[144:147], v[112:115]\n"
"v_cvt_pk_bf16_f32 v29, v14, v15\n"
"v_lshlrev_b32 v12, 16, v18\n"
"v_mfma_f32_16x16x32_bf16 v[112:115], a[52:55], a[148:151], v[112:115]\n"
"global_load_dwordx4 a[44:47], v0, s[34:35] offset:-1024 sc1\n"
"v_and_b32 v13, s63, v18\n"
"v_mfma_f32_16x16x32_bf16 v[140:143], a[60:63], a[148:151], v[140:143]\n"
"ds_read_b128 a[148:151], v5 offset:13312\n"
"v_lshlrev_b32 v14, 16, v19\n"
"v_mfma_f32_16x16x32_bf16 v[140:143], a[56:59], a[144:147], v[140:143]\n"
"ds_read_b128 a[144:147], v5 offset:12288\n"
"v_and_b32 v15, s63, v19\n"
"v_mfma_f32_16x16x32_bf16 a[12:15], a[56:59], a[152:155], a[12:15]\n"
"v_mul_f32 v12, v12, v244\n"
"v_mul_f32 v13, v13, v245\n"
"v_mfma_f32_16x16x32_bf16 a[12:15], a[60:63], a[156:159], a[12:15]\n"
"v_mul_f32 v14, v14, v246\n"
"v_mul_f32 v15, v15, v247\n"
"v_mfma_f32_16x16x32_bf16 a[8:11], a[52:55], a[156:159], a[8:11]\n"
"ds_read_b128 a[156:159], v5 offset:15360\n"
"v_cvt_pk_bf16_f32 v30, v12, v13\n"
"v_mfma_f32_16x16x32_bf16 a[8:11], a[48:51], a[152:155], a[8:11]\n"
"s_waitcnt vmcnt(9) lgkmcnt(12)\n"
"ds_read_b128 a[152:155], v5 offset:14336\n"
"v_cvt_pk_bf16_f32 v31, v14, v15\n"
"v_mfma_f32_16x16x32_bf16 v[32:35], a[64:67], a[96:99], v[32:35]\n"
"global_store_dwordx4 v0, v[28:31], s[54:55] offset:-3072 sc1\n"
"ds_write_b128 v2, v[28:31] offset:25600\n"
"v_mfma_f32_16x16x32_bf16 v[32:35], a[68:71], a[100:103], v[32:35]\n"
"global_load_dwordx4 a[48:51], v0, s[34:35] offset:0 sc1\n"
"v_accvgpr_read_b32 v16, a232\n"
"v_mfma_f32_16x16x32_bf16 v[60:63], a[76:79], a[100:103], v[60:63]\n"
"v_accvgpr_read_b32 v17, a233\n"
"v_accvgpr_read_b32 v18, a234\n"
"v_mfma_f32_16x16x32_bf16 v[60:63], a[72:75], a[96:99], v[60:63]\n"
"v_accvgpr_read_b32 v19, a235\n"
"v_lshlrev_b32 v12, 16, v16\n"
"v_mfma_f32_16x16x32_bf16 v[64:67], a[72:75], a[104:107], v[64:67]\n"
"v_and_b32 v13, s63, v16\n"
"v_lshlrev_b32 v14, 16, v17\n"
"v_mfma_f32_16x16x32_bf16 v[64:67], a[76:79], a[108:111], v[64:67]\n"
"v_and_b32 v15, s63, v17\n"
"v_mul_f32 v12, v12, v164\n"
"v_mfma_f32_16x16x32_bf16 v[36:39], a[68:71], a[108:111], v[36:39]\n"
"v_mul_f32 v13, v13, v165\n"
"v_mul_f32 v14, v14, v166\n"
"v_mfma_f32_16x16x32_bf16 v[36:39], a[64:67], a[104:107], v[36:39]\n"
"s_waitcnt lgkmcnt(9)\n"
"v_mul_f32 v15, v15, v167\n"
"v_cvt_pk_bf16_f32 v24, v12, v13\n"
"v_mfma_f32_16x16x32_bf16 v[40:43], a[64:67], a[112:115], v[40:43]\n"
"v_cvt_pk_bf16_f32 v25, v14, v15\n"
"v_mfma_f32_16x16x32_bf16 v[40:43], a[68:71], a[116:119], v[40:43]\n"
"global_load_dwordx4 a[52:55], v0, s[34:35] offset:1024 sc1\n"
"v_mfma_f32_16x16x32_bf16 v[68:71], a[76:79], a[116:119], v[68:71]\n"
"ds_write_b64 v2, v[24:25] offset:26624\n"
"v_mfma_f32_16x16x32_bf16 v[68:71], a[72:75], a[112:115], v[68:71]\n"
"v_lshlrev_b32 v12, 16, v18\n"
"v_and_b32 v13, s63, v18\n"
"v_mfma_f32_16x16x32_bf16 v[72:75], a[72:75], a[120:123], v[72:75]\n"
"v_lshlrev_b32 v14, 16, v19\n"
"v_and_b32 v15, s63, v19\n"
"v_mfma_f32_16x16x32_bf16 v[72:75], a[76:79], a[124:127], v[72:75]\n"
"v_mul_f32 v12, v12, v192\n"
"v_mul_f32 v13, v13, v193\n"
"v_mfma_f32_16x16x32_bf16 v[44:47], a[68:71], a[124:127], v[44:47]\n"
"v_mul_f32 v14, v14, v194\n"
"v_mul_f32 v15, v15, v195\n"
"v_mfma_f32_16x16x32_bf16 v[44:47], a[64:67], a[120:123], v[44:47]\n"
"s_waitcnt lgkmcnt(6)\n"
"v_cvt_pk_bf16_f32 v26, v12, v13\n"
"v_cvt_pk_bf16_f32 v27, v14, v15\n"
"v_mfma_f32_16x16x32_bf16 v[48:51], a[64:67], a[128:131], v[48:51]\n"
"ds_write_b64 v2, v[26:27] offset:26632\n"
"v_mfma_f32_16x16x32_bf16 v[48:51], a[68:71], a[132:135], v[48:51]\n"
"global_load_dwordx4 a[56:59], v0, s[34:35] offset:2048 sc1\n"
"v_accvgpr_read_b32 v16, a236\n"
"v_mfma_f32_16x16x32_bf16 v[76:79], a[76:79], a[132:135], v[76:79]\n"
"v_accvgpr_read_b32 v17, a237\n"
"v_accvgpr_read_b32 v18, a238\n"
"v_mfma_f32_16x16x32_bf16 v[76:79], a[72:75], a[128:131], v[76:79]\n"
"v_accvgpr_read_b32 v19, a239\n"
"v_lshlrev_b32 v12, 16, v16\n"
"v_mfma_f32_16x16x32_bf16 v[80:83], a[72:75], a[136:139], v[80:83]\n"
"v_and_b32 v13, s63, v16\n"
"v_mfma_f32_16x16x32_bf16 v[80:83], a[76:79], a[140:143], v[80:83]\n"
"v_lshlrev_b32 v14, 16, v17\n"
"v_mfma_f32_16x16x32_bf16 v[52:55], a[68:71], a[140:143], v[52:55]\n"
"v_and_b32 v15, s63, v17\n"
"v_mul_f32 v12, v12, v220\n"
"v_mfma_f32_16x16x32_bf16 v[52:55], a[64:67], a[136:139], v[52:55]\n"
"s_waitcnt lgkmcnt(3)\n"
"v_mul_f32 v13, v13, v221\n"
"v_mfma_f32_16x16x32_bf16 v[56:59], a[64:67], a[144:147], v[56:59]\n"
"v_mul_f32 v14, v14, v222\n"
"v_mul_f32 v15, v15, v223\n"
"v_mfma_f32_16x16x32_bf16 v[56:59], a[68:71], a[148:151], v[56:59]\n"
"global_load_dwordx4 a[60:63], v0, s[34:35] offset:3072 sc1\n"
"v_cvt_pk_bf16_f32 v28, v12, v13\n"
"v_mfma_f32_16x16x32_bf16 v[84:87], a[76:79], a[148:151], v[84:87]\n"
"v_cvt_pk_bf16_f32 v29, v14, v15\n"
"v_mfma_f32_16x16x32_bf16 v[84:87], a[72:75], a[144:147], v[84:87]\n"
"v_lshlrev_b32 v12, 16, v18\n"
"v_and_b32 v13, s63, v18\n"
"v_mfma_f32_16x16x32_bf16 a[4:7], a[72:75], a[152:155], a[4:7]\n"
"s_add_u32 s34, s34, 8192\n"
"v_lshlrev_b32 v14, 16, v19\n"
"v_mfma_f32_16x16x32_bf16 a[4:7], a[76:79], a[156:159], a[4:7]\n"
"v_and_b32 v15, s63, v19\n"
"v_mfma_f32_16x16x32_bf16 a[0:3], a[68:71], a[156:159], a[0:3]\n"
"v_mul_f32 v12, v12, v248\n"
"v_mul_f32 v13, v13, v249\n"
"v_mfma_f32_16x16x32_bf16 a[0:3], a[64:67], a[152:155], a[0:3]\n"
"s_waitcnt vmcnt(9)\n"
"v_mul_f32 v14, v14, v250\n"
"v_mfma_f32_16x16x32_bf16 v[88:91], a[80:83], a[96:99], v[88:91]\n"
"v_mul_f32 v15, v15, v251\n"
"v_cvt_pk_bf16_f32 v30, v12, v13\n"
"v_mfma_f32_16x16x32_bf16 v[88:91], a[84:87], a[100:103], v[88:91]\n"
"global_load_dwordx4 a[64:67], v0, s[34:35] offset:-4096 sc1\n"
"v_cvt_pk_bf16_f32 v31, v14, v15\n"
"v_mfma_f32_16x16x32_bf16 v[116:119], a[92:95], a[100:103], v[116:119]\n"
"global_store_dwordx4 v0, v[28:31], s[54:55] offset:-1024 sc1\n"
"ds_write_b128 v2, v[28:31] offset:27648\n"
"v_mfma_f32_16x16x32_bf16 v[116:119], a[88:91], a[96:99], v[116:119]\n"
"v_accvgpr_read_b32 v16, a240\n"
"v_mfma_f32_16x16x32_bf16 v[120:123], a[88:91], a[104:107], v[120:123]\n"
"v_accvgpr_read_b32 v17, a241\n"
"v_mfma_f32_16x16x32_bf16 v[120:123], a[92:95], a[108:111], v[120:123]\n"
"v_accvgpr_read_b32 v18, a242\n"
"v_mfma_f32_16x16x32_bf16 v[92:95], a[84:87], a[108:111], v[92:95]\n"
"v_accvgpr_read_b32 v19, a243\n"
"v_lshlrev_b32 v12, 16, v16\n"
"v_mfma_f32_16x16x32_bf16 v[92:95], a[80:83], a[104:107], v[92:95]\n"
"v_and_b32 v13, s63, v16\n"
"v_mfma_f32_16x16x32_bf16 v[96:99], a[80:83], a[112:115], v[96:99]\n"
"v_lshlrev_b32 v14, 16, v17\n"
"v_and_b32 v15, s63, v17\n"
"v_mfma_f32_16x16x32_bf16 v[96:99], a[84:87], a[116:119], v[96:99]\n"
"global_load_dwordx4 a[68:71], v0, s[34:35] offset:-3072 sc1\n"
"v_mul_f32 v12, v12, v168\n"
"v_mfma_f32_16x16x32_bf16 v[124:127], a[92:95], a[116:119], v[124:127]\n"
"v_mul_f32 v13, v13, v169\n"
"v_mfma_f32_16x16x32_bf16 v[124:127], a[88:91], a[112:115], v[124:127]\n"
"v_mul_f32 v14, v14, v170\n"
"v_mul_f32 v15, v15, v171\n"
"v_mfma_f32_16x16x32_bf16 v[128:131], a[88:91], a[120:123], v[128:131]\n"
"v_cvt_pk_bf16_f32 v24, v12, v13\n"
"v_mfma_f32_16x16x32_bf16 v[128:131], a[92:95], a[124:127], v[128:131]\n"
"v_cvt_pk_bf16_f32 v25, v14, v15\n"
"v_lshlrev_b32 v12, 16, v18\n"
"v_mfma_f32_16x16x32_bf16 v[100:103], a[84:87], a[124:127], v[100:103]\n"
"v_and_b32 v13, s63, v18\n"
"v_lshlrev_b32 v14, 16, v19\n"
"v_mfma_f32_16x16x32_bf16 v[100:103], a[80:83], a[120:123], v[100:103]\n"
"v_mov_b32 v8, s65\n"
"v_and_b32 v15, s63, v19\n"
"v_mfma_f32_16x16x32_bf16 v[104:107], a[80:83], a[128:131], v[104:107]\n"
"v_mov_b32 v9, s60\n"
"v_mul_f32 v12, v12, v196\n"
"v_mfma_f32_16x16x32_bf16 v[104:107], a[84:87], a[132:135], v[104:107]\n"
"global_load_dwordx4 a[72:75], v0, s[34:35] offset:-2048 sc1\n"
"v_mul_f32 v13, v13, v197\n"
"v_mfma_f32_16x16x32_bf16 v[132:135], a[92:95], a[132:135], v[132:135]\n"
"buffer_store_dwordx2 v[8:9], off, s[56:59], %13 sc1\n"
"v_mul_f32 v14, v14, v198\n"
"v_mfma_f32_16x16x32_bf16 v[132:135], a[88:91], a[128:131], v[132:135]\n"
"v_mul_f32 v15, v15, v199\n"
"v_cvt_pk_bf16_f32 v26, v12, v13\n"
"v_mfma_f32_16x16x32_bf16 v[136:139], a[88:91], a[136:139], v[136:139]\n"
"v_cvt_pk_bf16_f32 v27, v14, v15\n"
"v_mfma_f32_16x16x32_bf16 v[136:139], a[92:95], a[140:143], v[136:139]\n"
"global_store_dwordx4 v0, v[24:27], s[54:55] offset:0 sc1\n"
"ds_write_b128 v2, v[24:27] offset:28672\n"
"v_mfma_f32_16x16x32_bf16 v[108:111], a[84:87], a[140:143], v[108:111]\n"
"v_accvgpr_read_b32 v16, a248\n"
"v_mfma_f32_16x16x32_bf16 v[108:111], a[80:83], a[136:139], v[108:111]\n"
"v_accvgpr_read_b32 v17, a249\n"
"v_mfma_f32_16x16x32_bf16 v[112:115], a[80:83], a[144:147], v[112:115]\n"
"v_accvgpr_read_b32 v18, a250\n"
"v_accvgpr_read_b32 v19, a251\n"
"v_mfma_f32_16x16x32_bf16 v[112:115], a[84:87], a[148:151], v[112:115]\n"
"global_load_dwordx4 a[76:79], v0, s[34:35] offset:-1024 sc1\n"
"v_lshlrev_b32 v12, 16, v16\n"
"v_mfma_f32_16x16x32_bf16 v[140:143], a[92:95], a[148:151], v[140:143]\n"
"v_and_b32 v13, s63, v16\n"
"v_mfma_f32_16x16x32_bf16 v[140:143], a[88:91], a[144:147], v[140:143]\n"
"v_lshlrev_b32 v14, 16, v17\n"
"v_and_b32 v15, s63, v17\n"
"v_mfma_f32_16x16x32_bf16 a[12:15], a[88:91], a[152:155], a[12:15]\n"
"v_accvgpr_read_b32 v10, a16\n"
"v_mfma_f32_16x16x32_bf16 a[12:15], a[92:95], a[156:159], a[12:15]\n"
"v_accvgpr_read_b32 v11, a17\n"
"v_mfma_f32_16x16x32_bf16 a[8:11], a[84:87], a[156:159], a[8:11]\n"
"v_mul_f32 v12, v12, v10\n"
"v_mul_f32 v13, v13, v11\n"
"v_mfma_f32_16x16x32_bf16 a[8:11], a[80:83], a[152:155], a[8:11]\n"
"s_waitcnt vmcnt(12)\n"
"v_accvgpr_read_b32 v10, a18\n"
"v_mfma_f32_16x16x32_bf16 v[144:147], a[32:35], a[96:99], v[144:147]\n"
"v_accvgpr_read_b32 v11, a19\n"
"v_mul_f32 v14, v14, v10\n"
"v_mfma_f32_16x16x32_bf16 v[144:147], a[36:39], a[100:103], v[144:147]\n"
"global_load_dwordx4 a[80:83], v0, s[34:35] offset:0 sc1\n"
"v_mul_f32 v15, v15, v11\n"
"v_mfma_f32_16x16x32_bf16 v[172:175], a[44:47], a[100:103], v[172:175]\n"
"v_cvt_pk_bf16_f32 v28, v12, v13\n"
"v_mfma_f32_16x16x32_bf16 v[172:175], a[40:43], a[96:99], v[172:175]\n"
"v_cvt_pk_bf16_f32 v29, v14, v15\n"
"v_lshlrev_b32 v12, 16, v18\n"
"v_mfma_f32_16x16x32_bf16 v[176:179], a[40:43], a[104:107], v[176:179]\n"
"v_and_b32 v13, s63, v18\n"
"v_mfma_f32_16x16x32_bf16 v[176:179], a[44:47], a[108:111], v[176:179]\n"
"v_lshlrev_b32 v14, 16, v19\n"
"v_mfma_f32_16x16x32_bf16 v[148:151], a[36:39], a[108:111], v[148:151]\n"
"v_and_b32 v15, s63, v19\n"
"v_accvgpr_read_b32 v10, a20\n"
"v_mfma_f32_16x16x32_bf16 v[148:151], a[32:35], a[104:107], v[148:151]\n"
"v_accvgpr_read_b32 v11, a21\n"
"v_mfma_f32_16x16x32_bf16 v[152:155], a[32:35], a[112:115], v[152:155]\n"
"v_mul_f32 v12, v12, v10\n"
"v_mul_f32 v13, v13, v11\n"
"v_mfma_f32_16x16x32_bf16 v[152:155], a[36:39], a[116:119], v[152:155]\n"
"global_load_dwordx4 a[84:87], v0, s[34:35] offset:1024 sc1\n"
"v_accvgpr_read_b32 v10, a22\n"
"v_mfma_f32_16x16x32_bf16 v[180:183], a[44:47], a[116:119], v[180:183]\n"
"v_accvgpr_read_b32 v11, a23\n"
"v_mfma_f32_16x16x32_bf16 v[180:183], a[40:43], a[112:115], v[180:183]\n"
"v_mul_f32 v14, v14, v10\n"
"v_mul_f32 v15, v15, v11\n"
"v_mfma_f32_16x16x32_bf16 v[184:187], a[40:43], a[120:123], v[184:187]\n"
"v_cvt_pk_bf16_f32 v30, v12, v13\n"
"v_mfma_f32_16x16x32_bf16 v[184:187], a[44:47], a[124:127], v[184:187]\n"
"v_cvt_pk_bf16_f32 v31, v14, v15\n"
"v_mfma_f32_16x16x32_bf16 v[156:159], a[36:39], a[124:127], v[156:159]\n"
"global_store_dwordx4 v0, v[28:31], s[54:55] offset:2048 sc1\n"
"ds_write_b128 v2, v[28:31] offset:30720\n"
"v_mfma_f32_16x16x32_bf16 v[156:159], a[32:35], a[120:123], v[156:159]\n"
"v_accvgpr_read_b32 v16, a244\n"
"v_mfma_f32_16x16x32_bf16 v[160:163], a[32:35], a[128:131], v[160:163]\n"
"v_accvgpr_read_b32 v17, a245\n"
"v_accvgpr_read_b32 v18, a246\n"
"v_mfma_f32_16x16x32_bf16 v[160:163], a[36:39], a[132:135], v[160:163]\n"
"global_load_dwordx4 a[88:91], v0, s[34:35] offset:2048 sc1\n"
"v_accvgpr_read_b32 v19, a247\n"
"v_mfma_f32_16x16x32_bf16 v[188:191], a[44:47], a[132:135], v[188:191]\n"
"v_lshlrev_b32 v12, 16, v16\n"
"v_mfma_f32_16x16x32_bf16 v[188:191], a[40:43], a[128:131], v[188:191]\n"
"v_and_b32 v13, s63, v16\n"
"v_lshlrev_b32 v14, 16, v17\n"
"v_mfma_f32_16x16x32_bf16 v[192:195], a[40:43], a[136:139], v[192:195]\n"
"v_and_b32 v15, s63, v17\n"
"v_mfma_f32_16x16x32_bf16 v[192:195], a[44:47], a[140:143], v[192:195]\n"
"v_mul_f32 v12, v12, v224\n"
"v_mul_f32 v13, v13, v225\n"
"v_mfma_f32_16x16x32_bf16 v[164:167], a[36:39], a[140:143], v[164:167]\n"
"v_mul_f32 v14, v14, v226\n"
"v_mul_f32 v15, v15, v227\n"
"v_mfma_f32_16x16x32_bf16 v[164:167], a[32:35], a[136:139], v[164:167]\n"
"v_cvt_pk_bf16_f32 v24, v12, v13\n"
"v_cvt_pk_bf16_f32 v25, v14, v15\n"
"v_mfma_f32_16x16x32_bf16 v[168:171], a[32:35], a[144:147], v[168:171]\n"
"ds_write_b64 v2, v[24:25] offset:29696\n"
"v_mfma_f32_16x16x32_bf16 v[168:171], a[36:39], a[148:151], v[168:171]\n"
"global_load_dwordx4 a[92:95], v0, s[34:35] offset:3072 sc1\n"
"v_mfma_f32_16x16x32_bf16 v[196:199], a[44:47], a[148:151], v[196:199]\n"
"v_lshlrev_b32 v12, 16, v18\n"
"v_and_b32 v13, s63, v18\n"
"v_mfma_f32_16x16x32_bf16 v[196:199], a[40:43], a[144:147], v[196:199]\n"
"v_lshlrev_b32 v14, 16, v19\n"
"v_and_b32 v15, s63, v19\n"
"v_mfma_f32_16x16x32_bf16 a[20:23], a[40:43], a[152:155], a[20:23]\n"
"s_add_u32 s34, s34, 8192\n"
"v_mul_f32 v12, v12, v252\n"
"v_mul_f32 v13, v13, v253\n"
"v_mfma_f32_16x16x32_bf16 a[20:23], a[44:47], a[156:159], a[20:23]\n"
"v_mul_f32 v14, v14, v254\n"
"v_mul_f32 v15, v15, v255\n"
"v_mfma_f32_16x16x32_bf16 a[16:19], a[36:39], a[156:159], a[16:19]\n"
"v_cvt_pk_bf16_f32 v26, v12, v13\n"
"v_cvt_pk_bf16_f32 v27, v14, v15\n"
"v_mfma_f32_16x16x32_bf16 a[16:19], a[32:35], a[152:155], a[16:19]\n"
"L_st5690:\n"
"s_cbranch_execz L_st7932\n"
"s_waitcnt vmcnt(12)\n"
"ds_write_b64 v2, v[26:27] offset:29704\n"
"v_mfma_f32_16x16x32_bf16 v[200:203], a[48:51], a[96:99], v[200:203]\n"
"v_accvgpr_read_b32 v16, a252\n"
"v_mfma_f32_16x16x32_bf16 v[200:203], a[52:55], a[100:103], v[200:203]\n"
"global_load_dwordx4 a[32:35], v0, s[34:35] offset:-4096 sc1\n"
"v_accvgpr_read_b32 v17, a253\n"
"v_mfma_f32_16x16x32_bf16 v[228:231], a[60:63], a[100:103], v[228:231]\n"
"ds_read_b128 a[100:103], v4 offset:1024\n"
"v_accvgpr_read_b32 v18, a254\n"
"v_mfma_f32_16x16x32_bf16 v[228:231], a[56:59], a[96:99], v[228:231]\n"
"ds_read_b128 a[96:99], v4 offset:0\n"
"v_accvgpr_read_b32 v19, a255\n"
"v_mfma_f32_16x16x32_bf16 v[232:235], a[56:59], a[104:107], v[232:235]\n"
"v_lshlrev_b32 v12, 16, v16\n"
"v_and_b32 v13, s63, v16\n"
"v_mfma_f32_16x16x32_bf16 v[232:235], a[60:63], a[108:111], v[232:235]\n"
"v_lshlrev_b32 v14, 16, v17\n"
"v_and_b32 v15, s63, v17\n"
"v_mfma_f32_16x16x32_bf16 v[204:207], a[52:55], a[108:111], v[204:207]\n"
"ds_read_b128 a[108:111], v4 offset:3072\n"
"v_accvgpr_read_b32 v10, a24\n"
"v_mfma_f32_16x16x32_bf16 v[204:207], a[48:51], a[104:107], v[204:207]\n"
"ds_read_b128 a[104:107], v4 offset:2048\n"
"v_accvgpr_read_b32 v11, a25\n"
"v_mfma_f32_16x16x32_bf16 v[208:211], a[48:51], a[112:115], v[208:211]\n"
"v_mul_f32 v12, v12, v10\n"
"v_mul_f32 v13, v13, v11\n"
"v_mfma_f32_16x16x32_bf16 v[208:211], a[52:55], a[116:119], v[208:211]\n"
"global_load_dwordx4 a[36:39], v0, s[34:35] offset:-3072 sc1\n"
"v_accvgpr_read_b32 v10, a26\n"
"v_mfma_f32_16x16x32_bf16 v[236:239], a[60:63], a[116:119], v[236:239]\n"
"ds_read_b128 a[116:119], v4 offset:5120\n"
"v_accvgpr_read_b32 v11, a27\n"
"v_mfma_f32_16x16x32_bf16 v[236:239], a[56:59], a[112:115], v[236:239]\n"
"ds_read_b128 a[112:115], v4 offset:4096\n"
"v_mul_f32 v14, v14, v10\n"
"v_mfma_f32_16x16x32_bf16 v[240:243], a[56:59], a[120:123], v[240:243]\n"
"v_mul_f32 v15, v15, v11\n"
"v_cvt_pk_bf16_f32 v28, v12, v13\n"
"v_mfma_f32_16x16x32_bf16 v[240:243], a[60:63], a[124:127], v[240:243]\n"
"v_cvt_pk_bf16_f32 v29, v14, v15\n"
"v_lshlrev_b32 v12, 16, v18\n"
"v_mfma_f32_16x16x32_bf16 v[212:215], a[52:55], a[124:127], v[212:215]\n"
"ds_read_b128 a[124:127], v4 offset:7168\n"
"v_and_b32 v13, s63, v18\n"
"v_mfma_f32_16x16x32_bf16 v[212:215], a[48:51], a[120:123], v[212:215]\n"
"ds_read_b128 a[120:123], v4 offset:6144\n"
"v_lshlrev_b32 v14, 16, v19\n"
"v_mfma_f32_16x16x32_bf16 v[216:219], a[48:51], a[128:131], v[216:219]\n"
"v_and_b32 v15, s63, v19\n"
"v_accvgpr_read_b32 v10, a28\n"
"v_mfma_f32_16x16x32_bf16 v[216:219], a[52:55], a[132:135], v[216:219]\n"
"global_load_dwordx4 a[40:43], v0, s[34:35] offset:-2048 sc1\n"
"v_accvgpr_read_b32 v11, a29\n"
"v_mfma_f32_16x16x32_bf16 v[244:247], a[60:63], a[132:135], v[244:247]\n"
"ds_read_b128 a[132:135], v4 offset:9216\n"
"v_mul_f32 v12, v12, v10\n"
"v_mfma_f32_16x16x32_bf16 v[244:247], a[56:59], a[128:131], v[244:247]\n"
"ds_read_b128 a[128:131], v4 offset:8192\n"
"v_mul_f32 v13, v13, v11\n"
"v_mfma_f32_16x16x32_bf16 v[248:251], a[56:59], a[136:139], v[248:251]\n"
"v_accvgpr_read_b32 v10, a30\n"
"v_accvgpr_read_b32 v11, a31\n"
"v_mfma_f32_16x16x32_bf16 v[248:251], a[60:63], a[140:143], v[248:251]\n"
"v_mul_f32 v14, v14, v10\n"
"v_mul_f32 v15, v15, v11\n"
"v_mfma_f32_16x16x32_bf16 v[220:223], a[52:55], a[140:143], v[220:223]\n"
"ds_read_b128 a[140:143], v4 offset:11264\n"
"v_cvt_pk_bf16_f32 v30, v12, v13\n"
"v_mfma_f32_16x16x32_bf16 v[220:223], a[48:51], a[136:139], v[220:223]\n"
"ds_read_b128 a[136:139], v4 offset:10240\n"
"v_cvt_pk_bf16_f32 v31, v14, v15\n"
"v_mfma_f32_16x16x32_bf16 v[224:227], a[48:51], a[144:147], v[224:227]\n"
"global_store_dwordx4 v0, v[28:31], s[54:55] offset:3072 sc1\n"
"ds_write_b128 v2, v[28:31] offset:31744\n"
"v_mfma_f32_16x16x32_bf16 v[224:227], a[52:55], a[148:151], v[224:227]\n"
"global_load_dwordx4 a[44:47], v0, s[34:35] offset:-1024 sc1\n"
"v_mfma_f32_16x16x32_bf16 v[252:255], a[60:63], a[148:151], v[252:255]\n"
"ds_read_b128 a[148:151], v4 offset:13312\n"
"v_mfma_f32_16x16x32_bf16 v[252:255], a[56:59], a[144:147], v[252:255]\n"
"ds_read_b128 a[144:147], v4 offset:12288\n"
"v_mfma_f32_16x16x32_bf16 a[28:31], a[56:59], a[152:155], a[28:31]\n"
"v_mfma_f32_16x16x32_bf16 a[28:31], a[60:63], a[156:159], a[28:31]\n"
"v_mfma_f32_16x16x32_bf16 a[24:27], a[52:55], a[156:159], a[24:27]\n"
"ds_read_b128 a[156:159], v4 offset:15360\n"
"v_mfma_f32_16x16x32_bf16 a[24:27], a[48:51], a[152:155], a[24:27]\n"
"s_waitcnt vmcnt(10) lgkmcnt(12)\n"
"ds_read_b128 a[152:155], v4 offset:14336\n"
"v_mfma_f32_16x16x32_bf16 v[144:147], a[64:67], a[96:99], v[144:147]\n"
"ds_read_b128 v[24:27], v2 offset:16384\n"
"v_mfma_f32_16x16x32_bf16 v[144:147], a[68:71], a[100:103], v[144:147]\n"
"global_load_dwordx4 a[48:51], v0, s[34:35] offset:0 sc1\n"
"v_mfma_f32_16x16x32_bf16 v[172:175], a[76:79], a[100:103], v[172:175]\n"
"v_mfma_f32_16x16x32_bf16 v[172:175], a[72:75], a[96:99], v[172:175]\n"
"v_mfma_f32_16x16x32_bf16 v[176:179], a[72:75], a[104:107], v[176:179]\n"
"v_mfma_f32_16x16x32_bf16 v[176:179], a[76:79], a[108:111], v[176:179]\n"
"v_mfma_f32_16x16x32_bf16 v[148:151], a[68:71], a[108:111], v[148:151]\n"
"v_mfma_f32_16x16x32_bf16 v[148:151], a[64:67], a[104:107], v[148:151]\n"
"s_waitcnt lgkmcnt(10)\n"
"v_mfma_f32_16x16x32_bf16 v[152:155], a[64:67], a[112:115], v[152:155]\n"
"v_mfma_f32_16x16x32_bf16 v[152:155], a[68:71], a[116:119], v[152:155]\n"
"global_load_dwordx4 a[52:55], v0, s[34:35] offset:1024 sc1\n"
"v_mfma_f32_16x16x32_bf16 v[180:183], a[76:79], a[116:119], v[180:183]\n"
"v_mfma_f32_16x16x32_bf16 v[180:183], a[72:75], a[112:115], v[180:183]\n"
"v_mfma_f32_16x16x32_bf16 v[184:187], a[72:75], a[120:123], v[184:187]\n"
"s_waitcnt lgkmcnt(0)\n"
"global_store_dwordx4 v0, v[24:27], s[52:53] offset:-4096 sc1\n"
"v_mfma_f32_16x16x32_bf16 v[184:187], a[76:79], a[124:127], v[184:187]\n"
"v_mfma_f32_16x16x32_bf16 v[156:159], a[68:71], a[124:127], v[156:159]\n"
"v_mfma_f32_16x16x32_bf16 v[156:159], a[64:67], a[120:123], v[156:159]\n"
"v_mfma_f32_16x16x32_bf16 v[160:163], a[64:67], a[128:131], v[160:163]\n"
"v_mfma_f32_16x16x32_bf16 v[160:163], a[68:71], a[132:135], v[160:163]\n"
"global_load_dwordx4 a[56:59], v0, s[34:35] offset:2048 sc1\n"
"v_mfma_f32_16x16x32_bf16 v[188:191], a[76:79], a[132:135], v[188:191]\n"
"v_mfma_f32_16x16x32_bf16 v[188:191], a[72:75], a[128:131], v[188:191]\n"
"v_mfma_f32_16x16x32_bf16 v[192:195], a[72:75], a[136:139], v[192:195]\n"
"v_mfma_f32_16x16x32_bf16 v[192:195], a[76:79], a[140:143], v[192:195]\n"
"v_mfma_f32_16x16x32_bf16 v[164:167], a[68:71], a[140:143], v[164:167]\n"
"v_mfma_f32_16x16x32_bf16 v[164:167], a[64:67], a[136:139], v[164:167]\n"
"v_mfma_f32_16x16x32_bf16 v[168:171], a[64:67], a[144:147], v[168:171]\n"
"v_mfma_f32_16x16x32_bf16 v[168:171], a[68:71], a[148:151], v[168:171]\n"
"global_load_dwordx4 a[60:63], v0, s[34:35] offset:3072 sc1\n"
"v_mfma_f32_16x16x32_bf16 v[196:199], a[76:79], a[148:151], v[196:199]\n"
"v_mfma_f32_16x16x32_bf16 v[196:199], a[72:75], a[144:147], v[196:199]\n"
"v_mfma_f32_16x16x32_bf16 a[20:23], a[72:75], a[152:155], a[20:23]\n"
"s_add_u32 s34, s34, 8192\n"
"v_mfma_f32_16x16x32_bf16 a[20:23], a[76:79], a[156:159], a[20:23]\n"
"v_mfma_f32_16x16x32_bf16 a[16:19], a[68:71], a[156:159], a[16:19]\n"
"v_mfma_f32_16x16x32_bf16 a[16:19], a[64:67], a[152:155], a[16:19]\n"
"s_waitcnt vmcnt(10)\n"
"ds_read_b128 v[28:31], v2 offset:17408\n"
"v_mfma_f32_16x16x32_bf16 v[200:203], a[80:83], a[96:99], v[200:203]\n"
"v_mfma_f32_16x16x32_bf16 v[200:203], a[84:87], a[100:103], v[200:203]\n"
"global_load_dwordx4 a[64:67], v0, s[34:35] offset:-4096 sc1\n"
"v_mfma_f32_16x16x32_bf16 v[228:231], a[92:95], a[100:103], v[228:231]\n"
"ds_read_b128 a[100:103], v3 offset:1024\n"
"v_mfma_f32_16x16x32_bf16 v[228:231], a[88:91], a[96:99], v[228:231]\n"
"ds_read_b128 a[96:99], v3 offset:0\n"
"v_mfma_f32_16x16x32_bf16 v[232:235], a[88:91], a[104:107], v[232:235]\n"
"v_mfma_f32_16x16x32_bf16 v[232:235], a[92:95], a[108:111], v[232:235]\n"
"v_mfma_f32_16x16x32_bf16 v[204:207], a[84:87], a[108:111], v[204:207]\n"
"ds_read_b128 a[108:111], v3 offset:3072\n"
"v_mfma_f32_16x16x32_bf16 v[204:207], a[80:83], a[104:107], v[204:207]\n"
"ds_read_b128 a[104:107], v3 offset:2048\n"
"v_mfma_f32_16x16x32_bf16 v[208:211], a[80:83], a[112:115], v[208:211]\n"
"v_mfma_f32_16x16x32_bf16 v[208:211], a[84:87], a[116:119], v[208:211]\n"
"global_load_dwordx4 a[68:71], v0, s[34:35] offset:-3072 sc1\n"
"v_mfma_f32_16x16x32_bf16 v[236:239], a[92:95], a[116:119], v[236:239]\n"
"s_waitcnt lgkmcnt(4)\n"
"global_store_dwordx4 v0, v[28:31], s[52:53] offset:-3072 sc1\n"
"ds_read_b128 a[116:119], v3 offset:5120\n"
"v_mfma_f32_16x16x32_bf16 v[236:239], a[88:91], a[112:115], v[236:239]\n"
"ds_read_b128 a[112:115], v3 offset:4096\n"
"v_mfma_f32_16x16x32_bf16 v[240:243], a[88:91], a[120:123], v[240:243]\n"
"v_mfma_f32_16x16x32_bf16 v[240:243], a[92:95], a[124:127], v[240:243]\n"
"v_mfma_f32_16x16x32_bf16 v[212:215], a[84:87], a[124:127], v[212:215]\n"
"ds_read_b128 a[124:127], v3 offset:7168\n"
"v_mfma_f32_16x16x32_bf16 v[212:215], a[80:83], a[120:123], v[212:215]\n"
"ds_read_b128 a[120:123], v3 offset:6144\n"
"v_mfma_f32_16x16x32_bf16 v[216:219], a[80:83], a[128:131], v[216:219]\n"
"v_mfma_f32_16x16x32_bf16 v[216:219], a[84:87], a[132:135], v[216:219]\n"
"global_load_dwordx4 a[72:75], v0, s[34:35] offset:-2048 sc1\n"
"v_mfma_f32_16x16x32_bf16 v[244:247], a[92:95], a[132:135], v[244:247]\n"
"ds_read_b128 a[132:135], v3 offset:9216\n"
"v_mfma_f32_16x16x32_bf16 v[244:247], a[88:91], a[128:131], v[244:247]\n"
"ds_read_b128 a[128:131], v3 offset:8192\n"
"v_mfma_f32_16x16x32_bf16 v[248:251], a[88:91], a[136:139], v[248:251]\n"
"v_mfma_f32_16x16x32_bf16 v[248:251], a[92:95], a[140:143], v[248:251]\n"
"v_mfma_f32_16x16x32_bf16 v[220:223], a[84:87], a[140:143], v[220:223]\n"
"ds_read_b128 a[140:143], v3 offset:11264\n"
"v_mfma_f32_16x16x32_bf16 v[220:223], a[80:83], a[136:139], v[220:223]\n"
"ds_read_b128 a[136:139], v3 offset:10240\n"
"v_mfma_f32_16x16x32_bf16 v[224:227], a[80:83], a[144:147], v[224:227]\n"
"v_mfma_f32_16x16x32_bf16 v[224:227], a[84:87], a[148:151], v[224:227]\n"
"global_load_dwordx4 a[76:79], v0, s[34:35] offset:-1024 sc1\n"
"v_mfma_f32_16x16x32_bf16 v[252:255], a[92:95], a[148:151], v[252:255]\n"
"ds_read_b128 a[148:151], v3 offset:13312\n"
"v_mfma_f32_16x16x32_bf16 v[252:255], a[88:91], a[144:147], v[252:255]\n"
"ds_read_b128 a[144:147], v3 offset:12288\n"
"v_mfma_f32_16x16x32_bf16 a[28:31], a[88:91], a[152:155], a[28:31]\n"
"v_mfma_f32_16x16x32_bf16 a[28:31], a[92:95], a[156:159], a[28:31]\n"
"ds_read_b128 v[24:27], v2 offset:18432\n"
"v_mfma_f32_16x16x32_bf16 a[24:27], a[84:87], a[156:159], a[24:27]\n"
"ds_read_b128 a[156:159], v3 offset:15360\n"
"v_mfma_f32_16x16x32_bf16 a[24:27], a[80:83], a[152:155], a[24:27]\n"
"s_waitcnt vmcnt(10) lgkmcnt(12)\n"
"ds_read_b128 a[152:155], v3 offset:14336\n"
"v_mfma_f32_16x16x32_bf16 v[144:147], a[32:35], a[96:99], v[144:147]\n"
"v_mfma_f32_16x16x32_bf16 v[144:147], a[36:39], a[100:103], v[144:147]\n"
"global_load_dwordx4 a[80:83], v0, s[34:35] offset:0 sc1\n"
"v_mfma_f32_16x16x32_bf16 v[172:175], a[44:47], a[100:103], v[172:175]\n"
"v_mfma_f32_16x16x32_bf16 v[172:175], a[40:43], a[96:99], v[172:175]\n"
"v_mfma_f32_16x16x32_bf16 v[176:179], a[40:43], a[104:107], v[176:179]\n"
"v_mfma_f32_16x16x32_bf16 v[176:179], a[44:47], a[108:111], v[176:179]\n"
"v_mfma_f32_16x16x32_bf16 v[148:151], a[36:39], a[108:111], v[148:151]\n"
"v_mfma_f32_16x16x32_bf16 v[148:151], a[32:35], a[104:107], v[148:151]\n"
"s_waitcnt lgkmcnt(9)\n"
"v_mfma_f32_16x16x32_bf16 v[152:155], a[32:35], a[112:115], v[152:155]\n"
"s_waitcnt lgkmcnt(2)\n"
"global_store_dwordx4 v0, v[24:27], s[52:53] offset:-2048 sc1\n"
"v_mfma_f32_16x16x32_bf16 v[152:155], a[36:39], a[116:119], v[152:155]\n"
"global_load_dwordx4 a[84:87], v0, s[34:35] offset:1024 sc1\n"
"v_mfma_f32_16x16x32_bf16 v[180:183], a[44:47], a[116:119], v[180:183]\n"
"v_mfma_f32_16x16x32_bf16 v[180:183], a[40:43], a[112:115], v[180:183]\n"
"v_mfma_f32_16x16x32_bf16 v[184:187], a[40:43], a[120:123], v[184:187]\n"
"v_mfma_f32_16x16x32_bf16 v[184:187], a[44:47], a[124:127], v[184:187]\n"
"v_mfma_f32_16x16x32_bf16 v[156:159], a[36:39], a[124:127], v[156:159]\n"
"v_mfma_f32_16x16x32_bf16 v[156:159], a[32:35], a[120:123], v[156:159]\n"
"v_mfma_f32_16x16x32_bf16 v[160:163], a[32:35], a[128:131], v[160:163]\n"
"v_mfma_f32_16x16x32_bf16 v[160:163], a[36:39], a[132:135], v[160:163]\n"
"global_load_dwordx4 a[88:91], v0, s[34:35] offset:2048 sc1\n"
"v_mfma_f32_16x16x32_bf16 v[188:191], a[44:47], a[132:135], v[188:191]\n"
"v_mfma_f32_16x16x32_bf16 v[188:191], a[40:43], a[128:131], v[188:191]\n"
"v_mfma_f32_16x16x32_bf16 v[192:195], a[40:43], a[136:139], v[192:195]\n"
"v_mfma_f32_16x16x32_bf16 v[192:195], a[44:47], a[140:143], v[192:195]\n"
"v_mfma_f32_16x16x32_bf16 v[164:167], a[36:39], a[140:143], v[164:167]\n"
"v_mfma_f32_16x16x32_bf16 v[164:167], a[32:35], a[136:139], v[164:167]\n"
"s_waitcnt lgkmcnt(0)\n"
"v_mfma_f32_16x16x32_bf16 v[168:171], a[32:35], a[144:147], v[168:171]\n"
"v_mfma_f32_16x16x32_bf16 v[168:171], a[36:39], a[148:151], v[168:171]\n"
"global_load_dwordx4 a[92:95], v0, s[34:35] offset:3072 sc1\n"
"v_mfma_f32_16x16x32_bf16 v[196:199], a[44:47], a[148:151], v[196:199]\n"
"v_mfma_f32_16x16x32_bf16 v[196:199], a[40:43], a[144:147], v[196:199]\n"
"ds_read_b128 v[28:31], v2 offset:19456\n"
"v_mfma_f32_16x16x32_bf16 a[20:23], a[40:43], a[152:155], a[20:23]\n"
"s_add_u32 s34, s34, 8192\n"
"v_mfma_f32_16x16x32_bf16 a[20:23], a[44:47], a[156:159], a[20:23]\n"
"v_mfma_f32_16x16x32_bf16 a[16:19], a[36:39], a[156:159], a[16:19]\n"
"v_mfma_f32_16x16x32_bf16 a[16:19], a[32:35], a[152:155], a[16:19]\n"
"s_waitcnt vmcnt(10)\n"
"v_mfma_f32_16x16x32_bf16 v[200:203], a[48:51], a[96:99], v[200:203]\n"
"v_mfma_f32_16x16x32_bf16 v[200:203], a[52:55], a[100:103], v[200:203]\n"
"global_load_dwordx4 a[32:35], v0, s[34:35] offset:-4096 sc1\n"
"v_mfma_f32_16x16x32_bf16 v[228:231], a[60:63], a[100:103], v[228:231]\n"
"ds_read_b128 a[100:103], v2 offset:1024\n"
"v_mfma_f32_16x16x32_bf16 v[228:231], a[56:59], a[96:99], v[228:231]\n"
"ds_read_b128 a[96:99], v2 offset:0\n"
"v_mfma_f32_16x16x32_bf16 v[232:235], a[56:59], a[104:107], v[232:235]\n"
"v_mfma_f32_16x16x32_bf16 v[232:235], a[60:63], a[108:111], v[232:235]\n"
"v_mfma_f32_16x16x32_bf16 v[204:207], a[52:55], a[108:111], v[204:207]\n"
"s_waitcnt lgkmcnt(2)\n"
"global_store_dwordx4 v0, v[28:31], s[52:53] offset:-1024 sc1\n"
"ds_read_b128 a[108:111], v2 offset:3072\n"
"v_mfma_f32_16x16x32_bf16 v[204:207], a[48:51], a[104:107], v[204:207]\n"
"ds_read_b128 a[104:107], v2 offset:2048\n"
"v_mfma_f32_16x16x32_bf16 v[208:211], a[48:51], a[112:115], v[208:211]\n"
"v_mfma_f32_16x16x32_bf16 v[208:211], a[52:55], a[116:119], v[208:211]\n"
"global_load_dwordx4 a[36:39], v0, s[34:35] offset:-3072 sc1\n"
"v_mfma_f32_16x16x32_bf16 v[236:239], a[60:63], a[116:119], v[236:239]\n"
"ds_read_b128 a[116:119], v2 offset:5120\n"
"v_mfma_f32_16x16x32_bf16 v[236:239], a[56:59], a[112:115], v[236:239]\n"
"ds_read_b128 a[112:115], v2 offset:4096\n"
"v_mfma_f32_16x16x32_bf16 v[240:243], a[56:59], a[120:123], v[240:243]\n"
"v_mfma_f32_16x16x32_bf16 v[240:243], a[60:63], a[124:127], v[240:243]\n"
"v_mfma_f32_16x16x32_bf16 v[212:215], a[52:55], a[124:127], v[212:215]\n"
"ds_read_b128 a[124:127], v2 offset:7168\n"
"v_mfma_f32_16x16x32_bf16 v[212:215], a[48:51], a[120:123], v[212:215]\n"
"ds_read_b128 a[120:123], v2 offset:6144\n"
"v_mfma_f32_16x16x32_bf16 v[216:219], a[48:51], a[128:131], v[216:219]\n"
"v_mfma_f32_16x16x32_bf16 v[216:219], a[52:55], a[132:135], v[216:219]\n"
"global_load_dwordx4 a[40:43], v0, s[34:35] offset:-2048 sc1\n"
"v_mfma_f32_16x16x32_bf16 v[244:247], a[60:63], a[132:135], v[244:247]\n"
"ds_read_b128 a[132:135], v2 offset:9216\n"
"v_mfma_f32_16x16x32_bf16 v[244:247], a[56:59], a[128:131], v[244:247]\n"
"ds_read_b128 a[128:131], v2 offset:8192\n"
"v_mfma_f32_16x16x32_bf16 v[248:251], a[56:59], a[136:139], v[248:251]\n"
"v_mfma_f32_16x16x32_bf16 v[248:251], a[60:63], a[140:143], v[248:251]\n"
"v_mfma_f32_16x16x32_bf16 v[220:223], a[52:55], a[140:143], v[220:223]\n"
"ds_read_b128 a[140:143], v2 offset:11264\n"
"v_mfma_f32_16x16x32_bf16 v[220:223], a[48:51], a[136:139], v[220:223]\n"
"ds_read_b128 a[136:139], v2 offset:10240\n"
"v_mfma_f32_16x16x32_bf16 v[224:227], a[48:51], a[144:147], v[224:227]\n"
"ds_read_b128 v[24:27], v2 offset:20480\n"
"v_mfma_f32_16x16x32_bf16 v[224:227], a[52:55], a[148:151], v[224:227]\n"
"global_load_dwordx4 a[44:47], v0, s[34:35] offset:-1024 sc1\n"
"v_mfma_f32_16x16x32_bf16 v[252:255], a[60:63], a[148:151], v[252:255]\n"
"ds_read_b128 a[148:151], v2 offset:13312\n"
"v_mfma_f32_16x16x32_bf16 v[252:255], a[56:59], a[144:147], v[252:255]\n"
"ds_read_b128 a[144:147], v2 offset:12288\n"
"v_mfma_f32_16x16x32_bf16 a[28:31], a[56:59], a[152:155], a[28:31]\n"
"v_mfma_f32_16x16x32_bf16 a[28:31], a[60:63], a[156:159], a[28:31]\n"
"v_mfma_f32_16x16x32_bf16 a[24:27], a[52:55], a[156:159], a[24:27]\n"
"ds_read_b128 a[156:159], v2 offset:15360\n"
"v_mfma_f32_16x16x32_bf16 a[24:27], a[48:51], a[152:155], a[24:27]\n"
"s_waitcnt vmcnt(10) lgkmcnt(12)\n"
"ds_read_b128 a[152:155], v2 offset:14336\n"
"v_mfma_f32_16x16x32_bf16 v[144:147], a[64:67], a[96:99], v[144:147]\n"
"v_mfma_f32_16x16x32_bf16 v[144:147], a[68:71], a[100:103], v[144:147]\n"
"global_load_dwordx4 a[48:51], v0, s[34:35] offset:0 sc1\n"
"v_mfma_f32_16x16x32_bf16 v[172:175], a[76:79], a[100:103], v[172:175]\n"
"v_mfma_f32_16x16x32_bf16 v[172:175], a[72:75], a[96:99], v[172:175]\n"
"v_mfma_f32_16x16x32_bf16 v[176:179], a[72:75], a[104:107], v[176:179]\n"
"s_waitcnt lgkmcnt(4)\n"
"global_store_dwordx4 v0, v[24:27], s[52:53] offset:0 sc1\n"
"v_mfma_f32_16x16x32_bf16 v[176:179], a[76:79], a[108:111], v[176:179]\n"
"v_mfma_f32_16x16x32_bf16 v[148:151], a[68:71], a[108:111], v[148:151]\n"
"v_mfma_f32_16x16x32_bf16 v[148:151], a[64:67], a[104:107], v[148:151]\n"
"v_mfma_f32_16x16x32_bf16 v[152:155], a[64:67], a[112:115], v[152:155]\n"
"v_mfma_f32_16x16x32_bf16 v[152:155], a[68:71], a[116:119], v[152:155]\n"
"global_load_dwordx4 a[52:55], v0, s[34:35] offset:1024 sc1\n"
"v_mfma_f32_16x16x32_bf16 v[180:183], a[76:79], a[116:119], v[180:183]\n"
"v_mfma_f32_16x16x32_bf16 v[180:183], a[72:75], a[112:115], v[180:183]\n"
"v_mfma_f32_16x16x32_bf16 v[184:187], a[72:75], a[120:123], v[184:187]\n"
"v_mfma_f32_16x16x32_bf16 v[184:187], a[76:79], a[124:127], v[184:187]\n"
"v_mfma_f32_16x16x32_bf16 v[156:159], a[68:71], a[124:127], v[156:159]\n"
"v_mfma_f32_16x16x32_bf16 v[156:159], a[64:67], a[120:123], v[156:159]\n"
"v_mfma_f32_16x16x32_bf16 v[160:163], a[64:67], a[128:131], v[160:163]\n"
"v_mfma_f32_16x16x32_bf16 v[160:163], a[68:71], a[132:135], v[160:163]\n"
"global_load_dwordx4 a[56:59], v0, s[34:35] offset:2048 sc1\n"
"v_mfma_f32_16x16x32_bf16 v[188:191], a[76:79], a[132:135], v[188:191]\n"
"v_mfma_f32_16x16x32_bf16 v[188:191], a[72:75], a[128:131], v[188:191]\n"
"v_mfma_f32_16x16x32_bf16 v[192:195], a[72:75], a[136:139], v[192:195]\n"
"v_mfma_f32_16x16x32_bf16 v[192:195], a[76:79], a[140:143], v[192:195]\n"
"v_mfma_f32_16x16x32_bf16 v[164:167], a[68:71], a[140:143], v[164:167]\n"
"v_mfma_f32_16x16x32_bf16 v[164:167], a[64:67], a[136:139], v[164:167]\n"
"s_waitcnt lgkmcnt(0)\n"
"ds_read_b128 v[28:31], v2 offset:21504\n"
"v_mfma_f32_16x16x32_bf16 v[168:171], a[64:67], a[144:147], v[168:171]\n"
"v_mfma_f32_16x16x32_bf16 v[168:171], a[68:71], a[148:151], v[168:171]\n"
"global_load_dwordx4 a[60:63], v0, s[34:35] offset:3072 sc1\n"
"v_mfma_f32_16x16x32_bf16 v[196:199], a[76:79], a[148:151], v[196:199]\n"
"v_mfma_f32_16x16x32_bf16 v[196:199], a[72:75], a[144:147], v[196:199]\n"
"v_mfma_f32_16x16x32_bf16 a[20:23], a[72:75], a[152:155], a[20:23]\n"
"s_add_u32 s34, s34, 8192\n"
"v_mfma_f32_16x16x32_bf16 a[20:23], a[76:79], a[156:159], a[20:23]\n"
"v_mfma_f32_16x16x32_bf16 a[16:19], a[68:71], a[156:159], a[16:19]\n"
"v_mfma_f32_16x16x32_bf16 a[16:19], a[64:67], a[152:155], a[16:19]\n"
"s_waitcnt vmcnt(10)\n"
"v_mfma_f32_16x16x32_bf16 v[200:203], a[80:83], a[96:99], v[200:203]\n"
"v_mfma_f32_16x16x32_bf16 v[200:203], a[84:87], a[100:103], v[200:203]\n"
"global_load_dwordx4 a[64:67], v0, s[34:35] offset:-4096 sc1\n"
"v_mfma_f32_16x16x32_bf16 v[228:231], a[92:95], a[100:103], v[228:231]\n"
"s_waitcnt lgkmcnt(0)\n"
"global_store_dwordx4 v0, v[28:31], s[52:53] offset:1024 sc1\n"
"ds_read_b128 a[100:103], v2 offset:17408\n"
"v_mfma_f32_16x16x32_bf16 v[228:231], a[88:91], a[96:99], v[228:231]\n"
"ds_read_b128 a[96:99], v2 offset:16384\n"
"v_mfma_f32_16x16x32_bf16 v[232:235], a[88:91], a[104:107], v[232:235]\n"
"v_mfma_f32_16x16x32_bf16 v[232:235], a[92:95], a[108:111], v[232:235]\n"
"v_mfma_f32_16x16x32_bf16 v[204:207], a[84:87], a[108:111], v[204:207]\n"
"ds_read_b128 a[108:111], v2 offset:19456\n"
"v_mfma_f32_16x16x32_bf16 v[204:207], a[80:83], a[104:107], v[204:207]\n"
"ds_read_b128 a[104:107], v2 offset:18432\n"
"v_mfma_f32_16x16x32_bf16 v[208:211], a[80:83], a[112:115], v[208:211]\n"
"v_mfma_f32_16x16x32_bf16 v[208:211], a[84:87], a[116:119], v[208:211]\n"
"global_load_dwordx4 a[68:71], v0, s[34:35] offset:-3072 sc1\n"
"v_mfma_f32_16x16x32_bf16 v[236:239], a[92:95], a[116:119], v[236:239]\n"
"ds_read_b128 a[116:119], v2 offset:21504\n"
"v_mfma_f32_16x16x32_bf16 v[236:239], a[88:91], a[112:115], v[236:239]\n"
"ds_read_b128 a[112:115], v2 offset:20480\n"
"v_mfma_f32_16x16x32_bf16 v[240:243], a[88:91], a[120:123], v[240:243]\n"
"v_mfma_f32_16x16x32_bf16 v[240:243], a[92:95], a[124:127], v[240:243]\n"
"v_mfma_f32_16x16x32_bf16 v[212:215], a[84:87], a[124:127], v[212:215]\n"
"ds_read_b128 a[124:127], v2 offset:23552\n"
"v_mfma_f32_16x16x32_bf16 v[212:215], a[80:83], a[120:123], v[212:215]\n"
"ds_read_b128 a[120:123], v2 offset:22528\n"
"v_mfma_f32_16x16x32_bf16 v[216:219], a[80:83], a[128:131], v[216:219]\n"
"v_mfma_f32_16x16x32_bf16 v[216:219], a[84:87], a[132:135], v[216:219]\n"
"global_load_dwordx4 a[72:75], v0, s[34:35] offset:-2048 sc1\n"
"v_mfma_f32_16x16x32_bf16 v[244:247], a[92:95], a[132:135], v[244:247]\n"
"ds_read_b128 a[132:135], v2 offset:25600\n"
"v_mfma_f32_16x16x32_bf16 v[244:247], a[88:91], a[128:131], v[244:247]\n"
"ds_read_b128 a[128:131], v2 offset:24576\n"
"v_mfma_f32_16x16x32_bf16 v[248:251], a[88:91], a[136:139], v[248:251]\n"
"v_mfma_f32_16x16x32_bf16 v[248:251], a[92:95], a[140:143], v[248:251]\n"
"ds_read_b128 v[24:27], v2 offset:22528\n"
"v_mfma_f32_16x16x32_bf16 v[220:223], a[84:87], a[140:143], v[220:223]\n"
"ds_read_b128 a[140:143], v2 offset:27648\n"
"v_mfma_f32_16x16x32_bf16 v[220:223], a[80:83], a[136:139], v[220:223]\n"
"ds_read_b128 a[136:139], v2 offset:26624\n"
"v_mfma_f32_16x16x32_bf16 v[224:227], a[80:83], a[144:147], v[224:227]\n"
"v_mfma_f32_16x16x32_bf16 v[224:227], a[84:87], a[148:151], v[224:227]\n"
"global_load_dwordx4 a[76:79], v0, s[34:35] offset:-1024 sc1\n"
"v_mfma_f32_16x16x32_bf16 v[252:255], a[92:95], a[148:151], v[252:255]\n"
"ds_read_b128 a[148:151], v2 offset:29696\n"
"v_mfma_f32_16x16x32_bf16 v[252:255], a[88:91], a[144:147], v[252:255]\n"
"ds_read_b128 a[144:147], v2 offset:28672\n"
"v_mfma_f32_16x16x32_bf16 a[28:31], a[88:91], a[152:155], a[28:31]\n"
"v_mfma_f32_16x16x32_bf16 a[28:31], a[92:95], a[156:159], a[28:31]\n"
"v_mfma_f32_16x16x32_bf16 a[24:27], a[84:87], a[156:159], a[24:27]\n"
"s_bitcmp0_b32 %26, 0\n"
"ds_read_b128 a[156:159], v2 offset:31744\n"
"v_mfma_f32_16x16x32_bf16 a[24:27], a[80:83], a[152:155], a[24:27]\n"
"ds_read_b128 a[152:155], v2 offset:30720\n"
"s_cbranch_scc1 L_P3F\n"
"s_waitcnt vmcnt(10) lgkmcnt(13)\n"
"v_mfma_f32_16x16x32_bf16 v[32:35], a[32:35], a[96:99], v[32:35]\n"
"s_waitcnt lgkmcnt(6)\n"
"global_store_dwordx4 v0, v[24:27], s[52:53] offset:2048 sc1\n"
"v_mfma_f32_16x16x32_bf16 v[32:35], a[36:39], a[100:103], v[32:35]\n"
"global_load_dwordx4 a[80:83], v0, s[34:35] offset:0 sc1\n"
"v_mfma_f32_16x16x32_bf16 v[60:63], a[44:47], a[100:103], v[60:63]\n"
"s_barrier\n"
"v_mfma_f32_16x16x32_bf16 v[60:63], a[40:43], a[96:99], v[60:63]\n"
"v_mfma_f32_16x16x32_bf16 v[64:67], a[40:43], a[104:107], v[64:67]\n"
"v_mfma_f32_16x16x32_bf16 v[64:67], a[44:47], a[108:111], v[64:67]\n"
"v_mfma_f32_16x16x32_bf16 v[36:39], a[36:39], a[108:111], v[36:39]\n"
"v_mfma_f32_16x16x32_bf16 v[36:39], a[32:35], a[104:107], v[36:39]\n"
"v_mfma_f32_16x16x32_bf16 v[40:43], a[32:35], a[112:115], v[40:43]\n"
"v_mfma_f32_16x16x32_bf16 v[40:43], a[36:39], a[116:119], v[40:43]\n"
"global_load_dwordx4 a[84:87], v0, s[34:35] offset:1024 sc1\n"
"v_mfma_f32_16x16x32_bf16 v[68:71], a[44:47], a[116:119], v[68:71]\n"
"v_mfma_f32_16x16x32_bf16 v[68:71], a[40:43], a[112:115], v[68:71]\n"
"v_mfma_f32_16x16x32_bf16 v[72:75], a[40:43], a[120:123], v[72:75]\n"
"v_mfma_f32_16x16x32_bf16 v[72:75], a[44:47], a[124:127], v[72:75]\n"
"v_mfma_f32_16x16x32_bf16 v[44:47], a[36:39], a[124:127], v[44:47]\n"
"v_mfma_f32_16x16x32_bf16 v[44:47], a[32:35], a[120:123], v[44:47]\n"
"s_waitcnt lgkmcnt(4)\n"
"v_mfma_f32_16x16x32_bf16 v[48:51], a[32:35], a[128:131], v[48:51]\n"
"v_mfma_f32_16x16x32_bf16 v[48:51], a[36:39], a[132:135], v[48:51]\n"
"global_load_dwordx4 a[88:91], v0, s[34:35] offset:2048 sc1\n"
"v_lshrrev_b32 v16, 4, v0\n"
"v_mfma_f32_16x16x32_bf16 v[76:79], a[44:47], a[132:135], v[76:79]\n"
"v_and_b32 v16, 0x30, v16\n"
"v_mfma_f32_16x16x32_bf16 v[76:79], a[40:43], a[128:131], v[76:79]\n"
"ds_read_b128 v[28:31], v2 offset:23552\n"
"buffer_load_dwordx2 v[18:19], v16, s[44:47], s61 offen offset:0\n"
"v_mfma_f32_16x16x32_bf16 v[80:83], a[40:43], a[136:139], v[80:83]\n"
"buffer_load_dwordx2 v[8:9], off, s[56:59], %14 sc1\n"
"v_mfma_f32_16x16x32_bf16 v[80:83], a[44:47], a[140:143], v[80:83]\n"
"v_mfma_f32_16x16x32_bf16 v[52:55], a[36:39], a[140:143], v[52:55]\n"
"v_mfma_f32_16x16x32_bf16 v[52:55], a[32:35], a[136:139], v[52:55]\n"
"s_waitcnt lgkmcnt(1)\n"
"v_mfma_f32_16x16x32_bf16 v[56:59], a[32:35], a[144:147], v[56:59]\n"
"v_mfma_f32_16x16x32_bf16 v[56:59], a[36:39], a[148:151], v[56:59]\n"
"global_load_dwordx4 a[92:95], v0, s[34:35] offset:3072 sc1\n"
"v_lshrrev_b32 v20, 4, v0\n"
"v_mfma_f32_16x16x32_bf16 v[84:87], a[44:47], a[148:151], v[84:87]\n"
"v_and_b32 v20, 0x30, v20\n"
"v_mfma_f32_16x16x32_bf16 v[84:87], a[40:43], a[144:147], v[84:87]\n"
"buffer_load_dwordx2 v[22:23], v20, s[44:47], s61 offen offset:8\n"
"v_mfma_f32_16x16x32_bf16 a[4:7], a[40:43], a[152:155], a[4:7]\n"
"s_add_u32 s34, s34, 8192\n"
"v_mfma_f32_16x16x32_bf16 a[4:7], a[44:47], a[156:159], a[4:7]\n"
"v_mfma_f32_16x16x32_bf16 a[0:3], a[36:39], a[156:159], a[0:3]\n"
"s_waitcnt lgkmcnt(0)\n"
"global_store_dwordx4 v0, v[28:31], s[52:53] offset:3072 sc1\n"
"v_mfma_f32_16x16x32_bf16 a[0:3], a[32:35], a[152:155], a[0:3]\n"
"s_waitcnt vmcnt(14)\n"
"v_mfma_f32_16x16x32_bf16 v[88:91], a[48:51], a[96:99], v[88:91]\n"
"v_mfma_f32_16x16x32_bf16 v[88:91], a[52:55], a[100:103], v[88:91]\n"
"global_load_dwordx4 a[32:35], v0, s[34:35] offset:-4096 sc1\n"
"v_mfma_f32_16x16x32_bf16 v[116:119], a[60:63], a[100:103], v[116:119]\n"
"v_mfma_f32_16x16x32_bf16 v[116:119], a[56:59], a[96:99], v[116:119]\n"
"v_mfma_f32_16x16x32_bf16 v[120:123], a[56:59], a[104:107], v[120:123]\n"
"v_mfma_f32_16x16x32_bf16 v[120:123], a[60:63], a[108:111], v[120:123]\n"
"v_mfma_f32_16x16x32_bf16 v[92:95], a[52:55], a[108:111], v[92:95]\n"
"v_mfma_f32_16x16x32_bf16 v[92:95], a[48:51], a[104:107], v[92:95]\n"
"v_mfma_f32_16x16x32_bf16 v[96:99], a[48:51], a[112:115], v[96:99]\n"
"v_mfma_f32_16x16x32_bf16 v[96:99], a[52:55], a[116:119], v[96:99]\n"
"global_load_dwordx4 a[36:39], v0, s[34:35] offset:-3072 sc1\n"
"v_mfma_f32_16x16x32_bf16 v[124:127], a[60:63], a[116:119], v[124:127]\n"
"v_mfma_f32_16x16x32_bf16 v[124:127], a[56:59], a[112:115], v[124:127]\n"
"v_mfma_f32_16x16x32_bf16 v[128:131], a[56:59], a[120:123], v[128:131]\n"
"v_mfma_f32_16x16x32_bf16 v[128:131], a[60:63], a[124:127], v[128:131]\n"
"v_mfma_f32_16x16x32_bf16 v[100:103], a[52:55], a[124:127], v[100:103]\n"
"v_mfma_f32_16x16x32_bf16 v[100:103], a[48:51], a[120:123], v[100:103]\n"
"v_mfma_f32_16x16x32_bf16 v[104:107], a[48:51], a[128:131], v[104:107]\n"
"ds_read_b128 v[24:27], v2 offset:24576\n"
"v_mfma_f32_16x16x32_bf16 v[104:107], a[52:55], a[132:135], v[104:107]\n"
"global_load_dwordx4 a[40:43], v0, s[34:35] offset:-2048 sc1\n"
"v_mfma_f32_16x16x32_bf16 v[132:135], a[60:63], a[132:135], v[132:135]\n"
"v_mfma_f32_16x16x32_bf16 v[132:135], a[56:59], a[128:131], v[132:135]\n"
"v_mfma_f32_16x16x32_bf16 v[136:139], a[56:59], a[136:139], v[136:139]\n"
"v_mfma_f32_16x16x32_bf16 v[136:139], a[60:63], a[140:143], v[136:139]\n"
"v_mfma_f32_16x16x32_bf16 v[108:111], a[52:55], a[140:143], v[108:111]\n"
"v_mfma_f32_16x16x32_bf16 v[108:111], a[48:51], a[136:139], v[108:111]\n"
"v_mfma_f32_16x16x32_bf16 v[112:115], a[48:51], a[144:147], v[112:115]\n"
"v_mfma_f32_16x16x32_bf16 v[112:115], a[52:55], a[148:151], v[112:115]\n"
"global_load_dwordx4 a[44:47], v0, s[34:35] offset:-1024 sc1\n"
"v_mfma_f32_16x16x32_bf16 v[140:143], a[60:63], a[148:151], v[140:143]\n"
"v_mfma_f32_16x16x32_bf16 v[140:143], a[56:59], a[144:147], v[140:143]\n"
"v_mfma_f32_16x16x32_bf16 a[12:15], a[56:59], a[152:155], a[12:15]\n"
"s_waitcnt lgkmcnt(0)\n"
"global_store_dwordx4 v0, v[24:27], s[54:55] offset:-4096 sc1\n"
"v_mfma_f32_16x16x32_bf16 a[12:15], a[60:63], a[156:159], a[12:15]\n"
"v_mfma_f32_16x16x32_bf16 a[8:11], a[52:55], a[156:159], a[8:11]\n"
"v_mfma_f32_16x16x32_bf16 a[8:11], a[48:51], a[152:155], a[8:11]\n"
"s_waitcnt vmcnt(14)\n"
"v_mfma_f32_16x16x32_bf16 v[144:147], a[64:67], a[96:99], v[144:147]\n"
"v_mfma_f32_16x16x32_bf16 v[144:147], a[68:71], a[100:103], v[144:147]\n"
"global_load_dwordx4 a[48:51], v0, s[34:35] offset:0 sc1\n"
"v_mfma_f32_16x16x32_bf16 v[172:175], a[76:79], a[100:103], v[172:175]\n"
"v_mfma_f32_16x16x32_bf16 v[172:175], a[72:75], a[96:99], v[172:175]\n"
"v_mfma_f32_16x16x32_bf16 v[176:179], a[72:75], a[104:107], v[176:179]\n"
"v_mfma_f32_16x16x32_bf16 v[176:179], a[76:79], a[108:111], v[176:179]\n"
"v_mfma_f32_16x16x32_bf16 v[148:151], a[68:71], a[108:111], v[148:151]\n"
"v_mfma_f32_16x16x32_bf16 v[148:151], a[64:67], a[104:107], v[148:151]\n"
"v_mfma_f32_16x16x32_bf16 v[152:155], a[64:67], a[112:115], v[152:155]\n"
"v_mfma_f32_16x16x32_bf16 v[152:155], a[68:71], a[116:119], v[152:155]\n"
"global_load_dwordx4 a[52:55], v0, s[34:35] offset:1024 sc1\n"
"v_mfma_f32_16x16x32_bf16 v[180:183], a[76:79], a[116:119], v[180:183]\n"
"v_mfma_f32_16x16x32_bf16 v[180:183], a[72:75], a[112:115], v[180:183]\n"
"v_mfma_f32_16x16x32_bf16 v[184:187], a[72:75], a[120:123], v[184:187]\n"
"v_mfma_f32_16x16x32_bf16 v[184:187], a[76:79], a[124:127], v[184:187]\n"
"v_mfma_f32_16x16x32_bf16 v[156:159], a[68:71], a[124:127], v[156:159]\n"
"v_mfma_f32_16x16x32_bf16 v[156:159], a[64:67], a[120:123], v[156:159]\n"
"ds_read_b128 v[28:31], v2 offset:26624\n"
"v_mfma_f32_16x16x32_bf16 v[160:163], a[64:67], a[128:131], v[160:163]\n"
"v_mfma_f32_16x16x32_bf16 v[160:163], a[68:71], a[132:135], v[160:163]\n"
"global_load_dwordx4 a[56:59], v0, s[34:35] offset:2048 sc1\n"
"v_mfma_f32_16x16x32_bf16 v[188:191], a[76:79], a[132:135], v[188:191]\n"
"v_mfma_f32_16x16x32_bf16 v[188:191], a[72:75], a[128:131], v[188:191]\n"
"v_mfma_f32_16x16x32_bf16 v[192:195], a[72:75], a[136:139], v[192:195]\n"
"v_mfma_f32_16x16x32_bf16 v[192:195], a[76:79], a[140:143], v[192:195]\n"
"v_mfma_f32_16x16x32_bf16 v[164:167], a[68:71], a[140:143], v[164:167]\n"
"v_mfma_f32_16x16x32_bf16 v[164:167], a[64:67], a[136:139], v[164:167]\n"
"v_mfma_f32_16x16x32_bf16 v[168:171], a[64:67], a[144:147], v[168:171]\n"
"v_mfma_f32_16x16x32_bf16 v[168:171], a[68:71], a[148:151], v[168:171]\n"
"global_load_dwordx4 a[60:63], v0, s[34:35] offset:3072 sc1\n"
"v_mfma_f32_16x16x32_bf16 v[196:199], a[76:79], a[148:151], v[196:199]\n"
"s_waitcnt lgkmcnt(0)\n"
"global_store_dwordx4 v0, v[28:31], s[54:55] offset:-2048 sc1\n"
"v_mfma_f32_16x16x32_bf16 v[196:199], a[72:75], a[144:147], v[196:199]\n"
"v_mfma_f32_16x16x32_bf16 a[20:23], a[72:75], a[152:155], a[20:23]\n"
"s_add_u32 s34, s34, 8192\n"
"v_mfma_f32_16x16x32_bf16 a[20:23], a[76:79], a[156:159], a[20:23]\n"
"v_mfma_f32_16x16x32_bf16 a[16:19], a[68:71], a[156:159], a[16:19]\n"
"v_mfma_f32_16x16x32_bf16 a[16:19], a[64:67], a[152:155], a[16:19]\n"
"s_waitcnt vmcnt(12)\n"
"v_mfma_f32_16x16x32_bf16 v[200:203], a[80:83], a[96:99], v[200:203]\n"
"v_mfma_f32_16x16x32_bf16 v[200:203], a[84:87], a[100:103], v[200:203]\n"
"global_load_dwordx4 a[64:67], v0, s[34:35] offset:-4096 sc1\n"
"v_mfma_f32_16x16x32_bf16 v[228:231], a[92:95], a[100:103], v[228:231]\n"
"ds_read_b128 a[100:103], v3 offset:17408\n"
"v_mfma_f32_16x16x32_bf16 v[228:231], a[88:91], a[96:99], v[228:231]\n"
"ds_read_b128 a[96:99], v3 offset:16384\n"
"v_mfma_f32_16x16x32_bf16 v[232:235], a[88:91], a[104:107], v[232:235]\n"
"v_mfma_f32_16x16x32_bf16 v[232:235], a[92:95], a[108:111], v[232:235]\n"
"v_mfma_f32_16x16x32_bf16 v[204:207], a[84:87], a[108:111], v[204:207]\n"
"ds_read_b128 a[108:111], v3 offset:19456\n"
"v_mfma_f32_16x16x32_bf16 v[204:207], a[80:83], a[104:107], v[204:207]\n"
"ds_read_b128 a[104:107], v3 offset:18432\n"
"v_mfma_f32_16x16x32_bf16 v[208:211], a[80:83], a[112:115], v[208:211]\n"
"v_mfma_f32_16x16x32_bf16 v[208:211], a[84:87], a[116:119], v[208:211]\n"
"L_st6226:\n"
"s_cbranch_execz L_st8516\n"
"global_load_dwordx4 a[68:71], v0, s[34:35] offset:-3072 sc1\n"
"v_mfma_f32_16x16x32_bf16 v[236:239], a[92:95], a[116:119], v[236:239]\n"
"ds_read_b128 a[116:119], v3 offset:21504\n"
"v_mfma_f32_16x16x32_bf16 v[236:239], a[88:91], a[112:115], v[236:239]\n"
"ds_read_b128 a[112:115], v3 offset:20480\n"
"v_mfma_f32_16x16x32_bf16 v[240:243], a[88:91], a[120:123], v[240:243]\n"
"v_mfma_f32_16x16x32_bf16 v[240:243], a[92:95], a[124:127], v[240:243]\n"
"ds_read_b128 v[24:27], v2 offset:29696\n"
"v_mfma_f32_16x16x32_bf16 v[212:215], a[84:87], a[124:127], v[212:215]\n"
"ds_read_b128 a[124:127], v3 offset:23552\n"
"v_mfma_f32_16x16x32_bf16 v[212:215], a[80:83], a[120:123], v[212:215]\n"
"ds_read_b128 a[120:123], v3 offset:22528\n"
"v_mfma_f32_16x16x32_bf16 v[216:219], a[80:83], a[128:131], v[216:219]\n"
"v_mfma_f32_16x16x32_bf16 v[216:219], a[84:87], a[132:135], v[216:219]\n"
"global_load_dwordx4 a[72:75], v0, s[34:35] offset:-2048 sc1\n"
"v_mfma_f32_16x16x32_bf16 v[244:247], a[92:95], a[132:135], v[244:247]\n"
"ds_read_b128 a[132:135], v3 offset:25600\n"
"v_mfma_f32_16x16x32_bf16 v[244:247], a[88:91], a[128:131], v[244:247]\n"
"ds_read_b128 a[128:131], v3 offset:24576\n"
"v_mfma_f32_16x16x32_bf16 v[248:251], a[88:91], a[136:139], v[248:251]\n"
"v_subrev_u32 v8, s65, v8\n"
"v_mfma_f32_16x16x32_bf16 v[248:251], a[92:95], a[140:143], v[248:251]\n"
"v_xor_b32 v9, s79, v9\n"
"v_mfma_f32_16x16x32_bf16 v[220:223], a[84:87], a[140:143], v[220:223]\n"
"ds_read_b128 a[140:143], v3 offset:27648\n"
"v_or_b32 v8, v8, v9\n"
"v_mfma_f32_16x16x32_bf16 v[220:223], a[80:83], a[136:139], v[220:223]\n"
"ds_read_b128 a[136:139], v3 offset:26624\n"
"v_cmp_lt_u32 vcc, 15, v8\n"
"v_mfma_f32_16x16x32_bf16 v[224:227], a[80:83], a[144:147], v[224:227]\n"
"s_waitcnt lgkmcnt(6)\n"
"global_store_dwordx4 v0, v[24:27], s[54:55] offset:1024 sc1\n"
"s_cbranch_vccnz L_slow0\n"
"L_back0:\n"
"v_mfma_f32_16x16x32_bf16 v[224:227], a[84:87], a[148:151], v[224:227]\n"
"global_load_dwordx4 a[76:79], v0, s[34:35] offset:-1024 sc1\n"
"v_readfirstlane_b32 s66, v9\n"
"v_mfma_f32_16x16x32_bf16 v[252:255], a[92:95], a[148:151], v[252:255]\n"
"ds_read_b128 a[148:151], v3 offset:29696\n"
"v_mfma_f32_16x16x32_bf16 v[252:255], a[88:91], a[144:147], v[252:255]\n"
"s_and_b32 s66, s66, 3\n"
"ds_read_b128 a[144:147], v3 offset:28672\n"
"v_mfma_f32_16x16x32_bf16 a[28:31], a[88:91], a[152:155], a[28:31]\n"
"s_lshl_b32 s69, s66, 13\n"
"v_mfma_f32_16x16x32_bf16 a[28:31], a[92:95], a[156:159], a[28:31]\n"
"s_cmp_eq_u32 s66, 3\n"
"s_cselect_b32 s75, 8192, 0\n"
"v_mfma_f32_16x16x32_bf16 a[24:27], a[84:87], a[156:159], a[24:27]\n"
"s_and_b32 s29, s66, s30\n"
"ds_read_b128 a[156:159], v3 offset:31744\n"
"v_mfma_f32_16x16x32_bf16 a[24:27], a[80:83], a[152:155], a[24:27]\n"
"s_waitcnt vmcnt(11)\n"
"ds_read_b128 a[152:155], v3 offset:30720\n"
"v_mfma_f32_16x16x32_bf16 v[32:35], a[32:35], a[96:99], v[32:35]\n"
"s_xor_b32 s31, s79, s66\n"
"v_mfma_f32_16x16x32_bf16 v[32:35], a[36:39], a[100:103], v[32:35]\n"
"global_load_dwordx4 a[80:83], v0, s[34:35] offset:0 sc1\n"
"v_mfma_f32_16x16x32_bf16 v[60:63], a[44:47], a[100:103], v[60:63]\n"
"v_mfma_f32_16x16x32_bf16 v[60:63], a[40:43], a[96:99], v[60:63]\n"
"v_mfma_f32_16x16x32_bf16 v[64:67], a[40:43], a[104:107], v[64:67]\n"
"v_mfma_f32_16x16x32_bf16 v[64:67], a[44:47], a[108:111], v[64:67]\n"
"v_mfma_f32_16x16x32_bf16 v[36:39], a[36:39], a[108:111], v[36:39]\n"
"s_add_u32 m0, s64, 0\n"
"v_mfma_f32_16x16x32_bf16 v[36:39], a[32:35], a[104:107], v[36:39]\n"
"s_waitcnt lgkmcnt(8)\n"
"v_mfma_f32_16x16x32_bf16 v[40:43], a[32:35], a[112:115], v[40:43]\n"
"v_mfma_f32_16x16x32_bf16 v[40:43], a[36:39], a[116:119], v[40:43]\n"
"global_load_dwordx4 a[84:87], v0, s[34:35] offset:1024 sc1\n"
"v_mfma_f32_16x16x32_bf16 v[68:71], a[44:47], a[116:119], v[68:71]\n"
"v_mfma_f32_16x16x32_bf16 v[68:71], a[40:43], a[112:115], v[68:71]\n"
"v_mfma_f32_16x16x32_bf16 v[72:75], a[40:43], a[120:123], v[72:75]\n"
"v_mfma_f32_16x16x32_bf16 v[72:75], a[44:47], a[124:127], v[72:75]\n"
"v_mfma_f32_16x16x32_bf16 v[44:47], a[36:39], a[124:127], v[44:47]\n"
"v_mfma_f32_16x16x32_bf16 v[44:47], a[32:35], a[120:123], v[44:47]\n"
"s_waitcnt lgkmcnt(4)\n"
"v_mfma_f32_16x16x32_bf16 v[48:51], a[32:35], a[128:131], v[48:51]\n"
"v_mfma_f32_16x16x32_bf16 v[48:51], a[36:39], a[132:135], v[48:51]\n"
"global_load_dwordx4 a[88:91], v0, s[34:35] offset:2048 sc1\n"
"v_mfma_f32_16x16x32_bf16 v[76:79], a[44:47], a[132:135], v[76:79]\n"
"v_mfma_f32_16x16x32_bf16 v[76:79], a[40:43], a[128:131], v[76:79]\n"
"v_mfma_f32_16x16x32_bf16 v[80:83], a[40:43], a[136:139], v[80:83]\n"
"v_mfma_f32_16x16x32_bf16 v[80:83], a[44:47], a[140:143], v[80:83]\n"
"buffer_load_dwordx4 v0, s[48:51], s75 offen offset:0 sc1 lds\n"
"v_mfma_f32_16x16x32_bf16 v[52:55], a[36:39], a[140:143], v[52:55]\n"
"v_mfma_f32_16x16x32_bf16 v[52:55], a[32:35], a[136:139], v[52:55]\n"
"s_waitcnt lgkmcnt(0)\n"
"v_mfma_f32_16x16x32_bf16 v[56:59], a[32:35], a[144:147], v[56:59]\n"
"v_mfma_f32_16x16x32_bf16 v[56:59], a[36:39], a[148:151], v[56:59]\n"
"global_load_dwordx4 a[92:95], v0, s[34:35] offset:3072 sc1\n"
"v_mfma_f32_16x16x32_bf16 v[84:87], a[44:47], a[148:151], v[84:87]\n"
"v_mfma_f32_16x16x32_bf16 v[84:87], a[40:43], a[144:147], v[84:87]\n"
"v_mfma_f32_16x16x32_bf16 a[4:7], a[40:43], a[152:155], a[4:7]\n"
"s_add_u32 s34, s34, 8192\n"
"v_mfma_f32_16x16x32_bf16 a[4:7], a[44:47], a[156:159], a[4:7]\n"
"v_mfma_f32_16x16x32_bf16 a[0:3], a[36:39], a[156:159], a[0:3]\n"
"v_mfma_f32_16x16x32_bf16 a[0:3], a[32:35], a[152:155], a[0:3]\n"
"s_waitcnt vmcnt(11)\n"
"v_mfma_f32_16x16x32_bf16 v[88:91], a[48:51], a[96:99], v[88:91]\n"
"v_mfma_f32_16x16x32_bf16 v[88:91], a[52:55], a[100:103], v[88:91]\n"
"global_load_dwordx4 a[32:35], v0, s[34:35] offset:-4096 sc1\n"
"v_mfma_f32_16x16x32_bf16 v[116:119], a[60:63], a[100:103], v[116:119]\n"
"v_mfma_f32_16x16x32_bf16 v[116:119], a[56:59], a[96:99], v[116:119]\n"
"buffer_load_dwordx4 v0, s[48:51], s75 offen offset:1024 sc1 lds\n"
"v_mfma_f32_16x16x32_bf16 v[120:123], a[56:59], a[104:107], v[120:123]\n"
"v_mfma_f32_16x16x32_bf16 v[120:123], a[60:63], a[108:111], v[120:123]\n"
"v_mfma_f32_16x16x32_bf16 v[92:95], a[52:55], a[108:111], v[92:95]\n"
"v_mfma_f32_16x16x32_bf16 v[92:95], a[48:51], a[104:107], v[92:95]\n"
"v_mfma_f32_16x16x32_bf16 v[96:99], a[48:51], a[112:115], v[96:99]\n"
"v_mfma_f32_16x16x32_bf16 v[96:99], a[52:55], a[116:119], v[96:99]\n"
"global_load_dwordx4 a[36:39], v0, s[34:35] offset:-3072 sc1\n"
"v_mfma_f32_16x16x32_bf16 v[124:127], a[60:63], a[116:119], v[124:127]\n"
"v_mfma_f32_16x16x32_bf16 v[124:127], a[56:59], a[112:115], v[124:127]\n"
"v_mfma_f32_16x16x32_bf16 v[128:131], a[56:59], a[120:123], v[128:131]\n"
"v_mfma_f32_16x16x32_bf16 v[128:131], a[60:63], a[124:127], v[128:131]\n"
"v_mfma_f32_16x16x32_bf16 v[100:103], a[52:55], a[124:127], v[100:103]\n"
"v_mfma_f32_16x16x32_bf16 v[100:103], a[48:51], a[120:123], v[100:103]\n"
"v_mfma_f32_16x16x32_bf16 v[104:107], a[48:51], a[128:131], v[104:107]\n"
"v_mfma_f32_16x16x32_bf16 v[104:107], a[52:55], a[132:135], v[104:107]\n"
"global_load_dwordx4 a[40:43], v0, s[34:35] offset:-2048 sc1\n"
"v_mfma_f32_16x16x32_bf16 v[132:135], a[60:63], a[132:135], v[132:135]\n"
"buffer_load_dwordx4 v0, s[48:51], s75 offen offset:2048 sc1 lds\n"
"v_mfma_f32_16x16x32_bf16 v[132:135], a[56:59], a[128:131], v[132:135]\n"
"v_mfma_f32_16x16x32_bf16 v[136:139], a[56:59], a[136:139], v[136:139]\n"
"v_mfma_f32_16x16x32_bf16 v[136:139], a[60:63], a[140:143], v[136:139]\n"
"v_mfma_f32_16x16x32_bf16 v[108:111], a[52:55], a[140:143], v[108:111]\n"
"v_mfma_f32_16x16x32_bf16 v[108:111], a[48:51], a[136:139], v[108:111]\n"
"v_mfma_f32_16x16x32_bf16 v[112:115], a[48:51], a[144:147], v[112:115]\n"
"v_mfma_f32_16x16x32_bf16 v[112:115], a[52:55], a[148:151], v[112:115]\n"
"global_load_dwordx4 a[44:47], v0, s[34:35] offset:-1024 sc1\n"
"v_mfma_f32_16x16x32_bf16 v[140:143], a[60:63], a[148:151], v[140:143]\n"
"v_mfma_f32_16x16x32_bf16 v[140:143], a[56:59], a[144:147], v[140:143]\n"
"v_mfma_f32_16x16x32_bf16 a[12:15], a[56:59], a[152:155], a[12:15]\n"
"v_mfma_f32_16x16x32_bf16 a[12:15], a[60:63], a[156:159], a[12:15]\n"
"v_mfma_f32_16x16x32_bf16 a[8:11], a[52:55], a[156:159], a[8:11]\n"
"v_mfma_f32_16x16x32_bf16 a[8:11], a[48:51], a[152:155], a[8:11]\n"
"s_waitcnt vmcnt(11)\n"
"v_mfma_f32_16x16x32_bf16 v[144:147], a[64:67], a[96:99], v[144:147]\n"
"buffer_load_dwordx4 v0, s[48:51], s75 offen offset:3072 sc1 lds\n"
"v_mfma_f32_16x16x32_bf16 v[144:147], a[68:71], a[100:103], v[144:147]\n"
"global_load_dwordx4 a[48:51], v0, s[34:35] offset:0 sc1\n"
"v_mfma_f32_16x16x32_bf16 v[172:175], a[76:79], a[100:103], v[172:175]\n"
"v_mfma_f32_16x16x32_bf16 v[172:175], a[72:75], a[96:99], v[172:175]\n"
"v_mfma_f32_16x16x32_bf16 v[176:179], a[72:75], a[104:107], v[176:179]\n"
"v_mfma_f32_16x16x32_bf16 v[176:179], a[76:79], a[108:111], v[176:179]\n"
"v_mfma_f32_16x16x32_bf16 v[148:151], a[68:71], a[108:111], v[148:151]\n"
"v_mfma_f32_16x16x32_bf16 v[148:151], a[64:67], a[104:107], v[148:151]\n"
"v_mfma_f32_16x16x32_bf16 v[152:155], a[64:67], a[112:115], v[152:155]\n"
"v_mfma_f32_16x16x32_bf16 v[152:155], a[68:71], a[116:119], v[152:155]\n"
"global_load_dwordx4 a[52:55], v0, s[34:35] offset:1024 sc1\n"
"v_mfma_f32_16x16x32_bf16 v[180:183], a[76:79], a[116:119], v[180:183]\n"
"v_mfma_f32_16x16x32_bf16 v[180:183], a[72:75], a[112:115], v[180:183]\n"
"v_mfma_f32_16x16x32_bf16 v[184:187], a[72:75], a[120:123], v[184:187]\n"
"v_mfma_f32_16x16x32_bf16 v[184:187], a[76:79], a[124:127], v[184:187]\n"
"v_mfma_f32_16x16x32_bf16 v[156:159], a[68:71], a[124:127], v[156:159]\n"
"v_mfma_f32_16x16x32_bf16 v[156:159], a[64:67], a[120:123], v[156:159]\n"
"s_add_u32 m0, s64, 4096\n"
"v_mfma_f32_16x16x32_bf16 v[160:163], a[64:67], a[128:131], v[160:163]\n"
"v_mfma_f32_16x16x32_bf16 v[160:163], a[68:71], a[132:135], v[160:163]\n"
"global_load_dwordx4 a[56:59], v0, s[34:35] offset:2048 sc1\n"
"v_mfma_f32_16x16x32_bf16 v[188:191], a[76:79], a[132:135], v[188:191]\n"
"v_mfma_f32_16x16x32_bf16 v[188:191], a[72:75], a[128:131], v[188:191]\n"
"v_mfma_f32_16x16x32_bf16 v[192:195], a[72:75], a[136:139], v[192:195]\n"
"v_mfma_f32_16x16x32_bf16 v[192:195], a[76:79], a[140:143], v[192:195]\n"
"v_mfma_f32_16x16x32_bf16 v[164:167], a[68:71], a[140:143], v[164:167]\n"
"v_mfma_f32_16x16x32_bf16 v[164:167], a[64:67], a[136:139], v[164:167]\n"
"v_mfma_f32_16x16x32_bf16 v[168:171], a[64:67], a[144:147], v[168:171]\n"
"v_mfma_f32_16x16x32_bf16 v[168:171], a[68:71], a[148:151], v[168:171]\n"
"global_load_dwordx4 a[60:63], v0, s[34:35] offset:3072 sc1\n"
"v_mfma_f32_16x16x32_bf16 v[196:199], a[76:79], a[148:151], v[196:199]\n"
"v_mfma_f32_16x16x32_bf16 v[196:199], a[72:75], a[144:147], v[196:199]\n"
"v_mfma_f32_16x16x32_bf16 a[20:23], a[72:75], a[152:155], a[20:23]\n"
"s_add_u32 s34, s34, 8192\n"
"v_mfma_f32_16x16x32_bf16 a[20:23], a[76:79], a[156:159], a[20:23]\n"
"v_mfma_f32_16x16x32_bf16 a[16:19], a[68:71], a[156:159], a[16:19]\n"
"buffer_load_dwordx4 v1, s[48:51], s75 offen offset:0 sc1 lds\n"
"v_mfma_f32_16x16x32_bf16 a[16:19], a[64:67], a[152:155], a[16:19]\n"
"s_waitcnt vmcnt(12)\n"
"v_mfma_f32_16x16x32_bf16 v[200:203], a[80:83], a[96:99], v[200:203]\n"
"v_mfma_f32_16x16x32_bf16 v[200:203], a[84:87], a[100:103], v[200:203]\n"
"global_load_dwordx4 a[64:67], v0, s[34:35] offset:-4096 sc1\n"
"s_add_u32 s65, s65, 1\n"
"v_mfma_f32_16x16x32_bf16 v[228:231], a[92:95], a[100:103], v[228:231]\n"
"ds_read_b128 a[100:103], v4 offset:17408\n"
"v_mfma_f32_16x16x32_bf16 v[228:231], a[88:91], a[96:99], v[228:231]\n"
"ds_read_b128 a[96:99], v4 offset:16384\n"
"v_mov_b32 v8, s65\n"
"v_mfma_f32_16x16x32_bf16 v[232:235], a[88:91], a[104:107], v[232:235]\n"
"v_mov_b32 v9, s60\n"
"v_mfma_f32_16x16x32_bf16 v[232:235], a[92:95], a[108:111], v[232:235]\n"
"v_mfma_f32_16x16x32_bf16 v[204:207], a[84:87], a[108:111], v[204:207]\n"
"buffer_store_dwordx2 v[8:9], off, s[56:59], %13 sc1\n"
"ds_read_b128 a[108:111], v4 offset:19456\n"
"v_mfma_f32_16x16x32_bf16 v[204:207], a[80:83], a[104:107], v[204:207]\n"
"ds_read_b128 a[104:107], v4 offset:18432\n"
"v_mfma_f32_16x16x32_bf16 v[208:211], a[80:83], a[112:115], v[208:211]\n"
"v_mfma_f32_16x16x32_bf16 v[208:211], a[84:87], a[116:119], v[208:211]\n"
"global_load_dwordx4 a[68:71], v0, s[34:35] offset:-3072 sc1\n"
"v_mfma_f32_16x16x32_bf16 v[236:239], a[92:95], a[116:119], v[236:239]\n"
"ds_read_b128 a[116:119], v4 offset:21504\n"
"v_mfma_f32_16x16x32_bf16 v[236:239], a[88:91], a[112:115], v[236:239]\n"
"ds_read_b128 a[112:115], v4 offset:20480\n"
"v_mfma_f32_16x16x32_bf16 v[240:243], a[88:91], a[120:123], v[240:243]\n"
"v_mfma_f32_16x16x32_bf16 v[240:243], a[92:95], a[124:127], v[240:243]\n"
"buffer_load_dwordx4 v1, s[48:51], s75 offen offset:1024 sc1 lds\n"
"v_mfma_f32_16x16x32_bf16 v[212:215], a[84:87], a[124:127], v[212:215]\n"
"ds_read_b128 a[124:127], v4 offset:23552\n"
"v_mfma_f32_16x16x32_bf16 v[212:215], a[80:83], a[120:123], v[212:215]\n"
"ds_read_b128 a[120:123], v4 offset:22528\n"
"v_mfma_f32_16x16x32_bf16 v[216:219], a[80:83], a[128:131], v[216:219]\n"
"v_mfma_f32_16x16x32_bf16 v[216:219], a[84:87], a[132:135], v[216:219]\n"
"global_load_dwordx4 a[72:75], v0, s[34:35] offset:-2048 sc1\n"
"v_mfma_f32_16x16x32_bf16 v[244:247], a[92:95], a[132:135], v[244:247]\n"
"ds_read_b128 a[132:135], v4 offset:25600\n"
"v_mfma_f32_16x16x32_bf16 v[244:247], a[88:91], a[128:131], v[244:247]\n"
"ds_read_b128 a[128:131], v4 offset:24576\n"
"v_mfma_f32_16x16x32_bf16 v[248:251], a[88:91], a[136:139], v[248:251]\n"
"v_mfma_f32_16x16x32_bf16 v[248:251], a[92:95], a[140:143], v[248:251]\n"
"v_mfma_f32_16x16x32_bf16 v[220:223], a[84:87], a[140:143], v[220:223]\n"
"ds_read_b128 a[140:143], v4 offset:27648\n"
"v_mfma_f32_16x16x32_bf16 v[220:223], a[80:83], a[136:139], v[220:223]\n"
"ds_read_b128 a[136:139], v4 offset:26624\n"
"v_mfma_f32_16x16x32_bf16 v[224:227], a[80:83], a[144:147], v[224:227]\n"
"v_mfma_f32_16x16x32_bf16 v[224:227], a[84:87], a[148:151], v[224:227]\n"
"global_load_dwordx4 a[76:79], v0, s[34:35] offset:-1024 sc1\n"
"v_mfma_f32_16x16x32_bf16 v[252:255], a[92:95], a[148:151], v[252:255]\n"
"ds_read_b128 a[148:151], v4 offset:29696\n"
"v_mfma_f32_16x16x32_bf16 v[252:255], a[88:91], a[144:147], v[252:255]\n"
"ds_read_b128 a[144:147], v4 offset:28672\n"
"v_mfma_f32_16x16x32_bf16 a[28:31], a[88:91], a[152:155], a[28:31]\n"
"buffer_load_dwordx4 v1, s[48:51], s75 offen offset:2048 sc1 lds\n"
"v_mfma_f32_16x16x32_bf16 a[28:31], a[92:95], a[156:159], a[28:31]\n"
"v_mfma_f32_16x16x32_bf16 a[24:27], a[84:87], a[156:159], a[24:27]\n"
"ds_read_b128 a[156:159], v4 offset:31744\n"
"v_mfma_f32_16x16x32_bf16 a[24:27], a[80:83], a[152:155], a[24:27]\n"
"s_waitcnt vmcnt(13) lgkmcnt(11)\n"
"ds_read_b128 a[152:155], v4 offset:30720\n"
"v_mfma_f32_16x16x32_bf16 v[32:35], a[32:35], a[96:99], v[32:35]\n"
"v_mfma_f32_16x16x32_bf16 v[32:35], a[36:39], a[100:103], v[32:35]\n"
"global_load_dwordx4 a[80:83], v0, s[34:35] offset:0 sc1\n"
"v_mfma_f32_16x16x32_bf16 v[60:63], a[44:47], a[100:103], v[60:63]\n"
"v_mfma_f32_16x16x32_bf16 v[60:63], a[40:43], a[96:99], v[60:63]\n"
"v_mfma_f32_16x16x32_bf16 v[64:67], a[40:43], a[104:107], v[64:67]\n"
"v_mfma_f32_16x16x32_bf16 v[64:67], a[44:47], a[108:111], v[64:67]\n"
"v_mfma_f32_16x16x32_bf16 v[36:39], a[36:39], a[108:111], v[36:39]\n"
"v_mfma_f32_16x16x32_bf16 v[36:39], a[32:35], a[104:107], v[36:39]\n"
"s_waitcnt lgkmcnt(8)\n"
"v_mfma_f32_16x16x32_bf16 v[40:43], a[32:35], a[112:115], v[40:43]\n"
"v_mfma_f32_16x16x32_bf16 v[40:43], a[36:39], a[116:119], v[40:43]\n"
"global_load_dwordx4 a[84:87], v0, s[34:35] offset:1024 sc1\n"
"v_mfma_f32_16x16x32_bf16 v[68:71], a[44:47], a[116:119], v[68:71]\n"
"buffer_load_dwordx4 v1, s[48:51], s75 offen offset:3072 sc1 lds\n"
"v_mfma_f32_16x16x32_bf16 v[68:71], a[40:43], a[112:115], v[68:71]\n"
"v_mfma_f32_16x16x32_bf16 v[72:75], a[40:43], a[120:123], v[72:75]\n"
"v_mfma_f32_16x16x32_bf16 v[72:75], a[44:47], a[124:127], v[72:75]\n"
"v_mfma_f32_16x16x32_bf16 v[44:47], a[36:39], a[124:127], v[44:47]\n"
"v_mfma_f32_16x16x32_bf16 v[44:47], a[32:35], a[120:123], v[44:47]\n"
"s_waitcnt lgkmcnt(4)\n"
"v_mfma_f32_16x16x32_bf16 v[48:51], a[32:35], a[128:131], v[48:51]\n"
"v_mfma_f32_16x16x32_bf16 v[48:51], a[36:39], a[132:135], v[48:51]\n"
"global_load_dwordx4 a[88:91], v0, s[34:35] offset:2048 sc1\n"
"v_mfma_f32_16x16x32_bf16 v[76:79], a[44:47], a[132:135], v[76:79]\n"
"v_mfma_f32_16x16x32_bf16 v[76:79], a[40:43], a[128:131], v[76:79]\n"
"v_mfma_f32_16x16x32_bf16 v[80:83], a[40:43], a[136:139], v[80:83]\n"
"v_mfma_f32_16x16x32_bf16 v[80:83], a[44:47], a[140:143], v[80:83]\n"
"v_mfma_f32_16x16x32_bf16 v[52:55], a[36:39], a[140:143], v[52:55]\n"
"v_mfma_f32_16x16x32_bf16 v[52:55], a[32:35], a[136:139], v[52:55]\n"
"s_waitcnt lgkmcnt(0)\n"
"v_mfma_f32_16x16x32_bf16 v[56:59], a[32:35], a[144:147], v[56:59]\n"
"v_mfma_f32_16x16x32_bf16 v[56:59], a[36:39], a[148:151], v[56:59]\n"
"s_add_u32 m0, s64, 8192\n"
"global_load_dwordx4 a[92:95], v0, s[34:35] offset:3072 sc1\n"
"v_mfma_f32_16x16x32_bf16 v[84:87], a[44:47], a[148:151], v[84:87]\n"
"v_mfma_f32_16x16x32_bf16 v[84:87], a[40:43], a[144:147], v[84:87]\n"
"v_mfma_f32_16x16x32_bf16 a[4:7], a[40:43], a[152:155], a[4:7]\n"
"s_add_u32 s34, s34, 8192\n"
"v_mfma_f32_16x16x32_bf16 a[4:7], a[44:47], a[156:159], a[4:7]\n"
"v_mfma_f32_16x16x32_bf16 a[0:3], a[36:39], a[156:159], a[0:3]\n"
"v_mfma_f32_16x16x32_bf16 a[0:3], a[32:35], a[152:155], a[0:3]\n"
"s_waitcnt vmcnt(13)\n"
"v_mfma_f32_16x16x32_bf16 v[88:91], a[48:51], a[96:99], v[88:91]\n"
"v_mfma_f32_16x16x32_bf16 v[88:91], a[52:55], a[100:103], v[88:91]\n"
"global_load_dwordx4 a[32:35], v0, s[34:35] offset:-4096 sc1\n"
"v_mfma_f32_16x16x32_bf16 v[116:119], a[60:63], a[100:103], v[116:119]\n"
"v_mfma_f32_16x16x32_bf16 v[116:119], a[56:59], a[96:99], v[116:119]\n"
"v_mfma_f32_16x16x32_bf16 v[120:123], a[56:59], a[104:107], v[120:123]\n"
"v_mfma_f32_16x16x32_bf16 v[120:123], a[60:63], a[108:111], v[120:123]\n"
"v_mfma_f32_16x16x32_bf16 v[92:95], a[52:55], a[108:111], v[92:95]\n"
"v_mfma_f32_16x16x32_bf16 v[92:95], a[48:51], a[104:107], v[92:95]\n"
"v_mfma_f32_16x16x32_bf16 v[96:99], a[48:51], a[112:115], v[96:99]\n"
"buffer_load_dwordx4 v0, s[48:51], s69 offen offset:0 sc1 lds\n"
"v_mfma_f32_16x16x32_bf16 v[96:99], a[52:55], a[116:119], v[96:99]\n"
"global_load_dwordx4 a[36:39], v0, s[34:35] offset:-3072 sc1\n"
"v_mfma_f32_16x16x32_bf16 v[124:127], a[60:63], a[116:119], v[124:127]\n"
"v_mfma_f32_16x16x32_bf16 v[124:127], a[56:59], a[112:115], v[124:127]\n"
"v_mfma_f32_16x16x32_bf16 v[128:131], a[56:59], a[120:123], v[128:131]\n"
"v_mfma_f32_16x16x32_bf16 v[128:131], a[60:63], a[124:127], v[128:131]\n"
"v_mfma_f32_16x16x32_bf16 v[100:103], a[52:55], a[124:127], v[100:103]\n"
"v_mfma_f32_16x16x32_bf16 v[100:103], a[48:51], a[120:123], v[100:103]\n"
"v_mfma_f32_16x16x32_bf16 v[104:107], a[48:51], a[128:131], v[104:107]\n"
"v_mfma_f32_16x16x32_bf16 v[104:107], a[52:55], a[132:135], v[104:107]\n"
"global_load_dwordx4 a[40:43], v0, s[34:35] offset:-2048 sc1\n"
"v_mfma_f32_16x16x32_bf16 v[132:135], a[60:63], a[132:135], v[132:135]\n"
"v_mfma_f32_16x16x32_bf16 v[132:135], a[56:59], a[128:131], v[132:135]\n"
"v_mfma_f32_16x16x32_bf16 v[136:139], a[56:59], a[136:139], v[136:139]\n"
"v_mfma_f32_16x16x32_bf16 v[136:139], a[60:63], a[140:143], v[136:139]\n"
"v_mfma_f32_16x16x32_bf16 v[108:111], a[52:55], a[140:143], v[108:111]\n"
"buffer_load_dwordx4 v0, s[48:51], s69 offen offset:1024 sc1 lds\n"
"v_mfma_f32_16x16x32_bf16 v[108:111], a[48:51], a[136:139], v[108:111]\n"
"v_mfma_f32_16x16x32_bf16 v[112:115], a[48:51], a[144:147], v[112:115]\n"
"v_mfma_f32_16x16x32_bf16 v[112:115], a[52:55], a[148:151], v[112:115]\n"
"global_load_dwordx4 a[44:47], v0, s[34:35] offset:-1024 sc1\n"
"v_mfma_f32_16x16x32_bf16 v[140:143], a[60:63], a[148:151], v[140:143]\n"
"v_mfma_f32_16x16x32_bf16 v[140:143], a[56:59], a[144:147], v[140:143]\n"
"v_mfma_f32_16x16x32_bf16 a[12:15], a[56:59], a[152:155], a[12:15]\n"
"v_mfma_f32_16x16x32_bf16 a[12:15], a[60:63], a[156:159], a[12:15]\n"
"v_mfma_f32_16x16x32_bf16 a[8:11], a[52:55], a[156:159], a[8:11]\n"
"v_mfma_f32_16x16x32_bf16 a[8:11], a[48:51], a[152:155], a[8:11]\n"
"s_waitcnt vmcnt(12)\n"
"v_mfma_f32_16x16x32_bf16 v[144:147], a[64:67], a[96:99], v[144:147]\n"
"v_mfma_f32_16x16x32_bf16 v[144:147], a[68:71], a[100:103], v[144:147]\n"
"global_load_dwordx4 a[48:51], v0, s[34:35] offset:0 sc1\n"
"v_mfma_f32_16x16x32_bf16 v[172:175], a[76:79], a[100:103], v[172:175]\n"
"v_mfma_f32_16x16x32_bf16 v[172:175], a[72:75], a[96:99], v[172:175]\n"
"v_mfma_f32_16x16x32_bf16 v[176:179], a[72:75], a[104:107], v[176:179]\n"
"v_mfma_f32_16x16x32_bf16 v[176:179], a[76:79], a[108:111], v[176:179]\n"
"buffer_load_dwordx4 v0, s[48:51], s69 offen offset:2048 sc1 lds\n"
"v_mfma_f32_16x16x32_bf16 v[148:151], a[68:71], a[108:111], v[148:151]\n"
"v_mfma_f32_16x16x32_bf16 v[148:151], a[64:67], a[104:107], v[148:151]\n"
"v_mfma_f32_16x16x32_bf16 v[152:155], a[64:67], a[112:115], v[152:155]\n"
"v_lshlrev_b32 v16, 16, v18\n"
"v_mfma_f32_16x16x32_bf16 v[152:155], a[68:71], a[116:119], v[152:155]\n"
"global_load_dwordx4 a[52:55], v0, s[34:35] offset:1024 sc1\n"
"v_and_b32 v17, s63, v18\n"
"v_mfma_f32_16x16x32_bf16 v[180:183], a[76:79], a[116:119], v[180:183]\n"
"v_lshlrev_b32 v18, 16, v19\n"
"v_mfma_f32_16x16x32_bf16 v[180:183], a[72:75], a[112:115], v[180:183]\n"
"v_and_b32 v19, s63, v19\n"
"v_mfma_f32_16x16x32_bf16 v[184:187], a[72:75], a[120:123], v[184:187]\n"
"v_add_f32 v32, v32, v16\n"
"v_mfma_f32_16x16x32_bf16 v[184:187], a[76:79], a[124:127], v[184:187]\n"
"v_add_f32 v33, v33, v17\n"
"v_add_f32 v34, v34, v18\n"
"v_mfma_f32_16x16x32_bf16 v[156:159], a[68:71], a[124:127], v[156:159]\n"
"v_add_f32 v35, v35, v19\n"
"v_mfma_f32_16x16x32_bf16 v[156:159], a[64:67], a[120:123], v[156:159]\n"
"v_add_f32 v36, v36, v16\n"
"v_mfma_f32_16x16x32_bf16 v[160:163], a[64:67], a[128:131], v[160:163]\n"
"v_add_f32 v37, v37, v17\n"
"v_mfma_f32_16x16x32_bf16 v[160:163], a[68:71], a[132:135], v[160:163]\n"
"global_load_dwordx4 a[56:59], v0, s[34:35] offset:2048 sc1\n"
"v_add_f32 v38, v38, v18\n"
"v_mfma_f32_16x16x32_bf16 v[188:191], a[76:79], a[132:135], v[188:191]\n"
"v_add_f32 v39, v39, v19\n"
"v_mfma_f32_16x16x32_bf16 v[188:191], a[72:75], a[128:131], v[188:191]\n"
"v_add_f32 v40, v40, v16\n"
"v_add_f32 v41, v41, v17\n"
"v_mfma_f32_16x16x32_bf16 v[192:195], a[72:75], a[136:139], v[192:195]\n"
"buffer_load_dwordx4 v0, s[48:51], s69 offen offset:3072 sc1 lds\n"
"v_mfma_f32_16x16x32_bf16 v[192:195], a[76:79], a[140:143], v[192:195]\n"
"v_add_f32 v42, v42, v18\n"
"v_mfma_f32_16x16x32_bf16 v[164:167], a[68:71], a[140:143], v[164:167]\n"
"v_add_f32 v43, v43, v19\n"
"v_mfma_f32_16x16x32_bf16 v[164:167], a[64:67], a[136:139], v[164:167]\n"
"v_add_f32 v44, v44, v16\n"
"v_add_f32 v45, v45, v17\n"
"v_mfma_f32_16x16x32_bf16 v[168:171], a[64:67], a[144:147], v[168:171]\n"
"v_add_f32 v46, v46, v18\n"
"v_mfma_f32_16x16x32_bf16 v[168:171], a[68:71], a[148:151], v[168:171]\n"
"global_load_dwordx4 a[60:63], v0, s[34:35] offset:3072 sc1\n"
"v_add_f32 v47, v47, v19\n"
"v_mfma_f32_16x16x32_bf16 v[196:199], a[76:79], a[148:151], v[196:199]\n"
"v_add_f32 v48, v48, v16\n"
"v_mfma_f32_16x16x32_bf16 v[196:199], a[72:75], a[144:147], v[196:199]\n"
"v_add_f32 v49, v49, v17\n"
"v_mfma_f32_16x16x32_bf16 a[20:23], a[72:75], a[152:155], a[20:23]\n"
"s_add_u32 s34, s34, 8192\n"
"v_add_f32 v50, v50, v18\n"
"v_mfma_f32_16x16x32_bf16 a[20:23], a[76:79], a[156:159], a[20:23]\n"
"v_add_f32 v51, v51, v19\n"
"v_mfma_f32_16x16x32_bf16 a[16:19], a[68:71], a[156:159], a[16:19]\n"
"v_add_f32 v52, v52, v16\n"
"v_mfma_f32_16x16x32_bf16 a[16:19], a[64:67], a[152:155], a[16:19]\n"
"s_waitcnt vmcnt(12)\n"
"v_add_f32 v53, v53, v17\n"
"v_mfma_f32_16x16x32_bf16 v[200:203], a[80:83], a[96:99], v[200:203]\n"
"v_add_f32 v54, v54, v18\n"
"v_add_f32 v55, v55, v19\n"
"v_mfma_f32_16x16x32_bf16 v[200:203], a[84:87], a[100:103], v[200:203]\n"
"global_load_dwordx4 a[64:67], v0, s[34:35] offset:-4096 sc1\n"
"v_add_f32 v56, v56, v16\n"
"v_mfma_f32_16x16x32_bf16 v[228:231], a[92:95], a[100:103], v[228:231]\n"
"s_add_u32 m0, s64, 12288\n"
"ds_read_b128 a[100:103], v5 offset:17408\n"
"v_add_f32 v57, v57, v17\n"
"v_mfma_f32_16x16x32_bf16 v[228:231], a[88:91], a[96:99], v[228:231]\n"
"ds_read_b128 a[96:99], v5 offset:16384\n"
"v_add_f32 v58, v58, v18\n"
"v_mfma_f32_16x16x32_bf16 v[232:235], a[88:91], a[104:107], v[232:235]\n"
"v_add_f32 v59, v59, v19\n"
"v_mfma_f32_16x16x32_bf16 v[232:235], a[92:95], a[108:111], v[232:235]\n"
"v_accvgpr_read_b32 v12, a0\n"
"v_mfma_f32_16x16x32_bf16 v[204:207], a[84:87], a[108:111], v[204:207]\n"
"ds_read_b128 a[108:111], v5 offset:19456\n"
"v_accvgpr_read_b32 v13, a1\n"
"v_mfma_f32_16x16x32_bf16 v[204:207], a[80:83], a[104:107], v[204:207]\n"
"ds_read_b128 a[104:107], v5 offset:18432\n"
"v_accvgpr_read_b32 v14, a2\n"
"v_mfma_f32_16x16x32_bf16 v[208:211], a[80:83], a[112:115], v[208:211]\n"
"v_accvgpr_read_b32 v15, a3\n"
"v_mfma_f32_16x16x32_bf16 v[208:211], a[84:87], a[116:119], v[208:211]\n"
"global_load_dwordx4 a[68:71], v0, s[34:35] offset:-3072 sc1\n"
"v_add_f32 v12, v12, v16\n"
"v_mfma_f32_16x16x32_bf16 v[236:239], a[92:95], a[116:119], v[236:239]\n"
"ds_read_b128 a[116:119], v5 offset:21504\n"
"v_add_f32 v13, v13, v17\n"
"v_mfma_f32_16x16x32_bf16 v[236:239], a[88:91], a[112:115], v[236:239]\n"
"ds_read_b128 a[112:115], v5 offset:20480\n"
"v_add_f32 v14, v14, v18\n"
"v_mfma_f32_16x16x32_bf16 v[240:243], a[88:91], a[120:123], v[240:243]\n"
"v_add_f32 v15, v15, v19\n"
"v_accvgpr_write_b32 a0, v12\n"
"v_mfma_f32_16x16x32_bf16 v[240:243], a[92:95], a[124:127], v[240:243]\n"
"v_accvgpr_write_b32 a1, v13\n"
"v_mfma_f32_16x16x32_bf16 v[212:215], a[84:87], a[124:127], v[212:215]\n"
"ds_read_b128 a[124:127], v5 offset:23552\n"
"v_accvgpr_write_b32 a2, v14\n"
"v_mfma_f32_16x16x32_bf16 v[212:215], a[80:83], a[120:123], v[212:215]\n"
"ds_read_b128 a[120:123], v5 offset:22528\n"
"v_accvgpr_write_b32 a3, v15\n"
"v_mfma_f32_16x16x32_bf16 v[216:219], a[80:83], a[128:131], v[216:219]\n"
"buffer_load_dwordx4 v1, s[48:51], s69 offen offset:0 sc1 lds\n"
"v_mfma_f32_16x16x32_bf16 v[216:219], a[84:87], a[132:135], v[216:219]\n"
"global_load_dwordx4 a[72:75], v0, s[34:35] offset:-2048 sc1\n"
"v_lshrrev_b32 v16, 4, v0\n"
"v_mfma_f32_16x16x32_bf16 v[244:247], a[92:95], a[132:135], v[244:247]\n"
"ds_read_b128 a[132:135], v5 offset:25600\n"
"v_and_b32 v16, 0x30, v16\n"
"v_mfma_f32_16x16x32_bf16 v[244:247], a[88:91], a[128:131], v[244:247]\n"
"ds_read_b128 a[128:131], v5 offset:24576\n"
"buffer_load_dwordx2 v[18:19], v16, s[44:47], s61 offen offset:64\n"
"v_mfma_f32_16x16x32_bf16 v[248:251], a[88:91], a[136:139], v[248:251]\n"
"s_getpc_b64 s[96:97]\n"
"L_c3_:\n"
"s_add_u32 s96, s96, L_last-L_c3_\n"
"s_addc_u32 s97, s97, 0\n"
"v_mfma_f32_16x16x32_bf16 v[248:251], a[92:95], a[140:143], v[248:251]\n"
"s_lshr_b32 s66, %26, 3\n"
"v_mfma_f32_16x16x32_bf16 v[220:223], a[84:87], a[140:143], v[220:223]\n"
"s_lshl2_add_u32 s66, s66, %16\n"
"ds_read_b128 a[140:143], v5 offset:27648\n"
"v_mfma_f32_16x16x32_bf16 v[220:223], a[80:83], a[136:139], v[220:223]\n"
"ds_read_b128 a[136:139], v5 offset:26624\n"
"v_mfma_f32_16x16x32_bf16 v[224:227], a[80:83], a[144:147], v[224:227]\n"
"s_mul_i32 s66, s66, 256\n"
"v_mfma_f32_16x16x32_bf16 v[224:227], a[84:87], a[148:151], v[224:227]\n"
"global_load_dwordx4 a[76:79], v0, s[34:35] offset:-1024 sc1\n"
"v_mfma_f32_16x16x32_bf16 v[252:255], a[92:95], a[148:151], v[252:255]\n"
"ds_read_b128 a[148:151], v5 offset:29696\n"
"v_mfma_f32_16x16x32_bf16 v[252:255], a[88:91], a[144:147], v[252:255]\n"
"s_min_u32 s66, s66, L_headF-L_last-256\n"
"ds_read_b128 a[144:147], v5 offset:28672\n"
"v_mfma_f32_16x16x32_bf16 a[28:31], a[88:91], a[152:155], a[28:31]\n"
"v_mfma_f32_16x16x32_bf16 a[28:31], a[92:95], a[156:159], a[28:31]\n"
"v_min_u32 v11, 16, v0\n"
"v_mfma_f32_16x16x32_bf16 a[24:27], a[84:87], a[156:159], a[24:27]\n"
"ds_read_b128 a[156:159], v5 offset:31744\n"
"v_mfma_f32_16x16x32_bf16 a[24:27], a[80:83], a[152:155], a[24:27]\n"
"s_waitcnt vmcnt(12) lgkmcnt(11)\n"
"ds_read_b128 a[152:155], v5 offset:30720\n"
"v_mfma_f32_16x16x32_bf16 v[32:35], a[32:35], a[96:99], v[32:35]\n"
"buffer_load_dwordx4 v1, s[48:51], s69 offen offset:1024 sc1 lds\n"
"v_mfma_f32_16x16x32_bf16 v[32:35], a[36:39], a[100:103], v[32:35]\n"
"global_load_dwordx4 a[80:83], v0, s[34:35] offset:0 sc1\n"
"v_mfma_f32_16x16x32_bf16 v[60:63], a[44:47], a[100:103], v[60:63]\n"
"v_lshl_add_u32 v11, v11, 3, s66\n"
"v_mfma_f32_16x16x32_bf16 v[60:63], a[40:43], a[96:99], v[60:63]\n"
"global_load_dword v10, v11, s[96:97]\n"
"v_mfma_f32_16x16x32_bf16 v[64:67], a[40:43], a[104:107], v[64:67]\n"
"v_mfma_f32_16x16x32_bf16 v[64:67], a[44:47], a[108:111], v[64:67]\n"
"v_mfma_f32_16x16x32_bf16 v[36:39], a[36:39], a[108:111], v[36:39]\n"
"v_mfma_f32_16x16x32_bf16 v[36:39], a[32:35], a[104:107], v[36:39]\n"
"s_waitcnt lgkmcnt(8)\n"
"v_mfma_f32_16x16x32_bf16 v[40:43], a[32:35], a[112:115], v[40:43]\n"
"v_mfma_f32_16x16x32_bf16 v[40:43], a[36:39], a[116:119], v[40:43]\n"
"global_load_dwordx4 a[84:87], v0, s[34:35] offset:1024 sc1\n"
"v_mfma_f32_16x16x32_bf16 v[68:71], a[44:47], a[116:119], v[68:71]\n"
"v_mfma_f32_16x16x32_bf16 v[68:71], a[40:43], a[112:115], v[68:71]\n"
"v_mfma_f32_16x16x32_bf16 v[72:75], a[40:43], a[120:123], v[72:75]\n"
"v_mfma_f32_16x16x32_bf16 v[72:75], a[44:47], a[124:127], v[72:75]\n"
"v_mfma_f32_16x16x32_bf16 v[44:47], a[36:39], a[124:127], v[44:47]\n"
"buffer_load_dwordx4 v1, s[48:51], s69 offen offset:2048 sc1 lds\n"
"v_mfma_f32_16x16x32_bf16 v[44:47], a[32:35], a[120:123], v[44:47]\n"
"s_waitcnt lgkmcnt(4)\n"
"v_mfma_f32_16x16x32_bf16 v[48:51], a[32:35], a[128:131], v[48:51]\n"
"v_mfma_f32_16x16x32_bf16 v[48:51], a[36:39], a[132:135], v[48:51]\n"
"global_load_dwordx4 a[88:91], v0, s[34:35] offset:2048 sc1\n"
"v_mfma_f32_16x16x32_bf16 v[76:79], a[44:47], a[132:135], v[76:79]\n"
"v_mfma_f32_16x16x32_bf16 v[76:79], a[40:43], a[128:131], v[76:79]\n"
"v_mfma_f32_16x16x32_bf16 v[80:83], a[40:43], a[136:139], v[80:83]\n"
"v_mfma_f32_16x16x32_bf16 v[80:83], a[44:47], a[140:143], v[80:83]\n"
"v_mfma_f32_16x16x32_bf16 v[52:55], a[36:39], a[140:143], v[52:55]\n"
"v_mfma_f32_16x16x32_bf16 v[52:55], a[32:35], a[136:139], v[52:55]\n"
"s_waitcnt lgkmcnt(0)\n"
"v_mfma_f32_16x16x32_bf16 v[56:59], a[32:35], a[144:147], v[56:59]\n"
"v_mfma_f32_16x16x32_bf16 v[56:59], a[36:39], a[148:151], v[56:59]\n"
"global_load_dwordx4 a[92:95], v0, s[34:35] offset:3072 sc1\n"
"v_mfma_f32_16x16x32_bf16 v[84:87], a[44:47], a[148:151], v[84:87]\n"
"v_mfma_f32_16x16x32_bf16 v[84:87], a[40:43], a[144:147], v[84:87]\n"
"v_mfma_f32_16x16x32_bf16 a[4:7], a[40:43], a[152:155], a[4:7]\n"
"s_add_u32 s34, s34, 8192\n"
"v_mfma_f32_16x16x32_bf16 a[4:7], a[44:47], a[156:159], a[4:7]\n"
"buffer_load_dwordx4 v1, s[48:51], s69 offen offset:3072 sc1 lds\n"
"v_mfma_f32_16x16x32_bf16 a[0:3], a[36:39], a[156:159], a[0:3]\n"
"v_mfma_f32_16x16x32_bf16 a[0:3], a[32:35], a[152:155], a[0:3]\n"
"s_waitcnt vmcnt(14)\n"
"v_mfma_f32_16x16x32_bf16 v[88:91], a[48:51], a[96:99], v[88:91]\n"
"v_mfma_f32_16x16x32_bf16 v[88:91], a[52:55], a[100:103], v[88:91]\n"
"global_load_dwordx4 a[32:35], v0, s[34:35] offset:-4096 sc1\n"
"v_mfma_f32_16x16x32_bf16 v[116:119], a[60:63], a[100:103], v[116:119]\n"
"v_mfma_f32_16x16x32_bf16 v[116:119], a[56:59], a[96:99], v[116:119]\n"
"v_mfma_f32_16x16x32_bf16 v[120:123], a[56:59], a[104:107], v[120:123]\n"
"v_mfma_f32_16x16x32_bf16 v[120:123], a[60:63], a[108:111], v[120:123]\n"
"v_mfma_f32_16x16x32_bf16 v[92:95], a[52:55], a[108:111], v[92:95]\n"
"v_mfma_f32_16x16x32_bf16 v[92:95], a[48:51], a[104:107], v[92:95]\n"
"v_mfma_f32_16x16x32_bf16 v[96:99], a[48:51], a[112:115], v[96:99]\n"
"v_mfma_f32_16x16x32_bf16 v[96:99], a[52:55], a[116:119], v[96:99]\n"
"global_load_dwordx4 a[36:39], v0, s[34:35] offset:-3072 sc1\n"
"v_mfma_f32_16x16x32_bf16 v[124:127], a[60:63], a[116:119], v[124:127]\n"
"v_mfma_f32_16x16x32_bf16 v[124:127], a[56:59], a[112:115], v[124:127]\n"
"v_mfma_f32_16x16x32_bf16 v[128:131], a[56:59], a[120:123], v[128:131]\n"
"v_mfma_f32_16x16x32_bf16 v[128:131], a[60:63], a[124:127], v[128:131]\n"
"v_mfma_f32_16x16x32_bf16 v[100:103], a[52:55], a[124:127], v[100:103]\n"
"v_mfma_f32_16x16x32_bf16 v[100:103], a[48:51], a[120:123], v[100:103]\n"
"v_mfma_f32_16x16x32_bf16 v[104:107], a[48:51], a[128:131], v[104:107]\n"
"buffer_load_dwordx2 v[8:9], off, s[56:59], %14 sc1\n"
"v_mfma_f32_16x16x32_bf16 v[104:107], a[52:55], a[132:135], v[104:107]\n"
"global_load_dwordx4 a[40:43], v0, s[34:35] offset:-2048 sc1\n"
"v_mfma_f32_16x16x32_bf16 v[132:135], a[60:63], a[132:135], v[132:135]\n"
"v_mfma_f32_16x16x32_bf16 v[132:135], a[56:59], a[128:131], v[132:135]\n"
"v_mfma_f32_16x16x32_bf16 v[136:139], a[56:59], a[136:139], v[136:139]\n"
"v_mfma_f32_16x16x32_bf16 v[136:139], a[60:63], a[140:143], v[136:139]\n"
"v_mfma_f32_16x16x32_bf16 v[108:111], a[52:55], a[140:143], v[108:111]\n"
"v_mfma_f32_16x16x32_bf16 v[108:111], a[48:51], a[136:139], v[108:111]\n"
"v_mfma_f32_16x16x32_bf16 v[112:115], a[48:51], a[144:147], v[112:115]\n"
"v_mfma_f32_16x16x32_bf16 v[112:115], a[52:55], a[148:151], v[112:115]\n"
"global_load_dwordx4 a[44:47], v0, s[34:35] offset:-1024 sc1\n"
"v_mfma_f32_16x16x32_bf16 v[140:143], a[60:63], a[148:151], v[140:143]\n"
"v_mfma_f32_16x16x32_bf16 v[140:143], a[56:59], a[144:147], v[140:143]\n"
"v_mfma_f32_16x16x32_bf16 a[12:15], a[56:59], a[152:155], a[12:15]\n"
"v_mfma_f32_16x16x32_bf16 a[12:15], a[60:63], a[156:159], a[12:15]\n"
"v_mfma_f32_16x16x32_bf16 a[8:11], a[52:55], a[156:159], a[8:11]\n"
"v_mfma_f32_16x16x32_bf16 a[8:11], a[48:51], a[152:155], a[8:11]\n"
"s_waitcnt vmcnt(13)\n"
"v_mfma_f32_16x16x32_bf16 v[144:147], a[64:67], a[96:99], v[144:147]\n"
"L_st6780:\n"
"s_cbranch_execz L_st9078\n"
"v_mfma_f32_16x16x32_bf16 v[144:147], a[68:71], a[100:103], v[144:147]\n"
"global_load_dwordx4 a[48:51], v0, s[34:35] offset:0 sc1\n"
"v_mfma_f32_16x16x32_bf16 v[172:175], a[76:79], a[100:103], v[172:175]\n"
"v_mfma_f32_16x16x32_bf16 v[172:175], a[72:75], a[96:99], v[172:175]\n"
"v_mfma_f32_16x16x32_bf16 v[176:179], a[72:75], a[104:107], v[176:179]\n"
"v_mfma_f32_16x16x32_bf16 v[176:179], a[76:79], a[108:111], v[176:179]\n"
"v_mfma_f32_16x16x32_bf16 v[148:151], a[68:71], a[108:111], v[148:151]\n"
"v_mfma_f32_16x16x32_bf16 v[148:151], a[64:67], a[104:107], v[148:151]\n"
"v_mfma_f32_16x16x32_bf16 v[152:155], a[64:67], a[112:115], v[152:155]\n"
"v_lshlrev_b32 v20, 16, v22\n"
"v_mfma_f32_16x16x32_bf16 v[152:155], a[68:71], a[116:119], v[152:155]\n"
"global_load_dwordx4 a[52:55], v0, s[34:35] offset:1024 sc1\n"
"v_and_b32 v21, s63, v22\n"
"v_mfma_f32_16x16x32_bf16 v[180:183], a[76:79], a[116:119], v[180:183]\n"
"v_lshlrev_b32 v22, 16, v23\n"
"v_mfma_f32_16x16x32_bf16 v[180:183], a[72:75], a[112:115], v[180:183]\n"
"v_and_b32 v23, s63, v23\n"
"v_mfma_f32_16x16x32_bf16 v[184:187], a[72:75], a[120:123], v[184:187]\n"
"v_add_f32 v60, v60, v20\n"
"v_mfma_f32_16x16x32_bf16 v[184:187], a[76:79], a[124:127], v[184:187]\n"
"v_add_f32 v61, v61, v21\n"
"v_add_f32 v62, v62, v22\n"
"v_mfma_f32_16x16x32_bf16 v[156:159], a[68:71], a[124:127], v[156:159]\n"
"v_add_f32 v63, v63, v23\n"
"v_mfma_f32_16x16x32_bf16 v[156:159], a[64:67], a[120:123], v[156:159]\n"
"v_add_f32 v64, v64, v20\n"
"v_mfma_f32_16x16x32_bf16 v[160:163], a[64:67], a[128:131], v[160:163]\n"
"v_add_f32 v65, v65, v21\n"
"v_mfma_f32_16x16x32_bf16 v[160:163], a[68:71], a[132:135], v[160:163]\n"
"global_load_dwordx4 a[56:59], v0, s[34:35] offset:2048 sc1\n"
"v_add_f32 v66, v66, v22\n"
"v_mfma_f32_16x16x32_bf16 v[188:191], a[76:79], a[132:135], v[188:191]\n"
"v_add_f32 v67, v67, v23\n"
"v_mfma_f32_16x16x32_bf16 v[188:191], a[72:75], a[128:131], v[188:191]\n"
"v_add_f32 v68, v68, v20\n"
"v_mfma_f32_16x16x32_bf16 v[192:195], a[72:75], a[136:139], v[192:195]\n"
"v_add_f32 v69, v69, v21\n"
"v_mfma_f32_16x16x32_bf16 v[192:195], a[76:79], a[140:143], v[192:195]\n"
"v_add_f32 v70, v70, v22\n"
"v_mfma_f32_16x16x32_bf16 v[164:167], a[68:71], a[140:143], v[164:167]\n"
"v_add_f32 v71, v71, v23\n"
"v_mfma_f32_16x16x32_bf16 v[164:167], a[64:67], a[136:139], v[164:167]\n"
"v_add_f32 v72, v72, v20\n"
"v_add_f32 v73, v73, v21\n"
"v_mfma_f32_16x16x32_bf16 v[168:171], a[64:67], a[144:147], v[168:171]\n"
"v_add_f32 v74, v74, v22\n"
"v_mfma_f32_16x16x32_bf16 v[168:171], a[68:71], a[148:151], v[168:171]\n"
"global_load_dwordx4 a[60:63], v0, s[34:35] offset:3072 sc1\n"
"v_add_f32 v75, v75, v23\n"
"v_mfma_f32_16x16x32_bf16 v[196:199], a[76:79], a[148:151], v[196:199]\n"
"v_add_f32 v76, v76, v20\n"
"v_mfma_f32_16x16x32_bf16 v[196:199], a[72:75], a[144:147], v[196:199]\n"
"v_add_f32 v77, v77, v21\n"
"v_mfma_f32_16x16x32_bf16 a[20:23], a[72:75], a[152:155], a[20:23]\n"
"s_add_u32 s34, s34, 8192\n"
"v_add_f32 v78, v78, v22\n"
"v_mfma_f32_16x16x32_bf16 a[20:23], a[76:79], a[156:159], a[20:23]\n"
"v_add_f32 v79, v79, v23\n"
"v_mfma_f32_16x16x32_bf16 a[16:19], a[68:71], a[156:159], a[16:19]\n"
"v_add_f32 v80, v80, v20\n"
"v_mfma_f32_16x16x32_bf16 a[16:19], a[64:67], a[152:155], a[16:19]\n"
"s_waitcnt vmcnt(10)\n"
"v_add_f32 v81, v81, v21\n"
"v_mfma_f32_16x16x32_bf16 v[200:203], a[80:83], a[96:99], v[200:203]\n"
"v_add_f32 v82, v82, v22\n"
"v_add_f32 v83, v83, v23\n"
"v_mfma_f32_16x16x32_bf16 v[200:203], a[84:87], a[100:103], v[200:203]\n"
"global_load_dwordx4 a[64:67], v0, s[34:35] offset:-4096 sc1\n"
"v_add_f32 v84, v84, v20\n"
"v_mfma_f32_16x16x32_bf16 v[228:231], a[92:95], a[100:103], v[228:231]\n"
"s_waitcnt vmcnt(10)\n"
"ds_read_b128 a[100:103], v2 offset:1024\n"
"v_add_f32 v85, v85, v21\n"
"v_mfma_f32_16x16x32_bf16 v[228:231], a[88:91], a[96:99], v[228:231]\n"
"ds_read_b128 a[96:99], v2 offset:0\n"
"v_add_f32 v86, v86, v22\n"
"v_mfma_f32_16x16x32_bf16 v[232:235], a[88:91], a[104:107], v[232:235]\n"
"v_add_f32 v87, v87, v23\n"
"v_mfma_f32_16x16x32_bf16 v[232:235], a[92:95], a[108:111], v[232:235]\n"
"v_accvgpr_read_b32 v12, a4\n"
"v_mfma_f32_16x16x32_bf16 v[204:207], a[84:87], a[108:111], v[204:207]\n"
"ds_read_b128 a[108:111], v2 offset:3072\n"
"v_accvgpr_read_b32 v13, a5\n"
"v_mfma_f32_16x16x32_bf16 v[204:207], a[80:83], a[104:107], v[204:207]\n"
"ds_read_b128 a[104:107], v2 offset:2048\n"
"v_accvgpr_read_b32 v14, a6\n"
"v_mfma_f32_16x16x32_bf16 v[208:211], a[80:83], a[112:115], v[208:211]\n"
"v_accvgpr_read_b32 v15, a7\n"
"v_mfma_f32_16x16x32_bf16 v[208:211], a[84:87], a[116:119], v[208:211]\n"
"global_load_dwordx4 a[68:71], v0, s[34:35] offset:-3072 sc1\n"
"v_add_f32 v12, v12, v20\n"
"v_mfma_f32_16x16x32_bf16 v[236:239], a[92:95], a[116:119], v[236:239]\n"
"ds_read_b128 a[116:119], v2 offset:5120\n"
"v_add_f32 v13, v13, v21\n"
"v_mfma_f32_16x16x32_bf16 v[236:239], a[88:91], a[112:115], v[236:239]\n"
"ds_read_b128 a[112:115], v2 offset:4096\n"
"v_add_f32 v14, v14, v22\n"
"v_mfma_f32_16x16x32_bf16 v[240:243], a[88:91], a[120:123], v[240:243]\n"
"v_add_f32 v15, v15, v23\n"
"v_accvgpr_write_b32 a4, v12\n"
"v_mfma_f32_16x16x32_bf16 v[240:243], a[92:95], a[124:127], v[240:243]\n"
"v_accvgpr_write_b32 a5, v13\n"
"v_mfma_f32_16x16x32_bf16 v[212:215], a[84:87], a[124:127], v[212:215]\n"
"ds_read_b128 a[124:127], v2 offset:7168\n"
"v_accvgpr_write_b32 a6, v14\n"
"v_mfma_f32_16x16x32_bf16 v[212:215], a[80:83], a[120:123], v[212:215]\n"
"ds_read_b128 a[120:123], v2 offset:6144\n"
"v_accvgpr_write_b32 a7, v15\n"
"v_mfma_f32_16x16x32_bf16 v[216:219], a[80:83], a[128:131], v[216:219]\n"
"v_mfma_f32_16x16x32_bf16 v[216:219], a[84:87], a[132:135], v[216:219]\n"
"global_load_dwordx4 a[72:75], v0, s[34:35] offset:-2048 sc1\n"
"v_lshrrev_b32 v20, 4, v0\n"
"v_mfma_f32_16x16x32_bf16 v[244:247], a[92:95], a[132:135], v[244:247]\n"
"ds_read_b128 a[132:135], v2 offset:9216\n"
"v_and_b32 v20, 0x30, v20\n"
"v_mfma_f32_16x16x32_bf16 v[244:247], a[88:91], a[128:131], v[244:247]\n"
"ds_read_b128 a[128:131], v2 offset:8192\n"
"buffer_load_dwordx2 v[22:23], v20, s[44:47], s61 offen offset:72\n"
"v_mfma_f32_16x16x32_bf16 v[248:251], a[88:91], a[136:139], v[248:251]\n"
"v_mfma_f32_16x16x32_bf16 v[248:251], a[92:95], a[140:143], v[248:251]\n"
"v_mfma_f32_16x16x32_bf16 v[220:223], a[84:87], a[140:143], v[220:223]\n"
"ds_read_b128 a[140:143], v2 offset:11264\n"
"v_mfma_f32_16x16x32_bf16 v[220:223], a[80:83], a[136:139], v[220:223]\n"
"ds_read_b128 a[136:139], v2 offset:10240\n"
"v_mfma_f32_16x16x32_bf16 v[224:227], a[80:83], a[144:147], v[224:227]\n"
"v_mfma_f32_16x16x32_bf16 v[224:227], a[84:87], a[148:151], v[224:227]\n"
"global_load_dwordx4 a[76:79], v0, s[34:35] offset:-1024 sc1\n"
"v_mfma_f32_16x16x32_bf16 v[252:255], a[92:95], a[148:151], v[252:255]\n"
"ds_read_b128 a[148:151], v2 offset:13312\n"
"v_mfma_f32_16x16x32_bf16 v[252:255], a[88:91], a[144:147], v[252:255]\n"
"ds_read_b128 a[144:147], v2 offset:12288\n"
"v_mfma_f32_16x16x32_bf16 a[28:31], a[88:91], a[152:155], a[28:31]\n"
"v_mfma_f32_16x16x32_bf16 a[28:31], a[92:95], a[156:159], a[28:31]\n"
"v_mfma_f32_16x16x32_bf16 a[24:27], a[84:87], a[156:159], a[24:27]\n"
"s_cmp_eq_u32 s62, 5\n"
"ds_read_b128 a[156:159], v2 offset:15360\n"
"v_mfma_f32_16x16x32_bf16 a[24:27], a[80:83], a[152:155], a[24:27]\n"
"ds_read_b128 a[152:155], v2 offset:14336\n"
"s_cbranch_scc1 L_last\n"
"L_P4:\n"
"s_waitcnt vmcnt(9) lgkmcnt(12)\n"
"v_mfma_f32_16x16x32_bf16 v[32:35], a[32:35], a[96:99], v[32:35]\n"
"s_add_u32 s48, s48, 131072\n"
"s_addc_u32 s49, s49, 0\n"
"v_mfma_f32_16x16x32_bf16 v[32:35], a[36:39], a[100:103], v[32:35]\n"
"global_load_dwordx4 a[80:83], v0, s[34:35] offset:0 sc1\n"
"v_mfma_f32_16x16x32_bf16 v[60:63], a[44:47], a[100:103], v[60:63]\n"
"s_barrier\n"
"v_mfma_f32_16x16x32_bf16 v[60:63], a[40:43], a[96:99], v[60:63]\n"
"v_mfma_f32_16x16x32_bf16 v[64:67], a[40:43], a[104:107], v[64:67]\n"
"v_mfma_f32_16x16x32_bf16 v[64:67], a[44:47], a[108:111], v[64:67]\n"
"v_mfma_f32_16x16x32_bf16 v[36:39], a[36:39], a[108:111], v[36:39]\n"
"v_subrev_u32 v8, s65, v8\n"
"v_mfma_f32_16x16x32_bf16 v[36:39], a[32:35], a[104:107], v[36:39]\n"
"s_waitcnt lgkmcnt(8)\n"
"v_xor_b32 v9, s79, v9\n"
"v_mfma_f32_16x16x32_bf16 v[40:43], a[32:35], a[112:115], v[40:43]\n"
"v_or_b32 v8, v8, v9\n"
"v_mfma_f32_16x16x32_bf16 v[40:43], a[36:39], a[116:119], v[40:43]\n"
"global_load_dwordx4 a[84:87], v0, s[34:35] offset:1024 sc1\n"
"v_cmp_lt_u32 vcc, 15, v8\n"
"v_mfma_f32_16x16x32_bf16 v[68:71], a[44:47], a[116:119], v[68:71]\n"
"s_cbranch_vccnz L_slow1\n"
"L_back1:\n"
"v_mfma_f32_16x16x32_bf16 v[68:71], a[40:43], a[112:115], v[68:71]\n"
"v_mfma_f32_16x16x32_bf16 v[72:75], a[40:43], a[120:123], v[72:75]\n"
"v_mfma_f32_16x16x32_bf16 v[72:75], a[44:47], a[124:127], v[72:75]\n"
"v_mfma_f32_16x16x32_bf16 v[44:47], a[36:39], a[124:127], v[44:47]\n"
"v_mfma_f32_16x16x32_bf16 v[44:47], a[32:35], a[120:123], v[44:47]\n"
"s_waitcnt lgkmcnt(4)\n"
"v_mfma_f32_16x16x32_bf16 v[48:51], a[32:35], a[128:131], v[48:51]\n"
"v_mfma_f32_16x16x32_bf16 v[48:51], a[36:39], a[132:135], v[48:51]\n"
"global_load_dwordx4 a[88:91], v0, s[34:35] offset:2048 sc1\n"
"v_mfma_f32_16x16x32_bf16 v[76:79], a[44:47], a[132:135], v[76:79]\n"
"v_mfma_f32_16x16x32_bf16 v[76:79], a[40:43], a[128:131], v[76:79]\n"
"s_add_u32 m0, s64, 16384\n"
"v_mfma_f32_16x16x32_bf16 v[80:83], a[40:43], a[136:139], v[80:83]\n"
"v_mfma_f32_16x16x32_bf16 v[80:83], a[44:47], a[140:143], v[80:83]\n"
"v_mfma_f32_16x16x32_bf16 v[52:55], a[36:39], a[140:143], v[52:55]\n"
"v_mfma_f32_16x16x32_bf16 v[52:55], a[32:35], a[136:139], v[52:55]\n"
"s_waitcnt lgkmcnt(0)\n"
"v_mfma_f32_16x16x32_bf16 v[56:59], a[32:35], a[144:147], v[56:59]\n"
"v_mfma_f32_16x16x32_bf16 v[56:59], a[36:39], a[148:151], v[56:59]\n"
"global_load_dwordx4 a[92:95], v0, s[34:35] offset:3072 sc1\n"
"v_mfma_f32_16x16x32_bf16 v[84:87], a[44:47], a[148:151], v[84:87]\n"
"v_mfma_f32_16x16x32_bf16 v[84:87], a[40:43], a[144:147], v[84:87]\n"
"v_mfma_f32_16x16x32_bf16 a[4:7], a[40:43], a[152:155], a[4:7]\n"
"s_add_u32 s34, s34, 8192\n"
"v_mfma_f32_16x16x32_bf16 a[4:7], a[44:47], a[156:159], a[4:7]\n"
"v_mfma_f32_16x16x32_bf16 a[0:3], a[36:39], a[156:159], a[0:3]\n"
"v_mfma_f32_16x16x32_bf16 a[0:3], a[32:35], a[152:155], a[0:3]\n"
"s_waitcnt vmcnt(9)\n"
"v_mfma_f32_16x16x32_bf16 v[88:91], a[48:51], a[96:99], v[88:91]\n"
"v_mfma_f32_16x16x32_bf16 v[88:91], a[52:55], a[100:103], v[88:91]\n"
"global_load_dwordx4 a[32:35], v0, s[34:35] offset:-4096 sc1\n"
"v_mfma_f32_16x16x32_bf16 v[116:119], a[60:63], a[100:103], v[116:119]\n"
"buffer_load_dwordx4 v0, s[48:51], s75 offen offset:0 sc1 lds\n"
"v_mfma_f32_16x16x32_bf16 v[116:119], a[56:59], a[96:99], v[116:119]\n"
"v_mfma_f32_16x16x32_bf16 v[120:123], a[56:59], a[104:107], v[120:123]\n"
"v_mfma_f32_16x16x32_bf16 v[120:123], a[60:63], a[108:111], v[120:123]\n"
"v_mfma_f32_16x16x32_bf16 v[92:95], a[52:55], a[108:111], v[92:95]\n"
"v_mfma_f32_16x16x32_bf16 v[92:95], a[48:51], a[104:107], v[92:95]\n"
"v_mfma_f32_16x16x32_bf16 v[96:99], a[48:51], a[112:115], v[96:99]\n"
"v_mfma_f32_16x16x32_bf16 v[96:99], a[52:55], a[116:119], v[96:99]\n"
"global_load_dwordx4 a[36:39], v0, s[34:35] offset:-3072 sc1\n"
"v_mfma_f32_16x16x32_bf16 v[124:127], a[60:63], a[116:119], v[124:127]\n"
"v_mfma_f32_16x16x32_bf16 v[124:127], a[56:59], a[112:115], v[124:127]\n"
"v_mfma_f32_16x16x32_bf16 v[128:131], a[56:59], a[120:123], v[128:131]\n"
"v_mfma_f32_16x16x32_bf16 v[128:131], a[60:63], a[124:127], v[128:131]\n"
"v_mfma_f32_16x16x32_bf16 v[100:103], a[52:55], a[124:127], v[100:103]\n"
"v_mfma_f32_16x16x32_bf16 v[100:103], a[48:51], a[120:123], v[100:103]\n"
"v_mfma_f32_16x16x32_bf16 v[104:107], a[48:51], a[128:131], v[104:107]\n"
"v_mfma_f32_16x16x32_bf16 v[104:107], a[52:55], a[132:135], v[104:107]\n"
"global_load_dwordx4 a[40:43], v0, s[34:35] offset:-2048 sc1\n"
"v_mfma_f32_16x16x32_bf16 v[132:135], a[60:63], a[132:135], v[132:135]\n"
"buffer_load_dwordx4 v0, s[48:51], s75 offen offset:1024 sc1 lds\n"
"v_mfma_f32_16x16x32_bf16 v[132:135], a[56:59], a[128:131], v[132:135]\n"
"v_mfma_f32_16x16x32_bf16 v[136:139], a[56:59], a[136:139], v[136:139]\n"
"v_mfma_f32_16x16x32_bf16 v[136:139], a[60:63], a[140:143], v[136:139]\n"
"v_mfma_f32_16x16x32_bf16 v[108:111], a[52:55], a[140:143], v[108:111]\n"
"v_mfma_f32_16x16x32_bf16 v[108:111], a[48:51], a[136:139], v[108:111]\n"
"v_mfma_f32_16x16x32_bf16 v[112:115], a[48:51], a[144:147], v[112:115]\n"
"v_mfma_f32_16x16x32_bf16 v[112:115], a[52:55], a[148:151], v[112:115]\n"
"global_load_dwordx4 a[44:47], v0, s[34:35] offset:-1024 sc1\n"
"v_mfma_f32_16x16x32_bf16 v[140:143], a[60:63], a[148:151], v[140:143]\n"
"v_mfma_f32_16x16x32_bf16 v[140:143], a[56:59], a[144:147], v[140:143]\n"
"v_mfma_f32_16x16x32_bf16 a[12:15], a[56:59], a[152:155], a[12:15]\n"
"v_mfma_f32_16x16x32_bf16 a[12:15], a[60:63], a[156:159], a[12:15]\n"
"v_mfma_f32_16x16x32_bf16 a[8:11], a[52:55], a[156:159], a[8:11]\n"
"v_mfma_f32_16x16x32_bf16 a[8:11], a[48:51], a[152:155], a[8:11]\n"
"s_waitcnt vmcnt(10)\n"
"v_mfma_f32_16x16x32_bf16 v[144:147], a[64:67], a[96:99], v[144:147]\n"
"v_mfma_f32_16x16x32_bf16 v[144:147], a[68:71], a[100:103], v[144:147]\n"
"global_load_dwordx4 a[48:51], v0, s[34:35] offset:0 sc1\n"
"v_mfma_f32_16x16x32_bf16 v[172:175], a[76:79], a[100:103], v[172:175]\n"
"v_mfma_f32_16x16x32_bf16 v[172:175], a[72:75], a[96:99], v[172:175]\n"
"buffer_load_dwordx4 v0, s[48:51], s75 offen offset:2048 sc1 lds\n"
"v_mfma_f32_16x16x32_bf16 v[176:179], a[72:75], a[104:107], v[176:179]\n"
"v_mfma_f32_16x16x32_bf16 v[176:179], a[76:79], a[108:111], v[176:179]\n"
"v_mfma_f32_16x16x32_bf16 v[148:151], a[68:71], a[108:111], v[148:151]\n"
"v_mfma_f32_16x16x32_bf16 v[148:151], a[64:67], a[104:107], v[148:151]\n"
"v_mfma_f32_16x16x32_bf16 v[152:155], a[64:67], a[112:115], v[152:155]\n"
"v_lshlrev_b32 v16, 16, v18\n"
"v_mfma_f32_16x16x32_bf16 v[152:155], a[68:71], a[116:119], v[152:155]\n"
"global_load_dwordx4 a[52:55], v0, s[34:35] offset:1024 sc1\n"
"v_and_b32 v17, s63, v18\n"
"v_mfma_f32_16x16x32_bf16 v[180:183], a[76:79], a[116:119], v[180:183]\n"
"v_lshlrev_b32 v18, 16, v19\n"
"v_mfma_f32_16x16x32_bf16 v[180:183], a[72:75], a[112:115], v[180:183]\n"
"v_and_b32 v19, s63, v19\n"
"v_mfma_f32_16x16x32_bf16 v[184:187], a[72:75], a[120:123], v[184:187]\n"
"v_add_f32 v88, v88, v16\n"
"v_mfma_f32_16x16x32_bf16 v[184:187], a[76:79], a[124:127], v[184:187]\n"
"v_add_f32 v89, v89, v17\n"
"v_add_f32 v90, v90, v18\n"
"v_mfma_f32_16x16x32_bf16 v[156:159], a[68:71], a[124:127], v[156:159]\n"
"v_add_f32 v91, v91, v19\n"
"v_mfma_f32_16x16x32_bf16 v[156:159], a[64:67], a[120:123], v[156:159]\n"
"v_add_f32 v92, v92, v16\n"
"v_mfma_f32_16x16x32_bf16 v[160:163], a[64:67], a[128:131], v[160:163]\n"
"v_add_f32 v93, v93, v17\n"
"v_mfma_f32_16x16x32_bf16 v[160:163], a[68:71], a[132:135], v[160:163]\n"
"global_load_dwordx4 a[56:59], v0, s[34:35] offset:2048 sc1\n"
"v_add_f32 v94, v94, v18\n"
"v_mfma_f32_16x16x32_bf16 v[188:191], a[76:79], a[132:135], v[188:191]\n"
"buffer_load_dwordx4 v0, s[48:51], s75 offen offset:3072 sc1 lds\n"
"v_mfma_f32_16x16x32_bf16 v[188:191], a[72:75], a[128:131], v[188:191]\n"
"v_add_f32 v95, v95, v19\n"
"v_add_f32 v96, v96, v16\n"
"v_mfma_f32_16x16x32_bf16 v[192:195], a[72:75], a[136:139], v[192:195]\n"
"v_add_f32 v97, v97, v17\n"
"v_mfma_f32_16x16x32_bf16 v[192:195], a[76:79], a[140:143], v[192:195]\n"
"v_add_f32 v98, v98, v18\n"
"v_mfma_f32_16x16x32_bf16 v[164:167], a[68:71], a[140:143], v[164:167]\n"
"v_add_f32 v99, v99, v19\n"
"v_mfma_f32_16x16x32_bf16 v[164:167], a[64:67], a[136:139], v[164:167]\n"
"v_add_f32 v100, v100, v16\n"
"v_add_f32 v101, v101, v17\n"
"v_mfma_f32_16x16x32_bf16 v[168:171], a[64:67], a[144:147], v[168:171]\n"
"v_add_f32 v102, v102, v18\n"
"v_mfma_f32_16x16x32_bf16 v[168:171], a[68:71], a[148:151], v[168:171]\n"
"global_load_dwordx4 a[60:63], v0, s[34:35] offset:3072 sc1\n"
"v_add_f32 v103, v103, v19\n"
"v_mfma_f32_16x16x32_bf16 v[196:199], a[76:79], a[148:151], v[196:199]\n"
"v_add_f32 v104, v104, v16\n"
"v_mfma_f32_16x16x32_bf16 v[196:199], a[72:75], a[144:147], v[196:199]\n"
"v_add_f32 v105, v105, v17\n"
"v_mfma_f32_16x16x32_bf16 a[20:23], a[72:75], a[152:155], a[20:23]\n"
"s_add_u32 s34, s34, 8192\n"
"v_add_f32 v106, v106, v18\n"
"v_mfma_f32_16x16x32_bf16 a[20:23], a[76:79], a[156:159], a[20:23]\n"
"v_add_f32 v107, v107, v19\n"
"v_mfma_f32_16x16x32_bf16 a[16:19], a[68:71], a[156:159], a[16:19]\n"
"v_add_f32 v108, v108, v16\n"
"v_mfma_f32_16x16x32_bf16 a[16:19], a[64:67], a[152:155], a[16:19]\n"
"s_waitcnt vmcnt(12)\n"
"v_add_f32 v109, v109, v17\n"
"v_mfma_f32_16x16x32_bf16 v[200:203], a[80:83], a[96:99], v[200:203]\n"
"v_add_f32 v110, v110, v18\n"
"v_add_f32 v111, v111, v19\n"
"v_mfma_f32_16x16x32_bf16 v[200:203], a[84:87], a[100:103], v[200:203]\n"
"global_load_dwordx4 a[64:67], v0, s[34:35] offset:-4096 sc1\n"
"v_add_f32 v112, v112, v16\n"
"v_mfma_f32_16x16x32_bf16 v[228:231], a[92:95], a[100:103], v[228:231]\n"
"ds_read_b128 a[100:103], v3 offset:1024\n"
"v_add_f32 v113, v113, v17\n"
"v_mfma_f32_16x16x32_bf16 v[228:231], a[88:91], a[96:99], v[228:231]\n"
"s_add_u32 m0, s64, 20480\n"
"ds_read_b128 a[96:99], v3 offset:0\n"
"v_add_f32 v114, v114, v18\n"
"v_mfma_f32_16x16x32_bf16 v[232:235], a[88:91], a[104:107], v[232:235]\n"
"v_add_f32 v115, v115, v19\n"
"v_mfma_f32_16x16x32_bf16 v[232:235], a[92:95], a[108:111], v[232:235]\n"
"v_accvgpr_read_b32 v12, a8\n"
"v_mfma_f32_16x16x32_bf16 v[204:207], a[84:87], a[108:111], v[204:207]\n"
"ds_read_b128 a[108:111], v3 offset:3072\n"
"v_accvgpr_read_b32 v13, a9\n"
"v_mfma_f32_16x16x32_bf16 v[204:207], a[80:83], a[104:107], v[204:207]\n"
"ds_read_b128 a[104:107], v3 offset:2048\n"
"v_accvgpr_read_b32 v14, a10\n"
"v_mfma_f32_16x16x32_bf16 v[208:211], a[80:83], a[112:115], v[208:211]\n"
"v_accvgpr_read_b32 v15, a11\n"
"v_mfma_f32_16x16x32_bf16 v[208:211], a[84:87], a[116:119], v[208:211]\n"
"global_load_dwordx4 a[68:71], v0, s[34:35] offset:-3072 sc1\n"
"v_add_f32 v12, v12, v16\n"
"v_mfma_f32_16x16x32_bf16 v[236:239], a[92:95], a[116:119], v[236:239]\n"
"ds_read_b128 a[116:119], v3 offset:5120\n"
"v_add_f32 v13, v13, v17\n"
"v_mfma_f32_16x16x32_bf16 v[236:239], a[88:91], a[112:115], v[236:239]\n"
"ds_read_b128 a[112:115], v3 offset:4096\n"
"v_add_f32 v14, v14, v18\n"
"v_mfma_f32_16x16x32_bf16 v[240:243], a[88:91], a[120:123], v[240:243]\n"
"v_add_f32 v15, v15, v19\n"
"v_accvgpr_write_b32 a8, v12\n"
"v_mfma_f32_16x16x32_bf16 v[240:243], a[92:95], a[124:127], v[240:243]\n"
"v_accvgpr_write_b32 a9, v13\n"
"v_mfma_f32_16x16x32_bf16 v[212:215], a[84:87], a[124:127], v[212:215]\n"
"ds_read_b128 a[124:127], v3 offset:7168\n"
"v_accvgpr_write_b32 a10, v14\n"
"v_mfma_f32_16x16x32_bf16 v[212:215], a[80:83], a[120:123], v[212:215]\n"
"ds_read_b128 a[120:123], v3 offset:6144\n"
"v_accvgpr_write_b32 a11, v15\n"
"v_mfma_f32_16x16x32_bf16 v[216:219], a[80:83], a[128:131], v[216:219]\n"
"v_mfma_f32_16x16x32_bf16 v[216:219], a[84:87], a[132:135], v[216:219]\n"
"global_load_dwordx4 a[72:75], v0, s[34:35] offset:-2048 sc1\n"
"v_mfma_f32_16x16x32_bf16 v[244:247], a[92:95], a[132:135], v[244:247]\n"
"ds_read_b128 a[132:135], v3 offset:9216\n"
"v_mfma_f32_16x16x32_bf16 v[244:247], a[88:91], a[128:131], v[244:247]\n"
"ds_read_b128 a[128:131], v3 offset:8192\n"
"v_mfma_f32_16x16x32_bf16 v[248:251], a[88:91], a[136:139], v[248:251]\n"
"buffer_load_dwordx4 v1, s[48:51], s75 offen offset:0 sc1 lds\n"
"v_mfma_f32_16x16x32_bf16 v[248:251], a[92:95], a[140:143], v[248:251]\n"
"v_mfma_f32_16x16x32_bf16 v[220:223], a[84:87], a[140:143], v[220:223]\n"
"ds_read_b128 a[140:143], v3 offset:11264\n"
"v_mfma_f32_16x16x32_bf16 v[220:223], a[80:83], a[136:139], v[220:223]\n"
"ds_read_b128 a[136:139], v3 offset:10240\n"
"v_mfma_f32_16x16x32_bf16 v[224:227], a[80:83], a[144:147], v[224:227]\n"
"v_mfma_f32_16x16x32_bf16 v[224:227], a[84:87], a[148:151], v[224:227]\n"
"global_load_dwordx4 a[76:79], v0, s[34:35] offset:-1024 sc1\n"
"v_mfma_f32_16x16x32_bf16 v[252:255], a[92:95], a[148:151], v[252:255]\n"
"ds_read_b128 a[148:151], v3 offset:13312\n"
"v_mfma_f32_16x16x32_bf16 v[252:255], a[88:91], a[144:147], v[252:255]\n"
"ds_read_b128 a[144:147], v3 offset:12288\n"
"v_mfma_f32_16x16x32_bf16 a[28:31], a[88:91], a[152:155], a[28:31]\n"
"v_mfma_f32_16x16x32_bf16 a[28:31], a[92:95], a[156:159], a[28:31]\n"
"v_mfma_f32_16x16x32_bf16 a[24:27], a[84:87], a[156:159], a[24:27]\n"
"ds_read_b128 a[156:159], v3 offset:15360\n"
"v_mfma_f32_16x16x32_bf16 a[24:27], a[80:83], a[152:155], a[24:27]\n"
"s_waitcnt vmcnt(11) lgkmcnt(11)\n"
"ds_read_b128 a[152:155], v3 offset:14336\n"
"v_mfma_f32_16x16x32_bf16 v[32:35], a[32:35], a[96:99], v[32:35]\n"
"v_mfma_f32_16x16x32_bf16 v[32:35], a[36:39], a[100:103], v[32:35]\n"
"global_load_dwordx4 a[80:83], v0, s[34:35] offset:0 sc1\n"
"v_mfma_f32_16x16x32_bf16 v[60:63], a[44:47], a[100:103], v[60:63]\n"
"v_mfma_f32_16x16x32_bf16 v[60:63], a[40:43], a[96:99], v[60:63]\n"
"buffer_load_dwordx4 v1, s[48:51], s75 offen offset:1024 sc1 lds\n"
"v_mfma_f32_16x16x32_bf16 v[64:67], a[40:43], a[104:107], v[64:67]\n"
"v_mfma_f32_16x16x32_bf16 v[64:67], a[44:47], a[108:111], v[64:67]\n"
"v_mfma_f32_16x16x32_bf16 v[36:39], a[36:39], a[108:111], v[36:39]\n"
"v_mfma_f32_16x16x32_bf16 v[36:39], a[32:35], a[104:107], v[36:39]\n"
"s_waitcnt lgkmcnt(8)\n"
"v_mfma_f32_16x16x32_bf16 v[40:43], a[32:35], a[112:115], v[40:43]\n"
"v_mfma_f32_16x16x32_bf16 v[40:43], a[36:39], a[116:119], v[40:43]\n"
"global_load_dwordx4 a[84:87], v0, s[34:35] offset:1024 sc1\n"
"v_mfma_f32_16x16x32_bf16 v[68:71], a[44:47], a[116:119], v[68:71]\n"
"v_mfma_f32_16x16x32_bf16 v[68:71], a[40:43], a[112:115], v[68:71]\n"
"v_mfma_f32_16x16x32_bf16 v[72:75], a[40:43], a[120:123], v[72:75]\n"
"v_mfma_f32_16x16x32_bf16 v[72:75], a[44:47], a[124:127], v[72:75]\n"
"v_mfma_f32_16x16x32_bf16 v[44:47], a[36:39], a[124:127], v[44:47]\n"
"v_mfma_f32_16x16x32_bf16 v[44:47], a[32:35], a[120:123], v[44:47]\n"
"s_waitcnt lgkmcnt(4)\n"
"v_mfma_f32_16x16x32_bf16 v[48:51], a[32:35], a[128:131], v[48:51]\n"
"v_mfma_f32_16x16x32_bf16 v[48:51], a[36:39], a[132:135], v[48:51]\n"
"global_load_dwordx4 a[88:91], v0, s[34:35] offset:2048 sc1\n"
"v_mfma_f32_16x16x32_bf16 v[76:79], a[44:47], a[132:135], v[76:79]\n"
"v_mfma_f32_16x16x32_bf16 v[76:79], a[40:43], a[128:131], v[76:79]\n"
"buffer_load_dwordx4 v1, s[48:51], s75 offen offset:2048 sc1 lds\n"
"v_mfma_f32_16x16x32_bf16 v[80:83], a[40:43], a[136:139], v[80:83]\n"
"v_mfma_f32_16x16x32_bf16 v[80:83], a[44:47], a[140:143], v[80:83]\n"
"v_mfma_f32_16x16x32_bf16 v[52:55], a[36:39], a[140:143], v[52:55]\n"
"v_mfma_f32_16x16x32_bf16 v[52:55], a[32:35], a[136:139], v[52:55]\n"
"s_waitcnt lgkmcnt(0)\n"
"v_mfma_f32_16x16x32_bf16 v[56:59], a[32:35], a[144:147], v[56:59]\n"
"v_mfma_f32_16x16x32_bf16 v[56:59], a[36:39], a[148:151], v[56:59]\n"
"global_load_dwordx4 a[92:95], v0, s[34:35] offset:3072 sc1\n"
"v_mfma_f32_16x16x32_bf16 v[84:87], a[44:47], a[148:151], v[84:87]\n"
"v_mfma_f32_16x16x32_bf16 v[84:87], a[40:43], a[144:147], v[84:87]\n"
"v_mfma_f32_16x16x32_bf16 a[4:7], a[40:43], a[152:155], a[4:7]\n"
"s_add_u32 s34, s34, 8192\n"
"v_mfma_f32_16x16x32_bf16 a[4:7], a[44:47], a[156:159], a[4:7]\n"
"v_mfma_f32_16x16x32_bf16 a[0:3], a[36:39], a[156:159], a[0:3]\n"
"v_mfma_f32_16x16x32_bf16 a[0:3], a[32:35], a[152:155], a[0:3]\n"
"s_waitcnt vmcnt(11)\n"
"v_mfma_f32_16x16x32_bf16 v[88:91], a[48:51], a[96:99], v[88:91]\n"
"v_mfma_f32_16x16x32_bf16 v[88:91], a[52:55], a[100:103], v[88:91]\n"
"global_load_dwordx4 a[32:35], v0, s[34:35] offset:-4096 sc1\n"
"v_mfma_f32_16x16x32_bf16 v[116:119], a[60:63], a[100:103], v[116:119]\n"
"v_mfma_f32_16x16x32_bf16 v[116:119], a[56:59], a[96:99], v[116:119]\n"
"buffer_load_dwordx4 v1, s[48:51], s75 offen offset:3072 sc1 lds\n"
"v_mfma_f32_16x16x32_bf16 v[120:123], a[56:59], a[104:107], v[120:123]\n"
"v_mfma_f32_16x16x32_bf16 v[120:123], a[60:63], a[108:111], v[120:123]\n"
"v_mfma_f32_16x16x32_bf16 v[92:95], a[52:55], a[108:111], v[92:95]\n"
"v_mfma_f32_16x16x32_bf16 v[92:95], a[48:51], a[104:107], v[92:95]\n"
"v_mfma_f32_16x16x32_bf16 v[96:99], a[48:51], a[112:115], v[96:99]\n"
"v_mfma_f32_16x16x32_bf16 v[96:99], a[52:55], a[116:119], v[96:99]\n"
"global_load_dwordx4 a[36:39], v0, s[34:35] offset:-3072 sc1\n"
"v_mfma_f32_16x16x32_bf16 v[124:127], a[60:63], a[116:119], v[124:127]\n"
"v_mfma_f32_16x16x32_bf16 v[124:127], a[56:59], a[112:115], v[124:127]\n"
"v_mfma_f32_16x16x32_bf16 v[128:131], a[56:59], a[120:123], v[128:131]\n"
"v_mfma_f32_16x16x32_bf16 v[128:131], a[60:63], a[124:127], v[128:131]\n"
"v_mfma_f32_16x16x32_bf16 v[100:103], a[52:55], a[124:127], v[100:103]\n"
"v_mfma_f32_16x16x32_bf16 v[100:103], a[48:51], a[120:123], v[100:103]\n"
"v_mfma_f32_16x16x32_bf16 v[104:107], a[48:51], a[128:131], v[104:107]\n"
"v_mfma_f32_16x16x32_bf16 v[104:107], a[52:55], a[132:135], v[104:107]\n"
"global_load_dwordx4 a[40:43], v0, s[34:35] offset:-2048 sc1\n"
"v_mfma_f32_16x16x32_bf16 v[132:135], a[60:63], a[132:135], v[132:135]\n"
"v_mfma_f32_16x16x32_bf16 v[132:135], a[56:59], a[128:131], v[132:135]\n"
"s_add_u32 m0, s64, 24576\n"
"v_mfma_f32_16x16x32_bf16 v[136:139], a[56:59], a[136:139], v[136:139]\n"
"v_mfma_f32_16x16x32_bf16 v[136:139], a[60:63], a[140:143], v[136:139]\n"
"v_mfma_f32_16x16x32_bf16 v[108:111], a[52:55], a[140:143], v[108:111]\n"
"v_mfma_f32_16x16x32_bf16 v[108:111], a[48:51], a[136:139], v[108:111]\n"
"v_mfma_f32_16x16x32_bf16 v[112:115], a[48:51], a[144:147], v[112:115]\n"
"v_mfma_f32_16x16x32_bf16 v[112:115], a[52:55], a[148:151], v[112:115]\n"
"global_load_dwordx4 a[44:47], v0, s[34:35] offset:-1024 sc1\n"
"v_mfma_f32_16x16x32_bf16 v[140:143], a[60:63], a[148:151], v[140:143]\n"
"v_mfma_f32_16x16x32_bf16 v[140:143], a[56:59], a[144:147], v[140:143]\n"
"v_mfma_f32_16x16x32_bf16 a[12:15], a[56:59], a[152:155], a[12:15]\n"
"v_mfma_f32_16x16x32_bf16 a[12:15], a[60:63], a[156:159], a[12:15]\n"
"v_mfma_f32_16x16x32_bf16 a[8:11], a[52:55], a[156:159], a[8:11]\n"
"v_mfma_f32_16x16x32_bf16 a[8:11], a[48:51], a[152:155], a[8:11]\n"
"s_waitcnt vmcnt(11)\n"
"v_mfma_f32_16x16x32_bf16 v[144:147], a[64:67], a[96:99], v[144:147]\n"
"v_mfma_f32_16x16x32_bf16 v[144:147], a[68:71], a[100:103], v[144:147]\n"
"global_load_dwordx4 a[48:51], v0, s[34:35] offset:0 sc1\n"
"v_mfma_f32_16x16x32_bf16 v[172:175], a[76:79], a[100:103], v[172:175]\n"
"v_mfma_f32_16x16x32_bf16 v[172:175], a[72:75], a[96:99], v[172:175]\n"
"buffer_load_dwordx4 v0, s[48:51], s69 offen offset:0 sc1 lds\n"
"v_mfma_f32_16x16x32_bf16 v[176:179], a[72:75], a[104:107], v[176:179]\n"
"v_mfma_f32_16x16x32_bf16 v[176:179], a[76:79], a[108:111], v[176:179]\n"
"v_mfma_f32_16x16x32_bf16 v[148:151], a[68:71], a[108:111], v[148:151]\n"
"v_mfma_f32_16x16x32_bf16 v[148:151], a[64:67], a[104:107], v[148:151]\n"
"v_mfma_f32_16x16x32_bf16 v[152:155], a[64:67], a[112:115], v[152:155]\n"
"v_lshlrev_b32 v20, 16, v22\n"
"v_mfma_f32_16x16x32_bf16 v[152:155], a[68:71], a[116:119], v[152:155]\n"
"global_load_dwordx4 a[52:55], v0, s[34:35] offset:1024 sc1\n"
"v_and_b32 v21, s63, v22\n"
"v_mfma_f32_16x16x32_bf16 v[180:183], a[76:79], a[116:119], v[180:183]\n"
"v_lshlrev_b32 v22, 16, v23\n"
"v_mfma_f32_16x16x32_bf16 v[180:183], a[72:75], a[112:115], v[180:183]\n"
"v_and_b32 v23, s63, v23\n"
"v_mfma_f32_16x16x32_bf16 v[184:187], a[72:75], a[120:123], v[184:187]\n"
"v_add_f32 v116, v116, v20\n"
"v_mfma_f32_16x16x32_bf16 v[184:187], a[76:79], a[124:127], v[184:187]\n"
"v_add_f32 v117, v117, v21\n"
"v_add_f32 v118, v118, v22\n"
"v_mfma_f32_16x16x32_bf16 v[156:159], a[68:71], a[124:127], v[156:159]\n"
"v_add_f32 v119, v119, v23\n"
"v_mfma_f32_16x16x32_bf16 v[156:159], a[64:67], a[120:123], v[156:159]\n"
"v_add_f32 v120, v120, v20\n"
"v_mfma_f32_16x16x32_bf16 v[160:163], a[64:67], a[128:131], v[160:163]\n"
"v_add_f32 v121, v121, v21\n"
"v_mfma_f32_16x16x32_bf16 v[160:163], a[68:71], a[132:135], v[160:163]\n"
"global_load_dwordx4 a[56:59], v0, s[34:35] offset:2048 sc1\n"
"v_add_f32 v122, v122, v22\n"
"v_mfma_f32_16x16x32_bf16 v[188:191], a[76:79], a[132:135], v[188:191]\n"
"v_add_f32 v123, v123, v23\n"
"v_add_f32 v124, v124, v20\n"
"v_mfma_f32_16x16x32_bf16 v[188:191], a[72:75], a[128:131], v[188:191]\n"
"buffer_load_dwordx4 v0, s[48:51], s69 offen offset:1024 sc1 lds\n"
"v_mfma_f32_16x16x32_bf16 v[192:195], a[72:75], a[136:139], v[192:195]\n"
"v_add_f32 v125, v125, v21\n"
"v_mfma_f32_16x16x32_bf16 v[192:195], a[76:79], a[140:143], v[192:195]\n"
"v_add_f32 v126, v126, v22\n"
"v_mfma_f32_16x16x32_bf16 v[164:167], a[68:71], a[140:143], v[164:167]\n"
"v_add_f32 v127, v127, v23\n"
"v_mfma_f32_16x16x32_bf16 v[164:167], a[64:67], a[136:139], v[164:167]\n"
"v_add_f32 v128, v128, v20\n"
"v_add_f32 v129, v129, v21\n"
"v_mfma_f32_16x16x32_bf16 v[168:171], a[64:67], a[144:147], v[168:171]\n"
"v_add_f32 v130, v130, v22\n"
"v_mfma_f32_16x16x32_bf16 v[168:171], a[68:71], a[148:151], v[168:171]\n"
"global_load_dwordx4 a[60:63], v0, s[34:35] offset:3072 sc1\n"
"v_add_f32 v131, v131, v23\n"
"v_mfma_f32_16x16x32_bf16 v[196:199], a[76:79], a[148:151], v[196:199]\n"
"v_add_f32 v132, v132, v20\n"
"v_mfma_f32_16x16x32_bf16 v[196:199], a[72:75], a[144:147], v[196:199]\n"
"v_add_f32 v133, v133, v21\n"
"v_mfma_f32_16x16x32_bf16 a[20:23], a[72:75], a[152:155], a[20:23]\n"
"s_add_u32 s34, s34, 8192\n"
"v_add_f32 v134, v134, v22\n"
"v_mfma_f32_16x16x32_bf16 a[20:23], a[76:79], a[156:159], a[20:23]\n"
"v_add_f32 v135, v135, v23\n"
"v_mfma_f32_16x16x32_bf16 a[16:19], a[68:71], a[156:159], a[16:19]\n"
"v_add_f32 v136, v136, v20\n"
"v_mfma_f32_16x16x32_bf16 a[16:19], a[64:67], a[152:155], a[16:19]\n"
"s_waitcnt vmcnt(11)\n"
"v_add_f32 v137, v137, v21\n"
"v_mfma_f32_16x16x32_bf16 v[200:203], a[80:83], a[96:99], v[200:203]\n"
"v_add_f32 v138, v138, v22\n"
"v_add_f32 v139, v139, v23\n"
"v_mfma_f32_16x16x32_bf16 v[200:203], a[84:87], a[100:103], v[200:203]\n"
"global_load_dwordx4 a[64:67], v0, s[34:35] offset:-4096 sc1\n"
"v_add_f32 v140, v140, v20\n"
"v_mfma_f32_16x16x32_bf16 v[228:231], a[92:95], a[100:103], v[228:231]\n"
"ds_read_b128 a[100:103], v4 offset:1024\n"
"v_add_f32 v141, v141, v21\n"
"v_mfma_f32_16x16x32_bf16 v[228:231], a[88:91], a[96:99], v[228:231]\n"
"ds_read_b128 a[96:99], v4 offset:0\n"
"v_add_f32 v142, v142, v22\n"
"v_mfma_f32_16x16x32_bf16 v[232:235], a[88:91], a[104:107], v[232:235]\n"
"buffer_load_dwordx4 v0, s[48:51], s69 offen offset:2048 sc1 lds\n"
"v_mfma_f32_16x16x32_bf16 v[232:235], a[92:95], a[108:111], v[232:235]\n"
"v_add_f32 v143, v143, v23\n"
"v_accvgpr_read_b32 v12, a12\n"
"v_mfma_f32_16x16x32_bf16 v[204:207], a[84:87], a[108:111], v[204:207]\n"
"ds_read_b128 a[108:111], v4 offset:3072\n"
"v_accvgpr_read_b32 v13, a13\n"
"v_mfma_f32_16x16x32_bf16 v[204:207], a[80:83], a[104:107], v[204:207]\n"
"ds_read_b128 a[104:107], v4 offset:2048\n"
"v_accvgpr_read_b32 v14, a14\n"
"v_mfma_f32_16x16x32_bf16 v[208:211], a[80:83], a[112:115], v[208:211]\n"
"v_accvgpr_read_b32 v15, a15\n"
"v_mfma_f32_16x16x32_bf16 v[208:211], a[84:87], a[116:119], v[208:211]\n"
"global_load_dwordx4 a[68:71], v0, s[34:35] offset:-3072 sc1\n"
"v_add_f32 v12, v12, v20\n"
"v_mfma_f32_16x16x32_bf16 v[236:239], a[92:95], a[116:119], v[236:239]\n"
"ds_read_b128 a[116:119], v4 offset:5120\n"
"v_add_f32 v13, v13, v21\n"
"v_mfma_f32_16x16x32_bf16 v[236:239], a[88:91], a[112:115], v[236:239]\n"
"ds_read_b128 a[112:115], v4 offset:4096\n"
"v_add_f32 v14, v14, v22\n"
"v_mfma_f32_16x16x32_bf16 v[240:243], a[88:91], a[120:123], v[240:243]\n"
"v_add_f32 v15, v15, v23\n"
"v_accvgpr_write_b32 a12, v12\n"
"v_mfma_f32_16x16x32_bf16 v[240:243], a[92:95], a[124:127], v[240:243]\n"
"v_accvgpr_write_b32 a13, v13\n"
"v_mfma_f32_16x16x32_bf16 v[212:215], a[84:87], a[124:127], v[212:215]\n"
"ds_read_b128 a[124:127], v4 offset:7168\n"
"v_accvgpr_write_b32 a14, v14\n"
"v_mfma_f32_16x16x32_bf16 v[212:215], a[80:83], a[120:123], v[212:215]\n"
"ds_read_b128 a[120:123], v4 offset:6144\n"
"v_accvgpr_write_b32 a15, v15\n"
"v_mfma_f32_16x16x32_bf16 v[216:219], a[80:83], a[128:131], v[216:219]\n"
"v_mfma_f32_16x16x32_bf16 v[216:219], a[84:87], a[132:135], v[216:219]\n"
"global_load_dwordx4 a[72:75], v0, s[34:35] offset:-2048 sc1\n"
"v_mfma_f32_16x16x32_bf16 v[244:247], a[92:95], a[132:135], v[244:247]\n"
"ds_read_b128 a[132:135], v4 offset:9216\n"
"v_mfma_f32_16x16x32_bf16 v[244:247], a[88:91], a[128:131], v[244:247]\n"
"ds_read_b128 a[128:131], v4 offset:8192\n"
"v_mfma_f32_16x16x32_bf16 v[248:251], a[88:91], a[136:139], v[248:251]\n"
"buffer_load_dwordx4 v0, s[48:51], s69 offen offset:3072 sc1 lds\n"
"v_mfma_f32_16x16x32_bf16 v[248:251], a[92:95], a[140:143], v[248:251]\n"
"v_mfma_f32_16x16x32_bf16 v[220:223], a[84:87], a[140:143], v[220:223]\n"
"ds_read_b128 a[140:143], v4 offset:11264\n"
"v_mfma_f32_16x16x32_bf16 v[220:223], a[80:83], a[136:139], v[220:223]\n"
"ds_read_b128 a[136:139], v4 offset:10240\n"
"v_mfma_f32_16x16x32_bf16 v[224:227], a[80:83], a[144:147], v[224:227]\n"
"v_mfma_f32_16x16x32_bf16 v[224:227], a[84:87], a[148:151], v[224:227]\n"
"global_load_dwordx4 a[76:79], v0, s[34:35] offset:-1024 sc1\n"
"v_mfma_f32_16x16x32_bf16 v[252:255], a[92:95], a[148:151], v[252:255]\n"
"L_st7361:\n"
"s_cbranch_execz L_st8230\n"
"ds_read_b128 a[148:151], v4 offset:13312\n"
"v_mfma_f32_16x16x32_bf16 v[252:255], a[88:91], a[144:147], v[252:255]\n"
"ds_read_b128 a[144:147], v4 offset:12288\n"
"v_mfma_f32_16x16x32_bf16 a[28:31], a[88:91], a[152:155], a[28:31]\n"
"v_mfma_f32_16x16x32_bf16 a[28:31], a[92:95], a[156:159], a[28:31]\n"
"v_mfma_f32_16x16x32_bf16 a[24:27], a[84:87], a[156:159], a[24:27]\n"
"ds_read_b128 a[156:159], v4 offset:15360\n"
"v_mfma_f32_16x16x32_bf16 a[24:27], a[80:83], a[152:155], a[24:27]\n"
"s_waitcnt vmcnt(12) lgkmcnt(11)\n"
"ds_read_b128 a[152:155], v4 offset:14336\n"
"v_mfma_f32_16x16x32_bf16 v[32:35], a[32:35], a[96:99], v[32:35]\n"
"v_mfma_f32_16x16x32_bf16 v[32:35], a[36:39], a[100:103], v[32:35]\n"
"global_load_dwordx4 a[80:83], v0, s[34:35] offset:0 sc1\n"
"v_lshrrev_b32 v16, 4, v0\n"
"v_mfma_f32_16x16x32_bf16 v[60:63], a[44:47], a[100:103], v[60:63]\n"
"v_and_b32 v16, 0x30, v16\n"
"v_mfma_f32_16x16x32_bf16 v[60:63], a[40:43], a[96:99], v[60:63]\n"
"s_add_u32 m0, s64, 28672\n"
"buffer_load_dwordx2 v[18:19], v16, s[44:47], s61 offen offset:128\n"
"v_mfma_f32_16x16x32_bf16 v[64:67], a[40:43], a[104:107], v[64:67]\n"
"v_mfma_f32_16x16x32_bf16 v[64:67], a[44:47], a[108:111], v[64:67]\n"
"v_mfma_f32_16x16x32_bf16 v[36:39], a[36:39], a[108:111], v[36:39]\n"
"v_mfma_f32_16x16x32_bf16 v[36:39], a[32:35], a[104:107], v[36:39]\n"
"s_waitcnt lgkmcnt(8)\n"
"v_mfma_f32_16x16x32_bf16 v[40:43], a[32:35], a[112:115], v[40:43]\n"
"v_mfma_f32_16x16x32_bf16 v[40:43], a[36:39], a[116:119], v[40:43]\n"
"global_load_dwordx4 a[84:87], v0, s[34:35] offset:1024 sc1\n"
"v_lshrrev_b32 v20, 4, v0\n"
"v_mfma_f32_16x16x32_bf16 v[68:71], a[44:47], a[116:119], v[68:71]\n"
"v_and_b32 v20, 0x30, v20\n"
"v_mfma_f32_16x16x32_bf16 v[68:71], a[40:43], a[112:115], v[68:71]\n"
"buffer_load_dwordx2 v[22:23], v20, s[44:47], s61 offen offset:136\n"
"v_mfma_f32_16x16x32_bf16 v[72:75], a[40:43], a[120:123], v[72:75]\n"
"v_mfma_f32_16x16x32_bf16 v[72:75], a[44:47], a[124:127], v[72:75]\n"
"v_mfma_f32_16x16x32_bf16 v[44:47], a[36:39], a[124:127], v[44:47]\n"
"v_mfma_f32_16x16x32_bf16 v[44:47], a[32:35], a[120:123], v[44:47]\n"
"s_waitcnt lgkmcnt(4)\n"
"v_mfma_f32_16x16x32_bf16 v[48:51], a[32:35], a[128:131], v[48:51]\n"
"v_mfma_f32_16x16x32_bf16 v[48:51], a[36:39], a[132:135], v[48:51]\n"
"global_load_dwordx4 a[88:91], v0, s[34:35] offset:2048 sc1\n"
"v_lshrrev_b32 v24, 4, v0\n"
"v_mfma_f32_16x16x32_bf16 v[76:79], a[44:47], a[132:135], v[76:79]\n"
"v_and_b32 v24, 0x30, v24\n"
"v_mfma_f32_16x16x32_bf16 v[76:79], a[40:43], a[128:131], v[76:79]\n"
"buffer_load_dwordx4 v1, s[48:51], s69 offen offset:0 sc1 lds\n"
"buffer_load_dwordx2 v[26:27], v24, s[44:47], s61 offen offset:192\n"
"v_mfma_f32_16x16x32_bf16 v[80:83], a[40:43], a[136:139], v[80:83]\n"
"v_mfma_f32_16x16x32_bf16 v[80:83], a[44:47], a[140:143], v[80:83]\n"
"v_mfma_f32_16x16x32_bf16 v[52:55], a[36:39], a[140:143], v[52:55]\n"
"v_mfma_f32_16x16x32_bf16 v[52:55], a[32:35], a[136:139], v[52:55]\n"
"s_waitcnt lgkmcnt(0)\n"
"v_mfma_f32_16x16x32_bf16 v[56:59], a[32:35], a[144:147], v[56:59]\n"
"v_mfma_f32_16x16x32_bf16 v[56:59], a[36:39], a[148:151], v[56:59]\n"
"global_load_dwordx4 a[92:95], v0, s[34:35] offset:3072 sc1\n"
"v_lshrrev_b32 v28, 4, v0\n"
"v_mfma_f32_16x16x32_bf16 v[84:87], a[44:47], a[148:151], v[84:87]\n"
"v_and_b32 v28, 0x30, v28\n"
"v_mfma_f32_16x16x32_bf16 v[84:87], a[40:43], a[144:147], v[84:87]\n"
"buffer_load_dwordx2 v[30:31], v28, s[44:47], s61 offen offset:200\n"
"v_mfma_f32_16x16x32_bf16 a[4:7], a[40:43], a[152:155], a[4:7]\n"
"s_add_u32 s34, s34, 8192\n"
"v_mfma_f32_16x16x32_bf16 a[4:7], a[44:47], a[156:159], a[4:7]\n"
"v_mfma_f32_16x16x32_bf16 a[0:3], a[36:39], a[156:159], a[0:3]\n"
"v_mfma_f32_16x16x32_bf16 a[0:3], a[32:35], a[152:155], a[0:3]\n"
"s_waitcnt vmcnt(15)\n"
"v_mfma_f32_16x16x32_bf16 v[88:91], a[48:51], a[96:99], v[88:91]\n"
"v_mfma_f32_16x16x32_bf16 v[88:91], a[52:55], a[100:103], v[88:91]\n"
"global_load_dwordx4 a[32:35], v0, s[34:35] offset:-4096 sc1\n"
"v_mfma_f32_16x16x32_bf16 v[116:119], a[60:63], a[100:103], v[116:119]\n"
"v_mfma_f32_16x16x32_bf16 v[116:119], a[56:59], a[96:99], v[116:119]\n"
"buffer_load_dwordx4 v1, s[48:51], s69 offen offset:1024 sc1 lds\n"
"v_mfma_f32_16x16x32_bf16 v[120:123], a[56:59], a[104:107], v[120:123]\n"
"v_mfma_f32_16x16x32_bf16 v[120:123], a[60:63], a[108:111], v[120:123]\n"
"v_mfma_f32_16x16x32_bf16 v[92:95], a[52:55], a[108:111], v[92:95]\n"
"v_mfma_f32_16x16x32_bf16 v[92:95], a[48:51], a[104:107], v[92:95]\n"
"v_mfma_f32_16x16x32_bf16 v[96:99], a[48:51], a[112:115], v[96:99]\n"
"v_mfma_f32_16x16x32_bf16 v[96:99], a[52:55], a[116:119], v[96:99]\n"
"global_load_dwordx4 a[36:39], v0, s[34:35] offset:-3072 sc1\n"
"v_mfma_f32_16x16x32_bf16 v[124:127], a[60:63], a[116:119], v[124:127]\n"
"v_mfma_f32_16x16x32_bf16 v[124:127], a[56:59], a[112:115], v[124:127]\n"
"v_mfma_f32_16x16x32_bf16 v[128:131], a[56:59], a[120:123], v[128:131]\n"
"v_mfma_f32_16x16x32_bf16 v[128:131], a[60:63], a[124:127], v[128:131]\n"
"v_mfma_f32_16x16x32_bf16 v[100:103], a[52:55], a[124:127], v[100:103]\n"
"v_mfma_f32_16x16x32_bf16 v[100:103], a[48:51], a[120:123], v[100:103]\n"
"v_mfma_f32_16x16x32_bf16 v[104:107], a[48:51], a[128:131], v[104:107]\n"
"v_mfma_f32_16x16x32_bf16 v[104:107], a[52:55], a[132:135], v[104:107]\n"
"global_load_dwordx4 a[40:43], v0, s[34:35] offset:-2048 sc1\n"
"v_mfma_f32_16x16x32_bf16 v[132:135], a[60:63], a[132:135], v[132:135]\n"
"v_mfma_f32_16x16x32_bf16 v[132:135], a[56:59], a[128:131], v[132:135]\n"
"buffer_load_dwordx4 v1, s[48:51], s69 offen offset:2048 sc1 lds\n"
"v_mfma_f32_16x16x32_bf16 v[136:139], a[56:59], a[136:139], v[136:139]\n"
"v_mfma_f32_16x16x32_bf16 v[136:139], a[60:63], a[140:143], v[136:139]\n"
"v_mfma_f32_16x16x32_bf16 v[108:111], a[52:55], a[140:143], v[108:111]\n"
"v_mfma_f32_16x16x32_bf16 v[108:111], a[48:51], a[136:139], v[108:111]\n"
"v_mfma_f32_16x16x32_bf16 v[112:115], a[48:51], a[144:147], v[112:115]\n"
"v_mfma_f32_16x16x32_bf16 v[112:115], a[52:55], a[148:151], v[112:115]\n"
"global_load_dwordx4 a[44:47], v0, s[34:35] offset:-1024 sc1\n"
"v_mfma_f32_16x16x32_bf16 v[140:143], a[60:63], a[148:151], v[140:143]\n"
"v_mfma_f32_16x16x32_bf16 v[140:143], a[56:59], a[144:147], v[140:143]\n"
"v_mfma_f32_16x16x32_bf16 a[12:15], a[56:59], a[152:155], a[12:15]\n"
"v_mfma_f32_16x16x32_bf16 a[12:15], a[60:63], a[156:159], a[12:15]\n"
"v_mfma_f32_16x16x32_bf16 a[8:11], a[52:55], a[156:159], a[8:11]\n"
"v_mfma_f32_16x16x32_bf16 a[8:11], a[48:51], a[152:155], a[8:11]\n"
"s_waitcnt vmcnt(15)\n"
"v_mfma_f32_16x16x32_bf16 v[144:147], a[64:67], a[96:99], v[144:147]\n"
"v_mfma_f32_16x16x32_bf16 v[144:147], a[68:71], a[100:103], v[144:147]\n"
"global_load_dwordx4 a[48:51], v0, s[34:35] offset:0 sc1\n"
"v_mfma_f32_16x16x32_bf16 v[172:175], a[76:79], a[100:103], v[172:175]\n"
"v_mfma_f32_16x16x32_bf16 v[172:175], a[72:75], a[96:99], v[172:175]\n"
"buffer_load_dwordx4 v1, s[48:51], s69 offen offset:3072 sc1 lds\n"
"v_mfma_f32_16x16x32_bf16 v[176:179], a[72:75], a[104:107], v[176:179]\n"
"v_mfma_f32_16x16x32_bf16 v[176:179], a[76:79], a[108:111], v[176:179]\n"
"v_mfma_f32_16x16x32_bf16 v[148:151], a[68:71], a[108:111], v[148:151]\n"
"v_mfma_f32_16x16x32_bf16 v[148:151], a[64:67], a[104:107], v[148:151]\n"
"v_mfma_f32_16x16x32_bf16 v[152:155], a[64:67], a[112:115], v[152:155]\n"
"v_mfma_f32_16x16x32_bf16 v[152:155], a[68:71], a[116:119], v[152:155]\n"
"global_load_dwordx4 a[52:55], v0, s[34:35] offset:1024 sc1\n"
"v_mfma_f32_16x16x32_bf16 v[180:183], a[76:79], a[116:119], v[180:183]\n"
"v_mfma_f32_16x16x32_bf16 v[180:183], a[72:75], a[112:115], v[180:183]\n"
"v_mfma_f32_16x16x32_bf16 v[184:187], a[72:75], a[120:123], v[184:187]\n"
"v_mfma_f32_16x16x32_bf16 v[184:187], a[76:79], a[124:127], v[184:187]\n"
"v_mfma_f32_16x16x32_bf16 v[156:159], a[68:71], a[124:127], v[156:159]\n"
"v_mfma_f32_16x16x32_bf16 v[156:159], a[64:67], a[120:123], v[156:159]\n"
"v_mfma_f32_16x16x32_bf16 v[160:163], a[64:67], a[128:131], v[160:163]\n"
"v_mfma_f32_16x16x32_bf16 v[160:163], a[68:71], a[132:135], v[160:163]\n"
"global_load_dwordx4 a[56:59], v0, s[34:35] offset:2048 sc1\n"
"v_mfma_f32_16x16x32_bf16 v[188:191], a[76:79], a[132:135], v[188:191]\n"
"v_mfma_f32_16x16x32_bf16 v[188:191], a[72:75], a[128:131], v[188:191]\n"
"v_mfma_f32_16x16x32_bf16 v[192:195], a[72:75], a[136:139], v[192:195]\n"
"s_cmp_lg_u32 s29, 0\n"
"s_cselect_b32 s60, s31, s60\n"
"s_cselect_b32 s30, 0, s30\n"
"v_mfma_f32_16x16x32_bf16 v[192:195], a[76:79], a[140:143], v[192:195]\n"
"v_mfma_f32_16x16x32_bf16 v[164:167], a[68:71], a[140:143], v[164:167]\n"
"s_cmp_lg_u32 s29, 0\n"
"s_cselect_b32 s31, s75, s72\n"
"v_mfma_f32_16x16x32_bf16 v[164:167], a[64:67], a[136:139], v[164:167]\n"
"s_sub_u32 s52, s52, s72\n"
"s_subb_u32 s53, s53, 0\n"
"v_mfma_f32_16x16x32_bf16 v[168:171], a[64:67], a[144:147], v[168:171]\n"
"s_add_u32 s52, s52, s31\n"
"s_addc_u32 s53, s53, 0\n"
"v_mfma_f32_16x16x32_bf16 v[168:171], a[68:71], a[148:151], v[168:171]\n"
"global_load_dwordx4 a[60:63], v0, s[34:35] offset:3072 sc1\n"
"v_mfma_f32_16x16x32_bf16 v[196:199], a[76:79], a[148:151], v[196:199]\n"
"s_mov_b32 s72, s31\n"
"v_mfma_f32_16x16x32_bf16 v[196:199], a[72:75], a[144:147], v[196:199]\n"
"v_mfma_f32_16x16x32_bf16 a[20:23], a[72:75], a[152:155], a[20:23]\n"
"s_add_u32 s34, s34, 8192\n"
"v_mfma_f32_16x16x32_bf16 a[20:23], a[76:79], a[156:159], a[20:23]\n"
"v_mfma_f32_16x16x32_bf16 a[16:19], a[68:71], a[156:159], a[16:19]\n"
"s_cmp_lg_u32 s29, 0\n"
"s_cselect_b32 s31, s69, s73\n"
"v_mfma_f32_16x16x32_bf16 a[16:19], a[64:67], a[152:155], a[16:19]\n"
"s_sub_u32 s54, s54, s73\n"
"s_subb_u32 s55, s55, 0\n"
"s_waitcnt vmcnt(12)\n"
"v_mfma_f32_16x16x32_bf16 v[200:203], a[80:83], a[96:99], v[200:203]\n"
"s_add_u32 s54, s54, s31\n"
"s_addc_u32 s55, s55, 0\n"
"v_mfma_f32_16x16x32_bf16 v[200:203], a[84:87], a[100:103], v[200:203]\n"
"global_load_dwordx4 a[64:67], v0, s[34:35] offset:-4096 sc1\n"
"v_mfma_f32_16x16x32_bf16 v[228:231], a[92:95], a[100:103], v[228:231]\n"
"s_mov_b32 s73, s31\n"
"ds_read_b128 a[100:103], v5 offset:1024\n"
"v_mfma_f32_16x16x32_bf16 v[228:231], a[88:91], a[96:99], v[228:231]\n"
"ds_read_b128 a[96:99], v5 offset:0\n"
"v_mfma_f32_16x16x32_bf16 v[232:235], a[88:91], a[104:107], v[232:235]\n"
"v_mfma_f32_16x16x32_bf16 v[232:235], a[92:95], a[108:111], v[232:235]\n"
"v_mfma_f32_16x16x32_bf16 v[204:207], a[84:87], a[108:111], v[204:207]\n"
"ds_read_b128 a[108:111], v5 offset:3072\n"
"v_mfma_f32_16x16x32_bf16 v[204:207], a[80:83], a[104:107], v[204:207]\n"
"ds_read_b128 a[104:107], v5 offset:2048\n"
"v_mfma_f32_16x16x32_bf16 v[208:211], a[80:83], a[112:115], v[208:211]\n"
"v_mfma_f32_16x16x32_bf16 v[208:211], a[84:87], a[116:119], v[208:211]\n"
"global_load_dwordx4 a[68:71], v0, s[34:35] offset:-3072 sc1\n"
"v_mfma_f32_16x16x32_bf16 v[236:239], a[92:95], a[116:119], v[236:239]\n"
"ds_read_b128 a[116:119], v5 offset:5120\n"
"v_mfma_f32_16x16x32_bf16 v[236:239], a[88:91], a[112:115], v[236:239]\n"
"ds_read_b128 a[112:115], v5 offset:4096\n"
"v_mfma_f32_16x16x32_bf16 v[240:243], a[88:91], a[120:123], v[240:243]\n"
"v_mfma_f32_16x16x32_bf16 v[240:243], a[92:95], a[124:127], v[240:243]\n"
"v_mfma_f32_16x16x32_bf16 v[212:215], a[84:87], a[124:127], v[212:215]\n"
"ds_read_b128 a[124:127], v5 offset:7168\n"
"v_mfma_f32_16x16x32_bf16 v[212:215], a[80:83], a[120:123], v[212:215]\n"
"ds_read_b128 a[120:123], v5 offset:6144\n"
"v_mfma_f32_16x16x32_bf16 v[216:219], a[80:83], a[128:131], v[216:219]\n"
"v_mfma_f32_16x16x32_bf16 v[216:219], a[84:87], a[132:135], v[216:219]\n"
"global_load_dwordx4 a[72:75], v0, s[34:35] offset:-2048 sc1\n"
"v_mfma_f32_16x16x32_bf16 v[244:247], a[92:95], a[132:135], v[244:247]\n"
"ds_read_b128 a[132:135], v5 offset:9216\n"
"v_mfma_f32_16x16x32_bf16 v[244:247], a[88:91], a[128:131], v[244:247]\n"
"ds_read_b128 a[128:131], v5 offset:8192\n"
"v_mfma_f32_16x16x32_bf16 v[248:251], a[88:91], a[136:139], v[248:251]\n"
"v_mfma_f32_16x16x32_bf16 v[248:251], a[92:95], a[140:143], v[248:251]\n"
"v_mfma_f32_16x16x32_bf16 v[220:223], a[84:87], a[140:143], v[220:223]\n"
"ds_read_b128 a[140:143], v5 offset:11264\n"
"v_mfma_f32_16x16x32_bf16 v[220:223], a[80:83], a[136:139], v[220:223]\n"
"ds_read_b128 a[136:139], v5 offset:10240\n"
"v_mfma_f32_16x16x32_bf16 v[224:227], a[80:83], a[144:147], v[224:227]\n"
"v_mfma_f32_16x16x32_bf16 v[224:227], a[84:87], a[148:151], v[224:227]\n"
"global_load_dwordx4 a[76:79], v0, s[34:35] offset:-1024 sc1\n"
"v_mfma_f32_16x16x32_bf16 v[252:255], a[92:95], a[148:151], v[252:255]\n"
"ds_read_b128 a[148:151], v5 offset:13312\n"
"v_mfma_f32_16x16x32_bf16 v[252:255], a[88:91], a[144:147], v[252:255]\n"
"ds_read_b128 a[144:147], v5 offset:12288\n"
"v_mfma_f32_16x16x32_bf16 a[28:31], a[88:91], a[152:155], a[28:31]\n"
"v_mfma_f32_16x16x32_bf16 a[28:31], a[92:95], a[156:159], a[28:31]\n"
"v_mfma_f32_16x16x32_bf16 a[24:27], a[84:87], a[156:159], a[24:27]\n"
"ds_read_b128 a[156:159], v5 offset:15360\n"
"v_mfma_f32_16x16x32_bf16 a[24:27], a[80:83], a[152:155], a[24:27]\n"
"s_waitcnt vmcnt(9) lgkmcnt(11)\n"
"ds_read_b128 a[152:155], v5 offset:14336\n"
"v_mfma_f32_16x16x32_bf16 v[32:35], a[32:35], a[96:99], v[32:35]\n"
"v_mfma_f32_16x16x32_bf16 v[32:35], a[36:39], a[100:103], v[32:35]\n"
"global_load_dwordx4 a[80:83], v0, s[34:35] offset:0 sc1\n"
"v_mfma_f32_16x16x32_bf16 v[60:63], a[44:47], a[100:103], v[60:63]\n"
"v_mfma_f32_16x16x32_bf16 v[60:63], a[40:43], a[96:99], v[60:63]\n"
"v_mfma_f32_16x16x32_bf16 v[64:67], a[40:43], a[104:107], v[64:67]\n"
"v_mfma_f32_16x16x32_bf16 v[64:67], a[44:47], a[108:111], v[64:67]\n"
"v_mfma_f32_16x16x32_bf16 v[36:39], a[36:39], a[108:111], v[36:39]\n"
"v_mfma_f32_16x16x32_bf16 v[36:39], a[32:35], a[104:107], v[36:39]\n"
"s_waitcnt lgkmcnt(8)\n"
"v_mfma_f32_16x16x32_bf16 v[40:43], a[32:35], a[112:115], v[40:43]\n"
"v_mfma_f32_16x16x32_bf16 v[40:43], a[36:39], a[116:119], v[40:43]\n"
"global_load_dwordx4 a[84:87], v0, s[34:35] offset:1024 sc1\n"
"v_mfma_f32_16x16x32_bf16 v[68:71], a[44:47], a[116:119], v[68:71]\n"
"v_mfma_f32_16x16x32_bf16 v[68:71], a[40:43], a[112:115], v[68:71]\n"
"v_mfma_f32_16x16x32_bf16 v[72:75], a[40:43], a[120:123], v[72:75]\n"
"v_mfma_f32_16x16x32_bf16 v[72:75], a[44:47], a[124:127], v[72:75]\n"
"v_mfma_f32_16x16x32_bf16 v[44:47], a[36:39], a[124:127], v[44:47]\n"
"v_mfma_f32_16x16x32_bf16 v[44:47], a[32:35], a[120:123], v[44:47]\n"
"s_waitcnt lgkmcnt(4)\n"
"v_mfma_f32_16x16x32_bf16 v[48:51], a[32:35], a[128:131], v[48:51]\n"
"s_lshr_b32 s66, 20, s62\n"
"v_mfma_f32_16x16x32_bf16 v[48:51], a[36:39], a[132:135], v[48:51]\n"
"global_load_dwordx4 a[88:91], v0, s[34:35] offset:2048 sc1\n"
"v_mfma_f32_16x16x32_bf16 v[76:79], a[44:47], a[132:135], v[76:79]\n"
"s_and_b32 s66, s66, 1\n"
"s_cselect_b32 s70, 4294574080, 0\n"
"s_cselect_b32 s71, -1, 0\n"
"s_cselect_b32 s66, 0, 131072\n"
"v_mfma_f32_16x16x32_bf16 v[76:79], a[40:43], a[128:131], v[76:79]\n"
"v_mfma_f32_16x16x32_bf16 v[80:83], a[40:43], a[136:139], v[80:83]\n"
"s_add_u32 s70, s70, s66\n"
"v_mfma_f32_16x16x32_bf16 v[80:83], a[44:47], a[140:143], v[80:83]\n"
"v_mfma_f32_16x16x32_bf16 v[52:55], a[36:39], a[140:143], v[52:55]\n"
"v_mfma_f32_16x16x32_bf16 v[52:55], a[32:35], a[136:139], v[52:55]\n"
"s_waitcnt lgkmcnt(0)\n"
"v_mfma_f32_16x16x32_bf16 v[56:59], a[32:35], a[144:147], v[56:59]\n"
"v_mfma_f32_16x16x32_bf16 v[56:59], a[36:39], a[148:151], v[56:59]\n"
"global_load_dwordx4 a[92:95], v0, s[34:35] offset:3072 sc1\n"
"v_mfma_f32_16x16x32_bf16 v[84:87], a[44:47], a[148:151], v[84:87]\n"
"v_mfma_f32_16x16x32_bf16 v[84:87], a[40:43], a[144:147], v[84:87]\n"
"v_mfma_f32_16x16x32_bf16 a[4:7], a[40:43], a[152:155], a[4:7]\n"
"s_add_u32 s34, s34, 8192\n"
"v_mfma_f32_16x16x32_bf16 a[4:7], a[44:47], a[156:159], a[4:7]\n"
"v_mfma_f32_16x16x32_bf16 a[0:3], a[36:39], a[156:159], a[0:3]\n"
"v_mfma_f32_16x16x32_bf16 a[0:3], a[32:35], a[152:155], a[0:3]\n"
"s_waitcnt vmcnt(8)\n"
"v_mfma_f32_16x16x32_bf16 v[88:91], a[48:51], a[96:99], v[88:91]\n"
"v_mfma_f32_16x16x32_bf16 v[88:91], a[52:55], a[100:103], v[88:91]\n"
"global_load_dwordx4 a[32:35], v0, s[34:35] offset:-4096 sc1\n"
"v_mfma_f32_16x16x32_bf16 v[116:119], a[60:63], a[100:103], v[116:119]\n"
"v_mfma_f32_16x16x32_bf16 v[116:119], a[56:59], a[96:99], v[116:119]\n"
"v_mfma_f32_16x16x32_bf16 v[120:123], a[56:59], a[104:107], v[120:123]\n"
"v_mfma_f32_16x16x32_bf16 v[120:123], a[60:63], a[108:111], v[120:123]\n"
"v_mfma_f32_16x16x32_bf16 v[92:95], a[52:55], a[108:111], v[92:95]\n"
"v_mfma_f32_16x16x32_bf16 v[92:95], a[48:51], a[104:107], v[92:95]\n"
"v_mfma_f32_16x16x32_bf16 v[96:99], a[48:51], a[112:115], v[96:99]\n"
"v_mfma_f32_16x16x32_bf16 v[96:99], a[52:55], a[116:119], v[96:99]\n"
"global_load_dwordx4 a[36:39], v0, s[34:35] offset:-3072 sc1\n"
"v_mfma_f32_16x16x32_bf16 v[124:127], a[60:63], a[116:119], v[124:127]\n"
"buffer_load_dwordx4 v[12:15], v0, s[56:59], %25 offen sc1\n"
"v_mfma_f32_16x16x32_bf16 v[124:127], a[56:59], a[112:115], v[124:127]\n"
"v_mfma_f32_16x16x32_bf16 v[128:131], a[56:59], a[120:123], v[128:131]\n"
"v_mfma_f32_16x16x32_bf16 v[128:131], a[60:63], a[124:127], v[128:131]\n"
"v_mfma_f32_16x16x32_bf16 v[100:103], a[52:55], a[124:127], v[100:103]\n"
"v_mfma_f32_16x16x32_bf16 v[100:103], a[48:51], a[120:123], v[100:103]\n"
"v_mfma_f32_16x16x32_bf16 v[104:107], a[48:51], a[128:131], v[104:107]\n"
"v_mfma_f32_16x16x32_bf16 v[104:107], a[52:55], a[132:135], v[104:107]\n"
"global_load_dwordx4 a[40:43], v0, s[34:35] offset:-2048 sc1\n"
"v_mfma_f32_16x16x32_bf16 v[132:135], a[60:63], a[132:135], v[132:135]\n"
"v_mfma_f32_16x16x32_bf16 v[132:135], a[56:59], a[128:131], v[132:135]\n"
"v_mfma_f32_16x16x32_bf16 v[136:139], a[56:59], a[136:139], v[136:139]\n"
"v_mfma_f32_16x16x32_bf16 v[136:139], a[60:63], a[140:143], v[136:139]\n"
"v_mfma_f32_16x16x32_bf16 v[108:111], a[52:55], a[140:143], v[108:111]\n"
"v_mfma_f32_16x16x32_bf16 v[108:111], a[48:51], a[136:139], v[108:111]\n"
"v_mfma_f32_16x16x32_bf16 v[112:115], a[48:51], a[144:147], v[112:115]\n"
"v_mfma_f32_16x16x32_bf16 v[112:115], a[52:55], a[148:151], v[112:115]\n"
"global_load_dwordx4 a[44:47], v0, s[34:35] offset:-1024 sc1\n"
"v_mfma_f32_16x16x32_bf16 v[140:143], a[60:63], a[148:151], v[140:143]\n"
"v_mfma_f32_16x16x32_bf16 v[140:143], a[56:59], a[144:147], v[140:143]\n"
"v_mfma_f32_16x16x32_bf16 a[12:15], a[56:59], a[152:155], a[12:15]\n"
"v_mfma_f32_16x16x32_bf16 a[12:15], a[60:63], a[156:159], a[12:15]\n"
"v_mfma_f32_16x16x32_bf16 a[8:11], a[52:55], a[156:159], a[8:11]\n"
"v_mfma_f32_16x16x32_bf16 a[8:11], a[48:51], a[152:155], a[8:11]\n"
"s_waitcnt vmcnt(9)\n"
"v_mfma_f32_16x16x32_bf16 v[144:147], a[64:67], a[96:99], v[144:147]\n"
"v_mfma_f32_16x16x32_bf16 v[144:147], a[68:71], a[100:103], v[144:147]\n"
"global_load_dwordx4 a[48:51], v0, s[34:35] offset:0 sc1\n"
"v_mfma_f32_16x16x32_bf16 v[172:175], a[76:79], a[100:103], v[172:175]\n"
"v_mfma_f32_16x16x32_bf16 v[172:175], a[72:75], a[96:99], v[172:175]\n"
"v_mfma_f32_16x16x32_bf16 v[176:179], a[72:75], a[104:107], v[176:179]\n"
"v_mfma_f32_16x16x32_bf16 v[176:179], a[76:79], a[108:111], v[176:179]\n"
"v_mfma_f32_16x16x32_bf16 v[148:151], a[68:71], a[108:111], v[148:151]\n"
"v_mfma_f32_16x16x32_bf16 v[148:151], a[64:67], a[104:107], v[148:151]\n"
"v_mfma_f32_16x16x32_bf16 v[152:155], a[64:67], a[112:115], v[152:155]\n"
"v_mfma_f32_16x16x32_bf16 v[152:155], a[68:71], a[116:119], v[152:155]\n"
"global_load_dwordx4 a[52:55], v0, s[34:35] offset:1024 sc1\n"
"v_mfma_f32_16x16x32_bf16 v[180:183], a[76:79], a[116:119], v[180:183]\n"
"v_mfma_f32_16x16x32_bf16 v[180:183], a[72:75], a[112:115], v[180:183]\n"
"v_mfma_f32_16x16x32_bf16 v[184:187], a[72:75], a[120:123], v[184:187]\n"
"v_mfma_f32_16x16x32_bf16 v[184:187], a[76:79], a[124:127], v[184:187]\n"
"v_mfma_f32_16x16x32_bf16 v[156:159], a[68:71], a[124:127], v[156:159]\n"
"v_mfma_f32_16x16x32_bf16 v[156:159], a[64:67], a[120:123], v[156:159]\n"
"v_mfma_f32_16x16x32_bf16 v[160:163], a[64:67], a[128:131], v[160:163]\n"
"v_mfma_f32_16x16x32_bf16 v[160:163], a[68:71], a[132:135], v[160:163]\n"
"global_load_dwordx4 a[56:59], v0, s[34:35] offset:2048 sc1\n"
"v_mfma_f32_16x16x32_bf16 v[188:191], a[76:79], a[132:135], v[188:191]\n"
"v_mfma_f32_16x16x32_bf16 v[188:191], a[72:75], a[128:131], v[188:191]\n"
"v_mfma_f32_16x16x32_bf16 v[192:195], a[72:75], a[136:139], v[192:195]\n"
"v_mfma_f32_16x16x32_bf16 v[192:195], a[76:79], a[140:143], v[192:195]\n"
"v_mfma_f32_16x16x32_bf16 v[164:167], a[68:71], a[140:143], v[164:167]\n"
"s_add_u32 s67, s78, s62\n"
"v_mfma_f32_16x16x32_bf16 v[164:167], a[64:67], a[136:139], v[164:167]\n"
"v_mfma_f32_16x16x32_bf16 v[168:171], a[64:67], a[144:147], v[168:171]\n"
"s_add_u32 s67, s67, 1\n"
"v_mfma_f32_16x16x32_bf16 v[168:171], a[68:71], a[148:151], v[168:171]\n"
"global_load_dwordx4 a[60:63], v0, s[34:35] offset:3072 sc1\n"
"v_mfma_f32_16x16x32_bf16 v[196:199], a[76:79], a[148:151], v[196:199]\n"
"v_mfma_f32_16x16x32_bf16 v[196:199], a[72:75], a[144:147], v[196:199]\n"
"v_mfma_f32_16x16x32_bf16 a[20:23], a[72:75], a[152:155], a[20:23]\n"
"s_add_u32 s34, s34, 8192\n"
"v_mfma_f32_16x16x32_bf16 a[20:23], a[76:79], a[156:159], a[20:23]\n"
"v_mfma_f32_16x16x32_bf16 a[16:19], a[68:71], a[156:159], a[16:19]\n"
"v_mfma_f32_16x16x32_bf16 a[16:19], a[64:67], a[152:155], a[16:19]\n"
"s_waitcnt vmcnt(9)\n"
"v_mfma_f32_16x16x32_bf16 v[200:203], a[80:83], a[96:99], v[200:203]\n"
"v_mfma_f32_16x16x32_bf16 v[200:203], a[84:87], a[100:103], v[200:203]\n"
"global_load_dwordx4 a[64:67], v0, s[34:35] offset:-4096 sc1\n"
"v_mfma_f32_16x16x32_bf16 v[228:231], a[92:95], a[100:103], v[228:231]\n"
"ds_read_b128 a[100:103], v2 offset:17408\n"
"v_mfma_f32_16x16x32_bf16 v[228:231], a[88:91], a[96:99], v[228:231]\n"
"ds_read_b128 a[96:99], v2 offset:16384\n"
"v_mfma_f32_16x16x32_bf16 v[232:235], a[88:91], a[104:107], v[232:235]\n"
"v_mfma_f32_16x16x32_bf16 v[232:235], a[92:95], a[108:111], v[232:235]\n"
"v_mfma_f32_16x16x32_bf16 v[204:207], a[84:87], a[108:111], v[204:207]\n"
"ds_read_b128 a[108:111], v2 offset:19456\n"
"v_mfma_f32_16x16x32_bf16 v[204:207], a[80:83], a[104:107], v[204:207]\n"
"ds_read_b128 a[104:107], v2 offset:18432\n"
"v_mfma_f32_16x16x32_bf16 v[208:211], a[80:83], a[112:115], v[208:211]\n"
"v_mfma_f32_16x16x32_bf16 v[208:211], a[84:87], a[116:119], v[208:211]\n"
"global_load_dwordx4 a[68:71], v0, s[34:35] offset:-3072 sc1\n"
"v_mfma_f32_16x16x32_bf16 v[236:239], a[92:95], a[116:119], v[236:239]\n"
"ds_read_b128 a[116:119], v2 offset:21504\n"
"v_mfma_f32_16x16x32_bf16 v[236:239], a[88:91], a[112:115], v[236:239]\n"
"ds_read_b128 a[112:115], v2 offset:20480\n"
"v_mfma_f32_16x16x32_bf16 v[240:243], a[88:91], a[120:123], v[240:243]\n"
"v_mfma_f32_16x16x32_bf16 v[240:243], a[92:95], a[124:127], v[240:243]\n"
"v_mfma_f32_16x16x32_bf16 v[212:215], a[84:87], a[124:127], v[212:215]\n"
"ds_read_b128 a[124:127], v2 offset:23552\n"
"v_mfma_f32_16x16x32_bf16 v[212:215], a[80:83], a[120:123], v[212:215]\n"
"ds_read_b128 a[120:123], v2 offset:22528\n"
"v_mfma_f32_16x16x32_bf16 v[216:219], a[80:83], a[128:131], v[216:219]\n"
"v_mfma_f32_16x16x32_bf16 v[216:219], a[84:87], a[132:135], v[216:219]\n"
"global_load_dwordx4 a[72:75], v0, s[34:35] offset:-2048 sc1\n"
"v_mfma_f32_16x16x32_bf16 v[244:247], a[92:95], a[132:135], v[244:247]\n"
"ds_read_b128 a[132:135], v2 offset:25600\n"
"v_mfma_f32_16x16x32_bf16 v[244:247], a[88:91], a[128:131], v[244:247]\n"
"ds_read_b128 a[128:131], v2 offset:24576\n"
"v_mfma_f32_16x16x32_bf16 v[248:251], a[88:91], a[136:139], v[248:251]\n"
"v_mfma_f32_16x16x32_bf16 v[248:251], a[92:95], a[140:143], v[248:251]\n"
"v_mfma_f32_16x16x32_bf16 v[220:223], a[84:87], a[140:143], v[220:223]\n"
"ds_read_b128 a[140:143], v2 offset:27648\n"
"v_mfma_f32_16x16x32_bf16 v[220:223], a[80:83], a[136:139], v[220:223]\n"
"ds_read_b128 a[136:139], v2 offset:26624\n"
"v_mfma_f32_16x16x32_bf16 v[224:227], a[80:83], a[144:147], v[224:227]\n"
"v_mfma_f32_16x16x32_bf16 v[224:227], a[84:87], a[148:151], v[224:227]\n"
"global_load_dwordx4 a[76:79], v0, s[34:35] offset:-1024 sc1\n"
"v_mfma_f32_16x16x32_bf16 v[252:255], a[92:95], a[148:151], v[252:255]\n"
"ds_read_b128 a[148:151], v2 offset:29696\n"
"v_mfma_f32_16x16x32_bf16 v[252:255], a[88:91], a[144:147], v[252:255]\n"
"ds_read_b128 a[144:147], v2 offset:28672\n"
"v_mfma_f32_16x16x32_bf16 a[28:31], a[88:91], a[152:155], a[28:31]\n"
"v_mfma_f32_16x16x32_bf16 a[28:31], a[92:95], a[156:159], a[28:31]\n"
"v_mfma_f32_16x16x32_bf16 a[24:27], a[84:87], a[156:159], a[24:27]\n"
"ds_read_b128 a[156:159], v2 offset:31744\n"
"v_mfma_f32_16x16x32_bf16 a[24:27], a[80:83], a[152:155], a[24:27]\n"
"ds_read_b128 a[152:155], v2 offset:30720\n"
"s_waitcnt vmcnt(8) lgkmcnt(12)\n"
"v_mfma_f32_16x16x32_bf16 v[32:35], a[32:35], a[96:99], v[32:35]\n"
"v_mfma_f32_16x16x32_bf16 v[32:35], a[36:39], a[100:103], v[32:35]\n"
"global_load_dwordx4 a[80:83], v0, s[34:35] offset:0 sc1\n"
"v_mfma_f32_16x16x32_bf16 v[60:63], a[44:47], a[100:103], v[60:63]\n"
"s_barrier\n"
"v_mfma_f32_16x16x32_bf16 v[60:63], a[40:43], a[96:99], v[60:63]\n"
"s_add_u32 s48, s48, s70\n"
"s_addc_u32 s49, s49, s71\n"
"v_mfma_f32_16x16x32_bf16 v[64:67], a[40:43], a[104:107], v[64:67]\n"
"v_mfma_f32_16x16x32_bf16 v[64:67], a[44:47], a[108:111], v[64:67]\n"
"v_mfma_f32_16x16x32_bf16 v[36:39], a[36:39], a[108:111], v[36:39]\n"
"s_add_u32 s52, s52, s70\n"
"s_addc_u32 s53, s53, s71\n"
"v_subrev_u32 v12, s67, v12\n"
"v_mfma_f32_16x16x32_bf16 v[36:39], a[32:35], a[104:107], v[36:39]\n"
"s_waitcnt lgkmcnt(8)\n"
"v_xor_b32 v13, s79, v13\n"
"v_mfma_f32_16x16x32_bf16 v[40:43], a[32:35], a[112:115], v[40:43]\n"
"s_add_u32 s54, s54, s70\n"
"s_addc_u32 s55, s55, s71\n"
"v_subrev_u32 v14, s67, v14\n"
"v_mfma_f32_16x16x32_bf16 v[40:43], a[36:39], a[116:119], v[40:43]\n"
"global_load_dwordx4 a[84:87], v0, s[34:35] offset:1024 sc1\n"
"v_xor_b32 v15, s79, v15\n"
"v_mfma_f32_16x16x32_bf16 v[68:71], a[44:47], a[116:119], v[68:71]\n"
"v_or_b32 v12, v12, v14\n"
"v_mfma_f32_16x16x32_bf16 v[68:71], a[40:43], a[112:115], v[68:71]\n"
"v_or_b32 v13, v13, v15\n"
"v_mfma_f32_16x16x32_bf16 v[72:75], a[40:43], a[120:123], v[72:75]\n"
"v_or_b32 v12, v12, v13\n"
"v_mfma_f32_16x16x32_bf16 v[72:75], a[44:47], a[124:127], v[72:75]\n"
"s_add_u32 s61, s61, 2048\n"
"v_cmp_lt_u32 vcc, 15, v12\n"
"v_mfma_f32_16x16x32_bf16 v[44:47], a[36:39], a[124:127], v[44:47]\n"
"s_cbranch_vccnz L_slow2\n"
"L_back2:\n"
"v_mfma_f32_16x16x32_bf16 v[44:47], a[32:35], a[120:123], v[44:47]\n"
"s_waitcnt lgkmcnt(4)\n"
"v_mfma_f32_16x16x32_bf16 v[48:51], a[32:35], a[128:131], v[48:51]\n"
"v_lshlrev_b32 v16, 16, v18\n"
"v_mfma_f32_16x16x32_bf16 v[48:51], a[36:39], a[132:135], v[48:51]\n"
"global_load_dwordx4 a[88:91], v0, s[34:35] offset:2048 sc1\n"
"v_and_b32 v17, s63, v18\n"
"v_mfma_f32_16x16x32_bf16 v[76:79], a[44:47], a[132:135], v[76:79]\n"
"v_lshlrev_b32 v18, 16, v19\n"
"v_mfma_f32_16x16x32_bf16 v[76:79], a[40:43], a[128:131], v[76:79]\n"
"v_and_b32 v19, s63, v19\n"
"v_mfma_f32_16x16x32_bf16 v[80:83], a[40:43], a[136:139], v[80:83]\n"
"v_add_f32 v144, v144, v16\n"
"v_add_f32 v145, v145, v17\n"
"v_mfma_f32_16x16x32_bf16 v[80:83], a[44:47], a[140:143], v[80:83]\n"
"v_add_f32 v146, v146, v18\n"
"v_mfma_f32_16x16x32_bf16 v[52:55], a[36:39], a[140:143], v[52:55]\n"
"v_add_f32 v147, v147, v19\n"
"v_mfma_f32_16x16x32_bf16 v[52:55], a[32:35], a[136:139], v[52:55]\n"
"s_waitcnt lgkmcnt(0)\n"
"v_add_f32 v148, v148, v16\n"
"v_mfma_f32_16x16x32_bf16 v[56:59], a[32:35], a[144:147], v[56:59]\n"
"v_add_f32 v149, v149, v17\n"
"v_mfma_f32_16x16x32_bf16 v[56:59], a[36:39], a[148:151], v[56:59]\n"
"global_load_dwordx4 a[92:95], v0, s[34:35] offset:3072 sc1\n"
"v_add_f32 v150, v150, v18\n"
"v_mfma_f32_16x16x32_bf16 v[84:87], a[44:47], a[148:151], v[84:87]\n"
"v_add_f32 v151, v151, v19\n"
"v_mfma_f32_16x16x32_bf16 v[84:87], a[40:43], a[144:147], v[84:87]\n"
"v_add_f32 v152, v152, v16\n"
"v_mfma_f32_16x16x32_bf16 a[4:7], a[40:43], a[152:155], a[4:7]\n"
"s_add_u32 s34, s34, 8192\n"
"v_add_f32 v153, v153, v17\n"
"v_mfma_f32_16x16x32_bf16 a[4:7], a[44:47], a[156:159], a[4:7]\n"
"v_add_f32 v154, v154, v18\n"
"v_mfma_f32_16x16x32_bf16 a[0:3], a[36:39], a[156:159], a[0:3]\n"
"v_add_f32 v155, v155, v19\n"
"v_add_f32 v156, v156, v16\n"
"v_mfma_f32_16x16x32_bf16 a[0:3], a[32:35], a[152:155], a[0:3]\n"
"s_waitcnt vmcnt(8)\n"
"v_add_f32 v157, v157, v17\n"
"v_mfma_f32_16x16x32_bf16 v[88:91], a[48:51], a[96:99], v[88:91]\n"
"v_add_f32 v158, v158, v18\n"
"v_mfma_f32_16x16x32_bf16 v[88:91], a[52:55], a[100:103], v[88:91]\n"
"global_load_dwordx4 a[32:35], v0, s[34:35] offset:-4096 sc1\n"
"v_add_f32 v159, v159, v19\n"
"v_mfma_f32_16x16x32_bf16 v[116:119], a[60:63], a[100:103], v[116:119]\n"
"ds_read_b128 a[100:103], v3 offset:17408\n"
"v_add_f32 v160, v160, v16\n"
"v_mfma_f32_16x16x32_bf16 v[116:119], a[56:59], a[96:99], v[116:119]\n"
"ds_read_b128 a[96:99], v3 offset:16384\n"
"v_add_f32 v161, v161, v17\n"
"v_mfma_f32_16x16x32_bf16 v[120:123], a[56:59], a[104:107], v[120:123]\n"
"v_add_f32 v162, v162, v18\n"
"v_mfma_f32_16x16x32_bf16 v[120:123], a[60:63], a[108:111], v[120:123]\n"
"v_add_f32 v163, v163, v19\n"
"v_mfma_f32_16x16x32_bf16 v[92:95], a[52:55], a[108:111], v[92:95]\n"
"ds_read_b128 a[108:111], v3 offset:19456\n"
"v_add_f32 v164, v164, v16\n"
"v_mfma_f32_16x16x32_bf16 v[92:95], a[48:51], a[104:107], v[92:95]\n"
"ds_read_b128 a[104:107], v3 offset:18432\n"
"v_add_f32 v165, v165, v17\n"
"v_mfma_f32_16x16x32_bf16 v[96:99], a[48:51], a[112:115], v[96:99]\n"
"v_add_f32 v166, v166, v18\n"
"v_add_f32 v167, v167, v19\n"
"v_mfma_f32_16x16x32_bf16 v[96:99], a[52:55], a[116:119], v[96:99]\n"
"global_load_dwordx4 a[36:39], v0, s[34:35] offset:-3072 sc1\n"
"v_add_f32 v168, v168, v16\n"
"v_mfma_f32_16x16x32_bf16 v[124:127], a[60:63], a[116:119], v[124:127]\n"
"ds_read_b128 a[116:119], v3 offset:21504\n"
"v_add_f32 v169, v169, v17\n"
"v_mfma_f32_16x16x32_bf16 v[124:127], a[56:59], a[112:115], v[124:127]\n"
"ds_read_b128 a[112:115], v3 offset:20480\n"
"v_add_f32 v170, v170, v18\n"
"v_mfma_f32_16x16x32_bf16 v[128:131], a[56:59], a[120:123], v[128:131]\n"
"v_add_f32 v171, v171, v19\n"
"v_mfma_f32_16x16x32_bf16 v[128:131], a[60:63], a[124:127], v[128:131]\n"
"v_accvgpr_read_b32 v12, a16\n"
"v_mfma_f32_16x16x32_bf16 v[100:103], a[52:55], a[124:127], v[100:103]\n"
"ds_read_b128 a[124:127], v3 offset:23552\n"
"v_accvgpr_read_b32 v13, a17\n"
"v_mfma_f32_16x16x32_bf16 v[100:103], a[48:51], a[120:123], v[100:103]\n"
"ds_read_b128 a[120:123], v3 offset:22528\n"
"v_accvgpr_read_b32 v14, a18\n"
"v_mfma_f32_16x16x32_bf16 v[104:107], a[48:51], a[128:131], v[104:107]\n"
"v_accvgpr_read_b32 v15, a19\n"
"v_add_f32 v12, v12, v16\n"
"v_mfma_f32_16x16x32_bf16 v[104:107], a[52:55], a[132:135], v[104:107]\n"
"global_load_dwordx4 a[40:43], v0, s[34:35] offset:-2048 sc1\n"
"v_add_f32 v13, v13, v17\n"
"v_mfma_f32_16x16x32_bf16 v[132:135], a[60:63], a[132:135], v[132:135]\n"
"ds_read_b128 a[132:135], v3 offset:25600\n"
"v_add_f32 v14, v14, v18\n"
"v_mfma_f32_16x16x32_bf16 v[132:135], a[56:59], a[128:131], v[132:135]\n"
"ds_read_b128 a[128:131], v3 offset:24576\n"
"v_add_f32 v15, v15, v19\n"
"v_mfma_f32_16x16x32_bf16 v[136:139], a[56:59], a[136:139], v[136:139]\n"
"v_accvgpr_write_b32 a16, v12\n"
"v_mfma_f32_16x16x32_bf16 v[136:139], a[60:63], a[140:143], v[136:139]\n"
"v_accvgpr_write_b32 a17, v13\n"
"v_mfma_f32_16x16x32_bf16 v[108:111], a[52:55], a[140:143], v[108:111]\n"
"ds_read_b128 a[140:143], v3 offset:27648\n"
"v_accvgpr_write_b32 a18, v14\n"
"v_mfma_f32_16x16x32_bf16 v[108:111], a[48:51], a[136:139], v[108:111]\n"
"ds_read_b128 a[136:139], v3 offset:26624\n"
"v_accvgpr_write_b32 a19, v15\n"
"v_mfma_f32_16x16x32_bf16 v[112:115], a[48:51], a[144:147], v[112:115]\n"
"v_lshlrev_b32 v20, 16, v22\n"
"v_and_b32 v21, s63, v22\n"
"v_mfma_f32_16x16x32_bf16 v[112:115], a[52:55], a[148:151], v[112:115]\n"
"global_load_dwordx4 a[44:47], v0, s[34:35] offset:-1024 sc1\n"
"v_lshlrev_b32 v22, 16, v23\n"
"v_mfma_f32_16x16x32_bf16 v[140:143], a[60:63], a[148:151], v[140:143]\n"
"ds_read_b128 a[148:151], v3 offset:29696\n"
"v_and_b32 v23, s63, v23\n"
"v_mfma_f32_16x16x32_bf16 v[140:143], a[56:59], a[144:147], v[140:143]\n"
"ds_read_b128 a[144:147], v3 offset:28672\n"
"v_add_f32 v172, v172, v20\n"
"v_mfma_f32_16x16x32_bf16 a[12:15], a[56:59], a[152:155], a[12:15]\n"
"v_add_f32 v173, v173, v21\n"
"v_mfma_f32_16x16x32_bf16 a[12:15], a[60:63], a[156:159], a[12:15]\n"
"v_add_f32 v174, v174, v22\n"
"v_mfma_f32_16x16x32_bf16 a[8:11], a[52:55], a[156:159], a[8:11]\n"
"ds_read_b128 a[156:159], v3 offset:31744\n"
"v_add_f32 v175, v175, v23\n"
"v_mfma_f32_16x16x32_bf16 a[8:11], a[48:51], a[152:155], a[8:11]\n"
"s_waitcnt vmcnt(8) lgkmcnt(11)\n"
"ds_read_b128 a[152:155], v3 offset:30720\n"
"v_add_f32 v176, v176, v20\n"
"v_mfma_f32_16x16x32_bf16 v[32:35], a[64:67], a[96:99], v[32:35]\n"
"v_add_f32 v177, v177, v21\n"
"v_mfma_f32_16x16x32_bf16 v[32:35], a[68:71], a[100:103], v[32:35]\n"
"global_load_dwordx4 a[48:51], v0, s[34:35] offset:0 sc1\n"
"v_add_f32 v178, v178, v22\n"
"v_mfma_f32_16x16x32_bf16 v[60:63], a[76:79], a[100:103], v[60:63]\n"
"v_add_f32 v179, v179, v23\n"
"v_mfma_f32_16x16x32_bf16 v[60:63], a[72:75], a[96:99], v[60:63]\n"
"v_add_f32 v180, v180, v20\n"
"v_add_f32 v181, v181, v21\n"
"v_mfma_f32_16x16x32_bf16 v[64:67], a[72:75], a[104:107], v[64:67]\n"
"v_add_f32 v182, v182, v22\n"
"v_mfma_f32_16x16x32_bf16 v[64:67], a[76:79], a[108:111], v[64:67]\n"
"v_add_f32 v183, v183, v23\n"
"v_mfma_f32_16x16x32_bf16 v[36:39], a[68:71], a[108:111], v[36:39]\n"
"L_st7932:\n"
"s_cbranch_execz L_st9115\n"
"v_add_f32 v184, v184, v20\n"
"v_mfma_f32_16x16x32_bf16 v[36:39], a[64:67], a[104:107], v[36:39]\n"
"s_waitcnt lgkmcnt(8)\n"
"v_add_f32 v185, v185, v21\n"
"v_mfma_f32_16x16x32_bf16 v[40:43], a[64:67], a[112:115], v[40:43]\n"
"v_add_f32 v186, v186, v22\n"
"v_mfma_f32_16x16x32_bf16 v[40:43], a[68:71], a[116:119], v[40:43]\n"
"global_load_dwordx4 a[52:55], v0, s[34:35] offset:1024 sc1\n"
"v_add_f32 v187, v187, v23\n"
"v_mfma_f32_16x16x32_bf16 v[68:71], a[76:79], a[116:119], v[68:71]\n"
"v_add_f32 v188, v188, v20\n"
"v_mfma_f32_16x16x32_bf16 v[68:71], a[72:75], a[112:115], v[68:71]\n"
"v_add_f32 v189, v189, v21\n"
"v_mfma_f32_16x16x32_bf16 v[72:75], a[72:75], a[120:123], v[72:75]\n"
"v_add_f32 v190, v190, v22\n"
"v_add_f32 v191, v191, v23\n"
"v_mfma_f32_16x16x32_bf16 v[72:75], a[76:79], a[124:127], v[72:75]\n"
"v_add_f32 v192, v192, v20\n"
"v_mfma_f32_16x16x32_bf16 v[44:47], a[68:71], a[124:127], v[44:47]\n"
"v_add_f32 v193, v193, v21\n"
"v_mfma_f32_16x16x32_bf16 v[44:47], a[64:67], a[120:123], v[44:47]\n"
"s_waitcnt lgkmcnt(4)\n"
"v_add_f32 v194, v194, v22\n"
"v_mfma_f32_16x16x32_bf16 v[48:51], a[64:67], a[128:131], v[48:51]\n"
"v_add_f32 v195, v195, v23\n"
"v_mfma_f32_16x16x32_bf16 v[48:51], a[68:71], a[132:135], v[48:51]\n"
"global_load_dwordx4 a[56:59], v0, s[34:35] offset:2048 sc1\n"
"v_add_f32 v196, v196, v20\n"
"v_mfma_f32_16x16x32_bf16 v[76:79], a[76:79], a[132:135], v[76:79]\n"
"v_add_f32 v197, v197, v21\n"
"v_mfma_f32_16x16x32_bf16 v[76:79], a[72:75], a[128:131], v[76:79]\n"
"v_add_f32 v198, v198, v22\n"
"v_mfma_f32_16x16x32_bf16 v[80:83], a[72:75], a[136:139], v[80:83]\n"
"v_add_f32 v199, v199, v23\n"
"v_mfma_f32_16x16x32_bf16 v[80:83], a[76:79], a[140:143], v[80:83]\n"
"v_accvgpr_read_b32 v12, a20\n"
"v_mfma_f32_16x16x32_bf16 v[52:55], a[68:71], a[140:143], v[52:55]\n"
"v_accvgpr_read_b32 v13, a21\n"
"v_accvgpr_read_b32 v14, a22\n"
"v_mfma_f32_16x16x32_bf16 v[52:55], a[64:67], a[136:139], v[52:55]\n"
"s_waitcnt lgkmcnt(0)\n"
"v_accvgpr_read_b32 v15, a23\n"
"v_mfma_f32_16x16x32_bf16 v[56:59], a[64:67], a[144:147], v[56:59]\n"
"v_add_f32 v12, v12, v20\n"
"v_mfma_f32_16x16x32_bf16 v[56:59], a[68:71], a[148:151], v[56:59]\n"
"global_load_dwordx4 a[60:63], v0, s[34:35] offset:3072 sc1\n"
"v_add_f32 v13, v13, v21\n"
"v_mfma_f32_16x16x32_bf16 v[84:87], a[76:79], a[148:151], v[84:87]\n"
"v_add_f32 v14, v14, v22\n"
"v_mfma_f32_16x16x32_bf16 v[84:87], a[72:75], a[144:147], v[84:87]\n"
"v_add_f32 v15, v15, v23\n"
"v_mfma_f32_16x16x32_bf16 a[4:7], a[72:75], a[152:155], a[4:7]\n"
"s_add_u32 s34, s34, 8192\n"
"v_accvgpr_write_b32 a20, v12\n"
"v_mfma_f32_16x16x32_bf16 a[4:7], a[76:79], a[156:159], a[4:7]\n"
"v_accvgpr_write_b32 a21, v13\n"
"v_mfma_f32_16x16x32_bf16 a[0:3], a[68:71], a[156:159], a[0:3]\n"
"v_accvgpr_write_b32 a22, v14\n"
"v_mfma_f32_16x16x32_bf16 a[0:3], a[64:67], a[152:155], a[0:3]\n"
"s_waitcnt vmcnt(8)\n"
"v_accvgpr_write_b32 a23, v15\n"
"v_lshlrev_b32 v24, 16, v26\n"
"v_mfma_f32_16x16x32_bf16 v[88:91], a[80:83], a[96:99], v[88:91]\n"
"v_and_b32 v25, s63, v26\n"
"v_mfma_f32_16x16x32_bf16 v[88:91], a[84:87], a[100:103], v[88:91]\n"
"global_load_dwordx4 a[64:67], v0, s[34:35] offset:-4096 sc1\n"
"v_lshlrev_b32 v26, 16, v27\n"
"v_mfma_f32_16x16x32_bf16 v[116:119], a[92:95], a[100:103], v[116:119]\n"
"ds_read_b128 a[100:103], v4 offset:17408\n"
"v_and_b32 v27, s63, v27\n"
"v_mfma_f32_16x16x32_bf16 v[116:119], a[88:91], a[96:99], v[116:119]\n"
"ds_read_b128 a[96:99], v4 offset:16384\n"
"v_add_f32 v200, v200, v24\n"
"v_mfma_f32_16x16x32_bf16 v[120:123], a[88:91], a[104:107], v[120:123]\n"
"v_add_f32 v201, v201, v25\n"
"v_mfma_f32_16x16x32_bf16 v[120:123], a[92:95], a[108:111], v[120:123]\n"
"v_add_f32 v202, v202, v26\n"
"v_mfma_f32_16x16x32_bf16 v[92:95], a[84:87], a[108:111], v[92:95]\n"
"ds_read_b128 a[108:111], v4 offset:19456\n"
"v_add_f32 v203, v203, v27\n"
"v_mfma_f32_16x16x32_bf16 v[92:95], a[80:83], a[104:107], v[92:95]\n"
"ds_read_b128 a[104:107], v4 offset:18432\n"
"v_add_f32 v204, v204, v24\n"
"v_mfma_f32_16x16x32_bf16 v[96:99], a[80:83], a[112:115], v[96:99]\n"
"v_add_f32 v205, v205, v25\n"
"v_add_f32 v206, v206, v26\n"
"v_mfma_f32_16x16x32_bf16 v[96:99], a[84:87], a[116:119], v[96:99]\n"
"global_load_dwordx4 a[68:71], v0, s[34:35] offset:-3072 sc1\n"
"v_add_f32 v207, v207, v27\n"
"v_mfma_f32_16x16x32_bf16 v[124:127], a[92:95], a[116:119], v[124:127]\n"
"ds_read_b128 a[116:119], v4 offset:21504\n"
"v_add_f32 v208, v208, v24\n"
"v_mfma_f32_16x16x32_bf16 v[124:127], a[88:91], a[112:115], v[124:127]\n"
"ds_read_b128 a[112:115], v4 offset:20480\n"
"v_add_f32 v209, v209, v25\n"
"v_mfma_f32_16x16x32_bf16 v[128:131], a[88:91], a[120:123], v[128:131]\n"
"v_add_f32 v210, v210, v26\n"
"v_mfma_f32_16x16x32_bf16 v[128:131], a[92:95], a[124:127], v[128:131]\n"
"v_add_f32 v211, v211, v27\n"
"v_mfma_f32_16x16x32_bf16 v[100:103], a[84:87], a[124:127], v[100:103]\n"
"ds_read_b128 a[124:127], v4 offset:23552\n"
"v_add_f32 v212, v212, v24\n"
"v_mfma_f32_16x16x32_bf16 v[100:103], a[80:83], a[120:123], v[100:103]\n"
"ds_read_b128 a[120:123], v4 offset:22528\n"
"v_add_f32 v213, v213, v25\n"
"v_mfma_f32_16x16x32_bf16 v[104:107], a[80:83], a[128:131], v[104:107]\n"
"v_add_f32 v214, v214, v26\n"
"v_add_f32 v215, v215, v27\n"
"v_mfma_f32_16x16x32_bf16 v[104:107], a[84:87], a[132:135], v[104:107]\n"
"global_load_dwordx4 a[72:75], v0, s[34:35] offset:-2048 sc1\n"
"v_add_f32 v216, v216, v24\n"
"v_mfma_f32_16x16x32_bf16 v[132:135], a[92:95], a[132:135], v[132:135]\n"
"ds_read_b128 a[132:135], v4 offset:25600\n"
"v_add_f32 v217, v217, v25\n"
"v_mfma_f32_16x16x32_bf16 v[132:135], a[88:91], a[128:131], v[132:135]\n"
"ds_read_b128 a[128:131], v4 offset:24576\n"
"v_add_f32 v218, v218, v26\n"
"v_mfma_f32_16x16x32_bf16 v[136:139], a[88:91], a[136:139], v[136:139]\n"
"v_add_f32 v219, v219, v27\n"
"v_mfma_f32_16x16x32_bf16 v[136:139], a[92:95], a[140:143], v[136:139]\n"
"v_add_f32 v220, v220, v24\n"
"v_mfma_f32_16x16x32_bf16 v[108:111], a[84:87], a[140:143], v[108:111]\n"
"ds_read_b128 a[140:143], v4 offset:27648\n"
"v_add_f32 v221, v221, v25\n"
"v_mfma_f32_16x16x32_bf16 v[108:111], a[80:83], a[136:139], v[108:111]\n"
"ds_read_b128 a[136:139], v4 offset:26624\n"
"v_add_f32 v222, v222, v26\n"
"v_mfma_f32_16x16x32_bf16 v[112:115], a[80:83], a[144:147], v[112:115]\n"
"v_add_f32 v223, v223, v27\n"
"v_mfma_f32_16x16x32_bf16 v[112:115], a[84:87], a[148:151], v[112:115]\n"
"global_load_dwordx4 a[76:79], v0, s[34:35] offset:-1024 sc1\n"
"v_add_f32 v224, v224, v24\n"
"v_mfma_f32_16x16x32_bf16 v[140:143], a[92:95], a[148:151], v[140:143]\n"
"ds_read_b128 a[148:151], v4 offset:29696\n"
"v_add_f32 v225, v225, v25\n"
"v_mfma_f32_16x16x32_bf16 v[140:143], a[88:91], a[144:147], v[140:143]\n"
"ds_read_b128 a[144:147], v4 offset:28672\n"
"v_add_f32 v226, v226, v26\n"
"v_mfma_f32_16x16x32_bf16 a[12:15], a[88:91], a[152:155], a[12:15]\n"
"v_add_f32 v227, v227, v27\n"
"v_accvgpr_read_b32 v12, a24\n"
"v_mfma_f32_16x16x32_bf16 a[12:15], a[92:95], a[156:159], a[12:15]\n"
"v_accvgpr_read_b32 v13, a25\n"
"v_mfma_f32_16x16x32_bf16 a[8:11], a[84:87], a[156:159], a[8:11]\n"
"ds_read_b128 a[156:159], v4 offset:31744\n"
"v_accvgpr_read_b32 v14, a26\n"
"v_mfma_f32_16x16x32_bf16 a[8:11], a[80:83], a[152:155], a[8:11]\n"
"s_waitcnt vmcnt(8) lgkmcnt(11)\n"
"ds_read_b128 a[152:155], v4 offset:30720\n"
"v_accvgpr_read_b32 v15, a27\n"
"v_mfma_f32_16x16x32_bf16 v[32:35], a[32:35], a[96:99], v[32:35]\n"
"v_add_f32 v12, v12, v24\n"
"v_mfma_f32_16x16x32_bf16 v[32:35], a[36:39], a[100:103], v[32:35]\n"
"global_load_dwordx4 a[80:83], v0, s[34:35] offset:0 sc1\n"
"v_add_f32 v13, v13, v25\n"
"v_mfma_f32_16x16x32_bf16 v[60:63], a[44:47], a[100:103], v[60:63]\n"
"v_add_f32 v14, v14, v26\n"
"v_mfma_f32_16x16x32_bf16 v[60:63], a[40:43], a[96:99], v[60:63]\n"
"v_add_f32 v15, v15, v27\n"
"v_mfma_f32_16x16x32_bf16 v[64:67], a[40:43], a[104:107], v[64:67]\n"
"v_accvgpr_write_b32 a24, v12\n"
"v_accvgpr_write_b32 a25, v13\n"
"v_mfma_f32_16x16x32_bf16 v[64:67], a[44:47], a[108:111], v[64:67]\n"
"v_accvgpr_write_b32 a26, v14\n"
"v_mfma_f32_16x16x32_bf16 v[36:39], a[36:39], a[108:111], v[36:39]\n"
"v_accvgpr_write_b32 a27, v15\n"
"v_mfma_f32_16x16x32_bf16 v[36:39], a[32:35], a[104:107], v[36:39]\n"
"s_waitcnt lgkmcnt(8)\n"
"v_lshlrev_b32 v28, 16, v30\n"
"v_mfma_f32_16x16x32_bf16 v[40:43], a[32:35], a[112:115], v[40:43]\n"
"v_and_b32 v29, s63, v30\n"
"v_mfma_f32_16x16x32_bf16 v[40:43], a[36:39], a[116:119], v[40:43]\n"
"global_load_dwordx4 a[84:87], v0, s[34:35] offset:1024 sc1\n"
"v_lshlrev_b32 v30, 16, v31\n"
"v_mfma_f32_16x16x32_bf16 v[68:71], a[44:47], a[116:119], v[68:71]\n"
"v_and_b32 v31, s63, v31\n"
"v_mfma_f32_16x16x32_bf16 v[68:71], a[40:43], a[112:115], v[68:71]\n"
"v_add_f32 v228, v228, v28\n"
"v_mfma_f32_16x16x32_bf16 v[72:75], a[40:43], a[120:123], v[72:75]\n"
"v_add_f32 v229, v229, v29\n"
"v_mfma_f32_16x16x32_bf16 v[72:75], a[44:47], a[124:127], v[72:75]\n"
"v_add_f32 v230, v230, v30\n"
"v_mfma_f32_16x16x32_bf16 v[44:47], a[36:39], a[124:127], v[44:47]\n"
"v_add_f32 v231, v231, v31\n"
"v_add_f32 v232, v232, v28\n"
"v_mfma_f32_16x16x32_bf16 v[44:47], a[32:35], a[120:123], v[44:47]\n"
"s_waitcnt lgkmcnt(4)\n"
"v_add_f32 v233, v233, v29\n"
"v_mfma_f32_16x16x32_bf16 v[48:51], a[32:35], a[128:131], v[48:51]\n"
"v_add_f32 v234, v234, v30\n"
"v_mfma_f32_16x16x32_bf16 v[48:51], a[36:39], a[132:135], v[48:51]\n"
"global_load_dwordx4 a[88:91], v0, s[34:35] offset:2048 sc1\n"
"v_add_f32 v235, v235, v31\n"
"v_mfma_f32_16x16x32_bf16 v[76:79], a[44:47], a[132:135], v[76:79]\n"
"v_add_f32 v236, v236, v28\n"
"v_mfma_f32_16x16x32_bf16 v[76:79], a[40:43], a[128:131], v[76:79]\n"
"v_add_f32 v237, v237, v29\n"
"v_mfma_f32_16x16x32_bf16 v[80:83], a[40:43], a[136:139], v[80:83]\n"
"v_add_f32 v238, v238, v30\n"
"v_mfma_f32_16x16x32_bf16 v[80:83], a[44:47], a[140:143], v[80:83]\n"
"v_add_f32 v239, v239, v31\n"
"v_mfma_f32_16x16x32_bf16 v[52:55], a[36:39], a[140:143], v[52:55]\n"
"v_add_f32 v240, v240, v28\n"
"v_mfma_f32_16x16x32_bf16 v[52:55], a[32:35], a[136:139], v[52:55]\n"
"s_waitcnt lgkmcnt(0)\n"
"v_add_f32 v241, v241, v29\n"
"v_add_f32 v242, v242, v30\n"
"v_mfma_f32_16x16x32_bf16 v[56:59], a[32:35], a[144:147], v[56:59]\n"
"v_add_f32 v243, v243, v31\n"
"v_mfma_f32_16x16x32_bf16 v[56:59], a[36:39], a[148:151], v[56:59]\n"
"global_load_dwordx4 a[92:95], v0, s[34:35] offset:3072 sc1\n"
"v_add_f32 v244, v244, v28\n"
"v_mfma_f32_16x16x32_bf16 v[84:87], a[44:47], a[148:151], v[84:87]\n"
"v_add_f32 v245, v245, v29\n"
"v_mfma_f32_16x16x32_bf16 v[84:87], a[40:43], a[144:147], v[84:87]\n"
"v_add_f32 v246, v246, v30\n"
"v_mfma_f32_16x16x32_bf16 a[4:7], a[40:43], a[152:155], a[4:7]\n"
"s_add_u32 s34, s34, 8192\n"
"v_add_f32 v247, v247, v31\n"
"v_mfma_f32_16x16x32_bf16 a[4:7], a[44:47], a[156:159], a[4:7]\n"
"v_add_f32 v248, v248, v28\n"
"v_mfma_f32_16x16x32_bf16 a[0:3], a[36:39], a[156:159], a[0:3]\n"
"v_add_f32 v249, v249, v29\n"
"v_mfma_f32_16x16x32_bf16 a[0:3], a[32:35], a[152:155], a[0:3]\n"
"s_waitcnt vmcnt(8)\n"
"v_add_f32 v250, v250, v30\n"
"v_mfma_f32_16x16x32_bf16 v[88:91], a[48:51], a[96:99], v[88:91]\n"
"v_add_f32 v251, v251, v31\n"
"v_add_f32 v252, v252, v28\n"
"v_mfma_f32_16x16x32_bf16 v[88:91], a[52:55], a[100:103], v[88:91]\n"
"global_load_dwordx4 a[32:35], v0, s[34:35] offset:-4096 sc1\n"
"v_add_f32 v253, v253, v29\n"
"v_mfma_f32_16x16x32_bf16 v[116:119], a[60:63], a[100:103], v[116:119]\n"
"ds_read_b128 a[100:103], v5 offset:17408\n"
"v_add_f32 v254, v254, v30\n"
"v_mfma_f32_16x16x32_bf16 v[116:119], a[56:59], a[96:99], v[116:119]\n"
"ds_read_b128 a[96:99], v5 offset:16384\n"
"v_add_f32 v255, v255, v31\n"
"v_mfma_f32_16x16x32_bf16 v[120:123], a[56:59], a[104:107], v[120:123]\n"
"v_accvgpr_read_b32 v12, a28\n"
"v_mfma_f32_16x16x32_bf16 v[120:123], a[60:63], a[108:111], v[120:123]\n"
"v_accvgpr_read_b32 v13, a29\n"
"v_mfma_f32_16x16x32_bf16 v[92:95], a[52:55], a[108:111], v[92:95]\n"
"ds_read_b128 a[108:111], v5 offset:19456\n"
"v_accvgpr_read_b32 v14, a30\n"
"v_mfma_f32_16x16x32_bf16 v[92:95], a[48:51], a[104:107], v[92:95]\n"
"ds_read_b128 a[104:107], v5 offset:18432\n"
"v_accvgpr_read_b32 v15, a31\n"
"v_mfma_f32_16x16x32_bf16 v[96:99], a[48:51], a[112:115], v[96:99]\n"
"v_add_f32 v12, v12, v28\n"
"v_add_f32 v13, v13, v29\n"
"v_mfma_f32_16x16x32_bf16 v[96:99], a[52:55], a[116:119], v[96:99]\n"
"global_load_dwordx4 a[36:39], v0, s[34:35] offset:-3072 sc1\n"
"v_add_f32 v14, v14, v30\n"
"v_mfma_f32_16x16x32_bf16 v[124:127], a[60:63], a[116:119], v[124:127]\n"
"ds_read_b128 a[116:119], v5 offset:21504\n"
"v_add_f32 v15, v15, v31\n"
"v_mfma_f32_16x16x32_bf16 v[124:127], a[56:59], a[112:115], v[124:127]\n"
"ds_read_b128 a[112:115], v5 offset:20480\n"
"v_accvgpr_write_b32 a28, v12\n"
"v_mfma_f32_16x16x32_bf16 v[128:131], a[56:59], a[120:123], v[128:131]\n"
"v_accvgpr_write_b32 a29, v13\n"
"v_mfma_f32_16x16x32_bf16 v[128:131], a[60:63], a[124:127], v[128:131]\n"
"v_accvgpr_write_b32 a30, v14\n"
"v_mfma_f32_16x16x32_bf16 v[100:103], a[52:55], a[124:127], v[100:103]\n"
"ds_read_b128 a[124:127], v5 offset:23552\n"
"v_accvgpr_write_b32 a31, v15\n"
"v_mfma_f32_16x16x32_bf16 v[100:103], a[48:51], a[120:123], v[100:103]\n"
"ds_read_b128 a[120:123], v5 offset:22528\n"
"v_mfma_f32_16x16x32_bf16 v[104:107], a[48:51], a[128:131], v[104:107]\n"
"v_mfma_f32_16x16x32_bf16 v[104:107], a[52:55], a[132:135], v[104:107]\n"
"global_load_dwordx4 a[40:43], v0, s[34:35] offset:-2048 sc1\n"
"v_mfma_f32_16x16x32_bf16 v[132:135], a[60:63], a[132:135], v[132:135]\n"
"ds_read_b128 a[132:135], v5 offset:25600\n"
"v_mfma_f32_16x16x32_bf16 v[132:135], a[56:59], a[128:131], v[132:135]\n"
"ds_read_b128 a[128:131], v5 offset:24576\n"
"v_mfma_f32_16x16x32_bf16 v[136:139], a[56:59], a[136:139], v[136:139]\n"
"ds_read_b128 v[16:19], v7 offset:32768\n"
"v_mfma_f32_16x16x32_bf16 v[136:139], a[60:63], a[140:143], v[136:139]\n"
"v_mfma_f32_16x16x32_bf16 v[108:111], a[52:55], a[140:143], v[108:111]\n"
"ds_read_b128 a[140:143], v5 offset:27648\n"
"v_mfma_f32_16x16x32_bf16 v[108:111], a[48:51], a[136:139], v[108:111]\n"
"ds_read_b128 a[136:139], v5 offset:26624\n"
"v_mfma_f32_16x16x32_bf16 v[112:115], a[48:51], a[144:147], v[112:115]\n"
"v_mfma_f32_16x16x32_bf16 v[112:115], a[52:55], a[148:151], v[112:115]\n"
"global_load_dwordx4 a[44:47], v0, s[34:35] offset:-1024 sc1\n"
"v_mfma_f32_16x16x32_bf16 v[140:143], a[60:63], a[148:151], v[140:143]\n"
"ds_read_b128 a[148:151], v5 offset:29696\n"
"v_mfma_f32_16x16x32_bf16 v[140:143], a[56:59], a[144:147], v[140:143]\n"
"ds_read_b128 a[144:147], v5 offset:28672\n"
"v_mfma_f32_16x16x32_bf16 a[12:15], a[56:59], a[152:155], a[12:15]\n"
"v_mfma_f32_16x16x32_bf16 a[12:15], a[60:63], a[156:159], a[12:15]\n"
"v_mfma_f32_16x16x32_bf16 a[8:11], a[52:55], a[156:159], a[8:11]\n"
"ds_read_b128 a[156:159], v5 offset:31744\n"
"v_mfma_f32_16x16x32_bf16 a[8:11], a[48:51], a[152:155], a[8:11]\n"
"ds_read_b128 a[152:155], v5 offset:30720\n"
"s_branch L_P6\n"
"L_P3F:\n"
"L_st8230:\n"
"s_cbranch_execz L_retB\n"
"s_waitcnt vmcnt(10) lgkmcnt(13)\n"
"v_mfma_f32_16x16x32_bf16 v[32:35], a[32:35], a[96:99], v[32:35]\n"
"s_waitcnt lgkmcnt(6)\n"
"global_store_dwordx4 v0, v[24:27], s[52:53] offset:2048 sc1\n"
"s_cmp_lt_u32 s27, 12582912\n"
"s_cselect_b32 s38, 8192000, 0\n"
"s_cselect_b32 s42, 12582144, 0\n"
"v_mfma_f32_16x16x32_bf16 v[32:35], a[36:39], a[100:103], v[32:35]\n"
"global_load_dwordx4 a[80:83], v0, s[34:35] offset:0 sc1\n"
"v_mfma_f32_16x16x32_bf16 v[60:63], a[44:47], a[100:103], v[60:63]\n"
"s_barrier\n"
"v_mfma_f32_16x16x32_bf16 v[60:63], a[40:43], a[96:99], v[60:63]\n"
"s_add_u32 m0, s64, 12288\n"
"v_mfma_f32_16x16x32_bf16 v[64:67], a[40:43], a[104:107], v[64:67]\n"
"v_mfma_f32_16x16x32_bf16 v[64:67], a[44:47], a[108:111], v[64:67]\n"
"s_add_u32 s67, s27, 0\n"
"v_mfma_f32_16x16x32_bf16 v[36:39], a[36:39], a[108:111], v[36:39]\n"
"v_mfma_f32_16x16x32_bf16 v[36:39], a[32:35], a[104:107], v[36:39]\n"
"v_mfma_f32_16x16x32_bf16 v[40:43], a[32:35], a[112:115], v[40:43]\n"
"buffer_load_dwordx4 v6, s[40:43], s67 offen offset:0 lds\n"
"v_mfma_f32_16x16x32_bf16 v[40:43], a[36:39], a[116:119], v[40:43]\n"
"global_load_dwordx4 a[84:87], v0, s[34:35] offset:1024 sc1\n"
"v_mfma_f32_16x16x32_bf16 v[68:71], a[44:47], a[116:119], v[68:71]\n"
"v_mfma_f32_16x16x32_bf16 v[68:71], a[40:43], a[112:115], v[68:71]\n"
"s_add_u32 s67, s27, 15360\n"
"v_mfma_f32_16x16x32_bf16 v[72:75], a[40:43], a[120:123], v[72:75]\n"
"v_mfma_f32_16x16x32_bf16 v[72:75], a[44:47], a[124:127], v[72:75]\n"
"v_mfma_f32_16x16x32_bf16 v[44:47], a[36:39], a[124:127], v[44:47]\n"
"buffer_load_dwordx4 v6, s[40:43], s67 offen offset:1024 lds\n"
"v_mfma_f32_16x16x32_bf16 v[44:47], a[32:35], a[120:123], v[44:47]\n"
"s_waitcnt lgkmcnt(4)\n"
"v_mfma_f32_16x16x32_bf16 v[48:51], a[32:35], a[128:131], v[48:51]\n"
"v_mfma_f32_16x16x32_bf16 v[48:51], a[36:39], a[132:135], v[48:51]\n"
"global_load_dwordx4 a[88:91], v0, s[34:35] offset:2048 sc1\n"
"v_lshrrev_b32 v16, 4, v0\n"
"v_mfma_f32_16x16x32_bf16 v[76:79], a[44:47], a[132:135], v[76:79]\n"
"s_add_u32 s67, s27, 30720\n"
"v_and_b32 v16, 0x30, v16\n"
"v_mfma_f32_16x16x32_bf16 v[76:79], a[40:43], a[128:131], v[76:79]\n"
"ds_read_b128 v[28:31], v2 offset:23552\n"
"buffer_load_dwordx2 v[18:19], v16, s[44:47], s61 offen offset:0\n"
"v_mfma_f32_16x16x32_bf16 v[80:83], a[40:43], a[136:139], v[80:83]\n"
"buffer_load_dwordx2 v[8:9], off, s[56:59], %14 sc1\n"
"v_mfma_f32_16x16x32_bf16 v[80:83], a[44:47], a[140:143], v[80:83]\n"
"buffer_load_dwordx4 v6, s[40:43], s67 offen offset:2048 lds\n"
"v_mfma_f32_16x16x32_bf16 v[52:55], a[36:39], a[140:143], v[52:55]\n"
"v_mfma_f32_16x16x32_bf16 v[52:55], a[32:35], a[136:139], v[52:55]\n"
"s_waitcnt lgkmcnt(1)\n"
"v_mfma_f32_16x16x32_bf16 v[56:59], a[32:35], a[144:147], v[56:59]\n"
"s_add_u32 s67, s27, 46080\n"
"v_mfma_f32_16x16x32_bf16 v[56:59], a[36:39], a[148:151], v[56:59]\n"
"global_load_dwordx4 a[92:95], v0, s[34:35] offset:3072 sc1\n"
"v_lshrrev_b32 v20, 4, v0\n"
"v_mfma_f32_16x16x32_bf16 v[84:87], a[44:47], a[148:151], v[84:87]\n"
"v_and_b32 v20, 0x30, v20\n"
"v_mfma_f32_16x16x32_bf16 v[84:87], a[40:43], a[144:147], v[84:87]\n"
"buffer_load_dwordx4 v6, s[40:43], s67 offen offset:3072 lds\n"
"buffer_load_dwordx2 v[22:23], v20, s[44:47], s61 offen offset:8\n"
"v_mfma_f32_16x16x32_bf16 a[4:7], a[40:43], a[152:155], a[4:7]\n"
"s_add_u32 s34, s34, 8192\n"
"v_mfma_f32_16x16x32_bf16 a[4:7], a[44:47], a[156:159], a[4:7]\n"
"v_mfma_f32_16x16x32_bf16 a[0:3], a[36:39], a[156:159], a[0:3]\n"
"s_waitcnt lgkmcnt(0)\n"
"global_store_dwordx4 v0, v[28:31], s[52:53] offset:3072 sc1\n"
"v_mfma_f32_16x16x32_bf16 a[0:3], a[32:35], a[152:155], a[0:3]\n"
"s_waitcnt vmcnt(18)\n"
"v_mfma_f32_16x16x32_bf16 v[88:91], a[48:51], a[96:99], v[88:91]\n"
"v_mfma_f32_16x16x32_bf16 v[88:91], a[52:55], a[100:103], v[88:91]\n"
"global_load_dwordx4 a[32:35], v0, s[34:35] offset:-4096 sc1\n"
"v_mfma_f32_16x16x32_bf16 v[116:119], a[60:63], a[100:103], v[116:119]\n"
"v_mfma_f32_16x16x32_bf16 v[116:119], a[56:59], a[96:99], v[116:119]\n"
"v_mfma_f32_16x16x32_bf16 v[120:123], a[56:59], a[104:107], v[120:123]\n"
"v_mfma_f32_16x16x32_bf16 v[120:123], a[60:63], a[108:111], v[120:123]\n"
"v_mfma_f32_16x16x32_bf16 v[92:95], a[52:55], a[108:111], v[92:95]\n"
"v_mfma_f32_16x16x32_bf16 v[92:95], a[48:51], a[104:107], v[92:95]\n"
"v_mfma_f32_16x16x32_bf16 v[96:99], a[48:51], a[112:115], v[96:99]\n"
"v_mfma_f32_16x16x32_bf16 v[96:99], a[52:55], a[116:119], v[96:99]\n"
"global_load_dwordx4 a[36:39], v0, s[34:35] offset:-3072 sc1\n"
"v_mfma_f32_16x16x32_bf16 v[124:127], a[60:63], a[116:119], v[124:127]\n"
"v_mfma_f32_16x16x32_bf16 v[124:127], a[56:59], a[112:115], v[124:127]\n"
"v_mfma_f32_16x16x32_bf16 v[128:131], a[56:59], a[120:123], v[128:131]\n"
"v_mfma_f32_16x16x32_bf16 v[128:131], a[60:63], a[124:127], v[128:131]\n"
"v_mfma_f32_16x16x32_bf16 v[100:103], a[52:55], a[124:127], v[100:103]\n"
"v_mfma_f32_16x16x32_bf16 v[100:103], a[48:51], a[120:123], v[100:103]\n"
"v_mfma_f32_16x16x32_bf16 v[104:107], a[48:51], a[128:131], v[104:107]\n"
"ds_read_b128 v[24:27], v2 offset:24576\n"
"v_mfma_f32_16x16x32_bf16 v[104:107], a[52:55], a[132:135], v[104:107]\n"
"global_load_dwordx4 a[40:43], v0, s[34:35] offset:-2048 sc1\n"
"v_mfma_f32_16x16x32_bf16 v[132:135], a[60:63], a[132:135], v[132:135]\n"
"v_mfma_f32_16x16x32_bf16 v[132:135], a[56:59], a[128:131], v[132:135]\n"
"v_mfma_f32_16x16x32_bf16 v[136:139], a[56:59], a[136:139], v[136:139]\n"
"v_mfma_f32_16x16x32_bf16 v[136:139], a[60:63], a[140:143], v[136:139]\n"
"v_mfma_f32_16x16x32_bf16 v[108:111], a[52:55], a[140:143], v[108:111]\n"
"v_mfma_f32_16x16x32_bf16 v[108:111], a[48:51], a[136:139], v[108:111]\n"
"v_mfma_f32_16x16x32_bf16 v[112:115], a[48:51], a[144:147], v[112:115]\n"
"v_mfma_f32_16x16x32_bf16 v[112:115], a[52:55], a[148:151], v[112:115]\n"
"global_load_dwordx4 a[44:47], v0, s[34:35] offset:-1024 sc1\n"
"v_mfma_f32_16x16x32_bf16 v[140:143], a[60:63], a[148:151], v[140:143]\n"
"v_mfma_f32_16x16x32_bf16 v[140:143], a[56:59], a[144:147], v[140:143]\n"
"v_mfma_f32_16x16x32_bf16 a[12:15], a[56:59], a[152:155], a[12:15]\n"
"s_waitcnt lgkmcnt(0)\n"
"global_store_dwordx4 v0, v[24:27], s[54:55] offset:-4096 sc1\n"
"v_mfma_f32_16x16x32_bf16 a[12:15], a[60:63], a[156:159], a[12:15]\n"
"v_mfma_f32_16x16x32_bf16 a[8:11], a[52:55], a[156:159], a[8:11]\n"
"v_mfma_f32_16x16x32_bf16 a[8:11], a[48:51], a[152:155], a[8:11]\n"
"s_waitcnt vmcnt(18)\n"
"v_mfma_f32_16x16x32_bf16 v[144:147], a[64:67], a[96:99], v[144:147]\n"
"v_mfma_f32_16x16x32_bf16 v[144:147], a[68:71], a[100:103], v[144:147]\n"
"global_load_dwordx4 a[48:51], v0, s[34:35] offset:0 sc1\n"
"v_mfma_f32_16x16x32_bf16 v[172:175], a[76:79], a[100:103], v[172:175]\n"
"v_mfma_f32_16x16x32_bf16 v[172:175], a[72:75], a[96:99], v[172:175]\n"
"v_mfma_f32_16x16x32_bf16 v[176:179], a[72:75], a[104:107], v[176:179]\n"
"v_mfma_f32_16x16x32_bf16 v[176:179], a[76:79], a[108:111], v[176:179]\n"
"v_mfma_f32_16x16x32_bf16 v[148:151], a[68:71], a[108:111], v[148:151]\n"
"v_mfma_f32_16x16x32_bf16 v[148:151], a[64:67], a[104:107], v[148:151]\n"
"v_mfma_f32_16x16x32_bf16 v[152:155], a[64:67], a[112:115], v[152:155]\n"
"v_mfma_f32_16x16x32_bf16 v[152:155], a[68:71], a[116:119], v[152:155]\n"
"global_load_dwordx4 a[52:55], v0, s[34:35] offset:1024 sc1\n"
"v_mfma_f32_16x16x32_bf16 v[180:183], a[76:79], a[116:119], v[180:183]\n"
"v_mfma_f32_16x16x32_bf16 v[180:183], a[72:75], a[112:115], v[180:183]\n"
"v_mfma_f32_16x16x32_bf16 v[184:187], a[72:75], a[120:123], v[184:187]\n"
"v_mfma_f32_16x16x32_bf16 v[184:187], a[76:79], a[124:127], v[184:187]\n"
"v_mfma_f32_16x16x32_bf16 v[156:159], a[68:71], a[124:127], v[156:159]\n"
"v_mfma_f32_16x16x32_bf16 v[156:159], a[64:67], a[120:123], v[156:159]\n"
"ds_read_b128 v[28:31], v2 offset:26624\n"
"v_mfma_f32_16x16x32_bf16 v[160:163], a[64:67], a[128:131], v[160:163]\n"
"v_mfma_f32_16x16x32_bf16 v[160:163], a[68:71], a[132:135], v[160:163]\n"
"global_load_dwordx4 a[56:59], v0, s[34:35] offset:2048 sc1\n"
"v_mfma_f32_16x16x32_bf16 v[188:191], a[76:79], a[132:135], v[188:191]\n"
"v_mfma_f32_16x16x32_bf16 v[188:191], a[72:75], a[128:131], v[188:191]\n"
"v_mfma_f32_16x16x32_bf16 v[192:195], a[72:75], a[136:139], v[192:195]\n"
"v_mfma_f32_16x16x32_bf16 v[192:195], a[76:79], a[140:143], v[192:195]\n"
"v_mfma_f32_16x16x32_bf16 v[164:167], a[68:71], a[140:143], v[164:167]\n"
"v_mfma_f32_16x16x32_bf16 v[164:167], a[64:67], a[136:139], v[164:167]\n"
"v_mfma_f32_16x16x32_bf16 v[168:171], a[64:67], a[144:147], v[168:171]\n"
"v_mfma_f32_16x16x32_bf16 v[168:171], a[68:71], a[148:151], v[168:171]\n"
"global_load_dwordx4 a[60:63], v0, s[34:35] offset:3072 sc1\n"
"v_mfma_f32_16x16x32_bf16 v[196:199], a[76:79], a[148:151], v[196:199]\n"
"s_waitcnt lgkmcnt(0)\n"
"global_store_dwordx4 v0, v[28:31], s[54:55] offset:-2048 sc1\n"
"v_mfma_f32_16x16x32_bf16 v[196:199], a[72:75], a[144:147], v[196:199]\n"
"v_mfma_f32_16x16x32_bf16 a[20:23], a[72:75], a[152:155], a[20:23]\n"
"s_add_u32 s34, s34, 8192\n"
"v_mfma_f32_16x16x32_bf16 a[20:23], a[76:79], a[156:159], a[20:23]\n"
"v_mfma_f32_16x16x32_bf16 a[16:19], a[68:71], a[156:159], a[16:19]\n"
"v_mfma_f32_16x16x32_bf16 a[16:19], a[64:67], a[152:155], a[16:19]\n"
"s_waitcnt vmcnt(13)\n"
"v_mfma_f32_16x16x32_bf16 v[200:203], a[80:83], a[96:99], v[200:203]\n"
"v_mfma_f32_16x16x32_bf16 v[200:203], a[84:87], a[100:103], v[200:203]\n"
"global_load_dwordx4 a[64:67], v0, s[34:35] offset:-4096 sc1\n"
"v_mfma_f32_16x16x32_bf16 v[228:231], a[92:95], a[100:103], v[228:231]\n"
"ds_read_b128 a[100:103], v3 offset:17408\n"
"v_mfma_f32_16x16x32_bf16 v[228:231], a[88:91], a[96:99], v[228:231]\n"
"ds_read_b128 a[96:99], v3 offset:16384\n"
"v_mfma_f32_16x16x32_bf16 v[232:235], a[88:91], a[104:107], v[232:235]\n"
"v_mfma_f32_16x16x32_bf16 v[232:235], a[92:95], a[108:111], v[232:235]\n"
"v_mfma_f32_16x16x32_bf16 v[204:207], a[84:87], a[108:111], v[204:207]\n"
"ds_read_b128 a[108:111], v3 offset:19456\n"
"v_mfma_f32_16x16x32_bf16 v[204:207], a[80:83], a[104:107], v[204:207]\n"
"ds_read_b128 a[104:107], v3 offset:18432\n"
"v_mfma_f32_16x16x32_bf16 v[208:211], a[80:83], a[112:115], v[208:211]\n"
"v_mfma_f32_16x16x32_bf16 v[208:211], a[84:87], a[116:119], v[208:211]\n"
"global_load_dwordx4 a[68:71], v0, s[34:35] offset:-3072 sc1\n"
"v_mfma_f32_16x16x32_bf16 v[236:239], a[92:95], a[116:119], v[236:239]\n"
"ds_read_b128 a[116:119], v3 offset:21504\n"
"v_mfma_f32_16x16x32_bf16 v[236:239], a[88:91], a[112:115], v[236:239]\n"
"ds_read_b128 a[112:115], v3 offset:20480\n"
"v_mfma_f32_16x16x32_bf16 v[240:243], a[88:91], a[120:123], v[240:243]\n"
"v_mfma_f32_16x16x32_bf16 v[240:243], a[92:95], a[124:127], v[240:243]\n"
"ds_read_b128 v[24:27], v2 offset:29696\n"
"v_mfma_f32_16x16x32_bf16 v[212:215], a[84:87], a[124:127], v[212:215]\n"
"ds_read_b128 a[124:127], v3 offset:23552\n"
"v_mfma_f32_16x16x32_bf16 v[212:215], a[80:83], a[120:123], v[212:215]\n"
"ds_read_b128 a[120:123], v3 offset:22528\n"
"v_mfma_f32_16x16x32_bf16 v[216:219], a[80:83], a[128:131], v[216:219]\n"
"v_mfma_f32_16x16x32_bf16 v[216:219], a[84:87], a[132:135], v[216:219]\n"
"global_load_dwordx4 a[72:75], v0, s[34:35] offset:-2048 sc1\n"
"v_mfma_f32_16x16x32_bf16 v[244:247], a[92:95], a[132:135], v[244:247]\n"
"ds_read_b128 a[132:135], v3 offset:25600\n"
"v_mfma_f32_16x16x32_bf16 v[244:247], a[88:91], a[128:131], v[244:247]\n"
"ds_read_b128 a[128:131], v3 offset:24576\n"
"v_mfma_f32_16x16x32_bf16 v[248:251], a[88:91], a[136:139], v[248:251]\n"
"v_subrev_u32 v8, s65, v8\n"
"v_mfma_f32_16x16x32_bf16 v[248:251], a[92:95], a[140:143], v[248:251]\n"
"v_xor_b32 v9, s79, v9\n"
"v_mfma_f32_16x16x32_bf16 v[220:223], a[84:87], a[140:143], v[220:223]\n"
"ds_read_b128 a[140:143], v3 offset:27648\n"
"v_or_b32 v8, v8, v9\n"
"v_mfma_f32_16x16x32_bf16 v[220:223], a[80:83], a[136:139], v[220:223]\n"
"ds_read_b128 a[136:139], v3 offset:26624\n"
"v_cmp_lt_u32 vcc, 15, v8\n"
"v_mfma_f32_16x16x32_bf16 v[224:227], a[80:83], a[144:147], v[224:227]\n"
"s_waitcnt lgkmcnt(6)\n"
"global_store_dwordx4 v0, v[24:27], s[54:55] offset:1024 sc1\n"
"s_cbranch_vccnz L_slow7\n"
"L_back7:\n"
"v_mfma_f32_16x16x32_bf16 v[224:227], a[84:87], a[148:151], v[224:227]\n"
"global_load_dwordx4 a[76:79], v0, s[34:35] offset:-1024 sc1\n"
"v_readfirstlane_b32 s66, v9\n"
"v_mfma_f32_16x16x32_bf16 v[252:255], a[92:95], a[148:151], v[252:255]\n"
"ds_read_b128 a[148:151], v3 offset:29696\n"
"v_mfma_f32_16x16x32_bf16 v[252:255], a[88:91], a[144:147], v[252:255]\n"
"s_and_b32 s66, s66, 3\n"
"ds_read_b128 a[144:147], v3 offset:28672\n"
"v_mfma_f32_16x16x32_bf16 a[28:31], a[88:91], a[152:155], a[28:31]\n"
"s_lshl_b32 s69, s66, 13\n"
"v_mfma_f32_16x16x32_bf16 a[28:31], a[92:95], a[156:159], a[28:31]\n"
"s_cmp_eq_u32 s66, 3\n"
"s_cselect_b32 s75, 8192, 0\n"
"v_mfma_f32_16x16x32_bf16 a[24:27], a[84:87], a[156:159], a[24:27]\n"
"s_and_b32 s29, s66, s30\n"
"ds_read_b128 a[156:159], v3 offset:31744\n"
"v_mfma_f32_16x16x32_bf16 a[24:27], a[80:83], a[152:155], a[24:27]\n"
"s_waitcnt vmcnt(11)\n"
"ds_read_b128 a[152:155], v3 offset:30720\n"
"v_mfma_f32_16x16x32_bf16 v[32:35], a[32:35], a[96:99], v[32:35]\n"
"s_xor_b32 s31, s79, s66\n"
"v_mfma_f32_16x16x32_bf16 v[32:35], a[36:39], a[100:103], v[32:35]\n"
"global_load_dwordx4 a[80:83], v0, s[34:35] offset:0 sc1\n"
"v_mfma_f32_16x16x32_bf16 v[60:63], a[44:47], a[100:103], v[60:63]\n"
"v_mfma_f32_16x16x32_bf16 v[60:63], a[40:43], a[96:99], v[60:63]\n"
"v_mfma_f32_16x16x32_bf16 v[64:67], a[40:43], a[104:107], v[64:67]\n"
"v_mfma_f32_16x16x32_bf16 v[64:67], a[44:47], a[108:111], v[64:67]\n"
"v_mfma_f32_16x16x32_bf16 v[36:39], a[36:39], a[108:111], v[36:39]\n"
"s_add_u32 m0, s64, 0\n"
"v_mfma_f32_16x16x32_bf16 v[36:39], a[32:35], a[104:107], v[36:39]\n"
"s_waitcnt lgkmcnt(8)\n"
"v_mfma_f32_16x16x32_bf16 v[40:43], a[32:35], a[112:115], v[40:43]\n"
"v_mfma_f32_16x16x32_bf16 v[40:43], a[36:39], a[116:119], v[40:43]\n"
"global_load_dwordx4 a[84:87], v0, s[34:35] offset:1024 sc1\n"
"v_mfma_f32_16x16x32_bf16 v[68:71], a[44:47], a[116:119], v[68:71]\n"
"v_mfma_f32_16x16x32_bf16 v[68:71], a[40:43], a[112:115], v[68:71]\n"
"v_mfma_f32_16x16x32_bf16 v[72:75], a[40:43], a[120:123], v[72:75]\n"
"v_mfma_f32_16x16x32_bf16 v[72:75], a[44:47], a[124:127], v[72:75]\n"
"v_and_b32 v10, 0x300, v0\n"
"v_mfma_f32_16x16x32_bf16 v[44:47], a[36:39], a[124:127], v[44:47]\n"
"v_mfma_f32_16x16x32_bf16 v[44:47], a[32:35], a[120:123], v[44:47]\n"
"s_waitcnt lgkmcnt(4)\n"
"v_mfma_f32_16x16x32_bf16 v[48:51], a[32:35], a[128:131], v[48:51]\n"
"v_and_b32 v11, 0x3c0, v0\n"
"v_mfma_f32_16x16x32_bf16 v[48:51], a[36:39], a[132:135], v[48:51]\n"
"global_load_dwordx4 a[88:91], v0, s[34:35] offset:2048 sc1\n"
"v_mfma_f32_16x16x32_bf16 v[76:79], a[44:47], a[132:135], v[76:79]\n"
"v_mfma_f32_16x16x32_bf16 v[76:79], a[40:43], a[128:131], v[76:79]\n"
"v_mfma_f32_16x16x32_bf16 v[80:83], a[40:43], a[136:139], v[80:83]\n"
"v_add_lshl_u32 v11, v10, v11, 1\n"
"v_mfma_f32_16x16x32_bf16 v[80:83], a[44:47], a[140:143], v[80:83]\n"
"buffer_load_dwordx4 v0, s[48:51], s75 offen offset:0 sc1 lds\n"
"v_mfma_f32_16x16x32_bf16 v[52:55], a[36:39], a[140:143], v[52:55]\n"
"v_mfma_f32_16x16x32_bf16 v[52:55], a[32:35], a[136:139], v[52:55]\n"
"s_waitcnt lgkmcnt(0)\n"
"v_mfma_f32_16x16x32_bf16 v[56:59], a[32:35], a[144:147], v[56:59]\n"
"v_lshrrev_b32 v10, 2, v0\n"
"v_mfma_f32_16x16x32_bf16 v[56:59], a[36:39], a[148:151], v[56:59]\n"
"global_load_dwordx4 a[92:95], v0, s[34:35] offset:3072 sc1\n"
"v_mfma_f32_16x16x32_bf16 v[84:87], a[44:47], a[148:151], v[84:87]\n"
"v_mfma_f32_16x16x32_bf16 v[84:87], a[40:43], a[144:147], v[84:87]\n"
"v_xor_b32 v10, v10, v0\n"
"v_mfma_f32_16x16x32_bf16 a[4:7], a[40:43], a[152:155], a[4:7]\n"
"s_add_u32 s34, s34, 8192\n"
"v_mfma_f32_16x16x32_bf16 a[4:7], a[44:47], a[156:159], a[4:7]\n"
"v_mfma_f32_16x16x32_bf16 a[0:3], a[36:39], a[156:159], a[0:3]\n"
"v_mfma_f32_16x16x32_bf16 a[0:3], a[32:35], a[152:155], a[0:3]\n"
"s_waitcnt vmcnt(11)\n"
"v_and_b32 v10, 0x30, v10\n"
"v_mfma_f32_16x16x32_bf16 v[88:91], a[48:51], a[96:99], v[88:91]\n"
"v_mfma_f32_16x16x32_bf16 v[88:91], a[52:55], a[100:103], v[88:91]\n"
"global_load_dwordx4 a[32:35], v0, s[34:35] offset:-4096 sc1\n"
"v_mfma_f32_16x16x32_bf16 v[116:119], a[60:63], a[100:103], v[116:119]\n"
"v_add3_u32 v11, v10, v11, s64\n"
"v_mfma_f32_16x16x32_bf16 v[116:119], a[56:59], a[96:99], v[116:119]\n"
"buffer_load_dwordx4 v0, s[48:51], s75 offen offset:1024 sc1 lds\n"
"v_mfma_f32_16x16x32_bf16 v[120:123], a[56:59], a[104:107], v[120:123]\n"
"v_mfma_f32_16x16x32_bf16 v[120:123], a[60:63], a[108:111], v[120:123]\n"
"v_mfma_f32_16x16x32_bf16 v[92:95], a[52:55], a[108:111], v[92:95]\n"
"s_add_u32 s27, s27, 0x200000\n"
"v_mfma_f32_16x16x32_bf16 v[92:95], a[48:51], a[104:107], v[92:95]\n"
"v_mfma_f32_16x16x32_bf16 v[96:99], a[48:51], a[112:115], v[96:99]\n"
"v_mfma_f32_16x16x32_bf16 v[96:99], a[52:55], a[116:119], v[96:99]\n"
"global_load_dwordx4 a[36:39], v0, s[34:35] offset:-3072 sc1\n"
"v_mfma_f32_16x16x32_bf16 v[124:127], a[60:63], a[116:119], v[124:127]\n"
"ds_read_b64_tr_b16 v[24:25], v11 offset:12288\n"
"v_mfma_f32_16x16x32_bf16 v[124:127], a[56:59], a[112:115], v[124:127]\n"
"v_mfma_f32_16x16x32_bf16 v[128:131], a[56:59], a[120:123], v[128:131]\n"
"ds_read_b64_tr_b16 v[26:27], v11 offset:12864\n"
"v_mfma_f32_16x16x32_bf16 v[128:131], a[60:63], a[124:127], v[128:131]\n"
"L_st8516:\n"
"s_cbranch_execz L_retB\n"
"v_mfma_f32_16x16x32_bf16 v[100:103], a[52:55], a[124:127], v[100:103]\n"
"ds_read_b64_tr_b16 v[28:29], v11 offset:12296\n"
"v_mfma_f32_16x16x32_bf16 v[100:103], a[48:51], a[120:123], v[100:103]\n"
"v_mfma_f32_16x16x32_bf16 v[104:107], a[48:51], a[128:131], v[104:107]\n"
"ds_read_b64_tr_b16 v[30:31], v11 offset:12872\n"
"v_mfma_f32_16x16x32_bf16 v[104:107], a[52:55], a[132:135], v[104:107]\n"
"global_load_dwordx4 a[40:43], v0, s[34:35] offset:-2048 sc1\n"
"v_mfma_f32_16x16x32_bf16 v[132:135], a[60:63], a[132:135], v[132:135]\n"
"buffer_load_dwordx4 v0, s[48:51], s75 offen offset:2048 sc1 lds\n"
"v_mfma_f32_16x16x32_bf16 v[132:135], a[56:59], a[128:131], v[132:135]\n"
"v_mfma_f32_16x16x32_bf16 v[136:139], a[56:59], a[136:139], v[136:139]\n"
"v_mfma_f32_16x16x32_bf16 v[136:139], a[60:63], a[140:143], v[136:139]\n"
"v_mfma_f32_16x16x32_bf16 v[108:111], a[52:55], a[140:143], v[108:111]\n"
"v_mfma_f32_16x16x32_bf16 v[108:111], a[48:51], a[136:139], v[108:111]\n"
"v_mfma_f32_16x16x32_bf16 v[112:115], a[48:51], a[144:147], v[112:115]\n"
"s_waitcnt lgkmcnt(0)\n"
"v_mfma_f32_16x16x32_bf16 v[112:115], a[52:55], a[148:151], v[112:115]\n"
"global_load_dwordx4 a[44:47], v0, s[34:35] offset:-1024 sc1\n"
"v_mfma_f32_16x16x32_bf16 v[140:143], a[60:63], a[148:151], v[140:143]\n"
"v_mfma_f32_16x16x32_bf16 v[140:143], a[56:59], a[144:147], v[140:143]\n"
"v_mfma_f32_16x16x32_bf16 a[12:15], a[56:59], a[152:155], a[12:15]\n"
"v_mfma_f32_16x16x32_bf16 a[12:15], a[60:63], a[156:159], a[12:15]\n"
"v_mfma_f32_16x16x32_bf16 a[8:11], a[52:55], a[156:159], a[8:11]\n"
"v_mfma_f32_16x16x32_bf16 a[8:11], a[48:51], a[152:155], a[8:11]\n"
"s_waitcnt vmcnt(11)\n"
"v_mfma_f32_16x16x32_bf16 v[144:147], a[64:67], a[96:99], v[144:147]\n"
"buffer_store_dwordx4 v[24:27], v0, s[36:39], s28 offen offset:0 sc1\n"
"buffer_load_dwordx4 v0, s[48:51], s75 offen offset:3072 sc1 lds\n"
"v_mfma_f32_16x16x32_bf16 v[144:147], a[68:71], a[100:103], v[144:147]\n"
"global_load_dwordx4 a[48:51], v0, s[34:35] offset:0 sc1\n"
"v_mfma_f32_16x16x32_bf16 v[172:175], a[76:79], a[100:103], v[172:175]\n"
"v_mfma_f32_16x16x32_bf16 v[172:175], a[72:75], a[96:99], v[172:175]\n"
"v_mfma_f32_16x16x32_bf16 v[176:179], a[72:75], a[104:107], v[176:179]\n"
"v_mfma_f32_16x16x32_bf16 v[176:179], a[76:79], a[108:111], v[176:179]\n"
"v_mfma_f32_16x16x32_bf16 v[148:151], a[68:71], a[108:111], v[148:151]\n"
"v_mfma_f32_16x16x32_bf16 v[148:151], a[64:67], a[104:107], v[148:151]\n"
"v_mfma_f32_16x16x32_bf16 v[152:155], a[64:67], a[112:115], v[152:155]\n"
"v_mfma_f32_16x16x32_bf16 v[152:155], a[68:71], a[116:119], v[152:155]\n"
"global_load_dwordx4 a[52:55], v0, s[34:35] offset:1024 sc1\n"
"v_mfma_f32_16x16x32_bf16 v[180:183], a[76:79], a[116:119], v[180:183]\n"
"v_mfma_f32_16x16x32_bf16 v[180:183], a[72:75], a[112:115], v[180:183]\n"
"v_mfma_f32_16x16x32_bf16 v[184:187], a[72:75], a[120:123], v[184:187]\n"
"v_mfma_f32_16x16x32_bf16 v[184:187], a[76:79], a[124:127], v[184:187]\n"
"v_mfma_f32_16x16x32_bf16 v[156:159], a[68:71], a[124:127], v[156:159]\n"
"v_mfma_f32_16x16x32_bf16 v[156:159], a[64:67], a[120:123], v[156:159]\n"
"s_add_u32 m0, s64, 4096\n"
"v_mfma_f32_16x16x32_bf16 v[160:163], a[64:67], a[128:131], v[160:163]\n"
"v_mfma_f32_16x16x32_bf16 v[160:163], a[68:71], a[132:135], v[160:163]\n"
"global_load_dwordx4 a[56:59], v0, s[34:35] offset:2048 sc1\n"
"v_mfma_f32_16x16x32_bf16 v[188:191], a[76:79], a[132:135], v[188:191]\n"
"v_mfma_f32_16x16x32_bf16 v[188:191], a[72:75], a[128:131], v[188:191]\n"
"v_mfma_f32_16x16x32_bf16 v[192:195], a[72:75], a[136:139], v[192:195]\n"
"v_mfma_f32_16x16x32_bf16 v[192:195], a[76:79], a[140:143], v[192:195]\n"
"v_mfma_f32_16x16x32_bf16 v[164:167], a[68:71], a[140:143], v[164:167]\n"
"v_mfma_f32_16x16x32_bf16 v[164:167], a[64:67], a[136:139], v[164:167]\n"
"v_mfma_f32_16x16x32_bf16 v[168:171], a[64:67], a[144:147], v[168:171]\n"
"v_mfma_f32_16x16x32_bf16 v[168:171], a[68:71], a[148:151], v[168:171]\n"
"global_load_dwordx4 a[60:63], v0, s[34:35] offset:3072 sc1\n"
"v_mfma_f32_16x16x32_bf16 v[196:199], a[76:79], a[148:151], v[196:199]\n"
"v_mfma_f32_16x16x32_bf16 v[196:199], a[72:75], a[144:147], v[196:199]\n"
"v_mfma_f32_16x16x32_bf16 a[20:23], a[72:75], a[152:155], a[20:23]\n"
"s_add_u32 s34, s34, 8192\n"
"v_mfma_f32_16x16x32_bf16 a[20:23], a[76:79], a[156:159], a[20:23]\n"
"v_mfma_f32_16x16x32_bf16 a[16:19], a[68:71], a[156:159], a[16:19]\n"
"buffer_load_dwordx4 v1, s[48:51], s75 offen offset:0 sc1 lds\n"
"v_mfma_f32_16x16x32_bf16 a[16:19], a[64:67], a[152:155], a[16:19]\n"
"s_waitcnt vmcnt(13)\n"
"v_mfma_f32_16x16x32_bf16 v[200:203], a[80:83], a[96:99], v[200:203]\n"
"v_mfma_f32_16x16x32_bf16 v[200:203], a[84:87], a[100:103], v[200:203]\n"
"global_load_dwordx4 a[64:67], v0, s[34:35] offset:-4096 sc1\n"
"s_add_u32 s65, s65, 1\n"
"v_mfma_f32_16x16x32_bf16 v[228:231], a[92:95], a[100:103], v[228:231]\n"
"ds_read_b128 a[100:103], v4 offset:17408\n"
"v_mfma_f32_16x16x32_bf16 v[228:231], a[88:91], a[96:99], v[228:231]\n"
"ds_read_b128 a[96:99], v4 offset:16384\n"
"v_mov_b32 v8, s65\n"
"v_mfma_f32_16x16x32_bf16 v[232:235], a[88:91], a[104:107], v[232:235]\n"
"v_mov_b32 v9, s60\n"
"v_mfma_f32_16x16x32_bf16 v[232:235], a[92:95], a[108:111], v[232:235]\n"
"v_mfma_f32_16x16x32_bf16 v[204:207], a[84:87], a[108:111], v[204:207]\n"
"buffer_store_dwordx2 v[8:9], off, s[56:59], %13 sc1\n"
"ds_read_b128 a[108:111], v4 offset:19456\n"
"v_mfma_f32_16x16x32_bf16 v[204:207], a[80:83], a[104:107], v[204:207]\n"
"ds_read_b128 a[104:107], v4 offset:18432\n"
"v_mfma_f32_16x16x32_bf16 v[208:211], a[80:83], a[112:115], v[208:211]\n"
"buffer_store_dwordx4 v[28:31], v0, s[36:39], s28 offen offset:2048 sc1\n"
"buffer_load_dwordx4 v1, s[48:51], s75 offen offset:1024 sc1 lds\n"
"v_mfma_f32_16x16x32_bf16 v[208:211], a[84:87], a[116:119], v[208:211]\n"
"global_load_dwordx4 a[68:71], v0, s[34:35] offset:-3072 sc1\n"
"v_mfma_f32_16x16x32_bf16 v[236:239], a[92:95], a[116:119], v[236:239]\n"
"ds_read_b128 a[116:119], v4 offset:21504\n"
"v_mfma_f32_16x16x32_bf16 v[236:239], a[88:91], a[112:115], v[236:239]\n"
"ds_read_b128 a[112:115], v4 offset:20480\n"
"v_mfma_f32_16x16x32_bf16 v[240:243], a[88:91], a[120:123], v[240:243]\n"
"v_mfma_f32_16x16x32_bf16 v[240:243], a[92:95], a[124:127], v[240:243]\n"
"v_mfma_f32_16x16x32_bf16 v[212:215], a[84:87], a[124:127], v[212:215]\n"
"ds_read_b128 a[124:127], v4 offset:23552\n"
"v_mfma_f32_16x16x32_bf16 v[212:215], a[80:83], a[120:123], v[212:215]\n"
"ds_read_b128 a[120:123], v4 offset:22528\n"
"v_mfma_f32_16x16x32_bf16 v[216:219], a[80:83], a[128:131], v[216:219]\n"
"ds_read_b64_tr_b16 v[24:25], v11 offset:12352\n"
"v_mfma_f32_16x16x32_bf16 v[216:219], a[84:87], a[132:135], v[216:219]\n"
"global_load_dwordx4 a[72:75], v0, s[34:35] offset:-2048 sc1\n"
"ds_read_b64_tr_b16 v[26:27], v11 offset:12800\n"
"v_mfma_f32_16x16x32_bf16 v[244:247], a[92:95], a[132:135], v[244:247]\n"
"ds_read_b128 a[132:135], v4 offset:25600\n"
"v_mfma_f32_16x16x32_bf16 v[244:247], a[88:91], a[128:131], v[244:247]\n"
"ds_read_b128 a[128:131], v4 offset:24576\n"
"v_mfma_f32_16x16x32_bf16 v[248:251], a[88:91], a[136:139], v[248:251]\n"
"ds_read_b64_tr_b16 v[28:29], v11 offset:12360\n"
"v_mfma_f32_16x16x32_bf16 v[248:251], a[92:95], a[140:143], v[248:251]\n"
"ds_read_b64_tr_b16 v[30:31], v11 offset:12808\n"
"v_mfma_f32_16x16x32_bf16 v[220:223], a[84:87], a[140:143], v[220:223]\n"
"ds_read_b128 a[140:143], v4 offset:27648\n"
"v_mfma_f32_16x16x32_bf16 v[220:223], a[80:83], a[136:139], v[220:223]\n"
"ds_read_b128 a[136:139], v4 offset:26624\n"
"v_mfma_f32_16x16x32_bf16 v[224:227], a[80:83], a[144:147], v[224:227]\n"
"v_mfma_f32_16x16x32_bf16 v[224:227], a[84:87], a[148:151], v[224:227]\n"
"global_load_dwordx4 a[76:79], v0, s[34:35] offset:-1024 sc1\n"
"v_mfma_f32_16x16x32_bf16 v[252:255], a[92:95], a[148:151], v[252:255]\n"
"ds_read_b128 a[148:151], v4 offset:29696\n"
"v_mfma_f32_16x16x32_bf16 v[252:255], a[88:91], a[144:147], v[252:255]\n"
"ds_read_b128 a[144:147], v4 offset:28672\n"
"v_mfma_f32_16x16x32_bf16 a[28:31], a[88:91], a[152:155], a[28:31]\n"
"buffer_load_dwordx4 v1, s[48:51], s75 offen offset:2048 sc1 lds\n"
"v_mfma_f32_16x16x32_bf16 a[28:31], a[92:95], a[156:159], a[28:31]\n"
"v_mfma_f32_16x16x32_bf16 a[24:27], a[84:87], a[156:159], a[24:27]\n"
"s_waitcnt lgkmcnt(4)\n"
"ds_read_b128 a[156:159], v4 offset:31744\n"
"v_mfma_f32_16x16x32_bf16 a[24:27], a[80:83], a[152:155], a[24:27]\n"
"s_waitcnt vmcnt(15)\n"
"ds_read_b128 a[152:155], v4 offset:30720\n"
"v_mfma_f32_16x16x32_bf16 v[32:35], a[32:35], a[96:99], v[32:35]\n"
"v_mfma_f32_16x16x32_bf16 v[32:35], a[36:39], a[100:103], v[32:35]\n"
"global_load_dwordx4 a[80:83], v0, s[34:35] offset:0 sc1\n"
"v_mfma_f32_16x16x32_bf16 v[60:63], a[44:47], a[100:103], v[60:63]\n"
"v_mfma_f32_16x16x32_bf16 v[60:63], a[40:43], a[96:99], v[60:63]\n"
"v_mfma_f32_16x16x32_bf16 v[64:67], a[40:43], a[104:107], v[64:67]\n"
"v_mfma_f32_16x16x32_bf16 v[64:67], a[44:47], a[108:111], v[64:67]\n"
"v_mfma_f32_16x16x32_bf16 v[36:39], a[36:39], a[108:111], v[36:39]\n"
"v_mfma_f32_16x16x32_bf16 v[36:39], a[32:35], a[104:107], v[36:39]\n"
"v_mfma_f32_16x16x32_bf16 v[40:43], a[32:35], a[112:115], v[40:43]\n"
"v_mfma_f32_16x16x32_bf16 v[40:43], a[36:39], a[116:119], v[40:43]\n"
"global_load_dwordx4 a[84:87], v0, s[34:35] offset:1024 sc1\n"
"v_mfma_f32_16x16x32_bf16 v[68:71], a[44:47], a[116:119], v[68:71]\n"
"buffer_load_dwordx4 v1, s[48:51], s75 offen offset:3072 sc1 lds\n"
"v_mfma_f32_16x16x32_bf16 v[68:71], a[40:43], a[112:115], v[68:71]\n"
"v_mfma_f32_16x16x32_bf16 v[72:75], a[40:43], a[120:123], v[72:75]\n"
"v_mfma_f32_16x16x32_bf16 v[72:75], a[44:47], a[124:127], v[72:75]\n"
"v_mfma_f32_16x16x32_bf16 v[44:47], a[36:39], a[124:127], v[44:47]\n"
"v_mfma_f32_16x16x32_bf16 v[44:47], a[32:35], a[120:123], v[44:47]\n"
"s_waitcnt lgkmcnt(4)\n"
"v_mfma_f32_16x16x32_bf16 v[48:51], a[32:35], a[128:131], v[48:51]\n"
"v_mfma_f32_16x16x32_bf16 v[48:51], a[36:39], a[132:135], v[48:51]\n"
"global_load_dwordx4 a[88:91], v0, s[34:35] offset:2048 sc1\n"
"v_mfma_f32_16x16x32_bf16 v[76:79], a[44:47], a[132:135], v[76:79]\n"
"v_mfma_f32_16x16x32_bf16 v[76:79], a[40:43], a[128:131], v[76:79]\n"
"v_mfma_f32_16x16x32_bf16 v[80:83], a[40:43], a[136:139], v[80:83]\n"
"v_mfma_f32_16x16x32_bf16 v[80:83], a[44:47], a[140:143], v[80:83]\n"
"v_mfma_f32_16x16x32_bf16 v[52:55], a[36:39], a[140:143], v[52:55]\n"
"v_mfma_f32_16x16x32_bf16 v[52:55], a[32:35], a[136:139], v[52:55]\n"
"s_waitcnt lgkmcnt(0)\n"
"v_mfma_f32_16x16x32_bf16 v[56:59], a[32:35], a[144:147], v[56:59]\n"
"v_mfma_f32_16x16x32_bf16 v[56:59], a[36:39], a[148:151], v[56:59]\n"
"s_add_u32 m0, s64, 8192\n"
"global_load_dwordx4 a[92:95], v0, s[34:35] offset:3072 sc1\n"
"v_mfma_f32_16x16x32_bf16 v[84:87], a[44:47], a[148:151], v[84:87]\n"
"v_mfma_f32_16x16x32_bf16 v[84:87], a[40:43], a[144:147], v[84:87]\n"
"v_mfma_f32_16x16x32_bf16 a[4:7], a[40:43], a[152:155], a[4:7]\n"
"s_add_u32 s34, s34, 8192\n"
"v_mfma_f32_16x16x32_bf16 a[4:7], a[44:47], a[156:159], a[4:7]\n"
"v_mfma_f32_16x16x32_bf16 a[0:3], a[36:39], a[156:159], a[0:3]\n"
"v_mfma_f32_16x16x32_bf16 a[0:3], a[32:35], a[152:155], a[0:3]\n"
"s_waitcnt vmcnt(14)\n"
"v_mfma_f32_16x16x32_bf16 v[88:91], a[48:51], a[96:99], v[88:91]\n"
"v_mfma_f32_16x16x32_bf16 v[88:91], a[52:55], a[100:103], v[88:91]\n"
"global_load_dwordx4 a[32:35], v0, s[34:35] offset:-4096 sc1\n"
"v_mfma_f32_16x16x32_bf16 v[116:119], a[60:63], a[100:103], v[116:119]\n"
"v_mfma_f32_16x16x32_bf16 v[116:119], a[56:59], a[96:99], v[116:119]\n"
"v_mfma_f32_16x16x32_bf16 v[120:123], a[56:59], a[104:107], v[120:123]\n"
"v_mfma_f32_16x16x32_bf16 v[120:123], a[60:63], a[108:111], v[120:123]\n"
"v_mfma_f32_16x16x32_bf16 v[92:95], a[52:55], a[108:111], v[92:95]\n"
"v_mfma_f32_16x16x32_bf16 v[92:95], a[48:51], a[104:107], v[92:95]\n"
"v_mfma_f32_16x16x32_bf16 v[96:99], a[48:51], a[112:115], v[96:99]\n"
"buffer_load_dwordx4 v0, s[48:51], s69 offen offset:0 sc1 lds\n"
"v_mfma_f32_16x16x32_bf16 v[96:99], a[52:55], a[116:119], v[96:99]\n"
"global_load_dwordx4 a[36:39], v0, s[34:35] offset:-3072 sc1\n"
"v_mfma_f32_16x16x32_bf16 v[124:127], a[60:63], a[116:119], v[124:127]\n"
"v_mfma_f32_16x16x32_bf16 v[124:127], a[56:59], a[112:115], v[124:127]\n"
"v_mfma_f32_16x16x32_bf16 v[128:131], a[56:59], a[120:123], v[128:131]\n"
"v_mfma_f32_16x16x32_bf16 v[128:131], a[60:63], a[124:127], v[128:131]\n"
"v_mfma_f32_16x16x32_bf16 v[100:103], a[52:55], a[124:127], v[100:103]\n"
"v_mfma_f32_16x16x32_bf16 v[100:103], a[48:51], a[120:123], v[100:103]\n"
"v_mfma_f32_16x16x32_bf16 v[104:107], a[48:51], a[128:131], v[104:107]\n"
"v_mfma_f32_16x16x32_bf16 v[104:107], a[52:55], a[132:135], v[104:107]\n"
"global_load_dwordx4 a[40:43], v0, s[34:35] offset:-2048 sc1\n"
"v_mfma_f32_16x16x32_bf16 v[132:135], a[60:63], a[132:135], v[132:135]\n"
"v_mfma_f32_16x16x32_bf16 v[132:135], a[56:59], a[128:131], v[132:135]\n"
"v_mfma_f32_16x16x32_bf16 v[136:139], a[56:59], a[136:139], v[136:139]\n"
"v_mfma_f32_16x16x32_bf16 v[136:139], a[60:63], a[140:143], v[136:139]\n"
"v_mfma_f32_16x16x32_bf16 v[108:111], a[52:55], a[140:143], v[108:111]\n"
"buffer_load_dwordx4 v0, s[48:51], s69 offen offset:1024 sc1 lds\n"
"v_mfma_f32_16x16x32_bf16 v[108:111], a[48:51], a[136:139], v[108:111]\n"
"v_mfma_f32_16x16x32_bf16 v[112:115], a[48:51], a[144:147], v[112:115]\n"
"v_mfma_f32_16x16x32_bf16 v[112:115], a[52:55], a[148:151], v[112:115]\n"
"global_load_dwordx4 a[44:47], v0, s[34:35] offset:-1024 sc1\n"
"v_mfma_f32_16x16x32_bf16 v[140:143], a[60:63], a[148:151], v[140:143]\n"
"v_mfma_f32_16x16x32_bf16 v[140:143], a[56:59], a[144:147], v[140:143]\n"
"v_mfma_f32_16x16x32_bf16 a[12:15], a[56:59], a[152:155], a[12:15]\n"
"v_mfma_f32_16x16x32_bf16 a[12:15], a[60:63], a[156:159], a[12:15]\n"
"v_mfma_f32_16x16x32_bf16 a[8:11], a[52:55], a[156:159], a[8:11]\n"
"v_mfma_f32_16x16x32_bf16 a[8:11], a[48:51], a[152:155], a[8:11]\n"
"s_waitcnt vmcnt(12)\n"
"v_mfma_f32_16x16x32_bf16 v[144:147], a[64:67], a[96:99], v[144:147]\n"
"v_mfma_f32_16x16x32_bf16 v[144:147], a[68:71], a[100:103], v[144:147]\n"
"global_load_dwordx4 a[48:51], v0, s[34:35] offset:0 sc1\n"
"v_mfma_f32_16x16x32_bf16 v[172:175], a[76:79], a[100:103], v[172:175]\n"
"v_mfma_f32_16x16x32_bf16 v[172:175], a[72:75], a[96:99], v[172:175]\n"
"v_mfma_f32_16x16x32_bf16 v[176:179], a[72:75], a[104:107], v[176:179]\n"
"v_mfma_f32_16x16x32_bf16 v[176:179], a[76:79], a[108:111], v[176:179]\n"
"buffer_load_dwordx4 v0, s[48:51], s69 offen offset:2048 sc1 lds\n"
"v_mfma_f32_16x16x32_bf16 v[148:151], a[68:71], a[108:111], v[148:151]\n"
"v_mfma_f32_16x16x32_bf16 v[148:151], a[64:67], a[104:107], v[148:151]\n"
"v_mfma_f32_16x16x32_bf16 v[152:155], a[64:67], a[112:115], v[152:155]\n"
"v_lshlrev_b32 v16, 16, v18\n"
"v_mfma_f32_16x16x32_bf16 v[152:155], a[68:71], a[116:119], v[152:155]\n"
"global_load_dwordx4 a[52:55], v0, s[34:35] offset:1024 sc1\n"
"v_and_b32 v17, s63, v18\n"
"v_mfma_f32_16x16x32_bf16 v[180:183], a[76:79], a[116:119], v[180:183]\n"
"v_lshlrev_b32 v18, 16, v19\n"
"v_mfma_f32_16x16x32_bf16 v[180:183], a[72:75], a[112:115], v[180:183]\n"
"v_and_b32 v19, s63, v19\n"
"v_mfma_f32_16x16x32_bf16 v[184:187], a[72:75], a[120:123], v[184:187]\n"
"v_add_f32 v32, v32, v16\n"
"v_mfma_f32_16x16x32_bf16 v[184:187], a[76:79], a[124:127], v[184:187]\n"
"v_add_f32 v33, v33, v17\n"
"v_add_f32 v34, v34, v18\n"
"v_mfma_f32_16x16x32_bf16 v[156:159], a[68:71], a[124:127], v[156:159]\n"
"v_add_f32 v35, v35, v19\n"
"v_mfma_f32_16x16x32_bf16 v[156:159], a[64:67], a[120:123], v[156:159]\n"
"v_add_f32 v36, v36, v16\n"
"v_mfma_f32_16x16x32_bf16 v[160:163], a[64:67], a[128:131], v[160:163]\n"
"v_add_f32 v37, v37, v17\n"
"v_mfma_f32_16x16x32_bf16 v[160:163], a[68:71], a[132:135], v[160:163]\n"
"global_load_dwordx4 a[56:59], v0, s[34:35] offset:2048 sc1\n"
"v_add_f32 v38, v38, v18\n"
"v_mfma_f32_16x16x32_bf16 v[188:191], a[76:79], a[132:135], v[188:191]\n"
"v_add_f32 v39, v39, v19\n"
"v_mfma_f32_16x16x32_bf16 v[188:191], a[72:75], a[128:131], v[188:191]\n"
"v_add_f32 v40, v40, v16\n"
"v_add_f32 v41, v41, v17\n"
"v_mfma_f32_16x16x32_bf16 v[192:195], a[72:75], a[136:139], v[192:195]\n"
"buffer_load_dwordx4 v0, s[48:51], s69 offen offset:3072 sc1 lds\n"
"v_mfma_f32_16x16x32_bf16 v[192:195], a[76:79], a[140:143], v[192:195]\n"
"v_add_f32 v42, v42, v18\n"
"v_mfma_f32_16x16x32_bf16 v[164:167], a[68:71], a[140:143], v[164:167]\n"
"v_add_f32 v43, v43, v19\n"
"v_mfma_f32_16x16x32_bf16 v[164:167], a[64:67], a[136:139], v[164:167]\n"
"v_add_f32 v44, v44, v16\n"
"v_add_f32 v45, v45, v17\n"
"v_mfma_f32_16x16x32_bf16 v[168:171], a[64:67], a[144:147], v[168:171]\n"
"v_add_f32 v46, v46, v18\n"
"v_mfma_f32_16x16x32_bf16 v[168:171], a[68:71], a[148:151], v[168:171]\n"
"global_load_dwordx4 a[60:63], v0, s[34:35] offset:3072 sc1\n"
"v_add_f32 v47, v47, v19\n"
"v_mfma_f32_16x16x32_bf16 v[196:199], a[76:79], a[148:151], v[196:199]\n"
"v_add_f32 v48, v48, v16\n"
"v_mfma_f32_16x16x32_bf16 v[196:199], a[72:75], a[144:147], v[196:199]\n"
"v_add_f32 v49, v49, v17\n"
"v_mfma_f32_16x16x32_bf16 a[20:23], a[72:75], a[152:155], a[20:23]\n"
"s_add_u32 s34, s34, 8192\n"
"v_add_f32 v50, v50, v18\n"
"v_mfma_f32_16x16x32_bf16 a[20:23], a[76:79], a[156:159], a[20:23]\n"
"v_add_f32 v51, v51, v19\n"
"v_mfma_f32_16x16x32_bf16 a[16:19], a[68:71], a[156:159], a[16:19]\n"
"v_add_f32 v52, v52, v16\n"
"v_mfma_f32_16x16x32_bf16 a[16:19], a[64:67], a[152:155], a[16:19]\n"
"s_waitcnt vmcnt(12)\n"
"v_add_f32 v53, v53, v17\n"
"v_mfma_f32_16x16x32_bf16 v[200:203], a[80:83], a[96:99], v[200:203]\n"
"v_add_f32 v54, v54, v18\n"
"v_add_f32 v55, v55, v19\n"
"v_mfma_f32_16x16x32_bf16 v[200:203], a[84:87], a[100:103], v[200:203]\n"
"global_load_dwordx4 a[64:67], v0, s[34:35] offset:-4096 sc1\n"
"v_add_f32 v56, v56, v16\n"
"v_mfma_f32_16x16x32_bf16 v[228:231], a[92:95], a[100:103], v[228:231]\n"
"s_add_u32 m0, s64, 12288\n"
"ds_read_b128 a[100:103], v5 offset:17408\n"
"v_add_f32 v57, v57, v17\n"
"v_mfma_f32_16x16x32_bf16 v[228:231], a[88:91], a[96:99], v[228:231]\n"
"ds_read_b128 a[96:99], v5 offset:16384\n"
"v_add_f32 v58, v58, v18\n"
"v_mfma_f32_16x16x32_bf16 v[232:235], a[88:91], a[104:107], v[232:235]\n"
"v_add_f32 v59, v59, v19\n"
"v_mfma_f32_16x16x32_bf16 v[232:235], a[92:95], a[108:111], v[232:235]\n"
"v_accvgpr_read_b32 v12, a0\n"
"v_mfma_f32_16x16x32_bf16 v[204:207], a[84:87], a[108:111], v[204:207]\n"
"ds_read_b128 a[108:111], v5 offset:19456\n"
"v_accvgpr_read_b32 v13, a1\n"
"v_mfma_f32_16x16x32_bf16 v[204:207], a[80:83], a[104:107], v[204:207]\n"
"ds_read_b128 a[104:107], v5 offset:18432\n"
"v_accvgpr_read_b32 v14, a2\n"
"v_mfma_f32_16x16x32_bf16 v[208:211], a[80:83], a[112:115], v[208:211]\n"
"v_accvgpr_read_b32 v15, a3\n"
"v_mfma_f32_16x16x32_bf16 v[208:211], a[84:87], a[116:119], v[208:211]\n"
"global_load_dwordx4 a[68:71], v0, s[34:35] offset:-3072 sc1\n"
"v_add_f32 v12, v12, v16\n"
"v_mfma_f32_16x16x32_bf16 v[236:239], a[92:95], a[116:119], v[236:239]\n"
"ds_read_b128 a[116:119], v5 offset:21504\n"
"v_add_f32 v13, v13, v17\n"
"v_mfma_f32_16x16x32_bf16 v[236:239], a[88:91], a[112:115], v[236:239]\n"
"ds_read_b128 a[112:115], v5 offset:20480\n"
"v_add_f32 v14, v14, v18\n"
"v_mfma_f32_16x16x32_bf16 v[240:243], a[88:91], a[120:123], v[240:243]\n"
"v_add_f32 v15, v15, v19\n"
"v_accvgpr_write_b32 a0, v12\n"
"v_mfma_f32_16x16x32_bf16 v[240:243], a[92:95], a[124:127], v[240:243]\n"
"v_accvgpr_write_b32 a1, v13\n"
"v_mfma_f32_16x16x32_bf16 v[212:215], a[84:87], a[124:127], v[212:215]\n"
"ds_read_b128 a[124:127], v5 offset:23552\n"
"v_accvgpr_write_b32 a2, v14\n"
"v_mfma_f32_16x16x32_bf16 v[212:215], a[80:83], a[120:123], v[212:215]\n"
"ds_read_b128 a[120:123], v5 offset:22528\n"
"v_accvgpr_write_b32 a3, v15\n"
"v_mfma_f32_16x16x32_bf16 v[216:219], a[80:83], a[128:131], v[216:219]\n"
"buffer_load_dwordx4 v1, s[48:51], s69 offen offset:0 sc1 lds\n"
"v_mfma_f32_16x16x32_bf16 v[216:219], a[84:87], a[132:135], v[216:219]\n"
"global_load_dwordx4 a[72:75], v0, s[34:35] offset:-2048 sc1\n"
"v_lshrrev_b32 v16, 4, v0\n"
"v_mfma_f32_16x16x32_bf16 v[244:247], a[92:95], a[132:135], v[244:247]\n"
"ds_read_b128 a[132:135], v5 offset:25600\n"
"v_and_b32 v16, 0x30, v16\n"
"v_mfma_f32_16x16x32_bf16 v[244:247], a[88:91], a[128:131], v[244:247]\n"
"ds_read_b128 a[128:131], v5 offset:24576\n"
"buffer_load_dwordx2 v[18:19], v16, s[44:47], s61 offen offset:64\n"
"v_mfma_f32_16x16x32_bf16 v[248:251], a[88:91], a[136:139], v[248:251]\n"
"s_getpc_b64 s[96:97]\n"
"L_c6_:\n"
"s_add_u32 s96, s96, L_last-L_c6_\n"
"s_addc_u32 s97, s97, 0\n"
"v_mfma_f32_16x16x32_bf16 v[248:251], a[92:95], a[140:143], v[248:251]\n"
"s_lshr_b32 s66, %26, 3\n"
"v_mfma_f32_16x16x32_bf16 v[220:223], a[84:87], a[140:143], v[220:223]\n"
"s_lshl2_add_u32 s66, s66, %16\n"
"ds_read_b128 a[140:143], v5 offset:27648\n"
"v_mfma_f32_16x16x32_bf16 v[220:223], a[80:83], a[136:139], v[220:223]\n"
"ds_read_b128 a[136:139], v5 offset:26624\n"
"v_mfma_f32_16x16x32_bf16 v[224:227], a[80:83], a[144:147], v[224:227]\n"
"s_mul_i32 s66, s66, 256\n"
"v_mfma_f32_16x16x32_bf16 v[224:227], a[84:87], a[148:151], v[224:227]\n"
"global_load_dwordx4 a[76:79], v0, s[34:35] offset:-1024 sc1\n"
"v_mfma_f32_16x16x32_bf16 v[252:255], a[92:95], a[148:151], v[252:255]\n"
"ds_read_b128 a[148:151], v5 offset:29696\n"
"v_mfma_f32_16x16x32_bf16 v[252:255], a[88:91], a[144:147], v[252:255]\n"
"s_min_u32 s66, s66, L_headF-L_last-256\n"
"ds_read_b128 a[144:147], v5 offset:28672\n"
"v_mfma_f32_16x16x32_bf16 a[28:31], a[88:91], a[152:155], a[28:31]\n"
"v_mfma_f32_16x16x32_bf16 a[28:31], a[92:95], a[156:159], a[28:31]\n"
"v_min_u32 v11, 16, v0\n"
"v_mfma_f32_16x16x32_bf16 a[24:27], a[84:87], a[156:159], a[24:27]\n"
"ds_read_b128 a[156:159], v5 offset:31744\n"
"v_mfma_f32_16x16x32_bf16 a[24:27], a[80:83], a[152:155], a[24:27]\n"
"s_waitcnt vmcnt(12) lgkmcnt(11)\n"
"ds_read_b128 a[152:155], v5 offset:30720\n"
"v_mfma_f32_16x16x32_bf16 v[32:35], a[32:35], a[96:99], v[32:35]\n"
"buffer_load_dwordx4 v1, s[48:51], s69 offen offset:1024 sc1 lds\n"
"v_mfma_f32_16x16x32_bf16 v[32:35], a[36:39], a[100:103], v[32:35]\n"
"global_load_dwordx4 a[80:83], v0, s[34:35] offset:0 sc1\n"
"v_mfma_f32_16x16x32_bf16 v[60:63], a[44:47], a[100:103], v[60:63]\n"
"v_lshl_add_u32 v11, v11, 3, s66\n"
"v_mfma_f32_16x16x32_bf16 v[60:63], a[40:43], a[96:99], v[60:63]\n"
"global_load_dword v10, v11, s[96:97]\n"
"v_mfma_f32_16x16x32_bf16 v[64:67], a[40:43], a[104:107], v[64:67]\n"
"v_mfma_f32_16x16x32_bf16 v[64:67], a[44:47], a[108:111], v[64:67]\n"
"v_mfma_f32_16x16x32_bf16 v[36:39], a[36:39], a[108:111], v[36:39]\n"
"v_mfma_f32_16x16x32_bf16 v[36:39], a[32:35], a[104:107], v[36:39]\n"
"s_waitcnt lgkmcnt(8)\n"
"v_mfma_f32_16x16x32_bf16 v[40:43], a[32:35], a[112:115], v[40:43]\n"
"v_mfma_f32_16x16x32_bf16 v[40:43], a[36:39], a[116:119], v[40:43]\n"
"global_load_dwordx4 a[84:87], v0, s[34:35] offset:1024 sc1\n"
"v_mfma_f32_16x16x32_bf16 v[68:71], a[44:47], a[116:119], v[68:71]\n"
"v_mfma_f32_16x16x32_bf16 v[68:71], a[40:43], a[112:115], v[68:71]\n"
"v_mfma_f32_16x16x32_bf16 v[72:75], a[40:43], a[120:123], v[72:75]\n"
"v_mfma_f32_16x16x32_bf16 v[72:75], a[44:47], a[124:127], v[72:75]\n"
"v_mfma_f32_16x16x32_bf16 v[44:47], a[36:39], a[124:127], v[44:47]\n"
"buffer_load_dwordx4 v1, s[48:51], s69 offen offset:2048 sc1 lds\n"
"v_mfma_f32_16x16x32_bf16 v[44:47], a[32:35], a[120:123], v[44:47]\n"
"s_waitcnt lgkmcnt(4)\n"
"v_mfma_f32_16x16x32_bf16 v[48:51], a[32:35], a[128:131], v[48:51]\n"
"v_mfma_f32_16x16x32_bf16 v[48:51], a[36:39], a[132:135], v[48:51]\n"
"global_load_dwordx4 a[88:91], v0, s[34:35] offset:2048 sc1\n"
"v_mfma_f32_16x16x32_bf16 v[76:79], a[44:47], a[132:135], v[76:79]\n"
"v_mfma_f32_16x16x32_bf16 v[76:79], a[40:43], a[128:131], v[76:79]\n"
"v_mfma_f32_16x16x32_bf16 v[80:83], a[40:43], a[136:139], v[80:83]\n"
"v_mfma_f32_16x16x32_bf16 v[80:83], a[44:47], a[140:143], v[80:83]\n"
"v_mfma_f32_16x16x32_bf16 v[52:55], a[36:39], a[140:143], v[52:55]\n"
"v_mfma_f32_16x16x32_bf16 v[52:55], a[32:35], a[136:139], v[52:55]\n"
"s_waitcnt lgkmcnt(0)\n"
"v_mfma_f32_16x16x32_bf16 v[56:59], a[32:35], a[144:147], v[56:59]\n"
"v_mfma_f32_16x16x32_bf16 v[56:59], a[36:39], a[148:151], v[56:59]\n"
"global_load_dwordx4 a[92:95], v0, s[34:35] offset:3072 sc1\n"
"v_mfma_f32_16x16x32_bf16 v[84:87], a[44:47], a[148:151], v[84:87]\n"
"v_mfma_f32_16x16x32_bf16 v[84:87], a[40:43], a[144:147], v[84:87]\n"
"v_mfma_f32_16x16x32_bf16 a[4:7], a[40:43], a[152:155], a[4:7]\n"
"s_add_u32 s34, s34, 8192\n"
"v_mfma_f32_16x16x32_bf16 a[4:7], a[44:47], a[156:159], a[4:7]\n"
"buffer_load_dwordx4 v1, s[48:51], s69 offen offset:3072 sc1 lds\n"
"v_mfma_f32_16x16x32_bf16 a[0:3], a[36:39], a[156:159], a[0:3]\n"
"v_mfma_f32_16x16x32_bf16 a[0:3], a[32:35], a[152:155], a[0:3]\n"
"s_waitcnt vmcnt(14)\n"
"v_mfma_f32_16x16x32_bf16 v[88:91], a[48:51], a[96:99], v[88:91]\n"
"v_mfma_f32_16x16x32_bf16 v[88:91], a[52:55], a[100:103], v[88:91]\n"
"global_load_dwordx4 a[32:35], v0, s[34:35] offset:-4096 sc1\n"
"v_mfma_f32_16x16x32_bf16 v[116:119], a[60:63], a[100:103], v[116:119]\n"
"v_mfma_f32_16x16x32_bf16 v[116:119], a[56:59], a[96:99], v[116:119]\n"
"v_mfma_f32_16x16x32_bf16 v[120:123], a[56:59], a[104:107], v[120:123]\n"
"v_mfma_f32_16x16x32_bf16 v[120:123], a[60:63], a[108:111], v[120:123]\n"
"v_mfma_f32_16x16x32_bf16 v[92:95], a[52:55], a[108:111], v[92:95]\n"
"v_mfma_f32_16x16x32_bf16 v[92:95], a[48:51], a[104:107], v[92:95]\n"
"v_mfma_f32_16x16x32_bf16 v[96:99], a[48:51], a[112:115], v[96:99]\n"
"v_mfma_f32_16x16x32_bf16 v[96:99], a[52:55], a[116:119], v[96:99]\n"
"global_load_dwordx4 a[36:39], v0, s[34:35] offset:-3072 sc1\n"
"v_mfma_f32_16x16x32_bf16 v[124:127], a[60:63], a[116:119], v[124:127]\n"
"v_mfma_f32_16x16x32_bf16 v[124:127], a[56:59], a[112:115], v[124:127]\n"
"v_mfma_f32_16x16x32_bf16 v[128:131], a[56:59], a[120:123], v[128:131]\n"
"v_mfma_f32_16x16x32_bf16 v[128:131], a[60:63], a[124:127], v[128:131]\n"
"v_mfma_f32_16x16x32_bf16 v[100:103], a[52:55], a[124:127], v[100:103]\n"
"v_mfma_f32_16x16x32_bf16 v[100:103], a[48:51], a[120:123], v[100:103]\n"
"v_mfma_f32_16x16x32_bf16 v[104:107], a[48:51], a[128:131], v[104:107]\n"
"buffer_store_dwordx4 v[24:27], v1, s[36:39], s28 offen offset:0 sc1\n"
"buffer_load_dwordx2 v[8:9], off, s[56:59], %14 sc1\n"
"v_mfma_f32_16x16x32_bf16 v[104:107], a[52:55], a[132:135], v[104:107]\n"
"global_load_dwordx4 a[40:43], v0, s[34:35] offset:-2048 sc1\n"
"v_mfma_f32_16x16x32_bf16 v[132:135], a[60:63], a[132:135], v[132:135]\n"
"v_mfma_f32_16x16x32_bf16 v[132:135], a[56:59], a[128:131], v[132:135]\n"
"v_mfma_f32_16x16x32_bf16 v[136:139], a[56:59], a[136:139], v[136:139]\n"
"v_mfma_f32_16x16x32_bf16 v[136:139], a[60:63], a[140:143], v[136:139]\n"
"v_mfma_f32_16x16x32_bf16 v[108:111], a[52:55], a[140:143], v[108:111]\n"
"v_mfma_f32_16x16x32_bf16 v[108:111], a[48:51], a[136:139], v[108:111]\n"
"v_mfma_f32_16x16x32_bf16 v[112:115], a[48:51], a[144:147], v[112:115]\n"
"v_mfma_f32_16x16x32_bf16 v[112:115], a[52:55], a[148:151], v[112:115]\n"
"global_load_dwordx4 a[44:47], v0, s[34:35] offset:-1024 sc1\n"
"v_mfma_f32_16x16x32_bf16 v[140:143], a[60:63], a[148:151], v[140:143]\n"
"v_mfma_f32_16x16x32_bf16 v[140:143], a[56:59], a[144:147], v[140:143]\n"
"v_mfma_f32_16x16x32_bf16 a[12:15], a[56:59], a[152:155], a[12:15]\n"
"v_mfma_f32_16x16x32_bf16 a[12:15], a[60:63], a[156:159], a[12:15]\n"
"v_mfma_f32_16x16x32_bf16 a[8:11], a[52:55], a[156:159], a[8:11]\n"
"v_mfma_f32_16x16x32_bf16 a[8:11], a[48:51], a[152:155], a[8:11]\n"
"s_waitcnt vmcnt(14)\n"
"v_mfma_f32_16x16x32_bf16 v[144:147], a[64:67], a[96:99], v[144:147]\n"
"v_mfma_f32_16x16x32_bf16 v[144:147], a[68:71], a[100:103], v[144:147]\n"
"global_load_dwordx4 a[48:51], v0, s[34:35] offset:0 sc1\n"
"v_mfma_f32_16x16x32_bf16 v[172:175], a[76:79], a[100:103], v[172:175]\n"
"v_mfma_f32_16x16x32_bf16 v[172:175], a[72:75], a[96:99], v[172:175]\n"
"v_mfma_f32_16x16x32_bf16 v[176:179], a[72:75], a[104:107], v[176:179]\n"
"v_mfma_f32_16x16x32_bf16 v[176:179], a[76:79], a[108:111], v[176:179]\n"
"v_mfma_f32_16x16x32_bf16 v[148:151], a[68:71], a[108:111], v[148:151]\n"
"v_mfma_f32_16x16x32_bf16 v[148:151], a[64:67], a[104:107], v[148:151]\n"
"v_mfma_f32_16x16x32_bf16 v[152:155], a[64:67], a[112:115], v[152:155]\n"
"v_lshlrev_b32 v20, 16, v22\n"
"v_mfma_f32_16x16x32_bf16 v[152:155], a[68:71], a[116:119], v[152:155]\n"
"global_load_dwordx4 a[52:55], v0, s[34:35] offset:1024 sc1\n"
"v_and_b32 v21, s63, v22\n"
"v_mfma_f32_16x16x32_bf16 v[180:183], a[76:79], a[116:119], v[180:183]\n"
"v_lshlrev_b32 v22, 16, v23\n"
"v_mfma_f32_16x16x32_bf16 v[180:183], a[72:75], a[112:115], v[180:183]\n"
"v_and_b32 v23, s63, v23\n"
"v_mfma_f32_16x16x32_bf16 v[184:187], a[72:75], a[120:123], v[184:187]\n"
"v_add_f32 v60, v60, v20\n"
"v_mfma_f32_16x16x32_bf16 v[184:187], a[76:79], a[124:127], v[184:187]\n"
"v_add_f32 v61, v61, v21\n"
"v_mfma_f32_16x16x32_bf16 v[156:159], a[68:71], a[124:127], v[156:159]\n"
"buffer_store_dwordx4 v[28:31], v1, s[36:39], s28 offen offset:2048 sc1\n"
"v_add_f32 v62, v62, v22\n"
"v_add_f32 v63, v63, v23\n"
"v_add_f32 v64, v64, v20\n"
"v_mfma_f32_16x16x32_bf16 v[156:159], a[64:67], a[120:123], v[156:159]\n"
"v_add_f32 v65, v65, v21\n"
"v_mfma_f32_16x16x32_bf16 v[160:163], a[64:67], a[128:131], v[160:163]\n"
"v_add_f32 v66, v66, v22\n"
"v_mfma_f32_16x16x32_bf16 v[160:163], a[68:71], a[132:135], v[160:163]\n"
"global_load_dwordx4 a[56:59], v0, s[34:35] offset:2048 sc1\n"
"v_mfma_f32_16x16x32_bf16 v[188:191], a[76:79], a[132:135], v[188:191]\n"
"v_add_f32 v67, v67, v23\n"
"v_mfma_f32_16x16x32_bf16 v[188:191], a[72:75], a[128:131], v[188:191]\n"
"v_add_f32 v68, v68, v20\n"
"v_mfma_f32_16x16x32_bf16 v[192:195], a[72:75], a[136:139], v[192:195]\n"
"v_add_f32 v69, v69, v21\n"
"v_mfma_f32_16x16x32_bf16 v[192:195], a[76:79], a[140:143], v[192:195]\n"
"v_add_f32 v70, v70, v22\n"
"v_mfma_f32_16x16x32_bf16 v[164:167], a[68:71], a[140:143], v[164:167]\n"
"v_add_f32 v71, v71, v23\n"
"v_mfma_f32_16x16x32_bf16 v[164:167], a[64:67], a[136:139], v[164:167]\n"
"v_add_f32 v72, v72, v20\n"
"v_add_f32 v73, v73, v21\n"
"v_mfma_f32_16x16x32_bf16 v[168:171], a[64:67], a[144:147], v[168:171]\n"
"v_add_f32 v74, v74, v22\n"
"v_mfma_f32_16x16x32_bf16 v[168:171], a[68:71], a[148:151], v[168:171]\n"
"global_load_dwordx4 a[60:63], v0, s[34:35] offset:3072 sc1\n"
"v_add_f32 v75, v75, v23\n"
"v_mfma_f32_16x16x32_bf16 v[196:199], a[76:79], a[148:151], v[196:199]\n"
"v_add_f32 v76, v76, v20\n"
"v_mfma_f32_16x16x32_bf16 v[196:199], a[72:75], a[144:147], v[196:199]\n"
"v_add_f32 v77, v77, v21\n"
"v_mfma_f32_16x16x32_bf16 a[20:23], a[72:75], a[152:155], a[20:23]\n"
"s_add_u32 s34, s34, 8192\n"
"v_add_f32 v78, v78, v22\n"
"v_mfma_f32_16x16x32_bf16 a[20:23], a[76:79], a[156:159], a[20:23]\n"
"v_add_f32 v79, v79, v23\n"
"v_mfma_f32_16x16x32_bf16 a[16:19], a[68:71], a[156:159], a[16:19]\n"
"v_add_f32 v80, v80, v20\n"
"v_mfma_f32_16x16x32_bf16 a[16:19], a[64:67], a[152:155], a[16:19]\n"
"s_waitcnt vmcnt(12)\n"
"v_add_f32 v81, v81, v21\n"
"v_mfma_f32_16x16x32_bf16 v[200:203], a[80:83], a[96:99], v[200:203]\n"
"v_add_f32 v82, v82, v22\n"
"v_add_f32 v83, v83, v23\n"
"v_mfma_f32_16x16x32_bf16 v[200:203], a[84:87], a[100:103], v[200:203]\n"
"global_load_dwordx4 a[64:67], v0, s[34:35] offset:-4096 sc1\n"
"v_add_f32 v84, v84, v20\n"
"v_mfma_f32_16x16x32_bf16 v[228:231], a[92:95], a[100:103], v[228:231]\n"
"s_waitcnt vmcnt(12)\n"
"ds_read_b128 a[100:103], v2 offset:1024\n"
"v_add_f32 v85, v85, v21\n"
"v_mfma_f32_16x16x32_bf16 v[228:231], a[88:91], a[96:99], v[228:231]\n"
"ds_read_b128 a[96:99], v2 offset:0\n"
"v_add_f32 v86, v86, v22\n"
"v_mfma_f32_16x16x32_bf16 v[232:235], a[88:91], a[104:107], v[232:235]\n"
"v_add_f32 v87, v87, v23\n"
"v_mfma_f32_16x16x32_bf16 v[232:235], a[92:95], a[108:111], v[232:235]\n"
"v_accvgpr_read_b32 v12, a4\n"
"v_mfma_f32_16x16x32_bf16 v[204:207], a[84:87], a[108:111], v[204:207]\n"
"ds_read_b128 a[108:111], v2 offset:3072\n"
"v_accvgpr_read_b32 v13, a5\n"
"v_mfma_f32_16x16x32_bf16 v[204:207], a[80:83], a[104:107], v[204:207]\n"
"ds_read_b128 a[104:107], v2 offset:2048\n"
"v_accvgpr_read_b32 v14, a6\n"
"v_mfma_f32_16x16x32_bf16 v[208:211], a[80:83], a[112:115], v[208:211]\n"
"v_accvgpr_read_b32 v15, a7\n"
"v_mfma_f32_16x16x32_bf16 v[208:211], a[84:87], a[116:119], v[208:211]\n"
"global_load_dwordx4 a[68:71], v0, s[34:35] offset:-3072 sc1\n"
"v_add_f32 v12, v12, v20\n"
"v_mfma_f32_16x16x32_bf16 v[236:239], a[92:95], a[116:119], v[236:239]\n"
"ds_read_b128 a[116:119], v2 offset:5120\n"
"v_add_f32 v13, v13, v21\n"
"v_mfma_f32_16x16x32_bf16 v[236:239], a[88:91], a[112:115], v[236:239]\n"
"ds_read_b128 a[112:115], v2 offset:4096\n"
"v_add_f32 v14, v14, v22\n"
"v_mfma_f32_16x16x32_bf16 v[240:243], a[88:91], a[120:123], v[240:243]\n"
"v_add_f32 v15, v15, v23\n"
"v_accvgpr_write_b32 a4, v12\n"
"v_mfma_f32_16x16x32_bf16 v[240:243], a[92:95], a[124:127], v[240:243]\n"
"v_accvgpr_write_b32 a5, v13\n"
"v_mfma_f32_16x16x32_bf16 v[212:215], a[84:87], a[124:127], v[212:215]\n"
"ds_read_b128 a[124:127], v2 offset:7168\n"
"v_accvgpr_write_b32 a6, v14\n"
"v_mfma_f32_16x16x32_bf16 v[212:215], a[80:83], a[120:123], v[212:215]\n"
"ds_read_b128 a[120:123], v2 offset:6144\n"
"v_accvgpr_write_b32 a7, v15\n"
"v_mfma_f32_16x16x32_bf16 v[216:219], a[80:83], a[128:131], v[216:219]\n"
"s_add_u32 s28, s28, 0x40000\n"
"v_mfma_f32_16x16x32_bf16 v[216:219], a[84:87], a[132:135], v[216:219]\n"
"L_st9078:\n"
"s_cbranch_execz L_retB\n"
"global_load_dwordx4 a[72:75], v0, s[34:35] offset:-2048 sc1\n"
"v_lshrrev_b32 v20, 4, v0\n"
"v_mfma_f32_16x16x32_bf16 v[244:247], a[92:95], a[132:135], v[244:247]\n"
"s_add_u32 s67, s78, s62\n"
"ds_read_b128 a[132:135], v2 offset:9216\n"
"v_and_b32 v20, 0x30, v20\n"
"v_mfma_f32_16x16x32_bf16 v[244:247], a[88:91], a[128:131], v[244:247]\n"
"ds_read_b128 a[128:131], v2 offset:8192\n"
"buffer_load_dwordx2 v[22:23], v20, s[44:47], s61 offen offset:72\n"
"v_mfma_f32_16x16x32_bf16 v[248:251], a[88:91], a[136:139], v[248:251]\n"
"s_add_u32 s67, s67, 1\n"
"v_mfma_f32_16x16x32_bf16 v[248:251], a[92:95], a[140:143], v[248:251]\n"
"v_mfma_f32_16x16x32_bf16 v[220:223], a[84:87], a[140:143], v[220:223]\n"
"ds_read_b128 a[140:143], v2 offset:11264\n"
"v_mov_b32 v24, s67\n"
"v_mfma_f32_16x16x32_bf16 v[220:223], a[80:83], a[136:139], v[220:223]\n"
"ds_read_b128 a[136:139], v2 offset:10240\n"
"v_mov_b32 v25, s79\n"
"v_mfma_f32_16x16x32_bf16 v[224:227], a[80:83], a[144:147], v[224:227]\n"
"v_mfma_f32_16x16x32_bf16 v[224:227], a[84:87], a[148:151], v[224:227]\n"
"s_waitcnt vmcnt(6)\n"
"global_load_dwordx4 a[76:79], v0, s[34:35] offset:-1024 sc1\n"
"buffer_store_dwordx2 v[24:25], off, s[56:59], %24 sc1\n"
"v_mfma_f32_16x16x32_bf16 v[252:255], a[92:95], a[148:151], v[252:255]\n"
"ds_read_b128 a[148:151], v2 offset:13312\n"
"v_mfma_f32_16x16x32_bf16 v[252:255], a[88:91], a[144:147], v[252:255]\n"
"ds_read_b128 a[144:147], v2 offset:12288\n"
"v_mfma_f32_16x16x32_bf16 a[28:31], a[88:91], a[152:155], a[28:31]\n"
"v_mfma_f32_16x16x32_bf16 a[28:31], a[92:95], a[156:159], a[28:31]\n"
"v_mfma_f32_16x16x32_bf16 a[24:27], a[84:87], a[156:159], a[24:27]\n"
"s_cmp_eq_u32 s62, 5\n"
"ds_read_b128 a[156:159], v2 offset:15360\n"
"v_mfma_f32_16x16x32_bf16 a[24:27], a[80:83], a[152:155], a[24:27]\n"
"ds_read_b128 a[152:155], v2 offset:14336\n"
"s_cbranch_scc1 L_last\n"
"s_branch L_P4\n"
"L_last:\n"
"L_st9115:\n"
"s_cbranch_execz L_retB\n"
"s_waitcnt vmcnt(9) lgkmcnt(12)\n"
"v_mfma_f32_16x16x32_bf16 v[32:35], a[32:35], a[96:99], v[32:35]\n"
"s_add_u32 s34, s36, 1454080\n"
"s_add_u32 s48, s48, 131072\n"
"s_addc_u32 s49, s49, 0\n"
"v_mfma_f32_16x16x32_bf16 v[32:35], a[36:39], a[100:103], v[32:35]\n"
"s_mov_b64 exec, 0\n"
"s_cmp_eq_u32 %16, 0\n"
"s_cbranch_scc1 L_st9657\n"
"s_cmp_eq_u32 %16, 1\n"
"s_cbranch_scc1 L_st10265\n"
"s_cmp_eq_u32 %16, 2\n"
"s_cbranch_scc1 L_st10880\n"
"s_branch L_st11495\n"
"L_retC:\n"
"s_mov_b64 exec, -1\n"
"s_nop 3\n"
"global_load_dwordx4 a[80:83], v0, s[34:35] offset:0 sc1\n"
"v_mfma_f32_16x16x32_bf16 v[60:63], a[44:47], a[100:103], v[60:63]\n"
"s_barrier\n"
"v_mfma_f32_16x16x32_bf16 v[60:63], a[40:43], a[96:99], v[60:63]\n"
"v_mfma_f32_16x16x32_bf16 v[64:67], a[40:43], a[104:107], v[64:67]\n"
"v_mfma_f32_16x16x32_bf16 v[64:67], a[44:47], a[108:111], v[64:67]\n"
"v_mfma_f32_16x16x32_bf16 v[36:39], a[36:39], a[108:111], v[36:39]\n"
"v_subrev_u32 v8, s65, v8\n"
"v_mfma_f32_16x16x32_bf16 v[36:39], a[32:35], a[104:107], v[36:39]\n"
"s_waitcnt lgkmcnt(8)\n"
"v_xor_b32 v9, s79, v9\n"
"v_mfma_f32_16x16x32_bf16 v[40:43], a[32:35], a[112:115], v[40:43]\n"
"v_or_b32 v8, v8, v9\n"
"v_mfma_f32_16x16x32_bf16 v[40:43], a[36:39], a[116:119], v[40:43]\n"
"global_load_dwordx4 a[84:87], v0, s[34:35] offset:1024 sc1\n"
"v_cmp_lt_u32 vcc, 15, v8\n"
"v_mfma_f32_16x16x32_bf16 v[68:71], a[44:47], a[116:119], v[68:71]\n"
"s_cbranch_vccnz L_slow4\n"
"L_back4:\n"
"v_mfma_f32_16x16x32_bf16 v[68:71], a[40:43], a[112:115], v[68:71]\n"
"v_mfma_f32_16x16x32_bf16 v[72:75], a[40:43], a[120:123], v[72:75]\n"
"v_mfma_f32_16x16x32_bf16 v[72:75], a[44:47], a[124:127], v[72:75]\n"
"v_mfma_f32_16x16x32_bf16 v[44:47], a[36:39], a[124:127], v[44:47]\n"
"v_mfma_f32_16x16x32_bf16 v[44:47], a[32:35], a[120:123], v[44:47]\n"
"s_add_u32 m0, s64, 16384\n"
"s_waitcnt lgkmcnt(4)\n"
"v_mfma_f32_16x16x32_bf16 v[48:51], a[32:35], a[128:131], v[48:51]\n"
"v_mfma_f32_16x16x32_bf16 v[48:51], a[36:39], a[132:135], v[48:51]\n"
"global_load_dwordx4 a[88:91], v0, s[34:35] offset:2048 sc1\n"
"v_mfma_f32_16x16x32_bf16 v[76:79], a[44:47], a[132:135], v[76:79]\n"
"v_mfma_f32_16x16x32_bf16 v[76:79], a[40:43], a[128:131], v[76:79]\n"
"v_mfma_f32_16x16x32_bf16 v[80:83], a[40:43], a[136:139], v[80:83]\n"
"s_mov_b32 s97, s37\n"
"v_mfma_f32_16x16x32_bf16 v[80:83], a[44:47], a[140:143], v[80:83]\n"
"v_mfma_f32_16x16x32_bf16 v[52:55], a[36:39], a[140:143], v[52:55]\n"
"v_mfma_f32_16x16x32_bf16 v[52:55], a[32:35], a[136:139], v[52:55]\n"
"s_waitcnt lgkmcnt(0)\n"
"v_mfma_f32_16x16x32_bf16 v[56:59], a[32:35], a[144:147], v[56:59]\n"
"buffer_load_dwordx4 v0, s[48:51], s75 offen offset:0 sc1 lds\n"
"v_mfma_f32_16x16x32_bf16 v[56:59], a[36:39], a[148:151], v[56:59]\n"
"global_load_dwordx4 a[92:95], v0, s[34:35] offset:3072 sc1\n"
"v_mfma_f32_16x16x32_bf16 v[84:87], a[44:47], a[148:151], v[84:87]\n"
"v_mfma_f32_16x16x32_bf16 v[84:87], a[40:43], a[144:147], v[84:87]\n"
"v_mfma_f32_16x16x32_bf16 a[4:7], a[40:43], a[152:155], a[4:7]\n"
"v_mfma_f32_16x16x32_bf16 a[4:7], a[44:47], a[156:159], a[4:7]\n"
"v_mfma_f32_16x16x32_bf16 a[0:3], a[36:39], a[156:159], a[0:3]\n"
"v_mfma_f32_16x16x32_bf16 a[0:3], a[32:35], a[152:155], a[0:3]\n"
"s_waitcnt vmcnt(10)\n"
"v_mfma_f32_16x16x32_bf16 v[88:91], a[48:51], a[96:99], v[88:91]\n"
"s_add_u32 s34, s36, 1458176\n"
"v_mfma_f32_16x16x32_bf16 v[88:91], a[52:55], a[100:103], v[88:91]\n"
"global_load_dwordx4 a[32:35], v0, s[34:35] offset:0 sc1\n"
"v_mfma_f32_16x16x32_bf16 v[116:119], a[60:63], a[100:103], v[116:119]\n"
"buffer_load_dwordx4 v0, s[48:51], s75 offen offset:1024 sc1 lds\n"
"v_mfma_f32_16x16x32_bf16 v[116:119], a[56:59], a[96:99], v[116:119]\n"
"v_mfma_f32_16x16x32_bf16 v[120:123], a[56:59], a[104:107], v[120:123]\n"
"v_mfma_f32_16x16x32_bf16 v[120:123], a[60:63], a[108:111], v[120:123]\n"
"v_mfma_f32_16x16x32_bf16 v[92:95], a[52:55], a[108:111], v[92:95]\n"
"v_mfma_f32_16x16x32_bf16 v[92:95], a[48:51], a[104:107], v[92:95]\n"
"v_mfma_f32_16x16x32_bf16 v[96:99], a[48:51], a[112:115], v[96:99]\n"
"v_mfma_f32_16x16x32_bf16 v[96:99], a[52:55], a[116:119], v[96:99]\n"
"global_load_dwordx4 a[36:39], v0, s[34:35] offset:1024 sc1\n"
"v_mfma_f32_16x16x32_bf16 v[124:127], a[60:63], a[116:119], v[124:127]\n"
"v_mfma_f32_16x16x32_bf16 v[124:127], a[56:59], a[112:115], v[124:127]\n"
"v_mfma_f32_16x16x32_bf16 v[128:131], a[56:59], a[120:123], v[128:131]\n"
"buffer_load_dwordx4 v0, s[48:51], s75 offen offset:2048 sc1 lds\n"
"v_mfma_f32_16x16x32_bf16 v[128:131], a[60:63], a[124:127], v[128:131]\n"
"v_mfma_f32_16x16x32_bf16 v[100:103], a[52:55], a[124:127], v[100:103]\n"
"v_mfma_f32_16x16x32_bf16 v[100:103], a[48:51], a[120:123], v[100:103]\n"
"v_mfma_f32_16x16x32_bf16 v[104:107], a[48:51], a[128:131], v[104:107]\n"
"v_mfma_f32_16x16x32_bf16 v[104:107], a[52:55], a[132:135], v[104:107]\n"
"global_load_dwordx4 a[40:43], v0, s[34:35] offset:2048 sc1\n"
"v_mfma_f32_16x16x32_bf16 v[132:135], a[60:63], a[132:135], v[132:135]\n"
"v_mfma_f32_16x16x32_bf16 v[132:135], a[56:59], a[128:131], v[132:135]\n"
"v_mfma_f32_16x16x32_bf16 v[136:139], a[56:59], a[136:139], v[136:139]\n"
"v_mfma_f32_16x16x32_bf16 v[136:139], a[60:63], a[140:143], v[136:139]\n"
"buffer_load_dwordx4 v0, s[48:51], s75 offen offset:3072 sc1 lds\n"
"v_mfma_f32_16x16x32_bf16 v[108:111], a[52:55], a[140:143], v[108:111]\n"
"v_mfma_f32_16x16x32_bf16 v[108:111], a[48:51], a[136:139], v[108:111]\n"
"v_mfma_f32_16x16x32_bf16 v[112:115], a[48:51], a[144:147], v[112:115]\n"
"v_mfma_f32_16x16x32_bf16 v[112:115], a[52:55], a[148:151], v[112:115]\n"
"global_load_dwordx4 a[44:47], v0, s[34:35] offset:3072 sc1\n"
"v_mfma_f32_16x16x32_bf16 v[140:143], a[60:63], a[148:151], v[140:143]\n"
"v_mfma_f32_16x16x32_bf16 v[140:143], a[56:59], a[144:147], v[140:143]\n"
"v_mfma_f32_16x16x32_bf16 a[12:15], a[56:59], a[152:155], a[12:15]\n"
"v_mfma_f32_16x16x32_bf16 a[12:15], a[60:63], a[156:159], a[12:15]\n"
"v_mfma_f32_16x16x32_bf16 a[8:11], a[52:55], a[156:159], a[8:11]\n"
"v_mfma_f32_16x16x32_bf16 a[8:11], a[48:51], a[152:155], a[8:11]\n"
"s_add_u32 m0, s64, 20480\n"
"s_waitcnt vmcnt(12)\n"
"v_mfma_f32_16x16x32_bf16 v[144:147], a[64:67], a[96:99], v[144:147]\n"
"s_add_u32 s34, s36, 1462272\n"
"v_mfma_f32_16x16x32_bf16 v[144:147], a[68:71], a[100:103], v[144:147]\n"
"global_load_dwordx4 a[48:51], v0, s[34:35] offset:0 sc1\n"
"v_mfma_f32_16x16x32_bf16 v[172:175], a[76:79], a[100:103], v[172:175]\n"
"v_mfma_f32_16x16x32_bf16 v[172:175], a[72:75], a[96:99], v[172:175]\n"
"v_mfma_f32_16x16x32_bf16 v[176:179], a[72:75], a[104:107], v[176:179]\n"
"v_mfma_f32_16x16x32_bf16 v[176:179], a[76:79], a[108:111], v[176:179]\n"
"v_mfma_f32_16x16x32_bf16 v[148:151], a[68:71], a[108:111], v[148:151]\n"
"v_lshlrev_b32 v16, 16, v18\n"
"v_and_b32 v17, s63, v18\n"
"v_mfma_f32_16x16x32_bf16 v[148:151], a[64:67], a[104:107], v[148:151]\n"
"v_lshlrev_b32 v18, 16, v19\n"
"v_and_b32 v19, s63, v19\n"
"v_mfma_f32_16x16x32_bf16 v[152:155], a[64:67], a[112:115], v[152:155]\n"
"buffer_load_dwordx4 v1, s[48:51], s75 offen offset:0 sc1 lds\n"
"v_mfma_f32_16x16x32_bf16 v[152:155], a[68:71], a[116:119], v[152:155]\n"
"global_load_dwordx4 a[52:55], v0, s[34:35] offset:1024 sc1\n"
"v_add_f32 v88, v88, v16\n"
"v_mfma_f32_16x16x32_bf16 v[180:183], a[76:79], a[116:119], v[180:183]\n"
"v_add_f32 v89, v89, v17\n"
"v_add_f32 v90, v90, v18\n"
"v_mfma_f32_16x16x32_bf16 v[180:183], a[72:75], a[112:115], v[180:183]\n"
"v_add_f32 v91, v91, v19\n"
"v_add_f32 v92, v92, v16\n"
"v_mfma_f32_16x16x32_bf16 v[184:187], a[72:75], a[120:123], v[184:187]\n"
"v_add_f32 v93, v93, v17\n"
"v_add_f32 v94, v94, v18\n"
"v_mfma_f32_16x16x32_bf16 v[184:187], a[76:79], a[124:127], v[184:187]\n"
"v_add_f32 v95, v95, v19\n"
"v_add_f32 v96, v96, v16\n"
"v_mfma_f32_16x16x32_bf16 v[156:159], a[68:71], a[124:127], v[156:159]\n"
"v_add_f32 v97, v97, v17\n"
"v_add_f32 v98, v98, v18\n"
"v_mfma_f32_16x16x32_bf16 v[156:159], a[64:67], a[120:123], v[156:159]\n"
"v_add_f32 v99, v99, v19\n"
"v_add_f32 v100, v100, v16\n"
"v_mfma_f32_16x16x32_bf16 v[160:163], a[64:67], a[128:131], v[160:163]\n"
"v_add_f32 v101, v101, v17\n"
"v_add_f32 v102, v102, v18\n"
"v_mfma_f32_16x16x32_bf16 v[160:163], a[68:71], a[132:135], v[160:163]\n"
"global_load_dwordx4 a[56:59], v0, s[34:35] offset:2048 sc1\n"
"v_add_f32 v103, v103, v19\n"
"v_mfma_f32_16x16x32_bf16 v[188:191], a[76:79], a[132:135], v[188:191]\n"
"buffer_load_dwordx4 v1, s[48:51], s75 offen offset:1024 sc1 lds\n"
"v_mfma_f32_16x16x32_bf16 v[188:191], a[72:75], a[128:131], v[188:191]\n"
"v_add_f32 v104, v104, v16\n"
"v_add_f32 v105, v105, v17\n"
"v_mfma_f32_16x16x32_bf16 v[192:195], a[72:75], a[136:139], v[192:195]\n"
"v_add_f32 v106, v106, v18\n"
"v_add_f32 v107, v107, v19\n"
"v_mfma_f32_16x16x32_bf16 v[192:195], a[76:79], a[140:143], v[192:195]\n"
"v_add_f32 v108, v108, v16\n"
"v_add_f32 v109, v109, v17\n"
"v_mfma_f32_16x16x32_bf16 v[164:167], a[68:71], a[140:143], v[164:167]\n"
"v_add_f32 v110, v110, v18\n"
"v_add_f32 v111, v111, v19\n"
"v_mfma_f32_16x16x32_bf16 v[164:167], a[64:67], a[136:139], v[164:167]\n"
"v_add_f32 v112, v112, v16\n"
"v_add_f32 v113, v113, v17\n"
"v_mfma_f32_16x16x32_bf16 v[168:171], a[64:67], a[144:147], v[168:171]\n"
"v_add_f32 v114, v114, v18\n"
"v_add_f32 v115, v115, v19\n"
"v_mfma_f32_16x16x32_bf16 v[168:171], a[68:71], a[148:151], v[168:171]\n"
"global_load_dwordx4 a[60:63], v0, s[34:35] offset:3072 sc1\n"
"v_accvgpr_read_b32 v12, a8\n"
"v_mfma_f32_16x16x32_bf16 v[196:199], a[76:79], a[148:151], v[196:199]\n"
"v_accvgpr_read_b32 v13, a9\n"
"v_accvgpr_read_b32 v14, a10\n"
"v_mfma_f32_16x16x32_bf16 v[196:199], a[72:75], a[144:147], v[196:199]\n"
"buffer_load_dwordx4 v1, s[48:51], s75 offen offset:2048 sc1 lds\n"
"v_mfma_f32_16x16x32_bf16 a[20:23], a[72:75], a[152:155], a[20:23]\n"
"v_accvgpr_read_b32 v15, a11\n"
"v_add_f32 v12, v12, v16\n"
"v_mfma_f32_16x16x32_bf16 a[20:23], a[76:79], a[156:159], a[20:23]\n"
"v_add_f32 v13, v13, v17\n"
"v_add_f32 v14, v14, v18\n"
"v_mfma_f32_16x16x32_bf16 a[16:19], a[68:71], a[156:159], a[16:19]\n"
"v_add_f32 v15, v15, v19\n"
"v_accvgpr_write_b32 a8, v12\n"
"v_mfma_f32_16x16x32_bf16 a[16:19], a[64:67], a[152:155], a[16:19]\n"
"s_waitcnt vmcnt(14)\n"
"v_accvgpr_write_b32 a9, v13\n"
"v_accvgpr_write_b32 a10, v14\n"
"v_accvgpr_write_b32 a11, v15\n"
"v_mfma_f32_16x16x32_bf16 v[200:203], a[80:83], a[96:99], v[200:203]\n"
"s_add_u32 s34, s36, 1474560\n"
"v_lshlrev_b32 v20, 16, v22\n"
"v_and_b32 v21, s63, v22\n"
"v_mfma_f32_16x16x32_bf16 v[200:203], a[84:87], a[100:103], v[200:203]\n"
"global_load_dwordx4 a[64:67], v0, s[34:35] offset:0 sc1\n"
"v_lshlrev_b32 v22, 16, v23\n"
"v_mfma_f32_16x16x32_bf16 v[228:231], a[92:95], a[100:103], v[228:231]\n"
"ds_read_b128 a[100:103], v3 offset:1024\n"
"v_and_b32 v23, s63, v23\n"
"v_mfma_f32_16x16x32_bf16 v[228:231], a[88:91], a[96:99], v[228:231]\n"
"ds_read_b128 a[96:99], v3 offset:0\n"
"v_add_f32 v116, v116, v20\n"
"v_mfma_f32_16x16x32_bf16 v[232:235], a[88:91], a[104:107], v[232:235]\n"
"v_add_f32 v117, v117, v21\n"
"v_add_f32 v118, v118, v22\n"
"v_mfma_f32_16x16x32_bf16 v[232:235], a[92:95], a[108:111], v[232:235]\n"
"buffer_load_dwordx4 v1, s[48:51], s75 offen offset:3072 sc1 lds\n"
"v_mfma_f32_16x16x32_bf16 v[204:207], a[84:87], a[108:111], v[204:207]\n"
"ds_read_b128 a[108:111], v3 offset:3072\n"
"v_add_f32 v119, v119, v23\n"
"v_add_f32 v120, v120, v20\n"
"v_mfma_f32_16x16x32_bf16 v[204:207], a[80:83], a[104:107], v[204:207]\n"
"ds_read_b128 a[104:107], v3 offset:2048\n"
"v_add_f32 v121, v121, v21\n"
"v_mfma_f32_16x16x32_bf16 v[208:211], a[80:83], a[112:115], v[208:211]\n"
"v_add_f32 v122, v122, v22\n"
"v_add_f32 v123, v123, v23\n"
"v_mfma_f32_16x16x32_bf16 v[208:211], a[84:87], a[116:119], v[208:211]\n"
"global_load_dwordx4 a[68:71], v0, s[34:35] offset:1024 sc1\n"
"v_add_f32 v124, v124, v20\n"
"v_add_f32 v125, v125, v21\n"
"v_mfma_f32_16x16x32_bf16 v[236:239], a[92:95], a[116:119], v[236:239]\n"
"ds_read_b128 a[116:119], v3 offset:5120\n"
"v_add_f32 v126, v126, v22\n"
"v_mfma_f32_16x16x32_bf16 v[236:239], a[88:91], a[112:115], v[236:239]\n"
"ds_read_b128 a[112:115], v3 offset:4096\n"
"v_add_f32 v127, v127, v23\n"
"v_mfma_f32_16x16x32_bf16 v[240:243], a[88:91], a[120:123], v[240:243]\n"
"v_add_f32 v128, v128, v20\n"
"v_add_f32 v129, v129, v21\n"
"v_mfma_f32_16x16x32_bf16 v[240:243], a[92:95], a[124:127], v[240:243]\n"
"v_add_f32 v130, v130, v22\n"
"v_add_f32 v131, v131, v23\n"
"v_mfma_f32_16x16x32_bf16 v[212:215], a[84:87], a[124:127], v[212:215]\n"
"s_add_u32 m0, s64, 24576\n"
"ds_read_b128 a[124:127], v3 offset:7168\n"
"v_add_f32 v132, v132, v20\n"
"v_add_f32 v133, v133, v21\n"
"v_mfma_f32_16x16x32_bf16 v[212:215], a[80:83], a[120:123], v[212:215]\n"
"ds_read_b128 a[120:123], v3 offset:6144\n"
"v_add_f32 v134, v134, v22\n"
"v_mfma_f32_16x16x32_bf16 v[216:219], a[80:83], a[128:131], v[216:219]\n"
"v_add_f32 v135, v135, v23\n"
"v_add_f32 v136, v136, v20\n"
"v_mfma_f32_16x16x32_bf16 v[216:219], a[84:87], a[132:135], v[216:219]\n"
"global_load_dwordx4 a[72:75], v0, s[34:35] offset:2048 sc1\n"
"v_add_f32 v137, v137, v21\n"
"v_mfma_f32_16x16x32_bf16 v[244:247], a[92:95], a[132:135], v[244:247]\n"
"ds_read_b128 a[132:135], v3 offset:9216\n"
"v_add_f32 v138, v138, v22\n"
"v_add_f32 v139, v139, v23\n"
"v_mfma_f32_16x16x32_bf16 v[244:247], a[88:91], a[128:131], v[244:247]\n"
"ds_read_b128 a[128:131], v3 offset:8192\n"
"v_add_f32 v140, v140, v20\n"
"v_mfma_f32_16x16x32_bf16 v[248:251], a[88:91], a[136:139], v[248:251]\n"
"v_add_f32 v141, v141, v21\n"
"v_add_f32 v142, v142, v22\n"
"v_mfma_f32_16x16x32_bf16 v[248:251], a[92:95], a[140:143], v[248:251]\n"
"v_add_f32 v143, v143, v23\n"
"v_accvgpr_read_b32 v12, a12\n"
"v_mfma_f32_16x16x32_bf16 v[220:223], a[84:87], a[140:143], v[220:223]\n"
"ds_read_b128 a[140:143], v3 offset:11264\n"
"v_accvgpr_read_b32 v13, a13\n"
"v_mfma_f32_16x16x32_bf16 v[220:223], a[80:83], a[136:139], v[220:223]\n"
"ds_read_b128 a[136:139], v3 offset:10240\n"
"v_accvgpr_read_b32 v14, a14\n"
"v_accvgpr_read_b32 v15, a15\n"
"v_mfma_f32_16x16x32_bf16 v[224:227], a[80:83], a[144:147], v[224:227]\n"
"buffer_load_dwordx4 v0, s[48:51], s69 offen offset:0 sc1 lds\n"
"v_add_f32 v12, v12, v20\n"
"v_mfma_f32_16x16x32_bf16 v[224:227], a[84:87], a[148:151], v[224:227]\n"
"global_load_dwordx4 a[76:79], v0, s[34:35] offset:3072 sc1\n"
"v_add_f32 v13, v13, v21\n"
"v_add_f32 v14, v14, v22\n"
"v_mfma_f32_16x16x32_bf16 v[252:255], a[92:95], a[148:151], v[252:255]\n"
"ds_read_b128 a[148:151], v3 offset:13312\n"
"v_add_f32 v15, v15, v23\n"
"v_accvgpr_write_b32 a12, v12\n"
"v_mfma_f32_16x16x32_bf16 v[252:255], a[88:91], a[144:147], v[252:255]\n"
"ds_read_b128 a[144:147], v3 offset:12288\n"
"v_accvgpr_write_b32 a13, v13\n"
"v_mfma_f32_16x16x32_bf16 a[28:31], a[88:91], a[152:155], a[28:31]\n"
"v_accvgpr_write_b32 a14, v14\n"
"v_accvgpr_write_b32 a15, v15\n"
"v_mfma_f32_16x16x32_bf16 a[28:31], a[92:95], a[156:159], a[28:31]\n"
"v_mfma_f32_16x16x32_bf16 a[24:27], a[84:87], a[156:159], a[24:27]\n"
"ds_read_b128 a[156:159], v3 offset:15360\n"
"v_mfma_f32_16x16x32_bf16 a[24:27], a[80:83], a[152:155], a[24:27]\n"
"s_waitcnt vmcnt(13) lgkmcnt(11)\n"
"ds_read_b128 a[152:155], v3 offset:14336\n"
"v_mfma_f32_16x16x32_bf16 v[32:35], a[32:35], a[96:99], v[32:35]\n"
"s_add_u32 s34, s36, 1478656\n"
"v_mfma_f32_16x16x32_bf16 v[32:35], a[36:39], a[100:103], v[32:35]\n"
"global_load_dwordx4 a[80:83], v0, s[34:35] offset:0 sc1\n"
"v_mfma_f32_16x16x32_bf16 v[60:63], a[44:47], a[100:103], v[60:63]\n"
"buffer_load_dwordx4 v0, s[48:51], s69 offen offset:1024 sc1 lds\n"
"v_mfma_f32_16x16x32_bf16 v[60:63], a[40:43], a[96:99], v[60:63]\n"
"v_lshrrev_b32 v16, 4, v0\n"
"v_mfma_f32_16x16x32_bf16 v[64:67], a[40:43], a[104:107], v[64:67]\n"
"v_mfma_f32_16x16x32_bf16 v[64:67], a[44:47], a[108:111], v[64:67]\n"
"v_mfma_f32_16x16x32_bf16 v[36:39], a[36:39], a[108:111], v[36:39]\n"
"v_mfma_f32_16x16x32_bf16 v[36:39], a[32:35], a[104:107], v[36:39]\n"
"s_waitcnt lgkmcnt(8)\n"
"v_and_b32 v16, 0x30, v16\n"
"v_mfma_f32_16x16x32_bf16 v[40:43], a[32:35], a[112:115], v[40:43]\n"
"v_mfma_f32_16x16x32_bf16 v[40:43], a[36:39], a[116:119], v[40:43]\n"
"global_load_dwordx4 a[84:87], v0, s[34:35] offset:1024 sc1\n"
"v_mfma_f32_16x16x32_bf16 v[68:71], a[44:47], a[116:119], v[68:71]\n"
"buffer_load_dwordx2 v[18:19], v16, s[44:47], s61 offen offset:128\n"
"v_mfma_f32_16x16x32_bf16 v[68:71], a[40:43], a[112:115], v[68:71]\n"
"buffer_load_dwordx4 v0, s[48:51], s69 offen offset:2048 sc1 lds\n"
"v_mfma_f32_16x16x32_bf16 v[72:75], a[40:43], a[120:123], v[72:75]\n"
"v_mfma_f32_16x16x32_bf16 v[72:75], a[44:47], a[124:127], v[72:75]\n"
"v_lshrrev_b32 v20, 4, v0\n"
"v_mfma_f32_16x16x32_bf16 v[44:47], a[36:39], a[124:127], v[44:47]\n"
"v_mfma_f32_16x16x32_bf16 v[44:47], a[32:35], a[120:123], v[44:47]\n"
"s_waitcnt lgkmcnt(4)\n"
"v_mfma_f32_16x16x32_bf16 v[48:51], a[32:35], a[128:131], v[48:51]\n"
"v_and_b32 v20, 0x30, v20\n"
"v_mfma_f32_16x16x32_bf16 v[48:51], a[36:39], a[132:135], v[48:51]\n"
"global_load_dwordx4 a[88:91], v0, s[34:35] offset:2048 sc1\n"
"v_mfma_f32_16x16x32_bf16 v[76:79], a[44:47], a[132:135], v[76:79]\n"
"v_mfma_f32_16x16x32_bf16 v[76:79], a[40:43], a[128:131], v[76:79]\n"
"v_mfma_f32_16x16x32_bf16 v[80:83], a[40:43], a[136:139], v[80:83]\n"
"buffer_load_dwordx4 v0, s[48:51], s69 offen offset:3072 sc1 lds\n"
"v_mfma_f32_16x16x32_bf16 v[80:83], a[44:47], a[140:143], v[80:83]\n"
"buffer_load_dwordx2 v[22:23], v20, s[44:47], s61 offen offset:136\n"
"v_mfma_f32_16x16x32_bf16 v[52:55], a[36:39], a[140:143], v[52:55]\n"
"v_mfma_f32_16x16x32_bf16 v[52:55], a[32:35], a[136:139], v[52:55]\n"
"s_waitcnt lgkmcnt(0)\n"
"v_mfma_f32_16x16x32_bf16 v[56:59], a[32:35], a[144:147], v[56:59]\n"
"v_lshrrev_b32 v24, 4, v0\n"
"v_mfma_f32_16x16x32_bf16 v[56:59], a[36:39], a[148:151], v[56:59]\n"
"global_load_dwordx4 a[92:95], v0, s[34:35] offset:3072 sc1\n"
"v_mfma_f32_16x16x32_bf16 v[84:87], a[44:47], a[148:151], v[84:87]\n"
"v_mfma_f32_16x16x32_bf16 v[84:87], a[40:43], a[144:147], v[84:87]\n"
"v_and_b32 v24, 0x30, v24\n"
"v_mfma_f32_16x16x32_bf16 a[4:7], a[40:43], a[152:155], a[4:7]\n"
"v_mfma_f32_16x16x32_bf16 a[4:7], a[44:47], a[156:159], a[4:7]\n"
"v_mfma_f32_16x16x32_bf16 a[0:3], a[36:39], a[156:159], a[0:3]\n"
"s_add_u32 m0, s64, 28672\n"
"v_mfma_f32_16x16x32_bf16 a[0:3], a[32:35], a[152:155], a[0:3]\n"
"s_waitcnt vmcnt(16)\n"
"buffer_load_dwordx2 v[26:27], v24, s[44:47], s61 offen offset:192\n"
"v_mfma_f32_16x16x32_bf16 v[88:91], a[48:51], a[96:99], v[88:91]\n"
"s_add_u32 s34, s36, 1490944\n"
"v_lshrrev_b32 v28, 4, v0\n"
"v_mfma_f32_16x16x32_bf16 v[88:91], a[52:55], a[100:103], v[88:91]\n"
"global_load_dwordx4 a[32:35], v0, s[34:35] offset:0 sc1\n"
"v_mfma_f32_16x16x32_bf16 v[116:119], a[60:63], a[100:103], v[116:119]\n"
"ds_read_b128 a[100:103], v4 offset:1024\n"
"v_mfma_f32_16x16x32_bf16 v[116:119], a[56:59], a[96:99], v[116:119]\n"
"ds_read_b128 a[96:99], v4 offset:0\n"
"v_mfma_f32_16x16x32_bf16 v[120:123], a[56:59], a[104:107], v[120:123]\n"
"v_and_b32 v28, 0x30, v28\n"
"v_mfma_f32_16x16x32_bf16 v[120:123], a[60:63], a[108:111], v[120:123]\n"
"buffer_load_dwordx2 v[30:31], v28, s[44:47], s61 offen offset:200\n"
"v_mfma_f32_16x16x32_bf16 v[92:95], a[52:55], a[108:111], v[92:95]\n"
"ds_read_b128 a[108:111], v4 offset:3072\n"
"v_mfma_f32_16x16x32_bf16 v[92:95], a[48:51], a[104:107], v[92:95]\n"
"ds_read_b128 a[104:107], v4 offset:2048\n"
"v_mfma_f32_16x16x32_bf16 v[96:99], a[48:51], a[112:115], v[96:99]\n"
"buffer_load_dwordx4 v1, s[48:51], s69 offen offset:0 sc1 lds\n"
"v_mfma_f32_16x16x32_bf16 v[96:99], a[52:55], a[116:119], v[96:99]\n"
"global_load_dwordx4 a[36:39], v0, s[34:35] offset:1024 sc1\n"
"v_mfma_f32_16x16x32_bf16 v[124:127], a[60:63], a[116:119], v[124:127]\n"
"ds_read_b128 a[116:119], v4 offset:5120\n"
"v_mfma_f32_16x16x32_bf16 v[124:127], a[56:59], a[112:115], v[124:127]\n"
"ds_read_b128 a[112:115], v4 offset:4096\n"
"v_mfma_f32_16x16x32_bf16 v[128:131], a[56:59], a[120:123], v[128:131]\n"
"v_mfma_f32_16x16x32_bf16 v[128:131], a[60:63], a[124:127], v[128:131]\n"
"v_mfma_f32_16x16x32_bf16 v[100:103], a[52:55], a[124:127], v[100:103]\n"
"ds_read_b128 a[124:127], v4 offset:7168\n"
"v_mfma_f32_16x16x32_bf16 v[100:103], a[48:51], a[120:123], v[100:103]\n"
"ds_read_b128 a[120:123], v4 offset:6144\n"
"v_mfma_f32_16x16x32_bf16 v[104:107], a[48:51], a[128:131], v[104:107]\n"
"buffer_load_dwordx4 v1, s[48:51], s69 offen offset:1024 sc1 lds\n"
"v_mfma_f32_16x16x32_bf16 v[104:107], a[52:55], a[132:135], v[104:107]\n"
"global_load_dwordx4 a[40:43], v0, s[34:35] offset:2048 sc1\n"
"v_mfma_f32_16x16x32_bf16 v[132:135], a[60:63], a[132:135], v[132:135]\n"
"ds_read_b128 a[132:135], v4 offset:9216\n"
"v_mfma_f32_16x16x32_bf16 v[132:135], a[56:59], a[128:131], v[132:135]\n"
"ds_read_b128 a[128:131], v4 offset:8192\n"
"v_mfma_f32_16x16x32_bf16 v[136:139], a[56:59], a[136:139], v[136:139]\n"
"v_mfma_f32_16x16x32_bf16 v[136:139], a[60:63], a[140:143], v[136:139]\n"
"v_mfma_f32_16x16x32_bf16 v[108:111], a[52:55], a[140:143], v[108:111]\n"
"ds_read_b128 a[140:143], v4 offset:11264\n"
"v_mfma_f32_16x16x32_bf16 v[108:111], a[48:51], a[136:139], v[108:111]\n"
"ds_read_b128 a[136:139], v4 offset:10240\n"
"v_mfma_f32_16x16x32_bf16 v[112:115], a[48:51], a[144:147], v[112:115]\n"
"v_mfma_f32_16x16x32_bf16 v[112:115], a[52:55], a[148:151], v[112:115]\n"
"global_load_dwordx4 a[44:47], v0, s[34:35] offset:3072 sc1\n"
"v_mfma_f32_16x16x32_bf16 v[140:143], a[60:63], a[148:151], v[140:143]\n"
"ds_read_b128 a[148:151], v4 offset:13312\n"
"v_mfma_f32_16x16x32_bf16 v[140:143], a[56:59], a[144:147], v[140:143]\n"
"ds_read_b128 a[144:147], v4 offset:12288\n"
"v_mfma_f32_16x16x32_bf16 a[12:15], a[56:59], a[152:155], a[12:15]\n"
"buffer_load_dwordx4 v1, s[48:51], s69 offen offset:2048 sc1 lds\n"
"v_mfma_f32_16x16x32_bf16 a[12:15], a[60:63], a[156:159], a[12:15]\n"
"v_mfma_f32_16x16x32_bf16 a[8:11], a[52:55], a[156:159], a[8:11]\n"
"ds_read_b128 a[156:159], v4 offset:15360\n"
"v_mfma_f32_16x16x32_bf16 a[8:11], a[48:51], a[152:155], a[8:11]\n"
"s_waitcnt vmcnt(18) lgkmcnt(11)\n"
"ds_read_b128 a[152:155], v4 offset:14336\n"
"v_mfma_f32_16x16x32_bf16 v[32:35], a[64:67], a[96:99], v[32:35]\n"
"s_add_u32 s34, s36, 1495040\n"
"v_mfma_f32_16x16x32_bf16 v[32:35], a[68:71], a[100:103], v[32:35]\n"
"global_load_dwordx4 a[48:51], v0, s[34:35] offset:0 sc1\n"
"v_mfma_f32_16x16x32_bf16 v[60:63], a[76:79], a[100:103], v[60:63]\n"
"v_mfma_f32_16x16x32_bf16 v[60:63], a[72:75], a[96:99], v[60:63]\n"
"v_mfma_f32_16x16x32_bf16 v[64:67], a[72:75], a[104:107], v[64:67]\n"
"buffer_load_dwordx4 v1, s[48:51], s69 offen offset:3072 sc1 lds\n"
"v_mfma_f32_16x16x32_bf16 v[64:67], a[76:79], a[108:111], v[64:67]\n"
"v_mfma_f32_16x16x32_bf16 v[36:39], a[68:71], a[108:111], v[36:39]\n"
"v_mfma_f32_16x16x32_bf16 v[36:39], a[64:67], a[104:107], v[36:39]\n"
"s_waitcnt lgkmcnt(8)\n"
"v_mfma_f32_16x16x32_bf16 v[40:43], a[64:67], a[112:115], v[40:43]\n"
"v_mfma_f32_16x16x32_bf16 v[40:43], a[68:71], a[116:119], v[40:43]\n"
"global_load_dwordx4 a[52:55], v0, s[34:35] offset:1024 sc1\n"
"v_mfma_f32_16x16x32_bf16 v[68:71], a[76:79], a[116:119], v[68:71]\n"
"v_mfma_f32_16x16x32_bf16 v[68:71], a[72:75], a[112:115], v[68:71]\n"
"v_mfma_f32_16x16x32_bf16 v[72:75], a[72:75], a[120:123], v[72:75]\n"
"v_mfma_f32_16x16x32_bf16 v[72:75], a[76:79], a[124:127], v[72:75]\n"
"v_mfma_f32_16x16x32_bf16 v[44:47], a[68:71], a[124:127], v[44:47]\n"
"v_mfma_f32_16x16x32_bf16 v[44:47], a[64:67], a[120:123], v[44:47]\n"
"s_waitcnt lgkmcnt(4)\n"
"v_mfma_f32_16x16x32_bf16 v[48:51], a[64:67], a[128:131], v[48:51]\n"
"v_mfma_f32_16x16x32_bf16 v[48:51], a[68:71], a[132:135], v[48:51]\n"
"global_load_dwordx4 a[56:59], v0, s[34:35] offset:2048 sc1\n"
"v_mfma_f32_16x16x32_bf16 v[76:79], a[76:79], a[132:135], v[76:79]\n"
"v_mfma_f32_16x16x32_bf16 v[76:79], a[72:75], a[128:131], v[76:79]\n"
"v_mfma_f32_16x16x32_bf16 v[80:83], a[72:75], a[136:139], v[80:83]\n"
"v_mfma_f32_16x16x32_bf16 v[80:83], a[76:79], a[140:143], v[80:83]\n"
"v_mfma_f32_16x16x32_bf16 v[52:55], a[68:71], a[140:143], v[52:55]\n"
"v_mfma_f32_16x16x32_bf16 v[52:55], a[64:67], a[136:139], v[52:55]\n"
"s_waitcnt lgkmcnt(0)\n"
"v_mfma_f32_16x16x32_bf16 v[56:59], a[64:67], a[144:147], v[56:59]\n"
"v_mfma_f32_16x16x32_bf16 v[56:59], a[68:71], a[148:151], v[56:59]\n"
"global_load_dwordx4 a[60:63], v0, s[34:35] offset:3072 sc1\n"
"v_mfma_f32_16x16x32_bf16 v[84:87], a[76:79], a[148:151], v[84:87]\n"
"v_mfma_f32_16x16x32_bf16 v[84:87], a[72:75], a[144:147], v[84:87]\n"
"v_mfma_f32_16x16x32_bf16 a[4:7], a[72:75], a[152:155], a[4:7]\n"
"v_mfma_f32_16x16x32_bf16 a[4:7], a[76:79], a[156:159], a[4:7]\n"
"v_mfma_f32_16x16x32_bf16 a[0:3], a[68:71], a[156:159], a[0:3]\n"
"v_mfma_f32_16x16x32_bf16 a[0:3], a[64:67], a[152:155], a[0:3]\n"
"s_waitcnt vmcnt(14)\n"
"v_mfma_f32_16x16x32_bf16 v[88:91], a[80:83], a[96:99], v[88:91]\n"
"s_add_u32 s34, s36, 1507328\n"
"v_mfma_f32_16x16x32_bf16 v[88:91], a[84:87], a[100:103], v[88:91]\n"
"global_load_dwordx4 a[64:67], v0, s[34:35] offset:0 sc1\n"
"v_mfma_f32_16x16x32_bf16 v[116:119], a[92:95], a[100:103], v[116:119]\n"
"ds_read_b128 a[100:103], v5 offset:1024\n"
"v_mfma_f32_16x16x32_bf16 v[116:119], a[88:91], a[96:99], v[116:119]\n"
"ds_read_b128 a[96:99], v5 offset:0\n"
"v_mfma_f32_16x16x32_bf16 v[120:123], a[88:91], a[104:107], v[120:123]\n"
"v_mfma_f32_16x16x32_bf16 v[120:123], a[92:95], a[108:111], v[120:123]\n"
"v_mfma_f32_16x16x32_bf16 v[92:95], a[84:87], a[108:111], v[92:95]\n"
"ds_read_b128 a[108:111], v5 offset:3072\n"
"v_mfma_f32_16x16x32_bf16 v[92:95], a[80:83], a[104:107], v[92:95]\n"
"ds_read_b128 a[104:107], v5 offset:2048\n"
"v_mfma_f32_16x16x32_bf16 v[96:99], a[80:83], a[112:115], v[96:99]\n"
"v_mfma_f32_16x16x32_bf16 v[96:99], a[84:87], a[116:119], v[96:99]\n"
"global_load_dwordx4 a[68:71], v0, s[34:35] offset:1024 sc1\n"
"v_mfma_f32_16x16x32_bf16 v[124:127], a[92:95], a[116:119], v[124:127]\n"
"ds_read_b128 a[116:119], v5 offset:5120\n"
"v_mfma_f32_16x16x32_bf16 v[124:127], a[88:91], a[112:115], v[124:127]\n"
"ds_read_b128 a[112:115], v5 offset:4096\n"
"v_mfma_f32_16x16x32_bf16 v[128:131], a[88:91], a[120:123], v[128:131]\n"
"v_mfma_f32_16x16x32_bf16 v[128:131], a[92:95], a[124:127], v[128:131]\n"
"v_mfma_f32_16x16x32_bf16 v[100:103], a[84:87], a[124:127], v[100:103]\n"
"ds_read_b128 a[124:127], v5 offset:7168\n"
"v_mfma_f32_16x16x32_bf16 v[100:103], a[80:83], a[120:123], v[100:103]\n"
"ds_read_b128 a[120:123], v5 offset:6144\n"
"v_mfma_f32_16x16x32_bf16 v[104:107], a[80:83], a[128:131], v[104:107]\n"
"v_mfma_f32_16x16x32_bf16 v[104:107], a[84:87], a[132:135], v[104:107]\n"
"global_load_dwordx4 a[72:75], v0, s[34:35] offset:2048 sc1\n"
"v_mfma_f32_16x16x32_bf16 v[132:135], a[92:95], a[132:135], v[132:135]\n"
"ds_read_b128 a[132:135], v5 offset:9216\n"
"v_mfma_f32_16x16x32_bf16 v[132:135], a[88:91], a[128:131], v[132:135]\n"
"ds_read_b128 a[128:131], v5 offset:8192\n"
"v_mfma_f32_16x16x32_bf16 v[136:139], a[88:91], a[136:139], v[136:139]\n"
"v_mfma_f32_16x16x32_bf16 v[136:139], a[92:95], a[140:143], v[136:139]\n"
"v_mfma_f32_16x16x32_bf16 v[108:111], a[84:87], a[140:143], v[108:111]\n"
"ds_read_b128 a[140:143], v5 offset:11264\n"
"v_mfma_f32_16x16x32_bf16 v[108:111], a[80:83], a[136:139], v[108:111]\n"
"ds_read_b128 a[136:139], v5 offset:10240\n"
"v_mfma_f32_16x16x32_bf16 v[112:115], a[80:83], a[144:147], v[112:115]\n"
"v_mfma_f32_16x16x32_bf16 v[112:115], a[84:87], a[148:151], v[112:115]\n"
"global_load_dwordx4 a[76:79], v0, s[34:35] offset:3072 sc1\n"
"v_mfma_f32_16x16x32_bf16 v[140:143], a[92:95], a[148:151], v[140:143]\n"
"ds_read_b128 a[148:151], v5 offset:13312\n"
"v_mfma_f32_16x16x32_bf16 v[140:143], a[88:91], a[144:147], v[140:143]\n"
"ds_read_b128 a[144:147], v5 offset:12288\n"
"v_mfma_f32_16x16x32_bf16 a[12:15], a[88:91], a[152:155], a[12:15]\n"
"v_mfma_f32_16x16x32_bf16 a[12:15], a[92:95], a[156:159], a[12:15]\n"
"v_mfma_f32_16x16x32_bf16 a[8:11], a[84:87], a[156:159], a[8:11]\n"
"ds_read_b128 a[156:159], v5 offset:15360\n"
"v_mfma_f32_16x16x32_bf16 a[8:11], a[80:83], a[152:155], a[8:11]\n"
"s_waitcnt vmcnt(10) lgkmcnt(11)\n"
"ds_read_b128 a[152:155], v5 offset:14336\n"
"v_mfma_f32_16x16x32_bf16 v[32:35], a[32:35], a[96:99], v[32:35]\n"
"s_add_u32 s34, s36, 1511424\n"
"v_mfma_f32_16x16x32_bf16 v[32:35], a[36:39], a[100:103], v[32:35]\n"
"global_load_dwordx4 a[80:83], v0, s[34:35] offset:0 sc1\n"
"v_mfma_f32_16x16x32_bf16 v[60:63], a[44:47], a[100:103], v[60:63]\n"
"v_mfma_f32_16x16x32_bf16 v[60:63], a[40:43], a[96:99], v[60:63]\n"
"v_mfma_f32_16x16x32_bf16 v[64:67], a[40:43], a[104:107], v[64:67]\n"
"v_mfma_f32_16x16x32_bf16 v[64:67], a[44:47], a[108:111], v[64:67]\n"
"v_mfma_f32_16x16x32_bf16 v[36:39], a[36:39], a[108:111], v[36:39]\n"
"v_mfma_f32_16x16x32_bf16 v[36:39], a[32:35], a[104:107], v[36:39]\n"
"s_waitcnt lgkmcnt(8)\n"
"v_mfma_f32_16x16x32_bf16 v[40:43], a[32:35], a[112:115], v[40:43]\n"
"v_mfma_f32_16x16x32_bf16 v[40:43], a[36:39], a[116:119], v[40:43]\n"
"global_load_dwordx4 a[84:87], v0, s[34:35] offset:1024 sc1\n"
"v_mfma_f32_16x16x32_bf16 v[68:71], a[44:47], a[116:119], v[68:71]\n"
"v_mfma_f32_16x16x32_bf16 v[68:71], a[40:43], a[112:115], v[68:71]\n"
"v_mfma_f32_16x16x32_bf16 v[72:75], a[40:43], a[120:123], v[72:75]\n"
"v_mfma_f32_16x16x32_bf16 v[72:75], a[44:47], a[124:127], v[72:75]\n"
"v_mfma_f32_16x16x32_bf16 v[44:47], a[36:39], a[124:127], v[44:47]\n"
"v_mfma_f32_16x16x32_bf16 v[44:47], a[32:35], a[120:123], v[44:47]\n"
"s_waitcnt lgkmcnt(4)\n"
"v_mfma_f32_16x16x32_bf16 v[48:51], a[32:35], a[128:131], v[48:51]\n"
"v_mfma_f32_16x16x32_bf16 v[48:51], a[36:39], a[132:135], v[48:51]\n"
"global_load_dwordx4 a[88:91], v0, s[34:35] offset:2048 sc1\n"
"v_mfma_f32_16x16x32_bf16 v[76:79], a[44:47], a[132:135], v[76:79]\n"
"v_mfma_f32_16x16x32_bf16 v[76:79], a[40:43], a[128:131], v[76:79]\n"
"v_mfma_f32_16x16x32_bf16 v[80:83], a[40:43], a[136:139], v[80:83]\n"
"v_mfma_f32_16x16x32_bf16 v[80:83], a[44:47], a[140:143], v[80:83]\n"
"v_mfma_f32_16x16x32_bf16 v[52:55], a[36:39], a[140:143], v[52:55]\n"
"v_mfma_f32_16x16x32_bf16 v[52:55], a[32:35], a[136:139], v[52:55]\n"
"s_waitcnt lgkmcnt(0)\n"
"v_mfma_f32_16x16x32_bf16 v[56:59], a[32:35], a[144:147], v[56:59]\n"
"v_mfma_f32_16x16x32_bf16 v[56:59], a[36:39], a[148:151], v[56:59]\n"
"global_load_dwordx4 a[92:95], v0, s[34:35] offset:3072 sc1\n"
"v_mfma_f32_16x16x32_bf16 v[84:87], a[44:47], a[148:151], v[84:87]\n"
"v_mfma_f32_16x16x32_bf16 v[84:87], a[40:43], a[144:147], v[84:87]\n"
"v_mfma_f32_16x16x32_bf16 a[4:7], a[40:43], a[152:155], a[4:7]\n"
"v_mfma_f32_16x16x32_bf16 a[4:7], a[44:47], a[156:159], a[4:7]\n"
"v_mfma_f32_16x16x32_bf16 a[0:3], a[36:39], a[156:159], a[0:3]\n"
"L_st9657:\n"
"s_cbranch_execz L_retC\n"
"v_mfma_f32_16x16x32_bf16 a[0:3], a[32:35], a[152:155], a[0:3]\n"
"s_waitcnt vmcnt(8)\n"
"v_mfma_f32_16x16x32_bf16 v[88:91], a[48:51], a[96:99], v[88:91]\n"
"s_add_u32 s34, s36, 1515520\n"
"v_mfma_f32_16x16x32_bf16 v[88:91], a[52:55], a[100:103], v[88:91]\n"
"global_load_dwordx4 a[32:35], v0, s[34:35] offset:0 sc1\n"
"v_mfma_f32_16x16x32_bf16 v[116:119], a[60:63], a[100:103], v[116:119]\n"
"ds_read_b128 a[100:103], v2 offset:17408\n"
"v_mfma_f32_16x16x32_bf16 v[116:119], a[56:59], a[96:99], v[116:119]\n"
"ds_read_b128 a[96:99], v2 offset:16384\n"
"v_mfma_f32_16x16x32_bf16 v[120:123], a[56:59], a[104:107], v[120:123]\n"
"v_mfma_f32_16x16x32_bf16 v[120:123], a[60:63], a[108:111], v[120:123]\n"
"v_mfma_f32_16x16x32_bf16 v[92:95], a[52:55], a[108:111], v[92:95]\n"
"ds_read_b128 a[108:111], v2 offset:19456\n"
"v_mfma_f32_16x16x32_bf16 v[92:95], a[48:51], a[104:107], v[92:95]\n"
"ds_read_b128 a[104:107], v2 offset:18432\n"
"v_mfma_f32_16x16x32_bf16 v[96:99], a[48:51], a[112:115], v[96:99]\n"
"v_mfma_f32_16x16x32_bf16 v[96:99], a[52:55], a[116:119], v[96:99]\n"
"global_load_dwordx4 a[36:39], v0, s[34:35] offset:1024 sc1\n"
"v_mfma_f32_16x16x32_bf16 v[124:127], a[60:63], a[116:119], v[124:127]\n"
"ds_read_b128 a[116:119], v2 offset:21504\n"
"v_mfma_f32_16x16x32_bf16 v[124:127], a[56:59], a[112:115], v[124:127]\n"
"ds_read_b128 a[112:115], v2 offset:20480\n"
"v_mfma_f32_16x16x32_bf16 v[128:131], a[56:59], a[120:123], v[128:131]\n"
"v_mfma_f32_16x16x32_bf16 v[128:131], a[60:63], a[124:127], v[128:131]\n"
"v_mfma_f32_16x16x32_bf16 v[100:103], a[52:55], a[124:127], v[100:103]\n"
"ds_read_b128 a[124:127], v2 offset:23552\n"
"v_mfma_f32_16x16x32_bf16 v[100:103], a[48:51], a[120:123], v[100:103]\n"
"ds_read_b128 a[120:123], v2 offset:22528\n"
"v_mfma_f32_16x16x32_bf16 v[104:107], a[48:51], a[128:131], v[104:107]\n"
"v_mfma_f32_16x16x32_bf16 v[104:107], a[52:55], a[132:135], v[104:107]\n"
"global_load_dwordx4 a[40:43], v0, s[34:35] offset:2048 sc1\n"
"v_mfma_f32_16x16x32_bf16 v[132:135], a[60:63], a[132:135], v[132:135]\n"
"ds_read_b128 a[132:135], v2 offset:25600\n"
"v_mfma_f32_16x16x32_bf16 v[132:135], a[56:59], a[128:131], v[132:135]\n"
"ds_read_b128 a[128:131], v2 offset:24576\n"
"v_mfma_f32_16x16x32_bf16 v[136:139], a[56:59], a[136:139], v[136:139]\n"
"v_mfma_f32_16x16x32_bf16 v[136:139], a[60:63], a[140:143], v[136:139]\n"
"v_mfma_f32_16x16x32_bf16 v[108:111], a[52:55], a[140:143], v[108:111]\n"
"ds_read_b128 a[140:143], v2 offset:27648\n"
"v_mfma_f32_16x16x32_bf16 v[108:111], a[48:51], a[136:139], v[108:111]\n"
"ds_read_b128 a[136:139], v2 offset:26624\n"
"v_mfma_f32_16x16x32_bf16 v[112:115], a[48:51], a[144:147], v[112:115]\n"
"v_mfma_f32_16x16x32_bf16 v[112:115], a[52:55], a[148:151], v[112:115]\n"
"global_load_dwordx4 a[44:47], v0, s[34:35] offset:3072 sc1\n"
"v_mfma_f32_16x16x32_bf16 v[140:143], a[60:63], a[148:151], v[140:143]\n"
"ds_read_b128 a[148:151], v2 offset:29696\n"
"v_mfma_f32_16x16x32_bf16 v[140:143], a[56:59], a[144:147], v[140:143]\n"
"ds_read_b128 a[144:147], v2 offset:28672\n"
"v_mfma_f32_16x16x32_bf16 a[12:15], a[56:59], a[152:155], a[12:15]\n"
"v_mfma_f32_16x16x32_bf16 a[12:15], a[60:63], a[156:159], a[12:15]\n"
"v_mfma_f32_16x16x32_bf16 a[8:11], a[52:55], a[156:159], a[8:11]\n"
"ds_read_b128 a[156:159], v2 offset:31744\n"
"v_mfma_f32_16x16x32_bf16 a[8:11], a[48:51], a[152:155], a[8:11]\n"
"s_waitcnt vmcnt(8) lgkmcnt(11)\n"
"ds_read_b128 a[152:155], v2 offset:30720\n"
"v_mfma_f32_16x16x32_bf16 v[32:35], a[64:67], a[96:99], v[32:35]\n"
"s_add_u32 s34, s36, 1519616\n"
"v_mfma_f32_16x16x32_bf16 v[32:35], a[68:71], a[100:103], v[32:35]\n"
"global_load_dwordx4 a[48:51], v0, s[34:35] offset:0 sc1\n"
"v_mfma_f32_16x16x32_bf16 v[60:63], a[76:79], a[100:103], v[60:63]\n"
"s_barrier\n"
"v_mfma_f32_16x16x32_bf16 v[60:63], a[72:75], a[96:99], v[60:63]\n"
"v_mfma_f32_16x16x32_bf16 v[64:67], a[72:75], a[104:107], v[64:67]\n"
"v_mfma_f32_16x16x32_bf16 v[64:67], a[76:79], a[108:111], v[64:67]\n"
"v_mfma_f32_16x16x32_bf16 v[36:39], a[68:71], a[108:111], v[36:39]\n"
"v_mfma_f32_16x16x32_bf16 v[36:39], a[64:67], a[104:107], v[36:39]\n"
"s_waitcnt lgkmcnt(8)\n"
"v_mfma_f32_16x16x32_bf16 v[40:43], a[64:67], a[112:115], v[40:43]\n"
"v_mfma_f32_16x16x32_bf16 v[40:43], a[68:71], a[116:119], v[40:43]\n"
"global_load_dwordx4 a[52:55], v0, s[34:35] offset:1024 sc1\n"
"v_mfma_f32_16x16x32_bf16 v[68:71], a[76:79], a[116:119], v[68:71]\n"
"v_mfma_f32_16x16x32_bf16 v[68:71], a[72:75], a[112:115], v[68:71]\n"
"v_mfma_f32_16x16x32_bf16 v[72:75], a[72:75], a[120:123], v[72:75]\n"
"v_mfma_f32_16x16x32_bf16 v[72:75], a[76:79], a[124:127], v[72:75]\n"
"v_mfma_f32_16x16x32_bf16 v[44:47], a[68:71], a[124:127], v[44:47]\n"
"v_mfma_f32_16x16x32_bf16 v[44:47], a[64:67], a[120:123], v[44:47]\n"
"s_waitcnt lgkmcnt(4)\n"
"v_mfma_f32_16x16x32_bf16 v[48:51], a[64:67], a[128:131], v[48:51]\n"
"v_mfma_f32_16x16x32_bf16 v[48:51], a[68:71], a[132:135], v[48:51]\n"
"global_load_dwordx4 a[56:59], v0, s[34:35] offset:2048 sc1\n"
"v_mfma_f32_16x16x32_bf16 v[76:79], a[76:79], a[132:135], v[76:79]\n"
"v_mfma_f32_16x16x32_bf16 v[76:79], a[72:75], a[128:131], v[76:79]\n"
"v_mfma_f32_16x16x32_bf16 v[80:83], a[72:75], a[136:139], v[80:83]\n"
"v_lshlrev_b32 v16, 16, v18\n"
"v_mfma_f32_16x16x32_bf16 v[80:83], a[76:79], a[140:143], v[80:83]\n"
"v_and_b32 v17, s63, v18\n"
"v_mfma_f32_16x16x32_bf16 v[52:55], a[68:71], a[140:143], v[52:55]\n"
"v_lshlrev_b32 v18, 16, v19\n"
"v_mfma_f32_16x16x32_bf16 v[52:55], a[64:67], a[136:139], v[52:55]\n"
"s_waitcnt lgkmcnt(0)\n"
"v_and_b32 v19, s63, v19\n"
"v_add_f32 v144, v144, v16\n"
"v_mfma_f32_16x16x32_bf16 v[56:59], a[64:67], a[144:147], v[56:59]\n"
"v_add_f32 v145, v145, v17\n"
"v_mfma_f32_16x16x32_bf16 v[56:59], a[68:71], a[148:151], v[56:59]\n"
"global_load_dwordx4 a[60:63], v0, s[34:35] offset:3072 sc1\n"
"v_add_f32 v146, v146, v18\n"
"v_mfma_f32_16x16x32_bf16 v[84:87], a[76:79], a[148:151], v[84:87]\n"
"v_add_f32 v147, v147, v19\n"
"v_mfma_f32_16x16x32_bf16 v[84:87], a[72:75], a[144:147], v[84:87]\n"
"v_add_f32 v148, v148, v16\n"
"v_mfma_f32_16x16x32_bf16 a[4:7], a[72:75], a[152:155], a[4:7]\n"
"v_add_f32 v149, v149, v17\n"
"v_mfma_f32_16x16x32_bf16 a[4:7], a[76:79], a[156:159], a[4:7]\n"
"v_add_f32 v150, v150, v18\n"
"v_mfma_f32_16x16x32_bf16 a[0:3], a[68:71], a[156:159], a[0:3]\n"
"v_add_f32 v151, v151, v19\n"
"v_mfma_f32_16x16x32_bf16 a[0:3], a[64:67], a[152:155], a[0:3]\n"
"s_waitcnt vmcnt(8)\n"
"v_add_f32 v152, v152, v16\n"
"v_add_f32 v153, v153, v17\n"
"v_mfma_f32_16x16x32_bf16 v[88:91], a[80:83], a[96:99], v[88:91]\n"
"s_add_u32 s34, s36, 1523712\n"
"v_add_f32 v154, v154, v18\n"
"v_mfma_f32_16x16x32_bf16 v[88:91], a[84:87], a[100:103], v[88:91]\n"
"global_load_dwordx4 a[64:67], v0, s[34:35] offset:0 sc1\n"
"v_add_f32 v155, v155, v19\n"
"v_mfma_f32_16x16x32_bf16 v[116:119], a[92:95], a[100:103], v[116:119]\n"
"ds_read_b128 a[100:103], v3 offset:17408\n"
"v_add_f32 v156, v156, v16\n"
"v_mfma_f32_16x16x32_bf16 v[116:119], a[88:91], a[96:99], v[116:119]\n"
"ds_read_b128 a[96:99], v3 offset:16384\n"
"v_add_f32 v157, v157, v17\n"
"v_mfma_f32_16x16x32_bf16 v[120:123], a[88:91], a[104:107], v[120:123]\n"
"v_add_f32 v158, v158, v18\n"
"v_mfma_f32_16x16x32_bf16 v[120:123], a[92:95], a[108:111], v[120:123]\n"
"v_add_f32 v159, v159, v19\n"
"v_add_f32 v160, v160, v16\n"
"v_mfma_f32_16x16x32_bf16 v[92:95], a[84:87], a[108:111], v[92:95]\n"
"ds_read_b128 a[108:111], v3 offset:19456\n"
"v_add_f32 v161, v161, v17\n"
"v_mfma_f32_16x16x32_bf16 v[92:95], a[80:83], a[104:107], v[92:95]\n"
"ds_read_b128 a[104:107], v3 offset:18432\n"
"v_add_f32 v162, v162, v18\n"
"v_mfma_f32_16x16x32_bf16 v[96:99], a[80:83], a[112:115], v[96:99]\n"
"v_add_f32 v163, v163, v19\n"
"v_mfma_f32_16x16x32_bf16 v[96:99], a[84:87], a[116:119], v[96:99]\n"
"global_load_dwordx4 a[68:71], v0, s[34:35] offset:1024 sc1\n"
"v_add_f32 v164, v164, v16\n"
"v_mfma_f32_16x16x32_bf16 v[124:127], a[92:95], a[116:119], v[124:127]\n"
"ds_read_b128 a[116:119], v3 offset:21504\n"
"v_add_f32 v165, v165, v17\n"
"v_mfma_f32_16x16x32_bf16 v[124:127], a[88:91], a[112:115], v[124:127]\n"
"ds_read_b128 a[112:115], v3 offset:20480\n"
"v_add_f32 v166, v166, v18\n"
"v_mfma_f32_16x16x32_bf16 v[128:131], a[88:91], a[120:123], v[128:131]\n"
"v_add_f32 v167, v167, v19\n"
"v_mfma_f32_16x16x32_bf16 v[128:131], a[92:95], a[124:127], v[128:131]\n"
"v_add_f32 v168, v168, v16\n"
"v_add_f32 v169, v169, v17\n"
"v_mfma_f32_16x16x32_bf16 v[100:103], a[84:87], a[124:127], v[100:103]\n"
"ds_read_b128 a[124:127], v3 offset:23552\n"
"v_add_f32 v170, v170, v18\n"
"v_mfma_f32_16x16x32_bf16 v[100:103], a[80:83], a[120:123], v[100:103]\n"
"ds_read_b128 a[120:123], v3 offset:22528\n"
"v_add_f32 v171, v171, v19\n"
"v_mfma_f32_16x16x32_bf16 v[104:107], a[80:83], a[128:131], v[104:107]\n"
"v_accvgpr_read_b32 v12, a16\n"
"v_mfma_f32_16x16x32_bf16 v[104:107], a[84:87], a[132:135], v[104:107]\n"
"global_load_dwordx4 a[72:75], v0, s[34:35] offset:2048 sc1\n"
"v_accvgpr_read_b32 v13, a17\n"
"v_mfma_f32_16x16x32_bf16 v[132:135], a[92:95], a[132:135], v[132:135]\n"
"ds_read_b128 a[132:135], v3 offset:25600\n"
"v_accvgpr_read_b32 v14, a18\n"
"v_mfma_f32_16x16x32_bf16 v[132:135], a[88:91], a[128:131], v[132:135]\n"
"ds_read_b128 a[128:131], v3 offset:24576\n"
"v_accvgpr_read_b32 v15, a19\n"
"v_mfma_f32_16x16x32_bf16 v[136:139], a[88:91], a[136:139], v[136:139]\n"
"v_add_f32 v12, v12, v16\n"
"v_mfma_f32_16x16x32_bf16 v[136:139], a[92:95], a[140:143], v[136:139]\n"
"v_add_f32 v13, v13, v17\n"
"v_add_f32 v14, v14, v18\n"
"v_mfma_f32_16x16x32_bf16 v[108:111], a[84:87], a[140:143], v[108:111]\n"
"ds_read_b128 a[140:143], v3 offset:27648\n"
"v_add_f32 v15, v15, v19\n"
"v_mfma_f32_16x16x32_bf16 v[108:111], a[80:83], a[136:139], v[108:111]\n"
"ds_read_b128 a[136:139], v3 offset:26624\n"
"v_accvgpr_write_b32 a16, v12\n"
"v_mfma_f32_16x16x32_bf16 v[112:115], a[80:83], a[144:147], v[112:115]\n"
"v_accvgpr_write_b32 a17, v13\n"
"v_mfma_f32_16x16x32_bf16 v[112:115], a[84:87], a[148:151], v[112:115]\n"
"global_load_dwordx4 a[76:79], v0, s[34:35] offset:3072 sc1\n"
"v_accvgpr_write_b32 a18, v14\n"
"v_mfma_f32_16x16x32_bf16 v[140:143], a[92:95], a[148:151], v[140:143]\n"
"ds_read_b128 a[148:151], v3 offset:29696\n"
"v_accvgpr_write_b32 a19, v15\n"
"v_mfma_f32_16x16x32_bf16 v[140:143], a[88:91], a[144:147], v[140:143]\n"
"ds_read_b128 a[144:147], v3 offset:28672\n"
"v_lshlrev_b32 v20, 16, v22\n"
"v_mfma_f32_16x16x32_bf16 a[12:15], a[88:91], a[152:155], a[12:15]\n"
"v_and_b32 v21, s63, v22\n"
"v_lshlrev_b32 v22, 16, v23\n"
"v_mfma_f32_16x16x32_bf16 a[12:15], a[92:95], a[156:159], a[12:15]\n"
"v_and_b32 v23, s63, v23\n"
"v_mfma_f32_16x16x32_bf16 a[8:11], a[84:87], a[156:159], a[8:11]\n"
"ds_read_b128 a[156:159], v3 offset:31744\n"
"v_add_f32 v172, v172, v20\n"
"v_mfma_f32_16x16x32_bf16 a[8:11], a[80:83], a[152:155], a[8:11]\n"
"s_waitcnt vmcnt(8) lgkmcnt(11)\n"
"ds_read_b128 a[152:155], v3 offset:30720\n"
"v_add_f32 v173, v173, v21\n"
"v_mfma_f32_16x16x32_bf16 v[32:35], a[32:35], a[96:99], v[32:35]\n"
"s_add_u32 s34, s36, 1527808\n"
"v_add_f32 v174, v174, v22\n"
"v_mfma_f32_16x16x32_bf16 v[32:35], a[36:39], a[100:103], v[32:35]\n"
"global_load_dwordx4 a[80:83], v0, s[34:35] offset:0 sc1\n"
"v_add_f32 v175, v175, v23\n"
"v_mfma_f32_16x16x32_bf16 v[60:63], a[44:47], a[100:103], v[60:63]\n"
"v_add_f32 v176, v176, v20\n"
"v_mfma_f32_16x16x32_bf16 v[60:63], a[40:43], a[96:99], v[60:63]\n"
"v_add_f32 v177, v177, v21\n"
"v_add_f32 v178, v178, v22\n"
"v_mfma_f32_16x16x32_bf16 v[64:67], a[40:43], a[104:107], v[64:67]\n"
"v_add_f32 v179, v179, v23\n"
"v_mfma_f32_16x16x32_bf16 v[64:67], a[44:47], a[108:111], v[64:67]\n"
"v_add_f32 v180, v180, v20\n"
"v_mfma_f32_16x16x32_bf16 v[36:39], a[36:39], a[108:111], v[36:39]\n"
"v_add_f32 v181, v181, v21\n"
"v_mfma_f32_16x16x32_bf16 v[36:39], a[32:35], a[104:107], v[36:39]\n"
"s_waitcnt lgkmcnt(8)\n"
"v_add_f32 v182, v182, v22\n"
"v_mfma_f32_16x16x32_bf16 v[40:43], a[32:35], a[112:115], v[40:43]\n"
"v_add_f32 v183, v183, v23\n"
"v_mfma_f32_16x16x32_bf16 v[40:43], a[36:39], a[116:119], v[40:43]\n"
"global_load_dwordx4 a[84:87], v0, s[34:35] offset:1024 sc1\n"
"v_add_f32 v184, v184, v20\n"
"v_mfma_f32_16x16x32_bf16 v[68:71], a[44:47], a[116:119], v[68:71]\n"
"v_add_f32 v185, v185, v21\n"
"v_mfma_f32_16x16x32_bf16 v[68:71], a[40:43], a[112:115], v[68:71]\n"
"v_add_f32 v186, v186, v22\n"
"v_add_f32 v187, v187, v23\n"
"v_mfma_f32_16x16x32_bf16 v[72:75], a[40:43], a[120:123], v[72:75]\n"
"v_add_f32 v188, v188, v20\n"
"v_mfma_f32_16x16x32_bf16 v[72:75], a[44:47], a[124:127], v[72:75]\n"
"v_add_f32 v189, v189, v21\n"
"v_mfma_f32_16x16x32_bf16 v[44:47], a[36:39], a[124:127], v[44:47]\n"
"v_add_f32 v190, v190, v22\n"
"v_mfma_f32_16x16x32_bf16 v[44:47], a[32:35], a[120:123], v[44:47]\n"
"s_waitcnt lgkmcnt(4)\n"
"v_add_f32 v191, v191, v23\n"
"v_mfma_f32_16x16x32_bf16 v[48:51], a[32:35], a[128:131], v[48:51]\n"
"v_add_f32 v192, v192, v20\n"
"v_mfma_f32_16x16x32_bf16 v[48:51], a[36:39], a[132:135], v[48:51]\n"
"global_load_dwordx4 a[88:91], v0, s[34:35] offset:2048 sc1\n"
"v_add_f32 v193, v193, v21\n"
"v_mfma_f32_16x16x32_bf16 v[76:79], a[44:47], a[132:135], v[76:79]\n"
"v_add_f32 v194, v194, v22\n"
"v_add_f32 v195, v195, v23\n"
"v_mfma_f32_16x16x32_bf16 v[76:79], a[40:43], a[128:131], v[76:79]\n"
"v_add_f32 v196, v196, v20\n"
"v_mfma_f32_16x16x32_bf16 v[80:83], a[40:43], a[136:139], v[80:83]\n"
"v_add_f32 v197, v197, v21\n"
"v_mfma_f32_16x16x32_bf16 v[80:83], a[44:47], a[140:143], v[80:83]\n"
"v_add_f32 v198, v198, v22\n"
"v_mfma_f32_16x16x32_bf16 v[52:55], a[36:39], a[140:143], v[52:55]\n"
"v_add_f32 v199, v199, v23\n"
"v_mfma_f32_16x16x32_bf16 v[52:55], a[32:35], a[136:139], v[52:55]\n"
"s_waitcnt lgkmcnt(0)\n"
"v_accvgpr_read_b32 v12, a20\n"
"v_mfma_f32_16x16x32_bf16 v[56:59], a[32:35], a[144:147], v[56:59]\n"
"v_accvgpr_read_b32 v13, a21\n"
"v_mfma_f32_16x16x32_bf16 v[56:59], a[36:39], a[148:151], v[56:59]\n"
"global_load_dwordx4 a[92:95], v0, s[34:35] offset:3072 sc1\n"
"v_accvgpr_read_b32 v14, a22\n"
"v_mfma_f32_16x16x32_bf16 v[84:87], a[44:47], a[148:151], v[84:87]\n"
"v_accvgpr_read_b32 v15, a23\n"
"v_add_f32 v12, v12, v20\n"
"v_mfma_f32_16x16x32_bf16 v[84:87], a[40:43], a[144:147], v[84:87]\n"
"v_add_f32 v13, v13, v21\n"
"v_mfma_f32_16x16x32_bf16 a[4:7], a[40:43], a[152:155], a[4:7]\n"
"v_add_f32 v14, v14, v22\n"
"v_mfma_f32_16x16x32_bf16 a[4:7], a[44:47], a[156:159], a[4:7]\n"
"v_add_f32 v15, v15, v23\n"
"v_mfma_f32_16x16x32_bf16 a[0:3], a[36:39], a[156:159], a[0:3]\n"
"v_accvgpr_write_b32 a20, v12\n"
"v_mfma_f32_16x16x32_bf16 a[0:3], a[32:35], a[152:155], a[0:3]\n"
"s_waitcnt vmcnt(8)\n"
"v_accvgpr_write_b32 a21, v13\n"
"v_mfma_f32_16x16x32_bf16 v[88:91], a[48:51], a[96:99], v[88:91]\n"
"s_add_u32 s34, s36, 1531904\n"
"v_accvgpr_write_b32 a22, v14\n"
"v_accvgpr_write_b32 a23, v15\n"
"v_mfma_f32_16x16x32_bf16 v[88:91], a[52:55], a[100:103], v[88:91]\n"
"global_load_dwordx4 a[32:35], v0, s[34:35] offset:0 sc1\n"
"v_lshlrev_b32 v24, 16, v26\n"
"v_mfma_f32_16x16x32_bf16 v[116:119], a[60:63], a[100:103], v[116:119]\n"
"ds_read_b128 a[100:103], v4 offset:17408\n"
"v_and_b32 v25, s63, v26\n"
"v_mfma_f32_16x16x32_bf16 v[116:119], a[56:59], a[96:99], v[116:119]\n"
"ds_read_b128 a[96:99], v4 offset:16384\n"
"v_lshlrev_b32 v26, 16, v27\n"
"v_mfma_f32_16x16x32_bf16 v[120:123], a[56:59], a[104:107], v[120:123]\n"
"v_and_b32 v27, s63, v27\n"
"v_mfma_f32_16x16x32_bf16 v[120:123], a[60:63], a[108:111], v[120:123]\n"
"v_add_f32 v200, v200, v24\n"
"v_mfma_f32_16x16x32_bf16 v[92:95], a[52:55], a[108:111], v[92:95]\n"
"ds_read_b128 a[108:111], v4 offset:19456\n"
"v_add_f32 v201, v201, v25\n"
"v_mfma_f32_16x16x32_bf16 v[92:95], a[48:51], a[104:107], v[92:95]\n"
"ds_read_b128 a[104:107], v4 offset:18432\n"
"v_add_f32 v202, v202, v26\n"
"v_mfma_f32_16x16x32_bf16 v[96:99], a[48:51], a[112:115], v[96:99]\n"
"v_add_f32 v203, v203, v27\n"
"v_add_f32 v204, v204, v24\n"
"v_mfma_f32_16x16x32_bf16 v[96:99], a[52:55], a[116:119], v[96:99]\n"
"global_load_dwordx4 a[36:39], v0, s[34:35] offset:1024 sc1\n"
"v_add_f32 v205, v205, v25\n"
"v_mfma_f32_16x16x32_bf16 v[124:127], a[60:63], a[116:119], v[124:127]\n"
"ds_read_b128 a[116:119], v4 offset:21504\n"
"v_add_f32 v206, v206, v26\n"
"v_mfma_f32_16x16x32_bf16 v[124:127], a[56:59], a[112:115], v[124:127]\n"
"ds_read_b128 a[112:115], v4 offset:20480\n"
"v_add_f32 v207, v207, v27\n"
"v_mfma_f32_16x16x32_bf16 v[128:131], a[56:59], a[120:123], v[128:131]\n"
"v_add_f32 v208, v208, v24\n"
"v_mfma_f32_16x16x32_bf16 v[128:131], a[60:63], a[124:127], v[128:131]\n"
"v_add_f32 v209, v209, v25\n"
"v_mfma_f32_16x16x32_bf16 v[100:103], a[52:55], a[124:127], v[100:103]\n"
"ds_read_b128 a[124:127], v4 offset:23552\n"
"v_add_f32 v210, v210, v26\n"
"v_mfma_f32_16x16x32_bf16 v[100:103], a[48:51], a[120:123], v[100:103]\n"
"ds_read_b128 a[120:123], v4 offset:22528\n"
"v_add_f32 v211, v211, v27\n"
"v_mfma_f32_16x16x32_bf16 v[104:107], a[48:51], a[128:131], v[104:107]\n"
"v_add_f32 v212, v212, v24\n"
"v_add_f32 v213, v213, v25\n"
"v_mfma_f32_16x16x32_bf16 v[104:107], a[52:55], a[132:135], v[104:107]\n"
"global_load_dwordx4 a[40:43], v0, s[34:35] offset:2048 sc1\n"
"v_add_f32 v214, v214, v26\n"
"v_mfma_f32_16x16x32_bf16 v[132:135], a[60:63], a[132:135], v[132:135]\n"
"ds_read_b128 a[132:135], v4 offset:25600\n"
"v_add_f32 v215, v215, v27\n"
"v_mfma_f32_16x16x32_bf16 v[132:135], a[56:59], a[128:131], v[132:135]\n"
"ds_read_b128 a[128:131], v4 offset:24576\n"
"v_add_f32 v216, v216, v24\n"
"v_mfma_f32_16x16x32_bf16 v[136:139], a[56:59], a[136:139], v[136:139]\n"
"v_add_f32 v217, v217, v25\n"
"v_mfma_f32_16x16x32_bf16 v[136:139], a[60:63], a[140:143], v[136:139]\n"
"v_add_f32 v218, v218, v26\n"
"v_mfma_f32_16x16x32_bf16 v[108:111], a[52:55], a[140:143], v[108:111]\n"
"ds_read_b128 a[140:143], v4 offset:27648\n"
"v_add_f32 v219, v219, v27\n"
"v_mfma_f32_16x16x32_bf16 v[108:111], a[48:51], a[136:139], v[108:111]\n"
"ds_read_b128 a[136:139], v4 offset:26624\n"
"v_add_f32 v220, v220, v24\n"
"v_mfma_f32_16x16x32_bf16 v[112:115], a[48:51], a[144:147], v[112:115]\n"
"v_add_f32 v221, v221, v25\n"
"v_add_f32 v222, v222, v26\n"
"v_mfma_f32_16x16x32_bf16 v[112:115], a[52:55], a[148:151], v[112:115]\n"
"global_load_dwordx4 a[44:47], v0, s[34:35] offset:3072 sc1\n"
"v_add_f32 v223, v223, v27\n"
"v_mfma_f32_16x16x32_bf16 v[140:143], a[60:63], a[148:151], v[140:143]\n"
"ds_read_b128 a[148:151], v4 offset:29696\n"
"v_add_f32 v224, v224, v24\n"
"v_mfma_f32_16x16x32_bf16 v[140:143], a[56:59], a[144:147], v[140:143]\n"
"ds_read_b128 a[144:147], v4 offset:28672\n"
"v_add_f32 v225, v225, v25\n"
"v_mfma_f32_16x16x32_bf16 a[12:15], a[56:59], a[152:155], a[12:15]\n"
"v_add_f32 v226, v226, v26\n"
"v_mfma_f32_16x16x32_bf16 a[12:15], a[60:63], a[156:159], a[12:15]\n"
"v_add_f32 v227, v227, v27\n"
"v_mfma_f32_16x16x32_bf16 a[8:11], a[52:55], a[156:159], a[8:11]\n"
"ds_read_b128 a[156:159], v4 offset:31744\n"
"v_accvgpr_read_b32 v12, a24\n"
"v_mfma_f32_16x16x32_bf16 a[8:11], a[48:51], a[152:155], a[8:11]\n"
"s_waitcnt vmcnt(8) lgkmcnt(11)\n"
"ds_read_b128 a[152:155], v4 offset:30720\n"
"v_accvgpr_read_b32 v13, a25\n"
"v_mfma_f32_16x16x32_bf16 v[32:35], a[64:67], a[96:99], v[32:35]\n"
"s_add_u32 s34, s36, 1536000\n"
"v_accvgpr_read_b32 v14, a26\n"
"v_accvgpr_read_b32 v15, a27\n"
"v_mfma_f32_16x16x32_bf16 v[32:35], a[68:71], a[100:103], v[32:35]\n"
"global_load_dwordx4 a[48:51], v0, s[34:35] offset:0 sc1\n"
"v_add_f32 v12, v12, v24\n"
"v_mfma_f32_16x16x32_bf16 v[60:63], a[76:79], a[100:103], v[60:63]\n"
"v_add_f32 v13, v13, v25\n"
"v_mfma_f32_16x16x32_bf16 v[60:63], a[72:75], a[96:99], v[60:63]\n"
"v_add_f32 v14, v14, v26\n"
"v_mfma_f32_16x16x32_bf16 v[64:67], a[72:75], a[104:107], v[64:67]\n"
"v_add_f32 v15, v15, v27\n"
"v_mfma_f32_16x16x32_bf16 v[64:67], a[76:79], a[108:111], v[64:67]\n"
"v_accvgpr_write_b32 a24, v12\n"
"v_mfma_f32_16x16x32_bf16 v[36:39], a[68:71], a[108:111], v[36:39]\n"
"v_accvgpr_write_b32 a25, v13\n"
"v_accvgpr_write_b32 a26, v14\n"
"v_mfma_f32_16x16x32_bf16 v[36:39], a[64:67], a[104:107], v[36:39]\n"
"s_waitcnt lgkmcnt(8)\n"
"v_accvgpr_write_b32 a27, v15\n"
"v_mfma_f32_16x16x32_bf16 v[40:43], a[64:67], a[112:115], v[40:43]\n"
"v_lshlrev_b32 v28, 16, v30\n"
"v_mfma_f32_16x16x32_bf16 v[40:43], a[68:71], a[116:119], v[40:43]\n"
"global_load_dwordx4 a[52:55], v0, s[34:35] offset:1024 sc1\n"
"v_and_b32 v29, s63, v30\n"
"v_mfma_f32_16x16x32_bf16 v[68:71], a[76:79], a[116:119], v[68:71]\n"
"v_lshlrev_b32 v30, 16, v31\n"
"v_mfma_f32_16x16x32_bf16 v[68:71], a[72:75], a[112:115], v[68:71]\n"
"v_and_b32 v31, s63, v31\n"
"v_mfma_f32_16x16x32_bf16 v[72:75], a[72:75], a[120:123], v[72:75]\n"
"v_add_f32 v228, v228, v28\n"
"v_mfma_f32_16x16x32_bf16 v[72:75], a[76:79], a[124:127], v[72:75]\n"
"v_add_f32 v229, v229, v29\n"
"v_add_f32 v230, v230, v30\n"
"v_mfma_f32_16x16x32_bf16 v[44:47], a[68:71], a[124:127], v[44:47]\n"
"v_add_f32 v231, v231, v31\n"
"v_mfma_f32_16x16x32_bf16 v[44:47], a[64:67], a[120:123], v[44:47]\n"
"s_waitcnt lgkmcnt(4)\n"
"v_add_f32 v232, v232, v28\n"
"v_mfma_f32_16x16x32_bf16 v[48:51], a[64:67], a[128:131], v[48:51]\n"
"v_add_f32 v233, v233, v29\n"
"v_mfma_f32_16x16x32_bf16 v[48:51], a[68:71], a[132:135], v[48:51]\n"
"global_load_dwordx4 a[56:59], v0, s[34:35] offset:2048 sc1\n"
"v_add_f32 v234, v234, v30\n"
"v_mfma_f32_16x16x32_bf16 v[76:79], a[76:79], a[132:135], v[76:79]\n"
"v_add_f32 v235, v235, v31\n"
"v_mfma_f32_16x16x32_bf16 v[76:79], a[72:75], a[128:131], v[76:79]\n"
"v_add_f32 v236, v236, v28\n"
"v_mfma_f32_16x16x32_bf16 v[80:83], a[72:75], a[136:139], v[80:83]\n"
"v_add_f32 v237, v237, v29\n"
"v_mfma_f32_16x16x32_bf16 v[80:83], a[76:79], a[140:143], v[80:83]\n"
"v_add_f32 v238, v238, v30\n"
"v_add_f32 v239, v239, v31\n"
"v_mfma_f32_16x16x32_bf16 v[52:55], a[68:71], a[140:143], v[52:55]\n"
"v_add_f32 v240, v240, v28\n"
"v_mfma_f32_16x16x32_bf16 v[52:55], a[64:67], a[136:139], v[52:55]\n"
"s_waitcnt lgkmcnt(0)\n"
"v_add_f32 v241, v241, v29\n"
"v_mfma_f32_16x16x32_bf16 v[56:59], a[64:67], a[144:147], v[56:59]\n"
"v_add_f32 v242, v242, v30\n"
"v_mfma_f32_16x16x32_bf16 v[56:59], a[68:71], a[148:151], v[56:59]\n"
"global_load_dwordx4 a[60:63], v0, s[34:35] offset:3072 sc1\n"
"v_add_f32 v243, v243, v31\n"
"v_mfma_f32_16x16x32_bf16 v[84:87], a[76:79], a[148:151], v[84:87]\n"
"v_add_f32 v244, v244, v28\n"
"v_mfma_f32_16x16x32_bf16 v[84:87], a[72:75], a[144:147], v[84:87]\n"
"v_add_f32 v245, v245, v29\n"
"v_mfma_f32_16x16x32_bf16 a[4:7], a[72:75], a[152:155], a[4:7]\n"
"v_add_f32 v246, v246, v30\n"
"v_add_f32 v247, v247, v31\n"
"v_mfma_f32_16x16x32_bf16 a[4:7], a[76:79], a[156:159], a[4:7]\n"
"v_add_f32 v248, v248, v28\n"
"v_mfma_f32_16x16x32_bf16 a[0:3], a[68:71], a[156:159], a[0:3]\n"
"v_add_f32 v249, v249, v29\n"
"v_mfma_f32_16x16x32_bf16 a[0:3], a[64:67], a[152:155], a[0:3]\n"
"s_waitcnt vmcnt(8)\n"
"v_add_f32 v250, v250, v30\n"
"v_mfma_f32_16x16x32_bf16 v[88:91], a[80:83], a[96:99], v[88:91]\n"
"s_add_u32 s34, s36, 1540096\n"
"v_add_f32 v251, v251, v31\n"
"v_mfma_f32_16x16x32_bf16 v[88:91], a[84:87], a[100:103], v[88:91]\n"
"global_load_dwordx4 a[64:67], v0, s[34:35] offset:0 sc1\n"
"v_add_f32 v252, v252, v28\n"
"v_mfma_f32_16x16x32_bf16 v[116:119], a[92:95], a[100:103], v[116:119]\n"
"ds_read_b128 a[100:103], v5 offset:17408\n"
"v_add_f32 v253, v253, v29\n"
"v_mfma_f32_16x16x32_bf16 v[116:119], a[88:91], a[96:99], v[116:119]\n"
"ds_read_b128 a[96:99], v5 offset:16384\n"
"v_add_f32 v254, v254, v30\n"
"v_mfma_f32_16x16x32_bf16 v[120:123], a[88:91], a[104:107], v[120:123]\n"
"v_add_f32 v255, v255, v31\n"
"v_accvgpr_read_b32 v12, a28\n"
"v_mfma_f32_16x16x32_bf16 v[120:123], a[92:95], a[108:111], v[120:123]\n"
"v_accvgpr_read_b32 v13, a29\n"
"v_mfma_f32_16x16x32_bf16 v[92:95], a[84:87], a[108:111], v[92:95]\n"
"ds_read_b128 a[108:111], v5 offset:19456\n"
"v_accvgpr_read_b32 v14, a30\n"
"v_mfma_f32_16x16x32_bf16 v[92:95], a[80:83], a[104:107], v[92:95]\n"
"ds_read_b128 a[104:107], v5 offset:18432\n"
"v_accvgpr_read_b32 v15, a31\n"
"v_mfma_f32_16x16x32_bf16 v[96:99], a[80:83], a[112:115], v[96:99]\n"
"v_add_f32 v12, v12, v28\n"
"v_mfma_f32_16x16x32_bf16 v[96:99], a[84:87], a[116:119], v[96:99]\n"
"global_load_dwordx4 a[68:71], v0, s[34:35] offset:1024 sc1\n"
"v_add_f32 v13, v13, v29\n"
"v_mfma_f32_16x16x32_bf16 v[124:127], a[92:95], a[116:119], v[124:127]\n"
"ds_read_b128 a[116:119], v5 offset:21504\n"
"v_add_f32 v14, v14, v30\n"
"v_mfma_f32_16x16x32_bf16 v[124:127], a[88:91], a[112:115], v[124:127]\n"
"ds_read_b128 a[112:115], v5 offset:20480\n"
"v_add_f32 v15, v15, v31\n"
"v_mfma_f32_16x16x32_bf16 v[128:131], a[88:91], a[120:123], v[128:131]\n"
"v_accvgpr_write_b32 a28, v12\n"
"v_accvgpr_write_b32 a29, v13\n"
"v_mfma_f32_16x16x32_bf16 v[128:131], a[92:95], a[124:127], v[128:131]\n"
"v_accvgpr_write_b32 a30, v14\n"
"v_mfma_f32_16x16x32_bf16 v[100:103], a[84:87], a[124:127], v[100:103]\n"
"ds_read_b128 a[124:127], v5 offset:23552\n"
"v_accvgpr_write_b32 a31, v15\n"
"v_mfma_f32_16x16x32_bf16 v[100:103], a[80:83], a[120:123], v[100:103]\n"
"ds_read_b128 a[120:123], v5 offset:22528\n"
"v_mfma_f32_16x16x32_bf16 v[104:107], a[80:83], a[128:131], v[104:107]\n"
"v_mfma_f32_16x16x32_bf16 v[104:107], a[84:87], a[132:135], v[104:107]\n"
"global_load_dwordx4 a[72:75], v0, s[34:35] offset:2048 sc1\n"
"v_mfma_f32_16x16x32_bf16 v[132:135], a[92:95], a[132:135], v[132:135]\n"
"ds_read_b128 a[132:135], v5 offset:25600\n"
"v_mfma_f32_16x16x32_bf16 v[132:135], a[88:91], a[128:131], v[132:135]\n"
"ds_read_b128 a[128:131], v5 offset:24576\n"
"v_mfma_f32_16x16x32_bf16 v[136:139], a[88:91], a[136:139], v[136:139]\n"
"ds_read_b128 v[16:19], v7 offset:32768\n"
"v_mfma_f32_16x16x32_bf16 v[136:139], a[92:95], a[140:143], v[136:139]\n"
"v_mfma_f32_16x16x32_bf16 v[108:111], a[84:87], a[140:143], v[108:111]\n"
"ds_read_b128 a[140:143], v5 offset:27648\n"
"v_mfma_f32_16x16x32_bf16 v[108:111], a[80:83], a[136:139], v[108:111]\n"
"ds_read_b128 a[136:139], v5 offset:26624\n"
"v_mfma_f32_16x16x32_bf16 v[112:115], a[80:83], a[144:147], v[112:115]\n"
"v_mfma_f32_16x16x32_bf16 v[112:115], a[84:87], a[148:151], v[112:115]\n"
"global_load_dwordx4 a[76:79], v0, s[34:35] offset:3072 sc1\n"
"v_mfma_f32_16x16x32_bf16 v[140:143], a[92:95], a[148:151], v[140:143]\n"
"ds_read_b128 a[148:151], v5 offset:29696\n"
"v_mfma_f32_16x16x32_bf16 v[140:143], a[88:91], a[144:147], v[140:143]\n"
"ds_read_b128 a[144:147], v5 offset:28672\n"
"v_mfma_f32_16x16x32_bf16 a[12:15], a[88:91], a[152:155], a[12:15]\n"
"v_mfma_f32_16x16x32_bf16 a[12:15], a[92:95], a[156:159], a[12:15]\n"
"v_mfma_f32_16x16x32_bf16 a[8:11], a[84:87], a[156:159], a[8:11]\n"
"ds_read_b128 a[156:159], v5 offset:31744\n"
"v_mfma_f32_16x16x32_bf16 a[8:11], a[80:83], a[152:155], a[8:11]\n"
"s_waitcnt vmcnt(8) lgkmcnt(12)\n"
"ds_read_b128 a[152:155], v5 offset:30720\n"
"v_mfma_f32_16x16x32_bf16 v[32:35], a[32:35], a[96:99], v[32:35]\n"
"s_add_u32 s34, s36, 1544192\n"
"v_mfma_f32_16x16x32_bf16 v[32:35], a[36:39], a[100:103], v[32:35]\n"
"global_load_dwordx4 a[80:83], v0, s[34:35] offset:0 sc1\n"
"v_mfma_f32_16x16x32_bf16 v[60:63], a[44:47], a[100:103], v[60:63]\n"
"v_mfma_f32_16x16x32_bf16 v[60:63], a[40:43], a[96:99], v[60:63]\n"
"v_mfma_f32_16x16x32_bf16 v[64:67], a[40:43], a[104:107], v[64:67]\n"
"v_mfma_f32_16x16x32_bf16 v[64:67], a[44:47], a[108:111], v[64:67]\n"
"v_mfma_f32_16x16x32_bf16 v[36:39], a[36:39], a[108:111], v[36:39]\n"
"v_mfma_f32_16x16x32_bf16 v[36:39], a[32:35], a[104:107], v[36:39]\n"
"s_waitcnt lgkmcnt(9)\n"
"v_mfma_f32_16x16x32_bf16 v[40:43], a[32:35], a[112:115], v[40:43]\n"
"s_mov_b32 s52, %4\n"
"s_and_b32 s53, %5, 0xffff\n"
"v_mfma_f32_16x16x32_bf16 v[40:43], a[36:39], a[116:119], v[40:43]\n"
"global_load_dwordx4 a[84:87], v0, s[34:35] offset:1024 sc1\n"
"v_mfma_f32_16x16x32_bf16 v[68:71], a[44:47], a[116:119], v[68:71]\n"
"s_mov_b32 s54, 260352\n"
"s_mov_b32 s55, 0x20000\n"
"v_mfma_f32_16x16x32_bf16 v[68:71], a[40:43], a[112:115], v[68:71]\n"
"v_xor_b32 v8, 64, v7\n"
"v_mfma_f32_16x16x32_bf16 v[72:75], a[40:43], a[120:123], v[72:75]\n"
"s_waitcnt lgkmcnt(6)\n"
"v_lshlrev_b32 v12, 16, v16\n"
"v_mfma_f32_16x16x32_bf16 v[72:75], a[44:47], a[124:127], v[72:75]\n"
"v_and_b32 v13, s63, v16\n"
"v_lshlrev_b32 v14, 16, v17\n"
"v_mfma_f32_16x16x32_bf16 v[44:47], a[36:39], a[124:127], v[44:47]\n"
"v_and_b32 v15, s63, v17\n"
"v_mfma_f32_16x16x32_bf16 v[44:47], a[32:35], a[120:123], v[44:47]\n"
"s_waitcnt lgkmcnt(4)\n"
"v_xor_b32 v20, 64, v7\n"
"v_mfma_f32_16x16x32_bf16 v[48:51], a[32:35], a[128:131], v[48:51]\n"
"ds_read_b128 v[20:23], v20 offset:32768\n"
"v_mul_f32 v12, v12, v32\n"
"v_mfma_f32_16x16x32_bf16 v[48:51], a[36:39], a[132:135], v[48:51]\n"
"global_load_dwordx4 a[88:91], v0, s[34:35] offset:2048 sc1\n"
"v_mul_f32 v13, v13, v33\n"
"v_mfma_f32_16x16x32_bf16 v[76:79], a[44:47], a[132:135], v[76:79]\n"
"v_mul_f32 v14, v14, v34\n"
"v_mfma_f32_16x16x32_bf16 v[76:79], a[40:43], a[128:131], v[76:79]\n"
"v_mul_f32 v15, v15, v35\n"
"v_mfma_f32_16x16x32_bf16 v[80:83], a[40:43], a[136:139], v[80:83]\n"
"v_cvt_pk_bf16_f32 v24, v12, v13\n"
"v_mfma_f32_16x16x32_bf16 v[80:83], a[44:47], a[140:143], v[80:83]\n"
"v_cvt_pk_bf16_f32 v25, v14, v15\n"
"v_mfma_f32_16x16x32_bf16 v[52:55], a[36:39], a[140:143], v[52:55]\n"
"ds_write_b64 v7, v[24:25] offset:32768\n"
"v_mfma_f32_16x16x32_bf16 v[52:55], a[32:35], a[136:139], v[52:55]\n"
"s_waitcnt lgkmcnt(2)\n"
"v_lshlrev_b32 v12, 16, v18\n"
"v_mfma_f32_16x16x32_bf16 v[56:59], a[32:35], a[144:147], v[56:59]\n"
"v_and_b32 v13, s63, v18\n"
"v_mfma_f32_16x16x32_bf16 v[56:59], a[36:39], a[148:151], v[56:59]\n"
"global_load_dwordx4 a[92:95], v0, s[34:35] offset:3072 sc1\n"
"v_mfma_f32_16x16x32_bf16 v[84:87], a[44:47], a[148:151], v[84:87]\n"
"v_lshlrev_b32 v14, 16, v19\n"
"v_mfma_f32_16x16x32_bf16 v[84:87], a[40:43], a[144:147], v[84:87]\n"
"v_and_b32 v15, s63, v19\n"
"v_mfma_f32_16x16x32_bf16 a[4:7], a[40:43], a[152:155], a[4:7]\n"
"v_mul_f32 v12, v12, v60\n"
"v_mfma_f32_16x16x32_bf16 a[4:7], a[44:47], a[156:159], a[4:7]\n"
"v_mul_f32 v13, v13, v61\n"
"v_mfma_f32_16x16x32_bf16 a[0:3], a[36:39], a[156:159], a[0:3]\n"
"v_mul_f32 v14, v14, v62\n"
"v_mfma_f32_16x16x32_bf16 a[0:3], a[32:35], a[152:155], a[0:3]\n"
"s_waitcnt vmcnt(8)\n"
"v_mul_f32 v15, v15, v63\n"
"v_mfma_f32_16x16x32_bf16 v[88:91], a[48:51], a[96:99], v[88:91]\n"
"s_add_u32 s34, s36, 1466368\n"
"v_cvt_pk_bf16_f32 v26, v12, v13\n"
"v_mfma_f32_16x16x32_bf16 v[88:91], a[52:55], a[100:103], v[88:91]\n"
"global_load_dwordx4 a[32:35], v0, s[34:35] offset:0 sc1\n"
"v_mfma_f32_16x16x32_bf16 v[116:119], a[60:63], a[100:103], v[116:119]\n"
"v_cvt_pk_bf16_f32 v27, v14, v15\n"
"v_mfma_f32_16x16x32_bf16 v[116:119], a[56:59], a[96:99], v[116:119]\n"
"ds_write_b64 v7, v[26:27] offset:32776\n"
"v_mfma_f32_16x16x32_bf16 v[120:123], a[56:59], a[104:107], v[120:123]\n"
"s_waitcnt lgkmcnt(2)\n"
"v_lshlrev_b32 v12, 16, v20\n"
"v_mfma_f32_16x16x32_bf16 v[120:123], a[60:63], a[108:111], v[120:123]\n"
"v_and_b32 v13, s63, v20\n"
"v_mfma_f32_16x16x32_bf16 v[92:95], a[52:55], a[108:111], v[92:95]\n"
"v_lshlrev_b32 v14, 16, v21\n"
"v_mfma_f32_16x16x32_bf16 v[92:95], a[48:51], a[104:107], v[92:95]\n"
"v_and_b32 v15, s63, v21\n"
"v_mfma_f32_16x16x32_bf16 v[96:99], a[48:51], a[112:115], v[96:99]\n"
"ds_read_b128 v[16:19], v7 offset:34816\n"
"v_mfma_f32_16x16x32_bf16 v[96:99], a[52:55], a[116:119], v[96:99]\n"
"L_st10265:\n"
"s_cbranch_execz L_retC\n"
"global_load_dwordx4 a[36:39], v0, s[34:35] offset:1024 sc1\n"
"v_mul_f32 v12, v12, v88\n"
"v_mul_f32 v13, v13, v89\n"
"v_mfma_f32_16x16x32_bf16 v[124:127], a[60:63], a[116:119], v[124:127]\n"
"v_mul_f32 v14, v14, v90\n"
"v_mul_f32 v15, v15, v91\n"
"v_mfma_f32_16x16x32_bf16 v[124:127], a[56:59], a[112:115], v[124:127]\n"
"v_cvt_pk_bf16_f32 v28, v12, v13\n"
"v_cvt_pk_bf16_f32 v29, v14, v15\n"
"v_mfma_f32_16x16x32_bf16 v[128:131], a[56:59], a[120:123], v[128:131]\n"
"ds_write_b64 v8, v[28:29] offset:32768\n"
"v_mfma_f32_16x16x32_bf16 v[128:131], a[60:63], a[124:127], v[128:131]\n"
"v_lshlrev_b32 v12, 16, v22\n"
"v_and_b32 v13, s63, v22\n"
"v_mfma_f32_16x16x32_bf16 v[100:103], a[52:55], a[124:127], v[100:103]\n"
"v_lshlrev_b32 v14, 16, v23\n"
"v_and_b32 v15, s63, v23\n"
"v_mfma_f32_16x16x32_bf16 v[100:103], a[48:51], a[120:123], v[100:103]\n"
"v_mul_f32 v12, v12, v116\n"
"v_mul_f32 v13, v13, v117\n"
"v_mfma_f32_16x16x32_bf16 v[104:107], a[48:51], a[128:131], v[104:107]\n"
"v_mul_f32 v14, v14, v118\n"
"v_mul_f32 v15, v15, v119\n"
"v_mfma_f32_16x16x32_bf16 v[104:107], a[52:55], a[132:135], v[104:107]\n"
"global_load_dwordx4 a[40:43], v0, s[34:35] offset:2048 sc1\n"
"v_cvt_pk_bf16_f32 v30, v12, v13\n"
"v_mfma_f32_16x16x32_bf16 v[132:135], a[60:63], a[132:135], v[132:135]\n"
"v_cvt_pk_bf16_f32 v31, v14, v15\n"
"v_mfma_f32_16x16x32_bf16 v[132:135], a[56:59], a[128:131], v[132:135]\n"
"ds_write_b64 v8, v[30:31] offset:32776\n"
"v_mfma_f32_16x16x32_bf16 v[136:139], a[56:59], a[136:139], v[136:139]\n"
"ds_read_b128 v[32:35], v2 offset:32768\n"
"v_mfma_f32_16x16x32_bf16 v[136:139], a[60:63], a[140:143], v[136:139]\n"
"s_waitcnt lgkmcnt(3)\n"
"ds_read_b128 v[60:63], v2 offset:33792\n"
"v_lshlrev_b32 v12, 16, v16\n"
"v_mfma_f32_16x16x32_bf16 v[108:111], a[52:55], a[140:143], v[108:111]\n"
"s_add_u32 s96, s36, 1568768\n"
"v_and_b32 v13, s63, v16\n"
"v_lshlrev_b32 v14, 16, v17\n"
"v_mfma_f32_16x16x32_bf16 v[108:111], a[48:51], a[136:139], v[108:111]\n"
"v_and_b32 v15, s63, v17\n"
"v_xor_b32 v20, 64, v7\n"
"v_mfma_f32_16x16x32_bf16 v[112:115], a[48:51], a[144:147], v[112:115]\n"
"global_load_dwordx4 v[88:91], v0, s[96:97] offset:-4096 sc1\n"
"ds_read_b128 v[20:23], v20 offset:34816\n"
"v_mfma_f32_16x16x32_bf16 v[112:115], a[52:55], a[148:151], v[112:115]\n"
"global_load_dwordx4 a[44:47], v0, s[34:35] offset:3072 sc1\n"
"v_mul_f32 v12, v12, v36\n"
"v_mfma_f32_16x16x32_bf16 v[140:143], a[60:63], a[148:151], v[140:143]\n"
"v_mul_f32 v13, v13, v37\n"
"v_mul_f32 v14, v14, v38\n"
"v_mfma_f32_16x16x32_bf16 v[140:143], a[56:59], a[144:147], v[140:143]\n"
"v_mul_f32 v15, v15, v39\n"
"v_mfma_f32_16x16x32_bf16 a[12:15], a[56:59], a[152:155], a[12:15]\n"
"v_cvt_pk_bf16_f32 v24, v12, v13\n"
"v_mfma_f32_16x16x32_bf16 a[12:15], a[60:63], a[156:159], a[12:15]\n"
"v_cvt_pk_bf16_f32 v25, v14, v15\n"
"v_mfma_f32_16x16x32_bf16 a[8:11], a[52:55], a[156:159], a[8:11]\n"
"s_waitcnt lgkmcnt(2)\n"
"buffer_store_dwordx4 v[32:35], v6, s[52:55], s80 offen offset:0 nt\n"
"ds_write_b64 v7, v[24:25] offset:34816\n"
"v_mfma_f32_16x16x32_bf16 a[8:11], a[48:51], a[152:155], a[8:11]\n"
"s_waitcnt vmcnt(10)\n"
"v_lshlrev_b32 v12, 16, v18\n"
"v_and_b32 v13, s63, v18\n"
"v_mfma_f32_16x16x32_bf16 v[144:147], a[64:67], a[96:99], v[144:147]\n"
"s_add_u32 s34, s36, 1470464\n"
"v_lshlrev_b32 v14, 16, v19\n"
"v_and_b32 v15, s63, v19\n"
"v_mfma_f32_16x16x32_bf16 v[144:147], a[68:71], a[100:103], v[144:147]\n"
"global_load_dwordx4 a[48:51], v0, s[34:35] offset:0 sc1\n"
"v_mfma_f32_16x16x32_bf16 v[172:175], a[76:79], a[100:103], v[172:175]\n"
"global_load_dwordx4 v[116:119], v0, s[96:97] offset:-3072 sc1\n"
"v_mfma_f32_16x16x32_bf16 v[172:175], a[72:75], a[96:99], v[172:175]\n"
"v_mul_f32 v12, v12, v64\n"
"v_mfma_f32_16x16x32_bf16 v[176:179], a[72:75], a[104:107], v[176:179]\n"
"v_mul_f32 v13, v13, v65\n"
"v_mfma_f32_16x16x32_bf16 v[176:179], a[76:79], a[108:111], v[176:179]\n"
"v_mul_f32 v14, v14, v66\n"
"v_mfma_f32_16x16x32_bf16 v[148:151], a[68:71], a[108:111], v[148:151]\n"
"v_mul_f32 v15, v15, v67\n"
"v_mfma_f32_16x16x32_bf16 v[148:151], a[64:67], a[104:107], v[148:151]\n"
"v_cvt_pk_bf16_f32 v26, v12, v13\n"
"v_mfma_f32_16x16x32_bf16 v[152:155], a[64:67], a[112:115], v[152:155]\n"
"v_cvt_pk_bf16_f32 v27, v14, v15\n"
"v_mfma_f32_16x16x32_bf16 v[152:155], a[68:71], a[116:119], v[152:155]\n"
"global_load_dwordx4 a[52:55], v0, s[34:35] offset:1024 sc1\n"
"v_mfma_f32_16x16x32_bf16 v[180:183], a[76:79], a[116:119], v[180:183]\n"
"s_waitcnt lgkmcnt(2)\n"
"buffer_store_dwordx4 v[60:63], v6, s[52:55], s81 offen offset:1024 nt\n"
"ds_write_b64 v7, v[26:27] offset:34824\n"
"v_mfma_f32_16x16x32_bf16 v[180:183], a[72:75], a[112:115], v[180:183]\n"
"s_waitcnt lgkmcnt(2)\n"
"v_lshlrev_b32 v12, 16, v20\n"
"v_and_b32 v13, s63, v20\n"
"v_mfma_f32_16x16x32_bf16 v[184:187], a[72:75], a[120:123], v[184:187]\n"
"v_lshlrev_b32 v14, 16, v21\n"
"v_and_b32 v15, s63, v21\n"
"v_mfma_f32_16x16x32_bf16 v[184:187], a[76:79], a[124:127], v[184:187]\n"
"ds_read_b128 v[16:19], v7 offset:36864\n"
"v_mul_f32 v12, v12, v92\n"
"v_mfma_f32_16x16x32_bf16 v[156:159], a[68:71], a[124:127], v[156:159]\n"
"v_mul_f32 v13, v13, v93\n"
"v_mul_f32 v14, v14, v94\n"
"v_mfma_f32_16x16x32_bf16 v[156:159], a[64:67], a[120:123], v[156:159]\n"
"v_mul_f32 v15, v15, v95\n"
"v_cvt_pk_bf16_f32 v28, v12, v13\n"
"v_mfma_f32_16x16x32_bf16 v[160:163], a[64:67], a[128:131], v[160:163]\n"
"v_cvt_pk_bf16_f32 v29, v14, v15\n"
"v_mfma_f32_16x16x32_bf16 v[160:163], a[68:71], a[132:135], v[160:163]\n"
"global_load_dwordx4 a[56:59], v0, s[34:35] offset:2048 sc1\n"
"v_mfma_f32_16x16x32_bf16 v[188:191], a[76:79], a[132:135], v[188:191]\n"
"ds_write_b64 v8, v[28:29] offset:34816\n"
"v_mfma_f32_16x16x32_bf16 v[188:191], a[72:75], a[128:131], v[188:191]\n"
"v_lshlrev_b32 v12, 16, v22\n"
"v_and_b32 v13, s63, v22\n"
"v_mfma_f32_16x16x32_bf16 v[192:195], a[72:75], a[136:139], v[192:195]\n"
"v_lshlrev_b32 v14, 16, v23\n"
"v_and_b32 v15, s63, v23\n"
"v_mfma_f32_16x16x32_bf16 v[192:195], a[76:79], a[140:143], v[192:195]\n"
"v_mul_f32 v12, v12, v120\n"
"v_mul_f32 v13, v13, v121\n"
"v_mfma_f32_16x16x32_bf16 v[164:167], a[68:71], a[140:143], v[164:167]\n"
"v_mul_f32 v14, v14, v122\n"
"v_mul_f32 v15, v15, v123\n"
"v_mfma_f32_16x16x32_bf16 v[164:167], a[64:67], a[136:139], v[164:167]\n"
"v_cvt_pk_bf16_f32 v30, v12, v13\n"
"v_cvt_pk_bf16_f32 v31, v14, v15\n"
"v_mfma_f32_16x16x32_bf16 v[168:171], a[64:67], a[144:147], v[168:171]\n"
"ds_write_b64 v8, v[30:31] offset:34824\n"
"v_mfma_f32_16x16x32_bf16 v[168:171], a[68:71], a[148:151], v[168:171]\n"
"global_load_dwordx4 a[60:63], v0, s[34:35] offset:3072 sc1\n"
"ds_read_b128 v[36:39], v2 offset:34816\n"
"v_mfma_f32_16x16x32_bf16 v[196:199], a[76:79], a[148:151], v[196:199]\n"
"s_waitcnt lgkmcnt(3)\n"
"ds_read_b128 v[64:67], v2 offset:35840\n"
"v_lshlrev_b32 v12, 16, v16\n"
"v_mfma_f32_16x16x32_bf16 v[196:199], a[72:75], a[144:147], v[196:199]\n"
"v_and_b32 v13, s63, v16\n"
"v_lshlrev_b32 v14, 16, v17\n"
"v_mfma_f32_16x16x32_bf16 a[20:23], a[72:75], a[152:155], a[20:23]\n"
"v_and_b32 v15, s63, v17\n"
"v_xor_b32 v20, 64, v7\n"
"v_mfma_f32_16x16x32_bf16 a[20:23], a[76:79], a[156:159], a[20:23]\n"
"ds_read_b128 v[20:23], v20 offset:36864\n"
"v_mul_f32 v12, v12, v40\n"
"v_mfma_f32_16x16x32_bf16 a[16:19], a[68:71], a[156:159], a[16:19]\n"
"v_mul_f32 v13, v13, v41\n"
"v_mul_f32 v14, v14, v42\n"
"v_mfma_f32_16x16x32_bf16 a[16:19], a[64:67], a[152:155], a[16:19]\n"
"s_waitcnt vmcnt(12)\n"
"global_load_dwordx4 v[92:95], v0, s[96:97] offset:-2048 sc1\n"
"v_mul_f32 v15, v15, v43\n"
"v_mfma_f32_16x16x32_bf16 v[200:203], a[80:83], a[96:99], v[200:203]\n"
"s_add_u32 s34, s36, 1482752\n"
"v_cvt_pk_bf16_f32 v24, v12, v13\n"
"v_mfma_f32_16x16x32_bf16 v[200:203], a[84:87], a[100:103], v[200:203]\n"
"global_load_dwordx4 a[64:67], v0, s[34:35] offset:0 sc1\n"
"v_cvt_pk_bf16_f32 v25, v14, v15\n"
"v_mfma_f32_16x16x32_bf16 v[228:231], a[92:95], a[100:103], v[228:231]\n"
"ds_read_b128 a[100:103], v3 offset:1024\n"
"v_mfma_f32_16x16x32_bf16 v[228:231], a[88:91], a[96:99], v[228:231]\n"
"ds_read_b128 a[96:99], v3 offset:0\n"
"v_mfma_f32_16x16x32_bf16 v[232:235], a[88:91], a[104:107], v[232:235]\n"
"ds_write_b64 v7, v[24:25] offset:36864\n"
"v_mfma_f32_16x16x32_bf16 v[232:235], a[92:95], a[108:111], v[232:235]\n"
"v_lshlrev_b32 v12, 16, v18\n"
"v_and_b32 v13, s63, v18\n"
"v_mfma_f32_16x16x32_bf16 v[204:207], a[84:87], a[108:111], v[204:207]\n"
"ds_read_b128 a[108:111], v3 offset:3072\n"
"v_lshlrev_b32 v14, 16, v19\n"
"v_mfma_f32_16x16x32_bf16 v[204:207], a[80:83], a[104:107], v[204:207]\n"
"ds_read_b128 a[104:107], v3 offset:2048\n"
"v_and_b32 v15, s63, v19\n"
"v_mfma_f32_16x16x32_bf16 v[208:211], a[80:83], a[112:115], v[208:211]\n"
"s_waitcnt lgkmcnt(7)\n"
"buffer_store_dwordx4 v[36:39], v6, s[52:55], s82 offen offset:2048 nt\n"
"global_load_dwordx4 v[120:123], v0, s[96:97] offset:-1024 sc1\n"
"v_mul_f32 v12, v12, v68\n"
"v_mul_f32 v13, v13, v69\n"
"v_mul_f32 v14, v14, v70\n"
"v_mfma_f32_16x16x32_bf16 v[208:211], a[84:87], a[116:119], v[208:211]\n"
"global_load_dwordx4 a[68:71], v0, s[34:35] offset:1024 sc1\n"
"v_mul_f32 v15, v15, v71\n"
"v_mfma_f32_16x16x32_bf16 v[236:239], a[92:95], a[116:119], v[236:239]\n"
"ds_read_b128 a[116:119], v3 offset:5120\n"
"v_cvt_pk_bf16_f32 v26, v12, v13\n"
"v_mfma_f32_16x16x32_bf16 v[236:239], a[88:91], a[112:115], v[236:239]\n"
"ds_read_b128 a[112:115], v3 offset:4096\n"
"v_cvt_pk_bf16_f32 v27, v14, v15\n"
"v_mfma_f32_16x16x32_bf16 v[240:243], a[88:91], a[120:123], v[240:243]\n"
"ds_write_b64 v7, v[26:27] offset:36872\n"
"v_mfma_f32_16x16x32_bf16 v[240:243], a[92:95], a[124:127], v[240:243]\n"
"s_waitcnt lgkmcnt(8)\n"
"v_lshlrev_b32 v12, 16, v20\n"
"v_and_b32 v13, s63, v20\n"
"v_mfma_f32_16x16x32_bf16 v[212:215], a[84:87], a[124:127], v[212:215]\n"
"ds_read_b128 a[124:127], v3 offset:7168\n"
"v_lshlrev_b32 v14, 16, v21\n"
"v_mfma_f32_16x16x32_bf16 v[212:215], a[80:83], a[120:123], v[212:215]\n"
"ds_read_b128 a[120:123], v3 offset:6144\n"
"v_and_b32 v15, s63, v21\n"
"v_mfma_f32_16x16x32_bf16 v[216:219], a[80:83], a[128:131], v[216:219]\n"
"ds_read_b128 v[16:19], v7 offset:38912\n"
"v_mul_f32 v12, v12, v96\n"
"v_mfma_f32_16x16x32_bf16 v[216:219], a[84:87], a[132:135], v[216:219]\n"
"global_load_dwordx4 a[72:75], v0, s[34:35] offset:2048 sc1\n"
"v_mul_f32 v13, v13, v97\n"
"v_mfma_f32_16x16x32_bf16 v[244:247], a[92:95], a[132:135], v[244:247]\n"
"ds_read_b128 a[132:135], v3 offset:9216\n"
"v_mul_f32 v14, v14, v98\n"
"v_mfma_f32_16x16x32_bf16 v[244:247], a[88:91], a[128:131], v[244:247]\n"
"ds_read_b128 a[128:131], v3 offset:8192\n"
"v_mul_f32 v15, v15, v99\n"
"v_mfma_f32_16x16x32_bf16 v[248:251], a[88:91], a[136:139], v[248:251]\n"
"buffer_store_dwordx4 v[64:67], v6, s[52:55], s83 offen offset:3072 nt\n"
"global_load_dwordx4 v[32:35], v0, s[96:97] offset:0 sc1\n"
"v_cvt_pk_bf16_f32 v28, v12, v13\n"
"v_cvt_pk_bf16_f32 v29, v14, v15\n"
"v_mfma_f32_16x16x32_bf16 v[248:251], a[92:95], a[140:143], v[248:251]\n"
"ds_write_b64 v8, v[28:29] offset:36864\n"
"v_mfma_f32_16x16x32_bf16 v[220:223], a[84:87], a[140:143], v[220:223]\n"
"ds_read_b128 a[140:143], v3 offset:11264\n"
"v_lshlrev_b32 v12, 16, v22\n"
"v_mfma_f32_16x16x32_bf16 v[220:223], a[80:83], a[136:139], v[220:223]\n"
"ds_read_b128 a[136:139], v3 offset:10240\n"
"v_and_b32 v13, s63, v22\n"
"v_mfma_f32_16x16x32_bf16 v[224:227], a[80:83], a[144:147], v[224:227]\n"
"v_lshlrev_b32 v14, 16, v23\n"
"v_and_b32 v15, s63, v23\n"
"v_mfma_f32_16x16x32_bf16 v[224:227], a[84:87], a[148:151], v[224:227]\n"
"global_load_dwordx4 a[76:79], v0, s[34:35] offset:3072 sc1\n"
"v_mul_f32 v12, v12, v124\n"
"v_mfma_f32_16x16x32_bf16 v[252:255], a[92:95], a[148:151], v[252:255]\n"
"ds_read_b128 a[148:151], v3 offset:13312\n"
"v_mul_f32 v13, v13, v125\n"
"v_mfma_f32_16x16x32_bf16 v[252:255], a[88:91], a[144:147], v[252:255]\n"
"ds_read_b128 a[144:147], v3 offset:12288\n"
"v_mul_f32 v14, v14, v126\n"
"v_mfma_f32_16x16x32_bf16 a[28:31], a[88:91], a[152:155], a[28:31]\n"
"v_mul_f32 v15, v15, v127\n"
"v_cvt_pk_bf16_f32 v30, v12, v13\n"
"v_mfma_f32_16x16x32_bf16 a[28:31], a[92:95], a[156:159], a[28:31]\n"
"global_load_dwordx4 v[60:63], v0, s[96:97] offset:1024 sc1\n"
"v_cvt_pk_bf16_f32 v31, v14, v15\n"
"v_mfma_f32_16x16x32_bf16 a[24:27], a[84:87], a[156:159], a[24:27]\n"
"ds_read_b128 a[156:159], v3 offset:15360\n"
"v_mfma_f32_16x16x32_bf16 a[24:27], a[80:83], a[152:155], a[24:27]\n"
"s_waitcnt vmcnt(17) lgkmcnt(14)\n"
"ds_read_b128 a[152:155], v3 offset:14336\n"
"v_mfma_f32_16x16x32_bf16 v[144:147], a[32:35], a[96:99], v[144:147]\n"
"s_add_u32 s34, s36, 1486848\n"
"ds_write_b64 v8, v[30:31] offset:36872\n"
"v_mfma_f32_16x16x32_bf16 v[144:147], a[36:39], a[100:103], v[144:147]\n"
"global_load_dwordx4 a[80:83], v0, s[34:35] offset:0 sc1\n"
"ds_read_b128 v[40:43], v2 offset:36864\n"
"v_mfma_f32_16x16x32_bf16 v[172:175], a[44:47], a[100:103], v[172:175]\n"
"s_waitcnt lgkmcnt(11)\n"
"ds_read_b128 v[68:71], v2 offset:37888\n"
"v_lshlrev_b32 v12, 16, v16\n"
"v_mfma_f32_16x16x32_bf16 v[172:175], a[40:43], a[96:99], v[172:175]\n"
"v_and_b32 v13, s63, v16\n"
"v_lshlrev_b32 v14, 16, v17\n"
"v_mfma_f32_16x16x32_bf16 v[176:179], a[40:43], a[104:107], v[176:179]\n"
"v_and_b32 v15, s63, v17\n"
"v_xor_b32 v20, 64, v7\n"
"v_mfma_f32_16x16x32_bf16 v[176:179], a[44:47], a[108:111], v[176:179]\n"
"ds_read_b128 v[20:23], v20 offset:38912\n"
"v_mul_f32 v12, v12, v44\n"
"v_mfma_f32_16x16x32_bf16 v[148:151], a[36:39], a[108:111], v[148:151]\n"
"v_mul_f32 v13, v13, v45\n"
"v_mul_f32 v14, v14, v46\n"
"v_mfma_f32_16x16x32_bf16 v[148:151], a[32:35], a[104:107], v[148:151]\n"
"v_mul_f32 v15, v15, v47\n"
"v_cvt_pk_bf16_f32 v24, v12, v13\n"
"v_mfma_f32_16x16x32_bf16 v[152:155], a[32:35], a[112:115], v[152:155]\n"
"v_cvt_pk_bf16_f32 v25, v14, v15\n"
"v_mfma_f32_16x16x32_bf16 v[152:155], a[36:39], a[116:119], v[152:155]\n"
"global_load_dwordx4 a[84:87], v0, s[34:35] offset:1024 sc1\n"
"v_mfma_f32_16x16x32_bf16 v[180:183], a[44:47], a[116:119], v[180:183]\n"
"global_load_dwordx4 v[96:99], v0, s[96:97] offset:2048 sc1\n"
"v_mfma_f32_16x16x32_bf16 v[180:183], a[40:43], a[112:115], v[180:183]\n"
"ds_write_b64 v7, v[24:25] offset:38912\n"
"v_mfma_f32_16x16x32_bf16 v[184:187], a[40:43], a[120:123], v[184:187]\n"
"v_lshlrev_b32 v12, 16, v18\n"
"v_and_b32 v13, s63, v18\n"
"v_mfma_f32_16x16x32_bf16 v[184:187], a[44:47], a[124:127], v[184:187]\n"
"v_lshlrev_b32 v14, 16, v19\n"
"v_and_b32 v15, s63, v19\n"
"v_mfma_f32_16x16x32_bf16 v[156:159], a[36:39], a[124:127], v[156:159]\n"
"v_mul_f32 v12, v12, v72\n"
"v_mul_f32 v13, v13, v73\n"
"v_mfma_f32_16x16x32_bf16 v[156:159], a[32:35], a[120:123], v[156:159]\n"
"s_waitcnt lgkmcnt(9)\n"
"v_mul_f32 v14, v14, v74\n"
"v_mul_f32 v15, v15, v75\n"
"v_mfma_f32_16x16x32_bf16 v[160:163], a[32:35], a[128:131], v[160:163]\n"
"v_cvt_pk_bf16_f32 v26, v12, v13\n"
"v_cvt_pk_bf16_f32 v27, v14, v15\n"
"v_mfma_f32_16x16x32_bf16 v[160:163], a[36:39], a[132:135], v[160:163]\n"
"global_load_dwordx4 a[88:91], v0, s[34:35] offset:2048 sc1\n"
"v_mfma_f32_16x16x32_bf16 v[188:191], a[44:47], a[132:135], v[188:191]\n"
"s_waitcnt lgkmcnt(3)\n"
"buffer_store_dwordx4 v[40:43], v6, s[52:55], s84 offen offset:0 nt\n"
"ds_write_b64 v7, v[26:27] offset:38920\n"
"v_mfma_f32_16x16x32_bf16 v[188:191], a[40:43], a[128:131], v[188:191]\n"
"s_waitcnt lgkmcnt(2)\n"
"global_load_dwordx4 v[124:127], v0, s[96:97] offset:3072 sc1\n"
"v_lshlrev_b32 v12, 16, v20\n"
"v_mfma_f32_16x16x32_bf16 v[192:195], a[40:43], a[136:139], v[192:195]\n"
"v_and_b32 v13, s63, v20\n"
"v_lshlrev_b32 v14, 16, v21\n"
"v_mfma_f32_16x16x32_bf16 v[192:195], a[44:47], a[140:143], v[192:195]\n"
"v_and_b32 v15, s63, v21\n"
"v_mul_f32 v12, v12, v100\n"
"v_mfma_f32_16x16x32_bf16 v[164:167], a[36:39], a[140:143], v[164:167]\n"
"v_mul_f32 v13, v13, v101\n"
"v_mul_f32 v14, v14, v102\n"
"v_mfma_f32_16x16x32_bf16 v[164:167], a[32:35], a[136:139], v[164:167]\n"
"v_mul_f32 v15, v15, v103\n"
"v_cvt_pk_bf16_f32 v28, v12, v13\n"
"v_mfma_f32_16x16x32_bf16 v[168:171], a[32:35], a[144:147], v[168:171]\n"
"v_cvt_pk_bf16_f32 v29, v14, v15\n"
"v_mfma_f32_16x16x32_bf16 v[168:171], a[36:39], a[148:151], v[168:171]\n"
"global_load_dwordx4 a[92:95], v0, s[34:35] offset:3072 sc1\n"
"v_mfma_f32_16x16x32_bf16 v[196:199], a[44:47], a[148:151], v[196:199]\n"
"ds_write_b64 v8, v[28:29] offset:38912\n"
"v_mfma_f32_16x16x32_bf16 v[196:199], a[40:43], a[144:147], v[196:199]\n"
"v_lshlrev_b32 v12, 16, v22\n"
"v_and_b32 v13, s63, v22\n"
"v_mfma_f32_16x16x32_bf16 a[20:23], a[40:43], a[152:155], a[20:23]\n"
"v_lshlrev_b32 v14, 16, v23\n"
"v_and_b32 v15, s63, v23\n"
"v_mfma_f32_16x16x32_bf16 a[20:23], a[44:47], a[156:159], a[20:23]\n"
"v_mul_f32 v12, v12, v128\n"
"v_mfma_f32_16x16x32_bf16 a[16:19], a[36:39], a[156:159], a[16:19]\n"
"buffer_store_dwordx4 v[68:71], v6, s[52:55], s85 offen offset:1024 nt\n"
"v_mul_f32 v13, v13, v129\n"
"v_mul_f32 v14, v14, v130\n"
"v_mul_f32 v15, v15, v131\n"
"v_mfma_f32_16x16x32_bf16 a[16:19], a[32:35], a[152:155], a[16:19]\n"
"s_waitcnt vmcnt(18)\n"
"v_cvt_pk_bf16_f32 v30, v12, v13\n"
"v_cvt_pk_bf16_f32 v31, v14, v15\n"
"v_mfma_f32_16x16x32_bf16 v[200:203], a[48:51], a[96:99], v[200:203]\n"
"s_add_u32 s34, s36, 1499136\n"
"ds_write_b64 v8, v[30:31] offset:38920\n"
"v_mfma_f32_16x16x32_bf16 v[200:203], a[52:55], a[100:103], v[200:203]\n"
"global_load_dwordx4 a[32:35], v0, s[34:35] offset:0 sc1\n"
"ds_read_b128 v[44:47], v2 offset:38912\n"
"v_mfma_f32_16x16x32_bf16 v[228:231], a[60:63], a[100:103], v[228:231]\n"
"ds_read_b128 a[100:103], v4 offset:1024\n"
"v_accvgpr_read_b32 v41, a232\n"
"v_mfma_f32_16x16x32_bf16 v[228:231], a[56:59], a[96:99], v[228:231]\n"
"ds_read_b128 a[96:99], v4 offset:0\n"
"v_accvgpr_read_b32 v43, a233\n"
"v_mfma_f32_16x16x32_bf16 v[232:235], a[56:59], a[104:107], v[232:235]\n"
"ds_read_b128 v[72:75], v2 offset:39936\n"
"v_accvgpr_read_b32 v16, a160\n"
"v_mfma_f32_16x16x32_bf16 v[232:235], a[60:63], a[108:111], v[232:235]\n"
"v_accvgpr_read_b32 v17, a161\n"
"v_accvgpr_read_b32 v18, a162\n"
"v_mfma_f32_16x16x32_bf16 v[204:207], a[52:55], a[108:111], v[204:207]\n"
"ds_read_b128 a[108:111], v4 offset:3072\n"
"v_accvgpr_read_b32 v19, a163\n"
"v_mfma_f32_16x16x32_bf16 v[204:207], a[48:51], a[104:107], v[204:207]\n"
"ds_read_b128 a[104:107], v4 offset:2048\n"
"v_lshlrev_b32 v12, 16, v16\n"
"v_mfma_f32_16x16x32_bf16 v[208:211], a[48:51], a[112:115], v[208:211]\n"
"v_and_b32 v13, s63, v16\n"
"v_lshlrev_b32 v14, 16, v17\n"
"v_mfma_f32_16x16x32_bf16 v[208:211], a[52:55], a[116:119], v[208:211]\n"
"global_load_dwordx4 a[36:39], v0, s[34:35] offset:1024 sc1\n"
"v_and_b32 v15, s63, v17\n"
"v_mfma_f32_16x16x32_bf16 v[236:239], a[60:63], a[116:119], v[236:239]\n"
"ds_read_b128 a[116:119], v4 offset:5120\n"
"v_mul_f32 v12, v12, v48\n"
"v_mfma_f32_16x16x32_bf16 v[236:239], a[56:59], a[112:115], v[236:239]\n"
"ds_read_b128 a[112:115], v4 offset:4096\n"
"v_mul_f32 v13, v13, v49\n"
"v_mfma_f32_16x16x32_bf16 v[240:243], a[56:59], a[120:123], v[240:243]\n"
"v_mul_f32 v14, v14, v50\n"
"v_mul_f32 v15, v15, v51\n"
"v_mfma_f32_16x16x32_bf16 v[240:243], a[60:63], a[124:127], v[240:243]\n"
"v_cvt_pk_bf16_f32 v24, v12, v13\n"
"v_lshlrev_b32 v40, 16, v41\n"
"v_mfma_f32_16x16x32_bf16 v[212:215], a[52:55], a[124:127], v[212:215]\n"
"ds_read_b128 a[124:127], v4 offset:7168\n"
"v_cvt_pk_bf16_f32 v25, v14, v15\n"
"v_mfma_f32_16x16x32_bf16 v[212:215], a[48:51], a[120:123], v[212:215]\n"
"ds_read_b128 a[120:123], v4 offset:6144\n"
"v_lshlrev_b32 v42, 16, v43\n"
"v_mfma_f32_16x16x32_bf16 v[216:219], a[48:51], a[128:131], v[216:219]\n"
"ds_write_b64 v7, v[24:25] offset:32768\n"
"v_mfma_f32_16x16x32_bf16 v[216:219], a[52:55], a[132:135], v[216:219]\n"
"global_load_dwordx4 a[40:43], v0, s[34:35] offset:2048 sc1\n"
"v_lshlrev_b32 v12, 16, v18\n"
"v_mfma_f32_16x16x32_bf16 v[244:247], a[60:63], a[132:135], v[244:247]\n"
"ds_read_b128 a[132:135], v4 offset:9216\n"
"v_and_b32 v13, s63, v18\n"
"v_mfma_f32_16x16x32_bf16 v[244:247], a[56:59], a[128:131], v[244:247]\n"
"ds_read_b128 a[128:131], v4 offset:8192\n"
"v_lshlrev_b32 v14, 16, v19\n"
"v_mfma_f32_16x16x32_bf16 v[248:251], a[56:59], a[136:139], v[248:251]\n"
"s_add_u32 s96, s36, 1560576\n"
"v_and_b32 v15, s63, v19\n"
"v_mul_f32 v12, v12, v76\n"
"v_mfma_f32_16x16x32_bf16 v[248:251], a[60:63], a[140:143], v[248:251]\n"
"global_load_dwordx4 v[100:103], v0, s[96:97] offset:-4096 sc1\n"
"v_mul_f32 v13, v13, v77\n"
"v_mfma_f32_16x16x32_bf16 v[220:223], a[52:55], a[140:143], v[220:223]\n"
"ds_read_b128 a[140:143], v4 offset:11264\n"
"v_mul_f32 v14, v14, v78\n"
"v_mfma_f32_16x16x32_bf16 v[220:223], a[48:51], a[136:139], v[220:223]\n"
"ds_read_b128 a[136:139], v4 offset:10240\n"
"v_mul_f32 v15, v15, v79\n"
"v_mfma_f32_16x16x32_bf16 v[224:227], a[48:51], a[144:147], v[224:227]\n"
"v_cvt_pk_bf16_f32 v26, v12, v13\n"
"v_cvt_pk_bf16_f32 v27, v14, v15\n"
"v_mfma_f32_16x16x32_bf16 v[224:227], a[52:55], a[148:151], v[224:227]\n"
"global_load_dwordx4 a[44:47], v0, s[34:35] offset:3072 sc1\n"
"v_and_b32 v41, s63, v41\n"
"v_mfma_f32_16x16x32_bf16 v[252:255], a[60:63], a[148:151], v[252:255]\n"
"ds_read_b128 a[148:151], v4 offset:13312\n"
"v_and_b32 v43, s63, v43\n"
"v_mfma_f32_16x16x32_bf16 v[252:255], a[56:59], a[144:147], v[252:255]\n"
"ds_read_b128 a[144:147], v4 offset:12288\n"
"v_accvgpr_read_b32 v69, a234\n"
"v_mfma_f32_16x16x32_bf16 a[28:31], a[56:59], a[152:155], a[28:31]\n"
"s_waitcnt lgkmcnt(15)\n"
"buffer_store_dwordx4 v[44:47], v6, s[52:55], s86 offen offset:2048 nt\n"
"ds_write_b64 v7, v[26:27] offset:32776\n"
"v_mfma_f32_16x16x32_bf16 a[28:31], a[60:63], a[156:159], a[28:31]\n"
"v_accvgpr_read_b32 v16, a164\n"
"v_accvgpr_read_b32 v17, a165\n"
"v_mfma_f32_16x16x32_bf16 a[24:27], a[52:55], a[156:159], a[24:27]\n"
"ds_read_b128 a[156:159], v4 offset:15360\n"
"v_accvgpr_read_b32 v18, a166\n"
"v_mfma_f32_16x16x32_bf16 a[24:27], a[48:51], a[152:155], a[24:27]\n"
"s_waitcnt vmcnt(15) lgkmcnt(13)\n"
"ds_read_b128 a[152:155], v4 offset:14336\n"
"v_accvgpr_read_b32 v19, a167\n"
"v_mfma_f32_16x16x32_bf16 v[144:147], a[64:67], a[96:99], v[144:147]\n"
"s_add_u32 s34, s36, 1503232\n"
"global_load_dwordx4 v[128:131], v0, s[96:97] offset:-3072 sc1\n"
"v_mfma_f32_16x16x32_bf16 v[144:147], a[68:71], a[100:103], v[144:147]\n"
"global_load_dwordx4 a[48:51], v0, s[34:35] offset:0 sc1\n"
"v_lshlrev_b32 v12, 16, v16\n"
"v_mfma_f32_16x16x32_bf16 v[172:175], a[76:79], a[100:103], v[172:175]\n"
"v_and_b32 v13, s63, v16\n"
"v_lshlrev_b32 v14, 16, v17\n"
"v_mfma_f32_16x16x32_bf16 v[172:175], a[72:75], a[96:99], v[172:175]\n"
"v_and_b32 v15, s63, v17\n"
"v_mul_f32 v12, v12, v104\n"
"v_mfma_f32_16x16x32_bf16 v[176:179], a[72:75], a[104:107], v[176:179]\n"
"v_mul_f32 v13, v13, v105\n"
"v_mul_f32 v14, v14, v106\n"
"v_mfma_f32_16x16x32_bf16 v[176:179], a[76:79], a[108:111], v[176:179]\n"
"v_mul_f32 v15, v15, v107\n"
"v_accvgpr_read_b32 v71, a235\n"
"v_mfma_f32_16x16x32_bf16 v[148:151], a[68:71], a[108:111], v[148:151]\n"
"v_cvt_pk_bf16_f32 v28, v12, v13\n"
"v_lshlrev_b32 v68, 16, v69\n"
"v_mfma_f32_16x16x32_bf16 v[148:151], a[64:67], a[104:107], v[148:151]\n"
"s_waitcnt lgkmcnt(10)\n"
"v_cvt_pk_bf16_f32 v29, v14, v15\n"
"v_lshlrev_b32 v70, 16, v71\n"
"v_mfma_f32_16x16x32_bf16 v[152:155], a[64:67], a[112:115], v[152:155]\n"
"buffer_store_dwordx4 v[72:75], v6, s[52:55], s87 offen offset:3072 nt\n"
"ds_write_b64 v8, v[28:29] offset:32768\n"
"v_mfma_f32_16x16x32_bf16 v[152:155], a[68:71], a[116:119], v[152:155]\n"
"global_load_dwordx4 a[52:55], v0, s[34:35] offset:1024 sc1\n"
"v_lshlrev_b32 v12, 16, v18\n"
"v_mfma_f32_16x16x32_bf16 v[180:183], a[76:79], a[116:119], v[180:183]\n"
"global_load_dwordx4 v[36:39], v0, s[96:97] offset:-2048 sc1\n"
"v_and_b32 v13, s63, v18\n"
"v_mfma_f32_16x16x32_bf16 v[180:183], a[72:75], a[112:115], v[180:183]\n"
"v_lshlrev_b32 v14, 16, v19\n"
"v_and_b32 v15, s63, v19\n"
"v_mfma_f32_16x16x32_bf16 v[184:187], a[72:75], a[120:123], v[184:187]\n"
"v_mul_f32 v12, v12, v132\n"
"v_mul_f32 v13, v13, v133\n"
"v_mfma_f32_16x16x32_bf16 v[184:187], a[76:79], a[124:127], v[184:187]\n"
"v_mul_f32 v14, v14, v134\n"
"v_mul_f32 v15, v15, v135\n"
"v_accvgpr_read_b32 v45, a236\n"
"v_mfma_f32_16x16x32_bf16 v[156:159], a[68:71], a[124:127], v[156:159]\n"
"v_cvt_pk_bf16_f32 v30, v12, v13\n"
"v_cvt_pk_bf16_f32 v31, v14, v15\n"
"v_mfma_f32_16x16x32_bf16 v[156:159], a[64:67], a[120:123], v[156:159]\n"
"s_waitcnt lgkmcnt(6)\n"
"ds_write_b64 v8, v[30:31] offset:32776\n"
"v_mfma_f32_16x16x32_bf16 v[160:163], a[64:67], a[128:131], v[160:163]\n"
"ds_read_b128 v[48:51], v2 offset:32768\n"
"v_and_b32 v69, s63, v69\n"
"v_mfma_f32_16x16x32_bf16 v[160:163], a[68:71], a[132:135], v[160:163]\n"
"global_load_dwordx4 a[56:59], v0, s[34:35] offset:2048 sc1\n"
"ds_read_b128 v[76:79], v2 offset:33792\n"
"v_mfma_f32_16x16x32_bf16 v[188:191], a[76:79], a[132:135], v[188:191]\n"
"v_accvgpr_read_b32 v16, a168\n"
"v_accvgpr_read_b32 v17, a169\n"
"v_accvgpr_read_b32 v47, a237\n"
"v_mfma_f32_16x16x32_bf16 v[188:191], a[72:75], a[128:131], v[188:191]\n"
"v_accvgpr_read_b32 v18, a170\n"
"v_accvgpr_read_b32 v19, a171\n"
"v_mfma_f32_16x16x32_bf16 v[192:195], a[72:75], a[136:139], v[192:195]\n"
"global_load_dwordx4 v[64:67], v0, s[96:97] offset:-1024 sc1\n"
"v_lshlrev_b32 v12, 16, v16\n"
"v_mfma_f32_16x16x32_bf16 v[192:195], a[76:79], a[140:143], v[192:195]\n"
"v_and_b32 v13, s63, v16\n"
"v_lshlrev_b32 v14, 16, v17\n"
"v_mfma_f32_16x16x32_bf16 v[164:167], a[68:71], a[140:143], v[164:167]\n"
"v_and_b32 v15, s63, v17\n"
"v_mul_f32 v12, v12, v52\n"
"v_mfma_f32_16x16x32_bf16 v[164:167], a[64:67], a[136:139], v[164:167]\n"
"s_waitcnt lgkmcnt(4)\n"
"v_mul_f32 v13, v13, v53\n"
"v_mul_f32 v14, v14, v54\n"
"v_lshlrev_b32 v44, 16, v45\n"
"v_mfma_f32_16x16x32_bf16 v[168:171], a[64:67], a[144:147], v[168:171]\n"
"v_mul_f32 v15, v15, v55\n"
"v_cvt_pk_bf16_f32 v24, v12, v13\n"
"v_mfma_f32_16x16x32_bf16 v[168:171], a[68:71], a[148:151], v[168:171]\n"
"global_load_dwordx4 a[60:63], v0, s[34:35] offset:3072 sc1\n"
"v_cvt_pk_bf16_f32 v25, v14, v15\n"
"v_mfma_f32_16x16x32_bf16 v[196:199], a[76:79], a[148:151], v[196:199]\n"
"ds_write_b64 v7, v[24:25] offset:34816\n"
"v_mfma_f32_16x16x32_bf16 v[196:199], a[72:75], a[144:147], v[196:199]\n"
"v_lshlrev_b32 v12, 16, v18\n"
"v_and_b32 v13, s63, v18\n"
"v_mfma_f32_16x16x32_bf16 a[20:23], a[72:75], a[152:155], a[20:23]\n"
"v_lshlrev_b32 v14, 16, v19\n"
"v_and_b32 v15, s63, v19\n"
"v_mfma_f32_16x16x32_bf16 a[20:23], a[76:79], a[156:159], a[20:23]\n"
"v_mul_f32 v12, v12, v80\n"
"v_and_b32 v71, s63, v71\n"
"v_mfma_f32_16x16x32_bf16 a[16:19], a[68:71], a[156:159], a[16:19]\n"
"global_load_dwordx4 v[104:107], v0, s[96:97] offset:0 sc1\n"
"v_mul_f32 v13, v13, v81\n"
"v_mfma_f32_16x16x32_bf16 a[16:19], a[64:67], a[152:155], a[16:19]\n"
"s_waitcnt vmcnt(16)\n"
"v_mul_f32 v14, v14, v82\n"
"v_mul_f32 v15, v15, v83\n"
"v_mfma_f32_16x16x32_bf16 v[200:203], a[80:83], a[96:99], v[200:203]\n"
"s_add_u32 s34, s36, 1548288\n"
"v_cvt_pk_bf16_f32 v26, v12, v13\n"
"v_lshlrev_b32 v46, 16, v47\n"
"v_mfma_f32_16x16x32_bf16 v[200:203], a[84:87], a[100:103], v[200:203]\n"
"global_load_dwordx4 a[64:67], v0, s[34:35] offset:0 sc1\n"
"v_cvt_pk_bf16_f32 v27, v14, v15\n"
"v_mfma_f32_16x16x32_bf16 v[228:231], a[92:95], a[100:103], v[228:231]\n"
"ds_read_b128 a[100:103], v5 offset:1024\n"
"v_and_b32 v45, s63, v45\n"
"v_mfma_f32_16x16x32_bf16 v[228:231], a[88:91], a[96:99], v[228:231]\n"
"ds_read_b128 a[96:99], v5 offset:0\n"
"v_and_b32 v47, s63, v47\n"
"v_mfma_f32_16x16x32_bf16 v[232:235], a[88:91], a[104:107], v[232:235]\n"
"ds_write_b64 v7, v[26:27] offset:34824\n"
"v_mfma_f32_16x16x32_bf16 v[232:235], a[92:95], a[108:111], v[232:235]\n"
"v_accvgpr_read_b32 v16, a172\n"
"v_accvgpr_read_b32 v17, a173\n"
"v_mfma_f32_16x16x32_bf16 v[204:207], a[84:87], a[108:111], v[204:207]\n"
"s_waitcnt lgkmcnt(5)\n"
"buffer_store_dwordx4 v[48:51], v6, s[52:55], s88 offen offset:0 nt\n"
"ds_read_b128 a[108:111], v5 offset:3072\n"
"v_accvgpr_read_b32 v18, a174\n"
"v_accvgpr_read_b32 v19, a175\n"
"v_accvgpr_read_b32 v73, a238\n"
"v_mfma_f32_16x16x32_bf16 v[204:207], a[80:83], a[104:107], v[204:207]\n"
"ds_read_b128 a[104:107], v5 offset:2048\n"
"v_lshlrev_b32 v12, 16, v16\n"
"v_mfma_f32_16x16x32_bf16 v[208:211], a[80:83], a[112:115], v[208:211]\n"
"global_load_dwordx4 v[132:135], v0, s[96:97] offset:1024 sc1\n"
"v_and_b32 v13, s63, v16\n"
"v_mfma_f32_16x16x32_bf16 v[208:211], a[84:87], a[116:119], v[208:211]\n"
"global_load_dwordx4 a[68:71], v0, s[34:35] offset:1024 sc1\n"
"v_lshlrev_b32 v14, 16, v17\n"
"v_mfma_f32_16x16x32_bf16 v[236:239], a[92:95], a[116:119], v[236:239]\n"
"ds_read_b128 a[116:119], v5 offset:5120\n"
"v_and_b32 v15, s63, v17\n"
"v_mfma_f32_16x16x32_bf16 v[236:239], a[88:91], a[112:115], v[236:239]\n"
"ds_read_b128 a[112:115], v5 offset:4096\n"
"v_mul_f32 v12, v12, v108\n"
"v_mfma_f32_16x16x32_bf16 v[240:243], a[88:91], a[120:123], v[240:243]\n"
"v_mul_f32 v13, v13, v109\n"
"v_mul_f32 v14, v14, v110\n"
"v_mfma_f32_16x16x32_bf16 v[240:243], a[92:95], a[124:127], v[240:243]\n"
"v_mul_f32 v15, v15, v111\n"
"v_cvt_pk_bf16_f32 v28, v12, v13\n"
"v_mfma_f32_16x16x32_bf16 v[212:215], a[84:87], a[124:127], v[212:215]\n"
"ds_read_b128 a[124:127], v5 offset:7168\n"
"v_cvt_pk_bf16_f32 v29, v14, v15\n"
"v_mfma_f32_16x16x32_bf16 v[212:215], a[80:83], a[120:123], v[212:215]\n"
"ds_read_b128 a[120:123], v5 offset:6144\n"
"v_accvgpr_read_b32 v75, a239\n"
"v_mfma_f32_16x16x32_bf16 v[216:219], a[80:83], a[128:131], v[216:219]\n"
"ds_write_b64 v8, v[28:29] offset:34816\n"
"v_mfma_f32_16x16x32_bf16 v[216:219], a[84:87], a[132:135], v[216:219]\n"
"global_load_dwordx4 a[72:75], v0, s[34:35] offset:2048 sc1\n"
"v_lshlrev_b32 v12, 16, v18\n"
"v_mfma_f32_16x16x32_bf16 v[244:247], a[92:95], a[132:135], v[244:247]\n"
"s_waitcnt lgkmcnt(11)\n"
"buffer_store_dwordx4 v[76:79], v6, s[52:55], s89 offen offset:1024 nt\n"
"ds_read_b128 a[132:135], v5 offset:9216\n"
"v_and_b32 v13, s63, v18\n"
"v_lshlrev_b32 v14, 16, v19\n"
"v_lshlrev_b32 v72, 16, v73\n"
"v_mfma_f32_16x16x32_bf16 v[244:247], a[88:91], a[128:131], v[244:247]\n"
"ds_read_b128 a[128:131], v5 offset:8192\n"
"v_and_b32 v15, s63, v19\n"
"v_mfma_f32_16x16x32_bf16 v[248:251], a[88:91], a[136:139], v[248:251]\n"
"v_mul_f32 v12, v12, v136\n"
"v_mul_f32 v13, v13, v137\n"
"v_mfma_f32_16x16x32_bf16 v[248:251], a[92:95], a[140:143], v[248:251]\n"
"L_st10880:\n"
"s_cbranch_execz L_retC\n"
"v_mul_f32 v14, v14, v138\n"
"v_mul_f32 v15, v15, v139\n"
"v_mfma_f32_16x16x32_bf16 v[220:223], a[84:87], a[140:143], v[220:223]\n"
"ds_read_b128 a[140:143], v5 offset:11264\n"
"v_cvt_pk_bf16_f32 v30, v12, v13\n"
"v_mfma_f32_16x16x32_bf16 v[220:223], a[80:83], a[136:139], v[220:223]\n"
"ds_read_b128 a[136:139], v5 offset:10240\n"
"v_cvt_pk_bf16_f32 v31, v14, v15\n"
"v_mfma_f32_16x16x32_bf16 v[224:227], a[80:83], a[144:147], v[224:227]\n"
"ds_write_b64 v8, v[30:31] offset:34824\n"
"v_mfma_f32_16x16x32_bf16 v[224:227], a[84:87], a[148:151], v[224:227]\n"
"global_load_dwordx4 a[76:79], v0, s[34:35] offset:3072 sc1\n"
"ds_read_b128 v[52:55], v2 offset:34816\n"
"v_mfma_f32_16x16x32_bf16 v[252:255], a[92:95], a[148:151], v[252:255]\n"
"ds_read_b128 a[148:151], v5 offset:13312\n"
"v_lshlrev_b32 v74, 16, v75\n"
"v_mfma_f32_16x16x32_bf16 v[252:255], a[88:91], a[144:147], v[252:255]\n"
"ds_read_b128 a[144:147], v5 offset:12288\n"
"v_and_b32 v73, s63, v73\n"
"v_mfma_f32_16x16x32_bf16 a[28:31], a[88:91], a[152:155], a[28:31]\n"
"ds_read_b128 v[80:83], v2 offset:35840\n"
"v_accvgpr_read_b32 v16, a176\n"
"v_mfma_f32_16x16x32_bf16 a[28:31], a[92:95], a[156:159], a[28:31]\n"
"v_accvgpr_read_b32 v17, a177\n"
"v_accvgpr_read_b32 v18, a178\n"
"v_mfma_f32_16x16x32_bf16 a[24:27], a[84:87], a[156:159], a[24:27]\n"
"ds_read_b128 a[156:159], v5 offset:15360\n"
"v_accvgpr_read_b32 v19, a179\n"
"v_mfma_f32_16x16x32_bf16 a[24:27], a[80:83], a[152:155], a[24:27]\n"
"s_waitcnt vmcnt(17) lgkmcnt(15)\n"
"ds_read_b128 a[152:155], v5 offset:14336\n"
"v_lshlrev_b32 v12, 16, v16\n"
"v_mfma_f32_16x16x32_bf16 v[144:147], a[32:35], a[96:99], v[144:147]\n"
"s_add_u32 s34, s36, 1552384\n"
"v_and_b32 v13, s63, v16\n"
"v_lshlrev_b32 v14, 16, v17\n"
"v_mfma_f32_16x16x32_bf16 v[144:147], a[36:39], a[100:103], v[144:147]\n"
"global_load_dwordx4 a[80:83], v0, s[34:35] offset:0 sc1\n"
"v_and_b32 v15, s63, v17\n"
"v_mfma_f32_16x16x32_bf16 v[172:175], a[44:47], a[100:103], v[172:175]\n"
"v_mul_f32 v12, v12, v56\n"
"v_mul_f32 v13, v13, v57\n"
"v_mfma_f32_16x16x32_bf16 v[172:175], a[40:43], a[96:99], v[172:175]\n"
"v_mul_f32 v14, v14, v58\n"
"v_mul_f32 v15, v15, v59\n"
"v_mfma_f32_16x16x32_bf16 v[176:179], a[40:43], a[104:107], v[176:179]\n"
"global_load_dwordx4 a[84:87], v0, s[34:35] offset:1024 sc1\n"
"v_cvt_pk_bf16_f32 v24, v12, v13\n"
"v_mfma_f32_16x16x32_bf16 v[176:179], a[44:47], a[108:111], v[176:179]\n"
"v_cvt_pk_bf16_f32 v25, v14, v15\n"
"v_and_b32 v75, s63, v75\n"
"v_mfma_f32_16x16x32_bf16 v[148:151], a[36:39], a[108:111], v[148:151]\n"
"ds_write_b64 v7, v[24:25] offset:36864\n"
"v_mfma_f32_16x16x32_bf16 v[148:151], a[32:35], a[104:107], v[148:151]\n"
"s_waitcnt lgkmcnt(13)\n"
"global_load_dwordx4 a[88:91], v0, s[34:35] offset:2048 sc1\n"
"v_lshlrev_b32 v12, 16, v18\n"
"v_mfma_f32_16x16x32_bf16 v[152:155], a[32:35], a[112:115], v[152:155]\n"
"global_load_dwordx4 v[108:111], v0, s[96:97] offset:2048 sc1\n"
"v_and_b32 v13, s63, v18\n"
"v_mfma_f32_16x16x32_bf16 v[152:155], a[36:39], a[116:119], v[152:155]\n"
"v_lshlrev_b32 v14, 16, v19\n"
"v_and_b32 v15, s63, v19\n"
"v_mfma_f32_16x16x32_bf16 v[180:183], a[44:47], a[116:119], v[180:183]\n"
"global_load_dwordx4 a[92:95], v0, s[34:35] offset:3072 sc1\n"
"v_mul_f32 v12, v12, v84\n"
"v_mfma_f32_16x16x32_bf16 v[180:183], a[40:43], a[112:115], v[180:183]\n"
"v_mul_f32 v13, v13, v85\n"
"v_mul_f32 v14, v14, v86\n"
"v_mfma_f32_16x16x32_bf16 v[184:187], a[40:43], a[120:123], v[184:187]\n"
"v_mul_f32 v15, v15, v87\n"
"v_cvt_pk_bf16_f32 v26, v12, v13\n"
"v_mfma_f32_16x16x32_bf16 v[184:187], a[44:47], a[124:127], v[184:187]\n"
"v_cvt_pk_bf16_f32 v27, v14, v15\n"
"v_accvgpr_read_b32 v49, a240\n"
"v_mfma_f32_16x16x32_bf16 v[156:159], a[36:39], a[124:127], v[156:159]\n"
"ds_write_b64 v7, v[26:27] offset:36872\n"
"v_mfma_f32_16x16x32_bf16 v[156:159], a[32:35], a[120:123], v[156:159]\n"
"s_waitcnt lgkmcnt(9)\n"
"v_accvgpr_read_b32 v16, a180\n"
"v_accvgpr_read_b32 v17, a181\n"
"v_mfma_f32_16x16x32_bf16 v[160:163], a[32:35], a[128:131], v[160:163]\n"
"s_waitcnt lgkmcnt(7)\n"
"buffer_store_dwordx4 v[52:55], v6, s[52:55], s90 offen offset:2048 nt\n"
"v_accvgpr_read_b32 v18, a182\n"
"v_accvgpr_read_b32 v19, a183\n"
"v_lshlrev_b32 v12, 16, v16\n"
"v_accvgpr_read_b32 v51, a241\n"
"v_mfma_f32_16x16x32_bf16 v[160:163], a[36:39], a[132:135], v[160:163]\n"
"global_load_dwordx4 v[136:139], v0, s[96:97] offset:3072 sc1\n"
"v_and_b32 v13, s63, v16\n"
"v_mfma_f32_16x16x32_bf16 v[188:191], a[44:47], a[132:135], v[188:191]\n"
"v_lshlrev_b32 v14, 16, v17\n"
"v_and_b32 v15, s63, v17\n"
"v_mfma_f32_16x16x32_bf16 v[188:191], a[40:43], a[128:131], v[188:191]\n"
"v_mul_f32 v12, v12, v112\n"
"v_mul_f32 v13, v13, v113\n"
"v_mfma_f32_16x16x32_bf16 v[192:195], a[40:43], a[136:139], v[192:195]\n"
"v_mul_f32 v14, v14, v114\n"
"v_mul_f32 v15, v15, v115\n"
"v_mfma_f32_16x16x32_bf16 v[192:195], a[44:47], a[140:143], v[192:195]\n"
"v_cvt_pk_bf16_f32 v28, v12, v13\n"
"v_cvt_pk_bf16_f32 v29, v14, v15\n"
"v_mfma_f32_16x16x32_bf16 v[164:167], a[36:39], a[140:143], v[164:167]\n"
"ds_write_b64 v8, v[28:29] offset:36864\n"
"v_mfma_f32_16x16x32_bf16 v[164:167], a[32:35], a[136:139], v[164:167]\n"
"s_waitcnt lgkmcnt(3)\n"
"v_lshlrev_b32 v12, 16, v18\n"
"v_and_b32 v13, s63, v18\n"
"v_mfma_f32_16x16x32_bf16 v[168:171], a[32:35], a[144:147], v[168:171]\n"
"v_lshlrev_b32 v14, 16, v19\n"
"v_and_b32 v15, s63, v19\n"
"v_mfma_f32_16x16x32_bf16 v[168:171], a[36:39], a[148:151], v[168:171]\n"
"v_mul_f32 v12, v12, v140\n"
"v_mul_f32 v13, v13, v141\n"
"v_mfma_f32_16x16x32_bf16 v[196:199], a[44:47], a[148:151], v[196:199]\n"
"v_mul_f32 v14, v14, v142\n"
"v_mul_f32 v15, v15, v143\n"
"v_mfma_f32_16x16x32_bf16 v[196:199], a[40:43], a[144:147], v[196:199]\n"
"v_cvt_pk_bf16_f32 v30, v12, v13\n"
"v_cvt_pk_bf16_f32 v31, v14, v15\n"
"v_mfma_f32_16x16x32_bf16 a[20:23], a[40:43], a[152:155], a[20:23]\n"
"ds_write_b64 v8, v[30:31] offset:36872\n"
"buffer_store_dwordx4 v[80:83], v6, s[52:55], s91 offen offset:3072 nt\n"
"v_lshlrev_b32 v48, 16, v49\n"
"v_lshlrev_b32 v50, 16, v51\n"
"v_and_b32 v49, s63, v49\n"
"v_and_b32 v51, s63, v51\n"
"v_mfma_f32_16x16x32_bf16 a[20:23], a[44:47], a[156:159], a[20:23]\n"
"ds_read_b128 v[56:59], v2 offset:36864\n"
"v_accvgpr_read_b32 v77, a242\n"
"v_mfma_f32_16x16x32_bf16 a[16:19], a[36:39], a[156:159], a[16:19]\n"
"ds_read_b128 v[84:87], v2 offset:37888\n"
"v_accvgpr_read_b32 v79, a243\n"
"v_mfma_f32_16x16x32_bf16 a[16:19], a[32:35], a[152:155], a[16:19]\n"
"s_waitcnt vmcnt(16)\n"
"v_accvgpr_read_b32 v16, a184\n"
"v_lshlrev_b32 v76, 16, v77\n"
"v_mfma_f32_16x16x32_bf16 v[200:203], a[48:51], a[96:99], v[200:203]\n"
"v_accvgpr_read_b32 v17, a185\n"
"v_lshlrev_b32 v78, 16, v79\n"
"v_mfma_f32_16x16x32_bf16 v[200:203], a[52:55], a[100:103], v[200:203]\n"
"v_accvgpr_read_b32 v18, a186\n"
"v_and_b32 v77, s63, v77\n"
"v_mfma_f32_16x16x32_bf16 v[228:231], a[60:63], a[100:103], v[228:231]\n"
"ds_read_b128 a[100:103], v2 offset:17408\n"
"v_and_b32 v79, s63, v79\n"
"v_mfma_f32_16x16x32_bf16 v[228:231], a[56:59], a[96:99], v[228:231]\n"
"ds_read_b128 a[96:99], v2 offset:16384\n"
"v_accvgpr_read_b32 v19, a187\n"
"v_mfma_f32_16x16x32_bf16 v[232:235], a[56:59], a[104:107], v[232:235]\n"
"v_lshlrev_b32 v12, 16, v16\n"
"v_and_b32 v13, s63, v16\n"
"v_mfma_f32_16x16x32_bf16 v[232:235], a[60:63], a[108:111], v[232:235]\n"
"v_lshlrev_b32 v14, 16, v17\n"
"v_and_b32 v15, s63, v17\n"
"v_mfma_f32_16x16x32_bf16 v[204:207], a[52:55], a[108:111], v[204:207]\n"
"ds_read_b128 a[108:111], v3 offset:17408\n"
"v_accvgpr_read_b32 v10, a0\n"
"v_mfma_f32_16x16x32_bf16 v[204:207], a[48:51], a[104:107], v[204:207]\n"
"ds_read_b128 a[104:107], v3 offset:16384\n"
"v_accvgpr_read_b32 v11, a1\n"
"v_mfma_f32_16x16x32_bf16 v[208:211], a[48:51], a[112:115], v[208:211]\n"
"v_mul_f32 v12, v12, v10\n"
"v_mul_f32 v13, v13, v11\n"
"v_mfma_f32_16x16x32_bf16 v[208:211], a[52:55], a[116:119], v[208:211]\n"
"v_accvgpr_read_b32 v10, a2\n"
"v_accvgpr_read_b32 v11, a3\n"
"v_mfma_f32_16x16x32_bf16 v[236:239], a[60:63], a[116:119], v[236:239]\n"
"ds_read_b128 a[116:119], v4 offset:17408\n"
"v_mul_f32 v14, v14, v10\n"
"v_mfma_f32_16x16x32_bf16 v[236:239], a[56:59], a[112:115], v[236:239]\n"
"ds_read_b128 a[112:115], v4 offset:16384\n"
"v_mul_f32 v15, v15, v11\n"
"v_mfma_f32_16x16x32_bf16 v[240:243], a[56:59], a[120:123], v[240:243]\n"
"v_cvt_pk_bf16_f32 v24, v12, v13\n"
"v_cvt_pk_bf16_f32 v25, v14, v15\n"
"v_mfma_f32_16x16x32_bf16 v[240:243], a[60:63], a[124:127], v[240:243]\n"
"ds_write_b64 v7, v[24:25] offset:38912\n"
"v_mfma_f32_16x16x32_bf16 v[212:215], a[52:55], a[124:127], v[212:215]\n"
"ds_read_b128 a[124:127], v2 offset:19456\n"
"v_lshlrev_b32 v12, 16, v18\n"
"v_mfma_f32_16x16x32_bf16 v[212:215], a[48:51], a[120:123], v[212:215]\n"
"ds_read_b128 a[120:123], v2 offset:18432\n"
"v_and_b32 v13, s63, v18\n"
"v_mfma_f32_16x16x32_bf16 v[216:219], a[48:51], a[128:131], v[216:219]\n"
"v_lshlrev_b32 v14, 16, v19\n"
"v_and_b32 v15, s63, v19\n"
"v_mfma_f32_16x16x32_bf16 v[216:219], a[52:55], a[132:135], v[216:219]\n"
"v_accvgpr_read_b32 v10, a4\n"
"v_accvgpr_read_b32 v11, a5\n"
"v_mfma_f32_16x16x32_bf16 v[244:247], a[60:63], a[132:135], v[244:247]\n"
"ds_read_b128 a[132:135], v3 offset:19456\n"
"v_mul_f32 v12, v12, v10\n"
"v_mfma_f32_16x16x32_bf16 v[244:247], a[56:59], a[128:131], v[244:247]\n"
"ds_read_b128 a[128:131], v3 offset:18432\n"
"v_mul_f32 v13, v13, v11\n"
"v_mfma_f32_16x16x32_bf16 v[248:251], a[56:59], a[136:139], v[248:251]\n"
"v_accvgpr_read_b32 v10, a6\n"
"v_accvgpr_read_b32 v11, a7\n"
"v_mfma_f32_16x16x32_bf16 v[248:251], a[60:63], a[140:143], v[248:251]\n"
"v_mul_f32 v14, v14, v10\n"
"v_mul_f32 v15, v15, v11\n"
"v_mfma_f32_16x16x32_bf16 v[220:223], a[52:55], a[140:143], v[220:223]\n"
"ds_read_b128 a[140:143], v4 offset:19456\n"
"v_cvt_pk_bf16_f32 v26, v12, v13\n"
"v_mfma_f32_16x16x32_bf16 v[220:223], a[48:51], a[136:139], v[220:223]\n"
"ds_read_b128 a[136:139], v4 offset:18432\n"
"v_cvt_pk_bf16_f32 v27, v14, v15\n"
"v_mfma_f32_16x16x32_bf16 v[224:227], a[48:51], a[144:147], v[224:227]\n"
"ds_write_b64 v7, v[26:27] offset:38920\n"
"v_mfma_f32_16x16x32_bf16 v[224:227], a[52:55], a[148:151], v[224:227]\n"
"v_accvgpr_read_b32 v16, a188\n"
"v_accvgpr_read_b32 v53, a244\n"
"v_mfma_f32_16x16x32_bf16 v[252:255], a[60:63], a[148:151], v[252:255]\n"
"s_waitcnt lgkmcnt(15)\n"
"buffer_store_dwordx4 v[56:59], v6, s[52:55], s92 offen offset:0 nt\n"
"ds_read_b128 a[148:151], v2 offset:21504\n"
"v_accvgpr_read_b32 v17, a189\n"
"v_accvgpr_read_b32 v18, a190\n"
"v_mfma_f32_16x16x32_bf16 v[252:255], a[56:59], a[144:147], v[252:255]\n"
"ds_read_b128 a[144:147], v2 offset:20480\n"
"v_accvgpr_read_b32 v19, a191\n"
"v_mfma_f32_16x16x32_bf16 a[28:31], a[56:59], a[152:155], a[28:31]\n"
"v_lshlrev_b32 v12, 16, v16\n"
"v_and_b32 v13, s63, v16\n"
"v_mfma_f32_16x16x32_bf16 a[28:31], a[60:63], a[156:159], a[28:31]\n"
"v_lshlrev_b32 v14, 16, v17\n"
"v_and_b32 v15, s63, v17\n"
"v_mfma_f32_16x16x32_bf16 a[24:27], a[52:55], a[156:159], a[24:27]\n"
"ds_read_b128 a[156:159], v3 offset:21504\n"
"v_accvgpr_read_b32 v10, a8\n"
"v_mfma_f32_16x16x32_bf16 a[24:27], a[48:51], a[152:155], a[24:27]\n"
"s_waitcnt lgkmcnt(15)\n"
"ds_read_b128 a[152:155], v3 offset:20480\n"
"v_accvgpr_read_b32 v11, a9\n"
"v_mfma_f32_16x16x32_bf16 v[144:147], v[88:91], a[96:99], v[144:147]\n"
"v_mul_f32 v12, v12, v10\n"
"v_mul_f32 v13, v13, v11\n"
"v_mfma_f32_16x16x32_bf16 v[144:147], v[116:119], a[100:103], v[144:147]\n"
"v_accvgpr_read_b32 v10, a10\n"
"v_accvgpr_read_b32 v11, a11\n"
"v_mfma_f32_16x16x32_bf16 v[172:175], v[120:123], a[100:103], v[172:175]\n"
"v_mul_f32 v14, v14, v10\n"
"v_mul_f32 v15, v15, v11\n"
"v_mfma_f32_16x16x32_bf16 v[172:175], v[92:95], a[96:99], v[172:175]\n"
"v_cvt_pk_bf16_f32 v28, v12, v13\n"
"v_cvt_pk_bf16_f32 v29, v14, v15\n"
"v_mfma_f32_16x16x32_bf16 v[200:203], v[32:35], a[96:99], v[200:203]\n"
"buffer_store_dwordx4 v[84:87], v6, s[52:55], s93 offen offset:1024 nt\n"
"ds_write_b64 v8, v[28:29] offset:38912\n"
"v_mfma_f32_16x16x32_bf16 v[200:203], v[60:63], a[100:103], v[200:203]\n"
"v_lshlrev_b32 v12, 16, v18\n"
"v_accvgpr_read_b32 v55, a245\n"
"v_mfma_f32_16x16x32_bf16 v[228:231], v[124:127], a[100:103], v[228:231]\n"
"ds_read_b128 a[100:103], v4 offset:21504\n"
"v_lshlrev_b32 v52, 16, v53\n"
"v_mfma_f32_16x16x32_bf16 v[228:231], v[96:99], a[96:99], v[228:231]\n"
"s_waitcnt vmcnt(3) lgkmcnt(15)\n"
"ds_read_b128 a[96:99], v4 offset:20480\n"
"v_lshlrev_b32 v54, 16, v55\n"
"v_mfma_f32_16x16x32_bf16 v[228:231], v[108:111], a[104:107], v[228:231]\n"
"v_and_b32 v13, s63, v18\n"
"v_and_b32 v53, s63, v53\n"
"v_mfma_f32_16x16x32_bf16 v[228:231], v[136:139], a[108:111], v[228:231]\n"
"v_lshlrev_b32 v14, 16, v19\n"
"v_and_b32 v55, s63, v55\n"
"v_mfma_f32_16x16x32_bf16 v[200:203], v[132:135], a[108:111], v[200:203]\n"
"v_and_b32 v15, s63, v19\n"
"v_accvgpr_read_b32 v81, a246\n"
"v_mfma_f32_16x16x32_bf16 v[200:203], v[104:107], a[104:107], v[200:203]\n"
"v_accvgpr_read_b32 v10, a12\n"
"v_accvgpr_read_b32 v83, a247\n"
"v_mfma_f32_16x16x32_bf16 v[172:175], v[36:39], a[104:107], v[172:175]\n"
"v_accvgpr_read_b32 v11, a13\n"
"v_lshlrev_b32 v80, 16, v81\n"
"v_mfma_f32_16x16x32_bf16 v[172:175], v[64:67], a[108:111], v[172:175]\n"
"v_mul_f32 v12, v12, v10\n"
"v_accvgpr_read_b32 v57, a248\n"
"v_mfma_f32_16x16x32_bf16 v[144:147], v[128:131], a[108:111], v[144:147]\n"
"ds_read_b128 a[108:111], v2 offset:23552\n"
"v_accvgpr_read_b32 v59, a249\n"
"v_mfma_f32_16x16x32_bf16 v[144:147], v[100:103], a[104:107], v[144:147]\n"
"s_waitcnt lgkmcnt(15)\n"
"ds_read_b128 a[104:107], v2 offset:22528\n"
"v_lshlrev_b32 v82, 16, v83\n"
"v_mfma_f32_16x16x32_bf16 v[144:147], a[64:67], a[112:115], v[144:147]\n"
"v_mul_f32 v13, v13, v11\n"
"v_lshlrev_b32 v56, 16, v57\n"
"v_mfma_f32_16x16x32_bf16 v[144:147], a[68:71], a[116:119], v[144:147]\n"
"v_accvgpr_read_b32 v10, a14\n"
"v_accvgpr_read_b32 v11, a15\n"
"v_mfma_f32_16x16x32_bf16 v[172:175], a[76:79], a[116:119], v[172:175]\n"
"v_mul_f32 v14, v14, v10\n"
"v_mul_f32 v15, v15, v11\n"
"v_mfma_f32_16x16x32_bf16 v[172:175], a[72:75], a[112:115], v[172:175]\n"
"v_cvt_pk_bf16_f32 v30, v12, v13\n"
"v_cvt_pk_bf16_f32 v31, v14, v15\n"
"v_mfma_f32_16x16x32_bf16 v[200:203], a[80:83], a[112:115], v[200:203]\n"
"ds_write_b64 v8, v[30:31] offset:38920\n"
"v_mfma_f32_16x16x32_bf16 v[200:203], a[84:87], a[116:119], v[200:203]\n"
"ds_read_b128 v[112:115], v2 offset:38912\n"
"v_and_b32 v81, s63, v81\n"
"v_mfma_f32_16x16x32_bf16 v[228:231], a[92:95], a[116:119], v[228:231]\n"
"ds_read_b128 a[116:119], v3 offset:23552\n"
"v_lshlrev_b32 v58, 16, v59\n"
"v_mfma_f32_16x16x32_bf16 v[228:231], a[88:91], a[112:115], v[228:231]\n"
"s_waitcnt lgkmcnt(15)\n"
"ds_read_b128 a[112:115], v3 offset:22528\n"
"v_and_b32 v57, s63, v57\n"
"v_mfma_f32_16x16x32_bf16 v[232:235], v[96:99], a[120:123], v[232:235]\n"
"ds_read_b128 v[140:143], v2 offset:39936\n"
"v_and_b32 v83, s63, v83\n"
"v_mfma_f32_16x16x32_bf16 v[232:235], v[124:127], a[124:127], v[232:235]\n"
"v_accvgpr_read_b32 v16, a192\n"
"v_accvgpr_read_b32 v17, a193\n"
"v_mfma_f32_16x16x32_bf16 v[204:207], v[60:63], a[124:127], v[204:207]\n"
"v_accvgpr_read_b32 v18, a194\n"
"v_accvgpr_read_b32 v19, a195\n"
"v_mfma_f32_16x16x32_bf16 v[204:207], v[32:35], a[120:123], v[204:207]\n"
"v_lshlrev_b32 v12, 16, v16\n"
"v_and_b32 v13, s63, v16\n"
"v_mfma_f32_16x16x32_bf16 v[176:179], v[92:95], a[120:123], v[176:179]\n"
"v_lshlrev_b32 v14, 16, v17\n"
"v_and_b32 v15, s63, v17\n"
"v_mfma_f32_16x16x32_bf16 v[176:179], v[120:123], a[124:127], v[176:179]\n"
"v_mul_f32 v12, v12, v144\n"
"v_mul_f32 v13, v13, v145\n"
"v_mfma_f32_16x16x32_bf16 v[148:151], v[116:119], a[124:127], v[148:151]\n"
"ds_read_b128 a[124:127], v4 offset:23552\n"
"v_mul_f32 v14, v14, v146\n"
"v_mfma_f32_16x16x32_bf16 v[148:151], v[88:91], a[120:123], v[148:151]\n"
"ds_read_b128 a[120:123], v4 offset:22528\n"
"v_mul_f32 v15, v15, v147\n"
"v_mfma_f32_16x16x32_bf16 v[148:151], v[100:103], a[128:131], v[148:151]\n"
"v_cvt_pk_bf16_f32 v24, v12, v13\n"
"v_cvt_pk_bf16_f32 v25, v14, v15\n"
"v_and_b32 v59, s63, v59\n"
"v_mfma_f32_16x16x32_bf16 v[148:151], v[128:131], a[132:135], v[148:151]\n"
"ds_write_b64 v7, v[24:25] offset:32768\n"
"v_mfma_f32_16x16x32_bf16 v[176:179], v[64:67], a[132:135], v[176:179]\n"
"v_lshlrev_b32 v12, 16, v18\n"
"v_and_b32 v13, s63, v18\n"
"v_lshlrev_b32 v14, 16, v19\n"
"v_mfma_f32_16x16x32_bf16 v[176:179], v[36:39], a[128:131], v[176:179]\n"
"v_and_b32 v15, s63, v19\n"
"v_mul_f32 v12, v12, v172\n"
"v_mul_f32 v13, v13, v173\n"
"v_mfma_f32_16x16x32_bf16 v[204:207], v[104:107], a[128:131], v[204:207]\n"
"s_waitcnt lgkmcnt(6)\n"
"buffer_store_dwordx4 v[112:115], v6, s[52:55], s94 offen offset:2048 nt\n"
"v_mul_f32 v14, v14, v174\n"
"v_mul_f32 v15, v15, v175\n"
"v_cvt_pk_bf16_f32 v26, v12, v13\n"
"v_cvt_pk_bf16_f32 v27, v14, v15\n"
"v_mfma_f32_16x16x32_bf16 v[204:207], v[132:135], a[132:135], v[204:207]\n"
"ds_write_b64 v7, v[26:27] offset:32776\n"
"v_mfma_f32_16x16x32_bf16 v[232:235], v[136:139], a[132:135], v[232:235]\n"
"ds_read_b128 a[132:135], v2 offset:25600\n"
"v_accvgpr_read_b32 v16, a196\n"
"v_mfma_f32_16x16x32_bf16 v[232:235], v[108:111], a[128:131], v[232:235]\n"
"ds_read_b128 a[128:131], v2 offset:24576\n"
"v_accvgpr_read_b32 v17, a197\n"
"v_mfma_f32_16x16x32_bf16 v[232:235], a[88:91], a[136:139], v[232:235]\n"
"v_accvgpr_read_b32 v18, a198\n"
"v_accvgpr_read_b32 v19, a199\n"
"v_accvgpr_read_b32 v85, a250\n"
"v_mfma_f32_16x16x32_bf16 v[232:235], a[92:95], a[140:143], v[232:235]\n"
"v_lshlrev_b32 v12, 16, v16\n"
"v_and_b32 v13, s63, v16\n"
"v_mfma_f32_16x16x32_bf16 v[204:207], a[84:87], a[140:143], v[204:207]\n"
"v_lshlrev_b32 v14, 16, v17\n"
"v_and_b32 v15, s63, v17\n"
"v_mfma_f32_16x16x32_bf16 v[204:207], a[80:83], a[136:139], v[204:207]\n"
"v_mul_f32 v12, v12, v200\n"
"v_mul_f32 v13, v13, v201\n"
"v_mul_f32 v14, v14, v202\n"
"v_mfma_f32_16x16x32_bf16 v[176:179], a[72:75], a[136:139], v[176:179]\n"
"v_mul_f32 v15, v15, v203\n"
"v_cvt_pk_bf16_f32 v28, v12, v13\n"
"v_cvt_pk_bf16_f32 v29, v14, v15\n"
"v_mfma_f32_16x16x32_bf16 v[176:179], a[76:79], a[140:143], v[176:179]\n"
"ds_write_b64 v8, v[28:29] offset:32768\n"
"v_mfma_f32_16x16x32_bf16 v[148:151], a[68:71], a[140:143], v[148:151]\n"
"s_waitcnt lgkmcnt(7)\n"
"buffer_store_dwordx4 v[140:143], v6, s[52:55], s95 offen offset:3072 nt\n"
"ds_read_b128 a[140:143], v3 offset:25600\n"
"v_lshlrev_b32 v12, 16, v18\n"
"v_and_b32 v13, s63, v18\n"
"v_lshlrev_b32 v14, 16, v19\n"
"v_mfma_f32_16x16x32_bf16 v[148:151], a[64:67], a[136:139], v[148:151]\n"
"ds_read_b128 a[136:139], v3 offset:24576\n"
"v_and_b32 v15, s63, v19\n"
"v_accvgpr_read_b32 v87, a251\n"
"v_mfma_f32_16x16x32_bf16 v[152:155], v[88:91], a[144:147], v[152:155]\n"
"v_mul_f32 v12, v12, v228\n"
"v_mul_f32 v13, v13, v229\n"
"v_mfma_f32_16x16x32_bf16 v[152:155], v[116:119], a[148:151], v[152:155]\n"
"v_mul_f32 v14, v14, v230\n"
"v_mul_f32 v15, v15, v231\n"
"v_mfma_f32_16x16x32_bf16 v[180:183], v[120:123], a[148:151], v[180:183]\n"
"v_cvt_pk_bf16_f32 v30, v12, v13\n"
"v_cvt_pk_bf16_f32 v31, v14, v15\n"
"v_mfma_f32_16x16x32_bf16 v[180:183], v[92:95], a[144:147], v[180:183]\n"
"ds_write_b64 v8, v[30:31] offset:32776\n"
"v_mfma_f32_16x16x32_bf16 v[208:211], v[32:35], a[144:147], v[208:211]\n"
"ds_read_b128 v[144:147], v2 offset:32768\n"
"v_lshlrev_b32 v84, 16, v85\n"
"v_mfma_f32_16x16x32_bf16 v[208:211], v[60:63], a[148:151], v[208:211]\n"
"ds_read_b128 v[172:175], v2 offset:33792\n"
"v_accvgpr_read_b32 v16, a200\n"
"v_mfma_f32_16x16x32_bf16 v[236:239], v[124:127], a[148:151], v[236:239]\n"
"ds_read_b128 a[148:151], v4 offset:25600\n"
"v_accvgpr_read_b32 v17, a201\n"
"v_mfma_f32_16x16x32_bf16 v[236:239], v[96:99], a[144:147], v[236:239]\n"
"ds_read_b128 a[144:147], v4 offset:24576\n"
"v_accvgpr_read_b32 v18, a202\n"
"v_lshlrev_b32 v86, 16, v87\n"
"v_mfma_f32_16x16x32_bf16 v[236:239], v[108:111], a[152:155], v[236:239]\n"
"v_accvgpr_read_b32 v19, a203\n"
"v_lshlrev_b32 v12, 16, v16\n"
"v_mfma_f32_16x16x32_bf16 v[236:239], v[136:139], a[156:159], v[236:239]\n"
"v_and_b32 v13, s63, v16\n"
"v_lshlrev_b32 v14, 16, v17\n"
"v_mfma_f32_16x16x32_bf16 v[208:211], v[132:135], a[156:159], v[208:211]\n"
"v_and_b32 v15, s63, v17\n"
"v_mul_f32 v12, v12, v148\n"
"v_mfma_f32_16x16x32_bf16 v[208:211], v[104:107], a[152:155], v[208:211]\n"
"v_mul_f32 v13, v13, v149\n"
"v_mul_f32 v14, v14, v150\n"
"v_and_b32 v85, s63, v85\n"
"v_mfma_f32_16x16x32_bf16 v[180:183], v[36:39], a[152:155], v[180:183]\n"
"v_mul_f32 v15, v15, v151\n"
"v_cvt_pk_bf16_f32 v24, v12, v13\n"
"v_cvt_pk_bf16_f32 v25, v14, v15\n"
"v_mfma_f32_16x16x32_bf16 v[180:183], v[64:67], a[156:159], v[180:183]\n"
"ds_write_b64 v7, v[24:25] offset:34816\n"
"v_mfma_f32_16x16x32_bf16 v[152:155], v[128:131], a[156:159], v[152:155]\n"
"ds_read_b128 a[156:159], v2 offset:27648\n"
"v_lshlrev_b32 v12, 16, v18\n"
"v_mfma_f32_16x16x32_bf16 v[152:155], v[100:103], a[152:155], v[152:155]\n"
"ds_read_b128 a[152:155], v2 offset:26624\n"
"v_and_b32 v13, s63, v18\n"
"v_mfma_f32_16x16x32_bf16 v[152:155], a[64:67], a[96:99], v[152:155]\n"
"v_lshlrev_b32 v14, 16, v19\n"
"v_and_b32 v15, s63, v19\n"
"v_mfma_f32_16x16x32_bf16 v[152:155], a[68:71], a[100:103], v[152:155]\n"
"v_mul_f32 v12, v12, v176\n"
"v_mul_f32 v13, v13, v177\n"
"v_and_b32 v87, s63, v87\n"
"v_mfma_f32_16x16x32_bf16 v[180:183], a[76:79], a[100:103], v[180:183]\n"
"v_mul_f32 v14, v14, v178\n"
"v_mul_f32 v15, v15, v179\n"
"v_mfma_f32_16x16x32_bf16 v[180:183], a[72:75], a[96:99], v[180:183]\n"
"v_cvt_pk_bf16_f32 v26, v12, v13\n"
"v_cvt_pk_bf16_f32 v27, v14, v15\n"
"v_mfma_f32_16x16x32_bf16 v[208:211], a[80:83], a[96:99], v[208:211]\n"
"s_waitcnt lgkmcnt(6)\n"
"buffer_store_dwordx4 v[144:147], v6, s[52:55], s80 offen offset:128 nt\n"
"ds_write_b64 v7, v[26:27] offset:34824\n"
"v_mfma_f32_16x16x32_bf16 v[208:211], a[84:87], a[100:103], v[208:211]\n"
"v_accvgpr_read_b32 v16, a204\n"
"v_accvgpr_read_b32 v17, a205\n"
"v_mfma_f32_16x16x32_bf16 v[236:239], a[92:95], a[100:103], v[236:239]\n"
"ds_read_b128 a[100:103], v3 offset:27648\n"
"v_accvgpr_read_b32 v18, a206\n"
"v_mfma_f32_16x16x32_bf16 v[236:239], a[88:91], a[96:99], v[236:239]\n"
"ds_read_b128 a[96:99], v3 offset:26624\n"
"v_accvgpr_read_b32 v19, a207\n"
"v_mfma_f32_16x16x32_bf16 v[240:243], v[96:99], a[104:107], v[240:243]\n"
"v_lshlrev_b32 v12, 16, v16\n"
"v_and_b32 v13, s63, v16\n"
"v_mfma_f32_16x16x32_bf16 v[240:243], v[124:127], a[108:111], v[240:243]\n"
"v_lshlrev_b32 v14, 16, v17\n"
"v_and_b32 v15, s63, v17\n"
"v_mfma_f32_16x16x32_bf16 v[212:215], v[60:63], a[108:111], v[212:215]\n"
"v_mul_f32 v12, v12, v204\n"
"v_mul_f32 v13, v13, v205\n"
"v_mfma_f32_16x16x32_bf16 v[212:215], v[32:35], a[104:107], v[212:215]\n"
"v_mul_f32 v14, v14, v206\n"
"v_mul_f32 v15, v15, v207\n"
"v_mfma_f32_16x16x32_bf16 v[184:187], v[92:95], a[104:107], v[184:187]\n"
"v_cvt_pk_bf16_f32 v28, v12, v13\n"
"v_cvt_pk_bf16_f32 v29, v14, v15\n"
"v_mfma_f32_16x16x32_bf16 v[184:187], v[120:123], a[108:111], v[184:187]\n"
"ds_write_b64 v8, v[28:29] offset:34816\n"
"v_mfma_f32_16x16x32_bf16 v[156:159], v[116:119], a[108:111], v[156:159]\n"
"s_waitcnt lgkmcnt(9)\n"
"buffer_store_dwordx4 v[172:175], v6, s[52:55], s81 offen offset:1152 nt\n"
"ds_read_b128 a[108:111], v4 offset:27648\n"
"v_lshlrev_b32 v12, 16, v18\n"
"v_and_b32 v13, s63, v18\n"
"v_lshlrev_b32 v14, 16, v19\n"
"v_mfma_f32_16x16x32_bf16 v[156:159], v[88:91], a[104:107], v[156:159]\n"
"ds_read_b128 a[104:107], v4 offset:26624\n"
"v_and_b32 v15, s63, v19\n"
"v_mfma_f32_16x16x32_bf16 v[156:159], v[100:103], a[112:115], v[156:159]\n"
"v_mul_f32 v12, v12, v232\n"
"v_mul_f32 v13, v13, v233\n"
"v_mfma_f32_16x16x32_bf16 v[156:159], v[128:131], a[116:119], v[156:159]\n"
"v_mul_f32 v14, v14, v234\n"
"v_mul_f32 v15, v15, v235\n"
"v_mfma_f32_16x16x32_bf16 v[184:187], v[64:67], a[116:119], v[184:187]\n"
"v_cvt_pk_bf16_f32 v30, v12, v13\n"
"v_cvt_pk_bf16_f32 v31, v14, v15\n"
"v_mfma_f32_16x16x32_bf16 v[184:187], v[36:39], a[112:115], v[184:187]\n"
"ds_write_b64 v8, v[30:31] offset:34824\n"
"v_mfma_f32_16x16x32_bf16 v[212:215], v[104:107], a[112:115], v[212:215]\n"
"ds_read_b128 v[148:151], v2 offset:34816\n"
"v_mfma_f32_16x16x32_bf16 v[212:215], v[132:135], a[116:119], v[212:215]\n"
"ds_read_b128 v[176:179], v2 offset:35840\n"
"v_accvgpr_read_b32 v16, a208\n"
"v_accvgpr_read_b32 v17, a209\n"
"v_mfma_f32_16x16x32_bf16 v[240:243], v[136:139], a[116:119], v[240:243]\n"
"ds_read_b128 a[116:119], v2 offset:29696\n"
"v_accvgpr_read_b32 v18, a210\n"
"v_mfma_f32_16x16x32_bf16 v[240:243], v[108:111], a[112:115], v[240:243]\n"
"ds_read_b128 a[112:115], v2 offset:28672\n"
"v_accvgpr_read_b32 v19, a211\n"
"v_mfma_f32_16x16x32_bf16 v[240:243], a[88:91], a[120:123], v[240:243]\n"
"v_lshlrev_b32 v12, 16, v16\n"
"v_and_b32 v13, s63, v16\n"
"v_mfma_f32_16x16x32_bf16 v[240:243], a[92:95], a[124:127], v[240:243]\n"
"v_lshlrev_b32 v14, 16, v17\n"
"v_and_b32 v15, s63, v17\n"
"v_mfma_f32_16x16x32_bf16 v[212:215], a[84:87], a[124:127], v[212:215]\n"
"v_mul_f32 v12, v12, v152\n"
"v_mul_f32 v13, v13, v153\n"
"v_mfma_f32_16x16x32_bf16 v[212:215], a[80:83], a[120:123], v[212:215]\n"
"v_mul_f32 v14, v14, v154\n"
"v_mul_f32 v15, v15, v155\n"
"v_mfma_f32_16x16x32_bf16 v[184:187], a[72:75], a[120:123], v[184:187]\n"
"v_cvt_pk_bf16_f32 v24, v12, v13\n"
"v_cvt_pk_bf16_f32 v25, v14, v15\n"
"v_mfma_f32_16x16x32_bf16 v[184:187], a[76:79], a[124:127], v[184:187]\n"
"ds_write_b64 v7, v[24:25] offset:36864\n"
"v_mfma_f32_16x16x32_bf16 v[156:159], a[68:71], a[124:127], v[156:159]\n"
"ds_read_b128 a[124:127], v3 offset:29696\n"
"v_lshlrev_b32 v12, 16, v18\n"
"v_mfma_f32_16x16x32_bf16 v[156:159], a[64:67], a[120:123], v[156:159]\n"
"ds_read_b128 a[120:123], v3 offset:28672\n"
"v_and_b32 v13, s63, v18\n"
"v_mfma_f32_16x16x32_bf16 v[160:163], v[88:91], a[128:131], v[160:163]\n"
"s_waitcnt lgkmcnt(6)\n"
"buffer_store_dwordx4 v[148:151], v6, s[52:55], s82 offen offset:2176 nt\n"
"v_lshlrev_b32 v14, 16, v19\n"
"v_and_b32 v15, s63, v19\n"
"v_mul_f32 v12, v12, v180\n"
"v_mul_f32 v13, v13, v181\n"
"v_mfma_f32_16x16x32_bf16 v[160:163], v[116:119], a[132:135], v[160:163]\n"
"v_mul_f32 v14, v14, v182\n"
"v_mul_f32 v15, v15, v183\n"
"v_mfma_f32_16x16x32_bf16 v[188:191], v[120:123], a[132:135], v[188:191]\n"
"v_cvt_pk_bf16_f32 v26, v12, v13\n"
"v_cvt_pk_bf16_f32 v27, v14, v15\n"
"v_mfma_f32_16x16x32_bf16 v[188:191], v[92:95], a[128:131], v[188:191]\n"
"ds_write_b64 v7, v[26:27] offset:36872\n"
"v_mfma_f32_16x16x32_bf16 v[216:219], v[32:35], a[128:131], v[216:219]\n"
"v_accvgpr_read_b32 v16, a212\n"
"v_accvgpr_read_b32 v17, a213\n"
"v_mfma_f32_16x16x32_bf16 v[216:219], v[60:63], a[132:135], v[216:219]\n"
"v_accvgpr_read_b32 v18, a214\n"
"v_accvgpr_read_b32 v19, a215\n"
"v_mfma_f32_16x16x32_bf16 v[244:247], v[124:127], a[132:135], v[244:247]\n"
"ds_read_b128 a[132:135], v4 offset:29696\n"
"v_lshlrev_b32 v12, 16, v16\n"
"v_mfma_f32_16x16x32_bf16 v[244:247], v[96:99], a[128:131], v[244:247]\n"
"ds_read_b128 a[128:131], v4 offset:28672\n"
"v_and_b32 v13, s63, v16\n"
"v_mfma_f32_16x16x32_bf16 v[244:247], v[108:111], a[136:139], v[244:247]\n"
"v_lshlrev_b32 v14, 16, v17\n"
"v_and_b32 v15, s63, v17\n"
"v_mfma_f32_16x16x32_bf16 v[244:247], v[136:139], a[140:143], v[244:247]\n"
"v_mul_f32 v12, v12, v208\n"
"v_mul_f32 v13, v13, v209\n"
"v_mfma_f32_16x16x32_bf16 v[216:219], v[132:135], a[140:143], v[216:219]\n"
"s_waitcnt lgkmcnt(8)\n"
"buffer_store_dwordx4 v[176:179], v6, s[52:55], s83 offen offset:3200 nt\n"
"v_mul_f32 v14, v14, v210\n"
"v_mul_f32 v15, v15, v211\n"
"v_cvt_pk_bf16_f32 v28, v12, v13\n"
"v_cvt_pk_bf16_f32 v29, v14, v15\n"
"v_mfma_f32_16x16x32_bf16 v[216:219], v[104:107], a[136:139], v[216:219]\n"
"ds_write_b64 v8, v[28:29] offset:36864\n"
"v_mfma_f32_16x16x32_bf16 v[188:191], v[36:39], a[136:139], v[188:191]\n"
"v_lshlrev_b32 v12, 16, v18\n"
"v_and_b32 v13, s63, v18\n"
"v_mfma_f32_16x16x32_bf16 v[188:191], v[64:67], a[140:143], v[188:191]\n"
"v_lshlrev_b32 v14, 16, v19\n"
"v_and_b32 v15, s63, v19\n"
"v_mfma_f32_16x16x32_bf16 v[160:163], v[128:131], a[140:143], v[160:163]\n"
"ds_read_b128 a[140:143], v2 offset:31744\n"
"v_mul_f32 v12, v12, v236\n"
"v_mfma_f32_16x16x32_bf16 v[160:163], v[100:103], a[136:139], v[160:163]\n"
"ds_read_b128 a[136:139], v2 offset:30720\n"
"v_mul_f32 v13, v13, v237\n"
"v_mul_f32 v14, v14, v238\n"
"v_mfma_f32_16x16x32_bf16 v[160:163], a[64:67], a[144:147], v[160:163]\n"
"v_mul_f32 v15, v15, v239\n"
"v_cvt_pk_bf16_f32 v30, v12, v13\n"
"v_cvt_pk_bf16_f32 v31, v14, v15\n"
"v_mfma_f32_16x16x32_bf16 v[160:163], a[68:71], a[148:151], v[160:163]\n"
"ds_write_b64 v8, v[30:31] offset:36872\n"
"v_mfma_f32_16x16x32_bf16 v[188:191], a[76:79], a[148:151], v[188:191]\n"
"ds_read_b128 v[152:155], v2 offset:36864\n"
"v_mfma_f32_16x16x32_bf16 v[188:191], a[72:75], a[144:147], v[188:191]\n"
"ds_read_b128 v[180:183], v2 offset:37888\n"
"v_accvgpr_read_b32 v16, a216\n"
"v_mfma_f32_16x16x32_bf16 v[216:219], a[80:83], a[144:147], v[216:219]\n"
"v_accvgpr_read_b32 v17, a217\n"
"v_accvgpr_read_b32 v18, a218\n"
"v_mfma_f32_16x16x32_bf16 v[216:219], a[84:87], a[148:151], v[216:219]\n"
"v_accvgpr_read_b32 v19, a219\n"
"v_lshlrev_b32 v12, 16, v16\n"
"v_mfma_f32_16x16x32_bf16 v[244:247], a[92:95], a[148:151], v[244:247]\n"
"L_st11495:\n"
"s_cbranch_execz L_retC\n"
"ds_read_b128 a[148:151], v3 offset:31744\n"
"v_and_b32 v13, s63, v16\n"
"v_lshlrev_b32 v14, 16, v17\n"
"v_mfma_f32_16x16x32_bf16 v[244:247], a[88:91], a[144:147], v[244:247]\n"
"ds_read_b128 a[144:147], v3 offset:30720\n"
"v_and_b32 v15, s63, v17\n"
"v_mfma_f32_16x16x32_bf16 v[248:251], v[96:99], a[152:155], v[248:251]\n"
"v_mul_f32 v12, v12, v156\n"
"v_mul_f32 v13, v13, v157\n"
"v_mfma_f32_16x16x32_bf16 v[248:251], v[124:127], a[156:159], v[248:251]\n"
"v_mul_f32 v14, v14, v158\n"
"v_mul_f32 v15, v15, v159\n"
"v_mfma_f32_16x16x32_bf16 v[220:223], v[60:63], a[156:159], v[220:223]\n"
"v_cvt_pk_bf16_f32 v24, v12, v13\n"
"v_cvt_pk_bf16_f32 v25, v14, v15\n"
"v_mfma_f32_16x16x32_bf16 v[220:223], v[32:35], a[152:155], v[220:223]\n"
"ds_write_b64 v7, v[24:25] offset:38912\n"
"v_mfma_f32_16x16x32_bf16 v[192:195], v[92:95], a[152:155], v[192:195]\n"
"s_waitcnt lgkmcnt(4)\n"
"buffer_store_dwordx4 v[152:155], v6, s[52:55], s84 offen offset:128 nt\n"
"v_lshlrev_b32 v12, 16, v18\n"
"v_and_b32 v13, s63, v18\n"
"v_lshlrev_b32 v14, 16, v19\n"
"v_and_b32 v15, s63, v19\n"
"v_mfma_f32_16x16x32_bf16 v[192:195], v[120:123], a[156:159], v[192:195]\n"
"v_mul_f32 v12, v12, v184\n"
"v_mul_f32 v13, v13, v185\n"
"v_mfma_f32_16x16x32_bf16 v[164:167], v[116:119], a[156:159], v[164:167]\n"
"ds_read_b128 a[156:159], v4 offset:31744\n"
"v_mul_f32 v14, v14, v186\n"
"v_mfma_f32_16x16x32_bf16 v[164:167], v[88:91], a[152:155], v[164:167]\n"
"ds_read_b128 a[152:155], v4 offset:30720\n"
"v_mul_f32 v15, v15, v187\n"
"v_mfma_f32_16x16x32_bf16 v[164:167], v[100:103], a[96:99], v[164:167]\n"
"v_cvt_pk_bf16_f32 v26, v12, v13\n"
"v_cvt_pk_bf16_f32 v27, v14, v15\n"
"v_mfma_f32_16x16x32_bf16 v[164:167], v[128:131], a[100:103], v[164:167]\n"
"ds_write_b64 v7, v[26:27] offset:38920\n"
"v_mfma_f32_16x16x32_bf16 v[192:195], v[64:67], a[100:103], v[192:195]\n"
"v_accvgpr_read_b32 v16, a220\n"
"v_accvgpr_read_b32 v17, a221\n"
"v_mfma_f32_16x16x32_bf16 v[192:195], v[36:39], a[96:99], v[192:195]\n"
"v_accvgpr_read_b32 v18, a222\n"
"v_accvgpr_read_b32 v19, a223\n"
"v_mfma_f32_16x16x32_bf16 v[220:223], v[104:107], a[96:99], v[220:223]\n"
"v_lshlrev_b32 v12, 16, v16\n"
"v_and_b32 v13, s63, v16\n"
"v_mfma_f32_16x16x32_bf16 v[220:223], v[132:135], a[100:103], v[220:223]\n"
"v_lshlrev_b32 v14, 16, v17\n"
"v_and_b32 v15, s63, v17\n"
"v_mfma_f32_16x16x32_bf16 v[248:251], v[136:139], a[100:103], v[248:251]\n"
"s_waitcnt lgkmcnt(6)\n"
"buffer_store_dwordx4 v[180:183], v6, s[52:55], s85 offen offset:1152 nt\n"
"v_mul_f32 v12, v12, v212\n"
"v_mul_f32 v13, v13, v213\n"
"v_mul_f32 v14, v14, v214\n"
"v_mul_f32 v15, v15, v215\n"
"v_mfma_f32_16x16x32_bf16 v[248:251], v[108:111], a[96:99], v[248:251]\n"
"v_cvt_pk_bf16_f32 v28, v12, v13\n"
"v_cvt_pk_bf16_f32 v29, v14, v15\n"
"v_mfma_f32_16x16x32_bf16 v[248:251], a[88:91], a[104:107], v[248:251]\n"
"ds_write_b64 v8, v[28:29] offset:38912\n"
"v_mfma_f32_16x16x32_bf16 v[248:251], a[92:95], a[108:111], v[248:251]\n"
"v_lshlrev_b32 v12, 16, v18\n"
"v_and_b32 v13, s63, v18\n"
"v_mfma_f32_16x16x32_bf16 v[220:223], a[84:87], a[108:111], v[220:223]\n"
"v_lshlrev_b32 v14, 16, v19\n"
"v_and_b32 v15, s63, v19\n"
"v_mfma_f32_16x16x32_bf16 v[220:223], a[80:83], a[104:107], v[220:223]\n"
"v_mul_f32 v12, v12, v240\n"
"v_mul_f32 v13, v13, v241\n"
"v_mfma_f32_16x16x32_bf16 v[192:195], a[72:75], a[104:107], v[192:195]\n"
"v_mul_f32 v14, v14, v242\n"
"v_mul_f32 v15, v15, v243\n"
"v_mfma_f32_16x16x32_bf16 v[192:195], a[76:79], a[108:111], v[192:195]\n"
"v_cvt_pk_bf16_f32 v30, v12, v13\n"
"v_cvt_pk_bf16_f32 v31, v14, v15\n"
"v_mfma_f32_16x16x32_bf16 v[164:167], a[68:71], a[108:111], v[164:167]\n"
"ds_write_b64 v8, v[30:31] offset:38920\n"
"v_mfma_f32_16x16x32_bf16 v[164:167], a[64:67], a[104:107], v[164:167]\n"
"ds_read_b128 v[156:159], v2 offset:38912\n"
"v_mfma_f32_16x16x32_bf16 v[168:171], v[88:91], a[112:115], v[168:171]\n"
"ds_read_b128 v[184:187], v2 offset:39936\n"
"v_accvgpr_read_b32 v16, a224\n"
"v_mfma_f32_16x16x32_bf16 v[168:171], v[116:119], a[116:119], v[168:171]\n"
"v_accvgpr_read_b32 v17, a225\n"
"v_accvgpr_read_b32 v18, a226\n"
"v_mfma_f32_16x16x32_bf16 v[196:199], v[120:123], a[116:119], v[196:199]\n"
"v_accvgpr_read_b32 v19, a227\n"
"v_lshlrev_b32 v12, 16, v16\n"
"v_mfma_f32_16x16x32_bf16 v[196:199], v[92:95], a[112:115], v[196:199]\n"
"v_and_b32 v13, s63, v16\n"
"v_lshlrev_b32 v14, 16, v17\n"
"v_mfma_f32_16x16x32_bf16 v[224:227], v[32:35], a[112:115], v[224:227]\n"
"v_and_b32 v15, s63, v17\n"
"v_mul_f32 v12, v12, v160\n"
"v_mfma_f32_16x16x32_bf16 v[224:227], v[60:63], a[116:119], v[224:227]\n"
"v_mul_f32 v13, v13, v161\n"
"v_mul_f32 v14, v14, v162\n"
"v_mfma_f32_16x16x32_bf16 v[252:255], v[124:127], a[116:119], v[252:255]\n"
"v_mul_f32 v15, v15, v163\n"
"v_cvt_pk_bf16_f32 v24, v12, v13\n"
"v_cvt_pk_bf16_f32 v25, v14, v15\n"
"v_mfma_f32_16x16x32_bf16 v[252:255], v[96:99], a[112:115], v[252:255]\n"
"ds_write_b64 v7, v[24:25] offset:32768\n"
"v_mfma_f32_16x16x32_bf16 v[252:255], v[108:111], a[120:123], v[252:255]\n"
"s_waitcnt lgkmcnt(2)\n"
"buffer_store_dwordx4 v[156:159], v6, s[52:55], s86 offen offset:2176 nt\n"
"v_lshlrev_b32 v12, 16, v18\n"
"v_and_b32 v13, s63, v18\n"
"v_lshlrev_b32 v14, 16, v19\n"
"v_mfma_f32_16x16x32_bf16 v[252:255], v[136:139], a[124:127], v[252:255]\n"
"v_and_b32 v15, s63, v19\n"
"v_mfma_f32_16x16x32_bf16 v[224:227], v[132:135], a[124:127], v[224:227]\n"
"v_mul_f32 v12, v12, v188\n"
"v_mul_f32 v13, v13, v189\n"
"v_mfma_f32_16x16x32_bf16 v[224:227], v[104:107], a[120:123], v[224:227]\n"
"v_mul_f32 v14, v14, v190\n"
"v_mul_f32 v15, v15, v191\n"
"v_mfma_f32_16x16x32_bf16 v[196:199], v[36:39], a[120:123], v[196:199]\n"
"v_cvt_pk_bf16_f32 v26, v12, v13\n"
"v_cvt_pk_bf16_f32 v27, v14, v15\n"
"v_mfma_f32_16x16x32_bf16 v[196:199], v[64:67], a[124:127], v[196:199]\n"
"ds_write_b64 v7, v[26:27] offset:32776\n"
"v_mfma_f32_16x16x32_bf16 v[168:171], v[128:131], a[124:127], v[168:171]\n"
"v_accvgpr_read_b32 v16, a228\n"
"v_accvgpr_read_b32 v17, a229\n"
"v_mfma_f32_16x16x32_bf16 v[168:171], v[100:103], a[120:123], v[168:171]\n"
"v_accvgpr_read_b32 v18, a230\n"
"v_accvgpr_read_b32 v19, a231\n"
"v_mfma_f32_16x16x32_bf16 v[168:171], a[64:67], a[128:131], v[168:171]\n"
"v_lshlrev_b32 v12, 16, v16\n"
"v_and_b32 v13, s63, v16\n"
"v_mfma_f32_16x16x32_bf16 v[168:171], a[68:71], a[132:135], v[168:171]\n"
"v_lshlrev_b32 v14, 16, v17\n"
"v_and_b32 v15, s63, v17\n"
"v_mfma_f32_16x16x32_bf16 v[196:199], a[76:79], a[132:135], v[196:199]\n"
"s_waitcnt lgkmcnt(2)\n"
"buffer_store_dwordx4 v[184:187], v6, s[52:55], s87 offen offset:3200 nt\n"
"v_mul_f32 v12, v12, v216\n"
"v_mul_f32 v13, v13, v217\n"
"v_mul_f32 v14, v14, v218\n"
"v_mul_f32 v15, v15, v219\n"
"v_mfma_f32_16x16x32_bf16 v[196:199], a[72:75], a[128:131], v[196:199]\n"
"v_cvt_pk_bf16_f32 v28, v12, v13\n"
"v_cvt_pk_bf16_f32 v29, v14, v15\n"
"v_mfma_f32_16x16x32_bf16 v[224:227], a[80:83], a[128:131], v[224:227]\n"
"ds_write_b64 v8, v[28:29] offset:32768\n"
"v_mfma_f32_16x16x32_bf16 v[224:227], a[84:87], a[132:135], v[224:227]\n"
"v_lshlrev_b32 v12, 16, v18\n"
"v_and_b32 v13, s63, v18\n"
"v_mfma_f32_16x16x32_bf16 v[252:255], a[92:95], a[132:135], v[252:255]\n"
"v_lshlrev_b32 v14, 16, v19\n"
"v_and_b32 v15, s63, v19\n"
"v_mfma_f32_16x16x32_bf16 v[252:255], a[88:91], a[128:131], v[252:255]\n"
"v_mul_f32 v12, v12, v244\n"
"v_mul_f32 v13, v13, v245\n"
"v_mfma_f32_16x16x32_bf16 a[28:31], v[96:99], a[136:139], a[28:31]\n"
"v_mul_f32 v14, v14, v246\n"
"v_mul_f32 v15, v15, v247\n"
"v_mfma_f32_16x16x32_bf16 a[28:31], v[124:127], a[140:143], a[28:31]\n"
"v_cvt_pk_bf16_f32 v30, v12, v13\n"
"v_cvt_pk_bf16_f32 v31, v14, v15\n"
"v_mfma_f32_16x16x32_bf16 a[24:27], v[60:63], a[140:143], a[24:27]\n"
"ds_write_b64 v8, v[30:31] offset:32776\n"
"v_mfma_f32_16x16x32_bf16 a[24:27], v[32:35], a[136:139], a[24:27]\n"
"ds_read_b128 v[160:163], v2 offset:32768\n"
"v_mfma_f32_16x16x32_bf16 a[20:23], v[92:95], a[136:139], a[20:23]\n"
"ds_read_b128 v[188:191], v2 offset:33792\n"
"v_mfma_f32_16x16x32_bf16 a[20:23], v[120:123], a[140:143], a[20:23]\n"
"v_mul_f32 v12, v40, v164\n"
"v_mul_f32 v13, v41, v165\n"
"v_mfma_f32_16x16x32_bf16 a[16:19], v[116:119], a[140:143], a[16:19]\n"
"v_mul_f32 v14, v42, v166\n"
"v_mul_f32 v15, v43, v167\n"
"v_mfma_f32_16x16x32_bf16 a[16:19], v[88:91], a[136:139], a[16:19]\n"
"v_cvt_pk_bf16_f32 v24, v12, v13\n"
"v_cvt_pk_bf16_f32 v25, v14, v15\n"
"v_mfma_f32_16x16x32_bf16 a[16:19], v[100:103], a[144:147], a[16:19]\n"
"ds_write_b64 v7, v[24:25] offset:34816\n"
"v_mfma_f32_16x16x32_bf16 a[16:19], v[128:131], a[148:151], a[16:19]\n"
"v_mul_f32 v12, v68, v192\n"
"v_mul_f32 v13, v69, v193\n"
"v_mfma_f32_16x16x32_bf16 a[20:23], v[64:67], a[148:151], a[20:23]\n"
"v_mul_f32 v14, v70, v194\n"
"v_mul_f32 v15, v71, v195\n"
"v_mfma_f32_16x16x32_bf16 a[20:23], v[36:39], a[144:147], a[20:23]\n"
"v_cvt_pk_bf16_f32 v26, v12, v13\n"
"v_cvt_pk_bf16_f32 v27, v14, v15\n"
"v_mfma_f32_16x16x32_bf16 a[24:27], v[104:107], a[144:147], a[24:27]\n"
"s_waitcnt lgkmcnt(2)\n"
"buffer_store_dwordx4 v[160:163], v6, s[52:55], s88 offen offset:128 nt\n"
"ds_write_b64 v7, v[26:27] offset:34824\n"
"v_mfma_f32_16x16x32_bf16 a[24:27], v[132:135], a[148:151], a[24:27]\n"
"v_mul_f32 v12, v44, v220\n"
"v_mfma_f32_16x16x32_bf16 a[28:31], v[136:139], a[148:151], a[28:31]\n"
"v_mul_f32 v13, v45, v221\n"
"v_mfma_f32_16x16x32_bf16 a[28:31], v[108:111], a[144:147], a[28:31]\n"
"v_mul_f32 v14, v46, v222\n"
"v_mfma_f32_16x16x32_bf16 a[28:31], a[88:91], a[152:155], a[28:31]\n"
"v_mul_f32 v15, v47, v223\n"
"v_mfma_f32_16x16x32_bf16 a[28:31], a[92:95], a[156:159], a[28:31]\n"
"v_cvt_pk_bf16_f32 v28, v12, v13\n"
"v_cvt_pk_bf16_f32 v29, v14, v15\n"
"v_mfma_f32_16x16x32_bf16 a[24:27], a[84:87], a[156:159], a[24:27]\n"
"ds_write_b64 v8, v[28:29] offset:34816\n"
"v_mfma_f32_16x16x32_bf16 a[24:27], a[80:83], a[152:155], a[24:27]\n"
"v_mul_f32 v12, v72, v248\n"
"v_mul_f32 v13, v73, v249\n"
"v_mfma_f32_16x16x32_bf16 a[20:23], a[72:75], a[152:155], a[20:23]\n"
"s_add_u32 s62, s62, 1\n"
"v_mul_f32 v14, v74, v250\n"
"v_mul_f32 v15, v75, v251\n"
"v_mfma_f32_16x16x32_bf16 a[20:23], a[76:79], a[156:159], a[20:23]\n"
"v_cvt_pk_bf16_f32 v30, v12, v13\n"
"v_cvt_pk_bf16_f32 v31, v14, v15\n"
"v_mfma_f32_16x16x32_bf16 a[16:19], a[68:71], a[156:159], a[16:19]\n"
"s_waitcnt lgkmcnt(3)\n"
"buffer_store_dwordx4 v[188:191], v6, s[52:55], s89 offen offset:1152 nt\n"
"ds_write_b64 v8, v[30:31] offset:34824\n"
"v_mfma_f32_16x16x32_bf16 a[16:19], a[64:67], a[152:155], a[16:19]\n"
"ds_read_b128 v[164:167], v2 offset:34816\n"
"ds_read_b128 v[192:195], v2 offset:35840\n"
"s_nop 7\n"
"v_pk_mul_f32 v[12:13], v[48:49], v[168:169]\n"
"v_pk_mul_f32 v[14:15], v[50:51], v[170:171]\n"
"v_cvt_pk_bf16_f32 v24, v12, v13\n"
"v_cvt_pk_bf16_f32 v25, v14, v15\n"
"v_pk_mul_f32 v[12:13], v[76:77], v[196:197]\n"
"v_pk_mul_f32 v[14:15], v[78:79], v[198:199]\n"
"v_cvt_pk_bf16_f32 v26, v12, v13\n"
"v_cvt_pk_bf16_f32 v27, v14, v15\n"
"ds_write_b128 v7, v[24:27] offset:36864\n"
"s_waitcnt lgkmcnt(2)\n"
"buffer_store_dwordx4 v[164:167], v6, s[52:55], s90 offen offset:2176 nt\n"
"s_waitcnt lgkmcnt(1)\n"
"buffer_store_dwordx4 v[192:195], v6, s[52:55], s91 offen offset:3200 nt\n"
"v_pk_mul_f32 v[12:13], v[52:53], v[224:225]\n"
"v_pk_mul_f32 v[14:15], v[54:55], v[226:227]\n"
"v_cvt_pk_bf16_f32 v28, v12, v13\n"
"v_cvt_pk_bf16_f32 v29, v14, v15\n"
"v_pk_mul_f32 v[12:13], v[80:81], v[252:253]\n"
"v_pk_mul_f32 v[14:15], v[82:83], v[254:255]\n"
"v_cvt_pk_bf16_f32 v30, v12, v13\n"
"v_cvt_pk_bf16_f32 v31, v14, v15\n"
"ds_write_b128 v8, v[28:31] offset:36864\n"
"ds_read_b128 v[168:171], v2 offset:36864\n"
"ds_read_b128 v[196:199], v2 offset:37888\n"
"v_accvgpr_read_b32 v10, a16\n"
"v_accvgpr_read_b32 v11, a17\n"
"v_mul_f32 v12, v56, v10\n"
"v_mul_f32 v13, v57, v11\n"
"v_accvgpr_read_b32 v10, a18\n"
"v_accvgpr_read_b32 v11, a19\n"
"v_mul_f32 v14, v58, v10\n"
"v_mul_f32 v15, v59, v11\n"
"v_cvt_pk_bf16_f32 v24, v12, v13\n"
"v_cvt_pk_bf16_f32 v25, v14, v15\n"
"v_accvgpr_read_b32 v10, a20\n"
"v_accvgpr_read_b32 v11, a21\n"
"v_mul_f32 v12, v84, v10\n"
"v_mul_f32 v13, v85, v11\n"
"v_accvgpr_read_b32 v10, a22\n"
"v_accvgpr_read_b32 v11, a23\n"
"v_mul_f32 v14, v86, v10\n"
"v_mul_f32 v15, v87, v11\n"
"v_cvt_pk_bf16_f32 v26, v12, v13\n"
"v_cvt_pk_bf16_f32 v27, v14, v15\n"
"ds_write_b128 v7, v[24:27] offset:38912\n"
"s_waitcnt lgkmcnt(2)\n"
"buffer_store_dwordx4 v[168:171], v6, s[52:55], s92 offen offset:128 nt\n"
"s_waitcnt lgkmcnt(1)\n"
"buffer_store_dwordx4 v[196:199], v6, s[52:55], s93 offen offset:1152 nt\n"
"v_accvgpr_read_b32 v16, a252\n"
"v_accvgpr_read_b32 v17, a253\n"
"v_accvgpr_read_b32 v18, a254\n"
"v_accvgpr_read_b32 v19, a255\n"
"v_lshlrev_b32 v12, 16, v16\n"
"v_and_b32 v13, s63, v16\n"
"v_lshlrev_b32 v14, 16, v17\n"
"v_and_b32 v15, s63, v17\n"
"v_accvgpr_read_b32 v10, a24\n"
"v_accvgpr_read_b32 v11, a25\n"
"v_mul_f32 v12, v12, v10\n"
"v_mul_f32 v13, v13, v11\n"
"v_accvgpr_read_b32 v10, a26\n"
"v_accvgpr_read_b32 v11, a27\n"
"v_mul_f32 v14, v14, v10\n"
"v_mul_f32 v15, v15, v11\n"
"v_cvt_pk_bf16_f32 v28, v12, v13\n"
"v_cvt_pk_bf16_f32 v29, v14, v15\n"
"v_lshlrev_b32 v12, 16, v18\n"
"v_and_b32 v13, s63, v18\n"
"v_lshlrev_b32 v14, 16, v19\n"
"v_and_b32 v15, s63, v19\n"
"v_accvgpr_read_b32 v10, a28\n"
"v_accvgpr_read_b32 v11, a29\n"
"v_mul_f32 v12, v12, v10\n"
"v_mul_f32 v13, v13, v11\n"
"v_accvgpr_read_b32 v10, a30\n"
"v_accvgpr_read_b32 v11, a31\n"
"v_mul_f32 v14, v14, v10\n"
"v_mul_f32 v15, v15, v11\n"
"v_cvt_pk_bf16_f32 v30, v12, v13\n"
"v_cvt_pk_bf16_f32 v31, v14, v15\n"
"ds_write_b128 v8, v[28:31] offset:38912\n"
"ds_read_b128 v[224:227], v2 offset:38912\n"
"ds_read_b128 v[252:255], v2 offset:39936\n"
"s_waitcnt lgkmcnt(1)\n"
"buffer_store_dwordx4 v[224:227], v6, s[52:55], s94 offen offset:2176 nt\n"
"s_waitcnt lgkmcnt(0)\n"
"buffer_store_dwordx4 v[252:255], v6, s[52:55], s95 offen offset:3200 nt\n"
"s_or_b32 s66, %26, %16\n"
"s_cbranch_scc1 L_end\n"
"v_mov_b32 v10, s78\n"
"v_mov_b32 v11, s79\n"
"s_mov_b32 s66, 139264\n"
"buffer_store_dwordx2 v[10:11], off, s[56:59], s66 sc1\n"
"s_branch L_end\n"
"L_slow0:\n"
"s_cmp_eq_u32 s68, 0\n"
"s_cbranch_scc1 L_back0\n"
"s_sub_u32 s68, s68, 1\n"
"s_sleep 2\n"
"buffer_load_dwordx2 v[8:9], off, s[56:59], %14 sc1\n"
"s_waitcnt vmcnt(0)\n"
"v_subrev_u32 v8, s65, v8\n"
"v_xor_b32 v9, s79, v9\n"
"v_or_b32 v8, v8, v9\n"
"v_cmp_lt_u32 vcc, 15, v8\n"
"s_nop 4\n"
"s_cbranch_vccnz L_slow0\n"
"s_branch L_back0\n"
"L_slow1:\n"
"s_cmp_eq_u32 s68, 0\n"
"s_cbranch_scc1 L_back1\n"
"s_sub_u32 s68, s68, 1\n"
"s_sleep 2\n"
"buffer_load_dwordx2 v[8:9], off, s[56:59], %14 sc1\n"
"s_waitcnt vmcnt(0)\n"
"v_subrev_u32 v8, s65, v8\n"
"v_xor_b32 v9, s79, v9\n"
"v_or_b32 v8, v8, v9\n"
"v_cmp_lt_u32 vcc, 15, v8\n"
"s_nop 4\n"
"s_cbranch_vccnz L_slow1\n"
"s_branch L_back1\n"
"L_slow2:\n"
"s_cmp_eq_u32 s68, 0\n"
"s_cbranch_scc1 L_back2\n"
"s_sub_u32 s68, s68, 1\n"
"s_sleep 2\n"
"buffer_load_dwordx4 v[12:15], v0, s[56:59], %25 offen sc1\n"
"s_waitcnt vmcnt(0)\n"
"v_subrev_u32 v12, s67, v12\n"
"v_xor_b32 v13, s79, v13\n"
"v_subrev_u32 v14, s67, v14\n"
"v_xor_b32 v15, s79, v15\n"
"v_or_b32 v12, v12, v14\n"
"v_or_b32 v13, v13, v15\n"
"v_or_b32 v12, v12, v13\n"
"v_cmp_lt_u32 vcc, 15, v12\n"
"s_nop 4\n"
"s_cbranch_vccnz L_slow2\n"
"s_branch L_back2\n"
"L_slow3:\n"
"s_cmp_eq_u32 s68, 0\n"
"s_cbranch_scc1 L_back3\n"
"s_sub_u32 s68, s68, 1\n"
"s_sleep 2\n"
"buffer_load_dwordx4 v[24:27], v0, s[56:59], %25 offen sc1\n"
"s_waitcnt vmcnt(0)\n"
"v_subrev_u32 v24, s70, v24\n"
"v_xor_b32 v25, s79, v25\n"
"v_subrev_u32 v26, s70, v26\n"
"v_xor_b32 v27, s79, v27\n"
"v_or_b32 v24, v24, v26\n"
"v_or_b32 v25, v25, v27\n"
"v_or_b32 v24, v24, v25\n"
"v_cmp_lt_u32 vcc, 15, v24\n"
"s_nop 4\n"
"s_cbranch_vccnz L_slow3\n"
"s_branch L_back3\n"
"L_slow4:\n"
"s_cmp_eq_u32 s68, 0\n"
"s_cbranch_scc1 L_back4\n"
"s_sub_u32 s68, s68, 1\n"
"s_sleep 2\n"
"buffer_load_dwordx2 v[8:9], off, s[56:59], %14 sc1\n"
"s_waitcnt vmcnt(0)\n"
"v_subrev_u32 v8, s65, v8\n"
"v_xor_b32 v9, s79, v9\n"
"v_or_b32 v8, v8, v9\n"
"v_cmp_lt_u32 vcc, 15, v8\n"
"s_nop 4\n"
"s_cbranch_vccnz L_slow4\n"
"s_branch L_back4\n"
"L_slow7:\n"
"s_cmp_eq_u32 s68, 0\n"
"s_cbranch_scc1 L_back7\n"
"s_sub_u32 s68, s68, 1\n"
"s_sleep 2\n"
"buffer_load_dwordx2 v[8:9], off, s[56:59], %14 sc1\n"
"s_waitcnt vmcnt(0)\n"
"v_subrev_u32 v8, s65, v8\n"
"v_xor_b32 v9, s79, v9\n"
"v_or_b32 v8, v8, v9\n"
"v_cmp_lt_u32 vcc, 15, v8\n"
"s_nop 4\n"
"s_cbranch_vccnz L_slow7\n"
"s_branch L_back7\n"
"L_headF:\n"
"L_end:\n"
"s_waitcnt vmcnt(0) lgkmcnt(0)\n"
    :: LO(wsb), HI(wsb), LO(xo), HI(xo), LO(ob), HI(ob), LO(wr), HI(wr), LO(bb), HI(bb), "s"(wo), "s"(eo), "s"(ep), "s"(fo), "s"(fp),
       "s"(ldsa), "s"(w), "s"(col), "v"(threadIdx.x), "s"(db), "s"(rs1), "s"(rs2), "s"(rd1), "s"(rn1), "s"(wfo), "s"(wfa), "s"(b)
    : "memory", "scc", "vcc",
"v1","v2","v3","v4","v5","v6","v7","v8","v9","v10","v11","v12","v13","v14","v15","v16","v17","v18","v19","v20","v21","v22","v23","v24","v25","v26","v27","v28","v29","v30","v31","v32","v33","v34","v35","v36","v37","v38","v39","v40","v41","v42","v43","v44","v45","v46","v47","v48","v49","v50","v51","v52","v53","v54","v55","v56","v57","v58","v59","v60","v61","v62","v63","v64","v65","v66","v67","v68","v69","v70","v71","v72","v73","v74","v75","v76","v77","v78","v79","v80","v81","v82","v83","v84","v85","v86","v87","v88","v89","v90","v91","v92","v93","v94","v95","v96","v97","v98","v99","v100","v101","v102","v103","v104","v105","v106","v107","v108","v109","v110","v111","v112","v113","v114","v115","v116","v117","v118","v119","v120","v121","v122","v123","v124","v125","v126","v127","v128","v129","v130","v131","v132","v133","v134","v135","v136","v137","v138","v139","v140","v141","v142","v143","v144","v145","v146","v147","v148","v149","v150","v151","v152","v153","v154","v155","v156","v157","v158","v159","v160","v161","v162","v163","v164","v165","v166","v167","v168","v169","v170","v171","v172","v173","v174","v175","v176","v177","v178","v179","v180","v181","v182","v183","v184","v185","v186","v187","v188","v189","v190","v191","v192","v193","v194","v195","v196","v197","v198","v199","v200","v201","v202","v203","v204","v205","v206","v207","v208","v209","v210","v211","v212","v213","v214","v215","v216","v217","v218","v219","v220","v221","v222","v223","v224","v225","v226","v227","v228","v229","v230","v231","v232","v233","v234","v235","v236","v237","v238","v239","v240","v241","v242","v243","v244","v245","v246","v247","v248","v249","v250","v251","v252","v253","v254","v255",
"a0","a1","a2","a3","a4","a5","a6","a7","a8","a9","a10","a11","a12","a13","a14","a15","a16","a17","a18","a19","a20","a21","a22","a23","a24","a25","a26","a27","a28","a29","a30","a31","a32","a33","a34","a35","a36","a37","a38","a39","a40","a41","a42","a43","a44","a45","a46","a47","a48","a49","a50","a51","a52","a53","a54","a55","a56","a57","a58","a59","a60","a61","a62","a63","a64","a65","a66","a67","a68","a69","a70","a71","a72","a73","a74","a75","a76","a77","a78","a79","a80","a81","a82","a83","a84","a85","a86","a87","a88","a89","a90","a91","a92","a93","a94","a95","a96","a97","a98","a99","a100","a101","a102","a103","a104","a105","a106","a107","a108","a109","a110","a111","a112","a113","a114","a115","a116","a117","a118","a119","a120","a121","a122","a123","a124","a125","a126","a127","a128","a129","a130","a131","a132","a133","a134","a135","a136","a137","a138","a139","a140","a141","a142","a143","a144","a145","a146","a147","a148","a149","a150","a151","a152","a153","a154","a155","a156","a157","a158","a159","a160","a161","a162","a163","a164","a165","a166","a167","a168","a169","a170","a171","a172","a173","a174","a175","a176","a177","a178","a179","a180","a181","a182","a183","a184","a185","a186","a187","a188","a189","a190","a191","a192","a193","a194","a195","a196","a197","a198","a199","a200","a201","a202","a203","a204","a205","a206","a207","a208","a209","a210","a211","a212","a213","a214","a215","a216","a217","a218","a219","a220","a221","a222","a223","a224","a225","a226","a227","a228","a229","a230","a231","a232","a233","a234","a235","a236","a237","a238","a239","a240","a241","a242","a243","a244","a245","a246","a247","a248","a249","a250","a251","a252","a253","a254","a255",
"s27","s28","s29","s30","s31","s33","s34","s35","s36","s37","s38","s39","s40","s41","s42","s43","s44","s45","s46","s47","s48","s49","s50","s51","s52","s53","s54","s55","s56","s57","s58","s59","s60","s61","s62","s63","s64","s65","s66","s67","s68","s69","s70","s71","s72","s73","s74","s75","s76","s77","s78","s79","s80","s81","s82","s83","s84","s85","s86","s87","s88","s89","s90","s91","s92","s93","s94","s95","s96","s97","s98","s99");
}

extern "C" void kernel_launch(void* const* d_in, const int* in_sizes, int n_in,
                              void* d_out, int out_size, void* d_ws, size_t ws_size,
                              hipStream_t stream) {
  const unsigned short* x0 = (const unsigned short*)d_in[0];
  const unsigned short* ws = (const unsigned short*)d_in[1];
  const unsigned short* bs = (const unsigned short*)d_in[2];
  dcn_cross_kernel<<<256, 256, 0, stream>>>(x0, ws, bs, (unsigned char*)d_ws, (unsigned short*)d_out);
}
```

```cpp
#include <hip/hip_runtime.h>


constexpr int kD = 1024;
constexpr unsigned kWFlagOff = 131072, kWOff = 1u << 20, kWStream = 200 * 8192, kXchOff = 32u << 20, kDbgOff = 224u << 20;

__device__ __forceinline__ unsigned unit_src(unsigned v, unsigned h, unsigned w) {
  const unsigned l = v >> 5, u = v & 31;
  const unsigned split = ((0xff0000ffu >> u) & 1) - 1;
  const unsigned part = l ? u >> 4 : 0u, g = (0xff00ff00u >> u) & 1;
  const unsigned jh = (u >> (~split & 2)) & 1, c = (u >> (split & 1)) ^ (~split & (0u - jh));
  const unsigned kc = 8 * (h ^ part) + 2 * ((w + c) & 3) + g;
  return (l * 1024 + 64 * kc) * 2048 + jh * 128;
}

__global__ __launch_bounds__(256, 1) __attribute__((aligned(4096)))
void dcn_cross_kernel(const unsigned short* __restrict__ x0, const unsigned short* __restrict__ ws, const unsigned short* __restrict__ bs,
                      unsigned char* __restrict__ wsp, unsigned short* __restrict__ out) {
  __shared__ __attribute__((aligned(16))) unsigned char lds[163840];
  __builtin_amdgcn_sched_barrier(0);
  const unsigned b = blockIdx.x;
  constexpr unsigned kPx = 1 ? 1u : 8u;
  const unsigned h = 1 ? (b & 1) : ((b >> 3) & 1), rb = 1 ? (b >> 1) : ((b >> 4) * 8 + (b & 7));
  const unsigned w = __builtin_amdgcn_readfirstlane(threadIdx.x >> 6);
  asm volatile("s_getpc_b64 s[96:97]\n" "L_pc:\n" "s_lshr_b32 s95, %0, 3\n" "s_lshl2_add_u32 s95, s95, %1\n" "s_mul_i32 s95, s95, 704\n" "s_and_b32 s97, s97, 0xffff\n" "s_mov_b32 s98, L_end-L_pc\n" "s_mov_b32 s99, 0x20000\n" "s_min_u32 s95, s95, L_end-L_pc-704\n" "v_and_b32 v9, 63, %2\n" "v_min_u32 v9, 10, v9\n" "v_lshlrev_b32 v9, 6, v9\n" "buffer_load_dword a252, v9, s[96:99], s95 offen\n"
    :: "s"(b), "s"(w), "v"(threadIdx.x) : "memory", "scc", "v9", "a252", "s95", "s96", "s97", "s98", "s99");
  __builtin_amdgcn_sched_barrier(0);
  unsigned col = (512 * h + 128 * w) * 2, rows = rb * 128 * kD * 2, oofs = rows + col;
  unsigned wo = (h * 4 + w) * kWStream, eo = kXchOff + b * 524288 + w * 32768, ep = kXchOff + (b ^ kPx) * 524288 + w * 32768;
  unsigned fo = b * 512 + w * 128, fp = (b ^ kPx) * 512 + w * 128;
  unsigned db = kDbgOff + (b * 4 + w) * 512;
  const unsigned v1 = 24 + (rb >> 1), v2 = 64 + ((rb >> 1) & 31), hk = rb & 1, h2 = rb >> 6;
  unsigned rs1 = unit_src(v1, 0, w) + hk * 65536, rd1 = v1 * 8192 + hk * 1024;
  unsigned rs2 = unit_src(v2, h2, w) + hk * 65536 + h2 * 1024 + h * ((4u << 20) - 1024), rn1 = v2 * 8192 + hk * 1024 + h2 * 4 * kWStream;
  unsigned wfa = kWFlagOff + w * 1024, wfo = wfa + h * 4096 + rb * 8;
  asm volatile("" : "+s"(col), "+s"(rows), "+s"(oofs), "+s"(wo), "+s"(eo), "+s"(ep), "+s"(fo), "+s"(fp), "+s"(db), "+s"(rs1), "+s"(rd1), "+s"(rs2), "+s"(rn1), "+s"(wfa), "+s"(wfo));
  __builtin_amdgcn_sched_barrier(0);
  const unsigned long long wsb = (unsigned long long)wsp, bb = (unsigned long long)bs;
  const unsigned long long xo = (unsigned long long)x0 + rows, ob = (unsigned long long)out + oofs, wr = (unsigned long long)ws + col;
  wo += (unsigned)wsb > ~(15u << 20) ? 16u << 20 : kWOff;
  const unsigned xa = (0u - (unsigned)wsb) & 0x1fffffu;
  eo += xa; ep += xa;
  const unsigned ldsa = (unsigned)(size_t)(__attribute__((address_space(3))) unsigned char*)lds;
#define LO(x) "s"((unsigned)(x))
#define HI(x) "s"((unsigned)((x) >> 32))
  asm volatile(
"L_main_entry:\n"
"v_and_b32 v12, 63, %18\n"
"v_lshrrev_b32 v13, 3, v12\n"
"v_and_b32 v15, 7, v12\n"
"v_lshlrev_b32 v10, 11, v13\n"
"s_andn2_b32 s60, 1024, %17\n"
"s_lshl_b32 s75, s60, 10\n"
"s_mov_b32 s52, %6\n"
"s_and_b32 s53, %7, 0xffff\n"
"s_mov_b32 s54, 12581120\n"
"s_mov_b32 s55, 0x20000\n"
"v_xor_b32 v6, v15, v13\n"
"v_lshl_add_u32 v6, v6, 4, v10\n"
"s_mov_b32 s48, %2\n"
"s_and_b32 s49, %3, 0xffff\n"
"s_mov_b32 s50, 65536\n"
"s_mov_b32 s51, 0x20000\n"
"s_lshl_b32 s66, %16, 16\n"
"s_add_u32 s48, s48, s66\n"
"s_addc_u32 s49, s49, 0\n"
"s_lshl_b32 s72, %16, 12\n"
"s_add_u32 s72, s72, %15\n"
"s_mul_i32 s73, %16, 40960\n"
"s_add_u32 s64, s73, %15\n"
"v_lshrrev_b32 v11, 2, v12\n"
"v_and_b32 v11, 4, v11\n"
"v_xor_b32 v11, v11, v15\n"
"v_lshl_add_u32 v11, v11, 4, v10\n"
"s_bitcmp1_b32 %26, 0\n"
"s_cbranch_scc0 L_fa\n"
"L_y:\n"
"s_add_u32 m0, s72, 16384\n"
"s_add_u32 s67, s60, 0\n"
"buffer_load_dwordx4 v6, s[48:51], s67 offen offset:0  lds\n"
"s_add_u32 s67, s60, 15360\n"
"buffer_load_dwordx4 v6, s[48:51], s67 offen offset:1024  lds\n"
"s_add_u32 s67, s60, 30720\n"
"buffer_load_dwordx4 v6, s[48:51], s67 offen offset:2048  lds\n"
"s_add_u32 s67, s60, 46080\n"
"buffer_load_dwordx4 v6, s[48:51], s67 offen offset:3072  lds\n"
"s_add_u32 m0, s64, 0\n"
"s_add_u32 s67, s75, 0\n"
"buffer_load_dwordx4 v11, s[52:55], s67 offen offset:0  lds\n"
"s_add_u32 s67, s75, 15360\n"
"buffer_load_dwordx4 v11, s[52:55], s67 offen offset:1024  lds\n"
"s_add_u32 s67, s75, 30720\n"
"buffer_load_dwordx4 v11, s[52:55], s67 offen offset:2048  lds\n"
"s_add_u32 s67, s75, 46080\n"
"buffer_load_dwordx4 v11, s[52:55], s67 offen offset:3072  lds\n"
"s_add_u32 m0, s64, 4096\n"
"s_add_u32 s67, s75, 65536\n"
"buffer_load_dwordx4 v11, s[52:55], s67 offen offset:0  lds\n"
"s_add_u32 s67, s75, 80896\n"
"buffer_load_dwordx4 v11, s[52:55], s67 offen offset:1024  lds\n"
"s_add_u32 s67, s75, 96256\n"
"buffer_load_dwordx4 v11, s[52:55], s67 offen offset:2048  lds\n"
"s_add_u32 s67, s75, 111616\n"
"buffer_load_dwordx4 v11, s[52:55], s67 offen offset:3072  lds\n"
"s_lshr_b32 s65, %26, 4\n"
"s_lshl_b32 s65, s65, 2\n"
"s_add_u32 s65, s65, %16\n"
"s_lshl_b32 s65, s65, 11\n"
"s_lshl_b32 s67, %16, 8\n"
"s_sub_u32 s65, s65, s67\n"
"v_and_b32 v14, 1, v13\n"
"v_lshlrev_b32 v14, 17, v14\n"
"v_lshl_add_u32 v14, v15, 7, v14\n"
"v_and_b32 v14, 0xfffffdff, v14\n"
"s_lshl_b32 s67, %26, 6\n"
"s_and_b32 s67, s67, 512\n"
"s_add_u32 s65, s65, s67\n"
"v_lshlrev_b32 v0, 4, v12\n"
"s_mov_b32 s56, %0\n"
"s_and_b32 s57, %1, 0xffff\n"
"s_mov_b32 s58, 171966464\n"
"s_mov_b32 s59, 0x20000\n"
"v_add_u32 v1, 4096, v0\n"
"s_add_u32 s36, s56, %10\n"
"s_addc_u32 s37, s57, 0\n"
"s_mov_b32 s38, 1572864\n"
"s_mov_b32 s39, 0x20000\n"
"s_add_u32 s34, s36, 200704\n"
"s_mov_b32 s35, s37\n"
"v_and_b32 v13, 15, v12\n"
"v_lshrrev_b32 v10, 4, v12\n"
"v_xor_b32 v15, v15, v10\n"
"v_lshlrev_b32 v7, 7, v13\n"
"v_lshl_add_u32 v7, v15, 4, v7\n"
"v_add_u32 v7, s64, v7\n"
"v_xor_b32 v8, 64, v7\n"
"v_bfe_u32 v13, v12, 2, 2\n"
"v_and_b32 v15, 3, v12\n"
"v_bfe_u32 v9, v12, 3, 1\n"
"v_lshl_add_u32 v15, v9, 2, v15\n"
"v_lshl_add_u32 v13, v10, 3, v13\n"
"v_lshlrev_b32 v13, 7, v13\n"
"v_lshl_add_u32 v9, v15, 4, v13\n"
"v_add_u32 v9, s64, v9\n"
"v_xor_b32 v15, 64, v9\n"
"s_mov_b32 s40, %2\n"
"s_and_b32 s41, %3, 0xffff\n"
"s_mov_b32 s42, 260352\n"
"s_mov_b32 s43, 0x20000\n"
"s_add_u32 s40, s40, %17\n"
"s_addc_u32 s41, s41, 0\n"
"s_mov_b32 s80, 0\n"
"s_mov_b32 s81, 15360\n"
"s_mov_b32 s82, 30720\n"
"s_mov_b32 s83, 46080\n"
"s_mov_b32 s84, 65536\n"
"s_mov_b32 s85, 80896\n"
"s_mov_b32 s86, 96256\n"
"s_mov_b32 s87, 111616\n"
"s_mov_b32 s88, 131072\n"
"s_mov_b32 s89, 146432\n"
"s_mov_b32 s90, 161792\n"
"s_mov_b32 s91, 177152\n"
"s_mov_b32 s92, 196608\n"
"s_mov_b32 s93, 211968\n"
"s_mov_b32 s94, 227328\n"
"s_mov_b32 s95, 242688\n"
"s_mov_b32 s62, 0\n"
"s_mov_b32 s63, 0xffff0000\n"
"s_mov_b32 s68, 0x8000\n"
"s_add_u32 s66, %16, 0\n"
"s_and_b32 s66, s66, 3\n"
"s_mul_i32 s66, s66, 40960\n"
"s_add_u32 s66, s66, %15\n"
"v_add_u32 v2, s66, v0\n"
"s_add_u32 s66, %16, 1\n"
"s_and_b32 s66, s66, 3\n"
"s_mul_i32 s66, s66, 40960\n"
"s_add_u32 s66, s66, %15\n"
"v_add_u32 v3, s66, v0\n"
"s_add_u32 s66, %16, 2\n"
"s_and_b32 s66, s66, 3\n"
"s_mul_i32 s66, s66, 40960\n"
"s_add_u32 s66, s66, %15\n"
"v_add_u32 v4, s66, v0\n"
"s_add_u32 s66, %16, 3\n"
"s_and_b32 s66, s66, 3\n"
"s_mul_i32 s66, s66, 40960\n"
"s_add_u32 s66, s66, %15\n"
"v_add_u32 v5, s66, v0\n"
"s_sub_u32 s66, 0, s73\n"
"v_add_u32 v12, s66, v7\n"
"v_add_u32 v13, s66, v8\n"
"s_mov_b64 exec, 0\n"
"s_cmp_eq_u32 %16, 0\n"
"s_cbranch_scc1 L_st511\n"
"s_cmp_eq_u32 %16, 1\n"
"s_cbranch_scc1 L_st1038\n"
"s_cmp_eq_u32 %16, 2\n"
"s_cbranch_scc1 L_st1568\n"
"s_branch L_st2111\n"
"L_retA:\n"
"s_mov_b64 exec, -1\n"
"s_nop 3\n"
"s_mov_b32 s44, %8\n"
"s_and_b32 s45, %9, 0xffff\n"
"s_mov_b32 s46, 12288\n"
"s_mov_b32 s47, 0x20000\n"
"s_mov_b32 s61, %17\n"
"v_lshrrev_b32 v16, 4, v0\n"
"v_and_b32 v16, 0x30, v16\n"
"buffer_load_dwordx2 v[18:19], v16, s[44:47], s61 offen offset:0\n"
"v_lshrrev_b32 v20, 4, v0\n"
"v_and_b32 v20, 0x30, v20\n"
"buffer_load_dwordx2 v[22:23], v20, s[44:47], s61 offen offset:8\n"
"v_lshrrev_b32 v24, 4, v0\n"
"v_and_b32 v24, 0x30, v24\n"
"buffer_load_dwordx2 v[26:27], v24, s[44:47], s61 offen offset:64\n"
"v_lshrrev_b32 v28, 4, v0\n"
"v_and_b32 v28, 0x30, v28\n"
"buffer_load_dwordx2 v[30:31], v28, s[44:47], s61 offen offset:72\n"
"s_add_u32 s70, s75, 131072\n"
"s_add_u32 s70, s70, s65\n"
"buffer_load_dword a252, v14, s[52:55], s70 offen\n"
"s_add_u32 m0, s64, 8192\n"
"s_add_u32 s67, s75, 128\n"
"buffer_load_dwordx4 v11, s[52:55], s67 offen offset:0  lds\n"
"s_add_u32 s67, s75, 15488\n"
"buffer_load_dwordx4 v11, s[52:55], s67 offen offset:1024  lds\n"
"s_add_u32 s67, s75, 30848\n"
"buffer_load_dwordx4 v11, s[52:55], s67 offen offset:2048  lds\n"
"s_add_u32 s67, s75, 46208\n"
"buffer_load_dwordx4 v11, s[52:55], s67 offen offset:3072  lds\n"
"s_add_u32 m0, s64, 12288\n"
"s_add_u32 s67, s75, 65664\n"
"buffer_load_dwordx4 v11, s[52:55], s67 offen offset:0  lds\n"
"s_add_u32 s67, s75, 81024\n"
"buffer_load_dwordx4 v11, s[52:55], s67 offen offset:1024  lds\n"
"s_add_u32 s67, s75, 96384\n"
"buffer_load_dwordx4 v11, s[52:55], s67 offen offset:2048  lds\n"
"s_add_u32 s67, s75, 111744\n"
"buffer_load_dwordx4 v11, s[52:55], s67 offen offset:3072  lds\n"
"s_mov_b32 s67, 139264\n"
"buffer_load_dwordx2 v[252:253], off, s[56:59], s67 sc1\n"
"s_waitcnt vmcnt(22)\n"
"s_barrier\n"
"ds_read_b128 a[100:103], v13 offset:16384\n"
"ds_read_b128 a[96:99], v12 offset:16384\n"
"ds_read_b128 a[108:111], v13 offset:18432\n"
"ds_read_b128 a[104:107], v12 offset:18432\n"
"ds_read_b128 a[116:119], v13 offset:20480\n"
"ds_read_b128 a[112:115], v12 offset:20480\n"
"ds_read_b128 a[124:127], v13 offset:22528\n"
"ds_read_b128 a[120:123], v12 offset:22528\n"
"ds_read_b128 a[132:135], v13 offset:24576\n"
"ds_read_b128 a[128:131], v12 offset:24576\n"
"ds_read_b128 a[140:143], v13 offset:26624\n"
"ds_read_b128 a[136:139], v12 offset:26624\n"
"ds_read_b128 a[148:151], v13 offset:28672\n"
"ds_read_b128 a[144:147], v12 offset:28672\n"
"ds_read_b128 a[156:159], v13 offset:30720\n"
"ds_read_b128 a[152:155], v12 offset:30720\n"
"s_add_u32 m0, s72, 57344\n"
"s_add_u32 s67, s60, 128\n"
"buffer_load_dwordx4 v6, s[48:51], s67 offen offset:0  lds\n"
"s_add_u32 s67, s60, 15488\n"
"buffer_load_dwordx4 v6, s[48:51], s67 offen offset:1024  lds\n"
"s_add_u32 s67, s60, 30848\n"
"buffer_load_dwordx4 v6, s[48:51], s67 offen offset:2048  lds\n"
"s_add_u32 s67, s60, 46208\n"
"buffer_load_dwordx4 v6, s[48:51], s67 offen offset:3072  lds\n"
"s_waitcnt vmcnt(18)\n"
"ds_read_b64_tr_b16 a[32:33], v9 offset:0\n"
"ds_read_b64_tr_b16 a[34:35], v9 offset:512\n"
"ds_read_b64_tr_b16 a[36:37], v9 offset:4096\n"
"ds_read_b64_tr_b16 a[38:39], v9 offset:4608\n"
"ds_read_b64_tr_b16 a[40:41], v9 offset:8\n"
"ds_read_b64_tr_b16 a[42:43], v9 offset:520\n"
"ds_read_b64_tr_b16 a[44:45], v9 offset:4104\n"
"ds_read_b64_tr_b16 a[46:47], v9 offset:4616\n"
"ds_read_b64_tr_b16 a[48:49], v15 offset:0\n"
"ds_read_b64_tr_b16 a[50:51], v15 offset:512\n"
"ds_read_b64_tr_b16 a[52:53], v15 offset:4096\n"
"ds_read_b64_tr_b16 a[54:55], v15 offset:4608\n"
"ds_read_b64_tr_b16 a[56:57], v15 offset:8\n"
"ds_read_b64_tr_b16 a[58:59], v15 offset:520\n"
"ds_read_b64_tr_b16 a[60:61], v15 offset:4104\n"
"ds_read_b64_tr_b16 a[62:63], v15 offset:4616\n"
"s_waitcnt vmcnt(14)\n"
"v_lshlrev_b32 v16, 16, v18\n"
"v_and_b32 v17, s63, v18\n"
"v_lshlrev_b32 v18, 16, v19\n"
"v_and_b32 v19, s63, v19\n"
"v_add_f32 v16, 1.0, v16\n"
"v_add_f32 v17, 1.0, v17\n"
"v_add_f32 v18, 1.0, v18\n"
"v_add_f32 v19, 1.0, v19\n"
"v_lshlrev_b32 v20, 16, v22\n"
"v_and_b32 v21, s63, v22\n"
"v_lshlrev_b32 v22, 16, v23\n"
"v_and_b32 v23, s63, v23\n"
"v_add_f32 v20, 1.0, v20\n"
"v_add_f32 v21, 1.0, v21\n"
"v_add_f32 v22, 1.0, v22\n"
"v_add_f32 v23, 1.0, v23\n"
"v_lshlrev_b32 v24, 16, v26\n"
"v_and_b32 v25, s63, v26\n"
"v_lshlrev_b32 v26, 16, v27\n"
"v_and_b32 v27, s63, v27\n"
"v_add_f32 v24, 1.0, v24\n"
"v_add_f32 v25, 1.0, v25\n"
"v_add_f32 v26, 1.0, v26\n"
"v_add_f32 v27, 1.0, v27\n"
"v_lshlrev_b32 v28, 16, v30\n"
"v_and_b32 v29, s63, v30\n"
"v_lshlrev_b32 v30, 16, v31\n"
"v_and_b32 v31, s63, v31\n"
"v_add_f32 v28, 1.0, v28\n"
"v_add_f32 v29, 1.0, v29\n"
"v_add_f32 v30, 1.0, v30\n"
"v_add_f32 v31, 1.0, v31\n"
"v_accvgpr_write_b32 a0, v16\n"
"v_accvgpr_write_b32 a1, v17\n"
"v_accvgpr_write_b32 a2, v18\n"
"v_accvgpr_write_b32 a3, v19\n"
"v_accvgpr_write_b32 a4, v20\n"
"v_accvgpr_write_b32 a5, v21\n"
"v_accvgpr_write_b32 a6, v22\n"
"v_accvgpr_write_b32 a7, v23\n"
"v_accvgpr_write_b32 a8, v24\n"
"v_accvgpr_write_b32 a9, v25\n"
"v_accvgpr_write_b32 a10, v26\n"
"v_accvgpr_write_b32 a11, v27\n"
"v_accvgpr_write_b32 a12, v28\n"
"v_accvgpr_write_b32 a13, v29\n"
"v_accvgpr_write_b32 a14, v30\n"
"v_accvgpr_write_b32 a15, v31\n"
"s_bitcmp1_b32 %26, 0\n"
"s_cbranch_scc0 L_fb\n"
"L_z:\n"
"s_nop 1\n"
"s_waitcnt lgkmcnt(8)\n"
"v_mfma_f32_16x16x32_bf16 v[32:35], a[32:35], a[96:99], v[16:19]\n"
"s_add_u32 s70, s75, 393216\n"
"v_mfma_f32_16x16x32_bf16 v[32:35], a[36:39], a[100:103], v[32:35]\n"
"s_add_u32 s70, s70, s65\n"
"v_mfma_f32_16x16x32_bf16 v[60:63], a[44:47], a[100:103], v[20:23]\n"
"buffer_load_dword a252, v14, s[52:55], s70 offen\n"
"v_mfma_f32_16x16x32_bf16 v[60:63], a[40:43], a[96:99], v[60:63]\n"
"s_add_u32 m0, s64, 32768\n"
"v_mfma_f32_16x16x32_bf16 v[64:67], a[40:43], a[104:107], v[20:23]\n"
"s_add_u32 s67, s75, 131072\n"
"v_mfma_f32_16x16x32_bf16 v[64:67], a[44:47], a[108:111], v[64:67]\n"
"buffer_load_dwordx4 v11, s[52:55], s67 offen offset:0  lds\n"
"v_mfma_f32_16x16x32_bf16 v[36:39], a[36:39], a[108:111], v[16:19]\n"
"s_add_u32 s67, s75, 146432\n"
"v_mfma_f32_16x16x32_bf16 v[36:39], a[32:35], a[104:107], v[36:39]\n"
"v_mfma_f32_16x16x32_bf16 v[40:43], a[32:35], a[112:115], v[16:19]\n"
"s_waitcnt lgkmcnt(0)\n"
"buffer_load_dwordx4 v11, s[52:55], s67 offen offset:1024  lds\n"
"v_mfma_f32_16x16x32_bf16 v[40:43], a[36:39], a[116:119], v[40:43]\n"
"v_mfma_f32_16x16x32_bf16 v[68:71], a[44:47], a[116:119], v[20:23]\n"
"s_add_u32 s67, s75, 161792\n"
"v_mfma_f32_16x16x32_bf16 v[68:71], a[40:43], a[112:115], v[68:71]\n"
"v_mfma_f32_16x16x32_bf16 v[72:75], a[40:43], a[120:123], v[20:23]\n"
"buffer_load_dwordx4 v11, s[52:55], s67 offen offset:2048  lds\n"
"v_mfma_f32_16x16x32_bf16 v[72:75], a[44:47], a[124:127], v[72:75]\n"
"v_mfma_f32_16x16x32_bf16 v[44:47], a[36:39], a[124:127], v[16:19]\n"
"s_add_u32 s67, s75, 177152\n"
"v_mfma_f32_16x16x32_bf16 v[44:47], a[32:35], a[120:123], v[44:47]\n"
"v_mfma_f32_16x16x32_bf16 v[48:51], a[32:35], a[128:131], v[16:19]\n"
"buffer_load_dwordx4 v11, s[52:55], s67 offen offset:3072  lds\n"
"v_mfma_f32_16x16x32_bf16 v[48:51], a[36:39], a[132:135], v[48:51]\n"
"v_mfma_f32_16x16x32_bf16 v[76:79], a[44:47], a[132:135], v[20:23]\n"
"s_add_u32 m0, s64, 36864\n"
"v_mfma_f32_16x16x32_bf16 v[76:79], a[40:43], a[128:131], v[76:79]\n"
"v_mfma_f32_16x16x32_bf16 v[80:83], a[40:43], a[136:139], v[20:23]\n"
"s_add_u32 s67, s75, 196608\n"
"v_mfma_f32_16x16x32_bf16 v[80:83], a[44:47], a[140:143], v[80:83]\n"
"v_mfma_f32_16x16x32_bf16 v[52:55], a[36:39], a[140:143], v[16:19]\n"
"buffer_load_dwordx4 v11, s[52:55], s67 offen offset:0  lds\n"
"v_mfma_f32_16x16x32_bf16 v[52:55], a[32:35], a[136:139], v[52:55]\n"
"v_mfma_f32_16x16x32_bf16 v[56:59], a[32:35], a[144:147], v[16:19]\n"
"s_mov_b64 exec, 0\n"
"s_cmp_eq_u32 %16, 0\n"
"s_cbranch_scc1 L_st5083\n"
"s_cmp_eq_u32 %16, 1\n"
"s_cbranch_scc1 L_st5691\n"
"s_cmp_eq_u32 %16, 2\n"
"s_cbranch_scc1 L_st6226\n"
"s_branch L_st6786\n"
"L_retB:\n"
"s_mov_b64 exec, -1\n"
"s_nop 3\n"
"s_add_u32 s67, s75, 211968\n"
"v_mfma_f32_16x16x32_bf16 v[56:59], a[36:39], a[148:151], v[56:59]\n"
"v_mfma_f32_16x16x32_bf16 v[84:87], a[44:47], a[148:151], v[20:23]\n"
"buffer_load_dwordx4 v11, s[52:55], s67 offen offset:1024  lds\n"
"v_mfma_f32_16x16x32_bf16 v[84:87], a[40:43], a[144:147], v[84:87]\n"
"v_mfma_f32_16x16x32_bf16 a[4:7], a[40:43], a[152:155], a[4:7]\n"
"s_add_u32 s67, s75, 227328\n"
"v_mfma_f32_16x16x32_bf16 a[4:7], a[44:47], a[156:159], a[4:7]\n"
"v_mfma_f32_16x16x32_bf16 a[0:3], a[36:39], a[156:159], a[0:3]\n"
"buffer_load_dwordx4 v11, s[52:55], s67 offen offset:2048  lds\n"
"v_mfma_f32_16x16x32_bf16 a[0:3], a[32:35], a[152:155], a[0:3]\n"
"v_mfma_f32_16x16x32_bf16 v[88:91], a[48:51], a[96:99], v[24:27]\n"
"s_add_u32 s67, s75, 242688\n"
"v_mfma_f32_16x16x32_bf16 v[88:91], a[52:55], a[100:103], v[88:91]\n"
"v_mfma_f32_16x16x32_bf16 v[116:119], a[60:63], a[100:103], v[28:31]\n"
"v_mfma_f32_16x16x32_bf16 v[116:119], a[56:59], a[96:99], v[116:119]\n"
"buffer_load_dwordx4 v11, s[52:55], s67 offen offset:3072  lds\n"
"v_mfma_f32_16x16x32_bf16 v[120:123], a[56:59], a[104:107], v[28:31]\n"
"s_add_u32 m0, s64, 0\n"
"v_mfma_f32_16x16x32_bf16 v[120:123], a[60:63], a[108:111], v[120:123]\n"
"v_mfma_f32_16x16x32_bf16 v[92:95], a[52:55], a[108:111], v[24:27]\n"
"s_add_u32 s67, s75, 131200\n"
"v_mfma_f32_16x16x32_bf16 v[92:95], a[48:51], a[104:107], v[92:95]\n"
"v_mfma_f32_16x16x32_bf16 v[96:99], a[48:51], a[112:115], v[24:27]\n"
"buffer_load_dwordx4 v11, s[52:55], s67 offen offset:0  lds\n"
"v_mfma_f32_16x16x32_bf16 v[96:99], a[52:55], a[116:119], v[96:99]\n"
"v_mfma_f32_16x16x32_bf16 v[124:127], a[60:63], a[116:119], v[28:31]\n"
"s_add_u32 s67, s75, 146560\n"
"v_mfma_f32_16x16x32_bf16 v[124:127], a[56:59], a[112:115], v[124:127]\n"
"buffer_load_dwordx4 v11, s[52:55], s67 offen offset:1024  lds\n"
"v_mfma_f32_16x16x32_bf16 v[128:131], a[56:59], a[120:123], v[28:31]\n"
"s_waitcnt vmcnt(16)\n"
"ds_read_b64_tr_b16 a[64:65], v9 offset:8192\n"
"v_mfma_f32_16x16x32_bf16 v[128:131], a[60:63], a[124:127], v[128:131]\n"
"s_add_u32 s67, s75, 161920\n"
"ds_read_b64_tr_b16 a[66:67], v9 offset:8704\n"
"v_mfma_f32_16x16x32_bf16 v[100:103], a[52:55], a[124:127], v[24:27]\n"
"ds_read_b64_tr_b16 a[68:69], v9 offset:12288\n"
"v_mfma_f32_16x16x32_bf16 v[100:103], a[48:51], a[120:123], v[100:103]\n"
"ds_read_b64_tr_b16 a[70:71], v9 offset:12800\n"
"v_mfma_f32_16x16x32_bf16 v[104:107], a[48:51], a[128:131], v[24:27]\n"
"ds_read_b64_tr_b16 a[72:73], v9 offset:8200\n"
"v_mfma_f32_16x16x32_bf16 v[104:107], a[52:55], a[132:135], v[104:107]\n"
"ds_read_b64_tr_b16 a[74:75], v9 offset:8712\n"
"buffer_load_dwordx4 v11, s[52:55], s67 offen offset:2048  lds\n"
"v_mfma_f32_16x16x32_bf16 v[132:135], a[60:63], a[132:135], v[28:31]\n"
"ds_read_b64_tr_b16 a[76:77], v9 offset:12296\n"
"v_mfma_f32_16x16x32_bf16 v[132:135], a[56:59], a[128:131], v[132:135]\n"
"s_add_u32 s67, s75, 177280\n"
"ds_read_b64_tr_b16 a[78:79], v9 offset:12808\n"
"v_mfma_f32_16x16x32_bf16 v[136:139], a[56:59], a[136:139], v[28:31]\n"
"buffer_load_dwordx4 v11, s[52:55], s67 offen offset:3072  lds\n"
"v_mfma_f32_16x16x32_bf16 v[136:139], a[60:63], a[140:143], v[136:139]\n"
"ds_read_b64_tr_b16 a[80:81], v15 offset:8192\n"
"v_mfma_f32_16x16x32_bf16 v[108:111], a[52:55], a[140:143], v[24:27]\n"
"s_add_u32 m0, s64, 4096\n"
"ds_read_b64_tr_b16 a[82:83], v15 offset:8704\n"
"v_mfma_f32_16x16x32_bf16 v[108:111], a[48:51], a[136:139], v[108:111]\n"
"ds_read_b64_tr_b16 a[84:85], v15 offset:12288\n"
"v_mfma_f32_16x16x32_bf16 v[112:115], a[48:51], a[144:147], v[24:27]\n"
"s_add_u32 s67, s75, 196736\n"
"ds_read_b64_tr_b16 a[86:87], v15 offset:12800\n"
"v_mfma_f32_16x16x32_bf16 v[112:115], a[52:55], a[148:151], v[112:115]\n"
"ds_read_b64_tr_b16 a[88:89], v15 offset:8200\n"
"v_mfma_f32_16x16x32_bf16 v[140:143], a[60:63], a[148:151], v[28:31]\n"
"ds_read_b64_tr_b16 a[90:91], v15 offset:8712\n"
"buffer_load_dwordx4 v11, s[52:55], s67 offen offset:0  lds\n"
"v_mfma_f32_16x16x32_bf16 v[140:143], a[56:59], a[144:147], v[140:143]\n"
"ds_read_b64_tr_b16 a[92:93], v15 offset:12296\n"
"v_mfma_f32_16x16x32_bf16 a[12:15], a[56:59], a[152:155], a[12:15]\n"
"s_add_u32 s67, s75, 212096\n"
"ds_read_b64_tr_b16 a[94:95], v15 offset:12808\n"
"v_mfma_f32_16x16x32_bf16 a[12:15], a[60:63], a[156:159], a[12:15]\n"
"v_mfma_f32_16x16x32_bf16 a[8:11], a[52:55], a[156:159], a[8:11]\n"
"buffer_load_dwordx4 v11, s[52:55], s67 offen offset:1024  lds\n"
"v_mfma_f32_16x16x32_bf16 a[8:11], a[48:51], a[152:155], a[8:11]\n"
"s_waitcnt lgkmcnt(8)\n"
"v_mfma_f32_16x16x32_bf16 v[144:147], a[64:67], a[96:99], 1.0\n"
"s_add_u32 s67, s75, 227456\n"
"v_mfma_f32_16x16x32_bf16 v[144:147], a[68:71], a[100:103], v[144:147]\n"
"v_mfma_f32_16x16x32_bf16 v[172:175], a[76:79], a[100:103], 1.0\n"
"s_waitcnt vmcnt(19) lgkmcnt(0)\n"
"v_readfirstlane_b32 s78, v252\n"
"v_readfirstlane_b32 s79, v253\n"
"v_mfma_f32_16x16x32_bf16 v[172:175], a[72:75], a[96:99], v[172:175]\n"
"buffer_load_dwordx4 v11, s[52:55], s67 offen offset:2048  lds\n"
"v_mfma_f32_16x16x32_bf16 v[176:179], a[72:75], a[104:107], 1.0\n"
"v_mfma_f32_16x16x32_bf16 v[176:179], a[76:79], a[108:111], v[176:179]\n"
"s_add_u32 s67, s75, 242816\n"
"v_mfma_f32_16x16x32_bf16 v[148:151], a[68:71], a[108:111], 1.0\n"
"s_add_u32 s78, s78, 16\n"
"v_mfma_f32_16x16x32_bf16 v[148:151], a[64:67], a[104:107], v[148:151]\n"
"s_add_u32 s79, s79, 16\n"
"v_mfma_f32_16x16x32_bf16 v[152:155], a[64:67], a[112:115], 1.0\n"
"s_sub_u32 s66, 40960, s73\n"
"v_mfma_f32_16x16x32_bf16 v[152:155], a[68:71], a[116:119], v[152:155]\n"
"buffer_load_dwordx4 v11, s[52:55], s67 offen offset:3072  lds\n"
"v_mfma_f32_16x16x32_bf16 v[180:183], a[76:79], a[116:119], 1.0\n"
"s_add_u32 m0, s72, 98304\n"
"v_add_u32 v12, s66, v7\n"
"v_mfma_f32_16x16x32_bf16 v[180:183], a[72:75], a[112:115], v[180:183]\n"
"v_mfma_f32_16x16x32_bf16 v[184:187], a[72:75], a[120:123], 1.0\n"
"s_add_u32 s67, s60, 256\n"
"v_add_u32 v13, s66, v8\n"
"v_mfma_f32_16x16x32_bf16 v[184:187], a[76:79], a[124:127], v[184:187]\n"
"buffer_load_dwordx4 v6, s[48:51], s67 offen offset:0  lds\n"
"v_mfma_f32_16x16x32_bf16 v[156:159], a[68:71], a[124:127], 1.0\n"
"v_mfma_f32_16x16x32_bf16 v[156:159], a[64:67], a[120:123], v[156:159]\n"
"s_add_u32 s67, s60, 15616\n"
"v_mfma_f32_16x16x32_bf16 v[160:163], a[64:67], a[128:131], 1.0\n"
"v_mfma_f32_16x16x32_bf16 v[160:163], a[68:71], a[132:135], v[160:163]\n"
"buffer_load_dwordx4 v6, s[48:51], s67 offen offset:1024  lds\n"
"v_mfma_f32_16x16x32_bf16 v[188:191], a[76:79], a[132:135], 1.0\n"
"v_mfma_f32_16x16x32_bf16 v[188:191], a[72:75], a[128:131], v[188:191]\n"
"s_add_u32 s67, s60, 30976\n"
"v_mfma_f32_16x16x32_bf16 v[192:195], a[72:75], a[136:139], 1.0\n"
"v_mfma_f32_16x16x32_bf16 v[192:195], a[76:79], a[140:143], v[192:195]\n"
"buffer_load_dwordx4 v6, s[48:51], s67 offen offset:2048  lds\n"
"v_mfma_f32_16x16x32_bf16 v[164:167], a[68:71], a[140:143], 1.0\n"
"v_mfma_f32_16x16x32_bf16 v[164:167], a[64:67], a[136:139], v[164:167]\n"
"s_add_u32 s67, s60, 46336\n"
"v_mfma_f32_16x16x32_bf16 v[168:171], a[64:67], a[144:147], 1.0\n"
"v_mfma_f32_16x16x32_bf16 v[168:171], a[68:71], a[148:151], v[168:171]\n"
"v_mfma_f32_16x16x32_bf16 v[196:199], a[76:79], a[148:151], 1.0\n"
"buffer_load_dwordx4 v6, s[48:51], s67 offen offset:3072  lds\n"
"v_mfma_f32_16x16x32_bf16 v[196:199], a[72:75], a[144:147], v[196:199]\n"
"v_mfma_f32_16x16x32_bf16 a[20:23], a[72:75], a[152:155], 1.0\n"
"v_mfma_f32_16x16x32_bf16 a[20:23], a[76:79], a[156:159], a[20:23]\n"
"v_mfma_f32_16x16x32_bf16 a[16:19], a[68:71], a[156:159], 1.0\n"
"v_mfma_f32_16x16x32_bf16 a[16:19], a[64:67], a[152:155], a[16:19]\n"
"s_waitcnt vmcnt(21)\n"
"s_barrier\n"
"v_mfma_f32_16x16x32_bf16 v[200:203], a[80:83], a[96:99], 1.0\n"
"s_waitcnt vmcnt(12)\n"
"ds_read_b64_tr_b16 a[32:33], v9 offset:32768\n"
"v_mfma_f32_16x16x32_bf16 v[200:203], a[84:87], a[100:103], v[200:203]\n"
"s_add_u32 m0, s64, 8192\n"
"ds_read_b64_tr_b16 a[34:35], v9 offset:33280\n"
"v_mfma_f32_16x16x32_bf16 v[228:231], a[92:95], a[100:103], 1.0\n"
"ds_read_b128 a[100:103], v13 offset:16384\n"
"v_mfma_f32_16x16x32_bf16 v[228:231], a[88:91], a[96:99], v[228:231]\n"
"ds_read_b128 a[96:99], v12 offset:16384\n"
"v_mfma_f32_16x16x32_bf16 v[232:235], a[88:91], a[104:107], 1.0\n"
"ds_read_b64_tr_b16 a[36:37], v9 offset:36864\n"
"v_mfma_f32_16x16x32_bf16 v[232:235], a[92:95], a[108:111], v[232:235]\n"
"ds_read_b64_tr_b16 a[38:39], v9 offset:37376\n"
"v_mfma_f32_16x16x32_bf16 v[204:207], a[84:87], a[108:111], 1.0\n"
"ds_read_b128 a[108:111], v13 offset:18432\n"
"v_mfma_f32_16x16x32_bf16 v[204:207], a[80:83], a[104:107], v[204:207]\n"
"ds_read_b128 a[104:107], v12 offset:18432\n"
"ds_read_b64_tr_b16 a[40:41], v9 offset:32776\n"
"v_mfma_f32_16x16x32_bf16 v[208:211], a[80:83], a[112:115], 1.0\n"
"ds_read_b64_tr_b16 a[42:43], v9 offset:33288\n"
"v_mfma_f32_16x16x32_bf16 v[208:211], a[84:87], a[116:119], v[208:211]\n"
"ds_read_b64_tr_b16 a[44:45], v9 offset:36872\n"
"v_mfma_f32_16x16x32_bf16 v[236:239], a[92:95], a[116:119], 1.0\n"
"ds_read_b128 a[116:119], v13 offset:20480\n"
"v_mfma_f32_16x16x32_bf16 v[236:239], a[88:91], a[112:115], v[236:239]\n"
"ds_read_b128 a[112:115], v12 offset:20480\n"
"v_mfma_f32_16x16x32_bf16 v[240:243], a[88:91], a[120:123], 1.0\n"
"ds_read_b64_tr_b16 a[46:47], v9 offset:37384\n"
"v_mfma_f32_16x16x32_bf16 v[240:243], a[92:95], a[124:127], v[240:243]\n"
"ds_read_b64_tr_b16 a[48:49], v15 offset:32768\n"
"v_mfma_f32_16x16x32_bf16 v[212:215], a[84:87], a[124:127], 1.0\n"
"L_st511:\n"
"s_cbranch_execz L_st2657\n"
"s_add_u32 s67, s75, 262144\n"
"ds_read_b128 a[124:127], v13 offset:22528\n"
"v_mfma_f32_16x16x32_bf16 v[212:215], a[80:83], a[120:123], v[212:215]\n"
"ds_read_b128 a[120:123], v12 offset:22528\n"
"v_mfma_f32_16x16x32_bf16 v[216:219], a[80:83], a[128:131], 1.0\n"
"ds_read_b64_tr_b16 a[50:51], v15 offset:33280\n"
"v_mfma_f32_16x16x32_bf16 v[216:219], a[84:87], a[132:135], v[216:219]\n"
"ds_read_b64_tr_b16 a[52:53], v15 offset:36864\n"
"v_mfma_f32_16x16x32_bf16 v[244:247], a[92:95], a[132:135], 1.0\n"
"ds_read_b128 a[132:135], v13 offset:24576\n"
"v_mfma_f32_16x16x32_bf16 v[244:247], a[88:91], a[128:131], v[244:247]\n"
"ds_read_b128 a[128:131], v12 offset:24576\n"
"v_mfma_f32_16x16x32_bf16 v[248:251], a[88:91], a[136:139], 1.0\n"
"ds_read_b64_tr_b16 a[54:55], v15 offset:37376\n"
"v_mfma_f32_16x16x32_bf16 v[248:251], a[92:95], a[140:143], v[248:251]\n"
"ds_read_b64_tr_b16 a[56:57], v15 offset:32776\n"
"v_mfma_f32_16x16x32_bf16 v[220:223], a[84:87], a[140:143], 1.0\n"
"ds_read_b128 a[140:143], v13 offset:26624\n"
"v_mfma_f32_16x16x32_bf16 v[220:223], a[80:83], a[136:139], v[220:223]\n"
"ds_read_b128 a[136:139], v12 offset:26624\n"
"v_mfma_f32_16x16x32_bf16 v[224:227], a[80:83], a[144:147], 1.0\n"
"ds_read_b64_tr_b16 a[58:59], v15 offset:33288\n"
"v_mfma_f32_16x16x32_bf16 v[224:227], a[84:87], a[148:151], v[224:227]\n"
"ds_read_b64_tr_b16 a[60:61], v15 offset:36872\n"
"v_mfma_f32_16x16x32_bf16 v[252:255], a[92:95], a[148:151], 1.0\n"
"ds_read_b128 a[148:151], v13 offset:28672\n"
"v_mfma_f32_16x16x32_bf16 v[252:255], a[88:91], a[144:147], v[252:255]\n"
"ds_read_b128 a[144:147], v12 offset:28672\n"
"ds_read_b64_tr_b16 a[62:63], v15 offset:37384\n"
"v_mfma_f32_16x16x32_bf16 a[28:31], a[88:91], a[152:155], 1.0\n"
"v_mfma_f32_16x16x32_bf16 a[28:31], a[92:95], a[156:159], a[28:31]\n"
"v_mfma_f32_16x16x32_bf16 a[24:27], a[84:87], a[156:159], 1.0\n"
"ds_read_b128 a[156:159], v13 offset:30720\n"
"v_mfma_f32_16x16x32_bf16 a[24:27], a[80:83], a[152:155], a[24:27]\n"
"s_waitcnt lgkmcnt(15)\n"
"ds_read_b128 a[152:155], v12 offset:30720\n"
"v_mfma_f32_16x16x32_bf16 v[32:35], a[32:35], a[96:99], v[32:35]\n"
"buffer_load_dwordx4 v11, s[52:55], s67 offen offset:0  lds\n"
"v_mfma_f32_16x16x32_bf16 v[32:35], a[36:39], a[100:103], v[32:35]\n"
"s_waitcnt lgkmcnt(2)\n"
"v_mfma_f32_16x16x32_bf16 v[60:63], a[44:47], a[100:103], v[60:63]\n"
"s_add_u32 s67, s75, 277504\n"
"v_mfma_f32_16x16x32_bf16 v[60:63], a[40:43], a[96:99], v[60:63]\n"
"v_mfma_f32_16x16x32_bf16 v[64:67], a[40:43], a[104:107], v[64:67]\n"
"v_mfma_f32_16x16x32_bf16 v[64:67], a[44:47], a[108:111], v[64:67]\n"
"buffer_load_dwordx4 v11, s[52:55], s67 offen offset:1024  lds\n"
"v_mfma_f32_16x16x32_bf16 v[36:39], a[36:39], a[108:111], v[36:39]\n"
"v_mfma_f32_16x16x32_bf16 v[36:39], a[32:35], a[104:107], v[36:39]\n"
"s_add_u32 s67, s75, 292864\n"
"v_mfma_f32_16x16x32_bf16 v[40:43], a[32:35], a[112:115], v[40:43]\n"
"v_mfma_f32_16x16x32_bf16 v[40:43], a[36:39], a[116:119], v[40:43]\n"
"v_mfma_f32_16x16x32_bf16 v[68:71], a[44:47], a[116:119], v[68:71]\n"
"buffer_load_dwordx4 v11, s[52:55], s67 offen offset:2048  lds\n"
"v_mfma_f32_16x16x32_bf16 v[68:71], a[40:43], a[112:115], v[68:71]\n"
"v_mfma_f32_16x16x32_bf16 v[72:75], a[40:43], a[120:123], v[72:75]\n"
"s_add_u32 s67, s75, 308224\n"
"v_mfma_f32_16x16x32_bf16 v[72:75], a[44:47], a[124:127], v[72:75]\n"
"v_mfma_f32_16x16x32_bf16 v[44:47], a[36:39], a[124:127], v[44:47]\n"
"v_mov_b32 v16, -1\n"
"v_mfma_f32_16x16x32_bf16 v[44:47], a[32:35], a[120:123], v[44:47]\n"
"buffer_load_dwordx4 v11, s[52:55], s67 offen offset:3072  lds\n"
"v_mfma_f32_16x16x32_bf16 v[48:51], a[32:35], a[128:131], v[48:51]\n"
"s_add_u32 m0, s64, 12288\n"
"v_mfma_f32_16x16x32_bf16 v[48:51], a[36:39], a[132:135], v[48:51]\n"
"v_mov_b32 v17, -1\n"
"v_mfma_f32_16x16x32_bf16 v[76:79], a[44:47], a[132:135], v[76:79]\n"
"s_waitcnt vmcnt(8)\n"
"ds_read_b64_tr_b16 a[64:65], v9 offset:0\n"
"v_mfma_f32_16x16x32_bf16 v[76:79], a[40:43], a[128:131], v[76:79]\n"
"s_add_u32 s67, s75, 327680\n"
"ds_read_b64_tr_b16 a[66:67], v9 offset:512\n"
"v_mfma_f32_16x16x32_bf16 v[80:83], a[40:43], a[136:139], v[80:83]\n"
"ds_read_b64_tr_b16 a[68:69], v9 offset:4096\n"
"buffer_load_dwordx4 v11, s[52:55], s67 offen offset:0  lds\n"
"v_mfma_f32_16x16x32_bf16 v[80:83], a[44:47], a[140:143], v[80:83]\n"
"ds_read_b64_tr_b16 a[70:71], v9 offset:4608\n"
"v_mfma_f32_16x16x32_bf16 v[52:55], a[36:39], a[140:143], v[52:55]\n"
"s_add_u32 s67, s75, 343040\n"
"ds_read_b64_tr_b16 a[72:73], v9 offset:8\n"
"v_mfma_f32_16x16x32_bf16 v[52:55], a[32:35], a[136:139], v[52:55]\n"
"s_waitcnt lgkmcnt(5)\n"
"ds_read_b64_tr_b16 a[74:75], v9 offset:520\n"
"v_mfma_f32_16x16x32_bf16 v[56:59], a[32:35], a[144:147], v[56:59]\n"
"ds_read_b64_tr_b16 a[76:77], v9 offset:4104\n"
"buffer_load_dwordx4 v11, s[52:55], s67 offen offset:1024  lds\n"
"v_mfma_f32_16x16x32_bf16 v[56:59], a[36:39], a[148:151], v[56:59]\n"
"s_add_u32 s67, s75, 358400\n"
"ds_read_b64_tr_b16 a[78:79], v9 offset:4616\n"
"v_mfma_f32_16x16x32_bf16 v[84:87], a[44:47], a[148:151], v[84:87]\n"
"s_add_u32 s69, %11, 8192\n"
"v_lshrrev_b32 v18, 1, v0\n"
"v_mfma_f32_16x16x32_bf16 v[84:87], a[40:43], a[144:147], v[84:87]\n"
"ds_read_b64_tr_b16 a[80:81], v15 offset:0\n"
"v_mfma_f32_16x16x32_bf16 a[4:7], a[40:43], a[152:155], a[4:7]\n"
"ds_read_b64_tr_b16 a[82:83], v15 offset:512\n"
"buffer_load_dwordx4 v11, s[52:55], s67 offen offset:2048  lds\n"
"v_mfma_f32_16x16x32_bf16 a[4:7], a[44:47], a[156:159], a[4:7]\n"
"ds_read_b64_tr_b16 a[84:85], v15 offset:4096\n"
"v_mfma_f32_16x16x32_bf16 a[0:3], a[36:39], a[156:159], a[0:3]\n"
"s_add_u32 s67, s75, 373760\n"
"ds_read_b64_tr_b16 a[86:87], v15 offset:4608\n"
"v_mfma_f32_16x16x32_bf16 a[0:3], a[32:35], a[152:155], a[0:3]\n"
"ds_read_b64_tr_b16 a[88:89], v15 offset:8\n"
"v_mfma_f32_16x16x32_bf16 v[88:91], a[48:51], a[96:99], v[88:91]\n"
"ds_read_b64_tr_b16 a[90:91], v15 offset:520\n"
"v_mfma_f32_16x16x32_bf16 v[88:91], a[52:55], a[100:103], v[88:91]\n"
"ds_read_b64_tr_b16 a[92:93], v15 offset:4104\n"
"buffer_load_dwordx4 v11, s[52:55], s67 offen offset:3072  lds\n"
"v_mfma_f32_16x16x32_bf16 v[116:119], a[60:63], a[100:103], v[116:119]\n"
"ds_read_b64_tr_b16 a[94:95], v15 offset:4616\n"
"v_mfma_f32_16x16x32_bf16 v[116:119], a[56:59], a[96:99], v[116:119]\n"
"s_add_u32 m0, s64, 0\n"
"v_mfma_f32_16x16x32_bf16 v[120:123], a[56:59], a[104:107], v[120:123]\n"
"v_mfma_f32_16x16x32_bf16 v[120:123], a[60:63], a[108:111], v[120:123]\n"
"v_mfma_f32_16x16x32_bf16 v[92:95], a[52:55], a[108:111], v[92:95]\n"
"s_add_u32 s67, s75, 262272\n"
"v_mfma_f32_16x16x32_bf16 v[92:95], a[48:51], a[104:107], v[92:95]\n"
"v_mfma_f32_16x16x32_bf16 v[96:99], a[48:51], a[112:115], v[96:99]\n"
"s_waitcnt lgkmcnt(0)\n"
"v_mfma_f32_16x16x32_bf16 v[96:99], a[52:55], a[116:119], v[96:99]\n"
"buffer_load_dwordx4 v11, s[52:55], s67 offen offset:0  lds\n"
"v_mfma_f32_16x16x32_bf16 v[124:127], a[60:63], a[116:119], v[124:127]\n"
"v_mfma_f32_16x16x32_bf16 v[124:127], a[56:59], a[112:115], v[124:127]\n"
"s_add_u32 s67, s75, 277632\n"
"v_mfma_f32_16x16x32_bf16 v[128:131], a[56:59], a[120:123], v[128:131]\n"
"v_mfma_f32_16x16x32_bf16 v[128:131], a[60:63], a[124:127], v[128:131]\n"
"v_mfma_f32_16x16x32_bf16 v[100:103], a[52:55], a[124:127], v[100:103]\n"
"buffer_load_dwordx4 v11, s[52:55], s67 offen offset:1024  lds\n"
"v_mfma_f32_16x16x32_bf16 v[100:103], a[48:51], a[120:123], v[100:103]\n"
"v_mfma_f32_16x16x32_bf16 v[104:107], a[48:51], a[128:131], v[104:107]\n"
"s_add_u32 s67, s75, 292992\n"
"v_mfma_f32_16x16x32_bf16 v[104:107], a[52:55], a[132:135], v[104:107]\n"
"v_mfma_f32_16x16x32_bf16 v[132:135], a[60:63], a[132:135], v[132:135]\n"
"buffer_load_dwordx4 v11, s[52:55], s67 offen offset:2048  lds\n"
"v_mfma_f32_16x16x32_bf16 v[132:135], a[56:59], a[128:131], v[132:135]\n"
"v_mfma_f32_16x16x32_bf16 v[136:139], a[56:59], a[136:139], v[136:139]\n"
"s_add_u32 s67, s75, 308352\n"
"v_mfma_f32_16x16x32_bf16 v[136:139], a[60:63], a[140:143], v[136:139]\n"
"v_mfma_f32_16x16x32_bf16 v[108:111], a[52:55], a[140:143], v[108:111]\n"
"v_mfma_f32_16x16x32_bf16 v[108:111], a[48:51], a[136:139], v[108:111]\n"
"buffer_load_dwordx4 v11, s[52:55], s67 offen offset:3072  lds\n"
"v_mfma_f32_16x16x32_bf16 v[112:115], a[48:51], a[144:147], v[112:115]\n"
"v_mfma_f32_16x16x32_bf16 v[112:115], a[52:55], a[148:151], v[112:115]\n"
"s_add_u32 m0, s64, 4096\n"
"v_mfma_f32_16x16x32_bf16 v[140:143], a[60:63], a[148:151], v[140:143]\n"
"v_mfma_f32_16x16x32_bf16 v[140:143], a[56:59], a[144:147], v[140:143]\n"
"s_add_u32 s67, s75, 327808\n"
"v_mfma_f32_16x16x32_bf16 a[12:15], a[56:59], a[152:155], a[12:15]\n"
"v_mfma_f32_16x16x32_bf16 a[12:15], a[60:63], a[156:159], a[12:15]\n"
"buffer_load_dwordx4 v11, s[52:55], s67 offen offset:0  lds\n"
"v_mfma_f32_16x16x32_bf16 a[8:11], a[52:55], a[156:159], a[8:11]\n"
"v_mfma_f32_16x16x32_bf16 a[8:11], a[48:51], a[152:155], a[8:11]\n"
"s_add_u32 s67, s75, 343168\n"
"v_mfma_f32_16x16x32_bf16 v[144:147], a[64:67], a[96:99], v[144:147]\n"
"v_mfma_f32_16x16x32_bf16 v[144:147], a[68:71], a[100:103], v[144:147]\n"
"buffer_load_dwordx4 v11, s[52:55], s67 offen offset:1024  lds\n"
"v_mfma_f32_16x16x32_bf16 v[172:175], a[76:79], a[100:103], v[172:175]\n"
"v_mfma_f32_16x16x32_bf16 v[172:175], a[72:75], a[96:99], v[172:175]\n"
"s_add_u32 s67, s75, 358528\n"
"v_mfma_f32_16x16x32_bf16 v[176:179], a[72:75], a[104:107], v[176:179]\n"
"v_mfma_f32_16x16x32_bf16 v[176:179], a[76:79], a[108:111], v[176:179]\n"
"buffer_load_dwordx4 v11, s[52:55], s67 offen offset:2048  lds\n"
"v_mfma_f32_16x16x32_bf16 v[148:151], a[68:71], a[108:111], v[148:151]\n"
"v_mfma_f32_16x16x32_bf16 v[148:151], a[64:67], a[104:107], v[148:151]\n"
"s_add_u32 s67, s75, 373888\n"
"v_mfma_f32_16x16x32_bf16 v[152:155], a[64:67], a[112:115], v[152:155]\n"
"s_sub_u32 s66, 81920, s73\n"
"v_mfma_f32_16x16x32_bf16 v[152:155], a[68:71], a[116:119], v[152:155]\n"
"buffer_load_dwordx4 v11, s[52:55], s67 offen offset:3072  lds\n"
"v_mfma_f32_16x16x32_bf16 v[180:183], a[76:79], a[116:119], v[180:183]\n"
"v_add_u32 v12, s66, v7\n"
"v_mfma_f32_16x16x32_bf16 v[180:183], a[72:75], a[112:115], v[180:183]\n"
"s_add_u32 m0, s72, 139264\n"
"v_mfma_f32_16x16x32_bf16 v[184:187], a[72:75], a[120:123], v[184:187]\n"
"v_add_u32 v13, s66, v8\n"
"v_mfma_f32_16x16x32_bf16 v[184:187], a[76:79], a[124:127], v[184:187]\n"
"s_add_u32 s67, s60, 384\n"
"v_mfma_f32_16x16x32_bf16 v[156:159], a[68:71], a[124:127], v[156:159]\n"
"v_mfma_f32_16x16x32_bf16 v[156:159], a[64:67], a[120:123], v[156:159]\n"
"buffer_load_dwordx4 v6, s[48:51], s67 offen offset:0  lds\n"
"v_mfma_f32_16x16x32_bf16 v[160:163], a[64:67], a[128:131], v[160:163]\n"
"v_mfma_f32_16x16x32_bf16 v[160:163], a[68:71], a[132:135], v[160:163]\n"
"s_add_u32 s67, s60, 15744\n"
"v_mfma_f32_16x16x32_bf16 v[188:191], a[76:79], a[132:135], v[188:191]\n"
"v_mfma_f32_16x16x32_bf16 v[188:191], a[72:75], a[128:131], v[188:191]\n"
"buffer_load_dwordx4 v6, s[48:51], s67 offen offset:1024  lds\n"
"v_mfma_f32_16x16x32_bf16 v[192:195], a[72:75], a[136:139], v[192:195]\n"
"v_mfma_f32_16x16x32_bf16 v[192:195], a[76:79], a[140:143], v[192:195]\n"
"s_add_u32 s67, s60, 31104\n"
"v_mfma_f32_16x16x32_bf16 v[164:167], a[68:71], a[140:143], v[164:167]\n"
"v_mfma_f32_16x16x32_bf16 v[164:167], a[64:67], a[136:139], v[164:167]\n"
"buffer_load_dwordx4 v6, s[48:51], s67 offen offset:2048  lds\n"
"v_mfma_f32_16x16x32_bf16 v[168:171], a[64:67], a[144:147], v[168:171]\n"
"v_mfma_f32_16x16x32_bf16 v[168:171], a[68:71], a[148:151], v[168:171]\n"
"s_add_u32 s67, s60, 46464\n"
"v_mfma_f32_16x16x32_bf16 v[196:199], a[76:79], a[148:151], v[196:199]\n"
"v_mfma_f32_16x16x32_bf16 v[196:199], a[72:75], a[144:147], v[196:199]\n"
"v_mfma_f32_16x16x32_bf16 a[20:23], a[72:75], a[152:155], a[20:23]\n"
"buffer_load_dwordx4 v6, s[48:51], s67 offen offset:3072  lds\n"
"v_mfma_f32_16x16x32_bf16 a[20:23], a[76:79], a[156:159], a[20:23]\n"
"s_add_u32 s70, s75, 655360\n"
"v_mfma_f32_16x16x32_bf16 a[16:19], a[68:71], a[156:159], a[16:19]\n"
"s_add_u32 s70, s70, s65\n"
"v_mfma_f32_16x16x32_bf16 a[16:19], a[64:67], a[152:155], a[16:19]\n"
"s_waitcnt vmcnt(20)\n"
"s_barrier\n"
"v_mfma_f32_16x16x32_bf16 v[200:203], a[80:83], a[96:99], v[200:203]\n"
"s_waitcnt vmcnt(12)\n"
"ds_read_b64_tr_b16 a[32:33], v9 offset:8192\n"
"v_mfma_f32_16x16x32_bf16 v[200:203], a[84:87], a[100:103], v[200:203]\n"
"ds_read_b64_tr_b16 a[34:35], v9 offset:8704\n"
"buffer_load_dword a252, v14, s[52:55], s70 offen\n"
"v_mfma_f32_16x16x32_bf16 v[228:231], a[92:95], a[100:103], v[228:231]\n"
"ds_read_b128 a[100:103], v13 offset:16384\n"
"v_mfma_f32_16x16x32_bf16 v[228:231], a[88:91], a[96:99], v[228:231]\n"
"ds_read_b128 a[96:99], v12 offset:16384\n"
"v_mfma_f32_16x16x32_bf16 v[232:235], a[88:91], a[104:107], v[232:235]\n"
"ds_read_b64_tr_b16 a[36:37], v9 offset:12288\n"
"v_mfma_f32_16x16x32_bf16 v[232:235], a[92:95], a[108:111], v[232:235]\n"
"ds_read_b64_tr_b16 a[38:39], v9 offset:12800\n"
"v_mfma_f32_16x16x32_bf16 v[204:207], a[84:87], a[108:111], v[204:207]\n"
"ds_read_b128 a[108:111], v13 offset:18432\n"
"v_mfma_f32_16x16x32_bf16 v[204:207], a[80:83], a[104:107], v[204:207]\n"
"ds_read_b128 a[104:107], v12 offset:18432\n"
"s_add_u32 m0, s64, 8192\n"
"ds_read_b64_tr_b16 a[40:41], v9 offset:8200\n"
"v_mfma_f32_16x16x32_bf16 v[208:211], a[80:83], a[112:115], v[208:211]\n"
"ds_read_b64_tr_b16 a[42:43], v9 offset:8712\n"
"v_mfma_f32_16x16x32_bf16 v[208:211], a[84:87], a[116:119], v[208:211]\n"
"ds_read_b64_tr_b16 a[44:45], v9 offset:12296\n"
"v_mfma_f32_16x16x32_bf16 v[236:239], a[92:95], a[116:119], v[236:239]\n"
"ds_read_b128 a[116:119], v13 offset:20480\n"
"v_mfma_f32_16x16x32_bf16 v[236:239], a[88:91], a[112:115], v[236:239]\n"
"ds_read_b128 a[112:115], v12 offset:20480\n"
"v_mfma_f32_16x16x32_bf16 v[240:243], a[88:91], a[120:123], v[240:243]\n"
"ds_read_b64_tr_b16 a[46:47], v9 offset:12808\n"
"v_mfma_f32_16x16x32_bf16 v[240:243], a[92:95], a[124:127], v[240:243]\n"
"ds_read_b64_tr_b16 a[48:49], v15 offset:8192\n"
"v_mfma_f32_16x16x32_bf16 v[212:215], a[84:87], a[124:127], v[212:215]\n"
"ds_read_b128 a[124:127], v13 offset:22528\n"
"v_mfma_f32_16x16x32_bf16 v[212:215], a[80:83], a[120:123], v[212:215]\n"
"ds_read_b128 a[120:123], v12 offset:22528\n"
"v_mfma_f32_16x16x32_bf16 v[216:219], a[80:83], a[128:131], v[216:219]\n"
"ds_read_b64_tr_b16 a[50:51], v15 offset:8704\n"
"v_mfma_f32_16x16x32_bf16 v[216:219], a[84:87], a[132:135], v[216:219]\n"
"ds_read_b64_tr_b16 a[52:53], v15 offset:12288\n"
"v_mfma_f32_16x16x32_bf16 v[244:247], a[92:95], a[132:135], v[244:247]\n"
"ds_read_b128 a[132:135], v13 offset:24576\n"
"v_mfma_f32_16x16x32_bf16 v[244:247], a[88:91], a[128:131], v[244:247]\n"
"s_add_u32 s67, s75, 393216\n"
"ds_read_b128 a[128:131], v12 offset:24576\n"
"v_mfma_f32_16x16x32_bf16 v[248:251], a[88:91], a[136:139], v[248:251]\n"
"ds_read_b64_tr_b16 a[54:55], v15 offset:12800\n"
"v_mfma_f32_16x16x32_bf16 v[248:251], a[92:95], a[140:143], v[248:251]\n"
"s_add_u32 s66, s78, 1\n"
"ds_read_b64_tr_b16 a[56:57], v15 offset:8200\n"
"v_mfma_f32_16x16x32_bf16 v[220:223], a[84:87], a[140:143], v[220:223]\n"
"ds_read_b128 a[140:143], v13 offset:26624\n"
"v_mfma_f32_16x16x32_bf16 v[220:223], a[80:83], a[136:139], v[220:223]\n"
"ds_read_b128 a[136:139], v12 offset:26624\n"
"v_mov_b32 v28, s66\n"
"v_mfma_f32_16x16x32_bf16 v[224:227], a[80:83], a[144:147], v[224:227]\n"
"ds_read_b64_tr_b16 a[58:59], v15 offset:8712\n"
"v_mov_b32 v29, s79\n"
"v_mfma_f32_16x16x32_bf16 v[224:227], a[84:87], a[148:151], v[224:227]\n"
"ds_read_b64_tr_b16 a[60:61], v15 offset:12296\n"
"v_mfma_f32_16x16x32_bf16 v[252:255], a[92:95], a[148:151], v[252:255]\n"
"buffer_store_dwordx2 v[28:29], off, s[56:59], %24 sc1\n"
"ds_read_b128 a[148:151], v13 offset:28672\n"
"v_mfma_f32_16x16x32_bf16 v[252:255], a[88:91], a[144:147], v[252:255]\n"
"ds_read_b128 a[144:147], v12 offset:28672\n"
"ds_read_b64_tr_b16 a[62:63], v15 offset:12808\n"
"v_mfma_f32_16x16x32_bf16 a[28:31], a[88:91], a[152:155], a[28:31]\n"
"v_mfma_f32_16x16x32_bf16 a[28:31], a[92:95], a[156:159], a[28:31]\n"
"v_mfma_f32_16x16x32_bf16 a[24:27], a[84:87], a[156:159], a[24:27]\n"
"ds_read_b128 a[156:159], v13 offset:30720\n"
"v_mfma_f32_16x16x32_bf16 a[24:27], a[80:83], a[152:155], a[24:27]\n"
"s_waitcnt lgkmcnt(15)\n"
"ds_read_b128 a[152:155], v12 offset:30720\n"
"v_mfma_f32_16x16x32_bf16 v[32:35], a[32:35], a[96:99], v[32:35]\n"
"v_mfma_f32_16x16x32_bf16 v[32:35], a[36:39], a[100:103], v[32:35]\n"
"s_waitcnt lgkmcnt(2)\n"
"v_mfma_f32_16x16x32_bf16 v[60:63], a[44:47], a[100:103], v[60:63]\n"
"buffer_load_dwordx4 v11, s[52:55], s67 offen offset:0  lds\n"
"v_mfma_f32_16x16x32_bf16 v[60:63], a[40:43], a[96:99], v[60:63]\n"
"v_mfma_f32_16x16x32_bf16 v[64:67], a[40:43], a[104:107], v[64:67]\n"
"s_add_u32 s67, s75, 408576\n"
"v_mfma_f32_16x16x32_bf16 v[64:67], a[44:47], a[108:111], v[64:67]\n"
"v_mfma_f32_16x16x32_bf16 v[36:39], a[36:39], a[108:111], v[36:39]\n"
"v_mfma_f32_16x16x32_bf16 v[36:39], a[32:35], a[104:107], v[36:39]\n"
"buffer_load_dwordx4 v11, s[52:55], s67 offen offset:1024  lds\n"
"v_mfma_f32_16x16x32_bf16 v[40:43], a[32:35], a[112:115], v[40:43]\n"
"v_mfma_f32_16x16x32_bf16 v[40:43], a[36:39], a[116:119], v[40:43]\n"
"s_add_u32 s67, s75, 423936\n"
"v_mfma_f32_16x16x32_bf16 v[68:71], a[44:47], a[116:119], v[68:71]\n"
"v_mfma_f32_16x16x32_bf16 v[68:71], a[40:43], a[112:115], v[68:71]\n"
"v_mfma_f32_16x16x32_bf16 v[72:75], a[40:43], a[120:123], v[72:75]\n"
"buffer_load_dwordx4 v11, s[52:55], s67 offen offset:2048  lds\n"
"v_mfma_f32_16x16x32_bf16 v[72:75], a[44:47], a[124:127], v[72:75]\n"
"v_mfma_f32_16x16x32_bf16 v[44:47], a[36:39], a[124:127], v[44:47]\n"
"s_add_u32 s67, s75, 439296\n"
"v_mfma_f32_16x16x32_bf16 v[44:47], a[32:35], a[120:123], v[44:47]\n"
"v_mfma_f32_16x16x32_bf16 v[48:51], a[32:35], a[128:131], v[48:51]\n"
"buffer_load_dwordx4 v11, s[52:55], s67 offen offset:3072  lds\n"
"v_mfma_f32_16x16x32_bf16 v[48:51], a[36:39], a[132:135], v[48:51]\n"
"s_add_u32 m0, s64, 12288\n"
"v_mfma_f32_16x16x32_bf16 v[76:79], a[44:47], a[132:135], v[76:79]\n"
"s_waitcnt vmcnt(10)\n"
"ds_read_b64_tr_b16 a[64:65], v9 offset:0\n"
"v_mfma_f32_16x16x32_bf16 v[76:79], a[40:43], a[128:131], v[76:79]\n"
"ds_read_b64_tr_b16 a[66:67], v9 offset:512\n"
"v_mfma_f32_16x16x32_bf16 v[80:83], a[40:43], a[136:139], v[80:83]\n"
"s_add_u32 s67, s75, 458752\n"
"ds_read_b64_tr_b16 a[68:69], v9 offset:4096\n"
"v_mfma_f32_16x16x32_bf16 v[80:83], a[44:47], a[140:143], v[80:83]\n"
"ds_read_b64_tr_b16 a[70:71], v9 offset:4608\n"
"v_mfma_f32_16x16x32_bf16 v[52:55], a[36:39], a[140:143], v[52:55]\n"
"ds_read_b64_tr_b16 a[72:73], v9 offset:8\n"
"buffer_load_dwordx4 v11, s[52:55], s67 offen offset:0  lds\n"
"v_mfma_f32_16x16x32_bf16 v[52:55], a[32:35], a[136:139], v[52:55]\n"
"s_waitcnt lgkmcnt(5)\n"
"ds_read_b64_tr_b16 a[74:75], v9 offset:520\n"
"v_mfma_f32_16x16x32_bf16 v[56:59], a[32:35], a[144:147], v[56:59]\n"
"s_add_u32 s67, s75, 474112\n"
"ds_read_b64_tr_b16 a[76:77], v9 offset:4104\n"
"v_mfma_f32_16x16x32_bf16 v[56:59], a[36:39], a[148:151], v[56:59]\n"
"ds_read_b64_tr_b16 a[78:79], v9 offset:4616\n"
"v_mfma_f32_16x16x32_bf16 v[84:87], a[44:47], a[148:151], v[84:87]\n"
"buffer_load_dwordx4 v11, s[52:55], s67 offen offset:1024  lds\n"
"v_mfma_f32_16x16x32_bf16 v[84:87], a[40:43], a[144:147], v[84:87]\n"
"ds_read_b64_tr_b16 a[80:81], v15 offset:0\n"
"v_mfma_f32_16x16x32_bf16 a[4:7], a[40:43], a[152:155], a[4:7]\n"
"s_add_u32 s67, s75, 489472\n"
"ds_read_b64_tr_b16 a[82:83], v15 offset:512\n"
"v_mfma_f32_16x16x32_bf16 a[4:7], a[44:47], a[156:159], a[4:7]\n"
"ds_read_b64_tr_b16 a[84:85], v15 offset:4096\n"
"v_mfma_f32_16x16x32_bf16 a[0:3], a[36:39], a[156:159], a[0:3]\n"
"ds_read_b64_tr_b16 a[86:87], v15 offset:4608\n"
"buffer_load_dwordx4 v11, s[52:55], s67 offen offset:2048  lds\n"
"v_mfma_f32_16x16x32_bf16 a[0:3], a[32:35], a[152:155], a[0:3]\n"
"ds_read_b64_tr_b16 a[88:89], v15 offset:8\n"
"v_mfma_f32_16x16x32_bf16 v[88:91], a[48:51], a[96:99], v[88:91]\n"
"s_add_u32 s67, s75, 504832\n"
"ds_read_b64_tr_b16 a[90:91], v15 offset:520\n"
"v_mfma_f32_16x16x32_bf16 v[88:91], a[52:55], a[100:103], v[88:91]\n"
"ds_read_b64_tr_b16 a[92:93], v15 offset:4104\n"
"v_mfma_f32_16x16x32_bf16 v[116:119], a[60:63], a[100:103], v[116:119]\n"
"ds_read_b64_tr_b16 a[94:95], v15 offset:4616\n"
"v_mfma_f32_16x16x32_bf16 v[116:119], a[56:59], a[96:99], v[116:119]\n"
"buffer_load_dwordx4 v11, s[52:55], s67 offen offset:3072  lds\n"
"v_mfma_f32_16x16x32_bf16 v[120:123], a[56:59], a[104:107], v[120:123]\n"
"v_mfma_f32_16x16x32_bf16 v[120:123], a[60:63], a[108:111], v[120:123]\n"
"s_add_u32 m0, s64, 0\n"
"v_mfma_f32_16x16x32_bf16 v[92:95], a[52:55], a[108:111], v[92:95]\n"
"v_mfma_f32_16x16x32_bf16 v[92:95], a[48:51], a[104:107], v[92:95]\n"
"s_add_u32 s67, s75, 393344\n"
"v_mfma_f32_16x16x32_bf16 v[96:99], a[48:51], a[112:115], v[96:99]\n"
"s_waitcnt lgkmcnt(0)\n"
"v_mfma_f32_16x16x32_bf16 v[96:99], a[52:55], a[116:119], v[96:99]\n"
"buffer_load_dwordx4 v11, s[52:55], s67 offen offset:0  lds\n"
"v_mfma_f32_16x16x32_bf16 v[124:127], a[60:63], a[116:119], v[124:127]\n"
"v_mfma_f32_16x16x32_bf16 v[124:127], a[56:59], a[112:115], v[124:127]\n"
"s_add_u32 s67, s75, 408704\n"
"v_mfma_f32_16x16x32_bf16 v[128:131], a[56:59], a[120:123], v[128:131]\n"
"v_mfma_f32_16x16x32_bf16 v[128:131], a[60:63], a[124:127], v[128:131]\n"
"v_mfma_f32_16x16x32_bf16 v[100:103], a[52:55], a[124:127], v[100:103]\n"
"buffer_load_dwordx4 v11, s[52:55], s67 offen offset:1024  lds\n"
"v_mfma_f32_16x16x32_bf16 v[100:103], a[48:51], a[120:123], v[100:103]\n"
"v_mfma_f32_16x16x32_bf16 v[104:107], a[48:51], a[128:131], v[104:107]\n"
"s_add_u32 s67, s75, 424064\n"
"v_mfma_f32_16x16x32_bf16 v[104:107], a[52:55], a[132:135], v[104:107]\n"
"v_mfma_f32_16x16x32_bf16 v[132:135], a[60:63], a[132:135], v[132:135]\n"
"v_mfma_f32_16x16x32_bf16 v[132:135], a[56:59], a[128:131], v[132:135]\n"
"buffer_load_dwordx4 v11, s[52:55], s67 offen offset:2048  lds\n"
"v_mfma_f32_16x16x32_bf16 v[136:139], a[56:59], a[136:139], v[136:139]\n"
"v_mfma_f32_16x16x32_bf16 v[136:139], a[60:63], a[140:143], v[136:139]\n"
"s_add_u32 s67, s75, 439424\n"
"v_mfma_f32_16x16x32_bf16 v[108:111], a[52:55], a[140:143], v[108:111]\n"
"v_mfma_f32_16x16x32_bf16 v[108:111], a[48:51], a[136:139], v[108:111]\n"
"buffer_load_dwordx4 v11, s[52:55], s67 offen offset:3072  lds\n"
"v_mfma_f32_16x16x32_bf16 v[112:115], a[48:51], a[144:147], v[112:115]\n"
"v_mfma_f32_16x16x32_bf16 v[112:115], a[52:55], a[148:151], v[112:115]\n"
"s_add_u32 m0, s64, 4096\n"
"v_mfma_f32_16x16x32_bf16 v[140:143], a[60:63], a[148:151], v[140:143]\n"
"v_mfma_f32_16x16x32_bf16 v[140:143], a[56:59], a[144:147], v[140:143]\n"
"s_add_u32 s67, s75, 458880\n"
"v_mfma_f32_16x16x32_bf16 a[12:15], a[56:59], a[152:155], a[12:15]\n"
"v_mfma_f32_16x16x32_bf16 a[12:15], a[60:63], a[156:159], a[12:15]\n"
"buffer_load_dwordx4 v11, s[52:55], s67 offen offset:0  lds\n"
"v_mfma_f32_16x16x32_bf16 a[8:11], a[52:55], a[156:159], a[8:11]\n"
"v_mfma_f32_16x16x32_bf16 a[8:11], a[48:51], a[152:155], a[8:11]\n"
"s_add_u32 s67, s75, 474240\n"
"v_mfma_f32_16x16x32_bf16 v[144:147], a[64:67], a[96:99], v[144:147]\n"
"v_mfma_f32_16x16x32_bf16 v[144:147], a[68:71], a[100:103], v[144:147]\n"
"buffer_load_dwordx4 v11, s[52:55], s67 offen offset:1024  lds\n"
"v_mfma_f32_16x16x32_bf16 v[172:175], a[76:79], a[100:103], v[172:175]\n"
"v_mfma_f32_16x16x32_bf16 v[172:175], a[72:75], a[96:99], v[172:175]\n"
"s_add_u32 s67, s75, 489600\n"
"v_mfma_f32_16x16x32_bf16 v[176:179], a[72:75], a[104:107], v[176:179]\n"
"v_mfma_f32_16x16x32_bf16 v[176:179], a[76:79], a[108:111], v[176:179]\n"
"buffer_load_dwordx4 v11, s[52:55], s67 offen offset:2048  lds\n"
"v_mfma_f32_16x16x32_bf16 v[148:151], a[68:71], a[108:111], v[148:151]\n"
"v_mfma_f32_16x16x32_bf16 v[148:151], a[64:67], a[104:107], v[148:151]\n"
"s_add_u32 s67, s75, 504960\n"
"v_mfma_f32_16x16x32_bf16 v[152:155], a[64:67], a[112:115], v[152:155]\n"
"s_sub_u32 s66, 122880, s73\n"
"v_mfma_f32_16x16x32_bf16 v[152:155], a[68:71], a[116:119], v[152:155]\n"
"v_mfma_f32_16x16x32_bf16 v[180:183], a[76:79], a[116:119], v[180:183]\n"
"v_add_u32 v12, s66, v7\n"
"v_mfma_f32_16x16x32_bf16 v[180:183], a[72:75], a[112:115], v[180:183]\n"
"buffer_load_dwordx4 v11, s[52:55], s67 offen offset:3072  lds\n"
"v_mfma_f32_16x16x32_bf16 v[184:187], a[72:75], a[120:123], v[184:187]\n"
"s_add_u32 m0, s72, 16384\n"
"v_add_u32 v13, s66, v8\n"
"v_mfma_f32_16x16x32_bf16 v[184:187], a[76:79], a[124:127], v[184:187]\n"
"v_mfma_f32_16x16x32_bf16 v[156:159], a[68:71], a[124:127], v[156:159]\n"
"s_add_u32 s67, s60, 512\n"
"v_mfma_f32_16x16x32_bf16 v[156:159], a[64:67], a[120:123], v[156:159]\n"
"buffer_load_dwordx4 v6, s[48:51], s67 offen offset:0  lds\n"
"v_mfma_f32_16x16x32_bf16 v[160:163], a[64:67], a[128:131], v[160:163]\n"
"v_mfma_f32_16x16x32_bf16 v[160:163], a[68:71], a[132:135], v[160:163]\n"
"s_add_u32 s67, s60, 15872\n"
"v_mfma_f32_16x16x32_bf16 v[188:191], a[76:79], a[132:135], v[188:191]\n"
"v_mfma_f32_16x16x32_bf16 v[188:191], a[72:75], a[128:131], v[188:191]\n"
"buffer_load_dwordx4 v6, s[48:51], s67 offen offset:1024  lds\n"
"v_mfma_f32_16x16x32_bf16 v[192:195], a[72:75], a[136:139], v[192:195]\n"
"v_mfma_f32_16x16x32_bf16 v[192:195], a[76:79], a[140:143], v[192:195]\n"
"s_add_u32 s67, s60, 31232\n"
"v_mfma_f32_16x16x32_bf16 v[164:167], a[68:71], a[140:143], v[164:167]\n"
"v_mfma_f32_16x16x32_bf16 v[164:167], a[64:67], a[136:139], v[164:167]\n"
"buffer_load_dwordx4 v6, s[48:51], s67 offen offset:2048  lds\n"
"v_mfma_f32_16x16x32_bf16 v[168:171], a[64:67], a[144:147], v[168:171]\n"
"v_mfma_f32_16x16x32_bf16 v[168:171], a[68:71], a[148:151], v[168:171]\n"
"s_add_u32 s67, s60, 46592\n"
"v_mfma_f32_16x16x32_bf16 v[196:199], a[76:79], a[148:151], v[196:199]\n"
"v_mfma_f32_16x16x32_bf16 v[196:199], a[72:75], a[144:147], v[196:199]\n"
"v_mfma_f32_16x16x32_bf16 a[20:23], a[72:75], a[152:155], a[20:23]\n"
"buffer_load_dwordx4 v6, s[48:51], s67 offen offset:3072  lds\n"
"v_mfma_f32_16x16x32_bf16 a[20:23], a[76:79], a[156:159], a[20:23]\n"
"v_mfma_f32_16x16x32_bf16 a[16:19], a[68:71], a[156:159], a[16:19]\n"
"s_add_u32 s70, s75, 786432\n"
"s_add_u32 s70, s70, s65\n"
"v_mfma_f32_16x16x32_bf16 a[16:19], a[64:67], a[152:155], a[16:19]\n"
"s_waitcnt vmcnt(22)\n"
"s_barrier\n"
"v_mfma_f32_16x16x32_bf16 v[200:203], a[80:83], a[96:99], v[200:203]\n"
"s_waitcnt vmcnt(12)\n"
"ds_read_b64_tr_b16 a[32:33], v9 offset:8192\n"
"v_mfma_f32_16x16x32_bf16 v[200:203], a[84:87], a[100:103], v[200:203]\n"
"ds_read_b64_tr_b16 a[34:35], v9 offset:8704\n"
"v_mfma_f32_16x16x32_bf16 v[228:231], a[92:95], a[100:103], v[228:231]\n"
"ds_read_b128 a[100:103], v13 offset:16384\n"
"buffer_load_dword a252, v14, s[52:55], s70 offen\n"
"v_mfma_f32_16x16x32_bf16 v[228:231], a[88:91], a[96:99], v[228:231]\n"
"ds_read_b128 a[96:99], v12 offset:16384\n"
"v_mfma_f32_16x16x32_bf16 v[232:235], a[88:91], a[104:107], v[232:235]\n"
"ds_read_b64_tr_b16 a[36:37], v9 offset:12288\n"
"v_mfma_f32_16x16x32_bf16 v[232:235], a[92:95], a[108:111], v[232:235]\n"
"ds_read_b64_tr_b16 a[38:39], v9 offset:12800\n"
"v_mfma_f32_16x16x32_bf16 v[204:207], a[84:87], a[108:111], v[204:207]\n"
"ds_read_b128 a[108:111], v13 offset:18432\n"
"v_mfma_f32_16x16x32_bf16 v[204:207], a[80:83], a[104:107], v[204:207]\n"
"ds_read_b128 a[104:107], v12 offset:18432\n"
"ds_read_b64_tr_b16 a[40:41], v9 offset:8200\n"
"v_mfma_f32_16x16x32_bf16 v[208:211], a[80:83], a[112:115], v[208:211]\n"
"s_add_u32 m0, s64, 8192\n"
"ds_read_b64_tr_b16 a[42:43], v9 offset:8712\n"
"v_mfma_f32_16x16x32_bf16 v[208:211], a[84:87], a[116:119], v[208:211]\n"
"ds_read_b64_tr_b16 a[44:45], v9 offset:12296\n"
"v_mfma_f32_16x16x32_bf16 v[236:239], a[92:95], a[116:119], v[236:239]\n"
"ds_read_b128 a[116:119], v13 offset:20480\n"
"v_mfma_f32_16x16x32_bf16 v[236:239], a[88:91], a[112:115], v[236:239]\n"
"ds_read_b128 a[112:115], v12 offset:20480\n"
"v_mfma_f32_16x16x32_bf16 v[240:243], a[88:91], a[120:123], v[240:243]\n"
"ds_read_b64_tr_b16 a[46:47], v9 offset:12808\n"
"v_mfma_f32_16x16x32_bf16 v[240:243], a[92:95], a[124:127], v[240:243]\n"
"ds_read_b64_tr_b16 a[48:49], v15 offset:8192\n"
"v_mfma_f32_16x16x32_bf16 v[212:215], a[84:87], a[124:127], v[212:215]\n"
"ds_read_b128 a[124:127], v13 offset:22528\n"
"v_mfma_f32_16x16x32_bf16 v[212:215], a[80:83], a[120:123], v[212:215]\n"
"ds_read_b128 a[120:123], v12 offset:22528\n"
"v_mfma_f32_16x16x32_bf16 v[216:219], a[80:83], a[128:131], v[216:219]\n"
"ds_read_b64_tr_b16 a[50:51], v15 offset:8704\n"
"v_mfma_f32_16x16x32_bf16 v[216:219], a[84:87], a[132:135], v[216:219]\n"
"ds_read_b64_tr_b16 a[52:53], v15 offset:12288\n"
"v_mfma_f32_16x16x32_bf16 v[244:247], a[92:95], a[132:135], v[244:247]\n"
"ds_read_b128 a[132:135], v13 offset:24576\n"
"v_mfma_f32_16x16x32_bf16 v[244:247], a[88:91], a[128:131], v[244:247]\n"
"s_add_u32 s67, s75, 524288\n"
"ds_read_b128 a[128:131], v12 offset:24576\n"
"v_mfma_f32_16x16x32_bf16 v[248:251], a[88:91], a[136:139], v[248:251]\n"
"ds_read_b64_tr_b16 a[54:55], v15 offset:12800\n"
"v_mfma_f32_16x16x32_bf16 v[248:251], a[92:95], a[140:143], v[248:251]\n"
"ds_read_b64_tr_b16 a[56:57], v15 offset:8200\n"
"v_mfma_f32_16x16x32_bf16 v[220:223], a[84:87], a[140:143], v[220:223]\n"
"ds_read_b128 a[140:143], v13 offset:26624\n"
"v_mfma_f32_16x16x32_bf16 v[220:223], a[80:83], a[136:139], v[220:223]\n"
"ds_read_b128 a[136:139], v12 offset:26624\n"
"v_mfma_f32_16x16x32_bf16 v[224:227], a[80:83], a[144:147], v[224:227]\n"
"ds_read_b64_tr_b16 a[58:59], v15 offset:8712\n"
"v_mfma_f32_16x16x32_bf16 v[224:227], a[84:87], a[148:151], v[224:227]\n"
"ds_read_b64_tr_b16 a[60:61], v15 offset:12296\n"
"v_mfma_f32_16x16x32_bf16 v[252:255], a[92:95], a[148:151], v[252:255]\n"
"ds_read_b128 a[148:151], v13 offset:28672\n"
"v_mfma_f32_16x16x32_bf16 v[252:255], a[88:91], a[144:147], v[252:255]\n"
"ds_read_b128 a[144:147], v12 offset:28672\n"
"ds_read_b64_tr_b16 a[62:63], v15 offset:12808\n"
"v_mfma_f32_16x16x32_bf16 a[28:31], a[88:91], a[152:155], a[28:31]\n"
"v_mfma_f32_16x16x32_bf16 a[28:31], a[92:95], a[156:159], a[28:31]\n"
"v_mfma_f32_16x16x32_bf16 a[24:27], a[84:87], a[156:159], a[24:27]\n"
"ds_read_b128 a[156:159], v13 offset:30720\n"
"v_mfma_f32_16x16x32_bf16 a[24:27], a[80:83], a[152:155], a[24:27]\n"
"s_waitcnt lgkmcnt(15)\n"
"ds_read_b128 a[152:155], v12 offset:30720\n"
"v_mfma_f32_16x16x32_bf16 v[32:35], a[32:35], a[96:99], v[32:35]\n"
"v_mfma_f32_16x16x32_bf16 v[32:35], a[36:39], a[100:103], v[32:35]\n"
"s_waitcnt lgkmcnt(2)\n"
"v_mfma_f32_16x16x32_bf16 v[60:63], a[44:47], a[100:103], v[60:63]\n"
"buffer_load_dwordx4 v11, s[52:55], s67 offen offset:0  lds\n"
"v_mfma_f32_16x16x32_bf16 v[60:63], a[40:43], a[96:99], v[60:63]\n"
"v_mfma_f32_16x16x32_bf16 v[64:67], a[40:43], a[104:107], v[64:67]\n"
"s_add_u32 s67, s75, 539648\n"
"v_mfma_f32_16x16x32_bf16 v[64:67], a[44:47], a[108:111], v[64:67]\n"
"v_mfma_f32_16x16x32_bf16 v[36:39], a[36:39], a[108:111], v[36:39]\n"
"buffer_load_dwordx4 v11, s[52:55], s67 offen offset:1024  lds\n"
"v_mfma_f32_16x16x32_bf16 v[36:39], a[32:35], a[104:107], v[36:39]\n"
"v_mfma_f32_16x16x32_bf16 v[40:43], a[32:35], a[112:115], v[40:43]\n"
"L_st1038:\n"
"s_cbranch_execz L_st3225\n"
"s_add_u32 s67, s75, 555008\n"
"v_mfma_f32_16x16x32_bf16 v[40:43], a[36:39], a[116:119], v[40:43]\n"
"v_mfma_f32_16x16x32_bf16 v[68:71], a[44:47], a[116:119], v[68:71]\n"
"v_mfma_f32_16x16x32_bf16 v[68:71], a[40:43], a[112:115], v[68:71]\n"
"buffer_load_dwordx4 v11, s[52:55], s67 offen offset:2048  lds\n"
"v_mfma_f32_16x16x32_bf16 v[72:75], a[40:43], a[120:123], v[72:75]\n"
"v_mfma_f32_16x16x32_bf16 v[72:75], a[44:47], a[124:127], v[72:75]\n"
"s_add_u32 s67, s75, 570368\n"
"v_mfma_f32_16x16x32_bf16 v[44:47], a[36:39], a[124:127], v[44:47]\n"
"v_mfma_f32_16x16x32_bf16 v[44:47], a[32:35], a[120:123], v[44:47]\n"
"buffer_load_dwordx4 v11, s[52:55], s67 offen offset:3072  lds\n"
"v_mfma_f32_16x16x32_bf16 v[48:51], a[32:35], a[128:131], v[48:51]\n"
"v_mfma_f32_16x16x32_bf16 v[48:51], a[36:39], a[132:135], v[48:51]\n"
"s_add_u32 m0, s64, 12288\n"
"v_mfma_f32_16x16x32_bf16 v[76:79], a[44:47], a[132:135], v[76:79]\n"
"s_waitcnt vmcnt(9)\n"
"ds_read_b64_tr_b16 a[64:65], v9 offset:0\n"
"v_mfma_f32_16x16x32_bf16 v[76:79], a[40:43], a[128:131], v[76:79]\n"
"s_add_u32 s67, s75, 589824\n"
"ds_read_b64_tr_b16 a[66:67], v9 offset:512\n"
"v_mfma_f32_16x16x32_bf16 v[80:83], a[40:43], a[136:139], v[80:83]\n"
"ds_read_b64_tr_b16 a[68:69], v9 offset:4096\n"
"v_mfma_f32_16x16x32_bf16 v[80:83], a[44:47], a[140:143], v[80:83]\n"
"ds_read_b64_tr_b16 a[70:71], v9 offset:4608\n"
"buffer_load_dwordx4 v11, s[52:55], s67 offen offset:0  lds\n"
"v_mfma_f32_16x16x32_bf16 v[52:55], a[36:39], a[140:143], v[52:55]\n"
"s_add_u32 s67, s75, 605184\n"
"ds_read_b64_tr_b16 a[72:73], v9 offset:8\n"
"v_mfma_f32_16x16x32_bf16 v[52:55], a[32:35], a[136:139], v[52:55]\n"
"s_waitcnt lgkmcnt(5)\n"
"ds_read_b64_tr_b16 a[74:75], v9 offset:520\n"
"v_mfma_f32_16x16x32_bf16 v[56:59], a[32:35], a[144:147], v[56:59]\n"
"ds_read_b64_tr_b16 a[76:77], v9 offset:4104\n"
"v_mfma_f32_16x16x32_bf16 v[56:59], a[36:39], a[148:151], v[56:59]\n"
"ds_read_b64_tr_b16 a[78:79], v9 offset:4616\n"
"v_mfma_f32_16x16x32_bf16 v[84:87], a[44:47], a[148:151], v[84:87]\n"
"buffer_load_dwordx4 v11, s[52:55], s67 offen offset:1024  lds\n"
"v_mfma_f32_16x16x32_bf16 v[84:87], a[40:43], a[144:147], v[84:87]\n"
"ds_read_b64_tr_b16 a[80:81], v15 offset:0\n"
"v_mfma_f32_16x16x32_bf16 a[4:7], a[40:43], a[152:155], a[4:7]\n"
"s_add_u32 s67, s75, 620544\n"
"ds_read_b64_tr_b16 a[82:83], v15 offset:512\n"
"v_mfma_f32_16x16x32_bf16 a[4:7], a[44:47], a[156:159], a[4:7]\n"
"ds_read_b64_tr_b16 a[84:85], v15 offset:4096\n"
"v_mfma_f32_16x16x32_bf16 a[0:3], a[36:39], a[156:159], a[0:3]\n"
"ds_read_b64_tr_b16 a[86:87], v15 offset:4608\n"
"buffer_load_dwordx4 v11, s[52:55], s67 offen offset:2048  lds\n"
"v_mfma_f32_16x16x32_bf16 a[0:3], a[32:35], a[152:155], a[0:3]\n"
"ds_read_b64_tr_b16 a[88:89], v15 offset:8\n"
"v_mfma_f32_16x16x32_bf16 v[88:91], a[48:51], a[96:99], v[88:91]\n"
"s_add_u32 s67, s75, 635904\n"
"ds_read_b64_tr_b16 a[90:91], v15 offset:520\n"
"v_mfma_f32_16x16x32_bf16 v[88:91], a[52:55], a[100:103], v[88:91]\n"
"ds_read_b64_tr_b16 a[92:93], v15 offset:4104\n"
"v_mfma_f32_16x16x32_bf16 v[116:119], a[60:63], a[100:103], v[116:119]\n"
"ds_read_b64_tr_b16 a[94:95], v15 offset:4616\n"
"v_mfma_f32_16x16x32_bf16 v[116:119], a[56:59], a[96:99], v[116:119]\n"
"buffer_load_dwordx4 v11, s[52:55], s67 offen offset:3072  lds\n"
"v_mfma_f32_16x16x32_bf16 v[120:123], a[56:59], a[104:107], v[120:123]\n"
"v_mfma_f32_16x16x32_bf16 v[120:123], a[60:63], a[108:111], v[120:123]\n"
"s_add_u32 m0, s64, 0\n"
"v_mfma_f32_16x16x32_bf16 v[92:95], a[52:55], a[108:111], v[92:95]\n"
"v_mfma_f32_16x16x32_bf16 v[92:95], a[48:51], a[104:107], v[92:95]\n"
"s_add_u32 s67, s75, 524416\n"
"v_mfma_f32_16x16x32_bf16 v[96:99], a[48:51], a[112:115], v[96:99]\n"
"s_waitcnt lgkmcnt(0)\n"
"v_mfma_f32_16x16x32_bf16 v[96:99], a[52:55], a[116:119], v[96:99]\n"
"buffer_load_dwordx4 v11, s[52:55], s67 offen offset:0  lds\n"
"v_mfma_f32_16x16x32_bf16 v[124:127], a[60:63], a[116:119], v[124:127]\n"
"v_mfma_f32_16x16x32_bf16 v[124:127], a[56:59], a[112:115], v[124:127]\n"
"s_add_u32 s67, s75, 539776\n"
"v_mfma_f32_16x16x32_bf16 v[128:131], a[56:59], a[120:123], v[128:131]\n"
"v_mfma_f32_16x16x32_bf16 v[128:131], a[60:63], a[124:127], v[128:131]\n"
"v_mfma_f32_16x16x32_bf16 v[100:103], a[52:55], a[124:127], v[100:103]\n"
"buffer_load_dwordx4 v11, s[52:55], s67 offen offset:1024  lds\n"
"v_mfma_f32_16x16x32_bf16 v[100:103], a[48:51], a[120:123], v[100:103]\n"
"v_mfma_f32_16x16x32_bf16 v[104:107], a[48:51], a[128:131], v[104:107]\n"
"s_add_u32 s67, s75, 555136\n"
"v_mfma_f32_16x16x32_bf16 v[104:107], a[52:55], a[132:135], v[104:107]\n"
"v_mfma_f32_16x16x32_bf16 v[132:135], a[60:63], a[132:135], v[132:135]\n"
"v_mfma_f32_16x16x32_bf16 v[132:135], a[56:59], a[128:131], v[132:135]\n"
"buffer_load_dwordx4 v11, s[52:55], s67 offen offset:2048  lds\n"
"v_mfma_f32_16x16x32_bf16 v[136:139], a[56:59], a[136:139], v[136:139]\n"
"v_mfma_f32_16x16x32_bf16 v[136:139], a[60:63], a[140:143], v[136:139]\n"
"s_add_u32 s67, s75, 570496\n"
"v_mfma_f32_16x16x32_bf16 v[108:111], a[52:55], a[140:143], v[108:111]\n"
"v_mfma_f32_16x16x32_bf16 v[108:111], a[48:51], a[136:139], v[108:111]\n"
"buffer_load_dwordx4 v11, s[52:55], s67 offen offset:3072  lds\n"
"v_mfma_f32_16x16x32_bf16 v[112:115], a[48:51], a[144:147], v[112:115]\n"
"v_mfma_f32_16x16x32_bf16 v[112:115], a[52:55], a[148:151], v[112:115]\n"
"s_add_u32 m0, s64, 4096\n"
"v_mfma_f32_16x16x32_bf16 v[140:143], a[60:63], a[148:151], v[140:143]\n"
"v_mfma_f32_16x16x32_bf16 v[140:143], a[56:59], a[144:147], v[140:143]\n"
"s_add_u32 s67, s75, 589952\n"
"v_mfma_f32_16x16x32_bf16 a[12:15], a[56:59], a[152:155], a[12:15]\n"
"v_mfma_f32_16x16x32_bf16 a[12:15], a[60:63], a[156:159], a[12:15]\n"
"buffer_load_dwordx4 v11, s[52:55], s67 offen offset:0  lds\n"
"v_mfma_f32_16x16x32_bf16 a[8:11], a[52:55], a[156:159], a[8:11]\n"
"v_mfma_f32_16x16x32_bf16 a[8:11], a[48:51], a[152:155], a[8:11]\n"
"s_add_u32 s67, s75, 605312\n"
"v_mfma_f32_16x16x32_bf16 v[144:147], a[64:67], a[96:99], v[144:147]\n"
"v_mfma_f32_16x16x32_bf16 v[144:147], a[68:71], a[100:103], v[144:147]\n"
"buffer_load_dwordx4 v11, s[52:55], s67 offen offset:1024  lds\n"
"v_mfma_f32_16x16x32_bf16 v[172:175], a[76:79], a[100:103], v[172:175]\n"
"v_mfma_f32_16x16x32_bf16 v[172:175], a[72:75], a[96:99], v[172:175]\n"
"s_add_u32 s67, s75, 620672\n"
"v_mfma_f32_16x16x32_bf16 v[176:179], a[72:75], a[104:107], v[176:179]\n"
"v_mfma_f32_16x16x32_bf16 v[176:179], a[76:79], a[108:111], v[176:179]\n"
"buffer_load_dwordx4 v11, s[52:55], s67 offen offset:2048  lds\n"
"v_mfma_f32_16x16x32_bf16 v[148:151], a[68:71], a[108:111], v[148:151]\n"
"v_mfma_f32_16x16x32_bf16 v[148:151], a[64:67], a[104:107], v[148:151]\n"
"s_add_u32 s67, s75, 636032\n"
"v_mfma_f32_16x16x32_bf16 v[152:155], a[64:67], a[112:115], v[152:155]\n"
"s_sub_u32 s66, 0, s73\n"
"v_mfma_f32_16x16x32_bf16 v[152:155], a[68:71], a[116:119], v[152:155]\n"
"v_mfma_f32_16x16x32_bf16 v[180:183], a[76:79], a[116:119], v[180:183]\n"
"v_add_u32 v12, s66, v7\n"
"v_mfma_f32_16x16x32_bf16 v[180:183], a[72:75], a[112:115], v[180:183]\n"
"buffer_load_dwordx4 v11, s[52:55], s67 offen offset:3072  lds\n"
"v_mfma_f32_16x16x32_bf16 v[184:187], a[72:75], a[120:123], v[184:187]\n"
"s_add_u32 m0, s72, 57344\n"
"v_add_u32 v13, s66, v8\n"
"v_mfma_f32_16x16x32_bf16 v[184:187], a[76:79], a[124:127], v[184:187]\n"
"v_mfma_f32_16x16x32_bf16 v[156:159], a[68:71], a[124:127], v[156:159]\n"
"s_add_u32 s67, s60, 640\n"
"v_mfma_f32_16x16x32_bf16 v[156:159], a[64:67], a[120:123], v[156:159]\n"
"buffer_load_dwordx4 v6, s[48:51], s67 offen offset:0  lds\n"
"v_mfma_f32_16x16x32_bf16 v[160:163], a[64:67], a[128:131], v[160:163]\n"
"v_mfma_f32_16x16x32_bf16 v[160:163], a[68:71], a[132:135], v[160:163]\n"
"s_add_u32 s67, s60, 16000\n"
"v_mfma_f32_16x16x32_bf16 v[188:191], a[76:79], a[132:135], v[188:191]\n"
"v_mfma_f32_16x16x32_bf16 v[188:191], a[72:75], a[128:131], v[188:191]\n"
"buffer_load_dwordx4 v6, s[48:51], s67 offen offset:1024  lds\n"
"v_mfma_f32_16x16x32_bf16 v[192:195], a[72:75], a[136:139], v[192:195]\n"
"v_mfma_f32_16x16x32_bf16 v[192:195], a[76:79], a[140:143], v[192:195]\n"
"s_add_u32 s67, s60, 31360\n"
"v_mfma_f32_16x16x32_bf16 v[164:167], a[68:71], a[140:143], v[164:167]\n"
"v_mfma_f32_16x16x32_bf16 v[164:167], a[64:67], a[136:139], v[164:167]\n"
"buffer_load_dwordx4 v6, s[48:51], s67 offen offset:2048  lds\n"
"v_mfma_f32_16x16x32_bf16 v[168:171], a[64:67], a[144:147], v[168:171]\n"
"v_mfma_f32_16x16x32_bf16 v[168:171], a[68:71], a[148:151], v[168:171]\n"
"s_add_u32 s67, s60, 46720\n"
"v_mfma_f32_16x16x32_bf16 v[196:199], a[76:79], a[148:151], v[196:199]\n"
"v_mfma_f32_16x16x32_bf16 v[196:199], a[72:75], a[144:147], v[196:199]\n"
"v_mfma_f32_16x16x32_bf16 a[20:23], a[72:75], a[152:155], a[20:23]\n"
"buffer_load_dwordx4 v6, s[48:51], s67 offen offset:3072  lds\n"
"v_mfma_f32_16x16x32_bf16 a[20:23], a[76:79], a[156:159], a[20:23]\n"
"s_sub_u32 s70, 1048576, s75\n"
"v_and_b32 v19, 0x20000, v14\n"
"v_mfma_f32_16x16x32_bf16 a[16:19], a[68:71], a[156:159], a[16:19]\n"
"v_add_u32 v14, v19, v14\n"
"v_mfma_f32_16x16x32_bf16 a[16:19], a[64:67], a[152:155], a[16:19]\n"
"s_waitcnt vmcnt(21)\n"
"s_barrier\n"
"v_mfma_f32_16x16x32_bf16 v[200:203], a[80:83], a[96:99], v[200:203]\n"
"s_waitcnt vmcnt(12)\n"
"ds_read_b64_tr_b16 a[32:33], v9 offset:8192\n"
"v_mfma_f32_16x16x32_bf16 v[200:203], a[84:87], a[100:103], v[200:203]\n"
"s_add_u32 s70, s70, s65\n"
"ds_read_b64_tr_b16 a[34:35], v9 offset:8704\n"
"s_getpc_b64 s[96:97]\n"
"L_c10_:\n"
"s_add_u32 s96, s96, L_H6S-L_c10_\n"
"s_addc_u32 s97, s97, 0\n"
"v_mfma_f32_16x16x32_bf16 v[228:231], a[92:95], a[100:103], v[228:231]\n"
"s_lshr_b32 s66, %26, 3\n"
"ds_read_b128 a[100:103], v13 offset:16384\n"
"buffer_load_dword a252, v14, s[52:55], s70 offen\n"
"v_mfma_f32_16x16x32_bf16 v[228:231], a[88:91], a[96:99], v[228:231]\n"
"s_lshl2_add_u32 s66, s66, %16\n"
"ds_read_b128 a[96:99], v12 offset:16384\n"
"v_mfma_f32_16x16x32_bf16 v[232:235], a[88:91], a[104:107], v[232:235]\n"
"s_mul_i32 s66, s66, 128\n"
"ds_read_b64_tr_b16 a[36:37], v9 offset:12288\n"
"v_mfma_f32_16x16x32_bf16 v[232:235], a[92:95], a[108:111], v[232:235]\n"
"s_min_u32 s66, s66, L_headF-L_H6S-128\n"
"ds_read_b64_tr_b16 a[38:39], v9 offset:12800\n"
"v_min_u32 v22, 0, v0\n"
"v_mfma_f32_16x16x32_bf16 v[204:207], a[84:87], a[108:111], v[204:207]\n"
"ds_read_b128 a[108:111], v13 offset:18432\n"
"v_lshl_add_u32 v22, v22, 3, s66\n"
"v_mfma_f32_16x16x32_bf16 v[204:207], a[80:83], a[104:107], v[204:207]\n"
"ds_read_b128 a[104:107], v12 offset:18432\n"
"global_load_dword v23, v22, s[96:97]\n"
"ds_read_b64_tr_b16 a[40:41], v9 offset:8200\n"
"v_mfma_f32_16x16x32_bf16 v[208:211], a[80:83], a[112:115], v[208:211]\n"
"s_add_u32 m0, s64, 8192\n"
"ds_read_b64_tr_b16 a[42:43], v9 offset:8712\n"
"v_mfma_f32_16x16x32_bf16 v[208:211], a[84:87], a[116:119], v[208:211]\n"
"ds_read_b64_tr_b16 a[44:45], v9 offset:12296\n"
"v_mfma_f32_16x16x32_bf16 v[236:239], a[92:95], a[116:119], v[236:239]\n"
"ds_read_b128 a[116:119], v13 offset:20480\n"
"v_mfma_f32_16x16x32_bf16 v[236:239], a[88:91], a[112:115], v[236:239]\n"
"ds_read_b128 a[112:115], v12 offset:20480\n"
"v_mfma_f32_16x16x32_bf16 v[240:243], a[88:91], a[120:123], v[240:243]\n"
"ds_read_b64_tr_b16 a[46:47], v9 offset:12808\n"
"v_mfma_f32_16x16x32_bf16 v[240:243], a[92:95], a[124:127], v[240:243]\n"
"ds_read_b64_tr_b16 a[48:49], v15 offset:8192\n"
"v_mfma_f32_16x16x32_bf16 v[212:215], a[84:87], a[124:127], v[212:215]\n"
"ds_read_b128 a[124:127], v13 offset:22528\n"
"v_mfma_f32_16x16x32_bf16 v[212:215], a[80:83], a[120:123], v[212:215]\n"
"ds_read_b128 a[120:123], v12 offset:22528\n"
"v_mfma_f32_16x16x32_bf16 v[216:219], a[80:83], a[128:131], v[216:219]\n"
"ds_read_b64_tr_b16 a[50:51], v15 offset:8704\n"
"v_mfma_f32_16x16x32_bf16 v[216:219], a[84:87], a[132:135], v[216:219]\n"
"ds_read_b64_tr_b16 a[52:53], v15 offset:12288\n"
"v_mfma_f32_16x16x32_bf16 v[244:247], a[92:95], a[132:135], v[244:247]\n"
"ds_read_b128 a[132:135], v13 offset:24576\n"
"v_mfma_f32_16x16x32_bf16 v[244:247], a[88:91], a[128:131], v[244:247]\n"
"s_add_u32 s67, s75, 655360\n"
"ds_read_b128 a[128:131], v12 offset:24576\n"
"v_mfma_f32_16x16x32_bf16 v[248:251], a[88:91], a[136:139], v[248:251]\n"
"ds_read_b64_tr_b16 a[54:55], v15 offset:12800\n"
"v_mfma_f32_16x16x32_bf16 v[248:251], a[92:95], a[140:143], v[248:251]\n"
"ds_read_b64_tr_b16 a[56:57], v15 offset:8200\n"
"v_mfma_f32_16x16x32_bf16 v[220:223], a[84:87], a[140:143], v[220:223]\n"
"ds_read_b128 a[140:143], v13 offset:26624\n"
"v_mfma_f32_16x16x32_bf16 v[220:223], a[80:83], a[136:139], v[220:223]\n"
"ds_read_b128 a[136:139], v12 offset:26624\n"
"v_mfma_f32_16x16x32_bf16 v[224:227], a[80:83], a[144:147], v[224:227]\n"
"ds_read_b64_tr_b16 a[58:59], v15 offset:8712\n"
"v_mfma_f32_16x16x32_bf16 v[224:227], a[84:87], a[148:151], v[224:227]\n"
"ds_read_b64_tr_b16 a[60:61], v15 offset:12296\n"
"v_mfma_f32_16x16x32_bf16 v[252:255], a[92:95], a[148:151], v[252:255]\n"
"ds_read_b128 a[148:151], v13 offset:28672\n"
"v_mfma_f32_16x16x32_bf16 v[252:255], a[88:91], a[144:147], v[252:255]\n"
"ds_read_b128 a[144:147], v12 offset:28672\n"
"ds_read_b64_tr_b16 a[62:63], v15 offset:12808\n"
"v_mfma_f32_16x16x32_bf16 a[28:31], a[88:91], a[152:155], a[28:31]\n"
"v_mfma_f32_16x16x32_bf16 a[28:31], a[92:95], a[156:159], a[28:31]\n"
"v_mfma_f32_16x16x32_bf16 a[24:27], a[84:87], a[156:159], a[24:27]\n"
"ds_read_b128 a[156:159], v13 offset:30720\n"
"v_mfma_f32_16x16x32_bf16 a[24:27], a[80:83], a[152:155], a[24:27]\n"
"s_waitcnt lgkmcnt(15)\n"
"ds_read_b128 a[152:155], v12 offset:30720\n"
"v_mfma_f32_16x16x32_bf16 v[32:35], a[32:35], a[96:99], v[32:35]\n"
"v_mfma_f32_16x16x32_bf16 v[32:35], a[36:39], a[100:103], v[32:35]\n"
"s_waitcnt lgkmcnt(2)\n"
"v_mfma_f32_16x16x32_bf16 v[60:63], a[44:47], a[100:103], v[60:63]\n"
"buffer_load_dwordx4 v11, s[52:55], s67 offen offset:0  lds\n"
"v_mfma_f32_16x16x32_bf16 v[60:63], a[40:43], a[96:99], v[60:63]\n"
"v_mfma_f32_16x16x32_bf16 v[64:67], a[40:43], a[104:107], v[64:67]\n"
"s_add_u32 s67, s75, 670720\n"
"v_mfma_f32_16x16x32_bf16 v[64:67], a[44:47], a[108:111], v[64:67]\n"
"v_mfma_f32_16x16x32_bf16 v[36:39], a[36:39], a[108:111], v[36:39]\n"
"buffer_load_dwordx4 v11, s[52:55], s67 offen offset:1024  lds\n"
"v_mfma_f32_16x16x32_bf16 v[36:39], a[32:35], a[104:107], v[36:39]\n"
"v_mfma_f32_16x16x32_bf16 v[40:43], a[32:35], a[112:115], v[40:43]\n"
"s_add_u32 s67, s75, 686080\n"
"v_mfma_f32_16x16x32_bf16 v[40:43], a[36:39], a[116:119], v[40:43]\n"
"v_mfma_f32_16x16x32_bf16 v[68:71], a[44:47], a[116:119], v[68:71]\n"
"v_mfma_f32_16x16x32_bf16 v[68:71], a[40:43], a[112:115], v[68:71]\n"
"buffer_load_dwordx4 v11, s[52:55], s67 offen offset:2048  lds\n"
"v_mfma_f32_16x16x32_bf16 v[72:75], a[40:43], a[120:123], v[72:75]\n"
"v_mfma_f32_16x16x32_bf16 v[72:75], a[44:47], a[124:127], v[72:75]\n"
"s_add_u32 s67, s75, 701440\n"
"v_mfma_f32_16x16x32_bf16 v[44:47], a[36:39], a[124:127], v[44:47]\n"
"v_mfma_f32_16x16x32_bf16 v[44:47], a[32:35], a[120:123], v[44:47]\n"
"buffer_load_dwordx4 v11, s[52:55], s67 offen offset:3072  lds\n"
"v_mfma_f32_16x16x32_bf16 v[48:51], a[32:35], a[128:131], v[48:51]\n"
"v_mfma_f32_16x16x32_bf16 v[48:51], a[36:39], a[132:135], v[48:51]\n"
"s_add_u32 m0, s64, 12288\n"
"v_mfma_f32_16x16x32_bf16 v[76:79], a[44:47], a[132:135], v[76:79]\n"
"s_waitcnt vmcnt(10)\n"
"ds_read_b64_tr_b16 a[64:65], v9 offset:0\n"
"v_mfma_f32_16x16x32_bf16 v[76:79], a[40:43], a[128:131], v[76:79]\n"
"s_add_u32 s67, s75, 720896\n"
"ds_read_b64_tr_b16 a[66:67], v9 offset:512\n"
"v_mfma_f32_16x16x32_bf16 v[80:83], a[40:43], a[136:139], v[80:83]\n"
"ds_read_b64_tr_b16 a[68:69], v9 offset:4096\n"
"v_mfma_f32_16x16x32_bf16 v[80:83], a[44:47], a[140:143], v[80:83]\n"
"ds_read_b64_tr_b16 a[70:71], v9 offset:4608\n"
"buffer_load_dwordx4 v11, s[52:55], s67 offen offset:0  lds\n"
"v_mfma_f32_16x16x32_bf16 v[52:55], a[36:39], a[140:143], v[52:55]\n"
"s_add_u32 s67, s75, 736256\n"
"ds_read_b64_tr_b16 a[72:73], v9 offset:8\n"
"v_mfma_f32_16x16x32_bf16 v[52:55], a[32:35], a[136:139], v[52:55]\n"
"s_waitcnt lgkmcnt(5)\n"
"ds_read_b64_tr_b16 a[74:75], v9 offset:520\n"
"v_mfma_f32_16x16x32_bf16 v[56:59], a[32:35], a[144:147], v[56:59]\n"
"ds_read_b64_tr_b16 a[76:77], v9 offset:4104\n"
"v_mfma_f32_16x16x32_bf16 v[56:59], a[36:39], a[148:151], v[56:59]\n"
"ds_read_b64_tr_b16 a[78:79], v9 offset:4616\n"
"v_mfma_f32_16x16x32_bf16 v[84:87], a[44:47], a[148:151], v[84:87]\n"
"buffer_load_dwordx4 v11, s[52:55], s67 offen offset:1024  lds\n"
"v_mfma_f32_16x16x32_bf16 v[84:87], a[40:43], a[144:147], v[84:87]\n"
"ds_read_b64_tr_b16 a[80:81], v15 offset:0\n"
"v_mfma_f32_16x16x32_bf16 a[4:7], a[40:43], a[152:155], a[4:7]\n"
"s_add_u32 s67, s75, 751616\n"
"ds_read_b64_tr_b16 a[82:83], v15 offset:512\n"
"v_mfma_f32_16x16x32_bf16 a[4:7], a[44:47], a[156:159], a[4:7]\n"
"ds_read_b64_tr_b16 a[84:85], v15 offset:4096\n"
"v_mfma_f32_16x16x32_bf16 a[0:3], a[36:39], a[156:159], a[0:3]\n"
"ds_read_b64_tr_b16 a[86:87], v15 offset:4608\n"
"buffer_load_dwordx4 v11, s[52:55], s67 offen offset:2048  lds\n"
"v_mfma_f32_16x16x32_bf16 a[0:3], a[32:35], a[152:155], a[0:3]\n"
"ds_read_b64_tr_b16 a[88:89], v15 offset:8\n"
"v_mfma_f32_16x16x32_bf16 v[88:91], a[48:51], a[96:99], v[88:91]\n"
"s_add_u32 s67, s75, 766976\n"
"ds_read_b64_tr_b16 a[90:91], v15 offset:520\n"
"v_mfma_f32_16x16x32_bf16 v[88:91], a[52:55], a[100:103], v[88:91]\n"
"ds_read_b64_tr_b16 a[92:93], v15 offset:4104\n"
"v_mfma_f32_16x16x32_bf16 v[116:119], a[60:63], a[100:103], v[116:119]\n"
"ds_read_b64_tr_b16 a[94:95], v15 offset:4616\n"
"v_mfma_f32_16x16x32_bf16 v[116:119], a[56:59], a[96:99], v[116:119]\n"
"buffer_load_dwordx4 v11, s[52:55], s67 offen offset:3072  lds\n"
"v_mfma_f32_16x16x32_bf16 v[120:123], a[56:59], a[104:107], v[120:123]\n"
"v_mfma_f32_16x16x32_bf16 v[120:123], a[60:63], a[108:111], v[120:123]\n"
"s_add_u32 m0, s64, 0\n"
"v_mfma_f32_16x16x32_bf16 v[92:95], a[52:55], a[108:111], v[92:95]\n"
"v_mfma_f32_16x16x32_bf16 v[92:95], a[48:51], a[104:107], v[92:95]\n"
"s_add_u32 s67, s75, 655488\n"
"v_mfma_f32_16x16x32_bf16 v[96:99], a[48:51], a[112:115], v[96:99]\n"
"s_waitcnt lgkmcnt(0)\n"
"v_mfma_f32_16x16x32_bf16 v[96:99], a[52:55], a[116:119], v[96:99]\n"
"buffer_load_dwordx4 v11, s[52:55], s67 offen offset:0  lds\n"
"v_mfma_f32_16x16x32_bf16 v[124:127], a[60:63], a[116:119], v[124:127]\n"
"v_mfma_f32_16x16x32_bf16 v[124:127], a[56:59], a[112:115], v[124:127]\n"
"s_add_u32 s67, s75, 670848\n"
"v_mfma_f32_16x16x32_bf16 v[128:131], a[56:59], a[120:123], v[128:131]\n"
"v_mfma_f32_16x16x32_bf16 v[128:131], a[60:63], a[124:127], v[128:131]\n"
"v_mfma_f32_16x16x32_bf16 v[100:103], a[52:55], a[124:127], v[100:103]\n"
"buffer_load_dwordx4 v11, s[52:55], s67 offen offset:1024  lds\n"
"v_mfma_f32_16x16x32_bf16 v[100:103], a[48:51], a[120:123], v[100:103]\n"
"v_mfma_f32_16x16x32_bf16 v[104:107], a[48:51], a[128:131], v[104:107]\n"
"s_add_u32 s67, s75, 686208\n"
"v_mfma_f32_16x16x32_bf16 v[104:107], a[52:55], a[132:135], v[104:107]\n"
"v_mfma_f32_16x16x32_bf16 v[132:135], a[60:63], a[132:135], v[132:135]\n"
"v_mfma_f32_16x16x32_bf16 v[132:135], a[56:59], a[128:131], v[132:135]\n"
"buffer_load_dwordx4 v11, s[52:55], s67 offen offset:2048  lds\n"
"v_mfma_f32_16x16x32_bf16 v[136:139], a[56:59], a[136:139], v[136:139]\n"
"v_mfma_f32_16x16x32_bf16 v[136:139], a[60:63], a[140:143], v[136:139]\n"
"s_add_u32 s67, s75, 701568\n"
"v_mfma_f32_16x16x32_bf16 v[108:111], a[52:55], a[140:143], v[108:111]\n"
"v_mfma_f32_16x16x32_bf16 v[108:111], a[48:51], a[136:139], v[108:111]\n"
"buffer_load_dwordx4 v11, s[52:55], s67 offen offset:3072  lds\n"
"v_mfma_f32_16x16x32_bf16 v[112:115], a[48:51], a[144:147], v[112:115]\n"
"v_mfma_f32_16x16x32_bf16 v[112:115], a[52:55], a[148:151], v[112:115]\n"
"s_add_u32 m0, s64, 4096\n"
"v_mfma_f32_16x16x32_bf16 v[140:143], a[60:63], a[148:151], v[140:143]\n"
"v_mfma_f32_16x16x32_bf16 v[140:143], a[56:59], a[144:147], v[140:143]\n"
"s_add_u32 s67, s75, 721024\n"
"v_mfma_f32_16x16x32_bf16 a[12:15], a[56:59], a[152:155], a[12:15]\n"
"v_mfma_f32_16x16x32_bf16 a[12:15], a[60:63], a[156:159], a[12:15]\n"
"buffer_load_dwordx4 v11, s[52:55], s67 offen offset:0  lds\n"
"v_mfma_f32_16x16x32_bf16 a[8:11], a[52:55], a[156:159], a[8:11]\n"
"v_mfma_f32_16x16x32_bf16 a[8:11], a[48:51], a[152:155], a[8:11]\n"
"s_add_u32 s67, s75, 736384\n"
"v_mfma_f32_16x16x32_bf16 v[144:147], a[64:67], a[96:99], v[144:147]\n"
"v_mfma_f32_16x16x32_bf16 v[144:147], a[68:71], a[100:103], v[144:147]\n"
"buffer_load_dwordx4 v11, s[52:55], s67 offen offset:1024  lds\n"
"v_mfma_f32_16x16x32_bf16 v[172:175], a[76:79], a[100:103], v[172:175]\n"
"v_mfma_f32_16x16x32_bf16 v[172:175], a[72:75], a[96:99], v[172:175]\n"
"s_add_u32 s67, s75, 751744\n"
"v_mfma_f32_16x16x32_bf16 v[176:179], a[72:75], a[104:107], v[176:179]\n"
"v_mfma_f32_16x16x32_bf16 v[176:179], a[76:79], a[108:111], v[176:179]\n"
"buffer_load_dwordx4 v11, s[52:55], s67 offen offset:2048  lds\n"
"v_mfma_f32_16x16x32_bf16 v[148:151], a[68:71], a[108:111], v[148:151]\n"
"v_mfma_f32_16x16x32_bf16 v[148:151], a[64:67], a[104:107], v[148:151]\n"
"s_add_u32 s67, s75, 767104\n"
"v_mfma_f32_16x16x32_bf16 v[152:155], a[64:67], a[112:115], v[152:155]\n"
"s_sub_u32 s66, 40960, s73\n"
"v_mfma_f32_16x16x32_bf16 v[152:155], a[68:71], a[116:119], v[152:155]\n"
"v_mfma_f32_16x16x32_bf16 v[180:183], a[76:79], a[116:119], v[180:183]\n"
"v_add_u32 v12, s66, v7\n"
"v_mfma_f32_16x16x32_bf16 v[180:183], a[72:75], a[112:115], v[180:183]\n"
"buffer_load_dwordx4 v11, s[52:55], s67 offen offset:3072  lds\n"
"v_mfma_f32_16x16x32_bf16 v[184:187], a[72:75], a[120:123], v[184:187]\n"
"s_add_u32 m0, s72, 98304\n"
"v_add_u32 v13, s66, v8\n"
"v_mfma_f32_16x16x32_bf16 v[184:187], a[76:79], a[124:127], v[184:187]\n"
"v_mfma_f32_16x16x32_bf16 v[156:159], a[68:71], a[124:127], v[156:159]\n"
"s_add_u32 s67, s60, 768\n"
"v_mfma_f32_16x16x32_bf16 v[156:159], a[64:67], a[120:123], v[156:159]\n"
"buffer_load_dwordx4 v6, s[48:51], s67 offen offset:0  lds\n"
"v_mfma_f32_16x16x32_bf16 v[160:163], a[64:67], a[128:131], v[160:163]\n"
"v_mfma_f32_16x16x32_bf16 v[160:163], a[68:71], a[132:135], v[160:163]\n"
"s_add_u32 s67, s60, 16128\n"
"v_mfma_f32_16x16x32_bf16 v[188:191], a[76:79], a[132:135], v[188:191]\n"
"v_mfma_f32_16x16x32_bf16 v[188:191], a[72:75], a[128:131], v[188:191]\n"
"buffer_load_dwordx4 v6, s[48:51], s67 offen offset:1024  lds\n"
"v_mfma_f32_16x16x32_bf16 v[192:195], a[72:75], a[136:139], v[192:195]\n"
"v_mfma_f32_16x16x32_bf16 v[192:195], a[76:79], a[140:143], v[192:195]\n"
"s_add_u32 s67, s60, 31488\n"
"v_mfma_f32_16x16x32_bf16 v[164:167], a[68:71], a[140:143], v[164:167]\n"
"v_mfma_f32_16x16x32_bf16 v[164:167], a[64:67], a[136:139], v[164:167]\n"
"buffer_load_dwordx4 v6, s[48:51], s67 offen offset:2048  lds\n"
"v_mfma_f32_16x16x32_bf16 v[168:171], a[64:67], a[144:147], v[168:171]\n"
"v_mfma_f32_16x16x32_bf16 v[168:171], a[68:71], a[148:151], v[168:171]\n"
"s_add_u32 s67, s60, 46848\n"
"v_mfma_f32_16x16x32_bf16 v[196:199], a[76:79], a[148:151], v[196:199]\n"
"v_mfma_f32_16x16x32_bf16 v[196:199], a[72:75], a[144:147], v[196:199]\n"
"v_mfma_f32_16x16x32_bf16 a[20:23], a[72:75], a[152:155], a[20:23]\n"
"buffer_load_dwordx4 v6, s[48:51], s67 offen offset:3072  lds\n"
"v_mfma_f32_16x16x32_bf16 a[20:23], a[76:79], a[156:159], a[20:23]\n"
"v_mfma_f32_16x16x32_bf16 a[16:19], a[68:71], a[156:159], a[16:19]\n"
"s_sub_u32 s70, 1572864, s75\n"
"s_add_u32 s70, s70, s65\n"
"v_mfma_f32_16x16x32_bf16 a[16:19], a[64:67], a[152:155], a[16:19]\n"
"s_waitcnt vmcnt(22)\n"
"s_barrier\n"
"v_mfma_f32_16x16x32_bf16 v[200:203], a[80:83], a[96:99], v[200:203]\n"
"s_waitcnt vmcnt(12)\n"
"ds_read_b64_tr_b16 a[32:33], v9 offset:8192\n"
"v_mfma_f32_16x16x32_bf16 v[200:203], a[84:87], a[100:103], v[200:203]\n"
"ds_read_b64_tr_b16 a[34:35], v9 offset:8704\n"
"v_mfma_f32_16x16x32_bf16 v[228:231], a[92:95], a[100:103], v[228:231]\n"
"ds_read_b128 a[100:103], v13 offset:16384\n"
"buffer_load_dword a252, v14, s[52:55], s70 offen\n"
"v_mfma_f32_16x16x32_bf16 v[228:231], a[88:91], a[96:99], v[228:231]\n"
"ds_read_b128 a[96:99], v12 offset:16384\n"
"v_mfma_f32_16x16x32_bf16 v[232:235], a[88:91], a[104:107], v[232:235]\n"
"buffer_store_dwordx2 v[16:17], v18, s[56:59], %11 offen sc1\n"
"ds_read_b64_tr_b16 a[36:37], v9 offset:12288\n"
"v_mfma_f32_16x16x32_bf16 v[232:235], a[92:95], a[108:111], v[232:235]\n"
"ds_read_b64_tr_b16 a[38:39], v9 offset:12800\n"
"v_mfma_f32_16x16x32_bf16 v[204:207], a[84:87], a[108:111], v[204:207]\n"
"ds_read_b128 a[108:111], v13 offset:18432\n"
"v_mfma_f32_16x16x32_bf16 v[204:207], a[80:83], a[104:107], v[204:207]\n"
"ds_read_b128 a[104:107], v12 offset:18432\n"
"buffer_store_dwordx2 v[16:17], v18, s[56:59], s69 offen sc1\n"
"ds_read_b64_tr_b16 a[40:41], v9 offset:8200\n"
"v_mfma_f32_16x16x32_bf16 v[208:211], a[80:83], a[112:115], v[208:211]\n"
"ds_read_b64_tr_b16 a[42:43], v9 offset:8712\n"
"v_mfma_f32_16x16x32_bf16 v[208:211], a[84:87], a[116:119], v[208:211]\n"
"ds_read_b64_tr_b16 a[44:45], v9 offset:12296\n"
"v_mfma_f32_16x16x32_bf16 v[236:239], a[92:95], a[116:119], v[236:239]\n"
"ds_read_b128 a[116:119], v13 offset:20480\n"
"v_mfma_f32_16x16x32_bf16 v[236:239], a[88:91], a[112:115], v[236:239]\n"
"ds_read_b128 a[112:115], v12 offset:20480\n"
"v_mfma_f32_16x16x32_bf16 v[240:243], a[88:91], a[120:123], v[240:243]\n"
"s_add_u32 m0, s64, 8192\n"
"ds_read_b64_tr_b16 a[46:47], v9 offset:12808\n"
"v_mfma_f32_16x16x32_bf16 v[240:243], a[92:95], a[124:127], v[240:243]\n"
"ds_read_b64_tr_b16 a[48:49], v15 offset:8192\n"
"v_mfma_f32_16x16x32_bf16 v[212:215], a[84:87], a[124:127], v[212:215]\n"
"ds_read_b128 a[124:127], v13 offset:22528\n"
"v_mfma_f32_16x16x32_bf16 v[212:215], a[80:83], a[120:123], v[212:215]\n"
"ds_read_b128 a[120:123], v12 offset:22528\n"
"v_mfma_f32_16x16x32_bf16 v[216:219], a[80:83], a[128:131], v[216:219]\n"
"ds_read_b64_tr_b16 a[50:51], v15 offset:8704\n"
"v_mfma_f32_16x16x32_bf16 v[216:219], a[84:87], a[132:135], v[216:219]\n"
"ds_read_b64_tr_b16 a[52:53], v15 offset:12288\n"
"v_mfma_f32_16x16x32_bf16 v[244:247], a[92:95], a[132:135], v[244:247]\n"
"ds_read_b128 a[132:135], v13 offset:24576\n"
"v_mfma_f32_16x16x32_bf16 v[244:247], a[88:91], a[128:131], v[244:247]\n"
"ds_read_b128 a[128:131], v12 offset:24576\n"
"v_mfma_f32_16x16x32_bf16 v[248:251], a[88:91], a[136:139], v[248:251]\n"
"ds_read_b64_tr_b16 a[54:55], v15 offset:12800\n"
"v_mfma_f32_16x16x32_bf16 v[248:251], a[92:95], a[140:143], v[248:251]\n"
"ds_read_b64_tr_b16 a[56:57], v15 offset:8200\n"
"v_mfma_f32_16x16x32_bf16 v[220:223], a[84:87], a[140:143], v[220:223]\n"
"s_add_u32 s67, s75, 786432\n"
"ds_read_b128 a[140:143], v13 offset:26624\n"
"v_mfma_f32_16x16x32_bf16 v[220:223], a[80:83], a[136:139], v[220:223]\n"
"ds_read_b128 a[136:139], v12 offset:26624\n"
"v_mfma_f32_16x16x32_bf16 v[224:227], a[80:83], a[144:147], v[224:227]\n"
"ds_read_b64_tr_b16 a[58:59], v15 offset:8712\n"
"v_mfma_f32_16x16x32_bf16 v[224:227], a[84:87], a[148:151], v[224:227]\n"
"ds_read_b64_tr_b16 a[60:61], v15 offset:12296\n"
"v_mfma_f32_16x16x32_bf16 v[252:255], a[92:95], a[148:151], v[252:255]\n"
"ds_read_b128 a[148:151], v13 offset:28672\n"
"v_mfma_f32_16x16x32_bf16 v[252:255], a[88:91], a[144:147], v[252:255]\n"
"ds_read_b128 a[144:147], v12 offset:28672\n"
"ds_read_b64_tr_b16 a[62:63], v15 offset:12808\n"
"v_mfma_f32_16x16x32_bf16 a[28:31], a[88:91], a[152:155], a[28:31]\n"
"v_mfma_f32_16x16x32_bf16 a[28:31], a[92:95], a[156:159], a[28:31]\n"
"v_mfma_f32_16x16x32_bf16 a[24:27], a[84:87], a[156:159], a[24:27]\n"
"ds_read_b128 a[156:159], v13 offset:30720\n"
"v_mfma_f32_16x16x32_bf16 a[24:27], a[80:83], a[152:155], a[24:27]\n"
"s_waitcnt lgkmcnt(15)\n"
"ds_read_b128 a[152:155], v12 offset:30720\n"
"v_mfma_f32_16x16x32_bf16 v[32:35], a[32:35], a[96:99], v[32:35]\n"
"v_mfma_f32_16x16x32_bf16 v[32:35], a[36:39], a[100:103], v[32:35]\n"
"s_waitcnt lgkmcnt(2)\n"
"v_mfma_f32_16x16x32_bf16 v[60:63], a[44:47], a[100:103], v[60:63]\n"
"buffer_load_dwordx4 v11, s[52:55], s67 offen offset:0  lds\n"
"v_mfma_f32_16x16x32_bf16 v[60:63], a[40:43], a[96:99], v[60:63]\n"
"v_mfma_f32_16x16x32_bf16 v[64:67], a[40:43], a[104:107], v[64:67]\n"
"s_add_u32 s67, s75, 801792\n"
"v_mfma_f32_16x16x32_bf16 v[64:67], a[44:47], a[108:111], v[64:67]\n"
"v_mfma_f32_16x16x32_bf16 v[36:39], a[36:39], a[108:111], v[36:39]\n"
"buffer_load_dwordx4 v11, s[52:55], s67 offen offset:1024  lds\n"
"v_mfma_f32_16x16x32_bf16 v[36:39], a[32:35], a[104:107], v[36:39]\n"
"v_mfma_f32_16x16x32_bf16 v[40:43], a[32:35], a[112:115], v[40:43]\n"
"s_add_u32 s67, s75, 817152\n"
"v_mfma_f32_16x16x32_bf16 v[40:43], a[36:39], a[116:119], v[40:43]\n"
"v_mfma_f32_16x16x32_bf16 v[68:71], a[44:47], a[116:119], v[68:71]\n"
"s_mov_b32 s30, 0\n"
"v_mfma_f32_16x16x32_bf16 v[68:71], a[40:43], a[112:115], v[68:71]\n"
"buffer_load_dwordx4 v11, s[52:55], s67 offen offset:2048  lds\n"
"v_mfma_f32_16x16x32_bf16 v[72:75], a[40:43], a[120:123], v[72:75]\n"
"s_mov_b32 s31, 0\n"
"v_mfma_f32_16x16x32_bf16 v[72:75], a[44:47], a[124:127], v[72:75]\n"
"s_add_u32 s67, s75, 832512\n"
"v_mfma_f32_16x16x32_bf16 v[44:47], a[36:39], a[124:127], v[44:47]\n"
"v_mfma_f32_16x16x32_bf16 v[44:47], a[32:35], a[120:123], v[44:47]\n"
"v_mfma_f32_16x16x32_bf16 v[48:51], a[32:35], a[128:131], v[48:51]\n"
"buffer_load_dwordx4 v11, s[52:55], s67 offen offset:3072  lds\n"
"v_mfma_f32_16x16x32_bf16 v[48:51], a[36:39], a[132:135], v[48:51]\n"
"s_add_u32 m0, s64, 12288\n"
"v_mfma_f32_16x16x32_bf16 v[76:79], a[44:47], a[132:135], v[76:79]\n"
"s_waitcnt vmcnt(11)\n"
"ds_read_b64_tr_b16 a[64:65], v9 offset:0\n"
"v_mfma_f32_16x16x32_bf16 v[76:79], a[40:43], a[128:131], v[76:79]\n"
"s_add_u32 s67, s75, 851968\n"
"ds_read_b64_tr_b16 a[66:67], v9 offset:512\n"
"v_mfma_f32_16x16x32_bf16 v[80:83], a[40:43], a[136:139], v[80:83]\n"
"ds_read_b64_tr_b16 a[68:69], v9 offset:4096\n"
"v_mfma_f32_16x16x32_bf16 v[80:83], a[44:47], a[140:143], v[80:83]\n"
"ds_read_b64_tr_b16 a[70:71], v9 offset:4608\n"
"buffer_load_dwordx4 v11, s[52:55], s67 offen offset:0  lds\n"
"v_mfma_f32_16x16x32_bf16 v[52:55], a[36:39], a[140:143], v[52:55]\n"
"s_add_u32 s67, s75, 867328\n"
"ds_read_b64_tr_b16 a[72:73], v9 offset:8\n"
"v_mfma_f32_16x16x32_bf16 v[52:55], a[32:35], a[136:139], v[52:55]\n"
"s_waitcnt lgkmcnt(5)\n"
"ds_read_b64_tr_b16 a[74:75], v9 offset:520\n"
"v_mfma_f32_16x16x32_bf16 v[56:59], a[32:35], a[144:147], v[56:59]\n"
"ds_read_b64_tr_b16 a[76:77], v9 offset:4104\n"
"v_mfma_f32_16x16x32_bf16 v[56:59], a[36:39], a[148:151], v[56:59]\n"
"ds_read_b64_tr_b16 a[78:79], v9 offset:4616\n"
"v_mfma_f32_16x16x32_bf16 v[84:87], a[44:47], a[148:151], v[84:87]\n"
"buffer_load_dwordx4 v11, s[52:55], s67 offen offset:1024  lds\n"
"v_mfma_f32_16x16x32_bf16 v[84:87], a[40:43], a[144:147], v[84:87]\n"
"ds_read_b64_tr_b16 a[80:81], v15 offset:0\n"
"v_mfma_f32_16x16x32_bf16 a[4:7], a[40:43], a[152:155], a[4:7]\n"
"s_add_u32 s67, s75, 882688\n"
"ds_read_b64_tr_b16 a[82:83], v15 offset:512\n"
"v_mfma_f32_16x16x32_bf16 a[4:7], a[44:47], a[156:159], a[4:7]\n"
"L_st1568:\n"
"s_cbranch_execz L_st3856\n"
"ds_read_b64_tr_b16 a[84:85], v15 offset:4096\n"
"buffer_load_dwordx4 v11, s[52:55], s67 offen offset:2048  lds\n"
"v_mfma_f32_16x16x32_bf16 a[0:3], a[36:39], a[156:159], a[0:3]\n"
"s_add_u32 s96, s56, %11\n"
"ds_read_b64_tr_b16 a[86:87], v15 offset:4608\n"
"s_addc_u32 s97, s57, 0\n"
"v_mfma_f32_16x16x32_bf16 a[0:3], a[32:35], a[152:155], a[0:3]\n"
"ds_read_b64_tr_b16 a[88:89], v15 offset:8\n"
"v_mfma_f32_16x16x32_bf16 v[88:91], a[48:51], a[96:99], v[88:91]\n"
"s_add_u32 s67, s75, 898048\n"
"ds_read_b64_tr_b16 a[90:91], v15 offset:520\n"
"v_mfma_f32_16x16x32_bf16 v[88:91], a[52:55], a[100:103], v[88:91]\n"
"ds_read_b64_tr_b16 a[92:93], v15 offset:4104\n"
"v_mfma_f32_16x16x32_bf16 v[116:119], a[60:63], a[100:103], v[116:119]\n"
"ds_read_b64_tr_b16 a[94:95], v15 offset:4616\n"
"v_mfma_f32_16x16x32_bf16 v[116:119], a[56:59], a[96:99], v[116:119]\n"
"buffer_load_dwordx4 v11, s[52:55], s67 offen offset:3072  lds\n"
"v_mfma_f32_16x16x32_bf16 v[120:123], a[56:59], a[104:107], v[120:123]\n"
"v_mfma_f32_16x16x32_bf16 v[120:123], a[60:63], a[108:111], v[120:123]\n"
"s_add_u32 m0, s64, 0\n"
"v_mfma_f32_16x16x32_bf16 v[92:95], a[52:55], a[108:111], v[92:95]\n"
"v_mfma_f32_16x16x32_bf16 v[92:95], a[48:51], a[104:107], v[92:95]\n"
"s_add_u32 s67, s75, 786560\n"
"v_mfma_f32_16x16x32_bf16 v[96:99], a[48:51], a[112:115], v[96:99]\n"
"s_waitcnt lgkmcnt(0)\n"
"v_mfma_f32_16x16x32_bf16 v[96:99], a[52:55], a[116:119], v[96:99]\n"
"buffer_load_dwordx4 v11, s[52:55], s67 offen offset:0  lds\n"
"v_mfma_f32_16x16x32_bf16 v[124:127], a[60:63], a[116:119], v[124:127]\n"
"s_load_dword s98, s[96:97], 512 glc\n"
"v_mfma_f32_16x16x32_bf16 v[124:127], a[56:59], a[112:115], v[124:127]\n"
"s_load_dword s99, s[96:97], 8704 glc\n"
"s_add_u32 s67, s75, 801920\n"
"v_mfma_f32_16x16x32_bf16 v[128:131], a[56:59], a[120:123], v[128:131]\n"
"v_mfma_f32_16x16x32_bf16 v[128:131], a[60:63], a[124:127], v[128:131]\n"
"buffer_load_dwordx4 v11, s[52:55], s67 offen offset:1024  lds\n"
"v_mfma_f32_16x16x32_bf16 v[100:103], a[52:55], a[124:127], v[100:103]\n"
"v_mfma_f32_16x16x32_bf16 v[100:103], a[48:51], a[120:123], v[100:103]\n"
"s_add_u32 s67, s75, 817280\n"
"v_mfma_f32_16x16x32_bf16 v[104:107], a[48:51], a[128:131], v[104:107]\n"
"v_mfma_f32_16x16x32_bf16 v[104:107], a[52:55], a[132:135], v[104:107]\n"
"v_mfma_f32_16x16x32_bf16 v[132:135], a[60:63], a[132:135], v[132:135]\n"
"buffer_load_dwordx4 v11, s[52:55], s67 offen offset:2048  lds\n"
"v_mfma_f32_16x16x32_bf16 v[132:135], a[56:59], a[128:131], v[132:135]\n"
"v_mfma_f32_16x16x32_bf16 v[136:139], a[56:59], a[136:139], v[136:139]\n"
"s_add_u32 s67, s75, 832640\n"
"v_mfma_f32_16x16x32_bf16 v[136:139], a[60:63], a[140:143], v[136:139]\n"
"v_mfma_f32_16x16x32_bf16 v[108:111], a[52:55], a[140:143], v[108:111]\n"
"buffer_load_dwordx4 v11, s[52:55], s67 offen offset:3072  lds\n"
"v_mfma_f32_16x16x32_bf16 v[108:111], a[48:51], a[136:139], v[108:111]\n"
"v_mfma_f32_16x16x32_bf16 v[112:115], a[48:51], a[144:147], v[112:115]\n"
"s_add_u32 m0, s64, 4096\n"
"v_mfma_f32_16x16x32_bf16 v[112:115], a[52:55], a[148:151], v[112:115]\n"
"v_mfma_f32_16x16x32_bf16 v[140:143], a[60:63], a[148:151], v[140:143]\n"
"s_add_u32 s67, s75, 852096\n"
"v_mfma_f32_16x16x32_bf16 v[140:143], a[56:59], a[144:147], v[140:143]\n"
"v_mfma_f32_16x16x32_bf16 a[12:15], a[56:59], a[152:155], a[12:15]\n"
"buffer_load_dwordx4 v11, s[52:55], s67 offen offset:0  lds\n"
"v_mfma_f32_16x16x32_bf16 a[12:15], a[60:63], a[156:159], a[12:15]\n"
"v_mfma_f32_16x16x32_bf16 a[8:11], a[52:55], a[156:159], a[8:11]\n"
"s_add_u32 s67, s75, 867456\n"
"v_mfma_f32_16x16x32_bf16 a[8:11], a[48:51], a[152:155], a[8:11]\n"
"v_mfma_f32_16x16x32_bf16 v[144:147], a[64:67], a[96:99], v[144:147]\n"
"buffer_load_dwordx4 v11, s[52:55], s67 offen offset:1024  lds\n"
"v_mfma_f32_16x16x32_bf16 v[144:147], a[68:71], a[100:103], v[144:147]\n"
"v_mfma_f32_16x16x32_bf16 v[172:175], a[76:79], a[100:103], v[172:175]\n"
"s_add_u32 s67, s75, 882816\n"
"v_mfma_f32_16x16x32_bf16 v[172:175], a[72:75], a[96:99], v[172:175]\n"
"v_mfma_f32_16x16x32_bf16 v[176:179], a[72:75], a[104:107], v[176:179]\n"
"buffer_load_dwordx4 v11, s[52:55], s67 offen offset:2048  lds\n"
"v_mfma_f32_16x16x32_bf16 v[176:179], a[76:79], a[108:111], v[176:179]\n"
"v_mfma_f32_16x16x32_bf16 v[148:151], a[68:71], a[108:111], v[148:151]\n"
"s_add_u32 s67, s75, 898176\n"
"v_mfma_f32_16x16x32_bf16 v[148:151], a[64:67], a[104:107], v[148:151]\n"
"v_mfma_f32_16x16x32_bf16 v[152:155], a[64:67], a[112:115], v[152:155]\n"
"s_sub_u32 s66, 81920, s73\n"
"v_mfma_f32_16x16x32_bf16 v[152:155], a[68:71], a[116:119], v[152:155]\n"
"buffer_load_dwordx4 v11, s[52:55], s67 offen offset:3072  lds\n"
"v_mfma_f32_16x16x32_bf16 v[180:183], a[76:79], a[116:119], v[180:183]\n"
"s_add_u32 m0, s72, 139264\n"
"v_add_u32 v12, s66, v7\n"
"v_mfma_f32_16x16x32_bf16 v[180:183], a[72:75], a[112:115], v[180:183]\n"
"v_mfma_f32_16x16x32_bf16 v[184:187], a[72:75], a[120:123], v[184:187]\n"
"s_add_u32 s67, s60, 896\n"
"v_add_u32 v13, s66, v8\n"
"v_mfma_f32_16x16x32_bf16 v[184:187], a[76:79], a[124:127], v[184:187]\n"
"v_mfma_f32_16x16x32_bf16 v[156:159], a[68:71], a[124:127], v[156:159]\n"
"buffer_load_dwordx4 v6, s[48:51], s67 offen offset:0  lds\n"
"v_mfma_f32_16x16x32_bf16 v[156:159], a[64:67], a[120:123], v[156:159]\n"
"v_mfma_f32_16x16x32_bf16 v[160:163], a[64:67], a[128:131], v[160:163]\n"
"s_add_u32 s67, s60, 16256\n"
"v_mfma_f32_16x16x32_bf16 v[160:163], a[68:71], a[132:135], v[160:163]\n"
"v_mfma_f32_16x16x32_bf16 v[188:191], a[76:79], a[132:135], v[188:191]\n"
"buffer_load_dwordx4 v6, s[48:51], s67 offen offset:1024  lds\n"
"v_mfma_f32_16x16x32_bf16 v[188:191], a[72:75], a[128:131], v[188:191]\n"
"v_mfma_f32_16x16x32_bf16 v[192:195], a[72:75], a[136:139], v[192:195]\n"
"s_add_u32 s67, s60, 31616\n"
"v_mfma_f32_16x16x32_bf16 v[192:195], a[76:79], a[140:143], v[192:195]\n"
"v_mfma_f32_16x16x32_bf16 v[164:167], a[68:71], a[140:143], v[164:167]\n"
"v_mfma_f32_16x16x32_bf16 v[164:167], a[64:67], a[136:139], v[164:167]\n"
"buffer_load_dwordx4 v6, s[48:51], s67 offen offset:2048  lds\n"
"v_mfma_f32_16x16x32_bf16 v[168:171], a[64:67], a[144:147], v[168:171]\n"
"v_mfma_f32_16x16x32_bf16 v[168:171], a[68:71], a[148:151], v[168:171]\n"
"s_add_u32 s67, s60, 46976\n"
"v_mfma_f32_16x16x32_bf16 v[196:199], a[76:79], a[148:151], v[196:199]\n"
"v_mfma_f32_16x16x32_bf16 v[196:199], a[72:75], a[144:147], v[196:199]\n"
"buffer_load_dwordx4 v6, s[48:51], s67 offen offset:3072  lds\n"
"v_mfma_f32_16x16x32_bf16 a[20:23], a[72:75], a[152:155], a[20:23]\n"
"v_mfma_f32_16x16x32_bf16 a[20:23], a[76:79], a[156:159], a[20:23]\n"
"s_waitcnt lgkmcnt(0)\n"
"v_mfma_f32_16x16x32_bf16 a[16:19], a[68:71], a[156:159], a[16:19]\n"
"v_mfma_f32_16x16x32_bf16 a[16:19], a[64:67], a[152:155], a[16:19]\n"
"s_waitcnt vmcnt(23)\n"
"s_barrier\n"
"v_mfma_f32_16x16x32_bf16 v[200:203], a[80:83], a[96:99], v[200:203]\n"
"s_waitcnt vmcnt(12)\n"
"ds_read_b64_tr_b16 a[32:33], v9 offset:8192\n"
"v_mfma_f32_16x16x32_bf16 v[200:203], a[84:87], a[100:103], v[200:203]\n"
"ds_read_b64_tr_b16 a[34:35], v9 offset:8704\n"
"v_mfma_f32_16x16x32_bf16 v[228:231], a[92:95], a[100:103], v[228:231]\n"
"s_add_u32 m0, s64, 8192\n"
"ds_read_b128 a[100:103], v13 offset:16384\n"
"v_mfma_f32_16x16x32_bf16 v[228:231], a[88:91], a[96:99], v[228:231]\n"
"ds_read_b128 a[96:99], v12 offset:16384\n"
"v_mfma_f32_16x16x32_bf16 v[232:235], a[88:91], a[104:107], v[232:235]\n"
"ds_read_b64_tr_b16 a[36:37], v9 offset:12288\n"
"v_mfma_f32_16x16x32_bf16 v[232:235], a[92:95], a[108:111], v[232:235]\n"
"ds_read_b64_tr_b16 a[38:39], v9 offset:12800\n"
"v_mfma_f32_16x16x32_bf16 v[204:207], a[84:87], a[108:111], v[204:207]\n"
"ds_read_b128 a[108:111], v13 offset:18432\n"
"v_mfma_f32_16x16x32_bf16 v[204:207], a[80:83], a[104:107], v[204:207]\n"
"ds_read_b128 a[104:107], v12 offset:18432\n"
"ds_read_b64_tr_b16 a[40:41], v9 offset:8200\n"
"v_mfma_f32_16x16x32_bf16 v[208:211], a[80:83], a[112:115], v[208:211]\n"
"ds_read_b64_tr_b16 a[42:43], v9 offset:8712\n"
"v_mfma_f32_16x16x32_bf16 v[208:211], a[84:87], a[116:119], v[208:211]\n"
"ds_read_b64_tr_b16 a[44:45], v9 offset:12296\n"
"v_mfma_f32_16x16x32_bf16 v[236:239], a[92:95], a[116:119], v[236:239]\n"
"ds_read_b128 a[116:119], v13 offset:20480\n"
"v_mfma_f32_16x16x32_bf16 v[236:239], a[88:91], a[112:115], v[236:239]\n"
"ds_read_b128 a[112:115], v12 offset:20480\n"
"v_mfma_f32_16x16x32_bf16 v[240:243], a[88:91], a[120:123], v[240:243]\n"
"ds_read_b64_tr_b16 a[46:47], v9 offset:12808\n"
"v_mfma_f32_16x16x32_bf16 v[240:243], a[92:95], a[124:127], v[240:243]\n"
"ds_read_b64_tr_b16 a[48:49], v15 offset:8192\n"
"v_mfma_f32_16x16x32_bf16 v[212:215], a[84:87], a[124:127], v[212:215]\n"
"ds_read_b128 a[124:127], v13 offset:22528\n"
"v_mfma_f32_16x16x32_bf16 v[212:215], a[80:83], a[120:123], v[212:215]\n"
"ds_read_b128 a[120:123], v12 offset:22528\n"
"v_mfma_f32_16x16x32_bf16 v[216:219], a[80:83], a[128:131], v[216:219]\n"
"s_add_u32 s67, s75, 917504\n"
"ds_read_b64_tr_b16 a[50:51], v15 offset:8704\n"
"v_mfma_f32_16x16x32_bf16 v[216:219], a[84:87], a[132:135], v[216:219]\n"
"ds_read_b64_tr_b16 a[52:53], v15 offset:12288\n"
"v_mfma_f32_16x16x32_bf16 v[244:247], a[92:95], a[132:135], v[244:247]\n"
"ds_read_b128 a[132:135], v13 offset:24576\n"
"v_mfma_f32_16x16x32_bf16 v[244:247], a[88:91], a[128:131], v[244:247]\n"
"ds_read_b128 a[128:131], v12 offset:24576\n"
"v_mfma_f32_16x16x32_bf16 v[248:251], a[88:91], a[136:139], v[248:251]\n"
"ds_read_b64_tr_b16 a[54:55], v15 offset:12800\n"
"v_mfma_f32_16x16x32_bf16 v[248:251], a[92:95], a[140:143], v[248:251]\n"
"ds_read_b64_tr_b16 a[56:57], v15 offset:8200\n"
"v_mfma_f32_16x16x32_bf16 v[220:223], a[84:87], a[140:143], v[220:223]\n"
"ds_read_b128 a[140:143], v13 offset:26624\n"
"v_mfma_f32_16x16x32_bf16 v[220:223], a[80:83], a[136:139], v[220:223]\n"
"ds_read_b128 a[136:139], v12 offset:26624\n"
"v_mfma_f32_16x16x32_bf16 v[224:227], a[80:83], a[144:147], v[224:227]\n"
"ds_read_b64_tr_b16 a[58:59], v15 offset:8712\n"
"v_mfma_f32_16x16x32_bf16 v[224:227], a[84:87], a[148:151], v[224:227]\n"
"ds_read_b64_tr_b16 a[60:61], v15 offset:12296\n"
"v_mfma_f32_16x16x32_bf16 v[252:255], a[92:95], a[148:151], v[252:255]\n"
"ds_read_b128 a[148:151], v13 offset:28672\n"
"v_mfma_f32_16x16x32_bf16 v[252:255], a[88:91], a[144:147], v[252:255]\n"
"ds_read_b128 a[144:147], v12 offset:28672\n"
"ds_read_b64_tr_b16 a[62:63], v15 offset:12808\n"
"v_mfma_f32_16x16x32_bf16 a[28:31], a[88:91], a[152:155], a[28:31]\n"
"v_mfma_f32_16x16x32_bf16 a[28:31], a[92:95], a[156:159], a[28:31]\n"
"v_mfma_f32_16x16x32_bf16 a[24:27], a[84:87], a[156:159], a[24:27]\n"
"ds_read_b128 a[156:159], v13 offset:30720\n"
"v_mfma_f32_16x16x32_bf16 a[24:27], a[80:83], a[152:155], a[24:27]\n"
"s_waitcnt lgkmcnt(15)\n"
"ds_read_b128 a[152:155], v12 offset:30720\n"
"v_mfma_f32_16x16x32_bf16 v[32:35], a[32:35], a[96:99], v[32:35]\n"
"v_mfma_f32_16x16x32_bf16 v[32:35], a[36:39], a[100:103], v[32:35]\n"
"s_waitcnt lgkmcnt(2)\n"
"L_H6S:\n"
"v_mfma_f32_16x16x32_bf16 v[60:63], a[44:47], a[100:103], v[60:63]\n"
"buffer_load_dwordx4 v11, s[52:55], s67 offen offset:0  lds\n"
"v_mfma_f32_16x16x32_bf16 v[60:63], a[40:43], a[96:99], v[60:63]\n"
"v_mfma_f32_16x16x32_bf16 v[64:67], a[40:43], a[104:107], v[64:67]\n"
"s_add_u32 s67, s75, 932864\n"
"v_mfma_f32_16x16x32_bf16 v[64:67], a[44:47], a[108:111], v[64:67]\n"
"v_mfma_f32_16x16x32_bf16 v[36:39], a[36:39], a[108:111], v[36:39]\n"
"buffer_load_dwordx4 v11, s[52:55], s67 offen offset:1024  lds\n"
"v_mfma_f32_16x16x32_bf16 v[36:39], a[32:35], a[104:107], v[36:39]\n"
"v_mfma_f32_16x16x32_bf16 v[40:43], a[32:35], a[112:115], v[40:43]\n"
"s_add_u32 s67, s75, 948224\n"
"v_mfma_f32_16x16x32_bf16 v[40:43], a[36:39], a[116:119], v[40:43]\n"
"v_mfma_f32_16x16x32_bf16 v[68:71], a[44:47], a[116:119], v[68:71]\n"
"v_mfma_f32_16x16x32_bf16 v[68:71], a[40:43], a[112:115], v[68:71]\n"
"buffer_load_dwordx4 v11, s[52:55], s67 offen offset:2048  lds\n"
"v_mfma_f32_16x16x32_bf16 v[72:75], a[40:43], a[120:123], v[72:75]\n"
"v_mfma_f32_16x16x32_bf16 v[72:75], a[44:47], a[124:127], v[72:75]\n"
"s_add_u32 s67, s75, 963584\n"
"v_mfma_f32_16x16x32_bf16 v[44:47], a[36:39], a[124:127], v[44:47]\n"
"v_mfma_f32_16x16x32_bf16 v[44:47], a[32:35], a[120:123], v[44:47]\n"
"v_mfma_f32_16x16x32_bf16 v[48:51], a[32:35], a[128:131], v[48:51]\n"
"buffer_load_dwordx4 v11, s[52:55], s67 offen offset:3072  lds\n"
"v_mfma_f32_16x16x32_bf16 v[48:51], a[36:39], a[132:135], v[48:51]\n"
"s_add_u32 m0, s64, 12288\n"
"v_mfma_f32_16x16x32_bf16 v[76:79], a[44:47], a[132:135], v[76:79]\n"
"s_waitcnt vmcnt(8)\n"
"ds_read_b64_tr_b16 a[64:65], v9 offset:0\n"
"v_mfma_f32_16x16x32_bf16 v[76:79], a[40:43], a[128:131], v[76:79]\n"
"s_add_u32 s67, s75, 983040\n"
"ds_read_b64_tr_b16 a[66:67], v9 offset:512\n"
"v_mfma_f32_16x16x32_bf16 v[80:83], a[40:43], a[136:139], v[80:83]\n"
"ds_read_b64_tr_b16 a[68:69], v9 offset:4096\n"
"v_mfma_f32_16x16x32_bf16 v[80:83], a[44:47], a[140:143], v[80:83]\n"
"ds_read_b64_tr_b16 a[70:71], v9 offset:4608\n"
"buffer_load_dwordx4 v11, s[52:55], s67 offen offset:0  lds\n"
"v_mfma_f32_16x16x32_bf16 v[52:55], a[36:39], a[140:143], v[52:55]\n"
"s_add_u32 s67, s75, 998400\n"
"ds_read_b64_tr_b16 a[72:73], v9 offset:8\n"
"v_mfma_f32_16x16x32_bf16 v[52:55], a[32:35], a[136:139], v[52:55]\n"
"s_waitcnt lgkmcnt(5)\n"
"ds_read_b64_tr_b16 a[74:75], v9 offset:520\n"
"v_mfma_f32_16x16x32_bf16 v[56:59], a[32:35], a[144:147], v[56:59]\n"
"ds_read_b64_tr_b16 a[76:77], v9 offset:4104\n"
"v_mfma_f32_16x16x32_bf16 v[56:59], a[36:39], a[148:151], v[56:59]\n"
"ds_read_b64_tr_b16 a[78:79], v9 offset:4616\n"
"v_mfma_f32_16x16x32_bf16 v[84:87], a[44:47], a[148:151], v[84:87]\n"
"buffer_load_dwordx4 v11, s[52:55], s67 offen offset:1024  lds\n"
"v_mfma_f32_16x16x32_bf16 v[84:87], a[40:43], a[144:147], v[84:87]\n"
"ds_read_b64_tr_b16 a[80:81], v15 offset:0\n"
"v_mfma_f32_16x16x32_bf16 a[4:7], a[40:43], a[152:155], a[4:7]\n"
"s_add_u32 s67, s75, 1013760\n"
"ds_read_b64_tr_b16 a[82:83], v15 offset:512\n"
"v_mfma_f32_16x16x32_bf16 a[4:7], a[44:47], a[156:159], a[4:7]\n"
"ds_read_b64_tr_b16 a[84:85], v15 offset:4096\n"
"v_mfma_f32_16x16x32_bf16 a[0:3], a[36:39], a[156:159], a[0:3]\n"
"ds_read_b64_tr_b16 a[86:87], v15 offset:4608\n"
"buffer_load_dwordx4 v11, s[52:55], s67 offen offset:2048  lds\n"
"v_mfma_f32_16x16x32_bf16 a[0:3], a[32:35], a[152:155], a[0:3]\n"
"ds_read_b64_tr_b16 a[88:89], v15 offset:8\n"
"v_mfma_f32_16x16x32_bf16 v[88:91], a[48:51], a[96:99], v[88:91]\n"
"s_add_u32 s67, s75, 1029120\n"
"ds_read_b64_tr_b16 a[90:91], v15 offset:520\n"
"v_mfma_f32_16x16x32_bf16 v[88:91], a[52:55], a[100:103], v[88:91]\n"
"ds_read_b64_tr_b16 a[92:93], v15 offset:4104\n"
"v_mfma_f32_16x16x32_bf16 v[116:119], a[60:63], a[100:103], v[116:119]\n"
"ds_read_b64_tr_b16 a[94:95], v15 offset:4616\n"
"v_mfma_f32_16x16x32_bf16 v[116:119], a[56:59], a[96:99], v[116:119]\n"
"buffer_load_dwordx4 v11, s[52:55], s67 offen offset:3072  lds\n"
"v_mfma_f32_16x16x32_bf16 v[120:123], a[56:59], a[104:107], v[120:123]\n"
"v_mfma_f32_16x16x32_bf16 v[120:123], a[60:63], a[108:111], v[120:123]\n"
"s_add_u32 m0, s64, 0\n"
"v_mfma_f32_16x16x32_bf16 v[92:95], a[52:55], a[108:111], v[92:95]\n"
"v_mfma_f32_16x16x32_bf16 v[92:95], a[48:51], a[104:107], v[92:95]\n"
"s_add_u32 s67, s75, 917632\n"
"v_mfma_f32_16x16x32_bf16 v[96:99], a[48:51], a[112:115], v[96:99]\n"
"s_waitcnt lgkmcnt(0)\n"
"s_mov_b32 s99, 0\n"
"v_mfma_f32_16x16x32_bf16 v[96:99], a[52:55], a[116:119], v[96:99]\n"
"s_mov_b32 s98, 0\n"
"buffer_load_dwordx4 v11, s[52:55], s67 offen offset:0  lds\n"
"v_mfma_f32_16x16x32_bf16 v[124:127], a[60:63], a[116:119], v[124:127]\n"
"s_memtime s[76:77]\n"
"s_load_dword s98, s[96:97], 8192 glc\n"
"v_mfma_f32_16x16x32_bf16 v[124:127], a[56:59], a[112:115], v[124:127]\n"
"s_add_u32 s67, s75, 932992\n"
"v_mfma_f32_16x16x32_bf16 v[128:131], a[56:59], a[120:123], v[128:131]\n"
"v_mfma_f32_16x16x32_bf16 v[128:131], a[60:63], a[124:127], v[128:131]\n"
"buffer_load_dwordx4 v11, s[52:55], s67 offen offset:1024  lds\n"
"v_mfma_f32_16x16x32_bf16 v[100:103], a[52:55], a[124:127], v[100:103]\n"
"v_mfma_f32_16x16x32_bf16 v[100:103], a[48:51], a[120:123], v[100:103]\n"
"s_add_u32 s67, s75, 948352\n"
"v_mfma_f32_16x16x32_bf16 v[104:107], a[48:51], a[128:131], v[104:107]\n"
"v_mfma_f32_16x16x32_bf16 v[104:107], a[52:55], a[132:135], v[104:107]\n"
"v_mfma_f32_16x16x32_bf16 v[132:135], a[60:63], a[132:135], v[132:135]\n"
"buffer_load_dwordx4 v11, s[52:55], s67 offen offset:2048  lds\n"
"v_mfma_f32_16x16x32_bf16 v[132:135], a[56:59], a[128:131], v[132:135]\n"
"s_mov_b32 s29, s76\n"
"v_mfma_f32_16x16x32_bf16 v[136:139], a[56:59], a[136:139], v[136:139]\n"
"s_add_u32 s67, s75, 963712\n"
"v_mfma_f32_16x16x32_bf16 v[136:139], a[60:63], a[140:143], v[136:139]\n"
"buffer_load_dwordx4 v11, s[52:55], s67 offen offset:3072  lds\n"
"v_mfma_f32_16x16x32_bf16 v[108:111], a[52:55], a[140:143], v[108:111]\n"
"s_sub_u32 s99, s99, s98\n"
"s_sub_u32 s99, s99, s98\n"
"v_mfma_f32_16x16x32_bf16 v[108:111], a[48:51], a[136:139], v[108:111]\n"
"s_sub_u32 s99, s99, s98\n"
"s_add_u32 m0, s64, 4096\n"
"v_mfma_f32_16x16x32_bf16 v[112:115], a[48:51], a[144:147], v[112:115]\n"
"s_sub_u32 s99, s99, s98\n"
"v_mfma_f32_16x16x32_bf16 v[112:115], a[52:55], a[148:151], v[112:115]\n"
"s_add_u32 s67, s75, 983168\n"
"v_mfma_f32_16x16x32_bf16 v[140:143], a[60:63], a[148:151], v[140:143]\n"
"s_sub_u32 s99, s99, s98\n"
"s_sub_u32 s99, s99, s98\n"
"v_mfma_f32_16x16x32_bf16 v[140:143], a[56:59], a[144:147], v[140:143]\n"
"s_sub_u32 s99, s99, s98\n"
"buffer_load_dwordx4 v11, s[52:55], s67 offen offset:0  lds\n"
"v_mfma_f32_16x16x32_bf16 a[12:15], a[56:59], a[152:155], a[12:15]\n"
"s_sub_u32 s99, s99, s98\n"
"v_mfma_f32_16x16x32_bf16 a[12:15], a[60:63], a[156:159], a[12:15]\n"
"s_add_u32 s67, s75, 998528\n"
"v_mfma_f32_16x16x32_bf16 a[8:11], a[52:55], a[156:159], a[8:11]\n"
"s_sub_u32 s99, s99, s98\n"
"s_sub_u32 s99, s99, s98\n"
"v_mfma_f32_16x16x32_bf16 a[8:11], a[48:51], a[152:155], a[8:11]\n"
"s_sub_u32 s99, s99, s98\n"
"v_mfma_f32_16x16x32_bf16 v[144:147], a[64:67], a[96:99], v[144:147]\n"
"s_sub_u32 s99, s99, s98\n"
"v_mfma_f32_16x16x32_bf16 v[144:147], a[68:71], a[100:103], v[144:147]\n"
"buffer_load_dwordx4 v11, s[52:55], s67 offen offset:1024  lds\n"
"v_mfma_f32_16x16x32_bf16 v[172:175], a[76:79], a[100:103], v[172:175]\n"
"s_sub_u32 s99, s99, s98\n"
"s_sub_u32 s99, s99, s98\n"
"v_mfma_f32_16x16x32_bf16 v[172:175], a[72:75], a[96:99], v[172:175]\n"
"s_sub_u32 s99, s99, s98\n"
"s_add_u32 s67, s75, 1013888\n"
"v_mfma_f32_16x16x32_bf16 v[176:179], a[72:75], a[104:107], v[176:179]\n"
"s_sub_u32 s99, s99, s98\n"
"v_mfma_f32_16x16x32_bf16 v[176:179], a[76:79], a[108:111], v[176:179]\n"
"buffer_load_dwordx4 v11, s[52:55], s67 offen offset:2048  lds\n"
"v_mfma_f32_16x16x32_bf16 v[148:151], a[68:71], a[108:111], v[148:151]\n"
"s_sub_u32 s99, s99, s98\n"
"s_sub_u32 s99, s99, s98\n"
"v_mfma_f32_16x16x32_bf16 v[148:151], a[64:67], a[104:107], v[148:151]\n"
"s_sub_u32 s99, s99, s98\n"
"s_add_u32 s67, s75, 1029248\n"
"v_mfma_f32_16x16x32_bf16 v[152:155], a[64:67], a[112:115], v[152:155]\n"
"s_sub_u32 s66, 122880, s73\n"
"s_sub_u32 s99, s99, s98\n"
"v_mfma_f32_16x16x32_bf16 v[152:155], a[68:71], a[116:119], v[152:155]\n"
"buffer_load_dwordx4 v11, s[52:55], s67 offen offset:3072  lds\n"
"v_mfma_f32_16x16x32_bf16 v[180:183], a[76:79], a[116:119], v[180:183]\n"
"s_sub_u32 s99, s99, s98\n"
"v_add_u32 v12, s66, v7\n"
"s_sub_u32 s99, s99, s98\n"
"v_mfma_f32_16x16x32_bf16 v[180:183], a[72:75], a[112:115], v[180:183]\n"
"s_sub_u32 s99, s99, s98\n"
"s_cmp_lt_u32 %16, 2\n"
"s_cselect_b32 s67, 16384, 0\n"
"v_mfma_f32_16x16x32_bf16 v[184:187], a[72:75], a[120:123], v[184:187]\n"
"s_sub_u32 s99, s99, s98\n"
"v_add_u32 v13, s66, v8\n"
"s_add_u32 s67, s67, s72\n"
"v_mfma_f32_16x16x32_bf16 v[184:187], a[76:79], a[124:127], v[184:187]\n"
"s_memtime s[76:77]\n"
"s_add_u32 m0, s67, 16384\n"
"s_sub_u32 s67, 1024, s60\n"
"v_mfma_f32_16x16x32_bf16 v[156:159], a[68:71], a[124:127], v[156:159]\n"
"buffer_load_dwordx4 v6, s[48:51], s67 offen offset:0  lds\n"
"v_mfma_f32_16x16x32_bf16 v[156:159], a[64:67], a[120:123], v[156:159]\n"
"s_waitcnt lgkmcnt(0)\n"
"v_mfma_f32_16x16x32_bf16 v[160:163], a[64:67], a[128:131], v[160:163]\n"
"s_sub_u32 s29, s76, s29\n"
"s_sub_u32 s67, 16384, s60\n"
"v_mfma_f32_16x16x32_bf16 v[160:163], a[68:71], a[132:135], v[160:163]\n"
"s_cmp_lt_u32 s29, 620\n"
"s_cselect_b32 s99, s99, 0\n"
"v_mfma_f32_16x16x32_bf16 v[188:191], a[76:79], a[132:135], v[188:191]\n"
"s_max_u32 s31, s31, s99\n"
"v_mfma_f32_16x16x32_bf16 v[188:191], a[72:75], a[128:131], v[188:191]\n"
"buffer_load_dwordx4 v6, s[48:51], s67 offen offset:1024  lds\n"
"v_mfma_f32_16x16x32_bf16 v[192:195], a[72:75], a[136:139], v[192:195]\n"
"v_mfma_f32_16x16x32_bf16 v[192:195], a[76:79], a[140:143], v[192:195]\n"
"s_sub_u32 s67, 31744, s60\n"
"v_mfma_f32_16x16x32_bf16 v[164:167], a[68:71], a[140:143], v[164:167]\n"
"v_mfma_f32_16x16x32_bf16 v[164:167], a[64:67], a[136:139], v[164:167]\n"
"buffer_load_dwordx4 v6, s[48:51], s67 offen offset:2048  lds\n"
"v_mfma_f32_16x16x32_bf16 v[168:171], a[64:67], a[144:147], v[168:171]\n"
"v_mfma_f32_16x16x32_bf16 v[168:171], a[68:71], a[148:151], v[168:171]\n"
"s_sub_u32 s67, 47104, s60\n"
"v_mfma_f32_16x16x32_bf16 v[196:199], a[76:79], a[148:151], v[196:199]\n"
"v_mfma_f32_16x16x32_bf16 v[196:199], a[72:75], a[144:147], v[196:199]\n"
"buffer_load_dwordx4 v6, s[48:51], s67 offen offset:3072  lds\n"
"v_mfma_f32_16x16x32_bf16 a[20:23], a[72:75], a[152:155], a[20:23]\n"
"v_mfma_f32_16x16x32_bf16 a[20:23], a[76:79], a[156:159], a[20:23]\n"
"s_add_u32 m0, s72, 57216\n"
"v_mfma_f32_16x16x32_bf16 a[16:19], a[68:71], a[156:159], a[16:19]\n"
"v_mfma_f32_16x16x32_bf16 a[16:19], a[64:67], a[152:155], a[16:19]\n"
"s_waitcnt vmcnt(20)\n"
"s_barrier\n"
"v_mfma_f32_16x16x32_bf16 v[200:203], a[80:83], a[96:99], v[200:203]\n"
"s_waitcnt vmcnt(12)\n"
"ds_read_b64_tr_b16 a[32:33], v9 offset:8192\n"
"buffer_load_dwordx4 v6, s[40:43], s80 offen offset:128  lds\n"
"v_mfma_f32_16x16x32_bf16 v[200:203], a[84:87], a[100:103], v[200:203]\n"
"ds_read_b64_tr_b16 a[34:35], v9 offset:8704\n"
"v_mfma_f32_16x16x32_bf16 v[228:231], a[92:95], a[100:103], v[228:231]\n"
"ds_read_b128 a[100:103], v13 offset:16384\n"
"v_mfma_f32_16x16x32_bf16 v[228:231], a[88:91], a[96:99], v[228:231]\n"
"ds_read_b128 a[96:99], v12 offset:16384\n"
"buffer_load_dwordx4 v6, s[40:43], s81 offen offset:1152  lds\n"
"v_mfma_f32_16x16x32_bf16 v[232:235], a[88:91], a[104:107], v[232:235]\n"
"ds_read_b64_tr_b16 a[36:37], v9 offset:12288\n"
"v_mfma_f32_16x16x32_bf16 v[232:235], a[92:95], a[108:111], v[232:235]\n"
"ds_read_b64_tr_b16 a[38:39], v9 offset:12800\n"
"v_mfma_f32_16x16x32_bf16 v[204:207], a[84:87], a[108:111], v[204:207]\n"
"ds_read_b128 a[108:111], v13 offset:18432\n"
"v_mfma_f32_16x16x32_bf16 v[204:207], a[80:83], a[104:107], v[204:207]\n"
"ds_read_b128 a[104:107], v12 offset:18432\n"
"ds_read_b64_tr_b16 a[40:41], v9 offset:8200\n"
"v_mfma_f32_16x16x32_bf16 v[208:211], a[80:83], a[112:115], v[208:211]\n"
"ds_read_b64_tr_b16 a[42:43], v9 offset:8712\n"
"buffer_load_dwordx4 v6, s[40:43], s82 offen offset:2176  lds\n"
"v_mfma_f32_16x16x32_bf16 v[208:211], a[84:87], a[116:119], v[208:211]\n"
"ds_read_b64_tr_b16 a[44:45], v9 offset:12296\n"
"v_mfma_f32_16x16x32_bf16 v[236:239], a[92:95], a[116:119], v[236:239]\n"
"ds_read_b128 a[116:119], v13 offset:20480\n"
"v_mfma_f32_16x16x32_bf16 v[236:239], a[88:91], a[112:115], v[236:239]\n"
"ds_read_b128 a[112:115], v12 offset:20480\n"
"v_mfma_f32_16x16x32_bf16 v[240:243], a[88:91], a[120:123], v[240:243]\n"
"ds_read_b64_tr_b16 a[46:47], v9 offset:12808\n"
"v_mfma_f32_16x16x32_bf16 v[240:243], a[92:95], a[124:127], v[240:243]\n"
"ds_read_b64_tr_b16 a[48:49], v15 offset:8192\n"
"buffer_load_dwordx4 v6, s[40:43], s83 offen offset:3200  lds\n"
"v_mfma_f32_16x16x32_bf16 v[212:215], a[84:87], a[124:127], v[212:215]\n"
"ds_read_b128 a[124:127], v13 offset:22528\n"
"v_mfma_f32_16x16x32_bf16 v[212:215], a[80:83], a[120:123], v[212:215]\n"
"ds_read_b128 a[120:123], v12 offset:22528\n"
"v_mfma_f32_16x16x32_bf16 v[216:219], a[80:83], a[128:131], v[216:219]\n"
"ds_read_b64_tr_b16 a[50:51], v15 offset:8704\n"
"v_mfma_f32_16x16x32_bf16 v[216:219], a[84:87], a[132:135], v[216:219]\n"
"s_add_u32 m0, s64, 8192\n"
"ds_read_b64_tr_b16 a[52:53], v15 offset:12288\n"
"v_mfma_f32_16x16x32_bf16 v[244:247], a[92:95], a[132:135], v[244:247]\n"
"ds_read_b128 a[132:135], v13 offset:24576\n"
"v_mfma_f32_16x16x32_bf16 v[244:247], a[88:91], a[128:131], v[244:247]\n"
"ds_read_b128 a[128:131], v12 offset:24576\n"
"v_mfma_f32_16x16x32_bf16 v[248:251], a[88:91], a[136:139], v[248:251]\n"
"ds_read_b64_tr_b16 a[54:55], v15 offset:12800\n"
"v_mfma_f32_16x16x32_bf16 v[248:251], a[92:95], a[140:143], v[248:251]\n"
"ds_read_b64_tr_b16 a[56:57], v15 offset:8200\n"
"v_mfma_f32_16x16x32_bf16 v[220:223], a[84:87], a[140:143], v[220:223]\n"
"ds_read_b128 a[140:143], v13 offset:26624\n"
"v_mfma_f32_16x16x32_bf16 v[220:223], a[80:83], a[136:139], v[220:223]\n"
"ds_read_b128 a[136:139], v12 offset:26624\n"
"v_mfma_f32_16x16x32_bf16 v[224:227], a[80:83], a[144:147], v[224:227]\n"
"ds_read_b64_tr_b16 a[58:59], v15 offset:8712\n"
"v_mfma_f32_16x16x32_bf16 v[224:227], a[84:87], a[148:151], v[224:227]\n"
"s_sub_u32 s67, 1048576, s75\n"
"ds_read_b64_tr_b16 a[60:61], v15 offset:12296\n"
"v_mfma_f32_16x16x32_bf16 v[252:255], a[92:95], a[148:151], v[252:255]\n"
"ds_read_b128 a[148:151], v13 offset:28672\n"
"v_mfma_f32_16x16x32_bf16 v[252:255], a[88:91], a[144:147], v[252:255]\n"
"ds_read_b128 a[144:147], v12 offset:28672\n"
"ds_read_b64_tr_b16 a[62:63], v15 offset:12808\n"
"v_mfma_f32_16x16x32_bf16 a[28:31], a[88:91], a[152:155], a[28:31]\n"
"v_mfma_f32_16x16x32_bf16 a[28:31], a[92:95], a[156:159], a[28:31]\n"
"v_mfma_f32_16x16x32_bf16 a[24:27], a[84:87], a[156:159], a[24:27]\n"
"ds_read_b128 a[156:159], v13 offset:30720\n"
"v_mfma_f32_16x16x32_bf16 a[24:27], a[80:83], a[152:155], a[24:27]\n"
"s_waitcnt lgkmcnt(15)\n"
"ds_read_b128 a[152:155], v12 offset:30720\n"
"v_mfma_f32_16x16x32_bf16 v[32:35], a[32:35], a[96:99], v[32:35]\n"
"v_mfma_f32_16x16x32_bf16 v[32:35], a[36:39], a[100:103], v[32:35]\n"
"s_waitcnt lgkmcnt(2)\n"
"v_mfma_f32_16x16x32_bf16 v[60:63], a[44:47], a[100:103], v[60:63]\n"
"buffer_load_dwordx4 v11, s[52:55], s67 offen offset:0  lds\n"
"v_mfma_f32_16x16x32_bf16 v[60:63], a[40:43], a[96:99], v[60:63]\n"
"v_mfma_f32_16x16x32_bf16 v[64:67], a[40:43], a[104:107], v[64:67]\n"
"s_sub_u32 s67, 1063936, s75\n"
"v_mfma_f32_16x16x32_bf16 v[64:67], a[44:47], a[108:111], v[64:67]\n"
"v_mfma_f32_16x16x32_bf16 v[36:39], a[36:39], a[108:111], v[36:39]\n"
"v_mfma_f32_16x16x32_bf16 v[36:39], a[32:35], a[104:107], v[36:39]\n"
"buffer_load_dwordx4 v11, s[52:55], s67 offen offset:1024  lds\n"
"v_mfma_f32_16x16x32_bf16 v[40:43], a[32:35], a[112:115], v[40:43]\n"
"v_mfma_f32_16x16x32_bf16 v[40:43], a[36:39], a[116:119], v[40:43]\n"
"s_sub_u32 s67, 1079296, s75\n"
"v_mfma_f32_16x16x32_bf16 v[68:71], a[44:47], a[116:119], v[68:71]\n"
"v_mfma_f32_16x16x32_bf16 v[68:71], a[40:43], a[112:115], v[68:71]\n"
"buffer_load_dwordx4 v11, s[52:55], s67 offen offset:2048  lds\n"
"v_mfma_f32_16x16x32_bf16 v[72:75], a[40:43], a[120:123], v[72:75]\n"
"v_mfma_f32_16x16x32_bf16 v[72:75], a[44:47], a[124:127], v[72:75]\n"
"s_sub_u32 s67, 1094656, s75\n"
"v_mfma_f32_16x16x32_bf16 v[44:47], a[36:39], a[124:127], v[44:47]\n"
"v_mfma_f32_16x16x32_bf16 v[44:47], a[32:35], a[120:123], v[44:47]\n"
"v_mfma_f32_16x16x32_bf16 v[48:51], a[32:35], a[128:131], v[48:51]\n"
"buffer_load_dwordx4 v11, s[52:55], s67 offen offset:3072  lds\n"
"v_mfma_f32_16x16x32_bf16 v[48:51], a[36:39], a[132:135], v[48:51]\n"
"s_add_u32 m0, s64, 12288\n"
"v_mfma_f32_16x16x32_bf16 v[76:79], a[44:47], a[132:135], v[76:79]\n"
"s_waitcnt vmcnt(12)\n"
"ds_read_b64_tr_b16 a[64:65], v9 offset:0\n"
"v_mfma_f32_16x16x32_bf16 v[76:79], a[40:43], a[128:131], v[76:79]\n"
"ds_read_b64_tr_b16 a[66:67], v9 offset:512\n"
"v_mfma_f32_16x16x32_bf16 v[80:83], a[40:43], a[136:139], v[80:83]\n"
"s_sub_u32 s67, 1114112, s75\n"
"ds_read_b64_tr_b16 a[68:69], v9 offset:4096\n"
"v_mfma_f32_16x16x32_bf16 v[80:83], a[44:47], a[140:143], v[80:83]\n"
"ds_read_b64_tr_b16 a[70:71], v9 offset:4608\n"
"v_mfma_f32_16x16x32_bf16 v[52:55], a[36:39], a[140:143], v[52:55]\n"
"ds_read_b64_tr_b16 a[72:73], v9 offset:8\n"
"buffer_load_dwordx4 v11, s[52:55], s67 offen offset:0  lds\n"
"v_mfma_f32_16x16x32_bf16 v[52:55], a[32:35], a[136:139], v[52:55]\n"
"s_waitcnt lgkmcnt(5)\n"
"ds_read_b64_tr_b16 a[74:75], v9 offset:520\n"
"v_mfma_f32_16x16x32_bf16 v[56:59], a[32:35], a[144:147], v[56:59]\n"
"s_sub_u32 s67, 1129472, s75\n"
"ds_read_b64_tr_b16 a[76:77], v9 offset:4104\n"
"v_mfma_f32_16x16x32_bf16 v[56:59], a[36:39], a[148:151], v[56:59]\n"
"ds_read_b64_tr_b16 a[78:79], v9 offset:4616\n"
"v_mfma_f32_16x16x32_bf16 v[84:87], a[44:47], a[148:151], v[84:87]\n"
"buffer_load_dwordx4 v11, s[52:55], s67 offen offset:1024  lds\n"
"v_mfma_f32_16x16x32_bf16 v[84:87], a[40:43], a[144:147], v[84:87]\n"
"ds_read_b64_tr_b16 a[80:81], v15 offset:0\n"
"v_mfma_f32_16x16x32_bf16 a[4:7], a[40:43], a[152:155], a[4:7]\n"
"s_sub_u32 s67, 1144832, s75\n"
"ds_read_b64_tr_b16 a[82:83], v15 offset:512\n"
"v_mfma_f32_16x16x32_bf16 a[4:7], a[44:47], a[156:159], a[4:7]\n"
"ds_read_b64_tr_b16 a[84:85], v15 offset:4096\n"
"v_mfma_f32_16x16x32_bf16 a[0:3], a[36:39], a[156:159], a[0:3]\n"
"ds_read_b64_tr_b16 a[86:87], v15 offset:4608\n"
"buffer_load_dwordx4 v11, s[52:55], s67 offen offset:2048  lds\n"
"v_mfma_f32_16x16x32_bf16 a[0:3], a[32:35], a[152:155], a[0:3]\n"
"ds_read_b64_tr_b16 a[88:89], v15 offset:8\n"
"v_mfma_f32_16x16x32_bf16 v[88:91], a[48:51], a[96:99], v[88:91]\n"
"s_sub_u32 s67, 1160192, s75\n"
"ds_read_b64_tr_b16 a[90:91], v15 offset:520\n"
"v_mfma_f32_16x16x32_bf16 v[88:91], a[52:55], a[100:103], v[88:91]\n"
"ds_read_b64_tr_b16 a[92:93], v15 offset:4104\n"
"v_mfma_f32_16x16x32_bf16 v[116:119], a[60:63], a[100:103], v[116:119]\n"
"ds_read_b64_tr_b16 a[94:95], v15 offset:4616\n"
"v_mfma_f32_16x16x32_bf16 v[116:119], a[56:59], a[96:99], v[116:119]\n"
"buffer_load_dwordx4 v11, s[52:55], s67 offen offset:3072  lds\n"
"v_mfma_f32_16x16x32_bf16 v[120:123], a[56:59], a[104:107], v[120:123]\n"
"v_mfma_f32_16x16x32_bf16 v[120:123], a[60:63], a[108:111], v[120:123]\n"
"s_add_u32 m0, s64, 0\n"
"v_mfma_f32_16x16x32_bf16 v[92:95], a[52:55], a[108:111], v[92:95]\n"
"v_mfma_f32_16x16x32_bf16 v[92:95], a[48:51], a[104:107], v[92:95]\n"
"s_sub_u32 s67, 1048704, s75\n"
"v_mfma_f32_16x16x32_bf16 v[96:99], a[48:51], a[112:115], v[96:99]\n"
"s_waitcnt lgkmcnt(0)\n"
"v_mfma_f32_16x16x32_bf16 v[96:99], a[52:55], a[116:119], v[96:99]\n"
"buffer_load_dwordx4 v11, s[52:55], s67 offen offset:0  lds\n"
"v_mfma_f32_16x16x32_bf16 v[124:127], a[60:63], a[116:119], v[124:127]\n"
"v_mfma_f32_16x16x32_bf16 v[124:127], a[56:59], a[112:115], v[124:127]\n"
"s_sub_u32 s67, 1064064, s75\n"
"v_mfma_f32_16x16x32_bf16 v[128:131], a[56:59], a[120:123], v[128:131]\n"
"v_mfma_f32_16x16x32_bf16 v[128:131], a[60:63], a[124:127], v[128:131]\n"
"L_st2111:\n"
"s_cbranch_execz L_st4479\n"
"v_mfma_f32_16x16x32_bf16 v[100:103], a[52:55], a[124:127], v[100:103]\n"
"buffer_load_dwordx4 v11, s[52:55], s67 offen offset:1024  lds\n"
"v_mfma_f32_16x16x32_bf16 v[100:103], a[48:51], a[120:123], v[100:103]\n"
"v_mfma_f32_16x16x32_bf16 v[104:107], a[48:51], a[128:131], v[104:107]\n"
"s_sub_u32 s67, 1079424, s75\n"
"v_mfma_f32_16x16x32_bf16 v[104:107], a[52:55], a[132:135], v[104:107]\n"
"v_mfma_f32_16x16x32_bf16 v[132:135], a[60:63], a[132:135], v[132:135]\n"
"buffer_load_dwordx4 v11, s[52:55], s67 offen offset:2048  lds\n"
"v_mfma_f32_16x16x32_bf16 v[132:135], a[56:59], a[128:131], v[132:135]\n"
"v_mfma_f32_16x16x32_bf16 v[136:139], a[56:59], a[136:139], v[136:139]\n"
"s_sub_u32 s67, 1094784, s75\n"
"v_mfma_f32_16x16x32_bf16 v[136:139], a[60:63], a[140:143], v[136:139]\n"
"v_mfma_f32_16x16x32_bf16 v[108:111], a[52:55], a[140:143], v[108:111]\n"
"v_mfma_f32_16x16x32_bf16 v[108:111], a[48:51], a[136:139], v[108:111]\n"
"buffer_load_dwordx4 v11, s[52:55], s67 offen offset:3072  lds\n"
"v_mfma_f32_16x16x32_bf16 v[112:115], a[48:51], a[144:147], v[112:115]\n"
"s_add_u32 m0, s64, 4096\n"
"v_mfma_f32_16x16x32_bf16 v[112:115], a[52:55], a[148:151], v[112:115]\n"
"v_mfma_f32_16x16x32_bf16 v[140:143], a[60:63], a[148:151], v[140:143]\n"
"s_sub_u32 s67, 1114240, s75\n"
"v_mfma_f32_16x16x32_bf16 v[140:143], a[56:59], a[144:147], v[140:143]\n"
"v_mfma_f32_16x16x32_bf16 a[12:15], a[56:59], a[152:155], a[12:15]\n"
"buffer_load_dwordx4 v11, s[52:55], s67 offen offset:0  lds\n"
"v_mfma_f32_16x16x32_bf16 a[12:15], a[60:63], a[156:159], a[12:15]\n"
"v_mfma_f32_16x16x32_bf16 a[8:11], a[52:55], a[156:159], a[8:11]\n"
"s_sub_u32 s67, 1129600, s75\n"
"v_mfma_f32_16x16x32_bf16 a[8:11], a[48:51], a[152:155], a[8:11]\n"
"v_mfma_f32_16x16x32_bf16 v[144:147], a[64:67], a[96:99], v[144:147]\n"
"buffer_load_dwordx4 v11, s[52:55], s67 offen offset:1024  lds\n"
"v_mfma_f32_16x16x32_bf16 v[144:147], a[68:71], a[100:103], v[144:147]\n"
"v_mfma_f32_16x16x32_bf16 v[172:175], a[76:79], a[100:103], v[172:175]\n"
"s_sub_u32 s67, 1144960, s75\n"
"v_mfma_f32_16x16x32_bf16 v[172:175], a[72:75], a[96:99], v[172:175]\n"
"v_mfma_f32_16x16x32_bf16 v[176:179], a[72:75], a[104:107], v[176:179]\n"
"v_mfma_f32_16x16x32_bf16 v[176:179], a[76:79], a[108:111], v[176:179]\n"
"s_sub_u32 s71, s72, s64\n"
"v_mfma_f32_16x16x32_bf16 v[148:151], a[68:71], a[108:111], v[148:151]\n"
"buffer_load_dwordx4 v11, s[52:55], s67 offen offset:2048  lds\n"
"v_mfma_f32_16x16x32_bf16 v[148:151], a[64:67], a[104:107], v[148:151]\n"
"s_sub_u32 s67, 1160320, s75\n"
"v_mfma_f32_16x16x32_bf16 v[152:155], a[64:67], a[112:115], v[152:155]\n"
"s_sub_u32 s66, 0, s73\n"
"s_add_u32 s71, s71, 57344\n"
"v_mfma_f32_16x16x32_bf16 v[152:155], a[68:71], a[116:119], v[152:155]\n"
"buffer_load_dwordx4 v11, s[52:55], s67 offen offset:3072  lds\n"
"v_mfma_f32_16x16x32_bf16 v[180:183], a[76:79], a[116:119], v[180:183]\n"
"v_add_u32 v12, s66, v7\n"
"v_mfma_f32_16x16x32_bf16 v[180:183], a[72:75], a[112:115], v[180:183]\n"
"v_add_u32 v20, s71, v7\n"
"v_mfma_f32_16x16x32_bf16 v[184:187], a[72:75], a[120:123], v[184:187]\n"
"v_add_u32 v13, s66, v8\n"
"v_mfma_f32_16x16x32_bf16 v[184:187], a[76:79], a[124:127], v[184:187]\n"
"v_add_u32 v21, s71, v8\n"
"v_mfma_f32_16x16x32_bf16 v[156:159], a[68:71], a[124:127], v[156:159]\n"
"s_cmp_lt_u32 %16, 2\n"
"s_cselect_b32 s67, 16384, 0\n"
"v_mfma_f32_16x16x32_bf16 v[156:159], a[64:67], a[120:123], v[156:159]\n"
"s_add_u32 s67, s67, s72\n"
"v_mfma_f32_16x16x32_bf16 v[160:163], a[64:67], a[128:131], v[160:163]\n"
"s_waitcnt vmcnt(16)\n"
"ds_read_b128 a[192:195], v20 offset:0\n"
"v_mfma_f32_16x16x32_bf16 v[160:163], a[68:71], a[132:135], v[160:163]\n"
"v_mfma_f32_16x16x32_bf16 v[188:191], a[76:79], a[132:135], v[188:191]\n"
"ds_read_b128 a[196:199], v21 offset:0\n"
"v_mfma_f32_16x16x32_bf16 v[188:191], a[72:75], a[128:131], v[188:191]\n"
"ds_read_b128 a[200:203], v20 offset:2048\n"
"v_mfma_f32_16x16x32_bf16 v[192:195], a[72:75], a[136:139], v[192:195]\n"
"s_add_u32 m0, s67, 57344\n"
"v_mfma_f32_16x16x32_bf16 v[192:195], a[76:79], a[140:143], v[192:195]\n"
"ds_read_b128 a[204:207], v21 offset:2048\n"
"v_mfma_f32_16x16x32_bf16 v[164:167], a[68:71], a[140:143], v[164:167]\n"
"v_mfma_f32_16x16x32_bf16 v[164:167], a[64:67], a[136:139], v[164:167]\n"
"v_mfma_f32_16x16x32_bf16 v[168:171], a[64:67], a[144:147], v[168:171]\n"
"s_sub_u32 s67, 1280, s60\n"
"v_mfma_f32_16x16x32_bf16 v[168:171], a[68:71], a[148:151], v[168:171]\n"
"v_mfma_f32_16x16x32_bf16 v[196:199], a[76:79], a[148:151], v[196:199]\n"
"v_mfma_f32_16x16x32_bf16 v[196:199], a[72:75], a[144:147], v[196:199]\n"
"s_waitcnt lgkmcnt(0)\n"
"v_mfma_f32_16x16x32_bf16 a[20:23], a[72:75], a[152:155], a[20:23]\n"
"buffer_load_dwordx4 v6, s[48:51], s67 offen offset:0  lds\n"
"v_mfma_f32_16x16x32_bf16 a[20:23], a[76:79], a[156:159], a[20:23]\n"
"v_mfma_f32_16x16x32_bf16 a[16:19], a[68:71], a[156:159], a[16:19]\n"
"s_sub_u32 s67, 16640, s60\n"
"v_mfma_f32_16x16x32_bf16 a[16:19], a[64:67], a[152:155], a[16:19]\n"
"s_barrier\n"
"s_waitcnt vmcnt(9)\n"
"ds_read_b64_tr_b16 a[32:33], v9 offset:8192\n"
"v_mfma_f32_16x16x32_bf16 v[200:203], a[80:83], a[96:99], v[200:203]\n"
"ds_read_b64_tr_b16 a[34:35], v9 offset:8704\n"
"buffer_load_dwordx4 v6, s[48:51], s67 offen offset:1024  lds\n"
"v_mfma_f32_16x16x32_bf16 v[200:203], a[84:87], a[100:103], v[200:203]\n"
"ds_read_b64_tr_b16 a[36:37], v9 offset:12288\n"
"v_mfma_f32_16x16x32_bf16 v[228:231], a[92:95], a[100:103], v[228:231]\n"
"s_sub_u32 s67, 32000, s60\n"
"ds_read_b128 a[100:103], v13 offset:32768\n"
"v_mfma_f32_16x16x32_bf16 v[228:231], a[88:91], a[96:99], v[228:231]\n"
"ds_read_b128 a[96:99], v12 offset:32768\n"
"v_mfma_f32_16x16x32_bf16 v[232:235], a[88:91], a[104:107], v[232:235]\n"
"ds_read_b64_tr_b16 a[38:39], v9 offset:12800\n"
"v_mfma_f32_16x16x32_bf16 v[232:235], a[92:95], a[108:111], v[232:235]\n"
"ds_read_b64_tr_b16 a[40:41], v9 offset:8200\n"
"buffer_load_dwordx4 v6, s[48:51], s67 offen offset:2048  lds\n"
"v_mfma_f32_16x16x32_bf16 v[204:207], a[84:87], a[108:111], v[204:207]\n"
"ds_read_b128 a[108:111], v13 offset:34816\n"
"v_mfma_f32_16x16x32_bf16 v[204:207], a[80:83], a[104:107], v[204:207]\n"
"s_sub_u32 s67, 47360, s60\n"
"ds_read_b128 a[104:107], v12 offset:34816\n"
"v_mfma_f32_16x16x32_bf16 v[208:211], a[80:83], a[112:115], v[208:211]\n"
"ds_read_b64_tr_b16 a[42:43], v9 offset:8712\n"
"v_mfma_f32_16x16x32_bf16 v[208:211], a[84:87], a[116:119], v[208:211]\n"
"ds_read_b64_tr_b16 a[44:45], v9 offset:12296\n"
"v_mfma_f32_16x16x32_bf16 v[236:239], a[92:95], a[116:119], v[236:239]\n"
"ds_read_b128 a[116:119], v13 offset:36864\n"
"buffer_load_dwordx4 v6, s[48:51], s67 offen offset:3072  lds\n"
"v_mfma_f32_16x16x32_bf16 v[236:239], a[88:91], a[112:115], v[236:239]\n"
"ds_read_b128 a[112:115], v12 offset:36864\n"
"v_mfma_f32_16x16x32_bf16 v[240:243], a[88:91], a[120:123], v[240:243]\n"
"s_add_u32 m0, s64, 16256\n"
"ds_read_b64_tr_b16 a[46:47], v9 offset:12808\n"
"v_mfma_f32_16x16x32_bf16 v[240:243], a[92:95], a[124:127], v[240:243]\n"
"ds_read_b64_tr_b16 a[48:49], v15 offset:8192\n"
"v_mfma_f32_16x16x32_bf16 v[212:215], a[84:87], a[124:127], v[212:215]\n"
"ds_read_b128 a[124:127], v13 offset:38912\n"
"buffer_load_dwordx4 v6, s[40:43], s84 offen offset:128  lds\n"
"v_mfma_f32_16x16x32_bf16 v[212:215], a[80:83], a[120:123], v[212:215]\n"
"ds_read_b128 a[120:123], v12 offset:38912\n"
"v_mfma_f32_16x16x32_bf16 v[216:219], a[80:83], a[128:131], v[216:219]\n"
"ds_read_b64_tr_b16 a[50:51], v15 offset:8704\n"
"v_mfma_f32_16x16x32_bf16 v[216:219], a[84:87], a[132:135], v[216:219]\n"
"ds_read_b64_tr_b16 a[52:53], v15 offset:12288\n"
"v_mfma_f32_16x16x32_bf16 v[244:247], a[92:95], a[132:135], v[244:247]\n"
"ds_read_b128 a[132:135], v13 offset:24576\n"
"buffer_load_dwordx4 v6, s[40:43], s85 offen offset:1152  lds\n"
"v_mfma_f32_16x16x32_bf16 v[244:247], a[88:91], a[128:131], v[244:247]\n"
"ds_read_b128 a[128:131], v12 offset:24576\n"
"v_mfma_f32_16x16x32_bf16 v[248:251], a[88:91], a[136:139], v[248:251]\n"
"ds_read_b64_tr_b16 a[54:55], v15 offset:12800\n"
"v_mfma_f32_16x16x32_bf16 v[248:251], a[92:95], a[140:143], v[248:251]\n"
"ds_read_b64_tr_b16 a[56:57], v15 offset:8200\n"
"v_mfma_f32_16x16x32_bf16 v[220:223], a[84:87], a[140:143], v[220:223]\n"
"ds_read_b128 a[140:143], v13 offset:26624\n"
"buffer_load_dwordx4 v6, s[40:43], s86 offen offset:2176  lds\n"
"v_mfma_f32_16x16x32_bf16 v[220:223], a[80:83], a[136:139], v[220:223]\n"
"ds_read_b128 a[136:139], v12 offset:26624\n"
"v_mfma_f32_16x16x32_bf16 v[224:227], a[80:83], a[144:147], v[224:227]\n"
"ds_read_b64_tr_b16 a[58:59], v15 offset:8712\n"
"v_mfma_f32_16x16x32_bf16 v[224:227], a[84:87], a[148:151], v[224:227]\n"
"ds_read_b64_tr_b16 a[60:61], v15 offset:12296\n"
"v_mfma_f32_16x16x32_bf16 v[252:255], a[92:95], a[148:151], v[252:255]\n"
"ds_read_b128 a[148:151], v13 offset:28672\n"
"v_mfma_f32_16x16x32_bf16 v[252:255], a[88:91], a[144:147], v[252:255]\n"
"ds_read_b128 a[144:147], v12 offset:28672\n"
"ds_read_b64_tr_b16 a[62:63], v15 offset:12808\n"
"v_mfma_f32_16x16x32_bf16 a[28:31], a[88:91], a[152:155], a[28:31]\n"
"buffer_load_dwordx4 v6, s[40:43], s87 offen offset:3200  lds\n"
"v_mfma_f32_16x16x32_bf16 a[28:31], a[92:95], a[156:159], a[28:31]\n"
"v_mfma_f32_16x16x32_bf16 a[24:27], a[84:87], a[156:159], a[24:27]\n"
"s_add_u32 m0, s64, 8192\n"
"ds_read_b128 a[156:159], v13 offset:30720\n"
"v_mfma_f32_16x16x32_bf16 a[24:27], a[80:83], a[152:155], a[24:27]\n"
"s_waitcnt lgkmcnt(15)\n"
"ds_read_b128 a[152:155], v12 offset:30720\n"
"v_mfma_f32_16x16x32_bf16 v[32:35], a[32:35], a[96:99], v[32:35]\n"
"s_sub_u32 s67, 1310720, s75\n"
"v_mfma_f32_16x16x32_bf16 v[32:35], a[36:39], a[100:103], v[32:35]\n"
"s_waitcnt lgkmcnt(2)\n"
"v_mfma_f32_16x16x32_bf16 v[60:63], a[44:47], a[100:103], v[60:63]\n"
"buffer_load_dwordx4 v11, s[52:55], s67 offen offset:0  lds\n"
"v_mfma_f32_16x16x32_bf16 v[60:63], a[40:43], a[96:99], v[60:63]\n"
"v_mfma_f32_16x16x32_bf16 v[64:67], a[40:43], a[104:107], v[64:67]\n"
"s_sub_u32 s67, 1326080, s75\n"
"v_mfma_f32_16x16x32_bf16 v[64:67], a[44:47], a[108:111], v[64:67]\n"
"v_mfma_f32_16x16x32_bf16 v[36:39], a[36:39], a[108:111], v[36:39]\n"
"buffer_load_dwordx4 v11, s[52:55], s67 offen offset:1024  lds\n"
"v_mfma_f32_16x16x32_bf16 v[36:39], a[32:35], a[104:107], v[36:39]\n"
"v_mfma_f32_16x16x32_bf16 v[40:43], a[32:35], a[112:115], v[40:43]\n"
"s_sub_u32 s67, 1341440, s75\n"
"v_mfma_f32_16x16x32_bf16 v[40:43], a[36:39], a[116:119], v[40:43]\n"
"v_mfma_f32_16x16x32_bf16 v[68:71], a[44:47], a[116:119], v[68:71]\n"
"v_mfma_f32_16x16x32_bf16 v[68:71], a[40:43], a[112:115], v[68:71]\n"
"buffer_load_dwordx4 v11, s[52:55], s67 offen offset:2048  lds\n"
"v_mfma_f32_16x16x32_bf16 v[72:75], a[40:43], a[120:123], v[72:75]\n"
"v_mfma_f32_16x16x32_bf16 v[72:75], a[44:47], a[124:127], v[72:75]\n"
"s_sub_u32 s67, 1356800, s75\n"
"v_mfma_f32_16x16x32_bf16 v[44:47], a[36:39], a[124:127], v[44:47]\n"
"v_mfma_f32_16x16x32_bf16 v[44:47], a[32:35], a[120:123], v[44:47]\n"
"buffer_load_dwordx4 v11, s[52:55], s67 offen offset:3072  lds\n"
"v_mfma_f32_16x16x32_bf16 v[48:51], a[32:35], a[128:131], v[48:51]\n"
"v_mfma_f32_16x16x32_bf16 v[48:51], a[36:39], a[132:135], v[48:51]\n"
"s_add_u32 m0, s64, 12288\n"
"v_mfma_f32_16x16x32_bf16 v[76:79], a[44:47], a[132:135], v[76:79]\n"
"s_waitcnt vmcnt(12)\n"
"ds_read_b64_tr_b16 a[64:65], v9 offset:0\n"
"v_mfma_f32_16x16x32_bf16 v[76:79], a[40:43], a[128:131], v[76:79]\n"
"s_sub_u32 s67, 1376256, s75\n"
"ds_read_b64_tr_b16 a[66:67], v9 offset:512\n"
"v_mfma_f32_16x16x32_bf16 v[80:83], a[40:43], a[136:139], v[80:83]\n"
"ds_read_b64_tr_b16 a[68:69], v9 offset:4096\n"
"v_mfma_f32_16x16x32_bf16 v[80:83], a[44:47], a[140:143], v[80:83]\n"
"ds_read_b64_tr_b16 a[70:71], v9 offset:4608\n"
"buffer_load_dwordx4 v11, s[52:55], s67 offen offset:0  lds\n"
"v_mfma_f32_16x16x32_bf16 v[52:55], a[36:39], a[140:143], v[52:55]\n"
"s_sub_u32 s67, 1391616, s75\n"
"ds_read_b64_tr_b16 a[72:73], v9 offset:8\n"
"v_mfma_f32_16x16x32_bf16 v[52:55], a[32:35], a[136:139], v[52:55]\n"
"s_waitcnt lgkmcnt(5)\n"
"ds_read_b64_tr_b16 a[74:75], v9 offset:520\n"
"v_mfma_f32_16x16x32_bf16 v[56:59], a[32:35], a[144:147], v[56:59]\n"
"ds_read_b64_tr_b16 a[76:77], v9 offset:4104\n"
"v_mfma_f32_16x16x32_bf16 v[56:59], a[36:39], a[148:151], v[56:59]\n"
"ds_read_b64_tr_b16 a[78:79], v9 offset:4616\n"
"v_mfma_f32_16x16x32_bf16 v[84:87], a[44:47], a[148:151], v[84:87]\n"
"buffer_load_dwordx4 v11, s[52:55], s67 offen offset:1024  lds\n"
"v_mfma_f32_16x16x32_bf16 v[84:87], a[40:43], a[144:147], v[84:87]\n"
"ds_read_b64_tr_b16 a[80:81], v15 offset:0\n"
"v_mfma_f32_16x16x32_bf16 a[4:7], a[40:43], a[152:155], a[4:7]\n"
"s_sub_u32 s67, 1406976, s75\n"
"ds_read_b64_tr_b16 a[82:83], v15 offset:512\n"
"v_mfma_f32_16x16x32_bf16 a[4:7], a[44:47], a[156:159], a[4:7]\n"
"ds_read_b64_tr_b16 a[84:85], v15 offset:4096\n"
"v_mfma_f32_16x16x32_bf16 a[0:3], a[36:39], a[156:159], a[0:3]\n"
"ds_read_b64_tr_b16 a[86:87], v15 offset:4608\n"
"buffer_load_dwordx4 v11, s[52:55], s67 offen offset:2048  lds\n"
"v_mfma_f32_16x16x32_bf16 a[0:3], a[32:35], a[152:155], a[0:3]\n"
"ds_read_b64_tr_b16 a[88:89], v15 offset:8\n"
"v_mfma_f32_16x16x32_bf16 v[88:91], a[48:51], a[96:99], v[88:91]\n"
"s_sub_u32 s67, 1422336, s75\n"
"ds_read_b64_tr_b16 a[90:91], v15 offset:520\n"
"v_mfma_f32_16x16x32_bf16 v[88:91], a[52:55], a[100:103], v[88:91]\n"
"ds_read_b64_tr_b16 a[92:93], v15 offset:4104\n"
"v_mfma_f32_16x16x32_bf16 v[116:119], a[60:63], a[100:103], v[116:119]\n"
"ds_read_b64_tr_b16 a[94:95], v15 offset:4616\n"
"v_mfma_f32_16x16x32_bf16 v[116:119], a[56:59], a[96:99], v[116:119]\n"
"buffer_load_dwordx4 v11, s[52:55], s67 offen offset:3072  lds\n"
"v_mfma_f32_16x16x32_bf16 v[120:123], a[56:59], a[104:107], v[120:123]\n"
"v_mfma_f32_16x16x32_bf16 v[120:123], a[60:63], a[108:111], v[120:123]\n"
"s_add_u32 m0, s64, 0\n"
"v_mfma_f32_16x16x32_bf16 v[92:95], a[52:55], a[108:111], v[92:95]\n"
"v_mfma_f32_16x16x32_bf16 v[92:95], a[48:51], a[104:107], v[92:95]\n"
"s_sub_u32 s67, 1310848, s75\n"
"v_mfma_f32_16x16x32_bf16 v[96:99], a[48:51], a[112:115], v[96:99]\n"
"s_waitcnt lgkmcnt(0)\n"
"s_mov_b32 s99, 0\n"
"v_mfma_f32_16x16x32_bf16 v[96:99], a[52:55], a[116:119], v[96:99]\n"
"s_mov_b32 s98, 0\n"
"buffer_load_dwordx4 v11, s[52:55], s67 offen offset:0  lds\n"
"v_mfma_f32_16x16x32_bf16 v[124:127], a[60:63], a[116:119], v[124:127]\n"
"s_memtime s[76:77]\n"
"s_load_dword s98, s[96:97], 0 glc\n"
"v_mfma_f32_16x16x32_bf16 v[124:127], a[56:59], a[112:115], v[124:127]\n"
"s_sub_u32 s67, 1326208, s75\n"
"v_mfma_f32_16x16x32_bf16 v[128:131], a[56:59], a[120:123], v[128:131]\n"
"v_mfma_f32_16x16x32_bf16 v[128:131], a[60:63], a[124:127], v[128:131]\n"
"v_mfma_f32_16x16x32_bf16 v[100:103], a[52:55], a[124:127], v[100:103]\n"
"buffer_load_dwordx4 v11, s[52:55], s67 offen offset:1024  lds\n"
"v_mfma_f32_16x16x32_bf16 v[100:103], a[48:51], a[120:123], v[100:103]\n"
"v_mfma_f32_16x16x32_bf16 v[104:107], a[48:51], a[128:131], v[104:107]\n"
"s_sub_u32 s67, 1341568, s75\n"
"v_mfma_f32_16x16x32_bf16 v[104:107], a[52:55], a[132:135], v[104:107]\n"
"v_mfma_f32_16x16x32_bf16 v[132:135], a[60:63], a[132:135], v[132:135]\n"
"v_mfma_f32_16x16x32_bf16 v[132:135], a[56:59], a[128:131], v[132:135]\n"
"s_mov_b32 s29, s76\n"
"v_mfma_f32_16x16x32_bf16 v[136:139], a[56:59], a[136:139], v[136:139]\n"
"buffer_load_dwordx4 v11, s[52:55], s67 offen offset:2048  lds\n"
"v_mfma_f32_16x16x32_bf16 v[136:139], a[60:63], a[140:143], v[136:139]\n"
"s_sub_u32 s67, 1356928, s75\n"
"v_mfma_f32_16x16x32_bf16 v[108:111], a[52:55], a[140:143], v[108:111]\n"
"s_sub_u32 s99, s99, s98\n"
"s_sub_u32 s99, s99, s98\n"
"v_mfma_f32_16x16x32_bf16 v[108:111], a[48:51], a[136:139], v[108:111]\n"
"s_sub_u32 s99, s99, s98\n"
"v_mfma_f32_16x16x32_bf16 v[112:115], a[48:51], a[144:147], v[112:115]\n"
"s_sub_u32 s99, s99, s98\n"
"buffer_load_dwordx4 v11, s[52:55], s67 offen offset:3072  lds\n"
"v_mfma_f32_16x16x32_bf16 v[112:115], a[52:55], a[148:151], v[112:115]\n"
"s_add_u32 m0, s64, 4096\n"
"v_mfma_f32_16x16x32_bf16 v[140:143], a[60:63], a[148:151], v[140:143]\n"
"s_sub_u32 s99, s99, s98\n"
"s_sub_u32 s99, s99, s98\n"
"v_mfma_f32_16x16x32_bf16 v[140:143], a[56:59], a[144:147], v[140:143]\n"
"s_sub_u32 s99, s99, s98\n"
"s_sub_u32 s67, 1376384, s75\n"
"v_mfma_f32_16x16x32_bf16 a[12:15], a[56:59], a[152:155], a[12:15]\n"
"s_sub_u32 s99, s99, s98\n"
"v_mfma_f32_16x16x32_bf16 a[12:15], a[60:63], a[156:159], a[12:15]\n"
"buffer_load_dwordx4 v11, s[52:55], s67 offen offset:0  lds\n"
"v_mfma_f32_16x16x32_bf16 a[8:11], a[52:55], a[156:159], a[8:11]\n"
"s_sub_u32 s99, s99, s98\n"
"s_sub_u32 s99, s99, s98\n"
"v_mfma_f32_16x16x32_bf16 a[8:11], a[48:51], a[152:155], a[8:11]\n"
"s_sub_u32 s99, s99, s98\n"
"s_sub_u32 s67, 1391744, s75\n"
"v_mfma_f32_16x16x32_bf16 v[144:147], a[64:67], a[96:99], v[144:147]\n"
"s_sub_u32 s99, s99, s98\n"
"v_mfma_f32_16x16x32_bf16 v[144:147], a[68:71], a[100:103], v[144:147]\n"
"buffer_load_dwordx4 v11, s[52:55], s67 offen offset:1024  lds\n"
"v_mfma_f32_16x16x32_bf16 v[172:175], a[76:79], a[100:103], v[172:175]\n"
"s_sub_u32 s99, s99, s98\n"
"s_sub_u32 s99, s99, s98\n"
"v_mfma_f32_16x16x32_bf16 v[172:175], a[72:75], a[96:99], v[172:175]\n"
"s_sub_u32 s99, s99, s98\n"
"s_sub_u32 s67, 1407104, s75\n"
"v_mfma_f32_16x16x32_bf16 v[176:179], a[72:75], a[104:107], v[176:179]\n"
"s_sub_u32 s99, s99, s98\n"
"v_mfma_f32_16x16x32_bf16 v[176:179], a[76:79], a[108:111], v[176:179]\n"
"buffer_load_dwordx4 v11, s[52:55], s67 offen offset:2048  lds\n"
"v_mfma_f32_16x16x32_bf16 v[148:151], a[68:71], a[108:111], v[148:151]\n"
"s_sub_u32 s99, s99, s98\n"
"s_sub_u32 s99, s99, s98\n"
"v_mfma_f32_16x16x32_bf16 v[148:151], a[64:67], a[104:107], v[148:151]\n"
"s_sub_u32 s99, s99, s98\n"
"s_sub_u32 s71, s64, s64\n"
"v_mfma_f32_16x16x32_bf16 v[152:155], a[64:67], a[112:115], v[152:155]\n"
"s_sub_u32 s66, 40960, s73\n"
"s_sub_u32 s99, s99, s98\n"
"v_mfma_f32_16x16x32_bf16 v[152:155], a[68:71], a[116:119], v[152:155]\n"
"s_add_u32 s71, s71, 16384\n"
"s_sub_u32 s67, 1422464, s75\n"
"v_mfma_f32_16x16x32_bf16 v[180:183], a[76:79], a[116:119], v[180:183]\n"
"s_sub_u32 s99, s99, s98\n"
"v_add_u32 v12, s66, v7\n"
"s_sub_u32 s99, s99, s98\n"
"v_mfma_f32_16x16x32_bf16 v[180:183], a[72:75], a[112:115], v[180:183]\n"
"s_sub_u32 s99, s99, s98\n"
"buffer_load_dwordx4 v11, s[52:55], s67 offen offset:3072  lds\n"
"v_mfma_f32_16x16x32_bf16 v[184:187], a[72:75], a[120:123], v[184:187]\n"
"s_sub_u32 s99, s99, s98\n"
"v_add_u32 v13, s66, v8\n"
"s_cmp_lt_u32 %16, 2\n"
"s_cselect_b32 s67, 16384, 0\n"
"v_mfma_f32_16x16x32_bf16 v[184:187], a[76:79], a[124:127], v[184:187]\n"
"s_memtime s[76:77]\n"
"v_add_u32 v20, s71, v7\n"
"s_add_u32 s67, s67, s72\n"
"v_mfma_f32_16x16x32_bf16 v[156:159], a[68:71], a[124:127], v[156:159]\n"
"s_add_u32 m0, s67, 98304\n"
"s_sub_u32 s67, 1536, s60\n"
"v_mfma_f32_16x16x32_bf16 v[156:159], a[64:67], a[120:123], v[156:159]\n"
"s_waitcnt lgkmcnt(0)\n"
"buffer_load_dwordx4 v6, s[48:51], s67 offen offset:0  lds\n"
"v_add_u32 v21, s71, v8\n"
"v_mfma_f32_16x16x32_bf16 v[160:163], a[64:67], a[128:131], v[160:163]\n"
"s_sub_u32 s29, s76, s29\n"
"s_sub_u32 s67, 16896, s60\n"
"v_mfma_f32_16x16x32_bf16 v[160:163], a[68:71], a[132:135], v[160:163]\n"
"s_cmp_lt_u32 s29, 620\n"
"s_cselect_b32 s99, s99, 0\n"
"v_mfma_f32_16x16x32_bf16 v[188:191], a[76:79], a[132:135], v[188:191]\n"
"s_max_u32 s30, s30, s99\n"
"v_mfma_f32_16x16x32_bf16 v[188:191], a[72:75], a[128:131], v[188:191]\n"
"s_waitcnt vmcnt(17)\n"
"ds_read_b128 a[208:211], v20 offset:0\n"
"buffer_load_dwordx4 v6, s[48:51], s67 offen offset:1024  lds\n"
"v_mfma_f32_16x16x32_bf16 v[192:195], a[72:75], a[136:139], v[192:195]\n"
"ds_read_b128 a[212:215], v21 offset:0\n"
"v_mfma_f32_16x16x32_bf16 v[192:195], a[76:79], a[140:143], v[192:195]\n"
"s_sub_u32 s67, 32256, s60\n"
"ds_read_b128 a[216:219], v20 offset:2048\n"
"v_mfma_f32_16x16x32_bf16 v[164:167], a[68:71], a[140:143], v[164:167]\n"
"v_mfma_f32_16x16x32_bf16 v[164:167], a[64:67], a[136:139], v[164:167]\n"
"ds_read_b128 a[220:223], v21 offset:2048\n"
"v_mfma_f32_16x16x32_bf16 v[168:171], a[64:67], a[144:147], v[168:171]\n"
"buffer_load_dwordx4 v6, s[48:51], s67 offen offset:2048  lds\n"
"v_mfma_f32_16x16x32_bf16 v[168:171], a[68:71], a[148:151], v[168:171]\n"
"v_mfma_f32_16x16x32_bf16 v[196:199], a[76:79], a[148:151], v[196:199]\n"
"s_sub_u32 s67, 47616, s60\n"
"v_mfma_f32_16x16x32_bf16 v[196:199], a[72:75], a[144:147], v[196:199]\n"
"v_mfma_f32_16x16x32_bf16 a[20:23], a[72:75], a[152:155], a[20:23]\n"
"buffer_load_dwordx4 v6, s[48:51], s67 offen offset:3072  lds\n"
"v_mfma_f32_16x16x32_bf16 a[20:23], a[76:79], a[156:159], a[20:23]\n"
"s_waitcnt lgkmcnt(0)\n"
"v_mfma_f32_16x16x32_bf16 a[16:19], a[68:71], a[156:159], a[16:19]\n"
"s_add_u32 m0, s64, 20352\n"
"v_mfma_f32_16x16x32_bf16 a[16:19], a[64:67], a[152:155], a[16:19]\n"
"s_barrier\n"
"s_waitcnt vmcnt(12)\n"
"ds_read_b64_tr_b16 a[32:33], v9 offset:8192\n"
"v_mfma_f32_16x16x32_bf16 v[200:203], a[80:83], a[96:99], v[200:203]\n"
"ds_read_b64_tr_b16 a[34:35], v9 offset:8704\n"
"v_mfma_f32_16x16x32_bf16 v[200:203], a[84:87], a[100:103], v[200:203]\n"
"ds_read_b64_tr_b16 a[36:37], v9 offset:12288\n"
"buffer_load_dwordx4 v6, s[40:43], s88 offen offset:128  lds\n"
"v_mfma_f32_16x16x32_bf16 v[228:231], a[92:95], a[100:103], v[228:231]\n"
"ds_read_b128 a[100:103], v13 offset:32768\n"
"v_mfma_f32_16x16x32_bf16 v[228:231], a[88:91], a[96:99], v[228:231]\n"
"ds_read_b128 a[96:99], v12 offset:32768\n"
"v_mfma_f32_16x16x32_bf16 v[232:235], a[88:91], a[104:107], v[232:235]\n"
"ds_read_b64_tr_b16 a[38:39], v9 offset:12800\n"
"buffer_load_dwordx4 v6, s[40:43], s89 offen offset:1152  lds\n"
"v_mfma_f32_16x16x32_bf16 v[232:235], a[92:95], a[108:111], v[232:235]\n"
"ds_read_b64_tr_b16 a[40:41], v9 offset:8200\n"
"v_mfma_f32_16x16x32_bf16 v[204:207], a[84:87], a[108:111], v[204:207]\n"
"ds_read_b128 a[108:111], v13 offset:34816\n"
"v_mfma_f32_16x16x32_bf16 v[204:207], a[80:83], a[104:107], v[204:207]\n"
"ds_read_b128 a[104:107], v12 offset:34816\n"
"v_mfma_f32_16x16x32_bf16 v[208:211], a[80:83], a[112:115], v[208:211]\n"
"ds_read_b64_tr_b16 a[42:43], v9 offset:8712\n"
"v_mfma_f32_16x16x32_bf16 v[208:211], a[84:87], a[116:119], v[208:211]\n"
"ds_read_b64_tr_b16 a[44:45], v9 offset:12296\n"
"buffer_load_dwordx4 v6, s[40:43], s90 offen offset:2176  lds\n"
"v_mfma_f32_16x16x32_bf16 v[236:239], a[92:95], a[116:119], v[236:239]\n"
"ds_read_b128 a[116:119], v13 offset:36864\n"
"v_mfma_f32_16x16x32_bf16 v[236:239], a[88:91], a[112:115], v[236:239]\n"
"ds_read_b128 a[112:115], v12 offset:36864\n"
"v_mfma_f32_16x16x32_bf16 v[240:243], a[88:91], a[120:123], v[240:243]\n"
"ds_read_b64_tr_b16 a[46:47], v9 offset:12808\n"
"v_mfma_f32_16x16x32_bf16 v[240:243], a[92:95], a[124:127], v[240:243]\n"
"ds_read_b64_tr_b16 a[48:49], v15 offset:8192\n"
"buffer_load_dwordx4 v6, s[40:43], s91 offen offset:3200  lds\n"
"v_mfma_f32_16x16x32_bf16 v[212:215], a[84:87], a[124:127], v[212:215]\n"
"ds_read_b128 a[124:127], v13 offset:38912\n"
"v_mfma_f32_16x16x32_bf16 v[212:215], a[80:83], a[120:123], v[212:215]\n"
"ds_read_b128 a[120:123], v12 offset:38912\n"
"v_mfma_f32_16x16x32_bf16 v[216:219], a[80:83], a[128:131], v[216:219]\n"
"ds_read_b64_tr_b16 a[50:51], v15 offset:8704\n"
"v_mfma_f32_16x16x32_bf16 v[216:219], a[84:87], a[132:135], v[216:219]\n"
"s_add_u32 m0, s64, 8192\n"
"ds_read_b64_tr_b16 a[52:53], v15 offset:12288\n"
"v_mfma_f32_16x16x32_bf16 v[244:247], a[92:95], a[132:135], v[244:247]\n"
"ds_read_b128 a[132:135], v13 offset:24576\n"
"v_mfma_f32_16x16x32_bf16 v[244:247], a[88:91], a[128:131], v[244:247]\n"
"ds_read_b128 a[128:131], v12 offset:24576\n"
"v_mfma_f32_16x16x32_bf16 v[248:251], a[88:91], a[136:139], v[248:251]\n"
"ds_read_b64_tr_b16 a[54:55], v15 offset:12800\n"
"v_mfma_f32_16x16x32_bf16 v[248:251], a[92:95], a[140:143], v[248:251]\n"
"ds_read_b64_tr_b16 a[56:57], v15 offset:8200\n"
"v_mfma_f32_16x16x32_bf16 v[220:223], a[84:87], a[140:143], v[220:223]\n"
"ds_read_b128 a[140:143], v13 offset:26624\n"
"v_mfma_f32_16x16x32_bf16 v[220:223], a[80:83], a[136:139], v[220:223]\n"
"ds_read_b128 a[136:139], v12 offset:26624\n"
"v_mfma_f32_16x16x32_bf16 v[224:227], a[80:83], a[144:147], v[224:227]\n"
"ds_read_b64_tr_b16 a[58:59], v15 offset:8712\n"
"v_mfma_f32_16x16x32_bf16 v[224:227], a[84:87], a[148:151], v[224:227]\n"
"s_sub_u32 s67, 1572864, s75\n"
"ds_read_b64_tr_b16 a[60:61], v15 offset:12296\n"
"v_mfma_f32_16x16x32_bf16 v[252:255], a[92:95], a[148:151], v[252:255]\n"
"ds_read_b128 a[148:151], v13 offset:28672\n"
"v_mfma_f32_16x16x32_bf16 v[252:255], a[88:91], a[144:147], v[252:255]\n"
"ds_read_b128 a[144:147], v12 offset:28672\n"
"ds_read_b64_tr_b16 a[62:63], v15 offset:12808\n"
"v_mfma_f32_16x16x32_bf16 a[28:31], a[88:91], a[152:155], a[28:31]\n"
"v_mfma_f32_16x16x32_bf16 a[28:31], a[92:95], a[156:159], a[28:31]\n"
"v_mfma_f32_16x16x32_bf16 a[24:27], a[84:87], a[156:159], a[24:27]\n"
"ds_read_b128 a[156:159], v13 offset:30720\n"
"v_mfma_f32_16x16x32_bf16 a[24:27], a[80:83], a[152:155], a[24:27]\n"
"s_waitcnt lgkmcnt(15)\n"
"ds_read_b128 a[152:155], v12 offset:30720\n"
"v_mfma_f32_16x16x32_bf16 v[32:35], a[32:35], a[96:99], v[32:35]\n"
"v_mfma_f32_16x16x32_bf16 v[32:35], a[36:39], a[100:103], v[32:35]\n"
"s_waitcnt lgkmcnt(2)\n"
"v_mfma_f32_16x16x32_bf16 v[60:63], a[44:47], a[100:103], v[60:63]\n"
"buffer_load_dwordx4 v11, s[52:55], s67 offen offset:0  lds\n"
"v_mfma_f32_16x16x32_bf16 v[60:63], a[40:43], a[96:99], v[60:63]\n"
"v_mfma_f32_16x16x32_bf16 v[64:67], a[40:43], a[104:107], v[64:67]\n"
"s_sub_u32 s67, 1588224, s75\n"
"v_mfma_f32_16x16x32_bf16 v[64:67], a[44:47], a[108:111], v[64:67]\n"
"v_mfma_f32_16x16x32_bf16 v[36:39], a[36:39], a[108:111], v[36:39]\n"
"v_mfma_f32_16x16x32_bf16 v[36:39], a[32:35], a[104:107], v[36:39]\n"
"buffer_load_dwordx4 v11, s[52:55], s67 offen offset:1024  lds\n"
"v_mfma_f32_16x16x32_bf16 v[40:43], a[32:35], a[112:115], v[40:43]\n"
"v_mfma_f32_16x16x32_bf16 v[40:43], a[36:39], a[116:119], v[40:43]\n"
"s_sub_u32 s67, 1603584, s75\n"
"v_mfma_f32_16x16x32_bf16 v[68:71], a[44:47], a[116:119], v[68:71]\n"
"v_mfma_f32_16x16x32_bf16 v[68:71], a[40:43], a[112:115], v[68:71]\n"
"buffer_load_dwordx4 v11, s[52:55], s67 offen offset:2048  lds\n"
"v_mfma_f32_16x16x32_bf16 v[72:75], a[40:43], a[120:123], v[72:75]\n"
"v_mfma_f32_16x16x32_bf16 v[72:75], a[44:47], a[124:127], v[72:75]\n"
"s_sub_u32 s67, 1618944, s75\n"
"v_mfma_f32_16x16x32_bf16 v[44:47], a[36:39], a[124:127], v[44:47]\n"
"v_mfma_f32_16x16x32_bf16 v[44:47], a[32:35], a[120:123], v[44:47]\n"
"v_mfma_f32_16x16x32_bf16 v[48:51], a[32:35], a[128:131], v[48:51]\n"
"buffer_load_dwordx4 v11, s[52:55], s67 offen offset:3072  lds\n"
"v_mfma_f32_16x16x32_bf16 v[48:51], a[36:39], a[132:135], v[48:51]\n"
"s_add_u32 m0, s64, 12288\n"
"v_mfma_f32_16x16x32_bf16 v[76:79], a[44:47], a[132:135], v[76:79]\n"
"s_waitcnt vmcnt(12)\n"
"ds_read_b64_tr_b16 a[64:65], v9 offset:0\n"
"v_mfma_f32_16x16x32_bf16 v[76:79], a[40:43], a[128:131], v[76:79]\n"
"s_sub_u32 s67, 1638400, s75\n"
"ds_read_b64_tr_b16 a[66:67], v9 offset:512\n"
"v_mfma_f32_16x16x32_bf16 v[80:83], a[40:43], a[136:139], v[80:83]\n"
"ds_read_b64_tr_b16 a[68:69], v9 offset:4096\n"
"v_mfma_f32_16x16x32_bf16 v[80:83], a[44:47], a[140:143], v[80:83]\n"
"ds_read_b64_tr_b16 a[70:71], v9 offset:4608\n"
"buffer_load_dwordx4 v11, s[52:55], s67 offen offset:0  lds\n"
"v_mfma_f32_16x16x32_bf16 v[52:55], a[36:39], a[140:143], v[52:55]\n"
"s_sub_u32 s67, 1653760, s75\n"
"ds_read_b64_tr_b16 a[72:73], v9 offset:8\n"
"v_mfma_f32_16x16x32_bf16 v[52:55], a[32:35], a[136:139], v[52:55]\n"
"s_waitcnt lgkmcnt(5)\n"
"ds_read_b64_tr_b16 a[74:75], v9 offset:520\n"
"v_mfma_f32_16x16x32_bf16 v[56:59], a[32:35], a[144:147], v[56:59]\n"
"ds_read_b64_tr_b16 a[76:77], v9 offset:4104\n"
"v_mfma_f32_16x16x32_bf16 v[56:59], a[36:39], a[148:151], v[56:59]\n"
"ds_read_b64_tr_b16 a[78:79], v9 offset:4616\n"
"v_mfma_f32_16x16x32_bf16 v[84:87], a[44:47], a[148:151], v[84:87]\n"
"buffer_load_dwordx4 v11, s[52:55], s67 offen offset:1024  lds\n"
"v_mfma_f32_16x16x32_bf16 v[84:87], a[40:43], a[144:147], v[84:87]\n"
"ds_read_b64_tr_b16 a[80:81], v15 offset:0\n"
"v_mfma_f32_16x16x32_bf16 a[4:7], a[40:43], a[152:155], a[4:7]\n"
"s_sub_u32 s67, 1669120, s75\n"
"ds_read_b64_tr_b16 a[82:83], v15 offset:512\n"
"v_mfma_f32_16x16x32_bf16 a[4:7], a[44:47], a[156:159], a[4:7]\n"
"ds_read_b64_tr_b16 a[84:85], v15 offset:4096\n"
"v_mfma_f32_16x16x32_bf16 a[0:3], a[36:39], a[156:159], a[0:3]\n"
"ds_read_b64_tr_b16 a[86:87], v15 offset:4608\n"
"buffer_load_dwordx4 v11, s[52:55], s67 offen offset:2048  lds\n"
"v_mfma_f32_16x16x32_bf16 a[0:3], a[32:35], a[152:155], a[0:3]\n"
"ds_read_b64_tr_b16 a[88:89], v15 offset:8\n"
"v_mfma_f32_16x16x32_bf16 v[88:91], a[48:51], a[96:99], v[88:91]\n"
"s_sub_u32 s67, 1684480, s75\n"
"ds_read_b64_tr_b16 a[90:91], v15 offset:520\n"
"v_mfma_f32_16x16x32_bf16 v[88:91], a[52:55], a[100:103], v[88:91]\n"
"ds_read_b64_tr_b16 a[92:93], v15 offset:4104\n"
"v_mfma_f32_16x16x32_bf16 v[116:119], a[60:63], a[100:103], v[116:119]\n"
"ds_read_b64_tr_b16 a[94:95], v15 offset:4616\n"
"v_mfma_f32_16x16x32_bf16 v[116:119], a[56:59], a[96:99], v[116:119]\n"
"buffer_load_dwordx4 v11, s[52:55], s67 offen offset:3072  lds\n"
"v_mfma_f32_16x16x32_bf16 v[120:123], a[56:59], a[104:107], v[120:123]\n"
"v_mfma_f32_16x16x32_bf16 v[120:123], a[60:63], a[108:111], v[120:123]\n"
"s_add_u32 m0, s64, 0\n"
"v_mfma_f32_16x16x32_bf16 v[92:95], a[52:55], a[108:111], v[92:95]\n"
"v_mfma_f32_16x16x32_bf16 v[92:95], a[48:51], a[104:107], v[92:95]\n"
"s_sub_u32 s67, 1572992, s75\n"
"v_mfma_f32_16x16x32_bf16 v[96:99], a[48:51], a[112:115], v[96:99]\n"
"s_waitcnt lgkmcnt(0)\n"
"s_mov_b32 s99, 0\n"
"v_mfma_f32_16x16x32_bf16 v[96:99], a[52:55], a[116:119], v[96:99]\n"
"s_mov_b32 s98, 0\n"
"buffer_load_dwordx4 v11, s[52:55], s67 offen offset:0  lds\n"
"v_mfma_f32_16x16x32_bf16 v[124:127], a[60:63], a[116:119], v[124:127]\n"
"s_memtime s[76:77]\n"
"s_load_dword s98, s[96:97], 8320 glc\n"
"v_mfma_f32_16x16x32_bf16 v[124:127], a[56:59], a[112:115], v[124:127]\n"
"s_sub_u32 s67, 1588352, s75\n"
"v_mfma_f32_16x16x32_bf16 v[128:131], a[56:59], a[120:123], v[128:131]\n"
"v_mfma_f32_16x16x32_bf16 v[128:131], a[60:63], a[124:127], v[128:131]\n"
"buffer_load_dwordx4 v11, s[52:55], s67 offen offset:1024  lds\n"
"v_mfma_f32_16x16x32_bf16 v[100:103], a[52:55], a[124:127], v[100:103]\n"
"v_mfma_f32_16x16x32_bf16 v[100:103], a[48:51], a[120:123], v[100:103]\n"
"s_sub_u32 s67, 1603712, s75\n"
"v_mfma_f32_16x16x32_bf16 v[104:107], a[48:51], a[128:131], v[104:107]\n"
"v_mfma_f32_16x16x32_bf16 v[104:107], a[52:55], a[132:135], v[104:107]\n"
"v_mfma_f32_16x16x32_bf16 v[132:135], a[60:63], a[132:135], v[132:135]\n"
"buffer_load_dwordx4 v11, s[52:55], s67 offen offset:2048  lds\n"
"v_mfma_f32_16x16x32_bf16 v[132:135], a[56:59], a[128:131], v[132:135]\n"
"L_st2657:\n"
"s_cbranch_execz L_retA\n"
"s_mov_b32 s29, s76\n"
"v_mfma_f32_16x16x32_bf16 v[136:139], a[56:59], a[136:139], v[136:139]\n"
"s_sub_u32 s67, 1619072, s75\n"
"v_mfma_f32_16x16x32_bf16 v[136:139], a[60:63], a[140:143], v[136:139]\n"
"buffer_load_dwordx4 v11, s[52:55], s67 offen offset:3072  lds\n"
"v_mfma_f32_16x16x32_bf16 v[108:111], a[52:55], a[140:143], v[108:111]\n"
"s_sub_u32 s99, s99, s98\n"
"s_sub_u32 s99, s99, s98\n"
"v_mfma_f32_16x16x32_bf16 v[108:111], a[48:51], a[136:139], v[108:111]\n"
"s_sub_u32 s99, s99, s98\n"
"s_add_u32 m0, s64, 4096\n"
"v_mfma_f32_16x16x32_bf16 v[112:115], a[48:51], a[144:147], v[112:115]\n"
"s_sub_u32 s99, s99, s98\n"
"v_mfma_f32_16x16x32_bf16 v[112:115], a[52:55], a[148:151], v[112:115]\n"
"s_sub_u32 s67, 1638528, s75\n"
"v_mfma_f32_16x16x32_bf16 v[140:143], a[60:63], a[148:151], v[140:143]\n"
"s_sub_u32 s99, s99, s98\n"
"s_sub_u32 s99, s99, s98\n"
"v_mfma_f32_16x16x32_bf16 v[140:143], a[56:59], a[144:147], v[140:143]\n"
"s_sub_u32 s99, s99, s98\n"
"buffer_load_dwordx4 v11, s[52:55], s67 offen offset:0  lds\n"
"v_mfma_f32_16x16x32_bf16 a[12:15], a[56:59], a[152:155], a[12:15]\n"
"s_sub_u32 s99, s99, s98\n"
"v_mfma_f32_16x16x32_bf16 a[12:15], a[60:63], a[156:159], a[12:15]\n"
"s_sub_u32 s67, 1653888, s75\n"
"v_mfma_f32_16x16x32_bf16 a[8:11], a[52:55], a[156:159], a[8:11]\n"
"s_sub_u32 s99, s99, s98\n"
"s_sub_u32 s99, s99, s98\n"
"v_mfma_f32_16x16x32_bf16 a[8:11], a[48:51], a[152:155], a[8:11]\n"
"s_sub_u32 s99, s99, s98\n"
"v_mfma_f32_16x16x32_bf16 v[144:147], a[64:67], a[96:99], v[144:147]\n"
"s_sub_u32 s99, s99, s98\n"
"v_mfma_f32_16x16x32_bf16 v[144:147], a[68:71], a[100:103], v[144:147]\n"
"buffer_load_dwordx4 v11, s[52:55], s67 offen offset:1024  lds\n"
"v_mfma_f32_16x16x32_bf16 v[172:175], a[76:79], a[100:103], v[172:175]\n"
"s_sub_u32 s99, s99, s98\n"
"s_sub_u32 s99, s99, s98\n"
"v_mfma_f32_16x16x32_bf16 v[172:175], a[72:75], a[96:99], v[172:175]\n"
"s_sub_u32 s99, s99, s98\n"
"s_sub_u32 s67, 1669248, s75\n"
"v_mfma_f32_16x16x32_bf16 v[176:179], a[72:75], a[104:107], v[176:179]\n"
"s_sub_u32 s99, s99, s98\n"
"v_mfma_f32_16x16x32_bf16 v[176:179], a[76:79], a[108:111], v[176:179]\n"
"buffer_load_dwordx4 v11, s[52:55], s67 offen offset:2048  lds\n"
"v_mfma_f32_16x16x32_bf16 v[148:151], a[68:71], a[108:111], v[148:151]\n"
"s_sub_u32 s99, s99, s98\n"
"s_sub_u32 s99, s99, s98\n"
"v_mfma_f32_16x16x32_bf16 v[148:151], a[64:67], a[104:107], v[148:151]\n"
"s_sub_u32 s99, s99, s98\n"
"s_sub_u32 s71, s64, s64\n"
"s_sub_u32 s67, 1684608, s75\n"
"v_mfma_f32_16x16x32_bf16 v[152:155], a[64:67], a[112:115], v[152:155]\n"
"s_sub_u32 s66, 81920, s73\n"
"s_sub_u32 s99, s99, s98\n"
"v_mfma_f32_16x16x32_bf16 v[152:155], a[68:71], a[116:119], v[152:155]\n"
"s_add_u32 s71, s71, 20480\n"
"buffer_load_dwordx4 v11, s[52:55], s67 offen offset:3072  lds\n"
"v_mfma_f32_16x16x32_bf16 v[180:183], a[76:79], a[116:119], v[180:183]\n"
"s_sub_u32 s99, s99, s98\n"
"v_add_u32 v12, s66, v7\n"
"s_sub_u32 s99, s99, s98\n"
"v_mfma_f32_16x16x32_bf16 v[180:183], a[72:75], a[112:115], v[180:183]\n"
"s_sub_u32 s99, s99, s98\n"
"s_cmp_lt_u32 %16, 2\n"
"s_cselect_b32 s67, 16384, 0\n"
"s_add_u32 s67, s67, s72\n"
"v_mfma_f32_16x16x32_bf16 v[184:187], a[72:75], a[120:123], v[184:187]\n"
"s_sub_u32 s99, s99, s98\n"
"v_add_u32 v13, s66, v8\n"
"s_add_u32 m0, s67, 139264\n"
"v_mfma_f32_16x16x32_bf16 v[184:187], a[76:79], a[124:127], v[184:187]\n"
"s_memtime s[76:77]\n"
"v_add_u32 v20, s71, v7\n"
"s_sub_u32 s67, 1792, s60\n"
"v_mfma_f32_16x16x32_bf16 v[156:159], a[68:71], a[124:127], v[156:159]\n"
"buffer_load_dwordx4 v6, s[48:51], s67 offen offset:0  lds\n"
"v_mfma_f32_16x16x32_bf16 v[156:159], a[64:67], a[120:123], v[156:159]\n"
"s_waitcnt lgkmcnt(0)\n"
"v_add_u32 v21, s71, v8\n"
"v_mfma_f32_16x16x32_bf16 v[160:163], a[64:67], a[128:131], v[160:163]\n"
"s_sub_u32 s29, s76, s29\n"
"s_sub_u32 s67, 17152, s60\n"
"v_mfma_f32_16x16x32_bf16 v[160:163], a[68:71], a[132:135], v[160:163]\n"
"s_cmp_lt_u32 s29, 620\n"
"s_cselect_b32 s99, s99, 0\n"
"v_mfma_f32_16x16x32_bf16 v[188:191], a[76:79], a[132:135], v[188:191]\n"
"s_max_u32 s31, s31, s99\n"
"buffer_load_dwordx4 v6, s[48:51], s67 offen offset:1024  lds\n"
"v_mfma_f32_16x16x32_bf16 v[188:191], a[72:75], a[128:131], v[188:191]\n"
"s_waitcnt vmcnt(18)\n"
"ds_read_b128 a[224:227], v20 offset:0\n"
"v_mfma_f32_16x16x32_bf16 v[192:195], a[72:75], a[136:139], v[192:195]\n"
"s_sub_u32 s67, 32512, s60\n"
"ds_read_b128 a[228:231], v21 offset:0\n"
"v_mfma_f32_16x16x32_bf16 v[192:195], a[76:79], a[140:143], v[192:195]\n"
"ds_read_b128 a[232:235], v20 offset:2048\n"
"v_mfma_f32_16x16x32_bf16 v[164:167], a[68:71], a[140:143], v[164:167]\n"
"v_mfma_f32_16x16x32_bf16 v[164:167], a[64:67], a[136:139], v[164:167]\n"
"ds_read_b128 a[236:239], v21 offset:2048\n"
"v_mfma_f32_16x16x32_bf16 v[168:171], a[64:67], a[144:147], v[168:171]\n"
"buffer_load_dwordx4 v6, s[48:51], s67 offen offset:2048  lds\n"
"v_mfma_f32_16x16x32_bf16 v[168:171], a[68:71], a[148:151], v[168:171]\n"
"v_mfma_f32_16x16x32_bf16 v[196:199], a[76:79], a[148:151], v[196:199]\n"
"s_sub_u32 s67, 47872, s60\n"
"v_mfma_f32_16x16x32_bf16 v[196:199], a[72:75], a[144:147], v[196:199]\n"
"v_mfma_f32_16x16x32_bf16 a[20:23], a[72:75], a[152:155], a[20:23]\n"
"buffer_load_dwordx4 v6, s[48:51], s67 offen offset:3072  lds\n"
"v_mfma_f32_16x16x32_bf16 a[20:23], a[76:79], a[156:159], a[20:23]\n"
"s_waitcnt lgkmcnt(0)\n"
"v_mfma_f32_16x16x32_bf16 a[16:19], a[68:71], a[156:159], a[16:19]\n"
"s_add_u32 m0, s64, 16256\n"
"v_mfma_f32_16x16x32_bf16 a[16:19], a[64:67], a[152:155], a[16:19]\n"
"s_barrier\n"
"s_waitcnt vmcnt(12)\n"
"ds_read_b64_tr_b16 a[32:33], v9 offset:8192\n"
"v_mfma_f32_16x16x32_bf16 v[200:203], a[80:83], a[96:99], v[200:203]\n"
"ds_read_b64_tr_b16 a[34:35], v9 offset:8704\n"
"buffer_load_dwordx4 v6, s[40:43], s92 offen offset:128  lds\n"
"v_mfma_f32_16x16x32_bf16 v[200:203], a[84:87], a[100:103], v[200:203]\n"
"ds_read_b64_tr_b16 a[36:37], v9 offset:12288\n"
"v_mfma_f32_16x16x32_bf16 v[228:231], a[92:95], a[100:103], v[228:231]\n"
"ds_read_b128 a[100:103], v13 offset:32768\n"
"v_mfma_f32_16x16x32_bf16 v[228:231], a[88:91], a[96:99], v[228:231]\n"
"ds_read_b128 a[96:99], v12 offset:32768\n"
"buffer_load_dwordx4 v6, s[40:43], s93 offen offset:1152  lds\n"
"v_mfma_f32_16x16x32_bf16 v[232:235], a[88:91], a[104:107], v[232:235]\n"
"ds_read_b64_tr_b16 a[38:39], v9 offset:12800\n"
"v_mfma_f32_16x16x32_bf16 v[232:235], a[92:95], a[108:111], v[232:235]\n"
"ds_read_b64_tr_b16 a[40:41], v9 offset:8200\n"
"v_mfma_f32_16x16x32_bf16 v[204:207], a[84:87], a[108:111], v[204:207]\n"
"ds_read_b128 a[108:111], v13 offset:34816\n"
"v_mfma_f32_16x16x32_bf16 v[204:207], a[80:83], a[104:107], v[204:207]\n"
"ds_read_b128 a[104:107], v12 offset:34816\n"
"v_mfma_f32_16x16x32_bf16 v[208:211], a[80:83], a[112:115], v[208:211]\n"
"ds_read_b64_tr_b16 a[42:43], v9 offset:8712\n"
"buffer_load_dwordx4 v6, s[40:43], s94 offen offset:2176  lds\n"
"v_mfma_f32_16x16x32_bf16 v[208:211], a[84:87], a[116:119], v[208:211]\n"
"ds_read_b64_tr_b16 a[44:45], v9 offset:12296\n"
"v_mfma_f32_16x16x32_bf16 v[236:239], a[92:95], a[116:119], v[236:239]\n"
"ds_read_b128 a[116:119], v13 offset:36864\n"
"v_mfma_f32_16x16x32_bf16 v[236:239], a[88:91], a[112:115], v[236:239]\n"
"ds_read_b128 a[112:115], v12 offset:36864\n"
"v_mfma_f32_16x16x32_bf16 v[240:243], a[88:91], a[120:123], v[240:243]\n"
"ds_read_b64_tr_b16 a[46:47], v9 offset:12808\n"
"v_mfma_f32_16x16x32_bf16 v[240:243], a[92:95], a[124:127], v[240:243]\n"
"ds_read_b64_tr_b16 a[48:49], v15 offset:8192\n"
"buffer_load_dwordx4 v6, s[40:43], s95 offen offset:3200  lds\n"
"v_mfma_f32_16x16x32_bf16 v[212:215], a[84:87], a[124:127], v[212:215]\n"
"ds_read_b128 a[124:127], v13 offset:38912\n"
"v_mfma_f32_16x16x32_bf16 v[212:215], a[80:83], a[120:123], v[212:215]\n"
"ds_read_b128 a[120:123], v12 offset:38912\n"
"v_mfma_f32_16x16x32_bf16 v[216:219], a[80:83], a[128:131], v[216:219]\n"
"ds_read_b64_tr_b16 a[50:51], v15 offset:8704\n"
"v_mfma_f32_16x16x32_bf16 v[216:219], a[84:87], a[132:135], v[216:219]\n"
"s_add_u32 m0, s64, 8192\n"
"ds_read_b64_tr_b16 a[52:53], v15 offset:12288\n"
"v_mfma_f32_16x16x32_bf16 v[244:247], a[92:95], a[132:135], v[244:247]\n"
"ds_read_b128 a[132:135], v13 offset:24576\n"
"v_mfma_f32_16x16x32_bf16 v[244:247], a[88:91], a[128:131], v[244:247]\n"
"ds_read_b128 a[128:131], v12 offset:24576\n"
"v_mfma_f32_16x16x32_bf16 v[248:251], a[88:91], a[136:139], v[248:251]\n"
"ds_read_b64_tr_b16 a[54:55], v15 offset:12800\n"
"v_mfma_f32_16x16x32_bf16 v[248:251], a[92:95], a[140:143], v[248:251]\n"
"ds_read_b64_tr_b16 a[56:57], v15 offset:8200\n"
"v_mfma_f32_16x16x32_bf16 v[220:223], a[84:87], a[140:143], v[220:223]\n"
"ds_read_b128 a[140:143], v13 offset:26624\n"
"v_mfma_f32_16x16x32_bf16 v[220:223], a[80:83], a[136:139], v[220:223]\n"
"ds_read_b128 a[136:139], v12 offset:26624\n"
"v_mfma_f32_16x16x32_bf16 v[224:227], a[80:83], a[144:147], v[224:227]\n"
"ds_read_b64_tr_b16 a[58:59], v15 offset:8712\n"
"v_mfma_f32_16x16x32_bf16 v[224:227], a[84:87], a[148:151], v[224:227]\n"
"s_sub_u32 s67, 1835008, s75\n"
"ds_read_b64_tr_b16 a[60:61], v15 offset:12296\n"
"v_mfma_f32_16x16x32_bf16 v[252:255], a[92:95], a[148:151], v[252:255]\n"
"ds_read_b128 a[148:151], v13 offset:28672\n"
"v_mfma_f32_16x16x32_bf16 v[252:255], a[88:91], a[144:147], v[252:255]\n"
"ds_read_b128 a[144:147], v12 offset:28672\n"
"ds_read_b64_tr_b16 a[62:63], v15 offset:12808\n"
"v_mfma_f32_16x16x32_bf16 a[28:31], a[88:91], a[152:155], a[28:31]\n"
"v_mfma_f32_16x16x32_bf16 a[28:31], a[92:95], a[156:159], a[28:31]\n"
"v_mfma_f32_16x16x32_bf16 a[24:27], a[84:87], a[156:159], a[24:27]\n"
"ds_read_b128 a[156:159], v13 offset:30720\n"
"v_mfma_f32_16x16x32_bf16 a[24:27], a[80:83], a[152:155], a[24:27]\n"
"s_waitcnt lgkmcnt(15)\n"
"ds_read_b128 a[152:155], v12 offset:30720\n"
"v_mfma_f32_16x16x32_bf16 v[32:35], a[32:35], a[96:99], v[32:35]\n"
"v_mfma_f32_16x16x32_bf16 v[32:35], a[36:39], a[100:103], v[32:35]\n"
"s_waitcnt lgkmcnt(2)\n"
"v_mfma_f32_16x16x32_bf16 v[60:63], a[44:47], a[100:103], v[60:63]\n"
"buffer_load_dwordx4 v11, s[52:55], s67 offen offset:0  lds\n"
"v_mfma_f32_16x16x32_bf16 v[60:63], a[40:43], a[96:99], v[60:63]\n"
"v_mfma_f32_16x16x32_bf16 v[64:67], a[40:43], a[104:107], v[64:67]\n"
"s_sub_u32 s67, 1850368, s75\n"
"v_mfma_f32_16x16x32_bf16 v[64:67], a[44:47], a[108:111], v[64:67]\n"
"v_mfma_f32_16x16x32_bf16 v[36:39], a[36:39], a[108:111], v[36:39]\n"
"v_mfma_f32_16x16x32_bf16 v[36:39], a[32:35], a[104:107], v[36:39]\n"
"buffer_load_dwordx4 v11, s[52:55], s67 offen offset:1024  lds\n"
"v_mfma_f32_16x16x32_bf16 v[40:43], a[32:35], a[112:115], v[40:43]\n"
"v_mfma_f32_16x16x32_bf16 v[40:43], a[36:39], a[116:119], v[40:43]\n"
"s_sub_u32 s67, 1865728, s75\n"
"v_mfma_f32_16x16x32_bf16 v[68:71], a[44:47], a[116:119], v[68:71]\n"
"v_mfma_f32_16x16x32_bf16 v[68:71], a[40:43], a[112:115], v[68:71]\n"
"buffer_load_dwordx4 v11, s[52:55], s67 offen offset:2048  lds\n"
"v_mfma_f32_16x16x32_bf16 v[72:75], a[40:43], a[120:123], v[72:75]\n"
"v_mfma_f32_16x16x32_bf16 v[72:75], a[44:47], a[124:127], v[72:75]\n"
"s_sub_u32 s67, 1881088, s75\n"
"v_mfma_f32_16x16x32_bf16 v[44:47], a[36:39], a[124:127], v[44:47]\n"
"v_mfma_f32_16x16x32_bf16 v[44:47], a[32:35], a[120:123], v[44:47]\n"
"v_mfma_f32_16x16x32_bf16 v[48:51], a[32:35], a[128:131], v[48:51]\n"
"buffer_load_dwordx4 v11, s[52:55], s67 offen offset:3072  lds\n"
"v_mfma_f32_16x16x32_bf16 v[48:51], a[36:39], a[132:135], v[48:51]\n"
"s_add_u32 m0, s64, 12288\n"
"v_mfma_f32_16x16x32_bf16 v[76:79], a[44:47], a[132:135], v[76:79]\n"
"s_waitcnt vmcnt(12)\n"
"ds_read_b64_tr_b16 a[64:65], v9 offset:0\n"
"v_mfma_f32_16x16x32_bf16 v[76:79], a[40:43], a[128:131], v[76:79]\n"
"ds_read_b64_tr_b16 a[66:67], v9 offset:512\n"
"v_mfma_f32_16x16x32_bf16 v[80:83], a[40:43], a[136:139], v[80:83]\n"
"s_sub_u32 s67, 1900544, s75\n"
"ds_read_b64_tr_b16 a[68:69], v9 offset:4096\n"
"v_mfma_f32_16x16x32_bf16 v[80:83], a[44:47], a[140:143], v[80:83]\n"
"ds_read_b64_tr_b16 a[70:71], v9 offset:4608\n"
"v_mfma_f32_16x16x32_bf16 v[52:55], a[36:39], a[140:143], v[52:55]\n"
"ds_read_b64_tr_b16 a[72:73], v9 offset:8\n"
"buffer_load_dwordx4 v11, s[52:55], s67 offen offset:0  lds\n"
"v_mfma_f32_16x16x32_bf16 v[52:55], a[32:35], a[136:139], v[52:55]\n"
"s_waitcnt lgkmcnt(5)\n"
"ds_read_b64_tr_b16 a[74:75], v9 offset:520\n"
"v_mfma_f32_16x16x32_bf16 v[56:59], a[32:35], a[144:147], v[56:59]\n"
"s_sub_u32 s67, 1915904, s75\n"
"ds_read_b64_tr_b16 a[76:77], v9 offset:4104\n"
"v_mfma_f32_16x16x32_bf16 v[56:59], a[36:39], a[148:151], v[56:59]\n"
"ds_read_b64_tr_b16 a[78:79], v9 offset:4616\n"
"v_mfma_f32_16x16x32_bf16 v[84:87], a[44:47], a[148:151], v[84:87]\n"
"buffer_load_dwordx4 v11, s[52:55], s67 offen offset:1024  lds\n"
"v_mfma_f32_16x16x32_bf16 v[84:87], a[40:43], a[144:147], v[84:87]\n"
"ds_read_b64_tr_b16 a[80:81], v15 offset:0\n"
"v_mfma_f32_16x16x32_bf16 a[4:7], a[40:43], a[152:155], a[4:7]\n"
"s_sub_u32 s67, 1931264, s75\n"
"ds_read_b64_tr_b16 a[82:83], v15 offset:512\n"
"v_mfma_f32_16x16x32_bf16 a[4:7], a[44:47], a[156:159], a[4:7]\n"
"ds_read_b64_tr_b16 a[84:85], v15 offset:4096\n"
"v_mfma_f32_16x16x32_bf16 a[0:3], a[36:39], a[156:159], a[0:3]\n"
"ds_read_b64_tr_b16 a[86:87], v15 offset:4608\n"
"buffer_load_dwordx4 v11, s[52:55], s67 offen offset:2048  lds\n"
"v_mfma_f32_16x16x32_bf16 a[0:3], a[32:35], a[152:155], a[0:3]\n"
"ds_read_b64_tr_b16 a[88:89], v15 offset:8\n"
"v_mfma_f32_16x16x32_bf16 v[88:91], a[48:51], a[96:99], v[88:91]\n"
"s_sub_u32 s67, 1946624, s75\n"
"ds_read_b64_tr_b16 a[90:91], v15 offset:520\n"
"v_mfma_f32_16x16x32_bf16 v[88:91], a[52:55], a[100:103], v[88:91]\n"
"ds_read_b64_tr_b16 a[92:93], v15 offset:4104\n"
"v_mfma_f32_16x16x32_bf16 v[116:119], a[60:63], a[100:103], v[116:119]\n"
"ds_read_b64_tr_b16 a[94:95], v15 offset:4616\n"
"v_mfma_f32_16x16x32_bf16 v[116:119], a[56:59], a[96:99], v[116:119]\n"
"buffer_load_dwordx4 v11, s[52:55], s67 offen offset:3072  lds\n"
"v_mfma_f32_16x16x32_bf16 v[120:123], a[56:59], a[104:107], v[120:123]\n"
"v_mfma_f32_16x16x32_bf16 v[120:123], a[60:63], a[108:111], v[120:123]\n"
"s_add_u32 m0, s64, 0\n"
"v_mfma_f32_16x16x32_bf16 v[92:95], a[52:55], a[108:111], v[92:95]\n"
"v_mfma_f32_16x16x32_bf16 v[92:95], a[48:51], a[104:107], v[92:95]\n"
"s_sub_u32 s67, 1835136, s75\n"
"v_mfma_f32_16x16x32_bf16 v[96:99], a[48:51], a[112:115], v[96:99]\n"
"s_waitcnt lgkmcnt(0)\n"
"s_mov_b32 s99, 0\n"
"v_mfma_f32_16x16x32_bf16 v[96:99], a[52:55], a[116:119], v[96:99]\n"
"s_mov_b32 s98, 0\n"
"buffer_load_dwordx4 v11, s[52:55], s67 offen offset:0  lds\n"
"v_mfma_f32_16x16x32_bf16 v[124:127], a[60:63], a[116:119], v[124:127]\n"
"s_memtime s[76:77]\n"
"s_load_dword s98, s[96:97], 128 glc\n"
"v_mfma_f32_16x16x32_bf16 v[124:127], a[56:59], a[112:115], v[124:127]\n"
"s_sub_u32 s67, 1850496, s75\n"
"v_mfma_f32_16x16x32_bf16 v[128:131], a[56:59], a[120:123], v[128:131]\n"
"v_mfma_f32_16x16x32_bf16 v[128:131], a[60:63], a[124:127], v[128:131]\n"
"v_mfma_f32_16x16x32_bf16 v[100:103], a[52:55], a[124:127], v[100:103]\n"
"buffer_load_dwordx4 v11, s[52:55], s67 offen offset:1024  lds\n"
"v_mfma_f32_16x16x32_bf16 v[100:103], a[48:51], a[120:123], v[100:103]\n"
"v_mfma_f32_16x16x32_bf16 v[104:107], a[48:51], a[128:131], v[104:107]\n"
"s_sub_u32 s67, 1865856, s75\n"
"v_mfma_f32_16x16x32_bf16 v[104:107], a[52:55], a[132:135], v[104:107]\n"
"v_mfma_f32_16x16x32_bf16 v[132:135], a[60:63], a[132:135], v[132:135]\n"
"buffer_load_dwordx4 v11, s[52:55], s67 offen offset:2048  lds\n"
"v_mfma_f32_16x16x32_bf16 v[132:135], a[56:59], a[128:131], v[132:135]\n"
"s_mov_b32 s29, s76\n"
"v_mfma_f32_16x16x32_bf16 v[136:139], a[56:59], a[136:139], v[136:139]\n"
"s_sub_u32 s67, 1881216, s75\n"
"v_mfma_f32_16x16x32_bf16 v[136:139], a[60:63], a[140:143], v[136:139]\n"
"v_mfma_f32_16x16x32_bf16 v[108:111], a[52:55], a[140:143], v[108:111]\n"
"s_sub_u32 s99, s99, s98\n"
"s_sub_u32 s99, s99, s98\n"
"v_mfma_f32_16x16x32_bf16 v[108:111], a[48:51], a[136:139], v[108:111]\n"
"s_sub_u32 s99, s99, s98\n"
"buffer_load_dwordx4 v11, s[52:55], s67 offen offset:3072  lds\n"
"v_mfma_f32_16x16x32_bf16 v[112:115], a[48:51], a[144:147], v[112:115]\n"
"s_sub_u32 s99, s99, s98\n"
"s_add_u32 m0, s64, 4096\n"
"v_mfma_f32_16x16x32_bf16 v[112:115], a[52:55], a[148:151], v[112:115]\n"
"v_mfma_f32_16x16x32_bf16 v[140:143], a[60:63], a[148:151], v[140:143]\n"
"s_sub_u32 s99, s99, s98\n"
"s_sub_u32 s99, s99, s98\n"
"buffer_load_dwordx4 v[24:27], v0, s[56:59], %25 offen sc1\n"
"v_mfma_f32_16x16x32_bf16 v[140:143], a[56:59], a[144:147], v[140:143]\n"
"s_sub_u32 s99, s99, s98\n"
"s_sub_u32 s67, 1900672, s75\n"
"v_mfma_f32_16x16x32_bf16 a[12:15], a[56:59], a[152:155], a[12:15]\n"
"s_sub_u32 s99, s99, s98\n"
"buffer_load_dwordx4 v11, s[52:55], s67 offen offset:0  lds\n"
"v_mfma_f32_16x16x32_bf16 a[12:15], a[60:63], a[156:159], a[12:15]\n"
"s_sub_u32 s67, 1916032, s75\n"
"v_mfma_f32_16x16x32_bf16 a[8:11], a[52:55], a[156:159], a[8:11]\n"
"s_sub_u32 s99, s99, s98\n"
"s_sub_u32 s99, s99, s98\n"
"v_mfma_f32_16x16x32_bf16 a[8:11], a[48:51], a[152:155], a[8:11]\n"
"s_sub_u32 s99, s99, s98\n"
"v_mfma_f32_16x16x32_bf16 v[144:147], a[64:67], a[96:99], v[144:147]\n"
"s_sub_u32 s99, s99, s98\n"
"v_mfma_f32_16x16x32_bf16 v[144:147], a[68:71], a[100:103], v[144:147]\n"
"buffer_load_dwordx4 v11, s[52:55], s67 offen offset:1024  lds\n"
"v_mfma_f32_16x16x32_bf16 v[172:175], a[76:79], a[100:103], v[172:175]\n"
"s_sub_u32 s99, s99, s98\n"
"s_sub_u32 s99, s99, s98\n"
"v_mfma_f32_16x16x32_bf16 v[172:175], a[72:75], a[96:99], v[172:175]\n"
"s_sub_u32 s99, s99, s98\n"
"s_sub_u32 s67, 1931392, s75\n"
"v_mfma_f32_16x16x32_bf16 v[176:179], a[72:75], a[104:107], v[176:179]\n"
"s_sub_u32 s99, s99, s98\n"
"v_mfma_f32_16x16x32_bf16 v[176:179], a[76:79], a[108:111], v[176:179]\n"
"buffer_load_dwordx4 v11, s[52:55], s67 offen offset:2048  lds\n"
"v_mfma_f32_16x16x32_bf16 v[148:151], a[68:71], a[108:111], v[148:151]\n"
"s_sub_u32 s99, s99, s98\n"
"s_sub_u32 s99, s99, s98\n"
"v_mfma_f32_16x16x32_bf16 v[148:151], a[64:67], a[104:107], v[148:151]\n"
"s_sub_u32 s99, s99, s98\n"
"s_sub_u32 s71, s64, s64\n"
"s_sub_u32 s67, 1946752, s75\n"
"v_mfma_f32_16x16x32_bf16 v[152:155], a[64:67], a[112:115], v[152:155]\n"
"s_sub_u32 s66, 122880, s73\n"
"s_sub_u32 s99, s99, s98\n"
"v_mfma_f32_16x16x32_bf16 v[152:155], a[68:71], a[116:119], v[152:155]\n"
"s_add_u32 s71, s71, 16384\n"
"buffer_load_dwordx4 v11, s[52:55], s67 offen offset:3072  lds\n"
"v_mfma_f32_16x16x32_bf16 v[180:183], a[76:79], a[116:119], v[180:183]\n"
"s_sub_u32 s99, s99, s98\n"
"v_add_u32 v12, s66, v7\n"
"s_sub_u32 s99, s99, s98\n"
"v_mfma_f32_16x16x32_bf16 v[180:183], a[72:75], a[112:115], v[180:183]\n"
"s_sub_u32 s99, s99, s98\n"
"v_min_u32 v19, 496, v0\n"
"v_mfma_f32_16x16x32_bf16 v[184:187], a[72:75], a[120:123], v[184:187]\n"
"s_sub_u32 s99, s99, s98\n"
"v_add_u32 v13, s66, v8\n"
"s_add_u32 s70, %21, 0\n"
"v_mfma_f32_16x16x32_bf16 v[184:187], a[76:79], a[124:127], v[184:187]\n"
"s_memtime s[76:77]\n"
"v_add_u32 v20, s71, v7\n"
"v_lshl_add_u32 v19, v19, 7, s70\n"
"v_mfma_f32_16x16x32_bf16 v[156:159], a[68:71], a[124:127], v[156:159]\n"
"buffer_load_dword v23, v19, s[52:55], 0 offen\n"
"v_mfma_f32_16x16x32_bf16 v[156:159], a[64:67], a[120:123], v[156:159]\n"
"s_waitcnt lgkmcnt(0)\n"
"v_add_u32 v21, s71, v8\n"
"v_min_u32 v19, 496, v0\n"
"v_mfma_f32_16x16x32_bf16 v[160:163], a[64:67], a[128:131], v[160:163]\n"
"s_sub_u32 s29, s76, s29\n"
"s_add_u32 s70, %21, 2097152\n"
"v_lshl_add_u32 v19, v19, 7, s70\n"
"v_mfma_f32_16x16x32_bf16 v[160:163], a[68:71], a[132:135], v[160:163]\n"
"s_cmp_lt_u32 s29, 620\n"
"s_cselect_b32 s99, s99, 0\n"
"buffer_load_dword v23, v19, s[52:55], 0 offen\n"
"v_mfma_f32_16x16x32_bf16 v[188:191], a[76:79], a[132:135], v[188:191]\n"
"s_max_u32 s30, s30, s99\n"
"v_mfma_f32_16x16x32_bf16 v[188:191], a[72:75], a[128:131], v[188:191]\n"
"s_waitcnt vmcnt(19)\n"
"ds_read_b128 a[240:243], v20 offset:0\n"
"v_mfma_f32_16x16x32_bf16 v[192:195], a[72:75], a[136:139], v[192:195]\n"
"ds_read_b128 a[244:247], v21 offset:0\n"
"v_mfma_f32_16x16x32_bf16 v[192:195], a[76:79], a[140:143], v[192:195]\n"
"ds_read_b128 a[248:251], v20 offset:2048\n"
"v_mfma_f32_16x16x32_bf16 v[164:167], a[68:71], a[140:143], v[164:167]\n"
"v_mfma_f32_16x16x32_bf16 v[164:167], a[64:67], a[136:139], v[164:167]\n"
"ds_read_b128 a[252:255], v21 offset:2048\n"
"v_mfma_f32_16x16x32_bf16 v[168:171], a[64:67], a[144:147], v[168:171]\n"
"v_mfma_f32_16x16x32_bf16 v[168:171], a[68:71], a[148:151], v[168:171]\n"
"v_mfma_f32_16x16x32_bf16 v[196:199], a[76:79], a[148:151], v[196:199]\n"
"v_mfma_f32_16x16x32_bf16 v[196:199], a[72:75], a[144:147], v[196:199]\n"
"v_mfma_f32_16x16x32_bf16 a[20:23], a[72:75], a[152:155], a[20:23]\n"
"v_mfma_f32_16x16x32_bf16 a[20:23], a[76:79], a[156:159], a[20:23]\n"
"s_waitcnt lgkmcnt(0)\n"
"v_mfma_f32_16x16x32_bf16 a[16:19], a[68:71], a[156:159], a[16:19]\n"
"v_mfma_f32_16x16x32_bf16 a[16:19], a[64:67], a[152:155], a[16:19]\n"
"s_barrier\n"
"s_waitcnt vmcnt(11)\n"
"ds_read_b64_tr_b16 a[32:33], v9 offset:8192\n"
"v_mfma_f32_16x16x32_bf16 v[200:203], a[80:83], a[96:99], v[200:203]\n"
"ds_read_b64_tr_b16 a[34:35], v9 offset:8704\n"
"v_mfma_f32_16x16x32_bf16 v[200:203], a[84:87], a[100:103], v[200:203]\n"
"ds_read_b64_tr_b16 a[36:37], v9 offset:12288\n"
"v_mfma_f32_16x16x32_bf16 v[228:231], a[92:95], a[100:103], v[228:231]\n"
"ds_read_b128 a[100:103], v13 offset:32768\n"
"v_mfma_f32_16x16x32_bf16 v[228:231], a[88:91], a[96:99], v[228:231]\n"
"ds_read_b128 a[96:99], v12 offset:32768\n"
"v_mfma_f32_16x16x32_bf16 v[232:235], a[88:91], a[104:107], v[232:235]\n"
"ds_read_b64_tr_b16 a[38:39], v9 offset:12800\n"
"v_mfma_f32_16x16x32_bf16 v[232:235], a[92:95], a[108:111], v[232:235]\n"
"ds_read_b64_tr_b16 a[40:41], v9 offset:8200\n"
"s_getpc_b64 s[70:71]\n"
"L_c11_:\n"
"s_add_u32 s70, s70, L_P6-L_c11_\n"
"s_addc_u32 s71, s71, 0\n"
"v_mfma_f32_16x16x32_bf16 v[204:207], a[84:87], a[108:111], v[204:207]\n"
"ds_read_b128 a[108:111], v13 offset:34816\n"
"v_mfma_f32_16x16x32_bf16 v[204:207], a[80:83], a[104:107], v[204:207]\n"
"s_lshr_b32 s66, %26, 3\n"
"ds_read_b128 a[104:107], v12 offset:34816\n"
"v_mfma_f32_16x16x32_bf16 v[208:211], a[80:83], a[112:115], v[208:211]\n"
"ds_read_b64_tr_b16 a[42:43], v9 offset:8712\n"
"v_mfma_f32_16x16x32_bf16 v[208:211], a[84:87], a[116:119], v[208:211]\n"
"ds_read_b64_tr_b16 a[44:45], v9 offset:12296\n"
"v_mfma_f32_16x16x32_bf16 v[236:239], a[92:95], a[116:119], v[236:239]\n"
"s_lshl2_add_u32 s66, s66, %16\n"
"ds_read_b128 a[116:119], v13 offset:36864\n"
"v_mfma_f32_16x16x32_bf16 v[236:239], a[88:91], a[112:115], v[236:239]\n"
"ds_read_b128 a[112:115], v12 offset:36864\n"
"v_mfma_f32_16x16x32_bf16 v[240:243], a[88:91], a[120:123], v[240:243]\n"
"ds_read_b64_tr_b16 a[46:47], v9 offset:12808\n"
"v_mfma_f32_16x16x32_bf16 v[240:243], a[92:95], a[124:127], v[240:243]\n"
"s_mul_i32 s66, s66, 384\n"
"ds_read_b64_tr_b16 a[48:49], v15 offset:8192\n"
"v_mfma_f32_16x16x32_bf16 v[212:215], a[84:87], a[124:127], v[212:215]\n"
"ds_read_b128 a[124:127], v13 offset:38912\n"
"v_mfma_f32_16x16x32_bf16 v[212:215], a[80:83], a[120:123], v[212:215]\n"
"s_min_u32 s66, s66, L_headF-L_P6-384\n"
"ds_read_b128 a[120:123], v12 offset:38912\n"
"v_mfma_f32_16x16x32_bf16 v[216:219], a[80:83], a[128:131], v[216:219]\n"
"ds_read_b64_tr_b16 a[50:51], v15 offset:8704\n"
"v_mfma_f32_16x16x32_bf16 v[216:219], a[84:87], a[132:135], v[216:219]\n"
"ds_read_b64_tr_b16 a[52:53], v15 offset:12288\n"
"v_mfma_f32_16x16x32_bf16 v[244:247], a[92:95], a[132:135], v[244:247]\n"
"ds_read_b128 a[132:135], v13 offset:24576\n"
"v_min_u32 v22, 32, v0\n"
"v_mfma_f32_16x16x32_bf16 v[244:247], a[88:91], a[128:131], v[244:247]\n"
"ds_read_b128 a[128:131], v12 offset:24576\n"
"v_mfma_f32_16x16x32_bf16 v[248:251], a[88:91], a[136:139], v[248:251]\n"
"ds_read_b64_tr_b16 a[54:55], v15 offset:12800\n"
"v_mfma_f32_16x16x32_bf16 v[248:251], a[92:95], a[140:143], v[248:251]\n"
"ds_read_b64_tr_b16 a[56:57], v15 offset:8200\n"
"v_lshl_add_u32 v22, v22, 3, s66\n"
"v_mfma_f32_16x16x32_bf16 v[220:223], a[84:87], a[140:143], v[220:223]\n"
"ds_read_b128 a[140:143], v13 offset:26624\n"
"v_mfma_f32_16x16x32_bf16 v[220:223], a[80:83], a[136:139], v[220:223]\n"
"ds_read_b128 a[136:139], v12 offset:26624\n"
"global_load_dword v23, v22, s[70:71]\n"
"v_mfma_f32_16x16x32_bf16 v[224:227], a[80:83], a[144:147], v[224:227]\n"
"ds_read_b64_tr_b16 a[58:59], v15 offset:8712\n"
"v_mfma_f32_16x16x32_bf16 v[224:227], a[84:87], a[148:151], v[224:227]\n"
"ds_read_b64_tr_b16 a[60:61], v15 offset:12296\n"
"v_mfma_f32_16x16x32_bf16 v[252:255], a[92:95], a[148:151], v[252:255]\n"
"ds_read_b128 a[148:151], v13 offset:28672\n"
"v_mfma_f32_16x16x32_bf16 v[252:255], a[88:91], a[144:147], v[252:255]\n"
"ds_read_b128 a[144:147], v12 offset:28672\n"
"ds_read_b64_tr_b16 a[62:63], v15 offset:12808\n"
"v_mfma_f32_16x16x32_bf16 a[28:31], a[88:91], a[152:155], a[28:31]\n"
"v_mfma_f32_16x16x32_bf16 a[28:31], a[92:95], a[156:159], a[28:31]\n"
"v_mfma_f32_16x16x32_bf16 a[24:27], a[84:87], a[156:159], a[24:27]\n"
"ds_read_b128 a[156:159], v13 offset:30720\n"
"v_mfma_f32_16x16x32_bf16 a[24:27], a[80:83], a[152:155], a[24:27]\n"
"s_waitcnt lgkmcnt(15)\n"
"ds_read_b128 a[152:155], v12 offset:30720\n"
"v_mfma_f32_16x16x32_bf16 v[32:35], a[32:35], a[96:99], v[32:35]\n"
"v_mfma_f32_16x16x32_bf16 v[32:35], a[36:39], a[100:103], v[32:35]\n"
"s_waitcnt lgkmcnt(2)\n"
"v_mfma_f32_16x16x32_bf16 v[60:63], a[44:47], a[100:103], v[60:63]\n"
"v_mfma_f32_16x16x32_bf16 v[60:63], a[40:43], a[96:99], v[60:63]\n"
"v_mfma_f32_16x16x32_bf16 v[64:67], a[40:43], a[104:107], v[64:67]\n"
"ds_read_b128 a[160:163], v7 offset:24576\n"
"v_mfma_f32_16x16x32_bf16 v[64:67], a[44:47], a[108:111], v[64:67]\n"
"v_mfma_f32_16x16x32_bf16 v[36:39], a[36:39], a[108:111], v[36:39]\n"
"ds_read_b128 a[164:167], v8 offset:24576\n"
"v_mfma_f32_16x16x32_bf16 v[36:39], a[32:35], a[104:107], v[36:39]\n"
"v_mfma_f32_16x16x32_bf16 v[40:43], a[32:35], a[112:115], v[40:43]\n"
"ds_read_b128 a[168:171], v7 offset:26624\n"
"v_mfma_f32_16x16x32_bf16 v[40:43], a[36:39], a[116:119], v[40:43]\n"
"ds_read_b128 a[172:175], v8 offset:26624\n"
"v_mfma_f32_16x16x32_bf16 v[68:71], a[44:47], a[116:119], v[68:71]\n"
"s_add_u32 s70, s78, 1\n"
"v_mfma_f32_16x16x32_bf16 v[68:71], a[40:43], a[112:115], v[68:71]\n"
"ds_read_b128 a[176:179], v7 offset:28672\n"
"v_mfma_f32_16x16x32_bf16 v[72:75], a[40:43], a[120:123], v[72:75]\n"
"ds_read_b128 a[180:183], v8 offset:28672\n"
"v_mfma_f32_16x16x32_bf16 v[72:75], a[44:47], a[124:127], v[72:75]\n"
"v_mfma_f32_16x16x32_bf16 v[44:47], a[36:39], a[124:127], v[44:47]\n"
"ds_read_b128 a[184:187], v7 offset:30720\n"
"v_mfma_f32_16x16x32_bf16 v[44:47], a[32:35], a[120:123], v[44:47]\n"
"v_mfma_f32_16x16x32_bf16 v[48:51], a[32:35], a[128:131], v[48:51]\n"
"ds_read_b128 a[188:191], v8 offset:30720\n"
"v_mfma_f32_16x16x32_bf16 v[48:51], a[36:39], a[132:135], v[48:51]\n"
"v_mfma_f32_16x16x32_bf16 v[76:79], a[44:47], a[132:135], v[76:79]\n"
"s_waitcnt vmcnt(3)\n"
"ds_read_b64_tr_b16 a[64:65], v9 offset:0\n"
"v_mfma_f32_16x16x32_bf16 v[76:79], a[40:43], a[128:131], v[76:79]\n"
"ds_read_b64_tr_b16 a[66:67], v9 offset:512\n"
"v_mfma_f32_16x16x32_bf16 v[80:83], a[40:43], a[136:139], v[80:83]\n"
"ds_read_b64_tr_b16 a[68:69], v9 offset:4096\n"
"v_mfma_f32_16x16x32_bf16 v[80:83], a[44:47], a[140:143], v[80:83]\n"
"ds_read_b64_tr_b16 a[70:71], v9 offset:4608\n"
"v_mfma_f32_16x16x32_bf16 v[52:55], a[36:39], a[140:143], v[52:55]\n"
"ds_read_b64_tr_b16 a[72:73], v9 offset:8\n"
"v_subrev_u32 v24, s70, v24\n"
"v_mfma_f32_16x16x32_bf16 v[52:55], a[32:35], a[136:139], v[52:55]\n"
"s_waitcnt lgkmcnt(13)\n"
"ds_read_b64_tr_b16 a[74:75], v9 offset:520\n"
"v_xor_b32 v25, s79, v25\n"
"v_mfma_f32_16x16x32_bf16 v[56:59], a[32:35], a[144:147], v[56:59]\n"
"ds_read_b64_tr_b16 a[76:77], v9 offset:4104\n"
"v_subrev_u32 v26, s70, v26\n"
"v_mfma_f32_16x16x32_bf16 v[56:59], a[36:39], a[148:151], v[56:59]\n"
"ds_read_b64_tr_b16 a[78:79], v9 offset:4616\n"
"v_xor_b32 v27, s79, v27\n"
"v_mfma_f32_16x16x32_bf16 v[84:87], a[44:47], a[148:151], v[84:87]\n"
"ds_read_b64_tr_b16 a[80:81], v15 offset:0\n"
"v_or_b32 v24, v24, v26\n"
"v_mfma_f32_16x16x32_bf16 v[84:87], a[40:43], a[144:147], v[84:87]\n"
"ds_read_b64_tr_b16 a[82:83], v15 offset:512\n"
"v_or_b32 v25, v25, v27\n"
"v_mfma_f32_16x16x32_bf16 a[4:7], a[40:43], a[152:155], a[4:7]\n"
"ds_read_b64_tr_b16 a[84:85], v15 offset:4096\n"
"v_or_b32 v24, v24, v25\n"
"v_mfma_f32_16x16x32_bf16 a[4:7], a[44:47], a[156:159], a[4:7]\n"
"ds_read_b64_tr_b16 a[86:87], v15 offset:4608\n"
"v_cmp_lt_u32 vcc, 15, v24\n"
"v_mfma_f32_16x16x32_bf16 a[0:3], a[36:39], a[156:159], a[0:3]\n"
"ds_read_b64_tr_b16 a[88:89], v15 offset:8\n"
"s_cbranch_vccnz L_slow3\n"
"L_back3:\n"
"v_mfma_f32_16x16x32_bf16 a[0:3], a[32:35], a[152:155], a[0:3]\n"
"ds_read_b64_tr_b16 a[90:91], v15 offset:520\n"
"v_mfma_f32_16x16x32_bf16 v[88:91], a[48:51], a[96:99], v[88:91]\n"
"ds_read_b64_tr_b16 a[92:93], v15 offset:4104\n"
"v_mfma_f32_16x16x32_bf16 v[88:91], a[52:55], a[100:103], v[88:91]\n"
"global_load_dwordx4 a[32:35], v0, s[34:35] offset:-4096 sc1\n"
"s_barrier\n"
"v_mfma_f32_16x16x32_bf16 v[116:119], a[60:63], a[100:103], v[116:119]\n"
"ds_read_b64_tr_b16 a[94:95], v15 offset:4616\n"
"v_mfma_f32_16x16x32_bf16 v[116:119], a[56:59], a[96:99], v[116:119]\n"
"ds_write_b64 v2, a[192:193] offset:16384\n"
"v_mfma_f32_16x16x32_bf16 v[120:123], a[56:59], a[104:107], v[120:123]\n"
"v_mfma_f32_16x16x32_bf16 v[120:123], a[60:63], a[108:111], v[120:123]\n"
"ds_write_b64 v2, a[194:195] offset:16392\n"
"v_mfma_f32_16x16x32_bf16 v[92:95], a[52:55], a[108:111], v[92:95]\n"
"v_mfma_f32_16x16x32_bf16 v[92:95], a[48:51], a[104:107], v[92:95]\n"
"ds_write_b64 v2, a[196:197] offset:17408\n"
"v_mfma_f32_16x16x32_bf16 v[96:99], a[48:51], a[112:115], v[96:99]\n"
"s_waitcnt lgkmcnt(3)\n"
"v_mfma_f32_16x16x32_bf16 v[96:99], a[52:55], a[116:119], v[96:99]\n"
"global_load_dwordx4 a[36:39], v0, s[34:35] offset:-3072 sc1\n"
"v_mfma_f32_16x16x32_bf16 v[124:127], a[60:63], a[116:119], v[124:127]\n"
"ds_write_b64 v2, a[198:199] offset:17416\n"
"v_mfma_f32_16x16x32_bf16 v[124:127], a[56:59], a[112:115], v[124:127]\n"
"ds_write_b64 v2, a[200:201] offset:18432\n"
"v_mfma_f32_16x16x32_bf16 v[128:131], a[56:59], a[120:123], v[128:131]\n"
"v_mfma_f32_16x16x32_bf16 v[128:131], a[60:63], a[124:127], v[128:131]\n"
"ds_write_b64 v2, a[202:203] offset:18440\n"
"v_mfma_f32_16x16x32_bf16 v[100:103], a[52:55], a[124:127], v[100:103]\n"
"v_mfma_f32_16x16x32_bf16 v[100:103], a[48:51], a[120:123], v[100:103]\n"
"ds_write_b64 v2, a[204:205] offset:19456\n"
"v_mfma_f32_16x16x32_bf16 v[104:107], a[48:51], a[128:131], v[104:107]\n"
"L_st3225:\n"
"s_cbranch_execz L_retA\n"
"ds_write_b64 v2, a[206:207] offset:19464\n"
"v_mfma_f32_16x16x32_bf16 v[104:107], a[52:55], a[132:135], v[104:107]\n"
"global_load_dwordx4 a[40:43], v0, s[34:35] offset:-2048 sc1\n"
"v_mfma_f32_16x16x32_bf16 v[132:135], a[60:63], a[132:135], v[132:135]\n"
"ds_write_b64 v2, a[208:209] offset:20480\n"
"v_mfma_f32_16x16x32_bf16 v[132:135], a[56:59], a[128:131], v[132:135]\n"
"v_mfma_f32_16x16x32_bf16 v[136:139], a[56:59], a[136:139], v[136:139]\n"
"ds_write_b64 v2, a[210:211] offset:20488\n"
"v_mfma_f32_16x16x32_bf16 v[136:139], a[60:63], a[140:143], v[136:139]\n"
"v_mfma_f32_16x16x32_bf16 v[108:111], a[52:55], a[140:143], v[108:111]\n"
"ds_write_b64 v2, a[212:213] offset:21504\n"
"v_mfma_f32_16x16x32_bf16 v[108:111], a[48:51], a[136:139], v[108:111]\n"
"v_mfma_f32_16x16x32_bf16 v[112:115], a[48:51], a[144:147], v[112:115]\n"
"ds_write_b64 v2, a[214:215] offset:21512\n"
"v_mfma_f32_16x16x32_bf16 v[112:115], a[52:55], a[148:151], v[112:115]\n"
"global_load_dwordx4 a[44:47], v0, s[34:35] offset:-1024 sc1\n"
"v_mfma_f32_16x16x32_bf16 v[140:143], a[60:63], a[148:151], v[140:143]\n"
"ds_write_b64 v2, a[216:217] offset:22528\n"
"v_mfma_f32_16x16x32_bf16 v[140:143], a[56:59], a[144:147], v[140:143]\n"
"v_mfma_f32_16x16x32_bf16 a[12:15], a[56:59], a[152:155], a[12:15]\n"
"ds_write_b64 v2, a[218:219] offset:22536\n"
"v_mfma_f32_16x16x32_bf16 a[12:15], a[60:63], a[156:159], a[12:15]\n"
"v_mfma_f32_16x16x32_bf16 a[8:11], a[52:55], a[156:159], a[8:11]\n"
"ds_write_b64 v2, a[220:221] offset:23552\n"
"v_mfma_f32_16x16x32_bf16 a[8:11], a[48:51], a[152:155], a[8:11]\n"
"v_mfma_f32_16x16x32_bf16 v[144:147], a[64:67], a[96:99], v[144:147]\n"
"ds_write_b64 v2, a[222:223] offset:23560\n"
"v_mfma_f32_16x16x32_bf16 v[144:147], a[68:71], a[100:103], v[144:147]\n"
"global_load_dwordx4 a[48:51], v0, s[34:35] offset:0 sc1\n"
"v_mfma_f32_16x16x32_bf16 v[172:175], a[76:79], a[100:103], v[172:175]\n"
"ds_write_b64 v2, a[224:225] offset:24576\n"
"v_mfma_f32_16x16x32_bf16 v[172:175], a[72:75], a[96:99], v[172:175]\n"
"ds_write_b64 v2, a[226:227] offset:24584\n"
"v_mfma_f32_16x16x32_bf16 v[176:179], a[72:75], a[104:107], v[176:179]\n"
"v_mfma_f32_16x16x32_bf16 v[176:179], a[76:79], a[108:111], v[176:179]\n"
"ds_write_b64 v2, a[228:229] offset:25600\n"
"v_mfma_f32_16x16x32_bf16 v[148:151], a[68:71], a[108:111], v[148:151]\n"
"v_mfma_f32_16x16x32_bf16 v[148:151], a[64:67], a[104:107], v[148:151]\n"
"ds_write_b64 v2, a[230:231] offset:25608\n"
"v_mfma_f32_16x16x32_bf16 v[152:155], a[64:67], a[112:115], v[152:155]\n"
"ds_write_b64 v2, a[232:233] offset:26624\n"
"v_mfma_f32_16x16x32_bf16 v[152:155], a[68:71], a[116:119], v[152:155]\n"
"global_load_dwordx4 a[52:55], v0, s[34:35] offset:1024 sc1\n"
"v_mfma_f32_16x16x32_bf16 v[180:183], a[76:79], a[116:119], v[180:183]\n"
"v_mfma_f32_16x16x32_bf16 v[180:183], a[72:75], a[112:115], v[180:183]\n"
"ds_write_b64 v2, a[234:235] offset:26632\n"
"v_mfma_f32_16x16x32_bf16 v[184:187], a[72:75], a[120:123], v[184:187]\n"
"v_mfma_f32_16x16x32_bf16 v[184:187], a[76:79], a[124:127], v[184:187]\n"
"ds_write_b64 v2, a[236:237] offset:27648\n"
"v_mfma_f32_16x16x32_bf16 v[156:159], a[68:71], a[124:127], v[156:159]\n"
"v_mfma_f32_16x16x32_bf16 v[156:159], a[64:67], a[120:123], v[156:159]\n"
"ds_write_b64 v2, a[238:239] offset:27656\n"
"v_mfma_f32_16x16x32_bf16 v[160:163], a[64:67], a[128:131], v[160:163]\n"
"ds_write_b64 v2, a[240:241] offset:28672\n"
"v_mfma_f32_16x16x32_bf16 v[160:163], a[68:71], a[132:135], v[160:163]\n"
"global_load_dwordx4 a[56:59], v0, s[34:35] offset:2048 sc1\n"
"v_mfma_f32_16x16x32_bf16 v[188:191], a[76:79], a[132:135], v[188:191]\n"
"ds_write_b64 v2, a[242:243] offset:28680\n"
"v_mfma_f32_16x16x32_bf16 v[188:191], a[72:75], a[128:131], v[188:191]\n"
"v_mfma_f32_16x16x32_bf16 v[192:195], a[72:75], a[136:139], v[192:195]\n"
"ds_write_b64 v2, a[244:245] offset:29696\n"
"v_mfma_f32_16x16x32_bf16 v[192:195], a[76:79], a[140:143], v[192:195]\n"
"v_mfma_f32_16x16x32_bf16 v[164:167], a[68:71], a[140:143], v[164:167]\n"
"ds_write_b64 v2, a[246:247] offset:29704\n"
"v_mfma_f32_16x16x32_bf16 v[164:167], a[64:67], a[136:139], v[164:167]\n"
"v_mfma_f32_16x16x32_bf16 v[168:171], a[64:67], a[144:147], v[168:171]\n"
"ds_write_b64 v2, a[248:249] offset:30720\n"
"v_mfma_f32_16x16x32_bf16 v[168:171], a[68:71], a[148:151], v[168:171]\n"
"global_load_dwordx4 a[60:63], v0, s[34:35] offset:3072 sc1\n"
"v_mfma_f32_16x16x32_bf16 v[196:199], a[76:79], a[148:151], v[196:199]\n"
"ds_write_b64 v2, a[250:251] offset:30728\n"
"v_mfma_f32_16x16x32_bf16 v[196:199], a[72:75], a[144:147], v[196:199]\n"
"v_mfma_f32_16x16x32_bf16 a[20:23], a[72:75], a[152:155], a[20:23]\n"
"s_add_u32 s34, s34, 8192\n"
"ds_write_b64 v2, a[252:253] offset:31744\n"
"v_mfma_f32_16x16x32_bf16 a[20:23], a[76:79], a[156:159], a[20:23]\n"
"v_mfma_f32_16x16x32_bf16 a[16:19], a[68:71], a[156:159], a[16:19]\n"
"ds_write_b64 v2, a[254:255] offset:31752\n"
"v_mfma_f32_16x16x32_bf16 a[16:19], a[64:67], a[152:155], a[16:19]\n"
"v_mfma_f32_16x16x32_bf16 v[200:203], a[80:83], a[96:99], v[200:203]\n"
"v_lshrrev_b32 v16, 4, v0\n"
"v_mfma_f32_16x16x32_bf16 v[200:203], a[84:87], a[100:103], v[200:203]\n"
"global_load_dwordx4 a[64:67], v0, s[34:35] offset:-4096 sc1\n"
"v_mfma_f32_16x16x32_bf16 v[228:231], a[92:95], a[100:103], v[228:231]\n"
"ds_read_b128 a[100:103], v2 offset:17408\n"
"v_mfma_f32_16x16x32_bf16 v[228:231], a[88:91], a[96:99], v[228:231]\n"
"ds_read_b128 a[96:99], v2 offset:16384\n"
"v_mfma_f32_16x16x32_bf16 v[232:235], a[88:91], a[104:107], v[232:235]\n"
"v_and_b32 v16, 0x30, v16\n"
"v_mfma_f32_16x16x32_bf16 v[232:235], a[92:95], a[108:111], v[232:235]\n"
"buffer_load_dwordx2 v[18:19], v16, s[44:47], s61 offen offset:128\n"
"v_mfma_f32_16x16x32_bf16 v[204:207], a[84:87], a[108:111], v[204:207]\n"
"ds_read_b128 a[108:111], v2 offset:19456\n"
"v_mfma_f32_16x16x32_bf16 v[204:207], a[80:83], a[104:107], v[204:207]\n"
"ds_read_b128 a[104:107], v2 offset:18432\n"
"v_mfma_f32_16x16x32_bf16 v[208:211], a[80:83], a[112:115], v[208:211]\n"
"v_lshrrev_b32 v20, 4, v0\n"
"v_mfma_f32_16x16x32_bf16 v[208:211], a[84:87], a[116:119], v[208:211]\n"
"global_load_dwordx4 a[68:71], v0, s[34:35] offset:-3072 sc1\n"
"v_mfma_f32_16x16x32_bf16 v[236:239], a[92:95], a[116:119], v[236:239]\n"
"ds_read_b128 a[116:119], v2 offset:21504\n"
"v_mfma_f32_16x16x32_bf16 v[236:239], a[88:91], a[112:115], v[236:239]\n"
"ds_read_b128 a[112:115], v2 offset:20480\n"
"v_mfma_f32_16x16x32_bf16 v[240:243], a[88:91], a[120:123], v[240:243]\n"
"v_and_b32 v20, 0x30, v20\n"
"v_mfma_f32_16x16x32_bf16 v[240:243], a[92:95], a[124:127], v[240:243]\n"
"buffer_load_dwordx2 v[22:23], v20, s[44:47], s61 offen offset:136\n"
"v_mfma_f32_16x16x32_bf16 v[212:215], a[84:87], a[124:127], v[212:215]\n"
"ds_read_b128 a[124:127], v2 offset:23552\n"
"v_mfma_f32_16x16x32_bf16 v[212:215], a[80:83], a[120:123], v[212:215]\n"
"ds_read_b128 a[120:123], v2 offset:22528\n"
"v_mfma_f32_16x16x32_bf16 v[216:219], a[80:83], a[128:131], v[216:219]\n"
"v_lshrrev_b32 v24, 4, v0\n"
"v_mfma_f32_16x16x32_bf16 v[216:219], a[84:87], a[132:135], v[216:219]\n"
"global_load_dwordx4 a[72:75], v0, s[34:35] offset:-2048 sc1\n"
"v_mfma_f32_16x16x32_bf16 v[244:247], a[92:95], a[132:135], v[244:247]\n"
"ds_read_b128 a[132:135], v2 offset:25600\n"
"v_and_b32 v24, 0x30, v24\n"
"v_mfma_f32_16x16x32_bf16 v[244:247], a[88:91], a[128:131], v[244:247]\n"
"ds_read_b128 a[128:131], v2 offset:24576\n"
"v_mfma_f32_16x16x32_bf16 v[248:251], a[88:91], a[136:139], v[248:251]\n"
"buffer_load_dwordx2 v[26:27], v24, s[44:47], s61 offen offset:192\n"
"v_mfma_f32_16x16x32_bf16 v[248:251], a[92:95], a[140:143], v[248:251]\n"
"v_lshrrev_b32 v28, 4, v0\n"
"v_mfma_f32_16x16x32_bf16 v[220:223], a[84:87], a[140:143], v[220:223]\n"
"ds_read_b128 a[140:143], v2 offset:27648\n"
"v_mfma_f32_16x16x32_bf16 v[220:223], a[80:83], a[136:139], v[220:223]\n"
"ds_read_b128 a[136:139], v2 offset:26624\n"
"v_mfma_f32_16x16x32_bf16 v[224:227], a[80:83], a[144:147], v[224:227]\n"
"v_and_b32 v28, 0x30, v28\n"
"v_mfma_f32_16x16x32_bf16 v[224:227], a[84:87], a[148:151], v[224:227]\n"
"global_load_dwordx4 a[76:79], v0, s[34:35] offset:-1024 sc1\n"
"v_mfma_f32_16x16x32_bf16 v[252:255], a[92:95], a[148:151], v[252:255]\n"
"ds_read_b128 a[148:151], v2 offset:29696\n"
"v_mfma_f32_16x16x32_bf16 v[252:255], a[88:91], a[144:147], v[252:255]\n"
"ds_read_b128 a[144:147], v2 offset:28672\n"
"v_mfma_f32_16x16x32_bf16 a[28:31], a[88:91], a[152:155], a[28:31]\n"
"buffer_load_dwordx2 v[30:31], v28, s[44:47], s61 offen offset:200\n"
"v_mfma_f32_16x16x32_bf16 a[28:31], a[92:95], a[156:159], a[28:31]\n"
"v_mfma_f32_16x16x32_bf16 a[24:27], a[84:87], a[156:159], a[24:27]\n"
"ds_read_b128 a[156:159], v2 offset:31744\n"
"v_mfma_f32_16x16x32_bf16 a[24:27], a[80:83], a[152:155], a[24:27]\n"
"s_waitcnt vmcnt(12) lgkmcnt(11)\n"
"ds_read_b128 a[152:155], v2 offset:30720\n"
"v_mfma_f32_16x16x32_bf16 v[32:35], a[32:35], a[96:99], v[32:35]\n"
"v_mfma_f32_16x16x32_bf16 v[32:35], a[36:39], a[100:103], v[32:35]\n"
"global_load_dwordx4 a[80:83], v0, s[34:35] offset:0 sc1\n"
"v_mfma_f32_16x16x32_bf16 v[60:63], a[44:47], a[100:103], v[60:63]\n"
"s_barrier\n"
"v_mfma_f32_16x16x32_bf16 v[60:63], a[40:43], a[96:99], v[60:63]\n"
"v_mfma_f32_16x16x32_bf16 v[64:67], a[40:43], a[104:107], v[64:67]\n"
"v_mfma_f32_16x16x32_bf16 v[64:67], a[44:47], a[108:111], v[64:67]\n"
"v_mfma_f32_16x16x32_bf16 v[36:39], a[36:39], a[108:111], v[36:39]\n"
"v_mfma_f32_16x16x32_bf16 v[36:39], a[32:35], a[104:107], v[36:39]\n"
"s_waitcnt lgkmcnt(8)\n"
"v_mfma_f32_16x16x32_bf16 v[40:43], a[32:35], a[112:115], v[40:43]\n"
"v_mfma_f32_16x16x32_bf16 v[40:43], a[36:39], a[116:119], v[40:43]\n"
"global_load_dwordx4 a[84:87], v0, s[34:35] offset:1024 sc1\n"
"v_mfma_f32_16x16x32_bf16 v[68:71], a[44:47], a[116:119], v[68:71]\n"
"v_mfma_f32_16x16x32_bf16 v[68:71], a[40:43], a[112:115], v[68:71]\n"
"v_mfma_f32_16x16x32_bf16 v[72:75], a[40:43], a[120:123], v[72:75]\n"
"v_mfma_f32_16x16x32_bf16 v[72:75], a[44:47], a[124:127], v[72:75]\n"
"s_add_u32 s61, s61, 2048\n"
"v_mfma_f32_16x16x32_bf16 v[44:47], a[36:39], a[124:127], v[44:47]\n"
"v_mfma_f32_16x16x32_bf16 v[44:47], a[32:35], a[120:123], v[44:47]\n"
"s_waitcnt lgkmcnt(4)\n"
"v_mfma_f32_16x16x32_bf16 v[48:51], a[32:35], a[128:131], v[48:51]\n"
"s_waitcnt vmcnt(8)\n"
"v_lshlrev_b32 v16, 16, v18\n"
"v_mfma_f32_16x16x32_bf16 v[48:51], a[36:39], a[132:135], v[48:51]\n"
"global_load_dwordx4 a[88:91], v0, s[34:35] offset:2048 sc1\n"
"v_and_b32 v17, s63, v18\n"
"v_mfma_f32_16x16x32_bf16 v[76:79], a[44:47], a[132:135], v[76:79]\n"
"v_lshlrev_b32 v18, 16, v19\n"
"v_and_b32 v19, s63, v19\n"
"v_mfma_f32_16x16x32_bf16 v[76:79], a[40:43], a[128:131], v[76:79]\n"
"v_add_f32 v144, v144, v16\n"
"v_mfma_f32_16x16x32_bf16 v[80:83], a[40:43], a[136:139], v[80:83]\n"
"v_add_f32 v145, v145, v17\n"
"v_add_f32 v146, v146, v18\n"
"v_mfma_f32_16x16x32_bf16 v[80:83], a[44:47], a[140:143], v[80:83]\n"
"v_add_f32 v147, v147, v19\n"
"v_mfma_f32_16x16x32_bf16 v[52:55], a[36:39], a[140:143], v[52:55]\n"
"v_add_f32 v148, v148, v16\n"
"v_mfma_f32_16x16x32_bf16 v[52:55], a[32:35], a[136:139], v[52:55]\n"
"s_mov_b32 s99, 0\n"
"v_add_f32 v149, v149, v17\n"
"s_waitcnt lgkmcnt(0)\n"
"v_mfma_f32_16x16x32_bf16 v[56:59], a[32:35], a[144:147], v[56:59]\n"
"s_mov_b32 s98, 0\n"
"v_add_f32 v150, v150, v18\n"
"v_add_f32 v151, v151, v19\n"
"v_mfma_f32_16x16x32_bf16 v[56:59], a[36:39], a[148:151], v[56:59]\n"
"global_load_dwordx4 a[92:95], v0, s[34:35] offset:3072 sc1\n"
"s_memtime s[76:77]\n"
"s_load_dword s98, s[96:97], 8448 glc\n"
"v_mfma_f32_16x16x32_bf16 v[84:87], a[44:47], a[148:151], v[84:87]\n"
"v_add_f32 v152, v152, v16\n"
"v_add_f32 v153, v153, v17\n"
"v_mfma_f32_16x16x32_bf16 v[84:87], a[40:43], a[144:147], v[84:87]\n"
"v_add_f32 v154, v154, v18\n"
"v_add_f32 v155, v155, v19\n"
"v_mfma_f32_16x16x32_bf16 a[4:7], a[40:43], a[152:155], a[4:7]\n"
"s_add_u32 s34, s34, 8192\n"
"v_add_f32 v156, v156, v16\n"
"v_mfma_f32_16x16x32_bf16 a[4:7], a[44:47], a[156:159], a[4:7]\n"
"v_add_f32 v157, v157, v17\n"
"v_mfma_f32_16x16x32_bf16 a[0:3], a[36:39], a[156:159], a[0:3]\n"
"v_add_f32 v158, v158, v18\n"
"v_add_f32 v159, v159, v19\n"
"v_mfma_f32_16x16x32_bf16 a[0:3], a[32:35], a[152:155], a[0:3]\n"
"v_add_f32 v160, v160, v16\n"
"v_mfma_f32_16x16x32_bf16 v[88:91], a[48:51], a[96:99], v[88:91]\n"
"v_add_f32 v161, v161, v17\n"
"v_add_f32 v162, v162, v18\n"
"v_mfma_f32_16x16x32_bf16 v[88:91], a[52:55], a[100:103], v[88:91]\n"
"global_load_dwordx4 a[32:35], v0, s[34:35] offset:-4096 sc1\n"
"v_add_f32 v163, v163, v19\n"
"v_mfma_f32_16x16x32_bf16 v[116:119], a[60:63], a[100:103], v[116:119]\n"
"s_mov_b32 s29, s76\n"
"ds_read_b128 a[100:103], v3 offset:17408\n"
"v_add_f32 v164, v164, v16\n"
"v_mfma_f32_16x16x32_bf16 v[116:119], a[56:59], a[96:99], v[116:119]\n"
"ds_read_b128 a[96:99], v3 offset:16384\n"
"v_add_f32 v165, v165, v17\n"
"v_mfma_f32_16x16x32_bf16 v[120:123], a[56:59], a[104:107], v[120:123]\n"
"s_sub_u32 s99, s99, s98\n"
"v_add_f32 v166, v166, v18\n"
"v_add_f32 v167, v167, v19\n"
"v_mfma_f32_16x16x32_bf16 v[120:123], a[60:63], a[108:111], v[120:123]\n"
"s_sub_u32 s99, s99, s98\n"
"v_add_f32 v168, v168, v16\n"
"v_add_f32 v169, v169, v17\n"
"v_mfma_f32_16x16x32_bf16 v[92:95], a[52:55], a[108:111], v[92:95]\n"
"s_sub_u32 s99, s99, s98\n"
"ds_read_b128 a[108:111], v3 offset:19456\n"
"v_add_f32 v170, v170, v18\n"
"v_mfma_f32_16x16x32_bf16 v[92:95], a[48:51], a[104:107], v[92:95]\n"
"s_sub_u32 s99, s99, s98\n"
"ds_read_b128 a[104:107], v3 offset:18432\n"
"v_add_f32 v171, v171, v19\n"
"v_mfma_f32_16x16x32_bf16 v[96:99], a[48:51], a[112:115], v[96:99]\n"
"s_sub_u32 s99, s99, s98\n"
"v_accvgpr_read_b32 v12, a16\n"
"v_accvgpr_read_b32 v13, a17\n"
"v_mfma_f32_16x16x32_bf16 v[96:99], a[52:55], a[116:119], v[96:99]\n"
"global_load_dwordx4 a[36:39], v0, s[34:35] offset:-3072 sc1\n"
"s_sub_u32 s99, s99, s98\n"
"v_mfma_f32_16x16x32_bf16 v[124:127], a[60:63], a[116:119], v[124:127]\n"
"s_sub_u32 s99, s99, s98\n"
"ds_read_b128 a[116:119], v3 offset:21504\n"
"v_accvgpr_read_b32 v14, a18\n"
"v_mfma_f32_16x16x32_bf16 v[124:127], a[56:59], a[112:115], v[124:127]\n"
"s_sub_u32 s99, s99, s98\n"
"ds_read_b128 a[112:115], v3 offset:20480\n"
"v_accvgpr_read_b32 v15, a19\n"
"v_mfma_f32_16x16x32_bf16 v[128:131], a[56:59], a[120:123], v[128:131]\n"
"s_sub_u32 s99, s99, s98\n"
"v_add_f32 v12, v12, v16\n"
"v_add_f32 v13, v13, v17\n"
"v_mfma_f32_16x16x32_bf16 v[128:131], a[60:63], a[124:127], v[128:131]\n"
"s_sub_u32 s99, s99, s98\n"
"v_add_f32 v14, v14, v18\n"
"v_add_f32 v15, v15, v19\n"
"v_mfma_f32_16x16x32_bf16 v[100:103], a[52:55], a[124:127], v[100:103]\n"
"s_sub_u32 s99, s99, s98\n"
"ds_read_b128 a[124:127], v3 offset:23552\n"
"v_accvgpr_write_b32 a16, v12\n"
"v_mfma_f32_16x16x32_bf16 v[100:103], a[48:51], a[120:123], v[100:103]\n"
"s_sub_u32 s99, s99, s98\n"
"ds_read_b128 a[120:123], v3 offset:22528\n"
"v_accvgpr_write_b32 a17, v13\n"
"v_mfma_f32_16x16x32_bf16 v[104:107], a[48:51], a[128:131], v[104:107]\n"
"s_sub_u32 s99, s99, s98\n"
"v_accvgpr_write_b32 a18, v14\n"
"v_accvgpr_write_b32 a19, v15\n"
"v_mfma_f32_16x16x32_bf16 v[104:107], a[52:55], a[132:135], v[104:107]\n"
"s_sub_u32 s99, s99, s98\n"
"s_waitcnt vmcnt(10)\n"
"global_load_dwordx4 a[40:43], v0, s[34:35] offset:-2048 sc1\n"
"v_mfma_f32_16x16x32_bf16 v[132:135], a[60:63], a[132:135], v[132:135]\n"
"s_sub_u32 s99, s99, s98\n"
"ds_read_b128 a[132:135], v3 offset:25600\n"
"v_lshlrev_b32 v20, 16, v22\n"
"v_mfma_f32_16x16x32_bf16 v[132:135], a[56:59], a[128:131], v[132:135]\n"
"s_sub_u32 s99, s99, s98\n"
"ds_read_b128 a[128:131], v3 offset:24576\n"
"v_and_b32 v21, s63, v22\n"
"v_mfma_f32_16x16x32_bf16 v[136:139], a[56:59], a[136:139], v[136:139]\n"
"s_sub_u32 s99, s99, s98\n"
"v_lshlrev_b32 v22, 16, v23\n"
"v_and_b32 v23, s63, v23\n"
"v_mfma_f32_16x16x32_bf16 v[136:139], a[60:63], a[140:143], v[136:139]\n"
"s_sub_u32 s99, s99, s98\n"
"v_add_f32 v172, v172, v20\n"
"v_add_f32 v173, v173, v21\n"
"v_mfma_f32_16x16x32_bf16 v[108:111], a[52:55], a[140:143], v[108:111]\n"
"s_sub_u32 s99, s99, s98\n"
"ds_read_b128 a[140:143], v3 offset:27648\n"
"v_add_f32 v174, v174, v22\n"
"v_mfma_f32_16x16x32_bf16 v[108:111], a[48:51], a[136:139], v[108:111]\n"
"s_sub_u32 s99, s99, s98\n"
"ds_read_b128 a[136:139], v3 offset:26624\n"
"v_add_f32 v175, v175, v23\n"
"v_mfma_f32_16x16x32_bf16 v[112:115], a[48:51], a[144:147], v[112:115]\n"
"s_sub_u32 s99, s99, s98\n"
"v_add_f32 v176, v176, v20\n"
"v_add_f32 v177, v177, v21\n"
"v_mfma_f32_16x16x32_bf16 v[112:115], a[52:55], a[148:151], v[112:115]\n"
"global_load_dwordx4 a[44:47], v0, s[34:35] offset:-1024 sc1\n"
"s_sub_u32 s99, s99, s98\n"
"v_mfma_f32_16x16x32_bf16 v[140:143], a[60:63], a[148:151], v[140:143]\n"
"s_sub_u32 s99, s99, s98\n"
"ds_read_b128 a[148:151], v3 offset:29696\n"
"v_add_f32 v178, v178, v22\n"
"v_mfma_f32_16x16x32_bf16 v[140:143], a[56:59], a[144:147], v[140:143]\n"
"s_sub_u32 s99, s99, s98\n"
"ds_read_b128 a[144:147], v3 offset:28672\n"
"v_add_f32 v179, v179, v23\n"
"v_mfma_f32_16x16x32_bf16 a[12:15], a[56:59], a[152:155], a[12:15]\n"
"s_memtime s[76:77]\n"
"v_add_f32 v180, v180, v20\n"
"v_add_f32 v181, v181, v21\n"
"v_mfma_f32_16x16x32_bf16 a[12:15], a[60:63], a[156:159], a[12:15]\n"
"v_add_f32 v182, v182, v22\n"
"v_add_f32 v183, v183, v23\n"
"v_mfma_f32_16x16x32_bf16 a[8:11], a[52:55], a[156:159], a[8:11]\n"
"ds_read_b128 a[156:159], v3 offset:31744\n"
"v_add_f32 v184, v184, v20\n"
"v_mfma_f32_16x16x32_bf16 a[8:11], a[48:51], a[152:155], a[8:11]\n"
"s_waitcnt vmcnt(9) lgkmcnt(11)\n"
"ds_read_b128 a[152:155], v3 offset:30720\n"
"v_add_f32 v185, v185, v21\n"
"v_mfma_f32_16x16x32_bf16 v[32:35], a[64:67], a[96:99], v[32:35]\n"
"v_add_f32 v186, v186, v22\n"
"v_add_f32 v187, v187, v23\n"
"v_mfma_f32_16x16x32_bf16 v[32:35], a[68:71], a[100:103], v[32:35]\n"
"global_load_dwordx4 a[48:51], v0, s[34:35] offset:0 sc1\n"
"v_add_f32 v188, v188, v20\n"
"v_mfma_f32_16x16x32_bf16 v[60:63], a[76:79], a[100:103], v[60:63]\n"
"v_add_f32 v189, v189, v21\n"
"v_mfma_f32_16x16x32_bf16 v[60:63], a[72:75], a[96:99], v[60:63]\n"
"v_add_f32 v190, v190, v22\n"
"v_add_f32 v191, v191, v23\n"
"v_mfma_f32_16x16x32_bf16 v[64:67], a[72:75], a[104:107], v[64:67]\n"
"v_add_f32 v192, v192, v20\n"
"v_mfma_f32_16x16x32_bf16 v[64:67], a[76:79], a[108:111], v[64:67]\n"
"v_add_f32 v193, v193, v21\n"
"v_mfma_f32_16x16x32_bf16 v[36:39], a[68:71], a[108:111], v[36:39]\n"
"v_add_f32 v194, v194, v22\n"
"v_add_f32 v195, v195, v23\n"
"v_mfma_f32_16x16x32_bf16 v[36:39], a[64:67], a[104:107], v[36:39]\n"
"s_waitcnt lgkmcnt(8)\n"
"v_add_f32 v196, v196, v20\n"
"v_mfma_f32_16x16x32_bf16 v[40:43], a[64:67], a[112:115], v[40:43]\n"
"v_add_f32 v197, v197, v21\n"
"v_mfma_f32_16x16x32_bf16 v[40:43], a[68:71], a[116:119], v[40:43]\n"
"global_load_dwordx4 a[52:55], v0, s[34:35] offset:1024 sc1\n"
"v_add_f32 v198, v198, v22\n"
"v_mfma_f32_16x16x32_bf16 v[68:71], a[76:79], a[116:119], v[68:71]\n"
"v_add_f32 v199, v199, v23\n"
"v_accvgpr_read_b32 v12, a20\n"
"v_mfma_f32_16x16x32_bf16 v[68:71], a[72:75], a[112:115], v[68:71]\n"
"v_accvgpr_read_b32 v13, a21\n"
"v_mfma_f32_16x16x32_bf16 v[72:75], a[72:75], a[120:123], v[72:75]\n"
"v_accvgpr_read_b32 v14, a22\n"
"v_accvgpr_read_b32 v15, a23\n"
"v_mfma_f32_16x16x32_bf16 v[72:75], a[76:79], a[124:127], v[72:75]\n"
"v_add_f32 v12, v12, v20\n"
"v_mfma_f32_16x16x32_bf16 v[44:47], a[68:71], a[124:127], v[44:47]\n"
"v_add_f32 v13, v13, v21\n"
"v_mfma_f32_16x16x32_bf16 v[44:47], a[64:67], a[120:123], v[44:47]\n"
"s_waitcnt lgkmcnt(4)\n"
"v_add_f32 v14, v14, v22\n"
"v_mfma_f32_16x16x32_bf16 v[48:51], a[64:67], a[128:131], v[48:51]\n"
"v_add_f32 v15, v15, v23\n"
"v_accvgpr_write_b32 a20, v12\n"
"v_mfma_f32_16x16x32_bf16 v[48:51], a[68:71], a[132:135], v[48:51]\n"
"global_load_dwordx4 a[56:59], v0, s[34:35] offset:2048 sc1\n"
"v_accvgpr_write_b32 a21, v13\n"
"v_mfma_f32_16x16x32_bf16 v[76:79], a[76:79], a[132:135], v[76:79]\n"
"s_waitcnt lgkmcnt(0)\n"
"v_accvgpr_write_b32 a22, v14\n"
"v_mfma_f32_16x16x32_bf16 v[76:79], a[72:75], a[128:131], v[76:79]\n"
"s_sub_u32 s29, s76, s29\n"
"v_accvgpr_write_b32 a23, v15\n"
"v_lshlrev_b32 v24, 16, v26\n"
"v_mfma_f32_16x16x32_bf16 v[80:83], a[72:75], a[136:139], v[80:83]\n"
"s_cmp_lt_u32 s29, 620\n"
"s_cselect_b32 s99, s99, 0\n"
"v_and_b32 v25, s63, v26\n"
"v_mfma_f32_16x16x32_bf16 v[80:83], a[76:79], a[140:143], v[80:83]\n"
"s_max_u32 s31, s31, s99\n"
"v_lshlrev_b32 v26, 16, v27\n"
"v_mfma_f32_16x16x32_bf16 v[52:55], a[68:71], a[140:143], v[52:55]\n"
"v_and_b32 v27, s63, v27\n"
"v_add_f32 v200, v200, v24\n"
"v_mfma_f32_16x16x32_bf16 v[52:55], a[64:67], a[136:139], v[52:55]\n"
"s_mov_b32 s99, 0\n"
"v_add_f32 v201, v201, v25\n"
"v_mfma_f32_16x16x32_bf16 v[56:59], a[64:67], a[144:147], v[56:59]\n"
"s_mov_b32 s98, 0\n"
"v_add_f32 v202, v202, v26\n"
"v_add_f32 v203, v203, v27\n"
"v_mfma_f32_16x16x32_bf16 v[56:59], a[68:71], a[148:151], v[56:59]\n"
"global_load_dwordx4 a[60:63], v0, s[34:35] offset:3072 sc1\n"
"s_memtime s[76:77]\n"
"s_load_dword s98, s[96:97], 256 glc\n"
"v_mfma_f32_16x16x32_bf16 v[84:87], a[76:79], a[148:151], v[84:87]\n"
"v_add_f32 v204, v204, v24\n"
"v_add_f32 v205, v205, v25\n"
"v_mfma_f32_16x16x32_bf16 v[84:87], a[72:75], a[144:147], v[84:87]\n"
"v_add_f32 v206, v206, v26\n"
"v_mfma_f32_16x16x32_bf16 a[4:7], a[72:75], a[152:155], a[4:7]\n"
"s_add_u32 s34, s34, 8192\n"
"v_add_f32 v207, v207, v27\n"
"v_mfma_f32_16x16x32_bf16 a[4:7], a[76:79], a[156:159], a[4:7]\n"
"v_add_f32 v208, v208, v24\n"
"v_add_f32 v209, v209, v25\n"
"v_mfma_f32_16x16x32_bf16 a[0:3], a[68:71], a[156:159], a[0:3]\n"
"v_add_f32 v210, v210, v26\n"
"v_mfma_f32_16x16x32_bf16 a[0:3], a[64:67], a[152:155], a[0:3]\n"
"s_waitcnt vmcnt(8)\n"
"v_add_f32 v211, v211, v27\n"
"v_mfma_f32_16x16x32_bf16 v[88:91], a[80:83], a[96:99], v[88:91]\n"
"v_add_f32 v212, v212, v24\n"
"v_add_f32 v213, v213, v25\n"
"v_mfma_f32_16x16x32_bf16 v[88:91], a[84:87], a[100:103], v[88:91]\n"
"global_load_dwordx4 a[64:67], v0, s[34:35] offset:-4096 sc1\n"
"v_add_f32 v214, v214, v26\n"
"v_mfma_f32_16x16x32_bf16 v[116:119], a[92:95], a[100:103], v[116:119]\n"
"s_mov_b32 s29, s76\n"
"ds_read_b128 a[100:103], v4 offset:17408\n"
"v_add_f32 v215, v215, v27\n"
"v_mfma_f32_16x16x32_bf16 v[116:119], a[88:91], a[96:99], v[116:119]\n"
"ds_read_b128 a[96:99], v4 offset:16384\n"
"v_add_f32 v216, v216, v24\n"
"v_mfma_f32_16x16x32_bf16 v[120:123], a[88:91], a[104:107], v[120:123]\n"
"s_sub_u32 s99, s99, s98\n"
"v_add_f32 v217, v217, v25\n"
"v_add_f32 v218, v218, v26\n"
"v_mfma_f32_16x16x32_bf16 v[120:123], a[92:95], a[108:111], v[120:123]\n"
"s_sub_u32 s99, s99, s98\n"
"v_add_f32 v219, v219, v27\n"
"v_add_f32 v220, v220, v24\n"
"v_mfma_f32_16x16x32_bf16 v[92:95], a[84:87], a[108:111], v[92:95]\n"
"s_sub_u32 s99, s99, s98\n"
"ds_read_b128 a[108:111], v4 offset:19456\n"
"v_add_f32 v221, v221, v25\n"
"v_mfma_f32_16x16x32_bf16 v[92:95], a[80:83], a[104:107], v[92:95]\n"
"s_sub_u32 s99, s99, s98\n"
"ds_read_b128 a[104:107], v4 offset:18432\n"
"v_add_f32 v222, v222, v26\n"
"v_mfma_f32_16x16x32_bf16 v[96:99], a[80:83], a[112:115], v[96:99]\n"
"s_sub_u32 s99, s99, s98\n"
"v_add_f32 v223, v223, v27\n"
"v_add_f32 v224, v224, v24\n"
"v_mfma_f32_16x16x32_bf16 v[96:99], a[84:87], a[116:119], v[96:99]\n"
"global_load_dwordx4 a[68:71], v0, s[34:35] offset:-3072 sc1\n"
"s_sub_u32 s99, s99, s98\n"
"v_mfma_f32_16x16x32_bf16 v[124:127], a[92:95], a[116:119], v[124:127]\n"
"s_sub_u32 s99, s99, s98\n"
"ds_read_b128 a[116:119], v4 offset:21504\n"
"v_add_f32 v225, v225, v25\n"
"v_mfma_f32_16x16x32_bf16 v[124:127], a[88:91], a[112:115], v[124:127]\n"
"s_sub_u32 s99, s99, s98\n"
"ds_read_b128 a[112:115], v4 offset:20480\n"
"v_add_f32 v226, v226, v26\n"
"v_mfma_f32_16x16x32_bf16 v[128:131], a[88:91], a[120:123], v[128:131]\n"
"s_sub_u32 s99, s99, s98\n"
"v_add_f32 v227, v227, v27\n"
"v_accvgpr_read_b32 v12, a24\n"
"v_mfma_f32_16x16x32_bf16 v[128:131], a[92:95], a[124:127], v[128:131]\n"
"s_sub_u32 s99, s99, s98\n"
"v_accvgpr_read_b32 v13, a25\n"
"v_accvgpr_read_b32 v14, a26\n"
"v_mfma_f32_16x16x32_bf16 v[100:103], a[84:87], a[124:127], v[100:103]\n"
"s_sub_u32 s99, s99, s98\n"
"ds_read_b128 a[124:127], v4 offset:23552\n"
"v_accvgpr_read_b32 v15, a27\n"
"v_mfma_f32_16x16x32_bf16 v[100:103], a[80:83], a[120:123], v[100:103]\n"
"s_sub_u32 s99, s99, s98\n"
"ds_read_b128 a[120:123], v4 offset:22528\n"
"v_add_f32 v12, v12, v24\n"
"v_mfma_f32_16x16x32_bf16 v[104:107], a[80:83], a[128:131], v[104:107]\n"
"s_sub_u32 s99, s99, s98\n"
"v_add_f32 v13, v13, v25\n"
"v_add_f32 v14, v14, v26\n"
"v_mfma_f32_16x16x32_bf16 v[104:107], a[84:87], a[132:135], v[104:107]\n"
"global_load_dwordx4 a[72:75], v0, s[34:35] offset:-2048 sc1\n"
"s_sub_u32 s99, s99, s98\n"
"v_mfma_f32_16x16x32_bf16 v[132:135], a[92:95], a[132:135], v[132:135]\n"
"s_sub_u32 s99, s99, s98\n"
"ds_read_b128 a[132:135], v4 offset:25600\n"
"v_add_f32 v15, v15, v27\n"
"v_mfma_f32_16x16x32_bf16 v[132:135], a[88:91], a[128:131], v[132:135]\n"
"s_sub_u32 s99, s99, s98\n"
"ds_read_b128 a[128:131], v4 offset:24576\n"
"v_accvgpr_write_b32 a24, v12\n"
"v_mfma_f32_16x16x32_bf16 v[136:139], a[88:91], a[136:139], v[136:139]\n"
"s_sub_u32 s99, s99, s98\n"
"v_accvgpr_write_b32 a25, v13\n"
"v_accvgpr_write_b32 a26, v14\n"
"v_mfma_f32_16x16x32_bf16 v[136:139], a[92:95], a[140:143], v[136:139]\n"
"s_sub_u32 s99, s99, s98\n"
"v_accvgpr_write_b32 a27, v15\n"
"v_lshlrev_b32 v28, 16, v30\n"
"v_mfma_f32_16x16x32_bf16 v[108:111], a[84:87], a[140:143], v[108:111]\n"
"s_sub_u32 s99, s99, s98\n"
"ds_read_b128 a[140:143], v4 offset:27648\n"
"v_and_b32 v29, s63, v30\n"
"v_mfma_f32_16x16x32_bf16 v[108:111], a[80:83], a[136:139], v[108:111]\n"
"s_sub_u32 s99, s99, s98\n"
"ds_read_b128 a[136:139], v4 offset:26624\n"
"v_lshlrev_b32 v30, 16, v31\n"
"v_mfma_f32_16x16x32_bf16 v[112:115], a[80:83], a[144:147], v[112:115]\n"
"s_sub_u32 s99, s99, s98\n"
"v_and_b32 v31, s63, v31\n"
"v_add_f32 v228, v228, v28\n"
"v_mfma_f32_16x16x32_bf16 v[112:115], a[84:87], a[148:151], v[112:115]\n"
"global_load_dwordx4 a[76:79], v0, s[34:35] offset:-1024 sc1\n"
"s_sub_u32 s99, s99, s98\n"
"v_mfma_f32_16x16x32_bf16 v[140:143], a[92:95], a[148:151], v[140:143]\n"
"s_sub_u32 s99, s99, s98\n"
"ds_read_b128 a[148:151], v4 offset:29696\n"
"v_add_f32 v229, v229, v29\n"
"v_mfma_f32_16x16x32_bf16 v[140:143], a[88:91], a[144:147], v[140:143]\n"
"s_sub_u32 s99, s99, s98\n"
"ds_read_b128 a[144:147], v4 offset:28672\n"
"v_add_f32 v230, v230, v30\n"
"v_mfma_f32_16x16x32_bf16 a[12:15], a[88:91], a[152:155], a[12:15]\n"
"s_memtime s[76:77]\n"
"v_add_f32 v231, v231, v31\n"
"v_add_f32 v232, v232, v28\n"
"v_mfma_f32_16x16x32_bf16 a[12:15], a[92:95], a[156:159], a[12:15]\n"
"v_add_f32 v233, v233, v29\n"
"v_add_f32 v234, v234, v30\n"
"v_mfma_f32_16x16x32_bf16 a[8:11], a[84:87], a[156:159], a[8:11]\n"
"ds_read_b128 a[156:159], v4 offset:31744\n"
"v_add_f32 v235, v235, v31\n"
"v_mfma_f32_16x16x32_bf16 a[8:11], a[80:83], a[152:155], a[8:11]\n"
"s_waitcnt vmcnt(8) lgkmcnt(11)\n"
"ds_read_b128 a[152:155], v4 offset:30720\n"
"v_add_f32 v236, v236, v28\n"
"v_mfma_f32_16x16x32_bf16 v[32:35], a[32:35], a[96:99], v[32:35]\n"
"v_add_f32 v237, v237, v29\n"
"v_add_f32 v238, v238, v30\n"
"v_mfma_f32_16x16x32_bf16 v[32:35], a[36:39], a[100:103], v[32:35]\n"
"global_load_dwordx4 a[80:83], v0, s[34:35] offset:0 sc1\n"
"v_add_f32 v239, v239, v31\n"
"v_mfma_f32_16x16x32_bf16 v[60:63], a[44:47], a[100:103], v[60:63]\n"
"v_add_f32 v240, v240, v28\n"
"v_add_f32 v241, v241, v29\n"
"v_mfma_f32_16x16x32_bf16 v[60:63], a[40:43], a[96:99], v[60:63]\n"
"v_add_f32 v242, v242, v30\n"
"v_mfma_f32_16x16x32_bf16 v[64:67], a[40:43], a[104:107], v[64:67]\n"
"v_add_f32 v243, v243, v31\n"
"v_add_f32 v244, v244, v28\n"
"v_mfma_f32_16x16x32_bf16 v[64:67], a[44:47], a[108:111], v[64:67]\n"
"v_add_f32 v245, v245, v29\n"
"v_mfma_f32_16x16x32_bf16 v[36:39], a[36:39], a[108:111], v[36:39]\n"
"v_add_f32 v246, v246, v30\n"
"v_mfma_f32_16x16x32_bf16 v[36:39], a[32:35], a[104:107], v[36:39]\n"
"s_waitcnt lgkmcnt(8)\n"
"v_add_f32 v247, v247, v31\n"
"v_mfma_f32_16x16x32_bf16 v[40:43], a[32:35], a[112:115], v[40:43]\n"
"v_add_f32 v248, v248, v28\n"
"v_add_f32 v249, v249, v29\n"
"v_mfma_f32_16x16x32_bf16 v[40:43], a[36:39], a[116:119], v[40:43]\n"
"global_load_dwordx4 a[84:87], v0, s[34:35] offset:1024 sc1\n"
"s_add_u32 s48, s56, %12\n"
"s_addc_u32 s49, s57, 0\n"
"v_mfma_f32_16x16x32_bf16 v[68:71], a[44:47], a[116:119], v[68:71]\n"
"v_add_f32 v250, v250, v30\n"
"v_add_f32 v251, v251, v31\n"
"v_mfma_f32_16x16x32_bf16 v[68:71], a[40:43], a[112:115], v[68:71]\n"
"s_mov_b32 s50, 32768\n"
"v_add_f32 v252, v252, v28\n"
"v_add_f32 v253, v253, v29\n"
"v_mfma_f32_16x16x32_bf16 v[72:75], a[40:43], a[120:123], v[72:75]\n"
"v_add_f32 v254, v254, v30\n"
"v_mfma_f32_16x16x32_bf16 v[72:75], a[44:47], a[124:127], v[72:75]\n"
"s_mov_b32 s65, s78\n"
"v_add_f32 v255, v255, v31\n"
"v_mfma_f32_16x16x32_bf16 v[44:47], a[36:39], a[124:127], v[44:47]\n"
"v_accvgpr_read_b32 v12, a28\n"
"v_accvgpr_read_b32 v13, a29\n"
"v_mfma_f32_16x16x32_bf16 v[44:47], a[32:35], a[120:123], v[44:47]\n"
"s_mov_b64 s[40:41], s[52:53]\n"
"v_accvgpr_read_b32 v14, a30\n"
"s_waitcnt lgkmcnt(4)\n"
"v_mfma_f32_16x16x32_bf16 v[48:51], a[32:35], a[128:131], v[48:51]\n"
"s_mov_b32 s27, %21\n"
"v_accvgpr_read_b32 v15, a31\n"
"v_add_f32 v12, v12, v28\n"
"v_mfma_f32_16x16x32_bf16 v[48:51], a[36:39], a[132:135], v[48:51]\n"
"global_load_dwordx4 a[88:91], v0, s[34:35] offset:2048 sc1\n"
"s_mov_b32 s28, %23\n"
"v_mfma_f32_16x16x32_bf16 v[76:79], a[44:47], a[132:135], v[76:79]\n"
"s_waitcnt lgkmcnt(0)\n"
"v_add_f32 v13, v13, v29\n"
"v_add_f32 v14, v14, v30\n"
"v_mfma_f32_16x16x32_bf16 v[76:79], a[40:43], a[128:131], v[76:79]\n"
"s_sub_u32 s29, s76, s29\n"
"v_add_f32 v15, v15, v31\n"
"v_mfma_f32_16x16x32_bf16 v[80:83], a[40:43], a[136:139], v[80:83]\n"
"s_cmp_lt_u32 s29, 620\n"
"s_cselect_b32 s99, s99, 0\n"
"v_accvgpr_write_b32 a28, v12\n"
"v_mfma_f32_16x16x32_bf16 v[80:83], a[44:47], a[140:143], v[80:83]\n"
"s_max_u32 s30, s30, s99\n"
"v_accvgpr_write_b32 a29, v13\n"
"v_accvgpr_write_b32 a30, v14\n"
"v_mfma_f32_16x16x32_bf16 v[52:55], a[36:39], a[140:143], v[52:55]\n"
"s_cmp_eq_u32 s30, 0\n"
"s_cselect_b32 s30, 10, s30\n"
"v_accvgpr_write_b32 a31, v15\n"
"v_mfma_f32_16x16x32_bf16 v[52:55], a[32:35], a[136:139], v[52:55]\n"
"s_cmp_eq_u32 s31, 0\n"
"s_cselect_b32 s31, 10, s31\n"
"v_mfma_f32_16x16x32_bf16 v[56:59], a[32:35], a[144:147], v[56:59]\n"
"s_sub_u32 s69, s31, s30\n"
"v_mfma_f32_16x16x32_bf16 v[56:59], a[36:39], a[148:151], v[56:59]\n"
"global_load_dwordx4 a[92:95], v0, s[34:35] offset:3072 sc1\n"
"v_mov_b32 v20, s69\n"
"v_mfma_f32_16x16x32_bf16 v[84:87], a[44:47], a[148:151], v[84:87]\n"
"ds_write_b32 v2, v20 offset:8192\n"
"v_mfma_f32_16x16x32_bf16 v[84:87], a[40:43], a[144:147], v[84:87]\n"
"v_mfma_f32_16x16x32_bf16 a[4:7], a[40:43], a[152:155], a[4:7]\n"
"s_add_u32 s34, s34, 8192\n"
"v_mfma_f32_16x16x32_bf16 a[4:7], a[44:47], a[156:159], a[4:7]\n"
"L_st3856:\n"
"s_cbranch_execz L_retA\n"
"v_mfma_f32_16x16x32_bf16 a[0:3], a[36:39], a[156:159], a[0:3]\n"
"v_mfma_f32_16x16x32_bf16 a[0:3], a[32:35], a[152:155], a[0:3]\n"
"s_waitcnt lgkmcnt(0)\n"
"s_barrier\n"
"s_waitcnt vmcnt(8)\n"
"v_mfma_f32_16x16x32_bf16 v[88:91], a[48:51], a[96:99], v[88:91]\n"
"ds_read_b32 v21, v3 offset:8192\n"
"v_mfma_f32_16x16x32_bf16 v[88:91], a[52:55], a[100:103], v[88:91]\n"
"global_load_dwordx4 a[32:35], v0, s[34:35] offset:-4096 sc1\n"
"v_mfma_f32_16x16x32_bf16 v[116:119], a[60:63], a[100:103], v[116:119]\n"
"ds_read_b128 a[100:103], v5 offset:17408\n"
"v_mfma_f32_16x16x32_bf16 v[116:119], a[56:59], a[96:99], v[116:119]\n"
"ds_read_b128 a[96:99], v5 offset:16384\n"
"v_mfma_f32_16x16x32_bf16 v[120:123], a[56:59], a[104:107], v[120:123]\n"
"ds_read_b32 v22, v4 offset:8192\n"
"v_mfma_f32_16x16x32_bf16 v[120:123], a[60:63], a[108:111], v[120:123]\n"
"ds_read_b32 v23, v5 offset:8192\n"
"v_mfma_f32_16x16x32_bf16 v[92:95], a[52:55], a[108:111], v[92:95]\n"
"ds_read_b128 a[108:111], v5 offset:19456\n"
"v_mfma_f32_16x16x32_bf16 v[92:95], a[48:51], a[104:107], v[92:95]\n"
"ds_read_b128 a[104:107], v5 offset:18432\n"
"v_mfma_f32_16x16x32_bf16 v[96:99], a[48:51], a[112:115], v[96:99]\n"
"v_mfma_f32_16x16x32_bf16 v[96:99], a[52:55], a[116:119], v[96:99]\n"
"global_load_dwordx4 a[36:39], v0, s[34:35] offset:-3072 sc1\n"
"v_mfma_f32_16x16x32_bf16 v[124:127], a[60:63], a[116:119], v[124:127]\n"
"s_waitcnt lgkmcnt(2)\n"
"ds_read_b128 a[116:119], v5 offset:21504\n"
"v_add_u32 v20, v20, v21\n"
"v_mfma_f32_16x16x32_bf16 v[124:127], a[56:59], a[112:115], v[124:127]\n"
"ds_read_b128 a[112:115], v5 offset:20480\n"
"v_mfma_f32_16x16x32_bf16 v[128:131], a[56:59], a[120:123], v[128:131]\n"
"v_add3_u32 v20, v20, v22, v23\n"
"v_mfma_f32_16x16x32_bf16 v[128:131], a[60:63], a[124:127], v[128:131]\n"
"v_mfma_f32_16x16x32_bf16 v[100:103], a[52:55], a[124:127], v[100:103]\n"
"ds_read_b128 a[124:127], v5 offset:23552\n"
"v_readfirstlane_b32 s69, v20\n"
"v_mfma_f32_16x16x32_bf16 v[100:103], a[48:51], a[120:123], v[100:103]\n"
"ds_read_b128 a[120:123], v5 offset:22528\n"
"v_mfma_f32_16x16x32_bf16 v[104:107], a[48:51], a[128:131], v[104:107]\n"
"v_mfma_f32_16x16x32_bf16 v[104:107], a[52:55], a[132:135], v[104:107]\n"
"s_cmp_le_i32 s69, -2\n"
"global_load_dwordx4 a[40:43], v0, s[34:35] offset:-2048 sc1\n"
"s_cselect_b32 s66, 2, 1\n"
"v_mfma_f32_16x16x32_bf16 v[132:135], a[60:63], a[132:135], v[132:135]\n"
"ds_read_b128 a[132:135], v5 offset:25600\n"
"v_mfma_f32_16x16x32_bf16 v[132:135], a[56:59], a[128:131], v[132:135]\n"
"s_cmp_ge_i32 s69, 2\n"
"ds_read_b128 a[128:131], v5 offset:24576\n"
"s_cselect_b32 s66, 3, s66\n"
"v_mfma_f32_16x16x32_bf16 v[136:139], a[56:59], a[136:139], v[136:139]\n"
"ds_read_b128 v[16:19], v7 offset:32768\n"
"v_mfma_f32_16x16x32_bf16 v[136:139], a[60:63], a[140:143], v[136:139]\n"
"s_lshl_b32 s73, s66, 13\n"
"s_xor_b32 s60, s79, s66\n"
"v_mfma_f32_16x16x32_bf16 v[108:111], a[52:55], a[140:143], v[108:111]\n"
"ds_read_b128 a[140:143], v5 offset:27648\n"
"v_mfma_f32_16x16x32_bf16 v[108:111], a[48:51], a[136:139], v[108:111]\n"
"s_cmp_eq_u32 s66, 3\n"
"ds_read_b128 a[136:139], v5 offset:26624\n"
"s_cselect_b32 s72, 8192, 0\n"
"v_mfma_f32_16x16x32_bf16 v[112:115], a[48:51], a[144:147], v[112:115]\n"
"s_abs_i32 s30, s69\n"
"v_mfma_f32_16x16x32_bf16 v[112:115], a[52:55], a[148:151], v[112:115]\n"
"global_load_dwordx4 a[44:47], v0, s[34:35] offset:-1024 sc1\n"
"s_add_u32 s52, s56, %11\n"
"s_addc_u32 s53, s57, 0\n"
"v_mfma_f32_16x16x32_bf16 v[140:143], a[60:63], a[148:151], v[140:143]\n"
"s_cmp_lt_u32 s30, 26\n"
"s_cselect_b32 s30, 2, 0\n"
"ds_read_b128 a[148:151], v5 offset:29696\n"
"v_mfma_f32_16x16x32_bf16 v[140:143], a[56:59], a[144:147], v[140:143]\n"
"s_add_u32 s52, s52, 4096\n"
"ds_read_b128 a[144:147], v5 offset:28672\n"
"s_addc_u32 s53, s53, 0\n"
"v_mfma_f32_16x16x32_bf16 a[12:15], a[56:59], a[152:155], a[12:15]\n"
"v_mfma_f32_16x16x32_bf16 a[12:15], a[60:63], a[156:159], a[12:15]\n"
"s_add_u32 s54, s52, s73\n"
"s_addc_u32 s55, s53, 0\n"
"v_mfma_f32_16x16x32_bf16 a[8:11], a[52:55], a[156:159], a[8:11]\n"
"ds_read_b128 a[156:159], v5 offset:31744\n"
"v_mfma_f32_16x16x32_bf16 a[8:11], a[48:51], a[152:155], a[8:11]\n"
"s_add_u32 s52, s52, s72\n"
"ds_read_b128 a[152:155], v5 offset:30720\n"
"s_addc_u32 s53, s53, 0\n"
"L_P6:\n"
"s_waitcnt vmcnt(8) lgkmcnt(13)\n"
"v_mfma_f32_16x16x32_bf16 v[32:35], a[64:67], a[96:99], v[32:35]\n"
"v_mfma_f32_16x16x32_bf16 v[32:35], a[68:71], a[100:103], v[32:35]\n"
"global_load_dwordx4 a[48:51], v0, s[34:35] offset:0 sc1\n"
"v_mfma_f32_16x16x32_bf16 v[60:63], a[76:79], a[100:103], v[60:63]\n"
"v_mfma_f32_16x16x32_bf16 v[60:63], a[72:75], a[96:99], v[60:63]\n"
"v_mfma_f32_16x16x32_bf16 v[64:67], a[72:75], a[104:107], v[64:67]\n"
"v_mfma_f32_16x16x32_bf16 v[64:67], a[76:79], a[108:111], v[64:67]\n"
"v_mfma_f32_16x16x32_bf16 v[36:39], a[68:71], a[108:111], v[36:39]\n"
"s_waitcnt lgkmcnt(6)\n"
"v_lshlrev_b32 v12, 16, v16\n"
"v_mfma_f32_16x16x32_bf16 v[36:39], a[64:67], a[104:107], v[36:39]\n"
"v_and_b32 v13, s63, v16\n"
"v_lshlrev_b32 v14, 16, v17\n"
"v_mfma_f32_16x16x32_bf16 v[40:43], a[64:67], a[112:115], v[40:43]\n"
"v_and_b32 v15, s63, v17\n"
"v_mfma_f32_16x16x32_bf16 v[40:43], a[68:71], a[116:119], v[40:43]\n"
"global_load_dwordx4 a[52:55], v0, s[34:35] offset:1024 sc1\n"
"ds_read_b128 v[20:23], v7 offset:34816\n"
"v_mfma_f32_16x16x32_bf16 v[68:71], a[76:79], a[116:119], v[68:71]\n"
"v_mul_f32 v12, v12, v32\n"
"v_mul_f32 v13, v13, v33\n"
"v_mfma_f32_16x16x32_bf16 v[68:71], a[72:75], a[112:115], v[68:71]\n"
"v_mul_f32 v14, v14, v34\n"
"v_mfma_f32_16x16x32_bf16 v[72:75], a[72:75], a[120:123], v[72:75]\n"
"v_mul_f32 v15, v15, v35\n"
"v_mfma_f32_16x16x32_bf16 v[72:75], a[76:79], a[124:127], v[72:75]\n"
"v_cvt_pk_bf16_f32 v24, v12, v13\n"
"v_cvt_pk_bf16_f32 v25, v14, v15\n"
"v_mfma_f32_16x16x32_bf16 v[44:47], a[68:71], a[124:127], v[44:47]\n"
"ds_write_b64 v2, v[24:25] offset:0\n"
"v_mfma_f32_16x16x32_bf16 v[44:47], a[64:67], a[120:123], v[44:47]\n"
"s_waitcnt lgkmcnt(6)\n"
"v_lshlrev_b32 v12, 16, v18\n"
"v_mfma_f32_16x16x32_bf16 v[48:51], a[64:67], a[128:131], v[48:51]\n"
"v_and_b32 v13, s63, v18\n"
"v_lshlrev_b32 v14, 16, v19\n"
"v_mfma_f32_16x16x32_bf16 v[48:51], a[68:71], a[132:135], v[48:51]\n"
"global_load_dwordx4 a[56:59], v0, s[34:35] offset:2048 sc1\n"
"v_and_b32 v15, s63, v19\n"
"v_mfma_f32_16x16x32_bf16 v[76:79], a[76:79], a[132:135], v[76:79]\n"
"v_mul_f32 v12, v12, v60\n"
"v_mfma_f32_16x16x32_bf16 v[76:79], a[72:75], a[128:131], v[76:79]\n"
"v_mul_f32 v13, v13, v61\n"
"v_mul_f32 v14, v14, v62\n"
"v_mfma_f32_16x16x32_bf16 v[80:83], a[72:75], a[136:139], v[80:83]\n"
"v_mul_f32 v15, v15, v63\n"
"v_mfma_f32_16x16x32_bf16 v[80:83], a[76:79], a[140:143], v[80:83]\n"
"v_cvt_pk_bf16_f32 v26, v12, v13\n"
"v_cvt_pk_bf16_f32 v27, v14, v15\n"
"v_mfma_f32_16x16x32_bf16 v[52:55], a[68:71], a[140:143], v[52:55]\n"
"ds_write_b64 v2, v[26:27] offset:8\n"
"v_mfma_f32_16x16x32_bf16 v[52:55], a[64:67], a[136:139], v[52:55]\n"
"s_waitcnt lgkmcnt(2)\n"
"v_lshlrev_b32 v12, 16, v20\n"
"v_mfma_f32_16x16x32_bf16 v[56:59], a[64:67], a[144:147], v[56:59]\n"
"v_and_b32 v13, s63, v20\n"
"v_mfma_f32_16x16x32_bf16 v[56:59], a[68:71], a[148:151], v[56:59]\n"
"global_load_dwordx4 a[60:63], v0, s[34:35] offset:3072 sc1\n"
"v_lshlrev_b32 v14, 16, v21\n"
"v_mfma_f32_16x16x32_bf16 v[84:87], a[76:79], a[148:151], v[84:87]\n"
"v_and_b32 v15, s63, v21\n"
"v_xor_b32 v16, 64, v7\n"
"v_mfma_f32_16x16x32_bf16 v[84:87], a[72:75], a[144:147], v[84:87]\n"
"ds_read_b128 v[16:19], v16 offset:32768\n"
"v_mfma_f32_16x16x32_bf16 a[4:7], a[72:75], a[152:155], a[4:7]\n"
"s_add_u32 s34, s34, 8192\n"
"v_mul_f32 v12, v12, v36\n"
"v_mul_f32 v13, v13, v37\n"
"v_mfma_f32_16x16x32_bf16 a[4:7], a[76:79], a[156:159], a[4:7]\n"
"v_mul_f32 v14, v14, v38\n"
"v_mfma_f32_16x16x32_bf16 a[0:3], a[68:71], a[156:159], a[0:3]\n"
"v_mul_f32 v15, v15, v39\n"
"v_mfma_f32_16x16x32_bf16 a[0:3], a[64:67], a[152:155], a[0:3]\n"
"s_waitcnt vmcnt(8)\n"
"v_cvt_pk_bf16_f32 v28, v12, v13\n"
"v_cvt_pk_bf16_f32 v29, v14, v15\n"
"v_mfma_f32_16x16x32_bf16 v[88:91], a[80:83], a[96:99], v[88:91]\n"
"v_lshlrev_b32 v12, 16, v22\n"
"v_mfma_f32_16x16x32_bf16 v[88:91], a[84:87], a[100:103], v[88:91]\n"
"global_load_dwordx4 a[64:67], v0, s[34:35] offset:-4096 sc1\n"
"v_and_b32 v13, s63, v22\n"
"v_mfma_f32_16x16x32_bf16 v[116:119], a[92:95], a[100:103], v[116:119]\n"
"v_lshlrev_b32 v14, 16, v23\n"
"v_and_b32 v15, s63, v23\n"
"v_mfma_f32_16x16x32_bf16 v[116:119], a[88:91], a[96:99], v[116:119]\n"
"v_mul_f32 v12, v12, v64\n"
"v_mfma_f32_16x16x32_bf16 v[120:123], a[88:91], a[104:107], v[120:123]\n"
"v_mul_f32 v13, v13, v65\n"
"v_mfma_f32_16x16x32_bf16 v[120:123], a[92:95], a[108:111], v[120:123]\n"
"v_mul_f32 v14, v14, v66\n"
"v_mul_f32 v15, v15, v67\n"
"v_mfma_f32_16x16x32_bf16 v[92:95], a[84:87], a[108:111], v[92:95]\n"
"v_cvt_pk_bf16_f32 v30, v12, v13\n"
"v_mfma_f32_16x16x32_bf16 v[92:95], a[80:83], a[104:107], v[92:95]\n"
"v_cvt_pk_bf16_f32 v31, v14, v15\n"
"v_mfma_f32_16x16x32_bf16 v[96:99], a[80:83], a[112:115], v[96:99]\n"
"global_store_dwordx4 v0, v[28:31], s[52:53] offset:-2048 sc1\n"
"ds_write_b128 v2, v[28:31] offset:2048\n"
"v_mfma_f32_16x16x32_bf16 v[96:99], a[84:87], a[116:119], v[96:99]\n"
"s_waitcnt lgkmcnt(1)\n"
"global_load_dwordx4 a[68:71], v0, s[34:35] offset:-3072 sc1\n"
"v_lshlrev_b32 v12, 16, v16\n"
"v_mfma_f32_16x16x32_bf16 v[124:127], a[92:95], a[116:119], v[124:127]\n"
"v_and_b32 v13, s63, v16\n"
"v_mfma_f32_16x16x32_bf16 v[124:127], a[88:91], a[112:115], v[124:127]\n"
"v_lshlrev_b32 v14, 16, v17\n"
"v_and_b32 v15, s63, v17\n"
"v_mfma_f32_16x16x32_bf16 v[128:131], a[88:91], a[120:123], v[128:131]\n"
"v_xor_b32 v20, 64, v7\n"
"v_mfma_f32_16x16x32_bf16 v[128:131], a[92:95], a[124:127], v[128:131]\n"
"ds_read_b128 v[20:23], v20 offset:34816\n"
"v_mfma_f32_16x16x32_bf16 v[100:103], a[84:87], a[124:127], v[100:103]\n"
"v_mul_f32 v12, v12, v88\n"
"v_mul_f32 v13, v13, v89\n"
"v_mfma_f32_16x16x32_bf16 v[100:103], a[80:83], a[120:123], v[100:103]\n"
"v_mul_f32 v14, v14, v90\n"
"v_mfma_f32_16x16x32_bf16 v[104:107], a[80:83], a[128:131], v[104:107]\n"
"v_mul_f32 v15, v15, v91\n"
"v_mfma_f32_16x16x32_bf16 v[104:107], a[84:87], a[132:135], v[104:107]\n"
"global_load_dwordx4 a[72:75], v0, s[34:35] offset:-2048 sc1\n"
"v_cvt_pk_bf16_f32 v24, v12, v13\n"
"v_mfma_f32_16x16x32_bf16 v[132:135], a[92:95], a[132:135], v[132:135]\n"
"v_cvt_pk_bf16_f32 v25, v14, v15\n"
"v_lshlrev_b32 v12, 16, v18\n"
"v_mfma_f32_16x16x32_bf16 v[132:135], a[88:91], a[128:131], v[132:135]\n"
"v_and_b32 v13, s63, v18\n"
"v_mfma_f32_16x16x32_bf16 v[136:139], a[88:91], a[136:139], v[136:139]\n"
"v_lshlrev_b32 v14, 16, v19\n"
"v_and_b32 v15, s63, v19\n"
"v_mfma_f32_16x16x32_bf16 v[136:139], a[92:95], a[140:143], v[136:139]\n"
"v_mul_f32 v12, v12, v116\n"
"v_mfma_f32_16x16x32_bf16 v[108:111], a[84:87], a[140:143], v[108:111]\n"
"v_mul_f32 v13, v13, v117\n"
"v_mfma_f32_16x16x32_bf16 v[108:111], a[80:83], a[136:139], v[108:111]\n"
"v_mul_f32 v14, v14, v118\n"
"v_mul_f32 v15, v15, v119\n"
"v_mfma_f32_16x16x32_bf16 v[112:115], a[80:83], a[144:147], v[112:115]\n"
"v_cvt_pk_bf16_f32 v26, v12, v13\n"
"v_mfma_f32_16x16x32_bf16 v[112:115], a[84:87], a[148:151], v[112:115]\n"
"global_load_dwordx4 a[76:79], v0, s[34:35] offset:-1024 sc1\n"
"v_cvt_pk_bf16_f32 v27, v14, v15\n"
"v_mfma_f32_16x16x32_bf16 v[140:143], a[92:95], a[148:151], v[140:143]\n"
"global_store_dwordx4 v0, v[24:27], s[52:53] offset:-3072 sc1\n"
"ds_write_b128 v2, v[24:27] offset:1024\n"
"v_mfma_f32_16x16x32_bf16 v[140:143], a[88:91], a[144:147], v[140:143]\n"
"s_waitcnt lgkmcnt(1)\n"
"v_lshlrev_b32 v12, 16, v20\n"
"v_mfma_f32_16x16x32_bf16 a[12:15], a[88:91], a[152:155], a[12:15]\n"
"v_and_b32 v13, s63, v20\n"
"v_mfma_f32_16x16x32_bf16 a[12:15], a[92:95], a[156:159], a[12:15]\n"
"v_lshlrev_b32 v14, 16, v21\n"
"v_mfma_f32_16x16x32_bf16 a[8:11], a[84:87], a[156:159], a[8:11]\n"
"ds_read_b128 v[16:19], v7 offset:36864\n"
"v_and_b32 v15, s63, v21\n"
"v_mfma_f32_16x16x32_bf16 a[8:11], a[80:83], a[152:155], a[8:11]\n"
"s_waitcnt vmcnt(10)\n"
"v_mul_f32 v12, v12, v92\n"
"v_mfma_f32_16x16x32_bf16 v[144:147], a[32:35], a[96:99], v[144:147]\n"
"v_mul_f32 v13, v13, v93\n"
"v_mul_f32 v14, v14, v94\n"
"v_mfma_f32_16x16x32_bf16 v[144:147], a[36:39], a[100:103], v[144:147]\n"
"global_load_dwordx4 a[80:83], v0, s[34:35] offset:0 sc1\n"
"v_mul_f32 v15, v15, v95\n"
"v_mfma_f32_16x16x32_bf16 v[172:175], a[44:47], a[100:103], v[172:175]\n"
"v_cvt_pk_bf16_f32 v28, v12, v13\n"
"v_mfma_f32_16x16x32_bf16 v[172:175], a[40:43], a[96:99], v[172:175]\n"
"v_cvt_pk_bf16_f32 v29, v14, v15\n"
"v_mfma_f32_16x16x32_bf16 v[176:179], a[40:43], a[104:107], v[176:179]\n"
"ds_write_b64 v2, v[28:29] offset:3072\n"
"v_mfma_f32_16x16x32_bf16 v[176:179], a[44:47], a[108:111], v[176:179]\n"
"v_lshlrev_b32 v12, 16, v22\n"
"v_and_b32 v13, s63, v22\n"
"v_mfma_f32_16x16x32_bf16 v[148:151], a[36:39], a[108:111], v[148:151]\n"
"v_lshlrev_b32 v14, 16, v23\n"
"v_mfma_f32_16x16x32_bf16 v[148:151], a[32:35], a[104:107], v[148:151]\n"
"v_and_b32 v15, s63, v23\n"
"v_mul_f32 v12, v12, v120\n"
"v_mfma_f32_16x16x32_bf16 v[152:155], a[32:35], a[112:115], v[152:155]\n"
"v_mul_f32 v13, v13, v121\n"
"v_mfma_f32_16x16x32_bf16 v[152:155], a[36:39], a[116:119], v[152:155]\n"
"global_load_dwordx4 a[84:87], v0, s[34:35] offset:1024 sc1\n"
"v_mul_f32 v14, v14, v122\n"
"v_mfma_f32_16x16x32_bf16 v[180:183], a[44:47], a[116:119], v[180:183]\n"
"v_mul_f32 v15, v15, v123\n"
"v_cvt_pk_bf16_f32 v30, v12, v13\n"
"v_mfma_f32_16x16x32_bf16 v[180:183], a[40:43], a[112:115], v[180:183]\n"
"v_cvt_pk_bf16_f32 v31, v14, v15\n"
"v_mfma_f32_16x16x32_bf16 v[184:187], a[40:43], a[120:123], v[184:187]\n"
"ds_write_b64 v2, v[30:31] offset:3080\n"
"v_mfma_f32_16x16x32_bf16 v[184:187], a[44:47], a[124:127], v[184:187]\n"
"s_waitcnt lgkmcnt(2)\n"
"v_lshlrev_b32 v12, 16, v16\n"
"v_and_b32 v13, s63, v16\n"
"v_mfma_f32_16x16x32_bf16 v[156:159], a[36:39], a[124:127], v[156:159]\n"
"v_lshlrev_b32 v14, 16, v17\n"
"v_mfma_f32_16x16x32_bf16 v[156:159], a[32:35], a[120:123], v[156:159]\n"
"v_and_b32 v15, s63, v17\n"
"v_xor_b32 v20, 64, v7\n"
"v_mfma_f32_16x16x32_bf16 v[160:163], a[32:35], a[128:131], v[160:163]\n"
"ds_read_b128 v[20:23], v20 offset:36864\n"
"v_mfma_f32_16x16x32_bf16 v[160:163], a[36:39], a[132:135], v[160:163]\n"
"global_load_dwordx4 a[88:91], v0, s[34:35] offset:2048 sc1\n"
"v_mul_f32 v12, v12, v40\n"
"v_mfma_f32_16x16x32_bf16 v[188:191], a[44:47], a[132:135], v[188:191]\n"
"v_mul_f32 v13, v13, v41\n"
"v_mfma_f32_16x16x32_bf16 v[188:191], a[40:43], a[128:131], v[188:191]\n"
"v_mul_f32 v14, v14, v42\n"
"v_mul_f32 v15, v15, v43\n"
"v_mfma_f32_16x16x32_bf16 v[192:195], a[40:43], a[136:139], v[192:195]\n"
"v_cvt_pk_bf16_f32 v24, v12, v13\n"
"v_mfma_f32_16x16x32_bf16 v[192:195], a[44:47], a[140:143], v[192:195]\n"
"v_cvt_pk_bf16_f32 v25, v14, v15\n"
"v_mfma_f32_16x16x32_bf16 v[164:167], a[36:39], a[140:143], v[164:167]\n"
"v_lshlrev_b32 v12, 16, v18\n"
"v_and_b32 v13, s63, v18\n"
"v_mfma_f32_16x16x32_bf16 v[164:167], a[32:35], a[136:139], v[164:167]\n"
"v_lshlrev_b32 v14, 16, v19\n"
"v_mfma_f32_16x16x32_bf16 v[168:171], a[32:35], a[144:147], v[168:171]\n"
"v_and_b32 v15, s63, v19\n"
"v_mul_f32 v12, v12, v68\n"
"v_mfma_f32_16x16x32_bf16 v[168:171], a[36:39], a[148:151], v[168:171]\n"
"global_load_dwordx4 a[92:95], v0, s[34:35] offset:3072 sc1\n"
"v_mul_f32 v13, v13, v69\n"
"v_mfma_f32_16x16x32_bf16 v[196:199], a[44:47], a[148:151], v[196:199]\n"
"v_mul_f32 v14, v14, v70\n"
"v_mfma_f32_16x16x32_bf16 v[196:199], a[40:43], a[144:147], v[196:199]\n"
"v_mul_f32 v15, v15, v71\n"
"v_mfma_f32_16x16x32_bf16 a[20:23], a[40:43], a[152:155], a[20:23]\n"
"s_add_u32 s34, s34, 8192\n"
"v_cvt_pk_bf16_f32 v26, v12, v13\n"
"v_cvt_pk_bf16_f32 v27, v14, v15\n"
"v_mfma_f32_16x16x32_bf16 a[20:23], a[44:47], a[156:159], a[20:23]\n"
"global_store_dwordx4 v0, v[24:27], s[52:53] offset:0 sc1\n"
"ds_write_b128 v2, v[24:27] offset:4096\n"
"v_mfma_f32_16x16x32_bf16 a[16:19], a[36:39], a[156:159], a[16:19]\n"
"s_waitcnt lgkmcnt(1)\n"
"v_lshlrev_b32 v12, 16, v20\n"
"v_mfma_f32_16x16x32_bf16 a[16:19], a[32:35], a[152:155], a[16:19]\n"
"s_waitcnt vmcnt(11)\n"
"v_and_b32 v13, s63, v20\n"
"v_mfma_f32_16x16x32_bf16 v[200:203], a[48:51], a[96:99], v[200:203]\n"
"v_lshlrev_b32 v14, 16, v21\n"
"v_and_b32 v15, s63, v21\n"
"v_mfma_f32_16x16x32_bf16 v[200:203], a[52:55], a[100:103], v[200:203]\n"
"global_load_dwordx4 a[32:35], v0, s[34:35] offset:-4096 sc1\n"
"ds_read_b128 v[16:19], v7 offset:38912\n"
"v_mfma_f32_16x16x32_bf16 v[228:231], a[60:63], a[100:103], v[228:231]\n"
"ds_read_b128 a[100:103], v4 offset:17408\n"
"v_mul_f32 v12, v12, v96\n"
"v_mfma_f32_16x16x32_bf16 v[228:231], a[56:59], a[96:99], v[228:231]\n"
"ds_read_b128 a[96:99], v4 offset:16384\n"
"v_mul_f32 v13, v13, v97\n"
"v_mfma_f32_16x16x32_bf16 v[232:235], a[56:59], a[104:107], v[232:235]\n"
"v_mul_f32 v14, v14, v98\n"
"v_mfma_f32_16x16x32_bf16 v[232:235], a[60:63], a[108:111], v[232:235]\n"
"v_mul_f32 v15, v15, v99\n"
"v_cvt_pk_bf16_f32 v28, v12, v13\n"
"v_mfma_f32_16x16x32_bf16 v[204:207], a[52:55], a[108:111], v[204:207]\n"
"ds_read_b128 a[108:111], v4 offset:19456\n"
"v_cvt_pk_bf16_f32 v29, v14, v15\n"
"v_mfma_f32_16x16x32_bf16 v[204:207], a[48:51], a[104:107], v[204:207]\n"
"ds_read_b128 a[104:107], v4 offset:18432\n"
"v_lshlrev_b32 v12, 16, v22\n"
"v_mfma_f32_16x16x32_bf16 v[208:211], a[48:51], a[112:115], v[208:211]\n"
"v_and_b32 v13, s63, v22\n"
"v_lshlrev_b32 v14, 16, v23\n"
"v_mfma_f32_16x16x32_bf16 v[208:211], a[52:55], a[116:119], v[208:211]\n"
"global_load_dwordx4 a[36:39], v0, s[34:35] offset:-3072 sc1\n"
"v_and_b32 v15, s63, v23\n"
"v_mfma_f32_16x16x32_bf16 v[236:239], a[60:63], a[116:119], v[236:239]\n"
"ds_read_b128 a[116:119], v4 offset:21504\n"
"v_mul_f32 v12, v12, v124\n"
"v_mfma_f32_16x16x32_bf16 v[236:239], a[56:59], a[112:115], v[236:239]\n"
"ds_read_b128 a[112:115], v4 offset:20480\n"
"v_mul_f32 v13, v13, v125\n"
"v_mfma_f32_16x16x32_bf16 v[240:243], a[56:59], a[120:123], v[240:243]\n"
"v_mul_f32 v14, v14, v126\n"
"v_mul_f32 v15, v15, v127\n"
"v_mfma_f32_16x16x32_bf16 v[240:243], a[60:63], a[124:127], v[240:243]\n"
"v_cvt_pk_bf16_f32 v30, v12, v13\n"
"v_mfma_f32_16x16x32_bf16 v[212:215], a[52:55], a[124:127], v[212:215]\n"
"ds_read_b128 a[124:127], v4 offset:23552\n"
"v_cvt_pk_bf16_f32 v31, v14, v15\n"
"v_mfma_f32_16x16x32_bf16 v[212:215], a[48:51], a[120:123], v[212:215]\n"
"ds_read_b128 a[120:123], v4 offset:22528\n"
"v_mfma_f32_16x16x32_bf16 v[216:219], a[48:51], a[128:131], v[216:219]\n"
"global_store_dwordx4 v0, v[28:31], s[52:53] offset:1024 sc1\n"
"ds_write_b128 v2, v[28:31] offset:5120\n"
"v_mfma_f32_16x16x32_bf16 v[216:219], a[52:55], a[132:135], v[216:219]\n"
"s_waitcnt lgkmcnt(9)\n"
"global_load_dwordx4 a[40:43], v0, s[34:35] offset:-2048 sc1\n"
"v_lshlrev_b32 v12, 16, v16\n"
"v_mfma_f32_16x16x32_bf16 v[244:247], a[60:63], a[132:135], v[244:247]\n"
"ds_read_b128 a[132:135], v4 offset:25600\n"
"v_and_b32 v13, s63, v16\n"
"v_mfma_f32_16x16x32_bf16 v[244:247], a[56:59], a[128:131], v[244:247]\n"
"ds_read_b128 a[128:131], v4 offset:24576\n"
"v_lshlrev_b32 v14, 16, v17\n"
"v_mfma_f32_16x16x32_bf16 v[248:251], a[56:59], a[136:139], v[248:251]\n"
"v_and_b32 v15, s63, v17\n"
"v_xor_b32 v20, 64, v7\n"
"v_mfma_f32_16x16x32_bf16 v[248:251], a[60:63], a[140:143], v[248:251]\n"
"ds_read_b128 v[20:23], v20 offset:38912\n"
"v_mul_f32 v12, v12, v44\n"
"v_mfma_f32_16x16x32_bf16 v[220:223], a[52:55], a[140:143], v[220:223]\n"
"ds_read_b128 a[140:143], v4 offset:27648\n"
"v_mul_f32 v13, v13, v45\n"
"v_mfma_f32_16x16x32_bf16 v[220:223], a[48:51], a[136:139], v[220:223]\n"
"ds_read_b128 a[136:139], v4 offset:26624\n"
"v_mul_f32 v14, v14, v46\n"
"v_mfma_f32_16x16x32_bf16 v[224:227], a[48:51], a[144:147], v[224:227]\n"
"v_mul_f32 v15, v15, v47\n"
"v_cvt_pk_bf16_f32 v24, v12, v13\n"
"v_mfma_f32_16x16x32_bf16 v[224:227], a[52:55], a[148:151], v[224:227]\n"
"global_load_dwordx4 a[44:47], v0, s[34:35] offset:-1024 sc1\n"
"v_cvt_pk_bf16_f32 v25, v14, v15\n"
"v_mfma_f32_16x16x32_bf16 v[252:255], a[60:63], a[148:151], v[252:255]\n"
"ds_read_b128 a[148:151], v4 offset:29696\n"
"v_mfma_f32_16x16x32_bf16 v[252:255], a[56:59], a[144:147], v[252:255]\n"
"ds_read_b128 a[144:147], v4 offset:28672\n"
"v_mfma_f32_16x16x32_bf16 a[28:31], a[56:59], a[152:155], a[28:31]\n"
"ds_write_b64 v2, v[24:25] offset:6144\n"
"v_mfma_f32_16x16x32_bf16 a[28:31], a[60:63], a[156:159], a[28:31]\n"
"v_lshlrev_b32 v12, 16, v18\n"
"v_and_b32 v13, s63, v18\n"
"v_mfma_f32_16x16x32_bf16 a[24:27], a[52:55], a[156:159], a[24:27]\n"
"ds_read_b128 a[156:159], v4 offset:31744\n"
"v_lshlrev_b32 v14, 16, v19\n"
"v_mfma_f32_16x16x32_bf16 a[24:27], a[48:51], a[152:155], a[24:27]\n"
"s_waitcnt vmcnt(11) lgkmcnt(14)\n"
"ds_read_b128 a[152:155], v4 offset:30720\n"
"v_and_b32 v15, s63, v19\n"
"v_mfma_f32_16x16x32_bf16 v[144:147], a[64:67], a[96:99], v[144:147]\n"
"v_mul_f32 v12, v12, v72\n"
"v_mul_f32 v13, v13, v73\n"
"v_mfma_f32_16x16x32_bf16 v[144:147], a[68:71], a[100:103], v[144:147]\n"
"global_load_dwordx4 a[48:51], v0, s[34:35] offset:0 sc1\n"
"v_mul_f32 v14, v14, v74\n"
"v_mfma_f32_16x16x32_bf16 v[172:175], a[76:79], a[100:103], v[172:175]\n"
"v_mul_f32 v15, v15, v75\n"
"v_cvt_pk_bf16_f32 v26, v12, v13\n"
"v_mfma_f32_16x16x32_bf16 v[172:175], a[72:75], a[96:99], v[172:175]\n"
"v_cvt_pk_bf16_f32 v27, v14, v15\n"
"v_mfma_f32_16x16x32_bf16 v[176:179], a[72:75], a[104:107], v[176:179]\n"
"ds_write_b64 v2, v[26:27] offset:6152\n"
"v_mfma_f32_16x16x32_bf16 v[176:179], a[76:79], a[108:111], v[176:179]\n"
"s_waitcnt lgkmcnt(8)\n"
"v_lshlrev_b32 v12, 16, v20\n"
"v_and_b32 v13, s63, v20\n"
"v_mfma_f32_16x16x32_bf16 v[148:151], a[68:71], a[108:111], v[148:151]\n"
"v_lshlrev_b32 v14, 16, v21\n"
"v_and_b32 v15, s63, v21\n"
"v_mfma_f32_16x16x32_bf16 v[148:151], a[64:67], a[104:107], v[148:151]\n"
"v_mul_f32 v12, v12, v100\n"
"v_mul_f32 v13, v13, v101\n"
"v_mfma_f32_16x16x32_bf16 v[152:155], a[64:67], a[112:115], v[152:155]\n"
"v_mul_f32 v14, v14, v102\n"
"v_mul_f32 v15, v15, v103\n"
"v_mfma_f32_16x16x32_bf16 v[152:155], a[68:71], a[116:119], v[152:155]\n"
"global_load_dwordx4 a[52:55], v0, s[34:35] offset:1024 sc1\n"
"v_cvt_pk_bf16_f32 v28, v12, v13\n"
"v_mfma_f32_16x16x32_bf16 v[180:183], a[76:79], a[116:119], v[180:183]\n"
"v_cvt_pk_bf16_f32 v29, v14, v15\n"
"v_lshlrev_b32 v12, 16, v22\n"
"v_mfma_f32_16x16x32_bf16 v[180:183], a[72:75], a[112:115], v[180:183]\n"
"v_and_b32 v13, s63, v22\n"
"v_lshlrev_b32 v14, 16, v23\n"
"v_mfma_f32_16x16x32_bf16 v[184:187], a[72:75], a[120:123], v[184:187]\n"
"v_and_b32 v15, s63, v23\n"
"v_mul_f32 v12, v12, v128\n"
"v_mfma_f32_16x16x32_bf16 v[184:187], a[76:79], a[124:127], v[184:187]\n"
"v_mul_f32 v13, v13, v129\n"
"v_mfma_f32_16x16x32_bf16 v[156:159], a[68:71], a[124:127], v[156:159]\n"
"v_mul_f32 v14, v14, v130\n"
"v_mfma_f32_16x16x32_bf16 v[156:159], a[64:67], a[120:123], v[156:159]\n"
"s_waitcnt lgkmcnt(6)\n"
"v_mul_f32 v15, v15, v131\n"
"v_cvt_pk_bf16_f32 v30, v12, v13\n"
"v_mfma_f32_16x16x32_bf16 v[160:163], a[64:67], a[128:131], v[160:163]\n"
"v_cvt_pk_bf16_f32 v31, v14, v15\n"
"v_mfma_f32_16x16x32_bf16 v[160:163], a[68:71], a[132:135], v[160:163]\n"
"global_load_dwordx4 a[56:59], v0, s[34:35] offset:2048 sc1\n"
"v_mfma_f32_16x16x32_bf16 v[188:191], a[76:79], a[132:135], v[188:191]\n"
"global_store_dwordx4 v0, v[28:31], s[52:53] offset:3072 sc1\n"
"ds_write_b128 v2, v[28:31] offset:7168\n"
"v_mfma_f32_16x16x32_bf16 v[188:191], a[72:75], a[128:131], v[188:191]\n"
"v_accvgpr_read_b32 v16, a160\n"
"v_accvgpr_read_b32 v17, a161\n"
"v_mfma_f32_16x16x32_bf16 v[192:195], a[72:75], a[136:139], v[192:195]\n"
"v_accvgpr_read_b32 v18, a162\n"
"v_mfma_f32_16x16x32_bf16 v[192:195], a[76:79], a[140:143], v[192:195]\n"
"v_accvgpr_read_b32 v19, a163\n"
"v_lshlrev_b32 v12, 16, v16\n"
"v_mfma_f32_16x16x32_bf16 v[164:167], a[68:71], a[140:143], v[164:167]\n"
"v_and_b32 v13, s63, v16\n"
"v_mfma_f32_16x16x32_bf16 v[164:167], a[64:67], a[136:139], v[164:167]\n"
"s_waitcnt lgkmcnt(2)\n"
"v_lshlrev_b32 v14, 16, v17\n"
"v_mfma_f32_16x16x32_bf16 v[168:171], a[64:67], a[144:147], v[168:171]\n"
"v_and_b32 v15, s63, v17\n"
"v_mul_f32 v12, v12, v48\n"
"v_mfma_f32_16x16x32_bf16 v[168:171], a[68:71], a[148:151], v[168:171]\n"
"global_load_dwordx4 a[60:63], v0, s[34:35] offset:3072 sc1\n"
"v_mul_f32 v13, v13, v49\n"
"v_mfma_f32_16x16x32_bf16 v[196:199], a[76:79], a[148:151], v[196:199]\n"
"v_mul_f32 v14, v14, v50\n"
"v_mfma_f32_16x16x32_bf16 v[196:199], a[72:75], a[144:147], v[196:199]\n"
"v_mul_f32 v15, v15, v51\n"
"v_cvt_pk_bf16_f32 v24, v12, v13\n"
"v_mfma_f32_16x16x32_bf16 a[20:23], a[72:75], a[152:155], a[20:23]\n"
"s_add_u32 s34, s34, 8192\n"
"v_cvt_pk_bf16_f32 v25, v14, v15\n"
"v_mfma_f32_16x16x32_bf16 a[20:23], a[76:79], a[156:159], a[20:23]\n"
"v_lshlrev_b32 v12, 16, v18\n"
"v_mfma_f32_16x16x32_bf16 a[16:19], a[68:71], a[156:159], a[16:19]\n"
"v_and_b32 v13, s63, v18\n"
"v_lshlrev_b32 v14, 16, v19\n"
"v_mfma_f32_16x16x32_bf16 a[16:19], a[64:67], a[152:155], a[16:19]\n"
"s_waitcnt vmcnt(11)\n"
"v_and_b32 v15, s63, v19\n"
"v_mul_f32 v12, v12, v76\n"
"v_mfma_f32_16x16x32_bf16 v[200:203], a[80:83], a[96:99], v[200:203]\n"
"v_mul_f32 v13, v13, v77\n"
"v_mul_f32 v14, v14, v78\n"
"v_mfma_f32_16x16x32_bf16 v[200:203], a[84:87], a[100:103], v[200:203]\n"
"global_load_dwordx4 a[64:67], v0, s[34:35] offset:-4096 sc1\n"
"v_mul_f32 v15, v15, v79\n"
"v_mfma_f32_16x16x32_bf16 v[228:231], a[92:95], a[100:103], v[228:231]\n"
"ds_read_b128 a[100:103], v3 offset:17408\n"
"v_cvt_pk_bf16_f32 v26, v12, v13\n"
"v_mfma_f32_16x16x32_bf16 v[228:231], a[88:91], a[96:99], v[228:231]\n"
"ds_read_b128 a[96:99], v3 offset:16384\n"
"v_cvt_pk_bf16_f32 v27, v14, v15\n"
"v_mfma_f32_16x16x32_bf16 v[232:235], a[88:91], a[104:107], v[232:235]\n"
"global_store_dwordx4 v0, v[24:27], s[54:55] offset:-4096 sc1\n"
"ds_write_b128 v2, v[24:27] offset:8192\n"
"v_mfma_f32_16x16x32_bf16 v[232:235], a[92:95], a[108:111], v[232:235]\n"
"v_accvgpr_read_b32 v16, a164\n"
"v_mfma_f32_16x16x32_bf16 v[204:207], a[84:87], a[108:111], v[204:207]\n"
"ds_read_b128 a[108:111], v3 offset:19456\n"
"v_accvgpr_read_b32 v17, a165\n"
"v_mfma_f32_16x16x32_bf16 v[204:207], a[80:83], a[104:107], v[204:207]\n"
"ds_read_b128 a[104:107], v3 offset:18432\n"
"v_accvgpr_read_b32 v18, a166\n"
"v_mfma_f32_16x16x32_bf16 v[208:211], a[80:83], a[112:115], v[208:211]\n"
"v_accvgpr_read_b32 v19, a167\n"
"v_lshlrev_b32 v12, 16, v16\n"
"v_mfma_f32_16x16x32_bf16 v[208:211], a[84:87], a[116:119], v[208:211]\n"
"global_load_dwordx4 a[68:71], v0, s[34:35] offset:-3072 sc1\n"
"v_and_b32 v13, s63, v16\n"
"v_mfma_f32_16x16x32_bf16 v[236:239], a[92:95], a[116:119], v[236:239]\n"
"ds_read_b128 a[116:119], v3 offset:21504\n"
"v_lshlrev_b32 v14, 16, v17\n"
"v_mfma_f32_16x16x32_bf16 v[236:239], a[88:91], a[112:115], v[236:239]\n"
"ds_read_b128 a[112:115], v3 offset:20480\n"
"v_and_b32 v15, s63, v17\n"
"v_mfma_f32_16x16x32_bf16 v[240:243], a[88:91], a[120:123], v[240:243]\n"
"v_mul_f32 v12, v12, v104\n"
"v_mul_f32 v13, v13, v105\n"
"v_mfma_f32_16x16x32_bf16 v[240:243], a[92:95], a[124:127], v[240:243]\n"
"v_mul_f32 v14, v14, v106\n"
"v_mul_f32 v15, v15, v107\n"
"v_mfma_f32_16x16x32_bf16 v[212:215], a[84:87], a[124:127], v[212:215]\n"
"ds_read_b128 a[124:127], v3 offset:23552\n"
"v_cvt_pk_bf16_f32 v28, v12, v13\n"
"v_mfma_f32_16x16x32_bf16 v[212:215], a[80:83], a[120:123], v[212:215]\n"
"ds_read_b128 a[120:123], v3 offset:22528\n"
"v_cvt_pk_bf16_f32 v29, v14, v15\n"
"v_mfma_f32_16x16x32_bf16 v[216:219], a[80:83], a[128:131], v[216:219]\n"
"ds_write_b64 v2, v[28:29] offset:9216\n"
"v_mfma_f32_16x16x32_bf16 v[216:219], a[84:87], a[132:135], v[216:219]\n"
"global_load_dwordx4 a[72:75], v0, s[34:35] offset:-2048 sc1\n"
"v_lshlrev_b32 v12, 16, v18\n"
"v_mfma_f32_16x16x32_bf16 v[244:247], a[92:95], a[132:135], v[244:247]\n"
"ds_read_b128 a[132:135], v3 offset:25600\n"
"v_and_b32 v13, s63, v18\n"
"v_mfma_f32_16x16x32_bf16 v[244:247], a[88:91], a[128:131], v[244:247]\n"
"ds_read_b128 a[128:131], v3 offset:24576\n"
"v_lshlrev_b32 v14, 16, v19\n"
"v_mfma_f32_16x16x32_bf16 v[248:251], a[88:91], a[136:139], v[248:251]\n"
"v_and_b32 v15, s63, v19\n"
"v_mul_f32 v12, v12, v132\n"
"v_mfma_f32_16x16x32_bf16 v[248:251], a[92:95], a[140:143], v[248:251]\n"
"v_mul_f32 v13, v13, v133\n"
"v_mfma_f32_16x16x32_bf16 v[220:223], a[84:87], a[140:143], v[220:223]\n"
"ds_read_b128 a[140:143], v3 offset:27648\n"
"v_mul_f32 v14, v14, v134\n"
"v_mfma_f32_16x16x32_bf16 v[220:223], a[80:83], a[136:139], v[220:223]\n"
"ds_read_b128 a[136:139], v3 offset:26624\n"
"v_mul_f32 v15, v15, v135\n"
"v_mfma_f32_16x16x32_bf16 v[224:227], a[80:83], a[144:147], v[224:227]\n"
"v_cvt_pk_bf16_f32 v30, v12, v13\n"
"v_cvt_pk_bf16_f32 v31, v14, v15\n"
"v_mfma_f32_16x16x32_bf16 v[224:227], a[84:87], a[148:151], v[224:227]\n"
"global_load_dwordx4 a[76:79], v0, s[34:35] offset:-1024 sc1\n"
"v_mfma_f32_16x16x32_bf16 v[252:255], a[92:95], a[148:151], v[252:255]\n"
"ds_read_b128 a[148:151], v3 offset:29696\n"
"v_mfma_f32_16x16x32_bf16 v[252:255], a[88:91], a[144:147], v[252:255]\n"
"ds_read_b128 a[144:147], v3 offset:28672\n"
"v_mfma_f32_16x16x32_bf16 a[28:31], a[88:91], a[152:155], a[28:31]\n"
"ds_write_b64 v2, v[30:31] offset:9224\n"
"v_mfma_f32_16x16x32_bf16 a[28:31], a[92:95], a[156:159], a[28:31]\n"
"v_accvgpr_read_b32 v16, a168\n"
"v_accvgpr_read_b32 v17, a169\n"
"v_mfma_f32_16x16x32_bf16 a[24:27], a[84:87], a[156:159], a[24:27]\n"
"ds_read_b128 a[156:159], v3 offset:31744\n"
"v_accvgpr_read_b32 v18, a170\n"
"v_mfma_f32_16x16x32_bf16 a[24:27], a[80:83], a[152:155], a[24:27]\n"
"s_waitcnt vmcnt(10) lgkmcnt(13)\n"
"ds_read_b128 a[152:155], v3 offset:30720\n"
"v_accvgpr_read_b32 v19, a171\n"
"v_mfma_f32_16x16x32_bf16 v[144:147], a[32:35], a[96:99], v[144:147]\n"
"v_lshlrev_b32 v12, 16, v16\n"
"v_and_b32 v13, s63, v16\n"
"v_mfma_f32_16x16x32_bf16 v[144:147], a[36:39], a[100:103], v[144:147]\n"
"global_load_dwordx4 a[80:83], v0, s[34:35] offset:0 sc1\n"
"v_lshlrev_b32 v14, 16, v17\n"
"v_mfma_f32_16x16x32_bf16 v[172:175], a[44:47], a[100:103], v[172:175]\n"
"v_and_b32 v15, s63, v17\n"
"v_mul_f32 v12, v12, v52\n"
"v_mfma_f32_16x16x32_bf16 v[172:175], a[40:43], a[96:99], v[172:175]\n"
"v_mul_f32 v13, v13, v53\n"
"v_mul_f32 v14, v14, v54\n"
"v_mfma_f32_16x16x32_bf16 v[176:179], a[40:43], a[104:107], v[176:179]\n"
"v_mul_f32 v15, v15, v55\n"
"v_cvt_pk_bf16_f32 v24, v12, v13\n"
"v_mfma_f32_16x16x32_bf16 v[176:179], a[44:47], a[108:111], v[176:179]\n"
"v_cvt_pk_bf16_f32 v25, v14, v15\n"
"v_lshlrev_b32 v12, 16, v18\n"
"v_mfma_f32_16x16x32_bf16 v[148:151], a[36:39], a[108:111], v[148:151]\n"
"v_and_b32 v13, s63, v18\n"
"v_lshlrev_b32 v14, 16, v19\n"
"v_mfma_f32_16x16x32_bf16 v[148:151], a[32:35], a[104:107], v[148:151]\n"
"s_waitcnt lgkmcnt(10)\n"
"v_and_b32 v15, s63, v19\n"
"v_mul_f32 v12, v12, v80\n"
"v_mfma_f32_16x16x32_bf16 v[152:155], a[32:35], a[112:115], v[152:155]\n"
"L_st4479:\n"
"s_cbranch_execz L_retA\n"
"v_mul_f32 v13, v13, v81\n"
"v_mul_f32 v14, v14, v82\n"
"v_mfma_f32_16x16x32_bf16 v[152:155], a[36:39], a[116:119], v[152:155]\n"
"global_load_dwordx4 a[84:87], v0, s[34:35] offset:1024 sc1\n"
"v_mul_f32 v15, v15, v83\n"
"v_mfma_f32_16x16x32_bf16 v[180:183], a[44:47], a[116:119], v[180:183]\n"
"v_cvt_pk_bf16_f32 v26, v12, v13\n"
"v_mfma_f32_16x16x32_bf16 v[180:183], a[40:43], a[112:115], v[180:183]\n"
"v_cvt_pk_bf16_f32 v27, v14, v15\n"
"v_mfma_f32_16x16x32_bf16 v[184:187], a[40:43], a[120:123], v[184:187]\n"
"global_store_dwordx4 v0, v[24:27], s[54:55] offset:-2048 sc1\n"
"ds_write_b128 v2, v[24:27] offset:10240\n"
"v_mfma_f32_16x16x32_bf16 v[184:187], a[44:47], a[124:127], v[184:187]\n"
"v_accvgpr_read_b32 v16, a172\n"
"v_mfma_f32_16x16x32_bf16 v[156:159], a[36:39], a[124:127], v[156:159]\n"
"v_accvgpr_read_b32 v17, a173\n"
"v_mfma_f32_16x16x32_bf16 v[156:159], a[32:35], a[120:123], v[156:159]\n"
"s_waitcnt lgkmcnt(6)\n"
"v_accvgpr_read_b32 v18, a174\n"
"v_accvgpr_read_b32 v19, a175\n"
"v_mfma_f32_16x16x32_bf16 v[160:163], a[32:35], a[128:131], v[160:163]\n"
"v_lshlrev_b32 v12, 16, v16\n"
"v_mfma_f32_16x16x32_bf16 v[160:163], a[36:39], a[132:135], v[160:163]\n"
"global_load_dwordx4 a[88:91], v0, s[34:35] offset:2048 sc1\n"
"v_and_b32 v13, s63, v16\n"
"v_mfma_f32_16x16x32_bf16 v[188:191], a[44:47], a[132:135], v[188:191]\n"
"v_lshlrev_b32 v14, 16, v17\n"
"v_and_b32 v15, s63, v17\n"
"v_mfma_f32_16x16x32_bf16 v[188:191], a[40:43], a[128:131], v[188:191]\n"
"v_mul_f32 v12, v12, v108\n"
"v_mfma_f32_16x16x32_bf16 v[192:195], a[40:43], a[136:139], v[192:195]\n"
"v_mul_f32 v13, v13, v109\n"
"v_mfma_f32_16x16x32_bf16 v[192:195], a[44:47], a[140:143], v[192:195]\n"
"v_mul_f32 v14, v14, v110\n"
"v_mul_f32 v15, v15, v111\n"
"v_mfma_f32_16x16x32_bf16 v[164:167], a[36:39], a[140:143], v[164:167]\n"
"v_cvt_pk_bf16_f32 v28, v12, v13\n"
"v_mfma_f32_16x16x32_bf16 v[164:167], a[32:35], a[136:139], v[164:167]\n"
"s_waitcnt lgkmcnt(1)\n"
"v_cvt_pk_bf16_f32 v29, v14, v15\n"
"v_mfma_f32_16x16x32_bf16 v[168:171], a[32:35], a[144:147], v[168:171]\n"
"v_lshlrev_b32 v12, 16, v18\n"
"v_and_b32 v13, s63, v18\n"
"v_mfma_f32_16x16x32_bf16 v[168:171], a[36:39], a[148:151], v[168:171]\n"
"global_load_dwordx4 a[92:95], v0, s[34:35] offset:3072 sc1\n"
"v_lshlrev_b32 v14, 16, v19\n"
"v_mfma_f32_16x16x32_bf16 v[196:199], a[44:47], a[148:151], v[196:199]\n"
"v_and_b32 v15, s63, v19\n"
"v_mfma_f32_16x16x32_bf16 v[196:199], a[40:43], a[144:147], v[196:199]\n"
"v_mul_f32 v12, v12, v136\n"
"v_mul_f32 v13, v13, v137\n"
"v_mfma_f32_16x16x32_bf16 a[20:23], a[40:43], a[152:155], a[20:23]\n"
"s_add_u32 s34, s34, 8192\n"
"v_mul_f32 v14, v14, v138\n"
"v_mfma_f32_16x16x32_bf16 a[20:23], a[44:47], a[156:159], a[20:23]\n"
"v_mul_f32 v15, v15, v139\n"
"v_mfma_f32_16x16x32_bf16 a[16:19], a[36:39], a[156:159], a[16:19]\n"
"v_cvt_pk_bf16_f32 v30, v12, v13\n"
"v_cvt_pk_bf16_f32 v31, v14, v15\n"
"v_mfma_f32_16x16x32_bf16 a[16:19], a[32:35], a[152:155], a[16:19]\n"
"s_waitcnt vmcnt(10)\n"
"global_store_dwordx4 v0, v[28:31], s[54:55] offset:-1024 sc1\n"
"ds_write_b128 v2, v[28:31] offset:11264\n"
"v_mfma_f32_16x16x32_bf16 v[200:203], a[48:51], a[96:99], v[200:203]\n"
"v_accvgpr_read_b32 v16, a176\n"
"v_mfma_f32_16x16x32_bf16 v[200:203], a[52:55], a[100:103], v[200:203]\n"
"global_load_dwordx4 a[32:35], v0, s[34:35] offset:-4096 sc1\n"
"v_accvgpr_read_b32 v17, a177\n"
"v_mfma_f32_16x16x32_bf16 v[228:231], a[60:63], a[100:103], v[228:231]\n"
"ds_read_b128 a[100:103], v2 offset:17408\n"
"v_accvgpr_read_b32 v18, a178\n"
"v_mfma_f32_16x16x32_bf16 v[228:231], a[56:59], a[96:99], v[228:231]\n"
"ds_read_b128 a[96:99], v2 offset:16384\n"
"v_accvgpr_read_b32 v19, a179\n"
"v_mfma_f32_16x16x32_bf16 v[232:235], a[56:59], a[104:107], v[232:235]\n"
"v_lshlrev_b32 v12, 16, v16\n"
"v_and_b32 v13, s63, v16\n"
"v_mfma_f32_16x16x32_bf16 v[232:235], a[60:63], a[108:111], v[232:235]\n"
"v_lshlrev_b32 v14, 16, v17\n"
"v_mfma_f32_16x16x32_bf16 v[204:207], a[52:55], a[108:111], v[204:207]\n"
"ds_read_b128 a[108:111], v2 offset:19456\n"
"v_and_b32 v15, s63, v17\n"
"v_mfma_f32_16x16x32_bf16 v[204:207], a[48:51], a[104:107], v[204:207]\n"
"ds_read_b128 a[104:107], v2 offset:18432\n"
"v_mul_f32 v12, v12, v56\n"
"v_mfma_f32_16x16x32_bf16 v[208:211], a[48:51], a[112:115], v[208:211]\n"
"v_mul_f32 v13, v13, v57\n"
"v_mul_f32 v14, v14, v58\n"
"v_mfma_f32_16x16x32_bf16 v[208:211], a[52:55], a[116:119], v[208:211]\n"
"global_load_dwordx4 a[36:39], v0, s[34:35] offset:-3072 sc1\n"
"v_mul_f32 v15, v15, v59\n"
"v_mfma_f32_16x16x32_bf16 v[236:239], a[60:63], a[116:119], v[236:239]\n"
"ds_read_b128 a[116:119], v2 offset:21504\n"
"v_cvt_pk_bf16_f32 v24, v12, v13\n"
"v_mfma_f32_16x16x32_bf16 v[236:239], a[56:59], a[112:115], v[236:239]\n"
"ds_read_b128 a[112:115], v2 offset:20480\n"
"v_cvt_pk_bf16_f32 v25, v14, v15\n"
"v_mfma_f32_16x16x32_bf16 v[240:243], a[56:59], a[120:123], v[240:243]\n"
"ds_write_b64 v2, v[24:25] offset:12288\n"
"v_mfma_f32_16x16x32_bf16 v[240:243], a[60:63], a[124:127], v[240:243]\n"
"v_lshlrev_b32 v12, 16, v18\n"
"v_and_b32 v13, s63, v18\n"
"v_mfma_f32_16x16x32_bf16 v[212:215], a[52:55], a[124:127], v[212:215]\n"
"ds_read_b128 a[124:127], v2 offset:23552\n"
"v_lshlrev_b32 v14, 16, v19\n"
"v_mfma_f32_16x16x32_bf16 v[212:215], a[48:51], a[120:123], v[212:215]\n"
"ds_read_b128 a[120:123], v2 offset:22528\n"
"v_and_b32 v15, s63, v19\n"
"v_mfma_f32_16x16x32_bf16 v[216:219], a[48:51], a[128:131], v[216:219]\n"
"v_mul_f32 v12, v12, v84\n"
"v_mul_f32 v13, v13, v85\n"
"v_mfma_f32_16x16x32_bf16 v[216:219], a[52:55], a[132:135], v[216:219]\n"
"global_load_dwordx4 a[40:43], v0, s[34:35] offset:-2048 sc1\n"
"v_mul_f32 v14, v14, v86\n"
"v_mfma_f32_16x16x32_bf16 v[244:247], a[60:63], a[132:135], v[244:247]\n"
"ds_read_b128 a[132:135], v2 offset:25600\n"
"v_mul_f32 v15, v15, v87\n"
"v_mfma_f32_16x16x32_bf16 v[244:247], a[56:59], a[128:131], v[244:247]\n"
"ds_read_b128 a[128:131], v2 offset:24576\n"
"v_cvt_pk_bf16_f32 v26, v12, v13\n"
"v_mfma_f32_16x16x32_bf16 v[248:251], a[56:59], a[136:139], v[248:251]\n"
"v_cvt_pk_bf16_f32 v27, v14, v15\n"
"v_mfma_f32_16x16x32_bf16 v[248:251], a[60:63], a[140:143], v[248:251]\n"
"ds_write_b64 v2, v[26:27] offset:12296\n"
"v_mfma_f32_16x16x32_bf16 v[220:223], a[52:55], a[140:143], v[220:223]\n"
"ds_read_b128 a[140:143], v2 offset:27648\n"
"v_accvgpr_read_b32 v16, a180\n"
"v_mfma_f32_16x16x32_bf16 v[220:223], a[48:51], a[136:139], v[220:223]\n"
"ds_read_b128 a[136:139], v2 offset:26624\n"
"v_accvgpr_read_b32 v17, a181\n"
"v_mfma_f32_16x16x32_bf16 v[224:227], a[48:51], a[144:147], v[224:227]\n"
"v_accvgpr_read_b32 v18, a182\n"
"v_accvgpr_read_b32 v19, a183\n"
"v_mfma_f32_16x16x32_bf16 v[224:227], a[52:55], a[148:151], v[224:227]\n"
"global_load_dwordx4 a[44:47], v0, s[34:35] offset:-1024 sc1\n"
"v_lshlrev_b32 v12, 16, v16\n"
"v_mfma_f32_16x16x32_bf16 v[252:255], a[60:63], a[148:151], v[252:255]\n"
"ds_read_b128 a[148:151], v2 offset:29696\n"
"v_and_b32 v13, s63, v16\n"
"v_mfma_f32_16x16x32_bf16 v[252:255], a[56:59], a[144:147], v[252:255]\n"
"ds_read_b128 a[144:147], v2 offset:28672\n"
"v_lshlrev_b32 v14, 16, v17\n"
"v_mfma_f32_16x16x32_bf16 a[28:31], a[56:59], a[152:155], a[28:31]\n"
"v_and_b32 v15, s63, v17\n"
"v_mul_f32 v12, v12, v112\n"
"v_mfma_f32_16x16x32_bf16 a[28:31], a[60:63], a[156:159], a[28:31]\n"
"v_mul_f32 v13, v13, v113\n"
"v_mul_f32 v14, v14, v114\n"
"v_mfma_f32_16x16x32_bf16 a[24:27], a[52:55], a[156:159], a[24:27]\n"
"ds_read_b128 a[156:159], v2 offset:31744\n"
"v_mul_f32 v15, v15, v115\n"
"v_mfma_f32_16x16x32_bf16 a[24:27], a[48:51], a[152:155], a[24:27]\n"
"s_waitcnt vmcnt(10) lgkmcnt(13)\n"
"ds_read_b128 a[152:155], v2 offset:30720\n"
"v_cvt_pk_bf16_f32 v28, v12, v13\n"
"v_mfma_f32_16x16x32_bf16 v[144:147], a[64:67], a[96:99], v[144:147]\n"
"v_cvt_pk_bf16_f32 v29, v14, v15\n"
"v_lshlrev_b32 v12, 16, v18\n"
"v_mfma_f32_16x16x32_bf16 v[144:147], a[68:71], a[100:103], v[144:147]\n"
"global_load_dwordx4 a[48:51], v0, s[34:35] offset:0 sc1\n"
"v_and_b32 v13, s63, v18\n"
"v_mfma_f32_16x16x32_bf16 v[172:175], a[76:79], a[100:103], v[172:175]\n"
"v_lshlrev_b32 v14, 16, v19\n"
"v_and_b32 v15, s63, v19\n"
"v_mfma_f32_16x16x32_bf16 v[172:175], a[72:75], a[96:99], v[172:175]\n"
"v_mul_f32 v12, v12, v140\n"
"v_mul_f32 v13, v13, v141\n"
"v_mfma_f32_16x16x32_bf16 v[176:179], a[72:75], a[104:107], v[176:179]\n"
"v_mul_f32 v14, v14, v142\n"
"v_mul_f32 v15, v15, v143\n"
"v_mfma_f32_16x16x32_bf16 v[176:179], a[76:79], a[108:111], v[176:179]\n"
"v_cvt_pk_bf16_f32 v30, v12, v13\n"
"v_mfma_f32_16x16x32_bf16 v[148:151], a[68:71], a[108:111], v[148:151]\n"
"v_cvt_pk_bf16_f32 v31, v14, v15\n"
"v_mfma_f32_16x16x32_bf16 v[148:151], a[64:67], a[104:107], v[148:151]\n"
"s_waitcnt lgkmcnt(9)\n"
"global_store_dwordx4 v0, v[28:31], s[54:55] offset:1024 sc1\n"
"ds_write_b128 v2, v[28:31] offset:13312\n"
"v_mfma_f32_16x16x32_bf16 v[152:155], a[64:67], a[112:115], v[152:155]\n"
"v_accvgpr_read_b32 v16, a184\n"
"v_accvgpr_read_b32 v17, a185\n"
"v_mfma_f32_16x16x32_bf16 v[152:155], a[68:71], a[116:119], v[152:155]\n"
"global_load_dwordx4 a[52:55], v0, s[34:35] offset:1024 sc1\n"
"v_accvgpr_read_b32 v18, a186\n"
"v_mfma_f32_16x16x32_bf16 v[180:183], a[76:79], a[116:119], v[180:183]\n"
"v_accvgpr_read_b32 v19, a187\n"
"v_mfma_f32_16x16x32_bf16 v[180:183], a[72:75], a[112:115], v[180:183]\n"
"v_lshlrev_b32 v12, 16, v16\n"
"v_mfma_f32_16x16x32_bf16 v[184:187], a[72:75], a[120:123], v[184:187]\n"
"v_and_b32 v13, s63, v16\n"
"v_lshlrev_b32 v14, 16, v17\n"
"v_mfma_f32_16x16x32_bf16 v[184:187], a[76:79], a[124:127], v[184:187]\n"
"v_and_b32 v15, s63, v17\n"
"v_mfma_f32_16x16x32_bf16 v[156:159], a[68:71], a[124:127], v[156:159]\n"
"v_accvgpr_read_b32 v10, a0\n"
"v_mfma_f32_16x16x32_bf16 v[156:159], a[64:67], a[120:123], v[156:159]\n"
"s_waitcnt lgkmcnt(5)\n"
"v_accvgpr_read_b32 v11, a1\n"
"v_mul_f32 v12, v12, v10\n"
"v_mfma_f32_16x16x32_bf16 v[160:163], a[64:67], a[128:131], v[160:163]\n"
"v_mul_f32 v13, v13, v11\n"
"v_mfma_f32_16x16x32_bf16 v[160:163], a[68:71], a[132:135], v[160:163]\n"
"global_load_dwordx4 a[56:59], v0, s[34:35] offset:2048 sc1\n"
"v_accvgpr_read_b32 v10, a2\n"
"v_mfma_f32_16x16x32_bf16 v[188:191], a[76:79], a[132:135], v[188:191]\n"
"v_accvgpr_read_b32 v11, a3\n"
"v_mul_f32 v14, v14, v10\n"
"v_mfma_f32_16x16x32_bf16 v[188:191], a[72:75], a[128:131], v[188:191]\n"
"v_mul_f32 v15, v15, v11\n"
"v_mfma_f32_16x16x32_bf16 v[192:195], a[72:75], a[136:139], v[192:195]\n"
"v_cvt_pk_bf16_f32 v24, v12, v13\n"
"v_mfma_f32_16x16x32_bf16 v[192:195], a[76:79], a[140:143], v[192:195]\n"
"v_cvt_pk_bf16_f32 v25, v14, v15\n"
"v_lshlrev_b32 v12, 16, v18\n"
"v_mfma_f32_16x16x32_bf16 v[164:167], a[68:71], a[140:143], v[164:167]\n"
"v_and_b32 v13, s63, v18\n"
"v_mfma_f32_16x16x32_bf16 v[164:167], a[64:67], a[136:139], v[164:167]\n"
"s_waitcnt lgkmcnt(1)\n"
"v_lshlrev_b32 v14, 16, v19\n"
"v_mfma_f32_16x16x32_bf16 v[168:171], a[64:67], a[144:147], v[168:171]\n"
"v_and_b32 v15, s63, v19\n"
"v_accvgpr_read_b32 v10, a4\n"
"v_mfma_f32_16x16x32_bf16 v[168:171], a[68:71], a[148:151], v[168:171]\n"
"global_load_dwordx4 a[60:63], v0, s[34:35] offset:3072 sc1\n"
"v_accvgpr_read_b32 v11, a5\n"
"v_mfma_f32_16x16x32_bf16 v[196:199], a[76:79], a[148:151], v[196:199]\n"
"v_mul_f32 v12, v12, v10\n"
"v_mfma_f32_16x16x32_bf16 v[196:199], a[72:75], a[144:147], v[196:199]\n"
"v_mul_f32 v13, v13, v11\n"
"v_accvgpr_read_b32 v10, a6\n"
"v_mfma_f32_16x16x32_bf16 a[20:23], a[72:75], a[152:155], a[20:23]\n"
"s_add_u32 s34, s34, 8192\n"
"v_accvgpr_read_b32 v11, a7\n"
"v_mfma_f32_16x16x32_bf16 a[20:23], a[76:79], a[156:159], a[20:23]\n"
"v_mul_f32 v14, v14, v10\n"
"v_mul_f32 v15, v15, v11\n"
"v_mfma_f32_16x16x32_bf16 a[16:19], a[68:71], a[156:159], a[16:19]\n"
"v_cvt_pk_bf16_f32 v26, v12, v13\n"
"v_cvt_pk_bf16_f32 v27, v14, v15\n"
"v_mfma_f32_16x16x32_bf16 a[16:19], a[64:67], a[152:155], a[16:19]\n"
"s_waitcnt vmcnt(10)\n"
"global_store_dwordx4 v0, v[24:27], s[54:55] offset:2048 sc1\n"
"ds_write_b128 v2, v[24:27] offset:14336\n"
"v_mfma_f32_16x16x32_bf16 v[200:203], a[80:83], a[96:99], v[200:203]\n"
"v_accvgpr_read_b32 v16, a188\n"
"v_mfma_f32_16x16x32_bf16 v[200:203], a[84:87], a[100:103], v[200:203]\n"
"global_load_dwordx4 a[64:67], v0, s[34:35] offset:-4096 sc1\n"
"v_accvgpr_read_b32 v17, a189\n"
"v_mfma_f32_16x16x32_bf16 v[228:231], a[92:95], a[100:103], v[228:231]\n"
"ds_read_b128 a[100:103], v2 offset:1024\n"
"v_accvgpr_read_b32 v18, a190\n"
"v_mfma_f32_16x16x32_bf16 v[228:231], a[88:91], a[96:99], v[228:231]\n"
"ds_read_b128 a[96:99], v2 offset:0\n"
"v_accvgpr_read_b32 v19, a191\n"
"v_mfma_f32_16x16x32_bf16 v[232:235], a[88:91], a[104:107], v[232:235]\n"
"v_lshlrev_b32 v12, 16, v16\n"
"v_and_b32 v13, s63, v16\n"
"v_mfma_f32_16x16x32_bf16 v[232:235], a[92:95], a[108:111], v[232:235]\n"
"v_lshlrev_b32 v14, 16, v17\n"
"v_and_b32 v15, s63, v17\n"
"v_mfma_f32_16x16x32_bf16 v[204:207], a[84:87], a[108:111], v[204:207]\n"
"ds_read_b128 a[108:111], v2 offset:3072\n"
"v_accvgpr_read_b32 v10, a8\n"
"v_mfma_f32_16x16x32_bf16 v[204:207], a[80:83], a[104:107], v[204:207]\n"
"ds_read_b128 a[104:107], v2 offset:2048\n"
"v_accvgpr_read_b32 v11, a9\n"
"v_mfma_f32_16x16x32_bf16 v[208:211], a[80:83], a[112:115], v[208:211]\n"
"v_mul_f32 v12, v12, v10\n"
"v_mul_f32 v13, v13, v11\n"
"v_mfma_f32_16x16x32_bf16 v[208:211], a[84:87], a[116:119], v[208:211]\n"
"global_load_dwordx4 a[68:71], v0, s[34:35] offset:-3072 sc1\n"
"v_accvgpr_read_b32 v10, a10\n"
"v_mfma_f32_16x16x32_bf16 v[236:239], a[92:95], a[116:119], v[236:239]\n"
"ds_read_b128 a[116:119], v2 offset:5120\n"
"v_accvgpr_read_b32 v11, a11\n"
"v_mfma_f32_16x16x32_bf16 v[236:239], a[88:91], a[112:115], v[236:239]\n"
"ds_read_b128 a[112:115], v2 offset:4096\n"
"v_mul_f32 v14, v14, v10\n"
"v_mfma_f32_16x16x32_bf16 v[240:243], a[88:91], a[120:123], v[240:243]\n"
"v_mul_f32 v15, v15, v11\n"
"v_cvt_pk_bf16_f32 v28, v12, v13\n"
"v_mfma_f32_16x16x32_bf16 v[240:243], a[92:95], a[124:127], v[240:243]\n"
"v_cvt_pk_bf16_f32 v29, v14, v15\n"
"v_lshlrev_b32 v12, 16, v18\n"
"v_mfma_f32_16x16x32_bf16 v[212:215], a[84:87], a[124:127], v[212:215]\n"
"ds_read_b128 a[124:127], v2 offset:7168\n"
"v_and_b32 v13, s63, v18\n"
"v_mfma_f32_16x16x32_bf16 v[212:215], a[80:83], a[120:123], v[212:215]\n"
"ds_read_b128 a[120:123], v2 offset:6144\n"
"v_lshlrev_b32 v14, 16, v19\n"
"v_mfma_f32_16x16x32_bf16 v[216:219], a[80:83], a[128:131], v[216:219]\n"
"v_and_b32 v15, s63, v19\n"
"v_accvgpr_read_b32 v10, a12\n"
"v_mfma_f32_16x16x32_bf16 v[216:219], a[84:87], a[132:135], v[216:219]\n"
"global_load_dwordx4 a[72:75], v0, s[34:35] offset:-2048 sc1\n"
"v_accvgpr_read_b32 v11, a13\n"
"v_mfma_f32_16x16x32_bf16 v[244:247], a[92:95], a[132:135], v[244:247]\n"
"ds_read_b128 a[132:135], v2 offset:9216\n"
"v_mul_f32 v12, v12, v10\n"
"v_mfma_f32_16x16x32_bf16 v[244:247], a[88:91], a[128:131], v[244:247]\n"
"ds_read_b128 a[128:131], v2 offset:8192\n"
"v_mul_f32 v13, v13, v11\n"
"v_mfma_f32_16x16x32_bf16 v[248:251], a[88:91], a[136:139], v[248:251]\n"
"v_accvgpr_read_b32 v10, a14\n"
"v_accvgpr_read_b32 v11, a15\n"
"v_mfma_f32_16x16x32_bf16 v[248:251], a[92:95], a[140:143], v[248:251]\n"
"v_mul_f32 v14, v14, v10\n"
"v_mul_f32 v15, v15, v11\n"
"v_mfma_f32_16x16x32_bf16 v[220:223], a[84:87], a[140:143], v[220:223]\n"
"ds_read_b128 a[140:143], v2 offset:11264\n"
"v_cvt_pk_bf16_f32 v30, v12, v13\n"
"v_mfma_f32_16x16x32_bf16 v[220:223], a[80:83], a[136:139], v[220:223]\n"
"ds_read_b128 a[136:139], v2 offset:10240\n"
"v_cvt_pk_bf16_f32 v31, v14, v15\n"
"v_mfma_f32_16x16x32_bf16 v[224:227], a[80:83], a[144:147], v[224:227]\n"
"global_store_dwordx4 v0, v[28:31], s[54:55] offset:3072 sc1\n"
"ds_write_b128 v2, v[28:31] offset:15360\n"
"v_mfma_f32_16x16x32_bf16 v[224:227], a[84:87], a[148:151], v[224:227]\n"
"global_load_dwordx4 a[76:79], v0, s[34:35] offset:-1024 sc1\n"
"v_mfma_f32_16x16x32_bf16 v[252:255], a[92:95], a[148:151], v[252:255]\n"
"ds_read_b128 a[148:151], v2 offset:13312\n"
"v_mfma_f32_16x16x32_bf16 v[252:255], a[88:91], a[144:147], v[252:255]\n"
"ds_read_b128 a[144:147], v2 offset:12288\n"
"v_mfma_f32_16x16x32_bf16 a[28:31], a[88:91], a[152:155], a[28:31]\n"
"s_add_u32 s62, s62, 1\n"
"v_mfma_f32_16x16x32_bf16 a[28:31], a[92:95], a[156:159], a[28:31]\n"
"v_mfma_f32_16x16x32_bf16 a[24:27], a[84:87], a[156:159], a[24:27]\n"
"ds_read_b128 a[156:159], v2 offset:15360\n"
"v_mfma_f32_16x16x32_bf16 a[24:27], a[80:83], a[152:155], a[24:27]\n"
"ds_read_b128 a[152:155], v2 offset:14336\n"
"L_layer:\n"
"s_waitcnt vmcnt(11) lgkmcnt(13)\n"
"v_mfma_f32_16x16x32_bf16 v[32:35], a[32:35], a[96:99], v[32:35]\n"
"s_mov_b64 s[96:97], s[52:53]\n"
"v_accvgpr_read_b32 v16, a192\n"
"v_accvgpr_read_b32 v17, a193\n"
"v_mfma_f32_16x16x32_bf16 v[32:35], a[36:39], a[100:103], v[32:35]\n"
"global_load_dwordx4 a[80:83], v0, s[34:35] offset:0 sc1\n"
"v_accvgpr_read_b32 v18, a194\n"
"v_mfma_f32_16x16x32_bf16 v[60:63], a[44:47], a[100:103], v[60:63]\n"
"s_mov_b64 s[98:99], s[54:55]\n"
"v_accvgpr_read_b32 v19, a195\n"
"v_mfma_f32_16x16x32_bf16 v[60:63], a[40:43], a[96:99], v[60:63]\n"
"v_lshlrev_b32 v12, 16, v16\n"
"v_and_b32 v13, s63, v16\n"
"v_mfma_f32_16x16x32_bf16 v[64:67], a[40:43], a[104:107], v[64:67]\n"
"s_add_u32 s52, s52, 131072\n"
"s_addc_u32 s53, s53, 0\n"
"v_lshlrev_b32 v14, 16, v17\n"
"v_mfma_f32_16x16x32_bf16 v[64:67], a[44:47], a[108:111], v[64:67]\n"
"v_and_b32 v15, s63, v17\n"
"v_mul_f32 v12, v12, v144\n"
"v_mfma_f32_16x16x32_bf16 v[36:39], a[36:39], a[108:111], v[36:39]\n"
"s_add_u32 s54, s54, 131072\n"
"s_addc_u32 s55, s55, 0\n"
"v_mul_f32 v13, v13, v145\n"
"v_mfma_f32_16x16x32_bf16 v[36:39], a[32:35], a[104:107], v[36:39]\n"
"s_waitcnt lgkmcnt(9)\n"
"v_mul_f32 v14, v14, v146\n"
"v_mul_f32 v15, v15, v147\n"
"v_mfma_f32_16x16x32_bf16 v[40:43], a[32:35], a[112:115], v[40:43]\n"
"ds_read_b128 v[20:23], v2 offset:0\n"
"v_cvt_pk_bf16_f32 v24, v12, v13\n"
"v_mfma_f32_16x16x32_bf16 v[40:43], a[36:39], a[116:119], v[40:43]\n"
"global_load_dwordx4 a[84:87], v0, s[34:35] offset:1024 sc1\n"
"v_cvt_pk_bf16_f32 v25, v14, v15\n"
"v_mfma_f32_16x16x32_bf16 v[68:71], a[44:47], a[116:119], v[68:71]\n"
"ds_write_b64 v2, v[24:25] offset:16384\n"
"v_mfma_f32_16x16x32_bf16 v[68:71], a[40:43], a[112:115], v[68:71]\n"
"v_lshlrev_b32 v12, 16, v18\n"
"v_and_b32 v13, s63, v18\n"
"v_mfma_f32_16x16x32_bf16 v[72:75], a[40:43], a[120:123], v[72:75]\n"
"s_waitcnt lgkmcnt(6)\n"
"s_barrier\n"
"v_mfma_f32_16x16x32_bf16 v[72:75], a[44:47], a[124:127], v[72:75]\n"
"v_lshlrev_b32 v14, 16, v19\n"
"v_and_b32 v15, s63, v19\n"
"v_mfma_f32_16x16x32_bf16 v[44:47], a[36:39], a[124:127], v[44:47]\n"
"v_mul_f32 v12, v12, v172\n"
"v_mul_f32 v13, v13, v173\n"
"v_mfma_f32_16x16x32_bf16 v[44:47], a[32:35], a[120:123], v[44:47]\n"
"v_mul_f32 v14, v14, v174\n"
"v_mfma_f32_16x16x32_bf16 v[48:51], a[32:35], a[128:131], v[48:51]\n"
"v_mul_f32 v15, v15, v175\n"
"v_mfma_f32_16x16x32_bf16 v[48:51], a[36:39], a[132:135], v[48:51]\n"
"global_load_dwordx4 a[88:91], v0, s[34:35] offset:2048 sc1\n"
"v_mfma_f32_16x16x32_bf16 v[76:79], a[44:47], a[132:135], v[76:79]\n"
"v_cvt_pk_bf16_f32 v26, v12, v13\n"
"v_mfma_f32_16x16x32_bf16 v[76:79], a[40:43], a[128:131], v[76:79]\n"
"v_cvt_pk_bf16_f32 v27, v14, v15\n"
"v_mfma_f32_16x16x32_bf16 v[80:83], a[40:43], a[136:139], v[80:83]\n"
"s_waitcnt lgkmcnt(1)\n"
"global_store_dwordx4 v0, v[20:23], s[96:97] offset:-4096 sc1\n"
"ds_write_b64 v2, v[26:27] offset:16392\n"
"v_accvgpr_read_b32 v16, a196\n"
"v_mfma_f32_16x16x32_bf16 v[80:83], a[44:47], a[140:143], v[80:83]\n"
"v_accvgpr_read_b32 v17, a197\n"
"v_accvgpr_read_b32 v18, a198\n"
"v_mfma_f32_16x16x32_bf16 v[52:55], a[36:39], a[140:143], v[52:55]\n"
"v_accvgpr_read_b32 v19, a199\n"
"v_lshlrev_b32 v12, 16, v16\n"
"v_mfma_f32_16x16x32_bf16 v[52:55], a[32:35], a[136:139], v[52:55]\n"
"v_and_b32 v13, s63, v16\n"
"v_lshlrev_b32 v14, 16, v17\n"
"v_mfma_f32_16x16x32_bf16 v[56:59], a[32:35], a[144:147], v[56:59]\n"
"v_and_b32 v15, s63, v17\n"
"v_mul_f32 v12, v12, v200\n"
"v_mfma_f32_16x16x32_bf16 v[56:59], a[36:39], a[148:151], v[56:59]\n"
"global_load_dwordx4 a[92:95], v0, s[34:35] offset:3072 sc1\n"
"v_mul_f32 v13, v13, v201\n"
"v_mfma_f32_16x16x32_bf16 v[84:87], a[44:47], a[148:151], v[84:87]\n"
"v_mul_f32 v14, v14, v202\n"
"v_mul_f32 v15, v15, v203\n"
"v_mfma_f32_16x16x32_bf16 v[84:87], a[40:43], a[144:147], v[84:87]\n"
"v_cvt_pk_bf16_f32 v28, v12, v13\n"
"v_cvt_pk_bf16_f32 v29, v14, v15\n"
"v_mfma_f32_16x16x32_bf16 a[4:7], a[40:43], a[152:155], a[4:7]\n"
"s_add_u32 s34, s34, 8192\n"
"ds_write_b64 v2, v[28:29] offset:17408\n"
"v_mfma_f32_16x16x32_bf16 a[4:7], a[44:47], a[156:159], a[4:7]\n"
"v_lshlrev_b32 v12, 16, v18\n"
"v_mfma_f32_16x16x32_bf16 a[0:3], a[36:39], a[156:159], a[0:3]\n"
"v_and_b32 v13, s63, v18\n"
"v_lshlrev_b32 v14, 16, v19\n"
"v_mfma_f32_16x16x32_bf16 a[0:3], a[32:35], a[152:155], a[0:3]\n"
"s_waitcnt vmcnt(11)\n"
"v_and_b32 v15, s63, v19\n"
"v_mul_f32 v12, v12, v228\n"
"v_mfma_f32_16x16x32_bf16 v[88:91], a[48:51], a[96:99], v[88:91]\n"
"v_mul_f32 v13, v13, v229\n"
"v_mul_f32 v14, v14, v230\n"
"v_mfma_f32_16x16x32_bf16 v[88:91], a[52:55], a[100:103], v[88:91]\n"
"global_load_dwordx4 a[32:35], v0, s[34:35] offset:-4096 sc1\n"
"v_mul_f32 v15, v15, v231\n"
"v_mfma_f32_16x16x32_bf16 v[116:119], a[60:63], a[100:103], v[116:119]\n"
"ds_read_b128 a[100:103], v3 offset:1024\n"
"v_cvt_pk_bf16_f32 v30, v12, v13\n"
"v_mfma_f32_16x16x32_bf16 v[116:119], a[56:59], a[96:99], v[116:119]\n"
"ds_read_b128 a[96:99], v3 offset:0\n"
"v_cvt_pk_bf16_f32 v31, v14, v15\n"
"v_mfma_f32_16x16x32_bf16 v[120:123], a[56:59], a[104:107], v[120:123]\n"
"ds_write_b64 v2, v[30:31] offset:17416\n"
"v_mfma_f32_16x16x32_bf16 v[120:123], a[60:63], a[108:111], v[120:123]\n"
"ds_read_b128 v[20:23], v2 offset:3072\n"
"v_accvgpr_read_b32 v16, a200\n"
"v_mfma_f32_16x16x32_bf16 v[92:95], a[52:55], a[108:111], v[92:95]\n"
"ds_read_b128 a[108:111], v3 offset:3072\n"
"v_accvgpr_read_b32 v17, a201\n"
"v_mfma_f32_16x16x32_bf16 v[92:95], a[48:51], a[104:107], v[92:95]\n"
"ds_read_b128 a[104:107], v3 offset:2048\n"
"v_accvgpr_read_b32 v18, a202\n"
"v_mfma_f32_16x16x32_bf16 v[96:99], a[48:51], a[112:115], v[96:99]\n"
"v_accvgpr_read_b32 v19, a203\n"
"v_lshlrev_b32 v12, 16, v16\n"
"v_mfma_f32_16x16x32_bf16 v[96:99], a[52:55], a[116:119], v[96:99]\n"
"global_load_dwordx4 a[36:39], v0, s[34:35] offset:-3072 sc1\n"
"v_and_b32 v13, s63, v16\n"
"v_mfma_f32_16x16x32_bf16 v[124:127], a[60:63], a[116:119], v[124:127]\n"
"ds_read_b128 a[116:119], v3 offset:5120\n"
"v_lshlrev_b32 v14, 16, v17\n"
"v_mfma_f32_16x16x32_bf16 v[124:127], a[56:59], a[112:115], v[124:127]\n"
"ds_read_b128 a[112:115], v3 offset:4096\n"
"v_and_b32 v15, s63, v17\n"
"v_mfma_f32_16x16x32_bf16 v[128:131], a[56:59], a[120:123], v[128:131]\n"
"v_mul_f32 v12, v12, v148\n"
"v_mul_f32 v13, v13, v149\n"
"v_mfma_f32_16x16x32_bf16 v[128:131], a[60:63], a[124:127], v[128:131]\n"
"v_mul_f32 v14, v14, v150\n"
"v_mul_f32 v15, v15, v151\n"
"v_mfma_f32_16x16x32_bf16 v[100:103], a[52:55], a[124:127], v[100:103]\n"
"ds_read_b128 a[124:127], v3 offset:7168\n"
"v_cvt_pk_bf16_f32 v24, v12, v13\n"
"v_mfma_f32_16x16x32_bf16 v[100:103], a[48:51], a[120:123], v[100:103]\n"
"ds_read_b128 a[120:123], v3 offset:6144\n"
"v_cvt_pk_bf16_f32 v25, v14, v15\n"
"v_mfma_f32_16x16x32_bf16 v[104:107], a[48:51], a[128:131], v[104:107]\n"
"s_waitcnt lgkmcnt(6)\n"
"global_store_dwordx4 v0, v[20:23], s[96:97] offset:-1024 sc1\n"
"ds_write_b64 v2, v[24:25] offset:18432\n"
"v_lshlrev_b32 v12, 16, v18\n"
"v_mfma_f32_16x16x32_bf16 v[104:107], a[52:55], a[132:135], v[104:107]\n"
"global_load_dwordx4 a[40:43], v0, s[34:35] offset:-2048 sc1\n"
"v_and_b32 v13, s63, v18\n"
"v_mfma_f32_16x16x32_bf16 v[132:135], a[60:63], a[132:135], v[132:135]\n"
"ds_read_b128 a[132:135], v3 offset:9216\n"
"v_lshlrev_b32 v14, 16, v19\n"
"v_mfma_f32_16x16x32_bf16 v[132:135], a[56:59], a[128:131], v[132:135]\n"
"ds_read_b128 a[128:131], v3 offset:8192\n"
"v_and_b32 v15, s63, v19\n"
"v_mfma_f32_16x16x32_bf16 v[136:139], a[56:59], a[136:139], v[136:139]\n"
"v_mul_f32 v12, v12, v176\n"
"v_mul_f32 v13, v13, v177\n"
"v_mfma_f32_16x16x32_bf16 v[136:139], a[60:63], a[140:143], v[136:139]\n"
"v_mul_f32 v14, v14, v178\n"
"v_mul_f32 v15, v15, v179\n"
"v_mfma_f32_16x16x32_bf16 v[108:111], a[52:55], a[140:143], v[108:111]\n"
"ds_read_b128 a[140:143], v3 offset:11264\n"
"v_cvt_pk_bf16_f32 v26, v12, v13\n"
"v_mfma_f32_16x16x32_bf16 v[108:111], a[48:51], a[136:139], v[108:111]\n"
"ds_read_b128 a[136:139], v3 offset:10240\n"
"v_cvt_pk_bf16_f32 v27, v14, v15\n"
"v_mfma_f32_16x16x32_bf16 v[112:115], a[48:51], a[144:147], v[112:115]\n"
"ds_write_b64 v2, v[26:27] offset:18440\n"
"v_mfma_f32_16x16x32_bf16 v[112:115], a[52:55], a[148:151], v[112:115]\n"
"global_load_dwordx4 a[44:47], v0, s[34:35] offset:-1024 sc1\n"
"v_accvgpr_read_b32 v16, a204\n"
"v_mfma_f32_16x16x32_bf16 v[140:143], a[60:63], a[148:151], v[140:143]\n"
"ds_read_b128 a[148:151], v3 offset:13312\n"
"v_accvgpr_read_b32 v17, a205\n"
"v_mfma_f32_16x16x32_bf16 v[140:143], a[56:59], a[144:147], v[140:143]\n"
"ds_read_b128 a[144:147], v3 offset:12288\n"
"v_accvgpr_read_b32 v18, a206\n"
"v_mfma_f32_16x16x32_bf16 a[12:15], a[56:59], a[152:155], a[12:15]\n"
"v_accvgpr_read_b32 v19, a207\n"
"v_lshlrev_b32 v12, 16, v16\n"
"v_mfma_f32_16x16x32_bf16 a[12:15], a[60:63], a[156:159], a[12:15]\n"
"v_and_b32 v13, s63, v16\n"
"v_lshlrev_b32 v14, 16, v17\n"
"v_mfma_f32_16x16x32_bf16 a[8:11], a[52:55], a[156:159], a[8:11]\n"
"ds_read_b128 a[156:159], v3 offset:15360\n"
"v_and_b32 v15, s63, v17\n"
"v_mfma_f32_16x16x32_bf16 a[8:11], a[48:51], a[152:155], a[8:11]\n"
"s_waitcnt vmcnt(10) lgkmcnt(13)\n"
"ds_read_b128 a[152:155], v3 offset:14336\n"
"v_mul_f32 v12, v12, v204\n"
"v_mfma_f32_16x16x32_bf16 v[32:35], a[64:67], a[96:99], v[32:35]\n"
"ds_read_b128 v[20:23], v2 offset:6144\n"
"v_mul_f32 v13, v13, v205\n"
"v_mfma_f32_16x16x32_bf16 v[32:35], a[68:71], a[100:103], v[32:35]\n"
"global_load_dwordx4 a[48:51], v0, s[34:35] offset:0 sc1\n"
"v_mul_f32 v14, v14, v206\n"
"v_mfma_f32_16x16x32_bf16 v[60:63], a[76:79], a[100:103], v[60:63]\n"
"v_mul_f32 v15, v15, v207\n"
"v_cvt_pk_bf16_f32 v28, v12, v13\n"
"v_mfma_f32_16x16x32_bf16 v[60:63], a[72:75], a[96:99], v[60:63]\n"
"v_cvt_pk_bf16_f32 v29, v14, v15\n"
"v_mfma_f32_16x16x32_bf16 v[64:67], a[72:75], a[104:107], v[64:67]\n"
"ds_write_b64 v2, v[28:29] offset:19456\n"
"v_mfma_f32_16x16x32_bf16 v[64:67], a[76:79], a[108:111], v[64:67]\n"
"v_lshlrev_b32 v12, 16, v18\n"
"v_and_b32 v13, s63, v18\n"
"v_mfma_f32_16x16x32_bf16 v[36:39], a[68:71], a[108:111], v[36:39]\n"
"v_lshlrev_b32 v14, 16, v19\n"
"v_and_b32 v15, s63, v19\n"
"v_mfma_f32_16x16x32_bf16 v[36:39], a[64:67], a[104:107], v[36:39]\n"
"s_waitcnt lgkmcnt(12)\n"
"v_mul_f32 v12, v12, v232\n"
"v_mul_f32 v13, v13, v233\n"
"v_mfma_f32_16x16x32_bf16 v[40:43], a[64:67], a[112:115], v[40:43]\n"
"v_mul_f32 v14, v14, v234\n"
"v_mfma_f32_16x16x32_bf16 v[40:43], a[68:71], a[116:119], v[40:43]\n"
"global_load_dwordx4 a[52:55], v0, s[34:35] offset:1024 sc1\n"
"v_mul_f32 v15, v15, v235\n"
"v_mfma_f32_16x16x32_bf16 v[68:71], a[76:79], a[116:119], v[68:71]\n"
"v_cvt_pk_bf16_f32 v30, v12, v13\n"
"v_mfma_f32_16x16x32_bf16 v[68:71], a[72:75], a[112:115], v[68:71]\n"
"v_cvt_pk_bf16_f32 v31, v14, v15\n"
"v_mfma_f32_16x16x32_bf16 v[72:75], a[72:75], a[120:123], v[72:75]\n"
"s_waitcnt lgkmcnt(1)\n"
"global_store_dwordx4 v0, v[20:23], s[96:97] offset:2048 sc1\n"
"ds_write_b64 v2, v[30:31] offset:19464\n"
"v_accvgpr_read_b32 v16, a208\n"
"v_mfma_f32_16x16x32_bf16 v[72:75], a[76:79], a[124:127], v[72:75]\n"
"v_accvgpr_read_b32 v17, a209\n"
"v_accvgpr_read_b32 v18, a210\n"
"v_mfma_f32_16x16x32_bf16 v[44:47], a[68:71], a[124:127], v[44:47]\n"
"v_accvgpr_read_b32 v19, a211\n"
"v_mfma_f32_16x16x32_bf16 v[44:47], a[64:67], a[120:123], v[44:47]\n"
"v_lshlrev_b32 v12, 16, v16\n"
"v_and_b32 v13, s63, v16\n"
"v_mfma_f32_16x16x32_bf16 v[48:51], a[64:67], a[128:131], v[48:51]\n"
"v_lshlrev_b32 v14, 16, v17\n"
"v_mfma_f32_16x16x32_bf16 v[48:51], a[68:71], a[132:135], v[48:51]\n"
"global_load_dwordx4 a[56:59], v0, s[34:35] offset:2048 sc1\n"
"v_and_b32 v15, s63, v17\n"
"v_mfma_f32_16x16x32_bf16 v[76:79], a[76:79], a[132:135], v[76:79]\n"
"v_mul_f32 v12, v12, v152\n"
"v_mul_f32 v13, v13, v153\n"
"v_mfma_f32_16x16x32_bf16 v[76:79], a[72:75], a[128:131], v[76:79]\n"
"v_mul_f32 v14, v14, v154\n"
"v_mul_f32 v15, v15, v155\n"
"v_mfma_f32_16x16x32_bf16 v[80:83], a[72:75], a[136:139], v[80:83]\n"
"v_cvt_pk_bf16_f32 v24, v12, v13\n"
"v_cvt_pk_bf16_f32 v25, v14, v15\n"
"v_mfma_f32_16x16x32_bf16 v[80:83], a[76:79], a[140:143], v[80:83]\n"
"ds_write_b64 v2, v[24:25] offset:20480\n"
"v_mfma_f32_16x16x32_bf16 v[52:55], a[68:71], a[140:143], v[52:55]\n"
"v_lshlrev_b32 v12, 16, v18\n"
"v_mfma_f32_16x16x32_bf16 v[52:55], a[64:67], a[136:139], v[52:55]\n"
"v_and_b32 v13, s63, v18\n"
"v_lshlrev_b32 v14, 16, v19\n"
"v_mfma_f32_16x16x32_bf16 v[56:59], a[64:67], a[144:147], v[56:59]\n"
"v_and_b32 v15, s63, v19\n"
"v_mul_f32 v12, v12, v180\n"
"v_mfma_f32_16x16x32_bf16 v[56:59], a[68:71], a[148:151], v[56:59]\n"
"global_load_dwordx4 a[60:63], v0, s[34:35] offset:3072 sc1\n"
"v_mul_f32 v13, v13, v181\n"
"v_mfma_f32_16x16x32_bf16 v[84:87], a[76:79], a[148:151], v[84:87]\n"
"v_mul_f32 v14, v14, v182\n"
"v_mul_f32 v15, v15, v183\n"
"v_mfma_f32_16x16x32_bf16 v[84:87], a[72:75], a[144:147], v[84:87]\n"
"v_cvt_pk_bf16_f32 v26, v12, v13\n"
"v_cvt_pk_bf16_f32 v27, v14, v15\n"
"v_mfma_f32_16x16x32_bf16 a[4:7], a[72:75], a[152:155], a[4:7]\n"
"L_st5083:\n"
"s_cbranch_execz L_st7359\n"
"s_add_u32 s34, s34, 8192\n"
"ds_write_b64 v2, v[26:27] offset:20488\n"
"v_mfma_f32_16x16x32_bf16 a[4:7], a[76:79], a[156:159], a[4:7]\n"
"ds_read_b128 v[20:23], v2 offset:9216\n"
"v_mfma_f32_16x16x32_bf16 a[0:3], a[68:71], a[156:159], a[0:3]\n"
"v_accvgpr_read_b32 v16, a212\n"
"v_mfma_f32_16x16x32_bf16 a[0:3], a[64:67], a[152:155], a[0:3]\n"
"s_waitcnt vmcnt(10)\n"
"v_accvgpr_read_b32 v17, a213\n"
"v_accvgpr_read_b32 v18, a214\n"
"v_mfma_f32_16x16x32_bf16 v[88:91], a[80:83], a[96:99], v[88:91]\n"
"v_accvgpr_read_b32 v19, a215\n"
"v_lshlrev_b32 v12, 16, v16\n"
"v_mfma_f32_16x16x32_bf16 v[88:91], a[84:87], a[100:103], v[88:91]\n"
"global_load_dwordx4 a[64:67], v0, s[34:35] offset:-4096 sc1\n"
"v_and_b32 v13, s63, v16\n"
"v_mfma_f32_16x16x32_bf16 v[116:119], a[92:95], a[100:103], v[116:119]\n"
"ds_read_b128 a[100:103], v4 offset:1024\n"
"v_lshlrev_b32 v14, 16, v17\n"
"v_mfma_f32_16x16x32_bf16 v[116:119], a[88:91], a[96:99], v[116:119]\n"
"ds_read_b128 a[96:99], v4 offset:0\n"
"v_and_b32 v15, s63, v17\n"
"v_mfma_f32_16x16x32_bf16 v[120:123], a[88:91], a[104:107], v[120:123]\n"
"v_mul_f32 v12, v12, v208\n"
"v_mul_f32 v13, v13, v209\n"
"v_mfma_f32_16x16x32_bf16 v[120:123], a[92:95], a[108:111], v[120:123]\n"
"v_mul_f32 v14, v14, v210\n"
"v_mul_f32 v15, v15, v211\n"
"v_mfma_f32_16x16x32_bf16 v[92:95], a[84:87], a[108:111], v[92:95]\n"
"ds_read_b128 a[108:111], v4 offset:3072\n"
"v_cvt_pk_bf16_f32 v28, v12, v13\n"
"v_mfma_f32_16x16x32_bf16 v[92:95], a[80:83], a[104:107], v[92:95]\n"
"ds_read_b128 a[104:107], v4 offset:2048\n"
"v_cvt_pk_bf16_f32 v29, v14, v15\n"
"v_mfma_f32_16x16x32_bf16 v[96:99], a[80:83], a[112:115], v[96:99]\n"
"s_waitcnt lgkmcnt(4)\n"
"global_store_dwordx4 v0, v[20:23], s[98:99] offset:-3072 sc1\n"
"ds_write_b64 v2, v[28:29] offset:21504\n"
"v_lshlrev_b32 v12, 16, v18\n"
"v_mfma_f32_16x16x32_bf16 v[96:99], a[84:87], a[116:119], v[96:99]\n"
"global_load_dwordx4 a[68:71], v0, s[34:35] offset:-3072 sc1\n"
"v_and_b32 v13, s63, v18\n"
"v_mfma_f32_16x16x32_bf16 v[124:127], a[92:95], a[116:119], v[124:127]\n"
"ds_read_b128 a[116:119], v4 offset:5120\n"
"v_lshlrev_b32 v14, 16, v19\n"
"v_mfma_f32_16x16x32_bf16 v[124:127], a[88:91], a[112:115], v[124:127]\n"
"ds_read_b128 a[112:115], v4 offset:4096\n"
"v_and_b32 v15, s63, v19\n"
"v_mfma_f32_16x16x32_bf16 v[128:131], a[88:91], a[120:123], v[128:131]\n"
"v_mul_f32 v12, v12, v236\n"
"v_mul_f32 v13, v13, v237\n"
"v_mfma_f32_16x16x32_bf16 v[128:131], a[92:95], a[124:127], v[128:131]\n"
"v_mul_f32 v14, v14, v238\n"
"v_mul_f32 v15, v15, v239\n"
"v_mfma_f32_16x16x32_bf16 v[100:103], a[84:87], a[124:127], v[100:103]\n"
"ds_read_b128 a[124:127], v4 offset:7168\n"
"v_cvt_pk_bf16_f32 v30, v12, v13\n"
"v_mfma_f32_16x16x32_bf16 v[100:103], a[80:83], a[120:123], v[100:103]\n"
"ds_read_b128 a[120:123], v4 offset:6144\n"
"v_cvt_pk_bf16_f32 v31, v14, v15\n"
"v_mfma_f32_16x16x32_bf16 v[104:107], a[80:83], a[128:131], v[104:107]\n"
"ds_write_b64 v2, v[30:31] offset:21512\n"
"v_mfma_f32_16x16x32_bf16 v[104:107], a[84:87], a[132:135], v[104:107]\n"
"global_load_dwordx4 a[72:75], v0, s[34:35] offset:-2048 sc1\n"
"v_accvgpr_read_b32 v16, a216\n"
"v_mfma_f32_16x16x32_bf16 v[132:135], a[92:95], a[132:135], v[132:135]\n"
"ds_read_b128 a[132:135], v4 offset:9216\n"
"v_accvgpr_read_b32 v17, a217\n"
"v_mfma_f32_16x16x32_bf16 v[132:135], a[88:91], a[128:131], v[132:135]\n"
"ds_read_b128 a[128:131], v4 offset:8192\n"
"v_accvgpr_read_b32 v18, a218\n"
"v_mfma_f32_16x16x32_bf16 v[136:139], a[88:91], a[136:139], v[136:139]\n"
"v_accvgpr_read_b32 v19, a219\n"
"v_mfma_f32_16x16x32_bf16 v[136:139], a[92:95], a[140:143], v[136:139]\n"
"v_lshlrev_b32 v12, 16, v16\n"
"v_and_b32 v13, s63, v16\n"
"v_mfma_f32_16x16x32_bf16 v[108:111], a[84:87], a[140:143], v[108:111]\n"
"ds_read_b128 a[140:143], v4 offset:11264\n"
"v_lshlrev_b32 v14, 16, v17\n"
"v_mfma_f32_16x16x32_bf16 v[108:111], a[80:83], a[136:139], v[108:111]\n"
"ds_read_b128 a[136:139], v4 offset:10240\n"
"v_and_b32 v15, s63, v17\n"
"v_mfma_f32_16x16x32_bf16 v[112:115], a[80:83], a[144:147], v[112:115]\n"
"ds_read_b128 v[20:23], v2 offset:12288\n"
"v_mul_f32 v12, v12, v156\n"
"v_mfma_f32_16x16x32_bf16 v[112:115], a[84:87], a[148:151], v[112:115]\n"
"global_load_dwordx4 a[76:79], v0, s[34:35] offset:-1024 sc1\n"
"v_mfma_f32_16x16x32_bf16 v[140:143], a[92:95], a[148:151], v[140:143]\n"
"ds_read_b128 a[148:151], v4 offset:13312\n"
"v_mfma_f32_16x16x32_bf16 v[140:143], a[88:91], a[144:147], v[140:143]\n"
"ds_read_b128 a[144:147], v4 offset:12288\n"
"v_mfma_f32_16x16x32_bf16 a[12:15], a[88:91], a[152:155], a[12:15]\n"
"v_mul_f32 v13, v13, v157\n"
"v_mfma_f32_16x16x32_bf16 a[12:15], a[92:95], a[156:159], a[12:15]\n"
"v_mul_f32 v14, v14, v158\n"
"v_mfma_f32_16x16x32_bf16 a[8:11], a[84:87], a[156:159], a[8:11]\n"
"ds_read_b128 a[156:159], v4 offset:15360\n"
"v_mfma_f32_16x16x32_bf16 a[8:11], a[80:83], a[152:155], a[8:11]\n"
"s_waitcnt vmcnt(10) lgkmcnt(14)\n"
"ds_read_b128 a[152:155], v4 offset:14336\n"
"v_mfma_f32_16x16x32_bf16 v[32:35], a[32:35], a[96:99], v[32:35]\n"
"v_mul_f32 v15, v15, v159\n"
"v_mfma_f32_16x16x32_bf16 v[32:35], a[36:39], a[100:103], v[32:35]\n"
"global_load_dwordx4 a[80:83], v0, s[34:35] offset:0 sc1\n"
"v_mfma_f32_16x16x32_bf16 v[60:63], a[44:47], a[100:103], v[60:63]\n"
"s_add_u32 s65, s65, 1\n"
"v_cvt_pk_bf16_f32 v24, v12, v13\n"
"v_mfma_f32_16x16x32_bf16 v[60:63], a[40:43], a[96:99], v[60:63]\n"
"v_cvt_pk_bf16_f32 v25, v14, v15\n"
"v_mfma_f32_16x16x32_bf16 v[64:67], a[40:43], a[104:107], v[64:67]\n"
"s_waitcnt lgkmcnt(4)\n"
"global_store_dwordx4 v0, v[20:23], s[98:99] offset:0 sc1\n"
"ds_write_b64 v2, v[24:25] offset:22528\n"
"v_lshlrev_b32 v12, 16, v18\n"
"v_mfma_f32_16x16x32_bf16 v[64:67], a[44:47], a[108:111], v[64:67]\n"
"v_and_b32 v13, s63, v18\n"
"v_lshlrev_b32 v14, 16, v19\n"
"v_mfma_f32_16x16x32_bf16 v[36:39], a[36:39], a[108:111], v[36:39]\n"
"v_and_b32 v15, s63, v19\n"
"v_mul_f32 v12, v12, v184\n"
"v_mfma_f32_16x16x32_bf16 v[36:39], a[32:35], a[104:107], v[36:39]\n"
"v_mul_f32 v13, v13, v185\n"
"v_mul_f32 v14, v14, v186\n"
"v_mfma_f32_16x16x32_bf16 v[40:43], a[32:35], a[112:115], v[40:43]\n"
"v_mul_f32 v15, v15, v187\n"
"v_cvt_pk_bf16_f32 v26, v12, v13\n"
"v_mfma_f32_16x16x32_bf16 v[40:43], a[36:39], a[116:119], v[40:43]\n"
"global_load_dwordx4 a[84:87], v0, s[34:35] offset:1024 sc1\n"
"v_cvt_pk_bf16_f32 v27, v14, v15\n"
"v_mfma_f32_16x16x32_bf16 v[68:71], a[44:47], a[116:119], v[68:71]\n"
"ds_write_b64 v2, v[26:27] offset:22536\n"
"v_mfma_f32_16x16x32_bf16 v[68:71], a[40:43], a[112:115], v[68:71]\n"
"v_accvgpr_read_b32 v16, a220\n"
"v_accvgpr_read_b32 v17, a221\n"
"v_mfma_f32_16x16x32_bf16 v[72:75], a[40:43], a[120:123], v[72:75]\n"
"v_accvgpr_read_b32 v18, a222\n"
"v_accvgpr_read_b32 v19, a223\n"
"v_mfma_f32_16x16x32_bf16 v[72:75], a[44:47], a[124:127], v[72:75]\n"
"v_lshlrev_b32 v12, 16, v16\n"
"v_and_b32 v13, s63, v16\n"
"v_mfma_f32_16x16x32_bf16 v[44:47], a[36:39], a[124:127], v[44:47]\n"
"v_lshlrev_b32 v14, 16, v17\n"
"v_and_b32 v15, s63, v17\n"
"v_mfma_f32_16x16x32_bf16 v[44:47], a[32:35], a[120:123], v[44:47]\n"
"v_mul_f32 v12, v12, v212\n"
"v_mul_f32 v13, v13, v213\n"
"v_mfma_f32_16x16x32_bf16 v[48:51], a[32:35], a[128:131], v[48:51]\n"
"v_mul_f32 v14, v14, v214\n"
"v_mul_f32 v15, v15, v215\n"
"v_mfma_f32_16x16x32_bf16 v[48:51], a[36:39], a[132:135], v[48:51]\n"
"global_load_dwordx4 a[88:91], v0, s[34:35] offset:2048 sc1\n"
"v_cvt_pk_bf16_f32 v28, v12, v13\n"
"v_mfma_f32_16x16x32_bf16 v[76:79], a[44:47], a[132:135], v[76:79]\n"
"v_cvt_pk_bf16_f32 v29, v14, v15\n"
"v_mfma_f32_16x16x32_bf16 v[76:79], a[40:43], a[128:131], v[76:79]\n"
"ds_write_b64 v2, v[28:29] offset:23552\n"
"v_mfma_f32_16x16x32_bf16 v[80:83], a[40:43], a[136:139], v[80:83]\n"
"v_lshlrev_b32 v12, 16, v18\n"
"v_and_b32 v13, s63, v18\n"
"v_mfma_f32_16x16x32_bf16 v[80:83], a[44:47], a[140:143], v[80:83]\n"
"v_lshlrev_b32 v14, 16, v19\n"
"v_and_b32 v15, s63, v19\n"
"v_mfma_f32_16x16x32_bf16 v[52:55], a[36:39], a[140:143], v[52:55]\n"
"v_mul_f32 v12, v12, v240\n"
"v_mul_f32 v13, v13, v241\n"
"v_mfma_f32_16x16x32_bf16 v[52:55], a[32:35], a[136:139], v[52:55]\n"
"s_waitcnt lgkmcnt(3)\n"
"v_mul_f32 v14, v14, v242\n"
"v_mul_f32 v15, v15, v243\n"
"v_mfma_f32_16x16x32_bf16 v[56:59], a[32:35], a[144:147], v[56:59]\n"
"v_cvt_pk_bf16_f32 v30, v12, v13\n"
"v_cvt_pk_bf16_f32 v31, v14, v15\n"
"v_mfma_f32_16x16x32_bf16 v[56:59], a[36:39], a[148:151], v[56:59]\n"
"global_load_dwordx4 a[92:95], v0, s[34:35] offset:3072 sc1\n"
"v_mfma_f32_16x16x32_bf16 v[84:87], a[44:47], a[148:151], v[84:87]\n"
"ds_write_b64 v2, v[30:31] offset:23560\n"
"v_mfma_f32_16x16x32_bf16 v[84:87], a[40:43], a[144:147], v[84:87]\n"
"v_accvgpr_read_b32 v16, a224\n"
"v_accvgpr_read_b32 v17, a225\n"
"v_mfma_f32_16x16x32_bf16 a[4:7], a[40:43], a[152:155], a[4:7]\n"
"s_add_u32 s34, s34, 8192\n"
"v_accvgpr_read_b32 v18, a226\n"
"v_accvgpr_read_b32 v19, a227\n"
"v_mfma_f32_16x16x32_bf16 a[4:7], a[44:47], a[156:159], a[4:7]\n"
"v_lshlrev_b32 v12, 16, v16\n"
"v_and_b32 v13, s63, v16\n"
"v_mfma_f32_16x16x32_bf16 a[0:3], a[36:39], a[156:159], a[0:3]\n"
"v_lshlrev_b32 v14, 16, v17\n"
"v_and_b32 v15, s63, v17\n"
"v_mfma_f32_16x16x32_bf16 a[0:3], a[32:35], a[152:155], a[0:3]\n"
"s_waitcnt vmcnt(10)\n"
"v_mul_f32 v12, v12, v160\n"
"v_mul_f32 v13, v13, v161\n"
"v_mfma_f32_16x16x32_bf16 v[88:91], a[48:51], a[96:99], v[88:91]\n"
"v_mul_f32 v14, v14, v162\n"
"v_mul_f32 v15, v15, v163\n"
"v_mfma_f32_16x16x32_bf16 v[88:91], a[52:55], a[100:103], v[88:91]\n"
"global_load_dwordx4 a[32:35], v0, s[34:35] offset:-4096 sc1\n"
"v_cvt_pk_bf16_f32 v24, v12, v13\n"
"v_mfma_f32_16x16x32_bf16 v[116:119], a[60:63], a[100:103], v[116:119]\n"
"ds_read_b128 a[100:103], v5 offset:1024\n"
"v_cvt_pk_bf16_f32 v25, v14, v15\n"
"v_mfma_f32_16x16x32_bf16 v[116:119], a[56:59], a[96:99], v[116:119]\n"
"ds_read_b128 a[96:99], v5 offset:0\n"
"v_mfma_f32_16x16x32_bf16 v[120:123], a[56:59], a[104:107], v[120:123]\n"
"ds_write_b64 v2, v[24:25] offset:24576\n"
"v_mfma_f32_16x16x32_bf16 v[120:123], a[60:63], a[108:111], v[120:123]\n"
"v_lshlrev_b32 v12, 16, v18\n"
"v_and_b32 v13, s63, v18\n"
"v_mfma_f32_16x16x32_bf16 v[92:95], a[52:55], a[108:111], v[92:95]\n"
"ds_read_b128 a[108:111], v5 offset:3072\n"
"v_lshlrev_b32 v14, 16, v19\n"
"v_mfma_f32_16x16x32_bf16 v[92:95], a[48:51], a[104:107], v[92:95]\n"
"ds_read_b128 a[104:107], v5 offset:2048\n"
"v_and_b32 v15, s63, v19\n"
"v_mfma_f32_16x16x32_bf16 v[96:99], a[48:51], a[112:115], v[96:99]\n"
"v_mul_f32 v12, v12, v188\n"
"v_mul_f32 v13, v13, v189\n"
"v_mfma_f32_16x16x32_bf16 v[96:99], a[52:55], a[116:119], v[96:99]\n"
"global_load_dwordx4 a[36:39], v0, s[34:35] offset:-3072 sc1\n"
"v_mul_f32 v14, v14, v190\n"
"v_mfma_f32_16x16x32_bf16 v[124:127], a[60:63], a[116:119], v[124:127]\n"
"ds_read_b128 a[116:119], v5 offset:5120\n"
"v_mul_f32 v15, v15, v191\n"
"v_mfma_f32_16x16x32_bf16 v[124:127], a[56:59], a[112:115], v[124:127]\n"
"ds_read_b128 a[112:115], v5 offset:4096\n"
"v_cvt_pk_bf16_f32 v26, v12, v13\n"
"v_mfma_f32_16x16x32_bf16 v[128:131], a[56:59], a[120:123], v[128:131]\n"
"v_cvt_pk_bf16_f32 v27, v14, v15\n"
"v_mfma_f32_16x16x32_bf16 v[128:131], a[60:63], a[124:127], v[128:131]\n"
"ds_write_b64 v2, v[26:27] offset:24584\n"
"v_mfma_f32_16x16x32_bf16 v[100:103], a[52:55], a[124:127], v[100:103]\n"
"ds_read_b128 a[124:127], v5 offset:7168\n"
"v_accvgpr_read_b32 v16, a228\n"
"v_mfma_f32_16x16x32_bf16 v[100:103], a[48:51], a[120:123], v[100:103]\n"
"ds_read_b128 a[120:123], v5 offset:6144\n"
"v_accvgpr_read_b32 v17, a229\n"
"v_mfma_f32_16x16x32_bf16 v[104:107], a[48:51], a[128:131], v[104:107]\n"
"v_accvgpr_read_b32 v18, a230\n"
"v_accvgpr_read_b32 v19, a231\n"
"v_mfma_f32_16x16x32_bf16 v[104:107], a[52:55], a[132:135], v[104:107]\n"
"global_load_dwordx4 a[40:43], v0, s[34:35] offset:-2048 sc1\n"
"v_lshlrev_b32 v12, 16, v16\n"
"v_mfma_f32_16x16x32_bf16 v[132:135], a[60:63], a[132:135], v[132:135]\n"
"ds_read_b128 a[132:135], v5 offset:9216\n"
"v_and_b32 v13, s63, v16\n"
"v_mfma_f32_16x16x32_bf16 v[132:135], a[56:59], a[128:131], v[132:135]\n"
"ds_read_b128 a[128:131], v5 offset:8192\n"
"v_lshlrev_b32 v14, 16, v17\n"
"v_mfma_f32_16x16x32_bf16 v[136:139], a[56:59], a[136:139], v[136:139]\n"
"v_and_b32 v15, s63, v17\n"
"v_mul_f32 v12, v12, v216\n"
"v_mfma_f32_16x16x32_bf16 v[136:139], a[60:63], a[140:143], v[136:139]\n"
"v_mul_f32 v13, v13, v217\n"
"v_mul_f32 v14, v14, v218\n"
"v_mfma_f32_16x16x32_bf16 v[108:111], a[52:55], a[140:143], v[108:111]\n"
"ds_read_b128 a[140:143], v5 offset:11264\n"
"v_mul_f32 v15, v15, v219\n"
"v_mfma_f32_16x16x32_bf16 v[108:111], a[48:51], a[136:139], v[108:111]\n"
"ds_read_b128 a[136:139], v5 offset:10240\n"
"v_cvt_pk_bf16_f32 v28, v12, v13\n"
"v_mfma_f32_16x16x32_bf16 v[112:115], a[48:51], a[144:147], v[112:115]\n"
"v_cvt_pk_bf16_f32 v29, v14, v15\n"
"v_lshlrev_b32 v12, 16, v18\n"
"v_mfma_f32_16x16x32_bf16 v[112:115], a[52:55], a[148:151], v[112:115]\n"
"global_load_dwordx4 a[44:47], v0, s[34:35] offset:-1024 sc1\n"
"v_and_b32 v13, s63, v18\n"
"v_mfma_f32_16x16x32_bf16 v[140:143], a[60:63], a[148:151], v[140:143]\n"
"ds_read_b128 a[148:151], v5 offset:13312\n"
"v_lshlrev_b32 v14, 16, v19\n"
"v_mfma_f32_16x16x32_bf16 v[140:143], a[56:59], a[144:147], v[140:143]\n"
"ds_read_b128 a[144:147], v5 offset:12288\n"
"v_and_b32 v15, s63, v19\n"
"v_mfma_f32_16x16x32_bf16 a[12:15], a[56:59], a[152:155], a[12:15]\n"
"v_mul_f32 v12, v12, v244\n"
"v_mul_f32 v13, v13, v245\n"
"v_mfma_f32_16x16x32_bf16 a[12:15], a[60:63], a[156:159], a[12:15]\n"
"v_mul_f32 v14, v14, v246\n"
"v_mul_f32 v15, v15, v247\n"
"v_mfma_f32_16x16x32_bf16 a[8:11], a[52:55], a[156:159], a[8:11]\n"
"ds_read_b128 a[156:159], v5 offset:15360\n"
"v_cvt_pk_bf16_f32 v30, v12, v13\n"
"v_mfma_f32_16x16x32_bf16 a[8:11], a[48:51], a[152:155], a[8:11]\n"
"s_waitcnt vmcnt(9) lgkmcnt(12)\n"
"ds_read_b128 a[152:155], v5 offset:14336\n"
"v_cvt_pk_bf16_f32 v31, v14, v15\n"
"v_mfma_f32_16x16x32_bf16 v[32:35], a[64:67], a[96:99], v[32:35]\n"
"global_store_dwordx4 v0, v[28:31], s[54:55] offset:-3072 sc1\n"
"ds_write_b128 v2, v[28:31] offset:25600\n"
"v_mfma_f32_16x16x32_bf16 v[32:35], a[68:71], a[100:103], v[32:35]\n"
"global_load_dwordx4 a[48:51], v0, s[34:35] offset:0 sc1\n"
"v_accvgpr_read_b32 v16, a232\n"
"v_mfma_f32_16x16x32_bf16 v[60:63], a[76:79], a[100:103], v[60:63]\n"
"v_accvgpr_read_b32 v17, a233\n"
"v_accvgpr_read_b32 v18, a234\n"
"v_mfma_f32_16x16x32_bf16 v[60:63], a[72:75], a[96:99], v[60:63]\n"
"v_accvgpr_read_b32 v19, a235\n"
"v_lshlrev_b32 v12, 16, v16\n"
"v_mfma_f32_16x16x32_bf16 v[64:67], a[72:75], a[104:107], v[64:67]\n"
"v_and_b32 v13, s63, v16\n"
"v_lshlrev_b32 v14, 16, v17\n"
"v_mfma_f32_16x16x32_bf16 v[64:67], a[76:79], a[108:111], v[64:67]\n"
"v_and_b32 v15, s63, v17\n"
"v_mul_f32 v12, v12, v164\n"
"v_mfma_f32_16x16x32_bf16 v[36:39], a[68:71], a[108:111], v[36:39]\n"
"v_mul_f32 v13, v13, v165\n"
"v_mul_f32 v14, v14, v166\n"
"v_mfma_f32_16x16x32_bf16 v[36:39], a[64:67], a[104:107], v[36:39]\n"
"s_waitcnt lgkmcnt(9)\n"
"v_mul_f32 v15, v15, v167\n"
"v_cvt_pk_bf16_f32 v24, v12, v13\n"
"v_mfma_f32_16x16x32_bf16 v[40:43], a[64:67], a[112:115], v[40:43]\n"
"v_cvt_pk_bf16_f32 v25, v14, v15\n"
"v_mfma_f32_16x16x32_bf16 v[40:43], a[68:71], a[116:119], v[40:43]\n"
"global_load_dwordx4 a[52:55], v0, s[34:35] offset:1024 sc1\n"
"v_mfma_f32_16x16x32_bf16 v[68:71], a[76:79], a[116:119], v[68:71]\n"
"ds_write_b64 v2, v[24:25] offset:26624\n"
"v_mfma_f32_16x16x32_bf16 v[68:71], a[72:75], a[112:115], v[68:71]\n"
"v_lshlrev_b32 v12, 16, v18\n"
"v_and_b32 v13, s63, v18\n"
"v_mfma_f32_16x16x32_bf16 v[72:75], a[72:75], a[120:123], v[72:75]\n"
"v_lshlrev_b32 v14, 16, v19\n"
"v_and_b32 v15, s63, v19\n"
"v_mfma_f32_16x16x32_bf16 v[72:75], a[76:79], a[124:127], v[72:75]\n"
"v_mul_f32 v12, v12, v192\n"
"v_mul_f32 v13, v13, v193\n"
"v_mfma_f32_16x16x32_bf16 v[44:47], a[68:71], a[124:127], v[44:47]\n"
"v_mul_f32 v14, v14, v194\n"
"v_mul_f32 v15, v15, v195\n"
"v_mfma_f32_16x16x32_bf16 v[44:47], a[64:67], a[120:123], v[44:47]\n"
"s_waitcnt lgkmcnt(6)\n"
"v_cvt_pk_bf16_f32 v26, v12, v13\n"
"v_cvt_pk_bf16_f32 v27, v14, v15\n"
"v_mfma_f32_16x16x32_bf16 v[48:51], a[64:67], a[128:131], v[48:51]\n"
"ds_write_b64 v2, v[26:27] offset:26632\n"
"v_mfma_f32_16x16x32_bf16 v[48:51], a[68:71], a[132:135], v[48:51]\n"
"global_load_dwordx4 a[56:59], v0, s[34:35] offset:2048 sc1\n"
"v_accvgpr_read_b32 v16, a236\n"
"v_mfma_f32_16x16x32_bf16 v[76:79], a[76:79], a[132:135], v[76:79]\n"
"v_accvgpr_read_b32 v17, a237\n"
"v_accvgpr_read_b32 v18, a238\n"
"v_mfma_f32_16x16x32_bf16 v[76:79], a[72:75], a[128:131], v[76:79]\n"
"v_accvgpr_read_b32 v19, a239\n"
"v_lshlrev_b32 v12, 16, v16\n"
"v_mfma_f32_16x16x32_bf16 v[80:83], a[72:75], a[136:139], v[80:83]\n"
"v_and_b32 v13, s63, v16\n"
"v_mfma_f32_16x16x32_bf16 v[80:83], a[76:79], a[140:143], v[80:83]\n"
"v_lshlrev_b32 v14, 16, v17\n"
"v_mfma_f32_16x16x32_bf16 v[52:55], a[68:71], a[140:143], v[52:55]\n"
"v_and_b32 v15, s63, v17\n"
"v_mul_f32 v12, v12, v220\n"
"v_mfma_f32_16x16x32_bf16 v[52:55], a[64:67], a[136:139], v[52:55]\n"
"s_waitcnt lgkmcnt(3)\n"
"v_mul_f32 v13, v13, v221\n"
"v_mfma_f32_16x16x32_bf16 v[56:59], a[64:67], a[144:147], v[56:59]\n"
"v_mul_f32 v14, v14, v222\n"
"v_mul_f32 v15, v15, v223\n"
"v_mfma_f32_16x16x32_bf16 v[56:59], a[68:71], a[148:151], v[56:59]\n"
"global_load_dwordx4 a[60:63], v0, s[34:35] offset:3072 sc1\n"
"v_cvt_pk_bf16_f32 v28, v12, v13\n"
"v_mfma_f32_16x16x32_bf16 v[84:87], a[76:79], a[148:151], v[84:87]\n"
"v_cvt_pk_bf16_f32 v29, v14, v15\n"
"v_mfma_f32_16x16x32_bf16 v[84:87], a[72:75], a[144:147], v[84:87]\n"
"v_lshlrev_b32 v12, 16, v18\n"
"v_and_b32 v13, s63, v18\n"
"v_mfma_f32_16x16x32_bf16 a[4:7], a[72:75], a[152:155], a[4:7]\n"
"s_add_u32 s34, s34, 8192\n"
"v_lshlrev_b32 v14, 16, v19\n"
"v_mfma_f32_16x16x32_bf16 a[4:7], a[76:79], a[156:159], a[4:7]\n"
"v_and_b32 v15, s63, v19\n"
"v_mfma_f32_16x16x32_bf16 a[0:3], a[68:71], a[156:159], a[0:3]\n"
"v_mul_f32 v12, v12, v248\n"
"v_mul_f32 v13, v13, v249\n"
"v_mfma_f32_16x16x32_bf16 a[0:3], a[64:67], a[152:155], a[0:3]\n"
"s_waitcnt vmcnt(9)\n"
"v_mul_f32 v14, v14, v250\n"
"v_mfma_f32_16x16x32_bf16 v[88:91], a[80:83], a[96:99], v[88:91]\n"
"v_mul_f32 v15, v15, v251\n"
"v_cvt_pk_bf16_f32 v30, v12, v13\n"
"v_mfma_f32_16x16x32_bf16 v[88:91], a[84:87], a[100:103], v[88:91]\n"
"global_load_dwordx4 a[64:67], v0, s[34:35] offset:-4096 sc1\n"
"v_cvt_pk_bf16_f32 v31, v14, v15\n"
"v_mfma_f32_16x16x32_bf16 v[116:119], a[92:95], a[100:103], v[116:119]\n"
"global_store_dwordx4 v0, v[28:31], s[54:55] offset:-1024 sc1\n"
"ds_write_b128 v2, v[28:31] offset:27648\n"
"v_mfma_f32_16x16x32_bf16 v[116:119], a[88:91], a[96:99], v[116:119]\n"
"v_accvgpr_read_b32 v16, a240\n"
"v_mfma_f32_16x16x32_bf16 v[120:123], a[88:91], a[104:107], v[120:123]\n"
"v_accvgpr_read_b32 v17, a241\n"
"v_mfma_f32_16x16x32_bf16 v[120:123], a[92:95], a[108:111], v[120:123]\n"
"v_accvgpr_read_b32 v18, a242\n"
"v_mfma_f32_16x16x32_bf16 v[92:95], a[84:87], a[108:111], v[92:95]\n"
"v_accvgpr_read_b32 v19, a243\n"
"v_lshlrev_b32 v12, 16, v16\n"
"v_mfma_f32_16x16x32_bf16 v[92:95], a[80:83], a[104:107], v[92:95]\n"
"v_and_b32 v13, s63, v16\n"
"v_mfma_f32_16x16x32_bf16 v[96:99], a[80:83], a[112:115], v[96:99]\n"
"v_lshlrev_b32 v14, 16, v17\n"
"v_and_b32 v15, s63, v17\n"
"v_mfma_f32_16x16x32_bf16 v[96:99], a[84:87], a[116:119], v[96:99]\n"
"global_load_dwordx4 a[68:71], v0, s[34:35] offset:-3072 sc1\n"
"v_mul_f32 v12, v12, v168\n"
"v_mfma_f32_16x16x32_bf16 v[124:127], a[92:95], a[116:119], v[124:127]\n"
"v_mul_f32 v13, v13, v169\n"
"v_mfma_f32_16x16x32_bf16 v[124:127], a[88:91], a[112:115], v[124:127]\n"
"v_mul_f32 v14, v14, v170\n"
"v_mul_f32 v15, v15, v171\n"
"v_mfma_f32_16x16x32_bf16 v[128:131], a[88:91], a[120:123], v[128:131]\n"
"v_cvt_pk_bf16_f32 v24, v12, v13\n"
"v_mfma_f32_16x16x32_bf16 v[128:131], a[92:95], a[124:127], v[128:131]\n"
"v_cvt_pk_bf16_f32 v25, v14, v15\n"
"v_lshlrev_b32 v12, 16, v18\n"
"v_mfma_f32_16x16x32_bf16 v[100:103], a[84:87], a[124:127], v[100:103]\n"
"v_and_b32 v13, s63, v18\n"
"v_lshlrev_b32 v14, 16, v19\n"
"v_mfma_f32_16x16x32_bf16 v[100:103], a[80:83], a[120:123], v[100:103]\n"
"v_mov_b32 v8, s65\n"
"v_and_b32 v15, s63, v19\n"
"v_mfma_f32_16x16x32_bf16 v[104:107], a[80:83], a[128:131], v[104:107]\n"
"v_mov_b32 v9, s60\n"
"v_mul_f32 v12, v12, v196\n"
"v_mfma_f32_16x16x32_bf16 v[104:107], a[84:87], a[132:135], v[104:107]\n"
"global_load_dwordx4 a[72:75], v0, s[34:35] offset:-2048 sc1\n"
"v_mul_f32 v13, v13, v197\n"
"v_mfma_f32_16x16x32_bf16 v[132:135], a[92:95], a[132:135], v[132:135]\n"
"buffer_store_dwordx2 v[8:9], off, s[56:59], %13 sc1\n"
"v_mul_f32 v14, v14, v198\n"
"v_mfma_f32_16x16x32_bf16 v[132:135], a[88:91], a[128:131], v[132:135]\n"
"v_mul_f32 v15, v15, v199\n"
"v_cvt_pk_bf16_f32 v26, v12, v13\n"
"v_mfma_f32_16x16x32_bf16 v[136:139], a[88:91], a[136:139], v[136:139]\n"
"v_cvt_pk_bf16_f32 v27, v14, v15\n"
"v_mfma_f32_16x16x32_bf16 v[136:139], a[92:95], a[140:143], v[136:139]\n"
"global_store_dwordx4 v0, v[24:27], s[54:55] offset:0 sc1\n"
"ds_write_b128 v2, v[24:27] offset:28672\n"
"v_mfma_f32_16x16x32_bf16 v[108:111], a[84:87], a[140:143], v[108:111]\n"
"v_accvgpr_read_b32 v16, a248\n"
"v_mfma_f32_16x16x32_bf16 v[108:111], a[80:83], a[136:139], v[108:111]\n"
"v_accvgpr_read_b32 v17, a249\n"
"v_mfma_f32_16x16x32_bf16 v[112:115], a[80:83], a[144:147], v[112:115]\n"
"v_accvgpr_read_b32 v18, a250\n"
"v_accvgpr_read_b32 v19, a251\n"
"v_mfma_f32_16x16x32_bf16 v[112:115], a[84:87], a[148:151], v[112:115]\n"
"global_load_dwordx4 a[76:79], v0, s[34:35] offset:-1024 sc1\n"
"v_lshlrev_b32 v12, 16, v16\n"
"v_mfma_f32_16x16x32_bf16 v[140:143], a[92:95], a[148:151], v[140:143]\n"
"v_and_b32 v13, s63, v16\n"
"v_mfma_f32_16x16x32_bf16 v[140:143], a[88:91], a[144:147], v[140:143]\n"
"v_lshlrev_b32 v14, 16, v17\n"
"v_and_b32 v15, s63, v17\n"
"v_mfma_f32_16x16x32_bf16 a[12:15], a[88:91], a[152:155], a[12:15]\n"
"v_accvgpr_read_b32 v10, a16\n"
"v_mfma_f32_16x16x32_bf16 a[12:15], a[92:95], a[156:159], a[12:15]\n"
"v_accvgpr_read_b32 v11, a17\n"
"v_mfma_f32_16x16x32_bf16 a[8:11], a[84:87], a[156:159], a[8:11]\n"
"v_mul_f32 v12, v12, v10\n"
"v_mul_f32 v13, v13, v11\n"
"v_mfma_f32_16x16x32_bf16 a[8:11], a[80:83], a[152:155], a[8:11]\n"
"s_waitcnt vmcnt(12)\n"
"v_accvgpr_read_b32 v10, a18\n"
"v_mfma_f32_16x16x32_bf16 v[144:147], a[32:35], a[96:99], v[144:147]\n"
"v_accvgpr_read_b32 v11, a19\n"
"v_mul_f32 v14, v14, v10\n"
"v_mfma_f32_16x16x32_bf16 v[144:147], a[36:39], a[100:103], v[144:147]\n"
"global_load_dwordx4 a[80:83], v0, s[34:35] offset:0 sc1\n"
"v_mul_f32 v15, v15, v11\n"
"v_mfma_f32_16x16x32_bf16 v[172:175], a[44:47], a[100:103], v[172:175]\n"
"v_cvt_pk_bf16_f32 v28, v12, v13\n"
"v_mfma_f32_16x16x32_bf16 v[172:175], a[40:43], a[96:99], v[172:175]\n"
"v_cvt_pk_bf16_f32 v29, v14, v15\n"
"v_lshlrev_b32 v12, 16, v18\n"
"v_mfma_f32_16x16x32_bf16 v[176:179], a[40:43], a[104:107], v[176:179]\n"
"v_and_b32 v13, s63, v18\n"
"v_mfma_f32_16x16x32_bf16 v[176:179], a[44:47], a[108:111], v[176:179]\n"
"v_lshlrev_b32 v14, 16, v19\n"
"v_mfma_f32_16x16x32_bf16 v[148:151], a[36:39], a[108:111], v[148:151]\n"
"v_and_b32 v15, s63, v19\n"
"v_accvgpr_read_b32 v10, a20\n"
"v_mfma_f32_16x16x32_bf16 v[148:151], a[32:35], a[104:107], v[148:151]\n"
"v_accvgpr_read_b32 v11, a21\n"
"v_mfma_f32_16x16x32_bf16 v[152:155], a[32:35], a[112:115], v[152:155]\n"
"v_mul_f32 v12, v12, v10\n"
"v_mul_f32 v13, v13, v11\n"
"v_mfma_f32_16x16x32_bf16 v[152:155], a[36:39], a[116:119], v[152:155]\n"
"global_load_dwordx4 a[84:87], v0, s[34:35] offset:1024 sc1\n"
"v_accvgpr_read_b32 v10, a22\n"
"v_mfma_f32_16x16x32_bf16 v[180:183], a[44:47], a[116:119], v[180:183]\n"
"v_accvgpr_read_b32 v11, a23\n"
"v_mfma_f32_16x16x32_bf16 v[180:183], a[40:43], a[112:115], v[180:183]\n"
"v_mul_f32 v14, v14, v10\n"
"v_mul_f32 v15, v15, v11\n"
"v_mfma_f32_16x16x32_bf16 v[184:187], a[40:43], a[120:123], v[184:187]\n"
"v_cvt_pk_bf16_f32 v30, v12, v13\n"
"v_mfma_f32_16x16x32_bf16 v[184:187], a[44:47], a[124:127], v[184:187]\n"
"v_cvt_pk_bf16_f32 v31, v14, v15\n"
"v_mfma_f32_16x16x32_bf16 v[156:159], a[36:39], a[124:127], v[156:159]\n"
"global_store_dwordx4 v0, v[28:31], s[54:55] offset:2048 sc1\n"
"ds_write_b128 v2, v[28:31] offset:30720\n"
"v_mfma_f32_16x16x32_bf16 v[156:159], a[32:35], a[120:123], v[156:159]\n"
"v_accvgpr_read_b32 v16, a244\n"
"v_mfma_f32_16x16x32_bf16 v[160:163], a[32:35], a[128:131], v[160:163]\n"
"v_accvgpr_read_b32 v17, a245\n"
"v_accvgpr_read_b32 v18, a246\n"
"v_mfma_f32_16x16x32_bf16 v[160:163], a[36:39], a[132:135], v[160:163]\n"
"global_load_dwordx4 a[88:91], v0, s[34:35] offset:2048 sc1\n"
"v_accvgpr_read_b32 v19, a247\n"
"v_mfma_f32_16x16x32_bf16 v[188:191], a[44:47], a[132:135], v[188:191]\n"
"v_lshlrev_b32 v12, 16, v16\n"
"v_mfma_f32_16x16x32_bf16 v[188:191], a[40:43], a[128:131], v[188:191]\n"
"v_and_b32 v13, s63, v16\n"
"v_lshlrev_b32 v14, 16, v17\n"
"v_mfma_f32_16x16x32_bf16 v[192:195], a[40:43], a[136:139], v[192:195]\n"
"v_and_b32 v15, s63, v17\n"
"v_mfma_f32_16x16x32_bf16 v[192:195], a[44:47], a[140:143], v[192:195]\n"
"v_mul_f32 v12, v12, v224\n"
"v_mul_f32 v13, v13, v225\n"
"v_mfma_f32_16x16x32_bf16 v[164:167], a[36:39], a[140:143], v[164:167]\n"
"v_mul_f32 v14, v14, v226\n"
"v_mul_f32 v15, v15, v227\n"
"v_mfma_f32_16x16x32_bf16 v[164:167], a[32:35], a[136:139], v[164:167]\n"
"v_cvt_pk_bf16_f32 v24, v12, v13\n"
"v_cvt_pk_bf16_f32 v25, v14, v15\n"
"v_mfma_f32_16x16x32_bf16 v[168:171], a[32:35], a[144:147], v[168:171]\n"
"ds_write_b64 v2, v[24:25] offset:29696\n"
"v_mfma_f32_16x16x32_bf16 v[168:171], a[36:39], a[148:151], v[168:171]\n"
"global_load_dwordx4 a[92:95], v0, s[34:35] offset:3072 sc1\n"
"v_mfma_f32_16x16x32_bf16 v[196:199], a[44:47], a[148:151], v[196:199]\n"
"v_lshlrev_b32 v12, 16, v18\n"
"v_and_b32 v13, s63, v18\n"
"v_mfma_f32_16x16x32_bf16 v[196:199], a[40:43], a[144:147], v[196:199]\n"
"v_lshlrev_b32 v14, 16, v19\n"
"v_and_b32 v15, s63, v19\n"
"v_mfma_f32_16x16x32_bf16 a[20:23], a[40:43], a[152:155], a[20:23]\n"
"s_add_u32 s34, s34, 8192\n"
"v_mul_f32 v12, v12, v252\n"
"v_mul_f32 v13, v13, v253\n"
"v_mfma_f32_16x16x32_bf16 a[20:23], a[44:47], a[156:159], a[20:23]\n"
"v_mul_f32 v14, v14, v254\n"
"v_mul_f32 v15, v15, v255\n"
"v_mfma_f32_16x16x32_bf16 a[16:19], a[36:39], a[156:159], a[16:19]\n"
"v_cvt_pk_bf16_f32 v26, v12, v13\n"
"v_cvt_pk_bf16_f32 v27, v14, v15\n"
"v_mfma_f32_16x16x32_bf16 a[16:19], a[32:35], a[152:155], a[16:19]\n"
"s_waitcnt vmcnt(12)\n"
"ds_write_b64 v2, v[26:27] offset:29704\n"
"v_mfma_f32_16x16x32_bf16 v[200:203], a[48:51], a[96:99], v[200:203]\n"
"v_accvgpr_read_b32 v16, a252\n"
"v_mfma_f32_16x16x32_bf16 v[200:203], a[52:55], a[100:103], v[200:203]\n"
"global_load_dwordx4 a[32:35], v0, s[34:35] offset:-4096 sc1\n"
"v_accvgpr_read_b32 v17, a253\n"
"v_mfma_f32_16x16x32_bf16 v[228:231], a[60:63], a[100:103], v[228:231]\n"
"ds_read_b128 a[100:103], v4 offset:1024\n"
"v_accvgpr_read_b32 v18, a254\n"
"v_mfma_f32_16x16x32_bf16 v[228:231], a[56:59], a[96:99], v[228:231]\n"
"ds_read_b128 a[96:99], v4 offset:0\n"
"v_accvgpr_read_b32 v19, a255\n"
"v_mfma_f32_16x16x32_bf16 v[232:235], a[56:59], a[104:107], v[232:235]\n"
"v_lshlrev_b32 v12, 16, v16\n"
"v_and_b32 v13, s63, v16\n"
"v_mfma_f32_16x16x32_bf16 v[232:235], a[60:63], a[108:111], v[232:235]\n"
"v_lshlrev_b32 v14, 16, v17\n"
"v_and_b32 v15, s63, v17\n"
"v_mfma_f32_16x16x32_bf16 v[204:207], a[52:55], a[108:111], v[204:207]\n"
"ds_read_b128 a[108:111], v4 offset:3072\n"
"v_accvgpr_read_b32 v10, a24\n"
"v_mfma_f32_16x16x32_bf16 v[204:207], a[48:51], a[104:107], v[204:207]\n"
"ds_read_b128 a[104:107], v4 offset:2048\n"
"v_accvgpr_read_b32 v11, a25\n"
"v_mfma_f32_16x16x32_bf16 v[208:211], a[48:51], a[112:115], v[208:211]\n"
"v_mul_f32 v12, v12, v10\n"
"v_mul_f32 v13, v13, v11\n"
"v_mfma_f32_16x16x32_bf16 v[208:211], a[52:55], a[116:119], v[208:211]\n"
"global_load_dwordx4 a[36:39], v0, s[34:35] offset:-3072 sc1\n"
"v_accvgpr_read_b32 v10, a26\n"
"v_mfma_f32_16x16x32_bf16 v[236:239], a[60:63], a[116:119], v[236:239]\n"
"ds_read_b128 a[116:119], v4 offset:5120\n"
"v_accvgpr_read_b32 v11, a27\n"
"v_mfma_f32_16x16x32_bf16 v[236:239], a[56:59], a[112:115], v[236:239]\n"
"ds_read_b128 a[112:115], v4 offset:4096\n"
"v_mul_f32 v14, v14, v10\n"
"v_mfma_f32_16x16x32_bf16 v[240:243], a[56:59], a[120:123], v[240:243]\n"
"v_mul_f32 v15, v15, v11\n"
"v_cvt_pk_bf16_f32 v28, v12, v13\n"
"v_mfma_f32_16x16x32_bf16 v[240:243], a[60:63], a[124:127], v[240:243]\n"
"v_cvt_pk_bf16_f32 v29, v14, v15\n"
"v_lshlrev_b32 v12, 16, v18\n"
"v_mfma_f32_16x16x32_bf16 v[212:215], a[52:55], a[124:127], v[212:215]\n"
"ds_read_b128 a[124:127], v4 offset:7168\n"
"v_and_b32 v13, s63, v18\n"
"v_mfma_f32_16x16x32_bf16 v[212:215], a[48:51], a[120:123], v[212:215]\n"
"ds_read_b128 a[120:123], v4 offset:6144\n"
"v_lshlrev_b32 v14, 16, v19\n"
"v_mfma_f32_16x16x32_bf16 v[216:219], a[48:51], a[128:131], v[216:219]\n"
"v_and_b32 v15, s63, v19\n"
"v_accvgpr_read_b32 v10, a28\n"
"v_mfma_f32_16x16x32_bf16 v[216:219], a[52:55], a[132:135], v[216:219]\n"
"global_load_dwordx4 a[40:43], v0, s[34:35] offset:-2048 sc1\n"
"v_accvgpr_read_b32 v11, a29\n"
"v_mfma_f32_16x16x32_bf16 v[244:247], a[60:63], a[132:135], v[244:247]\n"
"ds_read_b128 a[132:135], v4 offset:9216\n"
"v_mul_f32 v12, v12, v10\n"
"v_mfma_f32_16x16x32_bf16 v[244:247], a[56:59], a[128:131], v[244:247]\n"
"ds_read_b128 a[128:131], v4 offset:8192\n"
"v_mul_f32 v13, v13, v11\n"
"v_mfma_f32_16x16x32_bf16 v[248:251], a[56:59], a[136:139], v[248:251]\n"
"v_accvgpr_read_b32 v10, a30\n"
"v_accvgpr_read_b32 v11, a31\n"
"v_mfma_f32_16x16x32_bf16 v[248:251], a[60:63], a[140:143], v[248:251]\n"
"L_st5691:\n"
"s_cbranch_execz L_st7939\n"
"v_mul_f32 v14, v14, v10\n"
"v_mul_f32 v15, v15, v11\n"
"v_mfma_f32_16x16x32_bf16 v[220:223], a[52:55], a[140:143], v[220:223]\n"
"ds_read_b128 a[140:143], v4 offset:11264\n"
"v_cvt_pk_bf16_f32 v30, v12, v13\n"
"v_mfma_f32_16x16x32_bf16 v[220:223], a[48:51], a[136:139], v[220:223]\n"
"ds_read_b128 a[136:139], v4 offset:10240\n"
"v_cvt_pk_bf16_f32 v31, v14, v15\n"
"v_mfma_f32_16x16x32_bf16 v[224:227], a[48:51], a[144:147], v[224:227]\n"
"global_store_dwordx4 v0, v[28:31], s[54:55] offset:3072 sc1\n"
"ds_write_b128 v2, v[28:31] offset:31744\n"
"v_mfma_f32_16x16x32_bf16 v[224:227], a[52:55], a[148:151], v[224:227]\n"
"global_load_dwordx4 a[44:47], v0, s[34:35] offset:-1024 sc1\n"
"v_mfma_f32_16x16x32_bf16 v[252:255], a[60:63], a[148:151], v[252:255]\n"
"ds_read_b128 a[148:151], v4 offset:13312\n"
"v_mfma_f32_16x16x32_bf16 v[252:255], a[56:59], a[144:147], v[252:255]\n"
"ds_read_b128 a[144:147], v4 offset:12288\n"
"v_mfma_f32_16x16x32_bf16 a[28:31], a[56:59], a[152:155], a[28:31]\n"
"v_mfma_f32_16x16x32_bf16 a[28:31], a[60:63], a[156:159], a[28:31]\n"
"v_mfma_f32_16x16x32_bf16 a[24:27], a[52:55], a[156:159], a[24:27]\n"
"ds_read_b128 a[156:159], v4 offset:15360\n"
"v_mfma_f32_16x16x32_bf16 a[24:27], a[48:51], a[152:155], a[24:27]\n"
"s_waitcnt vmcnt(10) lgkmcnt(12)\n"
"ds_read_b128 a[152:155], v4 offset:14336\n"
"v_mfma_f32_16x16x32_bf16 v[144:147], a[64:67], a[96:99], v[144:147]\n"
"ds_read_b128 v[24:27], v2 offset:16384\n"
"v_mfma_f32_16x16x32_bf16 v[144:147], a[68:71], a[100:103], v[144:147]\n"
"global_load_dwordx4 a[48:51], v0, s[34:35] offset:0 sc1\n"
"v_mfma_f32_16x16x32_bf16 v[172:175], a[76:79], a[100:103], v[172:175]\n"
"v_mfma_f32_16x16x32_bf16 v[172:175], a[72:75], a[96:99], v[172:175]\n"
"v_mfma_f32_16x16x32_bf16 v[176:179], a[72:75], a[104:107], v[176:179]\n"
"v_mfma_f32_16x16x32_bf16 v[176:179], a[76:79], a[108:111], v[176:179]\n"
"v_mfma_f32_16x16x32_bf16 v[148:151], a[68:71], a[108:111], v[148:151]\n"
"v_mfma_f32_16x16x32_bf16 v[148:151], a[64:67], a[104:107], v[148:151]\n"
"s_waitcnt lgkmcnt(10)\n"
"v_mfma_f32_16x16x32_bf16 v[152:155], a[64:67], a[112:115], v[152:155]\n"
"v_mfma_f32_16x16x32_bf16 v[152:155], a[68:71], a[116:119], v[152:155]\n"
"global_load_dwordx4 a[52:55], v0, s[34:35] offset:1024 sc1\n"
"v_mfma_f32_16x16x32_bf16 v[180:183], a[76:79], a[116:119], v[180:183]\n"
"v_mfma_f32_16x16x32_bf16 v[180:183], a[72:75], a[112:115], v[180:183]\n"
"v_mfma_f32_16x16x32_bf16 v[184:187], a[72:75], a[120:123], v[184:187]\n"
"s_waitcnt lgkmcnt(0)\n"
"global_store_dwordx4 v0, v[24:27], s[52:53] offset:-4096 sc1\n"
"v_mfma_f32_16x16x32_bf16 v[184:187], a[76:79], a[124:127], v[184:187]\n"
"v_mfma_f32_16x16x32_bf16 v[156:159], a[68:71], a[124:127], v[156:159]\n"
"v_mfma_f32_16x16x32_bf16 v[156:159], a[64:67], a[120:123], v[156:159]\n"
"v_mfma_f32_16x16x32_bf16 v[160:163], a[64:67], a[128:131], v[160:163]\n"
"v_mfma_f32_16x16x32_bf16 v[160:163], a[68:71], a[132:135], v[160:163]\n"
"global_load_dwordx4 a[56:59], v0, s[34:35] offset:2048 sc1\n"
"v_mfma_f32_16x16x32_bf16 v[188:191], a[76:79], a[132:135], v[188:191]\n"
"v_mfma_f32_16x16x32_bf16 v[188:191], a[72:75], a[128:131], v[188:191]\n"
"v_mfma_f32_16x16x32_bf16 v[192:195], a[72:75], a[136:139], v[192:195]\n"
"v_mfma_f32_16x16x32_bf16 v[192:195], a[76:79], a[140:143], v[192:195]\n"
"v_mfma_f32_16x16x32_bf16 v[164:167], a[68:71], a[140:143], v[164:167]\n"
"v_mfma_f32_16x16x32_bf16 v[164:167], a[64:67], a[136:139], v[164:167]\n"
"v_mfma_f32_16x16x32_bf16 v[168:171], a[64:67], a[144:147], v[168:171]\n"
"v_mfma_f32_16x16x32_bf16 v[168:171], a[68:71], a[148:151], v[168:171]\n"
"global_load_dwordx4 a[60:63], v0, s[34:35] offset:3072 sc1\n"
"v_mfma_f32_16x16x32_bf16 v[196:199], a[76:79], a[148:151], v[196:199]\n"
"v_mfma_f32_16x16x32_bf16 v[196:199], a[72:75], a[144:147], v[196:199]\n"
"v_mfma_f32_16x16x32_bf16 a[20:23], a[72:75], a[152:155], a[20:23]\n"
"s_add_u32 s34, s34, 8192\n"
"v_mfma_f32_16x16x32_bf16 a[20:23], a[76:79], a[156:159], a[20:23]\n"
"v_mfma_f32_16x16x32_bf16 a[16:19], a[68:71], a[156:159], a[16:19]\n"
"v_mfma_f32_16x16x32_bf16 a[16:19], a[64:67], a[152:155], a[16:19]\n"
"s_waitcnt vmcnt(10)\n"
"ds_read_b128 v[28:31], v2 offset:17408\n"
"v_mfma_f32_16x16x32_bf16 v[200:203], a[80:83], a[96:99], v[200:203]\n"
"v_mfma_f32_16x16x32_bf16 v[200:203], a[84:87], a[100:103], v[200:203]\n"
"global_load_dwordx4 a[64:67], v0, s[34:35] offset:-4096 sc1\n"
"v_mfma_f32_16x16x32_bf16 v[228:231], a[92:95], a[100:103], v[228:231]\n"
"ds_read_b128 a[100:103], v3 offset:1024\n"
"v_mfma_f32_16x16x32_bf16 v[228:231], a[88:91], a[96:99], v[228:231]\n"
"ds_read_b128 a[96:99], v3 offset:0\n"
"v_mfma_f32_16x16x32_bf16 v[232:235], a[88:91], a[104:107], v[232:235]\n"
"v_mfma_f32_16x16x32_bf16 v[232:235], a[92:95], a[108:111], v[232:235]\n"
"v_mfma_f32_16x16x32_bf16 v[204:207], a[84:87], a[108:111], v[204:207]\n"
"ds_read_b128 a[108:111], v3 offset:3072\n"
"v_mfma_f32_16x16x32_bf16 v[204:207], a[80:83], a[104:107], v[204:207]\n"
"ds_read_b128 a[104:107], v3 offset:2048\n"
"v_mfma_f32_16x16x32_bf16 v[208:211], a[80:83], a[112:115], v[208:211]\n"
"v_mfma_f32_16x16x32_bf16 v[208:211], a[84:87], a[116:119], v[208:211]\n"
"global_load_dwordx4 a[68:71], v0, s[34:35] offset:-3072 sc1\n"
"v_mfma_f32_16x16x32_bf16 v[236:239], a[92:95], a[116:119], v[236:239]\n"
"s_waitcnt lgkmcnt(4)\n"
"global_store_dwordx4 v0, v[28:31], s[52:53] offset:-3072 sc1\n"
"ds_read_b128 a[116:119], v3 offset:5120\n"
"v_mfma_f32_16x16x32_bf16 v[236:239], a[88:91], a[112:115], v[236:239]\n"
"ds_read_b128 a[112:115], v3 offset:4096\n"
"v_mfma_f32_16x16x32_bf16 v[240:243], a[88:91], a[120:123], v[240:243]\n"
"v_mfma_f32_16x16x32_bf16 v[240:243], a[92:95], a[124:127], v[240:243]\n"
"v_mfma_f32_16x16x32_bf16 v[212:215], a[84:87], a[124:127], v[212:215]\n"
"ds_read_b128 a[124:127], v3 offset:7168\n"
"v_mfma_f32_16x16x32_bf16 v[212:215], a[80:83], a[120:123], v[212:215]\n"
"ds_read_b128 a[120:123], v3 offset:6144\n"
"v_mfma_f32_16x16x32_bf16 v[216:219], a[80:83], a[128:131], v[216:219]\n"
"v_mfma_f32_16x16x32_bf16 v[216:219], a[84:87], a[132:135], v[216:219]\n"
"global_load_dwordx4 a[72:75], v0, s[34:35] offset:-2048 sc1\n"
"v_mfma_f32_16x16x32_bf16 v[244:247], a[92:95], a[132:135], v[244:247]\n"
"ds_read_b128 a[132:135], v3 offset:9216\n"
"v_mfma_f32_16x16x32_bf16 v[244:247], a[88:91], a[128:131], v[244:247]\n"
"ds_read_b128 a[128:131], v3 offset:8192\n"
"v_mfma_f32_16x16x32_bf16 v[248:251], a[88:91], a[136:139], v[248:251]\n"
"v_mfma_f32_16x16x32_bf16 v[248:251], a[92:95], a[140:143], v[248:251]\n"
"v_mfma_f32_16x16x32_bf16 v[220:223], a[84:87], a[140:143], v[220:223]\n"
"ds_read_b128 a[140:143], v3 offset:11264\n"
"v_mfma_f32_16x16x32_bf16 v[220:223], a[80:83], a[136:139], v[220:223]\n"
"ds_read_b128 a[136:139], v3 offset:10240\n"
"v_mfma_f32_16x16x32_bf16 v[224:227], a[80:83], a[144:147], v[224:227]\n"
"v_mfma_f32_16x16x32_bf16 v[224:227], a[84:87], a[148:151], v[224:227]\n"
"global_load_dwordx4 a[76:79], v0, s[34:35] offset:-1024 sc1\n"
"v_mfma_f32_16x16x32_bf16 v[252:255], a[92:95], a[148:151], v[252:255]\n"
"ds_read_b128 a[148:151], v3 offset:13312\n"
"v_mfma_f32_16x16x32_bf16 v[252:255], a[88:91], a[144:147], v[252:255]\n"
"ds_read_b128 a[144:147], v3 offset:12288\n"
"v_mfma_f32_16x16x32_bf16 a[28:31], a[88:91], a[152:155], a[28:31]\n"
"v_mfma_f32_16x16x32_bf16 a[28:31], a[92:95], a[156:159], a[28:31]\n"
"ds_read_b128 v[24:27], v2 offset:18432\n"
"v_mfma_f32_16x16x32_bf16 a[24:27], a[84:87], a[156:159], a[24:27]\n"
"ds_read_b128 a[156:159], v3 offset:15360\n"
"v_mfma_f32_16x16x32_bf16 a[24:27], a[80:83], a[152:155], a[24:27]\n"
"s_waitcnt vmcnt(10) lgkmcnt(12)\n"
"ds_read_b128 a[152:155], v3 offset:14336\n"
"v_mfma_f32_16x16x32_bf16 v[144:147], a[32:35], a[96:99], v[144:147]\n"
"v_mfma_f32_16x16x32_bf16 v[144:147], a[36:39], a[100:103], v[144:147]\n"
"global_load_dwordx4 a[80:83], v0, s[34:35] offset:0 sc1\n"
"v_mfma_f32_16x16x32_bf16 v[172:175], a[44:47], a[100:103], v[172:175]\n"
"v_mfma_f32_16x16x32_bf16 v[172:175], a[40:43], a[96:99], v[172:175]\n"
"v_mfma_f32_16x16x32_bf16 v[176:179], a[40:43], a[104:107], v[176:179]\n"
"v_mfma_f32_16x16x32_bf16 v[176:179], a[44:47], a[108:111], v[176:179]\n"
"v_mfma_f32_16x16x32_bf16 v[148:151], a[36:39], a[108:111], v[148:151]\n"
"v_mfma_f32_16x16x32_bf16 v[148:151], a[32:35], a[104:107], v[148:151]\n"
"s_waitcnt lgkmcnt(9)\n"
"v_mfma_f32_16x16x32_bf16 v[152:155], a[32:35], a[112:115], v[152:155]\n"
"s_waitcnt lgkmcnt(2)\n"
"global_store_dwordx4 v0, v[24:27], s[52:53] offset:-2048 sc1\n"
"v_mfma_f32_16x16x32_bf16 v[152:155], a[36:39], a[116:119], v[152:155]\n"
"global_load_dwordx4 a[84:87], v0, s[34:35] offset:1024 sc1\n"
"v_mfma_f32_16x16x32_bf16 v[180:183], a[44:47], a[116:119], v[180:183]\n"
"v_mfma_f32_16x16x32_bf16 v[180:183], a[40:43], a[112:115], v[180:183]\n"
"v_mfma_f32_16x16x32_bf16 v[184:187], a[40:43], a[120:123], v[184:187]\n"
"v_mfma_f32_16x16x32_bf16 v[184:187], a[44:47], a[124:127], v[184:187]\n"
"v_mfma_f32_16x16x32_bf16 v[156:159], a[36:39], a[124:127], v[156:159]\n"
"v_mfma_f32_16x16x32_bf16 v[156:159], a[32:35], a[120:123], v[156:159]\n"
"v_mfma_f32_16x16x32_bf16 v[160:163], a[32:35], a[128:131], v[160:163]\n"
"v_mfma_f32_16x16x32_bf16 v[160:163], a[36:39], a[132:135], v[160:163]\n"
"global_load_dwordx4 a[88:91], v0, s[34:35] offset:2048 sc1\n"
"v_mfma_f32_16x16x32_bf16 v[188:191], a[44:47], a[132:135], v[188:191]\n"
"v_mfma_f32_16x16x32_bf16 v[188:191], a[40:43], a[128:131], v[188:191]\n"
"v_mfma_f32_16x16x32_bf16 v[192:195], a[40:43], a[136:139], v[192:195]\n"
"v_mfma_f32_16x16x32_bf16 v[192:195], a[44:47], a[140:143], v[192:195]\n"
"v_mfma_f32_16x16x32_bf16 v[164:167], a[36:39], a[140:143], v[164:167]\n"
"v_mfma_f32_16x16x32_bf16 v[164:167], a[32:35], a[136:139], v[164:167]\n"
"s_waitcnt lgkmcnt(0)\n"
"v_mfma_f32_16x16x32_bf16 v[168:171], a[32:35], a[144:147], v[168:171]\n"
"v_mfma_f32_16x16x32_bf16 v[168:171], a[36:39], a[148:151], v[168:171]\n"
"global_load_dwordx4 a[92:95], v0, s[34:35] offset:3072 sc1\n"
"v_mfma_f32_16x16x32_bf16 v[196:199], a[44:47], a[148:151], v[196:199]\n"
"v_mfma_f32_16x16x32_bf16 v[196:199], a[40:43], a[144:147], v[196:199]\n"
"ds_read_b128 v[28:31], v2 offset:19456\n"
"v_mfma_f32_16x16x32_bf16 a[20:23], a[40:43], a[152:155], a[20:23]\n"
"s_add_u32 s34, s34, 8192\n"
"v_mfma_f32_16x16x32_bf16 a[20:23], a[44:47], a[156:159], a[20:23]\n"
"v_mfma_f32_16x16x32_bf16 a[16:19], a[36:39], a[156:159], a[16:19]\n"
"v_mfma_f32_16x16x32_bf16 a[16:19], a[32:35], a[152:155], a[16:19]\n"
"s_waitcnt vmcnt(10)\n"
"v_mfma_f32_16x16x32_bf16 v[200:203], a[48:51], a[96:99], v[200:203]\n"
"v_mfma_f32_16x16x32_bf16 v[200:203], a[52:55], a[100:103], v[200:203]\n"
"global_load_dwordx4 a[32:35], v0, s[34:35] offset:-4096 sc1\n"
"v_mfma_f32_16x16x32_bf16 v[228:231], a[60:63], a[100:103], v[228:231]\n"
"ds_read_b128 a[100:103], v2 offset:1024\n"
"v_mfma_f32_16x16x32_bf16 v[228:231], a[56:59], a[96:99], v[228:231]\n"
"ds_read_b128 a[96:99], v2 offset:0\n"
"v_mfma_f32_16x16x32_bf16 v[232:235], a[56:59], a[104:107], v[232:235]\n"
"v_mfma_f32_16x16x32_bf16 v[232:235], a[60:63], a[108:111], v[232:235]\n"
"v_mfma_f32_16x16x32_bf16 v[204:207], a[52:55], a[108:111], v[204:207]\n"
"s_waitcnt lgkmcnt(2)\n"
"global_store_dwordx4 v0, v[28:31], s[52:53] offset:-1024 sc1\n"
"ds_read_b128 a[108:111], v2 offset:3072\n"
"v_mfma_f32_16x16x32_bf16 v[204:207], a[48:51], a[104:107], v[204:207]\n"
"ds_read_b128 a[104:107], v2 offset:2048\n"
"v_mfma_f32_16x16x32_bf16 v[208:211], a[48:51], a[112:115], v[208:211]\n"
"v_mfma_f32_16x16x32_bf16 v[208:211], a[52:55], a[116:119], v[208:211]\n"
"global_load_dwordx4 a[36:39], v0, s[34:35] offset:-3072 sc1\n"
"v_mfma_f32_16x16x32_bf16 v[236:239], a[60:63], a[116:119], v[236:239]\n"
"ds_read_b128 a[116:119], v2 offset:5120\n"
"v_mfma_f32_16x16x32_bf16 v[236:239], a[56:59], a[112:115], v[236:239]\n"
"ds_read_b128 a[112:115], v2 offset:4096\n"
"v_mfma_f32_16x16x32_bf16 v[240:243], a[56:59], a[120:123], v[240:243]\n"
"v_mfma_f32_16x16x32_bf16 v[240:243], a[60:63], a[124:127], v[240:243]\n"
"v_mfma_f32_16x16x32_bf16 v[212:215], a[52:55], a[124:127], v[212:215]\n"
"ds_read_b128 a[124:127], v2 offset:7168\n"
"v_mfma_f32_16x16x32_bf16 v[212:215], a[48:51], a[120:123], v[212:215]\n"
"ds_read_b128 a[120:123], v2 offset:6144\n"
"v_mfma_f32_16x16x32_bf16 v[216:219], a[48:51], a[128:131], v[216:219]\n"
"v_mfma_f32_16x16x32_bf16 v[216:219], a[52:55], a[132:135], v[216:219]\n"
"global_load_dwordx4 a[40:43], v0, s[34:35] offset:-2048 sc1\n"
"v_mfma_f32_16x16x32_bf16 v[244:247], a[60:63], a[132:135], v[244:247]\n"
"ds_read_b128 a[132:135], v2 offset:9216\n"
"v_mfma_f32_16x16x32_bf16 v[244:247], a[56:59], a[128:131], v[244:247]\n"
"ds_read_b128 a[128:131], v2 offset:8192\n"
"v_mfma_f32_16x16x32_bf16 v[248:251], a[56:59], a[136:139], v[248:251]\n"
"v_mfma_f32_16x16x32_bf16 v[248:251], a[60:63], a[140:143], v[248:251]\n"
"v_mfma_f32_16x16x32_bf16 v[220:223], a[52:55], a[140:143], v[220:223]\n"
"ds_read_b128 a[140:143], v2 offset:11264\n"
"v_mfma_f32_16x16x32_bf16 v[220:223], a[48:51], a[136:139], v[220:223]\n"
"ds_read_b128 a[136:139], v2 offset:10240\n"
"v_mfma_f32_16x16x32_bf16 v[224:227], a[48:51], a[144:147], v[224:227]\n"
"ds_read_b128 v[24:27], v2 offset:20480\n"
"v_mfma_f32_16x16x32_bf16 v[224:227], a[52:55], a[148:151], v[224:227]\n"
"global_load_dwordx4 a[44:47], v0, s[34:35] offset:-1024 sc1\n"
"v_mfma_f32_16x16x32_bf16 v[252:255], a[60:63], a[148:151], v[252:255]\n"
"ds_read_b128 a[148:151], v2 offset:13312\n"
"v_mfma_f32_16x16x32_bf16 v[252:255], a[56:59], a[144:147], v[252:255]\n"
"ds_read_b128 a[144:147], v2 offset:12288\n"
"v_mfma_f32_16x16x32_bf16 a[28:31], a[56:59], a[152:155], a[28:31]\n"
"v_mfma_f32_16x16x32_bf16 a[28:31], a[60:63], a[156:159], a[28:31]\n"
"v_mfma_f32_16x16x32_bf16 a[24:27], a[52:55], a[156:159], a[24:27]\n"
"ds_read_b128 a[156:159], v2 offset:15360\n"
"v_mfma_f32_16x16x32_bf16 a[24:27], a[48:51], a[152:155], a[24:27]\n"
"s_waitcnt vmcnt(10) lgkmcnt(12)\n"
"ds_read_b128 a[152:155], v2 offset:14336\n"
"v_mfma_f32_16x16x32_bf16 v[144:147], a[64:67], a[96:99], v[144:147]\n"
"v_mfma_f32_16x16x32_bf16 v[144:147], a[68:71], a[100:103], v[144:147]\n"
"global_load_dwordx4 a[48:51], v0, s[34:35] offset:0 sc1\n"
"v_mfma_f32_16x16x32_bf16 v[172:175], a[76:79], a[100:103], v[172:175]\n"
"v_mfma_f32_16x16x32_bf16 v[172:175], a[72:75], a[96:99], v[172:175]\n"
"v_mfma_f32_16x16x32_bf16 v[176:179], a[72:75], a[104:107], v[176:179]\n"
"s_waitcnt lgkmcnt(4)\n"
"global_store_dwordx4 v0, v[24:27], s[52:53] offset:0 sc1\n"
"v_mfma_f32_16x16x32_bf16 v[176:179], a[76:79], a[108:111], v[176:179]\n"
"v_mfma_f32_16x16x32_bf16 v[148:151], a[68:71], a[108:111], v[148:151]\n"
"v_mfma_f32_16x16x32_bf16 v[148:151], a[64:67], a[104:107], v[148:151]\n"
"v_mfma_f32_16x16x32_bf16 v[152:155], a[64:67], a[112:115], v[152:155]\n"
"v_mfma_f32_16x16x32_bf16 v[152:155], a[68:71], a[116:119], v[152:155]\n"
"global_load_dwordx4 a[52:55], v0, s[34:35] offset:1024 sc1\n"
"v_mfma_f32_16x16x32_bf16 v[180:183], a[76:79], a[116:119], v[180:183]\n"
"v_mfma_f32_16x16x32_bf16 v[180:183], a[72:75], a[112:115], v[180:183]\n"
"v_mfma_f32_16x16x32_bf16 v[184:187], a[72:75], a[120:123], v[184:187]\n"
"v_mfma_f32_16x16x32_bf16 v[184:187], a[76:79], a[124:127], v[184:187]\n"
"v_mfma_f32_16x16x32_bf16 v[156:159], a[68:71], a[124:127], v[156:159]\n"
"v_mfma_f32_16x16x32_bf16 v[156:159], a[64:67], a[120:123], v[156:159]\n"
"v_mfma_f32_16x16x32_bf16 v[160:163], a[64:67], a[128:131], v[160:163]\n"
"v_mfma_f32_16x16x32_bf16 v[160:163], a[68:71], a[132:135], v[160:163]\n"
"global_load_dwordx4 a[56:59], v0, s[34:35] offset:2048 sc1\n"
"v_mfma_f32_16x16x32_bf16 v[188:191], a[76:79], a[132:135], v[188:191]\n"
"v_mfma_f32_16x16x32_bf16 v[188:191], a[72:75], a[128:131], v[188:191]\n"
"v_mfma_f32_16x16x32_bf16 v[192:195], a[72:75], a[136:139], v[192:195]\n"
"v_mfma_f32_16x16x32_bf16 v[192:195], a[76:79], a[140:143], v[192:195]\n"
"v_mfma_f32_16x16x32_bf16 v[164:167], a[68:71], a[140:143], v[164:167]\n"
"v_mfma_f32_16x16x32_bf16 v[164:167], a[64:67], a[136:139], v[164:167]\n"
"s_waitcnt lgkmcnt(0)\n"
"ds_read_b128 v[28:31], v2 offset:21504\n"
"v_mfma_f32_16x16x32_bf16 v[168:171], a[64:67], a[144:147], v[168:171]\n"
"v_mfma_f32_16x16x32_bf16 v[168:171], a[68:71], a[148:151], v[168:171]\n"
"global_load_dwordx4 a[60:63], v0, s[34:35] offset:3072 sc1\n"
"v_mfma_f32_16x16x32_bf16 v[196:199], a[76:79], a[148:151], v[196:199]\n"
"v_mfma_f32_16x16x32_bf16 v[196:199], a[72:75], a[144:147], v[196:199]\n"
"v_mfma_f32_16x16x32_bf16 a[20:23], a[72:75], a[152:155], a[20:23]\n"
"s_add_u32 s34, s34, 8192\n"
"v_mfma_f32_16x16x32_bf16 a[20:23], a[76:79], a[156:159], a[20:23]\n"
"v_mfma_f32_16x16x32_bf16 a[16:19], a[68:71], a[156:159], a[16:19]\n"
"v_mfma_f32_16x16x32_bf16 a[16:19], a[64:67], a[152:155], a[16:19]\n"
"s_waitcnt vmcnt(10)\n"
"v_mfma_f32_16x16x32_bf16 v[200:203], a[80:83], a[96:99], v[200:203]\n"
"v_mfma_f32_16x16x32_bf16 v[200:203], a[84:87], a[100:103], v[200:203]\n"
"global_load_dwordx4 a[64:67], v0, s[34:35] offset:-4096 sc1\n"
"v_mfma_f32_16x16x32_bf16 v[228:231], a[92:95], a[100:103], v[228:231]\n"
"s_waitcnt lgkmcnt(0)\n"
"global_store_dwordx4 v0, v[28:31], s[52:53] offset:1024 sc1\n"
"ds_read_b128 a[100:103], v2 offset:17408\n"
"v_mfma_f32_16x16x32_bf16 v[228:231], a[88:91], a[96:99], v[228:231]\n"
"ds_read_b128 a[96:99], v2 offset:16384\n"
"v_mfma_f32_16x16x32_bf16 v[232:235], a[88:91], a[104:107], v[232:235]\n"
"v_mfma_f32_16x16x32_bf16 v[232:235], a[92:95], a[108:111], v[232:235]\n"
"v_mfma_f32_16x16x32_bf16 v[204:207], a[84:87], a[108:111], v[204:207]\n"
"ds_read_b128 a[108:111], v2 offset:19456\n"
"v_mfma_f32_16x16x32_bf16 v[204:207], a[80:83], a[104:107], v[204:207]\n"
"ds_read_b128 a[104:107], v2 offset:18432\n"
"v_mfma_f32_16x16x32_bf16 v[208:211], a[80:83], a[112:115], v[208:211]\n"
"v_mfma_f32_16x16x32_bf16 v[208:211], a[84:87], a[116:119], v[208:211]\n"
"global_load_dwordx4 a[68:71], v0, s[34:35] offset:-3072 sc1\n"
"v_mfma_f32_16x16x32_bf16 v[236:239], a[92:95], a[116:119], v[236:239]\n"
"ds_read_b128 a[116:119], v2 offset:21504\n"
"v_mfma_f32_16x16x32_bf16 v[236:239], a[88:91], a[112:115], v[236:239]\n"
"ds_read_b128 a[112:115], v2 offset:20480\n"
"v_mfma_f32_16x16x32_bf16 v[240:243], a[88:91], a[120:123], v[240:243]\n"
"v_mfma_f32_16x16x32_bf16 v[240:243], a[92:95], a[124:127], v[240:243]\n"
"v_mfma_f32_16x16x32_bf16 v[212:215], a[84:87], a[124:127], v[212:215]\n"
"ds_read_b128 a[124:127], v2 offset:23552\n"
"v_mfma_f32_16x16x32_bf16 v[212:215], a[80:83], a[120:123], v[212:215]\n"
"ds_read_b128 a[120:123], v2 offset:22528\n"
"v_mfma_f32_16x16x32_bf16 v[216:219], a[80:83], a[128:131], v[216:219]\n"
"v_mfma_f32_16x16x32_bf16 v[216:219], a[84:87], a[132:135], v[216:219]\n"
"global_load_dwordx4 a[72:75], v0, s[34:35] offset:-2048 sc1\n"
"v_mfma_f32_16x16x32_bf16 v[244:247], a[92:95], a[132:135], v[244:247]\n"
"ds_read_b128 a[132:135], v2 offset:25600\n"
"v_mfma_f32_16x16x32_bf16 v[244:247], a[88:91], a[128:131], v[244:247]\n"
"ds_read_b128 a[128:131], v2 offset:24576\n"
"v_mfma_f32_16x16x32_bf16 v[248:251], a[88:91], a[136:139], v[248:251]\n"
"v_mfma_f32_16x16x32_bf16 v[248:251], a[92:95], a[140:143], v[248:251]\n"
"ds_read_b128 v[24:27], v2 offset:22528\n"
"v_mfma_f32_16x16x32_bf16 v[220:223], a[84:87], a[140:143], v[220:223]\n"
"ds_read_b128 a[140:143], v2 offset:27648\n"
"v_mfma_f32_16x16x32_bf16 v[220:223], a[80:83], a[136:139], v[220:223]\n"
"ds_read_b128 a[136:139], v2 offset:26624\n"
"v_mfma_f32_16x16x32_bf16 v[224:227], a[80:83], a[144:147], v[224:227]\n"
"v_mfma_f32_16x16x32_bf16 v[224:227], a[84:87], a[148:151], v[224:227]\n"
"global_load_dwordx4 a[76:79], v0, s[34:35] offset:-1024 sc1\n"
"v_mfma_f32_16x16x32_bf16 v[252:255], a[92:95], a[148:151], v[252:255]\n"
"ds_read_b128 a[148:151], v2 offset:29696\n"
"v_mfma_f32_16x16x32_bf16 v[252:255], a[88:91], a[144:147], v[252:255]\n"
"ds_read_b128 a[144:147], v2 offset:28672\n"
"v_mfma_f32_16x16x32_bf16 a[28:31], a[88:91], a[152:155], a[28:31]\n"
"v_mfma_f32_16x16x32_bf16 a[28:31], a[92:95], a[156:159], a[28:31]\n"
"v_mfma_f32_16x16x32_bf16 a[24:27], a[84:87], a[156:159], a[24:27]\n"
"s_bitcmp0_b32 %26, 0\n"
"ds_read_b128 a[156:159], v2 offset:31744\n"
"v_mfma_f32_16x16x32_bf16 a[24:27], a[80:83], a[152:155], a[24:27]\n"
"ds_read_b128 a[152:155], v2 offset:30720\n"
"s_cbranch_scc1 L_P3F\n"
"s_waitcnt vmcnt(10) lgkmcnt(13)\n"
"v_mfma_f32_16x16x32_bf16 v[32:35], a[32:35], a[96:99], v[32:35]\n"
"s_waitcnt lgkmcnt(6)\n"
"global_store_dwordx4 v0, v[24:27], s[52:53] offset:2048 sc1\n"
"v_mfma_f32_16x16x32_bf16 v[32:35], a[36:39], a[100:103], v[32:35]\n"
"global_load_dwordx4 a[80:83], v0, s[34:35] offset:0 sc1\n"
"v_mfma_f32_16x16x32_bf16 v[60:63], a[44:47], a[100:103], v[60:63]\n"
"s_barrier\n"
"v_mfma_f32_16x16x32_bf16 v[60:63], a[40:43], a[96:99], v[60:63]\n"
"v_mfma_f32_16x16x32_bf16 v[64:67], a[40:43], a[104:107], v[64:67]\n"
"v_mfma_f32_16x16x32_bf16 v[64:67], a[44:47], a[108:111], v[64:67]\n"
"v_mfma_f32_16x16x32_bf16 v[36:39], a[36:39], a[108:111], v[36:39]\n"
"v_mfma_f32_16x16x32_bf16 v[36:39], a[32:35], a[104:107], v[36:39]\n"
"v_mfma_f32_16x16x32_bf16 v[40:43], a[32:35], a[112:115], v[40:43]\n"
"v_mfma_f32_16x16x32_bf16 v[40:43], a[36:39], a[116:119], v[40:43]\n"
"global_load_dwordx4 a[84:87], v0, s[34:35] offset:1024 sc1\n"
"v_mfma_f32_16x16x32_bf16 v[68:71], a[44:47], a[116:119], v[68:71]\n"
"v_mfma_f32_16x16x32_bf16 v[68:71], a[40:43], a[112:115], v[68:71]\n"
"v_mfma_f32_16x16x32_bf16 v[72:75], a[40:43], a[120:123], v[72:75]\n"
"v_mfma_f32_16x16x32_bf16 v[72:75], a[44:47], a[124:127], v[72:75]\n"
"v_mfma_f32_16x16x32_bf16 v[44:47], a[36:39], a[124:127], v[44:47]\n"
"v_mfma_f32_16x16x32_bf16 v[44:47], a[32:35], a[120:123], v[44:47]\n"
"s_waitcnt lgkmcnt(4)\n"
"v_mfma_f32_16x16x32_bf16 v[48:51], a[32:35], a[128:131], v[48:51]\n"
"v_mfma_f32_16x16x32_bf16 v[48:51], a[36:39], a[132:135], v[48:51]\n"
"global_load_dwordx4 a[88:91], v0, s[34:35] offset:2048 sc1\n"
"v_lshrrev_b32 v16, 4, v0\n"
"v_mfma_f32_16x16x32_bf16 v[76:79], a[44:47], a[132:135], v[76:79]\n"
"v_and_b32 v16, 0x30, v16\n"
"v_mfma_f32_16x16x32_bf16 v[76:79], a[40:43], a[128:131], v[76:79]\n"
"ds_read_b128 v[28:31], v2 offset:23552\n"
"buffer_load_dwordx2 v[18:19], v16, s[44:47], s61 offen offset:0\n"
"v_mfma_f32_16x16x32_bf16 v[80:83], a[40:43], a[136:139], v[80:83]\n"
"buffer_load_dwordx2 v[8:9], off, s[56:59], %14 sc1\n"
"v_mfma_f32_16x16x32_bf16 v[80:83], a[44:47], a[140:143], v[80:83]\n"
"v_mfma_f32_16x16x32_bf16 v[52:55], a[36:39], a[140:143], v[52:55]\n"
"v_mfma_f32_16x16x32_bf16 v[52:55], a[32:35], a[136:139], v[52:55]\n"
"s_waitcnt lgkmcnt(1)\n"
"v_mfma_f32_16x16x32_bf16 v[56:59], a[32:35], a[144:147], v[56:59]\n"
"v_mfma_f32_16x16x32_bf16 v[56:59], a[36:39], a[148:151], v[56:59]\n"
"global_load_dwordx4 a[92:95], v0, s[34:35] offset:3072 sc1\n"
"v_lshrrev_b32 v20, 4, v0\n"
"v_mfma_f32_16x16x32_bf16 v[84:87], a[44:47], a[148:151], v[84:87]\n"
"v_and_b32 v20, 0x30, v20\n"
"v_mfma_f32_16x16x32_bf16 v[84:87], a[40:43], a[144:147], v[84:87]\n"
"buffer_load_dwordx2 v[22:23], v20, s[44:47], s61 offen offset:8\n"
"v_mfma_f32_16x16x32_bf16 a[4:7], a[40:43], a[152:155], a[4:7]\n"
"s_add_u32 s34, s34, 8192\n"
"v_mfma_f32_16x16x32_bf16 a[4:7], a[44:47], a[156:159], a[4:7]\n"
"v_mfma_f32_16x16x32_bf16 a[0:3], a[36:39], a[156:159], a[0:3]\n"
"s_waitcnt lgkmcnt(0)\n"
"global_store_dwordx4 v0, v[28:31], s[52:53] offset:3072 sc1\n"
"v_mfma_f32_16x16x32_bf16 a[0:3], a[32:35], a[152:155], a[0:3]\n"
"s_waitcnt vmcnt(14)\n"
"v_mfma_f32_16x16x32_bf16 v[88:91], a[48:51], a[96:99], v[88:91]\n"
"v_mfma_f32_16x16x32_bf16 v[88:91], a[52:55], a[100:103], v[88:91]\n"
"global_load_dwordx4 a[32:35], v0, s[34:35] offset:-4096 sc1\n"
"v_mfma_f32_16x16x32_bf16 v[116:119], a[60:63], a[100:103], v[116:119]\n"
"v_mfma_f32_16x16x32_bf16 v[116:119], a[56:59], a[96:99], v[116:119]\n"
"v_mfma_f32_16x16x32_bf16 v[120:123], a[56:59], a[104:107], v[120:123]\n"
"v_mfma_f32_16x16x32_bf16 v[120:123], a[60:63], a[108:111], v[120:123]\n"
"v_mfma_f32_16x16x32_bf16 v[92:95], a[52:55], a[108:111], v[92:95]\n"
"v_mfma_f32_16x16x32_bf16 v[92:95], a[48:51], a[104:107], v[92:95]\n"
"v_mfma_f32_16x16x32_bf16 v[96:99], a[48:51], a[112:115], v[96:99]\n"
"v_mfma_f32_16x16x32_bf16 v[96:99], a[52:55], a[116:119], v[96:99]\n"
"global_load_dwordx4 a[36:39], v0, s[34:35] offset:-3072 sc1\n"
"v_mfma_f32_16x16x32_bf16 v[124:127], a[60:63], a[116:119], v[124:127]\n"
"v_mfma_f32_16x16x32_bf16 v[124:127], a[56:59], a[112:115], v[124:127]\n"
"v_mfma_f32_16x16x32_bf16 v[128:131], a[56:59], a[120:123], v[128:131]\n"
"v_mfma_f32_16x16x32_bf16 v[128:131], a[60:63], a[124:127], v[128:131]\n"
"v_mfma_f32_16x16x32_bf16 v[100:103], a[52:55], a[124:127], v[100:103]\n"
"v_mfma_f32_16x16x32_bf16 v[100:103], a[48:51], a[120:123], v[100:103]\n"
"v_mfma_f32_16x16x32_bf16 v[104:107], a[48:51], a[128:131], v[104:107]\n"
"ds_read_b128 v[24:27], v2 offset:24576\n"
"v_mfma_f32_16x16x32_bf16 v[104:107], a[52:55], a[132:135], v[104:107]\n"
"global_load_dwordx4 a[40:43], v0, s[34:35] offset:-2048 sc1\n"
"v_mfma_f32_16x16x32_bf16 v[132:135], a[60:63], a[132:135], v[132:135]\n"
"v_mfma_f32_16x16x32_bf16 v[132:135], a[56:59], a[128:131], v[132:135]\n"
"v_mfma_f32_16x16x32_bf16 v[136:139], a[56:59], a[136:139], v[136:139]\n"
"v_mfma_f32_16x16x32_bf16 v[136:139], a[60:63], a[140:143], v[136:139]\n"
"v_mfma_f32_16x16x32_bf16 v[108:111], a[52:55], a[140:143], v[108:111]\n"
"v_mfma_f32_16x16x32_bf16 v[108:111], a[48:51], a[136:139], v[108:111]\n"
"v_mfma_f32_16x16x32_bf16 v[112:115], a[48:51], a[144:147], v[112:115]\n"
"v_mfma_f32_16x16x32_bf16 v[112:115], a[52:55], a[148:151], v[112:115]\n"
"global_load_dwordx4 a[44:47], v0, s[34:35] offset:-1024 sc1\n"
"v_mfma_f32_16x16x32_bf16 v[140:143], a[60:63], a[148:151], v[140:143]\n"
"v_mfma_f32_16x16x32_bf16 v[140:143], a[56:59], a[144:147], v[140:143]\n"
"v_mfma_f32_16x16x32_bf16 a[12:15], a[56:59], a[152:155], a[12:15]\n"
"s_waitcnt lgkmcnt(0)\n"
"global_store_dwordx4 v0, v[24:27], s[54:55] offset:-4096 sc1\n"
"v_mfma_f32_16x16x32_bf16 a[12:15], a[60:63], a[156:159], a[12:15]\n"
"v_mfma_f32_16x16x32_bf16 a[8:11], a[52:55], a[156:159], a[8:11]\n"
"v_mfma_f32_16x16x32_bf16 a[8:11], a[48:51], a[152:155], a[8:11]\n"
"s_waitcnt vmcnt(14)\n"
"v_mfma_f32_16x16x32_bf16 v[144:147], a[64:67], a[96:99], v[144:147]\n"
"v_mfma_f32_16x16x32_bf16 v[144:147], a[68:71], a[100:103], v[144:147]\n"
"global_load_dwordx4 a[48:51], v0, s[34:35] offset:0 sc1\n"
"v_mfma_f32_16x16x32_bf16 v[172:175], a[76:79], a[100:103], v[172:175]\n"
"v_mfma_f32_16x16x32_bf16 v[172:175], a[72:75], a[96:99], v[172:175]\n"
"v_mfma_f32_16x16x32_bf16 v[176:179], a[72:75], a[104:107], v[176:179]\n"
"v_mfma_f32_16x16x32_bf16 v[176:179], a[76:79], a[108:111], v[176:179]\n"
"v_mfma_f32_16x16x32_bf16 v[148:151], a[68:71], a[108:111], v[148:151]\n"
"v_mfma_f32_16x16x32_bf16 v[148:151], a[64:67], a[104:107], v[148:151]\n"
"v_mfma_f32_16x16x32_bf16 v[152:155], a[64:67], a[112:115], v[152:155]\n"
"v_mfma_f32_16x16x32_bf16 v[152:155], a[68:71], a[116:119], v[152:155]\n"
"global_load_dwordx4 a[52:55], v0, s[34:35] offset:1024 sc1\n"
"v_mfma_f32_16x16x32_bf16 v[180:183], a[76:79], a[116:119], v[180:183]\n"
"v_mfma_f32_16x16x32_bf16 v[180:183], a[72:75], a[112:115], v[180:183]\n"
"v_mfma_f32_16x16x32_bf16 v[184:187], a[72:75], a[120:123], v[184:187]\n"
"v_mfma_f32_16x16x32_bf16 v[184:187], a[76:79], a[124:127], v[184:187]\n"
"v_mfma_f32_16x16x32_bf16 v[156:159], a[68:71], a[124:127], v[156:159]\n"
"v_mfma_f32_16x16x32_bf16 v[156:159], a[64:67], a[120:123], v[156:159]\n"
"ds_read_b128 v[28:31], v2 offset:26624\n"
"v_mfma_f32_16x16x32_bf16 v[160:163], a[64:67], a[128:131], v[160:163]\n"
"v_mfma_f32_16x16x32_bf16 v[160:163], a[68:71], a[132:135], v[160:163]\n"
"global_load_dwordx4 a[56:59], v0, s[34:35] offset:2048 sc1\n"
"v_mfma_f32_16x16x32_bf16 v[188:191], a[76:79], a[132:135], v[188:191]\n"
"v_mfma_f32_16x16x32_bf16 v[188:191], a[72:75], a[128:131], v[188:191]\n"
"v_mfma_f32_16x16x32_bf16 v[192:195], a[72:75], a[136:139], v[192:195]\n"
"v_mfma_f32_16x16x32_bf16 v[192:195], a[76:79], a[140:143], v[192:195]\n"
"v_mfma_f32_16x16x32_bf16 v[164:167], a[68:71], a[140:143], v[164:167]\n"
"v_mfma_f32_16x16x32_bf16 v[164:167], a[64:67], a[136:139], v[164:167]\n"
"v_mfma_f32_16x16x32_bf16 v[168:171], a[64:67], a[144:147], v[168:171]\n"
"v_mfma_f32_16x16x32_bf16 v[168:171], a[68:71], a[148:151], v[168:171]\n"
"global_load_dwordx4 a[60:63], v0, s[34:35] offset:3072 sc1\n"
"v_mfma_f32_16x16x32_bf16 v[196:199], a[76:79], a[148:151], v[196:199]\n"
"s_waitcnt lgkmcnt(0)\n"
"global_store_dwordx4 v0, v[28:31], s[54:55] offset:-2048 sc1\n"
"v_mfma_f32_16x16x32_bf16 v[196:199], a[72:75], a[144:147], v[196:199]\n"
"v_mfma_f32_16x16x32_bf16 a[20:23], a[72:75], a[152:155], a[20:23]\n"
"s_add_u32 s34, s34, 8192\n"
"v_mfma_f32_16x16x32_bf16 a[20:23], a[76:79], a[156:159], a[20:23]\n"
"v_mfma_f32_16x16x32_bf16 a[16:19], a[68:71], a[156:159], a[16:19]\n"
"v_mfma_f32_16x16x32_bf16 a[16:19], a[64:67], a[152:155], a[16:19]\n"
"s_waitcnt vmcnt(12)\n"
"v_mfma_f32_16x16x32_bf16 v[200:203], a[80:83], a[96:99], v[200:203]\n"
"v_mfma_f32_16x16x32_bf16 v[200:203], a[84:87], a[100:103], v[200:203]\n"
"global_load_dwordx4 a[64:67], v0, s[34:35] offset:-4096 sc1\n"
"v_mfma_f32_16x16x32_bf16 v[228:231], a[92:95], a[100:103], v[228:231]\n"
"ds_read_b128 a[100:103], v3 offset:17408\n"
"v_mfma_f32_16x16x32_bf16 v[228:231], a[88:91], a[96:99], v[228:231]\n"
"ds_read_b128 a[96:99], v3 offset:16384\n"
"v_mfma_f32_16x16x32_bf16 v[232:235], a[88:91], a[104:107], v[232:235]\n"
"v_mfma_f32_16x16x32_bf16 v[232:235], a[92:95], a[108:111], v[232:235]\n"
"v_mfma_f32_16x16x32_bf16 v[204:207], a[84:87], a[108:111], v[204:207]\n"
"ds_read_b128 a[108:111], v3 offset:19456\n"
"v_mfma_f32_16x16x32_bf16 v[204:207], a[80:83], a[104:107], v[204:207]\n"
"ds_read_b128 a[104:107], v3 offset:18432\n"
"v_mfma_f32_16x16x32_bf16 v[208:211], a[80:83], a[112:115], v[208:211]\n"
"v_mfma_f32_16x16x32_bf16 v[208:211], a[84:87], a[116:119], v[208:211]\n"
"global_load_dwordx4 a[68:71], v0, s[34:35] offset:-3072 sc1\n"
"v_mfma_f32_16x16x32_bf16 v[236:239], a[92:95], a[116:119], v[236:239]\n"
"ds_read_b128 a[116:119], v3 offset:21504\n"
"v_mfma_f32_16x16x32_bf16 v[236:239], a[88:91], a[112:115], v[236:239]\n"
"ds_read_b128 a[112:115], v3 offset:20480\n"
"v_mfma_f32_16x16x32_bf16 v[240:243], a[88:91], a[120:123], v[240:243]\n"
"v_mfma_f32_16x16x32_bf16 v[240:243], a[92:95], a[124:127], v[240:243]\n"
"ds_read_b128 v[24:27], v2 offset:29696\n"
"v_mfma_f32_16x16x32_bf16 v[212:215], a[84:87], a[124:127], v[212:215]\n"
"ds_read_b128 a[124:127], v3 offset:23552\n"
"v_mfma_f32_16x16x32_bf16 v[212:215], a[80:83], a[120:123], v[212:215]\n"
"ds_read_b128 a[120:123], v3 offset:22528\n"
"v_mfma_f32_16x16x32_bf16 v[216:219], a[80:83], a[128:131], v[216:219]\n"
"v_mfma_f32_16x16x32_bf16 v[216:219], a[84:87], a[132:135], v[216:219]\n"
"global_load_dwordx4 a[72:75], v0, s[34:35] offset:-2048 sc1\n"
"v_mfma_f32_16x16x32_bf16 v[244:247], a[92:95], a[132:135], v[244:247]\n"
"ds_read_b128 a[132:135], v3 offset:25600\n"
"v_mfma_f32_16x16x32_bf16 v[244:247], a[88:91], a[128:131], v[244:247]\n"
"ds_read_b128 a[128:131], v3 offset:24576\n"
"v_mfma_f32_16x16x32_bf16 v[248:251], a[88:91], a[136:139], v[248:251]\n"
"v_subrev_u32 v8, s65, v8\n"
"v_mfma_f32_16x16x32_bf16 v[248:251], a[92:95], a[140:143], v[248:251]\n"
"v_xor_b32 v9, s79, v9\n"
"v_mfma_f32_16x16x32_bf16 v[220:223], a[84:87], a[140:143], v[220:223]\n"
"ds_read_b128 a[140:143], v3 offset:27648\n"
"v_or_b32 v8, v8, v9\n"
"v_mfma_f32_16x16x32_bf16 v[220:223], a[80:83], a[136:139], v[220:223]\n"
"ds_read_b128 a[136:139], v3 offset:26624\n"
"v_cmp_lt_u32 vcc, 15, v8\n"
"v_mfma_f32_16x16x32_bf16 v[224:227], a[80:83], a[144:147], v[224:227]\n"
"s_waitcnt lgkmcnt(6)\n"
"global_store_dwordx4 v0, v[24:27], s[54:55] offset:1024 sc1\n"
"s_cbranch_vccnz L_slow0\n"
"L_back0:\n"
"v_mfma_f32_16x16x32_bf16 v[224:227], a[84:87], a[148:151], v[224:227]\n"
"global_load_dwordx4 a[76:79], v0, s[34:35] offset:-1024 sc1\n"
"v_readfirstlane_b32 s66, v9\n"
"v_mfma_f32_16x16x32_bf16 v[252:255], a[92:95], a[148:151], v[252:255]\n"
"ds_read_b128 a[148:151], v3 offset:29696\n"
"v_mfma_f32_16x16x32_bf16 v[252:255], a[88:91], a[144:147], v[252:255]\n"
"s_and_b32 s66, s66, 3\n"
"ds_read_b128 a[144:147], v3 offset:28672\n"
"v_mfma_f32_16x16x32_bf16 a[28:31], a[88:91], a[152:155], a[28:31]\n"
"s_lshl_b32 s69, s66, 13\n"
"v_mfma_f32_16x16x32_bf16 a[28:31], a[92:95], a[156:159], a[28:31]\n"
"s_cmp_eq_u32 s66, 3\n"
"s_cselect_b32 s75, 8192, 0\n"
"v_mfma_f32_16x16x32_bf16 a[24:27], a[84:87], a[156:159], a[24:27]\n"
"s_and_b32 s29, s66, s30\n"
"ds_read_b128 a[156:159], v3 offset:31744\n"
"v_mfma_f32_16x16x32_bf16 a[24:27], a[80:83], a[152:155], a[24:27]\n"
"s_waitcnt vmcnt(11)\n"
"ds_read_b128 a[152:155], v3 offset:30720\n"
"v_mfma_f32_16x16x32_bf16 v[32:35], a[32:35], a[96:99], v[32:35]\n"
"s_xor_b32 s31, s79, s66\n"
"v_mfma_f32_16x16x32_bf16 v[32:35], a[36:39], a[100:103], v[32:35]\n"
"global_load_dwordx4 a[80:83], v0, s[34:35] offset:0 sc1\n"
"v_mfma_f32_16x16x32_bf16 v[60:63], a[44:47], a[100:103], v[60:63]\n"
"v_mfma_f32_16x16x32_bf16 v[60:63], a[40:43], a[96:99], v[60:63]\n"
"v_mfma_f32_16x16x32_bf16 v[64:67], a[40:43], a[104:107], v[64:67]\n"
"v_mfma_f32_16x16x32_bf16 v[64:67], a[44:47], a[108:111], v[64:67]\n"
"v_mfma_f32_16x16x32_bf16 v[36:39], a[36:39], a[108:111], v[36:39]\n"
"s_add_u32 m0, s64, 0\n"
"v_mfma_f32_16x16x32_bf16 v[36:39], a[32:35], a[104:107], v[36:39]\n"
"L_st6226:\n"
"s_cbranch_execz L_st8508\n"
"s_waitcnt lgkmcnt(8)\n"
"v_mfma_f32_16x16x32_bf16 v[40:43], a[32:35], a[112:115], v[40:43]\n"
"v_mfma_f32_16x16x32_bf16 v[40:43], a[36:39], a[116:119], v[40:43]\n"
"global_load_dwordx4 a[84:87], v0, s[34:35] offset:1024 sc1\n"
"v_mfma_f32_16x16x32_bf16 v[68:71], a[44:47], a[116:119], v[68:71]\n"
"v_mfma_f32_16x16x32_bf16 v[68:71], a[40:43], a[112:115], v[68:71]\n"
"v_mfma_f32_16x16x32_bf16 v[72:75], a[40:43], a[120:123], v[72:75]\n"
"v_mfma_f32_16x16x32_bf16 v[72:75], a[44:47], a[124:127], v[72:75]\n"
"v_mfma_f32_16x16x32_bf16 v[44:47], a[36:39], a[124:127], v[44:47]\n"
"v_mfma_f32_16x16x32_bf16 v[44:47], a[32:35], a[120:123], v[44:47]\n"
"s_waitcnt lgkmcnt(4)\n"
"v_mfma_f32_16x16x32_bf16 v[48:51], a[32:35], a[128:131], v[48:51]\n"
"v_mfma_f32_16x16x32_bf16 v[48:51], a[36:39], a[132:135], v[48:51]\n"
"global_load_dwordx4 a[88:91], v0, s[34:35] offset:2048 sc1\n"
"v_mfma_f32_16x16x32_bf16 v[76:79], a[44:47], a[132:135], v[76:79]\n"
"v_mfma_f32_16x16x32_bf16 v[76:79], a[40:43], a[128:131], v[76:79]\n"
"v_mfma_f32_16x16x32_bf16 v[80:83], a[40:43], a[136:139], v[80:83]\n"
"v_mfma_f32_16x16x32_bf16 v[80:83], a[44:47], a[140:143], v[80:83]\n"
"buffer_load_dwordx4 v0, s[48:51], s75 offen offset:0 sc1 lds\n"
"v_mfma_f32_16x16x32_bf16 v[52:55], a[36:39], a[140:143], v[52:55]\n"
"v_mfma_f32_16x16x32_bf16 v[52:55], a[32:35], a[136:139], v[52:55]\n"
"s_waitcnt lgkmcnt(0)\n"
"v_mfma_f32_16x16x32_bf16 v[56:59], a[32:35], a[144:147], v[56:59]\n"
"v_mfma_f32_16x16x32_bf16 v[56:59], a[36:39], a[148:151], v[56:59]\n"
"global_load_dwordx4 a[92:95], v0, s[34:35] offset:3072 sc1\n"
"v_mfma_f32_16x16x32_bf16 v[84:87], a[44:47], a[148:151], v[84:87]\n"
"v_mfma_f32_16x16x32_bf16 v[84:87], a[40:43], a[144:147], v[84:87]\n"
"v_mfma_f32_16x16x32_bf16 a[4:7], a[40:43], a[152:155], a[4:7]\n"
"s_add_u32 s34, s34, 8192\n"
"v_mfma_f32_16x16x32_bf16 a[4:7], a[44:47], a[156:159], a[4:7]\n"
"v_mfma_f32_16x16x32_bf16 a[0:3], a[36:39], a[156:159], a[0:3]\n"
"v_mfma_f32_16x16x32_bf16 a[0:3], a[32:35], a[152:155], a[0:3]\n"
"s_waitcnt vmcnt(11)\n"
"v_mfma_f32_16x16x32_bf16 v[88:91], a[48:51], a[96:99], v[88:91]\n"
"v_mfma_f32_16x16x32_bf16 v[88:91], a[52:55], a[100:103], v[88:91]\n"
"global_load_dwordx4 a[32:35], v0, s[34:35] offset:-4096 sc1\n"
"v_mfma_f32_16x16x32_bf16 v[116:119], a[60:63], a[100:103], v[116:119]\n"
"v_mfma_f32_16x16x32_bf16 v[116:119], a[56:59], a[96:99], v[116:119]\n"
"buffer_load_dwordx4 v0, s[48:51], s75 offen offset:1024 sc1 lds\n"
"v_mfma_f32_16x16x32_bf16 v[120:123], a[56:59], a[104:107], v[120:123]\n"
"v_mfma_f32_16x16x32_bf16 v[120:123], a[60:63], a[108:111], v[120:123]\n"
"v_mfma_f32_16x16x32_bf16 v[92:95], a[52:55], a[108:111], v[92:95]\n"
"v_mfma_f32_16x16x32_bf16 v[92:95], a[48:51], a[104:107], v[92:95]\n"
"v_mfma_f32_16x16x32_bf16 v[96:99], a[48:51], a[112:115], v[96:99]\n"
"v_mfma_f32_16x16x32_bf16 v[96:99], a[52:55], a[116:119], v[96:99]\n"
"global_load_dwordx4 a[36:39], v0, s[34:35] offset:-3072 sc1\n"
"v_mfma_f32_16x16x32_bf16 v[124:127], a[60:63], a[116:119], v[124:127]\n"
"v_mfma_f32_16x16x32_bf16 v[124:127], a[56:59], a[112:115], v[124:127]\n"
"v_mfma_f32_16x16x32_bf16 v[128:131], a[56:59], a[120:123], v[128:131]\n"
"v_mfma_f32_16x16x32_bf16 v[128:131], a[60:63], a[124:127], v[128:131]\n"
"v_mfma_f32_16x16x32_bf16 v[100:103], a[52:55], a[124:127], v[100:103]\n"
"v_mfma_f32_16x16x32_bf16 v[100:103], a[48:51], a[120:123], v[100:103]\n"
"v_mfma_f32_16x16x32_bf16 v[104:107], a[48:51], a[128:131], v[104:107]\n"
"v_mfma_f32_16x16x32_bf16 v[104:107], a[52:55], a[132:135], v[104:107]\n"
"global_load_dwordx4 a[40:43], v0, s[34:35] offset:-2048 sc1\n"
"v_mfma_f32_16x16x32_bf16 v[132:135], a[60:63], a[132:135], v[132:135]\n"
"buffer_load_dwordx4 v0, s[48:51], s75 offen offset:2048 sc1 lds\n"
"v_mfma_f32_16x16x32_bf16 v[132:135], a[56:59], a[128:131], v[132:135]\n"
"v_mfma_f32_16x16x32_bf16 v[136:139], a[56:59], a[136:139], v[136:139]\n"
"v_mfma_f32_16x16x32_bf16 v[136:139], a[60:63], a[140:143], v[136:139]\n"
"v_mfma_f32_16x16x32_bf16 v[108:111], a[52:55], a[140:143], v[108:111]\n"
"v_mfma_f32_16x16x32_bf16 v[108:111], a[48:51], a[136:139], v[108:111]\n"
"v_mfma_f32_16x16x32_bf16 v[112:115], a[48:51], a[144:147], v[112:115]\n"
"v_mfma_f32_16x16x32_bf16 v[112:115], a[52:55], a[148:151], v[112:115]\n"
"global_load_dwordx4 a[44:47], v0, s[34:35] offset:-1024 sc1\n"
"v_mfma_f32_16x16x32_bf16 v[140:143], a[60:63], a[148:151], v[140:143]\n"
"v_mfma_f32_16x16x32_bf16 v[140:143], a[56:59], a[144:147], v[140:143]\n"
"v_mfma_f32_16x16x32_bf16 a[12:15], a[56:59], a[152:155], a[12:15]\n"
"v_mfma_f32_16x16x32_bf16 a[12:15], a[60:63], a[156:159], a[12:15]\n"
"v_mfma_f32_16x16x32_bf16 a[8:11], a[52:55], a[156:159], a[8:11]\n"
"v_mfma_f32_16x16x32_bf16 a[8:11], a[48:51], a[152:155], a[8:11]\n"
"s_waitcnt vmcnt(11)\n"
"v_mfma_f32_16x16x32_bf16 v[144:147], a[64:67], a[96:99], v[144:147]\n"
"buffer_load_dwordx4 v0, s[48:51], s75 offen offset:3072 sc1 lds\n"
"v_mfma_f32_16x16x32_bf16 v[144:147], a[68:71], a[100:103], v[144:147]\n"
"global_load_dwordx4 a[48:51], v0, s[34:35] offset:0 sc1\n"
"v_mfma_f32_16x16x32_bf16 v[172:175], a[76:79], a[100:103], v[172:175]\n"
"v_mfma_f32_16x16x32_bf16 v[172:175], a[72:75], a[96:99], v[172:175]\n"
"v_mfma_f32_16x16x32_bf16 v[176:179], a[72:75], a[104:107], v[176:179]\n"
"v_mfma_f32_16x16x32_bf16 v[176:179], a[76:79], a[108:111], v[176:179]\n"
"v_mfma_f32_16x16x32_bf16 v[148:151], a[68:71], a[108:111], v[148:151]\n"
"v_mfma_f32_16x16x32_bf16 v[148:151], a[64:67], a[104:107], v[148:151]\n"
"v_mfma_f32_16x16x32_bf16 v[152:155], a[64:67], a[112:115], v[152:155]\n"
"v_mfma_f32_16x16x32_bf16 v[152:155], a[68:71], a[116:119], v[152:155]\n"
"global_load_dwordx4 a[52:55], v0, s[34:35] offset:1024 sc1\n"
"v_mfma_f32_16x16x32_bf16 v[180:183], a[76:79], a[116:119], v[180:183]\n"
"v_mfma_f32_16x16x32_bf16 v[180:183], a[72:75], a[112:115], v[180:183]\n"
"v_mfma_f32_16x16x32_bf16 v[184:187], a[72:75], a[120:123], v[184:187]\n"
"v_mfma_f32_16x16x32_bf16 v[184:187], a[76:79], a[124:127], v[184:187]\n"
"v_mfma_f32_16x16x32_bf16 v[156:159], a[68:71], a[124:127], v[156:159]\n"
"v_mfma_f32_16x16x32_bf16 v[156:159], a[64:67], a[120:123], v[156:159]\n"
"s_add_u32 m0, s64, 4096\n"
"v_mfma_f32_16x16x32_bf16 v[160:163], a[64:67], a[128:131], v[160:163]\n"
"v_mfma_f32_16x16x32_bf16 v[160:163], a[68:71], a[132:135], v[160:163]\n"
"global_load_dwordx4 a[56:59], v0, s[34:35] offset:2048 sc1\n"
"v_mfma_f32_16x16x32_bf16 v[188:191], a[76:79], a[132:135], v[188:191]\n"
"v_mfma_f32_16x16x32_bf16 v[188:191], a[72:75], a[128:131], v[188:191]\n"
"v_mfma_f32_16x16x32_bf16 v[192:195], a[72:75], a[136:139], v[192:195]\n"
"v_mfma_f32_16x16x32_bf16 v[192:195], a[76:79], a[140:143], v[192:195]\n"
"v_mfma_f32_16x16x32_bf16 v[164:167], a[68:71], a[140:143], v[164:167]\n"
"v_mfma_f32_16x16x32_bf16 v[164:167], a[64:67], a[136:139], v[164:167]\n"
"v_mfma_f32_16x16x32_bf16 v[168:171], a[64:67], a[144:147], v[168:171]\n"
"v_mfma_f32_16x16x32_bf16 v[168:171], a[68:71], a[148:151], v[168:171]\n"
"global_load_dwordx4 a[60:63], v0, s[34:35] offset:3072 sc1\n"
"v_mfma_f32_16x16x32_bf16 v[196:199], a[76:79], a[148:151], v[196:199]\n"
"v_mfma_f32_16x16x32_bf16 v[196:199], a[72:75], a[144:147], v[196:199]\n"
"v_mfma_f32_16x16x32_bf16 a[20:23], a[72:75], a[152:155], a[20:23]\n"
"s_add_u32 s34, s34, 8192\n"
"v_mfma_f32_16x16x32_bf16 a[20:23], a[76:79], a[156:159], a[20:23]\n"
"v_mfma_f32_16x16x32_bf16 a[16:19], a[68:71], a[156:159], a[16:19]\n"
"buffer_load_dwordx4 v1, s[48:51], s75 offen offset:0 sc1 lds\n"
"v_mfma_f32_16x16x32_bf16 a[16:19], a[64:67], a[152:155], a[16:19]\n"
"s_waitcnt vmcnt(12)\n"
"v_mfma_f32_16x16x32_bf16 v[200:203], a[80:83], a[96:99], v[200:203]\n"
"v_mfma_f32_16x16x32_bf16 v[200:203], a[84:87], a[100:103], v[200:203]\n"
"global_load_dwordx4 a[64:67], v0, s[34:35] offset:-4096 sc1\n"
"s_add_u32 s65, s65, 1\n"
"v_mfma_f32_16x16x32_bf16 v[228:231], a[92:95], a[100:103], v[228:231]\n"
"ds_read_b128 a[100:103], v4 offset:17408\n"
"v_mfma_f32_16x16x32_bf16 v[228:231], a[88:91], a[96:99], v[228:231]\n"
"ds_read_b128 a[96:99], v4 offset:16384\n"
"v_mov_b32 v8, s65\n"
"v_mfma_f32_16x16x32_bf16 v[232:235], a[88:91], a[104:107], v[232:235]\n"
"v_mov_b32 v9, s60\n"
"v_mfma_f32_16x16x32_bf16 v[232:235], a[92:95], a[108:111], v[232:235]\n"
"v_mfma_f32_16x16x32_bf16 v[204:207], a[84:87], a[108:111], v[204:207]\n"
"buffer_store_dwordx2 v[8:9], off, s[56:59], %13 sc1\n"
"ds_read_b128 a[108:111], v4 offset:19456\n"
"v_mfma_f32_16x16x32_bf16 v[204:207], a[80:83], a[104:107], v[204:207]\n"
"ds_read_b128 a[104:107], v4 offset:18432\n"
"v_mfma_f32_16x16x32_bf16 v[208:211], a[80:83], a[112:115], v[208:211]\n"
"v_mfma_f32_16x16x32_bf16 v[208:211], a[84:87], a[116:119], v[208:211]\n"
"global_load_dwordx4 a[68:71], v0, s[34:35] offset:-3072 sc1\n"
"v_mfma_f32_16x16x32_bf16 v[236:239], a[92:95], a[116:119], v[236:239]\n"
"ds_read_b128 a[116:119], v4 offset:21504\n"
"v_mfma_f32_16x16x32_bf16 v[236:239], a[88:91], a[112:115], v[236:239]\n"
"ds_read_b128 a[112:115], v4 offset:20480\n"
"v_mfma_f32_16x16x32_bf16 v[240:243], a[88:91], a[120:123], v[240:243]\n"
"v_mfma_f32_16x16x32_bf16 v[240:243], a[92:95], a[124:127], v[240:243]\n"
"buffer_load_dwordx4 v1, s[48:51], s75 offen offset:1024 sc1 lds\n"
"v_mfma_f32_16x16x32_bf16 v[212:215], a[84:87], a[124:127], v[212:215]\n"
"ds_read_b128 a[124:127], v4 offset:23552\n"
"v_mfma_f32_16x16x32_bf16 v[212:215], a[80:83], a[120:123], v[212:215]\n"
"ds_read_b128 a[120:123], v4 offset:22528\n"
"v_mfma_f32_16x16x32_bf16 v[216:219], a[80:83], a[128:131], v[216:219]\n"
"v_mfma_f32_16x16x32_bf16 v[216:219], a[84:87], a[132:135], v[216:219]\n"
"global_load_dwordx4 a[72:75], v0, s[34:35] offset:-2048 sc1\n"
"v_mfma_f32_16x16x32_bf16 v[244:247], a[92:95], a[132:135], v[244:247]\n"
"ds_read_b128 a[132:135], v4 offset:25600\n"
"v_mfma_f32_16x16x32_bf16 v[244:247], a[88:91], a[128:131], v[244:247]\n"
"ds_read_b128 a[128:131], v4 offset:24576\n"
"v_mfma_f32_16x16x32_bf16 v[248:251], a[88:91], a[136:139], v[248:251]\n"
"v_mfma_f32_16x16x32_bf16 v[248:251], a[92:95], a[140:143], v[248:251]\n"
"v_mfma_f32_16x16x32_bf16 v[220:223], a[84:87], a[140:143], v[220:223]\n"
"ds_read_b128 a[140:143], v4 offset:27648\n"
"v_mfma_f32_16x16x32_bf16 v[220:223], a[80:83], a[136:139], v[220:223]\n"
"ds_read_b128 a[136:139], v4 offset:26624\n"
"v_mfma_f32_16x16x32_bf16 v[224:227], a[80:83], a[144:147], v[224:227]\n"
"v_mfma_f32_16x16x32_bf16 v[224:227], a[84:87], a[148:151], v[224:227]\n"
"global_load_dwordx4 a[76:79], v0, s[34:35] offset:-1024 sc1\n"
"v_mfma_f32_16x16x32_bf16 v[252:255], a[92:95], a[148:151], v[252:255]\n"
"ds_read_b128 a[148:151], v4 offset:29696\n"
"v_mfma_f32_16x16x32_bf16 v[252:255], a[88:91], a[144:147], v[252:255]\n"
"ds_read_b128 a[144:147], v4 offset:28672\n"
"v_mfma_f32_16x16x32_bf16 a[28:31], a[88:91], a[152:155], a[28:31]\n"
"buffer_load_dwordx4 v1, s[48:51], s75 offen offset:2048 sc1 lds\n"
"v_mfma_f32_16x16x32_bf16 a[28:31], a[92:95], a[156:159], a[28:31]\n"
"v_mfma_f32_16x16x32_bf16 a[24:27], a[84:87], a[156:159], a[24:27]\n"
"ds_read_b128 a[156:159], v4 offset:31744\n"
"v_mfma_f32_16x16x32_bf16 a[24:27], a[80:83], a[152:155], a[24:27]\n"
"s_waitcnt vmcnt(13) lgkmcnt(11)\n"
"ds_read_b128 a[152:155], v4 offset:30720\n"
"v_mfma_f32_16x16x32_bf16 v[32:35], a[32:35], a[96:99], v[32:35]\n"
"v_mfma_f32_16x16x32_bf16 v[32:35], a[36:39], a[100:103], v[32:35]\n"
"global_load_dwordx4 a[80:83], v0, s[34:35] offset:0 sc1\n"
"v_mfma_f32_16x16x32_bf16 v[60:63], a[44:47], a[100:103], v[60:63]\n"
"v_mfma_f32_16x16x32_bf16 v[60:63], a[40:43], a[96:99], v[60:63]\n"
"v_mfma_f32_16x16x32_bf16 v[64:67], a[40:43], a[104:107], v[64:67]\n"
"v_mfma_f32_16x16x32_bf16 v[64:67], a[44:47], a[108:111], v[64:67]\n"
"v_mfma_f32_16x16x32_bf16 v[36:39], a[36:39], a[108:111], v[36:39]\n"
"v_mfma_f32_16x16x32_bf16 v[36:39], a[32:35], a[104:107], v[36:39]\n"
"s_waitcnt lgkmcnt(8)\n"
"v_mfma_f32_16x16x32_bf16 v[40:43], a[32:35], a[112:115], v[40:43]\n"
"v_mfma_f32_16x16x32_bf16 v[40:43], a[36:39], a[116:119], v[40:43]\n"
"global_load_dwordx4 a[84:87], v0, s[34:35] offset:1024 sc1\n"
"v_mfma_f32_16x16x32_bf16 v[68:71], a[44:47], a[116:119], v[68:71]\n"
"buffer_load_dwordx4 v1, s[48:51], s75 offen offset:3072 sc1 lds\n"
"v_mfma_f32_16x16x32_bf16 v[68:71], a[40:43], a[112:115], v[68:71]\n"
"v_mfma_f32_16x16x32_bf16 v[72:75], a[40:43], a[120:123], v[72:75]\n"
"v_mfma_f32_16x16x32_bf16 v[72:75], a[44:47], a[124:127], v[72:75]\n"
"v_mfma_f32_16x16x32_bf16 v[44:47], a[36:39], a[124:127], v[44:47]\n"
"v_mfma_f32_16x16x32_bf16 v[44:47], a[32:35], a[120:123], v[44:47]\n"
"s_waitcnt lgkmcnt(4)\n"
"v_mfma_f32_16x16x32_bf16 v[48:51], a[32:35], a[128:131], v[48:51]\n"
"v_mfma_f32_16x16x32_bf16 v[48:51], a[36:39], a[132:135], v[48:51]\n"
"global_load_dwordx4 a[88:91], v0, s[34:35] offset:2048 sc1\n"
"v_mfma_f32_16x16x32_bf16 v[76:79], a[44:47], a[132:135], v[76:79]\n"
"v_mfma_f32_16x16x32_bf16 v[76:79], a[40:43], a[128:131], v[76:79]\n"
"v_mfma_f32_16x16x32_bf16 v[80:83], a[40:43], a[136:139], v[80:83]\n"
"v_mfma_f32_16x16x32_bf16 v[80:83], a[44:47], a[140:143], v[80:83]\n"
"v_mfma_f32_16x16x32_bf16 v[52:55], a[36:39], a[140:143], v[52:55]\n"
"v_mfma_f32_16x16x32_bf16 v[52:55], a[32:35], a[136:139], v[52:55]\n"
"s_waitcnt lgkmcnt(0)\n"
"v_mfma_f32_16x16x32_bf16 v[56:59], a[32:35], a[144:147], v[56:59]\n"
"v_mfma_f32_16x16x32_bf16 v[56:59], a[36:39], a[148:151], v[56:59]\n"
"s_add_u32 m0, s64, 8192\n"
"global_load_dwordx4 a[92:95], v0, s[34:35] offset:3072 sc1\n"
"v_mfma_f32_16x16x32_bf16 v[84:87], a[44:47], a[148:151], v[84:87]\n"
"v_mfma_f32_16x16x32_bf16 v[84:87], a[40:43], a[144:147], v[84:87]\n"
"v_mfma_f32_16x16x32_bf16 a[4:7], a[40:43], a[152:155], a[4:7]\n"
"s_add_u32 s34, s34, 8192\n"
"v_mfma_f32_16x16x32_bf16 a[4:7], a[44:47], a[156:159], a[4:7]\n"
"v_mfma_f32_16x16x32_bf16 a[0:3], a[36:39], a[156:159], a[0:3]\n"
"v_mfma_f32_16x16x32_bf16 a[0:3], a[32:35], a[152:155], a[0:3]\n"
"s_waitcnt vmcnt(13)\n"
"v_mfma_f32_16x16x32_bf16 v[88:91], a[48:51], a[96:99], v[88:91]\n"
"v_mfma_f32_16x16x32_bf16 v[88:91], a[52:55], a[100:103], v[88:91]\n"
"global_load_dwordx4 a[32:35], v0, s[34:35] offset:-4096 sc1\n"
"v_mfma_f32_16x16x32_bf16 v[116:119], a[60:63], a[100:103], v[116:119]\n"
"v_mfma_f32_16x16x32_bf16 v[116:119], a[56:59], a[96:99], v[116:119]\n"
"v_mfma_f32_16x16x32_bf16 v[120:123], a[56:59], a[104:107], v[120:123]\n"
"v_mfma_f32_16x16x32_bf16 v[120:123], a[60:63], a[108:111], v[120:123]\n"
"v_mfma_f32_16x16x32_bf16 v[92:95], a[52:55], a[108:111], v[92:95]\n"
"v_mfma_f32_16x16x32_bf16 v[92:95], a[48:51], a[104:107], v[92:95]\n"
"v_mfma_f32_16x16x32_bf16 v[96:99], a[48:51], a[112:115], v[96:99]\n"
"buffer_load_dwordx4 v0, s[48:51], s69 offen offset:0 sc1 lds\n"
"v_mfma_f32_16x16x32_bf16 v[96:99], a[52:55], a[116:119], v[96:99]\n"
"global_load_dwordx4 a[36:39], v0, s[34:35] offset:-3072 sc1\n"
"v_mfma_f32_16x16x32_bf16 v[124:127], a[60:63], a[116:119], v[124:127]\n"
"v_mfma_f32_16x16x32_bf16 v[124:127], a[56:59], a[112:115], v[124:127]\n"
"v_mfma_f32_16x16x32_bf16 v[128:131], a[56:59], a[120:123], v[128:131]\n"
"v_mfma_f32_16x16x32_bf16 v[128:131], a[60:63], a[124:127], v[128:131]\n"
"v_mfma_f32_16x16x32_bf16 v[100:103], a[52:55], a[124:127], v[100:103]\n"
"v_mfma_f32_16x16x32_bf16 v[100:103], a[48:51], a[120:123], v[100:103]\n"
"v_mfma_f32_16x16x32_bf16 v[104:107], a[48:51], a[128:131], v[104:107]\n"
"v_mfma_f32_16x16x32_bf16 v[104:107], a[52:55], a[132:135], v[104:107]\n"
"global_load_dwordx4 a[40:43], v0, s[34:35] offset:-2048 sc1\n"
"v_mfma_f32_16x16x32_bf16 v[132:135], a[60:63], a[132:135], v[132:135]\n"
"v_mfma_f32_16x16x32_bf16 v[132:135], a[56:59], a[128:131], v[132:135]\n"
"v_mfma_f32_16x16x32_bf16 v[136:139], a[56:59], a[136:139], v[136:139]\n"
"v_mfma_f32_16x16x32_bf16 v[136:139], a[60:63], a[140:143], v[136:139]\n"
"v_mfma_f32_16x16x32_bf16 v[108:111], a[52:55], a[140:143], v[108:111]\n"
"buffer_load_dwordx4 v0, s[48:51], s69 offen offset:1024 sc1 lds\n"
"v_mfma_f32_16x16x32_bf16 v[108:111], a[48:51], a[136:139], v[108:111]\n"
"v_mfma_f32_16x16x32_bf16 v[112:115], a[48:51], a[144:147], v[112:115]\n"
"v_mfma_f32_16x16x32_bf16 v[112:115], a[52:55], a[148:151], v[112:115]\n"
"global_load_dwordx4 a[44:47], v0, s[34:35] offset:-1024 sc1\n"
"v_mfma_f32_16x16x32_bf16 v[140:143], a[60:63], a[148:151], v[140:143]\n"
"v_mfma_f32_16x16x32_bf16 v[140:143], a[56:59], a[144:147], v[140:143]\n"
"v_mfma_f32_16x16x32_bf16 a[12:15], a[56:59], a[152:155], a[12:15]\n"
"v_mfma_f32_16x16x32_bf16 a[12:15], a[60:63], a[156:159], a[12:15]\n"
"v_mfma_f32_16x16x32_bf16 a[8:11], a[52:55], a[156:159], a[8:11]\n"
"v_mfma_f32_16x16x32_bf16 a[8:11], a[48:51], a[152:155], a[8:11]\n"
"s_waitcnt vmcnt(12)\n"
"v_mfma_f32_16x16x32_bf16 v[144:147], a[64:67], a[96:99], v[144:147]\n"
"v_mfma_f32_16x16x32_bf16 v[144:147], a[68:71], a[100:103], v[144:147]\n"
"global_load_dwordx4 a[48:51], v0, s[34:35] offset:0 sc1\n"
"v_mfma_f32_16x16x32_bf16 v[172:175], a[76:79], a[100:103], v[172:175]\n"
"v_mfma_f32_16x16x32_bf16 v[172:175], a[72:75], a[96:99], v[172:175]\n"
"v_mfma_f32_16x16x32_bf16 v[176:179], a[72:75], a[104:107], v[176:179]\n"
"v_mfma_f32_16x16x32_bf16 v[176:179], a[76:79], a[108:111], v[176:179]\n"
"buffer_load_dwordx4 v0, s[48:51], s69 offen offset:2048 sc1 lds\n"
"v_mfma_f32_16x16x32_bf16 v[148:151], a[68:71], a[108:111], v[148:151]\n"
"v_mfma_f32_16x16x32_bf16 v[148:151], a[64:67], a[104:107], v[148:151]\n"
"v_mfma_f32_16x16x32_bf16 v[152:155], a[64:67], a[112:115], v[152:155]\n"
"v_lshlrev_b32 v16, 16, v18\n"
"v_mfma_f32_16x16x32_bf16 v[152:155], a[68:71], a[116:119], v[152:155]\n"
"global_load_dwordx4 a[52:55], v0, s[34:35] offset:1024 sc1\n"
"v_and_b32 v17, s63, v18\n"
"v_mfma_f32_16x16x32_bf16 v[180:183], a[76:79], a[116:119], v[180:183]\n"
"v_lshlrev_b32 v18, 16, v19\n"
"v_mfma_f32_16x16x32_bf16 v[180:183], a[72:75], a[112:115], v[180:183]\n"
"v_and_b32 v19, s63, v19\n"
"v_mfma_f32_16x16x32_bf16 v[184:187], a[72:75], a[120:123], v[184:187]\n"
"v_add_f32 v32, v32, v16\n"
"v_mfma_f32_16x16x32_bf16 v[184:187], a[76:79], a[124:127], v[184:187]\n"
"v_add_f32 v33, v33, v17\n"
"v_add_f32 v34, v34, v18\n"
"v_mfma_f32_16x16x32_bf16 v[156:159], a[68:71], a[124:127], v[156:159]\n"
"v_add_f32 v35, v35, v19\n"
"v_mfma_f32_16x16x32_bf16 v[156:159], a[64:67], a[120:123], v[156:159]\n"
"v_add_f32 v36, v36, v16\n"
"v_mfma_f32_16x16x32_bf16 v[160:163], a[64:67], a[128:131], v[160:163]\n"
"v_add_f32 v37, v37, v17\n"
"v_mfma_f32_16x16x32_bf16 v[160:163], a[68:71], a[132:135], v[160:163]\n"
"global_load_dwordx4 a[56:59], v0, s[34:35] offset:2048 sc1\n"
"v_add_f32 v38, v38, v18\n"
"v_mfma_f32_16x16x32_bf16 v[188:191], a[76:79], a[132:135], v[188:191]\n"
"v_add_f32 v39, v39, v19\n"
"v_mfma_f32_16x16x32_bf16 v[188:191], a[72:75], a[128:131], v[188:191]\n"
"v_add_f32 v40, v40, v16\n"
"v_add_f32 v41, v41, v17\n"
"v_mfma_f32_16x16x32_bf16 v[192:195], a[72:75], a[136:139], v[192:195]\n"
"buffer_load_dwordx4 v0, s[48:51], s69 offen offset:3072 sc1 lds\n"
"v_mfma_f32_16x16x32_bf16 v[192:195], a[76:79], a[140:143], v[192:195]\n"
"v_add_f32 v42, v42, v18\n"
"v_mfma_f32_16x16x32_bf16 v[164:167], a[68:71], a[140:143], v[164:167]\n"
"v_add_f32 v43, v43, v19\n"
"v_mfma_f32_16x16x32_bf16 v[164:167], a[64:67], a[136:139], v[164:167]\n"
"v_add_f32 v44, v44, v16\n"
"v_add_f32 v45, v45, v17\n"
"v_mfma_f32_16x16x32_bf16 v[168:171], a[64:67], a[144:147], v[168:171]\n"
"v_add_f32 v46, v46, v18\n"
"v_mfma_f32_16x16x32_bf16 v[168:171], a[68:71], a[148:151], v[168:171]\n"
"global_load_dwordx4 a[60:63], v0, s[34:35] offset:3072 sc1\n"
"v_add_f32 v47, v47, v19\n"
"v_mfma_f32_16x16x32_bf16 v[196:199], a[76:79], a[148:151], v[196:199]\n"
"v_add_f32 v48, v48, v16\n"
"v_mfma_f32_16x16x32_bf16 v[196:199], a[72:75], a[144:147], v[196:199]\n"
"v_add_f32 v49, v49, v17\n"
"v_mfma_f32_16x16x32_bf16 a[20:23], a[72:75], a[152:155], a[20:23]\n"
"s_add_u32 s34, s34, 8192\n"
"v_add_f32 v50, v50, v18\n"
"v_mfma_f32_16x16x32_bf16 a[20:23], a[76:79], a[156:159], a[20:23]\n"
"v_add_f32 v51, v51, v19\n"
"v_mfma_f32_16x16x32_bf16 a[16:19], a[68:71], a[156:159], a[16:19]\n"
"v_add_f32 v52, v52, v16\n"
"v_mfma_f32_16x16x32_bf16 a[16:19], a[64:67], a[152:155], a[16:19]\n"
"s_waitcnt vmcnt(12)\n"
"v_add_f32 v53, v53, v17\n"
"v_mfma_f32_16x16x32_bf16 v[200:203], a[80:83], a[96:99], v[200:203]\n"
"v_add_f32 v54, v54, v18\n"
"v_add_f32 v55, v55, v19\n"
"v_mfma_f32_16x16x32_bf16 v[200:203], a[84:87], a[100:103], v[200:203]\n"
"global_load_dwordx4 a[64:67], v0, s[34:35] offset:-4096 sc1\n"
"v_add_f32 v56, v56, v16\n"
"v_mfma_f32_16x16x32_bf16 v[228:231], a[92:95], a[100:103], v[228:231]\n"
"s_add_u32 m0, s64, 12288\n"
"ds_read_b128 a[100:103], v5 offset:17408\n"
"v_add_f32 v57, v57, v17\n"
"v_mfma_f32_16x16x32_bf16 v[228:231], a[88:91], a[96:99], v[228:231]\n"
"ds_read_b128 a[96:99], v5 offset:16384\n"
"v_add_f32 v58, v58, v18\n"
"v_mfma_f32_16x16x32_bf16 v[232:235], a[88:91], a[104:107], v[232:235]\n"
"v_add_f32 v59, v59, v19\n"
"v_mfma_f32_16x16x32_bf16 v[232:235], a[92:95], a[108:111], v[232:235]\n"
"v_accvgpr_read_b32 v12, a0\n"
"v_mfma_f32_16x16x32_bf16 v[204:207], a[84:87], a[108:111], v[204:207]\n"
"ds_read_b128 a[108:111], v5 offset:19456\n"
"v_accvgpr_read_b32 v13, a1\n"
"v_mfma_f32_16x16x32_bf16 v[204:207], a[80:83], a[104:107], v[204:207]\n"
"ds_read_b128 a[104:107], v5 offset:18432\n"
"v_accvgpr_read_b32 v14, a2\n"
"v_mfma_f32_16x16x32_bf16 v[208:211], a[80:83], a[112:115], v[208:211]\n"
"v_accvgpr_read_b32 v15, a3\n"
"v_mfma_f32_16x16x32_bf16 v[208:211], a[84:87], a[116:119], v[208:211]\n"
"global_load_dwordx4 a[68:71], v0, s[34:35] offset:-3072 sc1\n"
"v_add_f32 v12, v12, v16\n"
"v_mfma_f32_16x16x32_bf16 v[236:239], a[92:95], a[116:119], v[236:239]\n"
"ds_read_b128 a[116:119], v5 offset:21504\n"
"v_add_f32 v13, v13, v17\n"
"v_mfma_f32_16x16x32_bf16 v[236:239], a[88:91], a[112:115], v[236:239]\n"
"ds_read_b128 a[112:115], v5 offset:20480\n"
"v_add_f32 v14, v14, v18\n"
"v_mfma_f32_16x16x32_bf16 v[240:243], a[88:91], a[120:123], v[240:243]\n"
"v_add_f32 v15, v15, v19\n"
"v_accvgpr_write_b32 a0, v12\n"
"v_mfma_f32_16x16x32_bf16 v[240:243], a[92:95], a[124:127], v[240:243]\n"
"v_accvgpr_write_b32 a1, v13\n"
"v_mfma_f32_16x16x32_bf16 v[212:215], a[84:87], a[124:127], v[212:215]\n"
"ds_read_b128 a[124:127], v5 offset:23552\n"
"v_accvgpr_write_b32 a2, v14\n"
"v_mfma_f32_16x16x32_bf16 v[212:215], a[80:83], a[120:123], v[212:215]\n"
"ds_read_b128 a[120:123], v5 offset:22528\n"
"v_accvgpr_write_b32 a3, v15\n"
"v_mfma_f32_16x16x32_bf16 v[216:219], a[80:83], a[128:131], v[216:219]\n"
"buffer_load_dwordx4 v1, s[48:51], s69 offen offset:0 sc1 lds\n"
"v_mfma_f32_16x16x32_bf16 v[216:219], a[84:87], a[132:135], v[216:219]\n"
"global_load_dwordx4 a[72:75], v0, s[34:35] offset:-2048 sc1\n"
"v_lshrrev_b32 v16, 4, v0\n"
"v_mfma_f32_16x16x32_bf16 v[244:247], a[92:95], a[132:135], v[244:247]\n"
"ds_read_b128 a[132:135], v5 offset:25600\n"
"v_and_b32 v16, 0x30, v16\n"
"v_mfma_f32_16x16x32_bf16 v[244:247], a[88:91], a[128:131], v[244:247]\n"
"ds_read_b128 a[128:131], v5 offset:24576\n"
"buffer_load_dwordx2 v[18:19], v16, s[44:47], s61 offen offset:64\n"
"v_mfma_f32_16x16x32_bf16 v[248:251], a[88:91], a[136:139], v[248:251]\n"
"s_getpc_b64 s[96:97]\n"
"L_c3_:\n"
"s_add_u32 s96, s96, L_last-L_c3_\n"
"s_addc_u32 s97, s97, 0\n"
"v_mfma_f32_16x16x32_bf16 v[248:251], a[92:95], a[140:143], v[248:251]\n"
"s_lshr_b32 s66, %26, 3\n"
"v_mfma_f32_16x16x32_bf16 v[220:223], a[84:87], a[140:143], v[220:223]\n"
"s_lshl2_add_u32 s66, s66, %16\n"
"ds_read_b128 a[140:143], v5 offset:27648\n"
"v_mfma_f32_16x16x32_bf16 v[220:223], a[80:83], a[136:139], v[220:223]\n"
"ds_read_b128 a[136:139], v5 offset:26624\n"
"v_mfma_f32_16x16x32_bf16 v[224:227], a[80:83], a[144:147], v[224:227]\n"
"s_mul_i32 s66, s66, 256\n"
"v_mfma_f32_16x16x32_bf16 v[224:227], a[84:87], a[148:151], v[224:227]\n"
"global_load_dwordx4 a[76:79], v0, s[34:35] offset:-1024 sc1\n"
"v_mfma_f32_16x16x32_bf16 v[252:255], a[92:95], a[148:151], v[252:255]\n"
"ds_read_b128 a[148:151], v5 offset:29696\n"
"v_mfma_f32_16x16x32_bf16 v[252:255], a[88:91], a[144:147], v[252:255]\n"
"s_min_u32 s66, s66, L_headF-L_last-256\n"
"ds_read_b128 a[144:147], v5 offset:28672\n"
"v_mfma_f32_16x16x32_bf16 a[28:31], a[88:91], a[152:155], a[28:31]\n"
"v_mfma_f32_16x16x32_bf16 a[28:31], a[92:95], a[156:159], a[28:31]\n"
"v_min_u32 v11, 16, v0\n"
"v_mfma_f32_16x16x32_bf16 a[24:27], a[84:87], a[156:159], a[24:27]\n"
"ds_read_b128 a[156:159], v5 offset:31744\n"
"v_mfma_f32_16x16x32_bf16 a[24:27], a[80:83], a[152:155], a[24:27]\n"
"s_waitcnt vmcnt(12) lgkmcnt(11)\n"
"ds_read_b128 a[152:155], v5 offset:30720\n"
"v_mfma_f32_16x16x32_bf16 v[32:35], a[32:35], a[96:99], v[32:35]\n"
"buffer_load_dwordx4 v1, s[48:51], s69 offen offset:1024 sc1 lds\n"
"v_mfma_f32_16x16x32_bf16 v[32:35], a[36:39], a[100:103], v[32:35]\n"
"global_load_dwordx4 a[80:83], v0, s[34:35] offset:0 sc1\n"
"v_mfma_f32_16x16x32_bf16 v[60:63], a[44:47], a[100:103], v[60:63]\n"
"v_lshl_add_u32 v11, v11, 3, s66\n"
"v_mfma_f32_16x16x32_bf16 v[60:63], a[40:43], a[96:99], v[60:63]\n"
"global_load_dword v10, v11, s[96:97]\n"
"v_mfma_f32_16x16x32_bf16 v[64:67], a[40:43], a[104:107], v[64:67]\n"
"v_mfma_f32_16x16x32_bf16 v[64:67], a[44:47], a[108:111], v[64:67]\n"
"v_mfma_f32_16x16x32_bf16 v[36:39], a[36:39], a[108:111], v[36:39]\n"
"v_mfma_f32_16x16x32_bf16 v[36:39], a[32:35], a[104:107], v[36:39]\n"
"s_waitcnt lgkmcnt(8)\n"
"v_mfma_f32_16x16x32_bf16 v[40:43], a[32:35], a[112:115], v[40:43]\n"
"v_mfma_f32_16x16x32_bf16 v[40:43], a[36:39], a[116:119], v[40:43]\n"
"global_load_dwordx4 a[84:87], v0, s[34:35] offset:1024 sc1\n"
"v_mfma_f32_16x16x32_bf16 v[68:71], a[44:47], a[116:119], v[68:71]\n"
"v_mfma_f32_16x16x32_bf16 v[68:71], a[40:43], a[112:115], v[68:71]\n"
"v_mfma_f32_16x16x32_bf16 v[72:75], a[40:43], a[120:123], v[72:75]\n"
"v_mfma_f32_16x16x32_bf16 v[72:75], a[44:47], a[124:127], v[72:75]\n"
"v_mfma_f32_16x16x32_bf16 v[44:47], a[36:39], a[124:127], v[44:47]\n"
"buffer_load_dwordx4 v1, s[48:51], s69 offen offset:2048 sc1 lds\n"
"v_mfma_f32_16x16x32_bf16 v[44:47], a[32:35], a[120:123], v[44:47]\n"
"s_waitcnt lgkmcnt(4)\n"
"v_mfma_f32_16x16x32_bf16 v[48:51], a[32:35], a[128:131], v[48:51]\n"
"v_mfma_f32_16x16x32_bf16 v[48:51], a[36:39], a[132:135], v[48:51]\n"
"global_load_dwordx4 a[88:91], v0, s[34:35] offset:2048 sc1\n"
"v_mfma_f32_16x16x32_bf16 v[76:79], a[44:47], a[132:135], v[76:79]\n"
"v_mfma_f32_16x16x32_bf16 v[76:79], a[40:43], a[128:131], v[76:79]\n"
"v_mfma_f32_16x16x32_bf16 v[80:83], a[40:43], a[136:139], v[80:83]\n"
"v_mfma_f32_16x16x32_bf16 v[80:83], a[44:47], a[140:143], v[80:83]\n"
"v_mfma_f32_16x16x32_bf16 v[52:55], a[36:39], a[140:143], v[52:55]\n"
"v_mfma_f32_16x16x32_bf16 v[52:55], a[32:35], a[136:139], v[52:55]\n"
"s_waitcnt lgkmcnt(0)\n"
"v_mfma_f32_16x16x32_bf16 v[56:59], a[32:35], a[144:147], v[56:59]\n"
"v_mfma_f32_16x16x32_bf16 v[56:59], a[36:39], a[148:151], v[56:59]\n"
"global_load_dwordx4 a[92:95], v0, s[34:35] offset:3072 sc1\n"
"v_mfma_f32_16x16x32_bf16 v[84:87], a[44:47], a[148:151], v[84:87]\n"
"v_mfma_f32_16x16x32_bf16 v[84:87], a[40:43], a[144:147], v[84:87]\n"
"v_mfma_f32_16x16x32_bf16 a[4:7], a[40:43], a[152:155], a[4:7]\n"
"s_add_u32 s34, s34, 8192\n"
"v_mfma_f32_16x16x32_bf16 a[4:7], a[44:47], a[156:159], a[4:7]\n"
"buffer_load_dwordx4 v1, s[48:51], s69 offen offset:3072 sc1 lds\n"
"v_mfma_f32_16x16x32_bf16 a[0:3], a[36:39], a[156:159], a[0:3]\n"
"v_mfma_f32_16x16x32_bf16 a[0:3], a[32:35], a[152:155], a[0:3]\n"
"s_waitcnt vmcnt(14)\n"
"v_mfma_f32_16x16x32_bf16 v[88:91], a[48:51], a[96:99], v[88:91]\n"
"v_mfma_f32_16x16x32_bf16 v[88:91], a[52:55], a[100:103], v[88:91]\n"
"global_load_dwordx4 a[32:35], v0, s[34:35] offset:-4096 sc1\n"
"v_mfma_f32_16x16x32_bf16 v[116:119], a[60:63], a[100:103], v[116:119]\n"
"v_mfma_f32_16x16x32_bf16 v[116:119], a[56:59], a[96:99], v[116:119]\n"
"v_mfma_f32_16x16x32_bf16 v[120:123], a[56:59], a[104:107], v[120:123]\n"
"v_mfma_f32_16x16x32_bf16 v[120:123], a[60:63], a[108:111], v[120:123]\n"
"v_mfma_f32_16x16x32_bf16 v[92:95], a[52:55], a[108:111], v[92:95]\n"
"v_mfma_f32_16x16x32_bf16 v[92:95], a[48:51], a[104:107], v[92:95]\n"
"v_mfma_f32_16x16x32_bf16 v[96:99], a[48:51], a[112:115], v[96:99]\n"
"v_mfma_f32_16x16x32_bf16 v[96:99], a[52:55], a[116:119], v[96:99]\n"
"global_load_dwordx4 a[36:39], v0, s[34:35] offset:-3072 sc1\n"
"v_mfma_f32_16x16x32_bf16 v[124:127], a[60:63], a[116:119], v[124:127]\n"
"v_mfma_f32_16x16x32_bf16 v[124:127], a[56:59], a[112:115], v[124:127]\n"
"v_mfma_f32_16x16x32_bf16 v[128:131], a[56:59], a[120:123], v[128:131]\n"
"v_mfma_f32_16x16x32_bf16 v[128:131], a[60:63], a[124:127], v[128:131]\n"
"v_mfma_f32_16x16x32_bf16 v[100:103], a[52:55], a[124:127], v[100:103]\n"
"v_mfma_f32_16x16x32_bf16 v[100:103], a[48:51], a[120:123], v[100:103]\n"
"v_mfma_f32_16x16x32_bf16 v[104:107], a[48:51], a[128:131], v[104:107]\n"
"buffer_load_dwordx2 v[8:9], off, s[56:59], %14 sc1\n"
"v_mfma_f32_16x16x32_bf16 v[104:107], a[52:55], a[132:135], v[104:107]\n"
"global_load_dwordx4 a[40:43], v0, s[34:35] offset:-2048 sc1\n"
"v_mfma_f32_16x16x32_bf16 v[132:135], a[60:63], a[132:135], v[132:135]\n"
"v_mfma_f32_16x16x32_bf16 v[132:135], a[56:59], a[128:131], v[132:135]\n"
"v_mfma_f32_16x16x32_bf16 v[136:139], a[56:59], a[136:139], v[136:139]\n"
"v_mfma_f32_16x16x32_bf16 v[136:139], a[60:63], a[140:143], v[136:139]\n"
"v_mfma_f32_16x16x32_bf16 v[108:111], a[52:55], a[140:143], v[108:111]\n"
"v_mfma_f32_16x16x32_bf16 v[108:111], a[48:51], a[136:139], v[108:111]\n"
"v_mfma_f32_16x16x32_bf16 v[112:115], a[48:51], a[144:147], v[112:115]\n"
"v_mfma_f32_16x16x32_bf16 v[112:115], a[52:55], a[148:151], v[112:115]\n"
"global_load_dwordx4 a[44:47], v0, s[34:35] offset:-1024 sc1\n"
"v_mfma_f32_16x16x32_bf16 v[140:143], a[60:63], a[148:151], v[140:143]\n"
"v_mfma_f32_16x16x32_bf16 v[140:143], a[56:59], a[144:147], v[140:143]\n"
"v_mfma_f32_16x16x32_bf16 a[12:15], a[56:59], a[152:155], a[12:15]\n"
"v_mfma_f32_16x16x32_bf16 a[12:15], a[60:63], a[156:159], a[12:15]\n"
"v_mfma_f32_16x16x32_bf16 a[8:11], a[52:55], a[156:159], a[8:11]\n"
"v_mfma_f32_16x16x32_bf16 a[8:11], a[48:51], a[152:155], a[8:11]\n"
"s_waitcnt vmcnt(13)\n"
"v_mfma_f32_16x16x32_bf16 v[144:147], a[64:67], a[96:99], v[144:147]\n"
"v_mfma_f32_16x16x32_bf16 v[144:147], a[68:71], a[100:103], v[144:147]\n"
"global_load_dwordx4 a[48:51], v0, s[34:35] offset:0 sc1\n"
"v_mfma_f32_16x16x32_bf16 v[172:175], a[76:79], a[100:103], v[172:175]\n"
"v_mfma_f32_16x16x32_bf16 v[172:175], a[72:75], a[96:99], v[172:175]\n"
"v_mfma_f32_16x16x32_bf16 v[176:179], a[72:75], a[104:107], v[176:179]\n"
"v_mfma_f32_16x16x32_bf16 v[176:179], a[76:79], a[108:111], v[176:179]\n"
"v_mfma_f32_16x16x32_bf16 v[148:151], a[68:71], a[108:111], v[148:151]\n"
"v_mfma_f32_16x16x32_bf16 v[148:151], a[64:67], a[104:107], v[148:151]\n"
"v_mfma_f32_16x16x32_bf16 v[152:155], a[64:67], a[112:115], v[152:155]\n"
"v_lshlrev_b32 v20, 16, v22\n"
"v_mfma_f32_16x16x32_bf16 v[152:155], a[68:71], a[116:119], v[152:155]\n"
"global_load_dwordx4 a[52:55], v0, s[34:35] offset:1024 sc1\n"
"v_and_b32 v21, s63, v22\n"
"v_mfma_f32_16x16x32_bf16 v[180:183], a[76:79], a[116:119], v[180:183]\n"
"v_lshlrev_b32 v22, 16, v23\n"
"v_mfma_f32_16x16x32_bf16 v[180:183], a[72:75], a[112:115], v[180:183]\n"
"v_and_b32 v23, s63, v23\n"
"v_mfma_f32_16x16x32_bf16 v[184:187], a[72:75], a[120:123], v[184:187]\n"
"v_add_f32 v60, v60, v20\n"
"v_mfma_f32_16x16x32_bf16 v[184:187], a[76:79], a[124:127], v[184:187]\n"
"v_add_f32 v61, v61, v21\n"
"v_add_f32 v62, v62, v22\n"
"v_mfma_f32_16x16x32_bf16 v[156:159], a[68:71], a[124:127], v[156:159]\n"
"v_add_f32 v63, v63, v23\n"
"v_mfma_f32_16x16x32_bf16 v[156:159], a[64:67], a[120:123], v[156:159]\n"
"v_add_f32 v64, v64, v20\n"
"v_mfma_f32_16x16x32_bf16 v[160:163], a[64:67], a[128:131], v[160:163]\n"
"v_add_f32 v65, v65, v21\n"
"v_mfma_f32_16x16x32_bf16 v[160:163], a[68:71], a[132:135], v[160:163]\n"
"global_load_dwordx4 a[56:59], v0, s[34:35] offset:2048 sc1\n"
"v_add_f32 v66, v66, v22\n"
"v_mfma_f32_16x16x32_bf16 v[188:191], a[76:79], a[132:135], v[188:191]\n"
"v_add_f32 v67, v67, v23\n"
"v_mfma_f32_16x16x32_bf16 v[188:191], a[72:75], a[128:131], v[188:191]\n"
"v_add_f32 v68, v68, v20\n"
"v_mfma_f32_16x16x32_bf16 v[192:195], a[72:75], a[136:139], v[192:195]\n"
"v_add_f32 v69, v69, v21\n"
"v_mfma_f32_16x16x32_bf16 v[192:195], a[76:79], a[140:143], v[192:195]\n"
"v_add_f32 v70, v70, v22\n"
"v_mfma_f32_16x16x32_bf16 v[164:167], a[68:71], a[140:143], v[164:167]\n"
"v_add_f32 v71, v71, v23\n"
"v_mfma_f32_16x16x32_bf16 v[164:167], a[64:67], a[136:139], v[164:167]\n"
"v_add_f32 v72, v72, v20\n"
"v_add_f32 v73, v73, v21\n"
"v_mfma_f32_16x16x32_bf16 v[168:171], a[64:67], a[144:147], v[168:171]\n"
"v_add_f32 v74, v74, v22\n"
"v_mfma_f32_16x16x32_bf16 v[168:171], a[68:71], a[148:151], v[168:171]\n"
"global_load_dwordx4 a[60:63], v0, s[34:35] offset:3072 sc1\n"
"v_add_f32 v75, v75, v23\n"
"v_mfma_f32_16x16x32_bf16 v[196:199], a[76:79], a[148:151], v[196:199]\n"
"v_add_f32 v76, v76, v20\n"
"v_mfma_f32_16x16x32_bf16 v[196:199], a[72:75], a[144:147], v[196:199]\n"
"v_add_f32 v77, v77, v21\n"
"v_mfma_f32_16x16x32_bf16 a[20:23], a[72:75], a[152:155], a[20:23]\n"
"s_add_u32 s34, s34, 8192\n"
"v_add_f32 v78, v78, v22\n"
"v_mfma_f32_16x16x32_bf16 a[20:23], a[76:79], a[156:159], a[20:23]\n"
"v_add_f32 v79, v79, v23\n"
"v_mfma_f32_16x16x32_bf16 a[16:19], a[68:71], a[156:159], a[16:19]\n"
"v_add_f32 v80, v80, v20\n"
"v_mfma_f32_16x16x32_bf16 a[16:19], a[64:67], a[152:155], a[16:19]\n"
"s_waitcnt vmcnt(10)\n"
"v_add_f32 v81, v81, v21\n"
"v_mfma_f32_16x16x32_bf16 v[200:203], a[80:83], a[96:99], v[200:203]\n"
"v_add_f32 v82, v82, v22\n"
"v_add_f32 v83, v83, v23\n"
"v_mfma_f32_16x16x32_bf16 v[200:203], a[84:87], a[100:103], v[200:203]\n"
"global_load_dwordx4 a[64:67], v0, s[34:35] offset:-4096 sc1\n"
"v_add_f32 v84, v84, v20\n"
"v_mfma_f32_16x16x32_bf16 v[228:231], a[92:95], a[100:103], v[228:231]\n"
"L_st6786:\n"
"s_cbranch_execz L_st8166\n"
"s_waitcnt vmcnt(10)\n"
"ds_read_b128 a[100:103], v2 offset:1024\n"
"v_add_f32 v85, v85, v21\n"
"v_mfma_f32_16x16x32_bf16 v[228:231], a[88:91], a[96:99], v[228:231]\n"
"ds_read_b128 a[96:99], v2 offset:0\n"
"v_add_f32 v86, v86, v22\n"
"v_mfma_f32_16x16x32_bf16 v[232:235], a[88:91], a[104:107], v[232:235]\n"
"v_add_f32 v87, v87, v23\n"
"v_mfma_f32_16x16x32_bf16 v[232:235], a[92:95], a[108:111], v[232:235]\n"
"v_accvgpr_read_b32 v12, a4\n"
"v_mfma_f32_16x16x32_bf16 v[204:207], a[84:87], a[108:111], v[204:207]\n"
"ds_read_b128 a[108:111], v2 offset:3072\n"
"v_accvgpr_read_b32 v13, a5\n"
"v_mfma_f32_16x16x32_bf16 v[204:207], a[80:83], a[104:107], v[204:207]\n"
"ds_read_b128 a[104:107], v2 offset:2048\n"
"v_accvgpr_read_b32 v14, a6\n"
"v_mfma_f32_16x16x32_bf16 v[208:211], a[80:83], a[112:115], v[208:211]\n"
"v_accvgpr_read_b32 v15, a7\n"
"v_mfma_f32_16x16x32_bf16 v[208:211], a[84:87], a[116:119], v[208:211]\n"
"global_load_dwordx4 a[68:71], v0, s[34:35] offset:-3072 sc1\n"
"v_add_f32 v12, v12, v20\n"
"v_mfma_f32_16x16x32_bf16 v[236:239], a[92:95], a[116:119], v[236:239]\n"
"ds_read_b128 a[116:119], v2 offset:5120\n"
"v_add_f32 v13, v13, v21\n"
"v_mfma_f32_16x16x32_bf16 v[236:239], a[88:91], a[112:115], v[236:239]\n"
"ds_read_b128 a[112:115], v2 offset:4096\n"
"v_add_f32 v14, v14, v22\n"
"v_mfma_f32_16x16x32_bf16 v[240:243], a[88:91], a[120:123], v[240:243]\n"
"v_add_f32 v15, v15, v23\n"
"v_accvgpr_write_b32 a4, v12\n"
"v_mfma_f32_16x16x32_bf16 v[240:243], a[92:95], a[124:127], v[240:243]\n"
"v_accvgpr_write_b32 a5, v13\n"
"v_mfma_f32_16x16x32_bf16 v[212:215], a[84:87], a[124:127], v[212:215]\n"
"ds_read_b128 a[124:127], v2 offset:7168\n"
"v_accvgpr_write_b32 a6, v14\n"
"v_mfma_f32_16x16x32_bf16 v[212:215], a[80:83], a[120:123], v[212:215]\n"
"ds_read_b128 a[120:123], v2 offset:6144\n"
"v_accvgpr_write_b32 a7, v15\n"
"v_mfma_f32_16x16x32_bf16 v[216:219], a[80:83], a[128:131], v[216:219]\n"
"v_mfma_f32_16x16x32_bf16 v[216:219], a[84:87], a[132:135], v[216:219]\n"
"global_load_dwordx4 a[72:75], v0, s[34:35] offset:-2048 sc1\n"
"v_lshrrev_b32 v20, 4, v0\n"
"v_mfma_f32_16x16x32_bf16 v[244:247], a[92:95], a[132:135], v[244:247]\n"
"ds_read_b128 a[132:135], v2 offset:9216\n"
"v_and_b32 v20, 0x30, v20\n"
"v_mfma_f32_16x16x32_bf16 v[244:247], a[88:91], a[128:131], v[244:247]\n"
"ds_read_b128 a[128:131], v2 offset:8192\n"
"buffer_load_dwordx2 v[22:23], v20, s[44:47], s61 offen offset:72\n"
"v_mfma_f32_16x16x32_bf16 v[248:251], a[88:91], a[136:139], v[248:251]\n"
"v_mfma_f32_16x16x32_bf16 v[248:251], a[92:95], a[140:143], v[248:251]\n"
"v_mfma_f32_16x16x32_bf16 v[220:223], a[84:87], a[140:143], v[220:223]\n"
"ds_read_b128 a[140:143], v2 offset:11264\n"
"v_mfma_f32_16x16x32_bf16 v[220:223], a[80:83], a[136:139], v[220:223]\n"
"ds_read_b128 a[136:139], v2 offset:10240\n"
"v_mfma_f32_16x16x32_bf16 v[224:227], a[80:83], a[144:147], v[224:227]\n"
"v_mfma_f32_16x16x32_bf16 v[224:227], a[84:87], a[148:151], v[224:227]\n"
"global_load_dwordx4 a[76:79], v0, s[34:35] offset:-1024 sc1\n"
"v_mfma_f32_16x16x32_bf16 v[252:255], a[92:95], a[148:151], v[252:255]\n"
"ds_read_b128 a[148:151], v2 offset:13312\n"
"v_mfma_f32_16x16x32_bf16 v[252:255], a[88:91], a[144:147], v[252:255]\n"
"ds_read_b128 a[144:147], v2 offset:12288\n"
"v_mfma_f32_16x16x32_bf16 a[28:31], a[88:91], a[152:155], a[28:31]\n"
"v_mfma_f32_16x16x32_bf16 a[28:31], a[92:95], a[156:159], a[28:31]\n"
"v_mfma_f32_16x16x32_bf16 a[24:27], a[84:87], a[156:159], a[24:27]\n"
"s_cmp_eq_u32 s62, 5\n"
"ds_read_b128 a[156:159], v2 offset:15360\n"
"v_mfma_f32_16x16x32_bf16 a[24:27], a[80:83], a[152:155], a[24:27]\n"
"ds_read_b128 a[152:155], v2 offset:14336\n"
"s_cbranch_scc1 L_last\n"
"L_P4:\n"
"s_waitcnt vmcnt(9) lgkmcnt(12)\n"
"v_mfma_f32_16x16x32_bf16 v[32:35], a[32:35], a[96:99], v[32:35]\n"
"s_add_u32 s48, s48, 131072\n"
"s_addc_u32 s49, s49, 0\n"
"v_mfma_f32_16x16x32_bf16 v[32:35], a[36:39], a[100:103], v[32:35]\n"
"global_load_dwordx4 a[80:83], v0, s[34:35] offset:0 sc1\n"
"v_mfma_f32_16x16x32_bf16 v[60:63], a[44:47], a[100:103], v[60:63]\n"
"s_barrier\n"
"v_mfma_f32_16x16x32_bf16 v[60:63], a[40:43], a[96:99], v[60:63]\n"
"v_mfma_f32_16x16x32_bf16 v[64:67], a[40:43], a[104:107], v[64:67]\n"
"v_mfma_f32_16x16x32_bf16 v[64:67], a[44:47], a[108:111], v[64:67]\n"
"v_mfma_f32_16x16x32_bf16 v[36:39], a[36:39], a[108:111], v[36:39]\n"
"v_subrev_u32 v8, s65, v8\n"
"v_mfma_f32_16x16x32_bf16 v[36:39], a[32:35], a[104:107], v[36:39]\n"
"s_waitcnt lgkmcnt(8)\n"
"v_xor_b32 v9, s79, v9\n"
"v_mfma_f32_16x16x32_bf16 v[40:43], a[32:35], a[112:115], v[40:43]\n"
"v_or_b32 v8, v8, v9\n"
"v_mfma_f32_16x16x32_bf16 v[40:43], a[36:39], a[116:119], v[40:43]\n"
"global_load_dwordx4 a[84:87], v0, s[34:35] offset:1024 sc1\n"
"v_cmp_lt_u32 vcc, 15, v8\n"
"v_mfma_f32_16x16x32_bf16 v[68:71], a[44:47], a[116:119], v[68:71]\n"
"s_cbranch_vccnz L_slow1\n"
"L_back1:\n"
"v_mfma_f32_16x16x32_bf16 v[68:71], a[40:43], a[112:115], v[68:71]\n"
"v_mfma_f32_16x16x32_bf16 v[72:75], a[40:43], a[120:123], v[72:75]\n"
"v_mfma_f32_16x16x32_bf16 v[72:75], a[44:47], a[124:127], v[72:75]\n"
"v_mfma_f32_16x16x32_bf16 v[44:47], a[36:39], a[124:127], v[44:47]\n"
"v_mfma_f32_16x16x32_bf16 v[44:47], a[32:35], a[120:123], v[44:47]\n"
"s_waitcnt lgkmcnt(4)\n"
"v_mfma_f32_16x16x32_bf16 v[48:51], a[32:35], a[128:131], v[48:51]\n"
"v_mfma_f32_16x16x32_bf16 v[48:51], a[36:39], a[132:135], v[48:51]\n"
"global_load_dwordx4 a[88:91], v0, s[34:35] offset:2048 sc1\n"
"v_mfma_f32_16x16x32_bf16 v[76:79], a[44:47], a[132:135], v[76:79]\n"
"v_mfma_f32_16x16x32_bf16 v[76:79], a[40:43], a[128:131], v[76:79]\n"
"s_add_u32 m0, s64, 16384\n"
"v_mfma_f32_16x16x32_bf16 v[80:83], a[40:43], a[136:139], v[80:83]\n"
"v_mfma_f32_16x16x32_bf16 v[80:83], a[44:47], a[140:143], v[80:83]\n"
"v_mfma_f32_16x16x32_bf16 v[52:55], a[36:39], a[140:143], v[52:55]\n"
"v_mfma_f32_16x16x32_bf16 v[52:55], a[32:35], a[136:139], v[52:55]\n"
"s_waitcnt lgkmcnt(0)\n"
"v_mfma_f32_16x16x32_bf16 v[56:59], a[32:35], a[144:147], v[56:59]\n"
"v_mfma_f32_16x16x32_bf16 v[56:59], a[36:39], a[148:151], v[56:59]\n"
"global_load_dwordx4 a[92:95], v0, s[34:35] offset:3072 sc1\n"
"v_mfma_f32_16x16x32_bf16 v[84:87], a[44:47], a[148:151], v[84:87]\n"
"v_mfma_f32_16x16x32_bf16 v[84:87], a[40:43], a[144:147], v[84:87]\n"
"v_mfma_f32_16x16x32_bf16 a[4:7], a[40:43], a[152:155], a[4:7]\n"
"s_add_u32 s34, s34, 8192\n"
"v_mfma_f32_16x16x32_bf16 a[4:7], a[44:47], a[156:159], a[4:7]\n"
"v_mfma_f32_16x16x32_bf16 a[0:3], a[36:39], a[156:159], a[0:3]\n"
"v_mfma_f32_16x16x32_bf16 a[0:3], a[32:35], a[152:155], a[0:3]\n"
"s_waitcnt vmcnt(9)\n"
"v_mfma_f32_16x16x32_bf16 v[88:91], a[48:51], a[96:99], v[88:91]\n"
"v_mfma_f32_16x16x32_bf16 v[88:91], a[52:55], a[100:103], v[88:91]\n"
"global_load_dwordx4 a[32:35], v0, s[34:35] offset:-4096 sc1\n"
"v_mfma_f32_16x16x32_bf16 v[116:119], a[60:63], a[100:103], v[116:119]\n"
"buffer_load_dwordx4 v0, s[48:51], s75 offen offset:0 sc1 lds\n"
"v_mfma_f32_16x16x32_bf16 v[116:119], a[56:59], a[96:99], v[116:119]\n"
"v_mfma_f32_16x16x32_bf16 v[120:123], a[56:59], a[104:107], v[120:123]\n"
"v_mfma_f32_16x16x32_bf16 v[120:123], a[60:63], a[108:111], v[120:123]\n"
"v_mfma_f32_16x16x32_bf16 v[92:95], a[52:55], a[108:111], v[92:95]\n"
"v_mfma_f32_16x16x32_bf16 v[92:95], a[48:51], a[104:107], v[92:95]\n"
"v_mfma_f32_16x16x32_bf16 v[96:99], a[48:51], a[112:115], v[96:99]\n"
"v_mfma_f32_16x16x32_bf16 v[96:99], a[52:55], a[116:119], v[96:99]\n"
"global_load_dwordx4 a[36:39], v0, s[34:35] offset:-3072 sc1\n"
"v_mfma_f32_16x16x32_bf16 v[124:127], a[60:63], a[116:119], v[124:127]\n"
"v_mfma_f32_16x16x32_bf16 v[124:127], a[56:59], a[112:115], v[124:127]\n"
"v_mfma_f32_16x16x32_bf16 v[128:131], a[56:59], a[120:123], v[128:131]\n"
"v_mfma_f32_16x16x32_bf16 v[128:131], a[60:63], a[124:127], v[128:131]\n"
"v_mfma_f32_16x16x32_bf16 v[100:103], a[52:55], a[124:127], v[100:103]\n"
"v_mfma_f32_16x16x32_bf16 v[100:103], a[48:51], a[120:123], v[100:103]\n"
"v_mfma_f32_16x16x32_bf16 v[104:107], a[48:51], a[128:131], v[104:107]\n"
"v_mfma_f32_16x16x32_bf16 v[104:107], a[52:55], a[132:135], v[104:107]\n"
"global_load_dwordx4 a[40:43], v0, s[34:35] offset:-2048 sc1\n"
"v_mfma_f32_16x16x32_bf16 v[132:135], a[60:63], a[132:135], v[132:135]\n"
"buffer_load_dwordx4 v0, s[48:51], s75 offen offset:1024 sc1 lds\n"
"v_mfma_f32_16x16x32_bf16 v[132:135], a[56:59], a[128:131], v[132:135]\n"
"v_mfma_f32_16x16x32_bf16 v[136:139], a[56:59], a[136:139], v[136:139]\n"
"v_mfma_f32_16x16x32_bf16 v[136:139], a[60:63], a[140:143], v[136:139]\n"
"v_mfma_f32_16x16x32_bf16 v[108:111], a[52:55], a[140:143], v[108:111]\n"
"v_mfma_f32_16x16x32_bf16 v[108:111], a[48:51], a[136:139], v[108:111]\n"
"v_mfma_f32_16x16x32_bf16 v[112:115], a[48:51], a[144:147], v[112:115]\n"
"v_mfma_f32_16x16x32_bf16 v[112:115], a[52:55], a[148:151], v[112:115]\n"
"global_load_dwordx4 a[44:47], v0, s[34:35] offset:-1024 sc1\n"
"v_mfma_f32_16x16x32_bf16 v[140:143], a[60:63], a[148:151], v[140:143]\n"
"v_mfma_f32_16x16x32_bf16 v[140:143], a[56:59], a[144:147], v[140:143]\n"
"v_mfma_f32_16x16x32_bf16 a[12:15], a[56:59], a[152:155], a[12:15]\n"
"v_mfma_f32_16x16x32_bf16 a[12:15], a[60:63], a[156:159], a[12:15]\n"
"v_mfma_f32_16x16x32_bf16 a[8:11], a[52:55], a[156:159], a[8:11]\n"
"v_mfma_f32_16x16x32_bf16 a[8:11], a[48:51], a[152:155], a[8:11]\n"
"s_waitcnt vmcnt(10)\n"
"v_mfma_f32_16x16x32_bf16 v[144:147], a[64:67], a[96:99], v[144:147]\n"
"v_mfma_f32_16x16x32_bf16 v[144:147], a[68:71], a[100:103], v[144:147]\n"
"global_load_dwordx4 a[48:51], v0, s[34:35] offset:0 sc1\n"
"v_mfma_f32_16x16x32_bf16 v[172:175], a[76:79], a[100:103], v[172:175]\n"
"v_mfma_f32_16x16x32_bf16 v[172:175], a[72:75], a[96:99], v[172:175]\n"
"buffer_load_dwordx4 v0, s[48:51], s75 offen offset:2048 sc1 lds\n"
"v_mfma_f32_16x16x32_bf16 v[176:179], a[72:75], a[104:107], v[176:179]\n"
"v_mfma_f32_16x16x32_bf16 v[176:179], a[76:79], a[108:111], v[176:179]\n"
"v_mfma_f32_16x16x32_bf16 v[148:151], a[68:71], a[108:111], v[148:151]\n"
"v_mfma_f32_16x16x32_bf16 v[148:151], a[64:67], a[104:107], v[148:151]\n"
"v_mfma_f32_16x16x32_bf16 v[152:155], a[64:67], a[112:115], v[152:155]\n"
"v_lshlrev_b32 v16, 16, v18\n"
"v_mfma_f32_16x16x32_bf16 v[152:155], a[68:71], a[116:119], v[152:155]\n"
"global_load_dwordx4 a[52:55], v0, s[34:35] offset:1024 sc1\n"
"v_and_b32 v17, s63, v18\n"
"v_mfma_f32_16x16x32_bf16 v[180:183], a[76:79], a[116:119], v[180:183]\n"
"v_lshlrev_b32 v18, 16, v19\n"
"v_mfma_f32_16x16x32_bf16 v[180:183], a[72:75], a[112:115], v[180:183]\n"
"v_and_b32 v19, s63, v19\n"
"v_mfma_f32_16x16x32_bf16 v[184:187], a[72:75], a[120:123], v[184:187]\n"
"v_add_f32 v88, v88, v16\n"
"v_mfma_f32_16x16x32_bf16 v[184:187], a[76:79], a[124:127], v[184:187]\n"
"v_add_f32 v89, v89, v17\n"
"v_add_f32 v90, v90, v18\n"
"v_mfma_f32_16x16x32_bf16 v[156:159], a[68:71], a[124:127], v[156:159]\n"
"v_add_f32 v91, v91, v19\n"
"v_mfma_f32_16x16x32_bf16 v[156:159], a[64:67], a[120:123], v[156:159]\n"
"v_add_f32 v92, v92, v16\n"
"v_mfma_f32_16x16x32_bf16 v[160:163], a[64:67], a[128:131], v[160:163]\n"
"v_add_f32 v93, v93, v17\n"
"v_mfma_f32_16x16x32_bf16 v[160:163], a[68:71], a[132:135], v[160:163]\n"
"global_load_dwordx4 a[56:59], v0, s[34:35] offset:2048 sc1\n"
"v_add_f32 v94, v94, v18\n"
"v_mfma_f32_16x16x32_bf16 v[188:191], a[76:79], a[132:135], v[188:191]\n"
"buffer_load_dwordx4 v0, s[48:51], s75 offen offset:3072 sc1 lds\n"
"v_mfma_f32_16x16x32_bf16 v[188:191], a[72:75], a[128:131], v[188:191]\n"
"v_add_f32 v95, v95, v19\n"
"v_add_f32 v96, v96, v16\n"
"v_mfma_f32_16x16x32_bf16 v[192:195], a[72:75], a[136:139], v[192:195]\n"
"v_add_f32 v97, v97, v17\n"
"v_mfma_f32_16x16x32_bf16 v[192:195], a[76:79], a[140:143], v[192:195]\n"
"v_add_f32 v98, v98, v18\n"
"v_mfma_f32_16x16x32_bf16 v[164:167], a[68:71], a[140:143], v[164:167]\n"
"v_add_f32 v99, v99, v19\n"
"v_mfma_f32_16x16x32_bf16 v[164:167], a[64:67], a[136:139], v[164:167]\n"
"v_add_f32 v100, v100, v16\n"
"v_add_f32 v101, v101, v17\n"
"v_mfma_f32_16x16x32_bf16 v[168:171], a[64:67], a[144:147], v[168:171]\n"
"v_add_f32 v102, v102, v18\n"
"v_mfma_f32_16x16x32_bf16 v[168:171], a[68:71], a[148:151], v[168:171]\n"
"global_load_dwordx4 a[60:63], v0, s[34:35] offset:3072 sc1\n"
"v_add_f32 v103, v103, v19\n"
"v_mfma_f32_16x16x32_bf16 v[196:199], a[76:79], a[148:151], v[196:199]\n"
"v_add_f32 v104, v104, v16\n"
"v_mfma_f32_16x16x32_bf16 v[196:199], a[72:75], a[144:147], v[196:199]\n"
"v_add_f32 v105, v105, v17\n"
"v_mfma_f32_16x16x32_bf16 a[20:23], a[72:75], a[152:155], a[20:23]\n"
"s_add_u32 s34, s34, 8192\n"
"v_add_f32 v106, v106, v18\n"
"v_mfma_f32_16x16x32_bf16 a[20:23], a[76:79], a[156:159], a[20:23]\n"
"v_add_f32 v107, v107, v19\n"
"v_mfma_f32_16x16x32_bf16 a[16:19], a[68:71], a[156:159], a[16:19]\n"
"v_add_f32 v108, v108, v16\n"
"v_mfma_f32_16x16x32_bf16 a[16:19], a[64:67], a[152:155], a[16:19]\n"
"s_waitcnt vmcnt(12)\n"
"v_add_f32 v109, v109, v17\n"
"v_mfma_f32_16x16x32_bf16 v[200:203], a[80:83], a[96:99], v[200:203]\n"
"v_add_f32 v110, v110, v18\n"
"v_add_f32 v111, v111, v19\n"
"v_mfma_f32_16x16x32_bf16 v[200:203], a[84:87], a[100:103], v[200:203]\n"
"global_load_dwordx4 a[64:67], v0, s[34:35] offset:-4096 sc1\n"
"v_add_f32 v112, v112, v16\n"
"v_mfma_f32_16x16x32_bf16 v[228:231], a[92:95], a[100:103], v[228:231]\n"
"ds_read_b128 a[100:103], v3 offset:1024\n"
"v_add_f32 v113, v113, v17\n"
"v_mfma_f32_16x16x32_bf16 v[228:231], a[88:91], a[96:99], v[228:231]\n"
"s_add_u32 m0, s64, 20480\n"
"ds_read_b128 a[96:99], v3 offset:0\n"
"v_add_f32 v114, v114, v18\n"
"v_mfma_f32_16x16x32_bf16 v[232:235], a[88:91], a[104:107], v[232:235]\n"
"v_add_f32 v115, v115, v19\n"
"v_mfma_f32_16x16x32_bf16 v[232:235], a[92:95], a[108:111], v[232:235]\n"
"v_accvgpr_read_b32 v12, a8\n"
"v_mfma_f32_16x16x32_bf16 v[204:207], a[84:87], a[108:111], v[204:207]\n"
"ds_read_b128 a[108:111], v3 offset:3072\n"
"v_accvgpr_read_b32 v13, a9\n"
"v_mfma_f32_16x16x32_bf16 v[204:207], a[80:83], a[104:107], v[204:207]\n"
"ds_read_b128 a[104:107], v3 offset:2048\n"
"v_accvgpr_read_b32 v14, a10\n"
"v_mfma_f32_16x16x32_bf16 v[208:211], a[80:83], a[112:115], v[208:211]\n"
"v_accvgpr_read_b32 v15, a11\n"
"v_mfma_f32_16x16x32_bf16 v[208:211], a[84:87], a[116:119], v[208:211]\n"
"global_load_dwordx4 a[68:71], v0, s[34:35] offset:-3072 sc1\n"
"v_add_f32 v12, v12, v16\n"
"v_mfma_f32_16x16x32_bf16 v[236:239], a[92:95], a[116:119], v[236:239]\n"
"ds_read_b128 a[116:119], v3 offset:5120\n"
"v_add_f32 v13, v13, v17\n"
"v_mfma_f32_16x16x32_bf16 v[236:239], a[88:91], a[112:115], v[236:239]\n"
"ds_read_b128 a[112:115], v3 offset:4096\n"
"v_add_f32 v14, v14, v18\n"
"v_mfma_f32_16x16x32_bf16 v[240:243], a[88:91], a[120:123], v[240:243]\n"
"v_add_f32 v15, v15, v19\n"
"v_accvgpr_write_b32 a8, v12\n"
"v_mfma_f32_16x16x32_bf16 v[240:243], a[92:95], a[124:127], v[240:243]\n"
"v_accvgpr_write_b32 a9, v13\n"
"v_mfma_f32_16x16x32_bf16 v[212:215], a[84:87], a[124:127], v[212:215]\n"
"ds_read_b128 a[124:127], v3 offset:7168\n"
"v_accvgpr_write_b32 a10, v14\n"
"v_mfma_f32_16x16x32_bf16 v[212:215], a[80:83], a[120:123], v[212:215]\n"
"ds_read_b128 a[120:123], v3 offset:6144\n"
"v_accvgpr_write_b32 a11, v15\n"
"v_mfma_f32_16x16x32_bf16 v[216:219], a[80:83], a[128:131], v[216:219]\n"
"v_mfma_f32_16x16x32_bf16 v[216:219], a[84:87], a[132:135], v[216:219]\n"
"global_load_dwordx4 a[72:75], v0, s[34:35] offset:-2048 sc1\n"
"v_mfma_f32_16x16x32_bf16 v[244:247], a[92:95], a[132:135], v[244:247]\n"
"ds_read_b128 a[132:135], v3 offset:9216\n"
"v_mfma_f32_16x16x32_bf16 v[244:247], a[88:91], a[128:131], v[244:247]\n"
"ds_read_b128 a[128:131], v3 offset:8192\n"
"v_mfma_f32_16x16x32_bf16 v[248:251], a[88:91], a[136:139], v[248:251]\n"
"buffer_load_dwordx4 v1, s[48:51], s75 offen offset:0 sc1 lds\n"
"v_mfma_f32_16x16x32_bf16 v[248:251], a[92:95], a[140:143], v[248:251]\n"
"v_mfma_f32_16x16x32_bf16 v[220:223], a[84:87], a[140:143], v[220:223]\n"
"ds_read_b128 a[140:143], v3 offset:11264\n"
"v_mfma_f32_16x16x32_bf16 v[220:223], a[80:83], a[136:139], v[220:223]\n"
"ds_read_b128 a[136:139], v3 offset:10240\n"
"v_mfma_f32_16x16x32_bf16 v[224:227], a[80:83], a[144:147], v[224:227]\n"
"v_mfma_f32_16x16x32_bf16 v[224:227], a[84:87], a[148:151], v[224:227]\n"
"global_load_dwordx4 a[76:79], v0, s[34:35] offset:-1024 sc1\n"
"v_mfma_f32_16x16x32_bf16 v[252:255], a[92:95], a[148:151], v[252:255]\n"
"ds_read_b128 a[148:151], v3 offset:13312\n"
"v_mfma_f32_16x16x32_bf16 v[252:255], a[88:91], a[144:147], v[252:255]\n"
"ds_read_b128 a[144:147], v3 offset:12288\n"
"v_mfma_f32_16x16x32_bf16 a[28:31], a[88:91], a[152:155], a[28:31]\n"
"v_mfma_f32_16x16x32_bf16 a[28:31], a[92:95], a[156:159], a[28:31]\n"
"v_mfma_f32_16x16x32_bf16 a[24:27], a[84:87], a[156:159], a[24:27]\n"
"ds_read_b128 a[156:159], v3 offset:15360\n"
"v_mfma_f32_16x16x32_bf16 a[24:27], a[80:83], a[152:155], a[24:27]\n"
"s_waitcnt vmcnt(11) lgkmcnt(11)\n"
"ds_read_b128 a[152:155], v3 offset:14336\n"
"v_mfma_f32_16x16x32_bf16 v[32:35], a[32:35], a[96:99], v[32:35]\n"
"v_mfma_f32_16x16x32_bf16 v[32:35], a[36:39], a[100:103], v[32:35]\n"
"global_load_dwordx4 a[80:83], v0, s[34:35] offset:0 sc1\n"
"v_mfma_f32_16x16x32_bf16 v[60:63], a[44:47], a[100:103], v[60:63]\n"
"v_mfma_f32_16x16x32_bf16 v[60:63], a[40:43], a[96:99], v[60:63]\n"
"buffer_load_dwordx4 v1, s[48:51], s75 offen offset:1024 sc1 lds\n"
"v_mfma_f32_16x16x32_bf16 v[64:67], a[40:43], a[104:107], v[64:67]\n"
"v_mfma_f32_16x16x32_bf16 v[64:67], a[44:47], a[108:111], v[64:67]\n"
"v_mfma_f32_16x16x32_bf16 v[36:39], a[36:39], a[108:111], v[36:39]\n"
"v_mfma_f32_16x16x32_bf16 v[36:39], a[32:35], a[104:107], v[36:39]\n"
"s_waitcnt lgkmcnt(8)\n"
"v_mfma_f32_16x16x32_bf16 v[40:43], a[32:35], a[112:115], v[40:43]\n"
"v_mfma_f32_16x16x32_bf16 v[40:43], a[36:39], a[116:119], v[40:43]\n"
"global_load_dwordx4 a[84:87], v0, s[34:35] offset:1024 sc1\n"
"v_mfma_f32_16x16x32_bf16 v[68:71], a[44:47], a[116:119], v[68:71]\n"
"v_mfma_f32_16x16x32_bf16 v[68:71], a[40:43], a[112:115], v[68:71]\n"
"v_mfma_f32_16x16x32_bf16 v[72:75], a[40:43], a[120:123], v[72:75]\n"
"v_mfma_f32_16x16x32_bf16 v[72:75], a[44:47], a[124:127], v[72:75]\n"
"v_mfma_f32_16x16x32_bf16 v[44:47], a[36:39], a[124:127], v[44:47]\n"
"v_mfma_f32_16x16x32_bf16 v[44:47], a[32:35], a[120:123], v[44:47]\n"
"s_waitcnt lgkmcnt(4)\n"
"v_mfma_f32_16x16x32_bf16 v[48:51], a[32:35], a[128:131], v[48:51]\n"
"v_mfma_f32_16x16x32_bf16 v[48:51], a[36:39], a[132:135], v[48:51]\n"
"global_load_dwordx4 a[88:91], v0, s[34:35] offset:2048 sc1\n"
"v_mfma_f32_16x16x32_bf16 v[76:79], a[44:47], a[132:135], v[76:79]\n"
"v_mfma_f32_16x16x32_bf16 v[76:79], a[40:43], a[128:131], v[76:79]\n"
"buffer_load_dwordx4 v1, s[48:51], s75 offen offset:2048 sc1 lds\n"
"v_mfma_f32_16x16x32_bf16 v[80:83], a[40:43], a[136:139], v[80:83]\n"
"v_mfma_f32_16x16x32_bf16 v[80:83], a[44:47], a[140:143], v[80:83]\n"
"v_mfma_f32_16x16x32_bf16 v[52:55], a[36:39], a[140:143], v[52:55]\n"
"v_mfma_f32_16x16x32_bf16 v[52:55], a[32:35], a[136:139], v[52:55]\n"
"s_waitcnt lgkmcnt(0)\n"
"v_mfma_f32_16x16x32_bf16 v[56:59], a[32:35], a[144:147], v[56:59]\n"
"v_mfma_f32_16x16x32_bf16 v[56:59], a[36:39], a[148:151], v[56:59]\n"
"global_load_dwordx4 a[92:95], v0, s[34:35] offset:3072 sc1\n"
"v_mfma_f32_16x16x32_bf16 v[84:87], a[44:47], a[148:151], v[84:87]\n"
"v_mfma_f32_16x16x32_bf16 v[84:87], a[40:43], a[144:147], v[84:87]\n"
"v_mfma_f32_16x16x32_bf16 a[4:7], a[40:43], a[152:155], a[4:7]\n"
"s_add_u32 s34, s34, 8192\n"
"v_mfma_f32_16x16x32_bf16 a[4:7], a[44:47], a[156:159], a[4:7]\n"
"v_mfma_f32_16x16x32_bf16 a[0:3], a[36:39], a[156:159], a[0:3]\n"
"v_mfma_f32_16x16x32_bf16 a[0:3], a[32:35], a[152:155], a[0:3]\n"
"s_waitcnt vmcnt(11)\n"
"v_mfma_f32_16x16x32_bf16 v[88:91], a[48:51], a[96:99], v[88:91]\n"
"v_mfma_f32_16x16x32_bf16 v[88:91], a[52:55], a[100:103], v[88:91]\n"
"global_load_dwordx4 a[32:35], v0, s[34:35] offset:-4096 sc1\n"
"v_mfma_f32_16x16x32_bf16 v[116:119], a[60:63], a[100:103], v[116:119]\n"
"v_mfma_f32_16x16x32_bf16 v[116:119], a[56:59], a[96:99], v[116:119]\n"
"buffer_load_dwordx4 v1, s[48:51], s75 offen offset:3072 sc1 lds\n"
"v_mfma_f32_16x16x32_bf16 v[120:123], a[56:59], a[104:107], v[120:123]\n"
"v_mfma_f32_16x16x32_bf16 v[120:123], a[60:63], a[108:111], v[120:123]\n"
"v_mfma_f32_16x16x32_bf16 v[92:95], a[52:55], a[108:111], v[92:95]\n"
"v_mfma_f32_16x16x32_bf16 v[92:95], a[48:51], a[104:107], v[92:95]\n"
"v_mfma_f32_16x16x32_bf16 v[96:99], a[48:51], a[112:115], v[96:99]\n"
"v_mfma_f32_16x16x32_bf16 v[96:99], a[52:55], a[116:119], v[96:99]\n"
"global_load_dwordx4 a[36:39], v0, s[34:35] offset:-3072 sc1\n"
"v_mfma_f32_16x16x32_bf16 v[124:127], a[60:63], a[116:119], v[124:127]\n"
"v_mfma_f32_16x16x32_bf16 v[124:127], a[56:59], a[112:115], v[124:127]\n"
"v_mfma_f32_16x16x32_bf16 v[128:131], a[56:59], a[120:123], v[128:131]\n"
"v_mfma_f32_16x16x32_bf16 v[128:131], a[60:63], a[124:127], v[128:131]\n"
"v_mfma_f32_16x16x32_bf16 v[100:103], a[52:55], a[124:127], v[100:103]\n"
"v_mfma_f32_16x16x32_bf16 v[100:103], a[48:51], a[120:123], v[100:103]\n"
"v_mfma_f32_16x16x32_bf16 v[104:107], a[48:51], a[128:131], v[104:107]\n"
"v_mfma_f32_16x16x32_bf16 v[104:107], a[52:55], a[132:135], v[104:107]\n"
"global_load_dwordx4 a[40:43], v0, s[34:35] offset:-2048 sc1\n"
"v_mfma_f32_16x16x32_bf16 v[132:135], a[60:63], a[132:135], v[132:135]\n"
"v_mfma_f32_16x16x32_bf16 v[132:135], a[56:59], a[128:131], v[132:135]\n"
"s_add_u32 m0, s64, 24576\n"
"v_mfma_f32_16x16x32_bf16 v[136:139], a[56:59], a[136:139], v[136:139]\n"
"v_mfma_f32_16x16x32_bf16 v[136:139], a[60:63], a[140:143], v[136:139]\n"
"v_mfma_f32_16x16x32_bf16 v[108:111], a[52:55], a[140:143], v[108:111]\n"
"v_mfma_f32_16x16x32_bf16 v[108:111], a[48:51], a[136:139], v[108:111]\n"
"v_mfma_f32_16x16x32_bf16 v[112:115], a[48:51], a[144:147], v[112:115]\n"
"v_mfma_f32_16x16x32_bf16 v[112:115], a[52:55], a[148:151], v[112:115]\n"
"global_load_dwordx4 a[44:47], v0, s[34:35] offset:-1024 sc1\n"
"v_mfma_f32_16x16x32_bf16 v[140:143], a[60:63], a[148:151], v[140:143]\n"
"v_mfma_f32_16x16x32_bf16 v[140:143], a[56:59], a[144:147], v[140:143]\n"
"v_mfma_f32_16x16x32_bf16 a[12:15], a[56:59], a[152:155], a[12:15]\n"
"v_mfma_f32_16x16x32_bf16 a[12:15], a[60:63], a[156:159], a[12:15]\n"
"v_mfma_f32_16x16x32_bf16 a[8:11], a[52:55], a[156:159], a[8:11]\n"
"v_mfma_f32_16x16x32_bf16 a[8:11], a[48:51], a[152:155], a[8:11]\n"
"s_waitcnt vmcnt(11)\n"
"v_mfma_f32_16x16x32_bf16 v[144:147], a[64:67], a[96:99], v[144:147]\n"
"v_mfma_f32_16x16x32_bf16 v[144:147], a[68:71], a[100:103], v[144:147]\n"
"global_load_dwordx4 a[48:51], v0, s[34:35] offset:0 sc1\n"
"v_mfma_f32_16x16x32_bf16 v[172:175], a[76:79], a[100:103], v[172:175]\n"
"v_mfma_f32_16x16x32_bf16 v[172:175], a[72:75], a[96:99], v[172:175]\n"
"buffer_load_dwordx4 v0, s[48:51], s69 offen offset:0 sc1 lds\n"
"v_mfma_f32_16x16x32_bf16 v[176:179], a[72:75], a[104:107], v[176:179]\n"
"v_mfma_f32_16x16x32_bf16 v[176:179], a[76:79], a[108:111], v[176:179]\n"
"v_mfma_f32_16x16x32_bf16 v[148:151], a[68:71], a[108:111], v[148:151]\n"
"v_mfma_f32_16x16x32_bf16 v[148:151], a[64:67], a[104:107], v[148:151]\n"
"v_mfma_f32_16x16x32_bf16 v[152:155], a[64:67], a[112:115], v[152:155]\n"
"v_lshlrev_b32 v20, 16, v22\n"
"v_mfma_f32_16x16x32_bf16 v[152:155], a[68:71], a[116:119], v[152:155]\n"
"global_load_dwordx4 a[52:55], v0, s[34:35] offset:1024 sc1\n"
"v_and_b32 v21, s63, v22\n"
"v_mfma_f32_16x16x32_bf16 v[180:183], a[76:79], a[116:119], v[180:183]\n"
"v_lshlrev_b32 v22, 16, v23\n"
"v_mfma_f32_16x16x32_bf16 v[180:183], a[72:75], a[112:115], v[180:183]\n"
"v_and_b32 v23, s63, v23\n"
"v_mfma_f32_16x16x32_bf16 v[184:187], a[72:75], a[120:123], v[184:187]\n"
"v_add_f32 v116, v116, v20\n"
"v_mfma_f32_16x16x32_bf16 v[184:187], a[76:79], a[124:127], v[184:187]\n"
"v_add_f32 v117, v117, v21\n"
"v_add_f32 v118, v118, v22\n"
"v_mfma_f32_16x16x32_bf16 v[156:159], a[68:71], a[124:127], v[156:159]\n"
"v_add_f32 v119, v119, v23\n"
"v_mfma_f32_16x16x32_bf16 v[156:159], a[64:67], a[120:123], v[156:159]\n"
"v_add_f32 v120, v120, v20\n"
"v_mfma_f32_16x16x32_bf16 v[160:163], a[64:67], a[128:131], v[160:163]\n"
"v_add_f32 v121, v121, v21\n"
"v_mfma_f32_16x16x32_bf16 v[160:163], a[68:71], a[132:135], v[160:163]\n"
"global_load_dwordx4 a[56:59], v0, s[34:35] offset:2048 sc1\n"
"v_add_f32 v122, v122, v22\n"
"v_mfma_f32_16x16x32_bf16 v[188:191], a[76:79], a[132:135], v[188:191]\n"
"v_add_f32 v123, v123, v23\n"
"v_add_f32 v124, v124, v20\n"
"v_mfma_f32_16x16x32_bf16 v[188:191], a[72:75], a[128:131], v[188:191]\n"
"buffer_load_dwordx4 v0, s[48:51], s69 offen offset:1024 sc1 lds\n"
"v_mfma_f32_16x16x32_bf16 v[192:195], a[72:75], a[136:139], v[192:195]\n"
"v_add_f32 v125, v125, v21\n"
"v_mfma_f32_16x16x32_bf16 v[192:195], a[76:79], a[140:143], v[192:195]\n"
"v_add_f32 v126, v126, v22\n"
"v_mfma_f32_16x16x32_bf16 v[164:167], a[68:71], a[140:143], v[164:167]\n"
"v_add_f32 v127, v127, v23\n"
"v_mfma_f32_16x16x32_bf16 v[164:167], a[64:67], a[136:139], v[164:167]\n"
"v_add_f32 v128, v128, v20\n"
"v_add_f32 v129, v129, v21\n"
"v_mfma_f32_16x16x32_bf16 v[168:171], a[64:67], a[144:147], v[168:171]\n"
"v_add_f32 v130, v130, v22\n"
"v_mfma_f32_16x16x32_bf16 v[168:171], a[68:71], a[148:151], v[168:171]\n"
"global_load_dwordx4 a[60:63], v0, s[34:35] offset:3072 sc1\n"
"v_add_f32 v131, v131, v23\n"
"v_mfma_f32_16x16x32_bf16 v[196:199], a[76:79], a[148:151], v[196:199]\n"
"v_add_f32 v132, v132, v20\n"
"v_mfma_f32_16x16x32_bf16 v[196:199], a[72:75], a[144:147], v[196:199]\n"
"v_add_f32 v133, v133, v21\n"
"v_mfma_f32_16x16x32_bf16 a[20:23], a[72:75], a[152:155], a[20:23]\n"
"s_add_u32 s34, s34, 8192\n"
"v_add_f32 v134, v134, v22\n"
"v_mfma_f32_16x16x32_bf16 a[20:23], a[76:79], a[156:159], a[20:23]\n"
"v_add_f32 v135, v135, v23\n"
"v_mfma_f32_16x16x32_bf16 a[16:19], a[68:71], a[156:159], a[16:19]\n"
"v_add_f32 v136, v136, v20\n"
"v_mfma_f32_16x16x32_bf16 a[16:19], a[64:67], a[152:155], a[16:19]\n"
"s_waitcnt vmcnt(11)\n"
"v_add_f32 v137, v137, v21\n"
"v_mfma_f32_16x16x32_bf16 v[200:203], a[80:83], a[96:99], v[200:203]\n"
"v_add_f32 v138, v138, v22\n"
"v_add_f32 v139, v139, v23\n"
"v_mfma_f32_16x16x32_bf16 v[200:203], a[84:87], a[100:103], v[200:203]\n"
"global_load_dwordx4 a[64:67], v0, s[34:35] offset:-4096 sc1\n"
"v_add_f32 v140, v140, v20\n"
"v_mfma_f32_16x16x32_bf16 v[228:231], a[92:95], a[100:103], v[228:231]\n"
"ds_read_b128 a[100:103], v4 offset:1024\n"
"v_add_f32 v141, v141, v21\n"
"v_mfma_f32_16x16x32_bf16 v[228:231], a[88:91], a[96:99], v[228:231]\n"
"ds_read_b128 a[96:99], v4 offset:0\n"
"v_add_f32 v142, v142, v22\n"
"v_mfma_f32_16x16x32_bf16 v[232:235], a[88:91], a[104:107], v[232:235]\n"
"buffer_load_dwordx4 v0, s[48:51], s69 offen offset:2048 sc1 lds\n"
"v_mfma_f32_16x16x32_bf16 v[232:235], a[92:95], a[108:111], v[232:235]\n"
"v_add_f32 v143, v143, v23\n"
"v_accvgpr_read_b32 v12, a12\n"
"v_mfma_f32_16x16x32_bf16 v[204:207], a[84:87], a[108:111], v[204:207]\n"
"ds_read_b128 a[108:111], v4 offset:3072\n"
"v_accvgpr_read_b32 v13, a13\n"
"v_mfma_f32_16x16x32_bf16 v[204:207], a[80:83], a[104:107], v[204:207]\n"
"ds_read_b128 a[104:107], v4 offset:2048\n"
"v_accvgpr_read_b32 v14, a14\n"
"v_mfma_f32_16x16x32_bf16 v[208:211], a[80:83], a[112:115], v[208:211]\n"
"v_accvgpr_read_b32 v15, a15\n"
"v_mfma_f32_16x16x32_bf16 v[208:211], a[84:87], a[116:119], v[208:211]\n"
"global_load_dwordx4 a[68:71], v0, s[34:35] offset:-3072 sc1\n"
"v_add_f32 v12, v12, v20\n"
"v_mfma_f32_16x16x32_bf16 v[236:239], a[92:95], a[116:119], v[236:239]\n"
"ds_read_b128 a[116:119], v4 offset:5120\n"
"v_add_f32 v13, v13, v21\n"
"v_mfma_f32_16x16x32_bf16 v[236:239], a[88:91], a[112:115], v[236:239]\n"
"ds_read_b128 a[112:115], v4 offset:4096\n"
"v_add_f32 v14, v14, v22\n"
"v_mfma_f32_16x16x32_bf16 v[240:243], a[88:91], a[120:123], v[240:243]\n"
"v_add_f32 v15, v15, v23\n"
"v_accvgpr_write_b32 a12, v12\n"
"v_mfma_f32_16x16x32_bf16 v[240:243], a[92:95], a[124:127], v[240:243]\n"
"v_accvgpr_write_b32 a13, v13\n"
"v_mfma_f32_16x16x32_bf16 v[212:215], a[84:87], a[124:127], v[212:215]\n"
"ds_read_b128 a[124:127], v4 offset:7168\n"
"v_accvgpr_write_b32 a14, v14\n"
"v_mfma_f32_16x16x32_bf16 v[212:215], a[80:83], a[120:123], v[212:215]\n"
"ds_read_b128 a[120:123], v4 offset:6144\n"
"v_accvgpr_write_b32 a15, v15\n"
"v_mfma_f32_16x16x32_bf16 v[216:219], a[80:83], a[128:131], v[216:219]\n"
"v_mfma_f32_16x16x32_bf16 v[216:219], a[84:87], a[132:135], v[216:219]\n"
"global_load_dwordx4 a[72:75], v0, s[34:35] offset:-2048 sc1\n"
"v_mfma_f32_16x16x32_bf16 v[244:247], a[92:95], a[132:135], v[244:247]\n"
"ds_read_b128 a[132:135], v4 offset:9216\n"
"v_mfma_f32_16x16x32_bf16 v[244:247], a[88:91], a[128:131], v[244:247]\n"
"ds_read_b128 a[128:131], v4 offset:8192\n"
"v_mfma_f32_16x16x32_bf16 v[248:251], a[88:91], a[136:139], v[248:251]\n"
"buffer_load_dwordx4 v0, s[48:51], s69 offen offset:3072 sc1 lds\n"
"v_mfma_f32_16x16x32_bf16 v[248:251], a[92:95], a[140:143], v[248:251]\n"
"v_mfma_f32_16x16x32_bf16 v[220:223], a[84:87], a[140:143], v[220:223]\n"
"ds_read_b128 a[140:143], v4 offset:11264\n"
"v_mfma_f32_16x16x32_bf16 v[220:223], a[80:83], a[136:139], v[220:223]\n"
"ds_read_b128 a[136:139], v4 offset:10240\n"
"v_mfma_f32_16x16x32_bf16 v[224:227], a[80:83], a[144:147], v[224:227]\n"
"v_mfma_f32_16x16x32_bf16 v[224:227], a[84:87], a[148:151], v[224:227]\n"
"global_load_dwordx4 a[76:79], v0, s[34:35] offset:-1024 sc1\n"
"v_mfma_f32_16x16x32_bf16 v[252:255], a[92:95], a[148:151], v[252:255]\n"
"ds_read_b128 a[148:151], v4 offset:13312\n"
"v_mfma_f32_16x16x32_bf16 v[252:255], a[88:91], a[144:147], v[252:255]\n"
"ds_read_b128 a[144:147], v4 offset:12288\n"
"v_mfma_f32_16x16x32_bf16 a[28:31], a[88:91], a[152:155], a[28:31]\n"
"v_mfma_f32_16x16x32_bf16 a[28:31], a[92:95], a[156:159], a[28:31]\n"
"v_mfma_f32_16x16x32_bf16 a[24:27], a[84:87], a[156:159], a[24:27]\n"
"ds_read_b128 a[156:159], v4 offset:15360\n"
"v_mfma_f32_16x16x32_bf16 a[24:27], a[80:83], a[152:155], a[24:27]\n"
"s_waitcnt vmcnt(12) lgkmcnt(11)\n"
"ds_read_b128 a[152:155], v4 offset:14336\n"
"v_mfma_f32_16x16x32_bf16 v[32:35], a[32:35], a[96:99], v[32:35]\n"
"v_mfma_f32_16x16x32_bf16 v[32:35], a[36:39], a[100:103], v[32:35]\n"
"global_load_dwordx4 a[80:83], v0, s[34:35] offset:0 sc1\n"
"v_lshrrev_b32 v16, 4, v0\n"
"v_mfma_f32_16x16x32_bf16 v[60:63], a[44:47], a[100:103], v[60:63]\n"
"v_and_b32 v16, 0x30, v16\n"
"v_mfma_f32_16x16x32_bf16 v[60:63], a[40:43], a[96:99], v[60:63]\n"
"s_add_u32 m0, s64, 28672\n"
"buffer_load_dwordx2 v[18:19], v16, s[44:47], s61 offen offset:128\n"
"v_mfma_f32_16x16x32_bf16 v[64:67], a[40:43], a[104:107], v[64:67]\n"
"v_mfma_f32_16x16x32_bf16 v[64:67], a[44:47], a[108:111], v[64:67]\n"
"v_mfma_f32_16x16x32_bf16 v[36:39], a[36:39], a[108:111], v[36:39]\n"
"v_mfma_f32_16x16x32_bf16 v[36:39], a[32:35], a[104:107], v[36:39]\n"
"s_waitcnt lgkmcnt(8)\n"
"v_mfma_f32_16x16x32_bf16 v[40:43], a[32:35], a[112:115], v[40:43]\n"
"v_mfma_f32_16x16x32_bf16 v[40:43], a[36:39], a[116:119], v[40:43]\n"
"global_load_dwordx4 a[84:87], v0, s[34:35] offset:1024 sc1\n"
"v_lshrrev_b32 v20, 4, v0\n"
"v_mfma_f32_16x16x32_bf16 v[68:71], a[44:47], a[116:119], v[68:71]\n"
"v_and_b32 v20, 0x30, v20\n"
"v_mfma_f32_16x16x32_bf16 v[68:71], a[40:43], a[112:115], v[68:71]\n"
"buffer_load_dwordx2 v[22:23], v20, s[44:47], s61 offen offset:136\n"
"v_mfma_f32_16x16x32_bf16 v[72:75], a[40:43], a[120:123], v[72:75]\n"
"v_mfma_f32_16x16x32_bf16 v[72:75], a[44:47], a[124:127], v[72:75]\n"
"v_mfma_f32_16x16x32_bf16 v[44:47], a[36:39], a[124:127], v[44:47]\n"
"v_mfma_f32_16x16x32_bf16 v[44:47], a[32:35], a[120:123], v[44:47]\n"
"s_waitcnt lgkmcnt(4)\n"
"v_mfma_f32_16x16x32_bf16 v[48:51], a[32:35], a[128:131], v[48:51]\n"
"v_mfma_f32_16x16x32_bf16 v[48:51], a[36:39], a[132:135], v[48:51]\n"
"global_load_dwordx4 a[88:91], v0, s[34:35] offset:2048 sc1\n"
"v_lshrrev_b32 v24, 4, v0\n"
"v_mfma_f32_16x16x32_bf16 v[76:79], a[44:47], a[132:135], v[76:79]\n"
"v_and_b32 v24, 0x30, v24\n"
"v_mfma_f32_16x16x32_bf16 v[76:79], a[40:43], a[128:131], v[76:79]\n"
"buffer_load_dwordx4 v1, s[48:51], s69 offen offset:0 sc1 lds\n"
"buffer_load_dwordx2 v[26:27], v24, s[44:47], s61 offen offset:192\n"
"v_mfma_f32_16x16x32_bf16 v[80:83], a[40:43], a[136:139], v[80:83]\n"
"v_mfma_f32_16x16x32_bf16 v[80:83], a[44:47], a[140:143], v[80:83]\n"
"v_mfma_f32_16x16x32_bf16 v[52:55], a[36:39], a[140:143], v[52:55]\n"
"v_mfma_f32_16x16x32_bf16 v[52:55], a[32:35], a[136:139], v[52:55]\n"
"s_waitcnt lgkmcnt(0)\n"
"v_mfma_f32_16x16x32_bf16 v[56:59], a[32:35], a[144:147], v[56:59]\n"
"v_mfma_f32_16x16x32_bf16 v[56:59], a[36:39], a[148:151], v[56:59]\n"
"global_load_dwordx4 a[92:95], v0, s[34:35] offset:3072 sc1\n"
"v_lshrrev_b32 v28, 4, v0\n"
"v_mfma_f32_16x16x32_bf16 v[84:87], a[44:47], a[148:151], v[84:87]\n"
"v_and_b32 v28, 0x30, v28\n"
"v_mfma_f32_16x16x32_bf16 v[84:87], a[40:43], a[144:147], v[84:87]\n"
"buffer_load_dwordx2 v[30:31], v28, s[44:47], s61 offen offset:200\n"
"v_mfma_f32_16x16x32_bf16 a[4:7], a[40:43], a[152:155], a[4:7]\n"
"s_add_u32 s34, s34, 8192\n"
"v_mfma_f32_16x16x32_bf16 a[4:7], a[44:47], a[156:159], a[4:7]\n"
"L_st7359:\n"
"s_cbranch_execz L_st9051\n"
"v_mfma_f32_16x16x32_bf16 a[0:3], a[36:39], a[156:159], a[0:3]\n"
"v_mfma_f32_16x16x32_bf16 a[0:3], a[32:35], a[152:155], a[0:3]\n"
"s_waitcnt vmcnt(15)\n"
"v_mfma_f32_16x16x32_bf16 v[88:91], a[48:51], a[96:99], v[88:91]\n"
"v_mfma_f32_16x16x32_bf16 v[88:91], a[52:55], a[100:103], v[88:91]\n"
"global_load_dwordx4 a[32:35], v0, s[34:35] offset:-4096 sc1\n"
"v_mfma_f32_16x16x32_bf16 v[116:119], a[60:63], a[100:103], v[116:119]\n"
"v_mfma_f32_16x16x32_bf16 v[116:119], a[56:59], a[96:99], v[116:119]\n"
"buffer_load_dwordx4 v1, s[48:51], s69 offen offset:1024 sc1 lds\n"
"v_mfma_f32_16x16x32_bf16 v[120:123], a[56:59], a[104:107], v[120:123]\n"
"v_mfma_f32_16x16x32_bf16 v[120:123], a[60:63], a[108:111], v[120:123]\n"
"v_mfma_f32_16x16x32_bf16 v[92:95], a[52:55], a[108:111], v[92:95]\n"
"v_mfma_f32_16x16x32_bf16 v[92:95], a[48:51], a[104:107], v[92:95]\n"
"v_mfma_f32_16x16x32_bf16 v[96:99], a[48:51], a[112:115], v[96:99]\n"
"v_mfma_f32_16x16x32_bf16 v[96:99], a[52:55], a[116:119], v[96:99]\n"
"global_load_dwordx4 a[36:39], v0, s[34:35] offset:-3072 sc1\n"
"v_mfma_f32_16x16x32_bf16 v[124:127], a[60:63], a[116:119], v[124:127]\n"
"v_mfma_f32_16x16x32_bf16 v[124:127], a[56:59], a[112:115], v[124:127]\n"
"v_mfma_f32_16x16x32_bf16 v[128:131], a[56:59], a[120:123], v[128:131]\n"
"v_mfma_f32_16x16x32_bf16 v[128:131], a[60:63], a[124:127], v[128:131]\n"
"v_mfma_f32_16x16x32_bf16 v[100:103], a[52:55], a[124:127], v[100:103]\n"
"v_mfma_f32_16x16x32_bf16 v[100:103], a[48:51], a[120:123], v[100:103]\n"
"v_mfma_f32_16x16x32_bf16 v[104:107], a[48:51], a[128:131], v[104:107]\n"
"v_mfma_f32_16x16x32_bf16 v[104:107], a[52:55], a[132:135], v[104:107]\n"
"global_load_dwordx4 a[40:43], v0, s[34:35] offset:-2048 sc1\n"
"v_mfma_f32_16x16x32_bf16 v[132:135], a[60:63], a[132:135], v[132:135]\n"
"v_mfma_f32_16x16x32_bf16 v[132:135], a[56:59], a[128:131], v[132:135]\n"
"buffer_load_dwordx4 v1, s[48:51], s69 offen offset:2048 sc1 lds\n"
"v_mfma_f32_16x16x32_bf16 v[136:139], a[56:59], a[136:139], v[136:139]\n"
"v_mfma_f32_16x16x32_bf16 v[136:139], a[60:63], a[140:143], v[136:139]\n"
"v_mfma_f32_16x16x32_bf16 v[108:111], a[52:55], a[140:143], v[108:111]\n"
"v_mfma_f32_16x16x32_bf16 v[108:111], a[48:51], a[136:139], v[108:111]\n"
"v_mfma_f32_16x16x32_bf16 v[112:115], a[48:51], a[144:147], v[112:115]\n"
"v_mfma_f32_16x16x32_bf16 v[112:115], a[52:55], a[148:151], v[112:115]\n"
"global_load_dwordx4 a[44:47], v0, s[34:35] offset:-1024 sc1\n"
"v_mfma_f32_16x16x32_bf16 v[140:143], a[60:63], a[148:151], v[140:143]\n"
"v_mfma_f32_16x16x32_bf16 v[140:143], a[56:59], a[144:147], v[140:143]\n"
"v_mfma_f32_16x16x32_bf16 a[12:15], a[56:59], a[152:155], a[12:15]\n"
"v_mfma_f32_16x16x32_bf16 a[12:15], a[60:63], a[156:159], a[12:15]\n"
"v_mfma_f32_16x16x32_bf16 a[8:11], a[52:55], a[156:159], a[8:11]\n"
"v_mfma_f32_16x16x32_bf16 a[8:11], a[48:51], a[152:155], a[8:11]\n"
"s_waitcnt vmcnt(15)\n"
"v_mfma_f32_16x16x32_bf16 v[144:147], a[64:67], a[96:99], v[144:147]\n"
"v_mfma_f32_16x16x32_bf16 v[144:147], a[68:71], a[100:103], v[144:147]\n"
"global_load_dwordx4 a[48:51], v0, s[34:35] offset:0 sc1\n"
"v_mfma_f32_16x16x32_bf16 v[172:175], a[76:79], a[100:103], v[172:175]\n"
"v_mfma_f32_16x16x32_bf16 v[172:175], a[72:75], a[96:99], v[172:175]\n"
"buffer_load_dwordx4 v1, s[48:51], s69 offen offset:3072 sc1 lds\n"
"v_mfma_f32_16x16x32_bf16 v[176:179], a[72:75], a[104:107], v[176:179]\n"
"v_mfma_f32_16x16x32_bf16 v[176:179], a[76:79], a[108:111], v[176:179]\n"
"v_mfma_f32_16x16x32_bf16 v[148:151], a[68:71], a[108:111], v[148:151]\n"
"v_mfma_f32_16x16x32_bf16 v[148:151], a[64:67], a[104:107], v[148:151]\n"
"v_mfma_f32_16x16x32_bf16 v[152:155], a[64:67], a[112:115], v[152:155]\n"
"v_mfma_f32_16x16x32_bf16 v[152:155], a[68:71], a[116:119], v[152:155]\n"
"global_load_dwordx4 a[52:55], v0, s[34:35] offset:1024 sc1\n"
"v_mfma_f32_16x16x32_bf16 v[180:183], a[76:79], a[116:119], v[180:183]\n"
"v_mfma_f32_16x16x32_bf16 v[180:183], a[72:75], a[112:115], v[180:183]\n"
"v_mfma_f32_16x16x32_bf16 v[184:187], a[72:75], a[120:123], v[184:187]\n"
"v_mfma_f32_16x16x32_bf16 v[184:187], a[76:79], a[124:127], v[184:187]\n"
"v_mfma_f32_16x16x32_bf16 v[156:159], a[68:71], a[124:127], v[156:159]\n"
"v_mfma_f32_16x16x32_bf16 v[156:159], a[64:67], a[120:123], v[156:159]\n"
"v_mfma_f32_16x16x32_bf16 v[160:163], a[64:67], a[128:131], v[160:163]\n"
"v_mfma_f32_16x16x32_bf16 v[160:163], a[68:71], a[132:135], v[160:163]\n"
"global_load_dwordx4 a[56:59], v0, s[34:35] offset:2048 sc1\n"
"v_mfma_f32_16x16x32_bf16 v[188:191], a[76:79], a[132:135], v[188:191]\n"
"v_mfma_f32_16x16x32_bf16 v[188:191], a[72:75], a[128:131], v[188:191]\n"
"v_mfma_f32_16x16x32_bf16 v[192:195], a[72:75], a[136:139], v[192:195]\n"
"s_cmp_lg_u32 s29, 0\n"
"s_cselect_b32 s60, s31, s60\n"
"s_cselect_b32 s30, 0, s30\n"
"v_mfma_f32_16x16x32_bf16 v[192:195], a[76:79], a[140:143], v[192:195]\n"
"v_mfma_f32_16x16x32_bf16 v[164:167], a[68:71], a[140:143], v[164:167]\n"
"s_cmp_lg_u32 s29, 0\n"
"s_cselect_b32 s31, s75, s72\n"
"v_mfma_f32_16x16x32_bf16 v[164:167], a[64:67], a[136:139], v[164:167]\n"
"s_sub_u32 s52, s52, s72\n"
"s_subb_u32 s53, s53, 0\n"
"v_mfma_f32_16x16x32_bf16 v[168:171], a[64:67], a[144:147], v[168:171]\n"
"s_add_u32 s52, s52, s31\n"
"s_addc_u32 s53, s53, 0\n"
"v_mfma_f32_16x16x32_bf16 v[168:171], a[68:71], a[148:151], v[168:171]\n"
"global_load_dwordx4 a[60:63], v0, s[34:35] offset:3072 sc1\n"
"v_mfma_f32_16x16x32_bf16 v[196:199], a[76:79], a[148:151], v[196:199]\n"
"s_mov_b32 s72, s31\n"
"v_mfma_f32_16x16x32_bf16 v[196:199], a[72:75], a[144:147], v[196:199]\n"
"v_mfma_f32_16x16x32_bf16 a[20:23], a[72:75], a[152:155], a[20:23]\n"
"s_add_u32 s34, s34, 8192\n"
"v_mfma_f32_16x16x32_bf16 a[20:23], a[76:79], a[156:159], a[20:23]\n"
"v_mfma_f32_16x16x32_bf16 a[16:19], a[68:71], a[156:159], a[16:19]\n"
"s_cmp_lg_u32 s29, 0\n"
"s_cselect_b32 s31, s69, s73\n"
"v_mfma_f32_16x16x32_bf16 a[16:19], a[64:67], a[152:155], a[16:19]\n"
"s_sub_u32 s54, s54, s73\n"
"s_subb_u32 s55, s55, 0\n"
"s_waitcnt vmcnt(12)\n"
"v_mfma_f32_16x16x32_bf16 v[200:203], a[80:83], a[96:99], v[200:203]\n"
"s_add_u32 s54, s54, s31\n"
"s_addc_u32 s55, s55, 0\n"
"v_mfma_f32_16x16x32_bf16 v[200:203], a[84:87], a[100:103], v[200:203]\n"
"global_load_dwordx4 a[64:67], v0, s[34:35] offset:-4096 sc1\n"
"v_mfma_f32_16x16x32_bf16 v[228:231], a[92:95], a[100:103], v[228:231]\n"
"s_mov_b32 s73, s31\n"
"ds_read_b128 a[100:103], v5 offset:1024\n"
"v_mfma_f32_16x16x32_bf16 v[228:231], a[88:91], a[96:99], v[228:231]\n"
"ds_read_b128 a[96:99], v5 offset:0\n"
"v_mfma_f32_16x16x32_bf16 v[232:235], a[88:91], a[104:107], v[232:235]\n"
"v_mfma_f32_16x16x32_bf16 v[232:235], a[92:95], a[108:111], v[232:235]\n"
"v_mfma_f32_16x16x32_bf16 v[204:207], a[84:87], a[108:111], v[204:207]\n"
"ds_read_b128 a[108:111], v5 offset:3072\n"
"v_mfma_f32_16x16x32_bf16 v[204:207], a[80:83], a[104:107], v[204:207]\n"
"ds_read_b128 a[104:107], v5 offset:2048\n"
"v_mfma_f32_16x16x32_bf16 v[208:211], a[80:83], a[112:115], v[208:211]\n"
"v_mfma_f32_16x16x32_bf16 v[208:211], a[84:87], a[116:119], v[208:211]\n"
"global_load_dwordx4 a[68:71], v0, s[34:35] offset:-3072 sc1\n"
"v_mfma_f32_16x16x32_bf16 v[236:239], a[92:95], a[116:119], v[236:239]\n"
"ds_read_b128 a[116:119], v5 offset:5120\n"
"v_mfma_f32_16x16x32_bf16 v[236:239], a[88:91], a[112:115], v[236:239]\n"
"ds_read_b128 a[112:115], v5 offset:4096\n"
"v_mfma_f32_16x16x32_bf16 v[240:243], a[88:91], a[120:123], v[240:243]\n"
"v_mfma_f32_16x16x32_bf16 v[240:243], a[92:95], a[124:127], v[240:243]\n"
"v_mfma_f32_16x16x32_bf16 v[212:215], a[84:87], a[124:127], v[212:215]\n"
"ds_read_b128 a[124:127], v5 offset:7168\n"
"v_mfma_f32_16x16x32_bf16 v[212:215], a[80:83], a[120:123], v[212:215]\n"
"ds_read_b128 a[120:123], v5 offset:6144\n"
"v_mfma_f32_16x16x32_bf16 v[216:219], a[80:83], a[128:131], v[216:219]\n"
"v_mfma_f32_16x16x32_bf16 v[216:219], a[84:87], a[132:135], v[216:219]\n"
"global_load_dwordx4 a[72:75], v0, s[34:35] offset:-2048 sc1\n"
"v_mfma_f32_16x16x32_bf16 v[244:247], a[92:95], a[132:135], v[244:247]\n"
"ds_read_b128 a[132:135], v5 offset:9216\n"
"v_mfma_f32_16x16x32_bf16 v[244:247], a[88:91], a[128:131], v[244:247]\n"
"ds_read_b128 a[128:131], v5 offset:8192\n"
"v_mfma_f32_16x16x32_bf16 v[248:251], a[88:91], a[136:139], v[248:251]\n"
"v_mfma_f32_16x16x32_bf16 v[248:251], a[92:95], a[140:143], v[248:251]\n"
"v_mfma_f32_16x16x32_bf16 v[220:223], a[84:87], a[140:143], v[220:223]\n"
"ds_read_b128 a[140:143], v5 offset:11264\n"
"v_mfma_f32_16x16x32_bf16 v[220:223], a[80:83], a[136:139], v[220:223]\n"
"ds_read_b128 a[136:139], v5 offset:10240\n"
"v_mfma_f32_16x16x32_bf16 v[224:227], a[80:83], a[144:147], v[224:227]\n"
"v_mfma_f32_16x16x32_bf16 v[224:227], a[84:87], a[148:151], v[224:227]\n"
"global_load_dwordx4 a[76:79], v0, s[34:35] offset:-1024 sc1\n"
"v_mfma_f32_16x16x32_bf16 v[252:255], a[92:95], a[148:151], v[252:255]\n"
"ds_read_b128 a[148:151], v5 offset:13312\n"
"v_mfma_f32_16x16x32_bf16 v[252:255], a[88:91], a[144:147], v[252:255]\n"
"ds_read_b128 a[144:147], v5 offset:12288\n"
"v_mfma_f32_16x16x32_bf16 a[28:31], a[88:91], a[152:155], a[28:31]\n"
"v_mfma_f32_16x16x32_bf16 a[28:31], a[92:95], a[156:159], a[28:31]\n"
"v_mfma_f32_16x16x32_bf16 a[24:27], a[84:87], a[156:159], a[24:27]\n"
"ds_read_b128 a[156:159], v5 offset:15360\n"
"v_mfma_f32_16x16x32_bf16 a[24:27], a[80:83], a[152:155], a[24:27]\n"
"s_waitcnt vmcnt(9) lgkmcnt(11)\n"
"ds_read_b128 a[152:155], v5 offset:14336\n"
"v_mfma_f32_16x16x32_bf16 v[32:35], a[32:35], a[96:99], v[32:35]\n"
"v_mfma_f32_16x16x32_bf16 v[32:35], a[36:39], a[100:103], v[32:35]\n"
"global_load_dwordx4 a[80:83], v0, s[34:35] offset:0 sc1\n"
"v_mfma_f32_16x16x32_bf16 v[60:63], a[44:47], a[100:103], v[60:63]\n"
"v_mfma_f32_16x16x32_bf16 v[60:63], a[40:43], a[96:99], v[60:63]\n"
"v_mfma_f32_16x16x32_bf16 v[64:67], a[40:43], a[104:107], v[64:67]\n"
"v_mfma_f32_16x16x32_bf16 v[64:67], a[44:47], a[108:111], v[64:67]\n"
"v_mfma_f32_16x16x32_bf16 v[36:39], a[36:39], a[108:111], v[36:39]\n"
"v_mfma_f32_16x16x32_bf16 v[36:39], a[32:35], a[104:107], v[36:39]\n"
"s_waitcnt lgkmcnt(8)\n"
"v_mfma_f32_16x16x32_bf16 v[40:43], a[32:35], a[112:115], v[40:43]\n"
"v_mfma_f32_16x16x32_bf16 v[40:43], a[36:39], a[116:119], v[40:43]\n"
"global_load_dwordx4 a[84:87], v0, s[34:35] offset:1024 sc1\n"
"v_mfma_f32_16x16x32_bf16 v[68:71], a[44:47], a[116:119], v[68:71]\n"
"v_mfma_f32_16x16x32_bf16 v[68:71], a[40:43], a[112:115], v[68:71]\n"
"v_mfma_f32_16x16x32_bf16 v[72:75], a[40:43], a[120:123], v[72:75]\n"
"v_mfma_f32_16x16x32_bf16 v[72:75], a[44:47], a[124:127], v[72:75]\n"
"v_mfma_f32_16x16x32_bf16 v[44:47], a[36:39], a[124:127], v[44:47]\n"
"v_mfma_f32_16x16x32_bf16 v[44:47], a[32:35], a[120:123], v[44:47]\n"
"s_waitcnt lgkmcnt(4)\n"
"v_mfma_f32_16x16x32_bf16 v[48:51], a[32:35], a[128:131], v[48:51]\n"
"s_lshr_b32 s66, 20, s62\n"
"v_mfma_f32_16x16x32_bf16 v[48:51], a[36:39], a[132:135], v[48:51]\n"
"global_load_dwordx4 a[88:91], v0, s[34:35] offset:2048 sc1\n"
"v_mfma_f32_16x16x32_bf16 v[76:79], a[44:47], a[132:135], v[76:79]\n"
"s_and_b32 s66, s66, 1\n"
"s_cselect_b32 s70, 4294574080, 0\n"
"s_cselect_b32 s71, -1, 0\n"
"s_cselect_b32 s66, 0, 131072\n"
"v_mfma_f32_16x16x32_bf16 v[76:79], a[40:43], a[128:131], v[76:79]\n"
"v_mfma_f32_16x16x32_bf16 v[80:83], a[40:43], a[136:139], v[80:83]\n"
"s_add_u32 s70, s70, s66\n"
"v_mfma_f32_16x16x32_bf16 v[80:83], a[44:47], a[140:143], v[80:83]\n"
"v_mfma_f32_16x16x32_bf16 v[52:55], a[36:39], a[140:143], v[52:55]\n"
"v_mfma_f32_16x16x32_bf16 v[52:55], a[32:35], a[136:139], v[52:55]\n"
"s_waitcnt lgkmcnt(0)\n"
"v_mfma_f32_16x16x32_bf16 v[56:59], a[32:35], a[144:147], v[56:59]\n"
"v_mfma_f32_16x16x32_bf16 v[56:59], a[36:39], a[148:151], v[56:59]\n"
"global_load_dwordx4 a[92:95], v0, s[34:35] offset:3072 sc1\n"
"v_mfma_f32_16x16x32_bf16 v[84:87], a[44:47], a[148:151], v[84:87]\n"
"v_mfma_f32_16x16x32_bf16 v[84:87], a[40:43], a[144:147], v[84:87]\n"
"v_mfma_f32_16x16x32_bf16 a[4:7], a[40:43], a[152:155], a[4:7]\n"
"s_add_u32 s34, s34, 8192\n"
"v_mfma_f32_16x16x32_bf16 a[4:7], a[44:47], a[156:159], a[4:7]\n"
"v_mfma_f32_16x16x32_bf16 a[0:3], a[36:39], a[156:159], a[0:3]\n"
"v_mfma_f32_16x16x32_bf16 a[0:3], a[32:35], a[152:155], a[0:3]\n"
"s_waitcnt vmcnt(8)\n"
"v_mfma_f32_16x16x32_bf16 v[88:91], a[48:51], a[96:99], v[88:91]\n"
"v_mfma_f32_16x16x32_bf16 v[88:91], a[52:55], a[100:103], v[88:91]\n"
"global_load_dwordx4 a[32:35], v0, s[34:35] offset:-4096 sc1\n"
"v_mfma_f32_16x16x32_bf16 v[116:119], a[60:63], a[100:103], v[116:119]\n"
"v_mfma_f32_16x16x32_bf16 v[116:119], a[56:59], a[96:99], v[116:119]\n"
"v_mfma_f32_16x16x32_bf16 v[120:123], a[56:59], a[104:107], v[120:123]\n"
"v_mfma_f32_16x16x32_bf16 v[120:123], a[60:63], a[108:111], v[120:123]\n"
"v_mfma_f32_16x16x32_bf16 v[92:95], a[52:55], a[108:111], v[92:95]\n"
"v_mfma_f32_16x16x32_bf16 v[92:95], a[48:51], a[104:107], v[92:95]\n"
"v_mfma_f32_16x16x32_bf16 v[96:99], a[48:51], a[112:115], v[96:99]\n"
"v_mfma_f32_16x16x32_bf16 v[96:99], a[52:55], a[116:119], v[96:99]\n"
"global_load_dwordx4 a[36:39], v0, s[34:35] offset:-3072 sc1\n"
"v_mfma_f32_16x16x32_bf16 v[124:127], a[60:63], a[116:119], v[124:127]\n"
"buffer_load_dwordx4 v[12:15], v0, s[56:59], %25 offen sc1\n"
"v_mfma_f32_16x16x32_bf16 v[124:127], a[56:59], a[112:115], v[124:127]\n"
"v_mfma_f32_16x16x32_bf16 v[128:131], a[56:59], a[120:123], v[128:131]\n"
"v_mfma_f32_16x16x32_bf16 v[128:131], a[60:63], a[124:127], v[128:131]\n"
"v_mfma_f32_16x16x32_bf16 v[100:103], a[52:55], a[124:127], v[100:103]\n"
"v_mfma_f32_16x16x32_bf16 v[100:103], a[48:51], a[120:123], v[100:103]\n"
"v_mfma_f32_16x16x32_bf16 v[104:107], a[48:51], a[128:131], v[104:107]\n"
"v_mfma_f32_16x16x32_bf16 v[104:107], a[52:55], a[132:135], v[104:107]\n"
"global_load_dwordx4 a[40:43], v0, s[34:35] offset:-2048 sc1\n"
"v_mfma_f32_16x16x32_bf16 v[132:135], a[60:63], a[132:135], v[132:135]\n"
"v_mfma_f32_16x16x32_bf16 v[132:135], a[56:59], a[128:131], v[132:135]\n"
"v_mfma_f32_16x16x32_bf16 v[136:139], a[56:59], a[136:139], v[136:139]\n"
"v_mfma_f32_16x16x32_bf16 v[136:139], a[60:63], a[140:143], v[136:139]\n"
"v_mfma_f32_16x16x32_bf16 v[108:111], a[52:55], a[140:143], v[108:111]\n"
"v_mfma_f32_16x16x32_bf16 v[108:111], a[48:51], a[136:139], v[108:111]\n"
"v_mfma_f32_16x16x32_bf16 v[112:115], a[48:51], a[144:147], v[112:115]\n"
"v_mfma_f32_16x16x32_bf16 v[112:115], a[52:55], a[148:151], v[112:115]\n"
"global_load_dwordx4 a[44:47], v0, s[34:35] offset:-1024 sc1\n"
"v_mfma_f32_16x16x32_bf16 v[140:143], a[60:63], a[148:151], v[140:143]\n"
"v_mfma_f32_16x16x32_bf16 v[140:143], a[56:59], a[144:147], v[140:143]\n"
"v_mfma_f32_16x16x32_bf16 a[12:15], a[56:59], a[152:155], a[12:15]\n"
"v_mfma_f32_16x16x32_bf16 a[12:15], a[60:63], a[156:159], a[12:15]\n"
"v_mfma_f32_16x16x32_bf16 a[8:11], a[52:55], a[156:159], a[8:11]\n"
"v_mfma_f32_16x16x32_bf16 a[8:11], a[48:51], a[152:155], a[8:11]\n"
"s_waitcnt vmcnt(9)\n"
"v_mfma_f32_16x16x32_bf16 v[144:147], a[64:67], a[96:99], v[144:147]\n"
"v_mfma_f32_16x16x32_bf16 v[144:147], a[68:71], a[100:103], v[144:147]\n"
"global_load_dwordx4 a[48:51], v0, s[34:35] offset:0 sc1\n"
"v_mfma_f32_16x16x32_bf16 v[172:175], a[76:79], a[100:103], v[172:175]\n"
"v_mfma_f32_16x16x32_bf16 v[172:175], a[72:75], a[96:99], v[172:175]\n"
"v_mfma_f32_16x16x32_bf16 v[176:179], a[72:75], a[104:107], v[176:179]\n"
"v_mfma_f32_16x16x32_bf16 v[176:179], a[76:79], a[108:111], v[176:179]\n"
"v_mfma_f32_16x16x32_bf16 v[148:151], a[68:71], a[108:111], v[148:151]\n"
"v_mfma_f32_16x16x32_bf16 v[148:151], a[64:67], a[104:107], v[148:151]\n"
"v_mfma_f32_16x16x32_bf16 v[152:155], a[64:67], a[112:115], v[152:155]\n"
"v_mfma_f32_16x16x32_bf16 v[152:155], a[68:71], a[116:119], v[152:155]\n"
"global_load_dwordx4 a[52:55], v0, s[34:35] offset:1024 sc1\n"
"v_mfma_f32_16x16x32_bf16 v[180:183], a[76:79], a[116:119], v[180:183]\n"
"v_mfma_f32_16x16x32_bf16 v[180:183], a[72:75], a[112:115], v[180:183]\n"
"v_mfma_f32_16x16x32_bf16 v[184:187], a[72:75], a[120:123], v[184:187]\n"
"v_mfma_f32_16x16x32_bf16 v[184:187], a[76:79], a[124:127], v[184:187]\n"
"v_mfma_f32_16x16x32_bf16 v[156:159], a[68:71], a[124:127], v[156:159]\n"
"v_mfma_f32_16x16x32_bf16 v[156:159], a[64:67], a[120:123], v[156:159]\n"
"v_mfma_f32_16x16x32_bf16 v[160:163], a[64:67], a[128:131], v[160:163]\n"
"v_mfma_f32_16x16x32_bf16 v[160:163], a[68:71], a[132:135], v[160:163]\n"
"global_load_dwordx4 a[56:59], v0, s[34:35] offset:2048 sc1\n"
"v_mfma_f32_16x16x32_bf16 v[188:191], a[76:79], a[132:135], v[188:191]\n"
"v_mfma_f32_16x16x32_bf16 v[188:191], a[72:75], a[128:131], v[188:191]\n"
"v_mfma_f32_16x16x32_bf16 v[192:195], a[72:75], a[136:139], v[192:195]\n"
"v_mfma_f32_16x16x32_bf16 v[192:195], a[76:79], a[140:143], v[192:195]\n"
"v_mfma_f32_16x16x32_bf16 v[164:167], a[68:71], a[140:143], v[164:167]\n"
"s_add_u32 s67, s78, s62\n"
"v_mfma_f32_16x16x32_bf16 v[164:167], a[64:67], a[136:139], v[164:167]\n"
"v_mfma_f32_16x16x32_bf16 v[168:171], a[64:67], a[144:147], v[168:171]\n"
"s_add_u32 s67, s67, 1\n"
"v_mfma_f32_16x16x32_bf16 v[168:171], a[68:71], a[148:151], v[168:171]\n"
"global_load_dwordx4 a[60:63], v0, s[34:35] offset:3072 sc1\n"
"v_mfma_f32_16x16x32_bf16 v[196:199], a[76:79], a[148:151], v[196:199]\n"
"v_mfma_f32_16x16x32_bf16 v[196:199], a[72:75], a[144:147], v[196:199]\n"
"v_mfma_f32_16x16x32_bf16 a[20:23], a[72:75], a[152:155], a[20:23]\n"
"s_add_u32 s34, s34, 8192\n"
"v_mfma_f32_16x16x32_bf16 a[20:23], a[76:79], a[156:159], a[20:23]\n"
"v_mfma_f32_16x16x32_bf16 a[16:19], a[68:71], a[156:159], a[16:19]\n"
"v_mfma_f32_16x16x32_bf16 a[16:19], a[64:67], a[152:155], a[16:19]\n"
"s_waitcnt vmcnt(9)\n"
"v_mfma_f32_16x16x32_bf16 v[200:203], a[80:83], a[96:99], v[200:203]\n"
"v_mfma_f32_16x16x32_bf16 v[200:203], a[84:87], a[100:103], v[200:203]\n"
"global_load_dwordx4 a[64:67], v0, s[34:35] offset:-4096 sc1\n"
"v_mfma_f32_16x16x32_bf16 v[228:231], a[92:95], a[100:103], v[228:231]\n"
"ds_read_b128 a[100:103], v2 offset:17408\n"
"v_mfma_f32_16x16x32_bf16 v[228:231], a[88:91], a[96:99], v[228:231]\n"
"ds_read_b128 a[96:99], v2 offset:16384\n"
"v_mfma_f32_16x16x32_bf16 v[232:235], a[88:91], a[104:107], v[232:235]\n"
"v_mfma_f32_16x16x32_bf16 v[232:235], a[92:95], a[108:111], v[232:235]\n"
"v_mfma_f32_16x16x32_bf16 v[204:207], a[84:87], a[108:111], v[204:207]\n"
"ds_read_b128 a[108:111], v2 offset:19456\n"
"v_mfma_f32_16x16x32_bf16 v[204:207], a[80:83], a[104:107], v[204:207]\n"
"ds_read_b128 a[104:107], v2 offset:18432\n"
"v_mfma_f32_16x16x32_bf16 v[208:211], a[80:83], a[112:115], v[208:211]\n"
"v_mfma_f32_16x16x32_bf16 v[208:211], a[84:87], a[116:119], v[208:211]\n"
"global_load_dwordx4 a[68:71], v0, s[34:35] offset:-3072 sc1\n"
"v_mfma_f32_16x16x32_bf16 v[236:239], a[92:95], a[116:119], v[236:239]\n"
"ds_read_b128 a[116:119], v2 offset:21504\n"
"v_mfma_f32_16x16x32_bf16 v[236:239], a[88:91], a[112:115], v[236:239]\n"
"ds_read_b128 a[112:115], v2 offset:20480\n"
"v_mfma_f32_16x16x32_bf16 v[240:243], a[88:91], a[120:123], v[240:243]\n"
"v_mfma_f32_16x16x32_bf16 v[240:243], a[92:95], a[124:127], v[240:243]\n"
"v_mfma_f32_16x16x32_bf16 v[212:215], a[84:87], a[124:127], v[212:215]\n"
"ds_read_b128 a[124:127], v2 offset:23552\n"
"v_mfma_f32_16x16x32_bf16 v[212:215], a[80:83], a[120:123], v[212:215]\n"
"ds_read_b128 a[120:123], v2 offset:22528\n"
"v_mfma_f32_16x16x32_bf16 v[216:219], a[80:83], a[128:131], v[216:219]\n"
"v_mfma_f32_16x16x32_bf16 v[216:219], a[84:87], a[132:135], v[216:219]\n"
"global_load_dwordx4 a[72:75], v0, s[34:35] offset:-2048 sc1\n"
"v_mfma_f32_16x16x32_bf16 v[244:247], a[92:95], a[132:135], v[244:247]\n"
"ds_read_b128 a[132:135], v2 offset:25600\n"
"v_mfma_f32_16x16x32_bf16 v[244:247], a[88:91], a[128:131], v[244:247]\n"
"ds_read_b128 a[128:131], v2 offset:24576\n"
"v_mfma_f32_16x16x32_bf16 v[248:251], a[88:91], a[136:139], v[248:251]\n"
"v_mfma_f32_16x16x32_bf16 v[248:251], a[92:95], a[140:143], v[248:251]\n"
"v_mfma_f32_16x16x32_bf16 v[220:223], a[84:87], a[140:143], v[220:223]\n"
"ds_read_b128 a[140:143], v2 offset:27648\n"
"v_mfma_f32_16x16x32_bf16 v[220:223], a[80:83], a[136:139], v[220:223]\n"
"ds_read_b128 a[136:139], v2 offset:26624\n"
"v_mfma_f32_16x16x32_bf16 v[224:227], a[80:83], a[144:147], v[224:227]\n"
"v_mfma_f32_16x16x32_bf16 v[224:227], a[84:87], a[148:151], v[224:227]\n"
"global_load_dwordx4 a[76:79], v0, s[34:35] offset:-1024 sc1\n"
"v_mfma_f32_16x16x32_bf16 v[252:255], a[92:95], a[148:151], v[252:255]\n"
"ds_read_b128 a[148:151], v2 offset:29696\n"
"v_mfma_f32_16x16x32_bf16 v[252:255], a[88:91], a[144:147], v[252:255]\n"
"ds_read_b128 a[144:147], v2 offset:28672\n"
"v_mfma_f32_16x16x32_bf16 a[28:31], a[88:91], a[152:155], a[28:31]\n"
"v_mfma_f32_16x16x32_bf16 a[28:31], a[92:95], a[156:159], a[28:31]\n"
"v_mfma_f32_16x16x32_bf16 a[24:27], a[84:87], a[156:159], a[24:27]\n"
"ds_read_b128 a[156:159], v2 offset:31744\n"
"v_mfma_f32_16x16x32_bf16 a[24:27], a[80:83], a[152:155], a[24:27]\n"
"ds_read_b128 a[152:155], v2 offset:30720\n"
"s_waitcnt vmcnt(8) lgkmcnt(12)\n"
"v_mfma_f32_16x16x32_bf16 v[32:35], a[32:35], a[96:99], v[32:35]\n"
"v_mfma_f32_16x16x32_bf16 v[32:35], a[36:39], a[100:103], v[32:35]\n"
"global_load_dwordx4 a[80:83], v0, s[34:35] offset:0 sc1\n"
"v_mfma_f32_16x16x32_bf16 v[60:63], a[44:47], a[100:103], v[60:63]\n"
"s_barrier\n"
"v_mfma_f32_16x16x32_bf16 v[60:63], a[40:43], a[96:99], v[60:63]\n"
"s_add_u32 s48, s48, s70\n"
"s_addc_u32 s49, s49, s71\n"
"v_mfma_f32_16x16x32_bf16 v[64:67], a[40:43], a[104:107], v[64:67]\n"
"v_mfma_f32_16x16x32_bf16 v[64:67], a[44:47], a[108:111], v[64:67]\n"
"v_mfma_f32_16x16x32_bf16 v[36:39], a[36:39], a[108:111], v[36:39]\n"
"s_add_u32 s52, s52, s70\n"
"s_addc_u32 s53, s53, s71\n"
"v_subrev_u32 v12, s67, v12\n"
"v_mfma_f32_16x16x32_bf16 v[36:39], a[32:35], a[104:107], v[36:39]\n"
"s_waitcnt lgkmcnt(8)\n"
"v_xor_b32 v13, s79, v13\n"
"v_mfma_f32_16x16x32_bf16 v[40:43], a[32:35], a[112:115], v[40:43]\n"
"s_add_u32 s54, s54, s70\n"
"s_addc_u32 s55, s55, s71\n"
"v_subrev_u32 v14, s67, v14\n"
"v_mfma_f32_16x16x32_bf16 v[40:43], a[36:39], a[116:119], v[40:43]\n"
"global_load_dwordx4 a[84:87], v0, s[34:35] offset:1024 sc1\n"
"v_xor_b32 v15, s79, v15\n"
"v_mfma_f32_16x16x32_bf16 v[68:71], a[44:47], a[116:119], v[68:71]\n"
"v_or_b32 v12, v12, v14\n"
"v_mfma_f32_16x16x32_bf16 v[68:71], a[40:43], a[112:115], v[68:71]\n"
"v_or_b32 v13, v13, v15\n"
"v_mfma_f32_16x16x32_bf16 v[72:75], a[40:43], a[120:123], v[72:75]\n"
"v_or_b32 v12, v12, v13\n"
"v_mfma_f32_16x16x32_bf16 v[72:75], a[44:47], a[124:127], v[72:75]\n"
"s_add_u32 s61, s61, 2048\n"
"v_cmp_lt_u32 vcc, 15, v12\n"
"v_mfma_f32_16x16x32_bf16 v[44:47], a[36:39], a[124:127], v[44:47]\n"
"s_cbranch_vccnz L_slow2\n"
"L_back2:\n"
"v_mfma_f32_16x16x32_bf16 v[44:47], a[32:35], a[120:123], v[44:47]\n"
"s_waitcnt lgkmcnt(4)\n"
"v_mfma_f32_16x16x32_bf16 v[48:51], a[32:35], a[128:131], v[48:51]\n"
"v_lshlrev_b32 v16, 16, v18\n"
"v_mfma_f32_16x16x32_bf16 v[48:51], a[36:39], a[132:135], v[48:51]\n"
"global_load_dwordx4 a[88:91], v0, s[34:35] offset:2048 sc1\n"
"v_and_b32 v17, s63, v18\n"
"v_mfma_f32_16x16x32_bf16 v[76:79], a[44:47], a[132:135], v[76:79]\n"
"v_lshlrev_b32 v18, 16, v19\n"
"v_mfma_f32_16x16x32_bf16 v[76:79], a[40:43], a[128:131], v[76:79]\n"
"v_and_b32 v19, s63, v19\n"
"v_mfma_f32_16x16x32_bf16 v[80:83], a[40:43], a[136:139], v[80:83]\n"
"v_add_f32 v144, v144, v16\n"
"v_add_f32 v145, v145, v17\n"
"v_mfma_f32_16x16x32_bf16 v[80:83], a[44:47], a[140:143], v[80:83]\n"
"v_add_f32 v146, v146, v18\n"
"v_mfma_f32_16x16x32_bf16 v[52:55], a[36:39], a[140:143], v[52:55]\n"
"v_add_f32 v147, v147, v19\n"
"v_mfma_f32_16x16x32_bf16 v[52:55], a[32:35], a[136:139], v[52:55]\n"
"s_waitcnt lgkmcnt(0)\n"
"v_add_f32 v148, v148, v16\n"
"v_mfma_f32_16x16x32_bf16 v[56:59], a[32:35], a[144:147], v[56:59]\n"
"v_add_f32 v149, v149, v17\n"
"v_mfma_f32_16x16x32_bf16 v[56:59], a[36:39], a[148:151], v[56:59]\n"
"global_load_dwordx4 a[92:95], v0, s[34:35] offset:3072 sc1\n"
"v_add_f32 v150, v150, v18\n"
"v_mfma_f32_16x16x32_bf16 v[84:87], a[44:47], a[148:151], v[84:87]\n"
"v_add_f32 v151, v151, v19\n"
"v_mfma_f32_16x16x32_bf16 v[84:87], a[40:43], a[144:147], v[84:87]\n"
"v_add_f32 v152, v152, v16\n"
"v_mfma_f32_16x16x32_bf16 a[4:7], a[40:43], a[152:155], a[4:7]\n"
"s_add_u32 s34, s34, 8192\n"
"v_add_f32 v153, v153, v17\n"
"v_mfma_f32_16x16x32_bf16 a[4:7], a[44:47], a[156:159], a[4:7]\n"
"v_add_f32 v154, v154, v18\n"
"v_mfma_f32_16x16x32_bf16 a[0:3], a[36:39], a[156:159], a[0:3]\n"
"v_add_f32 v155, v155, v19\n"
"v_add_f32 v156, v156, v16\n"
"v_mfma_f32_16x16x32_bf16 a[0:3], a[32:35], a[152:155], a[0:3]\n"
"s_waitcnt vmcnt(8)\n"
"v_add_f32 v157, v157, v17\n"
"v_mfma_f32_16x16x32_bf16 v[88:91], a[48:51], a[96:99], v[88:91]\n"
"v_add_f32 v158, v158, v18\n"
"v_mfma_f32_16x16x32_bf16 v[88:91], a[52:55], a[100:103], v[88:91]\n"
"global_load_dwordx4 a[32:35], v0, s[34:35] offset:-4096 sc1\n"
"v_add_f32 v159, v159, v19\n"
"v_mfma_f32_16x16x32_bf16 v[116:119], a[60:63], a[100:103], v[116:119]\n"
"ds_read_b128 a[100:103], v3 offset:17408\n"
"v_add_f32 v160, v160, v16\n"
"v_mfma_f32_16x16x32_bf16 v[116:119], a[56:59], a[96:99], v[116:119]\n"
"ds_read_b128 a[96:99], v3 offset:16384\n"
"v_add_f32 v161, v161, v17\n"
"v_mfma_f32_16x16x32_bf16 v[120:123], a[56:59], a[104:107], v[120:123]\n"
"v_add_f32 v162, v162, v18\n"
"v_mfma_f32_16x16x32_bf16 v[120:123], a[60:63], a[108:111], v[120:123]\n"
"v_add_f32 v163, v163, v19\n"
"v_mfma_f32_16x16x32_bf16 v[92:95], a[52:55], a[108:111], v[92:95]\n"
"ds_read_b128 a[108:111], v3 offset:19456\n"
"v_add_f32 v164, v164, v16\n"
"v_mfma_f32_16x16x32_bf16 v[92:95], a[48:51], a[104:107], v[92:95]\n"
"ds_read_b128 a[104:107], v3 offset:18432\n"
"v_add_f32 v165, v165, v17\n"
"v_mfma_f32_16x16x32_bf16 v[96:99], a[48:51], a[112:115], v[96:99]\n"
"v_add_f32 v166, v166, v18\n"
"v_add_f32 v167, v167, v19\n"
"v_mfma_f32_16x16x32_bf16 v[96:99], a[52:55], a[116:119], v[96:99]\n"
"global_load_dwordx4 a[36:39], v0, s[34:35] offset:-3072 sc1\n"
"v_add_f32 v168, v168, v16\n"
"v_mfma_f32_16x16x32_bf16 v[124:127], a[60:63], a[116:119], v[124:127]\n"
"ds_read_b128 a[116:119], v3 offset:21504\n"
"v_add_f32 v169, v169, v17\n"
"v_mfma_f32_16x16x32_bf16 v[124:127], a[56:59], a[112:115], v[124:127]\n"
"ds_read_b128 a[112:115], v3 offset:20480\n"
"v_add_f32 v170, v170, v18\n"
"v_mfma_f32_16x16x32_bf16 v[128:131], a[56:59], a[120:123], v[128:131]\n"
"v_add_f32 v171, v171, v19\n"
"v_mfma_f32_16x16x32_bf16 v[128:131], a[60:63], a[124:127], v[128:131]\n"
"v_accvgpr_read_b32 v12, a16\n"
"v_mfma_f32_16x16x32_bf16 v[100:103], a[52:55], a[124:127], v[100:103]\n"
"ds_read_b128 a[124:127], v3 offset:23552\n"
"v_accvgpr_read_b32 v13, a17\n"
"v_mfma_f32_16x16x32_bf16 v[100:103], a[48:51], a[120:123], v[100:103]\n"
"ds_read_b128 a[120:123], v3 offset:22528\n"
"v_accvgpr_read_b32 v14, a18\n"
"v_mfma_f32_16x16x32_bf16 v[104:107], a[48:51], a[128:131], v[104:107]\n"
"v_accvgpr_read_b32 v15, a19\n"
"v_add_f32 v12, v12, v16\n"
"v_mfma_f32_16x16x32_bf16 v[104:107], a[52:55], a[132:135], v[104:107]\n"
"global_load_dwordx4 a[40:43], v0, s[34:35] offset:-2048 sc1\n"
"v_add_f32 v13, v13, v17\n"
"v_mfma_f32_16x16x32_bf16 v[132:135], a[60:63], a[132:135], v[132:135]\n"
"ds_read_b128 a[132:135], v3 offset:25600\n"
"v_add_f32 v14, v14, v18\n"
"v_mfma_f32_16x16x32_bf16 v[132:135], a[56:59], a[128:131], v[132:135]\n"
"ds_read_b128 a[128:131], v3 offset:24576\n"
"v_add_f32 v15, v15, v19\n"
"v_mfma_f32_16x16x32_bf16 v[136:139], a[56:59], a[136:139], v[136:139]\n"
"v_accvgpr_write_b32 a16, v12\n"
"v_mfma_f32_16x16x32_bf16 v[136:139], a[60:63], a[140:143], v[136:139]\n"
"v_accvgpr_write_b32 a17, v13\n"
"v_mfma_f32_16x16x32_bf16 v[108:111], a[52:55], a[140:143], v[108:111]\n"
"ds_read_b128 a[140:143], v3 offset:27648\n"
"v_accvgpr_write_b32 a18, v14\n"
"v_mfma_f32_16x16x32_bf16 v[108:111], a[48:51], a[136:139], v[108:111]\n"
"ds_read_b128 a[136:139], v3 offset:26624\n"
"v_accvgpr_write_b32 a19, v15\n"
"v_mfma_f32_16x16x32_bf16 v[112:115], a[48:51], a[144:147], v[112:115]\n"
"v_lshlrev_b32 v20, 16, v22\n"
"v_and_b32 v21, s63, v22\n"
"v_mfma_f32_16x16x32_bf16 v[112:115], a[52:55], a[148:151], v[112:115]\n"
"global_load_dwordx4 a[44:47], v0, s[34:35] offset:-1024 sc1\n"
"v_lshlrev_b32 v22, 16, v23\n"
"v_mfma_f32_16x16x32_bf16 v[140:143], a[60:63], a[148:151], v[140:143]\n"
"ds_read_b128 a[148:151], v3 offset:29696\n"
"v_and_b32 v23, s63, v23\n"
"v_mfma_f32_16x16x32_bf16 v[140:143], a[56:59], a[144:147], v[140:143]\n"
"ds_read_b128 a[144:147], v3 offset:28672\n"
"v_add_f32 v172, v172, v20\n"
"v_mfma_f32_16x16x32_bf16 a[12:15], a[56:59], a[152:155], a[12:15]\n"
"v_add_f32 v173, v173, v21\n"
"v_mfma_f32_16x16x32_bf16 a[12:15], a[60:63], a[156:159], a[12:15]\n"
"v_add_f32 v174, v174, v22\n"
"v_mfma_f32_16x16x32_bf16 a[8:11], a[52:55], a[156:159], a[8:11]\n"
"ds_read_b128 a[156:159], v3 offset:31744\n"
"v_add_f32 v175, v175, v23\n"
"v_mfma_f32_16x16x32_bf16 a[8:11], a[48:51], a[152:155], a[8:11]\n"
"s_waitcnt vmcnt(8) lgkmcnt(11)\n"
"ds_read_b128 a[152:155], v3 offset:30720\n"
"v_add_f32 v176, v176, v20\n"
"v_mfma_f32_16x16x32_bf16 v[32:35], a[64:67], a[96:99], v[32:35]\n"
"v_add_f32 v177, v177, v21\n"
"v_mfma_f32_16x16x32_bf16 v[32:35], a[68:71], a[100:103], v[32:35]\n"
"global_load_dwordx4 a[48:51], v0, s[34:35] offset:0 sc1\n"
"v_add_f32 v178, v178, v22\n"
"v_mfma_f32_16x16x32_bf16 v[60:63], a[76:79], a[100:103], v[60:63]\n"
"v_add_f32 v179, v179, v23\n"
"v_mfma_f32_16x16x32_bf16 v[60:63], a[72:75], a[96:99], v[60:63]\n"
"v_add_f32 v180, v180, v20\n"
"v_add_f32 v181, v181, v21\n"
"v_mfma_f32_16x16x32_bf16 v[64:67], a[72:75], a[104:107], v[64:67]\n"
"v_add_f32 v182, v182, v22\n"
"v_mfma_f32_16x16x32_bf16 v[64:67], a[76:79], a[108:111], v[64:67]\n"
"v_add_f32 v183, v183, v23\n"
"v_mfma_f32_16x16x32_bf16 v[36:39], a[68:71], a[108:111], v[36:39]\n"
"v_add_f32 v184, v184, v20\n"
"v_mfma_f32_16x16x32_bf16 v[36:39], a[64:67], a[104:107], v[36:39]\n"
"s_waitcnt lgkmcnt(8)\n"
"v_add_f32 v185, v185, v21\n"
"v_mfma_f32_16x16x32_bf16 v[40:43], a[64:67], a[112:115], v[40:43]\n"
"v_add_f32 v186, v186, v22\n"
"v_mfma_f32_16x16x32_bf16 v[40:43], a[68:71], a[116:119], v[40:43]\n"
"global_load_dwordx4 a[52:55], v0, s[34:35] offset:1024 sc1\n"
"v_add_f32 v187, v187, v23\n"
"v_mfma_f32_16x16x32_bf16 v[68:71], a[76:79], a[116:119], v[68:71]\n"
"v_add_f32 v188, v188, v20\n"
"v_mfma_f32_16x16x32_bf16 v[68:71], a[72:75], a[112:115], v[68:71]\n"
"v_add_f32 v189, v189, v21\n"
"v_mfma_f32_16x16x32_bf16 v[72:75], a[72:75], a[120:123], v[72:75]\n"
"v_add_f32 v190, v190, v22\n"
"v_add_f32 v191, v191, v23\n"
"v_mfma_f32_16x16x32_bf16 v[72:75], a[76:79], a[124:127], v[72:75]\n"
"v_add_f32 v192, v192, v20\n"
"v_mfma_f32_16x16x32_bf16 v[44:47], a[68:71], a[124:127], v[44:47]\n"
"v_add_f32 v193, v193, v21\n"
"v_mfma_f32_16x16x32_bf16 v[44:47], a[64:67], a[120:123], v[44:47]\n"
"s_waitcnt lgkmcnt(4)\n"
"v_add_f32 v194, v194, v22\n"
"v_mfma_f32_16x16x32_bf16 v[48:51], a[64:67], a[128:131], v[48:51]\n"
"v_add_f32 v195, v195, v23\n"
"v_mfma_f32_16x16x32_bf16 v[48:51], a[68:71], a[132:135], v[48:51]\n"
"global_load_dwordx4 a[56:59], v0, s[34:35] offset:2048 sc1\n"
"v_add_f32 v196, v196, v20\n"
"v_mfma_f32_16x16x32_bf16 v[76:79], a[76:79], a[132:135], v[76:79]\n"
"v_add_f32 v197, v197, v21\n"
"v_mfma_f32_16x16x32_bf16 v[76:79], a[72:75], a[128:131], v[76:79]\n"
"v_add_f32 v198, v198, v22\n"
"v_mfma_f32_16x16x32_bf16 v[80:83], a[72:75], a[136:139], v[80:83]\n"
"v_add_f32 v199, v199, v23\n"
"v_mfma_f32_16x16x32_bf16 v[80:83], a[76:79], a[140:143], v[80:83]\n"
"v_accvgpr_read_b32 v12, a20\n"
"v_mfma_f32_16x16x32_bf16 v[52:55], a[68:71], a[140:143], v[52:55]\n"
"v_accvgpr_read_b32 v13, a21\n"
"v_accvgpr_read_b32 v14, a22\n"
"v_mfma_f32_16x16x32_bf16 v[52:55], a[64:67], a[136:139], v[52:55]\n"
"s_waitcnt lgkmcnt(0)\n"
"v_accvgpr_read_b32 v15, a23\n"
"v_mfma_f32_16x16x32_bf16 v[56:59], a[64:67], a[144:147], v[56:59]\n"
"v_add_f32 v12, v12, v20\n"
"v_mfma_f32_16x16x32_bf16 v[56:59], a[68:71], a[148:151], v[56:59]\n"
"global_load_dwordx4 a[60:63], v0, s[34:35] offset:3072 sc1\n"
"v_add_f32 v13, v13, v21\n"
"v_mfma_f32_16x16x32_bf16 v[84:87], a[76:79], a[148:151], v[84:87]\n"
"v_add_f32 v14, v14, v22\n"
"v_mfma_f32_16x16x32_bf16 v[84:87], a[72:75], a[144:147], v[84:87]\n"
"v_add_f32 v15, v15, v23\n"
"v_mfma_f32_16x16x32_bf16 a[4:7], a[72:75], a[152:155], a[4:7]\n"
"s_add_u32 s34, s34, 8192\n"
"v_accvgpr_write_b32 a20, v12\n"
"v_mfma_f32_16x16x32_bf16 a[4:7], a[76:79], a[156:159], a[4:7]\n"
"v_accvgpr_write_b32 a21, v13\n"
"v_mfma_f32_16x16x32_bf16 a[0:3], a[68:71], a[156:159], a[0:3]\n"
"v_accvgpr_write_b32 a22, v14\n"
"v_mfma_f32_16x16x32_bf16 a[0:3], a[64:67], a[152:155], a[0:3]\n"
"s_waitcnt vmcnt(8)\n"
"v_accvgpr_write_b32 a23, v15\n"
"v_lshlrev_b32 v24, 16, v26\n"
"v_mfma_f32_16x16x32_bf16 v[88:91], a[80:83], a[96:99], v[88:91]\n"
"v_and_b32 v25, s63, v26\n"
"v_mfma_f32_16x16x32_bf16 v[88:91], a[84:87], a[100:103], v[88:91]\n"
"global_load_dwordx4 a[64:67], v0, s[34:35] offset:-4096 sc1\n"
"v_lshlrev_b32 v26, 16, v27\n"
"v_mfma_f32_16x16x32_bf16 v[116:119], a[92:95], a[100:103], v[116:119]\n"
"ds_read_b128 a[100:103], v4 offset:17408\n"
"v_and_b32 v27, s63, v27\n"
"v_mfma_f32_16x16x32_bf16 v[116:119], a[88:91], a[96:99], v[116:119]\n"
"L_st7939:\n"
"s_cbranch_execz L_retB\n"
"ds_read_b128 a[96:99], v4 offset:16384\n"
"v_add_f32 v200, v200, v24\n"
"v_mfma_f32_16x16x32_bf16 v[120:123], a[88:91], a[104:107], v[120:123]\n"
"v_add_f32 v201, v201, v25\n"
"v_mfma_f32_16x16x32_bf16 v[120:123], a[92:95], a[108:111], v[120:123]\n"
"v_add_f32 v202, v202, v26\n"
"v_mfma_f32_16x16x32_bf16 v[92:95], a[84:87], a[108:111], v[92:95]\n"
"ds_read_b128 a[108:111], v4 offset:19456\n"
"v_add_f32 v203, v203, v27\n"
"v_mfma_f32_16x16x32_bf16 v[92:95], a[80:83], a[104:107], v[92:95]\n"
"ds_read_b128 a[104:107], v4 offset:18432\n"
"v_add_f32 v204, v204, v24\n"
"v_mfma_f32_16x16x32_bf16 v[96:99], a[80:83], a[112:115], v[96:99]\n"
"v_add_f32 v205, v205, v25\n"
"v_add_f32 v206, v206, v26\n"
"v_mfma_f32_16x16x32_bf16 v[96:99], a[84:87], a[116:119], v[96:99]\n"
"global_load_dwordx4 a[68:71], v0, s[34:35] offset:-3072 sc1\n"
"v_add_f32 v207, v207, v27\n"
"v_mfma_f32_16x16x32_bf16 v[124:127], a[92:95], a[116:119], v[124:127]\n"
"ds_read_b128 a[116:119], v4 offset:21504\n"
"v_add_f32 v208, v208, v24\n"
"v_mfma_f32_16x16x32_bf16 v[124:127], a[88:91], a[112:115], v[124:127]\n"
"ds_read_b128 a[112:115], v4 offset:20480\n"
"v_add_f32 v209, v209, v25\n"
"v_mfma_f32_16x16x32_bf16 v[128:131], a[88:91], a[120:123], v[128:131]\n"
"v_add_f32 v210, v210, v26\n"
"v_mfma_f32_16x16x32_bf16 v[128:131], a[92:95], a[124:127], v[128:131]\n"
"v_add_f32 v211, v211, v27\n"
"v_mfma_f32_16x16x32_bf16 v[100:103], a[84:87], a[124:127], v[100:103]\n"
"ds_read_b128 a[124:127], v4 offset:23552\n"
"v_add_f32 v212, v212, v24\n"
"v_mfma_f32_16x16x32_bf16 v[100:103], a[80:83], a[120:123], v[100:103]\n"
"ds_read_b128 a[120:123], v4 offset:22528\n"
"v_add_f32 v213, v213, v25\n"
"v_mfma_f32_16x16x32_bf16 v[104:107], a[80:83], a[128:131], v[104:107]\n"
"v_add_f32 v214, v214, v26\n"
"v_add_f32 v215, v215, v27\n"
"v_mfma_f32_16x16x32_bf16 v[104:107], a[84:87], a[132:135], v[104:107]\n"
"global_load_dwordx4 a[72:75], v0, s[34:35] offset:-2048 sc1\n"
"v_add_f32 v216, v216, v24\n"
"v_mfma_f32_16x16x32_bf16 v[132:135], a[92:95], a[132:135], v[132:135]\n"
"ds_read_b128 a[132:135], v4 offset:25600\n"
"v_add_f32 v217, v217, v25\n"
"v_mfma_f32_16x16x32_bf16 v[132:135], a[88:91], a[128:131], v[132:135]\n"
"ds_read_b128 a[128:131], v4 offset:24576\n"
"v_add_f32 v218, v218, v26\n"
"v_mfma_f32_16x16x32_bf16 v[136:139], a[88:91], a[136:139], v[136:139]\n"
"v_add_f32 v219, v219, v27\n"
"v_mfma_f32_16x16x32_bf16 v[136:139], a[92:95], a[140:143], v[136:139]\n"
"v_add_f32 v220, v220, v24\n"
"v_mfma_f32_16x16x32_bf16 v[108:111], a[84:87], a[140:143], v[108:111]\n"
"ds_read_b128 a[140:143], v4 offset:27648\n"
"v_add_f32 v221, v221, v25\n"
"v_mfma_f32_16x16x32_bf16 v[108:111], a[80:83], a[136:139], v[108:111]\n"
"ds_read_b128 a[136:139], v4 offset:26624\n"
"v_add_f32 v222, v222, v26\n"
"v_mfma_f32_16x16x32_bf16 v[112:115], a[80:83], a[144:147], v[112:115]\n"
"v_add_f32 v223, v223, v27\n"
"v_mfma_f32_16x16x32_bf16 v[112:115], a[84:87], a[148:151], v[112:115]\n"
"global_load_dwordx4 a[76:79], v0, s[34:35] offset:-1024 sc1\n"
"v_add_f32 v224, v224, v24\n"
"v_mfma_f32_16x16x32_bf16 v[140:143], a[92:95], a[148:151], v[140:143]\n"
"ds_read_b128 a[148:151], v4 offset:29696\n"
"v_add_f32 v225, v225, v25\n"
"v_mfma_f32_16x16x32_bf16 v[140:143], a[88:91], a[144:147], v[140:143]\n"
"ds_read_b128 a[144:147], v4 offset:28672\n"
"v_add_f32 v226, v226, v26\n"
"v_mfma_f32_16x16x32_bf16 a[12:15], a[88:91], a[152:155], a[12:15]\n"
"v_add_f32 v227, v227, v27\n"
"v_accvgpr_read_b32 v12, a24\n"
"v_mfma_f32_16x16x32_bf16 a[12:15], a[92:95], a[156:159], a[12:15]\n"
"v_accvgpr_read_b32 v13, a25\n"
"v_mfma_f32_16x16x32_bf16 a[8:11], a[84:87], a[156:159], a[8:11]\n"
"ds_read_b128 a[156:159], v4 offset:31744\n"
"v_accvgpr_read_b32 v14, a26\n"
"v_mfma_f32_16x16x32_bf16 a[8:11], a[80:83], a[152:155], a[8:11]\n"
"s_waitcnt vmcnt(8) lgkmcnt(11)\n"
"ds_read_b128 a[152:155], v4 offset:30720\n"
"v_accvgpr_read_b32 v15, a27\n"
"v_mfma_f32_16x16x32_bf16 v[32:35], a[32:35], a[96:99], v[32:35]\n"
"v_add_f32 v12, v12, v24\n"
"v_mfma_f32_16x16x32_bf16 v[32:35], a[36:39], a[100:103], v[32:35]\n"
"global_load_dwordx4 a[80:83], v0, s[34:35] offset:0 sc1\n"
"v_add_f32 v13, v13, v25\n"
"v_mfma_f32_16x16x32_bf16 v[60:63], a[44:47], a[100:103], v[60:63]\n"
"v_add_f32 v14, v14, v26\n"
"v_mfma_f32_16x16x32_bf16 v[60:63], a[40:43], a[96:99], v[60:63]\n"
"v_add_f32 v15, v15, v27\n"
"v_mfma_f32_16x16x32_bf16 v[64:67], a[40:43], a[104:107], v[64:67]\n"
"v_accvgpr_write_b32 a24, v12\n"
"v_accvgpr_write_b32 a25, v13\n"
"v_mfma_f32_16x16x32_bf16 v[64:67], a[44:47], a[108:111], v[64:67]\n"
"v_accvgpr_write_b32 a26, v14\n"
"v_mfma_f32_16x16x32_bf16 v[36:39], a[36:39], a[108:111], v[36:39]\n"
"v_accvgpr_write_b32 a27, v15\n"
"v_mfma_f32_16x16x32_bf16 v[36:39], a[32:35], a[104:107], v[36:39]\n"
"s_waitcnt lgkmcnt(8)\n"
"v_lshlrev_b32 v28, 16, v30\n"
"v_mfma_f32_16x16x32_bf16 v[40:43], a[32:35], a[112:115], v[40:43]\n"
"v_and_b32 v29, s63, v30\n"
"v_mfma_f32_16x16x32_bf16 v[40:43], a[36:39], a[116:119], v[40:43]\n"
"global_load_dwordx4 a[84:87], v0, s[34:35] offset:1024 sc1\n"
"v_lshlrev_b32 v30, 16, v31\n"
"v_mfma_f32_16x16x32_bf16 v[68:71], a[44:47], a[116:119], v[68:71]\n"
"v_and_b32 v31, s63, v31\n"
"v_mfma_f32_16x16x32_bf16 v[68:71], a[40:43], a[112:115], v[68:71]\n"
"v_add_f32 v228, v228, v28\n"
"v_mfma_f32_16x16x32_bf16 v[72:75], a[40:43], a[120:123], v[72:75]\n"
"v_add_f32 v229, v229, v29\n"
"v_mfma_f32_16x16x32_bf16 v[72:75], a[44:47], a[124:127], v[72:75]\n"
"v_add_f32 v230, v230, v30\n"
"v_mfma_f32_16x16x32_bf16 v[44:47], a[36:39], a[124:127], v[44:47]\n"
"v_add_f32 v231, v231, v31\n"
"v_add_f32 v232, v232, v28\n"
"v_mfma_f32_16x16x32_bf16 v[44:47], a[32:35], a[120:123], v[44:47]\n"
"s_waitcnt lgkmcnt(4)\n"
"v_add_f32 v233, v233, v29\n"
"v_mfma_f32_16x16x32_bf16 v[48:51], a[32:35], a[128:131], v[48:51]\n"
"v_add_f32 v234, v234, v30\n"
"v_mfma_f32_16x16x32_bf16 v[48:51], a[36:39], a[132:135], v[48:51]\n"
"global_load_dwordx4 a[88:91], v0, s[34:35] offset:2048 sc1\n"
"v_add_f32 v235, v235, v31\n"
"v_mfma_f32_16x16x32_bf16 v[76:79], a[44:47], a[132:135], v[76:79]\n"
"v_add_f32 v236, v236, v28\n"
"v_mfma_f32_16x16x32_bf16 v[76:79], a[40:43], a[128:131], v[76:79]\n"
"v_add_f32 v237, v237, v29\n"
"v_mfma_f32_16x16x32_bf16 v[80:83], a[40:43], a[136:139], v[80:83]\n"
"v_add_f32 v238, v238, v30\n"
"v_mfma_f32_16x16x32_bf16 v[80:83], a[44:47], a[140:143], v[80:83]\n"
"v_add_f32 v239, v239, v31\n"
"v_mfma_f32_16x16x32_bf16 v[52:55], a[36:39], a[140:143], v[52:55]\n"
"v_add_f32 v240, v240, v28\n"
"v_mfma_f32_16x16x32_bf16 v[52:55], a[32:35], a[136:139], v[52:55]\n"
"s_waitcnt lgkmcnt(0)\n"
"v_add_f32 v241, v241, v29\n"
"v_add_f32 v242, v242, v30\n"
"v_mfma_f32_16x16x32_bf16 v[56:59], a[32:35], a[144:147], v[56:59]\n"
"v_add_f32 v243, v243, v31\n"
"v_mfma_f32_16x16x32_bf16 v[56:59], a[36:39], a[148:151], v[56:59]\n"
"global_load_dwordx4 a[92:95], v0, s[34:35] offset:3072 sc1\n"
"v_add_f32 v244, v244, v28\n"
"v_mfma_f32_16x16x32_bf16 v[84:87], a[44:47], a[148:151], v[84:87]\n"
"v_add_f32 v245, v245, v29\n"
"v_mfma_f32_16x16x32_bf16 v[84:87], a[40:43], a[144:147], v[84:87]\n"
"v_add_f32 v246, v246, v30\n"
"v_mfma_f32_16x16x32_bf16 a[4:7], a[40:43], a[152:155], a[4:7]\n"
"s_add_u32 s34, s34, 8192\n"
"v_add_f32 v247, v247, v31\n"
"v_mfma_f32_16x16x32_bf16 a[4:7], a[44:47], a[156:159], a[4:7]\n"
"v_add_f32 v248, v248, v28\n"
"v_mfma_f32_16x16x32_bf16 a[0:3], a[36:39], a[156:159], a[0:3]\n"
"v_add_f32 v249, v249, v29\n"
"v_mfma_f32_16x16x32_bf16 a[0:3], a[32:35], a[152:155], a[0:3]\n"
"s_waitcnt vmcnt(8)\n"
"v_add_f32 v250, v250, v30\n"
"v_mfma_f32_16x16x32_bf16 v[88:91], a[48:51], a[96:99], v[88:91]\n"
"v_add_f32 v251, v251, v31\n"
"v_add_f32 v252, v252, v28\n"
"v_mfma_f32_16x16x32_bf16 v[88:91], a[52:55], a[100:103], v[88:91]\n"
"global_load_dwordx4 a[32:35], v0, s[34:35] offset:-4096 sc1\n"
"v_add_f32 v253, v253, v29\n"
"v_mfma_f32_16x16x32_bf16 v[116:119], a[60:63], a[100:103], v[116:119]\n"
"ds_read_b128 a[100:103], v5 offset:17408\n"
"v_add_f32 v254, v254, v30\n"
"v_mfma_f32_16x16x32_bf16 v[116:119], a[56:59], a[96:99], v[116:119]\n"
"ds_read_b128 a[96:99], v5 offset:16384\n"
"v_add_f32 v255, v255, v31\n"
"v_mfma_f32_16x16x32_bf16 v[120:123], a[56:59], a[104:107], v[120:123]\n"
"v_accvgpr_read_b32 v12, a28\n"
"v_mfma_f32_16x16x32_bf16 v[120:123], a[60:63], a[108:111], v[120:123]\n"
"v_accvgpr_read_b32 v13, a29\n"
"v_mfma_f32_16x16x32_bf16 v[92:95], a[52:55], a[108:111], v[92:95]\n"
"ds_read_b128 a[108:111], v5 offset:19456\n"
"v_accvgpr_read_b32 v14, a30\n"
"v_mfma_f32_16x16x32_bf16 v[92:95], a[48:51], a[104:107], v[92:95]\n"
"ds_read_b128 a[104:107], v5 offset:18432\n"
"v_accvgpr_read_b32 v15, a31\n"
"v_mfma_f32_16x16x32_bf16 v[96:99], a[48:51], a[112:115], v[96:99]\n"
"v_add_f32 v12, v12, v28\n"
"v_add_f32 v13, v13, v29\n"
"v_mfma_f32_16x16x32_bf16 v[96:99], a[52:55], a[116:119], v[96:99]\n"
"global_load_dwordx4 a[36:39], v0, s[34:35] offset:-3072 sc1\n"
"v_add_f32 v14, v14, v30\n"
"v_mfma_f32_16x16x32_bf16 v[124:127], a[60:63], a[116:119], v[124:127]\n"
"ds_read_b128 a[116:119], v5 offset:21504\n"
"v_add_f32 v15, v15, v31\n"
"v_mfma_f32_16x16x32_bf16 v[124:127], a[56:59], a[112:115], v[124:127]\n"
"ds_read_b128 a[112:115], v5 offset:20480\n"
"v_accvgpr_write_b32 a28, v12\n"
"v_mfma_f32_16x16x32_bf16 v[128:131], a[56:59], a[120:123], v[128:131]\n"
"v_accvgpr_write_b32 a29, v13\n"
"v_mfma_f32_16x16x32_bf16 v[128:131], a[60:63], a[124:127], v[128:131]\n"
"v_accvgpr_write_b32 a30, v14\n"
"v_mfma_f32_16x16x32_bf16 v[100:103], a[52:55], a[124:127], v[100:103]\n"
"ds_read_b128 a[124:127], v5 offset:23552\n"
"v_accvgpr_write_b32 a31, v15\n"
"v_mfma_f32_16x16x32_bf16 v[100:103], a[48:51], a[120:123], v[100:103]\n"
"ds_read_b128 a[120:123], v5 offset:22528\n"
"v_mfma_f32_16x16x32_bf16 v[104:107], a[48:51], a[128:131], v[104:107]\n"
"v_mfma_f32_16x16x32_bf16 v[104:107], a[52:55], a[132:135], v[104:107]\n"
"global_load_dwordx4 a[40:43], v0, s[34:35] offset:-2048 sc1\n"
"v_mfma_f32_16x16x32_bf16 v[132:135], a[60:63], a[132:135], v[132:135]\n"
"ds_read_b128 a[132:135], v5 offset:25600\n"
"v_mfma_f32_16x16x32_bf16 v[132:135], a[56:59], a[128:131], v[132:135]\n"
"ds_read_b128 a[128:131], v5 offset:24576\n"
"v_mfma_f32_16x16x32_bf16 v[136:139], a[56:59], a[136:139], v[136:139]\n"
"ds_read_b128 v[16:19], v7 offset:32768\n"
"v_mfma_f32_16x16x32_bf16 v[136:139], a[60:63], a[140:143], v[136:139]\n"
"v_mfma_f32_16x16x32_bf16 v[108:111], a[52:55], a[140:143], v[108:111]\n"
"ds_read_b128 a[140:143], v5 offset:27648\n"
"v_mfma_f32_16x16x32_bf16 v[108:111], a[48:51], a[136:139], v[108:111]\n"
"ds_read_b128 a[136:139], v5 offset:26624\n"
"v_mfma_f32_16x16x32_bf16 v[112:115], a[48:51], a[144:147], v[112:115]\n"
"v_mfma_f32_16x16x32_bf16 v[112:115], a[52:55], a[148:151], v[112:115]\n"
"global_load_dwordx4 a[44:47], v0, s[34:35] offset:-1024 sc1\n"
"v_mfma_f32_16x16x32_bf16 v[140:143], a[60:63], a[148:151], v[140:143]\n"
"ds_read_b128 a[148:151], v5 offset:29696\n"
"v_mfma_f32_16x16x32_bf16 v[140:143], a[56:59], a[144:147], v[140:143]\n"
"ds_read_b128 a[144:147], v5 offset:28672\n"
"v_mfma_f32_16x16x32_bf16 a[12:15], a[56:59], a[152:155], a[12:15]\n"
"v_mfma_f32_16x16x32_bf16 a[12:15], a[60:63], a[156:159], a[12:15]\n"
"v_mfma_f32_16x16x32_bf16 a[8:11], a[52:55], a[156:159], a[8:11]\n"
"ds_read_b128 a[156:159], v5 offset:31744\n"
"v_mfma_f32_16x16x32_bf16 a[8:11], a[48:51], a[152:155], a[8:11]\n"
"ds_read_b128 a[152:155], v5 offset:30720\n"
"s_branch L_P6\n"
"L_P3F:\n"
"L_st8166:\n"
"s_cbranch_execz L_retB\n"
"s_waitcnt vmcnt(10) lgkmcnt(13)\n"
"v_mfma_f32_16x16x32_bf16 v[32:35], a[32:35], a[96:99], v[32:35]\n"
"s_waitcnt lgkmcnt(6)\n"
"global_store_dwordx4 v0, v[24:27], s[52:53] offset:2048 sc1\n"
"s_cmp_lt_u32 s27, 12582912\n"
"s_cselect_b32 s38, 8192000, 0\n"
"s_cselect_b32 s42, 12582144, 0\n"
"v_mfma_f32_16x16x32_bf16 v[32:35], a[36:39], a[100:103], v[32:35]\n"
"global_load_dwordx4 a[80:83], v0, s[34:35] offset:0 sc1\n"
"v_mfma_f32_16x16x32_bf16 v[60:63], a[44:47], a[100:103], v[60:63]\n"
"s_barrier\n"
"v_mfma_f32_16x16x32_bf16 v[60:63], a[40:43], a[96:99], v[60:63]\n"
"s_add_u32 m0, s64, 12288\n"
"v_mfma_f32_16x16x32_bf16 v[64:67], a[40:43], a[104:107], v[64:67]\n"
"v_mfma_f32_16x16x32_bf16 v[64:67], a[44:47], a[108:111], v[64:67]\n"
"s_add_u32 s67, s27, 0\n"
"v_mfma_f32_16x16x32_bf16 v[36:39], a[36:39], a[108:111], v[36:39]\n"
"v_mfma_f32_16x16x32_bf16 v[36:39], a[32:35], a[104:107], v[36:39]\n"
"v_mfma_f32_16x16x32_bf16 v[40:43], a[32:35], a[112:115], v[40:43]\n"
"buffer_load_dwordx4 v6, s[40:43], s67 offen offset:0 lds\n"
"v_mfma_f32_16x16x32_bf16 v[40:43], a[36:39], a[116:119], v[40:43]\n"
"global_load_dwordx4 a[84:87], v0, s[34:35] offset:1024 sc1\n"
"v_mfma_f32_16x16x32_bf16 v[68:71], a[44:47], a[116:119], v[68:71]\n"
"v_mfma_f32_16x16x32_bf16 v[68:71], a[40:43], a[112:115], v[68:71]\n"
"s_add_u32 s67, s27, 15360\n"
"v_mfma_f32_16x16x32_bf16 v[72:75], a[40:43], a[120:123], v[72:75]\n"
"v_mfma_f32_16x16x32_bf16 v[72:75], a[44:47], a[124:127], v[72:75]\n"
"v_mfma_f32_16x16x32_bf16 v[44:47], a[36:39], a[124:127], v[44:47]\n"
"buffer_load_dwordx4 v6, s[40:43], s67 offen offset:1024 lds\n"
"v_mfma_f32_16x16x32_bf16 v[44:47], a[32:35], a[120:123], v[44:47]\n"
"s_waitcnt lgkmcnt(4)\n"
"v_mfma_f32_16x16x32_bf16 v[48:51], a[32:35], a[128:131], v[48:51]\n"
"v_mfma_f32_16x16x32_bf16 v[48:51], a[36:39], a[132:135], v[48:51]\n"
"global_load_dwordx4 a[88:91], v0, s[34:35] offset:2048 sc1\n"
"v_lshrrev_b32 v16, 4, v0\n"
"v_mfma_f32_16x16x32_bf16 v[76:79], a[44:47], a[132:135], v[76:79]\n"
"s_add_u32 s67, s27, 30720\n"
"v_and_b32 v16, 0x30, v16\n"
"v_mfma_f32_16x16x32_bf16 v[76:79], a[40:43], a[128:131], v[76:79]\n"
"ds_read_b128 v[28:31], v2 offset:23552\n"
"buffer_load_dwordx2 v[18:19], v16, s[44:47], s61 offen offset:0\n"
"v_mfma_f32_16x16x32_bf16 v[80:83], a[40:43], a[136:139], v[80:83]\n"
"buffer_load_dwordx2 v[8:9], off, s[56:59], %14 sc1\n"
"v_mfma_f32_16x16x32_bf16 v[80:83], a[44:47], a[140:143], v[80:83]\n"
"buffer_load_dwordx4 v6, s[40:43], s67 offen offset:2048 lds\n"
"v_mfma_f32_16x16x32_bf16 v[52:55], a[36:39], a[140:143], v[52:55]\n"
"v_mfma_f32_16x16x32_bf16 v[52:55], a[32:35], a[136:139], v[52:55]\n"
"s_waitcnt lgkmcnt(1)\n"
"v_mfma_f32_16x16x32_bf16 v[56:59], a[32:35], a[144:147], v[56:59]\n"
"s_add_u32 s67, s27, 46080\n"
"v_mfma_f32_16x16x32_bf16 v[56:59], a[36:39], a[148:151], v[56:59]\n"
"global_load_dwordx4 a[92:95], v0, s[34:35] offset:3072 sc1\n"
"v_lshrrev_b32 v20, 4, v0\n"
"v_mfma_f32_16x16x32_bf16 v[84:87], a[44:47], a[148:151], v[84:87]\n"
"v_and_b32 v20, 0x30, v20\n"
"v_mfma_f32_16x16x32_bf16 v[84:87], a[40:43], a[144:147], v[84:87]\n"
"buffer_load_dwordx4 v6, s[40:43], s67 offen offset:3072 lds\n"
"buffer_load_dwordx2 v[22:23], v20, s[44:47], s61 offen offset:8\n"
"v_mfma_f32_16x16x32_bf16 a[4:7], a[40:43], a[152:155], a[4:7]\n"
"s_add_u32 s34, s34, 8192\n"
"v_mfma_f32_16x16x32_bf16 a[4:7], a[44:47], a[156:159], a[4:7]\n"
"v_mfma_f32_16x16x32_bf16 a[0:3], a[36:39], a[156:159], a[0:3]\n"
"s_waitcnt lgkmcnt(0)\n"
"global_store_dwordx4 v0, v[28:31], s[52:53] offset:3072 sc1\n"
"v_mfma_f32_16x16x32_bf16 a[0:3], a[32:35], a[152:155], a[0:3]\n"
"s_waitcnt vmcnt(18)\n"
"v_mfma_f32_16x16x32_bf16 v[88:91], a[48:51], a[96:99], v[88:91]\n"
"v_mfma_f32_16x16x32_bf16 v[88:91], a[52:55], a[100:103], v[88:91]\n"
"global_load_dwordx4 a[32:35], v0, s[34:35] offset:-4096 sc1\n"
"v_mfma_f32_16x16x32_bf16 v[116:119], a[60:63], a[100:103], v[116:119]\n"
"v_mfma_f32_16x16x32_bf16 v[116:119], a[56:59], a[96:99], v[116:119]\n"
"v_mfma_f32_16x16x32_bf16 v[120:123], a[56:59], a[104:107], v[120:123]\n"
"v_mfma_f32_16x16x32_bf16 v[120:123], a[60:63], a[108:111], v[120:123]\n"
"v_mfma_f32_16x16x32_bf16 v[92:95], a[52:55], a[108:111], v[92:95]\n"
"v_mfma_f32_16x16x32_bf16 v[92:95], a[48:51], a[104:107], v[92:95]\n"
"v_mfma_f32_16x16x32_bf16 v[96:99], a[48:51], a[112:115], v[96:99]\n"
"v_mfma_f32_16x16x32_bf16 v[96:99], a[52:55], a[116:119], v[96:99]\n"
"global_load_dwordx4 a[36:39], v0, s[34:35] offset:-3072 sc1\n"
"v_mfma_f32_16x16x32_bf16 v[124:127], a[60:63], a[116:119], v[124:127]\n"
"v_mfma_f32_16x16x32_bf16 v[124:127], a[56:59], a[112:115], v[124:127]\n"
"v_mfma_f32_16x16x32_bf16 v[128:131], a[56:59], a[120:123], v[128:131]\n"
"v_mfma_f32_16x16x32_bf16 v[128:131], a[60:63], a[124:127], v[128:131]\n"
"v_mfma_f32_16x16x32_bf16 v[100:103], a[52:55], a[124:127], v[100:103]\n"
"v_mfma_f32_16x16x32_bf16 v[100:103], a[48:51], a[120:123], v[100:103]\n"
"v_mfma_f32_16x16x32_bf16 v[104:107], a[48:51], a[128:131], v[104:107]\n"
"ds_read_b128 v[24:27], v2 offset:24576\n"
"v_mfma_f32_16x16x32_bf16 v[104:107], a[52:55], a[132:135], v[104:107]\n"
"global_load_dwordx4 a[40:43], v0, s[34:35] offset:-2048 sc1\n"
"v_mfma_f32_16x16x32_bf16 v[132:135], a[60:63], a[132:135], v[132:135]\n"
"v_mfma_f32_16x16x32_bf16 v[132:135], a[56:59], a[128:131], v[132:135]\n"
"v_mfma_f32_16x16x32_bf16 v[136:139], a[56:59], a[136:139], v[136:139]\n"
"v_mfma_f32_16x16x32_bf16 v[136:139], a[60:63], a[140:143], v[136:139]\n"
"v_mfma_f32_16x16x32_bf16 v[108:111], a[52:55], a[140:143], v[108:111]\n"
"v_mfma_f32_16x16x32_bf16 v[108:111], a[48:51], a[136:139], v[108:111]\n"
"v_mfma_f32_16x16x32_bf16 v[112:115], a[48:51], a[144:147], v[112:115]\n"
"v_mfma_f32_16x16x32_bf16 v[112:115], a[52:55], a[148:151], v[112:115]\n"
"global_load_dwordx4 a[44:47], v0, s[34:35] offset:-1024 sc1\n"
"v_mfma_f32_16x16x32_bf16 v[140:143], a[60:63], a[148:151], v[140:143]\n"
"v_mfma_f32_16x16x32_bf16 v[140:143], a[56:59], a[144:147], v[140:143]\n"
"v_mfma_f32_16x16x32_bf16 a[12:15], a[56:59], a[152:155], a[12:15]\n"
"s_waitcnt lgkmcnt(0)\n"
"global_store_dwordx4 v0, v[24:27], s[54:55] offset:-4096 sc1\n"
"v_mfma_f32_16x16x32_bf16 a[12:15], a[60:63], a[156:159], a[12:15]\n"
"v_mfma_f32_16x16x32_bf16 a[8:11], a[52:55], a[156:159], a[8:11]\n"
"v_mfma_f32_16x16x32_bf16 a[8:11], a[48:51], a[152:155], a[8:11]\n"
"s_waitcnt vmcnt(18)\n"
"v_mfma_f32_16x16x32_bf16 v[144:147], a[64:67], a[96:99], v[144:147]\n"
"v_mfma_f32_16x16x32_bf16 v[144:147], a[68:71], a[100:103], v[144:147]\n"
"global_load_dwordx4 a[48:51], v0, s[34:35] offset:0 sc1\n"
"v_mfma_f32_16x16x32_bf16 v[172:175], a[76:79], a[100:103], v[172:175]\n"
"v_mfma_f32_16x16x32_bf16 v[172:175], a[72:75], a[96:99], v[172:175]\n"
"v_mfma_f32_16x16x32_bf16 v[176:179], a[72:75], a[104:107], v[176:179]\n"
"v_mfma_f32_16x16x32_bf16 v[176:179], a[76:79], a[108:111], v[176:179]\n"
"v_mfma_f32_16x16x32_bf16 v[148:151], a[68:71], a[108:111], v[148:151]\n"
"v_mfma_f32_16x16x32_bf16 v[148:151], a[64:67], a[104:107], v[148:151]\n"
"v_mfma_f32_16x16x32_bf16 v[152:155], a[64:67], a[112:115], v[152:155]\n"
"v_mfma_f32_16x16x32_bf16 v[152:155], a[68:71], a[116:119], v[152:155]\n"
"global_load_dwordx4 a[52:55], v0, s[34:35] offset:1024 sc1\n"
"v_mfma_f32_16x16x32_bf16 v[180:183], a[76:79], a[116:119], v[180:183]\n"
"v_mfma_f32_16x16x32_bf16 v[180:183], a[72:75], a[112:115], v[180:183]\n"
"v_mfma_f32_16x16x32_bf16 v[184:187], a[72:75], a[120:123], v[184:187]\n"
"v_mfma_f32_16x16x32_bf16 v[184:187], a[76:79], a[124:127], v[184:187]\n"
"v_mfma_f32_16x16x32_bf16 v[156:159], a[68:71], a[124:127], v[156:159]\n"
"v_mfma_f32_16x16x32_bf16 v[156:159], a[64:67], a[120:123], v[156:159]\n"
"ds_read_b128 v[28:31], v2 offset:26624\n"
"v_mfma_f32_16x16x32_bf16 v[160:163], a[64:67], a[128:131], v[160:163]\n"
"v_mfma_f32_16x16x32_bf16 v[160:163], a[68:71], a[132:135], v[160:163]\n"
"global_load_dwordx4 a[56:59], v0, s[34:35] offset:2048 sc1\n"
"v_mfma_f32_16x16x32_bf16 v[188:191], a[76:79], a[132:135], v[188:191]\n"
"v_mfma_f32_16x16x32_bf16 v[188:191], a[72:75], a[128:131], v[188:191]\n"
"v_mfma_f32_16x16x32_bf16 v[192:195], a[72:75], a[136:139], v[192:195]\n"
"v_mfma_f32_16x16x32_bf16 v[192:195], a[76:79], a[140:143], v[192:195]\n"
"v_mfma_f32_16x16x32_bf16 v[164:167], a[68:71], a[140:143], v[164:167]\n"
"v_mfma_f32_16x16x32_bf16 v[164:167], a[64:67], a[136:139], v[164:167]\n"
"v_mfma_f32_16x16x32_bf16 v[168:171], a[64:67], a[144:147], v[168:171]\n"
"v_mfma_f32_16x16x32_bf16 v[168:171], a[68:71], a[148:151], v[168:171]\n"
"global_load_dwordx4 a[60:63], v0, s[34:35] offset:3072 sc1\n"
"v_mfma_f32_16x16x32_bf16 v[196:199], a[76:79], a[148:151], v[196:199]\n"
"s_waitcnt lgkmcnt(0)\n"
"global_store_dwordx4 v0, v[28:31], s[54:55] offset:-2048 sc1\n"
"v_mfma_f32_16x16x32_bf16 v[196:199], a[72:75], a[144:147], v[196:199]\n"
"v_mfma_f32_16x16x32_bf16 a[20:23], a[72:75], a[152:155], a[20:23]\n"
"s_add_u32 s34, s34, 8192\n"
"v_mfma_f32_16x16x32_bf16 a[20:23], a[76:79], a[156:159], a[20:23]\n"
"v_mfma_f32_16x16x32_bf16 a[16:19], a[68:71], a[156:159], a[16:19]\n"
"v_mfma_f32_16x16x32_bf16 a[16:19], a[64:67], a[152:155], a[16:19]\n"
"s_waitcnt vmcnt(13)\n"
"v_mfma_f32_16x16x32_bf16 v[200:203], a[80:83], a[96:99], v[200:203]\n"
"v_mfma_f32_16x16x32_bf16 v[200:203], a[84:87], a[100:103], v[200:203]\n"
"global_load_dwordx4 a[64:67], v0, s[34:35] offset:-4096 sc1\n"
"v_mfma_f32_16x16x32_bf16 v[228:231], a[92:95], a[100:103], v[228:231]\n"
"ds_read_b128 a[100:103], v3 offset:17408\n"
"v_mfma_f32_16x16x32_bf16 v[228:231], a[88:91], a[96:99], v[228:231]\n"
"ds_read_b128 a[96:99], v3 offset:16384\n"
"v_mfma_f32_16x16x32_bf16 v[232:235], a[88:91], a[104:107], v[232:235]\n"
"v_mfma_f32_16x16x32_bf16 v[232:235], a[92:95], a[108:111], v[232:235]\n"
"v_mfma_f32_16x16x32_bf16 v[204:207], a[84:87], a[108:111], v[204:207]\n"
"ds_read_b128 a[108:111], v3 offset:19456\n"
"v_mfma_f32_16x16x32_bf16 v[204:207], a[80:83], a[104:107], v[204:207]\n"
"ds_read_b128 a[104:107], v3 offset:18432\n"
"v_mfma_f32_16x16x32_bf16 v[208:211], a[80:83], a[112:115], v[208:211]\n"
"v_mfma_f32_16x16x32_bf16 v[208:211], a[84:87], a[116:119], v[208:211]\n"
"global_load_dwordx4 a[68:71], v0, s[34:35] offset:-3072 sc1\n"
"v_mfma_f32_16x16x32_bf16 v[236:239], a[92:95], a[116:119], v[236:239]\n"
"ds_read_b128 a[116:119], v3 offset:21504\n"
"v_mfma_f32_16x16x32_bf16 v[236:239], a[88:91], a[112:115], v[236:239]\n"
"ds_read_b128 a[112:115], v3 offset:20480\n"
"v_mfma_f32_16x16x32_bf16 v[240:243], a[88:91], a[120:123], v[240:243]\n"
"v_mfma_f32_16x16x32_bf16 v[240:243], a[92:95], a[124:127], v[240:243]\n"
"ds_read_b128 v[24:27], v2 offset:29696\n"
"v_mfma_f32_16x16x32_bf16 v[212:215], a[84:87], a[124:127], v[212:215]\n"
"ds_read_b128 a[124:127], v3 offset:23552\n"
"v_mfma_f32_16x16x32_bf16 v[212:215], a[80:83], a[120:123], v[212:215]\n"
"ds_read_b128 a[120:123], v3 offset:22528\n"
"v_mfma_f32_16x16x32_bf16 v[216:219], a[80:83], a[128:131], v[216:219]\n"
"v_mfma_f32_16x16x32_bf16 v[216:219], a[84:87], a[132:135], v[216:219]\n"
"global_load_dwordx4 a[72:75], v0, s[34:35] offset:-2048 sc1\n"
"v_mfma_f32_16x16x32_bf16 v[244:247], a[92:95], a[132:135], v[244:247]\n"
"ds_read_b128 a[132:135], v3 offset:25600\n"
"v_mfma_f32_16x16x32_bf16 v[244:247], a[88:91], a[128:131], v[244:247]\n"
"ds_read_b128 a[128:131], v3 offset:24576\n"
"v_mfma_f32_16x16x32_bf16 v[248:251], a[88:91], a[136:139], v[248:251]\n"
"v_subrev_u32 v8, s65, v8\n"
"v_mfma_f32_16x16x32_bf16 v[248:251], a[92:95], a[140:143], v[248:251]\n"
"v_xor_b32 v9, s79, v9\n"
"v_mfma_f32_16x16x32_bf16 v[220:223], a[84:87], a[140:143], v[220:223]\n"
"ds_read_b128 a[140:143], v3 offset:27648\n"
"v_or_b32 v8, v8, v9\n"
"v_mfma_f32_16x16x32_bf16 v[220:223], a[80:83], a[136:139], v[220:223]\n"
"ds_read_b128 a[136:139], v3 offset:26624\n"
"v_cmp_lt_u32 vcc, 15, v8\n"
"v_mfma_f32_16x16x32_bf16 v[224:227], a[80:83], a[144:147], v[224:227]\n"
"s_waitcnt lgkmcnt(6)\n"
"global_store_dwordx4 v0, v[24:27], s[54:55] offset:1024 sc1\n"
"s_cbranch_vccnz L_slow7\n"
"L_back7:\n"
"v_mfma_f32_16x16x32_bf16 v[224:227], a[84:87], a[148:151], v[224:227]\n"
"global_load_dwordx4 a[76:79], v0, s[34:35] offset:-1024 sc1\n"
"v_readfirstlane_b32 s66, v9\n"
"v_mfma_f32_16x16x32_bf16 v[252:255], a[92:95], a[148:151], v[252:255]\n"
"ds_read_b128 a[148:151], v3 offset:29696\n"
"v_mfma_f32_16x16x32_bf16 v[252:255], a[88:91], a[144:147], v[252:255]\n"
"s_and_b32 s66, s66, 3\n"
"ds_read_b128 a[144:147], v3 offset:28672\n"
"v_mfma_f32_16x16x32_bf16 a[28:31], a[88:91], a[152:155], a[28:31]\n"
"s_lshl_b32 s69, s66, 13\n"
"v_mfma_f32_16x16x32_bf16 a[28:31], a[92:95], a[156:159], a[28:31]\n"
"s_cmp_eq_u32 s66, 3\n"
"s_cselect_b32 s75, 8192, 0\n"
"v_mfma_f32_16x16x32_bf16 a[24:27], a[84:87], a[156:159], a[24:27]\n"
"s_and_b32 s29, s66, s30\n"
"ds_read_b128 a[156:159], v3 offset:31744\n"
"v_mfma_f32_16x16x32_bf16 a[24:27], a[80:83], a[152:155], a[24:27]\n"
"s_waitcnt vmcnt(11)\n"
"ds_read_b128 a[152:155], v3 offset:30720\n"
"v_mfma_f32_16x16x32_bf16 v[32:35], a[32:35], a[96:99], v[32:35]\n"
"s_xor_b32 s31, s79, s66\n"
"v_mfma_f32_16x16x32_bf16 v[32:35], a[36:39], a[100:103], v[32:35]\n"
"global_load_dwordx4 a[80:83], v0, s[34:35] offset:0 sc1\n"
"v_mfma_f32_16x16x32_bf16 v[60:63], a[44:47], a[100:103], v[60:63]\n"
"v_mfma_f32_16x16x32_bf16 v[60:63], a[40:43], a[96:99], v[60:63]\n"
"v_mfma_f32_16x16x32_bf16 v[64:67], a[40:43], a[104:107], v[64:67]\n"
"v_mfma_f32_16x16x32_bf16 v[64:67], a[44:47], a[108:111], v[64:67]\n"
"v_mfma_f32_16x16x32_bf16 v[36:39], a[36:39], a[108:111], v[36:39]\n"
"s_add_u32 m0, s64, 0\n"
"v_mfma_f32_16x16x32_bf16 v[36:39], a[32:35], a[104:107], v[36:39]\n"
"s_waitcnt lgkmcnt(8)\n"
"v_mfma_f32_16x16x32_bf16 v[40:43], a[32:35], a[112:115], v[40:43]\n"
"v_mfma_f32_16x16x32_bf16 v[40:43], a[36:39], a[116:119], v[40:43]\n"
"global_load_dwordx4 a[84:87], v0, s[34:35] offset:1024 sc1\n"
"v_mfma_f32_16x16x32_bf16 v[68:71], a[44:47], a[116:119], v[68:71]\n"
"v_mfma_f32_16x16x32_bf16 v[68:71], a[40:43], a[112:115], v[68:71]\n"
"v_mfma_f32_16x16x32_bf16 v[72:75], a[40:43], a[120:123], v[72:75]\n"
"v_mfma_f32_16x16x32_bf16 v[72:75], a[44:47], a[124:127], v[72:75]\n"
"v_and_b32 v10, 0x300, v0\n"
"v_mfma_f32_16x16x32_bf16 v[44:47], a[36:39], a[124:127], v[44:47]\n"
"v_mfma_f32_16x16x32_bf16 v[44:47], a[32:35], a[120:123], v[44:47]\n"
"s_waitcnt lgkmcnt(4)\n"
"v_mfma_f32_16x16x32_bf16 v[48:51], a[32:35], a[128:131], v[48:51]\n"
"v_and_b32 v11, 0x3c0, v0\n"
"v_mfma_f32_16x16x32_bf16 v[48:51], a[36:39], a[132:135], v[48:51]\n"
"global_load_dwordx4 a[88:91], v0, s[34:35] offset:2048 sc1\n"
"v_mfma_f32_16x16x32_bf16 v[76:79], a[44:47], a[132:135], v[76:79]\n"
"v_mfma_f32_16x16x32_bf16 v[76:79], a[40:43], a[128:131], v[76:79]\n"
"v_mfma_f32_16x16x32_bf16 v[80:83], a[40:43], a[136:139], v[80:83]\n"
"v_add_lshl_u32 v11, v10, v11, 1\n"
"v_mfma_f32_16x16x32_bf16 v[80:83], a[44:47], a[140:143], v[80:83]\n"
"buffer_load_dwordx4 v0, s[48:51], s75 offen offset:0 sc1 lds\n"
"v_mfma_f32_16x16x32_bf16 v[52:55], a[36:39], a[140:143], v[52:55]\n"
"v_mfma_f32_16x16x32_bf16 v[52:55], a[32:35], a[136:139], v[52:55]\n"
"s_waitcnt lgkmcnt(0)\n"
"v_mfma_f32_16x16x32_bf16 v[56:59], a[32:35], a[144:147], v[56:59]\n"
"v_lshrrev_b32 v10, 2, v0\n"
"v_mfma_f32_16x16x32_bf16 v[56:59], a[36:39], a[148:151], v[56:59]\n"
"global_load_dwordx4 a[92:95], v0, s[34:35] offset:3072 sc1\n"
"v_mfma_f32_16x16x32_bf16 v[84:87], a[44:47], a[148:151], v[84:87]\n"
"v_mfma_f32_16x16x32_bf16 v[84:87], a[40:43], a[144:147], v[84:87]\n"
"v_xor_b32 v10, v10, v0\n"
"v_mfma_f32_16x16x32_bf16 a[4:7], a[40:43], a[152:155], a[4:7]\n"
"s_add_u32 s34, s34, 8192\n"
"v_mfma_f32_16x16x32_bf16 a[4:7], a[44:47], a[156:159], a[4:7]\n"
"v_mfma_f32_16x16x32_bf16 a[0:3], a[36:39], a[156:159], a[0:3]\n"
"v_mfma_f32_16x16x32_bf16 a[0:3], a[32:35], a[152:155], a[0:3]\n"
"s_waitcnt vmcnt(11)\n"
"v_and_b32 v10, 0x30, v10\n"
"v_mfma_f32_16x16x32_bf16 v[88:91], a[48:51], a[96:99], v[88:91]\n"
"v_mfma_f32_16x16x32_bf16 v[88:91], a[52:55], a[100:103], v[88:91]\n"
"global_load_dwordx4 a[32:35], v0, s[34:35] offset:-4096 sc1\n"
"v_mfma_f32_16x16x32_bf16 v[116:119], a[60:63], a[100:103], v[116:119]\n"
"v_add3_u32 v11, v10, v11, s64\n"
"v_mfma_f32_16x16x32_bf16 v[116:119], a[56:59], a[96:99], v[116:119]\n"
"buffer_load_dwordx4 v0, s[48:51], s75 offen offset:1024 sc1 lds\n"
"v_mfma_f32_16x16x32_bf16 v[120:123], a[56:59], a[104:107], v[120:123]\n"
"v_mfma_f32_16x16x32_bf16 v[120:123], a[60:63], a[108:111], v[120:123]\n"
"v_mfma_f32_16x16x32_bf16 v[92:95], a[52:55], a[108:111], v[92:95]\n"
"s_add_u32 s27, s27, 0x200000\n"
"v_mfma_f32_16x16x32_bf16 v[92:95], a[48:51], a[104:107], v[92:95]\n"
"v_mfma_f32_16x16x32_bf16 v[96:99], a[48:51], a[112:115], v[96:99]\n"
"v_mfma_f32_16x16x32_bf16 v[96:99], a[52:55], a[116:119], v[96:99]\n"
"global_load_dwordx4 a[36:39], v0, s[34:35] offset:-3072 sc1\n"
"v_mfma_f32_16x16x32_bf16 v[124:127], a[60:63], a[116:119], v[124:127]\n"
"ds_read_b64_tr_b16 v[24:25], v11 offset:12288\n"
"v_mfma_f32_16x16x32_bf16 v[124:127], a[56:59], a[112:115], v[124:127]\n"
"v_mfma_f32_16x16x32_bf16 v[128:131], a[56:59], a[120:123], v[128:131]\n"
"ds_read_b64_tr_b16 v[26:27], v11 offset:12864\n"
"v_mfma_f32_16x16x32_bf16 v[128:131], a[60:63], a[124:127], v[128:131]\n"
"v_mfma_f32_16x16x32_bf16 v[100:103], a[52:55], a[124:127], v[100:103]\n"
"ds_read_b64_tr_b16 v[28:29], v11 offset:12296\n"
"v_mfma_f32_16x16x32_bf16 v[100:103], a[48:51], a[120:123], v[100:103]\n"
"v_mfma_f32_16x16x32_bf16 v[104:107], a[48:51], a[128:131], v[104:107]\n"
"ds_read_b64_tr_b16 v[30:31], v11 offset:12872\n"
"v_mfma_f32_16x16x32_bf16 v[104:107], a[52:55], a[132:135], v[104:107]\n"
"global_load_dwordx4 a[40:43], v0, s[34:35] offset:-2048 sc1\n"
"v_mfma_f32_16x16x32_bf16 v[132:135], a[60:63], a[132:135], v[132:135]\n"
"buffer_load_dwordx4 v0, s[48:51], s75 offen offset:2048 sc1 lds\n"
"v_mfma_f32_16x16x32_bf16 v[132:135], a[56:59], a[128:131], v[132:135]\n"
"v_mfma_f32_16x16x32_bf16 v[136:139], a[56:59], a[136:139], v[136:139]\n"
"v_mfma_f32_16x16x32_bf16 v[136:139], a[60:63], a[140:143], v[136:139]\n"
"v_mfma_f32_16x16x32_bf16 v[108:111], a[52:55], a[140:143], v[108:111]\n"
"v_mfma_f32_16x16x32_bf16 v[108:111], a[48:51], a[136:139], v[108:111]\n"
"v_mfma_f32_16x16x32_bf16 v[112:115], a[48:51], a[144:147], v[112:115]\n"
"s_waitcnt lgkmcnt(0)\n"
"v_mfma_f32_16x16x32_bf16 v[112:115], a[52:55], a[148:151], v[112:115]\n"
"global_load_dwordx4 a[44:47], v0, s[34:35] offset:-1024 sc1\n"
"v_mfma_f32_16x16x32_bf16 v[140:143], a[60:63], a[148:151], v[140:143]\n"
"v_mfma_f32_16x16x32_bf16 v[140:143], a[56:59], a[144:147], v[140:143]\n"
"v_mfma_f32_16x16x32_bf16 a[12:15], a[56:59], a[152:155], a[12:15]\n"
"v_mfma_f32_16x16x32_bf16 a[12:15], a[60:63], a[156:159], a[12:15]\n"
"v_mfma_f32_16x16x32_bf16 a[8:11], a[52:55], a[156:159], a[8:11]\n"
"v_mfma_f32_16x16x32_bf16 a[8:11], a[48:51], a[152:155], a[8:11]\n"
"s_waitcnt vmcnt(11)\n"
"v_mfma_f32_16x16x32_bf16 v[144:147], a[64:67], a[96:99], v[144:147]\n"
"buffer_store_dwordx4 v[24:27], v0, s[36:39], s28 offen offset:0 sc1\n"
"buffer_load_dwordx4 v0, s[48:51], s75 offen offset:3072 sc1 lds\n"
"v_mfma_f32_16x16x32_bf16 v[144:147], a[68:71], a[100:103], v[144:147]\n"
"global_load_dwordx4 a[48:51], v0, s[34:35] offset:0 sc1\n"
"v_mfma_f32_16x16x32_bf16 v[172:175], a[76:79], a[100:103], v[172:175]\n"
"v_mfma_f32_16x16x32_bf16 v[172:175], a[72:75], a[96:99], v[172:175]\n"
"v_mfma_f32_16x16x32_bf16 v[176:179], a[72:75], a[104:107], v[176:179]\n"
"v_mfma_f32_16x16x32_bf16 v[176:179], a[76:79], a[108:111], v[176:179]\n"
"v_mfma_f32_16x16x32_bf16 v[148:151], a[68:71], a[108:111], v[148:151]\n"
"v_mfma_f32_16x16x32_bf16 v[148:151], a[64:67], a[104:107], v[148:151]\n"
"v_mfma_f32_16x16x32_bf16 v[152:155], a[64:67], a[112:115], v[152:155]\n"
"v_mfma_f32_16x16x32_bf16 v[152:155], a[68:71], a[116:119], v[152:155]\n"
"global_load_dwordx4 a[52:55], v0, s[34:35] offset:1024 sc1\n"
"v_mfma_f32_16x16x32_bf16 v[180:183], a[76:79], a[116:119], v[180:183]\n"
"v_mfma_f32_16x16x32_bf16 v[180:183], a[72:75], a[112:115], v[180:183]\n"
"v_mfma_f32_16x16x32_bf16 v[184:187], a[72:75], a[120:123], v[184:187]\n"
"v_mfma_f32_16x16x32_bf16 v[184:187], a[76:79], a[124:127], v[184:187]\n"
"v_mfma_f32_16x16x32_bf16 v[156:159], a[68:71], a[124:127], v[156:159]\n"
"v_mfma_f32_16x16x32_bf16 v[156:159], a[64:67], a[120:123], v[156:159]\n"
"s_add_u32 m0, s64, 4096\n"
"v_mfma_f32_16x16x32_bf16 v[160:163], a[64:67], a[128:131], v[160:163]\n"
"v_mfma_f32_16x16x32_bf16 v[160:163], a[68:71], a[132:135], v[160:163]\n"
"global_load_dwordx4 a[56:59], v0, s[34:35] offset:2048 sc1\n"
"v_mfma_f32_16x16x32_bf16 v[188:191], a[76:79], a[132:135], v[188:191]\n"
"v_mfma_f32_16x16x32_bf16 v[188:191], a[72:75], a[128:131], v[188:191]\n"
"v_mfma_f32_16x16x32_bf16 v[192:195], a[72:75], a[136:139], v[192:195]\n"
"v_mfma_f32_16x16x32_bf16 v[192:195], a[76:79], a[140:143], v[192:195]\n"
"v_mfma_f32_16x16x32_bf16 v[164:167], a[68:71], a[140:143], v[164:167]\n"
"v_mfma_f32_16x16x32_bf16 v[164:167], a[64:67], a[136:139], v[164:167]\n"
"v_mfma_f32_16x16x32_bf16 v[168:171], a[64:67], a[144:147], v[168:171]\n"
"L_st8508:\n"
"s_cbranch_execz L_retB\n"
"v_mfma_f32_16x16x32_bf16 v[168:171], a[68:71], a[148:151], v[168:171]\n"
"global_load_dwordx4 a[60:63], v0, s[34:35] offset:3072 sc1\n"
"v_mfma_f32_16x16x32_bf16 v[196:199], a[76:79], a[148:151], v[196:199]\n"
"v_mfma_f32_16x16x32_bf16 v[196:199], a[72:75], a[144:147], v[196:199]\n"
"v_mfma_f32_16x16x32_bf16 a[20:23], a[72:75], a[152:155], a[20:23]\n"
"s_add_u32 s34, s34, 8192\n"
"v_mfma_f32_16x16x32_bf16 a[20:23], a[76:79], a[156:159], a[20:23]\n"
"v_mfma_f32_16x16x32_bf16 a[16:19], a[68:71], a[156:159], a[16:19]\n"
"buffer_load_dwordx4 v1, s[48:51], s75 offen offset:0 sc1 lds\n"
"v_mfma_f32_16x16x32_bf16 a[16:19], a[64:67], a[152:155], a[16:19]\n"
"s_waitcnt vmcnt(13)\n"
"v_mfma_f32_16x16x32_bf16 v[200:203], a[80:83], a[96:99], v[200:203]\n"
"v_mfma_f32_16x16x32_bf16 v[200:203], a[84:87], a[100:103], v[200:203]\n"
"global_load_dwordx4 a[64:67], v0, s[34:35] offset:-4096 sc1\n"
"s_add_u32 s65, s65, 1\n"
"v_mfma_f32_16x16x32_bf16 v[228:231], a[92:95], a[100:103], v[228:231]\n"
"ds_read_b128 a[100:103], v4 offset:17408\n"
"v_mfma_f32_16x16x32_bf16 v[228:231], a[88:91], a[96:99], v[228:231]\n"
"ds_read_b128 a[96:99], v4 offset:16384\n"
"v_mov_b32 v8, s65\n"
"v_mfma_f32_16x16x32_bf16 v[232:235], a[88:91], a[104:107], v[232:235]\n"
"v_mov_b32 v9, s60\n"
"v_mfma_f32_16x16x32_bf16 v[232:235], a[92:95], a[108:111], v[232:235]\n"
"v_mfma_f32_16x16x32_bf16 v[204:207], a[84:87], a[108:111], v[204:207]\n"
"buffer_store_dwordx2 v[8:9], off, s[56:59], %13 sc1\n"
"ds_read_b128 a[108:111], v4 offset:19456\n"
"v_mfma_f32_16x16x32_bf16 v[204:207], a[80:83], a[104:107], v[204:207]\n"
"ds_read_b128 a[104:107], v4 offset:18432\n"
"v_mfma_f32_16x16x32_bf16 v[208:211], a[80:83], a[112:115], v[208:211]\n"
"buffer_store_dwordx4 v[28:31], v0, s[36:39], s28 offen offset:2048 sc1\n"
"buffer_load_dwordx4 v1, s[48:51], s75 offen offset:1024 sc1 lds\n"
"v_mfma_f32_16x16x32_bf16 v[208:211], a[84:87], a[116:119], v[208:211]\n"
"global_load_dwordx4 a[68:71], v0, s[34:35] offset:-3072 sc1\n"
"v_mfma_f32_16x16x32_bf16 v[236:239], a[92:95], a[116:119], v[236:239]\n"
"ds_read_b128 a[116:119], v4 offset:21504\n"
"v_mfma_f32_16x16x32_bf16 v[236:239], a[88:91], a[112:115], v[236:239]\n"
"ds_read_b128 a[112:115], v4 offset:20480\n"
"v_mfma_f32_16x16x32_bf16 v[240:243], a[88:91], a[120:123], v[240:243]\n"
"v_mfma_f32_16x16x32_bf16 v[240:243], a[92:95], a[124:127], v[240:243]\n"
"v_mfma_f32_16x16x32_bf16 v[212:215], a[84:87], a[124:127], v[212:215]\n"
"ds_read_b128 a[124:127], v4 offset:23552\n"
"v_mfma_f32_16x16x32_bf16 v[212:215], a[80:83], a[120:123], v[212:215]\n"
"ds_read_b128 a[120:123], v4 offset:22528\n"
"v_mfma_f32_16x16x32_bf16 v[216:219], a[80:83], a[128:131], v[216:219]\n"
"ds_read_b64_tr_b16 v[24:25], v11 offset:12352\n"
"v_mfma_f32_16x16x32_bf16 v[216:219], a[84:87], a[132:135], v[216:219]\n"
"global_load_dwordx4 a[72:75], v0, s[34:35] offset:-2048 sc1\n"
"ds_read_b64_tr_b16 v[26:27], v11 offset:12800\n"
"v_mfma_f32_16x16x32_bf16 v[244:247], a[92:95], a[132:135], v[244:247]\n"
"ds_read_b128 a[132:135], v4 offset:25600\n"
"v_mfma_f32_16x16x32_bf16 v[244:247], a[88:91], a[128:131], v[244:247]\n"
"ds_read_b128 a[128:131], v4 offset:24576\n"
"v_mfma_f32_16x16x32_bf16 v[248:251], a[88:91], a[136:139], v[248:251]\n"
"ds_read_b64_tr_b16 v[28:29], v11 offset:12360\n"
"v_mfma_f32_16x16x32_bf16 v[248:251], a[92:95], a[140:143], v[248:251]\n"
"ds_read_b64_tr_b16 v[30:31], v11 offset:12808\n"
"v_mfma_f32_16x16x32_bf16 v[220:223], a[84:87], a[140:143], v[220:223]\n"
"ds_read_b128 a[140:143], v4 offset:27648\n"
"v_mfma_f32_16x16x32_bf16 v[220:223], a[80:83], a[136:139], v[220:223]\n"
"ds_read_b128 a[136:139], v4 offset:26624\n"
"v_mfma_f32_16x16x32_bf16 v[224:227], a[80:83], a[144:147], v[224:227]\n"
"v_mfma_f32_16x16x32_bf16 v[224:227], a[84:87], a[148:151], v[224:227]\n"
"global_load_dwordx4 a[76:79], v0, s[34:35] offset:-1024 sc1\n"
"v_mfma_f32_16x16x32_bf16 v[252:255], a[92:95], a[148:151], v[252:255]\n"
"ds_read_b128 a[148:151], v4 offset:29696\n"
"v_mfma_f32_16x16x32_bf16 v[252:255], a[88:91], a[144:147], v[252:255]\n"
"ds_read_b128 a[144:147], v4 offset:28672\n"
"v_mfma_f32_16x16x32_bf16 a[28:31], a[88:91], a[152:155], a[28:31]\n"
"buffer_load_dwordx4 v1, s[48:51], s75 offen offset:2048 sc1 lds\n"
"v_mfma_f32_16x16x32_bf16 a[28:31], a[92:95], a[156:159], a[28:31]\n"
"v_mfma_f32_16x16x32_bf16 a[24:27], a[84:87], a[156:159], a[24:27]\n"
"s_waitcnt lgkmcnt(4)\n"
"ds_read_b128 a[156:159], v4 offset:31744\n"
"v_mfma_f32_16x16x32_bf16 a[24:27], a[80:83], a[152:155], a[24:27]\n"
"s_waitcnt vmcnt(15)\n"
"ds_read_b128 a[152:155], v4 offset:30720\n"
"v_mfma_f32_16x16x32_bf16 v[32:35], a[32:35], a[96:99], v[32:35]\n"
"v_mfma_f32_16x16x32_bf16 v[32:35], a[36:39], a[100:103], v[32:35]\n"
"global_load_dwordx4 a[80:83], v0, s[34:35] offset:0 sc1\n"
"v_mfma_f32_16x16x32_bf16 v[60:63], a[44:47], a[100:103], v[60:63]\n"
"v_mfma_f32_16x16x32_bf16 v[60:63], a[40:43], a[96:99], v[60:63]\n"
"v_mfma_f32_16x16x32_bf16 v[64:67], a[40:43], a[104:107], v[64:67]\n"
"v_mfma_f32_16x16x32_bf16 v[64:67], a[44:47], a[108:111], v[64:67]\n"
"v_mfma_f32_16x16x32_bf16 v[36:39], a[36:39], a[108:111], v[36:39]\n"
"v_mfma_f32_16x16x32_bf16 v[36:39], a[32:35], a[104:107], v[36:39]\n"
"v_mfma_f32_16x16x32_bf16 v[40:43], a[32:35], a[112:115], v[40:43]\n"
"v_mfma_f32_16x16x32_bf16 v[40:43], a[36:39], a[116:119], v[40:43]\n"
"global_load_dwordx4 a[84:87], v0, s[34:35] offset:1024 sc1\n"
"v_mfma_f32_16x16x32_bf16 v[68:71], a[44:47], a[116:119], v[68:71]\n"
"buffer_load_dwordx4 v1, s[48:51], s75 offen offset:3072 sc1 lds\n"
"v_mfma_f32_16x16x32_bf16 v[68:71], a[40:43], a[112:115], v[68:71]\n"
"v_mfma_f32_16x16x32_bf16 v[72:75], a[40:43], a[120:123], v[72:75]\n"
"v_mfma_f32_16x16x32_bf16 v[72:75], a[44:47], a[124:127], v[72:75]\n"
"v_mfma_f32_16x16x32_bf16 v[44:47], a[36:39], a[124:127], v[44:47]\n"
"v_mfma_f32_16x16x32_bf16 v[44:47], a[32:35], a[120:123], v[44:47]\n"
"s_waitcnt lgkmcnt(4)\n"
"v_mfma_f32_16x16x32_bf16 v[48:51], a[32:35], a[128:131], v[48:51]\n"
"v_mfma_f32_16x16x32_bf16 v[48:51], a[36:39], a[132:135], v[48:51]\n"
"global_load_dwordx4 a[88:91], v0, s[34:35] offset:2048 sc1\n"
"v_mfma_f32_16x16x32_bf16 v[76:79], a[44:47], a[132:135], v[76:79]\n"
"v_mfma_f32_16x16x32_bf16 v[76:79], a[40:43], a[128:131], v[76:79]\n"
"v_mfma_f32_16x16x32_bf16 v[80:83], a[40:43], a[136:139], v[80:83]\n"
"v_mfma_f32_16x16x32_bf16 v[80:83], a[44:47], a[140:143], v[80:83]\n"
"v_mfma_f32_16x16x32_bf16 v[52:55], a[36:39], a[140:143], v[52:55]\n"
"v_mfma_f32_16x16x32_bf16 v[52:55], a[32:35], a[136:139], v[52:55]\n"
"s_waitcnt lgkmcnt(0)\n"
"v_mfma_f32_16x16x32_bf16 v[56:59], a[32:35], a[144:147], v[56:59]\n"
"v_mfma_f32_16x16x32_bf16 v[56:59], a[36:39], a[148:151], v[56:59]\n"
"s_add_u32 m0, s64, 8192\n"
"global_load_dwordx4 a[92:95], v0, s[34:35] offset:3072 sc1\n"
"v_mfma_f32_16x16x32_bf16 v[84:87], a[44:47], a[148:151], v[84:87]\n"
"v_mfma_f32_16x16x32_bf16 v[84:87], a[40:43], a[144:147], v[84:87]\n"
"v_mfma_f32_16x16x32_bf16 a[4:7], a[40:43], a[152:155], a[4:7]\n"
"s_add_u32 s34, s34, 8192\n"
"v_mfma_f32_16x16x32_bf16 a[4:7], a[44:47], a[156:159], a[4:7]\n"
"v_mfma_f32_16x16x32_bf16 a[0:3], a[36:39], a[156:159], a[0:3]\n"
"v_mfma_f32_16x16x32_bf16 a[0:3], a[32:35], a[152:155], a[0:3]\n"
"s_waitcnt vmcnt(14)\n"
"v_mfma_f32_16x16x32_bf16 v[88:91], a[48:51], a[96:99], v[88:91]\n"
"v_mfma_f32_16x16x32_bf16 v[88:91], a[52:55], a[100:103], v[88:91]\n"
"global_load_dwordx4 a[32:35], v0, s[34:35] offset:-4096 sc1\n"
"v_mfma_f32_16x16x32_bf16 v[116:119], a[60:63], a[100:103], v[116:119]\n"
"v_mfma_f32_16x16x32_bf16 v[116:119], a[56:59], a[96:99], v[116:119]\n"
"v_mfma_f32_16x16x32_bf16 v[120:123], a[56:59], a[104:107], v[120:123]\n"
"v_mfma_f32_16x16x32_bf16 v[120:123], a[60:63], a[108:111], v[120:123]\n"
"v_mfma_f32_16x16x32_bf16 v[92:95], a[52:55], a[108:111], v[92:95]\n"
"v_mfma_f32_16x16x32_bf16 v[92:95], a[48:51], a[104:107], v[92:95]\n"
"v_mfma_f32_16x16x32_bf16 v[96:99], a[48:51], a[112:115], v[96:99]\n"
"buffer_load_dwordx4 v0, s[48:51], s69 offen offset:0 sc1 lds\n"
"v_mfma_f32_16x16x32_bf16 v[96:99], a[52:55], a[116:119], v[96:99]\n"
"global_load_dwordx4 a[36:39], v0, s[34:35] offset:-3072 sc1\n"
"v_mfma_f32_16x16x32_bf16 v[124:127], a[60:63], a[116:119], v[124:127]\n"
"v_mfma_f32_16x16x32_bf16 v[124:127], a[56:59], a[112:115], v[124:127]\n"
"v_mfma_f32_16x16x32_bf16 v[128:131], a[56:59], a[120:123], v[128:131]\n"
"v_mfma_f32_16x16x32_bf16 v[128:131], a[60:63], a[124:127], v[128:131]\n"
"v_mfma_f32_16x16x32_bf16 v[100:103], a[52:55], a[124:127], v[100:103]\n"
"v_mfma_f32_16x16x32_bf16 v[100:103], a[48:51], a[120:123], v[100:103]\n"
"v_mfma_f32_16x16x32_bf16 v[104:107], a[48:51], a[128:131], v[104:107]\n"
"v_mfma_f32_16x16x32_bf16 v[104:107], a[52:55], a[132:135], v[104:107]\n"
"global_load_dwordx4 a[40:43], v0, s[34:35] offset:-2048 sc1\n"
"v_mfma_f32_16x16x32_bf16 v[132:135], a[60:63], a[132:135], v[132:135]\n"
"v_mfma_f32_16x16x32_bf16 v[132:135], a[56:59], a[128:131], v[132:135]\n"
"v_mfma_f32_16x16x32_bf16 v[136:139], a[56:59], a[136:139], v[136:139]\n"
"v_mfma_f32_16x16x32_bf16 v[136:139], a[60:63], a[140:143], v[136:139]\n"
"v_mfma_f32_16x16x32_bf16 v[108:111], a[52:55], a[140:143], v[108:111]\n"
"buffer_load_dwordx4 v0, s[48:51], s69 offen offset:1024 sc1 lds\n"
"v_mfma_f32_16x16x32_bf16 v[108:111], a[48:51], a[136:139], v[108:111]\n"
"v_mfma_f32_16x16x32_bf16 v[112:115], a[48:51], a[144:147], v[112:115]\n"
"v_mfma_f32_16x16x32_bf16 v[112:115], a[52:55], a[148:151], v[112:115]\n"
"global_load_dwordx4 a[44:47], v0, s[34:35] offset:-1024 sc1\n"
"v_mfma_f32_16x16x32_bf16 v[140:143], a[60:63], a[148:151], v[140:143]\n"
"v_mfma_f32_16x16x32_bf16 v[140:143], a[56:59], a[144:147], v[140:143]\n"
"v_mfma_f32_16x16x32_bf16 a[12:15], a[56:59], a[152:155], a[12:15]\n"
"v_mfma_f32_16x16x32_bf16 a[12:15], a[60:63], a[156:159], a[12:15]\n"
"v_mfma_f32_16x16x32_bf16 a[8:11], a[52:55], a[156:159], a[8:11]\n"
"v_mfma_f32_16x16x32_bf16 a[8:11], a[48:51], a[152:155], a[8:11]\n"
"s_waitcnt vmcnt(12)\n"
"v_mfma_f32_16x16x32_bf16 v[144:147], a[64:67], a[96:99], v[144:147]\n"
"v_mfma_f32_16x16x32_bf16 v[144:147], a[68:71], a[100:103], v[144:147]\n"
"global_load_dwordx4 a[48:51], v0, s[34:35] offset:0 sc1\n"
"v_mfma_f32_16x16x32_bf16 v[172:175], a[76:79], a[100:103], v[172:175]\n"
"v_mfma_f32_16x16x32_bf16 v[172:175], a[72:75], a[96:99], v[172:175]\n"
"v_mfma_f32_16x16x32_bf16 v[176:179], a[72:75], a[104:107], v[176:179]\n"
"v_mfma_f32_16x16x32_bf16 v[176:179], a[76:79], a[108:111], v[176:179]\n"
"buffer_load_dwordx4 v0, s[48:51], s69 offen offset:2048 sc1 lds\n"
"v_mfma_f32_16x16x32_bf16 v[148:151], a[68:71], a[108:111], v[148:151]\n"
"v_mfma_f32_16x16x32_bf16 v[148:151], a[64:67], a[104:107], v[148:151]\n"
"v_mfma_f32_16x16x32_bf16 v[152:155], a[64:67], a[112:115], v[152:155]\n"
"v_lshlrev_b32 v16, 16, v18\n"
"v_mfma_f32_16x16x32_bf16 v[152:155], a[68:71], a[116:119], v[152:155]\n"
"global_load_dwordx4 a[52:55], v0, s[34:35] offset:1024 sc1\n"
"v_and_b32 v17, s63, v18\n"
"v_mfma_f32_16x16x32_bf16 v[180:183], a[76:79], a[116:119], v[180:183]\n"
"v_lshlrev_b32 v18, 16, v19\n"
"v_mfma_f32_16x16x32_bf16 v[180:183], a[72:75], a[112:115], v[180:183]\n"
"v_and_b32 v19, s63, v19\n"
"v_mfma_f32_16x16x32_bf16 v[184:187], a[72:75], a[120:123], v[184:187]\n"
"v_add_f32 v32, v32, v16\n"
"v_mfma_f32_16x16x32_bf16 v[184:187], a[76:79], a[124:127], v[184:187]\n"
"v_add_f32 v33, v33, v17\n"
"v_add_f32 v34, v34, v18\n"
"v_mfma_f32_16x16x32_bf16 v[156:159], a[68:71], a[124:127], v[156:159]\n"
"v_add_f32 v35, v35, v19\n"
"v_mfma_f32_16x16x32_bf16 v[156:159], a[64:67], a[120:123], v[156:159]\n"
"v_add_f32 v36, v36, v16\n"
"v_mfma_f32_16x16x32_bf16 v[160:163], a[64:67], a[128:131], v[160:163]\n"
"v_add_f32 v37, v37, v17\n"
"v_mfma_f32_16x16x32_bf16 v[160:163], a[68:71], a[132:135], v[160:163]\n"
"global_load_dwordx4 a[56:59], v0, s[34:35] offset:2048 sc1\n"
"v_add_f32 v38, v38, v18\n"
"v_mfma_f32_16x16x32_bf16 v[188:191], a[76:79], a[132:135], v[188:191]\n"
"v_add_f32 v39, v39, v19\n"
"v_mfma_f32_16x16x32_bf16 v[188:191], a[72:75], a[128:131], v[188:191]\n"
"v_add_f32 v40, v40, v16\n"
"v_add_f32 v41, v41, v17\n"
"v_mfma_f32_16x16x32_bf16 v[192:195], a[72:75], a[136:139], v[192:195]\n"
"buffer_load_dwordx4 v0, s[48:51], s69 offen offset:3072 sc1 lds\n"
"v_mfma_f32_16x16x32_bf16 v[192:195], a[76:79], a[140:143], v[192:195]\n"
"v_add_f32 v42, v42, v18\n"
"v_mfma_f32_16x16x32_bf16 v[164:167], a[68:71], a[140:143], v[164:167]\n"
"v_add_f32 v43, v43, v19\n"
"v_mfma_f32_16x16x32_bf16 v[164:167], a[64:67], a[136:139], v[164:167]\n"
"v_add_f32 v44, v44, v16\n"
"v_add_f32 v45, v45, v17\n"
"v_mfma_f32_16x16x32_bf16 v[168:171], a[64:67], a[144:147], v[168:171]\n"
"v_add_f32 v46, v46, v18\n"
"v_mfma_f32_16x16x32_bf16 v[168:171], a[68:71], a[148:151], v[168:171]\n"
"global_load_dwordx4 a[60:63], v0, s[34:35] offset:3072 sc1\n"
"v_add_f32 v47, v47, v19\n"
"v_mfma_f32_16x16x32_bf16 v[196:199], a[76:79], a[148:151], v[196:199]\n"
"v_add_f32 v48, v48, v16\n"
"v_mfma_f32_16x16x32_bf16 v[196:199], a[72:75], a[144:147], v[196:199]\n"
"v_add_f32 v49, v49, v17\n"
"v_mfma_f32_16x16x32_bf16 a[20:23], a[72:75], a[152:155], a[20:23]\n"
"s_add_u32 s34, s34, 8192\n"
"v_add_f32 v50, v50, v18\n"
"v_mfma_f32_16x16x32_bf16 a[20:23], a[76:79], a[156:159], a[20:23]\n"
"v_add_f32 v51, v51, v19\n"
"v_mfma_f32_16x16x32_bf16 a[16:19], a[68:71], a[156:159], a[16:19]\n"
"v_add_f32 v52, v52, v16\n"
"v_mfma_f32_16x16x32_bf16 a[16:19], a[64:67], a[152:155], a[16:19]\n"
"s_waitcnt vmcnt(12)\n"
"v_add_f32 v53, v53, v17\n"
"v_mfma_f32_16x16x32_bf16 v[200:203], a[80:83], a[96:99], v[200:203]\n"
"v_add_f32 v54, v54, v18\n"
"v_add_f32 v55, v55, v19\n"
"v_mfma_f32_16x16x32_bf16 v[200:203], a[84:87], a[100:103], v[200:203]\n"
"global_load_dwordx4 a[64:67], v0, s[34:35] offset:-4096 sc1\n"
"v_add_f32 v56, v56, v16\n"
"v_mfma_f32_16x16x32_bf16 v[228:231], a[92:95], a[100:103], v[228:231]\n"
"s_add_u32 m0, s64, 12288\n"
"ds_read_b128 a[100:103], v5 offset:17408\n"
"v_add_f32 v57, v57, v17\n"
"v_mfma_f32_16x16x32_bf16 v[228:231], a[88:91], a[96:99], v[228:231]\n"
"ds_read_b128 a[96:99], v5 offset:16384\n"
"v_add_f32 v58, v58, v18\n"
"v_mfma_f32_16x16x32_bf16 v[232:235], a[88:91], a[104:107], v[232:235]\n"
"v_add_f32 v59, v59, v19\n"
"v_mfma_f32_16x16x32_bf16 v[232:235], a[92:95], a[108:111], v[232:235]\n"
"v_accvgpr_read_b32 v12, a0\n"
"v_mfma_f32_16x16x32_bf16 v[204:207], a[84:87], a[108:111], v[204:207]\n"
"ds_read_b128 a[108:111], v5 offset:19456\n"
"v_accvgpr_read_b32 v13, a1\n"
"v_mfma_f32_16x16x32_bf16 v[204:207], a[80:83], a[104:107], v[204:207]\n"
"ds_read_b128 a[104:107], v5 offset:18432\n"
"v_accvgpr_read_b32 v14, a2\n"
"v_mfma_f32_16x16x32_bf16 v[208:211], a[80:83], a[112:115], v[208:211]\n"
"v_accvgpr_read_b32 v15, a3\n"
"v_mfma_f32_16x16x32_bf16 v[208:211], a[84:87], a[116:119], v[208:211]\n"
"global_load_dwordx4 a[68:71], v0, s[34:35] offset:-3072 sc1\n"
"v_add_f32 v12, v12, v16\n"
"v_mfma_f32_16x16x32_bf16 v[236:239], a[92:95], a[116:119], v[236:239]\n"
"ds_read_b128 a[116:119], v5 offset:21504\n"
"v_add_f32 v13, v13, v17\n"
"v_mfma_f32_16x16x32_bf16 v[236:239], a[88:91], a[112:115], v[236:239]\n"
"ds_read_b128 a[112:115], v5 offset:20480\n"
"v_add_f32 v14, v14, v18\n"
"v_mfma_f32_16x16x32_bf16 v[240:243], a[88:91], a[120:123], v[240:243]\n"
"v_add_f32 v15, v15, v19\n"
"v_accvgpr_write_b32 a0, v12\n"
"v_mfma_f32_16x16x32_bf16 v[240:243], a[92:95], a[124:127], v[240:243]\n"
"v_accvgpr_write_b32 a1, v13\n"
"v_mfma_f32_16x16x32_bf16 v[212:215], a[84:87], a[124:127], v[212:215]\n"
"ds_read_b128 a[124:127], v5 offset:23552\n"
"v_accvgpr_write_b32 a2, v14\n"
"v_mfma_f32_16x16x32_bf16 v[212:215], a[80:83], a[120:123], v[212:215]\n"
"ds_read_b128 a[120:123], v5 offset:22528\n"
"v_accvgpr_write_b32 a3, v15\n"
"v_mfma_f32_16x16x32_bf16 v[216:219], a[80:83], a[128:131], v[216:219]\n"
"buffer_load_dwordx4 v1, s[48:51], s69 offen offset:0 sc1 lds\n"
"v_mfma_f32_16x16x32_bf16 v[216:219], a[84:87], a[132:135], v[216:219]\n"
"global_load_dwordx4 a[72:75], v0, s[34:35] offset:-2048 sc1\n"
"v_lshrrev_b32 v16, 4, v0\n"
"v_mfma_f32_16x16x32_bf16 v[244:247], a[92:95], a[132:135], v[244:247]\n"
"ds_read_b128 a[132:135], v5 offset:25600\n"
"v_and_b32 v16, 0x30, v16\n"
"v_mfma_f32_16x16x32_bf16 v[244:247], a[88:91], a[128:131], v[244:247]\n"
"ds_read_b128 a[128:131], v5 offset:24576\n"
"buffer_load_dwordx2 v[18:19], v16, s[44:47], s61 offen offset:64\n"
"v_mfma_f32_16x16x32_bf16 v[248:251], a[88:91], a[136:139], v[248:251]\n"
"s_getpc_b64 s[96:97]\n"
"L_c6_:\n"
"s_add_u32 s96, s96, L_last-L_c6_\n"
"s_addc_u32 s97, s97, 0\n"
"v_mfma_f32_16x16x32_bf16 v[248:251], a[92:95], a[140:143], v[248:251]\n"
"s_lshr_b32 s66, %26, 3\n"
"v_mfma_f32_16x16x32_bf16 v[220:223], a[84:87], a[140:143], v[220:223]\n"
"s_lshl2_add_u32 s66, s66, %16\n"
"ds_read_b128 a[140:143], v5 offset:27648\n"
"v_mfma_f32_16x16x32_bf16 v[220:223], a[80:83], a[136:139], v[220:223]\n"
"ds_read_b128 a[136:139], v5 offset:26624\n"
"v_mfma_f32_16x16x32_bf16 v[224:227], a[80:83], a[144:147], v[224:227]\n"
"s_mul_i32 s66, s66, 256\n"
"v_mfma_f32_16x16x32_bf16 v[224:227], a[84:87], a[148:151], v[224:227]\n"
"global_load_dwordx4 a[76:79], v0, s[34:35] offset:-1024 sc1\n"
"v_mfma_f32_16x16x32_bf16 v[252:255], a[92:95], a[148:151], v[252:255]\n"
"ds_read_b128 a[148:151], v5 offset:29696\n"
"v_mfma_f32_16x16x32_bf16 v[252:255], a[88:91], a[144:147], v[252:255]\n"
"s_min_u32 s66, s66, L_headF-L_last-256\n"
"ds_read_b128 a[144:147], v5 offset:28672\n"
"v_mfma_f32_16x16x32_bf16 a[28:31], a[88:91], a[152:155], a[28:31]\n"
"v_mfma_f32_16x16x32_bf16 a[28:31], a[92:95], a[156:159], a[28:31]\n"
"v_min_u32 v11, 16, v0\n"
"v_mfma_f32_16x16x32_bf16 a[24:27], a[84:87], a[156:159], a[24:27]\n"
"ds_read_b128 a[156:159], v5 offset:31744\n"
"v_mfma_f32_16x16x32_bf16 a[24:27], a[80:83], a[152:155], a[24:27]\n"
"s_waitcnt vmcnt(12) lgkmcnt(11)\n"
"ds_read_b128 a[152:155], v5 offset:30720\n"
"v_mfma_f32_16x16x32_bf16 v[32:35], a[32:35], a[96:99], v[32:35]\n"
"buffer_load_dwordx4 v1, s[48:51], s69 offen offset:1024 sc1 lds\n"
"v_mfma_f32_16x16x32_bf16 v[32:35], a[36:39], a[100:103], v[32:35]\n"
"global_load_dwordx4 a[80:83], v0, s[34:35] offset:0 sc1\n"
"v_mfma_f32_16x16x32_bf16 v[60:63], a[44:47], a[100:103], v[60:63]\n"
"v_lshl_add_u32 v11, v11, 3, s66\n"
"v_mfma_f32_16x16x32_bf16 v[60:63], a[40:43], a[96:99], v[60:63]\n"
"global_load_dword v10, v11, s[96:97]\n"
"v_mfma_f32_16x16x32_bf16 v[64:67], a[40:43], a[104:107], v[64:67]\n"
"v_mfma_f32_16x16x32_bf16 v[64:67], a[44:47], a[108:111], v[64:67]\n"
"v_mfma_f32_16x16x32_bf16 v[36:39], a[36:39], a[108:111], v[36:39]\n"
"v_mfma_f32_16x16x32_bf16 v[36:39], a[32:35], a[104:107], v[36:39]\n"
"s_waitcnt lgkmcnt(8)\n"
"v_mfma_f32_16x16x32_bf16 v[40:43], a[32:35], a[112:115], v[40:43]\n"
"v_mfma_f32_16x16x32_bf16 v[40:43], a[36:39], a[116:119], v[40:43]\n"
"global_load_dwordx4 a[84:87], v0, s[34:35] offset:1024 sc1\n"
"v_mfma_f32_16x16x32_bf16 v[68:71], a[44:47], a[116:119], v[68:71]\n"
"v_mfma_f32_16x16x32_bf16 v[68:71], a[40:43], a[112:115], v[68:71]\n"
"v_mfma_f32_16x16x32_bf16 v[72:75], a[40:43], a[120:123], v[72:75]\n"
"v_mfma_f32_16x16x32_bf16 v[72:75], a[44:47], a[124:127], v[72:75]\n"
"v_mfma_f32_16x16x32_bf16 v[44:47], a[36:39], a[124:127], v[44:47]\n"
"buffer_load_dwordx4 v1, s[48:51], s69 offen offset:2048 sc1 lds\n"
"v_mfma_f32_16x16x32_bf16 v[44:47], a[32:35], a[120:123], v[44:47]\n"
"s_waitcnt lgkmcnt(4)\n"
"v_mfma_f32_16x16x32_bf16 v[48:51], a[32:35], a[128:131], v[48:51]\n"
"v_mfma_f32_16x16x32_bf16 v[48:51], a[36:39], a[132:135], v[48:51]\n"
"global_load_dwordx4 a[88:91], v0, s[34:35] offset:2048 sc1\n"
"v_mfma_f32_16x16x32_bf16 v[76:79], a[44:47], a[132:135], v[76:79]\n"
"v_mfma_f32_16x16x32_bf16 v[76:79], a[40:43], a[128:131], v[76:79]\n"
"v_mfma_f32_16x16x32_bf16 v[80:83], a[40:43], a[136:139], v[80:83]\n"
"v_mfma_f32_16x16x32_bf16 v[80:83], a[44:47], a[140:143], v[80:83]\n"
"v_mfma_f32_16x16x32_bf16 v[52:55], a[36:39], a[140:143], v[52:55]\n"
"v_mfma_f32_16x16x32_bf16 v[52:55], a[32:35], a[136:139], v[52:55]\n"
"s_waitcnt lgkmcnt(0)\n"
"v_mfma_f32_16x16x32_bf16 v[56:59], a[32:35], a[144:147], v[56:59]\n"
"v_mfma_f32_16x16x32_bf16 v[56:59], a[36:39], a[148:151], v[56:59]\n"
"global_load_dwordx4 a[92:95], v0, s[34:35] offset:3072 sc1\n"
"v_mfma_f32_16x16x32_bf16 v[84:87], a[44:47], a[148:151], v[84:87]\n"
"v_mfma_f32_16x16x32_bf16 v[84:87], a[40:43], a[144:147], v[84:87]\n"
"v_mfma_f32_16x16x32_bf16 a[4:7], a[40:43], a[152:155], a[4:7]\n"
"s_add_u32 s34, s34, 8192\n"
"v_mfma_f32_16x16x32_bf16 a[4:7], a[44:47], a[156:159], a[4:7]\n"
"buffer_load_dwordx4 v1, s[48:51], s69 offen offset:3072 sc1 lds\n"
"v_mfma_f32_16x16x32_bf16 a[0:3], a[36:39], a[156:159], a[0:3]\n"
"v_mfma_f32_16x16x32_bf16 a[0:3], a[32:35], a[152:155], a[0:3]\n"
"s_waitcnt vmcnt(14)\n"
"v_mfma_f32_16x16x32_bf16 v[88:91], a[48:51], a[96:99], v[88:91]\n"
"v_mfma_f32_16x16x32_bf16 v[88:91], a[52:55], a[100:103], v[88:91]\n"
"global_load_dwordx4 a[32:35], v0, s[34:35] offset:-4096 sc1\n"
"v_mfma_f32_16x16x32_bf16 v[116:119], a[60:63], a[100:103], v[116:119]\n"
"v_mfma_f32_16x16x32_bf16 v[116:119], a[56:59], a[96:99], v[116:119]\n"
"v_mfma_f32_16x16x32_bf16 v[120:123], a[56:59], a[104:107], v[120:123]\n"
"v_mfma_f32_16x16x32_bf16 v[120:123], a[60:63], a[108:111], v[120:123]\n"
"v_mfma_f32_16x16x32_bf16 v[92:95], a[52:55], a[108:111], v[92:95]\n"
"v_mfma_f32_16x16x32_bf16 v[92:95], a[48:51], a[104:107], v[92:95]\n"
"v_mfma_f32_16x16x32_bf16 v[96:99], a[48:51], a[112:115], v[96:99]\n"
"v_mfma_f32_16x16x32_bf16 v[96:99], a[52:55], a[116:119], v[96:99]\n"
"global_load_dwordx4 a[36:39], v0, s[34:35] offset:-3072 sc1\n"
"v_mfma_f32_16x16x32_bf16 v[124:127], a[60:63], a[116:119], v[124:127]\n"
"v_mfma_f32_16x16x32_bf16 v[124:127], a[56:59], a[112:115], v[124:127]\n"
"v_mfma_f32_16x16x32_bf16 v[128:131], a[56:59], a[120:123], v[128:131]\n"
"v_mfma_f32_16x16x32_bf16 v[128:131], a[60:63], a[124:127], v[128:131]\n"
"v_mfma_f32_16x16x32_bf16 v[100:103], a[52:55], a[124:127], v[100:103]\n"
"v_mfma_f32_16x16x32_bf16 v[100:103], a[48:51], a[120:123], v[100:103]\n"
"v_mfma_f32_16x16x32_bf16 v[104:107], a[48:51], a[128:131], v[104:107]\n"
"buffer_store_dwordx4 v[24:27], v1, s[36:39], s28 offen offset:0 sc1\n"
"buffer_load_dwordx2 v[8:9], off, s[56:59], %14 sc1\n"
"v_mfma_f32_16x16x32_bf16 v[104:107], a[52:55], a[132:135], v[104:107]\n"
"global_load_dwordx4 a[40:43], v0, s[34:35] offset:-2048 sc1\n"
"v_mfma_f32_16x16x32_bf16 v[132:135], a[60:63], a[132:135], v[132:135]\n"
"v_mfma_f32_16x16x32_bf16 v[132:135], a[56:59], a[128:131], v[132:135]\n"
"v_mfma_f32_16x16x32_bf16 v[136:139], a[56:59], a[136:139], v[136:139]\n"
"v_mfma_f32_16x16x32_bf16 v[136:139], a[60:63], a[140:143], v[136:139]\n"
"v_mfma_f32_16x16x32_bf16 v[108:111], a[52:55], a[140:143], v[108:111]\n"
"v_mfma_f32_16x16x32_bf16 v[108:111], a[48:51], a[136:139], v[108:111]\n"
"v_mfma_f32_16x16x32_bf16 v[112:115], a[48:51], a[144:147], v[112:115]\n"
"v_mfma_f32_16x16x32_bf16 v[112:115], a[52:55], a[148:151], v[112:115]\n"
"global_load_dwordx4 a[44:47], v0, s[34:35] offset:-1024 sc1\n"
"v_mfma_f32_16x16x32_bf16 v[140:143], a[60:63], a[148:151], v[140:143]\n"
"v_mfma_f32_16x16x32_bf16 v[140:143], a[56:59], a[144:147], v[140:143]\n"
"v_mfma_f32_16x16x32_bf16 a[12:15], a[56:59], a[152:155], a[12:15]\n"
"v_mfma_f32_16x16x32_bf16 a[12:15], a[60:63], a[156:159], a[12:15]\n"
"v_mfma_f32_16x16x32_bf16 a[8:11], a[52:55], a[156:159], a[8:11]\n"
"v_mfma_f32_16x16x32_bf16 a[8:11], a[48:51], a[152:155], a[8:11]\n"
"s_waitcnt vmcnt(14)\n"
"v_mfma_f32_16x16x32_bf16 v[144:147], a[64:67], a[96:99], v[144:147]\n"
"v_mfma_f32_16x16x32_bf16 v[144:147], a[68:71], a[100:103], v[144:147]\n"
"global_load_dwordx4 a[48:51], v0, s[34:35] offset:0 sc1\n"
"v_mfma_f32_16x16x32_bf16 v[172:175], a[76:79], a[100:103], v[172:175]\n"
"v_mfma_f32_16x16x32_bf16 v[172:175], a[72:75], a[96:99], v[172:175]\n"
"v_mfma_f32_16x16x32_bf16 v[176:179], a[72:75], a[104:107], v[176:179]\n"
"v_mfma_f32_16x16x32_bf16 v[176:179], a[76:79], a[108:111], v[176:179]\n"
"v_mfma_f32_16x16x32_bf16 v[148:151], a[68:71], a[108:111], v[148:151]\n"
"v_mfma_f32_16x16x32_bf16 v[148:151], a[64:67], a[104:107], v[148:151]\n"
"v_mfma_f32_16x16x32_bf16 v[152:155], a[64:67], a[112:115], v[152:155]\n"
"v_lshlrev_b32 v20, 16, v22\n"
"v_mfma_f32_16x16x32_bf16 v[152:155], a[68:71], a[116:119], v[152:155]\n"
"global_load_dwordx4 a[52:55], v0, s[34:35] offset:1024 sc1\n"
"v_and_b32 v21, s63, v22\n"
"v_mfma_f32_16x16x32_bf16 v[180:183], a[76:79], a[116:119], v[180:183]\n"
"v_lshlrev_b32 v22, 16, v23\n"
"v_mfma_f32_16x16x32_bf16 v[180:183], a[72:75], a[112:115], v[180:183]\n"
"v_and_b32 v23, s63, v23\n"
"v_mfma_f32_16x16x32_bf16 v[184:187], a[72:75], a[120:123], v[184:187]\n"
"v_add_f32 v60, v60, v20\n"
"v_mfma_f32_16x16x32_bf16 v[184:187], a[76:79], a[124:127], v[184:187]\n"
"v_add_f32 v61, v61, v21\n"
"v_mfma_f32_16x16x32_bf16 v[156:159], a[68:71], a[124:127], v[156:159]\n"
"buffer_store_dwordx4 v[28:31], v1, s[36:39], s28 offen offset:2048 sc1\n"
"v_add_f32 v62, v62, v22\n"
"v_add_f32 v63, v63, v23\n"
"v_add_f32 v64, v64, v20\n"
"v_mfma_f32_16x16x32_bf16 v[156:159], a[64:67], a[120:123], v[156:159]\n"
"v_add_f32 v65, v65, v21\n"
"v_mfma_f32_16x16x32_bf16 v[160:163], a[64:67], a[128:131], v[160:163]\n"
"v_add_f32 v66, v66, v22\n"
"v_mfma_f32_16x16x32_bf16 v[160:163], a[68:71], a[132:135], v[160:163]\n"
"global_load_dwordx4 a[56:59], v0, s[34:35] offset:2048 sc1\n"
"v_mfma_f32_16x16x32_bf16 v[188:191], a[76:79], a[132:135], v[188:191]\n"
"v_add_f32 v67, v67, v23\n"
"v_mfma_f32_16x16x32_bf16 v[188:191], a[72:75], a[128:131], v[188:191]\n"
"v_add_f32 v68, v68, v20\n"
"v_mfma_f32_16x16x32_bf16 v[192:195], a[72:75], a[136:139], v[192:195]\n"
"v_add_f32 v69, v69, v21\n"
"v_mfma_f32_16x16x32_bf16 v[192:195], a[76:79], a[140:143], v[192:195]\n"
"v_add_f32 v70, v70, v22\n"
"v_mfma_f32_16x16x32_bf16 v[164:167], a[68:71], a[140:143], v[164:167]\n"
"v_add_f32 v71, v71, v23\n"
"v_mfma_f32_16x16x32_bf16 v[164:167], a[64:67], a[136:139], v[164:167]\n"
"v_add_f32 v72, v72, v20\n"
"v_add_f32 v73, v73, v21\n"
"v_mfma_f32_16x16x32_bf16 v[168:171], a[64:67], a[144:147], v[168:171]\n"
"v_add_f32 v74, v74, v22\n"
"v_mfma_f32_16x16x32_bf16 v[168:171], a[68:71], a[148:151], v[168:171]\n"
"global_load_dwordx4 a[60:63], v0, s[34:35] offset:3072 sc1\n"
"v_add_f32 v75, v75, v23\n"
"v_mfma_f32_16x16x32_bf16 v[196:199], a[76:79], a[148:151], v[196:199]\n"
"v_add_f32 v76, v76, v20\n"
"v_mfma_f32_16x16x32_bf16 v[196:199], a[72:75], a[144:147], v[196:199]\n"
"v_add_f32 v77, v77, v21\n"
"v_mfma_f32_16x16x32_bf16 a[20:23], a[72:75], a[152:155], a[20:23]\n"
"s_add_u32 s34, s34, 8192\n"
"v_add_f32 v78, v78, v22\n"
"v_mfma_f32_16x16x32_bf16 a[20:23], a[76:79], a[156:159], a[20:23]\n"
"v_add_f32 v79, v79, v23\n"
"v_mfma_f32_16x16x32_bf16 a[16:19], a[68:71], a[156:159], a[16:19]\n"
"v_add_f32 v80, v80, v20\n"
"v_mfma_f32_16x16x32_bf16 a[16:19], a[64:67], a[152:155], a[16:19]\n"
"s_waitcnt vmcnt(12)\n"
"v_add_f32 v81, v81, v21\n"
"v_mfma_f32_16x16x32_bf16 v[200:203], a[80:83], a[96:99], v[200:203]\n"
"v_add_f32 v82, v82, v22\n"
"v_add_f32 v83, v83, v23\n"
"v_mfma_f32_16x16x32_bf16 v[200:203], a[84:87], a[100:103], v[200:203]\n"
"global_load_dwordx4 a[64:67], v0, s[34:35] offset:-4096 sc1\n"
"v_add_f32 v84, v84, v20\n"
"v_mfma_f32_16x16x32_bf16 v[228:231], a[92:95], a[100:103], v[228:231]\n"
"s_waitcnt vmcnt(12)\n"
"ds_read_b128 a[100:103], v2 offset:1024\n"
"v_add_f32 v85, v85, v21\n"
"v_mfma_f32_16x16x32_bf16 v[228:231], a[88:91], a[96:99], v[228:231]\n"
"ds_read_b128 a[96:99], v2 offset:0\n"
"v_add_f32 v86, v86, v22\n"
"v_mfma_f32_16x16x32_bf16 v[232:235], a[88:91], a[104:107], v[232:235]\n"
"v_add_f32 v87, v87, v23\n"
"v_mfma_f32_16x16x32_bf16 v[232:235], a[92:95], a[108:111], v[232:235]\n"
"v_accvgpr_read_b32 v12, a4\n"
"v_mfma_f32_16x16x32_bf16 v[204:207], a[84:87], a[108:111], v[204:207]\n"
"ds_read_b128 a[108:111], v2 offset:3072\n"
"v_accvgpr_read_b32 v13, a5\n"
"v_mfma_f32_16x16x32_bf16 v[204:207], a[80:83], a[104:107], v[204:207]\n"
"ds_read_b128 a[104:107], v2 offset:2048\n"
"v_accvgpr_read_b32 v14, a6\n"
"v_mfma_f32_16x16x32_bf16 v[208:211], a[80:83], a[112:115], v[208:211]\n"
"v_accvgpr_read_b32 v15, a7\n"
"v_mfma_f32_16x16x32_bf16 v[208:211], a[84:87], a[116:119], v[208:211]\n"
"global_load_dwordx4 a[68:71], v0, s[34:35] offset:-3072 sc1\n"
"v_add_f32 v12, v12, v20\n"
"v_mfma_f32_16x16x32_bf16 v[236:239], a[92:95], a[116:119], v[236:239]\n"
"ds_read_b128 a[116:119], v2 offset:5120\n"
"v_add_f32 v13, v13, v21\n"
"v_mfma_f32_16x16x32_bf16 v[236:239], a[88:91], a[112:115], v[236:239]\n"
"ds_read_b128 a[112:115], v2 offset:4096\n"
"v_add_f32 v14, v14, v22\n"
"v_mfma_f32_16x16x32_bf16 v[240:243], a[88:91], a[120:123], v[240:243]\n"
"v_add_f32 v15, v15, v23\n"
"v_accvgpr_write_b32 a4, v12\n"
"v_mfma_f32_16x16x32_bf16 v[240:243], a[92:95], a[124:127], v[240:243]\n"
"v_accvgpr_write_b32 a5, v13\n"
"v_mfma_f32_16x16x32_bf16 v[212:215], a[84:87], a[124:127], v[212:215]\n"
"ds_read_b128 a[124:127], v2 offset:7168\n"
"v_accvgpr_write_b32 a6, v14\n"
"v_mfma_f32_16x16x32_bf16 v[212:215], a[80:83], a[120:123], v[212:215]\n"
"ds_read_b128 a[120:123], v2 offset:6144\n"
"v_accvgpr_write_b32 a7, v15\n"
"v_mfma_f32_16x16x32_bf16 v[216:219], a[80:83], a[128:131], v[216:219]\n"
"s_add_u32 s28, s28, 0x40000\n"
"v_mfma_f32_16x16x32_bf16 v[216:219], a[84:87], a[132:135], v[216:219]\n"
"global_load_dwordx4 a[72:75], v0, s[34:35] offset:-2048 sc1\n"
"v_lshrrev_b32 v20, 4, v0\n"
"v_mfma_f32_16x16x32_bf16 v[244:247], a[92:95], a[132:135], v[244:247]\n"
"s_add_u32 s67, s78, s62\n"
"ds_read_b128 a[132:135], v2 offset:9216\n"
"v_and_b32 v20, 0x30, v20\n"
"v_mfma_f32_16x16x32_bf16 v[244:247], a[88:91], a[128:131], v[244:247]\n"
"ds_read_b128 a[128:131], v2 offset:8192\n"
"buffer_load_dwordx2 v[22:23], v20, s[44:47], s61 offen offset:72\n"
"v_mfma_f32_16x16x32_bf16 v[248:251], a[88:91], a[136:139], v[248:251]\n"
"s_add_u32 s67, s67, 1\n"
"v_mfma_f32_16x16x32_bf16 v[248:251], a[92:95], a[140:143], v[248:251]\n"
"v_mfma_f32_16x16x32_bf16 v[220:223], a[84:87], a[140:143], v[220:223]\n"
"ds_read_b128 a[140:143], v2 offset:11264\n"
"v_mov_b32 v24, s67\n"
"v_mfma_f32_16x16x32_bf16 v[220:223], a[80:83], a[136:139], v[220:223]\n"
"ds_read_b128 a[136:139], v2 offset:10240\n"
"v_mov_b32 v25, s79\n"
"v_mfma_f32_16x16x32_bf16 v[224:227], a[80:83], a[144:147], v[224:227]\n"
"v_mfma_f32_16x16x32_bf16 v[224:227], a[84:87], a[148:151], v[224:227]\n"
"s_waitcnt vmcnt(6)\n"
"global_load_dwordx4 a[76:79], v0, s[34:35] offset:-1024 sc1\n"
"buffer_store_dwordx2 v[24:25], off, s[56:59], %24 sc1\n"
"v_mfma_f32_16x16x32_bf16 v[252:255], a[92:95], a[148:151], v[252:255]\n"
"ds_read_b128 a[148:151], v2 offset:13312\n"
"v_mfma_f32_16x16x32_bf16 v[252:255], a[88:91], a[144:147], v[252:255]\n"
"ds_read_b128 a[144:147], v2 offset:12288\n"
"v_mfma_f32_16x16x32_bf16 a[28:31], a[88:91], a[152:155], a[28:31]\n"
"v_mfma_f32_16x16x32_bf16 a[28:31], a[92:95], a[156:159], a[28:31]\n"
"v_mfma_f32_16x16x32_bf16 a[24:27], a[84:87], a[156:159], a[24:27]\n"
"s_cmp_eq_u32 s62, 5\n"
"ds_read_b128 a[156:159], v2 offset:15360\n"
"v_mfma_f32_16x16x32_bf16 a[24:27], a[80:83], a[152:155], a[24:27]\n"
"ds_read_b128 a[152:155], v2 offset:14336\n"
"s_cbranch_scc1 L_last\n"
"s_branch L_P4\n"
"L_last:\n"
"L_st9051:\n"
"s_cbranch_execz L_retB\n"
"s_waitcnt vmcnt(9) lgkmcnt(12)\n"
"v_mfma_f32_16x16x32_bf16 v[32:35], a[32:35], a[96:99], v[32:35]\n"
"s_add_u32 s34, s36, 1454080\n"
"s_add_u32 s48, s48, 131072\n"
"s_addc_u32 s49, s49, 0\n"
"v_mfma_f32_16x16x32_bf16 v[32:35], a[36:39], a[100:103], v[32:35]\n"
"s_mov_b64 exec, 0\n"
"s_cmp_eq_u32 %16, 0\n"
"s_cbranch_scc1 L_st9088\n"
"s_cmp_eq_u32 %16, 1\n"
"s_cbranch_scc1 L_st9650\n"
"s_cmp_eq_u32 %16, 2\n"
"s_cbranch_scc1 L_st10272\n"
"s_branch L_st10883\n"
"L_retC:\n"
"s_mov_b64 exec, -1\n"
"s_nop 3\n"
"global_load_dwordx4 a[80:83], v0, s[34:35] offset:0 sc1\n"
"v_mfma_f32_16x16x32_bf16 v[60:63], a[44:47], a[100:103], v[60:63]\n"
"s_barrier\n"
"v_mfma_f32_16x16x32_bf16 v[60:63], a[40:43], a[96:99], v[60:63]\n"
"v_mfma_f32_16x16x32_bf16 v[64:67], a[40:43], a[104:107], v[64:67]\n"
"v_mfma_f32_16x16x32_bf16 v[64:67], a[44:47], a[108:111], v[64:67]\n"
"v_mfma_f32_16x16x32_bf16 v[36:39], a[36:39], a[108:111], v[36:39]\n"
"v_subrev_u32 v8, s65, v8\n"
"v_mfma_f32_16x16x32_bf16 v[36:39], a[32:35], a[104:107], v[36:39]\n"
"s_waitcnt lgkmcnt(8)\n"
"v_xor_b32 v9, s79, v9\n"
"v_mfma_f32_16x16x32_bf16 v[40:43], a[32:35], a[112:115], v[40:43]\n"
"v_or_b32 v8, v8, v9\n"
"v_mfma_f32_16x16x32_bf16 v[40:43], a[36:39], a[116:119], v[40:43]\n"
"global_load_dwordx4 a[84:87], v0, s[34:35] offset:1024 sc1\n"
"v_cmp_lt_u32 vcc, 15, v8\n"
"v_mfma_f32_16x16x32_bf16 v[68:71], a[44:47], a[116:119], v[68:71]\n"
"s_cbranch_vccnz L_slow4\n"
"L_back4:\n"
"v_mfma_f32_16x16x32_bf16 v[68:71], a[40:43], a[112:115], v[68:71]\n"
"L_st9088:\n"
"s_cbranch_execz L_st11503\n"
"v_mfma_f32_16x16x32_bf16 v[72:75], a[40:43], a[120:123], v[72:75]\n"
"v_mfma_f32_16x16x32_bf16 v[72:75], a[44:47], a[124:127], v[72:75]\n"
"v_mfma_f32_16x16x32_bf16 v[44:47], a[36:39], a[124:127], v[44:47]\n"
"v_mfma_f32_16x16x32_bf16 v[44:47], a[32:35], a[120:123], v[44:47]\n"
"s_add_u32 m0, s64, 16384\n"
"s_waitcnt lgkmcnt(4)\n"
"v_mfma_f32_16x16x32_bf16 v[48:51], a[32:35], a[128:131], v[48:51]\n"
"v_mfma_f32_16x16x32_bf16 v[48:51], a[36:39], a[132:135], v[48:51]\n"
"global_load_dwordx4 a[88:91], v0, s[34:35] offset:2048 sc1\n"
"v_mfma_f32_16x16x32_bf16 v[76:79], a[44:47], a[132:135], v[76:79]\n"
"v_mfma_f32_16x16x32_bf16 v[76:79], a[40:43], a[128:131], v[76:79]\n"
"v_mfma_f32_16x16x32_bf16 v[80:83], a[40:43], a[136:139], v[80:83]\n"
"s_mov_b32 s97, s37\n"
"v_mfma_f32_16x16x32_bf16 v[80:83], a[44:47], a[140:143], v[80:83]\n"
"v_mfma_f32_16x16x32_bf16 v[52:55], a[36:39], a[140:143], v[52:55]\n"
"v_mfma_f32_16x16x32_bf16 v[52:55], a[32:35], a[136:139], v[52:55]\n"
"s_waitcnt lgkmcnt(0)\n"
"v_mfma_f32_16x16x32_bf16 v[56:59], a[32:35], a[144:147], v[56:59]\n"
"buffer_load_dwordx4 v0, s[48:51], s75 offen offset:0 sc1 lds\n"
"v_mfma_f32_16x16x32_bf16 v[56:59], a[36:39], a[148:151], v[56:59]\n"
"global_load_dwordx4 a[92:95], v0, s[34:35] offset:3072 sc1\n"
"v_mfma_f32_16x16x32_bf16 v[84:87], a[44:47], a[148:151], v[84:87]\n"
"v_mfma_f32_16x16x32_bf16 v[84:87], a[40:43], a[144:147], v[84:87]\n"
"v_mfma_f32_16x16x32_bf16 a[4:7], a[40:43], a[152:155], a[4:7]\n"
"v_mfma_f32_16x16x32_bf16 a[4:7], a[44:47], a[156:159], a[4:7]\n"
"v_mfma_f32_16x16x32_bf16 a[0:3], a[36:39], a[156:159], a[0:3]\n"
"v_mfma_f32_16x16x32_bf16 a[0:3], a[32:35], a[152:155], a[0:3]\n"
"s_waitcnt vmcnt(10)\n"
"v_mfma_f32_16x16x32_bf16 v[88:91], a[48:51], a[96:99], v[88:91]\n"
"s_add_u32 s34, s36, 1458176\n"
"v_mfma_f32_16x16x32_bf16 v[88:91], a[52:55], a[100:103], v[88:91]\n"
"global_load_dwordx4 a[32:35], v0, s[34:35] offset:0 sc1\n"
"v_mfma_f32_16x16x32_bf16 v[116:119], a[60:63], a[100:103], v[116:119]\n"
"buffer_load_dwordx4 v0, s[48:51], s75 offen offset:1024 sc1 lds\n"
"v_mfma_f32_16x16x32_bf16 v[116:119], a[56:59], a[96:99], v[116:119]\n"
"v_mfma_f32_16x16x32_bf16 v[120:123], a[56:59], a[104:107], v[120:123]\n"
"v_mfma_f32_16x16x32_bf16 v[120:123], a[60:63], a[108:111], v[120:123]\n"
"v_mfma_f32_16x16x32_bf16 v[92:95], a[52:55], a[108:111], v[92:95]\n"
"v_mfma_f32_16x16x32_bf16 v[92:95], a[48:51], a[104:107], v[92:95]\n"
"v_mfma_f32_16x16x32_bf16 v[96:99], a[48:51], a[112:115], v[96:99]\n"
"v_mfma_f32_16x16x32_bf16 v[96:99], a[52:55], a[116:119], v[96:99]\n"
"global_load_dwordx4 a[36:39], v0, s[34:35] offset:1024 sc1\n"
"v_mfma_f32_16x16x32_bf16 v[124:127], a[60:63], a[116:119], v[124:127]\n"
"v_mfma_f32_16x16x32_bf16 v[124:127], a[56:59], a[112:115], v[124:127]\n"
"v_mfma_f32_16x16x32_bf16 v[128:131], a[56:59], a[120:123], v[128:131]\n"
"buffer_load_dwordx4 v0, s[48:51], s75 offen offset:2048 sc1 lds\n"
"v_mfma_f32_16x16x32_bf16 v[128:131], a[60:63], a[124:127], v[128:131]\n"
"v_mfma_f32_16x16x32_bf16 v[100:103], a[52:55], a[124:127], v[100:103]\n"
"v_mfma_f32_16x16x32_bf16 v[100:103], a[48:51], a[120:123], v[100:103]\n"
"v_mfma_f32_16x16x32_bf16 v[104:107], a[48:51], a[128:131], v[104:107]\n"
"v_mfma_f32_16x16x32_bf16 v[104:107], a[52:55], a[132:135], v[104:107]\n"
"global_load_dwordx4 a[40:43], v0, s[34:35] offset:2048 sc1\n"
"v_mfma_f32_16x16x32_bf16 v[132:135], a[60:63], a[132:135], v[132:135]\n"
"v_mfma_f32_16x16x32_bf16 v[132:135], a[56:59], a[128:131], v[132:135]\n"
"v_mfma_f32_16x16x32_bf16 v[136:139], a[56:59], a[136:139], v[136:139]\n"
"v_mfma_f32_16x16x32_bf16 v[136:139], a[60:63], a[140:143], v[136:139]\n"
"buffer_load_dwordx4 v0, s[48:51], s75 offen offset:3072 sc1 lds\n"
"v_mfma_f32_16x16x32_bf16 v[108:111], a[52:55], a[140:143], v[108:111]\n"
"v_mfma_f32_16x16x32_bf16 v[108:111], a[48:51], a[136:139], v[108:111]\n"
"v_mfma_f32_16x16x32_bf16 v[112:115], a[48:51], a[144:147], v[112:115]\n"
"v_mfma_f32_16x16x32_bf16 v[112:115], a[52:55], a[148:151], v[112:115]\n"
"global_load_dwordx4 a[44:47], v0, s[34:35] offset:3072 sc1\n"
"v_mfma_f32_16x16x32_bf16 v[140:143], a[60:63], a[148:151], v[140:143]\n"
"v_mfma_f32_16x16x32_bf16 v[140:143], a[56:59], a[144:147], v[140:143]\n"
"v_mfma_f32_16x16x32_bf16 a[12:15], a[56:59], a[152:155], a[12:15]\n"
"v_mfma_f32_16x16x32_bf16 a[12:15], a[60:63], a[156:159], a[12:15]\n"
"v_mfma_f32_16x16x32_bf16 a[8:11], a[52:55], a[156:159], a[8:11]\n"
"v_mfma_f32_16x16x32_bf16 a[8:11], a[48:51], a[152:155], a[8:11]\n"
"s_add_u32 m0, s64, 20480\n"
"s_waitcnt vmcnt(12)\n"
"v_mfma_f32_16x16x32_bf16 v[144:147], a[64:67], a[96:99], v[144:147]\n"
"s_add_u32 s34, s36, 1462272\n"
"v_mfma_f32_16x16x32_bf16 v[144:147], a[68:71], a[100:103], v[144:147]\n"
"global_load_dwordx4 a[48:51], v0, s[34:35] offset:0 sc1\n"
"v_mfma_f32_16x16x32_bf16 v[172:175], a[76:79], a[100:103], v[172:175]\n"
"v_mfma_f32_16x16x32_bf16 v[172:175], a[72:75], a[96:99], v[172:175]\n"
"v_mfma_f32_16x16x32_bf16 v[176:179], a[72:75], a[104:107], v[176:179]\n"
"v_mfma_f32_16x16x32_bf16 v[176:179], a[76:79], a[108:111], v[176:179]\n"
"v_mfma_f32_16x16x32_bf16 v[148:151], a[68:71], a[108:111], v[148:151]\n"
"v_lshlrev_b32 v16, 16, v18\n"
"v_and_b32 v17, s63, v18\n"
"v_mfma_f32_16x16x32_bf16 v[148:151], a[64:67], a[104:107], v[148:151]\n"
"v_lshlrev_b32 v18, 16, v19\n"
"v_and_b32 v19, s63, v19\n"
"v_mfma_f32_16x16x32_bf16 v[152:155], a[64:67], a[112:115], v[152:155]\n"
"buffer_load_dwordx4 v1, s[48:51], s75 offen offset:0 sc1 lds\n"
"v_mfma_f32_16x16x32_bf16 v[152:155], a[68:71], a[116:119], v[152:155]\n"
"global_load_dwordx4 a[52:55], v0, s[34:35] offset:1024 sc1\n"
"v_add_f32 v88, v88, v16\n"
"v_mfma_f32_16x16x32_bf16 v[180:183], a[76:79], a[116:119], v[180:183]\n"
"v_add_f32 v89, v89, v17\n"
"v_add_f32 v90, v90, v18\n"
"v_mfma_f32_16x16x32_bf16 v[180:183], a[72:75], a[112:115], v[180:183]\n"
"v_add_f32 v91, v91, v19\n"
"v_add_f32 v92, v92, v16\n"
"v_mfma_f32_16x16x32_bf16 v[184:187], a[72:75], a[120:123], v[184:187]\n"
"v_add_f32 v93, v93, v17\n"
"v_add_f32 v94, v94, v18\n"
"v_mfma_f32_16x16x32_bf16 v[184:187], a[76:79], a[124:127], v[184:187]\n"
"v_add_f32 v95, v95, v19\n"
"v_add_f32 v96, v96, v16\n"
"v_mfma_f32_16x16x32_bf16 v[156:159], a[68:71], a[124:127], v[156:159]\n"
"v_add_f32 v97, v97, v17\n"
"v_add_f32 v98, v98, v18\n"
"v_mfma_f32_16x16x32_bf16 v[156:159], a[64:67], a[120:123], v[156:159]\n"
"v_add_f32 v99, v99, v19\n"
"v_add_f32 v100, v100, v16\n"
"v_mfma_f32_16x16x32_bf16 v[160:163], a[64:67], a[128:131], v[160:163]\n"
"v_add_f32 v101, v101, v17\n"
"v_add_f32 v102, v102, v18\n"
"v_mfma_f32_16x16x32_bf16 v[160:163], a[68:71], a[132:135], v[160:163]\n"
"global_load_dwordx4 a[56:59], v0, s[34:35] offset:2048 sc1\n"
"v_add_f32 v103, v103, v19\n"
"v_mfma_f32_16x16x32_bf16 v[188:191], a[76:79], a[132:135], v[188:191]\n"
"buffer_load_dwordx4 v1, s[48:51], s75 offen offset:1024 sc1 lds\n"
"v_mfma_f32_16x16x32_bf16 v[188:191], a[72:75], a[128:131], v[188:191]\n"
"v_add_f32 v104, v104, v16\n"
"v_add_f32 v105, v105, v17\n"
"v_mfma_f32_16x16x32_bf16 v[192:195], a[72:75], a[136:139], v[192:195]\n"
"v_add_f32 v106, v106, v18\n"
"v_add_f32 v107, v107, v19\n"
"v_mfma_f32_16x16x32_bf16 v[192:195], a[76:79], a[140:143], v[192:195]\n"
"v_add_f32 v108, v108, v16\n"
"v_add_f32 v109, v109, v17\n"
"v_mfma_f32_16x16x32_bf16 v[164:167], a[68:71], a[140:143], v[164:167]\n"
"v_add_f32 v110, v110, v18\n"
"v_add_f32 v111, v111, v19\n"
"v_mfma_f32_16x16x32_bf16 v[164:167], a[64:67], a[136:139], v[164:167]\n"
"v_add_f32 v112, v112, v16\n"
"v_add_f32 v113, v113, v17\n"
"v_mfma_f32_16x16x32_bf16 v[168:171], a[64:67], a[144:147], v[168:171]\n"
"v_add_f32 v114, v114, v18\n"
"v_add_f32 v115, v115, v19\n"
"v_mfma_f32_16x16x32_bf16 v[168:171], a[68:71], a[148:151], v[168:171]\n"
"global_load_dwordx4 a[60:63], v0, s[34:35] offset:3072 sc1\n"
"v_accvgpr_read_b32 v12, a8\n"
"v_mfma_f32_16x16x32_bf16 v[196:199], a[76:79], a[148:151], v[196:199]\n"
"v_accvgpr_read_b32 v13, a9\n"
"v_accvgpr_read_b32 v14, a10\n"
"v_mfma_f32_16x16x32_bf16 v[196:199], a[72:75], a[144:147], v[196:199]\n"
"buffer_load_dwordx4 v1, s[48:51], s75 offen offset:2048 sc1 lds\n"
"v_mfma_f32_16x16x32_bf16 a[20:23], a[72:75], a[152:155], a[20:23]\n"
"v_accvgpr_read_b32 v15, a11\n"
"v_add_f32 v12, v12, v16\n"
"v_mfma_f32_16x16x32_bf16 a[20:23], a[76:79], a[156:159], a[20:23]\n"
"v_add_f32 v13, v13, v17\n"
"v_add_f32 v14, v14, v18\n"
"v_mfma_f32_16x16x32_bf16 a[16:19], a[68:71], a[156:159], a[16:19]\n"
"v_add_f32 v15, v15, v19\n"
"v_accvgpr_write_b32 a8, v12\n"
"v_mfma_f32_16x16x32_bf16 a[16:19], a[64:67], a[152:155], a[16:19]\n"
"s_waitcnt vmcnt(14)\n"
"v_accvgpr_write_b32 a9, v13\n"
"v_accvgpr_write_b32 a10, v14\n"
"v_accvgpr_write_b32 a11, v15\n"
"v_mfma_f32_16x16x32_bf16 v[200:203], a[80:83], a[96:99], v[200:203]\n"
"s_add_u32 s34, s36, 1474560\n"
"v_lshlrev_b32 v20, 16, v22\n"
"v_and_b32 v21, s63, v22\n"
"v_mfma_f32_16x16x32_bf16 v[200:203], a[84:87], a[100:103], v[200:203]\n"
"global_load_dwordx4 a[64:67], v0, s[34:35] offset:0 sc1\n"
"v_lshlrev_b32 v22, 16, v23\n"
"v_mfma_f32_16x16x32_bf16 v[228:231], a[92:95], a[100:103], v[228:231]\n"
"ds_read_b128 a[100:103], v3 offset:1024\n"
"v_and_b32 v23, s63, v23\n"
"v_mfma_f32_16x16x32_bf16 v[228:231], a[88:91], a[96:99], v[228:231]\n"
"ds_read_b128 a[96:99], v3 offset:0\n"
"v_add_f32 v116, v116, v20\n"
"v_mfma_f32_16x16x32_bf16 v[232:235], a[88:91], a[104:107], v[232:235]\n"
"v_add_f32 v117, v117, v21\n"
"v_add_f32 v118, v118, v22\n"
"v_mfma_f32_16x16x32_bf16 v[232:235], a[92:95], a[108:111], v[232:235]\n"
"buffer_load_dwordx4 v1, s[48:51], s75 offen offset:3072 sc1 lds\n"
"v_mfma_f32_16x16x32_bf16 v[204:207], a[84:87], a[108:111], v[204:207]\n"
"ds_read_b128 a[108:111], v3 offset:3072\n"
"v_add_f32 v119, v119, v23\n"
"v_add_f32 v120, v120, v20\n"
"v_mfma_f32_16x16x32_bf16 v[204:207], a[80:83], a[104:107], v[204:207]\n"
"ds_read_b128 a[104:107], v3 offset:2048\n"
"v_add_f32 v121, v121, v21\n"
"v_mfma_f32_16x16x32_bf16 v[208:211], a[80:83], a[112:115], v[208:211]\n"
"v_add_f32 v122, v122, v22\n"
"v_add_f32 v123, v123, v23\n"
"v_mfma_f32_16x16x32_bf16 v[208:211], a[84:87], a[116:119], v[208:211]\n"
"global_load_dwordx4 a[68:71], v0, s[34:35] offset:1024 sc1\n"
"v_add_f32 v124, v124, v20\n"
"v_add_f32 v125, v125, v21\n"
"v_mfma_f32_16x16x32_bf16 v[236:239], a[92:95], a[116:119], v[236:239]\n"
"ds_read_b128 a[116:119], v3 offset:5120\n"
"v_add_f32 v126, v126, v22\n"
"v_mfma_f32_16x16x32_bf16 v[236:239], a[88:91], a[112:115], v[236:239]\n"
"ds_read_b128 a[112:115], v3 offset:4096\n"
"v_add_f32 v127, v127, v23\n"
"v_mfma_f32_16x16x32_bf16 v[240:243], a[88:91], a[120:123], v[240:243]\n"
"v_add_f32 v128, v128, v20\n"
"v_add_f32 v129, v129, v21\n"
"v_mfma_f32_16x16x32_bf16 v[240:243], a[92:95], a[124:127], v[240:243]\n"
"v_add_f32 v130, v130, v22\n"
"v_add_f32 v131, v131, v23\n"
"v_mfma_f32_16x16x32_bf16 v[212:215], a[84:87], a[124:127], v[212:215]\n"
"s_add_u32 m0, s64, 24576\n"
"ds_read_b128 a[124:127], v3 offset:7168\n"
"v_add_f32 v132, v132, v20\n"
"v_add_f32 v133, v133, v21\n"
"v_mfma_f32_16x16x32_bf16 v[212:215], a[80:83], a[120:123], v[212:215]\n"
"ds_read_b128 a[120:123], v3 offset:6144\n"
"v_add_f32 v134, v134, v22\n"
"v_mfma_f32_16x16x32_bf16 v[216:219], a[80:83], a[128:131], v[216:219]\n"
"v_add_f32 v135, v135, v23\n"
"v_add_f32 v136, v136, v20\n"
"v_mfma_f32_16x16x32_bf16 v[216:219], a[84:87], a[132:135], v[216:219]\n"
"global_load_dwordx4 a[72:75], v0, s[34:35] offset:2048 sc1\n"
"v_add_f32 v137, v137, v21\n"
"v_mfma_f32_16x16x32_bf16 v[244:247], a[92:95], a[132:135], v[244:247]\n"
"ds_read_b128 a[132:135], v3 offset:9216\n"
"v_add_f32 v138, v138, v22\n"
"v_add_f32 v139, v139, v23\n"
"v_mfma_f32_16x16x32_bf16 v[244:247], a[88:91], a[128:131], v[244:247]\n"
"ds_read_b128 a[128:131], v3 offset:8192\n"
"v_add_f32 v140, v140, v20\n"
"v_mfma_f32_16x16x32_bf16 v[248:251], a[88:91], a[136:139], v[248:251]\n"
"v_add_f32 v141, v141, v21\n"
"v_add_f32 v142, v142, v22\n"
"v_mfma_f32_16x16x32_bf16 v[248:251], a[92:95], a[140:143], v[248:251]\n"
"v_add_f32 v143, v143, v23\n"
"v_accvgpr_read_b32 v12, a12\n"
"v_mfma_f32_16x16x32_bf16 v[220:223], a[84:87], a[140:143], v[220:223]\n"
"ds_read_b128 a[140:143], v3 offset:11264\n"
"v_accvgpr_read_b32 v13, a13\n"
"v_mfma_f32_16x16x32_bf16 v[220:223], a[80:83], a[136:139], v[220:223]\n"
"ds_read_b128 a[136:139], v3 offset:10240\n"
"v_accvgpr_read_b32 v14, a14\n"
"v_accvgpr_read_b32 v15, a15\n"
"v_mfma_f32_16x16x32_bf16 v[224:227], a[80:83], a[144:147], v[224:227]\n"
"buffer_load_dwordx4 v0, s[48:51], s69 offen offset:0 sc1 lds\n"
"v_add_f32 v12, v12, v20\n"
"v_mfma_f32_16x16x32_bf16 v[224:227], a[84:87], a[148:151], v[224:227]\n"
"global_load_dwordx4 a[76:79], v0, s[34:35] offset:3072 sc1\n"
"v_add_f32 v13, v13, v21\n"
"v_add_f32 v14, v14, v22\n"
"v_mfma_f32_16x16x32_bf16 v[252:255], a[92:95], a[148:151], v[252:255]\n"
"ds_read_b128 a[148:151], v3 offset:13312\n"
"v_add_f32 v15, v15, v23\n"
"v_accvgpr_write_b32 a12, v12\n"
"v_mfma_f32_16x16x32_bf16 v[252:255], a[88:91], a[144:147], v[252:255]\n"
"ds_read_b128 a[144:147], v3 offset:12288\n"
"v_accvgpr_write_b32 a13, v13\n"
"v_mfma_f32_16x16x32_bf16 a[28:31], a[88:91], a[152:155], a[28:31]\n"
"v_accvgpr_write_b32 a14, v14\n"
"v_accvgpr_write_b32 a15, v15\n"
"v_mfma_f32_16x16x32_bf16 a[28:31], a[92:95], a[156:159], a[28:31]\n"
"v_mfma_f32_16x16x32_bf16 a[24:27], a[84:87], a[156:159], a[24:27]\n"
"ds_read_b128 a[156:159], v3 offset:15360\n"
"v_mfma_f32_16x16x32_bf16 a[24:27], a[80:83], a[152:155], a[24:27]\n"
"s_waitcnt vmcnt(13) lgkmcnt(11)\n"
"ds_read_b128 a[152:155], v3 offset:14336\n"
"v_mfma_f32_16x16x32_bf16 v[32:35], a[32:35], a[96:99], v[32:35]\n"
"s_add_u32 s34, s36, 1478656\n"
"v_mfma_f32_16x16x32_bf16 v[32:35], a[36:39], a[100:103], v[32:35]\n"
"global_load_dwordx4 a[80:83], v0, s[34:35] offset:0 sc1\n"
"v_mfma_f32_16x16x32_bf16 v[60:63], a[44:47], a[100:103], v[60:63]\n"
"buffer_load_dwordx4 v0, s[48:51], s69 offen offset:1024 sc1 lds\n"
"v_mfma_f32_16x16x32_bf16 v[60:63], a[40:43], a[96:99], v[60:63]\n"
"v_lshrrev_b32 v16, 4, v0\n"
"v_mfma_f32_16x16x32_bf16 v[64:67], a[40:43], a[104:107], v[64:67]\n"
"v_mfma_f32_16x16x32_bf16 v[64:67], a[44:47], a[108:111], v[64:67]\n"
"v_mfma_f32_16x16x32_bf16 v[36:39], a[36:39], a[108:111], v[36:39]\n"
"v_mfma_f32_16x16x32_bf16 v[36:39], a[32:35], a[104:107], v[36:39]\n"
"s_waitcnt lgkmcnt(8)\n"
"v_and_b32 v16, 0x30, v16\n"
"v_mfma_f32_16x16x32_bf16 v[40:43], a[32:35], a[112:115], v[40:43]\n"
"v_mfma_f32_16x16x32_bf16 v[40:43], a[36:39], a[116:119], v[40:43]\n"
"global_load_dwordx4 a[84:87], v0, s[34:35] offset:1024 sc1\n"
"v_mfma_f32_16x16x32_bf16 v[68:71], a[44:47], a[116:119], v[68:71]\n"
"buffer_load_dwordx2 v[18:19], v16, s[44:47], s61 offen offset:128\n"
"v_mfma_f32_16x16x32_bf16 v[68:71], a[40:43], a[112:115], v[68:71]\n"
"buffer_load_dwordx4 v0, s[48:51], s69 offen offset:2048 sc1 lds\n"
"v_mfma_f32_16x16x32_bf16 v[72:75], a[40:43], a[120:123], v[72:75]\n"
"v_mfma_f32_16x16x32_bf16 v[72:75], a[44:47], a[124:127], v[72:75]\n"
"v_lshrrev_b32 v20, 4, v0\n"
"v_mfma_f32_16x16x32_bf16 v[44:47], a[36:39], a[124:127], v[44:47]\n"
"v_mfma_f32_16x16x32_bf16 v[44:47], a[32:35], a[120:123], v[44:47]\n"
"s_waitcnt lgkmcnt(4)\n"
"v_mfma_f32_16x16x32_bf16 v[48:51], a[32:35], a[128:131], v[48:51]\n"
"v_and_b32 v20, 0x30, v20\n"
"v_mfma_f32_16x16x32_bf16 v[48:51], a[36:39], a[132:135], v[48:51]\n"
"global_load_dwordx4 a[88:91], v0, s[34:35] offset:2048 sc1\n"
"v_mfma_f32_16x16x32_bf16 v[76:79], a[44:47], a[132:135], v[76:79]\n"
"v_mfma_f32_16x16x32_bf16 v[76:79], a[40:43], a[128:131], v[76:79]\n"
"v_mfma_f32_16x16x32_bf16 v[80:83], a[40:43], a[136:139], v[80:83]\n"
"buffer_load_dwordx4 v0, s[48:51], s69 offen offset:3072 sc1 lds\n"
"v_mfma_f32_16x16x32_bf16 v[80:83], a[44:47], a[140:143], v[80:83]\n"
"buffer_load_dwordx2 v[22:23], v20, s[44:47], s61 offen offset:136\n"
"v_mfma_f32_16x16x32_bf16 v[52:55], a[36:39], a[140:143], v[52:55]\n"
"v_mfma_f32_16x16x32_bf16 v[52:55], a[32:35], a[136:139], v[52:55]\n"
"s_waitcnt lgkmcnt(0)\n"
"v_mfma_f32_16x16x32_bf16 v[56:59], a[32:35], a[144:147], v[56:59]\n"
"v_lshrrev_b32 v24, 4, v0\n"
"v_mfma_f32_16x16x32_bf16 v[56:59], a[36:39], a[148:151], v[56:59]\n"
"global_load_dwordx4 a[92:95], v0, s[34:35] offset:3072 sc1\n"
"v_mfma_f32_16x16x32_bf16 v[84:87], a[44:47], a[148:151], v[84:87]\n"
"v_mfma_f32_16x16x32_bf16 v[84:87], a[40:43], a[144:147], v[84:87]\n"
"v_and_b32 v24, 0x30, v24\n"
"v_mfma_f32_16x16x32_bf16 a[4:7], a[40:43], a[152:155], a[4:7]\n"
"v_mfma_f32_16x16x32_bf16 a[4:7], a[44:47], a[156:159], a[4:7]\n"
"v_mfma_f32_16x16x32_bf16 a[0:3], a[36:39], a[156:159], a[0:3]\n"
"s_add_u32 m0, s64, 28672\n"
"v_mfma_f32_16x16x32_bf16 a[0:3], a[32:35], a[152:155], a[0:3]\n"
"s_waitcnt vmcnt(16)\n"
"buffer_load_dwordx2 v[26:27], v24, s[44:47], s61 offen offset:192\n"
"v_mfma_f32_16x16x32_bf16 v[88:91], a[48:51], a[96:99], v[88:91]\n"
"s_add_u32 s34, s36, 1490944\n"
"v_lshrrev_b32 v28, 4, v0\n"
"v_mfma_f32_16x16x32_bf16 v[88:91], a[52:55], a[100:103], v[88:91]\n"
"global_load_dwordx4 a[32:35], v0, s[34:35] offset:0 sc1\n"
"v_mfma_f32_16x16x32_bf16 v[116:119], a[60:63], a[100:103], v[116:119]\n"
"ds_read_b128 a[100:103], v4 offset:1024\n"
"v_mfma_f32_16x16x32_bf16 v[116:119], a[56:59], a[96:99], v[116:119]\n"
"ds_read_b128 a[96:99], v4 offset:0\n"
"v_mfma_f32_16x16x32_bf16 v[120:123], a[56:59], a[104:107], v[120:123]\n"
"v_and_b32 v28, 0x30, v28\n"
"v_mfma_f32_16x16x32_bf16 v[120:123], a[60:63], a[108:111], v[120:123]\n"
"buffer_load_dwordx2 v[30:31], v28, s[44:47], s61 offen offset:200\n"
"v_mfma_f32_16x16x32_bf16 v[92:95], a[52:55], a[108:111], v[92:95]\n"
"ds_read_b128 a[108:111], v4 offset:3072\n"
"v_mfma_f32_16x16x32_bf16 v[92:95], a[48:51], a[104:107], v[92:95]\n"
"ds_read_b128 a[104:107], v4 offset:2048\n"
"v_mfma_f32_16x16x32_bf16 v[96:99], a[48:51], a[112:115], v[96:99]\n"
"buffer_load_dwordx4 v1, s[48:51], s69 offen offset:0 sc1 lds\n"
"v_mfma_f32_16x16x32_bf16 v[96:99], a[52:55], a[116:119], v[96:99]\n"
"global_load_dwordx4 a[36:39], v0, s[34:35] offset:1024 sc1\n"
"v_mfma_f32_16x16x32_bf16 v[124:127], a[60:63], a[116:119], v[124:127]\n"
"ds_read_b128 a[116:119], v4 offset:5120\n"
"v_mfma_f32_16x16x32_bf16 v[124:127], a[56:59], a[112:115], v[124:127]\n"
"ds_read_b128 a[112:115], v4 offset:4096\n"
"v_mfma_f32_16x16x32_bf16 v[128:131], a[56:59], a[120:123], v[128:131]\n"
"v_mfma_f32_16x16x32_bf16 v[128:131], a[60:63], a[124:127], v[128:131]\n"
"v_mfma_f32_16x16x32_bf16 v[100:103], a[52:55], a[124:127], v[100:103]\n"
"ds_read_b128 a[124:127], v4 offset:7168\n"
"v_mfma_f32_16x16x32_bf16 v[100:103], a[48:51], a[120:123], v[100:103]\n"
"ds_read_b128 a[120:123], v4 offset:6144\n"
"v_mfma_f32_16x16x32_bf16 v[104:107], a[48:51], a[128:131], v[104:107]\n"
"buffer_load_dwordx4 v1, s[48:51], s69 offen offset:1024 sc1 lds\n"
"v_mfma_f32_16x16x32_bf16 v[104:107], a[52:55], a[132:135], v[104:107]\n"
"global_load_dwordx4 a[40:43], v0, s[34:35] offset:2048 sc1\n"
"v_mfma_f32_16x16x32_bf16 v[132:135], a[60:63], a[132:135], v[132:135]\n"
"ds_read_b128 a[132:135], v4 offset:9216\n"
"v_mfma_f32_16x16x32_bf16 v[132:135], a[56:59], a[128:131], v[132:135]\n"
"ds_read_b128 a[128:131], v4 offset:8192\n"
"v_mfma_f32_16x16x32_bf16 v[136:139], a[56:59], a[136:139], v[136:139]\n"
"v_mfma_f32_16x16x32_bf16 v[136:139], a[60:63], a[140:143], v[136:139]\n"
"v_mfma_f32_16x16x32_bf16 v[108:111], a[52:55], a[140:143], v[108:111]\n"
"ds_read_b128 a[140:143], v4 offset:11264\n"
"v_mfma_f32_16x16x32_bf16 v[108:111], a[48:51], a[136:139], v[108:111]\n"
"ds_read_b128 a[136:139], v4 offset:10240\n"
"v_mfma_f32_16x16x32_bf16 v[112:115], a[48:51], a[144:147], v[112:115]\n"
"v_mfma_f32_16x16x32_bf16 v[112:115], a[52:55], a[148:151], v[112:115]\n"
"global_load_dwordx4 a[44:47], v0, s[34:35] offset:3072 sc1\n"
"v_mfma_f32_16x16x32_bf16 v[140:143], a[60:63], a[148:151], v[140:143]\n"
"ds_read_b128 a[148:151], v4 offset:13312\n"
"v_mfma_f32_16x16x32_bf16 v[140:143], a[56:59], a[144:147], v[140:143]\n"
"ds_read_b128 a[144:147], v4 offset:12288\n"
"v_mfma_f32_16x16x32_bf16 a[12:15], a[56:59], a[152:155], a[12:15]\n"
"buffer_load_dwordx4 v1, s[48:51], s69 offen offset:2048 sc1 lds\n"
"v_mfma_f32_16x16x32_bf16 a[12:15], a[60:63], a[156:159], a[12:15]\n"
"v_mfma_f32_16x16x32_bf16 a[8:11], a[52:55], a[156:159], a[8:11]\n"
"ds_read_b128 a[156:159], v4 offset:15360\n"
"v_mfma_f32_16x16x32_bf16 a[8:11], a[48:51], a[152:155], a[8:11]\n"
"s_waitcnt vmcnt(18) lgkmcnt(11)\n"
"ds_read_b128 a[152:155], v4 offset:14336\n"
"v_mfma_f32_16x16x32_bf16 v[32:35], a[64:67], a[96:99], v[32:35]\n"
"s_add_u32 s34, s36, 1495040\n"
"v_mfma_f32_16x16x32_bf16 v[32:35], a[68:71], a[100:103], v[32:35]\n"
"global_load_dwordx4 a[48:51], v0, s[34:35] offset:0 sc1\n"
"v_mfma_f32_16x16x32_bf16 v[60:63], a[76:79], a[100:103], v[60:63]\n"
"v_mfma_f32_16x16x32_bf16 v[60:63], a[72:75], a[96:99], v[60:63]\n"
"v_mfma_f32_16x16x32_bf16 v[64:67], a[72:75], a[104:107], v[64:67]\n"
"buffer_load_dwordx4 v1, s[48:51], s69 offen offset:3072 sc1 lds\n"
"v_mfma_f32_16x16x32_bf16 v[64:67], a[76:79], a[108:111], v[64:67]\n"
"v_mfma_f32_16x16x32_bf16 v[36:39], a[68:71], a[108:111], v[36:39]\n"
"v_mfma_f32_16x16x32_bf16 v[36:39], a[64:67], a[104:107], v[36:39]\n"
"s_waitcnt lgkmcnt(8)\n"
"v_mfma_f32_16x16x32_bf16 v[40:43], a[64:67], a[112:115], v[40:43]\n"
"v_mfma_f32_16x16x32_bf16 v[40:43], a[68:71], a[116:119], v[40:43]\n"
"global_load_dwordx4 a[52:55], v0, s[34:35] offset:1024 sc1\n"
"v_mfma_f32_16x16x32_bf16 v[68:71], a[76:79], a[116:119], v[68:71]\n"
"v_mfma_f32_16x16x32_bf16 v[68:71], a[72:75], a[112:115], v[68:71]\n"
"v_mfma_f32_16x16x32_bf16 v[72:75], a[72:75], a[120:123], v[72:75]\n"
"v_mfma_f32_16x16x32_bf16 v[72:75], a[76:79], a[124:127], v[72:75]\n"
"v_mfma_f32_16x16x32_bf16 v[44:47], a[68:71], a[124:127], v[44:47]\n"
"v_mfma_f32_16x16x32_bf16 v[44:47], a[64:67], a[120:123], v[44:47]\n"
"s_waitcnt lgkmcnt(4)\n"
"v_mfma_f32_16x16x32_bf16 v[48:51], a[64:67], a[128:131], v[48:51]\n"
"v_mfma_f32_16x16x32_bf16 v[48:51], a[68:71], a[132:135], v[48:51]\n"
"global_load_dwordx4 a[56:59], v0, s[34:35] offset:2048 sc1\n"
"v_mfma_f32_16x16x32_bf16 v[76:79], a[76:79], a[132:135], v[76:79]\n"
"v_mfma_f32_16x16x32_bf16 v[76:79], a[72:75], a[128:131], v[76:79]\n"
"v_mfma_f32_16x16x32_bf16 v[80:83], a[72:75], a[136:139], v[80:83]\n"
"v_mfma_f32_16x16x32_bf16 v[80:83], a[76:79], a[140:143], v[80:83]\n"
"v_mfma_f32_16x16x32_bf16 v[52:55], a[68:71], a[140:143], v[52:55]\n"
"v_mfma_f32_16x16x32_bf16 v[52:55], a[64:67], a[136:139], v[52:55]\n"
"s_waitcnt lgkmcnt(0)\n"
"v_mfma_f32_16x16x32_bf16 v[56:59], a[64:67], a[144:147], v[56:59]\n"
"v_mfma_f32_16x16x32_bf16 v[56:59], a[68:71], a[148:151], v[56:59]\n"
"global_load_dwordx4 a[60:63], v0, s[34:35] offset:3072 sc1\n"
"v_mfma_f32_16x16x32_bf16 v[84:87], a[76:79], a[148:151], v[84:87]\n"
"v_mfma_f32_16x16x32_bf16 v[84:87], a[72:75], a[144:147], v[84:87]\n"
"v_mfma_f32_16x16x32_bf16 a[4:7], a[72:75], a[152:155], a[4:7]\n"
"v_mfma_f32_16x16x32_bf16 a[4:7], a[76:79], a[156:159], a[4:7]\n"
"v_mfma_f32_16x16x32_bf16 a[0:3], a[68:71], a[156:159], a[0:3]\n"
"v_mfma_f32_16x16x32_bf16 a[0:3], a[64:67], a[152:155], a[0:3]\n"
"s_waitcnt vmcnt(14)\n"
"v_mfma_f32_16x16x32_bf16 v[88:91], a[80:83], a[96:99], v[88:91]\n"
"s_add_u32 s34, s36, 1507328\n"
"v_mfma_f32_16x16x32_bf16 v[88:91], a[84:87], a[100:103], v[88:91]\n"
"global_load_dwordx4 a[64:67], v0, s[34:35] offset:0 sc1\n"
"v_mfma_f32_16x16x32_bf16 v[116:119], a[92:95], a[100:103], v[116:119]\n"
"ds_read_b128 a[100:103], v5 offset:1024\n"
"v_mfma_f32_16x16x32_bf16 v[116:119], a[88:91], a[96:99], v[116:119]\n"
"ds_read_b128 a[96:99], v5 offset:0\n"
"v_mfma_f32_16x16x32_bf16 v[120:123], a[88:91], a[104:107], v[120:123]\n"
"v_mfma_f32_16x16x32_bf16 v[120:123], a[92:95], a[108:111], v[120:123]\n"
"v_mfma_f32_16x16x32_bf16 v[92:95], a[84:87], a[108:111], v[92:95]\n"
"ds_read_b128 a[108:111], v5 offset:3072\n"
"v_mfma_f32_16x16x32_bf16 v[92:95], a[80:83], a[104:107], v[92:95]\n"
"ds_read_b128 a[104:107], v5 offset:2048\n"
"v_mfma_f32_16x16x32_bf16 v[96:99], a[80:83], a[112:115], v[96:99]\n"
"v_mfma_f32_16x16x32_bf16 v[96:99], a[84:87], a[116:119], v[96:99]\n"
"global_load_dwordx4 a[68:71], v0, s[34:35] offset:1024 sc1\n"
"v_mfma_f32_16x16x32_bf16 v[124:127], a[92:95], a[116:119], v[124:127]\n"
"ds_read_b128 a[116:119], v5 offset:5120\n"
"v_mfma_f32_16x16x32_bf16 v[124:127], a[88:91], a[112:115], v[124:127]\n"
"ds_read_b128 a[112:115], v5 offset:4096\n"
"v_mfma_f32_16x16x32_bf16 v[128:131], a[88:91], a[120:123], v[128:131]\n"
"v_mfma_f32_16x16x32_bf16 v[128:131], a[92:95], a[124:127], v[128:131]\n"
"v_mfma_f32_16x16x32_bf16 v[100:103], a[84:87], a[124:127], v[100:103]\n"
"ds_read_b128 a[124:127], v5 offset:7168\n"
"v_mfma_f32_16x16x32_bf16 v[100:103], a[80:83], a[120:123], v[100:103]\n"
"ds_read_b128 a[120:123], v5 offset:6144\n"
"v_mfma_f32_16x16x32_bf16 v[104:107], a[80:83], a[128:131], v[104:107]\n"
"v_mfma_f32_16x16x32_bf16 v[104:107], a[84:87], a[132:135], v[104:107]\n"
"global_load_dwordx4 a[72:75], v0, s[34:35] offset:2048 sc1\n"
"v_mfma_f32_16x16x32_bf16 v[132:135], a[92:95], a[132:135], v[132:135]\n"
"ds_read_b128 a[132:135], v5 offset:9216\n"
"v_mfma_f32_16x16x32_bf16 v[132:135], a[88:91], a[128:131], v[132:135]\n"
"ds_read_b128 a[128:131], v5 offset:8192\n"
"v_mfma_f32_16x16x32_bf16 v[136:139], a[88:91], a[136:139], v[136:139]\n"
"v_mfma_f32_16x16x32_bf16 v[136:139], a[92:95], a[140:143], v[136:139]\n"
"v_mfma_f32_16x16x32_bf16 v[108:111], a[84:87], a[140:143], v[108:111]\n"
"ds_read_b128 a[140:143], v5 offset:11264\n"
"v_mfma_f32_16x16x32_bf16 v[108:111], a[80:83], a[136:139], v[108:111]\n"
"ds_read_b128 a[136:139], v5 offset:10240\n"
"v_mfma_f32_16x16x32_bf16 v[112:115], a[80:83], a[144:147], v[112:115]\n"
"v_mfma_f32_16x16x32_bf16 v[112:115], a[84:87], a[148:151], v[112:115]\n"
"global_load_dwordx4 a[76:79], v0, s[34:35] offset:3072 sc1\n"
"v_mfma_f32_16x16x32_bf16 v[140:143], a[92:95], a[148:151], v[140:143]\n"
"ds_read_b128 a[148:151], v5 offset:13312\n"
"v_mfma_f32_16x16x32_bf16 v[140:143], a[88:91], a[144:147], v[140:143]\n"
"ds_read_b128 a[144:147], v5 offset:12288\n"
"v_mfma_f32_16x16x32_bf16 a[12:15], a[88:91], a[152:155], a[12:15]\n"
"v_mfma_f32_16x16x32_bf16 a[12:15], a[92:95], a[156:159], a[12:15]\n"
"v_mfma_f32_16x16x32_bf16 a[8:11], a[84:87], a[156:159], a[8:11]\n"
"ds_read_b128 a[156:159], v5 offset:15360\n"
"v_mfma_f32_16x16x32_bf16 a[8:11], a[80:83], a[152:155], a[8:11]\n"
"s_waitcnt vmcnt(10) lgkmcnt(11)\n"
"ds_read_b128 a[152:155], v5 offset:14336\n"
"v_mfma_f32_16x16x32_bf16 v[32:35], a[32:35], a[96:99], v[32:35]\n"
"s_add_u32 s34, s36, 1511424\n"
"v_mfma_f32_16x16x32_bf16 v[32:35], a[36:39], a[100:103], v[32:35]\n"
"global_load_dwordx4 a[80:83], v0, s[34:35] offset:0 sc1\n"
"v_mfma_f32_16x16x32_bf16 v[60:63], a[44:47], a[100:103], v[60:63]\n"
"v_mfma_f32_16x16x32_bf16 v[60:63], a[40:43], a[96:99], v[60:63]\n"
"v_mfma_f32_16x16x32_bf16 v[64:67], a[40:43], a[104:107], v[64:67]\n"
"v_mfma_f32_16x16x32_bf16 v[64:67], a[44:47], a[108:111], v[64:67]\n"
"v_mfma_f32_16x16x32_bf16 v[36:39], a[36:39], a[108:111], v[36:39]\n"
"v_mfma_f32_16x16x32_bf16 v[36:39], a[32:35], a[104:107], v[36:39]\n"
"s_waitcnt lgkmcnt(8)\n"
"v_mfma_f32_16x16x32_bf16 v[40:43], a[32:35], a[112:115], v[40:43]\n"
"v_mfma_f32_16x16x32_bf16 v[40:43], a[36:39], a[116:119], v[40:43]\n"
"global_load_dwordx4 a[84:87], v0, s[34:35] offset:1024 sc1\n"
"v_mfma_f32_16x16x32_bf16 v[68:71], a[44:47], a[116:119], v[68:71]\n"
"v_mfma_f32_16x16x32_bf16 v[68:71], a[40:43], a[112:115], v[68:71]\n"
"v_mfma_f32_16x16x32_bf16 v[72:75], a[40:43], a[120:123], v[72:75]\n"
"v_mfma_f32_16x16x32_bf16 v[72:75], a[44:47], a[124:127], v[72:75]\n"
"v_mfma_f32_16x16x32_bf16 v[44:47], a[36:39], a[124:127], v[44:47]\n"
"v_mfma_f32_16x16x32_bf16 v[44:47], a[32:35], a[120:123], v[44:47]\n"
"s_waitcnt lgkmcnt(4)\n"
"v_mfma_f32_16x16x32_bf16 v[48:51], a[32:35], a[128:131], v[48:51]\n"
"v_mfma_f32_16x16x32_bf16 v[48:51], a[36:39], a[132:135], v[48:51]\n"
"global_load_dwordx4 a[88:91], v0, s[34:35] offset:2048 sc1\n"
"v_mfma_f32_16x16x32_bf16 v[76:79], a[44:47], a[132:135], v[76:79]\n"
"v_mfma_f32_16x16x32_bf16 v[76:79], a[40:43], a[128:131], v[76:79]\n"
"v_mfma_f32_16x16x32_bf16 v[80:83], a[40:43], a[136:139], v[80:83]\n"
"v_mfma_f32_16x16x32_bf16 v[80:83], a[44:47], a[140:143], v[80:83]\n"
"v_mfma_f32_16x16x32_bf16 v[52:55], a[36:39], a[140:143], v[52:55]\n"
"v_mfma_f32_16x16x32_bf16 v[52:55], a[32:35], a[136:139], v[52:55]\n"
"s_waitcnt lgkmcnt(0)\n"
"v_mfma_f32_16x16x32_bf16 v[56:59], a[32:35], a[144:147], v[56:59]\n"
"v_mfma_f32_16x16x32_bf16 v[56:59], a[36:39], a[148:151], v[56:59]\n"
"global_load_dwordx4 a[92:95], v0, s[34:35] offset:3072 sc1\n"
"v_mfma_f32_16x16x32_bf16 v[84:87], a[44:47], a[148:151], v[84:87]\n"
"v_mfma_f32_16x16x32_bf16 v[84:87], a[40:43], a[144:147], v[84:87]\n"
"v_mfma_f32_16x16x32_bf16 a[4:7], a[40:43], a[152:155], a[4:7]\n"
"v_mfma_f32_16x16x32_bf16 a[4:7], a[44:47], a[156:159], a[4:7]\n"
"v_mfma_f32_16x16x32_bf16 a[0:3], a[36:39], a[156:159], a[0:3]\n"
"v_mfma_f32_16x16x32_bf16 a[0:3], a[32:35], a[152:155], a[0:3]\n"
"s_waitcnt vmcnt(8)\n"
"v_mfma_f32_16x16x32_bf16 v[88:91], a[48:51], a[96:99], v[88:91]\n"
"s_add_u32 s34, s36, 1515520\n"
"v_mfma_f32_16x16x32_bf16 v[88:91], a[52:55], a[100:103], v[88:91]\n"
"global_load_dwordx4 a[32:35], v0, s[34:35] offset:0 sc1\n"
"v_mfma_f32_16x16x32_bf16 v[116:119], a[60:63], a[100:103], v[116:119]\n"
"ds_read_b128 a[100:103], v2 offset:17408\n"
"v_mfma_f32_16x16x32_bf16 v[116:119], a[56:59], a[96:99], v[116:119]\n"
"ds_read_b128 a[96:99], v2 offset:16384\n"
"v_mfma_f32_16x16x32_bf16 v[120:123], a[56:59], a[104:107], v[120:123]\n"
"v_mfma_f32_16x16x32_bf16 v[120:123], a[60:63], a[108:111], v[120:123]\n"
"v_mfma_f32_16x16x32_bf16 v[92:95], a[52:55], a[108:111], v[92:95]\n"
"ds_read_b128 a[108:111], v2 offset:19456\n"
"v_mfma_f32_16x16x32_bf16 v[92:95], a[48:51], a[104:107], v[92:95]\n"
"ds_read_b128 a[104:107], v2 offset:18432\n"
"v_mfma_f32_16x16x32_bf16 v[96:99], a[48:51], a[112:115], v[96:99]\n"
"v_mfma_f32_16x16x32_bf16 v[96:99], a[52:55], a[116:119], v[96:99]\n"
"global_load_dwordx4 a[36:39], v0, s[34:35] offset:1024 sc1\n"
"v_mfma_f32_16x16x32_bf16 v[124:127], a[60:63], a[116:119], v[124:127]\n"
"ds_read_b128 a[116:119], v2 offset:21504\n"
"v_mfma_f32_16x16x32_bf16 v[124:127], a[56:59], a[112:115], v[124:127]\n"
"ds_read_b128 a[112:115], v2 offset:20480\n"
"v_mfma_f32_16x16x32_bf16 v[128:131], a[56:59], a[120:123], v[128:131]\n"
"v_mfma_f32_16x16x32_bf16 v[128:131], a[60:63], a[124:127], v[128:131]\n"
"v_mfma_f32_16x16x32_bf16 v[100:103], a[52:55], a[124:127], v[100:103]\n"
"ds_read_b128 a[124:127], v2 offset:23552\n"
"v_mfma_f32_16x16x32_bf16 v[100:103], a[48:51], a[120:123], v[100:103]\n"
"ds_read_b128 a[120:123], v2 offset:22528\n"
"v_mfma_f32_16x16x32_bf16 v[104:107], a[48:51], a[128:131], v[104:107]\n"
"v_mfma_f32_16x16x32_bf16 v[104:107], a[52:55], a[132:135], v[104:107]\n"
"global_load_dwordx4 a[40:43], v0, s[34:35] offset:2048 sc1\n"
"v_mfma_f32_16x16x32_bf16 v[132:135], a[60:63], a[132:135], v[132:135]\n"
"ds_read_b128 a[132:135], v2 offset:25600\n"
"v_mfma_f32_16x16x32_bf16 v[132:135], a[56:59], a[128:131], v[132:135]\n"
"ds_read_b128 a[128:131], v2 offset:24576\n"
"v_mfma_f32_16x16x32_bf16 v[136:139], a[56:59], a[136:139], v[136:139]\n"
"v_mfma_f32_16x16x32_bf16 v[136:139], a[60:63], a[140:143], v[136:139]\n"
"v_mfma_f32_16x16x32_bf16 v[108:111], a[52:55], a[140:143], v[108:111]\n"
"ds_read_b128 a[140:143], v2 offset:27648\n"
"v_mfma_f32_16x16x32_bf16 v[108:111], a[48:51], a[136:139], v[108:111]\n"
"ds_read_b128 a[136:139], v2 offset:26624\n"
"v_mfma_f32_16x16x32_bf16 v[112:115], a[48:51], a[144:147], v[112:115]\n"
"v_mfma_f32_16x16x32_bf16 v[112:115], a[52:55], a[148:151], v[112:115]\n"
"global_load_dwordx4 a[44:47], v0, s[34:35] offset:3072 sc1\n"
"v_mfma_f32_16x16x32_bf16 v[140:143], a[60:63], a[148:151], v[140:143]\n"
"ds_read_b128 a[148:151], v2 offset:29696\n"
"v_mfma_f32_16x16x32_bf16 v[140:143], a[56:59], a[144:147], v[140:143]\n"
"ds_read_b128 a[144:147], v2 offset:28672\n"
"v_mfma_f32_16x16x32_bf16 a[12:15], a[56:59], a[152:155], a[12:15]\n"
"v_mfma_f32_16x16x32_bf16 a[12:15], a[60:63], a[156:159], a[12:15]\n"
"v_mfma_f32_16x16x32_bf16 a[8:11], a[52:55], a[156:159], a[8:11]\n"
"ds_read_b128 a[156:159], v2 offset:31744\n"
"v_mfma_f32_16x16x32_bf16 a[8:11], a[48:51], a[152:155], a[8:11]\n"
"s_waitcnt vmcnt(8) lgkmcnt(11)\n"
"ds_read_b128 a[152:155], v2 offset:30720\n"
"v_mfma_f32_16x16x32_bf16 v[32:35], a[64:67], a[96:99], v[32:35]\n"
"L_st9650:\n"
"s_cbranch_execz L_retC\n"
"s_add_u32 s34, s36, 1519616\n"
"v_mfma_f32_16x16x32_bf16 v[32:35], a[68:71], a[100:103], v[32:35]\n"
"global_load_dwordx4 a[48:51], v0, s[34:35] offset:0 sc1\n"
"v_mfma_f32_16x16x32_bf16 v[60:63], a[76:79], a[100:103], v[60:63]\n"
"s_barrier\n"
"v_mfma_f32_16x16x32_bf16 v[60:63], a[72:75], a[96:99], v[60:63]\n"
"v_mfma_f32_16x16x32_bf16 v[64:67], a[72:75], a[104:107], v[64:67]\n"
"v_mfma_f32_16x16x32_bf16 v[64:67], a[76:79], a[108:111], v[64:67]\n"
"v_mfma_f32_16x16x32_bf16 v[36:39], a[68:71], a[108:111], v[36:39]\n"
"v_mfma_f32_16x16x32_bf16 v[36:39], a[64:67], a[104:107], v[36:39]\n"
"s_waitcnt lgkmcnt(8)\n"
"v_mfma_f32_16x16x32_bf16 v[40:43], a[64:67], a[112:115], v[40:43]\n"
"v_mfma_f32_16x16x32_bf16 v[40:43], a[68:71], a[116:119], v[40:43]\n"
"global_load_dwordx4 a[52:55], v0, s[34:35] offset:1024 sc1\n"
"v_mfma_f32_16x16x32_bf16 v[68:71], a[76:79], a[116:119], v[68:71]\n"
"v_mfma_f32_16x16x32_bf16 v[68:71], a[72:75], a[112:115], v[68:71]\n"
"v_mfma_f32_16x16x32_bf16 v[72:75], a[72:75], a[120:123], v[72:75]\n"
"v_mfma_f32_16x16x32_bf16 v[72:75], a[76:79], a[124:127], v[72:75]\n"
"v_mfma_f32_16x16x32_bf16 v[44:47], a[68:71], a[124:127], v[44:47]\n"
"v_mfma_f32_16x16x32_bf16 v[44:47], a[64:67], a[120:123], v[44:47]\n"
"s_waitcnt lgkmcnt(4)\n"
"v_mfma_f32_16x16x32_bf16 v[48:51], a[64:67], a[128:131], v[48:51]\n"
"v_mfma_f32_16x16x32_bf16 v[48:51], a[68:71], a[132:135], v[48:51]\n"
"global_load_dwordx4 a[56:59], v0, s[34:35] offset:2048 sc1\n"
"v_mfma_f32_16x16x32_bf16 v[76:79], a[76:79], a[132:135], v[76:79]\n"
"v_mfma_f32_16x16x32_bf16 v[76:79], a[72:75], a[128:131], v[76:79]\n"
"v_mfma_f32_16x16x32_bf16 v[80:83], a[72:75], a[136:139], v[80:83]\n"
"v_lshlrev_b32 v16, 16, v18\n"
"v_mfma_f32_16x16x32_bf16 v[80:83], a[76:79], a[140:143], v[80:83]\n"
"v_and_b32 v17, s63, v18\n"
"v_mfma_f32_16x16x32_bf16 v[52:55], a[68:71], a[140:143], v[52:55]\n"
"v_lshlrev_b32 v18, 16, v19\n"
"v_mfma_f32_16x16x32_bf16 v[52:55], a[64:67], a[136:139], v[52:55]\n"
"s_waitcnt lgkmcnt(0)\n"
"v_and_b32 v19, s63, v19\n"
"v_add_f32 v144, v144, v16\n"
"v_mfma_f32_16x16x32_bf16 v[56:59], a[64:67], a[144:147], v[56:59]\n"
"v_add_f32 v145, v145, v17\n"
"v_mfma_f32_16x16x32_bf16 v[56:59], a[68:71], a[148:151], v[56:59]\n"
"global_load_dwordx4 a[60:63], v0, s[34:35] offset:3072 sc1\n"
"v_add_f32 v146, v146, v18\n"
"v_mfma_f32_16x16x32_bf16 v[84:87], a[76:79], a[148:151], v[84:87]\n"
"v_add_f32 v147, v147, v19\n"
"v_mfma_f32_16x16x32_bf16 v[84:87], a[72:75], a[144:147], v[84:87]\n"
"v_add_f32 v148, v148, v16\n"
"v_mfma_f32_16x16x32_bf16 a[4:7], a[72:75], a[152:155], a[4:7]\n"
"v_add_f32 v149, v149, v17\n"
"v_mfma_f32_16x16x32_bf16 a[4:7], a[76:79], a[156:159], a[4:7]\n"
"v_add_f32 v150, v150, v18\n"
"v_mfma_f32_16x16x32_bf16 a[0:3], a[68:71], a[156:159], a[0:3]\n"
"v_add_f32 v151, v151, v19\n"
"v_mfma_f32_16x16x32_bf16 a[0:3], a[64:67], a[152:155], a[0:3]\n"
"s_waitcnt vmcnt(8)\n"
"v_add_f32 v152, v152, v16\n"
"v_add_f32 v153, v153, v17\n"
"v_mfma_f32_16x16x32_bf16 v[88:91], a[80:83], a[96:99], v[88:91]\n"
"s_add_u32 s34, s36, 1523712\n"
"v_add_f32 v154, v154, v18\n"
"v_mfma_f32_16x16x32_bf16 v[88:91], a[84:87], a[100:103], v[88:91]\n"
"global_load_dwordx4 a[64:67], v0, s[34:35] offset:0 sc1\n"
"v_add_f32 v155, v155, v19\n"
"v_mfma_f32_16x16x32_bf16 v[116:119], a[92:95], a[100:103], v[116:119]\n"
"ds_read_b128 a[100:103], v3 offset:17408\n"
"v_add_f32 v156, v156, v16\n"
"v_mfma_f32_16x16x32_bf16 v[116:119], a[88:91], a[96:99], v[116:119]\n"
"ds_read_b128 a[96:99], v3 offset:16384\n"
"v_add_f32 v157, v157, v17\n"
"v_mfma_f32_16x16x32_bf16 v[120:123], a[88:91], a[104:107], v[120:123]\n"
"v_add_f32 v158, v158, v18\n"
"v_mfma_f32_16x16x32_bf16 v[120:123], a[92:95], a[108:111], v[120:123]\n"
"v_add_f32 v159, v159, v19\n"
"v_add_f32 v160, v160, v16\n"
"v_mfma_f32_16x16x32_bf16 v[92:95], a[84:87], a[108:111], v[92:95]\n"
"ds_read_b128 a[108:111], v3 offset:19456\n"
"v_add_f32 v161, v161, v17\n"
"v_mfma_f32_16x16x32_bf16 v[92:95], a[80:83], a[104:107], v[92:95]\n"
"ds_read_b128 a[104:107], v3 offset:18432\n"
"v_add_f32 v162, v162, v18\n"
"v_mfma_f32_16x16x32_bf16 v[96:99], a[80:83], a[112:115], v[96:99]\n"
"v_add_f32 v163, v163, v19\n"
"v_mfma_f32_16x16x32_bf16 v[96:99], a[84:87], a[116:119], v[96:99]\n"
"global_load_dwordx4 a[68:71], v0, s[34:35] offset:1024 sc1\n"
"v_add_f32 v164, v164, v16\n"
"v_mfma_f32_16x16x32_bf16 v[124:127], a[92:95], a[116:119], v[124:127]\n"
"ds_read_b128 a[116:119], v3 offset:21504\n"
"v_add_f32 v165, v165, v17\n"
"v_mfma_f32_16x16x32_bf16 v[124:127], a[88:91], a[112:115], v[124:127]\n"
"ds_read_b128 a[112:115], v3 offset:20480\n"
"v_add_f32 v166, v166, v18\n"
"v_mfma_f32_16x16x32_bf16 v[128:131], a[88:91], a[120:123], v[128:131]\n"
"v_add_f32 v167, v167, v19\n"
"v_mfma_f32_16x16x32_bf16 v[128:131], a[92:95], a[124:127], v[128:131]\n"
"v_add_f32 v168, v168, v16\n"
"v_add_f32 v169, v169, v17\n"
"v_mfma_f32_16x16x32_bf16 v[100:103], a[84:87], a[124:127], v[100:103]\n"
"ds_read_b128 a[124:127], v3 offset:23552\n"
"v_add_f32 v170, v170, v18\n"
"v_mfma_f32_16x16x32_bf16 v[100:103], a[80:83], a[120:123], v[100:103]\n"
"ds_read_b128 a[120:123], v3 offset:22528\n"
"v_add_f32 v171, v171, v19\n"
"v_mfma_f32_16x16x32_bf16 v[104:107], a[80:83], a[128:131], v[104:107]\n"
"v_accvgpr_read_b32 v12, a16\n"
"v_mfma_f32_16x16x32_bf16 v[104:107], a[84:87], a[132:135], v[104:107]\n"
"global_load_dwordx4 a[72:75], v0, s[34:35] offset:2048 sc1\n"
"v_accvgpr_read_b32 v13, a17\n"
"v_mfma_f32_16x16x32_bf16 v[132:135], a[92:95], a[132:135], v[132:135]\n"
"ds_read_b128 a[132:135], v3 offset:25600\n"
"v_accvgpr_read_b32 v14, a18\n"
"v_mfma_f32_16x16x32_bf16 v[132:135], a[88:91], a[128:131], v[132:135]\n"
"ds_read_b128 a[128:131], v3 offset:24576\n"
"v_accvgpr_read_b32 v15, a19\n"
"v_mfma_f32_16x16x32_bf16 v[136:139], a[88:91], a[136:139], v[136:139]\n"
"v_add_f32 v12, v12, v16\n"
"v_mfma_f32_16x16x32_bf16 v[136:139], a[92:95], a[140:143], v[136:139]\n"
"v_add_f32 v13, v13, v17\n"
"v_add_f32 v14, v14, v18\n"
"v_mfma_f32_16x16x32_bf16 v[108:111], a[84:87], a[140:143], v[108:111]\n"
"ds_read_b128 a[140:143], v3 offset:27648\n"
"v_add_f32 v15, v15, v19\n"
"v_mfma_f32_16x16x32_bf16 v[108:111], a[80:83], a[136:139], v[108:111]\n"
"ds_read_b128 a[136:139], v3 offset:26624\n"
"v_accvgpr_write_b32 a16, v12\n"
"v_mfma_f32_16x16x32_bf16 v[112:115], a[80:83], a[144:147], v[112:115]\n"
"v_accvgpr_write_b32 a17, v13\n"
"v_mfma_f32_16x16x32_bf16 v[112:115], a[84:87], a[148:151], v[112:115]\n"
"global_load_dwordx4 a[76:79], v0, s[34:35] offset:3072 sc1\n"
"v_accvgpr_write_b32 a18, v14\n"
"v_mfma_f32_16x16x32_bf16 v[140:143], a[92:95], a[148:151], v[140:143]\n"
"ds_read_b128 a[148:151], v3 offset:29696\n"
"v_accvgpr_write_b32 a19, v15\n"
"v_mfma_f32_16x16x32_bf16 v[140:143], a[88:91], a[144:147], v[140:143]\n"
"ds_read_b128 a[144:147], v3 offset:28672\n"
"v_lshlrev_b32 v20, 16, v22\n"
"v_mfma_f32_16x16x32_bf16 a[12:15], a[88:91], a[152:155], a[12:15]\n"
"v_and_b32 v21, s63, v22\n"
"v_lshlrev_b32 v22, 16, v23\n"
"v_mfma_f32_16x16x32_bf16 a[12:15], a[92:95], a[156:159], a[12:15]\n"
"v_and_b32 v23, s63, v23\n"
"v_mfma_f32_16x16x32_bf16 a[8:11], a[84:87], a[156:159], a[8:11]\n"
"ds_read_b128 a[156:159], v3 offset:31744\n"
"v_add_f32 v172, v172, v20\n"
"v_mfma_f32_16x16x32_bf16 a[8:11], a[80:83], a[152:155], a[8:11]\n"
"s_waitcnt vmcnt(8) lgkmcnt(11)\n"
"ds_read_b128 a[152:155], v3 offset:30720\n"
"v_add_f32 v173, v173, v21\n"
"v_mfma_f32_16x16x32_bf16 v[32:35], a[32:35], a[96:99], v[32:35]\n"
"s_add_u32 s34, s36, 1527808\n"
"v_add_f32 v174, v174, v22\n"
"v_mfma_f32_16x16x32_bf16 v[32:35], a[36:39], a[100:103], v[32:35]\n"
"global_load_dwordx4 a[80:83], v0, s[34:35] offset:0 sc1\n"
"v_add_f32 v175, v175, v23\n"
"v_mfma_f32_16x16x32_bf16 v[60:63], a[44:47], a[100:103], v[60:63]\n"
"v_add_f32 v176, v176, v20\n"
"v_mfma_f32_16x16x32_bf16 v[60:63], a[40:43], a[96:99], v[60:63]\n"
"v_add_f32 v177, v177, v21\n"
"v_add_f32 v178, v178, v22\n"
"v_mfma_f32_16x16x32_bf16 v[64:67], a[40:43], a[104:107], v[64:67]\n"
"v_add_f32 v179, v179, v23\n"
"v_mfma_f32_16x16x32_bf16 v[64:67], a[44:47], a[108:111], v[64:67]\n"
"v_add_f32 v180, v180, v20\n"
"v_mfma_f32_16x16x32_bf16 v[36:39], a[36:39], a[108:111], v[36:39]\n"
"v_add_f32 v181, v181, v21\n"
"v_mfma_f32_16x16x32_bf16 v[36:39], a[32:35], a[104:107], v[36:39]\n"
"s_waitcnt lgkmcnt(8)\n"
"v_add_f32 v182, v182, v22\n"
"v_mfma_f32_16x16x32_bf16 v[40:43], a[32:35], a[112:115], v[40:43]\n"
"v_add_f32 v183, v183, v23\n"
"v_mfma_f32_16x16x32_bf16 v[40:43], a[36:39], a[116:119], v[40:43]\n"
"global_load_dwordx4 a[84:87], v0, s[34:35] offset:1024 sc1\n"
"v_add_f32 v184, v184, v20\n"
"v_mfma_f32_16x16x32_bf16 v[68:71], a[44:47], a[116:119], v[68:71]\n"
"v_add_f32 v185, v185, v21\n"
"v_mfma_f32_16x16x32_bf16 v[68:71], a[40:43], a[112:115], v[68:71]\n"
"v_add_f32 v186, v186, v22\n"
"v_add_f32 v187, v187, v23\n"
"v_mfma_f32_16x16x32_bf16 v[72:75], a[40:43], a[120:123], v[72:75]\n"
"v_add_f32 v188, v188, v20\n"
"v_mfma_f32_16x16x32_bf16 v[72:75], a[44:47], a[124:127], v[72:75]\n"
"v_add_f32 v189, v189, v21\n"
"v_mfma_f32_16x16x32_bf16 v[44:47], a[36:39], a[124:127], v[44:47]\n"
"v_add_f32 v190, v190, v22\n"
"v_mfma_f32_16x16x32_bf16 v[44:47], a[32:35], a[120:123], v[44:47]\n"
"s_waitcnt lgkmcnt(4)\n"
"v_add_f32 v191, v191, v23\n"
"v_mfma_f32_16x16x32_bf16 v[48:51], a[32:35], a[128:131], v[48:51]\n"
"v_add_f32 v192, v192, v20\n"
"v_mfma_f32_16x16x32_bf16 v[48:51], a[36:39], a[132:135], v[48:51]\n"
"global_load_dwordx4 a[88:91], v0, s[34:35] offset:2048 sc1\n"
"v_add_f32 v193, v193, v21\n"
"v_mfma_f32_16x16x32_bf16 v[76:79], a[44:47], a[132:135], v[76:79]\n"
"v_add_f32 v194, v194, v22\n"
"v_add_f32 v195, v195, v23\n"
"v_mfma_f32_16x16x32_bf16 v[76:79], a[40:43], a[128:131], v[76:79]\n"
"v_add_f32 v196, v196, v20\n"
"v_mfma_f32_16x16x32_bf16 v[80:83], a[40:43], a[136:139], v[80:83]\n"
"v_add_f32 v197, v197, v21\n"
"v_mfma_f32_16x16x32_bf16 v[80:83], a[44:47], a[140:143], v[80:83]\n"
"v_add_f32 v198, v198, v22\n"
"v_mfma_f32_16x16x32_bf16 v[52:55], a[36:39], a[140:143], v[52:55]\n"
"v_add_f32 v199, v199, v23\n"
"v_mfma_f32_16x16x32_bf16 v[52:55], a[32:35], a[136:139], v[52:55]\n"
"s_waitcnt lgkmcnt(0)\n"
"v_accvgpr_read_b32 v12, a20\n"
"v_mfma_f32_16x16x32_bf16 v[56:59], a[32:35], a[144:147], v[56:59]\n"
"v_accvgpr_read_b32 v13, a21\n"
"v_mfma_f32_16x16x32_bf16 v[56:59], a[36:39], a[148:151], v[56:59]\n"
"global_load_dwordx4 a[92:95], v0, s[34:35] offset:3072 sc1\n"
"v_accvgpr_read_b32 v14, a22\n"
"v_mfma_f32_16x16x32_bf16 v[84:87], a[44:47], a[148:151], v[84:87]\n"
"v_accvgpr_read_b32 v15, a23\n"
"v_add_f32 v12, v12, v20\n"
"v_mfma_f32_16x16x32_bf16 v[84:87], a[40:43], a[144:147], v[84:87]\n"
"v_add_f32 v13, v13, v21\n"
"v_mfma_f32_16x16x32_bf16 a[4:7], a[40:43], a[152:155], a[4:7]\n"
"v_add_f32 v14, v14, v22\n"
"v_mfma_f32_16x16x32_bf16 a[4:7], a[44:47], a[156:159], a[4:7]\n"
"v_add_f32 v15, v15, v23\n"
"v_mfma_f32_16x16x32_bf16 a[0:3], a[36:39], a[156:159], a[0:3]\n"
"v_accvgpr_write_b32 a20, v12\n"
"v_mfma_f32_16x16x32_bf16 a[0:3], a[32:35], a[152:155], a[0:3]\n"
"s_waitcnt vmcnt(8)\n"
"v_accvgpr_write_b32 a21, v13\n"
"v_mfma_f32_16x16x32_bf16 v[88:91], a[48:51], a[96:99], v[88:91]\n"
"s_add_u32 s34, s36, 1531904\n"
"v_accvgpr_write_b32 a22, v14\n"
"v_accvgpr_write_b32 a23, v15\n"
"v_mfma_f32_16x16x32_bf16 v[88:91], a[52:55], a[100:103], v[88:91]\n"
"global_load_dwordx4 a[32:35], v0, s[34:35] offset:0 sc1\n"
"v_lshlrev_b32 v24, 16, v26\n"
"v_mfma_f32_16x16x32_bf16 v[116:119], a[60:63], a[100:103], v[116:119]\n"
"ds_read_b128 a[100:103], v4 offset:17408\n"
"v_and_b32 v25, s63, v26\n"
"v_mfma_f32_16x16x32_bf16 v[116:119], a[56:59], a[96:99], v[116:119]\n"
"ds_read_b128 a[96:99], v4 offset:16384\n"
"v_lshlrev_b32 v26, 16, v27\n"
"v_mfma_f32_16x16x32_bf16 v[120:123], a[56:59], a[104:107], v[120:123]\n"
"v_and_b32 v27, s63, v27\n"
"v_mfma_f32_16x16x32_bf16 v[120:123], a[60:63], a[108:111], v[120:123]\n"
"v_add_f32 v200, v200, v24\n"
"v_mfma_f32_16x16x32_bf16 v[92:95], a[52:55], a[108:111], v[92:95]\n"
"ds_read_b128 a[108:111], v4 offset:19456\n"
"v_add_f32 v201, v201, v25\n"
"v_mfma_f32_16x16x32_bf16 v[92:95], a[48:51], a[104:107], v[92:95]\n"
"ds_read_b128 a[104:107], v4 offset:18432\n"
"v_add_f32 v202, v202, v26\n"
"v_mfma_f32_16x16x32_bf16 v[96:99], a[48:51], a[112:115], v[96:99]\n"
"v_add_f32 v203, v203, v27\n"
"v_add_f32 v204, v204, v24\n"
"v_mfma_f32_16x16x32_bf16 v[96:99], a[52:55], a[116:119], v[96:99]\n"
"global_load_dwordx4 a[36:39], v0, s[34:35] offset:1024 sc1\n"
"v_add_f32 v205, v205, v25\n"
"v_mfma_f32_16x16x32_bf16 v[124:127], a[60:63], a[116:119], v[124:127]\n"
"ds_read_b128 a[116:119], v4 offset:21504\n"
"v_add_f32 v206, v206, v26\n"
"v_mfma_f32_16x16x32_bf16 v[124:127], a[56:59], a[112:115], v[124:127]\n"
"ds_read_b128 a[112:115], v4 offset:20480\n"
"v_add_f32 v207, v207, v27\n"
"v_mfma_f32_16x16x32_bf16 v[128:131], a[56:59], a[120:123], v[128:131]\n"
"v_add_f32 v208, v208, v24\n"
"v_mfma_f32_16x16x32_bf16 v[128:131], a[60:63], a[124:127], v[128:131]\n"
"v_add_f32 v209, v209, v25\n"
"v_mfma_f32_16x16x32_bf16 v[100:103], a[52:55], a[124:127], v[100:103]\n"
"ds_read_b128 a[124:127], v4 offset:23552\n"
"v_add_f32 v210, v210, v26\n"
"v_mfma_f32_16x16x32_bf16 v[100:103], a[48:51], a[120:123], v[100:103]\n"
"ds_read_b128 a[120:123], v4 offset:22528\n"
"v_add_f32 v211, v211, v27\n"
"v_mfma_f32_16x16x32_bf16 v[104:107], a[48:51], a[128:131], v[104:107]\n"
"v_add_f32 v212, v212, v24\n"
"v_add_f32 v213, v213, v25\n"
"v_mfma_f32_16x16x32_bf16 v[104:107], a[52:55], a[132:135], v[104:107]\n"
"global_load_dwordx4 a[40:43], v0, s[34:35] offset:2048 sc1\n"
"v_add_f32 v214, v214, v26\n"
"v_mfma_f32_16x16x32_bf16 v[132:135], a[60:63], a[132:135], v[132:135]\n"
"ds_read_b128 a[132:135], v4 offset:25600\n"
"v_add_f32 v215, v215, v27\n"
"v_mfma_f32_16x16x32_bf16 v[132:135], a[56:59], a[128:131], v[132:135]\n"
"ds_read_b128 a[128:131], v4 offset:24576\n"
"v_add_f32 v216, v216, v24\n"
"v_mfma_f32_16x16x32_bf16 v[136:139], a[56:59], a[136:139], v[136:139]\n"
"v_add_f32 v217, v217, v25\n"
"v_mfma_f32_16x16x32_bf16 v[136:139], a[60:63], a[140:143], v[136:139]\n"
"v_add_f32 v218, v218, v26\n"
"v_mfma_f32_16x16x32_bf16 v[108:111], a[52:55], a[140:143], v[108:111]\n"
"ds_read_b128 a[140:143], v4 offset:27648\n"
"v_add_f32 v219, v219, v27\n"
"v_mfma_f32_16x16x32_bf16 v[108:111], a[48:51], a[136:139], v[108:111]\n"
"ds_read_b128 a[136:139], v4 offset:26624\n"
"v_add_f32 v220, v220, v24\n"
"v_mfma_f32_16x16x32_bf16 v[112:115], a[48:51], a[144:147], v[112:115]\n"
"v_add_f32 v221, v221, v25\n"
"v_add_f32 v222, v222, v26\n"
"v_mfma_f32_16x16x32_bf16 v[112:115], a[52:55], a[148:151], v[112:115]\n"
"global_load_dwordx4 a[44:47], v0, s[34:35] offset:3072 sc1\n"
"v_add_f32 v223, v223, v27\n"
"v_mfma_f32_16x16x32_bf16 v[140:143], a[60:63], a[148:151], v[140:143]\n"
"ds_read_b128 a[148:151], v4 offset:29696\n"
"v_add_f32 v224, v224, v24\n"
"v_mfma_f32_16x16x32_bf16 v[140:143], a[56:59], a[144:147], v[140:143]\n"
"ds_read_b128 a[144:147], v4 offset:28672\n"
"v_add_f32 v225, v225, v25\n"
"v_mfma_f32_16x16x32_bf16 a[12:15], a[56:59], a[152:155], a[12:15]\n"
"v_add_f32 v226, v226, v26\n"
"v_mfma_f32_16x16x32_bf16 a[12:15], a[60:63], a[156:159], a[12:15]\n"
"v_add_f32 v227, v227, v27\n"
"v_mfma_f32_16x16x32_bf16 a[8:11], a[52:55], a[156:159], a[8:11]\n"
"ds_read_b128 a[156:159], v4 offset:31744\n"
"v_accvgpr_read_b32 v12, a24\n"
"v_mfma_f32_16x16x32_bf16 a[8:11], a[48:51], a[152:155], a[8:11]\n"
"s_waitcnt vmcnt(8) lgkmcnt(11)\n"
"ds_read_b128 a[152:155], v4 offset:30720\n"
"v_accvgpr_read_b32 v13, a25\n"
"v_mfma_f32_16x16x32_bf16 v[32:35], a[64:67], a[96:99], v[32:35]\n"
"s_add_u32 s34, s36, 1536000\n"
"v_accvgpr_read_b32 v14, a26\n"
"v_accvgpr_read_b32 v15, a27\n"
"v_mfma_f32_16x16x32_bf16 v[32:35], a[68:71], a[100:103], v[32:35]\n"
"global_load_dwordx4 a[48:51], v0, s[34:35] offset:0 sc1\n"
"v_add_f32 v12, v12, v24\n"
"v_mfma_f32_16x16x32_bf16 v[60:63], a[76:79], a[100:103], v[60:63]\n"
"v_add_f32 v13, v13, v25\n"
"v_mfma_f32_16x16x32_bf16 v[60:63], a[72:75], a[96:99], v[60:63]\n"
"v_add_f32 v14, v14, v26\n"
"v_mfma_f32_16x16x32_bf16 v[64:67], a[72:75], a[104:107], v[64:67]\n"
"v_add_f32 v15, v15, v27\n"
"v_mfma_f32_16x16x32_bf16 v[64:67], a[76:79], a[108:111], v[64:67]\n"
"v_accvgpr_write_b32 a24, v12\n"
"v_mfma_f32_16x16x32_bf16 v[36:39], a[68:71], a[108:111], v[36:39]\n"
"v_accvgpr_write_b32 a25, v13\n"
"v_accvgpr_write_b32 a26, v14\n"
"v_mfma_f32_16x16x32_bf16 v[36:39], a[64:67], a[104:107], v[36:39]\n"
"s_waitcnt lgkmcnt(8)\n"
"v_accvgpr_write_b32 a27, v15\n"
"v_mfma_f32_16x16x32_bf16 v[40:43], a[64:67], a[112:115], v[40:43]\n"
"v_lshlrev_b32 v28, 16, v30\n"
"v_mfma_f32_16x16x32_bf16 v[40:43], a[68:71], a[116:119], v[40:43]\n"
"global_load_dwordx4 a[52:55], v0, s[34:35] offset:1024 sc1\n"
"v_and_b32 v29, s63, v30\n"
"v_mfma_f32_16x16x32_bf16 v[68:71], a[76:79], a[116:119], v[68:71]\n"
"v_lshlrev_b32 v30, 16, v31\n"
"v_mfma_f32_16x16x32_bf16 v[68:71], a[72:75], a[112:115], v[68:71]\n"
"v_and_b32 v31, s63, v31\n"
"v_mfma_f32_16x16x32_bf16 v[72:75], a[72:75], a[120:123], v[72:75]\n"
"v_add_f32 v228, v228, v28\n"
"v_mfma_f32_16x16x32_bf16 v[72:75], a[76:79], a[124:127], v[72:75]\n"
"v_add_f32 v229, v229, v29\n"
"v_add_f32 v230, v230, v30\n"
"v_mfma_f32_16x16x32_bf16 v[44:47], a[68:71], a[124:127], v[44:47]\n"
"v_add_f32 v231, v231, v31\n"
"v_mfma_f32_16x16x32_bf16 v[44:47], a[64:67], a[120:123], v[44:47]\n"
"s_waitcnt lgkmcnt(4)\n"
"v_add_f32 v232, v232, v28\n"
"v_mfma_f32_16x16x32_bf16 v[48:51], a[64:67], a[128:131], v[48:51]\n"
"v_add_f32 v233, v233, v29\n"
"v_mfma_f32_16x16x32_bf16 v[48:51], a[68:71], a[132:135], v[48:51]\n"
"global_load_dwordx4 a[56:59], v0, s[34:35] offset:2048 sc1\n"
"v_add_f32 v234, v234, v30\n"
"v_mfma_f32_16x16x32_bf16 v[76:79], a[76:79], a[132:135], v[76:79]\n"
"v_add_f32 v235, v235, v31\n"
"v_mfma_f32_16x16x32_bf16 v[76:79], a[72:75], a[128:131], v[76:79]\n"
"v_add_f32 v236, v236, v28\n"
"v_mfma_f32_16x16x32_bf16 v[80:83], a[72:75], a[136:139], v[80:83]\n"
"v_add_f32 v237, v237, v29\n"
"v_mfma_f32_16x16x32_bf16 v[80:83], a[76:79], a[140:143], v[80:83]\n"
"v_add_f32 v238, v238, v30\n"
"v_add_f32 v239, v239, v31\n"
"v_mfma_f32_16x16x32_bf16 v[52:55], a[68:71], a[140:143], v[52:55]\n"
"v_add_f32 v240, v240, v28\n"
"v_mfma_f32_16x16x32_bf16 v[52:55], a[64:67], a[136:139], v[52:55]\n"
"s_waitcnt lgkmcnt(0)\n"
"v_add_f32 v241, v241, v29\n"
"v_mfma_f32_16x16x32_bf16 v[56:59], a[64:67], a[144:147], v[56:59]\n"
"v_add_f32 v242, v242, v30\n"
"v_mfma_f32_16x16x32_bf16 v[56:59], a[68:71], a[148:151], v[56:59]\n"
"global_load_dwordx4 a[60:63], v0, s[34:35] offset:3072 sc1\n"
"v_add_f32 v243, v243, v31\n"
"v_mfma_f32_16x16x32_bf16 v[84:87], a[76:79], a[148:151], v[84:87]\n"
"v_add_f32 v244, v244, v28\n"
"v_mfma_f32_16x16x32_bf16 v[84:87], a[72:75], a[144:147], v[84:87]\n"
"v_add_f32 v245, v245, v29\n"
"v_mfma_f32_16x16x32_bf16 a[4:7], a[72:75], a[152:155], a[4:7]\n"
"v_add_f32 v246, v246, v30\n"
"v_add_f32 v247, v247, v31\n"
"v_mfma_f32_16x16x32_bf16 a[4:7], a[76:79], a[156:159], a[4:7]\n"
"v_add_f32 v248, v248, v28\n"
"v_mfma_f32_16x16x32_bf16 a[0:3], a[68:71], a[156:159], a[0:3]\n"
"v_add_f32 v249, v249, v29\n"
"v_mfma_f32_16x16x32_bf16 a[0:3], a[64:67], a[152:155], a[0:3]\n"
"s_waitcnt vmcnt(8)\n"
"v_add_f32 v250, v250, v30\n"
"v_mfma_f32_16x16x32_bf16 v[88:91], a[80:83], a[96:99], v[88:91]\n"
"s_add_u32 s34, s36, 1540096\n"
"v_add_f32 v251, v251, v31\n"
"v_mfma_f32_16x16x32_bf16 v[88:91], a[84:87], a[100:103], v[88:91]\n"
"global_load_dwordx4 a[64:67], v0, s[34:35] offset:0 sc1\n"
"v_add_f32 v252, v252, v28\n"
"v_mfma_f32_16x16x32_bf16 v[116:119], a[92:95], a[100:103], v[116:119]\n"
"ds_read_b128 a[100:103], v5 offset:17408\n"
"v_add_f32 v253, v253, v29\n"
"v_mfma_f32_16x16x32_bf16 v[116:119], a[88:91], a[96:99], v[116:119]\n"
"ds_read_b128 a[96:99], v5 offset:16384\n"
"v_add_f32 v254, v254, v30\n"
"v_mfma_f32_16x16x32_bf16 v[120:123], a[88:91], a[104:107], v[120:123]\n"
"v_add_f32 v255, v255, v31\n"
"v_accvgpr_read_b32 v12, a28\n"
"v_mfma_f32_16x16x32_bf16 v[120:123], a[92:95], a[108:111], v[120:123]\n"
"v_accvgpr_read_b32 v13, a29\n"
"v_mfma_f32_16x16x32_bf16 v[92:95], a[84:87], a[108:111], v[92:95]\n"
"ds_read_b128 a[108:111], v5 offset:19456\n"
"v_accvgpr_read_b32 v14, a30\n"
"v_mfma_f32_16x16x32_bf16 v[92:95], a[80:83], a[104:107], v[92:95]\n"
"ds_read_b128 a[104:107], v5 offset:18432\n"
"v_accvgpr_read_b32 v15, a31\n"
"v_mfma_f32_16x16x32_bf16 v[96:99], a[80:83], a[112:115], v[96:99]\n"
"v_add_f32 v12, v12, v28\n"
"v_mfma_f32_16x16x32_bf16 v[96:99], a[84:87], a[116:119], v[96:99]\n"
"global_load_dwordx4 a[68:71], v0, s[34:35] offset:1024 sc1\n"
"v_add_f32 v13, v13, v29\n"
"v_mfma_f32_16x16x32_bf16 v[124:127], a[92:95], a[116:119], v[124:127]\n"
"ds_read_b128 a[116:119], v5 offset:21504\n"
"v_add_f32 v14, v14, v30\n"
"v_mfma_f32_16x16x32_bf16 v[124:127], a[88:91], a[112:115], v[124:127]\n"
"ds_read_b128 a[112:115], v5 offset:20480\n"
"v_add_f32 v15, v15, v31\n"
"v_mfma_f32_16x16x32_bf16 v[128:131], a[88:91], a[120:123], v[128:131]\n"
"v_accvgpr_write_b32 a28, v12\n"
"v_accvgpr_write_b32 a29, v13\n"
"v_mfma_f32_16x16x32_bf16 v[128:131], a[92:95], a[124:127], v[128:131]\n"
"v_accvgpr_write_b32 a30, v14\n"
"v_mfma_f32_16x16x32_bf16 v[100:103], a[84:87], a[124:127], v[100:103]\n"
"ds_read_b128 a[124:127], v5 offset:23552\n"
"v_accvgpr_write_b32 a31, v15\n"
"v_mfma_f32_16x16x32_bf16 v[100:103], a[80:83], a[120:123], v[100:103]\n"
"ds_read_b128 a[120:123], v5 offset:22528\n"
"v_mfma_f32_16x16x32_bf16 v[104:107], a[80:83], a[128:131], v[104:107]\n"
"v_mfma_f32_16x16x32_bf16 v[104:107], a[84:87], a[132:135], v[104:107]\n"
"global_load_dwordx4 a[72:75], v0, s[34:35] offset:2048 sc1\n"
"v_mfma_f32_16x16x32_bf16 v[132:135], a[92:95], a[132:135], v[132:135]\n"
"ds_read_b128 a[132:135], v5 offset:25600\n"
"v_mfma_f32_16x16x32_bf16 v[132:135], a[88:91], a[128:131], v[132:135]\n"
"ds_read_b128 a[128:131], v5 offset:24576\n"
"v_mfma_f32_16x16x32_bf16 v[136:139], a[88:91], a[136:139], v[136:139]\n"
"ds_read_b128 v[16:19], v7 offset:32768\n"
"v_mfma_f32_16x16x32_bf16 v[136:139], a[92:95], a[140:143], v[136:139]\n"
"v_mfma_f32_16x16x32_bf16 v[108:111], a[84:87], a[140:143], v[108:111]\n"
"ds_read_b128 a[140:143], v5 offset:27648\n"
"v_mfma_f32_16x16x32_bf16 v[108:111], a[80:83], a[136:139], v[108:111]\n"
"ds_read_b128 a[136:139], v5 offset:26624\n"
"v_mfma_f32_16x16x32_bf16 v[112:115], a[80:83], a[144:147], v[112:115]\n"
"v_mfma_f32_16x16x32_bf16 v[112:115], a[84:87], a[148:151], v[112:115]\n"
"global_load_dwordx4 a[76:79], v0, s[34:35] offset:3072 sc1\n"
"v_mfma_f32_16x16x32_bf16 v[140:143], a[92:95], a[148:151], v[140:143]\n"
"ds_read_b128 a[148:151], v5 offset:29696\n"
"v_mfma_f32_16x16x32_bf16 v[140:143], a[88:91], a[144:147], v[140:143]\n"
"ds_read_b128 a[144:147], v5 offset:28672\n"
"v_mfma_f32_16x16x32_bf16 a[12:15], a[88:91], a[152:155], a[12:15]\n"
"v_mfma_f32_16x16x32_bf16 a[12:15], a[92:95], a[156:159], a[12:15]\n"
"v_mfma_f32_16x16x32_bf16 a[8:11], a[84:87], a[156:159], a[8:11]\n"
"ds_read_b128 a[156:159], v5 offset:31744\n"
"v_mfma_f32_16x16x32_bf16 a[8:11], a[80:83], a[152:155], a[8:11]\n"
"s_waitcnt vmcnt(8) lgkmcnt(12)\n"
"ds_read_b128 a[152:155], v5 offset:30720\n"
"v_mfma_f32_16x16x32_bf16 v[32:35], a[32:35], a[96:99], v[32:35]\n"
"s_add_u32 s34, s36, 1544192\n"
"v_mfma_f32_16x16x32_bf16 v[32:35], a[36:39], a[100:103], v[32:35]\n"
"global_load_dwordx4 a[80:83], v0, s[34:35] offset:0 sc1\n"
"v_mfma_f32_16x16x32_bf16 v[60:63], a[44:47], a[100:103], v[60:63]\n"
"v_mfma_f32_16x16x32_bf16 v[60:63], a[40:43], a[96:99], v[60:63]\n"
"v_mfma_f32_16x16x32_bf16 v[64:67], a[40:43], a[104:107], v[64:67]\n"
"v_mfma_f32_16x16x32_bf16 v[64:67], a[44:47], a[108:111], v[64:67]\n"
"v_mfma_f32_16x16x32_bf16 v[36:39], a[36:39], a[108:111], v[36:39]\n"
"v_mfma_f32_16x16x32_bf16 v[36:39], a[32:35], a[104:107], v[36:39]\n"
"s_waitcnt lgkmcnt(9)\n"
"v_mfma_f32_16x16x32_bf16 v[40:43], a[32:35], a[112:115], v[40:43]\n"
"s_mov_b32 s52, %4\n"
"s_and_b32 s53, %5, 0xffff\n"
"v_mfma_f32_16x16x32_bf16 v[40:43], a[36:39], a[116:119], v[40:43]\n"
"global_load_dwordx4 a[84:87], v0, s[34:35] offset:1024 sc1\n"
"v_mfma_f32_16x16x32_bf16 v[68:71], a[44:47], a[116:119], v[68:71]\n"
"s_mov_b32 s54, 260352\n"
"s_mov_b32 s55, 0x20000\n"
"v_mfma_f32_16x16x32_bf16 v[68:71], a[40:43], a[112:115], v[68:71]\n"
"v_xor_b32 v8, 64, v7\n"
"v_mfma_f32_16x16x32_bf16 v[72:75], a[40:43], a[120:123], v[72:75]\n"
"s_waitcnt lgkmcnt(6)\n"
"v_lshlrev_b32 v12, 16, v16\n"
"v_mfma_f32_16x16x32_bf16 v[72:75], a[44:47], a[124:127], v[72:75]\n"
"v_and_b32 v13, s63, v16\n"
"v_lshlrev_b32 v14, 16, v17\n"
"v_mfma_f32_16x16x32_bf16 v[44:47], a[36:39], a[124:127], v[44:47]\n"
"v_and_b32 v15, s63, v17\n"
"v_mfma_f32_16x16x32_bf16 v[44:47], a[32:35], a[120:123], v[44:47]\n"
"s_waitcnt lgkmcnt(4)\n"
"v_xor_b32 v20, 64, v7\n"
"v_mfma_f32_16x16x32_bf16 v[48:51], a[32:35], a[128:131], v[48:51]\n"
"ds_read_b128 v[20:23], v20 offset:32768\n"
"v_mul_f32 v12, v12, v32\n"
"v_mfma_f32_16x16x32_bf16 v[48:51], a[36:39], a[132:135], v[48:51]\n"
"global_load_dwordx4 a[88:91], v0, s[34:35] offset:2048 sc1\n"
"v_mul_f32 v13, v13, v33\n"
"v_mfma_f32_16x16x32_bf16 v[76:79], a[44:47], a[132:135], v[76:79]\n"
"v_mul_f32 v14, v14, v34\n"
"v_mfma_f32_16x16x32_bf16 v[76:79], a[40:43], a[128:131], v[76:79]\n"
"v_mul_f32 v15, v15, v35\n"
"v_mfma_f32_16x16x32_bf16 v[80:83], a[40:43], a[136:139], v[80:83]\n"
"v_cvt_pk_bf16_f32 v24, v12, v13\n"
"v_mfma_f32_16x16x32_bf16 v[80:83], a[44:47], a[140:143], v[80:83]\n"
"v_cvt_pk_bf16_f32 v25, v14, v15\n"
"v_mfma_f32_16x16x32_bf16 v[52:55], a[36:39], a[140:143], v[52:55]\n"
"ds_write_b64 v7, v[24:25] offset:32768\n"
"v_mfma_f32_16x16x32_bf16 v[52:55], a[32:35], a[136:139], v[52:55]\n"
"s_waitcnt lgkmcnt(2)\n"
"v_lshlrev_b32 v12, 16, v18\n"
"v_mfma_f32_16x16x32_bf16 v[56:59], a[32:35], a[144:147], v[56:59]\n"
"v_and_b32 v13, s63, v18\n"
"v_mfma_f32_16x16x32_bf16 v[56:59], a[36:39], a[148:151], v[56:59]\n"
"global_load_dwordx4 a[92:95], v0, s[34:35] offset:3072 sc1\n"
"v_mfma_f32_16x16x32_bf16 v[84:87], a[44:47], a[148:151], v[84:87]\n"
"v_lshlrev_b32 v14, 16, v19\n"
"v_mfma_f32_16x16x32_bf16 v[84:87], a[40:43], a[144:147], v[84:87]\n"
"v_and_b32 v15, s63, v19\n"
"v_mfma_f32_16x16x32_bf16 a[4:7], a[40:43], a[152:155], a[4:7]\n"
"v_mul_f32 v12, v12, v60\n"
"v_mfma_f32_16x16x32_bf16 a[4:7], a[44:47], a[156:159], a[4:7]\n"
"v_mul_f32 v13, v13, v61\n"
"v_mfma_f32_16x16x32_bf16 a[0:3], a[36:39], a[156:159], a[0:3]\n"
"v_mul_f32 v14, v14, v62\n"
"v_mfma_f32_16x16x32_bf16 a[0:3], a[32:35], a[152:155], a[0:3]\n"
"s_waitcnt vmcnt(8)\n"
"v_mul_f32 v15, v15, v63\n"
"v_mfma_f32_16x16x32_bf16 v[88:91], a[48:51], a[96:99], v[88:91]\n"
"s_add_u32 s34, s36, 1466368\n"
"v_cvt_pk_bf16_f32 v26, v12, v13\n"
"v_mfma_f32_16x16x32_bf16 v[88:91], a[52:55], a[100:103], v[88:91]\n"
"global_load_dwordx4 a[32:35], v0, s[34:35] offset:0 sc1\n"
"v_mfma_f32_16x16x32_bf16 v[116:119], a[60:63], a[100:103], v[116:119]\n"
"v_cvt_pk_bf16_f32 v27, v14, v15\n"
"v_mfma_f32_16x16x32_bf16 v[116:119], a[56:59], a[96:99], v[116:119]\n"
"ds_write_b64 v7, v[26:27] offset:32776\n"
"v_mfma_f32_16x16x32_bf16 v[120:123], a[56:59], a[104:107], v[120:123]\n"
"s_waitcnt lgkmcnt(2)\n"
"v_lshlrev_b32 v12, 16, v20\n"
"v_mfma_f32_16x16x32_bf16 v[120:123], a[60:63], a[108:111], v[120:123]\n"
"v_and_b32 v13, s63, v20\n"
"v_mfma_f32_16x16x32_bf16 v[92:95], a[52:55], a[108:111], v[92:95]\n"
"v_lshlrev_b32 v14, 16, v21\n"
"v_mfma_f32_16x16x32_bf16 v[92:95], a[48:51], a[104:107], v[92:95]\n"
"v_and_b32 v15, s63, v21\n"
"v_mfma_f32_16x16x32_bf16 v[96:99], a[48:51], a[112:115], v[96:99]\n"
"ds_read_b128 v[16:19], v7 offset:34816\n"
"v_mfma_f32_16x16x32_bf16 v[96:99], a[52:55], a[116:119], v[96:99]\n"
"global_load_dwordx4 a[36:39], v0, s[34:35] offset:1024 sc1\n"
"v_mul_f32 v12, v12, v88\n"
"v_mul_f32 v13, v13, v89\n"
"v_mfma_f32_16x16x32_bf16 v[124:127], a[60:63], a[116:119], v[124:127]\n"
"v_mul_f32 v14, v14, v90\n"
"v_mul_f32 v15, v15, v91\n"
"v_mfma_f32_16x16x32_bf16 v[124:127], a[56:59], a[112:115], v[124:127]\n"
"v_cvt_pk_bf16_f32 v28, v12, v13\n"
"v_cvt_pk_bf16_f32 v29, v14, v15\n"
"v_mfma_f32_16x16x32_bf16 v[128:131], a[56:59], a[120:123], v[128:131]\n"
"ds_write_b64 v8, v[28:29] offset:32768\n"
"v_mfma_f32_16x16x32_bf16 v[128:131], a[60:63], a[124:127], v[128:131]\n"
"v_lshlrev_b32 v12, 16, v22\n"
"v_and_b32 v13, s63, v22\n"
"v_mfma_f32_16x16x32_bf16 v[100:103], a[52:55], a[124:127], v[100:103]\n"
"v_lshlrev_b32 v14, 16, v23\n"
"v_and_b32 v15, s63, v23\n"
"v_mfma_f32_16x16x32_bf16 v[100:103], a[48:51], a[120:123], v[100:103]\n"
"v_mul_f32 v12, v12, v116\n"
"v_mul_f32 v13, v13, v117\n"
"v_mfma_f32_16x16x32_bf16 v[104:107], a[48:51], a[128:131], v[104:107]\n"
"v_mul_f32 v14, v14, v118\n"
"v_mul_f32 v15, v15, v119\n"
"v_mfma_f32_16x16x32_bf16 v[104:107], a[52:55], a[132:135], v[104:107]\n"
"global_load_dwordx4 a[40:43], v0, s[34:35] offset:2048 sc1\n"
"v_cvt_pk_bf16_f32 v30, v12, v13\n"
"v_mfma_f32_16x16x32_bf16 v[132:135], a[60:63], a[132:135], v[132:135]\n"
"v_cvt_pk_bf16_f32 v31, v14, v15\n"
"v_mfma_f32_16x16x32_bf16 v[132:135], a[56:59], a[128:131], v[132:135]\n"
"ds_write_b64 v8, v[30:31] offset:32776\n"
"v_mfma_f32_16x16x32_bf16 v[136:139], a[56:59], a[136:139], v[136:139]\n"
"ds_read_b128 v[32:35], v2 offset:32768\n"
"v_mfma_f32_16x16x32_bf16 v[136:139], a[60:63], a[140:143], v[136:139]\n"
"s_waitcnt lgkmcnt(3)\n"
"ds_read_b128 v[60:63], v2 offset:33792\n"
"v_lshlrev_b32 v12, 16, v16\n"
"v_mfma_f32_16x16x32_bf16 v[108:111], a[52:55], a[140:143], v[108:111]\n"
"s_add_u32 s96, s36, 1568768\n"
"v_and_b32 v13, s63, v16\n"
"v_lshlrev_b32 v14, 16, v17\n"
"v_mfma_f32_16x16x32_bf16 v[108:111], a[48:51], a[136:139], v[108:111]\n"
"v_and_b32 v15, s63, v17\n"
"v_xor_b32 v20, 64, v7\n"
"v_mfma_f32_16x16x32_bf16 v[112:115], a[48:51], a[144:147], v[112:115]\n"
"global_load_dwordx4 v[88:91], v0, s[96:97] offset:-4096 sc1\n"
"ds_read_b128 v[20:23], v20 offset:34816\n"
"v_mfma_f32_16x16x32_bf16 v[112:115], a[52:55], a[148:151], v[112:115]\n"
"global_load_dwordx4 a[44:47], v0, s[34:35] offset:3072 sc1\n"
"v_mul_f32 v12, v12, v36\n"
"v_mfma_f32_16x16x32_bf16 v[140:143], a[60:63], a[148:151], v[140:143]\n"
"v_mul_f32 v13, v13, v37\n"
"v_mul_f32 v14, v14, v38\n"
"v_mfma_f32_16x16x32_bf16 v[140:143], a[56:59], a[144:147], v[140:143]\n"
"v_mul_f32 v15, v15, v39\n"
"v_mfma_f32_16x16x32_bf16 a[12:15], a[56:59], a[152:155], a[12:15]\n"
"v_cvt_pk_bf16_f32 v24, v12, v13\n"
"v_mfma_f32_16x16x32_bf16 a[12:15], a[60:63], a[156:159], a[12:15]\n"
"v_cvt_pk_bf16_f32 v25, v14, v15\n"
"v_mfma_f32_16x16x32_bf16 a[8:11], a[52:55], a[156:159], a[8:11]\n"
"s_waitcnt lgkmcnt(2)\n"
"buffer_store_dwordx4 v[32:35], v6, s[52:55], s80 offen offset:0 nt\n"
"ds_write_b64 v7, v[24:25] offset:34816\n"
"v_mfma_f32_16x16x32_bf16 a[8:11], a[48:51], a[152:155], a[8:11]\n"
"s_waitcnt vmcnt(10)\n"
"v_lshlrev_b32 v12, 16, v18\n"
"v_and_b32 v13, s63, v18\n"
"v_mfma_f32_16x16x32_bf16 v[144:147], a[64:67], a[96:99], v[144:147]\n"
"s_add_u32 s34, s36, 1470464\n"
"v_lshlrev_b32 v14, 16, v19\n"
"v_and_b32 v15, s63, v19\n"
"v_mfma_f32_16x16x32_bf16 v[144:147], a[68:71], a[100:103], v[144:147]\n"
"L_st10272:\n"
"s_cbranch_execz L_retC\n"
"global_load_dwordx4 a[48:51], v0, s[34:35] offset:0 sc1\n"
"v_mfma_f32_16x16x32_bf16 v[172:175], a[76:79], a[100:103], v[172:175]\n"
"global_load_dwordx4 v[116:119], v0, s[96:97] offset:-3072 sc1\n"
"v_mfma_f32_16x16x32_bf16 v[172:175], a[72:75], a[96:99], v[172:175]\n"
"v_mul_f32 v12, v12, v64\n"
"v_mfma_f32_16x16x32_bf16 v[176:179], a[72:75], a[104:107], v[176:179]\n"
"v_mul_f32 v13, v13, v65\n"
"v_mfma_f32_16x16x32_bf16 v[176:179], a[76:79], a[108:111], v[176:179]\n"
"v_mul_f32 v14, v14, v66\n"
"v_mfma_f32_16x16x32_bf16 v[148:151], a[68:71], a[108:111], v[148:151]\n"
"v_mul_f32 v15, v15, v67\n"
"v_mfma_f32_16x16x32_bf16 v[148:151], a[64:67], a[104:107], v[148:151]\n"
"v_cvt_pk_bf16_f32 v26, v12, v13\n"
"v_mfma_f32_16x16x32_bf16 v[152:155], a[64:67], a[112:115], v[152:155]\n"
"v_cvt_pk_bf16_f32 v27, v14, v15\n"
"v_mfma_f32_16x16x32_bf16 v[152:155], a[68:71], a[116:119], v[152:155]\n"
"global_load_dwordx4 a[52:55], v0, s[34:35] offset:1024 sc1\n"
"v_mfma_f32_16x16x32_bf16 v[180:183], a[76:79], a[116:119], v[180:183]\n"
"s_waitcnt lgkmcnt(2)\n"
"buffer_store_dwordx4 v[60:63], v6, s[52:55], s81 offen offset:1024 nt\n"
"ds_write_b64 v7, v[26:27] offset:34824\n"
"v_mfma_f32_16x16x32_bf16 v[180:183], a[72:75], a[112:115], v[180:183]\n"
"s_waitcnt lgkmcnt(2)\n"
"v_lshlrev_b32 v12, 16, v20\n"
"v_and_b32 v13, s63, v20\n"
"v_mfma_f32_16x16x32_bf16 v[184:187], a[72:75], a[120:123], v[184:187]\n"
"v_lshlrev_b32 v14, 16, v21\n"
"v_and_b32 v15, s63, v21\n"
"v_mfma_f32_16x16x32_bf16 v[184:187], a[76:79], a[124:127], v[184:187]\n"
"ds_read_b128 v[16:19], v7 offset:36864\n"
"v_mul_f32 v12, v12, v92\n"
"v_mfma_f32_16x16x32_bf16 v[156:159], a[68:71], a[124:127], v[156:159]\n"
"v_mul_f32 v13, v13, v93\n"
"v_mul_f32 v14, v14, v94\n"
"v_mfma_f32_16x16x32_bf16 v[156:159], a[64:67], a[120:123], v[156:159]\n"
"v_mul_f32 v15, v15, v95\n"
"v_cvt_pk_bf16_f32 v28, v12, v13\n"
"v_mfma_f32_16x16x32_bf16 v[160:163], a[64:67], a[128:131], v[160:163]\n"
"v_cvt_pk_bf16_f32 v29, v14, v15\n"
"v_mfma_f32_16x16x32_bf16 v[160:163], a[68:71], a[132:135], v[160:163]\n"
"global_load_dwordx4 a[56:59], v0, s[34:35] offset:2048 sc1\n"
"v_mfma_f32_16x16x32_bf16 v[188:191], a[76:79], a[132:135], v[188:191]\n"
"ds_write_b64 v8, v[28:29] offset:34816\n"
"v_mfma_f32_16x16x32_bf16 v[188:191], a[72:75], a[128:131], v[188:191]\n"
"v_lshlrev_b32 v12, 16, v22\n"
"v_and_b32 v13, s63, v22\n"
"v_mfma_f32_16x16x32_bf16 v[192:195], a[72:75], a[136:139], v[192:195]\n"
"v_lshlrev_b32 v14, 16, v23\n"
"v_and_b32 v15, s63, v23\n"
"v_mfma_f32_16x16x32_bf16 v[192:195], a[76:79], a[140:143], v[192:195]\n"
"v_mul_f32 v12, v12, v120\n"
"v_mul_f32 v13, v13, v121\n"
"v_mfma_f32_16x16x32_bf16 v[164:167], a[68:71], a[140:143], v[164:167]\n"
"v_mul_f32 v14, v14, v122\n"
"v_mul_f32 v15, v15, v123\n"
"v_mfma_f32_16x16x32_bf16 v[164:167], a[64:67], a[136:139], v[164:167]\n"
"v_cvt_pk_bf16_f32 v30, v12, v13\n"
"v_cvt_pk_bf16_f32 v31, v14, v15\n"
"v_mfma_f32_16x16x32_bf16 v[168:171], a[64:67], a[144:147], v[168:171]\n"
"ds_write_b64 v8, v[30:31] offset:34824\n"
"v_mfma_f32_16x16x32_bf16 v[168:171], a[68:71], a[148:151], v[168:171]\n"
"global_load_dwordx4 a[60:63], v0, s[34:35] offset:3072 sc1\n"
"ds_read_b128 v[36:39], v2 offset:34816\n"
"v_mfma_f32_16x16x32_bf16 v[196:199], a[76:79], a[148:151], v[196:199]\n"
"s_waitcnt lgkmcnt(3)\n"
"ds_read_b128 v[64:67], v2 offset:35840\n"
"v_lshlrev_b32 v12, 16, v16\n"
"v_mfma_f32_16x16x32_bf16 v[196:199], a[72:75], a[144:147], v[196:199]\n"
"v_and_b32 v13, s63, v16\n"
"v_lshlrev_b32 v14, 16, v17\n"
"v_mfma_f32_16x16x32_bf16 a[20:23], a[72:75], a[152:155], a[20:23]\n"
"v_and_b32 v15, s63, v17\n"
"v_xor_b32 v20, 64, v7\n"
"v_mfma_f32_16x16x32_bf16 a[20:23], a[76:79], a[156:159], a[20:23]\n"
"ds_read_b128 v[20:23], v20 offset:36864\n"
"v_mul_f32 v12, v12, v40\n"
"v_mfma_f32_16x16x32_bf16 a[16:19], a[68:71], a[156:159], a[16:19]\n"
"v_mul_f32 v13, v13, v41\n"
"v_mul_f32 v14, v14, v42\n"
"v_mfma_f32_16x16x32_bf16 a[16:19], a[64:67], a[152:155], a[16:19]\n"
"s_waitcnt vmcnt(12)\n"
"global_load_dwordx4 v[92:95], v0, s[96:97] offset:-2048 sc1\n"
"v_mul_f32 v15, v15, v43\n"
"v_mfma_f32_16x16x32_bf16 v[200:203], a[80:83], a[96:99], v[200:203]\n"
"s_add_u32 s34, s36, 1482752\n"
"v_cvt_pk_bf16_f32 v24, v12, v13\n"
"v_mfma_f32_16x16x32_bf16 v[200:203], a[84:87], a[100:103], v[200:203]\n"
"global_load_dwordx4 a[64:67], v0, s[34:35] offset:0 sc1\n"
"v_cvt_pk_bf16_f32 v25, v14, v15\n"
"v_mfma_f32_16x16x32_bf16 v[228:231], a[92:95], a[100:103], v[228:231]\n"
"ds_read_b128 a[100:103], v3 offset:1024\n"
"v_mfma_f32_16x16x32_bf16 v[228:231], a[88:91], a[96:99], v[228:231]\n"
"ds_read_b128 a[96:99], v3 offset:0\n"
"v_mfma_f32_16x16x32_bf16 v[232:235], a[88:91], a[104:107], v[232:235]\n"
"ds_write_b64 v7, v[24:25] offset:36864\n"
"v_mfma_f32_16x16x32_bf16 v[232:235], a[92:95], a[108:111], v[232:235]\n"
"v_lshlrev_b32 v12, 16, v18\n"
"v_and_b32 v13, s63, v18\n"
"v_mfma_f32_16x16x32_bf16 v[204:207], a[84:87], a[108:111], v[204:207]\n"
"ds_read_b128 a[108:111], v3 offset:3072\n"
"v_lshlrev_b32 v14, 16, v19\n"
"v_mfma_f32_16x16x32_bf16 v[204:207], a[80:83], a[104:107], v[204:207]\n"
"ds_read_b128 a[104:107], v3 offset:2048\n"
"v_and_b32 v15, s63, v19\n"
"v_mfma_f32_16x16x32_bf16 v[208:211], a[80:83], a[112:115], v[208:211]\n"
"s_waitcnt lgkmcnt(7)\n"
"buffer_store_dwordx4 v[36:39], v6, s[52:55], s82 offen offset:2048 nt\n"
"global_load_dwordx4 v[120:123], v0, s[96:97] offset:-1024 sc1\n"
"v_mul_f32 v12, v12, v68\n"
"v_mul_f32 v13, v13, v69\n"
"v_mul_f32 v14, v14, v70\n"
"v_mfma_f32_16x16x32_bf16 v[208:211], a[84:87], a[116:119], v[208:211]\n"
"global_load_dwordx4 a[68:71], v0, s[34:35] offset:1024 sc1\n"
"v_mul_f32 v15, v15, v71\n"
"v_mfma_f32_16x16x32_bf16 v[236:239], a[92:95], a[116:119], v[236:239]\n"
"ds_read_b128 a[116:119], v3 offset:5120\n"
"v_cvt_pk_bf16_f32 v26, v12, v13\n"
"v_mfma_f32_16x16x32_bf16 v[236:239], a[88:91], a[112:115], v[236:239]\n"
"ds_read_b128 a[112:115], v3 offset:4096\n"
"v_cvt_pk_bf16_f32 v27, v14, v15\n"
"v_mfma_f32_16x16x32_bf16 v[240:243], a[88:91], a[120:123], v[240:243]\n"
"ds_write_b64 v7, v[26:27] offset:36872\n"
"v_mfma_f32_16x16x32_bf16 v[240:243], a[92:95], a[124:127], v[240:243]\n"
"s_waitcnt lgkmcnt(8)\n"
"v_lshlrev_b32 v12, 16, v20\n"
"v_and_b32 v13, s63, v20\n"
"v_mfma_f32_16x16x32_bf16 v[212:215], a[84:87], a[124:127], v[212:215]\n"
"ds_read_b128 a[124:127], v3 offset:7168\n"
"v_lshlrev_b32 v14, 16, v21\n"
"v_mfma_f32_16x16x32_bf16 v[212:215], a[80:83], a[120:123], v[212:215]\n"
"ds_read_b128 a[120:123], v3 offset:6144\n"
"v_and_b32 v15, s63, v21\n"
"v_mfma_f32_16x16x32_bf16 v[216:219], a[80:83], a[128:131], v[216:219]\n"
"ds_read_b128 v[16:19], v7 offset:38912\n"
"v_mul_f32 v12, v12, v96\n"
"v_mfma_f32_16x16x32_bf16 v[216:219], a[84:87], a[132:135], v[216:219]\n"
"global_load_dwordx4 a[72:75], v0, s[34:35] offset:2048 sc1\n"
"v_mul_f32 v13, v13, v97\n"
"v_mfma_f32_16x16x32_bf16 v[244:247], a[92:95], a[132:135], v[244:247]\n"
"ds_read_b128 a[132:135], v3 offset:9216\n"
"v_mul_f32 v14, v14, v98\n"
"v_mfma_f32_16x16x32_bf16 v[244:247], a[88:91], a[128:131], v[244:247]\n"
"ds_read_b128 a[128:131], v3 offset:8192\n"
"v_mul_f32 v15, v15, v99\n"
"v_mfma_f32_16x16x32_bf16 v[248:251], a[88:91], a[136:139], v[248:251]\n"
"buffer_store_dwordx4 v[64:67], v6, s[52:55], s83 offen offset:3072 nt\n"
"global_load_dwordx4 v[32:35], v0, s[96:97] offset:0 sc1\n"
"v_cvt_pk_bf16_f32 v28, v12, v13\n"
"v_cvt_pk_bf16_f32 v29, v14, v15\n"
"v_mfma_f32_16x16x32_bf16 v[248:251], a[92:95], a[140:143], v[248:251]\n"
"ds_write_b64 v8, v[28:29] offset:36864\n"
"v_mfma_f32_16x16x32_bf16 v[220:223], a[84:87], a[140:143], v[220:223]\n"
"ds_read_b128 a[140:143], v3 offset:11264\n"
"v_lshlrev_b32 v12, 16, v22\n"
"v_mfma_f32_16x16x32_bf16 v[220:223], a[80:83], a[136:139], v[220:223]\n"
"ds_read_b128 a[136:139], v3 offset:10240\n"
"v_and_b32 v13, s63, v22\n"
"v_mfma_f32_16x16x32_bf16 v[224:227], a[80:83], a[144:147], v[224:227]\n"
"v_lshlrev_b32 v14, 16, v23\n"
"v_and_b32 v15, s63, v23\n"
"v_mfma_f32_16x16x32_bf16 v[224:227], a[84:87], a[148:151], v[224:227]\n"
"global_load_dwordx4 a[76:79], v0, s[34:35] offset:3072 sc1\n"
"v_mul_f32 v12, v12, v124\n"
"v_mfma_f32_16x16x32_bf16 v[252:255], a[92:95], a[148:151], v[252:255]\n"
"ds_read_b128 a[148:151], v3 offset:13312\n"
"v_mul_f32 v13, v13, v125\n"
"v_mfma_f32_16x16x32_bf16 v[252:255], a[88:91], a[144:147], v[252:255]\n"
"ds_read_b128 a[144:147], v3 offset:12288\n"
"v_mul_f32 v14, v14, v126\n"
"v_mfma_f32_16x16x32_bf16 a[28:31], a[88:91], a[152:155], a[28:31]\n"
"v_mul_f32 v15, v15, v127\n"
"v_cvt_pk_bf16_f32 v30, v12, v13\n"
"v_mfma_f32_16x16x32_bf16 a[28:31], a[92:95], a[156:159], a[28:31]\n"
"global_load_dwordx4 v[60:63], v0, s[96:97] offset:1024 sc1\n"
"v_cvt_pk_bf16_f32 v31, v14, v15\n"
"v_mfma_f32_16x16x32_bf16 a[24:27], a[84:87], a[156:159], a[24:27]\n"
"ds_read_b128 a[156:159], v3 offset:15360\n"
"v_mfma_f32_16x16x32_bf16 a[24:27], a[80:83], a[152:155], a[24:27]\n"
"s_waitcnt vmcnt(17) lgkmcnt(14)\n"
"ds_read_b128 a[152:155], v3 offset:14336\n"
"v_mfma_f32_16x16x32_bf16 v[144:147], a[32:35], a[96:99], v[144:147]\n"
"s_add_u32 s34, s36, 1486848\n"
"ds_write_b64 v8, v[30:31] offset:36872\n"
"v_mfma_f32_16x16x32_bf16 v[144:147], a[36:39], a[100:103], v[144:147]\n"
"global_load_dwordx4 a[80:83], v0, s[34:35] offset:0 sc1\n"
"ds_read_b128 v[40:43], v2 offset:36864\n"
"v_mfma_f32_16x16x32_bf16 v[172:175], a[44:47], a[100:103], v[172:175]\n"
"s_waitcnt lgkmcnt(11)\n"
"ds_read_b128 v[68:71], v2 offset:37888\n"
"v_lshlrev_b32 v12, 16, v16\n"
"v_mfma_f32_16x16x32_bf16 v[172:175], a[40:43], a[96:99], v[172:175]\n"
"v_and_b32 v13, s63, v16\n"
"v_lshlrev_b32 v14, 16, v17\n"
"v_mfma_f32_16x16x32_bf16 v[176:179], a[40:43], a[104:107], v[176:179]\n"
"v_and_b32 v15, s63, v17\n"
"v_xor_b32 v20, 64, v7\n"
"v_mfma_f32_16x16x32_bf16 v[176:179], a[44:47], a[108:111], v[176:179]\n"
"ds_read_b128 v[20:23], v20 offset:38912\n"
"v_mul_f32 v12, v12, v44\n"
"v_mfma_f32_16x16x32_bf16 v[148:151], a[36:39], a[108:111], v[148:151]\n"
"v_mul_f32 v13, v13, v45\n"
"v_mul_f32 v14, v14, v46\n"
"v_mfma_f32_16x16x32_bf16 v[148:151], a[32:35], a[104:107], v[148:151]\n"
"v_mul_f32 v15, v15, v47\n"
"v_cvt_pk_bf16_f32 v24, v12, v13\n"
"v_mfma_f32_16x16x32_bf16 v[152:155], a[32:35], a[112:115], v[152:155]\n"
"v_cvt_pk_bf16_f32 v25, v14, v15\n"
"v_mfma_f32_16x16x32_bf16 v[152:155], a[36:39], a[116:119], v[152:155]\n"
"global_load_dwordx4 a[84:87], v0, s[34:35] offset:1024 sc1\n"
"v_mfma_f32_16x16x32_bf16 v[180:183], a[44:47], a[116:119], v[180:183]\n"
"global_load_dwordx4 v[96:99], v0, s[96:97] offset:2048 sc1\n"
"v_mfma_f32_16x16x32_bf16 v[180:183], a[40:43], a[112:115], v[180:183]\n"
"ds_write_b64 v7, v[24:25] offset:38912\n"
"v_mfma_f32_16x16x32_bf16 v[184:187], a[40:43], a[120:123], v[184:187]\n"
"v_lshlrev_b32 v12, 16, v18\n"
"v_and_b32 v13, s63, v18\n"
"v_mfma_f32_16x16x32_bf16 v[184:187], a[44:47], a[124:127], v[184:187]\n"
"v_lshlrev_b32 v14, 16, v19\n"
"v_and_b32 v15, s63, v19\n"
"v_mfma_f32_16x16x32_bf16 v[156:159], a[36:39], a[124:127], v[156:159]\n"
"v_mul_f32 v12, v12, v72\n"
"v_mul_f32 v13, v13, v73\n"
"v_mfma_f32_16x16x32_bf16 v[156:159], a[32:35], a[120:123], v[156:159]\n"
"s_waitcnt lgkmcnt(9)\n"
"v_mul_f32 v14, v14, v74\n"
"v_mul_f32 v15, v15, v75\n"
"v_mfma_f32_16x16x32_bf16 v[160:163], a[32:35], a[128:131], v[160:163]\n"
"v_cvt_pk_bf16_f32 v26, v12, v13\n"
"v_cvt_pk_bf16_f32 v27, v14, v15\n"
"v_mfma_f32_16x16x32_bf16 v[160:163], a[36:39], a[132:135], v[160:163]\n"
"global_load_dwordx4 a[88:91], v0, s[34:35] offset:2048 sc1\n"
"v_mfma_f32_16x16x32_bf16 v[188:191], a[44:47], a[132:135], v[188:191]\n"
"s_waitcnt lgkmcnt(3)\n"
"buffer_store_dwordx4 v[40:43], v6, s[52:55], s84 offen offset:0 nt\n"
"ds_write_b64 v7, v[26:27] offset:38920\n"
"v_mfma_f32_16x16x32_bf16 v[188:191], a[40:43], a[128:131], v[188:191]\n"
"s_waitcnt lgkmcnt(2)\n"
"global_load_dwordx4 v[124:127], v0, s[96:97] offset:3072 sc1\n"
"v_lshlrev_b32 v12, 16, v20\n"
"v_mfma_f32_16x16x32_bf16 v[192:195], a[40:43], a[136:139], v[192:195]\n"
"v_and_b32 v13, s63, v20\n"
"v_lshlrev_b32 v14, 16, v21\n"
"v_mfma_f32_16x16x32_bf16 v[192:195], a[44:47], a[140:143], v[192:195]\n"
"v_and_b32 v15, s63, v21\n"
"v_mul_f32 v12, v12, v100\n"
"v_mfma_f32_16x16x32_bf16 v[164:167], a[36:39], a[140:143], v[164:167]\n"
"v_mul_f32 v13, v13, v101\n"
"v_mul_f32 v14, v14, v102\n"
"v_mfma_f32_16x16x32_bf16 v[164:167], a[32:35], a[136:139], v[164:167]\n"
"v_mul_f32 v15, v15, v103\n"
"v_cvt_pk_bf16_f32 v28, v12, v13\n"
"v_mfma_f32_16x16x32_bf16 v[168:171], a[32:35], a[144:147], v[168:171]\n"
"v_cvt_pk_bf16_f32 v29, v14, v15\n"
"v_mfma_f32_16x16x32_bf16 v[168:171], a[36:39], a[148:151], v[168:171]\n"
"global_load_dwordx4 a[92:95], v0, s[34:35] offset:3072 sc1\n"
"v_mfma_f32_16x16x32_bf16 v[196:199], a[44:47], a[148:151], v[196:199]\n"
"ds_write_b64 v8, v[28:29] offset:38912\n"
"v_mfma_f32_16x16x32_bf16 v[196:199], a[40:43], a[144:147], v[196:199]\n"
"v_lshlrev_b32 v12, 16, v22\n"
"v_and_b32 v13, s63, v22\n"
"v_mfma_f32_16x16x32_bf16 a[20:23], a[40:43], a[152:155], a[20:23]\n"
"v_lshlrev_b32 v14, 16, v23\n"
"v_and_b32 v15, s63, v23\n"
"v_mfma_f32_16x16x32_bf16 a[20:23], a[44:47], a[156:159], a[20:23]\n"
"v_mul_f32 v12, v12, v128\n"
"v_mfma_f32_16x16x32_bf16 a[16:19], a[36:39], a[156:159], a[16:19]\n"
"buffer_store_dwordx4 v[68:71], v6, s[52:55], s85 offen offset:1024 nt\n"
"v_mul_f32 v13, v13, v129\n"
"v_mul_f32 v14, v14, v130\n"
"v_mul_f32 v15, v15, v131\n"
"v_mfma_f32_16x16x32_bf16 a[16:19], a[32:35], a[152:155], a[16:19]\n"
"s_waitcnt vmcnt(18)\n"
"v_cvt_pk_bf16_f32 v30, v12, v13\n"
"v_cvt_pk_bf16_f32 v31, v14, v15\n"
"v_mfma_f32_16x16x32_bf16 v[200:203], a[48:51], a[96:99], v[200:203]\n"
"s_add_u32 s34, s36, 1499136\n"
"ds_write_b64 v8, v[30:31] offset:38920\n"
"v_mfma_f32_16x16x32_bf16 v[200:203], a[52:55], a[100:103], v[200:203]\n"
"global_load_dwordx4 a[32:35], v0, s[34:35] offset:0 sc1\n"
"ds_read_b128 v[44:47], v2 offset:38912\n"
"v_mfma_f32_16x16x32_bf16 v[228:231], a[60:63], a[100:103], v[228:231]\n"
"ds_read_b128 a[100:103], v4 offset:1024\n"
"v_accvgpr_read_b32 v41, a232\n"
"v_mfma_f32_16x16x32_bf16 v[228:231], a[56:59], a[96:99], v[228:231]\n"
"ds_read_b128 a[96:99], v4 offset:0\n"
"v_accvgpr_read_b32 v43, a233\n"
"v_mfma_f32_16x16x32_bf16 v[232:235], a[56:59], a[104:107], v[232:235]\n"
"ds_read_b128 v[72:75], v2 offset:39936\n"
"v_accvgpr_read_b32 v16, a160\n"
"v_mfma_f32_16x16x32_bf16 v[232:235], a[60:63], a[108:111], v[232:235]\n"
"v_accvgpr_read_b32 v17, a161\n"
"v_accvgpr_read_b32 v18, a162\n"
"v_mfma_f32_16x16x32_bf16 v[204:207], a[52:55], a[108:111], v[204:207]\n"
"ds_read_b128 a[108:111], v4 offset:3072\n"
"v_accvgpr_read_b32 v19, a163\n"
"v_mfma_f32_16x16x32_bf16 v[204:207], a[48:51], a[104:107], v[204:207]\n"
"ds_read_b128 a[104:107], v4 offset:2048\n"
"v_lshlrev_b32 v12, 16, v16\n"
"v_mfma_f32_16x16x32_bf16 v[208:211], a[48:51], a[112:115], v[208:211]\n"
"v_and_b32 v13, s63, v16\n"
"v_lshlrev_b32 v14, 16, v17\n"
"v_mfma_f32_16x16x32_bf16 v[208:211], a[52:55], a[116:119], v[208:211]\n"
"global_load_dwordx4 a[36:39], v0, s[34:35] offset:1024 sc1\n"
"v_and_b32 v15, s63, v17\n"
"v_mfma_f32_16x16x32_bf16 v[236:239], a[60:63], a[116:119], v[236:239]\n"
"ds_read_b128 a[116:119], v4 offset:5120\n"
"v_mul_f32 v12, v12, v48\n"
"v_mfma_f32_16x16x32_bf16 v[236:239], a[56:59], a[112:115], v[236:239]\n"
"ds_read_b128 a[112:115], v4 offset:4096\n"
"v_mul_f32 v13, v13, v49\n"
"v_mfma_f32_16x16x32_bf16 v[240:243], a[56:59], a[120:123], v[240:243]\n"
"v_mul_f32 v14, v14, v50\n"
"v_mul_f32 v15, v15, v51\n"
"v_mfma_f32_16x16x32_bf16 v[240:243], a[60:63], a[124:127], v[240:243]\n"
"v_cvt_pk_bf16_f32 v24, v12, v13\n"
"v_lshlrev_b32 v40, 16, v41\n"
"v_mfma_f32_16x16x32_bf16 v[212:215], a[52:55], a[124:127], v[212:215]\n"
"ds_read_b128 a[124:127], v4 offset:7168\n"
"v_cvt_pk_bf16_f32 v25, v14, v15\n"
"v_mfma_f32_16x16x32_bf16 v[212:215], a[48:51], a[120:123], v[212:215]\n"
"ds_read_b128 a[120:123], v4 offset:6144\n"
"v_lshlrev_b32 v42, 16, v43\n"
"v_mfma_f32_16x16x32_bf16 v[216:219], a[48:51], a[128:131], v[216:219]\n"
"ds_write_b64 v7, v[24:25] offset:32768\n"
"v_mfma_f32_16x16x32_bf16 v[216:219], a[52:55], a[132:135], v[216:219]\n"
"global_load_dwordx4 a[40:43], v0, s[34:35] offset:2048 sc1\n"
"v_lshlrev_b32 v12, 16, v18\n"
"v_mfma_f32_16x16x32_bf16 v[244:247], a[60:63], a[132:135], v[244:247]\n"
"ds_read_b128 a[132:135], v4 offset:9216\n"
"v_and_b32 v13, s63, v18\n"
"v_mfma_f32_16x16x32_bf16 v[244:247], a[56:59], a[128:131], v[244:247]\n"
"ds_read_b128 a[128:131], v4 offset:8192\n"
"v_lshlrev_b32 v14, 16, v19\n"
"v_mfma_f32_16x16x32_bf16 v[248:251], a[56:59], a[136:139], v[248:251]\n"
"s_add_u32 s96, s36, 1560576\n"
"v_and_b32 v15, s63, v19\n"
"v_mul_f32 v12, v12, v76\n"
"v_mfma_f32_16x16x32_bf16 v[248:251], a[60:63], a[140:143], v[248:251]\n"
"global_load_dwordx4 v[100:103], v0, s[96:97] offset:-4096 sc1\n"
"v_mul_f32 v13, v13, v77\n"
"v_mfma_f32_16x16x32_bf16 v[220:223], a[52:55], a[140:143], v[220:223]\n"
"ds_read_b128 a[140:143], v4 offset:11264\n"
"v_mul_f32 v14, v14, v78\n"
"v_mfma_f32_16x16x32_bf16 v[220:223], a[48:51], a[136:139], v[220:223]\n"
"ds_read_b128 a[136:139], v4 offset:10240\n"
"v_mul_f32 v15, v15, v79\n"
"v_mfma_f32_16x16x32_bf16 v[224:227], a[48:51], a[144:147], v[224:227]\n"
"v_cvt_pk_bf16_f32 v26, v12, v13\n"
"v_cvt_pk_bf16_f32 v27, v14, v15\n"
"v_mfma_f32_16x16x32_bf16 v[224:227], a[52:55], a[148:151], v[224:227]\n"
"global_load_dwordx4 a[44:47], v0, s[34:35] offset:3072 sc1\n"
"v_and_b32 v41, s63, v41\n"
"v_mfma_f32_16x16x32_bf16 v[252:255], a[60:63], a[148:151], v[252:255]\n"
"ds_read_b128 a[148:151], v4 offset:13312\n"
"v_and_b32 v43, s63, v43\n"
"v_mfma_f32_16x16x32_bf16 v[252:255], a[56:59], a[144:147], v[252:255]\n"
"ds_read_b128 a[144:147], v4 offset:12288\n"
"v_accvgpr_read_b32 v69, a234\n"
"v_mfma_f32_16x16x32_bf16 a[28:31], a[56:59], a[152:155], a[28:31]\n"
"s_waitcnt lgkmcnt(15)\n"
"buffer_store_dwordx4 v[44:47], v6, s[52:55], s86 offen offset:2048 nt\n"
"ds_write_b64 v7, v[26:27] offset:32776\n"
"v_mfma_f32_16x16x32_bf16 a[28:31], a[60:63], a[156:159], a[28:31]\n"
"v_accvgpr_read_b32 v16, a164\n"
"v_accvgpr_read_b32 v17, a165\n"
"v_mfma_f32_16x16x32_bf16 a[24:27], a[52:55], a[156:159], a[24:27]\n"
"ds_read_b128 a[156:159], v4 offset:15360\n"
"v_accvgpr_read_b32 v18, a166\n"
"v_mfma_f32_16x16x32_bf16 a[24:27], a[48:51], a[152:155], a[24:27]\n"
"s_waitcnt vmcnt(15) lgkmcnt(13)\n"
"ds_read_b128 a[152:155], v4 offset:14336\n"
"v_accvgpr_read_b32 v19, a167\n"
"v_mfma_f32_16x16x32_bf16 v[144:147], a[64:67], a[96:99], v[144:147]\n"
"s_add_u32 s34, s36, 1503232\n"
"global_load_dwordx4 v[128:131], v0, s[96:97] offset:-3072 sc1\n"
"v_mfma_f32_16x16x32_bf16 v[144:147], a[68:71], a[100:103], v[144:147]\n"
"global_load_dwordx4 a[48:51], v0, s[34:35] offset:0 sc1\n"
"v_lshlrev_b32 v12, 16, v16\n"
"v_mfma_f32_16x16x32_bf16 v[172:175], a[76:79], a[100:103], v[172:175]\n"
"v_and_b32 v13, s63, v16\n"
"v_lshlrev_b32 v14, 16, v17\n"
"v_mfma_f32_16x16x32_bf16 v[172:175], a[72:75], a[96:99], v[172:175]\n"
"v_and_b32 v15, s63, v17\n"
"v_mul_f32 v12, v12, v104\n"
"v_mfma_f32_16x16x32_bf16 v[176:179], a[72:75], a[104:107], v[176:179]\n"
"v_mul_f32 v13, v13, v105\n"
"v_mul_f32 v14, v14, v106\n"
"v_mfma_f32_16x16x32_bf16 v[176:179], a[76:79], a[108:111], v[176:179]\n"
"v_mul_f32 v15, v15, v107\n"
"v_accvgpr_read_b32 v71, a235\n"
"v_mfma_f32_16x16x32_bf16 v[148:151], a[68:71], a[108:111], v[148:151]\n"
"v_cvt_pk_bf16_f32 v28, v12, v13\n"
"v_lshlrev_b32 v68, 16, v69\n"
"v_mfma_f32_16x16x32_bf16 v[148:151], a[64:67], a[104:107], v[148:151]\n"
"s_waitcnt lgkmcnt(10)\n"
"v_cvt_pk_bf16_f32 v29, v14, v15\n"
"v_lshlrev_b32 v70, 16, v71\n"
"v_mfma_f32_16x16x32_bf16 v[152:155], a[64:67], a[112:115], v[152:155]\n"
"buffer_store_dwordx4 v[72:75], v6, s[52:55], s87 offen offset:3072 nt\n"
"ds_write_b64 v8, v[28:29] offset:32768\n"
"v_mfma_f32_16x16x32_bf16 v[152:155], a[68:71], a[116:119], v[152:155]\n"
"global_load_dwordx4 a[52:55], v0, s[34:35] offset:1024 sc1\n"
"v_lshlrev_b32 v12, 16, v18\n"
"v_mfma_f32_16x16x32_bf16 v[180:183], a[76:79], a[116:119], v[180:183]\n"
"global_load_dwordx4 v[36:39], v0, s[96:97] offset:-2048 sc1\n"
"v_and_b32 v13, s63, v18\n"
"v_mfma_f32_16x16x32_bf16 v[180:183], a[72:75], a[112:115], v[180:183]\n"
"v_lshlrev_b32 v14, 16, v19\n"
"v_and_b32 v15, s63, v19\n"
"v_mfma_f32_16x16x32_bf16 v[184:187], a[72:75], a[120:123], v[184:187]\n"
"v_mul_f32 v12, v12, v132\n"
"v_mul_f32 v13, v13, v133\n"
"v_mfma_f32_16x16x32_bf16 v[184:187], a[76:79], a[124:127], v[184:187]\n"
"v_mul_f32 v14, v14, v134\n"
"v_mul_f32 v15, v15, v135\n"
"v_accvgpr_read_b32 v45, a236\n"
"v_mfma_f32_16x16x32_bf16 v[156:159], a[68:71], a[124:127], v[156:159]\n"
"v_cvt_pk_bf16_f32 v30, v12, v13\n"
"v_cvt_pk_bf16_f32 v31, v14, v15\n"
"v_mfma_f32_16x16x32_bf16 v[156:159], a[64:67], a[120:123], v[156:159]\n"
"s_waitcnt lgkmcnt(6)\n"
"ds_write_b64 v8, v[30:31] offset:32776\n"
"v_mfma_f32_16x16x32_bf16 v[160:163], a[64:67], a[128:131], v[160:163]\n"
"ds_read_b128 v[48:51], v2 offset:32768\n"
"v_and_b32 v69, s63, v69\n"
"v_mfma_f32_16x16x32_bf16 v[160:163], a[68:71], a[132:135], v[160:163]\n"
"global_load_dwordx4 a[56:59], v0, s[34:35] offset:2048 sc1\n"
"ds_read_b128 v[76:79], v2 offset:33792\n"
"v_mfma_f32_16x16x32_bf16 v[188:191], a[76:79], a[132:135], v[188:191]\n"
"v_accvgpr_read_b32 v16, a168\n"
"v_accvgpr_read_b32 v17, a169\n"
"v_accvgpr_read_b32 v47, a237\n"
"v_mfma_f32_16x16x32_bf16 v[188:191], a[72:75], a[128:131], v[188:191]\n"
"v_accvgpr_read_b32 v18, a170\n"
"v_accvgpr_read_b32 v19, a171\n"
"v_mfma_f32_16x16x32_bf16 v[192:195], a[72:75], a[136:139], v[192:195]\n"
"global_load_dwordx4 v[64:67], v0, s[96:97] offset:-1024 sc1\n"
"v_lshlrev_b32 v12, 16, v16\n"
"v_mfma_f32_16x16x32_bf16 v[192:195], a[76:79], a[140:143], v[192:195]\n"
"v_and_b32 v13, s63, v16\n"
"v_lshlrev_b32 v14, 16, v17\n"
"v_mfma_f32_16x16x32_bf16 v[164:167], a[68:71], a[140:143], v[164:167]\n"
"v_and_b32 v15, s63, v17\n"
"v_mul_f32 v12, v12, v52\n"
"v_mfma_f32_16x16x32_bf16 v[164:167], a[64:67], a[136:139], v[164:167]\n"
"s_waitcnt lgkmcnt(4)\n"
"v_mul_f32 v13, v13, v53\n"
"v_mul_f32 v14, v14, v54\n"
"v_lshlrev_b32 v44, 16, v45\n"
"v_mfma_f32_16x16x32_bf16 v[168:171], a[64:67], a[144:147], v[168:171]\n"
"v_mul_f32 v15, v15, v55\n"
"v_cvt_pk_bf16_f32 v24, v12, v13\n"
"v_mfma_f32_16x16x32_bf16 v[168:171], a[68:71], a[148:151], v[168:171]\n"
"global_load_dwordx4 a[60:63], v0, s[34:35] offset:3072 sc1\n"
"v_cvt_pk_bf16_f32 v25, v14, v15\n"
"v_mfma_f32_16x16x32_bf16 v[196:199], a[76:79], a[148:151], v[196:199]\n"
"ds_write_b64 v7, v[24:25] offset:34816\n"
"v_mfma_f32_16x16x32_bf16 v[196:199], a[72:75], a[144:147], v[196:199]\n"
"v_lshlrev_b32 v12, 16, v18\n"
"v_and_b32 v13, s63, v18\n"
"v_mfma_f32_16x16x32_bf16 a[20:23], a[72:75], a[152:155], a[20:23]\n"
"v_lshlrev_b32 v14, 16, v19\n"
"v_and_b32 v15, s63, v19\n"
"v_mfma_f32_16x16x32_bf16 a[20:23], a[76:79], a[156:159], a[20:23]\n"
"v_mul_f32 v12, v12, v80\n"
"v_and_b32 v71, s63, v71\n"
"v_mfma_f32_16x16x32_bf16 a[16:19], a[68:71], a[156:159], a[16:19]\n"
"global_load_dwordx4 v[104:107], v0, s[96:97] offset:0 sc1\n"
"v_mul_f32 v13, v13, v81\n"
"v_mfma_f32_16x16x32_bf16 a[16:19], a[64:67], a[152:155], a[16:19]\n"
"s_waitcnt vmcnt(16)\n"
"v_mul_f32 v14, v14, v82\n"
"v_mul_f32 v15, v15, v83\n"
"v_mfma_f32_16x16x32_bf16 v[200:203], a[80:83], a[96:99], v[200:203]\n"
"s_add_u32 s34, s36, 1548288\n"
"v_cvt_pk_bf16_f32 v26, v12, v13\n"
"v_lshlrev_b32 v46, 16, v47\n"
"v_mfma_f32_16x16x32_bf16 v[200:203], a[84:87], a[100:103], v[200:203]\n"
"global_load_dwordx4 a[64:67], v0, s[34:35] offset:0 sc1\n"
"v_cvt_pk_bf16_f32 v27, v14, v15\n"
"v_mfma_f32_16x16x32_bf16 v[228:231], a[92:95], a[100:103], v[228:231]\n"
"ds_read_b128 a[100:103], v5 offset:1024\n"
"v_and_b32 v45, s63, v45\n"
"v_mfma_f32_16x16x32_bf16 v[228:231], a[88:91], a[96:99], v[228:231]\n"
"ds_read_b128 a[96:99], v5 offset:0\n"
"v_and_b32 v47, s63, v47\n"
"v_mfma_f32_16x16x32_bf16 v[232:235], a[88:91], a[104:107], v[232:235]\n"
"ds_write_b64 v7, v[26:27] offset:34824\n"
"v_mfma_f32_16x16x32_bf16 v[232:235], a[92:95], a[108:111], v[232:235]\n"
"v_accvgpr_read_b32 v16, a172\n"
"v_accvgpr_read_b32 v17, a173\n"
"v_mfma_f32_16x16x32_bf16 v[204:207], a[84:87], a[108:111], v[204:207]\n"
"s_waitcnt lgkmcnt(5)\n"
"buffer_store_dwordx4 v[48:51], v6, s[52:55], s88 offen offset:0 nt\n"
"ds_read_b128 a[108:111], v5 offset:3072\n"
"v_accvgpr_read_b32 v18, a174\n"
"v_accvgpr_read_b32 v19, a175\n"
"v_accvgpr_read_b32 v73, a238\n"
"v_mfma_f32_16x16x32_bf16 v[204:207], a[80:83], a[104:107], v[204:207]\n"
"ds_read_b128 a[104:107], v5 offset:2048\n"
"v_lshlrev_b32 v12, 16, v16\n"
"v_mfma_f32_16x16x32_bf16 v[208:211], a[80:83], a[112:115], v[208:211]\n"
"global_load_dwordx4 v[132:135], v0, s[96:97] offset:1024 sc1\n"
"v_and_b32 v13, s63, v16\n"
"v_mfma_f32_16x16x32_bf16 v[208:211], a[84:87], a[116:119], v[208:211]\n"
"global_load_dwordx4 a[68:71], v0, s[34:35] offset:1024 sc1\n"
"v_lshlrev_b32 v14, 16, v17\n"
"v_mfma_f32_16x16x32_bf16 v[236:239], a[92:95], a[116:119], v[236:239]\n"
"ds_read_b128 a[116:119], v5 offset:5120\n"
"v_and_b32 v15, s63, v17\n"
"v_mfma_f32_16x16x32_bf16 v[236:239], a[88:91], a[112:115], v[236:239]\n"
"ds_read_b128 a[112:115], v5 offset:4096\n"
"v_mul_f32 v12, v12, v108\n"
"v_mfma_f32_16x16x32_bf16 v[240:243], a[88:91], a[120:123], v[240:243]\n"
"v_mul_f32 v13, v13, v109\n"
"v_mul_f32 v14, v14, v110\n"
"v_mfma_f32_16x16x32_bf16 v[240:243], a[92:95], a[124:127], v[240:243]\n"
"v_mul_f32 v15, v15, v111\n"
"v_cvt_pk_bf16_f32 v28, v12, v13\n"
"v_mfma_f32_16x16x32_bf16 v[212:215], a[84:87], a[124:127], v[212:215]\n"
"ds_read_b128 a[124:127], v5 offset:7168\n"
"v_cvt_pk_bf16_f32 v29, v14, v15\n"
"v_mfma_f32_16x16x32_bf16 v[212:215], a[80:83], a[120:123], v[212:215]\n"
"ds_read_b128 a[120:123], v5 offset:6144\n"
"v_accvgpr_read_b32 v75, a239\n"
"v_mfma_f32_16x16x32_bf16 v[216:219], a[80:83], a[128:131], v[216:219]\n"
"ds_write_b64 v8, v[28:29] offset:34816\n"
"v_mfma_f32_16x16x32_bf16 v[216:219], a[84:87], a[132:135], v[216:219]\n"
"global_load_dwordx4 a[72:75], v0, s[34:35] offset:2048 sc1\n"
"v_lshlrev_b32 v12, 16, v18\n"
"v_mfma_f32_16x16x32_bf16 v[244:247], a[92:95], a[132:135], v[244:247]\n"
"s_waitcnt lgkmcnt(11)\n"
"buffer_store_dwordx4 v[76:79], v6, s[52:55], s89 offen offset:1024 nt\n"
"ds_read_b128 a[132:135], v5 offset:9216\n"
"v_and_b32 v13, s63, v18\n"
"v_lshlrev_b32 v14, 16, v19\n"
"v_lshlrev_b32 v72, 16, v73\n"
"v_mfma_f32_16x16x32_bf16 v[244:247], a[88:91], a[128:131], v[244:247]\n"
"ds_read_b128 a[128:131], v5 offset:8192\n"
"v_and_b32 v15, s63, v19\n"
"v_mfma_f32_16x16x32_bf16 v[248:251], a[88:91], a[136:139], v[248:251]\n"
"v_mul_f32 v12, v12, v136\n"
"v_mul_f32 v13, v13, v137\n"
"v_mfma_f32_16x16x32_bf16 v[248:251], a[92:95], a[140:143], v[248:251]\n"
"v_mul_f32 v14, v14, v138\n"
"v_mul_f32 v15, v15, v139\n"
"v_mfma_f32_16x16x32_bf16 v[220:223], a[84:87], a[140:143], v[220:223]\n"
"ds_read_b128 a[140:143], v5 offset:11264\n"
"v_cvt_pk_bf16_f32 v30, v12, v13\n"
"v_mfma_f32_16x16x32_bf16 v[220:223], a[80:83], a[136:139], v[220:223]\n"
"ds_read_b128 a[136:139], v5 offset:10240\n"
"v_cvt_pk_bf16_f32 v31, v14, v15\n"
"v_mfma_f32_16x16x32_bf16 v[224:227], a[80:83], a[144:147], v[224:227]\n"
"ds_write_b64 v8, v[30:31] offset:34824\n"
"v_mfma_f32_16x16x32_bf16 v[224:227], a[84:87], a[148:151], v[224:227]\n"
"global_load_dwordx4 a[76:79], v0, s[34:35] offset:3072 sc1\n"
"ds_read_b128 v[52:55], v2 offset:34816\n"
"v_mfma_f32_16x16x32_bf16 v[252:255], a[92:95], a[148:151], v[252:255]\n"
"ds_read_b128 a[148:151], v5 offset:13312\n"
"v_lshlrev_b32 v74, 16, v75\n"
"v_mfma_f32_16x16x32_bf16 v[252:255], a[88:91], a[144:147], v[252:255]\n"
"ds_read_b128 a[144:147], v5 offset:12288\n"
"v_and_b32 v73, s63, v73\n"
"v_mfma_f32_16x16x32_bf16 a[28:31], a[88:91], a[152:155], a[28:31]\n"
"ds_read_b128 v[80:83], v2 offset:35840\n"
"v_accvgpr_read_b32 v16, a176\n"
"v_mfma_f32_16x16x32_bf16 a[28:31], a[92:95], a[156:159], a[28:31]\n"
"v_accvgpr_read_b32 v17, a177\n"
"v_accvgpr_read_b32 v18, a178\n"
"v_mfma_f32_16x16x32_bf16 a[24:27], a[84:87], a[156:159], a[24:27]\n"
"ds_read_b128 a[156:159], v5 offset:15360\n"
"v_accvgpr_read_b32 v19, a179\n"
"v_mfma_f32_16x16x32_bf16 a[24:27], a[80:83], a[152:155], a[24:27]\n"
"s_waitcnt vmcnt(17) lgkmcnt(15)\n"
"ds_read_b128 a[152:155], v5 offset:14336\n"
"v_lshlrev_b32 v12, 16, v16\n"
"v_mfma_f32_16x16x32_bf16 v[144:147], a[32:35], a[96:99], v[144:147]\n"
"s_add_u32 s34, s36, 1552384\n"
"v_and_b32 v13, s63, v16\n"
"v_lshlrev_b32 v14, 16, v17\n"
"v_mfma_f32_16x16x32_bf16 v[144:147], a[36:39], a[100:103], v[144:147]\n"
"global_load_dwordx4 a[80:83], v0, s[34:35] offset:0 sc1\n"
"v_and_b32 v15, s63, v17\n"
"v_mfma_f32_16x16x32_bf16 v[172:175], a[44:47], a[100:103], v[172:175]\n"
"v_mul_f32 v12, v12, v56\n"
"v_mul_f32 v13, v13, v57\n"
"v_mfma_f32_16x16x32_bf16 v[172:175], a[40:43], a[96:99], v[172:175]\n"
"v_mul_f32 v14, v14, v58\n"
"v_mul_f32 v15, v15, v59\n"
"v_mfma_f32_16x16x32_bf16 v[176:179], a[40:43], a[104:107], v[176:179]\n"
"global_load_dwordx4 a[84:87], v0, s[34:35] offset:1024 sc1\n"
"v_cvt_pk_bf16_f32 v24, v12, v13\n"
"v_mfma_f32_16x16x32_bf16 v[176:179], a[44:47], a[108:111], v[176:179]\n"
"v_cvt_pk_bf16_f32 v25, v14, v15\n"
"v_and_b32 v75, s63, v75\n"
"v_mfma_f32_16x16x32_bf16 v[148:151], a[36:39], a[108:111], v[148:151]\n"
"ds_write_b64 v7, v[24:25] offset:36864\n"
"v_mfma_f32_16x16x32_bf16 v[148:151], a[32:35], a[104:107], v[148:151]\n"
"s_waitcnt lgkmcnt(13)\n"
"global_load_dwordx4 a[88:91], v0, s[34:35] offset:2048 sc1\n"
"v_lshlrev_b32 v12, 16, v18\n"
"v_mfma_f32_16x16x32_bf16 v[152:155], a[32:35], a[112:115], v[152:155]\n"
"global_load_dwordx4 v[108:111], v0, s[96:97] offset:2048 sc1\n"
"v_and_b32 v13, s63, v18\n"
"v_mfma_f32_16x16x32_bf16 v[152:155], a[36:39], a[116:119], v[152:155]\n"
"v_lshlrev_b32 v14, 16, v19\n"
"v_and_b32 v15, s63, v19\n"
"v_mfma_f32_16x16x32_bf16 v[180:183], a[44:47], a[116:119], v[180:183]\n"
"global_load_dwordx4 a[92:95], v0, s[34:35] offset:3072 sc1\n"
"v_mul_f32 v12, v12, v84\n"
"v_mfma_f32_16x16x32_bf16 v[180:183], a[40:43], a[112:115], v[180:183]\n"
"L_st10883:\n"
"s_cbranch_execz L_retC\n"
"v_mul_f32 v13, v13, v85\n"
"v_mul_f32 v14, v14, v86\n"
"v_mfma_f32_16x16x32_bf16 v[184:187], a[40:43], a[120:123], v[184:187]\n"
"v_mul_f32 v15, v15, v87\n"
"v_cvt_pk_bf16_f32 v26, v12, v13\n"
"v_mfma_f32_16x16x32_bf16 v[184:187], a[44:47], a[124:127], v[184:187]\n"
"v_cvt_pk_bf16_f32 v27, v14, v15\n"
"v_accvgpr_read_b32 v49, a240\n"
"v_mfma_f32_16x16x32_bf16 v[156:159], a[36:39], a[124:127], v[156:159]\n"
"ds_write_b64 v7, v[26:27] offset:36872\n"
"v_mfma_f32_16x16x32_bf16 v[156:159], a[32:35], a[120:123], v[156:159]\n"
"s_waitcnt lgkmcnt(9)\n"
"v_accvgpr_read_b32 v16, a180\n"
"v_accvgpr_read_b32 v17, a181\n"
"v_mfma_f32_16x16x32_bf16 v[160:163], a[32:35], a[128:131], v[160:163]\n"
"s_waitcnt lgkmcnt(7)\n"
"buffer_store_dwordx4 v[52:55], v6, s[52:55], s90 offen offset:2048 nt\n"
"v_accvgpr_read_b32 v18, a182\n"
"v_accvgpr_read_b32 v19, a183\n"
"v_lshlrev_b32 v12, 16, v16\n"
"v_accvgpr_read_b32 v51, a241\n"
"v_mfma_f32_16x16x32_bf16 v[160:163], a[36:39], a[132:135], v[160:163]\n"
"global_load_dwordx4 v[136:139], v0, s[96:97] offset:3072 sc1\n"
"v_and_b32 v13, s63, v16\n"
"v_mfma_f32_16x16x32_bf16 v[188:191], a[44:47], a[132:135], v[188:191]\n"
"v_lshlrev_b32 v14, 16, v17\n"
"v_and_b32 v15, s63, v17\n"
"v_mfma_f32_16x16x32_bf16 v[188:191], a[40:43], a[128:131], v[188:191]\n"
"v_mul_f32 v12, v12, v112\n"
"v_mul_f32 v13, v13, v113\n"
"v_mfma_f32_16x16x32_bf16 v[192:195], a[40:43], a[136:139], v[192:195]\n"
"v_mul_f32 v14, v14, v114\n"
"v_mul_f32 v15, v15, v115\n"
"v_mfma_f32_16x16x32_bf16 v[192:195], a[44:47], a[140:143], v[192:195]\n"
"v_cvt_pk_bf16_f32 v28, v12, v13\n"
"v_cvt_pk_bf16_f32 v29, v14, v15\n"
"v_mfma_f32_16x16x32_bf16 v[164:167], a[36:39], a[140:143], v[164:167]\n"
"ds_write_b64 v8, v[28:29] offset:36864\n"
"v_mfma_f32_16x16x32_bf16 v[164:167], a[32:35], a[136:139], v[164:167]\n"
"s_waitcnt lgkmcnt(3)\n"
"v_lshlrev_b32 v12, 16, v18\n"
"v_and_b32 v13, s63, v18\n"
"v_mfma_f32_16x16x32_bf16 v[168:171], a[32:35], a[144:147], v[168:171]\n"
"v_lshlrev_b32 v14, 16, v19\n"
"v_and_b32 v15, s63, v19\n"
"v_mfma_f32_16x16x32_bf16 v[168:171], a[36:39], a[148:151], v[168:171]\n"
"v_mul_f32 v12, v12, v140\n"
"v_mul_f32 v13, v13, v141\n"
"v_mfma_f32_16x16x32_bf16 v[196:199], a[44:47], a[148:151], v[196:199]\n"
"v_mul_f32 v14, v14, v142\n"
"v_mul_f32 v15, v15, v143\n"
"v_mfma_f32_16x16x32_bf16 v[196:199], a[40:43], a[144:147], v[196:199]\n"
"v_cvt_pk_bf16_f32 v30, v12, v13\n"
"v_cvt_pk_bf16_f32 v31, v14, v15\n"
"v_mfma_f32_16x16x32_bf16 a[20:23], a[40:43], a[152:155], a[20:23]\n"
"ds_write_b64 v8, v[30:31] offset:36872\n"
"buffer_store_dwordx4 v[80:83], v6, s[52:55], s91 offen offset:3072 nt\n"
"v_lshlrev_b32 v48, 16, v49\n"
"v_lshlrev_b32 v50, 16, v51\n"
"v_and_b32 v49, s63, v49\n"
"v_and_b32 v51, s63, v51\n"
"v_mfma_f32_16x16x32_bf16 a[20:23], a[44:47], a[156:159], a[20:23]\n"
"ds_read_b128 v[56:59], v2 offset:36864\n"
"v_accvgpr_read_b32 v77, a242\n"
"v_mfma_f32_16x16x32_bf16 a[16:19], a[36:39], a[156:159], a[16:19]\n"
"ds_read_b128 v[84:87], v2 offset:37888\n"
"v_accvgpr_read_b32 v79, a243\n"
"v_mfma_f32_16x16x32_bf16 a[16:19], a[32:35], a[152:155], a[16:19]\n"
"s_waitcnt vmcnt(16)\n"
"v_accvgpr_read_b32 v16, a184\n"
"v_lshlrev_b32 v76, 16, v77\n"
"v_mfma_f32_16x16x32_bf16 v[200:203], a[48:51], a[96:99], v[200:203]\n"
"v_accvgpr_read_b32 v17, a185\n"
"v_lshlrev_b32 v78, 16, v79\n"
"v_mfma_f32_16x16x32_bf16 v[200:203], a[52:55], a[100:103], v[200:203]\n"
"v_accvgpr_read_b32 v18, a186\n"
"v_and_b32 v77, s63, v77\n"
"v_mfma_f32_16x16x32_bf16 v[228:231], a[60:63], a[100:103], v[228:231]\n"
"ds_read_b128 a[100:103], v2 offset:17408\n"
"v_and_b32 v79, s63, v79\n"
"v_mfma_f32_16x16x32_bf16 v[228:231], a[56:59], a[96:99], v[228:231]\n"
"ds_read_b128 a[96:99], v2 offset:16384\n"
"v_accvgpr_read_b32 v19, a187\n"
"v_mfma_f32_16x16x32_bf16 v[232:235], a[56:59], a[104:107], v[232:235]\n"
"v_lshlrev_b32 v12, 16, v16\n"
"v_and_b32 v13, s63, v16\n"
"v_mfma_f32_16x16x32_bf16 v[232:235], a[60:63], a[108:111], v[232:235]\n"
"v_lshlrev_b32 v14, 16, v17\n"
"v_and_b32 v15, s63, v17\n"
"v_mfma_f32_16x16x32_bf16 v[204:207], a[52:55], a[108:111], v[204:207]\n"
"ds_read_b128 a[108:111], v3 offset:17408\n"
"v_accvgpr_read_b32 v10, a0\n"
"v_mfma_f32_16x16x32_bf16 v[204:207], a[48:51], a[104:107], v[204:207]\n"
"ds_read_b128 a[104:107], v3 offset:16384\n"
"v_accvgpr_read_b32 v11, a1\n"
"v_mfma_f32_16x16x32_bf16 v[208:211], a[48:51], a[112:115], v[208:211]\n"
"v_mul_f32 v12, v12, v10\n"
"v_mul_f32 v13, v13, v11\n"
"v_mfma_f32_16x16x32_bf16 v[208:211], a[52:55], a[116:119], v[208:211]\n"
"v_accvgpr_read_b32 v10, a2\n"
"v_accvgpr_read_b32 v11, a3\n"
"v_mfma_f32_16x16x32_bf16 v[236:239], a[60:63], a[116:119], v[236:239]\n"
"ds_read_b128 a[116:119], v4 offset:17408\n"
"v_mul_f32 v14, v14, v10\n"
"v_mfma_f32_16x16x32_bf16 v[236:239], a[56:59], a[112:115], v[236:239]\n"
"ds_read_b128 a[112:115], v4 offset:16384\n"
"v_mul_f32 v15, v15, v11\n"
"v_mfma_f32_16x16x32_bf16 v[240:243], a[56:59], a[120:123], v[240:243]\n"
"v_cvt_pk_bf16_f32 v24, v12, v13\n"
"v_cvt_pk_bf16_f32 v25, v14, v15\n"
"v_mfma_f32_16x16x32_bf16 v[240:243], a[60:63], a[124:127], v[240:243]\n"
"ds_write_b64 v7, v[24:25] offset:38912\n"
"v_mfma_f32_16x16x32_bf16 v[212:215], a[52:55], a[124:127], v[212:215]\n"
"ds_read_b128 a[124:127], v2 offset:19456\n"
"v_lshlrev_b32 v12, 16, v18\n"
"v_mfma_f32_16x16x32_bf16 v[212:215], a[48:51], a[120:123], v[212:215]\n"
"ds_read_b128 a[120:123], v2 offset:18432\n"
"v_and_b32 v13, s63, v18\n"
"v_mfma_f32_16x16x32_bf16 v[216:219], a[48:51], a[128:131], v[216:219]\n"
"v_lshlrev_b32 v14, 16, v19\n"
"v_and_b32 v15, s63, v19\n"
"v_mfma_f32_16x16x32_bf16 v[216:219], a[52:55], a[132:135], v[216:219]\n"
"v_accvgpr_read_b32 v10, a4\n"
"v_accvgpr_read_b32 v11, a5\n"
"v_mfma_f32_16x16x32_bf16 v[244:247], a[60:63], a[132:135], v[244:247]\n"
"ds_read_b128 a[132:135], v3 offset:19456\n"
"v_mul_f32 v12, v12, v10\n"
"v_mfma_f32_16x16x32_bf16 v[244:247], a[56:59], a[128:131], v[244:247]\n"
"ds_read_b128 a[128:131], v3 offset:18432\n"
"v_mul_f32 v13, v13, v11\n"
"v_mfma_f32_16x16x32_bf16 v[248:251], a[56:59], a[136:139], v[248:251]\n"
"v_accvgpr_read_b32 v10, a6\n"
"v_accvgpr_read_b32 v11, a7\n"
"v_mfma_f32_16x16x32_bf16 v[248:251], a[60:63], a[140:143], v[248:251]\n"
"v_mul_f32 v14, v14, v10\n"
"v_mul_f32 v15, v15, v11\n"
"v_mfma_f32_16x16x32_bf16 v[220:223], a[52:55], a[140:143], v[220:223]\n"
"ds_read_b128 a[140:143], v4 offset:19456\n"
"v_cvt_pk_bf16_f32 v26, v12, v13\n"
"v_mfma_f32_16x16x32_bf16 v[220:223], a[48:51], a[136:139], v[220:223]\n"
"ds_read_b128 a[136:139], v4 offset:18432\n"
"v_cvt_pk_bf16_f32 v27, v14, v15\n"
"v_mfma_f32_16x16x32_bf16 v[224:227], a[48:51], a[144:147], v[224:227]\n"
"ds_write_b64 v7, v[26:27] offset:38920\n"
"v_mfma_f32_16x16x32_bf16 v[224:227], a[52:55], a[148:151], v[224:227]\n"
"v_accvgpr_read_b32 v16, a188\n"
"v_accvgpr_read_b32 v53, a244\n"
"v_mfma_f32_16x16x32_bf16 v[252:255], a[60:63], a[148:151], v[252:255]\n"
"s_waitcnt lgkmcnt(15)\n"
"buffer_store_dwordx4 v[56:59], v6, s[52:55], s92 offen offset:0 nt\n"
"ds_read_b128 a[148:151], v2 offset:21504\n"
"v_accvgpr_read_b32 v17, a189\n"
"v_accvgpr_read_b32 v18, a190\n"
"v_mfma_f32_16x16x32_bf16 v[252:255], a[56:59], a[144:147], v[252:255]\n"
"ds_read_b128 a[144:147], v2 offset:20480\n"
"v_accvgpr_read_b32 v19, a191\n"
"v_mfma_f32_16x16x32_bf16 a[28:31], a[56:59], a[152:155], a[28:31]\n"
"v_lshlrev_b32 v12, 16, v16\n"
"v_and_b32 v13, s63, v16\n"
"v_mfma_f32_16x16x32_bf16 a[28:31], a[60:63], a[156:159], a[28:31]\n"
"v_lshlrev_b32 v14, 16, v17\n"
"v_and_b32 v15, s63, v17\n"
"v_mfma_f32_16x16x32_bf16 a[24:27], a[52:55], a[156:159], a[24:27]\n"
"ds_read_b128 a[156:159], v3 offset:21504\n"
"v_accvgpr_read_b32 v10, a8\n"
"v_mfma_f32_16x16x32_bf16 a[24:27], a[48:51], a[152:155], a[24:27]\n"
"s_waitcnt lgkmcnt(15)\n"
"ds_read_b128 a[152:155], v3 offset:20480\n"
"v_accvgpr_read_b32 v11, a9\n"
"v_mfma_f32_16x16x32_bf16 v[144:147], v[88:91], a[96:99], v[144:147]\n"
"v_mul_f32 v12, v12, v10\n"
"v_mul_f32 v13, v13, v11\n"
"v_mfma_f32_16x16x32_bf16 v[144:147], v[116:119], a[100:103], v[144:147]\n"
"v_accvgpr_read_b32 v10, a10\n"
"v_accvgpr_read_b32 v11, a11\n"
"v_mfma_f32_16x16x32_bf16 v[172:175], v[120:123], a[100:103], v[172:175]\n"
"v_mul_f32 v14, v14, v10\n"
"v_mul_f32 v15, v15, v11\n"
"v_mfma_f32_16x16x32_bf16 v[172:175], v[92:95], a[96:99], v[172:175]\n"
"v_cvt_pk_bf16_f32 v28, v12, v13\n"
"v_cvt_pk_bf16_f32 v29, v14, v15\n"
"v_mfma_f32_16x16x32_bf16 v[200:203], v[32:35], a[96:99], v[200:203]\n"
"buffer_store_dwordx4 v[84:87], v6, s[52:55], s93 offen offset:1024 nt\n"
"ds_write_b64 v8, v[28:29] offset:38912\n"
"v_mfma_f32_16x16x32_bf16 v[200:203], v[60:63], a[100:103], v[200:203]\n"
"v_lshlrev_b32 v12, 16, v18\n"
"v_accvgpr_read_b32 v55, a245\n"
"v_mfma_f32_16x16x32_bf16 v[228:231], v[124:127], a[100:103], v[228:231]\n"
"ds_read_b128 a[100:103], v4 offset:21504\n"
"v_lshlrev_b32 v52, 16, v53\n"
"v_mfma_f32_16x16x32_bf16 v[228:231], v[96:99], a[96:99], v[228:231]\n"
"s_waitcnt vmcnt(3) lgkmcnt(15)\n"
"ds_read_b128 a[96:99], v4 offset:20480\n"
"v_lshlrev_b32 v54, 16, v55\n"
"v_mfma_f32_16x16x32_bf16 v[228:231], v[108:111], a[104:107], v[228:231]\n"
"v_and_b32 v13, s63, v18\n"
"v_and_b32 v53, s63, v53\n"
"v_mfma_f32_16x16x32_bf16 v[228:231], v[136:139], a[108:111], v[228:231]\n"
"v_lshlrev_b32 v14, 16, v19\n"
"v_and_b32 v55, s63, v55\n"
"v_mfma_f32_16x16x32_bf16 v[200:203], v[132:135], a[108:111], v[200:203]\n"
"v_and_b32 v15, s63, v19\n"
"v_accvgpr_read_b32 v81, a246\n"
"v_mfma_f32_16x16x32_bf16 v[200:203], v[104:107], a[104:107], v[200:203]\n"
"v_accvgpr_read_b32 v10, a12\n"
"v_accvgpr_read_b32 v83, a247\n"
"v_mfma_f32_16x16x32_bf16 v[172:175], v[36:39], a[104:107], v[172:175]\n"
"v_accvgpr_read_b32 v11, a13\n"
"v_lshlrev_b32 v80, 16, v81\n"
"v_mfma_f32_16x16x32_bf16 v[172:175], v[64:67], a[108:111], v[172:175]\n"
"v_mul_f32 v12, v12, v10\n"
"v_accvgpr_read_b32 v57, a248\n"
"v_mfma_f32_16x16x32_bf16 v[144:147], v[128:131], a[108:111], v[144:147]\n"
"ds_read_b128 a[108:111], v2 offset:23552\n"
"v_accvgpr_read_b32 v59, a249\n"
"v_mfma_f32_16x16x32_bf16 v[144:147], v[100:103], a[104:107], v[144:147]\n"
"s_waitcnt lgkmcnt(15)\n"
"ds_read_b128 a[104:107], v2 offset:22528\n"
"v_lshlrev_b32 v82, 16, v83\n"
"v_mfma_f32_16x16x32_bf16 v[144:147], a[64:67], a[112:115], v[144:147]\n"
"v_mul_f32 v13, v13, v11\n"
"v_lshlrev_b32 v56, 16, v57\n"
"v_mfma_f32_16x16x32_bf16 v[144:147], a[68:71], a[116:119], v[144:147]\n"
"v_accvgpr_read_b32 v10, a14\n"
"v_accvgpr_read_b32 v11, a15\n"
"v_mfma_f32_16x16x32_bf16 v[172:175], a[76:79], a[116:119], v[172:175]\n"
"v_mul_f32 v14, v14, v10\n"
"v_mul_f32 v15, v15, v11\n"
"v_mfma_f32_16x16x32_bf16 v[172:175], a[72:75], a[112:115], v[172:175]\n"
"v_cvt_pk_bf16_f32 v30, v12, v13\n"
"v_cvt_pk_bf16_f32 v31, v14, v15\n"
"v_mfma_f32_16x16x32_bf16 v[200:203], a[80:83], a[112:115], v[200:203]\n"
"ds_write_b64 v8, v[30:31] offset:38920\n"
"v_mfma_f32_16x16x32_bf16 v[200:203], a[84:87], a[116:119], v[200:203]\n"
"ds_read_b128 v[112:115], v2 offset:38912\n"
"v_and_b32 v81, s63, v81\n"
"v_mfma_f32_16x16x32_bf16 v[228:231], a[92:95], a[116:119], v[228:231]\n"
"ds_read_b128 a[116:119], v3 offset:23552\n"
"v_lshlrev_b32 v58, 16, v59\n"
"v_mfma_f32_16x16x32_bf16 v[228:231], a[88:91], a[112:115], v[228:231]\n"
"s_waitcnt lgkmcnt(15)\n"
"ds_read_b128 a[112:115], v3 offset:22528\n"
"v_and_b32 v57, s63, v57\n"
"v_mfma_f32_16x16x32_bf16 v[232:235], v[96:99], a[120:123], v[232:235]\n"
"ds_read_b128 v[140:143], v2 offset:39936\n"
"v_and_b32 v83, s63, v83\n"
"v_mfma_f32_16x16x32_bf16 v[232:235], v[124:127], a[124:127], v[232:235]\n"
"v_accvgpr_read_b32 v16, a192\n"
"v_accvgpr_read_b32 v17, a193\n"
"v_mfma_f32_16x16x32_bf16 v[204:207], v[60:63], a[124:127], v[204:207]\n"
"v_accvgpr_read_b32 v18, a194\n"
"v_accvgpr_read_b32 v19, a195\n"
"v_mfma_f32_16x16x32_bf16 v[204:207], v[32:35], a[120:123], v[204:207]\n"
"v_lshlrev_b32 v12, 16, v16\n"
"v_and_b32 v13, s63, v16\n"
"v_mfma_f32_16x16x32_bf16 v[176:179], v[92:95], a[120:123], v[176:179]\n"
"v_lshlrev_b32 v14, 16, v17\n"
"v_and_b32 v15, s63, v17\n"
"v_mfma_f32_16x16x32_bf16 v[176:179], v[120:123], a[124:127], v[176:179]\n"
"v_mul_f32 v12, v12, v144\n"
"v_mul_f32 v13, v13, v145\n"
"v_mfma_f32_16x16x32_bf16 v[148:151], v[116:119], a[124:127], v[148:151]\n"
"ds_read_b128 a[124:127], v4 offset:23552\n"
"v_mul_f32 v14, v14, v146\n"
"v_mfma_f32_16x16x32_bf16 v[148:151], v[88:91], a[120:123], v[148:151]\n"
"ds_read_b128 a[120:123], v4 offset:22528\n"
"v_mul_f32 v15, v15, v147\n"
"v_mfma_f32_16x16x32_bf16 v[148:151], v[100:103], a[128:131], v[148:151]\n"
"v_cvt_pk_bf16_f32 v24, v12, v13\n"
"v_cvt_pk_bf16_f32 v25, v14, v15\n"
"v_and_b32 v59, s63, v59\n"
"v_mfma_f32_16x16x32_bf16 v[148:151], v[128:131], a[132:135], v[148:151]\n"
"ds_write_b64 v7, v[24:25] offset:32768\n"
"v_mfma_f32_16x16x32_bf16 v[176:179], v[64:67], a[132:135], v[176:179]\n"
"v_lshlrev_b32 v12, 16, v18\n"
"v_and_b32 v13, s63, v18\n"
"v_lshlrev_b32 v14, 16, v19\n"
"v_mfma_f32_16x16x32_bf16 v[176:179], v[36:39], a[128:131], v[176:179]\n"
"v_and_b32 v15, s63, v19\n"
"v_mul_f32 v12, v12, v172\n"
"v_mul_f32 v13, v13, v173\n"
"v_mfma_f32_16x16x32_bf16 v[204:207], v[104:107], a[128:131], v[204:207]\n"
"s_waitcnt lgkmcnt(6)\n"
"buffer_store_dwordx4 v[112:115], v6, s[52:55], s94 offen offset:2048 nt\n"
"v_mul_f32 v14, v14, v174\n"
"v_mul_f32 v15, v15, v175\n"
"v_cvt_pk_bf16_f32 v26, v12, v13\n"
"v_cvt_pk_bf16_f32 v27, v14, v15\n"
"v_mfma_f32_16x16x32_bf16 v[204:207], v[132:135], a[132:135], v[204:207]\n"
"ds_write_b64 v7, v[26:27] offset:32776\n"
"v_mfma_f32_16x16x32_bf16 v[232:235], v[136:139], a[132:135], v[232:235]\n"
"ds_read_b128 a[132:135], v2 offset:25600\n"
"v_accvgpr_read_b32 v16, a196\n"
"v_mfma_f32_16x16x32_bf16 v[232:235], v[108:111], a[128:131], v[232:235]\n"
"ds_read_b128 a[128:131], v2 offset:24576\n"
"v_accvgpr_read_b32 v17, a197\n"
"v_mfma_f32_16x16x32_bf16 v[232:235], a[88:91], a[136:139], v[232:235]\n"
"v_accvgpr_read_b32 v18, a198\n"
"v_accvgpr_read_b32 v19, a199\n"
"v_accvgpr_read_b32 v85, a250\n"
"v_mfma_f32_16x16x32_bf16 v[232:235], a[92:95], a[140:143], v[232:235]\n"
"v_lshlrev_b32 v12, 16, v16\n"
"v_and_b32 v13, s63, v16\n"
"v_mfma_f32_16x16x32_bf16 v[204:207], a[84:87], a[140:143], v[204:207]\n"
"v_lshlrev_b32 v14, 16, v17\n"
"v_and_b32 v15, s63, v17\n"
"v_mfma_f32_16x16x32_bf16 v[204:207], a[80:83], a[136:139], v[204:207]\n"
"v_mul_f32 v12, v12, v200\n"
"v_mul_f32 v13, v13, v201\n"
"v_mul_f32 v14, v14, v202\n"
"v_mfma_f32_16x16x32_bf16 v[176:179], a[72:75], a[136:139], v[176:179]\n"
"v_mul_f32 v15, v15, v203\n"
"v_cvt_pk_bf16_f32 v28, v12, v13\n"
"v_cvt_pk_bf16_f32 v29, v14, v15\n"
"v_mfma_f32_16x16x32_bf16 v[176:179], a[76:79], a[140:143], v[176:179]\n"
"ds_write_b64 v8, v[28:29] offset:32768\n"
"v_mfma_f32_16x16x32_bf16 v[148:151], a[68:71], a[140:143], v[148:151]\n"
"s_waitcnt lgkmcnt(7)\n"
"buffer_store_dwordx4 v[140:143], v6, s[52:55], s95 offen offset:3072 nt\n"
"ds_read_b128 a[140:143], v3 offset:25600\n"
"v_lshlrev_b32 v12, 16, v18\n"
"v_and_b32 v13, s63, v18\n"
"v_lshlrev_b32 v14, 16, v19\n"
"v_mfma_f32_16x16x32_bf16 v[148:151], a[64:67], a[136:139], v[148:151]\n"
"ds_read_b128 a[136:139], v3 offset:24576\n"
"v_and_b32 v15, s63, v19\n"
"v_accvgpr_read_b32 v87, a251\n"
"v_mfma_f32_16x16x32_bf16 v[152:155], v[88:91], a[144:147], v[152:155]\n"
"v_mul_f32 v12, v12, v228\n"
"v_mul_f32 v13, v13, v229\n"
"v_mfma_f32_16x16x32_bf16 v[152:155], v[116:119], a[148:151], v[152:155]\n"
"v_mul_f32 v14, v14, v230\n"
"v_mul_f32 v15, v15, v231\n"
"v_mfma_f32_16x16x32_bf16 v[180:183], v[120:123], a[148:151], v[180:183]\n"
"v_cvt_pk_bf16_f32 v30, v12, v13\n"
"v_cvt_pk_bf16_f32 v31, v14, v15\n"
"v_mfma_f32_16x16x32_bf16 v[180:183], v[92:95], a[144:147], v[180:183]\n"
"ds_write_b64 v8, v[30:31] offset:32776\n"
"v_mfma_f32_16x16x32_bf16 v[208:211], v[32:35], a[144:147], v[208:211]\n"
"ds_read_b128 v[144:147], v2 offset:32768\n"
"v_lshlrev_b32 v84, 16, v85\n"
"v_mfma_f32_16x16x32_bf16 v[208:211], v[60:63], a[148:151], v[208:211]\n"
"ds_read_b128 v[172:175], v2 offset:33792\n"
"v_accvgpr_read_b32 v16, a200\n"
"v_mfma_f32_16x16x32_bf16 v[236:239], v[124:127], a[148:151], v[236:239]\n"
"ds_read_b128 a[148:151], v4 offset:25600\n"
"v_accvgpr_read_b32 v17, a201\n"
"v_mfma_f32_16x16x32_bf16 v[236:239], v[96:99], a[144:147], v[236:239]\n"
"ds_read_b128 a[144:147], v4 offset:24576\n"
"v_accvgpr_read_b32 v18, a202\n"
"v_lshlrev_b32 v86, 16, v87\n"
"v_mfma_f32_16x16x32_bf16 v[236:239], v[108:111], a[152:155], v[236:239]\n"
"v_accvgpr_read_b32 v19, a203\n"
"v_lshlrev_b32 v12, 16, v16\n"
"v_mfma_f32_16x16x32_bf16 v[236:239], v[136:139], a[156:159], v[236:239]\n"
"v_and_b32 v13, s63, v16\n"
"v_lshlrev_b32 v14, 16, v17\n"
"v_mfma_f32_16x16x32_bf16 v[208:211], v[132:135], a[156:159], v[208:211]\n"
"v_and_b32 v15, s63, v17\n"
"v_mul_f32 v12, v12, v148\n"
"v_mfma_f32_16x16x32_bf16 v[208:211], v[104:107], a[152:155], v[208:211]\n"
"v_mul_f32 v13, v13, v149\n"
"v_mul_f32 v14, v14, v150\n"
"v_and_b32 v85, s63, v85\n"
"v_mfma_f32_16x16x32_bf16 v[180:183], v[36:39], a[152:155], v[180:183]\n"
"v_mul_f32 v15, v15, v151\n"
"v_cvt_pk_bf16_f32 v24, v12, v13\n"
"v_cvt_pk_bf16_f32 v25, v14, v15\n"
"v_mfma_f32_16x16x32_bf16 v[180:183], v[64:67], a[156:159], v[180:183]\n"
"ds_write_b64 v7, v[24:25] offset:34816\n"
"v_mfma_f32_16x16x32_bf16 v[152:155], v[128:131], a[156:159], v[152:155]\n"
"ds_read_b128 a[156:159], v2 offset:27648\n"
"v_lshlrev_b32 v12, 16, v18\n"
"v_mfma_f32_16x16x32_bf16 v[152:155], v[100:103], a[152:155], v[152:155]\n"
"ds_read_b128 a[152:155], v2 offset:26624\n"
"v_and_b32 v13, s63, v18\n"
"v_mfma_f32_16x16x32_bf16 v[152:155], a[64:67], a[96:99], v[152:155]\n"
"v_lshlrev_b32 v14, 16, v19\n"
"v_and_b32 v15, s63, v19\n"
"v_mfma_f32_16x16x32_bf16 v[152:155], a[68:71], a[100:103], v[152:155]\n"
"v_mul_f32 v12, v12, v176\n"
"v_mul_f32 v13, v13, v177\n"
"v_and_b32 v87, s63, v87\n"
"v_mfma_f32_16x16x32_bf16 v[180:183], a[76:79], a[100:103], v[180:183]\n"
"v_mul_f32 v14, v14, v178\n"
"v_mul_f32 v15, v15, v179\n"
"v_mfma_f32_16x16x32_bf16 v[180:183], a[72:75], a[96:99], v[180:183]\n"
"v_cvt_pk_bf16_f32 v26, v12, v13\n"
"v_cvt_pk_bf16_f32 v27, v14, v15\n"
"v_mfma_f32_16x16x32_bf16 v[208:211], a[80:83], a[96:99], v[208:211]\n"
"s_waitcnt lgkmcnt(6)\n"
"buffer_store_dwordx4 v[144:147], v6, s[52:55], s80 offen offset:128 nt\n"
"ds_write_b64 v7, v[26:27] offset:34824\n"
"v_mfma_f32_16x16x32_bf16 v[208:211], a[84:87], a[100:103], v[208:211]\n"
"v_accvgpr_read_b32 v16, a204\n"
"v_accvgpr_read_b32 v17, a205\n"
"v_mfma_f32_16x16x32_bf16 v[236:239], a[92:95], a[100:103], v[236:239]\n"
"ds_read_b128 a[100:103], v3 offset:27648\n"
"v_accvgpr_read_b32 v18, a206\n"
"v_mfma_f32_16x16x32_bf16 v[236:239], a[88:91], a[96:99], v[236:239]\n"
"ds_read_b128 a[96:99], v3 offset:26624\n"
"v_accvgpr_read_b32 v19, a207\n"
"v_mfma_f32_16x16x32_bf16 v[240:243], v[96:99], a[104:107], v[240:243]\n"
"v_lshlrev_b32 v12, 16, v16\n"
"v_and_b32 v13, s63, v16\n"
"v_mfma_f32_16x16x32_bf16 v[240:243], v[124:127], a[108:111], v[240:243]\n"
"v_lshlrev_b32 v14, 16, v17\n"
"v_and_b32 v15, s63, v17\n"
"v_mfma_f32_16x16x32_bf16 v[212:215], v[60:63], a[108:111], v[212:215]\n"
"v_mul_f32 v12, v12, v204\n"
"v_mul_f32 v13, v13, v205\n"
"v_mfma_f32_16x16x32_bf16 v[212:215], v[32:35], a[104:107], v[212:215]\n"
"v_mul_f32 v14, v14, v206\n"
"v_mul_f32 v15, v15, v207\n"
"v_mfma_f32_16x16x32_bf16 v[184:187], v[92:95], a[104:107], v[184:187]\n"
"v_cvt_pk_bf16_f32 v28, v12, v13\n"
"v_cvt_pk_bf16_f32 v29, v14, v15\n"
"v_mfma_f32_16x16x32_bf16 v[184:187], v[120:123], a[108:111], v[184:187]\n"
"ds_write_b64 v8, v[28:29] offset:34816\n"
"v_mfma_f32_16x16x32_bf16 v[156:159], v[116:119], a[108:111], v[156:159]\n"
"s_waitcnt lgkmcnt(9)\n"
"buffer_store_dwordx4 v[172:175], v6, s[52:55], s81 offen offset:1152 nt\n"
"ds_read_b128 a[108:111], v4 offset:27648\n"
"v_lshlrev_b32 v12, 16, v18\n"
"v_and_b32 v13, s63, v18\n"
"v_lshlrev_b32 v14, 16, v19\n"
"v_mfma_f32_16x16x32_bf16 v[156:159], v[88:91], a[104:107], v[156:159]\n"
"ds_read_b128 a[104:107], v4 offset:26624\n"
"v_and_b32 v15, s63, v19\n"
"v_mfma_f32_16x16x32_bf16 v[156:159], v[100:103], a[112:115], v[156:159]\n"
"v_mul_f32 v12, v12, v232\n"
"v_mul_f32 v13, v13, v233\n"
"v_mfma_f32_16x16x32_bf16 v[156:159], v[128:131], a[116:119], v[156:159]\n"
"v_mul_f32 v14, v14, v234\n"
"v_mul_f32 v15, v15, v235\n"
"v_mfma_f32_16x16x32_bf16 v[184:187], v[64:67], a[116:119], v[184:187]\n"
"v_cvt_pk_bf16_f32 v30, v12, v13\n"
"v_cvt_pk_bf16_f32 v31, v14, v15\n"
"v_mfma_f32_16x16x32_bf16 v[184:187], v[36:39], a[112:115], v[184:187]\n"
"ds_write_b64 v8, v[30:31] offset:34824\n"
"v_mfma_f32_16x16x32_bf16 v[212:215], v[104:107], a[112:115], v[212:215]\n"
"ds_read_b128 v[148:151], v2 offset:34816\n"
"v_mfma_f32_16x16x32_bf16 v[212:215], v[132:135], a[116:119], v[212:215]\n"
"ds_read_b128 v[176:179], v2 offset:35840\n"
"v_accvgpr_read_b32 v16, a208\n"
"v_accvgpr_read_b32 v17, a209\n"
"v_mfma_f32_16x16x32_bf16 v[240:243], v[136:139], a[116:119], v[240:243]\n"
"ds_read_b128 a[116:119], v2 offset:29696\n"
"v_accvgpr_read_b32 v18, a210\n"
"v_mfma_f32_16x16x32_bf16 v[240:243], v[108:111], a[112:115], v[240:243]\n"
"ds_read_b128 a[112:115], v2 offset:28672\n"
"v_accvgpr_read_b32 v19, a211\n"
"v_mfma_f32_16x16x32_bf16 v[240:243], a[88:91], a[120:123], v[240:243]\n"
"v_lshlrev_b32 v12, 16, v16\n"
"v_and_b32 v13, s63, v16\n"
"v_mfma_f32_16x16x32_bf16 v[240:243], a[92:95], a[124:127], v[240:243]\n"
"v_lshlrev_b32 v14, 16, v17\n"
"v_and_b32 v15, s63, v17\n"
"v_mfma_f32_16x16x32_bf16 v[212:215], a[84:87], a[124:127], v[212:215]\n"
"v_mul_f32 v12, v12, v152\n"
"v_mul_f32 v13, v13, v153\n"
"v_mfma_f32_16x16x32_bf16 v[212:215], a[80:83], a[120:123], v[212:215]\n"
"v_mul_f32 v14, v14, v154\n"
"v_mul_f32 v15, v15, v155\n"
"v_mfma_f32_16x16x32_bf16 v[184:187], a[72:75], a[120:123], v[184:187]\n"
"v_cvt_pk_bf16_f32 v24, v12, v13\n"
"v_cvt_pk_bf16_f32 v25, v14, v15\n"
"v_mfma_f32_16x16x32_bf16 v[184:187], a[76:79], a[124:127], v[184:187]\n"
"ds_write_b64 v7, v[24:25] offset:36864\n"
"v_mfma_f32_16x16x32_bf16 v[156:159], a[68:71], a[124:127], v[156:159]\n"
"ds_read_b128 a[124:127], v3 offset:29696\n"
"v_lshlrev_b32 v12, 16, v18\n"
"v_mfma_f32_16x16x32_bf16 v[156:159], a[64:67], a[120:123], v[156:159]\n"
"ds_read_b128 a[120:123], v3 offset:28672\n"
"v_and_b32 v13, s63, v18\n"
"v_mfma_f32_16x16x32_bf16 v[160:163], v[88:91], a[128:131], v[160:163]\n"
"s_waitcnt lgkmcnt(6)\n"
"buffer_store_dwordx4 v[148:151], v6, s[52:55], s82 offen offset:2176 nt\n"
"v_lshlrev_b32 v14, 16, v19\n"
"v_and_b32 v15, s63, v19\n"
"v_mul_f32 v12, v12, v180\n"
"v_mul_f32 v13, v13, v181\n"
"v_mfma_f32_16x16x32_bf16 v[160:163], v[116:119], a[132:135], v[160:163]\n"
"v_mul_f32 v14, v14, v182\n"
"v_mul_f32 v15, v15, v183\n"
"v_mfma_f32_16x16x32_bf16 v[188:191], v[120:123], a[132:135], v[188:191]\n"
"v_cvt_pk_bf16_f32 v26, v12, v13\n"
"v_cvt_pk_bf16_f32 v27, v14, v15\n"
"v_mfma_f32_16x16x32_bf16 v[188:191], v[92:95], a[128:131], v[188:191]\n"
"ds_write_b64 v7, v[26:27] offset:36872\n"
"v_mfma_f32_16x16x32_bf16 v[216:219], v[32:35], a[128:131], v[216:219]\n"
"v_accvgpr_read_b32 v16, a212\n"
"v_accvgpr_read_b32 v17, a213\n"
"v_mfma_f32_16x16x32_bf16 v[216:219], v[60:63], a[132:135], v[216:219]\n"
"v_accvgpr_read_b32 v18, a214\n"
"v_accvgpr_read_b32 v19, a215\n"
"v_mfma_f32_16x16x32_bf16 v[244:247], v[124:127], a[132:135], v[244:247]\n"
"ds_read_b128 a[132:135], v4 offset:29696\n"
"v_lshlrev_b32 v12, 16, v16\n"
"v_mfma_f32_16x16x32_bf16 v[244:247], v[96:99], a[128:131], v[244:247]\n"
"ds_read_b128 a[128:131], v4 offset:28672\n"
"v_and_b32 v13, s63, v16\n"
"v_mfma_f32_16x16x32_bf16 v[244:247], v[108:111], a[136:139], v[244:247]\n"
"v_lshlrev_b32 v14, 16, v17\n"
"v_and_b32 v15, s63, v17\n"
"v_mfma_f32_16x16x32_bf16 v[244:247], v[136:139], a[140:143], v[244:247]\n"
"v_mul_f32 v12, v12, v208\n"
"v_mul_f32 v13, v13, v209\n"
"v_mfma_f32_16x16x32_bf16 v[216:219], v[132:135], a[140:143], v[216:219]\n"
"s_waitcnt lgkmcnt(8)\n"
"buffer_store_dwordx4 v[176:179], v6, s[52:55], s83 offen offset:3200 nt\n"
"v_mul_f32 v14, v14, v210\n"
"v_mul_f32 v15, v15, v211\n"
"v_cvt_pk_bf16_f32 v28, v12, v13\n"
"v_cvt_pk_bf16_f32 v29, v14, v15\n"
"v_mfma_f32_16x16x32_bf16 v[216:219], v[104:107], a[136:139], v[216:219]\n"
"ds_write_b64 v8, v[28:29] offset:36864\n"
"v_mfma_f32_16x16x32_bf16 v[188:191], v[36:39], a[136:139], v[188:191]\n"
"v_lshlrev_b32 v12, 16, v18\n"
"v_and_b32 v13, s63, v18\n"
"v_mfma_f32_16x16x32_bf16 v[188:191], v[64:67], a[140:143], v[188:191]\n"
"v_lshlrev_b32 v14, 16, v19\n"
"v_and_b32 v15, s63, v19\n"
"v_mfma_f32_16x16x32_bf16 v[160:163], v[128:131], a[140:143], v[160:163]\n"
"ds_read_b128 a[140:143], v2 offset:31744\n"
"v_mul_f32 v12, v12, v236\n"
"v_mfma_f32_16x16x32_bf16 v[160:163], v[100:103], a[136:139], v[160:163]\n"
"ds_read_b128 a[136:139], v2 offset:30720\n"
"v_mul_f32 v13, v13, v237\n"
"v_mul_f32 v14, v14, v238\n"
"v_mfma_f32_16x16x32_bf16 v[160:163], a[64:67], a[144:147], v[160:163]\n"
"v_mul_f32 v15, v15, v239\n"
"v_cvt_pk_bf16_f32 v30, v12, v13\n"
"v_cvt_pk_bf16_f32 v31, v14, v15\n"
"v_mfma_f32_16x16x32_bf16 v[160:163], a[68:71], a[148:151], v[160:163]\n"
"ds_write_b64 v8, v[30:31] offset:36872\n"
"v_mfma_f32_16x16x32_bf16 v[188:191], a[76:79], a[148:151], v[188:191]\n"
"ds_read_b128 v[152:155], v2 offset:36864\n"
"v_mfma_f32_16x16x32_bf16 v[188:191], a[72:75], a[144:147], v[188:191]\n"
"ds_read_b128 v[180:183], v2 offset:37888\n"
"v_accvgpr_read_b32 v16, a216\n"
"v_mfma_f32_16x16x32_bf16 v[216:219], a[80:83], a[144:147], v[216:219]\n"
"v_accvgpr_read_b32 v17, a217\n"
"v_accvgpr_read_b32 v18, a218\n"
"v_mfma_f32_16x16x32_bf16 v[216:219], a[84:87], a[148:151], v[216:219]\n"
"v_accvgpr_read_b32 v19, a219\n"
"v_lshlrev_b32 v12, 16, v16\n"
"v_mfma_f32_16x16x32_bf16 v[244:247], a[92:95], a[148:151], v[244:247]\n"
"ds_read_b128 a[148:151], v3 offset:31744\n"
"v_and_b32 v13, s63, v16\n"
"v_lshlrev_b32 v14, 16, v17\n"
"v_mfma_f32_16x16x32_bf16 v[244:247], a[88:91], a[144:147], v[244:247]\n"
"ds_read_b128 a[144:147], v3 offset:30720\n"
"v_and_b32 v15, s63, v17\n"
"v_mfma_f32_16x16x32_bf16 v[248:251], v[96:99], a[152:155], v[248:251]\n"
"v_mul_f32 v12, v12, v156\n"
"v_mul_f32 v13, v13, v157\n"
"v_mfma_f32_16x16x32_bf16 v[248:251], v[124:127], a[156:159], v[248:251]\n"
"v_mul_f32 v14, v14, v158\n"
"v_mul_f32 v15, v15, v159\n"
"v_mfma_f32_16x16x32_bf16 v[220:223], v[60:63], a[156:159], v[220:223]\n"
"v_cvt_pk_bf16_f32 v24, v12, v13\n"
"v_cvt_pk_bf16_f32 v25, v14, v15\n"
"v_mfma_f32_16x16x32_bf16 v[220:223], v[32:35], a[152:155], v[220:223]\n"
"ds_write_b64 v7, v[24:25] offset:38912\n"
"v_mfma_f32_16x16x32_bf16 v[192:195], v[92:95], a[152:155], v[192:195]\n"
"s_waitcnt lgkmcnt(4)\n"
"buffer_store_dwordx4 v[152:155], v6, s[52:55], s84 offen offset:128 nt\n"
"v_lshlrev_b32 v12, 16, v18\n"
"v_and_b32 v13, s63, v18\n"
"v_lshlrev_b32 v14, 16, v19\n"
"v_and_b32 v15, s63, v19\n"
"v_mfma_f32_16x16x32_bf16 v[192:195], v[120:123], a[156:159], v[192:195]\n"
"v_mul_f32 v12, v12, v184\n"
"v_mul_f32 v13, v13, v185\n"
"v_mfma_f32_16x16x32_bf16 v[164:167], v[116:119], a[156:159], v[164:167]\n"
"ds_read_b128 a[156:159], v4 offset:31744\n"
"v_mul_f32 v14, v14, v186\n"
"v_mfma_f32_16x16x32_bf16 v[164:167], v[88:91], a[152:155], v[164:167]\n"
"ds_read_b128 a[152:155], v4 offset:30720\n"
"v_mul_f32 v15, v15, v187\n"
"v_mfma_f32_16x16x32_bf16 v[164:167], v[100:103], a[96:99], v[164:167]\n"
"v_cvt_pk_bf16_f32 v26, v12, v13\n"
"v_cvt_pk_bf16_f32 v27, v14, v15\n"
"v_mfma_f32_16x16x32_bf16 v[164:167], v[128:131], a[100:103], v[164:167]\n"
"ds_write_b64 v7, v[26:27] offset:38920\n"
"v_mfma_f32_16x16x32_bf16 v[192:195], v[64:67], a[100:103], v[192:195]\n"
"v_accvgpr_read_b32 v16, a220\n"
"v_accvgpr_read_b32 v17, a221\n"
"v_mfma_f32_16x16x32_bf16 v[192:195], v[36:39], a[96:99], v[192:195]\n"
"v_accvgpr_read_b32 v18, a222\n"
"v_accvgpr_read_b32 v19, a223\n"
"v_mfma_f32_16x16x32_bf16 v[220:223], v[104:107], a[96:99], v[220:223]\n"
"v_lshlrev_b32 v12, 16, v16\n"
"v_and_b32 v13, s63, v16\n"
"v_mfma_f32_16x16x32_bf16 v[220:223], v[132:135], a[100:103], v[220:223]\n"
"v_lshlrev_b32 v14, 16, v17\n"
"v_and_b32 v15, s63, v17\n"
"v_mfma_f32_16x16x32_bf16 v[248:251], v[136:139], a[100:103], v[248:251]\n"
"s_waitcnt lgkmcnt(6)\n"
"buffer_store_dwordx4 v[180:183], v6, s[52:55], s85 offen offset:1152 nt\n"
"v_mul_f32 v12, v12, v212\n"
"v_mul_f32 v13, v13, v213\n"
"v_mul_f32 v14, v14, v214\n"
"v_mul_f32 v15, v15, v215\n"
"v_mfma_f32_16x16x32_bf16 v[248:251], v[108:111], a[96:99], v[248:251]\n"
"v_cvt_pk_bf16_f32 v28, v12, v13\n"
"v_cvt_pk_bf16_f32 v29, v14, v15\n"
"v_mfma_f32_16x16x32_bf16 v[248:251], a[88:91], a[104:107], v[248:251]\n"
"ds_write_b64 v8, v[28:29] offset:38912\n"
"v_mfma_f32_16x16x32_bf16 v[248:251], a[92:95], a[108:111], v[248:251]\n"
"v_lshlrev_b32 v12, 16, v18\n"
"v_and_b32 v13, s63, v18\n"
"v_mfma_f32_16x16x32_bf16 v[220:223], a[84:87], a[108:111], v[220:223]\n"
"v_lshlrev_b32 v14, 16, v19\n"
"v_and_b32 v15, s63, v19\n"
"v_mfma_f32_16x16x32_bf16 v[220:223], a[80:83], a[104:107], v[220:223]\n"
"v_mul_f32 v12, v12, v240\n"
"v_mul_f32 v13, v13, v241\n"
"v_mfma_f32_16x16x32_bf16 v[192:195], a[72:75], a[104:107], v[192:195]\n"
"L_st11503:\n"
"s_cbranch_execz L_retC\n"
"v_mul_f32 v14, v14, v242\n"
"v_mul_f32 v15, v15, v243\n"
"v_mfma_f32_16x16x32_bf16 v[192:195], a[76:79], a[108:111], v[192:195]\n"
"v_cvt_pk_bf16_f32 v30, v12, v13\n"
"v_cvt_pk_bf16_f32 v31, v14, v15\n"
"v_mfma_f32_16x16x32_bf16 v[164:167], a[68:71], a[108:111], v[164:167]\n"
"ds_write_b64 v8, v[30:31] offset:38920\n"
"v_mfma_f32_16x16x32_bf16 v[164:167], a[64:67], a[104:107], v[164:167]\n"
"ds_read_b128 v[156:159], v2 offset:38912\n"
"v_mfma_f32_16x16x32_bf16 v[168:171], v[88:91], a[112:115], v[168:171]\n"
"ds_read_b128 v[184:187], v2 offset:39936\n"
"v_accvgpr_read_b32 v16, a224\n"
"v_mfma_f32_16x16x32_bf16 v[168:171], v[116:119], a[116:119], v[168:171]\n"
"v_accvgpr_read_b32 v17, a225\n"
"v_accvgpr_read_b32 v18, a226\n"
"v_mfma_f32_16x16x32_bf16 v[196:199], v[120:123], a[116:119], v[196:199]\n"
"v_accvgpr_read_b32 v19, a227\n"
"v_lshlrev_b32 v12, 16, v16\n"
"v_mfma_f32_16x16x32_bf16 v[196:199], v[92:95], a[112:115], v[196:199]\n"
"v_and_b32 v13, s63, v16\n"
"v_lshlrev_b32 v14, 16, v17\n"
"v_mfma_f32_16x16x32_bf16 v[224:227], v[32:35], a[112:115], v[224:227]\n"
"v_and_b32 v15, s63, v17\n"
"v_mul_f32 v12, v12, v160\n"
"v_mfma_f32_16x16x32_bf16 v[224:227], v[60:63], a[116:119], v[224:227]\n"
"v_mul_f32 v13, v13, v161\n"
"v_mul_f32 v14, v14, v162\n"
"v_mfma_f32_16x16x32_bf16 v[252:255], v[124:127], a[116:119], v[252:255]\n"
"v_mul_f32 v15, v15, v163\n"
"v_cvt_pk_bf16_f32 v24, v12, v13\n"
"v_cvt_pk_bf16_f32 v25, v14, v15\n"
"v_mfma_f32_16x16x32_bf16 v[252:255], v[96:99], a[112:115], v[252:255]\n"
"ds_write_b64 v7, v[24:25] offset:32768\n"
"v_mfma_f32_16x16x32_bf16 v[252:255], v[108:111], a[120:123], v[252:255]\n"
"s_waitcnt lgkmcnt(2)\n"
"buffer_store_dwordx4 v[156:159], v6, s[52:55], s86 offen offset:2176 nt\n"
"v_lshlrev_b32 v12, 16, v18\n"
"v_and_b32 v13, s63, v18\n"
"v_lshlrev_b32 v14, 16, v19\n"
"v_mfma_f32_16x16x32_bf16 v[252:255], v[136:139], a[124:127], v[252:255]\n"
"v_and_b32 v15, s63, v19\n"
"v_mfma_f32_16x16x32_bf16 v[224:227], v[132:135], a[124:127], v[224:227]\n"
"v_mul_f32 v12, v12, v188\n"
"v_mul_f32 v13, v13, v189\n"
"v_mfma_f32_16x16x32_bf16 v[224:227], v[104:107], a[120:123], v[224:227]\n"
"v_mul_f32 v14, v14, v190\n"
"v_mul_f32 v15, v15, v191\n"
"v_mfma_f32_16x16x32_bf16 v[196:199], v[36:39], a[120:123], v[196:199]\n"
"v_cvt_pk_bf16_f32 v26, v12, v13\n"
"v_cvt_pk_bf16_f32 v27, v14, v15\n"
"v_mfma_f32_16x16x32_bf16 v[196:199], v[64:67], a[124:127], v[196:199]\n"
"ds_write_b64 v7, v[26:27] offset:32776\n"
"v_mfma_f32_16x16x32_bf16 v[168:171], v[128:131], a[124:127], v[168:171]\n"
"v_accvgpr_read_b32 v16, a228\n"
"v_accvgpr_read_b32 v17, a229\n"
"v_mfma_f32_16x16x32_bf16 v[168:171], v[100:103], a[120:123], v[168:171]\n"
"v_accvgpr_read_b32 v18, a230\n"
"v_accvgpr_read_b32 v19, a231\n"
"v_mfma_f32_16x16x32_bf16 v[168:171], a[64:67], a[128:131], v[168:171]\n"
"v_lshlrev_b32 v12, 16, v16\n"
"v_and_b32 v13, s63, v16\n"
"v_mfma_f32_16x16x32_bf16 v[168:171], a[68:71], a[132:135], v[168:171]\n"
"v_lshlrev_b32 v14, 16, v17\n"
"v_and_b32 v15, s63, v17\n"
"v_mfma_f32_16x16x32_bf16 v[196:199], a[76:79], a[132:135], v[196:199]\n"
"s_waitcnt lgkmcnt(2)\n"
"buffer_store_dwordx4 v[184:187], v6, s[52:55], s87 offen offset:3200 nt\n"
"v_mul_f32 v12, v12, v216\n"
"v_mul_f32 v13, v13, v217\n"
"v_mul_f32 v14, v14, v218\n"
"v_mul_f32 v15, v15, v219\n"
"v_mfma_f32_16x16x32_bf16 v[196:199], a[72:75], a[128:131], v[196:199]\n"
"v_cvt_pk_bf16_f32 v28, v12, v13\n"
"v_cvt_pk_bf16_f32 v29, v14, v15\n"
"v_mfma_f32_16x16x32_bf16 v[224:227], a[80:83], a[128:131], v[224:227]\n"
"ds_write_b64 v8, v[28:29] offset:32768\n"
"v_mfma_f32_16x16x32_bf16 v[224:227], a[84:87], a[132:135], v[224:227]\n"
"v_lshlrev_b32 v12, 16, v18\n"
"v_and_b32 v13, s63, v18\n"
"v_mfma_f32_16x16x32_bf16 v[252:255], a[92:95], a[132:135], v[252:255]\n"
"v_lshlrev_b32 v14, 16, v19\n"
"v_and_b32 v15, s63, v19\n"
"v_mfma_f32_16x16x32_bf16 v[252:255], a[88:91], a[128:131], v[252:255]\n"
"v_mul_f32 v12, v12, v244\n"
"v_mul_f32 v13, v13, v245\n"
"v_mfma_f32_16x16x32_bf16 a[28:31], v[96:99], a[136:139], a[28:31]\n"
"v_mul_f32 v14, v14, v246\n"
"v_mul_f32 v15, v15, v247\n"
"v_mfma_f32_16x16x32_bf16 a[28:31], v[124:127], a[140:143], a[28:31]\n"
"v_cvt_pk_bf16_f32 v30, v12, v13\n"
"v_cvt_pk_bf16_f32 v31, v14, v15\n"
"v_mfma_f32_16x16x32_bf16 a[24:27], v[60:63], a[140:143], a[24:27]\n"
"ds_write_b64 v8, v[30:31] offset:32776\n"
"v_mfma_f32_16x16x32_bf16 a[24:27], v[32:35], a[136:139], a[24:27]\n"
"ds_read_b128 v[160:163], v2 offset:32768\n"
"v_mfma_f32_16x16x32_bf16 a[20:23], v[92:95], a[136:139], a[20:23]\n"
"ds_read_b128 v[188:191], v2 offset:33792\n"
"v_mfma_f32_16x16x32_bf16 a[20:23], v[120:123], a[140:143], a[20:23]\n"
"v_mul_f32 v12, v40, v164\n"
"v_mul_f32 v13, v41, v165\n"
"v_mfma_f32_16x16x32_bf16 a[16:19], v[116:119], a[140:143], a[16:19]\n"
"v_mul_f32 v14, v42, v166\n"
"v_mul_f32 v15, v43, v167\n"
"v_mfma_f32_16x16x32_bf16 a[16:19], v[88:91], a[136:139], a[16:19]\n"
"v_cvt_pk_bf16_f32 v24, v12, v13\n"
"v_cvt_pk_bf16_f32 v25, v14, v15\n"
"v_mfma_f32_16x16x32_bf16 a[16:19], v[100:103], a[144:147], a[16:19]\n"
"ds_write_b64 v7, v[24:25] offset:34816\n"
"v_mfma_f32_16x16x32_bf16 a[16:19], v[128:131], a[148:151], a[16:19]\n"
"v_mul_f32 v12, v68, v192\n"
"v_mul_f32 v13, v69, v193\n"
"v_mfma_f32_16x16x32_bf16 a[20:23], v[64:67], a[148:151], a[20:23]\n"
"v_mul_f32 v14, v70, v194\n"
"v_mul_f32 v15, v71, v195\n"
"v_mfma_f32_16x16x32_bf16 a[20:23], v[36:39], a[144:147], a[20:23]\n"
"v_cvt_pk_bf16_f32 v26, v12, v13\n"
"v_cvt_pk_bf16_f32 v27, v14, v15\n"
"v_mfma_f32_16x16x32_bf16 a[24:27], v[104:107], a[144:147], a[24:27]\n"
"s_waitcnt lgkmcnt(2)\n"
"buffer_store_dwordx4 v[160:163], v6, s[52:55], s88 offen offset:128 nt\n"
"ds_write_b64 v7, v[26:27] offset:34824\n"
"v_mfma_f32_16x16x32_bf16 a[24:27], v[132:135], a[148:151], a[24:27]\n"
"v_mul_f32 v12, v44, v220\n"
"v_mfma_f32_16x16x32_bf16 a[28:31], v[136:139], a[148:151], a[28:31]\n"
"v_mul_f32 v13, v45, v221\n"
"v_mfma_f32_16x16x32_bf16 a[28:31], v[108:111], a[144:147], a[28:31]\n"
"v_mul_f32 v14, v46, v222\n"
"v_mfma_f32_16x16x32_bf16 a[28:31], a[88:91], a[152:155], a[28:31]\n"
"v_mul_f32 v15, v47, v223\n"
"v_mfma_f32_16x16x32_bf16 a[28:31], a[92:95], a[156:159], a[28:31]\n"
"v_cvt_pk_bf16_f32 v28, v12, v13\n"
"v_cvt_pk_bf16_f32 v29, v14, v15\n"
"v_mfma_f32_16x16x32_bf16 a[24:27], a[84:87], a[156:159], a[24:27]\n"
"ds_write_b64 v8, v[28:29] offset:34816\n"
"v_mfma_f32_16x16x32_bf16 a[24:27], a[80:83], a[152:155], a[24:27]\n"
"v_mul_f32 v12, v72, v248\n"
"v_mul_f32 v13, v73, v249\n"
"v_mfma_f32_16x16x32_bf16 a[20:23], a[72:75], a[152:155], a[20:23]\n"
"s_add_u32 s62, s62, 1\n"
"v_mul_f32 v14, v74, v250\n"
"v_mul_f32 v15, v75, v251\n"
"v_mfma_f32_16x16x32_bf16 a[20:23], a[76:79], a[156:159], a[20:23]\n"
"v_cvt_pk_bf16_f32 v30, v12, v13\n"
"v_cvt_pk_bf16_f32 v31, v14, v15\n"
"v_mfma_f32_16x16x32_bf16 a[16:19], a[68:71], a[156:159], a[16:19]\n"
"s_waitcnt lgkmcnt(3)\n"
"buffer_store_dwordx4 v[188:191], v6, s[52:55], s89 offen offset:1152 nt\n"
"ds_write_b64 v8, v[30:31] offset:34824\n"
"v_mfma_f32_16x16x32_bf16 a[16:19], a[64:67], a[152:155], a[16:19]\n"
"ds_read_b128 v[164:167], v2 offset:34816\n"
"ds_read_b128 v[192:195], v2 offset:35840\n"
"s_nop 7\n"
"v_pk_mul_f32 v[12:13], v[48:49], v[168:169]\n"
"v_pk_mul_f32 v[14:15], v[50:51], v[170:171]\n"
"v_cvt_pk_bf16_f32 v24, v12, v13\n"
"v_cvt_pk_bf16_f32 v25, v14, v15\n"
"v_pk_mul_f32 v[12:13], v[76:77], v[196:197]\n"
"v_pk_mul_f32 v[14:15], v[78:79], v[198:199]\n"
"v_cvt_pk_bf16_f32 v26, v12, v13\n"
"v_cvt_pk_bf16_f32 v27, v14, v15\n"
"ds_write_b128 v7, v[24:27] offset:36864\n"
"s_waitcnt lgkmcnt(2)\n"
"buffer_store_dwordx4 v[164:167], v6, s[52:55], s90 offen offset:2176 nt\n"
"s_waitcnt lgkmcnt(1)\n"
"buffer_store_dwordx4 v[192:195], v6, s[52:55], s91 offen offset:3200 nt\n"
"v_pk_mul_f32 v[12:13], v[52:53], v[224:225]\n"
"v_pk_mul_f32 v[14:15], v[54:55], v[226:227]\n"
"v_cvt_pk_bf16_f32 v28, v12, v13\n"
"v_cvt_pk_bf16_f32 v29, v14, v15\n"
"v_pk_mul_f32 v[12:13], v[80:81], v[252:253]\n"
"v_pk_mul_f32 v[14:15], v[82:83], v[254:255]\n"
"v_cvt_pk_bf16_f32 v30, v12, v13\n"
"v_cvt_pk_bf16_f32 v31, v14, v15\n"
"ds_write_b128 v8, v[28:31] offset:36864\n"
"ds_read_b128 v[168:171], v2 offset:36864\n"
"ds_read_b128 v[196:199], v2 offset:37888\n"
"v_accvgpr_read_b32 v10, a16\n"
"v_accvgpr_read_b32 v11, a17\n"
"v_mul_f32 v12, v56, v10\n"
"v_mul_f32 v13, v57, v11\n"
"v_accvgpr_read_b32 v10, a18\n"
"v_accvgpr_read_b32 v11, a19\n"
"v_mul_f32 v14, v58, v10\n"
"v_mul_f32 v15, v59, v11\n"
"v_cvt_pk_bf16_f32 v24, v12, v13\n"
"v_cvt_pk_bf16_f32 v25, v14, v15\n"
"v_accvgpr_read_b32 v10, a20\n"
"v_accvgpr_read_b32 v11, a21\n"
"v_mul_f32 v12, v84, v10\n"
"v_mul_f32 v13, v85, v11\n"
"v_accvgpr_read_b32 v10, a22\n"
"v_accvgpr_read_b32 v11, a23\n"
"v_mul_f32 v14, v86, v10\n"
"v_mul_f32 v15, v87, v11\n"
"v_cvt_pk_bf16_f32 v26, v12, v13\n"
"v_cvt_pk_bf16_f32 v27, v14, v15\n"
"ds_write_b128 v7, v[24:27] offset:38912\n"
"s_waitcnt lgkmcnt(2)\n"
"buffer_store_dwordx4 v[168:171], v6, s[52:55], s92 offen offset:128 nt\n"
"s_waitcnt lgkmcnt(1)\n"
"buffer_store_dwordx4 v[196:199], v6, s[52:55], s93 offen offset:1152 nt\n"
"v_accvgpr_read_b32 v16, a252\n"
"v_accvgpr_read_b32 v17, a253\n"
"v_accvgpr_read_b32 v18, a254\n"
"v_accvgpr_read_b32 v19, a255\n"
"v_lshlrev_b32 v12, 16, v16\n"
"v_and_b32 v13, s63, v16\n"
"v_lshlrev_b32 v14, 16, v17\n"
"v_and_b32 v15, s63, v17\n"
"v_accvgpr_read_b32 v10, a24\n"
"v_accvgpr_read_b32 v11, a25\n"
"v_mul_f32 v12, v12, v10\n"
"v_mul_f32 v13, v13, v11\n"
"v_accvgpr_read_b32 v10, a26\n"
"v_accvgpr_read_b32 v11, a27\n"
"v_mul_f32 v14, v14, v10\n"
"v_mul_f32 v15, v15, v11\n"
"v_cvt_pk_bf16_f32 v28, v12, v13\n"
"v_cvt_pk_bf16_f32 v29, v14, v15\n"
"v_lshlrev_b32 v12, 16, v18\n"
"v_and_b32 v13, s63, v18\n"
"v_lshlrev_b32 v14, 16, v19\n"
"v_and_b32 v15, s63, v19\n"
"v_accvgpr_read_b32 v10, a28\n"
"v_accvgpr_read_b32 v11, a29\n"
"v_mul_f32 v12, v12, v10\n"
"v_mul_f32 v13, v13, v11\n"
"v_accvgpr_read_b32 v10, a30\n"
"v_accvgpr_read_b32 v11, a31\n"
"v_mul_f32 v14, v14, v10\n"
"v_mul_f32 v15, v15, v11\n"
"v_cvt_pk_bf16_f32 v30, v12, v13\n"
"v_cvt_pk_bf16_f32 v31, v14, v15\n"
"ds_write_b128 v8, v[28:31] offset:38912\n"
"ds_read_b128 v[224:227], v2 offset:38912\n"
"ds_read_b128 v[252:255], v2 offset:39936\n"
"s_waitcnt lgkmcnt(1)\n"
"buffer_store_dwordx4 v[224:227], v6, s[52:55], s94 offen offset:2176 nt\n"
"s_waitcnt lgkmcnt(0)\n"
"buffer_store_dwordx4 v[252:255], v6, s[52:55], s95 offen offset:3200 nt\n"
"s_or_b32 s66, %26, %16\n"
"s_cbranch_scc1 L_end\n"
"v_mov_b32 v10, s78\n"
"v_mov_b32 v11, s79\n"
"s_mov_b32 s66, 139264\n"
"buffer_store_dwordx2 v[10:11], off, s[56:59], s66 sc1\n"
"s_branch L_end\n"
"L_slow0:\n"
"s_cmp_eq_u32 s68, 0\n"
"s_cbranch_scc1 L_back0\n"
"s_sub_u32 s68, s68, 1\n"
"s_sleep 2\n"
"buffer_load_dwordx2 v[8:9], off, s[56:59], %14 sc1\n"
"s_waitcnt vmcnt(0)\n"
"v_subrev_u32 v8, s65, v8\n"
"v_xor_b32 v9, s79, v9\n"
"v_or_b32 v8, v8, v9\n"
"v_cmp_lt_u32 vcc, 15, v8\n"
"s_nop 4\n"
"s_cbranch_vccnz L_slow0\n"
"s_branch L_back0\n"
"L_slow1:\n"
"s_cmp_eq_u32 s68, 0\n"
"s_cbranch_scc1 L_back1\n"
"s_sub_u32 s68, s68, 1\n"
"s_sleep 2\n"
"buffer_load_dwordx2 v[8:9], off, s[56:59], %14 sc1\n"
"s_waitcnt vmcnt(0)\n"
"v_subrev_u32 v8, s65, v8\n"
"v_xor_b32 v9, s79, v9\n"
"v_or_b32 v8, v8, v9\n"
"v_cmp_lt_u32 vcc, 15, v8\n"
"s_nop 4\n"
"s_cbranch_vccnz L_slow1\n"
"s_branch L_back1\n"
"L_slow2:\n"
"s_cmp_eq_u32 s68, 0\n"
"s_cbranch_scc1 L_back2\n"
"s_sub_u32 s68, s68, 1\n"
"s_sleep 2\n"
"buffer_load_dwordx4 v[12:15], v0, s[56:59], %25 offen sc1\n"
"s_waitcnt vmcnt(0)\n"
"v_subrev_u32 v12, s67, v12\n"
"v_xor_b32 v13, s79, v13\n"
"v_subrev_u32 v14, s67, v14\n"
"v_xor_b32 v15, s79, v15\n"
"v_or_b32 v12, v12, v14\n"
"v_or_b32 v13, v13, v15\n"
"v_or_b32 v12, v12, v13\n"
"v_cmp_lt_u32 vcc, 15, v12\n"
"s_nop 4\n"
"s_cbranch_vccnz L_slow2\n"
"s_branch L_back2\n"
"L_slow3:\n"
"s_cmp_eq_u32 s68, 0\n"
"s_cbranch_scc1 L_back3\n"
"s_sub_u32 s68, s68, 1\n"
"s_sleep 2\n"
"buffer_load_dwordx4 v[24:27], v0, s[56:59], %25 offen sc1\n"
"s_waitcnt vmcnt(0)\n"
"v_subrev_u32 v24, s70, v24\n"
"v_xor_b32 v25, s79, v25\n"
"v_subrev_u32 v26, s70, v26\n"
"v_xor_b32 v27, s79, v27\n"
"v_or_b32 v24, v24, v26\n"
"v_or_b32 v25, v25, v27\n"
"v_or_b32 v24, v24, v25\n"
"v_cmp_lt_u32 vcc, 15, v24\n"
"s_nop 4\n"
"s_cbranch_vccnz L_slow3\n"
"s_branch L_back3\n"
"L_slow4:\n"
"s_cmp_eq_u32 s68, 0\n"
"s_cbranch_scc1 L_back4\n"
"s_sub_u32 s68, s68, 1\n"
"s_sleep 2\n"
"buffer_load_dwordx2 v[8:9], off, s[56:59], %14 sc1\n"
"s_waitcnt vmcnt(0)\n"
"v_subrev_u32 v8, s65, v8\n"
"v_xor_b32 v9, s79, v9\n"
"v_or_b32 v8, v8, v9\n"
"v_cmp_lt_u32 vcc, 15, v8\n"
"s_nop 4\n"
"s_cbranch_vccnz L_slow4\n"
"s_branch L_back4\n"
"L_slow7:\n"
"s_cmp_eq_u32 s68, 0\n"
"s_cbranch_scc1 L_back7\n"
"s_sub_u32 s68, s68, 1\n"
"s_sleep 2\n"
"buffer_load_dwordx2 v[8:9], off, s[56:59], %14 sc1\n"
"s_waitcnt vmcnt(0)\n"
"v_subrev_u32 v8, s65, v8\n"
"v_xor_b32 v9, s79, v9\n"
"v_or_b32 v8, v8, v9\n"
"v_cmp_lt_u32 vcc, 15, v8\n"
"s_nop 4\n"
"s_cbranch_vccnz L_slow7\n"
"s_branch L_back7\n"
"L_headF:\n"
"L_fa:\n"
"s_mov_b64 s[96:97], s[52:53]\n"
"s_cmp_lt_u32 %22, 524288\n"
"s_cselect_b32 s98, s54, 0\n"
"s_mov_b32 s99, 0x20000\n"
"s_xor_b32 s71, %20, 0x100000\n"
"s_add_u32 m0, s72, 139264\n"
"s_add_u32 s67, %20, 0\n"
"buffer_load_dwordx4 v11, s[96:99], s67 offen offset:0  lds\n"
"s_add_u32 s67, %20, 15360\n"
"buffer_load_dwordx4 v11, s[96:99], s67 offen offset:1024  lds\n"
"s_add_u32 s67, %20, 30720\n"
"buffer_load_dwordx4 v11, s[96:99], s67 offen offset:2048  lds\n"
"s_add_u32 s67, %20, 46080\n"
"buffer_load_dwordx4 v11, s[96:99], s67 offen offset:3072  lds\n"
"s_add_u32 m0, s72, 98304\n"
"s_add_u32 s67, s71, 1024\n"
"buffer_load_dwordx4 v11, s[96:99], s67 offen offset:0  lds\n"
"s_add_u32 s67, s71, 16384\n"
"buffer_load_dwordx4 v11, s[96:99], s67 offen offset:1024  lds\n"
"s_add_u32 s67, s71, 31744\n"
"buffer_load_dwordx4 v11, s[96:99], s67 offen offset:2048  lds\n"
"s_add_u32 s67, s71, 47104\n"
"buffer_load_dwordx4 v11, s[96:99], s67 offen offset:3072  lds\n"
"s_branch L_y\n"
"L_fb:\n"
"s_cmp_lt_u32 %22, 524288\n"
"s_cselect_b32 s38, 8192000, 0\n"
"s_add_u32 s70, %22, 6553600\n"
"s_sub_u32 s67, s72, s64\n"
"s_add_u32 s67, s67, 98304\n"
"v_add_u32 v40, s67, v9\n"
"v_add_u32 v41, s67, v15\n"
"s_sleep 1\n"
"ds_read_b64_tr_b16 a[160:161], v40 offset:40960\n"
"ds_read_b64_tr_b16 a[162:163], v40 offset:41472\n"
"ds_read_b64_tr_b16 a[164:165], v40 offset:40968\n"
"ds_read_b64_tr_b16 a[166:167], v40 offset:41480\n"
"ds_read_b64_tr_b16 a[168:169], v41 offset:40960\n"
"ds_read_b64_tr_b16 a[170:171], v41 offset:41472\n"
"ds_read_b64_tr_b16 a[172:173], v41 offset:40968\n"
"ds_read_b64_tr_b16 a[174:175], v41 offset:41480\n"
"ds_read_b64_tr_b16 a[176:177], v40 offset:0\n"
"ds_read_b64_tr_b16 a[178:179], v40 offset:512\n"
"ds_read_b64_tr_b16 a[180:181], v40 offset:8\n"
"ds_read_b64_tr_b16 a[182:183], v40 offset:520\n"
"ds_read_b64_tr_b16 a[184:185], v41 offset:0\n"
"ds_read_b64_tr_b16 a[186:187], v41 offset:512\n"
"ds_read_b64_tr_b16 a[188:189], v41 offset:8\n"
"ds_read_b64_tr_b16 a[190:191], v41 offset:520\n"
"s_waitcnt lgkmcnt(14)\n"
"buffer_store_dwordx4 a[160:163], v0, s[36:39], %22 offen offset:0 sc1\n"
"s_waitcnt lgkmcnt(12)\n"
"buffer_store_dwordx4 a[164:167], v0, s[36:39], %22 offen offset:2048 sc1\n"
"s_waitcnt lgkmcnt(10)\n"
"buffer_store_dwordx4 a[168:171], v1, s[36:39], %22 offen offset:0 sc1\n"
"s_waitcnt lgkmcnt(8)\n"
"buffer_store_dwordx4 a[172:175], v1, s[36:39], %22 offen offset:2048 sc1\n"
"s_waitcnt lgkmcnt(6)\n"
"buffer_store_dwordx4 a[176:179], v0, s[36:39], s70 offen offset:0 sc1\n"
"s_waitcnt lgkmcnt(4)\n"
"buffer_store_dwordx4 a[180:183], v0, s[36:39], s70 offen offset:2048 sc1\n"
"s_waitcnt lgkmcnt(2)\n"
"buffer_store_dwordx4 a[184:187], v1, s[36:39], s70 offen offset:0 sc1\n"
"s_waitcnt lgkmcnt(0)\n"
"buffer_store_dwordx4 a[188:191], v1, s[36:39], s70 offen offset:2048 sc1\n"
"s_sleep 10\n"
"s_branch L_z\n"
"L_end:\n"
"s_waitcnt vmcnt(0) lgkmcnt(0)\n"
    :: LO(wsb), HI(wsb), LO(xo), HI(xo), LO(ob), HI(ob), LO(wr), HI(wr), LO(bb), HI(bb), "s"(wo), "s"(eo), "s"(ep), "s"(fo), "s"(fp),
       "s"(ldsa), "s"(w), "s"(col), "v"(threadIdx.x), "s"(db), "s"(rs1), "s"(rs2), "s"(rd1), "s"(rn1), "s"(wfo), "s"(wfa), "s"(b)
    : "memory", "scc", "vcc",
"v1","v2","v3","v4","v5","v6","v7","v8","v9","v10","v11","v12","v13","v14","v15","v16","v17","v18","v19","v20","v21","v22","v23","v24","v25","v26","v27","v28","v29","v30","v31","v32","v33","v34","v35","v36","v37","v38","v39","v40","v41","v42","v43","v44","v45","v46","v47","v48","v49","v50","v51","v52","v53","v54","v55","v56","v57","v58","v59","v60","v61","v62","v63","v64","v65","v66","v67","v68","v69","v70","v71","v72","v73","v74","v75","v76","v77","v78","v79","v80","v81","v82","v83","v84","v85","v86","v87","v88","v89","v90","v91","v92","v93","v94","v95","v96","v97","v98","v99","v100","v101","v102","v103","v104","v105","v106","v107","v108","v109","v110","v111","v112","v113","v114","v115","v116","v117","v118","v119","v120","v121","v122","v123","v124","v125","v126","v127","v128","v129","v130","v131","v132","v133","v134","v135","v136","v137","v138","v139","v140","v141","v142","v143","v144","v145","v146","v147","v148","v149","v150","v151","v152","v153","v154","v155","v156","v157","v158","v159","v160","v161","v162","v163","v164","v165","v166","v167","v168","v169","v170","v171","v172","v173","v174","v175","v176","v177","v178","v179","v180","v181","v182","v183","v184","v185","v186","v187","v188","v189","v190","v191","v192","v193","v194","v195","v196","v197","v198","v199","v200","v201","v202","v203","v204","v205","v206","v207","v208","v209","v210","v211","v212","v213","v214","v215","v216","v217","v218","v219","v220","v221","v222","v223","v224","v225","v226","v227","v228","v229","v230","v231","v232","v233","v234","v235","v236","v237","v238","v239","v240","v241","v242","v243","v244","v245","v246","v247","v248","v249","v250","v251","v252","v253","v254","v255",
"a0","a1","a2","a3","a4","a5","a6","a7","a8","a9","a10","a11","a12","a13","a14","a15","a16","a17","a18","a19","a20","a21","a22","a23","a24","a25","a26","a27","a28","a29","a30","a31","a32","a33","a34","a35","a36","a37","a38","a39","a40","a41","a42","a43","a44","a45","a46","a47","a48","a49","a50","a51","a52","a53","a54","a55","a56","a57","a58","a59","a60","a61","a62","a63","a64","a65","a66","a67","a68","a69","a70","a71","a72","a73","a74","a75","a76","a77","a78","a79","a80","a81","a82","a83","a84","a85","a86","a87","a88","a89","a90","a91","a92","a93","a94","a95","a96","a97","a98","a99","a100","a101","a102","a103","a104","a105","a106","a107","a108","a109","a110","a111","a112","a113","a114","a115","a116","a117","a118","a119","a120","a121","a122","a123","a124","a125","a126","a127","a128","a129","a130","a131","a132","a133","a134","a135","a136","a137","a138","a139","a140","a141","a142","a143","a144","a145","a146","a147","a148","a149","a150","a151","a152","a153","a154","a155","a156","a157","a158","a159","a160","a161","a162","a163","a164","a165","a166","a167","a168","a169","a170","a171","a172","a173","a174","a175","a176","a177","a178","a179","a180","a181","a182","a183","a184","a185","a186","a187","a188","a189","a190","a191","a192","a193","a194","a195","a196","a197","a198","a199","a200","a201","a202","a203","a204","a205","a206","a207","a208","a209","a210","a211","a212","a213","a214","a215","a216","a217","a218","a219","a220","a221","a222","a223","a224","a225","a226","a227","a228","a229","a230","a231","a232","a233","a234","a235","a236","a237","a238","a239","a240","a241","a242","a243","a244","a245","a246","a247","a248","a249","a250","a251","a252","a253","a254","a255",
"s27","s28","s29","s30","s31","s33","s34","s35","s36","s37","s38","s39","s40","s41","s42","s43","s44","s45","s46","s47","s48","s49","s50","s51","s52","s53","s54","s55","s56","s57","s58","s59","s60","s61","s62","s63","s64","s65","s66","s67","s68","s69","s70","s71","s72","s73","s74","s75","s76","s77","s78","s79","s80","s81","s82","s83","s84","s85","s86","s87","s88","s89","s90","s91","s92","s93","s94","s95","s96","s97","s98","s99");
}

extern "C" void kernel_launch(void* const* d_in, const int* in_sizes, int n_in,
                              void* d_out, int out_size, void* d_ws, size_t ws_size,
                              hipStream_t stream) {
  const unsigned short* x0 = (const unsigned short*)d_in[0];
  const unsigned short* ws = (const unsigned short*)d_in[1];
  const unsigned short* bs = (const unsigned short*)d_in[2];
  dcn_cross_kernel<<<256, 256, 0, stream>>>(x0, ws, bs, (unsigned char*)d_ws, (unsigned short*)d_out);
}
```
